```python
import torch

def _softmax(x, axis=-1):
    return torch.softmax(x, dim=axis)
def _gelu(x):
    return torch.nn.functional.gelu(x, approximate="tanh")
def _silu(x):
    return torch.nn.functional.silu(x)
def _sigmoid(x):
    return torch.sigmoid(x)
def _layernorm(x, axis=-1, eps=1e-5):
    m = x.mean(dim=axis, keepdim=True)
    v = x.var(dim=axis, keepdim=True, unbiased=False)
    return (x - m) / torch.sqrt(v + eps)
def _scatter_add(tgt, idx, src, axis=0):
    return tgt.clone().index_add_(axis, idx, src)
def _segment_sum(src, seg, n):
    out = torch.zeros((n,) + src.shape[1:], dtype=src.dtype)
    return out.index_add_(0, seg, src)
def _pad(x, pw):
    flat = tuple(v for pair in reversed(pw) for v in pair)
    return torch.nn.functional.pad(x, flat)

kernel_name = 'standalone__window_attn_swin_v2__vision'

def setup_inputs():

    _w = (lambda a: (lambda t: t.to(torch.bfloat16) if t.is_floating_point() else t)(torch.as_tensor(a)))
    win = 7
    H = W = (512 // win) * win
    C = 192
    nh = 4
    shift = win // 2
    coords = torch.stack(torch.meshgrid(torch.arange(win), torch.arange(win), indexing="ij"), -1).reshape(-1, 2)
    rel = coords[:, None, :] - coords[None, :, :]
    rel_idx = ((rel[..., 0] + win - 1) * (2 * win - 1) + (rel[..., 1] + win - 1)).to(torch.int64)
    tab = torch.stack(torch.meshgrid(torch.arange(1 - win, win), torch.arange(1 - win, win), indexing="ij"), -1).reshape(-1, 2).to(float)
    tab = (lambda _x: torch.sign(torch.as_tensor(_x)))(tab) * (lambda _x: torch.log1p(torch.as_tensor(_x)))(torch.abs(tab))
    img = torch.zeros((H, W), dtype=torch.int64)
    cnt = 0
    for hs in (slice(0, H - win), slice(H - win, H - shift), slice(H - shift, H)):
        for ws in (slice(0, W - win), slice(W - win, W - shift), slice(W - shift, W)):
            img[hs, ws] = cnt; cnt += 1
    mw = img.reshape(H // win, win, W // win, win).permute(0, 2, 1, 3).reshape(-1, win * win)
    mask = (mw[:, :, None] != mw[:, None, :]).to(float) * (-100.0)
    return {
        "x": _w((lambda *s: torch.randn(*s, dtype=torch.bfloat16) * 0.05)(1, H, W, C)),
        "wqkv": _w((lambda *s: torch.randn(*s, dtype=torch.bfloat16) * 1.6)(C, 3 * C)),
        "wo": _w((lambda *s: torch.randn(*s, dtype=torch.bfloat16) * 0.05)(C, C)),
        "logit_scale": _w((lambda *s: torch.randn(*s, dtype=torch.bfloat16) * 1.6)(nh)),
        "cpb_w1": _w((lambda *s: torch.randn(*s, dtype=torch.bfloat16) * 1.6)(2, 4 * nh)),
        "cpb_w2": _w((lambda *s: torch.randn(*s, dtype=torch.bfloat16) * 1.6)(4 * nh, nh)),
        "rel_coords": _w(tab),
        "rel_idx": _w(rel_idx),
        "attn_mask": _w(mask),
    }

def reference(x, wqkv, wo, logit_scale, cpb_w1, cpb_w2, rel_coords, rel_idx, attn_mask):
    B, H, W, C = x.shape
    win = 7
    nh = 4
    hd = C // nh
    shift = win // 2
    ww = win * win
    nH, nW = H // win, W // win
    xs = torch.roll(x, (-shift, -shift), (1, 2))
    xw = (lambda x, *d: x.permute(*d))(xs.reshape(B, nH, win, nW, win, C), 0, 1, 3, 2, 4, 5).reshape(B * nH * nW, ww, C)
    qkv = (lambda x, *d: x.permute(*d))((xw @ wqkv).reshape(B * nH * nW, ww, 3, nh, hd), 2, 0, 3, 1, 4)
    q, k, v = qkv[0], qkv[1], qkv[2]
    qn = q * (torch.einsum("bhqd,bhqd->bhq", q, q)[..., None] + 1e-12) ** -0.5
    kn = k * (torch.einsum("bhkd,bhkd->bhk", k, k)[..., None] + 1e-12) ** -0.5
    logits = torch.einsum("bhqd,bhkd->bhqk", qn, kn)
    tau = torch.exp(torch.clip(logit_scale, -100.0, 4.6052))
    logits = logits * tau[None, :, None, None]
    hcpb = rel_coords @ cpb_w1
    hcpb = torch.where(hcpb > 0, hcpb, hcpb * 0)
    bias_tab = 16.0 * _sigmoid(hcpb @ cpb_w2)
    bias = (lambda x, *d: x.permute(*d))(bias_tab[rel_idx], 2, 0, 1)
    logits = logits.reshape(B, nH * nW, nh, ww, ww) + bias[None, None, :, :, :] + attn_mask[None, :, None, :, :]
    p = _softmax(logits.reshape(B * nH * nW, nh, ww, ww), axis=-1)
    o = (lambda x, *d: x.permute(*d))(torch.einsum("bhqk,bhkd->bhqd", p, v), 0, 2, 1, 3).reshape(B * nH * nW, ww, C) @ wo
    o = (lambda x, *d: x.permute(*d))(o.reshape(B, nH, nW, win, win, C), 0, 1, 3, 2, 4, 5).reshape(B, H, W, C)
    o = torch.roll(o, (shift, shift), (1, 2))
    return _layernorm(o, axis=-1)
```

```cpp
#include <hip/hip_runtime.h>
#include <hip/hip_bf16.h>
#include <stdint.h>

namespace swin {

constexpr unsigned CT_WORDS = 10240u;
constexpr unsigned LDS_BYTES = 163840u;

__device__ __attribute__((aligned(256))) const unsigned CT_TAB[CT_WORDS] = {0x0u,0x40u,0x9000u,0x9000u,0x80u,0xc0u,0x9010u,0x9010u,0x110u,0x150u,0x9020u,0x9020u,0x190u,0x1d0u,0x9030u,0x9030u,0x220u,0x260u,0x9040u,0x9040u,0x2a0u,0x2e0u,0x9050u,0x9050u,0x330u,0x370u,0x9060u,0x9060u,0x3b0u,0x3f0u,0x9070u,0x9070u,0x440u,0x400u,0x9080u,0x9080u,0x4c0u,0x480u,0x9090u,0x9090u,0x550u,0x510u,0x90a0u,0x90a0u,0x5d0u,0x590u,0x90b0u,0x90b0u,0x660u,0x620u,0x90c0u,0x90c0u,0x6e0u,0x6a0u,0x90d0u,0x90d0u,0x770u,0x730u,0x90e0u,0x90e0u,0x7f0u,0x7b0u,0x90f0u,0x90f0u,0x10u,0x50u,0x9300u,0x9300u,0x90u,0xd0u,0x9310u,0x9310u,0x100u,0x140u,0x9320u,0x9320u,0x180u,0x1c0u,0x9330u,0x9330u,0x230u,0x270u,0x9340u,0x9340u,0x2b0u,0x2f0u,0x9350u,0x9350u,0x320u,0x360u,0x9360u,0x9360u,0x3a0u,0x3e0u,0x9370u,0x9370u,0x450u,0x410u,0x9380u,0x9380u,0x4d0u,0x490u,0x9390u,0x9390u,0x540u,0x500u,0x93a0u,0x93a0u,0x5c0u,0x580u,0x93b0u,0x93b0u,0x670u,0x630u,0x93c0u,0x93c0u,0x6f0u,0x6b0u,0x93d0u,0x93d0u,0x760u,0x720u,0x93e0u,0x93e0u,0x7e0u,0x7a0u,0x93f0u,0x93f0u,0x20u,0x60u,0x9600u,0x9600u,0xa0u,0xe0u,0x9610u,0x9610u,0x130u,0x170u,0x9620u,0x9620u,0x1b0u,0x1f0u,0x9630u,0x9630u,0x200u,0x240u,0x9640u,0x9640u,0x280u,0x2c0u,0x9650u,0x9650u,0x310u,0x350u,0x9660u,0x9660u,0x390u,0x3d0u,0x9670u,0x9670u,0x460u,0x420u,0x9680u,0x9680u,0x4e0u,0x4a0u,0x9690u,0x9690u,0x570u,0x530u,0x96a0u,0x96a0u,0x5f0u,0x5b0u,0x96b0u,0x96b0u,0x640u,0x600u,0x96c0u,0x96c0u,0x6c0u,0x680u,0x96d0u,0x96d0u,0x750u,0x710u,0x96e0u,0x96e0u,0x7d0u,0x790u,0x96f0u,0x96f0u,0x30u,0x70u,0x9900u,0x9900u,0xb0u,0xf0u,0x9910u,0x9910u,0x120u,0x160u,0x9920u,0x9920u,0x1a0u,0x1e0u,0x9930u,0x9930u,0x210u,0x250u,0x9940u,0x9940u,0x290u,0x2d0u,0x9950u,0x9950u,0x300u,0x340u,0x9960u,0x9960u,0x380u,0x3c0u,0x9970u,0x9970u,0x470u,0x430u,0x9980u,0x9980u,0x4f0u,0x4b0u,0x9990u,0x9990u,0x560u,0x520u,0x99a0u,0x99a0u,0x5e0u,0x5a0u,0x99b0u,0x99b0u,0x650u,0x610u,0x99c0u,0x99c0u,0x6d0u,0x690u,0x99d0u,0x99d0u,0x740u,0x700u,0x99e0u,0x99e0u,0x7c0u,0x780u,0x99f0u,0x99f0u,0x9c00u,0x12000u,0x12040u,0x12000u,0x9c10u,0x12190u,0x121d0u,0x12010u,0x9c20u,0x12320u,0x12360u,0x12020u,0x9c30u,0x124b0u,0x124f0u,0x12030u,0x9c40u,0x12640u,0x12600u,0x12040u,0x9c50u,0x127d0u,0x12790u,0x12050u,0x9c60u,0x12960u,0x12920u,0x12060u,0x9c70u,0x12af0u,0x12ab0u,0x12070u,0x9c80u,0x12c00u,0x12c40u,0x12080u,0x9c90u,0x12d90u,0x12dd0u,0x12090u,0x9ca0u,0x12f20u,0x12f60u,0x120a0u,0x9cb0u,0x130b0u,0x130f0u,0x120b0u,0x9cc0u,0x13240u,0x13200u,0x120c0u,0x9cd0u,0x133d0u,0x13390u,0x120d0u,0x9ce0u,0x13560u,0x13520u,0x120e0u,0x9cf0u,0x136f0u,0x136b0u,0x120f0u,0x9f00u,0x12010u,0x12048u,0x12100u,0x9f10u,0x12180u,0x121d8u,0x12110u,0x9f20u,0x12330u,0x12368u,0x12120u,0x9f30u,0x124a0u,0x124f8u,0x12130u,0x9f40u,0x12650u,0x12608u,0x12140u,0x9f50u,0x127c0u,0x12798u,0x12150u,0x9f60u,0x12970u,0x12928u,0x12160u,0x9f70u,0x12ae0u,0x12ab8u,0x12170u,0x9f80u,0x12c10u,0x12c48u,0x12180u,0x9f90u,0x12d80u,0x12dd8u,0x12190u,0x9fa0u,0x12f30u,0x12f68u,0x121a0u,0x9fb0u,0x130a0u,0x130f8u,0x121b0u,0x9fc0u,0x13250u,0x13208u,0x121c0u,0x9fd0u,0x133c0u,0x13398u,0x121d0u,0x9fe0u,0x13570u,0x13528u,0x121e0u,0x9ff0u,0x136e0u,0x136b8u,0x121f0u,0xa200u,0x12020u,0x12050u,0x12200u,0xa210u,0x121b0u,0x121c0u,0x12210u,0xa220u,0x12300u,0x12370u,0x12220u,0xa230u,0x12490u,0x124e0u,0x12230u,0xa240u,0x12660u,0x12610u,0x12240u,0xa250u,0x127f0u,0x12780u,0x12250u,0xa260u,0x12940u,0x12930u,0x12260u,0xa270u,0x12ad0u,0x12aa0u,0x12270u,0xa280u,0x12c20u,0x12c50u,0x12280u,0xa290u,0x12db0u,0x12dc0u,0x12290u,0xa2a0u,0x12f00u,0x12f70u,0x122a0u,0xa2b0u,0x13090u,0x130e0u,0x122b0u,0xa2c0u,0x13260u,0x13210u,0x122c0u,0xa2d0u,0x133f0u,0x13380u,0x122d0u,0xa2e0u,0x13540u,0x13530u,0x122e0u,0xa2f0u,0x136d0u,0x136a0u,0x122f0u,0xa500u,0x12030u,0x12058u,0x12300u,0xa510u,0x121a0u,0x121c8u,0x12310u,0xa520u,0x12310u,0x12378u,0x12320u,0xa530u,0x12480u,0x124e8u,0x12330u,0xa540u,0x12670u,0x12618u,0x12340u,0xa550u,0x127e0u,0x12788u,0x12350u,0xa560u,0x12950u,0x12938u,0x12360u,0xa570u,0x12ac0u,0x12aa8u,0x12370u,0xa580u,0x12c30u,0x12c58u,0x12380u,0xa590u,0x12da0u,0x12dc8u,0x12390u,0xa5a0u,0x12f10u,0x12f78u,0x123a0u,0xa5b0u,0x13080u,0x130e8u,0x123b0u,0xa5c0u,0x13270u,0x13218u,0x123c0u,0xa5d0u,0x133e0u,0x13388u,0x123d0u,0xa5e0u,0x13550u,0x13538u,0x123e0u,0xa5f0u,0x136c0u,0x136a8u,0x123f0u,0x25180u,0x25180u,0x1b000u,0x1b300u,0x25190u,0x25190u,0x100000u,0x1b302u,0x251a0u,0x251a0u,0x100000u,0x1b304u,0x251b0u,0x251b0u,0x100000u,0x1b306u,0x251c0u,0x251c0u,0x1b000u,0x1b308u,0x251d0u,0x251d0u,0x100000u,0x1b30au,0x251e0u,0x251e0u,0x100000u,0x1b30cu,0x251f0u,0x251f0u,0x100000u,0x1b30eu,0x25200u,0x25200u,0x1b000u,0x1b310u,0x25210u,0x25210u,0x100000u,0x1b312u,0x25220u,0x25220u,0x100000u,0x1b314u,0x25230u,0x25230u,0x100000u,0x1b316u,0x25240u,0x25240u,0x1b000u,0x1b318u,0x25250u,0x25250u,0x100000u,0x1b31au,0x25260u,0x25260u,0x100000u,0x1b31cu,0x25270u,0x25270u,0x100000u,0x1b31eu,0x25184u,0x25280u,0x1b030u,0x1b300u,0x25194u,0x25290u,0x100000u,0x1b302u,0x251a4u,0x252a0u,0x100000u,0x1b304u,0x251b4u,0x252b0u,0x100000u,0x1b306u,0x251c4u,0x252c0u,0x1b030u,0x1b308u,0x251d4u,0x252d0u,0x100000u,0x1b30au,0x251e4u,0x252e0u,0x100000u,0x1b30cu,0x251f4u,0x252f0u,0x100000u,0x1b30eu,0x25204u,0x25300u,0x1b030u,0x1b310u,0x25214u,0x25310u,0x100000u,0x1b312u,0x25224u,0x25320u,0x100000u,0x1b314u,0x25234u,0x25330u,0x100000u,0x1b316u,0x25244u,0x25340u,0x1b030u,0x1b318u,0x25254u,0x25350u,0x100000u,0x1b31au,0x25264u,0x25360u,0x100000u,0x1b31cu,0x25274u,0x25370u,0x100000u,0x1b31eu,0x25280u,0x25380u,0x1b060u,0x1b300u,0x25290u,0x25390u,0x100000u,0x1b302u,0x252a0u,0x253a0u,0x100000u,0x1b304u,0x252b0u,0x253b0u,0x100000u,0x1b306u,0x252c0u,0x253c0u,0x1b060u,0x1b308u,0x252d0u,0x253d0u,0x100000u,0x1b30au,0x252e0u,0x253e0u,0x100000u,0x1b30cu,0x252f0u,0x253f0u,0x100000u,0x1b30eu,0x25300u,0x25400u,0x1b060u,0x1b310u,0x25310u,0x25410u,0x100000u,0x1b312u,0x25320u,0x25420u,0x100000u,0x1b314u,0x25330u,0x25430u,0x100000u,0x1b316u,0x25340u,0x25440u,0x1b060u,0x1b318u,0x25350u,0x25450u,0x100000u,0x1b31au,0x25360u,0x25460u,0x100000u,0x1b31cu,0x25370u,0x25470u,0x100000u,0x1b31eu,0x25284u,0x25480u,0x1b090u,0x1b300u,0x25294u,0x25490u,0x100000u,0x1b302u,0x252a4u,0x254a0u,0x100000u,0x1b304u,0x252b4u,0x254b0u,0x100000u,0x1b306u,0x252c4u,0x254c0u,0x1b090u,0x1b308u,0x252d4u,0x254d0u,0x100000u,0x1b30au,0x252e4u,0x254e0u,0x100000u,0x1b30cu,0x252f4u,0x254f0u,0x100000u,0x1b30eu,0x25304u,0x25500u,0x1b090u,0x1b310u,0x25314u,0x25510u,0x100000u,0x1b312u,0x25324u,0x25520u,0x100000u,0x1b314u,0x25334u,0x25530u,0x100000u,0x1b316u,0x25344u,0x25540u,0x1b090u,0x1b318u,0x25354u,0x25550u,0x100000u,0x1b31au,0x25364u,0x25560u,0x100000u,0x1b31cu,0x25374u,0x25570u,0x100000u,0x1b31eu,0x20e80u,0x26980u,0x26980u,0xc0100u,0x20e82u,0x26982u,0x100000u,0xc0110u,0x20e84u,0x26984u,0x100000u,0xc0120u,0x20e86u,0x26986u,0x100000u,0xc0130u,0x21080u,0x26990u,0x26980u,0xc0140u,0x21082u,0x26992u,0x100000u,0xc0150u,0x21084u,0x26994u,0x100000u,0xc0160u,0x21086u,0x26996u,0x100000u,0xc0170u,0x21280u,0x269a0u,0x26980u,0xc0280u,0x21282u,0x269a2u,0x100000u,0xc0290u,0x21284u,0x269a4u,0x100000u,0xc02a0u,0x21286u,0x269a6u,0x100000u,0xc02b0u,0x21480u,0x269b0u,0x26980u,0xc02c0u,0x21482u,0x269b2u,0x100000u,0xc02d0u,0x21484u,0x269b4u,0x100000u,0xc02e0u,0x21486u,0x269b6u,0x100000u,0xc02f0u,0x20e88u,0x26988u,0x26990u,0xc0410u,0x20e8au,0x2698au,0x100000u,0xc0400u,0x20e8cu,0x2698cu,0x100000u,0xc0430u,0x20e8eu,0x2698eu,0x100000u,0xc0420u,0x21088u,0x26998u,0x26990u,0xc0450u,0x2108au,0x2699au,0x100000u,0xc0440u,0x2108cu,0x2699cu,0x100000u,0xc0470u,0x2108eu,0x2699eu,0x100000u,0xc0460u,0x21288u,0x269a8u,0x26990u,0xef990u,0x2128au,0x269aau,0x100000u,0xef980u,0x2128cu,0x269acu,0x100000u,0xef9b0u,0x2128eu,0x269aeu,0x100000u,0xef9a0u,0x21488u,0x269b8u,0x26990u,0xef9d0u,0x2148au,0x269bau,0x100000u,0xef9c0u,0x2148cu,0x269bcu,0x100000u,0xef9f0u,0x2148eu,0x269beu,0x100000u,0xef9e0u,0x21680u,0x269c0u,0x269a0u,0xefb20u,0x21682u,0x269c2u,0x100000u,0xefb30u,0x21684u,0x269c4u,0x100000u,0xefb00u,0x21686u,0x269c6u,0x100000u,0xefb10u,0x21880u,0x269d0u,0x269a0u,0xefb60u,0x21882u,0x269d2u,0x100000u,0xefb70u,0x21884u,0x269d4u,0x100000u,0xefb40u,0x21886u,0x269d6u,0x100000u,0xefb50u,0x21a80u,0x269e0u,0x269a0u,0xefca0u,0x21a82u,0x269e2u,0x100000u,0xefcb0u,0x21a84u,0x269e4u,0x100000u,0xefc80u,0x21a86u,0x269e6u,0x100000u,0xefc90u,0x21c80u,0x269f0u,0x269a0u,0xefce0u,0x21c82u,0x269f2u,0x100000u,0xefcf0u,0x21c84u,0x269f4u,0x100000u,0xefcc0u,0x21c86u,0x269f6u,0x100000u,0xefcd0u,0x100000u,0x269c8u,0x269b0u,0xefe30u,0x100000u,0x269cau,0x100000u,0xefe20u,0x100000u,0x269ccu,0x100000u,0xefe10u,0x100000u,0x269ceu,0x100000u,0xefe00u,0x100000u,0x269d8u,0x269b0u,0xefe70u,0x100000u,0x269dau,0x100000u,0xefe60u,0x100000u,0x269dcu,0x100000u,0xefe50u,0x100000u,0x269deu,0x100000u,0xefe40u,0x100000u,0x269e8u,0x269b0u,0xeffb0u,0x100000u,0x269eau,0x100000u,0xeffa0u,0x100000u,0x269ecu,0x100000u,0xeff90u,0x100000u,0x269eeu,0x100000u,0xeff80u,0x100000u,0x269f8u,0x269b0u,0xefff0u,0x100000u,0x269fau,0x100000u,0xeffe0u,0x100000u,0x269fcu,0x100000u,0xeffd0u,0x100000u,0x269feu,0x100000u,0xeffc0u,0x0u,0xc0000u,0x60080u,0x100u,0x10u,0xc0010u,0x60090u,0x110u,0x20u,0xc0020u,0x600a0u,0x120u,0x30u,0xc0030u,0x600b0u,0x130u,0x40u,0xc0040u,0x600c0u,0x140u,0x50u,0xc0050u,0x600d0u,0x150u,0x60u,0xc0060u,0x600e0u,0x160u,0x70u,0xc0070u,0x600f0u,0x170u,0x180u,0xc0180u,0x60200u,0x280u,0x190u,0xc0190u,0x60210u,0x290u,0x1a0u,0xc01a0u,0x60220u,0x2a0u,0x1b0u,0xc01b0u,0x60230u,0x2b0u,0x1c0u,0xc01c0u,0x60240u,0x2c0u,0x1d0u,0xc01d0u,0x60250u,0x2d0u,0x1e0u,0xc01e0u,0x60260u,0x2e0u,0x1f0u,0xc01f0u,0x60270u,0x2f0u,0x310u,0xc0310u,0x60390u,0x410u,0x300u,0xc0300u,0x60380u,0x400u,0x330u,0xc0330u,0x603b0u,0x430u,0x320u,0xc0320u,0x603a0u,0x420u,0x350u,0xc0350u,0x603d0u,0x450u,0x340u,0xc0340u,0x603c0u,0x440u,0x370u,0xc0370u,0x603f0u,0x470u,0x360u,0xc0360u,0x603e0u,0x460u,0x490u,0xef890u,0x60510u,0x590u,0x480u,0xef880u,0x60500u,0x580u,0x4b0u,0xef8b0u,0x60530u,0x5b0u,0x4a0u,0xef8a0u,0x60520u,0x5a0u,0x4d0u,0xef8d0u,0x60550u,0x5d0u,0x4c0u,0xef8c0u,0x60540u,0x5c0u,0x4f0u,0xef8f0u,0x60570u,0x5f0u,0x4e0u,0xef8e0u,0x60560u,0x5e0u,0x620u,0xefa20u,0x606a0u,0x720u,0x630u,0xefa30u,0x606b0u,0x730u,0x600u,0xefa00u,0x60680u,0x700u,0x610u,0xefa10u,0x60690u,0x710u,0x660u,0xefa60u,0x606e0u,0x760u,0x670u,0xefa70u,0x606f0u,0x770u,0x640u,0xefa40u,0x606c0u,0x740u,0x650u,0xefa50u,0x606d0u,0x750u,0x7a0u,0xefba0u,0x8fc20u,0x8a0u,0x7b0u,0xefbb0u,0x8fc30u,0x8b0u,0x780u,0xefb80u,0x8fc00u,0x880u,0x790u,0xefb90u,0x8fc10u,0x890u,0x7e0u,0xefbe0u,0x8fc60u,0x8e0u,0x7f0u,0xefbf0u,0x8fc70u,0x8f0u,0x7c0u,0xefbc0u,0x8fc40u,0x8c0u,0x7d0u,0xefbd0u,0x8fc50u,0x8d0u,0x930u,0xefd30u,0x8fdb0u,0xa30u,0x920u,0xefd20u,0x8fda0u,0xa20u,0x910u,0xefd10u,0x8fd90u,0xa10u,0x900u,0xefd00u,0x8fd80u,0xa00u,0x970u,0xefd70u,0x8fdf0u,0xa70u,0x960u,0xefd60u,0x8fde0u,0xa60u,0x950u,0xefd50u,0x8fdd0u,0xa50u,0x940u,0xefd40u,0x8fdc0u,0xa40u,0x2feb0u,0xefeb0u,0x8ff30u,0x2ffb0u,0x2fea0u,0xefea0u,0x8ff20u,0x2ffa0u,0x2fe90u,0xefe90u,0x8ff10u,0x2ff90u,0x2fe80u,0xefe80u,0x8ff00u,0x2ff80u,0x2fef0u,0xefef0u,0x8ff70u,0x2fff0u,0x2fee0u,0xefee0u,0x8ff60u,0x2ffe0u,0x2fed0u,0xefed0u,0x8ff50u,0x2ffd0u,0x2fec0u,0xefec0u,0x8ff40u,0x2ffc0u,0x0u,0x30420u,0x8fc50u,0xc0000u,0x10u,0x30430u,0x8fc40u,0xc0010u,0x20u,0x30400u,0x8fc70u,0xc0020u,0x30u,0x30410u,0x8fc60u,0xc0030u,0x40u,0x30460u,0x8fc10u,0xc0040u,0x50u,0x30470u,0x8fc00u,0xc0050u,0x60u,0x30440u,0x8fc30u,0xc0060u,0x70u,0x30450u,0x8fc20u,0xc0070u,0x80u,0x304b0u,0x8fcd0u,0xc0080u,0x90u,0x304a0u,0x8fcc0u,0xc0090u,0xa0u,0x30490u,0x8fcf0u,0xc00a0u,0xb0u,0x30480u,0x8fce0u,0xc00b0u,0xc0u,0x304f0u,0x8fc90u,0xc00c0u,0xd0u,0x304e0u,0x8fc80u,0xc00d0u,0xe0u,0x304d0u,0x8fcb0u,0xc00e0u,0xf0u,0x304c0u,0x8fca0u,0xc00f0u,0x100u,0x30530u,0x8fd60u,0xc0100u,0x110u,0x30520u,0x8fd70u,0xc0110u,0x120u,0x30510u,0x8fd40u,0xc0120u,0x130u,0x30500u,0x8fd50u,0xc0130u,0x140u,0x30570u,0x8fd20u,0xc0140u,0x150u,0x30560u,0x8fd30u,0xc0150u,0x160u,0x30550u,0x8fd00u,0xc0160u,0x170u,0x30540u,0x8fd10u,0xc0170u,0x190u,0x305b0u,0x8fde0u,0xc0190u,0x180u,0x305a0u,0x8fdf0u,0xc0180u,0x1b0u,0x30590u,0x8fdc0u,0xc01b0u,0x1a0u,0x30580u,0x8fdd0u,0xc01a0u,0x1d0u,0x305f0u,0x8fda0u,0xc01d0u,0x1c0u,0x305e0u,0x8fdb0u,0xc01c0u,0x1f0u,0x305d0u,0x8fd80u,0xc01f0u,0x1e0u,0x305c0u,0x8fd90u,0xc01e0u,0x210u,0x30640u,0x8fe60u,0xc0210u,0x200u,0x30650u,0x8fe70u,0xc0200u,0x230u,0x30660u,0x8fe40u,0xc0230u,0x220u,0x30670u,0x8fe50u,0xc0220u,0x250u,0x30600u,0x8fe20u,0xc0250u,0x240u,0x30610u,0x8fe30u,0xc0240u,0x270u,0x30620u,0x8fe00u,0xc0270u,0x260u,0x30630u,0x8fe10u,0xc0260u,0x290u,0x306c0u,0x8fef0u,0xc0290u,0x280u,0x306d0u,0x8fee0u,0xc0280u,0x2b0u,0x306e0u,0x8fed0u,0xc02b0u,0x2a0u,0x306f0u,0x8fec0u,0xc02a0u,0x2d0u,0x30680u,0x8feb0u,0xc02d0u,0x2c0u,0x30690u,0x8fea0u,0xc02c0u,0x2f0u,0x306a0u,0x8fe90u,0xc02f0u,0x2e0u,0x306b0u,0x8fe80u,0xc02e0u,0x320u,0x30740u,0x8ff70u,0xc0320u,0x330u,0x30750u,0x8ff60u,0xc0330u,0x300u,0x30760u,0x8ff50u,0xc0300u,0x310u,0x30770u,0x8ff40u,0xc0310u,0x360u,0x30700u,0x8ff30u,0xc0360u,0x370u,0x30710u,0x8ff20u,0xc0370u,0x340u,0x30720u,0x8ff10u,0xc0340u,0x350u,0x30730u,0x8ff00u,0xc0350u,0x3a0u,0x307d0u,0x8fff0u,0xc03a0u,0x3b0u,0x307c0u,0x8ffe0u,0xc03b0u,0x380u,0x307f0u,0x8ffd0u,0xc0380u,0x390u,0x307e0u,0x8ffc0u,0xc0390u,0x3e0u,0x30790u,0x8ffb0u,0xc03e0u,0x3f0u,0x30780u,0x8ffa0u,0xc03f0u,0x3c0u,0x307b0u,0x8ff90u,0xc03c0u,0x3d0u,0x307a0u,0x8ff80u,0xc03d0u,0x0u,0x11f820u,0x0u,0x0u,0x0u,0x11f830u,0x0u,0x10u,0x0u,0x11f800u,0x0u,0x20u,0x0u,0x11f810u,0x0u,0x30u,0x0u,0x11f860u,0x0u,0x40u,0x0u,0x11f870u,0x0u,0x50u,0x0u,0x11f840u,0x0u,0x60u,0x0u,0x11f850u,0x0u,0x70u,0x0u,0x11f8b0u,0x0u,0x80u,0x0u,0x11f8a0u,0x0u,0x90u,0x0u,0x11f890u,0x0u,0xa0u,0x0u,0x11f880u,0x0u,0xb0u,0x0u,0x11f8f0u,0x0u,0xc0u,0x0u,0x11f8e0u,0x0u,0xd0u,0x0u,0x11f8d0u,0x0u,0xe0u,0x0u,0x11f8c0u,0x0u,0xf0u,0x0u,0x11f930u,0x0u,0x100u,0x0u,0x11f920u,0x0u,0x110u,0x0u,0x11f910u,0x0u,0x120u,0x0u,0x11f900u,0x0u,0x130u,0x0u,0x11f970u,0x0u,0x140u,0x0u,0x11f960u,0x0u,0x150u,0x0u,0x11f950u,0x0u,0x160u,0x0u,0x11f940u,0x0u,0x170u,0x0u,0x11f9b0u,0x0u,0x180u,0x0u,0x11f9a0u,0x0u,0x190u,0x0u,0x11f990u,0x0u,0x1a0u,0x0u,0x11f980u,0x0u,0x1b0u,0x0u,0x11f9f0u,0x0u,0x1c0u,0x0u,0x11f9e0u,0x0u,0x1d0u,0x0u,0x11f9d0u,0x0u,0x1e0u,0x0u,0x11f9c0u,0x0u,0x1f0u,0x0u,0x11fa40u,0x0u,0x200u,0x0u,0x11fa50u,0x0u,0x210u,0x0u,0x11fa60u,0x0u,0x220u,0x0u,0x11fa70u,0x0u,0x230u,0x0u,0x11fa00u,0x0u,0x240u,0x0u,0x11fa10u,0x0u,0x250u,0x0u,0x11fa20u,0x0u,0x260u,0x0u,0x11fa30u,0x0u,0x270u,0x0u,0x11fac0u,0x0u,0x280u,0x0u,0x11fad0u,0x0u,0x290u,0x0u,0x11fae0u,0x0u,0x2a0u,0x0u,0x11faf0u,0x0u,0x2b0u,0x0u,0x11fa80u,0x0u,0x2c0u,0x0u,0x11fa90u,0x0u,0x2d0u,0x0u,0x11faa0u,0x0u,0x2e0u,0x0u,0x11fab0u,0x0u,0x2f0u,0x0u,0x11fb40u,0x0u,0x300u,0x0u,0x11fb50u,0x0u,0x310u,0x0u,0x11fb60u,0x0u,0x320u,0x0u,0x11fb70u,0x0u,0x330u,0x0u,0x11fb00u,0x0u,0x340u,0x0u,0x11fb10u,0x0u,0x350u,0x0u,0x11fb20u,0x0u,0x360u,0x0u,0x11fb30u,0x0u,0x370u,0x0u,0x11fbd0u,0x0u,0x380u,0x0u,0x11fbc0u,0x0u,0x390u,0x0u,0x11fbf0u,0x0u,0x3a0u,0x0u,0x11fbe0u,0x0u,0x3b0u,0x0u,0x11fb90u,0x0u,0x3c0u,0x0u,0x11fb80u,0x0u,0x3d0u,0x0u,0x11fbb0u,0x0u,0x3e0u,0x0u,0x11fba0u,0x0u,0x3f0u,0x0u,0x1000u,0x0u,0x26180u,0x0u,0x1004u,0x0u,0x26190u,0x0u,0x1008u,0x0u,0x261a0u,0x0u,0x100cu,0x0u,0x261b0u,0x0u,0x1010u,0x0u,0x261c0u,0x0u,0x1014u,0x0u,0x261d0u,0x0u,0x1018u,0x0u,0x261e0u,0x0u,0x101cu,0x0u,0x261f0u,0x0u,0x1020u,0x0u,0x26200u,0x0u,0x1024u,0x0u,0x26210u,0x0u,0x1028u,0x0u,0x26220u,0x0u,0x102cu,0x0u,0x26230u,0x0u,0x1030u,0x0u,0x26240u,0x0u,0x1034u,0x0u,0x26250u,0x0u,0x1038u,0x0u,0x26260u,0x0u,0x103cu,0x0u,0x26270u,0x0u,0x1040u,0x0u,0x26280u,0x0u,0x1044u,0x0u,0x26290u,0x0u,0x1048u,0x0u,0x262a0u,0x0u,0x104cu,0x0u,0x262b0u,0x0u,0x1050u,0x0u,0x262c0u,0x0u,0x1054u,0x0u,0x262d0u,0x0u,0x1058u,0x0u,0x262e0u,0x0u,0x105cu,0x0u,0x262f0u,0x0u,0x1060u,0x0u,0x26300u,0x0u,0x1064u,0x0u,0x26310u,0x0u,0x1068u,0x0u,0x26320u,0x0u,0x106cu,0x0u,0x26330u,0x0u,0x1070u,0x0u,0x26340u,0x0u,0x1074u,0x0u,0x26350u,0x0u,0x1078u,0x0u,0x26360u,0x0u,0x107cu,0x0u,0x26370u,0x0u,0x1080u,0x0u,0x26380u,0x0u,0x1084u,0x0u,0x26390u,0x0u,0x1088u,0x0u,0x263a0u,0x0u,0x108cu,0x0u,0x263b0u,0x0u,0x1090u,0x0u,0x263c0u,0x0u,0x1094u,0x0u,0x263d0u,0x0u,0x1098u,0x0u,0x263e0u,0x0u,0x109cu,0x0u,0x263f0u,0x0u,0x10a0u,0x0u,0x26400u,0x0u,0x10a4u,0x0u,0x26410u,0x0u,0x10a8u,0x0u,0x26420u,0x0u,0x10acu,0x0u,0x26430u,0x0u,0x10b0u,0x0u,0x26440u,0x0u,0x10b4u,0x0u,0x26450u,0x0u,0x10b8u,0x0u,0x26460u,0x0u,0x10bcu,0x0u,0x26470u,0x0u,0x10c0u,0x0u,0x26480u,0x0u,0x10c4u,0x0u,0x26490u,0x0u,0x10c8u,0x0u,0x264a0u,0x0u,0x10ccu,0x0u,0x264b0u,0x0u,0x10d0u,0x0u,0x264c0u,0x0u,0x10d4u,0x0u,0x264d0u,0x0u,0x10d8u,0x0u,0x264e0u,0x0u,0x10dcu,0x0u,0x264f0u,0x0u,0x10e0u,0x0u,0x26500u,0x0u,0x10e4u,0x0u,0x26510u,0x0u,0x10e8u,0x0u,0x26520u,0x0u,0x10ecu,0x0u,0x26530u,0x0u,0x10f0u,0x0u,0x26540u,0x0u,0x10f4u,0x0u,0x26550u,0x0u,0x10f8u,0x0u,0x26560u,0x0u,0x10fcu,0x0u,0x26570u,0x3f803f80u,0x3f803f80u,0x3f803f80u,0x3f803f80u,0x0u,0x0u,0x0u,0x0u,0x0u,0x0u,0x0u,0x0u,0x0u,0x0u,0x0u,0x0u,0x3f803f80u,0x3f803f80u,0x3f803f80u,0x3f803f80u,0x0u,0x0u,0x0u,0x0u,0x0u,0x0u,0x0u,0x0u,0x0u,0x0u,0x0u,0x0u,0x3f803f80u,0x3f803f80u,0x3f803f80u,0x3f803f80u,0x0u,0x0u,0x0u,0x0u,0x0u,0x0u,0x0u,0x0u,0x0u,0x0u,0x0u,0x0u,0x3f803f80u,0x3f803f80u,0x3f803f80u,0x3f803f80u,0x0u,0x0u,0x0u,0x0u,0x0u,0x0u,0x0u,0x0u,0x0u,0x0u,0x0u,0x0u,0x3f803f80u,0x3f803f80u,0x3f803f80u,0x3f803f80u,0x0u,0x0u,0x0u,0x0u,0x0u,0x0u,0x0u,0x0u,0x0u,0x0u,0x0u,0x0u,0x3f803f80u,0x3f803f80u,0x3f803f80u,0x3f803f80u,0x0u,0x0u,0x0u,0x0u,0x0u,0x0u,0x0u,0x0u,0x0u,0x0u,0x0u,0x0u,0x3f803f80u,0x3f803f80u,0x3f803f80u,0x3f803f80u,0x0u,0x0u,0x0u,0x0u,0x0u,0x0u,0x0u,0x0u,0x0u,0x0u,0x0u,0x0u,0x3f803f80u,0x3f803f80u,0x3f803f80u,0x3f803f80u,0x0u,0x0u,0x0u,0x0u,0x0u,0x0u,0x0u,0x0u,0x0u,0x0u,0x0u,0x0u,0x3f803f80u,0x3f803f80u,0x3f803f80u,0x3f803f80u,0x0u,0x0u,0x0u,0x0u,0x0u,0x0u,0x0u,0x0u,0x0u,0x0u,0x0u,0x0u,0x3f803f80u,0x3f803f80u,0x3f803f80u,0x3f803f80u,0x0u,0x0u,0x0u,0x0u,0x0u,0x0u,0x0u,0x0u,0x0u,0x0u,0x0u,0x0u,0x3f803f80u,0x3f803f80u,0x3f803f80u,0x3f803f80u,0x0u,0x0u,0x0u,0x0u,0x0u,0x0u,0x0u,0x0u,0x0u,0x0u,0x0u,0x0u,0x3f803f80u,0x3f803f80u,0x3f803f80u,0x3f803f80u,0x0u,0x0u,0x0u,0x0u,0x0u,0x0u,0x0u,0x0u,0x0u,0x0u,0x0u,0x0u,0x3f803f80u,0x3f803f80u,0x3f803f80u,0x3f803f80u,0x0u,0x0u,0x0u,0x0u,0x0u,0x0u,0x0u,0x0u,0x0u,0x0u,0x0u,0x0u,0x3f803f80u,0x3f803f80u,0x3f803f80u,0x3f803f80u,0x0u,0x0u,0x0u,0x0u,0x0u,0x0u,0x0u,0x0u,0x0u,0x0u,0x0u,0x0u,0x3f803f80u,0x3f803f80u,0x3f803f80u,0x3f803f80u,0x0u,0x0u,0x0u,0x0u,0x0u,0x0u,0x0u,0x0u,0x0u,0x0u,0x0u,0x0u,0x3f803f80u,0x3f803f80u,0x3f803f80u,0x3f803f80u,0x0u,0x0u,0x0u,0x0u,0x0u,0x0u,0x0u,0x0u,0x0u,0x0u,0x0u,0x0u,0x7030cu,0x0u,0x0u,0x0u,0x7030cu,0x0u,0x0u,0x0u,0x7030cu,0x0u,0x0u,0x0u,0x7030cu,0x0u,0x0u,0x0u,0x7030cu,0x0u,0x0u,0x0u,0x7030cu,0x0u,0x0u,0x0u,0x7030cu,0x0u,0x0u,0x0u,0x7030cu,0x0u,0x0u,0x0u,0x7230cu,0x0u,0x0u,0x0u,0x7230cu,0x0u,0x0u,0x0u,0x7230cu,0x0u,0x0u,0x0u,0x7230cu,0x0u,0x0u,0x0u,0x7230cu,0x0u,0x0u,0x0u,0x7230cu,0x0u,0x0u,0x0u,0x7230cu,0x0u,0x0u,0x0u,0x7230cu,0x0u,0x0u,0x0u,0x7232cu,0x0u,0x0u,0x0u,0x7232cu,0x0u,0x0u,0x0u,0x7232cu,0x0u,0x0u,0x0u,0x7232cu,0x0u,0x0u,0x0u,0x7232cu,0x0u,0x0u,0x0u,0x7232cu,0x0u,0x0u,0x0u,0x7232cu,0x0u,0x0u,0x0u,0x7232cu,0x0u,0x0u,0x0u,0x72124u,0x0u,0x0u,0x0u,0x72124u,0x0u,0x0u,0x0u,0x72124u,0x0u,0x0u,0x0u,0x72124u,0x0u,0x0u,0x0u,0x72124u,0x0u,0x0u,0x0u,0x72124u,0x0u,0x0u,0x0u,0x72124u,0x0u,0x0u,0x0u,0x72124u,0x0u,0x0u,0x0u,0x721a6u,0x0u,0x0u,0x0u,0x721a6u,0x0u,0x0u,0x0u,0x721a6u,0x0u,0x0u,0x0u,0x721a6u,0x0u,0x0u,0x0u,0x721a6u,0x0u,0x0u,0x0u,0x721a6u,0x0u,0x0u,0x0u,0x721a6u,0x0u,0x0u,0x0u,0x721a6u,0x0u,0x0u,0x0u,0x72186u,0x0u,0x0u,0x0u,0x72186u,0x0u,0x0u,0x0u,0x72186u,0x0u,0x0u,0x0u,0x72186u,0x0u,0x0u,0x0u,0x72186u,0x0u,0x0u,0x0u,0x72186u,0x0u,0x0u,0x0u,0x72186u,0x0u,0x0u,0x0u,0x72186u,0x0u,0x0u,0x0u,0x72186u,0x0u,0x0u,0x0u,0x72186u,0x0u,0x0u,0x0u,0x72186u,0x0u,0x0u,0x0u,0x72186u,0x0u,0x0u,0x0u,0x72186u,0x0u,0x0u,0x0u,0x72186u,0x0u,0x0u,0x0u,0x72186u,0x0u,0x0u,0x0u,0x72186u,0x0u,0x0u,0x0u,0x7230cu,0x0u,0x0u,0x0u,0x7230cu,0x0u,0x0u,0x0u,0x7230cu,0x0u,0x0u,0x0u,0x7230cu,0x0u,0x0u,0x0u,0x7230cu,0x0u,0x0u,0x0u,0x7230cu,0x0u,0x0u,0x0u,0x7230cu,0x0u,0x0u,0x0u,0x7230cu,0x0u,0x0u,0x0u,0x0u,0x40u,0x9000u,0xa800u,0x80u,0xc0u,0x9010u,0xa810u,0x110u,0x150u,0x9020u,0xa820u,0x190u,0x1d0u,0x9030u,0xa830u,0x220u,0x260u,0x9040u,0xa840u,0x2a0u,0x2e0u,0x9050u,0xa850u,0x330u,0x370u,0x9060u,0xa860u,0x3b0u,0x3f0u,0x9070u,0xa870u,0x440u,0x400u,0x9080u,0xa880u,0x4c0u,0x480u,0x9090u,0xa890u,0x550u,0x510u,0x90a0u,0xa8a0u,0x5d0u,0x590u,0x90b0u,0xa8b0u,0x660u,0x620u,0x90c0u,0xa8c0u,0x6e0u,0x6a0u,0x90d0u,0xa8d0u,0x770u,0x730u,0x90e0u,0xa8e0u,0x7f0u,0x7b0u,0x90f0u,0xa8f0u,0x10u,0x50u,0x9300u,0xab00u,0x90u,0xd0u,0x9310u,0xab10u,0x100u,0x140u,0x9320u,0xab20u,0x180u,0x1c0u,0x9330u,0xab30u,0x230u,0x270u,0x9340u,0xab40u,0x2b0u,0x2f0u,0x9350u,0xab50u,0x320u,0x360u,0x9360u,0xab60u,0x3a0u,0x3e0u,0x9370u,0xab70u,0x450u,0x410u,0x9380u,0xab80u,0x4d0u,0x490u,0x9390u,0xab90u,0x540u,0x500u,0x93a0u,0xaba0u,0x5c0u,0x580u,0x93b0u,0xabb0u,0x670u,0x630u,0x93c0u,0xabc0u,0x6f0u,0x6b0u,0x93d0u,0xabd0u,0x760u,0x720u,0x93e0u,0xabe0u,0x7e0u,0x7a0u,0x93f0u,0xabf0u,0x20u,0x60u,0x9600u,0xae00u,0xa0u,0xe0u,0x9610u,0xae10u,0x130u,0x170u,0x9620u,0xae20u,0x1b0u,0x1f0u,0x9630u,0xae30u,0x200u,0x240u,0x9640u,0xae40u,0x280u,0x2c0u,0x9650u,0xae50u,0x310u,0x350u,0x9660u,0xae60u,0x390u,0x3d0u,0x9670u,0xae70u,0x460u,0x420u,0x9680u,0xae80u,0x4e0u,0x4a0u,0x9690u,0xae90u,0x570u,0x530u,0x96a0u,0xaea0u,0x5f0u,0x5b0u,0x96b0u,0xaeb0u,0x640u,0x600u,0x96c0u,0xaec0u,0x6c0u,0x680u,0x96d0u,0xaed0u,0x750u,0x710u,0x96e0u,0xaee0u,0x7d0u,0x790u,0x96f0u,0xaef0u,0x30u,0x70u,0x9900u,0xb100u,0xb0u,0xf0u,0x9910u,0xb110u,0x120u,0x160u,0x9920u,0xb120u,0x1a0u,0x1e0u,0x9930u,0xb130u,0x210u,0x250u,0x9940u,0xb140u,0x290u,0x2d0u,0x9950u,0xb150u,0x300u,0x340u,0x9960u,0xb160u,0x380u,0x3c0u,0x9970u,0xb170u,0x470u,0x430u,0x9980u,0xb180u,0x4f0u,0x4b0u,0x9990u,0xb190u,0x560u,0x520u,0x99a0u,0xb1a0u,0x5e0u,0x5a0u,0x99b0u,0xb1b0u,0x650u,0x610u,0x99c0u,0xb1c0u,0x6d0u,0x690u,0x99d0u,0xb1d0u,0x740u,0x700u,0x99e0u,0xb1e0u,0x7c0u,0x780u,0x99f0u,0xb1f0u,0x9c08u,0x12060u,0x120a0u,0x12400u,0x9c18u,0x121f0u,0x12230u,0x12410u,0x9c28u,0x12340u,0x12380u,0x12420u,0x9c38u,0x124d0u,0x12510u,0x12430u,0x9c48u,0x12620u,0x126e0u,0x12440u,0x9c58u,0x127b0u,0x12870u,0x12450u,0x9c68u,0x12900u,0x129c0u,0x12460u,0x9c78u,0x12a90u,0x12b50u,0x12470u,0x9c88u,0x12c60u,0x12ca0u,0x12480u,0x9c98u,0x12df0u,0x12e30u,0x12490u,0x9ca8u,0x12f40u,0x12f80u,0x124a0u,0x9cb8u,0x130d0u,0x13110u,0x124b0u,0x9cc8u,0x13220u,0x132e0u,0x124c0u,0x9cd8u,0x133b0u,0x13470u,0x124d0u,0x9ce8u,0x13500u,0x135c0u,0x124e0u,0x9cf8u,0x13690u,0x13750u,0x124f0u,0x9f08u,0x12070u,0x120a8u,0x12500u,0x9f18u,0x121e0u,0x12238u,0x12510u,0x9f28u,0x12350u,0x12388u,0x12520u,0x9f38u,0x124c0u,0x12518u,0x12530u,0x9f48u,0x12630u,0x126e8u,0x12540u,0x9f58u,0x127a0u,0x12878u,0x12550u,0x9f68u,0x12910u,0x129c8u,0x12560u,0x9f78u,0x12a80u,0x12b58u,0x12570u,0x9f88u,0x12c70u,0x12ca8u,0x12580u,0x9f98u,0x12de0u,0x12e38u,0x12590u,0x9fa8u,0x12f50u,0x12f88u,0x125a0u,0x9fb8u,0x130c0u,0x13118u,0x125b0u,0x9fc8u,0x13230u,0x132e8u,0x125c0u,0x9fd8u,0x133a0u,0x13478u,0x125d0u,0x9fe8u,0x13510u,0x135c8u,0x125e0u,0x9ff8u,0x13680u,0x13758u,0x125f0u,0xa208u,0x12080u,0x120b0u,0x12600u,0xa218u,0x12210u,0x12220u,0x12610u,0xa228u,0x123a0u,0x12390u,0x12620u,0xa238u,0x12530u,0x12500u,0x12630u,0xa248u,0x126c0u,0x126f0u,0x12640u,0xa258u,0x12850u,0x12860u,0x12650u,0xa268u,0x129e0u,0x129d0u,0x12660u,0xa278u,0x12b70u,0x12b40u,0x12670u,0xa288u,0x12c80u,0x12cb0u,0x12680u,0xa298u,0x12e10u,0x12e20u,0x12690u,0xa2a8u,0x12fa0u,0x12f90u,0x126a0u,0xa2b8u,0x13130u,0x13100u,0x126b0u,0xa2c8u,0x132c0u,0x132f0u,0x126c0u,0xa2d8u,0x13450u,0x13460u,0x126d0u,0xa2e8u,0x135e0u,0x135d0u,0x126e0u,0xa2f8u,0x13770u,0x13740u,0x126f0u,0xa508u,0x12090u,0x120b8u,0x12700u,0xa518u,0x12200u,0x12228u,0x12710u,0xa528u,0x123b0u,0x12398u,0x12720u,0xa538u,0x12520u,0x12508u,0x12730u,0xa548u,0x126d0u,0x126f8u,0x12740u,0xa558u,0x12840u,0x12868u,0x12750u,0xa568u,0x129f0u,0x129d8u,0x12760u,0xa578u,0x12b60u,0x12b48u,0x12770u,0xa588u,0x12c90u,0x12cb8u,0x12780u,0xa598u,0x12e00u,0x12e28u,0x12790u,0xa5a8u,0x12fb0u,0x12f98u,0x127a0u,0xa5b8u,0x13120u,0x13108u,0x127b0u,0xa5c8u,0x132d0u,0x132f8u,0x127c0u,0xa5d8u,0x13440u,0x13468u,0x127d0u,0xa5e8u,0x135f0u,0x135d8u,0x127e0u,0xa5f8u,0x13760u,0x13748u,0x127f0u,0x25188u,0x25180u,0x1b0c0u,0x1b360u,0x25198u,0x25190u,0x100000u,0x1b362u,0x251a8u,0x251a0u,0x100000u,0x1b364u,0x251b8u,0x251b0u,0x100000u,0x1b366u,0x251c8u,0x251c0u,0x1b0c0u,0x1b368u,0x251d8u,0x251d0u,0x100000u,0x1b36au,0x251e8u,0x251e0u,0x100000u,0x1b36cu,0x251f8u,0x251f0u,0x100000u,0x1b36eu,0x25208u,0x25200u,0x1b0c0u,0x1b370u,0x25218u,0x25210u,0x100000u,0x1b372u,0x25228u,0x25220u,0x100000u,0x1b374u,0x25238u,0x25230u,0x100000u,0x1b376u,0x25248u,0x25240u,0x1b0c0u,0x1b378u,0x25258u,0x25250u,0x100000u,0x1b37au,0x25268u,0x25260u,0x100000u,0x1b37cu,0x25278u,0x25270u,0x100000u,0x1b37eu,0x2518cu,0x25280u,0x1b0f0u,0x1b360u,0x2519cu,0x25290u,0x100000u,0x1b362u,0x251acu,0x252a0u,0x100000u,0x1b364u,0x251bcu,0x252b0u,0x100000u,0x1b366u,0x251ccu,0x252c0u,0x1b0f0u,0x1b368u,0x251dcu,0x252d0u,0x100000u,0x1b36au,0x251ecu,0x252e0u,0x100000u,0x1b36cu,0x251fcu,0x252f0u,0x100000u,0x1b36eu,0x2520cu,0x25300u,0x1b0f0u,0x1b370u,0x2521cu,0x25310u,0x100000u,0x1b372u,0x2522cu,0x25320u,0x100000u,0x1b374u,0x2523cu,0x25330u,0x100000u,0x1b376u,0x2524cu,0x25340u,0x1b0f0u,0x1b378u,0x2525cu,0x25350u,0x100000u,0x1b37au,0x2526cu,0x25360u,0x100000u,0x1b37cu,0x2527cu,0x25370u,0x100000u,0x1b37eu,0x25288u,0x25380u,0x1b120u,0x1b360u,0x25298u,0x25390u,0x100000u,0x1b362u,0x252a8u,0x253a0u,0x100000u,0x1b364u,0x252b8u,0x253b0u,0x100000u,0x1b366u,0x252c8u,0x253c0u,0x1b120u,0x1b368u,0x252d8u,0x253d0u,0x100000u,0x1b36au,0x252e8u,0x253e0u,0x100000u,0x1b36cu,0x252f8u,0x253f0u,0x100000u,0x1b36eu,0x25308u,0x25400u,0x1b120u,0x1b370u,0x25318u,0x25410u,0x100000u,0x1b372u,0x25328u,0x25420u,0x100000u,0x1b374u,0x25338u,0x25430u,0x100000u,0x1b376u,0x25348u,0x25440u,0x1b120u,0x1b378u,0x25358u,0x25450u,0x100000u,0x1b37au,0x25368u,0x25460u,0x100000u,0x1b37cu,0x25378u,0x25470u,0x100000u,0x1b37eu,0x2528cu,0x25480u,0x1b150u,0x1b360u,0x2529cu,0x25490u,0x100000u,0x1b362u,0x252acu,0x254a0u,0x100000u,0x1b364u,0x252bcu,0x254b0u,0x100000u,0x1b366u,0x252ccu,0x254c0u,0x1b150u,0x1b368u,0x252dcu,0x254d0u,0x100000u,0x1b36au,0x252ecu,0x254e0u,0x100000u,0x1b36cu,0x252fcu,0x254f0u,0x100000u,0x1b36eu,0x2530cu,0x25500u,0x1b150u,0x1b370u,0x2531cu,0x25510u,0x100000u,0x1b372u,0x2532cu,0x25520u,0x100000u,0x1b374u,0x2533cu,0x25530u,0x100000u,0x1b376u,0x2534cu,0x25540u,0x1b150u,0x1b378u,0x2535cu,0x25550u,0x100000u,0x1b37au,0x2536cu,0x25560u,0x100000u,0x1b37cu,0x2537cu,0x25570u,0x100000u,0x1b37eu,0x21e80u,0x26a00u,0x26a00u,0xf0140u,0x21e82u,0x26a02u,0x100000u,0xf0150u,0x21e84u,0x26a04u,0x100000u,0xf0160u,0x21e86u,0x26a06u,0x100000u,0xf0170u,0x22080u,0x26a10u,0x26a00u,0xf0100u,0x22082u,0x26a12u,0x100000u,0xf0110u,0x22084u,0x26a14u,0x100000u,0xf0120u,0x22086u,0x26a16u,0x100000u,0xf0130u,0x22280u,0x26a20u,0x26a00u,0xf02c0u,0x22282u,0x26a22u,0x100000u,0xf02d0u,0x22284u,0x26a24u,0x100000u,0xf02e0u,0x22286u,0x26a26u,0x100000u,0xf02f0u,0x22480u,0x26a30u,0x26a00u,0xf0280u,0x22482u,0x26a32u,0x100000u,0xf0290u,0x22484u,0x26a34u,0x100000u,0xf02a0u,0x22486u,0x26a36u,0x100000u,0xf02b0u,0x21e88u,0x26a08u,0x26a10u,0x11f850u,0x21e8au,0x26a0au,0x100000u,0x11f840u,0x21e8cu,0x26a0cu,0x100000u,0x11f870u,0x21e8eu,0x26a0eu,0x100000u,0x11f860u,0x22088u,0x26a18u,0x26a10u,0x11f810u,0x2208au,0x26a1au,0x100000u,0x11f800u,0x2208cu,0x26a1cu,0x100000u,0x11f830u,0x2208eu,0x26a1eu,0x100000u,0x11f820u,0x22288u,0x26a28u,0x26a10u,0x11f9d0u,0x2228au,0x26a2au,0x100000u,0x11f9c0u,0x2228cu,0x26a2cu,0x100000u,0x11f9f0u,0x2228eu,0x26a2eu,0x100000u,0x11f9e0u,0x22488u,0x26a38u,0x26a10u,0x11f990u,0x2248au,0x26a3au,0x100000u,0x11f980u,0x2248cu,0x26a3cu,0x100000u,0x11f9b0u,0x2248eu,0x26a3eu,0x100000u,0x11f9a0u,0x21688u,0x26a40u,0x26a20u,0x11fb60u,0x2168au,0x26a42u,0x100000u,0x11fb70u,0x2168cu,0x26a44u,0x100000u,0x11fb40u,0x2168eu,0x26a46u,0x100000u,0x11fb50u,0x21888u,0x26a50u,0x26a20u,0x11fb20u,0x2188au,0x26a52u,0x100000u,0x11fb30u,0x2188cu,0x26a54u,0x100000u,0x11fb00u,0x2188eu,0x26a56u,0x100000u,0x11fb10u,0x21a88u,0x26a60u,0x26a20u,0x11fce0u,0x21a8au,0x26a62u,0x100000u,0x11fcf0u,0x21a8cu,0x26a64u,0x100000u,0x11fcc0u,0x21a8eu,0x26a66u,0x100000u,0x11fcd0u,0x21c88u,0x26a70u,0x26a20u,0x11fca0u,0x21c8au,0x26a72u,0x100000u,0x11fcb0u,0x21c8cu,0x26a74u,0x100000u,0x11fc80u,0x21c8eu,0x26a76u,0x100000u,0x11fc90u,0x100000u,0x26a48u,0x26a30u,0x11fe70u,0x100000u,0x26a4au,0x100000u,0x11fe60u,0x100000u,0x26a4cu,0x100000u,0x11fe50u,0x100000u,0x26a4eu,0x100000u,0x11fe40u,0x100000u,0x26a58u,0x26a30u,0x11fe30u,0x100000u,0x26a5au,0x100000u,0x11fe20u,0x100000u,0x26a5cu,0x100000u,0x11fe10u,0x100000u,0x26a5eu,0x100000u,0x11fe00u,0x100000u,0x26a68u,0x26a30u,0x11fff0u,0x100000u,0x26a6au,0x100000u,0x11ffe0u,0x100000u,0x26a6cu,0x100000u,0x11ffd0u,0x100000u,0x26a6eu,0x100000u,0x11ffc0u,0x100000u,0x26a78u,0x26a30u,0x11ffb0u,0x100000u,0x26a7au,0x100000u,0x11ffa0u,0x100000u,0x26a7cu,0x100000u,0x11ff90u,0x100000u,0x26a7eu,0x100000u,0x11ff80u,0x30040u,0xf0040u,0x900c0u,0x30140u,0x30050u,0xf0050u,0x900d0u,0x30150u,0x30060u,0xf0060u,0x900e0u,0x30160u,0x30070u,0xf0070u,0x900f0u,0x30170u,0x30000u,0xf0000u,0x90080u,0x30100u,0x30010u,0xf0010u,0x90090u,0x30110u,0x30020u,0xf0020u,0x900a0u,0x30120u,0x30030u,0xf0030u,0x900b0u,0x30130u,0x301c0u,0xf01c0u,0x90240u,0x302c0u,0x301d0u,0xf01d0u,0x90250u,0x302d0u,0x301e0u,0xf01e0u,0x90260u,0x302e0u,0x301f0u,0xf01f0u,0x90270u,0x302f0u,0x30180u,0xf0180u,0x90200u,0x30280u,0x30190u,0xf0190u,0x90210u,0x30290u,0x301a0u,0xf01a0u,0x90220u,0x302a0u,0x301b0u,0xf01b0u,0x90230u,0x302b0u,0x30350u,0x11f750u,0x903d0u,0x30450u,0x30340u,0x11f740u,0x903c0u,0x30440u,0x30370u,0x11f770u,0x903f0u,0x30470u,0x30360u,0x11f760u,0x903e0u,0x30460u,0x30310u,0x11f710u,0x90390u,0x30410u,0x30300u,0x11f700u,0x90380u,0x30400u,0x30330u,0x11f730u,0x903b0u,0x30430u,0x30320u,0x11f720u,0x903a0u,0x30420u,0x304d0u,0x11f8d0u,0x90550u,0x305d0u,0x304c0u,0x11f8c0u,0x90540u,0x305c0u,0x304f0u,0x11f8f0u,0x90570u,0x305f0u,0x304e0u,0x11f8e0u,0x90560u,0x305e0u,0x30490u,0x11f890u,0x90510u,0x30590u,0x30480u,0x11f880u,0x90500u,0x30580u,0x304b0u,0x11f8b0u,0x90530u,0x305b0u,0x304a0u,0x11f8a0u,0x90520u,0x305a0u,0x30660u,0x11fa60u,0xbfae0u,0x30760u,0x30670u,0x11fa70u,0xbfaf0u,0x30770u,0x30640u,0x11fa40u,0xbfac0u,0x30740u,0x30650u,0x11fa50u,0xbfad0u,0x30750u,0x30620u,0x11fa20u,0xbfaa0u,0x30720u,0x30630u,0x11fa30u,0xbfab0u,0x30730u,0x30600u,0x11fa00u,0xbfa80u,0x30700u,0x30610u,0x11fa10u,0xbfa90u,0x30710u,0x307e0u,0x11fbe0u,0xbfc60u,0x308e0u,0x307f0u,0x11fbf0u,0xbfc70u,0x308f0u,0x307c0u,0x11fbc0u,0xbfc40u,0x308c0u,0x307d0u,0x11fbd0u,0xbfc50u,0x308d0u,0x307a0u,0x11fba0u,0xbfc20u,0x308a0u,0x307b0u,0x11fbb0u,0xbfc30u,0x308b0u,0x30780u,0x11fb80u,0xbfc00u,0x30880u,0x30790u,0x11fb90u,0xbfc10u,0x30890u,0x5fd70u,0x11fd70u,0xbfdf0u,0x5fe70u,0x5fd60u,0x11fd60u,0xbfde0u,0x5fe60u,0x5fd50u,0x11fd50u,0xbfdd0u,0x5fe50u,0x5fd40u,0x11fd40u,0xbfdc0u,0x5fe40u,0x5fd30u,0x11fd30u,0xbfdb0u,0x5fe30u,0x5fd20u,0x11fd20u,0xbfda0u,0x5fe20u,0x5fd10u,0x11fd10u,0xbfd90u,0x5fe10u,0x5fd00u,0x11fd00u,0xbfd80u,0x5fe00u,0x5fef0u,0x11fef0u,0xbff70u,0x5fff0u,0x5fee0u,0x11fee0u,0xbff60u,0x5ffe0u,0x5fed0u,0x11fed0u,0xbff50u,0x5ffd0u,0x5fec0u,0x11fec0u,0xbff40u,0x5ffc0u,0x5feb0u,0x11feb0u,0xbff30u,0x5ffb0u,0x5fea0u,0x11fea0u,0xbff20u,0x5ffa0u,0x5fe90u,0x11fe90u,0xbff10u,0x5ff90u,0x5fe80u,0x11fe80u,0xbff00u,0x5ff80u,0x420u,0x30850u,0x90000u,0xc0420u,0x430u,0x30840u,0x90010u,0xc0430u,0x400u,0x30870u,0x90020u,0xc0400u,0x410u,0x30860u,0x90030u,0xc0410u,0x460u,0x30810u,0x90040u,0xc0460u,0x470u,0x30800u,0x90050u,0xc0470u,0x440u,0x30830u,0x90060u,0xc0440u,0x450u,0x30820u,0x90070u,0xc0450u,0x4b0u,0x308d0u,0x90080u,0xef8b0u,0x4a0u,0x308c0u,0x90090u,0xef8a0u,0x490u,0x308f0u,0x900a0u,0xef890u,0x480u,0x308e0u,0x900b0u,0xef880u,0x4f0u,0x30890u,0x900c0u,0xef8f0u,0x4e0u,0x30880u,0x900d0u,0xef8e0u,0x4d0u,0x308b0u,0x900e0u,0xef8d0u,0x4c0u,0x308a0u,0x900f0u,0xef8c0u,0x530u,0x5fd60u,0x90100u,0xef930u,0x520u,0x5fd70u,0x90110u,0xef920u,0x510u,0x5fd40u,0x90120u,0xef910u,0x500u,0x5fd50u,0x90130u,0xef900u,0x570u,0x5fd20u,0x90140u,0xef970u,0x560u,0x5fd30u,0x90150u,0xef960u,0x550u,0x5fd00u,0x90160u,0xef950u,0x540u,0x5fd10u,0x90170u,0xef940u,0x5b0u,0x5fde0u,0x90190u,0xef9b0u,0x5a0u,0x5fdf0u,0x90180u,0xef9a0u,0x590u,0x5fdc0u,0x901b0u,0xef990u,0x580u,0x5fdd0u,0x901a0u,0xef980u,0x5f0u,0x5fda0u,0x901d0u,0xef9f0u,0x5e0u,0x5fdb0u,0x901c0u,0xef9e0u,0x5d0u,0x5fd80u,0x901f0u,0xef9d0u,0x5c0u,0x5fd90u,0x901e0u,0xef9c0u,0x640u,0x5fe60u,0x90210u,0xefa40u,0x650u,0x5fe70u,0x90200u,0xefa50u,0x660u,0x5fe40u,0x90230u,0xefa60u,0x670u,0x5fe50u,0x90220u,0xefa70u,0x600u,0x5fe20u,0x90250u,0xefa00u,0x610u,0x5fe30u,0x90240u,0xefa10u,0x620u,0x5fe00u,0x90270u,0xefa20u,0x630u,0x5fe10u,0x90260u,0xefa30u,0x6c0u,0x5fef0u,0x90290u,0xefac0u,0x6d0u,0x5fee0u,0x90280u,0xefad0u,0x6e0u,0x5fed0u,0x902b0u,0xefae0u,0x6f0u,0x5fec0u,0x902a0u,0xefaf0u,0x680u,0x5feb0u,0x902d0u,0xefa80u,0x690u,0x5fea0u,0x902c0u,0xefa90u,0x6a0u,0x5fe90u,0x902f0u,0xefaa0u,0x6b0u,0x5fe80u,0x902e0u,0xefab0u,0x740u,0x5ff70u,0x90320u,0xefb40u,0x750u,0x5ff60u,0x90330u,0xefb50u,0x760u,0x5ff50u,0x90300u,0xefb60u,0x770u,0x5ff40u,0x90310u,0xefb70u,0x700u,0x5ff30u,0x90360u,0xefb00u,0x710u,0x5ff20u,0x90370u,0xefb10u,0x720u,0x5ff10u,0x90340u,0xefb20u,0x730u,0x5ff00u,0x90350u,0xefb30u,0x7d0u,0x5fff0u,0x903a0u,0xefbd0u,0x7c0u,0x5ffe0u,0x903b0u,0xefbc0u,0x7f0u,0x5ffd0u,0x90380u,0xefbf0u,0x7e0u,0x5ffc0u,0x90390u,0xefbe0u,0x790u,0x5ffb0u,0x903e0u,0xefb90u,0x780u,0x5ffa0u,0x903f0u,0xefb80u,0x7b0u,0x5ff90u,0x903c0u,0xefbb0u,0x7a0u,0x5ff80u,0x903d0u,0xefba0u,0x0u,0x11fc50u,0x0u,0x400u,0x0u,0x11fc40u,0x0u,0x410u,0x0u,0x11fc70u,0x0u,0x420u,0x0u,0x11fc60u,0x0u,0x430u,0x0u,0x11fc10u,0x0u,0x440u,0x0u,0x11fc00u,0x0u,0x450u,0x0u,0x11fc30u,0x0u,0x460u,0x0u,0x11fc20u,0x0u,0x470u,0x0u,0x11fcd0u,0x0u,0x480u,0x0u,0x11fcc0u,0x0u,0x490u,0x0u,0x11fcf0u,0x0u,0x4a0u,0x0u,0x11fce0u,0x0u,0x4b0u,0x0u,0x11fc90u,0x0u,0x4c0u,0x0u,0x11fc80u,0x0u,0x4d0u,0x0u,0x11fcb0u,0x0u,0x4e0u,0x0u,0x11fca0u,0x0u,0x4f0u,0x0u,0x11fd60u,0x0u,0x500u,0x0u,0x11fd70u,0x0u,0x510u,0x0u,0x11fd40u,0x0u,0x520u,0x0u,0x11fd50u,0x0u,0x530u,0x0u,0x11fd20u,0x0u,0x540u,0x0u,0x11fd30u,0x0u,0x550u,0x0u,0x11fd00u,0x0u,0x560u,0x0u,0x11fd10u,0x0u,0x570u,0x0u,0x11fde0u,0x0u,0x580u,0x0u,0x11fdf0u,0x0u,0x590u,0x0u,0x11fdc0u,0x0u,0x5a0u,0x0u,0x11fdd0u,0x0u,0x5b0u,0x0u,0x11fda0u,0x0u,0x5c0u,0x0u,0x11fdb0u,0x0u,0x5d0u,0x0u,0x11fd80u,0x0u,0x5e0u,0x0u,0x11fd90u,0x0u,0x5f0u,0x0u,0x11fe60u,0x0u,0x600u,0x0u,0x11fe70u,0x0u,0x610u,0x0u,0x11fe40u,0x0u,0x620u,0x0u,0x11fe50u,0x0u,0x630u,0x0u,0x11fe20u,0x0u,0x640u,0x0u,0x11fe30u,0x0u,0x650u,0x0u,0x11fe00u,0x0u,0x660u,0x0u,0x11fe10u,0x0u,0x670u,0x0u,0x11fef0u,0x0u,0x680u,0x0u,0x11fee0u,0x0u,0x690u,0x0u,0x11fed0u,0x0u,0x6a0u,0x0u,0x11fec0u,0x0u,0x6b0u,0x0u,0x11feb0u,0x0u,0x6c0u,0x0u,0x11fea0u,0x0u,0x6d0u,0x0u,0x11fe90u,0x0u,0x6e0u,0x0u,0x11fe80u,0x0u,0x6f0u,0x0u,0x11ff70u,0x0u,0x700u,0x0u,0x11ff60u,0x0u,0x710u,0x0u,0x11ff50u,0x0u,0x720u,0x0u,0x11ff40u,0x0u,0x730u,0x0u,0x11ff30u,0x0u,0x740u,0x0u,0x11ff20u,0x0u,0x750u,0x0u,0x11ff10u,0x0u,0x760u,0x0u,0x11ff00u,0x0u,0x770u,0x0u,0x11fff0u,0x0u,0x780u,0x0u,0x11ffe0u,0x0u,0x790u,0x0u,0x11ffd0u,0x0u,0x7a0u,0x0u,0x11ffc0u,0x0u,0x7b0u,0x0u,0x11ffb0u,0x0u,0x7c0u,0x0u,0x11ffa0u,0x0u,0x7d0u,0x0u,0x11ff90u,0x0u,0x7e0u,0x0u,0x11ff80u,0x0u,0x7f0u,0x0u,0x1100u,0x0u,0x26184u,0x0u,0x1104u,0x0u,0x26194u,0x0u,0x1108u,0x0u,0x261a4u,0x0u,0x110cu,0x0u,0x261b4u,0x0u,0x1110u,0x0u,0x261c4u,0x0u,0x1114u,0x0u,0x261d4u,0x0u,0x1118u,0x0u,0x261e4u,0x0u,0x111cu,0x0u,0x261f4u,0x0u,0x1120u,0x0u,0x26204u,0x0u,0x1124u,0x0u,0x26214u,0x0u,0x1128u,0x0u,0x26224u,0x0u,0x112cu,0x0u,0x26234u,0x0u,0x1130u,0x0u,0x26244u,0x0u,0x1134u,0x0u,0x26254u,0x0u,0x1138u,0x0u,0x26264u,0x0u,0x113cu,0x0u,0x26274u,0x0u,0x1140u,0x0u,0x26284u,0x0u,0x1144u,0x0u,0x26294u,0x0u,0x1148u,0x0u,0x262a4u,0x0u,0x114cu,0x0u,0x262b4u,0x0u,0x1150u,0x0u,0x262c4u,0x0u,0x1154u,0x0u,0x262d4u,0x0u,0x1158u,0x0u,0x262e4u,0x0u,0x115cu,0x0u,0x262f4u,0x0u,0x1160u,0x0u,0x26304u,0x0u,0x1164u,0x0u,0x26314u,0x0u,0x1168u,0x0u,0x26324u,0x0u,0x116cu,0x0u,0x26334u,0x0u,0x1170u,0x0u,0x26344u,0x0u,0x1174u,0x0u,0x26354u,0x0u,0x1178u,0x0u,0x26364u,0x0u,0x117cu,0x0u,0x26374u,0x0u,0x1180u,0x0u,0x26384u,0x0u,0x1184u,0x0u,0x26394u,0x0u,0x1188u,0x0u,0x263a4u,0x0u,0x118cu,0x0u,0x263b4u,0x0u,0x1190u,0x0u,0x263c4u,0x0u,0x1194u,0x0u,0x263d4u,0x0u,0x1198u,0x0u,0x263e4u,0x0u,0x119cu,0x0u,0x263f4u,0x0u,0x11a0u,0x0u,0x26404u,0x0u,0x11a4u,0x0u,0x26414u,0x0u,0x11a8u,0x0u,0x26424u,0x0u,0x11acu,0x0u,0x26434u,0x0u,0x11b0u,0x0u,0x26444u,0x0u,0x11b4u,0x0u,0x26454u,0x0u,0x11b8u,0x0u,0x26464u,0x0u,0x11bcu,0x0u,0x26474u,0x0u,0x11c0u,0x0u,0x26484u,0x0u,0x11c4u,0x0u,0x26494u,0x0u,0x11c8u,0x0u,0x264a4u,0x0u,0x11ccu,0x0u,0x264b4u,0x0u,0x11d0u,0x0u,0x264c4u,0x0u,0x11d4u,0x0u,0x264d4u,0x0u,0x11d8u,0x0u,0x264e4u,0x0u,0x11dcu,0x0u,0x264f4u,0x0u,0x11e0u,0x0u,0x26504u,0x0u,0x11e4u,0x0u,0x26514u,0x0u,0x11e8u,0x0u,0x26524u,0x0u,0x11ecu,0x0u,0x26534u,0x0u,0x11f0u,0x0u,0x26544u,0x0u,0x11f4u,0x0u,0x26554u,0x0u,0x11f8u,0x0u,0x26564u,0x0u,0x11fcu,0x0u,0x26574u,0x3f803f80u,0x3f803f80u,0x3f803f80u,0x3f803f80u,0x0u,0x0u,0x0u,0x0u,0x0u,0x0u,0x0u,0x0u,0x0u,0x0u,0x0u,0x0u,0x3f803f80u,0x3f803f80u,0x3f803f80u,0x3f803f80u,0x0u,0x0u,0x0u,0x0u,0x0u,0x0u,0x0u,0x0u,0x0u,0x0u,0x0u,0x0u,0x3f803f80u,0x3f803f80u,0x3f803f80u,0x3f803f80u,0x0u,0x0u,0x0u,0x0u,0x0u,0x0u,0x0u,0x0u,0x0u,0x0u,0x0u,0x0u,0x3f803f80u,0x3f803f80u,0x3f803f80u,0x3f803f80u,0x0u,0x0u,0x0u,0x0u,0x0u,0x0u,0x0u,0x0u,0x0u,0x0u,0x0u,0x0u,0x3f803f80u,0x3f803f80u,0x3f803f80u,0x3f803f80u,0x0u,0x0u,0x0u,0x0u,0x0u,0x0u,0x0u,0x0u,0x0u,0x0u,0x0u,0x0u,0x3f803f80u,0x3f803f80u,0x3f803f80u,0x3f803f80u,0x0u,0x0u,0x0u,0x0u,0x0u,0x0u,0x0u,0x0u,0x0u,0x0u,0x0u,0x0u,0x3f803f80u,0x3f803f80u,0x3f803f80u,0x3f803f80u,0x0u,0x0u,0x0u,0x0u,0x0u,0x0u,0x0u,0x0u,0x0u,0x0u,0x0u,0x0u,0x3f803f80u,0x3f803f80u,0x3f803f80u,0x3f803f80u,0x0u,0x0u,0x0u,0x0u,0x0u,0x0u,0x0u,0x0u,0x0u,0x0u,0x0u,0x0u,0x3f803f80u,0x3f803f80u,0x3f803f80u,0x3f803f80u,0x0u,0x0u,0x0u,0x0u,0x0u,0x0u,0x0u,0x0u,0x0u,0x0u,0x0u,0x0u,0x3f803f80u,0x3f803f80u,0x3f803f80u,0x3f803f80u,0x0u,0x0u,0x0u,0x0u,0x0u,0x0u,0x0u,0x0u,0x0u,0x0u,0x0u,0x0u,0x3f803f80u,0x3f803f80u,0x3f803f80u,0x3f803f80u,0x0u,0x0u,0x0u,0x0u,0x0u,0x0u,0x0u,0x0u,0x0u,0x0u,0x0u,0x0u,0x3f803f80u,0x3f803f80u,0x3f803f80u,0x3f803f80u,0x0u,0x0u,0x0u,0x0u,0x0u,0x0u,0x0u,0x0u,0x0u,0x0u,0x0u,0x0u,0x3f803f80u,0x3f803f80u,0x3f803f80u,0x3f803f80u,0x0u,0x0u,0x0u,0x0u,0x0u,0x0u,0x0u,0x0u,0x0u,0x0u,0x0u,0x0u,0x3f803f80u,0x3f803f80u,0x3f803f80u,0x3f803f80u,0x0u,0x0u,0x0u,0x0u,0x0u,0x0u,0x0u,0x0u,0x0u,0x0u,0x0u,0x0u,0x3f803f80u,0x3f803f80u,0x3f803f80u,0x3f803f80u,0x0u,0x0u,0x0u,0x0u,0x0u,0x0u,0x0u,0x0u,0x0u,0x0u,0x0u,0x0u,0x3f803f80u,0x3f803f80u,0x3f803f80u,0x3f803f80u,0x0u,0x0u,0x0u,0x0u,0x0u,0x0u,0x0u,0x0u,0x0u,0x0u,0x0u,0x0u,0x7230cu,0x0u,0x0u,0x0u,0x7230cu,0x0u,0x0u,0x0u,0x7230cu,0x0u,0x0u,0x0u,0x7230cu,0x0u,0x0u,0x0u,0x7230cu,0x0u,0x0u,0x0u,0x7230cu,0x0u,0x0u,0x0u,0x7230cu,0x0u,0x0u,0x0u,0x7230cu,0x0u,0x0u,0x0u,0x5232cu,0x0u,0x0u,0x0u,0x5232cu,0x0u,0x0u,0x0u,0x5232cu,0x0u,0x0u,0x0u,0x5232cu,0x0u,0x0u,0x0u,0x5232cu,0x0u,0x0u,0x0u,0x5232cu,0x0u,0x0u,0x0u,0x5232cu,0x0u,0x0u,0x0u,0x5232cu,0x0u,0x0u,0x0u,0xd0124u,0x0u,0x0u,0x0u,0xd0124u,0x0u,0x0u,0x0u,0xd0124u,0x0u,0x0u,0x0u,0xd0124u,0x0u,0x0u,0x0u,0xd0124u,0x0u,0x0u,0x0u,0xd0124u,0x0u,0x0u,0x0u,0xd0124u,0x0u,0x0u,0x0u,0xd0124u,0x0u,0x0u,0x0u,0xd81a6u,0x0u,0x0u,0x0u,0xd81a6u,0x0u,0x0u,0x0u,0xd81a6u,0x0u,0x0u,0x0u,0xd81a6u,0x0u,0x0u,0x0u,0xd81a6u,0x0u,0x0u,0x0u,0xd81a6u,0x0u,0x0u,0x0u,0xd81a6u,0x0u,0x0u,0x0u,0xd81a6u,0x0u,0x0u,0x0u,0xd8996u,0x0u,0x0u,0x0u,0xd8996u,0x0u,0x0u,0x0u,0xd8996u,0x0u,0x0u,0x0u,0xd8996u,0x0u,0x0u,0x0u,0xd8996u,0x0u,0x0u,0x0u,0xd8996u,0x0u,0x0u,0x0u,0xd8996u,0x0u,0x0u,0x0u,0xd8996u,0x0u,0x0u,0x0u,0xd8996u,0x0u,0x0u,0x0u,0xd8996u,0x0u,0x0u,0x0u,0xd8996u,0x0u,0x0u,0x0u,0xd8996u,0x0u,0x0u,0x0u,0xd8996u,0x0u,0x0u,0x0u,0xd8996u,0x0u,0x0u,0x0u,0xd8996u,0x0u,0x0u,0x0u,0xd8996u,0x0u,0x0u,0x0u,0xd8b1cu,0x0u,0x0u,0x0u,0xd8b1cu,0x0u,0x0u,0x0u,0xd8b1cu,0x0u,0x0u,0x0u,0xd8b1cu,0x0u,0x0u,0x0u,0xd8b1cu,0x0u,0x0u,0x0u,0xd8b1cu,0x0u,0x0u,0x0u,0xd8b1cu,0x0u,0x0u,0x0u,0xd8b1cu,0x0u,0x0u,0x0u,0xd8b1cu,0x0u,0x0u,0x0u,0xd8b1cu,0x0u,0x0u,0x0u,0xd8b1cu,0x0u,0x0u,0x0u,0xd8b1cu,0x0u,0x0u,0x0u,0xd8b1cu,0x0u,0x0u,0x0u,0xd8b1cu,0x0u,0x0u,0x0u,0xd8b1cu,0x0u,0x0u,0x0u,0xd8b1cu,0x0u,0x0u,0x0u,0x0u,0x40u,0x9000u,0xb400u,0x80u,0xc0u,0x9010u,0xb410u,0x110u,0x150u,0x9020u,0xb420u,0x190u,0x1d0u,0x9030u,0xb430u,0x220u,0x260u,0x9040u,0xb440u,0x2a0u,0x2e0u,0x9050u,0xb450u,0x330u,0x370u,0x9060u,0xb460u,0x3b0u,0x3f0u,0x9070u,0xb470u,0x440u,0x400u,0x9080u,0xb480u,0x4c0u,0x480u,0x9090u,0xb490u,0x550u,0x510u,0x90a0u,0xb4a0u,0x5d0u,0x590u,0x90b0u,0xb4b0u,0x660u,0x620u,0x90c0u,0xb4c0u,0x6e0u,0x6a0u,0x90d0u,0xb4d0u,0x770u,0x730u,0x90e0u,0xb4e0u,0x7f0u,0x7b0u,0x90f0u,0xb4f0u,0x10u,0x50u,0x9300u,0xb700u,0x90u,0xd0u,0x9310u,0xb710u,0x100u,0x140u,0x9320u,0xb720u,0x180u,0x1c0u,0x9330u,0xb730u,0x230u,0x270u,0x9340u,0xb740u,0x2b0u,0x2f0u,0x9350u,0xb750u,0x320u,0x360u,0x9360u,0xb760u,0x3a0u,0x3e0u,0x9370u,0xb770u,0x450u,0x410u,0x9380u,0xb780u,0x4d0u,0x490u,0x9390u,0xb790u,0x540u,0x500u,0x93a0u,0xb7a0u,0x5c0u,0x580u,0x93b0u,0xb7b0u,0x670u,0x630u,0x93c0u,0xb7c0u,0x6f0u,0x6b0u,0x93d0u,0xb7d0u,0x760u,0x720u,0x93e0u,0xb7e0u,0x7e0u,0x7a0u,0x93f0u,0xb7f0u,0x20u,0x60u,0x9600u,0xba00u,0xa0u,0xe0u,0x9610u,0xba10u,0x130u,0x170u,0x9620u,0xba20u,0x1b0u,0x1f0u,0x9630u,0xba30u,0x200u,0x240u,0x9640u,0xba40u,0x280u,0x2c0u,0x9650u,0xba50u,0x310u,0x350u,0x9660u,0xba60u,0x390u,0x3d0u,0x9670u,0xba70u,0x460u,0x420u,0x9680u,0xba80u,0x4e0u,0x4a0u,0x9690u,0xba90u,0x570u,0x530u,0x96a0u,0xbaa0u,0x5f0u,0x5b0u,0x96b0u,0xbab0u,0x640u,0x600u,0x96c0u,0xbac0u,0x6c0u,0x680u,0x96d0u,0xbad0u,0x750u,0x710u,0x96e0u,0xbae0u,0x7d0u,0x790u,0x96f0u,0xbaf0u,0x30u,0x70u,0x9900u,0xbd00u,0xb0u,0xf0u,0x9910u,0xbd10u,0x120u,0x160u,0x9920u,0xbd20u,0x1a0u,0x1e0u,0x9930u,0xbd30u,0x210u,0x250u,0x9940u,0xbd40u,0x290u,0x2d0u,0x9950u,0xbd50u,0x300u,0x340u,0x9960u,0xbd60u,0x380u,0x3c0u,0x9970u,0xbd70u,0x470u,0x430u,0x9980u,0xbd80u,0x4f0u,0x4b0u,0x9990u,0xbd90u,0x560u,0x520u,0x99a0u,0xbda0u,0x5e0u,0x5a0u,0x99b0u,0xbdb0u,0x650u,0x610u,0x99c0u,0xbdc0u,0x6d0u,0x690u,0x99d0u,0xbdd0u,0x740u,0x700u,0x99e0u,0xbde0u,0x7c0u,0x780u,0x99f0u,0xbdf0u,0xc000u,0x120c0u,0x12100u,0x12800u,0xc010u,0x12250u,0x12290u,0x12810u,0xc020u,0x123e0u,0x12420u,0x12820u,0xc030u,0x12570u,0x125b0u,0x12830u,0xc040u,0x12680u,0x12740u,0x12840u,0xc050u,0x12810u,0x128d0u,0x12850u,0xc060u,0x129a0u,0x12a60u,0x12860u,0xc070u,0x12b30u,0x12bf0u,0x12870u,0xc080u,0x12cc0u,0x12d00u,0x12880u,0xc090u,0x12e50u,0x12e90u,0x12890u,0xc0a0u,0x12fe0u,0x13020u,0x128a0u,0xc0b0u,0x13170u,0x131b0u,0x128b0u,0xc0c0u,0x13280u,0x13340u,0x128c0u,0xc0d0u,0x13410u,0x134d0u,0x128d0u,0xc0e0u,0x135a0u,0x13660u,0x128e0u,0xc0f0u,0x13730u,0x137f0u,0x128f0u,0xc300u,0x120d0u,0x12108u,0x12900u,0xc310u,0x12240u,0x12298u,0x12910u,0xc320u,0x123f0u,0x12428u,0x12920u,0xc330u,0x12560u,0x125b8u,0x12930u,0xc340u,0x12690u,0x12748u,0x12940u,0xc350u,0x12800u,0x128d8u,0x12950u,0xc360u,0x129b0u,0x12a68u,0x12960u,0xc370u,0x12b20u,0x12bf8u,0x12970u,0xc380u,0x12cd0u,0x12d08u,0x12980u,0xc390u,0x12e40u,0x12e98u,0x12990u,0xc3a0u,0x12ff0u,0x13028u,0x129a0u,0xc3b0u,0x13160u,0x131b8u,0x129b0u,0xc3c0u,0x13290u,0x13348u,0x129c0u,0xc3d0u,0x13400u,0x134d8u,0x129d0u,0xc3e0u,0x135b0u,0x13668u,0x129e0u,0xc3f0u,0x13720u,0x137f8u,0x129f0u,0xc600u,0x120e0u,0x12110u,0x12a00u,0xc610u,0x12270u,0x12280u,0x12a10u,0xc620u,0x123c0u,0x12430u,0x12a20u,0xc630u,0x12550u,0x125a0u,0x12a30u,0xc640u,0x126a0u,0x12750u,0x12a40u,0xc650u,0x12830u,0x128c0u,0x12a50u,0xc660u,0x12980u,0x12a70u,0x12a60u,0xc670u,0x12b10u,0x12be0u,0x12a70u,0xc680u,0x12ce0u,0x12d10u,0x12a80u,0xc690u,0x12e70u,0x12e80u,0x12a90u,0xc6a0u,0x12fc0u,0x13030u,0x12aa0u,0xc6b0u,0x13150u,0x131a0u,0x12ab0u,0xc6c0u,0x132a0u,0x13350u,0x12ac0u,0xc6d0u,0x13430u,0x134c0u,0x12ad0u,0xc6e0u,0x13580u,0x13670u,0x12ae0u,0xc6f0u,0x13710u,0x137e0u,0x12af0u,0xc900u,0x120f0u,0x12118u,0x12b00u,0xc910u,0x12260u,0x12288u,0x12b10u,0xc920u,0x123d0u,0x12438u,0x12b20u,0xc930u,0x12540u,0x125a8u,0x12b30u,0xc940u,0x126b0u,0x12758u,0x12b40u,0xc950u,0x12820u,0x128c8u,0x12b50u,0xc960u,0x12990u,0x12a78u,0x12b60u,0xc970u,0x12b00u,0x12be8u,0x12b70u,0xc980u,0x12cf0u,0x12d18u,0x12b80u,0xc990u,0x12e60u,0x12e88u,0x12b90u,0xc9a0u,0x12fd0u,0x13038u,0x12ba0u,0xc9b0u,0x13140u,0x131a8u,0x12bb0u,0xc9c0u,0x132b0u,0x13358u,0x12bc0u,0xc9d0u,0x13420u,0x134c8u,0x12bd0u,0xc9e0u,0x13590u,0x13678u,0x12be0u,0xc9f0u,0x13700u,0x137e8u,0x12bf0u,0x25580u,0x25180u,0x1b180u,0x1b3c0u,0x25590u,0x25190u,0x100000u,0x1b3c2u,0x255a0u,0x251a0u,0x100000u,0x1b3c4u,0x255b0u,0x251b0u,0x100000u,0x1b3c6u,0x255c0u,0x251c0u,0x1b180u,0x1b3c8u,0x255d0u,0x251d0u,0x100000u,0x1b3cau,0x255e0u,0x251e0u,0x100000u,0x1b3ccu,0x255f0u,0x251f0u,0x100000u,0x1b3ceu,0x25600u,0x25200u,0x1b180u,0x1b3d0u,0x25610u,0x25210u,0x100000u,0x1b3d2u,0x25620u,0x25220u,0x100000u,0x1b3d4u,0x25630u,0x25230u,0x100000u,0x1b3d6u,0x25640u,0x25240u,0x1b180u,0x1b3d8u,0x25650u,0x25250u,0x100000u,0x1b3dau,0x25660u,0x25260u,0x100000u,0x1b3dcu,0x25670u,0x25270u,0x100000u,0x1b3deu,0x25584u,0x25280u,0x1b1b0u,0x1b3c0u,0x25594u,0x25290u,0x100000u,0x1b3c2u,0x255a4u,0x252a0u,0x100000u,0x1b3c4u,0x255b4u,0x252b0u,0x100000u,0x1b3c6u,0x255c4u,0x252c0u,0x1b1b0u,0x1b3c8u,0x255d4u,0x252d0u,0x100000u,0x1b3cau,0x255e4u,0x252e0u,0x100000u,0x1b3ccu,0x255f4u,0x252f0u,0x100000u,0x1b3ceu,0x25604u,0x25300u,0x1b1b0u,0x1b3d0u,0x25614u,0x25310u,0x100000u,0x1b3d2u,0x25624u,0x25320u,0x100000u,0x1b3d4u,0x25634u,0x25330u,0x100000u,0x1b3d6u,0x25644u,0x25340u,0x1b1b0u,0x1b3d8u,0x25654u,0x25350u,0x100000u,0x1b3dau,0x25664u,0x25360u,0x100000u,0x1b3dcu,0x25674u,0x25370u,0x100000u,0x1b3deu,0x25680u,0x25380u,0x1b1e0u,0x1b3c0u,0x25690u,0x25390u,0x100000u,0x1b3c2u,0x256a0u,0x253a0u,0x100000u,0x1b3c4u,0x256b0u,0x253b0u,0x100000u,0x1b3c6u,0x256c0u,0x253c0u,0x1b1e0u,0x1b3c8u,0x256d0u,0x253d0u,0x100000u,0x1b3cau,0x256e0u,0x253e0u,0x100000u,0x1b3ccu,0x256f0u,0x253f0u,0x100000u,0x1b3ceu,0x25700u,0x25400u,0x1b1e0u,0x1b3d0u,0x25710u,0x25410u,0x100000u,0x1b3d2u,0x25720u,0x25420u,0x100000u,0x1b3d4u,0x25730u,0x25430u,0x100000u,0x1b3d6u,0x25740u,0x25440u,0x1b1e0u,0x1b3d8u,0x25750u,0x25450u,0x100000u,0x1b3dau,0x25760u,0x25460u,0x100000u,0x1b3dcu,0x25770u,0x25470u,0x100000u,0x1b3deu,0x25684u,0x25480u,0x1b210u,0x1b3c0u,0x25694u,0x25490u,0x100000u,0x1b3c2u,0x256a4u,0x254a0u,0x100000u,0x1b3c4u,0x256b4u,0x254b0u,0x100000u,0x1b3c6u,0x256c4u,0x254c0u,0x1b210u,0x1b3c8u,0x256d4u,0x254d0u,0x100000u,0x1b3cau,0x256e4u,0x254e0u,0x100000u,0x1b3ccu,0x256f4u,0x254f0u,0x100000u,0x1b3ceu,0x25704u,0x25500u,0x1b210u,0x1b3d0u,0x25714u,0x25510u,0x100000u,0x1b3d2u,0x25724u,0x25520u,0x100000u,0x1b3d4u,0x25734u,0x25530u,0x100000u,0x1b3d6u,0x25744u,0x25540u,0x1b210u,0x1b3d8u,0x25754u,0x25550u,0x100000u,0x1b3dau,0x25764u,0x25560u,0x100000u,0x1b3dcu,0x25774u,0x25570u,0x100000u,0x1b3deu,0x22680u,0x26a80u,0x26a80u,0x80000000u,0x22682u,0x26a82u,0x100000u,0x80000000u,0x22684u,0x26a84u,0x100000u,0x80000000u,0x22686u,0x26a86u,0x100000u,0x80000000u,0x22880u,0x26a90u,0x26a80u,0x80000000u,0x22882u,0x26a92u,0x100000u,0x80000000u,0x22884u,0x26a94u,0x100000u,0x80000000u,0x22886u,0x26a96u,0x100000u,0x80000000u,0x22a80u,0x26aa0u,0x26a80u,0x80000000u,0x22a82u,0x26aa2u,0x100000u,0x80000000u,0x22a84u,0x26aa4u,0x100000u,0x80000000u,0x22a86u,0x26aa6u,0x100000u,0x80000000u,0x22c80u,0x26ab0u,0x26a80u,0x80000000u,0x22c82u,0x26ab2u,0x100000u,0x80000000u,0x22c84u,0x26ab4u,0x100000u,0x80000000u,0x22c86u,0x26ab6u,0x100000u,0x80000000u,0x22688u,0x26a88u,0x26a90u,0x80000000u,0x2268au,0x26a8au,0x100000u,0x80000000u,0x2268cu,0x26a8cu,0x100000u,0x80000000u,0x2268eu,0x26a8eu,0x100000u,0x80000000u,0x22888u,0x26a98u,0x26a90u,0x80000000u,0x2288au,0x26a9au,0x100000u,0x80000000u,0x2288cu,0x26a9cu,0x100000u,0x80000000u,0x2288eu,0x26a9eu,0x100000u,0x80000000u,0x22a88u,0x26aa8u,0x26a90u,0x80000000u,0x22a8au,0x26aaau,0x100000u,0x80000000u,0x22a8cu,0x26aacu,0x100000u,0x80000000u,0x22a8eu,0x26aaeu,0x100000u,0x80000000u,0x22c88u,0x26ab8u,0x26a90u,0x80000000u,0x22c8au,0x26abau,0x100000u,0x80000000u,0x22c8cu,0x26abcu,0x100000u,0x80000000u,0x22c8eu,0x26abeu,0x100000u,0x80000000u,0x22e80u,0x26ac0u,0x26aa0u,0x80000000u,0x22e82u,0x26ac2u,0x100000u,0x80000000u,0x22e84u,0x26ac4u,0x100000u,0x80000000u,0x22e86u,0x26ac6u,0x100000u,0x80000000u,0x23080u,0x26ad0u,0x26aa0u,0x80000000u,0x23082u,0x26ad2u,0x100000u,0x80000000u,0x23084u,0x26ad4u,0x100000u,0x80000000u,0x23086u,0x26ad6u,0x100000u,0x80000000u,0x23280u,0x26ae0u,0x26aa0u,0x80000000u,0x23282u,0x26ae2u,0x100000u,0x80000000u,0x23284u,0x26ae4u,0x100000u,0x80000000u,0x23286u,0x26ae6u,0x100000u,0x80000000u,0x23480u,0x26af0u,0x26aa0u,0x80000000u,0x23482u,0x26af2u,0x100000u,0x80000000u,0x23484u,0x26af4u,0x100000u,0x80000000u,0x23486u,0x26af6u,0x100000u,0x80000000u,0x100000u,0x26ac8u,0x26ab0u,0x80000000u,0x100000u,0x26acau,0x100000u,0x80000000u,0x100000u,0x26accu,0x100000u,0x80000000u,0x100000u,0x26aceu,0x100000u,0x80000000u,0x100000u,0x26ad8u,0x26ab0u,0x80000000u,0x100000u,0x26adau,0x100000u,0x80000000u,0x100000u,0x26adcu,0x100000u,0x80000000u,0x100000u,0x26adeu,0x100000u,0x80000000u,0x100000u,0x26ae8u,0x26ab0u,0x80000000u,0x100000u,0x26aeau,0x100000u,0x80000000u,0x100000u,0x26aecu,0x100000u,0x80000000u,0x100000u,0x26aeeu,0x100000u,0x80000000u,0x100000u,0x26af8u,0x26ab0u,0x80000000u,0x100000u,0x26afau,0x100000u,0x80000000u,0x100000u,0x26afcu,0x100000u,0x80000000u,0x100000u,0x26afeu,0x100000u,0x80000000u,0x60000u,0x80u,0xc0080u,0x60100u,0x60010u,0x90u,0xc0090u,0x60110u,0x60020u,0xa0u,0xc00a0u,0x60120u,0x60030u,0xb0u,0xc00b0u,0x60130u,0x60040u,0xc0u,0xc00c0u,0x60140u,0x60050u,0xd0u,0xc00d0u,0x60150u,0x60060u,0xe0u,0xc00e0u,0x60160u,0x60070u,0xf0u,0xc00f0u,0x60170u,0x60180u,0x200u,0xc0200u,0x60280u,0x60190u,0x210u,0xc0210u,0x60290u,0x601a0u,0x220u,0xc0220u,0x602a0u,0x601b0u,0x230u,0xc0230u,0x602b0u,0x601c0u,0x240u,0xc0240u,0x602c0u,0x601d0u,0x250u,0xc0250u,0x602d0u,0x601e0u,0x260u,0xc0260u,0x602e0u,0x601f0u,0x270u,0xc0270u,0x602f0u,0x60310u,0x390u,0xc0390u,0x60410u,0x60300u,0x380u,0xc0380u,0x60400u,0x60330u,0x3b0u,0xc03b0u,0x60430u,0x60320u,0x3a0u,0xc03a0u,0x60420u,0x60350u,0x3d0u,0xc03d0u,0x60450u,0x60340u,0x3c0u,0xc03c0u,0x60440u,0x60370u,0x3f0u,0xc03f0u,0x60470u,0x60360u,0x3e0u,0xc03e0u,0x60460u,0x60490u,0x510u,0xef910u,0x60590u,0x60480u,0x500u,0xef900u,0x60580u,0x604b0u,0x530u,0xef930u,0x605b0u,0x604a0u,0x520u,0xef920u,0x605a0u,0x604d0u,0x550u,0xef950u,0x605d0u,0x604c0u,0x540u,0xef940u,0x605c0u,0x604f0u,0x570u,0xef970u,0x605f0u,0x604e0u,0x560u,0xef960u,0x605e0u,0x60620u,0x6a0u,0xefaa0u,0x60720u,0x60630u,0x6b0u,0xefab0u,0x60730u,0x60600u,0x680u,0xefa80u,0x60700u,0x60610u,0x690u,0xefa90u,0x60710u,0x60660u,0x6e0u,0xefae0u,0x60760u,0x60670u,0x6f0u,0xefaf0u,0x60770u,0x60640u,0x6c0u,0xefac0u,0x60740u,0x60650u,0x6d0u,0xefad0u,0x60750u,0x8fba0u,0x820u,0xefc20u,0x8fca0u,0x8fbb0u,0x830u,0xefc30u,0x8fcb0u,0x8fb80u,0x800u,0xefc00u,0x8fc80u,0x8fb90u,0x810u,0xefc10u,0x8fc90u,0x8fbe0u,0x860u,0xefc60u,0x8fce0u,0x8fbf0u,0x870u,0xefc70u,0x8fcf0u,0x8fbc0u,0x840u,0xefc40u,0x8fcc0u,0x8fbd0u,0x850u,0xefc50u,0x8fcd0u,0x8fd30u,0x9b0u,0xefdb0u,0x8fe30u,0x8fd20u,0x9a0u,0xefda0u,0x8fe20u,0x8fd10u,0x990u,0xefd90u,0x8fe10u,0x8fd00u,0x980u,0xefd80u,0x8fe00u,0x8fd70u,0x9f0u,0xefdf0u,0x8fe70u,0x8fd60u,0x9e0u,0xefde0u,0x8fe60u,0x8fd50u,0x9d0u,0xefdd0u,0x8fe50u,0x8fd40u,0x9c0u,0xefdc0u,0x8fe40u,0x8feb0u,0x2ff30u,0xeff30u,0x8ffb0u,0x8fea0u,0x2ff20u,0xeff20u,0x8ffa0u,0x8fe90u,0x2ff10u,0xeff10u,0x8ff90u,0x8fe80u,0x2ff00u,0xeff00u,0x8ff80u,0x8fef0u,0x2ff70u,0xeff70u,0x8fff0u,0x8fee0u,0x2ff60u,0xeff60u,0x8ffe0u,0x8fed0u,0x2ff50u,0xeff50u,0x8ffd0u,0x8fec0u,0x2ff40u,0xeff40u,0x8ffc0u,0x850u,0x60000u,0x90420u,0xefc50u,0x840u,0x60010u,0x90430u,0xefc40u,0x870u,0x60020u,0x90400u,0xefc70u,0x860u,0x60030u,0x90410u,0xefc60u,0x810u,0x60040u,0x90460u,0xefc10u,0x800u,0x60050u,0x90470u,0xefc00u,0x830u,0x60060u,0x90440u,0xefc30u,0x820u,0x60070u,0x90450u,0xefc20u,0x8d0u,0x60080u,0x904b0u,0xefcd0u,0x8c0u,0x60090u,0x904a0u,0xefcc0u,0x8f0u,0x600a0u,0x90490u,0xefcf0u,0x8e0u,0x600b0u,0x90480u,0xefce0u,0x890u,0x600c0u,0x904f0u,0xefc90u,0x880u,0x600d0u,0x904e0u,0xefc80u,0x8b0u,0x600e0u,0x904d0u,0xefcb0u,0x8a0u,0x600f0u,0x904c0u,0xefca0u,0x960u,0x60100u,0x90530u,0xefd60u,0x970u,0x60110u,0x90520u,0xefd70u,0x940u,0x60120u,0x90510u,0xefd40u,0x950u,0x60130u,0x90500u,0xefd50u,0x920u,0x60140u,0x90570u,0xefd20u,0x930u,0x60150u,0x90560u,0xefd30u,0x900u,0x60160u,0x90550u,0xefd00u,0x910u,0x60170u,0x90540u,0xefd10u,0x9e0u,0x60190u,0x905b0u,0xefde0u,0x9f0u,0x60180u,0x905a0u,0xefdf0u,0x9c0u,0x601b0u,0x90590u,0xefdc0u,0x9d0u,0x601a0u,0x90580u,0xefdd0u,0x9a0u,0x601d0u,0x905f0u,0xefda0u,0x9b0u,0x601c0u,0x905e0u,0xefdb0u,0x980u,0x601f0u,0x905d0u,0xefd80u,0x990u,0x601e0u,0x905c0u,0xefd90u,0xa60u,0x60210u,0xbfa40u,0xefe60u,0xa70u,0x60200u,0xbfa50u,0xefe70u,0xa40u,0x60230u,0xbfa60u,0xefe40u,0xa50u,0x60220u,0xbfa70u,0xefe50u,0xa20u,0x60250u,0xbfa00u,0xefe20u,0xa30u,0x60240u,0xbfa10u,0xefe30u,0xa00u,0x60270u,0xbfa20u,0xefe00u,0xa10u,0x60260u,0xbfa30u,0xefe10u,0x2fef0u,0x60290u,0xbfac0u,0xefef0u,0x2fee0u,0x60280u,0xbfad0u,0xefee0u,0x2fed0u,0x602b0u,0xbfae0u,0xefed0u,0x2fec0u,0x602a0u,0xbfaf0u,0xefec0u,0x2feb0u,0x602d0u,0xbfa80u,0xefeb0u,0x2fea0u,0x602c0u,0xbfa90u,0xefea0u,0x2fe90u,0x602f0u,0xbfaa0u,0xefe90u,0x2fe80u,0x602e0u,0xbfab0u,0xefe80u,0x2ff70u,0x60320u,0xbfb40u,0xeff70u,0x2ff60u,0x60330u,0xbfb50u,0xeff60u,0x2ff50u,0x60300u,0xbfb60u,0xeff50u,0x2ff40u,0x60310u,0xbfb70u,0xeff40u,0x2ff30u,0x60360u,0xbfb00u,0xeff30u,0x2ff20u,0x60370u,0xbfb10u,0xeff20u,0x2ff10u,0x60340u,0xbfb20u,0xeff10u,0x2ff00u,0x60350u,0xbfb30u,0xeff00u,0x2fff0u,0x603a0u,0xbfbd0u,0xefff0u,0x2ffe0u,0x603b0u,0xbfbc0u,0xeffe0u,0x2ffd0u,0x60380u,0xbfbf0u,0xeffd0u,0x2ffc0u,0x60390u,0xbfbe0u,0xeffc0u,0x2ffb0u,0x603e0u,0xbfb90u,0xeffb0u,0x2ffa0u,0x603f0u,0xbfb80u,0xeffa0u,0x2ff90u,0x603c0u,0xbfbb0u,0xeff90u,0x2ff80u,0x603d0u,0xbfba0u,0xeff80u,0x0u,0x80000000u,0x0u,0x800u,0x0u,0x80000000u,0x0u,0x810u,0x0u,0x80000000u,0x0u,0x820u,0x0u,0x80000000u,0x0u,0x830u,0x0u,0x80000000u,0x0u,0x840u,0x0u,0x80000000u,0x0u,0x850u,0x0u,0x80000000u,0x0u,0x860u,0x0u,0x80000000u,0x0u,0x870u,0x0u,0x80000000u,0x0u,0x880u,0x0u,0x80000000u,0x0u,0x890u,0x0u,0x80000000u,0x0u,0x8a0u,0x0u,0x80000000u,0x0u,0x8b0u,0x0u,0x80000000u,0x0u,0x8c0u,0x0u,0x80000000u,0x0u,0x8d0u,0x0u,0x80000000u,0x0u,0x8e0u,0x0u,0x80000000u,0x0u,0x8f0u,0x0u,0x80000000u,0x0u,0x900u,0x0u,0x80000000u,0x0u,0x910u,0x0u,0x80000000u,0x0u,0x920u,0x0u,0x80000000u,0x0u,0x930u,0x0u,0x80000000u,0x0u,0x940u,0x0u,0x80000000u,0x0u,0x950u,0x0u,0x80000000u,0x0u,0x960u,0x0u,0x80000000u,0x0u,0x970u,0x0u,0x80000000u,0x0u,0x980u,0x0u,0x80000000u,0x0u,0x990u,0x0u,0x80000000u,0x0u,0x9a0u,0x0u,0x80000000u,0x0u,0x9b0u,0x0u,0x80000000u,0x0u,0x9c0u,0x0u,0x80000000u,0x0u,0x9d0u,0x0u,0x80000000u,0x0u,0x9e0u,0x0u,0x80000000u,0x0u,0x9f0u,0x0u,0x80000000u,0x0u,0xa00u,0x0u,0x80000000u,0x0u,0xa10u,0x0u,0x80000000u,0x0u,0xa20u,0x0u,0x80000000u,0x0u,0xa30u,0x0u,0x80000000u,0x0u,0xa40u,0x0u,0x80000000u,0x0u,0xa50u,0x0u,0x80000000u,0x0u,0xa60u,0x0u,0x80000000u,0x0u,0xa70u,0x0u,0x80000000u,0x0u,0xa80u,0x0u,0x80000000u,0x0u,0xa90u,0x0u,0x80000000u,0x0u,0xaa0u,0x0u,0x80000000u,0x0u,0xab0u,0x0u,0x80000000u,0x0u,0xac0u,0x0u,0x80000000u,0x0u,0xad0u,0x0u,0x80000000u,0x0u,0xae0u,0x0u,0x80000000u,0x0u,0xaf0u,0x0u,0x80000000u,0x0u,0xb00u,0x0u,0x80000000u,0x0u,0xb10u,0x0u,0x80000000u,0x0u,0xb20u,0x0u,0x80000000u,0x0u,0xb30u,0x0u,0x80000000u,0x0u,0xb40u,0x0u,0x80000000u,0x0u,0xb50u,0x0u,0x80000000u,0x0u,0xb60u,0x0u,0x80000000u,0x0u,0xb70u,0x0u,0x80000000u,0x0u,0xb80u,0x0u,0x80000000u,0x0u,0xb90u,0x0u,0x80000000u,0x0u,0xba0u,0x0u,0x80000000u,0x0u,0xbb0u,0x0u,0x80000000u,0x0u,0xbc0u,0x0u,0x80000000u,0x0u,0xbd0u,0x0u,0x80000000u,0x0u,0xbe0u,0x0u,0x80000000u,0x0u,0xbf0u,0x0u,0x1200u,0x0u,0x26188u,0x0u,0x1204u,0x0u,0x26198u,0x0u,0x1208u,0x0u,0x261a8u,0x0u,0x120cu,0x0u,0x261b8u,0x0u,0x1210u,0x0u,0x261c8u,0x0u,0x1214u,0x0u,0x261d8u,0x0u,0x1218u,0x0u,0x261e8u,0x0u,0x121cu,0x0u,0x261f8u,0x0u,0x1220u,0x0u,0x26208u,0x0u,0x1224u,0x0u,0x26218u,0x0u,0x1228u,0x0u,0x26228u,0x0u,0x122cu,0x0u,0x26238u,0x0u,0x1230u,0x0u,0x26248u,0x0u,0x1234u,0x0u,0x26258u,0x0u,0x1238u,0x0u,0x26268u,0x0u,0x123cu,0x0u,0x26278u,0x0u,0x1240u,0x0u,0x26288u,0x0u,0x1244u,0x0u,0x26298u,0x0u,0x1248u,0x0u,0x262a8u,0x0u,0x124cu,0x0u,0x262b8u,0x0u,0x1250u,0x0u,0x262c8u,0x0u,0x1254u,0x0u,0x262d8u,0x0u,0x1258u,0x0u,0x262e8u,0x0u,0x125cu,0x0u,0x262f8u,0x0u,0x1260u,0x0u,0x26308u,0x0u,0x1264u,0x0u,0x26318u,0x0u,0x1268u,0x0u,0x26328u,0x0u,0x126cu,0x0u,0x26338u,0x0u,0x1270u,0x0u,0x26348u,0x0u,0x1274u,0x0u,0x26358u,0x0u,0x1278u,0x0u,0x26368u,0x0u,0x127cu,0x0u,0x26378u,0x0u,0x1280u,0x0u,0x26388u,0x0u,0x1284u,0x0u,0x26398u,0x0u,0x1288u,0x0u,0x263a8u,0x0u,0x128cu,0x0u,0x263b8u,0x0u,0x1290u,0x0u,0x263c8u,0x0u,0x1294u,0x0u,0x263d8u,0x0u,0x1298u,0x0u,0x263e8u,0x0u,0x129cu,0x0u,0x263f8u,0x0u,0x12a0u,0x0u,0x26408u,0x0u,0x12a4u,0x0u,0x26418u,0x0u,0x12a8u,0x0u,0x26428u,0x0u,0x12acu,0x0u,0x26438u,0x0u,0x12b0u,0x0u,0x26448u,0x0u,0x12b4u,0x0u,0x26458u,0x0u,0x12b8u,0x0u,0x26468u,0x0u,0x12bcu,0x0u,0x26478u,0x0u,0x12c0u,0x0u,0x26488u,0x0u,0x12c0u,0x0u,0x26498u,0x0u,0x12c0u,0x0u,0x264a8u,0x0u,0x12c0u,0x0u,0x264b8u,0x0u,0x12c0u,0x0u,0x264c8u,0x0u,0x12c0u,0x0u,0x264d8u,0x0u,0x12c0u,0x0u,0x264e8u,0x0u,0x12c0u,0x0u,0x264f8u,0x0u,0x12c0u,0x0u,0x26508u,0x0u,0x12c0u,0x0u,0x26518u,0x0u,0x12c0u,0x0u,0x26528u,0x0u,0x12c0u,0x0u,0x26538u,0x0u,0x12c0u,0x0u,0x26548u,0x0u,0x12c0u,0x0u,0x26558u,0x0u,0x12c0u,0x0u,0x26568u,0x0u,0x12c0u,0x0u,0x26578u,0x3f803f80u,0x3f803f80u,0x3f803f80u,0x3f803f80u,0x0u,0x0u,0x0u,0x0u,0x0u,0x0u,0x0u,0x0u,0x0u,0x0u,0x0u,0x0u,0x3f803f80u,0x3f803f80u,0x3f803f80u,0x3f803f80u,0x0u,0x0u,0x0u,0x0u,0x0u,0x0u,0x0u,0x0u,0x0u,0x0u,0x0u,0x0u,0x3f803f80u,0x3f803f80u,0x3f803f80u,0x3f803f80u,0x0u,0x0u,0x0u,0x0u,0x0u,0x0u,0x0u,0x0u,0x0u,0x0u,0x0u,0x0u,0x3f803f80u,0x3f803f80u,0x3f803f80u,0x3f803f80u,0x0u,0x0u,0x0u,0x0u,0x0u,0x0u,0x0u,0x0u,0x0u,0x0u,0x0u,0x0u,0x3f803f80u,0x3f803f80u,0x3f803f80u,0x3f803f80u,0x0u,0x0u,0x0u,0x0u,0x0u,0x0u,0x0u,0x0u,0x0u,0x0u,0x0u,0x0u,0x3f803f80u,0x3f803f80u,0x3f803f80u,0x3f803f80u,0x0u,0x0u,0x0u,0x0u,0x0u,0x0u,0x0u,0x0u,0x0u,0x0u,0x0u,0x0u,0x3f803f80u,0x3f803f80u,0x3f803f80u,0x3f803f80u,0x0u,0x0u,0x0u,0x0u,0x0u,0x0u,0x0u,0x0u,0x0u,0x0u,0x0u,0x0u,0x3f803f80u,0x3f803f80u,0x3f803f80u,0x3f803f80u,0x0u,0x0u,0x0u,0x0u,0x0u,0x0u,0x0u,0x0u,0x0u,0x0u,0x0u,0x0u,0x3f803f80u,0x3f803f80u,0x3f803f80u,0x3f803f80u,0x0u,0x0u,0x0u,0x0u,0x0u,0x0u,0x0u,0x0u,0x0u,0x0u,0x0u,0x0u,0x3f803f80u,0x3f803f80u,0x3f803f80u,0x3f803f80u,0x0u,0x0u,0x0u,0x0u,0x0u,0x0u,0x0u,0x0u,0x0u,0x0u,0x0u,0x0u,0x3f803f80u,0x3f803f80u,0x3f803f80u,0x3f803f80u,0x0u,0x0u,0x0u,0x0u,0x0u,0x0u,0x0u,0x0u,0x0u,0x0u,0x0u,0x0u,0x3f803f80u,0x3f803f80u,0x3f803f80u,0x3f803f80u,0x0u,0x0u,0x0u,0x0u,0x0u,0x0u,0x0u,0x0u,0x0u,0x0u,0x0u,0x0u,0x3f803f80u,0x3f803f80u,0x3f803f80u,0x3f803f80u,0x0u,0x0u,0x0u,0x0u,0x0u,0x0u,0x0u,0x0u,0x0u,0x0u,0x0u,0x0u,0x3f803f80u,0x3f803f80u,0x3f803f80u,0x3f803f80u,0x0u,0x0u,0x0u,0x0u,0x0u,0x0u,0x0u,0x0u,0x0u,0x0u,0x0u,0x0u,0x3f803f80u,0x3f803f80u,0x3f803f80u,0x3f803f80u,0x0u,0x0u,0x0u,0x0u,0x0u,0x0u,0x0u,0x0u,0x0u,0x0u,0x0u,0x0u,0x3f803f80u,0x3f803f80u,0x3f803f80u,0x3f803f80u,0x0u,0x0u,0x0u,0x0u,0x0u,0x0u,0x0u,0x0u,0x0u,0x0u,0x0u,0x0u,0x18830u,0x0u,0x0u,0x0u,0x18830u,0x0u,0x0u,0x0u,0x18830u,0x0u,0x0u,0x0u,0x18830u,0x0u,0x0u,0x0u,0x18830u,0x0u,0x0u,0x0u,0x18830u,0x0u,0x0u,0x0u,0x18830u,0x0u,0x0u,0x0u,0x18830u,0x0u,0x0u,0x0u,0x18830u,0x0u,0x0u,0x0u,0x18830u,0x0u,0x0u,0x0u,0x18830u,0x0u,0x0u,0x0u,0x18830u,0x0u,0x0u,0x0u,0x18830u,0x0u,0x0u,0x0u,0x18830u,0x0u,0x0u,0x0u,0x18830u,0x0u,0x0u,0x0u,0x18830u,0x0u,0x0u,0x0u,0x188b2u,0x0u,0x0u,0x0u,0x188b2u,0x0u,0x0u,0x0u,0x188b2u,0x0u,0x0u,0x0u,0x188b2u,0x0u,0x0u,0x0u,0x188b2u,0x0u,0x0u,0x0u,0x188b2u,0x0u,0x0u,0x0u,0x188b2u,0x0u,0x0u,0x0u,0x188b2u,0x0u,0x0u,0x0u,0x18892u,0x0u,0x0u,0x0u,0x18892u,0x0u,0x0u,0x0u,0x18892u,0x0u,0x0u,0x0u,0x18892u,0x0u,0x0u,0x0u,0x18892u,0x0u,0x0u,0x0u,0x18892u,0x0u,0x0u,0x0u,0x18892u,0x0u,0x0u,0x0u,0x18892u,0x0u,0x0u,0x0u,0x1489au,0x0u,0x0u,0x0u,0x1489au,0x0u,0x0u,0x0u,0x1489au,0x0u,0x0u,0x0u,0x1489au,0x0u,0x0u,0x0u,0x1489au,0x0u,0x0u,0x0u,0x1489au,0x0u,0x0u,0x0u,0x1489au,0x0u,0x0u,0x0u,0x1489au,0x0u,0x0u,0x0u,0x34018u,0x0u,0x0u,0x0u,0x34018u,0x0u,0x0u,0x0u,0x34018u,0x0u,0x0u,0x0u,0x34018u,0x0u,0x0u,0x0u,0x34018u,0x0u,0x0u,0x0u,0x34018u,0x0u,0x0u,0x0u,0x34018u,0x0u,0x0u,0x0u,0x34018u,0x0u,0x0u,0x0u,0x36018u,0x0u,0x0u,0x0u,0x36018u,0x0u,0x0u,0x0u,0x36018u,0x0u,0x0u,0x0u,0x36018u,0x0u,0x0u,0x0u,0x36018u,0x0u,0x0u,0x0u,0x36018u,0x0u,0x0u,0x0u,0x36018u,0x0u,0x0u,0x0u,0x36018u,0x0u,0x0u,0x0u,0x36030u,0x0u,0x0u,0x0u,0x36030u,0x0u,0x0u,0x0u,0x36030u,0x0u,0x0u,0x0u,0x36030u,0x0u,0x0u,0x0u,0x36030u,0x0u,0x0u,0x0u,0x36030u,0x0u,0x0u,0x0u,0x36030u,0x0u,0x0u,0x0u,0x36030u,0x0u,0x0u,0x0u,0x0u,0x40u,0x9000u,0xcc00u,0x80u,0xc0u,0x9010u,0xcc10u,0x110u,0x150u,0x9020u,0xcc20u,0x190u,0x1d0u,0x9030u,0xcc30u,0x220u,0x260u,0x9040u,0xcc40u,0x2a0u,0x2e0u,0x9050u,0xcc50u,0x330u,0x370u,0x9060u,0xcc60u,0x3b0u,0x3f0u,0x9070u,0xcc70u,0x440u,0x400u,0x9080u,0xcc80u,0x4c0u,0x480u,0x9090u,0xcc90u,0x550u,0x510u,0x90a0u,0xcca0u,0x5d0u,0x590u,0x90b0u,0xccb0u,0x660u,0x620u,0x90c0u,0xccc0u,0x6e0u,0x6a0u,0x90d0u,0xccd0u,0x770u,0x730u,0x90e0u,0xcce0u,0x7f0u,0x7b0u,0x90f0u,0xccf0u,0x10u,0x50u,0x9300u,0xcf00u,0x90u,0xd0u,0x9310u,0xcf10u,0x100u,0x140u,0x9320u,0xcf20u,0x180u,0x1c0u,0x9330u,0xcf30u,0x230u,0x270u,0x9340u,0xcf40u,0x2b0u,0x2f0u,0x9350u,0xcf50u,0x320u,0x360u,0x9360u,0xcf60u,0x3a0u,0x3e0u,0x9370u,0xcf70u,0x450u,0x410u,0x9380u,0xcf80u,0x4d0u,0x490u,0x9390u,0xcf90u,0x540u,0x500u,0x93a0u,0xcfa0u,0x5c0u,0x580u,0x93b0u,0xcfb0u,0x670u,0x630u,0x93c0u,0xcfc0u,0x6f0u,0x6b0u,0x93d0u,0xcfd0u,0x760u,0x720u,0x93e0u,0xcfe0u,0x7e0u,0x7a0u,0x93f0u,0xcff0u,0x20u,0x60u,0x9600u,0xd200u,0xa0u,0xe0u,0x9610u,0xd210u,0x130u,0x170u,0x9620u,0xd220u,0x1b0u,0x1f0u,0x9630u,0xd230u,0x200u,0x240u,0x9640u,0xd240u,0x280u,0x2c0u,0x9650u,0xd250u,0x310u,0x350u,0x9660u,0xd260u,0x390u,0x3d0u,0x9670u,0xd270u,0x460u,0x420u,0x9680u,0xd280u,0x4e0u,0x4a0u,0x9690u,0xd290u,0x570u,0x530u,0x96a0u,0xd2a0u,0x5f0u,0x5b0u,0x96b0u,0xd2b0u,0x640u,0x600u,0x96c0u,0xd2c0u,0x6c0u,0x680u,0x96d0u,0xd2d0u,0x750u,0x710u,0x96e0u,0xd2e0u,0x7d0u,0x790u,0x96f0u,0xd2f0u,0x30u,0x70u,0x9900u,0xd500u,0xb0u,0xf0u,0x9910u,0xd510u,0x120u,0x160u,0x9920u,0xd520u,0x1a0u,0x1e0u,0x9930u,0xd530u,0x210u,0x250u,0x9940u,0xd540u,0x290u,0x2d0u,0x9950u,0xd550u,0x300u,0x340u,0x9960u,0xd560u,0x380u,0x3c0u,0x9970u,0xd570u,0x470u,0x430u,0x9980u,0xd580u,0x4f0u,0x4b0u,0x9990u,0xd590u,0x560u,0x520u,0x99a0u,0xd5a0u,0x5e0u,0x5a0u,0x99b0u,0xd5b0u,0x650u,0x610u,0x99c0u,0xd5c0u,0x6d0u,0x690u,0x99d0u,0xd5d0u,0x740u,0x700u,0x99e0u,0xd5e0u,0x7c0u,0x780u,0x99f0u,0xd5f0u,0xc008u,0x12120u,0x12160u,0x12c00u,0xc018u,0x122b0u,0x122f0u,0x12c10u,0xc028u,0x12400u,0x12440u,0x12c20u,0xc038u,0x12590u,0x125d0u,0x12c30u,0xc048u,0x12760u,0x12720u,0x12c40u,0xc058u,0x128f0u,0x128b0u,0x12c50u,0xc068u,0x12a40u,0x12a00u,0x12c60u,0xc078u,0x12bd0u,0x12b90u,0x12c70u,0xc088u,0x12d20u,0x12d60u,0x12c80u,0xc098u,0x12eb0u,0x12ef0u,0x12c90u,0xc0a8u,0x13000u,0x13040u,0x12ca0u,0xc0b8u,0x13190u,0x131d0u,0x12cb0u,0xc0c8u,0x13360u,0x13320u,0x12cc0u,0xc0d8u,0x134f0u,0x134b0u,0x12cd0u,0xc0e8u,0x13640u,0x13600u,0x12ce0u,0xc0f8u,0x137d0u,0x13790u,0x12cf0u,0xc308u,0x12130u,0x12168u,0x12d00u,0xc318u,0x122a0u,0x122f8u,0x12d10u,0xc328u,0x12410u,0x12448u,0x12d20u,0xc338u,0x12580u,0x125d8u,0x12d30u,0xc348u,0x12770u,0x12728u,0x12d40u,0xc358u,0x128e0u,0x128b8u,0x12d50u,0xc368u,0x12a50u,0x12a08u,0x12d60u,0xc378u,0x12bc0u,0x12b98u,0x12d70u,0xc388u,0x12d30u,0x12d68u,0x12d80u,0xc398u,0x12ea0u,0x12ef8u,0x12d90u,0xc3a8u,0x13010u,0x13048u,0x12da0u,0xc3b8u,0x13180u,0x131d8u,0x12db0u,0xc3c8u,0x13370u,0x13328u,0x12dc0u,0xc3d8u,0x134e0u,0x134b8u,0x12dd0u,0xc3e8u,0x13650u,0x13608u,0x12de0u,0xc3f8u,0x137c0u,0x13798u,0x12df0u,0xc608u,0x12140u,0x12170u,0x12e00u,0xc618u,0x122d0u,0x122e0u,0x12e10u,0xc628u,0x12460u,0x12450u,0x12e20u,0xc638u,0x125f0u,0x125c0u,0x12e30u,0xc648u,0x12700u,0x12730u,0x12e40u,0xc658u,0x12890u,0x128a0u,0x12e50u,0xc668u,0x12a20u,0x12a10u,0x12e60u,0xc678u,0x12bb0u,0x12b80u,0x12e70u,0xc688u,0x12d40u,0x12d70u,0x12e80u,0xc698u,0x12ed0u,0x12ee0u,0x12e90u,0xc6a8u,0x13060u,0x13050u,0x12ea0u,0xc6b8u,0x131f0u,0x131c0u,0x12eb0u,0xc6c8u,0x13300u,0x13330u,0x12ec0u,0xc6d8u,0x13490u,0x134a0u,0x12ed0u,0xc6e8u,0x13620u,0x13610u,0x12ee0u,0xc6f8u,0x137b0u,0x13780u,0x12ef0u,0xc908u,0x12150u,0x12178u,0x12f00u,0xc918u,0x122c0u,0x122e8u,0x12f10u,0xc928u,0x12470u,0x12458u,0x12f20u,0xc938u,0x125e0u,0x125c8u,0x12f30u,0xc948u,0x12710u,0x12738u,0x12f40u,0xc958u,0x12880u,0x128a8u,0x12f50u,0xc968u,0x12a30u,0x12a18u,0x12f60u,0xc978u,0x12ba0u,0x12b88u,0x12f70u,0xc988u,0x12d50u,0x12d78u,0x12f80u,0xc998u,0x12ec0u,0x12ee8u,0x12f90u,0xc9a8u,0x13070u,0x13058u,0x12fa0u,0xc9b8u,0x131e0u,0x131c8u,0x12fb0u,0xc9c8u,0x13310u,0x13338u,0x12fc0u,0xc9d8u,0x13480u,0x134a8u,0x12fd0u,0xc9e8u,0x13630u,0x13618u,0x12fe0u,0xc9f8u,0x137a0u,0x13788u,0x12ff0u,0x25588u,0x25180u,0x1b240u,0x1b420u,0x25598u,0x25190u,0x100000u,0x1b422u,0x255a8u,0x251a0u,0x100000u,0x1b424u,0x255b8u,0x251b0u,0x100000u,0x1b426u,0x255c8u,0x251c0u,0x1b240u,0x1b428u,0x255d8u,0x251d0u,0x100000u,0x1b42au,0x255e8u,0x251e0u,0x100000u,0x1b42cu,0x255f8u,0x251f0u,0x100000u,0x1b42eu,0x25608u,0x25200u,0x1b240u,0x1b430u,0x25618u,0x25210u,0x100000u,0x1b432u,0x25628u,0x25220u,0x100000u,0x1b434u,0x25638u,0x25230u,0x100000u,0x1b436u,0x25648u,0x25240u,0x1b240u,0x1b438u,0x25658u,0x25250u,0x100000u,0x1b43au,0x25668u,0x25260u,0x100000u,0x1b43cu,0x25678u,0x25270u,0x100000u,0x1b43eu,0x2558cu,0x25280u,0x1b270u,0x1b420u,0x2559cu,0x25290u,0x100000u,0x1b422u,0x255acu,0x252a0u,0x100000u,0x1b424u,0x255bcu,0x252b0u,0x100000u,0x1b426u,0x255ccu,0x252c0u,0x1b270u,0x1b428u,0x255dcu,0x252d0u,0x100000u,0x1b42au,0x255ecu,0x252e0u,0x100000u,0x1b42cu,0x255fcu,0x252f0u,0x100000u,0x1b42eu,0x2560cu,0x25300u,0x1b270u,0x1b430u,0x2561cu,0x25310u,0x100000u,0x1b432u,0x2562cu,0x25320u,0x100000u,0x1b434u,0x2563cu,0x25330u,0x100000u,0x1b436u,0x2564cu,0x25340u,0x1b270u,0x1b438u,0x2565cu,0x25350u,0x100000u,0x1b43au,0x2566cu,0x25360u,0x100000u,0x1b43cu,0x2567cu,0x25370u,0x100000u,0x1b43eu,0x25688u,0x25380u,0x1b2a0u,0x1b420u,0x25698u,0x25390u,0x100000u,0x1b422u,0x256a8u,0x253a0u,0x100000u,0x1b424u,0x256b8u,0x253b0u,0x100000u,0x1b426u,0x256c8u,0x253c0u,0x1b2a0u,0x1b428u,0x256d8u,0x253d0u,0x100000u,0x1b42au,0x256e8u,0x253e0u,0x100000u,0x1b42cu,0x256f8u,0x253f0u,0x100000u,0x1b42eu,0x25708u,0x25400u,0x1b2a0u,0x1b430u,0x25718u,0x25410u,0x100000u,0x1b432u,0x25728u,0x25420u,0x100000u,0x1b434u,0x25738u,0x25430u,0x100000u,0x1b436u,0x25748u,0x25440u,0x1b2a0u,0x1b438u,0x25758u,0x25450u,0x100000u,0x1b43au,0x25768u,0x25460u,0x100000u,0x1b43cu,0x25778u,0x25470u,0x100000u,0x1b43eu,0x2568cu,0x25480u,0x1b2d0u,0x1b420u,0x2569cu,0x25490u,0x100000u,0x1b422u,0x256acu,0x254a0u,0x100000u,0x1b424u,0x256bcu,0x254b0u,0x100000u,0x1b426u,0x256ccu,0x254c0u,0x1b2d0u,0x1b428u,0x256dcu,0x254d0u,0x100000u,0x1b42au,0x256ecu,0x254e0u,0x100000u,0x1b42cu,0x256fcu,0x254f0u,0x100000u,0x1b42eu,0x2570cu,0x25500u,0x1b2d0u,0x1b430u,0x2571cu,0x25510u,0x100000u,0x1b432u,0x2572cu,0x25520u,0x100000u,0x1b434u,0x2573cu,0x25530u,0x100000u,0x1b436u,0x2574cu,0x25540u,0x1b2d0u,0x1b438u,0x2575cu,0x25550u,0x100000u,0x1b43au,0x2576cu,0x25560u,0x100000u,0x1b43cu,0x2577cu,0x25570u,0x100000u,0x1b43eu,0x23680u,0x26b00u,0x26b00u,0x80000000u,0x23682u,0x26b02u,0x100000u,0x80000000u,0x23684u,0x26b04u,0x100000u,0x80000000u,0x23686u,0x26b06u,0x100000u,0x80000000u,0x23880u,0x26b10u,0x26b00u,0x80000000u,0x23882u,0x26b12u,0x100000u,0x80000000u,0x23884u,0x26b14u,0x100000u,0x80000000u,0x23886u,0x26b16u,0x100000u,0x80000000u,0x23a80u,0x26b20u,0x26b00u,0x80000000u,0x23a82u,0x26b22u,0x100000u,0x80000000u,0x23a84u,0x26b24u,0x100000u,0x80000000u,0x23a86u,0x26b26u,0x100000u,0x80000000u,0x23c80u,0x26b30u,0x26b00u,0x80000000u,0x23c82u,0x26b32u,0x100000u,0x80000000u,0x23c84u,0x26b34u,0x100000u,0x80000000u,0x23c86u,0x26b36u,0x100000u,0x80000000u,0x23688u,0x26b08u,0x26b10u,0x80000000u,0x2368au,0x26b0au,0x100000u,0x80000000u,0x2368cu,0x26b0cu,0x100000u,0x80000000u,0x2368eu,0x26b0eu,0x100000u,0x80000000u,0x23888u,0x26b18u,0x26b10u,0x80000000u,0x2388au,0x26b1au,0x100000u,0x80000000u,0x2388cu,0x26b1cu,0x100000u,0x80000000u,0x2388eu,0x26b1eu,0x100000u,0x80000000u,0x23a88u,0x26b28u,0x26b10u,0x80000000u,0x23a8au,0x26b2au,0x100000u,0x80000000u,0x23a8cu,0x26b2cu,0x100000u,0x80000000u,0x23a8eu,0x26b2eu,0x100000u,0x80000000u,0x23c88u,0x26b38u,0x26b10u,0x80000000u,0x23c8au,0x26b3au,0x100000u,0x80000000u,0x23c8cu,0x26b3cu,0x100000u,0x80000000u,0x23c8eu,0x26b3eu,0x100000u,0x80000000u,0x22e88u,0x26b40u,0x26b20u,0x80000000u,0x22e8au,0x26b42u,0x100000u,0x80000000u,0x22e8cu,0x26b44u,0x100000u,0x80000000u,0x22e8eu,0x26b46u,0x100000u,0x80000000u,0x23088u,0x26b50u,0x26b20u,0x80000000u,0x2308au,0x26b52u,0x100000u,0x80000000u,0x2308cu,0x26b54u,0x100000u,0x80000000u,0x2308eu,0x26b56u,0x100000u,0x80000000u,0x23288u,0x26b60u,0x26b20u,0x80000000u,0x2328au,0x26b62u,0x100000u,0x80000000u,0x2328cu,0x26b64u,0x100000u,0x80000000u,0x2328eu,0x26b66u,0x100000u,0x80000000u,0x23488u,0x26b70u,0x26b20u,0x80000000u,0x2348au,0x26b72u,0x100000u,0x80000000u,0x2348cu,0x26b74u,0x100000u,0x80000000u,0x2348eu,0x26b76u,0x100000u,0x80000000u,0x100000u,0x26b48u,0x26b30u,0x80000000u,0x100000u,0x26b4au,0x100000u,0x80000000u,0x100000u,0x26b4cu,0x100000u,0x80000000u,0x100000u,0x26b4eu,0x100000u,0x80000000u,0x100000u,0x26b58u,0x26b30u,0x80000000u,0x100000u,0x26b5au,0x100000u,0x80000000u,0x100000u,0x26b5cu,0x100000u,0x80000000u,0x100000u,0x26b5eu,0x100000u,0x80000000u,0x100000u,0x26b68u,0x26b30u,0x80000000u,0x100000u,0x26b6au,0x100000u,0x80000000u,0x100000u,0x26b6cu,0x100000u,0x80000000u,0x100000u,0x26b6eu,0x100000u,0x80000000u,0x100000u,0x26b78u,0x26b30u,0x80000000u,0x100000u,0x26b7au,0x100000u,0x80000000u,0x100000u,0x26b7cu,0x100000u,0x80000000u,0x100000u,0x26b7eu,0x100000u,0x80000000u,0x90040u,0x300c0u,0xf00c0u,0x90140u,0x90050u,0x300d0u,0xf00d0u,0x90150u,0x90060u,0x300e0u,0xf00e0u,0x90160u,0x90070u,0x300f0u,0xf00f0u,0x90170u,0x90000u,0x30080u,0xf0080u,0x90100u,0x90010u,0x30090u,0xf0090u,0x90110u,0x90020u,0x300a0u,0xf00a0u,0x90120u,0x90030u,0x300b0u,0xf00b0u,0x90130u,0x901c0u,0x30240u,0xf0240u,0x902c0u,0x901d0u,0x30250u,0xf0250u,0x902d0u,0x901e0u,0x30260u,0xf0260u,0x902e0u,0x901f0u,0x30270u,0xf0270u,0x902f0u,0x90180u,0x30200u,0xf0200u,0x90280u,0x90190u,0x30210u,0xf0210u,0x90290u,0x901a0u,0x30220u,0xf0220u,0x902a0u,0x901b0u,0x30230u,0xf0230u,0x902b0u,0x90350u,0x303d0u,0x11f7d0u,0x90450u,0x90340u,0x303c0u,0x11f7c0u,0x90440u,0x90370u,0x303f0u,0x11f7f0u,0x90470u,0x90360u,0x303e0u,0x11f7e0u,0x90460u,0x90310u,0x30390u,0x11f790u,0x90410u,0x90300u,0x30380u,0x11f780u,0x90400u,0x90330u,0x303b0u,0x11f7b0u,0x90430u,0x90320u,0x303a0u,0x11f7a0u,0x90420u,0x904d0u,0x30550u,0x11f950u,0x905d0u,0x904c0u,0x30540u,0x11f940u,0x905c0u,0x904f0u,0x30570u,0x11f970u,0x905f0u,0x904e0u,0x30560u,0x11f960u,0x905e0u,0x90490u,0x30510u,0x11f910u,0x90590u,0x90480u,0x30500u,0x11f900u,0x90580u,0x904b0u,0x30530u,0x11f930u,0x905b0u,0x904a0u,0x30520u,0x11f920u,0x905a0u,0xbfa60u,0x306e0u,0x11fae0u,0xbfb60u,0xbfa70u,0x306f0u,0x11faf0u,0xbfb70u,0xbfa40u,0x306c0u,0x11fac0u,0xbfb40u,0xbfa50u,0x306d0u,0x11fad0u,0xbfb50u,0xbfa20u,0x306a0u,0x11faa0u,0xbfb20u,0xbfa30u,0x306b0u,0x11fab0u,0xbfb30u,0xbfa00u,0x30680u,0x11fa80u,0xbfb00u,0xbfa10u,0x30690u,0x11fa90u,0xbfb10u,0xbfbe0u,0x30860u,0x11fc60u,0xbfce0u,0xbfbf0u,0x30870u,0x11fc70u,0xbfcf0u,0xbfbc0u,0x30840u,0x11fc40u,0xbfcc0u,0xbfbd0u,0x30850u,0x11fc50u,0xbfcd0u,0xbfba0u,0x30820u,0x11fc20u,0xbfca0u,0xbfbb0u,0x30830u,0x11fc30u,0xbfcb0u,0xbfb80u,0x30800u,0x11fc00u,0xbfc80u,0xbfb90u,0x30810u,0x11fc10u,0xbfc90u,0xbfd70u,0x5fdf0u,0x11fdf0u,0xbfe70u,0xbfd60u,0x5fde0u,0x11fde0u,0xbfe60u,0xbfd50u,0x5fdd0u,0x11fdd0u,0xbfe50u,0xbfd40u,0x5fdc0u,0x11fdc0u,0xbfe40u,0xbfd30u,0x5fdb0u,0x11fdb0u,0xbfe30u,0xbfd20u,0x5fda0u,0x11fda0u,0xbfe20u,0xbfd10u,0x5fd90u,0x11fd90u,0xbfe10u,0xbfd00u,0x5fd80u,0x11fd80u,0xbfe00u,0xbfef0u,0x5ff70u,0x11ff70u,0xbfff0u,0xbfee0u,0x5ff60u,0x11ff60u,0xbffe0u,0xbfed0u,0x5ff50u,0x11ff50u,0xbffd0u,0xbfec0u,0x5ff40u,0x11ff40u,0xbffc0u,0xbfeb0u,0x5ff30u,0x11ff30u,0xbffb0u,0xbfea0u,0x5ff20u,0x11ff20u,0xbffa0u,0xbfe90u,0x5ff10u,0x11ff10u,0xbff90u,0xbfe80u,0x5ff00u,0x11ff00u,0xbff80u,0x30000u,0x60420u,0xbfc50u,0xf0000u,0x30010u,0x60430u,0xbfc40u,0xf0010u,0x30020u,0x60400u,0xbfc70u,0xf0020u,0x30030u,0x60410u,0xbfc60u,0xf0030u,0x30040u,0x60460u,0xbfc10u,0xf0040u,0x30050u,0x60470u,0xbfc00u,0xf0050u,0x30060u,0x60440u,0xbfc30u,0xf0060u,0x30070u,0x60450u,0xbfc20u,0xf0070u,0x30080u,0x604b0u,0xbfcd0u,0xf0080u,0x30090u,0x604a0u,0xbfcc0u,0xf0090u,0x300a0u,0x60490u,0xbfcf0u,0xf00a0u,0x300b0u,0x60480u,0xbfce0u,0xf00b0u,0x300c0u,0x604f0u,0xbfc90u,0xf00c0u,0x300d0u,0x604e0u,0xbfc80u,0xf00d0u,0x300e0u,0x604d0u,0xbfcb0u,0xf00e0u,0x300f0u,0x604c0u,0xbfca0u,0xf00f0u,0x30100u,0x60530u,0xbfd60u,0xf0100u,0x30110u,0x60520u,0xbfd70u,0xf0110u,0x30120u,0x60510u,0xbfd40u,0xf0120u,0x30130u,0x60500u,0xbfd50u,0xf0130u,0x30140u,0x60570u,0xbfd20u,0xf0140u,0x30150u,0x60560u,0xbfd30u,0xf0150u,0x30160u,0x60550u,0xbfd00u,0xf0160u,0x30170u,0x60540u,0xbfd10u,0xf0170u,0x30190u,0x605b0u,0xbfde0u,0xf0190u,0x30180u,0x605a0u,0xbfdf0u,0xf0180u,0x301b0u,0x60590u,0xbfdc0u,0xf01b0u,0x301a0u,0x60580u,0xbfdd0u,0xf01a0u,0x301d0u,0x605f0u,0xbfda0u,0xf01d0u,0x301c0u,0x605e0u,0xbfdb0u,0xf01c0u,0x301f0u,0x605d0u,0xbfd80u,0xf01f0u,0x301e0u,0x605c0u,0xbfd90u,0xf01e0u,0x30210u,0x60640u,0xbfe60u,0xf0210u,0x30200u,0x60650u,0xbfe70u,0xf0200u,0x30230u,0x60660u,0xbfe40u,0xf0230u,0x30220u,0x60670u,0xbfe50u,0xf0220u,0x30250u,0x60600u,0xbfe20u,0xf0250u,0x30240u,0x60610u,0xbfe30u,0xf0240u,0x30270u,0x60620u,0xbfe00u,0xf0270u,0x30260u,0x60630u,0xbfe10u,0xf0260u,0x30290u,0x606c0u,0xbfef0u,0xf0290u,0x30280u,0x606d0u,0xbfee0u,0xf0280u,0x302b0u,0x606e0u,0xbfed0u,0xf02b0u,0x302a0u,0x606f0u,0xbfec0u,0xf02a0u,0x302d0u,0x60680u,0xbfeb0u,0xf02d0u,0x302c0u,0x60690u,0xbfea0u,0xf02c0u,0x302f0u,0x606a0u,0xbfe90u,0xf02f0u,0x302e0u,0x606b0u,0xbfe80u,0xf02e0u,0x30320u,0x60740u,0xbff70u,0x11f720u,0x30330u,0x60750u,0xbff60u,0x11f730u,0x30300u,0x60760u,0xbff50u,0x11f700u,0x30310u,0x60770u,0xbff40u,0x11f710u,0x30360u,0x60700u,0xbff30u,0x11f760u,0x30370u,0x60710u,0xbff20u,0x11f770u,0x30340u,0x60720u,0xbff10u,0x11f740u,0x30350u,0x60730u,0xbff00u,0x11f750u,0x303a0u,0x8fbd0u,0xbfff0u,0x11f7a0u,0x303b0u,0x8fbc0u,0xbffe0u,0x11f7b0u,0x30380u,0x8fbf0u,0xbffd0u,0x11f780u,0x30390u,0x8fbe0u,0xbffc0u,0x11f790u,0x303e0u,0x8fb90u,0xbffb0u,0x11f7e0u,0x303f0u,0x8fb80u,0xbffa0u,0x11f7f0u,0x303c0u,0x8fbb0u,0xbff90u,0x11f7c0u,0x303d0u,0x8fba0u,0xbff80u,0x11f7d0u,0x0u,0x80000000u,0x0u,0xc00u,0x0u,0x80000000u,0x0u,0xc10u,0x0u,0x80000000u,0x0u,0xc20u,0x0u,0x80000000u,0x0u,0xc30u,0x0u,0x80000000u,0x0u,0xc40u,0x0u,0x80000000u,0x0u,0xc50u,0x0u,0x80000000u,0x0u,0xc60u,0x0u,0x80000000u,0x0u,0xc70u,0x0u,0x80000000u,0x0u,0xc80u,0x0u,0x80000000u,0x0u,0xc90u,0x0u,0x80000000u,0x0u,0xca0u,0x0u,0x80000000u,0x0u,0xcb0u,0x0u,0x80000000u,0x0u,0xcc0u,0x0u,0x80000000u,0x0u,0xcd0u,0x0u,0x80000000u,0x0u,0xce0u,0x0u,0x80000000u,0x0u,0xcf0u,0x0u,0x80000000u,0x0u,0xd00u,0x0u,0x80000000u,0x0u,0xd10u,0x0u,0x80000000u,0x0u,0xd20u,0x0u,0x80000000u,0x0u,0xd30u,0x0u,0x80000000u,0x0u,0xd40u,0x0u,0x80000000u,0x0u,0xd50u,0x0u,0x80000000u,0x0u,0xd60u,0x0u,0x80000000u,0x0u,0xd70u,0x0u,0x80000000u,0x0u,0xd80u,0x0u,0x80000000u,0x0u,0xd90u,0x0u,0x80000000u,0x0u,0xda0u,0x0u,0x80000000u,0x0u,0xdb0u,0x0u,0x80000000u,0x0u,0xdc0u,0x0u,0x80000000u,0x0u,0xdd0u,0x0u,0x80000000u,0x0u,0xde0u,0x0u,0x80000000u,0x0u,0xdf0u,0x0u,0x80000000u,0x0u,0xe00u,0x0u,0x80000000u,0x0u,0xe10u,0x0u,0x80000000u,0x0u,0xe20u,0x0u,0x80000000u,0x0u,0xe30u,0x0u,0x80000000u,0x0u,0xe40u,0x0u,0x80000000u,0x0u,0xe50u,0x0u,0x80000000u,0x0u,0xe60u,0x0u,0x80000000u,0x0u,0xe70u,0x0u,0x80000000u,0x0u,0xe80u,0x0u,0x80000000u,0x0u,0xe90u,0x0u,0x80000000u,0x0u,0xea0u,0x0u,0x80000000u,0x0u,0xeb0u,0x0u,0x80000000u,0x0u,0xec0u,0x0u,0x80000000u,0x0u,0xed0u,0x0u,0x80000000u,0x0u,0xee0u,0x0u,0x80000000u,0x0u,0xef0u,0x0u,0x80000000u,0x0u,0xf00u,0x0u,0x80000000u,0x0u,0xf10u,0x0u,0x80000000u,0x0u,0xf20u,0x0u,0x80000000u,0x0u,0xf30u,0x0u,0x80000000u,0x0u,0xf40u,0x0u,0x80000000u,0x0u,0xf50u,0x0u,0x80000000u,0x0u,0xf60u,0x0u,0x80000000u,0x0u,0xf70u,0x0u,0x80000000u,0x0u,0xf80u,0x0u,0x80000000u,0x0u,0xf90u,0x0u,0x80000000u,0x0u,0xfa0u,0x0u,0x80000000u,0x0u,0xfb0u,0x0u,0x80000000u,0x0u,0xfc0u,0x0u,0x80000000u,0x0u,0xfd0u,0x0u,0x80000000u,0x0u,0xfe0u,0x0u,0x80000000u,0x0u,0xff0u,0x0u,0x12c0u,0x0u,0x2618cu,0x0u,0x12c0u,0x0u,0x2619cu,0x0u,0x12c0u,0x0u,0x261acu,0x0u,0x12c0u,0x0u,0x261bcu,0x0u,0x12c0u,0x0u,0x261ccu,0x0u,0x12c0u,0x0u,0x261dcu,0x0u,0x12c0u,0x0u,0x261ecu,0x0u,0x12c0u,0x0u,0x261fcu,0x0u,0x12c0u,0x0u,0x2620cu,0x0u,0x12c0u,0x0u,0x2621cu,0x0u,0x12c0u,0x0u,0x2622cu,0x0u,0x12c0u,0x0u,0x2623cu,0x0u,0x12c0u,0x0u,0x2624cu,0x0u,0x12c0u,0x0u,0x2625cu,0x0u,0x12c0u,0x0u,0x2626cu,0x0u,0x12c0u,0x0u,0x2627cu,0x0u,0x12c0u,0x0u,0x2628cu,0x0u,0x12c0u,0x0u,0x2629cu,0x0u,0x12c0u,0x0u,0x262acu,0x0u,0x12c0u,0x0u,0x262bcu,0x0u,0x12c0u,0x0u,0x262ccu,0x0u,0x12c0u,0x0u,0x262dcu,0x0u,0x12c0u,0x0u,0x262ecu,0x0u,0x12c0u,0x0u,0x262fcu,0x0u,0x12c0u,0x0u,0x2630cu,0x0u,0x12c0u,0x0u,0x2631cu,0x0u,0x12c0u,0x0u,0x2632cu,0x0u,0x12c0u,0x0u,0x2633cu,0x0u,0x12c0u,0x0u,0x2634cu,0x0u,0x12c0u,0x0u,0x2635cu,0x0u,0x12c0u,0x0u,0x2636cu,0x0u,0x12c0u,0x0u,0x2637cu,0x0u,0x12c0u,0x0u,0x2638cu,0x0u,0x12c0u,0x0u,0x2639cu,0x0u,0x12c0u,0x0u,0x263acu,0x0u,0x12c0u,0x0u,0x263bcu,0x0u,0x12c0u,0x0u,0x263ccu,0x0u,0x12c0u,0x0u,0x263dcu,0x0u,0x12c0u,0x0u,0x263ecu,0x0u,0x12c0u,0x0u,0x263fcu,0x0u,0x12c0u,0x0u,0x2640cu,0x0u,0x12c0u,0x0u,0x2641cu,0x0u,0x12c0u,0x0u,0x2642cu,0x0u,0x12c0u,0x0u,0x2643cu,0x0u,0x12c0u,0x0u,0x2644cu,0x0u,0x12c0u,0x0u,0x2645cu,0x0u,0x12c0u,0x0u,0x2646cu,0x0u,0x12c0u,0x0u,0x2647cu,0x0u,0x12c0u,0x0u,0x2648cu,0x0u,0x12c0u,0x0u,0x2649cu,0x0u,0x12c0u,0x0u,0x264acu,0x0u,0x12c0u,0x0u,0x264bcu,0x0u,0x12c0u,0x0u,0x264ccu,0x0u,0x12c0u,0x0u,0x264dcu,0x0u,0x12c0u,0x0u,0x264ecu,0x0u,0x12c0u,0x0u,0x264fcu,0x0u,0x12c0u,0x0u,0x2650cu,0x0u,0x12c0u,0x0u,0x2651cu,0x0u,0x12c0u,0x0u,0x2652cu,0x0u,0x12c0u,0x0u,0x2653cu,0x0u,0x12c0u,0x0u,0x2654cu,0x0u,0x12c0u,0x0u,0x2655cu,0x0u,0x12c0u,0x0u,0x2656cu,0x0u,0x12c0u,0x0u,0x2657cu,0x3f803f80u,0x3f803f80u,0x3f803f80u,0x3f803f80u,0x0u,0x0u,0x0u,0x0u,0x0u,0x0u,0x0u,0x0u,0x0u,0x0u,0x0u,0x0u,0x3f803f80u,0x3f803f80u,0x3f803f80u,0x3f803f80u,0x0u,0x0u,0x0u,0x0u,0x0u,0x0u,0x0u,0x0u,0x0u,0x0u,0x0u,0x0u,0x3f803f80u,0x3f803f80u,0x3f803f80u,0x3f803f80u,0x0u,0x0u,0x0u,0x0u,0x0u,0x0u,0x0u,0x0u,0x0u,0x0u,0x0u,0x0u,0x3f803f80u,0x3f803f80u,0x3f803f80u,0x3f803f80u,0x0u,0x0u,0x0u,0x0u,0x0u,0x0u,0x0u,0x0u,0x0u,0x0u,0x0u,0x0u,0x3f803f80u,0x3f803f80u,0x3f803f80u,0x3f803f80u,0x0u,0x0u,0x0u,0x0u,0x0u,0x0u,0x0u,0x0u,0x0u,0x0u,0x0u,0x0u,0x3f803f80u,0x3f803f80u,0x3f803f80u,0x3f803f80u,0x0u,0x0u,0x0u,0x0u,0x0u,0x0u,0x0u,0x0u,0x0u,0x0u,0x0u,0x0u,0x3f803f80u,0x3f803f80u,0x3f803f80u,0x3f803f80u,0x0u,0x0u,0x0u,0x0u,0x0u,0x0u,0x0u,0x0u,0x0u,0x0u,0x0u,0x0u,0x3f803f80u,0x3f803f80u,0x3f803f80u,0x3f803f80u,0x0u,0x0u,0x0u,0x0u,0x0u,0x0u,0x0u,0x0u,0x0u,0x0u,0x0u,0x0u,0x3f803f80u,0x3f803f80u,0x3f803f80u,0x3f803f80u,0x0u,0x0u,0x0u,0x0u,0x0u,0x0u,0x0u,0x0u,0x0u,0x0u,0x0u,0x0u,0x3f803f80u,0x3f803f80u,0x3f803f80u,0x3f803f80u,0x0u,0x0u,0x0u,0x0u,0x0u,0x0u,0x0u,0x0u,0x0u,0x0u,0x0u,0x0u,0x3f803f80u,0x3f803f80u,0x3f803f80u,0x3f803f80u,0x0u,0x0u,0x0u,0x0u,0x0u,0x0u,0x0u,0x0u,0x0u,0x0u,0x0u,0x0u,0x3f803f80u,0x3f803f80u,0x3f803f80u,0x3f803f80u,0x0u,0x0u,0x0u,0x0u,0x0u,0x0u,0x0u,0x0u,0x0u,0x0u,0x0u,0x0u,0x3f803f80u,0x3f803f80u,0x3f803f80u,0x3f803f80u,0x0u,0x0u,0x0u,0x0u,0x0u,0x0u,0x0u,0x0u,0x0u,0x0u,0x0u,0x0u,0x3f803f80u,0x3f803f80u,0x3f803f80u,0x3f803f80u,0x0u,0x0u,0x0u,0x0u,0x0u,0x0u,0x0u,0x0u,0x0u,0x0u,0x0u,0x0u,0x3f803f80u,0x3f803f80u,0x3f803f80u,0x3f803f80u,0x0u,0x0u,0x0u,0x0u,0x0u,0x0u,0x0u,0x0u,0x0u,0x0u,0x0u,0x0u,0x3f803f80u,0x3f803f80u,0x3f803f80u,0x3f803f80u,0x0u,0x0u,0x0u,0x0u,0x0u,0x0u,0x0u,0x0u,0x0u,0x0u,0x0u,0x0u,0x36030u,0x0u,0x0u,0x0u,0x36030u,0x0u,0x0u,0x0u,0x36030u,0x0u,0x0u,0x0u,0x36030u,0x0u,0x0u,0x0u,0x36030u,0x0u,0x0u,0x0u,0x36030u,0x0u,0x0u,0x0u,0x36030u,0x0u,0x0u,0x0u,0x36030u,0x0u,0x0u,0x0u,0x360b2u,0x0u,0x0u,0x0u,0x360b2u,0x0u,0x0u,0x0u,0x360b2u,0x0u,0x0u,0x0u,0x360b2u,0x0u,0x0u,0x0u,0x360b2u,0x0u,0x0u,0x0u,0x360b2u,0x0u,0x0u,0x0u,0x360b2u,0x0u,0x0u,0x0u,0x360b2u,0x0u,0x0u,0x0u,0x36092u,0x0u,0x0u,0x0u,0x36092u,0x0u,0x0u,0x0u,0x36092u,0x0u,0x0u,0x0u,0x36092u,0x0u,0x0u,0x0u,0x36092u,0x0u,0x0u,0x0u,0x36092u,0x0u,0x0u,0x0u,0x36092u,0x0u,0x0u,0x0u,0x36092u,0x0u,0x0u,0x0u,0x3609au,0x0u,0x0u,0x0u,0x3609au,0x0u,0x0u,0x0u,0x3609au,0x0u,0x0u,0x0u,0x3609au,0x0u,0x0u,0x0u,0x3609au,0x0u,0x0u,0x0u,0x3609au,0x0u,0x0u,0x0u,0x3609au,0x0u,0x0u,0x0u,0x3609au,0x0u,0x0u,0x0u,0x36059u,0x0u,0x0u,0x0u,0x36059u,0x0u,0x0u,0x0u,0x36059u,0x0u,0x0u,0x0u,0x36059u,0x0u,0x0u,0x0u,0x36059u,0x0u,0x0u,0x0u,0x36059u,0x0u,0x0u,0x0u,0x36059u,0x0u,0x0u,0x0u,0x36059u,0x0u,0x0u,0x0u,0x36059u,0x0u,0x0u,0x0u,0x36059u,0x0u,0x0u,0x0u,0x36059u,0x0u,0x0u,0x0u,0x36059u,0x0u,0x0u,0x0u,0x36059u,0x0u,0x0u,0x0u,0x36059u,0x0u,0x0u,0x0u,0x36059u,0x0u,0x0u,0x0u,0x36059u,0x0u,0x0u,0x0u,0x16071u,0x0u,0x0u,0x0u,0x16071u,0x0u,0x0u,0x0u,0x16071u,0x0u,0x0u,0x0u,0x16071u,0x0u,0x0u,0x0u,0x16071u,0x0u,0x0u,0x0u,0x16071u,0x0u,0x0u,0x0u,0x16071u,0x0u,0x0u,0x0u,0x16071u,0x0u,0x0u,0x0u,0x14071u,0x0u,0x0u,0x0u,0x14071u,0x0u,0x0u,0x0u,0x14071u,0x0u,0x0u,0x0u,0x14071u,0x0u,0x0u,0x0u,0x14071u,0x0u,0x0u,0x0u,0x14071u,0x0u,0x0u,0x0u,0x14071u,0x0u,0x0u,0x0u,0x14071u,0x0u,0x0u,0x0u};

__global__ __launch_bounds__(256, 1) void swin_main(const uint16_t* x, const uint16_t* mask, char* ws, uint16_t* out,
                                                    const uint16_t* wqkv, const uint16_t* wo, const uint16_t* ls,
                                                    const uint16_t* w1, const uint16_t* w2, const uint16_t* rc,
                                                    const int* ridx) {
  __shared__ __attribute__((aligned(16))) char smem[LDS_BYTES];
  const int wave = __builtin_amdgcn_readfirstlane(threadIdx.x >> 6);
  const unsigned lds0 = (unsigned)(size_t)smem;
  asm volatile(
      ".p2align 5\n"
      "s_mov_b64 s[88:89], %0\n"
      "s_mov_b32 s40, %1\n"
      "s_mov_b32 s41, %2\n"
      "s_mov_b64 s[92:93], %3\n"
      "s_load_dwordx2 s[16:17], s[88:89], 0\n"
      "s_load_dwordx2 s[24:25], s[88:89], 8\n"
      "s_load_dwordx2 s[34:35], s[88:89], 16\n"
      "s_load_dwordx2 s[20:21], s[88:89], 24\n"
      "s_load_dwordx2 s[28:29], s[88:89], 32\n"
      "s_load_dwordx2 s[36:37], s[88:89], 40\n"
      "s_load_dwordx8 s[80:87], s[88:89], 48\n"
      "s_load_dwordx2 s[90:91], s[88:89], 80\n"
      "s_getpc_b64 s[98:99]\n"
      "L_ipf0_%=:\n"
      "v_mbcnt_lo_u32_b32 v96, -1, 0\n"
      "v_mbcnt_hi_u32_b32 v96, -1, v96\n"
      "v_lshlrev_b32 v97, 2, v96\n"
      "v_lshlrev_b32 v96, 4, v96\n"
      "s_cmp_lt_u32 s41, 209\n"
      "s_cselect_b32 s44, 20, 19\n"
      "s_mov_b32 s18, 100270464\n"
      "s_mov_b32 s19, 0x20000\n"
      "s_mov_b32 s22, 100270464\n"
      "s_mov_b32 s23, 0x20000\n"
      "s_mov_b32 s26, 25589860\n"
      "s_mov_b32 s27, 0x20000\n"
      "s_mov_b32 s30, 221184\n"
      "s_mov_b32 s31, 0x20000\n"
      "s_mov_b32 s38, 73728\n"
      "s_mov_b32 s39, 0x20000\n"
      "s_lshl_b32 s70, s40, 3\n"
      "v_lshrrev_b32 v98, 7, v96\n"
      "v_add_u32_e64 v98, s70, v98\n"
      "v_bfe_u32 v99, v98, 1, 3\n"
      "v_min_u32 v98, s44, v98\n"
      "s_movk_i32 s68, 4176\n"
      "s_cmp_lt_u32 s41, 209\n"
      "s_cselect_b32 s67, 209, 47\n"
      "s_cselect_b32 s68, 799, s68\n"
      "s_cselect_b32 s69, 141, 0\n"
      "s_add_u32 s68, s68, s41\n"
      "v_lshlrev_b32 v100, 8, v98\n"
      "v_add_u32 v100, s41, v100\n"
      "v_mul_u32_u24 v101, s67, v98\n"
      "v_add_u32 v101, s68, v101\n"
      "v_cmp_lt_u32 vcc, 16, v98\n"
      "s_nop 1\n"
      "v_cndmask_b32 v100, v100, v101, vcc\n"
      "v_subrev_u32 v98, 5184, v100\n"
      "v_cmp_le_u32 vcc, 5184, v100\n"
      "v_add_u32_e64 v98, s69, v98\n"
      "v_mul_u32_u24 v102, 58255, v100\n"
      "v_lshrrev_b32 v102, 22, v102\n"
      "v_mul_u32_u24 v103, 72, v102\n"
      "v_sub_u32 v103, v100, v103\n"
      "v_min_u32 v101, 72, v98\n"
      "v_cndmask_b32 v102, v102, v101, vcc\n"
      "v_subrev_u32 v101, 72, v98\n"
      "v_min_u32 v101, 72, v101\n"
      "v_cndmask_b32 v103, v103, v101, vcc\n"
      "v_mul_u32_u24 v98, 7, v102\n"
      "v_add_u32 v98, 9, v98\n"
      "v_subrev_u32 v102, 0x1ff, v98\n"
      "v_min_u32 v98, v98, v102\n"
      "v_mul_u32_u24 v102, 7, v103\n"
      "v_add_u32 v102, 9, v102\n"
      "v_subrev_u32 v103, 0x1ff, v102\n"
      "v_min_u32 v102, v102, v103\n"
      "v_mul_u32_u24 v98, 0x1ff, v98\n"
      "v_add_u32 v98, v98, v102\n"
      "v_mul_u32_u24 v102, 0x180, v98\n"
      "v_bfe_u32 v98, v96, 4, 3\n"
      "v_xor_b32 v99, v99, v98\n"
      "v_lshl_add_u32 v102, v99, 4, v102\n"
      "s_mul_i32 s70, s40, 96\n"
      "v_lshrrev_b32 v99, 6, v96\n"
      "v_and_b32 v98, 48, v96\n"
      "v_add_u32_e64 v98, s70, v98\n"
      "v_mul_u32_u24 v103, 384, v99\n"
      "v_mul_u32_u24 v99, 1152, v99\n"
      "v_add_u32 v99, v99, v98\n"
      "v_add_u32 v103, v103, v98\n"
      "v_bfe_u32 v98, v96, 5, 1\n"
      "v_mul_u32_u24 v98, 0x160, v98\n"
      "v_add_u32 v98, v98, v99\n"
      "v_bfe_u32 v101, v96, 4, 4\n"
      "v_mul_u32_u24 v100, 196, v101\n"
      "v_lshrrev_b32 v101, 4, v96\n"
      "v_and_b32 v101, 48, v101\n"
      "v_add_u32 v101, v101, v100\n"
      "v_min_u32 v104, 192, v97\n"
      "s_lshl_b32 s67, s40, 8\n"
      "v_add_u32 v105, s67, v97\n"
      "v_min_u32 v105, 672, v105\n"
      "v_add_u32 v97, 158592, v105\n"
      "s_mul_i32 s78, s40, 18432\n"
      "s_add_u32 s78, s78, 36864\n"
      "s_waitcnt lgkmcnt(0)\n"
      "s_load_dwordx2 s[94:95], s[80:81], 0\n"
      "s_load_dwordx16 s[0:15], s[82:83], 0\n"
      "s_lshr_b32 s67, s41, 3\n"
      "s_mov_b64 s[72:73], s[28:29]\n"
      "s_movk_i32 s68, 54\n"
      "s_mov_b32 s69, 221056\n"
      "s_cmp_eq_u32 s40, 1\n"
      "s_cselect_b64 s[72:73], s[36:37], s[72:73]\n"
      "s_cselect_b32 s68, 18, s68\n"
      "s_cselect_b32 s69, 73600, s69\n"
      "s_cmp_eq_u32 s40, 2\n"
      "s_cselect_b64 s[72:73], s[98:99], s[72:73]\n"
      "s_cselect_b32 s68, 16, s68\n"
      "s_cselect_b32 s69, L_cend_%=-L_ipf0_%=-128, s69\n"
      "s_cmp_eq_u32 s40, 3\n"
      "s_cselect_b64 s[72:73], s[92:93], s[72:73]\n"
      "s_cselect_b32 s68, 10, s68\n"
      "s_cselect_b32 s69, 40832, s69\n"
      "s_mul_i32 s67, s67, s68\n"
      "s_lshl_b32 s67, s67, 7\n"
      "s_sub_u32 s68, s68, 1\n"
      "s_lshl_b32 s68, s68, 7\n"
      "v_lshlrev_b32 v106, 3, v96\n"
      "v_min_u32_e64 v106, s68, v106\n"
      "v_add_u32_e64 v106, s67, v106\n"
      "v_min_u32_e64 v106, s69, v106\n"
      "global_load_dword v107, v106, s[72:73]\n"
      "s_lshl_b32 s67, s40, 10\n"
      "s_add_u32 m0, s67, 134784\n"
      "s_nop 0\n"
      "buffer_load_dwordx4 v102, s[16:19], 0 offen  lds\n"
      "s_add_u32 m0, s67, 138752\n"
      "s_nop 0\n"
      "buffer_load_dwordx4 v102, s[16:19], 0 offen offset:128  lds\n"
      "s_add_u32 m0, s67, 142720\n"
      "s_nop 0\n"
      "buffer_load_dwordx4 v102, s[16:19], 0 offen offset:256  lds\n"
      "global_load_dword v105, v105, s[86:87]\n"
      "s_mov_b32 s68, 0\n"
      "s_add_u32 m0, s78, 0\n"
      "s_nop 0\n"
      "buffer_load_dwordx4 v99, s[28:31], s68 offen  lds\n"
      "s_add_u32 m0, s78, 1984\n"
      "s_nop 0\n"
      "buffer_load_dwordx4 v98, s[28:31], s68 offen offset:64  lds\n"
      "s_add_u32 m0, s78, 3712\n"
      "s_nop 0\n"
      "buffer_load_dwordx4 v99, s[28:31], s68 offen offset:384  lds\n"
      "s_mov_b32 s68, 18432\n"
      "s_add_u32 m0, s78, 1024\n"
      "s_nop 0\n"
      "buffer_load_dwordx4 v99, s[28:31], s68 offen  lds\n"
      "s_add_u32 m0, s78, 3008\n"
      "s_nop 0\n"
      "buffer_load_dwordx4 v98, s[28:31], s68 offen offset:64  lds\n"
      "s_add_u32 m0, s78, 4736\n"
      "s_nop 0\n"
      "buffer_load_dwordx4 v99, s[28:31], s68 offen offset:384  lds\n"
      "s_mul_i32 s67, s40, 10240\n"
      "s_add_u32 s92, s92, s67\n"
      "s_addc_u32 s93, s93, 0\n"
      "global_load_dwordx4 v[0:3], v96, s[92:93]\n"
      "global_load_dwordx4 v[4:7], v96, s[92:93] offset:1024\n"
      "global_load_dwordx4 v[8:11], v96, s[92:93] offset:2048\n"
      "global_load_dwordx4 v[12:15], v96, s[92:93] offset:3072\n"
      "s_add_u32 s92, s92, 4096\n"
      "s_addc_u32 s93, s93, 0\n"
      "global_load_dwordx4 v[16:19], v96, s[92:93]\n"
      "global_load_dwordx4 v[20:23], v96, s[92:93] offset:1024\n"
      "global_load_dwordx4 v[24:27], v96, s[92:93] offset:2048\n"
      "global_load_dwordx4 v[28:31], v96, s[92:93] offset:3072\n"
      "s_add_u32 s92, s92, 4096\n"
      "s_addc_u32 s93, s93, 0\n"
      "global_load_dwordx4 v[32:35], v96, s[92:93]\n"
      "global_load_dword v36, v96, s[92:93] offset:1024\n"
      "s_mov_b32 s68, 36864\n"
      "s_add_u32 m0, s78, 6144\n"
      "s_nop 0\n"
      "buffer_load_dwordx4 v99, s[28:31], s68 offen  lds\n"
      "s_add_u32 m0, s78, 8128\n"
      "s_nop 0\n"
      "buffer_load_dwordx4 v98, s[28:31], s68 offen offset:64  lds\n"
      "s_add_u32 m0, s78, 9856\n"
      "s_nop 0\n"
      "buffer_load_dwordx4 v99, s[28:31], s68 offen offset:384  lds\n"
      "s_mov_b32 s68, 55296\n"
      "s_add_u32 m0, s78, 7168\n"
      "s_nop 0\n"
      "buffer_load_dwordx4 v99, s[28:31], s68 offen  lds\n"
      "s_add_u32 m0, s78, 9152\n"
      "s_nop 0\n"
      "buffer_load_dwordx4 v98, s[28:31], s68 offen offset:64  lds\n"
      "s_add_u32 m0, s78, 10880\n"
      "s_nop 0\n"
      "buffer_load_dwordx4 v99, s[28:31], s68 offen offset:384  lds\n"
      "global_load_dwordx4 v[40:43], v101, s[90:91]\n"
      "global_load_dwordx4 v[44:47], v101, s[90:91] offset:64\n"
      "global_load_dwordx4 v[48:51], v101, s[90:91] offset:128\n"
      "global_load_dword v102, v100, s[90:91] offset:192\n"
      "s_add_u32 s90, s90, 3136\n"
      "s_addc_u32 s91, s91, 0\n"
      "global_load_dwordx4 v[52:55], v101, s[90:91]\n"
      "global_load_dwordx4 v[56:59], v101, s[90:91] offset:64\n"
      "global_load_dwordx4 v[60:63], v101, s[90:91] offset:128\n"
      "global_load_dword v106, v100, s[90:91] offset:192\n"
      "s_add_u32 s90, s90, 3136\n"
      "s_addc_u32 s91, s91, 0\n"
      "global_load_dwordx4 v[64:67], v101, s[90:91]\n"
      "global_load_dwordx4 v[68:71], v101, s[90:91] offset:64\n"
      "global_load_dwordx4 v[72:75], v101, s[90:91] offset:128\n"
      "global_load_dword v101, v100, s[90:91] offset:192\n"
      "s_add_u32 s90, s90, 3136\n"
      "s_addc_u32 s91, s91, 0\n"
      "global_load_dword v100, v104, s[90:91]\n"
      "s_mov_b32 s68, 73728\n"
      "s_add_u32 m0, s78, 12288\n"
      "s_nop 0\n"
      "buffer_load_dwordx4 v99, s[28:31], s68 offen  lds\n"
      "s_add_u32 m0, s78, 14272\n"
      "s_nop 0\n"
      "buffer_load_dwordx4 v98, s[28:31], s68 offen offset:64  lds\n"
      "s_add_u32 m0, s78, 16000\n"
      "s_nop 0\n"
      "buffer_load_dwordx4 v99, s[28:31], s68 offen offset:384  lds\n"
      "s_mov_b32 s68, 92160\n"
      "s_add_u32 m0, s78, 13312\n"
      "s_nop 0\n"
      "buffer_load_dwordx4 v99, s[28:31], s68 offen  lds\n"
      "s_add_u32 m0, s78, 15296\n"
      "s_nop 0\n"
      "buffer_load_dwordx4 v98, s[28:31], s68 offen offset:64  lds\n"
      "s_add_u32 m0, s78, 17024\n"
      "s_nop 0\n"
      "buffer_load_dwordx4 v99, s[28:31], s68 offen offset:384  lds\n"
      "s_and_b32 s67, s40, 1\n"
      "s_lshl_b32 s67, s67, 5\n"
      "s_add_u32 s68, s67, 768\n"
      "v_add_u32 v104, s68, v99\n"
      "s_waitcnt vmcnt(45)\n"
      "v_lshrrev_b32 v107, 5, v96\n"
      "v_and_b32 v76, 16, v96\n"
      "v_mul_u32_u24 v77, 384, v107\n"
      "v_mul_u32_u24 v107, 1152, v107\n"
      "s_lshl_b32 s67, s67, 1\n"
      "s_sub_u32 s68, 832, s67\n"
      "s_add_u32 s68, s68, s70\n"
      "v_add3_u32 v107, v107, v76, s68\n"
      "s_add_u32 s68, s70, 64\n"
      "v_add3_u32 v77, v77, v76, s68\n"
      "v_and_b32 v76, 0x300, v96\n"
      "v_and_b32 v80, 0xc0, v96\n"
      "v_bfe_u32 v81, v96, 4, 2\n"
      "v_lshl_or_b32 v84, v76, 1, v80\n"
      "v_lshl_or_b32 v84, v81, 3, v84\n"
      "v_add_u32 v84, s78, v84\n"
      "v_lshrrev_b32 v80, 1, v80\n"
      "v_lshl_or_b32 v80, v81, 3, v80\n"
      "v_add3_u32 v81, v76, v80, s78\n"
      "v_and_b32 v108, 0x3f0, v96\n"
      "v_add_u32 v108, s78, v108\n"
      "v_lshrrev_b32 v76, 1, v76\n"
      "v_add3_u32 v109, v76, v80, s78\n"
      "v_lshrrev_b32 v76, 4, v96\n"
      "v_subrev_u32 v76, 3, v76\n"
      "v_cmp_ge_u32 s[56:57], s44, v76\n"
      "v_min_u32 v76, s44, v76\n"
      "s_movk_i32 s68, 4176\n"
      "s_cmp_lt_u32 s41, 209\n"
      "s_cselect_b32 s67, 209, 47\n"
      "s_cselect_b32 s68, 799, s68\n"
      "s_cselect_b32 s69, 141, 0\n"
      "s_add_u32 s68, s68, s41\n"
      "v_lshlrev_b32 v80, 8, v76\n"
      "v_add_u32 v80, s41, v80\n"
      "v_mul_u32_u24 v85, s67, v76\n"
      "v_add_u32 v85, s68, v85\n"
      "v_cmp_lt_u32 vcc, 16, v76\n"
      "s_nop 1\n"
      "v_cndmask_b32 v80, v80, v85, vcc\n"
      "v_subrev_u32 v76, 5184, v80\n"
      "v_cmp_le_u32 vcc, 5184, v80\n"
      "v_add_u32 v76, s69, v76\n"
      "v_mul_u32_u24 v88, 58255, v80\n"
      "v_lshrrev_b32 v88, 22, v88\n"
      "v_mul_u32_u24 v89, 72, v88\n"
      "v_sub_u32_e64 v89, v80, v89\n"
      "v_min_u32 v85, 72, v76\n"
      "v_cndmask_b32 v88, v88, v85, vcc\n"
      "v_subrev_u32 v85, 72, v76\n"
      "v_min_u32 v85, 72, v85\n"
      "v_cndmask_b32 v89, v89, v85, vcc\n"
      "v_mul_u32_u24 v38, 73, v88\n"
      "v_add_u32 v38, v38, v89\n"
      "v_mul_u32_u24 v38, 4802, v38\n"
      "v_mul_u32_u24 v37, 0x1ff, v88\n"
      "v_add_u32 v37, v37, v89\n"
      "v_mul_u32_u24 v37, 2688, v37\n"
      "v_add_u32 v37, 589824, v37\n"
      "v_cmp_eq_u32 vcc, 72, v88\n"
      "v_or_b32 v76, 0x80000000, v37\n"
      "s_nop 0\n"
      "v_cndmask_b32 v37, v37, v76, vcc\n"
      "v_cmp_eq_u32 vcc, 72, v89\n"
      "v_or_b32_e64 v76, 0x40000000, v37\n"
      "s_nop 0\n"
      "v_cndmask_b32 v37, v37, v76, vcc\n"
      "v_mov_b32 v80, 0x20000000\n"
      "v_cndmask_b32_e64 v37, v80, v37, s[56:57]\n"
      "v_cndmask_b32_e64 v38, v80, v38, s[56:57]\n"
      "v_mov_b32 v79, 0\n"
      "v_mov_b32 v83, 0\n"
      "v_mov_b32 v87, 0\n"
      "v_mov_b32 v91, 0\n"
      "v_mov_b32_e64 v95, 0\n"
      "s_mov_b32 s53, 0x3fb8aa3b\n"
      "s_mov_b32 s51, 0x2a8cbccc\n"
      "s_mov_b32 s52, 0x3ebcbe62\n"
      "s_mov_b32 s58, 0xffff0000\n"
      "s_mov_b32 s59, 0x0\n"
      "s_mov_b32 s60, 0x0\n"
      "s_mov_b32 s61, 0xffff\n"
      "s_mov_b32 s62, 0x0\n"
      "s_mov_b32 s63, 0xffff0000\n"
      "s_lshl_b32 s64, s40, 10\n"
      "s_cmp_lt_u32 s40, 2\n"
      "s_add_u32 s67, s64, 16384\n"
      "s_cmp_lt_u32 s40, 2\n"
      "s_cselect_b32 s65, s67, 161408\n"
      "s_add_u32 s67, s64, 34816\n"
      "s_cmp_lt_u32 s40, 2\n"
      "s_cselect_b32 s66, s67, 161408\n"
      "s_mov_b32 s42, -1\n"
      "s_mov_b32 s43, 3\n"
      "s_waitcnt lgkmcnt(0)\n"
      "s_lshl_b32 s67, s40, 4\n"
      "s_lshr_b64 s[94:95], s[94:95], s67\n"
      "s_lshl_b32 s94, s94, 16\n"
      "v_mov_b32 v80, s94\n"
      "v_max_f32 v80, 0xc2c80000, v80\n"
      "v_min_f32 v80, 0x40930000, v80\n"
      "v_mul_f32 v80, s53, v80\n"
      "v_exp_f32 v80, v80\n"
      "s_nop 0\n"
      "v_cvt_pk_bf16_f32 v80, v80, v80\n"
      "v_lshlrev_b32 v80, 16, v80\n"
      "v_mov_b32 v76, 0x41f00000\n"
      "v_cmp_nle_f32 s[54:55], v80, v76\n"
      "v_mul_f32 v80, s53, v80\n"
      "s_nop 0\n"
      "v_readfirstlane_b32 s50, v80\n"
      "s_pack_ll_b32_b16 s67, s0, s8\n"
      "s_waitcnt vmcnt(41)\n"
      "v_dot2_f32_bf16 v76, s67, v105, 0\n"
      "s_pack_hh_b32_b16 s68, s0, s8\n"
      "v_dot2_f32_bf16 v80, s68, v105, 0\n"
      "s_pack_ll_b32_b16 s69, s1, s9\n"
      "v_dot2_f32_bf16 v85, s69, v105, 0\n"
      "s_pack_hh_b32_b16 s70, s1, s9\n"
      "v_dot2_f32_bf16 v88, s70, v105, 0\n"
      "s_pack_ll_b32_b16 s67, s2, s10\n"
      "v_dot2_f32_bf16 v89, s67, v105, 0\n"
      "s_pack_hh_b32_b16 s68, s2, s10\n"
      "v_dot2_f32_bf16 v90, s68, v105, 0\n"
      "s_pack_ll_b32_b16 s69, s3, s11\n"
      "v_dot2_f32_bf16 v78, s69, v105, 0\n"
      "s_pack_hh_b32_b16 s70, s3, s11\n"
      "v_dot2_f32_bf16 v82, s70, v105, 0\n"
      "s_pack_ll_b32_b16 s67, s4, s12\n"
      "v_dot2_f32_bf16 v86, s67, v105, 0\n"
      "s_pack_hh_b32_b16 s68, s4, s12\n"
      "v_dot2_f32_bf16 v92, s68, v105, 0\n"
      "s_pack_ll_b32_b16 s69, s5, s13\n"
      "v_dot2_f32_bf16 v93, s69, v105, 0\n"
      "s_pack_hh_b32_b16 s70, s5, s13\n"
      "v_dot2_f32_bf16 v94, s70, v105, 0\n"
      "s_pack_ll_b32_b16 s67, s6, s14\n"
      "v_dot2_f32_bf16 v110, s67, v105, 0\n"
      "s_pack_hh_b32_b16 s68, s6, s14\n"
      "v_dot2_f32_bf16 v111, s68, v105, 0\n"
      "s_pack_ll_b32_b16 s69, s7, s15\n"
      "v_dot2_f32_bf16 v112, s69, v105, 0\n"
      "s_pack_hh_b32_b16 s70, s7, s15\n"
      "v_dot2_f32_bf16 v113, s70, v105, 0\n"
      "s_nop 2\n"
      "s_load_dwordx16 s[0:15], s[84:85], 0\n"
      "v_max_f32 v76, 0, v76\n"
      "v_max_f32 v80, 0, v80\n"
      "v_cvt_pk_bf16_f32 v76, v76, v80\n"
      "v_max_f32 v85, 0, v85\n"
      "v_max_f32 v88, 0, v88\n"
      "v_cvt_pk_bf16_f32 v85, v85, v88\n"
      "v_max_f32 v89, 0, v89\n"
      "v_max_f32 v90, 0, v90\n"
      "v_cvt_pk_bf16_f32 v89, v89, v90\n"
      "v_max_f32 v78, 0, v78\n"
      "v_max_f32 v82, 0, v82\n"
      "v_cvt_pk_bf16_f32 v78, v78, v82\n"
      "v_max_f32 v86, 0, v86\n"
      "v_max_f32 v92, 0, v92\n"
      "v_cvt_pk_bf16_f32 v86, v86, v92\n"
      "v_max_f32 v93, 0, v93\n"
      "v_max_f32 v94, 0, v94\n"
      "v_cvt_pk_bf16_f32 v93, v93, v94\n"
      "v_max_f32 v110, 0, v110\n"
      "v_max_f32 v111, 0, v111\n"
      "v_cvt_pk_bf16_f32 v110, v110, v111\n"
      "v_max_f32 v112, 0, v112\n"
      "v_max_f32 v113, 0, v113\n"
      "v_cvt_pk_bf16_f32 v112, v112, v113\n"
      "s_waitcnt lgkmcnt(0)\n"
      "s_pack_ll_b32_b16 s67, s0, s2\n"
      "v_dot2_f32_bf16 v105, s67, v76, 0\n"
      "s_pack_hh_b32_b16 s68, s0, s2\n"
      "v_dot2_f32_bf16 v80, s68, v76, 0\n"
      "s_pack_ll_b32_b16 s69, s1, s3\n"
      "v_dot2_f32_bf16 v88, s69, v76, 0\n"
      "s_pack_hh_b32_b16 s70, s1, s3\n"
      "v_dot2_f32_bf16 v90, s70, v76, 0\n"
      "s_pack_ll_b32_b16 s67, s4, s6\n"
      "v_dot2_f32_bf16 v105, s67, v85, v105\n"
      "s_pack_hh_b32_b16 s68, s4, s6\n"
      "v_dot2_f32_bf16 v80, s68, v85, v80\n"
      "s_pack_ll_b32_b16 s69, s5, s7\n"
      "v_dot2_f32_bf16 v88, s69, v85, v88\n"
      "s_pack_hh_b32_b16 s70, s5, s7\n"
      "v_dot2_f32_bf16 v90, s70, v85, v90\n"
      "s_pack_ll_b32_b16 s67, s8, s10\n"
      "v_dot2_f32_bf16 v105, s67, v89, v105\n"
      "s_pack_hh_b32_b16 s68, s8, s10\n"
      "v_dot2_f32_bf16 v80, s68, v89, v80\n"
      "s_pack_ll_b32_b16 s69, s9, s11\n"
      "v_dot2_f32_bf16 v88, s69, v89, v88\n"
      "s_pack_hh_b32_b16 s70, s9, s11\n"
      "v_dot2_f32_bf16 v90, s70, v89, v90\n"
      "s_pack_ll_b32_b16 s67, s12, s14\n"
      "v_dot2_f32_bf16 v105, s67, v78, v105\n"
      "s_pack_hh_b32_b16 s68, s12, s14\n"
      "v_dot2_f32_bf16 v80, s68, v78, v80\n"
      "s_pack_ll_b32_b16 s69, s13, s15\n"
      "v_dot2_f32_bf16 v88, s69, v78, v88\n"
      "s_pack_hh_b32_b16 s70, s13, s15\n"
      "v_dot2_f32_bf16 v90, s70, v78, v90\n"
      "s_load_dwordx16 s[0:15], s[84:85], 64\n"
      "s_waitcnt lgkmcnt(0)\n"
      "s_pack_ll_b32_b16 s67, s0, s2\n"
      "v_dot2_f32_bf16 v105, s67, v86, v105\n"
      "s_pack_hh_b32_b16 s68, s0, s2\n"
      "v_dot2_f32_bf16 v80, s68, v86, v80\n"
      "s_pack_ll_b32_b16 s69, s1, s3\n"
      "v_dot2_f32_bf16 v88, s69, v86, v88\n"
      "s_pack_hh_b32_b16 s70, s1, s3\n"
      "v_dot2_f32_bf16 v90, s70, v86, v90\n"
      "s_pack_ll_b32_b16 s67, s4, s6\n"
      "v_dot2_f32_bf16 v105, s67, v93, v105\n"
      "s_pack_hh_b32_b16 s68, s4, s6\n"
      "v_dot2_f32_bf16 v80, s68, v93, v80\n"
      "s_pack_ll_b32_b16 s69, s5, s7\n"
      "v_dot2_f32_bf16 v88, s69, v93, v88\n"
      "s_pack_hh_b32_b16 s70, s5, s7\n"
      "v_dot2_f32_bf16 v90, s70, v93, v90\n"
      "s_pack_ll_b32_b16 s67, s8, s10\n"
      "v_dot2_f32_bf16 v105, s67, v110, v105\n"
      "s_pack_hh_b32_b16 s68, s8, s10\n"
      "v_dot2_f32_bf16 v80, s68, v110, v80\n"
      "s_pack_ll_b32_b16 s69, s9, s11\n"
      "v_dot2_f32_bf16 v88, s69, v110, v88\n"
      "s_pack_hh_b32_b16 s70, s9, s11\n"
      "v_dot2_f32_bf16 v90, s70, v110, v90\n"
      "s_pack_ll_b32_b16 s67, s12, s14\n"
      "v_dot2_f32_bf16 v105, s67, v112, v105\n"
      "s_pack_hh_b32_b16 s68, s12, s14\n"
      "v_dot2_f32_bf16 v80, s68, v112, v80\n"
      "s_pack_ll_b32_b16 s69, s13, s15\n"
      "v_dot2_f32_bf16 v88, s69, v112, v88\n"
      "s_pack_hh_b32_b16 s70, s13, s15\n"
      "v_dot2_f32_bf16 v90, s70, v112, v90\n"
      "s_nop 2\n"
      "v_cvt_pk_bf16_f32 v105, v105, v105\n"
      "v_lshlrev_b32 v105, 16, v105\n"
      "v_mul_f32 v105, 0xbfb8aa3b, v105\n"
      "v_exp_f32 v105, v105\n"
      "s_nop 0\n"
      "v_add_f32 v105, 1.0, v105\n"
      "v_rcp_f32 v105, v105\n"
      "s_nop 0\n"
      "v_cvt_pk_bf16_f32 v105, v105, v105\n"
      "v_lshlrev_b32 v105, 16, v105\n"
      "v_add_f32 v105, -0.5, v105\n"
      "v_mul_f32 v105, 0x41b8aa3b, v105\n"
      "ds_write_b32 v97, v105\n"
      "v_cvt_pk_bf16_f32 v80, v80, v80\n"
      "v_lshlrev_b32 v80, 16, v80\n"
      "v_mul_f32 v80, 0xbfb8aa3b, v80\n"
      "v_exp_f32 v80, v80\n"
      "s_nop 0\n"
      "v_add_f32 v80, 1.0, v80\n"
      "v_rcp_f32 v80, v80\n"
      "s_nop 0\n"
      "v_cvt_pk_bf16_f32 v80, v80, v80\n"
      "v_lshlrev_b32 v80, 16, v80\n"
      "v_add_f32 v80, -0.5, v80\n"
      "v_mul_f32 v80, 0x41b8aa3b, v80\n"
      "ds_write_b32 v97, v80 offset:704\n"
      "v_cvt_pk_bf16_f32 v88, v88, v88\n"
      "v_lshlrev_b32_e64 v88, 16, v88\n"
      "v_mul_f32 v88, 0xbfb8aa3b, v88\n"
      "v_exp_f32 v88, v88\n"
      "s_nop 0\n"
      "v_add_f32 v88, 1.0, v88\n"
      "v_rcp_f32_e64 v88, v88\n"
      "s_nop 0\n"
      "v_cvt_pk_bf16_f32 v88, v88, v88\n"
      "v_lshlrev_b32 v88, 16, v88\n"
      "v_add_f32 v88, -0.5, v88\n"
      "v_mul_f32 v88, 0x41b8aa3b, v88\n"
      "ds_write_b32 v97, v88 offset:1408\n"
      "v_cvt_pk_bf16_f32 v90, v90, v90\n"
      "v_lshlrev_b32 v90, 16, v90\n"
      "v_mul_f32 v90, 0xbfb8aa3b, v90\n"
      "v_exp_f32_e64 v90, v90\n"
      "s_nop 0\n"
      "v_add_f32 v90, 1.0, v90\n"
      "v_rcp_f32_e64 v90, v90\n"
      "s_nop 0\n"
      "v_cvt_pk_bf16_f32 v90, v90, v90\n"
      "v_lshlrev_b32_e64 v90, 16, v90\n"
      "v_add_f32 v90, -0.5, v90\n"
      "v_mul_f32 v90, 0x41b8aa3b, v90\n"
      "ds_write_b32 v97, v90 offset:2112\n"
      "s_waitcnt lgkmcnt(0)\n"
      "s_barrier\n"
      "s_waitcnt vmcnt(35)\n"
      "ds_read_b64_tr_b16 a[0:1], v84\n"
      "ds_read_b64_tr_b16 a[2:3], v84 offset:256\n"
      "ds_read_b64_tr_b16 a[24:25], v84 offset:32\n"
      "ds_read_b64_tr_b16 a[26:27], v84 offset:288\n"
      "ds_read_b64_tr_b16 a[48:49], v84 offset:2048\n"
      "ds_read_b64_tr_b16 a[50:51], v84 offset:2304\n"
      "ds_read_b64_tr_b16 a[72:73], v84 offset:4096\n"
      "ds_read_b64_tr_b16 a[74:75], v84 offset:4352\n"
      "ds_read_b64_tr_b16 a[96:97], v84 offset:4128\n"
      "ds_read_b64_tr_b16 a[98:99], v84 offset:4384\n"
      "ds_read_b64_tr_b16 a[120:121], v84 offset:2080\n"
      "ds_read_b64_tr_b16 a[122:123], v84 offset:2336\n"
      "s_waitcnt vmcnt(25)\n"
      "v_readlane_b32 s49, v38, s43\n"
      "s_nop 0\n"
      "s_and_b32 s49, s49, -4\n"
      "buffer_load_dwordx4 v[110:113], v27, s[24:27], s49 offen\n"
      "buffer_load_dword v97, v29, s[24:27], s49 offen\n"
      "v_readlane_b32 s45, v37, s43\n"
      "s_nop 0\n"
      "s_mov_b32 s46, s45\n"
      "s_cmp_ge_u32 s45, 0x40000000\n"
      "s_cbranch_scc1 L_rare_1_%=\n"
      "L_back_2_%=:\n"
      "s_add_u32 m0, s64, 0\n"
      "s_nop 0\n"
      "buffer_load_dwordx4 v16, s[16:19], s46 offen  lds\n"
      "s_add_u32 m0, s64, 4096\n"
      "s_nop 0\n"
      "buffer_load_dwordx4 v17, s[16:19], s46 offen  lds\n"
      "s_add_u32 m0, s64, 8192\n"
      "s_nop 0\n"
      "buffer_load_dwordx4 v18, s[16:19], s46 offen  lds\n"
      "s_add_u32 m0, s64, 12288\n"
      "s_nop 0\n"
      "buffer_load_dwordx4 v19, s[16:19], s46 offen  lds\n"
      "s_mov_b32 m0, s65\n"
      "s_nop 0\n"
      "buffer_load_dwordx4 v15, s[16:19], s46 offen  lds\n"
      "s_cmp_ge_u32 s45, 0x40000000\n"
      "s_cbranch_scc1 L_rare_3_%=\n"
      "L_back_4_%=:\n"
      "s_waitcnt lgkmcnt(0)\n"
      "s_mov_b32 s68, 110592\n"
      "s_add_u32 m0, s78, 0\n"
      "s_nop 0\n"
      "buffer_load_dwordx4 v99, s[28:31], s68 offen  lds\n"
      "s_add_u32 m0, s78, 1984\n"
      "s_nop 0\n"
      "buffer_load_dwordx4 v98, s[28:31], s68 offen offset:64  lds\n"
      "s_add_u32 m0, s78, 3712\n"
      "s_nop 0\n"
      "buffer_load_dwordx4 v99, s[28:31], s68 offen offset:384  lds\n"
      "s_mov_b32 s68, 129024\n"
      "s_add_u32 m0, s78, 1024\n"
      "s_nop 0\n"
      "buffer_load_dwordx4 v99, s[28:31], s68 offen  lds\n"
      "s_add_u32 m0, s78, 3008\n"
      "s_nop 0\n"
      "buffer_load_dwordx4 v98, s[28:31], s68 offen offset:64  lds\n"
      "s_add_u32 m0, s78, 4736\n"
      "s_nop 0\n"
      "buffer_load_dwordx4 v99, s[28:31], s68 offen offset:384  lds\n"
      "v_add_u32 v105, 134784, v0\n"
      "v_add_u32 v80, 134784, v1\n"
      "ds_read_b128 v[114:117], v105\n"
      "ds_read_b128 v[118:121], v105 offset:2048\n"
      "s_mul_i32 s70, s40, 704\n"
      "s_add_u32 s70, s70, 158592\n"
      "ds_read_b128 v[122:125], v80\n"
      "ds_read_b128 v[126:129], v80 offset:2048\n"
      "s_waitcnt lgkmcnt(3)\n"
      "v_mfma_f32_16x16x32_bf16 v[130:133], a[0:3], v[114:117], 0\n"
      "v_mfma_f32_16x16x32_bf16 v[134:137], a[24:27], v[114:117], 0\n"
      "v_mfma_f32_16x16x32_bf16 v[138:141], a[48:51], v[114:117], 0\n"
      "v_mfma_f32_16x16x32_bf16 v[142:145], a[72:75], v[114:117], 0\n"
      "v_mfma_f32_16x16x32_bf16 v[146:149], a[96:99], v[114:117], 0\n"
      "v_mfma_f32_16x16x32_bf16 v[150:153], a[120:123], v[114:117], 0\n"
      "s_waitcnt lgkmcnt(0)\n"
      "v_mfma_f32_16x16x32_bf16 v[114:117], a[0:3], v[118:121], 0\n"
      "v_mfma_f32_16x16x32_bf16 v[154:157], a[24:27], v[118:121], 0\n"
      "v_mfma_f32_16x16x32_bf16 v[158:161], a[48:51], v[118:121], 0\n"
      "v_mfma_f32_16x16x32_bf16 v[162:165], a[72:75], v[118:121], 0\n"
      "v_mfma_f32_16x16x32_bf16 v[166:169], a[96:99], v[118:121], 0\n"
      "v_mfma_f32_16x16x32_bf16 v[170:173], a[120:123], v[118:121], 0\n"
      "s_waitcnt vmcnt(32)\n"
      "ds_read_b64_tr_b16 a[4:5], v84 offset:6144\n"
      "ds_read_b64_tr_b16 a[6:7], v84 offset:6400\n"
      "ds_read_b64_tr_b16 a[28:29], v84 offset:6176\n"
      "ds_read_b64_tr_b16 a[30:31], v84 offset:6432\n"
      "ds_read_b64_tr_b16 a[52:53], v84 offset:8192\n"
      "ds_read_b64_tr_b16 a[54:55], v84 offset:8448\n"
      "ds_read_b64_tr_b16 a[76:77], v84 offset:10240\n"
      "ds_read_b64_tr_b16 a[78:79], v84 offset:10496\n"
      "ds_read_b64_tr_b16 a[100:101], v84 offset:10272\n"
      "ds_read_b64_tr_b16 a[102:103], v84 offset:10528\n"
      "ds_read_b64_tr_b16 a[124:125], v84 offset:8224\n"
      "ds_read_b64_tr_b16 a[126:127], v84 offset:8480\n"
      "ds_read_b128 v[118:121], v105 offset:4096\n"
      "ds_read_b128 v[174:177], v105 offset:6144\n"
      "s_waitcnt lgkmcnt(12)\n"
      "v_mfma_f32_16x16x32_bf16 v[130:133], a[4:7], v[122:125], v[130:133]\n"
      "s_waitcnt lgkmcnt(10)\n"
      "v_mfma_f32_16x16x32_bf16 v[134:137], a[28:31], v[122:125], v[134:137]\n"
      "s_waitcnt lgkmcnt(7)\n"
      "v_mfma_f32_16x16x32_bf16 v[138:141], a[52:55], v[122:125], v[138:141]\n"
      "s_waitcnt lgkmcnt(3)\n"
      "v_mfma_f32_16x16x32_bf16 v[142:145], a[76:79], v[122:125], v[142:145]\n"
      "v_mfma_f32_16x16x32_bf16 v[146:149], a[100:103], v[122:125], v[146:149]\n"
      "s_waitcnt lgkmcnt(0)\n"
      "v_mfma_f32_16x16x32_bf16 v[150:153], a[124:127], v[122:125], v[150:153]\n"
      "v_mfma_f32_16x16x32_bf16 v[114:117], a[4:7], v[126:129], v[114:117]\n"
      "v_mfma_f32_16x16x32_bf16 v[154:157], a[28:31], v[126:129], v[154:157]\n"
      "v_mfma_f32_16x16x32_bf16 v[158:161], a[52:55], v[126:129], v[158:161]\n"
      "v_mfma_f32_16x16x32_bf16 v[162:165], a[76:79], v[126:129], v[162:165]\n"
      "v_mfma_f32_16x16x32_bf16 v[166:169], a[100:103], v[126:129], v[166:169]\n"
      "v_mfma_f32_16x16x32_bf16 v[170:173], a[124:127], v[126:129], v[170:173]\n"
      "s_waitcnt vmcnt(31)\n"
      "v_lshl_add_u32 v40, v40, 2, s70\n"
      "ds_read_b32 v40, v40\n"
      "v_lshl_add_u32 v41, v41, 2, s70\n"
      "ds_read_b32 v41, v41\n"
      "v_lshl_add_u32 v42, v42, 2, s70\n"
      "ds_read_b32 v42, v42\n"
      "v_lshl_add_u32 v43, v43, 2, s70\n"
      "ds_read_b32 v43, v43\n"
      "s_waitcnt vmcnt(30)\n"
      "v_lshl_add_u32 v44, v44, 2, s70\n"
      "ds_read_b32 v44, v44\n"
      "v_lshl_add_u32 v45, v45, 2, s70\n"
      "ds_read_b32 v45, v45\n"
      "v_lshl_add_u32 v46, v46, 2, s70\n"
      "ds_read_b32 v46, v46\n"
      "v_lshl_add_u32 v47, v47, 2, s70\n"
      "ds_read_b32 v47, v47\n"
      "s_waitcnt vmcnt(29)\n"
      "v_lshl_add_u32 v48, v48, 2, s70\n"
      "ds_read_b32 v48, v48\n"
      "v_lshl_add_u32 v49, v49, 2, s70\n"
      "ds_read_b32 v49, v49\n"
      "v_lshl_add_u32 v50, v50, 2, s70\n"
      "ds_read_b32 v50, v50\n"
      "v_lshl_add_u32 v51, v51, 2, s70\n"
      "ds_read_b32 v51, v51\n"
      "s_waitcnt vmcnt(27)\n"
      "v_lshl_add_u32 v52, v52, 2, s70\n"
      "ds_read_b32 v52, v52\n"
      "v_lshl_add_u32 v53, v53, 2, s70\n"
      "ds_read_b32 v53, v53\n"
      "v_lshl_add_u32 v54, v54, 2, s70\n"
      "ds_read_b32 v54, v54\n"
      "v_lshl_add_u32 v55, v55, 2, s70\n"
      "ds_read_b32 v55, v55\n"
      "s_waitcnt vmcnt(26)\n"
      "v_lshl_add_u32 v56, v56, 2, s70\n"
      "ds_read_b32 v56, v56\n"
      "v_lshl_add_u32 v57, v57, 2, s70\n"
      "ds_read_b32 v57, v57\n"
      "v_lshl_add_u32 v58, v58, 2, s70\n"
      "ds_read_b32 v58, v58\n"
      "v_lshl_add_u32 v59, v59, 2, s70\n"
      "ds_read_b32 v59, v59\n"
      "s_waitcnt lgkmcnt(0)\n"
      "s_mov_b32 s68, 147456\n"
      "s_add_u32 m0, s78, 6144\n"
      "s_nop 0\n"
      "buffer_load_dwordx4 v99, s[28:31], s68 offen  lds\n"
      "s_add_u32 m0, s78, 8128\n"
      "s_nop 0\n"
      "buffer_load_dwordx4 v98, s[28:31], s68 offen offset:64  lds\n"
      "s_add_u32 m0, s78, 9856\n"
      "s_nop 0\n"
      "buffer_load_dwordx4 v99, s[28:31], s68 offen offset:384  lds\n"
      "s_mov_b32 s68, 165888\n"
      "s_add_u32 m0, s78, 7168\n"
      "s_nop 0\n"
      "buffer_load_dwordx4 v99, s[28:31], s68 offen  lds\n"
      "s_add_u32 m0, s78, 9152\n"
      "s_nop 0\n"
      "buffer_load_dwordx4 v98, s[28:31], s68 offen offset:64  lds\n"
      "s_add_u32 m0, s78, 10880\n"
      "s_nop 0\n"
      "buffer_load_dwordx4 v99, s[28:31], s68 offen offset:384  lds\n"
      "s_waitcnt vmcnt(19)\n"
      "ds_read_b64_tr_b16 a[8:9], v84 offset:12288\n"
      "ds_read_b64_tr_b16 a[10:11], v84 offset:12544\n"
      "ds_read_b64_tr_b16 a[32:33], v84 offset:12320\n"
      "ds_read_b64_tr_b16 a[34:35], v84 offset:12576\n"
      "ds_read_b64_tr_b16 a[56:57], v84 offset:14336\n"
      "ds_read_b64_tr_b16 a[58:59], v84 offset:14592\n"
      "ds_read_b64_tr_b16 a[80:81], v84 offset:16384\n"
      "ds_read_b64_tr_b16 a[82:83], v84 offset:16640\n"
      "ds_read_b64_tr_b16 a[104:105], v84 offset:16416\n"
      "ds_read_b64_tr_b16 a[106:107], v84 offset:16672\n"
      "ds_read_b64_tr_b16 a[128:129], v84 offset:14368\n"
      "ds_read_b64_tr_b16 a[130:131], v84 offset:14624\n"
      "ds_read_b128 v[126:129], v80 offset:4096\n"
      "ds_read_b128 v[122:125], v80 offset:6144\n"
      "s_waitcnt lgkmcnt(12)\n"
      "v_mfma_f32_16x16x32_bf16 v[130:133], a[8:11], v[118:121], v[130:133]\n"
      "s_waitcnt lgkmcnt(10)\n"
      "v_mfma_f32_16x16x32_bf16 v[134:137], a[32:35], v[118:121], v[134:137]\n"
      "s_waitcnt lgkmcnt(7)\n"
      "v_mfma_f32_16x16x32_bf16 v[138:141], a[56:59], v[118:121], v[138:141]\n"
      "s_waitcnt lgkmcnt(3)\n"
      "v_mfma_f32_16x16x32_bf16 v[142:145], a[80:83], v[118:121], v[142:145]\n"
      "v_mfma_f32_16x16x32_bf16 v[146:149], a[104:107], v[118:121], v[146:149]\n"
      "s_waitcnt lgkmcnt(0)\n"
      "v_mfma_f32_16x16x32_bf16 v[150:153], a[128:131], v[118:121], v[150:153]\n"
      "v_mfma_f32_16x16x32_bf16 v[114:117], a[8:11], v[174:177], v[114:117]\n"
      "v_mfma_f32_16x16x32_bf16 v[154:157], a[32:35], v[174:177], v[154:157]\n"
      "v_mfma_f32_16x16x32_bf16 v[158:161], a[56:59], v[174:177], v[158:161]\n"
      "v_mfma_f32_16x16x32_bf16 v[162:165], a[80:83], v[174:177], v[162:165]\n"
      "v_mfma_f32_16x16x32_bf16 v[166:169], a[104:107], v[174:177], v[166:169]\n"
      "v_mfma_f32_16x16x32_bf16 v[170:173], a[128:131], v[174:177], v[170:173]\n"
      "v_lshl_add_u32 v60, v60, 2, s70\n"
      "ds_read_b32 v60, v60\n"
      "v_lshl_add_u32 v61, v61, 2, s70\n"
      "ds_read_b32 v61, v61\n"
      "v_lshl_add_u32 v62, v62, 2, s70\n"
      "ds_read_b32 v62, v62\n"
      "v_lshl_add_u32 v63, v63, 2, s70\n"
      "ds_read_b32 v63, v63\n"
      "v_lshl_add_u32 v64, v64, 2, s70\n"
      "ds_read_b32 v64, v64\n"
      "v_lshl_add_u32 v65, v65, 2, s70\n"
      "ds_read_b32 v65, v65\n"
      "v_lshl_add_u32 v66, v66, 2, s70\n"
      "ds_read_b32 v66, v66\n"
      "v_lshl_add_u32 v67, v67, 2, s70\n"
      "ds_read_b32 v67, v67\n"
      "v_lshl_add_u32 v68, v68, 2, s70\n"
      "ds_read_b32 v68, v68\n"
      "v_lshl_add_u32 v69, v69, 2, s70\n"
      "ds_read_b32 v69, v69\n"
      "v_lshl_add_u32 v70, v70, 2, s70\n"
      "ds_read_b32 v70, v70\n"
      "v_lshl_add_u32 v71, v71, 2, s70\n"
      "ds_read_b32 v71, v71\n"
      "v_lshl_add_u32 v72, v72, 2, s70\n"
      "ds_read_b32 v72, v72\n"
      "v_lshl_add_u32 v73, v73, 2, s70\n"
      "ds_read_b32 v73, v73\n"
      "v_lshl_add_u32 v74, v74, 2, s70\n"
      "ds_read_b32 v74, v74\n"
      "v_lshl_add_u32 v75, v75, 2, s70\n"
      "ds_read_b32 v75, v75\n"
      "v_lshl_add_u32 v102, v102, 2, s70\n"
      "ds_read_b32 v102, v102\n"
      "v_lshl_add_u32 v106, v106, 2, s70\n"
      "ds_read_b32 v106, v106\n"
      "v_lshl_add_u32 v101, v101, 2, s70\n"
      "ds_read_b32 v101, v101\n"
      "v_lshl_add_u32 v100, v100, 2, s70\n"
      "ds_read_b32 v100, v100\n"
      "s_waitcnt lgkmcnt(0)\n"
      "s_mov_b32 s68, 184320\n"
      "s_add_u32 m0, s78, 12288\n"
      "s_nop 0\n"
      "buffer_load_dwordx4 v99, s[28:31], s68 offen  lds\n"
      "s_add_u32 m0, s78, 14272\n"
      "s_nop 0\n"
      "buffer_load_dwordx4 v98, s[28:31], s68 offen offset:64  lds\n"
      "s_add_u32 m0, s78, 16000\n"
      "s_nop 0\n"
      "buffer_load_dwordx4 v99, s[28:31], s68 offen offset:384  lds\n"
      "s_mov_b32 s68, 202752\n"
      "s_add_u32 m0, s78, 13312\n"
      "s_nop 0\n"
      "buffer_load_dwordx4 v99, s[28:31], s68 offen  lds\n"
      "s_add_u32 m0, s78, 15296\n"
      "s_nop 0\n"
      "buffer_load_dwordx4 v98, s[28:31], s68 offen offset:64  lds\n"
      "s_add_u32 m0, s78, 17024\n"
      "s_nop 0\n"
      "buffer_load_dwordx4 v99, s[28:31], s68 offen offset:384  lds\n"
      "s_waitcnt vmcnt(12)\n"
      "ds_read_b64_tr_b16 a[12:13], v84\n"
      "ds_read_b64_tr_b16 a[14:15], v84 offset:256\n"
      "ds_read_b64_tr_b16 a[36:37], v84 offset:32\n"
      "ds_read_b64_tr_b16 a[38:39], v84 offset:288\n"
      "ds_read_b64_tr_b16 a[60:61], v84 offset:2048\n"
      "ds_read_b64_tr_b16 a[62:63], v84 offset:2304\n"
      "ds_read_b64_tr_b16 a[84:85], v84 offset:4096\n"
      "ds_read_b64_tr_b16 a[86:87], v84 offset:4352\n"
      "ds_read_b64_tr_b16 a[108:109], v84 offset:4128\n"
      "ds_read_b64_tr_b16 a[110:111], v84 offset:4384\n"
      "ds_read_b64_tr_b16 a[132:133], v84 offset:2080\n"
      "ds_read_b64_tr_b16 a[134:135], v84 offset:2336\n"
      "ds_read_b128 v[118:121], v105 offset:8192\n"
      "ds_read_b128 v[174:177], v105 offset:10240\n"
      "s_waitcnt lgkmcnt(12)\n"
      "v_mfma_f32_16x16x32_bf16 v[130:133], a[12:15], v[126:129], v[130:133]\n"
      "s_waitcnt lgkmcnt(10)\n"
      "v_mfma_f32_16x16x32_bf16 v[134:137], a[36:39], v[126:129], v[134:137]\n"
      "s_waitcnt lgkmcnt(7)\n"
      "v_mfma_f32_16x16x32_bf16 v[138:141], a[60:63], v[126:129], v[138:141]\n"
      "s_waitcnt lgkmcnt(3)\n"
      "v_mfma_f32_16x16x32_bf16 v[142:145], a[84:87], v[126:129], v[142:145]\n"
      "v_mfma_f32_16x16x32_bf16 v[146:149], a[108:111], v[126:129], v[146:149]\n"
      "s_waitcnt lgkmcnt(0)\n"
      "v_mfma_f32_16x16x32_bf16 v[150:153], a[132:135], v[126:129], v[150:153]\n"
      "v_mfma_f32_16x16x32_bf16 v[114:117], a[12:15], v[122:125], v[114:117]\n"
      "v_mfma_f32_16x16x32_bf16 v[154:157], a[36:39], v[122:125], v[154:157]\n"
      "v_mfma_f32_16x16x32_bf16 v[158:161], a[60:63], v[122:125], v[158:161]\n"
      "v_mfma_f32_16x16x32_bf16 v[162:165], a[84:87], v[122:125], v[162:165]\n"
      "v_mfma_f32_16x16x32_bf16 v[166:169], a[108:111], v[122:125], v[166:169]\n"
      "v_mfma_f32_16x16x32_bf16 v[170:173], a[132:135], v[122:125], v[170:173]\n"
      "s_waitcnt lgkmcnt(0)\n"
      "s_mov_b32 s68, 0\n"
      "s_add_u32 m0, s78, 0\n"
      "s_nop 0\n"
      "buffer_load_dwordx4 v104, s[28:31], s68 offen  lds\n"
      "s_mov_b32 s68, 18432\n"
      "s_add_u32 m0, s78, 1024\n"
      "s_nop 0\n"
      "buffer_load_dwordx4 v104, s[28:31], s68 offen  lds\n"
      "s_mov_b32 s68, 0\n"
      "s_add_u32 m0, s78, 2048\n"
      "s_nop 0\n"
      "buffer_load_dwordx4 v107, s[28:31], s68 offen  lds\n"
      "s_mov_b32 s68, 36864\n"
      "s_add_u32 m0, s78, 3072\n"
      "s_nop 0\n"
      "buffer_load_dwordx4 v104, s[28:31], s68 offen  lds\n"
      "s_mov_b32 s68, 55296\n"
      "s_add_u32 m0, s78, 4096\n"
      "s_nop 0\n"
      "buffer_load_dwordx4 v104, s[28:31], s68 offen  lds\n"
      "s_mov_b32 s68, 36864\n"
      "s_add_u32 m0, s78, 5120\n"
      "s_nop 0\n"
      "buffer_load_dwordx4 v107, s[28:31], s68 offen  lds\n"
      "s_waitcnt vmcnt(12)\n"
      "ds_read_b64_tr_b16 a[16:17], v84 offset:6144\n"
      "ds_read_b64_tr_b16 a[18:19], v84 offset:6400\n"
      "ds_read_b64_tr_b16 a[40:41], v84 offset:6176\n"
      "ds_read_b64_tr_b16 a[42:43], v84 offset:6432\n"
      "ds_read_b64_tr_b16 a[64:65], v84 offset:8192\n"
      "ds_read_b64_tr_b16 a[66:67], v84 offset:8448\n"
      "ds_read_b64_tr_b16 a[88:89], v84 offset:10240\n"
      "ds_read_b64_tr_b16 a[90:91], v84 offset:10496\n"
      "ds_read_b64_tr_b16 a[112:113], v84 offset:10272\n"
      "ds_read_b64_tr_b16 a[114:115], v84 offset:10528\n"
      "ds_read_b64_tr_b16 a[136:137], v84 offset:8224\n"
      "ds_read_b64_tr_b16 a[138:139], v84 offset:8480\n"
      "ds_read_b128 v[122:125], v80 offset:8192\n"
      "ds_read_b128 v[126:129], v80 offset:10240\n"
      "s_waitcnt lgkmcnt(12)\n"
      "v_mfma_f32_16x16x32_bf16 v[130:133], a[16:19], v[118:121], v[130:133]\n"
      "s_waitcnt lgkmcnt(10)\n"
      "v_mfma_f32_16x16x32_bf16 v[134:137], a[40:43], v[118:121], v[134:137]\n"
      "s_waitcnt lgkmcnt(7)\n"
      "v_mfma_f32_16x16x32_bf16 v[138:141], a[64:67], v[118:121], v[138:141]\n"
      "s_waitcnt lgkmcnt(3)\n"
      "v_mfma_f32_16x16x32_bf16 v[142:145], a[88:91], v[118:121], v[142:145]\n"
      "v_mfma_f32_16x16x32_bf16 v[146:149], a[112:115], v[118:121], v[146:149]\n"
      "s_waitcnt lgkmcnt(0)\n"
      "v_mfma_f32_16x16x32_bf16 v[150:153], a[136:139], v[118:121], v[150:153]\n"
      "v_mfma_f32_16x16x32_bf16 v[114:117], a[16:19], v[174:177], v[114:117]\n"
      "v_mfma_f32_16x16x32_bf16 v[154:157], a[40:43], v[174:177], v[154:157]\n"
      "v_mfma_f32_16x16x32_bf16 v[158:161], a[64:67], v[174:177], v[158:161]\n"
      "v_mfma_f32_16x16x32_bf16 v[162:165], a[88:91], v[174:177], v[162:165]\n"
      "v_mfma_f32_16x16x32_bf16 v[166:169], a[112:115], v[174:177], v[166:169]\n"
      "v_mfma_f32_16x16x32_bf16 v[170:173], a[136:139], v[174:177], v[170:173]\n"
      "s_waitcnt lgkmcnt(0)\n"
      "s_mov_b32 s68, 73728\n"
      "s_add_u32 m0, s78, 6144\n"
      "s_nop 0\n"
      "buffer_load_dwordx4 v104, s[28:31], s68 offen  lds\n"
      "s_mov_b32 s68, 92160\n"
      "s_add_u32 m0, s78, 7168\n"
      "s_nop 0\n"
      "buffer_load_dwordx4 v104, s[28:31], s68 offen  lds\n"
      "s_mov_b32 s68, 73728\n"
      "s_add_u32 m0, s78, 8192\n"
      "s_nop 0\n"
      "buffer_load_dwordx4 v107, s[28:31], s68 offen  lds\n"
      "s_mov_b32 s68, 110592\n"
      "s_add_u32 m0, s78, 9216\n"
      "s_nop 0\n"
      "buffer_load_dwordx4 v104, s[28:31], s68 offen  lds\n"
      "s_mov_b32 s68, 129024\n"
      "s_add_u32 m0, s78, 10240\n"
      "s_nop 0\n"
      "buffer_load_dwordx4 v104, s[28:31], s68 offen  lds\n"
      "s_mov_b32 s68, 110592\n"
      "s_add_u32 m0, s78, 11264\n"
      "s_nop 0\n"
      "buffer_load_dwordx4 v107, s[28:31], s68 offen  lds\n"
      "s_waitcnt vmcnt(12)\n"
      "ds_read_b64_tr_b16 a[20:21], v84 offset:12288\n"
      "ds_read_b64_tr_b16 a[22:23], v84 offset:12544\n"
      "ds_read_b64_tr_b16 a[44:45], v84 offset:12320\n"
      "ds_read_b64_tr_b16 a[46:47], v84 offset:12576\n"
      "ds_read_b64_tr_b16 a[68:69], v84 offset:14336\n"
      "ds_read_b64_tr_b16 a[70:71], v84 offset:14592\n"
      "ds_read_b64_tr_b16 a[92:93], v84 offset:16384\n"
      "ds_read_b64_tr_b16 a[94:95], v84 offset:16640\n"
      "ds_read_b64_tr_b16 a[116:117], v84 offset:16416\n"
      "ds_read_b64_tr_b16 a[118:119], v84 offset:16672\n"
      "ds_read_b64_tr_b16 a[140:141], v84 offset:14368\n"
      "ds_read_b64_tr_b16 a[142:143], v84 offset:14624\n"
      "s_waitcnt lgkmcnt(10)\n"
      "v_mfma_f32_16x16x32_bf16 v[130:133], a[20:23], v[122:125], v[130:133]\n"
      "s_waitcnt lgkmcnt(8)\n"
      "v_mfma_f32_16x16x32_bf16 v[134:137], a[44:47], v[122:125], v[134:137]\n"
      "s_waitcnt lgkmcnt(6)\n"
      "v_mfma_f32_16x16x32_bf16 v[138:141], a[68:71], v[122:125], v[138:141]\n"
      "s_waitcnt lgkmcnt(3)\n"
      "v_mfma_f32_16x16x32_bf16 v[142:145], a[92:95], v[122:125], v[142:145]\n"
      "s_waitcnt lgkmcnt(0)\n"
      "v_mfma_f32_16x16x32_bf16 v[146:149], a[116:119], v[122:125], v[146:149]\n"
      "v_mfma_f32_16x16x32_bf16 v[150:153], a[140:143], v[122:125], v[150:153]\n"
      "v_mfma_f32_16x16x32_bf16 v[114:117], a[20:23], v[126:129], v[114:117]\n"
      "v_mfma_f32_16x16x32_bf16 v[154:157], a[44:47], v[126:129], v[154:157]\n"
      "v_mfma_f32_16x16x32_bf16 v[158:161], a[68:71], v[126:129], v[158:161]\n"
      "v_mfma_f32_16x16x32_bf16 v[162:165], a[92:95], v[126:129], v[162:165]\n"
      "v_mfma_f32_16x16x32_bf16 v[166:169], a[116:119], v[126:129], v[166:169]\n"
      "v_mfma_f32_16x16x32_bf16 v[170:173], a[140:143], v[126:129], v[170:173]\n"
      "s_waitcnt lgkmcnt(0)\n"
      "s_mov_b32 s68, 147456\n"
      "s_add_u32 m0, s78, 12288\n"
      "s_nop 0\n"
      "buffer_load_dwordx4 v104, s[28:31], s68 offen  lds\n"
      "s_mov_b32 s68, 165888\n"
      "s_add_u32 m0, s78, 13312\n"
      "s_nop 0\n"
      "buffer_load_dwordx4 v104, s[28:31], s68 offen  lds\n"
      "s_mov_b32 s68, 147456\n"
      "s_add_u32 m0, s78, 14336\n"
      "s_nop 0\n"
      "buffer_load_dwordx4 v107, s[28:31], s68 offen  lds\n"
      "s_mov_b32 s68, 184320\n"
      "s_add_u32 m0, s78, 15360\n"
      "s_nop 0\n"
      "buffer_load_dwordx4 v104, s[28:31], s68 offen  lds\n"
      "s_mov_b32 s68, 202752\n"
      "s_add_u32 m0, s78, 16384\n"
      "s_nop 0\n"
      "buffer_load_dwordx4 v104, s[28:31], s68 offen  lds\n"
      "s_mov_b32 s68, 184320\n"
      "s_add_u32 m0, s78, 17408\n"
      "s_nop 0\n"
      "buffer_load_dwordx4 v107, s[28:31], s68 offen  lds\n"
      "v_mov_b32 v107, 0xf149f2ca\n"
      "s_mov_b32 s72, 0xffff\n"
      "s_mov_b32 s73, 0x0\n"
      "v_cndmask_b32_e64 v24, v107, v102, s[72:73]\n"
      "v_cndmask_b32_e64 v26, v107, v106, s[72:73]\n"
      "v_cndmask_b32_e64 v28, v107, v101, s[72:73]\n"
      "s_mov_b32 s72, 0xffffffff\n"
      "s_mov_b32 s73, 0x1ffff\n"
      "v_cndmask_b32_e64 v30, v107, v100, s[72:73]\n"
      "v_bfe_u32 v100, v96, 4, 4\n"
      "v_mul_u32_u24 v100, 1152, v100\n"
      "v_lshrrev_b32 v101, 8, v96\n"
      "s_mul_i32 s67, s40, 96\n"
      "s_add_u32 s68, s67, 111360\n"
      "v_lshl_add_u32 v96, v101, 3, v100\n"
      "v_add_u32 v96, s68, v96\n"
      "v_mul_u32_u24 v101, 48, v101\n"
      "s_lshl_b32 s67, s67, 1\n"
      "s_add_u32 s67, s67, 110592\n"
      "v_add3_u32 v100, v100, v101, s67\n"
      "v_fma_f32 v101, v130, v130, s51\n"
      "v_fmac_f32 v101, v131, v131\n"
      "v_fmac_f32 v101, v132, v132\n"
      "v_fmac_f32 v101, v133, v133\n"
      "v_fmac_f32 v101, v134, v134\n"
      "v_fmac_f32 v101, v135, v135\n"
      "v_fmac_f32 v101, v136, v136\n"
      "v_fmac_f32 v101, v137, v137\n"
      "v_fmac_f32 v101, v138, v138\n"
      "v_fmac_f32 v101, v139, v139\n"
      "v_fmac_f32 v101, v140, v140\n"
      "v_fmac_f32 v101, v141, v141\n"
      "v_fma_f32 v102, v142, v142, s51\n"
      "v_fmac_f32 v102, v143, v143\n"
      "v_fmac_f32 v102, v144, v144\n"
      "v_fmac_f32 v102, v145, v145\n"
      "v_fmac_f32 v102, v146, v146\n"
      "v_fmac_f32 v102, v147, v147\n"
      "v_fmac_f32 v102, v148, v148\n"
      "v_fmac_f32 v102, v149, v149\n"
      "v_fmac_f32 v102, v150, v150\n"
      "v_fmac_f32 v102, v151, v151\n"
      "v_fmac_f32 v102, v152, v152\n"
      "v_fmac_f32 v102, v153, v153\n"
      "s_nop 1\n"
      "v_permlane16_swap_b32 v101, v102\n"
      "v_add_f32 v101, v101, v102\n"
      "v_mov_b32 v102, v101\n"
      "s_nop 1\n"
      "v_permlane32_swap_b32 v101, v102\n"
      "v_add_f32 v101, v101, v102\n"
      "v_rsq_f32 v101, v101\n"
      "s_nop 0\n"
      "v_mov_b32 v102, v101\n"
      "s_nop 1\n"
      "v_permlane16_swap_b32_e64 v101, v102\n"
      "v_mul_f32_e64 v142, v102, v142\n"
      "v_mul_f32_e64 v143, v102, v143\n"
      "v_cvt_pk_bf16_f32 v142, v142, v143\n"
      "v_mul_f32_e64 v144, v102, v144\n"
      "v_mul_f32_e64 v145, v102, v145\n"
      "v_cvt_pk_bf16_f32 v143, v144, v145\n"
      "v_mul_f32 v146, v102, v146\n"
      "v_mul_f32_e64 v147, v102, v147\n"
      "ds_read_b128 v[126:129], v105\n"
      "ds_read_b128 v[122:125], v105 offset:2048\n"
      "ds_read_b128 v[118:121], v80\n"
      "ds_read_b128 v[174:177], v80 offset:2048\n"
      "s_waitcnt vmcnt(12)\n"
      "ds_read_b64_tr_b16 a[144:145], v84\n"
      "ds_read_b64_tr_b16 a[146:147], v84 offset:256\n"
      "ds_read_b64_tr_b16 a[168:169], v84 offset:32\n"
      "ds_read_b64_tr_b16 a[170:171], v84 offset:288\n"
      "ds_read_b64_tr_b16 a[192:193], v81 offset:2048\n"
      "ds_read_b64_tr_b16 a[194:195], v81 offset:2176\n"
      "ds_read_b64_tr_b16 a[148:149], v84 offset:3072\n"
      "ds_read_b64_tr_b16 a[150:151], v84 offset:3328\n"
      "ds_read_b64_tr_b16 a[172:173], v84 offset:3104\n"
      "ds_read_b64_tr_b16 a[174:175], v84 offset:3360\n"
      "ds_read_b64_tr_b16 a[196:197], v81 offset:5120\n"
      "ds_read_b64_tr_b16 a[198:199], v81 offset:5248\n"
      "s_waitcnt lgkmcnt(10)\n"
      "v_mfma_f32_16x16x32_bf16 v[178:181], a[144:147], v[126:129], 0\n"
      "s_waitcnt lgkmcnt(8)\n"
      "v_mfma_f32_16x16x32_bf16 v[182:185], a[168:171], v[126:129], 0\n"
      "s_waitcnt lgkmcnt(6)\n"
      "v_mfma_f32_16x16x32_bf16 v[186:189], a[192:195], v[126:129], 0\n"
      "v_mfma_f32_16x16x32_bf16 v[126:129], a[144:147], v[122:125], 0\n"
      "v_mfma_f32_16x16x32_bf16 v[190:193], a[168:171], v[122:125], 0\n"
      "v_mfma_f32_16x16x32_bf16 v[194:197], a[192:195], v[122:125], 0\n"
      "s_waitcnt lgkmcnt(0)\n"
      "v_mfma_f32_16x16x32_bf16 v[178:181], a[148:151], v[118:121], v[178:181]\n"
      "v_mfma_f32_16x16x32_bf16 v[182:185], a[172:175], v[118:121], v[182:185]\n"
      "v_mfma_f32_16x16x32_bf16 v[186:189], a[196:199], v[118:121], v[186:189]\n"
      "v_mfma_f32_16x16x32_bf16 v[126:129], a[148:151], v[174:177], v[126:129]\n"
      "v_mfma_f32_16x16x32_bf16 v[190:193], a[172:175], v[174:177], v[190:193]\n"
      "v_mfma_f32_16x16x32_bf16 v[194:197], a[196:199], v[174:177], v[194:197]\n"
      "s_waitcnt lgkmcnt(0)\n"
      "s_mov_b32 s68, 0\n"
      "s_add_u32 m0, s78, 0\n"
      "s_nop 0\n"
      "buffer_load_dwordx4 v103, s[36:39], s68 offen  lds\n"
      "s_mov_b32 s68, 6144\n"
      "s_add_u32 m0, s78, 1024\n"
      "s_nop 0\n"
      "buffer_load_dwordx4 v103, s[36:39], s68 offen  lds\n"
      "s_mov_b32 s68, 0\n"
      "s_add_u32 m0, s78, 2048\n"
      "s_nop 0\n"
      "buffer_load_dwordx4 v77, s[36:39], s68 offen  lds\n"
      "s_mov_b32 s68, 12288\n"
      "s_add_u32 m0, s78, 3072\n"
      "s_nop 0\n"
      "buffer_load_dwordx4 v103, s[36:39], s68 offen  lds\n"
      "s_mov_b32 s68, 18432\n"
      "s_add_u32 m0, s78, 4096\n"
      "s_nop 0\n"
      "buffer_load_dwordx4 v103, s[36:39], s68 offen  lds\n"
      "s_mov_b32 s68, 12288\n"
      "s_add_u32 m0, s78, 5120\n"
      "s_nop 0\n"
      "buffer_load_dwordx4 v77, s[36:39], s68 offen  lds\n"
      "v_cvt_pk_bf16_f32 v144, v146, v147\n"
      "v_mul_f32 v148, v102, v148\n"
      "v_mul_f32 v149, v102, v149\n"
      "v_cvt_pk_bf16_f32 v145, v148, v149\n"
      "v_mul_f32 v150, v102, v150\n"
      "v_mul_f32 v151, v102, v151\n"
      "v_cvt_pk_bf16_f32 v150, v150, v151\n"
      "v_mul_f32 v152, v102, v152\n"
      "v_mul_f32 v153, v102, v153\n"
      "v_cvt_pk_bf16_f32 v151, v152, v153\n"
      "v_mul_f32 v101, s50, v101\n"
      "v_mul_f32 v130, v101, v130\n"
      "v_mul_f32 v131, v101, v131\n"
      "v_cvt_pk_bf16_f32 v130, v130, v131\n"
      "v_mul_f32 v132, v101, v132\n"
      "v_mul_f32 v133, v101, v133\n"
      "v_cvt_pk_bf16_f32 v131, v132, v133\n"
      "v_mul_f32 v134, v101, v134\n"
      "v_mul_f32 v135, v101, v135\n"
      "v_cvt_pk_bf16_f32 v132, v134, v135\n"
      "v_mul_f32 v136, v101, v136\n"
      "v_mul_f32 v137, v101, v137\n"
      "v_cvt_pk_bf16_f32 v133, v136, v137\n"
      "v_mul_f32 v138, v101, v138\n"
      "v_mul_f32 v139, v101, v139\n"
      "v_cvt_pk_bf16_f32 v138, v138, v139\n"
      "v_mul_f32 v140, v101, v140\n"
      "v_mul_f32_e64 v141, v101, v141\n"
      "v_cvt_pk_bf16_f32 v139, v140, v141\n"
      "v_fma_f32 v101, v114, v114, s51\n"
      "v_fmac_f32 v101, v115, v115\n"
      "v_fmac_f32 v101, v116, v116\n"
      "v_fmac_f32 v101, v117, v117\n"
      "v_fmac_f32 v101, v154, v154\n"
      "v_fmac_f32 v101, v155, v155\n"
      "v_fmac_f32 v101, v156, v156\n"
      "v_fmac_f32 v101, v157, v157\n"
      "v_fmac_f32 v101, v158, v158\n"
      "v_fmac_f32 v101, v159, v159\n"
      "v_fmac_f32 v101, v160, v160\n"
      "v_fmac_f32 v101, v161, v161\n"
      "v_fma_f32 v102, v162, v162, s51\n"
      "v_fmac_f32 v102, v163, v163\n"
      "v_fmac_f32 v102, v164, v164\n"
      "v_fmac_f32 v102, v165, v165\n"
      "v_fmac_f32 v102, v166, v166\n"
      "v_fmac_f32 v102, v167, v167\n"
      "v_fmac_f32 v102, v168, v168\n"
      "v_fmac_f32_e64 v102, v169, v169\n"
      "ds_read_b128 v[118:121], v105 offset:4096\n"
      "ds_read_b128 v[122:125], v105 offset:6144\n"
      "ds_read_b128 v[134:137], v80 offset:4096\n"
      "ds_read_b128 v[146:149], v80 offset:6144\n"
      "s_waitcnt vmcnt(12)\n"
      "ds_read_b64_tr_b16 a[152:153], v84 offset:6144\n"
      "ds_read_b64_tr_b16 a[154:155], v84 offset:6400\n"
      "ds_read_b64_tr_b16 a[176:177], v84 offset:6176\n"
      "ds_read_b64_tr_b16 a[178:179], v84 offset:6432\n"
      "ds_read_b64_tr_b16 a[200:201], v81 offset:8192\n"
      "ds_read_b64_tr_b16 a[202:203], v81 offset:8320\n"
      "ds_read_b64_tr_b16 a[156:157], v84 offset:9216\n"
      "ds_read_b64_tr_b16 a[158:159], v84 offset:9472\n"
      "ds_read_b64_tr_b16 a[180:181], v84 offset:9248\n"
      "ds_read_b64_tr_b16 a[182:183], v84 offset:9504\n"
      "ds_read_b64_tr_b16 a[204:205], v81 offset:11264\n"
      "ds_read_b64_tr_b16 a[206:207], v81 offset:11392\n"
      "s_waitcnt lgkmcnt(10)\n"
      "v_mfma_f32_16x16x32_bf16 v[178:181], a[152:155], v[118:121], v[178:181]\n"
      "s_waitcnt lgkmcnt(8)\n"
      "v_mfma_f32_16x16x32_bf16 v[182:185], a[176:179], v[118:121], v[182:185]\n"
      "s_waitcnt lgkmcnt(6)\n"
      "v_mfma_f32_16x16x32_bf16 v[186:189], a[200:203], v[118:121], v[186:189]\n"
      "v_mfma_f32_16x16x32_bf16 v[126:129], a[152:155], v[122:125], v[126:129]\n"
      "v_mfma_f32_16x16x32_bf16 v[190:193], a[176:179], v[122:125], v[190:193]\n"
      "v_mfma_f32_16x16x32_bf16 v[194:197], a[200:203], v[122:125], v[194:197]\n"
      "s_waitcnt lgkmcnt(0)\n"
      "v_mfma_f32_16x16x32_bf16 v[178:181], a[156:159], v[134:137], v[178:181]\n"
      "v_mfma_f32_16x16x32_bf16 v[182:185], a[180:183], v[134:137], v[182:185]\n"
      "v_mfma_f32_16x16x32_bf16 v[186:189], a[204:207], v[134:137], v[186:189]\n"
      "v_mfma_f32_16x16x32_bf16 v[126:129], a[156:159], v[146:149], v[126:129]\n"
      "v_mfma_f32_16x16x32_bf16 v[190:193], a[180:183], v[146:149], v[190:193]\n"
      "v_mfma_f32_16x16x32_bf16 v[194:197], a[204:207], v[146:149], v[194:197]\n"
      "s_waitcnt lgkmcnt(0)\n"
      "s_mov_b32 s68, 24576\n"
      "s_add_u32 m0, s78, 6144\n"
      "s_nop 0\n"
      "buffer_load_dwordx4 v103, s[36:39], s68 offen  lds\n"
      "s_mov_b32 s68, 30720\n"
      "s_add_u32 m0, s78, 7168\n"
      "s_nop 0\n"
      "buffer_load_dwordx4 v103, s[36:39], s68 offen  lds\n"
      "s_mov_b32 s68, 24576\n"
      "s_add_u32 m0, s78, 8192\n"
      "s_nop 0\n"
      "buffer_load_dwordx4 v77, s[36:39], s68 offen  lds\n"
      "s_mov_b32 s68, 36864\n"
      "s_add_u32 m0, s78, 9216\n"
      "s_nop 0\n"
      "buffer_load_dwordx4 v103, s[36:39], s68 offen  lds\n"
      "s_mov_b32 s68, 43008\n"
      "s_add_u32 m0, s78, 10240\n"
      "s_nop 0\n"
      "buffer_load_dwordx4 v103, s[36:39], s68 offen  lds\n"
      "s_mov_b32 s68, 36864\n"
      "s_add_u32 m0, s78, 11264\n"
      "s_nop 0\n"
      "buffer_load_dwordx4 v77, s[36:39], s68 offen  lds\n"
      "v_fmac_f32 v102, v170, v170\n"
      "v_fmac_f32 v102, v171, v171\n"
      "v_fmac_f32 v102, v172, v172\n"
      "v_fmac_f32 v102, v173, v173\n"
      "s_nop 1\n"
      "v_permlane16_swap_b32 v101, v102\n"
      "v_add_f32 v101, v101, v102\n"
      "v_mov_b32 v102, v101\n"
      "s_nop 1\n"
      "v_permlane32_swap_b32 v101, v102\n"
      "v_add_f32 v101, v101, v102\n"
      "v_rsq_f32 v101, v101\n"
      "s_nop 0\n"
      "v_mov_b32 v102, v101\n"
      "s_nop 1\n"
      "v_permlane16_swap_b32 v101, v102\n"
      "v_mul_f32 v162, v102, v162\n"
      "v_mul_f32 v163, v102, v163\n"
      "v_cvt_pk_bf16_f32 v162, v162, v163\n"
      "v_mul_f32 v164, v102, v164\n"
      "v_mul_f32 v165, v102, v165\n"
      "v_cvt_pk_bf16_f32 v163, v164, v165\n"
      "v_mul_f32 v166, v102, v166\n"
      "v_mul_f32 v167, v102, v167\n"
      "v_cvt_pk_bf16_f32 v164, v166, v167\n"
      "v_mul_f32 v168, v102, v168\n"
      "v_mul_f32 v169, v102, v169\n"
      "v_cvt_pk_bf16_f32 v165, v168, v169\n"
      "v_mul_f32 v170, v102, v170\n"
      "v_mul_f32 v171, v102, v171\n"
      "v_cvt_pk_bf16_f32 v170, v170, v171\n"
      "v_mul_f32 v172, v102, v172\n"
      "v_mul_f32 v173, v102, v173\n"
      "v_cvt_pk_bf16_f32 v171, v172, v173\n"
      "v_mul_f32 v101, s50, v101\n"
      "v_mul_f32 v114, v101, v114\n"
      "v_mul_f32 v115, v101, v115\n"
      "v_cvt_pk_bf16_f32 v114, v114, v115\n"
      "v_mul_f32 v116, v101, v116\n"
      "v_mul_f32 v117, v101, v117\n"
      "v_cvt_pk_bf16_f32 v115, v116, v117\n"
      "v_mul_f32 v154, v101, v154\n"
      "v_mul_f32 v155, v101, v155\n"
      "v_cvt_pk_bf16_f32 v116, v154, v155\n"
      "v_mul_f32 v156, v101, v156\n"
      "v_mul_f32 v157, v101, v157\n"
      "v_cvt_pk_bf16_f32 v117, v156, v157\n"
      "v_mul_f32 v158, v101, v158\n"
      "v_mul_f32 v159, v101, v159\n"
      "v_cvt_pk_bf16_f32 v158, v158, v159\n"
      "v_mul_f32 v160, v101, v160\n"
      "v_mul_f32 v161, v101, v161\n"
      "v_cvt_pk_bf16_f32 v159, v160, v161\n"
      "ds_read_b128 v[122:125], v105 offset:8192\n"
      "ds_read_b128 v[118:121], v105 offset:10240\n"
      "ds_read_b128 v[104:107], v80 offset:8192\n"
      "ds_read_b128 v[134:137], v80 offset:10240\n"
      "s_waitcnt vmcnt(12)\n"
      "ds_read_b64_tr_b16 a[160:161], v84 offset:12288\n"
      "ds_read_b64_tr_b16 a[162:163], v84 offset:12544\n"
      "ds_read_b64_tr_b16 a[184:185], v84 offset:12320\n"
      "ds_read_b64_tr_b16 a[186:187], v84 offset:12576\n"
      "ds_read_b64_tr_b16 a[208:209], v81 offset:14336\n"
      "ds_read_b64_tr_b16 a[210:211], v81 offset:14464\n"
      "ds_read_b64_tr_b16 a[164:165], v84 offset:15360\n"
      "ds_read_b64_tr_b16 a[166:167], v84 offset:15616\n"
      "ds_read_b64_tr_b16 a[188:189], v84 offset:15392\n"
      "ds_read_b64_tr_b16 a[190:191], v84 offset:15648\n"
      "ds_read_b64_tr_b16 a[212:213], v81 offset:17408\n"
      "ds_read_b64_tr_b16 a[214:215], v81 offset:17536\n"
      "s_waitcnt lgkmcnt(10)\n"
      "v_mfma_f32_16x16x32_bf16 v[178:181], a[160:163], v[122:125], v[178:181]\n"
      "s_waitcnt lgkmcnt(8)\n"
      "v_mfma_f32_16x16x32_bf16 v[182:185], a[184:187], v[122:125], v[182:185]\n"
      "s_waitcnt lgkmcnt(6)\n"
      "v_mfma_f32_16x16x32_bf16 v[186:189], a[208:211], v[122:125], v[186:189]\n"
      "v_mfma_f32_16x16x32_bf16 v[126:129], a[160:163], v[118:121], v[126:129]\n"
      "v_mfma_f32_16x16x32_bf16 v[190:193], a[184:187], v[118:121], v[190:193]\n"
      "v_mfma_f32_16x16x32_bf16 v[194:197], a[208:211], v[118:121], v[194:197]\n"
      "s_waitcnt lgkmcnt(0)\n"
      "v_mfma_f32_16x16x32_bf16 v[178:181], a[164:167], v[104:107], v[178:181]\n"
      "v_mfma_f32_16x16x32_bf16 v[182:185], a[188:191], v[104:107], v[182:185]\n"
      "v_mfma_f32_16x16x32_bf16 v[186:189], a[212:215], v[104:107], v[186:189]\n"
      "v_mfma_f32_16x16x32_bf16 v[126:129], a[164:167], v[134:137], v[126:129]\n"
      "v_mfma_f32_16x16x32_bf16 v[190:193], a[188:191], v[134:137], v[190:193]\n"
      "v_mfma_f32_16x16x32_bf16 v[194:197], a[212:215], v[134:137], v[194:197]\n"
      "s_waitcnt lgkmcnt(0)\n"
      "s_mov_b32 s68, 49152\n"
      "s_add_u32 m0, s78, 12288\n"
      "s_nop 0\n"
      "buffer_load_dwordx4 v103, s[36:39], s68 offen  lds\n"
      "s_mov_b32 s68, 55296\n"
      "s_add_u32 m0, s78, 13312\n"
      "s_nop 0\n"
      "buffer_load_dwordx4 v103, s[36:39], s68 offen  lds\n"
      "s_mov_b32 s68, 49152\n"
      "s_add_u32 m0, s78, 14336\n"
      "s_nop 0\n"
      "buffer_load_dwordx4 v77, s[36:39], s68 offen  lds\n"
      "s_mov_b32 s68, 61440\n"
      "s_add_u32 m0, s78, 15360\n"
      "s_nop 0\n"
      "buffer_load_dwordx4 v103, s[36:39], s68 offen  lds\n"
      "s_mov_b32 s68, 67584\n"
      "s_add_u32 m0, s78, 16384\n"
      "s_nop 0\n"
      "buffer_load_dwordx4 v103, s[36:39], s68 offen  lds\n"
      "s_mov_b32 s68, 61440\n"
      "s_add_u32 m0, s78, 17408\n"
      "s_nop 0\n"
      "buffer_load_dwordx4 v77, s[36:39], s68 offen  lds\n"
      "s_mov_b32 s72, 0x1f001f\n"
      "s_mov_b32 s73, 0x1f001f\n"
      "v_cvt_pk_bf16_f32 v178, v178, v179\n"
      "v_cvt_pk_bf16_f32 v179, v180, v181\n"
      "v_cvt_pk_bf16_f32 v182, v182, v183\n"
      "v_cvt_pk_bf16_f32 v183, v184, v185\n"
      "v_cvt_pk_bf16_f32 v186, v186, v187\n"
      "v_cvt_pk_bf16_f32 v187, v188, v189\n"
      "ds_write_b128 v100, v[130:133]\n"
      "ds_write_b64 v100, v[138:139] offset:16\n"
      "ds_write_b64 v100, v[150:151] offset:24\n"
      "ds_write_b128 v100, v[142:145] offset:32\n"
      "ds_write_b64 v96, v[178:179]\n"
      "ds_write_b64 v96, v[182:183] offset:32\n"
      "ds_write_b64 v96, v[186:187] offset:64\n"
      "v_cvt_pk_bf16_f32 v126, v126, v127\n"
      "v_cvt_pk_bf16_f32 v127, v128, v129\n"
      "v_cvt_pk_bf16_f32 v190, v190, v191\n"
      "v_cvt_pk_bf16_f32 v191, v192, v193\n"
      "v_cvt_pk_bf16_f32 v194, v194, v195\n"
      "v_cvt_pk_bf16_f32 v195, v196, v197\n"
      "s_mov_b64 exec, s[72:73]\n"
      "ds_write_b128 v100, v[114:117] offset:18432\n"
      "ds_write_b64 v100, v[158:159] offset:18448\n"
      "ds_write_b64 v100, v[170:171] offset:18456\n"
      "ds_write_b128 v100, v[162:165] offset:18464\n"
      "ds_write_b64 v96, v[126:127] offset:18432\n"
      "ds_write_b64 v96, v[190:191] offset:18464\n"
      "ds_write_b64 v96, v[194:195] offset:18496\n"
      "s_mov_b64 exec, -1\n"
      "v_or3_b32 v110, v110, v111, v112\n"
      "v_or3_b32 v110, v110, v113, v97\n"
      "v_and_b32 v110, 0x7fff7fff, v110\n"
      "ds_write_b32 v31, v110\n"
      "s_mov_b32 s42, 0\n"
      "s_mov_b32 s43, 4\n"
      "s_waitcnt lgkmcnt(0)\n"
      "s_barrier\n"
      "ds_read_b128 v[100:103], v9 offset:4096\n"
      "ds_read_b128 v[104:107], v0\n"
      "v_readlane_b32 s49, v38, s43\n"
      "ds_read_b128 v[96:99], v1\n"
      "v_readlane_b32 s45, v37, s43\n"
      "ds_read_b128 v[110:113], v0 offset:6144\n"
      "ds_read_b128 v[114:117], v1 offset:6144\n"
      "ds_read_b128 v[126:129], v0 offset:12288\n"
      "s_and_b32 s49, s49, -4\n"
      "ds_read_b128 v[118:121], v1 offset:12288\n"
      "s_mov_b32 s46, s45\n"
      "s_cmp_ge_u32 s45, 0x40000000\n"
      "s_cbranch_scc1 L_rare_7_%=\n"
      "L_back_8_%=:\n"
      "s_add_u32 s43, s43, 1\n"
      "s_waitcnt lgkmcnt(6)\n"
      "v_or3_b32 v100, v100, v101, v102\n"
      "v_or_b32 v100, v100, v103\n"
      "v_cmp_ne_u32 s[56:57], 0, v100\n"
      "s_waitcnt lgkmcnt(5)\n"
      "v_mfma_f32_16x16x32_bf16 v[122:125], a[0:3], v[104:107], 0\n"
      "s_cmp_lg_u64 s[56:57], 0\n"
      "s_cbranch_scc1 L_rare_5_%=\n"
      "L_back_6_%=:\n"
      "buffer_load_dwordx4 v[130:133], v27, s[24:27], s49 offen\n"
      "s_waitcnt lgkmcnt(4)\n"
      "v_mfma_f32_16x16x32_bf16 v[122:125], a[4:7], v[96:99], v[122:125]\n"
      "s_waitcnt lgkmcnt(3)\n"
      "buffer_load_dword v78, v29, s[24:27], s49 offen\n"
      "v_mfma_f32_16x16x32_bf16 v[122:125], a[8:11], v[110:113], v[122:125]\n"
      "s_waitcnt lgkmcnt(2)\n"
      "s_add_u32 m0, s64, 18432\n"
      "s_or_b64 vcc, s[56:57], s[54:55]\n"
      "v_mfma_f32_16x16x32_bf16 v[122:125], a[12:15], v[114:117], v[122:125]\n"
      "s_waitcnt lgkmcnt(1)\n"
      "buffer_load_dwordx4 v16, s[16:19], s46 offen  lds\n"
      "v_mfma_f32_16x16x32_bf16 v[122:125], a[16:19], v[126:129], v[122:125]\n"
      "s_waitcnt lgkmcnt(0)\n"
      "s_add_u32 m0, s64, 22528\n"
      "v_mfma_f32_16x16x32_bf16 v[122:125], a[20:23], v[118:121], v[122:125]\n"
      "buffer_load_dwordx4 v17, s[16:19], s46 offen  lds\n"
      "s_add_u32 m0, s64, 26624\n"
      "v_mfma_f32_16x16x32_bf16 v[100:103], a[24:27], v[104:107], 0\n"
      "buffer_load_dwordx4 v18, s[16:19], s46 offen  lds\n"
      "s_add_u32 m0, s64, 30720\n"
      "v_mfma_f32_16x16x32_bf16 v[100:103], a[28:31], v[96:99], v[100:103]\n"
      "buffer_load_dwordx4 v19, s[16:19], s46 offen  lds\n"
      "s_mov_b32 m0, s66\n"
      "v_mfma_f32_16x16x32_bf16 v[100:103], a[32:35], v[110:113], v[100:103]\n"
      "buffer_load_dwordx4 v15, s[16:19], s46 offen  lds\n"
      "v_fma_f32 v77, v122, v122, s51\n"
      "v_mfma_f32_16x16x32_bf16 v[100:103], a[36:39], v[114:117], v[100:103]\n"
      "s_cmp_ge_u32 s45, 0x40000000\n"
      "s_cbranch_scc1 L_rare_9_%=\n"
      "L_back_10_%=:\n"
      "v_fmac_f32 v77, v123, v123\n"
      "v_mfma_f32_16x16x32_bf16 v[100:103], a[40:43], v[126:129], v[100:103]\n"
      "v_fmac_f32 v77, v124, v124\n"
      "v_fmac_f32 v77, v125, v125\n"
      "v_mfma_f32_16x16x32_bf16 v[100:103], a[44:47], v[118:121], v[100:103]\n"
      "v_mfma_f32_16x16x32_bf16 v[134:137], a[48:51], v[104:107], 0\n"
      "v_mfma_f32_16x16x32_bf16 v[134:137], a[52:55], v[96:99], v[134:137]\n"
      "v_mfma_f32_16x16x32_bf16 v[134:137], a[56:59], v[110:113], v[134:137]\n"
      "v_mfma_f32_16x16x32_bf16 v[134:137], a[60:63], v[114:117], v[134:137]\n"
      "v_mfma_f32_16x16x32_bf16 v[134:137], a[64:67], v[126:129], v[134:137]\n"
      "v_mfma_f32_16x16x32_bf16 v[134:137], a[68:71], v[118:121], v[134:137]\n"
      "v_mfma_f32_16x16x32_bf16 v[138:141], a[72:75], v[104:107], 0\n"
      "v_mfma_f32_16x16x32_bf16 v[138:141], a[76:79], v[96:99], v[138:141]\n"
      "v_fmac_f32 v77, v100, v100\n"
      "v_fmac_f32 v77, v101, v101\n"
      "v_mfma_f32_16x16x32_bf16 v[138:141], a[80:83], v[110:113], v[138:141]\n"
      "v_fmac_f32 v77, v102, v102\n"
      "v_fmac_f32 v77, v103, v103\n"
      "v_mfma_f32_16x16x32_bf16 v[138:141], a[84:87], v[114:117], v[138:141]\n"
      "v_fmac_f32 v77, v134, v134\n"
      "v_fmac_f32 v77, v135, v135\n"
      "v_mfma_f32_16x16x32_bf16 v[138:141], a[88:91], v[126:129], v[138:141]\n"
      "v_fmac_f32 v77, v136, v136\n"
      "v_fmac_f32 v77, v137, v137\n"
      "v_mfma_f32_16x16x32_bf16 v[138:141], a[92:95], v[118:121], v[138:141]\n"
      "v_mfma_f32_16x16x32_bf16 v[142:145], a[96:99], v[104:107], 0\n"
      "v_mfma_f32_16x16x32_bf16 v[142:145], a[100:103], v[96:99], v[142:145]\n"
      "v_mfma_f32_16x16x32_bf16 v[142:145], a[104:107], v[110:113], v[142:145]\n"
      "v_mfma_f32_16x16x32_bf16 v[142:145], a[108:111], v[114:117], v[142:145]\n"
      "v_mfma_f32_16x16x32_bf16 v[142:145], a[112:115], v[126:129], v[142:145]\n"
      "v_mfma_f32_16x16x32_bf16 v[142:145], a[116:119], v[118:121], v[142:145]\n"
      "ds_read_b128 v[162:165], v0 offset:2048\n"
      "ds_read_b128 v[194:197], v1 offset:2048\n"
      "v_mfma_f32_16x16x32_bf16 v[190:193], a[120:123], v[104:107], 0\n"
      "ds_read_b128 v[104:107], v0 offset:8192\n"
      "v_fma_f32 v80, v138, v138, s51\n"
      "v_mfma_f32_16x16x32_bf16 v[190:193], a[124:127], v[96:99], v[190:193]\n"
      "s_waitcnt vmcnt(19)\n"
      "v_fmac_f32 v80, v139, v139\n"
      "v_fmac_f32_e64 v80, v140, v140\n"
      "v_mfma_f32_16x16x32_bf16 v[190:193], a[128:131], v[110:113], v[190:193]\n"
      "ds_read_b128 v[96:99], v1 offset:8192\n"
      "v_fmac_f32 v80, v141, v141\n"
      "v_mfma_f32_16x16x32_bf16 v[190:193], a[132:135], v[114:117], v[190:193]\n"
      "ds_read_b128 v[114:117], v0 offset:14336\n"
      "v_fmac_f32 v80, v142, v142\n"
      "v_mfma_f32_16x16x32_bf16 v[190:193], a[136:139], v[126:129], v[190:193]\n"
      "ds_read_b128 v[126:129], v1 offset:14336\n"
      "v_fmac_f32 v80, v143, v143\n"
      "v_mfma_f32_16x16x32_bf16 v[190:193], a[140:143], v[118:121], v[190:193]\n"
      "s_waitcnt lgkmcnt(5)\n"
      "v_fmac_f32 v80, v144, v144\n"
      "v_fmac_f32 v80, v145, v145\n"
      "v_mfma_f32_16x16x32_bf16 v[118:121], a[0:3], v[162:165], 0\n"
      "s_waitcnt lgkmcnt(4)\n"
      "v_mfma_f32_16x16x32_bf16 v[118:121], a[4:7], v[194:197], v[118:121]\n"
      "s_waitcnt lgkmcnt(3)\n"
      "v_mfma_f32_16x16x32_bf16 v[118:121], a[8:11], v[104:107], v[118:121]\n"
      "s_waitcnt lgkmcnt(2)\n"
      "v_fmac_f32 v80, v190, v190\n"
      "v_fmac_f32 v80, v191, v191\n"
      "v_mfma_f32_16x16x32_bf16 v[118:121], a[12:15], v[96:99], v[118:121]\n"
      "s_waitcnt lgkmcnt(1)\n"
      "v_fmac_f32 v80, v192, v192\n"
      "v_fmac_f32 v80, v193, v193\n"
      "v_mfma_f32_16x16x32_bf16 v[118:121], a[16:19], v[114:117], v[118:121]\n"
      "s_waitcnt lgkmcnt(0)\n"
      "v_permlane16_swap_b32 v77, v80\n"
      "v_mfma_f32_16x16x32_bf16 v[118:121], a[20:23], v[126:129], v[118:121]\n"
      "v_add_f32 v77, v77, v80\n"
      "v_mov_b32 v80, v77\n"
      "v_mfma_f32_16x16x32_bf16 v[110:113], a[24:27], v[162:165], 0\n"
      "v_mfma_f32_16x16x32_bf16 v[110:113], a[28:31], v[194:197], v[110:113]\n"
      "v_permlane32_swap_b32_e64 v77, v80\n"
      "v_mfma_f32_16x16x32_bf16 v[110:113], a[32:35], v[104:107], v[110:113]\n"
      "v_add_f32 v77, v77, v80\n"
      "v_rsq_f32 v77, v77\n"
      "v_mfma_f32_16x16x32_bf16 v[110:113], a[36:39], v[96:99], v[110:113]\n"
      "v_mov_b32_e64 v80, v77\n"
      "v_fma_f32 v81, v118, v118, s51\n"
      "v_mfma_f32_16x16x32_bf16 v[110:113], a[40:43], v[114:117], v[110:113]\n"
      "v_permlane16_swap_b32 v77, v80\n"
      "v_mfma_f32_16x16x32_bf16 v[110:113], a[44:47], v[126:129], v[110:113]\n"
      "v_mul_f32 v77, s50, v77\n"
      "v_mul_f32 v138, v80, v138\n"
      "v_mfma_f32_16x16x32_bf16 v[186:189], a[48:51], v[162:165], 0\n"
      "v_mul_f32 v139, v80, v139\n"
      "v_mul_f32 v122, v77, v122\n"
      "v_mfma_f32_16x16x32_bf16 v[186:189], a[52:55], v[194:197], v[186:189]\n"
      "v_mul_f32 v123, v77, v123\n"
      "v_cvt_pk_bf16_f32 v138, v138, v139\n"
      "v_mfma_f32_16x16x32_bf16 v[186:189], a[56:59], v[104:107], v[186:189]\n"
      "v_mul_f32 v140, v80, v140\n"
      "v_mul_f32 v141, v80, v141\n"
      "v_mfma_f32_16x16x32_bf16 v[186:189], a[60:63], v[96:99], v[186:189]\n"
      "v_mul_f32 v190, v80, v190\n"
      "v_mul_f32 v191, v80, v191\n"
      "v_mfma_f32_16x16x32_bf16 v[186:189], a[64:67], v[114:117], v[186:189]\n"
      "v_cvt_pk_bf16_f32 v122, v122, v123\n"
      "v_mul_f32 v124, v77, v124\n"
      "v_mfma_f32_16x16x32_bf16 v[186:189], a[68:71], v[126:129], v[186:189]\n"
      "v_mul_f32 v125, v77, v125\n"
      "v_mul_f32 v134, v77, v134\n"
      "v_mfma_f32_16x16x32_bf16 v[182:185], a[72:75], v[162:165], 0\n"
      "v_mul_f32 v135, v77, v135\n"
      "v_cvt_pk_bf16_f32 v139, v140, v141\n"
      "v_mfma_f32_16x16x32_bf16 v[182:185], a[76:79], v[194:197], v[182:185]\n"
      "v_mul_f32 v142, v80, v142\n"
      "v_mul_f32 v143, v80, v143\n"
      "v_mfma_f32_16x16x32_bf16 v[182:185], a[80:83], v[104:107], v[182:185]\n"
      "v_mul_f32 v144, v80, v144\n"
      "v_mul_f32 v145, v80, v145\n"
      "v_mfma_f32_16x16x32_bf16 v[182:185], a[84:87], v[96:99], v[182:185]\n"
      "v_cvt_pk_bf16_f32 v190, v190, v191\n"
      "v_mul_f32 v192, v80, v192\n"
      "v_mfma_f32_16x16x32_bf16 v[182:185], a[88:91], v[114:117], v[182:185]\n"
      "v_mul_f32 v193, v80, v193\n"
      "v_cvt_pk_bf16_f32 v123, v124, v125\n"
      "v_mfma_f32_16x16x32_bf16 v[182:185], a[92:95], v[126:129], v[182:185]\n"
      "s_waitcnt vmcnt(13)\n"
      "v_mul_f32 v100, v77, v100\n"
      "v_mul_f32_e64 v101, v77, v101\n"
      "v_mfma_f32_16x16x32_bf16 v[178:181], a[96:99], v[162:165], 0\n"
      "v_mul_f32 v102, v77, v102\n"
      "v_mul_f32 v103, v77, v103\n"
      "v_mfma_f32_16x16x32_bf16 v[178:181], a[100:103], v[194:197], v[178:181]\n"
      "v_cvt_pk_bf16_f32 v134, v134, v135\n"
      "v_mul_f32 v136, v77, v136\n"
      "v_mfma_f32_16x16x32_bf16 v[178:181], a[104:107], v[104:107], v[178:181]\n"
      "v_mul_f32 v137, v77, v137\n"
      "v_cvt_pk_bf16_f32 v140, v142, v143\n"
      "v_mfma_f32_16x16x32_bf16 v[178:181], a[108:111], v[96:99], v[178:181]\n"
      "v_cvt_pk_bf16_f32 v141, v144, v145\n"
      "v_cvt_pk_bf16_f32 v191, v192, v193\n"
      "v_mfma_f32_16x16x32_bf16 v[178:181], a[112:115], v[114:117], v[178:181]\n"
      "v_cvt_pk_bf16_f32 v124, v100, v101\n"
      "v_cvt_pk_bf16_f32 v125, v102, v103\n"
      "v_mfma_f32_16x16x32_bf16 v[178:181], a[116:119], v[126:129], v[178:181]\n"
      "v_cvt_pk_bf16_f32 v135, v136, v137\n"
      "v_fma_f32 v102, v182, v182, s51\n"
      "v_mfma_f32_16x16x32_bf16 v[142:145], a[120:123], v[162:165], 0\n"
      "v_fmac_f32 v81, v119, v119\n"
      "v_fmac_f32 v102, v183, v183\n"
      "v_mfma_f32_16x16x32_bf16 v[142:145], a[124:127], v[194:197], v[142:145]\n"
      "ds_read_b128 v[194:197], v0 offset:4096\n"
      "v_fmac_f32 v81, v120, v120\n"
      "v_mfma_f32_16x16x32_bf16 v[142:145], a[128:131], v[104:107], v[142:145]\n"
      "ds_read_b128 v[104:107], v1 offset:4096\n"
      "v_fmac_f32 v102, v184, v184\n"
      "v_mfma_f32_16x16x32_bf16 v[142:145], a[132:135], v[96:99], v[142:145]\n"
      "ds_read_b128 v[96:99], v0 offset:10240\n"
      "v_fmac_f32 v81, v121, v121\n"
      "v_mfma_f32_16x16x32_bf16 v[142:145], a[136:139], v[114:117], v[142:145]\n"
      "ds_read_b128 v[114:117], v1 offset:10240\n"
      "v_fmac_f32 v102, v185, v185\n"
      "v_mfma_f32_16x16x32_bf16 v[142:145], a[140:143], v[126:129], v[142:145]\n"
      "ds_read_b128 v[126:129], v0 offset:16384\n"
      "v_fmac_f32 v81, v110, v110\n"
      "v_fmac_f32 v102, v178, v178\n"
      "v_fmac_f32 v81, v111, v111\n"
      "v_fmac_f32 v102, v179, v179\n"
      "v_fmac_f32 v81, v112, v112\n"
      "v_fmac_f32 v102, v180, v180\n"
      "s_waitcnt lgkmcnt(4)\n"
      "v_mfma_f32_16x16x32_bf16 v[162:165], a[0:3], v[194:197], 0\n"
      "ds_read_b128 v[158:161], v1 offset:16384\n"
      "v_fmac_f32 v81, v113, v113\n"
      "s_waitcnt lgkmcnt(4)\n"
      "v_mfma_f32_16x16x32_bf16 v[162:165], a[4:7], v[104:107], v[162:165]\n"
      "v_fmac_f32 v102, v181, v181\n"
      "v_fmac_f32 v81, v186, v186\n"
      "s_waitcnt lgkmcnt(3)\n"
      "v_mfma_f32_16x16x32_bf16 v[162:165], a[8:11], v[96:99], v[162:165]\n"
      "s_waitcnt lgkmcnt(2)\n"
      "v_fmac_f32 v102, v142, v142\n"
      "v_fmac_f32 v81, v187, v187\n"
      "v_mfma_f32_16x16x32_bf16 v[162:165], a[12:15], v[114:117], v[162:165]\n"
      "s_waitcnt lgkmcnt(1)\n"
      "v_fmac_f32 v102, v143, v143\n"
      "v_fmac_f32 v81, v188, v188\n"
      "v_mfma_f32_16x16x32_bf16 v[162:165], a[16:19], v[126:129], v[162:165]\n"
      "v_fmac_f32 v102, v144, v144\n"
      "v_fmac_f32 v81, v189, v189\n"
      "v_fmac_f32 v102, v145, v145\n"
      "s_nop 0\n"
      "s_nop 0\n"
      "s_waitcnt lgkmcnt(0)\n"
      "v_mfma_f32_16x16x32_bf16 v[162:165], a[20:23], v[158:161], v[162:165]\n"
      "v_permlane16_swap_b32 v81, v102\n"
      "v_mfma_f32_16x16x32_bf16 v[170:173], a[24:27], v[194:197], 0\n"
      "v_add_f32 v81, v81, v102\n"
      "v_mov_b32_e64 v102, v81\n"
      "v_mfma_f32_16x16x32_bf16 v[170:173], a[28:31], v[104:107], v[170:173]\n"
      "v_mfma_f32_16x16x32_bf16 v[170:173], a[32:35], v[96:99], v[170:173]\n"
      "v_permlane32_swap_b32 v81, v102\n"
      "v_mfma_f32_16x16x32_bf16 v[170:173], a[36:39], v[114:117], v[170:173]\n"
      "v_add_f32 v81, v81, v102\n"
      "v_rsq_f32 v81, v81\n"
      "v_mfma_f32_16x16x32_bf16 v[170:173], a[40:43], v[126:129], v[170:173]\n"
      "v_mov_b32 v102, v81\n"
      "v_fma_f32 v77, v162, v162, s51\n"
      "v_mfma_f32_16x16x32_bf16 v[170:173], a[44:47], v[158:161], v[170:173]\n"
      "v_permlane16_swap_b32 v81, v102\n"
      "v_mfma_f32_16x16x32_bf16 v[148:151], a[48:51], v[194:197], 0\n"
      "v_mul_f32 v81, s50, v81\n"
      "v_mul_f32 v182, v102, v182\n"
      "v_mfma_f32_16x16x32_bf16 v[148:151], a[52:55], v[104:107], v[148:151]\n"
      "v_mul_f32 v183, v102, v183\n"
      "v_mul_f32 v118, v81, v118\n"
      "v_mfma_f32_16x16x32_bf16 v[148:151], a[56:59], v[96:99], v[148:151]\n"
      "s_waitcnt vmcnt(7)\n"
      "v_mul_f32_e64 v119, v81, v119\n"
      "v_cvt_pk_bf16_f32 v182, v182, v183\n"
      "v_mfma_f32_16x16x32_bf16 v[148:151], a[60:63], v[114:117], v[148:151]\n"
      "v_mul_f32 v184, v102, v184\n"
      "v_mul_f32 v185, v102, v185\n"
      "v_mfma_f32_16x16x32_bf16 v[148:151], a[64:67], v[126:129], v[148:151]\n"
      "v_mul_f32 v142, v102, v142\n"
      "v_mul_f32 v143, v102, v143\n"
      "v_mfma_f32_16x16x32_bf16 v[148:151], a[68:71], v[158:161], v[148:151]\n"
      "v_cvt_pk_bf16_f32 v118, v118, v119\n"
      "v_mul_f32 v120, v81, v120\n"
      "v_mfma_f32_16x16x32_bf16 v[154:157], a[72:75], v[194:197], 0\n"
      "v_mul_f32 v121, v81, v121\n"
      "v_mul_f32 v186, v81, v186\n"
      "v_mfma_f32_16x16x32_bf16 v[154:157], a[76:79], v[104:107], v[154:157]\n"
      "v_mul_f32 v187, v81, v187\n"
      "v_cvt_pk_bf16_f32 v183, v184, v185\n"
      "v_mfma_f32_16x16x32_bf16 v[154:157], a[80:83], v[96:99], v[154:157]\n"
      "v_mul_f32 v178, v102, v178\n"
      "v_mul_f32 v179, v102, v179\n"
      "v_mfma_f32_16x16x32_bf16 v[154:157], a[84:87], v[114:117], v[154:157]\n"
      "v_mul_f32 v180, v102, v180\n"
      "v_mul_f32 v181, v102, v181\n"
      "v_mfma_f32_16x16x32_bf16 v[154:157], a[88:91], v[126:129], v[154:157]\n"
      "v_cvt_pk_bf16_f32 v142, v142, v143\n"
      "v_mul_f32 v144, v102, v144\n"
      "v_mfma_f32_16x16x32_bf16 v[154:157], a[92:95], v[158:161], v[154:157]\n"
      "v_mul_f32 v145, v102, v145\n"
      "v_cvt_pk_bf16_f32 v119, v120, v121\n"
      "v_mfma_f32_16x16x32_bf16 v[166:169], a[96:99], v[194:197], 0\n"
      "v_mul_f32 v110, v81, v110\n"
      "v_mul_f32 v111, v81, v111\n"
      "v_mfma_f32_16x16x32_bf16 v[166:169], a[100:103], v[104:107], v[166:169]\n"
      "v_mul_f32 v112, v81, v112\n"
      "v_mul_f32 v113, v81, v113\n"
      "v_mfma_f32_16x16x32_bf16 v[166:169], a[104:107], v[96:99], v[166:169]\n"
      "v_cvt_pk_bf16_f32 v186, v186, v187\n"
      "v_mul_f32 v188, v81, v188\n"
      "v_mfma_f32_16x16x32_bf16 v[166:169], a[108:111], v[114:117], v[166:169]\n"
      "v_mul_f32 v189, v81, v189\n"
      "v_cvt_pk_bf16_f32 v184, v178, v179\n"
      "v_mfma_f32_16x16x32_bf16 v[166:169], a[112:115], v[126:129], v[166:169]\n"
      "v_cvt_pk_bf16_f32 v185, v180, v181\n"
      "v_cvt_pk_bf16_f32 v143, v144, v145\n"
      "v_mfma_f32_16x16x32_bf16 v[166:169], a[116:119], v[158:161], v[166:169]\n"
      "v_cvt_pk_bf16_f32 v120, v110, v111\n"
      "v_cvt_pk_bf16_f32 v121, v112, v113\n"
      "v_mfma_f32_16x16x32_bf16 v[110:113], a[120:123], v[194:197], 0\n"
      "v_cvt_pk_bf16_f32 v187, v188, v189\n"
      "v_fma_f32 v81, v154, v154, s51\n"
      "v_mfma_f32_16x16x32_bf16 v[110:113], a[124:127], v[104:107], v[110:113]\n"
      "v_fmac_f32 v77, v163, v163\n"
      "v_fmac_f32 v81, v155, v155\n"
      "v_mfma_f32_16x16x32_bf16 v[110:113], a[128:131], v[96:99], v[110:113]\n"
      "v_fmac_f32 v77, v164, v164\n"
      "v_fmac_f32 v81, v156, v156\n"
      "v_mfma_f32_16x16x32_bf16 v[110:113], a[132:135], v[114:117], v[110:113]\n"
      "v_fmac_f32 v77, v165, v165\n"
      "v_fmac_f32 v81, v157, v157\n"
      "v_mfma_f32_16x16x32_bf16 v[110:113], a[136:139], v[126:129], v[110:113]\n"
      "v_fmac_f32 v77, v170, v170\n"
      "v_fmac_f32 v81, v166, v166\n"
      "v_mfma_f32_16x16x32_bf16 v[110:113], a[140:143], v[158:161], v[110:113]\n"
      "v_fmac_f32 v77, v171, v171\n"
      "v_fmac_f32 v81, v167, v167\n"
      "v_mfma_f32_16x16x32_bf16 v[178:181], v[194:197], a[144:147], 0\n"
      "v_fmac_f32 v77, v172, v172\n"
      "v_fmac_f32 v81, v168, v168\n"
      "v_mfma_f32_16x16x32_bf16 v[178:181], v[104:107], a[148:151], v[178:181]\n"
      "v_fmac_f32 v77, v173, v173\n"
      "v_fmac_f32 v81, v169, v169\n"
      "v_mfma_f32_16x16x32_bf16 v[178:181], v[96:99], a[152:155], v[178:181]\n"
      "v_fmac_f32 v77, v148, v148\n"
      "v_fmac_f32 v81, v110, v110\n"
      "v_mfma_f32_16x16x32_bf16 v[144:147], v[194:197], a[168:171], 0\n"
      "v_fmac_f32 v77, v149, v149\n"
      "v_fmac_f32 v81, v111, v111\n"
      "v_mfma_f32_16x16x32_bf16 v[144:147], v[104:107], a[172:175], v[144:147]\n"
      "v_fmac_f32 v77, v150, v150\n"
      "v_fmac_f32 v81, v112, v112\n"
      "v_mfma_f32_16x16x32_bf16 v[144:147], v[96:99], a[176:179], v[144:147]\n"
      "v_fmac_f32 v77, v151, v151\n"
      "v_fmac_f32 v81, v113, v113\n"
      "v_mfma_f32_16x16x32_bf16 v[174:177], v[194:197], a[192:195], 0\n"
      "v_mfma_f32_16x16x32_bf16 v[174:177], v[104:107], a[196:199], v[174:177]\n"
      "v_permlane16_swap_b32 v77, v81\n"
      "v_mfma_f32_16x16x32_bf16 v[174:177], v[96:99], a[200:203], v[174:177]\n"
      "v_add_f32 v77, v77, v81\n"
      "v_mov_b32 v81, v77\n"
      "v_mfma_f32_16x16x32_bf16 v[178:181], v[114:117], a[156:159], v[178:181]\n"
      "v_mfma_f32_16x16x32_bf16 v[178:181], v[126:129], a[160:163], v[178:181]\n"
      "v_permlane32_swap_b32 v77, v81\n"
      "v_mfma_f32_16x16x32_bf16 v[178:181], v[158:161], a[164:167], v[178:181]\n"
      "v_add_f32 v77, v77, v81\n"
      "v_rsq_f32 v77, v77\n"
      "v_mfma_f32_16x16x32_bf16 v[144:147], v[114:117], a[180:183], v[144:147]\n"
      "v_mov_b32_e64 v81, v77\n"
      "v_mfma_f32_16x16x32_bf16 v[144:147], v[126:129], a[184:187], v[144:147]\n"
      "ds_read_b128 v[194:197], v0 offset:2048\n"
      "v_permlane16_swap_b32 v77, v81\n"
      "v_mfma_f32_16x16x32_bf16 v[144:147], v[158:161], a[188:191], v[144:147]\n"
      "ds_read_b128 v[198:201], v1 offset:2048\n"
      "v_mul_f32 v77, s50, v77\n"
      "v_mfma_f32_16x16x32_bf16 v[174:177], v[114:117], a[204:207], v[174:177]\n"
      "ds_read_b128 v[114:117], v0 offset:8192\n"
      "v_mul_f32 v154, v81, v154\n"
      "v_mfma_f32_16x16x32_bf16 v[174:177], v[126:129], a[208:211], v[174:177]\n"
      "v_mul_f32 v155, v81, v155\n"
      "v_mul_f32 v162, v77, v162\n"
      "v_mfma_f32_16x16x32_bf16 v[174:177], v[158:161], a[212:215], v[174:177]\n"
      "v_mul_f32 v163, v77, v163\n"
      "v_cvt_pk_bf16_f32 v154, v154, v155\n"
      "v_mul_f32 v156, v81, v156\n"
      "v_mul_f32 v157, v81, v157\n"
      "v_mul_f32 v110, v81, v110\n"
      "v_mul_f32 v111, v81, v111\n"
      "s_waitcnt lgkmcnt(2)\n"
      "v_mfma_f32_16x16x32_bf16 v[96:99], v[194:197], a[144:147], 0\n"
      "v_cvt_pk_bf16_f32 v162, v162, v163\n"
      "v_mul_f32 v164, v77, v164\n"
      "s_waitcnt lgkmcnt(1)\n"
      "v_mfma_f32_16x16x32_bf16 v[96:99], v[198:201], a[148:151], v[96:99]\n"
      "v_mul_f32 v165, v77, v165\n"
      "v_mul_f32 v148, v77, v148\n"
      "s_waitcnt lgkmcnt(0)\n"
      "v_mfma_f32_16x16x32_bf16 v[96:99], v[114:117], a[152:155], v[96:99]\n"
      "v_mul_f32 v149, v77, v149\n"
      "v_cvt_pk_bf16_f32 v155, v156, v157\n"
      "v_mfma_f32_16x16x32_bf16 v[100:103], v[194:197], a[168:171], 0\n"
      "ds_read_b128 v[126:129], v1 offset:8192\n"
      "v_mul_f32 v166, v81, v166\n"
      "v_mfma_f32_16x16x32_bf16 v[100:103], v[198:201], a[172:175], v[100:103]\n"
      "ds_read_b128 v[158:161], v0 offset:14336\n"
      "v_mul_f32_e64 v167, v81, v167\n"
      "v_mfma_f32_16x16x32_bf16 v[100:103], v[114:117], a[176:179], v[100:103]\n"
      "ds_read_b128 v[202:205], v1 offset:14336\n"
      "v_mul_f32 v168, v81, v168\n"
      "v_mfma_f32_16x16x32_bf16 v[104:107], v[194:197], a[192:195], 0\n"
      "v_mul_f32 v169, v81, v169\n"
      "v_cvt_pk_bf16_f32 v110, v110, v111\n"
      "v_mfma_f32_16x16x32_bf16 v[104:107], v[198:201], a[196:199], v[104:107]\n"
      "v_mul_f32_e64 v112, v81, v112\n"
      "v_mul_f32_e64 v113, v81, v113\n"
      "v_mfma_f32_16x16x32_bf16 v[104:107], v[114:117], a[200:203], v[104:107]\n"
      "v_cvt_pk_bf16_f32 v163, v164, v165\n"
      "v_mul_f32 v170, v77, v170\n"
      "v_mul_f32_e64 v171, v77, v171\n"
      "s_waitcnt lgkmcnt(2)\n"
      "v_mfma_f32_16x16x32_bf16 v[96:99], v[126:129], a[156:159], v[96:99]\n"
      "v_mul_f32 v172, v77, v172\n"
      "v_mul_f32_e64 v173, v77, v173\n"
      "s_waitcnt lgkmcnt(1)\n"
      "v_mfma_f32_16x16x32_bf16 v[96:99], v[158:161], a[160:163], v[96:99]\n"
      "v_cvt_pk_bf16_f32 v148, v148, v149\n"
      "v_mul_f32 v150, v77, v150\n"
      "s_waitcnt lgkmcnt(0)\n"
      "v_mfma_f32_16x16x32_bf16 v[96:99], v[202:205], a[164:167], v[96:99]\n"
      "v_mul_f32_e64 v151, v77, v151\n"
      "v_cvt_pk_bf16_f32 v156, v166, v167\n"
      "v_mfma_f32_16x16x32_bf16 v[100:103], v[126:129], a[180:183], v[100:103]\n"
      "v_cvt_pk_bf16_f32 v157, v168, v169\n"
      "v_cvt_pk_bf16_f32 v111, v112, v113\n"
      "v_mfma_f32_16x16x32_bf16 v[100:103], v[158:161], a[184:187], v[100:103]\n"
      "v_cvt_pk_bf16_f32 v164, v170, v171\n"
      "v_cvt_pk_bf16_f32 v165, v172, v173\n"
      "v_mfma_f32_16x16x32_bf16 v[100:103], v[202:205], a[188:191], v[100:103]\n"
      "v_cvt_pk_bf16_f32 v149, v150, v151\n"
      "v_cvt_pk_bf16_f32 v99, v98, v99\n"
      "v_mfma_f32_16x16x32_bf16 v[104:107], v[126:129], a[204:207], v[104:107]\n"
      "ds_read_b128 v[126:129], v10 offset:32\n"
      "v_cvt_pk_bf16_f32 v98, v96, v97\n"
      "v_mfma_f32_16x16x32_bf16 v[104:107], v[158:161], a[208:211], v[104:107]\n"
      "v_cvt_pk_bf16_f32 v76, v178, v179\n"
      "v_cvt_pk_bf16_f32 v77, v180, v181\n"
      "v_mfma_f32_16x16x32_bf16 v[104:107], v[202:205], a[212:215], v[104:107]\n"
      "v_cvt_pk_bf16_f32 v103, v102, v103\n"
      "v_cvt_pk_bf16_f32 v102, v100, v101\n"
      "v_mfma_f32_16x16x32_bf16 v[112:115], v[138:141], v[122:125], v[40:43]\n"
      "v_cvt_pk_bf16_f32 v80, v144, v145\n"
      "v_cvt_pk_bf16_f32 v81, v146, v147\n"
      "v_mfma_f32_16x16x32_bf16 v[144:147], v[182:185], v[122:125], v[44:47]\n"
      "ds_read_b64 v[116:117], v10 offset:24\n"
      "v_cvt_pk_bf16_f32 v84, v174, v175\n"
      "v_mfma_f32_16x16x32_bf16 v[202:205], v[154:157], v[122:125], v[48:51]\n"
      "ds_read_b128 v[172:175], v0\n"
      "v_cvt_pk_bf16_f32 v107, v106, v107\n"
      "v_cvt_pk_bf16_f32 v106, v104, v105\n"
      "s_waitcnt lgkmcnt(2)\n"
      "v_mfma_f32_16x16x32_bf16 v[178:181], v[126:129], v[122:125], v[24:27]\n"
      "ds_read_b128 v[122:125], v1\n"
      "v_cvt_pk_bf16_f32 v85, v176, v177\n"
      "v_mfma_f32_16x16x16_bf16 v[112:115], v[190:191], v[134:135], v[112:115]\n"
      "ds_read_b128 v[158:161], v0 offset:6144\n"
      "v_mfma_f32_16x16x16_bf16 v[144:147], v[142:143], v[134:135], v[144:147]\n"
      "v_mfma_f32_16x16x16_bf16 v[202:205], v[110:111], v[134:135], v[202:205]\n"
      "s_waitcnt lgkmcnt(3)\n"
      "v_mfma_f32_16x16x16_bf16 v[178:181], v[116:117], v[134:135], v[178:181]\n"
      "s_nop 0\n"
      "s_waitcnt lgkmcnt(2)\n"
      "v_mfma_f32_16x16x32_bf16 v[134:137], v[172:175], a[144:147], 0\n"
      "s_nop 0\n"
      "s_nop 0\n"
      "s_waitcnt lgkmcnt(1)\n"
      "v_mfma_f32_16x16x32_bf16 v[134:137], v[122:125], a[148:151], v[134:137]\n"
      "s_nop 0\n"
      "s_cbranch_vccnz L_rare_12_%=\n"
      "L_back_13_%=:\n"
      "v_exp_f32_e64 v112, v112\n"
      "s_waitcnt lgkmcnt(0)\n"
      "v_mfma_f32_16x16x32_bf16 v[134:137], v[158:161], a[152:155], v[134:137]\n"
      "ds_read_b128 v[168:171], v1 offset:6144\n"
      "v_exp_f32_e64 v113, v113\n"
      "v_mfma_f32_16x16x32_bf16 v[198:201], v[172:175], a[168:171], 0\n"
      "ds_read_b128 v[194:197], v0 offset:12288\n"
      "v_exp_f32 v114, v114\n"
      "v_mfma_f32_16x16x32_bf16 v[198:201], v[122:125], a[172:175], v[198:201]\n"
      "ds_read_b128 v[150:153], v1 offset:12288\n"
      "v_exp_f32 v115, v115\n"
      "v_mfma_f32_16x16x32_bf16 v[198:201], v[158:161], a[176:179], v[198:201]\n"
      "v_exp_f32 v144, v144\n"
      "v_mfma_f32_16x16x32_bf16 v[206:209], v[172:175], a[192:195], 0\n"
      "v_exp_f32 v145, v145\n"
      "v_mfma_f32_16x16x32_bf16 v[206:209], v[122:125], a[196:199], v[206:209]\n"
      "v_exp_f32 v146, v146\n"
      "v_mfma_f32_16x16x32_bf16 v[206:209], v[158:161], a[200:203], v[206:209]\n"
      "s_waitcnt lgkmcnt(2)\n"
      "v_exp_f32_e64 v147, v147\n"
      "v_mfma_f32_16x16x32_bf16 v[134:137], v[168:171], a[156:159], v[134:137]\n"
      "s_waitcnt lgkmcnt(1)\n"
      "v_cvt_pk_bf16_f32 v112, v112, v113\n"
      "v_cvt_pk_bf16_f32 v113, v114, v115\n"
      "v_mfma_f32_16x16x32_bf16 v[134:137], v[194:197], a[160:163], v[134:137]\n"
      "s_waitcnt lgkmcnt(0)\n"
      "v_cvt_pk_bf16_f32 v114, v144, v145\n"
      "v_cvt_pk_bf16_f32 v115, v146, v147\n"
      "v_mfma_f32_16x16x32_bf16 v[134:137], v[150:153], a[164:167], v[134:137]\n"
      "v_exp_f32_e64 v202, v202\n"
      "v_mfma_f32_16x16x32_bf16 v[198:201], v[168:171], a[180:183], v[198:201]\n"
      "v_exp_f32 v203, v203\n"
      "v_mfma_f32_16x16x32_bf16 v[198:201], v[194:197], a[184:187], v[198:201]\n"
      "v_exp_f32 v204, v204\n"
      "v_mfma_f32_16x16x32_bf16 v[198:201], v[150:153], a[188:191], v[198:201]\n"
      "v_exp_f32 v205, v205\n"
      "v_mfma_f32_16x16x32_bf16 v[206:209], v[168:171], a[204:207], v[206:209]\n"
      "v_exp_f32 v178, v178\n"
      "v_mfma_f32_16x16x32_bf16 v[206:209], v[194:197], a[208:211], v[206:209]\n"
      "v_cvt_pk_bf16_f32 v88, v202, v203\n"
      "v_cvt_pk_bf16_f32 v89, v204, v205\n"
      "v_mfma_f32_16x16x32_bf16 v[206:209], v[150:153], a[212:215], v[206:209]\n"
      "v_cvt_pk_bf16_f32 v90, v178, 0\n"
      "v_cvt_pk_bf16_f32 v96, v134, v135\n"
      "v_mfma_f32_16x16x32_bf16 v[122:125], v[138:141], v[118:121], v[52:55]\n"
      "v_cvt_pk_bf16_f32 v97, v136, v137\n"
      "v_cvt_pk_bf16_f32 v100, v198, v199\n"
      "v_mfma_f32_16x16x32_bf16 v[134:137], v[182:185], v[118:121], v[56:59]\n"
      "v_cvt_pk_bf16_f32 v101, v200, v201\n"
      "s_waitcnt vmcnt(6)\n"
      "v_or3_b32 v130, v130, v131, v132\n"
      "v_mfma_f32_16x16x32_bf16 v[198:201], v[154:157], v[118:121], v[60:63]\n"
      "v_cvt_pk_bf16_f32 v104, v206, v207\n"
      "v_cvt_pk_bf16_f32 v105, v208, v209\n"
      "v_mfma_f32_16x16x32_bf16 v[206:209], v[126:129], v[118:121], v[26:29]\n"
      "s_waitcnt vmcnt(5)\n"
      "v_or3_b32 v130, v130, v133, v78\n"
      "v_and_b32 v130, 0x7fff7fff, v130\n"
      "v_mfma_f32_16x16x16_bf16 v[122:125], v[190:191], v[186:187], v[122:125]\n"
      "ds_write_b32 v31, v130 offset:1024\n"
      "v_mfma_f32_16x16x16_bf16 v[134:137], v[142:143], v[186:187], v[134:137]\n"
      "ds_read_u16 v78, v11\n"
      "v_mfma_f32_16x16x16_bf16 v[198:201], v[110:111], v[186:187], v[198:201]\n"
      "v_mfma_f32_16x16x16_bf16 v[206:209], v[116:117], v[186:187], v[206:209]\n"
      "ds_read_u16 v82, v11 offset:32\n"
      "v_mfma_f32_16x16x32_bf16 v[118:121], v[32:35], v[112:115], 0\n"
      "v_mfma_f32_16x16x32_bf16 v[118:121], v[32:35], v[88:91], v[118:121]\n"
      "ds_read_u16 v86, v11 offset:64\n"
      "v_mfma_f32_16x16x32_bf16 v[130:133], v[96:99], v[112:115], 0\n"
      "s_nop 0\n"
      "s_nop 0\n"
      "s_nop 0\n"
      "s_nop 2\n"
      "s_cbranch_vccnz L_rare_15_%=\n"
      "L_back_16_%=:\n"
      "v_exp_f32_e64 v122, v122\n"
      "s_waitcnt lgkmcnt(2)\n"
      "v_mfma_f32_16x16x32_bf16 v[130:133], v[76:79], v[88:91], v[130:133]\n"
      "v_exp_f32 v123, v123\n"
      "v_mfma_f32_16x16x32_bf16 v[150:153], v[100:103], v[112:115], 0\n"
      "s_waitcnt lgkmcnt(1)\n"
      "v_exp_f32_e64 v124, v124\n"
      "v_mfma_f32_16x16x32_bf16 v[150:153], v[80:83], v[88:91], v[150:153]\n"
      "v_exp_f32 v125, v125\n"
      "v_mfma_f32_16x16x32_bf16 v[202:205], v[104:107], v[112:115], 0\n"
      "s_waitcnt lgkmcnt(0)\n"
      "v_exp_f32_e64 v134, v134\n"
      "v_mfma_f32_16x16x32_bf16 v[202:205], v[84:87], v[88:91], v[202:205]\n"
      "v_exp_f32 v135, v135\n"
      "v_mfma_f32_16x16x32_bf16 v[112:115], v[138:141], v[162:165], v[64:67]\n"
      "v_exp_f32 v136, v136\n"
      "v_mfma_f32_16x16x32_bf16 v[194:197], v[182:185], v[162:165], v[68:71]\n"
      "v_exp_f32_e64 v137, v137\n"
      "v_mfma_f32_16x16x32_bf16 v[168:171], v[154:157], v[162:165], v[72:75]\n"
      "v_cvt_pk_bf16_f32 v122, v122, v123\n"
      "v_cvt_pk_bf16_f32 v123, v124, v125\n"
      "v_mfma_f32_16x16x32_bf16 v[144:147], v[126:129], v[162:165], v[28:31]\n"
      "v_cvt_pk_bf16_f32 v124, v134, v135\n"
      "v_cvt_pk_bf16_f32 v125, v136, v137\n"
      "v_mfma_f32_16x16x16_bf16 v[112:115], v[190:191], v[148:149], v[112:115]\n"
      "v_exp_f32 v198, v198\n"
      "v_exp_f32 v199, v199\n"
      "v_mfma_f32_16x16x16_bf16 v[194:197], v[142:143], v[148:149], v[194:197]\n"
      "v_exp_f32 v200, v200\n"
      "v_exp_f32 v201, v201\n"
      "v_mfma_f32_16x16x16_bf16 v[168:171], v[110:111], v[148:149], v[168:171]\n"
      "v_exp_f32_e64 v206, v206\n"
      "v_cvt_pk_bf16_f32 v92, v198, v199\n"
      "v_mfma_f32_16x16x16_bf16 v[144:147], v[116:117], v[148:149], v[144:147]\n"
      "v_cvt_pk_bf16_f32 v93, v200, v201\n"
      "v_cvt_pk_bf16_f32 v94, v206, 0\n"
      "v_rcp_f32 v118, v118\n"
      "v_mfma_f32_16x16x32_bf16 v[134:137], v[32:35], v[122:125], 0\n"
      "v_mul_f32 v130, v118, v130\n"
      "v_mul_f32_e64 v131, v118, v131\n"
      "v_mfma_f32_16x16x32_bf16 v[134:137], v[32:35], v[92:95], v[134:137]\n"
      "v_cvt_pk_bf16_f32 v130, v130, v131\n"
      "v_mul_f32_e64 v132, v118, v132\n"
      "v_mfma_f32_16x16x32_bf16 v[198:201], v[96:99], v[122:125], 0\n"
      "ds_read_b128 v[146:149], v10\n"
      "v_mul_f32 v133, v118, v133\n"
      "v_mfma_f32_16x16x32_bf16 v[198:201], v[76:79], v[92:95], v[198:201]\n"
      "s_nop 1\n"
      "s_cbranch_vccnz L_rare_18_%=\n"
      "L_back_19_%=:\n"
      "v_exp_f32 v112, v112\n"
      "v_mfma_f32_16x16x32_bf16 v[162:165], v[100:103], v[122:125], 0\n"
      "v_exp_f32 v113, v113\n"
      "v_mfma_f32_16x16x32_bf16 v[162:165], v[80:83], v[92:95], v[162:165]\n"
      "ds_read_b64 v[88:89], v10 offset:16\n"
      "v_exp_f32 v114, v114\n"
      "v_mfma_f32_16x16x32_bf16 v[206:209], v[104:107], v[122:125], 0\n"
      "v_exp_f32 v115, v115\n"
      "v_mfma_f32_16x16x32_bf16 v[206:209], v[84:87], v[92:95], v[206:209]\n"
      "v_exp_f32_e64 v194, v194\n"
      "s_waitcnt lgkmcnt(1)\n"
      "v_mfma_f32_16x16x32_bf16 v[122:125], v[146:149], v[138:141], v[30:33]\n"
      "v_exp_f32 v195, v195\n"
      "v_mfma_f32_16x16x32_bf16 v[136:139], v[146:149], v[182:185], v[30:33]\n"
      "v_exp_f32 v196, v196\n"
      "v_mfma_f32_16x16x32_bf16 v[182:185], v[146:149], v[154:157], v[30:33]\n"
      "v_exp_f32 v197, v197\n"
      "v_mfma_f32_16x16x32_bf16 v[154:157], v[146:149], v[126:129], v[30:33]\n"
      "s_waitcnt lgkmcnt(0)\n"
      "v_cvt_pk_bf16_f32 v112, v112, v113\n"
      "v_cvt_pk_bf16_f32 v113, v114, v115\n"
      "v_mfma_f32_16x16x16_bf16 v[122:125], v[88:89], v[190:191], v[122:125]\n"
      "v_cvt_pk_bf16_f32 v114, v194, v195\n"
      "v_cvt_pk_bf16_f32 v115, v196, v197\n"
      "v_exp_f32_e64 v168, v168\n"
      "v_mfma_f32_16x16x16_bf16 v[136:139], v[88:89], v[142:143], v[136:139]\n"
      "v_exp_f32 v169, v169\n"
      "v_exp_f32 v170, v170\n"
      "v_mfma_f32_16x16x16_bf16 v[182:185], v[88:89], v[110:111], v[182:185]\n"
      "v_exp_f32 v171, v171\n"
      "v_exp_f32 v144, v144\n"
      "v_mfma_f32_16x16x16_bf16 v[154:157], v[88:89], v[116:117], v[154:157]\n"
      "v_cvt_pk_bf16_f32 v88, v168, v169\n"
      "v_cvt_pk_bf16_f32 v89, v170, v171\n"
      "v_cndmask_b32 v122, v122, v136, s[58:59]\n"
      "v_mfma_f32_16x16x32_bf16 v[124:127], v[32:35], v[112:115], 0\n"
      "v_cvt_pk_bf16_f32 v90, v144, 0\n"
      "v_cndmask_b32 v122, v122, v182, s[60:61]\n"
      "v_rcp_f32_e64 v134, v134\n"
      "v_mfma_f32_16x16x32_bf16 v[124:127], v[32:35], v[88:91], v[124:127]\n"
      "v_cndmask_b32 v122, v122, v154, s[62:63]\n"
      "s_nop 6\n"
      "s_cbranch_vccnz L_rare_21_%=\n"
      "L_back_22_%=:\n"
      "v_mfma_f32_16x16x32_bf16 v[126:129], v[96:99], v[112:115], 0\n"
      "s_add_u32 s42, s42, 1\n"
      "v_exp_f32 v122, v122\n"
      "v_mfma_f32_16x16x32_bf16 v[126:129], v[76:79], v[88:91], v[126:129]\n"
      "v_cvt_pk_bf16_f32 v122, v122, v122\n"
      "ds_write_b16 v13, v122\n"
      "v_mfma_f32_16x16x32_bf16 v[120:123], v[100:103], v[112:115], 0\n"
      "ds_read_b128 v[136:139], v14\n"
      "ds_read_b128 v[140:143], v14 offset:64\n"
      "v_mfma_f32_16x16x32_bf16 v[120:123], v[80:83], v[88:91], v[120:123]\n"
      "v_rcp_f32 v124, v124\n"
      "v_mfma_f32_16x16x32_bf16 v[154:157], v[104:107], v[112:115], 0\n"
      "v_mul_f32 v198, v134, v198\n"
      "v_mul_f32 v199, v134, v199\n"
      "v_mfma_f32_16x16x32_bf16 v[154:157], v[84:87], v[88:91], v[154:157]\n"
      "v_mul_f32 v126, v124, v126\n"
      "v_mul_f32 v127, v124, v127\n"
      "v_cvt_pk_bf16_f32 v198, v198, v199\n"
      "v_mul_f32 v200, v134, v200\n"
      "v_mul_f32 v201, v134, v201\n"
      "v_cvt_pk_bf16_f32 v126, v126, v127\n"
      "v_mul_f32 v128, v124, v128\n"
      "v_mul_f32 v129, v124, v129\n"
      "v_cvt_pk_bf16_f32 v131, v132, v133\n"
      "v_mul_f32 v150, v118, v150\n"
      "v_mul_f32 v151, v118, v151\n"
      "v_mul_f32 v152, v118, v152\n"
      "v_mul_f32_e64 v153, v118, v153\n"
      "s_waitcnt lgkmcnt(1)\n"
      "v_mfma_f32_16x16x32_bf16 v[112:115], v[136:139], v[32:35], 0\n"
      "ds_read_b64_tr_b16 a[216:217], v108\n"
      "ds_read_b64_tr_b16 a[218:219], v108 offset:1024\n"
      "s_waitcnt lgkmcnt(2)\n"
      "v_mfma_f32_16x16x32_bf16 v[112:115], v[140:143], v[32:35], v[112:115]\n"
      "ds_read_b64_tr_b16 a[240:241], v108 offset:8\n"
      "ds_read_b64_tr_b16 a[242:243], v108 offset:1032\n"
      "s_nop 5\n"
      "v_mfma_f32_16x16x32_bf16 v[114:117], v[136:139], v[96:99], 0\n"
      "ds_read_b64_tr_b16 v[232:233], v109 offset:2048\n"
      "ds_read_b64_tr_b16 v[234:235], v109 offset:2560\n"
      "v_mfma_f32_16x16x32_bf16 v[114:117], v[140:143], v[76:79], v[114:117]\n"
      "ds_read_b64_tr_b16 a[220:221], v108 offset:3072\n"
      "ds_read_b64_tr_b16 a[222:223], v108 offset:4096\n"
      "v_mfma_f32_16x16x32_bf16 v[96:99], v[136:139], v[100:103], 0\n"
      "ds_read_b64_tr_b16 a[244:245], v108 offset:3080\n"
      "ds_read_b64_tr_b16 a[246:247], v108 offset:4104\n"
      "v_mfma_f32_16x16x32_bf16 v[96:99], v[140:143], v[80:83], v[96:99]\n"
      "ds_read_b64_tr_b16 v[236:237], v109 offset:5120\n"
      "ds_read_b64_tr_b16 v[238:239], v109 offset:5632\n"
      "s_nop 5\n"
      "v_mfma_f32_16x16x32_bf16 v[98:101], v[136:139], v[104:107], 0\n"
      "ds_read_b64_tr_b16 a[224:225], v108 offset:6144\n"
      "ds_read_b64_tr_b16 a[226:227], v108 offset:7168\n"
      "v_mfma_f32_16x16x32_bf16 v[98:101], v[140:143], v[84:87], v[98:101]\n"
      "ds_read_b64_tr_b16 a[248:249], v108 offset:6152\n"
      "ds_read_b64_tr_b16 a[250:251], v108 offset:7176\n"
      "ds_read_b64_tr_b16 v[240:241], v109 offset:8192\n"
      "ds_read_b64_tr_b16 v[242:243], v109 offset:8704\n"
      "ds_read_b64_tr_b16 a[228:229], v108 offset:9216\n"
      "ds_read_b64_tr_b16 a[230:231], v108 offset:10240\n"
      "ds_read_b64_tr_b16 a[252:253], v108 offset:9224\n"
      "ds_read_b64_tr_b16 a[254:255], v108 offset:10248\n"
      "ds_read_b64_tr_b16 v[244:245], v109 offset:11264\n"
      "ds_read_b64_tr_b16 v[246:247], v109 offset:11776\n"
      "ds_read_b64_tr_b16 a[232:233], v108 offset:12288\n"
      "ds_read_b64_tr_b16 a[234:235], v108 offset:13312\n"
      "ds_read_b64_tr_b16 v[224:225], v108 offset:12296\n"
      "ds_read_b64_tr_b16 v[226:227], v108 offset:13320\n"
      "ds_read_b64_tr_b16 v[248:249], v109 offset:14336\n"
      "ds_read_b64_tr_b16 v[250:251], v109 offset:14848\n"
      "ds_read_b64_tr_b16 a[236:237], v108 offset:15360\n"
      "ds_read_b64_tr_b16 a[238:239], v108 offset:16384\n"
      "ds_read_b64_tr_b16 v[228:229], v108 offset:15368\n"
      "ds_read_b64_tr_b16 v[230:231], v108 offset:16392\n"
      "ds_read_b64_tr_b16 v[252:253], v109 offset:17408\n"
      "ds_read_b64_tr_b16 v[254:255], v109 offset:17920\n"
      "v_mov_b32_dpp v112, v112 quad_perm:[0,0,0,0] row_mask:0xf bank_mask:0xf\n"
      "v_rcp_f32 v112, v112\n"
      "s_waitcnt lgkmcnt(0)\n"
      "s_barrier\n"
      "v_cvt_pk_bf16_f32 v199, v200, v201\n"
      "v_mul_f32 v162, v134, v162\n"
      "v_mul_f32 v163, v134, v163\n"
      "v_mul_f32 v164, v134, v164\n"
      "v_mul_f32_e64 v165, v134, v165\n"
      "v_cvt_pk_bf16_f32 v127, v128, v129\n"
      "v_mul_f32 v120, v124, v120\n"
      "v_mul_f32 v121, v124, v121\n"
      "v_mul_f32 v122, v124, v122\n"
      "v_mul_f32 v123, v124, v123\n"
      "v_cvt_pk_bf16_f32 v132, v150, v151\n"
      "v_cvt_pk_bf16_f32 v133, v152, v153\n"
      "v_mul_f32 v202, v118, v202\n"
      "v_mul_f32 v203, v118, v203\n"
      "v_cvt_pk_bf16_f32 v200, v162, v163\n"
      "v_cvt_pk_bf16_f32 v201, v164, v165\n"
      "v_mul_f32 v206, v134, v206\n"
      "v_mul_f32 v207, v134, v207\n"
      "v_cvt_pk_bf16_f32 v128, v120, v121\n"
      "v_cvt_pk_bf16_f32 v129, v122, v123\n"
      "v_mul_f32 v154, v124, v154\n"
      "v_mul_f32 v155, v124, v155\n"
      "v_cndmask_b32 v114, v114, v96, s[58:59]\n"
      "ds_write_b128 v3, v[130:133]\n"
      "ds_write_b128 v3, v[198:201] offset:256\n"
      "ds_write_b128 v3, v[126:129] offset:512\n"
      "v_cvt_pk_bf16_f32 v202, v202, v203\n"
      "v_mul_f32 v204, v118, v204\n"
      "v_mul_f32 v205, v118, v205\n"
      "v_cvt_pk_bf16_f32 v206, v206, v207\n"
      "v_mul_f32 v208, v134, v208\n"
      "v_mul_f32 v209, v134, v209\n"
      "v_cvt_pk_bf16_f32 v154, v154, v155\n"
      "v_mul_f32 v156, v124, v156\n"
      "v_mul_f32 v157, v124, v157\n"
      "v_cndmask_b32 v114, v114, v98, s[60:61]\n"
      "v_cvt_pk_bf16_f32 v203, v204, v205\n"
      "v_cvt_pk_bf16_f32 v207, v208, v209\n"
      "v_cvt_pk_bf16_f32 v155, v156, v157\n"
      "v_mul_f32 v114, v112, v114\n"
      "ds_write_b64 v4, v[202:203]\n"
      "ds_write_b64 v4, v[206:207] offset:256\n"
      "ds_write_b64 v4, v[154:155] offset:512\n"
      "v_cvt_pk_bf16_f32 v114, v114, v114\n"
      "ds_write_b16 v12, v114\n"
      "s_waitcnt vmcnt(0)\n"
      "s_waitcnt lgkmcnt(0)\n"
      "s_barrier\n"
      "s_nop 7\n"
      "s_nop 7\n"
      "L_loop_%=:\n"
      "ds_read_b128 v[154:157], v9 offset:5120\n"
      "ds_read_b128 v[132:135], v9\n"
      "ds_read_b128 v[186:189], v9 offset:1024\n"
      "ds_read_b128 v[136:139], v0 offset:18432\n"
      "ds_read_b128 v[208:211], v1 offset:18432\n"
      "ds_read_b128 v[176:179], v0 offset:24576\n"
      "v_readlane_b32 s49, v38, s43\n"
      "ds_read_b128 v[198:201], v1 offset:24576\n"
      "v_readlane_b32 s45, v37, s43\n"
      "v_readlane_b32 s47, v37, s42\n"
      "ds_read_b128 v[164:167], v0 offset:30720\n"
      "ds_read_b128 v[140:143], v1 offset:30720\n"
      "s_and_b32 s49, s49, -4\n"
      "s_mov_b32 s46, s45\n"
      "s_cmp_ge_u32 s45, 0x40000000\n"
      "s_cbranch_scc1 L_rare_25_%=\n"
      "L_back_26_%=:\n"
      "s_waitcnt lgkmcnt(8)\n"
      "v_or3_b32 v154, v154, v155, v156\n"
      "v_or_b32 v154, v154, v157\n"
      "s_waitcnt lgkmcnt(7)\n"
      "v_add_f32 v132, v132, v134\n"
      "v_add_f32 v133, v133, v135\n"
      "v_cmp_ne_u32 s[56:57], 0, v154\n"
      "s_waitcnt lgkmcnt(6)\n"
      "v_add_f32 v186, v186, v188\n"
      "v_add_f32 v187, v187, v189\n"
      "v_add_f32 v132, v132, v186\n"
      "s_waitcnt lgkmcnt(5)\n"
      "v_mfma_f32_16x16x32_bf16 v[202:205], a[0:3], v[136:139], 0\n"
      "s_cmp_lg_u64 s[56:57], 0\n"
      "s_cbranch_scc1 L_rare_23_%=\n"
      "L_back_24_%=:\n"
      "v_add_f32 v133, v133, v187\n"
      "s_waitcnt lgkmcnt(4)\n"
      "v_mfma_f32_16x16x32_bf16 v[202:205], a[4:7], v[208:211], v[202:205]\n"
      "buffer_load_dwordx4 v[168:171], v27, s[24:27], s49 offen\n"
      "v_fma_f32 v186, -v132, v132, s52\n"
      "s_waitcnt lgkmcnt(3)\n"
      "v_mfma_f32_16x16x32_bf16 v[202:205], a[8:11], v[176:179], v[202:205]\n"
      "s_waitcnt lgkmcnt(2)\n"
      "v_fmac_f32 v186, 0x43400000, v133\n"
      "v_max_f32 v186, s52, v186\n"
      "v_mfma_f32_16x16x32_bf16 v[202:205], a[12:15], v[198:201], v[202:205]\n"
      "s_waitcnt lgkmcnt(1)\n"
      "v_rsq_f32 v186, v186\n"
      "v_mfma_f32_16x16x32_bf16 v[202:205], a[16:19], v[164:167], v[202:205]\n"
      "s_waitcnt lgkmcnt(0)\n"
      "v_mul_f32 v187, 0x43400000, v186\n"
      "v_mul_f32 v189, -v132, v186\n"
      "v_mfma_f32_16x16x32_bf16 v[202:205], a[20:23], v[140:143], v[202:205]\n"
      "buffer_load_dword v39, v29, s[24:27], s49 offen\n"
      "v_mov_b32 v76, v187\n"
      "v_mfma_f32_16x16x32_bf16 v[132:135], a[24:27], v[136:139], 0\n"
      "s_add_u32 m0, s64, 0\n"
      "v_mov_b32 v77, v189\n"
      "v_permlane32_swap_b32 v187, v76\n"
      "v_mfma_f32_16x16x32_bf16 v[132:135], a[28:31], v[208:211], v[132:135]\n"
      "buffer_load_dwordx4 v16, s[16:19], s46 offen  lds\n"
      "v_permlane32_swap_b32 v189, v77\n"
      "v_mfma_f32_16x16x32_bf16 v[132:135], a[32:35], v[176:179], v[132:135]\n"
      "s_add_u32 m0, s64, 4096\n"
      "v_mov_b32 v90, v187\n"
      "v_mov_b32 v88, v189\n"
      "v_mfma_f32_16x16x32_bf16 v[132:135], a[36:39], v[198:201], v[132:135]\n"
      "buffer_load_dwordx4 v17, s[16:19], s46 offen  lds\n"
      "v_permlane16_swap_b32 v187, v90\n"
      "v_mfma_f32_16x16x32_bf16 v[132:135], a[40:43], v[164:167], v[132:135]\n"
      "s_add_u32 m0, s64, 8192\n"
      "v_permlane16_swap_b32_e64 v189, v88\n"
      "v_mfma_f32_16x16x32_bf16 v[132:135], a[44:47], v[140:143], v[132:135]\n"
      "buffer_load_dwordx4 v18, s[16:19], s46 offen  lds\n"
      "v_fma_f32 v96, v96, v187, v189\n"
      "v_mfma_f32_16x16x32_bf16 v[156:159], a[48:51], v[136:139], 0\n"
      "s_add_u32 m0, s64, 12288\n"
      "v_fma_f32 v97, v97, v187, v189\n"
      "v_cvt_pk_bf16_f32 v96, v96, v97\n"
      "v_mfma_f32_16x16x32_bf16 v[156:159], a[52:55], v[208:211], v[156:159]\n"
      "buffer_load_dwordx4 v19, s[16:19], s46 offen  lds\n"
      "v_fma_f32 v98, v98, v187, v189\n"
      "v_mfma_f32_16x16x32_bf16 v[156:159], a[56:59], v[176:179], v[156:159]\n"
      "s_mov_b32 m0, s65\n"
      "v_fma_f32 v99, v99, v187, v189\n"
      "v_fma_f32 v104, v104, v187, v189\n"
      "v_mfma_f32_16x16x32_bf16 v[156:159], a[60:63], v[198:201], v[156:159]\n"
      "buffer_load_dwordx4 v15, s[16:19], s46 offen  lds\n"
      "v_fma_f32 v105, v105, v187, v189\n"
      "v_mfma_f32_16x16x32_bf16 v[156:159], a[64:67], v[164:167], v[156:159]\n"
      "s_mov_b32 s48, s47\n"
      "v_cvt_pk_bf16_f32 v97, v98, v99\n"
      "v_fma_f32 v100, v100, v187, v189\n"
      "v_mfma_f32_16x16x32_bf16 v[156:159], a[68:71], v[140:143], v[156:159]\n"
      "s_cmp_ge_u32 s47, 0x40000000\n"
      "s_cbranch_scc1 L_rare_29_%=\n"
      "L_back_30_%=:\n"
      "v_fma_f32 v101, v101, v187, v189\n"
      "v_mfma_f32_16x16x32_bf16 v[190:193], a[72:75], v[136:139], 0\n"
      "s_or_b64 vcc, s[56:57], s[54:55]\n"
      "v_fma_f32 v102, v102, v187, v189\n"
      "v_fma_f32 v103, v103, v187, v189\n"
      "v_mfma_f32_16x16x32_bf16 v[190:193], a[76:79], v[208:211], v[190:193]\n"
      "s_cmp_ge_u32 s45, 0x40000000\n"
      "s_cbranch_scc1 L_rare_27_%=\n"
      "L_back_28_%=:\n"
      "v_cvt_pk_bf16_f32 v104, v104, v105\n"
      "v_mfma_f32_16x16x32_bf16 v[190:193], a[80:83], v[176:179], v[190:193]\n"
      "s_add_u32 s43, s43, 1\n"
      "v_fma_f32 v106, v106, v187, v189\n"
      "v_fma_f32 v107, v107, v187, v189\n"
      "v_mfma_f32_16x16x32_bf16 v[190:193], a[84:87], v[198:201], v[190:193]\n"
      "v_cvt_pk_bf16_f32 v98, v100, v101\n"
      "v_cvt_pk_bf16_f32 v99, v102, v103\n"
      "v_mfma_f32_16x16x32_bf16 v[190:193], a[88:91], v[164:167], v[190:193]\n"
      "ds_write_b128 v5, v[96:99] offset:18432\n"
      "v_mfma_f32_16x16x32_bf16 v[190:193], a[92:95], v[140:143], v[190:193]\n"
      "v_cvt_pk_bf16_f32 v105, v106, v107\n"
      "ds_write_b64 v6, v[104:105] offset:18432\n"
      "v_mfma_f32_16x16x32_bf16 v[102:105], a[96:99], v[136:139], 0\n"
      "ds_read_b128 v[98:101], v0 offset:20480\n"
      "v_fma_f32 v108, v108, v90, v88\n"
      "v_mfma_f32_16x16x32_bf16 v[102:105], a[100:103], v[208:211], v[102:105]\n"
      "ds_read_b128 v[144:147], v1 offset:20480\n"
      "v_fma_f32 v109, v109, v90, v88\n"
      "v_mfma_f32_16x16x32_bf16 v[102:105], a[104:107], v[176:179], v[102:105]\n"
      "ds_read_b128 v[186:189], v0 offset:26624\n"
      "v_cvt_pk_bf16_f32 v108, v108, v109\n"
      "v_mfma_f32_16x16x32_bf16 v[102:105], a[108:111], v[198:201], v[102:105]\n"
      "v_fma_f32 v110, v110, v90, v88\n"
      "v_fma_f32 v111, v111, v90, v88\n"
      "v_mfma_f32_16x16x32_bf16 v[102:105], a[112:115], v[164:167], v[102:105]\n"
      "ds_read_b128 v[216:219], v1 offset:26624\n"
      "v_fma_f32 v116, v116, v90, v88\n"
      "v_mfma_f32_16x16x32_bf16 v[102:105], a[116:119], v[140:143], v[102:105]\n"
      "ds_read_b128 v[152:155], v0 offset:32768\n"
      "v_fma_f32 v117, v117, v90, v88\n"
      "v_mfma_f32_16x16x32_bf16 v[194:197], a[120:123], v[136:139], 0\n"
      "ds_read_b128 v[136:139], v1 offset:32768\n"
      "v_cvt_pk_bf16_f32 v109, v110, v111\n"
      "v_mfma_f32_16x16x32_bf16 v[194:197], a[124:127], v[208:211], v[194:197]\n"
      "s_waitcnt lgkmcnt(5)\n"
      "v_fma_f32 v112, v112, v90, v88\n"
      "v_fma_f32 v113, v113, v90, v88\n"
      "v_mfma_f32_16x16x32_bf16 v[194:197], a[128:131], v[176:179], v[194:197]\n"
      "s_waitcnt lgkmcnt(4)\n"
      "v_fma_f32 v114, v114, v90, v88\n"
      "v_fma_f32 v115, v115, v90, v88\n"
      "v_mfma_f32_16x16x32_bf16 v[194:197], a[132:135], v[198:201], v[194:197]\n"
      "s_waitcnt lgkmcnt(3)\n"
      "v_cvt_pk_bf16_f32 v116, v116, v117\n"
      "v_fma_f32 v118, v118, v90, v88\n"
      "v_mfma_f32_16x16x32_bf16 v[194:197], a[136:139], v[164:167], v[194:197]\n"
      "s_waitcnt lgkmcnt(2)\n"
      "v_fma_f32 v119, v119, v90, v88\n"
      "v_cvt_pk_bf16_f32 v110, v112, v113\n"
      "v_mfma_f32_16x16x32_bf16 v[194:197], a[140:143], v[140:143], v[194:197]\n"
      "s_waitcnt lgkmcnt(1)\n"
      "v_cvt_pk_bf16_f32 v111, v114, v115\n"
      "v_cvt_pk_bf16_f32 v117, v118, v119\n"
      "v_mfma_f32_16x16x32_bf16 v[112:115], a[0:3], v[98:101], 0\n"
      "s_waitcnt lgkmcnt(0)\n"
      "ds_write_b128 v5, v[108:111] offset:24576\n"
      "v_mfma_f32_16x16x32_bf16 v[112:115], a[4:7], v[144:147], v[112:115]\n"
      "ds_write_b64 v6, v[116:117] offset:24576\n"
      "v_mfma_f32_16x16x32_bf16 v[112:115], a[8:11], v[186:189], v[112:115]\n"
      "v_fma_f32 v120, v120, v76, v77\n"
      "v_fma_f32 v121, v121, v76, v77\n"
      "v_mfma_f32_16x16x32_bf16 v[112:115], a[12:15], v[216:219], v[112:115]\n"
      "v_cvt_pk_bf16_f32 v120, v120, v121\n"
      "v_fma_f32 v122, v122, v76, v77\n"
      "v_mfma_f32_16x16x32_bf16 v[112:115], a[16:19], v[152:155], v[112:115]\n"
      "v_fma_f32 v123, v123, v76, v77\n"
      "v_fma_f32 v128, v128, v76, v77\n"
      "v_mfma_f32_16x16x32_bf16 v[112:115], a[20:23], v[136:139], v[112:115]\n"
      "v_fma_f32 v129, v129, v76, v77\n"
      "v_cvt_pk_bf16_f32 v121, v122, v123\n"
      "v_mfma_f32_16x16x32_bf16 v[140:143], a[24:27], v[98:101], 0\n"
      "v_fma_f32 v124, v124, v76, v77\n"
      "v_fma_f32 v125, v125, v76, v77\n"
      "v_mfma_f32_16x16x32_bf16 v[140:143], a[28:31], v[144:147], v[140:143]\n"
      "v_fma_f32 v126, v126, v76, v77\n"
      "v_fma_f32 v127, v127, v76, v77\n"
      "v_mfma_f32_16x16x32_bf16 v[140:143], a[32:35], v[186:189], v[140:143]\n"
      "v_cvt_pk_bf16_f32 v128, v128, v129\n"
      "v_fma_f32 v130, v130, v76, v77\n"
      "v_mfma_f32_16x16x32_bf16 v[140:143], a[36:39], v[216:219], v[140:143]\n"
      "v_fma_f32 v131, v131, v76, v77\n"
      "v_cvt_pk_bf16_f32 v122, v124, v125\n"
      "v_mfma_f32_16x16x32_bf16 v[140:143], a[40:43], v[152:155], v[140:143]\n"
      "v_cvt_pk_bf16_f32 v123, v126, v127\n"
      "v_cvt_pk_bf16_f32 v129, v130, v131\n"
      "v_mfma_f32_16x16x32_bf16 v[140:143], a[44:47], v[136:139], v[140:143]\n"
      "ds_write_b128 v5, v[120:123] offset:30720\n"
      "v_mfma_f32_16x16x32_bf16 v[106:109], a[48:51], v[98:101], 0\n"
      "ds_write_b64 v6, v[128:129] offset:30720\n"
      "v_mfma_f32_16x16x32_bf16 v[106:109], a[52:55], v[144:147], v[106:109]\n"
      "v_fma_f32 v177, v202, v202, s51\n"
      "v_fma_f32 v198, v190, v190, s51\n"
      "v_mfma_f32_16x16x32_bf16 v[106:109], a[56:59], v[186:189], v[106:109]\n"
      "v_fmac_f32 v177, v203, v203\n"
      "v_fmac_f32 v198, v191, v191\n"
      "v_mfma_f32_16x16x32_bf16 v[106:109], a[60:63], v[216:219], v[106:109]\n"
      "v_fmac_f32 v177, v204, v204\n"
      "v_fmac_f32 v198, v192, v192\n"
      "v_mfma_f32_16x16x32_bf16 v[106:109], a[64:67], v[152:155], v[106:109]\n"
      "v_fmac_f32 v177, v205, v205\n"
      "v_fmac_f32 v198, v193, v193\n"
      "v_mfma_f32_16x16x32_bf16 v[106:109], a[68:71], v[136:139], v[106:109]\n"
      "v_fmac_f32 v177, v132, v132\n"
      "v_fmac_f32 v198, v102, v102\n"
      "v_mfma_f32_16x16x32_bf16 v[124:127], a[72:75], v[98:101], 0\n"
      "v_fmac_f32 v177, v133, v133\n"
      "v_fmac_f32 v198, v103, v103\n"
      "v_mfma_f32_16x16x32_bf16 v[124:127], a[76:79], v[144:147], v[124:127]\n"
      "v_fmac_f32 v177, v134, v134\n"
      "v_fmac_f32 v198, v104, v104\n"
      "v_mfma_f32_16x16x32_bf16 v[124:127], a[80:83], v[186:189], v[124:127]\n"
      "v_fmac_f32 v177, v135, v135\n"
      "v_fmac_f32 v198, v105, v105\n"
      "v_mfma_f32_16x16x32_bf16 v[124:127], a[84:87], v[216:219], v[124:127]\n"
      "v_fmac_f32 v177, v156, v156\n"
      "v_fmac_f32 v198, v194, v194\n"
      "v_mfma_f32_16x16x32_bf16 v[124:127], a[88:91], v[152:155], v[124:127]\n"
      "v_fmac_f32 v177, v157, v157\n"
      "v_fmac_f32 v198, v195, v195\n"
      "v_mfma_f32_16x16x32_bf16 v[124:127], a[92:95], v[136:139], v[124:127]\n"
      "v_fmac_f32 v177, v158, v158\n"
      "v_fmac_f32 v198, v196, v196\n"
      "v_mfma_f32_16x16x32_bf16 v[116:119], a[96:99], v[98:101], 0\n"
      "v_fmac_f32 v177, v159, v159\n"
      "v_fmac_f32 v198, v197, v197\n"
      "v_mfma_f32_16x16x32_bf16 v[116:119], a[100:103], v[144:147], v[116:119]\n"
      "ds_read_b128 v[128:131], v0 offset:22528\n"
      "v_permlane16_swap_b32 v177, v198\n"
      "v_mfma_f32_16x16x32_bf16 v[116:119], a[104:107], v[186:189], v[116:119]\n"
      "ds_read_b128 v[120:123], v1 offset:22528\n"
      "v_add_f32 v177, v177, v198\n"
      "v_mfma_f32_16x16x32_bf16 v[116:119], a[108:111], v[216:219], v[116:119]\n"
      "ds_read_b128 v[178:181], v0 offset:28672\n"
      "v_mov_b32 v198, v177\n"
      "v_mfma_f32_16x16x32_bf16 v[116:119], a[112:115], v[152:155], v[116:119]\n"
      "ds_read_b128 v[148:151], v1 offset:28672\n"
      "v_permlane32_swap_b32 v177, v198\n"
      "v_mfma_f32_16x16x32_bf16 v[116:119], a[116:119], v[136:139], v[116:119]\n"
      "ds_read_b128 v[164:167], v0 offset:34816\n"
      "v_add_f32 v177, v177, v198\n"
      "v_mfma_f32_16x16x32_bf16 v[212:215], a[120:123], v[98:101], 0\n"
      "ds_read_b128 v[96:99], v7\n"
      "v_rsq_f32 v177, v177\n"
      "v_mfma_f32_16x16x32_bf16 v[212:215], a[124:127], v[144:147], v[212:215]\n"
      "s_waitcnt lgkmcnt(5)\n"
      "v_mov_b32 v198, v177\n"
      "ds_read_b128 v[208:211], v1 offset:34816\n"
      "v_mfma_f32_16x16x32_bf16 v[212:215], a[128:131], v[186:189], v[212:215]\n"
      "s_waitcnt lgkmcnt(5)\n"
      "v_permlane16_swap_b32 v177, v198\n"
      "v_mfma_f32_16x16x32_bf16 v[212:215], a[132:135], v[216:219], v[212:215]\n"
      "s_waitcnt lgkmcnt(4)\n"
      "v_mul_f32 v177, s50, v177\n"
      "v_mul_f32 v190, v198, v190\n"
      "v_mfma_f32_16x16x32_bf16 v[212:215], a[136:139], v[152:155], v[212:215]\n"
      "s_waitcnt lgkmcnt(3)\n"
      "v_mul_f32 v191, v198, v191\n"
      "v_mul_f32 v202, v177, v202\n"
      "v_mfma_f32_16x16x32_bf16 v[212:215], a[140:143], v[136:139], v[212:215]\n"
      "s_waitcnt lgkmcnt(2)\n"
      "v_mul_f32 v203, v177, v203\n"
      "v_cvt_pk_bf16_f32 v190, v190, v191\n"
      "v_mfma_f32_16x16x32_bf16 v[152:155], a[0:3], v[128:131], 0\n"
      "s_waitcnt lgkmcnt(1)\n"
      "v_mul_f32 v192, v198, v192\n"
      "v_mul_f32 v193, v198, v193\n"
      "v_mfma_f32_16x16x32_bf16 v[152:155], a[4:7], v[120:123], v[152:155]\n"
      "ds_read_b128 v[136:139], v7 offset:4096\n"
      "v_mul_f32 v194, v198, v194\n"
      "v_mfma_f32_16x16x32_bf16 v[152:155], a[8:11], v[178:181], v[152:155]\n"
      "s_waitcnt lgkmcnt(1)\n"
      "v_mul_f32 v195, v198, v195\n"
      "v_cvt_pk_bf16_f32 v202, v202, v203\n"
      "v_mfma_f32_16x16x32_bf16 v[152:155], a[12:15], v[148:151], v[152:155]\n"
      "ds_read_b128 v[172:175], v7 offset:8192\n"
      "v_mul_f32 v204, v177, v204\n"
      "v_mfma_f32_16x16x32_bf16 v[152:155], a[16:19], v[164:167], v[152:155]\n"
      "ds_read_b128 v[184:187], v7 offset:12288\n"
      "v_mul_f32 v205, v177, v205\n"
      "v_mfma_f32_16x16x32_bf16 v[152:155], a[20:23], v[208:211], v[152:155]\n"
      "ds_read_b128 v[160:163], v7 offset:16384\n"
      "v_mul_f32 v156, v177, v156\n"
      "v_mfma_f32_16x16x32_bf16 v[218:221], a[24:27], v[128:131], 0\n"
      "buffer_store_dwordx4 v[96:99], v20, s[20:23], s48 offen sc1\n"
      "v_mul_f32 v157, v177, v157\n"
      "v_mfma_f32_16x16x32_bf16 v[218:221], a[28:31], v[120:123], v[218:221]\n"
      "s_waitcnt lgkmcnt(3)\n"
      "v_cvt_pk_bf16_f32 v191, v192, v193\n"
      "v_mul_f32 v102, v198, v102\n"
      "v_mfma_f32_16x16x32_bf16 v[218:221], a[32:35], v[178:181], v[218:221]\n"
      "buffer_store_dwordx4 v[136:139], v21, s[20:23], s48 offen sc1\n"
      "v_mul_f32 v103, v198, v103\n"
      "v_mfma_f32_16x16x32_bf16 v[218:221], a[36:39], v[148:151], v[218:221]\n"
      "s_waitcnt lgkmcnt(2)\n"
      "v_mul_f32 v104, v198, v104\n"
      "v_mul_f32 v105, v198, v105\n"
      "v_mfma_f32_16x16x32_bf16 v[218:221], a[40:43], v[164:167], v[218:221]\n"
      "s_waitcnt lgkmcnt(1)\n"
      "v_cvt_pk_bf16_f32 v194, v194, v195\n"
      "v_mul_f32 v196, v198, v196\n"
      "v_mfma_f32_16x16x32_bf16 v[218:221], a[44:47], v[208:211], v[218:221]\n"
      "s_waitcnt lgkmcnt(0)\n"
      "v_mul_f32 v197, v198, v197\n"
      "v_cvt_pk_bf16_f32 v203, v204, v205\n"
      "v_mfma_f32_16x16x32_bf16 v[198:201], a[48:51], v[128:131], 0\n"
      "buffer_store_dwordx4 v[172:175], v22, s[20:23], s48 offen sc1\n"
      "v_mul_f32 v132, v177, v132\n"
      "v_mfma_f32_16x16x32_bf16 v[198:201], a[52:55], v[120:123], v[198:201]\n"
      "buffer_store_dwordx4 v[184:187], v23, s[20:23], s48 offen sc1\n"
      "v_mul_f32 v133, v177, v133\n"
      "v_mfma_f32_16x16x32_bf16 v[198:201], a[56:59], v[178:181], v[198:201]\n"
      "buffer_store_dwordx4 v[160:163], v25, s[20:23], s48 offen sc1\n"
      "v_mul_f32 v134, v177, v134\n"
      "v_mfma_f32_16x16x32_bf16 v[198:201], a[60:63], v[148:151], v[198:201]\n"
      "s_cmp_ge_u32 s47, 0x40000000\n"
      "s_cbranch_scc1 L_rare_31_%=\n"
      "L_back_32_%=:\n"
      "v_mul_f32 v135, v177, v135\n"
      "v_mfma_f32_16x16x32_bf16 v[198:201], a[64:67], v[164:167], v[198:201]\n"
      "v_cvt_pk_bf16_f32 v156, v156, v157\n"
      "v_mul_f32 v158, v177, v158\n"
      "v_mfma_f32_16x16x32_bf16 v[198:201], a[68:71], v[208:211], v[198:201]\n"
      "v_mul_f32 v159, v177, v159\n"
      "v_cvt_pk_bf16_f32 v192, v102, v103\n"
      "v_mfma_f32_16x16x32_bf16 v[160:163], a[72:75], v[128:131], 0\n"
      "v_cvt_pk_bf16_f32 v193, v104, v105\n"
      "v_cvt_pk_bf16_f32 v195, v196, v197\n"
      "v_mfma_f32_16x16x32_bf16 v[160:163], a[76:79], v[120:123], v[160:163]\n"
      "v_cvt_pk_bf16_f32 v204, v132, v133\n"
      "v_cvt_pk_bf16_f32 v205, v134, v135\n"
      "v_mfma_f32_16x16x32_bf16 v[160:163], a[80:83], v[178:181], v[160:163]\n"
      "v_cvt_pk_bf16_f32 v157, v158, v159\n"
      "v_fma_f32 v137, v112, v112, s51\n"
      "v_mfma_f32_16x16x32_bf16 v[160:163], a[84:87], v[148:151], v[160:163]\n"
      "v_fma_f32 v134, v124, v124, s51\n"
      "v_fmac_f32 v137, v113, v113\n"
      "v_mfma_f32_16x16x32_bf16 v[160:163], a[88:91], v[164:167], v[160:163]\n"
      "v_fmac_f32 v134, v125, v125\n"
      "v_fmac_f32 v137, v114, v114\n"
      "v_mfma_f32_16x16x32_bf16 v[160:163], a[92:95], v[208:211], v[160:163]\n"
      "v_fmac_f32 v134, v126, v126\n"
      "v_fmac_f32 v137, v115, v115\n"
      "v_mfma_f32_16x16x32_bf16 v[96:99], a[96:99], v[128:131], 0\n"
      "v_fmac_f32 v134, v127, v127\n"
      "v_fmac_f32 v137, v140, v140\n"
      "v_mfma_f32_16x16x32_bf16 v[96:99], a[100:103], v[120:123], v[96:99]\n"
      "v_fmac_f32 v134, v116, v116\n"
      "v_fmac_f32 v137, v141, v141\n"
      "v_mfma_f32_16x16x32_bf16 v[96:99], a[104:107], v[178:181], v[96:99]\n"
      "v_fmac_f32 v134, v117, v117\n"
      "v_fmac_f32 v137, v142, v142\n"
      "v_mfma_f32_16x16x32_bf16 v[96:99], a[108:111], v[148:151], v[96:99]\n"
      "v_fmac_f32 v134, v118, v118\n"
      "v_fmac_f32 v137, v143, v143\n"
      "v_mfma_f32_16x16x32_bf16 v[96:99], a[112:115], v[164:167], v[96:99]\n"
      "v_fmac_f32 v134, v119, v119\n"
      "v_fmac_f32 v137, v106, v106\n"
      "v_mfma_f32_16x16x32_bf16 v[96:99], a[116:119], v[208:211], v[96:99]\n"
      "v_fmac_f32 v134, v212, v212\n"
      "v_fmac_f32 v137, v107, v107\n"
      "v_mfma_f32_16x16x32_bf16 v[174:177], a[120:123], v[128:131], 0\n"
      "v_fmac_f32 v134, v213, v213\n"
      "v_fmac_f32 v137, v108, v108\n"
      "v_mfma_f32_16x16x32_bf16 v[174:177], a[124:127], v[120:123], v[174:177]\n"
      "v_fmac_f32 v134, v214, v214\n"
      "v_fmac_f32 v137, v109, v109\n"
      "v_mfma_f32_16x16x32_bf16 v[174:177], a[128:131], v[178:181], v[174:177]\n"
      "v_fmac_f32 v134, v215, v215\n"
      "v_fma_f32 v159, v152, v152, s51\n"
      "v_mfma_f32_16x16x32_bf16 v[174:177], a[132:135], v[148:151], v[174:177]\n"
      "v_permlane16_swap_b32 v137, v134\n"
      "v_mfma_f32_16x16x32_bf16 v[174:177], a[136:139], v[164:167], v[174:177]\n"
      "v_add_f32 v137, v137, v134\n"
      "v_mov_b32_e64 v134, v137\n"
      "v_mfma_f32_16x16x32_bf16 v[174:177], a[140:143], v[208:211], v[174:177]\n"
      "v_fma_f32 v158, v160, v160, s51\n"
      "v_permlane32_swap_b32 v137, v134\n"
      "v_mfma_f32_16x16x32_bf16 v[102:105], v[128:131], a[144:147], 0\n"
      "v_add_f32 v137, v137, v134\n"
      "v_rsq_f32_e64 v137, v137\n"
      "v_mfma_f32_16x16x32_bf16 v[102:105], v[120:123], a[148:151], v[102:105]\n"
      "v_mov_b32 v134, v137\n"
      "v_fmac_f32 v159, v153, v153\n"
      "v_mfma_f32_16x16x32_bf16 v[102:105], v[178:181], a[152:155], v[102:105]\n"
      "v_permlane16_swap_b32_e64 v137, v134\n"
      "v_mfma_f32_16x16x32_bf16 v[144:147], v[128:131], a[168:171], 0\n"
      "v_mul_f32 v137, s50, v137\n"
      "v_mul_f32 v124, v134, v124\n"
      "v_mfma_f32_16x16x32_bf16 v[144:147], v[120:123], a[172:175], v[144:147]\n"
      "v_mul_f32 v125, v134, v125\n"
      "v_mul_f32 v112, v137, v112\n"
      "v_mfma_f32_16x16x32_bf16 v[144:147], v[178:181], a[176:179], v[144:147]\n"
      "v_mul_f32 v113, v137, v113\n"
      "v_cvt_pk_bf16_f32 v124, v124, v125\n"
      "v_mfma_f32_16x16x32_bf16 v[186:189], v[128:131], a[192:195], 0\n"
      "v_mul_f32 v126, v134, v126\n"
      "v_mul_f32 v127, v134, v127\n"
      "v_mfma_f32_16x16x32_bf16 v[186:189], v[120:123], a[196:199], v[186:189]\n"
      "v_mul_f32 v212, v134, v212\n"
      "v_mul_f32_e64 v213, v134, v213\n"
      "v_mfma_f32_16x16x32_bf16 v[186:189], v[178:181], a[200:203], v[186:189]\n"
      "v_cvt_pk_bf16_f32 v112, v112, v113\n"
      "v_mul_f32 v114, v137, v114\n"
      "v_mfma_f32_16x16x32_bf16 v[102:105], v[148:151], a[156:159], v[102:105]\n"
      "ds_read_b128 v[178:181], v0 offset:20480\n"
      "v_mul_f32 v115, v137, v115\n"
      "v_mfma_f32_16x16x32_bf16 v[102:105], v[164:167], a[160:163], v[102:105]\n"
      "ds_read_b128 v[120:123], v1 offset:20480\n"
      "v_mul_f32_e64 v106, v137, v106\n"
      "v_mfma_f32_16x16x32_bf16 v[102:105], v[208:211], a[164:167], v[102:105]\n"
      "ds_read_b128 v[128:131], v0 offset:26624\n"
      "v_mul_f32 v107, v137, v107\n"
      "v_mfma_f32_16x16x32_bf16 v[144:147], v[148:151], a[180:183], v[144:147]\n"
      "v_cvt_pk_bf16_f32 v125, v126, v127\n"
      "v_mul_f32 v116, v134, v116\n"
      "v_mfma_f32_16x16x32_bf16 v[144:147], v[164:167], a[184:187], v[144:147]\n"
      "v_mul_f32 v117, v134, v117\n"
      "v_mul_f32 v118, v134, v118\n"
      "v_mfma_f32_16x16x32_bf16 v[144:147], v[208:211], a[188:191], v[144:147]\n"
      "v_mul_f32_e64 v119, v134, v119\n"
      "v_cvt_pk_bf16_f32 v212, v212, v213\n"
      "v_mfma_f32_16x16x32_bf16 v[186:189], v[148:151], a[204:207], v[186:189]\n"
      "s_waitcnt lgkmcnt(2)\n"
      "v_mul_f32 v214, v134, v214\n"
      "v_mul_f32_e64 v215, v134, v215\n"
      "v_mfma_f32_16x16x32_bf16 v[186:189], v[164:167], a[208:211], v[186:189]\n"
      "s_waitcnt lgkmcnt(1)\n"
      "v_cvt_pk_bf16_f32 v113, v114, v115\n"
      "v_mul_f32 v140, v137, v140\n"
      "v_mfma_f32_16x16x32_bf16 v[186:189], v[208:211], a[212:215], v[186:189]\n"
      "s_waitcnt lgkmcnt(0)\n"
      "v_mul_f32 v141, v137, v141\n"
      "v_mul_f32 v142, v137, v142\n"
      "v_mfma_f32_16x16x32_bf16 v[132:135], v[178:181], a[144:147], 0\n"
      "ds_read_b128 v[208:211], v1 offset:26624\n"
      "v_mul_f32 v143, v137, v143\n"
      "v_mfma_f32_16x16x32_bf16 v[132:135], v[120:123], a[148:151], v[132:135]\n"
      "ds_read_b128 v[164:167], v0 offset:32768\n"
      "v_cvt_pk_bf16_f32 v106, v106, v107\n"
      "v_mfma_f32_16x16x32_bf16 v[132:135], v[128:131], a[152:155], v[132:135]\n"
      "v_mul_f32_e64 v108, v137, v108\n"
      "v_mul_f32_e64 v109, v137, v109\n"
      "v_mfma_f32_16x16x32_bf16 v[136:139], v[178:181], a[168:171], 0\n"
      "ds_read_b128 v[148:151], v1 offset:32768\n"
      "v_cvt_pk_bf16_f32 v126, v116, v117\n"
      "v_mfma_f32_16x16x32_bf16 v[136:139], v[120:123], a[172:175], v[136:139]\n"
      "v_cvt_pk_bf16_f32 v127, v118, v119\n"
      "v_cvt_pk_bf16_f32 v213, v214, v215\n"
      "v_mfma_f32_16x16x32_bf16 v[136:139], v[128:131], a[176:179], v[136:139]\n"
      "v_cvt_pk_bf16_f32 v114, v140, v141\n"
      "v_cvt_pk_bf16_f32 v115, v142, v143\n"
      "v_mfma_f32_16x16x32_bf16 v[140:143], v[178:181], a[192:195], 0\n"
      "s_waitcnt lgkmcnt(2)\n"
      "v_cvt_pk_bf16_f32 v107, v108, v109\n"
      "v_fmac_f32 v158, v161, v161\n"
      "v_mfma_f32_16x16x32_bf16 v[140:143], v[120:123], a[196:199], v[140:143]\n"
      "s_waitcnt lgkmcnt(1)\n"
      "v_fmac_f32 v159, v154, v154\n"
      "v_fmac_f32 v158, v162, v162\n"
      "v_mfma_f32_16x16x32_bf16 v[140:143], v[128:131], a[200:203], v[140:143]\n"
      "v_fmac_f32 v159, v155, v155\n"
      "v_fmac_f32 v158, v163, v163\n"
      "v_mfma_f32_16x16x32_bf16 v[132:135], v[208:211], a[156:159], v[132:135]\n"
      "s_waitcnt lgkmcnt(0)\n"
      "v_fmac_f32 v159, v218, v218\n"
      "v_fmac_f32 v158, v96, v96\n"
      "v_mfma_f32_16x16x32_bf16 v[132:135], v[164:167], a[160:163], v[132:135]\n"
      "v_fmac_f32 v159, v219, v219\n"
      "v_fmac_f32 v158, v97, v97\n"
      "v_mfma_f32_16x16x32_bf16 v[132:135], v[148:151], a[164:167], v[132:135]\n"
      "v_fmac_f32 v159, v220, v220\n"
      "v_fmac_f32 v158, v98, v98\n"
      "v_mfma_f32_16x16x32_bf16 v[136:139], v[208:211], a[180:183], v[136:139]\n"
      "v_fmac_f32 v159, v221, v221\n"
      "v_fmac_f32 v158, v99, v99\n"
      "v_mfma_f32_16x16x32_bf16 v[136:139], v[164:167], a[184:187], v[136:139]\n"
      "ds_read_b128 v[120:123], v10 offset:1184\n"
      "v_fmac_f32 v159, v198, v198\n"
      "v_mfma_f32_16x16x32_bf16 v[136:139], v[148:151], a[188:191], v[136:139]\n"
      "v_fmac_f32 v158, v174, v174\n"
      "v_fmac_f32_e64 v159, v199, v199\n"
      "v_mfma_f32_16x16x32_bf16 v[140:143], v[208:211], a[204:207], v[140:143]\n"
      "v_fmac_f32 v158, v175, v175\n"
      "v_fmac_f32 v159, v200, v200\n"
      "v_mfma_f32_16x16x32_bf16 v[140:143], v[164:167], a[208:211], v[140:143]\n"
      "v_fmac_f32 v158, v176, v176\n"
      "v_fmac_f32 v159, v201, v201\n"
      "v_mfma_f32_16x16x32_bf16 v[140:143], v[148:151], a[212:215], v[140:143]\n"
      "v_fmac_f32_e64 v158, v177, v177\n"
      "v_cvt_pk_bf16_f32 v135, v134, v135\n"
      "v_mfma_f32_16x16x32_bf16 v[108:111], v[190:193], v[202:205], v[40:43]\n"
      "ds_read_b64 v[172:173], v10 offset:1176\n"
      "v_permlane16_swap_b32 v159, v158\n"
      "v_mfma_f32_16x16x32_bf16 v[116:119], v[124:127], v[202:205], v[44:47]\n"
      "s_waitcnt lgkmcnt(1)\n"
      "v_add_f32 v159, v159, v158\n"
      "v_mov_b32 v158, v159\n"
      "ds_read_b128 v[208:211], v0 offset:18432\n"
      "v_cvt_pk_bf16_f32 v134, v132, v133\n"
      "v_permlane32_swap_b32 v159, v158\n"
      "v_add_f32 v159, v159, v158\n"
      "v_rsq_f32_e64 v159, v159\n"
      "ds_read_b128 v[130:133], v1 offset:18432\n"
      "v_mov_b32 v158, v159\n"
      "ds_read_b128 v[164:167], v0 offset:24576\n"
      "v_cvt_pk_bf16_f32 v76, v102, v103\n"
      "v_permlane16_swap_b32 v159, v158\n"
      "v_mul_f32 v160, v158, v160\n"
      "v_mul_f32 v161, v158, v161\n"
      "v_cvt_pk_bf16_f32 v160, v160, v161\n"
      "v_mul_f32 v162, v158, v162\n"
      "v_mul_f32 v163, v158, v163\n"
      "v_cvt_pk_bf16_f32 v161, v162, v163\n"
      "v_mul_f32 v96, v158, v96\n"
      "v_mul_f32 v97, v158, v97\n"
      "v_mul_f32 v98, v158, v98\n"
      "v_mul_f32 v99, v158, v99\n"
      "v_cvt_pk_bf16_f32 v162, v96, v97\n"
      "v_cvt_pk_bf16_f32 v163, v98, v99\n"
      "v_mul_f32 v174, v158, v174\n"
      "v_mul_f32 v175, v158, v175\n"
      "v_mfma_f32_16x16x32_bf16 v[96:99], v[160:163], v[202:205], v[48:51]\n"
      "s_waitcnt lgkmcnt(2)\n"
      "v_cvt_pk_bf16_f32 v174, v174, v175\n"
      "v_mul_f32 v176, v158, v176\n"
      "v_mfma_f32_16x16x32_bf16 v[100:103], v[120:123], v[202:205], v[24:27]\n"
      "s_waitcnt lgkmcnt(1)\n"
      "v_mul_f32 v177, v158, v177\n"
      "v_cvt_pk_bf16_f32 v175, v176, v177\n"
      "v_mfma_f32_16x16x16_bf16 v[108:111], v[194:195], v[156:157], v[108:111]\n"
      "v_mul_f32 v159, s50, v159\n"
      "v_mul_f32 v152, v159, v152\n"
      "v_mul_f32 v153, v159, v153\n"
      "v_mfma_f32_16x16x16_bf16 v[116:119], v[212:213], v[156:157], v[116:119]\n"
      "v_cvt_pk_bf16_f32 v152, v152, v153\n"
      "v_mul_f32 v154, v159, v154\n"
      "v_mul_f32 v155, v159, v155\n"
      "v_mfma_f32_16x16x16_bf16 v[96:99], v[174:175], v[156:157], v[96:99]\n"
      "v_mul_f32 v198, v159, v198\n"
      "v_mul_f32_e64 v199, v159, v199\n"
      "v_cvt_pk_bf16_f32 v153, v154, v155\n"
      "v_mfma_f32_16x16x16_bf16 v[100:103], v[172:173], v[156:157], v[100:103]\n"
      "v_mul_f32 v218, v159, v218\n"
      "v_mul_f32 v219, v159, v219\n"
      "v_mul_f32 v220, v159, v220\n"
      "v_mfma_f32_16x16x32_bf16 v[214:217], v[208:211], a[144:147], 0\n"
      "s_waitcnt lgkmcnt(0)\n"
      "v_mul_f32 v221, v159, v221\n"
      "v_cvt_pk_bf16_f32 v198, v198, v199\n"
      "v_mfma_f32_16x16x32_bf16 v[214:217], v[130:133], a[148:151], v[214:217]\n"
      "ds_read_b128 v[176:179], v1 offset:24576\n"
      "v_mul_f32 v200, v159, v200\n"
      "v_mfma_f32_16x16x32_bf16 v[214:217], v[164:167], a[152:155], v[214:217]\n"
      "ds_read_b128 v[202:205], v0 offset:30720\n"
      "v_mul_f32 v201, v159, v201\n"
      "v_mfma_f32_16x16x32_bf16 v[156:159], v[208:211], a[168:171], 0\n"
      "ds_read_b128 v[148:151], v1 offset:30720\n"
      "v_cvt_pk_bf16_f32 v154, v218, v219\n"
      "v_mfma_f32_16x16x32_bf16 v[156:159], v[130:133], a[172:175], v[156:159]\n"
      "s_cbranch_vccnz L_rare_34_%=\n"
      "L_back_35_%=:\n"
      "v_cvt_pk_bf16_f32 v155, v220, v221\n"
      "v_cvt_pk_bf16_f32 v199, v200, v201\n"
      "v_mfma_f32_16x16x32_bf16 v[156:159], v[164:167], a[176:179], v[156:159]\n"
      "v_exp_f32 v108, v108\n"
      "v_mfma_f32_16x16x32_bf16 v[218:221], v[208:211], a[192:195], 0\n"
      "v_exp_f32 v109, v109\n"
      "v_mfma_f32_16x16x32_bf16 v[218:221], v[130:133], a[196:199], v[218:221]\n"
      "s_waitcnt lgkmcnt(2)\n"
      "v_exp_f32 v110, v110\n"
      "v_mfma_f32_16x16x32_bf16 v[218:221], v[164:167], a[200:203], v[218:221]\n"
      "s_waitcnt lgkmcnt(1)\n"
      "v_exp_f32 v111, v111\n"
      "v_mfma_f32_16x16x32_bf16 v[214:217], v[176:179], a[156:159], v[214:217]\n"
      "s_waitcnt lgkmcnt(0)\n"
      "v_exp_f32 v116, v116\n"
      "v_mfma_f32_16x16x32_bf16 v[214:217], v[202:205], a[160:163], v[214:217]\n"
      "v_exp_f32_e64 v117, v117\n"
      "v_mfma_f32_16x16x32_bf16 v[214:217], v[148:151], a[164:167], v[214:217]\n"
      "v_exp_f32 v118, v118\n"
      "v_mfma_f32_16x16x32_bf16 v[156:159], v[176:179], a[180:183], v[156:159]\n"
      "v_exp_f32 v119, v119\n"
      "v_mfma_f32_16x16x32_bf16 v[156:159], v[202:205], a[184:187], v[156:159]\n"
      "ds_read_b128 v[128:131], v2\n"
      "v_cvt_pk_bf16_f32 v108, v108, v109\n"
      "v_mfma_f32_16x16x32_bf16 v[156:159], v[148:151], a[188:191], v[156:159]\n"
      "ds_read_b128 v[208:211], v2 offset:3072\n"
      "v_cvt_pk_bf16_f32 v109, v110, v111\n"
      "v_mfma_f32_16x16x32_bf16 v[218:221], v[176:179], a[204:207], v[218:221]\n"
      "v_cvt_pk_bf16_f32 v110, v116, v117\n"
      "v_cvt_pk_bf16_f32 v111, v118, v119\n"
      "v_mfma_f32_16x16x32_bf16 v[218:221], v[202:205], a[208:211], v[218:221]\n"
      "v_exp_f32_e64 v96, v96\n"
      "v_mfma_f32_16x16x32_bf16 v[218:221], v[148:151], a[212:215], v[218:221]\n"
      "v_exp_f32_e64 v97, v97\n"
      "v_mfma_f32_16x16x32_bf16 v[202:205], v[190:193], v[112:115], v[52:55]\n"
      "v_exp_f32 v98, v98\n"
      "v_mfma_f32_16x16x32_bf16 v[182:185], v[124:127], v[112:115], v[56:59]\n"
      "s_waitcnt lgkmcnt(1)\n"
      "v_exp_f32_e64 v99, v99\n"
      "v_mfma_f32_16x16x32_bf16 v[178:181], v[160:163], v[112:115], v[60:63]\n"
      "s_waitcnt lgkmcnt(0)\n"
      "v_exp_f32 v100, v100\n"
      "v_mfma_f32_16x16x32_bf16 v[116:119], v[120:123], v[112:115], v[26:29]\n"
      "ds_read_b128 v[112:115], v2 offset:6144\n"
      "v_cvt_pk_bf16_f32 v88, v96, v97\n"
      "v_mfma_f32_16x16x16_bf16 v[202:205], v[194:195], v[106:107], v[202:205]\n"
      "ds_read_b128 v[148:151], v2 offset:9216\n"
      "v_cvt_pk_bf16_f32 v89, v98, v99\n"
      "v_mfma_f32_16x16x16_bf16 v[182:185], v[212:213], v[106:107], v[182:185]\n"
      "v_cvt_pk_bf16_f32 v90, v100, 0\n"
      "v_cvt_pk_bf16_f32 v132, v214, v215\n"
      "v_cvt_pk_bf16_f32 v133, v216, v217\n"
      "v_mfma_f32_16x16x16_bf16 v[178:181], v[174:175], v[106:107], v[178:181]\n"
      "v_cvt_pk_bf16_f32 v77, v104, v105\n"
      "v_cvt_pk_bf16_f32 v139, v138, v139\n"
      "v_cvt_pk_bf16_f32 v138, v136, v137\n"
      "v_mfma_f32_16x16x16_bf16 v[116:119], v[172:173], v[106:107], v[116:119]\n"
      "v_cvt_pk_bf16_f32 v136, v156, v157\n"
      "v_cvt_pk_bf16_f32 v137, v158, v159\n"
      "v_cvt_pk_bf16_f32 v80, v144, v145\n"
      "v_mfma_f32_16x16x32_bf16 v[96:99], a[216:219], v[128:131], 0\n"
      "ds_read_u16 v78, v11 offset:1152\n"
      "v_cvt_pk_bf16_f32 v81, v146, v147\n"
      "v_mfma_f32_16x16x32_bf16 v[96:99], a[220:223], v[208:211], v[96:99]\n"
      "s_waitcnt lgkmcnt(2)\n"
      "v_cvt_pk_bf16_f32 v143, v142, v143\n"
      "v_cvt_pk_bf16_f32 v142, v140, v141\n"
      "v_mfma_f32_16x16x32_bf16 v[100:103], a[240:243], v[128:131], 0\n"
      "s_waitcnt lgkmcnt(1)\n"
      "v_cvt_pk_bf16_f32 v140, v218, v219\n"
      "v_cvt_pk_bf16_f32 v141, v220, v221\n"
      "v_mfma_f32_16x16x32_bf16 v[100:103], a[244:247], v[208:211], v[100:103]\n"
      "ds_read_u16 v82, v11 offset:1184\n"
      "v_cvt_pk_bf16_f32 v84, v186, v187\n"
      "v_mfma_f32_16x16x32_bf16 v[104:107], v[232:235], v[128:131], 0\n"
      "ds_read_u16 v86, v11 offset:1216\n"
      "v_cvt_pk_bf16_f32 v85, v188, v189\n"
      "v_mfma_f32_16x16x32_bf16 v[104:107], v[236:239], v[208:211], v[104:107]\n"
      "s_cbranch_vccnz L_rare_37_%=\n"
      "L_back_38_%=:\n"
      "v_exp_f32 v202, v202\n"
      "v_mfma_f32_16x16x32_bf16 v[96:99], a[224:227], v[112:115], v[96:99]\n"
      "s_waitcnt lgkmcnt(2)\n"
      "v_exp_f32 v203, v203\n"
      "v_mfma_f32_16x16x32_bf16 v[96:99], a[228:231], v[148:151], v[96:99]\n"
      "v_exp_f32 v204, v204\n"
      "v_mfma_f32_16x16x32_bf16 v[100:103], a[248:251], v[112:115], v[100:103]\n"
      "v_exp_f32 v205, v205\n"
      "v_mfma_f32_16x16x32_bf16 v[218:221], v[32:35], v[108:111], 0\n"
      "s_waitcnt lgkmcnt(1)\n"
      "v_exp_f32 v182, v182\n"
      "v_mfma_f32_16x16x32_bf16 v[218:221], v[32:35], v[88:91], v[218:221]\n"
      "s_waitcnt lgkmcnt(0)\n"
      "v_exp_f32 v183, v183\n"
      "v_mfma_f32_16x16x32_bf16 v[164:167], v[132:135], v[108:111], 0\n"
      "v_exp_f32 v184, v184\n"
      "v_mfma_f32_16x16x32_bf16 v[164:167], v[76:79], v[88:91], v[164:167]\n"
      "v_exp_f32 v185, v185\n"
      "v_mfma_f32_16x16x32_bf16 v[214:217], v[136:139], v[108:111], 0\n"
      "v_cvt_pk_bf16_f32 v202, v202, v203\n"
      "v_cvt_pk_bf16_f32 v203, v204, v205\n"
      "v_mfma_f32_16x16x32_bf16 v[214:217], v[80:83], v[88:91], v[214:217]\n"
      "v_cvt_pk_bf16_f32 v204, v182, v183\n"
      "v_cvt_pk_bf16_f32 v205, v184, v185\n"
      "v_mfma_f32_16x16x32_bf16 v[184:187], v[140:143], v[108:111], 0\n"
      "v_exp_f32 v178, v178\n"
      "v_mfma_f32_16x16x32_bf16 v[184:187], v[84:87], v[88:91], v[184:187]\n"
      "v_exp_f32 v179, v179\n"
      "v_mfma_f32_16x16x32_bf16 v[128:131], v[190:193], v[152:155], v[64:67]\n"
      "ds_read_b128 v[108:111], v2 offset:12288\n"
      "v_exp_f32 v180, v180\n"
      "v_mfma_f32_16x16x32_bf16 v[220:223], v[124:127], v[152:155], v[68:71]\n"
      "ds_read_b128 v[208:211], v2 offset:15360\n"
      "v_exp_f32 v181, v181\n"
      "v_mfma_f32_16x16x32_bf16 v[156:159], v[160:163], v[152:155], v[72:75]\n"
      "v_exp_f32 v116, v116\n"
      "v_mfma_f32_16x16x32_bf16 v[144:147], v[120:123], v[152:155], v[28:31]\n"
      "v_cvt_pk_bf16_f32 v92, v178, v179\n"
      "v_cvt_pk_bf16_f32 v93, v180, v181\n"
      "v_mfma_f32_16x16x16_bf16 v[128:131], v[194:195], v[198:199], v[128:131]\n"
      "v_cvt_pk_bf16_f32 v94, v116, 0\n"
      "v_rcp_f32 v218, v218\n"
      "v_mfma_f32_16x16x16_bf16 v[220:223], v[212:213], v[198:199], v[220:223]\n"
      "v_mul_f32 v164, v218, v164\n"
      "v_mul_f32 v165, v218, v165\n"
      "v_cvt_pk_bf16_f32 v164, v164, v165\n"
      "v_mfma_f32_16x16x16_bf16 v[156:159], v[174:175], v[198:199], v[156:159]\n"
      "v_mul_f32 v166, v218, v166\n"
      "v_mul_f32 v167, v218, v167\n"
      "v_cvt_pk_bf16_f32 v165, v166, v167\n"
      "v_mfma_f32_16x16x16_bf16 v[144:147], v[172:173], v[198:199], v[144:147]\n"
      "v_mul_f32_e64 v214, v218, v214\n"
      "ds_read_b128 v[152:155], v2 offset:256\n"
      "v_mfma_f32_16x16x32_bf16 v[100:103], a[252:255], v[148:151], v[100:103]\n"
      "s_waitcnt lgkmcnt(1)\n"
      "ds_read_b128 v[180:183], v2 offset:3328\n"
      "v_mfma_f32_16x16x32_bf16 v[104:107], v[240:243], v[112:115], v[104:107]\n"
      "v_mul_f32 v215, v218, v215\n"
      "v_mul_f32 v216, v218, v216\n"
      "v_mfma_f32_16x16x32_bf16 v[104:107], v[244:247], v[148:151], v[104:107]\n"
      "s_cbranch_vccnz L_rare_40_%=\n"
      "L_back_41_%=:\n"
      "v_exp_f32 v128, v128\n"
      "v_mfma_f32_16x16x32_bf16 v[96:99], a[232:235], v[108:111], v[96:99]\n"
      "v_exp_f32 v129, v129\n"
      "v_mfma_f32_16x16x32_bf16 v[96:99], a[236:239], v[208:211], v[96:99]\n"
      "ds_read_b128 v[176:179], v2 offset:6400\n"
      "v_exp_f32 v130, v130\n"
      "v_mfma_f32_16x16x32_bf16 v[100:103], v[224:227], v[108:111], v[100:103]\n"
      "s_waitcnt lgkmcnt(2)\n"
      "v_exp_f32 v131, v131\n"
      "v_mfma_f32_16x16x32_bf16 v[100:103], v[228:231], v[208:211], v[100:103]\n"
      "s_waitcnt lgkmcnt(1)\n"
      "v_exp_f32_e64 v220, v220\n"
      "v_mfma_f32_16x16x32_bf16 v[104:107], v[248:251], v[108:111], v[104:107]\n"
      "ds_read_b128 v[146:149], v2 offset:9472\n"
      "v_exp_f32 v221, v221\n"
      "v_mfma_f32_16x16x32_bf16 v[104:107], v[252:255], v[208:211], v[104:107]\n"
      "v_exp_f32 v222, v222\n"
      "v_mfma_f32_16x16x32_bf16 v[108:111], a[216:219], v[152:155], 0\n"
      "v_exp_f32 v223, v223\n"
      "v_mfma_f32_16x16x32_bf16 v[108:111], a[220:223], v[180:183], v[108:111]\n"
      "s_waitcnt lgkmcnt(1)\n"
      "v_cvt_pk_bf16_f32 v128, v128, v129\n"
      "v_cvt_pk_bf16_f32 v129, v130, v131\n"
      "v_mfma_f32_16x16x32_bf16 v[112:115], a[240:243], v[152:155], 0\n"
      "v_cvt_pk_bf16_f32 v130, v220, v221\n"
      "v_cvt_pk_bf16_f32 v131, v222, v223\n"
      "v_mfma_f32_16x16x32_bf16 v[112:115], a[244:247], v[180:183], v[112:115]\n"
      "v_exp_f32 v156, v156\n"
      "v_mfma_f32_16x16x32_bf16 v[116:119], v[232:235], v[152:155], 0\n"
      "s_waitcnt lgkmcnt(0)\n"
      "v_exp_f32 v157, v157\n"
      "v_mfma_f32_16x16x32_bf16 v[116:119], v[236:239], v[180:183], v[116:119]\n"
      "v_exp_f32 v158, v158\n"
      "v_mfma_f32_16x16x32_bf16 v[108:111], a[224:227], v[176:179], v[108:111]\n"
      "v_exp_f32 v159, v159\n"
      "v_mfma_f32_16x16x32_bf16 v[108:111], a[228:231], v[146:149], v[108:111]\n"
      "ds_read_b128 v[220:223], v10 offset:1152\n"
      "v_exp_f32 v144, v144\n"
      "v_mfma_f32_16x16x32_bf16 v[112:115], a[248:251], v[176:179], v[112:115]\n"
      "v_cvt_pk_bf16_f32 v88, v156, v157\n"
      "v_cvt_pk_bf16_f32 v89, v158, v159\n"
      "v_mfma_f32_16x16x32_bf16 v[150:153], v[32:35], v[202:205], 0\n"
      "v_cvt_pk_bf16_f32 v90, v144, 0\n"
      "v_mul_f32 v145, v96, v96\n"
      "v_mfma_f32_16x16x32_bf16 v[150:153], v[32:35], v[92:95], v[150:153]\n"
      "v_add_f32 v144, v96, v97\n"
      "v_fmac_f32_e64 v145, v97, v97\n"
      "v_mfma_f32_16x16x32_bf16 v[208:211], v[132:135], v[202:205], 0\n"
      "ds_read_b64 v[206:207], v10 offset:1168\n"
      "v_add_f32 v144, v144, v98\n"
      "v_mfma_f32_16x16x32_bf16 v[208:211], v[76:79], v[92:95], v[208:211]\n"
      "v_fmac_f32 v145, v98, v98\n"
      "v_add_f32 v144, v144, v99\n"
      "v_mfma_f32_16x16x32_bf16 v[152:155], v[136:139], v[202:205], 0\n"
      "s_waitcnt lgkmcnt(1)\n"
      "v_fmac_f32 v145, v99, v99\n"
      "v_add_f32 v144, v144, v100\n"
      "v_mfma_f32_16x16x32_bf16 v[152:155], v[80:83], v[92:95], v[152:155]\n"
      "v_fmac_f32 v145, v100, v100\n"
      "v_add_f32 v144, v144, v101\n"
      "v_mfma_f32_16x16x32_bf16 v[196:199], v[140:143], v[202:205], 0\n"
      "v_fmac_f32 v145, v101, v101\n"
      "v_add_f32 v144, v144, v102\n"
      "v_mfma_f32_16x16x32_bf16 v[196:199], v[84:87], v[92:95], v[196:199]\n"
      "v_fmac_f32 v145, v102, v102\n"
      "v_add_f32 v144, v144, v103\n"
      "v_mfma_f32_16x16x32_bf16 v[156:159], v[220:223], v[190:193], v[30:33]\n"
      "s_waitcnt lgkmcnt(0)\n"
      "v_fmac_f32 v145, v103, v103\n"
      "v_add_f32_e64 v144, v144, v104\n"
      "v_mfma_f32_16x16x32_bf16 v[188:191], v[220:223], v[124:127], v[30:33]\n"
      "ds_read_b128 v[124:127], v2 offset:12544\n"
      "v_fmac_f32_e64 v145, v104, v104\n"
      "v_mfma_f32_16x16x32_bf16 v[180:183], v[220:223], v[160:163], v[30:33]\n"
      "ds_read_b128 v[200:203], v2 offset:15616\n"
      "v_add_f32 v144, v144, v105\n"
      "v_mfma_f32_16x16x32_bf16 v[160:163], v[220:223], v[120:123], v[30:33]\n"
      "v_fmac_f32 v145, v105, v105\n"
      "v_add_f32_e64 v144, v144, v106\n"
      "v_mfma_f32_16x16x16_bf16 v[156:159], v[206:207], v[194:195], v[156:159]\n"
      "v_fmac_f32 v145, v106, v106\n"
      "v_add_f32 v144, v144, v107\n"
      "v_fmac_f32 v145, v107, v107\n"
      "v_mfma_f32_16x16x16_bf16 v[188:191], v[206:207], v[212:213], v[188:191]\n"
      "v_rcp_f32 v150, v150\n"
      "v_permlane16_swap_b32 v144, v145\n"
      "v_mfma_f32_16x16x16_bf16 v[180:183], v[206:207], v[174:175], v[180:183]\n"
      "v_mul_f32 v208, v150, v208\n"
      "v_mul_f32 v209, v150, v209\n"
      "v_add_f32 v144, v144, v145\n"
      "v_mfma_f32_16x16x16_bf16 v[160:163], v[206:207], v[172:173], v[160:163]\n"
      "v_cvt_pk_bf16_f32 v208, v208, v209\n"
      "v_cndmask_b32 v156, v156, v188, s[58:59]\n"
      "v_mul_f32 v210, v150, v210\n"
      "v_mfma_f32_16x16x32_bf16 v[112:115], a[252:255], v[146:149], v[112:115]\n"
      "s_waitcnt lgkmcnt(0)\n"
      "v_cndmask_b32 v156, v156, v180, s[60:61]\n"
      "v_mul_f32 v211, v150, v211\n"
      "v_mfma_f32_16x16x32_bf16 v[116:119], v[240:243], v[176:179], v[116:119]\n"
      "v_cndmask_b32 v156, v156, v160, s[62:63]\n"
      "v_mul_f32 v217, v218, v217\n"
      "v_mfma_f32_16x16x32_bf16 v[116:119], v[244:247], v[146:149], v[116:119]\n"
      "s_cbranch_vccnz L_rare_43_%=\n"
      "L_back_44_%=:\n"
      "v_exp_f32_e64 v156, v156\n"
      "v_mfma_f32_16x16x32_bf16 v[108:111], a[232:235], v[124:127], v[108:111]\n"
      "s_add_u32 s42, s42, 1\n"
      "v_cvt_pk_bf16_f32 v156, v156, v156\n"
      "v_cvt_pk_bf16_f32 v209, v210, v211\n"
      "v_mfma_f32_16x16x32_bf16 v[108:111], a[236:239], v[200:203], v[108:111]\n"
      "ds_write_b16 v13, v156\n"
      "v_mfma_f32_16x16x32_bf16 v[112:115], v[224:227], v[124:127], v[112:115]\n"
      "v_mul_f32 v152, v150, v152\n"
      "v_mul_f32 v153, v150, v153\n"
      "v_mfma_f32_16x16x32_bf16 v[112:115], v[228:231], v[200:203], v[112:115]\n"
      "v_mul_f32 v154, v150, v154\n"
      "v_mul_f32 v155, v150, v155\n"
      "v_mfma_f32_16x16x32_bf16 v[116:119], v[248:251], v[124:127], v[116:119]\n"
      "v_mul_f32 v94, v108, v108\n"
      "v_add_f32 v93, v108, v109\n"
      "v_mfma_f32_16x16x32_bf16 v[116:119], v[252:255], v[200:203], v[116:119]\n"
      "ds_read_b128 v[204:207], v2 offset:512\n"
      "v_fmac_f32 v94, v109, v109\n"
      "v_mfma_f32_16x16x32_bf16 v[172:175], v[32:35], v[128:131], 0\n"
      "ds_read_b128 v[146:149], v2 offset:3584\n"
      "v_add_f32 v93, v93, v110\n"
      "v_mfma_f32_16x16x32_bf16 v[172:175], v[32:35], v[88:91], v[172:175]\n"
      "v_fmac_f32 v94, v110, v110\n"
      "v_add_f32 v93, v93, v111\n"
      "v_mfma_f32_16x16x32_bf16 v[188:191], v[132:135], v[128:131], 0\n"
      "v_fmac_f32 v94, v111, v111\n"
      "v_add_f32 v93, v93, v112\n"
      "v_mfma_f32_16x16x32_bf16 v[188:191], v[76:79], v[88:91], v[188:191]\n"
      "v_fmac_f32 v94, v112, v112\n"
      "v_add_f32 v93, v93, v113\n"
      "v_mfma_f32_16x16x32_bf16 v[174:177], v[136:139], v[128:131], 0\n"
      "v_fmac_f32 v94, v113, v113\n"
      "v_add_f32 v93, v93, v114\n"
      "v_mfma_f32_16x16x32_bf16 v[174:177], v[80:83], v[88:91], v[174:177]\n"
      "s_waitcnt lgkmcnt(1)\n"
      "v_fmac_f32 v94, v114, v114\n"
      "v_add_f32 v93, v93, v115\n"
      "v_mfma_f32_16x16x32_bf16 v[192:195], v[140:143], v[128:131], 0\n"
      "s_waitcnt lgkmcnt(0)\n"
      "v_fmac_f32 v94, v115, v115\n"
      "v_add_f32 v93, v93, v116\n"
      "v_mfma_f32_16x16x32_bf16 v[192:195], v[84:87], v[88:91], v[192:195]\n"
      "ds_read_b128 v[220:223], v2 offset:6656\n"
      "v_fmac_f32_e64 v94, v116, v116\n"
      "v_mfma_f32_16x16x32_bf16 v[120:123], a[216:219], v[204:207], 0\n"
      "ds_read_b128 v[160:163], v2 offset:9728\n"
      "v_add_f32 v93, v93, v117\n"
      "v_mfma_f32_16x16x32_bf16 v[120:123], a[220:223], v[146:149], v[120:123]\n"
      "v_fmac_f32 v94, v117, v117\n"
      "v_add_f32_e64 v93, v93, v118\n"
      "v_mfma_f32_16x16x32_bf16 v[124:127], a[240:243], v[204:207], 0\n"
      "v_fmac_f32 v94, v118, v118\n"
      "v_add_f32 v93, v93, v119\n"
      "v_mfma_f32_16x16x32_bf16 v[124:127], a[244:247], v[146:149], v[124:127]\n"
      "v_fmac_f32 v94, v119, v119\n"
      "v_rcp_f32 v172, v172\n"
      "v_mfma_f32_16x16x32_bf16 v[128:131], v[232:235], v[204:207], 0\n"
      "ds_read_b128 v[200:203], v2 offset:12800\n"
      "v_permlane16_swap_b32 v93, v94\n"
      "v_mfma_f32_16x16x32_bf16 v[128:131], v[236:239], v[146:149], v[128:131]\n"
      "s_waitcnt lgkmcnt(2)\n"
      "v_mul_f32 v188, v172, v188\n"
      "v_mul_f32_e64 v189, v172, v189\n"
      "v_mfma_f32_16x16x32_bf16 v[120:123], a[224:227], v[220:223], v[120:123]\n"
      "s_waitcnt lgkmcnt(1)\n"
      "ds_read_b128 v[204:207], v2 offset:15872\n"
      "v_mfma_f32_16x16x32_bf16 v[120:123], a[228:231], v[160:163], v[120:123]\n"
      "v_add_f32_e64 v93, v93, v94\n"
      "v_cvt_pk_bf16_f32 v188, v188, v189\n"
      "v_mfma_f32_16x16x32_bf16 v[124:127], a[248:251], v[220:223], v[124:127]\n"
      "ds_read_b128 v[146:149], v14\n"
      "v_mul_f32 v190, v172, v190\n"
      "v_mfma_f32_16x16x32_bf16 v[124:127], a[252:255], v[160:163], v[124:127]\n"
      "ds_read_b128 v[178:181], v14 offset:64\n"
      "v_mul_f32 v191, v172, v191\n"
      "v_mfma_f32_16x16x32_bf16 v[128:131], v[240:243], v[220:223], v[128:131]\n"
      "s_waitcnt lgkmcnt(3)\n"
      "v_cvt_pk_bf16_f32 v189, v190, v191\n"
      "v_mul_f32 v174, v172, v174\n"
      "v_mfma_f32_16x16x32_bf16 v[128:131], v[244:247], v[160:163], v[128:131]\n"
      "v_mul_f32 v175, v172, v175\n"
      "v_mul_f32 v176, v172, v176\n"
      "v_mfma_f32_16x16x32_bf16 v[120:123], a[232:235], v[200:203], v[120:123]\n"
      "v_mul_f32 v177, v172, v177\n"
      "v_cvt_pk_bf16_f32 v166, v214, v215\n"
      "s_waitcnt lgkmcnt(2)\n"
      "v_mfma_f32_16x16x32_bf16 v[120:123], a[236:239], v[204:207], v[120:123]\n"
      "v_cvt_pk_bf16_f32 v167, v216, v217\n"
      "v_mul_f32 v184, v218, v184\n"
      "v_mfma_f32_16x16x32_bf16 v[124:127], v[224:227], v[200:203], v[124:127]\n"
      "s_waitcnt lgkmcnt(1)\n"
      "v_mul_f32 v185, v218, v185\n"
      "v_cvt_pk_bf16_f32 v210, v152, v153\n"
      "v_mfma_f32_16x16x32_bf16 v[124:127], v[228:231], v[204:207], v[124:127]\n"
      "s_waitcnt lgkmcnt(0)\n"
      "v_mul_f32 v89, v120, v120\n"
      "v_add_f32 v88, v120, v121\n"
      "v_mfma_f32_16x16x32_bf16 v[128:131], v[248:251], v[200:203], v[128:131]\n"
      "ds_write_b128 v3, v[164:167] offset:18432\n"
      "v_mfma_f32_16x16x32_bf16 v[128:131], v[252:255], v[204:207], v[128:131]\n"
      "v_fmac_f32 v89, v121, v121\n"
      "v_add_f32 v88, v88, v122\n"
      "v_mfma_f32_16x16x32_bf16 v[158:161], v[146:149], v[32:35], 0\n"
      "v_fmac_f32 v89, v122, v122\n"
      "v_add_f32 v88, v88, v123\n"
      "v_mfma_f32_16x16x32_bf16 v[158:161], v[178:181], v[32:35], v[158:161]\n"
      "v_fmac_f32 v89, v123, v123\n"
      "v_add_f32 v88, v88, v124\n"
      "v_mfma_f32_16x16x32_bf16 v[212:215], v[146:149], v[132:135], 0\n"
      "v_fmac_f32 v89, v124, v124\n"
      "v_add_f32 v88, v88, v125\n"
      "v_mfma_f32_16x16x32_bf16 v[212:215], v[178:181], v[76:79], v[212:215]\n"
      "v_fmac_f32 v89, v125, v125\n"
      "v_add_f32 v88, v88, v126\n"
      "v_mfma_f32_16x16x32_bf16 v[132:135], v[146:149], v[136:139], 0\n"
      "v_fmac_f32 v89, v126, v126\n"
      "v_add_f32 v88, v88, v127\n"
      "v_mfma_f32_16x16x32_bf16 v[132:135], v[178:181], v[80:83], v[132:135]\n"
      "v_fmac_f32 v89, v127, v127\n"
      "v_add_f32 v88, v88, v128\n"
      "v_mfma_f32_16x16x32_bf16 v[136:139], v[146:149], v[140:143], 0\n"
      "v_fmac_f32 v89, v128, v128\n"
      "v_add_f32 v88, v88, v129\n"
      "v_mfma_f32_16x16x32_bf16 v[136:139], v[178:181], v[84:87], v[136:139]\n"
      "v_fmac_f32 v89, v129, v129\n"
      "v_add_f32 v88, v88, v130\n"
      "v_fmac_f32 v89, v130, v130\n"
      "v_add_f32 v88, v88, v131\n"
      "v_fmac_f32 v89, v131, v131\n"
      "v_mov_b32_dpp v158, v158 quad_perm:[0,0,0,0] row_mask:0xf bank_mask:0xf\n"
      "v_rcp_f32 v158, v158\n"
      "v_permlane16_swap_b32 v88, v89\n"
      "v_add_f32 v88, v88, v89\n"
      "v_mov_b32 v89, v88\n"
      "v_cvt_pk_bf16_f32 v211, v154, v155\n"
      "v_mul_f32 v196, v150, v196\n"
      "v_mul_f32_e64 v197, v150, v197\n"
      "v_cvt_pk_bf16_f32 v190, v174, v175\n"
      "v_cvt_pk_bf16_f32 v191, v176, v177\n"
      "v_mul_f32 v192, v172, v192\n"
      "v_mul_f32 v193, v172, v193\n"
      "v_cndmask_b32 v212, v212, v132, s[58:59]\n"
      "ds_write_b128 v3, v[208:211] offset:18688\n"
      "ds_write_b128 v3, v[188:191] offset:18944\n"
      "v_cvt_pk_bf16_f32 v184, v184, v185\n"
      "v_mul_f32 v186, v218, v186\n"
      "v_mul_f32 v187, v218, v187\n"
      "v_cvt_pk_bf16_f32 v196, v196, v197\n"
      "v_mul_f32 v198, v150, v198\n"
      "v_mul_f32 v199, v150, v199\n"
      "v_permlane32_swap_b32 v144, v93\n"
      "v_cvt_pk_bf16_f32 v192, v192, v193\n"
      "v_mul_f32 v194, v172, v194\n"
      "v_mul_f32_e64 v195, v172, v195\n"
      "v_permlane32_swap_b32 v88, v89\n"
      "v_cndmask_b32 v212, v212, v136, s[60:61]\n"
      "s_waitcnt vmcnt(11)\n"
      "v_or3_b32 v168, v168, v169, v170\n"
      "v_cvt_pk_bf16_f32 v185, v186, v187\n"
      "v_cvt_pk_bf16_f32 v197, v198, v199\n"
      "v_cvt_pk_bf16_f32 v193, v194, v195\n"
      "v_mul_f32 v212, v158, v212\n"
      "s_waitcnt vmcnt(10)\n"
      "v_or3_b32 v168, v168, v171, v39\n"
      "ds_write_b64 v4, v[184:185] offset:18432\n"
      "ds_write_b64 v4, v[196:197] offset:18688\n"
      "ds_write_b64 v4, v[192:193] offset:18944\n"
      "v_add_f32 v144, v144, v93\n"
      "v_add_f32 v88, v88, v89\n"
      "v_cvt_pk_bf16_f32 v212, v212, v212\n"
      "v_and_b32 v168, 0x7fff7fff, v168\n"
      "ds_write_b32 v8, v144 offset:2048\n"
      "ds_write_b32 v8, v88 offset:2560\n"
      "ds_write_b16 v12, v212 offset:16\n"
      "ds_write_b32 v31, v168\n"
      "v_add_u32 v10, 2304, v10\n"
      "v_add_u32 v11, 2304, v11\n"
      "v_add_u32 v12, 32, v12\n"
      "s_waitcnt vmcnt(5)\n"
      "s_waitcnt lgkmcnt(0)\n"
      "s_barrier\n"
      "s_cmp_gt_u32 s42, s44\n"
      "s_cbranch_scc1 L_tailB_%=\n"
      "ds_read_b128 v[140:143], v9 offset:4096\n"
      "ds_read_b128 v[132:135], v9 offset:2048\n"
      "ds_read_b128 v[156:159], v9 offset:3072\n"
      "ds_read_b128 v[146:149], v0\n"
      "ds_read_b128 v[152:155], v1\n"
      "ds_read_b128 v[196:199], v0 offset:6144\n"
      "v_readlane_b32 s49, v38, s43\n"
      "ds_read_b128 v[136:139], v1 offset:6144\n"
      "v_readlane_b32 s45, v37, s43\n"
      "v_readlane_b32 s47, v37, s42\n"
      "ds_read_b128 v[166:169], v0 offset:12288\n"
      "ds_read_b128 v[182:185], v1 offset:12288\n"
      "s_and_b32 s49, s49, -4\n"
      "s_mov_b32 s46, s45\n"
      "s_cmp_ge_u32 s45, 0x40000000\n"
      "s_cbranch_scc1 L_rare_47_%=\n"
      "L_back_48_%=:\n"
      "s_waitcnt lgkmcnt(8)\n"
      "v_or3_b32 v140, v140, v141, v142\n"
      "v_or_b32 v140, v140, v143\n"
      "s_waitcnt lgkmcnt(7)\n"
      "v_add_f32 v132, v132, v134\n"
      "v_add_f32 v133, v133, v135\n"
      "v_cmp_ne_u32 s[56:57], 0, v140\n"
      "s_waitcnt lgkmcnt(6)\n"
      "v_add_f32 v156, v156, v158\n"
      "v_add_f32 v157, v157, v159\n"
      "v_add_f32 v132, v132, v156\n"
      "s_waitcnt lgkmcnt(5)\n"
      "v_mfma_f32_16x16x32_bf16 v[178:181], a[0:3], v[146:149], 0\n"
      "s_cmp_lg_u64 s[56:57], 0\n"
      "s_cbranch_scc1 L_rare_45_%=\n"
      "L_back_46_%=:\n"
      "v_add_f32 v133, v133, v157\n"
      "s_waitcnt lgkmcnt(4)\n"
      "v_mfma_f32_16x16x32_bf16 v[178:181], a[4:7], v[152:155], v[178:181]\n"
      "buffer_load_dwordx4 v[220:223], v27, s[24:27], s49 offen\n"
      "v_fma_f32 v156, -v132, v132, s52\n"
      "s_waitcnt lgkmcnt(3)\n"
      "v_mfma_f32_16x16x32_bf16 v[178:181], a[8:11], v[196:199], v[178:181]\n"
      "s_waitcnt lgkmcnt(2)\n"
      "v_fmac_f32 v156, 0x43400000, v133\n"
      "v_max_f32 v156, s52, v156\n"
      "v_mfma_f32_16x16x32_bf16 v[178:181], a[12:15], v[136:139], v[178:181]\n"
      "s_waitcnt lgkmcnt(1)\n"
      "v_rsq_f32 v156, v156\n"
      "v_mfma_f32_16x16x32_bf16 v[178:181], a[16:19], v[166:169], v[178:181]\n"
      "s_waitcnt lgkmcnt(0)\n"
      "v_mul_f32 v140, 0x43400000, v156\n"
      "v_mul_f32 v143, -v132, v156\n"
      "v_mfma_f32_16x16x32_bf16 v[178:181], a[20:23], v[182:185], v[178:181]\n"
      "buffer_load_dword v39, v29, s[24:27], s49 offen\n"
      "v_mov_b32 v76, v140\n"
      "v_mfma_f32_16x16x32_bf16 v[132:135], a[24:27], v[146:149], 0\n"
      "s_add_u32 m0, s64, 18432\n"
      "v_mov_b32 v156, v143\n"
      "v_permlane32_swap_b32 v140, v76\n"
      "v_mfma_f32_16x16x32_bf16 v[132:135], a[28:31], v[152:155], v[132:135]\n"
      "buffer_load_dwordx4 v16, s[16:19], s46 offen  lds\n"
      "v_permlane32_swap_b32 v143, v156\n"
      "v_mfma_f32_16x16x32_bf16 v[132:135], a[32:35], v[196:199], v[132:135]\n"
      "s_add_u32 m0, s64, 22528\n"
      "v_mov_b32 v80, v140\n"
      "v_mov_b32 v157, v143\n"
      "v_mfma_f32_16x16x32_bf16 v[132:135], a[36:39], v[136:139], v[132:135]\n"
      "buffer_load_dwordx4 v17, s[16:19], s46 offen  lds\n"
      "v_permlane16_swap_b32_e64 v140, v80\n"
      "v_mfma_f32_16x16x32_bf16 v[132:135], a[40:43], v[166:169], v[132:135]\n"
      "s_add_u32 m0, s64, 26624\n"
      "v_permlane16_swap_b32_e64 v143, v157\n"
      "v_mfma_f32_16x16x32_bf16 v[132:135], a[44:47], v[182:185], v[132:135]\n"
      "buffer_load_dwordx4 v18, s[16:19], s46 offen  lds\n"
      "v_fma_f32 v96, v96, v140, v143\n"
      "v_mfma_f32_16x16x32_bf16 v[158:161], a[48:51], v[146:149], 0\n"
      "s_add_u32 m0, s64, 30720\n"
      "v_fma_f32 v97, v97, v140, v143\n"
      "v_cvt_pk_bf16_f32 v96, v96, v97\n"
      "v_mfma_f32_16x16x32_bf16 v[158:161], a[52:55], v[152:155], v[158:161]\n"
      "buffer_load_dwordx4 v19, s[16:19], s46 offen  lds\n"
      "v_fma_f32 v98, v98, v140, v143\n"
      "v_mfma_f32_16x16x32_bf16 v[158:161], a[56:59], v[196:199], v[158:161]\n"
      "s_mov_b32 m0, s66\n"
      "v_fma_f32 v99, v99, v140, v143\n"
      "v_fma_f32 v104, v104, v140, v143\n"
      "v_mfma_f32_16x16x32_bf16 v[158:161], a[60:63], v[136:139], v[158:161]\n"
      "buffer_load_dwordx4 v15, s[16:19], s46 offen  lds\n"
      "v_fma_f32 v105, v105, v140, v143\n"
      "v_mfma_f32_16x16x32_bf16 v[158:161], a[64:67], v[166:169], v[158:161]\n"
      "s_mov_b32 s48, s47\n"
      "v_cvt_pk_bf16_f32 v97, v98, v99\n"
      "v_fma_f32 v100, v100, v140, v143\n"
      "v_mfma_f32_16x16x32_bf16 v[158:161], a[68:71], v[182:185], v[158:161]\n"
      "s_cmp_ge_u32 s47, 0x40000000\n"
      "s_cbranch_scc1 L_rare_51_%=\n"
      "L_back_52_%=:\n"
      "v_fma_f32 v101, v101, v140, v143\n"
      "v_mfma_f32_16x16x32_bf16 v[212:215], a[72:75], v[146:149], 0\n"
      "s_or_b64 vcc, s[56:57], s[54:55]\n"
      "v_fma_f32 v102, v102, v140, v143\n"
      "v_fma_f32 v103, v103, v140, v143\n"
      "v_mfma_f32_16x16x32_bf16 v[212:215], a[76:79], v[152:155], v[212:215]\n"
      "s_cmp_ge_u32 s45, 0x40000000\n"
      "s_cbranch_scc1 L_rare_49_%=\n"
      "L_back_50_%=:\n"
      "v_cvt_pk_bf16_f32 v104, v104, v105\n"
      "v_mfma_f32_16x16x32_bf16 v[212:215], a[80:83], v[196:199], v[212:215]\n"
      "s_add_u32 s43, s43, 1\n"
      "v_fma_f32 v106, v106, v140, v143\n"
      "v_fma_f32 v107, v107, v140, v143\n"
      "v_mfma_f32_16x16x32_bf16 v[212:215], a[84:87], v[136:139], v[212:215]\n"
      "v_cvt_pk_bf16_f32 v98, v100, v101\n"
      "v_cvt_pk_bf16_f32 v99, v102, v103\n"
      "v_mfma_f32_16x16x32_bf16 v[212:215], a[88:91], v[166:169], v[212:215]\n"
      "ds_write_b128 v5, v[96:99]\n"
      "v_mfma_f32_16x16x32_bf16 v[212:215], a[92:95], v[182:185], v[212:215]\n"
      "v_cvt_pk_bf16_f32 v105, v106, v107\n"
      "ds_write_b64 v6, v[104:105]\n"
      "v_mfma_f32_16x16x32_bf16 v[140:143], a[96:99], v[146:149], 0\n"
      "ds_read_b128 v[98:101], v0 offset:2048\n"
      "v_fma_f32 v108, v108, v80, v157\n"
      "v_mfma_f32_16x16x32_bf16 v[140:143], a[100:103], v[152:155], v[140:143]\n"
      "ds_read_b128 v[162:165], v1 offset:2048\n"
      "v_fma_f32 v109, v109, v80, v157\n"
      "v_mfma_f32_16x16x32_bf16 v[140:143], a[104:107], v[196:199], v[140:143]\n"
      "ds_read_b128 v[170:173], v0 offset:8192\n"
      "v_cvt_pk_bf16_f32 v108, v108, v109\n"
      "v_mfma_f32_16x16x32_bf16 v[140:143], a[108:111], v[136:139], v[140:143]\n"
      "v_fma_f32 v110, v110, v80, v157\n"
      "v_fma_f32 v111, v111, v80, v157\n"
      "v_mfma_f32_16x16x32_bf16 v[140:143], a[112:115], v[166:169], v[140:143]\n"
      "ds_read_b128 v[102:105], v1 offset:8192\n"
      "v_fma_f32 v116, v116, v80, v157\n"
      "v_mfma_f32_16x16x32_bf16 v[140:143], a[116:119], v[182:185], v[140:143]\n"
      "ds_read_b128 v[174:177], v0 offset:14336\n"
      "v_fma_f32 v117, v117, v80, v157\n"
      "v_mfma_f32_16x16x32_bf16 v[204:207], a[120:123], v[146:149], 0\n"
      "ds_read_b128 v[146:149], v1 offset:14336\n"
      "v_cvt_pk_bf16_f32 v109, v110, v111\n"
      "v_mfma_f32_16x16x32_bf16 v[204:207], a[124:127], v[152:155], v[204:207]\n"
      "s_waitcnt lgkmcnt(5)\n"
      "v_fma_f32 v112, v112, v80, v157\n"
      "v_fma_f32 v113, v113, v80, v157\n"
      "v_mfma_f32_16x16x32_bf16 v[204:207], a[128:131], v[196:199], v[204:207]\n"
      "s_waitcnt lgkmcnt(4)\n"
      "v_fma_f32 v114, v114, v80, v157\n"
      "v_fma_f32 v115, v115, v80, v157\n"
      "v_mfma_f32_16x16x32_bf16 v[204:207], a[132:135], v[136:139], v[204:207]\n"
      "s_waitcnt lgkmcnt(3)\n"
      "v_cvt_pk_bf16_f32 v116, v116, v117\n"
      "v_fma_f32 v118, v118, v80, v157\n"
      "v_mfma_f32_16x16x32_bf16 v[204:207], a[136:139], v[166:169], v[204:207]\n"
      "s_waitcnt lgkmcnt(2)\n"
      "v_fma_f32 v119, v119, v80, v157\n"
      "v_cvt_pk_bf16_f32 v110, v112, v113\n"
      "v_mfma_f32_16x16x32_bf16 v[204:207], a[140:143], v[182:185], v[204:207]\n"
      "s_waitcnt lgkmcnt(1)\n"
      "v_cvt_pk_bf16_f32 v111, v114, v115\n"
      "v_cvt_pk_bf16_f32 v117, v118, v119\n"
      "v_mfma_f32_16x16x32_bf16 v[112:115], a[0:3], v[98:101], 0\n"
      "s_waitcnt lgkmcnt(0)\n"
      "ds_write_b128 v5, v[108:111] offset:6144\n"
      "v_mfma_f32_16x16x32_bf16 v[112:115], a[4:7], v[162:165], v[112:115]\n"
      "ds_write_b64 v6, v[116:117] offset:6144\n"
      "v_mfma_f32_16x16x32_bf16 v[112:115], a[8:11], v[170:173], v[112:115]\n"
      "v_fma_f32 v120, v120, v76, v156\n"
      "v_fma_f32 v121, v121, v76, v156\n"
      "v_mfma_f32_16x16x32_bf16 v[112:115], a[12:15], v[102:105], v[112:115]\n"
      "v_cvt_pk_bf16_f32 v120, v120, v121\n"
      "v_fma_f32 v122, v122, v76, v156\n"
      "v_mfma_f32_16x16x32_bf16 v[112:115], a[16:19], v[174:177], v[112:115]\n"
      "v_fma_f32 v123, v123, v76, v156\n"
      "v_fma_f32 v128, v128, v76, v156\n"
      "v_mfma_f32_16x16x32_bf16 v[112:115], a[20:23], v[146:149], v[112:115]\n"
      "v_fma_f32 v129, v129, v76, v156\n"
      "v_cvt_pk_bf16_f32 v121, v122, v123\n"
      "v_mfma_f32_16x16x32_bf16 v[106:109], a[24:27], v[98:101], 0\n"
      "v_fma_f32 v124, v124, v76, v156\n"
      "v_fma_f32 v125, v125, v76, v156\n"
      "v_mfma_f32_16x16x32_bf16 v[106:109], a[28:31], v[162:165], v[106:109]\n"
      "v_fma_f32 v126, v126, v76, v156\n"
      "v_fma_f32 v127, v127, v76, v156\n"
      "v_mfma_f32_16x16x32_bf16 v[106:109], a[32:35], v[170:173], v[106:109]\n"
      "v_cvt_pk_bf16_f32 v128, v128, v129\n"
      "v_fma_f32 v130, v130, v76, v156\n"
      "v_mfma_f32_16x16x32_bf16 v[106:109], a[36:39], v[102:105], v[106:109]\n"
      "v_fma_f32 v131, v131, v76, v156\n"
      "v_cvt_pk_bf16_f32 v122, v124, v125\n"
      "v_mfma_f32_16x16x32_bf16 v[106:109], a[40:43], v[174:177], v[106:109]\n"
      "v_cvt_pk_bf16_f32 v123, v126, v127\n"
      "v_cvt_pk_bf16_f32 v129, v130, v131\n"
      "v_mfma_f32_16x16x32_bf16 v[106:109], a[44:47], v[146:149], v[106:109]\n"
      "ds_write_b128 v5, v[120:123] offset:12288\n"
      "v_mfma_f32_16x16x32_bf16 v[116:119], a[48:51], v[98:101], 0\n"
      "ds_write_b64 v6, v[128:129] offset:12288\n"
      "v_mfma_f32_16x16x32_bf16 v[116:119], a[52:55], v[162:165], v[116:119]\n"
      "v_fma_f32 v110, v178, v178, s51\n"
      "v_fma_f32 v111, v212, v212, s51\n"
      "v_mfma_f32_16x16x32_bf16 v[116:119], a[56:59], v[170:173], v[116:119]\n"
      "v_fmac_f32 v110, v179, v179\n"
      "v_fmac_f32 v111, v213, v213\n"
      "v_mfma_f32_16x16x32_bf16 v[116:119], a[60:63], v[102:105], v[116:119]\n"
      "v_fmac_f32 v110, v180, v180\n"
      "v_fmac_f32 v111, v214, v214\n"
      "v_mfma_f32_16x16x32_bf16 v[116:119], a[64:67], v[174:177], v[116:119]\n"
      "v_fmac_f32 v110, v181, v181\n"
      "v_fmac_f32 v111, v215, v215\n"
      "v_mfma_f32_16x16x32_bf16 v[116:119], a[68:71], v[146:149], v[116:119]\n"
      "v_fmac_f32 v110, v132, v132\n"
      "v_fmac_f32 v111, v140, v140\n"
      "v_mfma_f32_16x16x32_bf16 v[124:127], a[72:75], v[98:101], 0\n"
      "v_fmac_f32 v110, v133, v133\n"
      "v_fmac_f32 v111, v141, v141\n"
      "v_mfma_f32_16x16x32_bf16 v[124:127], a[76:79], v[162:165], v[124:127]\n"
      "v_fmac_f32 v110, v134, v134\n"
      "v_fmac_f32 v111, v142, v142\n"
      "v_mfma_f32_16x16x32_bf16 v[124:127], a[80:83], v[170:173], v[124:127]\n"
      "v_fmac_f32 v110, v135, v135\n"
      "v_fmac_f32 v111, v143, v143\n"
      "v_mfma_f32_16x16x32_bf16 v[124:127], a[84:87], v[102:105], v[124:127]\n"
      "v_fmac_f32 v110, v158, v158\n"
      "v_fmac_f32 v111, v204, v204\n"
      "v_mfma_f32_16x16x32_bf16 v[124:127], a[88:91], v[174:177], v[124:127]\n"
      "v_fmac_f32 v110, v159, v159\n"
      "v_fmac_f32 v111, v205, v205\n"
      "v_mfma_f32_16x16x32_bf16 v[124:127], a[92:95], v[146:149], v[124:127]\n"
      "v_fmac_f32 v110, v160, v160\n"
      "v_fmac_f32 v111, v206, v206\n"
      "v_mfma_f32_16x16x32_bf16 v[120:123], a[96:99], v[98:101], 0\n"
      "v_fmac_f32 v110, v161, v161\n"
      "v_fmac_f32 v111, v207, v207\n"
      "v_mfma_f32_16x16x32_bf16 v[120:123], a[100:103], v[162:165], v[120:123]\n"
      "ds_read_b128 v[136:139], v0 offset:4096\n"
      "v_permlane16_swap_b32_e64 v110, v111\n"
      "v_mfma_f32_16x16x32_bf16 v[120:123], a[104:107], v[170:173], v[120:123]\n"
      "ds_read_b128 v[128:131], v1 offset:4096\n"
      "v_add_f32 v110, v110, v111\n"
      "v_mfma_f32_16x16x32_bf16 v[120:123], a[108:111], v[102:105], v[120:123]\n"
      "ds_read_b128 v[182:185], v0 offset:10240\n"
      "v_mov_b32 v111, v110\n"
      "v_mfma_f32_16x16x32_bf16 v[120:123], a[112:115], v[174:177], v[120:123]\n"
      "ds_read_b128 v[166:169], v1 offset:10240\n"
      "v_permlane32_swap_b32_e64 v110, v111\n"
      "v_mfma_f32_16x16x32_bf16 v[120:123], a[116:119], v[146:149], v[120:123]\n"
      "ds_read_b128 v[186:189], v0 offset:16384\n"
      "v_add_f32 v110, v110, v111\n"
      "v_mfma_f32_16x16x32_bf16 v[152:155], a[120:123], v[98:101], 0\n"
      "ds_read_b128 v[208:211], v7 offset:18432\n"
      "v_rsq_f32 v110, v110\n"
      "v_mfma_f32_16x16x32_bf16 v[152:155], a[124:127], v[162:165], v[152:155]\n"
      "s_waitcnt lgkmcnt(5)\n"
      "v_mov_b32 v111, v110\n"
      "ds_read_b128 v[98:101], v1 offset:16384\n"
      "v_mfma_f32_16x16x32_bf16 v[152:155], a[128:131], v[170:173], v[152:155]\n"
      "s_waitcnt lgkmcnt(5)\n"
      "v_permlane16_swap_b32 v110, v111\n"
      "v_mfma_f32_16x16x32_bf16 v[152:155], a[132:135], v[102:105], v[152:155]\n"
      "s_waitcnt lgkmcnt(4)\n"
      "v_mul_f32 v110, s50, v110\n"
      "v_mul_f32_e64 v212, v111, v212\n"
      "v_mfma_f32_16x16x32_bf16 v[152:155], a[136:139], v[174:177], v[152:155]\n"
      "s_waitcnt lgkmcnt(3)\n"
      "v_mul_f32 v213, v111, v213\n"
      "v_mul_f32 v178, v110, v178\n"
      "v_mfma_f32_16x16x32_bf16 v[152:155], a[140:143], v[146:149], v[152:155]\n"
      "s_waitcnt lgkmcnt(2)\n"
      "v_mul_f32 v179, v110, v179\n"
      "v_cvt_pk_bf16_f32 v212, v212, v213\n"
      "v_mfma_f32_16x16x32_bf16 v[194:197], a[0:3], v[136:139], 0\n"
      "s_waitcnt lgkmcnt(1)\n"
      "v_mul_f32 v214, v111, v214\n"
      "v_mul_f32 v215, v111, v215\n"
      "v_mfma_f32_16x16x32_bf16 v[194:197], a[4:7], v[128:131], v[194:197]\n"
      "ds_read_b128 v[144:147], v7 offset:22528\n"
      "v_mul_f32 v204, v111, v204\n"
      "v_mfma_f32_16x16x32_bf16 v[194:197], a[8:11], v[182:185], v[194:197]\n"
      "s_waitcnt lgkmcnt(1)\n"
      "v_mul_f32 v205, v111, v205\n"
      "v_cvt_pk_bf16_f32 v178, v178, v179\n"
      "v_mfma_f32_16x16x32_bf16 v[194:197], a[12:15], v[166:169], v[194:197]\n"
      "ds_read_b128 v[172:175], v7 offset:26624\n"
      "v_mul_f32 v180, v110, v180\n"
      "v_mfma_f32_16x16x32_bf16 v[194:197], a[16:19], v[186:189], v[194:197]\n"
      "ds_read_b128 v[102:105], v7 offset:30720\n"
      "v_mul_f32_e64 v181, v110, v181\n"
      "v_mfma_f32_16x16x32_bf16 v[194:197], a[20:23], v[98:101], v[194:197]\n"
      "ds_read_b128 v[216:219], v7 offset:34816\n"
      "v_mul_f32 v158, v110, v158\n"
      "v_mfma_f32_16x16x32_bf16 v[198:201], a[24:27], v[136:139], 0\n"
      "buffer_store_dwordx4 v[208:211], v20, s[20:23], s48 offen sc1\n"
      "v_mul_f32 v159, v110, v159\n"
      "v_mfma_f32_16x16x32_bf16 v[198:201], a[28:31], v[128:131], v[198:201]\n"
      "s_waitcnt lgkmcnt(3)\n"
      "v_cvt_pk_bf16_f32 v213, v214, v215\n"
      "v_mul_f32 v140, v111, v140\n"
      "v_mfma_f32_16x16x32_bf16 v[198:201], a[32:35], v[182:185], v[198:201]\n"
      "buffer_store_dwordx4 v[144:147], v21, s[20:23], s48 offen sc1\n"
      "v_mul_f32 v141, v111, v141\n"
      "v_mfma_f32_16x16x32_bf16 v[198:201], a[36:39], v[166:169], v[198:201]\n"
      "s_waitcnt lgkmcnt(2)\n"
      "v_mul_f32 v142, v111, v142\n"
      "v_mul_f32 v143, v111, v143\n"
      "v_mfma_f32_16x16x32_bf16 v[198:201], a[40:43], v[186:189], v[198:201]\n"
      "s_waitcnt lgkmcnt(1)\n"
      "v_cvt_pk_bf16_f32 v204, v204, v205\n"
      "v_mul_f32 v206, v111, v206\n"
      "v_mfma_f32_16x16x32_bf16 v[198:201], a[44:47], v[98:101], v[198:201]\n"
      "s_waitcnt lgkmcnt(0)\n"
      "v_mul_f32 v207, v111, v207\n"
      "v_cvt_pk_bf16_f32 v179, v180, v181\n"
      "v_mfma_f32_16x16x32_bf16 v[162:165], a[48:51], v[136:139], 0\n"
      "buffer_store_dwordx4 v[172:175], v22, s[20:23], s48 offen sc1\n"
      "v_mul_f32 v132, v110, v132\n"
      "v_mfma_f32_16x16x32_bf16 v[162:165], a[52:55], v[128:131], v[162:165]\n"
      "buffer_store_dwordx4 v[102:105], v23, s[20:23], s48 offen sc1\n"
      "v_mul_f32 v133, v110, v133\n"
      "v_mfma_f32_16x16x32_bf16 v[162:165], a[56:59], v[182:185], v[162:165]\n"
      "buffer_store_dwordx4 v[216:219], v25, s[20:23], s48 offen sc1\n"
      "v_mul_f32 v134, v110, v134\n"
      "v_mfma_f32_16x16x32_bf16 v[162:165], a[60:63], v[166:169], v[162:165]\n"
      "s_cmp_ge_u32 s47, 0x40000000\n"
      "s_cbranch_scc1 L_rare_53_%=\n"
      "L_back_54_%=:\n"
      "v_mul_f32 v135, v110, v135\n"
      "v_mfma_f32_16x16x32_bf16 v[162:165], a[64:67], v[186:189], v[162:165]\n"
      "v_cvt_pk_bf16_f32 v158, v158, v159\n"
      "v_mul_f32 v160, v110, v160\n"
      "v_mfma_f32_16x16x32_bf16 v[162:165], a[68:71], v[98:101], v[162:165]\n"
      "v_mul_f32 v161, v110, v161\n"
      "v_cvt_pk_bf16_f32 v214, v140, v141\n"
      "v_mfma_f32_16x16x32_bf16 v[170:173], a[72:75], v[136:139], 0\n"
      "v_cvt_pk_bf16_f32 v215, v142, v143\n"
      "v_cvt_pk_bf16_f32 v205, v206, v207\n"
      "v_mfma_f32_16x16x32_bf16 v[170:173], a[76:79], v[128:131], v[170:173]\n"
      "v_cvt_pk_bf16_f32 v180, v132, v133\n"
      "v_cvt_pk_bf16_f32 v181, v134, v135\n"
      "v_mfma_f32_16x16x32_bf16 v[170:173], a[80:83], v[182:185], v[170:173]\n"
      "v_cvt_pk_bf16_f32 v159, v160, v161\n"
      "v_fma_f32 v142, v112, v112, s51\n"
      "v_mfma_f32_16x16x32_bf16 v[170:173], a[84:87], v[166:169], v[170:173]\n"
      "v_fma_f32 v135, v124, v124, s51\n"
      "v_fmac_f32 v142, v113, v113\n"
      "v_mfma_f32_16x16x32_bf16 v[170:173], a[88:91], v[186:189], v[170:173]\n"
      "v_fmac_f32 v135, v125, v125\n"
      "v_fmac_f32 v142, v114, v114\n"
      "v_mfma_f32_16x16x32_bf16 v[170:173], a[92:95], v[98:101], v[170:173]\n"
      "v_fmac_f32 v135, v126, v126\n"
      "v_fmac_f32 v142, v115, v115\n"
      "v_mfma_f32_16x16x32_bf16 v[102:105], a[96:99], v[136:139], 0\n"
      "v_fmac_f32 v135, v127, v127\n"
      "v_fmac_f32 v142, v106, v106\n"
      "v_mfma_f32_16x16x32_bf16 v[102:105], a[100:103], v[128:131], v[102:105]\n"
      "v_fmac_f32 v135, v120, v120\n"
      "v_fmac_f32 v142, v107, v107\n"
      "v_mfma_f32_16x16x32_bf16 v[102:105], a[104:107], v[182:185], v[102:105]\n"
      "v_fmac_f32 v135, v121, v121\n"
      "v_fmac_f32 v142, v108, v108\n"
      "v_mfma_f32_16x16x32_bf16 v[102:105], a[108:111], v[166:169], v[102:105]\n"
      "v_fmac_f32 v135, v122, v122\n"
      "v_fmac_f32 v142, v109, v109\n"
      "v_mfma_f32_16x16x32_bf16 v[102:105], a[112:115], v[186:189], v[102:105]\n"
      "v_fmac_f32 v135, v123, v123\n"
      "v_fmac_f32 v142, v116, v116\n"
      "v_mfma_f32_16x16x32_bf16 v[102:105], a[116:119], v[98:101], v[102:105]\n"
      "v_fmac_f32 v135, v152, v152\n"
      "v_fmac_f32 v142, v117, v117\n"
      "v_mfma_f32_16x16x32_bf16 v[208:211], a[120:123], v[136:139], 0\n"
      "v_fmac_f32 v135, v153, v153\n"
      "v_fmac_f32 v142, v118, v118\n"
      "v_mfma_f32_16x16x32_bf16 v[208:211], a[124:127], v[128:131], v[208:211]\n"
      "v_fmac_f32 v135, v154, v154\n"
      "v_fmac_f32 v142, v119, v119\n"
      "v_mfma_f32_16x16x32_bf16 v[208:211], a[128:131], v[182:185], v[208:211]\n"
      "v_fmac_f32 v135, v155, v155\n"
      "v_fma_f32 v145, v194, v194, s51\n"
      "v_mfma_f32_16x16x32_bf16 v[208:211], a[132:135], v[166:169], v[208:211]\n"
      "v_permlane16_swap_b32 v142, v135\n"
      "v_mfma_f32_16x16x32_bf16 v[208:211], a[136:139], v[186:189], v[208:211]\n"
      "v_add_f32 v142, v142, v135\n"
      "v_mov_b32_e64 v135, v142\n"
      "v_mfma_f32_16x16x32_bf16 v[208:211], a[140:143], v[98:101], v[208:211]\n"
      "v_fma_f32 v160, v170, v170, s51\n"
      "v_permlane32_swap_b32 v142, v135\n"
      "v_mfma_f32_16x16x32_bf16 v[146:149], v[136:139], a[144:147], 0\n"
      "v_add_f32 v142, v142, v135\n"
      "v_rsq_f32_e64 v142, v142\n"
      "v_mfma_f32_16x16x32_bf16 v[146:149], v[128:131], a[148:151], v[146:149]\n"
      "v_mov_b32 v135, v142\n"
      "v_fmac_f32 v145, v195, v195\n"
      "v_mfma_f32_16x16x32_bf16 v[146:149], v[182:185], a[152:155], v[146:149]\n"
      "v_permlane16_swap_b32_e64 v142, v135\n"
      "v_mfma_f32_16x16x32_bf16 v[174:177], v[136:139], a[168:171], 0\n"
      "v_mul_f32 v142, s50, v142\n"
      "v_mul_f32 v124, v135, v124\n"
      "v_mfma_f32_16x16x32_bf16 v[174:177], v[128:131], a[172:175], v[174:177]\n"
      "v_mul_f32 v125, v135, v125\n"
      "v_mul_f32 v112, v142, v112\n"
      "v_mfma_f32_16x16x32_bf16 v[174:177], v[182:185], a[176:179], v[174:177]\n"
      "v_mul_f32 v113, v142, v113\n"
      "v_cvt_pk_bf16_f32 v124, v124, v125\n"
      "v_mfma_f32_16x16x32_bf16 v[216:219], v[136:139], a[192:195], 0\n"
      "v_mul_f32 v126, v135, v126\n"
      "v_mul_f32 v127, v135, v127\n"
      "v_mfma_f32_16x16x32_bf16 v[216:219], v[128:131], a[196:199], v[216:219]\n"
      "v_mul_f32 v152, v135, v152\n"
      "v_mul_f32_e64 v153, v135, v153\n"
      "v_mfma_f32_16x16x32_bf16 v[216:219], v[182:185], a[200:203], v[216:219]\n"
      "v_cvt_pk_bf16_f32 v112, v112, v113\n"
      "v_mul_f32 v114, v142, v114\n"
      "v_mfma_f32_16x16x32_bf16 v[146:149], v[166:169], a[156:159], v[146:149]\n"
      "ds_read_b128 v[128:131], v0 offset:2048\n"
      "v_mul_f32 v115, v142, v115\n"
      "v_mfma_f32_16x16x32_bf16 v[146:149], v[186:189], a[160:163], v[146:149]\n"
      "ds_read_b128 v[182:185], v1 offset:2048\n"
      "v_mul_f32_e64 v116, v142, v116\n"
      "v_mfma_f32_16x16x32_bf16 v[146:149], v[98:101], a[164:167], v[146:149]\n"
      "ds_read_b128 v[190:193], v0 offset:8192\n"
      "v_mul_f32 v117, v142, v117\n"
      "v_mfma_f32_16x16x32_bf16 v[174:177], v[166:169], a[180:183], v[174:177]\n"
      "v_cvt_pk_bf16_f32 v125, v126, v127\n"
      "v_mul_f32 v120, v135, v120\n"
      "v_mfma_f32_16x16x32_bf16 v[174:177], v[186:189], a[184:187], v[174:177]\n"
      "v_mul_f32 v121, v135, v121\n"
      "v_mul_f32 v122, v135, v122\n"
      "v_mfma_f32_16x16x32_bf16 v[174:177], v[98:101], a[188:191], v[174:177]\n"
      "v_mul_f32_e64 v123, v135, v123\n"
      "v_cvt_pk_bf16_f32 v152, v152, v153\n"
      "v_mfma_f32_16x16x32_bf16 v[216:219], v[166:169], a[204:207], v[216:219]\n"
      "s_waitcnt lgkmcnt(2)\n"
      "v_mul_f32 v154, v135, v154\n"
      "v_mul_f32_e64 v155, v135, v155\n"
      "v_mfma_f32_16x16x32_bf16 v[216:219], v[186:189], a[208:211], v[216:219]\n"
      "s_waitcnt lgkmcnt(1)\n"
      "v_cvt_pk_bf16_f32 v113, v114, v115\n"
      "v_mul_f32 v106, v142, v106\n"
      "v_mfma_f32_16x16x32_bf16 v[216:219], v[98:101], a[212:215], v[216:219]\n"
      "s_waitcnt lgkmcnt(0)\n"
      "v_mul_f32 v107, v142, v107\n"
      "v_mul_f32 v108, v142, v108\n"
      "v_mfma_f32_16x16x32_bf16 v[132:135], v[128:131], a[144:147], 0\n"
      "ds_read_b128 v[166:169], v1 offset:8192\n"
      "v_mul_f32 v109, v142, v109\n"
      "v_mfma_f32_16x16x32_bf16 v[132:135], v[182:185], a[148:151], v[132:135]\n"
      "ds_read_b128 v[98:101], v0 offset:14336\n"
      "v_cvt_pk_bf16_f32 v116, v116, v117\n"
      "v_mfma_f32_16x16x32_bf16 v[132:135], v[190:193], a[152:155], v[132:135]\n"
      "v_mul_f32_e64 v118, v142, v118\n"
      "v_mul_f32_e64 v119, v142, v119\n"
      "v_mfma_f32_16x16x32_bf16 v[136:139], v[128:131], a[168:171], 0\n"
      "ds_read_b128 v[186:189], v1 offset:14336\n"
      "v_cvt_pk_bf16_f32 v126, v120, v121\n"
      "v_mfma_f32_16x16x32_bf16 v[136:139], v[182:185], a[172:175], v[136:139]\n"
      "v_cvt_pk_bf16_f32 v127, v122, v123\n"
      "v_cvt_pk_bf16_f32 v153, v154, v155\n"
      "v_mfma_f32_16x16x32_bf16 v[136:139], v[190:193], a[176:179], v[136:139]\n"
      "v_cvt_pk_bf16_f32 v114, v106, v107\n"
      "v_cvt_pk_bf16_f32 v115, v108, v109\n"
      "v_mfma_f32_16x16x32_bf16 v[140:143], v[128:131], a[192:195], 0\n"
      "s_waitcnt lgkmcnt(2)\n"
      "v_cvt_pk_bf16_f32 v117, v118, v119\n"
      "v_fmac_f32 v160, v171, v171\n"
      "v_mfma_f32_16x16x32_bf16 v[140:143], v[182:185], a[196:199], v[140:143]\n"
      "s_waitcnt lgkmcnt(1)\n"
      "v_fmac_f32 v145, v196, v196\n"
      "v_fmac_f32 v160, v172, v172\n"
      "v_mfma_f32_16x16x32_bf16 v[140:143], v[190:193], a[200:203], v[140:143]\n"
      "v_fmac_f32 v145, v197, v197\n"
      "v_fmac_f32 v160, v173, v173\n"
      "v_mfma_f32_16x16x32_bf16 v[132:135], v[166:169], a[156:159], v[132:135]\n"
      "s_waitcnt lgkmcnt(0)\n"
      "v_fmac_f32 v145, v198, v198\n"
      "v_fmac_f32 v160, v102, v102\n"
      "v_mfma_f32_16x16x32_bf16 v[132:135], v[98:101], a[160:163], v[132:135]\n"
      "v_fmac_f32 v145, v199, v199\n"
      "v_fmac_f32 v160, v103, v103\n"
      "v_mfma_f32_16x16x32_bf16 v[132:135], v[186:189], a[164:167], v[132:135]\n"
      "v_fmac_f32 v145, v200, v200\n"
      "v_fmac_f32 v160, v104, v104\n"
      "v_mfma_f32_16x16x32_bf16 v[136:139], v[166:169], a[180:183], v[136:139]\n"
      "v_fmac_f32 v145, v201, v201\n"
      "v_fmac_f32 v160, v105, v105\n"
      "v_mfma_f32_16x16x32_bf16 v[136:139], v[98:101], a[184:187], v[136:139]\n"
      "ds_read_b128 v[128:131], v10 offset:32\n"
      "v_fmac_f32 v145, v162, v162\n"
      "v_mfma_f32_16x16x32_bf16 v[136:139], v[186:189], a[188:191], v[136:139]\n"
      "v_fmac_f32 v160, v208, v208\n"
      "v_fmac_f32_e64 v145, v163, v163\n"
      "v_mfma_f32_16x16x32_bf16 v[140:143], v[166:169], a[204:207], v[140:143]\n"
      "v_fmac_f32 v160, v209, v209\n"
      "v_fmac_f32 v145, v164, v164\n"
      "v_mfma_f32_16x16x32_bf16 v[140:143], v[98:101], a[208:211], v[140:143]\n"
      "v_fmac_f32 v160, v210, v210\n"
      "v_fmac_f32 v145, v165, v165\n"
      "v_mfma_f32_16x16x32_bf16 v[140:143], v[186:189], a[212:215], v[140:143]\n"
      "v_fmac_f32_e64 v160, v211, v211\n"
      "v_cvt_pk_bf16_f32 v135, v134, v135\n"
      "v_mfma_f32_16x16x32_bf16 v[108:111], v[212:215], v[178:181], v[40:43]\n"
      "ds_read_b64 v[206:207], v10 offset:24\n"
      "v_permlane16_swap_b32 v145, v160\n"
      "v_mfma_f32_16x16x32_bf16 v[118:121], v[124:127], v[178:181], v[44:47]\n"
      "s_waitcnt lgkmcnt(1)\n"
      "v_add_f32 v145, v145, v160\n"
      "v_mov_b32 v160, v145\n"
      "ds_read_b128 v[182:185], v0\n"
      "v_cvt_pk_bf16_f32 v134, v132, v133\n"
      "v_permlane32_swap_b32 v145, v160\n"
      "v_add_f32 v145, v145, v160\n"
      "v_rsq_f32_e64 v145, v145\n"
      "ds_read_b128 v[186:189], v1\n"
      "v_mov_b32 v160, v145\n"
      "ds_read_b128 v[98:101], v0 offset:6144\n"
      "v_cvt_pk_bf16_f32 v76, v146, v147\n"
      "v_permlane16_swap_b32 v145, v160\n"
      "v_mul_f32 v170, v160, v170\n"
      "v_mul_f32 v171, v160, v171\n"
      "v_cvt_pk_bf16_f32 v170, v170, v171\n"
      "v_mul_f32 v172, v160, v172\n"
      "v_mul_f32 v173, v160, v173\n"
      "v_cvt_pk_bf16_f32 v171, v172, v173\n"
      "v_mul_f32 v102, v160, v102\n"
      "v_mul_f32 v103, v160, v103\n"
      "v_mul_f32 v104, v160, v104\n"
      "v_mul_f32 v105, v160, v105\n"
      "v_cvt_pk_bf16_f32 v172, v102, v103\n"
      "v_cvt_pk_bf16_f32 v173, v104, v105\n"
      "v_mul_f32 v208, v160, v208\n"
      "v_mul_f32 v209, v160, v209\n"
      "v_mfma_f32_16x16x32_bf16 v[166:169], v[170:173], v[178:181], v[48:51]\n"
      "s_waitcnt lgkmcnt(2)\n"
      "v_cvt_pk_bf16_f32 v208, v208, v209\n"
      "v_mul_f32 v210, v160, v210\n"
      "v_mfma_f32_16x16x32_bf16 v[102:105], v[128:131], v[178:181], v[24:27]\n"
      "s_waitcnt lgkmcnt(1)\n"
      "v_mul_f32 v211, v160, v211\n"
      "v_cvt_pk_bf16_f32 v209, v210, v211\n"
      "v_mfma_f32_16x16x16_bf16 v[108:111], v[204:205], v[158:159], v[108:111]\n"
      "v_mul_f32 v145, s50, v145\n"
      "v_mul_f32 v194, v145, v194\n"
      "v_mul_f32 v195, v145, v195\n"
      "v_mfma_f32_16x16x16_bf16 v[118:121], v[152:153], v[158:159], v[118:121]\n"
      "v_cvt_pk_bf16_f32 v194, v194, v195\n"
      "v_mul_f32 v196, v145, v196\n"
      "v_mul_f32 v197, v145, v197\n"
      "v_mfma_f32_16x16x16_bf16 v[166:169], v[208:209], v[158:159], v[166:169]\n"
      "v_mul_f32 v162, v145, v162\n"
      "v_mul_f32_e64 v163, v145, v163\n"
      "v_cvt_pk_bf16_f32 v195, v196, v197\n"
      "v_mfma_f32_16x16x16_bf16 v[102:105], v[206:207], v[158:159], v[102:105]\n"
      "v_mul_f32 v198, v145, v198\n"
      "v_mul_f32 v199, v145, v199\n"
      "v_mul_f32 v200, v145, v200\n"
      "s_nop 4\n"
      "v_mfma_f32_16x16x32_bf16 v[104:107], v[182:185], a[144:147], 0\n"
      "s_waitcnt lgkmcnt(0)\n"
      "v_mul_f32 v201, v145, v201\n"
      "v_cvt_pk_bf16_f32 v162, v162, v163\n"
      "v_mfma_f32_16x16x32_bf16 v[104:107], v[186:189], a[148:151], v[104:107]\n"
      "ds_read_b128 v[190:193], v1 offset:6144\n"
      "v_mul_f32 v164, v145, v164\n"
      "v_mfma_f32_16x16x32_bf16 v[104:107], v[98:101], a[152:155], v[104:107]\n"
      "ds_read_b128 v[158:161], v0 offset:12288\n"
      "v_mul_f32 v165, v145, v165\n"
      "v_mfma_f32_16x16x32_bf16 v[154:157], v[182:185], a[168:171], 0\n"
      "ds_read_b128 v[144:147], v1 offset:12288\n"
      "v_cvt_pk_bf16_f32 v196, v198, v199\n"
      "v_mfma_f32_16x16x32_bf16 v[154:157], v[186:189], a[172:175], v[154:157]\n"
      "s_cbranch_vccnz L_rare_56_%=\n"
      "L_back_57_%=:\n"
      "v_cvt_pk_bf16_f32 v197, v200, v201\n"
      "v_cvt_pk_bf16_f32 v163, v164, v165\n"
      "v_mfma_f32_16x16x32_bf16 v[154:157], v[98:101], a[176:179], v[154:157]\n"
      "v_exp_f32 v108, v108\n"
      "v_mfma_f32_16x16x32_bf16 v[178:181], v[182:185], a[192:195], 0\n"
      "v_exp_f32 v109, v109\n"
      "v_mfma_f32_16x16x32_bf16 v[178:181], v[186:189], a[196:199], v[178:181]\n"
      "s_waitcnt lgkmcnt(2)\n"
      "v_exp_f32 v110, v110\n"
      "v_mfma_f32_16x16x32_bf16 v[178:181], v[98:101], a[200:203], v[178:181]\n"
      "s_waitcnt lgkmcnt(1)\n"
      "v_exp_f32 v111, v111\n"
      "v_mfma_f32_16x16x32_bf16 v[104:107], v[190:193], a[156:159], v[104:107]\n"
      "s_waitcnt lgkmcnt(0)\n"
      "v_exp_f32 v118, v118\n"
      "v_mfma_f32_16x16x32_bf16 v[104:107], v[158:161], a[160:163], v[104:107]\n"
      "v_exp_f32 v119, v119\n"
      "v_mfma_f32_16x16x32_bf16 v[104:107], v[144:147], a[164:167], v[104:107]\n"
      "v_exp_f32 v120, v120\n"
      "v_mfma_f32_16x16x32_bf16 v[154:157], v[190:193], a[180:183], v[154:157]\n"
      "v_exp_f32 v121, v121\n"
      "v_mfma_f32_16x16x32_bf16 v[154:157], v[158:161], a[184:187], v[154:157]\n"
      "ds_read_b128 v[198:201], v2 offset:18432\n"
      "v_cvt_pk_bf16_f32 v108, v108, v109\n"
      "v_mfma_f32_16x16x32_bf16 v[154:157], v[144:147], a[188:191], v[154:157]\n"
      "ds_read_b128 v[186:189], v2 offset:21504\n"
      "v_cvt_pk_bf16_f32 v109, v110, v111\n"
      "v_mfma_f32_16x16x32_bf16 v[178:181], v[190:193], a[204:207], v[178:181]\n"
      "v_cvt_pk_bf16_f32 v110, v118, v119\n"
      "v_cvt_pk_bf16_f32 v111, v120, v121\n"
      "v_mfma_f32_16x16x32_bf16 v[178:181], v[158:161], a[208:211], v[178:181]\n"
      "v_exp_f32_e64 v166, v166\n"
      "v_mfma_f32_16x16x32_bf16 v[178:181], v[144:147], a[212:215], v[178:181]\n"
      "v_exp_f32_e64 v167, v167\n"
      "v_mfma_f32_16x16x32_bf16 v[144:147], v[212:215], v[112:115], v[52:55]\n"
      "v_exp_f32 v168, v168\n"
      "v_mfma_f32_16x16x32_bf16 v[190:193], v[124:127], v[112:115], v[56:59]\n"
      "s_waitcnt lgkmcnt(1)\n"
      "v_exp_f32_e64 v169, v169\n"
      "v_mfma_f32_16x16x32_bf16 v[118:121], v[170:173], v[112:115], v[60:63]\n"
      "s_waitcnt lgkmcnt(0)\n"
      "v_exp_f32 v102, v102\n"
      "v_mfma_f32_16x16x32_bf16 v[182:185], v[128:131], v[112:115], v[26:29]\n"
      "ds_read_b128 v[158:161], v2 offset:24576\n"
      "v_cvt_pk_bf16_f32 v88, v166, v167\n"
      "v_mfma_f32_16x16x16_bf16 v[144:147], v[204:205], v[116:117], v[144:147]\n"
      "ds_read_b128 v[112:115], v2 offset:27648\n"
      "v_cvt_pk_bf16_f32 v89, v168, v169\n"
      "v_mfma_f32_16x16x16_bf16 v[190:193], v[152:153], v[116:117], v[190:193]\n"
      "v_cvt_pk_bf16_f32 v90, v102, 0\n"
      "v_cvt_pk_bf16_f32 v132, v104, v105\n"
      "v_cvt_pk_bf16_f32 v133, v106, v107\n"
      "v_mfma_f32_16x16x16_bf16 v[118:121], v[208:209], v[116:117], v[118:121]\n"
      "v_cvt_pk_bf16_f32 v77, v148, v149\n"
      "v_cvt_pk_bf16_f32 v139, v138, v139\n"
      "v_cvt_pk_bf16_f32 v138, v136, v137\n"
      "v_mfma_f32_16x16x16_bf16 v[182:185], v[206:207], v[116:117], v[182:185]\n"
      "v_cvt_pk_bf16_f32 v136, v154, v155\n"
      "v_cvt_pk_bf16_f32 v137, v156, v157\n"
      "v_cvt_pk_bf16_f32 v80, v174, v175\n"
      "v_mfma_f32_16x16x32_bf16 v[96:99], a[216:219], v[198:201], 0\n"
      "ds_read_u16 v78, v11\n"
      "v_cvt_pk_bf16_f32 v81, v176, v177\n"
      "v_mfma_f32_16x16x32_bf16 v[96:99], a[220:223], v[186:189], v[96:99]\n"
      "s_waitcnt lgkmcnt(2)\n"
      "v_cvt_pk_bf16_f32 v143, v142, v143\n"
      "v_cvt_pk_bf16_f32 v142, v140, v141\n"
      "v_mfma_f32_16x16x32_bf16 v[100:103], a[240:243], v[198:201], 0\n"
      "s_waitcnt lgkmcnt(1)\n"
      "v_cvt_pk_bf16_f32 v140, v178, v179\n"
      "v_cvt_pk_bf16_f32 v141, v180, v181\n"
      "v_mfma_f32_16x16x32_bf16 v[100:103], a[244:247], v[186:189], v[100:103]\n"
      "ds_read_u16 v82, v11 offset:32\n"
      "v_cvt_pk_bf16_f32 v84, v216, v217\n"
      "v_mfma_f32_16x16x32_bf16 v[104:107], v[232:235], v[198:201], 0\n"
      "ds_read_u16 v86, v11 offset:64\n"
      "v_cvt_pk_bf16_f32 v85, v218, v219\n"
      "v_mfma_f32_16x16x32_bf16 v[104:107], v[236:239], v[186:189], v[104:107]\n"
      "s_cbranch_vccnz L_rare_59_%=\n"
      "L_back_60_%=:\n"
      "v_exp_f32 v144, v144\n"
      "v_mfma_f32_16x16x32_bf16 v[96:99], a[224:227], v[158:161], v[96:99]\n"
      "s_waitcnt lgkmcnt(2)\n"
      "v_exp_f32 v145, v145\n"
      "v_mfma_f32_16x16x32_bf16 v[96:99], a[228:231], v[112:115], v[96:99]\n"
      "v_exp_f32 v146, v146\n"
      "v_mfma_f32_16x16x32_bf16 v[100:103], a[248:251], v[158:161], v[100:103]\n"
      "v_exp_f32 v147, v147\n"
      "v_mfma_f32_16x16x32_bf16 v[176:179], v[32:35], v[108:111], 0\n"
      "s_waitcnt lgkmcnt(1)\n"
      "v_exp_f32 v190, v190\n"
      "v_mfma_f32_16x16x32_bf16 v[176:179], v[32:35], v[88:91], v[176:179]\n"
      "s_waitcnt lgkmcnt(0)\n"
      "v_exp_f32 v191, v191\n"
      "v_mfma_f32_16x16x32_bf16 v[164:167], v[132:135], v[108:111], 0\n"
      "v_exp_f32 v192, v192\n"
      "v_mfma_f32_16x16x32_bf16 v[164:167], v[76:79], v[88:91], v[164:167]\n"
      "v_exp_f32 v193, v193\n"
      "v_mfma_f32_16x16x32_bf16 v[154:157], v[136:139], v[108:111], 0\n"
      "v_cvt_pk_bf16_f32 v144, v144, v145\n"
      "v_cvt_pk_bf16_f32 v145, v146, v147\n"
      "v_mfma_f32_16x16x32_bf16 v[154:157], v[80:83], v[88:91], v[154:157]\n"
      "v_cvt_pk_bf16_f32 v146, v190, v191\n"
      "v_cvt_pk_bf16_f32 v147, v192, v193\n"
      "v_mfma_f32_16x16x32_bf16 v[178:181], v[140:143], v[108:111], 0\n"
      "v_exp_f32 v118, v118\n"
      "v_mfma_f32_16x16x32_bf16 v[178:181], v[84:87], v[88:91], v[178:181]\n"
      "v_exp_f32 v119, v119\n"
      "v_mfma_f32_16x16x32_bf16 v[190:193], v[212:215], v[194:197], v[64:67]\n"
      "ds_read_b128 v[108:111], v2 offset:30720\n"
      "v_exp_f32 v120, v120\n"
      "v_mfma_f32_16x16x32_bf16 v[200:203], v[124:127], v[194:197], v[68:71]\n"
      "ds_read_b128 v[186:189], v2 offset:33792\n"
      "v_exp_f32 v121, v121\n"
      "v_mfma_f32_16x16x32_bf16 v[148:151], v[170:173], v[194:197], v[72:75]\n"
      "v_exp_f32 v182, v182\n"
      "v_mfma_f32_16x16x32_bf16 v[216:219], v[128:131], v[194:197], v[28:31]\n"
      "v_cvt_pk_bf16_f32 v92, v118, v119\n"
      "v_cvt_pk_bf16_f32 v93, v120, v121\n"
      "v_mfma_f32_16x16x16_bf16 v[190:193], v[204:205], v[162:163], v[190:193]\n"
      "v_cvt_pk_bf16_f32 v94, v182, 0\n"
      "v_rcp_f32 v176, v176\n"
      "v_mfma_f32_16x16x16_bf16 v[200:203], v[152:153], v[162:163], v[200:203]\n"
      "v_mul_f32 v164, v176, v164\n"
      "v_mul_f32 v165, v176, v165\n"
      "v_cvt_pk_bf16_f32 v164, v164, v165\n"
      "v_mfma_f32_16x16x16_bf16 v[148:151], v[208:209], v[162:163], v[148:151]\n"
      "v_mul_f32 v166, v176, v166\n"
      "v_mul_f32 v167, v176, v167\n"
      "v_cvt_pk_bf16_f32 v165, v166, v167\n"
      "v_mfma_f32_16x16x16_bf16 v[216:219], v[206:207], v[162:163], v[216:219]\n"
      "v_mul_f32_e64 v154, v176, v154\n"
      "ds_read_b128 v[120:123], v2 offset:18688\n"
      "v_mfma_f32_16x16x32_bf16 v[100:103], a[252:255], v[112:115], v[100:103]\n"
      "s_waitcnt lgkmcnt(1)\n"
      "ds_read_b128 v[194:197], v2 offset:21760\n"
      "v_mfma_f32_16x16x32_bf16 v[104:107], v[240:243], v[158:161], v[104:107]\n"
      "v_mul_f32 v155, v176, v155\n"
      "v_mul_f32 v156, v176, v156\n"
      "v_mfma_f32_16x16x32_bf16 v[104:107], v[244:247], v[112:115], v[104:107]\n"
      "s_cbranch_vccnz L_rare_62_%=\n"
      "L_back_63_%=:\n"
      "v_exp_f32 v190, v190\n"
      "v_mfma_f32_16x16x32_bf16 v[96:99], a[232:235], v[108:111], v[96:99]\n"
      "v_exp_f32 v191, v191\n"
      "v_mfma_f32_16x16x32_bf16 v[96:99], a[236:239], v[186:189], v[96:99]\n"
      "ds_read_b128 v[158:161], v2 offset:24832\n"
      "v_exp_f32 v192, v192\n"
      "v_mfma_f32_16x16x32_bf16 v[100:103], v[224:227], v[108:111], v[100:103]\n"
      "s_waitcnt lgkmcnt(2)\n"
      "v_exp_f32 v193, v193\n"
      "v_mfma_f32_16x16x32_bf16 v[100:103], v[228:231], v[186:189], v[100:103]\n"
      "s_waitcnt lgkmcnt(1)\n"
      "v_exp_f32_e64 v200, v200\n"
      "v_mfma_f32_16x16x32_bf16 v[104:107], v[248:251], v[108:111], v[104:107]\n"
      "ds_read_b128 v[182:185], v2 offset:27904\n"
      "v_exp_f32 v201, v201\n"
      "v_mfma_f32_16x16x32_bf16 v[104:107], v[252:255], v[186:189], v[104:107]\n"
      "v_exp_f32 v202, v202\n"
      "v_mfma_f32_16x16x32_bf16 v[108:111], a[216:219], v[120:123], 0\n"
      "v_exp_f32 v203, v203\n"
      "v_mfma_f32_16x16x32_bf16 v[108:111], a[220:223], v[194:197], v[108:111]\n"
      "s_waitcnt lgkmcnt(1)\n"
      "v_cvt_pk_bf16_f32 v190, v190, v191\n"
      "v_cvt_pk_bf16_f32 v191, v192, v193\n"
      "v_mfma_f32_16x16x32_bf16 v[112:115], a[240:243], v[120:123], 0\n"
      "v_cvt_pk_bf16_f32 v192, v200, v201\n"
      "v_cvt_pk_bf16_f32 v193, v202, v203\n"
      "v_mfma_f32_16x16x32_bf16 v[112:115], a[244:247], v[194:197], v[112:115]\n"
      "v_exp_f32 v148, v148\n"
      "v_mfma_f32_16x16x32_bf16 v[116:119], v[232:235], v[120:123], 0\n"
      "s_waitcnt lgkmcnt(0)\n"
      "v_exp_f32 v149, v149\n"
      "v_mfma_f32_16x16x32_bf16 v[116:119], v[236:239], v[194:197], v[116:119]\n"
      "v_exp_f32 v150, v150\n"
      "v_mfma_f32_16x16x32_bf16 v[108:111], a[224:227], v[158:161], v[108:111]\n"
      "v_exp_f32 v151, v151\n"
      "v_mfma_f32_16x16x32_bf16 v[108:111], a[228:231], v[182:185], v[108:111]\n"
      "ds_read_b128 v[120:123], v10\n"
      "v_exp_f32 v216, v216\n"
      "v_mfma_f32_16x16x32_bf16 v[112:115], a[248:251], v[158:161], v[112:115]\n"
      "v_cvt_pk_bf16_f32 v88, v148, v149\n"
      "v_cvt_pk_bf16_f32 v89, v150, v151\n"
      "v_mfma_f32_16x16x32_bf16 v[186:189], v[32:35], v[144:147], 0\n"
      "v_cvt_pk_bf16_f32 v90, v216, 0\n"
      "v_mul_f32 v163, v96, v96\n"
      "v_mfma_f32_16x16x32_bf16 v[186:189], v[32:35], v[92:95], v[186:189]\n"
      "v_add_f32 v162, v96, v97\n"
      "v_fmac_f32_e64 v163, v97, v97\n"
      "v_mfma_f32_16x16x32_bf16 v[216:219], v[132:135], v[144:147], 0\n"
      "ds_read_b64 v[198:199], v10 offset:16\n"
      "v_add_f32 v162, v162, v98\n"
      "v_mfma_f32_16x16x32_bf16 v[216:219], v[76:79], v[92:95], v[216:219]\n"
      "v_fmac_f32 v163, v98, v98\n"
      "v_add_f32 v162, v162, v99\n"
      "v_mfma_f32_16x16x32_bf16 v[200:203], v[136:139], v[144:147], 0\n"
      "s_waitcnt lgkmcnt(1)\n"
      "v_fmac_f32 v163, v99, v99\n"
      "v_add_f32 v162, v162, v100\n"
      "v_mfma_f32_16x16x32_bf16 v[200:203], v[80:83], v[92:95], v[200:203]\n"
      "v_fmac_f32 v163, v100, v100\n"
      "v_add_f32 v162, v162, v101\n"
      "v_mfma_f32_16x16x32_bf16 v[148:151], v[140:143], v[144:147], 0\n"
      "v_fmac_f32 v163, v101, v101\n"
      "v_add_f32 v162, v162, v102\n"
      "v_mfma_f32_16x16x32_bf16 v[148:151], v[84:87], v[92:95], v[148:151]\n"
      "v_fmac_f32 v163, v102, v102\n"
      "v_add_f32 v162, v162, v103\n"
      "v_mfma_f32_16x16x32_bf16 v[194:197], v[120:123], v[212:215], v[30:33]\n"
      "s_waitcnt lgkmcnt(0)\n"
      "v_fmac_f32 v163, v103, v103\n"
      "v_add_f32_e64 v162, v162, v104\n"
      "v_mfma_f32_16x16x32_bf16 v[210:213], v[120:123], v[124:127], v[30:33]\n"
      "ds_read_b128 v[144:147], v2 offset:30976\n"
      "v_fmac_f32_e64 v163, v104, v104\n"
      "v_mfma_f32_16x16x32_bf16 v[124:127], v[120:123], v[170:173], v[30:33]\n"
      "ds_read_b128 v[172:175], v2 offset:34048\n"
      "v_add_f32 v162, v162, v105\n"
      "v_mfma_f32_16x16x32_bf16 v[168:171], v[120:123], v[128:131], v[30:33]\n"
      "v_fmac_f32 v163, v105, v105\n"
      "v_add_f32_e64 v162, v162, v106\n"
      "v_mfma_f32_16x16x16_bf16 v[194:197], v[198:199], v[204:205], v[194:197]\n"
      "v_fmac_f32 v163, v106, v106\n"
      "v_add_f32 v162, v162, v107\n"
      "v_fmac_f32 v163, v107, v107\n"
      "v_mfma_f32_16x16x16_bf16 v[210:213], v[198:199], v[152:153], v[210:213]\n"
      "v_rcp_f32 v186, v186\n"
      "v_permlane16_swap_b32 v162, v163\n"
      "v_mfma_f32_16x16x16_bf16 v[124:127], v[198:199], v[208:209], v[124:127]\n"
      "v_mul_f32 v216, v186, v216\n"
      "v_mul_f32 v217, v186, v217\n"
      "v_add_f32 v162, v162, v163\n"
      "v_mfma_f32_16x16x16_bf16 v[168:171], v[198:199], v[206:207], v[168:171]\n"
      "v_cvt_pk_bf16_f32 v216, v216, v217\n"
      "v_cndmask_b32 v194, v194, v210, s[58:59]\n"
      "v_mul_f32 v218, v186, v218\n"
      "v_mfma_f32_16x16x32_bf16 v[112:115], a[252:255], v[182:185], v[112:115]\n"
      "s_waitcnt lgkmcnt(0)\n"
      "v_cndmask_b32 v194, v194, v124, s[60:61]\n"
      "v_mul_f32 v219, v186, v219\n"
      "v_mfma_f32_16x16x32_bf16 v[116:119], v[240:243], v[158:161], v[116:119]\n"
      "v_cndmask_b32 v194, v194, v168, s[62:63]\n"
      "v_mul_f32 v157, v176, v157\n"
      "v_mfma_f32_16x16x32_bf16 v[116:119], v[244:247], v[182:185], v[116:119]\n"
      "s_cbranch_vccnz L_rare_65_%=\n"
      "L_back_66_%=:\n"
      "v_exp_f32_e64 v194, v194\n"
      "v_mfma_f32_16x16x32_bf16 v[108:111], a[232:235], v[144:147], v[108:111]\n"
      "s_add_u32 s42, s42, 1\n"
      "v_cvt_pk_bf16_f32 v194, v194, v194\n"
      "v_cvt_pk_bf16_f32 v217, v218, v219\n"
      "v_mfma_f32_16x16x32_bf16 v[108:111], a[236:239], v[172:175], v[108:111]\n"
      "ds_write_b16 v13, v194\n"
      "v_mfma_f32_16x16x32_bf16 v[112:115], v[224:227], v[144:147], v[112:115]\n"
      "v_mul_f32 v200, v186, v200\n"
      "v_mul_f32 v201, v186, v201\n"
      "v_mfma_f32_16x16x32_bf16 v[112:115], v[228:231], v[172:175], v[112:115]\n"
      "v_mul_f32 v202, v186, v202\n"
      "v_mul_f32 v203, v186, v203\n"
      "v_mfma_f32_16x16x32_bf16 v[116:119], v[248:251], v[144:147], v[116:119]\n"
      "v_mul_f32 v94, v108, v108\n"
      "v_add_f32 v93, v108, v109\n"
      "v_mfma_f32_16x16x32_bf16 v[116:119], v[252:255], v[172:175], v[116:119]\n"
      "ds_read_b128 v[172:175], v2 offset:18944\n"
      "v_fmac_f32 v94, v109, v109\n"
      "v_mfma_f32_16x16x32_bf16 v[168:171], v[32:35], v[190:193], 0\n"
      "ds_read_b128 v[212:215], v2 offset:22016\n"
      "v_add_f32 v93, v93, v110\n"
      "v_mfma_f32_16x16x32_bf16 v[168:171], v[32:35], v[88:91], v[168:171]\n"
      "v_fmac_f32 v94, v110, v110\n"
      "v_add_f32 v93, v93, v111\n"
      "v_mfma_f32_16x16x32_bf16 v[204:207], v[132:135], v[190:193], 0\n"
      "v_fmac_f32 v94, v111, v111\n"
      "v_add_f32 v93, v93, v112\n"
      "v_mfma_f32_16x16x32_bf16 v[204:207], v[76:79], v[88:91], v[204:207]\n"
      "v_fmac_f32 v94, v112, v112\n"
      "v_add_f32 v93, v93, v113\n"
      "v_mfma_f32_16x16x32_bf16 v[158:161], v[136:139], v[190:193], 0\n"
      "v_fmac_f32 v94, v113, v113\n"
      "v_add_f32 v93, v93, v114\n"
      "v_mfma_f32_16x16x32_bf16 v[158:161], v[80:83], v[88:91], v[158:161]\n"
      "s_waitcnt lgkmcnt(1)\n"
      "v_fmac_f32 v94, v114, v114\n"
      "v_add_f32 v93, v93, v115\n"
      "v_mfma_f32_16x16x32_bf16 v[194:197], v[140:143], v[190:193], 0\n"
      "s_waitcnt lgkmcnt(0)\n"
      "v_fmac_f32 v94, v115, v115\n"
      "v_add_f32 v93, v93, v116\n"
      "v_mfma_f32_16x16x32_bf16 v[194:197], v[84:87], v[88:91], v[194:197]\n"
      "ds_read_b128 v[144:147], v2 offset:25088\n"
      "v_fmac_f32_e64 v94, v116, v116\n"
      "v_mfma_f32_16x16x32_bf16 v[120:123], a[216:219], v[172:175], 0\n"
      "ds_read_b128 v[182:185], v2 offset:28160\n"
      "v_add_f32 v93, v93, v117\n"
      "v_mfma_f32_16x16x32_bf16 v[120:123], a[220:223], v[212:215], v[120:123]\n"
      "v_fmac_f32 v94, v117, v117\n"
      "v_add_f32_e64 v93, v93, v118\n"
      "v_mfma_f32_16x16x32_bf16 v[124:127], a[240:243], v[172:175], 0\n"
      "v_fmac_f32 v94, v118, v118\n"
      "v_add_f32 v93, v93, v119\n"
      "v_mfma_f32_16x16x32_bf16 v[124:127], a[244:247], v[212:215], v[124:127]\n"
      "v_fmac_f32 v94, v119, v119\n"
      "v_rcp_f32 v168, v168\n"
      "v_mfma_f32_16x16x32_bf16 v[128:131], v[232:235], v[172:175], 0\n"
      "ds_read_b128 v[208:211], v2 offset:31232\n"
      "v_permlane16_swap_b32 v93, v94\n"
      "v_mfma_f32_16x16x32_bf16 v[128:131], v[236:239], v[212:215], v[128:131]\n"
      "s_waitcnt lgkmcnt(2)\n"
      "v_mul_f32 v204, v168, v204\n"
      "v_mul_f32_e64 v205, v168, v205\n"
      "v_mfma_f32_16x16x32_bf16 v[120:123], a[224:227], v[144:147], v[120:123]\n"
      "s_waitcnt lgkmcnt(1)\n"
      "ds_read_b128 v[172:175], v2 offset:34304\n"
      "v_mfma_f32_16x16x32_bf16 v[120:123], a[228:231], v[182:185], v[120:123]\n"
      "v_add_f32_e64 v93, v93, v94\n"
      "v_cvt_pk_bf16_f32 v204, v204, v205\n"
      "v_mfma_f32_16x16x32_bf16 v[124:127], a[248:251], v[144:147], v[124:127]\n"
      "ds_read_b128 v[212:215], v14\n"
      "v_mul_f32 v206, v168, v206\n"
      "v_mfma_f32_16x16x32_bf16 v[124:127], a[252:255], v[182:185], v[124:127]\n"
      "ds_read_b128 v[190:193], v14 offset:64\n"
      "v_mul_f32 v207, v168, v207\n"
      "v_mfma_f32_16x16x32_bf16 v[128:131], v[240:243], v[144:147], v[128:131]\n"
      "s_waitcnt lgkmcnt(3)\n"
      "v_cvt_pk_bf16_f32 v205, v206, v207\n"
      "v_mul_f32 v158, v168, v158\n"
      "v_mfma_f32_16x16x32_bf16 v[128:131], v[244:247], v[182:185], v[128:131]\n"
      "v_mul_f32 v159, v168, v159\n"
      "v_mul_f32 v160, v168, v160\n"
      "v_mfma_f32_16x16x32_bf16 v[120:123], a[232:235], v[208:211], v[120:123]\n"
      "v_mul_f32 v161, v168, v161\n"
      "v_cvt_pk_bf16_f32 v166, v154, v155\n"
      "s_waitcnt lgkmcnt(2)\n"
      "v_mfma_f32_16x16x32_bf16 v[120:123], a[236:239], v[172:175], v[120:123]\n"
      "v_cvt_pk_bf16_f32 v167, v156, v157\n"
      "v_mul_f32 v178, v176, v178\n"
      "v_mfma_f32_16x16x32_bf16 v[124:127], v[224:227], v[208:211], v[124:127]\n"
      "s_waitcnt lgkmcnt(1)\n"
      "v_mul_f32 v179, v176, v179\n"
      "v_cvt_pk_bf16_f32 v218, v200, v201\n"
      "v_mfma_f32_16x16x32_bf16 v[124:127], v[228:231], v[172:175], v[124:127]\n"
      "s_waitcnt lgkmcnt(0)\n"
      "v_mul_f32 v188, v120, v120\n"
      "v_add_f32 v187, v120, v121\n"
      "v_mfma_f32_16x16x32_bf16 v[128:131], v[248:251], v[208:211], v[128:131]\n"
      "ds_write_b128 v3, v[164:167]\n"
      "v_mfma_f32_16x16x32_bf16 v[128:131], v[252:255], v[172:175], v[128:131]\n"
      "v_fmac_f32 v188, v121, v121\n"
      "v_add_f32 v187, v187, v122\n"
      "v_mfma_f32_16x16x32_bf16 v[152:155], v[212:215], v[32:35], 0\n"
      "v_fmac_f32 v188, v122, v122\n"
      "v_add_f32 v187, v187, v123\n"
      "v_mfma_f32_16x16x32_bf16 v[152:155], v[190:193], v[32:35], v[152:155]\n"
      "v_fmac_f32 v188, v123, v123\n"
      "v_add_f32 v187, v187, v124\n"
      "v_mfma_f32_16x16x32_bf16 v[170:173], v[212:215], v[132:135], 0\n"
      "v_fmac_f32 v188, v124, v124\n"
      "v_add_f32 v187, v187, v125\n"
      "v_mfma_f32_16x16x32_bf16 v[170:173], v[190:193], v[76:79], v[170:173]\n"
      "v_fmac_f32 v188, v125, v125\n"
      "v_add_f32 v187, v187, v126\n"
      "v_mfma_f32_16x16x32_bf16 v[208:211], v[212:215], v[136:139], 0\n"
      "v_fmac_f32 v188, v126, v126\n"
      "v_add_f32 v187, v187, v127\n"
      "v_mfma_f32_16x16x32_bf16 v[208:211], v[190:193], v[80:83], v[208:211]\n"
      "v_fmac_f32 v188, v127, v127\n"
      "v_add_f32 v187, v187, v128\n"
      "v_mfma_f32_16x16x32_bf16 v[136:139], v[212:215], v[140:143], 0\n"
      "v_fmac_f32 v188, v128, v128\n"
      "v_add_f32 v187, v187, v129\n"
      "v_mfma_f32_16x16x32_bf16 v[136:139], v[190:193], v[84:87], v[136:139]\n"
      "v_fmac_f32 v188, v129, v129\n"
      "v_add_f32 v187, v187, v130\n"
      "v_fmac_f32 v188, v130, v130\n"
      "v_add_f32 v187, v187, v131\n"
      "v_fmac_f32 v188, v131, v131\n"
      "v_mov_b32_dpp v152, v152 quad_perm:[0,0,0,0] row_mask:0xf bank_mask:0xf\n"
      "v_rcp_f32 v152, v152\n"
      "v_permlane16_swap_b32 v187, v188\n"
      "v_add_f32 v187, v187, v188\n"
      "v_mov_b32 v188, v187\n"
      "v_cvt_pk_bf16_f32 v219, v202, v203\n"
      "v_mul_f32 v148, v186, v148\n"
      "v_mul_f32_e64 v149, v186, v149\n"
      "v_cvt_pk_bf16_f32 v206, v158, v159\n"
      "v_cvt_pk_bf16_f32 v207, v160, v161\n"
      "v_mul_f32 v194, v168, v194\n"
      "v_mul_f32 v195, v168, v195\n"
      "v_cndmask_b32 v170, v170, v208, s[58:59]\n"
      "ds_write_b128 v3, v[216:219] offset:256\n"
      "ds_write_b128 v3, v[204:207] offset:512\n"
      "v_cvt_pk_bf16_f32 v178, v178, v179\n"
      "v_mul_f32 v180, v176, v180\n"
      "v_mul_f32 v181, v176, v181\n"
      "v_cvt_pk_bf16_f32 v148, v148, v149\n"
      "v_mul_f32 v150, v186, v150\n"
      "v_mul_f32 v151, v186, v151\n"
      "v_permlane32_swap_b32 v162, v93\n"
      "v_cvt_pk_bf16_f32 v194, v194, v195\n"
      "v_mul_f32 v196, v168, v196\n"
      "v_mul_f32_e64 v197, v168, v197\n"
      "v_permlane32_swap_b32 v187, v188\n"
      "v_cndmask_b32 v170, v170, v136, s[60:61]\n"
      "s_waitcnt vmcnt(11)\n"
      "v_or3_b32 v220, v220, v221, v222\n"
      "v_cvt_pk_bf16_f32 v179, v180, v181\n"
      "v_cvt_pk_bf16_f32 v149, v150, v151\n"
      "v_cvt_pk_bf16_f32 v195, v196, v197\n"
      "v_mul_f32 v170, v152, v170\n"
      "s_waitcnt vmcnt(10)\n"
      "v_or3_b32 v220, v220, v223, v39\n"
      "ds_write_b64 v4, v[178:179]\n"
      "ds_write_b64 v4, v[148:149] offset:256\n"
      "ds_write_b64 v4, v[194:195] offset:512\n"
      "v_add_f32_e64 v162, v162, v93\n"
      "v_add_f32 v187, v187, v188\n"
      "v_cvt_pk_bf16_f32 v170, v170, v170\n"
      "v_and_b32 v220, 0x7fff7fff, v220\n"
      "ds_write_b32 v8, v162\n"
      "ds_write_b32 v8, v187 offset:512\n"
      "ds_write_b16 v12, v170\n"
      "ds_write_b32 v31, v220 offset:1024\n"
      "s_waitcnt vmcnt(5)\n"
      "s_waitcnt lgkmcnt(0)\n"
      "s_barrier\n"
      "s_cmp_le_u32 s42, s44\n"
      "s_cbranch_scc1 L_loop_%=\n"
      "ds_read_b128 v[140:143], v9\n"
      "ds_read_b128 v[136:139], v9 offset:1024\n"
      "ds_read_b128 v[132:135], v2\n"
      "ds_read_b128 v[144:147], v2 offset:3072\n"
      "v_subrev_u32 v84, 151936, v9\n"
      "v_and_b32 v85, 0x300, v84\n"
      "v_readlane_b32 s47, v37, s42\n"
      "s_mov_b32 s67, 134784\n"
      "v_add3_u32 v84, v84, v85, s67\n"
      "s_mov_b32 s48, s47\n"
      "s_cmp_ge_u32 s47, 0x40000000\n"
      "s_cbranch_scc1 L_rare_67_%=\n"
      "L_back_68_%=:\n"
      "s_add_u32 s42, s42, 1\n"
      "ds_read_b128 v[148:151], v2 offset:6144\n"
      "s_waitcnt lgkmcnt(4)\n"
      "v_add_f32 v140, v140, v142\n"
      "v_add_f32 v141, v141, v143\n"
      "ds_read_b128 v[152:155], v2 offset:9216\n"
      "s_waitcnt lgkmcnt(4)\n"
      "v_add_f32 v136, v136, v138\n"
      "v_add_f32 v137, v137, v139\n"
      "v_add_f32 v140, v140, v136\n"
      "v_add_f32 v141, v141, v137\n"
      "v_fma_f32 v136, -v140, v140, s52\n"
      "v_fmac_f32 v136, 0x43400000, v141\n"
      "v_max_f32 v136, s52, v136\n"
      "v_rsq_f32 v136, v136\n"
      "s_nop 0\n"
      "v_mul_f32 v141, 0x43400000, v136\n"
      "v_mul_f32 v137, -v140, v136\n"
      "v_mov_b32 v136, v141\n"
      "v_mov_b32 v140, v137\n"
      "s_nop 0\n"
      "v_permlane32_swap_b32 v141, v136\n"
      "v_permlane32_swap_b32 v137, v140\n"
      "v_mov_b32_e64 v139, v141\n"
      "v_mov_b32_e64 v138, v137\n"
      "v_fma_f32 v120, v120, v136, v140\n"
      "v_permlane16_swap_b32_e64 v141, v139\n"
      "v_permlane16_swap_b32_e64 v137, v138\n"
      "v_fma_f32 v96, v96, v141, v137\n"
      "v_fma_f32 v97, v97, v141, v137\n"
      "v_fma_f32 v108, v108, v139, v138\n"
      "v_fma_f32 v109, v109, v139, v138\n"
      "v_fma_f32 v121, v121, v136, v140\n"
      "v_cvt_pk_bf16_f32 v96, v96, v97\n"
      "v_fma_f32 v98, v98, v141, v137\n"
      "v_fma_f32 v99, v99, v141, v137\n"
      "v_cvt_pk_bf16_f32 v108, v108, v109\n"
      "v_fma_f32 v110, v110, v139, v138\n"
      "v_fma_f32 v111, v111, v139, v138\n"
      "v_cvt_pk_bf16_f32 v120, v120, v121\n"
      "v_fma_f32 v122, v122, v136, v140\n"
      "v_fma_f32 v123, v123, v136, v140\n"
      "v_cvt_pk_bf16_f32 v97, v98, v99\n"
      "v_fma_f32 v100, v100, v141, v137\n"
      "v_fma_f32 v101, v101, v141, v137\n"
      "v_fma_f32 v102, v102, v141, v137\n"
      "v_fma_f32 v103, v103, v141, v137\n"
      "v_cvt_pk_bf16_f32 v109, v110, v111\n"
      "v_fma_f32 v112, v112, v139, v138\n"
      "v_fma_f32 v113, v113, v139, v138\n"
      "v_fma_f32 v114, v114, v139, v138\n"
      "v_fma_f32 v115, v115, v139, v138\n"
      "v_cvt_pk_bf16_f32 v121, v122, v123\n"
      "v_fma_f32 v124, v124, v136, v140\n"
      "v_fma_f32 v125, v125, v136, v140\n"
      "v_fma_f32 v126, v126, v136, v140\n"
      "v_fma_f32 v127, v127, v136, v140\n"
      "v_cvt_pk_bf16_f32 v98, v100, v101\n"
      "v_cvt_pk_bf16_f32 v99, v102, v103\n"
      "v_fma_f32 v104, v104, v141, v137\n"
      "v_fma_f32 v105, v105, v141, v137\n"
      "v_cvt_pk_bf16_f32 v110, v112, v113\n"
      "v_cvt_pk_bf16_f32 v111, v114, v115\n"
      "v_fma_f32 v116, v116, v139, v138\n"
      "v_fma_f32 v117, v117, v139, v138\n"
      "v_cvt_pk_bf16_f32 v122, v124, v125\n"
      "v_cvt_pk_bf16_f32 v123, v126, v127\n"
      "v_fma_f32 v128, v128, v136, v140\n"
      "v_fma_f32 v129, v129, v136, v140\n"
      "ds_write_b128 v5, v[96:99] offset:18432\n"
      "ds_write_b128 v5, v[108:111] offset:24576\n"
      "ds_write_b128 v5, v[120:123] offset:30720\n"
      "v_cvt_pk_bf16_f32 v104, v104, v105\n"
      "v_fma_f32 v106, v106, v141, v137\n"
      "v_fma_f32 v107, v107, v141, v137\n"
      "v_cvt_pk_bf16_f32 v116, v116, v117\n"
      "v_fma_f32 v118, v118, v139, v138\n"
      "v_fma_f32 v119, v119, v139, v138\n"
      "v_cvt_pk_bf16_f32 v128, v128, v129\n"
      "v_fma_f32 v130, v130, v136, v140\n"
      "v_fma_f32 v131, v131, v136, v140\n"
      "v_cvt_pk_bf16_f32 v105, v106, v107\n"
      "v_cvt_pk_bf16_f32 v117, v118, v119\n"
      "v_cvt_pk_bf16_f32 v129, v130, v131\n"
      "ds_write_b64 v6, v[104:105] offset:18432\n"
      "ds_write_b64 v6, v[116:117] offset:24576\n"
      "ds_write_b64 v6, v[128:129] offset:30720\n"
      "s_waitcnt lgkmcnt(3)\n"
      "v_mfma_f32_16x16x32_bf16 v[96:99], a[216:219], v[132:135], 0\n"
      "ds_read_b128 v[108:111], v2 offset:12288\n"
      "ds_read_b128 v[112:115], v2 offset:15360\n"
      "v_mfma_f32_16x16x32_bf16 v[96:99], a[220:223], v[144:147], v[96:99]\n"
      "ds_read_b128 v[120:123], v2 offset:256\n"
      "ds_read_b128 v[124:127], v2 offset:3328\n"
      "v_mfma_f32_16x16x32_bf16 v[100:103], a[240:243], v[132:135], 0\n"
      "ds_read_b128 v[128:131], v2 offset:6400\n"
      "ds_read_b128 v[136:139], v2 offset:9472\n"
      "v_mfma_f32_16x16x32_bf16 v[100:103], a[244:247], v[144:147], v[100:103]\n"
      "ds_read_b128 v[140:143], v2 offset:12544\n"
      "ds_read_b128 v[156:159], v2 offset:15616\n"
      "v_mfma_f32_16x16x32_bf16 v[104:107], v[232:235], v[132:135], 0\n"
      "ds_read_b128 v[132:135], v2 offset:512\n"
      "ds_read_b128 v[160:163], v2 offset:3584\n"
      "v_mfma_f32_16x16x32_bf16 v[104:107], v[236:239], v[144:147], v[104:107]\n"
      "v_mfma_f32_16x16x32_bf16 v[96:99], a[224:227], v[148:151], v[96:99]\n"
      "s_waitcnt lgkmcnt(9)\n"
      "v_mfma_f32_16x16x32_bf16 v[96:99], a[228:231], v[152:155], v[96:99]\n"
      "s_waitcnt lgkmcnt(8)\n"
      "ds_read_b128 v[144:147], v2 offset:6656\n"
      "v_mfma_f32_16x16x32_bf16 v[100:103], a[248:251], v[148:151], v[100:103]\n"
      "s_waitcnt lgkmcnt(8)\n"
      "ds_read_b128 v[164:167], v2 offset:9728\n"
      "v_mfma_f32_16x16x32_bf16 v[100:103], a[252:255], v[152:155], v[100:103]\n"
      "s_waitcnt lgkmcnt(8)\n"
      "v_mfma_f32_16x16x32_bf16 v[104:107], v[240:243], v[148:151], v[104:107]\n"
      "s_waitcnt lgkmcnt(7)\n"
      "v_mfma_f32_16x16x32_bf16 v[104:107], v[244:247], v[152:155], v[104:107]\n"
      "s_waitcnt lgkmcnt(6)\n"
      "v_mfma_f32_16x16x32_bf16 v[96:99], a[232:235], v[108:111], v[96:99]\n"
      "s_waitcnt lgkmcnt(5)\n"
      "v_mfma_f32_16x16x32_bf16 v[96:99], a[236:239], v[112:115], v[96:99]\n"
      "s_waitcnt lgkmcnt(4)\n"
      "v_mfma_f32_16x16x32_bf16 v[100:103], v[224:227], v[108:111], v[100:103]\n"
      "s_waitcnt lgkmcnt(3)\n"
      "ds_read_b128 v[152:155], v2 offset:12800\n"
      "v_mfma_f32_16x16x32_bf16 v[100:103], v[228:231], v[112:115], v[100:103]\n"
      "s_waitcnt lgkmcnt(3)\n"
      "ds_read_b128 v[148:151], v2 offset:15872\n"
      "v_mfma_f32_16x16x32_bf16 v[104:107], v[248:251], v[108:111], v[104:107]\n"
      "s_waitcnt lgkmcnt(3)\n"
      "v_mul_f32 v86, v96, v96\n"
      "v_add_f32 v85, v96, v97\n"
      "v_mfma_f32_16x16x32_bf16 v[104:107], v[252:255], v[112:115], v[104:107]\n"
      "s_waitcnt lgkmcnt(2)\n"
      "v_fmac_f32 v86, v97, v97\n"
      "v_add_f32 v85, v85, v98\n"
      "v_mfma_f32_16x16x32_bf16 v[108:111], a[216:219], v[120:123], 0\n"
      "v_fmac_f32 v86, v98, v98\n"
      "v_add_f32 v85, v85, v99\n"
      "v_mfma_f32_16x16x32_bf16 v[108:111], a[220:223], v[124:127], v[108:111]\n"
      "v_fmac_f32 v86, v99, v99\n"
      "v_add_f32_e64 v85, v85, v100\n"
      "v_mfma_f32_16x16x32_bf16 v[112:115], a[240:243], v[120:123], 0\n"
      "ds_read_b128 v[168:171], v84\n"
      "v_fmac_f32 v86, v100, v100\n"
      "v_mfma_f32_16x16x32_bf16 v[112:115], a[244:247], v[124:127], v[112:115]\n"
      "s_waitcnt lgkmcnt(2)\n"
      "v_add_f32 v85, v85, v101\n"
      "v_fmac_f32 v86, v101, v101\n"
      "v_mfma_f32_16x16x32_bf16 v[116:119], v[232:235], v[120:123], 0\n"
      "s_waitcnt lgkmcnt(1)\n"
      "v_add_f32 v85, v85, v102\n"
      "v_fmac_f32_e64 v86, v102, v102\n"
      "v_mfma_f32_16x16x32_bf16 v[116:119], v[236:239], v[124:127], v[116:119]\n"
      "ds_read_b128 v[172:175], v84 offset:2048\n"
      "v_add_f32 v85, v85, v103\n"
      "v_mfma_f32_16x16x32_bf16 v[108:111], a[224:227], v[128:131], v[108:111]\n"
      "v_fmac_f32 v86, v103, v103\n"
      "v_add_f32_e64 v85, v85, v104\n"
      "v_mfma_f32_16x16x32_bf16 v[108:111], a[228:231], v[136:139], v[108:111]\n"
      "v_fmac_f32 v86, v104, v104\n"
      "v_add_f32 v85, v85, v105\n"
      "v_mfma_f32_16x16x32_bf16 v[112:115], a[248:251], v[128:131], v[112:115]\n"
      "s_waitcnt lgkmcnt(1)\n"
      "v_fmac_f32 v86, v105, v105\n"
      "v_add_f32 v85, v85, v106\n"
      "v_mfma_f32_16x16x32_bf16 v[112:115], a[252:255], v[136:139], v[112:115]\n"
      "ds_read_b128 v[176:179], v84 offset:4096\n"
      "v_fmac_f32 v86, v106, v106\n"
      "v_mfma_f32_16x16x32_bf16 v[116:119], v[240:243], v[128:131], v[116:119]\n"
      "ds_read_b128 v[180:183], v84 offset:6144\n"
      "v_add_f32 v85, v85, v107\n"
      "v_mfma_f32_16x16x32_bf16 v[116:119], v[244:247], v[136:139], v[116:119]\n"
      "s_waitcnt lgkmcnt(2)\n"
      "v_fmac_f32_e64 v86, v107, v107\n"
      "v_mfma_f32_16x16x32_bf16 v[108:111], a[232:235], v[140:143], v[108:111]\n"
      "v_mfma_f32_16x16x32_bf16 v[108:111], a[236:239], v[156:159], v[108:111]\n"
      "v_permlane16_swap_b32 v85, v86\n"
      "v_mfma_f32_16x16x32_bf16 v[112:115], v[224:227], v[140:143], v[112:115]\n"
      "v_add_f32 v85, v85, v86\n"
      "v_mfma_f32_16x16x32_bf16 v[112:115], v[228:231], v[156:159], v[112:115]\n"
      "s_waitcnt lgkmcnt(1)\n"
      "ds_read_b128 v[136:139], v84 offset:8192\n"
      "v_mfma_f32_16x16x32_bf16 v[116:119], v[248:251], v[140:143], v[116:119]\n"
      "s_waitcnt lgkmcnt(1)\n"
      "v_mul_f32 v141, v108, v108\n"
      "v_add_f32 v140, v108, v109\n"
      "v_mfma_f32_16x16x32_bf16 v[116:119], v[252:255], v[156:159], v[116:119]\n"
      "ds_read_b128 v[156:159], v84 offset:10240\n"
      "v_fmac_f32_e64 v141, v109, v109\n"
      "v_mfma_f32_16x16x32_bf16 v[120:123], a[216:219], v[132:135], 0\n"
      "v_add_f32 v140, v140, v110\n"
      "v_fmac_f32 v141, v110, v110\n"
      "v_mfma_f32_16x16x32_bf16 v[120:123], a[220:223], v[160:163], v[120:123]\n"
      "v_add_f32 v140, v140, v111\n"
      "v_fmac_f32 v141, v111, v111\n"
      "v_mfma_f32_16x16x32_bf16 v[124:127], a[240:243], v[132:135], 0\n"
      "v_add_f32 v140, v140, v112\n"
      "v_fmac_f32 v141, v112, v112\n"
      "v_mfma_f32_16x16x32_bf16 v[124:127], a[244:247], v[160:163], v[124:127]\n"
      "v_add_f32 v140, v140, v113\n"
      "v_fmac_f32 v141, v113, v113\n"
      "v_mfma_f32_16x16x32_bf16 v[128:131], v[232:235], v[132:135], 0\n"
      "s_waitcnt lgkmcnt(1)\n"
      "v_add_f32 v140, v140, v114\n"
      "v_fmac_f32 v141, v114, v114\n"
      "v_mfma_f32_16x16x32_bf16 v[128:131], v[236:239], v[160:163], v[128:131]\n"
      "s_waitcnt lgkmcnt(0)\n"
      "ds_read_b128 v[160:163], v84 offset:256\n"
      "v_mfma_f32_16x16x32_bf16 v[120:123], a[224:227], v[144:147], v[120:123]\n"
      "ds_read_b128 v[132:135], v84 offset:2304\n"
      "v_add_f32_e64 v140, v140, v115\n"
      "v_mfma_f32_16x16x32_bf16 v[120:123], a[228:231], v[164:167], v[120:123]\n"
      "v_fmac_f32 v141, v115, v115\n"
      "v_add_f32 v140, v140, v116\n"
      "v_mfma_f32_16x16x32_bf16 v[124:127], a[248:251], v[144:147], v[124:127]\n"
      "ds_read_b128 v[184:187], v84 offset:4352\n"
      "v_fmac_f32 v141, v116, v116\n"
      "v_mfma_f32_16x16x32_bf16 v[124:127], a[252:255], v[164:167], v[124:127]\n"
      "ds_read_b128 v[188:191], v84 offset:6400\n"
      "v_add_f32 v140, v140, v117\n"
      "v_mfma_f32_16x16x32_bf16 v[128:131], v[240:243], v[144:147], v[128:131]\n"
      "ds_read_b128 v[144:147], v84 offset:8448\n"
      "v_fmac_f32 v141, v117, v117\n"
      "v_mfma_f32_16x16x32_bf16 v[128:131], v[244:247], v[164:167], v[128:131]\n"
      "ds_read_b128 v[164:167], v84 offset:10496\n"
      "v_add_f32 v140, v140, v118\n"
      "v_mfma_f32_16x16x32_bf16 v[120:123], a[232:235], v[152:155], v[120:123]\n"
      "s_waitcnt lgkmcnt(5)\n"
      "v_fmac_f32 v141, v118, v118\n"
      "v_add_f32 v140, v140, v119\n"
      "v_mfma_f32_16x16x32_bf16 v[120:123], a[236:239], v[148:151], v[120:123]\n"
      "s_waitcnt lgkmcnt(4)\n"
      "v_fmac_f32 v141, v119, v119\n"
      "v_mfma_f32_16x16x32_bf16 v[124:127], v[224:227], v[152:155], v[124:127]\n"
      "s_waitcnt lgkmcnt(3)\n"
      "v_permlane16_swap_b32 v140, v141\n"
      "v_mfma_f32_16x16x32_bf16 v[124:127], v[228:231], v[148:151], v[124:127]\n"
      "s_waitcnt lgkmcnt(2)\n"
      "v_add_f32 v140, v140, v141\n"
      "v_mul_f32 v142, v120, v120\n"
      "v_mfma_f32_16x16x32_bf16 v[128:131], v[248:251], v[152:155], v[128:131]\n"
      "s_waitcnt lgkmcnt(1)\n"
      "v_add_f32 v141, v120, v121\n"
      "v_fmac_f32_e64 v142, v121, v121\n"
      "v_mfma_f32_16x16x32_bf16 v[128:131], v[252:255], v[148:151], v[128:131]\n"
      "s_waitcnt lgkmcnt(0)\n"
      "v_add_f32 v141, v141, v122\n"
      "v_fmac_f32 v142, v122, v122\n"
      "v_mfma_f32_16x16x32_bf16 v[148:151], a[216:219], v[168:171], 0\n"
      "v_add_f32 v141, v141, v123\n"
      "v_fmac_f32 v142, v123, v123\n"
      "v_mfma_f32_16x16x32_bf16 v[148:151], a[220:223], v[172:175], v[148:151]\n"
      "ds_read_b128 v[152:155], v7\n"
      "v_add_f32 v141, v141, v124\n"
      "v_mfma_f32_16x16x32_bf16 v[192:195], a[240:243], v[168:171], 0\n"
      "ds_read_b128 v[196:199], v7 offset:4096\n"
      "v_fmac_f32 v142, v124, v124\n"
      "v_mfma_f32_16x16x32_bf16 v[192:195], a[244:247], v[172:175], v[192:195]\n"
      "ds_read_b128 v[200:203], v7 offset:8192\n"
      "v_add_f32 v141, v141, v125\n"
      "v_mfma_f32_16x16x32_bf16 v[204:207], v[232:235], v[168:171], 0\n"
      "ds_read_b128 v[168:171], v7 offset:12288\n"
      "v_fmac_f32_e64 v142, v125, v125\n"
      "v_mfma_f32_16x16x32_bf16 v[204:207], v[236:239], v[172:175], v[204:207]\n"
      "ds_read_b128 v[172:175], v7 offset:16384\n"
      "v_add_f32 v141, v141, v126\n"
      "v_mfma_f32_16x16x32_bf16 v[148:151], a[224:227], v[176:179], v[148:151]\n"
      "v_fmac_f32 v142, v126, v126\n"
      "v_add_f32_e64 v141, v141, v127\n"
      "v_mfma_f32_16x16x32_bf16 v[148:151], a[228:231], v[180:183], v[148:151]\n"
      "s_waitcnt lgkmcnt(4)\n"
      "v_fmac_f32 v142, v127, v127\n"
      "v_add_f32 v141, v141, v128\n"
      "v_mfma_f32_16x16x32_bf16 v[192:195], a[248:251], v[176:179], v[192:195]\n"
      "s_waitcnt lgkmcnt(3)\n"
      "v_fmac_f32 v142, v128, v128\n"
      "v_add_f32 v141, v141, v129\n"
      "v_mfma_f32_16x16x32_bf16 v[192:195], a[252:255], v[180:183], v[192:195]\n"
      "s_waitcnt lgkmcnt(2)\n"
      "v_fmac_f32 v142, v129, v129\n"
      "v_add_f32_e64 v141, v141, v130\n"
      "v_mfma_f32_16x16x32_bf16 v[204:207], v[240:243], v[176:179], v[204:207]\n"
      "s_waitcnt lgkmcnt(1)\n"
      "v_fmac_f32 v142, v130, v130\n"
      "v_add_f32 v141, v141, v131\n"
      "v_mfma_f32_16x16x32_bf16 v[204:207], v[244:247], v[180:183], v[204:207]\n"
      "s_waitcnt lgkmcnt(0)\n"
      "v_fmac_f32 v142, v131, v131\n"
      "v_permlane32_swap_b32 v85, v140\n"
      "v_mfma_f32_16x16x32_bf16 v[148:151], a[232:235], v[136:139], v[148:151]\n"
      "buffer_store_dwordx4 v[152:155], v20, s[20:23], s48 offen sc1\n"
      "v_permlane16_swap_b32_e64 v141, v142\n"
      "v_mfma_f32_16x16x32_bf16 v[148:151], a[236:239], v[156:159], v[148:151]\n"
      "buffer_store_dwordx4 v[196:199], v21, s[20:23], s48 offen sc1\n"
      "v_add_f32_e64 v141, v141, v142\n"
      "v_mfma_f32_16x16x32_bf16 v[192:195], v[224:227], v[136:139], v[192:195]\n"
      "buffer_store_dwordx4 v[200:203], v22, s[20:23], s48 offen sc1\n"
      "v_mov_b32 v142, v141\n"
      "v_mfma_f32_16x16x32_bf16 v[192:195], v[228:231], v[156:159], v[192:195]\n"
      "buffer_store_dwordx4 v[168:171], v23, s[20:23], s48 offen sc1\n"
      "v_permlane32_swap_b32 v141, v142\n"
      "v_mfma_f32_16x16x32_bf16 v[204:207], v[248:251], v[136:139], v[204:207]\n"
      "buffer_store_dwordx4 v[172:175], v25, s[20:23], s48 offen sc1\n"
      "s_nop 1\n"
      "v_mul_f32 v173, v148, v148\n"
      "v_mfma_f32_16x16x32_bf16 v[204:207], v[252:255], v[156:159], v[204:207]\n"
      "s_cmp_ge_u32 s47, 0x40000000\n"
      "s_cbranch_scc1 L_rare_69_%=\n"
      "L_back_70_%=:\n"
      "v_add_f32 v172, v148, v149\n"
      "v_mfma_f32_16x16x32_bf16 v[156:159], a[216:219], v[160:163], 0\n"
      "v_fmac_f32 v173, v149, v149\n"
      "v_add_f32_e64 v172, v172, v150\n"
      "v_mfma_f32_16x16x32_bf16 v[156:159], a[220:223], v[132:135], v[156:159]\n"
      "v_fmac_f32 v173, v150, v150\n"
      "v_add_f32 v172, v172, v151\n"
      "v_mfma_f32_16x16x32_bf16 v[136:139], a[240:243], v[160:163], 0\n"
      "v_fmac_f32 v173, v151, v151\n"
      "v_add_f32 v172, v172, v192\n"
      "v_mfma_f32_16x16x32_bf16 v[136:139], a[244:247], v[132:135], v[136:139]\n"
      "v_fmac_f32 v173, v192, v192\n"
      "v_add_f32 v172, v172, v193\n"
      "v_mfma_f32_16x16x32_bf16 v[168:171], v[232:235], v[160:163], 0\n"
      "v_fmac_f32 v173, v193, v193\n"
      "v_add_f32 v172, v172, v194\n"
      "v_mfma_f32_16x16x32_bf16 v[168:171], v[236:239], v[132:135], v[168:171]\n"
      "v_fmac_f32 v173, v194, v194\n"
      "v_add_f32 v172, v172, v195\n"
      "v_mfma_f32_16x16x32_bf16 v[156:159], a[224:227], v[184:187], v[156:159]\n"
      "v_fmac_f32 v173, v195, v195\n"
      "v_add_f32 v172, v172, v204\n"
      "v_mfma_f32_16x16x32_bf16 v[156:159], a[228:231], v[188:191], v[156:159]\n"
      "v_fmac_f32 v173, v204, v204\n"
      "v_add_f32 v172, v172, v205\n"
      "v_mfma_f32_16x16x32_bf16 v[136:139], a[248:251], v[184:187], v[136:139]\n"
      "v_fmac_f32 v173, v205, v205\n"
      "v_add_f32 v172, v172, v206\n"
      "v_mfma_f32_16x16x32_bf16 v[136:139], a[252:255], v[188:191], v[136:139]\n"
      "v_fmac_f32 v173, v206, v206\n"
      "v_add_f32 v172, v172, v207\n"
      "v_mfma_f32_16x16x32_bf16 v[168:171], v[240:243], v[184:187], v[168:171]\n"
      "v_fmac_f32 v173, v207, v207\n"
      "v_add_f32 v85, v85, v140\n"
      "v_mfma_f32_16x16x32_bf16 v[168:171], v[244:247], v[188:191], v[168:171]\n"
      "ds_write_b32 v8, v85 offset:2048\n"
      "v_mfma_f32_16x16x32_bf16 v[156:159], a[232:235], v[144:147], v[156:159]\n"
      "v_permlane16_swap_b32_e64 v172, v173\n"
      "v_mfma_f32_16x16x32_bf16 v[156:159], a[236:239], v[164:167], v[156:159]\n"
      "v_add_f32 v172, v172, v173\n"
      "v_add_f32 v141, v141, v142\n"
      "v_mfma_f32_16x16x32_bf16 v[136:139], v[224:227], v[144:147], v[136:139]\n"
      "ds_write_b32 v8, v141 offset:2560\n"
      "v_mfma_f32_16x16x32_bf16 v[136:139], v[228:231], v[164:167], v[136:139]\n"
      "v_mfma_f32_16x16x32_bf16 v[168:171], v[248:251], v[144:147], v[168:171]\n"
      "v_mfma_f32_16x16x32_bf16 v[168:171], v[252:255], v[164:167], v[168:171]\n"
      "s_nop 0\n"
      "v_mul_f32 v165, v156, v156\n"
      "v_add_f32 v164, v156, v157\n"
      "v_fmac_f32 v165, v157, v157\n"
      "v_add_f32 v164, v164, v158\n"
      "v_fmac_f32 v165, v158, v158\n"
      "v_add_f32 v164, v164, v159\n"
      "v_fmac_f32 v165, v159, v159\n"
      "v_add_f32 v164, v164, v136\n"
      "v_fmac_f32 v165, v136, v136\n"
      "v_add_f32 v164, v164, v137\n"
      "v_fmac_f32 v165, v137, v137\n"
      "v_add_f32 v164, v164, v138\n"
      "v_fmac_f32 v165, v138, v138\n"
      "v_add_f32 v164, v164, v139\n"
      "v_fmac_f32 v165, v139, v139\n"
      "v_add_f32 v164, v164, v168\n"
      "v_fmac_f32 v165, v168, v168\n"
      "v_add_f32 v164, v164, v169\n"
      "v_fmac_f32 v165, v169, v169\n"
      "v_add_f32 v164, v164, v170\n"
      "v_fmac_f32 v165, v170, v170\n"
      "v_add_f32 v164, v164, v171\n"
      "v_fmac_f32 v165, v171, v171\n"
      "s_nop 0\n"
      "s_nop 0\n"
      "v_permlane16_swap_b32 v164, v165\n"
      "v_add_f32 v164, v164, v165\n"
      "s_nop 0\n"
      "s_nop 0\n"
      "v_permlane32_swap_b32 v172, v164\n"
      "v_add_f32 v172, v172, v164\n"
      "ds_write_b32 v8, v172 offset:4096\n"
      "s_waitcnt lgkmcnt(0)\n"
      "s_barrier\n"
      "ds_read_b128 v[164:167], v9 offset:2048\n"
      "ds_read_b128 v[144:147], v9 offset:3072\n"
      "ds_read_b128 v[188:191], v9 offset:4096\n"
      "ds_read_b128 v[184:187], v9 offset:5120\n"
      "ds_read_b128 v[172:175], v7 offset:18432\n"
      "ds_read_b128 v[132:135], v7 offset:22528\n"
      "ds_read_b128 v[160:163], v7 offset:26624\n"
      "s_mul_i32 s67, s40, 3072\n"
      "v_subrev_u32 v141, 151936, v9\n"
      "v_add_u32 v142, s67, v141\n"
      "v_lshrrev_b32 v141, 4, v142\n"
      "ds_read_b128 v[200:203], v7 offset:30720\n"
      "v_add_u32 v85, 0, v141\n"
      "v_add_u32 v140, 64, v141\n"
      "v_add_u32 v196, 128, v141\n"
      "v_mul_u32_u24 v141, 2731, v85\n"
      "s_waitcnt lgkmcnt(7)\n"
      "v_add_f32 v164, v164, v166\n"
      "v_add_f32 v165, v165, v167\n"
      "v_mul_u32_u24 v167, 2731, v140\n"
      "s_waitcnt lgkmcnt(6)\n"
      "v_add_f32 v144, v144, v146\n"
      "v_add_f32 v145, v145, v147\n"
      "v_add_f32 v164, v164, v144\n"
      "v_add_f32 v165, v165, v145\n"
      "v_fma_f32 v144, -v164, v164, s52\n"
      "v_fmac_f32 v144, 0x43400000, v165\n"
      "v_max_f32 v144, s52, v144\n"
      "v_rsq_f32 v144, v144\n"
      "s_waitcnt lgkmcnt(4)\n"
      "v_add_f32 v188, v188, v190\n"
      "v_add_f32 v184, v184, v186\n"
      "v_add_f32 v189, v189, v191\n"
      "v_add_f32 v185, v185, v187\n"
      "v_add_f32 v188, v188, v184\n"
      "v_add_f32_e64 v189, v189, v185\n"
      "v_fma_f32 v184, -v188, v188, s52\n"
      "v_fmac_f32 v184, 0x43400000, v189\n"
      "v_mul_f32 v189, 0x43400000, v144\n"
      "v_mul_f32 v185, -v164, v144\n"
      "v_max_f32 v184, s52, v184\n"
      "v_mov_b32 v144, v189\n"
      "v_mov_b32 v164, v185\n"
      "v_rsq_f32 v184, v184\n"
      "v_permlane32_swap_b32 v189, v144\n"
      "v_permlane32_swap_b32 v185, v164\n"
      "v_mul_f32 v187, 0x43400000, v184\n"
      "v_mul_f32 v191, -v188, v184\n"
      "v_mov_b32 v184, v187\n"
      "v_mov_b32 v188, v191\n"
      "v_mov_b32 v186, v189\n"
      "v_mov_b32 v190, v185\n"
      "v_mul_u32_u24 v165, 2731, v196\n"
      "ds_read_b128 v[152:155], v7 offset:34816\n"
      "v_and_b32 v145, 0x3fffffff, v37\n"
      "v_ashrrev_i32 v147, 31, v37\n"
      "v_lshrrev_b32 v141, 16, v141\n"
      "v_lshrrev_b32 v167, 16, v167\n"
      "v_lshrrev_b32 v165, 16, v165\n"
      "v_permlane32_swap_b32 v187, v184\n"
      "v_permlane32_swap_b32 v191, v188\n"
      "v_permlane16_swap_b32 v189, v186\n"
      "v_permlane16_swap_b32 v185, v190\n"
      "v_add_u32 v145, 1179648, v145\n"
      "v_and_b32 v147, 100270464, v147\n"
      "v_mul_u32_u24 v188, 24, v141\n"
      "v_mul_u32_u24 v184, 24, v167\n"
      "v_mul_u32_u24 v146, 24, v165\n"
      "v_sub_u32 v145, v145, v147\n"
      "v_sub_u32 v188, v85, v188\n"
      "v_sub_u32 v184, v140, v184\n"
      "v_sub_u32 v146, v196, v146\n"
      "v_mov_b32 v166, v187\n"
      "v_mov_b32 v197, v191\n"
      "v_fma_f32 v96, v96, v189, v185\n"
      "v_fma_f32 v97, v97, v189, v185\n"
      "v_bfe_i32 v147, v37, 30, 1\n"
      "v_and_b32 v85, 7, v141\n"
      "v_and_b32 v140, 7, v167\n"
      "v_and_b32 v196, 7, v165\n"
      "v_fma_f32 v108, v108, v186, v190\n"
      "v_fma_f32 v109, v109, v186, v190\n"
      "v_cvt_pk_bf16_f32 v96, v96, v97\n"
      "v_fma_f32 v98, v98, v189, v185\n"
      "v_fma_f32 v99, v99, v189, v185\n"
      "v_fma_f32 v104, v104, v189, v185\n"
      "v_fma_f32 v105, v105, v189, v185\n"
      "v_and_b32 v147, 196224, v147\n"
      "v_lshlrev_b32 v141, 2, v141\n"
      "v_lshlrev_b32 v167, 2, v167\n"
      "v_lshlrev_b32_e64 v165, 2, v165\n"
      "v_fma_f32 v120, v120, v144, v164\n"
      "v_fma_f32 v121, v121, v144, v164\n"
      "v_cvt_pk_bf16_f32 v108, v108, v109\n"
      "v_fma_f32 v110, v110, v186, v190\n"
      "v_fma_f32 v111, v111, v186, v190\n"
      "v_fma_f32 v116, v116, v186, v190\n"
      "v_fma_f32 v117, v117, v186, v190\n"
      "v_cvt_pk_bf16_f32 v97, v98, v99\n"
      "v_fma_f32 v100, v100, v189, v185\n"
      "v_fma_f32 v101, v101, v189, v185\n"
      "v_fma_f32 v102, v102, v189, v185\n"
      "v_fma_f32 v103, v103, v189, v185\n"
      "v_cvt_pk_bf16_f32 v104, v104, v105\n"
      "v_fma_f32 v106, v106, v189, v185\n"
      "v_fma_f32 v107, v107, v189, v185\n"
      "v_sub_u32 v145, v145, v147\n"
      "v_add_u32 v141, 12, v141\n"
      "v_add_u32 v167, 12, v167\n"
      "v_add_u32 v165, 12, v165\n"
      "v_cvt_pk_bf16_f32 v120, v120, v121\n"
      "v_fma_f32 v122, v122, v144, v164\n"
      "v_fma_f32 v123, v123, v144, v164\n"
      "v_fma_f32 v128, v128, v144, v164\n"
      "v_fma_f32 v129, v129, v144, v164\n"
      "v_cvt_pk_bf16_f32 v109, v110, v111\n"
      "v_fma_f32 v112, v112, v186, v190\n"
      "v_fma_f32 v113, v113, v186, v190\n"
      "v_fma_f32 v114, v114, v186, v190\n"
      "v_fma_f32 v115, v115, v186, v190\n"
      "v_cvt_pk_bf16_f32 v116, v116, v117\n"
      "v_fma_f32 v118, v118, v186, v190\n"
      "v_fma_f32 v119, v119, v186, v190\n"
      "ds_bpermute_b32 v186, v141, v145\n"
      "ds_bpermute_b32 v141, v167, v145\n"
      "ds_bpermute_b32 v167, v165, v145\n"
      "v_cvt_pk_bf16_f32 v98, v100, v101\n"
      "v_cvt_pk_bf16_f32 v99, v102, v103\n"
      "v_cvt_pk_bf16_f32 v105, v106, v107\n"
      "v_permlane16_swap_b32 v187, v166\n"
      "v_permlane16_swap_b32 v191, v197\n"
      "v_cvt_pk_bf16_f32 v121, v122, v123\n"
      "v_fma_f32 v124, v124, v144, v164\n"
      "v_fma_f32 v125, v125, v144, v164\n"
      "v_fma_f32 v126, v126, v144, v164\n"
      "v_fma_f32 v127, v127, v144, v164\n"
      "v_cvt_pk_bf16_f32 v128, v128, v129\n"
      "v_fma_f32 v130, v130, v144, v164\n"
      "v_fma_f32 v131, v131, v144, v164\n"
      "v_cvt_pk_bf16_f32 v110, v112, v113\n"
      "v_cvt_pk_bf16_f32 v111, v114, v115\n"
      "v_cvt_pk_bf16_f32 v117, v118, v119\n"
      "ds_write_b128 v5, v[96:99]\n"
      "ds_write_b64 v6, v[104:105]\n"
      "v_cvt_pk_bf16_f32 v122, v124, v125\n"
      "v_cvt_pk_bf16_f32 v123, v126, v127\n"
      "v_cvt_pk_bf16_f32 v129, v130, v131\n"
      "ds_write_b128 v5, v[108:111] offset:6144\n"
      "ds_write_b64 v6, v[116:117] offset:6144\n"
      "v_fma_f32 v148, v148, v187, v191\n"
      "v_fma_f32 v149, v149, v187, v191\n"
      "v_fma_f32 v156, v156, v166, v197\n"
      "v_fma_f32 v157, v157, v166, v197\n"
      "ds_write_b128 v5, v[120:123] offset:12288\n"
      "ds_write_b64 v6, v[128:129] offset:12288\n"
      "v_cvt_pk_bf16_f32 v148, v148, v149\n"
      "v_fma_f32 v150, v150, v187, v191\n"
      "v_fma_f32 v151, v151, v187, v191\n"
      "v_cvt_pk_bf16_f32 v156, v156, v157\n"
      "v_fma_f32 v158, v158, v166, v197\n"
      "v_fma_f32 v159, v159, v166, v197\n"
      "v_cvt_pk_bf16_f32 v149, v150, v151\n"
      "v_fma_f32 v192, v192, v187, v191\n"
      "v_fma_f32 v193, v193, v187, v191\n"
      "v_fma_f32 v194, v194, v187, v191\n"
      "v_fma_f32 v195, v195, v187, v191\n"
      "v_cvt_pk_bf16_f32 v157, v158, v159\n"
      "v_fma_f32 v136, v136, v166, v197\n"
      "v_fma_f32 v137, v137, v166, v197\n"
      "v_fma_f32 v138, v138, v166, v197\n"
      "v_fma_f32 v139, v139, v166, v197\n"
      "v_subrev_u32 v128, 73728, v5\n"
      "v_cvt_pk_bf16_f32 v150, v192, v193\n"
      "v_cvt_pk_bf16_f32 v151, v194, v195\n"
      "v_fma_f32 v204, v204, v187, v191\n"
      "v_fma_f32 v205, v205, v187, v191\n"
      "v_cvt_pk_bf16_f32 v158, v136, v137\n"
      "v_cvt_pk_bf16_f32 v159, v138, v139\n"
      "v_fma_f32 v168, v168, v166, v197\n"
      "v_fma_f32 v169, v169, v166, v197\n"
      "v_readlane_b32 s47, v37, s42\n"
      "ds_write_b128 v128, v[148:151]\n"
      "ds_write_b128 v128, v[156:159] offset:6144\n"
      "v_cvt_pk_bf16_f32 v204, v204, v205\n"
      "v_fma_f32 v206, v206, v187, v191\n"
      "v_fma_f32 v207, v207, v187, v191\n"
      "v_cvt_pk_bf16_f32 v168, v168, v169\n"
      "v_fma_f32 v170, v170, v166, v197\n"
      "v_fma_f32 v171, v171, v166, v197\n"
      "v_subrev_u32 v128, 73728, v6\n"
      "v_cvt_pk_bf16_f32 v205, v206, v207\n"
      "v_cvt_pk_bf16_f32 v169, v170, v171\n"
      "s_mov_b32 s48, s47\n"
      "ds_write_b64 v128, v[204:205]\n"
      "ds_write_b64 v128, v[168:169] offset:6144\n"
      "s_cmp_ge_u32 s47, 0x40000000\n"
      "s_cbranch_scc1 L_rare_71_%=\n"
      "L_back_72_%=:\n"
      "s_waitcnt lgkmcnt(3)\n"
      "buffer_store_dwordx4 v[172:175], v20, s[20:23], s48 offen sc1\n"
      "buffer_store_dwordx4 v[132:135], v21, s[20:23], s48 offen sc1\n"
      "buffer_store_dwordx4 v[160:163], v22, s[20:23], s48 offen sc1\n"
      "buffer_store_dwordx4 v[200:203], v23, s[20:23], s48 offen sc1\n"
      "buffer_store_dwordx4 v[152:155], v25, s[20:23], s48 offen sc1\n"
      "v_xor_b32 v188, v188, v85\n"
      "v_xor_b32 v184, v184, v140\n"
      "s_cmp_ge_u32 s47, 0x40000000\n"
      "s_cbranch_scc1 L_rare_73_%=\n"
      "L_back_74_%=:\n"
      "v_xor_b32_e64 v146, v146, v196\n"
      "s_add_u32 s42, s42, 1\n"
      "v_lshl_add_u32 v186, v188, 4, v186\n"
      "v_lshl_add_u32 v141, v184, 4, v141\n"
      "v_lshl_add_u32 v167, v146, 4, v167\n"
      "s_waitcnt lgkmcnt(0)\n"
      "s_barrier\n"
      "ds_read_b128 v[128:131], v7\n"
      "ds_read_b128 v[120:123], v7 offset:4096\n"
      "ds_read_b128 v[108:111], v7 offset:8192\n"
      "ds_read_b128 v[116:119], v7 offset:12288\n"
      "ds_read_b128 v[124:127], v7 offset:16384\n"
      "ds_read_b128 v[96:99], v142\n"
      "ds_read_b128 v[112:115], v142 offset:1024\n"
      "ds_read_b128 v[104:107], v142 offset:2048\n"
      "v_readlane_b32 s47, v37, s42\n"
      "s_nop 0\n"
      "s_mov_b32 s48, s47\n"
      "s_cmp_ge_u32 s47, 0x40000000\n"
      "s_cbranch_scc1 L_rare_75_%=\n"
      "L_back_76_%=:\n"
      "s_waitcnt lgkmcnt(7)\n"
      "buffer_store_dwordx4 v[128:131], v20, s[20:23], s48 offen sc1\n"
      "s_waitcnt lgkmcnt(6)\n"
      "buffer_store_dwordx4 v[120:123], v21, s[20:23], s48 offen sc1\n"
      "s_waitcnt lgkmcnt(5)\n"
      "buffer_store_dwordx4 v[108:111], v22, s[20:23], s48 offen sc1\n"
      "s_waitcnt lgkmcnt(4)\n"
      "buffer_store_dwordx4 v[116:119], v23, s[20:23], s48 offen sc1\n"
      "s_waitcnt lgkmcnt(3)\n"
      "buffer_store_dwordx4 v[124:127], v25, s[20:23], s48 offen sc1\n"
      "s_nop 0\n"
      "s_waitcnt lgkmcnt(2)\n"
      "buffer_store_dwordx4 v[96:99], v186, s[20:23], 0 offen sc1\n"
      "s_waitcnt lgkmcnt(1)\n"
      "buffer_store_dwordx4 v[112:115], v141, s[20:23], 0 offen sc1\n"
      "s_cmp_ge_u32 s47, 0x40000000\n"
      "s_nop 0\n"
      "s_cbranch_scc1 L_rare_77_%=\n"
      "L_back_78_%=:\n"
      "s_waitcnt lgkmcnt(0)\n"
      "buffer_store_dwordx4 v[104:107], v167, s[20:23], 0 offen sc1\n"
      "s_endpgm\n"
      "L_tailB_%=:\n"
      "s_nop 7\n"
      "s_nop 7\n"
      "ds_read_b128 v[140:143], v9 offset:2048\n"
      "ds_read_b128 v[136:139], v9 offset:3072\n"
      "ds_read_b128 v[132:135], v2 offset:18432\n"
      "ds_read_b128 v[144:147], v2 offset:21504\n"
      "v_subrev_u32 v84, 151936, v9\n"
      "v_and_b32 v85, 0x300, v84\n"
      "v_readlane_b32 s47, v37, s42\n"
      "s_mov_b32 s67, 134784\n"
      "v_add3_u32 v84, v84, v85, s67\n"
      "s_mov_b32 s48, s47\n"
      "s_cmp_ge_u32 s47, 0x40000000\n"
      "s_cbranch_scc1 L_rare_79_%=\n"
      "L_back_80_%=:\n"
      "s_add_u32 s42, s42, 1\n"
      "ds_read_b128 v[148:151], v2 offset:24576\n"
      "s_waitcnt lgkmcnt(4)\n"
      "v_add_f32 v140, v140, v142\n"
      "v_add_f32 v141, v141, v143\n"
      "ds_read_b128 v[152:155], v2 offset:27648\n"
      "s_waitcnt lgkmcnt(4)\n"
      "v_add_f32 v136, v136, v138\n"
      "v_add_f32 v137, v137, v139\n"
      "v_add_f32 v140, v140, v136\n"
      "v_add_f32 v141, v141, v137\n"
      "v_fma_f32 v136, -v140, v140, s52\n"
      "v_fmac_f32 v136, 0x43400000, v141\n"
      "v_max_f32 v136, s52, v136\n"
      "v_rsq_f32 v136, v136\n"
      "s_nop 0\n"
      "v_mul_f32 v141, 0x43400000, v136\n"
      "v_mul_f32 v137, -v140, v136\n"
      "v_mov_b32 v136, v141\n"
      "v_mov_b32 v140, v137\n"
      "s_nop 0\n"
      "v_permlane32_swap_b32 v141, v136\n"
      "v_permlane32_swap_b32 v137, v140\n"
      "v_mov_b32 v139, v141\n"
      "v_mov_b32 v138, v137\n"
      "v_fma_f32 v120, v120, v136, v140\n"
      "v_permlane16_swap_b32 v141, v139\n"
      "v_permlane16_swap_b32 v137, v138\n"
      "v_fma_f32 v96, v96, v141, v137\n"
      "v_fma_f32 v97, v97, v141, v137\n"
      "v_fma_f32 v108, v108, v139, v138\n"
      "v_fma_f32 v109, v109, v139, v138\n"
      "v_fma_f32 v121, v121, v136, v140\n"
      "v_cvt_pk_bf16_f32 v96, v96, v97\n"
      "v_fma_f32 v98, v98, v141, v137\n"
      "v_fma_f32 v99, v99, v141, v137\n"
      "v_cvt_pk_bf16_f32 v108, v108, v109\n"
      "v_fma_f32 v110, v110, v139, v138\n"
      "v_fma_f32 v111, v111, v139, v138\n"
      "v_cvt_pk_bf16_f32 v120, v120, v121\n"
      "v_fma_f32 v122, v122, v136, v140\n"
      "v_fma_f32 v123, v123, v136, v140\n"
      "v_cvt_pk_bf16_f32 v97, v98, v99\n"
      "v_fma_f32 v100, v100, v141, v137\n"
      "v_fma_f32 v101, v101, v141, v137\n"
      "v_fma_f32 v102, v102, v141, v137\n"
      "v_fma_f32 v103, v103, v141, v137\n"
      "v_cvt_pk_bf16_f32 v109, v110, v111\n"
      "v_fma_f32 v112, v112, v139, v138\n"
      "v_fma_f32 v113, v113, v139, v138\n"
      "v_fma_f32 v114, v114, v139, v138\n"
      "v_fma_f32 v115, v115, v139, v138\n"
      "v_cvt_pk_bf16_f32 v121, v122, v123\n"
      "v_fma_f32 v124, v124, v136, v140\n"
      "v_fma_f32 v125, v125, v136, v140\n"
      "v_fma_f32 v126, v126, v136, v140\n"
      "v_fma_f32 v127, v127, v136, v140\n"
      "v_cvt_pk_bf16_f32 v98, v100, v101\n"
      "v_cvt_pk_bf16_f32 v99, v102, v103\n"
      "v_fma_f32 v104, v104, v141, v137\n"
      "v_fma_f32 v105, v105, v141, v137\n"
      "v_cvt_pk_bf16_f32 v110, v112, v113\n"
      "v_cvt_pk_bf16_f32 v111, v114, v115\n"
      "v_fma_f32 v116, v116, v139, v138\n"
      "v_fma_f32 v117, v117, v139, v138\n"
      "v_cvt_pk_bf16_f32 v122, v124, v125\n"
      "v_cvt_pk_bf16_f32 v123, v126, v127\n"
      "v_fma_f32 v128, v128, v136, v140\n"
      "v_fma_f32 v129, v129, v136, v140\n"
      "ds_write_b128 v5, v[96:99]\n"
      "ds_write_b128 v5, v[108:111] offset:6144\n"
      "ds_write_b128 v5, v[120:123] offset:12288\n"
      "v_cvt_pk_bf16_f32 v104, v104, v105\n"
      "v_fma_f32 v106, v106, v141, v137\n"
      "v_fma_f32 v107, v107, v141, v137\n"
      "v_cvt_pk_bf16_f32 v116, v116, v117\n"
      "v_fma_f32 v118, v118, v139, v138\n"
      "v_fma_f32 v119, v119, v139, v138\n"
      "v_cvt_pk_bf16_f32 v128, v128, v129\n"
      "v_fma_f32 v130, v130, v136, v140\n"
      "v_fma_f32 v131, v131, v136, v140\n"
      "v_cvt_pk_bf16_f32 v105, v106, v107\n"
      "v_cvt_pk_bf16_f32 v117, v118, v119\n"
      "v_cvt_pk_bf16_f32 v129, v130, v131\n"
      "ds_write_b64 v6, v[104:105]\n"
      "ds_write_b64 v6, v[116:117] offset:6144\n"
      "ds_write_b64 v6, v[128:129] offset:12288\n"
      "s_waitcnt lgkmcnt(3)\n"
      "v_mfma_f32_16x16x32_bf16 v[96:99], a[216:219], v[132:135], 0\n"
      "ds_read_b128 v[108:111], v2 offset:30720\n"
      "ds_read_b128 v[112:115], v2 offset:33792\n"
      "v_mfma_f32_16x16x32_bf16 v[96:99], a[220:223], v[144:147], v[96:99]\n"
      "ds_read_b128 v[120:123], v2 offset:18688\n"
      "ds_read_b128 v[124:127], v2 offset:21760\n"
      "v_mfma_f32_16x16x32_bf16 v[100:103], a[240:243], v[132:135], 0\n"
      "ds_read_b128 v[128:131], v2 offset:24832\n"
      "ds_read_b128 v[136:139], v2 offset:27904\n"
      "v_mfma_f32_16x16x32_bf16 v[100:103], a[244:247], v[144:147], v[100:103]\n"
      "ds_read_b128 v[140:143], v2 offset:30976\n"
      "ds_read_b128 v[156:159], v2 offset:34048\n"
      "v_mfma_f32_16x16x32_bf16 v[104:107], v[232:235], v[132:135], 0\n"
      "ds_read_b128 v[132:135], v2 offset:18944\n"
      "ds_read_b128 v[160:163], v2 offset:22016\n"
      "v_mfma_f32_16x16x32_bf16 v[104:107], v[236:239], v[144:147], v[104:107]\n"
      "v_mfma_f32_16x16x32_bf16 v[96:99], a[224:227], v[148:151], v[96:99]\n"
      "s_waitcnt lgkmcnt(9)\n"
      "v_mfma_f32_16x16x32_bf16 v[96:99], a[228:231], v[152:155], v[96:99]\n"
      "s_waitcnt lgkmcnt(8)\n"
      "ds_read_b128 v[144:147], v2 offset:25088\n"
      "v_mfma_f32_16x16x32_bf16 v[100:103], a[248:251], v[148:151], v[100:103]\n"
      "s_waitcnt lgkmcnt(8)\n"
      "ds_read_b128 v[164:167], v2 offset:28160\n"
      "v_mfma_f32_16x16x32_bf16 v[100:103], a[252:255], v[152:155], v[100:103]\n"
      "s_waitcnt lgkmcnt(8)\n"
      "v_mfma_f32_16x16x32_bf16 v[104:107], v[240:243], v[148:151], v[104:107]\n"
      "s_waitcnt lgkmcnt(7)\n"
      "v_mfma_f32_16x16x32_bf16 v[104:107], v[244:247], v[152:155], v[104:107]\n"
      "s_waitcnt lgkmcnt(6)\n"
      "v_mfma_f32_16x16x32_bf16 v[96:99], a[232:235], v[108:111], v[96:99]\n"
      "s_waitcnt lgkmcnt(5)\n"
      "v_mfma_f32_16x16x32_bf16 v[96:99], a[236:239], v[112:115], v[96:99]\n"
      "s_waitcnt lgkmcnt(4)\n"
      "v_mfma_f32_16x16x32_bf16 v[100:103], v[224:227], v[108:111], v[100:103]\n"
      "s_waitcnt lgkmcnt(3)\n"
      "ds_read_b128 v[152:155], v2 offset:31232\n"
      "v_mfma_f32_16x16x32_bf16 v[100:103], v[228:231], v[112:115], v[100:103]\n"
      "s_waitcnt lgkmcnt(3)\n"
      "ds_read_b128 v[148:151], v2 offset:34304\n"
      "v_mfma_f32_16x16x32_bf16 v[104:107], v[248:251], v[108:111], v[104:107]\n"
      "s_waitcnt lgkmcnt(3)\n"
      "v_mul_f32 v86, v96, v96\n"
      "v_add_f32 v85, v96, v97\n"
      "v_mfma_f32_16x16x32_bf16 v[104:107], v[252:255], v[112:115], v[104:107]\n"
      "s_waitcnt lgkmcnt(2)\n"
      "v_fmac_f32 v86, v97, v97\n"
      "v_add_f32 v85, v85, v98\n"
      "v_mfma_f32_16x16x32_bf16 v[108:111], a[216:219], v[120:123], 0\n"
      "v_fmac_f32 v86, v98, v98\n"
      "v_add_f32 v85, v85, v99\n"
      "v_mfma_f32_16x16x32_bf16 v[108:111], a[220:223], v[124:127], v[108:111]\n"
      "v_fmac_f32 v86, v99, v99\n"
      "v_add_f32_e64 v85, v85, v100\n"
      "v_mfma_f32_16x16x32_bf16 v[112:115], a[240:243], v[120:123], 0\n"
      "ds_read_b128 v[168:171], v84\n"
      "v_fmac_f32 v86, v100, v100\n"
      "v_mfma_f32_16x16x32_bf16 v[112:115], a[244:247], v[124:127], v[112:115]\n"
      "s_waitcnt lgkmcnt(2)\n"
      "v_add_f32 v85, v85, v101\n"
      "v_fmac_f32 v86, v101, v101\n"
      "v_mfma_f32_16x16x32_bf16 v[116:119], v[232:235], v[120:123], 0\n"
      "s_waitcnt lgkmcnt(1)\n"
      "v_add_f32 v85, v85, v102\n"
      "v_fmac_f32_e64 v86, v102, v102\n"
      "v_mfma_f32_16x16x32_bf16 v[116:119], v[236:239], v[124:127], v[116:119]\n"
      "ds_read_b128 v[172:175], v84 offset:2048\n"
      "v_add_f32 v85, v85, v103\n"
      "v_mfma_f32_16x16x32_bf16 v[108:111], a[224:227], v[128:131], v[108:111]\n"
      "v_fmac_f32 v86, v103, v103\n"
      "v_add_f32_e64 v85, v85, v104\n"
      "v_mfma_f32_16x16x32_bf16 v[108:111], a[228:231], v[136:139], v[108:111]\n"
      "v_fmac_f32 v86, v104, v104\n"
      "v_add_f32 v85, v85, v105\n"
      "v_mfma_f32_16x16x32_bf16 v[112:115], a[248:251], v[128:131], v[112:115]\n"
      "s_waitcnt lgkmcnt(1)\n"
      "v_fmac_f32 v86, v105, v105\n"
      "v_add_f32 v85, v85, v106\n"
      "v_mfma_f32_16x16x32_bf16 v[112:115], a[252:255], v[136:139], v[112:115]\n"
      "ds_read_b128 v[176:179], v84 offset:4096\n"
      "v_fmac_f32 v86, v106, v106\n"
      "v_mfma_f32_16x16x32_bf16 v[116:119], v[240:243], v[128:131], v[116:119]\n"
      "ds_read_b128 v[180:183], v84 offset:6144\n"
      "v_add_f32 v85, v85, v107\n"
      "v_mfma_f32_16x16x32_bf16 v[116:119], v[244:247], v[136:139], v[116:119]\n"
      "s_waitcnt lgkmcnt(2)\n"
      "v_fmac_f32_e64 v86, v107, v107\n"
      "v_mfma_f32_16x16x32_bf16 v[108:111], a[232:235], v[140:143], v[108:111]\n"
      "v_mfma_f32_16x16x32_bf16 v[108:111], a[236:239], v[156:159], v[108:111]\n"
      "v_permlane16_swap_b32 v85, v86\n"
      "v_mfma_f32_16x16x32_bf16 v[112:115], v[224:227], v[140:143], v[112:115]\n"
      "v_add_f32 v85, v85, v86\n"
      "v_mfma_f32_16x16x32_bf16 v[112:115], v[228:231], v[156:159], v[112:115]\n"
      "s_waitcnt lgkmcnt(1)\n"
      "ds_read_b128 v[136:139], v84 offset:8192\n"
      "v_mfma_f32_16x16x32_bf16 v[116:119], v[248:251], v[140:143], v[116:119]\n"
      "s_waitcnt lgkmcnt(1)\n"
      "v_mul_f32 v141, v108, v108\n"
      "v_add_f32 v140, v108, v109\n"
      "v_mfma_f32_16x16x32_bf16 v[116:119], v[252:255], v[156:159], v[116:119]\n"
      "ds_read_b128 v[156:159], v84 offset:10240\n"
      "v_fmac_f32_e64 v141, v109, v109\n"
      "v_mfma_f32_16x16x32_bf16 v[120:123], a[216:219], v[132:135], 0\n"
      "v_add_f32 v140, v140, v110\n"
      "v_fmac_f32 v141, v110, v110\n"
      "v_mfma_f32_16x16x32_bf16 v[120:123], a[220:223], v[160:163], v[120:123]\n"
      "v_add_f32 v140, v140, v111\n"
      "v_fmac_f32 v141, v111, v111\n"
      "v_mfma_f32_16x16x32_bf16 v[124:127], a[240:243], v[132:135], 0\n"
      "v_add_f32 v140, v140, v112\n"
      "v_fmac_f32 v141, v112, v112\n"
      "v_mfma_f32_16x16x32_bf16 v[124:127], a[244:247], v[160:163], v[124:127]\n"
      "v_add_f32 v140, v140, v113\n"
      "v_fmac_f32 v141, v113, v113\n"
      "v_mfma_f32_16x16x32_bf16 v[128:131], v[232:235], v[132:135], 0\n"
      "s_waitcnt lgkmcnt(1)\n"
      "v_add_f32 v140, v140, v114\n"
      "v_fmac_f32 v141, v114, v114\n"
      "v_mfma_f32_16x16x32_bf16 v[128:131], v[236:239], v[160:163], v[128:131]\n"
      "s_waitcnt lgkmcnt(0)\n"
      "ds_read_b128 v[160:163], v84 offset:256\n"
      "v_mfma_f32_16x16x32_bf16 v[120:123], a[224:227], v[144:147], v[120:123]\n"
      "ds_read_b128 v[132:135], v84 offset:2304\n"
      "v_add_f32_e64 v140, v140, v115\n"
      "v_mfma_f32_16x16x32_bf16 v[120:123], a[228:231], v[164:167], v[120:123]\n"
      "v_fmac_f32 v141, v115, v115\n"
      "v_add_f32 v140, v140, v116\n"
      "v_mfma_f32_16x16x32_bf16 v[124:127], a[248:251], v[144:147], v[124:127]\n"
      "ds_read_b128 v[184:187], v84 offset:4352\n"
      "v_fmac_f32 v141, v116, v116\n"
      "v_mfma_f32_16x16x32_bf16 v[124:127], a[252:255], v[164:167], v[124:127]\n"
      "ds_read_b128 v[188:191], v84 offset:6400\n"
      "v_add_f32 v140, v140, v117\n"
      "v_mfma_f32_16x16x32_bf16 v[128:131], v[240:243], v[144:147], v[128:131]\n"
      "ds_read_b128 v[144:147], v84 offset:8448\n"
      "v_fmac_f32 v141, v117, v117\n"
      "v_mfma_f32_16x16x32_bf16 v[128:131], v[244:247], v[164:167], v[128:131]\n"
      "ds_read_b128 v[164:167], v84 offset:10496\n"
      "v_add_f32 v140, v140, v118\n"
      "v_mfma_f32_16x16x32_bf16 v[120:123], a[232:235], v[152:155], v[120:123]\n"
      "s_waitcnt lgkmcnt(5)\n"
      "v_fmac_f32 v141, v118, v118\n"
      "v_add_f32 v140, v140, v119\n"
      "v_mfma_f32_16x16x32_bf16 v[120:123], a[236:239], v[148:151], v[120:123]\n"
      "s_waitcnt lgkmcnt(4)\n"
      "v_fmac_f32 v141, v119, v119\n"
      "v_mfma_f32_16x16x32_bf16 v[124:127], v[224:227], v[152:155], v[124:127]\n"
      "s_waitcnt lgkmcnt(3)\n"
      "v_permlane16_swap_b32 v140, v141\n"
      "v_mfma_f32_16x16x32_bf16 v[124:127], v[228:231], v[148:151], v[124:127]\n"
      "s_waitcnt lgkmcnt(2)\n"
      "v_add_f32 v140, v140, v141\n"
      "v_mul_f32 v142, v120, v120\n"
      "v_mfma_f32_16x16x32_bf16 v[128:131], v[248:251], v[152:155], v[128:131]\n"
      "s_waitcnt lgkmcnt(1)\n"
      "v_add_f32 v141, v120, v121\n"
      "v_fmac_f32_e64 v142, v121, v121\n"
      "v_mfma_f32_16x16x32_bf16 v[128:131], v[252:255], v[148:151], v[128:131]\n"
      "s_waitcnt lgkmcnt(0)\n"
      "v_add_f32 v141, v141, v122\n"
      "v_fmac_f32 v142, v122, v122\n"
      "v_mfma_f32_16x16x32_bf16 v[148:151], a[216:219], v[168:171], 0\n"
      "v_add_f32 v141, v141, v123\n"
      "v_fmac_f32 v142, v123, v123\n"
      "v_mfma_f32_16x16x32_bf16 v[148:151], a[220:223], v[172:175], v[148:151]\n"
      "ds_read_b128 v[152:155], v7 offset:18432\n"
      "v_add_f32 v141, v141, v124\n"
      "v_mfma_f32_16x16x32_bf16 v[192:195], a[240:243], v[168:171], 0\n"
      "ds_read_b128 v[196:199], v7 offset:22528\n"
      "v_fmac_f32 v142, v124, v124\n"
      "v_mfma_f32_16x16x32_bf16 v[192:195], a[244:247], v[172:175], v[192:195]\n"
      "ds_read_b128 v[200:203], v7 offset:26624\n"
      "v_add_f32 v141, v141, v125\n"
      "v_mfma_f32_16x16x32_bf16 v[204:207], v[232:235], v[168:171], 0\n"
      "ds_read_b128 v[168:171], v7 offset:30720\n"
      "v_fmac_f32_e64 v142, v125, v125\n"
      "v_mfma_f32_16x16x32_bf16 v[204:207], v[236:239], v[172:175], v[204:207]\n"
      "ds_read_b128 v[172:175], v7 offset:34816\n"
      "v_add_f32 v141, v141, v126\n"
      "v_mfma_f32_16x16x32_bf16 v[148:151], a[224:227], v[176:179], v[148:151]\n"
      "v_fmac_f32 v142, v126, v126\n"
      "v_add_f32_e64 v141, v141, v127\n"
      "v_mfma_f32_16x16x32_bf16 v[148:151], a[228:231], v[180:183], v[148:151]\n"
      "s_waitcnt lgkmcnt(4)\n"
      "v_fmac_f32 v142, v127, v127\n"
      "v_add_f32 v141, v141, v128\n"
      "v_mfma_f32_16x16x32_bf16 v[192:195], a[248:251], v[176:179], v[192:195]\n"
      "s_waitcnt lgkmcnt(3)\n"
      "v_fmac_f32 v142, v128, v128\n"
      "v_add_f32 v141, v141, v129\n"
      "v_mfma_f32_16x16x32_bf16 v[192:195], a[252:255], v[180:183], v[192:195]\n"
      "s_waitcnt lgkmcnt(2)\n"
      "v_fmac_f32 v142, v129, v129\n"
      "v_add_f32_e64 v141, v141, v130\n"
      "v_mfma_f32_16x16x32_bf16 v[204:207], v[240:243], v[176:179], v[204:207]\n"
      "s_waitcnt lgkmcnt(1)\n"
      "v_fmac_f32 v142, v130, v130\n"
      "v_add_f32 v141, v141, v131\n"
      "v_mfma_f32_16x16x32_bf16 v[204:207], v[244:247], v[180:183], v[204:207]\n"
      "s_waitcnt lgkmcnt(0)\n"
      "v_fmac_f32 v142, v131, v131\n"
      "v_permlane32_swap_b32 v85, v140\n"
      "v_mfma_f32_16x16x32_bf16 v[148:151], a[232:235], v[136:139], v[148:151]\n"
      "buffer_store_dwordx4 v[152:155], v20, s[20:23], s48 offen sc1\n"
      "v_permlane16_swap_b32_e64 v141, v142\n"
      "v_mfma_f32_16x16x32_bf16 v[148:151], a[236:239], v[156:159], v[148:151]\n"
      "buffer_store_dwordx4 v[196:199], v21, s[20:23], s48 offen sc1\n"
      "v_add_f32_e64 v141, v141, v142\n"
      "v_mfma_f32_16x16x32_bf16 v[192:195], v[224:227], v[136:139], v[192:195]\n"
      "buffer_store_dwordx4 v[200:203], v22, s[20:23], s48 offen sc1\n"
      "v_mov_b32 v142, v141\n"
      "v_mfma_f32_16x16x32_bf16 v[192:195], v[228:231], v[156:159], v[192:195]\n"
      "buffer_store_dwordx4 v[168:171], v23, s[20:23], s48 offen sc1\n"
      "v_permlane32_swap_b32 v141, v142\n"
      "v_mfma_f32_16x16x32_bf16 v[204:207], v[248:251], v[136:139], v[204:207]\n"
      "buffer_store_dwordx4 v[172:175], v25, s[20:23], s48 offen sc1\n"
      "s_nop 1\n"
      "v_mul_f32 v173, v148, v148\n"
      "v_mfma_f32_16x16x32_bf16 v[204:207], v[252:255], v[156:159], v[204:207]\n"
      "s_cmp_ge_u32 s47, 0x40000000\n"
      "s_cbranch_scc1 L_rare_81_%=\n"
      "L_back_82_%=:\n"
      "v_add_f32 v172, v148, v149\n"
      "v_mfma_f32_16x16x32_bf16 v[156:159], a[216:219], v[160:163], 0\n"
      "v_fmac_f32 v173, v149, v149\n"
      "v_add_f32_e64 v172, v172, v150\n"
      "v_mfma_f32_16x16x32_bf16 v[156:159], a[220:223], v[132:135], v[156:159]\n"
      "v_fmac_f32 v173, v150, v150\n"
      "v_add_f32 v172, v172, v151\n"
      "v_mfma_f32_16x16x32_bf16 v[136:139], a[240:243], v[160:163], 0\n"
      "v_fmac_f32 v173, v151, v151\n"
      "v_add_f32 v172, v172, v192\n"
      "v_mfma_f32_16x16x32_bf16 v[136:139], a[244:247], v[132:135], v[136:139]\n"
      "v_fmac_f32 v173, v192, v192\n"
      "v_add_f32 v172, v172, v193\n"
      "v_mfma_f32_16x16x32_bf16 v[168:171], v[232:235], v[160:163], 0\n"
      "v_fmac_f32 v173, v193, v193\n"
      "v_add_f32 v172, v172, v194\n"
      "v_mfma_f32_16x16x32_bf16 v[168:171], v[236:239], v[132:135], v[168:171]\n"
      "v_fmac_f32 v173, v194, v194\n"
      "v_add_f32 v172, v172, v195\n"
      "v_mfma_f32_16x16x32_bf16 v[156:159], a[224:227], v[184:187], v[156:159]\n"
      "v_fmac_f32 v173, v195, v195\n"
      "v_add_f32 v172, v172, v204\n"
      "v_mfma_f32_16x16x32_bf16 v[156:159], a[228:231], v[188:191], v[156:159]\n"
      "v_fmac_f32 v173, v204, v204\n"
      "v_add_f32 v172, v172, v205\n"
      "v_mfma_f32_16x16x32_bf16 v[136:139], a[248:251], v[184:187], v[136:139]\n"
      "v_fmac_f32 v173, v205, v205\n"
      "v_add_f32 v172, v172, v206\n"
      "v_mfma_f32_16x16x32_bf16 v[136:139], a[252:255], v[188:191], v[136:139]\n"
      "v_fmac_f32 v173, v206, v206\n"
      "v_add_f32 v172, v172, v207\n"
      "v_mfma_f32_16x16x32_bf16 v[168:171], v[240:243], v[184:187], v[168:171]\n"
      "v_fmac_f32 v173, v207, v207\n"
      "v_add_f32 v85, v85, v140\n"
      "v_mfma_f32_16x16x32_bf16 v[168:171], v[244:247], v[188:191], v[168:171]\n"
      "ds_write_b32 v8, v85\n"
      "v_mfma_f32_16x16x32_bf16 v[156:159], a[232:235], v[144:147], v[156:159]\n"
      "v_permlane16_swap_b32_e64 v172, v173\n"
      "v_mfma_f32_16x16x32_bf16 v[156:159], a[236:239], v[164:167], v[156:159]\n"
      "v_add_f32 v172, v172, v173\n"
      "v_add_f32 v141, v141, v142\n"
      "v_mfma_f32_16x16x32_bf16 v[136:139], v[224:227], v[144:147], v[136:139]\n"
      "ds_write_b32 v8, v141 offset:512\n"
      "v_mfma_f32_16x16x32_bf16 v[136:139], v[228:231], v[164:167], v[136:139]\n"
      "v_mfma_f32_16x16x32_bf16 v[168:171], v[248:251], v[144:147], v[168:171]\n"
      "v_mfma_f32_16x16x32_bf16 v[168:171], v[252:255], v[164:167], v[168:171]\n"
      "s_nop 0\n"
      "v_mul_f32 v165, v156, v156\n"
      "v_add_f32 v164, v156, v157\n"
      "v_fmac_f32 v165, v157, v157\n"
      "v_add_f32 v164, v164, v158\n"
      "v_fmac_f32 v165, v158, v158\n"
      "v_add_f32 v164, v164, v159\n"
      "v_fmac_f32 v165, v159, v159\n"
      "v_add_f32 v164, v164, v136\n"
      "v_fmac_f32 v165, v136, v136\n"
      "v_add_f32 v164, v164, v137\n"
      "v_fmac_f32 v165, v137, v137\n"
      "v_add_f32 v164, v164, v138\n"
      "v_fmac_f32 v165, v138, v138\n"
      "v_add_f32 v164, v164, v139\n"
      "v_fmac_f32 v165, v139, v139\n"
      "v_add_f32 v164, v164, v168\n"
      "v_fmac_f32 v165, v168, v168\n"
      "v_add_f32 v164, v164, v169\n"
      "v_fmac_f32 v165, v169, v169\n"
      "v_add_f32 v164, v164, v170\n"
      "v_fmac_f32 v165, v170, v170\n"
      "v_add_f32 v164, v164, v171\n"
      "v_fmac_f32 v165, v171, v171\n"
      "s_nop 0\n"
      "s_nop 0\n"
      "v_permlane16_swap_b32 v164, v165\n"
      "v_add_f32 v164, v164, v165\n"
      "s_nop 0\n"
      "s_nop 0\n"
      "v_permlane32_swap_b32 v172, v164\n"
      "v_add_f32 v172, v172, v164\n"
      "ds_write_b32 v8, v172 offset:4096\n"
      "s_waitcnt lgkmcnt(0)\n"
      "s_barrier\n"
      "ds_read_b128 v[164:167], v9\n"
      "ds_read_b128 v[144:147], v9 offset:1024\n"
      "ds_read_b128 v[188:191], v9 offset:4096\n"
      "ds_read_b128 v[184:187], v9 offset:5120\n"
      "ds_read_b128 v[172:175], v7\n"
      "ds_read_b128 v[132:135], v7 offset:4096\n"
      "ds_read_b128 v[160:163], v7 offset:8192\n"
      "s_mul_i32 s67, s40, 3072\n"
      "v_subrev_u32 v141, 151936, v9\n"
      "v_add_u32 v142, s67, v141\n"
      "v_lshrrev_b32 v141, 4, v142\n"
      "ds_read_b128 v[200:203], v7 offset:12288\n"
      "v_add_u32 v85, 0, v141\n"
      "v_add_u32 v140, 64, v141\n"
      "v_add_u32 v196, 128, v141\n"
      "v_mul_u32_u24 v141, 2731, v85\n"
      "s_waitcnt lgkmcnt(7)\n"
      "v_add_f32 v164, v164, v166\n"
      "v_add_f32 v165, v165, v167\n"
      "v_mul_u32_u24 v167, 2731, v140\n"
      "s_waitcnt lgkmcnt(6)\n"
      "v_add_f32 v144, v144, v146\n"
      "v_add_f32 v145, v145, v147\n"
      "v_add_f32 v164, v164, v144\n"
      "v_add_f32 v165, v165, v145\n"
      "v_fma_f32 v144, -v164, v164, s52\n"
      "v_fmac_f32 v144, 0x43400000, v165\n"
      "v_max_f32 v144, s52, v144\n"
      "v_rsq_f32 v144, v144\n"
      "s_waitcnt lgkmcnt(4)\n"
      "v_add_f32 v188, v188, v190\n"
      "v_add_f32 v184, v184, v186\n"
      "v_add_f32 v189, v189, v191\n"
      "v_add_f32 v185, v185, v187\n"
      "v_add_f32 v188, v188, v184\n"
      "v_add_f32_e64 v189, v189, v185\n"
      "v_fma_f32 v184, -v188, v188, s52\n"
      "v_fmac_f32 v184, 0x43400000, v189\n"
      "v_mul_f32 v189, 0x43400000, v144\n"
      "v_mul_f32 v185, -v164, v144\n"
      "v_max_f32 v184, s52, v184\n"
      "v_mov_b32 v144, v189\n"
      "v_mov_b32 v164, v185\n"
      "v_rsq_f32 v184, v184\n"
      "v_permlane32_swap_b32 v189, v144\n"
      "v_permlane32_swap_b32 v185, v164\n"
      "v_mul_f32 v187, 0x43400000, v184\n"
      "v_mul_f32 v191, -v188, v184\n"
      "v_mov_b32 v184, v187\n"
      "v_mov_b32 v188, v191\n"
      "v_mov_b32 v186, v189\n"
      "v_mov_b32 v190, v185\n"
      "v_mul_u32_u24 v165, 2731, v196\n"
      "ds_read_b128 v[152:155], v7 offset:16384\n"
      "v_and_b32 v145, 0x3fffffff, v37\n"
      "v_ashrrev_i32 v147, 31, v37\n"
      "v_lshrrev_b32 v141, 16, v141\n"
      "v_lshrrev_b32 v167, 16, v167\n"
      "v_lshrrev_b32 v165, 16, v165\n"
      "v_permlane32_swap_b32 v187, v184\n"
      "v_permlane32_swap_b32 v191, v188\n"
      "v_permlane16_swap_b32 v189, v186\n"
      "v_permlane16_swap_b32 v185, v190\n"
      "v_add_u32 v145, 1179648, v145\n"
      "v_and_b32 v147, 100270464, v147\n"
      "v_mul_u32_u24 v188, 24, v141\n"
      "v_mul_u32_u24 v184, 24, v167\n"
      "v_mul_u32_u24 v146, 24, v165\n"
      "v_sub_u32 v145, v145, v147\n"
      "v_sub_u32 v188, v85, v188\n"
      "v_sub_u32 v184, v140, v184\n"
      "v_sub_u32 v146, v196, v146\n"
      "v_mov_b32 v166, v187\n"
      "v_mov_b32_e64 v197, v191\n"
      "v_fma_f32 v96, v96, v189, v185\n"
      "v_fma_f32 v97, v97, v189, v185\n"
      "v_bfe_i32 v147, v37, 30, 1\n"
      "v_and_b32 v85, 7, v141\n"
      "v_and_b32 v140, 7, v167\n"
      "v_and_b32_e64 v196, 7, v165\n"
      "v_fma_f32 v108, v108, v186, v190\n"
      "v_fma_f32 v109, v109, v186, v190\n"
      "v_cvt_pk_bf16_f32 v96, v96, v97\n"
      "v_fma_f32 v98, v98, v189, v185\n"
      "v_fma_f32 v99, v99, v189, v185\n"
      "v_fma_f32 v104, v104, v189, v185\n"
      "v_fma_f32 v105, v105, v189, v185\n"
      "v_and_b32 v147, 196224, v147\n"
      "v_lshlrev_b32 v141, 2, v141\n"
      "v_lshlrev_b32 v167, 2, v167\n"
      "v_lshlrev_b32_e64 v165, 2, v165\n"
      "v_fma_f32 v120, v120, v144, v164\n"
      "v_fma_f32 v121, v121, v144, v164\n"
      "v_cvt_pk_bf16_f32 v108, v108, v109\n"
      "v_fma_f32 v110, v110, v186, v190\n"
      "v_fma_f32 v111, v111, v186, v190\n"
      "v_fma_f32 v116, v116, v186, v190\n"
      "v_fma_f32 v117, v117, v186, v190\n"
      "v_cvt_pk_bf16_f32 v97, v98, v99\n"
      "v_fma_f32 v100, v100, v189, v185\n"
      "v_fma_f32 v101, v101, v189, v185\n"
      "v_fma_f32 v102, v102, v189, v185\n"
      "v_fma_f32 v103, v103, v189, v185\n"
      "v_cvt_pk_bf16_f32 v104, v104, v105\n"
      "v_fma_f32 v106, v106, v189, v185\n"
      "v_fma_f32 v107, v107, v189, v185\n"
      "v_sub_u32 v145, v145, v147\n"
      "v_add_u32 v141, 12, v141\n"
      "v_add_u32 v167, 12, v167\n"
      "v_add_u32 v165, 12, v165\n"
      "v_cvt_pk_bf16_f32 v120, v120, v121\n"
      "v_fma_f32 v122, v122, v144, v164\n"
      "v_fma_f32 v123, v123, v144, v164\n"
      "v_fma_f32 v128, v128, v144, v164\n"
      "v_fma_f32 v129, v129, v144, v164\n"
      "v_cvt_pk_bf16_f32 v109, v110, v111\n"
      "v_fma_f32 v112, v112, v186, v190\n"
      "v_fma_f32 v113, v113, v186, v190\n"
      "v_fma_f32 v114, v114, v186, v190\n"
      "v_fma_f32 v115, v115, v186, v190\n"
      "v_cvt_pk_bf16_f32 v116, v116, v117\n"
      "v_fma_f32 v118, v118, v186, v190\n"
      "v_fma_f32 v119, v119, v186, v190\n"
      "ds_bpermute_b32 v186, v141, v145\n"
      "ds_bpermute_b32 v141, v167, v145\n"
      "ds_bpermute_b32 v167, v165, v145\n"
      "v_cvt_pk_bf16_f32 v98, v100, v101\n"
      "v_cvt_pk_bf16_f32 v99, v102, v103\n"
      "v_cvt_pk_bf16_f32 v105, v106, v107\n"
      "v_permlane16_swap_b32 v187, v166\n"
      "v_permlane16_swap_b32 v191, v197\n"
      "v_cvt_pk_bf16_f32 v121, v122, v123\n"
      "v_fma_f32 v124, v124, v144, v164\n"
      "v_fma_f32 v125, v125, v144, v164\n"
      "v_fma_f32 v126, v126, v144, v164\n"
      "v_fma_f32 v127, v127, v144, v164\n"
      "v_cvt_pk_bf16_f32 v128, v128, v129\n"
      "v_fma_f32 v130, v130, v144, v164\n"
      "v_fma_f32 v131, v131, v144, v164\n"
      "v_cvt_pk_bf16_f32 v110, v112, v113\n"
      "v_cvt_pk_bf16_f32 v111, v114, v115\n"
      "v_cvt_pk_bf16_f32 v117, v118, v119\n"
      "ds_write_b128 v5, v[96:99] offset:18432\n"
      "ds_write_b64 v6, v[104:105] offset:18432\n"
      "v_cvt_pk_bf16_f32 v122, v124, v125\n"
      "v_cvt_pk_bf16_f32 v123, v126, v127\n"
      "v_cvt_pk_bf16_f32 v129, v130, v131\n"
      "ds_write_b128 v5, v[108:111] offset:24576\n"
      "ds_write_b64 v6, v[116:117] offset:24576\n"
      "v_fma_f32 v148, v148, v187, v191\n"
      "v_fma_f32 v149, v149, v187, v191\n"
      "v_fma_f32 v156, v156, v166, v197\n"
      "v_fma_f32 v157, v157, v166, v197\n"
      "ds_write_b128 v5, v[120:123] offset:30720\n"
      "ds_write_b64 v6, v[128:129] offset:30720\n"
      "v_cvt_pk_bf16_f32 v148, v148, v149\n"
      "v_fma_f32 v150, v150, v187, v191\n"
      "v_fma_f32 v151, v151, v187, v191\n"
      "v_cvt_pk_bf16_f32 v156, v156, v157\n"
      "v_fma_f32 v158, v158, v166, v197\n"
      "v_fma_f32 v159, v159, v166, v197\n"
      "v_cvt_pk_bf16_f32 v149, v150, v151\n"
      "v_fma_f32 v192, v192, v187, v191\n"
      "v_fma_f32 v193, v193, v187, v191\n"
      "v_fma_f32 v194, v194, v187, v191\n"
      "v_fma_f32 v195, v195, v187, v191\n"
      "v_cvt_pk_bf16_f32 v157, v158, v159\n"
      "v_fma_f32 v136, v136, v166, v197\n"
      "v_fma_f32 v137, v137, v166, v197\n"
      "v_fma_f32 v138, v138, v166, v197\n"
      "v_fma_f32 v139, v139, v166, v197\n"
      "v_subrev_u32 v128, 73728, v5\n"
      "v_cvt_pk_bf16_f32 v150, v192, v193\n"
      "v_cvt_pk_bf16_f32 v151, v194, v195\n"
      "v_fma_f32 v204, v204, v187, v191\n"
      "v_fma_f32 v205, v205, v187, v191\n"
      "v_cvt_pk_bf16_f32 v158, v136, v137\n"
      "v_cvt_pk_bf16_f32 v159, v138, v139\n"
      "v_fma_f32 v168, v168, v166, v197\n"
      "v_fma_f32 v169, v169, v166, v197\n"
      "v_readlane_b32 s47, v37, s42\n"
      "ds_write_b128 v128, v[148:151]\n"
      "ds_write_b128 v128, v[156:159] offset:6144\n"
      "v_cvt_pk_bf16_f32 v204, v204, v205\n"
      "v_fma_f32 v206, v206, v187, v191\n"
      "v_fma_f32 v207, v207, v187, v191\n"
      "v_cvt_pk_bf16_f32 v168, v168, v169\n"
      "v_fma_f32 v170, v170, v166, v197\n"
      "v_fma_f32 v171, v171, v166, v197\n"
      "v_subrev_u32 v166, 73728, v6\n"
      "v_cvt_pk_bf16_f32 v205, v206, v207\n"
      "v_cvt_pk_bf16_f32 v169, v170, v171\n"
      "s_mov_b32 s48, s47\n"
      "ds_write_b64 v166, v[204:205]\n"
      "ds_write_b64 v166, v[168:169] offset:6144\n"
      "s_cmp_ge_u32 s47, 0x40000000\n"
      "s_cbranch_scc1 L_rare_83_%=\n"
      "L_back_84_%=:\n"
      "s_waitcnt lgkmcnt(3)\n"
      "buffer_store_dwordx4 v[172:175], v20, s[20:23], s48 offen sc1\n"
      "buffer_store_dwordx4 v[132:135], v21, s[20:23], s48 offen sc1\n"
      "buffer_store_dwordx4 v[160:163], v22, s[20:23], s48 offen sc1\n"
      "buffer_store_dwordx4 v[200:203], v23, s[20:23], s48 offen sc1\n"
      "buffer_store_dwordx4 v[152:155], v25, s[20:23], s48 offen sc1\n"
      "v_xor_b32 v188, v188, v85\n"
      "v_xor_b32 v184, v184, v140\n"
      "s_cmp_ge_u32 s47, 0x40000000\n"
      "s_cbranch_scc1 L_rare_85_%=\n"
      "L_back_86_%=:\n"
      "v_xor_b32 v146, v146, v196\n"
      "s_add_u32 s42, s42, 1\n"
      "v_lshl_add_u32 v186, v188, 4, v186\n"
      "v_lshl_add_u32 v141, v184, 4, v141\n"
      "v_lshl_add_u32 v167, v146, 4, v167\n"
      "s_waitcnt lgkmcnt(0)\n"
      "s_barrier\n"
      "ds_read_b128 v[152:155], v7 offset:18432\n"
      "ds_read_b128 v[200:203], v7 offset:22528\n"
      "ds_read_b128 v[160:163], v7 offset:26624\n"
      "ds_read_b128 v[132:135], v7 offset:30720\n"
      "ds_read_b128 v[172:175], v7 offset:34816\n"
      "ds_read_b128 v[168:171], v142\n"
      "ds_read_b128 v[204:207], v142 offset:1024\n"
      "ds_read_b128 v[194:197], v142 offset:2048\n"
      "v_readlane_b32 s47, v37, s42\n"
      "s_nop 0\n"
      "s_mov_b32 s48, s47\n"
      "s_cmp_ge_u32 s47, 0x40000000\n"
      "s_cbranch_scc1 L_rare_87_%=\n"
      "L_back_88_%=:\n"
      "s_waitcnt lgkmcnt(7)\n"
      "buffer_store_dwordx4 v[152:155], v20, s[20:23], s48 offen sc1\n"
      "s_waitcnt lgkmcnt(6)\n"
      "buffer_store_dwordx4 v[200:203], v21, s[20:23], s48 offen sc1\n"
      "s_waitcnt lgkmcnt(5)\n"
      "buffer_store_dwordx4 v[160:163], v22, s[20:23], s48 offen sc1\n"
      "s_waitcnt lgkmcnt(4)\n"
      "buffer_store_dwordx4 v[132:135], v23, s[20:23], s48 offen sc1\n"
      "s_waitcnt lgkmcnt(3)\n"
      "buffer_store_dwordx4 v[172:175], v25, s[20:23], s48 offen sc1\n"
      "s_nop 0\n"
      "s_waitcnt lgkmcnt(2)\n"
      "buffer_store_dwordx4 v[168:171], v186, s[20:23], 0 offen sc1\n"
      "s_waitcnt lgkmcnt(1)\n"
      "buffer_store_dwordx4 v[204:207], v141, s[20:23], 0 offen sc1\n"
      "s_cmp_ge_u32 s47, 0x40000000\n"
      "s_nop 0\n"
      "s_cbranch_scc1 L_rare_89_%=\n"
      "L_back_90_%=:\n"
      "s_waitcnt lgkmcnt(0)\n"
      "buffer_store_dwordx4 v[194:197], v167, s[20:23], 0 offen sc1\n"
      "s_endpgm\n"
      "L_rare_1_%=:\n"
      "s_nop 7\n"
      "s_waitcnt lgkmcnt(0)\n"
      "s_and_b32 s74, s45, 0x3fffffff\n"
      "s_bitcmp1_b32 s45, 31\n"
      "s_cselect_b32 s75, 100270464, 0\n"
      "s_bitcmp1_b32 s45, 30\n"
      "s_cselect_b32 s76, 196224, 0\n"
      "s_mov_b32 s46, 0\n"
      "v_bfe_i32 v105, v36, 0, 1\n"
      "v_bfe_i32 v80, v36, 1, 1\n"
      "v_and_b32 v105, s75, v105\n"
      "v_and_b32 v80, s76, v80\n"
      "v_add_u32 v105, v105, v80\n"
      "v_sub_u32 v105, s74, v105\n"
      "v_add_u32 v16, v16, v105\n"
      "v_bfe_i32 v105, v36, 2, 1\n"
      "v_bfe_i32 v80, v36, 3, 1\n"
      "v_and_b32 v105, s75, v105\n"
      "v_and_b32 v80, s76, v80\n"
      "v_add_u32 v105, v105, v80\n"
      "v_sub_u32 v105, s74, v105\n"
      "v_add_u32 v17, v17, v105\n"
      "v_bfe_i32 v105, v36, 4, 1\n"
      "v_bfe_i32 v80, v36, 5, 1\n"
      "v_and_b32 v105, s75, v105\n"
      "v_and_b32 v80, s76, v80\n"
      "v_add_u32 v105, v105, v80\n"
      "v_sub_u32 v105, s74, v105\n"
      "v_add_u32 v18, v18, v105\n"
      "v_bfe_i32 v105, v36, 6, 1\n"
      "v_bfe_i32 v80, v36, 7, 1\n"
      "v_and_b32 v105, s75, v105\n"
      "v_and_b32 v80, s76, v80\n"
      "v_add_u32 v105, v105, v80\n"
      "v_sub_u32 v105, s74, v105\n"
      "v_add_u32 v19, v19, v105\n"
      "v_bfe_i32 v105, v36, 8, 1\n"
      "v_bfe_i32 v80, v36, 9, 1\n"
      "v_and_b32 v105, s75, v105\n"
      "v_and_b32 v80, s76, v80\n"
      "v_add_u32 v105, v105, v80\n"
      "v_sub_u32 v105, s74, v105\n"
      "v_add_u32 v15, v15, v105\n"
      "s_waitcnt lgkmcnt(0)\n"
      "s_nop 4\n"
      "s_branch L_back_2_%=\n"
      "L_rare_3_%=:\n"
      "s_nop 7\n"
      "s_waitcnt lgkmcnt(0)\n"
      "s_and_b32 s74, s45, 0x3fffffff\n"
      "s_bitcmp1_b32 s45, 31\n"
      "s_cselect_b32 s75, 100270464, 0\n"
      "s_bitcmp1_b32 s45, 30\n"
      "s_cselect_b32 s76, 196224, 0\n"
      "v_bfe_i32 v105, v36, 0, 1\n"
      "v_bfe_i32 v80, v36, 1, 1\n"
      "v_and_b32 v105, s75, v105\n"
      "v_and_b32 v80, s76, v80\n"
      "v_add_u32 v105, v105, v80\n"
      "v_sub_u32 v105, s74, v105\n"
      "v_sub_u32 v16, v16, v105\n"
      "v_bfe_i32 v105, v36, 2, 1\n"
      "v_bfe_i32 v80, v36, 3, 1\n"
      "v_and_b32 v105, s75, v105\n"
      "v_and_b32 v80, s76, v80\n"
      "v_add_u32 v105, v105, v80\n"
      "v_sub_u32 v105, s74, v105\n"
      "v_sub_u32 v17, v17, v105\n"
      "v_bfe_i32 v105, v36, 4, 1\n"
      "v_bfe_i32 v80, v36, 5, 1\n"
      "v_and_b32 v105, s75, v105\n"
      "v_and_b32 v80, s76, v80\n"
      "v_add_u32 v105, v105, v80\n"
      "v_sub_u32 v105, s74, v105\n"
      "v_sub_u32_e64 v18, v18, v105\n"
      "v_bfe_i32 v105, v36, 6, 1\n"
      "v_bfe_i32 v80, v36, 7, 1\n"
      "v_and_b32 v105, s75, v105\n"
      "v_and_b32 v80, s76, v80\n"
      "v_add_u32 v105, v105, v80\n"
      "v_sub_u32 v105, s74, v105\n"
      "v_sub_u32_e64 v19, v19, v105\n"
      "v_bfe_i32 v105, v36, 8, 1\n"
      "v_bfe_i32 v80, v36, 9, 1\n"
      "v_and_b32 v105, s75, v105\n"
      "v_and_b32 v80, s76, v80\n"
      "v_add_u32_e64 v105, v105, v80\n"
      "v_sub_u32 v105, s74, v105\n"
      "v_sub_u32_e64 v15, v15, v105\n"
      "s_waitcnt lgkmcnt(0)\n"
      "s_nop 4\n"
      "s_branch L_back_4_%=\n"
      "L_rare_5_%=:\n"
      "s_nop 7\n"
      "s_waitcnt lgkmcnt(0)\n"
      "s_add_u32 s74, s42, 3\n"
      "v_readlane_b32 s77, v38, s74\n"
      "v_subrev_u32 v100, 151936, v9\n"
      "s_and_b32 s74, s77, -16\n"
      "s_mov_b32 m0, 147072\n"
      "s_nop 0\n"
      "buffer_load_dwordx4 v100, s[24:27], s74 offen  lds\n"
      "s_add_u32 s74, s74, 1024\n"
      "s_mov_b32 m0, 148096\n"
      "s_nop 0\n"
      "buffer_load_dwordx4 v100, s[24:27], s74 offen  lds\n"
      "s_add_u32 s74, s74, 1024\n"
      "s_mov_b32 m0, 149120\n"
      "s_nop 0\n"
      "buffer_load_dwordx4 v100, s[24:27], s74 offen  lds\n"
      "s_add_u32 s74, s74, 1024\n"
      "s_mov_b32 m0, 150144\n"
      "s_nop 0\n"
      "buffer_load_dwordx4 v100, s[24:27], s74 offen  lds\n"
      "s_add_u32 s74, s74, 1024\n"
      "s_mov_b32 m0, 151168\n"
      "s_mov_b32 s72, 0xffffffff\n"
      "s_mov_b32 s73, 0xffff\n"
      "s_mov_b64 exec, s[72:73]\n"
      "buffer_load_dwordx4 v100, s[24:27], s74 offen  lds\n"
      "s_add_u32 s74, s74, 1024\n"
      "s_mov_b64 exec, -1\n"
      "s_and_b32 s77, s77, 15\n"
      "s_waitcnt lgkmcnt(0)\n"
      "s_nop 4\n"
      "s_branch L_back_6_%=\n"
      "L_rare_7_%=:\n"
      "s_nop 7\n"
      "s_waitcnt lgkmcnt(0)\n"
      "s_and_b32 s74, s45, 0x3fffffff\n"
      "s_bitcmp1_b32 s45, 31\n"
      "s_cselect_b32 s75, 100270464, 0\n"
      "s_bitcmp1_b32 s45, 30\n"
      "s_cselect_b32 s76, 196224, 0\n"
      "s_mov_b32 s46, 0\n"
      "v_bfe_i32 v77, v36, 0, 1\n"
      "v_bfe_i32 v81, v36, 1, 1\n"
      "v_and_b32 v77, s75, v77\n"
      "v_and_b32 v81, s76, v81\n"
      "v_add_u32 v77, v77, v81\n"
      "v_sub_u32 v77, s74, v77\n"
      "v_add_u32_e64 v16, v16, v77\n"
      "v_bfe_i32 v77, v36, 2, 1\n"
      "v_bfe_i32 v81, v36, 3, 1\n"
      "v_and_b32 v77, s75, v77\n"
      "v_and_b32 v81, s76, v81\n"
      "v_add_u32 v77, v77, v81\n"
      "v_sub_u32 v77, s74, v77\n"
      "v_add_u32_e64 v17, v17, v77\n"
      "v_bfe_i32 v77, v36, 4, 1\n"
      "v_bfe_i32 v81, v36, 5, 1\n"
      "v_and_b32 v77, s75, v77\n"
      "v_and_b32 v81, s76, v81\n"
      "v_add_u32 v77, v77, v81\n"
      "v_sub_u32 v77, s74, v77\n"
      "v_add_u32 v18, v18, v77\n"
      "v_bfe_i32 v77, v36, 6, 1\n"
      "v_bfe_i32 v81, v36, 7, 1\n"
      "v_and_b32 v77, s75, v77\n"
      "v_and_b32 v81, s76, v81\n"
      "v_add_u32 v77, v77, v81\n"
      "v_sub_u32 v77, s74, v77\n"
      "v_add_u32 v19, v19, v77\n"
      "v_bfe_i32 v77, v36, 8, 1\n"
      "v_bfe_i32 v81, v36, 9, 1\n"
      "v_and_b32 v77, s75, v77\n"
      "v_and_b32 v81, s76, v81\n"
      "v_add_u32 v77, v77, v81\n"
      "v_sub_u32 v77, s74, v77\n"
      "v_add_u32 v15, v15, v77\n"
      "s_waitcnt lgkmcnt(0)\n"
      "s_nop 4\n"
      "s_branch L_back_8_%=\n"
      "L_rare_9_%=:\n"
      "s_nop 7\n"
      "s_waitcnt lgkmcnt(0)\n"
      "s_and_b32 s74, s45, 0x3fffffff\n"
      "s_bitcmp1_b32 s45, 31\n"
      "s_cselect_b32 s75, 100270464, 0\n"
      "s_bitcmp1_b32 s45, 30\n"
      "s_cselect_b32 s76, 196224, 0\n"
      "v_bfe_i32 v81, v36, 0, 1\n"
      "v_bfe_i32 v80, v36, 1, 1\n"
      "v_and_b32 v81, s75, v81\n"
      "v_and_b32 v80, s76, v80\n"
      "v_add_u32 v81, v81, v80\n"
      "v_sub_u32 v81, s74, v81\n"
      "v_sub_u32 v16, v16, v81\n"
      "v_bfe_i32 v81, v36, 2, 1\n"
      "v_bfe_i32 v80, v36, 3, 1\n"
      "v_and_b32 v81, s75, v81\n"
      "v_and_b32 v80, s76, v80\n"
      "v_add_u32 v81, v81, v80\n"
      "v_sub_u32 v81, s74, v81\n"
      "v_sub_u32 v17, v17, v81\n"
      "v_bfe_i32 v81, v36, 4, 1\n"
      "v_bfe_i32 v80, v36, 5, 1\n"
      "v_and_b32 v81, s75, v81\n"
      "v_and_b32 v80, s76, v80\n"
      "v_add_u32 v81, v81, v80\n"
      "v_sub_u32 v81, s74, v81\n"
      "v_sub_u32 v18, v18, v81\n"
      "v_bfe_i32 v81, v36, 6, 1\n"
      "v_bfe_i32 v80, v36, 7, 1\n"
      "v_and_b32 v81, s75, v81\n"
      "v_and_b32 v80, s76, v80\n"
      "v_add_u32 v81, v81, v80\n"
      "v_sub_u32 v81, s74, v81\n"
      "v_sub_u32 v19, v19, v81\n"
      "v_bfe_i32 v81, v36, 8, 1\n"
      "v_bfe_i32 v80, v36, 9, 1\n"
      "v_and_b32 v81, s75, v81\n"
      "v_and_b32 v80, s76, v80\n"
      "v_add_u32 v81, v81, v80\n"
      "v_sub_u32 v81, s74, v81\n"
      "v_sub_u32_e64 v15, v15, v81\n"
      "s_waitcnt lgkmcnt(0)\n"
      "s_nop 4\n"
      "s_branch L_back_10_%=\n"
      "L_rare_12_%=:\n"
      "s_nop 7\n"
      "s_waitcnt lgkmcnt(0)\n"
      "s_cmp_eq_u64 s[56:57], 0\n"
      "s_cbranch_scc1 L_nomask_11_%=\n"
      "s_waitcnt vmcnt(7)\n"
      "v_subrev_u32 v82, 151936, v9\n"
      "v_bfe_u32 v90, v82, 4, 4\n"
      "v_mul_u32_u24 v90, 98, v90\n"
      "v_lshrrev_b32_e64 v82, 8, v82\n"
      "v_lshl_add_u32 v90, v82, 3, v90\n"
      "s_add_u32 s74, s77, 147072\n"
      "v_add_u32 v90, s74, v90\n"
      "v_and_b32 v86, 2, v90\n"
      "v_and_b32_e64 v90, -4, v90\n"
      "ds_read2_b32 v[96:97], v90 offset0:0 offset1:1\n"
      "ds_read2_b32 v[100:101], v90 offset0:8 offset1:9\n"
      "ds_read2_b32 v[104:105], v90 offset0:16 offset1:17\n"
      "ds_read2_b32 v[88:89], v90 offset0:2 offset1:10\n"
      "ds_read2_b32 v[92:93], v90 offset0:18 offset1:24\n"
      "s_waitcnt lgkmcnt(4)\n"
      "v_alignbyte_b32 v96, v97, v96, v86\n"
      "s_waitcnt lgkmcnt(1)\n"
      "v_alignbyte_b32 v97, v88, v97, v86\n"
      "v_lshlrev_b32 v82, 16, v96\n"
      "v_fmac_f32 v112, s53, v82\n"
      "v_and_b32 v82, 0xffff0000, v96\n"
      "v_fmac_f32 v113, s53, v82\n"
      "v_lshlrev_b32 v82, 16, v97\n"
      "v_fmac_f32 v114, s53, v82\n"
      "v_and_b32 v82, 0xffff0000, v97\n"
      "v_fmac_f32 v115, s53, v82\n"
      "v_alignbyte_b32 v100, v101, v100, v86\n"
      "v_alignbyte_b32 v101, v89, v101, v86\n"
      "v_lshlrev_b32 v82, 16, v100\n"
      "v_fmac_f32 v144, s53, v82\n"
      "v_and_b32 v82, 0xffff0000, v100\n"
      "v_fmac_f32 v145, s53, v82\n"
      "v_lshlrev_b32 v82, 16, v101\n"
      "v_fmac_f32 v146, s53, v82\n"
      "v_and_b32 v82, 0xffff0000, v101\n"
      "v_fmac_f32 v147, s53, v82\n"
      "v_alignbyte_b32 v104, v105, v104, v86\n"
      "s_waitcnt lgkmcnt(0)\n"
      "v_alignbyte_b32 v105, v92, v105, v86\n"
      "v_lshlrev_b32 v82, 16, v104\n"
      "v_fmac_f32 v202, s53, v82\n"
      "v_and_b32 v82, 0xffff0000, v104\n"
      "v_fmac_f32 v203, s53, v82\n"
      "v_lshlrev_b32 v82, 16, v105\n"
      "v_fmac_f32 v204, s53, v82\n"
      "v_and_b32 v82, 0xffff0000, v105\n"
      "v_fmac_f32 v205, s53, v82\n"
      "v_alignbyte_b32 v82, v93, v93, v86\n"
      "v_lshlrev_b32 v82, 16, v82\n"
      "v_fmac_f32_e64 v178, s53, v82\n"
      "L_nomask_11_%=:\n"
      "v_max3_f32 v90, v112, v113, v114\n"
      "v_max3_f32 v86, v115, v144, v145\n"
      "v_max3_f32 v90, v90, v146, v147\n"
      "v_max3_f32 v86, v86, v202, v203\n"
      "v_max3_f32 v90, v90, v204, v205\n"
      "v_max3_f32 v90, v90, v86, v178\n"
      "v_mov_b32 v82, v90\n"
      "s_nop 1\n"
      "v_permlane16_swap_b32 v90, v82\n"
      "v_max_f32 v90, v90, v82\n"
      "v_mov_b32 v82, v90\n"
      "s_nop 1\n"
      "v_permlane32_swap_b32 v90, v82\n"
      "v_max_f32 v90, v90, v82\n"
      "v_sub_f32 v112, v112, v90\n"
      "v_sub_f32 v113, v113, v90\n"
      "v_sub_f32 v114, v114, v90\n"
      "v_sub_f32 v115, v115, v90\n"
      "v_sub_f32 v144, v144, v90\n"
      "v_sub_f32 v145, v145, v90\n"
      "v_sub_f32 v146, v146, v90\n"
      "v_sub_f32 v147, v147, v90\n"
      "v_sub_f32 v202, v202, v90\n"
      "v_sub_f32 v203, v203, v90\n"
      "v_sub_f32 v204, v204, v90\n"
      "v_sub_f32 v205, v205, v90\n"
      "v_sub_f32_e64 v178, v178, v90\n"
      "s_waitcnt lgkmcnt(0)\n"
      "s_nop 4\n"
      "s_branch L_back_13_%=\n"
      "L_rare_15_%=:\n"
      "s_nop 7\n"
      "s_waitcnt lgkmcnt(0)\n"
      "s_cmp_eq_u64 s[56:57], 0\n"
      "s_cbranch_scc1 L_nomask_14_%=\n"
      "s_waitcnt vmcnt(7)\n"
      "v_subrev_u32 v207, 151936, v9\n"
      "v_bfe_u32 v119, v207, 4, 4\n"
      "v_mul_u32_u24 v119, 98, v119\n"
      "v_lshrrev_b32 v207, 8, v207\n"
      "v_lshl_add_u32 v119, v207, 3, v119\n"
      "s_add_u32 s74, s77, 148640\n"
      "v_add_u32 v119, s74, v119\n"
      "v_and_b32 v94, 2, v119\n"
      "v_and_b32 v119, -4, v119\n"
      "ds_read2_b32 v[92:93], v119 offset0:0 offset1:1\n"
      "ds_read2_b32 v[120:121], v119 offset0:8 offset1:9\n"
      "ds_read2_b32 v[186:187], v119 offset0:16 offset1:17\n"
      "ds_read2_b32 v[208:209], v119 offset0:2 offset1:10\n"
      "ds_read2_b32 v[150:151], v119 offset0:18 offset1:24\n"
      "s_waitcnt lgkmcnt(4)\n"
      "v_alignbyte_b32 v92, v93, v92, v94\n"
      "s_waitcnt lgkmcnt(1)\n"
      "v_alignbyte_b32 v93, v208, v93, v94\n"
      "v_lshlrev_b32 v207, 16, v92\n"
      "v_fmac_f32 v122, s53, v207\n"
      "v_and_b32 v207, 0xffff0000, v92\n"
      "v_fmac_f32 v123, s53, v207\n"
      "v_lshlrev_b32 v207, 16, v93\n"
      "v_fmac_f32 v124, s53, v207\n"
      "v_and_b32 v207, 0xffff0000, v93\n"
      "v_fmac_f32 v125, s53, v207\n"
      "v_alignbyte_b32 v120, v121, v120, v94\n"
      "v_alignbyte_b32 v121, v209, v121, v94\n"
      "v_lshlrev_b32 v207, 16, v120\n"
      "v_fmac_f32 v134, s53, v207\n"
      "v_and_b32 v207, 0xffff0000, v120\n"
      "v_fmac_f32 v135, s53, v207\n"
      "v_lshlrev_b32 v207, 16, v121\n"
      "v_fmac_f32_e64 v136, s53, v207\n"
      "v_and_b32 v207, 0xffff0000, v121\n"
      "v_fmac_f32 v137, s53, v207\n"
      "v_alignbyte_b32 v186, v187, v186, v94\n"
      "s_waitcnt lgkmcnt(0)\n"
      "v_alignbyte_b32 v187, v150, v187, v94\n"
      "v_lshlrev_b32 v207, 16, v186\n"
      "v_fmac_f32 v198, s53, v207\n"
      "v_and_b32 v207, 0xffff0000, v186\n"
      "v_fmac_f32 v199, s53, v207\n"
      "v_lshlrev_b32 v207, 16, v187\n"
      "v_fmac_f32_e64 v200, s53, v207\n"
      "v_and_b32 v207, 0xffff0000, v187\n"
      "v_fmac_f32 v201, s53, v207\n"
      "v_alignbyte_b32 v207, v151, v151, v94\n"
      "v_lshlrev_b32 v207, 16, v207\n"
      "v_fmac_f32_e64 v206, s53, v207\n"
      "L_nomask_14_%=:\n"
      "v_max3_f32 v119, v122, v123, v124\n"
      "v_max3_f32 v94, v125, v134, v135\n"
      "v_max3_f32 v119, v119, v136, v137\n"
      "v_max3_f32 v94, v94, v198, v199\n"
      "v_max3_f32 v119, v119, v200, v201\n"
      "v_max3_f32 v119, v119, v94, v206\n"
      "v_mov_b32 v207, v119\n"
      "s_nop 1\n"
      "v_permlane16_swap_b32 v119, v207\n"
      "v_max_f32 v119, v119, v207\n"
      "v_mov_b32 v207, v119\n"
      "s_nop 1\n"
      "v_permlane32_swap_b32 v119, v207\n"
      "v_max_f32 v119, v119, v207\n"
      "v_sub_f32 v122, v122, v119\n"
      "v_sub_f32 v123, v123, v119\n"
      "v_sub_f32 v124, v124, v119\n"
      "v_sub_f32 v125, v125, v119\n"
      "v_sub_f32 v134, v134, v119\n"
      "v_sub_f32 v135, v135, v119\n"
      "v_sub_f32 v136, v136, v119\n"
      "v_sub_f32 v137, v137, v119\n"
      "v_sub_f32 v198, v198, v119\n"
      "v_sub_f32 v199, v199, v119\n"
      "v_sub_f32 v200, v200, v119\n"
      "v_sub_f32 v201, v201, v119\n"
      "v_sub_f32 v206, v206, v119\n"
      "s_waitcnt lgkmcnt(0)\n"
      "s_nop 4\n"
      "s_branch L_back_16_%=\n"
      "L_rare_18_%=:\n"
      "s_nop 7\n"
      "s_waitcnt lgkmcnt(0)\n"
      "s_cmp_eq_u64 s[56:57], 0\n"
      "s_cbranch_scc1 L_nomask_17_%=\n"
      "s_waitcnt vmcnt(7)\n"
      "v_subrev_u32 v135, 151936, v9\n"
      "v_bfe_u32 v90, v135, 4, 4\n"
      "v_mul_u32_u24 v90, 98, v90\n"
      "v_lshrrev_b32 v135, 8, v135\n"
      "v_lshl_add_u32 v90, v135, 3, v90\n"
      "s_add_u32 s74, s77, 150208\n"
      "v_add_u32 v90, s74, v90\n"
      "v_and_b32 v119, 2, v90\n"
      "v_and_b32_e64 v90, -4, v90\n"
      "ds_read2_b32 v[88:89], v90 offset0:0 offset1:1\n"
      "ds_read2_b32 v[120:121], v90 offset0:8 offset1:9\n"
      "ds_read2_b32 v[136:137], v90 offset0:16 offset1:17\n"
      "ds_read2_b32 v[162:163], v90 offset0:2 offset1:10\n"
      "ds_read2_b32 v[164:165], v90 offset0:18 offset1:24\n"
      "s_waitcnt lgkmcnt(4)\n"
      "v_alignbyte_b32 v88, v89, v88, v119\n"
      "s_waitcnt lgkmcnt(1)\n"
      "v_alignbyte_b32 v89, v162, v89, v119\n"
      "v_lshlrev_b32 v135, 16, v88\n"
      "v_fmac_f32 v112, s53, v135\n"
      "v_and_b32 v135, 0xffff0000, v88\n"
      "v_fmac_f32 v113, s53, v135\n"
      "v_lshlrev_b32 v135, 16, v89\n"
      "v_fmac_f32_e64 v114, s53, v135\n"
      "v_and_b32 v135, 0xffff0000, v89\n"
      "v_fmac_f32 v115, s53, v135\n"
      "v_alignbyte_b32 v120, v121, v120, v119\n"
      "v_alignbyte_b32 v121, v163, v121, v119\n"
      "v_lshlrev_b32 v135, 16, v120\n"
      "v_fmac_f32 v194, s53, v135\n"
      "v_and_b32 v135, 0xffff0000, v120\n"
      "v_fmac_f32 v195, s53, v135\n"
      "v_lshlrev_b32 v135, 16, v121\n"
      "v_fmac_f32 v196, s53, v135\n"
      "v_and_b32 v135, 0xffff0000, v121\n"
      "v_fmac_f32 v197, s53, v135\n"
      "v_alignbyte_b32 v136, v137, v136, v119\n"
      "s_waitcnt lgkmcnt(0)\n"
      "v_alignbyte_b32 v137, v164, v137, v119\n"
      "v_lshlrev_b32 v135, 16, v136\n"
      "v_fmac_f32 v168, s53, v135\n"
      "v_and_b32 v135, 0xffff0000, v136\n"
      "v_fmac_f32 v169, s53, v135\n"
      "v_lshlrev_b32 v135, 16, v137\n"
      "v_fmac_f32 v170, s53, v135\n"
      "v_and_b32 v135, 0xffff0000, v137\n"
      "v_fmac_f32 v171, s53, v135\n"
      "v_alignbyte_b32 v135, v165, v165, v119\n"
      "v_lshlrev_b32 v135, 16, v135\n"
      "v_fmac_f32 v144, s53, v135\n"
      "L_nomask_17_%=:\n"
      "v_max3_f32 v90, v112, v113, v114\n"
      "v_max3_f32 v119, v115, v194, v195\n"
      "v_max3_f32 v90, v90, v196, v197\n"
      "v_max3_f32 v119, v119, v168, v169\n"
      "v_max3_f32 v90, v90, v170, v171\n"
      "v_max3_f32 v90, v90, v119, v144\n"
      "v_mov_b32 v135, v90\n"
      "s_nop 1\n"
      "v_permlane16_swap_b32 v90, v135\n"
      "v_max_f32 v90, v90, v135\n"
      "v_mov_b32 v135, v90\n"
      "s_nop 1\n"
      "v_permlane32_swap_b32 v90, v135\n"
      "v_max_f32 v90, v90, v135\n"
      "v_sub_f32 v112, v112, v90\n"
      "v_sub_f32 v113, v113, v90\n"
      "v_sub_f32 v114, v114, v90\n"
      "v_sub_f32 v115, v115, v90\n"
      "v_sub_f32 v194, v194, v90\n"
      "v_sub_f32 v195, v195, v90\n"
      "v_sub_f32 v196, v196, v90\n"
      "v_sub_f32 v197, v197, v90\n"
      "v_sub_f32 v168, v168, v90\n"
      "v_sub_f32 v169, v169, v90\n"
      "v_sub_f32 v170, v170, v90\n"
      "v_sub_f32 v171, v171, v90\n"
      "v_sub_f32 v144, v144, v90\n"
      "s_waitcnt lgkmcnt(0)\n"
      "s_nop 4\n"
      "s_branch L_back_19_%=\n"
      "L_rare_21_%=:\n"
      "s_nop 7\n"
      "s_waitcnt lgkmcnt(0)\n"
      "s_cmp_eq_u64 s[56:57], 0\n"
      "s_cbranch_scc1 L_nomask_20_%=\n"
      "s_waitcnt vmcnt(7)\n"
      "v_subrev_u32 v125, 151936, v9\n"
      "v_lshrrev_b32 v125, 3, v125\n"
      "v_min_u32 v125, 96, v125\n"
      "s_add_u32 s74, s77, 151776\n"
      "v_add_u32 v125, s74, v125\n"
      "ds_read_u16 v125, v125\n"
      "s_waitcnt lgkmcnt(0)\n"
      "v_lshlrev_b32_e64 v125, 16, v125\n"
      "v_fmac_f32_e64 v122, s53, v125\n"
      "L_nomask_20_%=:\n"
      "s_nop 1\n"
      "v_max_f32_dpp v126, v122, v122 row_ror:8 row_mask:0xf bank_mask:0xf\n"
      "s_nop 1\n"
      "v_max_f32_dpp v126, v126, v126 row_ror:4 row_mask:0xf bank_mask:0xf\n"
      "s_nop 1\n"
      "v_max_f32_dpp v126, v126, v126 row_ror:2 row_mask:0xf bank_mask:0xf\n"
      "s_nop 1\n"
      "v_max_f32_dpp v126, v126, v126 row_ror:1 row_mask:0xf bank_mask:0xf\n"
      "s_nop 1\n"
      "v_max_f32_dpp v126, v126, v126 row_bcast:15 row_mask:0xa bank_mask:0xf\n"
      "s_nop 1\n"
      "v_max_f32_dpp v126, v126, v126 row_bcast:31 row_mask:0xc bank_mask:0xf\n"
      "s_nop 0\n"
      "v_readlane_b32 s74, v126, 63\n"
      "s_nop 1\n"
      "v_subrev_f32 v122, s74, v122\n"
      "s_waitcnt lgkmcnt(0)\n"
      "s_nop 4\n"
      "s_branch L_back_22_%=\n"
      "L_rare_23_%=:\n"
      "s_nop 7\n"
      "s_waitcnt lgkmcnt(0)\n"
      "s_add_u32 s74, s42, 3\n"
      "v_readlane_b32 s77, v38, s74\n"
      "v_subrev_u32 v170, 151936, v9\n"
      "s_and_b32 s74, s77, -16\n"
      "s_mov_b32 m0, 147072\n"
      "s_nop 0\n"
      "buffer_load_dwordx4 v170, s[24:27], s74 offen  lds\n"
      "s_add_u32 s74, s74, 1024\n"
      "s_mov_b32 m0, 148096\n"
      "s_nop 0\n"
      "buffer_load_dwordx4 v170, s[24:27], s74 offen  lds\n"
      "s_add_u32 s74, s74, 1024\n"
      "s_mov_b32 m0, 149120\n"
      "s_nop 0\n"
      "buffer_load_dwordx4 v170, s[24:27], s74 offen  lds\n"
      "s_add_u32 s74, s74, 1024\n"
      "s_mov_b32 m0, 150144\n"
      "s_nop 0\n"
      "buffer_load_dwordx4 v170, s[24:27], s74 offen  lds\n"
      "s_add_u32 s74, s74, 1024\n"
      "s_mov_b32 m0, 151168\n"
      "s_mov_b32 s72, 0xffffffff\n"
      "s_mov_b32 s73, 0xffff\n"
      "s_mov_b64 exec, s[72:73]\n"
      "buffer_load_dwordx4 v170, s[24:27], s74 offen  lds\n"
      "s_add_u32 s74, s74, 1024\n"
      "s_mov_b64 exec, -1\n"
      "s_and_b32 s77, s77, 15\n"
      "s_waitcnt lgkmcnt(0)\n"
      "s_nop 4\n"
      "s_branch L_back_24_%=\n"
      "L_rare_25_%=:\n"
      "s_nop 7\n"
      "s_waitcnt lgkmcnt(0)\n"
      "s_and_b32 s74, s45, 0x3fffffff\n"
      "s_bitcmp1_b32 s45, 31\n"
      "s_cselect_b32 s75, 100270464, 0\n"
      "s_bitcmp1_b32 s45, 30\n"
      "s_cselect_b32 s76, 196224, 0\n"
      "s_mov_b32 s46, 0\n"
      "v_bfe_i32 v170, v36, 0, 1\n"
      "v_bfe_i32 v204, v36, 1, 1\n"
      "v_and_b32 v170, s75, v170\n"
      "v_and_b32 v204, s76, v204\n"
      "v_add_u32 v170, v170, v204\n"
      "v_sub_u32 v170, s74, v170\n"
      "v_add_u32_e64 v16, v16, v170\n"
      "v_bfe_i32 v170, v36, 2, 1\n"
      "v_bfe_i32 v204, v36, 3, 1\n"
      "v_and_b32 v170, s75, v170\n"
      "v_and_b32 v204, s76, v204\n"
      "v_add_u32 v170, v170, v204\n"
      "v_sub_u32 v170, s74, v170\n"
      "v_add_u32_e64 v17, v17, v170\n"
      "v_bfe_i32 v170, v36, 4, 1\n"
      "v_bfe_i32 v204, v36, 5, 1\n"
      "v_and_b32 v170, s75, v170\n"
      "v_and_b32 v204, s76, v204\n"
      "v_add_u32 v170, v170, v204\n"
      "v_sub_u32 v170, s74, v170\n"
      "v_add_u32 v18, v18, v170\n"
      "v_bfe_i32 v170, v36, 6, 1\n"
      "v_bfe_i32 v204, v36, 7, 1\n"
      "v_and_b32 v170, s75, v170\n"
      "v_and_b32 v204, s76, v204\n"
      "v_add_u32 v170, v170, v204\n"
      "v_sub_u32 v170, s74, v170\n"
      "v_add_u32 v19, v19, v170\n"
      "v_bfe_i32 v170, v36, 8, 1\n"
      "v_bfe_i32 v204, v36, 9, 1\n"
      "v_and_b32 v170, s75, v170\n"
      "v_and_b32 v204, s76, v204\n"
      "v_add_u32 v170, v170, v204\n"
      "v_sub_u32 v170, s74, v170\n"
      "v_add_u32 v15, v15, v170\n"
      "s_waitcnt lgkmcnt(0)\n"
      "s_nop 4\n"
      "s_branch L_back_26_%=\n"
      "L_rare_27_%=:\n"
      "s_nop 7\n"
      "s_waitcnt lgkmcnt(0)\n"
      "s_and_b32 s74, s45, 0x3fffffff\n"
      "s_bitcmp1_b32 s45, 31\n"
      "s_cselect_b32 s75, 100270464, 0\n"
      "s_bitcmp1_b32 s45, 30\n"
      "s_cselect_b32 s76, 196224, 0\n"
      "v_bfe_i32 v188, v36, 0, 1\n"
      "v_bfe_i32 v186, v36, 1, 1\n"
      "v_and_b32 v188, s75, v188\n"
      "v_and_b32 v186, s76, v186\n"
      "v_add_u32 v188, v188, v186\n"
      "v_sub_u32 v188, s74, v188\n"
      "v_sub_u32 v16, v16, v188\n"
      "v_bfe_i32 v188, v36, 2, 1\n"
      "v_bfe_i32 v186, v36, 3, 1\n"
      "v_and_b32 v188, s75, v188\n"
      "v_and_b32 v186, s76, v186\n"
      "v_add_u32 v188, v188, v186\n"
      "v_sub_u32 v188, s74, v188\n"
      "v_sub_u32 v17, v17, v188\n"
      "v_bfe_i32 v188, v36, 4, 1\n"
      "v_bfe_i32 v186, v36, 5, 1\n"
      "v_and_b32 v188, s75, v188\n"
      "v_and_b32 v186, s76, v186\n"
      "v_add_u32 v188, v188, v186\n"
      "v_sub_u32 v188, s74, v188\n"
      "v_sub_u32 v18, v18, v188\n"
      "v_bfe_i32 v188, v36, 6, 1\n"
      "v_bfe_i32 v186, v36, 7, 1\n"
      "v_and_b32 v188, s75, v188\n"
      "v_and_b32 v186, s76, v186\n"
      "v_add_u32 v188, v188, v186\n"
      "v_sub_u32 v188, s74, v188\n"
      "v_sub_u32 v19, v19, v188\n"
      "v_bfe_i32 v188, v36, 8, 1\n"
      "v_bfe_i32 v186, v36, 9, 1\n"
      "v_and_b32 v188, s75, v188\n"
      "v_and_b32 v186, s76, v186\n"
      "v_add_u32 v188, v188, v186\n"
      "v_sub_u32 v188, s74, v188\n"
      "v_sub_u32 v15, v15, v188\n"
      "s_waitcnt lgkmcnt(0)\n"
      "s_nop 4\n"
      "s_branch L_back_28_%=\n"
      "L_rare_29_%=:\n"
      "s_nop 7\n"
      "s_waitcnt lgkmcnt(0)\n"
      "s_and_b32 s74, s47, 0x3fffffff\n"
      "s_bitcmp1_b32 s47, 31\n"
      "s_cselect_b32 s75, 100270464, 0\n"
      "s_bitcmp1_b32 s47, 30\n"
      "s_cselect_b32 s76, 196224, 0\n"
      "s_mov_b32 s48, 0\n"
      "v_bfe_i32 v186, v36, 10, 1\n"
      "v_bfe_i32 v188, v36, 11, 1\n"
      "v_and_b32 v186, s75, v186\n"
      "v_and_b32 v188, s76, v188\n"
      "v_add_u32 v186, v186, v188\n"
      "v_sub_u32 v186, s74, v186\n"
      "v_add_u32 v20, v20, v186\n"
      "v_bfe_i32 v186, v36, 12, 1\n"
      "v_bfe_i32 v188, v36, 13, 1\n"
      "v_and_b32 v186, s75, v186\n"
      "v_and_b32 v188, s76, v188\n"
      "v_add_u32 v186, v186, v188\n"
      "v_sub_u32 v186, s74, v186\n"
      "v_add_u32_e64 v21, v21, v186\n"
      "v_bfe_i32 v186, v36, 14, 1\n"
      "v_bfe_i32 v188, v36, 15, 1\n"
      "v_and_b32 v186, s75, v186\n"
      "v_and_b32 v188, s76, v188\n"
      "v_add_u32 v186, v186, v188\n"
      "v_sub_u32 v186, s74, v186\n"
      "v_add_u32_e64 v22, v22, v186\n"
      "v_bfe_i32 v186, v36, 16, 1\n"
      "v_bfe_i32 v188, v36, 17, 1\n"
      "v_and_b32 v186, s75, v186\n"
      "v_and_b32 v188, s76, v188\n"
      "v_add_u32 v186, v186, v188\n"
      "v_sub_u32 v186, s74, v186\n"
      "v_add_u32 v23, v23, v186\n"
      "v_bfe_i32 v186, v36, 18, 1\n"
      "v_bfe_i32 v188, v36, 19, 1\n"
      "v_and_b32 v186, s75, v186\n"
      "v_and_b32 v188, s76, v188\n"
      "v_add_u32 v186, v186, v188\n"
      "v_sub_u32 v186, s74, v186\n"
      "v_add_u32_e64 v25, v25, v186\n"
      "s_waitcnt lgkmcnt(0)\n"
      "s_nop 4\n"
      "s_branch L_back_30_%=\n"
      "L_rare_31_%=:\n"
      "s_nop 7\n"
      "s_waitcnt lgkmcnt(0)\n"
      "s_and_b32 s74, s47, 0x3fffffff\n"
      "s_bitcmp1_b32 s47, 31\n"
      "s_cselect_b32 s75, 100270464, 0\n"
      "s_bitcmp1_b32 s47, 30\n"
      "s_cselect_b32 s76, 196224, 0\n"
      "v_bfe_i32 v160, v36, 10, 1\n"
      "v_bfe_i32 v163, v36, 11, 1\n"
      "v_and_b32 v160, s75, v160\n"
      "v_and_b32 v163, s76, v163\n"
      "v_add_u32 v160, v160, v163\n"
      "v_sub_u32 v160, s74, v160\n"
      "v_sub_u32 v20, v20, v160\n"
      "v_bfe_i32 v160, v36, 12, 1\n"
      "v_bfe_i32 v163, v36, 13, 1\n"
      "v_and_b32 v160, s75, v160\n"
      "v_and_b32 v163, s76, v163\n"
      "v_add_u32 v160, v160, v163\n"
      "v_sub_u32 v160, s74, v160\n"
      "v_sub_u32 v21, v21, v160\n"
      "v_bfe_i32 v160, v36, 14, 1\n"
      "v_bfe_i32 v163, v36, 15, 1\n"
      "v_and_b32 v160, s75, v160\n"
      "v_and_b32 v163, s76, v163\n"
      "v_add_u32 v160, v160, v163\n"
      "v_sub_u32 v160, s74, v160\n"
      "v_sub_u32 v22, v22, v160\n"
      "v_bfe_i32 v160, v36, 16, 1\n"
      "v_bfe_i32 v163, v36, 17, 1\n"
      "v_and_b32 v160, s75, v160\n"
      "v_and_b32 v163, s76, v163\n"
      "v_add_u32 v160, v160, v163\n"
      "v_sub_u32 v160, s74, v160\n"
      "v_sub_u32 v23, v23, v160\n"
      "v_bfe_i32 v160, v36, 18, 1\n"
      "v_bfe_i32 v163, v36, 19, 1\n"
      "v_and_b32 v160, s75, v160\n"
      "v_and_b32 v163, s76, v163\n"
      "v_add_u32 v160, v160, v163\n"
      "v_sub_u32 v160, s74, v160\n"
      "v_sub_u32_e64 v25, v25, v160\n"
      "s_waitcnt lgkmcnt(0)\n"
      "s_nop 4\n"
      "s_branch L_back_32_%=\n"
      "L_rare_34_%=:\n"
      "s_nop 7\n"
      "s_waitcnt lgkmcnt(0)\n"
      "s_cmp_eq_u64 s[56:57], 0\n"
      "s_cbranch_scc1 L_nomask_33_%=\n"
      "s_waitcnt vmcnt(12)\n"
      "v_subrev_u32 v101, 151936, v9\n"
      "v_bfe_u32 v90, v101, 4, 4\n"
      "v_mul_u32_u24 v90, 98, v90\n"
      "v_lshrrev_b32_e64 v101, 8, v101\n"
      "v_lshl_add_u32 v90, v101, 3, v90\n"
      "s_add_u32 s74, s77, 147072\n"
      "v_add_u32 v90, s74, v90\n"
      "v_and_b32 v88, 2, v90\n"
      "v_and_b32_e64 v90, -4, v90\n"
      "ds_read2_b32 v[102:103], v90 offset0:0 offset1:1\n"
      "ds_read2_b32 v[180:181], v90 offset0:8 offset1:9\n"
      "ds_read2_b32 v[128:129], v90 offset0:16 offset1:17\n"
      "ds_read2_b32 v[184:185], v90 offset0:2 offset1:10\n"
      "ds_read2_b32 v[218:219], v90 offset0:18 offset1:24\n"
      "s_waitcnt lgkmcnt(4)\n"
      "v_alignbyte_b32 v102, v103, v102, v88\n"
      "s_waitcnt lgkmcnt(1)\n"
      "v_alignbyte_b32 v103, v184, v103, v88\n"
      "v_lshlrev_b32 v101, 16, v102\n"
      "v_fmac_f32 v108, s53, v101\n"
      "v_and_b32 v101, 0xffff0000, v102\n"
      "v_fmac_f32 v109, s53, v101\n"
      "v_lshlrev_b32 v101, 16, v103\n"
      "v_fmac_f32 v110, s53, v101\n"
      "v_and_b32 v101, 0xffff0000, v103\n"
      "v_fmac_f32 v111, s53, v101\n"
      "v_alignbyte_b32 v180, v181, v180, v88\n"
      "v_alignbyte_b32 v181, v185, v181, v88\n"
      "v_lshlrev_b32 v101, 16, v180\n"
      "v_fmac_f32 v116, s53, v101\n"
      "v_and_b32 v101, 0xffff0000, v180\n"
      "v_fmac_f32 v117, s53, v101\n"
      "v_lshlrev_b32 v101, 16, v181\n"
      "v_fmac_f32 v118, s53, v101\n"
      "v_and_b32 v101, 0xffff0000, v181\n"
      "v_fmac_f32 v119, s53, v101\n"
      "v_alignbyte_b32 v128, v129, v128, v88\n"
      "s_waitcnt lgkmcnt(0)\n"
      "v_alignbyte_b32 v129, v218, v129, v88\n"
      "v_lshlrev_b32 v101, 16, v128\n"
      "v_fmac_f32 v96, s53, v101\n"
      "v_and_b32 v101, 0xffff0000, v128\n"
      "v_fmac_f32 v97, s53, v101\n"
      "v_lshlrev_b32 v101, 16, v129\n"
      "v_fmac_f32 v98, s53, v101\n"
      "v_and_b32 v101, 0xffff0000, v129\n"
      "v_fmac_f32 v99, s53, v101\n"
      "v_alignbyte_b32 v101, v219, v219, v88\n"
      "v_lshlrev_b32 v101, 16, v101\n"
      "v_fmac_f32_e64 v100, s53, v101\n"
      "L_nomask_33_%=:\n"
      "v_max3_f32 v90, v108, v109, v110\n"
      "v_max3_f32 v88, v111, v116, v117\n"
      "v_max3_f32 v90, v90, v118, v119\n"
      "v_max3_f32 v88, v88, v96, v97\n"
      "v_max3_f32 v90, v90, v98, v99\n"
      "v_max3_f32 v90, v90, v88, v100\n"
      "v_mov_b32 v101, v90\n"
      "s_nop 1\n"
      "v_permlane16_swap_b32 v90, v101\n"
      "v_max_f32 v90, v90, v101\n"
      "v_mov_b32 v101, v90\n"
      "s_nop 1\n"
      "v_permlane32_swap_b32 v90, v101\n"
      "v_max_f32 v90, v90, v101\n"
      "v_sub_f32 v108, v108, v90\n"
      "v_sub_f32 v109, v109, v90\n"
      "v_sub_f32 v110, v110, v90\n"
      "v_sub_f32 v111, v111, v90\n"
      "v_sub_f32 v116, v116, v90\n"
      "v_sub_f32 v117, v117, v90\n"
      "v_sub_f32 v118, v118, v90\n"
      "v_sub_f32 v119, v119, v90\n"
      "v_sub_f32 v96, v96, v90\n"
      "v_sub_f32 v97, v97, v90\n"
      "v_sub_f32 v98, v98, v90\n"
      "v_sub_f32 v99, v99, v90\n"
      "v_sub_f32_e64 v100, v100, v90\n"
      "s_waitcnt lgkmcnt(0)\n"
      "s_nop 4\n"
      "s_branch L_back_35_%=\n"
      "L_rare_37_%=:\n"
      "s_nop 7\n"
      "s_waitcnt lgkmcnt(0)\n"
      "s_cmp_eq_u64 s[56:57], 0\n"
      "s_cbranch_scc1 L_nomask_36_%=\n"
      "s_waitcnt vmcnt(12)\n"
      "v_subrev_u32 v130, 151936, v9\n"
      "v_bfe_u32 v215, v130, 4, 4\n"
      "v_mul_u32_u24 v215, 98, v215\n"
      "v_lshrrev_b32 v130, 8, v130\n"
      "v_lshl_add_u32 v215, v130, 3, v215\n"
      "s_add_u32 s74, s77, 148640\n"
      "v_add_u32 v215, s74, v215\n"
      "v_and_b32 v214, 2, v215\n"
      "v_and_b32 v215, -4, v215\n"
      "ds_read2_b32 v[218:219], v215 offset0:0 offset1:1\n"
      "ds_read2_b32 v[186:187], v215 offset0:8 offset1:9\n"
      "ds_read2_b32 v[216:217], v215 offset0:16 offset1:17\n"
      "ds_read2_b32 v[128:129], v215 offset0:2 offset1:10\n"
      "ds_read2_b32 v[220:221], v215 offset0:18 offset1:24\n"
      "s_waitcnt lgkmcnt(4)\n"
      "v_alignbyte_b32 v218, v219, v218, v214\n"
      "s_waitcnt lgkmcnt(1)\n"
      "v_alignbyte_b32 v219, v128, v219, v214\n"
      "v_lshlrev_b32 v130, 16, v218\n"
      "v_fmac_f32 v202, s53, v130\n"
      "v_and_b32 v130, 0xffff0000, v218\n"
      "v_fmac_f32 v203, s53, v130\n"
      "v_lshlrev_b32 v130, 16, v219\n"
      "v_fmac_f32 v204, s53, v130\n"
      "v_and_b32 v130, 0xffff0000, v219\n"
      "v_fmac_f32 v205, s53, v130\n"
      "v_alignbyte_b32 v186, v187, v186, v214\n"
      "v_alignbyte_b32 v187, v129, v187, v214\n"
      "v_lshlrev_b32 v130, 16, v186\n"
      "v_fmac_f32 v182, s53, v130\n"
      "v_and_b32 v130, 0xffff0000, v186\n"
      "v_fmac_f32 v183, s53, v130\n"
      "v_lshlrev_b32 v130, 16, v187\n"
      "v_fmac_f32_e64 v184, s53, v130\n"
      "v_and_b32 v130, 0xffff0000, v187\n"
      "v_fmac_f32 v185, s53, v130\n"
      "v_alignbyte_b32 v216, v217, v216, v214\n"
      "s_waitcnt lgkmcnt(0)\n"
      "v_alignbyte_b32 v217, v220, v217, v214\n"
      "v_lshlrev_b32 v130, 16, v216\n"
      "v_fmac_f32 v178, s53, v130\n"
      "v_and_b32 v130, 0xffff0000, v216\n"
      "v_fmac_f32 v179, s53, v130\n"
      "v_lshlrev_b32 v130, 16, v217\n"
      "v_fmac_f32_e64 v180, s53, v130\n"
      "v_and_b32 v130, 0xffff0000, v217\n"
      "v_fmac_f32 v181, s53, v130\n"
      "v_alignbyte_b32 v130, v221, v221, v214\n"
      "v_lshlrev_b32 v130, 16, v130\n"
      "v_fmac_f32_e64 v116, s53, v130\n"
      "L_nomask_36_%=:\n"
      "v_max3_f32 v215, v202, v203, v204\n"
      "v_max3_f32 v214, v205, v182, v183\n"
      "v_max3_f32 v215, v215, v184, v185\n"
      "v_max3_f32 v214, v214, v178, v179\n"
      "v_max3_f32 v215, v215, v180, v181\n"
      "v_max3_f32 v215, v215, v214, v116\n"
      "v_mov_b32 v130, v215\n"
      "s_nop 1\n"
      "v_permlane16_swap_b32 v215, v130\n"
      "v_max_f32 v215, v215, v130\n"
      "v_mov_b32 v130, v215\n"
      "s_nop 1\n"
      "v_permlane32_swap_b32 v215, v130\n"
      "v_max_f32 v215, v215, v130\n"
      "v_sub_f32 v202, v202, v215\n"
      "v_sub_f32 v203, v203, v215\n"
      "v_sub_f32 v204, v204, v215\n"
      "v_sub_f32 v205, v205, v215\n"
      "v_sub_f32 v182, v182, v215\n"
      "v_sub_f32 v183, v183, v215\n"
      "v_sub_f32 v184, v184, v215\n"
      "v_sub_f32 v185, v185, v215\n"
      "v_sub_f32 v178, v178, v215\n"
      "v_sub_f32 v179, v179, v215\n"
      "v_sub_f32 v180, v180, v215\n"
      "v_sub_f32 v181, v181, v215\n"
      "v_sub_f32 v116, v116, v215\n"
      "s_waitcnt lgkmcnt(0)\n"
      "s_nop 4\n"
      "s_branch L_back_38_%=\n"
      "L_rare_40_%=:\n"
      "s_nop 7\n"
      "s_waitcnt lgkmcnt(0)\n"
      "s_cmp_eq_u64 s[56:57], 0\n"
      "s_cbranch_scc1 L_nomask_39_%=\n"
      "s_waitcnt vmcnt(12)\n"
      "v_subrev_u32 v176, 151936, v9\n"
      "v_bfe_u32 v116, v176, 4, 4\n"
      "v_mul_u32_u24 v116, 98, v116\n"
      "v_lshrrev_b32 v176, 8, v176\n"
      "v_lshl_add_u32 v116, v176, 3, v116\n"
      "s_add_u32 s74, s77, 150208\n"
      "v_add_u32 v116, s74, v116\n"
      "v_and_b32 v177, 2, v116\n"
      "v_and_b32_e64 v116, -4, v116\n"
      "ds_read2_b32 v[148:149], v116 offset0:0 offset1:1\n"
      "ds_read2_b32 v[114:115], v116 offset0:8 offset1:9\n"
      "ds_read2_b32 v[112:113], v116 offset0:16 offset1:17\n"
      "ds_read2_b32 v[178:179], v116 offset0:2 offset1:10\n"
      "ds_read2_b32 v[146:147], v116 offset0:18 offset1:24\n"
      "s_waitcnt lgkmcnt(4)\n"
      "v_alignbyte_b32 v148, v149, v148, v177\n"
      "s_waitcnt lgkmcnt(1)\n"
      "v_alignbyte_b32 v149, v178, v149, v177\n"
      "v_lshlrev_b32 v176, 16, v148\n"
      "v_fmac_f32 v128, s53, v176\n"
      "v_and_b32 v176, 0xffff0000, v148\n"
      "v_fmac_f32 v129, s53, v176\n"
      "v_lshlrev_b32 v176, 16, v149\n"
      "v_fmac_f32_e64 v130, s53, v176\n"
      "v_and_b32 v176, 0xffff0000, v149\n"
      "v_fmac_f32 v131, s53, v176\n"
      "v_alignbyte_b32 v114, v115, v114, v177\n"
      "v_alignbyte_b32 v115, v179, v115, v177\n"
      "v_lshlrev_b32 v176, 16, v114\n"
      "v_fmac_f32 v220, s53, v176\n"
      "v_and_b32 v176, 0xffff0000, v114\n"
      "v_fmac_f32 v221, s53, v176\n"
      "v_lshlrev_b32 v176, 16, v115\n"
      "v_fmac_f32 v222, s53, v176\n"
      "v_and_b32 v176, 0xffff0000, v115\n"
      "v_fmac_f32 v223, s53, v176\n"
      "v_alignbyte_b32 v112, v113, v112, v177\n"
      "s_waitcnt lgkmcnt(0)\n"
      "v_alignbyte_b32 v113, v146, v113, v177\n"
      "v_lshlrev_b32 v176, 16, v112\n"
      "v_fmac_f32 v156, s53, v176\n"
      "v_and_b32 v176, 0xffff0000, v112\n"
      "v_fmac_f32 v157, s53, v176\n"
      "v_lshlrev_b32 v176, 16, v113\n"
      "v_fmac_f32 v158, s53, v176\n"
      "v_and_b32 v176, 0xffff0000, v113\n"
      "v_fmac_f32 v159, s53, v176\n"
      "v_alignbyte_b32 v176, v147, v147, v177\n"
      "v_lshlrev_b32 v176, 16, v176\n"
      "v_fmac_f32 v144, s53, v176\n"
      "L_nomask_39_%=:\n"
      "v_max3_f32 v116, v128, v129, v130\n"
      "v_max3_f32 v177, v131, v220, v221\n"
      "v_max3_f32 v116, v116, v222, v223\n"
      "v_max3_f32 v177, v177, v156, v157\n"
      "v_max3_f32 v116, v116, v158, v159\n"
      "v_max3_f32 v116, v116, v177, v144\n"
      "v_mov_b32 v176, v116\n"
      "s_nop 1\n"
      "v_permlane16_swap_b32 v116, v176\n"
      "v_max_f32 v116, v116, v176\n"
      "v_mov_b32 v176, v116\n"
      "s_nop 1\n"
      "v_permlane32_swap_b32 v116, v176\n"
      "v_max_f32 v116, v116, v176\n"
      "v_sub_f32 v128, v128, v116\n"
      "v_sub_f32 v129, v129, v116\n"
      "v_sub_f32 v130, v130, v116\n"
      "v_sub_f32 v131, v131, v116\n"
      "v_sub_f32 v220, v220, v116\n"
      "v_sub_f32 v221, v221, v116\n"
      "v_sub_f32 v222, v222, v116\n"
      "v_sub_f32 v223, v223, v116\n"
      "v_sub_f32 v156, v156, v116\n"
      "v_sub_f32 v157, v157, v116\n"
      "v_sub_f32 v158, v158, v116\n"
      "v_sub_f32 v159, v159, v116\n"
      "v_sub_f32 v144, v144, v116\n"
      "s_waitcnt lgkmcnt(0)\n"
      "s_nop 4\n"
      "s_branch L_back_41_%=\n"
      "L_rare_43_%=:\n"
      "s_nop 7\n"
      "s_waitcnt lgkmcnt(0)\n"
      "s_cmp_eq_u64 s[56:57], 0\n"
      "s_cbranch_scc1 L_nomask_42_%=\n"
      "s_waitcnt vmcnt(12)\n"
      "v_subrev_u32 v207, 151936, v9\n"
      "v_lshrrev_b32 v207, 3, v207\n"
      "v_min_u32 v207, 96, v207\n"
      "s_add_u32 s74, s77, 151776\n"
      "v_add_u32 v207, s74, v207\n"
      "ds_read_u16 v207, v207\n"
      "s_waitcnt lgkmcnt(0)\n"
      "v_lshlrev_b32_e64 v207, 16, v207\n"
      "v_fmac_f32_e64 v156, s53, v207\n"
      "L_nomask_42_%=:\n"
      "s_nop 1\n"
      "v_max_f32_dpp v146, v156, v156 row_ror:8 row_mask:0xf bank_mask:0xf\n"
      "s_nop 1\n"
      "v_max_f32_dpp v146, v146, v146 row_ror:4 row_mask:0xf bank_mask:0xf\n"
      "s_nop 1\n"
      "v_max_f32_dpp v146, v146, v146 row_ror:2 row_mask:0xf bank_mask:0xf\n"
      "s_nop 1\n"
      "v_max_f32_dpp v146, v146, v146 row_ror:1 row_mask:0xf bank_mask:0xf\n"
      "s_nop 1\n"
      "v_max_f32_dpp v146, v146, v146 row_bcast:15 row_mask:0xa bank_mask:0xf\n"
      "s_nop 1\n"
      "v_max_f32_dpp v146, v146, v146 row_bcast:31 row_mask:0xc bank_mask:0xf\n"
      "s_nop 0\n"
      "v_readlane_b32 s74, v146, 63\n"
      "s_nop 1\n"
      "v_subrev_f32 v156, s74, v156\n"
      "s_waitcnt lgkmcnt(0)\n"
      "s_nop 4\n"
      "s_branch L_back_44_%=\n"
      "L_rare_45_%=:\n"
      "s_nop 7\n"
      "s_waitcnt lgkmcnt(0)\n"
      "s_add_u32 s74, s42, 3\n"
      "v_readlane_b32 s77, v38, s74\n"
      "v_subrev_u32 v135, 151936, v9\n"
      "s_and_b32 s74, s77, -16\n"
      "s_mov_b32 m0, 147072\n"
      "s_nop 0\n"
      "buffer_load_dwordx4 v135, s[24:27], s74 offen  lds\n"
      "s_add_u32 s74, s74, 1024\n"
      "s_mov_b32 m0, 148096\n"
      "s_nop 0\n"
      "buffer_load_dwordx4 v135, s[24:27], s74 offen  lds\n"
      "s_add_u32 s74, s74, 1024\n"
      "s_mov_b32 m0, 149120\n"
      "s_nop 0\n"
      "buffer_load_dwordx4 v135, s[24:27], s74 offen  lds\n"
      "s_add_u32 s74, s74, 1024\n"
      "s_mov_b32 m0, 150144\n"
      "s_nop 0\n"
      "buffer_load_dwordx4 v135, s[24:27], s74 offen  lds\n"
      "s_add_u32 s74, s74, 1024\n"
      "s_mov_b32 m0, 151168\n"
      "s_mov_b32 s72, 0xffffffff\n"
      "s_mov_b32 s73, 0xffff\n"
      "s_mov_b64 exec, s[72:73]\n"
      "buffer_load_dwordx4 v135, s[24:27], s74 offen  lds\n"
      "s_add_u32 s74, s74, 1024\n"
      "s_mov_b64 exec, -1\n"
      "s_and_b32 s77, s77, 15\n"
      "s_waitcnt lgkmcnt(0)\n"
      "s_nop 4\n"
      "s_branch L_back_46_%=\n"
      "L_rare_47_%=:\n"
      "s_nop 7\n"
      "s_waitcnt lgkmcnt(0)\n"
      "s_and_b32 s74, s45, 0x3fffffff\n"
      "s_bitcmp1_b32 s45, 31\n"
      "s_cselect_b32 s75, 100270464, 0\n"
      "s_bitcmp1_b32 s45, 30\n"
      "s_cselect_b32 s76, 196224, 0\n"
      "s_mov_b32 s46, 0\n"
      "v_bfe_i32 v76, v36, 0, 1\n"
      "v_bfe_i32 v80, v36, 1, 1\n"
      "v_and_b32 v76, s75, v76\n"
      "v_and_b32 v80, s76, v80\n"
      "v_add_u32 v76, v76, v80\n"
      "v_sub_u32 v76, s74, v76\n"
      "v_add_u32_e64 v16, v16, v76\n"
      "v_bfe_i32 v76, v36, 2, 1\n"
      "v_bfe_i32 v80, v36, 3, 1\n"
      "v_and_b32 v76, s75, v76\n"
      "v_and_b32 v80, s76, v80\n"
      "v_add_u32 v76, v76, v80\n"
      "v_sub_u32 v76, s74, v76\n"
      "v_add_u32_e64 v17, v17, v76\n"
      "v_bfe_i32 v76, v36, 4, 1\n"
      "v_bfe_i32 v80, v36, 5, 1\n"
      "v_and_b32 v76, s75, v76\n"
      "v_and_b32 v80, s76, v80\n"
      "v_add_u32 v76, v76, v80\n"
      "v_sub_u32 v76, s74, v76\n"
      "v_add_u32 v18, v18, v76\n"
      "v_bfe_i32 v76, v36, 6, 1\n"
      "v_bfe_i32 v80, v36, 7, 1\n"
      "v_and_b32 v76, s75, v76\n"
      "v_and_b32 v80, s76, v80\n"
      "v_add_u32 v76, v76, v80\n"
      "v_sub_u32 v76, s74, v76\n"
      "v_add_u32 v19, v19, v76\n"
      "v_bfe_i32 v76, v36, 8, 1\n"
      "v_bfe_i32 v80, v36, 9, 1\n"
      "v_and_b32 v76, s75, v76\n"
      "v_and_b32 v80, s76, v80\n"
      "v_add_u32 v76, v76, v80\n"
      "v_sub_u32 v76, s74, v76\n"
      "v_add_u32 v15, v15, v76\n"
      "s_waitcnt lgkmcnt(0)\n"
      "s_nop 4\n"
      "s_branch L_back_48_%=\n"
      "L_rare_49_%=:\n"
      "s_nop 7\n"
      "s_waitcnt lgkmcnt(0)\n"
      "s_and_b32 s74, s45, 0x3fffffff\n"
      "s_bitcmp1_b32 s45, 31\n"
      "s_cselect_b32 s75, 100270464, 0\n"
      "s_bitcmp1_b32 s45, 30\n"
      "s_cselect_b32 s76, 196224, 0\n"
      "v_bfe_i32 v141, v36, 0, 1\n"
      "v_bfe_i32 v142, v36, 1, 1\n"
      "v_and_b32 v141, s75, v141\n"
      "v_and_b32 v142, s76, v142\n"
      "v_add_u32 v141, v141, v142\n"
      "v_sub_u32 v141, s74, v141\n"
      "v_sub_u32 v16, v16, v141\n"
      "v_bfe_i32 v141, v36, 2, 1\n"
      "v_bfe_i32 v142, v36, 3, 1\n"
      "v_and_b32 v141, s75, v141\n"
      "v_and_b32 v142, s76, v142\n"
      "v_add_u32 v141, v141, v142\n"
      "v_sub_u32 v141, s74, v141\n"
      "v_sub_u32 v17, v17, v141\n"
      "v_bfe_i32 v141, v36, 4, 1\n"
      "v_bfe_i32 v142, v36, 5, 1\n"
      "v_and_b32 v141, s75, v141\n"
      "v_and_b32 v142, s76, v142\n"
      "v_add_u32 v141, v141, v142\n"
      "v_sub_u32 v141, s74, v141\n"
      "v_sub_u32 v18, v18, v141\n"
      "v_bfe_i32 v141, v36, 6, 1\n"
      "v_bfe_i32 v142, v36, 7, 1\n"
      "v_and_b32 v141, s75, v141\n"
      "v_and_b32 v142, s76, v142\n"
      "v_add_u32 v141, v141, v142\n"
      "v_sub_u32 v141, s74, v141\n"
      "v_sub_u32 v19, v19, v141\n"
      "v_bfe_i32 v141, v36, 8, 1\n"
      "v_bfe_i32 v142, v36, 9, 1\n"
      "v_and_b32 v141, s75, v141\n"
      "v_and_b32 v142, s76, v142\n"
      "v_add_u32 v141, v141, v142\n"
      "v_sub_u32 v141, s74, v141\n"
      "v_sub_u32 v15, v15, v141\n"
      "s_waitcnt lgkmcnt(0)\n"
      "s_nop 4\n"
      "s_branch L_back_50_%=\n"
      "L_rare_51_%=:\n"
      "s_nop 7\n"
      "s_waitcnt lgkmcnt(0)\n"
      "s_and_b32 s74, s47, 0x3fffffff\n"
      "s_bitcmp1_b32 s47, 31\n"
      "s_cselect_b32 s75, 100270464, 0\n"
      "s_bitcmp1_b32 s47, 30\n"
      "s_cselect_b32 s76, 196224, 0\n"
      "s_mov_b32 s48, 0\n"
      "v_bfe_i32 v141, v36, 10, 1\n"
      "v_bfe_i32 v142, v36, 11, 1\n"
      "v_and_b32 v141, s75, v141\n"
      "v_and_b32 v142, s76, v142\n"
      "v_add_u32 v141, v141, v142\n"
      "v_sub_u32 v141, s74, v141\n"
      "v_add_u32 v20, v20, v141\n"
      "v_bfe_i32 v141, v36, 12, 1\n"
      "v_bfe_i32 v142, v36, 13, 1\n"
      "v_and_b32 v141, s75, v141\n"
      "v_and_b32 v142, s76, v142\n"
      "v_add_u32 v141, v141, v142\n"
      "v_sub_u32 v141, s74, v141\n"
      "v_add_u32 v21, v21, v141\n"
      "v_bfe_i32 v141, v36, 14, 1\n"
      "v_bfe_i32 v142, v36, 15, 1\n"
      "v_and_b32 v141, s75, v141\n"
      "v_and_b32 v142, s76, v142\n"
      "v_add_u32 v141, v141, v142\n"
      "v_sub_u32 v141, s74, v141\n"
      "v_add_u32 v22, v22, v141\n"
      "v_bfe_i32 v141, v36, 16, 1\n"
      "v_bfe_i32 v142, v36, 17, 1\n"
      "v_and_b32 v141, s75, v141\n"
      "v_and_b32 v142, s76, v142\n"
      "v_add_u32 v141, v141, v142\n"
      "v_sub_u32 v141, s74, v141\n"
      "v_add_u32_e64 v23, v23, v141\n"
      "v_bfe_i32 v141, v36, 18, 1\n"
      "v_bfe_i32 v142, v36, 19, 1\n"
      "v_and_b32 v141, s75, v141\n"
      "v_and_b32 v142, s76, v142\n"
      "v_add_u32 v141, v141, v142\n"
      "v_sub_u32 v141, s74, v141\n"
      "v_add_u32 v25, v25, v141\n"
      "s_waitcnt lgkmcnt(0)\n"
      "s_nop 4\n"
      "s_branch L_back_52_%=\n"
      "L_rare_53_%=:\n"
      "s_nop 7\n"
      "s_waitcnt lgkmcnt(0)\n"
      "s_and_b32 s74, s47, 0x3fffffff\n"
      "s_bitcmp1_b32 s47, 31\n"
      "s_cselect_b32 s75, 100270464, 0\n"
      "s_bitcmp1_b32 s47, 30\n"
      "s_cselect_b32 s76, 196224, 0\n"
      "v_bfe_i32 v172, v36, 10, 1\n"
      "v_bfe_i32 v173, v36, 11, 1\n"
      "v_and_b32 v172, s75, v172\n"
      "v_and_b32 v173, s76, v173\n"
      "v_add_u32 v172, v172, v173\n"
      "v_sub_u32 v172, s74, v172\n"
      "v_sub_u32_e64 v20, v20, v172\n"
      "v_bfe_i32 v172, v36, 12, 1\n"
      "v_bfe_i32 v173, v36, 13, 1\n"
      "v_and_b32 v172, s75, v172\n"
      "v_and_b32 v173, s76, v173\n"
      "v_add_u32 v172, v172, v173\n"
      "v_sub_u32 v172, s74, v172\n"
      "v_sub_u32 v21, v21, v172\n"
      "v_bfe_i32 v172, v36, 14, 1\n"
      "v_bfe_i32 v173, v36, 15, 1\n"
      "v_and_b32 v172, s75, v172\n"
      "v_and_b32 v173, s76, v173\n"
      "v_add_u32 v172, v172, v173\n"
      "v_sub_u32 v172, s74, v172\n"
      "v_sub_u32 v22, v22, v172\n"
      "v_bfe_i32 v172, v36, 16, 1\n"
      "v_bfe_i32 v173, v36, 17, 1\n"
      "v_and_b32 v172, s75, v172\n"
      "v_and_b32 v173, s76, v173\n"
      "v_add_u32 v172, v172, v173\n"
      "v_sub_u32 v172, s74, v172\n"
      "v_sub_u32 v23, v23, v172\n"
      "v_bfe_i32 v172, v36, 18, 1\n"
      "v_bfe_i32 v173, v36, 19, 1\n"
      "v_and_b32 v172, s75, v172\n"
      "v_and_b32 v173, s76, v173\n"
      "v_add_u32 v172, v172, v173\n"
      "v_sub_u32 v172, s74, v172\n"
      "v_sub_u32 v25, v25, v172\n"
      "s_waitcnt lgkmcnt(0)\n"
      "s_nop 4\n"
      "s_branch L_back_54_%=\n"
      "L_rare_56_%=:\n"
      "s_nop 7\n"
      "s_waitcnt lgkmcnt(0)\n"
      "s_cmp_eq_u64 s[56:57], 0\n"
      "s_cbranch_scc1 L_nomask_55_%=\n"
      "s_waitcnt vmcnt(12)\n"
      "v_subrev_u32 v80, 151936, v9\n"
      "v_bfe_u32 v97, v80, 4, 4\n"
      "v_mul_u32_u24 v97, 98, v97\n"
      "v_lshrrev_b32 v80, 8, v80\n"
      "v_lshl_add_u32 v97, v80, 3, v97\n"
      "s_add_u32 s74, s77, 147072\n"
      "v_add_u32 v97, s74, v97\n"
      "v_and_b32 v103, 2, v97\n"
      "v_and_b32 v97, -4, v97\n"
      "ds_read2_b32 v[198:199], v97 offset0:0 offset1:1\n"
      "ds_read2_b32 v[180:181], v97 offset0:8 offset1:9\n"
      "ds_read2_b32 v[178:179], v97 offset0:16 offset1:17\n"
      "ds_read2_b32 v[132:133], v97 offset0:2 offset1:10\n"
      "ds_read2_b32 v[122:123], v97 offset0:18 offset1:24\n"
      "s_waitcnt lgkmcnt(4)\n"
      "v_alignbyte_b32 v198, v199, v198, v103\n"
      "s_waitcnt lgkmcnt(1)\n"
      "v_alignbyte_b32 v199, v132, v199, v103\n"
      "v_lshlrev_b32 v80, 16, v198\n"
      "v_fmac_f32 v108, s53, v80\n"
      "v_and_b32 v80, 0xffff0000, v198\n"
      "v_fmac_f32 v109, s53, v80\n"
      "v_lshlrev_b32 v80, 16, v199\n"
      "v_fmac_f32 v110, s53, v80\n"
      "v_and_b32 v80, 0xffff0000, v199\n"
      "v_fmac_f32 v111, s53, v80\n"
      "v_alignbyte_b32 v180, v181, v180, v103\n"
      "v_alignbyte_b32 v181, v133, v181, v103\n"
      "v_lshlrev_b32 v80, 16, v180\n"
      "v_fmac_f32 v118, s53, v80\n"
      "v_and_b32 v80, 0xffff0000, v180\n"
      "v_fmac_f32 v119, s53, v80\n"
      "v_lshlrev_b32 v80, 16, v181\n"
      "v_fmac_f32_e64 v120, s53, v80\n"
      "v_and_b32 v80, 0xffff0000, v181\n"
      "v_fmac_f32 v121, s53, v80\n"
      "v_alignbyte_b32 v178, v179, v178, v103\n"
      "s_waitcnt lgkmcnt(0)\n"
      "v_alignbyte_b32 v179, v122, v179, v103\n"
      "v_lshlrev_b32 v80, 16, v178\n"
      "v_fmac_f32 v166, s53, v80\n"
      "v_and_b32 v80, 0xffff0000, v178\n"
      "v_fmac_f32 v167, s53, v80\n"
      "v_lshlrev_b32 v80, 16, v179\n"
      "v_fmac_f32_e64 v168, s53, v80\n"
      "v_and_b32 v80, 0xffff0000, v179\n"
      "v_fmac_f32 v169, s53, v80\n"
      "v_alignbyte_b32 v80, v123, v123, v103\n"
      "v_lshlrev_b32 v80, 16, v80\n"
      "v_fmac_f32_e64 v102, s53, v80\n"
      "L_nomask_55_%=:\n"
      "v_max3_f32 v97, v108, v109, v110\n"
      "v_max3_f32 v103, v111, v118, v119\n"
      "v_max3_f32 v97, v97, v120, v121\n"
      "v_max3_f32 v103, v103, v166, v167\n"
      "v_max3_f32 v97, v97, v168, v169\n"
      "v_max3_f32 v97, v97, v103, v102\n"
      "v_mov_b32 v80, v97\n"
      "s_nop 1\n"
      "v_permlane16_swap_b32 v97, v80\n"
      "v_max_f32 v97, v97, v80\n"
      "v_mov_b32 v80, v97\n"
      "s_nop 1\n"
      "v_permlane32_swap_b32 v97, v80\n"
      "v_max_f32 v97, v97, v80\n"
      "v_sub_f32 v108, v108, v97\n"
      "v_sub_f32 v109, v109, v97\n"
      "v_sub_f32 v110, v110, v97\n"
      "v_sub_f32 v111, v111, v97\n"
      "v_sub_f32 v118, v118, v97\n"
      "v_sub_f32 v119, v119, v97\n"
      "v_sub_f32 v120, v120, v97\n"
      "v_sub_f32 v121, v121, v97\n"
      "v_sub_f32 v166, v166, v97\n"
      "v_sub_f32 v167, v167, v97\n"
      "v_sub_f32 v168, v168, v97\n"
      "v_sub_f32 v169, v169, v97\n"
      "v_sub_f32 v102, v102, v97\n"
      "s_waitcnt lgkmcnt(0)\n"
      "s_nop 4\n"
      "s_branch L_back_57_%=\n"
      "L_rare_59_%=:\n"
      "s_nop 7\n"
      "s_waitcnt lgkmcnt(0)\n"
      "s_cmp_eq_u64 s[56:57], 0\n"
      "s_cbranch_scc1 L_nomask_58_%=\n"
      "s_waitcnt vmcnt(12)\n"
      "v_subrev_u32 v201, 151936, v9\n"
      "v_bfe_u32 v180, v201, 4, 4\n"
      "v_mul_u32_u24 v180, 98, v180\n"
      "v_lshrrev_b32 v201, 8, v201\n"
      "v_lshl_add_u32 v180, v201, 3, v180\n"
      "s_add_u32 s74, s77, 148640\n"
      "v_add_u32 v180, s74, v180\n"
      "v_and_b32 v181, 2, v180\n"
      "v_and_b32_e64 v180, -4, v180\n"
      "ds_read2_b32 v[178:179], v180 offset0:0 offset1:1\n"
      "ds_read2_b32 v[166:167], v180 offset0:8 offset1:9\n"
      "ds_read2_b32 v[156:157], v180 offset0:16 offset1:17\n"
      "ds_read2_b32 v[176:177], v180 offset0:2 offset1:10\n"
      "ds_read2_b32 v[154:155], v180 offset0:18 offset1:24\n"
      "s_waitcnt lgkmcnt(4)\n"
      "v_alignbyte_b32 v178, v179, v178, v181\n"
      "s_waitcnt lgkmcnt(1)\n"
      "v_alignbyte_b32 v179, v176, v179, v181\n"
      "v_lshlrev_b32 v201, 16, v178\n"
      "v_fmac_f32 v144, s53, v201\n"
      "v_and_b32 v201, 0xffff0000, v178\n"
      "v_fmac_f32 v145, s53, v201\n"
      "v_lshlrev_b32 v201, 16, v179\n"
      "v_fmac_f32_e64 v146, s53, v201\n"
      "v_and_b32 v201, 0xffff0000, v179\n"
      "v_fmac_f32 v147, s53, v201\n"
      "v_alignbyte_b32 v166, v167, v166, v181\n"
      "v_alignbyte_b32 v167, v177, v167, v181\n"
      "v_lshlrev_b32 v201, 16, v166\n"
      "v_fmac_f32 v190, s53, v201\n"
      "v_and_b32 v201, 0xffff0000, v166\n"
      "v_fmac_f32 v191, s53, v201\n"
      "v_lshlrev_b32 v201, 16, v167\n"
      "v_fmac_f32 v192, s53, v201\n"
      "v_and_b32 v201, 0xffff0000, v167\n"
      "v_fmac_f32 v193, s53, v201\n"
      "v_alignbyte_b32 v156, v157, v156, v181\n"
      "s_waitcnt lgkmcnt(0)\n"
      "v_alignbyte_b32 v157, v154, v157, v181\n"
      "v_lshlrev_b32 v201, 16, v156\n"
      "v_fmac_f32 v118, s53, v201\n"
      "v_and_b32 v201, 0xffff0000, v156\n"
      "v_fmac_f32 v119, s53, v201\n"
      "v_lshlrev_b32 v201, 16, v157\n"
      "v_fmac_f32 v120, s53, v201\n"
      "v_and_b32 v201, 0xffff0000, v157\n"
      "v_fmac_f32 v121, s53, v201\n"
      "v_alignbyte_b32 v201, v155, v155, v181\n"
      "v_lshlrev_b32 v201, 16, v201\n"
      "v_fmac_f32 v182, s53, v201\n"
      "L_nomask_58_%=:\n"
      "v_max3_f32 v180, v144, v145, v146\n"
      "v_max3_f32 v181, v147, v190, v191\n"
      "v_max3_f32 v180, v180, v192, v193\n"
      "v_max3_f32 v181, v181, v118, v119\n"
      "v_max3_f32 v180, v180, v120, v121\n"
      "v_max3_f32 v180, v180, v181, v182\n"
      "v_mov_b32 v201, v180\n"
      "s_nop 1\n"
      "v_permlane16_swap_b32 v180, v201\n"
      "v_max_f32 v180, v180, v201\n"
      "v_mov_b32 v201, v180\n"
      "s_nop 1\n"
      "v_permlane32_swap_b32 v180, v201\n"
      "v_max_f32 v180, v180, v201\n"
      "v_sub_f32 v144, v144, v180\n"
      "v_sub_f32 v145, v145, v180\n"
      "v_sub_f32 v146, v146, v180\n"
      "v_sub_f32 v147, v147, v180\n"
      "v_sub_f32 v190, v190, v180\n"
      "v_sub_f32 v191, v191, v180\n"
      "v_sub_f32 v192, v192, v180\n"
      "v_sub_f32 v193, v193, v180\n"
      "v_sub_f32 v118, v118, v180\n"
      "v_sub_f32 v119, v119, v180\n"
      "v_sub_f32 v120, v120, v180\n"
      "v_sub_f32 v121, v121, v180\n"
      "v_sub_f32_e64 v182, v182, v180\n"
      "s_waitcnt lgkmcnt(0)\n"
      "s_nop 4\n"
      "s_branch L_back_60_%=\n"
      "L_rare_62_%=:\n"
      "s_nop 7\n"
      "s_waitcnt lgkmcnt(0)\n"
      "s_cmp_eq_u64 s[56:57], 0\n"
      "s_cbranch_scc1 L_nomask_61_%=\n"
      "s_waitcnt vmcnt(12)\n"
      "v_subrev_u32 v118, 151936, v9\n"
      "v_bfe_u32 v119, v118, 4, 4\n"
      "v_mul_u32_u24 v119, 98, v119\n"
      "v_lshrrev_b32_e64 v118, 8, v118\n"
      "v_lshl_add_u32 v119, v118, 3, v119\n"
      "s_add_u32 s74, s77, 150208\n"
      "v_add_u32 v119, s74, v119\n"
      "v_and_b32 v185, 2, v119\n"
      "v_and_b32_e64 v119, -4, v119\n"
      "ds_read2_b32 v[182:183], v119 offset0:0 offset1:1\n"
      "ds_read2_b32 v[160:161], v119 offset0:8 offset1:9\n"
      "ds_read2_b32 v[114:115], v119 offset0:16 offset1:17\n"
      "ds_read2_b32 v[158:159], v119 offset0:2 offset1:10\n"
      "ds_read2_b32 v[112:113], v119 offset0:18 offset1:24\n"
      "s_waitcnt lgkmcnt(4)\n"
      "v_alignbyte_b32 v182, v183, v182, v185\n"
      "s_waitcnt lgkmcnt(1)\n"
      "v_alignbyte_b32 v183, v158, v183, v185\n"
      "v_lshlrev_b32 v118, 16, v182\n"
      "v_fmac_f32 v190, s53, v118\n"
      "v_and_b32 v118, 0xffff0000, v182\n"
      "v_fmac_f32 v191, s53, v118\n"
      "v_lshlrev_b32 v118, 16, v183\n"
      "v_fmac_f32 v192, s53, v118\n"
      "v_and_b32 v118, 0xffff0000, v183\n"
      "v_fmac_f32 v193, s53, v118\n"
      "v_alignbyte_b32 v160, v161, v160, v185\n"
      "v_alignbyte_b32 v161, v159, v161, v185\n"
      "v_lshlrev_b32 v118, 16, v160\n"
      "v_fmac_f32 v200, s53, v118\n"
      "v_and_b32 v118, 0xffff0000, v160\n"
      "v_fmac_f32 v201, s53, v118\n"
      "v_lshlrev_b32 v118, 16, v161\n"
      "v_fmac_f32 v202, s53, v118\n"
      "v_and_b32 v118, 0xffff0000, v161\n"
      "v_fmac_f32 v203, s53, v118\n"
      "v_alignbyte_b32 v114, v115, v114, v185\n"
      "s_waitcnt lgkmcnt(0)\n"
      "v_alignbyte_b32 v115, v112, v115, v185\n"
      "v_lshlrev_b32 v118, 16, v114\n"
      "v_fmac_f32 v148, s53, v118\n"
      "v_and_b32 v118, 0xffff0000, v114\n"
      "v_fmac_f32 v149, s53, v118\n"
      "v_lshlrev_b32 v118, 16, v115\n"
      "v_fmac_f32 v150, s53, v118\n"
      "v_and_b32 v118, 0xffff0000, v115\n"
      "v_fmac_f32 v151, s53, v118\n"
      "v_alignbyte_b32 v118, v113, v113, v185\n"
      "v_lshlrev_b32 v118, 16, v118\n"
      "v_fmac_f32_e64 v216, s53, v118\n"
      "L_nomask_61_%=:\n"
      "v_max3_f32 v119, v190, v191, v192\n"
      "v_max3_f32 v185, v193, v200, v201\n"
      "v_max3_f32 v119, v119, v202, v203\n"
      "v_max3_f32 v185, v185, v148, v149\n"
      "v_max3_f32 v119, v119, v150, v151\n"
      "v_max3_f32 v119, v119, v185, v216\n"
      "v_mov_b32 v118, v119\n"
      "s_nop 1\n"
      "v_permlane16_swap_b32 v119, v118\n"
      "v_max_f32 v119, v119, v118\n"
      "v_mov_b32 v118, v119\n"
      "s_nop 1\n"
      "v_permlane32_swap_b32 v119, v118\n"
      "v_max_f32 v119, v119, v118\n"
      "v_sub_f32 v190, v190, v119\n"
      "v_sub_f32 v191, v191, v119\n"
      "v_sub_f32 v192, v192, v119\n"
      "v_sub_f32 v193, v193, v119\n"
      "v_sub_f32 v200, v200, v119\n"
      "v_sub_f32 v201, v201, v119\n"
      "v_sub_f32 v202, v202, v119\n"
      "v_sub_f32 v203, v203, v119\n"
      "v_sub_f32 v148, v148, v119\n"
      "v_sub_f32 v149, v149, v119\n"
      "v_sub_f32 v150, v150, v119\n"
      "v_sub_f32 v151, v151, v119\n"
      "v_sub_f32 v216, v216, v119\n"
      "s_waitcnt lgkmcnt(0)\n"
      "s_nop 4\n"
      "s_branch L_back_63_%=\n"
      "L_rare_65_%=:\n"
      "s_nop 7\n"
      "s_waitcnt lgkmcnt(0)\n"
      "s_cmp_eq_u64 s[56:57], 0\n"
      "s_cbranch_scc1 L_nomask_64_%=\n"
      "s_waitcnt vmcnt(12)\n"
      "v_subrev_u32 v168, 151936, v9\n"
      "v_lshrrev_b32 v168, 3, v168\n"
      "v_min_u32 v168, 96, v168\n"
      "s_add_u32 s74, s77, 151776\n"
      "v_add_u32 v168, s74, v168\n"
      "ds_read_u16 v168, v168\n"
      "s_waitcnt lgkmcnt(0)\n"
      "v_lshlrev_b32_e64 v168, 16, v168\n"
      "v_fmac_f32 v194, s53, v168\n"
      "L_nomask_64_%=:\n"
      "s_nop 1\n"
      "v_max_f32_dpp v170, v194, v194 row_ror:8 row_mask:0xf bank_mask:0xf\n"
      "s_nop 1\n"
      "v_max_f32_dpp v170, v170, v170 row_ror:4 row_mask:0xf bank_mask:0xf\n"
      "s_nop 1\n"
      "v_max_f32_dpp v170, v170, v170 row_ror:2 row_mask:0xf bank_mask:0xf\n"
      "s_nop 1\n"
      "v_max_f32_dpp v170, v170, v170 row_ror:1 row_mask:0xf bank_mask:0xf\n"
      "s_nop 1\n"
      "v_max_f32_dpp v170, v170, v170 row_bcast:15 row_mask:0xa bank_mask:0xf\n"
      "s_nop 1\n"
      "v_max_f32_dpp v170, v170, v170 row_bcast:31 row_mask:0xc bank_mask:0xf\n"
      "s_nop 0\n"
      "v_readlane_b32 s74, v170, 63\n"
      "s_nop 1\n"
      "v_subrev_f32 v194, s74, v194\n"
      "s_waitcnt lgkmcnt(0)\n"
      "s_nop 4\n"
      "s_branch L_back_66_%=\n"
      "L_rare_67_%=:\n"
      "s_nop 7\n"
      "s_waitcnt lgkmcnt(0)\n"
      "s_and_b32 s74, s47, 0x3fffffff\n"
      "s_bitcmp1_b32 s47, 31\n"
      "s_cselect_b32 s75, 100270464, 0\n"
      "s_bitcmp1_b32 s47, 30\n"
      "s_cselect_b32 s76, 196224, 0\n"
      "s_mov_b32 s48, 0\n"
      "v_bfe_i32 v85, v36, 10, 1\n"
      "v_bfe_i32 v86, v36, 11, 1\n"
      "v_and_b32 v85, s75, v85\n"
      "v_and_b32 v86, s76, v86\n"
      "v_add_u32 v85, v85, v86\n"
      "v_sub_u32 v85, s74, v85\n"
      "v_add_u32 v20, v20, v85\n"
      "v_bfe_i32 v85, v36, 12, 1\n"
      "v_bfe_i32 v86, v36, 13, 1\n"
      "v_and_b32 v85, s75, v85\n"
      "v_and_b32 v86, s76, v86\n"
      "v_add_u32 v85, v85, v86\n"
      "v_sub_u32 v85, s74, v85\n"
      "v_add_u32 v21, v21, v85\n"
      "v_bfe_i32 v85, v36, 14, 1\n"
      "v_bfe_i32 v86, v36, 15, 1\n"
      "v_and_b32 v85, s75, v85\n"
      "v_and_b32 v86, s76, v86\n"
      "v_add_u32 v85, v85, v86\n"
      "v_sub_u32 v85, s74, v85\n"
      "v_add_u32 v22, v22, v85\n"
      "v_bfe_i32 v85, v36, 16, 1\n"
      "v_bfe_i32 v86, v36, 17, 1\n"
      "v_and_b32 v85, s75, v85\n"
      "v_and_b32 v86, s76, v86\n"
      "v_add_u32 v85, v85, v86\n"
      "v_sub_u32 v85, s74, v85\n"
      "v_add_u32 v23, v23, v85\n"
      "v_bfe_i32 v85, v36, 18, 1\n"
      "v_bfe_i32 v86, v36, 19, 1\n"
      "v_and_b32 v85, s75, v85\n"
      "v_and_b32 v86, s76, v86\n"
      "v_add_u32 v85, v85, v86\n"
      "v_sub_u32 v85, s74, v85\n"
      "v_add_u32 v25, v25, v85\n"
      "s_waitcnt lgkmcnt(0)\n"
      "s_nop 4\n"
      "s_branch L_back_68_%=\n"
      "L_rare_69_%=:\n"
      "s_nop 7\n"
      "s_waitcnt lgkmcnt(0)\n"
      "s_and_b32 s74, s47, 0x3fffffff\n"
      "s_bitcmp1_b32 s47, 31\n"
      "s_cselect_b32 s75, 100270464, 0\n"
      "s_bitcmp1_b32 s47, 30\n"
      "s_cselect_b32 s76, 196224, 0\n"
      "v_bfe_i32 v172, v36, 10, 1\n"
      "v_bfe_i32 v156, v36, 11, 1\n"
      "v_and_b32 v172, s75, v172\n"
      "v_and_b32 v156, s76, v156\n"
      "v_add_u32 v172, v172, v156\n"
      "v_sub_u32 v172, s74, v172\n"
      "v_sub_u32 v20, v20, v172\n"
      "v_bfe_i32 v172, v36, 12, 1\n"
      "v_bfe_i32 v156, v36, 13, 1\n"
      "v_and_b32 v172, s75, v172\n"
      "v_and_b32 v156, s76, v156\n"
      "v_add_u32 v172, v172, v156\n"
      "v_sub_u32 v172, s74, v172\n"
      "v_sub_u32 v21, v21, v172\n"
      "v_bfe_i32 v172, v36, 14, 1\n"
      "v_bfe_i32 v156, v36, 15, 1\n"
      "v_and_b32 v172, s75, v172\n"
      "v_and_b32 v156, s76, v156\n"
      "v_add_u32 v172, v172, v156\n"
      "v_sub_u32 v172, s74, v172\n"
      "v_sub_u32 v22, v22, v172\n"
      "v_bfe_i32 v172, v36, 16, 1\n"
      "v_bfe_i32 v156, v36, 17, 1\n"
      "v_and_b32 v172, s75, v172\n"
      "v_and_b32 v156, s76, v156\n"
      "v_add_u32 v172, v172, v156\n"
      "v_sub_u32 v172, s74, v172\n"
      "v_sub_u32 v23, v23, v172\n"
      "v_bfe_i32 v172, v36, 18, 1\n"
      "v_bfe_i32 v156, v36, 19, 1\n"
      "v_and_b32 v172, s75, v172\n"
      "v_and_b32 v156, s76, v156\n"
      "v_add_u32 v172, v172, v156\n"
      "v_sub_u32 v172, s74, v172\n"
      "v_sub_u32 v25, v25, v172\n"
      "s_waitcnt lgkmcnt(0)\n"
      "s_nop 4\n"
      "s_branch L_back_70_%=\n"
      "L_rare_71_%=:\n"
      "s_nop 7\n"
      "s_waitcnt lgkmcnt(0)\n"
      "s_and_b32 s74, s47, 0x3fffffff\n"
      "s_bitcmp1_b32 s47, 31\n"
      "s_cselect_b32 s75, 100270464, 0\n"
      "s_bitcmp1_b32 s47, 30\n"
      "s_cselect_b32 s76, 196224, 0\n"
      "s_mov_b32 s48, 0\n"
      "v_bfe_i32 v128, v36, 10, 1\n"
      "v_bfe_i32 v129, v36, 11, 1\n"
      "v_and_b32 v128, s75, v128\n"
      "v_and_b32 v129, s76, v129\n"
      "v_add_u32 v128, v128, v129\n"
      "v_sub_u32 v128, s74, v128\n"
      "v_add_u32 v20, v20, v128\n"
      "v_bfe_i32 v128, v36, 12, 1\n"
      "v_bfe_i32 v129, v36, 13, 1\n"
      "v_and_b32 v128, s75, v128\n"
      "v_and_b32 v129, s76, v129\n"
      "v_add_u32 v128, v128, v129\n"
      "v_sub_u32 v128, s74, v128\n"
      "v_add_u32_e64 v21, v21, v128\n"
      "v_bfe_i32 v128, v36, 14, 1\n"
      "v_bfe_i32 v129, v36, 15, 1\n"
      "v_and_b32 v128, s75, v128\n"
      "v_and_b32 v129, s76, v129\n"
      "v_add_u32 v128, v128, v129\n"
      "v_sub_u32 v128, s74, v128\n"
      "v_add_u32_e64 v22, v22, v128\n"
      "v_bfe_i32 v128, v36, 16, 1\n"
      "v_bfe_i32 v129, v36, 17, 1\n"
      "v_and_b32 v128, s75, v128\n"
      "v_and_b32 v129, s76, v129\n"
      "v_add_u32 v128, v128, v129\n"
      "v_sub_u32 v128, s74, v128\n"
      "v_add_u32 v23, v23, v128\n"
      "v_bfe_i32 v128, v36, 18, 1\n"
      "v_bfe_i32 v129, v36, 19, 1\n"
      "v_and_b32 v128, s75, v128\n"
      "v_and_b32 v129, s76, v129\n"
      "v_add_u32 v128, v128, v129\n"
      "v_sub_u32 v128, s74, v128\n"
      "v_add_u32_e64 v25, v25, v128\n"
      "s_waitcnt lgkmcnt(0)\n"
      "s_nop 4\n"
      "s_branch L_back_72_%=\n"
      "L_rare_73_%=:\n"
      "s_nop 7\n"
      "s_waitcnt lgkmcnt(0)\n"
      "s_and_b32 s74, s47, 0x3fffffff\n"
      "s_bitcmp1_b32 s47, 31\n"
      "s_cselect_b32 s75, 100270464, 0\n"
      "s_bitcmp1_b32 s47, 30\n"
      "s_cselect_b32 s76, 196224, 0\n"
      "v_bfe_i32 v128, v36, 10, 1\n"
      "v_bfe_i32 v129, v36, 11, 1\n"
      "v_and_b32 v128, s75, v128\n"
      "v_and_b32 v129, s76, v129\n"
      "v_add_u32 v128, v128, v129\n"
      "v_sub_u32 v128, s74, v128\n"
      "v_sub_u32 v20, v20, v128\n"
      "v_bfe_i32 v128, v36, 12, 1\n"
      "v_bfe_i32 v129, v36, 13, 1\n"
      "v_and_b32 v128, s75, v128\n"
      "v_and_b32 v129, s76, v129\n"
      "v_add_u32 v128, v128, v129\n"
      "v_sub_u32 v128, s74, v128\n"
      "v_sub_u32 v21, v21, v128\n"
      "v_bfe_i32 v128, v36, 14, 1\n"
      "v_bfe_i32 v129, v36, 15, 1\n"
      "v_and_b32 v128, s75, v128\n"
      "v_and_b32 v129, s76, v129\n"
      "v_add_u32 v128, v128, v129\n"
      "v_sub_u32 v128, s74, v128\n"
      "v_sub_u32 v22, v22, v128\n"
      "v_bfe_i32 v128, v36, 16, 1\n"
      "v_bfe_i32 v129, v36, 17, 1\n"
      "v_and_b32 v128, s75, v128\n"
      "v_and_b32 v129, s76, v129\n"
      "v_add_u32 v128, v128, v129\n"
      "v_sub_u32 v128, s74, v128\n"
      "v_sub_u32 v23, v23, v128\n"
      "v_bfe_i32 v128, v36, 18, 1\n"
      "v_bfe_i32 v129, v36, 19, 1\n"
      "v_and_b32 v128, s75, v128\n"
      "v_and_b32 v129, s76, v129\n"
      "v_add_u32 v128, v128, v129\n"
      "v_sub_u32 v128, s74, v128\n"
      "v_sub_u32 v25, v25, v128\n"
      "s_waitcnt lgkmcnt(0)\n"
      "s_nop 4\n"
      "s_branch L_back_74_%=\n"
      "L_rare_75_%=:\n"
      "s_nop 7\n"
      "s_waitcnt lgkmcnt(0)\n"
      "s_and_b32 s74, s47, 0x3fffffff\n"
      "s_bitcmp1_b32 s47, 31\n"
      "s_cselect_b32 s75, 100270464, 0\n"
      "s_bitcmp1_b32 s47, 30\n"
      "s_cselect_b32 s76, 196224, 0\n"
      "s_mov_b32 s48, 0\n"
      "v_bfe_i32 v102, v36, 10, 1\n"
      "v_bfe_i32 v103, v36, 11, 1\n"
      "v_and_b32 v102, s75, v102\n"
      "v_and_b32 v103, s76, v103\n"
      "v_add_u32 v102, v102, v103\n"
      "v_sub_u32 v102, s74, v102\n"
      "v_add_u32 v20, v20, v102\n"
      "v_bfe_i32 v102, v36, 12, 1\n"
      "v_bfe_i32 v103, v36, 13, 1\n"
      "v_and_b32 v102, s75, v102\n"
      "v_and_b32 v103, s76, v103\n"
      "v_add_u32 v102, v102, v103\n"
      "v_sub_u32 v102, s74, v102\n"
      "v_add_u32_e64 v21, v21, v102\n"
      "v_bfe_i32 v102, v36, 14, 1\n"
      "v_bfe_i32 v103, v36, 15, 1\n"
      "v_and_b32 v102, s75, v102\n"
      "v_and_b32 v103, s76, v103\n"
      "v_add_u32 v102, v102, v103\n"
      "v_sub_u32 v102, s74, v102\n"
      "v_add_u32_e64 v22, v22, v102\n"
      "v_bfe_i32 v102, v36, 16, 1\n"
      "v_bfe_i32 v103, v36, 17, 1\n"
      "v_and_b32 v102, s75, v102\n"
      "v_and_b32 v103, s76, v103\n"
      "v_add_u32 v102, v102, v103\n"
      "v_sub_u32 v102, s74, v102\n"
      "v_add_u32 v23, v23, v102\n"
      "v_bfe_i32 v102, v36, 18, 1\n"
      "v_bfe_i32 v103, v36, 19, 1\n"
      "v_and_b32 v102, s75, v102\n"
      "v_and_b32 v103, s76, v103\n"
      "v_add_u32 v102, v102, v103\n"
      "v_sub_u32 v102, s74, v102\n"
      "v_add_u32_e64 v25, v25, v102\n"
      "s_waitcnt lgkmcnt(0)\n"
      "s_nop 4\n"
      "s_branch L_back_76_%=\n"
      "L_rare_77_%=:\n"
      "s_nop 7\n"
      "s_waitcnt lgkmcnt(0)\n"
      "s_and_b32 s74, s47, 0x3fffffff\n"
      "s_bitcmp1_b32 s47, 31\n"
      "s_cselect_b32 s75, 100270464, 0\n"
      "s_bitcmp1_b32 s47, 30\n"
      "s_cselect_b32 s76, 196224, 0\n"
      "v_bfe_i32 v112, v36, 10, 1\n"
      "v_bfe_i32 v113, v36, 11, 1\n"
      "v_and_b32 v112, s75, v112\n"
      "v_and_b32 v113, s76, v113\n"
      "v_add_u32 v112, v112, v113\n"
      "v_sub_u32 v112, s74, v112\n"
      "v_sub_u32 v20, v20, v112\n"
      "v_bfe_i32 v112, v36, 12, 1\n"
      "v_bfe_i32 v113, v36, 13, 1\n"
      "v_and_b32 v112, s75, v112\n"
      "v_and_b32 v113, s76, v113\n"
      "v_add_u32 v112, v112, v113\n"
      "v_sub_u32 v112, s74, v112\n"
      "v_sub_u32 v21, v21, v112\n"
      "v_bfe_i32 v112, v36, 14, 1\n"
      "v_bfe_i32 v113, v36, 15, 1\n"
      "v_and_b32 v112, s75, v112\n"
      "v_and_b32 v113, s76, v113\n"
      "v_add_u32 v112, v112, v113\n"
      "v_sub_u32 v112, s74, v112\n"
      "v_sub_u32 v22, v22, v112\n"
      "v_bfe_i32 v112, v36, 16, 1\n"
      "v_bfe_i32 v113, v36, 17, 1\n"
      "v_and_b32 v112, s75, v112\n"
      "v_and_b32 v113, s76, v113\n"
      "v_add_u32 v112, v112, v113\n"
      "v_sub_u32 v112, s74, v112\n"
      "v_sub_u32 v23, v23, v112\n"
      "v_bfe_i32 v112, v36, 18, 1\n"
      "v_bfe_i32 v113, v36, 19, 1\n"
      "v_and_b32 v112, s75, v112\n"
      "v_and_b32 v113, s76, v113\n"
      "v_add_u32 v112, v112, v113\n"
      "v_sub_u32 v112, s74, v112\n"
      "v_sub_u32 v25, v25, v112\n"
      "s_waitcnt lgkmcnt(0)\n"
      "s_nop 4\n"
      "s_branch L_back_78_%=\n"
      "L_rare_79_%=:\n"
      "s_nop 7\n"
      "s_waitcnt lgkmcnt(0)\n"
      "s_and_b32 s74, s47, 0x3fffffff\n"
      "s_bitcmp1_b32 s47, 31\n"
      "s_cselect_b32 s75, 100270464, 0\n"
      "s_bitcmp1_b32 s47, 30\n"
      "s_cselect_b32 s76, 196224, 0\n"
      "s_mov_b32 s48, 0\n"
      "v_bfe_i32 v85, v36, 10, 1\n"
      "v_bfe_i32 v86, v36, 11, 1\n"
      "v_and_b32 v85, s75, v85\n"
      "v_and_b32 v86, s76, v86\n"
      "v_add_u32 v85, v85, v86\n"
      "v_sub_u32 v85, s74, v85\n"
      "v_add_u32 v20, v20, v85\n"
      "v_bfe_i32 v85, v36, 12, 1\n"
      "v_bfe_i32 v86, v36, 13, 1\n"
      "v_and_b32 v85, s75, v85\n"
      "v_and_b32 v86, s76, v86\n"
      "v_add_u32 v85, v85, v86\n"
      "v_sub_u32 v85, s74, v85\n"
      "v_add_u32_e64 v21, v21, v85\n"
      "v_bfe_i32 v85, v36, 14, 1\n"
      "v_bfe_i32 v86, v36, 15, 1\n"
      "v_and_b32 v85, s75, v85\n"
      "v_and_b32 v86, s76, v86\n"
      "v_add_u32 v85, v85, v86\n"
      "v_sub_u32 v85, s74, v85\n"
      "v_add_u32_e64 v22, v22, v85\n"
      "v_bfe_i32 v85, v36, 16, 1\n"
      "v_bfe_i32 v86, v36, 17, 1\n"
      "v_and_b32 v85, s75, v85\n"
      "v_and_b32 v86, s76, v86\n"
      "v_add_u32 v85, v85, v86\n"
      "v_sub_u32 v85, s74, v85\n"
      "v_add_u32 v23, v23, v85\n"
      "v_bfe_i32 v85, v36, 18, 1\n"
      "v_bfe_i32 v86, v36, 19, 1\n"
      "v_and_b32 v85, s75, v85\n"
      "v_and_b32 v86, s76, v86\n"
      "v_add_u32 v85, v85, v86\n"
      "v_sub_u32 v85, s74, v85\n"
      "v_add_u32_e64 v25, v25, v85\n"
      "s_waitcnt lgkmcnt(0)\n"
      "s_nop 4\n"
      "s_branch L_back_80_%=\n"
      "L_rare_81_%=:\n"
      "s_nop 7\n"
      "s_waitcnt lgkmcnt(0)\n"
      "s_and_b32 s74, s47, 0x3fffffff\n"
      "s_bitcmp1_b32 s47, 31\n"
      "s_cselect_b32 s75, 100270464, 0\n"
      "s_bitcmp1_b32 s47, 30\n"
      "s_cselect_b32 s76, 196224, 0\n"
      "v_bfe_i32 v172, v36, 10, 1\n"
      "v_bfe_i32 v156, v36, 11, 1\n"
      "v_and_b32 v172, s75, v172\n"
      "v_and_b32 v156, s76, v156\n"
      "v_add_u32 v172, v172, v156\n"
      "v_sub_u32 v172, s74, v172\n"
      "v_sub_u32 v20, v20, v172\n"
      "v_bfe_i32 v172, v36, 12, 1\n"
      "v_bfe_i32 v156, v36, 13, 1\n"
      "v_and_b32 v172, s75, v172\n"
      "v_and_b32 v156, s76, v156\n"
      "v_add_u32 v172, v172, v156\n"
      "v_sub_u32 v172, s74, v172\n"
      "v_sub_u32 v21, v21, v172\n"
      "v_bfe_i32 v172, v36, 14, 1\n"
      "v_bfe_i32 v156, v36, 15, 1\n"
      "v_and_b32 v172, s75, v172\n"
      "v_and_b32 v156, s76, v156\n"
      "v_add_u32 v172, v172, v156\n"
      "v_sub_u32 v172, s74, v172\n"
      "v_sub_u32 v22, v22, v172\n"
      "v_bfe_i32 v172, v36, 16, 1\n"
      "v_bfe_i32 v156, v36, 17, 1\n"
      "v_and_b32 v172, s75, v172\n"
      "v_and_b32 v156, s76, v156\n"
      "v_add_u32 v172, v172, v156\n"
      "v_sub_u32 v172, s74, v172\n"
      "v_sub_u32 v23, v23, v172\n"
      "v_bfe_i32 v172, v36, 18, 1\n"
      "v_bfe_i32 v156, v36, 19, 1\n"
      "v_and_b32 v172, s75, v172\n"
      "v_and_b32 v156, s76, v156\n"
      "v_add_u32 v172, v172, v156\n"
      "v_sub_u32 v172, s74, v172\n"
      "v_sub_u32 v25, v25, v172\n"
      "s_waitcnt lgkmcnt(0)\n"
      "s_nop 4\n"
      "s_branch L_back_82_%=\n"
      "L_rare_83_%=:\n"
      "s_nop 7\n"
      "s_waitcnt lgkmcnt(0)\n"
      "s_and_b32 s74, s47, 0x3fffffff\n"
      "s_bitcmp1_b32 s47, 31\n"
      "s_cselect_b32 s75, 100270464, 0\n"
      "s_bitcmp1_b32 s47, 30\n"
      "s_cselect_b32 s76, 196224, 0\n"
      "s_mov_b32 s48, 0\n"
      "v_bfe_i32 v166, v36, 10, 1\n"
      "v_bfe_i32 v168, v36, 11, 1\n"
      "v_and_b32 v166, s75, v166\n"
      "v_and_b32 v168, s76, v168\n"
      "v_add_u32 v166, v166, v168\n"
      "v_sub_u32 v166, s74, v166\n"
      "v_add_u32 v20, v20, v166\n"
      "v_bfe_i32 v166, v36, 12, 1\n"
      "v_bfe_i32 v168, v36, 13, 1\n"
      "v_and_b32 v166, s75, v166\n"
      "v_and_b32 v168, s76, v168\n"
      "v_add_u32 v166, v166, v168\n"
      "v_sub_u32 v166, s74, v166\n"
      "v_add_u32_e64 v21, v21, v166\n"
      "v_bfe_i32 v166, v36, 14, 1\n"
      "v_bfe_i32 v168, v36, 15, 1\n"
      "v_and_b32 v166, s75, v166\n"
      "v_and_b32 v168, s76, v168\n"
      "v_add_u32 v166, v166, v168\n"
      "v_sub_u32 v166, s74, v166\n"
      "v_add_u32_e64 v22, v22, v166\n"
      "v_bfe_i32 v166, v36, 16, 1\n"
      "v_bfe_i32 v168, v36, 17, 1\n"
      "v_and_b32 v166, s75, v166\n"
      "v_and_b32 v168, s76, v168\n"
      "v_add_u32 v166, v166, v168\n"
      "v_sub_u32 v166, s74, v166\n"
      "v_add_u32 v23, v23, v166\n"
      "v_bfe_i32 v166, v36, 18, 1\n"
      "v_bfe_i32 v168, v36, 19, 1\n"
      "v_and_b32 v166, s75, v166\n"
      "v_and_b32 v168, s76, v168\n"
      "v_add_u32 v166, v166, v168\n"
      "v_sub_u32 v166, s74, v166\n"
      "v_add_u32_e64 v25, v25, v166\n"
      "s_waitcnt lgkmcnt(0)\n"
      "s_nop 4\n"
      "s_branch L_back_84_%=\n"
      "L_rare_85_%=:\n"
      "s_nop 7\n"
      "s_waitcnt lgkmcnt(0)\n"
      "s_and_b32 s74, s47, 0x3fffffff\n"
      "s_bitcmp1_b32 s47, 31\n"
      "s_cselect_b32 s75, 100270464, 0\n"
      "s_bitcmp1_b32 s47, 30\n"
      "s_cselect_b32 s76, 196224, 0\n"
      "v_bfe_i32 v140, v36, 10, 1\n"
      "v_bfe_i32 v85, v36, 11, 1\n"
      "v_and_b32 v140, s75, v140\n"
      "v_and_b32 v85, s76, v85\n"
      "v_add_u32 v140, v140, v85\n"
      "v_sub_u32 v140, s74, v140\n"
      "v_sub_u32 v20, v20, v140\n"
      "v_bfe_i32 v140, v36, 12, 1\n"
      "v_bfe_i32 v85, v36, 13, 1\n"
      "v_and_b32 v140, s75, v140\n"
      "v_and_b32 v85, s76, v85\n"
      "v_add_u32 v140, v140, v85\n"
      "v_sub_u32 v140, s74, v140\n"
      "v_sub_u32 v21, v21, v140\n"
      "v_bfe_i32 v140, v36, 14, 1\n"
      "v_bfe_i32 v85, v36, 15, 1\n"
      "v_and_b32 v140, s75, v140\n"
      "v_and_b32 v85, s76, v85\n"
      "v_add_u32 v140, v140, v85\n"
      "v_sub_u32 v140, s74, v140\n"
      "v_sub_u32 v22, v22, v140\n"
      "v_bfe_i32 v140, v36, 16, 1\n"
      "v_bfe_i32 v85, v36, 17, 1\n"
      "v_and_b32 v140, s75, v140\n"
      "v_and_b32 v85, s76, v85\n"
      "v_add_u32 v140, v140, v85\n"
      "v_sub_u32 v140, s74, v140\n"
      "v_sub_u32 v23, v23, v140\n"
      "v_bfe_i32 v140, v36, 18, 1\n"
      "v_bfe_i32 v85, v36, 19, 1\n"
      "v_and_b32 v140, s75, v140\n"
      "v_and_b32 v85, s76, v85\n"
      "v_add_u32 v140, v140, v85\n"
      "v_sub_u32 v140, s74, v140\n"
      "v_sub_u32 v25, v25, v140\n"
      "s_waitcnt lgkmcnt(0)\n"
      "s_nop 4\n"
      "s_branch L_back_86_%=\n"
      "L_rare_87_%=:\n"
      "s_nop 7\n"
      "s_waitcnt lgkmcnt(0)\n"
      "s_and_b32 s74, s47, 0x3fffffff\n"
      "s_bitcmp1_b32 s47, 31\n"
      "s_cselect_b32 s75, 100270464, 0\n"
      "s_bitcmp1_b32 s47, 30\n"
      "s_cselect_b32 s76, 196224, 0\n"
      "s_mov_b32 s48, 0\n"
      "v_bfe_i32 v142, v36, 10, 1\n"
      "v_bfe_i32 v146, v36, 11, 1\n"
      "v_and_b32 v142, s75, v142\n"
      "v_and_b32 v146, s76, v146\n"
      "v_add_u32 v142, v142, v146\n"
      "v_sub_u32 v142, s74, v142\n"
      "v_add_u32 v20, v20, v142\n"
      "v_bfe_i32 v142, v36, 12, 1\n"
      "v_bfe_i32 v146, v36, 13, 1\n"
      "v_and_b32 v142, s75, v142\n"
      "v_and_b32 v146, s76, v146\n"
      "v_add_u32 v142, v142, v146\n"
      "v_sub_u32 v142, s74, v142\n"
      "v_add_u32_e64 v21, v21, v142\n"
      "v_bfe_i32 v142, v36, 14, 1\n"
      "v_bfe_i32 v146, v36, 15, 1\n"
      "v_and_b32 v142, s75, v142\n"
      "v_and_b32 v146, s76, v146\n"
      "v_add_u32 v142, v142, v146\n"
      "v_sub_u32 v142, s74, v142\n"
      "v_add_u32_e64 v22, v22, v142\n"
      "v_bfe_i32 v142, v36, 16, 1\n"
      "v_bfe_i32 v146, v36, 17, 1\n"
      "v_and_b32 v142, s75, v142\n"
      "v_and_b32 v146, s76, v146\n"
      "v_add_u32 v142, v142, v146\n"
      "v_sub_u32 v142, s74, v142\n"
      "v_add_u32 v23, v23, v142\n"
      "v_bfe_i32 v142, v36, 18, 1\n"
      "v_bfe_i32 v146, v36, 19, 1\n"
      "v_and_b32 v142, s75, v142\n"
      "v_and_b32 v146, s76, v146\n"
      "v_add_u32 v142, v142, v146\n"
      "v_sub_u32 v142, s74, v142\n"
      "v_add_u32_e64 v25, v25, v142\n"
      "s_waitcnt lgkmcnt(0)\n"
      "s_nop 4\n"
      "s_branch L_back_88_%=\n"
      "L_rare_89_%=:\n"
      "s_nop 7\n"
      "s_waitcnt lgkmcnt(0)\n"
      "s_and_b32 s74, s47, 0x3fffffff\n"
      "s_bitcmp1_b32 s47, 31\n"
      "s_cselect_b32 s75, 100270464, 0\n"
      "s_bitcmp1_b32 s47, 30\n"
      "s_cselect_b32 s76, 196224, 0\n"
      "v_bfe_i32 v141, v36, 10, 1\n"
      "v_bfe_i32 v204, v36, 11, 1\n"
      "v_and_b32 v141, s75, v141\n"
      "v_and_b32 v204, s76, v204\n"
      "v_add_u32 v141, v141, v204\n"
      "v_sub_u32 v141, s74, v141\n"
      "v_sub_u32 v20, v20, v141\n"
      "v_bfe_i32 v141, v36, 12, 1\n"
      "v_bfe_i32 v204, v36, 13, 1\n"
      "v_and_b32 v141, s75, v141\n"
      "v_and_b32 v204, s76, v204\n"
      "v_add_u32 v141, v141, v204\n"
      "v_sub_u32 v141, s74, v141\n"
      "v_sub_u32 v21, v21, v141\n"
      "v_bfe_i32 v141, v36, 14, 1\n"
      "v_bfe_i32 v204, v36, 15, 1\n"
      "v_and_b32 v141, s75, v141\n"
      "v_and_b32 v204, s76, v204\n"
      "v_add_u32 v141, v141, v204\n"
      "v_sub_u32 v141, s74, v141\n"
      "v_sub_u32 v22, v22, v141\n"
      "v_bfe_i32 v141, v36, 16, 1\n"
      "v_bfe_i32 v204, v36, 17, 1\n"
      "v_and_b32 v141, s75, v141\n"
      "v_and_b32 v204, s76, v204\n"
      "v_add_u32 v141, v141, v204\n"
      "v_sub_u32 v141, s74, v141\n"
      "v_sub_u32 v23, v23, v141\n"
      "v_bfe_i32 v141, v36, 18, 1\n"
      "v_bfe_i32 v204, v36, 19, 1\n"
      "v_and_b32 v141, s75, v141\n"
      "v_and_b32 v204, s76, v204\n"
      "v_add_u32 v141, v141, v204\n"
      "v_sub_u32 v141, s74, v141\n"
      "v_sub_u32 v25, v25, v141\n"
      "s_waitcnt lgkmcnt(0)\n"
      "s_nop 4\n"
      "s_branch L_back_90_%=\n"
      "L_cend_%=:\n"
      :
      : "s"(__builtin_amdgcn_kernarg_segment_ptr()), "s"(wave), "s"(blockIdx.x), "s"(CT_TAB), "s"(lds0)
      : "v0","v1","v2","v3","v4","v5","v6","v7","v8","v9","v10","v11","v12","v13","v14","v15","v16","v17","v18","v19","v20","v21","v22","v23","v24","v25","v26","v27","v28","v29","v30","v31","v32","v33","v34","v35","v36","v37","v38","v39","v40","v41","v42","v43","v44","v45","v46","v47","v48","v49","v50","v51","v52","v53","v54","v55","v56","v57","v58","v59","v60","v61","v62","v63","v64","v65","v66","v67","v68","v69","v70","v71","v72","v73","v74","v75","v76","v77","v78","v79","v80","v81","v82","v83","v84","v85","v86","v87","v88","v89","v90","v91","v92","v93","v94","v95","v96","v97","v98","v99","v100","v101","v102","v103","v104","v105","v106","v107","v108","v109","v110","v111","v112","v113","v114","v115","v116","v117","v118","v119","v120","v121","v122","v123","v124","v125","v126","v127","v128","v129","v130","v131","v132","v133","v134","v135","v136","v137","v138","v139","v140","v141","v142","v143","v144","v145","v146","v147","v148","v149","v150","v151","v152","v153","v154","v155","v156","v157","v158","v159","v160","v161","v162","v163","v164","v165","v166","v167","v168","v169","v170","v171","v172","v173","v174","v175","v176","v177","v178","v179","v180","v181","v182","v183","v184","v185","v186","v187","v188","v189","v190","v191","v192","v193","v194","v195","v196","v197","v198","v199","v200","v201","v202","v203","v204","v205","v206","v207","v208","v209","v210","v211","v212","v213","v214","v215","v216","v217","v218","v219","v220","v221","v222","v223","v224","v225","v226","v227","v228","v229","v230","v231","v232","v233","v234","v235","v236","v237","v238","v239","v240","v241","v242","v243","v244","v245","v246","v247","v248","v249","v250","v251","v252","v253","v254","v255","a0","a1","a2","a3","a4","a5","a6","a7","a8","a9","a10","a11","a12","a13","a14","a15","a16","a17","a18","a19","a20","a21","a22","a23","a24","a25","a26","a27","a28","a29","a30","a31","a32","a33","a34","a35","a36","a37","a38","a39","a40","a41","a42","a43","a44","a45","a46","a47","a48","a49","a50","a51","a52","a53","a54","a55","a56","a57","a58","a59","a60","a61","a62","a63","a64","a65","a66","a67","a68","a69","a70","a71","a72","a73","a74","a75","a76","a77","a78","a79","a80","a81","a82","a83","a84","a85","a86","a87","a88","a89","a90","a91","a92","a93","a94","a95","a96","a97","a98","a99","a100","a101","a102","a103","a104","a105","a106","a107","a108","a109","a110","a111","a112","a113","a114","a115","a116","a117","a118","a119","a120","a121","a122","a123","a124","a125","a126","a127","a128","a129","a130","a131","a132","a133","a134","a135","a136","a137","a138","a139","a140","a141","a142","a143","a144","a145","a146","a147","a148","a149","a150","a151","a152","a153","a154","a155","a156","a157","a158","a159","a160","a161","a162","a163","a164","a165","a166","a167","a168","a169","a170","a171","a172","a173","a174","a175","a176","a177","a178","a179","a180","a181","a182","a183","a184","a185","a186","a187","a188","a189","a190","a191","a192","a193","a194","a195","a196","a197","a198","a199","a200","a201","a202","a203","a204","a205","a206","a207","a208","a209","a210","a211","a212","a213","a214","a215","a216","a217","a218","a219","a220","a221","a222","a223","a224","a225","a226","a227","a228","a229","a230","a231","a232","a233","a234","a235","a236","a237","a238","a239","a240","a241","a242","a243","a244","a245","a246","a247","a248","a249","a250","a251","a252","a253","a254","a255","s16","s17","s18","s19","s20","s21","s22","s23","s24","s25","s26","s27","s28","s29","s30","s31","s34","s35","s36","s37","s38","s39","s40","s41","s42","s43","s44","s45","s46","s47","s48","s49","s50","s51","s52","s53","s54","s55","s56","s57","s58","s59","s60","s61","s62","s63","s64","s65","s66","s67","s68","s69","s70","s71","s72","s73","s74","s75","s76","s77","s78","s79","s80","s81","s82","s83","s84","s85","s86","s87","s88","s89","s90","s91","s92","s93","s94","s95","s96","s97","s98","s99","s100","s101", "vcc", "scc", "memory");
}

}

extern "C" void kernel_launch(void* const* d_in, const int* in_sizes, int n_in, void* d_out, int out_size, void* d_ws,
                              size_t ws_size, hipStream_t stream) {
  const uint16_t* x = (const uint16_t*)d_in[0];
  const uint16_t* wqkv = (const uint16_t*)d_in[1];
  const uint16_t* wo = (const uint16_t*)d_in[2];
  const uint16_t* ls = (const uint16_t*)d_in[3];
  const uint16_t* w1 = (const uint16_t*)d_in[4];
  const uint16_t* w2 = (const uint16_t*)d_in[5];
  const uint16_t* rc = (const uint16_t*)d_in[6];
  const int* ridx = (const int*)d_in[7];
  const uint16_t* mask = (const uint16_t*)d_in[8];
  swin::swin_main<<<256, 256, 0, stream>>>(x, mask, (char*)d_ws, (uint16_t*)d_out, wqkv, wo, ls, w1, w2, rc, ridx);
}
```

```cpp
#include <hip/hip_runtime.h>
#include <hip/hip_bf16.h>
#include <stdint.h>

namespace swin {

constexpr unsigned LDS_BYTES = 163840u;

__global__ __launch_bounds__(256, 1) void swin_main(const uint16_t* x, const uint16_t* mask, char* ws, uint16_t* out,
                                                    const uint16_t* wqkv, const uint16_t* wo, const uint16_t* ls,
                                                    const uint16_t* w1, const uint16_t* w2, const uint16_t* rc,
                                                    const int* ridx) {
  __shared__ __attribute__((aligned(16))) char smem[LDS_BYTES];
  const int wave = __builtin_amdgcn_readfirstlane(threadIdx.x >> 6);
  const unsigned lds0 = (unsigned)(size_t)smem;
  asm volatile(
      ".p2align 5\n"
      "s_mov_b64 s[88:89], %0\n"
      "s_mov_b32 s40, %1\n"
      "s_mov_b32 s41, %2\n"
      "s_load_dwordx2 s[16:17], s[88:89], 0\n"
      "s_load_dwordx2 s[24:25], s[88:89], 8\n"
      "s_load_dwordx2 s[34:35], s[88:89], 16\n"
      "s_load_dwordx2 s[20:21], s[88:89], 24\n"
      "s_load_dwordx2 s[28:29], s[88:89], 32\n"
      "s_load_dwordx2 s[36:37], s[88:89], 40\n"
      "s_load_dwordx8 s[80:87], s[88:89], 48\n"
      "s_load_dwordx2 s[90:91], s[88:89], 80\n"
      "s_getpc_b64 s[98:99]\n"
      "L_ipf0_%=:\n"
      "v_mbcnt_lo_u32_b32 v51, -1, 0\n"
      "v_mbcnt_hi_u32_b32 v51, -1, v51\n"
      "v_lshlrev_b32 v41, 2, v51\n"
      "v_lshlrev_b32_e64 v46, 4, v51\n"
      "s_cmp_lt_u32 s41, 209\n"
      "s_cselect_b32 s44, 20, 19\n"
      "s_mov_b32 s18, 100270464\n"
      "s_mov_b32 s19, 0x20000\n"
      "s_mov_b32 s22, 100270464\n"
      "s_mov_b32 s23, 0x20000\n"
      "s_mov_b32 s26, 25589860\n"
      "s_mov_b32 s27, 0x20000\n"
      "s_mov_b32 s30, 221184\n"
      "s_mov_b32 s31, 0x20000\n"
      "s_mov_b32 s38, 73728\n"
      "s_mov_b32 s39, 0x20000\n"
      "s_lshl_b32 s64, s40, 10\n"
      "s_cmp_lt_u32 s40, 2\n"
      "s_cselect_b32 s65, s64, 163840\n"
      "v_add_u32 v27, s64, v46\n"
      "s_lshl_b32 s92, s40, 8\n"
      "v_add_u32_e64 v44, s92, v41\n"
      "v_min_u32 v29, 704, v44\n"
      "v_add_u32 v29, 4096, v29\n"
      "v_min_u32 v44, 672, v44\n"
      "s_mul_i32 s75, s40, 96\n"
      "s_and_b32 s92, s40, 1\n"
      "s_lshl_b32 s92, s92, 2\n"
      "s_lshl_b32 s93, s40, 3\n"
      "v_lshrrev_b32 v98, 3, v51\n"
      "v_and_b32 v47, 2, v98\n"
      "v_and_b32 v49, 7, v51\n"
      "v_xor_b32 v47, v47, v49\n"
      "v_xor_b32 v47, s92, v47\n"
      "v_add_u32 v98, s93, v98\n"
      "v_lshlrev_b32 v47, 4, v47\n"
      "v_mul_u32_u24 v125, 384, v98\n"
      "v_mul_u32_u24 v98, 1152, v98\n"
      "v_add_u32 v98, v98, v47\n"
      "v_add_u32 v125, v125, v47\n"
      "s_add_u32 s33, s64, 18432\n"
      "v_add_u32 v47, 122112, v44\n"
      "s_waitcnt lgkmcnt(0)\n"
      "s_lshr_b32 s92, s41, 3\n"
      "s_mul_i32 s93, s92, 3456\n"
      "s_lshl_b32 s94, s40, 1\n"
      "s_mov_b64 s[72:73], s[28:29]\n"
      "s_movk_i32 s93, 27\n"
      "s_mov_b32 s94, 221056\n"
      "s_cmp_eq_u32 s40, 1\n"
      "s_cselect_b32 s95, 32, 0\n"
      "s_add_u32 s92, s92, s95\n"
      "s_cmp_eq_u32 s40, 2\n"
      "s_cselect_b64 s[72:73], s[36:37], s[72:73]\n"
      "s_cselect_b32 s93, 18, s93\n"
      "s_cselect_b32 s94, 73600, s94\n"
      "s_cmp_eq_u32 s40, 3\n"
      "s_cselect_b64 s[72:73], s[98:99], s[72:73]\n"
      "s_cselect_b32 s93, 20, s93\n"
      "s_cselect_b32 s94, L_cend_%=-L_ipf0_%=-128, s94\n"
      "s_mul_i32 s92, s92, s93\n"
      "s_lshl_b32 s92, s92, 7\n"
      "s_sub_u32 s93, s93, 1\n"
      "s_lshl_b32 s93, s93, 7\n"
      "v_lshlrev_b32 v49, 3, v46\n"
      "v_min_u32 v49, s93, v49\n"
      "v_add_u32 v49, s92, v49\n"
      "v_min_u32 v49, s94, v49\n"
      "global_load_dword a255, v49, s[72:73]\n"
      "global_load_dword v44, v44, s[86:87]\n"
      "s_load_dwordx16 s[0:15], s[82:83], 0\n"
      "s_load_dwordx16 s[48:63], s[84:85], 0\n"
      "s_load_dwordx2 s[66:67], s[80:81], 0\n"
      "s_mov_b32 s68, s90\n"
      "s_mov_b32 s69, s91\n"
      "s_movk_i32 s70, 9604\n"
      "s_mov_b32 s71, 0x20000\n"
      "s_add_u32 m0, s64, 92160\n"
      "s_movk_i32 s92, 0\n"
      "buffer_load_dwordx4 v27, s[68:71], s92 offen  lds\n"
      "s_add_u32 m0, s64, 96256\n"
      "s_movk_i32 s92, 4096\n"
      "buffer_load_dwordx4 v27, s[68:71], s92 offen  lds\n"
      "s_add_u32 m0, s64, 100352\n"
      "s_cmp_lt_u32 s40, 2\n"
      "s_cselect_b32 m0, m0, s65\n"
      "s_movk_i32 s92, 8192\n"
      "buffer_load_dwordx4 v27, s[68:71], s92 offen  lds\n"
      "s_lshl_b32 s95, s40, 3\n"
      "v_lshrrev_b32 v49, 3, v51\n"
      "v_and_b32_e64 v40, 7, v51\n"
      "v_add_u32 v43, s95, v49\n"
      "v_bfe_u32 v53, v43, 1, 3\n"
      "v_cmp_lt_u32 s[72:73], s44, v43\n"
      "v_min_u32_e64 v43, s44, v43\n"
      "s_movk_i32 s93, 4176\n"
      "s_cmp_lt_u32 s41, 209\n"
      "s_cselect_b32 s92, 209, 47\n"
      "s_cselect_b32 s93, 799, s93\n"
      "s_cselect_b32 s94, 141, 0\n"
      "s_add_u32 s93, s93, s41\n"
      "v_lshl_add_u32 v42, v43, 8, s41\n"
      "v_mul_u32_u24 v60, s92, v43\n"
      "v_add_u32 v60, s93, v60\n"
      "v_cmp_lt_u32 vcc, 16, v43\n"
      "v_subrev_u32 v54, 5184, v60\n"
      "v_add_u32 v54, s94, v54\n"
      "v_cndmask_b32 v42, v42, v60, vcc\n"
      "v_cmp_le_u32 vcc, 5184, v42\n"
      "v_mul_u32_u24 v52, 58255, v42\n"
      "v_lshrrev_b32_e64 v52, 22, v52\n"
      "v_mul_u32_u24 v45, 72, v52\n"
      "v_sub_u32 v45, v42, v45\n"
      "v_min_u32 v60, 72, v54\n"
      "v_cndmask_b32 v52, v52, v60, vcc\n"
      "v_subrev_u32 v60, 72, v54\n"
      "v_min_u32 v60, 72, v60\n"
      "v_cndmask_b32 v45, v45, v60, vcc\n"
      "v_mad_u32_u24 v54, v52, 7, 9\n"
      "v_subrev_u32 v42, 0x1ff, v54\n"
      "v_min_u32 v54, v54, v42\n"
      "v_mad_u32_u24 v52, v45, 7, 9\n"
      "v_subrev_u32 v45, 0x1ff, v52\n"
      "v_min_u32 v52, v52, v45\n"
      "v_mul_u32_u24 v54, 0x1ff, v54\n"
      "v_add_u32 v54, v54, v52\n"
      "v_mul_u32_u24 v45, 0x180, v54\n"
      "v_bfrev_b32 v43, 1\n"
      "v_cndmask_b32 v45, v45, v43, s[72:73]\n"
      "v_xor_b32 v53, v53, v40\n"
      "v_lshl_add_u32 v45, v53, 4, v45\n"
      "s_movk_i32 s95, 504\n"
      "s_add_u32 s92, s40, 0\n"
      "s_mul_i32 s93, s92, 43\n"
      "s_lshr_b32 s93, s93, 8\n"
      "s_mul_i32 s94, s93, 6\n"
      "s_sub_u32 s92, s92, s94\n"
      "s_lshl_b32 s92, s92, 3\n"
      "s_lshl_b32 s93, s93, 7\n"
      "v_add_u32 v42, s92, v49\n"
      "v_bfe_u32 v60, v42, 1, 3\n"
      "v_xor_b32 v60, v60, v40\n"
      "v_lshl_add_u32 v60, v60, 4, s93\n"
      "v_mul_u32_u24 v43, 37, v42\n"
      "v_lshrrev_b32 v43, 8, v43\n"
      "v_mad_u32_u24 v16, v43, s95, v42\n"
      "v_mul_u32_u24 v16, 384, v16\n"
      "v_add_u32 v16, v16, v60\n"
      "s_add_u32 s92, s40, 4\n"
      "s_mul_i32 s93, s92, 43\n"
      "s_lshr_b32 s93, s93, 8\n"
      "s_mul_i32 s94, s93, 6\n"
      "s_sub_u32 s92, s92, s94\n"
      "s_lshl_b32 s92, s92, 3\n"
      "s_lshl_b32 s93, s93, 7\n"
      "v_add_u32 v53, s92, v49\n"
      "v_bfe_u32 v52, v53, 1, 3\n"
      "v_xor_b32 v52, v52, v40\n"
      "v_lshl_add_u32 v52, v52, 4, s93\n"
      "v_mul_u32_u24 v60, 37, v53\n"
      "v_lshrrev_b32 v60, 8, v60\n"
      "v_mad_u32_u24 v17, v60, s95, v53\n"
      "v_mul_u32_u24 v17, 384, v17\n"
      "v_add_u32 v17, v17, v52\n"
      "s_add_u32 s92, s40, 8\n"
      "s_mul_i32 s93, s92, 43\n"
      "s_lshr_b32 s93, s93, 8\n"
      "s_mul_i32 s94, s93, 6\n"
      "s_sub_u32 s92, s92, s94\n"
      "s_lshl_b32 s92, s92, 3\n"
      "s_lshl_b32 s93, s93, 7\n"
      "v_add_u32 v52, s92, v49\n"
      "v_bfe_u32 v54, v52, 1, 3\n"
      "v_xor_b32 v54, v54, v40\n"
      "v_lshl_add_u32 v54, v54, 4, s93\n"
      "v_mul_u32_u24 v64, 37, v52\n"
      "v_lshrrev_b32 v64, 8, v64\n"
      "v_mad_u32_u24 v18, v64, s95, v52\n"
      "v_mul_u32_u24 v18, 384, v18\n"
      "v_add_u32 v18, v18, v54\n"
      "s_add_u32 s92, s40, 12\n"
      "s_mul_i32 s93, s92, 43\n"
      "s_lshr_b32 s93, s93, 8\n"
      "s_mul_i32 s94, s93, 6\n"
      "s_sub_u32 s92, s92, s94\n"
      "s_lshl_b32 s92, s92, 3\n"
      "s_lshl_b32 s93, s93, 7\n"
      "v_add_u32 v54, s92, v49\n"
      "v_bfe_u32 v55, v54, 1, 3\n"
      "v_xor_b32 v55, v55, v40\n"
      "v_lshl_add_u32 v55, v55, 4, s93\n"
      "v_mul_u32_u24 v67, 37, v54\n"
      "v_lshrrev_b32 v67, 8, v67\n"
      "v_mad_u32_u24 v19, v67, s95, v54\n"
      "v_mul_u32_u24 v19, 384, v19\n"
      "v_add_u32 v19, v19, v55\n"
      "s_add_u32 s92, s40, 16\n"
      "s_mul_i32 s93, s92, 43\n"
      "s_lshr_b32 s93, s93, 8\n"
      "s_mul_i32 s94, s93, 6\n"
      "s_sub_u32 s92, s92, s94\n"
      "s_lshl_b32 s92, s92, 3\n"
      "s_lshl_b32 s93, s93, 7\n"
      "s_cmp_lt_u32 s40, 2\n"
      "s_cselect_b32 s93, s93, 0x80000000\n"
      "v_add_u32 v55, s92, v49\n"
      "v_bfe_u32 v49, v55, 1, 3\n"
      "v_xor_b32_e64 v49, v49, v40\n"
      "v_lshl_add_u32 v49, v49, 4, s93\n"
      "v_mul_u32_u24 v58, 37, v55\n"
      "v_lshrrev_b32 v58, 8, v58\n"
      "v_mad_u32_u24 v15, v58, s95, v55\n"
      "v_mul_u32_u24 v15, 384, v15\n"
      "v_add_u32 v15, v15, v49\n"
      "s_add_u32 s92, s41, 0\n"
      "s_mul_i32 s93, s92, 58255\n"
      "s_lshr_b32 s93, s93, 22\n"
      "s_add_u32 s47, s92, s93\n"
      "s_mul_i32 s47, s47, 4802\n"
      "s_and_b32 s47, s47, -4\n"
      "s_mul_i32 s93, s93, 439\n"
      "s_add_u32 s45, s92, s93\n"
      "s_mul_i32 s45, s45, 2688\n"
      "s_add_u32 s45, s45, 589824\n"
      "s_add_u32 s92, s41, 256\n"
      "s_mul_i32 s93, s92, 58255\n"
      "s_lshr_b32 s93, s93, 22\n"
      "s_add_u32 s74, s92, s93\n"
      "s_mul_i32 s74, s74, 4802\n"
      "s_and_b32 s74, s74, -4\n"
      "s_mul_i32 s93, s93, 439\n"
      "s_add_u32 s46, s92, s93\n"
      "s_mul_i32 s46, s46, 2688\n"
      "s_add_u32 s46, s46, 589824\n"
      "s_add_u32 m0, s64, 149504\n"
      "s_nop 0\n"
      "buffer_load_dwordx4 v45, s[16:19], 0 offen  lds\n"
      "s_add_u32 m0, s64, 153472\n"
      "s_nop 0\n"
      "buffer_load_dwordx4 v45, s[16:19], 0 offen offset:128  lds\n"
      "s_add_u32 m0, s64, 157440\n"
      "s_nop 0\n"
      "buffer_load_dwordx4 v45, s[16:19], 0 offen offset:256  lds\n"
      "s_add_u32 m0, s64, 0\n"
      "s_nop 0\n"
      "buffer_load_dwordx4 v16, s[16:19], s45 offen  lds\n"
      "s_add_u32 m0, s64, 4096\n"
      "s_nop 0\n"
      "buffer_load_dwordx4 v17, s[16:19], s45 offen  lds\n"
      "s_add_u32 m0, s64, 8192\n"
      "s_nop 0\n"
      "buffer_load_dwordx4 v18, s[16:19], s45 offen  lds\n"
      "s_add_u32 m0, s64, 12288\n"
      "s_nop 0\n"
      "buffer_load_dwordx4 v19, s[16:19], s45 offen  lds\n"
      "s_add_u32 m0, s65, 16384\n"
      "s_nop 0\n"
      "buffer_load_dwordx4 v15, s[16:19], s45 offen  lds\n"
      "v_mul_u32_u24 v49, 7, v43\n"
      "v_sub_u32 v49, v42, v49\n"
      "v_lshrrev_b32 v49, 2, v49\n"
      "v_lshrrev_b32 v45, 2, v43\n"
      "v_lshl_or_b32 v49, v49, 1, v45\n"
      "v_mov_b32 v36, v49\n"
      "v_mul_u32_u24 v45, 7, v60\n"
      "v_sub_u32 v45, v53, v45\n"
      "v_lshrrev_b32 v45, 2, v45\n"
      "v_lshrrev_b32 v43, 2, v60\n"
      "v_lshl_or_b32 v45, v45, 1, v43\n"
      "v_lshl_or_b32 v36, v45, 2, v36\n"
      "v_mul_u32_u24 v42, 7, v64\n"
      "v_sub_u32 v42, v52, v42\n"
      "v_lshrrev_b32 v42, 2, v42\n"
      "v_lshrrev_b32 v43, 2, v64\n"
      "v_lshl_or_b32 v42, v42, 1, v43\n"
      "v_lshl_or_b32 v36, v42, 4, v36\n"
      "v_mul_u32_u24 v42, 7, v67\n"
      "v_sub_u32 v42, v54, v42\n"
      "v_lshrrev_b32 v42, 2, v42\n"
      "v_lshrrev_b32 v49, 2, v67\n"
      "v_lshl_or_b32 v42, v42, 1, v49\n"
      "v_lshl_or_b32 v36, v42, 6, v36\n"
      "v_mul_u32_u24 v42, 7, v58\n"
      "v_sub_u32 v42, v55, v42\n"
      "v_lshrrev_b32 v42, 2, v42\n"
      "v_lshrrev_b32 v43, 2, v58\n"
      "v_lshl_or_b32 v42, v42, 1, v43\n"
      "v_lshl_or_b32 v36, v42, 8, v36\n"
      "v_and_b32 v42, 15, v51\n"
      "v_lshrrev_b32 v43, 4, v51\n"
      "v_and_b32 v45, 3, v51\n"
      "v_bfe_u32 v49, v51, 2, 2\n"
      "s_mov_b32 s94, 18432\n"
      "v_and_b32 v58, 1, v43\n"
      "v_and_b32_e64 v53, 2, v49\n"
      "v_lshl_or_b32 v58, v58, 2, v53\n"
      "s_and_b32 s92, s40, 1\n"
      "s_mul_i32 s92, s92, 32\n"
      "s_add_u32 s92, s92, 0\n"
      "s_add_u32 s92, s92, s75\n"
      "v_lshl_add_u32 v53, v45, 3, s92\n"
      "v_lshrrev_b32 v219, 7, v53\n"
      "v_lshl_add_u32 v219, v219, 2, v43\n"
      "v_lshl_add_u32 v219, v219, 3, v49\n"
      "v_and_b32 v54, 0x7f, v53\n"
      "v_lshlrev_b32 v219, 7, v219\n"
      "v_lshlrev_b32 v55, 4, v58\n"
      "v_xor_b32 v54, v54, v55\n"
      "v_add3_u32 v219, v219, v54, s94\n"
      "s_and_b32 s92, s40, 1\n"
      "s_mul_i32 s92, s92, 32\n"
      "s_add_u32 s92, s92, 32\n"
      "s_add_u32 s92, s92, s75\n"
      "v_lshl_add_u32 v53, v45, 3, s92\n"
      "v_lshrrev_b32_e64 v218, 7, v53\n"
      "v_lshl_add_u32 v218, v218, 2, v43\n"
      "v_lshl_add_u32 v218, v218, 3, v49\n"
      "v_and_b32 v54, 0x7f, v53\n"
      "v_lshlrev_b32 v218, 7, v218\n"
      "v_lshlrev_b32 v55, 4, v58\n"
      "v_xor_b32 v54, v54, v55\n"
      "v_add3_u32 v218, v218, v54, s94\n"
      "s_and_b32 s92, s40, 1\n"
      "s_mul_i32 s92, s92, -64\n"
      "s_add_u32 s92, s92, 64\n"
      "s_add_u32 s92, s92, s75\n"
      "v_lshl_add_u32 v53, v45, 3, s92\n"
      "v_lshrrev_b32_e64 v126, 7, v53\n"
      "v_lshl_add_u32 v126, v126, 2, v43\n"
      "v_lshl_add_u32 v126, v126, 3, v49\n"
      "v_and_b32 v54, 0x7f, v53\n"
      "v_lshlrev_b32 v126, 7, v126\n"
      "v_lshlrev_b32 v55, 4, v58\n"
      "v_xor_b32 v54, v54, v55\n"
      "v_add3_u32 v126, v126, v54, s94\n"
      "v_lshrrev_b32 v54, 1, v43\n"
      "v_bfe_u32 v52, v51, 2, 3\n"
      "v_and_b32_e64 v53, 2, v49\n"
      "v_lshl_or_b32 v58, v54, 2, v53\n"
      "v_lshl_add_u32 v53, v45, 4, s75\n"
      "v_lshrrev_b32_e64 v175, 7, v53\n"
      "v_lshl_add_u32 v175, v175, 2, v54\n"
      "v_lshl_add_u32 v175, v175, 3, v52\n"
      "v_and_b32 v49, 0x7f, v53\n"
      "v_lshlrev_b32 v175, 7, v175\n"
      "v_lshlrev_b32 v55, 4, v58\n"
      "v_xor_b32_e64 v49, v49, v55\n"
      "v_add3_u32 v175, v175, v49, s94\n"
      "s_add_u32 s92, s75, 64\n"
      "v_lshl_add_u32 v53, v45, 3, s92\n"
      "v_lshrrev_b32 v174, 7, v53\n"
      "v_lshl_add_u32 v174, v174, 2, v54\n"
      "v_lshl_add_u32 v174, v174, 3, v52\n"
      "v_and_b32 v49, 0x7f, v53\n"
      "v_lshlrev_b32 v174, 7, v174\n"
      "v_lshlrev_b32 v55, 4, v58\n"
      "v_xor_b32_e64 v49, v49, v55\n"
      "v_add3_u32 v174, v174, v49, s94\n"
      "v_add_u32 v124, 49152, v219\n"
      "v_add_u32 v189, 49152, v218\n"
      "v_add_u32 v188, 49152, v126\n"
      "v_add_u32 v77, 49152, v175\n"
      "v_add_u32 v81, 49152, v174\n"
      "v_bfe_u32 v55, v51, 1, 3\n"
      "v_xor_b32 v55, v55, v43\n"
      "v_lshlrev_b32 v0, 7, v42\n"
      "v_lshl_add_u32 v0, v55, 4, v0\n"
      "v_xor_b32_e64 v1, 64, v0\n"
      "v_mul_u32_u24 v2, 768, v43\n"
      "v_lshl_add_u32 v2, v42, 4, v2\n"
      "v_add_u32 v2, 36864, v2\n"
      "s_add_u32 s92, s40, 1\n"
      "s_lshr_b32 s92, s92, 1\n"
      "s_add_u32 s92, s92, s40\n"
      "s_lshr_b32 s93, s40, 1\n"
      "s_mul_i32 s93, s93, 3\n"
      "s_add_u32 s93, s93, 1\n"
      "s_and_b32 s95, s40, 1\n"
      "s_lshl_b32 s95, s95, 3\n"
      "s_mul_i32 s94, s92, 3072\n"
      "v_add_u32 v3, s94, v2\n"
      "s_mul_i32 s94, s93, 3072\n"
      "s_add_u32 s94, s94, s95\n"
      "v_add_u32 v4, s94, v2\n"
      "s_lshl_b32 s92, s92, 11\n"
      "s_lshl_b32 s93, s93, 11\n"
      "s_add_u32 s93, s93, s95\n"
      "v_lshl_add_u32 v49, v43, 3, s92\n"
      "v_mov_b32 v58, s93\n"
      "v_cmp_eq_u32 vcc, 2, v43\n"
      "v_lshrrev_b32 v55, 2, v42\n"
      "v_lshlrev_b32 v12, 9, v55\n"
      "v_cndmask_b32 v49, v49, v58, vcc\n"
      "v_lshl_add_u32 v12, v45, 1, v12\n"
      "v_cmp_eq_u32 vcc, 3, v43\n"
      "v_mov_b32 v58, 0x100000\n"
      "v_add_u32 v12, 149504, v12\n"
      "v_cndmask_b32 v49, v49, v58, vcc\n"
      "v_add_u32 v12, v12, v49\n"
      "s_lshl_b32 s94, s40, 7\n"
      "s_add_u32 s94, s94, 119552\n"
      "v_lshlrev_b32 v13, 4, v55\n"
      "v_lshl_add_u32 v13, v45, 1, v13\n"
      "v_and_b32 v49, 1, v43\n"
      "v_lshl_add_u32 v13, v49, 3, v13\n"
      "v_lshrrev_b32 v55, 1, v43\n"
      "v_lshl_add_u32 v13, v55, 6, v13\n"
      "v_add_u32_e64 v13, s94, v13\n"
      "v_cmp_ne_u32 vcc, 0, v45\n"
      "v_lshl_add_u32 v14, v43, 4, s94\n"
      "s_lshl_b32 s94, s75, 1\n"
      "s_add_u32 s94, s94, 124928\n"
      "v_mad_u32_u24 v10, v43, 48, s94\n"
      "v_cndmask_b32 v14, v14, v58, vcc\n"
      "v_cndmask_b32 v10, v10, v58, vcc\n"
      "v_mov_b32 v58, 0x3f803f80\n"
      "v_cndmask_b32_e64 v32, v58, 0, vcc\n"
      "v_mov_b32 v33, v32\n"
      "v_mov_b32 v34, v32\n"
      "v_mov_b32 v35, v32\n"
      "s_add_u32 s94, s75, 125696\n"
      "v_lshl_add_u32 v11, v42, 1, s94\n"
      "s_lshr_b32 s94, s40, 1\n"
      "s_lshl_b32 s94, s94, 10\n"
      "s_add_u32 s94, s94, s95\n"
      "s_add_u32 s94, s94, 115456\n"
      "v_lshl_add_u32 v8, v55, 4, v42\n"
      "v_lshl_add_u32 v8, v8, 4, s94\n"
      "v_lshl_add_u32 v8, v49, 2, v8\n"
      "v_add_u32 v9, 115456, v46\n"
      "v_add_u32 v7, 73728, v27\n"
      "s_lshl_b32 s94, s40, 2\n"
      "s_add_u32 s94, s94, 120064\n"
      "v_add_u32 v31, s94, v46\n"
      "s_mul_i32 s94, s40, 6\n"
      "v_mul_u32_u24 v46, 384, v42\n"
      "v_add_u32 v46, 73728, v46\n"
      "v_add_u32 v5, s94, v43\n"
      "v_xor_b32 v5, v5, v40\n"
      "v_lshl_add_u32 v5, v5, 4, v46\n"
      "v_add3_u32 v6, v55, s94, 4\n"
      "v_xor_b32 v6, v6, v40\n"
      "v_lshl_add_u32 v6, v6, 4, v46\n"
      "v_lshl_add_u32 v6, v49, 3, v6\n"
      "s_movk_i32 s95, 504\n"
      "s_lshl_b32 s92, s40, 6\n"
      "s_cmp_lt_u32 s40, 2\n"
      "s_cselect_b32 s93, 0, 0x80000000\n"
      "v_add_u32 v49, s92, v51\n"
      "s_add_u32 s92, s92, 256\n"
      "v_mul_u32_u24 v46, 2731, v49\n"
      "v_lshrrev_b32 v46, 16, v46\n"
      "v_mul_u32_u24 v40, 24, v46\n"
      "v_sub_u32 v40, v49, v40\n"
      "v_and_b32 v49, 7, v46\n"
      "v_xor_b32 v40, v40, v49\n"
      "v_lshlrev_b32 v40, 4, v40\n"
      "v_mul_u32_u24 v49, 37, v46\n"
      "v_lshrrev_b32 v49, 8, v49\n"
      "v_mad_u32_u24 v20, v49, s95, v46\n"
      "v_mul_u32_u24 v20, 384, v20\n"
      "v_add_u32 v20, v20, v40\n"
      "v_mul_u32_u24 v40, 7, v49\n"
      "v_sub_u32 v40, v46, v40\n"
      "v_lshrrev_b32 v40, 2, v40\n"
      "v_lshrrev_b32_e64 v46, 2, v49\n"
      "v_lshl_or_b32 v40, v40, 1, v46\n"
      "v_lshl_or_b32 v36, v40, 10, v36\n"
      "v_add_u32 v46, s92, v51\n"
      "s_add_u32 s92, s92, 256\n"
      "v_mul_u32_u24 v40, 2731, v46\n"
      "v_lshrrev_b32 v40, 16, v40\n"
      "v_mul_u32_u24 v49, 24, v40\n"
      "v_sub_u32 v49, v46, v49\n"
      "v_and_b32 v46, 7, v40\n"
      "v_xor_b32 v49, v49, v46\n"
      "v_lshlrev_b32 v49, 4, v49\n"
      "v_mul_u32_u24 v46, 37, v40\n"
      "v_lshrrev_b32_e64 v46, 8, v46\n"
      "v_mad_u32_u24 v21, v46, s95, v40\n"
      "v_mul_u32_u24 v21, 384, v21\n"
      "v_add_u32 v21, v21, v49\n"
      "v_mul_u32_u24 v49, 7, v46\n"
      "v_sub_u32 v49, v40, v49\n"
      "v_lshrrev_b32 v49, 2, v49\n"
      "v_lshrrev_b32 v40, 2, v46\n"
      "v_lshl_or_b32 v49, v49, 1, v40\n"
      "v_lshl_or_b32 v36, v49, 12, v36\n"
      "v_add_u32 v40, s92, v51\n"
      "s_add_u32 s92, s92, 256\n"
      "v_mul_u32_u24 v49, 2731, v40\n"
      "v_lshrrev_b32 v49, 16, v49\n"
      "v_mul_u32_u24 v46, 24, v49\n"
      "v_sub_u32 v46, v40, v46\n"
      "v_and_b32 v40, 7, v49\n"
      "v_xor_b32 v46, v46, v40\n"
      "v_lshlrev_b32 v46, 4, v46\n"
      "v_mul_u32_u24 v40, 37, v49\n"
      "v_lshrrev_b32 v40, 8, v40\n"
      "v_mad_u32_u24 v22, v40, s95, v49\n"
      "v_mul_u32_u24 v22, 384, v22\n"
      "v_add_u32 v22, v22, v46\n"
      "v_mul_u32_u24 v46, 7, v40\n"
      "v_sub_u32 v46, v49, v46\n"
      "v_lshrrev_b32 v46, 2, v46\n"
      "v_lshrrev_b32 v49, 2, v40\n"
      "v_lshl_or_b32 v46, v46, 1, v49\n"
      "v_lshl_or_b32 v36, v46, 14, v36\n"
      "v_add_u32 v46, s92, v51\n"
      "s_add_u32 s92, s92, 256\n"
      "v_mul_u32_u24 v49, 2731, v46\n"
      "v_lshrrev_b32 v49, 16, v49\n"
      "v_mul_u32_u24 v40, 24, v49\n"
      "v_sub_u32 v40, v46, v40\n"
      "v_and_b32 v46, 7, v49\n"
      "v_xor_b32 v40, v40, v46\n"
      "v_lshlrev_b32 v40, 4, v40\n"
      "v_mul_u32_u24 v46, 37, v49\n"
      "v_lshrrev_b32 v46, 8, v46\n"
      "v_mad_u32_u24 v23, v46, s95, v49\n"
      "v_mul_u32_u24 v23, 384, v23\n"
      "v_add_u32 v23, v23, v40\n"
      "v_mul_u32_u24 v40, 7, v46\n"
      "v_sub_u32 v40, v49, v40\n"
      "v_lshrrev_b32 v40, 2, v40\n"
      "v_lshrrev_b32_e64 v49, 2, v46\n"
      "v_lshl_or_b32 v40, v40, 1, v49\n"
      "v_lshl_or_b32 v36, v40, 16, v36\n"
      "v_add_u32 v40, s92, v51\n"
      "s_add_u32 s92, s92, 256\n"
      "v_mul_u32_u24 v46, 2731, v40\n"
      "v_lshrrev_b32 v46, 16, v46\n"
      "v_mul_u32_u24 v49, 24, v46\n"
      "v_sub_u32 v49, v40, v49\n"
      "v_and_b32 v40, 7, v46\n"
      "v_xor_b32 v49, v49, v40\n"
      "v_lshl_add_u32 v49, v49, 4, s93\n"
      "v_mul_u32_u24 v40, 37, v46\n"
      "v_lshrrev_b32 v40, 8, v40\n"
      "v_mad_u32_u24 v25, v40, s95, v46\n"
      "v_mul_u32_u24 v25, 384, v25\n"
      "v_add_u32 v25, v25, v49\n"
      "v_mul_u32_u24 v49, 7, v40\n"
      "v_sub_u32 v49, v46, v49\n"
      "v_lshrrev_b32 v49, 2, v49\n"
      "v_lshrrev_b32 v46, 2, v40\n"
      "v_lshl_or_b32 v49, v49, 1, v46\n"
      "v_lshl_or_b32 v36, v49, 18, v36\n"
      "v_subrev_u32 v40, 2, v51\n"
      "v_cmp_ge_u32 s[72:73], s44, v40\n"
      "v_min_u32 v40, s44, v40\n"
      "s_movk_i32 s93, 4176\n"
      "s_cmp_lt_u32 s41, 209\n"
      "s_cselect_b32 s92, 209, 47\n"
      "s_cselect_b32 s93, 799, s93\n"
      "s_cselect_b32 s94, 141, 0\n"
      "s_add_u32 s93, s93, s41\n"
      "v_lshl_add_u32 v46, v40, 8, s41\n"
      "v_mul_u32_u24 v49, s92, v40\n"
      "v_add_u32 v49, s93, v49\n"
      "v_cmp_lt_u32 vcc, 16, v40\n"
      "v_subrev_u32 v45, 5184, v49\n"
      "v_add_u32 v45, s94, v45\n"
      "v_cndmask_b32 v46, v46, v49, vcc\n"
      "v_cmp_le_u32 vcc, 5184, v46\n"
      "v_mul_u32_u24 v55, 58255, v46\n"
      "v_lshrrev_b32 v55, 22, v55\n"
      "v_mul_u32_u24 v58, 72, v55\n"
      "v_sub_u32_e64 v58, v46, v58\n"
      "v_min_u32 v49, 72, v45\n"
      "v_cndmask_b32 v55, v55, v49, vcc\n"
      "v_subrev_u32 v49, 72, v45\n"
      "v_min_u32 v49, 72, v49\n"
      "v_cndmask_b32 v58, v58, v49, vcc\n"
      "v_mul_u32_u24 v38, 73, v55\n"
      "v_add_u32 v38, v38, v58\n"
      "v_mul_u32_u24 v38, 4802, v38\n"
      "v_mul_u32_u24 v40, 0x1ff, v55\n"
      "v_add_u32_e64 v40, v40, v58\n"
      "v_mul_u32_u24 v40, 2688, v40\n"
      "v_add_u32 v40, 589824, v40\n"
      "v_cmp_eq_u32 vcc, 72, v55\n"
      "v_or_b32 v49, 0x80000000, v40\n"
      "s_nop 0\n"
      "v_cndmask_b32 v40, v40, v49, vcc\n"
      "v_cmp_eq_u32 vcc, 72, v58\n"
      "v_or_b32_e64 v49, 0x40000000, v40\n"
      "s_nop 0\n"
      "v_cndmask_b32 v40, v40, v49, vcc\n"
      "v_mov_b32 v45, 0x20000000\n"
      "v_cndmask_b32 v40, v45, v40, s[72:73]\n"
      "v_cndmask_b32 v38, v45, v38, s[72:73]\n"
      "v_mov_b32 v37, v45\n"
      "s_nop 1\n"
      "v_mov_b32_dpp v37, v40 wave_shr:1 row_mask:0xf bank_mask:0xf\n"
      "v_mov_b32 v79, 0\n"
      "v_mov_b32 v83, 0\n"
      "v_mov_b32_e64 v87, 0\n"
      "v_mov_b32_e64 v91, 0\n"
      "v_mov_b32_e64 v95, 0\n"
      "v_mul_u32_u24 v80, 1168, v42\n"
      "s_add_u32 s93, s75, 125696\n"
      "v_lshl_add_u32 v78, v43, 3, v80\n"
      "v_add_u32 v78, s93, v78\n"
      "s_lshl_b32 s92, s75, 1\n"
      "s_add_u32 s92, s92, 124928\n"
      "v_mad_u32_u24 v49, v43, 48, s92\n"
      "v_add_u32 v80, v80, v49\n"
      "v_add_u32 v133, 149504, v0\n"
      "v_add_u32 v143, 149504, v1\n"
      "s_mov_b32 s78, 0\n"
      "s_add_u32 m0, s33, 0\n"
      "s_nop 0\n"
      "buffer_load_dwordx4 v98, s[28:31], s78 offen  lds\n"
      "s_add_u32 m0, s33, 3968\n"
      "s_nop 0\n"
      "buffer_load_dwordx4 v98, s[28:31], s78 offen offset:128  lds\n"
      "s_add_u32 m0, s33, 7936\n"
      "s_nop 0\n"
      "buffer_load_dwordx4 v98, s[28:31], s78 offen offset:256  lds\n"
      "s_add_u32 m0, s33, 11904\n"
      "s_nop 0\n"
      "buffer_load_dwordx4 v98, s[28:31], s78 offen offset:384  lds\n"
      "s_add_u32 m0, s33, 15872\n"
      "s_nop 0\n"
      "buffer_load_dwordx4 v98, s[28:31], s78 offen offset:512  lds\n"
      "s_add_u32 m0, s33, 19840\n"
      "s_nop 0\n"
      "buffer_load_dwordx4 v98, s[28:31], s78 offen offset:640  lds\n"
      "s_waitcnt lgkmcnt(0)\n"
      "s_pack_ll_b32_b16 s92, s0, s8\n"
      "s_waitcnt vmcnt(17)\n"
      "v_dot2_f32_bf16 v42, s92, v44, 0\n"
      "s_pack_hh_b32_b16 s93, s0, s8\n"
      "v_dot2_f32_bf16 v43, s93, v44, 0\n"
      "s_pack_ll_b32_b16 s94, s1, s9\n"
      "v_dot2_f32_bf16 v40, s94, v44, 0\n"
      "s_pack_hh_b32_b16 s95, s1, s9\n"
      "v_dot2_f32_bf16 v45, s95, v44, 0\n"
      "s_pack_ll_b32_b16 s92, s2, s10\n"
      "v_dot2_f32_bf16 v46, s92, v44, 0\n"
      "s_pack_hh_b32_b16 s93, s2, s10\n"
      "v_dot2_f32_bf16 v55, s93, v44, 0\n"
      "s_pack_ll_b32_b16 s94, s3, s11\n"
      "v_dot2_f32_bf16 v58, s94, v44, 0\n"
      "s_pack_hh_b32_b16 s95, s3, s11\n"
      "v_dot2_f32_bf16 v52, s95, v44, 0\n"
      "s_pack_ll_b32_b16 s92, s4, s12\n"
      "v_dot2_f32_bf16 v53, s92, v44, 0\n"
      "s_pack_hh_b32_b16 s93, s4, s12\n"
      "v_dot2_f32_bf16 v54, s93, v44, 0\n"
      "s_pack_ll_b32_b16 s94, s5, s13\n"
      "v_dot2_f32_bf16 v64, s94, v44, 0\n"
      "s_pack_hh_b32_b16 s95, s5, s13\n"
      "v_dot2_f32_bf16 v60, s95, v44, 0\n"
      "s_pack_ll_b32_b16 s92, s6, s14\n"
      "v_dot2_f32_bf16 v67, s92, v44, 0\n"
      "s_pack_hh_b32_b16 s93, s6, s14\n"
      "v_dot2_f32_bf16 v50, s93, v44, 0\n"
      "s_pack_ll_b32_b16 s94, s7, s15\n"
      "v_dot2_f32_bf16 v48, s94, v44, 0\n"
      "s_pack_hh_b32_b16 s95, s7, s15\n"
      "v_dot2_f32_bf16 v63, s95, v44, 0\n"
      "s_load_dwordx16 s[0:15], s[84:85], 64\n"
      "s_lshl_b32 s92, s40, 4\n"
      "s_lshr_b64 s[66:67], s[66:67], s92\n"
      "s_lshl_b32 s66, s66, 16\n"
      "v_mov_b32_e64 v44, s66\n"
      "v_max_f32 v44, 0xc2c80000, v44\n"
      "v_min_f32 v44, 0x40930000, v44\n"
      "v_mul_f32 v44, 0x3fb8aa3b, v44\n"
      "v_exp_f32 v44, v44\n"
      "s_nop 0\n"
      "v_cvt_pk_bf16_f32 v44, v44, v44\n"
      "v_lshlrev_b32 v44, 16, v44\n"
      "v_max_f32 v42, 0, v42\n"
      "v_max_f32 v43, 0, v43\n"
      "v_cvt_pk_bf16_f32 v42, v42, v43\n"
      "v_max_f32 v40, 0, v40\n"
      "v_max_f32 v45, 0, v45\n"
      "v_cvt_pk_bf16_f32 v40, v40, v45\n"
      "v_max_f32 v46, 0, v46\n"
      "v_max_f32 v55, 0, v55\n"
      "v_cvt_pk_bf16_f32 v46, v46, v55\n"
      "v_max_f32 v58, 0, v58\n"
      "v_max_f32 v52, 0, v52\n"
      "v_cvt_pk_bf16_f32 v58, v58, v52\n"
      "v_max_f32 v53, 0, v53\n"
      "v_max_f32 v54, 0, v54\n"
      "v_cvt_pk_bf16_f32 v53, v53, v54\n"
      "v_max_f32 v64, 0, v64\n"
      "v_max_f32 v60, 0, v60\n"
      "v_cvt_pk_bf16_f32 v64, v64, v60\n"
      "v_max_f32 v67, 0, v67\n"
      "v_max_f32 v50, 0, v50\n"
      "v_cvt_pk_bf16_f32 v67, v67, v50\n"
      "v_max_f32 v48, 0, v48\n"
      "v_max_f32 v63, 0, v63\n"
      "v_cvt_pk_bf16_f32 v48, v48, v63\n"
      "s_pack_ll_b32_b16 s92, s48, s50\n"
      "v_dot2_f32_bf16 v50, s92, v42, 0\n"
      "s_pack_hh_b32_b16 s93, s48, s50\n"
      "v_dot2_f32_bf16 v45, s93, v42, 0\n"
      "s_pack_ll_b32_b16 s94, s49, s51\n"
      "v_dot2_f32_bf16 v43, s94, v42, 0\n"
      "s_pack_hh_b32_b16 s95, s49, s51\n"
      "v_dot2_f32_bf16 v52, s95, v42, 0\n"
      "s_pack_ll_b32_b16 s92, s52, s54\n"
      "v_dot2_f32_bf16 v50, s92, v40, v50\n"
      "s_pack_hh_b32_b16 s93, s52, s54\n"
      "v_dot2_f32_bf16 v45, s93, v40, v45\n"
      "s_pack_ll_b32_b16 s94, s53, s55\n"
      "v_dot2_f32_bf16 v43, s94, v40, v43\n"
      "s_pack_hh_b32_b16 s95, s53, s55\n"
      "v_dot2_f32_bf16 v52, s95, v40, v52\n"
      "s_pack_ll_b32_b16 s92, s56, s58\n"
      "v_dot2_f32_bf16 v50, s92, v46, v50\n"
      "s_pack_hh_b32_b16 s93, s56, s58\n"
      "v_dot2_f32_bf16 v45, s93, v46, v45\n"
      "s_pack_ll_b32_b16 s94, s57, s59\n"
      "v_dot2_f32_bf16 v43, s94, v46, v43\n"
      "s_pack_hh_b32_b16 s95, s57, s59\n"
      "v_dot2_f32_bf16 v52, s95, v46, v52\n"
      "s_pack_ll_b32_b16 s92, s60, s62\n"
      "v_dot2_f32_bf16 v50, s92, v58, v50\n"
      "s_pack_hh_b32_b16 s93, s60, s62\n"
      "v_dot2_f32_bf16 v45, s93, v58, v45\n"
      "s_pack_ll_b32_b16 s94, s61, s63\n"
      "v_dot2_f32_bf16 v43, s94, v58, v43\n"
      "s_pack_hh_b32_b16 s95, s61, s63\n"
      "v_dot2_f32_bf16 v52, s95, v58, v52\n"
      "s_waitcnt lgkmcnt(0)\n"
      "s_pack_ll_b32_b16 s92, s0, s2\n"
      "v_dot2_f32_bf16 v50, s92, v53, v50\n"
      "s_pack_hh_b32_b16 s93, s0, s2\n"
      "v_dot2_f32_bf16 v45, s93, v53, v45\n"
      "s_pack_ll_b32_b16 s94, s1, s3\n"
      "v_dot2_f32_bf16 v43, s94, v53, v43\n"
      "s_pack_hh_b32_b16 s95, s1, s3\n"
      "v_dot2_f32_bf16 v52, s95, v53, v52\n"
      "s_pack_ll_b32_b16 s92, s4, s6\n"
      "v_dot2_f32_bf16 v50, s92, v64, v50\n"
      "s_pack_hh_b32_b16 s93, s4, s6\n"
      "v_dot2_f32_bf16 v45, s93, v64, v45\n"
      "s_pack_ll_b32_b16 s94, s5, s7\n"
      "v_dot2_f32_bf16 v43, s94, v64, v43\n"
      "s_pack_hh_b32_b16 s95, s5, s7\n"
      "v_dot2_f32_bf16 v52, s95, v64, v52\n"
      "s_pack_ll_b32_b16 s92, s8, s10\n"
      "v_dot2_f32_bf16 v50, s92, v67, v50\n"
      "s_pack_hh_b32_b16 s93, s8, s10\n"
      "v_dot2_f32_bf16 v45, s93, v67, v45\n"
      "s_pack_ll_b32_b16 s94, s9, s11\n"
      "v_dot2_f32_bf16 v43, s94, v67, v43\n"
      "s_pack_hh_b32_b16 s95, s9, s11\n"
      "v_dot2_f32_bf16 v52, s95, v67, v52\n"
      "s_pack_ll_b32_b16 s92, s12, s14\n"
      "v_dot2_f32_bf16 v50, s92, v48, v50\n"
      "s_pack_hh_b32_b16 s93, s12, s14\n"
      "v_dot2_f32_bf16 v45, s93, v48, v45\n"
      "s_pack_ll_b32_b16 s94, s13, s15\n"
      "v_dot2_f32_bf16 v43, s94, v48, v43\n"
      "s_pack_hh_b32_b16 s95, s13, s15\n"
      "v_dot2_f32_bf16 v52, s95, v48, v52\n"
      "s_mov_b32 s53, 0x3fb8aa3b\n"
      "s_mov_b32 s51, 0x2a8cbccc\n"
      "s_mov_b32 s52, 0x3ebcbe62\n"
      "s_mov_b32 s58, 0xffff0000\n"
      "s_mov_b32 s59, 0x0\n"
      "s_mov_b32 s60, 0x0\n"
      "s_mov_b32 s61, 0xffff\n"
      "s_mov_b32 s62, 0x0\n"
      "s_mov_b32 s63, 0xffff0000\n"
      "v_mov_b32 v49, 0x41f00000\n"
      "v_cmp_nle_f32 s[54:55], v44, v49\n"
      "v_mul_f32 v44, s53, v44\n"
      "s_nop 0\n"
      "v_readfirstlane_b32 s50, v44\n"
      "v_cvt_pk_bf16_f32 v50, v50, v50\n"
      "v_lshlrev_b32 v50, 16, v50\n"
      "v_mul_f32 v50, 0xbfb8aa3b, v50\n"
      "v_exp_f32 v50, v50\n"
      "s_nop 0\n"
      "v_add_f32 v50, 1.0, v50\n"
      "v_rcp_f32 v50, v50\n"
      "s_nop 0\n"
      "v_cvt_pk_bf16_f32 v50, v50, v50\n"
      "v_lshlrev_b32 v50, 16, v50\n"
      "v_add_f32 v50, -0.5, v50\n"
      "v_mul_f32 v50, 0x41b8aa3b, v50\n"
      "ds_write_b32 v47, v50\n"
      "v_cvt_pk_bf16_f32 v45, v45, v45\n"
      "v_lshlrev_b32_e64 v45, 16, v45\n"
      "v_mul_f32 v45, 0xbfb8aa3b, v45\n"
      "v_exp_f32 v45, v45\n"
      "s_nop 0\n"
      "v_add_f32 v45, 1.0, v45\n"
      "v_rcp_f32_e64 v45, v45\n"
      "s_nop 0\n"
      "v_cvt_pk_bf16_f32 v45, v45, v45\n"
      "v_lshlrev_b32 v45, 16, v45\n"
      "v_add_f32 v45, -0.5, v45\n"
      "v_mul_f32 v45, 0x41b8aa3b, v45\n"
      "ds_write_b32 v47, v45 offset:704\n"
      "v_cvt_pk_bf16_f32 v43, v43, v43\n"
      "v_lshlrev_b32 v43, 16, v43\n"
      "v_mul_f32 v43, 0xbfb8aa3b, v43\n"
      "v_exp_f32 v43, v43\n"
      "s_nop 0\n"
      "v_add_f32 v43, 1.0, v43\n"
      "v_rcp_f32 v43, v43\n"
      "s_nop 0\n"
      "v_cvt_pk_bf16_f32 v43, v43, v43\n"
      "v_lshlrev_b32 v43, 16, v43\n"
      "v_add_f32 v43, -0.5, v43\n"
      "v_mul_f32 v43, 0x41b8aa3b, v43\n"
      "ds_write_b32 v47, v43 offset:1408\n"
      "v_cvt_pk_bf16_f32 v52, v52, v52\n"
      "v_lshlrev_b32 v52, 16, v52\n"
      "v_mul_f32 v52, 0xbfb8aa3b, v52\n"
      "v_exp_f32 v52, v52\n"
      "s_nop 0\n"
      "v_add_f32 v52, 1.0, v52\n"
      "v_rcp_f32 v52, v52\n"
      "s_nop 0\n"
      "v_cvt_pk_bf16_f32 v52, v52, v52\n"
      "v_lshlrev_b32_e64 v52, 16, v52\n"
      "v_add_f32_e64 v52, -0.5, v52\n"
      "v_mul_f32 v52, 0x41b8aa3b, v52\n"
      "ds_write_b32 v47, v52 offset:2112\n"
      "s_mul_i32 s76, s40, 704\n"
      "s_add_u32 s76, s76, 122112\n"
      "s_mov_b32 s79, 36864\n"
      "s_add_u32 m0, s33, 24576\n"
      "s_nop 0\n"
      "buffer_load_dwordx4 v98, s[28:31], s79 offen  lds\n"
      "s_add_u32 m0, s33, 28544\n"
      "s_nop 0\n"
      "buffer_load_dwordx4 v98, s[28:31], s79 offen offset:128  lds\n"
      "s_add_u32 m0, s33, 32512\n"
      "s_nop 0\n"
      "buffer_load_dwordx4 v98, s[28:31], s79 offen offset:256  lds\n"
      "s_add_u32 m0, s33, 36480\n"
      "s_nop 0\n"
      "buffer_load_dwordx4 v98, s[28:31], s79 offen offset:384  lds\n"
      "s_add_u32 m0, s33, 40448\n"
      "s_nop 0\n"
      "buffer_load_dwordx4 v98, s[28:31], s79 offen offset:512  lds\n"
      "s_add_u32 m0, s33, 44416\n"
      "s_nop 0\n"
      "buffer_load_dwordx4 v98, s[28:31], s79 offen offset:640  lds\n"
      "s_waitcnt vmcnt(20)\n"
      "s_waitcnt lgkmcnt(0)\n"
      "s_barrier\n"
      "v_and_b32 v52, 15, v51\n"
      "v_lshrrev_b32_e64 v53, 4, v51\n"
      "v_mul_u32_u24 v52, 196, v52\n"
      "v_add_u32 v52, 92160, v52\n"
      "v_lshl_add_u32 v53, v53, 4, v52\n"
      "v_min_u32 v67, 192, v41\n"
      "v_add_u32 v67, 92160, v67\n"
      "ds_read2_b32 v[40:41], v53 offset0:0 offset1:1\n"
      "ds_read2_b32 v[42:43], v53 offset0:2 offset1:3\n"
      "ds_read2_b32 v[44:45], v53 offset0:16 offset1:17\n"
      "ds_read2_b32 v[46:47], v53 offset0:18 offset1:19\n"
      "ds_read2_b32 v[48:49], v53 offset0:32 offset1:33\n"
      "ds_read2_b32 v[50:51], v53 offset0:34 offset1:35\n"
      "ds_read_b32 v235, v52 offset:192\n"
      "ds_read_b32 v232, v52 offset:3328\n"
      "ds_read_b32 v239, v52 offset:6464\n"
      "ds_read_b32 v237, v67 offset:9408\n"
      "s_mov_b32 s72, 0xffff\n"
      "s_mov_b32 s73, 0x0\n"
      "s_mov_b32 s66, 0xffffffff\n"
      "s_mov_b32 s67, 0x1ffff\n"
      "v_add_u32 v67, 3136, v53\n"
      "v_add_u32 v234, 6272, v53\n"
      "ds_read2_b32 v[52:53], v67 offset0:0 offset1:1\n"
      "ds_read2_b32 v[54:55], v67 offset0:2 offset1:3\n"
      "ds_read2_b32 v[56:57], v67 offset0:16 offset1:17\n"
      "ds_read2_b32 v[58:59], v67 offset0:18 offset1:19\n"
      "ds_read2_b32 v[60:61], v67 offset0:32 offset1:33\n"
      "ds_read2_b32 v[62:63], v67 offset0:34 offset1:35\n"
      "ds_read2_b32 v[64:65], v234 offset0:0 offset1:1\n"
      "ds_read2_b32 v[66:67], v234 offset0:2 offset1:3\n"
      "ds_read2_b32 v[68:69], v234 offset0:16 offset1:17\n"
      "ds_read2_b32 v[70:71], v234 offset0:18 offset1:19\n"
      "ds_read2_b32 v[72:73], v234 offset0:32 offset1:33\n"
      "ds_read2_b32 v[74:75], v234 offset0:34 offset1:35\n"
      "s_waitcnt lgkmcnt(11)\n"
      "v_lshl_add_u32 v40, v40, 2, s76\n"
      "v_lshl_add_u32 v41, v41, 2, s76\n"
      "v_lshl_add_u32 v42, v42, 2, s76\n"
      "v_lshl_add_u32 v43, v43, 2, s76\n"
      "v_lshl_add_u32 v44, v44, 2, s76\n"
      "v_lshl_add_u32 v45, v45, 2, s76\n"
      "v_lshl_add_u32 v46, v46, 2, s76\n"
      "v_lshl_add_u32 v47, v47, 2, s76\n"
      "v_lshl_add_u32 v48, v48, 2, s76\n"
      "v_lshl_add_u32 v49, v49, 2, s76\n"
      "v_lshl_add_u32 v50, v50, 2, s76\n"
      "v_lshl_add_u32 v51, v51, 2, s76\n"
      "v_lshl_add_u32 v52, v52, 2, s76\n"
      "v_lshl_add_u32 v53, v53, 2, s76\n"
      "s_waitcnt lgkmcnt(4)\n"
      "v_lshl_add_u32 v54, v54, 2, s76\n"
      "v_lshl_add_u32 v55, v55, 2, s76\n"
      "v_lshl_add_u32 v56, v56, 2, s76\n"
      "v_lshl_add_u32 v57, v57, 2, s76\n"
      "v_lshl_add_u32 v58, v58, 2, s76\n"
      "v_lshl_add_u32 v59, v59, 2, s76\n"
      "v_lshl_add_u32 v60, v60, 2, s76\n"
      "v_lshl_add_u32 v61, v61, 2, s76\n"
      "v_lshl_add_u32 v62, v62, 2, s76\n"
      "v_lshl_add_u32 v63, v63, 2, s76\n"
      "v_lshl_add_u32 v64, v64, 2, s76\n"
      "v_lshl_add_u32 v65, v65, 2, s76\n"
      "v_lshl_add_u32 v66, v66, 2, s76\n"
      "v_lshl_add_u32 v67, v67, 2, s76\n"
      "s_waitcnt lgkmcnt(0)\n"
      "v_lshl_add_u32 v68, v68, 2, s76\n"
      "v_lshl_add_u32 v69, v69, 2, s76\n"
      "v_lshl_add_u32 v70, v70, 2, s76\n"
      "v_lshl_add_u32 v71, v71, 2, s76\n"
      "v_lshl_add_u32 v72, v72, 2, s76\n"
      "v_lshl_add_u32 v73, v73, 2, s76\n"
      "v_lshl_add_u32 v74, v74, 2, s76\n"
      "v_lshl_add_u32 v75, v75, 2, s76\n"
      "v_lshl_add_u32 v235, v235, 2, s76\n"
      "v_lshl_add_u32 v232, v232, 2, s76\n"
      "v_lshl_add_u32 v239, v239, 2, s76\n"
      "v_lshl_add_u32 v237, v237, 2, s76\n"
      "ds_read_b32 v40, v40\n"
      "ds_read_b32 v41, v41\n"
      "ds_read_b32 v42, v42\n"
      "ds_read_b32 v43, v43\n"
      "ds_read_b32 v44, v44\n"
      "ds_read_b32 v45, v45\n"
      "ds_read_b32 v46, v46\n"
      "ds_read_b32 v47, v47\n"
      "ds_read_b32 v48, v48\n"
      "ds_read_b32 v49, v49\n"
      "ds_read_b32 v50, v50\n"
      "ds_read_b32 v51, v51\n"
      "ds_read_b32 v52, v52\n"
      "ds_read_b32 v53, v53\n"
      "ds_read_b32 v54, v54\n"
      "ds_read_b32 v55, v55\n"
      "ds_read_b32 v56, v56\n"
      "ds_read_b32 v57, v57\n"
      "ds_read_b32 v58, v58\n"
      "ds_read_b32 v59, v59\n"
      "ds_read_b32 v60, v60\n"
      "ds_read_b32 v61, v61\n"
      "ds_read_b32 v62, v62\n"
      "ds_read_b32 v63, v63\n"
      "ds_read_b32 v64, v64\n"
      "ds_read_b32 v65, v65\n"
      "ds_read_b32 v66, v66\n"
      "ds_read_b32 v67, v67\n"
      "ds_read_b32 v68, v68\n"
      "ds_read_b32 v69, v69\n"
      "ds_read_b32 v70, v70\n"
      "ds_read_b32 v71, v71\n"
      "ds_read_b32 v72, v72\n"
      "ds_read_b32 v73, v73\n"
      "ds_read_b32 v74, v74\n"
      "ds_read_b32 v75, v75\n"
      "ds_read_b32 v235, v235\n"
      "ds_read_b32 v232, v232\n"
      "ds_read_b32 v239, v239\n"
      "ds_read_b32 v237, v237\n"
      "v_mov_b32 v234, 0xf149f2ca\n"
      "s_waitcnt lgkmcnt(3)\n"
      "v_cndmask_b32 v24, v234, v235, s[72:73]\n"
      "s_waitcnt lgkmcnt(2)\n"
      "v_cndmask_b32 v26, v234, v232, s[72:73]\n"
      "s_waitcnt lgkmcnt(1)\n"
      "v_cndmask_b32 v28, v234, v239, s[72:73]\n"
      "s_waitcnt lgkmcnt(0)\n"
      "v_cndmask_b32 v30, v234, v237, s[66:67]\n"
      "s_mov_b32 s78, 73728\n"
      "s_add_u32 m0, s33, 49152\n"
      "s_nop 0\n"
      "buffer_load_dwordx4 v98, s[28:31], s78 offen  lds\n"
      "s_add_u32 m0, s33, 53120\n"
      "s_nop 0\n"
      "buffer_load_dwordx4 v98, s[28:31], s78 offen offset:128  lds\n"
      "s_add_u32 m0, s33, 57088\n"
      "s_nop 0\n"
      "buffer_load_dwordx4 v98, s[28:31], s78 offen offset:256  lds\n"
      "s_add_u32 m0, s33, 61056\n"
      "s_nop 0\n"
      "buffer_load_dwordx4 v98, s[28:31], s78 offen offset:384  lds\n"
      "s_add_u32 m0, s33, 65024\n"
      "s_nop 0\n"
      "buffer_load_dwordx4 v98, s[28:31], s78 offen offset:512  lds\n"
      "s_add_u32 m0, s33, 68992\n"
      "s_nop 0\n"
      "buffer_load_dwordx4 v98, s[28:31], s78 offen offset:640  lds\n"
      "buffer_load_dwordx4 v[232:235], v27, s[24:27], s47 offen\n"
      "buffer_load_dword v100, v29, s[24:27], s47 offen\n"
      "buffer_load_dwordx4 v[236:239], v27, s[24:27], s74 offen\n"
      "buffer_load_dword v97, v29, s[24:27], s74 offen\n"
      "s_mov_b32 s45, s46\n"
      "s_mov_b32 s42, 1\n"
      "s_mov_b32 s43, 5\n"
      "v_readlane_b32 s47, v38, 4\n"
      "s_nop 0\n"
      "s_and_b32 s47, s47, -4\n"
      "s_waitcnt vmcnt(22)\n"
      "s_waitcnt vmcnt(16)\n"
      "s_waitcnt lgkmcnt(0)\n"
      "s_barrier\n"
      "ds_read_b128 v[244:247], v133\n"
      "ds_read_b128 v[240:243], v0\n"
      "s_add_u32 m0, s33, 73728\n"
      "s_mov_b32 s79, 110592\n"
      "buffer_load_dwordx4 v98, s[28:31], s79 offen  lds\n"
      "s_add_u32 m0, s33, 77696\n"
      "ds_read_b64_tr_b16 a[0:1], v219\n"
      "ds_read_b64_tr_b16 a[2:3], v219 offset:512\n"
      "ds_read_b64_tr_b16 a[24:25], v218\n"
      "ds_read_b64_tr_b16 a[26:27], v218 offset:512\n"
      "ds_read_b64_tr_b16 a[48:49], v126\n"
      "ds_read_b64_tr_b16 a[50:51], v126 offset:512\n"
      "ds_read_b64_tr_b16 a[72:73], v219 offset:12288\n"
      "ds_read_b64_tr_b16 a[74:75], v219 offset:12800\n"
      "ds_read_b64_tr_b16 a[96:97], v218 offset:12288\n"
      "ds_read_b64_tr_b16 a[98:99], v218 offset:12800\n"
      "ds_read_b64_tr_b16 a[120:121], v126 offset:12288\n"
      "ds_read_b64_tr_b16 a[122:123], v126 offset:12800\n"
      "buffer_load_dwordx4 v98, s[28:31], s79 offen offset:128  lds\n"
      "s_waitcnt lgkmcnt(9)\n"
      "v_mfma_f32_16x16x32_bf16 v[224:227], a[0:3], v[244:247], 0\n"
      "s_waitcnt lgkmcnt(8)\n"
      "s_add_u32 m0, s33, 81664\n"
      "v_readlane_b32 s46, v37, s42\n"
      "v_mfma_f32_16x16x32_bf16 v[220:223], a[24:27], v[244:247], 0\n"
      "s_waitcnt lgkmcnt(6)\n"
      "buffer_load_dwordx4 v98, s[28:31], s79 offen offset:256  lds\n"
      "v_mfma_f32_16x16x32_bf16 v[190:193], a[48:51], v[244:247], 0\n"
      "s_waitcnt lgkmcnt(3)\n"
      "s_add_u32 m0, s33, 85632\n"
      "ds_read_b128 v[228:231], v0 offset:2048\n"
      "v_mfma_f32_16x16x32_bf16 v[248:251], a[72:75], v[244:247], 0\n"
      "s_waitcnt lgkmcnt(2)\n"
      "buffer_load_dwordx4 v98, s[28:31], s79 offen offset:384  lds\n"
      "v_mfma_f32_16x16x32_bf16 v[194:197], a[96:99], v[244:247], 0\n"
      "s_waitcnt lgkmcnt(1)\n"
      "s_add_u32 m0, s33, 89600\n"
      "ds_read_b128 v[214:217], v0 offset:4096\n"
      "v_mfma_f32_16x16x32_bf16 v[116:119], a[120:123], v[244:247], 0\n"
      "buffer_load_dwordx4 v98, s[28:31], s79 offen offset:512  lds\n"
      "v_mfma_f32_16x16x32_bf16 v[144:147], a[0:3], v[240:243], 0\n"
      "s_add_u32 m0, s33, 93568\n"
      "v_mfma_f32_16x16x32_bf16 v[164:167], a[24:27], v[240:243], 0\n"
      "buffer_load_dwordx4 v98, s[28:31], s79 offen offset:640  lds\n"
      "v_mfma_f32_16x16x32_bf16 v[128:131], a[48:51], v[240:243], 0\n"
      "s_waitcnt vmcnt(16)\n"
      "s_barrier\n"
      "s_add_u32 m0, s33, 0\n"
      "v_mfma_f32_16x16x32_bf16 v[180:183], a[72:75], v[240:243], 0\n"
      "s_mov_b32 s78, 147456\n"
      "buffer_load_dwordx4 v98, s[28:31], s78 offen  lds\n"
      "v_mfma_f32_16x16x32_bf16 v[202:205], a[96:99], v[240:243], 0\n"
      "s_add_u32 m0, s33, 3968\n"
      "ds_read_b64_tr_b16 a[4:5], v219 offset:24576\n"
      "v_mfma_f32_16x16x32_bf16 v[160:163], a[120:123], v[240:243], 0\n"
      "s_waitcnt lgkmcnt(1)\n"
      "buffer_load_dwordx4 v98, s[28:31], s78 offen offset:128  lds\n"
      "v_mfma_f32_16x16x32_bf16 v[206:209], a[0:3], v[228:231], 0\n"
      "ds_read_b64_tr_b16 a[6:7], v219 offset:25088\n"
      "ds_read_b64_tr_b16 a[28:29], v218 offset:24576\n"
      "v_mfma_f32_16x16x32_bf16 v[210:213], a[24:27], v[228:231], 0\n"
      "ds_read_b64_tr_b16 a[30:31], v218 offset:25088\n"
      "ds_read_b64_tr_b16 a[52:53], v126 offset:24576\n"
      "v_mfma_f32_16x16x32_bf16 v[108:111], a[48:51], v[228:231], 0\n"
      "ds_read_b64_tr_b16 a[54:55], v126 offset:25088\n"
      "ds_read_b64_tr_b16 a[76:77], v219 offset:36864\n"
      "v_mfma_f32_16x16x32_bf16 v[152:155], a[72:75], v[228:231], 0\n"
      "ds_read_b64_tr_b16 a[78:79], v219 offset:37376\n"
      "ds_read_b64_tr_b16 a[100:101], v218 offset:36864\n"
      "v_mfma_f32_16x16x32_bf16 v[252:255], a[96:99], v[228:231], 0\n"
      "ds_read_b64_tr_b16 a[102:103], v218 offset:37376\n"
      "ds_read_b64_tr_b16 a[124:125], v126 offset:36864\n"
      "v_mfma_f32_16x16x32_bf16 v[138:141], a[120:123], v[228:231], 0\n"
      "s_waitcnt lgkmcnt(9)\n"
      "ds_read_b64_tr_b16 a[126:127], v126 offset:37376\n"
      "v_mfma_f32_16x16x32_bf16 v[198:201], a[0:3], v[214:217], 0\n"
      "s_waitcnt lgkmcnt(8)\n"
      "s_add_u32 m0, s33, 7936\n"
      "ds_read_b128 v[242:245], v143\n"
      "v_mfma_f32_16x16x32_bf16 v[184:187], a[24:27], v[214:217], 0\n"
      "s_waitcnt lgkmcnt(7)\n"
      "buffer_load_dwordx4 v98, s[28:31], s78 offen offset:256  lds\n"
      "v_mfma_f32_16x16x32_bf16 v[176:179], a[48:51], v[214:217], 0\n"
      "s_waitcnt lgkmcnt(5)\n"
      "s_add_u32 m0, s33, 11904\n"
      "ds_read_b128 v[228:231], v1\n"
      "v_mfma_f32_16x16x32_bf16 v[134:137], a[72:75], v[214:217], 0\n"
      "s_waitcnt lgkmcnt(4)\n"
      "buffer_load_dwordx4 v98, s[28:31], s78 offen offset:384  lds\n"
      "v_mfma_f32_16x16x32_bf16 v[156:159], a[96:99], v[214:217], 0\n"
      "s_waitcnt lgkmcnt(2)\n"
      "s_add_u32 m0, s33, 15872\n"
      "ds_read_b128 v[170:173], v1 offset:2048\n"
      "v_mfma_f32_16x16x32_bf16 v[112:115], a[120:123], v[214:217], 0\n"
      "s_waitcnt lgkmcnt(2)\n"
      "buffer_load_dwordx4 v98, s[28:31], s78 offen offset:512  lds\n"
      "v_mfma_f32_16x16x32_bf16 v[224:227], a[4:7], v[242:245], v[224:227]\n"
      "s_add_u32 m0, s33, 19840\n"
      "s_waitcnt vmcnt(15)\n"
      "s_barrier\n"
      "v_mfma_f32_16x16x32_bf16 v[220:223], a[28:31], v[242:245], v[220:223]\n"
      "buffer_load_dwordx4 v98, s[28:31], s78 offen offset:640  lds\n"
      "v_mfma_f32_16x16x32_bf16 v[190:193], a[52:55], v[242:245], v[190:193]\n"
      "s_add_u32 m0, s33, 24576\n"
      "s_mov_b32 s79, 184320\n"
      "buffer_load_dwordx4 v98, s[28:31], s79 offen  lds\n"
      "v_mfma_f32_16x16x32_bf16 v[248:251], a[76:79], v[242:245], v[248:251]\n"
      "s_add_u32 m0, s33, 28544\n"
      "ds_read_b64_tr_b16 a[8:9], v124\n"
      "v_mfma_f32_16x16x32_bf16 v[194:197], a[100:103], v[242:245], v[194:197]\n"
      "buffer_load_dwordx4 v98, s[28:31], s79 offen offset:128  lds\n"
      "v_mfma_f32_16x16x32_bf16 v[116:119], a[124:127], v[242:245], v[116:119]\n"
      "s_waitcnt lgkmcnt(1)\n"
      "ds_read_b64_tr_b16 a[10:11], v124 offset:512\n"
      "v_mfma_f32_16x16x32_bf16 v[144:147], a[4:7], v[228:231], v[144:147]\n"
      "ds_read_b64_tr_b16 a[32:33], v189\n"
      "ds_read_b64_tr_b16 a[34:35], v189 offset:512\n"
      "v_mfma_f32_16x16x32_bf16 v[164:167], a[28:31], v[228:231], v[164:167]\n"
      "ds_read_b64_tr_b16 a[56:57], v188\n"
      "ds_read_b64_tr_b16 a[58:59], v188 offset:512\n"
      "v_mfma_f32_16x16x32_bf16 v[128:131], a[52:55], v[228:231], v[128:131]\n"
      "ds_read_b64_tr_b16 a[80:81], v124 offset:12288\n"
      "ds_read_b64_tr_b16 a[82:83], v124 offset:12800\n"
      "v_mfma_f32_16x16x32_bf16 v[180:183], a[76:79], v[228:231], v[180:183]\n"
      "s_waitcnt lgkmcnt(7)\n"
      "ds_read_b64_tr_b16 a[104:105], v189 offset:12288\n"
      "v_mfma_f32_16x16x32_bf16 v[202:205], a[100:103], v[228:231], v[202:205]\n"
      "ds_read_b64_tr_b16 a[106:107], v189 offset:12800\n"
      "ds_read_b64_tr_b16 a[128:129], v188 offset:12288\n"
      "v_mfma_f32_16x16x32_bf16 v[160:163], a[124:127], v[228:231], v[160:163]\n"
      "s_waitcnt lgkmcnt(8)\n"
      "ds_read_b64_tr_b16 a[130:131], v188 offset:12800\n"
      "v_mfma_f32_16x16x32_bf16 v[206:209], a[4:7], v[170:173], v[206:209]\n"
      "s_waitcnt lgkmcnt(8)\n"
      "s_add_u32 m0, s33, 32512\n"
      "ds_read_b128 v[242:245], v1 offset:4096\n"
      "v_mfma_f32_16x16x32_bf16 v[210:213], a[28:31], v[170:173], v[210:213]\n"
      "s_waitcnt lgkmcnt(7)\n"
      "buffer_load_dwordx4 v98, s[28:31], s79 offen offset:256  lds\n"
      "v_mfma_f32_16x16x32_bf16 v[108:111], a[52:55], v[170:173], v[108:111]\n"
      "s_waitcnt lgkmcnt(5)\n"
      "s_add_u32 m0, s33, 36480\n"
      "ds_read_b128 v[228:231], v133 offset:4096\n"
      "v_mfma_f32_16x16x32_bf16 v[152:155], a[76:79], v[170:173], v[152:155]\n"
      "s_waitcnt lgkmcnt(4)\n"
      "buffer_load_dwordx4 v98, s[28:31], s79 offen offset:384  lds\n"
      "v_mfma_f32_16x16x32_bf16 v[252:255], a[100:103], v[170:173], v[252:255]\n"
      "s_waitcnt lgkmcnt(2)\n"
      "s_add_u32 m0, s33, 40448\n"
      "ds_read_b128 v[214:217], v0 offset:6144\n"
      "v_mfma_f32_16x16x32_bf16 v[138:141], a[124:127], v[170:173], v[138:141]\n"
      "s_waitcnt lgkmcnt(2)\n"
      "buffer_load_dwordx4 v98, s[28:31], s79 offen offset:512  lds\n"
      "v_mfma_f32_16x16x32_bf16 v[198:201], a[4:7], v[242:245], v[198:201]\n"
      "s_add_u32 m0, s33, 44416\n"
      "v_mfma_f32_16x16x32_bf16 v[184:187], a[28:31], v[242:245], v[184:187]\n"
      "buffer_load_dwordx4 v98, s[28:31], s79 offen offset:640  lds\n"
      "v_mfma_f32_16x16x32_bf16 v[176:179], a[52:55], v[242:245], v[176:179]\n"
      "s_add_u32 m0, s33, 48384\n"
      "v_mfma_f32_16x16x32_bf16 v[134:137], a[76:79], v[242:245], v[134:137]\n"
      "ds_read_b128 v[170:173], v0 offset:8192\n"
      "v_mfma_f32_16x16x32_bf16 v[156:159], a[100:103], v[242:245], v[156:159]\n"
      "s_waitcnt lgkmcnt(1)\n"
      "v_mfma_f32_16x16x32_bf16 v[112:115], a[124:127], v[242:245], v[112:115]\n"
      "v_mfma_f32_16x16x32_bf16 v[224:227], a[8:11], v[228:231], v[224:227]\n"
      "v_mfma_f32_16x16x32_bf16 v[220:223], a[32:35], v[228:231], v[220:223]\n"
      "v_mfma_f32_16x16x32_bf16 v[190:193], a[56:59], v[228:231], v[190:193]\n"
      "v_mfma_f32_16x16x32_bf16 v[248:251], a[80:83], v[228:231], v[248:251]\n"
      "ds_read_b128 v[242:245], v0 offset:10240\n"
      "v_mfma_f32_16x16x32_bf16 v[194:197], a[104:107], v[228:231], v[194:197]\n"
      "v_mfma_f32_16x16x32_bf16 v[116:119], a[128:131], v[228:231], v[116:119]\n"
      "v_mfma_f32_16x16x32_bf16 v[144:147], a[8:11], v[214:217], v[144:147]\n"
      "v_mfma_f32_16x16x32_bf16 v[164:167], a[32:35], v[214:217], v[164:167]\n"
      "v_mfma_f32_16x16x32_bf16 v[128:131], a[56:59], v[214:217], v[128:131]\n"
      "v_mfma_f32_16x16x32_bf16 v[180:183], a[80:83], v[214:217], v[180:183]\n"
      "ds_read_b128 v[228:231], v143 offset:4096\n"
      "v_mfma_f32_16x16x32_bf16 v[202:205], a[104:107], v[214:217], v[202:205]\n"
      "s_waitcnt lgkmcnt(1)\n"
      "v_mfma_f32_16x16x32_bf16 v[160:163], a[128:131], v[214:217], v[160:163]\n"
      "v_mfma_f32_16x16x32_bf16 v[206:209], a[8:11], v[170:173], v[206:209]\n"
      "v_mfma_f32_16x16x32_bf16 v[210:213], a[32:35], v[170:173], v[210:213]\n"
      "s_waitcnt vmcnt(12)\n"
      "s_barrier\n"
      "s_mov_b32 s78, 0\n"
      "v_mfma_f32_16x16x32_bf16 v[108:111], a[56:59], v[170:173], v[108:111]\n"
      "buffer_load_dwordx4 v98, s[28:31], s78 offen offset:768  lds\n"
      "v_mfma_f32_16x16x32_bf16 v[152:155], a[80:83], v[170:173], v[152:155]\n"
      "s_add_u32 m0, s33, 52352\n"
      "ds_read_b64_tr_b16 a[12:13], v124 offset:24576\n"
      "v_mfma_f32_16x16x32_bf16 v[252:255], a[104:107], v[170:173], v[252:255]\n"
      "buffer_load_dwordx4 v98, s[28:31], s78 offen offset:896  lds\n"
      "v_mfma_f32_16x16x32_bf16 v[138:141], a[128:131], v[170:173], v[138:141]\n"
      "ds_read_b64_tr_b16 a[14:15], v124 offset:25088\n"
      "ds_read_b64_tr_b16 a[36:37], v189 offset:24576\n"
      "v_mfma_f32_16x16x32_bf16 v[198:201], a[8:11], v[242:245], v[198:201]\n"
      "ds_read_b64_tr_b16 a[38:39], v189 offset:25088\n"
      "ds_read_b64_tr_b16 a[60:61], v188 offset:24576\n"
      "v_mfma_f32_16x16x32_bf16 v[184:187], a[32:35], v[242:245], v[184:187]\n"
      "ds_read_b64_tr_b16 a[62:63], v188 offset:25088\n"
      "ds_read_b64_tr_b16 a[84:85], v124 offset:36864\n"
      "v_mfma_f32_16x16x32_bf16 v[176:179], a[56:59], v[242:245], v[176:179]\n"
      "ds_read_b64_tr_b16 a[86:87], v124 offset:37376\n"
      "ds_read_b64_tr_b16 a[108:109], v189 offset:36864\n"
      "v_mfma_f32_16x16x32_bf16 v[134:137], a[80:83], v[242:245], v[134:137]\n"
      "s_waitcnt lgkmcnt(8)\n"
      "ds_read_b64_tr_b16 a[110:111], v189 offset:37376\n"
      "v_mfma_f32_16x16x32_bf16 v[156:159], a[104:107], v[242:245], v[156:159]\n"
      "s_waitcnt lgkmcnt(8)\n"
      "ds_read_b64_tr_b16 a[132:133], v188 offset:36864\n"
      "v_mfma_f32_16x16x32_bf16 v[112:115], a[128:131], v[242:245], v[112:115]\n"
      "s_waitcnt lgkmcnt(8)\n"
      "ds_read_b64_tr_b16 a[134:135], v188 offset:37376\n"
      "v_mfma_f32_16x16x32_bf16 v[224:227], a[12:15], v[228:231], v[224:227]\n"
      "s_waitcnt lgkmcnt(7)\n"
      "s_add_u32 m0, s33, 56320\n"
      "ds_read_b128 v[242:245], v1 offset:6144\n"
      "v_mfma_f32_16x16x32_bf16 v[220:223], a[36:39], v[228:231], v[220:223]\n"
      "s_waitcnt lgkmcnt(6)\n"
      "buffer_load_dwordx4 v98, s[28:31], s78 offen offset:1024  lds\n"
      "v_mfma_f32_16x16x32_bf16 v[190:193], a[60:63], v[228:231], v[190:193]\n"
      "s_waitcnt lgkmcnt(4)\n"
      "s_add_u32 m0, s33, 60672\n"
      "s_mov_b32 s78, 36864\n"
      "v_mfma_f32_16x16x32_bf16 v[248:251], a[84:87], v[228:231], v[248:251]\n"
      "s_waitcnt lgkmcnt(3)\n"
      "buffer_load_dwordx4 v98, s[28:31], s78 offen offset:768  lds\n"
      "v_mfma_f32_16x16x32_bf16 v[194:197], a[108:111], v[228:231], v[194:197]\n"
      "s_waitcnt lgkmcnt(1)\n"
      "s_add_u32 m0, s33, 64640\n"
      "ds_read_b128 v[214:217], v1 offset:8192\n"
      "v_mfma_f32_16x16x32_bf16 v[116:119], a[132:135], v[228:231], v[116:119]\n"
      "s_waitcnt lgkmcnt(1)\n"
      "buffer_load_dwordx4 v98, s[28:31], s78 offen offset:896  lds\n"
      "v_mfma_f32_16x16x32_bf16 v[144:147], a[12:15], v[242:245], v[144:147]\n"
      "s_add_u32 m0, s33, 68608\n"
      "ds_read_b128 v[228:231], v1 offset:10240\n"
      "v_mfma_f32_16x16x32_bf16 v[164:167], a[36:39], v[242:245], v[164:167]\n"
      "buffer_load_dwordx4 v98, s[28:31], s78 offen offset:1024  lds\n"
      "v_mfma_f32_16x16x32_bf16 v[128:131], a[60:63], v[242:245], v[128:131]\n"
      "s_add_u32 m0, s33, 72960\n"
      "v_mfma_f32_16x16x32_bf16 v[180:183], a[84:87], v[242:245], v[180:183]\n"
      "ds_read_b128 v[170:173], v133 offset:8192\n"
      "v_mfma_f32_16x16x32_bf16 v[202:205], a[108:111], v[242:245], v[202:205]\n"
      "s_waitcnt lgkmcnt(2)\n"
      "v_mfma_f32_16x16x32_bf16 v[160:163], a[132:135], v[242:245], v[160:163]\n"
      "v_mfma_f32_16x16x32_bf16 v[206:209], a[12:15], v[214:217], v[206:209]\n"
      "v_mfma_f32_16x16x32_bf16 v[210:213], a[36:39], v[214:217], v[210:213]\n"
      "v_mfma_f32_16x16x32_bf16 v[108:111], a[60:63], v[214:217], v[108:111]\n"
      "v_mfma_f32_16x16x32_bf16 v[152:155], a[84:87], v[214:217], v[152:155]\n"
      "ds_read_b128 v[242:245], v0 offset:12288\n"
      "v_mfma_f32_16x16x32_bf16 v[252:255], a[108:111], v[214:217], v[252:255]\n"
      "s_waitcnt lgkmcnt(1)\n"
      "v_mfma_f32_16x16x32_bf16 v[138:141], a[132:135], v[214:217], v[138:141]\n"
      "v_mfma_f32_16x16x32_bf16 v[198:201], a[12:15], v[228:231], v[198:201]\n"
      "v_mfma_f32_16x16x32_bf16 v[184:187], a[36:39], v[228:231], v[184:187]\n"
      "v_mfma_f32_16x16x32_bf16 v[176:179], a[60:63], v[228:231], v[176:179]\n"
      "v_mfma_f32_16x16x32_bf16 v[134:137], a[84:87], v[228:231], v[134:137]\n"
      "ds_read_b128 v[214:217], v0 offset:14336\n"
      "v_mfma_f32_16x16x32_bf16 v[156:159], a[108:111], v[228:231], v[156:159]\n"
      "v_mfma_f32_16x16x32_bf16 v[112:115], a[132:135], v[228:231], v[112:115]\n"
      "s_waitcnt vmcnt(12)\n"
      "s_barrier\n"
      "ds_read_b64_tr_b16 a[16:17], v219\n"
      "ds_read_b64_tr_b16 a[18:19], v219 offset:512\n"
      "ds_read_b64_tr_b16 a[40:41], v218\n"
      "ds_read_b64_tr_b16 a[42:43], v218 offset:512\n"
      "ds_read_b64_tr_b16 a[64:65], v126\n"
      "ds_read_b64_tr_b16 a[66:67], v126 offset:512\n"
      "ds_read_b64_tr_b16 a[88:89], v219 offset:12288\n"
      "ds_read_b64_tr_b16 a[90:91], v219 offset:12800\n"
      "ds_read_b64_tr_b16 a[112:113], v218 offset:12288\n"
      "ds_read_b64_tr_b16 a[114:115], v218 offset:12800\n"
      "ds_read_b64_tr_b16 a[136:137], v126 offset:12288\n"
      "ds_read_b64_tr_b16 a[138:139], v126 offset:12800\n"
      "s_mov_b32 s79, 73728\n"
      "buffer_load_dwordx4 v98, s[28:31], s79 offen offset:768  lds\n"
      "s_waitcnt lgkmcnt(9)\n"
      "v_mfma_f32_16x16x32_bf16 v[224:227], a[16:19], v[170:173], v[224:227]\n"
      "s_waitcnt lgkmcnt(8)\n"
      "s_add_u32 m0, s33, 76928\n"
      "v_mfma_f32_16x16x32_bf16 v[220:223], a[40:43], v[170:173], v[220:223]\n"
      "s_waitcnt lgkmcnt(6)\n"
      "buffer_load_dwordx4 v98, s[28:31], s79 offen offset:896  lds\n"
      "v_mfma_f32_16x16x32_bf16 v[190:193], a[64:67], v[170:173], v[190:193]\n"
      "s_waitcnt lgkmcnt(3)\n"
      "s_add_u32 m0, s33, 80896\n"
      "v_mfma_f32_16x16x32_bf16 v[248:251], a[88:91], v[170:173], v[248:251]\n"
      "s_waitcnt lgkmcnt(1)\n"
      "buffer_load_dwordx4 v98, s[28:31], s79 offen offset:1024  lds\n"
      "v_mfma_f32_16x16x32_bf16 v[194:197], a[112:115], v[170:173], v[194:197]\n"
      "s_waitcnt lgkmcnt(0)\n"
      "s_add_u32 m0, s33, 85248\n"
      "s_mov_b32 s79, 110592\n"
      "v_mfma_f32_16x16x32_bf16 v[116:119], a[136:139], v[170:173], v[116:119]\n"
      "buffer_load_dwordx4 v98, s[28:31], s79 offen offset:768  lds\n"
      "v_mfma_f32_16x16x32_bf16 v[144:147], a[16:19], v[242:245], v[144:147]\n"
      "s_add_u32 m0, s33, 89216\n"
      "ds_read_b128 v[228:231], v0 offset:16384\n"
      "v_mfma_f32_16x16x32_bf16 v[164:167], a[40:43], v[242:245], v[164:167]\n"
      "buffer_load_dwordx4 v98, s[28:31], s79 offen offset:896  lds\n"
      "v_mfma_f32_16x16x32_bf16 v[128:131], a[64:67], v[242:245], v[128:131]\n"
      "s_add_u32 m0, s33, 93184\n"
      "v_mfma_f32_16x16x32_bf16 v[180:183], a[88:91], v[242:245], v[180:183]\n"
      "buffer_load_dwordx4 v98, s[28:31], s79 offen offset:1024  lds\n"
      "v_mfma_f32_16x16x32_bf16 v[202:205], a[112:115], v[242:245], v[202:205]\n"
      "s_add_u32 m0, s33, -768\n"
      "ds_read_b128 v[170:173], v143 offset:8192\n"
      "v_mfma_f32_16x16x32_bf16 v[160:163], a[136:139], v[242:245], v[160:163]\n"
      "v_mfma_f32_16x16x32_bf16 v[206:209], a[16:19], v[214:217], v[206:209]\n"
      "v_mfma_f32_16x16x32_bf16 v[210:213], a[40:43], v[214:217], v[210:213]\n"
      "v_mfma_f32_16x16x32_bf16 v[108:111], a[64:67], v[214:217], v[108:111]\n"
      "v_mfma_f32_16x16x32_bf16 v[152:155], a[88:91], v[214:217], v[152:155]\n"
      "ds_read_b128 v[242:245], v1 offset:12288\n"
      "v_mfma_f32_16x16x32_bf16 v[252:255], a[112:115], v[214:217], v[252:255]\n"
      "s_waitcnt lgkmcnt(2)\n"
      "v_mfma_f32_16x16x32_bf16 v[138:141], a[136:139], v[214:217], v[138:141]\n"
      "v_mfma_f32_16x16x32_bf16 v[198:201], a[16:19], v[228:231], v[198:201]\n"
      "v_or3_b32 v232, v232, v233, v234\n"
      "v_or3_b32 v236, v236, v237, v238\n"
      "v_mfma_f32_16x16x32_bf16 v[184:187], a[40:43], v[228:231], v[184:187]\n"
      "v_or3_b32 v232, v232, v235, v100\n"
      "v_or3_b32 v236, v236, v239, v97\n"
      "v_mfma_f32_16x16x32_bf16 v[176:179], a[64:67], v[228:231], v[176:179]\n"
      "v_and_b32 v232, 0x7fff7fff, v232\n"
      "v_and_b32 v236, 0x7fff7fff, v236\n"
      "v_mfma_f32_16x16x32_bf16 v[134:137], a[88:91], v[228:231], v[134:137]\n"
      "ds_write_b32 v31, v232 offset:2048\n"
      "v_mfma_f32_16x16x32_bf16 v[156:159], a[112:115], v[228:231], v[156:159]\n"
      "ds_write_b32 v31, v236 offset:1024\n"
      "v_mfma_f32_16x16x32_bf16 v[112:115], a[136:139], v[228:231], v[112:115]\n"
      "s_waitcnt lgkmcnt(2)\n"
      "ds_read_b128 v[232:235], v1 offset:14336\n"
      "s_waitcnt vmcnt(12)\n"
      "s_barrier\n"
      "ds_read_b64_tr_b16 a[20:21], v219 offset:24576\n"
      "ds_read_b64_tr_b16 a[22:23], v219 offset:25088\n"
      "ds_read_b64_tr_b16 a[44:45], v218 offset:24576\n"
      "ds_read_b64_tr_b16 a[46:47], v218 offset:25088\n"
      "ds_read_b64_tr_b16 a[68:69], v126 offset:24576\n"
      "ds_read_b64_tr_b16 a[70:71], v126 offset:25088\n"
      "ds_read_b64_tr_b16 a[92:93], v219 offset:36864\n"
      "ds_read_b64_tr_b16 a[94:95], v219 offset:37376\n"
      "ds_read_b64_tr_b16 a[116:117], v218 offset:36864\n"
      "ds_read_b64_tr_b16 a[118:119], v218 offset:37376\n"
      "ds_read_b64_tr_b16 a[140:141], v126 offset:36864\n"
      "ds_read_b64_tr_b16 a[142:143], v126 offset:37376\n"
      "s_mov_b32 s78, 147456\n"
      "buffer_load_dwordx4 v98, s[28:31], s78 offen offset:768  lds\n"
      "s_waitcnt lgkmcnt(9)\n"
      "v_mfma_f32_16x16x32_bf16 v[224:227], a[20:23], v[170:173], v[224:227]\n"
      "s_waitcnt lgkmcnt(8)\n"
      "s_add_u32 m0, s33, 3200\n"
      "v_mfma_f32_16x16x32_bf16 v[220:223], a[44:47], v[170:173], v[220:223]\n"
      "s_waitcnt lgkmcnt(6)\n"
      "buffer_load_dwordx4 v98, s[28:31], s78 offen offset:896  lds\n"
      "v_mfma_f32_16x16x32_bf16 v[190:193], a[68:71], v[170:173], v[190:193]\n"
      "s_waitcnt lgkmcnt(3)\n"
      "s_add_u32 m0, s33, 7168\n"
      "v_fma_f32 v241, v224, v224, s51\n"
      "v_mfma_f32_16x16x32_bf16 v[248:251], a[92:95], v[170:173], v[248:251]\n"
      "s_waitcnt lgkmcnt(1)\n"
      "buffer_load_dwordx4 v98, s[28:31], s78 offen offset:1024  lds\n"
      "v_mfma_f32_16x16x32_bf16 v[194:197], a[116:119], v[170:173], v[194:197]\n"
      "s_waitcnt lgkmcnt(0)\n"
      "s_add_u32 m0, s33, 11520\n"
      "s_mov_b32 s78, 184320\n"
      "v_mfma_f32_16x16x32_bf16 v[116:119], a[140:143], v[170:173], v[116:119]\n"
      "buffer_load_dwordx4 v98, s[28:31], s78 offen offset:768  lds\n"
      "v_mfma_f32_16x16x32_bf16 v[144:147], a[20:23], v[242:245], v[144:147]\n"
      "s_add_u32 m0, s33, 15488\n"
      "v_fma_f32 v173, v248, v248, s51\n"
      "buffer_load_dwordx4 v98, s[28:31], s78 offen offset:896  lds\n"
      "v_mfma_f32_16x16x32_bf16 v[164:167], a[44:47], v[242:245], v[164:167]\n"
      "s_add_u32 m0, s33, 19456\n"
      "v_fmac_f32_e64 v241, v225, v225\n"
      "buffer_load_dwordx4 v98, s[28:31], s78 offen offset:1024  lds\n"
      "v_mfma_f32_16x16x32_bf16 v[128:131], a[68:71], v[242:245], v[128:131]\n"
      "s_add_u32 m0, s33, 24576\n"
      "v_fmac_f32_e64 v173, v249, v249\n"
      "v_fma_f32 v240, v144, v144, s51\n"
      "v_mfma_f32_16x16x32_bf16 v[180:183], a[92:95], v[242:245], v[180:183]\n"
      "ds_read_b128 v[236:239], v1 offset:16384\n"
      "v_fmac_f32 v241, v226, v226\n"
      "v_mfma_f32_16x16x32_bf16 v[202:205], a[116:119], v[242:245], v[202:205]\n"
      "ds_read_b128 v[228:231], v133 offset:2048\n"
      "v_fmac_f32 v173, v250, v250\n"
      "v_mfma_f32_16x16x32_bf16 v[160:163], a[140:143], v[242:245], v[160:163]\n"
      "ds_read_b128 v[242:245], v143 offset:2048\n"
      "v_fmac_f32_e64 v240, v145, v145\n"
      "v_mfma_f32_16x16x32_bf16 v[206:209], a[20:23], v[232:235], v[206:209]\n"
      "v_fma_f32 v246, v180, v180, s51\n"
      "v_fmac_f32 v246, v181, v181\n"
      "v_mfma_f32_16x16x32_bf16 v[210:213], a[44:47], v[232:235], v[210:213]\n"
      "v_fmac_f32 v241, v227, v227\n"
      "v_fmac_f32_e64 v173, v251, v251\n"
      "v_mfma_f32_16x16x32_bf16 v[108:111], a[68:71], v[232:235], v[108:111]\n"
      "v_fmac_f32 v240, v146, v146\n"
      "v_fmac_f32 v246, v182, v182\n"
      "v_mfma_f32_16x16x32_bf16 v[152:155], a[92:95], v[232:235], v[152:155]\n"
      "v_fma_f32 v214, v206, v206, s51\n"
      "v_fmac_f32 v214, v207, v207\n"
      "v_mfma_f32_16x16x32_bf16 v[252:255], a[116:119], v[232:235], v[252:255]\n"
      "s_waitcnt lgkmcnt(1)\n"
      "v_fmac_f32 v241, v220, v220\n"
      "v_fmac_f32 v173, v194, v194\n"
      "v_mfma_f32_16x16x32_bf16 v[138:141], a[140:143], v[232:235], v[138:141]\n"
      "v_fmac_f32 v240, v147, v147\n"
      "v_fma_f32 v215, v152, v152, s51\n"
      "v_mfma_f32_16x16x32_bf16 v[198:201], a[20:23], v[236:239], v[198:201]\n"
      "v_fmac_f32 v215, v153, v153\n"
      "v_fmac_f32_e64 v246, v183, v183\n"
      "v_mfma_f32_16x16x32_bf16 v[184:187], a[44:47], v[236:239], v[184:187]\n"
      "v_fmac_f32 v214, v208, v208\n"
      "v_fmac_f32 v215, v154, v154\n"
      "v_mfma_f32_16x16x32_bf16 v[176:179], a[68:71], v[236:239], v[176:179]\n"
      "v_fmac_f32 v241, v221, v221\n"
      "v_fmac_f32 v173, v195, v195\n"
      "v_mfma_f32_16x16x32_bf16 v[134:137], a[92:95], v[236:239], v[134:137]\n"
      "ds_read_b128 v[232:235], v133 offset:6144\n"
      "v_fma_f32 v142, v198, v198, s51\n"
      "v_mfma_f32_16x16x32_bf16 v[156:159], a[116:119], v[236:239], v[156:159]\n"
      "s_waitcnt vmcnt(12)\n"
      "s_barrier\n"
      "s_mov_b32 s79, 0\n"
      "v_mfma_f32_16x16x32_bf16 v[112:115], a[140:143], v[236:239], v[112:115]\n"
      "buffer_load_dwordx4 v125, s[36:39], s79 offen  lds\n"
      "v_mfma_f32_16x16x32_bf16 v[96:99], a[0:3], v[228:231], 0\n"
      "s_waitcnt lgkmcnt(1)\n"
      "ds_read_b64_tr_b16 a[144:145], v124\n"
      "v_mfma_f32_16x16x32_bf16 v[96:99], a[4:7], v[242:245], v[96:99]\n"
      "ds_read_b64_tr_b16 a[146:147], v124 offset:512\n"
      "ds_read_b64_tr_b16 a[168:169], v189\n"
      "v_mfma_f32_16x16x32_bf16 v[100:103], a[24:27], v[228:231], 0\n"
      "ds_read_b64_tr_b16 a[170:171], v189 offset:512\n"
      "ds_read_b64_tr_b16 a[192:193], v188\n"
      "v_mfma_f32_16x16x32_bf16 v[100:103], a[28:31], v[242:245], v[100:103]\n"
      "ds_read_b64_tr_b16 a[194:195], v188 offset:512\n"
      "ds_read_b64_tr_b16 a[148:149], v124 offset:12288\n"
      "v_mfma_f32_16x16x32_bf16 v[148:151], a[48:51], v[228:231], 0\n"
      "ds_read_b64_tr_b16 a[150:151], v124 offset:12800\n"
      "ds_read_b64_tr_b16 a[172:173], v189 offset:12288\n"
      "v_mfma_f32_16x16x32_bf16 v[148:151], a[52:55], v[242:245], v[148:151]\n"
      "ds_read_b64_tr_b16 a[174:175], v189 offset:12800\n"
      "ds_read_b64_tr_b16 a[196:197], v188 offset:12288\n"
      "v_mfma_f32_16x16x32_bf16 v[104:107], a[72:75], v[228:231], 0\n"
      "s_waitcnt lgkmcnt(9)\n"
      "ds_read_b64_tr_b16 a[198:199], v188 offset:12800\n"
      "v_mfma_f32_16x16x32_bf16 v[104:107], a[76:79], v[242:245], v[104:107]\n"
      "s_waitcnt lgkmcnt(8)\n"
      "s_add_u32 m0, s33, 28544\n"
      "v_fma_f32 v132, v134, v134, s51\n"
      "v_mfma_f32_16x16x32_bf16 v[120:123], a[96:99], v[228:231], 0\n"
      "s_waitcnt lgkmcnt(7)\n"
      "buffer_load_dwordx4 v125, s[36:39], s79 offen offset:128  lds\n"
      "v_mfma_f32_16x16x32_bf16 v[120:123], a[100:103], v[242:245], v[120:123]\n"
      "s_waitcnt lgkmcnt(4)\n"
      "s_add_u32 m0, s33, 32512\n"
      "v_fmac_f32 v142, v199, v199\n"
      "v_mfma_f32_16x16x32_bf16 v[168:171], a[120:123], v[228:231], 0\n"
      "s_waitcnt lgkmcnt(3)\n"
      "buffer_load_dwordx4 v125, s[36:39], s79 offen offset:256  lds\n"
      "v_mfma_f32_16x16x32_bf16 v[168:171], a[124:127], v[242:245], v[168:171]\n"
      "s_waitcnt lgkmcnt(1)\n"
      "s_add_u32 m0, s33, 36864\n"
      "s_mov_b32 s79, 12288\n"
      "v_mfma_f32_16x16x32_bf16 v[96:99], a[8:11], v[232:235], v[96:99]\n"
      "s_waitcnt lgkmcnt(0)\n"
      "ds_read_b128 v[236:239], v143 offset:6144\n"
      "buffer_load_dwordx4 v125, s[36:39], s79 offen  lds\n"
      "s_add_u32 m0, s33, 40832\n"
      "v_fmac_f32_e64 v132, v135, v135\n"
      "buffer_load_dwordx4 v125, s[36:39], s79 offen offset:128  lds\n"
      "s_add_u32 m0, s33, 44800\n"
      "v_fmac_f32_e64 v142, v200, v200\n"
      "buffer_load_dwordx4 v125, s[36:39], s79 offen offset:256  lds\n"
      "s_add_u32 m0, s33, 49152\n"
      "v_fmac_f32 v132, v136, v136\n"
      "v_fmac_f32 v240, v164, v164\n"
      "v_fmac_f32 v246, v202, v202\n"
      "s_waitcnt lgkmcnt(0)\n"
      "v_mfma_f32_16x16x32_bf16 v[96:99], a[12:15], v[236:239], v[96:99]\n"
      "v_fmac_f32 v214, v209, v209\n"
      "v_fmac_f32 v215, v155, v155\n"
      "v_mfma_f32_16x16x32_bf16 v[100:103], a[32:35], v[232:235], v[100:103]\n"
      "v_fmac_f32 v241, v222, v222\n"
      "v_fmac_f32 v173, v196, v196\n"
      "v_mfma_f32_16x16x32_bf16 v[100:103], a[36:39], v[236:239], v[100:103]\n"
      "v_fmac_f32 v142, v201, v201\n"
      "v_fmac_f32 v132, v137, v137\n"
      "v_mfma_f32_16x16x32_bf16 v[148:151], a[56:59], v[232:235], v[148:151]\n"
      "v_fmac_f32 v240, v165, v165\n"
      "v_fmac_f32 v246, v203, v203\n"
      "v_mfma_f32_16x16x32_bf16 v[148:151], a[60:63], v[236:239], v[148:151]\n"
      "v_fmac_f32 v214, v210, v210\n"
      "v_fmac_f32 v215, v252, v252\n"
      "v_mfma_f32_16x16x32_bf16 v[104:107], a[80:83], v[232:235], v[104:107]\n"
      "v_fmac_f32 v241, v223, v223\n"
      "v_fmac_f32 v173, v197, v197\n"
      "v_mfma_f32_16x16x32_bf16 v[104:107], a[84:87], v[236:239], v[104:107]\n"
      "v_fmac_f32 v142, v184, v184\n"
      "v_fmac_f32 v132, v156, v156\n"
      "v_mfma_f32_16x16x32_bf16 v[120:123], a[104:107], v[232:235], v[120:123]\n"
      "v_fmac_f32 v240, v166, v166\n"
      "v_fmac_f32 v246, v204, v204\n"
      "v_mfma_f32_16x16x32_bf16 v[120:123], a[108:111], v[236:239], v[120:123]\n"
      "v_fmac_f32 v214, v211, v211\n"
      "v_fmac_f32 v215, v253, v253\n"
      "v_mfma_f32_16x16x32_bf16 v[168:171], a[128:131], v[232:235], v[168:171]\n"
      "ds_read_b128 v[232:235], v133 offset:10240\n"
      "v_fmac_f32_e64 v241, v190, v190\n"
      "v_mfma_f32_16x16x32_bf16 v[168:171], a[132:135], v[236:239], v[168:171]\n"
      "ds_read_b128 v[236:239], v143 offset:10240\n"
      "v_fmac_f32 v173, v116, v116\n"
      "v_fmac_f32 v142, v185, v185\n"
      "v_fmac_f32 v132, v157, v157\n"
      "v_fmac_f32 v240, v167, v167\n"
      "v_fmac_f32 v246, v205, v205\n"
      "v_fmac_f32 v214, v212, v212\n"
      "v_fmac_f32 v215, v254, v254\n"
      "v_fmac_f32 v241, v191, v191\n"
      "v_fmac_f32 v173, v117, v117\n"
      "v_fmac_f32 v142, v186, v186\n"
      "v_fmac_f32 v132, v158, v158\n"
      "v_fmac_f32 v240, v128, v128\n"
      "v_fmac_f32 v246, v160, v160\n"
      "v_fmac_f32 v214, v213, v213\n"
      "s_waitcnt vmcnt(12)\n"
      "s_barrier\n"
      "ds_read_b64_tr_b16 a[152:153], v124 offset:24576\n"
      "s_waitcnt lgkmcnt(2)\n"
      "v_mfma_f32_16x16x32_bf16 v[96:99], a[16:19], v[232:235], v[96:99]\n"
      "ds_read_b64_tr_b16 a[154:155], v124 offset:25088\n"
      "ds_read_b64_tr_b16 a[176:177], v189 offset:24576\n"
      "s_waitcnt lgkmcnt(3)\n"
      "v_mfma_f32_16x16x32_bf16 v[96:99], a[20:23], v[236:239], v[96:99]\n"
      "ds_read_b64_tr_b16 a[178:179], v189 offset:25088\n"
      "ds_read_b64_tr_b16 a[200:201], v188 offset:24576\n"
      "v_mfma_f32_16x16x32_bf16 v[100:103], a[40:43], v[232:235], v[100:103]\n"
      "ds_read_b64_tr_b16 a[202:203], v188 offset:25088\n"
      "ds_read_b64_tr_b16 a[156:157], v124 offset:36864\n"
      "v_mfma_f32_16x16x32_bf16 v[100:103], a[44:47], v[236:239], v[100:103]\n"
      "ds_read_b64_tr_b16 a[158:159], v124 offset:37376\n"
      "ds_read_b64_tr_b16 a[180:181], v189 offset:36864\n"
      "v_mfma_f32_16x16x32_bf16 v[148:151], a[64:67], v[232:235], v[148:151]\n"
      "s_waitcnt lgkmcnt(8)\n"
      "ds_read_b64_tr_b16 a[182:183], v189 offset:37376\n"
      "v_mfma_f32_16x16x32_bf16 v[148:151], a[68:71], v[236:239], v[148:151]\n"
      "s_waitcnt lgkmcnt(8)\n"
      "ds_read_b64_tr_b16 a[204:205], v188 offset:36864\n"
      "v_mfma_f32_16x16x32_bf16 v[104:107], a[88:91], v[232:235], v[104:107]\n"
      "s_waitcnt lgkmcnt(8)\n"
      "ds_read_b64_tr_b16 a[206:207], v188 offset:37376\n"
      "v_mfma_f32_16x16x32_bf16 v[104:107], a[92:95], v[236:239], v[104:107]\n"
      "s_waitcnt lgkmcnt(7)\n"
      "s_mov_b32 s78, 24576\n"
      "buffer_load_dwordx4 v125, s[36:39], s78 offen  lds\n"
      "v_mfma_f32_16x16x32_bf16 v[120:123], a[112:115], v[232:235], v[120:123]\n"
      "s_waitcnt lgkmcnt(4)\n"
      "s_add_u32 m0, s33, 53120\n"
      "v_fma_f32 v76, v96, v96, s51\n"
      "v_mfma_f32_16x16x32_bf16 v[120:123], a[116:119], v[236:239], v[120:123]\n"
      "s_waitcnt lgkmcnt(3)\n"
      "buffer_load_dwordx4 v125, s[36:39], s78 offen offset:128  lds\n"
      "v_mfma_f32_16x16x32_bf16 v[168:171], a[136:139], v[232:235], v[168:171]\n"
      "s_waitcnt lgkmcnt(1)\n"
      "s_add_u32 m0, s33, 57088\n"
      "v_fma_f32 v85, v104, v104, s51\n"
      "v_mfma_f32_16x16x32_bf16 v[168:171], a[140:143], v[236:239], v[168:171]\n"
      "s_waitcnt lgkmcnt(0)\n"
      "ds_read_b128 v[242:245], v133\n"
      "ds_read_b128 v[228:231], v133 offset:2048\n"
      "buffer_load_dwordx4 v125, s[36:39], s78 offen offset:256  lds\n"
      "s_add_u32 m0, s33, 61440\n"
      "s_mov_b32 s78, 36864\n"
      "buffer_load_dwordx4 v125, s[36:39], s78 offen  lds\n"
      "s_add_u32 m0, s33, 65408\n"
      "v_fmac_f32 v76, v97, v97\n"
      "buffer_load_dwordx4 v125, s[36:39], s78 offen offset:128  lds\n"
      "s_add_u32 m0, s33, 69376\n"
      "v_fmac_f32 v85, v105, v105\n"
      "buffer_load_dwordx4 v125, s[36:39], s78 offen offset:256  lds\n"
      "s_add_u32 m0, s33, 73728\n"
      "v_fmac_f32 v76, v98, v98\n"
      "v_fmac_f32 v85, v106, v106\n"
      "v_fmac_f32 v76, v99, v99\n"
      "v_fmac_f32 v85, v107, v107\n"
      "v_fmac_f32 v76, v100, v100\n"
      "v_fmac_f32 v85, v120, v120\n"
      "v_fmac_f32 v76, v101, v101\n"
      "v_fmac_f32 v85, v121, v121\n"
      "v_fmac_f32 v76, v102, v102\n"
      "v_fmac_f32 v85, v122, v122\n"
      "v_fmac_f32 v215, v255, v255\n"
      "v_fmac_f32 v241, v192, v192\n"
      "v_fmac_f32 v173, v118, v118\n"
      "v_fmac_f32 v76, v103, v103\n"
      "v_fmac_f32 v85, v123, v123\n"
      "v_fmac_f32 v142, v187, v187\n"
      "v_fmac_f32 v132, v159, v159\n"
      "v_fmac_f32 v240, v129, v129\n"
      "v_fmac_f32 v246, v161, v161\n"
      "v_fmac_f32 v214, v108, v108\n"
      "v_fmac_f32 v215, v138, v138\n"
      "v_fmac_f32 v241, v193, v193\n"
      "v_fmac_f32 v173, v119, v119\n"
      "v_fmac_f32 v76, v148, v148\n"
      "v_fmac_f32 v85, v168, v168\n"
      "v_fmac_f32 v142, v176, v176\n"
      "v_fmac_f32 v132, v112, v112\n"
      "v_fmac_f32 v240, v130, v130\n"
      "v_fmac_f32 v246, v162, v162\n"
      "v_fmac_f32 v214, v109, v109\n"
      "v_fmac_f32 v215, v139, v139\n"
      "v_fmac_f32 v76, v149, v149\n"
      "v_fmac_f32 v85, v169, v169\n"
      "v_fmac_f32 v142, v177, v177\n"
      "v_fmac_f32 v132, v113, v113\n"
      "v_fmac_f32 v240, v131, v131\n"
      "v_fmac_f32 v246, v163, v163\n"
      "v_fmac_f32 v214, v110, v110\n"
      "v_fmac_f32 v215, v140, v140\n"
      "v_fmac_f32 v76, v150, v150\n"
      "v_fmac_f32 v85, v170, v170\n"
      "v_fmac_f32 v142, v178, v178\n"
      "v_fmac_f32 v132, v114, v114\n"
      "v_fmac_f32 v214, v111, v111\n"
      "v_fmac_f32 v215, v141, v141\n"
      "v_fmac_f32 v76, v151, v151\n"
      "v_fmac_f32 v85, v171, v171\n"
      "v_fmac_f32 v142, v179, v179\n"
      "v_fmac_f32 v132, v115, v115\n"
      "v_permlane16_swap_b32 v241, v173\n"
      "v_permlane16_swap_b32 v240, v246\n"
      "v_add_f32 v241, v241, v173\n"
      "v_permlane16_swap_b32_e64 v214, v215\n"
      "s_waitcnt vmcnt(12)\n"
      "s_barrier\n"
      "ds_read_b64_tr_b16 a[160:161], v219\n"
      "ds_read_b64_tr_b16 a[162:163], v219 offset:512\n"
      "ds_read_b64_tr_b16 a[184:185], v218\n"
      "ds_read_b64_tr_b16 a[186:187], v218 offset:512\n"
      "ds_read_b64_tr_b16 a[208:209], v126\n"
      "ds_read_b64_tr_b16 a[210:211], v126 offset:512\n"
      "ds_read_b64_tr_b16 a[164:165], v219 offset:12288\n"
      "ds_read_b64_tr_b16 a[166:167], v219 offset:12800\n"
      "ds_read_b64_tr_b16 a[188:189], v218 offset:12288\n"
      "ds_read_b64_tr_b16 a[190:191], v218 offset:12800\n"
      "ds_read_b64_tr_b16 a[212:213], v126 offset:12288\n"
      "ds_read_b64_tr_b16 a[214:215], v126 offset:12800\n"
      "s_waitcnt lgkmcnt(11)\n"
      "v_mfma_f32_16x16x32_bf16 v[216:219], a[144:147], v[242:245], 0\n"
      "s_waitcnt lgkmcnt(10)\n"
      "s_mov_b32 s79, 49152\n"
      "buffer_load_dwordx4 v125, s[36:39], s79 offen  lds\n"
      "s_add_u32 m0, s33, 77696\n"
      "v_mov_b32_e64 v173, v241\n"
      "buffer_load_dwordx4 v125, s[36:39], s79 offen offset:128  lds\n"
      "s_add_u32 m0, s33, 81664\n"
      "v_permlane16_swap_b32_e64 v76, v85\n"
      "buffer_load_dwordx4 v125, s[36:39], s79 offen offset:256  lds\n"
      "s_add_u32 m0, s33, 86016\n"
      "s_mov_b32 s79, 61440\n"
      "buffer_load_dwordx4 v125, s[36:39], s79 offen  lds\n"
      "s_add_u32 m0, s33, 89984\n"
      "v_permlane16_swap_b32 v142, v132\n"
      "buffer_load_dwordx4 v125, s[36:39], s79 offen offset:128  lds\n"
      "s_add_u32 m0, s33, 93952\n"
      "v_add_f32 v240, v240, v246\n"
      "buffer_load_dwordx4 v125, s[36:39], s79 offen offset:256  lds\n"
      "v_mov_b32 v246, v240\n"
      "v_add_f32 v214, v214, v215\n"
      "v_add_f32 v76, v76, v85\n"
      "v_add_f32 v142, v142, v132\n"
      "v_mov_b32 v215, v214\n"
      "v_mov_b32 v85, v76\n"
      "v_mov_b32 v132, v142\n"
      "v_permlane32_swap_b32 v241, v173\n"
      "v_permlane32_swap_b32 v240, v246\n"
      "v_add_f32 v241, v241, v173\n"
      "v_permlane32_swap_b32 v214, v215\n"
      "v_rsq_f32 v241, v241\n"
      "v_permlane32_swap_b32 v76, v85\n"
      "v_permlane32_swap_b32 v142, v132\n"
      "v_add_f32 v240, v240, v246\n"
      "v_rsq_f32 v240, v240\n"
      "v_add_f32 v214, v214, v215\n"
      "v_add_f32 v76, v76, v85\n"
      "v_add_f32 v142, v142, v132\n"
      "v_rsq_f32 v214, v214\n"
      "v_rsq_f32 v76, v76\n"
      "v_mov_b32 v173, v241\n"
      "v_rsq_f32 v142, v142\n"
      "v_mov_b32 v246, v240\n"
      "v_mov_b32 v215, v214\n"
      "v_mov_b32 v85, v76\n"
      "v_mov_b32 v132, v142\n"
      "v_permlane16_swap_b32 v241, v173\n"
      "v_permlane16_swap_b32 v240, v246\n"
      "v_mul_f32 v241, s50, v241\n"
      "v_permlane16_swap_b32 v214, v215\n"
      "v_mul_f32 v224, v241, v224\n"
      "v_mul_f32 v225, v241, v225\n"
      "v_mul_f32 v248, v173, v248\n"
      "v_mul_f32 v249, v173, v249\n"
      "v_permlane16_swap_b32 v76, v85\n"
      "v_permlane16_swap_b32 v142, v132\n"
      "v_mul_f32 v240, s50, v240\n"
      "v_cvt_pk_bf16_f32 v224, v224, v225\n"
      "v_mul_f32 v226, v241, v226\n"
      "v_mul_f32 v227, v241, v227\n"
      "v_mul_f32 v190, v241, v190\n"
      "v_mul_f32 v191, v241, v191\n"
      "v_mul_f32 v116, v173, v116\n"
      "v_mul_f32 v117, v173, v117\n"
      "v_cvt_pk_bf16_f32 v248, v248, v249\n"
      "v_mul_f32 v250, v173, v250\n"
      "v_mul_f32_e64 v251, v173, v251\n"
      "s_waitcnt lgkmcnt(0)\n"
      "s_waitcnt vmcnt(12)\n"
      "s_barrier\n"
      "ds_read_b64_tr_b16 a[216:217], v175 offset:24576\n"
      "ds_read_b64_tr_b16 a[218:219], v175 offset:26624\n"
      "ds_read_b64_tr_b16 a[240:241], v175 offset:24584\n"
      "ds_read_b64_tr_b16 a[242:243], v175 offset:26632\n"
      "ds_read_b64_tr_b16 a[220:221], v175 offset:36864\n"
      "ds_read_b64_tr_b16 a[222:223], v175 offset:38912\n"
      "ds_read_b64_tr_b16 a[244:245], v175 offset:36872\n"
      "ds_read_b64_tr_b16 a[246:247], v175 offset:38920\n"
      "ds_read_b64_tr_b16 v[232:233], v174 offset:24576\n"
      "s_add_u32 m0, s64, 18432\n"
      "v_mul_f32 v180, v246, v180\n"
      "buffer_load_dwordx4 v16, s[16:19], s45 offen  lds\n"
      "s_add_u32 m0, s64, 22528\n"
      "v_mul_f32 v181, v246, v181\n"
      "buffer_load_dwordx4 v17, s[16:19], s45 offen  lds\n"
      "s_add_u32 m0, s64, 26624\n"
      "v_mul_f32_e64 v144, v240, v144\n"
      "buffer_load_dwordx4 v18, s[16:19], s45 offen  lds\n"
      "s_add_u32 m0, s64, 30720\n"
      "v_mul_f32 v145, v240, v145\n"
      "buffer_load_dwordx4 v19, s[16:19], s45 offen  lds\n"
      "s_add_u32 m0, s65, 34816\n"
      "v_mul_f32 v214, s50, v214\n"
      "buffer_load_dwordx4 v15, s[16:19], s45 offen  lds\n"
      "v_cvt_pk_bf16_f32 v225, v226, v227\n"
      "v_mul_f32 v220, v241, v220\n"
      "v_mul_f32 v221, v241, v221\n"
      "v_mul_f32 v222, v241, v222\n"
      "v_mul_f32 v223, v241, v223\n"
      "v_cvt_pk_bf16_f32 v190, v190, v191\n"
      "v_mul_f32 v192, v241, v192\n"
      "v_mul_f32 v193, v241, v193\n"
      "v_cvt_pk_bf16_f32 v116, v116, v117\n"
      "v_mul_f32 v118, v173, v118\n"
      "v_mul_f32 v119, v173, v119\n"
      "v_cvt_pk_bf16_f32 v249, v250, v251\n"
      "v_mul_f32 v194, v173, v194\n"
      "v_mul_f32 v195, v173, v195\n"
      "v_mul_f32 v196, v173, v196\n"
      "v_mul_f32 v197, v173, v197\n"
      "ds_read_b64_tr_b16 v[234:235], v174 offset:26624\n"
      "v_mul_f32 v76, s50, v76\n"
      "v_mul_f32 v142, s50, v142\n"
      "v_cvt_pk_bf16_f32 v180, v180, v181\n"
      "v_mul_f32 v182, v246, v182\n"
      "v_mul_f32 v183, v246, v183\n"
      "v_mul_f32 v160, v246, v160\n"
      "v_mul_f32 v161, v246, v161\n"
      "v_mul_f32 v152, v215, v152\n"
      "v_mul_f32 v153, v215, v153\n"
      "v_cvt_pk_bf16_f32 v144, v144, v145\n"
      "v_mul_f32 v146, v240, v146\n"
      "v_mul_f32 v147, v240, v147\n"
      "v_mul_f32 v128, v240, v128\n"
      "v_mul_f32 v129, v240, v129\n"
      "v_mul_f32 v206, v214, v206\n"
      "v_mul_f32_e64 v207, v214, v207\n"
      "v_cvt_pk_bf16_f32 v226, v220, v221\n"
      "ds_read_b64_tr_b16 v[236:237], v174 offset:36864\n"
      "v_cvt_pk_bf16_f32 v227, v222, v223\n"
      "ds_read_b64_tr_b16 v[238:239], v174 offset:38912\n"
      "ds_write_b128 v80, v[224:227]\n"
      "v_mfma_f32_16x16x32_bf16 v[124:127], a[168:171], v[242:245], 0\n"
      "s_waitcnt lgkmcnt(3)\n"
      "v_cvt_pk_bf16_f32 v191, v192, v193\n"
      "v_cvt_pk_bf16_f32 v117, v118, v119\n"
      "v_mfma_f32_16x16x32_bf16 v[220:223], a[192:195], v[242:245], 0\n"
      "ds_write_b64 v80, v[190:191] offset:16\n"
      "v_mfma_f32_16x16x32_bf16 v[172:175], a[144:147], v[228:231], 0\n"
      "s_waitcnt lgkmcnt(3)\n"
      "ds_write_b64 v80, v[116:117] offset:24\n"
      "v_mfma_f32_16x16x32_bf16 v[116:119], a[168:171], v[228:231], 0\n"
      "s_waitcnt lgkmcnt(3)\n"
      "v_cvt_pk_bf16_f32 v250, v194, v195\n"
      "v_cvt_pk_bf16_f32 v251, v196, v197\n"
      "v_mfma_f32_16x16x32_bf16 v[188:191], a[192:195], v[228:231], 0\n"
      "ds_write_b128 v80, v[248:251] offset:32\n"
      "ds_read_b128 v[242:245], v143\n"
      "ds_read_b128 v[248:251], v143 offset:2048\n"
      "v_mul_f32 v104, v85, v104\n"
      "v_mul_f32 v105, v85, v105\n"
      "v_mul_f32 v96, v76, v96\n"
      "v_mul_f32 v97, v76, v97\n"
      "v_mul_f32 v134, v132, v134\n"
      "v_mul_f32 v135, v132, v135\n"
      "v_mul_f32 v198, v142, v198\n"
      "v_mul_f32 v199, v142, v199\n"
      "v_cvt_pk_bf16_f32 v181, v182, v183\n"
      "v_mul_f32 v202, v246, v202\n"
      "v_mul_f32 v203, v246, v203\n"
      "v_mul_f32 v204, v246, v204\n"
      "v_mul_f32 v205, v246, v205\n"
      "v_cvt_pk_bf16_f32 v160, v160, v161\n"
      "v_mul_f32 v162, v246, v162\n"
      "v_mul_f32 v163, v246, v163\n"
      "v_cvt_pk_bf16_f32 v152, v152, v153\n"
      "v_mul_f32 v154, v215, v154\n"
      "v_mul_f32 v155, v215, v155\n"
      "v_cvt_pk_bf16_f32 v145, v146, v147\n"
      "v_mul_f32 v164, v240, v164\n"
      "v_mul_f32 v165, v240, v165\n"
      "v_mul_f32 v138, v215, v138\n"
      "v_mul_f32 v139, v215, v139\n"
      "s_waitcnt lgkmcnt(1)\n"
      "v_mfma_f32_16x16x32_bf16 v[216:219], a[148:151], v[242:245], v[216:219]\n"
      "v_mul_f32 v166, v240, v166\n"
      "v_mul_f32 v167, v240, v167\n"
      "v_mfma_f32_16x16x32_bf16 v[124:127], a[172:175], v[242:245], v[124:127]\n"
      "s_waitcnt lgkmcnt(0)\n"
      "v_cvt_pk_bf16_f32 v128, v128, v129\n"
      "v_mul_f32 v130, v240, v130\n"
      "v_mfma_f32_16x16x32_bf16 v[220:223], a[196:199], v[242:245], v[220:223]\n"
      "buffer_load_dwordx4 v[194:197], v27, s[24:27], s47 offen\n"
      "v_mfma_f32_16x16x32_bf16 v[172:175], a[148:151], v[248:251], v[172:175]\n"
      "buffer_load_dword v192, v29, s[24:27], s47 offen\n"
      "v_mfma_f32_16x16x32_bf16 v[116:119], a[172:175], v[248:251], v[116:119]\n"
      "v_mul_f32 v131, v240, v131\n"
      "v_cvt_pk_bf16_f32 v206, v206, v207\n"
      "v_mfma_f32_16x16x32_bf16 v[188:191], a[196:199], v[248:251], v[188:191]\n"
      "ds_read_b128 v[242:245], v133 offset:4096\n"
      "v_mul_f32 v208, v214, v208\n"
      "v_mul_f32 v209, v214, v209\n"
      "v_mul_f32 v108, v214, v108\n"
      "v_mul_f32 v109, v214, v109\n"
      "v_cvt_pk_bf16_f32 v104, v104, v105\n"
      "v_mul_f32 v106, v85, v106\n"
      "v_mul_f32 v107, v85, v107\n"
      "v_mul_f32 v168, v85, v168\n"
      "v_mul_f32 v169, v85, v169\n"
      "v_cvt_pk_bf16_f32 v96, v96, v97\n"
      "v_mul_f32 v98, v76, v98\n"
      "v_mul_f32 v99, v76, v99\n"
      "v_mul_f32 v148, v76, v148\n"
      "v_mul_f32 v149, v76, v149\n"
      "v_cvt_pk_bf16_f32 v134, v134, v135\n"
      "v_mul_f32 v136, v132, v136\n"
      "v_mul_f32 v137, v132, v137\n"
      "v_mul_f32 v112, v132, v112\n"
      "s_waitcnt vmcnt(13)\n"
      "s_barrier\n"
      "ds_read_b64_tr_b16 a[224:225], v77\n"
      "s_waitcnt lgkmcnt(1)\n"
      "v_mfma_f32_16x16x32_bf16 v[216:219], a[152:155], v[242:245], v[216:219]\n"
      "ds_read_b64_tr_b16 a[226:227], v77 offset:2048\n"
      "ds_read_b64_tr_b16 a[248:249], v77 offset:8\n"
      "v_mfma_f32_16x16x32_bf16 v[124:127], a[176:179], v[242:245], v[124:127]\n"
      "ds_read_b64_tr_b16 a[250:251], v77 offset:2056\n"
      "ds_read_b64_tr_b16 v[240:241], v81\n"
      "v_mfma_f32_16x16x32_bf16 v[220:223], a[200:203], v[242:245], v[220:223]\n"
      "ds_read_b64_tr_b16 v[242:243], v81 offset:2048\n"
      "ds_read_b64_tr_b16 a[228:229], v77 offset:12288\n"
      "ds_read_b64_tr_b16 a[230:231], v77 offset:14336\n"
      "ds_read_b64_tr_b16 a[252:253], v77 offset:12296\n"
      "ds_read_b64_tr_b16 a[254:255], v77 offset:14344\n"
      "ds_read_b64_tr_b16 v[244:245], v81 offset:12288\n"
      "v_mul_f32 v113, v132, v113\n"
      "v_cvt_pk_bf16_f32 v198, v198, v199\n"
      "v_mul_f32 v200, v142, v200\n"
      "v_mul_f32_e64 v201, v142, v201\n"
      "v_cvt_pk_bf16_f32 v182, v202, v203\n"
      "ds_read_b64_tr_b16 v[246:247], v81 offset:14336\n"
      "v_cvt_pk_bf16_f32 v183, v204, v205\n"
      "v_mul_f32 v176, v142, v176\n"
      "v_mul_f32 v177, v142, v177\n"
      "v_cvt_pk_bf16_f32 v161, v162, v163\n"
      "ds_read_b128 v[248:251], v133 offset:6144\n"
      "v_cvt_pk_bf16_f32 v153, v154, v155\n"
      "v_mul_f32 v252, v215, v252\n"
      "v_mul_f32 v253, v215, v253\n"
      "v_mul_f32 v254, v215, v254\n"
      "v_mul_f32 v255, v215, v255\n"
      "v_cvt_pk_bf16_f32 v146, v164, v165\n"
      "v_cvt_pk_bf16_f32 v138, v138, v139\n"
      "v_mul_f32 v140, v215, v140\n"
      "v_mul_f32 v141, v215, v141\n"
      "v_cvt_pk_bf16_f32 v147, v166, v167\n"
      "ds_read_b128 v[224:227], v143 offset:4096\n"
      "v_cvt_pk_bf16_f32 v129, v130, v131\n"
      "v_cvt_pk_bf16_f32 v207, v208, v209\n"
      "v_mul_f32 v210, v214, v210\n"
      "v_mul_f32 v211, v214, v211\n"
      "v_mul_f32 v212, v214, v212\n"
      "v_mul_f32 v213, v214, v213\n"
      "v_cvt_pk_bf16_f32 v108, v108, v109\n"
      "v_mul_f32 v110, v214, v110\n"
      "v_mul_f32_e64 v111, v214, v111\n"
      "s_waitcnt lgkmcnt(1)\n"
      "v_mfma_f32_16x16x32_bf16 v[172:175], a[152:155], v[248:251], v[172:175]\n"
      "ds_read_b128 v[228:231], v143 offset:6144\n"
      "v_cvt_pk_bf16_f32 v105, v106, v107\n"
      "v_mfma_f32_16x16x32_bf16 v[116:119], a[176:179], v[248:251], v[116:119]\n"
      "v_mul_f32 v120, v85, v120\n"
      "v_mul_f32 v121, v85, v121\n"
      "v_mfma_f32_16x16x32_bf16 v[188:191], a[200:203], v[248:251], v[188:191]\n"
      "ds_read_b128 v[164:167], v133 offset:8192\n"
      "v_mul_f32 v122, v85, v122\n"
      "s_waitcnt lgkmcnt(2)\n"
      "v_mfma_f32_16x16x32_bf16 v[216:219], a[156:159], v[224:227], v[216:219]\n"
      "v_mul_f32 v123, v85, v123\n"
      "v_cvt_pk_bf16_f32 v168, v168, v169\n"
      "v_mfma_f32_16x16x32_bf16 v[124:127], a[180:183], v[224:227], v[124:127]\n"
      "v_mul_f32 v170, v85, v170\n"
      "v_mul_f32 v171, v85, v171\n"
      "v_mfma_f32_16x16x32_bf16 v[220:223], a[204:207], v[224:227], v[220:223]\n"
      "ds_read_b128 v[202:205], v133 offset:10240\n"
      "v_cvt_pk_bf16_f32 v97, v98, v99\n"
      "s_waitcnt lgkmcnt(2)\n"
      "v_mfma_f32_16x16x32_bf16 v[172:175], a[156:159], v[228:231], v[172:175]\n"
      "v_mul_f32 v100, v76, v100\n"
      "v_mul_f32 v101, v76, v101\n"
      "v_mfma_f32_16x16x32_bf16 v[116:119], a[180:183], v[228:231], v[116:119]\n"
      "v_mul_f32 v102, v76, v102\n"
      "v_mul_f32 v103, v76, v103\n"
      "v_mfma_f32_16x16x32_bf16 v[188:191], a[204:207], v[228:231], v[188:191]\n"
      "s_waitcnt lgkmcnt(1)\n"
      "v_cvt_pk_bf16_f32 v148, v148, v149\n"
      "v_mul_f32 v150, v76, v150\n"
      "v_mfma_f32_16x16x32_bf16 v[216:219], a[160:163], v[164:167], v[216:219]\n"
      "ds_read_b128 v[224:227], v143 offset:8192\n"
      "v_mul_f32_e64 v151, v76, v151\n"
      "v_mfma_f32_16x16x32_bf16 v[124:127], a[184:187], v[164:167], v[124:127]\n"
      "ds_read_b128 v[248:251], v143 offset:10240\n"
      "v_cvt_pk_bf16_f32 v135, v136, v137\n"
      "v_mfma_f32_16x16x32_bf16 v[220:223], a[208:211], v[164:167], v[220:223]\n"
      "s_waitcnt lgkmcnt(2)\n"
      "v_mul_f32 v156, v132, v156\n"
      "v_mul_f32_e64 v157, v132, v157\n"
      "v_mfma_f32_16x16x32_bf16 v[172:175], a[160:163], v[202:205], v[172:175]\n"
      "ds_read_b128 v[162:165], v0 offset:4096\n"
      "v_mul_f32 v158, v132, v158\n"
      "v_mfma_f32_16x16x32_bf16 v[116:119], a[184:187], v[202:205], v[116:119]\n"
      "v_mul_f32 v159, v132, v159\n"
      "v_cvt_pk_bf16_f32 v112, v112, v113\n"
      "v_mfma_f32_16x16x32_bf16 v[188:191], a[208:211], v[202:205], v[188:191]\n"
      "ds_read_b128 v[228:231], v1 offset:4096\n"
      "v_mul_f32 v114, v132, v114\n"
      "v_mul_f32 v115, v132, v115\n"
      "s_waitcnt lgkmcnt(3)\n"
      "v_mfma_f32_16x16x32_bf16 v[216:219], a[164:167], v[224:227], v[216:219]\n"
      "v_cvt_pk_bf16_f32 v199, v200, v201\n"
      "v_mul_f32 v184, v142, v184\n"
      "v_mfma_f32_16x16x32_bf16 v[124:127], a[188:191], v[224:227], v[124:127]\n"
      "s_waitcnt lgkmcnt(2)\n"
      "v_mul_f32 v185, v142, v185\n"
      "v_mul_f32_e64 v186, v142, v186\n"
      "v_mfma_f32_16x16x32_bf16 v[220:223], a[212:215], v[224:227], v[220:223]\n"
      "ds_read_b128 v[130:133], v0 offset:10240\n"
      "v_mul_f32_e64 v187, v142, v187\n"
      "v_mfma_f32_16x16x32_bf16 v[172:175], a[164:167], v[248:251], v[172:175]\n"
      "s_waitcnt vmcnt(7)\n"
      "s_barrier\n"
      "v_cvt_pk_bf16_f32 v176, v176, v177\n"
      "v_mfma_f32_16x16x32_bf16 v[116:119], a[188:191], v[248:251], v[116:119]\n"
      "ds_read_b64_tr_b16 a[232:233], v77 offset:24576\n"
      "v_mul_f32_e64 v178, v142, v178\n"
      "v_mfma_f32_16x16x32_bf16 v[188:191], a[212:215], v[248:251], v[188:191]\n"
      "s_waitcnt lgkmcnt(2)\n"
      "v_mul_f32_e64 v179, v142, v179\n"
      "v_cvt_pk_bf16_f32 v154, v252, v253\n"
      "v_mfma_f32_16x16x32_bf16 v[202:205], v[162:165], a[144:147], 0\n"
      "ds_read_b64_tr_b16 a[234:235], v77 offset:26624\n"
      "v_cvt_pk_bf16_f32 v155, v254, v255\n"
      "v_mfma_f32_16x16x32_bf16 v[202:205], v[228:231], a[148:151], v[202:205]\n"
      "ds_read_b64_tr_b16 v[224:225], v77 offset:24584\n"
      "v_cvt_pk_bf16_f32 v139, v140, v141\n"
      "v_mfma_f32_16x16x32_bf16 v[140:143], v[162:165], a[168:171], 0\n"
      "ds_read_b64_tr_b16 v[226:227], v77 offset:26632\n"
      "v_cvt_pk_bf16_f32 v208, v210, v211\n"
      "v_mfma_f32_16x16x32_bf16 v[140:143], v[228:231], a[172:175], v[140:143]\n"
      "ds_read_b64_tr_b16 v[248:249], v81 offset:24576\n"
      "v_cvt_pk_bf16_f32 v209, v212, v213\n"
      "v_mfma_f32_16x16x32_bf16 v[210:213], v[162:165], a[192:195], 0\n"
      "ds_read_b64_tr_b16 v[250:251], v81 offset:26624\n"
      "v_cvt_pk_bf16_f32 v109, v110, v111\n"
      "v_mfma_f32_16x16x32_bf16 v[210:213], v[228:231], a[196:199], v[210:213]\n"
      "s_waitcnt lgkmcnt(5)\n"
      "v_cvt_pk_bf16_f32 v106, v120, v121\n"
      "v_cvt_pk_bf16_f32 v107, v122, v123\n"
      "v_mfma_f32_16x16x32_bf16 v[202:205], v[130:133], a[152:155], v[202:205]\n"
      "ds_read_b64_tr_b16 a[236:237], v77 offset:36864\n"
      "ds_read_b64_tr_b16 a[238:239], v77 offset:38912\n"
      "ds_read_b64_tr_b16 v[228:229], v77 offset:36872\n"
      "ds_read_b64_tr_b16 v[230:231], v77 offset:38920\n"
      "ds_read_b64_tr_b16 v[252:253], v81 offset:36864\n"
      "ds_read_b64_tr_b16 v[254:255], v81 offset:38912\n"
      "ds_read_b128 v[120:123], v1 offset:10240\n"
      "s_waitcnt lgkmcnt(0)\n"
      "s_barrier\n"
      "v_cvt_pk_bf16_f32 v169, v170, v171\n"
      "v_cvt_pk_bf16_f32 v98, v100, v101\n"
      "ds_read_b128 v[162:165], v9 offset:5632\n"
      "v_cvt_pk_bf16_f32 v99, v102, v103\n"
      "v_cvt_pk_bf16_f32 v149, v150, v151\n"
      "ds_read_b128 v[100:103], v0 offset:16384\n"
      "s_mov_b32 exec_lo, 0x1f001f\n"
      "s_mov_b32 exec_hi, 0x1f001f\n"
      "ds_write_b128 v80, v[96:99] offset:18688\n"
      "ds_write_b64 v80, v[148:149] offset:18704\n"
      "ds_write_b64 v80, v[168:169] offset:18712\n"
      "ds_write_b128 v80, v[104:107] offset:18720\n"
      "s_mov_b64 exec, -1\n"
      "s_waitcnt lgkmcnt(4)\n"
      "v_mfma_f32_16x16x32_bf16 v[202:205], v[120:123], a[156:159], v[202:205]\n"
      "ds_read_b128 v[104:107], v1 offset:16384\n"
      "v_cvt_pk_bf16_f32 v136, v156, v157\n"
      "v_mfma_f32_16x16x32_bf16 v[140:143], v[130:133], a[176:179], v[140:143]\n"
      "ds_read_b128 v[96:99], v0 offset:2048\n"
      "v_cvt_pk_bf16_f32 v137, v158, v159\n"
      "v_mfma_f32_16x16x32_bf16 v[140:143], v[120:123], a[180:183], v[140:143]\n"
      "ds_read_b128 v[166:169], v1 offset:2048\n"
      "v_cvt_pk_bf16_f32 v113, v114, v115\n"
      "v_mfma_f32_16x16x32_bf16 v[210:213], v[130:133], a[200:203], v[210:213]\n"
      "ds_read_b128 v[130:133], v0 offset:8192\n"
      "v_cvt_pk_bf16_f32 v200, v184, v185\n"
      "v_mfma_f32_16x16x32_bf16 v[210:213], v[120:123], a[204:207], v[210:213]\n"
      "ds_read_b128 v[156:159], v1 offset:8192\n"
      "v_cvt_pk_bf16_f32 v201, v186, v187\n"
      "v_mfma_f32_16x16x32_bf16 v[202:205], v[100:103], a[160:163], v[202:205]\n"
      "v_cvt_pk_bf16_f32 v177, v178, v179\n"
      "v_cvt_pk_bf16_f32 v216, v216, v217\n"
      "v_cvt_pk_bf16_f32 v124, v124, v125\n"
      "v_cvt_pk_bf16_f32 v220, v220, v221\n"
      "s_waitcnt lgkmcnt(4)\n"
      "v_mfma_f32_16x16x32_bf16 v[202:205], v[104:107], a[164:167], v[202:205]\n"
      "v_cvt_pk_bf16_f32 v217, v218, v219\n"
      "v_cvt_pk_bf16_f32 v125, v126, v127\n"
      "v_mfma_f32_16x16x32_bf16 v[140:143], v[100:103], a[184:187], v[140:143]\n"
      "ds_write_b64 v78, v[216:217]\n"
      "v_mfma_f32_16x16x32_bf16 v[140:143], v[104:107], a[188:191], v[140:143]\n"
      "ds_read_b128 v[184:187], v0 offset:14336\n"
      "v_cvt_pk_bf16_f32 v221, v222, v223\n"
      "v_mfma_f32_16x16x32_bf16 v[210:213], v[100:103], a[208:211], v[210:213]\n"
      "ds_read_b128 v[148:151], v1 offset:14336\n"
      "ds_write_b64 v78, v[124:125] offset:32\n"
      "v_mfma_f32_16x16x32_bf16 v[210:213], v[104:107], a[212:215], v[210:213]\n"
      "s_waitcnt lgkmcnt(4)\n"
      "ds_write_b64 v78, v[220:221] offset:64\n"
      "v_mfma_f32_16x16x32_bf16 v[104:107], v[96:99], a[144:147], 0\n"
      "v_cvt_pk_bf16_f32 v76, v202, v203\n"
      "v_cvt_pk_bf16_f32 v77, v204, v205\n"
      "v_mfma_f32_16x16x32_bf16 v[104:107], v[166:169], a[148:151], v[104:107]\n"
      "v_cvt_pk_bf16_f32 v80, v140, v141\n"
      "v_cvt_pk_bf16_f32 v81, v142, v143\n"
      "v_mfma_f32_16x16x32_bf16 v[100:103], v[96:99], a[168:171], 0\n"
      "v_cvt_pk_bf16_f32 v84, v210, v211\n"
      "v_cvt_pk_bf16_f32 v85, v212, v213\n"
      "v_mfma_f32_16x16x32_bf16 v[100:103], v[166:169], a[172:175], v[100:103]\n"
      "v_cvt_pk_bf16_f32 v172, v172, v173\n"
      "v_cvt_pk_bf16_f32 v116, v116, v117\n"
      "v_mfma_f32_16x16x32_bf16 v[214:217], v[96:99], a[192:195], 0\n"
      "v_cvt_pk_bf16_f32 v188, v188, v189\n"
      "v_cvt_pk_bf16_f32 v173, v174, v175\n"
      "v_mfma_f32_16x16x32_bf16 v[214:217], v[166:169], a[196:199], v[214:217]\n"
      "v_cvt_pk_bf16_f32 v117, v118, v119\n"
      "v_cvt_pk_bf16_f32 v189, v190, v191\n"
      "v_mfma_f32_16x16x32_bf16 v[104:107], v[130:133], a[152:155], v[104:107]\n"
      "ds_read_b128 v[210:213], v10 offset:32\n"
      "s_mov_b32 exec_lo, 0x1f001f\n"
      "s_mov_b32 exec_hi, 0x1f001f\n"
      "ds_write_b64 v78, v[172:173] offset:18688\n"
      "ds_write_b64 v78, v[116:117] offset:18720\n"
      "ds_write_b64 v78, v[188:189] offset:18752\n"
      "s_mov_b64 exec, -1\n"
      "v_mfma_f32_16x16x32_bf16 v[104:107], v[156:159], a[156:159], v[104:107]\n"
      "s_waitcnt vmcnt(1)\n"
      "v_or3_b32 v194, v194, v195, v196\n"
      "s_waitcnt vmcnt(0)\n"
      "v_or3_b32 v194, v194, v197, v192\n"
      "v_mfma_f32_16x16x32_bf16 v[100:103], v[130:133], a[176:179], v[100:103]\n"
      "v_and_b32 v194, 0x7fff7fff, v194\n"
      "ds_write_b32 v31, v194\n"
      "v_mfma_f32_16x16x32_bf16 v[100:103], v[156:159], a[180:183], v[100:103]\n"
      "v_or3_b32 v162, v162, v163, v164\n"
      "v_readlane_b32 s45, v37, s43\n"
      "v_mfma_f32_16x16x32_bf16 v[214:217], v[130:133], a[200:203], v[214:217]\n"
      "ds_read_b64 v[222:223], v10 offset:24\n"
      "v_or_b32 v162, v162, v165\n"
      "v_mfma_f32_16x16x32_bf16 v[214:217], v[156:159], a[204:207], v[214:217]\n"
      "s_waitcnt lgkmcnt(3)\n"
      "ds_read_b128 v[188:191], v0\n"
      "v_mfma_f32_16x16x32_bf16 v[104:107], v[184:187], a[160:163], v[104:107]\n"
      "ds_read_b128 v[166:169], v1\n"
      "s_or_b32 s95, s45, s46\n"
      "v_mfma_f32_16x16x32_bf16 v[104:107], v[148:151], a[164:167], v[104:107]\n"
      "v_readlane_b32 s47, v38, s43\n"
      "s_add_u32 s43, s43, 1\n"
      "v_mfma_f32_16x16x32_bf16 v[100:103], v[184:187], a[184:187], v[100:103]\n"
      "s_and_b32 s47, s47, -4\n"
      "v_mfma_f32_16x16x32_bf16 v[100:103], v[148:151], a[188:191], v[100:103]\n"
      "v_mfma_f32_16x16x32_bf16 v[214:217], v[184:187], a[208:211], v[214:217]\n"
      "v_mfma_f32_16x16x32_bf16 v[214:217], v[148:151], a[212:215], v[214:217]\n"
      "ds_read_b128 v[130:133], v0 offset:6144\n"
      "v_cvt_pk_bf16_f32 v107, v106, v107\n"
      "v_mfma_f32_16x16x32_bf16 v[140:143], v[180:183], v[144:147], v[40:43]\n"
      "ds_read_b128 v[202:205], v1 offset:6144\n"
      "v_cvt_pk_bf16_f32 v106, v104, v105\n"
      "v_mfma_f32_16x16x32_bf16 v[218:221], v[152:155], v[144:147], v[44:47]\n"
      "v_cvt_pk_bf16_f32 v103, v102, v103\n"
      "v_cvt_pk_bf16_f32 v102, v100, v101\n"
      "v_mfma_f32_16x16x32_bf16 v[124:127], v[134:137], v[144:147], v[48:51]\n"
      "v_cvt_pk_bf16_f32 v217, v216, v217\n"
      "v_cvt_pk_bf16_f32 v216, v214, v215\n"
      "v_mfma_f32_16x16x32_bf16 v[194:197], v[210:213], v[144:147], v[24:27]\n"
      "v_mfma_f32_16x16x16_bf16 v[140:143], v[160:161], v[128:129], v[140:143]\n"
      "v_mfma_f32_16x16x16_bf16 v[218:221], v[138:139], v[128:129], v[218:221]\n"
      "ds_read_b128 v[144:147], v0 offset:12288\n"
      "v_mfma_f32_16x16x16_bf16 v[124:127], v[112:113], v[128:129], v[124:127]\n"
      "ds_read_b128 v[118:121], v1 offset:12288\n"
      "s_nop 0\n"
      "s_waitcnt lgkmcnt(2)\n"
      "v_mfma_f32_16x16x16_bf16 v[194:197], v[222:223], v[128:129], v[194:197]\n"
      "v_mfma_f32_16x16x32_bf16 v[148:151], v[188:191], a[144:147], 0\n"
      "v_mfma_f32_16x16x32_bf16 v[148:151], v[166:169], a[148:151], v[148:151]\n"
      "v_mfma_f32_16x16x32_bf16 v[172:175], v[188:191], a[168:171], 0\n"
      "v_mfma_f32_16x16x32_bf16 v[172:175], v[166:169], a[172:175], v[172:175]\n"
      "v_mfma_f32_16x16x32_bf16 v[156:159], v[188:191], a[192:195], 0\n"
      "v_mfma_f32_16x16x32_bf16 v[156:159], v[166:169], a[196:199], v[156:159]\n"
      "ds_read_b128 v[188:191], v9 offset:6656\n"
      "v_mfma_f32_16x16x32_bf16 v[148:151], v[130:133], a[152:155], v[148:151]\n"
      "v_mfma_f32_16x16x32_bf16 v[148:151], v[202:205], a[156:159], v[148:151]\n"
      "v_mfma_f32_16x16x32_bf16 v[172:175], v[130:133], a[176:179], v[172:175]\n"
      "v_mfma_f32_16x16x32_bf16 v[172:175], v[202:205], a[180:183], v[172:175]\n"
      "v_mfma_f32_16x16x32_bf16 v[156:159], v[130:133], a[200:203], v[156:159]\n"
      "s_waitcnt lgkmcnt(1)\n"
      "v_mfma_f32_16x16x32_bf16 v[156:159], v[202:205], a[204:207], v[156:159]\n"
      "s_waitcnt lgkmcnt(0)\n"
      "v_or3_b32 v188, v188, v189, v190\n"
      "v_or_b32_e64 v188, v188, v191\n"
      "v_mfma_f32_16x16x32_bf16 v[148:151], v[144:147], a[160:163], v[148:151]\n"
      "v_cmp_ne_u32 s[56:57], 0, v188\n"
      "v_mfma_f32_16x16x32_bf16 v[148:151], v[118:121], a[164:167], v[148:151]\n"
      "s_or_b64 vcc, s[56:57], s[54:55]\n"
      "s_cbranch_vccnz L_rare_1_%=\n"
      "L_back_2_%=:\n"
      "v_mfma_f32_16x16x32_bf16 v[172:175], v[144:147], a[184:187], v[172:175]\n"
      "ds_read_u16 v78, v11\n"
      "s_cbranch_vccnz L_rare_3_%=\n"
      "L_back_4_%=:\n"
      "v_mfma_f32_16x16x32_bf16 v[172:175], v[118:121], a[188:191], v[172:175]\n"
      "v_exp_f32 v140, v140\n"
      "v_mfma_f32_16x16x32_bf16 v[156:159], v[144:147], a[208:211], v[156:159]\n"
      "ds_read_u16 v82, v11 offset:32\n"
      "v_exp_f32 v141, v141\n"
      "v_mfma_f32_16x16x32_bf16 v[156:159], v[118:121], a[212:215], v[156:159]\n"
      "v_exp_f32 v142, v142\n"
      "v_mfma_f32_16x16x32_bf16 v[188:191], v[180:183], v[206:209], v[52:55]\n"
      "ds_read_u16 v86, v11 offset:64\n"
      "v_exp_f32 v143, v143\n"
      "v_mfma_f32_16x16x32_bf16 v[144:147], v[152:155], v[206:209], v[56:59]\n"
      "v_exp_f32 v218, v218\n"
      "v_mfma_f32_16x16x32_bf16 v[166:169], v[134:137], v[206:209], v[60:63]\n"
      "v_exp_f32_e64 v219, v219\n"
      "v_mfma_f32_16x16x32_bf16 v[202:205], v[210:213], v[206:209], v[26:29]\n"
      "v_exp_f32 v220, v220\n"
      "v_mfma_f32_16x16x16_bf16 v[188:191], v[160:161], v[108:109], v[188:191]\n"
      "v_exp_f32 v221, v221\n"
      "v_cvt_pk_bf16_f32 v140, v140, v141\n"
      "v_mfma_f32_16x16x16_bf16 v[144:147], v[138:139], v[108:109], v[144:147]\n"
      "v_cvt_pk_bf16_f32 v141, v142, v143\n"
      "v_cvt_pk_bf16_f32 v142, v218, v219\n"
      "v_cvt_pk_bf16_f32 v143, v220, v221\n"
      "v_mfma_f32_16x16x16_bf16 v[166:169], v[112:113], v[108:109], v[166:169]\n"
      "v_exp_f32 v124, v124\n"
      "v_exp_f32 v125, v125\n"
      "v_mfma_f32_16x16x16_bf16 v[202:205], v[222:223], v[108:109], v[202:205]\n"
      "v_exp_f32 v126, v126\n"
      "v_exp_f32 v127, v127\n"
      "v_mfma_f32_16x16x32_bf16 v[206:209], v[32:35], v[140:143], 0\n"
      "v_exp_f32_e64 v194, v194\n"
      "v_cvt_pk_bf16_f32 v88, v124, v125\n"
      "v_cvt_pk_bf16_f32 v89, v126, v127\n"
      "v_cvt_pk_bf16_f32 v90, v194, 0\n"
      "v_cvt_pk_bf16_f32 v104, v148, v149\n"
      "v_cvt_pk_bf16_f32 v105, v150, v151\n"
      "v_mfma_f32_16x16x32_bf16 v[206:209], v[32:35], v[88:91], v[206:209]\n"
      "s_cbranch_vccnz L_rare_5_%=\n"
      "L_back_6_%=:\n"
      "v_mfma_f32_16x16x32_bf16 v[124:127], v[104:107], v[140:143], 0\n"
      "s_waitcnt lgkmcnt(0)\n"
      "v_cvt_pk_bf16_f32 v100, v172, v173\n"
      "v_cvt_pk_bf16_f32 v101, v174, v175\n"
      "v_mfma_f32_16x16x32_bf16 v[124:127], v[76:79], v[88:91], v[124:127]\n"
      "v_cvt_pk_bf16_f32 v214, v156, v157\n"
      "v_cvt_pk_bf16_f32 v215, v158, v159\n"
      "v_mfma_f32_16x16x32_bf16 v[218:221], v[100:103], v[140:143], 0\n"
      "v_exp_f32 v188, v188\n"
      "v_mfma_f32_16x16x32_bf16 v[218:221], v[80:83], v[88:91], v[218:221]\n"
      "v_exp_f32 v189, v189\n"
      "v_mfma_f32_16x16x32_bf16 v[130:133], v[214:217], v[140:143], 0\n"
      "v_exp_f32_e64 v190, v190\n"
      "v_mfma_f32_16x16x32_bf16 v[130:133], v[84:87], v[88:91], v[130:133]\n"
      "v_exp_f32 v191, v191\n"
      "v_mfma_f32_16x16x32_bf16 v[140:143], v[180:183], v[198:201], v[64:67]\n"
      "v_exp_f32 v144, v144\n"
      "v_mfma_f32_16x16x32_bf16 v[108:111], v[152:155], v[198:201], v[68:71]\n"
      "ds_read_b128 v[194:197], v10\n"
      "v_exp_f32 v145, v145\n"
      "v_mfma_f32_16x16x32_bf16 v[120:123], v[134:137], v[198:201], v[72:75]\n"
      "v_exp_f32 v146, v146\n"
      "v_mfma_f32_16x16x32_bf16 v[172:175], v[210:213], v[198:201], v[28:31]\n"
      "v_exp_f32_e64 v147, v147\n"
      "v_mfma_f32_16x16x16_bf16 v[140:143], v[160:161], v[176:177], v[140:143]\n"
      "v_cvt_pk_bf16_f32 v188, v188, v189\n"
      "v_cvt_pk_bf16_f32 v189, v190, v191\n"
      "v_cvt_pk_bf16_f32 v190, v144, v145\n"
      "v_mfma_f32_16x16x16_bf16 v[108:111], v[138:139], v[176:177], v[108:111]\n"
      "v_cvt_pk_bf16_f32 v191, v146, v147\n"
      "v_exp_f32_e64 v166, v166\n"
      "v_mfma_f32_16x16x16_bf16 v[120:123], v[112:113], v[176:177], v[120:123]\n"
      "v_exp_f32 v167, v167\n"
      "v_exp_f32 v168, v168\n"
      "v_mfma_f32_16x16x16_bf16 v[172:175], v[222:223], v[176:177], v[172:175]\n"
      "v_exp_f32 v169, v169\n"
      "v_exp_f32 v202, v202\n"
      "v_mfma_f32_16x16x32_bf16 v[144:147], v[32:35], v[188:191], 0\n"
      "ds_read_b64 v[88:89], v10 offset:16\n"
      "v_cvt_pk_bf16_f32 v92, v166, v167\n"
      "v_cvt_pk_bf16_f32 v93, v168, v169\n"
      "v_cvt_pk_bf16_f32 v94, v202, 0\n"
      "v_rcp_f32 v206, v206\n"
      "s_cbranch_vccnz L_rare_7_%=\n"
      "L_back_8_%=:\n"
      "v_mfma_f32_16x16x32_bf16 v[144:147], v[32:35], v[92:95], v[144:147]\n"
      "v_mul_f32 v124, v206, v124\n"
      "v_mul_f32 v125, v206, v125\n"
      "v_mfma_f32_16x16x32_bf16 v[174:177], v[104:107], v[188:191], 0\n"
      "v_cvt_pk_bf16_f32 v124, v124, v125\n"
      "v_mul_f32 v126, v206, v126\n"
      "v_mfma_f32_16x16x32_bf16 v[174:177], v[76:79], v[92:95], v[174:177]\n"
      "v_mul_f32 v127, v206, v127\n"
      "v_cvt_pk_bf16_f32 v125, v126, v127\n"
      "v_mfma_f32_16x16x32_bf16 v[166:169], v[100:103], v[188:191], 0\n"
      "v_rcp_f32 v144, v144\n"
      "v_mfma_f32_16x16x32_bf16 v[166:169], v[80:83], v[92:95], v[166:169]\n"
      "v_mul_f32 v218, v206, v218\n"
      "v_mul_f32 v219, v206, v219\n"
      "v_mfma_f32_16x16x32_bf16 v[202:205], v[214:217], v[188:191], 0\n"
      "s_waitcnt lgkmcnt(0)\n"
      "v_mul_f32 v174, v144, v174\n"
      "v_mul_f32 v175, v144, v175\n"
      "v_mfma_f32_16x16x32_bf16 v[202:205], v[84:87], v[92:95], v[202:205]\n"
      "v_cvt_pk_bf16_f32 v174, v174, v175\n"
      "v_mul_f32 v176, v144, v176\n"
      "v_mfma_f32_16x16x32_bf16 v[188:191], v[194:197], v[180:183], v[30:33]\n"
      "v_mul_f32 v177, v144, v177\n"
      "v_mul_f32_e64 v220, v206, v220\n"
      "v_mfma_f32_16x16x32_bf16 v[180:183], v[194:197], v[152:155], v[30:33]\n"
      "v_mul_f32 v221, v206, v221\n"
      "v_cvt_pk_bf16_f32 v175, v176, v177\n"
      "v_mfma_f32_16x16x32_bf16 v[146:149], v[194:197], v[134:137], v[30:33]\n"
      "v_mul_f32 v166, v144, v166\n"
      "v_mul_f32 v167, v144, v167\n"
      "v_mfma_f32_16x16x32_bf16 v[134:137], v[194:197], v[210:213], v[30:33]\n"
      "v_mul_f32 v168, v144, v168\n"
      "v_mul_f32_e64 v169, v144, v169\n"
      "v_mfma_f32_16x16x16_bf16 v[188:191], v[88:89], v[160:161], v[188:191]\n"
      "v_cvt_pk_bf16_f32 v126, v218, v219\n"
      "v_cvt_pk_bf16_f32 v127, v220, v221\n"
      "v_cvt_pk_bf16_f32 v176, v166, v167\n"
      "v_mfma_f32_16x16x16_bf16 v[180:183], v[88:89], v[138:139], v[180:183]\n"
      "v_cvt_pk_bf16_f32 v177, v168, v169\n"
      "v_mul_f32 v130, v206, v130\n"
      "v_mul_f32 v131, v206, v131\n"
      "v_mfma_f32_16x16x16_bf16 v[146:149], v[88:89], v[112:113], v[146:149]\n"
      "v_mul_f32 v202, v144, v202\n"
      "v_mul_f32 v203, v144, v203\n"
      "ds_write_b128 v3, v[124:127]\n"
      "v_mfma_f32_16x16x16_bf16 v[134:137], v[88:89], v[222:223], v[134:137]\n"
      "ds_write_b128 v3, v[174:177] offset:256\n"
      "v_cndmask_b32 v188, v188, v180, s[58:59]\n"
      "v_cvt_pk_bf16_f32 v130, v130, v131\n"
      "v_mul_f32 v132, v206, v132\n"
      "v_cndmask_b32 v188, v188, v146, s[60:61]\n"
      "v_mul_f32 v133, v206, v133\n"
      "v_cvt_pk_bf16_f32 v202, v202, v203\n"
      "v_mul_f32 v204, v144, v204\n"
      "v_cndmask_b32 v188, v188, v134, s[62:63]\n"
      "s_cbranch_vccnz L_rare_9_%=\n"
      "L_back_10_%=:\n"
      "v_exp_f32 v188, v188\n"
      "v_mul_f32 v205, v144, v205\n"
      "v_cvt_pk_bf16_f32 v131, v132, v133\n"
      "v_cvt_pk_bf16_f32 v203, v204, v205\n"
      "ds_write_b64 v4, v[130:131]\n"
      "ds_write_b64 v4, v[202:203] offset:256\n"
      "v_cvt_pk_bf16_f32 v188, v188, v188\n"
      "ds_write_b16 v13, v188\n"
      "ds_read_b128 v[210:213], v14\n"
      "ds_read_b128 v[174:177], v14 offset:64\n"
      "v_exp_f32 v140, v140\n"
      "v_exp_f32 v141, v141\n"
      "v_exp_f32 v142, v142\n"
      "v_exp_f32 v143, v143\n"
      "v_exp_f32 v108, v108\n"
      "v_exp_f32 v109, v109\n"
      "v_exp_f32 v110, v110\n"
      "v_exp_f32 v111, v111\n"
      "v_cvt_pk_bf16_f32 v140, v140, v141\n"
      "v_cvt_pk_bf16_f32 v141, v142, v143\n"
      "v_cvt_pk_bf16_f32 v142, v108, v109\n"
      "v_cvt_pk_bf16_f32 v143, v110, v111\n"
      "v_exp_f32 v120, v120\n"
      "v_exp_f32 v121, v121\n"
      "v_mfma_f32_16x16x32_bf16 v[108:111], v[32:35], v[140:143], 0\n"
      "v_exp_f32 v122, v122\n"
      "v_exp_f32 v123, v123\n"
      "v_exp_f32 v172, v172\n"
      "v_cvt_pk_bf16_f32 v88, v120, v121\n"
      "v_cvt_pk_bf16_f32 v89, v122, v123\n"
      "v_cvt_pk_bf16_f32 v90, v172, 0\n"
      "v_cmp_ne_u32 s[56:57], 0, v162\n"
      "s_nop 0\n"
      "v_mfma_f32_16x16x32_bf16 v[108:111], v[32:35], v[88:91], v[108:111]\n"
      "s_cmp_ge_u32 s95, 0x40000000\n"
      "s_cselect_b64 s[56:57], -1, s[56:57]\n"
      "s_cmp_gt_u32 s42, s44\n"
      "s_cselect_b64 s[56:57], -1, s[56:57]\n"
      "v_mfma_f32_16x16x32_bf16 v[202:205], v[104:107], v[140:143], 0\n"
      "s_or_b64 vcc, s[56:57], s[54:55]\n"
      "s_add_u32 s42, s42, 1\n"
      "v_mfma_f32_16x16x32_bf16 v[202:205], v[76:79], v[88:91], v[202:205]\n"
      "v_rcp_f32 v108, v108\n"
      "v_mfma_f32_16x16x32_bf16 v[180:183], v[100:103], v[140:143], 0\n"
      "v_mfma_f32_16x16x32_bf16 v[180:183], v[80:83], v[88:91], v[180:183]\n"
      "v_mfma_f32_16x16x32_bf16 v[124:127], v[214:217], v[140:143], 0\n"
      "s_waitcnt lgkmcnt(0)\n"
      "v_mfma_f32_16x16x32_bf16 v[124:127], v[84:87], v[88:91], v[124:127]\n"
      "v_mfma_f32_16x16x32_bf16 v[188:191], v[210:213], v[32:35], 0\n"
      "v_mfma_f32_16x16x32_bf16 v[188:191], v[174:177], v[32:35], v[188:191]\n"
      "v_mul_f32 v202, v108, v202\n"
      "v_mul_f32 v203, v108, v203\n"
      "v_cvt_pk_bf16_f32 v202, v202, v203\n"
      "v_mul_f32 v204, v108, v204\n"
      "v_mul_f32 v205, v108, v205\n"
      "v_cvt_pk_bf16_f32 v203, v204, v205\n"
      "v_mul_f32 v180, v108, v180\n"
      "v_mul_f32 v181, v108, v181\n"
      "v_mul_f32 v182, v108, v182\n"
      "v_mul_f32 v183, v108, v183\n"
      "v_cvt_pk_bf16_f32 v204, v180, v181\n"
      "v_cvt_pk_bf16_f32 v205, v182, v183\n"
      "v_mul_f32 v124, v108, v124\n"
      "v_mul_f32 v125, v108, v125\n"
      "ds_write_b128 v3, v[202:205] offset:512\n"
      "v_cvt_pk_bf16_f32 v124, v124, v125\n"
      "v_mul_f32_e64 v126, v108, v126\n"
      "v_mul_f32 v127, v108, v127\n"
      "v_cvt_pk_bf16_f32 v125, v126, v127\n"
      "ds_write_b64 v4, v[124:125] offset:512\n"
      "s_waitcnt lgkmcnt(0)\n"
      "s_barrier\n"
      "s_cbranch_vccnz L_disp1_%=\n"
      "s_nop 7\n"
      "s_nop 7\n"
      "L_loop1_%=:\n"
      "v_mfma_f32_16x16x32_bf16 v[202:205], v[210:213], v[104:107], 0\n"
      "ds_read_b128 v[110:113], v9\n"
      "ds_read_b128 v[190:193], v9 offset:1024\n"
      "v_mfma_f32_16x16x32_bf16 v[202:205], v[174:177], v[76:79], v[202:205]\n"
      "ds_read_b128 v[152:155], v0 offset:18432\n"
      "v_mov_b32_dpp v188, v188 quad_perm:[0,0,0,0] row_mask:0xf bank_mask:0xf\n"
      "v_mfma_f32_16x16x32_bf16 v[130:133], v[210:213], v[100:103], 0\n"
      "ds_read_b128 v[102:105], v1 offset:18432\n"
      "v_rcp_f32 v188, v188\n"
      "v_mfma_f32_16x16x32_bf16 v[130:133], v[174:177], v[80:83], v[130:133]\n"
      "ds_read_b128 v[134:137], v0 offset:24576\n"
      "s_add_u32 m0, s64, 0\n"
      "v_mfma_f32_16x16x32_bf16 v[194:197], v[210:213], v[214:217], 0\n"
      "buffer_load_dwordx4 v16, s[16:19], s45 offen  lds\n"
      "v_mfma_f32_16x16x32_bf16 v[194:197], v[174:177], v[84:87], v[194:197]\n"
      "s_add_u32 m0, s64, 4096\n"
      "s_waitcnt lgkmcnt(4)\n"
      "v_add_f32 v110, v110, v112\n"
      "v_cndmask_b32 v202, v202, v130, s[58:59]\n"
      "v_add_f32 v111, v111, v113\n"
      "s_waitcnt lgkmcnt(3)\n"
      "v_add_f32_e64 v190, v190, v192\n"
      "v_add_f32 v191, v191, v193\n"
      "v_cndmask_b32 v202, v202, v194, s[60:61]\n"
      "s_waitcnt lgkmcnt(2)\n"
      "v_mfma_f32_16x16x32_bf16 v[170:173], a[0:3], v[152:155], 0\n"
      "buffer_load_dwordx4 v17, s[16:19], s45 offen  lds\n"
      "v_mfma_f32_16x16x32_bf16 v[198:201], a[24:27], v[152:155], 0\n"
      "s_add_u32 m0, s64, 8192\n"
      "v_mul_f32_e64 v202, v188, v202\n"
      "v_cvt_pk_bf16_f32 v202, v202, v202\n"
      "v_mfma_f32_16x16x32_bf16 v[156:159], a[48:51], v[152:155], 0\n"
      "ds_write_b16 v12, v202\n"
      "v_mfma_f32_16x16x32_bf16 v[174:177], a[72:75], v[152:155], 0\n"
      "s_waitcnt lgkmcnt(1)\n"
      "v_add_f32 v110, v110, v190\n"
      "v_add_f32_e64 v111, v111, v191\n"
      "v_mfma_f32_16x16x32_bf16 v[128:131], a[96:99], v[152:155], 0\n"
      "buffer_load_dwordx4 v18, s[16:19], s45 offen  lds\n"
      "v_mfma_f32_16x16x32_bf16 v[116:119], a[120:123], v[152:155], 0\n"
      "ds_read_b128 v[186:189], v1 offset:24576\n"
      "v_fma_f32 v190, -v110, v110, s52\n"
      "v_mfma_f32_16x16x32_bf16 v[170:173], a[4:7], v[102:105], v[170:173]\n"
      "ds_read_b128 v[152:155], v0 offset:30720\n"
      "v_fmac_f32 v190, 0x43400000, v111\n"
      "v_mfma_f32_16x16x32_bf16 v[170:173], a[8:11], v[134:137], v[170:173]\n"
      "ds_read_b128 v[210:213], v1 offset:30720\n"
      "v_max_f32_e64 v190, s52, v190\n"
      "v_mfma_f32_16x16x32_bf16 v[198:201], a[28:31], v[102:105], v[198:201]\n"
      "s_add_u32 m0, s64, 12288\n"
      "v_rsq_f32_e64 v190, v190\n"
      "v_mfma_f32_16x16x32_bf16 v[198:201], a[32:35], v[134:137], v[198:201]\n"
      "buffer_load_dwordx4 v19, s[16:19], s45 offen  lds\n"
      "v_mfma_f32_16x16x32_bf16 v[156:159], a[52:55], v[102:105], v[156:159]\n"
      "s_add_u32 m0, s65, 16384\n"
      "v_mul_f32 v141, 0x43400000, v190\n"
      "v_mul_f32 v140, -v110, v190\n"
      "v_mfma_f32_16x16x32_bf16 v[156:159], a[56:59], v[134:137], v[156:159]\n"
      "s_waitcnt lgkmcnt(2)\n"
      "v_mov_b32 v142, v141\n"
      "v_mov_b32_e64 v143, v140\n"
      "v_mfma_f32_16x16x32_bf16 v[174:177], a[76:79], v[102:105], v[174:177]\n"
      "s_waitcnt lgkmcnt(1)\n"
      "v_permlane32_swap_b32 v141, v142\n"
      "v_mfma_f32_16x16x32_bf16 v[174:177], a[80:83], v[134:137], v[174:177]\n"
      "buffer_load_dwordx4 v15, s[16:19], s45 offen  lds\n"
      "v_mfma_f32_16x16x32_bf16 v[128:131], a[100:103], v[102:105], v[128:131]\n"
      "s_waitcnt lgkmcnt(0)\n"
      "v_permlane32_swap_b32 v140, v143\n"
      "v_mfma_f32_16x16x32_bf16 v[128:131], a[104:107], v[134:137], v[128:131]\n"
      "v_mov_b32 v202, v141\n"
      "v_mov_b32 v203, v140\n"
      "v_mfma_f32_16x16x32_bf16 v[116:119], a[124:127], v[102:105], v[116:119]\n"
      "v_permlane16_swap_b32_e64 v141, v202\n"
      "v_mfma_f32_16x16x32_bf16 v[116:119], a[128:131], v[134:137], v[116:119]\n"
      "buffer_load_dwordx4 v[190:193], v27, s[24:27], s47 offen\n"
      "v_mfma_f32_16x16x32_bf16 v[170:173], a[12:15], v[186:189], v[170:173]\n"
      "v_permlane16_swap_b32_e64 v140, v203\n"
      "v_mfma_f32_16x16x32_bf16 v[170:173], a[16:19], v[152:155], v[170:173]\n"
      "v_fma_f32 v220, v220, v141, v140\n"
      "v_fma_f32 v221, v221, v141, v140\n"
      "v_mfma_f32_16x16x32_bf16 v[170:173], a[20:23], v[210:213], v[170:173]\n"
      "v_cvt_pk_bf16_f32 v220, v220, v221\n"
      "v_fma_f32 v222, v222, v141, v140\n"
      "v_mfma_f32_16x16x32_bf16 v[198:201], a[36:39], v[186:189], v[198:201]\n"
      "buffer_load_dword v78, v29, s[24:27], s47 offen\n"
      "v_mfma_f32_16x16x32_bf16 v[198:201], a[40:43], v[152:155], v[198:201]\n"
      "v_fma_f32 v223, v223, v141, v140\n"
      "v_fma_f32 v162, v162, v141, v140\n"
      "v_mfma_f32_16x16x32_bf16 v[198:201], a[44:47], v[210:213], v[198:201]\n"
      "ds_read_b128 v[132:135], v0 offset:20480\n"
      "v_fma_f32 v163, v163, v141, v140\n"
      "v_mfma_f32_16x16x32_bf16 v[156:159], a[60:63], v[186:189], v[156:159]\n"
      "ds_read_b128 v[102:105], v1 offset:20480\n"
      "v_cvt_pk_bf16_f32 v221, v222, v223\n"
      "v_mfma_f32_16x16x32_bf16 v[156:159], a[64:67], v[152:155], v[156:159]\n"
      "ds_read_b128 v[136:139], v0 offset:26624\n"
      "v_fma_f32 v206, v206, v141, v140\n"
      "v_mfma_f32_16x16x32_bf16 v[156:159], a[68:71], v[210:213], v[156:159]\n"
      "ds_read_b128 v[194:197], v1 offset:26624\n"
      "v_fma_f32 v207, v207, v141, v140\n"
      "v_mfma_f32_16x16x32_bf16 v[174:177], a[84:87], v[186:189], v[174:177]\n"
      "ds_read_b128 v[110:113], v0 offset:32768\n"
      "v_fma_f32 v208, v208, v141, v140\n"
      "v_mfma_f32_16x16x32_bf16 v[174:177], a[88:91], v[152:155], v[174:177]\n"
      "ds_read_b128 v[98:101], v1 offset:32768\n"
      "v_fma_f32 v209, v209, v141, v140\n"
      "v_mfma_f32_16x16x32_bf16 v[174:177], a[92:95], v[210:213], v[174:177]\n"
      "s_waitcnt lgkmcnt(5)\n"
      "v_cvt_pk_bf16_f32 v162, v162, v163\n"
      "v_fma_f32 v164, v164, v141, v140\n"
      "v_mfma_f32_16x16x32_bf16 v[128:131], a[108:111], v[186:189], v[128:131]\n"
      "s_waitcnt lgkmcnt(4)\n"
      "v_fma_f32 v165, v165, v141, v140\n"
      "v_cvt_pk_bf16_f32 v222, v206, v207\n"
      "v_mfma_f32_16x16x32_bf16 v[128:131], a[112:115], v[152:155], v[128:131]\n"
      "s_waitcnt lgkmcnt(3)\n"
      "v_cvt_pk_bf16_f32 v223, v208, v209\n"
      "v_cvt_pk_bf16_f32 v163, v164, v165\n"
      "v_mfma_f32_16x16x32_bf16 v[128:131], a[116:119], v[210:213], v[128:131]\n"
      "ds_write_b128 v5, v[220:223] offset:18432\n"
      "v_mfma_f32_16x16x32_bf16 v[116:119], a[132:135], v[186:189], v[116:119]\n"
      "s_waitcnt lgkmcnt(1)\n"
      "v_fma_f32 v166, v166, v202, v203\n"
      "v_fma_f32 v167, v167, v202, v203\n"
      "v_mfma_f32_16x16x32_bf16 v[116:119], a[136:139], v[152:155], v[116:119]\n"
      "v_cvt_pk_bf16_f32 v166, v166, v167\n"
      "v_fma_f32 v168, v168, v202, v203\n"
      "v_mfma_f32_16x16x32_bf16 v[116:119], a[140:143], v[210:213], v[116:119]\n"
      "ds_write_b64 v6, v[162:163] offset:18432\n"
      "v_mfma_f32_16x16x32_bf16 v[210:213], a[0:3], v[132:135], 0\n"
      "v_fma_f32 v169, v169, v202, v203\n"
      "v_fma_f32 v124, v124, v202, v203\n"
      "v_mfma_f32_16x16x32_bf16 v[210:213], a[4:7], v[102:105], v[210:213]\n"
      "v_fma_f32 v125, v125, v202, v203\n"
      "v_cvt_pk_bf16_f32 v167, v168, v169\n"
      "v_mfma_f32_16x16x32_bf16 v[210:213], a[8:11], v[136:139], v[210:213]\n"
      "v_fma_f32 v180, v180, v202, v203\n"
      "v_fma_f32 v181, v181, v202, v203\n"
      "v_mfma_f32_16x16x32_bf16 v[210:213], a[12:15], v[194:197], v[210:213]\n"
      "v_fma_f32 v182, v182, v202, v203\n"
      "v_fma_f32 v183, v183, v202, v203\n"
      "v_mfma_f32_16x16x32_bf16 v[210:213], a[16:19], v[110:113], v[210:213]\n"
      "v_cvt_pk_bf16_f32 v124, v124, v125\n"
      "v_fma_f32 v126, v126, v202, v203\n"
      "v_mfma_f32_16x16x32_bf16 v[210:213], a[20:23], v[98:101], v[210:213]\n"
      "v_fma_f32 v127, v127, v202, v203\n"
      "v_cvt_pk_bf16_f32 v168, v180, v181\n"
      "v_mfma_f32_16x16x32_bf16 v[220:223], a[24:27], v[132:135], 0\n"
      "v_cvt_pk_bf16_f32 v169, v182, v183\n"
      "v_cvt_pk_bf16_f32 v125, v126, v127\n"
      "v_mfma_f32_16x16x32_bf16 v[220:223], a[28:31], v[102:105], v[220:223]\n"
      "ds_write_b128 v5, v[166:169] offset:24576\n"
      "v_mfma_f32_16x16x32_bf16 v[220:223], a[32:35], v[136:139], v[220:223]\n"
      "v_fma_f32 v120, v120, v142, v143\n"
      "v_fma_f32 v121, v121, v142, v143\n"
      "v_mfma_f32_16x16x32_bf16 v[220:223], a[36:39], v[194:197], v[220:223]\n"
      "v_cvt_pk_bf16_f32 v120, v120, v121\n"
      "v_fma_f32 v122, v122, v142, v143\n"
      "v_mfma_f32_16x16x32_bf16 v[220:223], a[40:43], v[110:113], v[220:223]\n"
      "ds_write_b64 v6, v[124:125] offset:24576\n"
      "v_mfma_f32_16x16x32_bf16 v[220:223], a[44:47], v[98:101], v[220:223]\n"
      "v_fma_f32 v123, v123, v142, v143\n"
      "v_fma_f32 v144, v144, v142, v143\n"
      "v_mfma_f32_16x16x32_bf16 v[162:165], a[48:51], v[132:135], 0\n"
      "v_fma_f32 v145, v145, v142, v143\n"
      "v_cvt_pk_bf16_f32 v121, v122, v123\n"
      "v_mfma_f32_16x16x32_bf16 v[162:165], a[52:55], v[102:105], v[162:165]\n"
      "v_fma_f32 v148, v148, v142, v143\n"
      "v_fma_f32 v149, v149, v142, v143\n"
      "v_mfma_f32_16x16x32_bf16 v[162:165], a[56:59], v[136:139], v[162:165]\n"
      "v_fma_f32 v150, v150, v142, v143\n"
      "v_fma_f32 v151, v151, v142, v143\n"
      "v_mfma_f32_16x16x32_bf16 v[162:165], a[60:63], v[194:197], v[162:165]\n"
      "v_cvt_pk_bf16_f32 v144, v144, v145\n"
      "v_fma_f32 v146, v146, v142, v143\n"
      "v_mfma_f32_16x16x32_bf16 v[162:165], a[64:67], v[110:113], v[162:165]\n"
      "v_fma_f32 v147, v147, v142, v143\n"
      "v_cvt_pk_bf16_f32 v122, v148, v149\n"
      "v_mfma_f32_16x16x32_bf16 v[162:165], a[68:71], v[98:101], v[162:165]\n"
      "v_cvt_pk_bf16_f32 v123, v150, v151\n"
      "v_cvt_pk_bf16_f32 v145, v146, v147\n"
      "v_mfma_f32_16x16x32_bf16 v[140:143], a[72:75], v[132:135], 0\n"
      "ds_write_b128 v5, v[120:123] offset:30720\n"
      "v_mfma_f32_16x16x32_bf16 v[140:143], a[76:79], v[102:105], v[140:143]\n"
      "v_fma_f32 v150, v170, v170, s51\n"
      "v_fma_f32 v209, v174, v174, s51\n"
      "v_mfma_f32_16x16x32_bf16 v[140:143], a[80:83], v[136:139], v[140:143]\n"
      "v_fmac_f32 v150, v171, v171\n"
      "v_fmac_f32 v209, v175, v175\n"
      "v_mfma_f32_16x16x32_bf16 v[140:143], a[84:87], v[194:197], v[140:143]\n"
      "ds_write_b64 v6, v[144:145] offset:30720\n"
      "v_mfma_f32_16x16x32_bf16 v[140:143], a[88:91], v[110:113], v[140:143]\n"
      "v_fmac_f32 v150, v172, v172\n"
      "v_fmac_f32 v209, v176, v176\n"
      "v_mfma_f32_16x16x32_bf16 v[140:143], a[92:95], v[98:101], v[140:143]\n"
      "ds_read_b128 v[182:185], v0 offset:22528\n"
      "v_fmac_f32 v150, v173, v173\n"
      "v_mfma_f32_16x16x32_bf16 v[186:189], a[96:99], v[132:135], 0\n"
      "ds_read_b128 v[166:169], v1 offset:22528\n"
      "v_fmac_f32 v209, v177, v177\n"
      "v_mfma_f32_16x16x32_bf16 v[186:189], a[100:103], v[102:105], v[186:189]\n"
      "ds_read_b128 v[124:127], v0 offset:28672\n"
      "v_fmac_f32_e64 v150, v198, v198\n"
      "v_mfma_f32_16x16x32_bf16 v[186:189], a[104:107], v[136:139], v[186:189]\n"
      "ds_read_b128 v[106:109], v1 offset:28672\n"
      "v_fmac_f32 v209, v128, v128\n"
      "v_mfma_f32_16x16x32_bf16 v[186:189], a[108:111], v[194:197], v[186:189]\n"
      "ds_read_b128 v[202:205], v0 offset:34816\n"
      "v_fmac_f32 v150, v199, v199\n"
      "v_mfma_f32_16x16x32_bf16 v[186:189], a[112:115], v[110:113], v[186:189]\n"
      "ds_read_b128 v[214:217], v1 offset:34816\n"
      "v_fmac_f32 v209, v129, v129\n"
      "v_mfma_f32_16x16x32_bf16 v[186:189], a[116:119], v[98:101], v[186:189]\n"
      "s_waitcnt lgkmcnt(5)\n"
      "v_fmac_f32 v150, v200, v200\n"
      "v_fmac_f32 v209, v130, v130\n"
      "v_mfma_f32_16x16x32_bf16 v[152:155], a[120:123], v[132:135], 0\n"
      "s_waitcnt lgkmcnt(4)\n"
      "v_fmac_f32 v150, v201, v201\n"
      "v_fmac_f32_e64 v209, v131, v131\n"
      "v_mfma_f32_16x16x32_bf16 v[152:155], a[124:127], v[102:105], v[152:155]\n"
      "s_waitcnt lgkmcnt(3)\n"
      "v_fmac_f32 v150, v156, v156\n"
      "v_fmac_f32 v209, v116, v116\n"
      "v_mfma_f32_16x16x32_bf16 v[152:155], a[128:131], v[136:139], v[152:155]\n"
      "s_waitcnt lgkmcnt(2)\n"
      "v_fmac_f32 v150, v157, v157\n"
      "v_fmac_f32 v209, v117, v117\n"
      "v_mfma_f32_16x16x32_bf16 v[152:155], a[132:135], v[194:197], v[152:155]\n"
      "s_waitcnt lgkmcnt(1)\n"
      "v_fmac_f32 v150, v158, v158\n"
      "v_fmac_f32_e64 v209, v118, v118\n"
      "v_mfma_f32_16x16x32_bf16 v[152:155], a[136:139], v[110:113], v[152:155]\n"
      "s_waitcnt lgkmcnt(0)\n"
      "v_fmac_f32 v150, v159, v159\n"
      "v_fmac_f32_e64 v209, v119, v119\n"
      "v_mfma_f32_16x16x32_bf16 v[152:155], a[140:143], v[98:101], v[152:155]\n"
      "ds_read_b128 v[178:181], v9 offset:4608\n"
      "v_permlane16_swap_b32 v150, v209\n"
      "v_mfma_f32_16x16x32_bf16 v[112:115], a[0:3], v[182:185], 0\n"
      "v_add_f32 v150, v150, v209\n"
      "v_mov_b32_e64 v209, v150\n"
      "v_mfma_f32_16x16x32_bf16 v[112:115], a[4:7], v[166:169], v[112:115]\n"
      "v_fma_f32 v218, v210, v210, s51\n"
      "v_permlane32_swap_b32 v150, v209\n"
      "v_mfma_f32_16x16x32_bf16 v[112:115], a[8:11], v[124:127], v[112:115]\n"
      "v_add_f32 v150, v150, v209\n"
      "v_rsq_f32 v150, v150\n"
      "v_mfma_f32_16x16x32_bf16 v[112:115], a[12:15], v[106:109], v[112:115]\n"
      "v_mov_b32 v209, v150\n"
      "v_fma_f32 v76, v140, v140, s51\n"
      "v_mfma_f32_16x16x32_bf16 v[112:115], a[16:19], v[202:205], v[112:115]\n"
      "v_permlane16_swap_b32 v150, v209\n"
      "v_mfma_f32_16x16x32_bf16 v[112:115], a[20:23], v[214:217], v[112:115]\n"
      "v_mul_f32 v150, s50, v150\n"
      "v_mul_f32 v174, v209, v174\n"
      "v_mfma_f32_16x16x32_bf16 v[144:147], a[24:27], v[182:185], 0\n"
      "v_mul_f32 v175, v209, v175\n"
      "v_mul_f32 v170, v150, v170\n"
      "v_mfma_f32_16x16x32_bf16 v[144:147], a[28:31], v[166:169], v[144:147]\n"
      "v_mul_f32 v171, v150, v171\n"
      "v_cvt_pk_bf16_f32 v174, v174, v175\n"
      "v_mfma_f32_16x16x32_bf16 v[144:147], a[32:35], v[124:127], v[144:147]\n"
      "v_mul_f32 v176, v209, v176\n"
      "v_mul_f32 v177, v209, v177\n"
      "v_mfma_f32_16x16x32_bf16 v[144:147], a[36:39], v[106:109], v[144:147]\n"
      "v_mul_f32 v116, v209, v116\n"
      "v_mul_f32 v117, v209, v117\n"
      "v_mfma_f32_16x16x32_bf16 v[144:147], a[40:43], v[202:205], v[144:147]\n"
      "v_cvt_pk_bf16_f32 v170, v170, v171\n"
      "v_mul_f32 v172, v150, v172\n"
      "v_mfma_f32_16x16x32_bf16 v[144:147], a[44:47], v[214:217], v[144:147]\n"
      "v_mul_f32 v173, v150, v173\n"
      "v_mul_f32_e64 v156, v150, v156\n"
      "v_mfma_f32_16x16x32_bf16 v[132:135], a[48:51], v[182:185], 0\n"
      "v_mul_f32 v157, v150, v157\n"
      "v_cvt_pk_bf16_f32 v175, v176, v177\n"
      "v_mfma_f32_16x16x32_bf16 v[132:135], a[52:55], v[166:169], v[132:135]\n"
      "v_mul_f32 v128, v209, v128\n"
      "v_mul_f32 v129, v209, v129\n"
      "v_mfma_f32_16x16x32_bf16 v[132:135], a[56:59], v[124:127], v[132:135]\n"
      "v_mul_f32 v130, v209, v130\n"
      "v_mul_f32_e64 v131, v209, v131\n"
      "v_mfma_f32_16x16x32_bf16 v[132:135], a[60:63], v[106:109], v[132:135]\n"
      "ds_read_b128 v[194:197], v7\n"
      "v_cvt_pk_bf16_f32 v116, v116, v117\n"
      "v_mfma_f32_16x16x32_bf16 v[132:135], a[64:67], v[202:205], v[132:135]\n"
      "ds_read_b128 v[120:123], v7 offset:4096\n"
      "v_mul_f32 v118, v209, v118\n"
      "v_mfma_f32_16x16x32_bf16 v[132:135], a[68:71], v[214:217], v[132:135]\n"
      "v_mul_f32 v119, v209, v119\n"
      "v_cvt_pk_bf16_f32 v171, v172, v173\n"
      "v_mfma_f32_16x16x32_bf16 v[96:99], a[72:75], v[182:185], 0\n"
      "ds_read_b128 v[206:209], v7 offset:8192\n"
      "v_mul_f32 v198, v150, v198\n"
      "v_mfma_f32_16x16x32_bf16 v[96:99], a[76:79], v[166:169], v[96:99]\n"
      "ds_read_b128 v[102:105], v7 offset:12288\n"
      "v_mul_f32 v199, v150, v199\n"
      "v_mfma_f32_16x16x32_bf16 v[96:99], a[80:83], v[124:127], v[96:99]\n"
      "v_mul_f32 v200, v150, v200\n"
      "v_mul_f32 v201, v150, v201\n"
      "v_mfma_f32_16x16x32_bf16 v[96:99], a[84:87], v[106:109], v[96:99]\n"
      "ds_read_b128 v[136:139], v7 offset:16384\n"
      "v_cvt_pk_bf16_f32 v156, v156, v157\n"
      "v_mfma_f32_16x16x32_bf16 v[96:99], a[88:91], v[202:205], v[96:99]\n"
      "v_mul_f32 v158, v150, v158\n"
      "v_mul_f32 v159, v150, v159\n"
      "v_mfma_f32_16x16x32_bf16 v[96:99], a[92:95], v[214:217], v[96:99]\n"
      "v_cvt_pk_bf16_f32 v176, v128, v129\n"
      "v_cvt_pk_bf16_f32 v177, v130, v131\n"
      "v_mfma_f32_16x16x32_bf16 v[128:131], a[96:99], v[182:185], 0\n"
      "v_cvt_pk_bf16_f32 v117, v118, v119\n"
      "v_cvt_pk_bf16_f32 v172, v198, v199\n"
      "v_mfma_f32_16x16x32_bf16 v[128:131], a[100:103], v[166:169], v[128:131]\n"
      "s_waitcnt lgkmcnt(1)\n"
      "v_cvt_pk_bf16_f32 v173, v200, v201\n"
      "v_cvt_pk_bf16_f32 v157, v158, v159\n"
      "v_mfma_f32_16x16x32_bf16 v[128:131], a[104:107], v[124:127], v[128:131]\n"
      "buffer_store_dwordx4 v[194:197], v20, s[20:23], s46 offen sc1\n"
      "v_mfma_f32_16x16x32_bf16 v[128:131], a[108:111], v[106:109], v[128:131]\n"
      "s_waitcnt lgkmcnt(0)\n"
      "v_fmac_f32 v218, v211, v211\n"
      "v_fmac_f32 v76, v141, v141\n"
      "v_mfma_f32_16x16x32_bf16 v[128:131], a[112:115], v[202:205], v[128:131]\n"
      "v_fmac_f32 v218, v212, v212\n"
      "v_fmac_f32 v76, v142, v142\n"
      "v_mfma_f32_16x16x32_bf16 v[128:131], a[116:119], v[214:217], v[128:131]\n"
      "v_fmac_f32 v218, v213, v213\n"
      "v_fmac_f32 v76, v143, v143\n"
      "v_mfma_f32_16x16x32_bf16 v[194:197], a[120:123], v[182:185], 0\n"
      "v_fmac_f32 v218, v220, v220\n"
      "v_fmac_f32 v76, v186, v186\n"
      "v_mfma_f32_16x16x32_bf16 v[194:197], a[124:127], v[166:169], v[194:197]\n"
      "v_fmac_f32 v218, v221, v221\n"
      "v_fmac_f32 v76, v187, v187\n"
      "v_mfma_f32_16x16x32_bf16 v[194:197], a[128:131], v[124:127], v[194:197]\n"
      "v_fmac_f32 v218, v222, v222\n"
      "v_fmac_f32 v76, v188, v188\n"
      "v_mfma_f32_16x16x32_bf16 v[194:197], a[132:135], v[106:109], v[194:197]\n"
      "v_fmac_f32 v218, v223, v223\n"
      "v_fmac_f32 v76, v189, v189\n"
      "v_mfma_f32_16x16x32_bf16 v[194:197], a[136:139], v[202:205], v[194:197]\n"
      "buffer_store_dwordx4 v[120:123], v21, s[20:23], s46 offen sc1\n"
      "v_mfma_f32_16x16x32_bf16 v[194:197], a[140:143], v[214:217], v[194:197]\n"
      "v_fmac_f32 v218, v162, v162\n"
      "v_fmac_f32 v76, v152, v152\n"
      "v_mfma_f32_16x16x32_bf16 v[148:151], v[182:185], a[144:147], 0\n"
      "v_fmac_f32 v218, v163, v163\n"
      "v_fmac_f32 v76, v153, v153\n"
      "v_mfma_f32_16x16x32_bf16 v[148:151], v[166:169], a[148:151], v[148:151]\n"
      "v_fmac_f32 v218, v164, v164\n"
      "v_fmac_f32 v76, v154, v154\n"
      "v_mfma_f32_16x16x32_bf16 v[158:161], v[182:185], a[168:171], 0\n"
      "v_fmac_f32 v218, v165, v165\n"
      "v_fmac_f32 v76, v155, v155\n"
      "v_mfma_f32_16x16x32_bf16 v[158:161], v[166:169], a[172:175], v[158:161]\n"
      "v_fma_f32 v81, v112, v112, s51\n"
      "v_permlane16_swap_b32 v218, v76\n"
      "v_mfma_f32_16x16x32_bf16 v[118:121], v[182:185], a[192:195], 0\n"
      "v_add_f32 v218, v218, v76\n"
      "v_mov_b32_e64 v76, v218\n"
      "v_mfma_f32_16x16x32_bf16 v[118:121], v[166:169], a[196:199], v[118:121]\n"
      "ds_read_b128 v[198:201], v0 offset:20480\n"
      "v_permlane32_swap_b32_e64 v218, v76\n"
      "v_mfma_f32_16x16x32_bf16 v[148:151], v[124:127], a[152:155], v[148:151]\n"
      "buffer_store_dwordx4 v[206:209], v22, s[20:23], s46 offen sc1\n"
      "v_mfma_f32_16x16x32_bf16 v[148:151], v[106:109], a[156:159], v[148:151]\n"
      "ds_read_b128 v[182:185], v1 offset:20480\n"
      "v_add_f32_e64 v218, v218, v76\n"
      "v_mfma_f32_16x16x32_bf16 v[158:161], v[124:127], a[176:179], v[158:161]\n"
      "v_rsq_f32 v218, v218\n"
      "v_mfma_f32_16x16x32_bf16 v[158:161], v[106:109], a[180:183], v[158:161]\n"
      "v_mov_b32 v76, v218\n"
      "v_fma_f32 v89, v96, v96, s51\n"
      "v_mfma_f32_16x16x32_bf16 v[118:121], v[124:127], a[200:203], v[118:121]\n"
      "s_waitcnt lgkmcnt(1)\n"
      "v_permlane16_swap_b32 v218, v76\n"
      "v_mfma_f32_16x16x32_bf16 v[118:121], v[106:109], a[204:207], v[118:121]\n"
      "ds_read_b128 v[108:111], v0 offset:26624\n"
      "v_mul_f32 v218, s50, v218\n"
      "v_mfma_f32_16x16x32_bf16 v[148:151], v[202:205], a[160:163], v[148:151]\n"
      "ds_read_b128 v[124:127], v1 offset:26624\n"
      "v_mul_f32 v140, v76, v140\n"
      "v_mfma_f32_16x16x32_bf16 v[148:151], v[214:217], a[164:167], v[148:151]\n"
      "s_waitcnt lgkmcnt(2)\n"
      "v_mul_f32 v141, v76, v141\n"
      "v_mul_f32_e64 v210, v218, v210\n"
      "v_mfma_f32_16x16x32_bf16 v[158:161], v[202:205], a[184:187], v[158:161]\n"
      "buffer_store_dwordx4 v[102:105], v23, s[20:23], s46 offen sc1\n"
      "v_mfma_f32_16x16x32_bf16 v[158:161], v[214:217], a[188:191], v[158:161]\n"
      "v_mul_f32 v211, v218, v211\n"
      "v_cvt_pk_bf16_f32 v140, v140, v141\n"
      "v_mfma_f32_16x16x32_bf16 v[118:121], v[202:205], a[208:211], v[118:121]\n"
      "s_waitcnt lgkmcnt(1)\n"
      "v_mul_f32 v142, v76, v142\n"
      "v_mul_f32 v143, v76, v143\n"
      "v_mfma_f32_16x16x32_bf16 v[118:121], v[214:217], a[212:215], v[118:121]\n"
      "ds_read_b128 v[206:209], v0 offset:32768\n"
      "v_mul_f32_e64 v152, v76, v152\n"
      "v_mfma_f32_16x16x32_bf16 v[104:107], v[198:201], a[144:147], 0\n"
      "ds_read_b128 v[166:169], v1 offset:32768\n"
      "v_mul_f32 v153, v76, v153\n"
      "v_mfma_f32_16x16x32_bf16 v[104:107], v[182:185], a[148:151], v[104:107]\n"
      "s_waitcnt lgkmcnt(2)\n"
      "v_cvt_pk_bf16_f32 v210, v210, v211\n"
      "v_mul_f32 v212, v218, v212\n"
      "v_mfma_f32_16x16x32_bf16 v[100:103], v[198:201], a[168:171], 0\n"
      "v_mul_f32 v213, v218, v213\n"
      "v_mul_f32_e64 v162, v218, v162\n"
      "v_mfma_f32_16x16x32_bf16 v[100:103], v[182:185], a[172:175], v[100:103]\n"
      "v_mul_f32_e64 v163, v218, v163\n"
      "v_cvt_pk_bf16_f32 v141, v142, v143\n"
      "v_mfma_f32_16x16x32_bf16 v[214:217], v[198:201], a[192:195], 0\n"
      "buffer_store_dwordx4 v[136:139], v25, s[20:23], s46 offen sc1\n"
      "v_mfma_f32_16x16x32_bf16 v[214:217], v[182:185], a[196:199], v[214:217]\n"
      "s_waitcnt lgkmcnt(0)\n"
      "v_mul_f32 v186, v76, v186\n"
      "v_mul_f32_e64 v187, v76, v187\n"
      "v_mfma_f32_16x16x32_bf16 v[104:107], v[108:111], a[152:155], v[104:107]\n"
      "v_mul_f32 v188, v76, v188\n"
      "v_mul_f32 v189, v76, v189\n"
      "v_mfma_f32_16x16x32_bf16 v[104:107], v[124:127], a[156:159], v[104:107]\n"
      "v_cvt_pk_bf16_f32 v152, v152, v153\n"
      "v_mul_f32 v154, v76, v154\n"
      "v_mfma_f32_16x16x32_bf16 v[100:103], v[108:111], a[176:179], v[100:103]\n"
      "ds_read_b128 v[202:205], v10 offset:1200\n"
      "v_mul_f32 v155, v76, v155\n"
      "v_mfma_f32_16x16x32_bf16 v[100:103], v[124:127], a[180:183], v[100:103]\n"
      "v_cvt_pk_bf16_f32 v211, v212, v213\n"
      "v_mul_f32 v220, v218, v220\n"
      "v_mfma_f32_16x16x32_bf16 v[214:217], v[108:111], a[200:203], v[214:217]\n"
      "v_mul_f32 v221, v218, v221\n"
      "v_mul_f32_e64 v222, v218, v222\n"
      "v_mfma_f32_16x16x32_bf16 v[214:217], v[124:127], a[204:207], v[214:217]\n"
      "v_mul_f32_e64 v223, v218, v223\n"
      "v_cvt_pk_bf16_f32 v162, v162, v163\n"
      "v_mfma_f32_16x16x32_bf16 v[104:107], v[206:209], a[160:163], v[104:107]\n"
      "ds_read_b64 v[122:123], v10 offset:1192\n"
      "v_mul_f32 v164, v218, v164\n"
      "v_mfma_f32_16x16x32_bf16 v[104:107], v[166:169], a[164:167], v[104:107]\n"
      "ds_read_b128 v[124:127], v0 offset:18432\n"
      "v_mul_f32 v165, v218, v165\n"
      "v_mfma_f32_16x16x32_bf16 v[100:103], v[206:209], a[184:187], v[100:103]\n"
      "ds_read_b128 v[108:111], v1 offset:18432\n"
      "v_cvt_pk_bf16_f32 v142, v186, v187\n"
      "v_mfma_f32_16x16x32_bf16 v[100:103], v[166:169], a[188:191], v[100:103]\n"
      "s_waitcnt lgkmcnt(3)\n"
      "v_cvt_pk_bf16_f32 v143, v188, v189\n"
      "v_cvt_pk_bf16_f32 v153, v154, v155\n"
      "v_mfma_f32_16x16x32_bf16 v[214:217], v[206:209], a[208:211], v[214:217]\n"
      "v_cvt_pk_bf16_f32 v212, v220, v221\n"
      "v_cvt_pk_bf16_f32 v213, v222, v223\n"
      "v_mfma_f32_16x16x32_bf16 v[214:217], v[166:169], a[212:215], v[214:217]\n"
      "v_cvt_pk_bf16_f32 v163, v164, v165\n"
      "v_fmac_f32 v81, v113, v113\n"
      "v_mfma_f32_16x16x32_bf16 v[166:169], v[174:177], v[170:173], v[40:43]\n"
      "s_waitcnt lgkmcnt(2)\n"
      "v_fmac_f32 v89, v97, v97\n"
      "v_fmac_f32_e64 v81, v114, v114\n"
      "v_mfma_f32_16x16x32_bf16 v[206:209], v[140:143], v[170:173], v[44:47]\n"
      "ds_read_b128 v[218:221], v0 offset:24576\n"
      "v_fmac_f32 v89, v98, v98\n"
      "v_fmac_f32 v81, v115, v115\n"
      "v_fmac_f32 v89, v99, v99\n"
      "v_fmac_f32 v81, v144, v144\n"
      "v_fmac_f32 v89, v128, v128\n"
      "v_fmac_f32 v81, v145, v145\n"
      "v_fmac_f32 v89, v129, v129\n"
      "v_fmac_f32 v81, v146, v146\n"
      "v_fmac_f32 v89, v130, v130\n"
      "v_fmac_f32 v81, v147, v147\n"
      "v_fmac_f32 v89, v131, v131\n"
      "v_fmac_f32 v81, v132, v132\n"
      "v_fmac_f32 v89, v194, v194\n"
      "v_fmac_f32 v81, v133, v133\n"
      "v_fmac_f32 v89, v195, v195\n"
      "v_fmac_f32 v81, v134, v134\n"
      "v_fmac_f32 v89, v196, v196\n"
      "v_fmac_f32 v81, v135, v135\n"
      "v_fmac_f32_e64 v89, v197, v197\n"
      "v_cvt_pk_bf16_f32 v107, v106, v107\n"
      "v_cvt_pk_bf16_f32 v106, v104, v105\n"
      "v_permlane16_swap_b32 v81, v89\n"
      "v_add_f32 v81, v81, v89\n"
      "v_mov_b32_e64 v89, v81\n"
      "v_cvt_pk_bf16_f32 v76, v148, v149\n"
      "v_cvt_pk_bf16_f32 v77, v150, v151\n"
      "v_permlane32_swap_b32 v81, v89\n"
      "v_add_f32 v81, v81, v89\n"
      "v_rsq_f32 v81, v81\n"
      "v_cvt_pk_bf16_f32 v103, v102, v103\n"
      "v_mov_b32 v89, v81\n"
      "v_cvt_pk_bf16_f32 v102, v100, v101\n"
      "v_cvt_pk_bf16_f32 v80, v158, v159\n"
      "v_permlane16_swap_b32 v81, v89\n"
      "v_mul_f32 v96, v89, v96\n"
      "v_mul_f32_e64 v97, v89, v97\n"
      "v_cvt_pk_bf16_f32 v96, v96, v97\n"
      "v_mul_f32 v98, v89, v98\n"
      "v_mul_f32 v99, v89, v99\n"
      "v_cvt_pk_bf16_f32 v97, v98, v99\n"
      "v_mul_f32 v128, v89, v128\n"
      "v_mul_f32 v129, v89, v129\n"
      "v_mul_f32 v130, v89, v130\n"
      "v_mul_f32 v131, v89, v131\n"
      "v_cvt_pk_bf16_f32 v98, v128, v129\n"
      "v_cvt_pk_bf16_f32 v99, v130, v131\n"
      "v_mul_f32 v81, s50, v81\n"
      "v_mul_f32 v112, v81, v112\n"
      "v_mfma_f32_16x16x32_bf16 v[182:185], v[96:99], v[170:173], v[48:51]\n"
      "ds_read_b128 v[128:131], v1 offset:24576\n"
      "v_mul_f32 v113, v81, v113\n"
      "v_mfma_f32_16x16x32_bf16 v[186:189], v[202:205], v[170:173], v[24:27]\n"
      "s_waitcnt lgkmcnt(1)\n"
      "v_mul_f32 v194, v89, v194\n"
      "v_mul_f32 v195, v89, v195\n"
      "v_mfma_f32_16x16x16_bf16 v[166:169], v[116:117], v[156:157], v[166:169]\n"
      "v_cvt_pk_bf16_f32 v112, v112, v113\n"
      "v_mul_f32 v114, v81, v114\n"
      "v_mul_f32 v115, v81, v115\n"
      "v_mfma_f32_16x16x16_bf16 v[206:209], v[152:153], v[156:157], v[206:209]\n"
      "v_mul_f32 v132, v81, v132\n"
      "v_mul_f32 v133, v81, v133\n"
      "v_cvt_pk_bf16_f32 v194, v194, v195\n"
      "v_mul_f32 v196, v89, v196\n"
      "v_mul_f32 v197, v89, v197\n"
      "v_cvt_pk_bf16_f32 v113, v114, v115\n"
      "v_mul_f32 v144, v81, v144\n"
      "v_mul_f32 v145, v81, v145\n"
      "v_mul_f32 v146, v81, v146\n"
      "v_mul_f32 v147, v81, v147\n"
      "v_cvt_pk_bf16_f32 v132, v132, v133\n"
      "v_mul_f32 v134, v81, v134\n"
      "v_mul_f32 v135, v81, v135\n"
      "v_cvt_pk_bf16_f32 v195, v196, v197\n"
      "v_cvt_pk_bf16_f32 v114, v144, v145\n"
      "v_cvt_pk_bf16_f32 v115, v146, v147\n"
      "v_mfma_f32_16x16x16_bf16 v[182:185], v[194:195], v[156:157], v[182:185]\n"
      "v_cvt_pk_bf16_f32 v133, v134, v135\n"
      "v_exp_f32 v166, v166\n"
      "v_mfma_f32_16x16x16_bf16 v[186:189], v[122:123], v[156:157], v[186:189]\n"
      "v_exp_f32 v167, v167\n"
      "ds_read_b128 v[144:147], v0 offset:30720\n"
      "v_mfma_f32_16x16x32_bf16 v[196:199], v[124:127], a[144:147], 0\n"
      "ds_read_b128 v[170:173], v1 offset:30720\n"
      "v_exp_f32_e64 v168, v168\n"
      "v_mfma_f32_16x16x32_bf16 v[196:199], v[108:111], a[148:151], v[196:199]\n"
      "s_waitcnt lgkmcnt(2)\n"
      "v_exp_f32 v169, v169\n"
      "v_mfma_f32_16x16x32_bf16 v[148:151], v[124:127], a[168:171], 0\n"
      "v_exp_f32_e64 v206, v206\n"
      "v_mfma_f32_16x16x32_bf16 v[148:151], v[108:111], a[172:175], v[148:151]\n"
      "v_exp_f32 v207, v207\n"
      "v_mfma_f32_16x16x32_bf16 v[154:157], v[124:127], a[192:195], 0\n"
      "v_exp_f32 v208, v208\n"
      "v_mfma_f32_16x16x32_bf16 v[154:157], v[108:111], a[196:199], v[154:157]\n"
      "s_waitcnt lgkmcnt(1)\n"
      "v_exp_f32_e64 v209, v209\n"
      "v_mfma_f32_16x16x32_bf16 v[196:199], v[218:221], a[152:155], v[196:199]\n"
      "s_waitcnt lgkmcnt(0)\n"
      "v_cvt_pk_bf16_f32 v166, v166, v167\n"
      "v_cvt_pk_bf16_f32 v167, v168, v169\n"
      "v_mfma_f32_16x16x32_bf16 v[196:199], v[128:131], a[156:159], v[196:199]\n"
      "v_cvt_pk_bf16_f32 v168, v206, v207\n"
      "v_cvt_pk_bf16_f32 v169, v208, v209\n"
      "v_mfma_f32_16x16x32_bf16 v[148:151], v[218:221], a[176:179], v[148:151]\n"
      "v_exp_f32 v182, v182\n"
      "v_mfma_f32_16x16x32_bf16 v[148:151], v[128:131], a[180:183], v[148:151]\n"
      "v_exp_f32 v183, v183\n"
      "v_mfma_f32_16x16x32_bf16 v[154:157], v[218:221], a[200:203], v[154:157]\n"
      "v_exp_f32 v184, v184\n"
      "v_mfma_f32_16x16x32_bf16 v[154:157], v[128:131], a[204:207], v[154:157]\n"
      "v_exp_f32 v185, v185\n"
      "v_mfma_f32_16x16x32_bf16 v[196:199], v[144:147], a[160:163], v[196:199]\n"
      "v_exp_f32_e64 v186, v186\n"
      "v_mfma_f32_16x16x32_bf16 v[196:199], v[170:173], a[164:167], v[196:199]\n"
      "ds_read_b128 v[124:127], v2\n"
      "v_cvt_pk_bf16_f32 v88, v182, v183\n"
      "v_mfma_f32_16x16x32_bf16 v[148:151], v[144:147], a[184:187], v[148:151]\n"
      "v_cvt_pk_bf16_f32 v89, v184, v185\n"
      "v_cvt_pk_bf16_f32 v90, v186, 0\n"
      "v_mfma_f32_16x16x32_bf16 v[148:151], v[170:173], a[188:191], v[148:151]\n"
      "v_cvt_pk_bf16_f32 v81, v160, v161\n"
      "v_cvt_pk_bf16_f32 v217, v216, v217\n"
      "v_mfma_f32_16x16x32_bf16 v[154:157], v[144:147], a[208:211], v[154:157]\n"
      "ds_read_b128 v[108:111], v2 offset:3072\n"
      "v_cvt_pk_bf16_f32 v104, v196, v197\n"
      "v_mfma_f32_16x16x32_bf16 v[154:157], v[170:173], a[212:215], v[154:157]\n"
      "v_cvt_pk_bf16_f32 v105, v198, v199\n"
      "v_cvt_pk_bf16_f32 v216, v214, v215\n"
      "v_mfma_f32_16x16x32_bf16 v[144:147], v[174:177], v[210:213], v[52:55]\n"
      "v_cvt_pk_bf16_f32 v100, v148, v149\n"
      "v_cvt_pk_bf16_f32 v101, v150, v151\n"
      "v_mfma_f32_16x16x32_bf16 v[182:185], v[140:143], v[210:213], v[56:59]\n"
      "ds_read_b128 v[128:131], v2 offset:6144\n"
      "v_cvt_pk_bf16_f32 v84, v118, v119\n"
      "v_mfma_f32_16x16x32_bf16 v[186:189], v[96:99], v[210:213], v[60:63]\n"
      "s_waitcnt lgkmcnt(2)\n"
      "v_cvt_pk_bf16_f32 v214, v154, v155\n"
      "v_cvt_pk_bf16_f32 v215, v156, v157\n"
      "v_mfma_f32_16x16x32_bf16 v[158:161], v[202:205], v[210:213], v[26:29]\n"
      "v_cvt_pk_bf16_f32 v85, v120, v121\n"
      "s_waitcnt vmcnt(6)\n"
      "v_or3_b32 v190, v190, v191, v192\n"
      "v_mfma_f32_16x16x16_bf16 v[144:147], v[116:117], v[162:163], v[144:147]\n"
      "s_waitcnt vmcnt(5)\n"
      "v_or3_b32 v190, v190, v193, v78\n"
      "v_and_b32 v190, 0x7fff7fff, v190\n"
      "v_or3_b32 v178, v178, v179, v180\n"
      "v_mfma_f32_16x16x16_bf16 v[182:185], v[152:153], v[162:163], v[182:185]\n"
      "v_readlane_b32 s45, v37, s43\n"
      "v_readlane_b32 s46, v37, s42\n"
      "v_or_b32 v178, v178, v181\n"
      "v_mfma_f32_16x16x16_bf16 v[186:189], v[194:195], v[162:163], v[186:189]\n"
      "v_exp_f32_e64 v144, v144\n"
      "v_exp_f32_e64 v145, v145\n"
      "v_mfma_f32_16x16x16_bf16 v[158:161], v[122:123], v[162:163], v[158:161]\n"
      "v_exp_f32 v146, v146\n"
      "v_exp_f32_e64 v147, v147\n"
      "v_mfma_f32_16x16x32_bf16 v[220:223], a[216:219], v[124:127], 0\n"
      "s_waitcnt lgkmcnt(0)\n"
      "v_exp_f32_e64 v182, v182\n"
      "v_mfma_f32_16x16x32_bf16 v[206:209], a[240:243], v[124:127], 0\n"
      "ds_read_u16 v78, v11 offset:1168\n"
      "v_exp_f32_e64 v183, v183\n"
      "v_mfma_f32_16x16x32_bf16 v[162:165], v[232:235], v[124:127], 0\n"
      "ds_read_u16 v82, v11 offset:1200\n"
      "v_exp_f32_e64 v184, v184\n"
      "v_mfma_f32_16x16x32_bf16 v[220:223], a[220:223], v[108:111], v[220:223]\n"
      "ds_read_u16 v86, v11 offset:1232\n"
      "v_exp_f32 v185, v185\n"
      "v_mfma_f32_16x16x32_bf16 v[206:209], a[244:247], v[108:111], v[206:209]\n"
      "ds_write_b32 v31, v190 offset:1024\n"
      "v_mfma_f32_16x16x32_bf16 v[162:165], v[236:239], v[108:111], v[162:165]\n"
      "s_or_b32 s95, s45, s46\n"
      "v_cvt_pk_bf16_f32 v144, v144, v145\n"
      "v_cvt_pk_bf16_f32 v145, v146, v147\n"
      "v_mfma_f32_16x16x32_bf16 v[220:223], a[224:227], v[128:131], v[220:223]\n"
      "v_cvt_pk_bf16_f32 v146, v182, v183\n"
      "v_cvt_pk_bf16_f32 v147, v184, v185\n"
      "v_mfma_f32_16x16x32_bf16 v[206:209], a[248:251], v[128:131], v[206:209]\n"
      "s_waitcnt lgkmcnt(3)\n"
      "v_exp_f32 v186, v186\n"
      "v_mfma_f32_16x16x32_bf16 v[162:165], v[240:243], v[128:131], v[162:165]\n"
      "s_waitcnt lgkmcnt(2)\n"
      "v_exp_f32 v187, v187\n"
      "v_mfma_f32_16x16x32_bf16 v[108:111], v[32:35], v[166:169], 0\n"
      "s_waitcnt lgkmcnt(1)\n"
      "v_exp_f32 v188, v188\n"
      "v_mfma_f32_16x16x32_bf16 v[108:111], v[32:35], v[88:91], v[108:111]\n"
      "v_exp_f32 v189, v189\n"
      "v_mfma_f32_16x16x32_bf16 v[180:183], v[104:107], v[166:169], 0\n"
      "v_exp_f32 v158, v158\n"
      "v_mfma_f32_16x16x32_bf16 v[180:183], v[76:79], v[88:91], v[180:183]\n"
      "ds_read_b128 v[124:127], v2 offset:9216\n"
      "v_cvt_pk_bf16_f32 v92, v186, v187\n"
      "v_mfma_f32_16x16x32_bf16 v[210:213], v[100:103], v[166:169], 0\n"
      "v_cvt_pk_bf16_f32 v93, v188, v189\n"
      "v_cvt_pk_bf16_f32 v94, v158, 0\n"
      "v_mfma_f32_16x16x32_bf16 v[210:213], v[80:83], v[88:91], v[210:213]\n"
      "v_rcp_f32 v108, v108\n"
      "v_mfma_f32_16x16x32_bf16 v[188:191], v[214:217], v[166:169], 0\n"
      "ds_read_b128 v[166:169], v2 offset:12288\n"
      "v_mul_f32 v180, v108, v180\n"
      "v_mfma_f32_16x16x32_bf16 v[188:191], v[84:87], v[88:91], v[188:191]\n"
      "v_mul_f32 v181, v108, v181\n"
      "v_cvt_pk_bf16_f32 v180, v180, v181\n"
      "v_mfma_f32_16x16x32_bf16 v[128:131], v[174:177], v[112:115], v[64:67]\n"
      "v_mul_f32 v182, v108, v182\n"
      "v_mul_f32_e64 v183, v108, v183\n"
      "v_mfma_f32_16x16x32_bf16 v[196:199], v[140:143], v[112:115], v[68:71]\n"
      "ds_read_b128 v[148:151], v2 offset:15360\n"
      "v_cvt_pk_bf16_f32 v181, v182, v183\n"
      "v_mfma_f32_16x16x32_bf16 v[170:173], v[96:99], v[112:115], v[72:75]\n"
      "s_waitcnt lgkmcnt(2)\n"
      "v_mul_f32 v210, v108, v210\n"
      "v_mul_f32 v211, v108, v211\n"
      "v_mfma_f32_16x16x32_bf16 v[118:121], v[202:205], v[112:115], v[28:31]\n"
      "v_mul_f32 v212, v108, v212\n"
      "v_mul_f32 v213, v108, v213\n"
      "v_mfma_f32_16x16x16_bf16 v[128:131], v[116:117], v[132:133], v[128:131]\n"
      "v_cvt_pk_bf16_f32 v182, v210, v211\n"
      "v_cvt_pk_bf16_f32 v183, v212, v213\n"
      "v_mul_f32 v188, v108, v188\n"
      "v_mfma_f32_16x16x16_bf16 v[196:199], v[152:153], v[132:133], v[196:199]\n"
      "v_mul_f32 v189, v108, v189\n"
      "v_cvt_pk_bf16_f32 v188, v188, v189\n"
      "v_mul_f32 v190, v108, v190\n"
      "v_mfma_f32_16x16x16_bf16 v[170:173], v[194:195], v[132:133], v[170:173]\n"
      "v_exp_f32 v128, v128\n"
      "v_exp_f32 v129, v129\n"
      "v_mfma_f32_16x16x16_bf16 v[118:121], v[122:123], v[132:133], v[118:121]\n"
      "v_exp_f32 v130, v130\n"
      "v_exp_f32 v131, v131\n"
      "v_mfma_f32_16x16x32_bf16 v[220:223], a[228:231], v[124:127], v[220:223]\n"
      "s_waitcnt lgkmcnt(0)\n"
      "v_exp_f32 v196, v196\n"
      "v_mfma_f32_16x16x32_bf16 v[206:209], a[252:255], v[124:127], v[206:209]\n"
      "ds_read_b128 v[210:213], v2 offset:256\n"
      "v_exp_f32_e64 v197, v197\n"
      "v_mfma_f32_16x16x32_bf16 v[162:165], v[244:247], v[124:127], v[162:165]\n"
      "ds_read_b128 v[110:113], v2 offset:3328\n"
      "v_exp_f32_e64 v198, v198\n"
      "v_mfma_f32_16x16x32_bf16 v[220:223], a[232:235], v[166:169], v[220:223]\n"
      "ds_read_b128 v[132:135], v2 offset:6400\n"
      "v_exp_f32_e64 v199, v199\n"
      "v_mfma_f32_16x16x32_bf16 v[206:209], v[224:227], v[166:169], v[206:209]\n"
      "ds_write_b128 v3, v[180:183] offset:18432\n"
      "v_mfma_f32_16x16x32_bf16 v[162:165], v[248:251], v[166:169], v[162:165]\n"
      "v_cvt_pk_bf16_f32 v128, v128, v129\n"
      "v_cvt_pk_bf16_f32 v129, v130, v131\n"
      "v_mfma_f32_16x16x32_bf16 v[220:223], a[236:239], v[148:151], v[220:223]\n"
      "s_waitcnt lgkmcnt(3)\n"
      "v_cvt_pk_bf16_f32 v130, v196, v197\n"
      "v_cvt_pk_bf16_f32 v131, v198, v199\n"
      "v_mfma_f32_16x16x32_bf16 v[206:209], v[228:231], v[148:151], v[206:209]\n"
      "s_waitcnt lgkmcnt(2)\n"
      "v_exp_f32 v170, v170\n"
      "v_mfma_f32_16x16x32_bf16 v[162:165], v[252:255], v[148:151], v[162:165]\n"
      "s_waitcnt lgkmcnt(1)\n"
      "v_exp_f32 v171, v171\n"
      "v_mfma_f32_16x16x32_bf16 v[166:169], a[216:219], v[210:213], 0\n"
      "v_exp_f32 v172, v172\n"
      "v_mfma_f32_16x16x32_bf16 v[180:183], a[240:243], v[210:213], 0\n"
      "v_exp_f32 v173, v173\n"
      "v_mfma_f32_16x16x32_bf16 v[124:127], v[232:235], v[210:213], 0\n"
      "v_exp_f32 v118, v118\n"
      "v_mfma_f32_16x16x32_bf16 v[166:169], a[220:223], v[110:113], v[166:169]\n"
      "v_cvt_pk_bf16_f32 v88, v170, v171\n"
      "v_cvt_pk_bf16_f32 v89, v172, v173\n"
      "v_mfma_f32_16x16x32_bf16 v[180:183], a[244:247], v[110:113], v[180:183]\n"
      "ds_read_b128 v[210:213], v10 offset:1168\n"
      "v_cvt_pk_bf16_f32 v90, v118, 0\n"
      "v_mfma_f32_16x16x32_bf16 v[124:127], v[236:239], v[110:113], v[124:127]\n"
      "v_mul_f32 v201, v220, v220\n"
      "v_add_f32 v200, v220, v221\n"
      "v_mfma_f32_16x16x32_bf16 v[166:169], a[224:227], v[132:135], v[166:169]\n"
      "v_fmac_f32 v201, v221, v221\n"
      "v_add_f32 v200, v200, v222\n"
      "v_mfma_f32_16x16x32_bf16 v[180:183], a[248:251], v[132:135], v[180:183]\n"
      "v_fmac_f32 v201, v222, v222\n"
      "v_add_f32 v200, v200, v223\n"
      "v_mfma_f32_16x16x32_bf16 v[124:127], v[240:243], v[132:135], v[124:127]\n"
      "ds_read_b64 v[186:187], v10 offset:1184\n"
      "v_fmac_f32 v201, v223, v223\n"
      "v_mfma_f32_16x16x32_bf16 v[110:113], v[32:35], v[144:147], 0\n"
      "v_add_f32 v200, v200, v206\n"
      "v_fmac_f32_e64 v201, v206, v206\n"
      "v_mfma_f32_16x16x32_bf16 v[110:113], v[32:35], v[92:95], v[110:113]\n"
      "s_waitcnt lgkmcnt(1)\n"
      "v_add_f32 v200, v200, v207\n"
      "v_fmac_f32 v201, v207, v207\n"
      "v_mfma_f32_16x16x32_bf16 v[132:135], v[104:107], v[144:147], 0\n"
      "v_add_f32 v200, v200, v208\n"
      "v_fmac_f32 v201, v208, v208\n"
      "v_mfma_f32_16x16x32_bf16 v[132:135], v[76:79], v[92:95], v[132:135]\n"
      "ds_read_b128 v[196:199], v2 offset:9472\n"
      "v_add_f32 v200, v200, v209\n"
      "v_mfma_f32_16x16x32_bf16 v[118:121], v[100:103], v[144:147], 0\n"
      "v_fmac_f32 v201, v209, v209\n"
      "v_add_f32 v200, v200, v162\n"
      "v_mfma_f32_16x16x32_bf16 v[118:121], v[80:83], v[92:95], v[118:121]\n"
      "s_waitcnt lgkmcnt(1)\n"
      "v_fmac_f32 v201, v162, v162\n"
      "v_add_f32_e64 v200, v200, v163\n"
      "v_mfma_f32_16x16x32_bf16 v[112:115], v[214:217], v[144:147], 0\n"
      "ds_read_b128 v[144:147], v2 offset:12544\n"
      "v_fmac_f32 v201, v163, v163\n"
      "v_mfma_f32_16x16x32_bf16 v[112:115], v[84:87], v[92:95], v[112:115]\n"
      "v_add_f32 v200, v200, v164\n"
      "v_fmac_f32 v201, v164, v164\n"
      "v_mfma_f32_16x16x32_bf16 v[156:159], v[210:213], v[174:177], v[30:33]\n"
      "v_add_f32 v200, v200, v165\n"
      "v_fmac_f32_e64 v201, v165, v165\n"
      "v_mfma_f32_16x16x32_bf16 v[174:177], v[210:213], v[140:143], v[30:33]\n"
      "ds_read_b128 v[140:143], v2 offset:15616\n"
      "v_rcp_f32 v110, v110\n"
      "v_mfma_f32_16x16x32_bf16 v[170:173], v[210:213], v[96:99], v[30:33]\n"
      "s_waitcnt lgkmcnt(2)\n"
      "v_permlane16_swap_b32_e64 v200, v201\n"
      "v_mfma_f32_16x16x32_bf16 v[148:151], v[210:213], v[202:205], v[30:33]\n"
      "v_mul_f32 v132, v110, v132\n"
      "v_mul_f32 v133, v110, v133\n"
      "v_mfma_f32_16x16x16_bf16 v[156:159], v[186:187], v[116:117], v[156:159]\n"
      "v_cvt_pk_bf16_f32 v132, v132, v133\n"
      "v_mul_f32 v134, v110, v134\n"
      "v_mul_f32 v135, v110, v135\n"
      "v_mfma_f32_16x16x16_bf16 v[174:177], v[186:187], v[152:153], v[174:177]\n"
      "v_add_f32 v200, v200, v201\n"
      "v_cvt_pk_bf16_f32 v133, v134, v135\n"
      "v_mul_f32 v118, v110, v118\n"
      "v_mfma_f32_16x16x16_bf16 v[170:173], v[186:187], v[194:195], v[170:173]\n"
      "v_mul_f32 v119, v110, v119\n"
      "v_mul_f32 v120, v110, v120\n"
      "v_mul_f32_e64 v121, v110, v121\n"
      "v_mfma_f32_16x16x16_bf16 v[148:151], v[186:187], v[122:123], v[148:151]\n"
      "v_cndmask_b32 v156, v156, v174, s[58:59]\n"
      "v_cvt_pk_bf16_f32 v134, v118, v119\n"
      "v_cvt_pk_bf16_f32 v135, v120, v121\n"
      "v_mfma_f32_16x16x32_bf16 v[166:169], a[228:231], v[196:199], v[166:169]\n"
      "s_waitcnt lgkmcnt(0)\n"
      "v_cndmask_b32 v156, v156, v170, s[60:61]\n"
      "v_mul_f32 v112, v110, v112\n"
      "v_mfma_f32_16x16x32_bf16 v[180:183], a[252:255], v[196:199], v[180:183]\n"
      "ds_write_b128 v3, v[132:135] offset:18688\n"
      "v_mfma_f32_16x16x32_bf16 v[124:127], v[244:247], v[196:199], v[124:127]\n"
      "v_cndmask_b32 v156, v156, v148, s[62:63]\n"
      "v_mul_f32 v113, v110, v113\n"
      "v_mfma_f32_16x16x32_bf16 v[166:169], a[232:235], v[144:147], v[166:169]\n"
      "v_exp_f32 v156, v156\n"
      "v_mfma_f32_16x16x32_bf16 v[180:183], v[224:227], v[144:147], v[180:183]\n"
      "ds_read_b128 v[174:177], v2 offset:512\n"
      "v_mul_f32_e64 v191, v108, v191\n"
      "v_mfma_f32_16x16x32_bf16 v[124:127], v[248:251], v[144:147], v[124:127]\n"
      "v_cvt_pk_bf16_f32 v112, v112, v113\n"
      "v_mul_f32 v114, v110, v114\n"
      "v_mfma_f32_16x16x32_bf16 v[166:169], a[236:239], v[140:143], v[166:169]\n"
      "v_mul_f32 v115, v110, v115\n"
      "v_cvt_pk_bf16_f32 v189, v190, v191\n"
      "v_mfma_f32_16x16x32_bf16 v[180:183], v[228:231], v[140:143], v[180:183]\n"
      "ds_read_b128 v[210:213], v2 offset:3584\n"
      "v_cvt_pk_bf16_f32 v113, v114, v115\n"
      "v_mfma_f32_16x16x32_bf16 v[124:127], v[252:255], v[140:143], v[124:127]\n"
      "ds_write_b64 v4, v[188:189] offset:18432\n"
      "v_mfma_f32_16x16x32_bf16 v[188:191], v[32:35], v[128:131], 0\n"
      "v_mul_f32 v115, v166, v166\n"
      "v_add_f32 v114, v166, v167\n"
      "v_mfma_f32_16x16x32_bf16 v[188:191], v[32:35], v[88:91], v[188:191]\n"
      "ds_read_b128 v[202:205], v2 offset:6656\n"
      "v_fmac_f32_e64 v115, v167, v167\n"
      "v_mfma_f32_16x16x32_bf16 v[194:197], v[104:107], v[128:131], 0\n"
      "ds_write_b64 v4, v[112:113] offset:18688\n"
      "v_mfma_f32_16x16x32_bf16 v[194:197], v[76:79], v[88:91], v[194:197]\n"
      "s_waitcnt lgkmcnt(4)\n"
      "v_add_f32 v114, v114, v168\n"
      "v_fmac_f32_e64 v115, v168, v168\n"
      "v_mfma_f32_16x16x32_bf16 v[132:135], v[100:103], v[128:131], 0\n"
      "ds_read_b128 v[190:193], v2 offset:9728\n"
      "v_add_f32 v114, v114, v169\n"
      "v_mfma_f32_16x16x32_bf16 v[132:135], v[80:83], v[88:91], v[132:135]\n"
      "s_waitcnt lgkmcnt(4)\n"
      "v_fmac_f32 v115, v169, v169\n"
      "v_add_f32 v114, v114, v180\n"
      "v_mfma_f32_16x16x32_bf16 v[184:187], v[214:217], v[128:131], 0\n"
      "v_fmac_f32 v115, v180, v180\n"
      "v_add_f32 v114, v114, v181\n"
      "v_mfma_f32_16x16x32_bf16 v[184:187], v[84:87], v[88:91], v[184:187]\n"
      "ds_read_b128 v[128:131], v2 offset:12800\n"
      "v_fmac_f32 v115, v181, v181\n"
      "v_mfma_f32_16x16x32_bf16 v[120:123], a[216:219], v[174:177], 0\n"
      "s_waitcnt lgkmcnt(3)\n"
      "v_add_f32 v114, v114, v182\n"
      "v_fmac_f32 v115, v182, v182\n"
      "v_mfma_f32_16x16x32_bf16 v[148:151], a[240:243], v[174:177], 0\n"
      "v_add_f32 v114, v114, v183\n"
      "v_fmac_f32 v115, v183, v183\n"
      "v_mfma_f32_16x16x32_bf16 v[144:147], v[232:235], v[174:177], 0\n"
      "ds_read_b128 v[174:177], v2 offset:15872\n"
      "v_add_f32_e64 v114, v114, v124\n"
      "v_mfma_f32_16x16x32_bf16 v[120:123], a[220:223], v[210:213], v[120:123]\n"
      "s_waitcnt lgkmcnt(2)\n"
      "v_fmac_f32 v115, v124, v124\n"
      "v_add_f32 v114, v114, v125\n"
      "v_mfma_f32_16x16x32_bf16 v[148:151], a[244:247], v[210:213], v[148:151]\n"
      "v_fmac_f32 v115, v125, v125\n"
      "v_add_f32 v114, v114, v126\n"
      "v_mfma_f32_16x16x32_bf16 v[144:147], v[236:239], v[210:213], v[144:147]\n"
      "s_waitcnt lgkmcnt(1)\n"
      "v_fmac_f32 v115, v126, v126\n"
      "v_add_f32 v114, v114, v127\n"
      "v_mfma_f32_16x16x32_bf16 v[120:123], a[224:227], v[202:205], v[120:123]\n"
      "v_fmac_f32 v115, v127, v127\n"
      "v_rcp_f32 v188, v188\n"
      "v_mfma_f32_16x16x32_bf16 v[148:151], a[248:251], v[202:205], v[148:151]\n"
      "v_permlane16_swap_b32 v114, v115\n"
      "v_mfma_f32_16x16x32_bf16 v[144:147], v[240:243], v[202:205], v[144:147]\n"
      "s_waitcnt lgkmcnt(0)\n"
      "v_mul_f32 v194, v188, v194\n"
      "v_mul_f32 v195, v188, v195\n"
      "v_mfma_f32_16x16x32_bf16 v[120:123], a[228:231], v[190:193], v[120:123]\n"
      "v_cvt_pk_bf16_f32 v194, v194, v195\n"
      "v_mul_f32_e64 v196, v188, v196\n"
      "v_mfma_f32_16x16x32_bf16 v[148:151], a[252:255], v[190:193], v[148:151]\n"
      "v_mul_f32 v197, v188, v197\n"
      "v_add_f32 v114, v114, v115\n"
      "v_mfma_f32_16x16x32_bf16 v[144:147], v[244:247], v[190:193], v[144:147]\n"
      "v_cvt_pk_bf16_f32 v195, v196, v197\n"
      "v_mul_f32 v132, v188, v132\n"
      "v_mfma_f32_16x16x32_bf16 v[120:123], a[232:235], v[128:131], v[120:123]\n"
      "v_mul_f32 v133, v188, v133\n"
      "v_mul_f32 v134, v188, v134\n"
      "v_mfma_f32_16x16x32_bf16 v[148:151], v[224:227], v[128:131], v[148:151]\n"
      "v_mul_f32 v135, v188, v135\n"
      "v_cvt_pk_bf16_f32 v196, v132, v133\n"
      "v_mfma_f32_16x16x32_bf16 v[144:147], v[248:251], v[128:131], v[144:147]\n"
      "v_cvt_pk_bf16_f32 v197, v134, v135\n"
      "v_mul_f32_e64 v184, v188, v184\n"
      "v_mfma_f32_16x16x32_bf16 v[120:123], a[236:239], v[174:177], v[120:123]\n"
      "ds_write_b128 v3, v[194:197] offset:18944\n"
      "v_mfma_f32_16x16x32_bf16 v[148:151], v[228:231], v[174:177], v[148:151]\n"
      "v_mul_f32_e64 v185, v188, v185\n"
      "v_cvt_pk_bf16_f32 v184, v184, v185\n"
      "v_mfma_f32_16x16x32_bf16 v[144:147], v[252:255], v[174:177], v[144:147]\n"
      "v_mul_f32 v186, v188, v186\n"
      "v_mul_f32 v187, v188, v187\n"
      "v_permlane32_swap_b32 v200, v114\n"
      "v_mul_f32 v191, v120, v120\n"
      "v_add_f32 v190, v120, v121\n"
      "v_fmac_f32 v191, v121, v121\n"
      "v_add_f32 v190, v190, v122\n"
      "v_fmac_f32 v191, v122, v122\n"
      "v_add_f32 v190, v190, v123\n"
      "v_fmac_f32 v191, v123, v123\n"
      "v_add_f32 v190, v190, v148\n"
      "v_fmac_f32 v191, v148, v148\n"
      "v_add_f32 v190, v190, v149\n"
      "v_fmac_f32 v191, v149, v149\n"
      "v_add_f32 v190, v190, v150\n"
      "v_fmac_f32 v191, v150, v150\n"
      "v_add_f32 v190, v190, v151\n"
      "v_fmac_f32 v191, v151, v151\n"
      "v_add_f32 v190, v190, v144\n"
      "v_fmac_f32 v191, v144, v144\n"
      "v_add_f32 v190, v190, v145\n"
      "v_fmac_f32 v191, v145, v145\n"
      "v_add_f32 v190, v190, v146\n"
      "v_fmac_f32 v191, v146, v146\n"
      "v_add_f32 v190, v190, v147\n"
      "v_fmac_f32 v191, v147, v147\n"
      "v_cvt_pk_bf16_f32 v185, v186, v187\n"
      "ds_write_b64 v4, v[184:185] offset:18944\n"
      "v_permlane16_swap_b32 v190, v191\n"
      "v_add_f32 v190, v190, v191\n"
      "v_mov_b32_e64 v191, v190\n"
      "v_cvt_pk_bf16_f32 v156, v156, v156\n"
      "v_add_f32_e64 v200, v200, v114\n"
      "v_permlane32_swap_b32_e64 v190, v191\n"
      "v_add_f32_e64 v190, v190, v191\n"
      "ds_write_b16 v13, v156\n"
      "ds_read_b128 v[210:213], v14\n"
      "ds_read_b128 v[174:177], v14 offset:64\n"
      "ds_write_b32 v8, v200 offset:2048\n"
      "v_cmp_ne_u32 s[56:57], 0, v178\n"
      "v_readlane_b32 s47, v38, s43\n"
      "s_cmp_ge_u32 s95, 0x40000000\n"
      "s_cselect_b64 s[56:57], -1, s[56:57]\n"
      "ds_write_b32 v8, v190 offset:2560\n"
      "s_cmp_gt_u32 s42, s44\n"
      "s_cselect_b64 s[56:57], -1, s[56:57]\n"
      "s_and_b32 s47, s47, -4\n"
      "s_or_b64 vcc, s[56:57], s[54:55]\n"
      "s_add_u32 s42, s42, 1\n"
      "s_add_u32 s43, s43, 1\n"
      "v_add_u32 v10, 2336, v10\n"
      "v_add_u32 v11, 2336, v11\n"
      "s_waitcnt lgkmcnt(3)\n"
      "v_mfma_f32_16x16x32_bf16 v[188:191], v[210:213], v[32:35], 0\n"
      "s_waitcnt lgkmcnt(2)\n"
      "v_mfma_f32_16x16x32_bf16 v[188:191], v[174:177], v[32:35], v[188:191]\n"
      "s_waitcnt lgkmcnt(0)\n"
      "s_barrier\n"
      "s_cbranch_vccnz L_disp0_%=\n"
      "L_loop0_%=:\n"
      "v_mfma_f32_16x16x32_bf16 v[202:205], v[210:213], v[104:107], 0\n"
      "ds_read_b128 v[104:107], v9 offset:2048\n"
      "ds_read_b128 v[184:187], v9 offset:3072\n"
      "v_mfma_f32_16x16x32_bf16 v[202:205], v[174:177], v[76:79], v[202:205]\n"
      "s_nop 0\n"
      "ds_read_b128 v[190:193], v0\n"
      "v_mov_b32_dpp v188, v188 quad_perm:[0,0,0,0] row_mask:0xf bank_mask:0xf\n"
      "v_mfma_f32_16x16x32_bf16 v[130:133], v[210:213], v[100:103], 0\n"
      "ds_read_b128 v[100:103], v1\n"
      "v_rcp_f32_e64 v188, v188\n"
      "v_mfma_f32_16x16x32_bf16 v[130:133], v[174:177], v[80:83], v[130:133]\n"
      "ds_read_b128 v[112:115], v0 offset:6144\n"
      "s_add_u32 m0, s64, 18432\n"
      "v_mfma_f32_16x16x32_bf16 v[194:197], v[210:213], v[214:217], 0\n"
      "buffer_load_dwordx4 v16, s[16:19], s45 offen  lds\n"
      "v_mfma_f32_16x16x32_bf16 v[194:197], v[174:177], v[84:87], v[194:197]\n"
      "s_add_u32 m0, s64, 22528\n"
      "s_waitcnt lgkmcnt(4)\n"
      "v_add_f32 v104, v104, v106\n"
      "v_cndmask_b32 v202, v202, v130, s[58:59]\n"
      "v_add_f32 v105, v105, v107\n"
      "s_waitcnt lgkmcnt(3)\n"
      "v_add_f32 v184, v184, v186\n"
      "v_add_f32_e64 v185, v185, v187\n"
      "v_cndmask_b32 v202, v202, v194, s[60:61]\n"
      "s_waitcnt lgkmcnt(2)\n"
      "v_mfma_f32_16x16x32_bf16 v[198:201], a[0:3], v[190:193], 0\n"
      "buffer_load_dwordx4 v17, s[16:19], s45 offen  lds\n"
      "v_mfma_f32_16x16x32_bf16 v[174:177], a[24:27], v[190:193], 0\n"
      "s_add_u32 m0, s64, 26624\n"
      "v_mul_f32_e64 v202, v188, v202\n"
      "v_cvt_pk_bf16_f32 v202, v202, v202\n"
      "v_mfma_f32_16x16x32_bf16 v[212:215], a[48:51], v[190:193], 0\n"
      "ds_write_b16 v12, v202 offset:16\n"
      "v_mfma_f32_16x16x32_bf16 v[130:133], a[72:75], v[190:193], 0\n"
      "s_waitcnt lgkmcnt(1)\n"
      "v_add_f32 v104, v104, v184\n"
      "v_add_f32_e64 v105, v105, v185\n"
      "v_mfma_f32_16x16x32_bf16 v[194:197], a[96:99], v[190:193], 0\n"
      "buffer_load_dwordx4 v18, s[16:19], s45 offen  lds\n"
      "v_mfma_f32_16x16x32_bf16 v[202:205], a[120:123], v[190:193], 0\n"
      "ds_read_b128 v[188:191], v1 offset:6144\n"
      "v_fma_f32 v184, -v104, v104, s52\n"
      "v_mfma_f32_16x16x32_bf16 v[198:201], a[4:7], v[100:103], v[198:201]\n"
      "ds_read_b128 v[170:173], v0 offset:12288\n"
      "v_fmac_f32 v184, 0x43400000, v105\n"
      "v_mfma_f32_16x16x32_bf16 v[198:201], a[8:11], v[112:115], v[198:201]\n"
      "ds_read_b128 v[156:159], v1 offset:12288\n"
      "v_max_f32 v184, s52, v184\n"
      "v_mfma_f32_16x16x32_bf16 v[174:177], a[28:31], v[100:103], v[174:177]\n"
      "s_add_u32 m0, s64, 30720\n"
      "v_rsq_f32 v184, v184\n"
      "v_mfma_f32_16x16x32_bf16 v[174:177], a[32:35], v[112:115], v[174:177]\n"
      "buffer_load_dwordx4 v19, s[16:19], s45 offen  lds\n"
      "v_mfma_f32_16x16x32_bf16 v[212:215], a[52:55], v[100:103], v[212:215]\n"
      "s_add_u32 m0, s65, 34816\n"
      "v_mul_f32 v210, 0x43400000, v184\n"
      "v_mul_f32 v211, -v104, v184\n"
      "v_mfma_f32_16x16x32_bf16 v[212:215], a[56:59], v[112:115], v[212:215]\n"
      "s_waitcnt lgkmcnt(2)\n"
      "v_mov_b32 v216, v210\n"
      "v_mov_b32_e64 v217, v211\n"
      "v_mfma_f32_16x16x32_bf16 v[130:133], a[76:79], v[100:103], v[130:133]\n"
      "s_waitcnt lgkmcnt(1)\n"
      "v_permlane32_swap_b32 v210, v216\n"
      "v_mfma_f32_16x16x32_bf16 v[130:133], a[80:83], v[112:115], v[130:133]\n"
      "buffer_load_dwordx4 v15, s[16:19], s45 offen  lds\n"
      "v_mfma_f32_16x16x32_bf16 v[194:197], a[100:103], v[100:103], v[194:197]\n"
      "s_waitcnt lgkmcnt(0)\n"
      "v_permlane32_swap_b32 v211, v217\n"
      "v_mfma_f32_16x16x32_bf16 v[194:197], a[104:107], v[112:115], v[194:197]\n"
      "v_mov_b32 v108, v210\n"
      "v_mov_b32 v110, v211\n"
      "v_mfma_f32_16x16x32_bf16 v[202:205], a[124:127], v[100:103], v[202:205]\n"
      "v_permlane16_swap_b32_e64 v210, v108\n"
      "v_mfma_f32_16x16x32_bf16 v[202:205], a[128:131], v[112:115], v[202:205]\n"
      "buffer_load_dwordx4 v[112:115], v27, s[24:27], s47 offen\n"
      "v_mfma_f32_16x16x32_bf16 v[198:201], a[12:15], v[188:191], v[198:201]\n"
      "v_permlane16_swap_b32_e64 v211, v110\n"
      "v_mfma_f32_16x16x32_bf16 v[198:201], a[16:19], v[170:173], v[198:201]\n"
      "v_fma_f32 v220, v220, v210, v211\n"
      "v_fma_f32 v221, v221, v210, v211\n"
      "v_mfma_f32_16x16x32_bf16 v[198:201], a[20:23], v[156:159], v[198:201]\n"
      "v_cvt_pk_bf16_f32 v220, v220, v221\n"
      "v_fma_f32 v222, v222, v210, v211\n"
      "v_mfma_f32_16x16x32_bf16 v[174:177], a[36:39], v[188:191], v[174:177]\n"
      "buffer_load_dword v82, v29, s[24:27], s47 offen\n"
      "v_mfma_f32_16x16x32_bf16 v[174:177], a[40:43], v[170:173], v[174:177]\n"
      "v_fma_f32 v223, v223, v210, v211\n"
      "v_fma_f32 v162, v162, v210, v211\n"
      "v_mfma_f32_16x16x32_bf16 v[174:177], a[44:47], v[156:159], v[174:177]\n"
      "ds_read_b128 v[184:187], v0 offset:2048\n"
      "v_fma_f32 v163, v163, v210, v211\n"
      "v_mfma_f32_16x16x32_bf16 v[212:215], a[60:63], v[188:191], v[212:215]\n"
      "ds_read_b128 v[104:107], v1 offset:2048\n"
      "v_cvt_pk_bf16_f32 v221, v222, v223\n"
      "v_mfma_f32_16x16x32_bf16 v[212:215], a[64:67], v[170:173], v[212:215]\n"
      "ds_read_b128 v[116:119], v0 offset:8192\n"
      "v_fma_f32 v206, v206, v210, v211\n"
      "v_mfma_f32_16x16x32_bf16 v[212:215], a[68:71], v[156:159], v[212:215]\n"
      "ds_read_b128 v[96:99], v1 offset:8192\n"
      "v_fma_f32 v207, v207, v210, v211\n"
      "v_mfma_f32_16x16x32_bf16 v[130:133], a[84:87], v[188:191], v[130:133]\n"
      "ds_read_b128 v[134:137], v0 offset:14336\n"
      "v_fma_f32 v208, v208, v210, v211\n"
      "v_mfma_f32_16x16x32_bf16 v[130:133], a[88:91], v[170:173], v[130:133]\n"
      "ds_read_b128 v[100:103], v1 offset:14336\n"
      "v_fma_f32 v209, v209, v210, v211\n"
      "v_mfma_f32_16x16x32_bf16 v[130:133], a[92:95], v[156:159], v[130:133]\n"
      "s_waitcnt lgkmcnt(5)\n"
      "v_cvt_pk_bf16_f32 v162, v162, v163\n"
      "v_fma_f32 v164, v164, v210, v211\n"
      "v_mfma_f32_16x16x32_bf16 v[194:197], a[108:111], v[188:191], v[194:197]\n"
      "s_waitcnt lgkmcnt(4)\n"
      "v_fma_f32 v165, v165, v210, v211\n"
      "v_cvt_pk_bf16_f32 v222, v206, v207\n"
      "v_mfma_f32_16x16x32_bf16 v[194:197], a[112:115], v[170:173], v[194:197]\n"
      "s_waitcnt lgkmcnt(3)\n"
      "v_cvt_pk_bf16_f32 v223, v208, v209\n"
      "v_cvt_pk_bf16_f32 v163, v164, v165\n"
      "v_mfma_f32_16x16x32_bf16 v[194:197], a[116:119], v[156:159], v[194:197]\n"
      "ds_write_b128 v5, v[220:223]\n"
      "v_mfma_f32_16x16x32_bf16 v[202:205], a[132:135], v[188:191], v[202:205]\n"
      "s_waitcnt lgkmcnt(1)\n"
      "v_fma_f32 v166, v166, v108, v110\n"
      "v_fma_f32 v167, v167, v108, v110\n"
      "v_mfma_f32_16x16x32_bf16 v[202:205], a[136:139], v[170:173], v[202:205]\n"
      "v_cvt_pk_bf16_f32 v166, v166, v167\n"
      "v_fma_f32 v168, v168, v108, v110\n"
      "v_mfma_f32_16x16x32_bf16 v[202:205], a[140:143], v[156:159], v[202:205]\n"
      "ds_write_b64 v6, v[162:163]\n"
      "v_mfma_f32_16x16x32_bf16 v[138:141], a[0:3], v[184:187], 0\n"
      "v_fma_f32 v169, v169, v108, v110\n"
      "v_fma_f32 v124, v124, v108, v110\n"
      "v_mfma_f32_16x16x32_bf16 v[138:141], a[4:7], v[104:107], v[138:141]\n"
      "v_fma_f32 v125, v125, v108, v110\n"
      "v_cvt_pk_bf16_f32 v167, v168, v169\n"
      "v_mfma_f32_16x16x32_bf16 v[138:141], a[8:11], v[116:119], v[138:141]\n"
      "v_fma_f32 v180, v180, v108, v110\n"
      "v_fma_f32 v181, v181, v108, v110\n"
      "v_mfma_f32_16x16x32_bf16 v[138:141], a[12:15], v[96:99], v[138:141]\n"
      "v_fma_f32 v182, v182, v108, v110\n"
      "v_fma_f32 v183, v183, v108, v110\n"
      "v_mfma_f32_16x16x32_bf16 v[138:141], a[16:19], v[134:137], v[138:141]\n"
      "v_cvt_pk_bf16_f32 v124, v124, v125\n"
      "v_fma_f32 v126, v126, v108, v110\n"
      "v_mfma_f32_16x16x32_bf16 v[138:141], a[20:23], v[100:103], v[138:141]\n"
      "v_fma_f32 v127, v127, v108, v110\n"
      "v_cvt_pk_bf16_f32 v168, v180, v181\n"
      "v_mfma_f32_16x16x32_bf16 v[208:211], a[24:27], v[184:187], 0\n"
      "v_cvt_pk_bf16_f32 v169, v182, v183\n"
      "v_cvt_pk_bf16_f32 v125, v126, v127\n"
      "v_mfma_f32_16x16x32_bf16 v[208:211], a[28:31], v[104:107], v[208:211]\n"
      "ds_write_b128 v5, v[166:169] offset:6144\n"
      "v_mfma_f32_16x16x32_bf16 v[208:211], a[32:35], v[116:119], v[208:211]\n"
      "v_fma_f32 v120, v120, v216, v217\n"
      "v_fma_f32 v121, v121, v216, v217\n"
      "v_mfma_f32_16x16x32_bf16 v[208:211], a[36:39], v[96:99], v[208:211]\n"
      "v_cvt_pk_bf16_f32 v120, v120, v121\n"
      "v_fma_f32 v122, v122, v216, v217\n"
      "v_mfma_f32_16x16x32_bf16 v[208:211], a[40:43], v[134:137], v[208:211]\n"
      "ds_write_b64 v6, v[124:125] offset:6144\n"
      "v_mfma_f32_16x16x32_bf16 v[208:211], a[44:47], v[100:103], v[208:211]\n"
      "v_fma_f32 v123, v123, v216, v217\n"
      "v_fma_f32 v144, v144, v216, v217\n"
      "v_mfma_f32_16x16x32_bf16 v[154:157], a[48:51], v[184:187], 0\n"
      "v_fma_f32 v145, v145, v216, v217\n"
      "v_cvt_pk_bf16_f32 v121, v122, v123\n"
      "v_mfma_f32_16x16x32_bf16 v[154:157], a[52:55], v[104:107], v[154:157]\n"
      "v_fma_f32 v148, v148, v216, v217\n"
      "v_fma_f32 v149, v149, v216, v217\n"
      "v_mfma_f32_16x16x32_bf16 v[154:157], a[56:59], v[116:119], v[154:157]\n"
      "v_fma_f32 v150, v150, v216, v217\n"
      "v_fma_f32 v151, v151, v216, v217\n"
      "v_mfma_f32_16x16x32_bf16 v[154:157], a[60:63], v[96:99], v[154:157]\n"
      "v_cvt_pk_bf16_f32 v144, v144, v145\n"
      "v_fma_f32 v146, v146, v216, v217\n"
      "v_mfma_f32_16x16x32_bf16 v[154:157], a[64:67], v[134:137], v[154:157]\n"
      "v_fma_f32 v147, v147, v216, v217\n"
      "v_cvt_pk_bf16_f32 v122, v148, v149\n"
      "v_mfma_f32_16x16x32_bf16 v[154:157], a[68:71], v[100:103], v[154:157]\n"
      "v_cvt_pk_bf16_f32 v123, v150, v151\n"
      "v_cvt_pk_bf16_f32 v145, v146, v147\n"
      "v_mfma_f32_16x16x32_bf16 v[170:173], a[72:75], v[184:187], 0\n"
      "ds_write_b128 v5, v[120:123] offset:12288\n"
      "v_mfma_f32_16x16x32_bf16 v[170:173], a[76:79], v[104:107], v[170:173]\n"
      "v_fma_f32 v151, v198, v198, s51\n"
      "v_fma_f32 v150, v130, v130, s51\n"
      "v_mfma_f32_16x16x32_bf16 v[170:173], a[80:83], v[116:119], v[170:173]\n"
      "v_fmac_f32 v151, v199, v199\n"
      "v_fmac_f32 v150, v131, v131\n"
      "v_mfma_f32_16x16x32_bf16 v[170:173], a[84:87], v[96:99], v[170:173]\n"
      "ds_write_b64 v6, v[144:145] offset:12288\n"
      "v_mfma_f32_16x16x32_bf16 v[170:173], a[88:91], v[134:137], v[170:173]\n"
      "v_fmac_f32 v151, v200, v200\n"
      "v_fmac_f32 v150, v132, v132\n"
      "v_mfma_f32_16x16x32_bf16 v[170:173], a[92:95], v[100:103], v[170:173]\n"
      "ds_read_b128 v[108:111], v0 offset:4096\n"
      "v_fmac_f32 v151, v201, v201\n"
      "v_mfma_f32_16x16x32_bf16 v[158:161], a[96:99], v[184:187], 0\n"
      "ds_read_b128 v[188:191], v1 offset:4096\n"
      "v_fmac_f32 v150, v133, v133\n"
      "v_mfma_f32_16x16x32_bf16 v[158:161], a[100:103], v[104:107], v[158:161]\n"
      "ds_read_b128 v[180:183], v0 offset:10240\n"
      "v_fmac_f32_e64 v151, v174, v174\n"
      "v_mfma_f32_16x16x32_bf16 v[158:161], a[104:107], v[116:119], v[158:161]\n"
      "ds_read_b128 v[216:219], v1 offset:10240\n"
      "v_fmac_f32 v150, v194, v194\n"
      "v_mfma_f32_16x16x32_bf16 v[158:161], a[108:111], v[96:99], v[158:161]\n"
      "ds_read_b128 v[124:127], v0 offset:16384\n"
      "v_fmac_f32 v151, v175, v175\n"
      "v_mfma_f32_16x16x32_bf16 v[158:161], a[112:115], v[134:137], v[158:161]\n"
      "ds_read_b128 v[220:223], v1 offset:16384\n"
      "v_fmac_f32 v150, v195, v195\n"
      "v_mfma_f32_16x16x32_bf16 v[158:161], a[116:119], v[100:103], v[158:161]\n"
      "s_waitcnt lgkmcnt(5)\n"
      "v_fmac_f32 v151, v176, v176\n"
      "v_fmac_f32 v150, v196, v196\n"
      "v_mfma_f32_16x16x32_bf16 v[146:149], a[120:123], v[184:187], 0\n"
      "s_waitcnt lgkmcnt(4)\n"
      "v_fmac_f32 v151, v177, v177\n"
      "v_fmac_f32_e64 v150, v197, v197\n"
      "v_mfma_f32_16x16x32_bf16 v[146:149], a[124:127], v[104:107], v[146:149]\n"
      "s_waitcnt lgkmcnt(3)\n"
      "v_fmac_f32 v151, v212, v212\n"
      "v_fmac_f32 v150, v202, v202\n"
      "v_mfma_f32_16x16x32_bf16 v[146:149], a[128:131], v[116:119], v[146:149]\n"
      "s_waitcnt lgkmcnt(2)\n"
      "v_fmac_f32 v151, v213, v213\n"
      "v_fmac_f32 v150, v203, v203\n"
      "v_mfma_f32_16x16x32_bf16 v[146:149], a[132:135], v[96:99], v[146:149]\n"
      "s_waitcnt lgkmcnt(1)\n"
      "v_fmac_f32 v151, v214, v214\n"
      "v_fmac_f32_e64 v150, v204, v204\n"
      "v_mfma_f32_16x16x32_bf16 v[146:149], a[136:139], v[134:137], v[146:149]\n"
      "s_waitcnt lgkmcnt(0)\n"
      "v_fmac_f32 v151, v215, v215\n"
      "v_fmac_f32_e64 v150, v205, v205\n"
      "v_mfma_f32_16x16x32_bf16 v[146:149], a[140:143], v[100:103], v[146:149]\n"
      "ds_read_b128 v[134:137], v9 offset:5632\n"
      "v_permlane16_swap_b32 v151, v150\n"
      "v_mfma_f32_16x16x32_bf16 v[120:123], a[0:3], v[108:111], 0\n"
      "v_add_f32 v151, v151, v150\n"
      "v_mov_b32_e64 v150, v151\n"
      "v_mfma_f32_16x16x32_bf16 v[120:123], a[4:7], v[188:191], v[120:123]\n"
      "v_fma_f32 v76, v138, v138, s51\n"
      "v_permlane32_swap_b32 v151, v150\n"
      "v_mfma_f32_16x16x32_bf16 v[120:123], a[8:11], v[180:183], v[120:123]\n"
      "v_add_f32 v151, v151, v150\n"
      "v_rsq_f32 v151, v151\n"
      "v_mfma_f32_16x16x32_bf16 v[120:123], a[12:15], v[216:219], v[120:123]\n"
      "v_mov_b32 v150, v151\n"
      "v_fma_f32 v77, v170, v170, s51\n"
      "v_mfma_f32_16x16x32_bf16 v[120:123], a[16:19], v[124:127], v[120:123]\n"
      "v_permlane16_swap_b32 v151, v150\n"
      "v_mfma_f32_16x16x32_bf16 v[120:123], a[20:23], v[220:223], v[120:123]\n"
      "v_mul_f32 v151, s50, v151\n"
      "v_mul_f32 v130, v150, v130\n"
      "v_mfma_f32_16x16x32_bf16 v[162:165], a[24:27], v[108:111], 0\n"
      "v_mul_f32 v131, v150, v131\n"
      "v_mul_f32 v198, v151, v198\n"
      "v_mfma_f32_16x16x32_bf16 v[162:165], a[28:31], v[188:191], v[162:165]\n"
      "v_mul_f32 v199, v151, v199\n"
      "v_cvt_pk_bf16_f32 v130, v130, v131\n"
      "v_mfma_f32_16x16x32_bf16 v[162:165], a[32:35], v[180:183], v[162:165]\n"
      "v_mul_f32 v132, v150, v132\n"
      "v_mul_f32 v133, v150, v133\n"
      "v_mfma_f32_16x16x32_bf16 v[162:165], a[36:39], v[216:219], v[162:165]\n"
      "v_mul_f32 v202, v150, v202\n"
      "v_mul_f32 v203, v150, v203\n"
      "v_mfma_f32_16x16x32_bf16 v[162:165], a[40:43], v[124:127], v[162:165]\n"
      "v_cvt_pk_bf16_f32 v198, v198, v199\n"
      "v_mul_f32 v200, v151, v200\n"
      "v_mfma_f32_16x16x32_bf16 v[162:165], a[44:47], v[220:223], v[162:165]\n"
      "v_mul_f32 v201, v151, v201\n"
      "v_mul_f32_e64 v212, v151, v212\n"
      "v_mfma_f32_16x16x32_bf16 v[142:145], a[48:51], v[108:111], 0\n"
      "v_mul_f32 v213, v151, v213\n"
      "v_cvt_pk_bf16_f32 v131, v132, v133\n"
      "v_mfma_f32_16x16x32_bf16 v[142:145], a[52:55], v[188:191], v[142:145]\n"
      "v_mul_f32 v194, v150, v194\n"
      "v_mul_f32 v195, v150, v195\n"
      "v_mfma_f32_16x16x32_bf16 v[142:145], a[56:59], v[180:183], v[142:145]\n"
      "v_mul_f32 v196, v150, v196\n"
      "v_mul_f32_e64 v197, v150, v197\n"
      "v_mfma_f32_16x16x32_bf16 v[142:145], a[60:63], v[216:219], v[142:145]\n"
      "ds_read_b128 v[116:119], v7 offset:18432\n"
      "v_cvt_pk_bf16_f32 v202, v202, v203\n"
      "v_mfma_f32_16x16x32_bf16 v[142:145], a[64:67], v[124:127], v[142:145]\n"
      "ds_read_b128 v[166:169], v7 offset:22528\n"
      "v_mul_f32 v204, v150, v204\n"
      "v_mfma_f32_16x16x32_bf16 v[142:145], a[68:71], v[220:223], v[142:145]\n"
      "v_mul_f32 v205, v150, v205\n"
      "v_cvt_pk_bf16_f32 v199, v200, v201\n"
      "v_mfma_f32_16x16x32_bf16 v[184:187], a[72:75], v[108:111], 0\n"
      "ds_read_b128 v[104:107], v7 offset:26624\n"
      "v_mul_f32 v174, v151, v174\n"
      "v_mfma_f32_16x16x32_bf16 v[184:187], a[76:79], v[188:191], v[184:187]\n"
      "ds_read_b128 v[100:103], v7 offset:30720\n"
      "v_mul_f32 v175, v151, v175\n"
      "v_mfma_f32_16x16x32_bf16 v[184:187], a[80:83], v[180:183], v[184:187]\n"
      "v_mul_f32 v176, v151, v176\n"
      "v_mul_f32 v177, v151, v177\n"
      "v_mfma_f32_16x16x32_bf16 v[184:187], a[84:87], v[216:219], v[184:187]\n"
      "ds_read_b128 v[96:99], v7 offset:34816\n"
      "v_cvt_pk_bf16_f32 v212, v212, v213\n"
      "v_mfma_f32_16x16x32_bf16 v[184:187], a[88:91], v[124:127], v[184:187]\n"
      "v_mul_f32 v214, v151, v214\n"
      "v_mul_f32 v215, v151, v215\n"
      "v_mfma_f32_16x16x32_bf16 v[184:187], a[92:95], v[220:223], v[184:187]\n"
      "v_cvt_pk_bf16_f32 v132, v194, v195\n"
      "v_cvt_pk_bf16_f32 v133, v196, v197\n"
      "v_mfma_f32_16x16x32_bf16 v[150:153], a[96:99], v[108:111], 0\n"
      "v_cvt_pk_bf16_f32 v203, v204, v205\n"
      "v_cvt_pk_bf16_f32 v200, v174, v175\n"
      "v_mfma_f32_16x16x32_bf16 v[150:153], a[100:103], v[188:191], v[150:153]\n"
      "s_waitcnt lgkmcnt(1)\n"
      "v_cvt_pk_bf16_f32 v201, v176, v177\n"
      "v_cvt_pk_bf16_f32 v213, v214, v215\n"
      "v_mfma_f32_16x16x32_bf16 v[150:153], a[104:107], v[180:183], v[150:153]\n"
      "buffer_store_dwordx4 v[116:119], v20, s[20:23], s46 offen sc1\n"
      "v_mfma_f32_16x16x32_bf16 v[150:153], a[108:111], v[216:219], v[150:153]\n"
      "s_waitcnt lgkmcnt(0)\n"
      "v_fmac_f32 v76, v139, v139\n"
      "v_fmac_f32 v77, v171, v171\n"
      "v_mfma_f32_16x16x32_bf16 v[150:153], a[112:115], v[124:127], v[150:153]\n"
      "v_fmac_f32 v76, v140, v140\n"
      "v_fmac_f32 v77, v172, v172\n"
      "v_mfma_f32_16x16x32_bf16 v[150:153], a[116:119], v[220:223], v[150:153]\n"
      "v_fmac_f32 v76, v141, v141\n"
      "v_fmac_f32 v77, v173, v173\n"
      "v_mfma_f32_16x16x32_bf16 v[204:207], a[120:123], v[108:111], 0\n"
      "v_fmac_f32 v76, v208, v208\n"
      "v_fmac_f32 v77, v158, v158\n"
      "v_mfma_f32_16x16x32_bf16 v[204:207], a[124:127], v[188:191], v[204:207]\n"
      "v_fmac_f32 v76, v209, v209\n"
      "v_fmac_f32 v77, v159, v159\n"
      "v_mfma_f32_16x16x32_bf16 v[204:207], a[128:131], v[180:183], v[204:207]\n"
      "v_fmac_f32 v76, v210, v210\n"
      "v_fmac_f32 v77, v160, v160\n"
      "v_mfma_f32_16x16x32_bf16 v[204:207], a[132:135], v[216:219], v[204:207]\n"
      "v_fmac_f32 v76, v211, v211\n"
      "v_fmac_f32 v77, v161, v161\n"
      "v_mfma_f32_16x16x32_bf16 v[204:207], a[136:139], v[124:127], v[204:207]\n"
      "buffer_store_dwordx4 v[166:169], v21, s[20:23], s46 offen sc1\n"
      "v_mfma_f32_16x16x32_bf16 v[204:207], a[140:143], v[220:223], v[204:207]\n"
      "v_fmac_f32 v76, v154, v154\n"
      "v_fmac_f32 v77, v146, v146\n"
      "v_mfma_f32_16x16x32_bf16 v[166:169], v[108:111], a[144:147], 0\n"
      "v_fmac_f32 v76, v155, v155\n"
      "v_fmac_f32 v77, v147, v147\n"
      "v_mfma_f32_16x16x32_bf16 v[166:169], v[188:191], a[148:151], v[166:169]\n"
      "v_fmac_f32 v76, v156, v156\n"
      "v_fmac_f32 v77, v148, v148\n"
      "v_mfma_f32_16x16x32_bf16 v[194:197], v[108:111], a[168:171], 0\n"
      "v_fmac_f32 v76, v157, v157\n"
      "v_fmac_f32 v77, v149, v149\n"
      "v_mfma_f32_16x16x32_bf16 v[194:197], v[188:191], a[172:175], v[194:197]\n"
      "v_fma_f32 v193, v120, v120, s51\n"
      "v_permlane16_swap_b32 v76, v77\n"
      "v_mfma_f32_16x16x32_bf16 v[116:119], v[108:111], a[192:195], 0\n"
      "v_add_f32 v76, v76, v77\n"
      "v_mov_b32_e64 v77, v76\n"
      "v_mfma_f32_16x16x32_bf16 v[116:119], v[188:191], a[196:199], v[116:119]\n"
      "ds_read_b128 v[188:191], v0 offset:2048\n"
      "v_permlane32_swap_b32_e64 v76, v77\n"
      "v_mfma_f32_16x16x32_bf16 v[166:169], v[180:183], a[152:155], v[166:169]\n"
      "buffer_store_dwordx4 v[104:107], v22, s[20:23], s46 offen sc1\n"
      "v_mfma_f32_16x16x32_bf16 v[166:169], v[216:219], a[156:159], v[166:169]\n"
      "ds_read_b128 v[174:177], v1 offset:2048\n"
      "v_add_f32_e64 v76, v76, v77\n"
      "v_mfma_f32_16x16x32_bf16 v[194:197], v[180:183], a[176:179], v[194:197]\n"
      "v_rsq_f32 v76, v76\n"
      "v_mfma_f32_16x16x32_bf16 v[194:197], v[216:219], a[180:183], v[194:197]\n"
      "v_mov_b32 v77, v76\n"
      "v_fma_f32 v192, v184, v184, s51\n"
      "v_mfma_f32_16x16x32_bf16 v[116:119], v[180:183], a[200:203], v[116:119]\n"
      "s_waitcnt lgkmcnt(1)\n"
      "v_permlane16_swap_b32 v76, v77\n"
      "v_mfma_f32_16x16x32_bf16 v[116:119], v[216:219], a[204:207], v[116:119]\n"
      "ds_read_b128 v[108:111], v0 offset:8192\n"
      "v_mul_f32 v76, s50, v76\n"
      "v_mfma_f32_16x16x32_bf16 v[166:169], v[124:127], a[160:163], v[166:169]\n"
      "ds_read_b128 v[178:181], v1 offset:8192\n"
      "v_mul_f32 v170, v77, v170\n"
      "v_mfma_f32_16x16x32_bf16 v[166:169], v[220:223], a[164:167], v[166:169]\n"
      "s_waitcnt lgkmcnt(2)\n"
      "v_mul_f32 v171, v77, v171\n"
      "v_mul_f32_e64 v138, v76, v138\n"
      "v_mfma_f32_16x16x32_bf16 v[194:197], v[124:127], a[184:187], v[194:197]\n"
      "buffer_store_dwordx4 v[100:103], v23, s[20:23], s46 offen sc1\n"
      "v_mfma_f32_16x16x32_bf16 v[194:197], v[220:223], a[188:191], v[194:197]\n"
      "v_mul_f32 v139, v76, v139\n"
      "v_cvt_pk_bf16_f32 v170, v170, v171\n"
      "v_mfma_f32_16x16x32_bf16 v[116:119], v[124:127], a[208:211], v[116:119]\n"
      "s_waitcnt lgkmcnt(1)\n"
      "v_mul_f32 v172, v77, v172\n"
      "v_mul_f32 v173, v77, v173\n"
      "v_mfma_f32_16x16x32_bf16 v[116:119], v[220:223], a[212:215], v[116:119]\n"
      "ds_read_b128 v[218:221], v0 offset:14336\n"
      "v_mul_f32_e64 v146, v77, v146\n"
      "v_mfma_f32_16x16x32_bf16 v[104:107], v[188:191], a[144:147], 0\n"
      "ds_read_b128 v[124:127], v1 offset:14336\n"
      "v_mul_f32 v147, v77, v147\n"
      "v_mfma_f32_16x16x32_bf16 v[104:107], v[174:177], a[148:151], v[104:107]\n"
      "s_waitcnt lgkmcnt(2)\n"
      "v_cvt_pk_bf16_f32 v138, v138, v139\n"
      "v_mul_f32 v140, v76, v140\n"
      "v_mfma_f32_16x16x32_bf16 v[100:103], v[188:191], a[168:171], 0\n"
      "v_mul_f32 v141, v76, v141\n"
      "v_mul_f32_e64 v154, v76, v154\n"
      "v_mfma_f32_16x16x32_bf16 v[100:103], v[174:177], a[172:175], v[100:103]\n"
      "v_mul_f32_e64 v155, v76, v155\n"
      "v_cvt_pk_bf16_f32 v171, v172, v173\n"
      "v_mfma_f32_16x16x32_bf16 v[214:217], v[188:191], a[192:195], 0\n"
      "buffer_store_dwordx4 v[96:99], v25, s[20:23], s46 offen sc1\n"
      "v_mfma_f32_16x16x32_bf16 v[214:217], v[174:177], a[196:199], v[214:217]\n"
      "s_waitcnt lgkmcnt(0)\n"
      "v_mul_f32 v158, v77, v158\n"
      "v_mul_f32_e64 v159, v77, v159\n"
      "v_mfma_f32_16x16x32_bf16 v[104:107], v[108:111], a[152:155], v[104:107]\n"
      "v_mul_f32 v160, v77, v160\n"
      "v_mul_f32 v161, v77, v161\n"
      "v_mfma_f32_16x16x32_bf16 v[104:107], v[178:181], a[156:159], v[104:107]\n"
      "v_cvt_pk_bf16_f32 v146, v146, v147\n"
      "v_mul_f32 v148, v77, v148\n"
      "v_mfma_f32_16x16x32_bf16 v[100:103], v[108:111], a[176:179], v[100:103]\n"
      "ds_read_b128 v[174:177], v10 offset:32\n"
      "v_mul_f32 v149, v77, v149\n"
      "v_mfma_f32_16x16x32_bf16 v[100:103], v[178:181], a[180:183], v[100:103]\n"
      "v_cvt_pk_bf16_f32 v139, v140, v141\n"
      "v_mul_f32 v208, v76, v208\n"
      "v_mfma_f32_16x16x32_bf16 v[214:217], v[108:111], a[200:203], v[214:217]\n"
      "v_mul_f32 v209, v76, v209\n"
      "v_mul_f32_e64 v210, v76, v210\n"
      "v_mfma_f32_16x16x32_bf16 v[214:217], v[178:181], a[204:207], v[214:217]\n"
      "v_mul_f32_e64 v211, v76, v211\n"
      "v_cvt_pk_bf16_f32 v154, v154, v155\n"
      "v_mfma_f32_16x16x32_bf16 v[104:107], v[218:221], a[160:163], v[104:107]\n"
      "ds_read_b64 v[178:179], v10 offset:24\n"
      "v_mul_f32 v156, v76, v156\n"
      "v_mfma_f32_16x16x32_bf16 v[104:107], v[124:127], a[164:167], v[104:107]\n"
      "ds_read_b128 v[180:183], v0\n"
      "v_mul_f32 v157, v76, v157\n"
      "v_mfma_f32_16x16x32_bf16 v[100:103], v[218:221], a[184:187], v[100:103]\n"
      "ds_read_b128 v[108:111], v1\n"
      "v_cvt_pk_bf16_f32 v172, v158, v159\n"
      "v_mfma_f32_16x16x32_bf16 v[100:103], v[124:127], a[188:191], v[100:103]\n"
      "s_waitcnt lgkmcnt(3)\n"
      "v_cvt_pk_bf16_f32 v173, v160, v161\n"
      "v_cvt_pk_bf16_f32 v147, v148, v149\n"
      "v_mfma_f32_16x16x32_bf16 v[214:217], v[218:221], a[208:211], v[214:217]\n"
      "v_cvt_pk_bf16_f32 v140, v208, v209\n"
      "v_cvt_pk_bf16_f32 v141, v210, v211\n"
      "v_mfma_f32_16x16x32_bf16 v[214:217], v[124:127], a[212:215], v[214:217]\n"
      "v_cvt_pk_bf16_f32 v155, v156, v157\n"
      "v_fmac_f32 v193, v121, v121\n"
      "v_mfma_f32_16x16x32_bf16 v[124:127], v[130:133], v[198:201], v[40:43]\n"
      "s_waitcnt lgkmcnt(2)\n"
      "v_fmac_f32 v192, v185, v185\n"
      "v_fmac_f32_e64 v193, v122, v122\n"
      "v_mfma_f32_16x16x32_bf16 v[218:221], v[170:173], v[198:201], v[44:47]\n"
      "ds_read_b128 v[208:211], v0 offset:6144\n"
      "v_fmac_f32 v192, v186, v186\n"
      "v_fmac_f32 v193, v123, v123\n"
      "v_fmac_f32 v192, v187, v187\n"
      "v_fmac_f32 v193, v162, v162\n"
      "v_fmac_f32 v192, v150, v150\n"
      "v_fmac_f32 v193, v163, v163\n"
      "v_fmac_f32 v192, v151, v151\n"
      "v_fmac_f32 v193, v164, v164\n"
      "v_fmac_f32 v192, v152, v152\n"
      "v_fmac_f32 v193, v165, v165\n"
      "v_fmac_f32 v192, v153, v153\n"
      "v_fmac_f32 v193, v142, v142\n"
      "v_fmac_f32 v192, v204, v204\n"
      "v_fmac_f32 v193, v143, v143\n"
      "v_fmac_f32 v192, v205, v205\n"
      "v_fmac_f32 v193, v144, v144\n"
      "v_fmac_f32 v192, v206, v206\n"
      "v_fmac_f32 v193, v145, v145\n"
      "v_fmac_f32_e64 v192, v207, v207\n"
      "v_cvt_pk_bf16_f32 v107, v106, v107\n"
      "v_cvt_pk_bf16_f32 v106, v104, v105\n"
      "v_permlane16_swap_b32 v193, v192\n"
      "v_add_f32 v193, v193, v192\n"
      "v_mov_b32_e64 v192, v193\n"
      "v_cvt_pk_bf16_f32 v76, v166, v167\n"
      "v_cvt_pk_bf16_f32 v77, v168, v169\n"
      "v_permlane32_swap_b32 v193, v192\n"
      "v_add_f32 v193, v193, v192\n"
      "v_rsq_f32 v193, v193\n"
      "v_cvt_pk_bf16_f32 v103, v102, v103\n"
      "v_mov_b32 v192, v193\n"
      "v_cvt_pk_bf16_f32 v102, v100, v101\n"
      "v_cvt_pk_bf16_f32 v80, v194, v195\n"
      "v_permlane16_swap_b32 v193, v192\n"
      "v_mul_f32 v184, v192, v184\n"
      "v_mul_f32_e64 v185, v192, v185\n"
      "v_cvt_pk_bf16_f32 v184, v184, v185\n"
      "v_mul_f32 v186, v192, v186\n"
      "v_mul_f32 v187, v192, v187\n"
      "v_cvt_pk_bf16_f32 v185, v186, v187\n"
      "v_mul_f32 v150, v192, v150\n"
      "v_mul_f32 v151, v192, v151\n"
      "v_mul_f32 v152, v192, v152\n"
      "v_mul_f32 v153, v192, v153\n"
      "v_cvt_pk_bf16_f32 v186, v150, v151\n"
      "v_cvt_pk_bf16_f32 v187, v152, v153\n"
      "v_mul_f32 v193, s50, v193\n"
      "v_mul_f32 v120, v193, v120\n"
      "v_mfma_f32_16x16x32_bf16 v[166:169], v[184:187], v[198:201], v[48:51]\n"
      "ds_read_b128 v[188:191], v1 offset:6144\n"
      "v_mul_f32 v121, v193, v121\n"
      "v_mfma_f32_16x16x32_bf16 v[98:101], v[174:177], v[198:201], v[24:27]\n"
      "s_waitcnt lgkmcnt(1)\n"
      "v_mul_f32 v204, v192, v204\n"
      "v_mul_f32 v205, v192, v205\n"
      "v_mfma_f32_16x16x16_bf16 v[124:127], v[202:203], v[212:213], v[124:127]\n"
      "v_cvt_pk_bf16_f32 v120, v120, v121\n"
      "v_mul_f32 v122, v193, v122\n"
      "v_mul_f32 v123, v193, v123\n"
      "v_mfma_f32_16x16x16_bf16 v[218:221], v[146:147], v[212:213], v[218:221]\n"
      "v_mul_f32 v142, v193, v142\n"
      "v_mul_f32 v143, v193, v143\n"
      "v_cvt_pk_bf16_f32 v204, v204, v205\n"
      "v_mul_f32 v206, v192, v206\n"
      "v_mul_f32 v207, v192, v207\n"
      "v_cvt_pk_bf16_f32 v121, v122, v123\n"
      "v_mul_f32 v162, v193, v162\n"
      "v_mul_f32 v163, v193, v163\n"
      "v_mul_f32 v164, v193, v164\n"
      "v_mul_f32 v165, v193, v165\n"
      "v_cvt_pk_bf16_f32 v142, v142, v143\n"
      "v_mul_f32 v144, v193, v144\n"
      "v_mul_f32 v145, v193, v145\n"
      "v_cvt_pk_bf16_f32 v205, v206, v207\n"
      "v_cvt_pk_bf16_f32 v122, v162, v163\n"
      "v_cvt_pk_bf16_f32 v123, v164, v165\n"
      "v_mfma_f32_16x16x16_bf16 v[166:169], v[204:205], v[212:213], v[166:169]\n"
      "v_cvt_pk_bf16_f32 v143, v144, v145\n"
      "v_exp_f32 v124, v124\n"
      "v_mfma_f32_16x16x16_bf16 v[98:101], v[178:179], v[212:213], v[98:101]\n"
      "v_exp_f32 v125, v125\n"
      "ds_read_b128 v[162:165], v0 offset:12288\n"
      "v_mfma_f32_16x16x32_bf16 v[192:195], v[180:183], a[144:147], 0\n"
      "ds_read_b128 v[150:153], v1 offset:12288\n"
      "v_exp_f32_e64 v126, v126\n"
      "v_mfma_f32_16x16x32_bf16 v[192:195], v[108:111], a[148:151], v[192:195]\n"
      "s_waitcnt lgkmcnt(2)\n"
      "v_exp_f32 v127, v127\n"
      "v_mfma_f32_16x16x32_bf16 v[156:159], v[180:183], a[168:171], 0\n"
      "v_exp_f32_e64 v218, v218\n"
      "v_mfma_f32_16x16x32_bf16 v[156:159], v[108:111], a[172:175], v[156:159]\n"
      "v_exp_f32 v219, v219\n"
      "v_mfma_f32_16x16x32_bf16 v[198:201], v[180:183], a[192:195], 0\n"
      "v_exp_f32 v220, v220\n"
      "v_mfma_f32_16x16x32_bf16 v[198:201], v[108:111], a[196:199], v[198:201]\n"
      "s_waitcnt lgkmcnt(1)\n"
      "v_exp_f32_e64 v221, v221\n"
      "v_mfma_f32_16x16x32_bf16 v[192:195], v[208:211], a[152:155], v[192:195]\n"
      "s_waitcnt lgkmcnt(0)\n"
      "v_cvt_pk_bf16_f32 v124, v124, v125\n"
      "v_cvt_pk_bf16_f32 v125, v126, v127\n"
      "v_mfma_f32_16x16x32_bf16 v[192:195], v[188:191], a[156:159], v[192:195]\n"
      "v_cvt_pk_bf16_f32 v126, v218, v219\n"
      "v_cvt_pk_bf16_f32 v127, v220, v221\n"
      "v_mfma_f32_16x16x32_bf16 v[156:159], v[208:211], a[176:179], v[156:159]\n"
      "v_exp_f32 v166, v166\n"
      "v_mfma_f32_16x16x32_bf16 v[156:159], v[188:191], a[180:183], v[156:159]\n"
      "v_exp_f32 v167, v167\n"
      "v_mfma_f32_16x16x32_bf16 v[198:201], v[208:211], a[200:203], v[198:201]\n"
      "v_exp_f32 v168, v168\n"
      "v_mfma_f32_16x16x32_bf16 v[198:201], v[188:191], a[204:207], v[198:201]\n"
      "v_exp_f32 v169, v169\n"
      "v_mfma_f32_16x16x32_bf16 v[192:195], v[162:165], a[160:163], v[192:195]\n"
      "v_exp_f32_e64 v98, v98\n"
      "v_mfma_f32_16x16x32_bf16 v[192:195], v[150:153], a[164:167], v[192:195]\n"
      "ds_read_b128 v[180:183], v2 offset:18432\n"
      "v_cvt_pk_bf16_f32 v88, v166, v167\n"
      "v_mfma_f32_16x16x32_bf16 v[156:159], v[162:165], a[184:187], v[156:159]\n"
      "v_cvt_pk_bf16_f32 v89, v168, v169\n"
      "v_cvt_pk_bf16_f32 v90, v98, 0\n"
      "v_mfma_f32_16x16x32_bf16 v[156:159], v[150:153], a[188:191], v[156:159]\n"
      "v_cvt_pk_bf16_f32 v81, v196, v197\n"
      "v_cvt_pk_bf16_f32 v217, v216, v217\n"
      "v_mfma_f32_16x16x32_bf16 v[198:201], v[162:165], a[208:211], v[198:201]\n"
      "ds_read_b128 v[166:169], v2 offset:21504\n"
      "v_cvt_pk_bf16_f32 v104, v192, v193\n"
      "v_mfma_f32_16x16x32_bf16 v[198:201], v[150:153], a[212:215], v[198:201]\n"
      "v_cvt_pk_bf16_f32 v105, v194, v195\n"
      "v_cvt_pk_bf16_f32 v216, v214, v215\n"
      "v_mfma_f32_16x16x32_bf16 v[108:111], v[130:133], v[138:141], v[52:55]\n"
      "v_cvt_pk_bf16_f32 v100, v156, v157\n"
      "v_cvt_pk_bf16_f32 v101, v158, v159\n"
      "v_mfma_f32_16x16x32_bf16 v[210:213], v[170:173], v[138:141], v[56:59]\n"
      "ds_read_b128 v[188:191], v2 offset:24576\n"
      "v_cvt_pk_bf16_f32 v84, v116, v117\n"
      "v_mfma_f32_16x16x32_bf16 v[194:197], v[184:187], v[138:141], v[60:63]\n"
      "s_waitcnt lgkmcnt(2)\n"
      "v_cvt_pk_bf16_f32 v214, v198, v199\n"
      "v_cvt_pk_bf16_f32 v215, v200, v201\n"
      "v_mfma_f32_16x16x32_bf16 v[160:163], v[174:177], v[138:141], v[26:29]\n"
      "v_cvt_pk_bf16_f32 v85, v118, v119\n"
      "s_waitcnt vmcnt(6)\n"
      "v_or3_b32 v112, v112, v113, v114\n"
      "v_mfma_f32_16x16x16_bf16 v[108:111], v[202:203], v[154:155], v[108:111]\n"
      "s_waitcnt vmcnt(5)\n"
      "v_or3_b32 v112, v112, v115, v82\n"
      "v_and_b32 v112, 0x7fff7fff, v112\n"
      "v_or3_b32 v134, v134, v135, v136\n"
      "v_mfma_f32_16x16x16_bf16 v[210:213], v[146:147], v[154:155], v[210:213]\n"
      "v_readlane_b32 s45, v37, s43\n"
      "v_readlane_b32 s46, v37, s42\n"
      "v_or_b32 v134, v134, v137\n"
      "v_mfma_f32_16x16x16_bf16 v[194:197], v[204:205], v[154:155], v[194:197]\n"
      "v_exp_f32_e64 v108, v108\n"
      "v_exp_f32_e64 v109, v109\n"
      "v_mfma_f32_16x16x16_bf16 v[160:163], v[178:179], v[154:155], v[160:163]\n"
      "v_exp_f32 v110, v110\n"
      "v_exp_f32_e64 v111, v111\n"
      "v_mfma_f32_16x16x32_bf16 v[220:223], a[216:219], v[180:183], 0\n"
      "s_waitcnt lgkmcnt(0)\n"
      "v_exp_f32_e64 v210, v210\n"
      "v_mfma_f32_16x16x32_bf16 v[206:209], a[240:243], v[180:183], 0\n"
      "ds_read_u16 v78, v11\n"
      "v_exp_f32_e64 v211, v211\n"
      "v_mfma_f32_16x16x32_bf16 v[162:165], v[232:235], v[180:183], 0\n"
      "ds_read_u16 v82, v11 offset:32\n"
      "v_exp_f32_e64 v212, v212\n"
      "v_mfma_f32_16x16x32_bf16 v[220:223], a[220:223], v[166:169], v[220:223]\n"
      "ds_read_u16 v86, v11 offset:64\n"
      "v_exp_f32 v213, v213\n"
      "v_mfma_f32_16x16x32_bf16 v[206:209], a[244:247], v[166:169], v[206:209]\n"
      "ds_write_b32 v31, v112\n"
      "v_mfma_f32_16x16x32_bf16 v[162:165], v[236:239], v[166:169], v[162:165]\n"
      "s_or_b32 s95, s45, s46\n"
      "v_cvt_pk_bf16_f32 v108, v108, v109\n"
      "v_cvt_pk_bf16_f32 v109, v110, v111\n"
      "v_mfma_f32_16x16x32_bf16 v[220:223], a[224:227], v[188:191], v[220:223]\n"
      "v_cvt_pk_bf16_f32 v110, v210, v211\n"
      "v_cvt_pk_bf16_f32 v111, v212, v213\n"
      "v_mfma_f32_16x16x32_bf16 v[206:209], a[248:251], v[188:191], v[206:209]\n"
      "s_waitcnt lgkmcnt(3)\n"
      "v_exp_f32 v194, v194\n"
      "v_mfma_f32_16x16x32_bf16 v[162:165], v[240:243], v[188:191], v[162:165]\n"
      "s_waitcnt lgkmcnt(2)\n"
      "v_exp_f32 v195, v195\n"
      "v_mfma_f32_16x16x32_bf16 v[210:213], v[32:35], v[124:127], 0\n"
      "s_waitcnt lgkmcnt(1)\n"
      "v_exp_f32 v196, v196\n"
      "v_mfma_f32_16x16x32_bf16 v[210:213], v[32:35], v[88:91], v[210:213]\n"
      "v_exp_f32 v197, v197\n"
      "v_mfma_f32_16x16x32_bf16 v[166:169], v[104:107], v[124:127], 0\n"
      "v_exp_f32 v160, v160\n"
      "v_mfma_f32_16x16x32_bf16 v[166:169], v[76:79], v[88:91], v[166:169]\n"
      "ds_read_b128 v[180:183], v2 offset:27648\n"
      "v_cvt_pk_bf16_f32 v92, v194, v195\n"
      "v_mfma_f32_16x16x32_bf16 v[188:191], v[100:103], v[124:127], 0\n"
      "v_cvt_pk_bf16_f32 v93, v196, v197\n"
      "v_cvt_pk_bf16_f32 v94, v160, 0\n"
      "v_mfma_f32_16x16x32_bf16 v[188:191], v[80:83], v[88:91], v[188:191]\n"
      "v_rcp_f32 v210, v210\n"
      "v_mfma_f32_16x16x32_bf16 v[194:197], v[214:217], v[124:127], 0\n"
      "ds_read_b128 v[124:127], v2 offset:30720\n"
      "v_mul_f32 v166, v210, v166\n"
      "v_mfma_f32_16x16x32_bf16 v[194:197], v[84:87], v[88:91], v[194:197]\n"
      "v_mul_f32 v167, v210, v167\n"
      "v_cvt_pk_bf16_f32 v166, v166, v167\n"
      "v_mfma_f32_16x16x32_bf16 v[148:151], v[130:133], v[120:123], v[64:67]\n"
      "v_mul_f32 v168, v210, v168\n"
      "v_mul_f32_e64 v169, v210, v169\n"
      "v_mfma_f32_16x16x32_bf16 v[198:201], v[170:173], v[120:123], v[68:71]\n"
      "ds_read_b128 v[156:159], v2 offset:33792\n"
      "v_cvt_pk_bf16_f32 v167, v168, v169\n"
      "v_mfma_f32_16x16x32_bf16 v[96:99], v[184:187], v[120:123], v[72:75]\n"
      "s_waitcnt lgkmcnt(2)\n"
      "v_mul_f32 v188, v210, v188\n"
      "v_mul_f32 v189, v210, v189\n"
      "v_mfma_f32_16x16x32_bf16 v[114:117], v[174:177], v[120:123], v[28:31]\n"
      "v_mul_f32 v190, v210, v190\n"
      "v_mul_f32 v191, v210, v191\n"
      "v_mfma_f32_16x16x16_bf16 v[148:151], v[202:203], v[142:143], v[148:151]\n"
      "v_cvt_pk_bf16_f32 v168, v188, v189\n"
      "v_cvt_pk_bf16_f32 v169, v190, v191\n"
      "v_mul_f32 v194, v210, v194\n"
      "v_mfma_f32_16x16x16_bf16 v[198:201], v[146:147], v[142:143], v[198:201]\n"
      "v_mul_f32 v195, v210, v195\n"
      "v_cvt_pk_bf16_f32 v194, v194, v195\n"
      "v_mul_f32 v196, v210, v196\n"
      "v_mfma_f32_16x16x16_bf16 v[96:99], v[204:205], v[142:143], v[96:99]\n"
      "v_exp_f32 v148, v148\n"
      "v_exp_f32 v149, v149\n"
      "v_mfma_f32_16x16x16_bf16 v[114:117], v[178:179], v[142:143], v[114:117]\n"
      "v_exp_f32 v150, v150\n"
      "v_exp_f32 v151, v151\n"
      "v_mfma_f32_16x16x32_bf16 v[220:223], a[228:231], v[180:183], v[220:223]\n"
      "s_waitcnt lgkmcnt(0)\n"
      "v_exp_f32 v198, v198\n"
      "v_mfma_f32_16x16x32_bf16 v[206:209], a[252:255], v[180:183], v[206:209]\n"
      "ds_read_b128 v[138:141], v2 offset:18688\n"
      "v_exp_f32_e64 v199, v199\n"
      "v_mfma_f32_16x16x32_bf16 v[162:165], v[244:247], v[180:183], v[162:165]\n"
      "ds_read_b128 v[188:191], v2 offset:21760\n"
      "v_exp_f32_e64 v200, v200\n"
      "v_mfma_f32_16x16x32_bf16 v[220:223], a[232:235], v[124:127], v[220:223]\n"
      "ds_read_b128 v[142:145], v2 offset:24832\n"
      "v_exp_f32_e64 v201, v201\n"
      "v_mfma_f32_16x16x32_bf16 v[206:209], v[224:227], v[124:127], v[206:209]\n"
      "ds_write_b128 v3, v[166:169]\n"
      "v_mfma_f32_16x16x32_bf16 v[162:165], v[248:251], v[124:127], v[162:165]\n"
      "v_cvt_pk_bf16_f32 v148, v148, v149\n"
      "v_cvt_pk_bf16_f32 v149, v150, v151\n"
      "v_mfma_f32_16x16x32_bf16 v[220:223], a[236:239], v[156:159], v[220:223]\n"
      "s_waitcnt lgkmcnt(3)\n"
      "v_cvt_pk_bf16_f32 v150, v198, v199\n"
      "v_cvt_pk_bf16_f32 v151, v200, v201\n"
      "v_mfma_f32_16x16x32_bf16 v[206:209], v[228:231], v[156:159], v[206:209]\n"
      "s_waitcnt lgkmcnt(2)\n"
      "v_exp_f32 v96, v96\n"
      "v_mfma_f32_16x16x32_bf16 v[162:165], v[252:255], v[156:159], v[162:165]\n"
      "s_waitcnt lgkmcnt(1)\n"
      "v_exp_f32 v97, v97\n"
      "v_mfma_f32_16x16x32_bf16 v[166:169], a[216:219], v[138:141], 0\n"
      "v_exp_f32 v98, v98\n"
      "v_mfma_f32_16x16x32_bf16 v[180:183], a[240:243], v[138:141], 0\n"
      "v_exp_f32 v99, v99\n"
      "v_mfma_f32_16x16x32_bf16 v[124:127], v[232:235], v[138:141], 0\n"
      "v_exp_f32 v114, v114\n"
      "v_mfma_f32_16x16x32_bf16 v[166:169], a[220:223], v[188:191], v[166:169]\n"
      "v_cvt_pk_bf16_f32 v88, v96, v97\n"
      "v_cvt_pk_bf16_f32 v89, v98, v99\n"
      "v_mfma_f32_16x16x32_bf16 v[180:183], a[244:247], v[188:191], v[180:183]\n"
      "ds_read_b128 v[138:141], v10\n"
      "v_cvt_pk_bf16_f32 v90, v114, 0\n"
      "v_mfma_f32_16x16x32_bf16 v[124:127], v[236:239], v[188:191], v[124:127]\n"
      "v_mul_f32 v136, v220, v220\n"
      "v_add_f32 v135, v220, v221\n"
      "v_mfma_f32_16x16x32_bf16 v[166:169], a[224:227], v[142:145], v[166:169]\n"
      "v_fmac_f32 v136, v221, v221\n"
      "v_add_f32 v135, v135, v222\n"
      "v_mfma_f32_16x16x32_bf16 v[180:183], a[248:251], v[142:145], v[180:183]\n"
      "v_fmac_f32 v136, v222, v222\n"
      "v_add_f32 v135, v135, v223\n"
      "v_mfma_f32_16x16x32_bf16 v[124:127], v[240:243], v[142:145], v[124:127]\n"
      "ds_read_b64 v[212:213], v10 offset:16\n"
      "v_fmac_f32 v136, v223, v223\n"
      "v_mfma_f32_16x16x32_bf16 v[188:191], v[32:35], v[108:111], 0\n"
      "v_add_f32 v135, v135, v206\n"
      "v_fmac_f32_e64 v136, v206, v206\n"
      "v_mfma_f32_16x16x32_bf16 v[188:191], v[32:35], v[92:95], v[188:191]\n"
      "s_waitcnt lgkmcnt(1)\n"
      "v_add_f32 v135, v135, v207\n"
      "v_fmac_f32 v136, v207, v207\n"
      "v_mfma_f32_16x16x32_bf16 v[142:145], v[104:107], v[108:111], 0\n"
      "v_add_f32 v135, v135, v208\n"
      "v_fmac_f32 v136, v208, v208\n"
      "v_mfma_f32_16x16x32_bf16 v[142:145], v[76:79], v[92:95], v[142:145]\n"
      "ds_read_b128 v[96:99], v2 offset:27904\n"
      "v_add_f32 v135, v135, v209\n"
      "v_mfma_f32_16x16x32_bf16 v[190:193], v[100:103], v[108:111], 0\n"
      "v_fmac_f32 v136, v209, v209\n"
      "v_add_f32 v135, v135, v162\n"
      "v_mfma_f32_16x16x32_bf16 v[190:193], v[80:83], v[92:95], v[190:193]\n"
      "s_waitcnt lgkmcnt(1)\n"
      "v_fmac_f32 v136, v162, v162\n"
      "v_add_f32_e64 v135, v135, v163\n"
      "v_mfma_f32_16x16x32_bf16 v[198:201], v[214:217], v[108:111], 0\n"
      "ds_read_b128 v[108:111], v2 offset:30976\n"
      "v_fmac_f32 v136, v163, v163\n"
      "v_mfma_f32_16x16x32_bf16 v[198:201], v[84:87], v[92:95], v[198:201]\n"
      "v_add_f32 v135, v135, v164\n"
      "v_fmac_f32 v136, v164, v164\n"
      "v_mfma_f32_16x16x32_bf16 v[112:115], v[138:141], v[130:133], v[30:33]\n"
      "v_add_f32 v135, v135, v165\n"
      "v_fmac_f32_e64 v136, v165, v165\n"
      "v_mfma_f32_16x16x32_bf16 v[130:133], v[138:141], v[170:173], v[30:33]\n"
      "ds_read_b128 v[158:161], v2 offset:34048\n"
      "v_rcp_f32 v188, v188\n"
      "v_mfma_f32_16x16x32_bf16 v[152:155], v[138:141], v[184:187], v[30:33]\n"
      "s_waitcnt lgkmcnt(2)\n"
      "v_permlane16_swap_b32_e64 v135, v136\n"
      "v_mfma_f32_16x16x32_bf16 v[184:187], v[138:141], v[174:177], v[30:33]\n"
      "v_mul_f32 v142, v188, v142\n"
      "v_mul_f32 v143, v188, v143\n"
      "v_mfma_f32_16x16x16_bf16 v[112:115], v[212:213], v[202:203], v[112:115]\n"
      "v_cvt_pk_bf16_f32 v142, v142, v143\n"
      "v_mul_f32 v144, v188, v144\n"
      "v_mul_f32 v145, v188, v145\n"
      "v_mfma_f32_16x16x16_bf16 v[130:133], v[212:213], v[146:147], v[130:133]\n"
      "v_add_f32 v135, v135, v136\n"
      "v_cvt_pk_bf16_f32 v143, v144, v145\n"
      "v_mul_f32 v190, v188, v190\n"
      "v_mfma_f32_16x16x16_bf16 v[152:155], v[212:213], v[204:205], v[152:155]\n"
      "v_mul_f32 v191, v188, v191\n"
      "v_mul_f32 v192, v188, v192\n"
      "v_mul_f32_e64 v193, v188, v193\n"
      "v_mfma_f32_16x16x16_bf16 v[184:187], v[212:213], v[178:179], v[184:187]\n"
      "v_cndmask_b32 v112, v112, v130, s[58:59]\n"
      "v_cvt_pk_bf16_f32 v144, v190, v191\n"
      "v_cvt_pk_bf16_f32 v145, v192, v193\n"
      "v_mfma_f32_16x16x32_bf16 v[166:169], a[228:231], v[96:99], v[166:169]\n"
      "s_waitcnt lgkmcnt(0)\n"
      "v_cndmask_b32 v112, v112, v152, s[60:61]\n"
      "v_mul_f32 v198, v188, v198\n"
      "v_mfma_f32_16x16x32_bf16 v[180:183], a[252:255], v[96:99], v[180:183]\n"
      "ds_write_b128 v3, v[142:145] offset:256\n"
      "v_mfma_f32_16x16x32_bf16 v[124:127], v[244:247], v[96:99], v[124:127]\n"
      "v_cndmask_b32 v112, v112, v184, s[62:63]\n"
      "v_mul_f32 v199, v188, v199\n"
      "v_mfma_f32_16x16x32_bf16 v[166:169], a[232:235], v[108:111], v[166:169]\n"
      "v_exp_f32 v112, v112\n"
      "v_mfma_f32_16x16x32_bf16 v[180:183], v[224:227], v[108:111], v[180:183]\n"
      "ds_read_b128 v[174:177], v2 offset:18944\n"
      "v_mul_f32_e64 v197, v210, v197\n"
      "v_mfma_f32_16x16x32_bf16 v[124:127], v[248:251], v[108:111], v[124:127]\n"
      "v_cvt_pk_bf16_f32 v198, v198, v199\n"
      "v_mul_f32 v200, v188, v200\n"
      "v_mfma_f32_16x16x32_bf16 v[166:169], a[236:239], v[158:161], v[166:169]\n"
      "v_mul_f32 v201, v188, v201\n"
      "v_cvt_pk_bf16_f32 v195, v196, v197\n"
      "v_mfma_f32_16x16x32_bf16 v[180:183], v[228:231], v[158:161], v[180:183]\n"
      "ds_read_b128 v[188:191], v2 offset:22016\n"
      "v_cvt_pk_bf16_f32 v199, v200, v201\n"
      "v_mfma_f32_16x16x32_bf16 v[124:127], v[252:255], v[158:161], v[124:127]\n"
      "ds_write_b64 v4, v[194:195]\n"
      "v_mfma_f32_16x16x32_bf16 v[210:213], v[32:35], v[148:151], 0\n"
      "v_mul_f32 v193, v166, v166\n"
      "v_add_f32 v192, v166, v167\n"
      "v_mfma_f32_16x16x32_bf16 v[210:213], v[32:35], v[88:91], v[210:213]\n"
      "ds_read_b128 v[202:205], v2 offset:25088\n"
      "v_fmac_f32_e64 v193, v167, v167\n"
      "v_mfma_f32_16x16x32_bf16 v[130:133], v[104:107], v[148:151], 0\n"
      "ds_write_b64 v4, v[198:199] offset:256\n"
      "v_mfma_f32_16x16x32_bf16 v[130:133], v[76:79], v[88:91], v[130:133]\n"
      "s_waitcnt lgkmcnt(4)\n"
      "v_add_f32 v192, v192, v168\n"
      "v_fmac_f32_e64 v193, v168, v168\n"
      "v_mfma_f32_16x16x32_bf16 v[194:197], v[100:103], v[148:151], 0\n"
      "ds_read_b128 v[198:201], v2 offset:28160\n"
      "v_add_f32 v192, v192, v169\n"
      "v_mfma_f32_16x16x32_bf16 v[194:197], v[80:83], v[88:91], v[194:197]\n"
      "s_waitcnt lgkmcnt(4)\n"
      "v_fmac_f32 v193, v169, v169\n"
      "v_add_f32 v192, v192, v180\n"
      "v_mfma_f32_16x16x32_bf16 v[152:155], v[214:217], v[148:151], 0\n"
      "v_fmac_f32 v193, v180, v180\n"
      "v_add_f32 v192, v192, v181\n"
      "v_mfma_f32_16x16x32_bf16 v[152:155], v[84:87], v[88:91], v[152:155]\n"
      "ds_read_b128 v[116:119], v2 offset:31232\n"
      "v_fmac_f32 v193, v181, v181\n"
      "v_mfma_f32_16x16x32_bf16 v[120:123], a[216:219], v[174:177], 0\n"
      "s_waitcnt lgkmcnt(3)\n"
      "v_add_f32 v192, v192, v182\n"
      "v_fmac_f32 v193, v182, v182\n"
      "v_mfma_f32_16x16x32_bf16 v[148:151], a[240:243], v[174:177], 0\n"
      "v_add_f32 v192, v192, v183\n"
      "v_fmac_f32 v193, v183, v183\n"
      "v_mfma_f32_16x16x32_bf16 v[144:147], v[232:235], v[174:177], 0\n"
      "ds_read_b128 v[174:177], v2 offset:34304\n"
      "v_add_f32_e64 v192, v192, v124\n"
      "v_mfma_f32_16x16x32_bf16 v[120:123], a[220:223], v[188:191], v[120:123]\n"
      "s_waitcnt lgkmcnt(2)\n"
      "v_fmac_f32 v193, v124, v124\n"
      "v_add_f32 v192, v192, v125\n"
      "v_mfma_f32_16x16x32_bf16 v[148:151], a[244:247], v[188:191], v[148:151]\n"
      "v_fmac_f32 v193, v125, v125\n"
      "v_add_f32 v192, v192, v126\n"
      "v_mfma_f32_16x16x32_bf16 v[144:147], v[236:239], v[188:191], v[144:147]\n"
      "s_waitcnt lgkmcnt(1)\n"
      "v_fmac_f32 v193, v126, v126\n"
      "v_add_f32 v192, v192, v127\n"
      "v_mfma_f32_16x16x32_bf16 v[120:123], a[224:227], v[202:205], v[120:123]\n"
      "v_fmac_f32 v193, v127, v127\n"
      "v_rcp_f32 v210, v210\n"
      "v_mfma_f32_16x16x32_bf16 v[148:151], a[248:251], v[202:205], v[148:151]\n"
      "v_permlane16_swap_b32 v192, v193\n"
      "v_mfma_f32_16x16x32_bf16 v[144:147], v[240:243], v[202:205], v[144:147]\n"
      "s_waitcnt lgkmcnt(0)\n"
      "v_mul_f32 v130, v210, v130\n"
      "v_mul_f32 v131, v210, v131\n"
      "v_mfma_f32_16x16x32_bf16 v[120:123], a[228:231], v[198:201], v[120:123]\n"
      "v_cvt_pk_bf16_f32 v130, v130, v131\n"
      "v_mul_f32_e64 v132, v210, v132\n"
      "v_mfma_f32_16x16x32_bf16 v[148:151], a[252:255], v[198:201], v[148:151]\n"
      "v_mul_f32 v133, v210, v133\n"
      "v_add_f32 v192, v192, v193\n"
      "v_mfma_f32_16x16x32_bf16 v[144:147], v[244:247], v[198:201], v[144:147]\n"
      "v_cvt_pk_bf16_f32 v131, v132, v133\n"
      "v_mul_f32 v194, v210, v194\n"
      "v_mfma_f32_16x16x32_bf16 v[120:123], a[232:235], v[116:119], v[120:123]\n"
      "v_mul_f32 v195, v210, v195\n"
      "v_mul_f32 v196, v210, v196\n"
      "v_mfma_f32_16x16x32_bf16 v[148:151], v[224:227], v[116:119], v[148:151]\n"
      "v_mul_f32 v197, v210, v197\n"
      "v_cvt_pk_bf16_f32 v132, v194, v195\n"
      "v_mfma_f32_16x16x32_bf16 v[144:147], v[248:251], v[116:119], v[144:147]\n"
      "v_cvt_pk_bf16_f32 v133, v196, v197\n"
      "v_mul_f32_e64 v152, v210, v152\n"
      "v_mfma_f32_16x16x32_bf16 v[120:123], a[236:239], v[174:177], v[120:123]\n"
      "ds_write_b128 v3, v[130:133] offset:512\n"
      "v_mfma_f32_16x16x32_bf16 v[148:151], v[228:231], v[174:177], v[148:151]\n"
      "v_mul_f32_e64 v153, v210, v153\n"
      "v_cvt_pk_bf16_f32 v152, v152, v153\n"
      "v_mfma_f32_16x16x32_bf16 v[144:147], v[252:255], v[174:177], v[144:147]\n"
      "v_mul_f32 v154, v210, v154\n"
      "v_mul_f32 v155, v210, v155\n"
      "v_permlane32_swap_b32 v135, v192\n"
      "v_mul_f32 v190, v120, v120\n"
      "v_add_f32 v189, v120, v121\n"
      "v_fmac_f32 v190, v121, v121\n"
      "v_add_f32 v189, v189, v122\n"
      "v_fmac_f32 v190, v122, v122\n"
      "v_add_f32 v189, v189, v123\n"
      "v_fmac_f32 v190, v123, v123\n"
      "v_add_f32 v189, v189, v148\n"
      "v_fmac_f32 v190, v148, v148\n"
      "v_add_f32 v189, v189, v149\n"
      "v_fmac_f32 v190, v149, v149\n"
      "v_add_f32 v189, v189, v150\n"
      "v_fmac_f32 v190, v150, v150\n"
      "v_add_f32 v189, v189, v151\n"
      "v_fmac_f32 v190, v151, v151\n"
      "v_add_f32 v189, v189, v144\n"
      "v_fmac_f32 v190, v144, v144\n"
      "v_add_f32 v189, v189, v145\n"
      "v_fmac_f32 v190, v145, v145\n"
      "v_add_f32 v189, v189, v146\n"
      "v_fmac_f32 v190, v146, v146\n"
      "v_add_f32 v189, v189, v147\n"
      "v_fmac_f32 v190, v147, v147\n"
      "v_cvt_pk_bf16_f32 v153, v154, v155\n"
      "ds_write_b64 v4, v[152:153] offset:512\n"
      "v_permlane16_swap_b32 v189, v190\n"
      "v_add_f32 v189, v189, v190\n"
      "v_mov_b32 v190, v189\n"
      "v_cvt_pk_bf16_f32 v112, v112, v112\n"
      "v_add_f32 v135, v135, v192\n"
      "v_permlane32_swap_b32 v189, v190\n"
      "v_add_f32 v189, v189, v190\n"
      "ds_write_b16 v13, v112\n"
      "ds_read_b128 v[210:213], v14\n"
      "ds_read_b128 v[174:177], v14 offset:64\n"
      "ds_write_b32 v8, v135\n"
      "v_cmp_ne_u32 s[56:57], 0, v134\n"
      "v_readlane_b32 s47, v38, s43\n"
      "s_cmp_ge_u32 s95, 0x40000000\n"
      "s_cselect_b64 s[56:57], -1, s[56:57]\n"
      "ds_write_b32 v8, v189 offset:512\n"
      "s_cmp_gt_u32 s42, s44\n"
      "s_cselect_b64 s[56:57], -1, s[56:57]\n"
      "s_and_b32 s47, s47, -4\n"
      "s_or_b64 vcc, s[56:57], s[54:55]\n"
      "s_add_u32 s42, s42, 1\n"
      "s_add_u32 s43, s43, 1\n"
      "v_add_u32_e64 v12, 32, v12\n"
      "s_waitcnt lgkmcnt(3)\n"
      "v_mfma_f32_16x16x32_bf16 v[188:191], v[210:213], v[32:35], 0\n"
      "s_waitcnt lgkmcnt(2)\n"
      "v_mfma_f32_16x16x32_bf16 v[188:191], v[174:177], v[32:35], v[188:191]\n"
      "s_waitcnt lgkmcnt(0)\n"
      "s_barrier\n"
      "s_cbranch_vccz L_loop1_%=\n"
      "L_disp1_%=:\n"
      "s_sub_u32 s92, s42, 1\n"
      "s_cmp_gt_u32 s92, s44\n"
      "s_cbranch_scc1 L_tail1_%=\n"
      "s_nop 7\n"
      "s_nop 7\n"
      "L_slow1_%=:\n"
      "v_mfma_f32_16x16x32_bf16 v[202:205], v[210:213], v[104:107], 0\n"
      "ds_read_b128 v[104:107], v9\n"
      "ds_read_b128 v[190:193], v9 offset:1024\n"
      "v_mfma_f32_16x16x32_bf16 v[202:205], v[174:177], v[76:79], v[202:205]\n"
      "ds_read_b128 v[198:201], v0 offset:18432\n"
      "v_mov_b32_dpp v188, v188 quad_perm:[0,0,0,0] row_mask:0xf bank_mask:0xf\n"
      "v_mfma_f32_16x16x32_bf16 v[130:133], v[210:213], v[100:103], 0\n"
      "ds_read_b128 v[100:103], v1 offset:18432\n"
      "v_rcp_f32 v188, v188\n"
      "v_mfma_f32_16x16x32_bf16 v[130:133], v[174:177], v[80:83], v[130:133]\n"
      "ds_read_b128 v[152:155], v0 offset:24576\n"
      "v_subrev_u32 v98, 115456, v9\n"
      "v_mfma_f32_16x16x32_bf16 v[194:197], v[210:213], v[214:217], 0\n"
      "s_add_u32 s72, s42, 1\n"
      "v_readlane_b32 s77, v38, s72\n"
      "v_bfe_i32 v214, v36, 0, 1\n"
      "v_mfma_f32_16x16x32_bf16 v[194:197], v[174:177], v[84:87], v[194:197]\n"
      "s_mov_b32 m0, 110592\n"
      "v_cndmask_b32 v202, v202, v130, s[58:59]\n"
      "s_waitcnt lgkmcnt(4)\n"
      "v_add_f32 v104, v104, v106\n"
      "v_add_f32_e64 v105, v105, v107\n"
      "v_bfe_i32 v216, v36, 1, 1\n"
      "s_waitcnt lgkmcnt(3)\n"
      "v_add_f32 v190, v190, v192\n"
      "v_cndmask_b32 v202, v202, v194, s[60:61]\n"
      "v_mul_f32 v202, v188, v202\n"
      "s_waitcnt lgkmcnt(2)\n"
      "v_mfma_f32_16x16x32_bf16 v[194:197], a[0:3], v[198:201], 0\n"
      "s_and_b32 s73, s77, -16\n"
      "v_cvt_pk_bf16_f32 v202, v202, v202\n"
      "v_add_f32 v191, v191, v193\n"
      "v_mfma_f32_16x16x32_bf16 v[158:161], a[24:27], v[198:201], 0\n"
      "ds_write_b16 v12, v202\n"
      "v_mfma_f32_16x16x32_bf16 v[134:137], a[48:51], v[198:201], 0\n"
      "buffer_load_dwordx4 v98, s[24:27], s73 offen  lds\n"
      "v_mfma_f32_16x16x32_bf16 v[202:205], a[72:75], v[198:201], 0\n"
      "s_waitcnt lgkmcnt(1)\n"
      "v_add_f32 v104, v104, v190\n"
      "v_add_f32_e64 v105, v105, v191\n"
      "v_mfma_f32_16x16x32_bf16 v[116:119], a[96:99], v[198:201], 0\n"
      "s_mov_b32 m0, 111616\n"
      "v_fma_f32 v190, -v104, v104, s52\n"
      "v_fmac_f32 v190, 0x43400000, v105\n"
      "v_mfma_f32_16x16x32_bf16 v[108:111], a[120:123], v[198:201], 0\n"
      "ds_read_b128 v[186:189], v1 offset:24576\n"
      "v_max_f32 v190, s52, v190\n"
      "v_mfma_f32_16x16x32_bf16 v[194:197], a[4:7], v[100:103], v[194:197]\n"
      "ds_read_b128 v[198:201], v0 offset:30720\n"
      "v_rsq_f32 v190, v190\n"
      "v_mfma_f32_16x16x32_bf16 v[194:197], a[8:11], v[152:155], v[194:197]\n"
      "ds_read_b128 v[112:115], v1 offset:30720\n"
      "v_mul_f32 v176, 0x43400000, v190\n"
      "v_mfma_f32_16x16x32_bf16 v[158:161], a[28:31], v[100:103], v[158:161]\n"
      "s_add_u32 s73, s73, 1024\n"
      "v_mul_f32 v177, -v104, v190\n"
      "v_mov_b32_e64 v138, v176\n"
      "v_mfma_f32_16x16x32_bf16 v[158:161], a[32:35], v[152:155], v[158:161]\n"
      "buffer_load_dwordx4 v98, s[24:27], s73 offen  lds\n"
      "v_mfma_f32_16x16x32_bf16 v[134:137], a[52:55], v[100:103], v[134:137]\n"
      "s_mov_b32 m0, 112640\n"
      "v_mov_b32 v139, v177\n"
      "v_permlane32_swap_b32 v176, v138\n"
      "v_mfma_f32_16x16x32_bf16 v[134:137], a[56:59], v[152:155], v[134:137]\n"
      "s_add_u32 s73, s73, 1024\n"
      "v_permlane32_swap_b32 v177, v139\n"
      "v_mfma_f32_16x16x32_bf16 v[202:205], a[76:79], v[100:103], v[202:205]\n"
      "s_bitcmp1_b32 s45, 31\n"
      "s_cselect_b32 s75, 100270464, 0\n"
      "v_mov_b32_e64 v215, v176\n"
      "v_mfma_f32_16x16x32_bf16 v[202:205], a[80:83], v[152:155], v[202:205]\n"
      "buffer_load_dwordx4 v98, s[24:27], s73 offen  lds\n"
      "v_mfma_f32_16x16x32_bf16 v[116:119], a[100:103], v[100:103], v[116:119]\n"
      "s_mov_b32 m0, 113664\n"
      "v_mov_b32 v217, v177\n"
      "v_permlane16_swap_b32_e64 v176, v215\n"
      "v_mfma_f32_16x16x32_bf16 v[116:119], a[104:107], v[152:155], v[116:119]\n"
      "s_waitcnt lgkmcnt(0)\n"
      "v_permlane16_swap_b32_e64 v177, v217\n"
      "v_mfma_f32_16x16x32_bf16 v[108:111], a[124:127], v[100:103], v[108:111]\n"
      "s_add_u32 s73, s73, 1024\n"
      "v_fma_f32 v220, v220, v176, v177\n"
      "v_fma_f32 v221, v221, v176, v177\n"
      "v_mfma_f32_16x16x32_bf16 v[108:111], a[128:131], v[152:155], v[108:111]\n"
      "buffer_load_dwordx4 v98, s[24:27], s73 offen  lds\n"
      "v_mfma_f32_16x16x32_bf16 v[194:197], a[12:15], v[186:189], v[194:197]\n"
      "s_mov_b32 m0, 114688\n"
      "v_cvt_pk_bf16_f32 v220, v220, v221\n"
      "v_fma_f32 v222, v222, v176, v177\n"
      "v_mfma_f32_16x16x32_bf16 v[194:197], a[16:19], v[198:201], v[194:197]\n"
      "s_add_u32 s73, s73, 1024\n"
      "v_fma_f32 v223, v223, v176, v177\n"
      "v_fma_f32 v162, v162, v176, v177\n"
      "v_mfma_f32_16x16x32_bf16 v[194:197], a[20:23], v[112:115], v[194:197]\n"
      "s_bitcmp1_b32 s45, 30\n"
      "s_cselect_b32 s76, 196224, 0\n"
      "v_fma_f32 v163, v163, v176, v177\n"
      "v_mfma_f32_16x16x32_bf16 v[158:161], a[36:39], v[186:189], v[158:161]\n"
      "s_mov_b32 exec_hi, 0xffff\n"
      "buffer_load_dwordx4 v98, s[24:27], s73 offen  lds\n"
      "s_mov_b32 exec_hi, -1\n"
      "v_mfma_f32_16x16x32_bf16 v[158:161], a[40:43], v[198:201], v[158:161]\n"
      "s_and_b32 s74, s45, 0x3fffffff\n"
      "v_cvt_pk_bf16_f32 v221, v222, v223\n"
      "v_fma_f32 v206, v206, v176, v177\n"
      "v_mfma_f32_16x16x32_bf16 v[158:161], a[44:47], v[112:115], v[158:161]\n"
      "ds_read_b128 v[104:107], v0 offset:20480\n"
      "v_fma_f32 v207, v207, v176, v177\n"
      "v_mfma_f32_16x16x32_bf16 v[134:137], a[60:63], v[186:189], v[134:137]\n"
      "ds_read_b128 v[190:193], v1 offset:20480\n"
      "v_fma_f32 v208, v208, v176, v177\n"
      "v_mfma_f32_16x16x32_bf16 v[134:137], a[64:67], v[198:201], v[134:137]\n"
      "ds_read_b128 v[96:99], v0 offset:26624\n"
      "v_fma_f32 v209, v209, v176, v177\n"
      "v_mfma_f32_16x16x32_bf16 v[134:137], a[68:71], v[112:115], v[134:137]\n"
      "ds_read_b128 v[152:155], v1 offset:26624\n"
      "v_cvt_pk_bf16_f32 v162, v162, v163\n"
      "v_mfma_f32_16x16x32_bf16 v[202:205], a[84:87], v[186:189], v[202:205]\n"
      "ds_read_b128 v[140:143], v0 offset:32768\n"
      "v_fma_f32 v164, v164, v176, v177\n"
      "v_mfma_f32_16x16x32_bf16 v[202:205], a[88:91], v[198:201], v[202:205]\n"
      "ds_read_b128 v[130:133], v1 offset:32768\n"
      "v_fma_f32 v165, v165, v176, v177\n"
      "v_mfma_f32_16x16x32_bf16 v[202:205], a[92:95], v[112:115], v[202:205]\n"
      "s_add_u32 m0, s64, 0\n"
      "v_cvt_pk_bf16_f32 v222, v206, v207\n"
      "v_cvt_pk_bf16_f32 v223, v208, v209\n"
      "v_mfma_f32_16x16x32_bf16 v[116:119], a[108:111], v[186:189], v[116:119]\n"
      "ds_write_b128 v5, v[220:223] offset:18432\n"
      "v_mfma_f32_16x16x32_bf16 v[116:119], a[112:115], v[198:201], v[116:119]\n"
      "s_bitcmp1_b32 s46, 31\n"
      "s_cselect_b32 s93, 100270464, 0\n"
      "v_cvt_pk_bf16_f32 v163, v164, v165\n"
      "v_mfma_f32_16x16x32_bf16 v[116:119], a[116:119], v[112:115], v[116:119]\n"
      "s_bitcmp1_b32 s46, 30\n"
      "s_cselect_b32 s94, 196224, 0\n"
      "v_fma_f32 v166, v166, v215, v217\n"
      "v_mfma_f32_16x16x32_bf16 v[108:111], a[132:135], v[186:189], v[108:111]\n"
      "s_waitcnt lgkmcnt(1)\n"
      "v_fma_f32 v167, v167, v215, v217\n"
      "v_and_b32_e64 v214, s75, v214\n"
      "v_mfma_f32_16x16x32_bf16 v[108:111], a[136:139], v[198:201], v[108:111]\n"
      "ds_write_b64 v6, v[162:163] offset:18432\n"
      "v_mfma_f32_16x16x32_bf16 v[108:111], a[140:143], v[112:115], v[108:111]\n"
      "s_and_b32 s92, s46, 0x3fffffff\n"
      "v_and_b32 v216, s76, v216\n"
      "v_cvt_pk_bf16_f32 v166, v166, v167\n"
      "v_mfma_f32_16x16x32_bf16 v[174:177], a[0:3], v[104:107], 0\n"
      "s_and_b32 s77, s77, 15\n"
      "v_fma_f32 v168, v168, v215, v217\n"
      "v_fma_f32 v169, v169, v215, v217\n"
      "v_mfma_f32_16x16x32_bf16 v[174:177], a[4:7], v[190:193], v[174:177]\n"
      "s_add_u32 s73, s73, 1024\n"
      "v_fma_f32 v124, v124, v215, v217\n"
      "v_fma_f32 v125, v125, v215, v217\n"
      "v_mfma_f32_16x16x32_bf16 v[174:177], a[8:11], v[96:99], v[174:177]\n"
      "v_add_u32_e64 v214, v214, v216\n"
      "v_cvt_pk_bf16_f32 v167, v168, v169\n"
      "v_mfma_f32_16x16x32_bf16 v[174:177], a[12:15], v[152:155], v[174:177]\n"
      "v_fma_f32 v180, v180, v215, v217\n"
      "v_fma_f32 v181, v181, v215, v217\n"
      "v_mfma_f32_16x16x32_bf16 v[174:177], a[16:19], v[140:143], v[174:177]\n"
      "v_fma_f32 v182, v182, v215, v217\n"
      "v_fma_f32 v183, v183, v215, v217\n"
      "v_mfma_f32_16x16x32_bf16 v[174:177], a[20:23], v[130:133], v[174:177]\n"
      "v_cvt_pk_bf16_f32 v124, v124, v125\n"
      "v_fma_f32 v126, v126, v215, v217\n"
      "v_mfma_f32_16x16x32_bf16 v[206:209], a[24:27], v[104:107], 0\n"
      "v_fma_f32 v127, v127, v215, v217\n"
      "v_sub_u32_e64 v214, s74, v214\n"
      "v_mfma_f32_16x16x32_bf16 v[206:209], a[28:31], v[190:193], v[206:209]\n"
      "v_cvt_pk_bf16_f32 v168, v180, v181\n"
      "v_cvt_pk_bf16_f32 v169, v182, v183\n"
      "v_mfma_f32_16x16x32_bf16 v[206:209], a[32:35], v[96:99], v[206:209]\n"
      "ds_write_b128 v5, v[166:169] offset:24576\n"
      "v_mfma_f32_16x16x32_bf16 v[206:209], a[36:39], v[152:155], v[206:209]\n"
      "v_cvt_pk_bf16_f32 v125, v126, v127\n"
      "v_add_u32_e64 v214, v16, v214\n"
      "v_mfma_f32_16x16x32_bf16 v[206:209], a[40:43], v[140:143], v[206:209]\n"
      "buffer_load_dwordx4 v214, s[16:19], 0 offen  lds\n"
      "v_mfma_f32_16x16x32_bf16 v[206:209], a[44:47], v[130:133], v[206:209]\n"
      "ds_write_b64 v6, v[124:125] offset:24576\n"
      "v_mfma_f32_16x16x32_bf16 v[162:165], a[48:51], v[104:107], 0\n"
      "s_add_u32 m0, s64, 4096\n"
      "v_bfe_i32 v198, v36, 2, 1\n"
      "v_bfe_i32 v201, v36, 3, 1\n"
      "v_mfma_f32_16x16x32_bf16 v[162:165], a[52:55], v[190:193], v[162:165]\n"
      "v_and_b32 v198, s75, v198\n"
      "v_and_b32 v201, s76, v201\n"
      "v_mfma_f32_16x16x32_bf16 v[162:165], a[56:59], v[96:99], v[162:165]\n"
      "v_add_u32 v198, v198, v201\n"
      "v_sub_u32 v198, s74, v198\n"
      "v_mfma_f32_16x16x32_bf16 v[162:165], a[60:63], v[152:155], v[162:165]\n"
      "v_add_u32_e64 v198, v17, v198\n"
      "buffer_load_dwordx4 v198, s[16:19], 0 offen  lds\n"
      "v_mfma_f32_16x16x32_bf16 v[162:165], a[64:67], v[140:143], v[162:165]\n"
      "s_add_u32 m0, s64, 8192\n"
      "v_bfe_i32 v168, v36, 4, 1\n"
      "v_bfe_i32 v167, v36, 5, 1\n"
      "v_mfma_f32_16x16x32_bf16 v[162:165], a[68:71], v[130:133], v[162:165]\n"
      "v_and_b32 v168, s75, v168\n"
      "v_and_b32 v167, s76, v167\n"
      "v_mfma_f32_16x16x32_bf16 v[198:201], a[72:75], v[104:107], 0\n"
      "v_add_u32 v168, v168, v167\n"
      "v_sub_u32 v168, s74, v168\n"
      "v_mfma_f32_16x16x32_bf16 v[198:201], a[76:79], v[190:193], v[198:201]\n"
      "v_add_u32_e64 v168, v18, v168\n"
      "buffer_load_dwordx4 v168, s[16:19], 0 offen  lds\n"
      "v_mfma_f32_16x16x32_bf16 v[198:201], a[80:83], v[96:99], v[198:201]\n"
      "s_add_u32 m0, s64, 12288\n"
      "v_bfe_i32 v214, v36, 6, 1\n"
      "v_bfe_i32 v168, v36, 7, 1\n"
      "v_mfma_f32_16x16x32_bf16 v[198:201], a[84:87], v[152:155], v[198:201]\n"
      "v_and_b32 v214, s75, v214\n"
      "v_and_b32 v168, s76, v168\n"
      "v_mfma_f32_16x16x32_bf16 v[198:201], a[88:91], v[140:143], v[198:201]\n"
      "v_add_u32 v214, v214, v168\n"
      "v_sub_u32 v214, s74, v214\n"
      "v_mfma_f32_16x16x32_bf16 v[198:201], a[92:95], v[130:133], v[198:201]\n"
      "ds_read_b128 v[166:169], v0 offset:22528\n"
      "v_add_u32_e64 v214, v19, v214\n"
      "v_mfma_f32_16x16x32_bf16 v[186:189], a[96:99], v[104:107], 0\n"
      "buffer_load_dwordx4 v214, s[16:19], 0 offen  lds\n"
      "v_mfma_f32_16x16x32_bf16 v[186:189], a[100:103], v[190:193], v[186:189]\n"
      "s_add_u32 m0, s65, 16384\n"
      "v_bfe_i32 v180, v36, 8, 1\n"
      "v_bfe_i32 v183, v36, 9, 1\n"
      "v_mfma_f32_16x16x32_bf16 v[186:189], a[104:107], v[96:99], v[186:189]\n"
      "ds_read_b128 v[214:217], v1 offset:22528\n"
      "v_and_b32 v180, s75, v180\n"
      "v_mfma_f32_16x16x32_bf16 v[186:189], a[108:111], v[152:155], v[186:189]\n"
      "ds_read_b128 v[170:173], v0 offset:28672\n"
      "v_and_b32 v183, s76, v183\n"
      "v_mfma_f32_16x16x32_bf16 v[186:189], a[112:115], v[140:143], v[186:189]\n"
      "ds_read_b128 v[218:221], v1 offset:28672\n"
      "v_add_u32_e64 v180, v180, v183\n"
      "v_mfma_f32_16x16x32_bf16 v[186:189], a[116:119], v[130:133], v[186:189]\n"
      "ds_read_b128 v[182:185], v0 offset:34816\n"
      "v_sub_u32_e64 v180, s74, v180\n"
      "v_mfma_f32_16x16x32_bf16 v[210:213], a[120:123], v[104:107], 0\n"
      "ds_read_b128 v[104:107], v1 offset:34816\n"
      "v_add_u32_e64 v180, v15, v180\n"
      "v_mfma_f32_16x16x32_bf16 v[210:213], a[124:127], v[190:193], v[210:213]\n"
      "buffer_load_dwordx4 v180, s[16:19], 0 offen  lds\n"
      "v_mfma_f32_16x16x32_bf16 v[210:213], a[128:131], v[96:99], v[210:213]\n"
      "s_waitcnt lgkmcnt(4)\n"
      "v_fma_f32 v120, v120, v138, v139\n"
      "v_fma_f32 v121, v121, v138, v139\n"
      "v_mfma_f32_16x16x32_bf16 v[210:213], a[132:135], v[152:155], v[210:213]\n"
      "s_waitcnt lgkmcnt(3)\n"
      "v_cvt_pk_bf16_f32 v120, v120, v121\n"
      "v_fma_f32 v122, v122, v138, v139\n"
      "v_mfma_f32_16x16x32_bf16 v[210:213], a[136:139], v[140:143], v[210:213]\n"
      "s_waitcnt lgkmcnt(2)\n"
      "v_fma_f32 v123, v123, v138, v139\n"
      "v_fma_f32 v144, v144, v138, v139\n"
      "v_mfma_f32_16x16x32_bf16 v[210:213], a[140:143], v[130:133], v[210:213]\n"
      "ds_read_b128 v[190:193], v9 offset:4608\n"
      "v_fma_f32 v145, v145, v138, v139\n"
      "v_mfma_f32_16x16x32_bf16 v[178:181], a[0:3], v[166:169], 0\n"
      "buffer_load_dwordx4 v[154:157], v27, s[24:27], s47 offen\n"
      "v_mfma_f32_16x16x32_bf16 v[178:181], a[4:7], v[214:217], v[178:181]\n"
      "s_waitcnt lgkmcnt(1)\n"
      "v_cvt_pk_bf16_f32 v121, v122, v123\n"
      "v_fma_f32 v148, v148, v138, v139\n"
      "v_mfma_f32_16x16x32_bf16 v[178:181], a[8:11], v[170:173], v[178:181]\n"
      "v_fma_f32 v149, v149, v138, v139\n"
      "v_fma_f32 v150, v150, v138, v139\n"
      "v_mfma_f32_16x16x32_bf16 v[178:181], a[12:15], v[218:221], v[178:181]\n"
      "v_fma_f32 v151, v151, v138, v139\n"
      "v_cvt_pk_bf16_f32 v144, v144, v145\n"
      "v_mfma_f32_16x16x32_bf16 v[178:181], a[16:19], v[182:185], v[178:181]\n"
      "buffer_load_dword v93, v29, s[24:27], s47 offen\n"
      "v_mfma_f32_16x16x32_bf16 v[178:181], a[20:23], v[104:107], v[178:181]\n"
      "v_fma_f32 v146, v146, v138, v139\n"
      "v_fma_f32 v147, v147, v138, v139\n"
      "v_mfma_f32_16x16x32_bf16 v[96:99], a[24:27], v[166:169], 0\n"
      "v_cvt_pk_bf16_f32 v122, v148, v149\n"
      "v_cvt_pk_bf16_f32 v123, v150, v151\n"
      "v_mfma_f32_16x16x32_bf16 v[96:99], a[28:31], v[214:217], v[96:99]\n"
      "ds_write_b128 v5, v[120:123] offset:30720\n"
      "v_mfma_f32_16x16x32_bf16 v[96:99], a[32:35], v[170:173], v[96:99]\n"
      "v_cvt_pk_bf16_f32 v145, v146, v147\n"
      "v_fma_f32 v120, v194, v194, s51\n"
      "v_mfma_f32_16x16x32_bf16 v[96:99], a[36:39], v[218:221], v[96:99]\n"
      "v_fma_f32 v121, v202, v202, s51\n"
      "v_fmac_f32_e64 v120, v195, v195\n"
      "v_mfma_f32_16x16x32_bf16 v[96:99], a[40:43], v[182:185], v[96:99]\n"
      "ds_write_b64 v6, v[144:145] offset:30720\n"
      "v_mfma_f32_16x16x32_bf16 v[96:99], a[44:47], v[104:107], v[96:99]\n"
      "v_fmac_f32 v121, v203, v203\n"
      "v_fmac_f32 v120, v196, v196\n"
      "v_mfma_f32_16x16x32_bf16 v[138:141], a[48:51], v[166:169], 0\n"
      "v_fmac_f32 v121, v204, v204\n"
      "v_fmac_f32 v120, v197, v197\n"
      "v_mfma_f32_16x16x32_bf16 v[138:141], a[52:55], v[214:217], v[138:141]\n"
      "v_fmac_f32 v121, v205, v205\n"
      "v_fmac_f32 v120, v158, v158\n"
      "v_mfma_f32_16x16x32_bf16 v[138:141], a[56:59], v[170:173], v[138:141]\n"
      "v_fmac_f32 v121, v116, v116\n"
      "v_fmac_f32 v120, v159, v159\n"
      "v_mfma_f32_16x16x32_bf16 v[138:141], a[60:63], v[218:221], v[138:141]\n"
      "ds_read_b128 v[150:153], v7\n"
      "v_fmac_f32 v121, v117, v117\n"
      "v_mfma_f32_16x16x32_bf16 v[138:141], a[64:67], v[182:185], v[138:141]\n"
      "ds_read_b128 v[100:103], v7 offset:4096\n"
      "v_fmac_f32 v120, v160, v160\n"
      "v_mfma_f32_16x16x32_bf16 v[138:141], a[68:71], v[104:107], v[138:141]\n"
      "v_fmac_f32 v121, v118, v118\n"
      "v_fmac_f32 v120, v161, v161\n"
      "v_mfma_f32_16x16x32_bf16 v[142:145], a[72:75], v[166:169], 0\n"
      "ds_read_b128 v[146:149], v7 offset:8192\n"
      "v_fmac_f32_e64 v121, v119, v119\n"
      "v_mfma_f32_16x16x32_bf16 v[142:145], a[76:79], v[214:217], v[142:145]\n"
      "ds_read_b128 v[122:125], v7 offset:12288\n"
      "v_fmac_f32 v120, v134, v134\n"
      "v_mfma_f32_16x16x32_bf16 v[142:145], a[80:83], v[170:173], v[142:145]\n"
      "v_fmac_f32 v121, v108, v108\n"
      "v_fmac_f32 v120, v135, v135\n"
      "v_mfma_f32_16x16x32_bf16 v[142:145], a[84:87], v[218:221], v[142:145]\n"
      "ds_read_b128 v[126:129], v7 offset:16384\n"
      "v_fmac_f32 v121, v109, v109\n"
      "v_mfma_f32_16x16x32_bf16 v[142:145], a[88:91], v[182:185], v[142:145]\n"
      "s_waitcnt vmcnt(7)\n"
      "v_fmac_f32 v120, v136, v136\n"
      "v_fmac_f32 v121, v110, v110\n"
      "v_mfma_f32_16x16x32_bf16 v[142:145], a[92:95], v[104:107], v[142:145]\n"
      "s_waitcnt lgkmcnt(3)\n"
      "v_fmac_f32 v120, v137, v137\n"
      "v_fmac_f32 v121, v111, v111\n"
      "v_mfma_f32_16x16x32_bf16 v[130:133], a[96:99], v[166:169], 0\n"
      "s_waitcnt lgkmcnt(2)\n"
      "v_permlane16_swap_b32 v120, v121\n"
      "v_mfma_f32_16x16x32_bf16 v[130:133], a[100:103], v[214:217], v[130:133]\n"
      "s_waitcnt lgkmcnt(1)\n"
      "v_add_f32 v120, v120, v121\n"
      "v_mov_b32 v121, v120\n"
      "v_mfma_f32_16x16x32_bf16 v[130:133], a[104:107], v[170:173], v[130:133]\n"
      "v_or3_b32 v190, v190, v191, v192\n"
      "v_permlane32_swap_b32 v120, v121\n"
      "v_mfma_f32_16x16x32_bf16 v[130:133], a[108:111], v[218:221], v[130:133]\n"
      "s_waitcnt lgkmcnt(0)\n"
      "v_add_f32 v120, v120, v121\n"
      "v_rsq_f32 v120, v120\n"
      "v_mfma_f32_16x16x32_bf16 v[130:133], a[112:115], v[182:185], v[130:133]\n"
      "v_mov_b32 v121, v120\n"
      "v_fma_f32 v76, v174, v174, s51\n"
      "v_mfma_f32_16x16x32_bf16 v[130:133], a[116:119], v[104:107], v[130:133]\n"
      "v_permlane16_swap_b32 v120, v121\n"
      "v_mul_f32 v120, s50, v120\n"
      "v_mul_f32 v202, v121, v202\n"
      "v_mul_f32 v203, v121, v203\n"
      "v_mul_f32 v194, v120, v194\n"
      "v_mul_f32 v195, v120, v195\n"
      "v_cvt_pk_bf16_f32 v202, v202, v203\n"
      "v_mul_f32 v204, v121, v204\n"
      "v_mul_f32 v205, v121, v205\n"
      "v_mul_f32 v108, v121, v108\n"
      "v_mul_f32 v109, v121, v109\n"
      "v_cvt_pk_bf16_f32 v194, v194, v195\n"
      "v_mul_f32 v196, v120, v196\n"
      "v_mul_f32 v197, v120, v197\n"
      "v_mul_f32 v134, v120, v134\n"
      "v_mul_f32 v135, v120, v135\n"
      "v_cvt_pk_bf16_f32 v203, v204, v205\n"
      "v_mul_f32 v116, v121, v116\n"
      "v_mul_f32 v117, v121, v117\n"
      "v_mul_f32 v118, v121, v118\n"
      "v_mul_f32 v119, v121, v119\n"
      "v_cvt_pk_bf16_f32 v108, v108, v109\n"
      "v_mul_f32 v110, v121, v110\n"
      "v_mul_f32 v111, v121, v111\n"
      "v_cvt_pk_bf16_f32 v195, v196, v197\n"
      "v_mul_f32 v158, v120, v158\n"
      "v_mul_f32 v159, v120, v159\n"
      "v_mul_f32 v160, v120, v160\n"
      "v_mul_f32 v161, v120, v161\n"
      "v_cvt_pk_bf16_f32 v134, v134, v135\n"
      "v_mul_f32 v136, v120, v136\n"
      "v_mul_f32 v137, v120, v137\n"
      "v_cvt_pk_bf16_f32 v204, v116, v117\n"
      "v_mfma_f32_16x16x32_bf16 v[114:117], a[120:123], v[166:169], 0\n"
      "v_cvt_pk_bf16_f32 v205, v118, v119\n"
      "v_cvt_pk_bf16_f32 v109, v110, v111\n"
      "v_mfma_f32_16x16x32_bf16 v[114:117], a[124:127], v[214:217], v[114:117]\n"
      "v_cvt_pk_bf16_f32 v196, v158, v159\n"
      "v_cvt_pk_bf16_f32 v197, v160, v161\n"
      "v_mfma_f32_16x16x32_bf16 v[114:117], a[128:131], v[170:173], v[114:117]\n"
      "v_cvt_pk_bf16_f32 v135, v136, v137\n"
      "v_fma_f32 v77, v198, v198, s51\n"
      "v_mfma_f32_16x16x32_bf16 v[114:117], a[132:135], v[218:221], v[114:117]\n"
      "v_fmac_f32 v76, v175, v175\n"
      "v_fmac_f32 v77, v199, v199\n"
      "v_mfma_f32_16x16x32_bf16 v[114:117], a[136:139], v[182:185], v[114:117]\n"
      "v_fmac_f32 v76, v176, v176\n"
      "v_fmac_f32 v77, v200, v200\n"
      "v_mfma_f32_16x16x32_bf16 v[114:117], a[140:143], v[104:107], v[114:117]\n"
      "v_fmac_f32 v76, v177, v177\n"
      "v_fmac_f32 v77, v201, v201\n"
      "v_mfma_f32_16x16x32_bf16 v[110:113], v[166:169], a[144:147], 0\n"
      "v_bfe_i32 v137, v36, 10, 1\n"
      "v_bfe_i32 v136, v36, 11, 1\n"
      "v_mfma_f32_16x16x32_bf16 v[110:113], v[214:217], a[148:151], v[110:113]\n"
      "v_fmac_f32 v76, v206, v206\n"
      "v_fmac_f32 v77, v186, v186\n"
      "v_mfma_f32_16x16x32_bf16 v[158:161], v[166:169], a[168:171], 0\n"
      "v_and_b32 v137, s93, v137\n"
      "v_and_b32 v136, s94, v136\n"
      "v_mfma_f32_16x16x32_bf16 v[158:161], v[214:217], a[172:175], v[158:161]\n"
      "v_fmac_f32 v76, v207, v207\n"
      "v_fmac_f32 v77, v187, v187\n"
      "v_mfma_f32_16x16x32_bf16 v[118:121], v[166:169], a[192:195], 0\n"
      "v_add_u32 v137, v137, v136\n"
      "v_fmac_f32 v76, v208, v208\n"
      "v_mfma_f32_16x16x32_bf16 v[118:121], v[214:217], a[196:199], v[118:121]\n"
      "ds_read_b128 v[166:169], v0 offset:20480\n"
      "v_fmac_f32 v77, v188, v188\n"
      "v_mfma_f32_16x16x32_bf16 v[110:113], v[170:173], a[152:155], v[110:113]\n"
      "v_sub_u32 v137, s92, v137\n"
      "v_fmac_f32_e64 v76, v209, v209\n"
      "v_mfma_f32_16x16x32_bf16 v[110:113], v[218:221], a[156:159], v[110:113]\n"
      "v_fmac_f32 v77, v189, v189\n"
      "v_add_u32 v137, v20, v137\n"
      "v_mfma_f32_16x16x32_bf16 v[158:161], v[170:173], a[176:179], v[158:161]\n"
      "buffer_store_dwordx4 v[150:153], v137, s[20:23], 0 offen sc1\n"
      "v_mfma_f32_16x16x32_bf16 v[158:161], v[218:221], a[180:183], v[158:161]\n"
      "s_nop 0\n"
      "ds_read_b128 v[150:153], v1 offset:20480\n"
      "v_fmac_f32 v76, v162, v162\n"
      "v_mfma_f32_16x16x32_bf16 v[118:121], v[170:173], a[200:203], v[118:121]\n"
      "s_waitcnt lgkmcnt(1)\n"
      "v_fmac_f32 v77, v210, v210\n"
      "v_fmac_f32 v76, v163, v163\n"
      "v_mfma_f32_16x16x32_bf16 v[118:121], v[218:221], a[204:207], v[118:121]\n"
      "ds_read_b128 v[218:221], v0 offset:26624\n"
      "v_fmac_f32 v77, v211, v211\n"
      "v_mfma_f32_16x16x32_bf16 v[110:113], v[182:185], a[160:163], v[110:113]\n"
      "ds_read_b128 v[170:173], v1 offset:26624\n"
      "v_fmac_f32 v76, v164, v164\n"
      "v_mfma_f32_16x16x32_bf16 v[110:113], v[104:107], a[164:167], v[110:113]\n"
      "v_fmac_f32 v77, v212, v212\n"
      "v_bfe_i32 v215, v36, 12, 1\n"
      "v_mfma_f32_16x16x32_bf16 v[158:161], v[182:185], a[184:187], v[158:161]\n"
      "v_bfe_i32 v217, v36, 13, 1\n"
      "v_fmac_f32_e64 v76, v165, v165\n"
      "v_mfma_f32_16x16x32_bf16 v[158:161], v[104:107], a[188:191], v[158:161]\n"
      "s_waitcnt lgkmcnt(2)\n"
      "v_fmac_f32 v77, v213, v213\n"
      "v_and_b32 v215, s93, v215\n"
      "v_mfma_f32_16x16x32_bf16 v[118:121], v[182:185], a[208:211], v[118:121]\n"
      "v_and_b32 v217, s94, v217\n"
      "v_add_u32 v215, v215, v217\n"
      "v_mfma_f32_16x16x32_bf16 v[118:121], v[104:107], a[212:215], v[118:121]\n"
      "ds_read_b128 v[182:185], v0 offset:32768\n"
      "v_sub_u32 v215, s92, v215\n"
      "v_mfma_f32_16x16x32_bf16 v[104:107], v[166:169], a[144:147], 0\n"
      "s_waitcnt lgkmcnt(1)\n"
      "v_permlane16_swap_b32 v76, v77\n"
      "v_mfma_f32_16x16x32_bf16 v[104:107], v[150:153], a[148:151], v[104:107]\n"
      "v_add_u32 v215, v21, v215\n"
      "buffer_store_dwordx4 v[100:103], v215, s[20:23], 0 offen sc1\n"
      "s_nop 1\n"
      "v_mfma_f32_16x16x32_bf16 v[100:103], v[166:169], a[168:171], 0\n"
      "v_add_f32 v76, v76, v77\n"
      "v_mov_b32 v77, v76\n"
      "v_mfma_f32_16x16x32_bf16 v[100:103], v[150:153], a[172:175], v[100:103]\n"
      "v_bfe_i32 v137, v36, 14, 1\n"
      "v_bfe_i32 v136, v36, 15, 1\n"
      "v_mfma_f32_16x16x32_bf16 v[214:217], v[166:169], a[192:195], 0\n"
      "s_waitcnt lgkmcnt(0)\n"
      "v_and_b32 v137, s93, v137\n"
      "v_and_b32_e64 v136, s94, v136\n"
      "v_mfma_f32_16x16x32_bf16 v[214:217], v[150:153], a[196:199], v[214:217]\n"
      "ds_read_b128 v[150:153], v1 offset:32768\n"
      "v_add_u32 v137, v137, v136\n"
      "v_mfma_f32_16x16x32_bf16 v[104:107], v[218:221], a[152:155], v[104:107]\n"
      "v_sub_u32 v137, s92, v137\n"
      "v_permlane32_swap_b32 v76, v77\n"
      "v_mfma_f32_16x16x32_bf16 v[104:107], v[170:173], a[156:159], v[104:107]\n"
      "v_add_u32 v137, v22, v137\n"
      "v_add_f32_e64 v76, v76, v77\n"
      "v_mfma_f32_16x16x32_bf16 v[100:103], v[218:221], a[176:179], v[100:103]\n"
      "v_rsq_f32 v76, v76\n"
      "v_mfma_f32_16x16x32_bf16 v[100:103], v[170:173], a[180:183], v[100:103]\n"
      "buffer_store_dwordx4 v[146:149], v137, s[20:23], 0 offen sc1\n"
      "v_mfma_f32_16x16x32_bf16 v[214:217], v[218:221], a[200:203], v[214:217]\n"
      "s_waitcnt lgkmcnt(0)\n"
      "v_bfe_i32 v81, v36, 16, 1\n"
      "v_bfe_i32 v220, v36, 17, 1\n"
      "v_mfma_f32_16x16x32_bf16 v[214:217], v[170:173], a[204:207], v[214:217]\n"
      "ds_read_b128 v[146:149], v10 offset:1200\n"
      "v_and_b32 v81, s93, v81\n"
      "v_mfma_f32_16x16x32_bf16 v[104:107], v[182:185], a[160:163], v[104:107]\n"
      "ds_read_b64 v[136:137], v10 offset:1192\n"
      "v_and_b32 v220, s94, v220\n"
      "v_mfma_f32_16x16x32_bf16 v[104:107], v[150:153], a[164:167], v[104:107]\n"
      "ds_read_b128 v[166:169], v0 offset:18432\n"
      "v_add_u32 v81, v81, v220\n"
      "v_mfma_f32_16x16x32_bf16 v[100:103], v[182:185], a[184:187], v[100:103]\n"
      "v_mov_b32 v77, v76\n"
      "v_sub_u32_e64 v81, s92, v81\n"
      "v_mfma_f32_16x16x32_bf16 v[100:103], v[150:153], a[188:191], v[100:103]\n"
      "v_add_u32 v81, v23, v81\n"
      "v_permlane16_swap_b32 v76, v77\n"
      "v_mfma_f32_16x16x32_bf16 v[214:217], v[182:185], a[208:211], v[214:217]\n"
      "ds_read_b128 v[220:223], v1 offset:18432\n"
      "v_bfe_i32 v173, v36, 18, 1\n"
      "v_mfma_f32_16x16x32_bf16 v[214:217], v[150:153], a[212:215], v[214:217]\n"
      "buffer_store_dwordx4 v[122:125], v81, s[20:23], 0 offen sc1\n"
      "v_mfma_f32_16x16x32_bf16 v[182:185], v[202:205], v[194:197], v[40:43]\n"
      "s_waitcnt lgkmcnt(1)\n"
      "v_bfe_i32 v123, v36, 19, 1\n"
      "v_and_b32 v173, s93, v173\n"
      "v_and_b32 v123, s94, v123\n"
      "v_mul_f32 v76, s50, v76\n"
      "v_add_u32 v173, v173, v123\n"
      "v_mul_f32 v198, v77, v198\n"
      "v_mul_f32 v199, v77, v199\n"
      "v_mul_f32 v174, v76, v174\n"
      "v_mul_f32 v175, v76, v175\n"
      "v_sub_u32 v173, s92, v173\n"
      "v_cvt_pk_bf16_f32 v198, v198, v199\n"
      "v_mul_f32 v200, v77, v200\n"
      "v_mul_f32 v201, v77, v201\n"
      "v_mul_f32 v210, v77, v210\n"
      "v_mul_f32 v211, v77, v211\n"
      "v_cvt_pk_bf16_f32 v174, v174, v175\n"
      "v_mul_f32 v176, v76, v176\n"
      "v_mul_f32 v177, v76, v177\n"
      "v_mul_f32 v162, v76, v162\n"
      "v_mul_f32 v163, v76, v163\n"
      "v_add_u32 v173, v25, v173\n"
      "v_cvt_pk_bf16_f32 v199, v200, v201\n"
      "v_mul_f32 v186, v77, v186\n"
      "v_mul_f32 v187, v77, v187\n"
      "v_mul_f32 v188, v77, v188\n"
      "v_mul_f32_e64 v189, v77, v189\n"
      "v_cvt_pk_bf16_f32 v210, v210, v211\n"
      "v_mul_f32 v212, v77, v212\n"
      "v_mul_f32 v213, v77, v213\n"
      "v_cvt_pk_bf16_f32 v175, v176, v177\n"
      "v_mul_f32 v206, v76, v206\n"
      "v_mul_f32 v207, v76, v207\n"
      "v_mul_f32 v208, v76, v208\n"
      "v_mul_f32 v209, v76, v209\n"
      "v_cvt_pk_bf16_f32 v162, v162, v163\n"
      "v_mul_f32 v164, v76, v164\n"
      "v_mul_f32 v165, v76, v165\n"
      "buffer_store_dwordx4 v[126:129], v173, s[20:23], 0 offen sc1\n"
      "v_cvt_pk_bf16_f32 v200, v186, v187\n"
      "v_cvt_pk_bf16_f32 v201, v188, v189\n"
      "v_cvt_pk_bf16_f32 v211, v212, v213\n"
      "v_cvt_pk_bf16_f32 v176, v206, v207\n"
      "v_mfma_f32_16x16x32_bf16 v[122:125], v[198:201], v[194:197], v[44:47]\n"
      "ds_read_b128 v[150:153], v0 offset:24576\n"
      "v_cvt_pk_bf16_f32 v177, v208, v209\n"
      "v_cvt_pk_bf16_f32 v163, v164, v165\n"
      "v_fma_f32 v76, v178, v178, s51\n"
      "v_fma_f32 v127, v142, v142, s51\n"
      "v_fmac_f32 v76, v179, v179\n"
      "v_fmac_f32 v127, v143, v143\n"
      "v_fmac_f32 v76, v180, v180\n"
      "v_fmac_f32 v127, v144, v144\n"
      "v_fmac_f32 v76, v181, v181\n"
      "v_fmac_f32 v127, v145, v145\n"
      "v_fmac_f32 v76, v96, v96\n"
      "v_fmac_f32 v127, v130, v130\n"
      "v_fmac_f32 v76, v97, v97\n"
      "v_fmac_f32 v127, v131, v131\n"
      "v_fmac_f32 v76, v98, v98\n"
      "v_fmac_f32 v127, v132, v132\n"
      "v_fmac_f32 v76, v99, v99\n"
      "v_fmac_f32 v127, v133, v133\n"
      "v_fmac_f32 v76, v138, v138\n"
      "v_fmac_f32 v127, v114, v114\n"
      "v_fmac_f32 v76, v139, v139\n"
      "v_fmac_f32 v127, v115, v115\n"
      "v_fmac_f32 v76, v140, v140\n"
      "v_fmac_f32 v127, v116, v116\n"
      "v_fmac_f32 v76, v141, v141\n"
      "v_fmac_f32 v127, v117, v117\n"
      "v_subrev_u32 v88, 115456, v9\n"
      "v_bfe_u32 v213, v88, 4, 4\n"
      "v_permlane16_swap_b32 v76, v127\n"
      "v_add_f32 v76, v76, v127\n"
      "v_mov_b32 v127, v76\n"
      "v_mul_u32_u24 v213, 98, v213\n"
      "v_lshrrev_b32 v88, 8, v88\n"
      "v_permlane32_swap_b32 v76, v127\n"
      "v_add_f32 v76, v76, v127\n"
      "v_rsq_f32_e64 v76, v76\n"
      "v_lshl_add_u32 v213, v88, 3, v213\n"
      "v_mov_b32 v127, v76\n"
      "v_add_u32 v213, s77, v213\n"
      "v_and_b32 v89, 2, v213\n"
      "v_permlane16_swap_b32 v76, v127\n"
      "v_mul_f32 v142, v127, v142\n"
      "v_mul_f32 v143, v127, v143\n"
      "v_cvt_pk_bf16_f32 v142, v142, v143\n"
      "v_mul_f32 v144, v127, v144\n"
      "v_mul_f32 v145, v127, v145\n"
      "v_cvt_pk_bf16_f32 v143, v144, v145\n"
      "v_mul_f32 v130, v127, v130\n"
      "v_mul_f32 v131, v127, v131\n"
      "v_mul_f32 v132, v127, v132\n"
      "v_mul_f32 v133, v127, v133\n"
      "v_cvt_pk_bf16_f32 v144, v130, v131\n"
      "v_cvt_pk_bf16_f32 v145, v132, v133\n"
      "v_mul_f32 v76, s50, v76\n"
      "v_mul_f32 v178, v76, v178\n"
      "v_mfma_f32_16x16x32_bf16 v[186:189], v[142:145], v[194:197], v[48:51]\n"
      "ds_read_b128 v[206:209], v1 offset:24576\n"
      "v_mul_f32 v179, v76, v179\n"
      "v_mfma_f32_16x16x32_bf16 v[170:173], v[146:149], v[194:197], v[24:27]\n"
      "s_waitcnt lgkmcnt(1)\n"
      "v_mul_f32 v114, v127, v114\n"
      "v_mul_f32 v115, v127, v115\n"
      "v_mfma_f32_16x16x16_bf16 v[182:185], v[108:109], v[134:135], v[182:185]\n"
      "v_cvt_pk_bf16_f32 v178, v178, v179\n"
      "v_mul_f32 v180, v76, v180\n"
      "v_mul_f32 v181, v76, v181\n"
      "v_mfma_f32_16x16x16_bf16 v[122:125], v[210:211], v[134:135], v[122:125]\n"
      "v_mul_f32 v138, v76, v138\n"
      "v_mul_f32 v139, v76, v139\n"
      "v_cvt_pk_bf16_f32 v114, v114, v115\n"
      "v_mul_f32 v116, v127, v116\n"
      "v_mul_f32 v117, v127, v117\n"
      "v_cvt_pk_bf16_f32 v179, v180, v181\n"
      "v_mul_f32 v96, v76, v96\n"
      "v_mul_f32 v97, v76, v97\n"
      "v_mul_f32 v98, v76, v98\n"
      "v_mul_f32 v99, v76, v99\n"
      "v_cvt_pk_bf16_f32 v138, v138, v139\n"
      "v_mul_f32_e64 v140, v76, v140\n"
      "v_mul_f32_e64 v141, v76, v141\n"
      "v_cvt_pk_bf16_f32 v115, v116, v117\n"
      "v_cvt_pk_bf16_f32 v180, v96, v97\n"
      "v_cvt_pk_bf16_f32 v181, v98, v99\n"
      "v_mfma_f32_16x16x16_bf16 v[186:189], v[114:115], v[134:135], v[186:189]\n"
      "v_cvt_pk_bf16_f32 v139, v140, v141\n"
      "v_and_b32 v213, -4, v213\n"
      "v_add_u32 v213, 110592, v213\n"
      "v_mfma_f32_16x16x16_bf16 v[170:173], v[136:137], v[134:135], v[170:173]\n"
      "ds_read_b128 v[96:99], v0 offset:30720\n"
      "v_cvt_pk_bf16_f32 v107, v106, v107\n"
      "v_mfma_f32_16x16x32_bf16 v[194:197], v[166:169], a[144:147], 0\n"
      "ds_read_b128 v[132:135], v1 offset:30720\n"
      "v_cvt_pk_bf16_f32 v106, v104, v105\n"
      "v_mfma_f32_16x16x32_bf16 v[194:197], v[220:223], a[148:151], v[194:197]\n"
      "s_waitcnt lgkmcnt(2)\n"
      "v_cvt_pk_bf16_f32 v76, v110, v111\n"
      "v_cvt_pk_bf16_f32 v77, v112, v113\n"
      "v_mfma_f32_16x16x32_bf16 v[126:129], v[166:169], a[168:171], 0\n"
      "v_cvt_pk_bf16_f32 v103, v102, v103\n"
      "v_cvt_pk_bf16_f32 v102, v100, v101\n"
      "v_mfma_f32_16x16x32_bf16 v[126:129], v[220:223], a[172:175], v[126:129]\n"
      "v_cvt_pk_bf16_f32 v80, v158, v159\n"
      "v_cvt_pk_bf16_f32 v81, v160, v161\n"
      "v_mfma_f32_16x16x32_bf16 v[158:161], v[166:169], a[192:195], 0\n"
      "v_cvt_pk_bf16_f32 v217, v216, v217\n"
      "v_cvt_pk_bf16_f32 v216, v214, v215\n"
      "v_mfma_f32_16x16x32_bf16 v[158:161], v[220:223], a[196:199], v[158:161]\n"
      "s_waitcnt lgkmcnt(1)\n"
      "v_cvt_pk_bf16_f32 v84, v118, v119\n"
      "v_cvt_pk_bf16_f32 v85, v120, v121\n"
      "v_mfma_f32_16x16x32_bf16 v[194:197], v[150:153], a[152:155], v[194:197]\n"
      "s_waitcnt lgkmcnt(0)\n"
      "v_subrev_u32 v191, 115456, v9\n"
      "v_bfe_u32 v131, v191, 4, 4\n"
      "v_mfma_f32_16x16x32_bf16 v[194:197], v[206:209], a[156:159], v[194:197]\n"
      "v_mul_u32_u24 v131, 98, v131\n"
      "v_lshrrev_b32 v191, 8, v191\n"
      "v_mfma_f32_16x16x32_bf16 v[126:129], v[150:153], a[176:179], v[126:129]\n"
      "v_lshl_add_u32 v131, v191, 3, v131\n"
      "v_add_u32 v131, s77, v131\n"
      "v_mfma_f32_16x16x32_bf16 v[126:129], v[206:209], a[180:183], v[126:129]\n"
      "v_and_b32 v192, 2, v131\n"
      "v_and_b32 v131, -4, v131\n"
      "v_mfma_f32_16x16x32_bf16 v[158:161], v[150:153], a[200:203], v[158:161]\n"
      "v_add_u32 v131, 112160, v131\n"
      "v_subrev_u32 v151, 115456, v9\n"
      "v_mfma_f32_16x16x32_bf16 v[158:161], v[206:209], a[204:207], v[158:161]\n"
      "v_bfe_u32 v150, v151, 4, 4\n"
      "v_mul_u32_u24 v150, 98, v150\n"
      "v_mfma_f32_16x16x32_bf16 v[194:197], v[96:99], a[160:163], v[194:197]\n"
      "v_lshrrev_b32 v151, 8, v151\n"
      "v_lshl_add_u32 v150, v151, 3, v150\n"
      "v_mfma_f32_16x16x32_bf16 v[194:197], v[132:135], a[164:167], v[194:197]\n"
      "ds_read_b128 v[166:169], v2\n"
      "v_add_u32 v150, s77, v150\n"
      "v_mfma_f32_16x16x32_bf16 v[126:129], v[96:99], a[184:187], v[126:129]\n"
      "v_and_b32 v219, 2, v150\n"
      "v_and_b32 v150, -4, v150\n"
      "v_mfma_f32_16x16x32_bf16 v[126:129], v[132:135], a[188:191], v[126:129]\n"
      "ds_read2_b32 v[208:209], v213 offset0:0 offset1:1\n"
      "ds_read2_b32 v[206:207], v213 offset0:8 offset1:9\n"
      "v_mfma_f32_16x16x32_bf16 v[158:161], v[96:99], a[208:211], v[158:161]\n"
      "ds_read2_b32 v[220:221], v213 offset0:2 offset1:10\n"
      "ds_read2_b32 v[152:153], v213 offset0:16 offset1:17\n"
      "v_mfma_f32_16x16x32_bf16 v[158:161], v[132:135], a[212:215], v[158:161]\n"
      "ds_read2_b32 v[172:173], v213 offset0:18 offset1:24\n"
      "ds_read_b128 v[118:121], v2 offset:3072\n"
      "v_mfma_f32_16x16x32_bf16 v[96:99], v[202:205], v[174:177], v[52:55]\n"
      "v_cvt_pk_bf16_f32 v104, v194, v195\n"
      "v_cvt_pk_bf16_f32 v105, v196, v197\n"
      "v_mfma_f32_16x16x32_bf16 v[194:197], v[198:201], v[174:177], v[56:59]\n"
      "ds_read_b128 v[132:135], v2 offset:6144\n"
      "v_cvt_pk_bf16_f32 v100, v126, v127\n"
      "v_mfma_f32_16x16x32_bf16 v[110:113], v[142:145], v[174:177], v[60:63]\n"
      "s_waitcnt lgkmcnt(7)\n"
      "s_waitcnt lgkmcnt(6)\n"
      "v_alignbyte_b32 v208, v209, v208, v89\n"
      "v_mfma_f32_16x16x32_bf16 v[212:215], v[146:149], v[174:177], v[26:29]\n"
      "s_waitcnt lgkmcnt(5)\n"
      "v_alignbyte_b32 v206, v207, v206, v89\n"
      "s_waitcnt lgkmcnt(4)\n"
      "v_alignbyte_b32 v209, v220, v209, v89\n"
      "v_mfma_f32_16x16x16_bf16 v[96:99], v[108:109], v[162:163], v[96:99]\n"
      "v_lshlrev_b32 v220, 16, v208\n"
      "v_and_b32 v222, 0xffff0000, v208\n"
      "v_lshlrev_b32 v208, 16, v209\n"
      "v_mfma_f32_16x16x16_bf16 v[194:197], v[210:211], v[162:163], v[194:197]\n"
      "v_alignbyte_b32 v207, v221, v207, v89\n"
      "s_waitcnt lgkmcnt(2)\n"
      "v_alignbyte_b32 v152, v153, v152, v89\n"
      "v_mfma_f32_16x16x16_bf16 v[110:113], v[114:115], v[162:163], v[110:113]\n"
      "v_fmac_f32 v182, s53, v220\n"
      "v_fmac_f32 v183, s53, v222\n"
      "v_fmac_f32 v184, s53, v208\n"
      "v_mfma_f32_16x16x16_bf16 v[212:215], v[136:137], v[162:163], v[212:215]\n"
      "v_and_b32 v101, 0xffff0000, v209\n"
      "v_lshlrev_b32_e64 v177, 16, v206\n"
      "v_and_b32 v175, 0xffff0000, v206\n"
      "v_mfma_f32_16x16x32_bf16 v[220:223], a[216:219], v[166:169], 0\n"
      "s_waitcnt lgkmcnt(0)\n"
      "v_lshlrev_b32 v174, 16, v207\n"
      "v_and_b32 v176, 0xffff0000, v207\n"
      "v_mfma_f32_16x16x32_bf16 v[206:209], a[240:243], v[166:169], 0\n"
      "ds_read_u16 v78, v11 offset:1168\n"
      "v_alignbyte_b32 v153, v172, v153, v89\n"
      "v_mfma_f32_16x16x32_bf16 v[162:165], v[232:235], v[166:169], 0\n"
      "ds_read_u16 v82, v11 offset:1200\n"
      "v_fmac_f32 v185, s53, v101\n"
      "v_mfma_f32_16x16x32_bf16 v[220:223], a[220:223], v[118:121], v[220:223]\n"
      "ds_read_u16 v86, v11 offset:1232\n"
      "v_fmac_f32 v122, s53, v177\n"
      "v_mfma_f32_16x16x32_bf16 v[206:209], a[244:247], v[118:121], v[206:209]\n"
      "v_fmac_f32 v123, s53, v175\n"
      "v_fmac_f32 v124, s53, v174\n"
      "v_mfma_f32_16x16x32_bf16 v[162:165], v[236:239], v[118:121], v[162:165]\n"
      "v_fmac_f32 v125, s53, v176\n"
      "v_lshlrev_b32 v101, 16, v152\n"
      "v_mfma_f32_16x16x32_bf16 v[220:223], a[224:227], v[132:135], v[220:223]\n"
      "v_and_b32 v214, 0xffff0000, v152\n"
      "v_lshlrev_b32_e64 v215, 16, v153\n"
      "v_mfma_f32_16x16x32_bf16 v[206:209], a[248:251], v[132:135], v[206:209]\n"
      "s_waitcnt lgkmcnt(2)\n"
      "v_and_b32 v176, 0xffff0000, v153\n"
      "v_alignbyte_b32 v88, v173, v173, v89\n"
      "v_mfma_f32_16x16x32_bf16 v[162:165], v[240:243], v[132:135], v[162:165]\n"
      "s_waitcnt lgkmcnt(1)\n"
      "v_max3_f32 v89, v182, v183, v184\n"
      "v_fmac_f32 v186, s53, v101\n"
      "v_fmac_f32 v187, s53, v214\n"
      "v_fmac_f32 v188, s53, v215\n"
      "v_fmac_f32 v189, s53, v176\n"
      "v_lshlrev_b32_e64 v88, 16, v88\n"
      "v_max3_f32 v101, v185, v122, v123\n"
      "v_max3_f32 v89, v89, v124, v125\n"
      "v_fmac_f32_e64 v170, s53, v88\n"
      "v_max3_f32 v101, v101, v186, v187\n"
      "v_max3_f32 v89, v89, v188, v189\n"
      "v_max3_f32 v89, v89, v101, v170\n"
      "v_mov_b32_e64 v176, v89\n"
      "v_cvt_pk_bf16_f32 v101, v128, v129\n"
      "v_cvt_pk_bf16_f32 v214, v158, v159\n"
      "v_permlane16_swap_b32 v89, v176\n"
      "v_max_f32 v89, v89, v176\n"
      "v_mov_b32_e64 v176, v89\n"
      "v_cvt_pk_bf16_f32 v215, v160, v161\n"
      "v_add_u32 v150, 113728, v150\n"
      "v_permlane32_swap_b32 v89, v176\n"
      "v_max_f32 v89, v89, v176\n"
      "v_sub_f32 v182, v182, v89\n"
      "v_sub_f32 v183, v183, v89\n"
      "v_sub_f32 v184, v184, v89\n"
      "v_sub_f32 v185, v185, v89\n"
      "v_exp_f32 v182, v182\n"
      "v_exp_f32 v183, v183\n"
      "v_sub_f32 v122, v122, v89\n"
      "v_sub_f32 v123, v123, v89\n"
      "v_sub_f32 v124, v124, v89\n"
      "v_sub_f32 v125, v125, v89\n"
      "v_exp_f32 v184, v184\n"
      "v_exp_f32 v185, v185\n"
      "v_exp_f32 v122, v122\n"
      "v_exp_f32 v123, v123\n"
      "v_exp_f32 v124, v124\n"
      "v_exp_f32 v125, v125\n"
      "v_cvt_pk_bf16_f32 v182, v182, v183\n"
      "v_cvt_pk_bf16_f32 v183, v184, v185\n"
      "v_cvt_pk_bf16_f32 v184, v122, v123\n"
      "v_cvt_pk_bf16_f32 v185, v124, v125\n"
      "v_sub_f32 v186, v186, v89\n"
      "v_sub_f32 v187, v187, v89\n"
      "v_mfma_f32_16x16x32_bf16 v[174:177], v[32:35], v[182:185], 0\n"
      "s_waitcnt lgkmcnt(0)\n"
      "v_sub_f32 v188, v188, v89\n"
      "v_sub_f32 v189, v189, v89\n"
      "v_sub_f32 v170, v170, v89\n"
      "v_exp_f32 v186, v186\n"
      "v_exp_f32 v187, v187\n"
      "v_exp_f32_e64 v188, v188\n"
      "v_exp_f32_e64 v189, v189\n"
      "v_exp_f32_e64 v170, v170\n"
      "v_cvt_pk_bf16_f32 v88, v186, v187\n"
      "v_cvt_pk_bf16_f32 v89, v188, v189\n"
      "v_cvt_pk_bf16_f32 v90, v170, 0\n"
      "v_subrev_u32 v218, 115456, v9\n"
      "v_lshrrev_b32_e64 v218, 3, v218\n"
      "v_mfma_f32_16x16x32_bf16 v[174:177], v[32:35], v[88:91], v[174:177]\n"
      "v_min_u32 v218, 96, v218\n"
      "v_add_u32_e64 v218, s77, v218\n"
      "v_mfma_f32_16x16x32_bf16 v[124:127], v[104:107], v[182:185], 0\n"
      "v_add_u32 v218, 115296, v218\n"
      "s_waitcnt vmcnt(6)\n"
      "v_or3_b32 v154, v154, v155, v156\n"
      "v_mfma_f32_16x16x32_bf16 v[124:127], v[76:79], v[88:91], v[124:127]\n"
      "ds_read_b128 v[166:169], v2 offset:9216\n"
      "s_waitcnt vmcnt(5)\n"
      "v_or3_b32 v154, v154, v157, v93\n"
      "v_mfma_f32_16x16x32_bf16 v[186:189], v[100:103], v[182:185], 0\n"
      "v_rcp_f32_e64 v174, v174\n"
      "v_mfma_f32_16x16x32_bf16 v[186:189], v[80:83], v[88:91], v[186:189]\n"
      "v_and_b32 v154, 0x7fff7fff, v154\n"
      "v_readlane_b32 s45, v37, s43\n"
      "v_mfma_f32_16x16x32_bf16 v[132:135], v[214:217], v[182:185], 0\n"
      "ds_read_b128 v[182:185], v2 offset:12288\n"
      "v_mul_f32 v124, v174, v124\n"
      "v_mfma_f32_16x16x32_bf16 v[132:135], v[84:87], v[88:91], v[132:135]\n"
      "ds_write_b32 v31, v154 offset:1024\n"
      "v_mfma_f32_16x16x32_bf16 v[120:123], v[202:205], v[178:181], v[64:67]\n"
      "v_mul_f32 v125, v174, v125\n"
      "v_cvt_pk_bf16_f32 v124, v124, v125\n"
      "v_mfma_f32_16x16x32_bf16 v[152:155], v[198:201], v[178:181], v[68:71]\n"
      "ds_read_b128 v[170:173], v2 offset:15360\n"
      "v_mul_f32_e64 v126, v174, v126\n"
      "v_mfma_f32_16x16x32_bf16 v[116:119], v[142:145], v[178:181], v[72:75]\n"
      "s_waitcnt lgkmcnt(3)\n"
      "v_mul_f32 v127, v174, v127\n"
      "v_cvt_pk_bf16_f32 v125, v126, v127\n"
      "v_mfma_f32_16x16x32_bf16 v[156:159], v[146:149], v[178:181], v[28:31]\n"
      "v_mul_f32 v186, v174, v186\n"
      "v_mul_f32 v187, v174, v187\n"
      "v_mfma_f32_16x16x16_bf16 v[120:123], v[108:109], v[138:139], v[120:123]\n"
      "v_mul_f32_e64 v188, v174, v188\n"
      "v_mul_f32_e64 v189, v174, v189\n"
      "v_cvt_pk_bf16_f32 v126, v186, v187\n"
      "v_mfma_f32_16x16x16_bf16 v[152:155], v[210:211], v[138:139], v[152:155]\n"
      "v_cvt_pk_bf16_f32 v127, v188, v189\n"
      "ds_read_b128 v[176:179], v2 offset:256\n"
      "v_mfma_f32_16x16x16_bf16 v[116:119], v[114:115], v[138:139], v[116:119]\n"
      "v_mul_f32 v132, v174, v132\n"
      "v_mul_f32_e64 v133, v174, v133\n"
      "v_cvt_pk_bf16_f32 v132, v132, v133\n"
      "v_mfma_f32_16x16x16_bf16 v[156:159], v[136:137], v[138:139], v[156:159]\n"
      "ds_read2_b32 v[180:181], v131 offset0:0 offset1:1\n"
      "ds_read2_b32 v[160:161], v131 offset0:8 offset1:9\n"
      "v_mfma_f32_16x16x32_bf16 v[220:223], a[228:231], v[166:169], v[220:223]\n"
      "s_waitcnt lgkmcnt(3)\n"
      "ds_read2_b32 v[138:139], v131 offset0:2 offset1:10\n"
      "v_mfma_f32_16x16x32_bf16 v[206:209], a[252:255], v[166:169], v[206:209]\n"
      "ds_write_b128 v3, v[124:127] offset:18432\n"
      "v_mfma_f32_16x16x32_bf16 v[162:165], v[244:247], v[166:169], v[162:165]\n"
      "ds_read2_b32 v[140:141], v131 offset0:16 offset1:17\n"
      "ds_read2_b32 v[88:89], v131 offset0:18 offset1:24\n"
      "v_mfma_f32_16x16x32_bf16 v[220:223], a[232:235], v[182:185], v[220:223]\n"
      "ds_read_b128 v[186:189], v2 offset:3328\n"
      "s_waitcnt lgkmcnt(6)\n"
      "v_alignbyte_b32 v180, v181, v180, v192\n"
      "v_mfma_f32_16x16x32_bf16 v[206:209], v[224:227], v[182:185], v[206:209]\n"
      "s_waitcnt lgkmcnt(5)\n"
      "v_alignbyte_b32 v160, v161, v160, v192\n"
      "v_lshlrev_b32 v169, 16, v180\n"
      "v_mfma_f32_16x16x32_bf16 v[162:165], v[248:251], v[182:185], v[162:165]\n"
      "ds_read_b128 v[128:131], v2 offset:6400\n"
      "s_waitcnt lgkmcnt(5)\n"
      "v_alignbyte_b32 v181, v138, v181, v192\n"
      "v_mfma_f32_16x16x32_bf16 v[220:223], a[236:239], v[170:173], v[220:223]\n"
      "v_and_b32 v166, 0xffff0000, v180\n"
      "v_lshlrev_b32_e64 v180, 16, v181\n"
      "v_mfma_f32_16x16x32_bf16 v[206:209], v[228:231], v[170:173], v[206:209]\n"
      "v_alignbyte_b32 v161, v139, v161, v192\n"
      "s_waitcnt lgkmcnt(3)\n"
      "v_alignbyte_b32 v140, v141, v140, v192\n"
      "v_mfma_f32_16x16x32_bf16 v[162:165], v[252:255], v[170:173], v[162:165]\n"
      "s_waitcnt lgkmcnt(2)\n"
      "v_fmac_f32 v96, s53, v169\n"
      "v_fmac_f32 v97, s53, v166\n"
      "v_mfma_f32_16x16x32_bf16 v[166:169], a[216:219], v[176:179], 0\n"
      "s_waitcnt lgkmcnt(1)\n"
      "v_fmac_f32 v98, s53, v180\n"
      "v_and_b32 v158, 0xffff0000, v181\n"
      "v_mfma_f32_16x16x32_bf16 v[180:183], a[240:243], v[176:179], 0\n"
      "v_alignbyte_b32 v141, v88, v141, v192\n"
      "v_lshlrev_b32 v159, 16, v160\n"
      "v_mfma_f32_16x16x32_bf16 v[124:127], v[232:235], v[176:179], 0\n"
      "s_waitcnt lgkmcnt(0)\n"
      "v_and_b32 v176, 0xffff0000, v160\n"
      "v_fmac_f32 v99, s53, v158\n"
      "v_mfma_f32_16x16x32_bf16 v[166:169], a[220:223], v[186:189], v[166:169]\n"
      "v_lshlrev_b32 v179, 16, v161\n"
      "v_and_b32 v178, 0xffff0000, v161\n"
      "v_mfma_f32_16x16x32_bf16 v[180:183], a[244:247], v[186:189], v[180:183]\n"
      "v_fmac_f32 v194, s53, v159\n"
      "v_fmac_f32 v195, s53, v176\n"
      "v_mfma_f32_16x16x32_bf16 v[124:127], v[236:239], v[186:189], v[124:127]\n"
      "ds_read_b128 v[158:161], v10 offset:1168\n"
      "v_fmac_f32 v196, s53, v179\n"
      "v_mfma_f32_16x16x32_bf16 v[166:169], a[224:227], v[128:131], v[166:169]\n"
      "v_fmac_f32 v197, s53, v178\n"
      "v_lshlrev_b32_e64 v178, 16, v140\n"
      "v_mfma_f32_16x16x32_bf16 v[180:183], a[248:251], v[128:131], v[180:183]\n"
      "v_and_b32 v176, 0xffff0000, v140\n"
      "v_lshlrev_b32_e64 v179, 16, v141\n"
      "v_mfma_f32_16x16x32_bf16 v[124:127], v[240:243], v[128:131], v[124:127]\n"
      "ds_read_b64 v[188:189], v10 offset:1184\n"
      "v_and_b32 v187, 0xffff0000, v141\n"
      "v_alignbyte_b32 v191, v89, v89, v192\n"
      "v_max3_f32 v177, v96, v97, v98\n"
      "v_fmac_f32 v110, s53, v178\n"
      "v_fmac_f32 v111, s53, v176\n"
      "v_fmac_f32 v112, s53, v179\n"
      "v_fmac_f32 v113, s53, v187\n"
      "v_lshlrev_b32 v191, 16, v191\n"
      "v_max3_f32 v192, v99, v194, v195\n"
      "v_max3_f32 v177, v177, v196, v197\n"
      "v_fmac_f32 v212, s53, v191\n"
      "v_max3_f32 v192, v192, v110, v111\n"
      "v_max3_f32 v177, v177, v112, v113\n"
      "v_max3_f32 v177, v177, v192, v212\n"
      "v_mov_b32 v179, v177\n"
      "v_mul_f32 v192, v220, v220\n"
      "v_add_f32 v191, v220, v221\n"
      "v_permlane16_swap_b32 v177, v179\n"
      "v_max_f32 v177, v177, v179\n"
      "v_mov_b32 v179, v177\n"
      "v_fmac_f32 v192, v221, v221\n"
      "v_add_f32 v191, v191, v222\n"
      "v_permlane32_swap_b32 v177, v179\n"
      "v_max_f32 v177, v177, v179\n"
      "v_sub_f32 v96, v96, v177\n"
      "v_sub_f32 v97, v97, v177\n"
      "v_sub_f32 v98, v98, v177\n"
      "v_sub_f32 v99, v99, v177\n"
      "v_exp_f32 v96, v96\n"
      "v_exp_f32 v97, v97\n"
      "v_sub_f32 v194, v194, v177\n"
      "v_sub_f32 v195, v195, v177\n"
      "v_sub_f32 v196, v196, v177\n"
      "v_sub_f32 v197, v197, v177\n"
      "v_exp_f32 v98, v98\n"
      "v_exp_f32 v99, v99\n"
      "v_exp_f32 v194, v194\n"
      "v_exp_f32 v195, v195\n"
      "v_exp_f32 v196, v196\n"
      "v_exp_f32 v197, v197\n"
      "v_cvt_pk_bf16_f32 v96, v96, v97\n"
      "v_cvt_pk_bf16_f32 v97, v98, v99\n"
      "v_cvt_pk_bf16_f32 v98, v194, v195\n"
      "v_cvt_pk_bf16_f32 v99, v196, v197\n"
      "v_sub_f32 v110, v110, v177\n"
      "v_sub_f32 v111, v111, v177\n"
      "v_mfma_f32_16x16x32_bf16 v[194:197], v[32:35], v[96:99], 0\n"
      "s_waitcnt lgkmcnt(0)\n"
      "v_sub_f32 v112, v112, v177\n"
      "v_sub_f32 v113, v113, v177\n"
      "v_sub_f32 v212, v212, v177\n"
      "v_exp_f32 v110, v110\n"
      "v_exp_f32 v111, v111\n"
      "v_exp_f32 v112, v112\n"
      "v_exp_f32 v113, v113\n"
      "v_exp_f32_e64 v212, v212\n"
      "v_cvt_pk_bf16_f32 v92, v110, v111\n"
      "v_cvt_pk_bf16_f32 v93, v112, v113\n"
      "v_cvt_pk_bf16_f32 v94, v212, 0\n"
      "v_fmac_f32 v192, v222, v222\n"
      "v_add_f32 v191, v191, v223\n"
      "v_mfma_f32_16x16x32_bf16 v[194:197], v[32:35], v[92:95], v[194:197]\n"
      "v_fmac_f32 v192, v223, v223\n"
      "v_add_f32 v191, v191, v206\n"
      "v_mfma_f32_16x16x32_bf16 v[176:179], v[104:107], v[96:99], 0\n"
      "v_fmac_f32 v192, v206, v206\n"
      "v_add_f32 v191, v191, v207\n"
      "v_mfma_f32_16x16x32_bf16 v[176:179], v[76:79], v[92:95], v[176:179]\n"
      "ds_read_b128 v[184:187], v2 offset:9472\n"
      "v_fmac_f32 v192, v207, v207\n"
      "v_mfma_f32_16x16x32_bf16 v[170:173], v[100:103], v[96:99], 0\n"
      "v_add_f32 v191, v191, v208\n"
      "v_fmac_f32_e64 v192, v208, v208\n"
      "v_mfma_f32_16x16x32_bf16 v[170:173], v[80:83], v[92:95], v[170:173]\n"
      "ds_read2_b32 v[196:197], v150 offset0:0 offset1:1\n"
      "v_add_f32 v191, v191, v209\n"
      "v_mfma_f32_16x16x32_bf16 v[110:113], v[214:217], v[96:99], 0\n"
      "v_fmac_f32 v192, v209, v209\n"
      "v_add_f32 v191, v191, v162\n"
      "v_mfma_f32_16x16x32_bf16 v[110:113], v[84:87], v[92:95], v[110:113]\n"
      "ds_read2_b32 v[88:89], v150 offset0:8 offset1:9\n"
      "v_fmac_f32 v192, v162, v162\n"
      "v_mfma_f32_16x16x32_bf16 v[96:99], v[158:161], v[202:205], v[30:33]\n"
      "v_add_f32 v191, v191, v163\n"
      "v_fmac_f32_e64 v192, v163, v163\n"
      "v_mfma_f32_16x16x32_bf16 v[128:131], v[158:161], v[198:201], v[30:33]\n"
      "s_waitcnt lgkmcnt(2)\n"
      "v_add_f32_e64 v191, v191, v164\n"
      "v_fmac_f32_e64 v192, v164, v164\n"
      "v_mfma_f32_16x16x32_bf16 v[202:205], v[158:161], v[142:145], v[30:33]\n"
      "v_add_f32_e64 v191, v191, v165\n"
      "v_fmac_f32_e64 v192, v165, v165\n"
      "v_mfma_f32_16x16x32_bf16 v[138:141], v[158:161], v[146:149], v[30:33]\n"
      "ds_read2_b32 v[198:199], v150 offset0:2 offset1:10\n"
      "ds_read2_b32 v[160:161], v150 offset0:16 offset1:17\n"
      "v_mfma_f32_16x16x16_bf16 v[96:99], v[188:189], v[108:109], v[96:99]\n"
      "ds_read2_b32 v[158:159], v150 offset0:18 offset1:24\n"
      "ds_read_b128 v[142:145], v2 offset:12544\n"
      "v_mfma_f32_16x16x16_bf16 v[128:131], v[188:189], v[210:211], v[128:131]\n"
      "ds_read_b128 v[210:213], v2 offset:15616\n"
      "s_waitcnt lgkmcnt(5)\n"
      "v_alignbyte_b32 v196, v197, v196, v219\n"
      "v_mfma_f32_16x16x16_bf16 v[202:205], v[188:189], v[114:115], v[202:205]\n"
      "v_alignbyte_b32 v88, v89, v88, v219\n"
      "v_lshlrev_b32 v109, 16, v196\n"
      "v_and_b32 v108, 0xffff0000, v196\n"
      "v_mfma_f32_16x16x16_bf16 v[138:141], v[188:189], v[136:137], v[138:141]\n"
      "v_fmac_f32_e64 v120, s53, v109\n"
      "v_fmac_f32_e64 v121, s53, v108\n"
      "v_lshlrev_b32 v196, 16, v88\n"
      "v_mfma_f32_16x16x32_bf16 v[166:169], a[228:231], v[184:187], v[166:169]\n"
      "v_and_b32 v109, 0xffff0000, v88\n"
      "s_waitcnt lgkmcnt(4)\n"
      "v_alignbyte_b32 v197, v198, v197, v219\n"
      "v_mfma_f32_16x16x32_bf16 v[180:183], a[252:255], v[184:187], v[180:183]\n"
      "s_waitcnt lgkmcnt(3)\n"
      "v_lshlrev_b32 v188, 16, v197\n"
      "v_alignbyte_b32 v89, v199, v89, v219\n"
      "v_mfma_f32_16x16x32_bf16 v[124:127], v[244:247], v[184:187], v[124:127]\n"
      "s_waitcnt lgkmcnt(2)\n"
      "v_alignbyte_b32 v160, v161, v160, v219\n"
      "v_fmac_f32 v122, s53, v188\n"
      "s_waitcnt lgkmcnt(1)\n"
      "v_mfma_f32_16x16x32_bf16 v[166:169], a[232:235], v[142:145], v[166:169]\n"
      "s_waitcnt lgkmcnt(0)\n"
      "v_and_b32 v199, 0xffff0000, v197\n"
      "v_lshlrev_b32_e64 v88, 16, v89\n"
      "v_mfma_f32_16x16x32_bf16 v[180:183], v[224:227], v[142:145], v[180:183]\n"
      "ds_read_b128 v[186:189], v2 offset:512\n"
      "v_and_b32 v205, 0xffff0000, v89\n"
      "v_mfma_f32_16x16x32_bf16 v[124:127], v[248:251], v[142:145], v[124:127]\n"
      "ds_read_u16 v218, v218\n"
      "v_alignbyte_b32 v161, v158, v161, v219\n"
      "v_mfma_f32_16x16x32_bf16 v[166:169], a[236:239], v[210:213], v[166:169]\n"
      "v_fmac_f32 v123, s53, v199\n"
      "v_fmac_f32 v152, s53, v196\n"
      "v_mfma_f32_16x16x32_bf16 v[180:183], v[228:231], v[210:213], v[180:183]\n"
      "ds_read_b128 v[196:199], v2 offset:3584\n"
      "v_fmac_f32 v153, s53, v109\n"
      "v_mfma_f32_16x16x32_bf16 v[124:127], v[252:255], v[210:213], v[124:127]\n"
      "v_fmac_f32 v154, s53, v88\n"
      "v_fmac_f32 v155, s53, v205\n"
      "v_lshlrev_b32 v212, 16, v160\n"
      "v_and_b32 v211, 0xffff0000, v160\n"
      "v_lshlrev_b32 v210, 16, v161\n"
      "v_and_b32 v213, 0xffff0000, v161\n"
      "v_alignbyte_b32 v151, v159, v159, v219\n"
      "v_max3_f32 v88, v120, v121, v122\n"
      "v_fmac_f32 v116, s53, v212\n"
      "v_fmac_f32 v117, s53, v211\n"
      "v_fmac_f32 v118, s53, v210\n"
      "v_fmac_f32 v119, s53, v213\n"
      "v_lshlrev_b32 v151, 16, v151\n"
      "v_max3_f32 v210, v123, v152, v153\n"
      "v_max3_f32 v88, v88, v154, v155\n"
      "v_fmac_f32 v156, s53, v151\n"
      "v_max3_f32 v210, v210, v116, v117\n"
      "v_max3_f32 v88, v88, v118, v119\n"
      "v_max3_f32 v88, v88, v210, v156\n"
      "v_mov_b32 v210, v88\n"
      "v_cndmask_b32 v96, v96, v128, s[58:59]\n"
      "v_cndmask_b32 v96, v96, v202, s[60:61]\n"
      "v_permlane16_swap_b32 v88, v210\n"
      "v_max_f32 v88, v88, v210\n"
      "v_mov_b32_e64 v210, v88\n"
      "v_cndmask_b32 v96, v96, v138, s[62:63]\n"
      "s_waitcnt lgkmcnt(0)\n"
      "v_lshlrev_b32 v218, 16, v218\n"
      "v_permlane32_swap_b32 v88, v210\n"
      "v_max_f32 v88, v88, v210\n"
      "v_sub_f32 v120, v120, v88\n"
      "v_sub_f32 v121, v121, v88\n"
      "v_sub_f32 v122, v122, v88\n"
      "v_sub_f32 v123, v123, v88\n"
      "v_exp_f32 v120, v120\n"
      "v_exp_f32 v121, v121\n"
      "v_sub_f32 v152, v152, v88\n"
      "v_sub_f32 v153, v153, v88\n"
      "v_sub_f32 v154, v154, v88\n"
      "v_sub_f32 v155, v155, v88\n"
      "v_exp_f32 v122, v122\n"
      "v_exp_f32 v123, v123\n"
      "v_exp_f32 v152, v152\n"
      "v_exp_f32 v153, v153\n"
      "v_exp_f32 v154, v154\n"
      "v_exp_f32 v155, v155\n"
      "v_cvt_pk_bf16_f32 v120, v120, v121\n"
      "v_cvt_pk_bf16_f32 v121, v122, v123\n"
      "v_cvt_pk_bf16_f32 v122, v152, v153\n"
      "v_cvt_pk_bf16_f32 v123, v154, v155\n"
      "v_sub_f32 v116, v116, v88\n"
      "v_sub_f32 v117, v117, v88\n"
      "v_mfma_f32_16x16x32_bf16 v[210:213], v[32:35], v[120:123], 0\n"
      "v_sub_f32 v118, v118, v88\n"
      "v_sub_f32 v119, v119, v88\n"
      "v_sub_f32 v156, v156, v88\n"
      "v_exp_f32 v116, v116\n"
      "v_exp_f32 v117, v117\n"
      "v_exp_f32 v118, v118\n"
      "v_exp_f32 v119, v119\n"
      "v_exp_f32 v156, v156\n"
      "v_cvt_pk_bf16_f32 v88, v116, v117\n"
      "v_cvt_pk_bf16_f32 v89, v118, v119\n"
      "v_cvt_pk_bf16_f32 v90, v156, 0\n"
      "v_fmac_f32 v96, s53, v218\n"
      "v_mul_f32 v109, v166, v166\n"
      "v_mfma_f32_16x16x32_bf16 v[210:213], v[32:35], v[88:91], v[210:213]\n"
      "ds_read_b128 v[202:205], v2 offset:6656\n"
      "v_max_f32_dpp v115, v96, v96 row_ror:8 row_mask:0xf bank_mask:0xf\n"
      "v_mfma_f32_16x16x32_bf16 v[128:131], v[104:107], v[120:123], 0\n"
      "v_add_f32 v108, v166, v167\n"
      "v_max_f32_dpp v115, v115, v115 row_ror:4 row_mask:0xf bank_mask:0xf\n"
      "v_mfma_f32_16x16x32_bf16 v[128:131], v[76:79], v[88:91], v[128:131]\n"
      "v_fmac_f32 v109, v167, v167\n"
      "v_max_f32_dpp v115, v115, v115 row_ror:2 row_mask:0xf bank_mask:0xf\n"
      "v_mfma_f32_16x16x32_bf16 v[116:119], v[100:103], v[120:123], 0\n"
      "ds_read_b128 v[136:139], v2 offset:9728\n"
      "v_add_f32_e64 v108, v108, v168\n"
      "v_mfma_f32_16x16x32_bf16 v[116:119], v[80:83], v[88:91], v[116:119]\n"
      "v_fmac_f32 v109, v168, v168\n"
      "v_add_f32 v108, v108, v169\n"
      "v_mfma_f32_16x16x32_bf16 v[158:161], v[214:217], v[120:123], 0\n"
      "v_fmac_f32 v109, v169, v169\n"
      "v_add_f32 v108, v108, v180\n"
      "v_mfma_f32_16x16x32_bf16 v[158:161], v[84:87], v[88:91], v[158:161]\n"
      "ds_read_b128 v[154:157], v2 offset:12800\n"
      "v_fmac_f32 v109, v180, v180\n"
      "v_mfma_f32_16x16x32_bf16 v[120:123], a[216:219], v[186:189], 0\n"
      "s_waitcnt lgkmcnt(2)\n"
      "v_max_f32_dpp v115, v115, v115 row_ror:1 row_mask:0xf bank_mask:0xf\n"
      "v_add_f32 v108, v108, v181\n"
      "v_mfma_f32_16x16x32_bf16 v[148:151], a[240:243], v[186:189], 0\n"
      "v_fmac_f32 v109, v181, v181\n"
      "v_add_f32_e64 v108, v108, v182\n"
      "v_mfma_f32_16x16x32_bf16 v[144:147], v[232:235], v[186:189], 0\n"
      "ds_read_b128 v[186:189], v2 offset:15872\n"
      "v_fmac_f32 v109, v182, v182\n"
      "v_mfma_f32_16x16x32_bf16 v[120:123], a[220:223], v[196:199], v[120:123]\n"
      "s_waitcnt lgkmcnt(2)\n"
      "v_add_f32 v108, v108, v183\n"
      "v_fmac_f32 v109, v183, v183\n"
      "v_mfma_f32_16x16x32_bf16 v[148:151], a[244:247], v[196:199], v[148:151]\n"
      "v_add_f32 v108, v108, v124\n"
      "v_fmac_f32 v109, v124, v124\n"
      "v_mfma_f32_16x16x32_bf16 v[144:147], v[236:239], v[196:199], v[144:147]\n"
      "s_waitcnt lgkmcnt(1)\n"
      "v_max_f32_dpp v115, v115, v115 row_bcast:15 row_mask:0xa bank_mask:0xf\n"
      "v_add_f32 v108, v108, v125\n"
      "v_mfma_f32_16x16x32_bf16 v[120:123], a[224:227], v[202:205], v[120:123]\n"
      "v_fmac_f32 v109, v125, v125\n"
      "v_add_f32 v108, v108, v126\n"
      "v_mfma_f32_16x16x32_bf16 v[148:151], a[248:251], v[202:205], v[148:151]\n"
      "v_fmac_f32 v109, v126, v126\n"
      "v_add_f32 v108, v108, v127\n"
      "v_mfma_f32_16x16x32_bf16 v[144:147], v[240:243], v[202:205], v[144:147]\n"
      "s_waitcnt lgkmcnt(0)\n"
      "v_fmac_f32 v109, v127, v127\n"
      "v_rcp_f32 v194, v194\n"
      "v_mfma_f32_16x16x32_bf16 v[120:123], a[228:231], v[136:139], v[120:123]\n"
      "v_rcp_f32 v210, v210\n"
      "v_mfma_f32_16x16x32_bf16 v[148:151], a[252:255], v[136:139], v[148:151]\n"
      "v_max_f32_dpp v115, v115, v115 row_bcast:31 row_mask:0xc bank_mask:0xf\n"
      "v_permlane16_swap_b32 v191, v192\n"
      "v_mfma_f32_16x16x32_bf16 v[144:147], v[244:247], v[136:139], v[144:147]\n"
      "v_readlane_b32 s72, v115, 63\n"
      "v_permlane16_swap_b32 v108, v109\n"
      "v_mfma_f32_16x16x32_bf16 v[120:123], a[232:235], v[154:157], v[120:123]\n"
      "v_mul_f32 v176, v194, v176\n"
      "v_mul_f32 v177, v194, v177\n"
      "v_mfma_f32_16x16x32_bf16 v[148:151], v[224:227], v[154:157], v[148:151]\n"
      "v_mul_f32 v128, v210, v128\n"
      "v_mul_f32 v129, v210, v129\n"
      "v_mfma_f32_16x16x32_bf16 v[144:147], v[248:251], v[154:157], v[144:147]\n"
      "v_cvt_pk_bf16_f32 v176, v176, v177\n"
      "v_mul_f32 v178, v194, v178\n"
      "v_mfma_f32_16x16x32_bf16 v[120:123], a[236:239], v[186:189], v[120:123]\n"
      "v_mul_f32 v179, v194, v179\n"
      "v_cvt_pk_bf16_f32 v128, v128, v129\n"
      "v_mfma_f32_16x16x32_bf16 v[148:151], v[228:231], v[186:189], v[148:151]\n"
      "v_mul_f32 v130, v210, v130\n"
      "v_mul_f32 v131, v210, v131\n"
      "v_mfma_f32_16x16x32_bf16 v[144:147], v[252:255], v[186:189], v[144:147]\n"
      "v_add_f32 v191, v191, v192\n"
      "v_add_f32 v108, v108, v109\n"
      "v_mul_f32 v189, v120, v120\n"
      "v_add_f32 v188, v120, v121\n"
      "v_fmac_f32 v189, v121, v121\n"
      "v_add_f32 v188, v188, v122\n"
      "v_fmac_f32 v189, v122, v122\n"
      "v_add_f32 v188, v188, v123\n"
      "v_fmac_f32 v189, v123, v123\n"
      "v_add_f32 v188, v188, v148\n"
      "v_fmac_f32 v189, v148, v148\n"
      "v_add_f32 v188, v188, v149\n"
      "v_fmac_f32 v189, v149, v149\n"
      "v_add_f32 v188, v188, v150\n"
      "v_fmac_f32 v189, v150, v150\n"
      "v_add_f32 v188, v188, v151\n"
      "v_fmac_f32 v189, v151, v151\n"
      "v_add_f32 v188, v188, v144\n"
      "v_fmac_f32 v189, v144, v144\n"
      "v_add_f32 v188, v188, v145\n"
      "v_fmac_f32 v189, v145, v145\n"
      "v_add_f32 v188, v188, v146\n"
      "v_fmac_f32 v189, v146, v146\n"
      "v_add_f32 v188, v188, v147\n"
      "v_fmac_f32_e64 v189, v147, v147\n"
      "v_cvt_pk_bf16_f32 v177, v178, v179\n"
      "v_mul_f32 v170, v194, v170\n"
      "v_permlane16_swap_b32 v188, v189\n"
      "v_add_f32 v188, v188, v189\n"
      "v_mov_b32 v189, v188\n"
      "v_mul_f32 v171, v194, v171\n"
      "v_mul_f32 v172, v194, v172\n"
      "v_mul_f32 v173, v194, v173\n"
      "v_cvt_pk_bf16_f32 v129, v130, v131\n"
      "v_mul_f32 v116, v210, v116\n"
      "v_mul_f32 v117, v210, v117\n"
      "v_mul_f32 v118, v210, v118\n"
      "v_mul_f32 v119, v210, v119\n"
      "v_subrev_f32 v96, s72, v96\n"
      "v_cvt_pk_bf16_f32 v178, v170, v171\n"
      "v_cvt_pk_bf16_f32 v179, v172, v173\n"
      "v_cvt_pk_bf16_f32 v130, v116, v117\n"
      "v_cvt_pk_bf16_f32 v131, v118, v119\n"
      "v_mul_f32 v110, v194, v110\n"
      "v_mul_f32 v111, v194, v111\n"
      "v_mul_f32 v158, v210, v158\n"
      "v_mul_f32 v159, v210, v159\n"
      "v_exp_f32 v96, v96\n"
      "ds_write_b128 v3, v[176:179] offset:18688\n"
      "v_mul_f32 v134, v174, v134\n"
      "v_mul_f32 v135, v174, v135\n"
      "v_cvt_pk_bf16_f32 v110, v110, v111\n"
      "v_mul_f32 v112, v194, v112\n"
      "v_mul_f32 v113, v194, v113\n"
      "v_cvt_pk_bf16_f32 v158, v158, v159\n"
      "v_mul_f32 v160, v210, v160\n"
      "v_mul_f32_e64 v161, v210, v161\n"
      "ds_write_b128 v3, v[128:131] offset:18944\n"
      "v_permlane32_swap_b32_e64 v191, v108\n"
      "v_permlane32_swap_b32_e64 v188, v189\n"
      "v_cvt_pk_bf16_f32 v133, v134, v135\n"
      "v_cvt_pk_bf16_f32 v111, v112, v113\n"
      "v_cvt_pk_bf16_f32 v159, v160, v161\n"
      "v_cvt_pk_bf16_f32 v96, v96, v96\n"
      "ds_write_b64 v4, v[132:133] offset:18432\n"
      "v_add_f32_e64 v191, v191, v108\n"
      "v_add_f32_e64 v188, v188, v189\n"
      "v_readlane_b32 s46, v37, s42\n"
      "v_or_b32 v190, v190, v193\n"
      "v_cmp_ne_u32 s[56:57], 0, v190\n"
      "ds_write_b64 v4, v[110:111] offset:18688\n"
      "s_or_b32 s95, s45, s46\n"
      "s_cmp_ge_u32 s95, 0x40000000\n"
      "s_cselect_b64 s[56:57], -1, s[56:57]\n"
      "s_cmp_gt_u32 s42, s44\n"
      "s_cselect_b64 s[56:57], -1, s[56:57]\n"
      "ds_write_b64 v4, v[158:159] offset:18944\n"
      "s_or_b64 vcc, s[56:57], s[54:55]\n"
      "s_add_u32 s42, s42, 1\n"
      "v_readlane_b32 s47, v38, s43\n"
      "s_add_u32 s43, s43, 1\n"
      "s_and_b32 s47, s47, -4\n"
      "v_add_u32 v10, 2336, v10\n"
      "ds_write_b16 v13, v96\n"
      "ds_read_b128 v[210:213], v14\n"
      "ds_read_b128 v[174:177], v14 offset:64\n"
      "ds_write_b32 v8, v191 offset:2048\n"
      "v_add_u32 v11, 2336, v11\n"
      "ds_write_b32 v8, v188 offset:2560\n"
      "s_waitcnt lgkmcnt(3)\n"
      "v_mfma_f32_16x16x32_bf16 v[188:191], v[210:213], v[32:35], 0\n"
      "s_waitcnt lgkmcnt(2)\n"
      "v_mfma_f32_16x16x32_bf16 v[188:191], v[174:177], v[32:35], v[188:191]\n"
      "s_waitcnt lgkmcnt(0)\n"
      "s_barrier\n"
      "s_nop 7\n"
      "s_nop 7\n"
      "s_cbranch_vccz L_loop0_%=\n"
      "L_disp0_%=:\n"
      "s_sub_u32 s92, s42, 1\n"
      "s_cmp_gt_u32 s92, s44\n"
      "s_cbranch_scc1 L_tail0_%=\n"
      "s_nop 7\n"
      "s_nop 7\n"
      "L_slow0_%=:\n"
      "v_mfma_f32_16x16x32_bf16 v[202:205], v[210:213], v[104:107], 0\n"
      "ds_read_b128 v[116:119], v9 offset:2048\n"
      "ds_read_b128 v[190:193], v9 offset:3072\n"
      "v_mfma_f32_16x16x32_bf16 v[202:205], v[174:177], v[76:79], v[202:205]\n"
      "ds_read_b128 v[108:111], v0\n"
      "v_mov_b32_dpp v188, v188 quad_perm:[0,0,0,0] row_mask:0xf bank_mask:0xf\n"
      "v_mfma_f32_16x16x32_bf16 v[130:133], v[210:213], v[100:103], 0\n"
      "ds_read_b128 v[100:103], v1\n"
      "v_rcp_f32 v188, v188\n"
      "v_mfma_f32_16x16x32_bf16 v[130:133], v[174:177], v[80:83], v[130:133]\n"
      "ds_read_b128 v[112:115], v0 offset:6144\n"
      "v_subrev_u32 v179, 115456, v9\n"
      "v_mfma_f32_16x16x32_bf16 v[194:197], v[210:213], v[214:217], 0\n"
      "s_add_u32 s72, s42, 1\n"
      "v_readlane_b32 s77, v38, s72\n"
      "v_bfe_i32 v96, v36, 0, 1\n"
      "v_mfma_f32_16x16x32_bf16 v[194:197], v[174:177], v[84:87], v[194:197]\n"
      "s_mov_b32 m0, 110592\n"
      "v_cndmask_b32 v202, v202, v130, s[58:59]\n"
      "s_waitcnt lgkmcnt(4)\n"
      "v_add_f32 v116, v116, v118\n"
      "v_add_f32_e64 v117, v117, v119\n"
      "v_bfe_i32 v160, v36, 1, 1\n"
      "s_waitcnt lgkmcnt(3)\n"
      "v_add_f32 v190, v190, v192\n"
      "v_cndmask_b32 v202, v202, v194, s[60:61]\n"
      "v_mul_f32 v202, v188, v202\n"
      "s_waitcnt lgkmcnt(2)\n"
      "v_mfma_f32_16x16x32_bf16 v[132:135], a[0:3], v[108:111], 0\n"
      "s_and_b32 s73, s77, -16\n"
      "v_cvt_pk_bf16_f32 v202, v202, v202\n"
      "v_add_f32 v191, v191, v193\n"
      "v_mfma_f32_16x16x32_bf16 v[128:131], a[24:27], v[108:111], 0\n"
      "ds_write_b16 v12, v202 offset:16\n"
      "v_mfma_f32_16x16x32_bf16 v[212:215], a[48:51], v[108:111], 0\n"
      "buffer_load_dwordx4 v179, s[24:27], s73 offen  lds\n"
      "v_mfma_f32_16x16x32_bf16 v[156:159], a[72:75], v[108:111], 0\n"
      "s_waitcnt lgkmcnt(1)\n"
      "v_add_f32 v116, v116, v190\n"
      "v_add_f32_e64 v117, v117, v191\n"
      "v_mfma_f32_16x16x32_bf16 v[202:205], a[96:99], v[108:111], 0\n"
      "s_mov_b32 m0, 111616\n"
      "v_fma_f32 v190, -v116, v116, s52\n"
      "v_fmac_f32 v190, 0x43400000, v117\n"
      "v_mfma_f32_16x16x32_bf16 v[172:175], a[120:123], v[108:111], 0\n"
      "ds_read_b128 v[194:197], v1 offset:6144\n"
      "v_max_f32 v190, s52, v190\n"
      "v_mfma_f32_16x16x32_bf16 v[132:135], a[4:7], v[100:103], v[132:135]\n"
      "ds_read_b128 v[186:189], v0 offset:12288\n"
      "v_rsq_f32 v190, v190\n"
      "v_mfma_f32_16x16x32_bf16 v[132:135], a[8:11], v[112:115], v[132:135]\n"
      "ds_read_b128 v[108:111], v1 offset:12288\n"
      "v_mul_f32 v211, 0x43400000, v190\n"
      "v_mfma_f32_16x16x32_bf16 v[128:131], a[28:31], v[100:103], v[128:131]\n"
      "s_add_u32 s73, s73, 1024\n"
      "v_mul_f32 v210, -v116, v190\n"
      "v_mov_b32_e64 v170, v211\n"
      "v_mfma_f32_16x16x32_bf16 v[128:131], a[32:35], v[112:115], v[128:131]\n"
      "buffer_load_dwordx4 v179, s[24:27], s73 offen  lds\n"
      "v_mfma_f32_16x16x32_bf16 v[212:215], a[52:55], v[100:103], v[212:215]\n"
      "s_mov_b32 m0, 112640\n"
      "v_mov_b32 v171, v210\n"
      "v_permlane32_swap_b32 v211, v170\n"
      "v_mfma_f32_16x16x32_bf16 v[212:215], a[56:59], v[112:115], v[212:215]\n"
      "s_add_u32 s73, s73, 1024\n"
      "v_permlane32_swap_b32 v210, v171\n"
      "v_mfma_f32_16x16x32_bf16 v[156:159], a[76:79], v[100:103], v[156:159]\n"
      "s_bitcmp1_b32 s45, 31\n"
      "s_cselect_b32 s75, 100270464, 0\n"
      "v_mov_b32_e64 v104, v211\n"
      "v_mfma_f32_16x16x32_bf16 v[156:159], a[80:83], v[112:115], v[156:159]\n"
      "buffer_load_dwordx4 v179, s[24:27], s73 offen  lds\n"
      "v_mfma_f32_16x16x32_bf16 v[202:205], a[100:103], v[100:103], v[202:205]\n"
      "s_mov_b32 m0, 113664\n"
      "v_mov_b32 v107, v210\n"
      "v_permlane16_swap_b32_e64 v211, v104\n"
      "v_mfma_f32_16x16x32_bf16 v[202:205], a[104:107], v[112:115], v[202:205]\n"
      "s_waitcnt lgkmcnt(0)\n"
      "v_permlane16_swap_b32_e64 v210, v107\n"
      "v_mfma_f32_16x16x32_bf16 v[172:175], a[124:127], v[100:103], v[172:175]\n"
      "s_add_u32 s73, s73, 1024\n"
      "v_fma_f32 v220, v220, v211, v210\n"
      "v_fma_f32 v221, v221, v211, v210\n"
      "v_mfma_f32_16x16x32_bf16 v[172:175], a[128:131], v[112:115], v[172:175]\n"
      "buffer_load_dwordx4 v179, s[24:27], s73 offen  lds\n"
      "v_mfma_f32_16x16x32_bf16 v[132:135], a[12:15], v[194:197], v[132:135]\n"
      "s_mov_b32 m0, 114688\n"
      "v_cvt_pk_bf16_f32 v220, v220, v221\n"
      "v_fma_f32 v222, v222, v211, v210\n"
      "v_mfma_f32_16x16x32_bf16 v[132:135], a[16:19], v[186:189], v[132:135]\n"
      "s_add_u32 s73, s73, 1024\n"
      "v_fma_f32 v223, v223, v211, v210\n"
      "v_fma_f32 v162, v162, v211, v210\n"
      "v_mfma_f32_16x16x32_bf16 v[132:135], a[20:23], v[108:111], v[132:135]\n"
      "s_bitcmp1_b32 s45, 30\n"
      "s_cselect_b32 s76, 196224, 0\n"
      "v_fma_f32 v163, v163, v211, v210\n"
      "v_mfma_f32_16x16x32_bf16 v[128:131], a[36:39], v[194:197], v[128:131]\n"
      "s_mov_b32 exec_hi, 0xffff\n"
      "buffer_load_dwordx4 v179, s[24:27], s73 offen  lds\n"
      "s_mov_b32 exec_hi, -1\n"
      "v_mfma_f32_16x16x32_bf16 v[128:131], a[40:43], v[186:189], v[128:131]\n"
      "s_and_b32 s74, s45, 0x3fffffff\n"
      "v_cvt_pk_bf16_f32 v221, v222, v223\n"
      "v_fma_f32 v206, v206, v211, v210\n"
      "v_mfma_f32_16x16x32_bf16 v[128:131], a[44:47], v[108:111], v[128:131]\n"
      "ds_read_b128 v[198:201], v0 offset:2048\n"
      "v_fma_f32 v207, v207, v211, v210\n"
      "v_mfma_f32_16x16x32_bf16 v[212:215], a[60:63], v[194:197], v[212:215]\n"
      "ds_read_b128 v[116:119], v1 offset:2048\n"
      "v_fma_f32 v208, v208, v211, v210\n"
      "v_mfma_f32_16x16x32_bf16 v[212:215], a[64:67], v[186:189], v[212:215]\n"
      "ds_read_b128 v[190:193], v0 offset:8192\n"
      "v_fma_f32 v209, v209, v211, v210\n"
      "v_mfma_f32_16x16x32_bf16 v[212:215], a[68:71], v[108:111], v[212:215]\n"
      "ds_read_b128 v[176:179], v1 offset:8192\n"
      "v_cvt_pk_bf16_f32 v162, v162, v163\n"
      "v_mfma_f32_16x16x32_bf16 v[156:159], a[84:87], v[194:197], v[156:159]\n"
      "ds_read_b128 v[112:115], v0 offset:14336\n"
      "v_fma_f32 v164, v164, v211, v210\n"
      "v_mfma_f32_16x16x32_bf16 v[156:159], a[88:91], v[186:189], v[156:159]\n"
      "ds_read_b128 v[216:219], v1 offset:14336\n"
      "v_fma_f32 v165, v165, v211, v210\n"
      "v_mfma_f32_16x16x32_bf16 v[156:159], a[92:95], v[108:111], v[156:159]\n"
      "s_add_u32 m0, s64, 18432\n"
      "v_cvt_pk_bf16_f32 v222, v206, v207\n"
      "v_cvt_pk_bf16_f32 v223, v208, v209\n"
      "v_mfma_f32_16x16x32_bf16 v[202:205], a[108:111], v[194:197], v[202:205]\n"
      "ds_write_b128 v5, v[220:223]\n"
      "v_mfma_f32_16x16x32_bf16 v[202:205], a[112:115], v[186:189], v[202:205]\n"
      "s_bitcmp1_b32 s46, 31\n"
      "s_cselect_b32 s93, 100270464, 0\n"
      "v_cvt_pk_bf16_f32 v163, v164, v165\n"
      "v_mfma_f32_16x16x32_bf16 v[202:205], a[116:119], v[108:111], v[202:205]\n"
      "s_bitcmp1_b32 s46, 30\n"
      "s_cselect_b32 s94, 196224, 0\n"
      "v_fma_f32 v166, v166, v104, v107\n"
      "v_mfma_f32_16x16x32_bf16 v[172:175], a[132:135], v[194:197], v[172:175]\n"
      "s_waitcnt lgkmcnt(1)\n"
      "v_fma_f32 v167, v167, v104, v107\n"
      "v_and_b32 v96, s75, v96\n"
      "v_mfma_f32_16x16x32_bf16 v[172:175], a[136:139], v[186:189], v[172:175]\n"
      "ds_write_b64 v6, v[162:163]\n"
      "v_mfma_f32_16x16x32_bf16 v[172:175], a[140:143], v[108:111], v[172:175]\n"
      "s_and_b32 s92, s46, 0x3fffffff\n"
      "v_and_b32 v160, s76, v160\n"
      "v_cvt_pk_bf16_f32 v166, v166, v167\n"
      "v_mfma_f32_16x16x32_bf16 v[208:211], a[0:3], v[198:201], 0\n"
      "s_and_b32 s77, s77, 15\n"
      "v_fma_f32 v168, v168, v104, v107\n"
      "v_fma_f32 v169, v169, v104, v107\n"
      "v_mfma_f32_16x16x32_bf16 v[208:211], a[4:7], v[116:119], v[208:211]\n"
      "s_add_u32 s73, s73, 1024\n"
      "v_fma_f32 v124, v124, v104, v107\n"
      "v_fma_f32 v125, v125, v104, v107\n"
      "v_mfma_f32_16x16x32_bf16 v[208:211], a[8:11], v[190:193], v[208:211]\n"
      "v_add_u32_e64 v96, v96, v160\n"
      "v_cvt_pk_bf16_f32 v167, v168, v169\n"
      "v_mfma_f32_16x16x32_bf16 v[208:211], a[12:15], v[176:179], v[208:211]\n"
      "v_fma_f32 v180, v180, v104, v107\n"
      "v_fma_f32 v181, v181, v104, v107\n"
      "v_mfma_f32_16x16x32_bf16 v[208:211], a[16:19], v[112:115], v[208:211]\n"
      "v_fma_f32 v182, v182, v104, v107\n"
      "v_fma_f32 v183, v183, v104, v107\n"
      "v_mfma_f32_16x16x32_bf16 v[208:211], a[20:23], v[216:219], v[208:211]\n"
      "v_cvt_pk_bf16_f32 v124, v124, v125\n"
      "v_fma_f32 v126, v126, v104, v107\n"
      "v_mfma_f32_16x16x32_bf16 v[160:163], a[24:27], v[198:201], 0\n"
      "v_fma_f32 v127, v127, v104, v107\n"
      "v_sub_u32_e64 v96, s74, v96\n"
      "v_mfma_f32_16x16x32_bf16 v[160:163], a[28:31], v[116:119], v[160:163]\n"
      "v_cvt_pk_bf16_f32 v168, v180, v181\n"
      "v_cvt_pk_bf16_f32 v169, v182, v183\n"
      "v_mfma_f32_16x16x32_bf16 v[160:163], a[32:35], v[190:193], v[160:163]\n"
      "ds_write_b128 v5, v[166:169] offset:6144\n"
      "v_mfma_f32_16x16x32_bf16 v[160:163], a[36:39], v[176:179], v[160:163]\n"
      "v_cvt_pk_bf16_f32 v125, v126, v127\n"
      "v_add_u32_e64 v96, v16, v96\n"
      "v_mfma_f32_16x16x32_bf16 v[160:163], a[40:43], v[112:115], v[160:163]\n"
      "buffer_load_dwordx4 v96, s[16:19], 0 offen  lds\n"
      "v_mfma_f32_16x16x32_bf16 v[160:163], a[44:47], v[216:219], v[160:163]\n"
      "ds_write_b64 v6, v[124:125] offset:6144\n"
      "v_mfma_f32_16x16x32_bf16 v[164:167], a[48:51], v[198:201], 0\n"
      "s_add_u32 m0, s64, 22528\n"
      "v_bfe_i32 v96, v36, 2, 1\n"
      "v_bfe_i32 v223, v36, 3, 1\n"
      "v_mfma_f32_16x16x32_bf16 v[164:167], a[52:55], v[116:119], v[164:167]\n"
      "v_and_b32 v96, s75, v96\n"
      "v_and_b32 v223, s76, v223\n"
      "v_mfma_f32_16x16x32_bf16 v[164:167], a[56:59], v[190:193], v[164:167]\n"
      "v_add_u32 v96, v96, v223\n"
      "v_sub_u32 v96, s74, v96\n"
      "v_mfma_f32_16x16x32_bf16 v[164:167], a[60:63], v[176:179], v[164:167]\n"
      "v_add_u32_e64 v96, v17, v96\n"
      "buffer_load_dwordx4 v96, s[16:19], 0 offen  lds\n"
      "v_mfma_f32_16x16x32_bf16 v[164:167], a[64:67], v[112:115], v[164:167]\n"
      "s_add_u32 m0, s64, 26624\n"
      "v_bfe_i32 v223, v36, 4, 1\n"
      "v_bfe_i32 v220, v36, 5, 1\n"
      "v_mfma_f32_16x16x32_bf16 v[164:167], a[68:71], v[216:219], v[164:167]\n"
      "v_and_b32 v223, s75, v223\n"
      "v_and_b32 v220, s76, v220\n"
      "v_mfma_f32_16x16x32_bf16 v[96:99], a[72:75], v[198:201], 0\n"
      "v_add_u32 v223, v223, v220\n"
      "v_sub_u32 v223, s74, v223\n"
      "v_mfma_f32_16x16x32_bf16 v[96:99], a[76:79], v[116:119], v[96:99]\n"
      "v_add_u32_e64 v223, v18, v223\n"
      "buffer_load_dwordx4 v223, s[16:19], 0 offen  lds\n"
      "v_mfma_f32_16x16x32_bf16 v[96:99], a[80:83], v[190:193], v[96:99]\n"
      "s_add_u32 m0, s64, 30720\n"
      "v_bfe_i32 v107, v36, 6, 1\n"
      "v_bfe_i32 v223, v36, 7, 1\n"
      "v_mfma_f32_16x16x32_bf16 v[96:99], a[84:87], v[176:179], v[96:99]\n"
      "v_and_b32 v107, s75, v107\n"
      "v_and_b32 v223, s76, v223\n"
      "v_mfma_f32_16x16x32_bf16 v[96:99], a[88:91], v[112:115], v[96:99]\n"
      "v_add_u32 v107, v107, v223\n"
      "v_sub_u32 v107, s74, v107\n"
      "v_mfma_f32_16x16x32_bf16 v[96:99], a[92:95], v[216:219], v[96:99]\n"
      "ds_read_b128 v[220:223], v0 offset:4096\n"
      "v_add_u32_e64 v107, v19, v107\n"
      "v_mfma_f32_16x16x32_bf16 v[136:139], a[96:99], v[198:201], 0\n"
      "buffer_load_dwordx4 v107, s[16:19], 0 offen  lds\n"
      "v_mfma_f32_16x16x32_bf16 v[136:139], a[100:103], v[116:119], v[136:139]\n"
      "s_add_u32 m0, s65, 34816\n"
      "v_bfe_i32 v188, v36, 8, 1\n"
      "v_bfe_i32 v183, v36, 9, 1\n"
      "v_mfma_f32_16x16x32_bf16 v[136:139], a[104:107], v[190:193], v[136:139]\n"
      "ds_read_b128 v[152:155], v1 offset:4096\n"
      "v_and_b32 v188, s75, v188\n"
      "v_mfma_f32_16x16x32_bf16 v[136:139], a[108:111], v[176:179], v[136:139]\n"
      "ds_read_b128 v[140:143], v0 offset:10240\n"
      "v_and_b32 v183, s76, v183\n"
      "v_mfma_f32_16x16x32_bf16 v[136:139], a[112:115], v[112:115], v[136:139]\n"
      "ds_read_b128 v[104:107], v1 offset:10240\n"
      "v_add_u32_e64 v188, v188, v183\n"
      "v_mfma_f32_16x16x32_bf16 v[136:139], a[116:119], v[216:219], v[136:139]\n"
      "ds_read_b128 v[100:103], v0 offset:16384\n"
      "v_sub_u32_e64 v188, s74, v188\n"
      "v_mfma_f32_16x16x32_bf16 v[194:197], a[120:123], v[198:201], 0\n"
      "ds_read_b128 v[182:185], v1 offset:16384\n"
      "v_add_u32_e64 v188, v15, v188\n"
      "v_mfma_f32_16x16x32_bf16 v[194:197], a[124:127], v[116:119], v[194:197]\n"
      "buffer_load_dwordx4 v188, s[16:19], 0 offen  lds\n"
      "v_mfma_f32_16x16x32_bf16 v[194:197], a[128:131], v[190:193], v[194:197]\n"
      "s_waitcnt lgkmcnt(4)\n"
      "v_fma_f32 v120, v120, v170, v171\n"
      "v_fma_f32 v121, v121, v170, v171\n"
      "v_mfma_f32_16x16x32_bf16 v[194:197], a[132:135], v[176:179], v[194:197]\n"
      "s_waitcnt lgkmcnt(3)\n"
      "v_cvt_pk_bf16_f32 v120, v120, v121\n"
      "v_fma_f32 v122, v122, v170, v171\n"
      "v_mfma_f32_16x16x32_bf16 v[194:197], a[136:139], v[112:115], v[194:197]\n"
      "s_waitcnt lgkmcnt(2)\n"
      "v_fma_f32 v123, v123, v170, v171\n"
      "v_fma_f32 v144, v144, v170, v171\n"
      "v_mfma_f32_16x16x32_bf16 v[194:197], a[140:143], v[216:219], v[194:197]\n"
      "ds_read_b128 v[112:115], v9 offset:5632\n"
      "v_fma_f32 v145, v145, v170, v171\n"
      "v_mfma_f32_16x16x32_bf16 v[186:189], a[0:3], v[220:223], 0\n"
      "buffer_load_dwordx4 v[116:119], v27, s[24:27], s47 offen\n"
      "v_mfma_f32_16x16x32_bf16 v[186:189], a[4:7], v[152:155], v[186:189]\n"
      "s_waitcnt lgkmcnt(1)\n"
      "v_cvt_pk_bf16_f32 v121, v122, v123\n"
      "v_fma_f32 v148, v148, v170, v171\n"
      "v_mfma_f32_16x16x32_bf16 v[186:189], a[8:11], v[140:143], v[186:189]\n"
      "v_fma_f32 v149, v149, v170, v171\n"
      "v_fma_f32 v150, v150, v170, v171\n"
      "v_mfma_f32_16x16x32_bf16 v[186:189], a[12:15], v[104:107], v[186:189]\n"
      "v_fma_f32 v151, v151, v170, v171\n"
      "v_cvt_pk_bf16_f32 v144, v144, v145\n"
      "v_mfma_f32_16x16x32_bf16 v[186:189], a[16:19], v[100:103], v[186:189]\n"
      "buffer_load_dword v92, v29, s[24:27], s47 offen\n"
      "v_mfma_f32_16x16x32_bf16 v[186:189], a[20:23], v[182:185], v[186:189]\n"
      "v_fma_f32 v146, v146, v170, v171\n"
      "v_fma_f32 v147, v147, v170, v171\n"
      "v_mfma_f32_16x16x32_bf16 v[124:127], a[24:27], v[220:223], 0\n"
      "v_cvt_pk_bf16_f32 v122, v148, v149\n"
      "v_cvt_pk_bf16_f32 v123, v150, v151\n"
      "v_mfma_f32_16x16x32_bf16 v[124:127], a[28:31], v[152:155], v[124:127]\n"
      "ds_write_b128 v5, v[120:123] offset:12288\n"
      "v_mfma_f32_16x16x32_bf16 v[124:127], a[32:35], v[140:143], v[124:127]\n"
      "v_cvt_pk_bf16_f32 v145, v146, v147\n"
      "v_fma_f32 v146, v132, v132, s51\n"
      "v_mfma_f32_16x16x32_bf16 v[124:127], a[36:39], v[104:107], v[124:127]\n"
      "v_fma_f32 v147, v156, v156, s51\n"
      "v_fmac_f32_e64 v146, v133, v133\n"
      "v_mfma_f32_16x16x32_bf16 v[124:127], a[40:43], v[100:103], v[124:127]\n"
      "ds_write_b64 v6, v[144:145] offset:12288\n"
      "v_mfma_f32_16x16x32_bf16 v[124:127], a[44:47], v[182:185], v[124:127]\n"
      "v_fmac_f32 v147, v157, v157\n"
      "v_fmac_f32 v146, v134, v134\n"
      "v_mfma_f32_16x16x32_bf16 v[148:151], a[48:51], v[220:223], 0\n"
      "v_fmac_f32 v147, v158, v158\n"
      "v_fmac_f32 v146, v135, v135\n"
      "v_mfma_f32_16x16x32_bf16 v[148:151], a[52:55], v[152:155], v[148:151]\n"
      "v_fmac_f32 v147, v159, v159\n"
      "v_fmac_f32 v146, v128, v128\n"
      "v_mfma_f32_16x16x32_bf16 v[148:151], a[56:59], v[140:143], v[148:151]\n"
      "v_fmac_f32 v147, v202, v202\n"
      "v_fmac_f32 v146, v129, v129\n"
      "v_mfma_f32_16x16x32_bf16 v[148:151], a[60:63], v[104:107], v[148:151]\n"
      "ds_read_b128 v[168:171], v7 offset:18432\n"
      "v_fmac_f32 v147, v203, v203\n"
      "v_mfma_f32_16x16x32_bf16 v[148:151], a[64:67], v[100:103], v[148:151]\n"
      "ds_read_b128 v[216:219], v7 offset:22528\n"
      "v_fmac_f32 v146, v130, v130\n"
      "v_mfma_f32_16x16x32_bf16 v[148:151], a[68:71], v[182:185], v[148:151]\n"
      "v_fmac_f32 v147, v204, v204\n"
      "v_fmac_f32 v146, v131, v131\n"
      "v_mfma_f32_16x16x32_bf16 v[198:201], a[72:75], v[220:223], 0\n"
      "ds_read_b128 v[108:111], v7 offset:26624\n"
      "v_fmac_f32_e64 v147, v205, v205\n"
      "v_mfma_f32_16x16x32_bf16 v[198:201], a[76:79], v[152:155], v[198:201]\n"
      "ds_read_b128 v[178:181], v7 offset:30720\n"
      "v_fmac_f32 v146, v212, v212\n"
      "v_mfma_f32_16x16x32_bf16 v[198:201], a[80:83], v[140:143], v[198:201]\n"
      "v_fmac_f32 v147, v172, v172\n"
      "v_fmac_f32 v146, v213, v213\n"
      "v_mfma_f32_16x16x32_bf16 v[198:201], a[84:87], v[104:107], v[198:201]\n"
      "ds_read_b128 v[190:193], v7 offset:34816\n"
      "v_fmac_f32 v147, v173, v173\n"
      "v_mfma_f32_16x16x32_bf16 v[198:201], a[88:91], v[100:103], v[198:201]\n"
      "s_waitcnt vmcnt(7)\n"
      "v_fmac_f32 v146, v214, v214\n"
      "v_fmac_f32 v147, v174, v174\n"
      "v_mfma_f32_16x16x32_bf16 v[198:201], a[92:95], v[182:185], v[198:201]\n"
      "s_waitcnt lgkmcnt(3)\n"
      "v_fmac_f32 v146, v215, v215\n"
      "v_fmac_f32 v147, v175, v175\n"
      "v_mfma_f32_16x16x32_bf16 v[120:123], a[96:99], v[220:223], 0\n"
      "s_waitcnt lgkmcnt(2)\n"
      "v_permlane16_swap_b32 v146, v147\n"
      "v_mfma_f32_16x16x32_bf16 v[120:123], a[100:103], v[152:155], v[120:123]\n"
      "s_waitcnt lgkmcnt(1)\n"
      "v_add_f32 v146, v146, v147\n"
      "v_mov_b32 v147, v146\n"
      "v_mfma_f32_16x16x32_bf16 v[120:123], a[104:107], v[140:143], v[120:123]\n"
      "v_or3_b32 v112, v112, v113, v114\n"
      "v_permlane32_swap_b32 v146, v147\n"
      "v_mfma_f32_16x16x32_bf16 v[120:123], a[108:111], v[104:107], v[120:123]\n"
      "s_waitcnt lgkmcnt(0)\n"
      "v_add_f32 v146, v146, v147\n"
      "v_rsq_f32 v146, v146\n"
      "v_mfma_f32_16x16x32_bf16 v[120:123], a[112:115], v[100:103], v[120:123]\n"
      "v_mov_b32 v147, v146\n"
      "v_fma_f32 v77, v208, v208, s51\n"
      "v_mfma_f32_16x16x32_bf16 v[120:123], a[116:119], v[182:185], v[120:123]\n"
      "v_permlane16_swap_b32 v146, v147\n"
      "v_mul_f32 v146, s50, v146\n"
      "v_mul_f32 v156, v147, v156\n"
      "v_mul_f32 v157, v147, v157\n"
      "v_mul_f32 v132, v146, v132\n"
      "v_mul_f32 v133, v146, v133\n"
      "v_cvt_pk_bf16_f32 v156, v156, v157\n"
      "v_mul_f32 v158, v147, v158\n"
      "v_mul_f32 v159, v147, v159\n"
      "v_mul_f32 v172, v147, v172\n"
      "v_mul_f32 v173, v147, v173\n"
      "v_cvt_pk_bf16_f32 v132, v132, v133\n"
      "v_mul_f32 v134, v146, v134\n"
      "v_mul_f32 v135, v146, v135\n"
      "v_mul_f32 v212, v146, v212\n"
      "v_mul_f32 v213, v146, v213\n"
      "v_cvt_pk_bf16_f32 v157, v158, v159\n"
      "v_mul_f32 v202, v147, v202\n"
      "v_mul_f32 v203, v147, v203\n"
      "v_mul_f32 v204, v147, v204\n"
      "v_mul_f32 v205, v147, v205\n"
      "v_cvt_pk_bf16_f32 v172, v172, v173\n"
      "v_mul_f32 v174, v147, v174\n"
      "v_mul_f32 v175, v147, v175\n"
      "v_cvt_pk_bf16_f32 v133, v134, v135\n"
      "v_mul_f32 v128, v146, v128\n"
      "v_mul_f32 v129, v146, v129\n"
      "v_mul_f32 v130, v146, v130\n"
      "v_mul_f32 v131, v146, v131\n"
      "v_cvt_pk_bf16_f32 v212, v212, v213\n"
      "v_mul_f32 v214, v146, v214\n"
      "v_mul_f32 v215, v146, v215\n"
      "v_cvt_pk_bf16_f32 v158, v202, v203\n"
      "v_mfma_f32_16x16x32_bf16 v[144:147], a[120:123], v[220:223], 0\n"
      "v_cvt_pk_bf16_f32 v159, v204, v205\n"
      "v_cvt_pk_bf16_f32 v173, v174, v175\n"
      "v_mfma_f32_16x16x32_bf16 v[144:147], a[124:127], v[152:155], v[144:147]\n"
      "v_cvt_pk_bf16_f32 v134, v128, v129\n"
      "v_cvt_pk_bf16_f32 v135, v130, v131\n"
      "v_mfma_f32_16x16x32_bf16 v[144:147], a[128:131], v[140:143], v[144:147]\n"
      "v_cvt_pk_bf16_f32 v213, v214, v215\n"
      "v_fma_f32 v203, v96, v96, s51\n"
      "v_mfma_f32_16x16x32_bf16 v[144:147], a[132:135], v[104:107], v[144:147]\n"
      "v_fmac_f32 v77, v209, v209\n"
      "v_fmac_f32 v203, v97, v97\n"
      "v_mfma_f32_16x16x32_bf16 v[144:147], a[136:139], v[100:103], v[144:147]\n"
      "v_fmac_f32 v77, v210, v210\n"
      "v_fmac_f32 v203, v98, v98\n"
      "v_mfma_f32_16x16x32_bf16 v[144:147], a[140:143], v[182:185], v[144:147]\n"
      "v_fmac_f32 v77, v211, v211\n"
      "v_fmac_f32 v203, v99, v99\n"
      "v_mfma_f32_16x16x32_bf16 v[174:177], v[220:223], a[144:147], 0\n"
      "v_bfe_i32 v214, v36, 10, 1\n"
      "v_bfe_i32 v215, v36, 11, 1\n"
      "v_mfma_f32_16x16x32_bf16 v[174:177], v[152:155], a[148:151], v[174:177]\n"
      "v_fmac_f32 v77, v160, v160\n"
      "v_fmac_f32 v203, v136, v136\n"
      "v_mfma_f32_16x16x32_bf16 v[128:131], v[220:223], a[168:171], 0\n"
      "v_and_b32 v214, s93, v214\n"
      "v_and_b32 v215, s94, v215\n"
      "v_mfma_f32_16x16x32_bf16 v[128:131], v[152:155], a[172:175], v[128:131]\n"
      "v_fmac_f32 v77, v161, v161\n"
      "v_fmac_f32 v203, v137, v137\n"
      "v_mfma_f32_16x16x32_bf16 v[204:207], v[220:223], a[192:195], 0\n"
      "v_add_u32 v214, v214, v215\n"
      "v_fmac_f32 v77, v162, v162\n"
      "v_mfma_f32_16x16x32_bf16 v[204:207], v[152:155], a[196:199], v[204:207]\n"
      "ds_read_b128 v[152:155], v0 offset:2048\n"
      "v_fmac_f32 v203, v138, v138\n"
      "v_mfma_f32_16x16x32_bf16 v[174:177], v[140:143], a[152:155], v[174:177]\n"
      "v_sub_u32 v214, s92, v214\n"
      "v_fmac_f32_e64 v77, v163, v163\n"
      "v_mfma_f32_16x16x32_bf16 v[174:177], v[104:107], a[156:159], v[174:177]\n"
      "v_fmac_f32 v203, v139, v139\n"
      "v_add_u32 v214, v20, v214\n"
      "v_mfma_f32_16x16x32_bf16 v[128:131], v[140:143], a[176:179], v[128:131]\n"
      "buffer_store_dwordx4 v[168:171], v214, s[20:23], 0 offen sc1\n"
      "v_mfma_f32_16x16x32_bf16 v[128:131], v[104:107], a[180:183], v[128:131]\n"
      "s_nop 0\n"
      "ds_read_b128 v[168:171], v1 offset:2048\n"
      "v_fmac_f32 v77, v164, v164\n"
      "v_mfma_f32_16x16x32_bf16 v[204:207], v[140:143], a[200:203], v[204:207]\n"
      "s_waitcnt lgkmcnt(1)\n"
      "v_fmac_f32 v203, v194, v194\n"
      "v_fmac_f32 v77, v165, v165\n"
      "v_mfma_f32_16x16x32_bf16 v[204:207], v[104:107], a[204:207], v[204:207]\n"
      "ds_read_b128 v[140:143], v0 offset:8192\n"
      "v_fmac_f32 v203, v195, v195\n"
      "v_mfma_f32_16x16x32_bf16 v[174:177], v[100:103], a[160:163], v[174:177]\n"
      "ds_read_b128 v[220:223], v1 offset:8192\n"
      "v_fmac_f32 v77, v166, v166\n"
      "v_mfma_f32_16x16x32_bf16 v[174:177], v[182:185], a[164:167], v[174:177]\n"
      "v_fmac_f32 v203, v196, v196\n"
      "v_bfe_i32 v214, v36, 12, 1\n"
      "v_mfma_f32_16x16x32_bf16 v[128:131], v[100:103], a[184:187], v[128:131]\n"
      "v_bfe_i32 v105, v36, 13, 1\n"
      "v_fmac_f32_e64 v77, v167, v167\n"
      "v_mfma_f32_16x16x32_bf16 v[128:131], v[182:185], a[188:191], v[128:131]\n"
      "s_waitcnt lgkmcnt(2)\n"
      "v_fmac_f32 v203, v197, v197\n"
      "v_and_b32 v214, s93, v214\n"
      "v_mfma_f32_16x16x32_bf16 v[204:207], v[100:103], a[208:211], v[204:207]\n"
      "v_and_b32 v105, s94, v105\n"
      "v_add_u32 v214, v214, v105\n"
      "v_mfma_f32_16x16x32_bf16 v[204:207], v[182:185], a[212:215], v[204:207]\n"
      "ds_read_b128 v[182:185], v0 offset:14336\n"
      "v_sub_u32 v214, s92, v214\n"
      "v_mfma_f32_16x16x32_bf16 v[104:107], v[152:155], a[144:147], 0\n"
      "s_waitcnt lgkmcnt(1)\n"
      "v_permlane16_swap_b32 v77, v203\n"
      "v_mfma_f32_16x16x32_bf16 v[104:107], v[168:171], a[148:151], v[104:107]\n"
      "v_add_u32_e64 v214, v21, v214\n"
      "buffer_store_dwordx4 v[216:219], v214, s[20:23], 0 offen sc1\n"
      "v_mfma_f32_16x16x32_bf16 v[100:103], v[152:155], a[168:171], 0\n"
      "v_add_f32 v77, v77, v203\n"
      "v_mov_b32 v203, v77\n"
      "v_mfma_f32_16x16x32_bf16 v[100:103], v[168:171], a[172:175], v[100:103]\n"
      "v_bfe_i32 v219, v36, 14, 1\n"
      "v_bfe_i32 v218, v36, 15, 1\n"
      "v_mfma_f32_16x16x32_bf16 v[214:217], v[152:155], a[192:195], 0\n"
      "s_waitcnt lgkmcnt(0)\n"
      "v_and_b32 v219, s93, v219\n"
      "v_and_b32_e64 v218, s94, v218\n"
      "v_mfma_f32_16x16x32_bf16 v[214:217], v[168:171], a[196:199], v[214:217]\n"
      "ds_read_b128 v[168:171], v1 offset:14336\n"
      "v_add_u32 v219, v219, v218\n"
      "v_mfma_f32_16x16x32_bf16 v[104:107], v[140:143], a[152:155], v[104:107]\n"
      "v_sub_u32 v219, s92, v219\n"
      "v_permlane32_swap_b32 v77, v203\n"
      "v_mfma_f32_16x16x32_bf16 v[104:107], v[220:223], a[156:159], v[104:107]\n"
      "v_add_u32 v219, v22, v219\n"
      "v_add_f32_e64 v77, v77, v203\n"
      "v_mfma_f32_16x16x32_bf16 v[100:103], v[140:143], a[176:179], v[100:103]\n"
      "v_rsq_f32 v77, v77\n"
      "v_mfma_f32_16x16x32_bf16 v[100:103], v[220:223], a[180:183], v[100:103]\n"
      "buffer_store_dwordx4 v[108:111], v219, s[20:23], 0 offen sc1\n"
      "v_mfma_f32_16x16x32_bf16 v[214:217], v[140:143], a[200:203], v[214:217]\n"
      "s_waitcnt lgkmcnt(0)\n"
      "v_bfe_i32 v154, v36, 16, 1\n"
      "v_bfe_i32 v152, v36, 17, 1\n"
      "v_mfma_f32_16x16x32_bf16 v[214:217], v[220:223], a[204:207], v[214:217]\n"
      "ds_read_b128 v[140:143], v10 offset:32\n"
      "v_and_b32 v154, s93, v154\n"
      "v_mfma_f32_16x16x32_bf16 v[104:107], v[182:185], a[160:163], v[104:107]\n"
      "ds_read_b64 v[218:219], v10 offset:24\n"
      "v_and_b32 v152, s94, v152\n"
      "v_mfma_f32_16x16x32_bf16 v[104:107], v[168:171], a[164:167], v[104:107]\n"
      "ds_read_b128 v[220:223], v0\n"
      "v_add_u32 v154, v154, v152\n"
      "v_mfma_f32_16x16x32_bf16 v[100:103], v[182:185], a[184:187], v[100:103]\n"
      "v_mov_b32 v203, v77\n"
      "v_sub_u32_e64 v154, s92, v154\n"
      "v_mfma_f32_16x16x32_bf16 v[100:103], v[168:171], a[188:191], v[100:103]\n"
      "v_add_u32 v154, v23, v154\n"
      "v_permlane16_swap_b32 v77, v203\n"
      "v_mfma_f32_16x16x32_bf16 v[214:217], v[182:185], a[208:211], v[214:217]\n"
      "ds_read_b128 v[182:185], v1\n"
      "v_bfe_i32 v152, v36, 18, 1\n"
      "v_mfma_f32_16x16x32_bf16 v[214:217], v[168:171], a[212:215], v[214:217]\n"
      "buffer_store_dwordx4 v[178:181], v154, s[20:23], 0 offen sc1\n"
      "v_mfma_f32_16x16x32_bf16 v[168:171], v[156:159], v[132:135], v[40:43]\n"
      "s_waitcnt lgkmcnt(1)\n"
      "v_bfe_i32 v179, v36, 19, 1\n"
      "v_and_b32 v152, s93, v152\n"
      "v_and_b32 v179, s94, v179\n"
      "v_mul_f32 v77, s50, v77\n"
      "v_add_u32 v152, v152, v179\n"
      "v_mul_f32 v96, v203, v96\n"
      "v_mul_f32 v97, v203, v97\n"
      "v_mul_f32 v208, v77, v208\n"
      "v_mul_f32 v209, v77, v209\n"
      "v_sub_u32 v152, s92, v152\n"
      "v_cvt_pk_bf16_f32 v96, v96, v97\n"
      "v_mul_f32 v98, v203, v98\n"
      "v_mul_f32 v99, v203, v99\n"
      "v_mul_f32 v194, v203, v194\n"
      "v_mul_f32 v195, v203, v195\n"
      "v_cvt_pk_bf16_f32 v208, v208, v209\n"
      "v_mul_f32 v210, v77, v210\n"
      "v_mul_f32 v211, v77, v211\n"
      "v_mul_f32 v164, v77, v164\n"
      "v_mul_f32 v165, v77, v165\n"
      "v_add_u32 v152, v25, v152\n"
      "v_cvt_pk_bf16_f32 v97, v98, v99\n"
      "v_mul_f32 v136, v203, v136\n"
      "v_mul_f32 v137, v203, v137\n"
      "v_mul_f32 v138, v203, v138\n"
      "v_mul_f32_e64 v139, v203, v139\n"
      "v_cvt_pk_bf16_f32 v194, v194, v195\n"
      "v_mul_f32 v196, v203, v196\n"
      "v_mul_f32 v197, v203, v197\n"
      "v_cvt_pk_bf16_f32 v209, v210, v211\n"
      "v_mul_f32 v160, v77, v160\n"
      "v_mul_f32 v161, v77, v161\n"
      "v_mul_f32 v162, v77, v162\n"
      "v_mul_f32 v163, v77, v163\n"
      "v_cvt_pk_bf16_f32 v164, v164, v165\n"
      "v_mul_f32 v166, v77, v166\n"
      "v_mul_f32 v167, v77, v167\n"
      "buffer_store_dwordx4 v[190:193], v152, s[20:23], 0 offen sc1\n"
      "v_cvt_pk_bf16_f32 v98, v136, v137\n"
      "v_cvt_pk_bf16_f32 v99, v138, v139\n"
      "v_cvt_pk_bf16_f32 v195, v196, v197\n"
      "v_cvt_pk_bf16_f32 v210, v160, v161\n"
      "v_mfma_f32_16x16x32_bf16 v[178:181], v[96:99], v[132:135], v[44:47]\n"
      "ds_read_b128 v[190:193], v0 offset:6144\n"
      "v_cvt_pk_bf16_f32 v211, v162, v163\n"
      "v_cvt_pk_bf16_f32 v165, v166, v167\n"
      "v_fma_f32 v77, v186, v186, s51\n"
      "v_fma_f32 v109, v198, v198, s51\n"
      "v_fmac_f32 v77, v187, v187\n"
      "v_fmac_f32 v109, v199, v199\n"
      "v_fmac_f32 v77, v188, v188\n"
      "v_fmac_f32 v109, v200, v200\n"
      "v_fmac_f32 v77, v189, v189\n"
      "v_fmac_f32 v109, v201, v201\n"
      "v_fmac_f32 v77, v124, v124\n"
      "v_fmac_f32 v109, v120, v120\n"
      "v_fmac_f32 v77, v125, v125\n"
      "v_fmac_f32 v109, v121, v121\n"
      "v_fmac_f32 v77, v126, v126\n"
      "v_fmac_f32 v109, v122, v122\n"
      "v_fmac_f32 v77, v127, v127\n"
      "v_fmac_f32 v109, v123, v123\n"
      "v_fmac_f32 v77, v148, v148\n"
      "v_fmac_f32 v109, v144, v144\n"
      "v_fmac_f32 v77, v149, v149\n"
      "v_fmac_f32 v109, v145, v145\n"
      "v_fmac_f32 v77, v150, v150\n"
      "v_fmac_f32 v109, v146, v146\n"
      "v_fmac_f32 v77, v151, v151\n"
      "v_fmac_f32 v109, v147, v147\n"
      "v_subrev_u32 v138, 115456, v9\n"
      "v_bfe_u32 v203, v138, 4, 4\n"
      "v_permlane16_swap_b32 v77, v109\n"
      "v_add_f32 v77, v77, v109\n"
      "v_mov_b32 v109, v77\n"
      "v_mul_u32_u24 v203, 98, v203\n"
      "v_lshrrev_b32 v138, 8, v138\n"
      "v_permlane32_swap_b32 v77, v109\n"
      "v_add_f32 v77, v77, v109\n"
      "v_rsq_f32_e64 v77, v77\n"
      "v_lshl_add_u32 v203, v138, 3, v203\n"
      "v_mov_b32 v109, v77\n"
      "v_add_u32 v203, s77, v203\n"
      "v_and_b32 v139, 2, v203\n"
      "v_permlane16_swap_b32 v77, v109\n"
      "v_mul_f32 v198, v109, v198\n"
      "v_mul_f32 v199, v109, v199\n"
      "v_cvt_pk_bf16_f32 v198, v198, v199\n"
      "v_mul_f32 v200, v109, v200\n"
      "v_mul_f32 v201, v109, v201\n"
      "v_cvt_pk_bf16_f32 v199, v200, v201\n"
      "v_mul_f32 v120, v109, v120\n"
      "v_mul_f32 v121, v109, v121\n"
      "v_mul_f32 v122, v109, v122\n"
      "v_mul_f32 v123, v109, v123\n"
      "v_cvt_pk_bf16_f32 v200, v120, v121\n"
      "v_cvt_pk_bf16_f32 v201, v122, v123\n"
      "v_mul_f32 v77, s50, v77\n"
      "v_mul_f32 v186, v77, v186\n"
      "v_mfma_f32_16x16x32_bf16 v[120:123], v[198:201], v[132:135], v[48:51]\n"
      "ds_read_b128 v[160:163], v1 offset:6144\n"
      "v_mul_f32 v187, v77, v187\n"
      "v_mfma_f32_16x16x32_bf16 v[152:155], v[140:143], v[132:135], v[24:27]\n"
      "s_waitcnt lgkmcnt(1)\n"
      "v_mul_f32 v144, v109, v144\n"
      "v_mul_f32 v145, v109, v145\n"
      "v_mfma_f32_16x16x16_bf16 v[168:171], v[172:173], v[212:213], v[168:171]\n"
      "v_cvt_pk_bf16_f32 v186, v186, v187\n"
      "v_mul_f32 v188, v77, v188\n"
      "v_mul_f32 v189, v77, v189\n"
      "v_mfma_f32_16x16x16_bf16 v[178:181], v[194:195], v[212:213], v[178:181]\n"
      "v_mul_f32 v148, v77, v148\n"
      "v_mul_f32 v149, v77, v149\n"
      "v_cvt_pk_bf16_f32 v144, v144, v145\n"
      "v_mul_f32 v146, v109, v146\n"
      "v_mul_f32 v147, v109, v147\n"
      "v_cvt_pk_bf16_f32 v187, v188, v189\n"
      "v_mul_f32 v124, v77, v124\n"
      "v_mul_f32 v125, v77, v125\n"
      "v_mul_f32 v126, v77, v126\n"
      "v_mul_f32 v127, v77, v127\n"
      "v_cvt_pk_bf16_f32 v148, v148, v149\n"
      "v_mul_f32_e64 v150, v77, v150\n"
      "v_mul_f32_e64 v151, v77, v151\n"
      "v_cvt_pk_bf16_f32 v145, v146, v147\n"
      "v_cvt_pk_bf16_f32 v188, v124, v125\n"
      "v_cvt_pk_bf16_f32 v189, v126, v127\n"
      "v_mfma_f32_16x16x16_bf16 v[120:123], v[144:145], v[212:213], v[120:123]\n"
      "v_cvt_pk_bf16_f32 v149, v150, v151\n"
      "v_and_b32 v203, -4, v203\n"
      "v_add_u32 v203, 110592, v203\n"
      "v_mfma_f32_16x16x16_bf16 v[152:155], v[218:219], v[212:213], v[152:155]\n"
      "ds_read_b128 v[124:127], v0 offset:12288\n"
      "v_cvt_pk_bf16_f32 v107, v106, v107\n"
      "v_mfma_f32_16x16x32_bf16 v[132:135], v[220:223], a[144:147], 0\n"
      "ds_read_b128 v[108:111], v1 offset:12288\n"
      "v_cvt_pk_bf16_f32 v106, v104, v105\n"
      "v_mfma_f32_16x16x32_bf16 v[132:135], v[182:185], a[148:151], v[132:135]\n"
      "s_waitcnt lgkmcnt(2)\n"
      "v_cvt_pk_bf16_f32 v76, v174, v175\n"
      "v_cvt_pk_bf16_f32 v77, v176, v177\n"
      "v_mfma_f32_16x16x32_bf16 v[174:177], v[220:223], a[168:171], 0\n"
      "v_cvt_pk_bf16_f32 v103, v102, v103\n"
      "v_cvt_pk_bf16_f32 v102, v100, v101\n"
      "v_mfma_f32_16x16x32_bf16 v[174:177], v[182:185], a[172:175], v[174:177]\n"
      "v_cvt_pk_bf16_f32 v80, v128, v129\n"
      "v_cvt_pk_bf16_f32 v81, v130, v131\n"
      "v_mfma_f32_16x16x32_bf16 v[128:131], v[220:223], a[192:195], 0\n"
      "v_cvt_pk_bf16_f32 v217, v216, v217\n"
      "v_cvt_pk_bf16_f32 v216, v214, v215\n"
      "v_mfma_f32_16x16x32_bf16 v[128:131], v[182:185], a[196:199], v[128:131]\n"
      "s_waitcnt lgkmcnt(1)\n"
      "v_cvt_pk_bf16_f32 v84, v204, v205\n"
      "v_cvt_pk_bf16_f32 v85, v206, v207\n"
      "v_mfma_f32_16x16x32_bf16 v[132:135], v[190:193], a[152:155], v[132:135]\n"
      "s_waitcnt lgkmcnt(0)\n"
      "v_subrev_u32 v155, 115456, v9\n"
      "v_bfe_u32 v147, v155, 4, 4\n"
      "v_mfma_f32_16x16x32_bf16 v[132:135], v[160:163], a[156:159], v[132:135]\n"
      "v_mul_u32_u24 v147, 98, v147\n"
      "v_lshrrev_b32 v155, 8, v155\n"
      "v_mfma_f32_16x16x32_bf16 v[174:177], v[190:193], a[176:179], v[174:177]\n"
      "v_lshl_add_u32 v147, v155, 3, v147\n"
      "v_add_u32 v147, s77, v147\n"
      "v_mfma_f32_16x16x32_bf16 v[174:177], v[160:163], a[180:183], v[174:177]\n"
      "v_and_b32 v154, 2, v147\n"
      "v_and_b32 v147, -4, v147\n"
      "v_mfma_f32_16x16x32_bf16 v[128:131], v[190:193], a[200:203], v[128:131]\n"
      "v_add_u32 v147, 112160, v147\n"
      "v_subrev_u32 v114, 115456, v9\n"
      "v_mfma_f32_16x16x32_bf16 v[128:131], v[160:163], a[204:207], v[128:131]\n"
      "v_bfe_u32 v113, v114, 4, 4\n"
      "v_mul_u32_u24 v113, 98, v113\n"
      "v_mfma_f32_16x16x32_bf16 v[132:135], v[124:127], a[160:163], v[132:135]\n"
      "v_lshrrev_b32 v114, 8, v114\n"
      "v_lshl_add_u32 v113, v114, 3, v113\n"
      "v_mfma_f32_16x16x32_bf16 v[132:135], v[108:111], a[164:167], v[132:135]\n"
      "ds_read_b128 v[212:215], v2 offset:18432\n"
      "v_add_u32 v113, s77, v113\n"
      "v_mfma_f32_16x16x32_bf16 v[174:177], v[124:127], a[184:187], v[174:177]\n"
      "v_and_b32 v39, 2, v113\n"
      "v_and_b32 v113, -4, v113\n"
      "v_mfma_f32_16x16x32_bf16 v[174:177], v[108:111], a[188:191], v[174:177]\n"
      "ds_read2_b32 v[222:223], v203 offset0:0 offset1:1\n"
      "ds_read2_b32 v[206:207], v203 offset0:8 offset1:9\n"
      "v_mfma_f32_16x16x32_bf16 v[128:131], v[124:127], a[208:211], v[128:131]\n"
      "ds_read2_b32 v[220:221], v203 offset0:2 offset1:10\n"
      "ds_read2_b32 v[150:151], v203 offset0:16 offset1:17\n"
      "v_mfma_f32_16x16x32_bf16 v[128:131], v[108:111], a[212:215], v[128:131]\n"
      "ds_read2_b32 v[166:167], v203 offset0:18 offset1:24\n"
      "ds_read_b128 v[124:127], v2 offset:21504\n"
      "v_mfma_f32_16x16x32_bf16 v[202:205], v[156:159], v[208:211], v[52:55]\n"
      "v_cvt_pk_bf16_f32 v104, v132, v133\n"
      "v_cvt_pk_bf16_f32 v105, v134, v135\n"
      "v_mfma_f32_16x16x32_bf16 v[190:193], v[96:99], v[208:211], v[56:59]\n"
      "ds_read_b128 v[182:185], v2 offset:24576\n"
      "v_cvt_pk_bf16_f32 v100, v174, v175\n"
      "v_mfma_f32_16x16x32_bf16 v[132:135], v[198:201], v[208:211], v[60:63]\n"
      "s_waitcnt lgkmcnt(7)\n"
      "s_waitcnt lgkmcnt(6)\n"
      "v_alignbyte_b32 v222, v223, v222, v139\n"
      "v_mfma_f32_16x16x32_bf16 v[160:163], v[140:143], v[208:211], v[26:29]\n"
      "s_waitcnt lgkmcnt(5)\n"
      "v_alignbyte_b32 v206, v207, v206, v139\n"
      "s_waitcnt lgkmcnt(4)\n"
      "v_alignbyte_b32 v223, v220, v223, v139\n"
      "v_mfma_f32_16x16x16_bf16 v[202:205], v[172:173], v[164:165], v[202:205]\n"
      "v_lshlrev_b32 v220, 16, v222\n"
      "v_and_b32 v208, 0xffff0000, v222\n"
      "v_lshlrev_b32 v222, 16, v223\n"
      "v_mfma_f32_16x16x16_bf16 v[190:193], v[194:195], v[164:165], v[190:193]\n"
      "v_alignbyte_b32 v207, v221, v207, v139\n"
      "s_waitcnt lgkmcnt(2)\n"
      "v_alignbyte_b32 v150, v151, v150, v139\n"
      "v_mfma_f32_16x16x16_bf16 v[132:135], v[144:145], v[164:165], v[132:135]\n"
      "v_fmac_f32 v168, s53, v220\n"
      "v_fmac_f32 v169, s53, v208\n"
      "v_fmac_f32 v170, s53, v222\n"
      "v_mfma_f32_16x16x16_bf16 v[160:163], v[218:219], v[164:165], v[160:163]\n"
      "v_and_b32 v101, 0xffff0000, v223\n"
      "v_lshlrev_b32_e64 v174, 16, v206\n"
      "v_and_b32 v175, 0xffff0000, v206\n"
      "v_mfma_f32_16x16x32_bf16 v[220:223], a[216:219], v[212:215], 0\n"
      "s_waitcnt lgkmcnt(0)\n"
      "v_lshlrev_b32 v210, 16, v207\n"
      "v_and_b32 v211, 0xffff0000, v207\n"
      "v_mfma_f32_16x16x32_bf16 v[206:209], a[240:243], v[212:215], 0\n"
      "ds_read_u16 v78, v11\n"
      "v_alignbyte_b32 v151, v166, v151, v139\n"
      "v_mfma_f32_16x16x32_bf16 v[162:165], v[232:235], v[212:215], 0\n"
      "ds_read_u16 v82, v11 offset:32\n"
      "v_fmac_f32 v171, s53, v101\n"
      "v_mfma_f32_16x16x32_bf16 v[220:223], a[220:223], v[124:127], v[220:223]\n"
      "ds_read_u16 v86, v11 offset:64\n"
      "v_fmac_f32 v178, s53, v174\n"
      "v_mfma_f32_16x16x32_bf16 v[206:209], a[244:247], v[124:127], v[206:209]\n"
      "v_fmac_f32 v179, s53, v175\n"
      "v_fmac_f32 v180, s53, v210\n"
      "v_mfma_f32_16x16x32_bf16 v[162:165], v[236:239], v[124:127], v[162:165]\n"
      "v_fmac_f32 v181, s53, v211\n"
      "v_lshlrev_b32 v101, 16, v150\n"
      "v_mfma_f32_16x16x32_bf16 v[220:223], a[224:227], v[182:185], v[220:223]\n"
      "v_and_b32 v214, 0xffff0000, v150\n"
      "v_lshlrev_b32_e64 v215, 16, v151\n"
      "v_mfma_f32_16x16x32_bf16 v[206:209], a[248:251], v[182:185], v[206:209]\n"
      "s_waitcnt lgkmcnt(2)\n"
      "v_and_b32 v175, 0xffff0000, v151\n"
      "v_alignbyte_b32 v138, v167, v167, v139\n"
      "v_mfma_f32_16x16x32_bf16 v[162:165], v[240:243], v[182:185], v[162:165]\n"
      "s_waitcnt lgkmcnt(1)\n"
      "v_max3_f32 v182, v168, v169, v170\n"
      "v_fmac_f32 v120, s53, v101\n"
      "v_fmac_f32 v121, s53, v214\n"
      "v_fmac_f32 v122, s53, v215\n"
      "v_fmac_f32 v123, s53, v175\n"
      "v_lshlrev_b32_e64 v138, 16, v138\n"
      "v_max3_f32 v101, v171, v178, v179\n"
      "v_max3_f32 v182, v182, v180, v181\n"
      "v_fmac_f32_e64 v152, s53, v138\n"
      "v_max3_f32 v101, v101, v120, v121\n"
      "v_max3_f32 v182, v182, v122, v123\n"
      "v_max3_f32 v182, v182, v101, v152\n"
      "v_mov_b32_e64 v175, v182\n"
      "v_cvt_pk_bf16_f32 v101, v176, v177\n"
      "v_cvt_pk_bf16_f32 v214, v128, v129\n"
      "v_permlane16_swap_b32 v182, v175\n"
      "v_max_f32 v182, v182, v175\n"
      "v_mov_b32_e64 v175, v182\n"
      "v_cvt_pk_bf16_f32 v215, v130, v131\n"
      "v_add_u32 v113, 113728, v113\n"
      "v_permlane32_swap_b32 v182, v175\n"
      "v_max_f32 v182, v182, v175\n"
      "v_sub_f32 v168, v168, v182\n"
      "v_sub_f32 v169, v169, v182\n"
      "v_sub_f32 v170, v170, v182\n"
      "v_sub_f32 v171, v171, v182\n"
      "v_exp_f32 v168, v168\n"
      "v_exp_f32 v169, v169\n"
      "v_sub_f32 v178, v178, v182\n"
      "v_sub_f32 v179, v179, v182\n"
      "v_sub_f32 v180, v180, v182\n"
      "v_sub_f32 v181, v181, v182\n"
      "v_exp_f32 v170, v170\n"
      "v_exp_f32 v171, v171\n"
      "v_exp_f32 v178, v178\n"
      "v_exp_f32 v179, v179\n"
      "v_exp_f32 v180, v180\n"
      "v_exp_f32 v181, v181\n"
      "v_cvt_pk_bf16_f32 v168, v168, v169\n"
      "v_cvt_pk_bf16_f32 v169, v170, v171\n"
      "v_cvt_pk_bf16_f32 v170, v178, v179\n"
      "v_cvt_pk_bf16_f32 v171, v180, v181\n"
      "v_sub_f32 v120, v120, v182\n"
      "v_sub_f32 v121, v121, v182\n"
      "v_mfma_f32_16x16x32_bf16 v[174:177], v[32:35], v[168:171], 0\n"
      "s_waitcnt lgkmcnt(0)\n"
      "v_sub_f32 v122, v122, v182\n"
      "v_sub_f32 v123, v123, v182\n"
      "v_sub_f32 v152, v152, v182\n"
      "v_exp_f32 v120, v120\n"
      "v_exp_f32 v121, v121\n"
      "v_exp_f32_e64 v122, v122\n"
      "v_exp_f32_e64 v123, v123\n"
      "v_exp_f32_e64 v152, v152\n"
      "v_cvt_pk_bf16_f32 v88, v120, v121\n"
      "v_cvt_pk_bf16_f32 v89, v122, v123\n"
      "v_cvt_pk_bf16_f32 v90, v152, 0\n"
      "v_subrev_u32 v109, 115456, v9\n"
      "v_lshrrev_b32_e64 v109, 3, v109\n"
      "v_mfma_f32_16x16x32_bf16 v[174:177], v[32:35], v[88:91], v[174:177]\n"
      "v_min_u32 v109, 96, v109\n"
      "v_add_u32_e64 v109, s77, v109\n"
      "v_mfma_f32_16x16x32_bf16 v[180:183], v[104:107], v[168:171], 0\n"
      "v_add_u32 v109, 115296, v109\n"
      "s_waitcnt vmcnt(6)\n"
      "v_or3_b32 v116, v116, v117, v118\n"
      "v_mfma_f32_16x16x32_bf16 v[180:183], v[76:79], v[88:91], v[180:183]\n"
      "ds_read_b128 v[124:127], v2 offset:27648\n"
      "s_waitcnt vmcnt(5)\n"
      "v_or3_b32 v116, v116, v119, v92\n"
      "v_mfma_f32_16x16x32_bf16 v[120:123], v[100:103], v[168:171], 0\n"
      "v_rcp_f32_e64 v174, v174\n"
      "v_mfma_f32_16x16x32_bf16 v[120:123], v[80:83], v[88:91], v[120:123]\n"
      "v_and_b32 v116, 0x7fff7fff, v116\n"
      "v_readlane_b32 s45, v37, s43\n"
      "v_mfma_f32_16x16x32_bf16 v[210:213], v[214:217], v[168:171], 0\n"
      "ds_read_b128 v[166:169], v2 offset:30720\n"
      "v_mul_f32 v180, v174, v180\n"
      "v_mfma_f32_16x16x32_bf16 v[210:213], v[84:87], v[88:91], v[210:213]\n"
      "ds_write_b32 v31, v116\n"
      "v_mfma_f32_16x16x32_bf16 v[150:153], v[156:159], v[186:189], v[64:67]\n"
      "v_mul_f32 v181, v174, v181\n"
      "v_cvt_pk_bf16_f32 v180, v180, v181\n"
      "v_mfma_f32_16x16x32_bf16 v[176:179], v[96:99], v[186:189], v[68:71]\n"
      "ds_read_b128 v[128:131], v2 offset:33792\n"
      "v_mul_f32_e64 v182, v174, v182\n"
      "v_mfma_f32_16x16x32_bf16 v[116:119], v[198:201], v[186:189], v[72:75]\n"
      "s_waitcnt lgkmcnt(3)\n"
      "v_mul_f32 v183, v174, v183\n"
      "v_cvt_pk_bf16_f32 v181, v182, v183\n"
      "v_mfma_f32_16x16x32_bf16 v[136:139], v[140:143], v[186:189], v[28:31]\n"
      "v_mul_f32 v120, v174, v120\n"
      "v_mul_f32 v121, v174, v121\n"
      "v_mfma_f32_16x16x16_bf16 v[150:153], v[172:173], v[148:149], v[150:153]\n"
      "v_mul_f32_e64 v122, v174, v122\n"
      "v_mul_f32_e64 v123, v174, v123\n"
      "v_cvt_pk_bf16_f32 v182, v120, v121\n"
      "v_mfma_f32_16x16x16_bf16 v[176:179], v[194:195], v[148:149], v[176:179]\n"
      "v_cvt_pk_bf16_f32 v183, v122, v123\n"
      "ds_read_b128 v[120:123], v2 offset:18688\n"
      "v_mfma_f32_16x16x16_bf16 v[116:119], v[144:145], v[148:149], v[116:119]\n"
      "v_mul_f32 v210, v174, v210\n"
      "v_mul_f32_e64 v211, v174, v211\n"
      "v_cvt_pk_bf16_f32 v210, v210, v211\n"
      "v_mfma_f32_16x16x16_bf16 v[136:139], v[218:219], v[148:149], v[136:139]\n"
      "ds_read2_b32 v[184:185], v147 offset0:0 offset1:1\n"
      "ds_read2_b32 v[170:171], v147 offset0:8 offset1:9\n"
      "v_mfma_f32_16x16x32_bf16 v[220:223], a[228:231], v[124:127], v[220:223]\n"
      "s_waitcnt lgkmcnt(3)\n"
      "ds_read2_b32 v[196:197], v147 offset0:2 offset1:10\n"
      "v_mfma_f32_16x16x32_bf16 v[206:209], a[252:255], v[124:127], v[206:209]\n"
      "ds_write_b128 v3, v[180:183]\n"
      "v_mfma_f32_16x16x32_bf16 v[162:165], v[244:247], v[124:127], v[162:165]\n"
      "ds_read2_b32 v[138:139], v147 offset0:16 offset1:17\n"
      "ds_read2_b32 v[88:89], v147 offset0:18 offset1:24\n"
      "v_mfma_f32_16x16x32_bf16 v[220:223], a[232:235], v[166:169], v[220:223]\n"
      "ds_read_b128 v[146:149], v2 offset:21760\n"
      "s_waitcnt lgkmcnt(6)\n"
      "v_alignbyte_b32 v184, v185, v184, v154\n"
      "v_mfma_f32_16x16x32_bf16 v[206:209], v[224:227], v[166:169], v[206:209]\n"
      "s_waitcnt lgkmcnt(5)\n"
      "v_alignbyte_b32 v170, v171, v170, v154\n"
      "v_lshlrev_b32 v183, 16, v184\n"
      "v_mfma_f32_16x16x32_bf16 v[162:165], v[248:251], v[166:169], v[162:165]\n"
      "ds_read_b128 v[186:189], v2 offset:24832\n"
      "s_waitcnt lgkmcnt(5)\n"
      "v_alignbyte_b32 v185, v196, v185, v154\n"
      "v_mfma_f32_16x16x32_bf16 v[220:223], a[236:239], v[128:131], v[220:223]\n"
      "v_and_b32 v169, 0xffff0000, v184\n"
      "v_lshlrev_b32_e64 v181, 16, v185\n"
      "v_mfma_f32_16x16x32_bf16 v[206:209], v[228:231], v[128:131], v[206:209]\n"
      "v_alignbyte_b32 v171, v197, v171, v154\n"
      "s_waitcnt lgkmcnt(3)\n"
      "v_alignbyte_b32 v138, v139, v138, v154\n"
      "v_mfma_f32_16x16x32_bf16 v[162:165], v[252:255], v[128:131], v[162:165]\n"
      "s_waitcnt lgkmcnt(2)\n"
      "v_fmac_f32 v202, s53, v183\n"
      "v_fmac_f32 v203, s53, v169\n"
      "v_mfma_f32_16x16x32_bf16 v[166:169], a[216:219], v[120:123], 0\n"
      "s_waitcnt lgkmcnt(1)\n"
      "v_fmac_f32 v204, s53, v181\n"
      "v_and_b32 v131, 0xffff0000, v185\n"
      "v_mfma_f32_16x16x32_bf16 v[180:183], a[240:243], v[120:123], 0\n"
      "v_alignbyte_b32 v139, v88, v139, v154\n"
      "v_lshlrev_b32 v130, 16, v170\n"
      "v_mfma_f32_16x16x32_bf16 v[124:127], v[232:235], v[120:123], 0\n"
      "s_waitcnt lgkmcnt(0)\n"
      "v_and_b32 v121, 0xffff0000, v170\n"
      "v_fmac_f32 v205, s53, v131\n"
      "v_mfma_f32_16x16x32_bf16 v[166:169], a[220:223], v[146:149], v[166:169]\n"
      "v_lshlrev_b32 v170, 16, v171\n"
      "v_and_b32 v131, 0xffff0000, v171\n"
      "v_mfma_f32_16x16x32_bf16 v[180:183], a[244:247], v[146:149], v[180:183]\n"
      "v_fmac_f32 v190, s53, v130\n"
      "v_fmac_f32 v191, s53, v121\n"
      "v_mfma_f32_16x16x32_bf16 v[124:127], v[236:239], v[146:149], v[124:127]\n"
      "ds_read_b128 v[120:123], v10\n"
      "v_fmac_f32 v192, s53, v170\n"
      "v_mfma_f32_16x16x32_bf16 v[166:169], a[224:227], v[186:189], v[166:169]\n"
      "v_fmac_f32 v193, s53, v131\n"
      "v_lshlrev_b32_e64 v131, 16, v138\n"
      "v_mfma_f32_16x16x32_bf16 v[180:183], a[248:251], v[186:189], v[180:183]\n"
      "v_and_b32 v130, 0xffff0000, v138\n"
      "v_lshlrev_b32_e64 v146, 16, v139\n"
      "v_mfma_f32_16x16x32_bf16 v[124:127], v[240:243], v[186:189], v[124:127]\n"
      "ds_read_b64 v[170:171], v10 offset:16\n"
      "v_and_b32 v188, 0xffff0000, v139\n"
      "v_alignbyte_b32 v155, v89, v89, v154\n"
      "v_max3_f32 v147, v202, v203, v204\n"
      "v_fmac_f32 v132, s53, v131\n"
      "v_fmac_f32 v133, s53, v130\n"
      "v_fmac_f32 v134, s53, v146\n"
      "v_fmac_f32 v135, s53, v188\n"
      "v_lshlrev_b32 v155, 16, v155\n"
      "v_max3_f32 v188, v205, v190, v191\n"
      "v_max3_f32 v147, v147, v192, v193\n"
      "v_fmac_f32 v160, s53, v155\n"
      "v_max3_f32 v188, v188, v132, v133\n"
      "v_max3_f32 v147, v147, v134, v135\n"
      "v_max3_f32 v147, v147, v188, v160\n"
      "v_mov_b32 v188, v147\n"
      "v_mul_f32 v111, v220, v220\n"
      "v_add_f32 v110, v220, v221\n"
      "v_permlane16_swap_b32 v147, v188\n"
      "v_max_f32 v147, v147, v188\n"
      "v_mov_b32 v188, v147\n"
      "v_fmac_f32 v111, v221, v221\n"
      "v_add_f32 v110, v110, v222\n"
      "v_permlane32_swap_b32 v147, v188\n"
      "v_max_f32 v147, v147, v188\n"
      "v_sub_f32 v202, v202, v147\n"
      "v_sub_f32 v203, v203, v147\n"
      "v_sub_f32 v204, v204, v147\n"
      "v_sub_f32 v205, v205, v147\n"
      "v_exp_f32 v202, v202\n"
      "v_exp_f32 v203, v203\n"
      "v_sub_f32 v190, v190, v147\n"
      "v_sub_f32 v191, v191, v147\n"
      "v_sub_f32 v192, v192, v147\n"
      "v_sub_f32 v193, v193, v147\n"
      "v_exp_f32 v204, v204\n"
      "v_exp_f32 v205, v205\n"
      "v_exp_f32 v190, v190\n"
      "v_exp_f32 v191, v191\n"
      "v_exp_f32 v192, v192\n"
      "v_exp_f32 v193, v193\n"
      "v_cvt_pk_bf16_f32 v202, v202, v203\n"
      "v_cvt_pk_bf16_f32 v203, v204, v205\n"
      "v_cvt_pk_bf16_f32 v204, v190, v191\n"
      "v_cvt_pk_bf16_f32 v205, v192, v193\n"
      "v_sub_f32 v132, v132, v147\n"
      "v_sub_f32 v133, v133, v147\n"
      "v_mfma_f32_16x16x32_bf16 v[188:191], v[32:35], v[202:205], 0\n"
      "s_waitcnt lgkmcnt(0)\n"
      "v_sub_f32 v134, v134, v147\n"
      "v_sub_f32 v135, v135, v147\n"
      "v_sub_f32 v160, v160, v147\n"
      "v_exp_f32 v132, v132\n"
      "v_exp_f32 v133, v133\n"
      "v_exp_f32 v134, v134\n"
      "v_exp_f32 v135, v135\n"
      "v_exp_f32_e64 v160, v160\n"
      "v_cvt_pk_bf16_f32 v92, v132, v133\n"
      "v_cvt_pk_bf16_f32 v93, v134, v135\n"
      "v_cvt_pk_bf16_f32 v94, v160, 0\n"
      "v_fmac_f32 v111, v222, v222\n"
      "v_add_f32 v110, v110, v223\n"
      "v_mfma_f32_16x16x32_bf16 v[188:191], v[32:35], v[92:95], v[188:191]\n"
      "v_fmac_f32 v111, v223, v223\n"
      "v_add_f32 v110, v110, v206\n"
      "v_mfma_f32_16x16x32_bf16 v[130:133], v[104:107], v[202:205], 0\n"
      "v_fmac_f32 v111, v206, v206\n"
      "v_add_f32 v110, v110, v207\n"
      "v_mfma_f32_16x16x32_bf16 v[130:133], v[76:79], v[92:95], v[130:133]\n"
      "ds_read_b128 v[146:149], v2 offset:27904\n"
      "v_fmac_f32 v111, v207, v207\n"
      "v_mfma_f32_16x16x32_bf16 v[190:193], v[100:103], v[202:205], 0\n"
      "v_add_f32 v110, v110, v208\n"
      "v_fmac_f32_e64 v111, v208, v208\n"
      "v_mfma_f32_16x16x32_bf16 v[190:193], v[80:83], v[92:95], v[190:193]\n"
      "ds_read2_b32 v[88:89], v113 offset0:0 offset1:1\n"
      "v_add_f32 v110, v110, v209\n"
      "v_mfma_f32_16x16x32_bf16 v[184:187], v[214:217], v[202:205], 0\n"
      "v_fmac_f32 v111, v209, v209\n"
      "v_add_f32 v110, v110, v162\n"
      "v_mfma_f32_16x16x32_bf16 v[184:187], v[84:87], v[92:95], v[184:187]\n"
      "ds_read2_b32 v[134:135], v113 offset0:8 offset1:9\n"
      "v_fmac_f32 v111, v162, v162\n"
      "v_mfma_f32_16x16x32_bf16 v[202:205], v[120:123], v[156:159], v[30:33]\n"
      "v_add_f32 v110, v110, v163\n"
      "v_fmac_f32_e64 v111, v163, v163\n"
      "v_mfma_f32_16x16x32_bf16 v[158:161], v[120:123], v[96:99], v[30:33]\n"
      "s_waitcnt lgkmcnt(2)\n"
      "v_add_f32_e64 v110, v110, v164\n"
      "v_fmac_f32_e64 v111, v164, v164\n"
      "v_mfma_f32_16x16x32_bf16 v[96:99], v[120:123], v[198:201], v[30:33]\n"
      "v_add_f32_e64 v110, v110, v165\n"
      "v_fmac_f32_e64 v111, v165, v165\n"
      "v_mfma_f32_16x16x32_bf16 v[196:199], v[120:123], v[140:143], v[30:33]\n"
      "ds_read2_b32 v[200:201], v113 offset0:2 offset1:10\n"
      "ds_read2_b32 v[156:157], v113 offset0:16 offset1:17\n"
      "v_mfma_f32_16x16x16_bf16 v[202:205], v[170:171], v[172:173], v[202:205]\n"
      "ds_read2_b32 v[154:155], v113 offset0:18 offset1:24\n"
      "ds_read_b128 v[120:123], v2 offset:30976\n"
      "v_mfma_f32_16x16x16_bf16 v[158:161], v[170:171], v[194:195], v[158:161]\n"
      "ds_read_b128 v[138:141], v2 offset:34048\n"
      "s_waitcnt lgkmcnt(5)\n"
      "v_alignbyte_b32 v88, v89, v88, v39\n"
      "v_mfma_f32_16x16x16_bf16 v[96:99], v[170:171], v[144:145], v[96:99]\n"
      "v_alignbyte_b32 v134, v135, v134, v39\n"
      "v_lshlrev_b32 v172, 16, v88\n"
      "v_and_b32 v173, 0xffff0000, v88\n"
      "v_mfma_f32_16x16x16_bf16 v[196:199], v[170:171], v[218:219], v[196:199]\n"
      "v_fmac_f32_e64 v150, s53, v172\n"
      "v_fmac_f32_e64 v151, s53, v173\n"
      "v_lshlrev_b32 v173, 16, v134\n"
      "v_mfma_f32_16x16x32_bf16 v[166:169], a[228:231], v[146:149], v[166:169]\n"
      "v_and_b32 v194, 0xffff0000, v134\n"
      "s_waitcnt lgkmcnt(4)\n"
      "v_alignbyte_b32 v89, v200, v89, v39\n"
      "v_mfma_f32_16x16x32_bf16 v[180:183], a[252:255], v[146:149], v[180:183]\n"
      "s_waitcnt lgkmcnt(3)\n"
      "v_lshlrev_b32 v199, 16, v89\n"
      "v_alignbyte_b32 v135, v201, v135, v39\n"
      "v_mfma_f32_16x16x32_bf16 v[124:127], v[244:247], v[146:149], v[124:127]\n"
      "s_waitcnt lgkmcnt(2)\n"
      "v_alignbyte_b32 v156, v157, v156, v39\n"
      "v_fmac_f32 v152, s53, v199\n"
      "s_waitcnt lgkmcnt(1)\n"
      "v_mfma_f32_16x16x32_bf16 v[166:169], a[232:235], v[120:123], v[166:169]\n"
      "s_waitcnt lgkmcnt(0)\n"
      "v_and_b32 v172, 0xffff0000, v89\n"
      "v_lshlrev_b32_e64 v197, 16, v135\n"
      "v_mfma_f32_16x16x32_bf16 v[180:183], v[224:227], v[120:123], v[180:183]\n"
      "ds_read_b128 v[198:201], v2 offset:18944\n"
      "v_and_b32 v195, 0xffff0000, v135\n"
      "v_mfma_f32_16x16x32_bf16 v[124:127], v[248:251], v[120:123], v[124:127]\n"
      "ds_read_u16 v109, v109\n"
      "v_alignbyte_b32 v157, v154, v157, v39\n"
      "v_mfma_f32_16x16x32_bf16 v[166:169], a[236:239], v[138:141], v[166:169]\n"
      "v_fmac_f32 v153, s53, v172\n"
      "v_fmac_f32 v176, s53, v173\n"
      "v_mfma_f32_16x16x32_bf16 v[180:183], v[228:231], v[138:141], v[180:183]\n"
      "ds_read_b128 v[170:173], v2 offset:22016\n"
      "v_fmac_f32 v177, s53, v194\n"
      "v_mfma_f32_16x16x32_bf16 v[124:127], v[252:255], v[138:141], v[124:127]\n"
      "v_fmac_f32 v178, s53, v197\n"
      "v_fmac_f32 v179, s53, v195\n"
      "v_lshlrev_b32 v195, 16, v156\n"
      "v_and_b32 v194, 0xffff0000, v156\n"
      "v_lshlrev_b32 v197, 16, v157\n"
      "v_and_b32 v139, 0xffff0000, v157\n"
      "v_alignbyte_b32 v114, v155, v155, v39\n"
      "v_max3_f32 v89, v150, v151, v152\n"
      "v_fmac_f32 v116, s53, v195\n"
      "v_fmac_f32 v117, s53, v194\n"
      "v_fmac_f32 v118, s53, v197\n"
      "v_fmac_f32 v119, s53, v139\n"
      "v_lshlrev_b32 v114, 16, v114\n"
      "v_max3_f32 v195, v153, v176, v177\n"
      "v_max3_f32 v89, v89, v178, v179\n"
      "v_fmac_f32 v136, s53, v114\n"
      "v_max3_f32 v195, v195, v116, v117\n"
      "v_max3_f32 v89, v89, v118, v119\n"
      "v_max3_f32 v89, v89, v195, v136\n"
      "v_mov_b32 v195, v89\n"
      "v_cndmask_b32 v202, v202, v158, s[58:59]\n"
      "v_cndmask_b32 v202, v202, v96, s[60:61]\n"
      "v_permlane16_swap_b32 v89, v195\n"
      "v_max_f32 v89, v89, v195\n"
      "v_mov_b32_e64 v195, v89\n"
      "v_cndmask_b32 v202, v202, v196, s[62:63]\n"
      "s_waitcnt lgkmcnt(0)\n"
      "v_lshlrev_b32 v109, 16, v109\n"
      "v_permlane32_swap_b32 v89, v195\n"
      "v_max_f32 v89, v89, v195\n"
      "v_sub_f32 v150, v150, v89\n"
      "v_sub_f32 v151, v151, v89\n"
      "v_sub_f32 v152, v152, v89\n"
      "v_sub_f32 v153, v153, v89\n"
      "v_exp_f32 v150, v150\n"
      "v_exp_f32 v151, v151\n"
      "v_sub_f32 v176, v176, v89\n"
      "v_sub_f32 v177, v177, v89\n"
      "v_sub_f32 v178, v178, v89\n"
      "v_sub_f32 v179, v179, v89\n"
      "v_exp_f32 v152, v152\n"
      "v_exp_f32 v153, v153\n"
      "v_exp_f32 v176, v176\n"
      "v_exp_f32 v177, v177\n"
      "v_exp_f32 v178, v178\n"
      "v_exp_f32 v179, v179\n"
      "v_cvt_pk_bf16_f32 v150, v150, v151\n"
      "v_cvt_pk_bf16_f32 v151, v152, v153\n"
      "v_cvt_pk_bf16_f32 v152, v176, v177\n"
      "v_cvt_pk_bf16_f32 v153, v178, v179\n"
      "v_sub_f32 v116, v116, v89\n"
      "v_sub_f32 v117, v117, v89\n"
      "v_mfma_f32_16x16x32_bf16 v[194:197], v[32:35], v[150:153], 0\n"
      "v_sub_f32 v118, v118, v89\n"
      "v_sub_f32 v119, v119, v89\n"
      "v_sub_f32 v136, v136, v89\n"
      "v_exp_f32 v116, v116\n"
      "v_exp_f32 v117, v117\n"
      "v_exp_f32 v118, v118\n"
      "v_exp_f32 v119, v119\n"
      "v_exp_f32 v136, v136\n"
      "v_cvt_pk_bf16_f32 v88, v116, v117\n"
      "v_cvt_pk_bf16_f32 v89, v118, v119\n"
      "v_cvt_pk_bf16_f32 v90, v136, 0\n"
      "v_fmac_f32 v202, s53, v109\n"
      "v_mul_f32 v139, v166, v166\n"
      "v_mfma_f32_16x16x32_bf16 v[194:197], v[32:35], v[88:91], v[194:197]\n"
      "ds_read_b128 v[176:179], v2 offset:25088\n"
      "v_max_f32_dpp v189, v202, v202 row_ror:8 row_mask:0xf bank_mask:0xf\n"
      "v_mfma_f32_16x16x32_bf16 v[96:99], v[104:107], v[150:153], 0\n"
      "v_add_f32 v138, v166, v167\n"
      "v_max_f32_dpp v189, v189, v189 row_ror:4 row_mask:0xf bank_mask:0xf\n"
      "v_mfma_f32_16x16x32_bf16 v[96:99], v[76:79], v[88:91], v[96:99]\n"
      "v_fmac_f32 v139, v167, v167\n"
      "v_max_f32_dpp v189, v189, v189 row_ror:2 row_mask:0xf bank_mask:0xf\n"
      "v_mfma_f32_16x16x32_bf16 v[116:119], v[100:103], v[150:153], 0\n"
      "ds_read_b128 v[158:161], v2 offset:28160\n"
      "v_add_f32_e64 v138, v138, v168\n"
      "v_mfma_f32_16x16x32_bf16 v[116:119], v[80:83], v[88:91], v[116:119]\n"
      "v_fmac_f32 v139, v168, v168\n"
      "v_add_f32 v138, v138, v169\n"
      "v_mfma_f32_16x16x32_bf16 v[134:137], v[214:217], v[150:153], 0\n"
      "v_fmac_f32 v139, v169, v169\n"
      "v_add_f32 v138, v138, v180\n"
      "v_mfma_f32_16x16x32_bf16 v[134:137], v[84:87], v[88:91], v[134:137]\n"
      "ds_read_b128 v[154:157], v2 offset:31232\n"
      "v_fmac_f32 v139, v180, v180\n"
      "v_mfma_f32_16x16x32_bf16 v[120:123], a[216:219], v[198:201], 0\n"
      "s_waitcnt lgkmcnt(2)\n"
      "v_max_f32_dpp v189, v189, v189 row_ror:1 row_mask:0xf bank_mask:0xf\n"
      "v_add_f32 v138, v138, v181\n"
      "v_mfma_f32_16x16x32_bf16 v[148:151], a[240:243], v[198:201], 0\n"
      "v_fmac_f32 v139, v181, v181\n"
      "v_add_f32_e64 v138, v138, v182\n"
      "v_mfma_f32_16x16x32_bf16 v[144:147], v[232:235], v[198:201], 0\n"
      "ds_read_b128 v[196:199], v2 offset:34304\n"
      "v_fmac_f32 v139, v182, v182\n"
      "v_mfma_f32_16x16x32_bf16 v[120:123], a[220:223], v[170:173], v[120:123]\n"
      "s_waitcnt lgkmcnt(2)\n"
      "v_add_f32 v138, v138, v183\n"
      "v_fmac_f32 v139, v183, v183\n"
      "v_mfma_f32_16x16x32_bf16 v[148:151], a[244:247], v[170:173], v[148:151]\n"
      "v_add_f32 v138, v138, v124\n"
      "v_fmac_f32 v139, v124, v124\n"
      "v_mfma_f32_16x16x32_bf16 v[144:147], v[236:239], v[170:173], v[144:147]\n"
      "s_waitcnt lgkmcnt(1)\n"
      "v_max_f32_dpp v189, v189, v189 row_bcast:15 row_mask:0xa bank_mask:0xf\n"
      "v_add_f32 v138, v138, v125\n"
      "v_mfma_f32_16x16x32_bf16 v[120:123], a[224:227], v[176:179], v[120:123]\n"
      "v_fmac_f32 v139, v125, v125\n"
      "v_add_f32 v138, v138, v126\n"
      "v_mfma_f32_16x16x32_bf16 v[148:151], a[248:251], v[176:179], v[148:151]\n"
      "v_fmac_f32 v139, v126, v126\n"
      "v_add_f32 v138, v138, v127\n"
      "v_mfma_f32_16x16x32_bf16 v[144:147], v[240:243], v[176:179], v[144:147]\n"
      "s_waitcnt lgkmcnt(0)\n"
      "v_fmac_f32 v139, v127, v127\n"
      "v_rcp_f32 v188, v188\n"
      "v_mfma_f32_16x16x32_bf16 v[120:123], a[228:231], v[158:161], v[120:123]\n"
      "v_rcp_f32 v194, v194\n"
      "v_mfma_f32_16x16x32_bf16 v[148:151], a[252:255], v[158:161], v[148:151]\n"
      "v_max_f32_dpp v189, v189, v189 row_bcast:31 row_mask:0xc bank_mask:0xf\n"
      "v_permlane16_swap_b32 v110, v111\n"
      "v_mfma_f32_16x16x32_bf16 v[144:147], v[244:247], v[158:161], v[144:147]\n"
      "v_readlane_b32 s72, v189, 63\n"
      "v_permlane16_swap_b32 v138, v139\n"
      "v_mfma_f32_16x16x32_bf16 v[120:123], a[232:235], v[154:157], v[120:123]\n"
      "v_mul_f32 v130, v188, v130\n"
      "v_mul_f32 v131, v188, v131\n"
      "v_mfma_f32_16x16x32_bf16 v[148:151], v[224:227], v[154:157], v[148:151]\n"
      "v_mul_f32 v96, v194, v96\n"
      "v_mul_f32 v97, v194, v97\n"
      "v_mfma_f32_16x16x32_bf16 v[144:147], v[248:251], v[154:157], v[144:147]\n"
      "v_cvt_pk_bf16_f32 v130, v130, v131\n"
      "v_mul_f32 v132, v188, v132\n"
      "v_mfma_f32_16x16x32_bf16 v[120:123], a[236:239], v[196:199], v[120:123]\n"
      "v_mul_f32 v133, v188, v133\n"
      "v_cvt_pk_bf16_f32 v96, v96, v97\n"
      "v_mfma_f32_16x16x32_bf16 v[148:151], v[228:231], v[196:199], v[148:151]\n"
      "v_mul_f32 v98, v194, v98\n"
      "v_mul_f32 v99, v194, v99\n"
      "v_mfma_f32_16x16x32_bf16 v[144:147], v[252:255], v[196:199], v[144:147]\n"
      "v_add_f32 v110, v110, v111\n"
      "v_add_f32 v138, v138, v139\n"
      "v_mul_f32 v197, v120, v120\n"
      "v_add_f32 v196, v120, v121\n"
      "v_fmac_f32 v197, v121, v121\n"
      "v_add_f32 v196, v196, v122\n"
      "v_fmac_f32 v197, v122, v122\n"
      "v_add_f32 v196, v196, v123\n"
      "v_fmac_f32 v197, v123, v123\n"
      "v_add_f32 v196, v196, v148\n"
      "v_fmac_f32 v197, v148, v148\n"
      "v_add_f32 v196, v196, v149\n"
      "v_fmac_f32 v197, v149, v149\n"
      "v_add_f32 v196, v196, v150\n"
      "v_fmac_f32 v197, v150, v150\n"
      "v_add_f32 v196, v196, v151\n"
      "v_fmac_f32 v197, v151, v151\n"
      "v_add_f32 v196, v196, v144\n"
      "v_fmac_f32 v197, v144, v144\n"
      "v_add_f32 v196, v196, v145\n"
      "v_fmac_f32 v197, v145, v145\n"
      "v_add_f32 v196, v196, v146\n"
      "v_fmac_f32 v197, v146, v146\n"
      "v_add_f32 v196, v196, v147\n"
      "v_fmac_f32_e64 v197, v147, v147\n"
      "v_cvt_pk_bf16_f32 v131, v132, v133\n"
      "v_mul_f32 v190, v188, v190\n"
      "v_permlane16_swap_b32 v196, v197\n"
      "v_add_f32 v196, v196, v197\n"
      "v_mov_b32 v197, v196\n"
      "v_mul_f32 v191, v188, v191\n"
      "v_mul_f32 v192, v188, v192\n"
      "v_mul_f32 v193, v188, v193\n"
      "v_cvt_pk_bf16_f32 v97, v98, v99\n"
      "v_mul_f32 v116, v194, v116\n"
      "v_mul_f32 v117, v194, v117\n"
      "v_mul_f32 v118, v194, v118\n"
      "v_mul_f32 v119, v194, v119\n"
      "v_subrev_f32 v202, s72, v202\n"
      "v_cvt_pk_bf16_f32 v132, v190, v191\n"
      "v_cvt_pk_bf16_f32 v133, v192, v193\n"
      "v_cvt_pk_bf16_f32 v98, v116, v117\n"
      "v_cvt_pk_bf16_f32 v99, v118, v119\n"
      "v_mul_f32 v184, v188, v184\n"
      "v_mul_f32 v185, v188, v185\n"
      "v_mul_f32 v134, v194, v134\n"
      "v_mul_f32 v135, v194, v135\n"
      "v_exp_f32 v202, v202\n"
      "ds_write_b128 v3, v[130:133] offset:256\n"
      "v_mul_f32 v212, v174, v212\n"
      "v_mul_f32 v213, v174, v213\n"
      "v_cvt_pk_bf16_f32 v184, v184, v185\n"
      "v_mul_f32 v186, v188, v186\n"
      "v_mul_f32 v187, v188, v187\n"
      "v_cvt_pk_bf16_f32 v134, v134, v135\n"
      "v_mul_f32 v136, v194, v136\n"
      "v_mul_f32 v137, v194, v137\n"
      "ds_write_b128 v3, v[96:99] offset:512\n"
      "v_permlane32_swap_b32 v110, v138\n"
      "v_permlane32_swap_b32_e64 v196, v197\n"
      "v_cvt_pk_bf16_f32 v211, v212, v213\n"
      "v_cvt_pk_bf16_f32 v185, v186, v187\n"
      "v_cvt_pk_bf16_f32 v135, v136, v137\n"
      "v_cvt_pk_bf16_f32 v202, v202, v202\n"
      "ds_write_b64 v4, v[210:211]\n"
      "v_add_f32_e64 v110, v110, v138\n"
      "v_add_f32_e64 v196, v196, v197\n"
      "v_readlane_b32 s46, v37, s42\n"
      "v_or_b32_e64 v112, v112, v115\n"
      "v_cmp_ne_u32 s[56:57], 0, v112\n"
      "ds_write_b64 v4, v[184:185] offset:256\n"
      "s_or_b32 s95, s45, s46\n"
      "s_cmp_ge_u32 s95, 0x40000000\n"
      "s_cselect_b64 s[56:57], -1, s[56:57]\n"
      "s_cmp_gt_u32 s42, s44\n"
      "s_cselect_b64 s[56:57], -1, s[56:57]\n"
      "ds_write_b64 v4, v[134:135] offset:512\n"
      "s_or_b64 vcc, s[56:57], s[54:55]\n"
      "s_add_u32 s42, s42, 1\n"
      "v_readlane_b32 s47, v38, s43\n"
      "s_add_u32 s43, s43, 1\n"
      "s_and_b32 s47, s47, -4\n"
      "v_add_u32 v12, 32, v12\n"
      "ds_write_b16 v13, v202\n"
      "ds_read_b128 v[210:213], v14\n"
      "ds_read_b128 v[174:177], v14 offset:64\n"
      "ds_write_b32 v8, v110\n"
      "ds_write_b32 v8, v196 offset:512\n"
      "s_waitcnt lgkmcnt(3)\n"
      "v_mfma_f32_16x16x32_bf16 v[188:191], v[210:213], v[32:35], 0\n"
      "s_waitcnt lgkmcnt(2)\n"
      "v_mfma_f32_16x16x32_bf16 v[188:191], v[174:177], v[32:35], v[188:191]\n"
      "s_waitcnt lgkmcnt(0)\n"
      "s_barrier\n"
      "s_nop 7\n"
      "s_nop 7\n"
      "s_cbranch_vccz L_loop1_%=\n"
      "s_branch L_disp1_%=\n"
      "L_tail1_%=:\n"
      "s_nop 7\n"
      "s_nop 7\n"
      "v_mfma_f32_16x16x32_bf16 v[202:205], v[210:213], v[104:107], 0\n"
      "ds_read_b128 v[104:107], v9\n"
      "ds_read_b128 v[96:99], v9 offset:1024\n"
      "v_mfma_f32_16x16x32_bf16 v[202:205], v[174:177], v[76:79], v[202:205]\n"
      "ds_read_b128 v[136:139], v2\n"
      "v_mov_b32_dpp v188, v188 quad_perm:[0,0,0,0] row_mask:0xf bank_mask:0xf\n"
      "v_mfma_f32_16x16x32_bf16 v[130:133], v[210:213], v[100:103], 0\n"
      "ds_read_b128 v[100:103], v2 offset:3072\n"
      "v_rcp_f32 v188, v188\n"
      "v_mfma_f32_16x16x32_bf16 v[130:133], v[174:177], v[80:83], v[130:133]\n"
      "s_bitcmp1_b32 s46, 31\n"
      "s_cselect_b32 s93, 100270464, 0\n"
      "v_bfe_i32 v81, v36, 10, 1\n"
      "v_mfma_f32_16x16x32_bf16 v[194:197], v[210:213], v[214:217], 0\n"
      "ds_read_b128 v[214:217], v2 offset:6144\n"
      "v_bfe_i32 v203, v36, 11, 1\n"
      "v_mfma_f32_16x16x32_bf16 v[194:197], v[174:177], v[84:87], v[194:197]\n"
      "ds_read_b128 v[174:177], v2 offset:9216\n"
      "ds_read_b128 v[210:213], v2 offset:12288\n"
      "s_waitcnt lgkmcnt(6)\n"
      "v_add_f32 v104, v104, v106\n"
      "s_waitcnt lgkmcnt(5)\n"
      "v_add_f32 v96, v96, v98\n"
      "v_add_f32 v105, v105, v107\n"
      "v_add_f32 v97, v97, v99\n"
      "v_add_f32 v104, v104, v96\n"
      "v_add_f32 v105, v105, v97\n"
      "v_fma_f32 v96, -v104, v104, s52\n"
      "v_fmac_f32 v96, 0x43400000, v105\n"
      "v_max_f32 v96, s52, v96\n"
      "v_rsq_f32 v96, v96\n"
      "v_cndmask_b32 v202, v202, v130, s[58:59]\n"
      "v_cndmask_b32 v202, v202, v194, s[60:61]\n"
      "v_mul_f32 v202, v188, v202\n"
      "v_mul_f32 v107, 0x43400000, v96\n"
      "v_mul_f32 v105, -v104, v96\n"
      "v_cvt_pk_bf16_f32 v202, v202, v202\n"
      "v_mov_b32 v104, v107\n"
      "v_mov_b32 v106, v105\n"
      "ds_write_b16 v12, v202\n"
      "v_permlane32_swap_b32 v107, v104\n"
      "v_permlane32_swap_b32 v105, v106\n"
      "v_mov_b32 v202, v107\n"
      "v_mov_b32_e64 v204, v105\n"
      "v_fma_f32 v120, v120, v104, v106\n"
      "v_permlane16_swap_b32 v107, v202\n"
      "v_permlane16_swap_b32 v105, v204\n"
      "v_fma_f32 v220, v220, v107, v105\n"
      "v_fma_f32 v221, v221, v107, v105\n"
      "v_fma_f32 v166, v166, v202, v204\n"
      "v_fma_f32 v167, v167, v202, v204\n"
      "v_fma_f32 v121, v121, v104, v106\n"
      "v_cvt_pk_bf16_f32 v220, v220, v221\n"
      "v_fma_f32 v222, v222, v107, v105\n"
      "v_fma_f32 v223, v223, v107, v105\n"
      "v_cvt_pk_bf16_f32 v166, v166, v167\n"
      "v_fma_f32 v168, v168, v202, v204\n"
      "v_fma_f32 v169, v169, v202, v204\n"
      "v_cvt_pk_bf16_f32 v120, v120, v121\n"
      "v_fma_f32 v122, v122, v104, v106\n"
      "v_fma_f32 v123, v123, v104, v106\n"
      "v_fma_f32 v162, v162, v107, v105\n"
      "v_fma_f32 v163, v163, v107, v105\n"
      "v_cvt_pk_bf16_f32 v221, v222, v223\n"
      "v_fma_f32 v206, v206, v107, v105\n"
      "v_fma_f32 v207, v207, v107, v105\n"
      "v_fma_f32 v208, v208, v107, v105\n"
      "v_fma_f32 v209, v209, v107, v105\n"
      "v_cvt_pk_bf16_f32 v167, v168, v169\n"
      "v_fma_f32 v180, v180, v202, v204\n"
      "v_fma_f32 v181, v181, v202, v204\n"
      "v_fma_f32 v182, v182, v202, v204\n"
      "v_fma_f32 v183, v183, v202, v204\n"
      "v_cvt_pk_bf16_f32 v121, v122, v123\n"
      "v_fma_f32 v148, v148, v104, v106\n"
      "v_fma_f32 v149, v149, v104, v106\n"
      "v_fma_f32 v150, v150, v104, v106\n"
      "v_fma_f32 v151, v151, v104, v106\n"
      "v_cvt_pk_bf16_f32 v162, v162, v163\n"
      "v_fma_f32 v164, v164, v107, v105\n"
      "v_fma_f32 v165, v165, v107, v105\n"
      "v_cvt_pk_bf16_f32 v222, v206, v207\n"
      "v_cvt_pk_bf16_f32 v223, v208, v209\n"
      "v_cvt_pk_bf16_f32 v168, v180, v181\n"
      "v_cvt_pk_bf16_f32 v169, v182, v183\n"
      "v_cvt_pk_bf16_f32 v122, v148, v149\n"
      "v_cvt_pk_bf16_f32 v123, v150, v151\n"
      "v_cvt_pk_bf16_f32 v163, v164, v165\n"
      "v_fma_f32 v124, v124, v202, v204\n"
      "v_fma_f32 v125, v125, v202, v204\n"
      "v_fma_f32 v144, v144, v104, v106\n"
      "v_fma_f32 v145, v145, v104, v106\n"
      "ds_write_b128 v5, v[220:223] offset:18432\n"
      "ds_write_b128 v5, v[166:169] offset:24576\n"
      "ds_write_b128 v5, v[120:123] offset:30720\n"
      "ds_write_b64 v6, v[162:163] offset:18432\n"
      "v_cvt_pk_bf16_f32 v124, v124, v125\n"
      "v_fma_f32 v126, v126, v202, v204\n"
      "v_fma_f32 v127, v127, v202, v204\n"
      "v_cvt_pk_bf16_f32 v144, v144, v145\n"
      "v_fma_f32 v146, v146, v104, v106\n"
      "v_fma_f32 v147, v147, v104, v106\n"
      "v_cvt_pk_bf16_f32 v125, v126, v127\n"
      "v_cvt_pk_bf16_f32 v145, v146, v147\n"
      "ds_write_b64 v6, v[124:125] offset:24576\n"
      "ds_write_b64 v6, v[144:145] offset:30720\n"
      "s_waitcnt lgkmcnt(4)\n"
      "v_mfma_f32_16x16x32_bf16 v[220:223], a[216:219], v[136:139], 0\n"
      "s_bitcmp1_b32 s46, 30\n"
      "s_cselect_b32 s94, 196224, 0\n"
      "v_and_b32 v81, s93, v81\n"
      "v_mfma_f32_16x16x32_bf16 v[206:209], a[240:243], v[136:139], 0\n"
      "s_and_b32 s92, s46, 0x3fffffff\n"
      "v_and_b32 v203, s94, v203\n"
      "v_add_u32_e64 v81, v81, v203\n"
      "v_mfma_f32_16x16x32_bf16 v[162:165], v[232:235], v[136:139], 0\n"
      "ds_read_b128 v[166:169], v2 offset:15360\n"
      "v_sub_u32 v81, s92, v81\n"
      "v_mfma_f32_16x16x32_bf16 v[220:223], a[220:223], v[100:103], v[220:223]\n"
      "v_add_u32 v81, v20, v81\n"
      "v_bfe_i32 v80, v36, 12, 1\n"
      "v_mfma_f32_16x16x32_bf16 v[206:209], a[244:247], v[100:103], v[206:209]\n"
      "v_bfe_i32 v145, v36, 13, 1\n"
      "v_and_b32_e64 v80, s93, v80\n"
      "v_mfma_f32_16x16x32_bf16 v[162:165], v[236:239], v[100:103], v[162:165]\n"
      "ds_read_b128 v[120:123], v2 offset:256\n"
      "v_and_b32 v145, s94, v145\n"
      "v_mfma_f32_16x16x32_bf16 v[220:223], a[224:227], v[214:217], v[220:223]\n"
      "v_add_u32 v80, v80, v145\n"
      "v_sub_u32 v80, s92, v80\n"
      "v_mfma_f32_16x16x32_bf16 v[206:209], a[248:251], v[214:217], v[206:209]\n"
      "v_add_u32 v80, v21, v80\n"
      "v_bfe_i32 v97, v36, 14, 1\n"
      "v_mfma_f32_16x16x32_bf16 v[162:165], v[240:243], v[214:217], v[162:165]\n"
      "ds_read_b128 v[144:147], v2 offset:3328\n"
      "v_bfe_i32 v151, v36, 15, 1\n"
      "v_mfma_f32_16x16x32_bf16 v[220:223], a[228:231], v[174:177], v[220:223]\n"
      "s_waitcnt lgkmcnt(2)\n"
      "v_and_b32 v97, s93, v97\n"
      "v_and_b32 v151, s94, v151\n"
      "v_mfma_f32_16x16x32_bf16 v[206:209], a[252:255], v[174:177], v[206:209]\n"
      "v_add_u32 v97, v97, v151\n"
      "v_sub_u32_e64 v97, s92, v97\n"
      "v_mfma_f32_16x16x32_bf16 v[162:165], v[244:247], v[174:177], v[162:165]\n"
      "ds_read_b128 v[148:151], v2 offset:6400\n"
      "v_add_u32 v97, v22, v97\n"
      "v_mfma_f32_16x16x32_bf16 v[220:223], a[232:235], v[210:213], v[220:223]\n"
      "s_waitcnt lgkmcnt(2)\n"
      "v_bfe_i32 v96, v36, 16, 1\n"
      "v_bfe_i32 v214, v36, 17, 1\n"
      "v_mfma_f32_16x16x32_bf16 v[206:209], v[224:227], v[210:213], v[206:209]\n"
      "v_and_b32 v96, s93, v96\n"
      "v_and_b32 v214, s94, v214\n"
      "v_mfma_f32_16x16x32_bf16 v[162:165], v[248:251], v[210:213], v[162:165]\n"
      "ds_read_b128 v[174:177], v2 offset:9472\n"
      "v_add_u32 v96, v96, v214\n"
      "v_mfma_f32_16x16x32_bf16 v[220:223], a[236:239], v[166:169], v[220:223]\n"
      "s_waitcnt lgkmcnt(2)\n"
      "v_sub_u32 v96, s92, v96\n"
      "v_add_u32 v96, v23, v96\n"
      "v_mfma_f32_16x16x32_bf16 v[206:209], v[228:231], v[166:169], v[206:209]\n"
      "v_bfe_i32 v84, v36, 18, 1\n"
      "v_bfe_i32 v137, v36, 19, 1\n"
      "v_mfma_f32_16x16x32_bf16 v[162:165], v[252:255], v[166:169], v[162:165]\n"
      "ds_read_b128 v[212:215], v2 offset:12544\n"
      "v_mul_f32 v211, v220, v220\n"
      "v_mfma_f32_16x16x32_bf16 v[166:169], a[216:219], v[120:123], 0\n"
      "s_waitcnt lgkmcnt(2)\n"
      "v_add_f32 v210, v220, v221\n"
      "v_fmac_f32 v211, v221, v221\n"
      "v_mfma_f32_16x16x32_bf16 v[180:183], a[240:243], v[120:123], 0\n"
      "v_add_f32 v210, v210, v222\n"
      "v_fmac_f32 v211, v222, v222\n"
      "v_mfma_f32_16x16x32_bf16 v[124:127], v[232:235], v[120:123], 0\n"
      "ds_read_b128 v[120:123], v2 offset:15616\n"
      "v_add_f32 v210, v210, v223\n"
      "v_mfma_f32_16x16x32_bf16 v[166:169], a[220:223], v[144:147], v[166:169]\n"
      "s_waitcnt lgkmcnt(2)\n"
      "v_fmac_f32 v211, v223, v223\n"
      "v_add_f32 v210, v210, v206\n"
      "v_mfma_f32_16x16x32_bf16 v[180:183], a[244:247], v[144:147], v[180:183]\n"
      "v_fmac_f32 v211, v206, v206\n"
      "v_add_f32 v210, v210, v207\n"
      "v_mfma_f32_16x16x32_bf16 v[124:127], v[236:239], v[144:147], v[124:127]\n"
      "ds_read_b128 v[100:103], v2 offset:512\n"
      "v_fmac_f32 v211, v207, v207\n"
      "v_mfma_f32_16x16x32_bf16 v[166:169], a[224:227], v[148:151], v[166:169]\n"
      "s_waitcnt lgkmcnt(2)\n"
      "v_add_f32 v210, v210, v208\n"
      "v_fmac_f32 v211, v208, v208\n"
      "v_mfma_f32_16x16x32_bf16 v[180:183], a[248:251], v[148:151], v[180:183]\n"
      "v_add_f32 v210, v210, v209\n"
      "v_fmac_f32 v211, v209, v209\n"
      "v_mfma_f32_16x16x32_bf16 v[124:127], v[240:243], v[148:151], v[124:127]\n"
      "ds_read_b128 v[104:107], v2 offset:3584\n"
      "v_add_f32 v210, v210, v162\n"
      "v_mfma_f32_16x16x32_bf16 v[166:169], a[228:231], v[174:177], v[166:169]\n"
      "s_waitcnt lgkmcnt(2)\n"
      "v_fmac_f32 v211, v162, v162\n"
      "v_add_f32 v210, v210, v163\n"
      "v_mfma_f32_16x16x32_bf16 v[180:183], a[252:255], v[174:177], v[180:183]\n"
      "v_fmac_f32 v211, v163, v163\n"
      "v_add_f32 v210, v210, v164\n"
      "v_mfma_f32_16x16x32_bf16 v[124:127], v[244:247], v[174:177], v[124:127]\n"
      "ds_read_b128 v[174:177], v2 offset:6656\n"
      "v_fmac_f32 v211, v164, v164\n"
      "v_mfma_f32_16x16x32_bf16 v[166:169], a[232:235], v[212:215], v[166:169]\n"
      "s_waitcnt lgkmcnt(2)\n"
      "v_add_f32 v210, v210, v165\n"
      "v_fmac_f32 v211, v165, v165\n"
      "v_mfma_f32_16x16x32_bf16 v[180:183], v[224:227], v[212:215], v[180:183]\n"
      "v_and_b32 v84, s93, v84\n"
      "v_permlane16_swap_b32 v210, v211\n"
      "v_mfma_f32_16x16x32_bf16 v[124:127], v[248:251], v[212:215], v[124:127]\n"
      "ds_read_b128 v[212:215], v2 offset:9728\n"
      "v_add_f32 v210, v210, v211\n"
      "v_mfma_f32_16x16x32_bf16 v[166:169], a[236:239], v[120:123], v[166:169]\n"
      "ds_read_b128 v[202:205], v7\n"
      "v_and_b32 v137, s94, v137\n"
      "v_mfma_f32_16x16x32_bf16 v[180:183], v[228:231], v[120:123], v[180:183]\n"
      "s_waitcnt lgkmcnt(3)\n"
      "v_add_u32 v84, v84, v137\n"
      "v_sub_u32 v84, s92, v84\n"
      "v_mfma_f32_16x16x32_bf16 v[124:127], v[252:255], v[120:123], v[124:127]\n"
      "ds_read_b128 v[130:133], v7 offset:4096\n"
      "v_mul_f32 v137, v166, v166\n"
      "v_mfma_f32_16x16x32_bf16 v[120:123], a[216:219], v[100:103], 0\n"
      "ds_read_b128 v[194:197], v2 offset:12800\n"
      "v_add_f32_e64 v136, v166, v167\n"
      "v_mfma_f32_16x16x32_bf16 v[148:151], a[240:243], v[100:103], 0\n"
      "ds_read_b128 v[216:219], v7 offset:8192\n"
      "v_fmac_f32 v137, v167, v167\n"
      "v_mfma_f32_16x16x32_bf16 v[144:147], v[232:235], v[100:103], 0\n"
      "ds_read_b128 v[100:103], v7 offset:12288\n"
      "v_add_f32 v136, v136, v168\n"
      "v_mfma_f32_16x16x32_bf16 v[120:123], a[220:223], v[104:107], v[120:123]\n"
      "s_waitcnt lgkmcnt(4)\n"
      "v_fmac_f32 v137, v168, v168\n"
      "v_add_f32_e64 v136, v136, v169\n"
      "v_mfma_f32_16x16x32_bf16 v[148:151], a[244:247], v[104:107], v[148:151]\n"
      "ds_read_b128 v[188:191], v2 offset:15872\n"
      "v_fmac_f32_e64 v137, v169, v169\n"
      "v_mfma_f32_16x16x32_bf16 v[144:147], v[236:239], v[104:107], v[144:147]\n"
      "buffer_store_dwordx4 v[202:205], v81, s[20:23], 0 offen sc1\n"
      "v_mfma_f32_16x16x32_bf16 v[120:123], a[224:227], v[174:177], v[120:123]\n"
      "ds_read_b128 v[104:107], v7 offset:16384\n"
      "v_add_f32 v136, v136, v180\n"
      "v_mfma_f32_16x16x32_bf16 v[148:151], a[248:251], v[174:177], v[148:151]\n"
      "v_fmac_f32 v137, v180, v180\n"
      "v_add_f32 v136, v136, v181\n"
      "v_mfma_f32_16x16x32_bf16 v[144:147], v[240:243], v[174:177], v[144:147]\n"
      "v_fmac_f32 v137, v181, v181\n"
      "v_add_f32 v136, v136, v182\n"
      "v_mfma_f32_16x16x32_bf16 v[120:123], a[228:231], v[212:215], v[120:123]\n"
      "s_waitcnt lgkmcnt(1)\n"
      "v_fmac_f32 v137, v182, v182\n"
      "v_add_f32 v136, v136, v183\n"
      "v_mfma_f32_16x16x32_bf16 v[148:151], a[252:255], v[212:215], v[148:151]\n"
      "buffer_store_dwordx4 v[130:133], v80, s[20:23], 0 offen sc1\n"
      "v_mfma_f32_16x16x32_bf16 v[144:147], v[244:247], v[212:215], v[144:147]\n"
      "buffer_store_dwordx4 v[216:219], v97, s[20:23], 0 offen sc1\n"
      "v_mfma_f32_16x16x32_bf16 v[120:123], a[232:235], v[194:197], v[120:123]\n"
      "buffer_store_dwordx4 v[100:103], v96, s[20:23], 0 offen sc1\n"
      "v_mfma_f32_16x16x32_bf16 v[148:151], v[224:227], v[194:197], v[148:151]\n"
      "s_waitcnt lgkmcnt(0)\n"
      "v_fmac_f32 v137, v183, v183\n"
      "v_add_f32 v136, v136, v124\n"
      "v_mfma_f32_16x16x32_bf16 v[144:147], v[248:251], v[194:197], v[144:147]\n"
      "v_fmac_f32 v137, v124, v124\n"
      "v_add_f32_e64 v136, v136, v125\n"
      "v_mfma_f32_16x16x32_bf16 v[120:123], a[236:239], v[188:191], v[120:123]\n"
      "v_fmac_f32 v137, v125, v125\n"
      "v_add_f32 v136, v136, v126\n"
      "v_mfma_f32_16x16x32_bf16 v[148:151], v[228:231], v[188:191], v[148:151]\n"
      "v_fmac_f32 v137, v126, v126\n"
      "v_add_f32 v136, v136, v127\n"
      "v_mfma_f32_16x16x32_bf16 v[144:147], v[252:255], v[188:191], v[144:147]\n"
      "v_fmac_f32 v137, v127, v127\n"
      "v_add_u32 v84, v25, v84\n"
      "v_mul_f32 v103, v120, v120\n"
      "v_add_f32 v102, v120, v121\n"
      "v_fmac_f32 v103, v121, v121\n"
      "v_add_f32 v102, v102, v122\n"
      "v_fmac_f32 v103, v122, v122\n"
      "v_add_f32 v102, v102, v123\n"
      "v_fmac_f32 v103, v123, v123\n"
      "v_add_f32 v102, v102, v148\n"
      "v_fmac_f32 v103, v148, v148\n"
      "v_add_f32 v102, v102, v149\n"
      "v_fmac_f32 v103, v149, v149\n"
      "v_add_f32 v102, v102, v150\n"
      "v_fmac_f32 v103, v150, v150\n"
      "v_add_f32 v102, v102, v151\n"
      "v_fmac_f32 v103, v151, v151\n"
      "v_add_f32 v102, v102, v144\n"
      "v_fmac_f32 v103, v144, v144\n"
      "v_add_f32 v102, v102, v145\n"
      "v_fmac_f32 v103, v145, v145\n"
      "v_add_f32 v102, v102, v146\n"
      "v_fmac_f32 v103, v146, v146\n"
      "v_add_f32 v102, v102, v147\n"
      "v_fmac_f32 v103, v147, v147\n"
      "v_permlane16_swap_b32 v136, v137\n"
      "v_add_f32 v136, v136, v137\n"
      "v_permlane16_swap_b32 v102, v103\n"
      "v_add_f32 v102, v102, v103\n"
      "v_mov_b32 v103, v102\n"
      "v_permlane32_swap_b32 v210, v136\n"
      "v_add_f32 v210, v210, v136\n"
      "v_permlane32_swap_b32 v102, v103\n"
      "v_add_f32 v102, v102, v103\n"
      "ds_write_b32 v8, v210 offset:2048\n"
      "ds_write_b32 v8, v102 offset:2560\n"
      "buffer_store_dwordx4 v[104:107], v84, s[20:23], 0 offen sc1\n"
      "v_readlane_b32 s46, v37, s42\n"
      "s_add_u32 s42, s42, 1\n"
      "s_waitcnt lgkmcnt(0)\n"
      "s_barrier\n"
      "ds_read_b128 v[102:105], v9 offset:2048\n"
      "ds_read_b128 v[214:217], v9 offset:3072\n"
      "v_subrev_u32 v84, 115456, v9\n"
      "v_and_b32 v210, 0x300, v84\n"
      "v_add_u32 v84, v84, v210\n"
      "v_add_u32 v84, 149504, v84\n"
      "ds_read_b128 v[130:133], v84\n"
      "ds_read_b128 v[174:177], v84 offset:2048\n"
      "ds_read_b128 v[210:213], v84 offset:4096\n"
      "v_subrev_u32 v136, 73728, v5\n"
      "v_subrev_u32 v137, 73728, v6\n"
      "s_bitcmp1_b32 s46, 31\n"
      "s_cselect_b32 s93, 100270464, 0\n"
      "s_bitcmp1_b32 s46, 30\n"
      "s_cselect_b32 s94, 196224, 0\n"
      "s_and_b32 s92, s46, 0x3fffffff\n"
      "v_readlane_b32 s46, v37, s42\n"
      "s_mul_i32 s74, s40, 3072\n"
      "s_waitcnt lgkmcnt(4)\n"
      "v_add_f32 v102, v102, v104\n"
      "v_add_f32 v103, v103, v105\n"
      "v_bfe_i32 v80, v36, 10, 1\n"
      "s_waitcnt lgkmcnt(3)\n"
      "v_add_f32 v214, v214, v216\n"
      "v_add_f32 v215, v215, v217\n"
      "v_add_f32 v102, v102, v214\n"
      "v_add_f32 v103, v103, v215\n"
      "s_waitcnt lgkmcnt(2)\n"
      "v_mfma_f32_16x16x32_bf16 v[104:107], a[216:219], v[130:133], 0\n"
      "s_add_u32 s42, s42, 1\n"
      "v_fma_f32 v214, -v102, v102, s52\n"
      "v_fmac_f32 v214, 0x43400000, v103\n"
      "s_waitcnt lgkmcnt(1)\n"
      "v_mfma_f32_16x16x32_bf16 v[104:107], a[220:223], v[174:177], v[104:107]\n"
      "v_max_f32 v214, s52, v214\n"
      "v_rsq_f32_e64 v214, v214\n"
      "v_bfe_i32 v215, v36, 11, 1\n"
      "v_mul_f32 v81, -v102, v214\n"
      "v_mul_f32 v97, 0x43400000, v214\n"
      "v_and_b32 v80, s93, v80\n"
      "v_and_b32 v215, s94, v215\n"
      "v_add_u32 v80, v80, v215\n"
      "v_mov_b32 v96, v97\n"
      "v_sub_u32 v80, s92, v80\n"
      "v_add_u32 v80, v20, v80\n"
      "v_permlane32_swap_b32 v97, v96\n"
      "v_mfma_f32_16x16x32_bf16 v[214:217], a[240:243], v[130:133], 0\n"
      "s_waitcnt lgkmcnt(0)\n"
      "v_mfma_f32_16x16x32_bf16 v[214:217], a[244:247], v[174:177], v[214:217]\n"
      "v_mfma_f32_16x16x32_bf16 v[100:103], v[232:235], v[130:133], 0\n"
      "v_mfma_f32_16x16x32_bf16 v[100:103], v[236:239], v[174:177], v[100:103]\n"
      "v_mfma_f32_16x16x32_bf16 v[104:107], a[224:227], v[210:213], v[104:107]\n"
      "ds_read_b128 v[174:177], v84 offset:6144\n"
      "ds_read_b128 v[130:133], v84 offset:8192\n"
      "ds_read_b128 v[202:205], v84 offset:10240\n"
      "ds_read_b128 v[194:197], v84 offset:256\n"
      "ds_read_b128 v[188:191], v84 offset:2304\n"
      "v_mov_b32 v78, v81\n"
      "v_mov_b32 v76, v97\n"
      "v_subrev_u32 v82, 115456, v9\n"
      "v_permlane32_swap_b32_e64 v81, v78\n"
      "v_permlane16_swap_b32 v97, v76\n"
      "v_fma_f32 v120, v120, v96, v78\n"
      "v_fma_f32 v121, v121, v96, v78\n"
      "v_cvt_pk_bf16_f32 v120, v120, v121\n"
      "v_fma_f32 v122, v122, v96, v78\n"
      "v_fma_f32 v123, v123, v96, v78\n"
      "v_fma_f32 v144, v144, v96, v78\n"
      "v_fma_f32 v145, v145, v96, v78\n"
      "s_waitcnt lgkmcnt(4)\n"
      "v_mfma_f32_16x16x32_bf16 v[104:107], a[228:231], v[174:177], v[104:107]\n"
      "v_cvt_pk_bf16_f32 v121, v122, v123\n"
      "v_fma_f32 v148, v148, v96, v78\n"
      "v_mfma_f32_16x16x32_bf16 v[214:217], a[248:251], v[210:213], v[214:217]\n"
      "s_waitcnt lgkmcnt(3)\n"
      "v_fma_f32 v149, v149, v96, v78\n"
      "v_fma_f32 v150, v150, v96, v78\n"
      "v_mfma_f32_16x16x32_bf16 v[214:217], a[252:255], v[174:177], v[214:217]\n"
      "s_waitcnt lgkmcnt(2)\n"
      "v_fma_f32 v151, v151, v96, v78\n"
      "v_cvt_pk_bf16_f32 v144, v144, v145\n"
      "v_mfma_f32_16x16x32_bf16 v[100:103], v[240:243], v[210:213], v[100:103]\n"
      "s_waitcnt lgkmcnt(1)\n"
      "v_fma_f32 v146, v146, v96, v78\n"
      "v_fma_f32 v147, v147, v96, v78\n"
      "v_mfma_f32_16x16x32_bf16 v[100:103], v[244:247], v[174:177], v[100:103]\n"
      "s_waitcnt lgkmcnt(0)\n"
      "v_cvt_pk_bf16_f32 v122, v148, v149\n"
      "v_cvt_pk_bf16_f32 v123, v150, v151\n"
      "v_mfma_f32_16x16x32_bf16 v[104:107], a[232:235], v[130:133], v[104:107]\n"
      "ds_write_b128 v5, v[120:123] offset:12288\n"
      "v_mfma_f32_16x16x32_bf16 v[104:107], a[236:239], v[202:205], v[104:107]\n"
      "v_cvt_pk_bf16_f32 v145, v146, v147\n"
      "ds_write_b64 v6, v[144:145] offset:12288\n"
      "v_mfma_f32_16x16x32_bf16 v[214:217], v[224:227], v[130:133], v[214:217]\n"
      "v_mfma_f32_16x16x32_bf16 v[214:217], v[228:231], v[202:205], v[214:217]\n"
      "v_mfma_f32_16x16x32_bf16 v[100:103], v[248:251], v[130:133], v[100:103]\n"
      "v_mov_b32 v176, v81\n"
      "v_add_u32 v120, s74, v82\n"
      "v_mfma_f32_16x16x32_bf16 v[100:103], v[252:255], v[202:205], v[100:103]\n"
      "ds_read_b128 v[144:147], v84 offset:4352\n"
      "v_permlane16_swap_b32_e64 v81, v176\n"
      "v_fma_f32 v220, v220, v97, v81\n"
      "v_fma_f32 v221, v221, v97, v81\n"
      "v_cvt_pk_bf16_f32 v220, v220, v221\n"
      "v_fma_f32 v222, v222, v97, v81\n"
      "v_fma_f32 v223, v223, v97, v81\n"
      "v_fma_f32 v162, v162, v97, v81\n"
      "v_fma_f32 v163, v163, v97, v81\n"
      "v_cvt_pk_bf16_f32 v221, v222, v223\n"
      "v_fma_f32 v206, v206, v97, v81\n"
      "v_fma_f32 v207, v207, v97, v81\n"
      "v_fma_f32 v208, v208, v97, v81\n"
      "v_fma_f32 v209, v209, v97, v81\n"
      "v_cvt_pk_bf16_f32 v162, v162, v163\n"
      "v_fma_f32 v164, v164, v97, v81\n"
      "v_fma_f32 v165, v165, v97, v81\n"
      "v_cvt_pk_bf16_f32 v222, v206, v207\n"
      "v_mfma_f32_16x16x32_bf16 v[204:207], a[216:219], v[194:197], 0\n"
      "v_cvt_pk_bf16_f32 v223, v208, v209\n"
      "v_cvt_pk_bf16_f32 v163, v164, v165\n"
      "v_mfma_f32_16x16x32_bf16 v[204:207], a[220:223], v[188:191], v[204:207]\n"
      "ds_write_b128 v5, v[220:223]\n"
      "v_mfma_f32_16x16x32_bf16 v[220:223], a[240:243], v[194:197], 0\n"
      "ds_write_b64 v6, v[162:163]\n"
      "v_mfma_f32_16x16x32_bf16 v[220:223], a[244:247], v[188:191], v[220:223]\n"
      "s_waitcnt lgkmcnt(2)\n"
      "v_fma_f32 v166, v166, v76, v176\n"
      "v_fma_f32 v167, v167, v76, v176\n"
      "v_mfma_f32_16x16x32_bf16 v[162:165], v[232:235], v[194:197], 0\n"
      "ds_read_b128 v[208:211], v84 offset:6400\n"
      "v_cvt_pk_bf16_f32 v166, v166, v167\n"
      "v_mfma_f32_16x16x32_bf16 v[162:165], v[236:239], v[188:191], v[162:165]\n"
      "ds_read_b128 v[148:151], v7 offset:18432\n"
      "v_fma_f32 v168, v168, v76, v176\n"
      "v_mfma_f32_16x16x32_bf16 v[204:207], a[224:227], v[144:147], v[204:207]\n"
      "v_fma_f32 v169, v169, v76, v176\n"
      "v_fma_f32 v124, v124, v76, v176\n"
      "v_fma_f32 v125, v125, v76, v176\n"
      "v_cvt_pk_bf16_f32 v167, v168, v169\n"
      "v_fma_f32 v180, v180, v76, v176\n"
      "v_fma_f32 v181, v181, v76, v176\n"
      "v_fma_f32 v182, v182, v76, v176\n"
      "v_fma_f32 v183, v183, v76, v176\n"
      "v_cvt_pk_bf16_f32 v124, v124, v125\n"
      "v_fma_f32 v126, v126, v76, v176\n"
      "v_fma_f32 v127, v127, v76, v176\n"
      "ds_read_b128 v[174:177], v7 offset:22528\n"
      "v_cvt_pk_bf16_f32 v168, v180, v181\n"
      "s_waitcnt lgkmcnt(2)\n"
      "v_mfma_f32_16x16x32_bf16 v[204:207], a[228:231], v[208:211], v[204:207]\n"
      "v_cvt_pk_bf16_f32 v169, v182, v183\n"
      "v_cvt_pk_bf16_f32 v125, v126, v127\n"
      "v_mfma_f32_16x16x32_bf16 v[220:223], a[248:251], v[144:147], v[220:223]\n"
      "s_waitcnt lgkmcnt(1)\n"
      "ds_write_b128 v5, v[166:169] offset:6144\n"
      "v_mfma_f32_16x16x32_bf16 v[220:223], a[252:255], v[208:211], v[220:223]\n"
      "ds_write_b64 v6, v[124:125] offset:6144\n"
      "v_mfma_f32_16x16x32_bf16 v[162:165], v[240:243], v[144:147], v[162:165]\n"
      "s_waitcnt lgkmcnt(2)\n"
      "v_mul_f32 v203, v104, v104\n"
      "v_add_f32 v202, v104, v105\n"
      "v_mfma_f32_16x16x32_bf16 v[162:165], v[244:247], v[208:211], v[162:165]\n"
      "ds_read_b128 v[124:127], v84 offset:8448\n"
      "ds_read_b128 v[208:211], v84 offset:10496\n"
      "ds_read_b128 v[144:147], v7 offset:26624\n"
      "ds_read_b128 v[180:183], v7 offset:30720\n"
      "ds_read_b128 v[166:169], v7 offset:34816\n"
      "buffer_store_dwordx4 v[148:151], v80, s[20:23], 0 offen sc1\n"
      "v_fmac_f32 v203, v105, v105\n"
      "v_add_f32 v202, v202, v106\n"
      "v_fmac_f32 v203, v106, v106\n"
      "v_add_f32 v202, v202, v107\n"
      "v_fmac_f32 v203, v107, v107\n"
      "v_add_f32 v202, v202, v214\n"
      "v_fmac_f32 v203, v214, v214\n"
      "s_waitcnt lgkmcnt(4)\n"
      "v_mfma_f32_16x16x32_bf16 v[204:207], a[232:235], v[124:127], v[204:207]\n"
      "v_add_f32 v202, v202, v215\n"
      "v_fmac_f32_e64 v203, v215, v215\n"
      "s_waitcnt lgkmcnt(3)\n"
      "v_mfma_f32_16x16x32_bf16 v[204:207], a[236:239], v[208:211], v[204:207]\n"
      "v_add_f32 v202, v202, v216\n"
      "v_fmac_f32 v203, v216, v216\n"
      "v_mfma_f32_16x16x32_bf16 v[220:223], v[224:227], v[124:127], v[220:223]\n"
      "s_waitcnt lgkmcnt(2)\n"
      "v_add_f32 v202, v202, v217\n"
      "v_fmac_f32 v203, v217, v217\n"
      "v_mfma_f32_16x16x32_bf16 v[220:223], v[228:231], v[208:211], v[220:223]\n"
      "s_waitcnt lgkmcnt(1)\n"
      "v_mul_f32 v149, v204, v204\n"
      "v_add_f32 v148, v204, v205\n"
      "v_mfma_f32_16x16x32_bf16 v[162:165], v[248:251], v[124:127], v[162:165]\n"
      "s_waitcnt lgkmcnt(0)\n"
      "v_fmac_f32 v149, v205, v205\n"
      "v_add_f32 v148, v148, v206\n"
      "v_mfma_f32_16x16x32_bf16 v[162:165], v[252:255], v[208:211], v[162:165]\n"
      "v_fmac_f32 v149, v206, v206\n"
      "v_add_f32 v148, v148, v207\n"
      "v_fmac_f32 v149, v207, v207\n"
      "v_add_f32 v148, v148, v220\n"
      "v_fmac_f32 v149, v220, v220\n"
      "v_add_f32 v148, v148, v221\n"
      "v_fmac_f32 v149, v221, v221\n"
      "v_add_f32 v148, v148, v222\n"
      "v_fmac_f32 v149, v222, v222\n"
      "v_add_f32 v148, v148, v223\n"
      "v_fmac_f32 v149, v223, v223\n"
      "v_add_f32 v202, v202, v100\n"
      "v_fmac_f32 v203, v100, v100\n"
      "v_add_f32 v148, v148, v162\n"
      "v_fmac_f32 v149, v162, v162\n"
      "v_add_f32 v202, v202, v101\n"
      "v_fmac_f32 v203, v101, v101\n"
      "v_add_f32 v148, v148, v163\n"
      "v_fmac_f32 v149, v163, v163\n"
      "v_add_f32 v202, v202, v102\n"
      "v_fmac_f32 v203, v102, v102\n"
      "v_add_f32 v148, v148, v164\n"
      "v_fmac_f32 v149, v164, v164\n"
      "v_add_f32 v202, v202, v103\n"
      "v_fmac_f32 v203, v103, v103\n"
      "v_add_f32 v148, v148, v165\n"
      "v_fmac_f32 v149, v165, v165\n"
      "v_permlane16_swap_b32 v202, v203\n"
      "v_add_f32 v202, v202, v203\n"
      "v_permlane16_swap_b32 v148, v149\n"
      "v_add_f32 v148, v148, v149\n"
      "v_lshrrev_b32 v82, 4, v120\n"
      "v_add_u32 v211, 0, v82\n"
      "v_permlane32_swap_b32 v202, v148\n"
      "v_add_f32 v202, v202, v148\n"
      "ds_write_b32 v8, v202 offset:4608\n"
      "v_add_u32 v208, 64, v82\n"
      "v_add_u32 v209, 128, v82\n"
      "v_and_b32 v210, 0x3fffffff, v37\n"
      "v_ashrrev_i32 v148, 31, v37\n"
      "v_mul_u32_u24 v202, 2731, v211\n"
      "v_mul_u32_u24 v149, 2731, v208\n"
      "v_mul_u32_u24 v82, 2731, v209\n"
      "v_bfe_i32 v203, v36, 12, 1\n"
      "v_bfe_i32 v126, v36, 13, 1\n"
      "v_add_u32 v210, 1179648, v210\n"
      "v_and_b32 v148, 100270464, v148\n"
      "v_lshrrev_b32 v202, 16, v202\n"
      "v_lshrrev_b32 v149, 16, v149\n"
      "v_lshrrev_b32 v82, 16, v82\n"
      "v_and_b32 v203, s93, v203\n"
      "v_and_b32 v126, s94, v126\n"
      "v_sub_u32 v210, v210, v148\n"
      "v_mul_u32_u24 v125, 24, v202\n"
      "v_mul_u32_u24 v124, 24, v149\n"
      "v_mul_u32_u24 v127, 24, v82\n"
      "v_add_u32 v203, v203, v126\n"
      "v_bfe_i32 v148, v37, 30, 1\n"
      "v_sub_u32 v125, v211, v125\n"
      "v_sub_u32 v124, v208, v124\n"
      "v_sub_u32 v127, v209, v127\n"
      "v_sub_u32 v203, s92, v203\n"
      "v_and_b32 v148, 196224, v148\n"
      "v_and_b32 v211, 7, v202\n"
      "v_and_b32 v208, 7, v149\n"
      "v_and_b32 v209, 7, v82\n"
      "v_add_u32 v203, v21, v203\n"
      "v_sub_u32_e64 v210, v210, v148\n"
      "v_lshl_add_u32 v202, v202, 2, 12\n"
      "v_lshl_add_u32 v149, v149, 2, 12\n"
      "v_lshl_add_u32 v82, v82, 2, 12\n"
      "buffer_store_dwordx4 v[174:177], v203, s[20:23], 0 offen sc1\n"
      "ds_bpermute_b32 v148, v202, v210\n"
      "ds_bpermute_b32 v202, v149, v210\n"
      "ds_bpermute_b32 v149, v82, v210\n"
      "v_bfe_i32 v175, v36, 14, 1\n"
      "v_bfe_i32 v174, v36, 15, 1\n"
      "v_and_b32 v175, s93, v175\n"
      "v_and_b32 v174, s94, v174\n"
      "v_add_u32 v175, v175, v174\n"
      "v_sub_u32 v175, s92, v175\n"
      "v_add_u32 v175, v22, v175\n"
      "buffer_store_dwordx4 v[144:147], v175, s[20:23], 0 offen sc1\n"
      "s_nop 1\n"
      "v_bfe_i32 v147, v36, 16, 1\n"
      "v_bfe_i32 v145, v36, 17, 1\n"
      "v_and_b32 v147, s93, v147\n"
      "v_and_b32 v145, s94, v145\n"
      "v_add_u32 v147, v147, v145\n"
      "v_sub_u32 v147, s92, v147\n"
      "v_add_u32 v147, v23, v147\n"
      "buffer_store_dwordx4 v[180:183], v147, s[20:23], 0 offen sc1\n"
      "s_nop 1\n"
      "v_bfe_i32 v180, v36, 18, 1\n"
      "v_bfe_i32 v183, v36, 19, 1\n"
      "v_and_b32 v180, s93, v180\n"
      "v_and_b32 v183, s94, v183\n"
      "v_add_u32 v180, v180, v183\n"
      "v_sub_u32 v180, s92, v180\n"
      "v_add_u32 v180, v25, v180\n"
      "buffer_store_dwordx4 v[166:169], v180, s[20:23], 0 offen sc1\n"
      "v_xor_b32 v125, v125, v211\n"
      "v_xor_b32 v124, v124, v208\n"
      "v_xor_b32_e64 v127, v127, v209\n"
      "s_waitcnt lgkmcnt(0)\n"
      "v_lshl_add_u32 v148, v125, 4, v148\n"
      "v_lshl_add_u32 v202, v124, 4, v202\n"
      "v_lshl_add_u32 v149, v127, 4, v149\n"
      "s_waitcnt lgkmcnt(0)\n"
      "s_barrier\n"
      "ds_read_b128 v[166:169], v9 offset:4608\n"
      "ds_read_b128 v[180:183], v9 offset:5632\n"
      "v_bfe_i32 v127, v36, 10, 1\n"
      "v_bfe_i32 v125, v36, 11, 1\n"
      "v_bfe_i32 v124, v36, 12, 1\n"
      "v_bfe_i32 v82, v36, 13, 1\n"
      "v_bfe_i32 v203, v36, 14, 1\n"
      "v_bfe_i32 v126, v36, 15, 1\n"
      "v_bfe_i32 v150, v36, 16, 1\n"
      "v_bfe_i32 v151, v36, 17, 1\n"
      "v_bfe_i32 v80, v36, 18, 1\n"
      "v_bfe_i32 v84, v36, 19, 1\n"
      "ds_read_b128 v[144:147], v7\n"
      "ds_read_b128 v[174:177], v7 offset:4096\n"
      "ds_read_b128 v[208:211], v7 offset:8192\n"
      "s_bitcmp1_b32 s46, 31\n"
      "s_cselect_b32 s93, 100270464, 0\n"
      "v_and_b32 v127, s93, v127\n"
      "s_waitcnt lgkmcnt(4)\n"
      "v_add_f32 v166, v166, v168\n"
      "v_add_f32 v167, v167, v169\n"
      "v_and_b32 v124, s93, v124\n"
      "s_waitcnt lgkmcnt(3)\n"
      "v_add_f32 v180, v180, v182\n"
      "v_add_f32 v181, v181, v183\n"
      "v_add_f32 v166, v166, v180\n"
      "v_add_f32_e64 v167, v167, v181\n"
      "v_fma_f32 v180, -v166, v166, s52\n"
      "v_fmac_f32 v180, 0x43400000, v167\n"
      "v_max_f32 v180, s52, v180\n"
      "v_rsq_f32 v180, v180\n"
      "v_and_b32_e64 v203, s93, v203\n"
      "v_mul_f32 v167, 0x43400000, v180\n"
      "v_mul_f32 v183, -v166, v180\n"
      "v_mov_b32 v180, v167\n"
      "v_mov_b32 v166, v183\n"
      "v_and_b32 v150, s93, v150\n"
      "v_permlane32_swap_b32 v167, v180\n"
      "v_permlane32_swap_b32 v183, v166\n"
      "v_mov_b32 v166, v167\n"
      "v_mov_b32 v180, v183\n"
      "v_and_b32 v80, s93, v80\n"
      "v_permlane16_swap_b32 v167, v166\n"
      "v_permlane16_swap_b32 v183, v180\n"
      "v_fma_f32 v104, v104, v167, v183\n"
      "v_fma_f32 v105, v105, v167, v183\n"
      "v_fma_f32 v204, v204, v166, v180\n"
      "v_fma_f32 v205, v205, v166, v180\n"
      "v_cvt_pk_bf16_f32 v104, v104, v105\n"
      "v_fma_f32 v106, v106, v167, v183\n"
      "v_fma_f32 v107, v107, v167, v183\n"
      "v_fma_f32 v100, v100, v167, v183\n"
      "v_fma_f32 v101, v101, v167, v183\n"
      "v_cvt_pk_bf16_f32 v204, v204, v205\n"
      "v_fma_f32 v206, v206, v166, v180\n"
      "v_fma_f32 v207, v207, v166, v180\n"
      "v_fma_f32 v162, v162, v166, v180\n"
      "v_fma_f32 v163, v163, v166, v180\n"
      "v_cvt_pk_bf16_f32 v105, v106, v107\n"
      "v_fma_f32 v214, v214, v167, v183\n"
      "v_fma_f32 v215, v215, v167, v183\n"
      "v_fma_f32 v216, v216, v167, v183\n"
      "v_fma_f32 v217, v217, v167, v183\n"
      "v_cvt_pk_bf16_f32 v100, v100, v101\n"
      "v_fma_f32 v102, v102, v167, v183\n"
      "v_fma_f32 v103, v103, v167, v183\n"
      "v_cvt_pk_bf16_f32 v205, v206, v207\n"
      "v_fma_f32 v220, v220, v166, v180\n"
      "v_fma_f32 v221, v221, v166, v180\n"
      "v_fma_f32 v222, v222, v166, v180\n"
      "v_fma_f32 v223, v223, v166, v180\n"
      "v_cvt_pk_bf16_f32 v162, v162, v163\n"
      "v_fma_f32 v164, v164, v166, v180\n"
      "v_fma_f32 v165, v165, v166, v180\n"
      "v_cvt_pk_bf16_f32 v106, v214, v215\n"
      "v_cvt_pk_bf16_f32 v107, v216, v217\n"
      "v_cvt_pk_bf16_f32 v101, v102, v103\n"
      "v_cvt_pk_bf16_f32 v206, v220, v221\n"
      "v_cvt_pk_bf16_f32 v207, v222, v223\n"
      "v_cvt_pk_bf16_f32 v163, v164, v165\n"
      "ds_write_b128 v136, v[104:107]\n"
      "ds_write_b64 v137, v[100:101]\n"
      "ds_write_b128 v136, v[204:207] offset:6144\n"
      "ds_write_b64 v137, v[162:163] offset:6144\n"
      "s_bitcmp1_b32 s46, 30\n"
      "s_cselect_b32 s94, 196224, 0\n"
      "ds_read_b128 v[162:165], v7 offset:12288\n"
      "s_and_b32 s92, s46, 0x3fffffff\n"
      "ds_read_b128 v[204:207], v7 offset:16384\n"
      "v_and_b32 v125, s94, v125\n"
      "v_add_u32 v127, v127, v125\n"
      "v_sub_u32 v127, s92, v127\n"
      "v_add_u32 v127, v20, v127\n"
      "s_waitcnt lgkmcnt(5)\n"
      "buffer_store_dwordx4 v[144:147], v127, s[20:23], 0 offen sc1\n"
      "v_and_b32 v82, s94, v82\n"
      "v_add_u32 v124, v124, v82\n"
      "v_sub_u32 v124, s92, v124\n"
      "v_add_u32 v124, v21, v124\n"
      "buffer_store_dwordx4 v[174:177], v124, s[20:23], 0 offen sc1\n"
      "v_and_b32 v126, s94, v126\n"
      "v_add_u32 v203, v203, v126\n"
      "v_sub_u32 v203, s92, v203\n"
      "v_add_u32 v203, v22, v203\n"
      "buffer_store_dwordx4 v[208:211], v203, s[20:23], 0 offen sc1\n"
      "v_and_b32 v151, s94, v151\n"
      "v_add_u32 v150, v150, v151\n"
      "v_sub_u32 v150, s92, v150\n"
      "v_add_u32_e64 v150, v23, v150\n"
      "s_waitcnt lgkmcnt(0)\n"
      "buffer_store_dwordx4 v[162:165], v150, s[20:23], 0 offen sc1\n"
      "v_and_b32 v84, s94, v84\n"
      "v_add_u32 v80, v80, v84\n"
      "v_sub_u32 v80, s92, v80\n"
      "v_add_u32 v80, v25, v80\n"
      "buffer_store_dwordx4 v[204:207], v80, s[20:23], 0 offen sc1\n"
      "s_waitcnt lgkmcnt(0)\n"
      "s_barrier\n"
      "ds_read_b128 v[204:207], v120\n"
      "ds_read_b128 v[162:165], v120 offset:1024\n"
      "ds_read_b128 v[208:211], v120 offset:2048\n"
      "s_waitcnt lgkmcnt(2)\n"
      "buffer_store_dwordx4 v[204:207], v148, s[20:23], 0 offen sc1\n"
      "s_waitcnt lgkmcnt(1)\n"
      "buffer_store_dwordx4 v[162:165], v202, s[20:23], 0 offen sc1\n"
      "s_waitcnt lgkmcnt(0)\n"
      "buffer_store_dwordx4 v[208:211], v149, s[20:23], 0 offen sc1\n"
      "s_endpgm\n"
      "L_tail0_%=:\n"
      "s_nop 7\n"
      "s_nop 7\n"
      "v_mfma_f32_16x16x32_bf16 v[202:205], v[210:213], v[104:107], 0\n"
      "ds_read_b128 v[104:107], v9 offset:2048\n"
      "ds_read_b128 v[134:137], v9 offset:3072\n"
      "v_mfma_f32_16x16x32_bf16 v[202:205], v[174:177], v[76:79], v[202:205]\n"
      "ds_read_b128 v[96:99], v2 offset:18432\n"
      "v_mov_b32_dpp v188, v188 quad_perm:[0,0,0,0] row_mask:0xf bank_mask:0xf\n"
      "v_mfma_f32_16x16x32_bf16 v[130:133], v[210:213], v[100:103], 0\n"
      "ds_read_b128 v[100:103], v2 offset:21504\n"
      "v_rcp_f32 v188, v188\n"
      "v_mfma_f32_16x16x32_bf16 v[130:133], v[174:177], v[80:83], v[130:133]\n"
      "s_bitcmp1_b32 s46, 31\n"
      "s_cselect_b32 s93, 100270464, 0\n"
      "v_bfe_i32 v82, v36, 10, 1\n"
      "v_mfma_f32_16x16x32_bf16 v[194:197], v[210:213], v[214:217], 0\n"
      "ds_read_b128 v[210:213], v2 offset:24576\n"
      "v_bfe_i32 v205, v36, 11, 1\n"
      "v_mfma_f32_16x16x32_bf16 v[194:197], v[174:177], v[84:87], v[194:197]\n"
      "ds_read_b128 v[174:177], v2 offset:27648\n"
      "ds_read_b128 v[214:217], v2 offset:30720\n"
      "s_waitcnt lgkmcnt(6)\n"
      "v_add_f32 v104, v104, v106\n"
      "s_waitcnt lgkmcnt(5)\n"
      "v_add_f32 v134, v134, v136\n"
      "v_add_f32 v105, v105, v107\n"
      "v_add_f32 v135, v135, v137\n"
      "v_add_f32 v104, v104, v134\n"
      "v_add_f32 v105, v105, v135\n"
      "v_fma_f32 v134, -v104, v104, s52\n"
      "v_fmac_f32 v134, 0x43400000, v105\n"
      "v_max_f32 v134, s52, v134\n"
      "v_rsq_f32 v134, v134\n"
      "v_cndmask_b32 v202, v202, v130, s[58:59]\n"
      "v_cndmask_b32 v202, v202, v194, s[60:61]\n"
      "v_mul_f32_e64 v202, v188, v202\n"
      "v_mul_f32 v130, 0x43400000, v134\n"
      "v_mul_f32 v132, -v104, v134\n"
      "v_cvt_pk_bf16_f32 v202, v202, v202\n"
      "v_mov_b32 v131, v130\n"
      "v_mov_b32 v133, v132\n"
      "ds_write_b16 v12, v202 offset:16\n"
      "v_permlane32_swap_b32 v130, v131\n"
      "v_permlane32_swap_b32 v132, v133\n"
      "v_mov_b32 v104, v130\n"
      "v_mov_b32 v105, v132\n"
      "v_fma_f32 v120, v120, v131, v133\n"
      "v_permlane16_swap_b32 v130, v104\n"
      "v_permlane16_swap_b32 v132, v105\n"
      "v_fma_f32 v220, v220, v130, v132\n"
      "v_fma_f32 v221, v221, v130, v132\n"
      "v_fma_f32 v166, v166, v104, v105\n"
      "v_fma_f32 v167, v167, v104, v105\n"
      "v_fma_f32 v121, v121, v131, v133\n"
      "v_cvt_pk_bf16_f32 v220, v220, v221\n"
      "v_fma_f32 v222, v222, v130, v132\n"
      "v_fma_f32 v223, v223, v130, v132\n"
      "v_cvt_pk_bf16_f32 v166, v166, v167\n"
      "v_fma_f32 v168, v168, v104, v105\n"
      "v_fma_f32 v169, v169, v104, v105\n"
      "v_cvt_pk_bf16_f32 v120, v120, v121\n"
      "v_fma_f32 v122, v122, v131, v133\n"
      "v_fma_f32 v123, v123, v131, v133\n"
      "v_fma_f32 v162, v162, v130, v132\n"
      "v_fma_f32 v163, v163, v130, v132\n"
      "v_cvt_pk_bf16_f32 v221, v222, v223\n"
      "v_fma_f32 v206, v206, v130, v132\n"
      "v_fma_f32 v207, v207, v130, v132\n"
      "v_fma_f32 v208, v208, v130, v132\n"
      "v_fma_f32 v209, v209, v130, v132\n"
      "v_cvt_pk_bf16_f32 v167, v168, v169\n"
      "v_fma_f32 v180, v180, v104, v105\n"
      "v_fma_f32 v181, v181, v104, v105\n"
      "v_fma_f32 v182, v182, v104, v105\n"
      "v_fma_f32 v183, v183, v104, v105\n"
      "v_cvt_pk_bf16_f32 v121, v122, v123\n"
      "v_fma_f32 v148, v148, v131, v133\n"
      "v_fma_f32 v149, v149, v131, v133\n"
      "v_fma_f32 v150, v150, v131, v133\n"
      "v_fma_f32 v151, v151, v131, v133\n"
      "v_cvt_pk_bf16_f32 v162, v162, v163\n"
      "v_fma_f32 v164, v164, v130, v132\n"
      "v_fma_f32 v165, v165, v130, v132\n"
      "v_cvt_pk_bf16_f32 v222, v206, v207\n"
      "v_cvt_pk_bf16_f32 v223, v208, v209\n"
      "v_cvt_pk_bf16_f32 v168, v180, v181\n"
      "v_cvt_pk_bf16_f32 v169, v182, v183\n"
      "v_cvt_pk_bf16_f32 v122, v148, v149\n"
      "v_cvt_pk_bf16_f32 v123, v150, v151\n"
      "v_cvt_pk_bf16_f32 v163, v164, v165\n"
      "v_fma_f32 v124, v124, v104, v105\n"
      "v_fma_f32 v125, v125, v104, v105\n"
      "v_fma_f32 v144, v144, v131, v133\n"
      "v_fma_f32 v145, v145, v131, v133\n"
      "ds_write_b128 v5, v[220:223]\n"
      "ds_write_b128 v5, v[166:169] offset:6144\n"
      "ds_write_b128 v5, v[120:123] offset:12288\n"
      "ds_write_b64 v6, v[162:163]\n"
      "v_cvt_pk_bf16_f32 v124, v124, v125\n"
      "v_fma_f32 v126, v126, v104, v105\n"
      "v_fma_f32 v127, v127, v104, v105\n"
      "v_cvt_pk_bf16_f32 v144, v144, v145\n"
      "v_fma_f32 v146, v146, v131, v133\n"
      "v_fma_f32 v147, v147, v131, v133\n"
      "v_cvt_pk_bf16_f32 v125, v126, v127\n"
      "v_cvt_pk_bf16_f32 v145, v146, v147\n"
      "ds_write_b64 v6, v[124:125] offset:6144\n"
      "ds_write_b64 v6, v[144:145] offset:12288\n"
      "s_waitcnt lgkmcnt(4)\n"
      "v_mfma_f32_16x16x32_bf16 v[220:223], a[216:219], v[96:99], 0\n"
      "s_bitcmp1_b32 s46, 30\n"
      "s_cselect_b32 s94, 196224, 0\n"
      "v_and_b32 v82, s93, v82\n"
      "v_mfma_f32_16x16x32_bf16 v[206:209], a[240:243], v[96:99], 0\n"
      "s_and_b32 s92, s46, 0x3fffffff\n"
      "v_and_b32 v205, s94, v205\n"
      "v_add_u32 v82, v82, v205\n"
      "v_mfma_f32_16x16x32_bf16 v[162:165], v[232:235], v[96:99], 0\n"
      "ds_read_b128 v[166:169], v2 offset:33792\n"
      "v_sub_u32 v82, s92, v82\n"
      "v_mfma_f32_16x16x32_bf16 v[220:223], a[220:223], v[100:103], v[220:223]\n"
      "v_add_u32 v82, v20, v82\n"
      "v_bfe_i32 v107, v36, 12, 1\n"
      "v_mfma_f32_16x16x32_bf16 v[206:209], a[244:247], v[100:103], v[206:209]\n"
      "v_bfe_i32 v145, v36, 13, 1\n"
      "v_and_b32 v107, s93, v107\n"
      "v_mfma_f32_16x16x32_bf16 v[162:165], v[236:239], v[100:103], v[162:165]\n"
      "ds_read_b128 v[120:123], v2 offset:18688\n"
      "v_and_b32 v145, s94, v145\n"
      "v_mfma_f32_16x16x32_bf16 v[220:223], a[224:227], v[210:213], v[220:223]\n"
      "v_add_u32 v107, v107, v145\n"
      "v_sub_u32 v107, s92, v107\n"
      "v_mfma_f32_16x16x32_bf16 v[206:209], a[248:251], v[210:213], v[206:209]\n"
      "v_add_u32 v107, v21, v107\n"
      "v_bfe_i32 v77, v36, 14, 1\n"
      "v_mfma_f32_16x16x32_bf16 v[162:165], v[240:243], v[210:213], v[162:165]\n"
      "ds_read_b128 v[144:147], v2 offset:21760\n"
      "v_bfe_i32 v151, v36, 15, 1\n"
      "v_mfma_f32_16x16x32_bf16 v[220:223], a[228:231], v[174:177], v[220:223]\n"
      "s_waitcnt lgkmcnt(2)\n"
      "v_and_b32 v77, s93, v77\n"
      "v_and_b32 v151, s94, v151\n"
      "v_mfma_f32_16x16x32_bf16 v[206:209], a[252:255], v[174:177], v[206:209]\n"
      "v_add_u32 v77, v77, v151\n"
      "v_sub_u32_e64 v77, s92, v77\n"
      "v_mfma_f32_16x16x32_bf16 v[162:165], v[244:247], v[174:177], v[162:165]\n"
      "ds_read_b128 v[148:151], v2 offset:24832\n"
      "v_add_u32 v77, v22, v77\n"
      "v_mfma_f32_16x16x32_bf16 v[220:223], a[232:235], v[214:217], v[220:223]\n"
      "s_waitcnt lgkmcnt(2)\n"
      "v_bfe_i32 v78, v36, 16, 1\n"
      "v_bfe_i32 v211, v36, 17, 1\n"
      "v_mfma_f32_16x16x32_bf16 v[206:209], v[224:227], v[214:217], v[206:209]\n"
      "v_and_b32 v78, s93, v78\n"
      "v_and_b32 v211, s94, v211\n"
      "v_mfma_f32_16x16x32_bf16 v[162:165], v[248:251], v[214:217], v[162:165]\n"
      "ds_read_b128 v[214:217], v2 offset:27904\n"
      "v_add_u32 v78, v78, v211\n"
      "v_mfma_f32_16x16x32_bf16 v[220:223], a[236:239], v[166:169], v[220:223]\n"
      "s_waitcnt lgkmcnt(2)\n"
      "v_sub_u32 v78, s92, v78\n"
      "v_add_u32 v78, v23, v78\n"
      "v_mfma_f32_16x16x32_bf16 v[206:209], v[228:231], v[166:169], v[206:209]\n"
      "v_bfe_i32 v81, v36, 18, 1\n"
      "v_bfe_i32 v205, v36, 19, 1\n"
      "v_mfma_f32_16x16x32_bf16 v[162:165], v[252:255], v[166:169], v[162:165]\n"
      "ds_read_b128 v[130:133], v2 offset:30976\n"
      "v_mul_f32 v211, v220, v220\n"
      "v_mfma_f32_16x16x32_bf16 v[166:169], a[216:219], v[120:123], 0\n"
      "s_waitcnt lgkmcnt(2)\n"
      "v_add_f32 v210, v220, v221\n"
      "v_fmac_f32 v211, v221, v221\n"
      "v_mfma_f32_16x16x32_bf16 v[180:183], a[240:243], v[120:123], 0\n"
      "v_add_f32 v210, v210, v222\n"
      "v_fmac_f32 v211, v222, v222\n"
      "v_mfma_f32_16x16x32_bf16 v[124:127], v[232:235], v[120:123], 0\n"
      "ds_read_b128 v[120:123], v2 offset:34048\n"
      "v_add_f32 v210, v210, v223\n"
      "v_mfma_f32_16x16x32_bf16 v[166:169], a[220:223], v[144:147], v[166:169]\n"
      "s_waitcnt lgkmcnt(2)\n"
      "v_fmac_f32 v211, v223, v223\n"
      "v_add_f32 v210, v210, v206\n"
      "v_mfma_f32_16x16x32_bf16 v[180:183], a[244:247], v[144:147], v[180:183]\n"
      "v_fmac_f32 v211, v206, v206\n"
      "v_add_f32 v210, v210, v207\n"
      "v_mfma_f32_16x16x32_bf16 v[124:127], v[236:239], v[144:147], v[124:127]\n"
      "ds_read_b128 v[96:99], v2 offset:18944\n"
      "v_fmac_f32_e64 v211, v207, v207\n"
      "v_mfma_f32_16x16x32_bf16 v[166:169], a[224:227], v[148:151], v[166:169]\n"
      "s_waitcnt lgkmcnt(2)\n"
      "v_add_f32 v210, v210, v208\n"
      "v_fmac_f32 v211, v208, v208\n"
      "v_mfma_f32_16x16x32_bf16 v[180:183], a[248:251], v[148:151], v[180:183]\n"
      "v_add_f32 v210, v210, v209\n"
      "v_fmac_f32 v211, v209, v209\n"
      "v_mfma_f32_16x16x32_bf16 v[124:127], v[240:243], v[148:151], v[124:127]\n"
      "ds_read_b128 v[102:105], v2 offset:22016\n"
      "v_add_f32 v210, v210, v162\n"
      "v_mfma_f32_16x16x32_bf16 v[166:169], a[228:231], v[214:217], v[166:169]\n"
      "s_waitcnt lgkmcnt(2)\n"
      "v_fmac_f32 v211, v162, v162\n"
      "v_add_f32 v210, v210, v163\n"
      "v_mfma_f32_16x16x32_bf16 v[180:183], a[252:255], v[214:217], v[180:183]\n"
      "v_fmac_f32 v211, v163, v163\n"
      "v_add_f32_e64 v210, v210, v164\n"
      "v_mfma_f32_16x16x32_bf16 v[124:127], v[244:247], v[214:217], v[124:127]\n"
      "ds_read_b128 v[212:215], v2 offset:25088\n"
      "v_fmac_f32 v211, v164, v164\n"
      "v_mfma_f32_16x16x32_bf16 v[166:169], a[232:235], v[130:133], v[166:169]\n"
      "s_waitcnt lgkmcnt(2)\n"
      "v_add_f32 v210, v210, v165\n"
      "v_fmac_f32 v211, v165, v165\n"
      "v_mfma_f32_16x16x32_bf16 v[180:183], v[224:227], v[130:133], v[180:183]\n"
      "v_and_b32 v81, s93, v81\n"
      "v_permlane16_swap_b32 v210, v211\n"
      "v_mfma_f32_16x16x32_bf16 v[124:127], v[248:251], v[130:133], v[124:127]\n"
      "ds_read_b128 v[174:177], v2 offset:28160\n"
      "v_add_f32 v210, v210, v211\n"
      "v_mfma_f32_16x16x32_bf16 v[166:169], a[236:239], v[120:123], v[166:169]\n"
      "ds_read_b128 v[130:133], v7 offset:18432\n"
      "v_and_b32 v205, s94, v205\n"
      "v_mfma_f32_16x16x32_bf16 v[180:183], v[228:231], v[120:123], v[180:183]\n"
      "s_waitcnt lgkmcnt(3)\n"
      "v_add_u32 v81, v81, v205\n"
      "v_sub_u32_e64 v81, s92, v81\n"
      "v_mfma_f32_16x16x32_bf16 v[124:127], v[252:255], v[120:123], v[124:127]\n"
      "ds_read_b128 v[202:205], v7 offset:22528\n"
      "v_mul_f32 v217, v166, v166\n"
      "v_mfma_f32_16x16x32_bf16 v[120:123], a[216:219], v[96:99], 0\n"
      "ds_read_b128 v[134:137], v2 offset:31232\n"
      "v_add_f32 v216, v166, v167\n"
      "v_mfma_f32_16x16x32_bf16 v[148:151], a[240:243], v[96:99], 0\n"
      "ds_read_b128 v[194:197], v7 offset:26624\n"
      "v_fmac_f32_e64 v217, v167, v167\n"
      "v_mfma_f32_16x16x32_bf16 v[144:147], v[232:235], v[96:99], 0\n"
      "ds_read_b128 v[96:99], v7 offset:30720\n"
      "v_add_f32 v216, v216, v168\n"
      "v_mfma_f32_16x16x32_bf16 v[120:123], a[220:223], v[102:105], v[120:123]\n"
      "s_waitcnt lgkmcnt(4)\n"
      "v_fmac_f32 v217, v168, v168\n"
      "v_add_f32 v216, v216, v169\n"
      "v_mfma_f32_16x16x32_bf16 v[148:151], a[244:247], v[102:105], v[148:151]\n"
      "ds_read_b128 v[188:191], v2 offset:34304\n"
      "v_fmac_f32_e64 v217, v169, v169\n"
      "v_mfma_f32_16x16x32_bf16 v[144:147], v[236:239], v[102:105], v[144:147]\n"
      "buffer_store_dwordx4 v[130:133], v82, s[20:23], 0 offen sc1\n"
      "v_mfma_f32_16x16x32_bf16 v[120:123], a[224:227], v[212:215], v[120:123]\n"
      "ds_read_b128 v[100:103], v7 offset:34816\n"
      "v_add_f32_e64 v216, v216, v180\n"
      "v_mfma_f32_16x16x32_bf16 v[148:151], a[248:251], v[212:215], v[148:151]\n"
      "v_fmac_f32 v217, v180, v180\n"
      "v_add_f32 v216, v216, v181\n"
      "v_mfma_f32_16x16x32_bf16 v[144:147], v[240:243], v[212:215], v[144:147]\n"
      "v_fmac_f32 v217, v181, v181\n"
      "v_add_f32 v216, v216, v182\n"
      "v_mfma_f32_16x16x32_bf16 v[120:123], a[228:231], v[174:177], v[120:123]\n"
      "s_waitcnt lgkmcnt(1)\n"
      "v_fmac_f32 v217, v182, v182\n"
      "v_add_f32_e64 v216, v216, v183\n"
      "v_mfma_f32_16x16x32_bf16 v[148:151], a[252:255], v[174:177], v[148:151]\n"
      "buffer_store_dwordx4 v[202:205], v107, s[20:23], 0 offen sc1\n"
      "v_mfma_f32_16x16x32_bf16 v[144:147], v[244:247], v[174:177], v[144:147]\n"
      "buffer_store_dwordx4 v[194:197], v77, s[20:23], 0 offen sc1\n"
      "v_mfma_f32_16x16x32_bf16 v[120:123], a[232:235], v[134:137], v[120:123]\n"
      "buffer_store_dwordx4 v[96:99], v78, s[20:23], 0 offen sc1\n"
      "v_mfma_f32_16x16x32_bf16 v[148:151], v[224:227], v[134:137], v[148:151]\n"
      "s_waitcnt lgkmcnt(0)\n"
      "v_fmac_f32 v217, v183, v183\n"
      "v_add_f32_e64 v216, v216, v124\n"
      "v_mfma_f32_16x16x32_bf16 v[144:147], v[248:251], v[134:137], v[144:147]\n"
      "v_fmac_f32 v217, v124, v124\n"
      "v_add_f32 v216, v216, v125\n"
      "v_mfma_f32_16x16x32_bf16 v[120:123], a[236:239], v[188:191], v[120:123]\n"
      "v_fmac_f32 v217, v125, v125\n"
      "v_add_f32 v216, v216, v126\n"
      "v_mfma_f32_16x16x32_bf16 v[148:151], v[228:231], v[188:191], v[148:151]\n"
      "v_fmac_f32 v217, v126, v126\n"
      "v_add_f32 v216, v216, v127\n"
      "v_mfma_f32_16x16x32_bf16 v[144:147], v[252:255], v[188:191], v[144:147]\n"
      "v_fmac_f32 v217, v127, v127\n"
      "v_add_u32 v81, v25, v81\n"
      "v_mul_f32 v97, v120, v120\n"
      "v_add_f32 v96, v120, v121\n"
      "v_fmac_f32 v97, v121, v121\n"
      "v_add_f32 v96, v96, v122\n"
      "v_fmac_f32 v97, v122, v122\n"
      "v_add_f32 v96, v96, v123\n"
      "v_fmac_f32 v97, v123, v123\n"
      "v_add_f32 v96, v96, v148\n"
      "v_fmac_f32 v97, v148, v148\n"
      "v_add_f32 v96, v96, v149\n"
      "v_fmac_f32 v97, v149, v149\n"
      "v_add_f32 v96, v96, v150\n"
      "v_fmac_f32 v97, v150, v150\n"
      "v_add_f32 v96, v96, v151\n"
      "v_fmac_f32 v97, v151, v151\n"
      "v_add_f32 v96, v96, v144\n"
      "v_fmac_f32 v97, v144, v144\n"
      "v_add_f32 v96, v96, v145\n"
      "v_fmac_f32 v97, v145, v145\n"
      "v_add_f32 v96, v96, v146\n"
      "v_fmac_f32 v97, v146, v146\n"
      "v_add_f32 v96, v96, v147\n"
      "v_fmac_f32 v97, v147, v147\n"
      "v_permlane16_swap_b32 v216, v217\n"
      "v_add_f32 v216, v216, v217\n"
      "v_permlane16_swap_b32 v96, v97\n"
      "v_add_f32 v96, v96, v97\n"
      "v_mov_b32 v97, v96\n"
      "v_permlane32_swap_b32 v210, v216\n"
      "v_add_f32 v210, v210, v216\n"
      "v_permlane32_swap_b32 v96, v97\n"
      "v_add_f32 v96, v96, v97\n"
      "ds_write_b32 v8, v210\n"
      "ds_write_b32 v8, v96 offset:512\n"
      "buffer_store_dwordx4 v[100:103], v81, s[20:23], 0 offen sc1\n"
      "v_readlane_b32 s46, v37, s42\n"
      "s_add_u32 s42, s42, 1\n"
      "v_add_u32 v12, 32, v12\n"
      "s_waitcnt lgkmcnt(0)\n"
      "s_barrier\n"
      "ds_read_b128 v[96:99], v9\n"
      "ds_read_b128 v[100:103], v9 offset:1024\n"
      "v_subrev_u32 v131, 115456, v9\n"
      "v_and_b32 v210, 0x300, v131\n"
      "v_add_u32_e64 v131, v131, v210\n"
      "v_add_u32 v131, 149504, v131\n"
      "ds_read_b128 v[134:137], v131\n"
      "ds_read_b128 v[188:191], v131 offset:2048\n"
      "ds_read_b128 v[210:213], v131 offset:4096\n"
      "v_subrev_u32 v81, 73728, v5\n"
      "v_subrev_u32 v78, 73728, v6\n"
      "s_bitcmp1_b32 s46, 31\n"
      "s_cselect_b32 s93, 100270464, 0\n"
      "s_bitcmp1_b32 s46, 30\n"
      "s_cselect_b32 s94, 196224, 0\n"
      "s_and_b32 s92, s46, 0x3fffffff\n"
      "v_readlane_b32 s46, v37, s42\n"
      "s_mul_i32 s74, s40, 3072\n"
      "s_waitcnt lgkmcnt(4)\n"
      "v_add_f32 v96, v96, v98\n"
      "v_add_f32 v97, v97, v99\n"
      "v_bfe_i32 v107, v36, 10, 1\n"
      "s_waitcnt lgkmcnt(3)\n"
      "v_add_f32 v100, v100, v102\n"
      "v_add_f32 v101, v101, v103\n"
      "v_add_f32 v96, v96, v100\n"
      "v_add_f32 v97, v97, v101\n"
      "s_waitcnt lgkmcnt(2)\n"
      "v_mfma_f32_16x16x32_bf16 v[102:105], a[216:219], v[134:137], 0\n"
      "s_add_u32 s42, s42, 1\n"
      "v_fma_f32 v100, -v96, v96, s52\n"
      "v_fmac_f32 v100, 0x43400000, v97\n"
      "s_waitcnt lgkmcnt(1)\n"
      "v_mfma_f32_16x16x32_bf16 v[102:105], a[220:223], v[188:191], v[102:105]\n"
      "v_max_f32 v100, s52, v100\n"
      "v_rsq_f32_e64 v100, v100\n"
      "v_bfe_i32 v97, v36, 11, 1\n"
      "v_mul_f32 v101, -v96, v100\n"
      "v_mul_f32 v130, 0x43400000, v100\n"
      "v_and_b32 v107, s93, v107\n"
      "v_and_b32 v97, s94, v97\n"
      "v_add_u32 v107, v107, v97\n"
      "v_mov_b32 v100, v130\n"
      "v_sub_u32 v107, s92, v107\n"
      "v_add_u32 v107, v20, v107\n"
      "v_permlane32_swap_b32 v130, v100\n"
      "v_mfma_f32_16x16x32_bf16 v[96:99], a[240:243], v[134:137], 0\n"
      "s_waitcnt lgkmcnt(0)\n"
      "v_mfma_f32_16x16x32_bf16 v[96:99], a[244:247], v[188:191], v[96:99]\n"
      "v_mfma_f32_16x16x32_bf16 v[194:197], v[232:235], v[134:137], 0\n"
      "v_mfma_f32_16x16x32_bf16 v[194:197], v[236:239], v[188:191], v[194:197]\n"
      "v_mfma_f32_16x16x32_bf16 v[102:105], a[224:227], v[210:213], v[102:105]\n"
      "ds_read_b128 v[188:191], v131 offset:6144\n"
      "ds_read_b128 v[174:177], v131 offset:8192\n"
      "ds_read_b128 v[132:135], v131 offset:10240\n"
      "ds_read_b128 v[214:217], v131 offset:256\n"
      "ds_read_b128 v[202:205], v131 offset:2304\n"
      "v_mov_b32_e64 v136, v101\n"
      "v_mov_b32_e64 v137, v130\n"
      "v_subrev_u32 v82, 115456, v9\n"
      "v_permlane32_swap_b32_e64 v101, v136\n"
      "v_permlane16_swap_b32 v130, v137\n"
      "v_fma_f32 v120, v120, v100, v136\n"
      "v_fma_f32 v121, v121, v100, v136\n"
      "v_cvt_pk_bf16_f32 v120, v120, v121\n"
      "v_fma_f32 v122, v122, v100, v136\n"
      "v_fma_f32 v123, v123, v100, v136\n"
      "v_fma_f32 v144, v144, v100, v136\n"
      "v_fma_f32 v145, v145, v100, v136\n"
      "s_waitcnt lgkmcnt(4)\n"
      "v_mfma_f32_16x16x32_bf16 v[102:105], a[228:231], v[188:191], v[102:105]\n"
      "v_cvt_pk_bf16_f32 v121, v122, v123\n"
      "v_fma_f32 v148, v148, v100, v136\n"
      "v_mfma_f32_16x16x32_bf16 v[96:99], a[248:251], v[210:213], v[96:99]\n"
      "s_waitcnt lgkmcnt(3)\n"
      "v_fma_f32 v149, v149, v100, v136\n"
      "v_fma_f32 v150, v150, v100, v136\n"
      "v_mfma_f32_16x16x32_bf16 v[96:99], a[252:255], v[188:191], v[96:99]\n"
      "s_waitcnt lgkmcnt(2)\n"
      "v_fma_f32 v151, v151, v100, v136\n"
      "v_cvt_pk_bf16_f32 v144, v144, v145\n"
      "v_mfma_f32_16x16x32_bf16 v[194:197], v[240:243], v[210:213], v[194:197]\n"
      "s_waitcnt lgkmcnt(1)\n"
      "v_fma_f32 v146, v146, v100, v136\n"
      "v_fma_f32 v147, v147, v100, v136\n"
      "v_mfma_f32_16x16x32_bf16 v[194:197], v[244:247], v[188:191], v[194:197]\n"
      "s_waitcnt lgkmcnt(0)\n"
      "v_cvt_pk_bf16_f32 v122, v148, v149\n"
      "v_cvt_pk_bf16_f32 v123, v150, v151\n"
      "v_mfma_f32_16x16x32_bf16 v[102:105], a[232:235], v[174:177], v[102:105]\n"
      "ds_write_b128 v5, v[120:123] offset:30720\n"
      "v_mfma_f32_16x16x32_bf16 v[102:105], a[236:239], v[132:135], v[102:105]\n"
      "v_cvt_pk_bf16_f32 v145, v146, v147\n"
      "ds_write_b64 v6, v[144:145] offset:30720\n"
      "v_mfma_f32_16x16x32_bf16 v[96:99], v[224:227], v[174:177], v[96:99]\n"
      "v_mfma_f32_16x16x32_bf16 v[96:99], v[228:231], v[132:135], v[96:99]\n"
      "v_mfma_f32_16x16x32_bf16 v[194:197], v[248:251], v[174:177], v[194:197]\n"
      "v_mov_b32 v188, v101\n"
      "v_add_u32 v100, s74, v82\n"
      "v_mfma_f32_16x16x32_bf16 v[194:197], v[252:255], v[132:135], v[194:197]\n"
      "ds_read_b128 v[146:149], v131 offset:4352\n"
      "v_permlane16_swap_b32_e64 v101, v188\n"
      "v_fma_f32 v220, v220, v130, v101\n"
      "v_fma_f32 v221, v221, v130, v101\n"
      "v_cvt_pk_bf16_f32 v220, v220, v221\n"
      "v_fma_f32 v222, v222, v130, v101\n"
      "v_fma_f32 v223, v223, v130, v101\n"
      "v_fma_f32 v162, v162, v130, v101\n"
      "v_fma_f32 v163, v163, v130, v101\n"
      "v_cvt_pk_bf16_f32 v221, v222, v223\n"
      "v_fma_f32 v206, v206, v130, v101\n"
      "v_fma_f32 v207, v207, v130, v101\n"
      "v_fma_f32 v208, v208, v130, v101\n"
      "v_fma_f32 v209, v209, v130, v101\n"
      "v_cvt_pk_bf16_f32 v162, v162, v163\n"
      "v_fma_f32 v164, v164, v130, v101\n"
      "v_fma_f32 v165, v165, v130, v101\n"
      "v_cvt_pk_bf16_f32 v222, v206, v207\n"
      "v_mfma_f32_16x16x32_bf16 v[174:177], a[216:219], v[214:217], 0\n"
      "v_cvt_pk_bf16_f32 v223, v208, v209\n"
      "v_cvt_pk_bf16_f32 v163, v164, v165\n"
      "v_mfma_f32_16x16x32_bf16 v[174:177], a[220:223], v[202:205], v[174:177]\n"
      "ds_write_b128 v5, v[220:223] offset:18432\n"
      "v_mfma_f32_16x16x32_bf16 v[210:213], a[240:243], v[214:217], 0\n"
      "ds_write_b64 v6, v[162:163] offset:18432\n"
      "v_mfma_f32_16x16x32_bf16 v[210:213], a[244:247], v[202:205], v[210:213]\n"
      "s_waitcnt lgkmcnt(2)\n"
      "v_fma_f32 v166, v166, v137, v188\n"
      "v_fma_f32 v167, v167, v137, v188\n"
      "v_mfma_f32_16x16x32_bf16 v[120:123], v[232:235], v[214:217], 0\n"
      "ds_read_b128 v[162:165], v131 offset:6400\n"
      "v_cvt_pk_bf16_f32 v166, v166, v167\n"
      "v_mfma_f32_16x16x32_bf16 v[120:123], v[236:239], v[202:205], v[120:123]\n"
      "ds_read_b128 v[204:207], v7\n"
      "v_fma_f32 v168, v168, v137, v188\n"
      "v_mfma_f32_16x16x32_bf16 v[174:177], a[224:227], v[146:149], v[174:177]\n"
      "v_fma_f32 v169, v169, v137, v188\n"
      "v_fma_f32 v124, v124, v137, v188\n"
      "v_fma_f32 v125, v125, v137, v188\n"
      "v_cvt_pk_bf16_f32 v167, v168, v169\n"
      "v_fma_f32 v180, v180, v137, v188\n"
      "v_fma_f32 v181, v181, v137, v188\n"
      "v_fma_f32 v182, v182, v137, v188\n"
      "v_fma_f32 v183, v183, v137, v188\n"
      "v_cvt_pk_bf16_f32 v124, v124, v125\n"
      "v_fma_f32 v126, v126, v137, v188\n"
      "v_fma_f32 v127, v127, v137, v188\n"
      "ds_read_b128 v[188:191], v7 offset:4096\n"
      "v_cvt_pk_bf16_f32 v168, v180, v181\n"
      "s_waitcnt lgkmcnt(2)\n"
      "v_mfma_f32_16x16x32_bf16 v[174:177], a[228:231], v[162:165], v[174:177]\n"
      "v_cvt_pk_bf16_f32 v169, v182, v183\n"
      "v_cvt_pk_bf16_f32 v125, v126, v127\n"
      "v_mfma_f32_16x16x32_bf16 v[210:213], a[248:251], v[146:149], v[210:213]\n"
      "s_waitcnt lgkmcnt(1)\n"
      "ds_write_b128 v5, v[166:169] offset:24576\n"
      "v_mfma_f32_16x16x32_bf16 v[210:213], a[252:255], v[162:165], v[210:213]\n"
      "ds_write_b64 v6, v[124:125] offset:24576\n"
      "v_mfma_f32_16x16x32_bf16 v[120:123], v[240:243], v[146:149], v[120:123]\n"
      "s_waitcnt lgkmcnt(2)\n"
      "v_mul_f32 v203, v102, v102\n"
      "v_add_f32 v202, v102, v103\n"
      "v_mfma_f32_16x16x32_bf16 v[120:123], v[244:247], v[162:165], v[120:123]\n"
      "ds_read_b128 v[164:167], v131 offset:8448\n"
      "ds_read_b128 v[134:137], v131 offset:10496\n"
      "ds_read_b128 v[144:147], v7 offset:8192\n"
      "ds_read_b128 v[148:151], v7 offset:12288\n"
      "ds_read_b128 v[180:183], v7 offset:16384\n"
      "buffer_store_dwordx4 v[204:207], v107, s[20:23], 0 offen sc1\n"
      "v_fmac_f32 v203, v103, v103\n"
      "v_add_f32 v202, v202, v104\n"
      "v_fmac_f32 v203, v104, v104\n"
      "v_add_f32 v202, v202, v105\n"
      "v_fmac_f32 v203, v105, v105\n"
      "v_add_f32 v202, v202, v96\n"
      "v_fmac_f32 v203, v96, v96\n"
      "s_waitcnt lgkmcnt(4)\n"
      "v_mfma_f32_16x16x32_bf16 v[174:177], a[232:235], v[164:167], v[174:177]\n"
      "v_add_f32 v202, v202, v97\n"
      "v_fmac_f32_e64 v203, v97, v97\n"
      "s_waitcnt lgkmcnt(3)\n"
      "v_mfma_f32_16x16x32_bf16 v[174:177], a[236:239], v[134:137], v[174:177]\n"
      "v_add_f32 v202, v202, v98\n"
      "v_fmac_f32 v203, v98, v98\n"
      "v_mfma_f32_16x16x32_bf16 v[210:213], v[224:227], v[164:167], v[210:213]\n"
      "s_waitcnt lgkmcnt(2)\n"
      "v_add_f32 v202, v202, v99\n"
      "v_fmac_f32 v203, v99, v99\n"
      "v_mfma_f32_16x16x32_bf16 v[210:213], v[228:231], v[134:137], v[210:213]\n"
      "s_waitcnt lgkmcnt(1)\n"
      "v_mul_f32 v206, v174, v174\n"
      "v_add_f32 v205, v174, v175\n"
      "v_mfma_f32_16x16x32_bf16 v[120:123], v[248:251], v[164:167], v[120:123]\n"
      "s_waitcnt lgkmcnt(0)\n"
      "v_fmac_f32 v206, v175, v175\n"
      "v_add_f32 v205, v205, v176\n"
      "v_mfma_f32_16x16x32_bf16 v[120:123], v[252:255], v[134:137], v[120:123]\n"
      "v_fmac_f32 v206, v176, v176\n"
      "v_add_f32 v205, v205, v177\n"
      "v_fmac_f32 v206, v177, v177\n"
      "v_add_f32 v205, v205, v210\n"
      "v_fmac_f32 v206, v210, v210\n"
      "v_add_f32 v205, v205, v211\n"
      "v_fmac_f32 v206, v211, v211\n"
      "v_add_f32 v205, v205, v212\n"
      "v_fmac_f32 v206, v212, v212\n"
      "v_add_f32 v205, v205, v213\n"
      "v_fmac_f32 v206, v213, v213\n"
      "v_add_f32 v202, v202, v194\n"
      "v_fmac_f32 v203, v194, v194\n"
      "v_add_f32 v205, v205, v120\n"
      "v_fmac_f32 v206, v120, v120\n"
      "v_add_f32 v202, v202, v195\n"
      "v_fmac_f32 v203, v195, v195\n"
      "v_add_f32 v205, v205, v121\n"
      "v_fmac_f32 v206, v121, v121\n"
      "v_add_f32 v202, v202, v196\n"
      "v_fmac_f32 v203, v196, v196\n"
      "v_add_f32 v205, v205, v122\n"
      "v_fmac_f32 v206, v122, v122\n"
      "v_add_f32 v202, v202, v197\n"
      "v_fmac_f32 v203, v197, v197\n"
      "v_add_f32 v205, v205, v123\n"
      "v_fmac_f32 v206, v123, v123\n"
      "v_permlane16_swap_b32 v202, v203\n"
      "v_add_f32 v202, v202, v203\n"
      "v_permlane16_swap_b32 v205, v206\n"
      "v_add_f32 v205, v205, v206\n"
      "v_lshrrev_b32 v82, 4, v100\n"
      "v_add_u32 v166, 0, v82\n"
      "v_permlane32_swap_b32 v202, v205\n"
      "v_add_f32 v202, v202, v205\n"
      "ds_write_b32 v8, v202 offset:4608\n"
      "v_add_u32 v165, 64, v82\n"
      "v_add_u32 v164, 128, v82\n"
      "v_and_b32 v167, 0x3fffffff, v37\n"
      "v_ashrrev_i32 v205, 31, v37\n"
      "v_mul_u32_u24 v206, 2731, v166\n"
      "v_mul_u32_u24 v207, 2731, v165\n"
      "v_mul_u32_u24 v204, 2731, v164\n"
      "v_bfe_i32 v203, v36, 12, 1\n"
      "v_bfe_i32 v162, v36, 13, 1\n"
      "v_add_u32 v167, 1179648, v167\n"
      "v_and_b32 v205, 100270464, v205\n"
      "v_lshrrev_b32 v206, 16, v206\n"
      "v_lshrrev_b32 v207, 16, v207\n"
      "v_lshrrev_b32 v204, 16, v204\n"
      "v_and_b32 v203, s93, v203\n"
      "v_and_b32 v162, s94, v162\n"
      "v_sub_u32 v167, v167, v205\n"
      "v_mul_u32_u24 v136, 24, v206\n"
      "v_mul_u32_u24 v82, 24, v207\n"
      "v_mul_u32_u24 v107, 24, v204\n"
      "v_add_u32 v203, v203, v162\n"
      "v_bfe_i32 v205, v37, 30, 1\n"
      "v_sub_u32 v136, v166, v136\n"
      "v_sub_u32 v82, v165, v82\n"
      "v_sub_u32 v107, v164, v107\n"
      "v_sub_u32 v203, s92, v203\n"
      "v_and_b32 v205, 196224, v205\n"
      "v_and_b32 v166, 7, v206\n"
      "v_and_b32 v165, 7, v207\n"
      "v_and_b32 v164, 7, v204\n"
      "v_add_u32 v203, v21, v203\n"
      "v_sub_u32_e64 v167, v167, v205\n"
      "v_lshl_add_u32 v206, v206, 2, 12\n"
      "v_lshl_add_u32 v207, v207, 2, 12\n"
      "v_lshl_add_u32 v204, v204, 2, 12\n"
      "buffer_store_dwordx4 v[188:191], v203, s[20:23], 0 offen sc1\n"
      "ds_bpermute_b32 v203, v206, v167\n"
      "ds_bpermute_b32 v214, v207, v167\n"
      "ds_bpermute_b32 v188, v204, v167\n"
      "v_bfe_i32 v167, v36, 14, 1\n"
      "v_bfe_i32 v206, v36, 15, 1\n"
      "v_and_b32 v167, s93, v167\n"
      "v_and_b32 v206, s94, v206\n"
      "v_add_u32 v167, v167, v206\n"
      "v_sub_u32 v167, s92, v167\n"
      "v_add_u32_e64 v167, v22, v167\n"
      "buffer_store_dwordx4 v[144:147], v167, s[20:23], 0 offen sc1\n"
      "v_bfe_i32 v167, v36, 16, 1\n"
      "v_bfe_i32 v206, v36, 17, 1\n"
      "v_and_b32 v167, s93, v167\n"
      "v_and_b32 v206, s94, v206\n"
      "v_add_u32 v167, v167, v206\n"
      "v_sub_u32 v167, s92, v167\n"
      "v_add_u32 v167, v23, v167\n"
      "buffer_store_dwordx4 v[148:151], v167, s[20:23], 0 offen sc1\n"
      "v_bfe_i32 v167, v36, 18, 1\n"
      "v_bfe_i32 v206, v36, 19, 1\n"
      "v_and_b32 v167, s93, v167\n"
      "v_and_b32 v206, s94, v206\n"
      "v_add_u32 v167, v167, v206\n"
      "v_sub_u32 v167, s92, v167\n"
      "v_add_u32 v167, v25, v167\n"
      "buffer_store_dwordx4 v[180:183], v167, s[20:23], 0 offen sc1\n"
      "v_xor_b32 v136, v136, v166\n"
      "v_xor_b32 v82, v82, v165\n"
      "v_xor_b32 v107, v107, v164\n"
      "s_waitcnt lgkmcnt(0)\n"
      "v_lshl_add_u32 v203, v136, 4, v203\n"
      "v_lshl_add_u32 v214, v82, 4, v214\n"
      "v_lshl_add_u32 v188, v107, 4, v188\n"
      "s_waitcnt lgkmcnt(0)\n"
      "s_barrier\n"
      "ds_read_b128 v[164:167], v9 offset:4608\n"
      "ds_read_b128 v[180:183], v9 offset:5632\n"
      "v_bfe_i32 v136, v36, 10, 1\n"
      "v_bfe_i32 v137, v36, 11, 1\n"
      "v_bfe_i32 v107, v36, 12, 1\n"
      "v_bfe_i32 v163, v36, 13, 1\n"
      "v_bfe_i32 v191, v36, 14, 1\n"
      "v_bfe_i32 v202, v36, 15, 1\n"
      "v_bfe_i32 v82, v36, 16, 1\n"
      "v_bfe_i32 v189, v36, 17, 1\n"
      "v_bfe_i32 v125, v36, 18, 1\n"
      "v_bfe_i32 v190, v36, 19, 1\n"
      "ds_read_b128 v[204:207], v7 offset:18432\n"
      "ds_read_b128 v[144:147], v7 offset:22528\n"
      "ds_read_b128 v[148:151], v7 offset:26624\n"
      "s_bitcmp1_b32 s46, 31\n"
      "s_cselect_b32 s93, 100270464, 0\n"
      "v_and_b32 v136, s93, v136\n"
      "s_waitcnt lgkmcnt(4)\n"
      "v_add_f32 v164, v164, v166\n"
      "v_add_f32 v165, v165, v167\n"
      "v_and_b32 v107, s93, v107\n"
      "s_waitcnt lgkmcnt(3)\n"
      "v_add_f32 v180, v180, v182\n"
      "v_add_f32 v181, v181, v183\n"
      "v_add_f32 v164, v164, v180\n"
      "v_add_f32 v165, v165, v181\n"
      "v_fma_f32 v180, -v164, v164, s52\n"
      "v_fmac_f32 v180, 0x43400000, v165\n"
      "v_max_f32 v180, s52, v180\n"
      "v_rsq_f32 v180, v180\n"
      "v_and_b32 v191, s93, v191\n"
      "v_mul_f32 v165, 0x43400000, v180\n"
      "v_mul_f32 v166, -v164, v180\n"
      "v_mov_b32 v164, v165\n"
      "v_mov_b32 v167, v166\n"
      "v_and_b32 v82, s93, v82\n"
      "v_permlane32_swap_b32 v165, v164\n"
      "v_permlane32_swap_b32 v166, v167\n"
      "v_mov_b32 v167, v165\n"
      "v_mov_b32 v164, v166\n"
      "v_and_b32 v125, s93, v125\n"
      "v_permlane16_swap_b32 v165, v167\n"
      "v_permlane16_swap_b32 v166, v164\n"
      "v_fma_f32 v102, v102, v165, v166\n"
      "v_fma_f32 v103, v103, v165, v166\n"
      "v_fma_f32 v174, v174, v167, v164\n"
      "v_fma_f32 v175, v175, v167, v164\n"
      "v_cvt_pk_bf16_f32 v102, v102, v103\n"
      "v_fma_f32 v104, v104, v165, v166\n"
      "v_fma_f32 v105, v105, v165, v166\n"
      "v_fma_f32 v194, v194, v165, v166\n"
      "v_fma_f32 v195, v195, v165, v166\n"
      "v_cvt_pk_bf16_f32 v174, v174, v175\n"
      "v_fma_f32 v176, v176, v167, v164\n"
      "v_fma_f32 v177, v177, v167, v164\n"
      "v_fma_f32 v120, v120, v167, v164\n"
      "v_fma_f32 v121, v121, v167, v164\n"
      "v_cvt_pk_bf16_f32 v103, v104, v105\n"
      "v_fma_f32 v96, v96, v165, v166\n"
      "v_fma_f32 v97, v97, v165, v166\n"
      "v_fma_f32 v98, v98, v165, v166\n"
      "v_fma_f32 v99, v99, v165, v166\n"
      "v_cvt_pk_bf16_f32 v194, v194, v195\n"
      "v_fma_f32 v196, v196, v165, v166\n"
      "v_fma_f32 v197, v197, v165, v166\n"
      "v_cvt_pk_bf16_f32 v175, v176, v177\n"
      "v_fma_f32 v210, v210, v167, v164\n"
      "v_fma_f32 v211, v211, v167, v164\n"
      "v_fma_f32 v212, v212, v167, v164\n"
      "v_fma_f32 v213, v213, v167, v164\n"
      "v_cvt_pk_bf16_f32 v120, v120, v121\n"
      "v_fma_f32 v122, v122, v167, v164\n"
      "v_fma_f32 v123, v123, v167, v164\n"
      "v_cvt_pk_bf16_f32 v104, v96, v97\n"
      "v_cvt_pk_bf16_f32 v105, v98, v99\n"
      "v_cvt_pk_bf16_f32 v195, v196, v197\n"
      "v_cvt_pk_bf16_f32 v176, v210, v211\n"
      "v_cvt_pk_bf16_f32 v177, v212, v213\n"
      "v_cvt_pk_bf16_f32 v121, v122, v123\n"
      "ds_write_b128 v81, v[102:105]\n"
      "ds_write_b64 v78, v[194:195]\n"
      "ds_write_b128 v81, v[174:177] offset:6144\n"
      "ds_write_b64 v78, v[120:121] offset:6144\n"
      "s_bitcmp1_b32 s46, 30\n"
      "s_cselect_b32 s94, 196224, 0\n"
      "ds_read_b128 v[164:167], v7 offset:30720\n"
      "s_and_b32 s92, s46, 0x3fffffff\n"
      "ds_read_b128 v[96:99], v7 offset:34816\n"
      "v_and_b32 v137, s94, v137\n"
      "v_add_u32 v136, v136, v137\n"
      "v_sub_u32 v136, s92, v136\n"
      "v_add_u32 v136, v20, v136\n"
      "s_waitcnt lgkmcnt(5)\n"
      "buffer_store_dwordx4 v[204:207], v136, s[20:23], 0 offen sc1\n"
      "v_and_b32 v163, s94, v163\n"
      "v_add_u32 v107, v107, v163\n"
      "v_sub_u32 v107, s92, v107\n"
      "v_add_u32 v107, v21, v107\n"
      "buffer_store_dwordx4 v[144:147], v107, s[20:23], 0 offen sc1\n"
      "v_and_b32 v202, s94, v202\n"
      "v_add_u32 v191, v191, v202\n"
      "v_sub_u32 v191, s92, v191\n"
      "v_add_u32 v191, v22, v191\n"
      "buffer_store_dwordx4 v[148:151], v191, s[20:23], 0 offen sc1\n"
      "v_and_b32 v189, s94, v189\n"
      "v_add_u32 v82, v82, v189\n"
      "v_sub_u32 v82, s92, v82\n"
      "v_add_u32 v82, v23, v82\n"
      "s_waitcnt lgkmcnt(0)\n"
      "buffer_store_dwordx4 v[164:167], v82, s[20:23], 0 offen sc1\n"
      "v_and_b32 v190, s94, v190\n"
      "v_add_u32 v125, v125, v190\n"
      "v_sub_u32 v125, s92, v125\n"
      "v_add_u32_e64 v125, v25, v125\n"
      "buffer_store_dwordx4 v[96:99], v125, s[20:23], 0 offen sc1\n"
      "s_waitcnt lgkmcnt(0)\n"
      "s_barrier\n"
      "ds_read_b128 v[146:149], v100\n"
      "ds_read_b128 v[120:123], v100 offset:1024\n"
      "ds_read_b128 v[96:99], v100 offset:2048\n"
      "s_waitcnt lgkmcnt(2)\n"
      "buffer_store_dwordx4 v[146:149], v203, s[20:23], 0 offen sc1\n"
      "s_waitcnt lgkmcnt(1)\n"
      "buffer_store_dwordx4 v[120:123], v214, s[20:23], 0 offen sc1\n"
      "s_waitcnt lgkmcnt(0)\n"
      "buffer_store_dwordx4 v[96:99], v188, s[20:23], 0 offen sc1\n"
      "s_endpgm\n"
      "L_rare_1_%=:\n"
      "s_nop 7\n"
      "s_waitcnt lgkmcnt(0)\n"
      "s_add_u32 s72, s42, 1\n"
      "v_readlane_b32 s77, v38, s72\n"
      "v_subrev_u32 v188, 115456, v9\n"
      "s_and_b32 s73, s77, -16\n"
      "s_mov_b32 m0, 110592\n"
      "s_nop 0\n"
      "buffer_load_dwordx4 v188, s[24:27], s73 offen  lds\n"
      "s_add_u32 s73, s73, 1024\n"
      "s_mov_b32 m0, 111616\n"
      "s_nop 0\n"
      "buffer_load_dwordx4 v188, s[24:27], s73 offen  lds\n"
      "s_add_u32 s73, s73, 1024\n"
      "s_mov_b32 m0, 112640\n"
      "s_nop 0\n"
      "buffer_load_dwordx4 v188, s[24:27], s73 offen  lds\n"
      "s_add_u32 s73, s73, 1024\n"
      "s_mov_b32 m0, 113664\n"
      "s_nop 0\n"
      "buffer_load_dwordx4 v188, s[24:27], s73 offen  lds\n"
      "s_add_u32 s73, s73, 1024\n"
      "s_mov_b32 m0, 114688\n"
      "s_mov_b32 exec_hi, 0xffff\n"
      "buffer_load_dwordx4 v188, s[24:27], s73 offen  lds\n"
      "s_mov_b32 exec_hi, -1\n"
      "s_add_u32 s73, s73, 1024\n"
      "s_and_b32 s77, s77, 15\n"
      "s_waitcnt lgkmcnt(0)\n"
      "s_nop 4\n"
      "s_branch L_back_2_%=\n"
      "L_rare_3_%=:\n"
      "s_nop 7\n"
      "s_waitcnt lgkmcnt(0)\n"
      "s_waitcnt vmcnt(0)\n"
      "v_subrev_u32 v105, 115456, v9\n"
      "v_bfe_u32 v166, v105, 4, 4\n"
      "v_mul_u32_u24 v166, 98, v166\n"
      "v_lshrrev_b32 v105, 8, v105\n"
      "v_lshl_add_u32 v166, v105, 3, v166\n"
      "v_add_u32 v166, s77, v166\n"
      "v_and_b32 v167, 2, v166\n"
      "v_and_b32 v166, -4, v166\n"
      "v_add_u32 v166, 110592, v166\n"
      "ds_read2_b32 v[188:189], v166 offset0:0 offset1:1\n"
      "ds_read2_b32 v[190:191], v166 offset0:8 offset1:9\n"
      "ds_read2_b32 v[202:203], v166 offset0:16 offset1:17\n"
      "ds_read2_b32 v[204:205], v166 offset0:2 offset1:10\n"
      "ds_read2_b32 v[168:169], v166 offset0:18 offset1:24\n"
      "s_waitcnt lgkmcnt(4)\n"
      "v_alignbyte_b32 v188, v189, v188, v167\n"
      "s_waitcnt lgkmcnt(1)\n"
      "v_alignbyte_b32 v189, v204, v189, v167\n"
      "v_lshlrev_b32 v105, 16, v188\n"
      "v_fmac_f32 v140, s53, v105\n"
      "v_and_b32 v105, 0xffff0000, v188\n"
      "v_fmac_f32 v141, s53, v105\n"
      "v_lshlrev_b32 v105, 16, v189\n"
      "v_fmac_f32_e64 v142, s53, v105\n"
      "v_and_b32 v105, 0xffff0000, v189\n"
      "v_fmac_f32 v143, s53, v105\n"
      "v_alignbyte_b32 v190, v191, v190, v167\n"
      "v_alignbyte_b32 v191, v205, v191, v167\n"
      "v_lshlrev_b32 v105, 16, v190\n"
      "v_fmac_f32 v218, s53, v105\n"
      "v_and_b32 v105, 0xffff0000, v190\n"
      "v_fmac_f32 v219, s53, v105\n"
      "v_lshlrev_b32 v105, 16, v191\n"
      "v_fmac_f32 v220, s53, v105\n"
      "v_and_b32 v105, 0xffff0000, v191\n"
      "v_fmac_f32 v221, s53, v105\n"
      "v_alignbyte_b32 v202, v203, v202, v167\n"
      "s_waitcnt lgkmcnt(0)\n"
      "v_alignbyte_b32 v203, v168, v203, v167\n"
      "v_lshlrev_b32 v105, 16, v202\n"
      "v_fmac_f32 v124, s53, v105\n"
      "v_and_b32 v105, 0xffff0000, v202\n"
      "v_fmac_f32 v125, s53, v105\n"
      "v_lshlrev_b32 v105, 16, v203\n"
      "v_fmac_f32 v126, s53, v105\n"
      "v_and_b32 v105, 0xffff0000, v203\n"
      "v_fmac_f32 v127, s53, v105\n"
      "v_alignbyte_b32 v105, v169, v169, v167\n"
      "v_lshlrev_b32 v105, 16, v105\n"
      "v_fmac_f32 v194, s53, v105\n"
      "v_max3_f32 v166, v140, v141, v142\n"
      "v_max3_f32 v167, v143, v218, v219\n"
      "v_max3_f32 v166, v166, v220, v221\n"
      "v_max3_f32 v167, v167, v124, v125\n"
      "v_max3_f32 v166, v166, v126, v127\n"
      "v_max3_f32 v166, v166, v167, v194\n"
      "v_mov_b32 v105, v166\n"
      "s_nop 1\n"
      "v_permlane16_swap_b32 v166, v105\n"
      "v_max_f32 v166, v166, v105\n"
      "v_mov_b32 v105, v166\n"
      "s_nop 1\n"
      "v_permlane32_swap_b32 v166, v105\n"
      "v_max_f32 v166, v166, v105\n"
      "v_sub_f32 v140, v140, v166\n"
      "v_sub_f32 v141, v141, v166\n"
      "v_sub_f32 v142, v142, v166\n"
      "v_sub_f32 v143, v143, v166\n"
      "v_sub_f32 v218, v218, v166\n"
      "v_sub_f32 v219, v219, v166\n"
      "v_sub_f32 v220, v220, v166\n"
      "v_sub_f32 v221, v221, v166\n"
      "v_sub_f32 v124, v124, v166\n"
      "v_sub_f32 v125, v125, v166\n"
      "v_sub_f32 v126, v126, v166\n"
      "v_sub_f32 v127, v127, v166\n"
      "v_sub_f32_e64 v194, v194, v166\n"
      "s_waitcnt lgkmcnt(0)\n"
      "s_nop 4\n"
      "s_branch L_back_4_%=\n"
      "L_rare_5_%=:\n"
      "s_nop 7\n"
      "s_waitcnt lgkmcnt(0)\n"
      "s_waitcnt vmcnt(0)\n"
      "v_subrev_u32 v101, 115456, v9\n"
      "v_bfe_u32 v194, v101, 4, 4\n"
      "v_mul_u32_u24 v194, 98, v194\n"
      "v_lshrrev_b32 v101, 8, v101\n"
      "v_lshl_add_u32 v194, v101, 3, v194\n"
      "v_add_u32 v194, s77, v194\n"
      "v_and_b32 v100, 2, v194\n"
      "v_and_b32 v194, -4, v194\n"
      "v_add_u32 v194, 112160, v194\n"
      "ds_read2_b32 v[126:127], v194 offset0:0 offset1:1\n"
      "ds_read2_b32 v[124:125], v194 offset0:8 offset1:9\n"
      "ds_read2_b32 v[220:221], v194 offset0:16 offset1:17\n"
      "ds_read2_b32 v[218:219], v194 offset0:2 offset1:10\n"
      "ds_read2_b32 v[108:109], v194 offset0:18 offset1:24\n"
      "s_waitcnt lgkmcnt(4)\n"
      "v_alignbyte_b32 v126, v127, v126, v100\n"
      "s_waitcnt lgkmcnt(1)\n"
      "v_alignbyte_b32 v127, v218, v127, v100\n"
      "v_lshlrev_b32 v101, 16, v126\n"
      "v_fmac_f32 v188, s53, v101\n"
      "v_and_b32 v101, 0xffff0000, v126\n"
      "v_fmac_f32 v189, s53, v101\n"
      "v_lshlrev_b32 v101, 16, v127\n"
      "v_fmac_f32 v190, s53, v101\n"
      "v_and_b32 v101, 0xffff0000, v127\n"
      "v_fmac_f32 v191, s53, v101\n"
      "v_alignbyte_b32 v124, v125, v124, v100\n"
      "v_alignbyte_b32 v125, v219, v125, v100\n"
      "v_lshlrev_b32 v101, 16, v124\n"
      "v_fmac_f32 v144, s53, v101\n"
      "v_and_b32 v101, 0xffff0000, v124\n"
      "v_fmac_f32 v145, s53, v101\n"
      "v_lshlrev_b32 v101, 16, v125\n"
      "v_fmac_f32_e64 v146, s53, v101\n"
      "v_and_b32 v101, 0xffff0000, v125\n"
      "v_fmac_f32 v147, s53, v101\n"
      "v_alignbyte_b32 v220, v221, v220, v100\n"
      "s_waitcnt lgkmcnt(0)\n"
      "v_alignbyte_b32 v221, v108, v221, v100\n"
      "v_lshlrev_b32 v101, 16, v220\n"
      "v_fmac_f32 v166, s53, v101\n"
      "v_and_b32 v101, 0xffff0000, v220\n"
      "v_fmac_f32 v167, s53, v101\n"
      "v_lshlrev_b32 v101, 16, v221\n"
      "v_fmac_f32_e64 v168, s53, v101\n"
      "v_and_b32 v101, 0xffff0000, v221\n"
      "v_fmac_f32 v169, s53, v101\n"
      "v_alignbyte_b32 v101, v109, v109, v100\n"
      "v_lshlrev_b32 v101, 16, v101\n"
      "v_fmac_f32_e64 v202, s53, v101\n"
      "v_max3_f32 v194, v188, v189, v190\n"
      "v_max3_f32 v100, v191, v144, v145\n"
      "v_max3_f32 v194, v194, v146, v147\n"
      "v_max3_f32 v100, v100, v166, v167\n"
      "v_max3_f32 v194, v194, v168, v169\n"
      "v_max3_f32 v194, v194, v100, v202\n"
      "v_mov_b32 v101, v194\n"
      "s_nop 1\n"
      "v_permlane16_swap_b32 v194, v101\n"
      "v_max_f32 v194, v194, v101\n"
      "v_mov_b32 v101, v194\n"
      "s_nop 1\n"
      "v_permlane32_swap_b32 v194, v101\n"
      "v_max_f32 v194, v194, v101\n"
      "v_sub_f32 v188, v188, v194\n"
      "v_sub_f32 v189, v189, v194\n"
      "v_sub_f32 v190, v190, v194\n"
      "v_sub_f32 v191, v191, v194\n"
      "v_sub_f32 v144, v144, v194\n"
      "v_sub_f32 v145, v145, v194\n"
      "v_sub_f32 v146, v146, v194\n"
      "v_sub_f32 v147, v147, v194\n"
      "v_sub_f32 v166, v166, v194\n"
      "v_sub_f32 v167, v167, v194\n"
      "v_sub_f32 v168, v168, v194\n"
      "v_sub_f32 v169, v169, v194\n"
      "v_sub_f32_e64 v202, v202, v194\n"
      "s_waitcnt lgkmcnt(0)\n"
      "s_nop 4\n"
      "s_branch L_back_6_%=\n"
      "L_rare_7_%=:\n"
      "s_nop 7\n"
      "s_waitcnt lgkmcnt(0)\n"
      "s_waitcnt vmcnt(0)\n"
      "v_subrev_u32 v149, 115456, v9\n"
      "v_bfe_u32 v204, v149, 4, 4\n"
      "v_mul_u32_u24 v204, 98, v204\n"
      "v_lshrrev_b32_e64 v149, 8, v149\n"
      "v_lshl_add_u32 v204, v149, 3, v204\n"
      "v_add_u32 v204, s77, v204\n"
      "v_and_b32 v205, 2, v204\n"
      "v_and_b32_e64 v204, -4, v204\n"
      "v_add_u32 v204, 113728, v204\n"
      "ds_read2_b32 v[174:175], v204 offset0:0 offset1:1\n"
      "ds_read2_b32 v[176:177], v204 offset0:8 offset1:9\n"
      "ds_read2_b32 v[168:169], v204 offset0:16 offset1:17\n"
      "ds_read2_b32 v[166:167], v204 offset0:2 offset1:10\n"
      "ds_read2_b32 v[202:203], v204 offset0:18 offset1:24\n"
      "s_waitcnt lgkmcnt(4)\n"
      "v_alignbyte_b32 v174, v175, v174, v205\n"
      "s_waitcnt lgkmcnt(1)\n"
      "v_alignbyte_b32 v175, v166, v175, v205\n"
      "v_lshlrev_b32 v149, 16, v174\n"
      "v_fmac_f32 v140, s53, v149\n"
      "v_and_b32 v149, 0xffff0000, v174\n"
      "v_fmac_f32 v141, s53, v149\n"
      "v_lshlrev_b32 v149, 16, v175\n"
      "v_fmac_f32_e64 v142, s53, v149\n"
      "v_and_b32 v149, 0xffff0000, v175\n"
      "v_fmac_f32 v143, s53, v149\n"
      "v_alignbyte_b32 v176, v177, v176, v205\n"
      "v_alignbyte_b32 v177, v167, v177, v205\n"
      "v_lshlrev_b32 v149, 16, v176\n"
      "v_fmac_f32 v108, s53, v149\n"
      "v_and_b32 v149, 0xffff0000, v176\n"
      "v_fmac_f32 v109, s53, v149\n"
      "v_lshlrev_b32 v149, 16, v177\n"
      "v_fmac_f32 v110, s53, v149\n"
      "v_and_b32 v149, 0xffff0000, v177\n"
      "v_fmac_f32 v111, s53, v149\n"
      "v_alignbyte_b32 v168, v169, v168, v205\n"
      "s_waitcnt lgkmcnt(0)\n"
      "v_alignbyte_b32 v169, v202, v169, v205\n"
      "v_lshlrev_b32 v149, 16, v168\n"
      "v_fmac_f32 v120, s53, v149\n"
      "v_and_b32 v149, 0xffff0000, v168\n"
      "v_fmac_f32 v121, s53, v149\n"
      "v_lshlrev_b32 v149, 16, v169\n"
      "v_fmac_f32 v122, s53, v149\n"
      "v_and_b32 v149, 0xffff0000, v169\n"
      "v_fmac_f32 v123, s53, v149\n"
      "v_alignbyte_b32 v149, v203, v203, v205\n"
      "v_lshlrev_b32 v149, 16, v149\n"
      "v_fmac_f32 v172, s53, v149\n"
      "v_max3_f32 v204, v140, v141, v142\n"
      "v_max3_f32 v205, v143, v108, v109\n"
      "v_max3_f32 v204, v204, v110, v111\n"
      "v_max3_f32 v205, v205, v120, v121\n"
      "v_max3_f32 v204, v204, v122, v123\n"
      "v_max3_f32 v204, v204, v205, v172\n"
      "v_mov_b32 v149, v204\n"
      "s_nop 1\n"
      "v_permlane16_swap_b32 v204, v149\n"
      "v_max_f32 v204, v204, v149\n"
      "v_mov_b32 v149, v204\n"
      "s_nop 1\n"
      "v_permlane32_swap_b32 v204, v149\n"
      "v_max_f32 v204, v204, v149\n"
      "v_sub_f32 v140, v140, v204\n"
      "v_sub_f32 v141, v141, v204\n"
      "v_sub_f32 v142, v142, v204\n"
      "v_sub_f32 v143, v143, v204\n"
      "v_sub_f32 v108, v108, v204\n"
      "v_sub_f32 v109, v109, v204\n"
      "v_sub_f32 v110, v110, v204\n"
      "v_sub_f32 v111, v111, v204\n"
      "v_sub_f32 v120, v120, v204\n"
      "v_sub_f32 v121, v121, v204\n"
      "v_sub_f32 v122, v122, v204\n"
      "v_sub_f32 v123, v123, v204\n"
      "v_sub_f32 v172, v172, v204\n"
      "s_waitcnt lgkmcnt(0)\n"
      "s_nop 4\n"
      "s_branch L_back_8_%=\n"
      "L_rare_9_%=:\n"
      "s_nop 7\n"
      "s_waitcnt lgkmcnt(0)\n"
      "s_waitcnt vmcnt(0)\n"
      "v_subrev_u32 v174, 115456, v9\n"
      "v_lshrrev_b32 v174, 3, v174\n"
      "v_min_u32 v174, 96, v174\n"
      "v_add_u32 v174, s77, v174\n"
      "v_add_u32 v174, 115296, v174\n"
      "ds_read_u16 v174, v174\n"
      "s_waitcnt lgkmcnt(0)\n"
      "v_lshlrev_b32_e64 v174, 16, v174\n"
      "v_fmac_f32 v188, s53, v174\n"
      "s_nop 1\n"
      "v_max_f32_dpp v177, v188, v188 row_ror:8 row_mask:0xf bank_mask:0xf\n"
      "s_nop 1\n"
      "v_max_f32_dpp v177, v177, v177 row_ror:4 row_mask:0xf bank_mask:0xf\n"
      "s_nop 1\n"
      "v_max_f32_dpp v177, v177, v177 row_ror:2 row_mask:0xf bank_mask:0xf\n"
      "s_nop 1\n"
      "v_max_f32_dpp v177, v177, v177 row_ror:1 row_mask:0xf bank_mask:0xf\n"
      "s_nop 1\n"
      "v_max_f32_dpp v177, v177, v177 row_bcast:15 row_mask:0xa bank_mask:0xf\n"
      "s_nop 1\n"
      "v_max_f32_dpp v177, v177, v177 row_bcast:31 row_mask:0xc bank_mask:0xf\n"
      "s_nop 0\n"
      "v_readlane_b32 s72, v177, 63\n"
      "s_nop 1\n"
      "v_subrev_f32 v188, s72, v188\n"
      "s_waitcnt lgkmcnt(0)\n"
      "s_nop 4\n"
      "s_branch L_back_10_%=\n"
      "L_cend_%=:\n"
      :
      : "s"(__builtin_amdgcn_kernarg_segment_ptr()), "s"(wave), "s"(blockIdx.x), "s"(lds0)
      : "v0","v1","v2","v3","v4","v5","v6","v7","v8","v9","v10","v11","v12","v13","v14","v15","v16","v17","v18","v19","v20","v21","v22","v23","v24","v25","v26","v27","v28","v29","v30","v31","v32","v33","v34","v35","v36","v37","v38","v39","v40","v41","v42","v43","v44","v45","v46","v47","v48","v49","v50","v51","v52","v53","v54","v55","v56","v57","v58","v59","v60","v61","v62","v63","v64","v65","v66","v67","v68","v69","v70","v71","v72","v73","v74","v75","v76","v77","v78","v79","v80","v81","v82","v83","v84","v85","v86","v87","v88","v89","v90","v91","v92","v93","v94","v95","v96","v97","v98","v99","v100","v101","v102","v103","v104","v105","v106","v107","v108","v109","v110","v111","v112","v113","v114","v115","v116","v117","v118","v119","v120","v121","v122","v123","v124","v125","v126","v127","v128","v129","v130","v131","v132","v133","v134","v135","v136","v137","v138","v139","v140","v141","v142","v143","v144","v145","v146","v147","v148","v149","v150","v151","v152","v153","v154","v155","v156","v157","v158","v159","v160","v161","v162","v163","v164","v165","v166","v167","v168","v169","v170","v171","v172","v173","v174","v175","v176","v177","v178","v179","v180","v181","v182","v183","v184","v185","v186","v187","v188","v189","v190","v191","v192","v193","v194","v195","v196","v197","v198","v199","v200","v201","v202","v203","v204","v205","v206","v207","v208","v209","v210","v211","v212","v213","v214","v215","v216","v217","v218","v219","v220","v221","v222","v223","v224","v225","v226","v227","v228","v229","v230","v231","v232","v233","v234","v235","v236","v237","v238","v239","v240","v241","v242","v243","v244","v245","v246","v247","v248","v249","v250","v251","v252","v253","v254","v255","a0","a1","a2","a3","a4","a5","a6","a7","a8","a9","a10","a11","a12","a13","a14","a15","a16","a17","a18","a19","a20","a21","a22","a23","a24","a25","a26","a27","a28","a29","a30","a31","a32","a33","a34","a35","a36","a37","a38","a39","a40","a41","a42","a43","a44","a45","a46","a47","a48","a49","a50","a51","a52","a53","a54","a55","a56","a57","a58","a59","a60","a61","a62","a63","a64","a65","a66","a67","a68","a69","a70","a71","a72","a73","a74","a75","a76","a77","a78","a79","a80","a81","a82","a83","a84","a85","a86","a87","a88","a89","a90","a91","a92","a93","a94","a95","a96","a97","a98","a99","a100","a101","a102","a103","a104","a105","a106","a107","a108","a109","a110","a111","a112","a113","a114","a115","a116","a117","a118","a119","a120","a121","a122","a123","a124","a125","a126","a127","a128","a129","a130","a131","a132","a133","a134","a135","a136","a137","a138","a139","a140","a141","a142","a143","a144","a145","a146","a147","a148","a149","a150","a151","a152","a153","a154","a155","a156","a157","a158","a159","a160","a161","a162","a163","a164","a165","a166","a167","a168","a169","a170","a171","a172","a173","a174","a175","a176","a177","a178","a179","a180","a181","a182","a183","a184","a185","a186","a187","a188","a189","a190","a191","a192","a193","a194","a195","a196","a197","a198","a199","a200","a201","a202","a203","a204","a205","a206","a207","a208","a209","a210","a211","a212","a213","a214","a215","a216","a217","a218","a219","a220","a221","a222","a223","a224","a225","a226","a227","a228","a229","a230","a231","a232","a233","a234","a235","a236","a237","a238","a239","a240","a241","a242","a243","a244","a245","a246","a247","a248","a249","a250","a251","a252","a253","a254","a255","s16","s17","s18","s19","s20","s21","s22","s23","s24","s25","s26","s27","s28","s29","s30","s31","s33","s34","s35","s36","s37","s38","s39","s40","s41","s42","s43","s44","s45","s46","s47","s48","s49","s50","s51","s52","s53","s54","s55","s56","s57","s58","s59","s60","s61","s62","s63","s64","s65","s66","s67","s68","s69","s70","s71","s72","s73","s74","s75","s76","s77","s78","s79","s80","s81","s82","s83","s84","s85","s86","s87","s88","s89","s90","s91","s92","s93","s94","s95","s96","s97","s98","s99","s100","s101", "vcc", "scc", "memory");
}

}

extern "C" void kernel_launch(void* const* d_in, const int* in_sizes, int n_in, void* d_out, int out_size, void* d_ws,
                              size_t ws_size, hipStream_t stream) {
  const uint16_t* x = (const uint16_t*)d_in[0];
  const uint16_t* wqkv = (const uint16_t*)d_in[1];
  const uint16_t* wo = (const uint16_t*)d_in[2];
  const uint16_t* ls = (const uint16_t*)d_in[3];
  const uint16_t* w1 = (const uint16_t*)d_in[4];
  const uint16_t* w2 = (const uint16_t*)d_in[5];
  const uint16_t* rc = (const uint16_t*)d_in[6];
  const int* ridx = (const int*)d_in[7];
  const uint16_t* mask = (const uint16_t*)d_in[8];
  swin::swin_main<<<256, 256, 0, stream>>>(x, mask, (char*)d_ws, (uint16_t*)d_out, wqkv, wo, ls, w1, w2, rc, ridx);
}
```

```cpp
#include <hip/hip_runtime.h>
#include <hip/hip_bf16.h>
#include <stdint.h>

namespace swin {

constexpr unsigned LDS_BYTES = 163840u;

__global__ __launch_bounds__(256, 1) void swin_main(const uint16_t* x, const uint16_t* mask, char* ws, uint16_t* out,
                                                    const uint16_t* wqkv, const uint16_t* wo, const uint16_t* ls,
                                                    const uint16_t* w1, const uint16_t* w2, const uint16_t* rc,
                                                    const int* ridx) {
  __shared__ __attribute__((aligned(16))) char smem[LDS_BYTES];
  const int wave = __builtin_amdgcn_readfirstlane(threadIdx.x >> 6);
  const unsigned lds0 = (unsigned)(size_t)smem;
  asm volatile(
      ".p2align 5\n"
      "s_mov_b64 s[88:89], %0\n"
      "s_mov_b32 s40, %1\n"
      "s_mov_b32 s41, %2\n"
      "s_load_dwordx2 s[16:17], s[88:89], 0\n"
      "s_load_dwordx2 s[24:25], s[88:89], 8\n"
      "s_load_dwordx2 s[34:35], s[88:89], 16\n"
      "s_load_dwordx2 s[20:21], s[88:89], 24\n"
      "s_load_dwordx2 s[28:29], s[88:89], 32\n"
      "s_load_dwordx2 s[36:37], s[88:89], 40\n"
      "s_load_dwordx8 s[80:87], s[88:89], 48\n"
      "s_load_dwordx2 s[90:91], s[88:89], 80\n"
      "s_getpc_b64 s[98:99]\n"
      "L_ipf0_%=:\n"
      "v_mbcnt_lo_u32_b32 v44, -1, 0\n"
      "v_mbcnt_hi_u32_b32 v44, -1, v44\n"
      "v_lshlrev_b32 v41, 2, v44\n"
      "v_lshlrev_b32_e64 v48, 4, v44\n"
      "s_cmp_lt_u32 s41, 209\n"
      "s_cselect_b32 s44, 20, 19\n"
      "s_mov_b32 s18, 100270464\n"
      "s_mov_b32 s19, 0x20000\n"
      "s_mov_b32 s22, 100270464\n"
      "s_mov_b32 s23, 0x20000\n"
      "s_mov_b32 s26, 25589860\n"
      "s_mov_b32 s27, 0x20000\n"
      "s_mov_b32 s30, 221184\n"
      "s_mov_b32 s31, 0x20000\n"
      "s_mov_b32 s38, 73728\n"
      "s_mov_b32 s39, 0x20000\n"
      "s_lshl_b32 s64, s40, 10\n"
      "s_cmp_lt_u32 s40, 2\n"
      "s_cselect_b32 s65, s64, 163840\n"
      "v_add_u32 v27, s64, v48\n"
      "s_lshl_b32 s92, s40, 8\n"
      "v_add_u32_e64 v42, s92, v41\n"
      "v_min_u32 v29, 704, v42\n"
      "v_add_u32 v29, 4096, v29\n"
      "v_min_u32 v42, 672, v42\n"
      "s_mul_i32 s75, s40, 96\n"
      "s_and_b32 s92, s40, 1\n"
      "s_lshl_b32 s92, s92, 2\n"
      "s_lshl_b32 s93, s40, 3\n"
      "v_lshrrev_b32 v132, 3, v44\n"
      "v_and_b32 v51, 2, v132\n"
      "v_and_b32 v40, 7, v44\n"
      "v_xor_b32 v51, v51, v40\n"
      "v_xor_b32 v51, s92, v51\n"
      "v_add_u32 v132, s93, v132\n"
      "v_lshlrev_b32 v51, 4, v51\n"
      "v_mul_u32_u24 v176, 384, v132\n"
      "v_mul_u32_u24 v132, 1152, v132\n"
      "v_add_u32 v132, v132, v51\n"
      "v_add_u32 v176, v176, v51\n"
      "s_add_u32 s33, s64, 18432\n"
      "v_add_u32 v40, 122112, v42\n"
      "s_waitcnt lgkmcnt(0)\n"
      "s_lshr_b32 s92, s41, 3\n"
      "s_mul_i32 s93, s92, 3456\n"
      "s_lshl_b32 s94, s40, 1\n"
      "s_mov_b64 s[72:73], s[28:29]\n"
      "s_movk_i32 s93, 27\n"
      "s_mov_b32 s94, 221056\n"
      "s_cmp_eq_u32 s40, 1\n"
      "s_cselect_b32 s95, 32, 0\n"
      "s_add_u32 s92, s92, s95\n"
      "s_cmp_eq_u32 s40, 2\n"
      "s_cselect_b64 s[72:73], s[36:37], s[72:73]\n"
      "s_cselect_b32 s93, 18, s93\n"
      "s_cselect_b32 s94, 73600, s94\n"
      "s_cmp_eq_u32 s40, 3\n"
      "s_cselect_b64 s[72:73], s[98:99], s[72:73]\n"
      "s_cselect_b32 s93, 20, s93\n"
      "s_cselect_b32 s94, L_cend_%=-L_ipf0_%=-128, s94\n"
      "s_mul_i32 s92, s92, s93\n"
      "s_lshl_b32 s92, s92, 7\n"
      "s_sub_u32 s93, s93, 1\n"
      "s_lshl_b32 s93, s93, 7\n"
      "v_lshlrev_b32 v51, 3, v48\n"
      "v_min_u32 v51, s93, v51\n"
      "v_add_u32 v51, s92, v51\n"
      "v_min_u32 v51, s94, v51\n"
      "global_load_dword a255, v51, s[72:73]\n"
      "global_load_dword v42, v42, s[86:87]\n"
      "s_load_dwordx16 s[0:15], s[82:83], 0\n"
      "s_load_dwordx16 s[48:63], s[84:85], 0\n"
      "s_load_dwordx2 s[66:67], s[80:81], 0\n"
      "s_mov_b32 s68, s90\n"
      "s_mov_b32 s69, s91\n"
      "s_movk_i32 s70, 9604\n"
      "s_mov_b32 s71, 0x20000\n"
      "s_add_u32 m0, s64, 92160\n"
      "s_movk_i32 s92, 0\n"
      "buffer_load_dwordx4 v27, s[68:71], s92 offen  lds\n"
      "s_add_u32 m0, s64, 96256\n"
      "s_movk_i32 s92, 4096\n"
      "buffer_load_dwordx4 v27, s[68:71], s92 offen  lds\n"
      "s_add_u32 m0, s64, 100352\n"
      "s_cmp_lt_u32 s40, 2\n"
      "s_cselect_b32 m0, m0, s65\n"
      "s_movk_i32 s92, 8192\n"
      "buffer_load_dwordx4 v27, s[68:71], s92 offen  lds\n"
      "s_lshl_b32 s95, s40, 3\n"
      "v_lshrrev_b32 v51, 3, v44\n"
      "v_and_b32 v47, 7, v44\n"
      "v_add_u32 v43, s95, v51\n"
      "v_bfe_u32 v45, v43, 1, 3\n"
      "v_cmp_lt_u32 s[72:73], s44, v43\n"
      "v_min_u32 v43, s44, v43\n"
      "s_movk_i32 s93, 4180\n"
      "s_cmp_lt_u32 s41, 209\n"
      "s_cselect_b32 s92, 209, 47\n"
      "s_cselect_b32 s93, 795, s93\n"
      "s_cmp_lt_u32 s41, 252\n"
      "s_cselect_b32 s94, 16, 15\n"
      "s_add_u32 s93, s93, s41\n"
      "v_lshl_add_u32 v60, v43, 8, s41\n"
      "v_mul_u32_u24 v61, s92, v43\n"
      "v_add_u32 v61, s93, v61\n"
      "v_cmp_lt_u32 vcc, s94, v43\n"
      "v_subrev_u32 v64, 5184, v61\n"
      "s_nop 0\n"
      "v_cndmask_b32 v60, v60, v61, vcc\n"
      "v_cmp_le_u32 vcc, 5184, v60\n"
      "v_mul_u32_u24 v54, 58255, v60\n"
      "v_lshrrev_b32 v54, 22, v54\n"
      "v_mul_u32_u24 v46, 72, v54\n"
      "v_sub_u32 v46, v60, v46\n"
      "v_min_u32 v61, 72, v64\n"
      "v_cndmask_b32 v54, v54, v61, vcc\n"
      "v_subrev_u32 v61, 72, v64\n"
      "v_min_u32 v61, 72, v61\n"
      "v_cndmask_b32 v46, v46, v61, vcc\n"
      "v_mad_u32_u24 v61, v54, 7, 9\n"
      "v_subrev_u32 v64, 0x1ff, v61\n"
      "v_min_u32 v61, v61, v64\n"
      "v_mad_u32_u24 v54, v46, 7, 9\n"
      "v_subrev_u32 v46, 0x1ff, v54\n"
      "v_min_u32 v54, v54, v46\n"
      "v_mul_u32_u24 v61, 0x1ff, v61\n"
      "v_add_u32 v61, v61, v54\n"
      "v_mul_u32_u24 v64, 0x180, v61\n"
      "v_bfrev_b32 v43, 1\n"
      "v_cndmask_b32 v64, v64, v43, s[72:73]\n"
      "v_xor_b32 v45, v45, v47\n"
      "v_lshl_add_u32 v64, v45, 4, v64\n"
      "s_movk_i32 s95, 504\n"
      "s_add_u32 s92, s40, 0\n"
      "s_mul_i32 s93, s92, 43\n"
      "s_lshr_b32 s93, s93, 8\n"
      "s_mul_i32 s94, s93, 6\n"
      "s_sub_u32 s92, s92, s94\n"
      "s_lshl_b32 s92, s92, 3\n"
      "s_lshl_b32 s93, s93, 7\n"
      "v_add_u32_e64 v46, s92, v51\n"
      "v_bfe_u32 v45, v46, 1, 3\n"
      "v_xor_b32 v45, v45, v47\n"
      "v_lshl_add_u32 v45, v45, 4, s93\n"
      "v_mul_u32_u24 v43, 37, v46\n"
      "v_lshrrev_b32_e64 v43, 8, v43\n"
      "v_mad_u32_u24 v16, v43, s95, v46\n"
      "v_mul_u32_u24 v16, 384, v16\n"
      "v_add_u32 v16, v16, v45\n"
      "s_add_u32 s92, s40, 4\n"
      "s_mul_i32 s93, s92, 43\n"
      "s_lshr_b32 s93, s93, 8\n"
      "s_mul_i32 s94, s93, 6\n"
      "s_sub_u32 s92, s92, s94\n"
      "s_lshl_b32 s92, s92, 3\n"
      "s_lshl_b32 s93, s93, 7\n"
      "v_add_u32 v45, s92, v51\n"
      "v_bfe_u32 v61, v45, 1, 3\n"
      "v_xor_b32 v61, v61, v47\n"
      "v_lshl_add_u32 v61, v61, 4, s93\n"
      "v_mul_u32_u24 v60, 37, v45\n"
      "v_lshrrev_b32 v60, 8, v60\n"
      "v_mad_u32_u24 v17, v60, s95, v45\n"
      "v_mul_u32_u24 v17, 384, v17\n"
      "v_add_u32 v17, v17, v61\n"
      "s_add_u32 s92, s40, 8\n"
      "s_mul_i32 s93, s92, 43\n"
      "s_lshr_b32 s93, s93, 8\n"
      "s_mul_i32 s94, s93, 6\n"
      "s_sub_u32 s92, s92, s94\n"
      "s_lshl_b32 s92, s92, 3\n"
      "s_lshl_b32 s93, s93, 7\n"
      "v_add_u32 v54, s92, v51\n"
      "v_bfe_u32 v61, v54, 1, 3\n"
      "v_xor_b32 v61, v61, v47\n"
      "v_lshl_add_u32 v61, v61, 4, s93\n"
      "v_mul_u32_u24 v53, 37, v54\n"
      "v_lshrrev_b32 v53, 8, v53\n"
      "v_mad_u32_u24 v18, v53, s95, v54\n"
      "v_mul_u32_u24 v18, 384, v18\n"
      "v_add_u32 v18, v18, v61\n"
      "s_add_u32 s92, s40, 12\n"
      "s_mul_i32 s93, s92, 43\n"
      "s_lshr_b32 s93, s93, 8\n"
      "s_mul_i32 s94, s93, 6\n"
      "s_sub_u32 s92, s92, s94\n"
      "s_lshl_b32 s92, s92, 3\n"
      "s_lshl_b32 s93, s93, 7\n"
      "v_add_u32 v61, s92, v51\n"
      "v_bfe_u32 v57, v61, 1, 3\n"
      "v_xor_b32 v57, v57, v47\n"
      "v_lshl_add_u32 v57, v57, 4, s93\n"
      "v_mul_u32_u24 v63, 37, v61\n"
      "v_lshrrev_b32 v63, 8, v63\n"
      "v_mad_u32_u24 v19, v63, s95, v61\n"
      "v_mul_u32_u24 v19, 384, v19\n"
      "v_add_u32 v19, v19, v57\n"
      "s_add_u32 s92, s40, 16\n"
      "s_mul_i32 s93, s92, 43\n"
      "s_lshr_b32 s93, s93, 8\n"
      "s_mul_i32 s94, s93, 6\n"
      "s_sub_u32 s92, s92, s94\n"
      "s_lshl_b32 s92, s92, 3\n"
      "s_lshl_b32 s93, s93, 7\n"
      "s_cmp_lt_u32 s40, 2\n"
      "s_cselect_b32 s93, s93, 0x80000000\n"
      "v_add_u32 v57, s92, v51\n"
      "v_bfe_u32 v51, v57, 1, 3\n"
      "v_xor_b32_e64 v51, v51, v47\n"
      "v_lshl_add_u32 v51, v51, 4, s93\n"
      "v_mul_u32_u24 v50, 37, v57\n"
      "v_lshrrev_b32 v50, 8, v50\n"
      "v_mad_u32_u24 v15, v50, s95, v57\n"
      "v_mul_u32_u24 v15, 384, v15\n"
      "v_add_u32 v15, v15, v51\n"
      "s_add_u32 s92, s41, 0\n"
      "s_mul_i32 s93, s92, 58255\n"
      "s_lshr_b32 s93, s93, 22\n"
      "s_add_u32 s47, s92, s93\n"
      "s_mul_i32 s47, s47, 4802\n"
      "s_and_b32 s47, s47, -4\n"
      "s_mul_i32 s93, s93, 439\n"
      "s_add_u32 s45, s92, s93\n"
      "s_mul_i32 s45, s45, 2688\n"
      "s_add_u32 s45, s45, 589824\n"
      "s_add_u32 s92, s41, 256\n"
      "s_mul_i32 s93, s92, 58255\n"
      "s_lshr_b32 s93, s93, 22\n"
      "s_add_u32 s74, s92, s93\n"
      "s_mul_i32 s74, s74, 4802\n"
      "s_and_b32 s74, s74, -4\n"
      "s_mul_i32 s93, s93, 439\n"
      "s_add_u32 s46, s92, s93\n"
      "s_mul_i32 s46, s46, 2688\n"
      "s_add_u32 s46, s46, 589824\n"
      "s_add_u32 m0, s64, 149504\n"
      "s_nop 0\n"
      "buffer_load_dwordx4 v64, s[16:19], 0 offen  lds\n"
      "s_add_u32 m0, s64, 153472\n"
      "s_nop 0\n"
      "buffer_load_dwordx4 v64, s[16:19], 0 offen offset:128  lds\n"
      "s_add_u32 m0, s64, 157440\n"
      "s_nop 0\n"
      "buffer_load_dwordx4 v64, s[16:19], 0 offen offset:256  lds\n"
      "s_add_u32 m0, s64, 0\n"
      "s_nop 0\n"
      "buffer_load_dwordx4 v16, s[16:19], s45 offen  lds\n"
      "s_add_u32 m0, s64, 4096\n"
      "s_nop 0\n"
      "buffer_load_dwordx4 v17, s[16:19], s45 offen  lds\n"
      "s_add_u32 m0, s64, 8192\n"
      "s_nop 0\n"
      "buffer_load_dwordx4 v18, s[16:19], s45 offen  lds\n"
      "s_add_u32 m0, s64, 12288\n"
      "s_nop 0\n"
      "buffer_load_dwordx4 v19, s[16:19], s45 offen  lds\n"
      "s_add_u32 m0, s65, 16384\n"
      "s_nop 0\n"
      "buffer_load_dwordx4 v15, s[16:19], s45 offen  lds\n"
      "v_mul_u32_u24 v51, 7, v43\n"
      "v_sub_u32 v51, v46, v51\n"
      "v_lshrrev_b32 v51, 2, v51\n"
      "v_lshrrev_b32 v46, 2, v43\n"
      "v_lshl_or_b32 v51, v51, 1, v46\n"
      "v_mov_b32 v36, v51\n"
      "v_mul_u32_u24 v46, 7, v60\n"
      "v_sub_u32 v46, v45, v46\n"
      "v_lshrrev_b32 v46, 2, v46\n"
      "v_lshrrev_b32 v51, 2, v60\n"
      "v_lshl_or_b32 v46, v46, 1, v51\n"
      "v_lshl_or_b32 v36, v46, 2, v36\n"
      "v_mul_u32_u24 v46, 7, v53\n"
      "v_sub_u32 v46, v54, v46\n"
      "v_lshrrev_b32 v46, 2, v46\n"
      "v_lshrrev_b32 v43, 2, v53\n"
      "v_lshl_or_b32 v46, v46, 1, v43\n"
      "v_lshl_or_b32 v36, v46, 4, v36\n"
      "v_mul_u32_u24 v43, 7, v63\n"
      "v_sub_u32 v43, v61, v43\n"
      "v_lshrrev_b32 v43, 2, v43\n"
      "v_lshrrev_b32 v45, 2, v63\n"
      "v_lshl_or_b32 v43, v43, 1, v45\n"
      "v_lshl_or_b32 v36, v43, 6, v36\n"
      "v_mul_u32_u24 v46, 7, v50\n"
      "v_sub_u32 v46, v57, v46\n"
      "v_lshrrev_b32 v46, 2, v46\n"
      "v_lshrrev_b32 v43, 2, v50\n"
      "v_lshl_or_b32 v46, v46, 1, v43\n"
      "v_lshl_or_b32 v36, v46, 8, v36\n"
      "v_and_b32 v45, 15, v44\n"
      "v_lshrrev_b32 v43, 4, v44\n"
      "v_and_b32 v46, 3, v44\n"
      "v_bfe_u32 v51, v44, 2, 2\n"
      "s_mov_b32 s94, 18432\n"
      "v_and_b32 v50, 1, v43\n"
      "v_and_b32_e64 v54, 2, v51\n"
      "v_lshl_or_b32 v50, v50, 2, v54\n"
      "s_and_b32 s92, s40, 1\n"
      "s_mul_i32 s92, s92, 32\n"
      "s_add_u32 s92, s92, 0\n"
      "s_add_u32 s92, s92, s75\n"
      "v_lshl_add_u32 v54, v46, 3, s92\n"
      "v_lshrrev_b32 v203, 7, v54\n"
      "v_lshl_add_u32 v203, v203, 2, v43\n"
      "v_lshl_add_u32 v203, v203, 3, v51\n"
      "v_and_b32 v57, 0x7f, v54\n"
      "v_lshlrev_b32 v203, 7, v203\n"
      "v_lshlrev_b32 v63, 4, v50\n"
      "v_xor_b32 v57, v57, v63\n"
      "v_add3_u32 v203, v203, v57, s94\n"
      "s_and_b32 s92, s40, 1\n"
      "s_mul_i32 s92, s92, 32\n"
      "s_add_u32 s92, s92, 32\n"
      "s_add_u32 s92, s92, s75\n"
      "v_lshl_add_u32 v54, v46, 3, s92\n"
      "v_lshrrev_b32_e64 v205, 7, v54\n"
      "v_lshl_add_u32 v205, v205, 2, v43\n"
      "v_lshl_add_u32 v205, v205, 3, v51\n"
      "v_and_b32 v57, 0x7f, v54\n"
      "v_lshlrev_b32 v205, 7, v205\n"
      "v_lshlrev_b32 v63, 4, v50\n"
      "v_xor_b32 v57, v57, v63\n"
      "v_add3_u32 v205, v205, v57, s94\n"
      "s_and_b32 s92, s40, 1\n"
      "s_mul_i32 s92, s92, -64\n"
      "s_add_u32 s92, s92, 64\n"
      "s_add_u32 s92, s92, s75\n"
      "v_lshl_add_u32 v54, v46, 3, s92\n"
      "v_lshrrev_b32_e64 v202, 7, v54\n"
      "v_lshl_add_u32 v202, v202, 2, v43\n"
      "v_lshl_add_u32 v202, v202, 3, v51\n"
      "v_and_b32 v57, 0x7f, v54\n"
      "v_lshlrev_b32 v202, 7, v202\n"
      "v_lshlrev_b32 v63, 4, v50\n"
      "v_xor_b32 v57, v57, v63\n"
      "v_add3_u32 v202, v202, v57, s94\n"
      "v_lshrrev_b32 v53, 1, v43\n"
      "v_bfe_u32 v57, v44, 2, 3\n"
      "v_and_b32_e64 v54, 2, v51\n"
      "v_lshl_or_b32 v50, v53, 2, v54\n"
      "v_lshl_add_u32 v54, v46, 4, s75\n"
      "v_lshrrev_b32_e64 v204, 7, v54\n"
      "v_lshl_add_u32 v204, v204, 2, v53\n"
      "v_lshl_add_u32 v204, v204, 3, v57\n"
      "v_and_b32 v51, 0x7f, v54\n"
      "v_lshlrev_b32 v204, 7, v204\n"
      "v_lshlrev_b32 v63, 4, v50\n"
      "v_xor_b32_e64 v51, v51, v63\n"
      "v_add3_u32 v204, v204, v51, s94\n"
      "s_add_u32 s92, s75, 64\n"
      "v_lshl_add_u32 v54, v46, 3, s92\n"
      "v_lshrrev_b32 v179, 7, v54\n"
      "v_lshl_add_u32 v179, v179, 2, v53\n"
      "v_lshl_add_u32 v179, v179, 3, v57\n"
      "v_and_b32 v51, 0x7f, v54\n"
      "v_lshlrev_b32 v179, 7, v179\n"
      "v_lshlrev_b32 v63, 4, v50\n"
      "v_xor_b32_e64 v51, v51, v63\n"
      "v_add3_u32 v179, v179, v51, s94\n"
      "v_add_u32 v131, 49152, v203\n"
      "v_add_u32 v178, 49152, v205\n"
      "v_add_u32 v177, 49152, v202\n"
      "v_add_u32 v170, 49152, v204\n"
      "v_add_u32 v171, 49152, v179\n"
      "v_bfe_u32 v63, v44, 1, 3\n"
      "v_xor_b32 v63, v63, v43\n"
      "v_lshlrev_b32 v0, 7, v45\n"
      "v_lshl_add_u32 v0, v63, 4, v0\n"
      "v_xor_b32_e64 v1, 64, v0\n"
      "v_mul_u32_u24 v2, 768, v43\n"
      "v_lshl_add_u32 v2, v45, 4, v2\n"
      "v_add_u32 v2, 36864, v2\n"
      "s_add_u32 s92, s40, 1\n"
      "s_lshr_b32 s92, s92, 1\n"
      "s_add_u32 s92, s92, s40\n"
      "s_lshr_b32 s93, s40, 1\n"
      "s_mul_i32 s93, s93, 3\n"
      "s_add_u32 s93, s93, 1\n"
      "s_and_b32 s95, s40, 1\n"
      "s_lshl_b32 s95, s95, 3\n"
      "s_mul_i32 s94, s92, 3072\n"
      "v_add_u32 v3, s94, v2\n"
      "s_mul_i32 s94, s93, 3072\n"
      "s_add_u32 s94, s94, s95\n"
      "v_add_u32 v4, s94, v2\n"
      "s_lshl_b32 s92, s92, 11\n"
      "s_lshl_b32 s93, s93, 11\n"
      "s_add_u32 s93, s93, s95\n"
      "v_lshl_add_u32 v51, v43, 3, s92\n"
      "v_mov_b32 v50, s93\n"
      "v_cmp_eq_u32 vcc, 2, v43\n"
      "v_lshrrev_b32 v63, 2, v45\n"
      "v_lshlrev_b32 v12, 9, v63\n"
      "v_cndmask_b32 v51, v51, v50, vcc\n"
      "v_lshl_add_u32 v12, v46, 1, v12\n"
      "v_cmp_eq_u32 vcc, 3, v43\n"
      "v_mov_b32 v50, 0x100000\n"
      "v_add_u32 v12, 149504, v12\n"
      "v_cndmask_b32 v51, v51, v50, vcc\n"
      "v_add_u32 v12, v12, v51\n"
      "s_lshl_b32 s94, s40, 7\n"
      "s_add_u32 s94, s94, 119552\n"
      "v_lshlrev_b32 v13, 4, v63\n"
      "v_lshl_add_u32 v13, v46, 1, v13\n"
      "v_and_b32 v51, 1, v43\n"
      "v_lshl_add_u32 v13, v51, 3, v13\n"
      "v_lshrrev_b32 v63, 1, v43\n"
      "v_lshl_add_u32 v13, v63, 6, v13\n"
      "v_add_u32_e64 v13, s94, v13\n"
      "v_cmp_ne_u32 vcc, 0, v46\n"
      "v_lshl_add_u32 v14, v43, 4, s94\n"
      "s_lshl_b32 s94, s75, 1\n"
      "s_add_u32 s94, s94, 124928\n"
      "v_mad_u32_u24 v10, v43, 48, s94\n"
      "v_cndmask_b32 v14, v14, v50, vcc\n"
      "v_cndmask_b32 v10, v10, v50, vcc\n"
      "v_mov_b32 v50, 0x3f803f80\n"
      "v_cndmask_b32_e64 v32, v50, 0, vcc\n"
      "v_mov_b32 v33, v32\n"
      "v_mov_b32 v34, v32\n"
      "v_mov_b32 v35, v32\n"
      "s_add_u32 s94, s75, 125696\n"
      "v_lshl_add_u32 v11, v45, 1, s94\n"
      "s_lshr_b32 s94, s40, 1\n"
      "s_lshl_b32 s94, s94, 10\n"
      "s_add_u32 s94, s94, s95\n"
      "s_add_u32 s94, s94, 115456\n"
      "v_lshl_add_u32 v8, v63, 4, v45\n"
      "v_lshl_add_u32 v8, v8, 4, s94\n"
      "v_lshl_add_u32 v8, v51, 2, v8\n"
      "v_add_u32 v9, 115456, v48\n"
      "v_add_u32 v7, 73728, v27\n"
      "s_lshl_b32 s94, s40, 2\n"
      "s_add_u32 s94, s94, 120064\n"
      "v_add_u32 v31, s94, v48\n"
      "s_mul_i32 s94, s40, 6\n"
      "v_mul_u32_u24 v50, 384, v45\n"
      "v_add_u32 v50, 73728, v50\n"
      "v_add_u32 v5, s94, v43\n"
      "v_xor_b32 v5, v5, v47\n"
      "v_lshl_add_u32 v5, v5, 4, v50\n"
      "v_add3_u32 v6, v63, s94, 4\n"
      "v_xor_b32 v6, v6, v47\n"
      "v_lshl_add_u32 v6, v6, 4, v50\n"
      "v_lshl_add_u32 v6, v51, 3, v6\n"
      "s_movk_i32 s95, 504\n"
      "s_lshl_b32 s92, s40, 6\n"
      "s_cmp_lt_u32 s40, 2\n"
      "s_cselect_b32 s93, 0, 0x80000000\n"
      "v_add_u32 v47, s92, v44\n"
      "s_add_u32 s92, s92, 256\n"
      "v_mul_u32_u24 v48, 2731, v47\n"
      "v_lshrrev_b32 v48, 16, v48\n"
      "v_mul_u32_u24 v50, 24, v48\n"
      "v_sub_u32 v50, v47, v50\n"
      "v_and_b32 v47, 7, v48\n"
      "v_xor_b32 v50, v50, v47\n"
      "v_lshlrev_b32 v50, 4, v50\n"
      "v_mul_u32_u24 v51, 37, v48\n"
      "v_lshrrev_b32 v51, 8, v51\n"
      "v_mad_u32_u24 v20, v51, s95, v48\n"
      "v_mul_u32_u24 v20, 384, v20\n"
      "v_add_u32 v20, v20, v50\n"
      "v_mul_u32_u24 v50, 7, v51\n"
      "v_sub_u32 v50, v48, v50\n"
      "v_lshrrev_b32 v50, 2, v50\n"
      "v_lshrrev_b32_e64 v47, 2, v51\n"
      "v_lshl_or_b32 v50, v50, 1, v47\n"
      "v_lshl_or_b32 v36, v50, 10, v36\n"
      "v_add_u32 v48, s92, v44\n"
      "s_add_u32 s92, s92, 256\n"
      "v_mul_u32_u24 v47, 2731, v48\n"
      "v_lshrrev_b32 v47, 16, v47\n"
      "v_mul_u32_u24 v51, 24, v47\n"
      "v_sub_u32 v51, v48, v51\n"
      "v_and_b32 v48, 7, v47\n"
      "v_xor_b32 v51, v51, v48\n"
      "v_lshlrev_b32 v51, 4, v51\n"
      "v_mul_u32_u24 v50, 37, v47\n"
      "v_lshrrev_b32_e64 v50, 8, v50\n"
      "v_mad_u32_u24 v21, v50, s95, v47\n"
      "v_mul_u32_u24 v21, 384, v21\n"
      "v_add_u32 v21, v21, v51\n"
      "v_mul_u32_u24 v51, 7, v50\n"
      "v_sub_u32 v51, v47, v51\n"
      "v_lshrrev_b32 v51, 2, v51\n"
      "v_lshrrev_b32 v47, 2, v50\n"
      "v_lshl_or_b32 v51, v51, 1, v47\n"
      "v_lshl_or_b32 v36, v51, 12, v36\n"
      "v_add_u32 v47, s92, v44\n"
      "s_add_u32 s92, s92, 256\n"
      "v_mul_u32_u24 v50, 2731, v47\n"
      "v_lshrrev_b32 v50, 16, v50\n"
      "v_mul_u32_u24 v51, 24, v50\n"
      "v_sub_u32 v51, v47, v51\n"
      "v_and_b32 v47, 7, v50\n"
      "v_xor_b32 v51, v51, v47\n"
      "v_lshlrev_b32 v51, 4, v51\n"
      "v_mul_u32_u24 v47, 37, v50\n"
      "v_lshrrev_b32 v47, 8, v47\n"
      "v_mad_u32_u24 v22, v47, s95, v50\n"
      "v_mul_u32_u24 v22, 384, v22\n"
      "v_add_u32 v22, v22, v51\n"
      "v_mul_u32_u24 v51, 7, v47\n"
      "v_sub_u32 v51, v50, v51\n"
      "v_lshrrev_b32 v51, 2, v51\n"
      "v_lshrrev_b32 v50, 2, v47\n"
      "v_lshl_or_b32 v51, v51, 1, v50\n"
      "v_lshl_or_b32 v36, v51, 14, v36\n"
      "v_add_u32 v50, s92, v44\n"
      "s_add_u32 s92, s92, 256\n"
      "v_mul_u32_u24 v51, 2731, v50\n"
      "v_lshrrev_b32 v51, 16, v51\n"
      "v_mul_u32_u24 v48, 24, v51\n"
      "v_sub_u32 v48, v50, v48\n"
      "v_and_b32 v50, 7, v51\n"
      "v_xor_b32 v48, v48, v50\n"
      "v_lshlrev_b32 v48, 4, v48\n"
      "v_mul_u32_u24 v47, 37, v51\n"
      "v_lshrrev_b32 v47, 8, v47\n"
      "v_mad_u32_u24 v23, v47, s95, v51\n"
      "v_mul_u32_u24 v23, 384, v23\n"
      "v_add_u32 v23, v23, v48\n"
      "v_mul_u32_u24 v50, 7, v47\n"
      "v_sub_u32 v50, v51, v50\n"
      "v_lshrrev_b32 v50, 2, v50\n"
      "v_lshrrev_b32_e64 v51, 2, v47\n"
      "v_lshl_or_b32 v50, v50, 1, v51\n"
      "v_lshl_or_b32 v36, v50, 16, v36\n"
      "v_add_u32 v50, s92, v44\n"
      "s_add_u32 s92, s92, 256\n"
      "v_mul_u32_u24 v47, 2731, v50\n"
      "v_lshrrev_b32 v47, 16, v47\n"
      "v_mul_u32_u24 v51, 24, v47\n"
      "v_sub_u32 v51, v50, v51\n"
      "v_and_b32 v50, 7, v47\n"
      "v_xor_b32 v51, v51, v50\n"
      "v_lshl_add_u32 v51, v51, 4, s93\n"
      "v_mul_u32_u24 v50, 37, v47\n"
      "v_lshrrev_b32 v50, 8, v50\n"
      "v_mad_u32_u24 v25, v50, s95, v47\n"
      "v_mul_u32_u24 v25, 384, v25\n"
      "v_add_u32 v25, v25, v51\n"
      "v_mul_u32_u24 v51, 7, v50\n"
      "v_sub_u32 v51, v47, v51\n"
      "v_lshrrev_b32 v51, 2, v51\n"
      "v_lshrrev_b32 v47, 2, v50\n"
      "v_lshl_or_b32 v51, v51, 1, v47\n"
      "v_lshl_or_b32 v36, v51, 18, v36\n"
      "v_subrev_u32 v50, 2, v44\n"
      "v_cmp_ge_u32 s[72:73], s44, v50\n"
      "v_min_u32 v50, s44, v50\n"
      "s_movk_i32 s93, 4180\n"
      "s_cmp_lt_u32 s41, 209\n"
      "s_cselect_b32 s92, 209, 47\n"
      "s_cselect_b32 s93, 795, s93\n"
      "s_cmp_lt_u32 s41, 252\n"
      "s_cselect_b32 s94, 16, 15\n"
      "s_add_u32 s93, s93, s41\n"
      "v_lshl_add_u32 v51, v50, 8, s41\n"
      "v_mul_u32_u24_e64 v47, s92, v50\n"
      "v_add_u32 v47, s93, v47\n"
      "v_cmp_lt_u32 vcc, s94, v50\n"
      "v_subrev_u32 v48, 5184, v47\n"
      "s_nop 0\n"
      "v_cndmask_b32 v51, v51, v47, vcc\n"
      "v_cmp_le_u32 vcc, 5184, v51\n"
      "v_mul_u32_u24 v46, 58255, v51\n"
      "v_lshrrev_b32 v46, 22, v46\n"
      "v_mul_u32_u24 v63, 72, v46\n"
      "v_sub_u32 v63, v51, v63\n"
      "v_min_u32 v47, 72, v48\n"
      "v_cndmask_b32 v46, v46, v47, vcc\n"
      "v_subrev_u32 v47, 72, v48\n"
      "v_min_u32 v47, 72, v47\n"
      "v_cndmask_b32 v63, v63, v47, vcc\n"
      "v_mul_u32_u24 v38, 73, v46\n"
      "v_add_u32 v38, v38, v63\n"
      "v_mul_u32_u24 v38, 4802, v38\n"
      "v_mul_u32_u24 v50, 0x1ff, v46\n"
      "v_add_u32 v50, v50, v63\n"
      "v_mul_u32_u24 v50, 2688, v50\n"
      "v_add_u32 v50, 589824, v50\n"
      "v_cmp_eq_u32 vcc, 72, v46\n"
      "v_or_b32 v46, 0x80000000, v50\n"
      "s_nop 0\n"
      "v_cndmask_b32 v50, v50, v46, vcc\n"
      "v_cmp_eq_u32 vcc, 72, v63\n"
      "v_or_b32_e64 v46, 0x40000000, v50\n"
      "s_nop 0\n"
      "v_cndmask_b32 v50, v50, v46, vcc\n"
      "v_mov_b32 v47, 0x20000000\n"
      "v_cndmask_b32 v50, v47, v50, s[72:73]\n"
      "v_cndmask_b32 v38, v47, v38, s[72:73]\n"
      "v_mov_b32 v37, v47\n"
      "s_nop 1\n"
      "v_mov_b32_dpp v37, v50 wave_shr:1 row_mask:0xf bank_mask:0xf\n"
      "v_mov_b32 v79, 0\n"
      "v_mov_b32 v83, 0\n"
      "v_mov_b32_e64 v87, 0\n"
      "v_mov_b32_e64 v91, 0\n"
      "v_mov_b32_e64 v95, 0\n"
      "v_mul_u32_u24 v76, 1168, v45\n"
      "s_add_u32 s93, s75, 125696\n"
      "v_lshl_add_u32 v80, v43, 3, v76\n"
      "v_add_u32 v80, s93, v80\n"
      "s_lshl_b32 s92, s75, 1\n"
      "s_add_u32 s92, s92, 124928\n"
      "v_mad_u32_u24 v46, v43, 48, s92\n"
      "v_add_u32 v76, v76, v46\n"
      "v_add_u32 v193, 149504, v0\n"
      "v_add_u32 v81, 149504, v1\n"
      "s_mov_b32 s78, 0\n"
      "s_add_u32 m0, s33, 0\n"
      "s_nop 0\n"
      "buffer_load_dwordx4 v132, s[28:31], s78 offen  lds\n"
      "s_add_u32 m0, s33, 3968\n"
      "s_nop 0\n"
      "buffer_load_dwordx4 v132, s[28:31], s78 offen offset:128  lds\n"
      "s_add_u32 m0, s33, 7936\n"
      "s_nop 0\n"
      "buffer_load_dwordx4 v132, s[28:31], s78 offen offset:256  lds\n"
      "s_add_u32 m0, s33, 11904\n"
      "s_nop 0\n"
      "buffer_load_dwordx4 v132, s[28:31], s78 offen offset:384  lds\n"
      "s_add_u32 m0, s33, 15872\n"
      "s_nop 0\n"
      "buffer_load_dwordx4 v132, s[28:31], s78 offen offset:512  lds\n"
      "s_add_u32 m0, s33, 19840\n"
      "s_nop 0\n"
      "buffer_load_dwordx4 v132, s[28:31], s78 offen offset:640  lds\n"
      "s_waitcnt lgkmcnt(0)\n"
      "s_pack_ll_b32_b16 s92, s0, s8\n"
      "s_waitcnt vmcnt(17)\n"
      "v_dot2_f32_bf16 v43, s92, v42, 0\n"
      "s_pack_hh_b32_b16 s93, s0, s8\n"
      "v_dot2_f32_bf16 v45, s93, v42, 0\n"
      "s_pack_ll_b32_b16 s94, s1, s9\n"
      "v_dot2_f32_bf16 v47, s94, v42, 0\n"
      "s_pack_hh_b32_b16 s95, s1, s9\n"
      "v_dot2_f32_bf16 v51, s95, v42, 0\n"
      "s_pack_ll_b32_b16 s92, s2, s10\n"
      "v_dot2_f32_bf16 v50, s92, v42, 0\n"
      "s_pack_hh_b32_b16 s93, s2, s10\n"
      "v_dot2_f32_bf16 v48, s93, v42, 0\n"
      "s_pack_ll_b32_b16 s94, s3, s11\n"
      "v_dot2_f32_bf16 v63, s94, v42, 0\n"
      "s_pack_hh_b32_b16 s95, s3, s11\n"
      "v_dot2_f32_bf16 v54, s95, v42, 0\n"
      "s_pack_ll_b32_b16 s92, s4, s12\n"
      "v_dot2_f32_bf16 v53, s92, v42, 0\n"
      "s_pack_hh_b32_b16 s93, s4, s12\n"
      "v_dot2_f32_bf16 v57, s93, v42, 0\n"
      "s_pack_ll_b32_b16 s94, s5, s13\n"
      "v_dot2_f32_bf16 v60, s94, v42, 0\n"
      "s_pack_hh_b32_b16 s95, s5, s13\n"
      "v_dot2_f32_bf16 v61, s95, v42, 0\n"
      "s_pack_ll_b32_b16 s92, s6, s14\n"
      "v_dot2_f32_bf16 v64, s92, v42, 0\n"
      "s_pack_hh_b32_b16 s93, s6, s14\n"
      "v_dot2_f32_bf16 v49, s93, v42, 0\n"
      "s_pack_ll_b32_b16 s94, s7, s15\n"
      "v_dot2_f32_bf16 v59, s94, v42, 0\n"
      "s_pack_hh_b32_b16 s95, s7, s15\n"
      "v_dot2_f32_bf16 v55, s95, v42, 0\n"
      "s_load_dwordx16 s[0:15], s[84:85], 64\n"
      "s_lshl_b32 s92, s40, 4\n"
      "s_lshr_b64 s[66:67], s[66:67], s92\n"
      "s_lshl_b32 s66, s66, 16\n"
      "v_mov_b32_e64 v42, s66\n"
      "v_max_f32 v42, 0xc2c80000, v42\n"
      "v_min_f32 v42, 0x40930000, v42\n"
      "v_mul_f32 v42, 0x3fb8aa3b, v42\n"
      "v_exp_f32 v42, v42\n"
      "s_nop 0\n"
      "v_cvt_pk_bf16_f32 v42, v42, v42\n"
      "v_lshlrev_b32 v42, 16, v42\n"
      "v_max_f32 v43, 0, v43\n"
      "v_max_f32 v45, 0, v45\n"
      "v_cvt_pk_bf16_f32 v43, v43, v45\n"
      "v_max_f32 v47, 0, v47\n"
      "v_max_f32 v51, 0, v51\n"
      "v_cvt_pk_bf16_f32 v47, v47, v51\n"
      "v_max_f32 v50, 0, v50\n"
      "v_max_f32 v48, 0, v48\n"
      "v_cvt_pk_bf16_f32 v50, v50, v48\n"
      "v_max_f32 v63, 0, v63\n"
      "v_max_f32 v54, 0, v54\n"
      "v_cvt_pk_bf16_f32 v63, v63, v54\n"
      "v_max_f32 v53, 0, v53\n"
      "v_max_f32 v57, 0, v57\n"
      "v_cvt_pk_bf16_f32 v53, v53, v57\n"
      "v_max_f32 v60, 0, v60\n"
      "v_max_f32 v61, 0, v61\n"
      "v_cvt_pk_bf16_f32 v60, v60, v61\n"
      "v_max_f32 v64, 0, v64\n"
      "v_max_f32 v49, 0, v49\n"
      "v_cvt_pk_bf16_f32 v64, v64, v49\n"
      "v_max_f32 v59, 0, v59\n"
      "v_max_f32 v55, 0, v55\n"
      "v_cvt_pk_bf16_f32 v59, v59, v55\n"
      "s_pack_ll_b32_b16 s92, s48, s50\n"
      "v_dot2_f32_bf16 v49, s92, v43, 0\n"
      "s_pack_hh_b32_b16 s93, s48, s50\n"
      "v_dot2_f32_bf16 v48, s93, v43, 0\n"
      "s_pack_ll_b32_b16 s94, s49, s51\n"
      "v_dot2_f32_bf16 v45, s94, v43, 0\n"
      "s_pack_hh_b32_b16 s95, s49, s51\n"
      "v_dot2_f32_bf16 v51, s95, v43, 0\n"
      "s_pack_ll_b32_b16 s92, s52, s54\n"
      "v_dot2_f32_bf16 v49, s92, v47, v49\n"
      "s_pack_hh_b32_b16 s93, s52, s54\n"
      "v_dot2_f32_bf16 v48, s93, v47, v48\n"
      "s_pack_ll_b32_b16 s94, s53, s55\n"
      "v_dot2_f32_bf16 v45, s94, v47, v45\n"
      "s_pack_hh_b32_b16 s95, s53, s55\n"
      "v_dot2_f32_bf16 v51, s95, v47, v51\n"
      "s_pack_ll_b32_b16 s92, s56, s58\n"
      "v_dot2_f32_bf16 v49, s92, v50, v49\n"
      "s_pack_hh_b32_b16 s93, s56, s58\n"
      "v_dot2_f32_bf16 v48, s93, v50, v48\n"
      "s_pack_ll_b32_b16 s94, s57, s59\n"
      "v_dot2_f32_bf16 v45, s94, v50, v45\n"
      "s_pack_hh_b32_b16 s95, s57, s59\n"
      "v_dot2_f32_bf16 v51, s95, v50, v51\n"
      "s_pack_ll_b32_b16 s92, s60, s62\n"
      "v_dot2_f32_bf16 v49, s92, v63, v49\n"
      "s_pack_hh_b32_b16 s93, s60, s62\n"
      "v_dot2_f32_bf16 v48, s93, v63, v48\n"
      "s_pack_ll_b32_b16 s94, s61, s63\n"
      "v_dot2_f32_bf16 v45, s94, v63, v45\n"
      "s_pack_hh_b32_b16 s95, s61, s63\n"
      "v_dot2_f32_bf16 v51, s95, v63, v51\n"
      "s_waitcnt lgkmcnt(0)\n"
      "s_pack_ll_b32_b16 s92, s0, s2\n"
      "v_dot2_f32_bf16 v49, s92, v53, v49\n"
      "s_pack_hh_b32_b16 s93, s0, s2\n"
      "v_dot2_f32_bf16 v48, s93, v53, v48\n"
      "s_pack_ll_b32_b16 s94, s1, s3\n"
      "v_dot2_f32_bf16 v45, s94, v53, v45\n"
      "s_pack_hh_b32_b16 s95, s1, s3\n"
      "v_dot2_f32_bf16 v51, s95, v53, v51\n"
      "s_pack_ll_b32_b16 s92, s4, s6\n"
      "v_dot2_f32_bf16 v49, s92, v60, v49\n"
      "s_pack_hh_b32_b16 s93, s4, s6\n"
      "v_dot2_f32_bf16 v48, s93, v60, v48\n"
      "s_pack_ll_b32_b16 s94, s5, s7\n"
      "v_dot2_f32_bf16 v45, s94, v60, v45\n"
      "s_pack_hh_b32_b16 s95, s5, s7\n"
      "v_dot2_f32_bf16 v51, s95, v60, v51\n"
      "s_pack_ll_b32_b16 s92, s8, s10\n"
      "v_dot2_f32_bf16 v49, s92, v64, v49\n"
      "s_pack_hh_b32_b16 s93, s8, s10\n"
      "v_dot2_f32_bf16 v48, s93, v64, v48\n"
      "s_pack_ll_b32_b16 s94, s9, s11\n"
      "v_dot2_f32_bf16 v45, s94, v64, v45\n"
      "s_pack_hh_b32_b16 s95, s9, s11\n"
      "v_dot2_f32_bf16 v51, s95, v64, v51\n"
      "s_pack_ll_b32_b16 s92, s12, s14\n"
      "v_dot2_f32_bf16 v49, s92, v59, v49\n"
      "s_pack_hh_b32_b16 s93, s12, s14\n"
      "v_dot2_f32_bf16 v48, s93, v59, v48\n"
      "s_pack_ll_b32_b16 s94, s13, s15\n"
      "v_dot2_f32_bf16 v45, s94, v59, v45\n"
      "s_pack_hh_b32_b16 s95, s13, s15\n"
      "v_dot2_f32_bf16 v51, s95, v59, v51\n"
      "s_mov_b32 s53, 0x3fb8aa3b\n"
      "s_mov_b32 s51, 0x2a8cbccc\n"
      "s_mov_b32 s52, 0x3ebcbe62\n"
      "s_mov_b32 s58, 0xffff0000\n"
      "s_mov_b32 s59, 0x0\n"
      "s_mov_b32 s60, 0x0\n"
      "s_mov_b32 s61, 0xffff\n"
      "s_mov_b32 s62, 0x0\n"
      "s_mov_b32 s63, 0xffff0000\n"
      "v_mov_b32 v46, 0x41f00000\n"
      "v_cmp_nle_f32 s[54:55], v42, v46\n"
      "v_mul_f32 v42, s53, v42\n"
      "s_nop 0\n"
      "v_readfirstlane_b32 s50, v42\n"
      "v_cvt_pk_bf16_f32 v49, v49, v49\n"
      "v_lshlrev_b32 v49, 16, v49\n"
      "v_mul_f32 v49, 0xbfb8aa3b, v49\n"
      "v_exp_f32 v49, v49\n"
      "s_nop 0\n"
      "v_add_f32 v49, 1.0, v49\n"
      "v_rcp_f32 v49, v49\n"
      "s_nop 0\n"
      "v_cvt_pk_bf16_f32 v49, v49, v49\n"
      "v_lshlrev_b32 v49, 16, v49\n"
      "v_add_f32 v49, -0.5, v49\n"
      "v_mul_f32 v49, 0x41b8aa3b, v49\n"
      "ds_write_b32 v40, v49\n"
      "v_cvt_pk_bf16_f32 v48, v48, v48\n"
      "v_lshlrev_b32_e64 v48, 16, v48\n"
      "v_mul_f32 v48, 0xbfb8aa3b, v48\n"
      "v_exp_f32 v48, v48\n"
      "s_nop 0\n"
      "v_add_f32 v48, 1.0, v48\n"
      "v_rcp_f32_e64 v48, v48\n"
      "s_nop 0\n"
      "v_cvt_pk_bf16_f32 v48, v48, v48\n"
      "v_lshlrev_b32 v48, 16, v48\n"
      "v_add_f32 v48, -0.5, v48\n"
      "v_mul_f32 v48, 0x41b8aa3b, v48\n"
      "ds_write_b32 v40, v48 offset:704\n"
      "v_cvt_pk_bf16_f32 v45, v45, v45\n"
      "v_lshlrev_b32 v45, 16, v45\n"
      "v_mul_f32 v45, 0xbfb8aa3b, v45\n"
      "v_exp_f32 v45, v45\n"
      "s_nop 0\n"
      "v_add_f32 v45, 1.0, v45\n"
      "v_rcp_f32 v45, v45\n"
      "s_nop 0\n"
      "v_cvt_pk_bf16_f32 v45, v45, v45\n"
      "v_lshlrev_b32 v45, 16, v45\n"
      "v_add_f32 v45, -0.5, v45\n"
      "v_mul_f32 v45, 0x41b8aa3b, v45\n"
      "ds_write_b32 v40, v45 offset:1408\n"
      "v_cvt_pk_bf16_f32 v51, v51, v51\n"
      "v_lshlrev_b32 v51, 16, v51\n"
      "v_mul_f32 v51, 0xbfb8aa3b, v51\n"
      "v_exp_f32 v51, v51\n"
      "s_nop 0\n"
      "v_add_f32 v51, 1.0, v51\n"
      "v_rcp_f32 v51, v51\n"
      "s_nop 0\n"
      "v_cvt_pk_bf16_f32 v51, v51, v51\n"
      "v_lshlrev_b32 v51, 16, v51\n"
      "v_add_f32 v51, -0.5, v51\n"
      "v_mul_f32 v51, 0x41b8aa3b, v51\n"
      "ds_write_b32 v40, v51 offset:2112\n"
      "s_mul_i32 s76, s40, 704\n"
      "s_add_u32 s76, s76, 122112\n"
      "s_mov_b32 s79, 36864\n"
      "s_add_u32 m0, s33, 24576\n"
      "s_nop 0\n"
      "buffer_load_dwordx4 v132, s[28:31], s79 offen  lds\n"
      "s_add_u32 m0, s33, 28544\n"
      "s_nop 0\n"
      "buffer_load_dwordx4 v132, s[28:31], s79 offen offset:128  lds\n"
      "s_add_u32 m0, s33, 32512\n"
      "s_nop 0\n"
      "buffer_load_dwordx4 v132, s[28:31], s79 offen offset:256  lds\n"
      "s_add_u32 m0, s33, 36480\n"
      "s_nop 0\n"
      "buffer_load_dwordx4 v132, s[28:31], s79 offen offset:384  lds\n"
      "s_add_u32 m0, s33, 40448\n"
      "s_nop 0\n"
      "buffer_load_dwordx4 v132, s[28:31], s79 offen offset:512  lds\n"
      "s_add_u32 m0, s33, 44416\n"
      "s_nop 0\n"
      "buffer_load_dwordx4 v132, s[28:31], s79 offen offset:640  lds\n"
      "s_waitcnt vmcnt(20)\n"
      "s_waitcnt lgkmcnt(0)\n"
      "s_barrier\n"
      "v_and_b32 v59, 15, v44\n"
      "v_lshrrev_b32_e64 v53, 4, v44\n"
      "v_mul_u32_u24 v59, 196, v59\n"
      "v_add_u32 v59, 92160, v59\n"
      "v_lshl_add_u32 v53, v53, 4, v59\n"
      "v_min_u32 v60, 192, v41\n"
      "v_add_u32 v60, 92160, v60\n"
      "ds_read2_b32 v[40:41], v53 offset0:0 offset1:1\n"
      "ds_read2_b32 v[42:43], v53 offset0:2 offset1:3\n"
      "ds_read2_b32 v[44:45], v53 offset0:16 offset1:17\n"
      "ds_read2_b32 v[46:47], v53 offset0:18 offset1:19\n"
      "ds_read2_b32 v[48:49], v53 offset0:32 offset1:33\n"
      "ds_read2_b32 v[50:51], v53 offset0:34 offset1:35\n"
      "ds_read_b32 v99, v59 offset:192\n"
      "ds_read_b32 v234, v59 offset:3328\n"
      "ds_read_b32 v98, v59 offset:6464\n"
      "ds_read_b32 v96, v60 offset:9408\n"
      "s_mov_b32 s72, 0xffff\n"
      "s_mov_b32 s73, 0x0\n"
      "s_mov_b32 s66, 0xffffffff\n"
      "s_mov_b32 s67, 0x1ffff\n"
      "v_add_u32 v64, 3136, v53\n"
      "v_add_u32 v233, 6272, v53\n"
      "ds_read2_b32 v[52:53], v64 offset0:0 offset1:1\n"
      "ds_read2_b32 v[54:55], v64 offset0:2 offset1:3\n"
      "ds_read2_b32 v[56:57], v64 offset0:16 offset1:17\n"
      "ds_read2_b32 v[58:59], v64 offset0:18 offset1:19\n"
      "ds_read2_b32 v[60:61], v64 offset0:32 offset1:33\n"
      "ds_read2_b32 v[62:63], v64 offset0:34 offset1:35\n"
      "ds_read2_b32 v[64:65], v233 offset0:0 offset1:1\n"
      "ds_read2_b32 v[66:67], v233 offset0:2 offset1:3\n"
      "ds_read2_b32 v[68:69], v233 offset0:16 offset1:17\n"
      "ds_read2_b32 v[70:71], v233 offset0:18 offset1:19\n"
      "ds_read2_b32 v[72:73], v233 offset0:32 offset1:33\n"
      "ds_read2_b32 v[74:75], v233 offset0:34 offset1:35\n"
      "s_waitcnt lgkmcnt(11)\n"
      "v_lshl_add_u32 v40, v40, 2, s76\n"
      "v_lshl_add_u32 v41, v41, 2, s76\n"
      "v_lshl_add_u32 v42, v42, 2, s76\n"
      "v_lshl_add_u32 v43, v43, 2, s76\n"
      "v_lshl_add_u32 v44, v44, 2, s76\n"
      "v_lshl_add_u32 v45, v45, 2, s76\n"
      "v_lshl_add_u32 v46, v46, 2, s76\n"
      "v_lshl_add_u32 v47, v47, 2, s76\n"
      "v_lshl_add_u32 v48, v48, 2, s76\n"
      "v_lshl_add_u32 v49, v49, 2, s76\n"
      "v_lshl_add_u32 v50, v50, 2, s76\n"
      "v_lshl_add_u32 v51, v51, 2, s76\n"
      "v_lshl_add_u32 v52, v52, 2, s76\n"
      "v_lshl_add_u32 v53, v53, 2, s76\n"
      "s_waitcnt lgkmcnt(4)\n"
      "v_lshl_add_u32 v54, v54, 2, s76\n"
      "v_lshl_add_u32 v55, v55, 2, s76\n"
      "v_lshl_add_u32 v56, v56, 2, s76\n"
      "v_lshl_add_u32 v57, v57, 2, s76\n"
      "v_lshl_add_u32 v58, v58, 2, s76\n"
      "v_lshl_add_u32 v59, v59, 2, s76\n"
      "v_lshl_add_u32 v60, v60, 2, s76\n"
      "v_lshl_add_u32 v61, v61, 2, s76\n"
      "v_lshl_add_u32 v62, v62, 2, s76\n"
      "v_lshl_add_u32 v63, v63, 2, s76\n"
      "v_lshl_add_u32 v64, v64, 2, s76\n"
      "v_lshl_add_u32 v65, v65, 2, s76\n"
      "v_lshl_add_u32 v66, v66, 2, s76\n"
      "v_lshl_add_u32 v67, v67, 2, s76\n"
      "s_waitcnt lgkmcnt(0)\n"
      "v_lshl_add_u32 v68, v68, 2, s76\n"
      "v_lshl_add_u32 v69, v69, 2, s76\n"
      "v_lshl_add_u32 v70, v70, 2, s76\n"
      "v_lshl_add_u32 v71, v71, 2, s76\n"
      "v_lshl_add_u32 v72, v72, 2, s76\n"
      "v_lshl_add_u32 v73, v73, 2, s76\n"
      "v_lshl_add_u32 v74, v74, 2, s76\n"
      "v_lshl_add_u32 v75, v75, 2, s76\n"
      "v_lshl_add_u32 v99, v99, 2, s76\n"
      "v_lshl_add_u32 v234, v234, 2, s76\n"
      "v_lshl_add_u32 v98, v98, 2, s76\n"
      "v_lshl_add_u32 v96, v96, 2, s76\n"
      "ds_read_b32 v40, v40\n"
      "ds_read_b32 v41, v41\n"
      "ds_read_b32 v42, v42\n"
      "ds_read_b32 v43, v43\n"
      "ds_read_b32 v44, v44\n"
      "ds_read_b32 v45, v45\n"
      "ds_read_b32 v46, v46\n"
      "ds_read_b32 v47, v47\n"
      "ds_read_b32 v48, v48\n"
      "ds_read_b32 v49, v49\n"
      "ds_read_b32 v50, v50\n"
      "ds_read_b32 v51, v51\n"
      "ds_read_b32 v52, v52\n"
      "ds_read_b32 v53, v53\n"
      "ds_read_b32 v54, v54\n"
      "ds_read_b32 v55, v55\n"
      "ds_read_b32 v56, v56\n"
      "ds_read_b32 v57, v57\n"
      "ds_read_b32 v58, v58\n"
      "ds_read_b32 v59, v59\n"
      "ds_read_b32 v60, v60\n"
      "ds_read_b32 v61, v61\n"
      "ds_read_b32 v62, v62\n"
      "ds_read_b32 v63, v63\n"
      "ds_read_b32 v64, v64\n"
      "ds_read_b32 v65, v65\n"
      "ds_read_b32 v66, v66\n"
      "ds_read_b32 v67, v67\n"
      "ds_read_b32 v68, v68\n"
      "ds_read_b32 v69, v69\n"
      "ds_read_b32 v70, v70\n"
      "ds_read_b32 v71, v71\n"
      "ds_read_b32 v72, v72\n"
      "ds_read_b32 v73, v73\n"
      "ds_read_b32 v74, v74\n"
      "ds_read_b32 v75, v75\n"
      "ds_read_b32 v99, v99\n"
      "ds_read_b32 v234, v234\n"
      "ds_read_b32 v98, v98\n"
      "ds_read_b32 v96, v96\n"
      "v_mov_b32 v233, 0xf149f2ca\n"
      "s_waitcnt lgkmcnt(3)\n"
      "v_cndmask_b32 v24, v233, v99, s[72:73]\n"
      "s_waitcnt lgkmcnt(2)\n"
      "v_cndmask_b32 v26, v233, v234, s[72:73]\n"
      "s_waitcnt lgkmcnt(1)\n"
      "v_cndmask_b32 v28, v233, v98, s[72:73]\n"
      "s_waitcnt lgkmcnt(0)\n"
      "v_cndmask_b32 v30, v233, v96, s[66:67]\n"
      "s_mov_b32 s78, 73728\n"
      "s_add_u32 m0, s33, 49152\n"
      "s_nop 0\n"
      "buffer_load_dwordx4 v132, s[28:31], s78 offen  lds\n"
      "s_add_u32 m0, s33, 53120\n"
      "s_nop 0\n"
      "buffer_load_dwordx4 v132, s[28:31], s78 offen offset:128  lds\n"
      "s_add_u32 m0, s33, 57088\n"
      "s_nop 0\n"
      "buffer_load_dwordx4 v132, s[28:31], s78 offen offset:256  lds\n"
      "s_add_u32 m0, s33, 61056\n"
      "s_nop 0\n"
      "buffer_load_dwordx4 v132, s[28:31], s78 offen offset:384  lds\n"
      "s_add_u32 m0, s33, 65024\n"
      "s_nop 0\n"
      "buffer_load_dwordx4 v132, s[28:31], s78 offen offset:512  lds\n"
      "s_add_u32 m0, s33, 68992\n"
      "s_nop 0\n"
      "buffer_load_dwordx4 v132, s[28:31], s78 offen offset:640  lds\n"
      "buffer_load_dwordx4 v[232:235], v27, s[24:27], s47 offen\n"
      "buffer_load_dword v135, v29, s[24:27], s47 offen\n"
      "buffer_load_dwordx4 v[96:99], v27, s[24:27], s74 offen\n"
      "buffer_load_dword v134, v29, s[24:27], s74 offen\n"
      "s_mov_b32 s45, s46\n"
      "s_mov_b32 s66, 0\n"
      "s_mov_b32 s67, 0\n"
      "s_mov_b32 s68, 0\n"
      "s_mov_b32 s69, 0\n"
      "s_mov_b32 s70, 0\n"
      "s_mov_b32 s71, 0\n"
      "s_mov_b32 s48, 0\n"
      "s_mov_b32 s42, 1\n"
      "s_mov_b32 s43, 5\n"
      "v_readlane_b32 s47, v38, 4\n"
      "s_nop 0\n"
      "s_and_b32 s47, s47, -4\n"
      "s_waitcnt vmcnt(22)\n"
      "s_waitcnt vmcnt(16)\n"
      "s_waitcnt lgkmcnt(0)\n"
      "s_barrier\n"
      "ds_read_b128 v[236:239], v193\n"
      "ds_read_b128 v[244:247], v0\n"
      "s_add_u32 m0, s33, 73728\n"
      "s_mov_b32 s79, 110592\n"
      "buffer_load_dwordx4 v132, s[28:31], s79 offen  lds\n"
      "s_add_u32 m0, s33, 77696\n"
      "ds_read_b64_tr_b16 a[0:1], v203\n"
      "ds_read_b64_tr_b16 a[2:3], v203 offset:512\n"
      "ds_read_b64_tr_b16 a[24:25], v205\n"
      "ds_read_b64_tr_b16 a[26:27], v205 offset:512\n"
      "ds_read_b64_tr_b16 a[48:49], v202\n"
      "ds_read_b64_tr_b16 a[50:51], v202 offset:512\n"
      "ds_read_b64_tr_b16 a[72:73], v203 offset:12288\n"
      "ds_read_b64_tr_b16 a[74:75], v203 offset:12800\n"
      "ds_read_b64_tr_b16 a[96:97], v205 offset:12288\n"
      "ds_read_b64_tr_b16 a[98:99], v205 offset:12800\n"
      "ds_read_b64_tr_b16 a[120:121], v202 offset:12288\n"
      "ds_read_b64_tr_b16 a[122:123], v202 offset:12800\n"
      "buffer_load_dwordx4 v132, s[28:31], s79 offen offset:128  lds\n"
      "s_waitcnt lgkmcnt(9)\n"
      "v_mfma_f32_16x16x32_bf16 v[240:243], a[0:3], v[236:239], 0\n"
      "s_waitcnt lgkmcnt(8)\n"
      "s_add_u32 m0, s33, 81664\n"
      "v_readlane_b32 s46, v37, s42\n"
      "v_mfma_f32_16x16x32_bf16 v[184:187], a[24:27], v[236:239], 0\n"
      "s_waitcnt lgkmcnt(6)\n"
      "buffer_load_dwordx4 v132, s[28:31], s79 offen offset:256  lds\n"
      "v_mfma_f32_16x16x32_bf16 v[218:221], a[48:51], v[236:239], 0\n"
      "s_waitcnt lgkmcnt(3)\n"
      "s_add_u32 m0, s33, 85632\n"
      "ds_read_b128 v[228:231], v0 offset:2048\n"
      "v_mfma_f32_16x16x32_bf16 v[194:197], a[72:75], v[236:239], 0\n"
      "s_waitcnt lgkmcnt(2)\n"
      "buffer_load_dwordx4 v132, s[28:31], s79 offen offset:384  lds\n"
      "v_mfma_f32_16x16x32_bf16 v[136:139], a[96:99], v[236:239], 0\n"
      "s_waitcnt lgkmcnt(1)\n"
      "s_add_u32 m0, s33, 89600\n"
      "ds_read_b128 v[210:213], v0 offset:4096\n"
      "v_mfma_f32_16x16x32_bf16 v[214:217], a[120:123], v[236:239], 0\n"
      "buffer_load_dwordx4 v132, s[28:31], s79 offen offset:512  lds\n"
      "v_mfma_f32_16x16x32_bf16 v[116:119], a[0:3], v[244:247], 0\n"
      "s_add_u32 m0, s33, 93568\n"
      "v_mfma_f32_16x16x32_bf16 v[180:183], a[24:27], v[244:247], 0\n"
      "buffer_load_dwordx4 v132, s[28:31], s79 offen offset:640  lds\n"
      "v_mfma_f32_16x16x32_bf16 v[188:191], a[48:51], v[244:247], 0\n"
      "s_waitcnt vmcnt(16)\n"
      "s_barrier\n"
      "s_add_u32 m0, s33, 0\n"
      "v_mfma_f32_16x16x32_bf16 v[112:115], a[72:75], v[244:247], 0\n"
      "s_mov_b32 s78, 147456\n"
      "buffer_load_dwordx4 v132, s[28:31], s78 offen  lds\n"
      "v_mfma_f32_16x16x32_bf16 v[106:109], a[96:99], v[244:247], 0\n"
      "s_add_u32 m0, s33, 3968\n"
      "ds_read_b64_tr_b16 a[4:5], v203 offset:24576\n"
      "v_mfma_f32_16x16x32_bf16 v[222:225], a[120:123], v[244:247], 0\n"
      "s_waitcnt lgkmcnt(1)\n"
      "buffer_load_dwordx4 v132, s[28:31], s78 offen offset:128  lds\n"
      "v_mfma_f32_16x16x32_bf16 v[206:209], a[0:3], v[228:231], 0\n"
      "ds_read_b64_tr_b16 a[6:7], v203 offset:25088\n"
      "ds_read_b64_tr_b16 a[28:29], v205 offset:24576\n"
      "v_mfma_f32_16x16x32_bf16 v[198:201], a[24:27], v[228:231], 0\n"
      "ds_read_b64_tr_b16 a[30:31], v205 offset:25088\n"
      "ds_read_b64_tr_b16 a[52:53], v202 offset:24576\n"
      "v_mfma_f32_16x16x32_bf16 v[166:169], a[48:51], v[228:231], 0\n"
      "ds_read_b64_tr_b16 a[54:55], v202 offset:25088\n"
      "ds_read_b64_tr_b16 a[76:77], v203 offset:36864\n"
      "v_mfma_f32_16x16x32_bf16 v[124:127], a[72:75], v[228:231], 0\n"
      "ds_read_b64_tr_b16 a[78:79], v203 offset:37376\n"
      "ds_read_b64_tr_b16 a[100:101], v205 offset:36864\n"
      "v_mfma_f32_16x16x32_bf16 v[156:159], a[96:99], v[228:231], 0\n"
      "ds_read_b64_tr_b16 a[102:103], v205 offset:37376\n"
      "ds_read_b64_tr_b16 a[124:125], v202 offset:36864\n"
      "v_mfma_f32_16x16x32_bf16 v[120:123], a[120:123], v[228:231], 0\n"
      "ds_read_b64_tr_b16 a[126:127], v202 offset:37376\n"
      "s_add_u32 m0, s33, 7936\n"
      "v_mfma_f32_16x16x32_bf16 v[144:147], a[0:3], v[210:213], 0\n"
      "buffer_load_dwordx4 v132, s[28:31], s78 offen offset:256  lds\n"
      "v_mfma_f32_16x16x32_bf16 v[172:175], a[24:27], v[210:213], 0\n"
      "s_add_u32 m0, s33, 11904\n"
      "ds_read_b128 v[236:239], v81\n"
      "v_mfma_f32_16x16x32_bf16 v[140:143], a[48:51], v[210:213], 0\n"
      "buffer_load_dwordx4 v132, s[28:31], s78 offen offset:384  lds\n"
      "v_mfma_f32_16x16x32_bf16 v[152:155], a[72:75], v[210:213], 0\n"
      "s_add_u32 m0, s33, 15872\n"
      "ds_read_b128 v[244:247], v1\n"
      "v_mfma_f32_16x16x32_bf16 v[148:151], a[96:99], v[210:213], 0\n"
      "buffer_load_dwordx4 v132, s[28:31], s78 offen offset:512  lds\n"
      "v_mfma_f32_16x16x32_bf16 v[162:165], a[120:123], v[210:213], 0\n"
      "s_waitcnt lgkmcnt(2)\n"
      "s_add_u32 m0, s33, 19840\n"
      "ds_read_b128 v[210:213], v1 offset:2048\n"
      "buffer_load_dwordx4 v132, s[28:31], s78 offen offset:640  lds\n"
      "s_waitcnt lgkmcnt(2)\n"
      "v_mfma_f32_16x16x32_bf16 v[240:243], a[4:7], v[236:239], v[240:243]\n"
      "s_waitcnt vmcnt(16)\n"
      "s_barrier\n"
      "s_add_u32 m0, s33, 24576\n"
      "v_mfma_f32_16x16x32_bf16 v[184:187], a[28:31], v[236:239], v[184:187]\n"
      "s_mov_b32 s79, 184320\n"
      "buffer_load_dwordx4 v132, s[28:31], s79 offen  lds\n"
      "v_mfma_f32_16x16x32_bf16 v[218:221], a[52:55], v[236:239], v[218:221]\n"
      "s_add_u32 m0, s33, 28544\n"
      "ds_read_b64_tr_b16 a[8:9], v131\n"
      "v_mfma_f32_16x16x32_bf16 v[194:197], a[76:79], v[236:239], v[194:197]\n"
      "buffer_load_dwordx4 v132, s[28:31], s79 offen offset:128  lds\n"
      "v_mfma_f32_16x16x32_bf16 v[136:139], a[100:103], v[236:239], v[136:139]\n"
      "ds_read_b64_tr_b16 a[10:11], v131 offset:512\n"
      "ds_read_b64_tr_b16 a[32:33], v178\n"
      "v_mfma_f32_16x16x32_bf16 v[214:217], a[124:127], v[236:239], v[214:217]\n"
      "s_waitcnt lgkmcnt(3)\n"
      "ds_read_b64_tr_b16 a[34:35], v178 offset:512\n"
      "v_mfma_f32_16x16x32_bf16 v[116:119], a[4:7], v[244:247], v[116:119]\n"
      "ds_read_b64_tr_b16 a[56:57], v177\n"
      "ds_read_b64_tr_b16 a[58:59], v177 offset:512\n"
      "v_mfma_f32_16x16x32_bf16 v[180:183], a[28:31], v[244:247], v[180:183]\n"
      "ds_read_b64_tr_b16 a[80:81], v131 offset:12288\n"
      "ds_read_b64_tr_b16 a[82:83], v131 offset:12800\n"
      "v_mfma_f32_16x16x32_bf16 v[188:191], a[52:55], v[244:247], v[188:191]\n"
      "ds_read_b64_tr_b16 a[104:105], v178 offset:12288\n"
      "ds_read_b64_tr_b16 a[106:107], v178 offset:12800\n"
      "v_mfma_f32_16x16x32_bf16 v[112:115], a[76:79], v[244:247], v[112:115]\n"
      "ds_read_b64_tr_b16 a[128:129], v177 offset:12288\n"
      "ds_read_b64_tr_b16 a[130:131], v177 offset:12800\n"
      "v_mfma_f32_16x16x32_bf16 v[106:109], a[100:103], v[244:247], v[106:109]\n"
      "s_add_u32 m0, s33, 32512\n"
      "ds_read_b128 v[236:239], v1 offset:4096\n"
      "v_mfma_f32_16x16x32_bf16 v[222:225], a[124:127], v[244:247], v[222:225]\n"
      "buffer_load_dwordx4 v132, s[28:31], s79 offen offset:256  lds\n"
      "v_mfma_f32_16x16x32_bf16 v[206:209], a[4:7], v[210:213], v[206:209]\n"
      "s_add_u32 m0, s33, 36480\n"
      "ds_read_b128 v[244:247], v193 offset:4096\n"
      "v_mfma_f32_16x16x32_bf16 v[198:201], a[28:31], v[210:213], v[198:201]\n"
      "buffer_load_dwordx4 v132, s[28:31], s79 offen offset:384  lds\n"
      "v_mfma_f32_16x16x32_bf16 v[166:169], a[52:55], v[210:213], v[166:169]\n"
      "s_add_u32 m0, s33, 40448\n"
      "v_mfma_f32_16x16x32_bf16 v[124:127], a[76:79], v[210:213], v[124:127]\n"
      "buffer_load_dwordx4 v132, s[28:31], s79 offen offset:512  lds\n"
      "v_mfma_f32_16x16x32_bf16 v[156:159], a[100:103], v[210:213], v[156:159]\n"
      "s_add_u32 m0, s33, 44416\n"
      "ds_read_b128 v[102:105], v0 offset:6144\n"
      "v_mfma_f32_16x16x32_bf16 v[120:123], a[124:127], v[210:213], v[120:123]\n"
      "s_waitcnt lgkmcnt(2)\n"
      "buffer_load_dwordx4 v132, s[28:31], s79 offen offset:640  lds\n"
      "v_mfma_f32_16x16x32_bf16 v[144:147], a[4:7], v[236:239], v[144:147]\n"
      "s_add_u32 m0, s33, 48384\n"
      "v_mfma_f32_16x16x32_bf16 v[172:175], a[28:31], v[236:239], v[172:175]\n"
      "v_mfma_f32_16x16x32_bf16 v[140:143], a[52:55], v[236:239], v[140:143]\n"
      "v_mfma_f32_16x16x32_bf16 v[152:155], a[76:79], v[236:239], v[152:155]\n"
      "ds_read_b128 v[210:213], v0 offset:8192\n"
      "v_mfma_f32_16x16x32_bf16 v[148:151], a[100:103], v[236:239], v[148:151]\n"
      "s_waitcnt lgkmcnt(1)\n"
      "v_mfma_f32_16x16x32_bf16 v[162:165], a[124:127], v[236:239], v[162:165]\n"
      "v_mfma_f32_16x16x32_bf16 v[240:243], a[8:11], v[244:247], v[240:243]\n"
      "v_mfma_f32_16x16x32_bf16 v[184:187], a[32:35], v[244:247], v[184:187]\n"
      "v_mfma_f32_16x16x32_bf16 v[218:221], a[56:59], v[244:247], v[218:221]\n"
      "v_mfma_f32_16x16x32_bf16 v[194:197], a[80:83], v[244:247], v[194:197]\n"
      "ds_read_b128 v[236:239], v0 offset:10240\n"
      "v_mfma_f32_16x16x32_bf16 v[136:139], a[104:107], v[244:247], v[136:139]\n"
      "v_mfma_f32_16x16x32_bf16 v[214:217], a[128:131], v[244:247], v[214:217]\n"
      "v_mfma_f32_16x16x32_bf16 v[116:119], a[8:11], v[102:105], v[116:119]\n"
      "v_mfma_f32_16x16x32_bf16 v[180:183], a[32:35], v[102:105], v[180:183]\n"
      "v_mfma_f32_16x16x32_bf16 v[188:191], a[56:59], v[102:105], v[188:191]\n"
      "v_mfma_f32_16x16x32_bf16 v[112:115], a[80:83], v[102:105], v[112:115]\n"
      "ds_read_b128 v[244:247], v81 offset:4096\n"
      "v_mfma_f32_16x16x32_bf16 v[106:109], a[104:107], v[102:105], v[106:109]\n"
      "s_waitcnt lgkmcnt(1)\n"
      "v_mfma_f32_16x16x32_bf16 v[222:225], a[128:131], v[102:105], v[222:225]\n"
      "v_mfma_f32_16x16x32_bf16 v[206:209], a[8:11], v[210:213], v[206:209]\n"
      "s_waitcnt vmcnt(12)\n"
      "s_barrier\n"
      "s_mov_b32 s78, 0\n"
      "v_mfma_f32_16x16x32_bf16 v[198:201], a[32:35], v[210:213], v[198:201]\n"
      "buffer_load_dwordx4 v132, s[28:31], s78 offen offset:768  lds\n"
      "v_mfma_f32_16x16x32_bf16 v[166:169], a[56:59], v[210:213], v[166:169]\n"
      "s_add_u32 m0, s33, 52352\n"
      "ds_read_b64_tr_b16 a[12:13], v131 offset:24576\n"
      "v_mfma_f32_16x16x32_bf16 v[124:127], a[80:83], v[210:213], v[124:127]\n"
      "buffer_load_dwordx4 v132, s[28:31], s78 offen offset:896  lds\n"
      "v_mfma_f32_16x16x32_bf16 v[156:159], a[104:107], v[210:213], v[156:159]\n"
      "ds_read_b64_tr_b16 a[14:15], v131 offset:25088\n"
      "ds_read_b64_tr_b16 a[36:37], v178 offset:24576\n"
      "v_mfma_f32_16x16x32_bf16 v[120:123], a[128:131], v[210:213], v[120:123]\n"
      "ds_read_b64_tr_b16 a[38:39], v178 offset:25088\n"
      "ds_read_b64_tr_b16 a[60:61], v177 offset:24576\n"
      "v_mfma_f32_16x16x32_bf16 v[144:147], a[8:11], v[236:239], v[144:147]\n"
      "ds_read_b64_tr_b16 a[62:63], v177 offset:25088\n"
      "ds_read_b64_tr_b16 a[84:85], v131 offset:36864\n"
      "v_mfma_f32_16x16x32_bf16 v[172:175], a[32:35], v[236:239], v[172:175]\n"
      "ds_read_b64_tr_b16 a[86:87], v131 offset:37376\n"
      "ds_read_b64_tr_b16 a[108:109], v178 offset:36864\n"
      "v_mfma_f32_16x16x32_bf16 v[140:143], a[56:59], v[236:239], v[140:143]\n"
      "ds_read_b64_tr_b16 a[110:111], v178 offset:37376\n"
      "ds_read_b64_tr_b16 a[132:133], v177 offset:36864\n"
      "v_mfma_f32_16x16x32_bf16 v[152:155], a[80:83], v[236:239], v[152:155]\n"
      "ds_read_b64_tr_b16 a[134:135], v177 offset:37376\n"
      "s_add_u32 m0, s33, 56320\n"
      "v_mfma_f32_16x16x32_bf16 v[148:151], a[104:107], v[236:239], v[148:151]\n"
      "buffer_load_dwordx4 v132, s[28:31], s78 offen offset:1024  lds\n"
      "v_mfma_f32_16x16x32_bf16 v[162:165], a[128:131], v[236:239], v[162:165]\n"
      "s_waitcnt lgkmcnt(6)\n"
      "s_add_u32 m0, s33, 60672\n"
      "s_mov_b32 s78, 36864\n"
      "v_mfma_f32_16x16x32_bf16 v[240:243], a[12:15], v[244:247], v[240:243]\n"
      "buffer_load_dwordx4 v132, s[28:31], s78 offen offset:768  lds\n"
      "v_mfma_f32_16x16x32_bf16 v[184:187], a[36:39], v[244:247], v[184:187]\n"
      "s_add_u32 m0, s33, 64640\n"
      "ds_read_b128 v[236:239], v1 offset:6144\n"
      "v_mfma_f32_16x16x32_bf16 v[218:221], a[60:63], v[244:247], v[218:221]\n"
      "s_waitcnt lgkmcnt(2)\n"
      "buffer_load_dwordx4 v132, s[28:31], s78 offen offset:896  lds\n"
      "v_mfma_f32_16x16x32_bf16 v[194:197], a[84:87], v[244:247], v[194:197]\n"
      "s_add_u32 m0, s33, 68608\n"
      "ds_read_b128 v[210:213], v1 offset:8192\n"
      "v_mfma_f32_16x16x32_bf16 v[136:139], a[108:111], v[244:247], v[136:139]\n"
      "s_waitcnt lgkmcnt(2)\n"
      "buffer_load_dwordx4 v132, s[28:31], s78 offen offset:1024  lds\n"
      "v_mfma_f32_16x16x32_bf16 v[214:217], a[132:135], v[244:247], v[214:217]\n"
      "s_add_u32 m0, s33, 72960\n"
      "ds_read_b128 v[244:247], v1 offset:10240\n"
      "s_waitcnt lgkmcnt(2)\n"
      "v_mfma_f32_16x16x32_bf16 v[116:119], a[12:15], v[236:239], v[116:119]\n"
      "v_mfma_f32_16x16x32_bf16 v[180:183], a[36:39], v[236:239], v[180:183]\n"
      "v_mfma_f32_16x16x32_bf16 v[188:191], a[60:63], v[236:239], v[188:191]\n"
      "v_mfma_f32_16x16x32_bf16 v[112:115], a[84:87], v[236:239], v[112:115]\n"
      "ds_read_b128 v[102:105], v193 offset:8192\n"
      "v_mfma_f32_16x16x32_bf16 v[106:109], a[108:111], v[236:239], v[106:109]\n"
      "s_waitcnt lgkmcnt(2)\n"
      "v_mfma_f32_16x16x32_bf16 v[222:225], a[132:135], v[236:239], v[222:225]\n"
      "v_mfma_f32_16x16x32_bf16 v[206:209], a[12:15], v[210:213], v[206:209]\n"
      "v_mfma_f32_16x16x32_bf16 v[198:201], a[36:39], v[210:213], v[198:201]\n"
      "v_mfma_f32_16x16x32_bf16 v[166:169], a[60:63], v[210:213], v[166:169]\n"
      "v_mfma_f32_16x16x32_bf16 v[124:127], a[84:87], v[210:213], v[124:127]\n"
      "ds_read_b128 v[236:239], v0 offset:12288\n"
      "v_mfma_f32_16x16x32_bf16 v[156:159], a[108:111], v[210:213], v[156:159]\n"
      "s_waitcnt lgkmcnt(1)\n"
      "v_mfma_f32_16x16x32_bf16 v[120:123], a[132:135], v[210:213], v[120:123]\n"
      "v_mfma_f32_16x16x32_bf16 v[144:147], a[12:15], v[244:247], v[144:147]\n"
      "v_mfma_f32_16x16x32_bf16 v[172:175], a[36:39], v[244:247], v[172:175]\n"
      "v_mfma_f32_16x16x32_bf16 v[140:143], a[60:63], v[244:247], v[140:143]\n"
      "v_mfma_f32_16x16x32_bf16 v[152:155], a[84:87], v[244:247], v[152:155]\n"
      "ds_read_b128 v[210:213], v0 offset:14336\n"
      "v_mfma_f32_16x16x32_bf16 v[148:151], a[108:111], v[244:247], v[148:151]\n"
      "v_mfma_f32_16x16x32_bf16 v[162:165], a[132:135], v[244:247], v[162:165]\n"
      "s_waitcnt vmcnt(12)\n"
      "s_barrier\n"
      "ds_read_b64_tr_b16 a[16:17], v203\n"
      "ds_read_b64_tr_b16 a[18:19], v203 offset:512\n"
      "ds_read_b64_tr_b16 a[40:41], v205\n"
      "ds_read_b64_tr_b16 a[42:43], v205 offset:512\n"
      "ds_read_b64_tr_b16 a[64:65], v202\n"
      "ds_read_b64_tr_b16 a[66:67], v202 offset:512\n"
      "ds_read_b64_tr_b16 a[88:89], v203 offset:12288\n"
      "ds_read_b64_tr_b16 a[90:91], v203 offset:12800\n"
      "ds_read_b64_tr_b16 a[112:113], v205 offset:12288\n"
      "ds_read_b64_tr_b16 a[114:115], v205 offset:12800\n"
      "ds_read_b64_tr_b16 a[136:137], v202 offset:12288\n"
      "ds_read_b64_tr_b16 a[138:139], v202 offset:12800\n"
      "s_mov_b32 s79, 73728\n"
      "buffer_load_dwordx4 v132, s[28:31], s79 offen offset:768  lds\n"
      "s_waitcnt lgkmcnt(9)\n"
      "v_mfma_f32_16x16x32_bf16 v[240:243], a[16:19], v[102:105], v[240:243]\n"
      "s_waitcnt lgkmcnt(8)\n"
      "s_add_u32 m0, s33, 76928\n"
      "v_mfma_f32_16x16x32_bf16 v[184:187], a[40:43], v[102:105], v[184:187]\n"
      "s_waitcnt lgkmcnt(6)\n"
      "buffer_load_dwordx4 v132, s[28:31], s79 offen offset:896  lds\n"
      "v_mfma_f32_16x16x32_bf16 v[218:221], a[64:67], v[102:105], v[218:221]\n"
      "s_waitcnt lgkmcnt(3)\n"
      "s_add_u32 m0, s33, 80896\n"
      "v_mfma_f32_16x16x32_bf16 v[194:197], a[88:91], v[102:105], v[194:197]\n"
      "s_waitcnt lgkmcnt(1)\n"
      "buffer_load_dwordx4 v132, s[28:31], s79 offen offset:1024  lds\n"
      "v_mfma_f32_16x16x32_bf16 v[136:139], a[112:115], v[102:105], v[136:139]\n"
      "s_waitcnt lgkmcnt(0)\n"
      "s_add_u32 m0, s33, 85248\n"
      "s_mov_b32 s79, 110592\n"
      "v_mfma_f32_16x16x32_bf16 v[214:217], a[136:139], v[102:105], v[214:217]\n"
      "buffer_load_dwordx4 v132, s[28:31], s79 offen offset:768  lds\n"
      "v_mfma_f32_16x16x32_bf16 v[116:119], a[16:19], v[236:239], v[116:119]\n"
      "s_add_u32 m0, s33, 89216\n"
      "ds_read_b128 v[244:247], v0 offset:16384\n"
      "v_mfma_f32_16x16x32_bf16 v[180:183], a[40:43], v[236:239], v[180:183]\n"
      "buffer_load_dwordx4 v132, s[28:31], s79 offen offset:896  lds\n"
      "v_mfma_f32_16x16x32_bf16 v[188:191], a[64:67], v[236:239], v[188:191]\n"
      "s_add_u32 m0, s33, 93184\n"
      "v_mfma_f32_16x16x32_bf16 v[112:115], a[88:91], v[236:239], v[112:115]\n"
      "buffer_load_dwordx4 v132, s[28:31], s79 offen offset:1024  lds\n"
      "v_mfma_f32_16x16x32_bf16 v[106:109], a[112:115], v[236:239], v[106:109]\n"
      "s_add_u32 m0, s33, -768\n"
      "ds_read_b128 v[102:105], v81 offset:8192\n"
      "v_mfma_f32_16x16x32_bf16 v[222:225], a[136:139], v[236:239], v[222:225]\n"
      "v_mfma_f32_16x16x32_bf16 v[206:209], a[16:19], v[210:213], v[206:209]\n"
      "v_mfma_f32_16x16x32_bf16 v[198:201], a[40:43], v[210:213], v[198:201]\n"
      "v_mfma_f32_16x16x32_bf16 v[166:169], a[64:67], v[210:213], v[166:169]\n"
      "v_mfma_f32_16x16x32_bf16 v[124:127], a[88:91], v[210:213], v[124:127]\n"
      "ds_read_b128 v[236:239], v1 offset:12288\n"
      "v_mfma_f32_16x16x32_bf16 v[156:159], a[112:115], v[210:213], v[156:159]\n"
      "s_waitcnt lgkmcnt(2)\n"
      "v_mfma_f32_16x16x32_bf16 v[120:123], a[136:139], v[210:213], v[120:123]\n"
      "v_mfma_f32_16x16x32_bf16 v[144:147], a[16:19], v[244:247], v[144:147]\n"
      "v_mfma_f32_16x16x32_bf16 v[172:175], a[40:43], v[244:247], v[172:175]\n"
      "v_mfma_f32_16x16x32_bf16 v[140:143], a[64:67], v[244:247], v[140:143]\n"
      "v_mfma_f32_16x16x32_bf16 v[152:155], a[88:91], v[244:247], v[152:155]\n"
      "ds_read_b128 v[210:213], v1 offset:14336\n"
      "v_or3_b32 v232, v232, v233, v234\n"
      "v_mfma_f32_16x16x32_bf16 v[148:151], a[112:115], v[244:247], v[148:151]\n"
      "s_waitcnt lgkmcnt(1)\n"
      "v_or3_b32 v96, v96, v97, v98\n"
      "v_or3_b32 v232, v232, v235, v135\n"
      "v_mfma_f32_16x16x32_bf16 v[162:165], a[136:139], v[244:247], v[162:165]\n"
      "v_or3_b32 v96, v96, v99, v134\n"
      "s_waitcnt vmcnt(12)\n"
      "s_barrier\n"
      "ds_read_b64_tr_b16 a[20:21], v203 offset:24576\n"
      "ds_read_b64_tr_b16 a[22:23], v203 offset:25088\n"
      "ds_read_b64_tr_b16 a[44:45], v205 offset:24576\n"
      "ds_read_b64_tr_b16 a[46:47], v205 offset:25088\n"
      "ds_read_b64_tr_b16 a[68:69], v202 offset:24576\n"
      "ds_read_b64_tr_b16 a[70:71], v202 offset:25088\n"
      "ds_read_b64_tr_b16 a[92:93], v203 offset:36864\n"
      "ds_read_b64_tr_b16 a[94:95], v203 offset:37376\n"
      "ds_read_b64_tr_b16 a[116:117], v205 offset:36864\n"
      "ds_read_b64_tr_b16 a[118:119], v205 offset:37376\n"
      "ds_read_b64_tr_b16 a[140:141], v202 offset:36864\n"
      "ds_read_b64_tr_b16 a[142:143], v202 offset:37376\n"
      "s_mov_b32 s78, 147456\n"
      "buffer_load_dwordx4 v132, s[28:31], s78 offen offset:768  lds\n"
      "s_waitcnt lgkmcnt(9)\n"
      "v_mfma_f32_16x16x32_bf16 v[240:243], a[20:23], v[102:105], v[240:243]\n"
      "s_waitcnt lgkmcnt(8)\n"
      "s_add_u32 m0, s33, 3200\n"
      "v_and_b32 v232, 0x7fff7fff, v232\n"
      "v_mfma_f32_16x16x32_bf16 v[184:187], a[44:47], v[102:105], v[184:187]\n"
      "s_waitcnt lgkmcnt(6)\n"
      "buffer_load_dwordx4 v132, s[28:31], s78 offen offset:896  lds\n"
      "v_mfma_f32_16x16x32_bf16 v[218:221], a[68:71], v[102:105], v[218:221]\n"
      "s_waitcnt lgkmcnt(3)\n"
      "s_add_u32 m0, s33, 7168\n"
      "v_fma_f32 v233, v240, v240, s51\n"
      "v_mfma_f32_16x16x32_bf16 v[194:197], a[92:95], v[102:105], v[194:197]\n"
      "s_waitcnt lgkmcnt(1)\n"
      "buffer_load_dwordx4 v132, s[28:31], s78 offen offset:1024  lds\n"
      "v_mfma_f32_16x16x32_bf16 v[136:139], a[116:119], v[102:105], v[136:139]\n"
      "s_waitcnt lgkmcnt(0)\n"
      "s_add_u32 m0, s33, 11520\n"
      "s_mov_b32 s78, 184320\n"
      "v_mfma_f32_16x16x32_bf16 v[214:217], a[140:143], v[102:105], v[214:217]\n"
      "buffer_load_dwordx4 v132, s[28:31], s78 offen offset:768  lds\n"
      "v_mfma_f32_16x16x32_bf16 v[116:119], a[20:23], v[236:239], v[116:119]\n"
      "s_add_u32 m0, s33, 15488\n"
      "v_fma_f32 v97, v194, v194, s51\n"
      "buffer_load_dwordx4 v132, s[28:31], s78 offen offset:896  lds\n"
      "v_mfma_f32_16x16x32_bf16 v[180:183], a[44:47], v[236:239], v[180:183]\n"
      "s_add_u32 m0, s33, 19456\n"
      "v_fmac_f32 v233, v241, v241\n"
      "buffer_load_dwordx4 v132, s[28:31], s78 offen offset:1024  lds\n"
      "v_mfma_f32_16x16x32_bf16 v[188:191], a[68:71], v[236:239], v[188:191]\n"
      "s_add_u32 m0, s33, 24576\n"
      "v_fmac_f32 v97, v195, v195\n"
      "v_fma_f32 v227, v116, v116, s51\n"
      "v_mfma_f32_16x16x32_bf16 v[112:115], a[92:95], v[236:239], v[112:115]\n"
      "ds_read_b128 v[244:247], v1 offset:16384\n"
      "v_fmac_f32_e64 v233, v242, v242\n"
      "v_mfma_f32_16x16x32_bf16 v[106:109], a[116:119], v[236:239], v[106:109]\n"
      "ds_read_b128 v[132:135], v193 offset:2048\n"
      "v_fmac_f32_e64 v97, v196, v196\n"
      "v_mfma_f32_16x16x32_bf16 v[222:225], a[140:143], v[236:239], v[222:225]\n"
      "ds_read_b128 v[234:237], v81 offset:2048\n"
      "v_fmac_f32_e64 v227, v117, v117\n"
      "v_mfma_f32_16x16x32_bf16 v[206:209], a[20:23], v[210:213], v[206:209]\n"
      "ds_write_b32 v31, v232 offset:2048\n"
      "v_mfma_f32_16x16x32_bf16 v[198:201], a[44:47], v[210:213], v[198:201]\n"
      "v_fma_f32 v226, v112, v112, s51\n"
      "v_fmac_f32 v226, v113, v113\n"
      "v_mfma_f32_16x16x32_bf16 v[166:169], a[68:71], v[210:213], v[166:169]\n"
      "v_fmac_f32 v233, v243, v243\n"
      "v_fmac_f32 v97, v197, v197\n"
      "v_mfma_f32_16x16x32_bf16 v[124:127], a[92:95], v[210:213], v[124:127]\n"
      "v_fma_f32 v192, v206, v206, s51\n"
      "v_fmac_f32 v227, v118, v118\n"
      "v_mfma_f32_16x16x32_bf16 v[156:159], a[116:119], v[210:213], v[156:159]\n"
      "s_waitcnt lgkmcnt(2)\n"
      "v_fmac_f32 v226, v114, v114\n"
      "v_fmac_f32 v192, v207, v207\n"
      "v_mfma_f32_16x16x32_bf16 v[120:123], a[140:143], v[210:213], v[120:123]\n"
      "v_fmac_f32 v233, v184, v184\n"
      "v_fma_f32 v77, v124, v124, s51\n"
      "v_mfma_f32_16x16x32_bf16 v[144:147], a[20:23], v[244:247], v[144:147]\n"
      "v_fmac_f32 v77, v125, v125\n"
      "v_fmac_f32 v97, v136, v136\n"
      "v_mfma_f32_16x16x32_bf16 v[172:175], a[44:47], v[244:247], v[172:175]\n"
      "v_fmac_f32 v227, v119, v119\n"
      "v_fmac_f32 v226, v115, v115\n"
      "v_mfma_f32_16x16x32_bf16 v[140:143], a[68:71], v[244:247], v[140:143]\n"
      "v_fmac_f32_e64 v192, v208, v208\n"
      "v_fmac_f32_e64 v77, v126, v126\n"
      "v_mfma_f32_16x16x32_bf16 v[152:155], a[92:95], v[244:247], v[152:155]\n"
      "ds_read_b128 v[210:213], v193 offset:6144\n"
      "v_fma_f32 v111, v144, v144, s51\n"
      "v_mfma_f32_16x16x32_bf16 v[148:151], a[116:119], v[244:247], v[148:151]\n"
      "s_waitcnt vmcnt(12)\n"
      "s_barrier\n"
      "s_mov_b32 s79, 0\n"
      "v_mfma_f32_16x16x32_bf16 v[162:165], a[140:143], v[244:247], v[162:165]\n"
      "buffer_load_dwordx4 v176, s[36:39], s79 offen  lds\n"
      "v_mfma_f32_16x16x32_bf16 v[98:101], a[0:3], v[132:135], 0\n"
      "s_waitcnt lgkmcnt(1)\n"
      "ds_read_b64_tr_b16 a[144:145], v131\n"
      "v_mfma_f32_16x16x32_bf16 v[98:101], a[4:7], v[234:237], v[98:101]\n"
      "ds_read_b64_tr_b16 a[146:147], v131 offset:512\n"
      "ds_read_b64_tr_b16 a[168:169], v178\n"
      "v_mfma_f32_16x16x32_bf16 v[252:255], a[24:27], v[132:135], 0\n"
      "ds_read_b64_tr_b16 a[170:171], v178 offset:512\n"
      "ds_read_b64_tr_b16 a[192:193], v177\n"
      "v_mfma_f32_16x16x32_bf16 v[252:255], a[28:31], v[234:237], v[252:255]\n"
      "ds_read_b64_tr_b16 a[194:195], v177 offset:512\n"
      "ds_read_b64_tr_b16 a[148:149], v131 offset:12288\n"
      "v_mfma_f32_16x16x32_bf16 v[248:251], a[48:51], v[132:135], 0\n"
      "ds_read_b64_tr_b16 a[150:151], v131 offset:12800\n"
      "ds_read_b64_tr_b16 a[172:173], v178 offset:12288\n"
      "v_mfma_f32_16x16x32_bf16 v[248:251], a[52:55], v[234:237], v[248:251]\n"
      "ds_read_b64_tr_b16 a[174:175], v178 offset:12800\n"
      "ds_read_b64_tr_b16 a[196:197], v177 offset:12288\n"
      "v_mfma_f32_16x16x32_bf16 v[228:231], a[72:75], v[132:135], 0\n"
      "s_waitcnt lgkmcnt(9)\n"
      "ds_read_b64_tr_b16 a[198:199], v177 offset:12800\n"
      "v_mfma_f32_16x16x32_bf16 v[228:231], a[76:79], v[234:237], v[228:231]\n"
      "s_waitcnt lgkmcnt(8)\n"
      "s_add_u32 m0, s33, 28544\n"
      "v_fma_f32 v84, v152, v152, s51\n"
      "buffer_load_dwordx4 v176, s[36:39], s79 offen offset:128  lds\n"
      "s_add_u32 m0, s33, 32512\n"
      "v_fmac_f32_e64 v111, v145, v145\n"
      "buffer_load_dwordx4 v176, s[36:39], s79 offen offset:256  lds\n"
      "s_add_u32 m0, s33, 36864\n"
      "s_mov_b32 s79, 12288\n"
      "buffer_load_dwordx4 v176, s[36:39], s79 offen  lds\n"
      "s_add_u32 m0, s33, 40832\n"
      "v_fmac_f32_e64 v84, v153, v153\n"
      "buffer_load_dwordx4 v176, s[36:39], s79 offen offset:128  lds\n"
      "s_add_u32 m0, s33, 44800\n"
      "v_fmac_f32 v233, v185, v185\n"
      "buffer_load_dwordx4 v176, s[36:39], s79 offen offset:256  lds\n"
      "s_add_u32 m0, s33, 49152\n"
      "v_fmac_f32 v97, v137, v137\n"
      "v_fmac_f32 v111, v146, v146\n"
      "v_fmac_f32 v84, v154, v154\n"
      "v_fmac_f32 v227, v180, v180\n"
      "v_fmac_f32 v226, v106, v106\n"
      "v_fmac_f32 v192, v209, v209\n"
      "v_fmac_f32 v77, v127, v127\n"
      "v_fmac_f32 v233, v186, v186\n"
      "v_fmac_f32 v97, v138, v138\n"
      "v_fmac_f32 v111, v147, v147\n"
      "v_fmac_f32 v84, v155, v155\n"
      "v_fmac_f32 v227, v181, v181\n"
      "v_fmac_f32 v226, v107, v107\n"
      "v_fmac_f32 v192, v198, v198\n"
      "v_fmac_f32 v77, v156, v156\n"
      "v_fmac_f32 v233, v187, v187\n"
      "v_fmac_f32 v97, v139, v139\n"
      "v_fmac_f32 v111, v172, v172\n"
      "v_fmac_f32 v84, v148, v148\n"
      "v_fmac_f32 v227, v182, v182\n"
      "v_fmac_f32 v226, v108, v108\n"
      "v_fmac_f32 v192, v199, v199\n"
      "v_fmac_f32 v77, v157, v157\n"
      "v_fmac_f32 v233, v218, v218\n"
      "v_fmac_f32 v97, v214, v214\n"
      "v_fmac_f32 v111, v173, v173\n"
      "v_fmac_f32 v84, v149, v149\n"
      "v_fmac_f32 v227, v183, v183\n"
      "v_fmac_f32 v226, v109, v109\n"
      "v_fmac_f32 v192, v200, v200\n"
      "v_fmac_f32 v77, v158, v158\n"
      "v_fmac_f32 v233, v219, v219\n"
      "v_fmac_f32 v97, v215, v215\n"
      "v_fmac_f32 v111, v174, v174\n"
      "v_fmac_f32 v84, v150, v150\n"
      "v_fmac_f32 v227, v188, v188\n"
      "v_fmac_f32 v226, v222, v222\n"
      "v_fmac_f32 v192, v201, v201\n"
      "v_fmac_f32 v77, v159, v159\n"
      "v_fmac_f32 v233, v220, v220\n"
      "v_fmac_f32 v97, v216, v216\n"
      "v_fmac_f32 v111, v175, v175\n"
      "v_fmac_f32 v84, v151, v151\n"
      "v_fmac_f32 v227, v189, v189\n"
      "v_fmac_f32 v226, v223, v223\n"
      "v_fmac_f32 v192, v166, v166\n"
      "v_fmac_f32 v77, v120, v120\n"
      "v_fmac_f32 v233, v221, v221\n"
      "v_fmac_f32 v97, v217, v217\n"
      "v_fmac_f32 v111, v140, v140\n"
      "v_fmac_f32 v84, v162, v162\n"
      "v_fmac_f32 v227, v190, v190\n"
      "v_fmac_f32 v226, v224, v224\n"
      "v_fmac_f32 v192, v167, v167\n"
      "v_fmac_f32 v77, v121, v121\n"
      "v_fmac_f32 v111, v141, v141\n"
      "v_fmac_f32 v84, v163, v163\n"
      "v_fmac_f32 v227, v191, v191\n"
      "v_fmac_f32 v226, v225, v225\n"
      "v_fmac_f32 v192, v168, v168\n"
      "v_fmac_f32 v77, v122, v122\n"
      "v_fmac_f32 v111, v142, v142\n"
      "v_fmac_f32 v84, v164, v164\n"
      "v_fmac_f32 v192, v169, v169\n"
      "v_fmac_f32 v77, v123, v123\n"
      "v_fmac_f32 v111, v143, v143\n"
      "v_fmac_f32_e64 v84, v165, v165\n"
      "v_permlane16_swap_b32_e64 v233, v97\n"
      "v_permlane16_swap_b32_e64 v227, v226\n"
      "s_waitcnt lgkmcnt(0)\n"
      "s_waitcnt vmcnt(12)\n"
      "s_barrier\n"
      "ds_read_b64_tr_b16 a[152:153], v131 offset:24576\n"
      "ds_read_b64_tr_b16 a[154:155], v131 offset:25088\n"
      "ds_read_b64_tr_b16 a[176:177], v178 offset:24576\n"
      "ds_read_b64_tr_b16 a[178:179], v178 offset:25088\n"
      "ds_read_b64_tr_b16 a[200:201], v177 offset:24576\n"
      "ds_read_b64_tr_b16 a[202:203], v177 offset:25088\n"
      "ds_read_b64_tr_b16 a[156:157], v131 offset:36864\n"
      "ds_read_b64_tr_b16 a[158:159], v131 offset:37376\n"
      "v_mfma_f32_16x16x32_bf16 v[128:131], a[96:99], v[132:135], 0\n"
      "ds_read_b64_tr_b16 a[180:181], v178 offset:36864\n"
      "ds_read_b64_tr_b16 a[182:183], v178 offset:37376\n"
      "v_mfma_f32_16x16x32_bf16 v[128:131], a[100:103], v[234:237], v[128:131]\n"
      "ds_read_b64_tr_b16 a[204:205], v177 offset:36864\n"
      "ds_read_b64_tr_b16 a[206:207], v177 offset:37376\n"
      "v_mfma_f32_16x16x32_bf16 v[102:105], a[120:123], v[132:135], 0\n"
      "s_waitcnt lgkmcnt(10)\n"
      "s_mov_b32 s78, 24576\n"
      "buffer_load_dwordx4 v176, s[36:39], s78 offen  lds\n"
      "v_mfma_f32_16x16x32_bf16 v[102:105], a[124:127], v[234:237], v[102:105]\n"
      "s_waitcnt lgkmcnt(6)\n"
      "s_add_u32 m0, s33, 53120\n"
      "v_add_f32 v233, v233, v97\n"
      "v_mfma_f32_16x16x32_bf16 v[98:101], a[8:11], v[210:213], v[98:101]\n"
      "s_waitcnt lgkmcnt(4)\n"
      "ds_read_b128 v[234:237], v81 offset:6144\n"
      "buffer_load_dwordx4 v176, s[36:39], s78 offen offset:128  lds\n"
      "s_add_u32 m0, s33, 57088\n"
      "v_permlane16_swap_b32_e64 v192, v77\n"
      "buffer_load_dwordx4 v176, s[36:39], s78 offen offset:256  lds\n"
      "s_add_u32 m0, s33, 61440\n"
      "s_mov_b32 s78, 36864\n"
      "buffer_load_dwordx4 v176, s[36:39], s78 offen  lds\n"
      "s_add_u32 m0, s33, 65408\n"
      "v_mov_b32 v97, v233\n"
      "buffer_load_dwordx4 v176, s[36:39], s78 offen offset:128  lds\n"
      "s_waitcnt lgkmcnt(0)\n"
      "v_mfma_f32_16x16x32_bf16 v[98:101], a[12:15], v[234:237], v[98:101]\n"
      "s_add_u32 m0, s33, 69376\n"
      "v_permlane16_swap_b32_e64 v111, v84\n"
      "v_mfma_f32_16x16x32_bf16 v[252:255], a[32:35], v[210:213], v[252:255]\n"
      "buffer_load_dwordx4 v176, s[36:39], s78 offen offset:256  lds\n"
      "v_mfma_f32_16x16x32_bf16 v[252:255], a[36:39], v[234:237], v[252:255]\n"
      "s_add_u32 m0, s33, 73728\n"
      "v_add_f32 v227, v227, v226\n"
      "v_mov_b32 v226, v227\n"
      "v_mfma_f32_16x16x32_bf16 v[248:251], a[56:59], v[210:213], v[248:251]\n"
      "v_add_f32 v192, v192, v77\n"
      "v_add_f32 v111, v111, v84\n"
      "v_mfma_f32_16x16x32_bf16 v[248:251], a[60:63], v[234:237], v[248:251]\n"
      "v_mov_b32 v77, v192\n"
      "v_mov_b32 v84, v111\n"
      "v_mfma_f32_16x16x32_bf16 v[228:231], a[80:83], v[210:213], v[228:231]\n"
      "v_permlane32_swap_b32_e64 v233, v97\n"
      "v_mfma_f32_16x16x32_bf16 v[228:231], a[84:87], v[234:237], v[228:231]\n"
      "v_permlane32_swap_b32 v227, v226\n"
      "v_mfma_f32_16x16x32_bf16 v[128:131], a[104:107], v[210:213], v[128:131]\n"
      "v_add_f32 v233, v233, v97\n"
      "v_permlane32_swap_b32_e64 v192, v77\n"
      "v_mfma_f32_16x16x32_bf16 v[128:131], a[108:111], v[234:237], v[128:131]\n"
      "v_rsq_f32 v233, v233\n"
      "v_mfma_f32_16x16x32_bf16 v[102:105], a[128:131], v[210:213], v[102:105]\n"
      "ds_read_b128 v[244:247], v193 offset:10240\n"
      "v_permlane32_swap_b32 v111, v84\n"
      "v_mfma_f32_16x16x32_bf16 v[102:105], a[132:135], v[234:237], v[102:105]\n"
      "ds_read_b128 v[234:237], v81 offset:10240\n"
      "v_add_f32 v227, v227, v226\n"
      "v_rsq_f32 v227, v227\n"
      "v_add_f32 v192, v192, v77\n"
      "v_add_f32 v111, v111, v84\n"
      "v_rsq_f32 v192, v192\n"
      "v_mov_b32 v97, v233\n"
      "v_rsq_f32 v111, v111\n"
      "v_mov_b32 v226, v227\n"
      "v_mov_b32 v77, v192\n"
      "v_mov_b32 v84, v111\n"
      "v_permlane16_swap_b32 v233, v97\n"
      "v_permlane16_swap_b32 v227, v226\n"
      "s_waitcnt lgkmcnt(1)\n"
      "v_mfma_f32_16x16x32_bf16 v[98:101], a[16:19], v[244:247], v[98:101]\n"
      "v_mul_f32_e64 v233, s50, v233\n"
      "s_waitcnt vmcnt(12)\n"
      "s_barrier\n"
      "s_waitcnt lgkmcnt(0)\n"
      "v_mfma_f32_16x16x32_bf16 v[98:101], a[20:23], v[234:237], v[98:101]\n"
      "ds_read_b64_tr_b16 a[160:161], v203\n"
      "ds_read_b64_tr_b16 a[162:163], v203 offset:512\n"
      "v_mfma_f32_16x16x32_bf16 v[252:255], a[40:43], v[244:247], v[252:255]\n"
      "ds_read_b64_tr_b16 a[184:185], v205\n"
      "ds_read_b64_tr_b16 a[186:187], v205 offset:512\n"
      "v_mfma_f32_16x16x32_bf16 v[252:255], a[44:47], v[234:237], v[252:255]\n"
      "ds_read_b64_tr_b16 a[208:209], v202\n"
      "ds_read_b64_tr_b16 a[210:211], v202 offset:512\n"
      "v_mfma_f32_16x16x32_bf16 v[248:251], a[64:67], v[244:247], v[248:251]\n"
      "ds_read_b64_tr_b16 a[164:165], v203 offset:12288\n"
      "ds_read_b64_tr_b16 a[166:167], v203 offset:12800\n"
      "v_mfma_f32_16x16x32_bf16 v[248:251], a[68:71], v[234:237], v[248:251]\n"
      "ds_read_b64_tr_b16 a[188:189], v205 offset:12288\n"
      "ds_read_b64_tr_b16 a[190:191], v205 offset:12800\n"
      "v_mfma_f32_16x16x32_bf16 v[228:231], a[88:91], v[244:247], v[228:231]\n"
      "s_waitcnt lgkmcnt(9)\n"
      "ds_read_b64_tr_b16 a[212:213], v202 offset:12288\n"
      "v_mfma_f32_16x16x32_bf16 v[228:231], a[92:95], v[234:237], v[228:231]\n"
      "s_waitcnt lgkmcnt(8)\n"
      "ds_read_b64_tr_b16 a[214:215], v202 offset:12800\n"
      "v_mfma_f32_16x16x32_bf16 v[128:131], a[112:115], v[244:247], v[128:131]\n"
      "s_waitcnt lgkmcnt(8)\n"
      "s_mov_b32 s79, 49152\n"
      "buffer_load_dwordx4 v176, s[36:39], s79 offen  lds\n"
      "v_mfma_f32_16x16x32_bf16 v[128:131], a[116:119], v[234:237], v[128:131]\n"
      "s_waitcnt lgkmcnt(5)\n"
      "s_add_u32 m0, s33, 77696\n"
      "v_fma_f32 v85, v98, v98, s51\n"
      "v_mfma_f32_16x16x32_bf16 v[102:105], a[136:139], v[244:247], v[102:105]\n"
      "s_waitcnt lgkmcnt(3)\n"
      "buffer_load_dwordx4 v176, s[36:39], s79 offen offset:128  lds\n"
      "v_mfma_f32_16x16x32_bf16 v[102:105], a[140:143], v[234:237], v[102:105]\n"
      "s_waitcnt lgkmcnt(1)\n"
      "ds_read_b128 v[244:247], v193\n"
      "ds_read_b128 v[210:213], v193 offset:2048\n"
      "ds_read_b128 v[132:135], v81\n"
      "s_add_u32 m0, s33, 81664\n"
      "v_fmac_f32_e64 v85, v99, v99\n"
      "buffer_load_dwordx4 v176, s[36:39], s79 offen offset:256  lds\n"
      "s_add_u32 m0, s33, 86016\n"
      "s_mov_b32 s79, 61440\n"
      "buffer_load_dwordx4 v176, s[36:39], s79 offen  lds\n"
      "s_add_u32 m0, s33, 89984\n"
      "v_fmac_f32 v85, v100, v100\n"
      "buffer_load_dwordx4 v176, s[36:39], s79 offen offset:128  lds\n"
      "s_add_u32 m0, s33, 93952\n"
      "v_fmac_f32 v85, v101, v101\n"
      "buffer_load_dwordx4 v176, s[36:39], s79 offen offset:256  lds\n"
      "v_fma_f32 v160, v228, v228, s51\n"
      "v_fmac_f32 v160, v229, v229\n"
      "v_fmac_f32 v160, v230, v230\n"
      "v_fmac_f32 v160, v231, v231\n"
      "v_fmac_f32 v85, v252, v252\n"
      "v_fmac_f32 v160, v128, v128\n"
      "v_fmac_f32 v85, v253, v253\n"
      "v_fmac_f32 v160, v129, v129\n"
      "v_fmac_f32 v85, v254, v254\n"
      "v_fmac_f32 v160, v130, v130\n"
      "v_fmac_f32 v85, v255, v255\n"
      "v_fmac_f32 v160, v131, v131\n"
      "v_fmac_f32 v85, v248, v248\n"
      "v_fmac_f32 v160, v102, v102\n"
      "v_fmac_f32 v85, v249, v249\n"
      "v_fmac_f32 v160, v103, v103\n"
      "v_fmac_f32 v85, v250, v250\n"
      "v_fmac_f32 v160, v104, v104\n"
      "v_fmac_f32 v85, v251, v251\n"
      "v_fmac_f32 v160, v105, v105\n"
      "v_permlane16_swap_b32 v192, v77\n"
      "v_mul_f32 v240, v233, v240\n"
      "v_permlane16_swap_b32 v85, v160\n"
      "v_add_f32 v85, v85, v160\n"
      "v_mov_b32 v160, v85\n"
      "v_mul_f32 v241, v233, v241\n"
      "v_mul_f32 v194, v97, v194\n"
      "v_permlane32_swap_b32 v85, v160\n"
      "v_add_f32 v85, v85, v160\n"
      "v_rsq_f32 v85, v85\n"
      "v_mul_f32 v195, v97, v195\n"
      "v_mov_b32 v160, v85\n"
      "v_permlane16_swap_b32 v111, v84\n"
      "v_mul_f32 v227, s50, v227\n"
      "v_permlane16_swap_b32 v85, v160\n"
      "v_cvt_pk_bf16_f32 v240, v240, v241\n"
      "v_mul_f32 v242, v233, v242\n"
      "v_mul_f32 v243, v233, v243\n"
      "v_mul_f32 v218, v233, v218\n"
      "v_mul_f32 v219, v233, v219\n"
      "v_mul_f32 v214, v97, v214\n"
      "v_mul_f32 v215, v97, v215\n"
      "v_cvt_pk_bf16_f32 v194, v194, v195\n"
      "v_mul_f32 v196, v97, v196\n"
      "v_mul_f32 v197, v97, v197\n"
      "v_mul_f32 v112, v226, v112\n"
      "v_mul_f32 v113, v226, v113\n"
      "v_mul_f32 v116, v227, v116\n"
      "v_mul_f32 v117, v227, v117\n"
      "v_mul_f32 v192, s50, v192\n"
      "s_waitcnt lgkmcnt(0)\n"
      "s_waitcnt vmcnt(12)\n"
      "s_barrier\n"
      "ds_read_b64_tr_b16 a[216:217], v204 offset:24576\n"
      "ds_read_b64_tr_b16 a[218:219], v204 offset:26624\n"
      "ds_read_b64_tr_b16 a[240:241], v204 offset:24584\n"
      "ds_read_b64_tr_b16 a[242:243], v204 offset:26632\n"
      "ds_read_b64_tr_b16 a[220:221], v204 offset:36864\n"
      "ds_read_b64_tr_b16 a[222:223], v204 offset:38912\n"
      "ds_read_b64_tr_b16 a[244:245], v204 offset:36872\n"
      "ds_read_b64_tr_b16 a[246:247], v204 offset:38920\n"
      "s_add_u32 m0, s64, 18432\n"
      "v_cvt_pk_bf16_f32 v241, v242, v243\n"
      "buffer_load_dwordx4 v16, s[16:19], s45 offen  lds\n"
      "s_add_u32 m0, s64, 22528\n"
      "v_mul_f32 v184, v233, v184\n"
      "buffer_load_dwordx4 v17, s[16:19], s45 offen  lds\n"
      "s_add_u32 m0, s64, 26624\n"
      "v_mul_f32 v185, v233, v185\n"
      "buffer_load_dwordx4 v18, s[16:19], s45 offen  lds\n"
      "s_add_u32 m0, s64, 30720\n"
      "v_mul_f32_e64 v186, v233, v186\n"
      "buffer_load_dwordx4 v19, s[16:19], s45 offen  lds\n"
      "s_add_u32 m0, s65, 34816\n"
      "v_mul_f32 v187, v233, v187\n"
      "buffer_load_dwordx4 v15, s[16:19], s45 offen  lds\n"
      "v_cvt_pk_bf16_f32 v218, v218, v219\n"
      "v_mul_f32 v220, v233, v220\n"
      "v_mul_f32 v221, v233, v221\n"
      "ds_read_b64_tr_b16 v[232:233], v179 offset:24576\n"
      "v_cvt_pk_bf16_f32 v214, v214, v215\n"
      "v_mul_f32 v216, v97, v216\n"
      "v_mul_f32_e64 v217, v97, v217\n"
      "v_cvt_pk_bf16_f32 v195, v196, v197\n"
      "v_mul_f32 v136, v97, v136\n"
      "v_mul_f32 v137, v97, v137\n"
      "v_mul_f32 v138, v97, v138\n"
      "v_mul_f32 v139, v97, v139\n"
      "v_mul_f32 v85, s50, v85\n"
      "v_mul_f32 v111, s50, v111\n"
      "v_cvt_pk_bf16_f32 v112, v112, v113\n"
      "v_mul_f32 v114, v226, v114\n"
      "v_mul_f32 v115, v226, v115\n"
      "v_mul_f32 v222, v226, v222\n"
      "v_mul_f32 v223, v226, v223\n"
      "v_mul_f32 v124, v77, v124\n"
      "v_mul_f32 v125, v77, v125\n"
      "v_cvt_pk_bf16_f32 v116, v116, v117\n"
      "v_mul_f32 v118, v227, v118\n"
      "v_mul_f32 v119, v227, v119\n"
      "v_mul_f32 v188, v227, v188\n"
      "v_mul_f32 v189, v227, v189\n"
      "v_mul_f32_e64 v206, v192, v206\n"
      "v_mul_f32 v207, v192, v207\n"
      "v_cvt_pk_bf16_f32 v242, v184, v185\n"
      "ds_read_b64_tr_b16 v[234:235], v179 offset:26624\n"
      "v_cvt_pk_bf16_f32 v243, v186, v187\n"
      "ds_read_b64_tr_b16 v[236:237], v179 offset:36864\n"
      "ds_read_b64_tr_b16 v[238:239], v179 offset:38912\n"
      "ds_write_b128 v76, v[240:243]\n"
      "v_mfma_f32_16x16x32_bf16 v[184:187], a[144:147], v[244:247], 0\n"
      "s_waitcnt lgkmcnt(4)\n"
      "v_cvt_pk_bf16_f32 v219, v220, v221\n"
      "v_cvt_pk_bf16_f32 v215, v216, v217\n"
      "v_mfma_f32_16x16x32_bf16 v[176:179], a[168:171], v[244:247], 0\n"
      "ds_write_b64 v76, v[218:219] offset:16\n"
      "v_mfma_f32_16x16x32_bf16 v[218:221], a[192:195], v[244:247], 0\n"
      "s_waitcnt lgkmcnt(4)\n"
      "ds_write_b64 v76, v[214:215] offset:24\n"
      "v_mfma_f32_16x16x32_bf16 v[202:205], a[144:147], v[210:213], 0\n"
      "s_waitcnt lgkmcnt(3)\n"
      "v_cvt_pk_bf16_f32 v196, v136, v137\n"
      "v_cvt_pk_bf16_f32 v197, v138, v139\n"
      "v_mfma_f32_16x16x32_bf16 v[214:217], a[168:171], v[210:213], 0\n"
      "ds_write_b128 v76, v[194:197] offset:32\n"
      "v_mfma_f32_16x16x32_bf16 v[136:139], a[192:195], v[210:213], 0\n"
      "buffer_load_dwordx4 v[210:213], v27, s[24:27], s47 offen\n"
      "v_mfma_f32_16x16x32_bf16 v[184:187], a[148:151], v[132:135], v[184:187]\n"
      "v_mul_f32 v228, v160, v228\n"
      "v_mul_f32 v229, v160, v229\n"
      "v_mfma_f32_16x16x32_bf16 v[176:179], a[172:175], v[132:135], v[176:179]\n"
      "v_mul_f32 v98, v85, v98\n"
      "v_mul_f32 v99, v85, v99\n"
      "v_mfma_f32_16x16x32_bf16 v[218:221], a[196:199], v[132:135], v[218:221]\n"
      "buffer_load_dword v97, v29, s[24:27], s47 offen\n"
      "v_mul_f32 v152, v84, v152\n"
      "v_mul_f32 v153, v84, v153\n"
      "v_mul_f32 v144, v111, v144\n"
      "v_mul_f32 v145, v111, v145\n"
      "v_cvt_pk_bf16_f32 v113, v114, v115\n"
      "v_mul_f32 v106, v226, v106\n"
      "v_mul_f32 v107, v226, v107\n"
      "v_mul_f32 v108, v226, v108\n"
      "v_mul_f32 v109, v226, v109\n"
      "v_cvt_pk_bf16_f32 v222, v222, v223\n"
      "v_mul_f32 v224, v226, v224\n"
      "v_mul_f32 v225, v226, v225\n"
      "ds_read_b128 v[240:243], v81 offset:2048\n"
      "v_cvt_pk_bf16_f32 v124, v124, v125\n"
      "v_mul_f32 v126, v77, v126\n"
      "v_mul_f32 v127, v77, v127\n"
      "v_cvt_pk_bf16_f32 v117, v118, v119\n"
      "v_mul_f32 v180, v227, v180\n"
      "v_mul_f32 v181, v227, v181\n"
      "v_mul_f32 v120, v77, v120\n"
      "v_mul_f32 v121, v77, v121\n"
      "v_mul_f32 v182, v227, v182\n"
      "v_mul_f32 v183, v227, v183\n"
      "v_cvt_pk_bf16_f32 v188, v188, v189\n"
      "v_mul_f32 v190, v227, v190\n"
      "v_mul_f32 v191, v227, v191\n"
      "v_cvt_pk_bf16_f32 v206, v206, v207\n"
      "v_mul_f32 v208, v192, v208\n"
      "v_mul_f32 v209, v192, v209\n"
      "v_mul_f32 v166, v192, v166\n"
      "v_mul_f32 v167, v192, v167\n"
      "v_cvt_pk_bf16_f32 v228, v228, v229\n"
      "v_mul_f32 v230, v160, v230\n"
      "v_mul_f32 v231, v160, v231\n"
      "v_mul_f32 v102, v160, v102\n"
      "s_waitcnt lgkmcnt(0)\n"
      "v_mfma_f32_16x16x32_bf16 v[202:205], a[148:151], v[240:243], v[202:205]\n"
      "v_mul_f32_e64 v103, v160, v103\n"
      "v_cvt_pk_bf16_f32 v98, v98, v99\n"
      "v_mfma_f32_16x16x32_bf16 v[214:217], a[172:175], v[240:243], v[214:217]\n"
      "v_mul_f32 v100, v85, v100\n"
      "v_mul_f32 v101, v85, v101\n"
      "v_mfma_f32_16x16x32_bf16 v[136:139], a[196:199], v[240:243], v[136:139]\n"
      "ds_read_b128 v[240:243], v193 offset:4096\n"
      "v_mul_f32 v248, v85, v248\n"
      "v_mul_f32 v249, v85, v249\n"
      "v_cvt_pk_bf16_f32 v152, v152, v153\n"
      "v_mul_f32 v154, v84, v154\n"
      "v_mul_f32 v155, v84, v155\n"
      "v_mul_f32_e64 v162, v84, v162\n"
      "s_waitcnt vmcnt(13)\n"
      "s_barrier\n"
      "ds_read_b64_tr_b16 a[224:225], v170\n"
      "ds_read_b64_tr_b16 a[226:227], v170 offset:2048\n"
      "ds_read_b64_tr_b16 a[248:249], v170 offset:8\n"
      "ds_read_b64_tr_b16 a[250:251], v170 offset:2056\n"
      "ds_read_b64_tr_b16 a[228:229], v170 offset:12288\n"
      "ds_read_b64_tr_b16 a[230:231], v170 offset:14336\n"
      "ds_read_b64_tr_b16 a[252:253], v170 offset:12296\n"
      "s_waitcnt lgkmcnt(7)\n"
      "v_mfma_f32_16x16x32_bf16 v[184:187], a[152:155], v[240:243], v[184:187]\n"
      "ds_read_b64_tr_b16 a[254:255], v170 offset:14344\n"
      "v_mul_f32 v163, v84, v163\n"
      "v_mfma_f32_16x16x32_bf16 v[176:179], a[176:179], v[240:243], v[176:179]\n"
      "v_cvt_pk_bf16_f32 v144, v144, v145\n"
      "v_mul_f32 v146, v111, v146\n"
      "v_mfma_f32_16x16x32_bf16 v[218:221], a[200:203], v[240:243], v[218:221]\n"
      "s_waitcnt lgkmcnt(7)\n"
      "ds_read_b64_tr_b16 v[240:241], v171\n"
      "ds_read_b64_tr_b16 v[242:243], v171 offset:2048\n"
      "v_mul_f32_e64 v147, v111, v147\n"
      "v_cvt_pk_bf16_f32 v114, v106, v107\n"
      "ds_read_b64_tr_b16 v[244:245], v171 offset:12288\n"
      "v_cvt_pk_bf16_f32 v115, v108, v109\n"
      "ds_read_b64_tr_b16 v[246:247], v171 offset:14336\n"
      "v_mul_f32 v140, v111, v140\n"
      "v_mul_f32 v141, v111, v141\n"
      "v_cvt_pk_bf16_f32 v223, v224, v225\n"
      "v_cvt_pk_bf16_f32 v125, v126, v127\n"
      "v_mul_f32 v156, v77, v156\n"
      "v_mul_f32 v157, v77, v157\n"
      "v_mul_f32 v158, v77, v158\n"
      "v_mul_f32 v159, v77, v159\n"
      "v_cvt_pk_bf16_f32 v118, v180, v181\n"
      "ds_read_b128 v[224:227], v193 offset:6144\n"
      "v_cvt_pk_bf16_f32 v120, v120, v121\n"
      "v_mul_f32 v122, v77, v122\n"
      "v_mul_f32 v123, v77, v123\n"
      "v_cvt_pk_bf16_f32 v119, v182, v183\n"
      "ds_read_b128 v[180:183], v81 offset:4096\n"
      "v_cvt_pk_bf16_f32 v189, v190, v191\n"
      "v_cvt_pk_bf16_f32 v207, v208, v209\n"
      "v_mul_f32 v198, v192, v198\n"
      "v_mul_f32 v199, v192, v199\n"
      "v_mul_f32 v200, v192, v200\n"
      "v_mul_f32 v201, v192, v201\n"
      "v_cvt_pk_bf16_f32 v166, v166, v167\n"
      "v_mul_f32 v168, v192, v168\n"
      "v_mul_f32 v169, v192, v169\n"
      "v_cvt_pk_bf16_f32 v229, v230, v231\n"
      "v_mul_f32 v128, v160, v128\n"
      "v_mul_f32 v129, v160, v129\n"
      "v_mul_f32 v130, v160, v130\n"
      "v_mul_f32 v131, v160, v131\n"
      "v_cvt_pk_bf16_f32 v102, v102, v103\n"
      "s_waitcnt lgkmcnt(1)\n"
      "v_mfma_f32_16x16x32_bf16 v[202:205], a[152:155], v[224:227], v[202:205]\n"
      "v_mul_f32 v104, v160, v104\n"
      "v_mul_f32_e64 v105, v160, v105\n"
      "v_mfma_f32_16x16x32_bf16 v[214:217], a[176:179], v[224:227], v[214:217]\n"
      "ds_read_b128 v[132:135], v81 offset:6144\n"
      "v_cvt_pk_bf16_f32 v99, v100, v101\n"
      "v_mfma_f32_16x16x32_bf16 v[136:139], a[200:203], v[224:227], v[136:139]\n"
      "s_waitcnt lgkmcnt(1)\n"
      "v_mul_f32 v252, v85, v252\n"
      "v_mul_f32 v253, v85, v253\n"
      "v_mfma_f32_16x16x32_bf16 v[184:187], a[156:159], v[180:183], v[184:187]\n"
      "ds_read_b128 v[224:227], v193 offset:8192\n"
      "v_mul_f32 v254, v85, v254\n"
      "v_mfma_f32_16x16x32_bf16 v[176:179], a[180:183], v[180:183], v[176:179]\n"
      "v_mul_f32 v255, v85, v255\n"
      "v_cvt_pk_bf16_f32 v248, v248, v249\n"
      "v_mfma_f32_16x16x32_bf16 v[218:221], a[204:207], v[180:183], v[218:221]\n"
      "ds_read_b128 v[180:183], v193 offset:10240\n"
      "v_mul_f32 v250, v85, v250\n"
      "v_mul_f32 v251, v85, v251\n"
      "v_cvt_pk_bf16_f32 v153, v154, v155\n"
      "v_mul_f32 v148, v84, v148\n"
      "v_mul_f32 v149, v84, v149\n"
      "v_mul_f32_e64 v150, v84, v150\n"
      "s_waitcnt lgkmcnt(2)\n"
      "v_mfma_f32_16x16x32_bf16 v[202:205], a[156:159], v[132:135], v[202:205]\n"
      "v_mul_f32 v151, v84, v151\n"
      "v_cvt_pk_bf16_f32 v162, v162, v163\n"
      "v_mfma_f32_16x16x32_bf16 v[214:217], a[180:183], v[132:135], v[214:217]\n"
      "v_mul_f32 v164, v84, v164\n"
      "v_mul_f32 v165, v84, v165\n"
      "v_mfma_f32_16x16x32_bf16 v[136:139], a[204:207], v[132:135], v[136:139]\n"
      "s_waitcnt lgkmcnt(1)\n"
      "v_cvt_pk_bf16_f32 v145, v146, v147\n"
      "v_mul_f32_e64 v172, v111, v172\n"
      "v_mfma_f32_16x16x32_bf16 v[184:187], a[160:163], v[224:227], v[184:187]\n"
      "ds_read_b128 v[190:193], v81 offset:8192\n"
      "v_mul_f32_e64 v173, v111, v173\n"
      "v_mfma_f32_16x16x32_bf16 v[176:179], a[184:187], v[224:227], v[176:179]\n"
      "ds_read_b128 v[132:135], v81 offset:10240\n"
      "v_mul_f32 v174, v111, v174\n"
      "v_mfma_f32_16x16x32_bf16 v[218:221], a[208:211], v[224:227], v[218:221]\n"
      "s_waitcnt lgkmcnt(2)\n"
      "v_mul_f32_e64 v175, v111, v175\n"
      "v_cvt_pk_bf16_f32 v140, v140, v141\n"
      "v_mfma_f32_16x16x32_bf16 v[202:205], a[160:163], v[180:183], v[202:205]\n"
      "ds_read_b128 v[194:197], v0 offset:4096\n"
      "v_mul_f32_e64 v142, v111, v142\n"
      "v_mfma_f32_16x16x32_bf16 v[214:217], a[184:187], v[180:183], v[214:217]\n"
      "v_mul_f32 v143, v111, v143\n"
      "v_cvt_pk_bf16_f32 v126, v156, v157\n"
      "v_mfma_f32_16x16x32_bf16 v[136:139], a[208:211], v[180:183], v[136:139]\n"
      "ds_read_b128 v[106:109], v1 offset:4096\n"
      "v_cvt_pk_bf16_f32 v127, v158, v159\n"
      "v_cvt_pk_bf16_f32 v121, v122, v123\n"
      "s_waitcnt lgkmcnt(3)\n"
      "v_mfma_f32_16x16x32_bf16 v[184:187], a[164:167], v[190:193], v[184:187]\n"
      "ds_read_b128 v[156:159], v0 offset:10240\n"
      "v_cvt_pk_bf16_f32 v208, v198, v199\n"
      "v_mfma_f32_16x16x32_bf16 v[176:179], a[188:191], v[190:193], v[176:179]\n"
      "s_waitcnt vmcnt(7)\n"
      "s_barrier\n"
      "v_cvt_pk_bf16_f32 v209, v200, v201\n"
      "v_mfma_f32_16x16x32_bf16 v[218:221], a[212:215], v[190:193], v[218:221]\n"
      "s_waitcnt lgkmcnt(3)\n"
      "v_cvt_pk_bf16_f32 v167, v168, v169\n"
      "v_cvt_pk_bf16_f32 v230, v128, v129\n"
      "v_mfma_f32_16x16x32_bf16 v[202:205], a[164:167], v[132:135], v[202:205]\n"
      "ds_read_b64_tr_b16 a[232:233], v170 offset:24576\n"
      "v_cvt_pk_bf16_f32 v231, v130, v131\n"
      "v_mfma_f32_16x16x32_bf16 v[214:217], a[188:191], v[132:135], v[214:217]\n"
      "ds_read_b64_tr_b16 a[234:235], v170 offset:26624\n"
      "v_cvt_pk_bf16_f32 v103, v104, v105\n"
      "v_mfma_f32_16x16x32_bf16 v[136:139], a[212:215], v[132:135], v[136:139]\n"
      "s_waitcnt lgkmcnt(3)\n"
      "v_cvt_pk_bf16_f32 v100, v252, v253\n"
      "v_cvt_pk_bf16_f32 v101, v254, v255\n"
      "v_mfma_f32_16x16x32_bf16 v[130:133], v[194:197], a[144:147], 0\n"
      "ds_read_b64_tr_b16 v[224:225], v170 offset:24584\n"
      "v_cvt_pk_bf16_f32 v249, v250, v251\n"
      "v_mfma_f32_16x16x32_bf16 v[130:133], v[106:109], a[148:151], v[130:133]\n"
      "s_waitcnt lgkmcnt(3)\n"
      "v_cvt_pk_bf16_f32 v154, v148, v149\n"
      "v_cvt_pk_bf16_f32 v155, v150, v151\n"
      "v_mfma_f32_16x16x32_bf16 v[130:133], v[156:159], a[152:155], v[130:133]\n"
      "s_mov_b32 exec_lo, 0x1f001f\n"
      "s_mov_b32 exec_hi, 0x1f001f\n"
      "ds_write_b128 v76, v[98:101] offset:18688\n"
      "ds_write_b64 v76, v[248:249] offset:18704\n"
      "ds_write_b64 v76, v[102:103] offset:18712\n"
      "ds_write_b128 v76, v[228:231] offset:18720\n"
      "s_mov_b64 exec, -1\n"
      "ds_read_b64_tr_b16 v[226:227], v170 offset:26632\n"
      "ds_read_b64_tr_b16 v[248:249], v171 offset:24576\n"
      "ds_read_b64_tr_b16 v[250:251], v171 offset:26624\n"
      "ds_read_b64_tr_b16 a[236:237], v170 offset:36864\n"
      "ds_read_b64_tr_b16 a[238:239], v170 offset:38912\n"
      "ds_read_b64_tr_b16 v[228:229], v170 offset:36872\n"
      "ds_read_b64_tr_b16 v[230:231], v170 offset:38920\n"
      "ds_read_b64_tr_b16 v[252:253], v171 offset:36864\n"
      "ds_read_b64_tr_b16 v[254:255], v171 offset:38912\n"
      "ds_read_b128 v[190:193], v1 offset:10240\n"
      "ds_read_b128 v[168:171], v0 offset:16384\n"
      "ds_read_b128 v[198:201], v1 offset:16384\n"
      "v_cvt_pk_bf16_f32 v163, v164, v165\n"
      "v_cvt_pk_bf16_f32 v146, v172, v173\n"
      "v_cvt_pk_bf16_f32 v147, v174, v175\n"
      "v_cvt_pk_bf16_f32 v141, v142, v143\n"
      "v_and_b32 v96, 0x7fff7fff, v96\n"
      "ds_write_b32 v31, v96 offset:1024\n"
      "s_waitcnt lgkmcnt(0)\n"
      "s_barrier\n"
      "ds_read_b128 v[172:175], v9 offset:5632\n"
      "v_cvt_pk_bf16_f32 v184, v184, v185\n"
      "v_cvt_pk_bf16_f32 v176, v176, v177\n"
      "v_cvt_pk_bf16_f32 v218, v218, v219\n"
      "v_cvt_pk_bf16_f32 v185, v186, v187\n"
      "v_cvt_pk_bf16_f32 v177, v178, v179\n"
      "v_cvt_pk_bf16_f32 v219, v220, v221\n"
      "ds_write_b64 v80, v[184:185]\n"
      "s_waitcnt lgkmcnt(4)\n"
      "v_mfma_f32_16x16x32_bf16 v[130:133], v[190:193], a[156:159], v[130:133]\n"
      "ds_read_b128 v[98:101], v0 offset:2048\n"
      "ds_write_b64 v80, v[176:177] offset:32\n"
      "v_mfma_f32_16x16x32_bf16 v[130:133], v[168:171], a[160:163], v[130:133]\n"
      "s_waitcnt lgkmcnt(4)\n"
      "ds_read_b128 v[176:179], v1 offset:2048\n"
      "v_mfma_f32_16x16x32_bf16 v[130:133], v[198:201], a[164:167], v[130:133]\n"
      "ds_read_b128 v[148:151], v0 offset:8192\n"
      "ds_write_b64 v80, v[218:219] offset:64\n"
      "v_mfma_f32_16x16x32_bf16 v[218:221], v[194:197], a[168:171], 0\n"
      "ds_read_b128 v[184:187], v1 offset:8192\n"
      "v_cvt_pk_bf16_f32 v202, v202, v203\n"
      "v_mfma_f32_16x16x32_bf16 v[218:221], v[106:109], a[172:175], v[218:221]\n"
      "ds_read_b128 v[180:183], v0 offset:14336\n"
      "v_cvt_pk_bf16_f32 v214, v214, v215\n"
      "v_mfma_f32_16x16x32_bf16 v[218:221], v[156:159], a[176:179], v[218:221]\n"
      "ds_read_b128 v[102:105], v1 offset:14336\n"
      "v_cvt_pk_bf16_f32 v76, v130, v131\n"
      "v_mfma_f32_16x16x32_bf16 v[218:221], v[190:193], a[180:183], v[218:221]\n"
      "v_cvt_pk_bf16_f32 v77, v132, v133\n"
      "v_cvt_pk_bf16_f32 v136, v136, v137\n"
      "v_mfma_f32_16x16x32_bf16 v[218:221], v[168:171], a[184:187], v[218:221]\n"
      "v_cvt_pk_bf16_f32 v203, v204, v205\n"
      "v_cvt_pk_bf16_f32 v215, v216, v217\n"
      "v_mfma_f32_16x16x32_bf16 v[218:221], v[198:201], a[188:191], v[218:221]\n"
      "v_cvt_pk_bf16_f32 v137, v138, v139\n"
      "s_mov_b32 exec_lo, 0x1f001f\n"
      "s_mov_b32 exec_hi, 0x1f001f\n"
      "ds_write_b64 v80, v[202:203] offset:18688\n"
      "ds_write_b64 v80, v[214:215] offset:18720\n"
      "ds_write_b64 v80, v[136:137] offset:18752\n"
      "s_mov_b64 exec, -1\n"
      "v_mfma_f32_16x16x32_bf16 v[214:217], v[194:197], a[192:195], 0\n"
      "v_cvt_pk_bf16_f32 v80, v218, v219\n"
      "v_cvt_pk_bf16_f32 v81, v220, v221\n"
      "v_mfma_f32_16x16x32_bf16 v[214:217], v[106:109], a[196:199], v[214:217]\n"
      "s_waitcnt vmcnt(1)\n"
      "v_or3_b32 v210, v210, v211, v212\n"
      "s_waitcnt vmcnt(0)\n"
      "v_or3_b32 v210, v210, v213, v97\n"
      "v_mfma_f32_16x16x32_bf16 v[214:217], v[156:159], a[200:203], v[214:217]\n"
      "v_and_b32 v210, 0x7fff7fff, v210\n"
      "ds_write_b32 v31, v210\n"
      "v_mfma_f32_16x16x32_bf16 v[214:217], v[190:193], a[204:207], v[214:217]\n"
      "v_readlane_b32 s45, v37, s43\n"
      "s_waitcnt lgkmcnt(3)\n"
      "v_or3_b32 v172, v172, v173, v174\n"
      "v_mfma_f32_16x16x32_bf16 v[214:217], v[168:171], a[208:211], v[214:217]\n"
      "s_or_b32 s95, s45, s46\n"
      "s_or_b32 s95, s95, s48\n"
      "s_cmp_ge_u32 s95, 0x40000000\n"
      "s_cselect_b32 s94, -1, -2\n"
      "v_mfma_f32_16x16x32_bf16 v[214:217], v[198:201], a[212:215], v[214:217]\n"
      "s_cmp_gt_u32 s42, s44\n"
      "s_cselect_b32 s95, s94, s95\n"
      "v_or_b32_e64 v172, v172, v175\n"
      "v_mfma_f32_16x16x32_bf16 v[198:201], v[98:101], a[144:147], 0\n"
      "v_readlane_b32 s47, v38, s43\n"
      "s_add_u32 s43, s43, 1\n"
      "v_mfma_f32_16x16x32_bf16 v[198:201], v[176:179], a[148:151], v[198:201]\n"
      "s_and_b32 s47, s47, -4\n"
      "v_cvt_pk_bf16_f32 v84, v214, v215\n"
      "v_cvt_pk_bf16_f32 v85, v216, v217\n"
      "v_mfma_f32_16x16x32_bf16 v[198:201], v[148:151], a[152:155], v[198:201]\n"
      "v_mfma_f32_16x16x32_bf16 v[198:201], v[184:187], a[156:159], v[198:201]\n"
      "v_mfma_f32_16x16x32_bf16 v[198:201], v[180:183], a[160:163], v[198:201]\n"
      "v_mfma_f32_16x16x32_bf16 v[198:201], v[102:105], a[164:167], v[198:201]\n"
      "v_mfma_f32_16x16x32_bf16 v[214:217], v[98:101], a[168:171], 0\n"
      "ds_read_b128 v[210:213], v10 offset:32\n"
      "v_mfma_f32_16x16x32_bf16 v[214:217], v[176:179], a[172:175], v[214:217]\n"
      "v_mfma_f32_16x16x32_bf16 v[214:217], v[148:151], a[176:179], v[214:217]\n"
      "v_mfma_f32_16x16x32_bf16 v[214:217], v[184:187], a[180:183], v[214:217]\n"
      "v_mfma_f32_16x16x32_bf16 v[214:217], v[180:183], a[184:187], v[214:217]\n"
      "ds_read_b64 v[138:139], v10 offset:24\n"
      "v_mfma_f32_16x16x32_bf16 v[214:217], v[102:105], a[188:191], v[214:217]\n"
      "ds_read_b128 v[218:221], v0\n"
      "v_cvt_pk_bf16_f32 v201, v200, v201\n"
      "v_mfma_f32_16x16x32_bf16 v[194:197], v[98:101], a[192:195], 0\n"
      "ds_read_b128 v[202:205], v1\n"
      "v_cvt_pk_bf16_f32 v200, v198, v199\n"
      "v_mfma_f32_16x16x32_bf16 v[194:197], v[176:179], a[196:199], v[194:197]\n"
      "ds_read_b128 v[174:177], v0 offset:6144\n"
      "v_mfma_f32_16x16x32_bf16 v[194:197], v[148:151], a[200:203], v[194:197]\n"
      "ds_read_b128 v[108:111], v1 offset:6144\n"
      "v_cvt_pk_bf16_f32 v217, v216, v217\n"
      "v_mfma_f32_16x16x32_bf16 v[194:197], v[184:187], a[204:207], v[194:197]\n"
      "ds_read_b128 v[168:171], v0 offset:12288\n"
      "v_cvt_pk_bf16_f32 v216, v214, v215\n"
      "v_mfma_f32_16x16x32_bf16 v[194:197], v[180:183], a[208:211], v[194:197]\n"
      "ds_read_b128 v[180:183], v1 offset:12288\n"
      "v_mfma_f32_16x16x32_bf16 v[194:197], v[102:105], a[212:215], v[194:197]\n"
      "v_mfma_f32_16x16x32_bf16 v[190:193], v[112:115], v[116:119], v[40:43]\n"
      "v_mfma_f32_16x16x32_bf16 v[158:161], v[124:127], v[116:119], v[44:47]\n"
      "s_waitcnt lgkmcnt(3)\n"
      "v_mfma_f32_16x16x32_bf16 v[98:101], v[152:155], v[116:119], v[48:51]\n"
      "v_mfma_f32_16x16x32_bf16 v[128:131], v[210:213], v[116:119], v[24:27]\n"
      "s_nop 0\n"
      "s_nop 0\n"
      "v_mfma_f32_16x16x16_bf16 v[190:193], v[222:223], v[188:189], v[190:193]\n"
      "v_cvt_pk_bf16_f32 v197, v196, v197\n"
      "v_cvt_pk_bf16_f32 v196, v194, v195\n"
      "v_mfma_f32_16x16x16_bf16 v[158:161], v[120:121], v[188:189], v[158:161]\n"
      "v_mfma_f32_16x16x16_bf16 v[98:101], v[162:163], v[188:189], v[98:101]\n"
      "v_mfma_f32_16x16x16_bf16 v[128:131], v[138:139], v[188:189], v[128:131]\n"
      "v_mfma_f32_16x16x32_bf16 v[186:189], v[218:221], a[144:147], 0\n"
      "v_mfma_f32_16x16x32_bf16 v[186:189], v[202:205], a[148:151], v[186:189]\n"
      "s_waitcnt lgkmcnt(0)\n"
      "v_mfma_f32_16x16x32_bf16 v[186:189], v[174:177], a[152:155], v[186:189]\n"
      "v_mfma_f32_16x16x32_bf16 v[186:189], v[108:111], a[156:159], v[186:189]\n"
      "v_mfma_f32_16x16x32_bf16 v[186:189], v[168:171], a[160:163], v[186:189]\n"
      "v_mfma_f32_16x16x32_bf16 v[186:189], v[180:183], a[164:167], v[186:189]\n"
      "ds_read_b128 v[116:119], v9 offset:6656\n"
      "v_mfma_f32_16x16x32_bf16 v[134:137], v[218:221], a[168:171], 0\n"
      "v_mfma_f32_16x16x32_bf16 v[134:137], v[202:205], a[172:175], v[134:137]\n"
      "v_mfma_f32_16x16x32_bf16 v[134:137], v[174:177], a[176:179], v[134:137]\n"
      "v_mfma_f32_16x16x32_bf16 v[134:137], v[108:111], a[180:183], v[134:137]\n"
      "v_mfma_f32_16x16x32_bf16 v[134:137], v[168:171], a[184:187], v[134:137]\n"
      "v_mfma_f32_16x16x32_bf16 v[134:137], v[180:183], a[188:191], v[134:137]\n"
      "s_waitcnt lgkmcnt(0)\n"
      "v_or3_b32 v116, v116, v117, v118\n"
      "v_or_b32 v116, v116, v119\n"
      "v_mfma_f32_16x16x32_bf16 v[104:107], v[218:221], a[192:195], 0\n"
      "v_cmp_ne_u32 s[56:57], 0, v116\n"
      "v_cvt_pk_bf16_f32 v198, v186, v187\n"
      "v_mfma_f32_16x16x32_bf16 v[104:107], v[202:205], a[196:199], v[104:107]\n"
      "s_or_b64 vcc, s[56:57], s[54:55]\n"
      "s_cbranch_vccnz L_rare_1_%=\n"
      "L_back_2_%=:\n"
      "v_mfma_f32_16x16x32_bf16 v[104:107], v[174:177], a[200:203], v[104:107]\n"
      "ds_read_u16 v78, v11\n"
      "s_cbranch_vccnz L_rare_3_%=\n"
      "L_back_4_%=:\n"
      "v_mfma_f32_16x16x32_bf16 v[104:107], v[108:111], a[204:207], v[104:107]\n"
      "s_add_u32 s42, s42, 1\n"
      "v_exp_f32 v190, v190\n"
      "v_mfma_f32_16x16x32_bf16 v[104:107], v[168:171], a[208:211], v[104:107]\n"
      "ds_read_u16 v82, v11 offset:32\n"
      "v_exp_f32 v191, v191\n"
      "v_mfma_f32_16x16x32_bf16 v[104:107], v[180:183], a[212:215], v[104:107]\n"
      "v_exp_f32 v192, v192\n"
      "v_mfma_f32_16x16x32_bf16 v[174:177], v[112:115], v[206:209], v[52:55]\n"
      "ds_read_u16 v86, v11 offset:64\n"
      "v_exp_f32 v193, v193\n"
      "v_mfma_f32_16x16x32_bf16 v[108:111], v[124:127], v[206:209], v[56:59]\n"
      "v_exp_f32 v158, v158\n"
      "v_mfma_f32_16x16x32_bf16 v[202:205], v[152:155], v[206:209], v[60:63]\n"
      "v_exp_f32 v159, v159\n"
      "v_mfma_f32_16x16x32_bf16 v[180:183], v[210:213], v[206:209], v[26:29]\n"
      "v_exp_f32 v160, v160\n"
      "v_mfma_f32_16x16x16_bf16 v[174:177], v[222:223], v[166:167], v[174:177]\n"
      "v_exp_f32 v161, v161\n"
      "v_cvt_pk_bf16_f32 v190, v190, v191\n"
      "v_mfma_f32_16x16x16_bf16 v[108:111], v[120:121], v[166:167], v[108:111]\n"
      "v_cvt_pk_bf16_f32 v191, v192, v193\n"
      "v_cvt_pk_bf16_f32 v192, v158, v159\n"
      "v_cvt_pk_bf16_f32 v193, v160, v161\n"
      "v_mfma_f32_16x16x16_bf16 v[202:205], v[162:163], v[166:167], v[202:205]\n"
      "v_exp_f32 v98, v98\n"
      "v_exp_f32 v99, v99\n"
      "v_mfma_f32_16x16x16_bf16 v[180:183], v[138:139], v[166:167], v[180:183]\n"
      "v_exp_f32 v100, v100\n"
      "v_exp_f32 v101, v101\n"
      "v_mfma_f32_16x16x32_bf16 v[206:209], v[32:35], v[190:193], 0\n"
      "v_exp_f32_e64 v128, v128\n"
      "v_cvt_pk_bf16_f32 v88, v98, v99\n"
      "v_cvt_pk_bf16_f32 v89, v100, v101\n"
      "v_cvt_pk_bf16_f32 v90, v128, 0\n"
      "v_cvt_pk_bf16_f32 v199, v188, v189\n"
      "v_cvt_pk_bf16_f32 v214, v134, v135\n"
      "v_mfma_f32_16x16x32_bf16 v[206:209], v[32:35], v[88:91], v[206:209]\n"
      "s_cbranch_vccnz L_rare_5_%=\n"
      "L_back_6_%=:\n"
      "v_mfma_f32_16x16x32_bf16 v[166:169], v[198:201], v[190:193], 0\n"
      "s_waitcnt lgkmcnt(0)\n"
      "v_cvt_pk_bf16_f32 v215, v136, v137\n"
      "v_cvt_pk_bf16_f32 v194, v104, v105\n"
      "v_mfma_f32_16x16x32_bf16 v[166:169], v[76:79], v[88:91], v[166:169]\n"
      "v_cvt_pk_bf16_f32 v195, v106, v107\n"
      "v_exp_f32 v174, v174\n"
      "v_mfma_f32_16x16x32_bf16 v[218:221], v[214:217], v[190:193], 0\n"
      "v_exp_f32 v175, v175\n"
      "v_mfma_f32_16x16x32_bf16 v[218:221], v[80:83], v[88:91], v[218:221]\n"
      "v_exp_f32_e64 v176, v176\n"
      "v_mfma_f32_16x16x32_bf16 v[186:189], v[194:197], v[190:193], 0\n"
      "v_exp_f32 v177, v177\n"
      "v_mfma_f32_16x16x32_bf16 v[186:189], v[84:87], v[88:91], v[186:189]\n"
      "v_exp_f32 v108, v108\n"
      "v_mfma_f32_16x16x32_bf16 v[182:185], v[112:115], v[144:147], v[64:67]\n"
      "v_exp_f32 v109, v109\n"
      "v_mfma_f32_16x16x32_bf16 v[158:161], v[124:127], v[144:147], v[68:71]\n"
      "ds_read_b128 v[190:193], v10\n"
      "v_exp_f32 v110, v110\n"
      "v_mfma_f32_16x16x32_bf16 v[100:103], v[152:155], v[144:147], v[72:75]\n"
      "v_exp_f32_e64 v111, v111\n"
      "v_mfma_f32_16x16x32_bf16 v[134:137], v[210:213], v[144:147], v[28:31]\n"
      "v_cvt_pk_bf16_f32 v174, v174, v175\n"
      "v_cvt_pk_bf16_f32 v175, v176, v177\n"
      "v_mfma_f32_16x16x16_bf16 v[182:185], v[222:223], v[140:141], v[182:185]\n"
      "v_cvt_pk_bf16_f32 v176, v108, v109\n"
      "v_cvt_pk_bf16_f32 v177, v110, v111\n"
      "v_exp_f32_e64 v202, v202\n"
      "v_mfma_f32_16x16x16_bf16 v[158:161], v[120:121], v[140:141], v[158:161]\n"
      "v_exp_f32 v203, v203\n"
      "v_exp_f32 v204, v204\n"
      "v_mfma_f32_16x16x16_bf16 v[100:103], v[162:163], v[140:141], v[100:103]\n"
      "v_exp_f32 v205, v205\n"
      "v_exp_f32 v180, v180\n"
      "v_mfma_f32_16x16x16_bf16 v[134:137], v[138:139], v[140:141], v[134:137]\n"
      "v_cvt_pk_bf16_f32 v92, v202, v203\n"
      "v_cvt_pk_bf16_f32 v93, v204, v205\n"
      "v_cvt_pk_bf16_f32 v94, v180, 0\n"
      "v_mfma_f32_16x16x32_bf16 v[144:147], v[32:35], v[174:177], 0\n"
      "ds_read_b64 v[88:89], v10 offset:16\n"
      "v_rcp_f32 v206, v206\n"
      "v_mfma_f32_16x16x32_bf16 v[144:147], v[32:35], v[92:95], v[144:147]\n"
      "v_mul_f32 v166, v206, v166\n"
      "v_mul_f32 v167, v206, v167\n"
      "v_mfma_f32_16x16x32_bf16 v[202:205], v[198:201], v[174:177], 0\n"
      "s_cbranch_vccnz L_rare_7_%=\n"
      "L_back_8_%=:\n"
      "v_mfma_f32_16x16x32_bf16 v[202:205], v[76:79], v[92:95], v[202:205]\n"
      "v_cvt_pk_bf16_f32 v166, v166, v167\n"
      "v_mul_f32 v168, v206, v168\n"
      "v_mfma_f32_16x16x32_bf16 v[108:111], v[214:217], v[174:177], 0\n"
      "v_rcp_f32 v144, v144\n"
      "v_mfma_f32_16x16x32_bf16 v[108:111], v[80:83], v[92:95], v[108:111]\n"
      "v_mul_f32_e64 v169, v206, v169\n"
      "v_cvt_pk_bf16_f32 v167, v168, v169\n"
      "v_mfma_f32_16x16x32_bf16 v[140:143], v[194:197], v[174:177], 0\n"
      "s_waitcnt lgkmcnt(0)\n"
      "v_mul_f32 v202, v144, v202\n"
      "v_mul_f32_e64 v203, v144, v203\n"
      "v_mfma_f32_16x16x32_bf16 v[140:143], v[84:87], v[92:95], v[140:143]\n"
      "v_cvt_pk_bf16_f32 v202, v202, v203\n"
      "v_mul_f32 v204, v144, v204\n"
      "v_mfma_f32_16x16x32_bf16 v[174:177], v[190:193], v[112:115], v[30:33]\n"
      "v_mul_f32 v205, v144, v205\n"
      "v_mul_f32 v218, v206, v218\n"
      "v_mfma_f32_16x16x32_bf16 v[146:149], v[190:193], v[124:127], v[30:33]\n"
      "v_mul_f32 v219, v206, v219\n"
      "v_mul_f32 v220, v206, v220\n"
      "v_mfma_f32_16x16x32_bf16 v[124:127], v[190:193], v[152:155], v[30:33]\n"
      "v_mul_f32 v221, v206, v221\n"
      "v_cvt_pk_bf16_f32 v203, v204, v205\n"
      "v_mfma_f32_16x16x32_bf16 v[178:181], v[190:193], v[210:213], v[30:33]\n"
      "v_mul_f32 v108, v144, v108\n"
      "v_mul_f32 v109, v144, v109\n"
      "v_mfma_f32_16x16x16_bf16 v[174:177], v[88:89], v[222:223], v[174:177]\n"
      "v_mul_f32 v110, v144, v110\n"
      "v_mul_f32 v111, v144, v111\n"
      "v_cvt_pk_bf16_f32 v168, v218, v219\n"
      "v_mfma_f32_16x16x16_bf16 v[146:149], v[88:89], v[120:121], v[146:149]\n"
      "v_cvt_pk_bf16_f32 v169, v220, v221\n"
      "v_cvt_pk_bf16_f32 v204, v108, v109\n"
      "v_cvt_pk_bf16_f32 v205, v110, v111\n"
      "v_mfma_f32_16x16x16_bf16 v[124:127], v[88:89], v[162:163], v[124:127]\n"
      "v_mul_f32 v186, v206, v186\n"
      "v_mul_f32 v187, v206, v187\n"
      "v_mul_f32_e64 v140, v144, v140\n"
      "v_mfma_f32_16x16x16_bf16 v[178:181], v[88:89], v[138:139], v[178:181]\n"
      "ds_write_b128 v3, v[166:169]\n"
      "ds_write_b128 v3, v[202:205] offset:256\n"
      "v_cndmask_b32 v174, v174, v146, s[58:59]\n"
      "v_mul_f32 v141, v144, v141\n"
      "v_cndmask_b32 v174, v174, v124, s[60:61]\n"
      "v_cvt_pk_bf16_f32 v186, v186, v187\n"
      "v_mul_f32 v188, v206, v188\n"
      "v_mul_f32 v189, v206, v189\n"
      "v_cndmask_b32 v174, v174, v178, s[62:63]\n"
      "s_cbranch_vccnz L_rare_9_%=\n"
      "L_back_10_%=:\n"
      "v_exp_f32 v174, v174\n"
      "v_cvt_pk_bf16_f32 v140, v140, v141\n"
      "v_mul_f32 v142, v144, v142\n"
      "v_mul_f32_e64 v143, v144, v143\n"
      "v_cvt_pk_bf16_f32 v187, v188, v189\n"
      "v_cvt_pk_bf16_f32 v141, v142, v143\n"
      "ds_write_b64 v4, v[186:187]\n"
      "ds_write_b64 v4, v[140:141] offset:256\n"
      "v_cvt_pk_bf16_f32 v174, v174, v174\n"
      "ds_write_b16 v13, v174\n"
      "ds_read_b128 v[210:213], v14\n"
      "ds_read_b128 v[136:139], v14 offset:64\n"
      "v_exp_f32 v182, v182\n"
      "v_exp_f32 v183, v183\n"
      "v_exp_f32 v184, v184\n"
      "v_exp_f32 v185, v185\n"
      "v_exp_f32 v158, v158\n"
      "v_exp_f32 v159, v159\n"
      "v_exp_f32 v160, v160\n"
      "v_exp_f32 v161, v161\n"
      "v_cvt_pk_bf16_f32 v182, v182, v183\n"
      "v_cvt_pk_bf16_f32 v183, v184, v185\n"
      "v_cvt_pk_bf16_f32 v184, v158, v159\n"
      "v_cvt_pk_bf16_f32 v185, v160, v161\n"
      "v_exp_f32 v100, v100\n"
      "v_exp_f32 v101, v101\n"
      "v_mfma_f32_16x16x32_bf16 v[140:143], v[32:35], v[182:185], 0\n"
      "v_exp_f32 v102, v102\n"
      "v_exp_f32 v103, v103\n"
      "v_exp_f32 v134, v134\n"
      "v_cvt_pk_bf16_f32 v88, v100, v101\n"
      "v_cvt_pk_bf16_f32 v89, v102, v103\n"
      "v_cvt_pk_bf16_f32 v90, v134, 0\n"
      "v_cmp_ne_u32 s[56:57], 0, v172\n"
      "s_nop 0\n"
      "v_mfma_f32_16x16x32_bf16 v[140:143], v[32:35], v[88:91], v[140:143]\n"
      "s_cmp_ge_u32 s95, 0x40000000\n"
      "s_cselect_b64 vcc, -1, s[56:57]\n"
      "v_mfma_f32_16x16x32_bf16 v[160:163], v[198:201], v[182:185], 0\n"
      "v_mfma_f32_16x16x32_bf16 v[160:163], v[76:79], v[88:91], v[160:163]\n"
      "v_mfma_f32_16x16x32_bf16 v[124:127], v[214:217], v[182:185], 0\n"
      "v_mfma_f32_16x16x32_bf16 v[124:127], v[80:83], v[88:91], v[124:127]\n"
      "v_mfma_f32_16x16x32_bf16 v[166:169], v[194:197], v[182:185], 0\n"
      "s_waitcnt lgkmcnt(0)\n"
      "v_rcp_f32 v140, v140\n"
      "v_mfma_f32_16x16x32_bf16 v[166:169], v[84:87], v[88:91], v[166:169]\n"
      "v_mfma_f32_16x16x32_bf16 v[108:111], v[210:213], v[32:35], 0\n"
      "v_mfma_f32_16x16x32_bf16 v[108:111], v[136:139], v[32:35], v[108:111]\n"
      "v_mul_f32 v160, v140, v160\n"
      "v_mul_f32 v161, v140, v161\n"
      "v_cvt_pk_bf16_f32 v160, v160, v161\n"
      "v_mul_f32 v162, v140, v162\n"
      "v_mul_f32 v163, v140, v163\n"
      "v_cvt_pk_bf16_f32 v161, v162, v163\n"
      "v_mul_f32 v124, v140, v124\n"
      "v_mul_f32 v125, v140, v125\n"
      "v_mul_f32 v126, v140, v126\n"
      "v_mul_f32 v127, v140, v127\n"
      "v_cvt_pk_bf16_f32 v162, v124, v125\n"
      "v_cvt_pk_bf16_f32 v163, v126, v127\n"
      "v_mul_f32 v166, v140, v166\n"
      "v_mul_f32 v167, v140, v167\n"
      "ds_write_b128 v3, v[160:163] offset:512\n"
      "v_cvt_pk_bf16_f32 v166, v166, v167\n"
      "v_mul_f32 v168, v140, v168\n"
      "v_mul_f32 v169, v140, v169\n"
      "v_cvt_pk_bf16_f32 v167, v168, v169\n"
      "ds_write_b64 v4, v[166:167] offset:512\n"
      "s_waitcnt lgkmcnt(0)\n"
      "s_barrier\n"
      "s_or_b64 vcc, vcc, s[54:55]\n"
      "s_cbranch_vccnz L_disp1_%=\n"
      "s_nop 7\n"
      "s_nop 7\n"
      "L_loop1_%=:\n"
      "v_mfma_f32_16x16x32_bf16 v[174:177], v[210:213], v[198:201], 0\n"
      "ds_read_b128 v[116:119], v9\n"
      "ds_read_b128 v[198:201], v9 offset:1024\n"
      "v_mfma_f32_16x16x32_bf16 v[174:177], v[136:139], v[76:79], v[174:177]\n"
      "ds_read_b128 v[152:155], v0 offset:18432\n"
      "ds_read_b128 v[178:181], v1 offset:18432\n"
      "v_mfma_f32_16x16x32_bf16 v[190:193], v[210:213], v[214:217], 0\n"
      "ds_read_b128 v[120:123], v0 offset:24576\n"
      "s_add_u32 m0, s64, 0\n"
      "v_mfma_f32_16x16x32_bf16 v[190:193], v[136:139], v[80:83], v[190:193]\n"
      "buffer_load_dwordx4 v16, s[16:19], s45 offen  lds\n"
      "v_mfma_f32_16x16x32_bf16 v[214:217], v[210:213], v[194:197], 0\n"
      "s_add_u32 m0, s64, 4096\n"
      "v_mfma_f32_16x16x32_bf16 v[214:217], v[136:139], v[84:87], v[214:217]\n"
      "v_mov_b32_dpp v108, v108 quad_perm:[0,0,0,0] row_mask:0xf bank_mask:0xf\n"
      "v_rcp_f32 v108, v108\n"
      "s_waitcnt lgkmcnt(4)\n"
      "v_add_f32 v116, v116, v118\n"
      "v_cndmask_b32 v174, v174, v190, s[58:59]\n"
      "s_waitcnt lgkmcnt(3)\n"
      "v_add_f32_e64 v198, v198, v200\n"
      "s_waitcnt lgkmcnt(2)\n"
      "v_mfma_f32_16x16x32_bf16 v[156:159], a[0:3], v[152:155], 0\n"
      "buffer_load_dwordx4 v17, s[16:19], s45 offen  lds\n"
      "v_mfma_f32_16x16x32_bf16 v[210:213], a[24:27], v[152:155], 0\n"
      "s_add_u32 m0, s64, 8192\n"
      "v_cndmask_b32 v174, v174, v214, s[60:61]\n"
      "v_mul_f32 v174, v108, v174\n"
      "v_mfma_f32_16x16x32_bf16 v[134:137], a[48:51], v[152:155], 0\n"
      "s_waitcnt lgkmcnt(0)\n"
      "v_cvt_pk_bf16_f32 v174, v174, v174\n"
      "v_add_f32_e64 v117, v117, v119\n"
      "v_mfma_f32_16x16x32_bf16 v[190:193], a[72:75], v[152:155], 0\n"
      "ds_write_b16 v12, v174\n"
      "v_mfma_f32_16x16x32_bf16 v[202:205], a[96:99], v[152:155], 0\n"
      "buffer_load_dwordx4 v18, s[16:19], s45 offen  lds\n"
      "v_mfma_f32_16x16x32_bf16 v[164:167], a[120:123], v[152:155], 0\n"
      "ds_read_b128 v[112:115], v1 offset:24576\n"
      "v_add_f32_e64 v199, v199, v201\n"
      "v_mfma_f32_16x16x32_bf16 v[156:159], a[4:7], v[178:181], v[156:159]\n"
      "ds_read_b128 v[108:111], v0 offset:30720\n"
      "v_add_f32_e64 v116, v116, v198\n"
      "v_mfma_f32_16x16x32_bf16 v[156:159], a[8:11], v[120:123], v[156:159]\n"
      "ds_read_b128 v[174:177], v1 offset:30720\n"
      "v_add_f32_e64 v117, v117, v199\n"
      "v_mfma_f32_16x16x32_bf16 v[210:213], a[28:31], v[178:181], v[210:213]\n"
      "s_add_u32 m0, s64, 12288\n"
      "v_fma_f32 v198, -v116, v116, s52\n"
      "v_fmac_f32 v198, 0x43400000, v117\n"
      "v_mfma_f32_16x16x32_bf16 v[210:213], a[32:35], v[120:123], v[210:213]\n"
      "buffer_load_dwordx4 v19, s[16:19], s45 offen  lds\n"
      "v_mfma_f32_16x16x32_bf16 v[134:137], a[52:55], v[178:181], v[134:137]\n"
      "s_add_u32 m0, s65, 16384\n"
      "v_max_f32 v198, s52, v198\n"
      "v_rsq_f32 v198, v198\n"
      "v_mfma_f32_16x16x32_bf16 v[134:137], a[56:59], v[120:123], v[134:137]\n"
      "s_waitcnt lgkmcnt(2)\n"
      "v_mul_f32 v199, 0x43400000, v198\n"
      "v_mul_f32 v201, -v116, v198\n"
      "v_mfma_f32_16x16x32_bf16 v[190:193], a[76:79], v[178:181], v[190:193]\n"
      "s_waitcnt lgkmcnt(1)\n"
      "v_mov_b32 v125, v199\n"
      "v_mov_b32 v124, v201\n"
      "v_mfma_f32_16x16x32_bf16 v[190:193], a[80:83], v[120:123], v[190:193]\n"
      "buffer_load_dwordx4 v15, s[16:19], s45 offen  lds\n"
      "v_mfma_f32_16x16x32_bf16 v[202:205], a[100:103], v[178:181], v[202:205]\n"
      "s_waitcnt lgkmcnt(0)\n"
      "v_permlane32_swap_b32 v199, v125\n"
      "v_mfma_f32_16x16x32_bf16 v[202:205], a[104:107], v[120:123], v[202:205]\n"
      "v_permlane32_swap_b32 v201, v124\n"
      "v_mfma_f32_16x16x32_bf16 v[164:167], a[124:127], v[178:181], v[164:167]\n"
      "v_mov_b32 v214, v199\n"
      "v_mov_b32 v215, v201\n"
      "v_mfma_f32_16x16x32_bf16 v[164:167], a[128:131], v[120:123], v[164:167]\n"
      "buffer_load_dwordx4 v[130:133], v27, s[24:27], s47 offen\n"
      "v_mfma_f32_16x16x32_bf16 v[156:159], a[12:15], v[112:115], v[156:159]\n"
      "v_permlane16_swap_b32 v199, v214\n"
      "v_mfma_f32_16x16x32_bf16 v[156:159], a[16:19], v[108:111], v[156:159]\n"
      "v_permlane16_swap_b32_e64 v201, v215\n"
      "v_mfma_f32_16x16x32_bf16 v[156:159], a[20:23], v[174:177], v[156:159]\n"
      "v_fma_f32 v140, v140, v199, v201\n"
      "v_fma_f32 v141, v141, v199, v201\n"
      "v_mfma_f32_16x16x32_bf16 v[210:213], a[36:39], v[112:115], v[210:213]\n"
      "buffer_load_dword v39, v29, s[24:27], s47 offen\n"
      "v_mfma_f32_16x16x32_bf16 v[210:213], a[40:43], v[108:111], v[210:213]\n"
      "v_cvt_pk_bf16_f32 v140, v140, v141\n"
      "v_fma_f32 v142, v142, v199, v201\n"
      "v_mfma_f32_16x16x32_bf16 v[210:213], a[44:47], v[174:177], v[210:213]\n"
      "v_fma_f32 v143, v143, v199, v201\n"
      "v_fma_f32 v160, v160, v199, v201\n"
      "v_mfma_f32_16x16x32_bf16 v[134:137], a[60:63], v[112:115], v[134:137]\n"
      "v_fma_f32 v161, v161, v199, v201\n"
      "v_cvt_pk_bf16_f32 v141, v142, v143\n"
      "v_mfma_f32_16x16x32_bf16 v[134:137], a[64:67], v[108:111], v[134:137]\n"
      "v_fma_f32 v220, v220, v199, v201\n"
      "v_fma_f32 v221, v221, v199, v201\n"
      "v_mfma_f32_16x16x32_bf16 v[134:137], a[68:71], v[174:177], v[134:137]\n"
      "v_fma_f32 v222, v222, v199, v201\n"
      "v_fma_f32 v223, v223, v199, v201\n"
      "v_mfma_f32_16x16x32_bf16 v[190:193], a[84:87], v[112:115], v[190:193]\n"
      "v_cvt_pk_bf16_f32 v160, v160, v161\n"
      "v_fma_f32 v162, v162, v199, v201\n"
      "v_mfma_f32_16x16x32_bf16 v[190:193], a[88:91], v[108:111], v[190:193]\n"
      "v_fma_f32 v163, v163, v199, v201\n"
      "v_cvt_pk_bf16_f32 v142, v220, v221\n"
      "v_mfma_f32_16x16x32_bf16 v[190:193], a[92:95], v[174:177], v[190:193]\n"
      "v_cvt_pk_bf16_f32 v143, v222, v223\n"
      "v_cvt_pk_bf16_f32 v161, v162, v163\n"
      "v_mfma_f32_16x16x32_bf16 v[202:205], a[108:111], v[112:115], v[202:205]\n"
      "ds_write_b128 v5, v[140:143] offset:18432\n"
      "v_mfma_f32_16x16x32_bf16 v[202:205], a[112:115], v[108:111], v[202:205]\n"
      "v_fma_f32 v186, v186, v214, v215\n"
      "v_fma_f32 v187, v187, v214, v215\n"
      "v_mfma_f32_16x16x32_bf16 v[202:205], a[116:119], v[174:177], v[202:205]\n"
      "v_cvt_pk_bf16_f32 v186, v186, v187\n"
      "v_fma_f32 v188, v188, v214, v215\n"
      "v_mfma_f32_16x16x32_bf16 v[164:167], a[132:135], v[112:115], v[164:167]\n"
      "ds_write_b64 v6, v[160:161] offset:18432\n"
      "v_mfma_f32_16x16x32_bf16 v[164:167], a[136:139], v[108:111], v[164:167]\n"
      "v_fma_f32 v189, v189, v214, v215\n"
      "v_fma_f32 v104, v104, v214, v215\n"
      "v_mfma_f32_16x16x32_bf16 v[164:167], a[140:143], v[174:177], v[164:167]\n"
      "v_fma_f32 v105, v105, v214, v215\n"
      "v_cvt_pk_bf16_f32 v187, v188, v189\n"
      "v_mfma_f32_16x16x32_bf16 v[198:201], v[152:155], a[144:147], 0\n"
      "v_fma_f32 v182, v182, v214, v215\n"
      "v_fma_f32 v183, v183, v214, v215\n"
      "v_mfma_f32_16x16x32_bf16 v[198:201], v[178:181], a[148:151], v[198:201]\n"
      "v_fma_f32 v184, v184, v214, v215\n"
      "v_fma_f32 v185, v185, v214, v215\n"
      "v_mfma_f32_16x16x32_bf16 v[198:201], v[120:123], a[152:155], v[198:201]\n"
      "v_cvt_pk_bf16_f32 v104, v104, v105\n"
      "v_fma_f32 v106, v106, v214, v215\n"
      "v_mfma_f32_16x16x32_bf16 v[198:201], v[112:115], a[156:159], v[198:201]\n"
      "v_fma_f32 v107, v107, v214, v215\n"
      "v_cvt_pk_bf16_f32 v188, v182, v183\n"
      "v_mfma_f32_16x16x32_bf16 v[198:201], v[108:111], a[160:163], v[198:201]\n"
      "v_cvt_pk_bf16_f32 v189, v184, v185\n"
      "v_cvt_pk_bf16_f32 v105, v106, v107\n"
      "v_mfma_f32_16x16x32_bf16 v[198:201], v[174:177], a[164:167], v[198:201]\n"
      "ds_write_b128 v5, v[186:189] offset:24576\n"
      "v_mfma_f32_16x16x32_bf16 v[214:217], v[152:155], a[168:171], 0\n"
      "ds_read_b128 v[186:189], v0 offset:20480\n"
      "v_fma_f32 v100, v100, v125, v124\n"
      "v_mfma_f32_16x16x32_bf16 v[214:217], v[178:181], a[172:175], v[214:217]\n"
      "ds_read_b128 v[160:163], v1 offset:20480\n"
      "v_fma_f32 v101, v101, v125, v124\n"
      "v_mfma_f32_16x16x32_bf16 v[214:217], v[120:123], a[176:179], v[214:217]\n"
      "ds_write_b64 v6, v[104:105] offset:24576\n"
      "v_mfma_f32_16x16x32_bf16 v[214:217], v[112:115], a[180:183], v[214:217]\n"
      "ds_read_b128 v[170:173], v0 offset:26624\n"
      "v_cvt_pk_bf16_f32 v100, v100, v101\n"
      "v_mfma_f32_16x16x32_bf16 v[214:217], v[108:111], a[184:187], v[214:217]\n"
      "ds_read_b128 v[116:119], v1 offset:26624\n"
      "v_fma_f32 v102, v102, v125, v124\n"
      "v_mfma_f32_16x16x32_bf16 v[214:217], v[174:177], a[188:191], v[214:217]\n"
      "ds_read_b128 v[182:185], v0 offset:32768\n"
      "v_fma_f32 v103, v103, v125, v124\n"
      "v_mfma_f32_16x16x32_bf16 v[194:197], v[152:155], a[192:195], 0\n"
      "ds_read_b128 v[144:147], v1 offset:32768\n"
      "v_fma_f32 v96, v96, v125, v124\n"
      "v_mfma_f32_16x16x32_bf16 v[194:197], v[178:181], a[196:199], v[194:197]\n"
      "s_waitcnt lgkmcnt(6)\n"
      "v_fma_f32 v97, v97, v125, v124\n"
      "v_cvt_pk_bf16_f32 v101, v102, v103\n"
      "v_mfma_f32_16x16x32_bf16 v[194:197], v[120:123], a[200:203], v[194:197]\n"
      "s_waitcnt lgkmcnt(5)\n"
      "v_fma_f32 v126, v126, v125, v124\n"
      "v_fma_f32 v127, v127, v125, v124\n"
      "v_mfma_f32_16x16x32_bf16 v[194:197], v[112:115], a[204:207], v[194:197]\n"
      "v_fma_f32 v128, v128, v125, v124\n"
      "v_fma_f32 v129, v129, v125, v124\n"
      "v_mfma_f32_16x16x32_bf16 v[194:197], v[108:111], a[208:211], v[194:197]\n"
      "s_waitcnt lgkmcnt(3)\n"
      "v_cvt_pk_bf16_f32 v96, v96, v97\n"
      "v_fma_f32 v98, v98, v125, v124\n"
      "v_mfma_f32_16x16x32_bf16 v[194:197], v[174:177], a[212:215], v[194:197]\n"
      "s_waitcnt lgkmcnt(2)\n"
      "v_fma_f32 v99, v99, v125, v124\n"
      "v_cvt_pk_bf16_f32 v102, v126, v127\n"
      "v_mfma_f32_16x16x32_bf16 v[112:115], a[0:3], v[186:189], 0\n"
      "s_waitcnt lgkmcnt(1)\n"
      "v_cvt_pk_bf16_f32 v103, v128, v129\n"
      "v_cvt_pk_bf16_f32 v97, v98, v99\n"
      "v_mfma_f32_16x16x32_bf16 v[112:115], a[4:7], v[160:163], v[112:115]\n"
      "ds_write_b128 v5, v[100:103] offset:30720\n"
      "v_mfma_f32_16x16x32_bf16 v[112:115], a[8:11], v[170:173], v[112:115]\n"
      "s_waitcnt lgkmcnt(1)\n"
      "v_fma_f32 v99, v156, v156, s51\n"
      "v_fma_f32 v154, v190, v190, s51\n"
      "v_mfma_f32_16x16x32_bf16 v[112:115], a[12:15], v[116:119], v[112:115]\n"
      "v_fmac_f32 v99, v157, v157\n"
      "v_fmac_f32 v154, v191, v191\n"
      "v_mfma_f32_16x16x32_bf16 v[112:115], a[16:19], v[182:185], v[112:115]\n"
      "ds_write_b64 v6, v[96:97] offset:30720\n"
      "v_mfma_f32_16x16x32_bf16 v[112:115], a[20:23], v[144:147], v[112:115]\n"
      "v_fmac_f32 v99, v158, v158\n"
      "v_fmac_f32 v154, v192, v192\n"
      "v_mfma_f32_16x16x32_bf16 v[104:107], a[24:27], v[186:189], 0\n"
      "v_fmac_f32 v99, v159, v159\n"
      "v_fmac_f32 v154, v193, v193\n"
      "v_mfma_f32_16x16x32_bf16 v[104:107], a[28:31], v[160:163], v[104:107]\n"
      "v_fmac_f32 v99, v210, v210\n"
      "v_fmac_f32 v154, v202, v202\n"
      "v_mfma_f32_16x16x32_bf16 v[104:107], a[32:35], v[170:173], v[104:107]\n"
      "v_fmac_f32 v99, v211, v211\n"
      "v_fmac_f32 v154, v203, v203\n"
      "v_mfma_f32_16x16x32_bf16 v[104:107], a[36:39], v[116:119], v[104:107]\n"
      "v_fmac_f32 v99, v212, v212\n"
      "v_fmac_f32 v154, v204, v204\n"
      "v_mfma_f32_16x16x32_bf16 v[104:107], a[40:43], v[182:185], v[104:107]\n"
      "v_fmac_f32 v99, v213, v213\n"
      "v_fmac_f32 v154, v205, v205\n"
      "v_mfma_f32_16x16x32_bf16 v[104:107], a[44:47], v[144:147], v[104:107]\n"
      "v_fmac_f32 v99, v134, v134\n"
      "v_fmac_f32 v154, v164, v164\n"
      "v_mfma_f32_16x16x32_bf16 v[124:127], a[48:51], v[186:189], 0\n"
      "v_fmac_f32 v99, v135, v135\n"
      "v_fmac_f32 v154, v165, v165\n"
      "v_mfma_f32_16x16x32_bf16 v[124:127], a[52:55], v[160:163], v[124:127]\n"
      "v_fmac_f32 v99, v136, v136\n"
      "v_fmac_f32 v154, v166, v166\n"
      "v_mfma_f32_16x16x32_bf16 v[124:127], a[56:59], v[170:173], v[124:127]\n"
      "v_fmac_f32 v99, v137, v137\n"
      "v_fmac_f32 v154, v167, v167\n"
      "v_mfma_f32_16x16x32_bf16 v[124:127], a[60:63], v[116:119], v[124:127]\n"
      "v_fma_f32 v129, v112, v112, s51\n"
      "v_permlane16_swap_b32 v99, v154\n"
      "v_mfma_f32_16x16x32_bf16 v[124:127], a[64:67], v[182:185], v[124:127]\n"
      "v_add_f32 v99, v99, v154\n"
      "v_mov_b32 v154, v99\n"
      "v_mfma_f32_16x16x32_bf16 v[124:127], a[68:71], v[144:147], v[124:127]\n"
      "v_fmac_f32 v129, v113, v113\n"
      "v_permlane32_swap_b32 v99, v154\n"
      "v_mfma_f32_16x16x32_bf16 v[174:177], a[72:75], v[186:189], 0\n"
      "v_add_f32 v99, v99, v154\n"
      "v_rsq_f32 v99, v99\n"
      "v_mfma_f32_16x16x32_bf16 v[174:177], a[76:79], v[160:163], v[174:177]\n"
      "v_mov_b32 v154, v99\n"
      "v_fmac_f32 v129, v114, v114\n"
      "v_mfma_f32_16x16x32_bf16 v[174:177], a[80:83], v[170:173], v[174:177]\n"
      "v_permlane16_swap_b32 v99, v154\n"
      "v_mfma_f32_16x16x32_bf16 v[174:177], a[84:87], v[116:119], v[174:177]\n"
      "v_mul_f32 v99, s50, v99\n"
      "v_mul_f32 v190, v154, v190\n"
      "v_mfma_f32_16x16x32_bf16 v[174:177], a[88:91], v[182:185], v[174:177]\n"
      "v_mul_f32 v191, v154, v191\n"
      "v_mul_f32 v156, v99, v156\n"
      "v_mfma_f32_16x16x32_bf16 v[174:177], a[92:95], v[144:147], v[174:177]\n"
      "v_mul_f32_e64 v157, v99, v157\n"
      "v_cvt_pk_bf16_f32 v190, v190, v191\n"
      "v_mfma_f32_16x16x32_bf16 v[108:111], a[96:99], v[186:189], 0\n"
      "v_mul_f32 v192, v154, v192\n"
      "v_mul_f32 v193, v154, v193\n"
      "v_mfma_f32_16x16x32_bf16 v[108:111], a[100:103], v[160:163], v[108:111]\n"
      "v_mul_f32 v164, v154, v164\n"
      "v_mul_f32 v165, v154, v165\n"
      "v_mfma_f32_16x16x32_bf16 v[108:111], a[104:107], v[170:173], v[108:111]\n"
      "v_cvt_pk_bf16_f32 v156, v156, v157\n"
      "v_mul_f32_e64 v158, v99, v158\n"
      "v_mfma_f32_16x16x32_bf16 v[108:111], a[108:111], v[116:119], v[108:111]\n"
      "v_mul_f32 v159, v99, v159\n"
      "v_mul_f32 v134, v99, v134\n"
      "v_mfma_f32_16x16x32_bf16 v[108:111], a[112:115], v[182:185], v[108:111]\n"
      "v_mul_f32_e64 v135, v99, v135\n"
      "v_cvt_pk_bf16_f32 v191, v192, v193\n"
      "v_mfma_f32_16x16x32_bf16 v[108:111], a[116:119], v[144:147], v[108:111]\n"
      "v_mul_f32 v202, v154, v202\n"
      "v_mul_f32 v203, v154, v203\n"
      "v_mfma_f32_16x16x32_bf16 v[218:221], a[120:123], v[186:189], 0\n"
      "v_mul_f32 v204, v154, v204\n"
      "v_mul_f32 v205, v154, v205\n"
      "v_mfma_f32_16x16x32_bf16 v[218:221], a[124:127], v[160:163], v[218:221]\n"
      "v_cvt_pk_bf16_f32 v164, v164, v165\n"
      "v_mul_f32_e64 v166, v154, v166\n"
      "v_mfma_f32_16x16x32_bf16 v[218:221], a[128:131], v[170:173], v[218:221]\n"
      "v_mul_f32_e64 v167, v154, v167\n"
      "v_cvt_pk_bf16_f32 v157, v158, v159\n"
      "v_mfma_f32_16x16x32_bf16 v[218:221], a[132:135], v[116:119], v[218:221]\n"
      "ds_read_b128 v[152:155], v7\n"
      "v_mul_f32 v210, v99, v210\n"
      "v_mfma_f32_16x16x32_bf16 v[218:221], a[136:139], v[182:185], v[218:221]\n"
      "v_mul_f32 v211, v99, v211\n"
      "v_mul_f32_e64 v212, v99, v212\n"
      "v_mfma_f32_16x16x32_bf16 v[218:221], a[140:143], v[144:147], v[218:221]\n"
      "ds_read_b128 v[178:181], v7 offset:4096\n"
      "v_mul_f32_e64 v213, v99, v213\n"
      "v_mfma_f32_16x16x32_bf16 v[100:103], v[186:189], a[144:147], 0\n"
      "ds_read_b128 v[120:123], v7 offset:8192\n"
      "v_cvt_pk_bf16_f32 v134, v134, v135\n"
      "v_mfma_f32_16x16x32_bf16 v[100:103], v[160:163], a[148:151], v[100:103]\n"
      "v_mul_f32 v136, v99, v136\n"
      "v_mul_f32 v137, v99, v137\n"
      "v_mfma_f32_16x16x32_bf16 v[100:103], v[170:173], a[152:155], v[100:103]\n"
      "ds_read_b128 v[206:209], v7 offset:12288\n"
      "v_cvt_pk_bf16_f32 v192, v202, v203\n"
      "v_mfma_f32_16x16x32_bf16 v[100:103], v[116:119], a[156:159], v[100:103]\n"
      "ds_read_b128 v[96:99], v7 offset:16384\n"
      "v_cvt_pk_bf16_f32 v193, v204, v205\n"
      "v_mfma_f32_16x16x32_bf16 v[100:103], v[182:185], a[160:163], v[100:103]\n"
      "v_cvt_pk_bf16_f32 v165, v166, v167\n"
      "v_cvt_pk_bf16_f32 v158, v210, v211\n"
      "v_mfma_f32_16x16x32_bf16 v[100:103], v[144:147], a[164:167], v[100:103]\n"
      "ds_read_b128 v[166:169], v0 offset:22528\n"
      "v_cvt_pk_bf16_f32 v159, v212, v213\n"
      "v_mfma_f32_16x16x32_bf16 v[210:213], v[186:189], a[168:171], 0\n"
      "ds_read_b128 v[202:205], v1 offset:22528\n"
      "v_cvt_pk_bf16_f32 v135, v136, v137\n"
      "v_mfma_f32_16x16x32_bf16 v[210:213], v[160:163], a[172:175], v[210:213]\n"
      "s_waitcnt lgkmcnt(4)\n"
      "v_fma_f32 v128, v174, v174, s51\n"
      "v_fmac_f32 v128, v175, v175\n"
      "v_mfma_f32_16x16x32_bf16 v[210:213], v[170:173], a[176:179], v[210:213]\n"
      "buffer_store_dwordx4 v[152:155], v20, s[20:23], s46 offen sc1\n"
      "v_mfma_f32_16x16x32_bf16 v[210:213], v[116:119], a[180:183], v[210:213]\n"
      "s_nop 0\n"
      "ds_read_b128 v[152:155], v0 offset:28672\n"
      "v_fmac_f32 v128, v176, v176\n"
      "v_mfma_f32_16x16x32_bf16 v[210:213], v[182:185], a[184:187], v[210:213]\n"
      "ds_read_b128 v[148:151], v1 offset:28672\n"
      "v_fmac_f32 v129, v115, v115\n"
      "v_mfma_f32_16x16x32_bf16 v[210:213], v[144:147], a[188:191], v[210:213]\n"
      "ds_read_b128 v[140:143], v0 offset:34816\n"
      "v_fmac_f32 v128, v177, v177\n"
      "v_mfma_f32_16x16x32_bf16 v[136:139], v[186:189], a[192:195], 0\n"
      "s_waitcnt lgkmcnt(3)\n"
      "v_fmac_f32 v129, v104, v104\n"
      "v_fmac_f32_e64 v128, v108, v108\n"
      "v_mfma_f32_16x16x32_bf16 v[136:139], v[160:163], a[196:199], v[136:139]\n"
      "ds_read_b128 v[160:163], v1 offset:34816\n"
      "v_fmac_f32 v129, v105, v105\n"
      "v_mfma_f32_16x16x32_bf16 v[136:139], v[170:173], a[200:203], v[136:139]\n"
      "v_fmac_f32 v128, v109, v109\n"
      "v_fmac_f32_e64 v129, v106, v106\n"
      "v_mfma_f32_16x16x32_bf16 v[136:139], v[116:119], a[204:207], v[136:139]\n"
      "s_waitcnt lgkmcnt(3)\n"
      "v_fmac_f32 v128, v110, v110\n"
      "v_fmac_f32_e64 v129, v107, v107\n"
      "v_mfma_f32_16x16x32_bf16 v[136:139], v[182:185], a[208:211], v[136:139]\n"
      "buffer_store_dwordx4 v[178:181], v21, s[20:23], s46 offen sc1\n"
      "v_mfma_f32_16x16x32_bf16 v[136:139], v[144:147], a[212:215], v[136:139]\n"
      "s_waitcnt lgkmcnt(1)\n"
      "v_fmac_f32 v128, v111, v111\n"
      "v_fmac_f32_e64 v129, v124, v124\n"
      "v_mfma_f32_16x16x32_bf16 v[116:119], a[0:3], v[166:169], 0\n"
      "s_waitcnt lgkmcnt(0)\n"
      "v_fmac_f32 v128, v218, v218\n"
      "v_fmac_f32 v129, v125, v125\n"
      "v_mfma_f32_16x16x32_bf16 v[116:119], a[4:7], v[202:205], v[116:119]\n"
      "v_fmac_f32 v128, v219, v219\n"
      "v_fmac_f32 v129, v126, v126\n"
      "v_mfma_f32_16x16x32_bf16 v[116:119], a[8:11], v[152:155], v[116:119]\n"
      "v_fmac_f32 v128, v220, v220\n"
      "v_fmac_f32 v129, v127, v127\n"
      "v_mfma_f32_16x16x32_bf16 v[116:119], a[12:15], v[148:151], v[116:119]\n"
      "v_fmac_f32 v128, v221, v221\n"
      "v_cvt_pk_bf16_f32 v198, v198, v199\n"
      "v_mfma_f32_16x16x32_bf16 v[116:119], a[16:19], v[140:143], v[116:119]\n"
      "v_permlane16_swap_b32 v129, v128\n"
      "v_mfma_f32_16x16x32_bf16 v[116:119], a[20:23], v[160:163], v[116:119]\n"
      "v_add_f32 v129, v129, v128\n"
      "v_mov_b32 v128, v129\n"
      "v_mfma_f32_16x16x32_bf16 v[186:189], a[24:27], v[166:169], 0\n"
      "buffer_store_dwordx4 v[120:123], v22, s[20:23], s46 offen sc1\n"
      "v_mfma_f32_16x16x32_bf16 v[186:189], a[28:31], v[202:205], v[186:189]\n"
      "v_permlane32_swap_b32 v129, v128\n"
      "v_mfma_f32_16x16x32_bf16 v[186:189], a[32:35], v[152:155], v[186:189]\n"
      "v_add_f32 v129, v129, v128\n"
      "v_rsq_f32 v129, v129\n"
      "v_mfma_f32_16x16x32_bf16 v[186:189], a[36:39], v[148:151], v[186:189]\n"
      "v_mov_b32_e64 v128, v129\n"
      "v_fma_f32 v77, v116, v116, s51\n"
      "v_mfma_f32_16x16x32_bf16 v[186:189], a[40:43], v[140:143], v[186:189]\n"
      "v_permlane16_swap_b32 v129, v128\n"
      "v_mfma_f32_16x16x32_bf16 v[186:189], a[44:47], v[160:163], v[186:189]\n"
      "v_mul_f32 v129, s50, v129\n"
      "v_mul_f32 v174, v128, v174\n"
      "v_mfma_f32_16x16x32_bf16 v[170:173], a[48:51], v[166:169], 0\n"
      "v_mul_f32 v175, v128, v175\n"
      "v_mul_f32 v112, v129, v112\n"
      "v_mfma_f32_16x16x32_bf16 v[170:173], a[52:55], v[202:205], v[170:173]\n"
      "v_mul_f32 v113, v129, v113\n"
      "v_cvt_pk_bf16_f32 v174, v174, v175\n"
      "v_mfma_f32_16x16x32_bf16 v[170:173], a[56:59], v[152:155], v[170:173]\n"
      "buffer_store_dwordx4 v[206:209], v23, s[20:23], s46 offen sc1\n"
      "v_mfma_f32_16x16x32_bf16 v[170:173], a[60:63], v[148:151], v[170:173]\n"
      "v_mul_f32 v176, v128, v176\n"
      "v_mul_f32 v177, v128, v177\n"
      "v_mfma_f32_16x16x32_bf16 v[170:173], a[64:67], v[140:143], v[170:173]\n"
      "v_mul_f32 v218, v128, v218\n"
      "v_mul_f32 v219, v128, v219\n"
      "v_mfma_f32_16x16x32_bf16 v[170:173], a[68:71], v[160:163], v[170:173]\n"
      "v_cvt_pk_bf16_f32 v112, v112, v113\n"
      "v_mul_f32 v114, v129, v114\n"
      "v_mfma_f32_16x16x32_bf16 v[206:209], a[72:75], v[166:169], 0\n"
      "v_mul_f32 v115, v129, v115\n"
      "v_mul_f32 v124, v129, v124\n"
      "v_mfma_f32_16x16x32_bf16 v[206:209], a[76:79], v[202:205], v[206:209]\n"
      "ds_read_b128 v[144:147], v9 offset:4608\n"
      "v_mul_f32 v125, v129, v125\n"
      "v_mfma_f32_16x16x32_bf16 v[206:209], a[80:83], v[152:155], v[206:209]\n"
      "v_cvt_pk_bf16_f32 v175, v176, v177\n"
      "v_mul_f32 v108, v128, v108\n"
      "v_mfma_f32_16x16x32_bf16 v[206:209], a[84:87], v[148:151], v[206:209]\n"
      "v_mul_f32 v109, v128, v109\n"
      "v_mul_f32_e64 v110, v128, v110\n"
      "v_mfma_f32_16x16x32_bf16 v[206:209], a[88:91], v[140:143], v[206:209]\n"
      "buffer_store_dwordx4 v[96:99], v25, s[20:23], s46 offen sc1\n"
      "v_mfma_f32_16x16x32_bf16 v[206:209], a[92:95], v[160:163], v[206:209]\n"
      "v_mul_f32_e64 v111, v128, v111\n"
      "v_cvt_pk_bf16_f32 v218, v218, v219\n"
      "v_mfma_f32_16x16x32_bf16 v[120:123], a[96:99], v[166:169], 0\n"
      "v_mul_f32 v220, v128, v220\n"
      "v_mul_f32 v221, v128, v221\n"
      "v_mfma_f32_16x16x32_bf16 v[120:123], a[100:103], v[202:205], v[120:123]\n"
      "v_cvt_pk_bf16_f32 v113, v114, v115\n"
      "v_mul_f32 v104, v129, v104\n"
      "v_mfma_f32_16x16x32_bf16 v[120:123], a[104:107], v[152:155], v[120:123]\n"
      "v_mul_f32 v105, v129, v105\n"
      "v_mul_f32 v106, v129, v106\n"
      "v_mfma_f32_16x16x32_bf16 v[120:123], a[108:111], v[148:151], v[120:123]\n"
      "v_mul_f32 v107, v129, v107\n"
      "v_cvt_pk_bf16_f32 v124, v124, v125\n"
      "v_mfma_f32_16x16x32_bf16 v[120:123], a[112:115], v[140:143], v[120:123]\n"
      "v_mul_f32 v126, v129, v126\n"
      "v_mul_f32 v127, v129, v127\n"
      "v_mfma_f32_16x16x32_bf16 v[120:123], a[116:119], v[160:163], v[120:123]\n"
      "v_cvt_pk_bf16_f32 v176, v108, v109\n"
      "v_cvt_pk_bf16_f32 v177, v110, v111\n"
      "v_mfma_f32_16x16x32_bf16 v[178:181], a[120:123], v[166:169], 0\n"
      "v_cvt_pk_bf16_f32 v219, v220, v221\n"
      "v_cvt_pk_bf16_f32 v114, v104, v105\n"
      "v_mfma_f32_16x16x32_bf16 v[178:181], a[124:127], v[202:205], v[178:181]\n"
      "v_cvt_pk_bf16_f32 v115, v106, v107\n"
      "v_cvt_pk_bf16_f32 v125, v126, v127\n"
      "v_mfma_f32_16x16x32_bf16 v[178:181], a[128:131], v[152:155], v[178:181]\n"
      "v_fma_f32 v76, v206, v206, s51\n"
      "v_fmac_f32 v77, v117, v117\n"
      "v_mfma_f32_16x16x32_bf16 v[178:181], a[132:135], v[148:151], v[178:181]\n"
      "v_fmac_f32 v76, v207, v207\n"
      "v_fmac_f32 v77, v118, v118\n"
      "v_mfma_f32_16x16x32_bf16 v[178:181], a[136:139], v[140:143], v[178:181]\n"
      "v_fmac_f32 v76, v208, v208\n"
      "v_fmac_f32 v77, v119, v119\n"
      "v_mfma_f32_16x16x32_bf16 v[178:181], a[140:143], v[160:163], v[178:181]\n"
      "v_fmac_f32 v76, v209, v209\n"
      "v_fmac_f32 v77, v186, v186\n"
      "v_mfma_f32_16x16x32_bf16 v[182:185], v[166:169], a[144:147], 0\n"
      "v_fmac_f32 v76, v120, v120\n"
      "v_fmac_f32 v77, v187, v187\n"
      "v_mfma_f32_16x16x32_bf16 v[182:185], v[202:205], a[148:151], v[182:185]\n"
      "v_fmac_f32 v76, v121, v121\n"
      "v_fmac_f32 v77, v188, v188\n"
      "v_mfma_f32_16x16x32_bf16 v[182:185], v[152:155], a[152:155], v[182:185]\n"
      "v_fmac_f32 v76, v122, v122\n"
      "v_fmac_f32 v77, v189, v189\n"
      "v_mfma_f32_16x16x32_bf16 v[182:185], v[148:151], a[156:159], v[182:185]\n"
      "v_fmac_f32 v76, v123, v123\n"
      "v_fmac_f32 v77, v170, v170\n"
      "v_mfma_f32_16x16x32_bf16 v[182:185], v[140:143], a[160:163], v[182:185]\n"
      "v_fmac_f32 v76, v178, v178\n"
      "v_fmac_f32_e64 v77, v171, v171\n"
      "v_mfma_f32_16x16x32_bf16 v[182:185], v[160:163], a[164:167], v[182:185]\n"
      "ds_read_b128 v[96:99], v2\n"
      "v_fmac_f32 v76, v179, v179\n"
      "v_mfma_f32_16x16x32_bf16 v[104:107], v[166:169], a[168:171], 0\n"
      "ds_read_b128 v[126:129], v2 offset:3072\n"
      "v_fmac_f32 v77, v172, v172\n"
      "v_mfma_f32_16x16x32_bf16 v[104:107], v[202:205], a[172:175], v[104:107]\n"
      "v_fmac_f32 v76, v180, v180\n"
      "v_fmac_f32 v77, v173, v173\n"
      "v_mfma_f32_16x16x32_bf16 v[104:107], v[152:155], a[176:179], v[104:107]\n"
      "v_fmac_f32_e64 v76, v181, v181\n"
      "v_cvt_pk_bf16_f32 v199, v200, v201\n"
      "v_mfma_f32_16x16x32_bf16 v[104:107], v[148:151], a[180:183], v[104:107]\n"
      "v_permlane16_swap_b32 v77, v76\n"
      "v_mfma_f32_16x16x32_bf16 v[104:107], v[140:143], a[184:187], v[104:107]\n"
      "v_add_f32 v77, v77, v76\n"
      "v_mov_b32 v76, v77\n"
      "v_mfma_f32_16x16x32_bf16 v[104:107], v[160:163], a[188:191], v[104:107]\n"
      "s_waitcnt lgkmcnt(1)\n"
      "v_permlane32_swap_b32 v77, v76\n"
      "v_mfma_f32_16x16x32_bf16 v[108:111], v[166:169], a[192:195], 0\n"
      "s_waitcnt lgkmcnt(0)\n"
      "v_add_f32 v77, v77, v76\n"
      "v_rsq_f32 v77, v77\n"
      "v_mfma_f32_16x16x32_bf16 v[108:111], v[202:205], a[196:199], v[108:111]\n"
      "ds_read_b128 v[166:169], v2 offset:6144\n"
      "v_mov_b32_e64 v76, v77\n"
      "v_mfma_f32_16x16x32_bf16 v[108:111], v[152:155], a[200:203], v[108:111]\n"
      "ds_read_b128 v[152:155], v2 offset:9216\n"
      "v_permlane16_swap_b32_e64 v77, v76\n"
      "v_mfma_f32_16x16x32_bf16 v[108:111], v[148:151], a[204:207], v[108:111]\n"
      "ds_read_b128 v[148:151], v2 offset:12288\n"
      "v_mul_f32 v206, v76, v206\n"
      "v_mfma_f32_16x16x32_bf16 v[108:111], v[140:143], a[208:211], v[108:111]\n"
      "ds_read_b128 v[200:203], v2 offset:15360\n"
      "v_mul_f32 v207, v76, v207\n"
      "v_mfma_f32_16x16x32_bf16 v[108:111], v[160:163], a[212:215], v[108:111]\n"
      "v_cvt_pk_bf16_f32 v206, v206, v207\n"
      "v_mul_f32 v208, v76, v208\n"
      "v_mfma_f32_16x16x32_bf16 v[140:143], a[216:219], v[96:99], 0\n"
      "v_mul_f32 v209, v76, v209\n"
      "v_cvt_pk_bf16_f32 v207, v208, v209\n"
      "v_mfma_f32_16x16x32_bf16 v[140:143], a[220:223], v[126:129], v[140:143]\n"
      "s_waitcnt lgkmcnt(3)\n"
      "v_mul_f32 v120, v76, v120\n"
      "v_mul_f32_e64 v121, v76, v121\n"
      "v_mfma_f32_16x16x32_bf16 v[140:143], a[224:227], v[166:169], v[140:143]\n"
      "s_waitcnt lgkmcnt(2)\n"
      "v_mul_f32 v122, v76, v122\n"
      "v_mul_f32_e64 v123, v76, v123\n"
      "v_cvt_pk_bf16_f32 v208, v120, v121\n"
      "v_mfma_f32_16x16x32_bf16 v[220:223], a[240:243], v[96:99], 0\n"
      "s_waitcnt lgkmcnt(1)\n"
      "v_cvt_pk_bf16_f32 v209, v122, v123\n"
      "v_mul_f32 v178, v76, v178\n"
      "v_mfma_f32_16x16x32_bf16 v[220:223], a[244:247], v[126:129], v[220:223]\n"
      "s_waitcnt lgkmcnt(0)\n"
      "v_mul_f32 v179, v76, v179\n"
      "v_cvt_pk_bf16_f32 v178, v178, v179\n"
      "v_mfma_f32_16x16x32_bf16 v[220:223], a[248:251], v[166:169], v[220:223]\n"
      "v_mul_f32 v180, v76, v180\n"
      "v_mul_f32 v181, v76, v181\n"
      "v_mfma_f32_16x16x32_bf16 v[160:163], v[232:235], v[96:99], 0\n"
      "v_cvt_pk_bf16_f32 v179, v180, v181\n"
      "v_mul_f32 v77, s50, v77\n"
      "v_mfma_f32_16x16x32_bf16 v[160:163], v[236:239], v[126:129], v[160:163]\n"
      "v_mul_f32 v116, v77, v116\n"
      "v_mul_f32_e64 v117, v77, v117\n"
      "v_mfma_f32_16x16x32_bf16 v[160:163], v[240:243], v[166:169], v[160:163]\n"
      "ds_read_b128 v[120:123], v10 offset:1200\n"
      "v_cvt_pk_bf16_f32 v116, v116, v117\n"
      "v_mfma_f32_16x16x32_bf16 v[140:143], a[228:231], v[152:155], v[140:143]\n"
      "v_mul_f32 v118, v77, v118\n"
      "v_mul_f32 v119, v77, v119\n"
      "v_mfma_f32_16x16x32_bf16 v[140:143], a[232:235], v[148:151], v[140:143]\n"
      "v_mul_f32 v170, v77, v170\n"
      "v_mul_f32 v171, v77, v171\n"
      "v_mfma_f32_16x16x32_bf16 v[140:143], a[236:239], v[200:203], v[140:143]\n"
      "v_cvt_pk_bf16_f32 v117, v118, v119\n"
      "v_mul_f32_e64 v186, v77, v186\n"
      "v_mfma_f32_16x16x32_bf16 v[220:223], a[252:255], v[152:155], v[220:223]\n"
      "ds_read_b64 v[204:205], v10 offset:1192\n"
      "v_mul_f32 v187, v77, v187\n"
      "v_mfma_f32_16x16x32_bf16 v[220:223], v[224:227], v[148:151], v[220:223]\n"
      "ds_read_b128 v[166:169], v2 offset:256\n"
      "v_mul_f32 v188, v77, v188\n"
      "v_mfma_f32_16x16x32_bf16 v[220:223], v[228:231], v[200:203], v[220:223]\n"
      "s_waitcnt lgkmcnt(2)\n"
      "v_mul_f32 v189, v77, v189\n"
      "v_cvt_pk_bf16_f32 v170, v170, v171\n"
      "v_mfma_f32_16x16x32_bf16 v[160:163], v[244:247], v[152:155], v[160:163]\n"
      "ds_read_b128 v[152:155], v2 offset:3328\n"
      "v_mul_f32_e64 v172, v77, v172\n"
      "v_mfma_f32_16x16x32_bf16 v[160:163], v[248:251], v[148:151], v[160:163]\n"
      "ds_read_b128 v[96:99], v2 offset:6400\n"
      "v_mul_f32 v173, v77, v173\n"
      "v_mfma_f32_16x16x32_bf16 v[160:163], v[252:255], v[200:203], v[160:163]\n"
      "v_cvt_pk_bf16_f32 v118, v186, v187\n"
      "v_cvt_pk_bf16_f32 v119, v188, v189\n"
      "v_mfma_f32_16x16x32_bf16 v[148:151], v[190:193], v[156:159], v[40:43]\n"
      "s_waitcnt lgkmcnt(3)\n"
      "v_cvt_pk_bf16_f32 v171, v172, v173\n"
      "v_cvt_pk_bf16_f32 v200, v100, v101\n"
      "v_mfma_f32_16x16x32_bf16 v[126:129], v[174:177], v[156:159], v[44:47]\n"
      "s_waitcnt lgkmcnt(2)\n"
      "v_cvt_pk_bf16_f32 v201, v102, v103\n"
      "v_cvt_pk_bf16_f32 v76, v182, v183\n"
      "v_mfma_f32_16x16x32_bf16 v[100:103], v[206:209], v[156:159], v[48:51]\n"
      "v_cvt_pk_bf16_f32 v77, v184, v185\n"
      "v_cvt_pk_bf16_f32 v214, v214, v215\n"
      "v_mfma_f32_16x16x32_bf16 v[180:183], v[120:123], v[156:159], v[24:27]\n"
      "s_waitcnt lgkmcnt(1)\n"
      "v_cvt_pk_bf16_f32 v215, v216, v217\n"
      "v_cvt_pk_bf16_f32 v216, v210, v211\n"
      "v_mfma_f32_16x16x16_bf16 v[148:151], v[164:165], v[134:135], v[148:151]\n"
      "v_cvt_pk_bf16_f32 v217, v212, v213\n"
      "v_cvt_pk_bf16_f32 v80, v104, v105\n"
      "v_cvt_pk_bf16_f32 v81, v106, v107\n"
      "v_mfma_f32_16x16x16_bf16 v[126:129], v[218:219], v[134:135], v[126:129]\n"
      "v_cvt_pk_bf16_f32 v194, v194, v195\n"
      "v_cvt_pk_bf16_f32 v195, v196, v197\n"
      "v_cvt_pk_bf16_f32 v196, v136, v137\n"
      "v_mfma_f32_16x16x16_bf16 v[100:103], v[178:179], v[134:135], v[100:103]\n"
      "v_exp_f32 v148, v148\n"
      "v_exp_f32 v149, v149\n"
      "v_mfma_f32_16x16x16_bf16 v[180:183], v[204:205], v[134:135], v[180:183]\n"
      "v_exp_f32 v150, v150\n"
      "v_exp_f32 v151, v151\n"
      "v_mfma_f32_16x16x32_bf16 v[186:189], a[216:219], v[166:169], 0\n"
      "s_waitcnt lgkmcnt(0)\n"
      "v_exp_f32 v126, v126\n"
      "v_mfma_f32_16x16x32_bf16 v[186:189], a[220:223], v[152:155], v[186:189]\n"
      "v_exp_f32 v127, v127\n"
      "v_mfma_f32_16x16x32_bf16 v[186:189], a[224:227], v[96:99], v[186:189]\n"
      "v_exp_f32 v128, v128\n"
      "v_mfma_f32_16x16x32_bf16 v[182:185], a[240:243], v[166:169], 0\n"
      "v_exp_f32_e64 v129, v129\n"
      "v_mfma_f32_16x16x32_bf16 v[182:185], a[244:247], v[152:155], v[182:185]\n"
      "ds_read_b128 v[210:213], v2 offset:9472\n"
      "v_cvt_pk_bf16_f32 v148, v148, v149\n"
      "v_mfma_f32_16x16x32_bf16 v[182:185], a[248:251], v[96:99], v[182:185]\n"
      "ds_read_b128 v[156:159], v2 offset:12544\n"
      "v_cvt_pk_bf16_f32 v149, v150, v151\n"
      "v_mfma_f32_16x16x32_bf16 v[104:107], v[232:235], v[166:169], 0\n"
      "ds_read_b128 v[166:169], v2 offset:15616\n"
      "v_cvt_pk_bf16_f32 v150, v126, v127\n"
      "v_mfma_f32_16x16x32_bf16 v[104:107], v[236:239], v[152:155], v[104:107]\n"
      "v_cvt_pk_bf16_f32 v151, v128, v129\n"
      "v_exp_f32_e64 v100, v100\n"
      "v_mfma_f32_16x16x32_bf16 v[104:107], v[240:243], v[96:99], v[104:107]\n"
      "v_exp_f32_e64 v101, v101\n"
      "v_mfma_f32_16x16x32_bf16 v[134:137], v[190:193], v[112:115], v[52:55]\n"
      "v_exp_f32_e64 v102, v102\n"
      "v_mfma_f32_16x16x32_bf16 v[152:155], v[174:177], v[112:115], v[56:59]\n"
      "s_waitcnt lgkmcnt(2)\n"
      "v_exp_f32_e64 v103, v103\n"
      "v_mfma_f32_16x16x32_bf16 v[96:99], v[206:209], v[112:115], v[60:63]\n"
      "s_waitcnt lgkmcnt(1)\n"
      "v_exp_f32 v180, v180\n"
      "v_mfma_f32_16x16x32_bf16 v[126:129], v[120:123], v[112:115], v[26:29]\n"
      "s_waitcnt lgkmcnt(0)\n"
      "v_cvt_pk_bf16_f32 v88, v100, v101\n"
      "v_cvt_pk_bf16_f32 v89, v102, v103\n"
      "v_mfma_f32_16x16x16_bf16 v[134:137], v[164:165], v[124:125], v[134:137]\n"
      "v_cvt_pk_bf16_f32 v90, v180, 0\n"
      "v_cvt_pk_bf16_f32 v197, v138, v139\n"
      "v_cvt_pk_bf16_f32 v84, v108, v109\n"
      "v_mfma_f32_16x16x16_bf16 v[152:155], v[218:219], v[124:125], v[152:155]\n"
      "v_cvt_pk_bf16_f32 v85, v110, v111\n"
      "v_mul_f32 v173, v140, v140\n"
      "v_add_f32 v172, v140, v141\n"
      "v_mfma_f32_16x16x16_bf16 v[96:99], v[178:179], v[124:125], v[96:99]\n"
      "v_exp_f32 v134, v134\n"
      "v_exp_f32 v135, v135\n"
      "v_mfma_f32_16x16x16_bf16 v[126:129], v[204:205], v[124:125], v[126:129]\n"
      "v_exp_f32 v136, v136\n"
      "v_exp_f32 v137, v137\n"
      "v_mfma_f32_16x16x32_bf16 v[186:189], a[228:231], v[210:213], v[186:189]\n"
      "ds_read_u16 v78, v11 offset:1168\n"
      "v_exp_f32 v152, v152\n"
      "v_mfma_f32_16x16x32_bf16 v[186:189], a[232:235], v[156:159], v[186:189]\n"
      "ds_read_u16 v82, v11 offset:1200\n"
      "v_exp_f32 v153, v153\n"
      "v_mfma_f32_16x16x32_bf16 v[186:189], a[236:239], v[166:169], v[186:189]\n"
      "v_exp_f32 v154, v154\n"
      "v_mfma_f32_16x16x32_bf16 v[182:185], a[252:255], v[210:213], v[182:185]\n"
      "ds_read_u16 v86, v11 offset:1232\n"
      "v_exp_f32 v155, v155\n"
      "v_mfma_f32_16x16x32_bf16 v[182:185], v[224:227], v[156:159], v[182:185]\n"
      "v_cvt_pk_bf16_f32 v134, v134, v135\n"
      "v_cvt_pk_bf16_f32 v135, v136, v137\n"
      "v_mfma_f32_16x16x32_bf16 v[182:185], v[228:231], v[166:169], v[182:185]\n"
      "v_cvt_pk_bf16_f32 v136, v152, v153\n"
      "v_cvt_pk_bf16_f32 v137, v154, v155\n"
      "v_mfma_f32_16x16x32_bf16 v[104:107], v[244:247], v[210:213], v[104:107]\n"
      "s_waitcnt lgkmcnt(2)\n"
      "v_exp_f32 v96, v96\n"
      "v_mfma_f32_16x16x32_bf16 v[104:107], v[248:251], v[156:159], v[104:107]\n"
      "s_waitcnt lgkmcnt(1)\n"
      "v_exp_f32 v97, v97\n"
      "v_mfma_f32_16x16x32_bf16 v[104:107], v[252:255], v[166:169], v[104:107]\n"
      "v_exp_f32_e64 v98, v98\n"
      "v_mfma_f32_16x16x32_bf16 v[100:103], v[32:35], v[148:151], 0\n"
      "s_waitcnt lgkmcnt(0)\n"
      "v_exp_f32 v99, v99\n"
      "v_mfma_f32_16x16x32_bf16 v[100:103], v[32:35], v[88:91], v[100:103]\n"
      "v_exp_f32_e64 v126, v126\n"
      "v_mfma_f32_16x16x32_bf16 v[210:213], v[198:201], v[148:151], 0\n"
      "v_cvt_pk_bf16_f32 v92, v96, v97\n"
      "v_cvt_pk_bf16_f32 v93, v98, v99\n"
      "v_mfma_f32_16x16x32_bf16 v[210:213], v[76:79], v[88:91], v[210:213]\n"
      "ds_read_b128 v[154:157], v2 offset:512\n"
      "v_cvt_pk_bf16_f32 v94, v126, 0\n"
      "v_mfma_f32_16x16x32_bf16 v[124:127], v[214:217], v[148:151], 0\n"
      "ds_read_b128 v[112:115], v2 offset:3584\n"
      "v_mul_f32 v102, v186, v186\n"
      "v_mfma_f32_16x16x32_bf16 v[124:127], v[80:83], v[88:91], v[124:127]\n"
      "ds_read_b128 v[166:169], v2 offset:6656\n"
      "v_fmac_f32 v173, v141, v141\n"
      "v_mfma_f32_16x16x32_bf16 v[96:99], v[194:197], v[148:151], 0\n"
      "v_add_f32 v101, v186, v187\n"
      "v_fmac_f32 v102, v187, v187\n"
      "v_mfma_f32_16x16x32_bf16 v[96:99], v[84:87], v[88:91], v[96:99]\n"
      "v_add_f32 v172, v172, v142\n"
      "v_fmac_f32 v173, v142, v142\n"
      "v_add_f32 v101, v101, v188\n"
      "v_fmac_f32 v102, v188, v188\n"
      "v_add_f32 v172, v172, v143\n"
      "v_fmac_f32 v173, v143, v143\n"
      "v_add_f32 v101, v101, v189\n"
      "v_fmac_f32 v102, v189, v189\n"
      "v_add_f32 v172, v172, v220\n"
      "v_fmac_f32 v173, v220, v220\n"
      "v_add_f32 v101, v101, v182\n"
      "v_fmac_f32 v102, v182, v182\n"
      "v_add_f32 v172, v172, v221\n"
      "v_fmac_f32 v173, v221, v221\n"
      "v_add_f32 v101, v101, v183\n"
      "v_fmac_f32 v102, v183, v183\n"
      "v_add_f32 v172, v172, v222\n"
      "v_fmac_f32 v173, v222, v222\n"
      "v_add_f32 v101, v101, v184\n"
      "v_fmac_f32 v102, v184, v184\n"
      "v_add_f32 v172, v172, v223\n"
      "v_fmac_f32 v173, v223, v223\n"
      "v_add_f32 v101, v101, v185\n"
      "v_fmac_f32 v102, v185, v185\n"
      "v_add_f32 v172, v172, v160\n"
      "v_fmac_f32 v173, v160, v160\n"
      "v_add_f32 v101, v101, v104\n"
      "v_fmac_f32 v102, v104, v104\n"
      "v_add_f32 v172, v172, v161\n"
      "v_fmac_f32 v173, v161, v161\n"
      "v_add_f32 v101, v101, v105\n"
      "v_fmac_f32 v102, v105, v105\n"
      "v_add_f32 v172, v172, v162\n"
      "v_fmac_f32 v173, v162, v162\n"
      "v_add_f32 v101, v101, v106\n"
      "v_fmac_f32 v102, v106, v106\n"
      "v_add_f32 v172, v172, v163\n"
      "v_fmac_f32 v173, v163, v163\n"
      "v_add_f32 v101, v101, v107\n"
      "v_fmac_f32 v102, v107, v107\n"
      "v_rcp_f32 v100, v100\n"
      "v_permlane16_swap_b32 v172, v173\n"
      "v_permlane16_swap_b32 v101, v102\n"
      "v_mul_f32 v210, v100, v210\n"
      "v_mul_f32 v211, v100, v211\n"
      "v_cvt_pk_bf16_f32 v210, v210, v211\n"
      "v_mul_f32 v212, v100, v212\n"
      "v_mul_f32 v213, v100, v213\n"
      "v_add_f32 v172, v172, v173\n"
      "v_mfma_f32_16x16x32_bf16 v[108:111], v[190:193], v[116:119], v[64:67]\n"
      "s_waitcnt lgkmcnt(0)\n"
      "v_add_f32 v101, v101, v102\n"
      "v_cvt_pk_bf16_f32 v211, v212, v213\n"
      "v_mul_f32 v124, v100, v124\n"
      "v_mul_f32 v125, v100, v125\n"
      "v_mul_f32 v126, v100, v126\n"
      "v_mul_f32 v127, v100, v127\n"
      "v_cvt_pk_bf16_f32 v212, v124, v125\n"
      "v_cvt_pk_bf16_f32 v213, v126, v127\n"
      "v_mfma_f32_16x16x32_bf16 v[150:153], v[174:177], v[116:119], v[68:71]\n"
      "ds_write_b128 v3, v[210:213] offset:18432\n"
      "v_mfma_f32_16x16x32_bf16 v[210:213], v[206:209], v[116:119], v[72:75]\n"
      "v_mul_f32 v96, v100, v96\n"
      "v_mul_f32 v97, v100, v97\n"
      "v_mfma_f32_16x16x32_bf16 v[124:127], v[120:123], v[116:119], v[28:31]\n"
      "v_cvt_pk_bf16_f32 v96, v96, v97\n"
      "v_mul_f32 v98, v100, v98\n"
      "v_mfma_f32_16x16x16_bf16 v[108:111], v[164:165], v[170:171], v[108:111]\n"
      "v_mul_f32 v99, v100, v99\n"
      "v_permlane32_swap_b32_e64 v172, v101\n"
      "v_mfma_f32_16x16x16_bf16 v[150:153], v[218:219], v[170:171], v[150:153]\n"
      "s_waitcnt vmcnt(6)\n"
      "v_or3_b32 v130, v130, v131, v132\n"
      "v_cvt_pk_bf16_f32 v97, v98, v99\n"
      "s_waitcnt vmcnt(5)\n"
      "v_or3_b32 v130, v130, v133, v39\n"
      "v_mfma_f32_16x16x16_bf16 v[210:213], v[178:179], v[170:171], v[210:213]\n"
      "v_add_f32 v172, v172, v101\n"
      "v_exp_f32 v108, v108\n"
      "v_mfma_f32_16x16x16_bf16 v[124:127], v[204:205], v[170:171], v[124:127]\n"
      "v_exp_f32 v109, v109\n"
      "v_exp_f32 v110, v110\n"
      "v_mfma_f32_16x16x32_bf16 v[100:103], a[216:219], v[154:157], 0\n"
      "ds_write_b64 v4, v[96:97] offset:18432\n"
      "v_mfma_f32_16x16x32_bf16 v[100:103], a[220:223], v[112:115], v[100:103]\n"
      "v_exp_f32_e64 v111, v111\n"
      "v_mfma_f32_16x16x32_bf16 v[100:103], a[224:227], v[166:169], v[100:103]\n"
      "v_exp_f32_e64 v150, v150\n"
      "v_mfma_f32_16x16x32_bf16 v[126:129], a[240:243], v[154:157], 0\n"
      "ds_write_b32 v8, v172 offset:2048\n"
      "v_mfma_f32_16x16x32_bf16 v[126:129], a[244:247], v[112:115], v[126:129]\n"
      "ds_read_b128 v[170:173], v10 offset:1168\n"
      "v_exp_f32 v151, v151\n"
      "v_mfma_f32_16x16x32_bf16 v[126:129], a[248:251], v[166:169], v[126:129]\n"
      "v_exp_f32 v152, v152\n"
      "v_mfma_f32_16x16x32_bf16 v[96:99], v[232:235], v[154:157], 0\n"
      "v_exp_f32_e64 v153, v153\n"
      "v_mfma_f32_16x16x32_bf16 v[96:99], v[236:239], v[112:115], v[96:99]\n"
      "v_and_b32 v130, 0x7fff7fff, v130\n"
      "v_cvt_pk_bf16_f32 v108, v108, v109\n"
      "v_mfma_f32_16x16x32_bf16 v[96:99], v[240:243], v[166:169], v[96:99]\n"
      "ds_read_b64 v[88:89], v10 offset:1184\n"
      "v_cvt_pk_bf16_f32 v109, v110, v111\n"
      "v_mfma_f32_16x16x32_bf16 v[156:159], v[32:35], v[134:137], 0\n"
      "ds_write_b32 v31, v130 offset:1024\n"
      "v_mfma_f32_16x16x32_bf16 v[156:159], v[32:35], v[92:95], v[156:159]\n"
      "v_cvt_pk_bf16_f32 v110, v150, v151\n"
      "v_cvt_pk_bf16_f32 v111, v152, v153\n"
      "v_mfma_f32_16x16x32_bf16 v[112:115], v[198:201], v[134:137], 0\n"
      "s_waitcnt lgkmcnt(2)\n"
      "v_exp_f32 v210, v210\n"
      "v_mfma_f32_16x16x32_bf16 v[112:115], v[76:79], v[92:95], v[112:115]\n"
      "ds_read_b128 v[166:169], v2 offset:9728\n"
      "v_exp_f32 v211, v211\n"
      "v_mfma_f32_16x16x32_bf16 v[116:119], v[214:217], v[134:137], 0\n"
      "v_rcp_f32 v156, v156\n"
      "v_mfma_f32_16x16x32_bf16 v[116:119], v[80:83], v[92:95], v[116:119]\n"
      "s_waitcnt lgkmcnt(2)\n"
      "v_exp_f32 v212, v212\n"
      "v_mfma_f32_16x16x32_bf16 v[152:155], v[194:197], v[134:137], 0\n"
      "v_mul_f32 v112, v156, v112\n"
      "v_mul_f32 v113, v156, v113\n"
      "v_mfma_f32_16x16x32_bf16 v[152:155], v[84:87], v[92:95], v[152:155]\n"
      "ds_read_b128 v[136:139], v2 offset:12800\n"
      "v_cvt_pk_bf16_f32 v112, v112, v113\n"
      "v_mfma_f32_16x16x32_bf16 v[148:151], v[170:173], v[190:193], v[30:33]\n"
      "v_mul_f32 v114, v156, v114\n"
      "v_mul_f32 v115, v156, v115\n"
      "v_mfma_f32_16x16x32_bf16 v[190:193], v[170:173], v[174:177], v[30:33]\n"
      "s_waitcnt lgkmcnt(1)\n"
      "v_cvt_pk_bf16_f32 v113, v114, v115\n"
      "v_mul_f32 v116, v156, v116\n"
      "v_mfma_f32_16x16x32_bf16 v[130:133], v[170:173], v[206:209], v[30:33]\n"
      "v_mul_f32 v117, v156, v117\n"
      "v_mul_f32 v118, v156, v118\n"
      "v_mfma_f32_16x16x32_bf16 v[206:209], v[170:173], v[120:123], v[30:33]\n"
      "ds_read_b128 v[170:173], v2 offset:15872\n"
      "v_mul_f32_e64 v119, v156, v119\n"
      "v_mfma_f32_16x16x16_bf16 v[148:151], v[88:89], v[164:165], v[148:151]\n"
      "v_cvt_pk_bf16_f32 v114, v116, v117\n"
      "v_cvt_pk_bf16_f32 v115, v118, v119\n"
      "v_mul_f32_e64 v152, v156, v152\n"
      "v_mfma_f32_16x16x16_bf16 v[190:193], v[88:89], v[218:219], v[190:193]\n"
      "v_mul_f32 v153, v156, v153\n"
      "v_cvt_pk_bf16_f32 v152, v152, v153\n"
      "v_mul_f32 v154, v156, v154\n"
      "v_mfma_f32_16x16x16_bf16 v[130:133], v[88:89], v[178:179], v[130:133]\n"
      "v_mul_f32 v155, v156, v155\n"
      "v_cvt_pk_bf16_f32 v153, v154, v155\n"
      "v_exp_f32 v213, v213\n"
      "v_mfma_f32_16x16x16_bf16 v[206:209], v[88:89], v[204:205], v[206:209]\n"
      "v_cndmask_b32 v148, v148, v190, s[58:59]\n"
      "v_exp_f32 v124, v124\n"
      "v_mfma_f32_16x16x32_bf16 v[100:103], a[228:231], v[166:169], v[100:103]\n"
      "s_waitcnt lgkmcnt(1)\n"
      "v_cndmask_b32 v148, v148, v130, s[60:61]\n"
      "v_cvt_pk_bf16_f32 v88, v210, v211\n"
      "v_mfma_f32_16x16x32_bf16 v[100:103], a[232:235], v[136:139], v[100:103]\n"
      "s_waitcnt lgkmcnt(0)\n"
      "v_cndmask_b32 v148, v148, v206, s[62:63]\n"
      "v_exp_f32 v148, v148\n"
      "v_mfma_f32_16x16x32_bf16 v[100:103], a[236:239], v[170:173], v[100:103]\n"
      "ds_write_b128 v3, v[112:115] offset:18688\n"
      "v_mfma_f32_16x16x32_bf16 v[126:129], a[252:255], v[166:169], v[126:129]\n"
      "v_cvt_pk_bf16_f32 v148, v148, v148\n"
      "v_cvt_pk_bf16_f32 v89, v212, v213\n"
      "v_mfma_f32_16x16x32_bf16 v[126:129], v[224:227], v[136:139], v[126:129]\n"
      "v_cvt_pk_bf16_f32 v90, v124, 0\n"
      "v_readlane_b32 s45, v37, s43\n"
      "v_mfma_f32_16x16x32_bf16 v[126:129], v[228:231], v[170:173], v[126:129]\n"
      "ds_write_b64 v4, v[152:153] offset:18688\n"
      "v_mfma_f32_16x16x32_bf16 v[96:99], v[244:247], v[166:169], v[96:99]\n"
      "v_mul_f32 v115, v100, v100\n"
      "v_add_f32 v114, v100, v101\n"
      "v_mfma_f32_16x16x32_bf16 v[96:99], v[248:251], v[136:139], v[96:99]\n"
      "v_fmac_f32 v115, v101, v101\n"
      "v_add_f32 v114, v114, v102\n"
      "v_mfma_f32_16x16x32_bf16 v[96:99], v[252:255], v[170:173], v[96:99]\n"
      "ds_write_b16 v13, v148\n"
      "v_mfma_f32_16x16x32_bf16 v[202:205], v[32:35], v[108:111], 0\n"
      "ds_read_b128 v[210:213], v14\n"
      "v_fmac_f32 v115, v102, v102\n"
      "v_mfma_f32_16x16x32_bf16 v[202:205], v[32:35], v[88:91], v[202:205]\n"
      "ds_read_b128 v[136:139], v14 offset:64\n"
      "v_add_f32 v114, v114, v103\n"
      "v_mfma_f32_16x16x32_bf16 v[130:133], v[198:201], v[108:111], 0\n"
      "v_fmac_f32 v115, v103, v103\n"
      "v_add_f32 v114, v114, v126\n"
      "v_mfma_f32_16x16x32_bf16 v[130:133], v[76:79], v[88:91], v[130:133]\n"
      "v_fmac_f32 v115, v126, v126\n"
      "v_add_f32 v114, v114, v127\n"
      "v_mfma_f32_16x16x32_bf16 v[204:207], v[214:217], v[108:111], 0\n"
      "v_fmac_f32 v115, v127, v127\n"
      "v_add_f32 v114, v114, v128\n"
      "v_mfma_f32_16x16x32_bf16 v[204:207], v[80:83], v[88:91], v[204:207]\n"
      "v_fmac_f32 v115, v128, v128\n"
      "v_add_f32 v114, v114, v129\n"
      "v_mfma_f32_16x16x32_bf16 v[156:159], v[194:197], v[108:111], 0\n"
      "v_fmac_f32 v115, v129, v129\n"
      "v_add_f32 v114, v114, v96\n"
      "v_mfma_f32_16x16x32_bf16 v[156:159], v[84:87], v[88:91], v[156:159]\n"
      "s_waitcnt lgkmcnt(1)\n"
      "v_fmac_f32 v115, v96, v96\n"
      "v_add_f32 v114, v114, v97\n"
      "v_mfma_f32_16x16x32_bf16 v[108:111], v[210:213], v[32:35], 0\n"
      "s_waitcnt lgkmcnt(0)\n"
      "v_fmac_f32 v115, v97, v97\n"
      "v_add_f32 v114, v114, v98\n"
      "v_mfma_f32_16x16x32_bf16 v[108:111], v[136:139], v[32:35], v[108:111]\n"
      "v_fmac_f32 v115, v98, v98\n"
      "v_add_f32 v114, v114, v99\n"
      "v_fmac_f32 v115, v99, v99\n"
      "v_rcp_f32 v202, v202\n"
      "v_readlane_b32 s46, v37, s42\n"
      "v_permlane16_swap_b32 v114, v115\n"
      "v_mul_f32 v130, v202, v130\n"
      "v_mul_f32 v131, v202, v131\n"
      "v_add_f32 v114, v114, v115\n"
      "v_cvt_pk_bf16_f32 v130, v130, v131\n"
      "v_mul_f32 v132, v202, v132\n"
      "v_mul_f32 v133, v202, v133\n"
      "v_mov_b32_e64 v115, v114\n"
      "v_cvt_pk_bf16_f32 v131, v132, v133\n"
      "v_mul_f32 v204, v202, v204\n"
      "v_mul_f32 v205, v202, v205\n"
      "v_mul_f32 v206, v202, v206\n"
      "v_mul_f32 v207, v202, v207\n"
      "v_cvt_pk_bf16_f32 v132, v204, v205\n"
      "v_cvt_pk_bf16_f32 v133, v206, v207\n"
      "v_mul_f32 v156, v202, v156\n"
      "v_mul_f32 v157, v202, v157\n"
      "ds_write_b128 v3, v[130:133] offset:18944\n"
      "v_cvt_pk_bf16_f32 v156, v156, v157\n"
      "v_mul_f32 v158, v202, v158\n"
      "v_mul_f32 v159, v202, v159\n"
      "v_permlane32_swap_b32 v114, v115\n"
      "v_cvt_pk_bf16_f32 v157, v158, v159\n"
      "v_add_f32 v114, v114, v115\n"
      "v_or3_b32 v144, v144, v145, v146\n"
      "ds_write_b64 v4, v[156:157] offset:18944\n"
      "v_or_b32_e64 v144, v144, v147\n"
      "v_cmp_ne_u32 s[56:57], 0, v144\n"
      "v_readlane_b32 s47, v38, s43\n"
      "v_add_u32 v10, 2336, v10\n"
      "v_add_u32 v11, 2336, v11\n"
      "ds_write_b32 v8, v114 offset:2560\n"
      "s_or_b32 s95, s45, s46\n"
      "s_or_b32 s95, s95, s48\n"
      "s_cmp_ge_u32 s95, 0x40000000\n"
      "s_cselect_b32 s94, -1, -2\n"
      "s_cmp_gt_u32 s42, s44\n"
      "s_cselect_b32 s95, s94, s95\n"
      "s_cmp_ge_u32 s95, 0x40000000\n"
      "s_cselect_b64 vcc, -1, s[56:57]\n"
      "s_and_b32 s47, s47, -4\n"
      "s_add_u32 s42, s42, 1\n"
      "s_add_u32 s43, s43, 1\n"
      "s_waitcnt lgkmcnt(0)\n"
      "s_barrier\n"
      "s_cbranch_vccnz L_disp0_%=\n"
      "L_loop0_%=:\n"
      "v_mfma_f32_16x16x32_bf16 v[174:177], v[210:213], v[198:201], 0\n"
      "ds_read_b128 v[114:117], v9 offset:2048\n"
      "ds_read_b128 v[198:201], v9 offset:3072\n"
      "v_mfma_f32_16x16x32_bf16 v[174:177], v[136:139], v[76:79], v[174:177]\n"
      "ds_read_b128 v[178:181], v0\n"
      "ds_read_b128 v[156:159], v1\n"
      "v_mfma_f32_16x16x32_bf16 v[190:193], v[210:213], v[214:217], 0\n"
      "ds_read_b128 v[148:151], v0 offset:6144\n"
      "s_add_u32 m0, s64, 18432\n"
      "v_mfma_f32_16x16x32_bf16 v[190:193], v[136:139], v[80:83], v[190:193]\n"
      "buffer_load_dwordx4 v16, s[16:19], s45 offen  lds\n"
      "v_mfma_f32_16x16x32_bf16 v[214:217], v[210:213], v[194:197], 0\n"
      "s_add_u32 m0, s64, 22528\n"
      "v_mfma_f32_16x16x32_bf16 v[214:217], v[136:139], v[84:87], v[214:217]\n"
      "v_mov_b32_dpp v108, v108 quad_perm:[0,0,0,0] row_mask:0xf bank_mask:0xf\n"
      "v_rcp_f32 v108, v108\n"
      "s_waitcnt lgkmcnt(4)\n"
      "v_add_f32 v114, v114, v116\n"
      "v_cndmask_b32 v174, v174, v190, s[58:59]\n"
      "s_waitcnt lgkmcnt(3)\n"
      "v_add_f32 v198, v198, v200\n"
      "s_waitcnt lgkmcnt(2)\n"
      "v_mfma_f32_16x16x32_bf16 v[152:155], a[0:3], v[178:181], 0\n"
      "buffer_load_dwordx4 v17, s[16:19], s45 offen  lds\n"
      "v_mfma_f32_16x16x32_bf16 v[164:167], a[24:27], v[178:181], 0\n"
      "s_add_u32 m0, s64, 26624\n"
      "v_cndmask_b32 v174, v174, v214, s[60:61]\n"
      "v_mul_f32 v174, v108, v174\n"
      "v_mfma_f32_16x16x32_bf16 v[130:133], a[48:51], v[178:181], 0\n"
      "s_waitcnt lgkmcnt(0)\n"
      "v_cvt_pk_bf16_f32 v174, v174, v174\n"
      "v_add_f32_e64 v115, v115, v117\n"
      "v_mfma_f32_16x16x32_bf16 v[206:209], a[72:75], v[178:181], 0\n"
      "ds_write_b16 v12, v174 offset:16\n"
      "v_mfma_f32_16x16x32_bf16 v[110:113], a[96:99], v[178:181], 0\n"
      "buffer_load_dwordx4 v18, s[16:19], s45 offen  lds\n"
      "v_mfma_f32_16x16x32_bf16 v[172:175], a[120:123], v[178:181], 0\n"
      "ds_read_b128 v[118:121], v1 offset:6144\n"
      "v_add_f32 v199, v199, v201\n"
      "v_mfma_f32_16x16x32_bf16 v[152:155], a[4:7], v[156:159], v[152:155]\n"
      "ds_read_b128 v[210:213], v0 offset:12288\n"
      "v_add_f32 v114, v114, v198\n"
      "v_mfma_f32_16x16x32_bf16 v[152:155], a[8:11], v[148:151], v[152:155]\n"
      "ds_read_b128 v[190:193], v1 offset:12288\n"
      "v_add_f32_e64 v115, v115, v199\n"
      "v_mfma_f32_16x16x32_bf16 v[164:167], a[28:31], v[156:159], v[164:167]\n"
      "s_add_u32 m0, s64, 30720\n"
      "v_fma_f32 v198, -v114, v114, s52\n"
      "v_fmac_f32 v198, 0x43400000, v115\n"
      "v_mfma_f32_16x16x32_bf16 v[164:167], a[32:35], v[148:151], v[164:167]\n"
      "buffer_load_dwordx4 v19, s[16:19], s45 offen  lds\n"
      "v_mfma_f32_16x16x32_bf16 v[130:133], a[52:55], v[156:159], v[130:133]\n"
      "s_add_u32 m0, s65, 34816\n"
      "v_max_f32 v198, s52, v198\n"
      "v_rsq_f32 v198, v198\n"
      "v_mfma_f32_16x16x32_bf16 v[130:133], a[56:59], v[148:151], v[130:133]\n"
      "s_waitcnt lgkmcnt(2)\n"
      "v_mul_f32 v199, 0x43400000, v198\n"
      "v_mul_f32 v201, -v114, v198\n"
      "v_mfma_f32_16x16x32_bf16 v[206:209], a[76:79], v[156:159], v[206:209]\n"
      "s_waitcnt lgkmcnt(1)\n"
      "v_mov_b32 v202, v199\n"
      "v_mov_b32 v205, v201\n"
      "v_mfma_f32_16x16x32_bf16 v[206:209], a[80:83], v[148:151], v[206:209]\n"
      "buffer_load_dwordx4 v15, s[16:19], s45 offen  lds\n"
      "v_mfma_f32_16x16x32_bf16 v[110:113], a[100:103], v[156:159], v[110:113]\n"
      "s_waitcnt lgkmcnt(0)\n"
      "v_permlane32_swap_b32 v199, v202\n"
      "v_mfma_f32_16x16x32_bf16 v[110:113], a[104:107], v[148:151], v[110:113]\n"
      "v_permlane32_swap_b32 v201, v205\n"
      "v_mfma_f32_16x16x32_bf16 v[172:175], a[124:127], v[156:159], v[172:175]\n"
      "v_mov_b32 v114, v199\n"
      "v_mov_b32 v115, v201\n"
      "v_mfma_f32_16x16x32_bf16 v[172:175], a[128:131], v[148:151], v[172:175]\n"
      "buffer_load_dwordx4 v[136:139], v27, s[24:27], s47 offen\n"
      "v_mfma_f32_16x16x32_bf16 v[152:155], a[12:15], v[118:121], v[152:155]\n"
      "v_permlane16_swap_b32 v199, v114\n"
      "v_mfma_f32_16x16x32_bf16 v[152:155], a[16:19], v[210:213], v[152:155]\n"
      "v_permlane16_swap_b32_e64 v201, v115\n"
      "v_mfma_f32_16x16x32_bf16 v[152:155], a[20:23], v[190:193], v[152:155]\n"
      "v_fma_f32 v140, v140, v199, v201\n"
      "v_fma_f32 v141, v141, v199, v201\n"
      "v_mfma_f32_16x16x32_bf16 v[164:167], a[36:39], v[118:121], v[164:167]\n"
      "buffer_load_dword v39, v29, s[24:27], s47 offen\n"
      "v_mfma_f32_16x16x32_bf16 v[164:167], a[40:43], v[210:213], v[164:167]\n"
      "v_cvt_pk_bf16_f32 v140, v140, v141\n"
      "v_fma_f32 v142, v142, v199, v201\n"
      "v_mfma_f32_16x16x32_bf16 v[164:167], a[44:47], v[190:193], v[164:167]\n"
      "v_fma_f32 v143, v143, v199, v201\n"
      "v_fma_f32 v160, v160, v199, v201\n"
      "v_mfma_f32_16x16x32_bf16 v[130:133], a[60:63], v[118:121], v[130:133]\n"
      "v_fma_f32 v161, v161, v199, v201\n"
      "v_cvt_pk_bf16_f32 v141, v142, v143\n"
      "v_mfma_f32_16x16x32_bf16 v[130:133], a[64:67], v[210:213], v[130:133]\n"
      "v_fma_f32 v220, v220, v199, v201\n"
      "v_fma_f32 v221, v221, v199, v201\n"
      "v_mfma_f32_16x16x32_bf16 v[130:133], a[68:71], v[190:193], v[130:133]\n"
      "v_fma_f32 v222, v222, v199, v201\n"
      "v_fma_f32 v223, v223, v199, v201\n"
      "v_mfma_f32_16x16x32_bf16 v[206:209], a[84:87], v[118:121], v[206:209]\n"
      "v_cvt_pk_bf16_f32 v160, v160, v161\n"
      "v_fma_f32 v162, v162, v199, v201\n"
      "v_mfma_f32_16x16x32_bf16 v[206:209], a[88:91], v[210:213], v[206:209]\n"
      "v_fma_f32 v163, v163, v199, v201\n"
      "v_cvt_pk_bf16_f32 v142, v220, v221\n"
      "v_mfma_f32_16x16x32_bf16 v[206:209], a[92:95], v[190:193], v[206:209]\n"
      "v_cvt_pk_bf16_f32 v143, v222, v223\n"
      "v_cvt_pk_bf16_f32 v161, v162, v163\n"
      "v_mfma_f32_16x16x32_bf16 v[110:113], a[108:111], v[118:121], v[110:113]\n"
      "ds_write_b128 v5, v[140:143]\n"
      "v_mfma_f32_16x16x32_bf16 v[110:113], a[112:115], v[210:213], v[110:113]\n"
      "v_fma_f32 v186, v186, v114, v115\n"
      "v_fma_f32 v187, v187, v114, v115\n"
      "v_mfma_f32_16x16x32_bf16 v[110:113], a[116:119], v[190:193], v[110:113]\n"
      "v_cvt_pk_bf16_f32 v186, v186, v187\n"
      "v_fma_f32 v188, v188, v114, v115\n"
      "v_mfma_f32_16x16x32_bf16 v[172:175], a[132:135], v[118:121], v[172:175]\n"
      "ds_write_b64 v6, v[160:161]\n"
      "v_mfma_f32_16x16x32_bf16 v[172:175], a[136:139], v[210:213], v[172:175]\n"
      "v_fma_f32 v189, v189, v114, v115\n"
      "v_fma_f32 v104, v104, v114, v115\n"
      "v_mfma_f32_16x16x32_bf16 v[172:175], a[140:143], v[190:193], v[172:175]\n"
      "v_fma_f32 v105, v105, v114, v115\n"
      "v_cvt_pk_bf16_f32 v187, v188, v189\n"
      "v_mfma_f32_16x16x32_bf16 v[198:201], v[178:181], a[144:147], 0\n"
      "v_fma_f32 v182, v182, v114, v115\n"
      "v_fma_f32 v183, v183, v114, v115\n"
      "v_mfma_f32_16x16x32_bf16 v[198:201], v[156:159], a[148:151], v[198:201]\n"
      "v_fma_f32 v184, v184, v114, v115\n"
      "v_fma_f32 v185, v185, v114, v115\n"
      "v_mfma_f32_16x16x32_bf16 v[198:201], v[148:151], a[152:155], v[198:201]\n"
      "v_cvt_pk_bf16_f32 v104, v104, v105\n"
      "v_fma_f32 v106, v106, v114, v115\n"
      "v_mfma_f32_16x16x32_bf16 v[198:201], v[118:121], a[156:159], v[198:201]\n"
      "v_fma_f32 v107, v107, v114, v115\n"
      "v_cvt_pk_bf16_f32 v188, v182, v183\n"
      "v_mfma_f32_16x16x32_bf16 v[198:201], v[210:213], a[160:163], v[198:201]\n"
      "v_cvt_pk_bf16_f32 v189, v184, v185\n"
      "v_cvt_pk_bf16_f32 v105, v106, v107\n"
      "v_mfma_f32_16x16x32_bf16 v[198:201], v[190:193], a[164:167], v[198:201]\n"
      "ds_write_b128 v5, v[186:189] offset:6144\n"
      "v_mfma_f32_16x16x32_bf16 v[214:217], v[178:181], a[168:171], 0\n"
      "ds_read_b128 v[114:117], v0 offset:2048\n"
      "v_fma_f32 v100, v100, v202, v205\n"
      "v_mfma_f32_16x16x32_bf16 v[214:217], v[156:159], a[172:175], v[214:217]\n"
      "ds_read_b128 v[140:143], v1 offset:2048\n"
      "v_fma_f32 v101, v101, v202, v205\n"
      "v_mfma_f32_16x16x32_bf16 v[214:217], v[148:151], a[176:179], v[214:217]\n"
      "ds_write_b64 v6, v[104:105] offset:6144\n"
      "v_mfma_f32_16x16x32_bf16 v[214:217], v[118:121], a[180:183], v[214:217]\n"
      "ds_read_b128 v[186:189], v0 offset:8192\n"
      "v_cvt_pk_bf16_f32 v100, v100, v101\n"
      "v_mfma_f32_16x16x32_bf16 v[214:217], v[210:213], a[184:187], v[214:217]\n"
      "ds_read_b128 v[182:185], v1 offset:8192\n"
      "v_fma_f32 v102, v102, v202, v205\n"
      "v_mfma_f32_16x16x32_bf16 v[214:217], v[190:193], a[188:191], v[214:217]\n"
      "ds_read_b128 v[122:125], v0 offset:14336\n"
      "v_fma_f32 v103, v103, v202, v205\n"
      "v_mfma_f32_16x16x32_bf16 v[194:197], v[178:181], a[192:195], 0\n"
      "ds_read_b128 v[168:171], v1 offset:14336\n"
      "v_fma_f32 v96, v96, v202, v205\n"
      "v_mfma_f32_16x16x32_bf16 v[194:197], v[156:159], a[196:199], v[194:197]\n"
      "s_waitcnt lgkmcnt(6)\n"
      "v_fma_f32 v97, v97, v202, v205\n"
      "v_cvt_pk_bf16_f32 v101, v102, v103\n"
      "v_mfma_f32_16x16x32_bf16 v[194:197], v[148:151], a[200:203], v[194:197]\n"
      "s_waitcnt lgkmcnt(5)\n"
      "v_fma_f32 v126, v126, v202, v205\n"
      "v_fma_f32 v127, v127, v202, v205\n"
      "v_mfma_f32_16x16x32_bf16 v[194:197], v[118:121], a[204:207], v[194:197]\n"
      "v_fma_f32 v128, v128, v202, v205\n"
      "v_fma_f32 v129, v129, v202, v205\n"
      "v_mfma_f32_16x16x32_bf16 v[194:197], v[210:213], a[208:211], v[194:197]\n"
      "s_waitcnt lgkmcnt(3)\n"
      "v_cvt_pk_bf16_f32 v96, v96, v97\n"
      "v_fma_f32 v98, v98, v202, v205\n"
      "v_mfma_f32_16x16x32_bf16 v[194:197], v[190:193], a[212:215], v[194:197]\n"
      "s_waitcnt lgkmcnt(2)\n"
      "v_fma_f32 v99, v99, v202, v205\n"
      "v_cvt_pk_bf16_f32 v102, v126, v127\n"
      "v_mfma_f32_16x16x32_bf16 v[156:159], a[0:3], v[114:117], 0\n"
      "s_waitcnt lgkmcnt(1)\n"
      "v_cvt_pk_bf16_f32 v103, v128, v129\n"
      "v_cvt_pk_bf16_f32 v97, v98, v99\n"
      "v_mfma_f32_16x16x32_bf16 v[156:159], a[4:7], v[140:143], v[156:159]\n"
      "ds_write_b128 v5, v[100:103] offset:12288\n"
      "v_mfma_f32_16x16x32_bf16 v[156:159], a[8:11], v[186:189], v[156:159]\n"
      "s_waitcnt lgkmcnt(1)\n"
      "v_fma_f32 v148, v152, v152, s51\n"
      "v_fma_f32 v129, v206, v206, s51\n"
      "v_mfma_f32_16x16x32_bf16 v[156:159], a[12:15], v[182:185], v[156:159]\n"
      "v_fmac_f32 v148, v153, v153\n"
      "v_fmac_f32 v129, v207, v207\n"
      "v_mfma_f32_16x16x32_bf16 v[156:159], a[16:19], v[122:125], v[156:159]\n"
      "ds_write_b64 v6, v[96:97] offset:12288\n"
      "v_mfma_f32_16x16x32_bf16 v[156:159], a[20:23], v[168:171], v[156:159]\n"
      "v_fmac_f32 v148, v154, v154\n"
      "v_fmac_f32 v129, v208, v208\n"
      "v_mfma_f32_16x16x32_bf16 v[210:213], a[24:27], v[114:117], 0\n"
      "v_fmac_f32 v148, v155, v155\n"
      "v_fmac_f32 v129, v209, v209\n"
      "v_mfma_f32_16x16x32_bf16 v[210:213], a[28:31], v[140:143], v[210:213]\n"
      "v_fmac_f32 v148, v164, v164\n"
      "v_fmac_f32 v129, v110, v110\n"
      "v_mfma_f32_16x16x32_bf16 v[210:213], a[32:35], v[186:189], v[210:213]\n"
      "v_fmac_f32 v148, v165, v165\n"
      "v_fmac_f32 v129, v111, v111\n"
      "v_mfma_f32_16x16x32_bf16 v[210:213], a[36:39], v[182:185], v[210:213]\n"
      "v_fmac_f32 v148, v166, v166\n"
      "v_fmac_f32 v129, v112, v112\n"
      "v_mfma_f32_16x16x32_bf16 v[210:213], a[40:43], v[122:125], v[210:213]\n"
      "v_fmac_f32 v148, v167, v167\n"
      "v_fmac_f32 v129, v113, v113\n"
      "v_mfma_f32_16x16x32_bf16 v[210:213], a[44:47], v[168:171], v[210:213]\n"
      "v_fmac_f32 v148, v130, v130\n"
      "v_fmac_f32 v129, v172, v172\n"
      "v_mfma_f32_16x16x32_bf16 v[96:99], a[48:51], v[114:117], 0\n"
      "v_fmac_f32 v148, v131, v131\n"
      "v_fmac_f32 v129, v173, v173\n"
      "v_mfma_f32_16x16x32_bf16 v[96:99], a[52:55], v[140:143], v[96:99]\n"
      "v_fmac_f32 v148, v132, v132\n"
      "v_fmac_f32 v129, v174, v174\n"
      "v_mfma_f32_16x16x32_bf16 v[96:99], a[56:59], v[186:189], v[96:99]\n"
      "v_fmac_f32 v148, v133, v133\n"
      "v_fmac_f32 v129, v175, v175\n"
      "v_mfma_f32_16x16x32_bf16 v[96:99], a[60:63], v[182:185], v[96:99]\n"
      "v_fma_f32 v101, v156, v156, s51\n"
      "v_permlane16_swap_b32 v148, v129\n"
      "v_mfma_f32_16x16x32_bf16 v[96:99], a[64:67], v[122:125], v[96:99]\n"
      "v_add_f32 v148, v148, v129\n"
      "v_mov_b32 v129, v148\n"
      "v_mfma_f32_16x16x32_bf16 v[96:99], a[68:71], v[168:171], v[96:99]\n"
      "v_fmac_f32 v101, v157, v157\n"
      "v_permlane32_swap_b32 v148, v129\n"
      "v_mfma_f32_16x16x32_bf16 v[202:205], a[72:75], v[114:117], 0\n"
      "v_add_f32 v148, v148, v129\n"
      "v_rsq_f32 v148, v148\n"
      "v_mfma_f32_16x16x32_bf16 v[202:205], a[76:79], v[140:143], v[202:205]\n"
      "v_mov_b32 v129, v148\n"
      "v_fmac_f32 v101, v158, v158\n"
      "v_mfma_f32_16x16x32_bf16 v[202:205], a[80:83], v[186:189], v[202:205]\n"
      "v_permlane16_swap_b32 v148, v129\n"
      "v_mfma_f32_16x16x32_bf16 v[202:205], a[84:87], v[182:185], v[202:205]\n"
      "v_mul_f32 v148, s50, v148\n"
      "v_mul_f32 v206, v129, v206\n"
      "v_mfma_f32_16x16x32_bf16 v[202:205], a[88:91], v[122:125], v[202:205]\n"
      "v_mul_f32 v207, v129, v207\n"
      "v_mul_f32 v152, v148, v152\n"
      "v_mfma_f32_16x16x32_bf16 v[202:205], a[92:95], v[168:171], v[202:205]\n"
      "v_mul_f32 v153, v148, v153\n"
      "v_cvt_pk_bf16_f32 v206, v206, v207\n"
      "v_mfma_f32_16x16x32_bf16 v[178:181], a[96:99], v[114:117], 0\n"
      "v_mul_f32 v208, v129, v208\n"
      "v_mul_f32 v209, v129, v209\n"
      "v_mfma_f32_16x16x32_bf16 v[178:181], a[100:103], v[140:143], v[178:181]\n"
      "v_mul_f32 v172, v129, v172\n"
      "v_mul_f32 v173, v129, v173\n"
      "v_mfma_f32_16x16x32_bf16 v[178:181], a[104:107], v[186:189], v[178:181]\n"
      "v_cvt_pk_bf16_f32 v152, v152, v153\n"
      "v_mul_f32 v154, v148, v154\n"
      "v_mfma_f32_16x16x32_bf16 v[178:181], a[108:111], v[182:185], v[178:181]\n"
      "v_mul_f32 v155, v148, v155\n"
      "v_mul_f32 v130, v148, v130\n"
      "v_mfma_f32_16x16x32_bf16 v[178:181], a[112:115], v[122:125], v[178:181]\n"
      "v_mul_f32_e64 v131, v148, v131\n"
      "v_cvt_pk_bf16_f32 v207, v208, v209\n"
      "v_mfma_f32_16x16x32_bf16 v[178:181], a[116:119], v[168:171], v[178:181]\n"
      "v_mul_f32 v110, v129, v110\n"
      "v_mul_f32 v111, v129, v111\n"
      "v_mfma_f32_16x16x32_bf16 v[218:221], a[120:123], v[114:117], 0\n"
      "v_mul_f32 v112, v129, v112\n"
      "v_mul_f32 v113, v129, v113\n"
      "v_mfma_f32_16x16x32_bf16 v[218:221], a[124:127], v[140:143], v[218:221]\n"
      "v_cvt_pk_bf16_f32 v172, v172, v173\n"
      "v_mul_f32 v174, v129, v174\n"
      "v_mfma_f32_16x16x32_bf16 v[218:221], a[128:131], v[186:189], v[218:221]\n"
      "v_mul_f32 v175, v129, v175\n"
      "v_cvt_pk_bf16_f32 v153, v154, v155\n"
      "v_mfma_f32_16x16x32_bf16 v[218:221], a[132:135], v[182:185], v[218:221]\n"
      "ds_read_b128 v[126:129], v7 offset:18432\n"
      "v_mul_f32_e64 v164, v148, v164\n"
      "v_mfma_f32_16x16x32_bf16 v[218:221], a[136:139], v[122:125], v[218:221]\n"
      "v_mul_f32 v165, v148, v165\n"
      "v_mul_f32 v166, v148, v166\n"
      "v_mfma_f32_16x16x32_bf16 v[218:221], a[140:143], v[168:171], v[218:221]\n"
      "ds_read_b128 v[102:105], v7 offset:22528\n"
      "v_mul_f32_e64 v167, v148, v167\n"
      "v_mfma_f32_16x16x32_bf16 v[190:193], v[114:117], a[144:147], 0\n"
      "ds_read_b128 v[118:121], v7 offset:26624\n"
      "v_cvt_pk_bf16_f32 v130, v130, v131\n"
      "v_mfma_f32_16x16x32_bf16 v[190:193], v[140:143], a[148:151], v[190:193]\n"
      "v_mul_f32 v132, v148, v132\n"
      "v_mul_f32 v133, v148, v133\n"
      "v_mfma_f32_16x16x32_bf16 v[190:193], v[186:189], a[152:155], v[190:193]\n"
      "ds_read_b128 v[144:147], v7 offset:30720\n"
      "v_cvt_pk_bf16_f32 v208, v110, v111\n"
      "v_mfma_f32_16x16x32_bf16 v[190:193], v[182:185], a[156:159], v[190:193]\n"
      "ds_read_b128 v[148:151], v7 offset:34816\n"
      "v_cvt_pk_bf16_f32 v209, v112, v113\n"
      "v_mfma_f32_16x16x32_bf16 v[190:193], v[122:125], a[160:163], v[190:193]\n"
      "v_cvt_pk_bf16_f32 v173, v174, v175\n"
      "v_cvt_pk_bf16_f32 v154, v164, v165\n"
      "v_mfma_f32_16x16x32_bf16 v[190:193], v[168:171], a[164:167], v[190:193]\n"
      "ds_read_b128 v[110:113], v0 offset:4096\n"
      "v_cvt_pk_bf16_f32 v155, v166, v167\n"
      "v_mfma_f32_16x16x32_bf16 v[164:167], v[114:117], a[168:171], 0\n"
      "ds_read_b128 v[174:177], v1 offset:4096\n"
      "v_cvt_pk_bf16_f32 v131, v132, v133\n"
      "v_mfma_f32_16x16x32_bf16 v[164:167], v[140:143], a[172:175], v[164:167]\n"
      "s_waitcnt lgkmcnt(4)\n"
      "v_fma_f32 v100, v202, v202, s51\n"
      "v_fmac_f32 v100, v203, v203\n"
      "v_mfma_f32_16x16x32_bf16 v[164:167], v[186:189], a[176:179], v[164:167]\n"
      "buffer_store_dwordx4 v[126:129], v20, s[20:23], s46 offen sc1\n"
      "v_mfma_f32_16x16x32_bf16 v[164:167], v[182:185], a[180:183], v[164:167]\n"
      "ds_read_b128 v[132:135], v0 offset:10240\n"
      "v_fmac_f32 v100, v204, v204\n"
      "v_mfma_f32_16x16x32_bf16 v[164:167], v[122:125], a[184:187], v[164:167]\n"
      "ds_read_b128 v[126:129], v1 offset:10240\n"
      "v_fmac_f32 v101, v159, v159\n"
      "v_mfma_f32_16x16x32_bf16 v[164:167], v[168:171], a[188:191], v[164:167]\n"
      "ds_read_b128 v[160:163], v0 offset:16384\n"
      "v_fmac_f32 v100, v205, v205\n"
      "v_mfma_f32_16x16x32_bf16 v[106:109], v[114:117], a[192:195], 0\n"
      "s_waitcnt lgkmcnt(3)\n"
      "v_fmac_f32 v101, v210, v210\n"
      "v_fmac_f32 v100, v178, v178\n"
      "v_mfma_f32_16x16x32_bf16 v[106:109], v[140:143], a[196:199], v[106:109]\n"
      "ds_read_b128 v[140:143], v1 offset:16384\n"
      "v_fmac_f32 v101, v211, v211\n"
      "v_mfma_f32_16x16x32_bf16 v[106:109], v[186:189], a[200:203], v[106:109]\n"
      "v_fmac_f32 v100, v179, v179\n"
      "v_fmac_f32 v101, v212, v212\n"
      "v_mfma_f32_16x16x32_bf16 v[106:109], v[182:185], a[204:207], v[106:109]\n"
      "s_waitcnt lgkmcnt(3)\n"
      "v_fmac_f32 v100, v180, v180\n"
      "v_fmac_f32 v101, v213, v213\n"
      "v_mfma_f32_16x16x32_bf16 v[106:109], v[122:125], a[208:211], v[106:109]\n"
      "buffer_store_dwordx4 v[102:105], v21, s[20:23], s46 offen sc1\n"
      "v_mfma_f32_16x16x32_bf16 v[106:109], v[168:171], a[212:215], v[106:109]\n"
      "s_waitcnt lgkmcnt(1)\n"
      "v_fmac_f32 v100, v181, v181\n"
      "v_fmac_f32_e64 v101, v96, v96\n"
      "v_mfma_f32_16x16x32_bf16 v[168:171], a[0:3], v[110:113], 0\n"
      "s_waitcnt lgkmcnt(0)\n"
      "v_fmac_f32 v100, v218, v218\n"
      "v_fmac_f32 v101, v97, v97\n"
      "v_mfma_f32_16x16x32_bf16 v[168:171], a[4:7], v[174:177], v[168:171]\n"
      "v_fmac_f32 v100, v219, v219\n"
      "v_fmac_f32 v101, v98, v98\n"
      "v_mfma_f32_16x16x32_bf16 v[168:171], a[8:11], v[132:135], v[168:171]\n"
      "v_fmac_f32 v100, v220, v220\n"
      "v_fmac_f32 v101, v99, v99\n"
      "v_mfma_f32_16x16x32_bf16 v[168:171], a[12:15], v[126:129], v[168:171]\n"
      "v_fmac_f32 v100, v221, v221\n"
      "v_cvt_pk_bf16_f32 v198, v198, v199\n"
      "v_mfma_f32_16x16x32_bf16 v[168:171], a[16:19], v[160:163], v[168:171]\n"
      "v_permlane16_swap_b32 v101, v100\n"
      "v_mfma_f32_16x16x32_bf16 v[168:171], a[20:23], v[140:143], v[168:171]\n"
      "v_add_f32 v101, v101, v100\n"
      "v_mov_b32 v100, v101\n"
      "v_mfma_f32_16x16x32_bf16 v[182:185], a[24:27], v[110:113], 0\n"
      "buffer_store_dwordx4 v[118:121], v22, s[20:23], s46 offen sc1\n"
      "v_mfma_f32_16x16x32_bf16 v[182:185], a[28:31], v[174:177], v[182:185]\n"
      "v_permlane32_swap_b32 v101, v100\n"
      "v_mfma_f32_16x16x32_bf16 v[182:185], a[32:35], v[132:135], v[182:185]\n"
      "v_add_f32 v101, v101, v100\n"
      "v_rsq_f32 v101, v101\n"
      "v_mfma_f32_16x16x32_bf16 v[182:185], a[36:39], v[126:129], v[182:185]\n"
      "v_mov_b32_e64 v100, v101\n"
      "v_fma_f32 v76, v168, v168, s51\n"
      "v_mfma_f32_16x16x32_bf16 v[182:185], a[40:43], v[160:163], v[182:185]\n"
      "v_permlane16_swap_b32 v101, v100\n"
      "v_mfma_f32_16x16x32_bf16 v[182:185], a[44:47], v[140:143], v[182:185]\n"
      "v_mul_f32 v101, s50, v101\n"
      "v_mul_f32 v202, v100, v202\n"
      "v_mfma_f32_16x16x32_bf16 v[122:125], a[48:51], v[110:113], 0\n"
      "v_mul_f32 v203, v100, v203\n"
      "v_mul_f32 v156, v101, v156\n"
      "v_mfma_f32_16x16x32_bf16 v[122:125], a[52:55], v[174:177], v[122:125]\n"
      "v_mul_f32 v157, v101, v157\n"
      "v_cvt_pk_bf16_f32 v202, v202, v203\n"
      "v_mfma_f32_16x16x32_bf16 v[122:125], a[56:59], v[132:135], v[122:125]\n"
      "buffer_store_dwordx4 v[144:147], v23, s[20:23], s46 offen sc1\n"
      "v_mfma_f32_16x16x32_bf16 v[122:125], a[60:63], v[126:129], v[122:125]\n"
      "v_mul_f32 v204, v100, v204\n"
      "v_mul_f32 v205, v100, v205\n"
      "v_mfma_f32_16x16x32_bf16 v[122:125], a[64:67], v[160:163], v[122:125]\n"
      "v_mul_f32 v218, v100, v218\n"
      "v_mul_f32 v219, v100, v219\n"
      "v_mfma_f32_16x16x32_bf16 v[122:125], a[68:71], v[140:143], v[122:125]\n"
      "v_cvt_pk_bf16_f32 v156, v156, v157\n"
      "v_mul_f32 v158, v101, v158\n"
      "v_mfma_f32_16x16x32_bf16 v[118:121], a[72:75], v[110:113], 0\n"
      "v_mul_f32 v159, v101, v159\n"
      "v_mul_f32 v96, v101, v96\n"
      "v_mfma_f32_16x16x32_bf16 v[118:121], a[76:79], v[174:177], v[118:121]\n"
      "ds_read_b128 v[144:147], v9 offset:5632\n"
      "v_mul_f32 v97, v101, v97\n"
      "v_mfma_f32_16x16x32_bf16 v[118:121], a[80:83], v[132:135], v[118:121]\n"
      "v_cvt_pk_bf16_f32 v203, v204, v205\n"
      "v_mul_f32 v178, v100, v178\n"
      "v_mfma_f32_16x16x32_bf16 v[118:121], a[84:87], v[126:129], v[118:121]\n"
      "v_mul_f32 v179, v100, v179\n"
      "v_mul_f32_e64 v180, v100, v180\n"
      "v_mfma_f32_16x16x32_bf16 v[118:121], a[88:91], v[160:163], v[118:121]\n"
      "buffer_store_dwordx4 v[148:151], v25, s[20:23], s46 offen sc1\n"
      "v_mfma_f32_16x16x32_bf16 v[118:121], a[92:95], v[140:143], v[118:121]\n"
      "v_mul_f32_e64 v181, v100, v181\n"
      "v_cvt_pk_bf16_f32 v218, v218, v219\n"
      "v_mfma_f32_16x16x32_bf16 v[148:151], a[96:99], v[110:113], 0\n"
      "v_mul_f32 v220, v100, v220\n"
      "v_mul_f32 v221, v100, v221\n"
      "v_mfma_f32_16x16x32_bf16 v[148:151], a[100:103], v[174:177], v[148:151]\n"
      "v_cvt_pk_bf16_f32 v157, v158, v159\n"
      "v_mul_f32 v210, v101, v210\n"
      "v_mfma_f32_16x16x32_bf16 v[148:151], a[104:107], v[132:135], v[148:151]\n"
      "v_mul_f32 v211, v101, v211\n"
      "v_mul_f32 v212, v101, v212\n"
      "v_mfma_f32_16x16x32_bf16 v[148:151], a[108:111], v[126:129], v[148:151]\n"
      "v_mul_f32 v213, v101, v213\n"
      "v_cvt_pk_bf16_f32 v96, v96, v97\n"
      "v_mfma_f32_16x16x32_bf16 v[148:151], a[112:115], v[160:163], v[148:151]\n"
      "v_mul_f32 v98, v101, v98\n"
      "v_mul_f32 v99, v101, v99\n"
      "v_mfma_f32_16x16x32_bf16 v[148:151], a[116:119], v[140:143], v[148:151]\n"
      "v_cvt_pk_bf16_f32 v204, v178, v179\n"
      "v_cvt_pk_bf16_f32 v205, v180, v181\n"
      "v_mfma_f32_16x16x32_bf16 v[178:181], a[120:123], v[110:113], 0\n"
      "v_cvt_pk_bf16_f32 v219, v220, v221\n"
      "v_cvt_pk_bf16_f32 v158, v210, v211\n"
      "v_mfma_f32_16x16x32_bf16 v[178:181], a[124:127], v[174:177], v[178:181]\n"
      "v_cvt_pk_bf16_f32 v159, v212, v213\n"
      "v_cvt_pk_bf16_f32 v97, v98, v99\n"
      "v_mfma_f32_16x16x32_bf16 v[178:181], a[128:131], v[132:135], v[178:181]\n"
      "v_fma_f32 v77, v118, v118, s51\n"
      "v_fmac_f32 v76, v169, v169\n"
      "v_mfma_f32_16x16x32_bf16 v[178:181], a[132:135], v[126:129], v[178:181]\n"
      "v_fmac_f32 v77, v119, v119\n"
      "v_fmac_f32 v76, v170, v170\n"
      "v_mfma_f32_16x16x32_bf16 v[178:181], a[136:139], v[160:163], v[178:181]\n"
      "v_fmac_f32 v77, v120, v120\n"
      "v_fmac_f32 v76, v171, v171\n"
      "v_mfma_f32_16x16x32_bf16 v[178:181], a[140:143], v[140:143], v[178:181]\n"
      "v_fmac_f32 v77, v121, v121\n"
      "v_fmac_f32 v76, v182, v182\n"
      "v_mfma_f32_16x16x32_bf16 v[186:189], v[110:113], a[144:147], 0\n"
      "v_fmac_f32 v77, v148, v148\n"
      "v_fmac_f32 v76, v183, v183\n"
      "v_mfma_f32_16x16x32_bf16 v[186:189], v[174:177], a[148:151], v[186:189]\n"
      "v_fmac_f32 v77, v149, v149\n"
      "v_fmac_f32 v76, v184, v184\n"
      "v_mfma_f32_16x16x32_bf16 v[186:189], v[132:135], a[152:155], v[186:189]\n"
      "v_fmac_f32 v77, v150, v150\n"
      "v_fmac_f32 v76, v185, v185\n"
      "v_mfma_f32_16x16x32_bf16 v[186:189], v[126:129], a[156:159], v[186:189]\n"
      "v_fmac_f32 v77, v151, v151\n"
      "v_fmac_f32 v76, v122, v122\n"
      "v_mfma_f32_16x16x32_bf16 v[186:189], v[160:163], a[160:163], v[186:189]\n"
      "v_fmac_f32 v77, v178, v178\n"
      "v_fmac_f32_e64 v76, v123, v123\n"
      "v_mfma_f32_16x16x32_bf16 v[186:189], v[140:143], a[164:167], v[186:189]\n"
      "ds_read_b128 v[114:117], v2 offset:18432\n"
      "v_fmac_f32 v77, v179, v179\n"
      "v_mfma_f32_16x16x32_bf16 v[102:105], v[110:113], a[168:171], 0\n"
      "ds_read_b128 v[98:101], v2 offset:21504\n"
      "v_fmac_f32 v76, v124, v124\n"
      "v_mfma_f32_16x16x32_bf16 v[102:105], v[174:177], a[172:175], v[102:105]\n"
      "v_fmac_f32 v77, v180, v180\n"
      "v_fmac_f32 v76, v125, v125\n"
      "v_mfma_f32_16x16x32_bf16 v[102:105], v[132:135], a[176:179], v[102:105]\n"
      "v_fmac_f32_e64 v77, v181, v181\n"
      "v_cvt_pk_bf16_f32 v199, v200, v201\n"
      "v_mfma_f32_16x16x32_bf16 v[102:105], v[126:129], a[180:183], v[102:105]\n"
      "v_permlane16_swap_b32 v76, v77\n"
      "v_mfma_f32_16x16x32_bf16 v[102:105], v[160:163], a[184:187], v[102:105]\n"
      "v_add_f32 v76, v76, v77\n"
      "v_mov_b32 v77, v76\n"
      "v_mfma_f32_16x16x32_bf16 v[102:105], v[140:143], a[188:191], v[102:105]\n"
      "s_waitcnt lgkmcnt(1)\n"
      "v_permlane32_swap_b32 v76, v77\n"
      "v_mfma_f32_16x16x32_bf16 v[210:213], v[110:113], a[192:195], 0\n"
      "s_waitcnt lgkmcnt(0)\n"
      "v_add_f32 v76, v76, v77\n"
      "v_rsq_f32 v76, v76\n"
      "v_mfma_f32_16x16x32_bf16 v[210:213], v[174:177], a[196:199], v[210:213]\n"
      "ds_read_b128 v[174:177], v2 offset:24576\n"
      "v_mov_b32_e64 v77, v76\n"
      "v_mfma_f32_16x16x32_bf16 v[210:213], v[132:135], a[200:203], v[210:213]\n"
      "ds_read_b128 v[132:135], v2 offset:27648\n"
      "v_permlane16_swap_b32_e64 v76, v77\n"
      "v_mfma_f32_16x16x32_bf16 v[210:213], v[126:129], a[204:207], v[210:213]\n"
      "ds_read_b128 v[110:113], v2 offset:30720\n"
      "v_mul_f32 v118, v77, v118\n"
      "v_mfma_f32_16x16x32_bf16 v[210:213], v[160:163], a[208:211], v[210:213]\n"
      "ds_read_b128 v[126:129], v2 offset:33792\n"
      "v_mul_f32 v119, v77, v119\n"
      "v_mfma_f32_16x16x32_bf16 v[210:213], v[140:143], a[212:215], v[210:213]\n"
      "v_cvt_pk_bf16_f32 v118, v118, v119\n"
      "v_mul_f32 v120, v77, v120\n"
      "v_mfma_f32_16x16x32_bf16 v[140:143], a[216:219], v[114:117], 0\n"
      "v_mul_f32 v121, v77, v121\n"
      "v_cvt_pk_bf16_f32 v119, v120, v121\n"
      "v_mfma_f32_16x16x32_bf16 v[140:143], a[220:223], v[98:101], v[140:143]\n"
      "s_waitcnt lgkmcnt(3)\n"
      "v_mul_f32 v148, v77, v148\n"
      "v_mul_f32_e64 v149, v77, v149\n"
      "v_mfma_f32_16x16x32_bf16 v[140:143], a[224:227], v[174:177], v[140:143]\n"
      "s_waitcnt lgkmcnt(2)\n"
      "v_mul_f32 v150, v77, v150\n"
      "v_mul_f32_e64 v151, v77, v151\n"
      "v_cvt_pk_bf16_f32 v120, v148, v149\n"
      "v_mfma_f32_16x16x32_bf16 v[220:223], a[240:243], v[114:117], 0\n"
      "s_waitcnt lgkmcnt(1)\n"
      "v_cvt_pk_bf16_f32 v121, v150, v151\n"
      "v_mul_f32 v178, v77, v178\n"
      "v_mfma_f32_16x16x32_bf16 v[220:223], a[244:247], v[98:101], v[220:223]\n"
      "s_waitcnt lgkmcnt(0)\n"
      "v_mul_f32 v179, v77, v179\n"
      "v_cvt_pk_bf16_f32 v178, v178, v179\n"
      "v_mfma_f32_16x16x32_bf16 v[220:223], a[248:251], v[174:177], v[220:223]\n"
      "v_mul_f32 v180, v77, v180\n"
      "v_mul_f32 v181, v77, v181\n"
      "v_mfma_f32_16x16x32_bf16 v[160:163], v[232:235], v[114:117], 0\n"
      "v_cvt_pk_bf16_f32 v179, v180, v181\n"
      "v_mul_f32 v76, s50, v76\n"
      "v_mfma_f32_16x16x32_bf16 v[160:163], v[236:239], v[98:101], v[160:163]\n"
      "v_mul_f32 v168, v76, v168\n"
      "v_mul_f32_e64 v169, v76, v169\n"
      "v_mfma_f32_16x16x32_bf16 v[160:163], v[240:243], v[174:177], v[160:163]\n"
      "ds_read_b128 v[148:151], v10 offset:32\n"
      "v_cvt_pk_bf16_f32 v168, v168, v169\n"
      "v_mfma_f32_16x16x32_bf16 v[140:143], a[228:231], v[132:135], v[140:143]\n"
      "v_mul_f32 v170, v76, v170\n"
      "v_mul_f32 v171, v76, v171\n"
      "v_mfma_f32_16x16x32_bf16 v[140:143], a[232:235], v[110:113], v[140:143]\n"
      "v_mul_f32 v122, v76, v122\n"
      "v_mul_f32 v123, v76, v123\n"
      "v_mfma_f32_16x16x32_bf16 v[140:143], a[236:239], v[126:129], v[140:143]\n"
      "v_cvt_pk_bf16_f32 v169, v170, v171\n"
      "v_mul_f32_e64 v182, v76, v182\n"
      "v_mfma_f32_16x16x32_bf16 v[220:223], a[252:255], v[132:135], v[220:223]\n"
      "ds_read_b64 v[180:181], v10 offset:24\n"
      "v_mul_f32 v183, v76, v183\n"
      "v_mfma_f32_16x16x32_bf16 v[220:223], v[224:227], v[110:113], v[220:223]\n"
      "ds_read_b128 v[114:117], v2 offset:18688\n"
      "v_mul_f32 v184, v76, v184\n"
      "v_mfma_f32_16x16x32_bf16 v[220:223], v[228:231], v[126:129], v[220:223]\n"
      "s_waitcnt lgkmcnt(2)\n"
      "v_mul_f32 v185, v76, v185\n"
      "v_cvt_pk_bf16_f32 v122, v122, v123\n"
      "v_mfma_f32_16x16x32_bf16 v[160:163], v[244:247], v[132:135], v[160:163]\n"
      "ds_read_b128 v[132:135], v2 offset:21760\n"
      "v_mul_f32_e64 v124, v76, v124\n"
      "v_mfma_f32_16x16x32_bf16 v[160:163], v[248:251], v[110:113], v[160:163]\n"
      "ds_read_b128 v[98:101], v2 offset:24832\n"
      "v_mul_f32 v125, v76, v125\n"
      "v_mfma_f32_16x16x32_bf16 v[160:163], v[252:255], v[126:129], v[160:163]\n"
      "v_cvt_pk_bf16_f32 v170, v182, v183\n"
      "v_cvt_pk_bf16_f32 v171, v184, v185\n"
      "v_mfma_f32_16x16x32_bf16 v[174:177], v[206:209], v[152:155], v[40:43]\n"
      "s_waitcnt lgkmcnt(3)\n"
      "v_cvt_pk_bf16_f32 v123, v124, v125\n"
      "v_cvt_pk_bf16_f32 v200, v190, v191\n"
      "v_mfma_f32_16x16x32_bf16 v[110:113], v[202:205], v[152:155], v[44:47]\n"
      "s_waitcnt lgkmcnt(2)\n"
      "v_cvt_pk_bf16_f32 v201, v192, v193\n"
      "v_cvt_pk_bf16_f32 v76, v186, v187\n"
      "v_mfma_f32_16x16x32_bf16 v[124:127], v[118:121], v[152:155], v[48:51]\n"
      "v_cvt_pk_bf16_f32 v77, v188, v189\n"
      "v_cvt_pk_bf16_f32 v214, v214, v215\n"
      "v_mfma_f32_16x16x32_bf16 v[190:193], v[148:151], v[152:155], v[24:27]\n"
      "s_waitcnt lgkmcnt(1)\n"
      "v_cvt_pk_bf16_f32 v215, v216, v217\n"
      "v_cvt_pk_bf16_f32 v216, v164, v165\n"
      "v_mfma_f32_16x16x16_bf16 v[174:177], v[172:173], v[130:131], v[174:177]\n"
      "v_cvt_pk_bf16_f32 v217, v166, v167\n"
      "v_cvt_pk_bf16_f32 v80, v102, v103\n"
      "v_cvt_pk_bf16_f32 v81, v104, v105\n"
      "v_mfma_f32_16x16x16_bf16 v[110:113], v[218:219], v[130:131], v[110:113]\n"
      "v_cvt_pk_bf16_f32 v194, v194, v195\n"
      "v_cvt_pk_bf16_f32 v195, v196, v197\n"
      "v_cvt_pk_bf16_f32 v196, v106, v107\n"
      "v_mfma_f32_16x16x16_bf16 v[124:127], v[178:179], v[130:131], v[124:127]\n"
      "v_exp_f32 v174, v174\n"
      "v_exp_f32 v175, v175\n"
      "v_mfma_f32_16x16x16_bf16 v[190:193], v[180:181], v[130:131], v[190:193]\n"
      "v_exp_f32 v176, v176\n"
      "v_exp_f32 v177, v177\n"
      "v_mfma_f32_16x16x32_bf16 v[186:189], a[216:219], v[114:117], 0\n"
      "s_waitcnt lgkmcnt(0)\n"
      "v_exp_f32 v110, v110\n"
      "v_mfma_f32_16x16x32_bf16 v[186:189], a[220:223], v[132:135], v[186:189]\n"
      "v_exp_f32 v111, v111\n"
      "v_mfma_f32_16x16x32_bf16 v[186:189], a[224:227], v[98:101], v[186:189]\n"
      "v_exp_f32 v112, v112\n"
      "v_mfma_f32_16x16x32_bf16 v[182:185], a[240:243], v[114:117], 0\n"
      "v_exp_f32_e64 v113, v113\n"
      "v_mfma_f32_16x16x32_bf16 v[182:185], a[244:247], v[132:135], v[182:185]\n"
      "ds_read_b128 v[152:155], v2 offset:27904\n"
      "v_cvt_pk_bf16_f32 v174, v174, v175\n"
      "v_mfma_f32_16x16x32_bf16 v[182:185], a[248:251], v[98:101], v[182:185]\n"
      "ds_read_b128 v[128:131], v2 offset:30976\n"
      "v_cvt_pk_bf16_f32 v175, v176, v177\n"
      "v_mfma_f32_16x16x32_bf16 v[104:107], v[232:235], v[114:117], 0\n"
      "ds_read_b128 v[114:117], v2 offset:34048\n"
      "v_cvt_pk_bf16_f32 v176, v110, v111\n"
      "v_mfma_f32_16x16x32_bf16 v[104:107], v[236:239], v[132:135], v[104:107]\n"
      "v_cvt_pk_bf16_f32 v177, v112, v113\n"
      "v_exp_f32_e64 v124, v124\n"
      "v_mfma_f32_16x16x32_bf16 v[104:107], v[240:243], v[98:101], v[104:107]\n"
      "v_exp_f32_e64 v125, v125\n"
      "v_mfma_f32_16x16x32_bf16 v[132:135], v[206:209], v[156:159], v[52:55]\n"
      "v_exp_f32_e64 v126, v126\n"
      "v_mfma_f32_16x16x32_bf16 v[100:103], v[202:205], v[156:159], v[56:59]\n"
      "s_waitcnt lgkmcnt(2)\n"
      "v_exp_f32_e64 v127, v127\n"
      "v_mfma_f32_16x16x32_bf16 v[164:167], v[118:121], v[156:159], v[60:63]\n"
      "s_waitcnt lgkmcnt(1)\n"
      "v_exp_f32 v190, v190\n"
      "v_mfma_f32_16x16x32_bf16 v[110:113], v[148:151], v[156:159], v[26:29]\n"
      "s_waitcnt lgkmcnt(0)\n"
      "v_cvt_pk_bf16_f32 v88, v124, v125\n"
      "v_cvt_pk_bf16_f32 v89, v126, v127\n"
      "v_mfma_f32_16x16x16_bf16 v[132:135], v[172:173], v[96:97], v[132:135]\n"
      "v_cvt_pk_bf16_f32 v90, v190, 0\n"
      "v_cvt_pk_bf16_f32 v197, v108, v109\n"
      "v_cvt_pk_bf16_f32 v84, v210, v211\n"
      "v_mfma_f32_16x16x16_bf16 v[100:103], v[218:219], v[96:97], v[100:103]\n"
      "v_cvt_pk_bf16_f32 v85, v212, v213\n"
      "v_mul_f32 v158, v140, v140\n"
      "v_add_f32 v157, v140, v141\n"
      "v_mfma_f32_16x16x16_bf16 v[164:167], v[178:179], v[96:97], v[164:167]\n"
      "v_exp_f32 v132, v132\n"
      "v_exp_f32 v133, v133\n"
      "v_mfma_f32_16x16x16_bf16 v[110:113], v[180:181], v[96:97], v[110:113]\n"
      "v_exp_f32 v134, v134\n"
      "v_exp_f32 v135, v135\n"
      "v_mfma_f32_16x16x32_bf16 v[186:189], a[228:231], v[152:155], v[186:189]\n"
      "ds_read_u16 v78, v11\n"
      "v_exp_f32 v100, v100\n"
      "v_mfma_f32_16x16x32_bf16 v[186:189], a[232:235], v[128:131], v[186:189]\n"
      "ds_read_u16 v82, v11 offset:32\n"
      "v_exp_f32 v101, v101\n"
      "v_mfma_f32_16x16x32_bf16 v[186:189], a[236:239], v[114:117], v[186:189]\n"
      "v_exp_f32 v102, v102\n"
      "v_mfma_f32_16x16x32_bf16 v[182:185], a[252:255], v[152:155], v[182:185]\n"
      "ds_read_u16 v86, v11 offset:64\n"
      "v_exp_f32 v103, v103\n"
      "v_mfma_f32_16x16x32_bf16 v[182:185], v[224:227], v[128:131], v[182:185]\n"
      "v_cvt_pk_bf16_f32 v132, v132, v133\n"
      "v_cvt_pk_bf16_f32 v133, v134, v135\n"
      "v_mfma_f32_16x16x32_bf16 v[182:185], v[228:231], v[114:117], v[182:185]\n"
      "v_cvt_pk_bf16_f32 v134, v100, v101\n"
      "v_cvt_pk_bf16_f32 v135, v102, v103\n"
      "v_mfma_f32_16x16x32_bf16 v[104:107], v[244:247], v[152:155], v[104:107]\n"
      "s_waitcnt lgkmcnt(2)\n"
      "v_exp_f32 v164, v164\n"
      "v_mfma_f32_16x16x32_bf16 v[104:107], v[248:251], v[128:131], v[104:107]\n"
      "s_waitcnt lgkmcnt(1)\n"
      "v_exp_f32 v165, v165\n"
      "v_mfma_f32_16x16x32_bf16 v[104:107], v[252:255], v[114:117], v[104:107]\n"
      "v_exp_f32_e64 v166, v166\n"
      "v_mfma_f32_16x16x32_bf16 v[100:103], v[32:35], v[174:177], 0\n"
      "s_waitcnt lgkmcnt(0)\n"
      "v_exp_f32 v167, v167\n"
      "v_mfma_f32_16x16x32_bf16 v[100:103], v[32:35], v[88:91], v[100:103]\n"
      "v_exp_f32_e64 v110, v110\n"
      "v_mfma_f32_16x16x32_bf16 v[96:99], v[198:201], v[174:177], 0\n"
      "v_cvt_pk_bf16_f32 v92, v164, v165\n"
      "v_cvt_pk_bf16_f32 v93, v166, v167\n"
      "v_mfma_f32_16x16x32_bf16 v[96:99], v[76:79], v[88:91], v[96:99]\n"
      "ds_read_b128 v[152:155], v2 offset:18944\n"
      "v_cvt_pk_bf16_f32 v94, v110, 0\n"
      "v_mfma_f32_16x16x32_bf16 v[124:127], v[214:217], v[174:177], 0\n"
      "ds_read_b128 v[164:167], v2 offset:22016\n"
      "v_mul_f32 v102, v186, v186\n"
      "v_mfma_f32_16x16x32_bf16 v[124:127], v[80:83], v[88:91], v[124:127]\n"
      "ds_read_b128 v[210:213], v2 offset:25088\n"
      "v_fmac_f32 v158, v141, v141\n"
      "v_mfma_f32_16x16x32_bf16 v[108:111], v[194:197], v[174:177], 0\n"
      "v_add_f32 v101, v186, v187\n"
      "v_fmac_f32 v102, v187, v187\n"
      "v_mfma_f32_16x16x32_bf16 v[108:111], v[84:87], v[88:91], v[108:111]\n"
      "v_add_f32 v157, v157, v142\n"
      "v_fmac_f32 v158, v142, v142\n"
      "v_add_f32 v101, v101, v188\n"
      "v_fmac_f32 v102, v188, v188\n"
      "v_add_f32 v157, v157, v143\n"
      "v_fmac_f32 v158, v143, v143\n"
      "v_add_f32 v101, v101, v189\n"
      "v_fmac_f32 v102, v189, v189\n"
      "v_add_f32 v157, v157, v220\n"
      "v_fmac_f32 v158, v220, v220\n"
      "v_add_f32 v101, v101, v182\n"
      "v_fmac_f32 v102, v182, v182\n"
      "v_add_f32 v157, v157, v221\n"
      "v_fmac_f32 v158, v221, v221\n"
      "v_add_f32 v101, v101, v183\n"
      "v_fmac_f32 v102, v183, v183\n"
      "v_add_f32 v157, v157, v222\n"
      "v_fmac_f32 v158, v222, v222\n"
      "v_add_f32 v101, v101, v184\n"
      "v_fmac_f32 v102, v184, v184\n"
      "v_add_f32 v157, v157, v223\n"
      "v_fmac_f32 v158, v223, v223\n"
      "v_add_f32 v101, v101, v185\n"
      "v_fmac_f32 v102, v185, v185\n"
      "v_add_f32 v157, v157, v160\n"
      "v_fmac_f32 v158, v160, v160\n"
      "v_add_f32 v101, v101, v104\n"
      "v_fmac_f32 v102, v104, v104\n"
      "v_add_f32 v157, v157, v161\n"
      "v_fmac_f32 v158, v161, v161\n"
      "v_add_f32 v101, v101, v105\n"
      "v_fmac_f32 v102, v105, v105\n"
      "v_add_f32 v157, v157, v162\n"
      "v_fmac_f32 v158, v162, v162\n"
      "v_add_f32 v101, v101, v106\n"
      "v_fmac_f32 v102, v106, v106\n"
      "v_add_f32 v157, v157, v163\n"
      "v_fmac_f32 v158, v163, v163\n"
      "v_add_f32 v101, v101, v107\n"
      "v_fmac_f32 v102, v107, v107\n"
      "v_rcp_f32 v100, v100\n"
      "v_permlane16_swap_b32 v157, v158\n"
      "v_permlane16_swap_b32 v101, v102\n"
      "v_mul_f32 v96, v100, v96\n"
      "v_mul_f32 v97, v100, v97\n"
      "v_cvt_pk_bf16_f32 v96, v96, v97\n"
      "v_mul_f32 v98, v100, v98\n"
      "v_mul_f32 v99, v100, v99\n"
      "v_add_f32 v157, v157, v158\n"
      "v_mfma_f32_16x16x32_bf16 v[112:115], v[206:209], v[168:171], v[64:67]\n"
      "s_waitcnt lgkmcnt(0)\n"
      "v_add_f32 v101, v101, v102\n"
      "v_cvt_pk_bf16_f32 v97, v98, v99\n"
      "v_mul_f32 v124, v100, v124\n"
      "v_mul_f32 v125, v100, v125\n"
      "v_mul_f32 v126, v100, v126\n"
      "v_mul_f32 v127, v100, v127\n"
      "v_cvt_pk_bf16_f32 v98, v124, v125\n"
      "v_cvt_pk_bf16_f32 v99, v126, v127\n"
      "v_mfma_f32_16x16x32_bf16 v[190:193], v[202:205], v[168:171], v[68:71]\n"
      "ds_write_b128 v3, v[96:99]\n"
      "v_mfma_f32_16x16x32_bf16 v[174:177], v[118:121], v[168:171], v[72:75]\n"
      "v_mul_f32 v108, v100, v108\n"
      "v_mul_f32 v109, v100, v109\n"
      "v_mfma_f32_16x16x32_bf16 v[124:127], v[148:151], v[168:171], v[28:31]\n"
      "v_cvt_pk_bf16_f32 v108, v108, v109\n"
      "v_mul_f32 v110, v100, v110\n"
      "v_mfma_f32_16x16x16_bf16 v[112:115], v[172:173], v[122:123], v[112:115]\n"
      "v_mul_f32 v111, v100, v111\n"
      "v_permlane32_swap_b32_e64 v157, v101\n"
      "v_mfma_f32_16x16x16_bf16 v[190:193], v[218:219], v[122:123], v[190:193]\n"
      "s_waitcnt vmcnt(6)\n"
      "v_or3_b32 v136, v136, v137, v138\n"
      "v_cvt_pk_bf16_f32 v109, v110, v111\n"
      "s_waitcnt vmcnt(5)\n"
      "v_or3_b32 v136, v136, v139, v39\n"
      "v_mfma_f32_16x16x16_bf16 v[174:177], v[178:179], v[122:123], v[174:177]\n"
      "v_add_f32 v157, v157, v101\n"
      "v_exp_f32 v112, v112\n"
      "v_mfma_f32_16x16x16_bf16 v[124:127], v[180:181], v[122:123], v[124:127]\n"
      "v_exp_f32 v113, v113\n"
      "v_exp_f32 v114, v114\n"
      "v_mfma_f32_16x16x32_bf16 v[100:103], a[216:219], v[152:155], 0\n"
      "ds_write_b64 v4, v[108:109]\n"
      "v_mfma_f32_16x16x32_bf16 v[100:103], a[220:223], v[164:167], v[100:103]\n"
      "v_exp_f32 v115, v115\n"
      "v_mfma_f32_16x16x32_bf16 v[100:103], a[224:227], v[210:213], v[100:103]\n"
      "v_exp_f32 v190, v190\n"
      "v_mfma_f32_16x16x32_bf16 v[126:129], a[240:243], v[152:155], 0\n"
      "ds_write_b32 v8, v157\n"
      "v_mfma_f32_16x16x32_bf16 v[126:129], a[244:247], v[164:167], v[126:129]\n"
      "ds_read_b128 v[108:111], v10\n"
      "v_exp_f32 v191, v191\n"
      "v_mfma_f32_16x16x32_bf16 v[126:129], a[248:251], v[210:213], v[126:129]\n"
      "v_exp_f32 v192, v192\n"
      "v_mfma_f32_16x16x32_bf16 v[96:99], v[232:235], v[152:155], 0\n"
      "v_exp_f32_e64 v193, v193\n"
      "v_mfma_f32_16x16x32_bf16 v[96:99], v[236:239], v[164:167], v[96:99]\n"
      "v_and_b32 v136, 0x7fff7fff, v136\n"
      "v_cvt_pk_bf16_f32 v112, v112, v113\n"
      "v_mfma_f32_16x16x32_bf16 v[96:99], v[240:243], v[210:213], v[96:99]\n"
      "ds_read_b64 v[88:89], v10 offset:16\n"
      "v_cvt_pk_bf16_f32 v113, v114, v115\n"
      "v_mfma_f32_16x16x32_bf16 v[210:213], v[32:35], v[132:135], 0\n"
      "ds_write_b32 v31, v136\n"
      "v_mfma_f32_16x16x32_bf16 v[210:213], v[32:35], v[92:95], v[210:213]\n"
      "v_cvt_pk_bf16_f32 v114, v190, v191\n"
      "v_cvt_pk_bf16_f32 v115, v192, v193\n"
      "v_mfma_f32_16x16x32_bf16 v[164:167], v[198:201], v[132:135], 0\n"
      "s_waitcnt lgkmcnt(2)\n"
      "v_exp_f32 v174, v174\n"
      "v_mfma_f32_16x16x32_bf16 v[164:167], v[76:79], v[92:95], v[164:167]\n"
      "ds_read_b128 v[190:193], v2 offset:28160\n"
      "v_exp_f32 v175, v175\n"
      "v_mfma_f32_16x16x32_bf16 v[152:155], v[214:217], v[132:135], 0\n"
      "v_rcp_f32 v210, v210\n"
      "v_mfma_f32_16x16x32_bf16 v[152:155], v[80:83], v[92:95], v[152:155]\n"
      "s_waitcnt lgkmcnt(2)\n"
      "v_exp_f32 v176, v176\n"
      "v_mfma_f32_16x16x32_bf16 v[168:171], v[194:197], v[132:135], 0\n"
      "v_mul_f32 v164, v210, v164\n"
      "v_mul_f32 v165, v210, v165\n"
      "v_mfma_f32_16x16x32_bf16 v[168:171], v[84:87], v[92:95], v[168:171]\n"
      "ds_read_b128 v[156:159], v2 offset:31232\n"
      "v_cvt_pk_bf16_f32 v164, v164, v165\n"
      "v_mfma_f32_16x16x32_bf16 v[130:133], v[108:111], v[206:209], v[30:33]\n"
      "v_mul_f32 v166, v210, v166\n"
      "v_mul_f32 v167, v210, v167\n"
      "v_mfma_f32_16x16x32_bf16 v[136:139], v[108:111], v[202:205], v[30:33]\n"
      "s_waitcnt lgkmcnt(1)\n"
      "v_cvt_pk_bf16_f32 v165, v166, v167\n"
      "v_mul_f32 v152, v210, v152\n"
      "v_mfma_f32_16x16x32_bf16 v[202:205], v[108:111], v[118:121], v[30:33]\n"
      "v_mul_f32 v153, v210, v153\n"
      "v_mul_f32 v154, v210, v154\n"
      "v_mfma_f32_16x16x32_bf16 v[206:209], v[108:111], v[148:151], v[30:33]\n"
      "ds_read_b128 v[108:111], v2 offset:34304\n"
      "v_mul_f32_e64 v155, v210, v155\n"
      "v_mfma_f32_16x16x16_bf16 v[130:133], v[88:89], v[172:173], v[130:133]\n"
      "v_cvt_pk_bf16_f32 v166, v152, v153\n"
      "v_cvt_pk_bf16_f32 v167, v154, v155\n"
      "v_mul_f32 v168, v210, v168\n"
      "v_mfma_f32_16x16x16_bf16 v[136:139], v[88:89], v[218:219], v[136:139]\n"
      "v_mul_f32 v169, v210, v169\n"
      "v_cvt_pk_bf16_f32 v168, v168, v169\n"
      "v_mul_f32 v170, v210, v170\n"
      "v_mfma_f32_16x16x16_bf16 v[202:205], v[88:89], v[178:179], v[202:205]\n"
      "v_mul_f32 v171, v210, v171\n"
      "v_cvt_pk_bf16_f32 v169, v170, v171\n"
      "v_exp_f32 v177, v177\n"
      "v_mfma_f32_16x16x16_bf16 v[206:209], v[88:89], v[180:181], v[206:209]\n"
      "v_cndmask_b32 v130, v130, v136, s[58:59]\n"
      "v_exp_f32 v124, v124\n"
      "v_mfma_f32_16x16x32_bf16 v[100:103], a[228:231], v[190:193], v[100:103]\n"
      "s_waitcnt lgkmcnt(1)\n"
      "v_cndmask_b32 v130, v130, v202, s[60:61]\n"
      "v_cvt_pk_bf16_f32 v88, v174, v175\n"
      "v_mfma_f32_16x16x32_bf16 v[100:103], a[232:235], v[156:159], v[100:103]\n"
      "s_waitcnt lgkmcnt(0)\n"
      "v_cndmask_b32 v130, v130, v206, s[62:63]\n"
      "v_exp_f32_e64 v130, v130\n"
      "v_mfma_f32_16x16x32_bf16 v[100:103], a[236:239], v[108:111], v[100:103]\n"
      "ds_write_b128 v3, v[164:167] offset:256\n"
      "v_mfma_f32_16x16x32_bf16 v[126:129], a[252:255], v[190:193], v[126:129]\n"
      "v_cvt_pk_bf16_f32 v130, v130, v130\n"
      "v_cvt_pk_bf16_f32 v89, v176, v177\n"
      "v_mfma_f32_16x16x32_bf16 v[126:129], v[224:227], v[156:159], v[126:129]\n"
      "v_cvt_pk_bf16_f32 v90, v124, 0\n"
      "v_readlane_b32 s45, v37, s43\n"
      "v_mfma_f32_16x16x32_bf16 v[126:129], v[228:231], v[108:111], v[126:129]\n"
      "ds_write_b64 v4, v[168:169] offset:256\n"
      "v_mfma_f32_16x16x32_bf16 v[96:99], v[244:247], v[190:193], v[96:99]\n"
      "v_mul_f32 v192, v100, v100\n"
      "v_add_f32 v191, v100, v101\n"
      "v_mfma_f32_16x16x32_bf16 v[96:99], v[248:251], v[156:159], v[96:99]\n"
      "v_fmac_f32 v192, v101, v101\n"
      "v_add_f32 v191, v191, v102\n"
      "v_mfma_f32_16x16x32_bf16 v[96:99], v[252:255], v[108:111], v[96:99]\n"
      "ds_write_b16 v13, v130\n"
      "v_mfma_f32_16x16x32_bf16 v[202:205], v[32:35], v[112:115], 0\n"
      "ds_read_b128 v[210:213], v14\n"
      "v_fmac_f32 v192, v102, v102\n"
      "v_mfma_f32_16x16x32_bf16 v[202:205], v[32:35], v[88:91], v[202:205]\n"
      "ds_read_b128 v[136:139], v14 offset:64\n"
      "v_add_f32 v191, v191, v103\n"
      "v_mfma_f32_16x16x32_bf16 v[130:133], v[198:201], v[112:115], 0\n"
      "v_fmac_f32 v192, v103, v103\n"
      "v_add_f32 v191, v191, v126\n"
      "v_mfma_f32_16x16x32_bf16 v[130:133], v[76:79], v[88:91], v[130:133]\n"
      "v_fmac_f32 v192, v126, v126\n"
      "v_add_f32 v191, v191, v127\n"
      "v_mfma_f32_16x16x32_bf16 v[204:207], v[214:217], v[112:115], 0\n"
      "v_fmac_f32 v192, v127, v127\n"
      "v_add_f32 v191, v191, v128\n"
      "v_mfma_f32_16x16x32_bf16 v[204:207], v[80:83], v[88:91], v[204:207]\n"
      "v_fmac_f32 v192, v128, v128\n"
      "v_add_f32 v191, v191, v129\n"
      "v_mfma_f32_16x16x32_bf16 v[174:177], v[194:197], v[112:115], 0\n"
      "v_fmac_f32 v192, v129, v129\n"
      "v_add_f32 v191, v191, v96\n"
      "v_mfma_f32_16x16x32_bf16 v[174:177], v[84:87], v[88:91], v[174:177]\n"
      "s_waitcnt lgkmcnt(1)\n"
      "v_fmac_f32 v192, v96, v96\n"
      "v_add_f32_e64 v191, v191, v97\n"
      "v_mfma_f32_16x16x32_bf16 v[108:111], v[210:213], v[32:35], 0\n"
      "s_waitcnt lgkmcnt(0)\n"
      "v_fmac_f32 v192, v97, v97\n"
      "v_add_f32 v191, v191, v98\n"
      "v_mfma_f32_16x16x32_bf16 v[108:111], v[136:139], v[32:35], v[108:111]\n"
      "v_fmac_f32 v192, v98, v98\n"
      "v_add_f32 v191, v191, v99\n"
      "v_fmac_f32 v192, v99, v99\n"
      "v_rcp_f32 v202, v202\n"
      "v_readlane_b32 s46, v37, s42\n"
      "v_permlane16_swap_b32 v191, v192\n"
      "v_mul_f32 v130, v202, v130\n"
      "v_mul_f32 v131, v202, v131\n"
      "v_add_f32 v191, v191, v192\n"
      "v_cvt_pk_bf16_f32 v130, v130, v131\n"
      "v_mul_f32 v132, v202, v132\n"
      "v_mul_f32 v133, v202, v133\n"
      "v_mov_b32 v192, v191\n"
      "v_cvt_pk_bf16_f32 v131, v132, v133\n"
      "v_mul_f32 v204, v202, v204\n"
      "v_mul_f32 v205, v202, v205\n"
      "v_mul_f32 v206, v202, v206\n"
      "v_mul_f32 v207, v202, v207\n"
      "v_cvt_pk_bf16_f32 v132, v204, v205\n"
      "v_cvt_pk_bf16_f32 v133, v206, v207\n"
      "v_mul_f32 v174, v202, v174\n"
      "v_mul_f32 v175, v202, v175\n"
      "ds_write_b128 v3, v[130:133] offset:512\n"
      "v_cvt_pk_bf16_f32 v174, v174, v175\n"
      "v_mul_f32 v176, v202, v176\n"
      "v_mul_f32 v177, v202, v177\n"
      "v_permlane32_swap_b32 v191, v192\n"
      "v_cvt_pk_bf16_f32 v175, v176, v177\n"
      "v_add_f32 v191, v191, v192\n"
      "v_or3_b32 v144, v144, v145, v146\n"
      "ds_write_b64 v4, v[174:175] offset:512\n"
      "v_or_b32 v144, v144, v147\n"
      "v_cmp_ne_u32 s[56:57], 0, v144\n"
      "v_readlane_b32 s47, v38, s43\n"
      "v_add_u32_e64 v12, 32, v12\n"
      "s_or_b32 s95, s45, s46\n"
      "ds_write_b32 v8, v191 offset:512\n"
      "s_or_b32 s95, s95, s48\n"
      "s_cmp_ge_u32 s95, 0x40000000\n"
      "s_cselect_b32 s94, -1, -2\n"
      "s_cmp_gt_u32 s42, s44\n"
      "s_cselect_b32 s95, s94, s95\n"
      "s_cmp_ge_u32 s95, 0x40000000\n"
      "s_cselect_b64 vcc, -1, s[56:57]\n"
      "s_and_b32 s47, s47, -4\n"
      "s_add_u32 s42, s42, 1\n"
      "s_add_u32 s43, s43, 1\n"
      "s_waitcnt lgkmcnt(0)\n"
      "s_barrier\n"
      "s_cbranch_vccz L_loop1_%=\n"
      "s_branch L_disp1_%=\n"
      "s_nop 7\n"
      "s_nop 7\n"
      "L_max1_%=:\n"
      "v_mfma_f32_16x16x32_bf16 v[174:177], v[210:213], v[198:201], 0\n"
      "ds_read_b128 v[114:117], v9\n"
      "ds_read_b128 v[120:123], v9 offset:1024\n"
      "v_mfma_f32_16x16x32_bf16 v[174:177], v[136:139], v[76:79], v[174:177]\n"
      "ds_read_b128 v[144:147], v0 offset:18432\n"
      "ds_read_b128 v[202:205], v1 offset:18432\n"
      "v_mfma_f32_16x16x32_bf16 v[190:193], v[210:213], v[214:217], 0\n"
      "ds_read_b128 v[154:157], v0 offset:24576\n"
      "s_add_u32 m0, s64, 0\n"
      "v_mfma_f32_16x16x32_bf16 v[190:193], v[136:139], v[80:83], v[190:193]\n"
      "buffer_load_dwordx4 v16, s[16:19], s45 offen  lds\n"
      "v_mfma_f32_16x16x32_bf16 v[214:217], v[210:213], v[194:197], 0\n"
      "s_add_u32 m0, s64, 4096\n"
      "v_mfma_f32_16x16x32_bf16 v[214:217], v[136:139], v[84:87], v[214:217]\n"
      "v_mov_b32_dpp v108, v108 quad_perm:[0,0,0,0] row_mask:0xf bank_mask:0xf\n"
      "v_rcp_f32 v108, v108\n"
      "s_waitcnt lgkmcnt(4)\n"
      "v_add_f32 v114, v114, v116\n"
      "v_cndmask_b32 v174, v174, v190, s[58:59]\n"
      "s_waitcnt lgkmcnt(3)\n"
      "v_add_f32_e64 v120, v120, v122\n"
      "s_waitcnt lgkmcnt(2)\n"
      "v_mfma_f32_16x16x32_bf16 v[170:173], a[0:3], v[144:147], 0\n"
      "buffer_load_dwordx4 v17, s[16:19], s45 offen  lds\n"
      "v_mfma_f32_16x16x32_bf16 v[166:169], a[24:27], v[144:147], 0\n"
      "s_add_u32 m0, s64, 8192\n"
      "v_cndmask_b32 v174, v174, v214, s[60:61]\n"
      "v_mul_f32 v174, v108, v174\n"
      "v_mfma_f32_16x16x32_bf16 v[148:151], a[48:51], v[144:147], 0\n"
      "s_waitcnt lgkmcnt(0)\n"
      "v_cvt_pk_bf16_f32 v174, v174, v174\n"
      "v_add_f32_e64 v115, v115, v117\n"
      "v_mfma_f32_16x16x32_bf16 v[116:119], a[72:75], v[144:147], 0\n"
      "ds_write_b16 v12, v174\n"
      "v_mfma_f32_16x16x32_bf16 v[210:213], a[96:99], v[144:147], 0\n"
      "buffer_load_dwordx4 v18, s[16:19], s45 offen  lds\n"
      "v_mfma_f32_16x16x32_bf16 v[130:133], a[120:123], v[144:147], 0\n"
      "ds_read_b128 v[174:177], v1 offset:24576\n"
      "v_add_f32_e64 v121, v121, v123\n"
      "v_mfma_f32_16x16x32_bf16 v[170:173], a[4:7], v[202:205], v[170:173]\n"
      "ds_read_b128 v[108:111], v0 offset:30720\n"
      "v_add_f32_e64 v114, v114, v120\n"
      "v_mfma_f32_16x16x32_bf16 v[170:173], a[8:11], v[154:157], v[170:173]\n"
      "ds_read_b128 v[206:209], v1 offset:30720\n"
      "v_add_f32_e64 v115, v115, v121\n"
      "v_mfma_f32_16x16x32_bf16 v[166:169], a[28:31], v[202:205], v[166:169]\n"
      "s_add_u32 m0, s64, 12288\n"
      "v_fma_f32 v120, -v114, v114, s52\n"
      "v_fmac_f32 v120, 0x43400000, v115\n"
      "v_mfma_f32_16x16x32_bf16 v[166:169], a[32:35], v[154:157], v[166:169]\n"
      "buffer_load_dwordx4 v19, s[16:19], s45 offen  lds\n"
      "v_mfma_f32_16x16x32_bf16 v[148:151], a[52:55], v[202:205], v[148:151]\n"
      "s_add_u32 m0, s65, 16384\n"
      "v_max_f32 v120, s52, v120\n"
      "v_rsq_f32 v120, v120\n"
      "v_mfma_f32_16x16x32_bf16 v[148:151], a[56:59], v[154:157], v[148:151]\n"
      "s_waitcnt lgkmcnt(2)\n"
      "v_mul_f32 v199, 0x43400000, v120\n"
      "v_mul_f32 v200, -v114, v120\n"
      "v_mfma_f32_16x16x32_bf16 v[116:119], a[76:79], v[202:205], v[116:119]\n"
      "s_waitcnt lgkmcnt(1)\n"
      "v_mov_b32 v124, v199\n"
      "v_mov_b32 v125, v200\n"
      "v_mfma_f32_16x16x32_bf16 v[116:119], a[80:83], v[154:157], v[116:119]\n"
      "buffer_load_dwordx4 v15, s[16:19], s45 offen  lds\n"
      "v_mfma_f32_16x16x32_bf16 v[210:213], a[100:103], v[202:205], v[210:213]\n"
      "s_waitcnt lgkmcnt(0)\n"
      "v_permlane32_swap_b32 v199, v124\n"
      "v_mfma_f32_16x16x32_bf16 v[210:213], a[104:107], v[154:157], v[210:213]\n"
      "v_permlane32_swap_b32 v200, v125\n"
      "v_mfma_f32_16x16x32_bf16 v[130:133], a[124:127], v[202:205], v[130:133]\n"
      "v_mov_b32 v214, v199\n"
      "v_mov_b32 v114, v200\n"
      "v_mfma_f32_16x16x32_bf16 v[130:133], a[128:131], v[154:157], v[130:133]\n"
      "buffer_load_dwordx4 v[190:193], v27, s[24:27], s47 offen\n"
      "v_mfma_f32_16x16x32_bf16 v[170:173], a[12:15], v[174:177], v[170:173]\n"
      "v_permlane16_swap_b32 v199, v214\n"
      "v_mfma_f32_16x16x32_bf16 v[170:173], a[16:19], v[108:111], v[170:173]\n"
      "v_permlane16_swap_b32_e64 v200, v114\n"
      "v_mfma_f32_16x16x32_bf16 v[170:173], a[20:23], v[206:209], v[170:173]\n"
      "v_fma_f32 v140, v140, v199, v200\n"
      "v_fma_f32 v141, v141, v199, v200\n"
      "v_mfma_f32_16x16x32_bf16 v[166:169], a[36:39], v[174:177], v[166:169]\n"
      "buffer_load_dword v39, v29, s[24:27], s47 offen\n"
      "v_mfma_f32_16x16x32_bf16 v[166:169], a[40:43], v[108:111], v[166:169]\n"
      "v_cvt_pk_bf16_f32 v140, v140, v141\n"
      "v_fma_f32 v142, v142, v199, v200\n"
      "v_mfma_f32_16x16x32_bf16 v[166:169], a[44:47], v[206:209], v[166:169]\n"
      "v_fma_f32 v143, v143, v199, v200\n"
      "v_fma_f32 v160, v160, v199, v200\n"
      "v_mfma_f32_16x16x32_bf16 v[148:151], a[60:63], v[174:177], v[148:151]\n"
      "v_fma_f32 v161, v161, v199, v200\n"
      "v_cvt_pk_bf16_f32 v141, v142, v143\n"
      "v_mfma_f32_16x16x32_bf16 v[148:151], a[64:67], v[108:111], v[148:151]\n"
      "v_fma_f32 v220, v220, v199, v200\n"
      "v_fma_f32 v221, v221, v199, v200\n"
      "v_mfma_f32_16x16x32_bf16 v[148:151], a[68:71], v[206:209], v[148:151]\n"
      "v_fma_f32 v222, v222, v199, v200\n"
      "v_fma_f32 v223, v223, v199, v200\n"
      "v_mfma_f32_16x16x32_bf16 v[116:119], a[84:87], v[174:177], v[116:119]\n"
      "v_cvt_pk_bf16_f32 v160, v160, v161\n"
      "v_fma_f32 v162, v162, v199, v200\n"
      "v_mfma_f32_16x16x32_bf16 v[116:119], a[88:91], v[108:111], v[116:119]\n"
      "v_fma_f32 v163, v163, v199, v200\n"
      "v_cvt_pk_bf16_f32 v142, v220, v221\n"
      "v_mfma_f32_16x16x32_bf16 v[116:119], a[92:95], v[206:209], v[116:119]\n"
      "v_cvt_pk_bf16_f32 v143, v222, v223\n"
      "v_cvt_pk_bf16_f32 v161, v162, v163\n"
      "v_mfma_f32_16x16x32_bf16 v[210:213], a[108:111], v[174:177], v[210:213]\n"
      "ds_write_b128 v5, v[140:143] offset:18432\n"
      "v_mfma_f32_16x16x32_bf16 v[210:213], a[112:115], v[108:111], v[210:213]\n"
      "v_fma_f32 v186, v186, v214, v114\n"
      "v_fma_f32 v187, v187, v214, v114\n"
      "v_mfma_f32_16x16x32_bf16 v[210:213], a[116:119], v[206:209], v[210:213]\n"
      "v_cvt_pk_bf16_f32 v186, v186, v187\n"
      "v_fma_f32 v188, v188, v214, v114\n"
      "v_mfma_f32_16x16x32_bf16 v[130:133], a[132:135], v[174:177], v[130:133]\n"
      "ds_write_b64 v6, v[160:161] offset:18432\n"
      "v_mfma_f32_16x16x32_bf16 v[130:133], a[136:139], v[108:111], v[130:133]\n"
      "v_fma_f32 v189, v189, v214, v114\n"
      "v_fma_f32 v104, v104, v214, v114\n"
      "v_mfma_f32_16x16x32_bf16 v[130:133], a[140:143], v[206:209], v[130:133]\n"
      "v_fma_f32 v105, v105, v214, v114\n"
      "v_cvt_pk_bf16_f32 v187, v188, v189\n"
      "v_mfma_f32_16x16x32_bf16 v[198:201], v[144:147], a[144:147], 0\n"
      "v_fma_f32 v182, v182, v214, v114\n"
      "v_fma_f32 v183, v183, v214, v114\n"
      "v_mfma_f32_16x16x32_bf16 v[198:201], v[202:205], a[148:151], v[198:201]\n"
      "v_fma_f32 v184, v184, v214, v114\n"
      "v_fma_f32 v185, v185, v214, v114\n"
      "v_mfma_f32_16x16x32_bf16 v[198:201], v[154:157], a[152:155], v[198:201]\n"
      "v_cvt_pk_bf16_f32 v104, v104, v105\n"
      "v_fma_f32 v106, v106, v214, v114\n"
      "v_mfma_f32_16x16x32_bf16 v[198:201], v[174:177], a[156:159], v[198:201]\n"
      "v_fma_f32 v107, v107, v214, v114\n"
      "v_cvt_pk_bf16_f32 v188, v182, v183\n"
      "v_mfma_f32_16x16x32_bf16 v[198:201], v[108:111], a[160:163], v[198:201]\n"
      "v_cvt_pk_bf16_f32 v189, v184, v185\n"
      "v_cvt_pk_bf16_f32 v105, v106, v107\n"
      "v_mfma_f32_16x16x32_bf16 v[198:201], v[206:209], a[164:167], v[198:201]\n"
      "ds_write_b128 v5, v[186:189] offset:24576\n"
      "v_mfma_f32_16x16x32_bf16 v[214:217], v[144:147], a[168:171], 0\n"
      "ds_read_b128 v[136:139], v0 offset:20480\n"
      "v_fma_f32 v100, v100, v124, v125\n"
      "v_mfma_f32_16x16x32_bf16 v[214:217], v[202:205], a[172:175], v[214:217]\n"
      "ds_read_b128 v[218:221], v1 offset:20480\n"
      "v_fma_f32 v101, v101, v124, v125\n"
      "v_mfma_f32_16x16x32_bf16 v[214:217], v[154:157], a[176:179], v[214:217]\n"
      "ds_write_b64 v6, v[104:105] offset:24576\n"
      "v_mfma_f32_16x16x32_bf16 v[214:217], v[174:177], a[180:183], v[214:217]\n"
      "ds_read_b128 v[112:115], v0 offset:26624\n"
      "v_cvt_pk_bf16_f32 v100, v100, v101\n"
      "v_mfma_f32_16x16x32_bf16 v[214:217], v[108:111], a[184:187], v[214:217]\n"
      "ds_read_b128 v[178:181], v1 offset:26624\n"
      "v_fma_f32 v102, v102, v124, v125\n"
      "v_mfma_f32_16x16x32_bf16 v[214:217], v[206:209], a[188:191], v[214:217]\n"
      "ds_read_b128 v[162:165], v0 offset:32768\n"
      "v_fma_f32 v103, v103, v124, v125\n"
      "v_mfma_f32_16x16x32_bf16 v[194:197], v[144:147], a[192:195], 0\n"
      "ds_read_b128 v[120:123], v1 offset:32768\n"
      "v_fma_f32 v96, v96, v124, v125\n"
      "v_mfma_f32_16x16x32_bf16 v[194:197], v[202:205], a[196:199], v[194:197]\n"
      "s_waitcnt lgkmcnt(6)\n"
      "v_fma_f32 v97, v97, v124, v125\n"
      "v_cvt_pk_bf16_f32 v101, v102, v103\n"
      "v_mfma_f32_16x16x32_bf16 v[194:197], v[154:157], a[200:203], v[194:197]\n"
      "s_waitcnt lgkmcnt(5)\n"
      "v_fma_f32 v126, v126, v124, v125\n"
      "v_fma_f32 v127, v127, v124, v125\n"
      "v_mfma_f32_16x16x32_bf16 v[194:197], v[174:177], a[204:207], v[194:197]\n"
      "v_fma_f32 v128, v128, v124, v125\n"
      "v_fma_f32 v129, v129, v124, v125\n"
      "v_mfma_f32_16x16x32_bf16 v[194:197], v[108:111], a[208:211], v[194:197]\n"
      "s_waitcnt lgkmcnt(3)\n"
      "v_cvt_pk_bf16_f32 v96, v96, v97\n"
      "v_fma_f32 v98, v98, v124, v125\n"
      "v_mfma_f32_16x16x32_bf16 v[194:197], v[206:209], a[212:215], v[194:197]\n"
      "s_waitcnt lgkmcnt(2)\n"
      "v_fma_f32 v99, v99, v124, v125\n"
      "v_cvt_pk_bf16_f32 v102, v126, v127\n"
      "v_mfma_f32_16x16x32_bf16 v[174:177], a[0:3], v[136:139], 0\n"
      "s_waitcnt lgkmcnt(1)\n"
      "v_cvt_pk_bf16_f32 v103, v128, v129\n"
      "v_cvt_pk_bf16_f32 v97, v98, v99\n"
      "v_mfma_f32_16x16x32_bf16 v[174:177], a[4:7], v[218:221], v[174:177]\n"
      "ds_write_b128 v5, v[100:103] offset:30720\n"
      "v_mfma_f32_16x16x32_bf16 v[174:177], a[8:11], v[112:115], v[174:177]\n"
      "s_waitcnt lgkmcnt(1)\n"
      "v_fma_f32 v99, v170, v170, s51\n"
      "v_fma_f32 v146, v116, v116, s51\n"
      "v_mfma_f32_16x16x32_bf16 v[174:177], a[12:15], v[178:181], v[174:177]\n"
      "v_fmac_f32 v99, v171, v171\n"
      "v_fmac_f32 v146, v117, v117\n"
      "v_mfma_f32_16x16x32_bf16 v[174:177], a[16:19], v[162:165], v[174:177]\n"
      "ds_write_b64 v6, v[96:97] offset:30720\n"
      "v_mfma_f32_16x16x32_bf16 v[174:177], a[20:23], v[120:123], v[174:177]\n"
      "v_fmac_f32 v99, v172, v172\n"
      "v_fmac_f32 v146, v118, v118\n"
      "v_mfma_f32_16x16x32_bf16 v[206:209], a[24:27], v[136:139], 0\n"
      "v_fmac_f32 v99, v173, v173\n"
      "v_fmac_f32 v146, v119, v119\n"
      "v_mfma_f32_16x16x32_bf16 v[206:209], a[28:31], v[218:221], v[206:209]\n"
      "v_fmac_f32 v99, v166, v166\n"
      "v_fmac_f32 v146, v210, v210\n"
      "v_mfma_f32_16x16x32_bf16 v[206:209], a[32:35], v[112:115], v[206:209]\n"
      "v_fmac_f32 v99, v167, v167\n"
      "v_fmac_f32 v146, v211, v211\n"
      "v_mfma_f32_16x16x32_bf16 v[206:209], a[36:39], v[178:181], v[206:209]\n"
      "v_fmac_f32 v99, v168, v168\n"
      "v_fmac_f32 v146, v212, v212\n"
      "v_mfma_f32_16x16x32_bf16 v[206:209], a[40:43], v[162:165], v[206:209]\n"
      "v_fmac_f32 v99, v169, v169\n"
      "v_fmac_f32 v146, v213, v213\n"
      "v_mfma_f32_16x16x32_bf16 v[206:209], a[44:47], v[120:123], v[206:209]\n"
      "v_fmac_f32 v99, v148, v148\n"
      "v_fmac_f32 v146, v130, v130\n"
      "v_mfma_f32_16x16x32_bf16 v[124:127], a[48:51], v[136:139], 0\n"
      "v_fmac_f32 v99, v149, v149\n"
      "v_fmac_f32 v146, v131, v131\n"
      "v_mfma_f32_16x16x32_bf16 v[124:127], a[52:55], v[218:221], v[124:127]\n"
      "v_fmac_f32 v99, v150, v150\n"
      "v_fmac_f32 v146, v132, v132\n"
      "v_mfma_f32_16x16x32_bf16 v[124:127], a[56:59], v[112:115], v[124:127]\n"
      "v_fmac_f32 v99, v151, v151\n"
      "v_fmac_f32 v146, v133, v133\n"
      "v_mfma_f32_16x16x32_bf16 v[124:127], a[60:63], v[178:181], v[124:127]\n"
      "v_fma_f32 v128, v174, v174, s51\n"
      "v_permlane16_swap_b32 v99, v146\n"
      "v_mfma_f32_16x16x32_bf16 v[124:127], a[64:67], v[162:165], v[124:127]\n"
      "v_add_f32 v99, v99, v146\n"
      "v_mov_b32 v146, v99\n"
      "v_mfma_f32_16x16x32_bf16 v[124:127], a[68:71], v[120:123], v[124:127]\n"
      "v_fmac_f32 v128, v175, v175\n"
      "v_permlane32_swap_b32 v99, v146\n"
      "v_mfma_f32_16x16x32_bf16 v[108:111], a[72:75], v[136:139], 0\n"
      "v_add_f32 v99, v99, v146\n"
      "v_rsq_f32 v99, v99\n"
      "v_mfma_f32_16x16x32_bf16 v[108:111], a[76:79], v[218:221], v[108:111]\n"
      "v_mov_b32 v146, v99\n"
      "v_fmac_f32 v128, v176, v176\n"
      "v_mfma_f32_16x16x32_bf16 v[108:111], a[80:83], v[112:115], v[108:111]\n"
      "v_permlane16_swap_b32 v99, v146\n"
      "v_mfma_f32_16x16x32_bf16 v[108:111], a[84:87], v[178:181], v[108:111]\n"
      "v_mul_f32 v99, s50, v99\n"
      "v_mul_f32 v116, v146, v116\n"
      "v_mfma_f32_16x16x32_bf16 v[108:111], a[88:91], v[162:165], v[108:111]\n"
      "v_mul_f32 v117, v146, v117\n"
      "v_mul_f32 v170, v99, v170\n"
      "v_mfma_f32_16x16x32_bf16 v[108:111], a[92:95], v[120:123], v[108:111]\n"
      "v_mul_f32 v171, v99, v171\n"
      "v_cvt_pk_bf16_f32 v116, v116, v117\n"
      "v_mfma_f32_16x16x32_bf16 v[100:103], a[96:99], v[136:139], 0\n"
      "v_mul_f32 v118, v146, v118\n"
      "v_mul_f32 v119, v146, v119\n"
      "v_mfma_f32_16x16x32_bf16 v[100:103], a[100:103], v[218:221], v[100:103]\n"
      "v_mul_f32 v130, v146, v130\n"
      "v_mul_f32 v131, v146, v131\n"
      "v_mfma_f32_16x16x32_bf16 v[100:103], a[104:107], v[112:115], v[100:103]\n"
      "v_cvt_pk_bf16_f32 v170, v170, v171\n"
      "v_mul_f32 v172, v99, v172\n"
      "v_mfma_f32_16x16x32_bf16 v[100:103], a[108:111], v[178:181], v[100:103]\n"
      "v_mul_f32 v173, v99, v173\n"
      "v_mul_f32 v148, v99, v148\n"
      "v_mfma_f32_16x16x32_bf16 v[100:103], a[112:115], v[162:165], v[100:103]\n"
      "v_mul_f32_e64 v149, v99, v149\n"
      "v_cvt_pk_bf16_f32 v117, v118, v119\n"
      "v_mfma_f32_16x16x32_bf16 v[100:103], a[116:119], v[120:123], v[100:103]\n"
      "v_mul_f32 v210, v146, v210\n"
      "v_mul_f32 v211, v146, v211\n"
      "v_mfma_f32_16x16x32_bf16 v[158:161], a[120:123], v[136:139], 0\n"
      "v_mul_f32 v212, v146, v212\n"
      "v_mul_f32 v213, v146, v213\n"
      "v_mfma_f32_16x16x32_bf16 v[158:161], a[124:127], v[218:221], v[158:161]\n"
      "v_cvt_pk_bf16_f32 v130, v130, v131\n"
      "v_mul_f32 v132, v146, v132\n"
      "v_mfma_f32_16x16x32_bf16 v[158:161], a[128:131], v[112:115], v[158:161]\n"
      "v_mul_f32 v133, v146, v133\n"
      "v_cvt_pk_bf16_f32 v171, v172, v173\n"
      "v_mfma_f32_16x16x32_bf16 v[158:161], a[132:135], v[178:181], v[158:161]\n"
      "ds_read_b128 v[140:143], v7\n"
      "v_mul_f32_e64 v166, v99, v166\n"
      "v_mfma_f32_16x16x32_bf16 v[158:161], a[136:139], v[162:165], v[158:161]\n"
      "v_mul_f32 v167, v99, v167\n"
      "v_mul_f32 v168, v99, v168\n"
      "v_mfma_f32_16x16x32_bf16 v[158:161], a[140:143], v[120:123], v[158:161]\n"
      "ds_read_b128 v[144:147], v7 offset:4096\n"
      "v_mul_f32_e64 v169, v99, v169\n"
      "v_mfma_f32_16x16x32_bf16 v[154:157], v[136:139], a[144:147], 0\n"
      "ds_read_b128 v[182:185], v7 offset:8192\n"
      "v_cvt_pk_bf16_f32 v148, v148, v149\n"
      "v_mfma_f32_16x16x32_bf16 v[154:157], v[218:221], a[148:151], v[154:157]\n"
      "v_mul_f32 v150, v99, v150\n"
      "v_mul_f32 v151, v99, v151\n"
      "v_mfma_f32_16x16x32_bf16 v[154:157], v[112:115], a[152:155], v[154:157]\n"
      "ds_read_b128 v[202:205], v7 offset:12288\n"
      "v_cvt_pk_bf16_f32 v118, v210, v211\n"
      "v_mfma_f32_16x16x32_bf16 v[154:157], v[178:181], a[156:159], v[154:157]\n"
      "ds_read_b128 v[96:99], v7 offset:16384\n"
      "v_cvt_pk_bf16_f32 v119, v212, v213\n"
      "v_mfma_f32_16x16x32_bf16 v[154:157], v[162:165], a[160:163], v[154:157]\n"
      "v_cvt_pk_bf16_f32 v131, v132, v133\n"
      "v_cvt_pk_bf16_f32 v172, v166, v167\n"
      "v_mfma_f32_16x16x32_bf16 v[154:157], v[120:123], a[164:167], v[154:157]\n"
      "ds_read_b128 v[210:213], v0 offset:22528\n"
      "v_cvt_pk_bf16_f32 v173, v168, v169\n"
      "v_mfma_f32_16x16x32_bf16 v[166:169], v[136:139], a[168:171], 0\n"
      "ds_read_b128 v[104:107], v1 offset:22528\n"
      "v_cvt_pk_bf16_f32 v149, v150, v151\n"
      "v_mfma_f32_16x16x32_bf16 v[166:169], v[218:221], a[172:175], v[166:169]\n"
      "s_waitcnt lgkmcnt(4)\n"
      "v_fma_f32 v129, v108, v108, s51\n"
      "v_fmac_f32 v129, v109, v109\n"
      "v_mfma_f32_16x16x32_bf16 v[166:169], v[112:115], a[176:179], v[166:169]\n"
      "buffer_store_dwordx4 v[140:143], v20, s[20:23], s46 offen sc1\n"
      "v_mfma_f32_16x16x32_bf16 v[166:169], v[178:181], a[180:183], v[166:169]\n"
      "ds_read_b128 v[132:135], v0 offset:28672\n"
      "v_fmac_f32 v129, v110, v110\n"
      "v_mfma_f32_16x16x32_bf16 v[166:169], v[162:165], a[184:187], v[166:169]\n"
      "ds_read_b128 v[150:153], v1 offset:28672\n"
      "v_fmac_f32 v128, v177, v177\n"
      "v_mfma_f32_16x16x32_bf16 v[166:169], v[120:123], a[188:191], v[166:169]\n"
      "ds_read_b128 v[140:143], v0 offset:34816\n"
      "v_fmac_f32 v129, v111, v111\n"
      "v_mfma_f32_16x16x32_bf16 v[186:189], v[136:139], a[192:195], 0\n"
      "s_waitcnt lgkmcnt(3)\n"
      "v_fmac_f32 v128, v206, v206\n"
      "v_fmac_f32 v129, v100, v100\n"
      "v_mfma_f32_16x16x32_bf16 v[186:189], v[218:221], a[196:199], v[186:189]\n"
      "ds_read_b128 v[218:221], v1 offset:34816\n"
      "v_fmac_f32 v128, v207, v207\n"
      "v_mfma_f32_16x16x32_bf16 v[186:189], v[112:115], a[200:203], v[186:189]\n"
      "v_fmac_f32 v129, v101, v101\n"
      "v_fmac_f32 v128, v208, v208\n"
      "v_mfma_f32_16x16x32_bf16 v[186:189], v[178:181], a[204:207], v[186:189]\n"
      "s_waitcnt lgkmcnt(3)\n"
      "v_fmac_f32 v129, v102, v102\n"
      "v_fmac_f32 v128, v209, v209\n"
      "v_mfma_f32_16x16x32_bf16 v[186:189], v[162:165], a[208:211], v[186:189]\n"
      "buffer_store_dwordx4 v[144:147], v21, s[20:23], s46 offen sc1\n"
      "v_mfma_f32_16x16x32_bf16 v[186:189], v[120:123], a[212:215], v[186:189]\n"
      "s_waitcnt lgkmcnt(1)\n"
      "v_fmac_f32 v129, v103, v103\n"
      "v_fmac_f32_e64 v128, v124, v124\n"
      "v_mfma_f32_16x16x32_bf16 v[112:115], a[0:3], v[210:213], 0\n"
      "s_waitcnt lgkmcnt(0)\n"
      "v_fmac_f32 v129, v158, v158\n"
      "v_fmac_f32 v128, v125, v125\n"
      "v_mfma_f32_16x16x32_bf16 v[112:115], a[4:7], v[104:107], v[112:115]\n"
      "v_fmac_f32 v129, v159, v159\n"
      "v_fmac_f32 v128, v126, v126\n"
      "v_mfma_f32_16x16x32_bf16 v[112:115], a[8:11], v[132:135], v[112:115]\n"
      "v_fmac_f32 v129, v160, v160\n"
      "v_fmac_f32 v128, v127, v127\n"
      "v_mfma_f32_16x16x32_bf16 v[112:115], a[12:15], v[150:153], v[112:115]\n"
      "v_fmac_f32 v129, v161, v161\n"
      "v_cvt_pk_bf16_f32 v198, v198, v199\n"
      "v_mfma_f32_16x16x32_bf16 v[112:115], a[16:19], v[140:143], v[112:115]\n"
      "v_permlane16_swap_b32 v128, v129\n"
      "v_mfma_f32_16x16x32_bf16 v[112:115], a[20:23], v[218:221], v[112:115]\n"
      "v_add_f32 v128, v128, v129\n"
      "v_mov_b32 v129, v128\n"
      "v_mfma_f32_16x16x32_bf16 v[178:181], a[24:27], v[210:213], 0\n"
      "buffer_store_dwordx4 v[182:185], v22, s[20:23], s46 offen sc1\n"
      "v_mfma_f32_16x16x32_bf16 v[178:181], a[28:31], v[104:107], v[178:181]\n"
      "v_permlane32_swap_b32 v128, v129\n"
      "v_mfma_f32_16x16x32_bf16 v[178:181], a[32:35], v[132:135], v[178:181]\n"
      "v_add_f32 v128, v128, v129\n"
      "v_rsq_f32 v128, v128\n"
      "v_mfma_f32_16x16x32_bf16 v[178:181], a[36:39], v[150:153], v[178:181]\n"
      "v_mov_b32_e64 v129, v128\n"
      "v_fma_f32 v76, v112, v112, s51\n"
      "v_mfma_f32_16x16x32_bf16 v[178:181], a[40:43], v[140:143], v[178:181]\n"
      "v_permlane16_swap_b32 v128, v129\n"
      "v_mfma_f32_16x16x32_bf16 v[178:181], a[44:47], v[218:221], v[178:181]\n"
      "v_mul_f32 v128, s50, v128\n"
      "v_mul_f32 v108, v129, v108\n"
      "v_mfma_f32_16x16x32_bf16 v[120:123], a[48:51], v[210:213], 0\n"
      "v_mul_f32 v109, v129, v109\n"
      "v_mul_f32 v174, v128, v174\n"
      "v_mfma_f32_16x16x32_bf16 v[120:123], a[52:55], v[104:107], v[120:123]\n"
      "v_mul_f32 v175, v128, v175\n"
      "v_cvt_pk_bf16_f32 v108, v108, v109\n"
      "v_mfma_f32_16x16x32_bf16 v[120:123], a[56:59], v[132:135], v[120:123]\n"
      "buffer_store_dwordx4 v[202:205], v23, s[20:23], s46 offen sc1\n"
      "v_mfma_f32_16x16x32_bf16 v[120:123], a[60:63], v[150:153], v[120:123]\n"
      "v_mul_f32 v110, v129, v110\n"
      "v_mul_f32 v111, v129, v111\n"
      "v_mfma_f32_16x16x32_bf16 v[120:123], a[64:67], v[140:143], v[120:123]\n"
      "v_mul_f32 v158, v129, v158\n"
      "v_mul_f32 v159, v129, v159\n"
      "v_mfma_f32_16x16x32_bf16 v[120:123], a[68:71], v[218:221], v[120:123]\n"
      "v_cvt_pk_bf16_f32 v174, v174, v175\n"
      "v_mul_f32 v176, v128, v176\n"
      "v_mfma_f32_16x16x32_bf16 v[202:205], a[72:75], v[210:213], 0\n"
      "v_mul_f32 v177, v128, v177\n"
      "v_mul_f32 v124, v128, v124\n"
      "v_mfma_f32_16x16x32_bf16 v[202:205], a[76:79], v[104:107], v[202:205]\n"
      "ds_read_b128 v[144:147], v9 offset:4608\n"
      "v_mul_f32 v125, v128, v125\n"
      "v_mfma_f32_16x16x32_bf16 v[202:205], a[80:83], v[132:135], v[202:205]\n"
      "v_cvt_pk_bf16_f32 v109, v110, v111\n"
      "v_mul_f32 v100, v129, v100\n"
      "v_mfma_f32_16x16x32_bf16 v[202:205], a[84:87], v[150:153], v[202:205]\n"
      "v_mul_f32 v101, v129, v101\n"
      "v_mul_f32_e64 v102, v129, v102\n"
      "v_mfma_f32_16x16x32_bf16 v[202:205], a[88:91], v[140:143], v[202:205]\n"
      "buffer_store_dwordx4 v[96:99], v25, s[20:23], s46 offen sc1\n"
      "v_mfma_f32_16x16x32_bf16 v[202:205], a[92:95], v[218:221], v[202:205]\n"
      "v_mul_f32_e64 v103, v129, v103\n"
      "v_cvt_pk_bf16_f32 v158, v158, v159\n"
      "v_mfma_f32_16x16x32_bf16 v[162:165], a[96:99], v[210:213], 0\n"
      "v_mul_f32 v160, v129, v160\n"
      "v_mul_f32 v161, v129, v161\n"
      "v_mfma_f32_16x16x32_bf16 v[162:165], a[100:103], v[104:107], v[162:165]\n"
      "v_cvt_pk_bf16_f32 v175, v176, v177\n"
      "v_mul_f32 v206, v128, v206\n"
      "v_mfma_f32_16x16x32_bf16 v[162:165], a[104:107], v[132:135], v[162:165]\n"
      "v_mul_f32 v207, v128, v207\n"
      "v_mul_f32 v208, v128, v208\n"
      "v_mfma_f32_16x16x32_bf16 v[162:165], a[108:111], v[150:153], v[162:165]\n"
      "v_mul_f32 v209, v128, v209\n"
      "v_cvt_pk_bf16_f32 v124, v124, v125\n"
      "v_mfma_f32_16x16x32_bf16 v[162:165], a[112:115], v[140:143], v[162:165]\n"
      "v_mul_f32 v126, v128, v126\n"
      "v_mul_f32 v127, v128, v127\n"
      "v_mfma_f32_16x16x32_bf16 v[162:165], a[116:119], v[218:221], v[162:165]\n"
      "v_cvt_pk_bf16_f32 v110, v100, v101\n"
      "v_cvt_pk_bf16_f32 v111, v102, v103\n"
      "v_mfma_f32_16x16x32_bf16 v[136:139], a[120:123], v[210:213], 0\n"
      "v_cvt_pk_bf16_f32 v159, v160, v161\n"
      "v_cvt_pk_bf16_f32 v176, v206, v207\n"
      "v_mfma_f32_16x16x32_bf16 v[136:139], a[124:127], v[104:107], v[136:139]\n"
      "v_cvt_pk_bf16_f32 v177, v208, v209\n"
      "v_cvt_pk_bf16_f32 v125, v126, v127\n"
      "v_mfma_f32_16x16x32_bf16 v[136:139], a[128:131], v[132:135], v[136:139]\n"
      "v_fma_f32 v161, v202, v202, s51\n"
      "v_fmac_f32 v76, v113, v113\n"
      "v_mfma_f32_16x16x32_bf16 v[136:139], a[132:135], v[150:153], v[136:139]\n"
      "v_fmac_f32 v161, v203, v203\n"
      "v_fmac_f32 v76, v114, v114\n"
      "v_mfma_f32_16x16x32_bf16 v[136:139], a[136:139], v[140:143], v[136:139]\n"
      "v_fmac_f32 v161, v204, v204\n"
      "v_fmac_f32 v76, v115, v115\n"
      "v_mfma_f32_16x16x32_bf16 v[136:139], a[140:143], v[218:221], v[136:139]\n"
      "v_fmac_f32 v161, v205, v205\n"
      "v_fmac_f32 v76, v178, v178\n"
      "v_mfma_f32_16x16x32_bf16 v[100:103], v[210:213], a[144:147], 0\n"
      "v_fmac_f32 v161, v162, v162\n"
      "v_fmac_f32 v76, v179, v179\n"
      "v_mfma_f32_16x16x32_bf16 v[100:103], v[104:107], a[148:151], v[100:103]\n"
      "v_fmac_f32 v161, v163, v163\n"
      "v_fmac_f32 v76, v180, v180\n"
      "v_mfma_f32_16x16x32_bf16 v[100:103], v[132:135], a[152:155], v[100:103]\n"
      "v_fmac_f32 v161, v164, v164\n"
      "v_fmac_f32 v76, v181, v181\n"
      "v_mfma_f32_16x16x32_bf16 v[100:103], v[150:153], a[156:159], v[100:103]\n"
      "v_fmac_f32 v161, v165, v165\n"
      "v_fmac_f32 v76, v120, v120\n"
      "v_mfma_f32_16x16x32_bf16 v[100:103], v[140:143], a[160:163], v[100:103]\n"
      "v_fmac_f32 v161, v136, v136\n"
      "v_fmac_f32_e64 v76, v121, v121\n"
      "v_mfma_f32_16x16x32_bf16 v[100:103], v[218:221], a[164:167], v[100:103]\n"
      "ds_read_b128 v[126:129], v2\n"
      "v_fmac_f32 v161, v137, v137\n"
      "v_mfma_f32_16x16x32_bf16 v[206:209], v[210:213], a[168:171], 0\n"
      "ds_read_b128 v[96:99], v2 offset:3072\n"
      "v_fmac_f32 v76, v122, v122\n"
      "v_mfma_f32_16x16x32_bf16 v[206:209], v[104:107], a[172:175], v[206:209]\n"
      "v_fmac_f32 v161, v138, v138\n"
      "v_fmac_f32 v76, v123, v123\n"
      "v_mfma_f32_16x16x32_bf16 v[206:209], v[132:135], a[176:179], v[206:209]\n"
      "v_fmac_f32_e64 v161, v139, v139\n"
      "v_cvt_pk_bf16_f32 v199, v200, v201\n"
      "v_mfma_f32_16x16x32_bf16 v[206:209], v[150:153], a[180:183], v[206:209]\n"
      "v_permlane16_swap_b32 v76, v161\n"
      "v_mfma_f32_16x16x32_bf16 v[206:209], v[140:143], a[184:187], v[206:209]\n"
      "v_add_f32 v76, v76, v161\n"
      "v_mov_b32 v161, v76\n"
      "v_mfma_f32_16x16x32_bf16 v[206:209], v[218:221], a[188:191], v[206:209]\n"
      "s_waitcnt lgkmcnt(1)\n"
      "v_permlane32_swap_b32 v76, v161\n"
      "v_mfma_f32_16x16x32_bf16 v[182:185], v[210:213], a[192:195], 0\n"
      "s_waitcnt lgkmcnt(0)\n"
      "v_add_f32 v76, v76, v161\n"
      "v_rsq_f32 v76, v76\n"
      "v_mfma_f32_16x16x32_bf16 v[182:185], v[104:107], a[196:199], v[182:185]\n"
      "ds_read_b128 v[210:213], v2 offset:6144\n"
      "v_mov_b32_e64 v161, v76\n"
      "v_mfma_f32_16x16x32_bf16 v[182:185], v[132:135], a[200:203], v[182:185]\n"
      "ds_read_b128 v[132:135], v2 offset:9216\n"
      "v_permlane16_swap_b32_e64 v76, v161\n"
      "v_mfma_f32_16x16x32_bf16 v[182:185], v[150:153], a[204:207], v[182:185]\n"
      "ds_read_b128 v[150:153], v2 offset:12288\n"
      "v_mul_f32 v202, v161, v202\n"
      "v_mfma_f32_16x16x32_bf16 v[182:185], v[140:143], a[208:211], v[182:185]\n"
      "ds_read_b128 v[104:107], v2 offset:15360\n"
      "v_mul_f32 v203, v161, v203\n"
      "v_mfma_f32_16x16x32_bf16 v[182:185], v[218:221], a[212:215], v[182:185]\n"
      "v_cvt_pk_bf16_f32 v202, v202, v203\n"
      "v_mul_f32 v204, v161, v204\n"
      "v_mfma_f32_16x16x32_bf16 v[140:143], a[216:219], v[126:129], 0\n"
      "v_mul_f32 v205, v161, v205\n"
      "v_cvt_pk_bf16_f32 v203, v204, v205\n"
      "v_mfma_f32_16x16x32_bf16 v[140:143], a[220:223], v[96:99], v[140:143]\n"
      "s_waitcnt lgkmcnt(3)\n"
      "v_mul_f32 v162, v161, v162\n"
      "v_mul_f32_e64 v163, v161, v163\n"
      "v_mfma_f32_16x16x32_bf16 v[140:143], a[224:227], v[210:213], v[140:143]\n"
      "s_waitcnt lgkmcnt(2)\n"
      "v_mul_f32 v164, v161, v164\n"
      "v_mul_f32_e64 v165, v161, v165\n"
      "v_cvt_pk_bf16_f32 v204, v162, v163\n"
      "v_mfma_f32_16x16x32_bf16 v[220:223], a[240:243], v[126:129], 0\n"
      "s_waitcnt lgkmcnt(1)\n"
      "v_cvt_pk_bf16_f32 v205, v164, v165\n"
      "v_mul_f32 v136, v161, v136\n"
      "v_mfma_f32_16x16x32_bf16 v[220:223], a[244:247], v[96:99], v[220:223]\n"
      "s_waitcnt lgkmcnt(0)\n"
      "v_mul_f32 v137, v161, v137\n"
      "v_cvt_pk_bf16_f32 v136, v136, v137\n"
      "v_mfma_f32_16x16x32_bf16 v[220:223], a[248:251], v[210:213], v[220:223]\n"
      "v_mul_f32 v138, v161, v138\n"
      "v_mul_f32 v139, v161, v139\n"
      "v_mfma_f32_16x16x32_bf16 v[160:163], v[232:235], v[126:129], 0\n"
      "v_cvt_pk_bf16_f32 v137, v138, v139\n"
      "v_mul_f32 v76, s50, v76\n"
      "v_mfma_f32_16x16x32_bf16 v[160:163], v[236:239], v[96:99], v[160:163]\n"
      "v_mul_f32 v112, v76, v112\n"
      "v_mul_f32_e64 v113, v76, v113\n"
      "v_mfma_f32_16x16x32_bf16 v[160:163], v[240:243], v[210:213], v[160:163]\n"
      "ds_read_b128 v[210:213], v10 offset:1200\n"
      "v_cvt_pk_bf16_f32 v112, v112, v113\n"
      "v_mfma_f32_16x16x32_bf16 v[140:143], a[228:231], v[132:135], v[140:143]\n"
      "v_mul_f32 v114, v76, v114\n"
      "v_mul_f32 v115, v76, v115\n"
      "v_mfma_f32_16x16x32_bf16 v[140:143], a[232:235], v[150:153], v[140:143]\n"
      "v_mul_f32 v120, v76, v120\n"
      "v_mul_f32 v121, v76, v121\n"
      "v_mfma_f32_16x16x32_bf16 v[140:143], a[236:239], v[104:107], v[140:143]\n"
      "v_cvt_pk_bf16_f32 v113, v114, v115\n"
      "v_mul_f32_e64 v178, v76, v178\n"
      "v_mfma_f32_16x16x32_bf16 v[220:223], a[252:255], v[132:135], v[220:223]\n"
      "ds_read_b64 v[138:139], v10 offset:1192\n"
      "v_mul_f32 v179, v76, v179\n"
      "v_mfma_f32_16x16x32_bf16 v[220:223], v[224:227], v[150:153], v[220:223]\n"
      "ds_read_b128 v[126:129], v2 offset:256\n"
      "v_mul_f32 v180, v76, v180\n"
      "v_mfma_f32_16x16x32_bf16 v[220:223], v[228:231], v[104:107], v[220:223]\n"
      "s_waitcnt lgkmcnt(2)\n"
      "v_mul_f32 v181, v76, v181\n"
      "v_cvt_pk_bf16_f32 v120, v120, v121\n"
      "v_mfma_f32_16x16x32_bf16 v[160:163], v[244:247], v[132:135], v[160:163]\n"
      "ds_read_b128 v[96:99], v2 offset:3328\n"
      "v_mul_f32_e64 v122, v76, v122\n"
      "v_mfma_f32_16x16x32_bf16 v[160:163], v[248:251], v[150:153], v[160:163]\n"
      "ds_read_b128 v[132:135], v2 offset:6400\n"
      "v_mul_f32 v123, v76, v123\n"
      "v_mfma_f32_16x16x32_bf16 v[160:163], v[252:255], v[104:107], v[160:163]\n"
      "v_cvt_pk_bf16_f32 v114, v178, v179\n"
      "v_cvt_pk_bf16_f32 v115, v180, v181\n"
      "v_mfma_f32_16x16x32_bf16 v[178:181], v[116:119], v[170:173], v[40:43]\n"
      "s_waitcnt lgkmcnt(3)\n"
      "v_cvt_pk_bf16_f32 v121, v122, v123\n"
      "v_cvt_pk_bf16_f32 v200, v154, v155\n"
      "v_mfma_f32_16x16x32_bf16 v[150:153], v[108:111], v[170:173], v[44:47]\n"
      "s_waitcnt lgkmcnt(2)\n"
      "v_cvt_pk_bf16_f32 v201, v156, v157\n"
      "v_cvt_pk_bf16_f32 v76, v100, v101\n"
      "v_mfma_f32_16x16x32_bf16 v[154:157], v[202:205], v[170:173], v[48:51]\n"
      "v_cvt_pk_bf16_f32 v77, v102, v103\n"
      "v_cvt_pk_bf16_f32 v214, v214, v215\n"
      "v_mfma_f32_16x16x32_bf16 v[100:103], v[210:213], v[170:173], v[24:27]\n"
      "s_waitcnt lgkmcnt(1)\n"
      "v_cvt_pk_bf16_f32 v215, v216, v217\n"
      "v_cvt_pk_bf16_f32 v216, v166, v167\n"
      "v_mfma_f32_16x16x16_bf16 v[178:181], v[130:131], v[148:149], v[178:181]\n"
      "v_cvt_pk_bf16_f32 v217, v168, v169\n"
      "v_cvt_pk_bf16_f32 v80, v206, v207\n"
      "v_cvt_pk_bf16_f32 v81, v208, v209\n"
      "v_mfma_f32_16x16x16_bf16 v[150:153], v[158:159], v[148:149], v[150:153]\n"
      "v_cvt_pk_bf16_f32 v194, v194, v195\n"
      "v_cvt_pk_bf16_f32 v195, v196, v197\n"
      "v_cvt_pk_bf16_f32 v196, v186, v187\n"
      "v_mfma_f32_16x16x16_bf16 v[154:157], v[136:137], v[148:149], v[154:157]\n"
      "v_max3_f32 v123, v178, v179, v180\n"
      "v_cvt_pk_bf16_f32 v197, v188, v189\n"
      "v_cvt_pk_bf16_f32 v84, v182, v183\n"
      "v_mfma_f32_16x16x16_bf16 v[100:103], v[138:139], v[148:149], v[100:103]\n"
      "v_max3_f32 v183, v181, v150, v151\n"
      "v_max3_f32 v123, v123, v152, v153\n"
      "v_cvt_pk_bf16_f32 v85, v184, v185\n"
      "v_mfma_f32_16x16x32_bf16 v[186:189], a[216:219], v[126:129], 0\n"
      "s_waitcnt lgkmcnt(0)\n"
      "v_max3_f32 v183, v183, v154, v155\n"
      "v_max3_f32 v123, v123, v156, v157\n"
      "v_mfma_f32_16x16x32_bf16 v[186:189], a[220:223], v[96:99], v[186:189]\n"
      "v_max3_f32 v123, v123, v183, v100\n"
      "v_mov_b32 v106, v123\n"
      "v_mfma_f32_16x16x32_bf16 v[186:189], a[224:227], v[132:135], v[186:189]\n"
      "v_mul_f32 v173, v140, v140\n"
      "v_permlane16_swap_b32 v123, v106\n"
      "v_mfma_f32_16x16x32_bf16 v[182:185], a[240:243], v[126:129], 0\n"
      "v_max_f32 v123, v123, v106\n"
      "v_mov_b32 v106, v123\n"
      "v_mfma_f32_16x16x32_bf16 v[182:185], a[244:247], v[96:99], v[182:185]\n"
      "ds_read_b128 v[168:171], v2 offset:9472\n"
      "v_permlane32_swap_b32 v123, v106\n"
      "v_mfma_f32_16x16x32_bf16 v[182:185], a[248:251], v[132:135], v[182:185]\n"
      "ds_read_b128 v[164:167], v2 offset:12544\n"
      "v_max_f32 v123, v123, v106\n"
      "v_mfma_f32_16x16x32_bf16 v[104:107], v[232:235], v[126:129], 0\n"
      "ds_read_b128 v[126:129], v2 offset:15616\n"
      "v_sub_f32 v178, v178, v123\n"
      "v_mfma_f32_16x16x32_bf16 v[104:107], v[236:239], v[96:99], v[104:107]\n"
      "v_sub_f32 v179, v179, v123\n"
      "v_sub_f32 v180, v180, v123\n"
      "v_mfma_f32_16x16x32_bf16 v[104:107], v[240:243], v[132:135], v[104:107]\n"
      "v_sub_f32 v181, v181, v123\n"
      "v_exp_f32 v178, v178\n"
      "v_mfma_f32_16x16x32_bf16 v[132:135], v[116:119], v[174:177], v[52:55]\n"
      "v_exp_f32 v179, v179\n"
      "v_mfma_f32_16x16x32_bf16 v[206:209], v[108:111], v[174:177], v[56:59]\n"
      "s_waitcnt lgkmcnt(2)\n"
      "v_sub_f32 v150, v150, v123\n"
      "v_sub_f32 v151, v151, v123\n"
      "v_sub_f32 v152, v152, v123\n"
      "v_sub_f32 v153, v153, v123\n"
      "v_exp_f32 v180, v180\n"
      "v_exp_f32 v181, v181\n"
      "v_exp_f32 v150, v150\n"
      "v_exp_f32 v151, v151\n"
      "v_exp_f32 v152, v152\n"
      "v_exp_f32_e64 v153, v153\n"
      "v_cvt_pk_bf16_f32 v178, v178, v179\n"
      "v_cvt_pk_bf16_f32 v179, v180, v181\n"
      "v_cvt_pk_bf16_f32 v180, v150, v151\n"
      "v_cvt_pk_bf16_f32 v181, v152, v153\n"
      "v_mfma_f32_16x16x32_bf16 v[96:99], v[202:205], v[174:177], v[60:63]\n"
      "s_waitcnt lgkmcnt(0)\n"
      "v_sub_f32 v154, v154, v123\n"
      "v_sub_f32 v155, v155, v123\n"
      "v_mfma_f32_16x16x32_bf16 v[148:151], v[210:213], v[174:177], v[26:29]\n"
      "v_sub_f32 v156, v156, v123\n"
      "v_sub_f32_e64 v157, v157, v123\n"
      "v_mfma_f32_16x16x16_bf16 v[132:135], v[130:131], v[124:125], v[132:135]\n"
      "v_sub_f32 v100, v100, v123\n"
      "v_exp_f32 v154, v154\n"
      "v_mfma_f32_16x16x16_bf16 v[206:209], v[158:159], v[124:125], v[206:209]\n"
      "v_exp_f32 v155, v155\n"
      "v_exp_f32 v156, v156\n"
      "v_mfma_f32_16x16x16_bf16 v[96:99], v[136:137], v[124:125], v[96:99]\n"
      "v_exp_f32 v157, v157\n"
      "v_exp_f32 v100, v100\n"
      "v_mfma_f32_16x16x16_bf16 v[148:151], v[138:139], v[124:125], v[148:151]\n"
      "v_cvt_pk_bf16_f32 v88, v154, v155\n"
      "v_cvt_pk_bf16_f32 v89, v156, v157\n"
      "v_cvt_pk_bf16_f32 v90, v100, 0\n"
      "v_mfma_f32_16x16x32_bf16 v[186:189], a[228:231], v[168:171], v[186:189]\n"
      "ds_read_u16 v78, v11 offset:1168\n"
      "v_max3_f32 v152, v132, v133, v134\n"
      "v_mfma_f32_16x16x32_bf16 v[186:189], a[232:235], v[164:167], v[186:189]\n"
      "ds_read_u16 v82, v11 offset:1200\n"
      "v_max3_f32 v100, v135, v206, v207\n"
      "v_mfma_f32_16x16x32_bf16 v[186:189], a[236:239], v[126:129], v[186:189]\n"
      "v_max3_f32 v152, v152, v208, v209\n"
      "v_max3_f32 v100, v100, v96, v97\n"
      "v_mfma_f32_16x16x32_bf16 v[182:185], a[252:255], v[168:171], v[182:185]\n"
      "ds_read_u16 v86, v11 offset:1232\n"
      "v_max3_f32 v152, v152, v98, v99\n"
      "v_mfma_f32_16x16x32_bf16 v[182:185], v[224:227], v[164:167], v[182:185]\n"
      "v_max3_f32 v152, v152, v100, v148\n"
      "v_mov_b32 v157, v152\n"
      "v_mfma_f32_16x16x32_bf16 v[182:185], v[228:231], v[126:129], v[182:185]\n"
      "v_mul_f32 v100, v186, v186\n"
      "v_permlane16_swap_b32 v152, v157\n"
      "v_mfma_f32_16x16x32_bf16 v[104:107], v[244:247], v[168:171], v[104:107]\n"
      "s_waitcnt lgkmcnt(2)\n"
      "v_max_f32 v152, v152, v157\n"
      "v_mov_b32 v157, v152\n"
      "v_mfma_f32_16x16x32_bf16 v[104:107], v[248:251], v[164:167], v[104:107]\n"
      "s_waitcnt lgkmcnt(1)\n"
      "v_permlane32_swap_b32 v152, v157\n"
      "v_mfma_f32_16x16x32_bf16 v[104:107], v[252:255], v[126:129], v[104:107]\n"
      "v_max_f32 v152, v152, v157\n"
      "v_sub_f32 v132, v132, v152\n"
      "v_mfma_f32_16x16x32_bf16 v[126:129], v[32:35], v[178:181], 0\n"
      "s_waitcnt lgkmcnt(0)\n"
      "v_sub_f32 v133, v133, v152\n"
      "v_sub_f32_e64 v134, v134, v152\n"
      "v_mfma_f32_16x16x32_bf16 v[126:129], v[32:35], v[88:91], v[126:129]\n"
      "v_sub_f32 v135, v135, v152\n"
      "v_exp_f32 v132, v132\n"
      "v_mfma_f32_16x16x32_bf16 v[168:171], v[198:201], v[178:181], 0\n"
      "v_exp_f32_e64 v133, v133\n"
      "v_mfma_f32_16x16x32_bf16 v[168:171], v[76:79], v[88:91], v[168:171]\n"
      "ds_read_b128 v[154:157], v2 offset:512\n"
      "v_sub_f32 v206, v206, v152\n"
      "v_mfma_f32_16x16x32_bf16 v[122:125], v[214:217], v[178:181], 0\n"
      "v_sub_f32 v207, v207, v152\n"
      "v_sub_f32 v208, v208, v152\n"
      "v_mfma_f32_16x16x32_bf16 v[122:125], v[80:83], v[88:91], v[122:125]\n"
      "v_sub_f32 v209, v209, v152\n"
      "v_exp_f32 v134, v134\n"
      "v_mfma_f32_16x16x32_bf16 v[174:177], v[194:197], v[178:181], 0\n"
      "v_exp_f32 v135, v135\n"
      "v_mfma_f32_16x16x32_bf16 v[174:177], v[84:87], v[88:91], v[174:177]\n"
      "ds_read_b128 v[178:181], v2 offset:3584\n"
      "v_exp_f32 v206, v206\n"
      "v_exp_f32 v207, v207\n"
      "v_exp_f32 v208, v208\n"
      "v_exp_f32 v209, v209\n"
      "v_cvt_pk_bf16_f32 v132, v132, v133\n"
      "v_cvt_pk_bf16_f32 v133, v134, v135\n"
      "v_cvt_pk_bf16_f32 v134, v206, v207\n"
      "v_mfma_f32_16x16x32_bf16 v[164:167], v[116:119], v[112:115], v[64:67]\n"
      "s_waitcnt lgkmcnt(1)\n"
      "v_cvt_pk_bf16_f32 v135, v208, v209\n"
      "v_sub_f32 v96, v96, v152\n"
      "v_sub_f32 v97, v97, v152\n"
      "v_sub_f32 v98, v98, v152\n"
      "v_sub_f32 v99, v99, v152\n"
      "v_sub_f32 v148, v148, v152\n"
      "v_exp_f32 v96, v96\n"
      "v_exp_f32 v97, v97\n"
      "v_exp_f32 v98, v98\n"
      "v_exp_f32 v99, v99\n"
      "v_exp_f32_e64 v148, v148\n"
      "v_cvt_pk_bf16_f32 v92, v96, v97\n"
      "v_mfma_f32_16x16x32_bf16 v[150:153], v[108:111], v[112:115], v[68:71]\n"
      "s_waitcnt lgkmcnt(0)\n"
      "v_cvt_pk_bf16_f32 v93, v98, v99\n"
      "v_cvt_pk_bf16_f32 v94, v148, 0\n"
      "v_add_f32 v172, v140, v141\n"
      "v_fmac_f32 v173, v141, v141\n"
      "v_fmac_f32 v100, v187, v187\n"
      "v_add_f32 v172, v172, v142\n"
      "v_fmac_f32 v173, v142, v142\n"
      "v_fmac_f32 v100, v188, v188\n"
      "v_add_f32 v172, v172, v143\n"
      "v_fmac_f32 v173, v143, v143\n"
      "v_fmac_f32 v100, v189, v189\n"
      "v_add_f32 v172, v172, v220\n"
      "v_fmac_f32 v173, v220, v220\n"
      "v_fmac_f32 v100, v182, v182\n"
      "v_add_f32 v172, v172, v221\n"
      "v_fmac_f32 v173, v221, v221\n"
      "v_fmac_f32 v100, v183, v183\n"
      "v_add_f32 v172, v172, v222\n"
      "v_fmac_f32 v173, v222, v222\n"
      "v_fmac_f32 v100, v184, v184\n"
      "v_add_f32 v172, v172, v223\n"
      "v_fmac_f32 v173, v223, v223\n"
      "v_fmac_f32 v100, v185, v185\n"
      "v_add_f32 v172, v172, v160\n"
      "v_fmac_f32 v173, v160, v160\n"
      "v_fmac_f32 v100, v104, v104\n"
      "v_add_f32 v172, v172, v161\n"
      "v_fmac_f32 v173, v161, v161\n"
      "v_fmac_f32 v100, v105, v105\n"
      "v_add_f32 v172, v172, v162\n"
      "v_fmac_f32 v173, v162, v162\n"
      "v_fmac_f32 v100, v106, v106\n"
      "v_add_f32 v172, v172, v163\n"
      "v_fmac_f32 v173, v163, v163\n"
      "v_fmac_f32 v100, v107, v107\n"
      "v_rcp_f32 v126, v126\n"
      "v_permlane16_swap_b32 v172, v173\n"
      "v_mul_f32 v168, v126, v168\n"
      "v_mul_f32 v169, v126, v169\n"
      "v_cvt_pk_bf16_f32 v168, v168, v169\n"
      "v_mul_f32 v170, v126, v170\n"
      "v_mul_f32 v171, v126, v171\n"
      "v_add_f32 v172, v172, v173\n"
      "v_add_f32 v99, v186, v187\n"
      "v_add_f32 v99, v99, v188\n"
      "v_add_f32 v99, v99, v189\n"
      "v_add_f32 v99, v99, v182\n"
      "v_add_f32 v99, v99, v183\n"
      "v_add_f32 v99, v99, v184\n"
      "v_add_f32 v99, v99, v185\n"
      "v_add_f32 v99, v99, v104\n"
      "v_add_f32 v99, v99, v105\n"
      "v_add_f32 v99, v99, v106\n"
      "v_add_f32 v99, v99, v107\n"
      "v_cvt_pk_bf16_f32 v169, v170, v171\n"
      "v_mul_f32 v122, v126, v122\n"
      "v_permlane16_swap_b32 v99, v100\n"
      "v_add_f32 v99, v99, v100\n"
      "v_mul_f32 v123, v126, v123\n"
      "v_mul_f32 v124, v126, v124\n"
      "v_mul_f32 v125, v126, v125\n"
      "v_cvt_pk_bf16_f32 v170, v122, v123\n"
      "v_cvt_pk_bf16_f32 v171, v124, v125\n"
      "v_mfma_f32_16x16x32_bf16 v[122:125], v[202:205], v[112:115], v[72:75]\n"
      "ds_write_b128 v3, v[168:171] offset:18432\n"
      "v_mfma_f32_16x16x32_bf16 v[168:171], v[210:213], v[112:115], v[28:31]\n"
      "ds_read_b128 v[206:209], v2 offset:6656\n"
      "v_mul_f32 v174, v126, v174\n"
      "v_mfma_f32_16x16x16_bf16 v[164:167], v[130:131], v[120:121], v[164:167]\n"
      "v_mul_f32 v175, v126, v175\n"
      "v_cvt_pk_bf16_f32 v174, v174, v175\n"
      "v_mul_f32_e64 v176, v126, v176\n"
      "v_mfma_f32_16x16x16_bf16 v[150:153], v[158:159], v[120:121], v[150:153]\n"
      "v_mul_f32 v177, v126, v177\n"
      "v_permlane32_swap_b32 v172, v99\n"
      "v_mfma_f32_16x16x16_bf16 v[122:125], v[136:137], v[120:121], v[122:125]\n"
      "s_waitcnt vmcnt(6)\n"
      "v_or3_b32 v190, v190, v191, v192\n"
      "v_max3_f32 v191, v164, v165, v166\n"
      "v_cvt_pk_bf16_f32 v175, v176, v177\n"
      "v_mfma_f32_16x16x16_bf16 v[168:171], v[138:139], v[120:121], v[168:171]\n"
      "s_waitcnt vmcnt(5)\n"
      "v_or3_b32 v190, v190, v193, v39\n"
      "v_max3_f32 v126, v167, v150, v151\n"
      "v_max3_f32 v191, v191, v152, v153\n"
      "v_mfma_f32_16x16x32_bf16 v[100:103], a[216:219], v[154:157], 0\n"
      "ds_write_b64 v4, v[174:175] offset:18432\n"
      "v_mfma_f32_16x16x32_bf16 v[100:103], a[220:223], v[178:181], v[100:103]\n"
      "v_max3_f32 v126, v126, v122, v123\n"
      "v_max3_f32 v191, v191, v124, v125\n"
      "s_waitcnt lgkmcnt(1)\n"
      "v_mfma_f32_16x16x32_bf16 v[100:103], a[224:227], v[206:209], v[100:103]\n"
      "v_max3_f32 v191, v191, v126, v168\n"
      "v_mov_b32 v175, v191\n"
      "v_mfma_f32_16x16x32_bf16 v[126:129], a[240:243], v[154:157], 0\n"
      "v_add_f32 v172, v172, v99\n"
      "v_permlane16_swap_b32 v191, v175\n"
      "v_mfma_f32_16x16x32_bf16 v[126:129], a[244:247], v[178:181], v[126:129]\n"
      "ds_read_b128 v[112:115], v10 offset:1168\n"
      "v_max_f32 v191, v191, v175\n"
      "v_mfma_f32_16x16x32_bf16 v[126:129], a[248:251], v[206:209], v[126:129]\n"
      "ds_write_b32 v8, v172 offset:2048\n"
      "v_mfma_f32_16x16x32_bf16 v[96:99], v[232:235], v[154:157], 0\n"
      "v_mov_b32 v175, v191\n"
      "v_and_b32 v190, 0x7fff7fff, v190\n"
      "v_mfma_f32_16x16x32_bf16 v[96:99], v[236:239], v[178:181], v[96:99]\n"
      "ds_write_b32 v31, v190 offset:1024\n"
      "v_mfma_f32_16x16x32_bf16 v[96:99], v[240:243], v[206:209], v[96:99]\n"
      "ds_read_b64 v[88:89], v10 offset:1184\n"
      "v_permlane32_swap_b32 v191, v175\n"
      "v_mfma_f32_16x16x32_bf16 v[170:173], v[32:35], v[132:135], 0\n"
      "v_max_f32 v191, v191, v175\n"
      "v_sub_f32 v164, v164, v191\n"
      "v_mfma_f32_16x16x32_bf16 v[170:173], v[32:35], v[92:95], v[170:173]\n"
      "s_waitcnt lgkmcnt(3)\n"
      "v_sub_f32 v165, v165, v191\n"
      "v_sub_f32 v166, v166, v191\n"
      "v_mfma_f32_16x16x32_bf16 v[174:177], v[198:201], v[132:135], 0\n"
      "v_sub_f32 v167, v167, v191\n"
      "v_exp_f32 v164, v164\n"
      "v_mfma_f32_16x16x32_bf16 v[174:177], v[76:79], v[92:95], v[174:177]\n"
      "ds_read_b128 v[178:181], v2 offset:9728\n"
      "v_rcp_f32 v170, v170\n"
      "v_exp_f32 v165, v165\n"
      "v_sub_f32 v150, v150, v191\n"
      "v_sub_f32 v151, v151, v191\n"
      "v_sub_f32 v152, v152, v191\n"
      "v_sub_f32 v153, v153, v191\n"
      "v_exp_f32 v166, v166\n"
      "v_mul_f32 v174, v170, v174\n"
      "v_mul_f32 v175, v170, v175\n"
      "v_cvt_pk_bf16_f32 v174, v174, v175\n"
      "v_mul_f32 v176, v170, v176\n"
      "v_mul_f32_e64 v177, v170, v177\n"
      "v_cvt_pk_bf16_f32 v175, v176, v177\n"
      "v_exp_f32 v167, v167\n"
      "v_exp_f32 v150, v150\n"
      "v_exp_f32 v151, v151\n"
      "v_exp_f32 v152, v152\n"
      "v_exp_f32_e64 v153, v153\n"
      "v_cvt_pk_bf16_f32 v164, v164, v165\n"
      "v_cvt_pk_bf16_f32 v165, v166, v167\n"
      "v_cvt_pk_bf16_f32 v166, v150, v151\n"
      "v_mfma_f32_16x16x32_bf16 v[148:151], v[214:217], v[132:135], 0\n"
      "s_waitcnt lgkmcnt(0)\n"
      "v_cvt_pk_bf16_f32 v167, v152, v153\n"
      "v_sub_f32 v122, v122, v191\n"
      "v_mfma_f32_16x16x32_bf16 v[148:151], v[80:83], v[92:95], v[148:151]\n"
      "v_sub_f32 v123, v123, v191\n"
      "v_sub_f32 v124, v124, v191\n"
      "v_mfma_f32_16x16x32_bf16 v[206:209], v[194:197], v[132:135], 0\n"
      "v_sub_f32 v125, v125, v191\n"
      "v_sub_f32 v168, v168, v191\n"
      "v_mfma_f32_16x16x32_bf16 v[206:209], v[84:87], v[92:95], v[206:209]\n"
      "ds_read_b128 v[154:157], v2 offset:12800\n"
      "v_exp_f32 v122, v122\n"
      "v_mfma_f32_16x16x32_bf16 v[132:135], v[112:115], v[116:119], v[30:33]\n"
      "v_mul_f32 v148, v170, v148\n"
      "v_mul_f32 v149, v170, v149\n"
      "v_mfma_f32_16x16x32_bf16 v[118:121], v[112:115], v[108:111], v[30:33]\n"
      "v_mul_f32 v150, v170, v150\n"
      "v_mul_f32_e64 v151, v170, v151\n"
      "v_mfma_f32_16x16x32_bf16 v[108:111], v[112:115], v[202:205], v[30:33]\n"
      "v_cvt_pk_bf16_f32 v176, v148, v149\n"
      "ds_read_b128 v[202:205], v2 offset:15872\n"
      "v_mfma_f32_16x16x32_bf16 v[190:193], v[112:115], v[210:213], v[30:33]\n"
      "v_cvt_pk_bf16_f32 v177, v150, v151\n"
      "v_mul_f32 v206, v170, v206\n"
      "v_mfma_f32_16x16x16_bf16 v[132:135], v[88:89], v[130:131], v[132:135]\n"
      "v_mul_f32 v207, v170, v207\n"
      "v_cvt_pk_bf16_f32 v206, v206, v207\n"
      "v_mul_f32 v208, v170, v208\n"
      "v_mfma_f32_16x16x16_bf16 v[118:121], v[88:89], v[158:159], v[118:121]\n"
      "v_mul_f32 v209, v170, v209\n"
      "v_cvt_pk_bf16_f32 v207, v208, v209\n"
      "v_exp_f32 v123, v123\n"
      "v_mfma_f32_16x16x16_bf16 v[108:111], v[88:89], v[136:137], v[108:111]\n"
      "v_exp_f32 v124, v124\n"
      "v_exp_f32_e64 v125, v125\n"
      "v_mfma_f32_16x16x16_bf16 v[190:193], v[88:89], v[138:139], v[190:193]\n"
      "v_exp_f32 v168, v168\n"
      "v_cndmask_b32 v132, v132, v118, s[58:59]\n"
      "v_mfma_f32_16x16x32_bf16 v[100:103], a[228:231], v[178:181], v[100:103]\n"
      "s_waitcnt lgkmcnt(0)\n"
      "v_cvt_pk_bf16_f32 v88, v122, v123\n"
      "v_cndmask_b32 v132, v132, v108, s[60:61]\n"
      "v_mfma_f32_16x16x32_bf16 v[100:103], a[232:235], v[154:157], v[100:103]\n"
      "ds_write_b128 v3, v[174:177] offset:18688\n"
      "v_mfma_f32_16x16x32_bf16 v[100:103], a[236:239], v[202:205], v[100:103]\n"
      "v_cndmask_b32 v132, v132, v190, s[62:63]\n"
      "v_cvt_pk_bf16_f32 v89, v124, v125\n"
      "v_mfma_f32_16x16x32_bf16 v[126:129], a[252:255], v[178:181], v[126:129]\n"
      "v_max_f32_dpp v139, v132, v132 row_ror:8 row_mask:0xf bank_mask:0xf\n"
      "v_cvt_pk_bf16_f32 v90, v168, 0\n"
      "v_mfma_f32_16x16x32_bf16 v[126:129], v[224:227], v[154:157], v[126:129]\n"
      "ds_write_b64 v4, v[206:207] offset:18688\n"
      "v_mfma_f32_16x16x32_bf16 v[126:129], v[228:231], v[202:205], v[126:129]\n"
      "v_max_f32_dpp v139, v139, v139 row_ror:4 row_mask:0xf bank_mask:0xf\n"
      "v_mul_f32 v125, v100, v100\n"
      "v_mfma_f32_16x16x32_bf16 v[96:99], v[244:247], v[178:181], v[96:99]\n"
      "v_add_f32 v124, v100, v101\n"
      "v_fmac_f32_e64 v125, v101, v101\n"
      "v_mfma_f32_16x16x32_bf16 v[96:99], v[248:251], v[154:157], v[96:99]\n"
      "v_add_f32 v124, v124, v102\n"
      "v_fmac_f32 v125, v102, v102\n"
      "v_mfma_f32_16x16x32_bf16 v[96:99], v[252:255], v[202:205], v[96:99]\n"
      "v_max_f32_dpp v139, v139, v139 row_ror:2 row_mask:0xf bank_mask:0xf\n"
      "v_add_f32 v124, v124, v103\n"
      "v_mfma_f32_16x16x32_bf16 v[210:213], v[32:35], v[164:167], 0\n"
      "v_fmac_f32 v125, v103, v103\n"
      "v_add_f32 v124, v124, v126\n"
      "v_mfma_f32_16x16x32_bf16 v[210:213], v[32:35], v[88:91], v[210:213]\n"
      "v_fmac_f32 v125, v126, v126\n"
      "v_add_f32 v124, v124, v127\n"
      "v_mfma_f32_16x16x32_bf16 v[108:111], v[198:201], v[164:167], 0\n"
      "v_fmac_f32 v125, v127, v127\n"
      "v_max_f32_dpp v139, v139, v139 row_ror:1 row_mask:0xf bank_mask:0xf\n"
      "v_mfma_f32_16x16x32_bf16 v[108:111], v[76:79], v[88:91], v[108:111]\n"
      "v_add_f32 v124, v124, v128\n"
      "v_fmac_f32 v125, v128, v128\n"
      "v_mfma_f32_16x16x32_bf16 v[174:177], v[214:217], v[164:167], 0\n"
      "v_add_f32 v124, v124, v129\n"
      "v_fmac_f32 v125, v129, v129\n"
      "v_mfma_f32_16x16x32_bf16 v[174:177], v[80:83], v[88:91], v[174:177]\n"
      "v_add_f32 v124, v124, v96\n"
      "v_fmac_f32 v125, v96, v96\n"
      "v_mfma_f32_16x16x32_bf16 v[190:193], v[194:197], v[164:167], 0\n"
      "v_add_f32 v124, v124, v97\n"
      "v_fmac_f32 v125, v97, v97\n"
      "v_mfma_f32_16x16x32_bf16 v[190:193], v[84:87], v[88:91], v[190:193]\n"
      "v_max_f32_dpp v139, v139, v139 row_bcast:15 row_mask:0xa bank_mask:0xf\n"
      "v_add_f32 v124, v124, v98\n"
      "v_fmac_f32 v125, v98, v98\n"
      "v_add_f32 v124, v124, v99\n"
      "v_fmac_f32 v125, v99, v99\n"
      "v_rcp_f32_e64 v210, v210\n"
      "v_max_f32_dpp v139, v139, v139 row_bcast:31 row_mask:0xc bank_mask:0xf\n"
      "v_permlane16_swap_b32 v124, v125\n"
      "v_readlane_b32 s72, v139, 63\n"
      "v_mul_f32 v108, v210, v108\n"
      "v_mul_f32 v109, v210, v109\n"
      "v_add_f32 v124, v124, v125\n"
      "v_cvt_pk_bf16_f32 v108, v108, v109\n"
      "v_mul_f32 v110, v210, v110\n"
      "v_mul_f32 v111, v210, v111\n"
      "v_mov_b32 v125, v124\n"
      "v_cvt_pk_bf16_f32 v109, v110, v111\n"
      "v_mul_f32 v174, v210, v174\n"
      "v_mul_f32 v175, v210, v175\n"
      "v_mul_f32 v176, v210, v176\n"
      "v_mul_f32 v177, v210, v177\n"
      "v_subrev_f32 v132, s72, v132\n"
      "v_cvt_pk_bf16_f32 v110, v174, v175\n"
      "v_cvt_pk_bf16_f32 v111, v176, v177\n"
      "v_mul_f32 v190, v210, v190\n"
      "v_mul_f32 v191, v210, v191\n"
      "v_exp_f32 v132, v132\n"
      "ds_write_b128 v3, v[108:111] offset:18944\n"
      "v_cvt_pk_bf16_f32 v190, v190, v191\n"
      "v_mul_f32 v192, v210, v192\n"
      "v_mul_f32_e64 v193, v210, v193\n"
      "v_permlane32_swap_b32_e64 v124, v125\n"
      "v_cvt_pk_bf16_f32 v191, v192, v193\n"
      "v_cvt_pk_bf16_f32 v132, v132, v132\n"
      "v_add_f32_e64 v124, v124, v125\n"
      "ds_write_b64 v4, v[190:191] offset:18944\n"
      "v_readlane_b32 s45, v37, s43\n"
      "v_readlane_b32 s46, v37, s42\n"
      "v_or3_b32 v144, v144, v145, v146\n"
      "s_or_b32 s95, s45, s46\n"
      "s_or_b32 s95, s95, s48\n"
      "ds_write_b16 v13, v132\n"
      "ds_read_b128 v[210:213], v14\n"
      "ds_read_b128 v[136:139], v14 offset:64\n"
      "ds_write_b32 v8, v124 offset:2560\n"
      "s_cmp_ge_u32 s95, 0x40000000\n"
      "s_cselect_b32 s94, -1, -2\n"
      "s_cmp_gt_u32 s42, s44\n"
      "s_cselect_b32 s95, s94, s95\n"
      "s_add_u32 s42, s42, 1\n"
      "v_or_b32_e64 v144, v144, v147\n"
      "v_cmp_ne_u32 s[56:57], 0, v144\n"
      "v_readlane_b32 s47, v38, s43\n"
      "s_cmp_ge_u32 s95, 0x40000000\n"
      "s_cselect_b64 vcc, -1, s[56:57]\n"
      "s_and_b32 s47, s47, -4\n"
      "s_add_u32 s43, s43, 1\n"
      "v_add_u32 v10, 2336, v10\n"
      "v_add_u32 v11, 2336, v11\n"
      "s_waitcnt lgkmcnt(2)\n"
      "v_mfma_f32_16x16x32_bf16 v[108:111], v[210:213], v[32:35], 0\n"
      "s_waitcnt lgkmcnt(1)\n"
      "v_mfma_f32_16x16x32_bf16 v[108:111], v[136:139], v[32:35], v[108:111]\n"
      "s_waitcnt lgkmcnt(0)\n"
      "s_barrier\n"
      "s_cbranch_vccnz L_disp0_%=\n"
      "L_max0_%=:\n"
      "v_mfma_f32_16x16x32_bf16 v[174:177], v[210:213], v[198:201], 0\n"
      "ds_read_b128 v[198:201], v9 offset:2048\n"
      "ds_read_b128 v[122:125], v9 offset:3072\n"
      "v_mfma_f32_16x16x32_bf16 v[174:177], v[136:139], v[76:79], v[174:177]\n"
      "ds_read_b128 v[118:121], v0\n"
      "ds_read_b128 v[202:205], v1\n"
      "v_mfma_f32_16x16x32_bf16 v[190:193], v[210:213], v[214:217], 0\n"
      "ds_read_b128 v[178:181], v0 offset:6144\n"
      "s_add_u32 m0, s64, 18432\n"
      "v_mfma_f32_16x16x32_bf16 v[190:193], v[136:139], v[80:83], v[190:193]\n"
      "buffer_load_dwordx4 v16, s[16:19], s45 offen  lds\n"
      "v_mfma_f32_16x16x32_bf16 v[214:217], v[210:213], v[194:197], 0\n"
      "s_add_u32 m0, s64, 22528\n"
      "v_mfma_f32_16x16x32_bf16 v[214:217], v[136:139], v[84:87], v[214:217]\n"
      "v_mov_b32_dpp v108, v108 quad_perm:[0,0,0,0] row_mask:0xf bank_mask:0xf\n"
      "v_rcp_f32 v108, v108\n"
      "s_waitcnt lgkmcnt(4)\n"
      "v_add_f32 v198, v198, v200\n"
      "v_cndmask_b32 v174, v174, v190, s[58:59]\n"
      "s_waitcnt lgkmcnt(3)\n"
      "v_add_f32 v122, v122, v124\n"
      "s_waitcnt lgkmcnt(2)\n"
      "v_mfma_f32_16x16x32_bf16 v[164:167], a[0:3], v[118:121], 0\n"
      "buffer_load_dwordx4 v17, s[16:19], s45 offen  lds\n"
      "v_mfma_f32_16x16x32_bf16 v[112:115], a[24:27], v[118:121], 0\n"
      "s_add_u32 m0, s64, 26624\n"
      "v_cndmask_b32 v174, v174, v214, s[60:61]\n"
      "v_mul_f32 v174, v108, v174\n"
      "v_mfma_f32_16x16x32_bf16 v[108:111], a[48:51], v[118:121], 0\n"
      "s_waitcnt lgkmcnt(0)\n"
      "v_cvt_pk_bf16_f32 v174, v174, v174\n"
      "v_add_f32_e64 v199, v199, v201\n"
      "v_mfma_f32_16x16x32_bf16 v[210:213], a[72:75], v[118:121], 0\n"
      "ds_write_b16 v12, v174 offset:16\n"
      "v_mfma_f32_16x16x32_bf16 v[174:177], a[96:99], v[118:121], 0\n"
      "buffer_load_dwordx4 v18, s[16:19], s45 offen  lds\n"
      "v_mfma_f32_16x16x32_bf16 v[168:171], a[120:123], v[118:121], 0\n"
      "ds_read_b128 v[190:193], v1 offset:6144\n"
      "v_add_f32_e64 v123, v123, v125\n"
      "v_mfma_f32_16x16x32_bf16 v[164:167], a[4:7], v[202:205], v[164:167]\n"
      "ds_read_b128 v[136:139], v0 offset:12288\n"
      "v_add_f32 v198, v198, v122\n"
      "v_mfma_f32_16x16x32_bf16 v[164:167], a[8:11], v[178:181], v[164:167]\n"
      "ds_read_b128 v[152:155], v1 offset:12288\n"
      "v_add_f32 v199, v199, v123\n"
      "v_mfma_f32_16x16x32_bf16 v[112:115], a[28:31], v[202:205], v[112:115]\n"
      "s_add_u32 m0, s64, 30720\n"
      "v_fma_f32 v122, -v198, v198, s52\n"
      "v_fmac_f32 v122, 0x43400000, v199\n"
      "v_mfma_f32_16x16x32_bf16 v[112:115], a[32:35], v[178:181], v[112:115]\n"
      "buffer_load_dwordx4 v19, s[16:19], s45 offen  lds\n"
      "v_mfma_f32_16x16x32_bf16 v[108:111], a[52:55], v[202:205], v[108:111]\n"
      "s_add_u32 m0, s65, 34816\n"
      "v_max_f32 v122, s52, v122\n"
      "v_rsq_f32 v122, v122\n"
      "v_mfma_f32_16x16x32_bf16 v[108:111], a[56:59], v[178:181], v[108:111]\n"
      "s_waitcnt lgkmcnt(2)\n"
      "v_mul_f32 v199, 0x43400000, v122\n"
      "v_mul_f32 v201, -v198, v122\n"
      "v_mfma_f32_16x16x32_bf16 v[210:213], a[76:79], v[202:205], v[210:213]\n"
      "s_waitcnt lgkmcnt(1)\n"
      "v_mov_b32 v132, v199\n"
      "v_mov_b32 v133, v201\n"
      "v_mfma_f32_16x16x32_bf16 v[210:213], a[80:83], v[178:181], v[210:213]\n"
      "buffer_load_dwordx4 v15, s[16:19], s45 offen  lds\n"
      "v_mfma_f32_16x16x32_bf16 v[174:177], a[100:103], v[202:205], v[174:177]\n"
      "s_waitcnt lgkmcnt(0)\n"
      "v_permlane32_swap_b32 v199, v132\n"
      "v_mfma_f32_16x16x32_bf16 v[174:177], a[104:107], v[178:181], v[174:177]\n"
      "v_permlane32_swap_b32 v201, v133\n"
      "v_mfma_f32_16x16x32_bf16 v[168:171], a[124:127], v[202:205], v[168:171]\n"
      "v_mov_b32 v214, v199\n"
      "v_mov_b32_e64 v215, v201\n"
      "v_mfma_f32_16x16x32_bf16 v[168:171], a[128:131], v[178:181], v[168:171]\n"
      "buffer_load_dwordx4 v[144:147], v27, s[24:27], s47 offen\n"
      "v_mfma_f32_16x16x32_bf16 v[164:167], a[12:15], v[190:193], v[164:167]\n"
      "v_permlane16_swap_b32 v199, v214\n"
      "v_mfma_f32_16x16x32_bf16 v[164:167], a[16:19], v[136:139], v[164:167]\n"
      "v_permlane16_swap_b32 v201, v215\n"
      "v_mfma_f32_16x16x32_bf16 v[164:167], a[20:23], v[152:155], v[164:167]\n"
      "v_fma_f32 v140, v140, v199, v201\n"
      "v_fma_f32 v141, v141, v199, v201\n"
      "v_mfma_f32_16x16x32_bf16 v[112:115], a[36:39], v[190:193], v[112:115]\n"
      "buffer_load_dword v39, v29, s[24:27], s47 offen\n"
      "v_mfma_f32_16x16x32_bf16 v[112:115], a[40:43], v[136:139], v[112:115]\n"
      "v_cvt_pk_bf16_f32 v140, v140, v141\n"
      "v_fma_f32 v142, v142, v199, v201\n"
      "v_mfma_f32_16x16x32_bf16 v[112:115], a[44:47], v[152:155], v[112:115]\n"
      "v_fma_f32 v143, v143, v199, v201\n"
      "v_fma_f32 v160, v160, v199, v201\n"
      "v_mfma_f32_16x16x32_bf16 v[108:111], a[60:63], v[190:193], v[108:111]\n"
      "v_fma_f32 v161, v161, v199, v201\n"
      "v_cvt_pk_bf16_f32 v141, v142, v143\n"
      "v_mfma_f32_16x16x32_bf16 v[108:111], a[64:67], v[136:139], v[108:111]\n"
      "v_fma_f32 v220, v220, v199, v201\n"
      "v_fma_f32 v221, v221, v199, v201\n"
      "v_mfma_f32_16x16x32_bf16 v[108:111], a[68:71], v[152:155], v[108:111]\n"
      "v_fma_f32 v222, v222, v199, v201\n"
      "v_fma_f32 v223, v223, v199, v201\n"
      "v_mfma_f32_16x16x32_bf16 v[210:213], a[84:87], v[190:193], v[210:213]\n"
      "v_cvt_pk_bf16_f32 v160, v160, v161\n"
      "v_fma_f32 v162, v162, v199, v201\n"
      "v_mfma_f32_16x16x32_bf16 v[210:213], a[88:91], v[136:139], v[210:213]\n"
      "v_fma_f32 v163, v163, v199, v201\n"
      "v_cvt_pk_bf16_f32 v142, v220, v221\n"
      "v_mfma_f32_16x16x32_bf16 v[210:213], a[92:95], v[152:155], v[210:213]\n"
      "v_cvt_pk_bf16_f32 v143, v222, v223\n"
      "v_cvt_pk_bf16_f32 v161, v162, v163\n"
      "v_mfma_f32_16x16x32_bf16 v[174:177], a[108:111], v[190:193], v[174:177]\n"
      "ds_write_b128 v5, v[140:143]\n"
      "v_mfma_f32_16x16x32_bf16 v[174:177], a[112:115], v[136:139], v[174:177]\n"
      "v_fma_f32 v186, v186, v214, v215\n"
      "v_fma_f32 v187, v187, v214, v215\n"
      "v_mfma_f32_16x16x32_bf16 v[174:177], a[116:119], v[152:155], v[174:177]\n"
      "v_cvt_pk_bf16_f32 v186, v186, v187\n"
      "v_fma_f32 v188, v188, v214, v215\n"
      "v_mfma_f32_16x16x32_bf16 v[168:171], a[132:135], v[190:193], v[168:171]\n"
      "ds_write_b64 v6, v[160:161]\n"
      "v_mfma_f32_16x16x32_bf16 v[168:171], a[136:139], v[136:139], v[168:171]\n"
      "v_fma_f32 v189, v189, v214, v215\n"
      "v_fma_f32 v104, v104, v214, v215\n"
      "v_mfma_f32_16x16x32_bf16 v[168:171], a[140:143], v[152:155], v[168:171]\n"
      "v_fma_f32 v105, v105, v214, v215\n"
      "v_cvt_pk_bf16_f32 v187, v188, v189\n"
      "v_mfma_f32_16x16x32_bf16 v[198:201], v[118:121], a[144:147], 0\n"
      "v_fma_f32 v182, v182, v214, v215\n"
      "v_fma_f32 v183, v183, v214, v215\n"
      "v_mfma_f32_16x16x32_bf16 v[198:201], v[202:205], a[148:151], v[198:201]\n"
      "v_fma_f32 v184, v184, v214, v215\n"
      "v_fma_f32 v185, v185, v214, v215\n"
      "v_mfma_f32_16x16x32_bf16 v[198:201], v[178:181], a[152:155], v[198:201]\n"
      "v_cvt_pk_bf16_f32 v104, v104, v105\n"
      "v_fma_f32 v106, v106, v214, v215\n"
      "v_mfma_f32_16x16x32_bf16 v[198:201], v[190:193], a[156:159], v[198:201]\n"
      "v_fma_f32 v107, v107, v214, v215\n"
      "v_cvt_pk_bf16_f32 v188, v182, v183\n"
      "v_mfma_f32_16x16x32_bf16 v[198:201], v[136:139], a[160:163], v[198:201]\n"
      "v_cvt_pk_bf16_f32 v189, v184, v185\n"
      "v_cvt_pk_bf16_f32 v105, v106, v107\n"
      "v_mfma_f32_16x16x32_bf16 v[198:201], v[152:155], a[164:167], v[198:201]\n"
      "ds_write_b128 v5, v[186:189] offset:6144\n"
      "v_mfma_f32_16x16x32_bf16 v[214:217], v[118:121], a[168:171], 0\n"
      "ds_read_b128 v[206:209], v0 offset:2048\n"
      "v_fma_f32 v100, v100, v132, v133\n"
      "v_mfma_f32_16x16x32_bf16 v[214:217], v[202:205], a[172:175], v[214:217]\n"
      "ds_read_b128 v[140:143], v1 offset:2048\n"
      "v_fma_f32 v101, v101, v132, v133\n"
      "v_mfma_f32_16x16x32_bf16 v[214:217], v[178:181], a[176:179], v[214:217]\n"
      "ds_write_b64 v6, v[104:105] offset:6144\n"
      "v_mfma_f32_16x16x32_bf16 v[214:217], v[190:193], a[180:183], v[214:217]\n"
      "ds_read_b128 v[104:107], v0 offset:8192\n"
      "v_cvt_pk_bf16_f32 v100, v100, v101\n"
      "v_mfma_f32_16x16x32_bf16 v[214:217], v[136:139], a[184:187], v[214:217]\n"
      "ds_read_b128 v[122:125], v1 offset:8192\n"
      "v_fma_f32 v102, v102, v132, v133\n"
      "v_mfma_f32_16x16x32_bf16 v[214:217], v[152:155], a[188:191], v[214:217]\n"
      "ds_read_b128 v[148:151], v0 offset:14336\n"
      "v_fma_f32 v103, v103, v132, v133\n"
      "v_mfma_f32_16x16x32_bf16 v[194:197], v[118:121], a[192:195], 0\n"
      "ds_read_b128 v[156:159], v1 offset:14336\n"
      "v_fma_f32 v96, v96, v132, v133\n"
      "v_mfma_f32_16x16x32_bf16 v[194:197], v[202:205], a[196:199], v[194:197]\n"
      "s_waitcnt lgkmcnt(6)\n"
      "v_fma_f32 v97, v97, v132, v133\n"
      "v_cvt_pk_bf16_f32 v101, v102, v103\n"
      "v_mfma_f32_16x16x32_bf16 v[194:197], v[178:181], a[200:203], v[194:197]\n"
      "s_waitcnt lgkmcnt(5)\n"
      "v_fma_f32 v126, v126, v132, v133\n"
      "v_fma_f32 v127, v127, v132, v133\n"
      "v_mfma_f32_16x16x32_bf16 v[194:197], v[190:193], a[204:207], v[194:197]\n"
      "v_fma_f32 v128, v128, v132, v133\n"
      "v_fma_f32 v129, v129, v132, v133\n"
      "v_mfma_f32_16x16x32_bf16 v[194:197], v[136:139], a[208:211], v[194:197]\n"
      "s_waitcnt lgkmcnt(3)\n"
      "v_cvt_pk_bf16_f32 v96, v96, v97\n"
      "v_fma_f32 v98, v98, v132, v133\n"
      "v_mfma_f32_16x16x32_bf16 v[194:197], v[152:155], a[212:215], v[194:197]\n"
      "s_waitcnt lgkmcnt(2)\n"
      "v_fma_f32 v99, v99, v132, v133\n"
      "v_cvt_pk_bf16_f32 v102, v126, v127\n"
      "v_mfma_f32_16x16x32_bf16 v[178:181], a[0:3], v[206:209], 0\n"
      "s_waitcnt lgkmcnt(1)\n"
      "v_cvt_pk_bf16_f32 v103, v128, v129\n"
      "v_cvt_pk_bf16_f32 v97, v98, v99\n"
      "v_mfma_f32_16x16x32_bf16 v[178:181], a[4:7], v[140:143], v[178:181]\n"
      "ds_write_b128 v5, v[100:103] offset:12288\n"
      "v_mfma_f32_16x16x32_bf16 v[178:181], a[8:11], v[104:107], v[178:181]\n"
      "s_waitcnt lgkmcnt(1)\n"
      "v_fma_f32 v132, v164, v164, s51\n"
      "v_fma_f32 v102, v210, v210, s51\n"
      "v_mfma_f32_16x16x32_bf16 v[178:181], a[12:15], v[122:125], v[178:181]\n"
      "v_fmac_f32 v132, v165, v165\n"
      "v_fmac_f32 v102, v211, v211\n"
      "v_mfma_f32_16x16x32_bf16 v[178:181], a[16:19], v[148:151], v[178:181]\n"
      "ds_write_b64 v6, v[96:97] offset:12288\n"
      "v_mfma_f32_16x16x32_bf16 v[178:181], a[20:23], v[156:159], v[178:181]\n"
      "v_fmac_f32 v132, v166, v166\n"
      "v_fmac_f32 v102, v212, v212\n"
      "v_mfma_f32_16x16x32_bf16 v[202:205], a[24:27], v[206:209], 0\n"
      "v_fmac_f32 v132, v167, v167\n"
      "v_fmac_f32 v102, v213, v213\n"
      "v_mfma_f32_16x16x32_bf16 v[202:205], a[28:31], v[140:143], v[202:205]\n"
      "v_fmac_f32 v132, v112, v112\n"
      "v_fmac_f32 v102, v174, v174\n"
      "v_mfma_f32_16x16x32_bf16 v[202:205], a[32:35], v[104:107], v[202:205]\n"
      "v_fmac_f32 v132, v113, v113\n"
      "v_fmac_f32 v102, v175, v175\n"
      "v_mfma_f32_16x16x32_bf16 v[202:205], a[36:39], v[122:125], v[202:205]\n"
      "v_fmac_f32 v132, v114, v114\n"
      "v_fmac_f32 v102, v176, v176\n"
      "v_mfma_f32_16x16x32_bf16 v[202:205], a[40:43], v[148:151], v[202:205]\n"
      "v_fmac_f32 v132, v115, v115\n"
      "v_fmac_f32 v102, v177, v177\n"
      "v_mfma_f32_16x16x32_bf16 v[202:205], a[44:47], v[156:159], v[202:205]\n"
      "v_fmac_f32 v132, v108, v108\n"
      "v_fmac_f32 v102, v168, v168\n"
      "v_mfma_f32_16x16x32_bf16 v[126:129], a[48:51], v[206:209], 0\n"
      "v_fmac_f32 v132, v109, v109\n"
      "v_fmac_f32 v102, v169, v169\n"
      "v_mfma_f32_16x16x32_bf16 v[126:129], a[52:55], v[140:143], v[126:129]\n"
      "v_fmac_f32 v132, v110, v110\n"
      "v_fmac_f32 v102, v170, v170\n"
      "v_mfma_f32_16x16x32_bf16 v[126:129], a[56:59], v[104:107], v[126:129]\n"
      "v_fmac_f32 v132, v111, v111\n"
      "v_fmac_f32 v102, v171, v171\n"
      "v_mfma_f32_16x16x32_bf16 v[126:129], a[60:63], v[122:125], v[126:129]\n"
      "v_fma_f32 v222, v178, v178, s51\n"
      "v_permlane16_swap_b32 v132, v102\n"
      "v_mfma_f32_16x16x32_bf16 v[126:129], a[64:67], v[148:151], v[126:129]\n"
      "v_add_f32 v132, v132, v102\n"
      "v_mov_b32 v102, v132\n"
      "v_mfma_f32_16x16x32_bf16 v[126:129], a[68:71], v[156:159], v[126:129]\n"
      "v_fmac_f32 v222, v179, v179\n"
      "v_permlane32_swap_b32 v132, v102\n"
      "v_mfma_f32_16x16x32_bf16 v[152:155], a[72:75], v[206:209], 0\n"
      "v_add_f32 v132, v132, v102\n"
      "v_rsq_f32 v132, v132\n"
      "v_mfma_f32_16x16x32_bf16 v[152:155], a[76:79], v[140:143], v[152:155]\n"
      "v_mov_b32 v102, v132\n"
      "v_fmac_f32 v222, v180, v180\n"
      "v_mfma_f32_16x16x32_bf16 v[152:155], a[80:83], v[104:107], v[152:155]\n"
      "v_permlane16_swap_b32 v132, v102\n"
      "v_mfma_f32_16x16x32_bf16 v[152:155], a[84:87], v[122:125], v[152:155]\n"
      "v_mul_f32 v132, s50, v132\n"
      "v_mul_f32 v210, v102, v210\n"
      "v_mfma_f32_16x16x32_bf16 v[152:155], a[88:91], v[148:151], v[152:155]\n"
      "v_mul_f32 v211, v102, v211\n"
      "v_mul_f32 v164, v132, v164\n"
      "v_mfma_f32_16x16x32_bf16 v[152:155], a[92:95], v[156:159], v[152:155]\n"
      "v_mul_f32 v165, v132, v165\n"
      "v_cvt_pk_bf16_f32 v210, v210, v211\n"
      "v_mfma_f32_16x16x32_bf16 v[136:139], a[96:99], v[206:209], 0\n"
      "v_mul_f32 v212, v102, v212\n"
      "v_mul_f32 v213, v102, v213\n"
      "v_mfma_f32_16x16x32_bf16 v[136:139], a[100:103], v[140:143], v[136:139]\n"
      "v_mul_f32 v168, v102, v168\n"
      "v_mul_f32_e64 v169, v102, v169\n"
      "v_mfma_f32_16x16x32_bf16 v[136:139], a[104:107], v[104:107], v[136:139]\n"
      "v_cvt_pk_bf16_f32 v164, v164, v165\n"
      "v_mul_f32 v166, v132, v166\n"
      "v_mfma_f32_16x16x32_bf16 v[136:139], a[108:111], v[122:125], v[136:139]\n"
      "v_mul_f32 v167, v132, v167\n"
      "v_mul_f32_e64 v108, v132, v108\n"
      "v_mfma_f32_16x16x32_bf16 v[136:139], a[112:115], v[148:151], v[136:139]\n"
      "v_mul_f32 v109, v132, v109\n"
      "v_cvt_pk_bf16_f32 v211, v212, v213\n"
      "v_mfma_f32_16x16x32_bf16 v[136:139], a[116:119], v[156:159], v[136:139]\n"
      "v_mul_f32 v174, v102, v174\n"
      "v_mul_f32 v175, v102, v175\n"
      "v_mfma_f32_16x16x32_bf16 v[218:221], a[120:123], v[206:209], 0\n"
      "v_mul_f32 v176, v102, v176\n"
      "v_mul_f32_e64 v177, v102, v177\n"
      "v_mfma_f32_16x16x32_bf16 v[218:221], a[124:127], v[140:143], v[218:221]\n"
      "v_cvt_pk_bf16_f32 v168, v168, v169\n"
      "v_mul_f32 v170, v102, v170\n"
      "v_mfma_f32_16x16x32_bf16 v[218:221], a[128:131], v[104:107], v[218:221]\n"
      "v_mul_f32 v171, v102, v171\n"
      "v_cvt_pk_bf16_f32 v165, v166, v167\n"
      "v_mfma_f32_16x16x32_bf16 v[218:221], a[132:135], v[122:125], v[218:221]\n"
      "ds_read_b128 v[100:103], v7 offset:18432\n"
      "v_mul_f32 v112, v132, v112\n"
      "v_mfma_f32_16x16x32_bf16 v[218:221], a[136:139], v[148:151], v[218:221]\n"
      "v_mul_f32 v113, v132, v113\n"
      "v_mul_f32 v114, v132, v114\n"
      "v_mfma_f32_16x16x32_bf16 v[218:221], a[140:143], v[156:159], v[218:221]\n"
      "ds_read_b128 v[96:99], v7 offset:22528\n"
      "v_mul_f32 v115, v132, v115\n"
      "v_mfma_f32_16x16x32_bf16 v[190:193], v[206:209], a[144:147], 0\n"
      "ds_read_b128 v[160:163], v7 offset:26624\n"
      "v_cvt_pk_bf16_f32 v108, v108, v109\n"
      "v_mfma_f32_16x16x32_bf16 v[190:193], v[140:143], a[148:151], v[190:193]\n"
      "v_mul_f32 v110, v132, v110\n"
      "v_mul_f32 v111, v132, v111\n"
      "v_mfma_f32_16x16x32_bf16 v[190:193], v[104:107], a[152:155], v[190:193]\n"
      "ds_read_b128 v[130:133], v7 offset:30720\n"
      "v_cvt_pk_bf16_f32 v212, v174, v175\n"
      "v_mfma_f32_16x16x32_bf16 v[190:193], v[122:125], a[156:159], v[190:193]\n"
      "ds_read_b128 v[118:121], v7 offset:34816\n"
      "v_cvt_pk_bf16_f32 v213, v176, v177\n"
      "v_mfma_f32_16x16x32_bf16 v[190:193], v[148:151], a[160:163], v[190:193]\n"
      "v_cvt_pk_bf16_f32 v169, v170, v171\n"
      "v_cvt_pk_bf16_f32 v166, v112, v113\n"
      "v_mfma_f32_16x16x32_bf16 v[190:193], v[156:159], a[164:167], v[190:193]\n"
      "ds_read_b128 v[170:173], v0 offset:4096\n"
      "v_cvt_pk_bf16_f32 v167, v114, v115\n"
      "v_mfma_f32_16x16x32_bf16 v[182:185], v[206:209], a[168:171], 0\n"
      "ds_read_b128 v[114:117], v1 offset:4096\n"
      "v_cvt_pk_bf16_f32 v109, v110, v111\n"
      "v_mfma_f32_16x16x32_bf16 v[182:185], v[140:143], a[172:175], v[182:185]\n"
      "s_waitcnt lgkmcnt(4)\n"
      "v_fma_f32 v223, v152, v152, s51\n"
      "v_fmac_f32 v223, v153, v153\n"
      "v_mfma_f32_16x16x32_bf16 v[182:185], v[104:107], a[176:179], v[182:185]\n"
      "buffer_store_dwordx4 v[100:103], v20, s[20:23], s46 offen sc1\n"
      "v_mfma_f32_16x16x32_bf16 v[182:185], v[122:125], a[180:183], v[182:185]\n"
      "s_nop 0\n"
      "ds_read_b128 v[100:103], v0 offset:10240\n"
      "v_fmac_f32 v223, v154, v154\n"
      "v_mfma_f32_16x16x32_bf16 v[182:185], v[148:151], a[184:187], v[182:185]\n"
      "ds_read_b128 v[110:113], v1 offset:10240\n"
      "v_fmac_f32 v222, v181, v181\n"
      "v_mfma_f32_16x16x32_bf16 v[182:185], v[156:159], a[188:191], v[182:185]\n"
      "ds_read_b128 v[186:189], v0 offset:16384\n"
      "v_fmac_f32 v223, v155, v155\n"
      "v_mfma_f32_16x16x32_bf16 v[174:177], v[206:209], a[192:195], 0\n"
      "s_waitcnt lgkmcnt(3)\n"
      "v_fmac_f32 v222, v202, v202\n"
      "v_fmac_f32_e64 v223, v136, v136\n"
      "v_mfma_f32_16x16x32_bf16 v[174:177], v[140:143], a[196:199], v[174:177]\n"
      "ds_read_b128 v[140:143], v1 offset:16384\n"
      "v_fmac_f32 v222, v203, v203\n"
      "v_mfma_f32_16x16x32_bf16 v[174:177], v[104:107], a[200:203], v[174:177]\n"
      "v_fmac_f32 v223, v137, v137\n"
      "v_fmac_f32_e64 v222, v204, v204\n"
      "v_mfma_f32_16x16x32_bf16 v[174:177], v[122:125], a[204:207], v[174:177]\n"
      "s_waitcnt lgkmcnt(3)\n"
      "v_fmac_f32 v223, v138, v138\n"
      "v_fmac_f32_e64 v222, v205, v205\n"
      "v_mfma_f32_16x16x32_bf16 v[174:177], v[148:151], a[208:211], v[174:177]\n"
      "buffer_store_dwordx4 v[96:99], v21, s[20:23], s46 offen sc1\n"
      "v_mfma_f32_16x16x32_bf16 v[174:177], v[156:159], a[212:215], v[174:177]\n"
      "s_waitcnt lgkmcnt(1)\n"
      "v_fmac_f32 v223, v139, v139\n"
      "v_fmac_f32_e64 v222, v126, v126\n"
      "v_mfma_f32_16x16x32_bf16 v[148:151], a[0:3], v[170:173], 0\n"
      "s_waitcnt lgkmcnt(0)\n"
      "v_fmac_f32 v223, v218, v218\n"
      "v_fmac_f32 v222, v127, v127\n"
      "v_mfma_f32_16x16x32_bf16 v[148:151], a[4:7], v[114:117], v[148:151]\n"
      "v_fmac_f32 v223, v219, v219\n"
      "v_fmac_f32 v222, v128, v128\n"
      "v_mfma_f32_16x16x32_bf16 v[148:151], a[8:11], v[100:103], v[148:151]\n"
      "v_fmac_f32 v223, v220, v220\n"
      "v_fmac_f32 v222, v129, v129\n"
      "v_mfma_f32_16x16x32_bf16 v[148:151], a[12:15], v[110:113], v[148:151]\n"
      "v_fmac_f32 v223, v221, v221\n"
      "v_cvt_pk_bf16_f32 v198, v198, v199\n"
      "v_mfma_f32_16x16x32_bf16 v[148:151], a[16:19], v[186:189], v[148:151]\n"
      "v_permlane16_swap_b32 v222, v223\n"
      "v_mfma_f32_16x16x32_bf16 v[148:151], a[20:23], v[140:143], v[148:151]\n"
      "v_add_f32 v222, v222, v223\n"
      "v_mov_b32 v223, v222\n"
      "v_mfma_f32_16x16x32_bf16 v[96:99], a[24:27], v[170:173], 0\n"
      "buffer_store_dwordx4 v[160:163], v22, s[20:23], s46 offen sc1\n"
      "v_mfma_f32_16x16x32_bf16 v[96:99], a[28:31], v[114:117], v[96:99]\n"
      "v_permlane32_swap_b32 v222, v223\n"
      "v_mfma_f32_16x16x32_bf16 v[96:99], a[32:35], v[100:103], v[96:99]\n"
      "v_add_f32 v222, v222, v223\n"
      "v_rsq_f32 v222, v222\n"
      "v_mfma_f32_16x16x32_bf16 v[96:99], a[36:39], v[110:113], v[96:99]\n"
      "v_mov_b32_e64 v223, v222\n"
      "v_fma_f32 v76, v148, v148, s51\n"
      "v_mfma_f32_16x16x32_bf16 v[96:99], a[40:43], v[186:189], v[96:99]\n"
      "v_permlane16_swap_b32 v222, v223\n"
      "v_mfma_f32_16x16x32_bf16 v[96:99], a[44:47], v[140:143], v[96:99]\n"
      "v_mul_f32 v222, s50, v222\n"
      "v_mul_f32 v152, v223, v152\n"
      "v_mfma_f32_16x16x32_bf16 v[206:209], a[48:51], v[170:173], 0\n"
      "v_mul_f32 v153, v223, v153\n"
      "v_mul_f32 v178, v222, v178\n"
      "v_mfma_f32_16x16x32_bf16 v[206:209], a[52:55], v[114:117], v[206:209]\n"
      "v_mul_f32 v179, v222, v179\n"
      "v_cvt_pk_bf16_f32 v152, v152, v153\n"
      "v_mfma_f32_16x16x32_bf16 v[206:209], a[56:59], v[100:103], v[206:209]\n"
      "buffer_store_dwordx4 v[130:133], v23, s[20:23], s46 offen sc1\n"
      "v_mfma_f32_16x16x32_bf16 v[206:209], a[60:63], v[110:113], v[206:209]\n"
      "v_mul_f32 v154, v223, v154\n"
      "v_mul_f32 v155, v223, v155\n"
      "v_mfma_f32_16x16x32_bf16 v[206:209], a[64:67], v[186:189], v[206:209]\n"
      "v_mul_f32 v218, v223, v218\n"
      "v_mul_f32 v219, v223, v219\n"
      "v_mfma_f32_16x16x32_bf16 v[206:209], a[68:71], v[140:143], v[206:209]\n"
      "v_cvt_pk_bf16_f32 v178, v178, v179\n"
      "v_mul_f32 v180, v222, v180\n"
      "v_mfma_f32_16x16x32_bf16 v[122:125], a[72:75], v[170:173], 0\n"
      "v_mul_f32 v181, v222, v181\n"
      "v_mul_f32 v126, v222, v126\n"
      "v_mfma_f32_16x16x32_bf16 v[122:125], a[76:79], v[114:117], v[122:125]\n"
      "ds_read_b128 v[132:135], v9 offset:5632\n"
      "v_mul_f32 v127, v222, v127\n"
      "v_mfma_f32_16x16x32_bf16 v[122:125], a[80:83], v[100:103], v[122:125]\n"
      "v_cvt_pk_bf16_f32 v153, v154, v155\n"
      "v_mul_f32 v136, v223, v136\n"
      "v_mfma_f32_16x16x32_bf16 v[122:125], a[84:87], v[110:113], v[122:125]\n"
      "v_mul_f32 v137, v223, v137\n"
      "v_mul_f32_e64 v138, v223, v138\n"
      "v_mfma_f32_16x16x32_bf16 v[122:125], a[88:91], v[186:189], v[122:125]\n"
      "buffer_store_dwordx4 v[118:121], v25, s[20:23], s46 offen sc1\n"
      "v_mfma_f32_16x16x32_bf16 v[122:125], a[92:95], v[140:143], v[122:125]\n"
      "v_mul_f32_e64 v139, v223, v139\n"
      "v_cvt_pk_bf16_f32 v218, v218, v219\n"
      "v_mfma_f32_16x16x32_bf16 v[160:163], a[96:99], v[170:173], 0\n"
      "v_mul_f32 v220, v223, v220\n"
      "v_mul_f32 v221, v223, v221\n"
      "v_mfma_f32_16x16x32_bf16 v[160:163], a[100:103], v[114:117], v[160:163]\n"
      "v_cvt_pk_bf16_f32 v179, v180, v181\n"
      "v_mul_f32 v202, v222, v202\n"
      "v_mfma_f32_16x16x32_bf16 v[160:163], a[104:107], v[100:103], v[160:163]\n"
      "v_mul_f32 v203, v222, v203\n"
      "v_mul_f32 v204, v222, v204\n"
      "v_mfma_f32_16x16x32_bf16 v[160:163], a[108:111], v[110:113], v[160:163]\n"
      "v_mul_f32 v205, v222, v205\n"
      "v_cvt_pk_bf16_f32 v126, v126, v127\n"
      "v_mfma_f32_16x16x32_bf16 v[160:163], a[112:115], v[186:189], v[160:163]\n"
      "v_mul_f32 v128, v222, v128\n"
      "v_mul_f32 v129, v222, v129\n"
      "v_mfma_f32_16x16x32_bf16 v[160:163], a[116:119], v[140:143], v[160:163]\n"
      "v_cvt_pk_bf16_f32 v154, v136, v137\n"
      "v_cvt_pk_bf16_f32 v155, v138, v139\n"
      "v_mfma_f32_16x16x32_bf16 v[118:121], a[120:123], v[170:173], 0\n"
      "v_cvt_pk_bf16_f32 v219, v220, v221\n"
      "v_cvt_pk_bf16_f32 v180, v202, v203\n"
      "v_mfma_f32_16x16x32_bf16 v[118:121], a[124:127], v[114:117], v[118:121]\n"
      "v_cvt_pk_bf16_f32 v181, v204, v205\n"
      "v_cvt_pk_bf16_f32 v127, v128, v129\n"
      "v_mfma_f32_16x16x32_bf16 v[118:121], a[128:131], v[100:103], v[118:121]\n"
      "v_fma_f32 v77, v122, v122, s51\n"
      "v_fmac_f32 v76, v149, v149\n"
      "v_mfma_f32_16x16x32_bf16 v[118:121], a[132:135], v[110:113], v[118:121]\n"
      "v_fmac_f32 v77, v123, v123\n"
      "v_fmac_f32 v76, v150, v150\n"
      "v_mfma_f32_16x16x32_bf16 v[118:121], a[136:139], v[186:189], v[118:121]\n"
      "v_fmac_f32 v77, v124, v124\n"
      "v_fmac_f32 v76, v151, v151\n"
      "v_mfma_f32_16x16x32_bf16 v[118:121], a[140:143], v[140:143], v[118:121]\n"
      "v_fmac_f32 v77, v125, v125\n"
      "v_fmac_f32 v76, v96, v96\n"
      "v_mfma_f32_16x16x32_bf16 v[128:131], v[170:173], a[144:147], 0\n"
      "v_fmac_f32 v77, v160, v160\n"
      "v_fmac_f32 v76, v97, v97\n"
      "v_mfma_f32_16x16x32_bf16 v[128:131], v[114:117], a[148:151], v[128:131]\n"
      "v_fmac_f32 v77, v161, v161\n"
      "v_fmac_f32 v76, v98, v98\n"
      "v_mfma_f32_16x16x32_bf16 v[128:131], v[100:103], a[152:155], v[128:131]\n"
      "v_fmac_f32 v77, v162, v162\n"
      "v_fmac_f32 v76, v99, v99\n"
      "v_mfma_f32_16x16x32_bf16 v[128:131], v[110:113], a[156:159], v[128:131]\n"
      "v_fmac_f32 v77, v163, v163\n"
      "v_fmac_f32 v76, v206, v206\n"
      "v_mfma_f32_16x16x32_bf16 v[128:131], v[186:189], a[160:163], v[128:131]\n"
      "v_fmac_f32 v77, v118, v118\n"
      "v_fmac_f32_e64 v76, v207, v207\n"
      "v_mfma_f32_16x16x32_bf16 v[128:131], v[140:143], a[164:167], v[128:131]\n"
      "ds_read_b128 v[136:139], v2 offset:18432\n"
      "v_fmac_f32 v77, v119, v119\n"
      "v_mfma_f32_16x16x32_bf16 v[104:107], v[170:173], a[168:171], 0\n"
      "ds_read_b128 v[156:159], v2 offset:21504\n"
      "v_fmac_f32 v76, v208, v208\n"
      "v_mfma_f32_16x16x32_bf16 v[104:107], v[114:117], a[172:175], v[104:107]\n"
      "v_fmac_f32 v77, v120, v120\n"
      "v_fmac_f32 v76, v209, v209\n"
      "v_mfma_f32_16x16x32_bf16 v[104:107], v[100:103], a[176:179], v[104:107]\n"
      "v_fmac_f32_e64 v77, v121, v121\n"
      "v_cvt_pk_bf16_f32 v199, v200, v201\n"
      "v_mfma_f32_16x16x32_bf16 v[104:107], v[110:113], a[180:183], v[104:107]\n"
      "v_permlane16_swap_b32 v76, v77\n"
      "v_mfma_f32_16x16x32_bf16 v[104:107], v[186:189], a[184:187], v[104:107]\n"
      "v_add_f32 v76, v76, v77\n"
      "v_mov_b32 v77, v76\n"
      "v_mfma_f32_16x16x32_bf16 v[104:107], v[140:143], a[188:191], v[104:107]\n"
      "s_waitcnt lgkmcnt(1)\n"
      "v_permlane32_swap_b32 v76, v77\n"
      "v_mfma_f32_16x16x32_bf16 v[202:205], v[170:173], a[192:195], 0\n"
      "s_waitcnt lgkmcnt(0)\n"
      "v_add_f32 v76, v76, v77\n"
      "v_rsq_f32 v76, v76\n"
      "v_mfma_f32_16x16x32_bf16 v[202:205], v[114:117], a[196:199], v[202:205]\n"
      "ds_read_b128 v[170:173], v2 offset:24576\n"
      "v_mov_b32_e64 v77, v76\n"
      "v_mfma_f32_16x16x32_bf16 v[202:205], v[100:103], a[200:203], v[202:205]\n"
      "ds_read_b128 v[114:117], v2 offset:27648\n"
      "v_permlane16_swap_b32_e64 v76, v77\n"
      "v_mfma_f32_16x16x32_bf16 v[202:205], v[110:113], a[204:207], v[202:205]\n"
      "ds_read_b128 v[110:113], v2 offset:30720\n"
      "v_mul_f32 v122, v77, v122\n"
      "v_mfma_f32_16x16x32_bf16 v[202:205], v[186:189], a[208:211], v[202:205]\n"
      "ds_read_b128 v[186:189], v2 offset:33792\n"
      "v_mul_f32 v123, v77, v123\n"
      "v_mfma_f32_16x16x32_bf16 v[202:205], v[140:143], a[212:215], v[202:205]\n"
      "v_cvt_pk_bf16_f32 v122, v122, v123\n"
      "v_mul_f32 v124, v77, v124\n"
      "v_mfma_f32_16x16x32_bf16 v[140:143], a[216:219], v[136:139], 0\n"
      "v_mul_f32 v125, v77, v125\n"
      "v_cvt_pk_bf16_f32 v123, v124, v125\n"
      "v_mfma_f32_16x16x32_bf16 v[140:143], a[220:223], v[156:159], v[140:143]\n"
      "s_waitcnt lgkmcnt(3)\n"
      "v_mul_f32 v160, v77, v160\n"
      "v_mul_f32_e64 v161, v77, v161\n"
      "v_mfma_f32_16x16x32_bf16 v[140:143], a[224:227], v[170:173], v[140:143]\n"
      "s_waitcnt lgkmcnt(2)\n"
      "v_mul_f32 v162, v77, v162\n"
      "v_mul_f32_e64 v163, v77, v163\n"
      "v_cvt_pk_bf16_f32 v124, v160, v161\n"
      "v_mfma_f32_16x16x32_bf16 v[220:223], a[240:243], v[136:139], 0\n"
      "s_waitcnt lgkmcnt(1)\n"
      "v_cvt_pk_bf16_f32 v125, v162, v163\n"
      "v_mul_f32 v118, v77, v118\n"
      "v_mfma_f32_16x16x32_bf16 v[220:223], a[244:247], v[156:159], v[220:223]\n"
      "s_waitcnt lgkmcnt(0)\n"
      "v_mul_f32 v119, v77, v119\n"
      "v_cvt_pk_bf16_f32 v118, v118, v119\n"
      "v_mfma_f32_16x16x32_bf16 v[220:223], a[248:251], v[170:173], v[220:223]\n"
      "v_mul_f32 v120, v77, v120\n"
      "v_mul_f32 v121, v77, v121\n"
      "v_mfma_f32_16x16x32_bf16 v[160:163], v[232:235], v[136:139], 0\n"
      "v_cvt_pk_bf16_f32 v119, v120, v121\n"
      "v_mul_f32 v76, s50, v76\n"
      "v_mfma_f32_16x16x32_bf16 v[160:163], v[236:239], v[156:159], v[160:163]\n"
      "v_mul_f32 v148, v76, v148\n"
      "v_mul_f32_e64 v149, v76, v149\n"
      "v_mfma_f32_16x16x32_bf16 v[160:163], v[240:243], v[170:173], v[160:163]\n"
      "ds_read_b128 v[156:159], v10 offset:32\n"
      "v_cvt_pk_bf16_f32 v148, v148, v149\n"
      "v_mfma_f32_16x16x32_bf16 v[140:143], a[228:231], v[114:117], v[140:143]\n"
      "v_mul_f32 v150, v76, v150\n"
      "v_mul_f32 v151, v76, v151\n"
      "v_mfma_f32_16x16x32_bf16 v[140:143], a[232:235], v[110:113], v[140:143]\n"
      "v_mul_f32 v206, v76, v206\n"
      "v_mul_f32 v207, v76, v207\n"
      "v_mfma_f32_16x16x32_bf16 v[140:143], a[236:239], v[186:189], v[140:143]\n"
      "v_cvt_pk_bf16_f32 v149, v150, v151\n"
      "v_mul_f32_e64 v96, v76, v96\n"
      "v_mfma_f32_16x16x32_bf16 v[220:223], a[252:255], v[114:117], v[220:223]\n"
      "ds_read_b64 v[120:121], v10 offset:24\n"
      "v_mul_f32 v97, v76, v97\n"
      "v_mfma_f32_16x16x32_bf16 v[220:223], v[224:227], v[110:113], v[220:223]\n"
      "ds_read_b128 v[136:139], v2 offset:18688\n"
      "v_mul_f32 v98, v76, v98\n"
      "v_mfma_f32_16x16x32_bf16 v[220:223], v[228:231], v[186:189], v[220:223]\n"
      "s_waitcnt lgkmcnt(2)\n"
      "v_mul_f32 v99, v76, v99\n"
      "v_cvt_pk_bf16_f32 v206, v206, v207\n"
      "v_mfma_f32_16x16x32_bf16 v[160:163], v[244:247], v[114:117], v[160:163]\n"
      "ds_read_b128 v[170:173], v2 offset:21760\n"
      "v_mul_f32_e64 v208, v76, v208\n"
      "v_mfma_f32_16x16x32_bf16 v[160:163], v[248:251], v[110:113], v[160:163]\n"
      "ds_read_b128 v[100:103], v2 offset:24832\n"
      "v_mul_f32 v209, v76, v209\n"
      "v_mfma_f32_16x16x32_bf16 v[160:163], v[252:255], v[186:189], v[160:163]\n"
      "v_cvt_pk_bf16_f32 v150, v96, v97\n"
      "v_cvt_pk_bf16_f32 v151, v98, v99\n"
      "v_mfma_f32_16x16x32_bf16 v[114:117], v[210:213], v[164:167], v[40:43]\n"
      "s_waitcnt lgkmcnt(3)\n"
      "v_cvt_pk_bf16_f32 v207, v208, v209\n"
      "v_cvt_pk_bf16_f32 v200, v190, v191\n"
      "v_mfma_f32_16x16x32_bf16 v[110:113], v[152:155], v[164:167], v[44:47]\n"
      "s_waitcnt lgkmcnt(2)\n"
      "v_cvt_pk_bf16_f32 v201, v192, v193\n"
      "v_cvt_pk_bf16_f32 v76, v128, v129\n"
      "v_mfma_f32_16x16x32_bf16 v[96:99], v[122:125], v[164:167], v[48:51]\n"
      "v_cvt_pk_bf16_f32 v77, v130, v131\n"
      "v_cvt_pk_bf16_f32 v214, v214, v215\n"
      "v_mfma_f32_16x16x32_bf16 v[190:193], v[156:159], v[164:167], v[24:27]\n"
      "s_waitcnt lgkmcnt(1)\n"
      "v_cvt_pk_bf16_f32 v215, v216, v217\n"
      "v_cvt_pk_bf16_f32 v216, v182, v183\n"
      "v_mfma_f32_16x16x16_bf16 v[114:117], v[168:169], v[108:109], v[114:117]\n"
      "v_cvt_pk_bf16_f32 v217, v184, v185\n"
      "v_cvt_pk_bf16_f32 v80, v104, v105\n"
      "v_cvt_pk_bf16_f32 v81, v106, v107\n"
      "v_mfma_f32_16x16x16_bf16 v[110:113], v[218:219], v[108:109], v[110:113]\n"
      "v_cvt_pk_bf16_f32 v194, v194, v195\n"
      "v_cvt_pk_bf16_f32 v195, v196, v197\n"
      "v_cvt_pk_bf16_f32 v196, v174, v175\n"
      "v_mfma_f32_16x16x16_bf16 v[96:99], v[118:119], v[108:109], v[96:99]\n"
      "v_max3_f32 v208, v114, v115, v116\n"
      "v_cvt_pk_bf16_f32 v197, v176, v177\n"
      "v_cvt_pk_bf16_f32 v84, v202, v203\n"
      "v_mfma_f32_16x16x16_bf16 v[190:193], v[120:121], v[108:109], v[190:193]\n"
      "v_max3_f32 v184, v117, v110, v111\n"
      "v_max3_f32 v208, v208, v112, v113\n"
      "v_cvt_pk_bf16_f32 v85, v204, v205\n"
      "v_mfma_f32_16x16x32_bf16 v[186:189], a[216:219], v[136:139], 0\n"
      "s_waitcnt lgkmcnt(0)\n"
      "v_max3_f32 v184, v184, v96, v97\n"
      "v_max3_f32 v208, v208, v98, v99\n"
      "v_mfma_f32_16x16x32_bf16 v[186:189], a[220:223], v[170:173], v[186:189]\n"
      "v_max3_f32 v208, v208, v184, v190\n"
      "v_mov_b32 v107, v208\n"
      "v_mfma_f32_16x16x32_bf16 v[186:189], a[224:227], v[100:103], v[186:189]\n"
      "v_mul_f32 v209, v140, v140\n"
      "v_permlane16_swap_b32 v208, v107\n"
      "v_mfma_f32_16x16x32_bf16 v[182:185], a[240:243], v[136:139], 0\n"
      "v_max_f32 v208, v208, v107\n"
      "v_mov_b32 v107, v208\n"
      "v_mfma_f32_16x16x32_bf16 v[182:185], a[244:247], v[170:173], v[182:185]\n"
      "ds_read_b128 v[174:177], v2 offset:27904\n"
      "v_permlane32_swap_b32 v208, v107\n"
      "v_mfma_f32_16x16x32_bf16 v[182:185], a[248:251], v[100:103], v[182:185]\n"
      "ds_read_b128 v[128:131], v2 offset:30976\n"
      "v_max_f32 v208, v208, v107\n"
      "v_mfma_f32_16x16x32_bf16 v[104:107], v[232:235], v[136:139], 0\n"
      "ds_read_b128 v[136:139], v2 offset:34048\n"
      "v_sub_f32 v114, v114, v208\n"
      "v_mfma_f32_16x16x32_bf16 v[104:107], v[236:239], v[170:173], v[104:107]\n"
      "v_sub_f32 v115, v115, v208\n"
      "v_sub_f32 v116, v116, v208\n"
      "v_mfma_f32_16x16x32_bf16 v[104:107], v[240:243], v[100:103], v[104:107]\n"
      "v_sub_f32 v117, v117, v208\n"
      "v_exp_f32 v114, v114\n"
      "v_mfma_f32_16x16x32_bf16 v[164:167], v[210:213], v[178:181], v[52:55]\n"
      "v_exp_f32 v115, v115\n"
      "v_mfma_f32_16x16x32_bf16 v[100:103], v[152:155], v[178:181], v[56:59]\n"
      "s_waitcnt lgkmcnt(2)\n"
      "v_sub_f32 v110, v110, v208\n"
      "v_sub_f32 v111, v111, v208\n"
      "v_sub_f32 v112, v112, v208\n"
      "v_sub_f32 v113, v113, v208\n"
      "v_exp_f32 v116, v116\n"
      "v_exp_f32 v117, v117\n"
      "v_exp_f32 v110, v110\n"
      "v_exp_f32 v111, v111\n"
      "v_exp_f32 v112, v112\n"
      "v_exp_f32_e64 v113, v113\n"
      "v_cvt_pk_bf16_f32 v114, v114, v115\n"
      "v_cvt_pk_bf16_f32 v115, v116, v117\n"
      "v_cvt_pk_bf16_f32 v116, v110, v111\n"
      "v_cvt_pk_bf16_f32 v117, v112, v113\n"
      "v_mfma_f32_16x16x32_bf16 v[108:111], v[122:125], v[178:181], v[60:63]\n"
      "s_waitcnt lgkmcnt(0)\n"
      "v_sub_f32 v96, v96, v208\n"
      "v_sub_f32 v97, v97, v208\n"
      "v_mfma_f32_16x16x32_bf16 v[170:173], v[156:159], v[178:181], v[26:29]\n"
      "v_sub_f32 v98, v98, v208\n"
      "v_sub_f32_e64 v99, v99, v208\n"
      "v_mfma_f32_16x16x16_bf16 v[164:167], v[168:169], v[126:127], v[164:167]\n"
      "v_sub_f32 v190, v190, v208\n"
      "v_exp_f32 v96, v96\n"
      "v_mfma_f32_16x16x16_bf16 v[100:103], v[218:219], v[126:127], v[100:103]\n"
      "v_exp_f32 v97, v97\n"
      "v_exp_f32 v98, v98\n"
      "v_mfma_f32_16x16x16_bf16 v[108:111], v[118:119], v[126:127], v[108:111]\n"
      "v_exp_f32 v99, v99\n"
      "v_exp_f32 v190, v190\n"
      "v_mfma_f32_16x16x16_bf16 v[170:173], v[120:121], v[126:127], v[170:173]\n"
      "v_cvt_pk_bf16_f32 v88, v96, v97\n"
      "v_cvt_pk_bf16_f32 v89, v98, v99\n"
      "v_cvt_pk_bf16_f32 v90, v190, 0\n"
      "v_mfma_f32_16x16x32_bf16 v[186:189], a[228:231], v[174:177], v[186:189]\n"
      "ds_read_u16 v78, v11\n"
      "v_max3_f32 v208, v164, v165, v166\n"
      "v_mfma_f32_16x16x32_bf16 v[186:189], a[232:235], v[128:131], v[186:189]\n"
      "ds_read_u16 v82, v11 offset:32\n"
      "v_max3_f32 v171, v167, v100, v101\n"
      "v_mfma_f32_16x16x32_bf16 v[186:189], a[236:239], v[136:139], v[186:189]\n"
      "v_max3_f32 v208, v208, v102, v103\n"
      "v_max3_f32 v171, v171, v108, v109\n"
      "v_mfma_f32_16x16x32_bf16 v[182:185], a[252:255], v[174:177], v[182:185]\n"
      "ds_read_u16 v86, v11 offset:64\n"
      "v_max3_f32 v208, v208, v110, v111\n"
      "v_mfma_f32_16x16x32_bf16 v[182:185], v[224:227], v[128:131], v[182:185]\n"
      "v_max3_f32 v208, v208, v171, v170\n"
      "v_mov_b32 v127, v208\n"
      "v_mfma_f32_16x16x32_bf16 v[182:185], v[228:231], v[136:139], v[182:185]\n"
      "v_mul_f32 v171, v186, v186\n"
      "v_permlane16_swap_b32 v208, v127\n"
      "v_mfma_f32_16x16x32_bf16 v[104:107], v[244:247], v[174:177], v[104:107]\n"
      "s_waitcnt lgkmcnt(2)\n"
      "v_max_f32 v208, v208, v127\n"
      "v_mov_b32 v127, v208\n"
      "v_mfma_f32_16x16x32_bf16 v[104:107], v[248:251], v[128:131], v[104:107]\n"
      "s_waitcnt lgkmcnt(1)\n"
      "v_permlane32_swap_b32 v208, v127\n"
      "v_mfma_f32_16x16x32_bf16 v[104:107], v[252:255], v[136:139], v[104:107]\n"
      "v_max_f32 v208, v208, v127\n"
      "v_sub_f32 v164, v164, v208\n"
      "v_mfma_f32_16x16x32_bf16 v[126:129], v[32:35], v[114:117], 0\n"
      "s_waitcnt lgkmcnt(0)\n"
      "v_sub_f32 v165, v165, v208\n"
      "v_sub_f32_e64 v166, v166, v208\n"
      "v_mfma_f32_16x16x32_bf16 v[126:129], v[32:35], v[88:91], v[126:129]\n"
      "v_sub_f32 v167, v167, v208\n"
      "v_exp_f32 v164, v164\n"
      "v_mfma_f32_16x16x32_bf16 v[96:99], v[198:201], v[114:117], 0\n"
      "v_exp_f32_e64 v165, v165\n"
      "v_mfma_f32_16x16x32_bf16 v[96:99], v[76:79], v[88:91], v[96:99]\n"
      "ds_read_b128 v[136:139], v2 offset:18944\n"
      "v_sub_f32 v100, v100, v208\n"
      "v_mfma_f32_16x16x32_bf16 v[174:177], v[214:217], v[114:117], 0\n"
      "v_sub_f32 v101, v101, v208\n"
      "v_sub_f32 v102, v102, v208\n"
      "v_mfma_f32_16x16x32_bf16 v[174:177], v[80:83], v[88:91], v[174:177]\n"
      "v_sub_f32 v103, v103, v208\n"
      "v_exp_f32 v166, v166\n"
      "v_mfma_f32_16x16x32_bf16 v[190:193], v[194:197], v[114:117], 0\n"
      "v_exp_f32 v167, v167\n"
      "v_mfma_f32_16x16x32_bf16 v[190:193], v[84:87], v[88:91], v[190:193]\n"
      "ds_read_b128 v[178:181], v2 offset:22016\n"
      "v_exp_f32 v100, v100\n"
      "v_exp_f32 v101, v101\n"
      "v_exp_f32 v102, v102\n"
      "v_exp_f32 v103, v103\n"
      "v_cvt_pk_bf16_f32 v164, v164, v165\n"
      "v_cvt_pk_bf16_f32 v165, v166, v167\n"
      "v_cvt_pk_bf16_f32 v166, v100, v101\n"
      "v_mfma_f32_16x16x32_bf16 v[112:115], v[210:213], v[148:151], v[64:67]\n"
      "s_waitcnt lgkmcnt(1)\n"
      "v_cvt_pk_bf16_f32 v167, v102, v103\n"
      "v_sub_f32 v108, v108, v208\n"
      "v_sub_f32 v109, v109, v208\n"
      "v_sub_f32 v110, v110, v208\n"
      "v_sub_f32 v111, v111, v208\n"
      "v_sub_f32 v170, v170, v208\n"
      "v_exp_f32 v108, v108\n"
      "v_exp_f32 v109, v109\n"
      "v_exp_f32 v110, v110\n"
      "v_exp_f32 v111, v111\n"
      "v_exp_f32_e64 v170, v170\n"
      "v_cvt_pk_bf16_f32 v92, v108, v109\n"
      "v_mfma_f32_16x16x32_bf16 v[202:205], v[152:155], v[148:151], v[68:71]\n"
      "s_waitcnt lgkmcnt(0)\n"
      "v_cvt_pk_bf16_f32 v93, v110, v111\n"
      "v_cvt_pk_bf16_f32 v94, v170, 0\n"
      "v_add_f32 v208, v140, v141\n"
      "v_fmac_f32 v209, v141, v141\n"
      "v_fmac_f32 v171, v187, v187\n"
      "v_add_f32 v208, v208, v142\n"
      "v_fmac_f32 v209, v142, v142\n"
      "v_fmac_f32 v171, v188, v188\n"
      "v_add_f32 v208, v208, v143\n"
      "v_fmac_f32 v209, v143, v143\n"
      "v_fmac_f32 v171, v189, v189\n"
      "v_add_f32 v208, v208, v220\n"
      "v_fmac_f32 v209, v220, v220\n"
      "v_fmac_f32 v171, v182, v182\n"
      "v_add_f32 v208, v208, v221\n"
      "v_fmac_f32 v209, v221, v221\n"
      "v_fmac_f32 v171, v183, v183\n"
      "v_add_f32 v208, v208, v222\n"
      "v_fmac_f32 v209, v222, v222\n"
      "v_fmac_f32 v171, v184, v184\n"
      "v_add_f32 v208, v208, v223\n"
      "v_fmac_f32 v209, v223, v223\n"
      "v_fmac_f32 v171, v185, v185\n"
      "v_add_f32 v208, v208, v160\n"
      "v_fmac_f32 v209, v160, v160\n"
      "v_fmac_f32 v171, v104, v104\n"
      "v_add_f32 v208, v208, v161\n"
      "v_fmac_f32 v209, v161, v161\n"
      "v_fmac_f32 v171, v105, v105\n"
      "v_add_f32 v208, v208, v162\n"
      "v_fmac_f32 v209, v162, v162\n"
      "v_fmac_f32 v171, v106, v106\n"
      "v_add_f32 v208, v208, v163\n"
      "v_fmac_f32 v209, v163, v163\n"
      "v_fmac_f32 v171, v107, v107\n"
      "v_rcp_f32 v126, v126\n"
      "v_permlane16_swap_b32 v208, v209\n"
      "v_mul_f32 v96, v126, v96\n"
      "v_mul_f32 v97, v126, v97\n"
      "v_cvt_pk_bf16_f32 v96, v96, v97\n"
      "v_mul_f32 v98, v126, v98\n"
      "v_mul_f32 v99, v126, v99\n"
      "v_add_f32 v208, v208, v209\n"
      "v_add_f32 v170, v186, v187\n"
      "v_add_f32 v170, v170, v188\n"
      "v_add_f32 v170, v170, v189\n"
      "v_add_f32 v170, v170, v182\n"
      "v_add_f32 v170, v170, v183\n"
      "v_add_f32 v170, v170, v184\n"
      "v_add_f32 v170, v170, v185\n"
      "v_add_f32 v170, v170, v104\n"
      "v_add_f32 v170, v170, v105\n"
      "v_add_f32 v170, v170, v106\n"
      "v_add_f32 v170, v170, v107\n"
      "v_cvt_pk_bf16_f32 v97, v98, v99\n"
      "v_mul_f32 v174, v126, v174\n"
      "v_permlane16_swap_b32 v170, v171\n"
      "v_add_f32 v170, v170, v171\n"
      "v_mul_f32 v175, v126, v175\n"
      "v_mul_f32 v176, v126, v176\n"
      "v_mul_f32 v177, v126, v177\n"
      "v_cvt_pk_bf16_f32 v98, v174, v175\n"
      "v_cvt_pk_bf16_f32 v99, v176, v177\n"
      "v_mfma_f32_16x16x32_bf16 v[108:111], v[122:125], v[148:151], v[72:75]\n"
      "ds_write_b128 v3, v[96:99]\n"
      "v_mfma_f32_16x16x32_bf16 v[174:177], v[156:159], v[148:151], v[28:31]\n"
      "ds_read_b128 v[148:151], v2 offset:25088\n"
      "v_mul_f32 v190, v126, v190\n"
      "v_mfma_f32_16x16x16_bf16 v[112:115], v[168:169], v[206:207], v[112:115]\n"
      "v_mul_f32 v191, v126, v191\n"
      "v_cvt_pk_bf16_f32 v190, v190, v191\n"
      "v_mul_f32_e64 v192, v126, v192\n"
      "v_mfma_f32_16x16x16_bf16 v[202:205], v[218:219], v[206:207], v[202:205]\n"
      "v_mul_f32 v193, v126, v193\n"
      "v_permlane32_swap_b32 v208, v170\n"
      "v_mfma_f32_16x16x16_bf16 v[108:111], v[118:119], v[206:207], v[108:111]\n"
      "s_waitcnt vmcnt(6)\n"
      "v_or3_b32 v144, v144, v145, v146\n"
      "v_max3_f32 v171, v112, v113, v114\n"
      "v_cvt_pk_bf16_f32 v191, v192, v193\n"
      "v_mfma_f32_16x16x16_bf16 v[174:177], v[120:121], v[206:207], v[174:177]\n"
      "s_waitcnt vmcnt(5)\n"
      "v_or3_b32 v144, v144, v147, v39\n"
      "v_max3_f32 v126, v115, v202, v203\n"
      "v_max3_f32 v171, v171, v204, v205\n"
      "v_mfma_f32_16x16x32_bf16 v[100:103], a[216:219], v[136:139], 0\n"
      "ds_write_b64 v4, v[190:191]\n"
      "v_mfma_f32_16x16x32_bf16 v[100:103], a[220:223], v[178:181], v[100:103]\n"
      "v_max3_f32 v126, v126, v108, v109\n"
      "v_max3_f32 v171, v171, v110, v111\n"
      "s_waitcnt lgkmcnt(1)\n"
      "v_mfma_f32_16x16x32_bf16 v[100:103], a[224:227], v[148:151], v[100:103]\n"
      "v_max3_f32 v171, v171, v126, v174\n"
      "v_mov_b32 v176, v171\n"
      "v_mfma_f32_16x16x32_bf16 v[126:129], a[240:243], v[136:139], 0\n"
      "v_add_f32 v208, v208, v170\n"
      "v_permlane16_swap_b32 v171, v176\n"
      "v_mfma_f32_16x16x32_bf16 v[126:129], a[244:247], v[178:181], v[126:129]\n"
      "ds_read_b128 v[190:193], v10\n"
      "v_max_f32 v171, v171, v176\n"
      "v_mfma_f32_16x16x32_bf16 v[126:129], a[248:251], v[148:151], v[126:129]\n"
      "ds_write_b32 v8, v208\n"
      "v_mfma_f32_16x16x32_bf16 v[96:99], v[232:235], v[136:139], 0\n"
      "v_mov_b32 v176, v171\n"
      "v_and_b32 v144, 0x7fff7fff, v144\n"
      "v_mfma_f32_16x16x32_bf16 v[96:99], v[236:239], v[178:181], v[96:99]\n"
      "ds_write_b32 v31, v144\n"
      "v_mfma_f32_16x16x32_bf16 v[96:99], v[240:243], v[148:151], v[96:99]\n"
      "ds_read_b64 v[88:89], v10 offset:16\n"
      "v_permlane32_swap_b32 v171, v176\n"
      "v_mfma_f32_16x16x32_bf16 v[136:139], v[32:35], v[164:167], 0\n"
      "v_max_f32 v171, v171, v176\n"
      "v_sub_f32 v112, v112, v171\n"
      "v_mfma_f32_16x16x32_bf16 v[136:139], v[32:35], v[92:95], v[136:139]\n"
      "s_waitcnt lgkmcnt(3)\n"
      "v_sub_f32 v113, v113, v171\n"
      "v_sub_f32 v114, v114, v171\n"
      "v_mfma_f32_16x16x32_bf16 v[176:179], v[198:201], v[164:167], 0\n"
      "v_sub_f32 v115, v115, v171\n"
      "v_exp_f32 v112, v112\n"
      "v_mfma_f32_16x16x32_bf16 v[176:179], v[76:79], v[92:95], v[176:179]\n"
      "ds_read_b128 v[148:151], v2 offset:28160\n"
      "v_rcp_f32 v136, v136\n"
      "v_exp_f32 v113, v113\n"
      "v_sub_f32 v202, v202, v171\n"
      "v_sub_f32 v203, v203, v171\n"
      "v_sub_f32 v204, v204, v171\n"
      "v_sub_f32 v205, v205, v171\n"
      "v_exp_f32 v114, v114\n"
      "v_mul_f32 v176, v136, v176\n"
      "v_mul_f32 v177, v136, v177\n"
      "v_cvt_pk_bf16_f32 v176, v176, v177\n"
      "v_mul_f32 v178, v136, v178\n"
      "v_mul_f32_e64 v179, v136, v179\n"
      "v_cvt_pk_bf16_f32 v177, v178, v179\n"
      "v_exp_f32 v115, v115\n"
      "v_exp_f32 v202, v202\n"
      "v_exp_f32 v203, v203\n"
      "v_exp_f32 v204, v204\n"
      "v_exp_f32_e64 v205, v205\n"
      "v_cvt_pk_bf16_f32 v112, v112, v113\n"
      "v_cvt_pk_bf16_f32 v113, v114, v115\n"
      "v_cvt_pk_bf16_f32 v114, v202, v203\n"
      "v_mfma_f32_16x16x32_bf16 v[206:209], v[214:217], v[164:167], 0\n"
      "s_waitcnt lgkmcnt(0)\n"
      "v_cvt_pk_bf16_f32 v115, v204, v205\n"
      "v_sub_f32 v108, v108, v171\n"
      "v_mfma_f32_16x16x32_bf16 v[206:209], v[80:83], v[92:95], v[206:209]\n"
      "v_sub_f32 v109, v109, v171\n"
      "v_sub_f32 v110, v110, v171\n"
      "v_mfma_f32_16x16x32_bf16 v[144:147], v[194:197], v[164:167], 0\n"
      "v_sub_f32 v111, v111, v171\n"
      "v_sub_f32 v174, v174, v171\n"
      "v_mfma_f32_16x16x32_bf16 v[144:147], v[84:87], v[92:95], v[144:147]\n"
      "ds_read_b128 v[164:167], v2 offset:31232\n"
      "v_exp_f32 v108, v108\n"
      "v_mfma_f32_16x16x32_bf16 v[170:173], v[190:193], v[210:213], v[30:33]\n"
      "v_mul_f32 v206, v136, v206\n"
      "v_mul_f32 v207, v136, v207\n"
      "v_mfma_f32_16x16x32_bf16 v[210:213], v[190:193], v[152:155], v[30:33]\n"
      "v_mul_f32 v208, v136, v208\n"
      "v_mul_f32_e64 v209, v136, v209\n"
      "v_mfma_f32_16x16x32_bf16 v[152:155], v[190:193], v[122:125], v[30:33]\n"
      "v_cvt_pk_bf16_f32 v178, v206, v207\n"
      "ds_read_b128 v[122:125], v2 offset:34304\n"
      "v_mfma_f32_16x16x32_bf16 v[202:205], v[190:193], v[156:159], v[30:33]\n"
      "v_cvt_pk_bf16_f32 v179, v208, v209\n"
      "v_mul_f32 v144, v136, v144\n"
      "v_mfma_f32_16x16x16_bf16 v[170:173], v[88:89], v[168:169], v[170:173]\n"
      "v_mul_f32 v145, v136, v145\n"
      "v_cvt_pk_bf16_f32 v144, v144, v145\n"
      "v_mul_f32 v146, v136, v146\n"
      "v_mfma_f32_16x16x16_bf16 v[210:213], v[88:89], v[218:219], v[210:213]\n"
      "v_mul_f32 v147, v136, v147\n"
      "v_cvt_pk_bf16_f32 v145, v146, v147\n"
      "v_exp_f32 v109, v109\n"
      "v_mfma_f32_16x16x16_bf16 v[152:155], v[88:89], v[118:119], v[152:155]\n"
      "v_exp_f32 v110, v110\n"
      "v_exp_f32_e64 v111, v111\n"
      "v_mfma_f32_16x16x16_bf16 v[202:205], v[88:89], v[120:121], v[202:205]\n"
      "v_exp_f32 v174, v174\n"
      "v_cndmask_b32 v170, v170, v210, s[58:59]\n"
      "v_mfma_f32_16x16x32_bf16 v[100:103], a[228:231], v[148:151], v[100:103]\n"
      "s_waitcnt lgkmcnt(0)\n"
      "v_cvt_pk_bf16_f32 v88, v108, v109\n"
      "v_cndmask_b32 v170, v170, v152, s[60:61]\n"
      "v_mfma_f32_16x16x32_bf16 v[100:103], a[232:235], v[164:167], v[100:103]\n"
      "ds_write_b128 v3, v[176:179] offset:256\n"
      "v_mfma_f32_16x16x32_bf16 v[100:103], a[236:239], v[122:125], v[100:103]\n"
      "v_cndmask_b32 v170, v170, v202, s[62:63]\n"
      "v_cvt_pk_bf16_f32 v89, v110, v111\n"
      "v_mfma_f32_16x16x32_bf16 v[126:129], a[252:255], v[148:151], v[126:129]\n"
      "v_max_f32_dpp v193, v170, v170 row_ror:8 row_mask:0xf bank_mask:0xf\n"
      "v_cvt_pk_bf16_f32 v90, v174, 0\n"
      "v_mfma_f32_16x16x32_bf16 v[126:129], v[224:227], v[164:167], v[126:129]\n"
      "ds_write_b64 v4, v[144:145] offset:256\n"
      "v_mfma_f32_16x16x32_bf16 v[126:129], v[228:231], v[122:125], v[126:129]\n"
      "v_max_f32_dpp v193, v193, v193 row_ror:4 row_mask:0xf bank_mask:0xf\n"
      "v_mul_f32 v169, v100, v100\n"
      "v_mfma_f32_16x16x32_bf16 v[96:99], v[244:247], v[148:151], v[96:99]\n"
      "v_add_f32 v168, v100, v101\n"
      "v_fmac_f32_e64 v169, v101, v101\n"
      "v_mfma_f32_16x16x32_bf16 v[96:99], v[248:251], v[164:167], v[96:99]\n"
      "v_add_f32 v168, v168, v102\n"
      "v_fmac_f32 v169, v102, v102\n"
      "v_mfma_f32_16x16x32_bf16 v[96:99], v[252:255], v[122:125], v[96:99]\n"
      "v_max_f32_dpp v193, v193, v193 row_ror:2 row_mask:0xf bank_mask:0xf\n"
      "v_add_f32 v168, v168, v103\n"
      "v_mfma_f32_16x16x32_bf16 v[210:213], v[32:35], v[112:115], 0\n"
      "v_fmac_f32 v169, v103, v103\n"
      "v_add_f32 v168, v168, v126\n"
      "v_mfma_f32_16x16x32_bf16 v[210:213], v[32:35], v[88:91], v[210:213]\n"
      "v_fmac_f32 v169, v126, v126\n"
      "v_add_f32 v168, v168, v127\n"
      "v_mfma_f32_16x16x32_bf16 v[136:139], v[198:201], v[112:115], 0\n"
      "v_fmac_f32 v169, v127, v127\n"
      "v_max_f32_dpp v193, v193, v193 row_ror:1 row_mask:0xf bank_mask:0xf\n"
      "v_mfma_f32_16x16x32_bf16 v[136:139], v[76:79], v[88:91], v[136:139]\n"
      "v_add_f32 v168, v168, v128\n"
      "v_fmac_f32 v169, v128, v128\n"
      "v_mfma_f32_16x16x32_bf16 v[108:111], v[214:217], v[112:115], 0\n"
      "v_add_f32 v168, v168, v129\n"
      "v_fmac_f32 v169, v129, v129\n"
      "v_mfma_f32_16x16x32_bf16 v[108:111], v[80:83], v[88:91], v[108:111]\n"
      "v_add_f32 v168, v168, v96\n"
      "v_fmac_f32 v169, v96, v96\n"
      "v_mfma_f32_16x16x32_bf16 v[174:177], v[194:197], v[112:115], 0\n"
      "v_add_f32 v168, v168, v97\n"
      "v_fmac_f32 v169, v97, v97\n"
      "v_mfma_f32_16x16x32_bf16 v[174:177], v[84:87], v[88:91], v[174:177]\n"
      "v_max_f32_dpp v193, v193, v193 row_bcast:15 row_mask:0xa bank_mask:0xf\n"
      "v_add_f32 v168, v168, v98\n"
      "v_fmac_f32 v169, v98, v98\n"
      "v_add_f32 v168, v168, v99\n"
      "v_fmac_f32 v169, v99, v99\n"
      "v_rcp_f32_e64 v210, v210\n"
      "v_max_f32_dpp v193, v193, v193 row_bcast:31 row_mask:0xc bank_mask:0xf\n"
      "v_permlane16_swap_b32 v168, v169\n"
      "v_readlane_b32 s72, v193, 63\n"
      "v_mul_f32 v136, v210, v136\n"
      "v_mul_f32 v137, v210, v137\n"
      "v_add_f32 v168, v168, v169\n"
      "v_cvt_pk_bf16_f32 v136, v136, v137\n"
      "v_mul_f32 v138, v210, v138\n"
      "v_mul_f32 v139, v210, v139\n"
      "v_mov_b32 v169, v168\n"
      "v_cvt_pk_bf16_f32 v137, v138, v139\n"
      "v_mul_f32 v108, v210, v108\n"
      "v_mul_f32 v109, v210, v109\n"
      "v_mul_f32 v110, v210, v110\n"
      "v_mul_f32 v111, v210, v111\n"
      "v_subrev_f32 v170, s72, v170\n"
      "v_cvt_pk_bf16_f32 v138, v108, v109\n"
      "v_cvt_pk_bf16_f32 v139, v110, v111\n"
      "v_mul_f32 v174, v210, v174\n"
      "v_mul_f32 v175, v210, v175\n"
      "v_exp_f32 v170, v170\n"
      "ds_write_b128 v3, v[136:139] offset:512\n"
      "v_cvt_pk_bf16_f32 v174, v174, v175\n"
      "v_mul_f32 v176, v210, v176\n"
      "v_mul_f32 v177, v210, v177\n"
      "v_permlane32_swap_b32 v168, v169\n"
      "v_cvt_pk_bf16_f32 v175, v176, v177\n"
      "v_cvt_pk_bf16_f32 v170, v170, v170\n"
      "v_add_f32_e64 v168, v168, v169\n"
      "ds_write_b64 v4, v[174:175] offset:512\n"
      "v_readlane_b32 s45, v37, s43\n"
      "v_readlane_b32 s46, v37, s42\n"
      "v_or3_b32 v132, v132, v133, v134\n"
      "s_or_b32 s95, s45, s46\n"
      "s_or_b32 s95, s95, s48\n"
      "ds_write_b16 v13, v170\n"
      "ds_read_b128 v[210:213], v14\n"
      "ds_read_b128 v[136:139], v14 offset:64\n"
      "ds_write_b32 v8, v168 offset:512\n"
      "s_cmp_ge_u32 s95, 0x40000000\n"
      "s_cselect_b32 s94, -1, -2\n"
      "s_cmp_gt_u32 s42, s44\n"
      "s_cselect_b32 s95, s94, s95\n"
      "s_add_u32 s42, s42, 1\n"
      "v_or_b32 v132, v132, v135\n"
      "v_cmp_ne_u32 s[56:57], 0, v132\n"
      "v_readlane_b32 s47, v38, s43\n"
      "s_cmp_ge_u32 s95, 0x40000000\n"
      "s_cselect_b64 vcc, -1, s[56:57]\n"
      "s_and_b32 s47, s47, -4\n"
      "s_add_u32 s43, s43, 1\n"
      "v_add_u32_e64 v12, 32, v12\n"
      "s_waitcnt lgkmcnt(2)\n"
      "v_mfma_f32_16x16x32_bf16 v[108:111], v[210:213], v[32:35], 0\n"
      "s_waitcnt lgkmcnt(1)\n"
      "v_mfma_f32_16x16x32_bf16 v[108:111], v[136:139], v[32:35], v[108:111]\n"
      "s_waitcnt lgkmcnt(0)\n"
      "s_barrier\n"
      "s_cbranch_vccz L_max1_%=\n"
      "L_cold_%=:\n"
      "L_disp1_%=:\n"
      "s_cmp_lt_u32 s95, 0x40000000\n"
      "s_cbranch_scc1 L_nofix_11_%=\n"
      "s_cmp_eq_u32 s95, -2\n"
      "s_cbranch_scc1 L_tail1_%=\n"
      "s_nop 2\n"
      "v_lshlrev_b32 v174, 3, v27\n"
      "s_add_u32 s72, s98, L_cold_%=-L_ipf0_%=\n"
      "s_addc_u32 s73, s99, 0\n"
      "s_mov_b32 s74, L_cend_%=-L_cold_%=\n"
      "s_mov_b32 s75, 0x20000\n"
      "s_mov_b32 m0, 161792\n"
      "s_nop 0\n"
      "buffer_load_dwordx4 v174, s[72:75], 0 offen  lds\n"
      "s_lshr_b32 s92, s45, 30\n"
      "s_or_b32 s92, s92, s66\n"
      "s_cbranch_scc0 L_nf_12_%=\n"
      "s_bitcmp1_b32 s45, 31\n"
      "s_cselect_b32 s75, 100270464, 0\n"
      "s_bitcmp1_b32 s45, 30\n"
      "s_cselect_b32 s76, 196224, 0\n"
      "s_and_b32 s74, s45, 0x3fffffff\n"
      "s_cmp_ge_u32 s45, 0x40000000\n"
      "s_cselect_b32 s74, s74, 0\n"
      "s_cselect_b32 s45, 0, s45\n"
      "s_sub_u32 s74, s74, s66\n"
      "s_add_u32 s66, s66, s74\n"
      "s_sub_u32 s75, s75, s67\n"
      "s_add_u32 s67, s67, s75\n"
      "s_sub_u32 s76, s76, s68\n"
      "s_add_u32 s68, s68, s76\n"
      "v_add_u32 v16, s74, v16\n"
      "v_add_u32 v17, s74, v17\n"
      "v_add_u32 v18, s74, v18\n"
      "v_add_u32 v19, s74, v19\n"
      "v_add_u32 v15, s74, v15\n"
      "s_cmp_eq_u32 s75, 0\n"
      "s_cbranch_scc1 L_ax_13_%=\n"
      "v_bfe_i32 v174, v36, 0, 1\n"
      "v_and_b32 v174, s75, v174\n"
      "v_sub_u32 v16, v16, v174\n"
      "v_bfe_i32 v174, v36, 2, 1\n"
      "v_and_b32 v174, s75, v174\n"
      "v_sub_u32 v17, v17, v174\n"
      "v_bfe_i32 v174, v36, 4, 1\n"
      "v_and_b32 v174, s75, v174\n"
      "v_sub_u32 v18, v18, v174\n"
      "v_bfe_i32 v174, v36, 6, 1\n"
      "v_and_b32 v174, s75, v174\n"
      "v_sub_u32 v19, v19, v174\n"
      "v_bfe_i32 v174, v36, 8, 1\n"
      "v_and_b32 v174, s75, v174\n"
      "v_sub_u32 v15, v15, v174\n"
      "L_ax_13_%=:\n"
      "s_cmp_eq_u32 s76, 0\n"
      "s_cbranch_scc1 L_ax_14_%=\n"
      "v_bfe_i32 v174, v36, 1, 1\n"
      "v_and_b32 v174, s76, v174\n"
      "v_sub_u32 v16, v16, v174\n"
      "v_bfe_i32 v174, v36, 3, 1\n"
      "v_and_b32 v174, s76, v174\n"
      "v_sub_u32 v17, v17, v174\n"
      "v_bfe_i32 v174, v36, 5, 1\n"
      "v_and_b32 v174, s76, v174\n"
      "v_sub_u32 v18, v18, v174\n"
      "v_bfe_i32 v174, v36, 7, 1\n"
      "v_and_b32 v174, s76, v174\n"
      "v_sub_u32 v19, v19, v174\n"
      "v_bfe_i32 v174, v36, 9, 1\n"
      "v_and_b32 v174, s76, v174\n"
      "v_sub_u32 v15, v15, v174\n"
      "L_ax_14_%=:\n"
      "s_or_b32 s48, s66, s69\n"
      "s_cmp_lg_u32 s48, 0\n"
      "s_cselect_b32 s48, 0x40000000, 0\n"
      "L_nf_12_%=:\n"
      "s_lshr_b32 s92, s46, 30\n"
      "s_or_b32 s92, s92, s69\n"
      "s_cbranch_scc0 L_nf_15_%=\n"
      "s_bitcmp1_b32 s46, 31\n"
      "s_cselect_b32 s75, 100270464, 0\n"
      "s_bitcmp1_b32 s46, 30\n"
      "s_cselect_b32 s76, 196224, 0\n"
      "s_and_b32 s74, s46, 0x3fffffff\n"
      "s_cmp_ge_u32 s46, 0x40000000\n"
      "s_cselect_b32 s74, s74, 0\n"
      "s_cselect_b32 s46, 0, s46\n"
      "s_sub_u32 s74, s74, s69\n"
      "s_add_u32 s69, s69, s74\n"
      "s_sub_u32 s75, s75, s70\n"
      "s_add_u32 s70, s70, s75\n"
      "s_sub_u32 s76, s76, s71\n"
      "s_add_u32 s71, s71, s76\n"
      "v_add_u32 v20, s74, v20\n"
      "v_add_u32 v21, s74, v21\n"
      "v_add_u32 v22, s74, v22\n"
      "v_add_u32 v23, s74, v23\n"
      "v_add_u32 v25, s74, v25\n"
      "s_cmp_eq_u32 s75, 0\n"
      "s_cbranch_scc1 L_ax_16_%=\n"
      "v_bfe_i32 v174, v36, 10, 1\n"
      "v_and_b32 v174, s75, v174\n"
      "v_sub_u32 v20, v20, v174\n"
      "v_bfe_i32 v174, v36, 12, 1\n"
      "v_and_b32 v174, s75, v174\n"
      "v_sub_u32 v21, v21, v174\n"
      "v_bfe_i32 v174, v36, 14, 1\n"
      "v_and_b32 v174, s75, v174\n"
      "v_sub_u32 v22, v22, v174\n"
      "v_bfe_i32 v174, v36, 16, 1\n"
      "v_and_b32 v174, s75, v174\n"
      "v_sub_u32 v23, v23, v174\n"
      "v_bfe_i32 v174, v36, 18, 1\n"
      "v_and_b32 v174, s75, v174\n"
      "v_sub_u32 v25, v25, v174\n"
      "L_ax_16_%=:\n"
      "s_cmp_eq_u32 s76, 0\n"
      "s_cbranch_scc1 L_ax_17_%=\n"
      "v_bfe_i32 v174, v36, 11, 1\n"
      "v_and_b32 v174, s76, v174\n"
      "v_sub_u32 v20, v20, v174\n"
      "v_bfe_i32 v174, v36, 13, 1\n"
      "v_and_b32 v174, s76, v174\n"
      "v_sub_u32 v21, v21, v174\n"
      "v_bfe_i32 v174, v36, 15, 1\n"
      "v_and_b32 v174, s76, v174\n"
      "v_sub_u32 v22, v22, v174\n"
      "v_bfe_i32 v174, v36, 17, 1\n"
      "v_and_b32 v174, s76, v174\n"
      "v_sub_u32 v23, v23, v174\n"
      "v_bfe_i32 v174, v36, 19, 1\n"
      "v_and_b32 v174, s76, v174\n"
      "v_sub_u32 v25, v25, v174\n"
      "L_ax_17_%=:\n"
      "s_or_b32 s48, s66, s69\n"
      "s_cmp_lg_u32 s48, 0\n"
      "s_cselect_b32 s48, 0x40000000, 0\n"
      "L_nf_15_%=:\n"
      "s_sub_u32 s92, s42, 1\n"
      "s_cmp_gt_u32 s92, s44\n"
      "s_cbranch_scc1 L_tail1_%=\n"
      "L_nofix_11_%=:\n"
      "s_cmp_lg_u64 s[56:57], 0\n"
      "s_cbranch_scc1 L_slow1_%=\n"
      "L_sel1_%=:\n"
      "s_cmp_lg_u64 s[54:55], 0\n"
      "s_cbranch_scc1 L_max1_%=\n"
      "s_branch L_loop1_%=\n"
      "L_slow1_%=:\n"
      "s_nop 4\n"
      "v_mfma_f32_16x16x32_bf16 v[174:177], v[210:213], v[198:201], 0\n"
      "ds_read_b128 v[122:125], v0 offset:18432\n"
      "v_subrev_u32 v144, 115456, v9\n"
      "v_mfma_f32_16x16x32_bf16 v[174:177], v[136:139], v[76:79], v[174:177]\n"
      "ds_read_b128 v[166:169], v1 offset:18432\n"
      "v_subrev_u32 v76, 115456, v9\n"
      "v_mfma_f32_16x16x32_bf16 v[190:193], v[210:213], v[214:217], 0\n"
      "ds_read_b128 v[206:209], v0 offset:24576\n"
      "v_bfe_u32 v77, v76, 4, 4\n"
      "v_mfma_f32_16x16x32_bf16 v[190:193], v[136:139], v[80:83], v[190:193]\n"
      "ds_read_b128 v[198:201], v9\n"
      "v_mul_u32_u24 v219, 98, v77\n"
      "v_mfma_f32_16x16x32_bf16 v[214:217], v[210:213], v[194:197], 0\n"
      "ds_read_b128 v[194:197], v9 offset:1024\n"
      "v_lshrrev_b32 v76, 8, v76\n"
      "v_mfma_f32_16x16x32_bf16 v[214:217], v[136:139], v[84:87], v[214:217]\n"
      "s_add_u32 s72, s42, 1\n"
      "v_mov_b32_dpp v108, v108 quad_perm:[0,0,0,0] row_mask:0xf bank_mask:0xf\n"
      "v_rcp_f32 v108, v108\n"
      "s_waitcnt lgkmcnt(4)\n"
      "v_mfma_f32_16x16x32_bf16 v[148:151], a[0:3], v[122:125], 0\n"
      "s_mov_b32 m0, 110592\n"
      "v_cndmask_b32 v174, v174, v190, s[58:59]\n"
      "v_readlane_b32 s77, v38, s72\n"
      "v_mfma_f32_16x16x32_bf16 v[132:135], a[24:27], v[122:125], 0\n"
      "s_and_b32 s73, s77, -16\n"
      "v_cndmask_b32 v174, v174, v214, s[60:61]\n"
      "v_mul_f32 v174, v108, v174\n"
      "v_mfma_f32_16x16x32_bf16 v[118:121], a[48:51], v[122:125], 0\n"
      "buffer_load_dwordx4 v144, s[24:27], s73 offen  lds\n"
      "v_mfma_f32_16x16x32_bf16 v[136:139], a[72:75], v[122:125], 0\n"
      "ds_read_b128 v[176:179], v1 offset:24576\n"
      "v_cvt_pk_bf16_f32 v174, v174, v174\n"
      "v_mfma_f32_16x16x32_bf16 v[202:205], a[96:99], v[122:125], 0\n"
      "ds_write_b16 v12, v174\n"
      "v_mfma_f32_16x16x32_bf16 v[170:173], a[120:123], v[122:125], 0\n"
      "s_waitcnt lgkmcnt(2)\n"
      "v_add_f32 v198, v198, v200\n"
      "v_add_f32 v194, v194, v196\n"
      "v_mfma_f32_16x16x32_bf16 v[148:151], a[4:7], v[166:169], v[148:151]\n"
      "ds_read_b128 v[152:155], v0 offset:30720\n"
      "v_add_f32 v199, v199, v201\n"
      "v_mfma_f32_16x16x32_bf16 v[148:151], a[8:11], v[206:209], v[148:151]\n"
      "ds_read_b128 v[112:115], v1 offset:30720\n"
      "v_add_f32_e64 v195, v195, v197\n"
      "v_mfma_f32_16x16x32_bf16 v[132:135], a[28:31], v[166:169], v[132:135]\n"
      "s_mov_b32 m0, 111616\n"
      "v_add_f32 v198, v198, v194\n"
      "v_add_f32 v199, v199, v195\n"
      "v_mfma_f32_16x16x32_bf16 v[132:135], a[32:35], v[206:209], v[132:135]\n"
      "s_add_u32 s73, s73, 1024\n"
      "v_fma_f32 v194, -v198, v198, s52\n"
      "v_fmac_f32 v194, 0x43400000, v199\n"
      "v_mfma_f32_16x16x32_bf16 v[118:121], a[52:55], v[166:169], v[118:121]\n"
      "buffer_load_dwordx4 v144, s[24:27], s73 offen  lds\n"
      "v_mfma_f32_16x16x32_bf16 v[118:121], a[56:59], v[206:209], v[118:121]\n"
      "s_mov_b32 m0, 112640\n"
      "v_max_f32 v194, s52, v194\n"
      "v_rsq_f32 v194, v194\n"
      "v_mfma_f32_16x16x32_bf16 v[136:139], a[76:79], v[166:169], v[136:139]\n"
      "s_add_u32 s73, s73, 1024\n"
      "v_mul_f32 v199, 0x43400000, v194\n"
      "v_mul_f32 v201, -v198, v194\n"
      "v_mfma_f32_16x16x32_bf16 v[136:139], a[80:83], v[206:209], v[136:139]\n"
      "s_and_b32 s77, s77, 15\n"
      "v_mov_b32 v174, v199\n"
      "v_mov_b32_e64 v175, v201\n"
      "v_mfma_f32_16x16x32_bf16 v[202:205], a[100:103], v[166:169], v[202:205]\n"
      "buffer_load_dwordx4 v144, s[24:27], s73 offen  lds\n"
      "v_mfma_f32_16x16x32_bf16 v[202:205], a[104:107], v[206:209], v[202:205]\n"
      "s_mov_b32 m0, 113664\n"
      "v_permlane32_swap_b32_e64 v199, v174\n"
      "v_mfma_f32_16x16x32_bf16 v[170:173], a[124:127], v[166:169], v[170:173]\n"
      "s_add_u32 s73, s73, 1024\n"
      "v_permlane32_swap_b32 v201, v175\n"
      "v_mfma_f32_16x16x32_bf16 v[170:173], a[128:131], v[206:209], v[170:173]\n"
      "s_waitcnt lgkmcnt(0)\n"
      "v_mov_b32 v180, v199\n"
      "v_mov_b32 v181, v201\n"
      "v_mfma_f32_16x16x32_bf16 v[148:151], a[12:15], v[176:179], v[148:151]\n"
      "buffer_load_dwordx4 v144, s[24:27], s73 offen  lds\n"
      "v_mfma_f32_16x16x32_bf16 v[148:151], a[16:19], v[152:155], v[148:151]\n"
      "s_mov_b32 m0, 114688\n"
      "v_permlane16_swap_b32 v199, v180\n"
      "v_mfma_f32_16x16x32_bf16 v[148:151], a[20:23], v[112:115], v[148:151]\n"
      "s_add_u32 s73, s73, 1024\n"
      "v_permlane16_swap_b32 v201, v181\n"
      "v_mfma_f32_16x16x32_bf16 v[132:135], a[36:39], v[176:179], v[132:135]\n"
      "v_fma_f32 v140, v140, v199, v201\n"
      "v_fma_f32 v141, v141, v199, v201\n"
      "v_mfma_f32_16x16x32_bf16 v[132:135], a[40:43], v[152:155], v[132:135]\n"
      "s_mov_b32 exec_hi, 0xffff\n"
      "buffer_load_dwordx4 v144, s[24:27], s73 offen  lds\n"
      "s_mov_b32 exec_hi, -1\n"
      "v_mfma_f32_16x16x32_bf16 v[132:135], a[44:47], v[112:115], v[132:135]\n"
      "s_add_u32 m0, s64, 0\n"
      "v_cvt_pk_bf16_f32 v140, v140, v141\n"
      "v_fma_f32 v142, v142, v199, v201\n"
      "v_mfma_f32_16x16x32_bf16 v[118:121], a[60:63], v[176:179], v[118:121]\n"
      "s_add_u32 s73, s73, 1024\n"
      "v_fma_f32 v143, v143, v199, v201\n"
      "v_fma_f32 v160, v160, v199, v201\n"
      "v_mfma_f32_16x16x32_bf16 v[118:121], a[64:67], v[152:155], v[118:121]\n"
      "v_fma_f32 v161, v161, v199, v201\n"
      "v_cvt_pk_bf16_f32 v141, v142, v143\n"
      "v_mfma_f32_16x16x32_bf16 v[118:121], a[68:71], v[112:115], v[118:121]\n"
      "buffer_load_dwordx4 v16, s[16:19], s45 offen  lds\n"
      "v_mfma_f32_16x16x32_bf16 v[136:139], a[84:87], v[176:179], v[136:139]\n"
      "s_add_u32 m0, s64, 4096\n"
      "v_fma_f32 v220, v220, v199, v201\n"
      "v_fma_f32 v221, v221, v199, v201\n"
      "v_mfma_f32_16x16x32_bf16 v[136:139], a[88:91], v[152:155], v[136:139]\n"
      "v_fma_f32 v222, v222, v199, v201\n"
      "v_fma_f32 v223, v223, v199, v201\n"
      "v_mfma_f32_16x16x32_bf16 v[136:139], a[92:95], v[112:115], v[136:139]\n"
      "v_cvt_pk_bf16_f32 v160, v160, v161\n"
      "v_fma_f32 v162, v162, v199, v201\n"
      "v_mfma_f32_16x16x32_bf16 v[202:205], a[108:111], v[176:179], v[202:205]\n"
      "buffer_load_dwordx4 v17, s[16:19], s45 offen  lds\n"
      "v_mfma_f32_16x16x32_bf16 v[202:205], a[112:115], v[152:155], v[202:205]\n"
      "s_add_u32 m0, s64, 8192\n"
      "v_fma_f32 v163, v163, v199, v201\n"
      "v_cvt_pk_bf16_f32 v142, v220, v221\n"
      "v_mfma_f32_16x16x32_bf16 v[202:205], a[116:119], v[112:115], v[202:205]\n"
      "v_cvt_pk_bf16_f32 v143, v222, v223\n"
      "v_cvt_pk_bf16_f32 v161, v162, v163\n"
      "v_mfma_f32_16x16x32_bf16 v[170:173], a[132:135], v[176:179], v[170:173]\n"
      "ds_write_b128 v5, v[140:143] offset:18432\n"
      "v_mfma_f32_16x16x32_bf16 v[170:173], a[136:139], v[152:155], v[170:173]\n"
      "buffer_load_dwordx4 v18, s[16:19], s45 offen  lds\n"
      "v_mfma_f32_16x16x32_bf16 v[170:173], a[140:143], v[112:115], v[170:173]\n"
      "s_add_u32 m0, s64, 12288\n"
      "v_fma_f32 v186, v186, v180, v181\n"
      "v_fma_f32 v187, v187, v180, v181\n"
      "v_mfma_f32_16x16x32_bf16 v[198:201], v[122:125], a[144:147], 0\n"
      "ds_write_b64 v6, v[160:161] offset:18432\n"
      "v_mfma_f32_16x16x32_bf16 v[198:201], v[166:169], a[148:151], v[198:201]\n"
      "v_cvt_pk_bf16_f32 v186, v186, v187\n"
      "v_fma_f32 v188, v188, v180, v181\n"
      "v_mfma_f32_16x16x32_bf16 v[198:201], v[206:209], a[152:155], v[198:201]\n"
      "buffer_load_dwordx4 v19, s[16:19], s45 offen  lds\n"
      "v_mfma_f32_16x16x32_bf16 v[198:201], v[176:179], a[156:159], v[198:201]\n"
      "ds_read_b128 v[144:147], v0 offset:20480\n"
      "v_fma_f32 v189, v189, v180, v181\n"
      "v_mfma_f32_16x16x32_bf16 v[198:201], v[152:155], a[160:163], v[198:201]\n"
      "ds_read_b128 v[140:143], v1 offset:20480\n"
      "v_fma_f32 v104, v104, v180, v181\n"
      "v_mfma_f32_16x16x32_bf16 v[198:201], v[112:115], a[164:167], v[198:201]\n"
      "ds_read_b128 v[158:161], v0 offset:26624\n"
      "v_fma_f32 v105, v105, v180, v181\n"
      "v_mfma_f32_16x16x32_bf16 v[214:217], v[122:125], a[168:171], 0\n"
      "ds_read_b128 v[108:111], v1 offset:26624\n"
      "v_cvt_pk_bf16_f32 v187, v188, v189\n"
      "v_mfma_f32_16x16x32_bf16 v[214:217], v[166:169], a[172:175], v[214:217]\n"
      "ds_read_b128 v[190:193], v0 offset:32768\n"
      "v_fma_f32 v182, v182, v180, v181\n"
      "v_mfma_f32_16x16x32_bf16 v[214:217], v[206:209], a[176:179], v[214:217]\n"
      "ds_read_b128 v[210:213], v1 offset:32768\n"
      "v_fma_f32 v183, v183, v180, v181\n"
      "v_mfma_f32_16x16x32_bf16 v[214:217], v[176:179], a[180:183], v[214:217]\n"
      "s_add_u32 m0, s65, 16384\n"
      "v_fma_f32 v184, v184, v180, v181\n"
      "v_fma_f32 v185, v185, v180, v181\n"
      "v_mfma_f32_16x16x32_bf16 v[214:217], v[152:155], a[184:187], v[214:217]\n"
      "buffer_load_dwordx4 v15, s[16:19], s45 offen  lds\n"
      "v_mfma_f32_16x16x32_bf16 v[214:217], v[112:115], a[188:191], v[214:217]\n"
      "v_cvt_pk_bf16_f32 v104, v104, v105\n"
      "v_fma_f32 v106, v106, v180, v181\n"
      "v_mfma_f32_16x16x32_bf16 v[194:197], v[122:125], a[192:195], 0\n"
      "v_fma_f32 v107, v107, v180, v181\n"
      "v_cvt_pk_bf16_f32 v188, v182, v183\n"
      "v_mfma_f32_16x16x32_bf16 v[194:197], v[166:169], a[196:199], v[194:197]\n"
      "v_cvt_pk_bf16_f32 v189, v184, v185\n"
      "v_cvt_pk_bf16_f32 v105, v106, v107\n"
      "v_mfma_f32_16x16x32_bf16 v[194:197], v[206:209], a[200:203], v[194:197]\n"
      "ds_write_b128 v5, v[186:189] offset:24576\n"
      "v_mfma_f32_16x16x32_bf16 v[194:197], v[176:179], a[204:207], v[194:197]\n"
      "buffer_load_dwordx4 v[176:179], v27, s[24:27], s47 offen\n"
      "v_mfma_f32_16x16x32_bf16 v[194:197], v[152:155], a[208:211], v[194:197]\n"
      "s_waitcnt lgkmcnt(1)\n"
      "v_fma_f32 v100, v100, v174, v175\n"
      "v_fma_f32 v101, v101, v174, v175\n"
      "v_mfma_f32_16x16x32_bf16 v[194:197], v[112:115], a[212:215], v[194:197]\n"
      "ds_write_b64 v6, v[104:105] offset:24576\n"
      "v_mfma_f32_16x16x32_bf16 v[166:169], a[0:3], v[144:147], 0\n"
      "v_cvt_pk_bf16_f32 v100, v100, v101\n"
      "v_fma_f32 v102, v102, v174, v175\n"
      "v_mfma_f32_16x16x32_bf16 v[166:169], a[4:7], v[140:143], v[166:169]\n"
      "buffer_load_dword v78, v29, s[24:27], s47 offen\n"
      "v_mfma_f32_16x16x32_bf16 v[166:169], a[8:11], v[158:161], v[166:169]\n"
      "v_fma_f32 v103, v103, v174, v175\n"
      "v_fma_f32 v96, v96, v174, v175\n"
      "v_mfma_f32_16x16x32_bf16 v[166:169], a[12:15], v[108:111], v[166:169]\n"
      "v_fma_f32 v97, v97, v174, v175\n"
      "v_cvt_pk_bf16_f32 v101, v102, v103\n"
      "v_mfma_f32_16x16x32_bf16 v[166:169], a[16:19], v[190:193], v[166:169]\n"
      "v_fma_f32 v126, v126, v174, v175\n"
      "v_fma_f32 v127, v127, v174, v175\n"
      "v_mfma_f32_16x16x32_bf16 v[166:169], a[20:23], v[210:213], v[166:169]\n"
      "v_fma_f32 v128, v128, v174, v175\n"
      "v_fma_f32 v129, v129, v174, v175\n"
      "v_mfma_f32_16x16x32_bf16 v[112:115], a[24:27], v[144:147], 0\n"
      "v_cvt_pk_bf16_f32 v96, v96, v97\n"
      "v_fma_f32 v98, v98, v174, v175\n"
      "v_mfma_f32_16x16x32_bf16 v[112:115], a[28:31], v[140:143], v[112:115]\n"
      "v_fma_f32 v99, v99, v174, v175\n"
      "v_cvt_pk_bf16_f32 v102, v126, v127\n"
      "v_mfma_f32_16x16x32_bf16 v[112:115], a[32:35], v[158:161], v[112:115]\n"
      "v_cvt_pk_bf16_f32 v103, v128, v129\n"
      "v_cvt_pk_bf16_f32 v97, v98, v99\n"
      "v_mfma_f32_16x16x32_bf16 v[112:115], a[36:39], v[108:111], v[112:115]\n"
      "ds_write_b128 v5, v[100:103] offset:30720\n"
      "v_mfma_f32_16x16x32_bf16 v[112:115], a[40:43], v[190:193], v[112:115]\n"
      "v_fma_f32 v175, v148, v148, s51\n"
      "v_fma_f32 v100, v136, v136, s51\n"
      "v_mfma_f32_16x16x32_bf16 v[112:115], a[44:47], v[210:213], v[112:115]\n"
      "v_fmac_f32 v175, v149, v149\n"
      "v_fmac_f32_e64 v100, v137, v137\n"
      "v_mfma_f32_16x16x32_bf16 v[126:129], a[48:51], v[144:147], 0\n"
      "ds_write_b64 v6, v[96:97] offset:30720\n"
      "v_mfma_f32_16x16x32_bf16 v[126:129], a[52:55], v[140:143], v[126:129]\n"
      "v_fmac_f32 v175, v150, v150\n"
      "v_fmac_f32 v100, v138, v138\n"
      "v_mfma_f32_16x16x32_bf16 v[126:129], a[56:59], v[158:161], v[126:129]\n"
      "v_fmac_f32 v175, v151, v151\n"
      "v_fmac_f32 v100, v139, v139\n"
      "v_mfma_f32_16x16x32_bf16 v[126:129], a[60:63], v[108:111], v[126:129]\n"
      "v_fmac_f32 v175, v132, v132\n"
      "v_fmac_f32 v100, v202, v202\n"
      "v_mfma_f32_16x16x32_bf16 v[126:129], a[64:67], v[190:193], v[126:129]\n"
      "v_fmac_f32 v175, v133, v133\n"
      "v_fmac_f32 v100, v203, v203\n"
      "v_mfma_f32_16x16x32_bf16 v[126:129], a[68:71], v[210:213], v[126:129]\n"
      "v_fmac_f32 v175, v134, v134\n"
      "v_fmac_f32 v100, v204, v204\n"
      "v_mfma_f32_16x16x32_bf16 v[152:155], a[72:75], v[144:147], 0\n"
      "v_fmac_f32 v175, v135, v135\n"
      "v_fmac_f32 v100, v205, v205\n"
      "v_mfma_f32_16x16x32_bf16 v[152:155], a[76:79], v[140:143], v[152:155]\n"
      "v_fmac_f32 v175, v118, v118\n"
      "v_fmac_f32 v100, v170, v170\n"
      "v_mfma_f32_16x16x32_bf16 v[152:155], a[80:83], v[158:161], v[152:155]\n"
      "v_fmac_f32 v175, v119, v119\n"
      "v_fmac_f32 v100, v171, v171\n"
      "v_mfma_f32_16x16x32_bf16 v[152:155], a[84:87], v[108:111], v[152:155]\n"
      "v_fmac_f32 v175, v120, v120\n"
      "v_fmac_f32 v100, v172, v172\n"
      "v_mfma_f32_16x16x32_bf16 v[152:155], a[88:91], v[190:193], v[152:155]\n"
      "v_fmac_f32 v175, v121, v121\n"
      "v_fmac_f32 v100, v173, v173\n"
      "v_mfma_f32_16x16x32_bf16 v[152:155], a[92:95], v[210:213], v[152:155]\n"
      "v_fma_f32 v185, v166, v166, s51\n"
      "v_permlane16_swap_b32 v175, v100\n"
      "v_mfma_f32_16x16x32_bf16 v[122:125], a[96:99], v[144:147], 0\n"
      "v_add_f32 v175, v175, v100\n"
      "v_mov_b32 v100, v175\n"
      "v_mfma_f32_16x16x32_bf16 v[122:125], a[100:103], v[140:143], v[122:125]\n"
      "v_fmac_f32 v185, v167, v167\n"
      "v_permlane32_swap_b32 v175, v100\n"
      "v_mfma_f32_16x16x32_bf16 v[122:125], a[104:107], v[158:161], v[122:125]\n"
      "v_add_f32 v175, v175, v100\n"
      "v_rsq_f32 v175, v175\n"
      "v_mfma_f32_16x16x32_bf16 v[122:125], a[108:111], v[108:111], v[122:125]\n"
      "v_mov_b32 v100, v175\n"
      "v_fma_f32 v184, v152, v152, s51\n"
      "v_mfma_f32_16x16x32_bf16 v[122:125], a[112:115], v[190:193], v[122:125]\n"
      "v_permlane16_swap_b32 v175, v100\n"
      "v_mfma_f32_16x16x32_bf16 v[122:125], a[116:119], v[210:213], v[122:125]\n"
      "v_mul_f32 v175, s50, v175\n"
      "v_mul_f32 v136, v100, v136\n"
      "v_mfma_f32_16x16x32_bf16 v[180:183], a[120:123], v[144:147], 0\n"
      "v_mul_f32 v137, v100, v137\n"
      "v_mul_f32 v148, v175, v148\n"
      "v_mfma_f32_16x16x32_bf16 v[180:183], a[124:127], v[140:143], v[180:183]\n"
      "v_mul_f32 v149, v175, v149\n"
      "v_cvt_pk_bf16_f32 v136, v136, v137\n"
      "v_mfma_f32_16x16x32_bf16 v[180:183], a[128:131], v[158:161], v[180:183]\n"
      "v_mul_f32 v138, v100, v138\n"
      "v_mul_f32 v139, v100, v139\n"
      "v_mfma_f32_16x16x32_bf16 v[180:183], a[132:135], v[108:111], v[180:183]\n"
      "v_mul_f32 v170, v100, v170\n"
      "v_mul_f32 v171, v100, v171\n"
      "v_mfma_f32_16x16x32_bf16 v[180:183], a[136:139], v[190:193], v[180:183]\n"
      "v_cvt_pk_bf16_f32 v148, v148, v149\n"
      "v_mul_f32 v150, v175, v150\n"
      "v_mfma_f32_16x16x32_bf16 v[180:183], a[140:143], v[210:213], v[180:183]\n"
      "v_mul_f32 v151, v175, v151\n"
      "v_mul_f32 v118, v175, v118\n"
      "v_mfma_f32_16x16x32_bf16 v[96:99], v[144:147], a[144:147], 0\n"
      "v_mul_f32 v119, v175, v119\n"
      "v_cvt_pk_bf16_f32 v137, v138, v139\n"
      "v_mfma_f32_16x16x32_bf16 v[96:99], v[140:143], a[148:151], v[96:99]\n"
      "v_mul_f32 v202, v100, v202\n"
      "v_mul_f32 v203, v100, v203\n"
      "v_mfma_f32_16x16x32_bf16 v[96:99], v[158:161], a[152:155], v[96:99]\n"
      "v_mul_f32 v204, v100, v204\n"
      "v_mul_f32 v205, v100, v205\n"
      "v_mfma_f32_16x16x32_bf16 v[96:99], v[108:111], a[156:159], v[96:99]\n"
      "ds_read_b128 v[104:107], v0 offset:22528\n"
      "v_cvt_pk_bf16_f32 v170, v170, v171\n"
      "v_mfma_f32_16x16x32_bf16 v[96:99], v[190:193], a[160:163], v[96:99]\n"
      "ds_read_b128 v[186:189], v1 offset:22528\n"
      "v_mul_f32 v172, v100, v172\n"
      "v_mfma_f32_16x16x32_bf16 v[96:99], v[210:213], a[164:167], v[96:99]\n"
      "ds_read_b128 v[220:223], v7\n"
      "v_mul_f32 v173, v100, v173\n"
      "v_mfma_f32_16x16x32_bf16 v[100:103], v[144:147], a[168:171], 0\n"
      "ds_read_b128 v[206:209], v0 offset:28672\n"
      "v_cvt_pk_bf16_f32 v149, v150, v151\n"
      "v_mfma_f32_16x16x32_bf16 v[100:103], v[140:143], a[172:175], v[100:103]\n"
      "ds_read_b128 v[162:165], v1 offset:28672\n"
      "v_mul_f32 v132, v175, v132\n"
      "v_mfma_f32_16x16x32_bf16 v[100:103], v[158:161], a[176:179], v[100:103]\n"
      "s_waitcnt vmcnt(7)\n"
      "v_mul_f32 v133, v175, v133\n"
      "v_mul_f32 v134, v175, v134\n"
      "v_mfma_f32_16x16x32_bf16 v[100:103], v[108:111], a[180:183], v[100:103]\n"
      "v_mul_f32 v135, v175, v135\n"
      "v_cvt_pk_bf16_f32 v118, v118, v119\n"
      "v_mfma_f32_16x16x32_bf16 v[100:103], v[190:193], a[184:187], v[100:103]\n"
      "v_mul_f32 v120, v175, v120\n"
      "v_mul_f32_e64 v121, v175, v121\n"
      "v_mfma_f32_16x16x32_bf16 v[100:103], v[210:213], a[188:191], v[100:103]\n"
      "v_cvt_pk_bf16_f32 v138, v202, v203\n"
      "v_cvt_pk_bf16_f32 v139, v204, v205\n"
      "v_mfma_f32_16x16x32_bf16 v[202:205], v[144:147], a[192:195], 0\n"
      "ds_read_b128 v[144:147], v7 offset:4096\n"
      "v_cvt_pk_bf16_f32 v171, v172, v173\n"
      "v_mfma_f32_16x16x32_bf16 v[202:205], v[140:143], a[196:199], v[202:205]\n"
      "ds_read_b128 v[140:143], v0 offset:34816\n"
      "v_cvt_pk_bf16_f32 v150, v132, v133\n"
      "v_mfma_f32_16x16x32_bf16 v[202:205], v[158:161], a[200:203], v[202:205]\n"
      "ds_read_b128 v[158:161], v7 offset:8192\n"
      "v_cvt_pk_bf16_f32 v151, v134, v135\n"
      "v_mfma_f32_16x16x32_bf16 v[202:205], v[108:111], a[204:207], v[202:205]\n"
      "ds_read_b128 v[108:111], v1 offset:34816\n"
      "v_cvt_pk_bf16_f32 v119, v120, v121\n"
      "v_mfma_f32_16x16x32_bf16 v[202:205], v[190:193], a[208:211], v[202:205]\n"
      "ds_read_b128 v[190:193], v7 offset:12288\n"
      "v_fmac_f32 v184, v153, v153\n"
      "v_mfma_f32_16x16x32_bf16 v[202:205], v[210:213], a[212:215], v[202:205]\n"
      "s_waitcnt lgkmcnt(5)\n"
      "v_fmac_f32 v185, v168, v168\n"
      "v_fmac_f32 v184, v154, v154\n"
      "v_mfma_f32_16x16x32_bf16 v[132:135], a[0:3], v[104:107], 0\n"
      "buffer_store_dwordx4 v[220:223], v20, s[20:23], s46 offen sc1\n"
      "v_mfma_f32_16x16x32_bf16 v[132:135], a[4:7], v[186:189], v[132:135]\n"
      "ds_read_b128 v[172:175], v7 offset:16384\n"
      "v_fmac_f32 v185, v169, v169\n"
      "v_mfma_f32_16x16x32_bf16 v[132:135], a[8:11], v[206:209], v[132:135]\n"
      "s_waitcnt lgkmcnt(2)\n"
      "v_fmac_f32 v184, v155, v155\n"
      "v_fmac_f32 v185, v112, v112\n"
      "v_mfma_f32_16x16x32_bf16 v[132:135], a[12:15], v[162:165], v[132:135]\n"
      "s_waitcnt lgkmcnt(1)\n"
      "v_fmac_f32 v184, v122, v122\n"
      "v_fmac_f32 v185, v113, v113\n"
      "v_mfma_f32_16x16x32_bf16 v[132:135], a[16:19], v[140:143], v[132:135]\n"
      "v_fmac_f32 v184, v123, v123\n"
      "v_fmac_f32 v185, v114, v114\n"
      "v_mfma_f32_16x16x32_bf16 v[132:135], a[20:23], v[108:111], v[132:135]\n"
      "v_fmac_f32 v184, v124, v124\n"
      "v_fmac_f32 v185, v115, v115\n"
      "v_mfma_f32_16x16x32_bf16 v[220:223], a[24:27], v[104:107], 0\n"
      "v_fmac_f32 v184, v125, v125\n"
      "v_fmac_f32 v185, v126, v126\n"
      "v_mfma_f32_16x16x32_bf16 v[220:223], a[28:31], v[186:189], v[220:223]\n"
      "s_waitcnt lgkmcnt(0)\n"
      "v_fmac_f32 v184, v180, v180\n"
      "v_fmac_f32 v185, v127, v127\n"
      "v_mfma_f32_16x16x32_bf16 v[220:223], a[32:35], v[206:209], v[220:223]\n"
      "buffer_store_dwordx4 v[144:147], v21, s[20:23], s46 offen sc1\n"
      "v_mfma_f32_16x16x32_bf16 v[220:223], a[36:39], v[162:165], v[220:223]\n"
      "v_fmac_f32 v184, v181, v181\n"
      "v_fmac_f32 v185, v128, v128\n"
      "v_mfma_f32_16x16x32_bf16 v[220:223], a[40:43], v[140:143], v[220:223]\n"
      "v_fmac_f32 v184, v182, v182\n"
      "v_fmac_f32 v185, v129, v129\n"
      "v_mfma_f32_16x16x32_bf16 v[220:223], a[44:47], v[108:111], v[220:223]\n"
      "v_fmac_f32_e64 v184, v183, v183\n"
      "v_fma_f32 v120, v132, v132, s51\n"
      "v_mfma_f32_16x16x32_bf16 v[210:213], a[48:51], v[104:107], 0\n"
      "v_permlane16_swap_b32 v185, v184\n"
      "v_mfma_f32_16x16x32_bf16 v[210:213], a[52:55], v[186:189], v[210:213]\n"
      "v_add_f32 v185, v185, v184\n"
      "v_mov_b32_e64 v184, v185\n"
      "v_mfma_f32_16x16x32_bf16 v[210:213], a[56:59], v[206:209], v[210:213]\n"
      "v_fmac_f32 v120, v133, v133\n"
      "v_permlane32_swap_b32 v185, v184\n"
      "v_mfma_f32_16x16x32_bf16 v[210:213], a[60:63], v[162:165], v[210:213]\n"
      "v_add_f32 v185, v185, v184\n"
      "v_rsq_f32 v185, v185\n"
      "v_mfma_f32_16x16x32_bf16 v[210:213], a[64:67], v[140:143], v[210:213]\n"
      "buffer_store_dwordx4 v[158:161], v22, s[20:23], s46 offen sc1\n"
      "v_mfma_f32_16x16x32_bf16 v[210:213], a[68:71], v[108:111], v[210:213]\n"
      "v_mov_b32 v184, v185\n"
      "v_fmac_f32 v120, v134, v134\n"
      "v_mfma_f32_16x16x32_bf16 v[144:147], a[72:75], v[104:107], 0\n"
      "v_permlane16_swap_b32 v185, v184\n"
      "v_mfma_f32_16x16x32_bf16 v[144:147], a[76:79], v[186:189], v[144:147]\n"
      "v_mul_f32 v185, s50, v185\n"
      "v_mul_f32_e64 v152, v184, v152\n"
      "v_mfma_f32_16x16x32_bf16 v[144:147], a[80:83], v[206:209], v[144:147]\n"
      "v_mul_f32 v153, v184, v153\n"
      "v_mul_f32 v166, v185, v166\n"
      "v_mfma_f32_16x16x32_bf16 v[144:147], a[84:87], v[162:165], v[144:147]\n"
      "v_mul_f32_e64 v167, v185, v167\n"
      "v_cvt_pk_bf16_f32 v152, v152, v153\n"
      "v_mfma_f32_16x16x32_bf16 v[144:147], a[88:91], v[140:143], v[144:147]\n"
      "v_mul_f32 v154, v184, v154\n"
      "v_mul_f32 v155, v184, v155\n"
      "v_mfma_f32_16x16x32_bf16 v[144:147], a[92:95], v[108:111], v[144:147]\n"
      "v_mul_f32 v180, v184, v180\n"
      "v_mul_f32 v181, v184, v181\n"
      "v_mfma_f32_16x16x32_bf16 v[156:159], a[96:99], v[104:107], 0\n"
      "buffer_store_dwordx4 v[190:193], v23, s[20:23], s46 offen sc1\n"
      "v_mfma_f32_16x16x32_bf16 v[156:159], a[100:103], v[186:189], v[156:159]\n"
      "v_cvt_pk_bf16_f32 v166, v166, v167\n"
      "v_mul_f32 v168, v185, v168\n"
      "v_mfma_f32_16x16x32_bf16 v[156:159], a[104:107], v[206:209], v[156:159]\n"
      "v_mul_f32 v169, v185, v169\n"
      "v_mul_f32_e64 v126, v185, v126\n"
      "v_mfma_f32_16x16x32_bf16 v[156:159], a[108:111], v[162:165], v[156:159]\n"
      "v_mul_f32 v127, v185, v127\n"
      "v_cvt_pk_bf16_f32 v153, v154, v155\n"
      "v_mfma_f32_16x16x32_bf16 v[156:159], a[112:115], v[140:143], v[156:159]\n"
      "v_mul_f32 v122, v184, v122\n"
      "v_mul_f32 v123, v184, v123\n"
      "v_mfma_f32_16x16x32_bf16 v[156:159], a[116:119], v[108:111], v[156:159]\n"
      "v_mul_f32 v124, v184, v124\n"
      "v_mul_f32_e64 v125, v184, v125\n"
      "v_mfma_f32_16x16x32_bf16 v[190:193], a[120:123], v[104:107], 0\n"
      "v_cvt_pk_bf16_f32 v180, v180, v181\n"
      "v_mul_f32 v182, v184, v182\n"
      "v_mfma_f32_16x16x32_bf16 v[190:193], a[124:127], v[186:189], v[190:193]\n"
      "v_mul_f32 v183, v184, v183\n"
      "v_cvt_pk_bf16_f32 v167, v168, v169\n"
      "v_mfma_f32_16x16x32_bf16 v[190:193], a[128:131], v[206:209], v[190:193]\n"
      "buffer_store_dwordx4 v[172:175], v25, s[20:23], s46 offen sc1\n"
      "v_mfma_f32_16x16x32_bf16 v[190:193], a[132:135], v[162:165], v[190:193]\n"
      "v_mul_f32 v112, v185, v112\n"
      "v_mul_f32 v113, v185, v113\n"
      "v_mfma_f32_16x16x32_bf16 v[190:193], a[136:139], v[140:143], v[190:193]\n"
      "v_mul_f32 v114, v185, v114\n"
      "v_mul_f32 v115, v185, v115\n"
      "v_mfma_f32_16x16x32_bf16 v[190:193], a[140:143], v[108:111], v[190:193]\n"
      "v_cvt_pk_bf16_f32 v126, v126, v127\n"
      "v_mul_f32 v128, v185, v128\n"
      "v_mfma_f32_16x16x32_bf16 v[172:175], v[104:107], a[144:147], 0\n"
      "v_mul_f32 v129, v185, v129\n"
      "v_cvt_pk_bf16_f32 v154, v122, v123\n"
      "v_mfma_f32_16x16x32_bf16 v[172:175], v[186:189], a[148:151], v[172:175]\n"
      "v_cvt_pk_bf16_f32 v155, v124, v125\n"
      "v_cvt_pk_bf16_f32 v181, v182, v183\n"
      "v_mfma_f32_16x16x32_bf16 v[172:175], v[206:209], a[152:155], v[172:175]\n"
      "v_cvt_pk_bf16_f32 v168, v112, v113\n"
      "v_cvt_pk_bf16_f32 v169, v114, v115\n"
      "v_mfma_f32_16x16x32_bf16 v[172:175], v[162:165], a[156:159], v[172:175]\n"
      "ds_read_b128 v[182:185], v2\n"
      "v_cvt_pk_bf16_f32 v127, v128, v129\n"
      "v_mfma_f32_16x16x32_bf16 v[172:175], v[140:143], a[160:163], v[172:175]\n"
      "ds_read_b128 v[122:125], v2 offset:3072\n"
      "v_fma_f32 v114, v144, v144, s51\n"
      "v_mfma_f32_16x16x32_bf16 v[172:175], v[108:111], a[164:167], v[172:175]\n"
      "ds_read_b128 v[128:131], v2 offset:6144\n"
      "v_fmac_f32 v114, v145, v145\n"
      "v_fmac_f32 v114, v146, v146\n"
      "v_fmac_f32 v120, v135, v135\n"
      "v_fmac_f32 v114, v147, v147\n"
      "v_fmac_f32 v120, v220, v220\n"
      "v_fmac_f32 v114, v156, v156\n"
      "v_fmac_f32 v120, v221, v221\n"
      "v_fmac_f32 v114, v157, v157\n"
      "v_fmac_f32 v120, v222, v222\n"
      "v_fmac_f32 v114, v158, v158\n"
      "v_fmac_f32 v120, v223, v223\n"
      "v_fmac_f32 v114, v159, v159\n"
      "v_fmac_f32 v120, v210, v210\n"
      "v_fmac_f32 v114, v190, v190\n"
      "v_fmac_f32 v120, v211, v211\n"
      "v_fmac_f32 v114, v191, v191\n"
      "v_fmac_f32 v120, v212, v212\n"
      "v_fmac_f32 v114, v192, v192\n"
      "v_fmac_f32 v120, v213, v213\n"
      "v_fmac_f32 v114, v193, v193\n"
      "v_lshl_add_u32 v219, v76, 3, v219\n"
      "v_add_u32 v219, s77, v219\n"
      "v_permlane16_swap_b32 v120, v114\n"
      "v_add_f32 v120, v120, v114\n"
      "v_mov_b32 v114, v120\n"
      "v_and_b32 v112, 2, v219\n"
      "v_and_b32 v219, -4, v219\n"
      "v_permlane32_swap_b32 v120, v114\n"
      "v_add_f32 v120, v120, v114\n"
      "v_rsq_f32 v120, v120\n"
      "v_lshlrev_b32 v76, 2, v76\n"
      "v_mov_b32 v114, v120\n"
      "v_sub_u32 v77, v77, v76\n"
      "v_add_u32 v218, 110592, v219\n"
      "v_permlane16_swap_b32 v120, v114\n"
      "v_mul_f32 v120, s50, v120\n"
      "v_mul_f32 v144, v114, v144\n"
      "v_mul_f32 v145, v114, v145\n"
      "v_mul_f32 v132, v120, v132\n"
      "v_mul_f32 v133, v120, v133\n"
      "v_cvt_pk_bf16_f32 v144, v144, v145\n"
      "v_mul_f32 v146, v114, v146\n"
      "v_mul_f32 v147, v114, v147\n"
      "v_mul_f32 v190, v114, v190\n"
      "v_mul_f32 v191, v114, v191\n"
      "v_cvt_pk_bf16_f32 v132, v132, v133\n"
      "v_mul_f32 v134, v120, v134\n"
      "v_mul_f32 v135, v120, v135\n"
      "v_mul_f32 v210, v120, v210\n"
      "v_mul_f32 v211, v120, v211\n"
      "v_cvt_pk_bf16_f32 v145, v146, v147\n"
      "v_mul_f32 v156, v114, v156\n"
      "v_mul_f32 v157, v114, v157\n"
      "v_mul_f32 v158, v114, v158\n"
      "v_mul_f32 v159, v114, v159\n"
      "v_cvt_pk_bf16_f32 v190, v190, v191\n"
      "v_mul_f32 v192, v114, v192\n"
      "v_mul_f32 v193, v114, v193\n"
      "v_cvt_pk_bf16_f32 v133, v134, v135\n"
      "v_mul_f32 v220, v120, v220\n"
      "v_mul_f32 v221, v120, v221\n"
      "v_mul_f32 v222, v120, v222\n"
      "v_mul_f32 v223, v120, v223\n"
      "v_cvt_pk_bf16_f32 v210, v210, v211\n"
      "v_mul_f32_e64 v212, v120, v212\n"
      "v_mul_f32_e64 v213, v120, v213\n"
      "v_cvt_pk_bf16_f32 v146, v156, v157\n"
      "v_mfma_f32_16x16x32_bf16 v[114:117], v[104:107], a[168:171], 0\n"
      "v_cvt_pk_bf16_f32 v147, v158, v159\n"
      "v_cvt_pk_bf16_f32 v191, v192, v193\n"
      "v_mfma_f32_16x16x32_bf16 v[114:117], v[186:189], a[172:175], v[114:117]\n"
      "v_cvt_pk_bf16_f32 v134, v220, v221\n"
      "v_cvt_pk_bf16_f32 v135, v222, v223\n"
      "v_mfma_f32_16x16x32_bf16 v[114:117], v[206:209], a[176:179], v[114:117]\n"
      "v_cvt_pk_bf16_f32 v211, v212, v213\n"
      "v_lshlrev_b32_e64 v77, 4, v77\n"
      "v_mfma_f32_16x16x32_bf16 v[114:117], v[162:165], a[180:183], v[114:117]\n"
      "v_cmp_gt_u32 s[72:73], 64, v77\n"
      "v_mov_b32 v212, 0x3fb9\n"
      "v_mfma_f32_16x16x32_bf16 v[114:117], v[140:143], a[184:187], v[114:117]\n"
      "v_mov_b32_e64 v213, 0\n"
      "v_lshlrev_b64 v[212:213], v77, v[212:213]\n"
      "v_mfma_f32_16x16x32_bf16 v[114:117], v[108:111], a[188:191], v[114:117]\n"
      "v_cndmask_b32_e64 v212, 0, v212, s[72:73]\n"
      "v_cndmask_b32_e64 v213, 0, v213, s[72:73]\n"
      "v_mfma_f32_16x16x32_bf16 v[156:159], v[104:107], a[192:195], 0\n"
      "ds_read_b128 v[104:107], v2 offset:9216\n"
      "v_cmp_gt_u32 s[72:73], 64, v77\n"
      "v_mfma_f32_16x16x32_bf16 v[156:159], v[186:189], a[196:199], v[156:159]\n"
      "ds_read_b128 v[186:189], v2 offset:12288\n"
      "v_mov_b32 v192, 0xbb2b\n"
      "v_mfma_f32_16x16x32_bf16 v[156:159], v[206:209], a[200:203], v[156:159]\n"
      "ds_read_b128 v[206:209], v2 offset:15360\n"
      "v_mov_b32 v193, 0\n"
      "v_mfma_f32_16x16x32_bf16 v[156:159], v[162:165], a[204:207], v[156:159]\n"
      "s_waitcnt lgkmcnt(3)\n"
      "v_lshlrev_b64 v[192:193], v77, v[192:193]\n"
      "v_cndmask_b32_e64 v192, 0, v192, s[72:73]\n"
      "v_mfma_f32_16x16x32_bf16 v[156:159], v[140:143], a[208:211], v[156:159]\n"
      "v_cndmask_b32_e64 v193, 0, v193, s[72:73]\n"
      "v_add_u32 v113, 112160, v219\n"
      "v_mfma_f32_16x16x32_bf16 v[156:159], v[108:111], a[212:215], v[156:159]\n"
      "v_cvt_pk_bf16_f32 v198, v198, v199\n"
      "v_cvt_pk_bf16_f32 v199, v200, v201\n"
      "v_mfma_f32_16x16x32_bf16 v[140:143], a[216:219], v[182:185], 0\n"
      "v_cvt_pk_bf16_f32 v200, v96, v97\n"
      "v_cvt_pk_bf16_f32 v201, v98, v99\n"
      "v_mfma_f32_16x16x32_bf16 v[140:143], a[220:223], v[122:125], v[140:143]\n"
      "v_cvt_pk_bf16_f32 v76, v172, v173\n"
      "v_cvt_pk_bf16_f32 v77, v174, v175\n"
      "v_mfma_f32_16x16x32_bf16 v[140:143], a[224:227], v[128:131], v[140:143]\n"
      "v_cvt_pk_bf16_f32 v214, v214, v215\n"
      "v_cvt_pk_bf16_f32 v215, v216, v217\n"
      "v_mfma_f32_16x16x32_bf16 v[220:223], a[240:243], v[182:185], 0\n"
      "v_cvt_pk_bf16_f32 v216, v100, v101\n"
      "v_cvt_pk_bf16_f32 v217, v102, v103\n"
      "v_mfma_f32_16x16x32_bf16 v[220:223], a[244:247], v[122:125], v[220:223]\n"
      "v_cvt_pk_bf16_f32 v80, v114, v115\n"
      "v_cvt_pk_bf16_f32 v81, v116, v117\n"
      "v_mfma_f32_16x16x32_bf16 v[220:223], a[248:251], v[128:131], v[220:223]\n"
      "s_waitcnt lgkmcnt(0)\n"
      "v_cvt_pk_bf16_f32 v194, v194, v195\n"
      "v_cvt_pk_bf16_f32 v195, v196, v197\n"
      "v_mfma_f32_16x16x32_bf16 v[160:163], v[232:235], v[182:185], 0\n"
      "ds_read_b128 v[108:111], v10 offset:1200\n"
      "v_cvt_pk_bf16_f32 v196, v202, v203\n"
      "v_mfma_f32_16x16x32_bf16 v[160:163], v[236:239], v[122:125], v[160:163]\n"
      "v_cvt_pk_bf16_f32 v197, v204, v205\n"
      "v_cvt_pk_bf16_f32 v84, v156, v157\n"
      "v_mfma_f32_16x16x32_bf16 v[160:163], v[240:243], v[128:131], v[160:163]\n"
      "v_cvt_pk_bf16_f32 v85, v158, v159\n"
      "v_add_u32 v93, 113728, v219\n"
      "v_mfma_f32_16x16x32_bf16 v[140:143], a[228:231], v[104:107], v[140:143]\n"
      "v_subrev_u32 v39, 115456, v9\n"
      "v_lshrrev_b32 v39, 3, v39\n"
      "v_mfma_f32_16x16x32_bf16 v[140:143], a[232:235], v[186:189], v[140:143]\n"
      "ds_read_b64 v[120:121], v10 offset:1192\n"
      "v_min_u32 v39, 96, v39\n"
      "v_mfma_f32_16x16x32_bf16 v[140:143], a[236:239], v[206:209], v[140:143]\n"
      "ds_read2_b32 v[184:185], v218 offset0:0 offset1:1\n"
      "v_add_u32_e64 v39, s77, v39\n"
      "v_mfma_f32_16x16x32_bf16 v[220:223], a[252:255], v[104:107], v[220:223]\n"
      "ds_read2_b32 v[204:205], v218 offset0:2 offset1:10\n"
      "v_add_u32 v39, 115296, v39\n"
      "v_mfma_f32_16x16x32_bf16 v[220:223], v[224:227], v[186:189], v[220:223]\n"
      "ds_read2_b32 v[182:183], v218 offset0:8 offset1:9\n"
      "v_readlane_b32 s45, v37, s43\n"
      "v_mfma_f32_16x16x32_bf16 v[220:223], v[228:231], v[206:209], v[220:223]\n"
      "ds_read2_b32 v[156:157], v218 offset0:16 offset1:17\n"
      "v_mul_f32 v219, v140, v140\n"
      "v_mfma_f32_16x16x32_bf16 v[160:163], v[244:247], v[104:107], v[160:163]\n"
      "ds_read2_b32 v[104:105], v218 offset0:24 offset1:18\n"
      "v_add_f32 v218, v140, v141\n"
      "v_mfma_f32_16x16x32_bf16 v[160:163], v[248:251], v[186:189], v[160:163]\n"
      "v_fmac_f32 v219, v141, v141\n"
      "v_add_f32 v218, v218, v142\n"
      "v_mfma_f32_16x16x32_bf16 v[160:163], v[252:255], v[206:209], v[160:163]\n"
      "s_waitcnt lgkmcnt(4)\n"
      "v_alignbyte_b32 v184, v185, v184, v112\n"
      "v_fmac_f32 v219, v142, v142\n"
      "v_mfma_f32_16x16x32_bf16 v[100:103], v[136:139], v[148:151], v[40:43]\n"
      "s_waitcnt lgkmcnt(3)\n"
      "v_alignbyte_b32 v185, v204, v185, v112\n"
      "v_add_f32 v218, v218, v143\n"
      "v_mfma_f32_16x16x32_bf16 v[96:99], v[152:155], v[148:151], v[44:47]\n"
      "ds_read_b128 v[128:131], v2 offset:256\n"
      "s_waitcnt lgkmcnt(3)\n"
      "v_alignbyte_b32 v182, v183, v182, v112\n"
      "v_mfma_f32_16x16x32_bf16 v[172:175], v[144:147], v[148:151], v[48:51]\n"
      "ds_read_b128 v[206:209], v2 offset:3328\n"
      "v_alignbyte_b32 v183, v205, v183, v112\n"
      "v_mfma_f32_16x16x32_bf16 v[114:117], v[108:111], v[148:151], v[24:27]\n"
      "ds_read_b128 v[202:205], v2 offset:6400\n"
      "s_waitcnt lgkmcnt(3)\n"
      "v_alignbyte_b32 v156, v157, v156, v112\n"
      "v_mfma_f32_16x16x16_bf16 v[100:103], v[170:171], v[118:119], v[100:103]\n"
      "v_alignbyte_b32 v157, v105, v157, v112\n"
      "v_alignbyte_b32 v104, v104, v104, v112\n"
      "v_fmac_f32 v219, v143, v143\n"
      "v_mfma_f32_16x16x16_bf16 v[96:99], v[180:181], v[118:119], v[96:99]\n"
      "v_add_f32 v218, v218, v220\n"
      "v_fmac_f32 v219, v220, v220\n"
      "v_add_f32 v218, v218, v221\n"
      "v_mfma_f32_16x16x16_bf16 v[172:175], v[190:191], v[118:119], v[172:175]\n"
      "v_fmac_f32 v219, v221, v221\n"
      "v_add_f32 v218, v218, v222\n"
      "v_fmac_f32 v219, v222, v222\n"
      "v_mfma_f32_16x16x16_bf16 v[114:117], v[120:121], v[118:119], v[114:117]\n"
      "v_add_f32 v218, v218, v223\n"
      "v_fmac_f32 v219, v223, v223\n"
      "v_add_f32 v218, v218, v160\n"
      "v_mfma_f32_16x16x16_bf16 v[100:103], v[212:213], v[184:185], v[100:103]\n"
      "v_fmac_f32 v219, v160, v160\n"
      "v_add_f32 v218, v218, v161\n"
      "v_fmac_f32 v219, v161, v161\n"
      "v_mfma_f32_16x16x16_bf16 v[96:99], v[212:213], v[182:183], v[96:99]\n"
      "v_add_f32 v218, v218, v162\n"
      "v_fmac_f32 v219, v162, v162\n"
      "v_add_f32 v218, v218, v163\n"
      "v_mfma_f32_16x16x16_bf16 v[172:175], v[212:213], v[156:157], v[172:175]\n"
      "v_fmac_f32 v219, v163, v163\n"
      "s_waitcnt vmcnt(6)\n"
      "v_or3_b32 v176, v176, v177, v178\n"
      "v_readlane_b32 s46, v37, s42\n"
      "v_mfma_f32_16x16x16_bf16 v[114:117], v[212:213], v[104:105], v[114:117]\n"
      "v_permlane16_swap_b32 v218, v219\n"
      "v_add_f32 v218, v218, v219\n"
      "v_mfma_f32_16x16x16_bf16 v[100:103], v[192:193], v[184:185], v[100:103]\n"
      "s_waitcnt vmcnt(5)\n"
      "v_or3_b32 v176, v176, v179, v78\n"
      "v_and_b32 v176, 0x7fff7fff, v176\n"
      "v_readlane_b32 s47, v38, s43\n"
      "v_mfma_f32_16x16x16_bf16 v[96:99], v[192:193], v[182:183], v[96:99]\n"
      "ds_write_b32 v31, v176 offset:1024\n"
      "v_mfma_f32_16x16x16_bf16 v[172:175], v[192:193], v[156:157], v[172:175]\n"
      "s_or_b32 s95, s45, s46\n"
      "s_or_b32 s95, s95, s48\n"
      "v_max3_f32 v78, v100, v101, v102\n"
      "v_mfma_f32_16x16x16_bf16 v[114:117], v[192:193], v[104:105], v[114:117]\n"
      "s_cmp_ge_u32 s95, 0x40000000\n"
      "s_cselect_b32 s94, -1, -2\n"
      "v_max3_f32 v105, v103, v96, v97\n"
      "s_waitcnt lgkmcnt(1)\n"
      "v_mfma_f32_16x16x32_bf16 v[186:189], a[216:219], v[128:131], 0\n"
      "s_cmp_gt_u32 s42, s44\n"
      "s_cselect_b32 s95, s94, s95\n"
      "v_max3_f32 v78, v78, v98, v99\n"
      "v_mfma_f32_16x16x32_bf16 v[186:189], a[220:223], v[206:209], v[186:189]\n"
      "ds_read2_b32 v[124:125], v113 offset0:0 offset1:1\n"
      "v_max3_f32 v105, v105, v172, v173\n"
      "v_mfma_f32_16x16x32_bf16 v[186:189], a[224:227], v[202:205], v[186:189]\n"
      "ds_read2_b32 v[164:165], v113 offset0:2 offset1:10\n"
      "v_max3_f32 v78, v78, v174, v175\n"
      "v_mfma_f32_16x16x32_bf16 v[182:185], a[240:243], v[128:131], 0\n"
      "ds_read_b128 v[176:179], v9 offset:4608\n"
      "v_max3_f32 v78, v78, v105, v114\n"
      "v_mfma_f32_16x16x32_bf16 v[182:185], a[244:247], v[206:209], v[182:185]\n"
      "ds_read2_b32 v[116:117], v113 offset0:8 offset1:9\n"
      "v_mov_b32_e64 v115, v78\n"
      "v_mfma_f32_16x16x32_bf16 v[182:185], a[248:251], v[202:205], v[182:185]\n"
      "ds_read2_b32 v[118:119], v113 offset0:16 offset1:17\n"
      "v_permlane16_swap_b32 v78, v115\n"
      "v_mfma_f32_16x16x32_bf16 v[104:107], v[232:235], v[128:131], 0\n"
      "ds_read2_b32 v[122:123], v113 offset0:24 offset1:18\n"
      "v_max_f32 v78, v78, v115\n"
      "v_mfma_f32_16x16x32_bf16 v[104:107], v[236:239], v[206:209], v[104:107]\n"
      "s_and_b32 s47, s47, -4\n"
      "s_waitcnt lgkmcnt(5)\n"
      "v_alignbyte_b32 v124, v125, v124, v112\n"
      "v_mfma_f32_16x16x32_bf16 v[104:107], v[240:243], v[202:205], v[104:107]\n"
      "s_add_u32 s42, s42, 1\n"
      "s_waitcnt lgkmcnt(4)\n"
      "v_alignbyte_b32 v125, v164, v125, v112\n"
      "v_mfma_f32_16x16x32_bf16 v[156:159], v[136:139], v[166:169], v[52:55]\n"
      "s_add_u32 s43, s43, 1\n"
      "v_mov_b32 v115, v78\n"
      "s_waitcnt lgkmcnt(3)\n"
      "v_or3_b32 v176, v176, v177, v178\n"
      "v_mfma_f32_16x16x32_bf16 v[128:131], v[152:155], v[166:169], v[56:59]\n"
      "s_waitcnt lgkmcnt(2)\n"
      "v_alignbyte_b32 v116, v117, v116, v112\n"
      "v_alignbyte_b32 v117, v165, v117, v112\n"
      "v_mfma_f32_16x16x32_bf16 v[206:209], v[144:147], v[166:169], v[60:63]\n"
      "s_waitcnt lgkmcnt(1)\n"
      "v_alignbyte_b32 v118, v119, v118, v112\n"
      "v_permlane32_swap_b32 v78, v115\n"
      "v_mfma_f32_16x16x32_bf16 v[202:205], v[108:111], v[166:169], v[26:29]\n"
      "s_waitcnt lgkmcnt(0)\n"
      "v_alignbyte_b32 v119, v123, v119, v112\n"
      "v_alignbyte_b32 v122, v122, v122, v112\n"
      "v_mfma_f32_16x16x16_bf16 v[156:159], v[170:171], v[126:127], v[156:159]\n"
      "v_max_f32 v78, v78, v115\n"
      "v_sub_f32 v100, v100, v78\n"
      "v_sub_f32 v101, v101, v78\n"
      "v_mfma_f32_16x16x16_bf16 v[128:131], v[180:181], v[126:127], v[128:131]\n"
      "v_sub_f32 v102, v102, v78\n"
      "v_sub_f32 v103, v103, v78\n"
      "v_exp_f32 v100, v100\n"
      "v_mfma_f32_16x16x16_bf16 v[206:209], v[190:191], v[126:127], v[206:209]\n"
      "v_exp_f32 v101, v101\n"
      "v_sub_f32 v96, v96, v78\n"
      "v_mfma_f32_16x16x16_bf16 v[202:205], v[120:121], v[126:127], v[202:205]\n"
      "v_sub_f32 v97, v97, v78\n"
      "v_sub_f32 v98, v98, v78\n"
      "v_sub_f32 v99, v99, v78\n"
      "v_mfma_f32_16x16x16_bf16 v[156:159], v[212:213], v[124:125], v[156:159]\n"
      "v_exp_f32 v102, v102\n"
      "v_exp_f32 v103, v103\n"
      "v_mfma_f32_16x16x16_bf16 v[128:131], v[212:213], v[116:117], v[128:131]\n"
      "v_exp_f32 v96, v96\n"
      "v_exp_f32 v97, v97\n"
      "v_mfma_f32_16x16x16_bf16 v[206:209], v[212:213], v[118:119], v[206:209]\n"
      "v_exp_f32 v98, v98\n"
      "v_exp_f32 v99, v99\n"
      "v_mfma_f32_16x16x16_bf16 v[202:205], v[212:213], v[122:123], v[202:205]\n"
      "v_cvt_pk_bf16_f32 v100, v100, v101\n"
      "v_cvt_pk_bf16_f32 v101, v102, v103\n"
      "v_cvt_pk_bf16_f32 v102, v96, v97\n"
      "v_mfma_f32_16x16x16_bf16 v[156:159], v[192:193], v[124:125], v[156:159]\n"
      "v_cvt_pk_bf16_f32 v103, v98, v99\n"
      "v_sub_f32 v172, v172, v78\n"
      "v_sub_f32 v173, v173, v78\n"
      "v_mfma_f32_16x16x16_bf16 v[128:131], v[192:193], v[116:117], v[128:131]\n"
      "v_sub_f32 v174, v174, v78\n"
      "v_sub_f32 v175, v175, v78\n"
      "v_sub_f32 v114, v114, v78\n"
      "v_mfma_f32_16x16x16_bf16 v[206:209], v[192:193], v[118:119], v[206:209]\n"
      "v_exp_f32 v172, v172\n"
      "v_exp_f32_e64 v173, v173\n"
      "v_mfma_f32_16x16x16_bf16 v[202:205], v[192:193], v[122:123], v[202:205]\n"
      "ds_read_b128 v[96:99], v2 offset:9472\n"
      "ds_read_b128 v[124:127], v2 offset:12544\n"
      "ds_read_b128 v[164:167], v2 offset:15616\n"
      "v_exp_f32 v174, v174\n"
      "v_exp_f32 v175, v175\n"
      "v_exp_f32_e64 v114, v114\n"
      "v_cvt_pk_bf16_f32 v88, v172, v173\n"
      "v_cvt_pk_bf16_f32 v89, v174, v175\n"
      "v_cvt_pk_bf16_f32 v90, v114, 0\n"
      "v_max3_f32 v114, v156, v157, v158\n"
      "v_max3_f32 v78, v159, v128, v129\n"
      "v_max3_f32 v114, v114, v130, v131\n"
      "v_max3_f32 v78, v78, v206, v207\n"
      "v_max3_f32 v114, v114, v208, v209\n"
      "v_max3_f32 v114, v114, v78, v202\n"
      "v_mov_b32 v175, v114\n"
      "v_or_b32 v176, v176, v179\n"
      "v_cmp_ne_u32 s[56:57], 0, v176\n"
      "s_waitcnt lgkmcnt(2)\n"
      "v_mfma_f32_16x16x32_bf16 v[186:189], a[228:231], v[96:99], v[186:189]\n"
      "ds_read_u16 v78, v11 offset:1168\n"
      "v_permlane16_swap_b32_e64 v114, v175\n"
      "s_waitcnt lgkmcnt(2)\n"
      "v_mfma_f32_16x16x32_bf16 v[186:189], a[232:235], v[124:127], v[186:189]\n"
      "s_waitcnt lgkmcnt(1)\n"
      "v_max_f32 v114, v114, v175\n"
      "v_mov_b32_e64 v175, v114\n"
      "v_mfma_f32_16x16x32_bf16 v[186:189], a[236:239], v[164:167], v[186:189]\n"
      "ds_read_u16 v82, v11 offset:1200\n"
      "v_permlane32_swap_b32 v114, v175\n"
      "v_mfma_f32_16x16x32_bf16 v[182:185], a[252:255], v[96:99], v[182:185]\n"
      "ds_read_u16 v86, v11 offset:1232\n"
      "v_max_f32 v114, v114, v175\n"
      "v_mfma_f32_16x16x32_bf16 v[182:185], v[224:227], v[124:127], v[182:185]\n"
      "s_cmp_ge_u32 s95, 0x40000000\n"
      "s_cselect_b64 vcc, -1, s[56:57]\n"
      "v_sub_f32 v156, v156, v114\n"
      "v_mfma_f32_16x16x32_bf16 v[182:185], v[228:231], v[164:167], v[182:185]\n"
      "v_sub_f32 v157, v157, v114\n"
      "v_sub_f32 v158, v158, v114\n"
      "v_mfma_f32_16x16x32_bf16 v[104:107], v[244:247], v[96:99], v[104:107]\n"
      "v_sub_f32 v159, v159, v114\n"
      "v_exp_f32 v156, v156\n"
      "v_mfma_f32_16x16x32_bf16 v[104:107], v[248:251], v[124:127], v[104:107]\n"
      "v_exp_f32 v157, v157\n"
      "v_mfma_f32_16x16x32_bf16 v[104:107], v[252:255], v[164:167], v[104:107]\n"
      "s_waitcnt lgkmcnt(1)\n"
      "v_sub_f32 v128, v128, v114\n"
      "v_sub_f32_e64 v129, v129, v114\n"
      "v_mfma_f32_16x16x32_bf16 v[174:177], v[32:35], v[100:103], 0\n"
      "ds_read2_b32 v[96:97], v93 offset0:0 offset1:1\n"
      "v_sub_f32 v130, v130, v114\n"
      "v_mfma_f32_16x16x32_bf16 v[174:177], v[32:35], v[88:91], v[174:177]\n"
      "ds_read2_b32 v[126:127], v93 offset0:2 offset1:10\n"
      "v_sub_f32 v131, v131, v114\n"
      "v_mfma_f32_16x16x32_bf16 v[164:167], v[198:201], v[100:103], 0\n"
      "ds_read2_b32 v[98:99], v93 offset0:8 offset1:9\n"
      "v_exp_f32_e64 v158, v158\n"
      "v_mfma_f32_16x16x32_bf16 v[164:167], v[76:79], v[88:91], v[164:167]\n"
      "ds_read2_b32 v[172:173], v93 offset0:16 offset1:17\n"
      "v_exp_f32_e64 v159, v159\n"
      "v_mfma_f32_16x16x32_bf16 v[176:179], v[214:217], v[100:103], 0\n"
      "ds_read2_b32 v[204:205], v93 offset0:24 offset1:18\n"
      "v_exp_f32_e64 v128, v128\n"
      "v_mfma_f32_16x16x32_bf16 v[176:179], v[80:83], v[88:91], v[176:179]\n"
      "s_waitcnt lgkmcnt(5)\n"
      "s_waitcnt lgkmcnt(4)\n"
      "v_alignbyte_b32 v96, v97, v96, v112\n"
      "v_mfma_f32_16x16x32_bf16 v[116:119], v[194:197], v[100:103], 0\n"
      "v_exp_f32 v129, v129\n"
      "v_mfma_f32_16x16x32_bf16 v[116:119], v[84:87], v[88:91], v[116:119]\n"
      "s_waitcnt lgkmcnt(3)\n"
      "v_alignbyte_b32 v97, v126, v97, v112\n"
      "s_waitcnt lgkmcnt(2)\n"
      "v_alignbyte_b32 v98, v99, v98, v112\n"
      "v_mfma_f32_16x16x32_bf16 v[122:125], v[136:139], v[132:135], v[64:67]\n"
      "v_alignbyte_b32 v99, v127, v99, v112\n"
      "s_waitcnt lgkmcnt(1)\n"
      "v_alignbyte_b32 v172, v173, v172, v112\n"
      "v_exp_f32 v130, v130\n"
      "s_waitcnt lgkmcnt(0)\n"
      "v_alignbyte_b32 v173, v205, v173, v112\n"
      "v_alignbyte_b32 v204, v204, v204, v112\n"
      "v_exp_f32_e64 v131, v131\n"
      "v_cvt_pk_bf16_f32 v156, v156, v157\n"
      "v_cvt_pk_bf16_f32 v157, v158, v159\n"
      "v_cvt_pk_bf16_f32 v158, v128, v129\n"
      "v_mfma_f32_16x16x32_bf16 v[126:129], v[152:155], v[132:135], v[68:71]\n"
      "v_cvt_pk_bf16_f32 v159, v130, v131\n"
      "v_sub_f32 v206, v206, v114\n"
      "v_sub_f32 v207, v207, v114\n"
      "v_sub_f32 v208, v208, v114\n"
      "v_sub_f32 v209, v209, v114\n"
      "v_sub_f32 v202, v202, v114\n"
      "v_mfma_f32_16x16x32_bf16 v[148:151], v[144:147], v[132:135], v[72:75]\n"
      "v_exp_f32 v206, v206\n"
      "v_mfma_f32_16x16x32_bf16 v[112:115], v[108:111], v[132:135], v[28:31]\n"
      "v_exp_f32_e64 v207, v207\n"
      "v_mfma_f32_16x16x16_bf16 v[122:125], v[170:171], v[210:211], v[122:125]\n"
      "v_exp_f32 v208, v208\n"
      "v_exp_f32 v209, v209\n"
      "v_mfma_f32_16x16x16_bf16 v[126:129], v[180:181], v[210:211], v[126:129]\n"
      "v_exp_f32_e64 v202, v202\n"
      "v_cvt_pk_bf16_f32 v92, v206, v207\n"
      "v_mfma_f32_16x16x16_bf16 v[148:151], v[190:191], v[210:211], v[148:151]\n"
      "v_cvt_pk_bf16_f32 v93, v208, v209\n"
      "v_cvt_pk_bf16_f32 v94, v202, 0\n"
      "v_mul_f32 v203, v186, v186\n"
      "v_mfma_f32_16x16x16_bf16 v[112:115], v[120:121], v[210:211], v[112:115]\n"
      "v_add_f32 v202, v186, v187\n"
      "v_fmac_f32 v203, v187, v187\n"
      "v_add_f32 v202, v202, v188\n"
      "v_mfma_f32_16x16x16_bf16 v[122:125], v[212:213], v[96:97], v[122:125]\n"
      "v_fmac_f32 v203, v188, v188\n"
      "v_add_f32 v202, v202, v189\n"
      "v_fmac_f32 v203, v189, v189\n"
      "v_mfma_f32_16x16x16_bf16 v[126:129], v[212:213], v[98:99], v[126:129]\n"
      "v_add_f32 v202, v202, v182\n"
      "v_fmac_f32 v203, v182, v182\n"
      "v_add_f32 v202, v202, v183\n"
      "v_mfma_f32_16x16x16_bf16 v[148:151], v[212:213], v[172:173], v[148:151]\n"
      "v_fmac_f32 v203, v183, v183\n"
      "v_add_f32 v202, v202, v184\n"
      "v_fmac_f32 v203, v184, v184\n"
      "v_mfma_f32_16x16x16_bf16 v[112:115], v[212:213], v[204:205], v[112:115]\n"
      "v_add_f32 v202, v202, v185\n"
      "v_fmac_f32 v203, v185, v185\n"
      "v_add_f32 v202, v202, v104\n"
      "v_mfma_f32_16x16x16_bf16 v[122:125], v[192:193], v[96:97], v[122:125]\n"
      "v_fmac_f32 v203, v104, v104\n"
      "v_add_f32 v202, v202, v105\n"
      "v_fmac_f32_e64 v203, v105, v105\n"
      "v_mfma_f32_16x16x16_bf16 v[126:129], v[192:193], v[98:99], v[126:129]\n"
      "v_add_f32 v202, v202, v106\n"
      "v_fmac_f32 v203, v106, v106\n"
      "v_add_f32_e64 v202, v202, v107\n"
      "v_mfma_f32_16x16x16_bf16 v[148:151], v[192:193], v[172:173], v[148:151]\n"
      "ds_read_b128 v[210:213], v2 offset:512\n"
      "v_max3_f32 v98, v122, v123, v124\n"
      "v_mfma_f32_16x16x16_bf16 v[112:115], v[192:193], v[204:205], v[112:115]\n"
      "ds_read_b128 v[206:209], v2 offset:3584\n"
      "ds_read_b128 v[132:135], v2 offset:6656\n"
      "v_max3_f32 v101, v125, v126, v127\n"
      "v_max3_f32 v98, v98, v128, v129\n"
      "v_fmac_f32_e64 v203, v107, v107\n"
      "v_max3_f32 v101, v101, v148, v149\n"
      "v_max3_f32 v98, v98, v150, v151\n"
      "v_rcp_f32 v174, v174\n"
      "v_max3_f32 v98, v98, v101, v112\n"
      "v_mov_b32 v101, v98\n"
      "v_permlane16_swap_b32 v202, v203\n"
      "v_mul_f32 v164, v174, v164\n"
      "v_permlane16_swap_b32 v98, v101\n"
      "v_max_f32 v98, v98, v101\n"
      "v_mov_b32 v101, v98\n"
      "v_mul_f32 v165, v174, v165\n"
      "v_cvt_pk_bf16_f32 v164, v164, v165\n"
      "v_permlane32_swap_b32 v98, v101\n"
      "v_max_f32 v98, v98, v101\n"
      "s_waitcnt lgkmcnt(1)\n"
      "v_mfma_f32_16x16x32_bf16 v[100:103], a[216:219], v[210:213], 0\n"
      "v_sub_f32 v122, v122, v98\n"
      "v_sub_f32_e64 v123, v123, v98\n"
      "v_mfma_f32_16x16x32_bf16 v[100:103], a[220:223], v[206:209], v[100:103]\n"
      "s_waitcnt lgkmcnt(0)\n"
      "v_sub_f32 v124, v124, v98\n"
      "v_sub_f32 v125, v125, v98\n"
      "v_mfma_f32_16x16x32_bf16 v[100:103], a[224:227], v[132:135], v[100:103]\n"
      "v_exp_f32 v122, v122\n"
      "v_exp_f32 v123, v123\n"
      "v_sub_f32 v126, v126, v98\n"
      "v_sub_f32 v127, v127, v98\n"
      "v_sub_f32 v128, v128, v98\n"
      "v_sub_f32 v129, v129, v98\n"
      "v_exp_f32 v124, v124\n"
      "v_exp_f32 v125, v125\n"
      "v_exp_f32 v126, v126\n"
      "v_exp_f32 v127, v127\n"
      "v_exp_f32 v128, v128\n"
      "v_exp_f32_e64 v129, v129\n"
      "v_cvt_pk_bf16_f32 v122, v122, v123\n"
      "v_cvt_pk_bf16_f32 v123, v124, v125\n"
      "v_cvt_pk_bf16_f32 v124, v126, v127\n"
      "v_cvt_pk_bf16_f32 v125, v128, v129\n"
      "v_mfma_f32_16x16x32_bf16 v[126:129], a[240:243], v[210:213], 0\n"
      "v_sub_f32 v148, v148, v98\n"
      "v_sub_f32 v149, v149, v98\n"
      "v_mfma_f32_16x16x32_bf16 v[126:129], a[244:247], v[206:209], v[126:129]\n"
      "v_sub_f32 v150, v150, v98\n"
      "v_sub_f32 v151, v151, v98\n"
      "v_mfma_f32_16x16x32_bf16 v[126:129], a[248:251], v[132:135], v[126:129]\n"
      "v_sub_f32 v112, v112, v98\n"
      "v_exp_f32 v148, v148\n"
      "v_mfma_f32_16x16x32_bf16 v[96:99], v[232:235], v[210:213], 0\n"
      "v_exp_f32 v149, v149\n"
      "v_mfma_f32_16x16x32_bf16 v[96:99], v[236:239], v[206:209], v[96:99]\n"
      "ds_read_b128 v[210:213], v10 offset:1168\n"
      "v_exp_f32 v150, v150\n"
      "v_mfma_f32_16x16x32_bf16 v[96:99], v[240:243], v[132:135], v[96:99]\n"
      "ds_read_b64 v[192:193], v10 offset:1184\n"
      "v_exp_f32 v151, v151\n"
      "v_exp_f32 v112, v112\n"
      "v_cvt_pk_bf16_f32 v88, v148, v149\n"
      "v_mfma_f32_16x16x32_bf16 v[206:209], v[32:35], v[156:159], 0\n"
      "v_cvt_pk_bf16_f32 v89, v150, v151\n"
      "v_cvt_pk_bf16_f32 v90, v112, 0\n"
      "v_mfma_f32_16x16x32_bf16 v[206:209], v[32:35], v[92:95], v[206:209]\n"
      "ds_read_b128 v[130:133], v2 offset:9728\n"
      "v_mul_f32 v166, v174, v166\n"
      "v_mul_f32 v167, v174, v167\n"
      "v_add_f32 v202, v202, v203\n"
      "v_cvt_pk_bf16_f32 v165, v166, v167\n"
      "v_mul_f32 v176, v174, v176\n"
      "v_mul_f32 v177, v174, v177\n"
      "v_mul_f32 v178, v174, v178\n"
      "v_rcp_f32 v206, v206\n"
      "v_mul_f32 v179, v174, v179\n"
      "v_cvt_pk_bf16_f32 v166, v176, v177\n"
      "v_cvt_pk_bf16_f32 v167, v178, v179\n"
      "v_mfma_f32_16x16x32_bf16 v[176:179], v[198:201], v[156:159], 0\n"
      "ds_write_b128 v3, v[164:167] offset:18432\n"
      "v_mfma_f32_16x16x32_bf16 v[176:179], v[76:79], v[92:95], v[176:179]\n"
      "ds_read_b128 v[112:115], v2 offset:12800\n"
      "v_mul_f32 v116, v174, v116\n"
      "v_mul_f32 v117, v174, v117\n"
      "v_cvt_pk_bf16_f32 v116, v116, v117\n"
      "v_mul_f32 v118, v174, v118\n"
      "v_mul_f32 v119, v174, v119\n"
      "v_permlane32_swap_b32_e64 v218, v202\n"
      "v_cvt_pk_bf16_f32 v117, v118, v119\n"
      "v_mfma_f32_16x16x32_bf16 v[172:175], v[214:217], v[156:159], 0\n"
      "ds_write_b64 v4, v[116:117] offset:18432\n"
      "v_mfma_f32_16x16x32_bf16 v[172:175], v[80:83], v[92:95], v[172:175]\n"
      "ds_read_u16 v39, v39\n"
      "v_mul_f32 v176, v206, v176\n"
      "v_mfma_f32_16x16x32_bf16 v[164:167], v[194:197], v[156:159], 0\n"
      "s_waitcnt lgkmcnt(4)\n"
      "v_mul_f32_e64 v177, v206, v177\n"
      "v_cvt_pk_bf16_f32 v176, v176, v177\n"
      "v_mfma_f32_16x16x32_bf16 v[164:167], v[84:87], v[92:95], v[164:167]\n"
      "ds_read_b128 v[148:151], v2 offset:15872\n"
      "v_mul_f32_e64 v178, v206, v178\n"
      "v_mfma_f32_16x16x32_bf16 v[116:119], v[210:213], v[136:139], v[30:33]\n"
      "v_mul_f32 v179, v206, v179\n"
      "v_cvt_pk_bf16_f32 v177, v178, v179\n"
      "v_mfma_f32_16x16x32_bf16 v[136:139], v[210:213], v[152:155], v[30:33]\n"
      "v_mul_f32 v172, v206, v172\n"
      "v_mul_f32 v173, v206, v173\n"
      "v_mfma_f32_16x16x32_bf16 v[152:155], v[210:213], v[144:147], v[30:33]\n"
      "v_mul_f32 v174, v206, v174\n"
      "v_mul_f32_e64 v175, v206, v175\n"
      "v_mfma_f32_16x16x32_bf16 v[156:159], v[210:213], v[108:111], v[30:33]\n"
      "v_cvt_pk_bf16_f32 v178, v172, v173\n"
      "s_waitcnt lgkmcnt(1)\n"
      "v_lshlrev_b32 v39, 16, v39\n"
      "v_mfma_f32_16x16x16_bf16 v[116:119], v[192:193], v[170:171], v[116:119]\n"
      "v_cvt_pk_bf16_f32 v179, v174, v175\n"
      "v_mul_f32 v164, v206, v164\n"
      "v_mul_f32 v165, v206, v165\n"
      "v_mfma_f32_16x16x16_bf16 v[136:139], v[192:193], v[180:181], v[136:139]\n"
      "v_cvt_pk_bf16_f32 v164, v164, v165\n"
      "v_mul_f32 v166, v206, v166\n"
      "v_mul_f32 v167, v206, v167\n"
      "v_mfma_f32_16x16x16_bf16 v[152:155], v[192:193], v[190:191], v[152:155]\n"
      "v_cvt_pk_bf16_f32 v165, v166, v167\n"
      "v_add_f32_e64 v218, v218, v202\n"
      "v_add_u32 v10, 2336, v10\n"
      "v_mfma_f32_16x16x16_bf16 v[156:159], v[192:193], v[120:121], v[156:159]\n"
      "v_cndmask_b32 v116, v116, v136, s[58:59]\n"
      "v_add_u32 v11, 2336, v11\n"
      "ds_write_b128 v3, v[176:179] offset:18688\n"
      "v_mfma_f32_16x16x32_bf16 v[100:103], a[228:231], v[130:133], v[100:103]\n"
      "s_waitcnt lgkmcnt(1)\n"
      "v_cndmask_b32 v116, v116, v152, s[60:61]\n"
      "v_mfma_f32_16x16x32_bf16 v[100:103], a[232:235], v[112:115], v[100:103]\n"
      "v_mfma_f32_16x16x32_bf16 v[100:103], a[236:239], v[148:151], v[100:103]\n"
      "ds_write_b64 v4, v[164:165] offset:18688\n"
      "v_mfma_f32_16x16x32_bf16 v[126:129], a[252:255], v[130:133], v[126:129]\n"
      "v_cndmask_b32 v116, v116, v156, s[62:63]\n"
      "v_fmac_f32 v116, s53, v39\n"
      "v_mfma_f32_16x16x32_bf16 v[126:129], v[224:227], v[112:115], v[126:129]\n"
      "v_mfma_f32_16x16x32_bf16 v[126:129], v[228:231], v[148:151], v[126:129]\n"
      "ds_write_b32 v8, v218 offset:2048\n"
      "v_mfma_f32_16x16x32_bf16 v[96:99], v[244:247], v[130:133], v[96:99]\n"
      "v_max_f32_dpp v136, v116, v116 row_ror:8 row_mask:0xf bank_mask:0xf\n"
      "v_mul_f32 v171, v100, v100\n"
      "v_mfma_f32_16x16x32_bf16 v[96:99], v[248:251], v[112:115], v[96:99]\n"
      "v_max_f32_dpp v136, v136, v136 row_ror:4 row_mask:0xf bank_mask:0xf\n"
      "v_add_f32 v170, v100, v101\n"
      "v_mfma_f32_16x16x32_bf16 v[96:99], v[252:255], v[148:151], v[96:99]\n"
      "v_max_f32_dpp v136, v136, v136 row_ror:2 row_mask:0xf bank_mask:0xf\n"
      "v_fmac_f32_e64 v171, v101, v101\n"
      "v_mfma_f32_16x16x32_bf16 v[108:111], v[32:35], v[122:125], 0\n"
      "v_max_f32_dpp v136, v136, v136 row_ror:1 row_mask:0xf bank_mask:0xf\n"
      "v_add_f32 v170, v170, v102\n"
      "v_mfma_f32_16x16x32_bf16 v[108:111], v[32:35], v[88:91], v[108:111]\n"
      "v_fmac_f32 v171, v102, v102\n"
      "v_max_f32_dpp v136, v136, v136 row_bcast:15 row_mask:0xa bank_mask:0xf\n"
      "v_mfma_f32_16x16x32_bf16 v[174:177], v[198:201], v[122:125], 0\n"
      "v_add_f32 v170, v170, v103\n"
      "v_fmac_f32 v171, v103, v103\n"
      "v_mfma_f32_16x16x32_bf16 v[174:177], v[76:79], v[88:91], v[174:177]\n"
      "v_add_f32 v170, v170, v126\n"
      "v_fmac_f32 v171, v126, v126\n"
      "v_mfma_f32_16x16x32_bf16 v[190:193], v[214:217], v[122:125], 0\n"
      "v_add_f32 v170, v170, v127\n"
      "v_fmac_f32 v171, v127, v127\n"
      "v_mfma_f32_16x16x32_bf16 v[190:193], v[80:83], v[88:91], v[190:193]\n"
      "v_add_f32 v170, v170, v128\n"
      "v_fmac_f32 v171, v128, v128\n"
      "v_mfma_f32_16x16x32_bf16 v[154:157], v[194:197], v[122:125], 0\n"
      "v_max_f32_dpp v136, v136, v136 row_bcast:31 row_mask:0xc bank_mask:0xf\n"
      "v_add_f32 v170, v170, v129\n"
      "v_mfma_f32_16x16x32_bf16 v[154:157], v[84:87], v[88:91], v[154:157]\n"
      "v_fmac_f32 v171, v129, v129\n"
      "v_add_f32 v170, v170, v96\n"
      "v_fmac_f32 v171, v96, v96\n"
      "v_readlane_b32 s72, v136, 63\n"
      "v_add_f32 v170, v170, v97\n"
      "v_fmac_f32 v171, v97, v97\n"
      "v_add_f32 v170, v170, v98\n"
      "v_fmac_f32 v171, v98, v98\n"
      "v_add_f32 v170, v170, v99\n"
      "v_fmac_f32 v171, v99, v99\n"
      "v_subrev_f32 v116, s72, v116\n"
      "v_rcp_f32 v108, v108\n"
      "v_exp_f32 v116, v116\n"
      "v_permlane16_swap_b32 v170, v171\n"
      "v_mul_f32 v174, v108, v174\n"
      "v_mul_f32 v175, v108, v175\n"
      "v_cvt_pk_bf16_f32 v116, v116, v116\n"
      "v_add_f32 v170, v170, v171\n"
      "v_cvt_pk_bf16_f32 v174, v174, v175\n"
      "v_mul_f32 v176, v108, v176\n"
      "v_mul_f32_e64 v177, v108, v177\n"
      "ds_write_b16 v13, v116\n"
      "ds_read_b128 v[210:213], v14\n"
      "ds_read_b128 v[136:139], v14 offset:64\n"
      "v_mov_b32_e64 v171, v170\n"
      "v_cvt_pk_bf16_f32 v175, v176, v177\n"
      "v_mul_f32 v190, v108, v190\n"
      "v_mul_f32 v191, v108, v191\n"
      "v_mul_f32 v192, v108, v192\n"
      "v_mul_f32 v193, v108, v193\n"
      "v_cvt_pk_bf16_f32 v176, v190, v191\n"
      "v_cvt_pk_bf16_f32 v177, v192, v193\n"
      "v_mul_f32 v154, v108, v154\n"
      "v_mul_f32 v155, v108, v155\n"
      "ds_write_b128 v3, v[174:177] offset:18944\n"
      "v_cvt_pk_bf16_f32 v154, v154, v155\n"
      "v_mul_f32 v156, v108, v156\n"
      "v_mul_f32 v157, v108, v157\n"
      "v_permlane32_swap_b32 v170, v171\n"
      "v_cvt_pk_bf16_f32 v155, v156, v157\n"
      "s_waitcnt lgkmcnt(2)\n"
      "v_mfma_f32_16x16x32_bf16 v[108:111], v[210:213], v[32:35], 0\n"
      "ds_write_b64 v4, v[154:155] offset:18944\n"
      "s_waitcnt lgkmcnt(2)\n"
      "v_mfma_f32_16x16x32_bf16 v[108:111], v[136:139], v[32:35], v[108:111]\n"
      "v_add_f32 v170, v170, v171\n"
      "ds_write_b32 v8, v170 offset:2560\n"
      "s_waitcnt lgkmcnt(0)\n"
      "s_barrier\n"
      "s_cbranch_vccz L_sel0_%=\n"
      "L_disp0_%=:\n"
      "s_cmp_lt_u32 s95, 0x40000000\n"
      "s_cbranch_scc1 L_nofix_18_%=\n"
      "s_cmp_eq_u32 s95, -2\n"
      "s_cbranch_scc1 L_tail0_%=\n"
      "s_nop 2\n"
      "v_lshlrev_b32 v171, 3, v27\n"
      "s_add_u32 s72, s98, L_cold_%=-L_ipf0_%=\n"
      "s_addc_u32 s73, s99, 0\n"
      "s_mov_b32 s74, L_cend_%=-L_cold_%=\n"
      "s_mov_b32 s75, 0x20000\n"
      "s_mov_b32 m0, 161792\n"
      "s_nop 0\n"
      "buffer_load_dwordx4 v171, s[72:75], 0 offen  lds\n"
      "s_lshr_b32 s92, s45, 30\n"
      "s_or_b32 s92, s92, s66\n"
      "s_cbranch_scc0 L_nf_19_%=\n"
      "s_bitcmp1_b32 s45, 31\n"
      "s_cselect_b32 s75, 100270464, 0\n"
      "s_bitcmp1_b32 s45, 30\n"
      "s_cselect_b32 s76, 196224, 0\n"
      "s_and_b32 s74, s45, 0x3fffffff\n"
      "s_cmp_ge_u32 s45, 0x40000000\n"
      "s_cselect_b32 s74, s74, 0\n"
      "s_cselect_b32 s45, 0, s45\n"
      "s_sub_u32 s74, s74, s66\n"
      "s_add_u32 s66, s66, s74\n"
      "s_sub_u32 s75, s75, s67\n"
      "s_add_u32 s67, s67, s75\n"
      "s_sub_u32 s76, s76, s68\n"
      "s_add_u32 s68, s68, s76\n"
      "v_add_u32 v16, s74, v16\n"
      "v_add_u32 v17, s74, v17\n"
      "v_add_u32 v18, s74, v18\n"
      "v_add_u32 v19, s74, v19\n"
      "v_add_u32 v15, s74, v15\n"
      "s_cmp_eq_u32 s75, 0\n"
      "s_cbranch_scc1 L_ax_20_%=\n"
      "v_bfe_i32 v171, v36, 0, 1\n"
      "v_and_b32 v171, s75, v171\n"
      "v_sub_u32 v16, v16, v171\n"
      "v_bfe_i32 v171, v36, 2, 1\n"
      "v_and_b32 v171, s75, v171\n"
      "v_sub_u32 v17, v17, v171\n"
      "v_bfe_i32 v171, v36, 4, 1\n"
      "v_and_b32 v171, s75, v171\n"
      "v_sub_u32 v18, v18, v171\n"
      "v_bfe_i32 v171, v36, 6, 1\n"
      "v_and_b32 v171, s75, v171\n"
      "v_sub_u32 v19, v19, v171\n"
      "v_bfe_i32 v171, v36, 8, 1\n"
      "v_and_b32 v171, s75, v171\n"
      "v_sub_u32 v15, v15, v171\n"
      "L_ax_20_%=:\n"
      "s_cmp_eq_u32 s76, 0\n"
      "s_cbranch_scc1 L_ax_21_%=\n"
      "v_bfe_i32 v171, v36, 1, 1\n"
      "v_and_b32 v171, s76, v171\n"
      "v_sub_u32 v16, v16, v171\n"
      "v_bfe_i32 v171, v36, 3, 1\n"
      "v_and_b32 v171, s76, v171\n"
      "v_sub_u32 v17, v17, v171\n"
      "v_bfe_i32 v171, v36, 5, 1\n"
      "v_and_b32 v171, s76, v171\n"
      "v_sub_u32 v18, v18, v171\n"
      "v_bfe_i32 v171, v36, 7, 1\n"
      "v_and_b32 v171, s76, v171\n"
      "v_sub_u32 v19, v19, v171\n"
      "v_bfe_i32 v171, v36, 9, 1\n"
      "v_and_b32 v171, s76, v171\n"
      "v_sub_u32 v15, v15, v171\n"
      "L_ax_21_%=:\n"
      "s_or_b32 s48, s66, s69\n"
      "s_cmp_lg_u32 s48, 0\n"
      "s_cselect_b32 s48, 0x40000000, 0\n"
      "L_nf_19_%=:\n"
      "s_lshr_b32 s92, s46, 30\n"
      "s_or_b32 s92, s92, s69\n"
      "s_cbranch_scc0 L_nf_22_%=\n"
      "s_bitcmp1_b32 s46, 31\n"
      "s_cselect_b32 s75, 100270464, 0\n"
      "s_bitcmp1_b32 s46, 30\n"
      "s_cselect_b32 s76, 196224, 0\n"
      "s_and_b32 s74, s46, 0x3fffffff\n"
      "s_cmp_ge_u32 s46, 0x40000000\n"
      "s_cselect_b32 s74, s74, 0\n"
      "s_cselect_b32 s46, 0, s46\n"
      "s_sub_u32 s74, s74, s69\n"
      "s_add_u32 s69, s69, s74\n"
      "s_sub_u32 s75, s75, s70\n"
      "s_add_u32 s70, s70, s75\n"
      "s_sub_u32 s76, s76, s71\n"
      "s_add_u32 s71, s71, s76\n"
      "v_add_u32 v20, s74, v20\n"
      "v_add_u32 v21, s74, v21\n"
      "v_add_u32 v22, s74, v22\n"
      "v_add_u32 v23, s74, v23\n"
      "v_add_u32 v25, s74, v25\n"
      "s_cmp_eq_u32 s75, 0\n"
      "s_cbranch_scc1 L_ax_23_%=\n"
      "v_bfe_i32 v171, v36, 10, 1\n"
      "v_and_b32 v171, s75, v171\n"
      "v_sub_u32 v20, v20, v171\n"
      "v_bfe_i32 v171, v36, 12, 1\n"
      "v_and_b32 v171, s75, v171\n"
      "v_sub_u32 v21, v21, v171\n"
      "v_bfe_i32 v171, v36, 14, 1\n"
      "v_and_b32 v171, s75, v171\n"
      "v_sub_u32 v22, v22, v171\n"
      "v_bfe_i32 v171, v36, 16, 1\n"
      "v_and_b32 v171, s75, v171\n"
      "v_sub_u32 v23, v23, v171\n"
      "v_bfe_i32 v171, v36, 18, 1\n"
      "v_and_b32 v171, s75, v171\n"
      "v_sub_u32 v25, v25, v171\n"
      "L_ax_23_%=:\n"
      "s_cmp_eq_u32 s76, 0\n"
      "s_cbranch_scc1 L_ax_24_%=\n"
      "v_bfe_i32 v171, v36, 11, 1\n"
      "v_and_b32 v171, s76, v171\n"
      "v_sub_u32 v20, v20, v171\n"
      "v_bfe_i32 v171, v36, 13, 1\n"
      "v_and_b32 v171, s76, v171\n"
      "v_sub_u32 v21, v21, v171\n"
      "v_bfe_i32 v171, v36, 15, 1\n"
      "v_and_b32 v171, s76, v171\n"
      "v_sub_u32 v22, v22, v171\n"
      "v_bfe_i32 v171, v36, 17, 1\n"
      "v_and_b32 v171, s76, v171\n"
      "v_sub_u32 v23, v23, v171\n"
      "v_bfe_i32 v171, v36, 19, 1\n"
      "v_and_b32 v171, s76, v171\n"
      "v_sub_u32 v25, v25, v171\n"
      "L_ax_24_%=:\n"
      "s_or_b32 s48, s66, s69\n"
      "s_cmp_lg_u32 s48, 0\n"
      "s_cselect_b32 s48, 0x40000000, 0\n"
      "L_nf_22_%=:\n"
      "s_sub_u32 s92, s42, 1\n"
      "s_cmp_gt_u32 s92, s44\n"
      "s_cbranch_scc1 L_tail0_%=\n"
      "L_nofix_18_%=:\n"
      "s_cmp_lg_u64 s[56:57], 0\n"
      "s_cbranch_scc1 L_slow0_%=\n"
      "L_sel0_%=:\n"
      "s_cmp_lg_u64 s[54:55], 0\n"
      "s_cbranch_scc1 L_max0_%=\n"
      "s_branch L_loop0_%=\n"
      "L_slow0_%=:\n"
      "s_nop 4\n"
      "v_mfma_f32_16x16x32_bf16 v[174:177], v[210:213], v[198:201], 0\n"
      "ds_read_b128 v[170:173], v0\n"
      "v_subrev_u32 v164, 115456, v9\n"
      "v_mfma_f32_16x16x32_bf16 v[174:177], v[136:139], v[76:79], v[174:177]\n"
      "ds_read_b128 v[152:155], v1\n"
      "v_subrev_u32 v76, 115456, v9\n"
      "v_mfma_f32_16x16x32_bf16 v[190:193], v[210:213], v[214:217], 0\n"
      "ds_read_b128 v[114:117], v0 offset:6144\n"
      "v_bfe_u32 v77, v76, 4, 4\n"
      "v_mfma_f32_16x16x32_bf16 v[190:193], v[136:139], v[80:83], v[190:193]\n"
      "ds_read_b128 v[198:201], v9 offset:2048\n"
      "v_mul_u32_u24 v82, 98, v77\n"
      "v_mfma_f32_16x16x32_bf16 v[214:217], v[210:213], v[194:197], 0\n"
      "ds_read_b128 v[194:197], v9 offset:3072\n"
      "v_lshrrev_b32 v76, 8, v76\n"
      "v_mfma_f32_16x16x32_bf16 v[214:217], v[136:139], v[84:87], v[214:217]\n"
      "s_add_u32 s72, s42, 1\n"
      "v_mov_b32_dpp v108, v108 quad_perm:[0,0,0,0] row_mask:0xf bank_mask:0xf\n"
      "v_rcp_f32 v108, v108\n"
      "s_waitcnt lgkmcnt(4)\n"
      "v_mfma_f32_16x16x32_bf16 v[210:213], a[0:3], v[170:173], 0\n"
      "s_mov_b32 m0, 110592\n"
      "v_cndmask_b32 v174, v174, v190, s[58:59]\n"
      "v_readlane_b32 s77, v38, s72\n"
      "v_mfma_f32_16x16x32_bf16 v[144:147], a[24:27], v[170:173], 0\n"
      "s_and_b32 s73, s77, -16\n"
      "v_cndmask_b32 v174, v174, v214, s[60:61]\n"
      "v_mul_f32 v174, v108, v174\n"
      "v_mfma_f32_16x16x32_bf16 v[108:111], a[48:51], v[170:173], 0\n"
      "buffer_load_dwordx4 v164, s[24:27], s73 offen  lds\n"
      "v_mfma_f32_16x16x32_bf16 v[190:193], a[72:75], v[170:173], 0\n"
      "ds_read_b128 v[176:179], v1 offset:6144\n"
      "v_cvt_pk_bf16_f32 v174, v174, v174\n"
      "v_mfma_f32_16x16x32_bf16 v[148:151], a[96:99], v[170:173], 0\n"
      "ds_write_b16 v12, v174 offset:16\n"
      "v_mfma_f32_16x16x32_bf16 v[130:133], a[120:123], v[170:173], 0\n"
      "s_waitcnt lgkmcnt(2)\n"
      "v_add_f32 v198, v198, v200\n"
      "v_add_f32 v194, v194, v196\n"
      "v_mfma_f32_16x16x32_bf16 v[210:213], a[4:7], v[152:155], v[210:213]\n"
      "ds_read_b128 v[122:125], v0 offset:12288\n"
      "v_add_f32 v199, v199, v201\n"
      "v_mfma_f32_16x16x32_bf16 v[210:213], a[8:11], v[114:117], v[210:213]\n"
      "ds_read_b128 v[156:159], v1 offset:12288\n"
      "v_add_f32_e64 v195, v195, v197\n"
      "v_mfma_f32_16x16x32_bf16 v[144:147], a[28:31], v[152:155], v[144:147]\n"
      "s_mov_b32 m0, 111616\n"
      "v_add_f32 v198, v198, v194\n"
      "v_add_f32 v199, v199, v195\n"
      "v_mfma_f32_16x16x32_bf16 v[144:147], a[32:35], v[114:117], v[144:147]\n"
      "s_add_u32 s73, s73, 1024\n"
      "v_fma_f32 v194, -v198, v198, s52\n"
      "v_fmac_f32 v194, 0x43400000, v199\n"
      "v_mfma_f32_16x16x32_bf16 v[108:111], a[52:55], v[152:155], v[108:111]\n"
      "buffer_load_dwordx4 v164, s[24:27], s73 offen  lds\n"
      "v_mfma_f32_16x16x32_bf16 v[108:111], a[56:59], v[114:117], v[108:111]\n"
      "s_mov_b32 m0, 112640\n"
      "v_max_f32 v194, s52, v194\n"
      "v_rsq_f32 v194, v194\n"
      "v_mfma_f32_16x16x32_bf16 v[190:193], a[76:79], v[152:155], v[190:193]\n"
      "s_add_u32 s73, s73, 1024\n"
      "v_mul_f32 v199, 0x43400000, v194\n"
      "v_mul_f32 v201, -v198, v194\n"
      "v_mfma_f32_16x16x32_bf16 v[190:193], a[80:83], v[114:117], v[190:193]\n"
      "s_and_b32 s77, s77, 15\n"
      "v_mov_b32 v136, v199\n"
      "v_mov_b32_e64 v138, v201\n"
      "v_mfma_f32_16x16x32_bf16 v[148:151], a[100:103], v[152:155], v[148:151]\n"
      "buffer_load_dwordx4 v164, s[24:27], s73 offen  lds\n"
      "v_mfma_f32_16x16x32_bf16 v[148:151], a[104:107], v[114:117], v[148:151]\n"
      "s_mov_b32 m0, 113664\n"
      "v_permlane32_swap_b32_e64 v199, v136\n"
      "v_mfma_f32_16x16x32_bf16 v[130:133], a[124:127], v[152:155], v[130:133]\n"
      "s_add_u32 s73, s73, 1024\n"
      "v_permlane32_swap_b32 v201, v138\n"
      "v_mfma_f32_16x16x32_bf16 v[130:133], a[128:131], v[114:117], v[130:133]\n"
      "s_waitcnt lgkmcnt(0)\n"
      "v_mov_b32_e64 v174, v199\n"
      "v_mov_b32_e64 v175, v201\n"
      "v_mfma_f32_16x16x32_bf16 v[210:213], a[12:15], v[176:179], v[210:213]\n"
      "buffer_load_dwordx4 v164, s[24:27], s73 offen  lds\n"
      "v_mfma_f32_16x16x32_bf16 v[210:213], a[16:19], v[122:125], v[210:213]\n"
      "s_mov_b32 m0, 114688\n"
      "v_permlane16_swap_b32_e64 v199, v174\n"
      "v_mfma_f32_16x16x32_bf16 v[210:213], a[20:23], v[156:159], v[210:213]\n"
      "s_add_u32 s73, s73, 1024\n"
      "v_permlane16_swap_b32 v201, v175\n"
      "v_mfma_f32_16x16x32_bf16 v[144:147], a[36:39], v[176:179], v[144:147]\n"
      "v_fma_f32 v140, v140, v199, v201\n"
      "v_fma_f32 v141, v141, v199, v201\n"
      "v_mfma_f32_16x16x32_bf16 v[144:147], a[40:43], v[122:125], v[144:147]\n"
      "s_mov_b32 exec_hi, 0xffff\n"
      "buffer_load_dwordx4 v164, s[24:27], s73 offen  lds\n"
      "s_mov_b32 exec_hi, -1\n"
      "v_mfma_f32_16x16x32_bf16 v[144:147], a[44:47], v[156:159], v[144:147]\n"
      "s_add_u32 m0, s64, 18432\n"
      "v_cvt_pk_bf16_f32 v140, v140, v141\n"
      "v_fma_f32 v142, v142, v199, v201\n"
      "v_mfma_f32_16x16x32_bf16 v[108:111], a[60:63], v[176:179], v[108:111]\n"
      "s_add_u32 s73, s73, 1024\n"
      "v_fma_f32 v143, v143, v199, v201\n"
      "v_fma_f32 v160, v160, v199, v201\n"
      "v_mfma_f32_16x16x32_bf16 v[108:111], a[64:67], v[122:125], v[108:111]\n"
      "v_fma_f32 v161, v161, v199, v201\n"
      "v_cvt_pk_bf16_f32 v141, v142, v143\n"
      "v_mfma_f32_16x16x32_bf16 v[108:111], a[68:71], v[156:159], v[108:111]\n"
      "buffer_load_dwordx4 v16, s[16:19], s45 offen  lds\n"
      "v_mfma_f32_16x16x32_bf16 v[190:193], a[84:87], v[176:179], v[190:193]\n"
      "s_add_u32 m0, s64, 22528\n"
      "v_fma_f32 v220, v220, v199, v201\n"
      "v_fma_f32 v221, v221, v199, v201\n"
      "v_mfma_f32_16x16x32_bf16 v[190:193], a[88:91], v[122:125], v[190:193]\n"
      "v_fma_f32 v222, v222, v199, v201\n"
      "v_fma_f32 v223, v223, v199, v201\n"
      "v_mfma_f32_16x16x32_bf16 v[190:193], a[92:95], v[156:159], v[190:193]\n"
      "v_cvt_pk_bf16_f32 v160, v160, v161\n"
      "v_fma_f32 v162, v162, v199, v201\n"
      "v_mfma_f32_16x16x32_bf16 v[148:151], a[108:111], v[176:179], v[148:151]\n"
      "buffer_load_dwordx4 v17, s[16:19], s45 offen  lds\n"
      "v_mfma_f32_16x16x32_bf16 v[148:151], a[112:115], v[122:125], v[148:151]\n"
      "s_add_u32 m0, s64, 26624\n"
      "v_fma_f32 v163, v163, v199, v201\n"
      "v_cvt_pk_bf16_f32 v142, v220, v221\n"
      "v_mfma_f32_16x16x32_bf16 v[148:151], a[116:119], v[156:159], v[148:151]\n"
      "v_cvt_pk_bf16_f32 v143, v222, v223\n"
      "v_cvt_pk_bf16_f32 v161, v162, v163\n"
      "v_mfma_f32_16x16x32_bf16 v[130:133], a[132:135], v[176:179], v[130:133]\n"
      "ds_write_b128 v5, v[140:143]\n"
      "v_mfma_f32_16x16x32_bf16 v[130:133], a[136:139], v[122:125], v[130:133]\n"
      "buffer_load_dwordx4 v18, s[16:19], s45 offen  lds\n"
      "v_mfma_f32_16x16x32_bf16 v[130:133], a[140:143], v[156:159], v[130:133]\n"
      "s_add_u32 m0, s64, 30720\n"
      "v_fma_f32 v186, v186, v174, v175\n"
      "v_fma_f32 v187, v187, v174, v175\n"
      "v_mfma_f32_16x16x32_bf16 v[198:201], v[170:173], a[144:147], 0\n"
      "ds_write_b64 v6, v[160:161]\n"
      "v_mfma_f32_16x16x32_bf16 v[198:201], v[152:155], a[148:151], v[198:201]\n"
      "v_cvt_pk_bf16_f32 v186, v186, v187\n"
      "v_fma_f32 v188, v188, v174, v175\n"
      "v_mfma_f32_16x16x32_bf16 v[198:201], v[114:117], a[152:155], v[198:201]\n"
      "buffer_load_dwordx4 v19, s[16:19], s45 offen  lds\n"
      "v_mfma_f32_16x16x32_bf16 v[198:201], v[176:179], a[156:159], v[198:201]\n"
      "ds_read_b128 v[140:143], v0 offset:2048\n"
      "v_fma_f32 v189, v189, v174, v175\n"
      "v_mfma_f32_16x16x32_bf16 v[198:201], v[122:125], a[160:163], v[198:201]\n"
      "ds_read_b128 v[220:223], v1 offset:2048\n"
      "v_fma_f32 v104, v104, v174, v175\n"
      "v_mfma_f32_16x16x32_bf16 v[198:201], v[156:159], a[164:167], v[198:201]\n"
      "ds_read_b128 v[160:163], v0 offset:8192\n"
      "v_fma_f32 v105, v105, v174, v175\n"
      "v_mfma_f32_16x16x32_bf16 v[214:217], v[170:173], a[168:171], 0\n"
      "ds_read_b128 v[164:167], v1 offset:8192\n"
      "v_cvt_pk_bf16_f32 v187, v188, v189\n"
      "v_mfma_f32_16x16x32_bf16 v[214:217], v[152:155], a[172:175], v[214:217]\n"
      "ds_read_b128 v[206:209], v0 offset:14336\n"
      "v_fma_f32 v182, v182, v174, v175\n"
      "v_mfma_f32_16x16x32_bf16 v[214:217], v[114:117], a[176:179], v[214:217]\n"
      "ds_read_b128 v[118:121], v1 offset:14336\n"
      "v_fma_f32 v183, v183, v174, v175\n"
      "v_mfma_f32_16x16x32_bf16 v[214:217], v[176:179], a[180:183], v[214:217]\n"
      "s_add_u32 m0, s65, 34816\n"
      "v_fma_f32 v184, v184, v174, v175\n"
      "v_fma_f32 v185, v185, v174, v175\n"
      "v_mfma_f32_16x16x32_bf16 v[214:217], v[122:125], a[184:187], v[214:217]\n"
      "buffer_load_dwordx4 v15, s[16:19], s45 offen  lds\n"
      "v_mfma_f32_16x16x32_bf16 v[214:217], v[156:159], a[188:191], v[214:217]\n"
      "v_cvt_pk_bf16_f32 v104, v104, v105\n"
      "v_fma_f32 v106, v106, v174, v175\n"
      "v_mfma_f32_16x16x32_bf16 v[194:197], v[170:173], a[192:195], 0\n"
      "v_fma_f32 v107, v107, v174, v175\n"
      "v_cvt_pk_bf16_f32 v188, v182, v183\n"
      "v_mfma_f32_16x16x32_bf16 v[194:197], v[152:155], a[196:199], v[194:197]\n"
      "v_cvt_pk_bf16_f32 v189, v184, v185\n"
      "v_cvt_pk_bf16_f32 v105, v106, v107\n"
      "v_mfma_f32_16x16x32_bf16 v[194:197], v[114:117], a[200:203], v[194:197]\n"
      "ds_write_b128 v5, v[186:189] offset:6144\n"
      "v_mfma_f32_16x16x32_bf16 v[194:197], v[176:179], a[204:207], v[194:197]\n"
      "buffer_load_dwordx4 v[186:189], v27, s[24:27], s47 offen\n"
      "v_mfma_f32_16x16x32_bf16 v[194:197], v[122:125], a[208:211], v[194:197]\n"
      "s_waitcnt lgkmcnt(1)\n"
      "v_fma_f32 v100, v100, v136, v138\n"
      "v_fma_f32 v101, v101, v136, v138\n"
      "v_mfma_f32_16x16x32_bf16 v[194:197], v[156:159], a[212:215], v[194:197]\n"
      "ds_write_b64 v6, v[104:105] offset:6144\n"
      "v_mfma_f32_16x16x32_bf16 v[174:177], a[0:3], v[140:143], 0\n"
      "v_cvt_pk_bf16_f32 v100, v100, v101\n"
      "v_fma_f32 v102, v102, v136, v138\n"
      "v_mfma_f32_16x16x32_bf16 v[174:177], a[4:7], v[220:223], v[174:177]\n"
      "buffer_load_dword v78, v29, s[24:27], s47 offen\n"
      "v_mfma_f32_16x16x32_bf16 v[174:177], a[8:11], v[160:163], v[174:177]\n"
      "v_fma_f32 v103, v103, v136, v138\n"
      "v_fma_f32 v96, v96, v136, v138\n"
      "v_mfma_f32_16x16x32_bf16 v[174:177], a[12:15], v[164:167], v[174:177]\n"
      "v_fma_f32 v97, v97, v136, v138\n"
      "v_cvt_pk_bf16_f32 v101, v102, v103\n"
      "v_mfma_f32_16x16x32_bf16 v[174:177], a[16:19], v[206:209], v[174:177]\n"
      "v_fma_f32 v126, v126, v136, v138\n"
      "v_fma_f32 v127, v127, v136, v138\n"
      "v_mfma_f32_16x16x32_bf16 v[174:177], a[20:23], v[118:121], v[174:177]\n"
      "v_fma_f32 v128, v128, v136, v138\n"
      "v_fma_f32 v129, v129, v136, v138\n"
      "v_mfma_f32_16x16x32_bf16 v[104:107], a[24:27], v[140:143], 0\n"
      "v_cvt_pk_bf16_f32 v96, v96, v97\n"
      "v_fma_f32 v98, v98, v136, v138\n"
      "v_mfma_f32_16x16x32_bf16 v[104:107], a[28:31], v[220:223], v[104:107]\n"
      "v_fma_f32 v99, v99, v136, v138\n"
      "v_cvt_pk_bf16_f32 v102, v126, v127\n"
      "v_mfma_f32_16x16x32_bf16 v[104:107], a[32:35], v[160:163], v[104:107]\n"
      "v_cvt_pk_bf16_f32 v103, v128, v129\n"
      "v_cvt_pk_bf16_f32 v97, v98, v99\n"
      "v_mfma_f32_16x16x32_bf16 v[104:107], a[36:39], v[164:167], v[104:107]\n"
      "ds_write_b128 v5, v[100:103] offset:12288\n"
      "v_mfma_f32_16x16x32_bf16 v[104:107], a[40:43], v[206:209], v[104:107]\n"
      "v_fma_f32 v123, v210, v210, s51\n"
      "v_fma_f32 v136, v190, v190, s51\n"
      "v_mfma_f32_16x16x32_bf16 v[104:107], a[44:47], v[118:121], v[104:107]\n"
      "v_fmac_f32 v123, v211, v211\n"
      "v_fmac_f32_e64 v136, v191, v191\n"
      "v_mfma_f32_16x16x32_bf16 v[100:103], a[48:51], v[140:143], 0\n"
      "ds_write_b64 v6, v[96:97] offset:12288\n"
      "v_mfma_f32_16x16x32_bf16 v[100:103], a[52:55], v[220:223], v[100:103]\n"
      "v_fmac_f32 v123, v212, v212\n"
      "v_fmac_f32 v136, v192, v192\n"
      "v_mfma_f32_16x16x32_bf16 v[100:103], a[56:59], v[160:163], v[100:103]\n"
      "v_fmac_f32 v123, v213, v213\n"
      "v_fmac_f32 v136, v193, v193\n"
      "v_mfma_f32_16x16x32_bf16 v[100:103], a[60:63], v[164:167], v[100:103]\n"
      "v_fmac_f32 v123, v144, v144\n"
      "v_fmac_f32 v136, v148, v148\n"
      "v_mfma_f32_16x16x32_bf16 v[100:103], a[64:67], v[206:209], v[100:103]\n"
      "v_fmac_f32 v123, v145, v145\n"
      "v_fmac_f32 v136, v149, v149\n"
      "v_mfma_f32_16x16x32_bf16 v[100:103], a[68:71], v[118:121], v[100:103]\n"
      "v_fmac_f32 v123, v146, v146\n"
      "v_fmac_f32 v136, v150, v150\n"
      "v_mfma_f32_16x16x32_bf16 v[170:173], a[72:75], v[140:143], 0\n"
      "v_fmac_f32 v123, v147, v147\n"
      "v_fmac_f32 v136, v151, v151\n"
      "v_mfma_f32_16x16x32_bf16 v[170:173], a[76:79], v[220:223], v[170:173]\n"
      "v_fmac_f32 v123, v108, v108\n"
      "v_fmac_f32 v136, v130, v130\n"
      "v_mfma_f32_16x16x32_bf16 v[170:173], a[80:83], v[160:163], v[170:173]\n"
      "v_fmac_f32 v123, v109, v109\n"
      "v_fmac_f32 v136, v131, v131\n"
      "v_mfma_f32_16x16x32_bf16 v[170:173], a[84:87], v[164:167], v[170:173]\n"
      "v_fmac_f32 v123, v110, v110\n"
      "v_fmac_f32 v136, v132, v132\n"
      "v_mfma_f32_16x16x32_bf16 v[170:173], a[88:91], v[206:209], v[170:173]\n"
      "v_fmac_f32 v123, v111, v111\n"
      "v_fmac_f32 v136, v133, v133\n"
      "v_mfma_f32_16x16x32_bf16 v[170:173], a[92:95], v[118:121], v[170:173]\n"
      "v_fma_f32 v129, v174, v174, s51\n"
      "v_permlane16_swap_b32 v123, v136\n"
      "v_mfma_f32_16x16x32_bf16 v[182:185], a[96:99], v[140:143], 0\n"
      "v_add_f32 v123, v123, v136\n"
      "v_mov_b32 v136, v123\n"
      "v_mfma_f32_16x16x32_bf16 v[182:185], a[100:103], v[220:223], v[182:185]\n"
      "v_fmac_f32 v129, v175, v175\n"
      "v_permlane32_swap_b32 v123, v136\n"
      "v_mfma_f32_16x16x32_bf16 v[182:185], a[104:107], v[160:163], v[182:185]\n"
      "v_add_f32 v123, v123, v136\n"
      "v_rsq_f32 v123, v123\n"
      "v_mfma_f32_16x16x32_bf16 v[182:185], a[108:111], v[164:167], v[182:185]\n"
      "v_mov_b32 v136, v123\n"
      "v_fma_f32 v128, v170, v170, s51\n"
      "v_mfma_f32_16x16x32_bf16 v[182:185], a[112:115], v[206:209], v[182:185]\n"
      "v_permlane16_swap_b32 v123, v136\n"
      "v_mfma_f32_16x16x32_bf16 v[182:185], a[116:119], v[118:121], v[182:185]\n"
      "v_mul_f32 v123, s50, v123\n"
      "v_mul_f32 v190, v136, v190\n"
      "v_mfma_f32_16x16x32_bf16 v[124:127], a[120:123], v[140:143], 0\n"
      "v_mul_f32 v191, v136, v191\n"
      "v_mul_f32 v210, v123, v210\n"
      "v_mfma_f32_16x16x32_bf16 v[124:127], a[124:127], v[220:223], v[124:127]\n"
      "v_mul_f32 v211, v123, v211\n"
      "v_cvt_pk_bf16_f32 v190, v190, v191\n"
      "v_mfma_f32_16x16x32_bf16 v[124:127], a[128:131], v[160:163], v[124:127]\n"
      "v_mul_f32 v192, v136, v192\n"
      "v_mul_f32 v193, v136, v193\n"
      "v_mfma_f32_16x16x32_bf16 v[124:127], a[132:135], v[164:167], v[124:127]\n"
      "v_mul_f32 v130, v136, v130\n"
      "v_mul_f32 v131, v136, v131\n"
      "v_mfma_f32_16x16x32_bf16 v[124:127], a[136:139], v[206:209], v[124:127]\n"
      "v_cvt_pk_bf16_f32 v210, v210, v211\n"
      "v_mul_f32 v212, v123, v212\n"
      "v_mfma_f32_16x16x32_bf16 v[124:127], a[140:143], v[118:121], v[124:127]\n"
      "v_mul_f32 v213, v123, v213\n"
      "v_mul_f32_e64 v108, v123, v108\n"
      "v_mfma_f32_16x16x32_bf16 v[96:99], v[140:143], a[144:147], 0\n"
      "v_mul_f32 v109, v123, v109\n"
      "v_cvt_pk_bf16_f32 v191, v192, v193\n"
      "v_mfma_f32_16x16x32_bf16 v[96:99], v[220:223], a[148:151], v[96:99]\n"
      "v_mul_f32 v148, v136, v148\n"
      "v_mul_f32 v149, v136, v149\n"
      "v_mfma_f32_16x16x32_bf16 v[96:99], v[160:163], a[152:155], v[96:99]\n"
      "v_mul_f32 v150, v136, v150\n"
      "v_mul_f32_e64 v151, v136, v151\n"
      "v_mfma_f32_16x16x32_bf16 v[96:99], v[164:167], a[156:159], v[96:99]\n"
      "ds_read_b128 v[156:159], v0 offset:4096\n"
      "v_cvt_pk_bf16_f32 v130, v130, v131\n"
      "v_mfma_f32_16x16x32_bf16 v[96:99], v[206:209], a[160:163], v[96:99]\n"
      "ds_read_b128 v[152:155], v1 offset:4096\n"
      "v_mul_f32 v132, v136, v132\n"
      "v_mfma_f32_16x16x32_bf16 v[96:99], v[118:121], a[164:167], v[96:99]\n"
      "ds_read_b128 v[112:115], v7 offset:18432\n"
      "v_mul_f32 v133, v136, v133\n"
      "v_mfma_f32_16x16x32_bf16 v[178:181], v[140:143], a[168:171], 0\n"
      "ds_read_b128 v[202:205], v0 offset:10240\n"
      "v_cvt_pk_bf16_f32 v211, v212, v213\n"
      "v_mfma_f32_16x16x32_bf16 v[178:181], v[220:223], a[172:175], v[178:181]\n"
      "ds_read_b128 v[136:139], v1 offset:10240\n"
      "v_mul_f32 v144, v123, v144\n"
      "v_mfma_f32_16x16x32_bf16 v[178:181], v[160:163], a[176:179], v[178:181]\n"
      "s_waitcnt vmcnt(7)\n"
      "v_mul_f32 v145, v123, v145\n"
      "v_mul_f32 v146, v123, v146\n"
      "v_mfma_f32_16x16x32_bf16 v[178:181], v[164:167], a[180:183], v[178:181]\n"
      "v_mul_f32 v147, v123, v147\n"
      "v_cvt_pk_bf16_f32 v108, v108, v109\n"
      "v_mfma_f32_16x16x32_bf16 v[178:181], v[206:209], a[184:187], v[178:181]\n"
      "v_mul_f32 v110, v123, v110\n"
      "v_mul_f32_e64 v111, v123, v111\n"
      "v_mfma_f32_16x16x32_bf16 v[178:181], v[118:121], a[188:191], v[178:181]\n"
      "v_cvt_pk_bf16_f32 v192, v148, v149\n"
      "v_cvt_pk_bf16_f32 v193, v150, v151\n"
      "v_mfma_f32_16x16x32_bf16 v[148:151], v[140:143], a[192:195], 0\n"
      "ds_read_b128 v[140:143], v7 offset:22528\n"
      "v_cvt_pk_bf16_f32 v131, v132, v133\n"
      "v_mfma_f32_16x16x32_bf16 v[148:151], v[220:223], a[196:199], v[148:151]\n"
      "ds_read_b128 v[220:223], v0 offset:16384\n"
      "v_cvt_pk_bf16_f32 v212, v144, v145\n"
      "v_mfma_f32_16x16x32_bf16 v[148:151], v[160:163], a[200:203], v[148:151]\n"
      "ds_read_b128 v[160:163], v7 offset:26624\n"
      "v_cvt_pk_bf16_f32 v213, v146, v147\n"
      "v_mfma_f32_16x16x32_bf16 v[148:151], v[164:167], a[204:207], v[148:151]\n"
      "ds_read_b128 v[144:147], v1 offset:16384\n"
      "v_cvt_pk_bf16_f32 v109, v110, v111\n"
      "v_mfma_f32_16x16x32_bf16 v[148:151], v[206:209], a[208:211], v[148:151]\n"
      "ds_read_b128 v[206:209], v7 offset:30720\n"
      "v_fmac_f32 v128, v171, v171\n"
      "v_mfma_f32_16x16x32_bf16 v[148:151], v[118:121], a[212:215], v[148:151]\n"
      "s_waitcnt lgkmcnt(5)\n"
      "v_fmac_f32_e64 v129, v176, v176\n"
      "v_fmac_f32 v128, v172, v172\n"
      "v_mfma_f32_16x16x32_bf16 v[120:123], a[0:3], v[156:159], 0\n"
      "buffer_store_dwordx4 v[112:115], v20, s[20:23], s46 offen sc1\n"
      "v_mfma_f32_16x16x32_bf16 v[120:123], a[4:7], v[152:155], v[120:123]\n"
      "s_nop 0\n"
      "ds_read_b128 v[110:113], v7 offset:34816\n"
      "v_fmac_f32 v129, v177, v177\n"
      "v_mfma_f32_16x16x32_bf16 v[120:123], a[8:11], v[202:205], v[120:123]\n"
      "s_waitcnt lgkmcnt(2)\n"
      "v_fmac_f32 v128, v173, v173\n"
      "v_fmac_f32 v129, v104, v104\n"
      "v_mfma_f32_16x16x32_bf16 v[120:123], a[12:15], v[136:139], v[120:123]\n"
      "s_waitcnt lgkmcnt(1)\n"
      "v_fmac_f32 v128, v182, v182\n"
      "v_fmac_f32 v129, v105, v105\n"
      "v_mfma_f32_16x16x32_bf16 v[120:123], a[16:19], v[220:223], v[120:123]\n"
      "v_fmac_f32 v128, v183, v183\n"
      "v_fmac_f32 v129, v106, v106\n"
      "v_mfma_f32_16x16x32_bf16 v[120:123], a[20:23], v[144:147], v[120:123]\n"
      "v_fmac_f32 v128, v184, v184\n"
      "v_fmac_f32 v129, v107, v107\n"
      "v_mfma_f32_16x16x32_bf16 v[114:117], a[24:27], v[156:159], 0\n"
      "v_fmac_f32 v128, v185, v185\n"
      "v_fmac_f32 v129, v100, v100\n"
      "v_mfma_f32_16x16x32_bf16 v[114:117], a[28:31], v[152:155], v[114:117]\n"
      "s_waitcnt lgkmcnt(0)\n"
      "v_fmac_f32 v128, v124, v124\n"
      "v_fmac_f32 v129, v101, v101\n"
      "v_mfma_f32_16x16x32_bf16 v[114:117], a[32:35], v[202:205], v[114:117]\n"
      "buffer_store_dwordx4 v[140:143], v21, s[20:23], s46 offen sc1\n"
      "v_mfma_f32_16x16x32_bf16 v[114:117], a[36:39], v[136:139], v[114:117]\n"
      "v_fmac_f32 v128, v125, v125\n"
      "v_fmac_f32 v129, v102, v102\n"
      "v_mfma_f32_16x16x32_bf16 v[114:117], a[40:43], v[220:223], v[114:117]\n"
      "v_fmac_f32 v128, v126, v126\n"
      "v_fmac_f32 v129, v103, v103\n"
      "v_mfma_f32_16x16x32_bf16 v[114:117], a[44:47], v[144:147], v[114:117]\n"
      "v_fmac_f32_e64 v128, v127, v127\n"
      "v_fma_f32 v119, v120, v120, s51\n"
      "v_mfma_f32_16x16x32_bf16 v[164:167], a[48:51], v[156:159], 0\n"
      "v_permlane16_swap_b32 v129, v128\n"
      "v_mfma_f32_16x16x32_bf16 v[164:167], a[52:55], v[152:155], v[164:167]\n"
      "v_add_f32 v129, v129, v128\n"
      "v_mov_b32_e64 v128, v129\n"
      "v_mfma_f32_16x16x32_bf16 v[164:167], a[56:59], v[202:205], v[164:167]\n"
      "v_fmac_f32 v119, v121, v121\n"
      "v_permlane32_swap_b32 v129, v128\n"
      "v_mfma_f32_16x16x32_bf16 v[164:167], a[60:63], v[136:139], v[164:167]\n"
      "v_add_f32 v129, v129, v128\n"
      "v_rsq_f32 v129, v129\n"
      "v_mfma_f32_16x16x32_bf16 v[164:167], a[64:67], v[220:223], v[164:167]\n"
      "buffer_store_dwordx4 v[160:163], v22, s[20:23], s46 offen sc1\n"
      "v_mfma_f32_16x16x32_bf16 v[164:167], a[68:71], v[144:147], v[164:167]\n"
      "v_mov_b32 v128, v129\n"
      "v_fmac_f32 v119, v122, v122\n"
      "v_mfma_f32_16x16x32_bf16 v[132:135], a[72:75], v[156:159], 0\n"
      "v_permlane16_swap_b32 v129, v128\n"
      "v_mfma_f32_16x16x32_bf16 v[132:135], a[76:79], v[152:155], v[132:135]\n"
      "v_mul_f32 v129, s50, v129\n"
      "v_mul_f32_e64 v170, v128, v170\n"
      "v_mfma_f32_16x16x32_bf16 v[132:135], a[80:83], v[202:205], v[132:135]\n"
      "v_mul_f32 v171, v128, v171\n"
      "v_mul_f32 v174, v129, v174\n"
      "v_mfma_f32_16x16x32_bf16 v[132:135], a[84:87], v[136:139], v[132:135]\n"
      "v_mul_f32_e64 v175, v129, v175\n"
      "v_cvt_pk_bf16_f32 v170, v170, v171\n"
      "v_mfma_f32_16x16x32_bf16 v[132:135], a[88:91], v[220:223], v[132:135]\n"
      "v_mul_f32 v172, v128, v172\n"
      "v_mul_f32 v173, v128, v173\n"
      "v_mfma_f32_16x16x32_bf16 v[132:135], a[92:95], v[144:147], v[132:135]\n"
      "v_mul_f32 v124, v128, v124\n"
      "v_mul_f32 v125, v128, v125\n"
      "v_mfma_f32_16x16x32_bf16 v[140:143], a[96:99], v[156:159], 0\n"
      "buffer_store_dwordx4 v[206:209], v23, s[20:23], s46 offen sc1\n"
      "v_mfma_f32_16x16x32_bf16 v[140:143], a[100:103], v[152:155], v[140:143]\n"
      "v_cvt_pk_bf16_f32 v174, v174, v175\n"
      "v_mul_f32 v176, v129, v176\n"
      "v_mfma_f32_16x16x32_bf16 v[140:143], a[104:107], v[202:205], v[140:143]\n"
      "v_mul_f32 v177, v129, v177\n"
      "v_mul_f32_e64 v100, v129, v100\n"
      "v_mfma_f32_16x16x32_bf16 v[140:143], a[108:111], v[136:139], v[140:143]\n"
      "v_mul_f32 v101, v129, v101\n"
      "v_cvt_pk_bf16_f32 v171, v172, v173\n"
      "v_mfma_f32_16x16x32_bf16 v[140:143], a[112:115], v[220:223], v[140:143]\n"
      "v_mul_f32 v182, v128, v182\n"
      "v_mul_f32 v183, v128, v183\n"
      "v_mfma_f32_16x16x32_bf16 v[140:143], a[116:119], v[144:147], v[140:143]\n"
      "v_mul_f32 v184, v128, v184\n"
      "v_mul_f32_e64 v185, v128, v185\n"
      "v_mfma_f32_16x16x32_bf16 v[206:209], a[120:123], v[156:159], 0\n"
      "v_cvt_pk_bf16_f32 v124, v124, v125\n"
      "v_mul_f32 v126, v128, v126\n"
      "v_mfma_f32_16x16x32_bf16 v[206:209], a[124:127], v[152:155], v[206:209]\n"
      "v_mul_f32 v127, v128, v127\n"
      "v_cvt_pk_bf16_f32 v175, v176, v177\n"
      "v_mfma_f32_16x16x32_bf16 v[206:209], a[128:131], v[202:205], v[206:209]\n"
      "buffer_store_dwordx4 v[110:113], v25, s[20:23], s46 offen sc1\n"
      "v_mfma_f32_16x16x32_bf16 v[206:209], a[132:135], v[136:139], v[206:209]\n"
      "v_mul_f32 v104, v129, v104\n"
      "v_mul_f32 v105, v129, v105\n"
      "v_mfma_f32_16x16x32_bf16 v[206:209], a[136:139], v[220:223], v[206:209]\n"
      "v_mul_f32 v106, v129, v106\n"
      "v_mul_f32 v107, v129, v107\n"
      "v_mfma_f32_16x16x32_bf16 v[206:209], a[140:143], v[144:147], v[206:209]\n"
      "v_cvt_pk_bf16_f32 v100, v100, v101\n"
      "v_mul_f32 v102, v129, v102\n"
      "v_mfma_f32_16x16x32_bf16 v[160:163], v[156:159], a[144:147], 0\n"
      "v_mul_f32 v103, v129, v103\n"
      "v_cvt_pk_bf16_f32 v172, v182, v183\n"
      "v_mfma_f32_16x16x32_bf16 v[160:163], v[152:155], a[148:151], v[160:163]\n"
      "v_cvt_pk_bf16_f32 v173, v184, v185\n"
      "v_cvt_pk_bf16_f32 v125, v126, v127\n"
      "v_mfma_f32_16x16x32_bf16 v[160:163], v[202:205], a[152:155], v[160:163]\n"
      "v_cvt_pk_bf16_f32 v176, v104, v105\n"
      "v_cvt_pk_bf16_f32 v177, v106, v107\n"
      "v_mfma_f32_16x16x32_bf16 v[160:163], v[136:139], a[156:159], v[160:163]\n"
      "ds_read_b128 v[182:185], v2 offset:18432\n"
      "v_cvt_pk_bf16_f32 v101, v102, v103\n"
      "v_mfma_f32_16x16x32_bf16 v[160:163], v[220:223], a[160:163], v[160:163]\n"
      "ds_read_b128 v[104:107], v2 offset:21504\n"
      "v_fma_f32 v113, v132, v132, s51\n"
      "v_mfma_f32_16x16x32_bf16 v[160:163], v[144:147], a[164:167], v[160:163]\n"
      "ds_read_b128 v[126:129], v2 offset:24576\n"
      "v_fmac_f32 v113, v133, v133\n"
      "v_fmac_f32 v113, v134, v134\n"
      "v_fmac_f32 v119, v123, v123\n"
      "v_fmac_f32 v113, v135, v135\n"
      "v_fmac_f32 v119, v114, v114\n"
      "v_fmac_f32 v113, v140, v140\n"
      "v_fmac_f32 v119, v115, v115\n"
      "v_fmac_f32 v113, v141, v141\n"
      "v_fmac_f32 v119, v116, v116\n"
      "v_fmac_f32 v113, v142, v142\n"
      "v_fmac_f32 v119, v117, v117\n"
      "v_fmac_f32 v113, v143, v143\n"
      "v_fmac_f32 v119, v164, v164\n"
      "v_fmac_f32 v113, v206, v206\n"
      "v_fmac_f32 v119, v165, v165\n"
      "v_fmac_f32 v113, v207, v207\n"
      "v_fmac_f32 v119, v166, v166\n"
      "v_fmac_f32 v113, v208, v208\n"
      "v_fmac_f32 v119, v167, v167\n"
      "v_fmac_f32 v113, v209, v209\n"
      "v_lshl_add_u32 v82, v76, 3, v82\n"
      "v_add_u32 v82, s77, v82\n"
      "v_permlane16_swap_b32 v119, v113\n"
      "v_add_f32 v119, v119, v113\n"
      "v_mov_b32 v113, v119\n"
      "v_and_b32 v94, 2, v82\n"
      "v_and_b32 v82, -4, v82\n"
      "v_permlane32_swap_b32 v119, v113\n"
      "v_add_f32 v119, v119, v113\n"
      "v_rsq_f32 v119, v119\n"
      "v_lshlrev_b32 v76, 2, v76\n"
      "v_mov_b32 v113, v119\n"
      "v_sub_u32 v77, v77, v76\n"
      "v_add_u32 v102, 110592, v82\n"
      "v_permlane16_swap_b32 v119, v113\n"
      "v_mul_f32 v119, s50, v119\n"
      "v_mul_f32 v132, v113, v132\n"
      "v_mul_f32 v133, v113, v133\n"
      "v_mul_f32 v120, v119, v120\n"
      "v_mul_f32 v121, v119, v121\n"
      "v_cvt_pk_bf16_f32 v132, v132, v133\n"
      "v_mul_f32 v134, v113, v134\n"
      "v_mul_f32 v135, v113, v135\n"
      "v_mul_f32 v206, v113, v206\n"
      "v_mul_f32 v207, v113, v207\n"
      "v_cvt_pk_bf16_f32 v120, v120, v121\n"
      "v_mul_f32 v122, v119, v122\n"
      "v_mul_f32 v123, v119, v123\n"
      "v_mul_f32 v164, v119, v164\n"
      "v_mul_f32 v165, v119, v165\n"
      "v_cvt_pk_bf16_f32 v133, v134, v135\n"
      "v_mul_f32 v140, v113, v140\n"
      "v_mul_f32 v141, v113, v141\n"
      "v_mul_f32 v142, v113, v142\n"
      "v_mul_f32 v143, v113, v143\n"
      "v_cvt_pk_bf16_f32 v206, v206, v207\n"
      "v_mul_f32 v208, v113, v208\n"
      "v_mul_f32 v209, v113, v209\n"
      "v_cvt_pk_bf16_f32 v121, v122, v123\n"
      "v_mul_f32 v114, v119, v114\n"
      "v_mul_f32 v115, v119, v115\n"
      "v_mul_f32 v116, v119, v116\n"
      "v_mul_f32 v117, v119, v117\n"
      "v_cvt_pk_bf16_f32 v164, v164, v165\n"
      "v_mul_f32_e64 v166, v119, v166\n"
      "v_mul_f32_e64 v167, v119, v167\n"
      "v_cvt_pk_bf16_f32 v134, v140, v141\n"
      "v_mfma_f32_16x16x32_bf16 v[110:113], v[156:159], a[168:171], 0\n"
      "v_cvt_pk_bf16_f32 v135, v142, v143\n"
      "v_cvt_pk_bf16_f32 v207, v208, v209\n"
      "v_mfma_f32_16x16x32_bf16 v[110:113], v[152:155], a[172:175], v[110:113]\n"
      "v_cvt_pk_bf16_f32 v122, v114, v115\n"
      "v_cvt_pk_bf16_f32 v123, v116, v117\n"
      "v_mfma_f32_16x16x32_bf16 v[110:113], v[202:205], a[176:179], v[110:113]\n"
      "v_cvt_pk_bf16_f32 v165, v166, v167\n"
      "v_lshlrev_b32_e64 v77, 4, v77\n"
      "v_mfma_f32_16x16x32_bf16 v[110:113], v[136:139], a[180:183], v[110:113]\n"
      "v_cmp_gt_u32 s[72:73], 64, v77\n"
      "v_mov_b32 v166, 0x3fb9\n"
      "v_mfma_f32_16x16x32_bf16 v[110:113], v[220:223], a[184:187], v[110:113]\n"
      "v_mov_b32_e64 v167, 0\n"
      "v_lshlrev_b64 v[166:167], v77, v[166:167]\n"
      "v_mfma_f32_16x16x32_bf16 v[110:113], v[144:147], a[188:191], v[110:113]\n"
      "v_cndmask_b32_e64 v166, 0, v166, s[72:73]\n"
      "v_cndmask_b32_e64 v167, 0, v167, s[72:73]\n"
      "v_mfma_f32_16x16x32_bf16 v[116:119], v[156:159], a[192:195], 0\n"
      "ds_read_b128 v[156:159], v2 offset:27648\n"
      "v_cmp_gt_u32 s[72:73], 64, v77\n"
      "v_mfma_f32_16x16x32_bf16 v[116:119], v[152:155], a[196:199], v[116:119]\n"
      "ds_read_b128 v[152:155], v2 offset:30720\n"
      "v_mov_b32 v208, 0xbb2b\n"
      "v_mfma_f32_16x16x32_bf16 v[116:119], v[202:205], a[200:203], v[116:119]\n"
      "ds_read_b128 v[202:205], v2 offset:33792\n"
      "v_mov_b32 v209, 0\n"
      "v_mfma_f32_16x16x32_bf16 v[116:119], v[136:139], a[204:207], v[116:119]\n"
      "s_waitcnt lgkmcnt(3)\n"
      "v_lshlrev_b64 v[208:209], v77, v[208:209]\n"
      "v_cndmask_b32_e64 v208, 0, v208, s[72:73]\n"
      "v_mfma_f32_16x16x32_bf16 v[116:119], v[220:223], a[208:211], v[116:119]\n"
      "v_cndmask_b32_e64 v209, 0, v209, s[72:73]\n"
      "v_add_u32 v115, 112160, v82\n"
      "v_mfma_f32_16x16x32_bf16 v[116:119], v[144:147], a[212:215], v[116:119]\n"
      "v_cvt_pk_bf16_f32 v198, v198, v199\n"
      "v_cvt_pk_bf16_f32 v199, v200, v201\n"
      "v_mfma_f32_16x16x32_bf16 v[140:143], a[216:219], v[182:185], 0\n"
      "v_cvt_pk_bf16_f32 v200, v96, v97\n"
      "v_cvt_pk_bf16_f32 v201, v98, v99\n"
      "v_mfma_f32_16x16x32_bf16 v[140:143], a[220:223], v[104:107], v[140:143]\n"
      "v_cvt_pk_bf16_f32 v76, v160, v161\n"
      "v_cvt_pk_bf16_f32 v77, v162, v163\n"
      "v_mfma_f32_16x16x32_bf16 v[140:143], a[224:227], v[126:129], v[140:143]\n"
      "v_cvt_pk_bf16_f32 v214, v214, v215\n"
      "v_cvt_pk_bf16_f32 v215, v216, v217\n"
      "v_mfma_f32_16x16x32_bf16 v[220:223], a[240:243], v[182:185], 0\n"
      "v_cvt_pk_bf16_f32 v216, v178, v179\n"
      "v_cvt_pk_bf16_f32 v217, v180, v181\n"
      "v_mfma_f32_16x16x32_bf16 v[220:223], a[244:247], v[104:107], v[220:223]\n"
      "v_cvt_pk_bf16_f32 v80, v110, v111\n"
      "v_cvt_pk_bf16_f32 v81, v112, v113\n"
      "v_mfma_f32_16x16x32_bf16 v[220:223], a[248:251], v[126:129], v[220:223]\n"
      "s_waitcnt lgkmcnt(0)\n"
      "v_cvt_pk_bf16_f32 v194, v194, v195\n"
      "v_cvt_pk_bf16_f32 v195, v196, v197\n"
      "v_mfma_f32_16x16x32_bf16 v[160:163], v[232:235], v[182:185], 0\n"
      "ds_read_b128 v[136:139], v10 offset:32\n"
      "v_cvt_pk_bf16_f32 v196, v148, v149\n"
      "v_mfma_f32_16x16x32_bf16 v[160:163], v[236:239], v[104:107], v[160:163]\n"
      "v_cvt_pk_bf16_f32 v197, v150, v151\n"
      "v_cvt_pk_bf16_f32 v84, v116, v117\n"
      "v_mfma_f32_16x16x32_bf16 v[160:163], v[240:243], v[126:129], v[160:163]\n"
      "v_cvt_pk_bf16_f32 v85, v118, v119\n"
      "v_add_u32 v92, 113728, v82\n"
      "v_mfma_f32_16x16x32_bf16 v[140:143], a[228:231], v[156:159], v[140:143]\n"
      "v_subrev_u32 v39, 115456, v9\n"
      "v_lshrrev_b32 v39, 3, v39\n"
      "v_mfma_f32_16x16x32_bf16 v[140:143], a[232:235], v[152:155], v[140:143]\n"
      "ds_read_b64 v[144:145], v10 offset:24\n"
      "v_min_u32 v39, 96, v39\n"
      "v_mfma_f32_16x16x32_bf16 v[140:143], a[236:239], v[202:205], v[140:143]\n"
      "ds_read2_b32 v[182:183], v102 offset0:0 offset1:1\n"
      "v_add_u32_e64 v39, s77, v39\n"
      "v_mfma_f32_16x16x32_bf16 v[220:223], a[252:255], v[156:159], v[220:223]\n"
      "ds_read2_b32 v[104:105], v102 offset0:2 offset1:10\n"
      "v_add_u32 v39, 115296, v39\n"
      "v_mfma_f32_16x16x32_bf16 v[220:223], v[224:227], v[152:155], v[220:223]\n"
      "ds_read2_b32 v[184:185], v102 offset0:8 offset1:9\n"
      "v_readlane_b32 s45, v37, s43\n"
      "v_mfma_f32_16x16x32_bf16 v[220:223], v[228:231], v[202:205], v[220:223]\n"
      "ds_read2_b32 v[180:181], v102 offset0:16 offset1:17\n"
      "v_mul_f32 v147, v140, v140\n"
      "v_mfma_f32_16x16x32_bf16 v[160:163], v[244:247], v[156:159], v[160:163]\n"
      "ds_read2_b32 v[178:179], v102 offset0:24 offset1:18\n"
      "v_add_f32 v146, v140, v141\n"
      "v_mfma_f32_16x16x32_bf16 v[160:163], v[248:251], v[152:155], v[160:163]\n"
      "v_fmac_f32 v147, v141, v141\n"
      "v_add_f32 v146, v146, v142\n"
      "v_mfma_f32_16x16x32_bf16 v[160:163], v[252:255], v[202:205], v[160:163]\n"
      "s_waitcnt lgkmcnt(4)\n"
      "v_alignbyte_b32 v182, v183, v182, v94\n"
      "v_fmac_f32 v147, v142, v142\n"
      "v_mfma_f32_16x16x32_bf16 v[126:129], v[190:193], v[210:213], v[40:43]\n"
      "s_waitcnt lgkmcnt(3)\n"
      "v_alignbyte_b32 v183, v104, v183, v94\n"
      "v_add_f32 v146, v146, v143\n"
      "v_mfma_f32_16x16x32_bf16 v[96:99], v[170:173], v[210:213], v[44:47]\n"
      "ds_read_b128 v[110:113], v2 offset:18688\n"
      "s_waitcnt lgkmcnt(3)\n"
      "v_alignbyte_b32 v184, v185, v184, v94\n"
      "v_mfma_f32_16x16x32_bf16 v[148:151], v[132:135], v[210:213], v[48:51]\n"
      "ds_read_b128 v[154:157], v2 offset:21760\n"
      "v_alignbyte_b32 v185, v105, v185, v94\n"
      "v_mfma_f32_16x16x32_bf16 v[102:105], v[136:139], v[210:213], v[24:27]\n"
      "ds_read_b128 v[210:213], v2 offset:24832\n"
      "s_waitcnt lgkmcnt(3)\n"
      "v_alignbyte_b32 v180, v181, v180, v94\n"
      "v_mfma_f32_16x16x16_bf16 v[126:129], v[130:131], v[108:109], v[126:129]\n"
      "v_alignbyte_b32 v181, v179, v181, v94\n"
      "v_alignbyte_b32 v178, v178, v178, v94\n"
      "v_fmac_f32 v147, v143, v143\n"
      "v_mfma_f32_16x16x16_bf16 v[96:99], v[124:125], v[108:109], v[96:99]\n"
      "v_add_f32 v146, v146, v220\n"
      "v_fmac_f32 v147, v220, v220\n"
      "v_add_f32 v146, v146, v221\n"
      "v_mfma_f32_16x16x16_bf16 v[148:151], v[206:207], v[108:109], v[148:151]\n"
      "v_fmac_f32 v147, v221, v221\n"
      "v_add_f32 v146, v146, v222\n"
      "v_fmac_f32 v147, v222, v222\n"
      "v_mfma_f32_16x16x16_bf16 v[102:105], v[144:145], v[108:109], v[102:105]\n"
      "v_add_f32 v146, v146, v223\n"
      "v_fmac_f32 v147, v223, v223\n"
      "v_add_f32 v146, v146, v160\n"
      "v_mfma_f32_16x16x16_bf16 v[126:129], v[166:167], v[182:183], v[126:129]\n"
      "v_fmac_f32_e64 v147, v160, v160\n"
      "v_add_f32_e64 v146, v146, v161\n"
      "v_fmac_f32_e64 v147, v161, v161\n"
      "v_mfma_f32_16x16x16_bf16 v[96:99], v[166:167], v[184:185], v[96:99]\n"
      "v_add_f32_e64 v146, v146, v162\n"
      "v_fmac_f32_e64 v147, v162, v162\n"
      "v_add_f32_e64 v146, v146, v163\n"
      "v_mfma_f32_16x16x16_bf16 v[148:151], v[166:167], v[180:181], v[148:151]\n"
      "v_fmac_f32 v147, v163, v163\n"
      "s_waitcnt vmcnt(6)\n"
      "v_or3_b32 v186, v186, v187, v188\n"
      "v_readlane_b32 s46, v37, s42\n"
      "v_mfma_f32_16x16x16_bf16 v[102:105], v[166:167], v[178:179], v[102:105]\n"
      "v_permlane16_swap_b32 v146, v147\n"
      "v_add_f32_e64 v146, v146, v147\n"
      "v_mfma_f32_16x16x16_bf16 v[126:129], v[208:209], v[182:183], v[126:129]\n"
      "s_waitcnt vmcnt(5)\n"
      "v_or3_b32 v186, v186, v189, v78\n"
      "v_and_b32 v186, 0x7fff7fff, v186\n"
      "v_readlane_b32 s47, v38, s43\n"
      "v_mfma_f32_16x16x16_bf16 v[96:99], v[208:209], v[184:185], v[96:99]\n"
      "v_add_u32 v12, 32, v12\n"
      "ds_write_b32 v31, v186\n"
      "v_mfma_f32_16x16x16_bf16 v[148:151], v[208:209], v[180:181], v[148:151]\n"
      "s_or_b32 s95, s45, s46\n"
      "v_max3_f32 v78, v126, v127, v128\n"
      "s_or_b32 s95, s95, s48\n"
      "v_mfma_f32_16x16x16_bf16 v[102:105], v[208:209], v[178:179], v[102:105]\n"
      "s_cmp_ge_u32 s95, 0x40000000\n"
      "s_cselect_b32 s94, -1, -2\n"
      "v_max3_f32 v204, v129, v96, v97\n"
      "s_waitcnt lgkmcnt(1)\n"
      "v_mfma_f32_16x16x32_bf16 v[186:189], a[216:219], v[110:113], 0\n"
      "s_cmp_gt_u32 s42, s44\n"
      "s_cselect_b32 s95, s94, s95\n"
      "v_max3_f32 v78, v78, v98, v99\n"
      "v_mfma_f32_16x16x32_bf16 v[186:189], a[220:223], v[154:157], v[186:189]\n"
      "ds_read2_b32 v[158:159], v115 offset0:0 offset1:1\n"
      "v_max3_f32 v204, v204, v148, v149\n"
      "v_mfma_f32_16x16x32_bf16 v[186:189], a[224:227], v[210:213], v[186:189]\n"
      "ds_read2_b32 v[152:153], v115 offset0:2 offset1:10\n"
      "v_max3_f32 v78, v78, v150, v151\n"
      "v_mfma_f32_16x16x32_bf16 v[182:185], a[240:243], v[110:113], 0\n"
      "ds_read_b128 v[178:181], v9 offset:5632\n"
      "v_max3_f32 v78, v78, v204, v102\n"
      "v_mfma_f32_16x16x32_bf16 v[182:185], a[244:247], v[154:157], v[182:185]\n"
      "ds_read2_b32 v[118:119], v115 offset0:8 offset1:9\n"
      "v_mov_b32_e64 v82, v78\n"
      "v_mfma_f32_16x16x32_bf16 v[182:185], a[248:251], v[210:213], v[182:185]\n"
      "ds_read2_b32 v[204:205], v115 offset0:16 offset1:17\n"
      "v_permlane16_swap_b32 v78, v82\n"
      "v_mfma_f32_16x16x32_bf16 v[104:107], v[232:235], v[110:113], 0\n"
      "ds_read2_b32 v[202:203], v115 offset0:24 offset1:18\n"
      "v_max_f32 v78, v78, v82\n"
      "v_mfma_f32_16x16x32_bf16 v[104:107], v[236:239], v[154:157], v[104:107]\n"
      "s_and_b32 s47, s47, -4\n"
      "s_waitcnt lgkmcnt(5)\n"
      "v_alignbyte_b32 v158, v159, v158, v94\n"
      "v_mfma_f32_16x16x32_bf16 v[104:107], v[240:243], v[210:213], v[104:107]\n"
      "s_add_u32 s42, s42, 1\n"
      "s_waitcnt lgkmcnt(4)\n"
      "v_alignbyte_b32 v159, v152, v159, v94\n"
      "v_mfma_f32_16x16x32_bf16 v[210:213], v[190:193], v[174:177], v[52:55]\n"
      "s_add_u32 s43, s43, 1\n"
      "v_mov_b32 v82, v78\n"
      "s_waitcnt lgkmcnt(3)\n"
      "v_or3_b32 v178, v178, v179, v180\n"
      "v_mfma_f32_16x16x32_bf16 v[108:111], v[170:173], v[174:177], v[56:59]\n"
      "s_waitcnt lgkmcnt(2)\n"
      "v_alignbyte_b32 v118, v119, v118, v94\n"
      "v_alignbyte_b32 v119, v153, v119, v94\n"
      "v_mfma_f32_16x16x32_bf16 v[152:155], v[132:135], v[174:177], v[60:63]\n"
      "s_waitcnt lgkmcnt(1)\n"
      "v_alignbyte_b32 v204, v205, v204, v94\n"
      "v_permlane32_swap_b32 v78, v82\n"
      "v_mfma_f32_16x16x32_bf16 v[114:117], v[136:139], v[174:177], v[26:29]\n"
      "s_waitcnt lgkmcnt(0)\n"
      "v_alignbyte_b32 v205, v203, v205, v94\n"
      "v_alignbyte_b32 v202, v202, v202, v94\n"
      "v_mfma_f32_16x16x16_bf16 v[210:213], v[130:131], v[100:101], v[210:213]\n"
      "v_max_f32 v78, v78, v82\n"
      "v_sub_f32 v126, v126, v78\n"
      "v_sub_f32 v127, v127, v78\n"
      "v_mfma_f32_16x16x16_bf16 v[108:111], v[124:125], v[100:101], v[108:111]\n"
      "v_sub_f32 v128, v128, v78\n"
      "v_sub_f32 v129, v129, v78\n"
      "v_exp_f32 v126, v126\n"
      "v_mfma_f32_16x16x16_bf16 v[152:155], v[206:207], v[100:101], v[152:155]\n"
      "v_exp_f32 v127, v127\n"
      "v_sub_f32 v96, v96, v78\n"
      "v_mfma_f32_16x16x16_bf16 v[114:117], v[144:145], v[100:101], v[114:117]\n"
      "v_sub_f32 v97, v97, v78\n"
      "v_sub_f32 v98, v98, v78\n"
      "v_sub_f32 v99, v99, v78\n"
      "v_mfma_f32_16x16x16_bf16 v[210:213], v[166:167], v[158:159], v[210:213]\n"
      "v_exp_f32 v128, v128\n"
      "v_exp_f32 v129, v129\n"
      "v_mfma_f32_16x16x16_bf16 v[108:111], v[166:167], v[118:119], v[108:111]\n"
      "v_exp_f32 v96, v96\n"
      "v_exp_f32 v97, v97\n"
      "v_mfma_f32_16x16x16_bf16 v[152:155], v[166:167], v[204:205], v[152:155]\n"
      "v_exp_f32 v98, v98\n"
      "v_exp_f32 v99, v99\n"
      "v_mfma_f32_16x16x16_bf16 v[114:117], v[166:167], v[202:203], v[114:117]\n"
      "v_cvt_pk_bf16_f32 v126, v126, v127\n"
      "v_cvt_pk_bf16_f32 v127, v128, v129\n"
      "v_cvt_pk_bf16_f32 v128, v96, v97\n"
      "v_mfma_f32_16x16x16_bf16 v[210:213], v[208:209], v[158:159], v[210:213]\n"
      "v_cvt_pk_bf16_f32 v129, v98, v99\n"
      "v_sub_f32 v148, v148, v78\n"
      "v_sub_f32 v149, v149, v78\n"
      "v_mfma_f32_16x16x16_bf16 v[108:111], v[208:209], v[118:119], v[108:111]\n"
      "v_sub_f32 v150, v150, v78\n"
      "v_sub_f32 v151, v151, v78\n"
      "v_sub_f32 v102, v102, v78\n"
      "v_mfma_f32_16x16x16_bf16 v[152:155], v[208:209], v[204:205], v[152:155]\n"
      "v_exp_f32 v148, v148\n"
      "v_exp_f32_e64 v149, v149\n"
      "v_mfma_f32_16x16x16_bf16 v[114:117], v[208:209], v[202:203], v[114:117]\n"
      "ds_read_b128 v[98:101], v2 offset:27904\n"
      "ds_read_b128 v[174:177], v2 offset:30976\n"
      "ds_read_b128 v[156:159], v2 offset:34048\n"
      "v_exp_f32 v150, v150\n"
      "v_exp_f32 v151, v151\n"
      "v_exp_f32_e64 v102, v102\n"
      "v_cvt_pk_bf16_f32 v88, v148, v149\n"
      "v_cvt_pk_bf16_f32 v89, v150, v151\n"
      "v_cvt_pk_bf16_f32 v90, v102, 0\n"
      "v_max3_f32 v205, v210, v211, v212\n"
      "v_max3_f32 v78, v213, v108, v109\n"
      "v_max3_f32 v205, v205, v110, v111\n"
      "v_max3_f32 v78, v78, v152, v153\n"
      "v_max3_f32 v205, v205, v154, v155\n"
      "v_max3_f32 v205, v205, v78, v114\n"
      "v_mov_b32 v203, v205\n"
      "v_or_b32 v178, v178, v181\n"
      "v_cmp_ne_u32 s[56:57], 0, v178\n"
      "s_waitcnt lgkmcnt(2)\n"
      "v_mfma_f32_16x16x32_bf16 v[186:189], a[228:231], v[98:101], v[186:189]\n"
      "ds_read_u16 v78, v11\n"
      "v_permlane16_swap_b32_e64 v205, v203\n"
      "s_waitcnt lgkmcnt(2)\n"
      "v_mfma_f32_16x16x32_bf16 v[186:189], a[232:235], v[174:177], v[186:189]\n"
      "s_waitcnt lgkmcnt(1)\n"
      "v_max_f32 v205, v205, v203\n"
      "v_mov_b32_e64 v203, v205\n"
      "v_mfma_f32_16x16x32_bf16 v[186:189], a[236:239], v[156:159], v[186:189]\n"
      "ds_read_u16 v82, v11 offset:32\n"
      "v_permlane32_swap_b32 v205, v203\n"
      "v_mfma_f32_16x16x32_bf16 v[182:185], a[252:255], v[98:101], v[182:185]\n"
      "ds_read_u16 v86, v11 offset:64\n"
      "v_max_f32 v205, v205, v203\n"
      "v_mfma_f32_16x16x32_bf16 v[182:185], v[224:227], v[174:177], v[182:185]\n"
      "s_cmp_ge_u32 s95, 0x40000000\n"
      "s_cselect_b64 vcc, -1, s[56:57]\n"
      "v_sub_f32 v210, v210, v205\n"
      "v_mfma_f32_16x16x32_bf16 v[182:185], v[228:231], v[156:159], v[182:185]\n"
      "v_sub_f32 v211, v211, v205\n"
      "v_sub_f32 v212, v212, v205\n"
      "v_mfma_f32_16x16x32_bf16 v[104:107], v[244:247], v[98:101], v[104:107]\n"
      "v_sub_f32 v213, v213, v205\n"
      "v_exp_f32 v210, v210\n"
      "v_mfma_f32_16x16x32_bf16 v[104:107], v[248:251], v[174:177], v[104:107]\n"
      "v_exp_f32 v211, v211\n"
      "v_mfma_f32_16x16x32_bf16 v[104:107], v[252:255], v[156:159], v[104:107]\n"
      "s_waitcnt lgkmcnt(1)\n"
      "v_sub_f32 v108, v108, v205\n"
      "v_sub_f32_e64 v109, v109, v205\n"
      "v_mfma_f32_16x16x32_bf16 v[174:177], v[32:35], v[126:129], 0\n"
      "ds_read2_b32 v[100:101], v92 offset0:0 offset1:1\n"
      "v_sub_f32 v110, v110, v205\n"
      "v_mfma_f32_16x16x32_bf16 v[174:177], v[32:35], v[88:91], v[174:177]\n"
      "ds_read2_b32 v[202:203], v92 offset0:2 offset1:10\n"
      "v_sub_f32 v111, v111, v205\n"
      "v_mfma_f32_16x16x32_bf16 v[156:159], v[198:201], v[126:129], 0\n"
      "ds_read2_b32 v[112:113], v92 offset0:8 offset1:9\n"
      "v_exp_f32_e64 v212, v212\n"
      "v_mfma_f32_16x16x32_bf16 v[156:159], v[76:79], v[88:91], v[156:159]\n"
      "ds_read2_b32 v[102:103], v92 offset0:16 offset1:17\n"
      "v_exp_f32_e64 v213, v213\n"
      "v_mfma_f32_16x16x32_bf16 v[176:179], v[214:217], v[126:129], 0\n"
      "ds_read2_b32 v[98:99], v92 offset0:24 offset1:18\n"
      "v_exp_f32_e64 v108, v108\n"
      "v_mfma_f32_16x16x32_bf16 v[176:179], v[80:83], v[88:91], v[176:179]\n"
      "s_waitcnt lgkmcnt(5)\n"
      "s_waitcnt lgkmcnt(4)\n"
      "v_alignbyte_b32 v100, v101, v100, v94\n"
      "v_mfma_f32_16x16x32_bf16 v[148:151], v[194:197], v[126:129], 0\n"
      "v_exp_f32 v109, v109\n"
      "v_mfma_f32_16x16x32_bf16 v[148:151], v[84:87], v[88:91], v[148:151]\n"
      "s_waitcnt lgkmcnt(3)\n"
      "v_alignbyte_b32 v101, v202, v101, v94\n"
      "s_waitcnt lgkmcnt(2)\n"
      "v_alignbyte_b32 v112, v113, v112, v94\n"
      "v_mfma_f32_16x16x32_bf16 v[116:119], v[190:193], v[120:123], v[64:67]\n"
      "v_alignbyte_b32 v113, v203, v113, v94\n"
      "s_waitcnt lgkmcnt(1)\n"
      "v_alignbyte_b32 v102, v103, v102, v94\n"
      "v_exp_f32 v110, v110\n"
      "s_waitcnt lgkmcnt(0)\n"
      "v_alignbyte_b32 v103, v99, v103, v94\n"
      "v_alignbyte_b32 v98, v98, v98, v94\n"
      "v_exp_f32_e64 v111, v111\n"
      "v_cvt_pk_bf16_f32 v210, v210, v211\n"
      "v_cvt_pk_bf16_f32 v211, v212, v213\n"
      "v_cvt_pk_bf16_f32 v212, v108, v109\n"
      "v_mfma_f32_16x16x32_bf16 v[126:129], v[170:173], v[120:123], v[68:71]\n"
      "v_cvt_pk_bf16_f32 v213, v110, v111\n"
      "v_sub_f32 v152, v152, v205\n"
      "v_sub_f32 v153, v153, v205\n"
      "v_sub_f32 v154, v154, v205\n"
      "v_sub_f32 v155, v155, v205\n"
      "v_sub_f32 v114, v114, v205\n"
      "v_mfma_f32_16x16x32_bf16 v[108:111], v[132:135], v[120:123], v[72:75]\n"
      "v_exp_f32 v152, v152\n"
      "v_mfma_f32_16x16x32_bf16 v[202:205], v[136:139], v[120:123], v[28:31]\n"
      "v_exp_f32_e64 v153, v153\n"
      "v_mfma_f32_16x16x16_bf16 v[116:119], v[130:131], v[164:165], v[116:119]\n"
      "v_exp_f32 v154, v154\n"
      "v_exp_f32 v155, v155\n"
      "v_mfma_f32_16x16x16_bf16 v[126:129], v[124:125], v[164:165], v[126:129]\n"
      "v_exp_f32_e64 v114, v114\n"
      "v_cvt_pk_bf16_f32 v92, v152, v153\n"
      "v_mfma_f32_16x16x16_bf16 v[108:111], v[206:207], v[164:165], v[108:111]\n"
      "v_cvt_pk_bf16_f32 v93, v154, v155\n"
      "v_cvt_pk_bf16_f32 v94, v114, 0\n"
      "v_mul_f32 v181, v186, v186\n"
      "v_mfma_f32_16x16x16_bf16 v[202:205], v[144:145], v[164:165], v[202:205]\n"
      "v_add_f32 v180, v186, v187\n"
      "v_fmac_f32 v181, v187, v187\n"
      "v_add_f32 v180, v180, v188\n"
      "v_mfma_f32_16x16x16_bf16 v[116:119], v[166:167], v[100:101], v[116:119]\n"
      "v_fmac_f32 v181, v188, v188\n"
      "v_add_f32 v180, v180, v189\n"
      "v_fmac_f32 v181, v189, v189\n"
      "v_mfma_f32_16x16x16_bf16 v[126:129], v[166:167], v[112:113], v[126:129]\n"
      "v_add_f32 v180, v180, v182\n"
      "v_fmac_f32 v181, v182, v182\n"
      "v_add_f32 v180, v180, v183\n"
      "v_mfma_f32_16x16x16_bf16 v[108:111], v[166:167], v[102:103], v[108:111]\n"
      "v_fmac_f32 v181, v183, v183\n"
      "v_add_f32 v180, v180, v184\n"
      "v_fmac_f32 v181, v184, v184\n"
      "v_mfma_f32_16x16x16_bf16 v[202:205], v[166:167], v[98:99], v[202:205]\n"
      "v_add_f32 v180, v180, v185\n"
      "v_fmac_f32 v181, v185, v185\n"
      "v_add_f32 v180, v180, v104\n"
      "v_mfma_f32_16x16x16_bf16 v[116:119], v[208:209], v[100:101], v[116:119]\n"
      "v_fmac_f32 v181, v104, v104\n"
      "v_add_f32 v180, v180, v105\n"
      "v_fmac_f32_e64 v181, v105, v105\n"
      "v_mfma_f32_16x16x16_bf16 v[126:129], v[208:209], v[112:113], v[126:129]\n"
      "v_add_f32 v180, v180, v106\n"
      "v_fmac_f32 v181, v106, v106\n"
      "ds_read_b128 v[120:123], v2 offset:18944\n"
      "v_mfma_f32_16x16x16_bf16 v[108:111], v[208:209], v[102:103], v[108:111]\n"
      "ds_read_b128 v[152:155], v2 offset:22016\n"
      "v_max3_f32 v165, v116, v117, v118\n"
      "v_mfma_f32_16x16x16_bf16 v[202:205], v[208:209], v[98:99], v[202:205]\n"
      "ds_read_b128 v[112:115], v2 offset:25088\n"
      "v_max3_f32 v103, v119, v126, v127\n"
      "v_max3_f32 v165, v165, v128, v129\n"
      "v_add_f32 v180, v180, v107\n"
      "v_fmac_f32 v181, v107, v107\n"
      "v_max3_f32 v103, v103, v108, v109\n"
      "v_max3_f32 v165, v165, v110, v111\n"
      "v_rcp_f32 v174, v174\n"
      "v_max3_f32 v165, v165, v103, v202\n"
      "v_mov_b32 v103, v165\n"
      "v_permlane16_swap_b32 v180, v181\n"
      "v_mul_f32 v156, v174, v156\n"
      "v_permlane16_swap_b32 v165, v103\n"
      "v_max_f32 v165, v165, v103\n"
      "v_mov_b32 v103, v165\n"
      "v_mul_f32 v157, v174, v157\n"
      "v_cvt_pk_bf16_f32 v156, v156, v157\n"
      "v_permlane32_swap_b32 v165, v103\n"
      "v_max_f32 v165, v165, v103\n"
      "s_waitcnt lgkmcnt(1)\n"
      "v_mfma_f32_16x16x32_bf16 v[100:103], a[216:219], v[120:123], 0\n"
      "s_waitcnt lgkmcnt(0)\n"
      "v_sub_f32 v116, v116, v165\n"
      "v_sub_f32 v117, v117, v165\n"
      "v_mfma_f32_16x16x32_bf16 v[100:103], a[220:223], v[152:155], v[100:103]\n"
      "v_sub_f32 v118, v118, v165\n"
      "v_sub_f32 v119, v119, v165\n"
      "v_mfma_f32_16x16x32_bf16 v[100:103], a[224:227], v[112:115], v[100:103]\n"
      "v_exp_f32 v116, v116\n"
      "v_exp_f32 v117, v117\n"
      "v_sub_f32 v126, v126, v165\n"
      "v_sub_f32 v127, v127, v165\n"
      "v_sub_f32 v128, v128, v165\n"
      "v_sub_f32 v129, v129, v165\n"
      "v_exp_f32 v118, v118\n"
      "v_exp_f32 v119, v119\n"
      "v_exp_f32 v126, v126\n"
      "v_exp_f32 v127, v127\n"
      "v_exp_f32 v128, v128\n"
      "v_exp_f32 v129, v129\n"
      "v_cvt_pk_bf16_f32 v116, v116, v117\n"
      "v_cvt_pk_bf16_f32 v117, v118, v119\n"
      "v_cvt_pk_bf16_f32 v118, v126, v127\n"
      "v_cvt_pk_bf16_f32 v119, v128, v129\n"
      "v_mfma_f32_16x16x32_bf16 v[126:129], a[240:243], v[120:123], 0\n"
      "v_sub_f32 v108, v108, v165\n"
      "v_sub_f32 v109, v109, v165\n"
      "v_mfma_f32_16x16x32_bf16 v[126:129], a[244:247], v[152:155], v[126:129]\n"
      "v_sub_f32 v110, v110, v165\n"
      "v_sub_f32 v111, v111, v165\n"
      "v_mfma_f32_16x16x32_bf16 v[126:129], a[248:251], v[112:115], v[126:129]\n"
      "v_sub_f32 v202, v202, v165\n"
      "v_exp_f32 v108, v108\n"
      "v_mfma_f32_16x16x32_bf16 v[96:99], v[232:235], v[120:123], 0\n"
      "v_exp_f32_e64 v109, v109\n"
      "v_mfma_f32_16x16x32_bf16 v[96:99], v[236:239], v[152:155], v[96:99]\n"
      "ds_read_b128 v[152:155], v10\n"
      "v_exp_f32_e64 v110, v110\n"
      "v_mfma_f32_16x16x32_bf16 v[96:99], v[240:243], v[112:115], v[96:99]\n"
      "ds_read_b64 v[164:165], v10 offset:16\n"
      "v_exp_f32 v111, v111\n"
      "v_exp_f32 v202, v202\n"
      "v_cvt_pk_bf16_f32 v88, v108, v109\n"
      "v_mfma_f32_16x16x32_bf16 v[120:123], v[32:35], v[210:213], 0\n"
      "v_cvt_pk_bf16_f32 v89, v110, v111\n"
      "v_cvt_pk_bf16_f32 v90, v202, 0\n"
      "v_mfma_f32_16x16x32_bf16 v[120:123], v[32:35], v[92:95], v[120:123]\n"
      "ds_read_b128 v[108:111], v2 offset:28160\n"
      "v_mul_f32 v158, v174, v158\n"
      "v_mul_f32 v159, v174, v159\n"
      "v_add_f32 v180, v180, v181\n"
      "v_cvt_pk_bf16_f32 v157, v158, v159\n"
      "v_mul_f32 v176, v174, v176\n"
      "v_mul_f32 v177, v174, v177\n"
      "v_mul_f32 v178, v174, v178\n"
      "v_rcp_f32 v120, v120\n"
      "v_mul_f32 v179, v174, v179\n"
      "v_cvt_pk_bf16_f32 v158, v176, v177\n"
      "v_cvt_pk_bf16_f32 v159, v178, v179\n"
      "v_mfma_f32_16x16x32_bf16 v[176:179], v[198:201], v[210:213], 0\n"
      "ds_write_b128 v3, v[156:159]\n"
      "v_mfma_f32_16x16x32_bf16 v[176:179], v[76:79], v[92:95], v[176:179]\n"
      "ds_read_b128 v[202:205], v2 offset:31232\n"
      "v_mul_f32 v148, v174, v148\n"
      "v_mul_f32 v149, v174, v149\n"
      "v_cvt_pk_bf16_f32 v148, v148, v149\n"
      "v_mul_f32 v150, v174, v150\n"
      "v_mul_f32 v151, v174, v151\n"
      "v_permlane32_swap_b32_e64 v146, v180\n"
      "v_cvt_pk_bf16_f32 v149, v150, v151\n"
      "v_mfma_f32_16x16x32_bf16 v[112:115], v[214:217], v[210:213], 0\n"
      "ds_write_b64 v4, v[148:149]\n"
      "v_mfma_f32_16x16x32_bf16 v[112:115], v[80:83], v[92:95], v[112:115]\n"
      "ds_read_u16 v39, v39\n"
      "v_mul_f32 v176, v120, v176\n"
      "v_mfma_f32_16x16x32_bf16 v[148:151], v[194:197], v[210:213], 0\n"
      "s_waitcnt lgkmcnt(4)\n"
      "v_mul_f32_e64 v177, v120, v177\n"
      "v_cvt_pk_bf16_f32 v176, v176, v177\n"
      "v_mfma_f32_16x16x32_bf16 v[148:151], v[84:87], v[92:95], v[148:151]\n"
      "ds_read_b128 v[210:213], v2 offset:34304\n"
      "v_mul_f32 v178, v120, v178\n"
      "v_mfma_f32_16x16x32_bf16 v[156:159], v[152:155], v[190:193], v[30:33]\n"
      "v_mul_f32 v179, v120, v179\n"
      "v_cvt_pk_bf16_f32 v177, v178, v179\n"
      "v_mfma_f32_16x16x32_bf16 v[190:193], v[152:155], v[170:173], v[30:33]\n"
      "v_mul_f32 v112, v120, v112\n"
      "v_mul_f32 v113, v120, v113\n"
      "v_mfma_f32_16x16x32_bf16 v[172:175], v[152:155], v[132:135], v[30:33]\n"
      "v_mul_f32 v114, v120, v114\n"
      "v_mul_f32 v115, v120, v115\n"
      "v_mfma_f32_16x16x32_bf16 v[132:135], v[152:155], v[136:139], v[30:33]\n"
      "v_cvt_pk_bf16_f32 v178, v112, v113\n"
      "s_waitcnt lgkmcnt(1)\n"
      "v_lshlrev_b32 v39, 16, v39\n"
      "v_mfma_f32_16x16x16_bf16 v[156:159], v[164:165], v[130:131], v[156:159]\n"
      "v_cvt_pk_bf16_f32 v179, v114, v115\n"
      "v_mul_f32 v148, v120, v148\n"
      "v_mul_f32 v149, v120, v149\n"
      "v_mfma_f32_16x16x16_bf16 v[190:193], v[164:165], v[124:125], v[190:193]\n"
      "v_cvt_pk_bf16_f32 v148, v148, v149\n"
      "v_mul_f32 v150, v120, v150\n"
      "v_mul_f32 v151, v120, v151\n"
      "v_mfma_f32_16x16x16_bf16 v[172:175], v[164:165], v[206:207], v[172:175]\n"
      "v_cvt_pk_bf16_f32 v149, v150, v151\n"
      "v_add_f32 v146, v146, v180\n"
      "ds_write_b128 v3, v[176:179] offset:256\n"
      "v_mfma_f32_16x16x16_bf16 v[132:135], v[164:165], v[144:145], v[132:135]\n"
      "v_cndmask_b32 v156, v156, v190, s[58:59]\n"
      "v_mfma_f32_16x16x32_bf16 v[100:103], a[228:231], v[108:111], v[100:103]\n"
      "s_waitcnt lgkmcnt(1)\n"
      "v_mfma_f32_16x16x32_bf16 v[100:103], a[232:235], v[202:205], v[100:103]\n"
      "ds_write_b64 v4, v[148:149] offset:256\n"
      "v_mfma_f32_16x16x32_bf16 v[100:103], a[236:239], v[210:213], v[100:103]\n"
      "v_cndmask_b32 v156, v156, v172, s[60:61]\n"
      "v_mfma_f32_16x16x32_bf16 v[126:129], a[252:255], v[108:111], v[126:129]\n"
      "v_cndmask_b32 v156, v156, v132, s[62:63]\n"
      "v_fmac_f32_e64 v156, s53, v39\n"
      "v_mfma_f32_16x16x32_bf16 v[126:129], v[224:227], v[202:205], v[126:129]\n"
      "ds_write_b32 v8, v146\n"
      "v_mfma_f32_16x16x32_bf16 v[126:129], v[228:231], v[210:213], v[126:129]\n"
      "v_max_f32_dpp v139, v156, v156 row_ror:8 row_mask:0xf bank_mask:0xf\n"
      "v_mul_f32 v133, v100, v100\n"
      "v_mfma_f32_16x16x32_bf16 v[96:99], v[244:247], v[108:111], v[96:99]\n"
      "v_max_f32_dpp v139, v139, v139 row_ror:4 row_mask:0xf bank_mask:0xf\n"
      "v_add_f32 v132, v100, v101\n"
      "v_mfma_f32_16x16x32_bf16 v[96:99], v[248:251], v[202:205], v[96:99]\n"
      "v_max_f32_dpp v139, v139, v139 row_ror:2 row_mask:0xf bank_mask:0xf\n"
      "v_fmac_f32 v133, v101, v101\n"
      "v_mfma_f32_16x16x32_bf16 v[96:99], v[252:255], v[210:213], v[96:99]\n"
      "v_max_f32_dpp v139, v139, v139 row_ror:1 row_mask:0xf bank_mask:0xf\n"
      "v_add_f32 v132, v132, v102\n"
      "v_mfma_f32_16x16x32_bf16 v[108:111], v[32:35], v[116:119], 0\n"
      "v_fmac_f32 v133, v102, v102\n"
      "v_max_f32_dpp v139, v139, v139 row_bcast:15 row_mask:0xa bank_mask:0xf\n"
      "v_mfma_f32_16x16x32_bf16 v[108:111], v[32:35], v[88:91], v[108:111]\n"
      "v_add_f32 v132, v132, v103\n"
      "v_fmac_f32 v133, v103, v103\n"
      "v_mfma_f32_16x16x32_bf16 v[174:177], v[198:201], v[116:119], 0\n"
      "v_add_f32 v132, v132, v126\n"
      "v_fmac_f32 v133, v126, v126\n"
      "v_mfma_f32_16x16x32_bf16 v[174:177], v[76:79], v[88:91], v[174:177]\n"
      "v_add_f32 v132, v132, v127\n"
      "v_fmac_f32 v133, v127, v127\n"
      "v_mfma_f32_16x16x32_bf16 v[190:193], v[214:217], v[116:119], 0\n"
      "v_add_f32 v132, v132, v128\n"
      "v_fmac_f32_e64 v133, v128, v128\n"
      "v_mfma_f32_16x16x32_bf16 v[190:193], v[80:83], v[88:91], v[190:193]\n"
      "v_max_f32_dpp v139, v139, v139 row_bcast:31 row_mask:0xc bank_mask:0xf\n"
      "v_add_f32 v132, v132, v129\n"
      "v_mfma_f32_16x16x32_bf16 v[152:155], v[194:197], v[116:119], 0\n"
      "v_fmac_f32 v133, v129, v129\n"
      "v_add_f32_e64 v132, v132, v96\n"
      "v_mfma_f32_16x16x32_bf16 v[152:155], v[84:87], v[88:91], v[152:155]\n"
      "v_fmac_f32 v133, v96, v96\n"
      "v_readlane_b32 s72, v139, 63\n"
      "v_add_f32 v132, v132, v97\n"
      "v_fmac_f32 v133, v97, v97\n"
      "v_add_f32 v132, v132, v98\n"
      "v_fmac_f32 v133, v98, v98\n"
      "v_add_f32 v132, v132, v99\n"
      "v_fmac_f32 v133, v99, v99\n"
      "v_subrev_f32 v156, s72, v156\n"
      "v_rcp_f32 v108, v108\n"
      "v_exp_f32 v156, v156\n"
      "v_permlane16_swap_b32 v132, v133\n"
      "v_mul_f32 v174, v108, v174\n"
      "v_mul_f32 v175, v108, v175\n"
      "v_cvt_pk_bf16_f32 v156, v156, v156\n"
      "v_add_f32 v132, v132, v133\n"
      "v_cvt_pk_bf16_f32 v174, v174, v175\n"
      "v_mul_f32 v176, v108, v176\n"
      "v_mul_f32 v177, v108, v177\n"
      "ds_write_b16 v13, v156\n"
      "ds_read_b128 v[210:213], v14\n"
      "ds_read_b128 v[136:139], v14 offset:64\n"
      "v_mov_b32_e64 v133, v132\n"
      "v_cvt_pk_bf16_f32 v175, v176, v177\n"
      "v_mul_f32 v190, v108, v190\n"
      "v_mul_f32 v191, v108, v191\n"
      "v_mul_f32 v192, v108, v192\n"
      "v_mul_f32 v193, v108, v193\n"
      "v_cvt_pk_bf16_f32 v176, v190, v191\n"
      "v_cvt_pk_bf16_f32 v177, v192, v193\n"
      "v_mul_f32 v152, v108, v152\n"
      "v_mul_f32 v153, v108, v153\n"
      "ds_write_b128 v3, v[174:177] offset:512\n"
      "v_cvt_pk_bf16_f32 v152, v152, v153\n"
      "v_mul_f32_e64 v154, v108, v154\n"
      "v_mul_f32_e64 v155, v108, v155\n"
      "v_permlane32_swap_b32_e64 v132, v133\n"
      "v_cvt_pk_bf16_f32 v153, v154, v155\n"
      "s_waitcnt lgkmcnt(2)\n"
      "v_mfma_f32_16x16x32_bf16 v[108:111], v[210:213], v[32:35], 0\n"
      "ds_write_b64 v4, v[152:153] offset:512\n"
      "s_waitcnt lgkmcnt(2)\n"
      "v_mfma_f32_16x16x32_bf16 v[108:111], v[136:139], v[32:35], v[108:111]\n"
      "v_add_f32 v132, v132, v133\n"
      "ds_write_b32 v8, v132 offset:512\n"
      "s_waitcnt lgkmcnt(0)\n"
      "s_barrier\n"
      "s_cbranch_vccz L_sel1_%=\n"
      "s_branch L_disp1_%=\n"
      "L_tail1_%=:\n"
      "s_nop 4\n"
      "v_mfma_f32_16x16x32_bf16 v[174:177], v[210:213], v[198:201], 0\n"
      "ds_read_b128 v[198:201], v9\n"
      "ds_read_b128 v[154:157], v9 offset:1024\n"
      "v_mfma_f32_16x16x32_bf16 v[174:177], v[136:139], v[76:79], v[174:177]\n"
      "ds_read_b128 v[116:119], v2\n"
      "ds_read_b128 v[132:135], v2 offset:3072\n"
      "v_mfma_f32_16x16x32_bf16 v[190:193], v[210:213], v[214:217], 0\n"
      "ds_read_b128 v[170:173], v2 offset:6144\n"
      "v_mfma_f32_16x16x32_bf16 v[190:193], v[136:139], v[80:83], v[190:193]\n"
      "v_mfma_f32_16x16x32_bf16 v[214:217], v[210:213], v[194:197], 0\n"
      "v_mfma_f32_16x16x32_bf16 v[214:217], v[136:139], v[84:87], v[214:217]\n"
      "ds_read_b128 v[194:197], v2 offset:9216\n"
      "s_waitcnt lgkmcnt(5)\n"
      "v_add_f32 v198, v198, v200\n"
      "v_add_f32 v199, v199, v201\n"
      "v_mov_b32_dpp v108, v108 quad_perm:[0,0,0,0] row_mask:0xf bank_mask:0xf\n"
      "s_waitcnt lgkmcnt(4)\n"
      "v_add_f32 v154, v154, v156\n"
      "v_add_f32 v155, v155, v157\n"
      "v_add_f32 v198, v198, v154\n"
      "v_add_f32 v199, v199, v155\n"
      "v_fma_f32 v154, -v198, v198, s52\n"
      "v_fmac_f32 v154, 0x43400000, v199\n"
      "v_max_f32 v154, s52, v154\n"
      "v_rsq_f32 v154, v154\n"
      "v_rcp_f32_e64 v108, v108\n"
      "v_cndmask_b32 v174, v174, v190, s[58:59]\n"
      "v_cndmask_b32 v174, v174, v214, s[60:61]\n"
      "v_mul_f32 v111, 0x43400000, v154\n"
      "v_mul_f32 v110, -v198, v154\n"
      "v_mul_f32 v174, v108, v174\n"
      "v_mov_b32 v108, v111\n"
      "v_mov_b32_e64 v109, v110\n"
      "v_cvt_pk_bf16_f32 v174, v174, v174\n"
      "ds_write_b16 v12, v174\n"
      "ds_read_b128 v[174:177], v2 offset:12288\n"
      "v_permlane32_swap_b32 v111, v108\n"
      "v_permlane32_swap_b32 v110, v109\n"
      "v_mov_b32 v214, v111\n"
      "v_mov_b32 v215, v110\n"
      "v_fma_f32 v100, v100, v108, v109\n"
      "v_permlane16_swap_b32 v111, v214\n"
      "v_permlane16_swap_b32 v110, v215\n"
      "v_fma_f32 v140, v140, v111, v110\n"
      "v_fma_f32 v141, v141, v111, v110\n"
      "v_fma_f32 v186, v186, v214, v215\n"
      "v_fma_f32 v187, v187, v214, v215\n"
      "v_fma_f32 v101, v101, v108, v109\n"
      "v_cvt_pk_bf16_f32 v140, v140, v141\n"
      "v_fma_f32 v142, v142, v111, v110\n"
      "v_fma_f32 v143, v143, v111, v110\n"
      "v_cvt_pk_bf16_f32 v186, v186, v187\n"
      "v_fma_f32 v188, v188, v214, v215\n"
      "v_fma_f32 v189, v189, v214, v215\n"
      "v_cvt_pk_bf16_f32 v100, v100, v101\n"
      "v_fma_f32 v102, v102, v108, v109\n"
      "v_fma_f32 v103, v103, v108, v109\n"
      "v_cvt_pk_bf16_f32 v141, v142, v143\n"
      "v_fma_f32 v220, v220, v111, v110\n"
      "v_fma_f32 v221, v221, v111, v110\n"
      "v_fma_f32 v222, v222, v111, v110\n"
      "v_fma_f32 v223, v223, v111, v110\n"
      "v_cvt_pk_bf16_f32 v187, v188, v189\n"
      "v_fma_f32 v182, v182, v214, v215\n"
      "v_fma_f32 v183, v183, v214, v215\n"
      "v_fma_f32 v184, v184, v214, v215\n"
      "v_fma_f32 v185, v185, v214, v215\n"
      "v_cvt_pk_bf16_f32 v101, v102, v103\n"
      "v_fma_f32 v126, v126, v108, v109\n"
      "v_fma_f32 v127, v127, v108, v109\n"
      "v_fma_f32 v128, v128, v108, v109\n"
      "v_fma_f32 v129, v129, v108, v109\n"
      "v_fma_f32 v160, v160, v111, v110\n"
      "v_fma_f32 v161, v161, v111, v110\n"
      "v_cvt_pk_bf16_f32 v142, v220, v221\n"
      "v_cvt_pk_bf16_f32 v143, v222, v223\n"
      "v_cvt_pk_bf16_f32 v188, v182, v183\n"
      "v_cvt_pk_bf16_f32 v189, v184, v185\n"
      "v_cvt_pk_bf16_f32 v102, v126, v127\n"
      "v_cvt_pk_bf16_f32 v103, v128, v129\n"
      "v_fma_f32 v104, v104, v214, v215\n"
      "v_fma_f32 v105, v105, v214, v215\n"
      "v_fma_f32 v96, v96, v108, v109\n"
      "v_fma_f32 v97, v97, v108, v109\n"
      "v_cvt_pk_bf16_f32 v160, v160, v161\n"
      "v_fma_f32 v162, v162, v111, v110\n"
      "v_fma_f32 v163, v163, v111, v110\n"
      "ds_write_b128 v5, v[140:143] offset:18432\n"
      "ds_write_b128 v5, v[186:189] offset:24576\n"
      "ds_write_b128 v5, v[100:103] offset:30720\n"
      "ds_read_b128 v[186:189], v2 offset:15360\n"
      "v_cvt_pk_bf16_f32 v104, v104, v105\n"
      "v_fma_f32 v106, v106, v214, v215\n"
      "v_fma_f32 v107, v107, v214, v215\n"
      "v_cvt_pk_bf16_f32 v96, v96, v97\n"
      "v_fma_f32 v98, v98, v108, v109\n"
      "v_fma_f32 v99, v99, v108, v109\n"
      "v_cvt_pk_bf16_f32 v161, v162, v163\n"
      "v_cvt_pk_bf16_f32 v105, v106, v107\n"
      "v_cvt_pk_bf16_f32 v97, v98, v99\n"
      "ds_write_b64 v6, v[160:161] offset:18432\n"
      "ds_write_b64 v6, v[104:105] offset:24576\n"
      "ds_write_b64 v6, v[96:97] offset:30720\n"
      "s_waitcnt lgkmcnt(4)\n"
      "v_mfma_f32_16x16x32_bf16 v[140:143], a[216:219], v[116:119], 0\n"
      "s_waitcnt lgkmcnt(3)\n"
      "v_mfma_f32_16x16x32_bf16 v[140:143], a[220:223], v[132:135], v[140:143]\n"
      "v_mfma_f32_16x16x32_bf16 v[140:143], a[224:227], v[170:173], v[140:143]\n"
      "v_mfma_f32_16x16x32_bf16 v[220:223], a[240:243], v[116:119], 0\n"
      "v_mfma_f32_16x16x32_bf16 v[220:223], a[244:247], v[132:135], v[220:223]\n"
      "v_mfma_f32_16x16x32_bf16 v[220:223], a[248:251], v[170:173], v[220:223]\n"
      "ds_read_b128 v[100:103], v2 offset:256\n"
      "v_mfma_f32_16x16x32_bf16 v[160:163], v[232:235], v[116:119], 0\n"
      "ds_read_b128 v[126:129], v2 offset:3328\n"
      "v_mfma_f32_16x16x32_bf16 v[160:163], v[236:239], v[132:135], v[160:163]\n"
      "ds_read_b128 v[96:99], v2 offset:6400\n"
      "v_mfma_f32_16x16x32_bf16 v[160:163], v[240:243], v[170:173], v[160:163]\n"
      "v_mfma_f32_16x16x32_bf16 v[140:143], a[228:231], v[194:197], v[140:143]\n"
      "v_mfma_f32_16x16x32_bf16 v[140:143], a[232:235], v[174:177], v[140:143]\n"
      "v_mfma_f32_16x16x32_bf16 v[140:143], a[236:239], v[186:189], v[140:143]\n"
      "s_waitcnt lgkmcnt(2)\n"
      "v_mfma_f32_16x16x32_bf16 v[220:223], a[252:255], v[194:197], v[220:223]\n"
      "s_waitcnt lgkmcnt(1)\n"
      "v_mfma_f32_16x16x32_bf16 v[220:223], v[224:227], v[174:177], v[220:223]\n"
      "s_waitcnt lgkmcnt(0)\n"
      "v_mfma_f32_16x16x32_bf16 v[220:223], v[228:231], v[186:189], v[220:223]\n"
      "ds_read_b128 v[108:111], v2 offset:9472\n"
      "v_mfma_f32_16x16x32_bf16 v[160:163], v[244:247], v[194:197], v[160:163]\n"
      "ds_read_b128 v[194:197], v2 offset:12544\n"
      "v_mul_f32 v133, v140, v140\n"
      "v_mfma_f32_16x16x32_bf16 v[160:163], v[248:251], v[174:177], v[160:163]\n"
      "ds_read_b128 v[174:177], v2 offset:15616\n"
      "v_add_f32 v132, v140, v141\n"
      "v_mfma_f32_16x16x32_bf16 v[160:163], v[252:255], v[186:189], v[160:163]\n"
      "v_fmac_f32 v133, v141, v141\n"
      "v_add_f32 v132, v132, v142\n"
      "v_mfma_f32_16x16x32_bf16 v[186:189], a[216:219], v[100:103], 0\n"
      "v_fmac_f32 v133, v142, v142\n"
      "v_add_f32 v132, v132, v143\n"
      "v_mfma_f32_16x16x32_bf16 v[186:189], a[220:223], v[126:129], v[186:189]\n"
      "v_fmac_f32 v133, v143, v143\n"
      "v_add_f32 v132, v132, v220\n"
      "v_mfma_f32_16x16x32_bf16 v[186:189], a[224:227], v[96:99], v[186:189]\n"
      "s_waitcnt lgkmcnt(2)\n"
      "v_fmac_f32 v133, v220, v220\n"
      "v_add_f32 v132, v132, v221\n"
      "v_mfma_f32_16x16x32_bf16 v[182:185], a[240:243], v[100:103], 0\n"
      "s_waitcnt lgkmcnt(1)\n"
      "v_fmac_f32 v133, v221, v221\n"
      "v_add_f32_e64 v132, v132, v222\n"
      "v_mfma_f32_16x16x32_bf16 v[182:185], a[244:247], v[126:129], v[182:185]\n"
      "s_waitcnt lgkmcnt(0)\n"
      "v_fmac_f32 v133, v222, v222\n"
      "v_add_f32_e64 v132, v132, v223\n"
      "v_mfma_f32_16x16x32_bf16 v[182:185], a[248:251], v[96:99], v[182:185]\n"
      "ds_read_b128 v[214:217], v2 offset:512\n"
      "v_fmac_f32 v133, v223, v223\n"
      "v_mfma_f32_16x16x32_bf16 v[104:107], v[232:235], v[100:103], 0\n"
      "ds_read_b128 v[198:201], v2 offset:3584\n"
      "v_add_f32 v132, v132, v160\n"
      "v_mfma_f32_16x16x32_bf16 v[104:107], v[236:239], v[126:129], v[104:107]\n"
      "ds_read_b128 v[210:213], v2 offset:6656\n"
      "v_fmac_f32 v133, v160, v160\n"
      "v_mfma_f32_16x16x32_bf16 v[104:107], v[240:243], v[96:99], v[104:107]\n"
      "v_add_f32 v132, v132, v161\n"
      "v_fmac_f32 v133, v161, v161\n"
      "v_mfma_f32_16x16x32_bf16 v[186:189], a[228:231], v[108:111], v[186:189]\n"
      "v_add_f32 v132, v132, v162\n"
      "v_fmac_f32 v133, v162, v162\n"
      "v_mfma_f32_16x16x32_bf16 v[186:189], a[232:235], v[194:197], v[186:189]\n"
      "v_add_f32 v132, v132, v163\n"
      "v_fmac_f32 v133, v163, v163\n"
      "v_mfma_f32_16x16x32_bf16 v[186:189], a[236:239], v[174:177], v[186:189]\n"
      "s_waitcnt lgkmcnt(2)\n"
      "v_permlane16_swap_b32 v132, v133\n"
      "v_mfma_f32_16x16x32_bf16 v[182:185], a[252:255], v[108:111], v[182:185]\n"
      "s_waitcnt lgkmcnt(1)\n"
      "v_add_f32 v132, v132, v133\n"
      "v_mfma_f32_16x16x32_bf16 v[182:185], v[224:227], v[194:197], v[182:185]\n"
      "s_waitcnt lgkmcnt(0)\n"
      "v_mfma_f32_16x16x32_bf16 v[182:185], v[228:231], v[174:177], v[182:185]\n"
      "ds_read_b128 v[136:139], v7\n"
      "v_mul_f32 v82, v186, v186\n"
      "v_mfma_f32_16x16x32_bf16 v[104:107], v[244:247], v[108:111], v[104:107]\n"
      "ds_read_b128 v[108:111], v2 offset:9728\n"
      "v_add_f32 v81, v186, v187\n"
      "v_mfma_f32_16x16x32_bf16 v[104:107], v[248:251], v[194:197], v[104:107]\n"
      "ds_read_b128 v[194:197], v7 offset:4096\n"
      "v_fmac_f32 v82, v187, v187\n"
      "v_mfma_f32_16x16x32_bf16 v[104:107], v[252:255], v[174:177], v[104:107]\n"
      "ds_read_b128 v[174:177], v2 offset:12800\n"
      "v_add_f32 v81, v81, v188\n"
      "v_mfma_f32_16x16x32_bf16 v[100:103], a[216:219], v[214:217], 0\n"
      "ds_read_b128 v[190:193], v2 offset:15872\n"
      "v_fmac_f32_e64 v82, v188, v188\n"
      "v_mfma_f32_16x16x32_bf16 v[100:103], a[220:223], v[198:201], v[100:103]\n"
      "ds_read_b128 v[116:119], v7 offset:8192\n"
      "v_add_f32 v81, v81, v189\n"
      "v_mfma_f32_16x16x32_bf16 v[100:103], a[224:227], v[210:213], v[100:103]\n"
      "ds_read_b128 v[154:157], v7 offset:12288\n"
      "v_fmac_f32 v82, v189, v189\n"
      "v_mfma_f32_16x16x32_bf16 v[126:129], a[240:243], v[214:217], 0\n"
      "ds_read_b128 v[148:151], v7 offset:16384\n"
      "v_add_f32 v81, v81, v182\n"
      "v_mfma_f32_16x16x32_bf16 v[126:129], a[244:247], v[198:201], v[126:129]\n"
      "v_fmac_f32 v82, v182, v182\n"
      "v_add_f32 v81, v81, v183\n"
      "v_mfma_f32_16x16x32_bf16 v[126:129], a[248:251], v[210:213], v[126:129]\n"
      "s_waitcnt lgkmcnt(4)\n"
      "v_fmac_f32 v82, v183, v183\n"
      "v_add_f32 v81, v81, v184\n"
      "v_mfma_f32_16x16x32_bf16 v[96:99], v[232:235], v[214:217], 0\n"
      "buffer_store_dwordx4 v[136:139], v20, s[20:23], s46 offen sc1\n"
      "v_mfma_f32_16x16x32_bf16 v[96:99], v[236:239], v[198:201], v[96:99]\n"
      "buffer_store_dwordx4 v[194:197], v21, s[20:23], s46 offen sc1\n"
      "v_mfma_f32_16x16x32_bf16 v[96:99], v[240:243], v[210:213], v[96:99]\n"
      "s_waitcnt lgkmcnt(0)\n"
      "v_fmac_f32 v82, v184, v184\n"
      "v_add_f32_e64 v81, v81, v185\n"
      "v_mfma_f32_16x16x32_bf16 v[100:103], a[228:231], v[108:111], v[100:103]\n"
      "buffer_store_dwordx4 v[116:119], v22, s[20:23], s46 offen sc1\n"
      "v_mfma_f32_16x16x32_bf16 v[100:103], a[232:235], v[174:177], v[100:103]\n"
      "buffer_store_dwordx4 v[154:157], v23, s[20:23], s46 offen sc1\n"
      "v_mfma_f32_16x16x32_bf16 v[100:103], a[236:239], v[190:193], v[100:103]\n"
      "buffer_store_dwordx4 v[148:151], v25, s[20:23], s46 offen sc1\n"
      "v_mfma_f32_16x16x32_bf16 v[126:129], a[252:255], v[108:111], v[126:129]\n"
      "v_fmac_f32 v82, v185, v185\n"
      "v_add_f32 v81, v81, v104\n"
      "v_mfma_f32_16x16x32_bf16 v[126:129], v[224:227], v[174:177], v[126:129]\n"
      "v_fmac_f32 v82, v104, v104\n"
      "v_add_f32 v81, v81, v105\n"
      "v_mfma_f32_16x16x32_bf16 v[126:129], v[228:231], v[190:193], v[126:129]\n"
      "v_mul_f32 v217, v100, v100\n"
      "v_add_f32 v216, v100, v101\n"
      "v_mfma_f32_16x16x32_bf16 v[96:99], v[244:247], v[108:111], v[96:99]\n"
      "v_fmac_f32 v217, v101, v101\n"
      "v_add_f32 v216, v216, v102\n"
      "v_mfma_f32_16x16x32_bf16 v[96:99], v[248:251], v[174:177], v[96:99]\n"
      "v_fmac_f32 v217, v102, v102\n"
      "v_add_f32 v216, v216, v103\n"
      "v_mfma_f32_16x16x32_bf16 v[96:99], v[252:255], v[190:193], v[96:99]\n"
      "v_fmac_f32 v217, v103, v103\n"
      "v_add_f32 v216, v216, v126\n"
      "v_fmac_f32 v217, v126, v126\n"
      "v_add_f32 v216, v216, v127\n"
      "v_fmac_f32 v217, v127, v127\n"
      "v_add_f32 v216, v216, v128\n"
      "v_fmac_f32 v217, v128, v128\n"
      "v_add_f32 v216, v216, v129\n"
      "v_fmac_f32 v217, v129, v129\n"
      "v_add_f32 v216, v216, v96\n"
      "v_fmac_f32 v217, v96, v96\n"
      "v_add_f32 v216, v216, v97\n"
      "v_fmac_f32 v217, v97, v97\n"
      "v_fmac_f32 v82, v105, v105\n"
      "v_add_f32 v216, v216, v98\n"
      "v_fmac_f32 v217, v98, v98\n"
      "v_add_f32 v81, v81, v106\n"
      "v_fmac_f32 v82, v106, v106\n"
      "v_add_f32 v216, v216, v99\n"
      "v_fmac_f32 v217, v99, v99\n"
      "v_add_f32 v81, v81, v107\n"
      "v_fmac_f32 v82, v107, v107\n"
      "v_permlane16_swap_b32 v216, v217\n"
      "v_add_f32 v216, v216, v217\n"
      "v_permlane16_swap_b32 v81, v82\n"
      "v_add_f32 v81, v81, v82\n"
      "v_mov_b32 v217, v216\n"
      "v_readlane_b32 s46, v37, s42\n"
      "v_permlane32_swap_b32 v132, v81\n"
      "v_permlane32_swap_b32 v216, v217\n"
      "v_add_f32 v132, v132, v81\n"
      "v_add_f32_e64 v216, v216, v217\n"
      "ds_write_b32 v8, v132 offset:2048\n"
      "ds_write_b32 v8, v216 offset:2560\n"
      "s_or_b32 s95, s46, s48\n"
      "s_cmp_ge_u32 s95, 0x40000000\n"
      "s_cbranch_scc1 L_rare_27_%=\n"
      "L_back_28_%=:\n"
      "s_add_u32 s42, s42, 1\n"
      "s_waitcnt lgkmcnt(0)\n"
      "s_barrier\n"
      "ds_read_b128 v[174:177], v9 offset:2048\n"
      "ds_read_b128 v[108:111], v9 offset:3072\n"
      "v_subrev_u32 v82, 115456, v9\n"
      "v_and_b32 v216, 0x300, v82\n"
      "v_add_u32_e64 v82, v82, v216\n"
      "v_add_u32 v82, 149504, v82\n"
      "ds_read_b128 v[214:217], v82\n"
      "ds_read_b128 v[194:197], v82 offset:2048\n"
      "v_subrev_u32 v192, 115456, v9\n"
      "v_subrev_u32 v81, 73728, v5\n"
      "v_subrev_u32 v132, 73728, v6\n"
      "s_mul_i32 s92, s40, 3072\n"
      "ds_read_b128 v[210:213], v82 offset:4096\n"
      "ds_read_b128 v[198:201], v82 offset:6144\n"
      "v_add_u32 v119, s92, v192\n"
      "v_lshrrev_b32 v192, 4, v119\n"
      "v_add_u32 v117, 0, v192\n"
      "s_waitcnt lgkmcnt(5)\n"
      "v_add_f32 v174, v174, v176\n"
      "v_add_f32 v175, v175, v177\n"
      "v_add_u32 v118, 64, v192\n"
      "s_waitcnt lgkmcnt(4)\n"
      "v_add_f32 v108, v108, v110\n"
      "v_add_f32 v109, v109, v111\n"
      "v_add_f32 v174, v174, v108\n"
      "v_add_f32 v175, v175, v109\n"
      "v_fma_f32 v108, -v174, v174, s52\n"
      "v_fmac_f32 v108, 0x43400000, v175\n"
      "v_max_f32_e64 v108, s52, v108\n"
      "v_rsq_f32 v108, v108\n"
      "v_add_u32 v116, 128, v192\n"
      "v_mul_f32 v151, -v174, v108\n"
      "v_mul_f32 v154, 0x43400000, v108\n"
      "s_waitcnt lgkmcnt(1)\n"
      "v_mfma_f32_16x16x32_bf16 v[108:111], a[216:219], v[214:217], 0\n"
      "v_mfma_f32_16x16x32_bf16 v[108:111], a[220:223], v[194:197], v[108:111]\n"
      "ds_read_b128 v[174:177], v82 offset:8192\n"
      "v_mov_b32_e64 v148, v154\n"
      "v_mfma_f32_16x16x32_bf16 v[136:139], a[240:243], v[214:217], 0\n"
      "s_waitcnt lgkmcnt(1)\n"
      "v_permlane32_swap_b32_e64 v154, v148\n"
      "v_mfma_f32_16x16x32_bf16 v[136:139], a[244:247], v[194:197], v[136:139]\n"
      "v_mfma_f32_16x16x32_bf16 v[190:193], v[232:235], v[214:217], 0\n"
      "v_mfma_f32_16x16x32_bf16 v[190:193], v[236:239], v[194:197], v[190:193]\n"
      "v_mfma_f32_16x16x32_bf16 v[108:111], a[224:227], v[210:213], v[108:111]\n"
      "v_mfma_f32_16x16x32_bf16 v[108:111], a[228:231], v[198:201], v[108:111]\n"
      "s_waitcnt lgkmcnt(0)\n"
      "v_mfma_f32_16x16x32_bf16 v[136:139], a[248:251], v[210:213], v[136:139]\n"
      "v_mfma_f32_16x16x32_bf16 v[136:139], a[252:255], v[198:201], v[136:139]\n"
      "v_mfma_f32_16x16x32_bf16 v[190:193], v[240:243], v[210:213], v[190:193]\n"
      "v_mfma_f32_16x16x32_bf16 v[190:193], v[244:247], v[198:201], v[190:193]\n"
      "v_mfma_f32_16x16x32_bf16 v[108:111], a[232:235], v[174:177], v[108:111]\n"
      "ds_read_b128 v[196:199], v82 offset:10240\n"
      "ds_read_b128 v[214:217], v82 offset:256\n"
      "ds_read_b128 v[210:213], v82 offset:2304\n"
      "v_mov_b32_e64 v200, v151\n"
      "v_mov_b32_e64 v194, v154\n"
      "v_and_b32 v201, 0x3fffffff, v37\n"
      "v_permlane32_swap_b32_e64 v151, v200\n"
      "v_permlane16_swap_b32 v154, v194\n"
      "v_fma_f32 v100, v100, v148, v200\n"
      "v_fma_f32 v101, v101, v148, v200\n"
      "v_cvt_pk_bf16_f32 v100, v100, v101\n"
      "v_fma_f32 v102, v102, v148, v200\n"
      "v_fma_f32 v103, v103, v148, v200\n"
      "v_fma_f32 v96, v96, v148, v200\n"
      "v_fma_f32 v97, v97, v148, v200\n"
      "v_cvt_pk_bf16_f32 v101, v102, v103\n"
      "v_fma_f32 v126, v126, v148, v200\n"
      "v_fma_f32 v127, v127, v148, v200\n"
      "v_fma_f32 v128, v128, v148, v200\n"
      "s_waitcnt lgkmcnt(2)\n"
      "v_mfma_f32_16x16x32_bf16 v[108:111], a[236:239], v[196:199], v[108:111]\n"
      "v_fma_f32 v129, v129, v148, v200\n"
      "v_cvt_pk_bf16_f32 v96, v96, v97\n"
      "v_mfma_f32_16x16x32_bf16 v[136:139], v[224:227], v[174:177], v[136:139]\n"
      "s_waitcnt lgkmcnt(1)\n"
      "v_fma_f32 v98, v98, v148, v200\n"
      "v_fma_f32 v99, v99, v148, v200\n"
      "v_mfma_f32_16x16x32_bf16 v[136:139], v[228:231], v[196:199], v[136:139]\n"
      "s_waitcnt lgkmcnt(0)\n"
      "v_cvt_pk_bf16_f32 v102, v126, v127\n"
      "v_cvt_pk_bf16_f32 v103, v128, v129\n"
      "v_mfma_f32_16x16x32_bf16 v[190:193], v[248:251], v[174:177], v[190:193]\n"
      "ds_write_b128 v5, v[100:103] offset:12288\n"
      "v_mfma_f32_16x16x32_bf16 v[190:193], v[252:255], v[196:199], v[190:193]\n"
      "v_cvt_pk_bf16_f32 v97, v98, v99\n"
      "ds_write_b64 v6, v[96:97] offset:12288\n"
      "v_mov_b32 v100, v151\n"
      "v_mul_f32 v197, v108, v108\n"
      "v_add_f32 v196, v108, v109\n"
      "v_permlane16_swap_b32 v151, v100\n"
      "v_fma_f32 v140, v140, v154, v151\n"
      "v_fma_f32 v141, v141, v154, v151\n"
      "v_cvt_pk_bf16_f32 v140, v140, v141\n"
      "v_fma_f32 v142, v142, v154, v151\n"
      "v_fma_f32 v143, v143, v154, v151\n"
      "v_fma_f32 v160, v160, v154, v151\n"
      "v_fma_f32 v161, v161, v154, v151\n"
      "v_cvt_pk_bf16_f32 v141, v142, v143\n"
      "v_fma_f32 v220, v220, v154, v151\n"
      "v_fma_f32 v221, v221, v154, v151\n"
      "v_fma_f32 v222, v222, v154, v151\n"
      "v_fma_f32 v223, v223, v154, v151\n"
      "v_cvt_pk_bf16_f32 v160, v160, v161\n"
      "v_fma_f32 v162, v162, v154, v151\n"
      "v_fma_f32 v163, v163, v154, v151\n"
      "v_cvt_pk_bf16_f32 v142, v220, v221\n"
      "v_mfma_f32_16x16x32_bf16 v[96:99], a[216:219], v[214:217], 0\n"
      "v_cvt_pk_bf16_f32 v143, v222, v223\n"
      "v_cvt_pk_bf16_f32 v161, v162, v163\n"
      "v_mfma_f32_16x16x32_bf16 v[96:99], a[220:223], v[210:213], v[96:99]\n"
      "ds_write_b128 v5, v[140:143]\n"
      "v_mfma_f32_16x16x32_bf16 v[140:143], a[240:243], v[214:217], 0\n"
      "ds_write_b64 v6, v[160:161]\n"
      "v_mfma_f32_16x16x32_bf16 v[140:143], a[244:247], v[210:213], v[140:143]\n"
      "ds_read_b128 v[220:223], v82 offset:4352\n"
      "v_fma_f32 v186, v186, v194, v100\n"
      "v_mfma_f32_16x16x32_bf16 v[160:163], v[232:235], v[214:217], 0\n"
      "v_fma_f32 v187, v187, v194, v100\n"
      "v_cvt_pk_bf16_f32 v186, v186, v187\n"
      "v_mfma_f32_16x16x32_bf16 v[160:163], v[236:239], v[210:213], v[160:163]\n"
      "ds_read_b128 v[210:213], v82 offset:6400\n"
      "v_fma_f32 v188, v188, v194, v100\n"
      "v_fma_f32 v189, v189, v194, v100\n"
      "v_fma_f32 v104, v104, v194, v100\n"
      "v_fma_f32 v105, v105, v194, v100\n"
      "v_cvt_pk_bf16_f32 v187, v188, v189\n"
      "v_fma_f32 v182, v182, v194, v100\n"
      "v_fma_f32 v183, v183, v194, v100\n"
      "v_fma_f32 v184, v184, v194, v100\n"
      "v_fma_f32 v185, v185, v194, v100\n"
      "v_cvt_pk_bf16_f32 v104, v104, v105\n"
      "v_fma_f32 v106, v106, v194, v100\n"
      "v_fma_f32 v107, v107, v194, v100\n"
      "ds_read_b128 v[214:217], v7 offset:18432\n"
      "v_cvt_pk_bf16_f32 v188, v182, v183\n"
      "v_cvt_pk_bf16_f32 v189, v184, v185\n"
      "s_waitcnt lgkmcnt(2)\n"
      "v_mfma_f32_16x16x32_bf16 v[96:99], a[224:227], v[220:223], v[96:99]\n"
      "ds_write_b128 v5, v[186:189] offset:6144\n"
      "s_waitcnt lgkmcnt(2)\n"
      "v_mfma_f32_16x16x32_bf16 v[96:99], a[228:231], v[210:213], v[96:99]\n"
      "ds_read_b128 v[184:187], v7 offset:22528\n"
      "v_cvt_pk_bf16_f32 v105, v106, v107\n"
      "v_mfma_f32_16x16x32_bf16 v[140:143], a[248:251], v[220:223], v[140:143]\n"
      "ds_write_b64 v6, v[104:105] offset:6144\n"
      "v_mfma_f32_16x16x32_bf16 v[140:143], a[252:255], v[210:213], v[140:143]\n"
      "s_waitcnt lgkmcnt(3)\n"
      "v_fmac_f32 v197, v109, v109\n"
      "v_add_f32 v196, v196, v110\n"
      "v_mfma_f32_16x16x32_bf16 v[160:163], v[240:243], v[220:223], v[160:163]\n"
      "ds_read_b128 v[220:223], v7 offset:26624\n"
      "v_fmac_f32 v197, v110, v110\n"
      "v_mfma_f32_16x16x32_bf16 v[160:163], v[244:247], v[210:213], v[160:163]\n"
      "ds_read_b128 v[210:213], v82 offset:8448\n"
      "ds_read_b128 v[104:107], v7 offset:30720\n"
      "ds_read_b128 v[100:103], v82 offset:10496\n"
      "buffer_store_dwordx4 v[214:217], v20, s[20:23], s46 offen sc1\n"
      "s_nop 1\n"
      "ds_read_b128 v[214:217], v7 offset:34816\n"
      "v_add_f32 v196, v196, v111\n"
      "v_fmac_f32 v197, v111, v111\n"
      "v_add_f32 v196, v196, v136\n"
      "v_fmac_f32 v197, v136, v136\n"
      "v_add_f32 v196, v196, v137\n"
      "v_fmac_f32 v197, v137, v137\n"
      "v_add_f32 v196, v196, v138\n"
      "v_fmac_f32 v197, v138, v138\n"
      "v_add_f32 v196, v196, v139\n"
      "s_waitcnt lgkmcnt(3)\n"
      "v_mfma_f32_16x16x32_bf16 v[96:99], a[232:235], v[210:213], v[96:99]\n"
      "buffer_store_dwordx4 v[184:187], v21, s[20:23], s46 offen sc1\n"
      "s_waitcnt lgkmcnt(1)\n"
      "v_mfma_f32_16x16x32_bf16 v[96:99], a[236:239], v[100:103], v[96:99]\n"
      "buffer_store_dwordx4 v[220:223], v22, s[20:23], s46 offen sc1\n"
      "v_mfma_f32_16x16x32_bf16 v[140:143], v[224:227], v[210:213], v[140:143]\n"
      "s_waitcnt lgkmcnt(0)\n"
      "v_fmac_f32 v197, v139, v139\n"
      "v_add_f32_e64 v196, v196, v190\n"
      "v_mfma_f32_16x16x32_bf16 v[140:143], v[228:231], v[100:103], v[140:143]\n"
      "buffer_store_dwordx4 v[104:107], v23, s[20:23], s46 offen sc1\n"
      "v_mfma_f32_16x16x32_bf16 v[160:163], v[248:251], v[210:213], v[160:163]\n"
      "buffer_store_dwordx4 v[214:217], v25, s[20:23], s46 offen sc1\n"
      "v_mfma_f32_16x16x32_bf16 v[160:163], v[252:255], v[100:103], v[160:163]\n"
      "v_mul_f32 v213, v96, v96\n"
      "v_add_f32 v212, v96, v97\n"
      "v_fmac_f32 v213, v97, v97\n"
      "v_add_f32 v212, v212, v98\n"
      "v_fmac_f32 v213, v98, v98\n"
      "v_add_f32 v212, v212, v99\n"
      "v_fmac_f32 v213, v99, v99\n"
      "v_add_f32 v212, v212, v140\n"
      "v_fmac_f32 v213, v140, v140\n"
      "v_add_f32 v212, v212, v141\n"
      "v_fmac_f32 v213, v141, v141\n"
      "v_add_f32 v212, v212, v142\n"
      "v_fmac_f32 v213, v142, v142\n"
      "v_add_f32 v212, v212, v143\n"
      "v_fmac_f32 v213, v143, v143\n"
      "v_fmac_f32 v197, v190, v190\n"
      "v_add_f32 v212, v212, v160\n"
      "v_fmac_f32 v213, v160, v160\n"
      "v_add_f32 v196, v196, v191\n"
      "v_fmac_f32 v197, v191, v191\n"
      "v_add_f32 v212, v212, v161\n"
      "v_fmac_f32 v213, v161, v161\n"
      "v_add_f32 v196, v196, v192\n"
      "v_fmac_f32 v197, v192, v192\n"
      "v_add_f32 v212, v212, v162\n"
      "v_fmac_f32 v213, v162, v162\n"
      "v_add_f32 v196, v196, v193\n"
      "v_fmac_f32 v197, v193, v193\n"
      "v_add_f32 v212, v212, v163\n"
      "v_fmac_f32 v213, v163, v163\n"
      "v_permlane16_swap_b32 v196, v197\n"
      "v_add_f32 v196, v196, v197\n"
      "v_permlane16_swap_b32 v212, v213\n"
      "v_add_f32 v212, v212, v213\n"
      "v_ashrrev_i32 v213, 31, v37\n"
      "v_mul_u32_u24 v197, 2731, v117\n"
      "v_permlane32_swap_b32 v196, v212\n"
      "v_add_f32_e64 v196, v196, v212\n"
      "ds_write_b32 v8, v196 offset:4608\n"
      "v_mul_u32_u24 v212, 2731, v118\n"
      "v_mul_u32_u24 v196, 2731, v116\n"
      "v_add_u32 v201, 1179648, v201\n"
      "v_and_b32 v213, 100270464, v213\n"
      "v_lshrrev_b32 v197, 16, v197\n"
      "v_lshrrev_b32 v212, 16, v212\n"
      "v_lshrrev_b32 v196, 16, v196\n"
      "v_sub_u32 v201, v201, v213\n"
      "v_mul_u32_u24 v215, 24, v197\n"
      "v_mul_u32_u24 v214, 24, v212\n"
      "v_mul_u32_u24 v105, 24, v196\n"
      "v_bfe_i32 v213, v37, 30, 1\n"
      "v_sub_u32 v215, v117, v215\n"
      "v_sub_u32 v214, v118, v214\n"
      "v_sub_u32 v105, v116, v105\n"
      "v_and_b32 v213, 196224, v213\n"
      "v_and_b32 v117, 7, v197\n"
      "v_and_b32 v118, 7, v212\n"
      "v_and_b32 v116, 7, v196\n"
      "v_sub_u32 v201, v201, v213\n"
      "v_lshl_add_u32 v197, v197, 2, 12\n"
      "v_lshl_add_u32 v212, v212, 2, 12\n"
      "v_lshl_add_u32 v196, v196, 2, 12\n"
      "ds_bpermute_b32 v216, v197, v201\n"
      "ds_bpermute_b32 v217, v212, v201\n"
      "ds_bpermute_b32 v211, v196, v201\n"
      "v_readlane_b32 s46, v37, s42\n"
      "v_xor_b32 v215, v215, v117\n"
      "s_or_b32 s95, s46, s48\n"
      "s_cmp_ge_u32 s95, 0x40000000\n"
      "s_cbranch_scc1 L_rare_31_%=\n"
      "L_back_32_%=:\n"
      "s_add_u32 s42, s42, 1\n"
      "v_xor_b32 v214, v214, v118\n"
      "v_xor_b32 v105, v105, v116\n"
      "s_waitcnt lgkmcnt(2)\n"
      "v_lshl_add_u32 v216, v215, 4, v216\n"
      "s_waitcnt lgkmcnt(1)\n"
      "v_lshl_add_u32 v217, v214, 4, v217\n"
      "s_waitcnt lgkmcnt(0)\n"
      "v_lshl_add_u32 v211, v105, 4, v211\n"
      "s_waitcnt lgkmcnt(0)\n"
      "s_barrier\n"
      "ds_read_b128 v[212:215], v9 offset:4608\n"
      "ds_read_b128 v[104:107], v9 offset:5632\n"
      "ds_read_b128 v[100:103], v7\n"
      "ds_read_b128 v[220:223], v7 offset:4096\n"
      "ds_read_b128 v[184:187], v7 offset:8192\n"
      "ds_read_b128 v[196:199], v7 offset:12288\n"
      "ds_read_b128 v[126:129], v7 offset:16384\n"
      "s_waitcnt lgkmcnt(6)\n"
      "v_add_f32 v212, v212, v214\n"
      "v_add_f32 v213, v213, v215\n"
      "s_waitcnt lgkmcnt(5)\n"
      "v_add_f32 v104, v104, v106\n"
      "v_add_f32 v105, v105, v107\n"
      "v_add_f32 v212, v212, v104\n"
      "v_add_f32 v213, v213, v105\n"
      "v_fma_f32 v104, -v212, v212, s52\n"
      "v_fmac_f32 v104, 0x43400000, v213\n"
      "v_max_f32 v104, s52, v104\n"
      "v_rsq_f32 v104, v104\n"
      "s_waitcnt lgkmcnt(3)\n"
      "buffer_store_dwordx4 v[100:103], v20, s[20:23], s46 offen sc1\n"
      "s_nop 1\n"
      "v_mul_f32 v101, 0x43400000, v104\n"
      "v_mul_f32 v100, -v212, v104\n"
      "v_mov_b32 v212, v101\n"
      "v_mov_b32 v103, v100\n"
      "buffer_store_dwordx4 v[220:223], v21, s[20:23], s46 offen sc1\n"
      "v_permlane32_swap_b32 v101, v212\n"
      "v_permlane32_swap_b32 v100, v103\n"
      "v_mov_b32 v103, v101\n"
      "v_mov_b32 v222, v100\n"
      "s_waitcnt lgkmcnt(0)\n"
      "buffer_store_dwordx4 v[184:187], v22, s[20:23], s46 offen sc1\n"
      "v_permlane16_swap_b32 v101, v103\n"
      "v_permlane16_swap_b32_e64 v100, v222\n"
      "v_fma_f32 v108, v108, v101, v100\n"
      "v_fma_f32 v109, v109, v101, v100\n"
      "v_fma_f32 v96, v96, v103, v222\n"
      "v_fma_f32 v97, v97, v103, v222\n"
      "v_cvt_pk_bf16_f32 v108, v108, v109\n"
      "v_fma_f32 v110, v110, v101, v100\n"
      "v_fma_f32 v111, v111, v101, v100\n"
      "v_fma_f32 v190, v190, v101, v100\n"
      "v_fma_f32 v191, v191, v101, v100\n"
      "v_cvt_pk_bf16_f32 v96, v96, v97\n"
      "v_fma_f32 v98, v98, v103, v222\n"
      "v_fma_f32 v99, v99, v103, v222\n"
      "v_fma_f32 v160, v160, v103, v222\n"
      "v_fma_f32 v161, v161, v103, v222\n"
      "v_cvt_pk_bf16_f32 v109, v110, v111\n"
      "v_fma_f32 v136, v136, v101, v100\n"
      "v_fma_f32 v137, v137, v101, v100\n"
      "v_fma_f32 v138, v138, v101, v100\n"
      "v_fma_f32 v139, v139, v101, v100\n"
      "v_cvt_pk_bf16_f32 v190, v190, v191\n"
      "v_fma_f32 v192, v192, v101, v100\n"
      "v_fma_f32 v193, v193, v101, v100\n"
      "v_cvt_pk_bf16_f32 v97, v98, v99\n"
      "v_fma_f32 v140, v140, v103, v222\n"
      "v_fma_f32 v141, v141, v103, v222\n"
      "v_fma_f32 v142, v142, v103, v222\n"
      "v_fma_f32 v143, v143, v103, v222\n"
      "v_cvt_pk_bf16_f32 v160, v160, v161\n"
      "v_fma_f32 v162, v162, v103, v222\n"
      "v_fma_f32 v163, v163, v103, v222\n"
      "v_cvt_pk_bf16_f32 v110, v136, v137\n"
      "v_cvt_pk_bf16_f32 v111, v138, v139\n"
      "v_cvt_pk_bf16_f32 v191, v192, v193\n"
      "v_cvt_pk_bf16_f32 v98, v140, v141\n"
      "v_cvt_pk_bf16_f32 v99, v142, v143\n"
      "v_cvt_pk_bf16_f32 v161, v162, v163\n"
      "ds_write_b128 v81, v[108:111]\n"
      "ds_write_b64 v132, v[190:191]\n"
      "ds_write_b128 v81, v[96:99] offset:6144\n"
      "ds_write_b64 v132, v[160:161] offset:6144\n"
      "buffer_store_dwordx4 v[196:199], v23, s[20:23], s46 offen sc1\n"
      "buffer_store_dwordx4 v[126:129], v25, s[20:23], s46 offen sc1\n"
      "s_waitcnt lgkmcnt(0)\n"
      "s_barrier\n"
      "ds_read_b128 v[126:129], v119\n"
      "ds_read_b128 v[196:199], v119 offset:1024\n"
      "ds_read_b128 v[96:99], v119 offset:2048\n"
      "s_waitcnt lgkmcnt(2)\n"
      "buffer_store_dwordx4 v[126:129], v216, s[20:23], 0 offen sc1\n"
      "s_waitcnt lgkmcnt(1)\n"
      "buffer_store_dwordx4 v[196:199], v217, s[20:23], 0 offen sc1\n"
      "s_waitcnt lgkmcnt(0)\n"
      "buffer_store_dwordx4 v[96:99], v211, s[20:23], 0 offen sc1\n"
      "s_endpgm\n"
      "L_tail0_%=:\n"
      "s_nop 4\n"
      "v_mfma_f32_16x16x32_bf16 v[174:177], v[210:213], v[198:201], 0\n"
      "ds_read_b128 v[198:201], v9 offset:2048\n"
      "ds_read_b128 v[116:119], v9 offset:3072\n"
      "v_mfma_f32_16x16x32_bf16 v[174:177], v[136:139], v[76:79], v[174:177]\n"
      "ds_read_b128 v[154:157], v2 offset:18432\n"
      "ds_read_b128 v[148:151], v2 offset:21504\n"
      "v_mfma_f32_16x16x32_bf16 v[190:193], v[210:213], v[214:217], 0\n"
      "ds_read_b128 v[130:133], v2 offset:24576\n"
      "v_mfma_f32_16x16x32_bf16 v[190:193], v[136:139], v[80:83], v[190:193]\n"
      "v_mfma_f32_16x16x32_bf16 v[214:217], v[210:213], v[194:197], 0\n"
      "v_mfma_f32_16x16x32_bf16 v[214:217], v[136:139], v[84:87], v[214:217]\n"
      "ds_read_b128 v[210:213], v2 offset:27648\n"
      "s_waitcnt lgkmcnt(5)\n"
      "v_add_f32 v198, v198, v200\n"
      "v_add_f32 v199, v199, v201\n"
      "v_mov_b32_dpp v108, v108 quad_perm:[0,0,0,0] row_mask:0xf bank_mask:0xf\n"
      "s_waitcnt lgkmcnt(4)\n"
      "v_add_f32 v116, v116, v118\n"
      "v_add_f32 v117, v117, v119\n"
      "v_add_f32 v198, v198, v116\n"
      "v_add_f32_e64 v199, v199, v117\n"
      "v_fma_f32 v116, -v198, v198, s52\n"
      "v_fmac_f32 v116, 0x43400000, v199\n"
      "v_max_f32 v116, s52, v116\n"
      "v_rsq_f32 v116, v116\n"
      "v_rcp_f32_e64 v108, v108\n"
      "v_cndmask_b32 v174, v174, v190, s[58:59]\n"
      "v_cndmask_b32 v174, v174, v214, s[60:61]\n"
      "v_mul_f32 v175, 0x43400000, v116\n"
      "v_mul_f32 v117, -v198, v116\n"
      "v_mul_f32 v174, v108, v174\n"
      "v_mov_b32 v116, v175\n"
      "v_mov_b32_e64 v176, v117\n"
      "v_cvt_pk_bf16_f32 v174, v174, v174\n"
      "ds_write_b16 v12, v174 offset:16\n"
      "ds_read_b128 v[196:199], v2 offset:30720\n"
      "v_permlane32_swap_b32 v175, v116\n"
      "v_permlane32_swap_b32 v117, v176\n"
      "v_mov_b32 v174, v175\n"
      "v_mov_b32 v177, v117\n"
      "v_fma_f32 v100, v100, v116, v176\n"
      "v_permlane16_swap_b32 v175, v174\n"
      "v_permlane16_swap_b32 v117, v177\n"
      "v_fma_f32 v140, v140, v175, v117\n"
      "v_fma_f32 v141, v141, v175, v117\n"
      "v_fma_f32 v186, v186, v174, v177\n"
      "v_fma_f32 v187, v187, v174, v177\n"
      "v_fma_f32 v101, v101, v116, v176\n"
      "v_cvt_pk_bf16_f32 v140, v140, v141\n"
      "v_fma_f32 v142, v142, v175, v117\n"
      "v_fma_f32 v143, v143, v175, v117\n"
      "v_cvt_pk_bf16_f32 v186, v186, v187\n"
      "v_fma_f32 v188, v188, v174, v177\n"
      "v_fma_f32 v189, v189, v174, v177\n"
      "v_cvt_pk_bf16_f32 v100, v100, v101\n"
      "v_fma_f32 v102, v102, v116, v176\n"
      "v_fma_f32 v103, v103, v116, v176\n"
      "v_cvt_pk_bf16_f32 v141, v142, v143\n"
      "v_fma_f32 v220, v220, v175, v117\n"
      "v_fma_f32 v221, v221, v175, v117\n"
      "v_fma_f32 v222, v222, v175, v117\n"
      "v_fma_f32 v223, v223, v175, v117\n"
      "v_cvt_pk_bf16_f32 v187, v188, v189\n"
      "v_fma_f32 v182, v182, v174, v177\n"
      "v_fma_f32 v183, v183, v174, v177\n"
      "v_fma_f32 v184, v184, v174, v177\n"
      "v_fma_f32 v185, v185, v174, v177\n"
      "v_cvt_pk_bf16_f32 v101, v102, v103\n"
      "v_fma_f32 v126, v126, v116, v176\n"
      "v_fma_f32 v127, v127, v116, v176\n"
      "v_fma_f32 v128, v128, v116, v176\n"
      "v_fma_f32 v129, v129, v116, v176\n"
      "v_fma_f32 v160, v160, v175, v117\n"
      "v_fma_f32 v161, v161, v175, v117\n"
      "v_cvt_pk_bf16_f32 v142, v220, v221\n"
      "v_cvt_pk_bf16_f32 v143, v222, v223\n"
      "v_cvt_pk_bf16_f32 v188, v182, v183\n"
      "v_cvt_pk_bf16_f32 v189, v184, v185\n"
      "v_cvt_pk_bf16_f32 v102, v126, v127\n"
      "v_cvt_pk_bf16_f32 v103, v128, v129\n"
      "v_fma_f32 v104, v104, v174, v177\n"
      "v_fma_f32 v105, v105, v174, v177\n"
      "v_fma_f32 v96, v96, v116, v176\n"
      "v_fma_f32 v97, v97, v116, v176\n"
      "v_cvt_pk_bf16_f32 v160, v160, v161\n"
      "v_fma_f32 v162, v162, v175, v117\n"
      "v_fma_f32 v163, v163, v175, v117\n"
      "ds_write_b128 v5, v[140:143]\n"
      "ds_write_b128 v5, v[186:189] offset:6144\n"
      "ds_write_b128 v5, v[100:103] offset:12288\n"
      "ds_read_b128 v[186:189], v2 offset:33792\n"
      "v_cvt_pk_bf16_f32 v104, v104, v105\n"
      "v_fma_f32 v106, v106, v174, v177\n"
      "v_fma_f32 v107, v107, v174, v177\n"
      "v_cvt_pk_bf16_f32 v96, v96, v97\n"
      "v_fma_f32 v98, v98, v116, v176\n"
      "v_fma_f32 v99, v99, v116, v176\n"
      "v_cvt_pk_bf16_f32 v161, v162, v163\n"
      "v_cvt_pk_bf16_f32 v105, v106, v107\n"
      "v_cvt_pk_bf16_f32 v97, v98, v99\n"
      "ds_write_b64 v6, v[160:161]\n"
      "ds_write_b64 v6, v[104:105] offset:6144\n"
      "ds_write_b64 v6, v[96:97] offset:12288\n"
      "s_waitcnt lgkmcnt(4)\n"
      "v_mfma_f32_16x16x32_bf16 v[140:143], a[216:219], v[154:157], 0\n"
      "s_waitcnt lgkmcnt(3)\n"
      "v_add_u32_e64 v12, 32, v12\n"
      "v_mfma_f32_16x16x32_bf16 v[140:143], a[220:223], v[148:151], v[140:143]\n"
      "v_mfma_f32_16x16x32_bf16 v[140:143], a[224:227], v[130:133], v[140:143]\n"
      "v_mfma_f32_16x16x32_bf16 v[220:223], a[240:243], v[154:157], 0\n"
      "v_mfma_f32_16x16x32_bf16 v[220:223], a[244:247], v[148:151], v[220:223]\n"
      "v_mfma_f32_16x16x32_bf16 v[220:223], a[248:251], v[130:133], v[220:223]\n"
      "ds_read_b128 v[100:103], v2 offset:18688\n"
      "v_mfma_f32_16x16x32_bf16 v[160:163], v[232:235], v[154:157], 0\n"
      "ds_read_b128 v[126:129], v2 offset:21760\n"
      "v_mfma_f32_16x16x32_bf16 v[160:163], v[236:239], v[148:151], v[160:163]\n"
      "ds_read_b128 v[96:99], v2 offset:24832\n"
      "v_mfma_f32_16x16x32_bf16 v[160:163], v[240:243], v[130:133], v[160:163]\n"
      "v_mfma_f32_16x16x32_bf16 v[140:143], a[228:231], v[210:213], v[140:143]\n"
      "v_mfma_f32_16x16x32_bf16 v[140:143], a[232:235], v[196:199], v[140:143]\n"
      "v_mfma_f32_16x16x32_bf16 v[140:143], a[236:239], v[186:189], v[140:143]\n"
      "s_waitcnt lgkmcnt(2)\n"
      "v_mfma_f32_16x16x32_bf16 v[220:223], a[252:255], v[210:213], v[220:223]\n"
      "s_waitcnt lgkmcnt(1)\n"
      "v_mfma_f32_16x16x32_bf16 v[220:223], v[224:227], v[196:199], v[220:223]\n"
      "s_waitcnt lgkmcnt(0)\n"
      "v_mfma_f32_16x16x32_bf16 v[220:223], v[228:231], v[186:189], v[220:223]\n"
      "ds_read_b128 v[174:177], v2 offset:27904\n"
      "v_mfma_f32_16x16x32_bf16 v[160:163], v[244:247], v[210:213], v[160:163]\n"
      "ds_read_b128 v[116:119], v2 offset:30976\n"
      "v_mul_f32 v192, v140, v140\n"
      "v_mfma_f32_16x16x32_bf16 v[160:163], v[248:251], v[196:199], v[160:163]\n"
      "ds_read_b128 v[212:215], v2 offset:34048\n"
      "v_add_f32 v191, v140, v141\n"
      "v_mfma_f32_16x16x32_bf16 v[160:163], v[252:255], v[186:189], v[160:163]\n"
      "v_fmac_f32 v192, v141, v141\n"
      "v_add_f32 v191, v191, v142\n"
      "v_mfma_f32_16x16x32_bf16 v[186:189], a[216:219], v[100:103], 0\n"
      "v_fmac_f32 v192, v142, v142\n"
      "v_add_f32 v191, v191, v143\n"
      "v_mfma_f32_16x16x32_bf16 v[186:189], a[220:223], v[126:129], v[186:189]\n"
      "v_fmac_f32 v192, v143, v143\n"
      "v_add_f32 v191, v191, v220\n"
      "v_mfma_f32_16x16x32_bf16 v[186:189], a[224:227], v[96:99], v[186:189]\n"
      "s_waitcnt lgkmcnt(2)\n"
      "v_fmac_f32 v192, v220, v220\n"
      "v_add_f32 v191, v191, v221\n"
      "v_mfma_f32_16x16x32_bf16 v[182:185], a[240:243], v[100:103], 0\n"
      "s_waitcnt lgkmcnt(1)\n"
      "v_fmac_f32 v192, v221, v221\n"
      "v_add_f32_e64 v191, v191, v222\n"
      "v_mfma_f32_16x16x32_bf16 v[182:185], a[244:247], v[126:129], v[182:185]\n"
      "s_waitcnt lgkmcnt(0)\n"
      "v_fmac_f32 v192, v222, v222\n"
      "v_add_f32_e64 v191, v191, v223\n"
      "v_mfma_f32_16x16x32_bf16 v[182:185], a[248:251], v[96:99], v[182:185]\n"
      "ds_read_b128 v[148:151], v2 offset:18944\n"
      "v_fmac_f32 v192, v223, v223\n"
      "v_mfma_f32_16x16x32_bf16 v[104:107], v[232:235], v[100:103], 0\n"
      "ds_read_b128 v[154:157], v2 offset:22016\n"
      "v_add_f32 v191, v191, v160\n"
      "v_mfma_f32_16x16x32_bf16 v[104:107], v[236:239], v[126:129], v[104:107]\n"
      "ds_read_b128 v[130:133], v2 offset:25088\n"
      "v_fmac_f32 v192, v160, v160\n"
      "v_mfma_f32_16x16x32_bf16 v[104:107], v[240:243], v[96:99], v[104:107]\n"
      "v_add_f32 v191, v191, v161\n"
      "v_fmac_f32 v192, v161, v161\n"
      "v_mfma_f32_16x16x32_bf16 v[186:189], a[228:231], v[174:177], v[186:189]\n"
      "v_add_f32 v191, v191, v162\n"
      "v_fmac_f32 v192, v162, v162\n"
      "v_mfma_f32_16x16x32_bf16 v[186:189], a[232:235], v[116:119], v[186:189]\n"
      "v_add_f32 v191, v191, v163\n"
      "v_fmac_f32 v192, v163, v163\n"
      "v_mfma_f32_16x16x32_bf16 v[186:189], a[236:239], v[212:215], v[186:189]\n"
      "s_waitcnt lgkmcnt(2)\n"
      "v_permlane16_swap_b32 v191, v192\n"
      "v_mfma_f32_16x16x32_bf16 v[182:185], a[252:255], v[174:177], v[182:185]\n"
      "s_waitcnt lgkmcnt(1)\n"
      "v_add_f32 v191, v191, v192\n"
      "v_mfma_f32_16x16x32_bf16 v[182:185], v[224:227], v[116:119], v[182:185]\n"
      "s_waitcnt lgkmcnt(0)\n"
      "v_mfma_f32_16x16x32_bf16 v[182:185], v[228:231], v[212:215], v[182:185]\n"
      "ds_read_b128 v[108:111], v7 offset:18432\n"
      "v_mul_f32 v85, v186, v186\n"
      "v_mfma_f32_16x16x32_bf16 v[104:107], v[244:247], v[174:177], v[104:107]\n"
      "ds_read_b128 v[198:201], v2 offset:28160\n"
      "v_add_f32 v84, v186, v187\n"
      "v_mfma_f32_16x16x32_bf16 v[104:107], v[248:251], v[116:119], v[104:107]\n"
      "ds_read_b128 v[116:119], v7 offset:22528\n"
      "v_fmac_f32 v85, v187, v187\n"
      "v_mfma_f32_16x16x32_bf16 v[104:107], v[252:255], v[212:215], v[104:107]\n"
      "ds_read_b128 v[174:177], v2 offset:31232\n"
      "v_add_f32 v84, v84, v188\n"
      "v_mfma_f32_16x16x32_bf16 v[100:103], a[216:219], v[148:151], 0\n"
      "ds_read_b128 v[210:213], v2 offset:34304\n"
      "v_fmac_f32_e64 v85, v188, v188\n"
      "v_mfma_f32_16x16x32_bf16 v[100:103], a[220:223], v[154:157], v[100:103]\n"
      "ds_read_b128 v[192:195], v7 offset:26624\n"
      "v_add_f32 v84, v84, v189\n"
      "v_mfma_f32_16x16x32_bf16 v[100:103], a[224:227], v[130:133], v[100:103]\n"
      "ds_read_b128 v[136:139], v7 offset:30720\n"
      "v_fmac_f32 v85, v189, v189\n"
      "v_mfma_f32_16x16x32_bf16 v[126:129], a[240:243], v[148:151], 0\n"
      "ds_read_b128 v[214:217], v7 offset:34816\n"
      "v_add_f32 v84, v84, v182\n"
      "v_mfma_f32_16x16x32_bf16 v[126:129], a[244:247], v[154:157], v[126:129]\n"
      "v_fmac_f32 v85, v182, v182\n"
      "v_add_f32 v84, v84, v183\n"
      "v_mfma_f32_16x16x32_bf16 v[126:129], a[248:251], v[130:133], v[126:129]\n"
      "s_waitcnt lgkmcnt(4)\n"
      "v_fmac_f32 v85, v183, v183\n"
      "v_add_f32 v84, v84, v184\n"
      "v_mfma_f32_16x16x32_bf16 v[96:99], v[232:235], v[148:151], 0\n"
      "buffer_store_dwordx4 v[108:111], v20, s[20:23], s46 offen sc1\n"
      "v_mfma_f32_16x16x32_bf16 v[96:99], v[236:239], v[154:157], v[96:99]\n"
      "buffer_store_dwordx4 v[116:119], v21, s[20:23], s46 offen sc1\n"
      "v_mfma_f32_16x16x32_bf16 v[96:99], v[240:243], v[130:133], v[96:99]\n"
      "s_waitcnt lgkmcnt(0)\n"
      "v_fmac_f32 v85, v184, v184\n"
      "v_add_f32_e64 v84, v84, v185\n"
      "v_mfma_f32_16x16x32_bf16 v[100:103], a[228:231], v[198:201], v[100:103]\n"
      "buffer_store_dwordx4 v[192:195], v22, s[20:23], s46 offen sc1\n"
      "v_mfma_f32_16x16x32_bf16 v[100:103], a[232:235], v[174:177], v[100:103]\n"
      "buffer_store_dwordx4 v[136:139], v23, s[20:23], s46 offen sc1\n"
      "v_mfma_f32_16x16x32_bf16 v[100:103], a[236:239], v[210:213], v[100:103]\n"
      "buffer_store_dwordx4 v[214:217], v25, s[20:23], s46 offen sc1\n"
      "v_mfma_f32_16x16x32_bf16 v[126:129], a[252:255], v[198:201], v[126:129]\n"
      "v_fmac_f32 v85, v185, v185\n"
      "v_add_f32 v84, v84, v104\n"
      "v_mfma_f32_16x16x32_bf16 v[126:129], v[224:227], v[174:177], v[126:129]\n"
      "v_fmac_f32 v85, v104, v104\n"
      "v_add_f32 v84, v84, v105\n"
      "v_mfma_f32_16x16x32_bf16 v[126:129], v[228:231], v[210:213], v[126:129]\n"
      "v_mul_f32 v194, v100, v100\n"
      "v_add_f32 v193, v100, v101\n"
      "v_mfma_f32_16x16x32_bf16 v[96:99], v[244:247], v[198:201], v[96:99]\n"
      "v_fmac_f32 v194, v101, v101\n"
      "v_add_f32 v193, v193, v102\n"
      "v_mfma_f32_16x16x32_bf16 v[96:99], v[248:251], v[174:177], v[96:99]\n"
      "v_fmac_f32 v194, v102, v102\n"
      "v_add_f32 v193, v193, v103\n"
      "v_mfma_f32_16x16x32_bf16 v[96:99], v[252:255], v[210:213], v[96:99]\n"
      "v_fmac_f32 v194, v103, v103\n"
      "v_add_f32 v193, v193, v126\n"
      "v_fmac_f32 v194, v126, v126\n"
      "v_add_f32 v193, v193, v127\n"
      "v_fmac_f32 v194, v127, v127\n"
      "v_add_f32 v193, v193, v128\n"
      "v_fmac_f32 v194, v128, v128\n"
      "v_add_f32 v193, v193, v129\n"
      "v_fmac_f32 v194, v129, v129\n"
      "v_add_f32 v193, v193, v96\n"
      "v_fmac_f32 v194, v96, v96\n"
      "v_add_f32 v193, v193, v97\n"
      "v_fmac_f32 v194, v97, v97\n"
      "v_fmac_f32 v85, v105, v105\n"
      "v_add_f32 v193, v193, v98\n"
      "v_fmac_f32 v194, v98, v98\n"
      "v_add_f32 v84, v84, v106\n"
      "v_fmac_f32 v85, v106, v106\n"
      "v_add_f32 v193, v193, v99\n"
      "v_fmac_f32 v194, v99, v99\n"
      "v_add_f32 v84, v84, v107\n"
      "v_fmac_f32 v85, v107, v107\n"
      "v_permlane16_swap_b32 v193, v194\n"
      "v_add_f32 v193, v193, v194\n"
      "v_permlane16_swap_b32 v84, v85\n"
      "v_add_f32 v84, v84, v85\n"
      "v_mov_b32 v194, v193\n"
      "v_readlane_b32 s46, v37, s42\n"
      "v_permlane32_swap_b32 v191, v84\n"
      "v_permlane32_swap_b32 v193, v194\n"
      "v_add_f32 v191, v191, v84\n"
      "v_add_f32_e64 v193, v193, v194\n"
      "ds_write_b32 v8, v191\n"
      "ds_write_b32 v8, v193 offset:512\n"
      "s_or_b32 s95, s46, s48\n"
      "s_cmp_ge_u32 s95, 0x40000000\n"
      "s_cbranch_scc1 L_rare_35_%=\n"
      "L_back_36_%=:\n"
      "s_add_u32 s42, s42, 1\n"
      "s_waitcnt lgkmcnt(0)\n"
      "s_barrier\n"
      "ds_read_b128 v[192:195], v9\n"
      "ds_read_b128 v[174:177], v9 offset:1024\n"
      "v_subrev_u32 v210, 115456, v9\n"
      "v_and_b32 v213, 0x300, v210\n"
      "v_add_u32_e64 v210, v210, v213\n"
      "v_add_u32 v210, 149504, v210\n"
      "ds_read_b128 v[198:201], v210\n"
      "ds_read_b128 v[212:215], v210 offset:2048\n"
      "v_subrev_u32 v154, 115456, v9\n"
      "v_subrev_u32 v216, 73728, v5\n"
      "v_subrev_u32 v211, 73728, v6\n"
      "s_mul_i32 s92, s40, 3072\n"
      "ds_read_b128 v[108:111], v210 offset:4096\n"
      "ds_read_b128 v[148:151], v210 offset:6144\n"
      "v_add_u32 v197, s92, v154\n"
      "v_lshrrev_b32 v154, 4, v197\n"
      "v_add_u32 v191, 0, v154\n"
      "s_waitcnt lgkmcnt(5)\n"
      "v_add_f32 v192, v192, v194\n"
      "v_add_f32 v193, v193, v195\n"
      "v_add_u32 v84, 64, v154\n"
      "s_waitcnt lgkmcnt(4)\n"
      "v_add_f32 v174, v174, v176\n"
      "v_add_f32 v175, v175, v177\n"
      "v_add_f32 v192, v192, v174\n"
      "v_add_f32 v193, v193, v175\n"
      "v_fma_f32 v174, -v192, v192, s52\n"
      "v_fmac_f32 v174, 0x43400000, v193\n"
      "v_max_f32_e64 v174, s52, v174\n"
      "v_rsq_f32 v174, v174\n"
      "v_add_u32 v85, 128, v154\n"
      "v_mul_f32 v217, -v192, v174\n"
      "v_mul_f32 v196, 0x43400000, v174\n"
      "s_waitcnt lgkmcnt(1)\n"
      "v_mfma_f32_16x16x32_bf16 v[154:157], a[216:219], v[198:201], 0\n"
      "v_mfma_f32_16x16x32_bf16 v[154:157], a[220:223], v[212:215], v[154:157]\n"
      "ds_read_b128 v[130:133], v210 offset:8192\n"
      "v_mov_b32_e64 v80, v196\n"
      "v_mfma_f32_16x16x32_bf16 v[192:195], a[240:243], v[198:201], 0\n"
      "s_waitcnt lgkmcnt(1)\n"
      "v_permlane32_swap_b32_e64 v196, v80\n"
      "v_mfma_f32_16x16x32_bf16 v[192:195], a[244:247], v[212:215], v[192:195]\n"
      "v_mfma_f32_16x16x32_bf16 v[136:139], v[232:235], v[198:201], 0\n"
      "v_mfma_f32_16x16x32_bf16 v[136:139], v[236:239], v[212:215], v[136:139]\n"
      "v_mfma_f32_16x16x32_bf16 v[154:157], a[224:227], v[108:111], v[154:157]\n"
      "v_mfma_f32_16x16x32_bf16 v[154:157], a[228:231], v[148:151], v[154:157]\n"
      "s_waitcnt lgkmcnt(0)\n"
      "v_mfma_f32_16x16x32_bf16 v[192:195], a[248:251], v[108:111], v[192:195]\n"
      "v_mfma_f32_16x16x32_bf16 v[192:195], a[252:255], v[148:151], v[192:195]\n"
      "v_mfma_f32_16x16x32_bf16 v[136:139], v[240:243], v[108:111], v[136:139]\n"
      "v_mfma_f32_16x16x32_bf16 v[136:139], v[244:247], v[148:151], v[136:139]\n"
      "v_mfma_f32_16x16x32_bf16 v[154:157], a[232:235], v[130:133], v[154:157]\n"
      "ds_read_b128 v[108:111], v210 offset:10240\n"
      "ds_read_b128 v[174:177], v210 offset:256\n"
      "ds_read_b128 v[116:119], v210 offset:2304\n"
      "v_mov_b32_e64 v212, v217\n"
      "v_mov_b32_e64 v150, v196\n"
      "v_and_b32 v213, 0x3fffffff, v37\n"
      "v_permlane32_swap_b32_e64 v217, v212\n"
      "v_permlane16_swap_b32 v196, v150\n"
      "v_fma_f32 v100, v100, v80, v212\n"
      "v_fma_f32 v101, v101, v80, v212\n"
      "v_cvt_pk_bf16_f32 v100, v100, v101\n"
      "v_fma_f32 v102, v102, v80, v212\n"
      "v_fma_f32 v103, v103, v80, v212\n"
      "v_fma_f32 v96, v96, v80, v212\n"
      "v_fma_f32 v97, v97, v80, v212\n"
      "v_cvt_pk_bf16_f32 v101, v102, v103\n"
      "v_fma_f32 v126, v126, v80, v212\n"
      "v_fma_f32 v127, v127, v80, v212\n"
      "v_fma_f32 v128, v128, v80, v212\n"
      "s_waitcnt lgkmcnt(2)\n"
      "v_mfma_f32_16x16x32_bf16 v[154:157], a[236:239], v[108:111], v[154:157]\n"
      "v_fma_f32 v129, v129, v80, v212\n"
      "v_cvt_pk_bf16_f32 v96, v96, v97\n"
      "v_mfma_f32_16x16x32_bf16 v[192:195], v[224:227], v[130:133], v[192:195]\n"
      "s_waitcnt lgkmcnt(1)\n"
      "v_fma_f32 v98, v98, v80, v212\n"
      "v_fma_f32 v99, v99, v80, v212\n"
      "v_mfma_f32_16x16x32_bf16 v[192:195], v[228:231], v[108:111], v[192:195]\n"
      "s_waitcnt lgkmcnt(0)\n"
      "v_cvt_pk_bf16_f32 v102, v126, v127\n"
      "v_cvt_pk_bf16_f32 v103, v128, v129\n"
      "v_mfma_f32_16x16x32_bf16 v[136:139], v[248:251], v[130:133], v[136:139]\n"
      "ds_write_b128 v5, v[100:103] offset:30720\n"
      "v_mfma_f32_16x16x32_bf16 v[136:139], v[252:255], v[108:111], v[136:139]\n"
      "v_cvt_pk_bf16_f32 v97, v98, v99\n"
      "ds_write_b64 v6, v[96:97] offset:30720\n"
      "v_mov_b32 v132, v217\n"
      "v_mul_f32 v103, v154, v154\n"
      "v_add_f32 v102, v154, v155\n"
      "v_permlane16_swap_b32 v217, v132\n"
      "v_fma_f32 v140, v140, v196, v217\n"
      "v_fma_f32 v141, v141, v196, v217\n"
      "v_cvt_pk_bf16_f32 v140, v140, v141\n"
      "v_fma_f32 v142, v142, v196, v217\n"
      "v_fma_f32 v143, v143, v196, v217\n"
      "v_fma_f32 v160, v160, v196, v217\n"
      "v_fma_f32 v161, v161, v196, v217\n"
      "v_cvt_pk_bf16_f32 v141, v142, v143\n"
      "v_fma_f32 v220, v220, v196, v217\n"
      "v_fma_f32 v221, v221, v196, v217\n"
      "v_fma_f32 v222, v222, v196, v217\n"
      "v_fma_f32 v223, v223, v196, v217\n"
      "v_cvt_pk_bf16_f32 v160, v160, v161\n"
      "v_fma_f32 v162, v162, v196, v217\n"
      "v_fma_f32 v163, v163, v196, v217\n"
      "v_cvt_pk_bf16_f32 v142, v220, v221\n"
      "v_mfma_f32_16x16x32_bf16 v[98:101], a[216:219], v[174:177], 0\n"
      "v_cvt_pk_bf16_f32 v143, v222, v223\n"
      "v_cvt_pk_bf16_f32 v161, v162, v163\n"
      "v_mfma_f32_16x16x32_bf16 v[98:101], a[220:223], v[116:119], v[98:101]\n"
      "ds_write_b128 v5, v[140:143] offset:18432\n"
      "v_mfma_f32_16x16x32_bf16 v[220:223], a[240:243], v[174:177], 0\n"
      "ds_write_b64 v6, v[160:161] offset:18432\n"
      "v_mfma_f32_16x16x32_bf16 v[220:223], a[244:247], v[116:119], v[220:223]\n"
      "ds_read_b128 v[128:131], v210 offset:4352\n"
      "v_fma_f32 v186, v186, v150, v132\n"
      "v_mfma_f32_16x16x32_bf16 v[108:111], v[232:235], v[174:177], 0\n"
      "v_fma_f32 v187, v187, v150, v132\n"
      "v_cvt_pk_bf16_f32 v186, v186, v187\n"
      "v_mfma_f32_16x16x32_bf16 v[108:111], v[236:239], v[116:119], v[108:111]\n"
      "ds_read_b128 v[160:163], v210 offset:6400\n"
      "v_fma_f32 v188, v188, v150, v132\n"
      "v_fma_f32 v189, v189, v150, v132\n"
      "v_fma_f32 v104, v104, v150, v132\n"
      "v_fma_f32 v105, v105, v150, v132\n"
      "v_cvt_pk_bf16_f32 v187, v188, v189\n"
      "v_fma_f32 v182, v182, v150, v132\n"
      "v_fma_f32 v183, v183, v150, v132\n"
      "v_fma_f32 v184, v184, v150, v132\n"
      "v_fma_f32 v185, v185, v150, v132\n"
      "v_cvt_pk_bf16_f32 v104, v104, v105\n"
      "v_fma_f32 v106, v106, v150, v132\n"
      "v_fma_f32 v107, v107, v150, v132\n"
      "ds_read_b128 v[140:143], v7\n"
      "v_cvt_pk_bf16_f32 v188, v182, v183\n"
      "v_cvt_pk_bf16_f32 v189, v184, v185\n"
      "s_waitcnt lgkmcnt(2)\n"
      "v_mfma_f32_16x16x32_bf16 v[98:101], a[224:227], v[128:131], v[98:101]\n"
      "ds_write_b128 v5, v[186:189] offset:24576\n"
      "s_waitcnt lgkmcnt(2)\n"
      "v_mfma_f32_16x16x32_bf16 v[98:101], a[228:231], v[160:163], v[98:101]\n"
      "ds_read_b128 v[182:185], v7 offset:4096\n"
      "v_cvt_pk_bf16_f32 v105, v106, v107\n"
      "v_mfma_f32_16x16x32_bf16 v[220:223], a[248:251], v[128:131], v[220:223]\n"
      "ds_write_b64 v6, v[104:105] offset:24576\n"
      "v_mfma_f32_16x16x32_bf16 v[220:223], a[252:255], v[160:163], v[220:223]\n"
      "s_waitcnt lgkmcnt(3)\n"
      "v_fmac_f32 v103, v155, v155\n"
      "v_add_f32 v102, v102, v156\n"
      "v_mfma_f32_16x16x32_bf16 v[108:111], v[240:243], v[128:131], v[108:111]\n"
      "ds_read_b128 v[116:119], v7 offset:8192\n"
      "v_fmac_f32 v103, v156, v156\n"
      "v_mfma_f32_16x16x32_bf16 v[108:111], v[244:247], v[160:163], v[108:111]\n"
      "ds_read_b128 v[186:189], v210 offset:8448\n"
      "ds_read_b128 v[130:133], v7 offset:12288\n"
      "ds_read_b128 v[126:129], v210 offset:10496\n"
      "buffer_store_dwordx4 v[140:143], v20, s[20:23], s46 offen sc1\n"
      "s_nop 1\n"
      "ds_read_b128 v[140:143], v7 offset:16384\n"
      "v_add_f32 v102, v102, v157\n"
      "v_fmac_f32 v103, v157, v157\n"
      "v_add_f32 v102, v102, v192\n"
      "v_fmac_f32 v103, v192, v192\n"
      "v_add_f32 v102, v102, v193\n"
      "v_fmac_f32 v103, v193, v193\n"
      "v_add_f32 v102, v102, v194\n"
      "v_fmac_f32 v103, v194, v194\n"
      "v_add_f32 v102, v102, v195\n"
      "s_waitcnt lgkmcnt(3)\n"
      "v_mfma_f32_16x16x32_bf16 v[98:101], a[232:235], v[186:189], v[98:101]\n"
      "buffer_store_dwordx4 v[182:185], v21, s[20:23], s46 offen sc1\n"
      "s_waitcnt lgkmcnt(1)\n"
      "v_mfma_f32_16x16x32_bf16 v[98:101], a[236:239], v[126:129], v[98:101]\n"
      "buffer_store_dwordx4 v[116:119], v22, s[20:23], s46 offen sc1\n"
      "v_mfma_f32_16x16x32_bf16 v[220:223], v[224:227], v[186:189], v[220:223]\n"
      "s_waitcnt lgkmcnt(0)\n"
      "v_fmac_f32 v103, v195, v195\n"
      "v_add_f32_e64 v102, v102, v136\n"
      "v_mfma_f32_16x16x32_bf16 v[220:223], v[228:231], v[126:129], v[220:223]\n"
      "buffer_store_dwordx4 v[130:133], v23, s[20:23], s46 offen sc1\n"
      "v_mfma_f32_16x16x32_bf16 v[108:111], v[248:251], v[186:189], v[108:111]\n"
      "buffer_store_dwordx4 v[140:143], v25, s[20:23], s46 offen sc1\n"
      "v_mfma_f32_16x16x32_bf16 v[108:111], v[252:255], v[126:129], v[108:111]\n"
      "v_mul_f32 v119, v98, v98\n"
      "v_add_f32 v118, v98, v99\n"
      "v_fmac_f32 v119, v99, v99\n"
      "v_add_f32 v118, v118, v100\n"
      "v_fmac_f32 v119, v100, v100\n"
      "v_add_f32 v118, v118, v101\n"
      "v_fmac_f32 v119, v101, v101\n"
      "v_add_f32 v118, v118, v220\n"
      "v_fmac_f32 v119, v220, v220\n"
      "v_add_f32 v118, v118, v221\n"
      "v_fmac_f32 v119, v221, v221\n"
      "v_add_f32 v118, v118, v222\n"
      "v_fmac_f32 v119, v222, v222\n"
      "v_add_f32 v118, v118, v223\n"
      "v_fmac_f32 v119, v223, v223\n"
      "v_fmac_f32 v103, v136, v136\n"
      "v_add_f32 v118, v118, v108\n"
      "v_fmac_f32 v119, v108, v108\n"
      "v_add_f32 v102, v102, v137\n"
      "v_fmac_f32 v103, v137, v137\n"
      "v_add_f32 v118, v118, v109\n"
      "v_fmac_f32 v119, v109, v109\n"
      "v_add_f32 v102, v102, v138\n"
      "v_fmac_f32 v103, v138, v138\n"
      "v_add_f32 v118, v118, v110\n"
      "v_fmac_f32 v119, v110, v110\n"
      "v_add_f32 v102, v102, v139\n"
      "v_fmac_f32 v103, v139, v139\n"
      "v_add_f32 v118, v118, v111\n"
      "v_fmac_f32 v119, v111, v111\n"
      "v_permlane16_swap_b32 v102, v103\n"
      "v_add_f32 v102, v102, v103\n"
      "v_permlane16_swap_b32 v118, v119\n"
      "v_add_f32 v118, v118, v119\n"
      "v_ashrrev_i32 v103, 31, v37\n"
      "v_mul_u32_u24 v119, 2731, v191\n"
      "v_permlane32_swap_b32 v102, v118\n"
      "v_add_f32_e64 v102, v102, v118\n"
      "ds_write_b32 v8, v102 offset:4608\n"
      "v_mul_u32_u24 v102, 2731, v84\n"
      "v_mul_u32_u24 v118, 2731, v85\n"
      "v_add_u32 v213, 1179648, v213\n"
      "v_and_b32 v103, 100270464, v103\n"
      "v_lshrrev_b32 v119, 16, v119\n"
      "v_lshrrev_b32 v102, 16, v102\n"
      "v_lshrrev_b32 v118, 16, v118\n"
      "v_sub_u32 v213, v213, v103\n"
      "v_mul_u32_u24 v116, 24, v119\n"
      "v_mul_u32_u24 v117, 24, v102\n"
      "v_mul_u32_u24 v131, 24, v118\n"
      "v_bfe_i32 v103, v37, 30, 1\n"
      "v_sub_u32 v116, v191, v116\n"
      "v_sub_u32 v117, v84, v117\n"
      "v_sub_u32 v131, v85, v131\n"
      "v_and_b32 v103, 196224, v103\n"
      "v_and_b32 v191, 7, v119\n"
      "v_and_b32 v84, 7, v102\n"
      "v_and_b32 v85, 7, v118\n"
      "v_sub_u32 v213, v213, v103\n"
      "v_lshl_add_u32 v119, v119, 2, 12\n"
      "v_lshl_add_u32 v102, v102, 2, 12\n"
      "v_lshl_add_u32 v118, v118, 2, 12\n"
      "ds_bpermute_b32 v106, v119, v213\n"
      "ds_bpermute_b32 v185, v102, v213\n"
      "ds_bpermute_b32 v107, v118, v213\n"
      "v_readlane_b32 s46, v37, s42\n"
      "v_xor_b32 v116, v116, v191\n"
      "s_or_b32 s95, s46, s48\n"
      "s_cmp_ge_u32 s95, 0x40000000\n"
      "s_cbranch_scc1 L_rare_39_%=\n"
      "L_back_40_%=:\n"
      "s_add_u32 s42, s42, 1\n"
      "v_xor_b32 v117, v117, v84\n"
      "v_xor_b32 v131, v131, v85\n"
      "s_waitcnt lgkmcnt(2)\n"
      "v_lshl_add_u32 v106, v116, 4, v106\n"
      "s_waitcnt lgkmcnt(1)\n"
      "v_lshl_add_u32 v185, v117, 4, v185\n"
      "s_waitcnt lgkmcnt(0)\n"
      "v_lshl_add_u32 v107, v131, 4, v107\n"
      "s_waitcnt lgkmcnt(0)\n"
      "s_barrier\n"
      "ds_read_b128 v[102:105], v9 offset:4608\n"
      "ds_read_b128 v[116:119], v9 offset:5632\n"
      "ds_read_b128 v[160:163], v7 offset:18432\n"
      "ds_read_b128 v[130:133], v7 offset:22528\n"
      "ds_read_b128 v[186:189], v7 offset:26624\n"
      "ds_read_b128 v[126:129], v7 offset:30720\n"
      "ds_read_b128 v[174:177], v7 offset:34816\n"
      "s_waitcnt lgkmcnt(6)\n"
      "v_add_f32 v102, v102, v104\n"
      "v_add_f32 v103, v103, v105\n"
      "s_waitcnt lgkmcnt(5)\n"
      "v_add_f32 v116, v116, v118\n"
      "v_add_f32 v117, v117, v119\n"
      "v_add_f32 v102, v102, v116\n"
      "v_add_f32 v103, v103, v117\n"
      "v_fma_f32 v116, -v102, v102, s52\n"
      "v_fmac_f32 v116, 0x43400000, v103\n"
      "v_max_f32 v116, s52, v116\n"
      "v_rsq_f32 v116, v116\n"
      "s_waitcnt lgkmcnt(3)\n"
      "buffer_store_dwordx4 v[160:163], v20, s[20:23], s46 offen sc1\n"
      "v_mul_f32 v103, 0x43400000, v116\n"
      "v_mul_f32 v118, -v102, v116\n"
      "v_mov_b32 v102, v103\n"
      "v_mov_b32 v162, v118\n"
      "buffer_store_dwordx4 v[130:133], v21, s[20:23], s46 offen sc1\n"
      "v_permlane32_swap_b32 v103, v102\n"
      "v_permlane32_swap_b32_e64 v118, v162\n"
      "v_mov_b32_e64 v102, v103\n"
      "v_mov_b32 v133, v118\n"
      "s_waitcnt lgkmcnt(0)\n"
      "buffer_store_dwordx4 v[186:189], v22, s[20:23], s46 offen sc1\n"
      "v_permlane16_swap_b32_e64 v103, v102\n"
      "v_permlane16_swap_b32_e64 v118, v133\n"
      "v_fma_f32 v154, v154, v103, v118\n"
      "v_fma_f32 v155, v155, v103, v118\n"
      "v_fma_f32 v98, v98, v102, v133\n"
      "v_fma_f32 v99, v99, v102, v133\n"
      "v_cvt_pk_bf16_f32 v154, v154, v155\n"
      "v_fma_f32 v156, v156, v103, v118\n"
      "v_fma_f32 v157, v157, v103, v118\n"
      "v_fma_f32 v136, v136, v103, v118\n"
      "v_fma_f32 v137, v137, v103, v118\n"
      "v_cvt_pk_bf16_f32 v98, v98, v99\n"
      "v_fma_f32 v100, v100, v102, v133\n"
      "v_fma_f32 v101, v101, v102, v133\n"
      "v_fma_f32 v108, v108, v102, v133\n"
      "v_fma_f32 v109, v109, v102, v133\n"
      "v_cvt_pk_bf16_f32 v155, v156, v157\n"
      "v_fma_f32 v192, v192, v103, v118\n"
      "v_fma_f32 v193, v193, v103, v118\n"
      "v_fma_f32 v194, v194, v103, v118\n"
      "v_fma_f32 v195, v195, v103, v118\n"
      "v_cvt_pk_bf16_f32 v136, v136, v137\n"
      "v_fma_f32 v138, v138, v103, v118\n"
      "v_fma_f32 v139, v139, v103, v118\n"
      "v_cvt_pk_bf16_f32 v99, v100, v101\n"
      "v_fma_f32 v220, v220, v102, v133\n"
      "v_fma_f32 v221, v221, v102, v133\n"
      "v_fma_f32 v222, v222, v102, v133\n"
      "v_fma_f32 v223, v223, v102, v133\n"
      "v_cvt_pk_bf16_f32 v108, v108, v109\n"
      "v_fma_f32 v110, v110, v102, v133\n"
      "v_fma_f32 v111, v111, v102, v133\n"
      "v_cvt_pk_bf16_f32 v156, v192, v193\n"
      "v_cvt_pk_bf16_f32 v157, v194, v195\n"
      "v_cvt_pk_bf16_f32 v137, v138, v139\n"
      "v_cvt_pk_bf16_f32 v100, v220, v221\n"
      "v_cvt_pk_bf16_f32 v101, v222, v223\n"
      "v_cvt_pk_bf16_f32 v109, v110, v111\n"
      "ds_write_b128 v216, v[154:157]\n"
      "ds_write_b64 v211, v[136:137]\n"
      "ds_write_b128 v216, v[98:101] offset:6144\n"
      "ds_write_b64 v211, v[108:109] offset:6144\n"
      "buffer_store_dwordx4 v[126:129], v23, s[20:23], s46 offen sc1\n"
      "buffer_store_dwordx4 v[174:177], v25, s[20:23], s46 offen sc1\n"
      "s_waitcnt lgkmcnt(0)\n"
      "s_barrier\n"
      "ds_read_b128 v[108:111], v197\n"
      "ds_read_b128 v[100:103], v197 offset:1024\n"
      "ds_read_b128 v[126:129], v197 offset:2048\n"
      "s_waitcnt lgkmcnt(2)\n"
      "buffer_store_dwordx4 v[108:111], v106, s[20:23], 0 offen sc1\n"
      "s_waitcnt lgkmcnt(1)\n"
      "buffer_store_dwordx4 v[100:103], v185, s[20:23], 0 offen sc1\n"
      "s_waitcnt lgkmcnt(0)\n"
      "buffer_store_dwordx4 v[126:129], v107, s[20:23], 0 offen sc1\n"
      "s_endpgm\n"
      "L_rare_1_%=:\n"
      "s_nop 7\n"
      "s_waitcnt lgkmcnt(0)\n"
      "s_add_u32 s72, s42, 1\n"
      "v_readlane_b32 s77, v38, s72\n"
      "v_subrev_u32 v203, 115456, v9\n"
      "s_and_b32 s73, s77, -16\n"
      "s_mov_b32 m0, 110592\n"
      "s_nop 0\n"
      "buffer_load_dwordx4 v203, s[24:27], s73 offen  lds\n"
      "s_add_u32 s73, s73, 1024\n"
      "s_mov_b32 m0, 111616\n"
      "s_nop 0\n"
      "buffer_load_dwordx4 v203, s[24:27], s73 offen  lds\n"
      "s_add_u32 s73, s73, 1024\n"
      "s_mov_b32 m0, 112640\n"
      "s_nop 0\n"
      "buffer_load_dwordx4 v203, s[24:27], s73 offen  lds\n"
      "s_add_u32 s73, s73, 1024\n"
      "s_mov_b32 m0, 113664\n"
      "s_nop 0\n"
      "buffer_load_dwordx4 v203, s[24:27], s73 offen  lds\n"
      "s_add_u32 s73, s73, 1024\n"
      "s_mov_b32 m0, 114688\n"
      "s_mov_b32 exec_hi, 0xffff\n"
      "buffer_load_dwordx4 v203, s[24:27], s73 offen  lds\n"
      "s_mov_b32 exec_hi, -1\n"
      "s_add_u32 s73, s73, 1024\n"
      "s_and_b32 s77, s77, 15\n"
      "s_waitcnt lgkmcnt(0)\n"
      "s_nop 4\n"
      "s_branch L_back_2_%=\n"
      "L_rare_3_%=:\n"
      "s_nop 7\n"
      "s_waitcnt lgkmcnt(0)\n"
      "s_waitcnt vmcnt(0)\n"
      "v_subrev_u32 v219, 115456, v9\n"
      "v_bfe_u32 v199, v219, 4, 4\n"
      "v_mul_u32_u24 v199, 98, v199\n"
      "v_lshrrev_b32 v219, 8, v219\n"
      "v_lshl_add_u32 v199, v219, 3, v199\n"
      "v_add_u32 v199, s77, v199\n"
      "v_and_b32 v214, 2, v199\n"
      "v_and_b32 v199, -4, v199\n"
      "v_add_u32 v199, 110592, v199\n"
      "ds_read2_b32 v[174:175], v199 offset0:0 offset1:1\n"
      "ds_read2_b32 v[176:177], v199 offset0:8 offset1:9\n"
      "ds_read2_b32 v[204:205], v199 offset0:16 offset1:17\n"
      "ds_read2_b32 v[202:203], v199 offset0:2 offset1:10\n"
      "ds_read2_b32 v[220:221], v199 offset0:18 offset1:24\n"
      "s_waitcnt lgkmcnt(4)\n"
      "v_alignbyte_b32 v174, v175, v174, v214\n"
      "s_waitcnt lgkmcnt(1)\n"
      "v_alignbyte_b32 v175, v202, v175, v214\n"
      "v_lshlrev_b32 v219, 16, v174\n"
      "v_fmac_f32 v190, s53, v219\n"
      "v_and_b32 v219, 0xffff0000, v174\n"
      "v_fmac_f32 v191, s53, v219\n"
      "v_lshlrev_b32 v219, 16, v175\n"
      "v_fmac_f32_e64 v192, s53, v219\n"
      "v_and_b32 v219, 0xffff0000, v175\n"
      "v_fmac_f32 v193, s53, v219\n"
      "v_alignbyte_b32 v176, v177, v176, v214\n"
      "v_alignbyte_b32 v177, v203, v177, v214\n"
      "v_lshlrev_b32 v219, 16, v176\n"
      "v_fmac_f32 v158, s53, v219\n"
      "v_and_b32 v219, 0xffff0000, v176\n"
      "v_fmac_f32 v159, s53, v219\n"
      "v_lshlrev_b32 v219, 16, v177\n"
      "v_fmac_f32 v160, s53, v219\n"
      "v_and_b32 v219, 0xffff0000, v177\n"
      "v_fmac_f32 v161, s53, v219\n"
      "v_alignbyte_b32 v204, v205, v204, v214\n"
      "s_waitcnt lgkmcnt(0)\n"
      "v_alignbyte_b32 v205, v220, v205, v214\n"
      "v_lshlrev_b32 v219, 16, v204\n"
      "v_fmac_f32 v98, s53, v219\n"
      "v_and_b32 v219, 0xffff0000, v204\n"
      "v_fmac_f32 v99, s53, v219\n"
      "v_lshlrev_b32 v219, 16, v205\n"
      "v_fmac_f32 v100, s53, v219\n"
      "v_and_b32 v219, 0xffff0000, v205\n"
      "v_fmac_f32 v101, s53, v219\n"
      "v_alignbyte_b32 v219, v221, v221, v214\n"
      "v_lshlrev_b32 v219, 16, v219\n"
      "v_fmac_f32 v128, s53, v219\n"
      "v_max3_f32 v199, v190, v191, v192\n"
      "v_max3_f32 v214, v193, v158, v159\n"
      "v_max3_f32 v199, v199, v160, v161\n"
      "v_max3_f32 v214, v214, v98, v99\n"
      "v_max3_f32 v199, v199, v100, v101\n"
      "v_max3_f32 v199, v199, v214, v128\n"
      "v_mov_b32 v219, v199\n"
      "s_nop 1\n"
      "v_permlane16_swap_b32 v199, v219\n"
      "v_max_f32 v199, v199, v219\n"
      "v_mov_b32 v219, v199\n"
      "s_nop 1\n"
      "v_permlane32_swap_b32 v199, v219\n"
      "v_max_f32 v199, v199, v219\n"
      "v_sub_f32 v190, v190, v199\n"
      "v_sub_f32 v191, v191, v199\n"
      "v_sub_f32 v192, v192, v199\n"
      "v_sub_f32 v193, v193, v199\n"
      "v_sub_f32 v158, v158, v199\n"
      "v_sub_f32 v159, v159, v199\n"
      "v_sub_f32 v160, v160, v199\n"
      "v_sub_f32 v161, v161, v199\n"
      "v_sub_f32 v98, v98, v199\n"
      "v_sub_f32 v99, v99, v199\n"
      "v_sub_f32 v100, v100, v199\n"
      "v_sub_f32 v101, v101, v199\n"
      "v_sub_f32_e64 v128, v128, v199\n"
      "s_waitcnt lgkmcnt(0)\n"
      "s_nop 4\n"
      "s_branch L_back_4_%=\n"
      "L_rare_5_%=:\n"
      "s_nop 7\n"
      "s_waitcnt lgkmcnt(0)\n"
      "s_waitcnt vmcnt(0)\n"
      "v_subrev_u32 v194, 115456, v9\n"
      "v_bfe_u32 v219, v194, 4, 4\n"
      "v_mul_u32_u24 v219, 98, v219\n"
      "v_lshrrev_b32 v194, 8, v194\n"
      "v_lshl_add_u32 v219, v194, 3, v219\n"
      "v_add_u32 v219, s77, v219\n"
      "v_and_b32 v218, 2, v219\n"
      "v_and_b32 v219, -4, v219\n"
      "v_add_u32 v219, 112160, v219\n"
      "ds_read2_b32 v[166:167], v219 offset0:0 offset1:1\n"
      "ds_read2_b32 v[168:169], v219 offset0:8 offset1:9\n"
      "ds_read2_b32 v[188:189], v219 offset0:16 offset1:17\n"
      "ds_read2_b32 v[182:183], v219 offset0:2 offset1:10\n"
      "ds_read2_b32 v[220:221], v219 offset0:18 offset1:24\n"
      "s_waitcnt lgkmcnt(4)\n"
      "v_alignbyte_b32 v166, v167, v166, v218\n"
      "s_waitcnt lgkmcnt(1)\n"
      "v_alignbyte_b32 v167, v182, v167, v218\n"
      "v_lshlrev_b32 v194, 16, v166\n"
      "v_fmac_f32 v174, s53, v194\n"
      "v_and_b32 v194, 0xffff0000, v166\n"
      "v_fmac_f32 v175, s53, v194\n"
      "v_lshlrev_b32 v194, 16, v167\n"
      "v_fmac_f32 v176, s53, v194\n"
      "v_and_b32 v194, 0xffff0000, v167\n"
      "v_fmac_f32 v177, s53, v194\n"
      "v_alignbyte_b32 v168, v169, v168, v218\n"
      "v_alignbyte_b32 v169, v183, v169, v218\n"
      "v_lshlrev_b32 v194, 16, v168\n"
      "v_fmac_f32 v108, s53, v194\n"
      "v_and_b32 v194, 0xffff0000, v168\n"
      "v_fmac_f32 v109, s53, v194\n"
      "v_lshlrev_b32 v194, 16, v169\n"
      "v_fmac_f32_e64 v110, s53, v194\n"
      "v_and_b32 v194, 0xffff0000, v169\n"
      "v_fmac_f32 v111, s53, v194\n"
      "v_alignbyte_b32 v188, v189, v188, v218\n"
      "s_waitcnt lgkmcnt(0)\n"
      "v_alignbyte_b32 v189, v220, v189, v218\n"
      "v_lshlrev_b32 v194, 16, v188\n"
      "v_fmac_f32 v202, s53, v194\n"
      "v_and_b32 v194, 0xffff0000, v188\n"
      "v_fmac_f32 v203, s53, v194\n"
      "v_lshlrev_b32 v194, 16, v189\n"
      "v_fmac_f32_e64 v204, s53, v194\n"
      "v_and_b32 v194, 0xffff0000, v189\n"
      "v_fmac_f32 v205, s53, v194\n"
      "v_alignbyte_b32 v194, v221, v221, v218\n"
      "v_lshlrev_b32 v194, 16, v194\n"
      "v_fmac_f32_e64 v180, s53, v194\n"
      "v_max3_f32 v219, v174, v175, v176\n"
      "v_max3_f32 v218, v177, v108, v109\n"
      "v_max3_f32 v219, v219, v110, v111\n"
      "v_max3_f32 v218, v218, v202, v203\n"
      "v_max3_f32 v219, v219, v204, v205\n"
      "v_max3_f32 v219, v219, v218, v180\n"
      "v_mov_b32 v194, v219\n"
      "s_nop 1\n"
      "v_permlane16_swap_b32 v219, v194\n"
      "v_max_f32 v219, v219, v194\n"
      "v_mov_b32 v194, v219\n"
      "s_nop 1\n"
      "v_permlane32_swap_b32 v219, v194\n"
      "v_max_f32 v219, v219, v194\n"
      "v_sub_f32 v174, v174, v219\n"
      "v_sub_f32 v175, v175, v219\n"
      "v_sub_f32 v176, v176, v219\n"
      "v_sub_f32 v177, v177, v219\n"
      "v_sub_f32 v108, v108, v219\n"
      "v_sub_f32 v109, v109, v219\n"
      "v_sub_f32 v110, v110, v219\n"
      "v_sub_f32 v111, v111, v219\n"
      "v_sub_f32 v202, v202, v219\n"
      "v_sub_f32 v203, v203, v219\n"
      "v_sub_f32 v204, v204, v219\n"
      "v_sub_f32 v205, v205, v219\n"
      "v_sub_f32_e64 v180, v180, v219\n"
      "s_waitcnt lgkmcnt(0)\n"
      "s_nop 4\n"
      "s_branch L_back_6_%=\n"
      "L_rare_7_%=:\n"
      "s_nop 7\n"
      "s_waitcnt lgkmcnt(0)\n"
      "s_waitcnt vmcnt(0)\n"
      "v_subrev_u32 v137, 115456, v9\n"
      "v_bfe_u32 v179, v137, 4, 4\n"
      "v_mul_u32_u24 v179, 98, v179\n"
      "v_lshrrev_b32_e64 v137, 8, v137\n"
      "v_lshl_add_u32 v179, v137, 3, v179\n"
      "v_add_u32 v179, s77, v179\n"
      "v_and_b32 v178, 2, v179\n"
      "v_and_b32_e64 v179, -4, v179\n"
      "v_add_u32 v179, 113728, v179\n"
      "ds_read2_b32 v[110:111], v179 offset0:0 offset1:1\n"
      "ds_read2_b32 v[108:109], v179 offset0:8 offset1:9\n"
      "ds_read2_b32 v[140:141], v179 offset0:16 offset1:17\n"
      "ds_read2_b32 v[180:181], v179 offset0:2 offset1:10\n"
      "ds_read2_b32 v[148:149], v179 offset0:18 offset1:24\n"
      "s_waitcnt lgkmcnt(4)\n"
      "v_alignbyte_b32 v110, v111, v110, v178\n"
      "s_waitcnt lgkmcnt(1)\n"
      "v_alignbyte_b32 v111, v180, v111, v178\n"
      "v_lshlrev_b32 v137, 16, v110\n"
      "v_fmac_f32 v182, s53, v137\n"
      "v_and_b32 v137, 0xffff0000, v110\n"
      "v_fmac_f32 v183, s53, v137\n"
      "v_lshlrev_b32 v137, 16, v111\n"
      "v_fmac_f32_e64 v184, s53, v137\n"
      "v_and_b32 v137, 0xffff0000, v111\n"
      "v_fmac_f32 v185, s53, v137\n"
      "v_alignbyte_b32 v108, v109, v108, v178\n"
      "v_alignbyte_b32 v109, v181, v109, v178\n"
      "v_lshlrev_b32 v137, 16, v108\n"
      "v_fmac_f32 v158, s53, v137\n"
      "v_and_b32 v137, 0xffff0000, v108\n"
      "v_fmac_f32 v159, s53, v137\n"
      "v_lshlrev_b32 v137, 16, v109\n"
      "v_fmac_f32 v160, s53, v137\n"
      "v_and_b32 v137, 0xffff0000, v109\n"
      "v_fmac_f32 v161, s53, v137\n"
      "v_alignbyte_b32 v140, v141, v140, v178\n"
      "s_waitcnt lgkmcnt(0)\n"
      "v_alignbyte_b32 v141, v148, v141, v178\n"
      "v_lshlrev_b32 v137, 16, v140\n"
      "v_fmac_f32 v100, s53, v137\n"
      "v_and_b32 v137, 0xffff0000, v140\n"
      "v_fmac_f32 v101, s53, v137\n"
      "v_lshlrev_b32 v137, 16, v141\n"
      "v_fmac_f32 v102, s53, v137\n"
      "v_and_b32 v137, 0xffff0000, v141\n"
      "v_fmac_f32 v103, s53, v137\n"
      "v_alignbyte_b32 v137, v149, v149, v178\n"
      "v_lshlrev_b32 v137, 16, v137\n"
      "v_fmac_f32 v134, s53, v137\n"
      "v_max3_f32 v179, v182, v183, v184\n"
      "v_max3_f32 v178, v185, v158, v159\n"
      "v_max3_f32 v179, v179, v160, v161\n"
      "v_max3_f32 v178, v178, v100, v101\n"
      "v_max3_f32 v179, v179, v102, v103\n"
      "v_max3_f32 v179, v179, v178, v134\n"
      "v_mov_b32 v137, v179\n"
      "s_nop 1\n"
      "v_permlane16_swap_b32 v179, v137\n"
      "v_max_f32 v179, v179, v137\n"
      "v_mov_b32 v137, v179\n"
      "s_nop 1\n"
      "v_permlane32_swap_b32 v179, v137\n"
      "v_max_f32 v179, v179, v137\n"
      "v_sub_f32 v182, v182, v179\n"
      "v_sub_f32 v183, v183, v179\n"
      "v_sub_f32 v184, v184, v179\n"
      "v_sub_f32 v185, v185, v179\n"
      "v_sub_f32 v158, v158, v179\n"
      "v_sub_f32 v159, v159, v179\n"
      "v_sub_f32 v160, v160, v179\n"
      "v_sub_f32 v161, v161, v179\n"
      "v_sub_f32 v100, v100, v179\n"
      "v_sub_f32 v101, v101, v179\n"
      "v_sub_f32 v102, v102, v179\n"
      "v_sub_f32 v103, v103, v179\n"
      "v_sub_f32 v134, v134, v179\n"
      "s_waitcnt lgkmcnt(0)\n"
      "s_nop 4\n"
      "s_branch L_back_8_%=\n"
      "L_rare_9_%=:\n"
      "s_nop 7\n"
      "s_waitcnt lgkmcnt(0)\n"
      "s_waitcnt vmcnt(0)\n"
      "v_subrev_u32 v139, 115456, v9\n"
      "v_lshrrev_b32 v139, 3, v139\n"
      "v_min_u32 v139, 96, v139\n"
      "v_add_u32 v139, s77, v139\n"
      "v_add_u32 v139, 115296, v139\n"
      "ds_read_u16 v139, v139\n"
      "s_waitcnt lgkmcnt(0)\n"
      "v_lshlrev_b32_e64 v139, 16, v139\n"
      "v_fmac_f32 v174, s53, v139\n"
      "s_nop 1\n"
      "v_max_f32_dpp v138, v174, v174 row_ror:8 row_mask:0xf bank_mask:0xf\n"
      "s_nop 1\n"
      "v_max_f32_dpp v138, v138, v138 row_ror:4 row_mask:0xf bank_mask:0xf\n"
      "s_nop 1\n"
      "v_max_f32_dpp v138, v138, v138 row_ror:2 row_mask:0xf bank_mask:0xf\n"
      "s_nop 1\n"
      "v_max_f32_dpp v138, v138, v138 row_ror:1 row_mask:0xf bank_mask:0xf\n"
      "s_nop 1\n"
      "v_max_f32_dpp v138, v138, v138 row_bcast:15 row_mask:0xa bank_mask:0xf\n"
      "s_nop 1\n"
      "v_max_f32_dpp v138, v138, v138 row_bcast:31 row_mask:0xc bank_mask:0xf\n"
      "s_nop 0\n"
      "v_readlane_b32 s72, v138, 63\n"
      "s_nop 1\n"
      "v_subrev_f32 v174, s72, v174\n"
      "s_waitcnt lgkmcnt(0)\n"
      "s_nop 4\n"
      "s_branch L_back_10_%=\n"
      "L_rare_27_%=:\n"
      "s_nop 7\n"
      "s_waitcnt lgkmcnt(0)\n"
      "s_bitcmp1_b32 s46, 31\n"
      "s_cselect_b32 s75, 100270464, 0\n"
      "s_bitcmp1_b32 s46, 30\n"
      "s_cselect_b32 s76, 196224, 0\n"
      "s_and_b32 s74, s46, 0x3fffffff\n"
      "s_cmp_ge_u32 s46, 0x40000000\n"
      "s_cselect_b32 s74, s74, 0\n"
      "s_cselect_b32 s46, 0, s46\n"
      "s_sub_u32 s74, s74, s69\n"
      "s_add_u32 s69, s69, s74\n"
      "s_sub_u32 s75, s75, s70\n"
      "s_add_u32 s70, s70, s75\n"
      "s_sub_u32 s76, s76, s71\n"
      "s_add_u32 s71, s71, s76\n"
      "v_add_u32 v20, s74, v20\n"
      "v_add_u32 v21, s74, v21\n"
      "v_add_u32 v22, s74, v22\n"
      "v_add_u32 v23, s74, v23\n"
      "v_add_u32 v25, s74, v25\n"
      "s_cmp_eq_u32 s75, 0\n"
      "s_cbranch_scc1 L_ax_25_%=\n"
      "v_bfe_i32 v216, v36, 10, 1\n"
      "v_and_b32 v216, s75, v216\n"
      "v_sub_u32 v20, v20, v216\n"
      "v_bfe_i32 v216, v36, 12, 1\n"
      "v_and_b32 v216, s75, v216\n"
      "v_sub_u32 v21, v21, v216\n"
      "v_bfe_i32 v216, v36, 14, 1\n"
      "v_and_b32 v216, s75, v216\n"
      "v_sub_u32 v22, v22, v216\n"
      "v_bfe_i32 v216, v36, 16, 1\n"
      "v_and_b32 v216, s75, v216\n"
      "v_sub_u32 v23, v23, v216\n"
      "v_bfe_i32 v216, v36, 18, 1\n"
      "v_and_b32 v216, s75, v216\n"
      "v_sub_u32 v25, v25, v216\n"
      "L_ax_25_%=:\n"
      "s_cmp_eq_u32 s76, 0\n"
      "s_cbranch_scc1 L_ax_26_%=\n"
      "v_bfe_i32 v216, v36, 11, 1\n"
      "v_and_b32 v216, s76, v216\n"
      "v_sub_u32_e64 v20, v20, v216\n"
      "v_bfe_i32 v216, v36, 13, 1\n"
      "v_and_b32 v216, s76, v216\n"
      "v_sub_u32 v21, v21, v216\n"
      "v_bfe_i32 v216, v36, 15, 1\n"
      "v_and_b32 v216, s76, v216\n"
      "v_sub_u32 v22, v22, v216\n"
      "v_bfe_i32 v216, v36, 17, 1\n"
      "v_and_b32 v216, s76, v216\n"
      "v_sub_u32 v23, v23, v216\n"
      "v_bfe_i32 v216, v36, 19, 1\n"
      "v_and_b32 v216, s76, v216\n"
      "v_sub_u32 v25, v25, v216\n"
      "L_ax_26_%=:\n"
      "s_or_b32 s48, s66, s69\n"
      "s_cmp_lg_u32 s48, 0\n"
      "s_cselect_b32 s48, 0x40000000, 0\n"
      "s_waitcnt lgkmcnt(0)\n"
      "s_nop 4\n"
      "s_branch L_back_28_%=\n"
      "L_rare_31_%=:\n"
      "s_nop 7\n"
      "s_waitcnt lgkmcnt(0)\n"
      "s_bitcmp1_b32 s46, 31\n"
      "s_cselect_b32 s75, 100270464, 0\n"
      "s_bitcmp1_b32 s46, 30\n"
      "s_cselect_b32 s76, 196224, 0\n"
      "s_and_b32 s74, s46, 0x3fffffff\n"
      "s_cmp_ge_u32 s46, 0x40000000\n"
      "s_cselect_b32 s74, s74, 0\n"
      "s_cselect_b32 s46, 0, s46\n"
      "s_sub_u32 s74, s74, s69\n"
      "s_add_u32 s69, s69, s74\n"
      "s_sub_u32 s75, s75, s70\n"
      "s_add_u32 s70, s70, s75\n"
      "s_sub_u32 s76, s76, s71\n"
      "s_add_u32 s71, s71, s76\n"
      "v_add_u32 v20, s74, v20\n"
      "v_add_u32 v21, s74, v21\n"
      "v_add_u32 v22, s74, v22\n"
      "v_add_u32 v23, s74, v23\n"
      "v_add_u32 v25, s74, v25\n"
      "s_cmp_eq_u32 s75, 0\n"
      "s_cbranch_scc1 L_ax_29_%=\n"
      "v_bfe_i32 v212, v36, 10, 1\n"
      "v_and_b32 v212, s75, v212\n"
      "v_sub_u32 v20, v20, v212\n"
      "v_bfe_i32 v212, v36, 12, 1\n"
      "v_and_b32 v212, s75, v212\n"
      "v_sub_u32 v21, v21, v212\n"
      "v_bfe_i32 v212, v36, 14, 1\n"
      "v_and_b32 v212, s75, v212\n"
      "v_sub_u32 v22, v22, v212\n"
      "v_bfe_i32 v212, v36, 16, 1\n"
      "v_and_b32 v212, s75, v212\n"
      "v_sub_u32 v23, v23, v212\n"
      "v_bfe_i32 v212, v36, 18, 1\n"
      "v_and_b32 v212, s75, v212\n"
      "v_sub_u32 v25, v25, v212\n"
      "L_ax_29_%=:\n"
      "s_cmp_eq_u32 s76, 0\n"
      "s_cbranch_scc1 L_ax_30_%=\n"
      "v_bfe_i32 v212, v36, 11, 1\n"
      "v_and_b32 v212, s76, v212\n"
      "v_sub_u32 v20, v20, v212\n"
      "v_bfe_i32 v212, v36, 13, 1\n"
      "v_and_b32 v212, s76, v212\n"
      "v_sub_u32 v21, v21, v212\n"
      "v_bfe_i32 v212, v36, 15, 1\n"
      "v_and_b32 v212, s76, v212\n"
      "v_sub_u32 v22, v22, v212\n"
      "v_bfe_i32 v212, v36, 17, 1\n"
      "v_and_b32 v212, s76, v212\n"
      "v_sub_u32 v23, v23, v212\n"
      "v_bfe_i32 v212, v36, 19, 1\n"
      "v_and_b32 v212, s76, v212\n"
      "v_sub_u32 v25, v25, v212\n"
      "L_ax_30_%=:\n"
      "s_or_b32 s48, s66, s69\n"
      "s_cmp_lg_u32 s48, 0\n"
      "s_cselect_b32 s48, 0x40000000, 0\n"
      "s_waitcnt lgkmcnt(0)\n"
      "s_nop 4\n"
      "s_branch L_back_32_%=\n"
      "L_rare_35_%=:\n"
      "s_nop 7\n"
      "s_waitcnt lgkmcnt(0)\n"
      "s_bitcmp1_b32 s46, 31\n"
      "s_cselect_b32 s75, 100270464, 0\n"
      "s_bitcmp1_b32 s46, 30\n"
      "s_cselect_b32 s76, 196224, 0\n"
      "s_and_b32 s74, s46, 0x3fffffff\n"
      "s_cmp_ge_u32 s46, 0x40000000\n"
      "s_cselect_b32 s74, s74, 0\n"
      "s_cselect_b32 s46, 0, s46\n"
      "s_sub_u32 s74, s74, s69\n"
      "s_add_u32 s69, s69, s74\n"
      "s_sub_u32 s75, s75, s70\n"
      "s_add_u32 s70, s70, s75\n"
      "s_sub_u32 s76, s76, s71\n"
      "s_add_u32 s71, s71, s76\n"
      "v_add_u32 v20, s74, v20\n"
      "v_add_u32 v21, s74, v21\n"
      "v_add_u32 v22, s74, v22\n"
      "v_add_u32 v23, s74, v23\n"
      "v_add_u32 v25, s74, v25\n"
      "s_cmp_eq_u32 s75, 0\n"
      "s_cbranch_scc1 L_ax_33_%=\n"
      "v_bfe_i32 v193, v36, 10, 1\n"
      "v_and_b32 v193, s75, v193\n"
      "v_sub_u32 v20, v20, v193\n"
      "v_bfe_i32 v193, v36, 12, 1\n"
      "v_and_b32 v193, s75, v193\n"
      "v_sub_u32 v21, v21, v193\n"
      "v_bfe_i32 v193, v36, 14, 1\n"
      "v_and_b32 v193, s75, v193\n"
      "v_sub_u32 v22, v22, v193\n"
      "v_bfe_i32 v193, v36, 16, 1\n"
      "v_and_b32 v193, s75, v193\n"
      "v_sub_u32 v23, v23, v193\n"
      "v_bfe_i32 v193, v36, 18, 1\n"
      "v_and_b32 v193, s75, v193\n"
      "v_sub_u32 v25, v25, v193\n"
      "L_ax_33_%=:\n"
      "s_cmp_eq_u32 s76, 0\n"
      "s_cbranch_scc1 L_ax_34_%=\n"
      "v_bfe_i32 v193, v36, 11, 1\n"
      "v_and_b32 v193, s76, v193\n"
      "v_sub_u32 v20, v20, v193\n"
      "v_bfe_i32 v193, v36, 13, 1\n"
      "v_and_b32 v193, s76, v193\n"
      "v_sub_u32 v21, v21, v193\n"
      "v_bfe_i32 v193, v36, 15, 1\n"
      "v_and_b32 v193, s76, v193\n"
      "v_sub_u32 v22, v22, v193\n"
      "v_bfe_i32 v193, v36, 17, 1\n"
      "v_and_b32 v193, s76, v193\n"
      "v_sub_u32 v23, v23, v193\n"
      "v_bfe_i32 v193, v36, 19, 1\n"
      "v_and_b32 v193, s76, v193\n"
      "v_sub_u32 v25, v25, v193\n"
      "L_ax_34_%=:\n"
      "s_or_b32 s48, s66, s69\n"
      "s_cmp_lg_u32 s48, 0\n"
      "s_cselect_b32 s48, 0x40000000, 0\n"
      "s_waitcnt lgkmcnt(0)\n"
      "s_nop 4\n"
      "s_branch L_back_36_%=\n"
      "L_rare_39_%=:\n"
      "s_nop 7\n"
      "s_waitcnt lgkmcnt(0)\n"
      "s_bitcmp1_b32 s46, 31\n"
      "s_cselect_b32 s75, 100270464, 0\n"
      "s_bitcmp1_b32 s46, 30\n"
      "s_cselect_b32 s76, 196224, 0\n"
      "s_and_b32 s74, s46, 0x3fffffff\n"
      "s_cmp_ge_u32 s46, 0x40000000\n"
      "s_cselect_b32 s74, s74, 0\n"
      "s_cselect_b32 s46, 0, s46\n"
      "s_sub_u32 s74, s74, s69\n"
      "s_add_u32 s69, s69, s74\n"
      "s_sub_u32 s75, s75, s70\n"
      "s_add_u32 s70, s70, s75\n"
      "s_sub_u32 s76, s76, s71\n"
      "s_add_u32 s71, s71, s76\n"
      "v_add_u32 v20, s74, v20\n"
      "v_add_u32 v21, s74, v21\n"
      "v_add_u32 v22, s74, v22\n"
      "v_add_u32 v23, s74, v23\n"
      "v_add_u32 v25, s74, v25\n"
      "s_cmp_eq_u32 s75, 0\n"
      "s_cbranch_scc1 L_ax_37_%=\n"
      "v_bfe_i32 v102, v36, 10, 1\n"
      "v_and_b32 v102, s75, v102\n"
      "v_sub_u32 v20, v20, v102\n"
      "v_bfe_i32 v102, v36, 12, 1\n"
      "v_and_b32 v102, s75, v102\n"
      "v_sub_u32 v21, v21, v102\n"
      "v_bfe_i32 v102, v36, 14, 1\n"
      "v_and_b32 v102, s75, v102\n"
      "v_sub_u32 v22, v22, v102\n"
      "v_bfe_i32 v102, v36, 16, 1\n"
      "v_and_b32 v102, s75, v102\n"
      "v_sub_u32 v23, v23, v102\n"
      "v_bfe_i32 v102, v36, 18, 1\n"
      "v_and_b32 v102, s75, v102\n"
      "v_sub_u32 v25, v25, v102\n"
      "L_ax_37_%=:\n"
      "s_cmp_eq_u32 s76, 0\n"
      "s_cbranch_scc1 L_ax_38_%=\n"
      "v_bfe_i32 v102, v36, 11, 1\n"
      "v_and_b32 v102, s76, v102\n"
      "v_sub_u32 v20, v20, v102\n"
      "v_bfe_i32 v102, v36, 13, 1\n"
      "v_and_b32 v102, s76, v102\n"
      "v_sub_u32 v21, v21, v102\n"
      "v_bfe_i32 v102, v36, 15, 1\n"
      "v_and_b32 v102, s76, v102\n"
      "v_sub_u32 v22, v22, v102\n"
      "v_bfe_i32 v102, v36, 17, 1\n"
      "v_and_b32 v102, s76, v102\n"
      "v_sub_u32 v23, v23, v102\n"
      "v_bfe_i32 v102, v36, 19, 1\n"
      "v_and_b32 v102, s76, v102\n"
      "v_sub_u32 v25, v25, v102\n"
      "L_ax_38_%=:\n"
      "s_or_b32 s48, s66, s69\n"
      "s_cmp_lg_u32 s48, 0\n"
      "s_cselect_b32 s48, 0x40000000, 0\n"
      "s_waitcnt lgkmcnt(0)\n"
      "s_nop 4\n"
      "s_branch L_back_40_%=\n"
      "L_cend_%=:\n"
      :
      : "s"(__builtin_amdgcn_kernarg_segment_ptr()), "s"(wave), "s"(blockIdx.x), "s"(lds0)
      : "v0","v1","v2","v3","v4","v5","v6","v7","v8","v9","v10","v11","v12","v13","v14","v15","v16","v17","v18","v19","v20","v21","v22","v23","v24","v25","v26","v27","v28","v29","v30","v31","v32","v33","v34","v35","v36","v37","v38","v39","v40","v41","v42","v43","v44","v45","v46","v47","v48","v49","v50","v51","v52","v53","v54","v55","v56","v57","v58","v59","v60","v61","v62","v63","v64","v65","v66","v67","v68","v69","v70","v71","v72","v73","v74","v75","v76","v77","v78","v79","v80","v81","v82","v83","v84","v85","v86","v87","v88","v89","v90","v91","v92","v93","v94","v95","v96","v97","v98","v99","v100","v101","v102","v103","v104","v105","v106","v107","v108","v109","v110","v111","v112","v113","v114","v115","v116","v117","v118","v119","v120","v121","v122","v123","v124","v125","v126","v127","v128","v129","v130","v131","v132","v133","v134","v135","v136","v137","v138","v139","v140","v141","v142","v143","v144","v145","v146","v147","v148","v149","v150","v151","v152","v153","v154","v155","v156","v157","v158","v159","v160","v161","v162","v163","v164","v165","v166","v167","v168","v169","v170","v171","v172","v173","v174","v175","v176","v177","v178","v179","v180","v181","v182","v183","v184","v185","v186","v187","v188","v189","v190","v191","v192","v193","v194","v195","v196","v197","v198","v199","v200","v201","v202","v203","v204","v205","v206","v207","v208","v209","v210","v211","v212","v213","v214","v215","v216","v217","v218","v219","v220","v221","v222","v223","v224","v225","v226","v227","v228","v229","v230","v231","v232","v233","v234","v235","v236","v237","v238","v239","v240","v241","v242","v243","v244","v245","v246","v247","v248","v249","v250","v251","v252","v253","v254","v255","a0","a1","a2","a3","a4","a5","a6","a7","a8","a9","a10","a11","a12","a13","a14","a15","a16","a17","a18","a19","a20","a21","a22","a23","a24","a25","a26","a27","a28","a29","a30","a31","a32","a33","a34","a35","a36","a37","a38","a39","a40","a41","a42","a43","a44","a45","a46","a47","a48","a49","a50","a51","a52","a53","a54","a55","a56","a57","a58","a59","a60","a61","a62","a63","a64","a65","a66","a67","a68","a69","a70","a71","a72","a73","a74","a75","a76","a77","a78","a79","a80","a81","a82","a83","a84","a85","a86","a87","a88","a89","a90","a91","a92","a93","a94","a95","a96","a97","a98","a99","a100","a101","a102","a103","a104","a105","a106","a107","a108","a109","a110","a111","a112","a113","a114","a115","a116","a117","a118","a119","a120","a121","a122","a123","a124","a125","a126","a127","a128","a129","a130","a131","a132","a133","a134","a135","a136","a137","a138","a139","a140","a141","a142","a143","a144","a145","a146","a147","a148","a149","a150","a151","a152","a153","a154","a155","a156","a157","a158","a159","a160","a161","a162","a163","a164","a165","a166","a167","a168","a169","a170","a171","a172","a173","a174","a175","a176","a177","a178","a179","a180","a181","a182","a183","a184","a185","a186","a187","a188","a189","a190","a191","a192","a193","a194","a195","a196","a197","a198","a199","a200","a201","a202","a203","a204","a205","a206","a207","a208","a209","a210","a211","a212","a213","a214","a215","a216","a217","a218","a219","a220","a221","a222","a223","a224","a225","a226","a227","a228","a229","a230","a231","a232","a233","a234","a235","a236","a237","a238","a239","a240","a241","a242","a243","a244","a245","a246","a247","a248","a249","a250","a251","a252","a253","a254","a255","s16","s17","s18","s19","s20","s21","s22","s23","s24","s25","s26","s27","s28","s29","s30","s31","s33","s34","s35","s36","s37","s38","s39","s40","s41","s42","s43","s44","s45","s46","s47","s48","s49","s50","s51","s52","s53","s54","s55","s56","s57","s58","s59","s60","s61","s62","s63","s64","s65","s66","s67","s68","s69","s70","s71","s72","s73","s74","s75","s76","s77","s78","s79","s80","s81","s82","s83","s84","s85","s86","s87","s88","s89","s90","s91","s92","s93","s94","s95","s96","s97","s98","s99","s100","s101", "vcc", "scc", "memory");
}

}

extern "C" void kernel_launch(void* const* d_in, const int* in_sizes, int n_in, void* d_out, int out_size, void* d_ws,
                              size_t ws_size, hipStream_t stream) {
  const uint16_t* x = (const uint16_t*)d_in[0];
  const uint16_t* wqkv = (const uint16_t*)d_in[1];
  const uint16_t* wo = (const uint16_t*)d_in[2];
  const uint16_t* ls = (const uint16_t*)d_in[3];
  const uint16_t* w1 = (const uint16_t*)d_in[4];
  const uint16_t* w2 = (const uint16_t*)d_in[5];
  const uint16_t* rc = (const uint16_t*)d_in[6];
  const int* ridx = (const int*)d_in[7];
  const uint16_t* mask = (const uint16_t*)d_in[8];
  swin::swin_main<<<256, 256, 0, stream>>>(x, mask, (char*)d_ws, (uint16_t*)d_out, wqkv, wo, ls, w1, w2, rc, ridx);
}
```

```cpp
#include <hip/hip_runtime.h>
#include <hip/hip_bf16.h>
#include <stdint.h>

namespace swin {

constexpr unsigned CT_WORDS = 8192u;
constexpr unsigned LDS_BYTES = 163840u;

__device__ __attribute__((aligned(256))) const unsigned CT_TAB[CT_WORDS] = {0x4800u,0x4820u,0x4840u,0x4800u,0x4808u,0x4828u,0x4848u,0x4810u,0x4810u,0x4830u,0x4850u,0x4820u,0x4818u,0x4838u,0x4858u,0x4830u,0x4880u,0x48a0u,0x48c0u,0x4880u,0x4888u,0x48a8u,0x48c8u,0x4890u,0x4890u,0x48b0u,0x48d0u,0x48a0u,0x4898u,0x48b8u,0x48d8u,0x48b0u,0x4920u,0x4900u,0x4960u,0x4920u,0x4928u,0x4908u,0x4968u,0x4930u,0x4930u,0x4910u,0x4970u,0x4900u,0x4938u,0x4918u,0x4978u,0x4910u,0x49a0u,0x4980u,0x49e0u,0x49a0u,0x49a8u,0x4988u,0x49e8u,0x49b0u,0x49b0u,0x4990u,0x49f0u,0x4980u,0x49b8u,0x4998u,0x49f8u,0x4990u,0x4c40u,0x4c60u,0x4c00u,0x4a00u,0x4c48u,0x4c68u,0x4c08u,0x4a10u,0x4c50u,0x4c70u,0x4c10u,0x4a20u,0x4c58u,0x4c78u,0x4c18u,0x4a30u,0x4cc0u,0x4ce0u,0x4c80u,0x4a80u,0x4cc8u,0x4ce8u,0x4c88u,0x4a90u,0x4cd0u,0x4cf0u,0x4c90u,0x4aa0u,0x4cd8u,0x4cf8u,0x4c98u,0x4ab0u,0x4d60u,0x4d40u,0x4d20u,0x4b20u,0x4d68u,0x4d48u,0x4d28u,0x4b30u,0x4d70u,0x4d50u,0x4d30u,0x4b00u,0x4d78u,0x4d58u,0x4d38u,0x4b10u,0x4de0u,0x4dc0u,0x4da0u,0x4ba0u,0x4de8u,0x4dc8u,0x4da8u,0x4bb0u,0x4df0u,0x4dd0u,0x4db0u,0x4b80u,0x4df8u,0x4dd8u,0x4db8u,0x4b90u,0x5000u,0x5020u,0x5040u,0x4c40u,0x5008u,0x5028u,0x5048u,0x4c50u,0x5010u,0x5030u,0x5050u,0x4c60u,0x5018u,0x5038u,0x5058u,0x4c70u,0x5080u,0x50a0u,0x50c0u,0x4cc0u,0x5088u,0x50a8u,0x50c8u,0x4cd0u,0x5090u,0x50b0u,0x50d0u,0x4ce0u,0x5098u,0x50b8u,0x50d8u,0x4cf0u,0x5120u,0x5100u,0x5160u,0x4d60u,0x5128u,0x5108u,0x5168u,0x4d70u,0x5130u,0x5110u,0x5170u,0x4d40u,0x5138u,0x5118u,0x5178u,0x4d50u,0x51a0u,0x5180u,0x51e0u,0x4de0u,0x51a8u,0x5188u,0x51e8u,0x4df0u,0x51b0u,0x5190u,0x51f0u,0x4dc0u,0x51b8u,0x5198u,0x51f8u,0x4dd0u,0x5440u,0x5460u,0x5400u,0x4e40u,0x5448u,0x5468u,0x5408u,0x4e50u,0x5450u,0x5470u,0x5410u,0x4e60u,0x5458u,0x5478u,0x5418u,0x4e70u,0x54c0u,0x54e0u,0x5480u,0x4ec0u,0x54c8u,0x54e8u,0x5488u,0x4ed0u,0x54d0u,0x54f0u,0x5490u,0x4ee0u,0x54d8u,0x54f8u,0x5498u,0x4ef0u,0x5560u,0x5540u,0x5520u,0x4f60u,0x5568u,0x5548u,0x5528u,0x4f70u,0x5570u,0x5550u,0x5530u,0x4f40u,0x5578u,0x5558u,0x5538u,0x4f50u,0x55e0u,0x55c0u,0x55a0u,0x4fe0u,0x55e8u,0x55c8u,0x55a8u,0x4ff0u,0x55f0u,0x55d0u,0x55b0u,0x4fc0u,0x55f8u,0x55d8u,0x55b8u,0x4fd0u,0x4840u,0x1e800u,0x1eb00u,0x0u,0x4848u,0x1ec90u,0x1ef90u,0x10u,0x4850u,0x1f120u,0x1f420u,0x20u,0x4858u,0x1f5b0u,0x1f8b0u,0x30u,0x48c0u,0x1fa40u,0x1fd40u,0x40u,0x48c8u,0x1fed0u,0x201d0u,0x50u,0x48d0u,0x20360u,0x20660u,0x60u,0x48d8u,0x207f0u,0x20af0u,0x70u,0x4960u,0x20c80u,0x20f80u,0x180u,0x4968u,0x21110u,0x21410u,0x190u,0x4970u,0x215a0u,0x218a0u,0x1a0u,0x4978u,0x21a30u,0x21d30u,0x1b0u,0x49e0u,0x21ec0u,0x221c0u,0x1c0u,0x49e8u,0x22350u,0x22650u,0x1d0u,0x49f0u,0x227e0u,0x22ae0u,0x1e0u,0x49f8u,0x22c70u,0x22f70u,0x1f0u,0x4a40u,0x1e830u,0x1eb08u,0x320u,0x4a48u,0x1ecc0u,0x1ef98u,0x330u,0x4a50u,0x1f150u,0x1f428u,0x300u,0x4a58u,0x1f5e0u,0x1f8b8u,0x310u,0x4ac0u,0x1fa70u,0x1fd48u,0x360u,0x4ac8u,0x1ff00u,0x201d8u,0x370u,0x4ad0u,0x20390u,0x20668u,0x340u,0x4ad8u,0x20820u,0x20af8u,0x350u,0x4b60u,0x20cb0u,0x20f88u,0x4a0u,0x4b68u,0x21140u,0x21418u,0x4b0u,0x4b70u,0x215d0u,0x218a8u,0x480u,0x4b78u,0x21a60u,0x21d38u,0x490u,0x4be0u,0x21ef0u,0x221c8u,0x4e0u,0x4be8u,0x22380u,0x22658u,0x4f0u,0x4bf0u,0x22810u,0x22ae8u,0x4c0u,0x4bf8u,0x22ca0u,0x22f78u,0x4d0u,0x4c00u,0x1e860u,0x1eb10u,0x600u,0x4c08u,0x1ecf0u,0x1efa0u,0x610u,0x4c10u,0x1f180u,0x1f430u,0x620u,0x4c18u,0x1f610u,0x1f8c0u,0x630u,0x4c80u,0x1faa0u,0x1fd50u,0x640u,0x4c88u,0x1ff30u,0x201e0u,0x650u,0x4c90u,0x203c0u,0x20670u,0x660u,0x4c98u,0x20850u,0x20b00u,0x670u,0x4d20u,0x20ce0u,0x20f90u,0x780u,0x4d28u,0x21170u,0x21420u,0x790u,0x4d30u,0x21600u,0x218b0u,0x7a0u,0x4d38u,0x21a90u,0x21d40u,0x7b0u,0x4da0u,0x21f20u,0x221d0u,0x7c0u,0x4da8u,0x223b0u,0x22660u,0x7d0u,0x4db0u,0x22840u,0x22af0u,0x7e0u,0x4db8u,0x22cd0u,0x22f80u,0x7f0u,0x4e00u,0x1e890u,0x1eb18u,0x920u,0x4e08u,0x1ed20u,0x1efa8u,0x930u,0x4e10u,0x1f1b0u,0x1f438u,0x900u,0x4e18u,0x1f640u,0x1f8c8u,0x910u,0x4e80u,0x1fad0u,0x1fd58u,0x960u,0x4e88u,0x1ff60u,0x201e8u,0x970u,0x4e90u,0x203f0u,0x20678u,0x940u,0x4e98u,0x20880u,0x20b08u,0x950u,0x4f20u,0x20d10u,0x20f98u,0xaa0u,0x4f28u,0x211a0u,0x21428u,0xab0u,0x4f30u,0x21630u,0x218b8u,0xa80u,0x4f38u,0x21ac0u,0x21d48u,0xa90u,0x4fa0u,0x21f50u,0x221d8u,0xae0u,0x4fa8u,0x223e0u,0x22668u,0xaf0u,0x4fb0u,0x22870u,0x22af8u,0xac0u,0x4fb8u,0x22d00u,0x22f88u,0xad0u,0x0u,0x40u,0x9000u,0x9000u,0x80u,0xc0u,0x9010u,0x9010u,0x110u,0x150u,0x9020u,0x9020u,0x190u,0x1d0u,0x9030u,0x9030u,0x220u,0x260u,0x9040u,0x9040u,0x2a0u,0x2e0u,0x9050u,0x9050u,0x330u,0x370u,0x9060u,0x9060u,0x3b0u,0x3f0u,0x9070u,0x9070u,0x440u,0x400u,0x9080u,0x9080u,0x4c0u,0x480u,0x9090u,0x9090u,0x550u,0x510u,0x90a0u,0x90a0u,0x5d0u,0x590u,0x90b0u,0x90b0u,0x660u,0x620u,0x90c0u,0x90c0u,0x6e0u,0x6a0u,0x90d0u,0x90d0u,0x770u,0x730u,0x90e0u,0x90e0u,0x7f0u,0x7b0u,0x90f0u,0x90f0u,0x10u,0x50u,0x9300u,0x9300u,0x90u,0xd0u,0x9310u,0x9310u,0x100u,0x140u,0x9320u,0x9320u,0x180u,0x1c0u,0x9330u,0x9330u,0x230u,0x270u,0x9340u,0x9340u,0x2b0u,0x2f0u,0x9350u,0x9350u,0x320u,0x360u,0x9360u,0x9360u,0x3a0u,0x3e0u,0x9370u,0x9370u,0x450u,0x410u,0x9380u,0x9380u,0x4d0u,0x490u,0x9390u,0x9390u,0x540u,0x500u,0x93a0u,0x93a0u,0x5c0u,0x580u,0x93b0u,0x93b0u,0x670u,0x630u,0x93c0u,0x93c0u,0x6f0u,0x6b0u,0x93d0u,0x93d0u,0x760u,0x720u,0x93e0u,0x93e0u,0x7e0u,0x7a0u,0x93f0u,0x93f0u,0x20u,0x60u,0x9600u,0x9600u,0xa0u,0xe0u,0x9610u,0x9610u,0x130u,0x170u,0x9620u,0x9620u,0x1b0u,0x1f0u,0x9630u,0x9630u,0x200u,0x240u,0x9640u,0x9640u,0x280u,0x2c0u,0x9650u,0x9650u,0x310u,0x350u,0x9660u,0x9660u,0x390u,0x3d0u,0x9670u,0x9670u,0x460u,0x420u,0x9680u,0x9680u,0x4e0u,0x4a0u,0x9690u,0x9690u,0x570u,0x530u,0x96a0u,0x96a0u,0x5f0u,0x5b0u,0x96b0u,0x96b0u,0x640u,0x600u,0x96c0u,0x96c0u,0x6c0u,0x680u,0x96d0u,0x96d0u,0x750u,0x710u,0x96e0u,0x96e0u,0x7d0u,0x790u,0x96f0u,0x96f0u,0x30u,0x70u,0x9900u,0x9900u,0xb0u,0xf0u,0x9910u,0x9910u,0x120u,0x160u,0x9920u,0x9920u,0x1a0u,0x1e0u,0x9930u,0x9930u,0x210u,0x250u,0x9940u,0x9940u,0x290u,0x2d0u,0x9950u,0x9950u,0x300u,0x340u,0x9960u,0x9960u,0x380u,0x3c0u,0x9970u,0x9970u,0x470u,0x430u,0x9980u,0x9980u,0x4f0u,0x4b0u,0x9990u,0x9990u,0x560u,0x520u,0x99a0u,0x99a0u,0x5e0u,0x5a0u,0x99b0u,0x99b0u,0x650u,0x610u,0x99c0u,0x99c0u,0x6d0u,0x690u,0x99d0u,0x99d0u,0x740u,0x700u,0x99e0u,0x99e0u,0x7c0u,0x780u,0x99f0u,0x99f0u,0x9c00u,0x12000u,0x12040u,0x12000u,0x9c10u,0x12190u,0x121d0u,0x12010u,0x9c20u,0x12320u,0x12360u,0x12020u,0x9c30u,0x124b0u,0x124f0u,0x12030u,0x9c40u,0x12640u,0x12600u,0x12040u,0x9c50u,0x127d0u,0x12790u,0x12050u,0x9c60u,0x12960u,0x12920u,0x12060u,0x9c70u,0x12af0u,0x12ab0u,0x12070u,0x9c80u,0x12c00u,0x12c40u,0x12080u,0x9c90u,0x12d90u,0x12dd0u,0x12090u,0x9ca0u,0x12f20u,0x12f60u,0x120a0u,0x9cb0u,0x130b0u,0x130f0u,0x120b0u,0x9cc0u,0x13240u,0x13200u,0x120c0u,0x9cd0u,0x133d0u,0x13390u,0x120d0u,0x9ce0u,0x13560u,0x13520u,0x120e0u,0x9cf0u,0x136f0u,0x136b0u,0x120f0u,0x9f00u,0x12010u,0x12048u,0x12100u,0x9f10u,0x12180u,0x121d8u,0x12110u,0x9f20u,0x12330u,0x12368u,0x12120u,0x9f30u,0x124a0u,0x124f8u,0x12130u,0x9f40u,0x12650u,0x12608u,0x12140u,0x9f50u,0x127c0u,0x12798u,0x12150u,0x9f60u,0x12970u,0x12928u,0x12160u,0x9f70u,0x12ae0u,0x12ab8u,0x12170u,0x9f80u,0x12c10u,0x12c48u,0x12180u,0x9f90u,0x12d80u,0x12dd8u,0x12190u,0x9fa0u,0x12f30u,0x12f68u,0x121a0u,0x9fb0u,0x130a0u,0x130f8u,0x121b0u,0x9fc0u,0x13250u,0x13208u,0x121c0u,0x9fd0u,0x133c0u,0x13398u,0x121d0u,0x9fe0u,0x13570u,0x13528u,0x121e0u,0x9ff0u,0x136e0u,0x136b8u,0x121f0u,0xa200u,0x12020u,0x12050u,0x12200u,0xa210u,0x121b0u,0x121c0u,0x12210u,0xa220u,0x12300u,0x12370u,0x12220u,0xa230u,0x12490u,0x124e0u,0x12230u,0xa240u,0x12660u,0x12610u,0x12240u,0xa250u,0x127f0u,0x12780u,0x12250u,0xa260u,0x12940u,0x12930u,0x12260u,0xa270u,0x12ad0u,0x12aa0u,0x12270u,0xa280u,0x12c20u,0x12c50u,0x12280u,0xa290u,0x12db0u,0x12dc0u,0x12290u,0xa2a0u,0x12f00u,0x12f70u,0x122a0u,0xa2b0u,0x13090u,0x130e0u,0x122b0u,0xa2c0u,0x13260u,0x13210u,0x122c0u,0xa2d0u,0x133f0u,0x13380u,0x122d0u,0xa2e0u,0x13540u,0x13530u,0x122e0u,0xa2f0u,0x136d0u,0x136a0u,0x122f0u,0xa500u,0x12030u,0x12058u,0x12300u,0xa510u,0x121a0u,0x121c8u,0x12310u,0xa520u,0x12310u,0x12378u,0x12320u,0xa530u,0x12480u,0x124e8u,0x12330u,0xa540u,0x12670u,0x12618u,0x12340u,0xa550u,0x127e0u,0x12788u,0x12350u,0xa560u,0x12950u,0x12938u,0x12360u,0xa570u,0x12ac0u,0x12aa8u,0x12370u,0xa580u,0x12c30u,0x12c58u,0x12380u,0xa590u,0x12da0u,0x12dc8u,0x12390u,0xa5a0u,0x12f10u,0x12f78u,0x123a0u,0xa5b0u,0x13080u,0x130e8u,0x123b0u,0xa5c0u,0x13270u,0x13218u,0x123c0u,0xa5d0u,0x133e0u,0x13388u,0x123d0u,0xa5e0u,0x13550u,0x13538u,0x123e0u,0xa5f0u,0x136c0u,0x136a8u,0x123f0u,0x4800u,0x1c300u,0x1e800u,0x1eb00u,0x4880u,0x1c310u,0x100000u,0x1eb02u,0x4910u,0x1c320u,0x100000u,0x1eb04u,0x4990u,0x1c330u,0x100000u,0x1eb06u,0x4a20u,0x1c340u,0x1e800u,0x1eb08u,0x4aa0u,0x1c350u,0x100000u,0x1eb0au,0x4b30u,0x1c360u,0x100000u,0x1eb0cu,0x4bb0u,0x1c370u,0x100000u,0x1eb0eu,0x4c40u,0x1c380u,0x1e800u,0x1eb10u,0x4cc0u,0x1c390u,0x100000u,0x1eb12u,0x4d50u,0x1c3a0u,0x100000u,0x1eb14u,0x4dd0u,0x1c3b0u,0x100000u,0x1eb16u,0x4e60u,0x1c3c0u,0x1e800u,0x1eb18u,0x4ee0u,0x1c3d0u,0x100000u,0x1eb1au,0x4f70u,0x1c3e0u,0x100000u,0x1eb1cu,0x4ff0u,0x1c3f0u,0x100000u,0x1eb1eu,0x4810u,0x1c400u,0x1e830u,0x1eb00u,0x4890u,0x1c410u,0x100000u,0x1eb02u,0x4900u,0x1c420u,0x100000u,0x1eb04u,0x4980u,0x1c430u,0x100000u,0x1eb06u,0x4a30u,0x1c440u,0x1e830u,0x1eb08u,0x4ab0u,0x1c450u,0x100000u,0x1eb0au,0x4b20u,0x1c460u,0x100000u,0x1eb0cu,0x4ba0u,0x1c470u,0x100000u,0x1eb0eu,0x4c50u,0x1c480u,0x1e830u,0x1eb10u,0x4cd0u,0x1c490u,0x100000u,0x1eb12u,0x4d40u,0x1c4a0u,0x100000u,0x1eb14u,0x4dc0u,0x1c4b0u,0x100000u,0x1eb16u,0x4e70u,0x1c4c0u,0x1e830u,0x1eb18u,0x4ef0u,0x1c4d0u,0x100000u,0x1eb1au,0x4f60u,0x1c4e0u,0x100000u,0x1eb1cu,0x4fe0u,0x1c4f0u,0x100000u,0x1eb1eu,0x4820u,0x1c500u,0x1e860u,0x1eb00u,0x48a0u,0x1c510u,0x100000u,0x1eb02u,0x4930u,0x1c520u,0x100000u,0x1eb04u,0x49b0u,0x1c530u,0x100000u,0x1eb06u,0x4a00u,0x1c540u,0x1e860u,0x1eb08u,0x4a80u,0x1c550u,0x100000u,0x1eb0au,0x4b10u,0x1c560u,0x100000u,0x1eb0cu,0x4b90u,0x1c570u,0x100000u,0x1eb0eu,0x4c60u,0x1c580u,0x1e860u,0x1eb10u,0x4ce0u,0x1c590u,0x100000u,0x1eb12u,0x4d70u,0x1c5a0u,0x100000u,0x1eb14u,0x4df0u,0x1c5b0u,0x100000u,0x1eb16u,0x4e40u,0x1c5c0u,0x1e860u,0x1eb18u,0x4ec0u,0x1c5d0u,0x100000u,0x1eb1au,0x4f50u,0x1c5e0u,0x100000u,0x1eb1cu,0x4fd0u,0x1c5f0u,0x100000u,0x1eb1eu,0x4830u,0x1c600u,0x1e890u,0x1eb00u,0x48b0u,0x1c610u,0x100000u,0x1eb02u,0x4920u,0x1c620u,0x100000u,0x1eb04u,0x49a0u,0x1c630u,0x100000u,0x1eb06u,0x4a10u,0x1c640u,0x1e890u,0x1eb08u,0x4a90u,0x1c650u,0x100000u,0x1eb0au,0x4b00u,0x1c660u,0x100000u,0x1eb0cu,0x4b80u,0x1c670u,0x100000u,0x1eb0eu,0x4c70u,0x1c680u,0x1e890u,0x1eb10u,0x4cf0u,0x1c690u,0x100000u,0x1eb12u,0x4d60u,0x1c6a0u,0x100000u,0x1eb14u,0x4de0u,0x1c6b0u,0x100000u,0x1eb16u,0x4e50u,0x1c6c0u,0x1e890u,0x1eb18u,0x4ed0u,0x1c6d0u,0x100000u,0x1eb1au,0x4f40u,0x1c6e0u,0x100000u,0x1eb1cu,0x4fc0u,0x1c6f0u,0x100000u,0x1eb1eu,0x24800u,0x1d300u,0x1d300u,0xc0100u,0x24802u,0x1d302u,0x100000u,0xc0110u,0x24804u,0x1d304u,0x100000u,0xc0120u,0x24806u,0x1d306u,0x100000u,0xc0130u,0x24a00u,0x1d310u,0x1d300u,0xc0140u,0x24a02u,0x1d312u,0x100000u,0xc0150u,0x24a04u,0x1d314u,0x100000u,0xc0160u,0x24a06u,0x1d316u,0x100000u,0xc0170u,0x24c00u,0x1d320u,0x1d300u,0xc0280u,0x24c02u,0x1d322u,0x100000u,0xc0290u,0x24c04u,0x1d324u,0x100000u,0xc02a0u,0x24c06u,0x1d326u,0x100000u,0xc02b0u,0x24e00u,0x1d330u,0x1d300u,0xc02c0u,0x24e02u,0x1d332u,0x100000u,0xc02d0u,0x24e04u,0x1d334u,0x100000u,0xc02e0u,0x24e06u,0x1d336u,0x100000u,0xc02f0u,0x24808u,0x1d308u,0x1d310u,0xc0410u,0x2480au,0x1d30au,0x100000u,0xc0400u,0x2480cu,0x1d30cu,0x100000u,0xc0430u,0x2480eu,0x1d30eu,0x100000u,0xc0420u,0x24a08u,0x1d318u,0x1d310u,0xc0450u,0x24a0au,0x1d31au,0x100000u,0xc0440u,0x24a0cu,0x1d31cu,0x100000u,0xc0470u,0x24a0eu,0x1d31eu,0x100000u,0xc0460u,0x24c08u,0x1d328u,0x1d310u,0xef990u,0x24c0au,0x1d32au,0x100000u,0xef980u,0x24c0cu,0x1d32cu,0x100000u,0xef9b0u,0x24c0eu,0x1d32eu,0x100000u,0xef9a0u,0x24e08u,0x1d338u,0x1d310u,0xef9d0u,0x24e0au,0x1d33au,0x100000u,0xef9c0u,0x24e0cu,0x1d33cu,0x100000u,0xef9f0u,0x24e0eu,0x1d33eu,0x100000u,0xef9e0u,0x25000u,0x1d340u,0x1d320u,0xefb20u,0x25002u,0x1d342u,0x100000u,0xefb30u,0x25004u,0x1d344u,0x100000u,0xefb00u,0x25006u,0x1d346u,0x100000u,0xefb10u,0x25200u,0x1d350u,0x1d320u,0xefb60u,0x25202u,0x1d352u,0x100000u,0xefb70u,0x25204u,0x1d354u,0x100000u,0xefb40u,0x25206u,0x1d356u,0x100000u,0xefb50u,0x25400u,0x1d360u,0x1d320u,0xefca0u,0x25402u,0x1d362u,0x100000u,0xefcb0u,0x25404u,0x1d364u,0x100000u,0xefc80u,0x25406u,0x1d366u,0x100000u,0xefc90u,0x25600u,0x1d370u,0x1d320u,0xefce0u,0x25602u,0x1d372u,0x100000u,0xefcf0u,0x25604u,0x1d374u,0x100000u,0xefcc0u,0x25606u,0x1d376u,0x100000u,0xefcd0u,0x100000u,0x1d348u,0x1d330u,0xefe30u,0x100000u,0x1d34au,0x100000u,0xefe20u,0x100000u,0x1d34cu,0x100000u,0xefe10u,0x100000u,0x1d34eu,0x100000u,0xefe00u,0x100000u,0x1d358u,0x1d330u,0xefe70u,0x100000u,0x1d35au,0x100000u,0xefe60u,0x100000u,0x1d35cu,0x100000u,0xefe50u,0x100000u,0x1d35eu,0x100000u,0xefe40u,0x100000u,0x1d368u,0x1d330u,0xeffb0u,0x100000u,0x1d36au,0x100000u,0xeffa0u,0x100000u,0x1d36cu,0x100000u,0xeff90u,0x100000u,0x1d36eu,0x100000u,0xeff80u,0x100000u,0x1d378u,0x1d330u,0xefff0u,0x100000u,0x1d37au,0x100000u,0xeffe0u,0x100000u,0x1d37cu,0x100000u,0xeffd0u,0x100000u,0x1d37eu,0x100000u,0xeffc0u,0x0u,0x30420u,0x8fc50u,0xc0000u,0x10u,0x30430u,0x8fc40u,0xc0010u,0x20u,0x30400u,0x8fc70u,0xc0020u,0x30u,0x30410u,0x8fc60u,0xc0030u,0x40u,0x30460u,0x8fc10u,0xc0040u,0x50u,0x30470u,0x8fc00u,0xc0050u,0x60u,0x30440u,0x8fc30u,0xc0060u,0x70u,0x30450u,0x8fc20u,0xc0070u,0x80u,0x304b0u,0x8fcd0u,0xc0080u,0x90u,0x304a0u,0x8fcc0u,0xc0090u,0xa0u,0x30490u,0x8fcf0u,0xc00a0u,0xb0u,0x30480u,0x8fce0u,0xc00b0u,0xc0u,0x304f0u,0x8fc90u,0xc00c0u,0xd0u,0x304e0u,0x8fc80u,0xc00d0u,0xe0u,0x304d0u,0x8fcb0u,0xc00e0u,0xf0u,0x304c0u,0x8fca0u,0xc00f0u,0x100u,0x30530u,0x8fd60u,0xc0100u,0x110u,0x30520u,0x8fd70u,0xc0110u,0x120u,0x30510u,0x8fd40u,0xc0120u,0x130u,0x30500u,0x8fd50u,0xc0130u,0x140u,0x30570u,0x8fd20u,0xc0140u,0x150u,0x30560u,0x8fd30u,0xc0150u,0x160u,0x30550u,0x8fd00u,0xc0160u,0x170u,0x30540u,0x8fd10u,0xc0170u,0x190u,0x305b0u,0x8fde0u,0xc0190u,0x180u,0x305a0u,0x8fdf0u,0xc0180u,0x1b0u,0x30590u,0x8fdc0u,0xc01b0u,0x1a0u,0x30580u,0x8fdd0u,0xc01a0u,0x1d0u,0x305f0u,0x8fda0u,0xc01d0u,0x1c0u,0x305e0u,0x8fdb0u,0xc01c0u,0x1f0u,0x305d0u,0x8fd80u,0xc01f0u,0x1e0u,0x305c0u,0x8fd90u,0xc01e0u,0x210u,0x30640u,0x8fe60u,0xc0210u,0x200u,0x30650u,0x8fe70u,0xc0200u,0x230u,0x30660u,0x8fe40u,0xc0230u,0x220u,0x30670u,0x8fe50u,0xc0220u,0x250u,0x30600u,0x8fe20u,0xc0250u,0x240u,0x30610u,0x8fe30u,0xc0240u,0x270u,0x30620u,0x8fe00u,0xc0270u,0x260u,0x30630u,0x8fe10u,0xc0260u,0x290u,0x306c0u,0x8fef0u,0xc0290u,0x280u,0x306d0u,0x8fee0u,0xc0280u,0x2b0u,0x306e0u,0x8fed0u,0xc02b0u,0x2a0u,0x306f0u,0x8fec0u,0xc02a0u,0x2d0u,0x30680u,0x8feb0u,0xc02d0u,0x2c0u,0x30690u,0x8fea0u,0xc02c0u,0x2f0u,0x306a0u,0x8fe90u,0xc02f0u,0x2e0u,0x306b0u,0x8fe80u,0xc02e0u,0x320u,0x30740u,0x8ff70u,0xc0320u,0x330u,0x30750u,0x8ff60u,0xc0330u,0x300u,0x30760u,0x8ff50u,0xc0300u,0x310u,0x30770u,0x8ff40u,0xc0310u,0x360u,0x30700u,0x8ff30u,0xc0360u,0x370u,0x30710u,0x8ff20u,0xc0370u,0x340u,0x30720u,0x8ff10u,0xc0340u,0x350u,0x30730u,0x8ff00u,0xc0350u,0x3a0u,0x307d0u,0x8fff0u,0xc03a0u,0x3b0u,0x307c0u,0x8ffe0u,0xc03b0u,0x380u,0x307f0u,0x8ffd0u,0xc0380u,0x390u,0x307e0u,0x8ffc0u,0xc0390u,0x3e0u,0x30790u,0x8ffb0u,0xc03e0u,0x3f0u,0x30780u,0x8ffa0u,0xc03f0u,0x3c0u,0x307b0u,0x8ff90u,0xc03c0u,0x3d0u,0x307a0u,0x8ff80u,0xc03d0u,0x0u,0x11f820u,0x0u,0x7030cu,0x0u,0x11f830u,0x0u,0x7030cu,0x0u,0x11f800u,0x0u,0x7030cu,0x0u,0x11f810u,0x0u,0x7030cu,0x0u,0x11f860u,0x0u,0x7030cu,0x0u,0x11f870u,0x0u,0x7030cu,0x0u,0x11f840u,0x0u,0x7030cu,0x0u,0x11f850u,0x0u,0x7030cu,0x0u,0x11f8b0u,0x0u,0x7230cu,0x0u,0x11f8a0u,0x0u,0x7230cu,0x0u,0x11f890u,0x0u,0x7230cu,0x0u,0x11f880u,0x0u,0x7230cu,0x0u,0x11f8f0u,0x0u,0x7230cu,0x0u,0x11f8e0u,0x0u,0x7230cu,0x0u,0x11f8d0u,0x0u,0x7230cu,0x0u,0x11f8c0u,0x0u,0x7230cu,0x0u,0x11f930u,0x0u,0x7232cu,0x0u,0x11f920u,0x0u,0x7232cu,0x0u,0x11f910u,0x0u,0x7232cu,0x0u,0x11f900u,0x0u,0x7232cu,0x0u,0x11f970u,0x0u,0x7232cu,0x0u,0x11f960u,0x0u,0x7232cu,0x0u,0x11f950u,0x0u,0x7232cu,0x0u,0x11f940u,0x0u,0x7232cu,0x0u,0x11f9b0u,0x0u,0x72124u,0x0u,0x11f9a0u,0x0u,0x72124u,0x0u,0x11f990u,0x0u,0x72124u,0x0u,0x11f980u,0x0u,0x72124u,0x0u,0x11f9f0u,0x0u,0x72124u,0x0u,0x11f9e0u,0x0u,0x72124u,0x0u,0x11f9d0u,0x0u,0x72124u,0x0u,0x11f9c0u,0x0u,0x72124u,0x0u,0x11fa40u,0x0u,0x721a6u,0x0u,0x11fa50u,0x0u,0x721a6u,0x0u,0x11fa60u,0x0u,0x721a6u,0x0u,0x11fa70u,0x0u,0x721a6u,0x0u,0x11fa00u,0x0u,0x721a6u,0x0u,0x11fa10u,0x0u,0x721a6u,0x0u,0x11fa20u,0x0u,0x721a6u,0x0u,0x11fa30u,0x0u,0x721a6u,0x0u,0x11fac0u,0x0u,0x72186u,0x0u,0x11fad0u,0x0u,0x72186u,0x0u,0x11fae0u,0x0u,0x72186u,0x0u,0x11faf0u,0x0u,0x72186u,0x0u,0x11fa80u,0x0u,0x72186u,0x0u,0x11fa90u,0x0u,0x72186u,0x0u,0x11faa0u,0x0u,0x72186u,0x0u,0x11fab0u,0x0u,0x72186u,0x0u,0x11fb40u,0x0u,0x72186u,0x0u,0x11fb50u,0x0u,0x72186u,0x0u,0x11fb60u,0x0u,0x72186u,0x0u,0x11fb70u,0x0u,0x72186u,0x0u,0x11fb00u,0x0u,0x72186u,0x0u,0x11fb10u,0x0u,0x72186u,0x0u,0x11fb20u,0x0u,0x72186u,0x0u,0x11fb30u,0x0u,0x72186u,0x0u,0x11fbd0u,0x0u,0x7230cu,0x0u,0x11fbc0u,0x0u,0x7230cu,0x0u,0x11fbf0u,0x0u,0x7230cu,0x0u,0x11fbe0u,0x0u,0x7230cu,0x0u,0x11fb90u,0x0u,0x7230cu,0x0u,0x11fb80u,0x0u,0x7230cu,0x0u,0x11fbb0u,0x0u,0x7230cu,0x0u,0x11fba0u,0x0u,0x7230cu,0x5800u,0x5820u,0x4860u,0x4860u,0x5808u,0x5828u,0x4868u,0x4870u,0x5810u,0x5830u,0x4870u,0x5800u,0x5818u,0x5838u,0x4878u,0x5810u,0x5880u,0x58a0u,0x48e0u,0x48e0u,0x5888u,0x58a8u,0x48e8u,0x48f0u,0x5890u,0x58b0u,0x48f0u,0x5880u,0x5898u,0x58b8u,0x48f8u,0x5890u,0x5920u,0x5900u,0x4940u,0x4940u,0x5928u,0x5908u,0x4948u,0x4950u,0x5930u,0x5910u,0x4950u,0x5920u,0x5938u,0x5918u,0x4958u,0x5930u,0x59a0u,0x5980u,0x49c0u,0x49c0u,0x59a8u,0x5988u,0x49c8u,0x49d0u,0x59b0u,0x5990u,0x49d0u,0x59a0u,0x59b8u,0x5998u,0x49d8u,0x59b0u,0x5c40u,0x5c60u,0x4c20u,0x4a60u,0x5c48u,0x5c68u,0x4c28u,0x4a70u,0x5c50u,0x5c70u,0x4c30u,0x5a00u,0x5c58u,0x5c78u,0x4c38u,0x5a10u,0x5cc0u,0x5ce0u,0x4ca0u,0x4ae0u,0x5cc8u,0x5ce8u,0x4ca8u,0x4af0u,0x5cd0u,0x5cf0u,0x4cb0u,0x5a80u,0x5cd8u,0x5cf8u,0x4cb8u,0x5a90u,0x5d60u,0x5d40u,0x4d00u,0x4b40u,0x5d68u,0x5d48u,0x4d08u,0x4b50u,0x5d70u,0x5d50u,0x4d10u,0x5b20u,0x5d78u,0x5d58u,0x4d18u,0x5b30u,0x5de0u,0x5dc0u,0x4d80u,0x4bc0u,0x5de8u,0x5dc8u,0x4d88u,0x4bd0u,0x5df0u,0x5dd0u,0x4d90u,0x5ba0u,0x5df8u,0x5dd8u,0x4d98u,0x5bb0u,0x6000u,0x6020u,0x5060u,0x4c20u,0x6008u,0x6028u,0x5068u,0x4c30u,0x6010u,0x6030u,0x5070u,0x5c40u,0x6018u,0x6038u,0x5078u,0x5c50u,0x6080u,0x60a0u,0x50e0u,0x4ca0u,0x6088u,0x60a8u,0x50e8u,0x4cb0u,0x6090u,0x60b0u,0x50f0u,0x5cc0u,0x6098u,0x60b8u,0x50f8u,0x5cd0u,0x6120u,0x6100u,0x5140u,0x4d00u,0x6128u,0x6108u,0x5148u,0x4d10u,0x6130u,0x6110u,0x5150u,0x5d60u,0x6138u,0x6118u,0x5158u,0x5d70u,0x61a0u,0x6180u,0x51c0u,0x4d80u,0x61a8u,0x6188u,0x51c8u,0x4d90u,0x61b0u,0x6190u,0x51d0u,0x5de0u,0x61b8u,0x6198u,0x51d8u,0x5df0u,0x6440u,0x6460u,0x5420u,0x4e20u,0x6448u,0x6468u,0x5428u,0x4e30u,0x6450u,0x6470u,0x5430u,0x5e40u,0x6458u,0x6478u,0x5438u,0x5e50u,0x64c0u,0x64e0u,0x54a0u,0x4ea0u,0x64c8u,0x64e8u,0x54a8u,0x4eb0u,0x64d0u,0x64f0u,0x54b0u,0x5ec0u,0x64d8u,0x64f8u,0x54b8u,0x5ed0u,0x6560u,0x6540u,0x5500u,0x4f00u,0x6568u,0x6548u,0x5508u,0x4f10u,0x6570u,0x6550u,0x5510u,0x5f60u,0x6578u,0x6558u,0x5518u,0x5f70u,0x65e0u,0x65c0u,0x5580u,0x4f80u,0x65e8u,0x65c8u,0x5588u,0x4f90u,0x65f0u,0x65d0u,0x5590u,0x5fe0u,0x65f8u,0x65d8u,0x5598u,0x5ff0u,0x5820u,0x1e8c0u,0x1eb60u,0xc40u,0x5828u,0x1ed50u,0x1eff0u,0xc50u,0x5830u,0x1f1e0u,0x1f480u,0xc60u,0x5838u,0x1f670u,0x1f910u,0xc70u,0x58a0u,0x1fb00u,0x1fda0u,0xc00u,0x58a8u,0x1ff90u,0x20230u,0xc10u,0x58b0u,0x20420u,0x206c0u,0xc20u,0x58b8u,0x208b0u,0x20b50u,0xc30u,0x5900u,0x20d40u,0x20fe0u,0xdc0u,0x5908u,0x211d0u,0x21470u,0xdd0u,0x5910u,0x21660u,0x21900u,0xde0u,0x5918u,0x21af0u,0x21d90u,0xdf0u,0x5980u,0x21f80u,0x22220u,0xd80u,0x5988u,0x22410u,0x226b0u,0xd90u,0x5990u,0x228a0u,0x22b40u,0xda0u,0x5998u,0x22d30u,0x22fd0u,0xdb0u,0x5a20u,0x1e8f0u,0x1eb68u,0xf60u,0x5a28u,0x1ed80u,0x1eff8u,0xf70u,0x5a30u,0x1f210u,0x1f488u,0xf40u,0x5a38u,0x1f6a0u,0x1f918u,0xf50u,0x5aa0u,0x1fb30u,0x1fda8u,0xf20u,0x5aa8u,0x1ffc0u,0x20238u,0xf30u,0x5ab0u,0x20450u,0x206c8u,0xf00u,0x5ab8u,0x208e0u,0x20b58u,0xf10u,0x5b00u,0x20d70u,0x20fe8u,0x10e0u,0x5b08u,0x21200u,0x21478u,0x10f0u,0x5b10u,0x21690u,0x21908u,0x10c0u,0x5b18u,0x21b20u,0x21d98u,0x10d0u,0x5b80u,0x21fb0u,0x22228u,0x10a0u,0x5b88u,0x22440u,0x226b8u,0x10b0u,0x5b90u,0x228d0u,0x22b48u,0x1080u,0x5b98u,0x22d60u,0x22fd8u,0x1090u,0x5c60u,0x1e920u,0x1eb70u,0x1240u,0x5c68u,0x1edb0u,0x1f000u,0x1250u,0x5c70u,0x1f240u,0x1f490u,0x1260u,0x5c78u,0x1f6d0u,0x1f920u,0x1270u,0x5ce0u,0x1fb60u,0x1fdb0u,0x1200u,0x5ce8u,0x1fff0u,0x20240u,0x1210u,0x5cf0u,0x20480u,0x206d0u,0x1220u,0x5cf8u,0x20910u,0x20b60u,0x1230u,0x5d40u,0x20da0u,0x20ff0u,0x13c0u,0x5d48u,0x21230u,0x21480u,0x13d0u,0x5d50u,0x216c0u,0x21910u,0x13e0u,0x5d58u,0x21b50u,0x21da0u,0x13f0u,0x5dc0u,0x21fe0u,0x22230u,0x1380u,0x5dc8u,0x22470u,0x226c0u,0x1390u,0x5dd0u,0x22900u,0x22b50u,0x13a0u,0x5dd8u,0x22d90u,0x22fe0u,0x13b0u,0x5e60u,0x1e950u,0x1eb78u,0x1560u,0x5e68u,0x1ede0u,0x1f008u,0x1570u,0x5e70u,0x1f270u,0x1f498u,0x1540u,0x5e78u,0x1f700u,0x1f928u,0x1550u,0x5ee0u,0x1fb90u,0x1fdb8u,0x1520u,0x5ee8u,0x20020u,0x20248u,0x1530u,0x5ef0u,0x204b0u,0x206d8u,0x1500u,0x5ef8u,0x20940u,0x20b68u,0x1510u,0x5f40u,0x20dd0u,0x20ff8u,0x16e0u,0x5f48u,0x21260u,0x21488u,0x16f0u,0x5f50u,0x216f0u,0x21918u,0x16c0u,0x5f58u,0x21b80u,0x21da8u,0x16d0u,0x5fc0u,0x22010u,0x22238u,0x16a0u,0x5fc8u,0x224a0u,0x226c8u,0x16b0u,0x5fd0u,0x22930u,0x22b58u,0x1680u,0x5fd8u,0x22dc0u,0x22fe8u,0x1690u,0x0u,0x40u,0x9000u,0xa800u,0x80u,0xc0u,0x9010u,0xa810u,0x110u,0x150u,0x9020u,0xa820u,0x190u,0x1d0u,0x9030u,0xa830u,0x220u,0x260u,0x9040u,0xa840u,0x2a0u,0x2e0u,0x9050u,0xa850u,0x330u,0x370u,0x9060u,0xa860u,0x3b0u,0x3f0u,0x9070u,0xa870u,0x440u,0x400u,0x9080u,0xa880u,0x4c0u,0x480u,0x9090u,0xa890u,0x550u,0x510u,0x90a0u,0xa8a0u,0x5d0u,0x590u,0x90b0u,0xa8b0u,0x660u,0x620u,0x90c0u,0xa8c0u,0x6e0u,0x6a0u,0x90d0u,0xa8d0u,0x770u,0x730u,0x90e0u,0xa8e0u,0x7f0u,0x7b0u,0x90f0u,0xa8f0u,0x10u,0x50u,0x9300u,0xab00u,0x90u,0xd0u,0x9310u,0xab10u,0x100u,0x140u,0x9320u,0xab20u,0x180u,0x1c0u,0x9330u,0xab30u,0x230u,0x270u,0x9340u,0xab40u,0x2b0u,0x2f0u,0x9350u,0xab50u,0x320u,0x360u,0x9360u,0xab60u,0x3a0u,0x3e0u,0x9370u,0xab70u,0x450u,0x410u,0x9380u,0xab80u,0x4d0u,0x490u,0x9390u,0xab90u,0x540u,0x500u,0x93a0u,0xaba0u,0x5c0u,0x580u,0x93b0u,0xabb0u,0x670u,0x630u,0x93c0u,0xabc0u,0x6f0u,0x6b0u,0x93d0u,0xabd0u,0x760u,0x720u,0x93e0u,0xabe0u,0x7e0u,0x7a0u,0x93f0u,0xabf0u,0x20u,0x60u,0x9600u,0xae00u,0xa0u,0xe0u,0x9610u,0xae10u,0x130u,0x170u,0x9620u,0xae20u,0x1b0u,0x1f0u,0x9630u,0xae30u,0x200u,0x240u,0x9640u,0xae40u,0x280u,0x2c0u,0x9650u,0xae50u,0x310u,0x350u,0x9660u,0xae60u,0x390u,0x3d0u,0x9670u,0xae70u,0x460u,0x420u,0x9680u,0xae80u,0x4e0u,0x4a0u,0x9690u,0xae90u,0x570u,0x530u,0x96a0u,0xaea0u,0x5f0u,0x5b0u,0x96b0u,0xaeb0u,0x640u,0x600u,0x96c0u,0xaec0u,0x6c0u,0x680u,0x96d0u,0xaed0u,0x750u,0x710u,0x96e0u,0xaee0u,0x7d0u,0x790u,0x96f0u,0xaef0u,0x30u,0x70u,0x9900u,0xb100u,0xb0u,0xf0u,0x9910u,0xb110u,0x120u,0x160u,0x9920u,0xb120u,0x1a0u,0x1e0u,0x9930u,0xb130u,0x210u,0x250u,0x9940u,0xb140u,0x290u,0x2d0u,0x9950u,0xb150u,0x300u,0x340u,0x9960u,0xb160u,0x380u,0x3c0u,0x9970u,0xb170u,0x470u,0x430u,0x9980u,0xb180u,0x4f0u,0x4b0u,0x9990u,0xb190u,0x560u,0x520u,0x99a0u,0xb1a0u,0x5e0u,0x5a0u,0x99b0u,0xb1b0u,0x650u,0x610u,0x99c0u,0xb1c0u,0x6d0u,0x690u,0x99d0u,0xb1d0u,0x740u,0x700u,0x99e0u,0xb1e0u,0x7c0u,0x780u,0x99f0u,0xb1f0u,0x9c08u,0x12060u,0x120a0u,0x12400u,0x9c18u,0x121f0u,0x12230u,0x12410u,0x9c28u,0x12340u,0x12380u,0x12420u,0x9c38u,0x124d0u,0x12510u,0x12430u,0x9c48u,0x12620u,0x126e0u,0x12440u,0x9c58u,0x127b0u,0x12870u,0x12450u,0x9c68u,0x12900u,0x129c0u,0x12460u,0x9c78u,0x12a90u,0x12b50u,0x12470u,0x9c88u,0x12c60u,0x12ca0u,0x12480u,0x9c98u,0x12df0u,0x12e30u,0x12490u,0x9ca8u,0x12f40u,0x12f80u,0x124a0u,0x9cb8u,0x130d0u,0x13110u,0x124b0u,0x9cc8u,0x13220u,0x132e0u,0x124c0u,0x9cd8u,0x133b0u,0x13470u,0x124d0u,0x9ce8u,0x13500u,0x135c0u,0x124e0u,0x9cf8u,0x13690u,0x13750u,0x124f0u,0x9f08u,0x12070u,0x120a8u,0x12500u,0x9f18u,0x121e0u,0x12238u,0x12510u,0x9f28u,0x12350u,0x12388u,0x12520u,0x9f38u,0x124c0u,0x12518u,0x12530u,0x9f48u,0x12630u,0x126e8u,0x12540u,0x9f58u,0x127a0u,0x12878u,0x12550u,0x9f68u,0x12910u,0x129c8u,0x12560u,0x9f78u,0x12a80u,0x12b58u,0x12570u,0x9f88u,0x12c70u,0x12ca8u,0x12580u,0x9f98u,0x12de0u,0x12e38u,0x12590u,0x9fa8u,0x12f50u,0x12f88u,0x125a0u,0x9fb8u,0x130c0u,0x13118u,0x125b0u,0x9fc8u,0x13230u,0x132e8u,0x125c0u,0x9fd8u,0x133a0u,0x13478u,0x125d0u,0x9fe8u,0x13510u,0x135c8u,0x125e0u,0x9ff8u,0x13680u,0x13758u,0x125f0u,0xa208u,0x12080u,0x120b0u,0x12600u,0xa218u,0x12210u,0x12220u,0x12610u,0xa228u,0x123a0u,0x12390u,0x12620u,0xa238u,0x12530u,0x12500u,0x12630u,0xa248u,0x126c0u,0x126f0u,0x12640u,0xa258u,0x12850u,0x12860u,0x12650u,0xa268u,0x129e0u,0x129d0u,0x12660u,0xa278u,0x12b70u,0x12b40u,0x12670u,0xa288u,0x12c80u,0x12cb0u,0x12680u,0xa298u,0x12e10u,0x12e20u,0x12690u,0xa2a8u,0x12fa0u,0x12f90u,0x126a0u,0xa2b8u,0x13130u,0x13100u,0x126b0u,0xa2c8u,0x132c0u,0x132f0u,0x126c0u,0xa2d8u,0x13450u,0x13460u,0x126d0u,0xa2e8u,0x135e0u,0x135d0u,0x126e0u,0xa2f8u,0x13770u,0x13740u,0x126f0u,0xa508u,0x12090u,0x120b8u,0x12700u,0xa518u,0x12200u,0x12228u,0x12710u,0xa528u,0x123b0u,0x12398u,0x12720u,0xa538u,0x12520u,0x12508u,0x12730u,0xa548u,0x126d0u,0x126f8u,0x12740u,0xa558u,0x12840u,0x12868u,0x12750u,0xa568u,0x129f0u,0x129d8u,0x12760u,0xa578u,0x12b60u,0x12b48u,0x12770u,0xa588u,0x12c90u,0x12cb8u,0x12780u,0xa598u,0x12e00u,0x12e28u,0x12790u,0xa5a8u,0x12fb0u,0x12f98u,0x127a0u,0xa5b8u,0x13120u,0x13108u,0x127b0u,0xa5c8u,0x132d0u,0x132f8u,0x127c0u,0xa5d8u,0x13440u,0x13468u,0x127d0u,0xa5e8u,0x135f0u,0x135d8u,0x127e0u,0xa5f8u,0x13760u,0x13748u,0x127f0u,0x5000u,0x1c300u,0x1e8c0u,0x1eb60u,0x5080u,0x1c310u,0x100000u,0x1eb62u,0x5110u,0x1c320u,0x100000u,0x1eb64u,0x5190u,0x1c330u,0x100000u,0x1eb66u,0x5220u,0x1c340u,0x1e8c0u,0x1eb68u,0x52a0u,0x1c350u,0x100000u,0x1eb6au,0x5330u,0x1c360u,0x100000u,0x1eb6cu,0x53b0u,0x1c370u,0x100000u,0x1eb6eu,0x5440u,0x1c380u,0x1e8c0u,0x1eb70u,0x54c0u,0x1c390u,0x100000u,0x1eb72u,0x5550u,0x1c3a0u,0x100000u,0x1eb74u,0x55d0u,0x1c3b0u,0x100000u,0x1eb76u,0x5660u,0x1c3c0u,0x1e8c0u,0x1eb78u,0x56e0u,0x1c3d0u,0x100000u,0x1eb7au,0x5770u,0x1c3e0u,0x100000u,0x1eb7cu,0x57f0u,0x1c3f0u,0x100000u,0x1eb7eu,0x5010u,0x1c400u,0x1e8f0u,0x1eb60u,0x5090u,0x1c410u,0x100000u,0x1eb62u,0x5100u,0x1c420u,0x100000u,0x1eb64u,0x5180u,0x1c430u,0x100000u,0x1eb66u,0x5230u,0x1c440u,0x1e8f0u,0x1eb68u,0x52b0u,0x1c450u,0x100000u,0x1eb6au,0x5320u,0x1c460u,0x100000u,0x1eb6cu,0x53a0u,0x1c470u,0x100000u,0x1eb6eu,0x5450u,0x1c480u,0x1e8f0u,0x1eb70u,0x54d0u,0x1c490u,0x100000u,0x1eb72u,0x5540u,0x1c4a0u,0x100000u,0x1eb74u,0x55c0u,0x1c4b0u,0x100000u,0x1eb76u,0x5670u,0x1c4c0u,0x1e8f0u,0x1eb78u,0x56f0u,0x1c4d0u,0x100000u,0x1eb7au,0x5760u,0x1c4e0u,0x100000u,0x1eb7cu,0x57e0u,0x1c4f0u,0x100000u,0x1eb7eu,0x5020u,0x1c500u,0x1e920u,0x1eb60u,0x50a0u,0x1c510u,0x100000u,0x1eb62u,0x5130u,0x1c520u,0x100000u,0x1eb64u,0x51b0u,0x1c530u,0x100000u,0x1eb66u,0x5200u,0x1c540u,0x1e920u,0x1eb68u,0x5280u,0x1c550u,0x100000u,0x1eb6au,0x5310u,0x1c560u,0x100000u,0x1eb6cu,0x5390u,0x1c570u,0x100000u,0x1eb6eu,0x5460u,0x1c580u,0x1e920u,0x1eb70u,0x54e0u,0x1c590u,0x100000u,0x1eb72u,0x5570u,0x1c5a0u,0x100000u,0x1eb74u,0x55f0u,0x1c5b0u,0x100000u,0x1eb76u,0x5640u,0x1c5c0u,0x1e920u,0x1eb78u,0x56c0u,0x1c5d0u,0x100000u,0x1eb7au,0x5750u,0x1c5e0u,0x100000u,0x1eb7cu,0x57d0u,0x1c5f0u,0x100000u,0x1eb7eu,0x5030u,0x1c600u,0x1e950u,0x1eb60u,0x50b0u,0x1c610u,0x100000u,0x1eb62u,0x5120u,0x1c620u,0x100000u,0x1eb64u,0x51a0u,0x1c630u,0x100000u,0x1eb66u,0x5210u,0x1c640u,0x1e950u,0x1eb68u,0x5290u,0x1c650u,0x100000u,0x1eb6au,0x5300u,0x1c660u,0x100000u,0x1eb6cu,0x5380u,0x1c670u,0x100000u,0x1eb6eu,0x5470u,0x1c680u,0x1e950u,0x1eb70u,0x54f0u,0x1c690u,0x100000u,0x1eb72u,0x5560u,0x1c6a0u,0x100000u,0x1eb74u,0x55e0u,0x1c6b0u,0x100000u,0x1eb76u,0x5650u,0x1c6c0u,0x1e950u,0x1eb78u,0x56d0u,0x1c6d0u,0x100000u,0x1eb7au,0x5740u,0x1c6e0u,0x100000u,0x1eb7cu,0x57c0u,0x1c6f0u,0x100000u,0x1eb7eu,0x25800u,0x1d380u,0x1d380u,0xf0140u,0x25802u,0x1d382u,0x100000u,0xf0150u,0x25804u,0x1d384u,0x100000u,0xf0160u,0x25806u,0x1d386u,0x100000u,0xf0170u,0x25a00u,0x1d390u,0x1d380u,0xf0100u,0x25a02u,0x1d392u,0x100000u,0xf0110u,0x25a04u,0x1d394u,0x100000u,0xf0120u,0x25a06u,0x1d396u,0x100000u,0xf0130u,0x25c00u,0x1d3a0u,0x1d380u,0xf02c0u,0x25c02u,0x1d3a2u,0x100000u,0xf02d0u,0x25c04u,0x1d3a4u,0x100000u,0xf02e0u,0x25c06u,0x1d3a6u,0x100000u,0xf02f0u,0x25e00u,0x1d3b0u,0x1d380u,0xf0280u,0x25e02u,0x1d3b2u,0x100000u,0xf0290u,0x25e04u,0x1d3b4u,0x100000u,0xf02a0u,0x25e06u,0x1d3b6u,0x100000u,0xf02b0u,0x25808u,0x1d388u,0x1d390u,0x11f850u,0x2580au,0x1d38au,0x100000u,0x11f840u,0x2580cu,0x1d38cu,0x100000u,0x11f870u,0x2580eu,0x1d38eu,0x100000u,0x11f860u,0x25a08u,0x1d398u,0x1d390u,0x11f810u,0x25a0au,0x1d39au,0x100000u,0x11f800u,0x25a0cu,0x1d39cu,0x100000u,0x11f830u,0x25a0eu,0x1d39eu,0x100000u,0x11f820u,0x25c08u,0x1d3a8u,0x1d390u,0x11f9d0u,0x25c0au,0x1d3aau,0x100000u,0x11f9c0u,0x25c0cu,0x1d3acu,0x100000u,0x11f9f0u,0x25c0eu,0x1d3aeu,0x100000u,0x11f9e0u,0x25e08u,0x1d3b8u,0x1d390u,0x11f990u,0x25e0au,0x1d3bau,0x100000u,0x11f980u,0x25e0cu,0x1d3bcu,0x100000u,0x11f9b0u,0x25e0eu,0x1d3beu,0x100000u,0x11f9a0u,0x25008u,0x1d3c0u,0x1d3a0u,0x11fb60u,0x2500au,0x1d3c2u,0x100000u,0x11fb70u,0x2500cu,0x1d3c4u,0x100000u,0x11fb40u,0x2500eu,0x1d3c6u,0x100000u,0x11fb50u,0x25208u,0x1d3d0u,0x1d3a0u,0x11fb20u,0x2520au,0x1d3d2u,0x100000u,0x11fb30u,0x2520cu,0x1d3d4u,0x100000u,0x11fb00u,0x2520eu,0x1d3d6u,0x100000u,0x11fb10u,0x25408u,0x1d3e0u,0x1d3a0u,0x11fce0u,0x2540au,0x1d3e2u,0x100000u,0x11fcf0u,0x2540cu,0x1d3e4u,0x100000u,0x11fcc0u,0x2540eu,0x1d3e6u,0x100000u,0x11fcd0u,0x25608u,0x1d3f0u,0x1d3a0u,0x11fca0u,0x2560au,0x1d3f2u,0x100000u,0x11fcb0u,0x2560cu,0x1d3f4u,0x100000u,0x11fc80u,0x2560eu,0x1d3f6u,0x100000u,0x11fc90u,0x100000u,0x1d3c8u,0x1d3b0u,0x11fe70u,0x100000u,0x1d3cau,0x100000u,0x11fe60u,0x100000u,0x1d3ccu,0x100000u,0x11fe50u,0x100000u,0x1d3ceu,0x100000u,0x11fe40u,0x100000u,0x1d3d8u,0x1d3b0u,0x11fe30u,0x100000u,0x1d3dau,0x100000u,0x11fe20u,0x100000u,0x1d3dcu,0x100000u,0x11fe10u,0x100000u,0x1d3deu,0x100000u,0x11fe00u,0x100000u,0x1d3e8u,0x1d3b0u,0x11fff0u,0x100000u,0x1d3eau,0x100000u,0x11ffe0u,0x100000u,0x1d3ecu,0x100000u,0x11ffd0u,0x100000u,0x1d3eeu,0x100000u,0x11ffc0u,0x100000u,0x1d3f8u,0x1d3b0u,0x11ffb0u,0x100000u,0x1d3fau,0x100000u,0x11ffa0u,0x100000u,0x1d3fcu,0x100000u,0x11ff90u,0x100000u,0x1d3feu,0x100000u,0x11ff80u,0x420u,0x30850u,0x90000u,0xc0420u,0x430u,0x30840u,0x90010u,0xc0430u,0x400u,0x30870u,0x90020u,0xc0400u,0x410u,0x30860u,0x90030u,0xc0410u,0x460u,0x30810u,0x90040u,0xc0460u,0x470u,0x30800u,0x90050u,0xc0470u,0x440u,0x30830u,0x90060u,0xc0440u,0x450u,0x30820u,0x90070u,0xc0450u,0x4b0u,0x308d0u,0x90080u,0xef8b0u,0x4a0u,0x308c0u,0x90090u,0xef8a0u,0x490u,0x308f0u,0x900a0u,0xef890u,0x480u,0x308e0u,0x900b0u,0xef880u,0x4f0u,0x30890u,0x900c0u,0xef8f0u,0x4e0u,0x30880u,0x900d0u,0xef8e0u,0x4d0u,0x308b0u,0x900e0u,0xef8d0u,0x4c0u,0x308a0u,0x900f0u,0xef8c0u,0x530u,0x5fd60u,0x90100u,0xef930u,0x520u,0x5fd70u,0x90110u,0xef920u,0x510u,0x5fd40u,0x90120u,0xef910u,0x500u,0x5fd50u,0x90130u,0xef900u,0x570u,0x5fd20u,0x90140u,0xef970u,0x560u,0x5fd30u,0x90150u,0xef960u,0x550u,0x5fd00u,0x90160u,0xef950u,0x540u,0x5fd10u,0x90170u,0xef940u,0x5b0u,0x5fde0u,0x90190u,0xef9b0u,0x5a0u,0x5fdf0u,0x90180u,0xef9a0u,0x590u,0x5fdc0u,0x901b0u,0xef990u,0x580u,0x5fdd0u,0x901a0u,0xef980u,0x5f0u,0x5fda0u,0x901d0u,0xef9f0u,0x5e0u,0x5fdb0u,0x901c0u,0xef9e0u,0x5d0u,0x5fd80u,0x901f0u,0xef9d0u,0x5c0u,0x5fd90u,0x901e0u,0xef9c0u,0x640u,0x5fe60u,0x90210u,0xefa40u,0x650u,0x5fe70u,0x90200u,0xefa50u,0x660u,0x5fe40u,0x90230u,0xefa60u,0x670u,0x5fe50u,0x90220u,0xefa70u,0x600u,0x5fe20u,0x90250u,0xefa00u,0x610u,0x5fe30u,0x90240u,0xefa10u,0x620u,0x5fe00u,0x90270u,0xefa20u,0x630u,0x5fe10u,0x90260u,0xefa30u,0x6c0u,0x5fef0u,0x90290u,0xefac0u,0x6d0u,0x5fee0u,0x90280u,0xefad0u,0x6e0u,0x5fed0u,0x902b0u,0xefae0u,0x6f0u,0x5fec0u,0x902a0u,0xefaf0u,0x680u,0x5feb0u,0x902d0u,0xefa80u,0x690u,0x5fea0u,0x902c0u,0xefa90u,0x6a0u,0x5fe90u,0x902f0u,0xefaa0u,0x6b0u,0x5fe80u,0x902e0u,0xefab0u,0x740u,0x5ff70u,0x90320u,0xefb40u,0x750u,0x5ff60u,0x90330u,0xefb50u,0x760u,0x5ff50u,0x90300u,0xefb60u,0x770u,0x5ff40u,0x90310u,0xefb70u,0x700u,0x5ff30u,0x90360u,0xefb00u,0x710u,0x5ff20u,0x90370u,0xefb10u,0x720u,0x5ff10u,0x90340u,0xefb20u,0x730u,0x5ff00u,0x90350u,0xefb30u,0x7d0u,0x5fff0u,0x903a0u,0xefbd0u,0x7c0u,0x5ffe0u,0x903b0u,0xefbc0u,0x7f0u,0x5ffd0u,0x90380u,0xefbf0u,0x7e0u,0x5ffc0u,0x90390u,0xefbe0u,0x790u,0x5ffb0u,0x903e0u,0xefb90u,0x780u,0x5ffa0u,0x903f0u,0xefb80u,0x7b0u,0x5ff90u,0x903c0u,0xefbb0u,0x7a0u,0x5ff80u,0x903d0u,0xefba0u,0x0u,0x11fc50u,0x0u,0x7230cu,0x0u,0x11fc40u,0x0u,0x7230cu,0x0u,0x11fc70u,0x0u,0x7230cu,0x0u,0x11fc60u,0x0u,0x7230cu,0x0u,0x11fc10u,0x0u,0x7230cu,0x0u,0x11fc00u,0x0u,0x7230cu,0x0u,0x11fc30u,0x0u,0x7230cu,0x0u,0x11fc20u,0x0u,0x7230cu,0x0u,0x11fcd0u,0x0u,0x5232cu,0x0u,0x11fcc0u,0x0u,0x5232cu,0x0u,0x11fcf0u,0x0u,0x5232cu,0x0u,0x11fce0u,0x0u,0x5232cu,0x0u,0x11fc90u,0x0u,0x5232cu,0x0u,0x11fc80u,0x0u,0x5232cu,0x0u,0x11fcb0u,0x0u,0x5232cu,0x0u,0x11fca0u,0x0u,0x5232cu,0x0u,0x11fd60u,0x0u,0xd0124u,0x0u,0x11fd70u,0x0u,0xd0124u,0x0u,0x11fd40u,0x0u,0xd0124u,0x0u,0x11fd50u,0x0u,0xd0124u,0x0u,0x11fd20u,0x0u,0xd0124u,0x0u,0x11fd30u,0x0u,0xd0124u,0x0u,0x11fd00u,0x0u,0xd0124u,0x0u,0x11fd10u,0x0u,0xd0124u,0x0u,0x11fde0u,0x0u,0xd81a6u,0x0u,0x11fdf0u,0x0u,0xd81a6u,0x0u,0x11fdc0u,0x0u,0xd81a6u,0x0u,0x11fdd0u,0x0u,0xd81a6u,0x0u,0x11fda0u,0x0u,0xd81a6u,0x0u,0x11fdb0u,0x0u,0xd81a6u,0x0u,0x11fd80u,0x0u,0xd81a6u,0x0u,0x11fd90u,0x0u,0xd81a6u,0x0u,0x11fe60u,0x0u,0xd8996u,0x0u,0x11fe70u,0x0u,0xd8996u,0x0u,0x11fe40u,0x0u,0xd8996u,0x0u,0x11fe50u,0x0u,0xd8996u,0x0u,0x11fe20u,0x0u,0xd8996u,0x0u,0x11fe30u,0x0u,0xd8996u,0x0u,0x11fe00u,0x0u,0xd8996u,0x0u,0x11fe10u,0x0u,0xd8996u,0x0u,0x11fef0u,0x0u,0xd8996u,0x0u,0x11fee0u,0x0u,0xd8996u,0x0u,0x11fed0u,0x0u,0xd8996u,0x0u,0x11fec0u,0x0u,0xd8996u,0x0u,0x11feb0u,0x0u,0xd8996u,0x0u,0x11fea0u,0x0u,0xd8996u,0x0u,0x11fe90u,0x0u,0xd8996u,0x0u,0x11fe80u,0x0u,0xd8996u,0x0u,0x11ff70u,0x0u,0xd8b1cu,0x0u,0x11ff60u,0x0u,0xd8b1cu,0x0u,0x11ff50u,0x0u,0xd8b1cu,0x0u,0x11ff40u,0x0u,0xd8b1cu,0x0u,0x11ff30u,0x0u,0xd8b1cu,0x0u,0x11ff20u,0x0u,0xd8b1cu,0x0u,0x11ff10u,0x0u,0xd8b1cu,0x0u,0x11ff00u,0x0u,0xd8b1cu,0x0u,0x11fff0u,0x0u,0xd8b1cu,0x0u,0x11ffe0u,0x0u,0xd8b1cu,0x0u,0x11ffd0u,0x0u,0xd8b1cu,0x0u,0x11ffc0u,0x0u,0xd8b1cu,0x0u,0x11ffb0u,0x0u,0xd8b1cu,0x0u,0x11ffa0u,0x0u,0xd8b1cu,0x0u,0x11ff90u,0x0u,0xd8b1cu,0x0u,0x11ff80u,0x0u,0xd8b1cu,0x5840u,0x5860u,0x6800u,0x5840u,0x5848u,0x5868u,0x6808u,0x5850u,0x5850u,0x5870u,0x6810u,0x5860u,0x5858u,0x5878u,0x6818u,0x5870u,0x58c0u,0x58e0u,0x6880u,0x58c0u,0x58c8u,0x58e8u,0x6888u,0x58d0u,0x58d0u,0x58f0u,0x6890u,0x58e0u,0x58d8u,0x58f8u,0x6898u,0x58f0u,0x5960u,0x5940u,0x6920u,0x5960u,0x5968u,0x5948u,0x6928u,0x5970u,0x5970u,0x5950u,0x6930u,0x5940u,0x5978u,0x5958u,0x6938u,0x5950u,0x59e0u,0x59c0u,0x69a0u,0x59e0u,0x59e8u,0x59c8u,0x69a8u,0x59f0u,0x59f0u,0x59d0u,0x69b0u,0x59c0u,0x59f8u,0x59d8u,0x69b8u,0x59d0u,0x5c00u,0x5c20u,0x6c40u,0x5a40u,0x5c08u,0x5c28u,0x6c48u,0x5a50u,0x5c10u,0x5c30u,0x6c50u,0x5a60u,0x5c18u,0x5c38u,0x6c58u,0x5a70u,0x5c80u,0x5ca0u,0x6cc0u,0x5ac0u,0x5c88u,0x5ca8u,0x6cc8u,0x5ad0u,0x5c90u,0x5cb0u,0x6cd0u,0x5ae0u,0x5c98u,0x5cb8u,0x6cd8u,0x5af0u,0x5d20u,0x5d00u,0x6d60u,0x5b60u,0x5d28u,0x5d08u,0x6d68u,0x5b70u,0x5d30u,0x5d10u,0x6d70u,0x5b40u,0x5d38u,0x5d18u,0x6d78u,0x5b50u,0x5da0u,0x5d80u,0x6de0u,0x5be0u,0x5da8u,0x5d88u,0x6de8u,0x5bf0u,0x5db0u,0x5d90u,0x6df0u,0x5bc0u,0x5db8u,0x5d98u,0x6df8u,0x5bd0u,0x6040u,0x6060u,0x7000u,0x5c00u,0x6048u,0x6068u,0x7008u,0x5c10u,0x6050u,0x6070u,0x7010u,0x5c20u,0x6058u,0x6078u,0x7018u,0x5c30u,0x60c0u,0x60e0u,0x7080u,0x5c80u,0x60c8u,0x60e8u,0x7088u,0x5c90u,0x60d0u,0x60f0u,0x7090u,0x5ca0u,0x60d8u,0x60f8u,0x7098u,0x5cb0u,0x6160u,0x6140u,0x7120u,0x5d20u,0x6168u,0x6148u,0x7128u,0x5d30u,0x6170u,0x6150u,0x7130u,0x5d00u,0x6178u,0x6158u,0x7138u,0x5d10u,0x61e0u,0x61c0u,0x71a0u,0x5da0u,0x61e8u,0x61c8u,0x71a8u,0x5db0u,0x61f0u,0x61d0u,0x71b0u,0x5d80u,0x61f8u,0x61d8u,0x71b8u,0x5d90u,0x6400u,0x6420u,0x7440u,0x5e00u,0x6408u,0x6428u,0x7448u,0x5e10u,0x6410u,0x6430u,0x7450u,0x5e20u,0x6418u,0x6438u,0x7458u,0x5e30u,0x6480u,0x64a0u,0x74c0u,0x5e80u,0x6488u,0x64a8u,0x74c8u,0x5e90u,0x6490u,0x64b0u,0x74d0u,0x5ea0u,0x6498u,0x64b8u,0x74d8u,0x5eb0u,0x6520u,0x6500u,0x7560u,0x5f20u,0x6528u,0x6508u,0x7568u,0x5f30u,0x6530u,0x6510u,0x7570u,0x5f00u,0x6538u,0x6518u,0x7578u,0x5f10u,0x65a0u,0x6580u,0x75e0u,0x5fa0u,0x65a8u,0x6588u,0x75e8u,0x5fb0u,0x65b0u,0x6590u,0x75f0u,0x5f80u,0x65b8u,0x6598u,0x75f8u,0x5f90u,0x6800u,0x1e980u,0x1ebc0u,0x1800u,0x6808u,0x1ee10u,0x1f050u,0x1810u,0x6810u,0x1f2a0u,0x1f4e0u,0x1820u,0x6818u,0x1f730u,0x1f970u,0x1830u,0x6880u,0x1fbc0u,0x1fe00u,0x1840u,0x6888u,0x20050u,0x20290u,0x1850u,0x6890u,0x204e0u,0x20720u,0x1860u,0x6898u,0x20970u,0x20bb0u,0x1870u,0x6920u,0x20e00u,0x21040u,0x1980u,0x6928u,0x21290u,0x214d0u,0x1990u,0x6930u,0x21720u,0x21960u,0x19a0u,0x6938u,0x21bb0u,0x21df0u,0x19b0u,0x69a0u,0x22040u,0x22280u,0x19c0u,0x69a8u,0x224d0u,0x22710u,0x19d0u,0x69b0u,0x22960u,0x22ba0u,0x19e0u,0x69b8u,0x22df0u,0x23030u,0x19f0u,0x6a00u,0x1e9b0u,0x1ebc8u,0x1b20u,0x6a08u,0x1ee40u,0x1f058u,0x1b30u,0x6a10u,0x1f2d0u,0x1f4e8u,0x1b00u,0x6a18u,0x1f760u,0x1f978u,0x1b10u,0x6a80u,0x1fbf0u,0x1fe08u,0x1b60u,0x6a88u,0x20080u,0x20298u,0x1b70u,0x6a90u,0x20510u,0x20728u,0x1b40u,0x6a98u,0x209a0u,0x20bb8u,0x1b50u,0x6b20u,0x20e30u,0x21048u,0x1ca0u,0x6b28u,0x212c0u,0x214d8u,0x1cb0u,0x6b30u,0x21750u,0x21968u,0x1c80u,0x6b38u,0x21be0u,0x21df8u,0x1c90u,0x6ba0u,0x22070u,0x22288u,0x1ce0u,0x6ba8u,0x22500u,0x22718u,0x1cf0u,0x6bb0u,0x22990u,0x22ba8u,0x1cc0u,0x6bb8u,0x22e20u,0x23038u,0x1cd0u,0x6c40u,0x1e9e0u,0x1ebd0u,0x1e00u,0x6c48u,0x1ee70u,0x1f060u,0x1e10u,0x6c50u,0x1f300u,0x1f4f0u,0x1e20u,0x6c58u,0x1f790u,0x1f980u,0x1e30u,0x6cc0u,0x1fc20u,0x1fe10u,0x1e40u,0x6cc8u,0x200b0u,0x202a0u,0x1e50u,0x6cd0u,0x20540u,0x20730u,0x1e60u,0x6cd8u,0x209d0u,0x20bc0u,0x1e70u,0x6d60u,0x20e60u,0x21050u,0x1f80u,0x6d68u,0x212f0u,0x214e0u,0x1f90u,0x6d70u,0x21780u,0x21970u,0x1fa0u,0x6d78u,0x21c10u,0x21e00u,0x1fb0u,0x6de0u,0x220a0u,0x22290u,0x1fc0u,0x6de8u,0x22530u,0x22720u,0x1fd0u,0x6df0u,0x229c0u,0x22bb0u,0x1fe0u,0x6df8u,0x22e50u,0x23040u,0x1ff0u,0x6e40u,0x1ea10u,0x1ebd8u,0x2120u,0x6e48u,0x1eea0u,0x1f068u,0x2130u,0x6e50u,0x1f330u,0x1f4f8u,0x2100u,0x6e58u,0x1f7c0u,0x1f988u,0x2110u,0x6ec0u,0x1fc50u,0x1fe18u,0x2160u,0x6ec8u,0x200e0u,0x202a8u,0x2170u,0x6ed0u,0x20570u,0x20738u,0x2140u,0x6ed8u,0x20a00u,0x20bc8u,0x2150u,0x6f60u,0x20e90u,0x21058u,0x22a0u,0x6f68u,0x21320u,0x214e8u,0x22b0u,0x6f70u,0x217b0u,0x21978u,0x2280u,0x6f78u,0x21c40u,0x21e08u,0x2290u,0x6fe0u,0x220d0u,0x22298u,0x22e0u,0x6fe8u,0x22560u,0x22728u,0x22f0u,0x6ff0u,0x229f0u,0x22bb8u,0x22c0u,0x6ff8u,0x22e80u,0x23048u,0x22d0u,0x0u,0x40u,0x9000u,0xb400u,0x80u,0xc0u,0x9010u,0xb410u,0x110u,0x150u,0x9020u,0xb420u,0x190u,0x1d0u,0x9030u,0xb430u,0x220u,0x260u,0x9040u,0xb440u,0x2a0u,0x2e0u,0x9050u,0xb450u,0x330u,0x370u,0x9060u,0xb460u,0x3b0u,0x3f0u,0x9070u,0xb470u,0x440u,0x400u,0x9080u,0xb480u,0x4c0u,0x480u,0x9090u,0xb490u,0x550u,0x510u,0x90a0u,0xb4a0u,0x5d0u,0x590u,0x90b0u,0xb4b0u,0x660u,0x620u,0x90c0u,0xb4c0u,0x6e0u,0x6a0u,0x90d0u,0xb4d0u,0x770u,0x730u,0x90e0u,0xb4e0u,0x7f0u,0x7b0u,0x90f0u,0xb4f0u,0x10u,0x50u,0x9300u,0xb700u,0x90u,0xd0u,0x9310u,0xb710u,0x100u,0x140u,0x9320u,0xb720u,0x180u,0x1c0u,0x9330u,0xb730u,0x230u,0x270u,0x9340u,0xb740u,0x2b0u,0x2f0u,0x9350u,0xb750u,0x320u,0x360u,0x9360u,0xb760u,0x3a0u,0x3e0u,0x9370u,0xb770u,0x450u,0x410u,0x9380u,0xb780u,0x4d0u,0x490u,0x9390u,0xb790u,0x540u,0x500u,0x93a0u,0xb7a0u,0x5c0u,0x580u,0x93b0u,0xb7b0u,0x670u,0x630u,0x93c0u,0xb7c0u,0x6f0u,0x6b0u,0x93d0u,0xb7d0u,0x760u,0x720u,0x93e0u,0xb7e0u,0x7e0u,0x7a0u,0x93f0u,0xb7f0u,0x20u,0x60u,0x9600u,0xba00u,0xa0u,0xe0u,0x9610u,0xba10u,0x130u,0x170u,0x9620u,0xba20u,0x1b0u,0x1f0u,0x9630u,0xba30u,0x200u,0x240u,0x9640u,0xba40u,0x280u,0x2c0u,0x9650u,0xba50u,0x310u,0x350u,0x9660u,0xba60u,0x390u,0x3d0u,0x9670u,0xba70u,0x460u,0x420u,0x9680u,0xba80u,0x4e0u,0x4a0u,0x9690u,0xba90u,0x570u,0x530u,0x96a0u,0xbaa0u,0x5f0u,0x5b0u,0x96b0u,0xbab0u,0x640u,0x600u,0x96c0u,0xbac0u,0x6c0u,0x680u,0x96d0u,0xbad0u,0x750u,0x710u,0x96e0u,0xbae0u,0x7d0u,0x790u,0x96f0u,0xbaf0u,0x30u,0x70u,0x9900u,0xbd00u,0xb0u,0xf0u,0x9910u,0xbd10u,0x120u,0x160u,0x9920u,0xbd20u,0x1a0u,0x1e0u,0x9930u,0xbd30u,0x210u,0x250u,0x9940u,0xbd40u,0x290u,0x2d0u,0x9950u,0xbd50u,0x300u,0x340u,0x9960u,0xbd60u,0x380u,0x3c0u,0x9970u,0xbd70u,0x470u,0x430u,0x9980u,0xbd80u,0x4f0u,0x4b0u,0x9990u,0xbd90u,0x560u,0x520u,0x99a0u,0xbda0u,0x5e0u,0x5a0u,0x99b0u,0xbdb0u,0x650u,0x610u,0x99c0u,0xbdc0u,0x6d0u,0x690u,0x99d0u,0xbdd0u,0x740u,0x700u,0x99e0u,0xbde0u,0x7c0u,0x780u,0x99f0u,0xbdf0u,0xc000u,0x120c0u,0x12100u,0x12800u,0xc010u,0x12250u,0x12290u,0x12810u,0xc020u,0x123e0u,0x12420u,0x12820u,0xc030u,0x12570u,0x125b0u,0x12830u,0xc040u,0x12680u,0x12740u,0x12840u,0xc050u,0x12810u,0x128d0u,0x12850u,0xc060u,0x129a0u,0x12a60u,0x12860u,0xc070u,0x12b30u,0x12bf0u,0x12870u,0xc080u,0x12cc0u,0x12d00u,0x12880u,0xc090u,0x12e50u,0x12e90u,0x12890u,0xc0a0u,0x12fe0u,0x13020u,0x128a0u,0xc0b0u,0x13170u,0x131b0u,0x128b0u,0xc0c0u,0x13280u,0x13340u,0x128c0u,0xc0d0u,0x13410u,0x134d0u,0x128d0u,0xc0e0u,0x135a0u,0x13660u,0x128e0u,0xc0f0u,0x13730u,0x137f0u,0x128f0u,0xc300u,0x120d0u,0x12108u,0x12900u,0xc310u,0x12240u,0x12298u,0x12910u,0xc320u,0x123f0u,0x12428u,0x12920u,0xc330u,0x12560u,0x125b8u,0x12930u,0xc340u,0x12690u,0x12748u,0x12940u,0xc350u,0x12800u,0x128d8u,0x12950u,0xc360u,0x129b0u,0x12a68u,0x12960u,0xc370u,0x12b20u,0x12bf8u,0x12970u,0xc380u,0x12cd0u,0x12d08u,0x12980u,0xc390u,0x12e40u,0x12e98u,0x12990u,0xc3a0u,0x12ff0u,0x13028u,0x129a0u,0xc3b0u,0x13160u,0x131b8u,0x129b0u,0xc3c0u,0x13290u,0x13348u,0x129c0u,0xc3d0u,0x13400u,0x134d8u,0x129d0u,0xc3e0u,0x135b0u,0x13668u,0x129e0u,0xc3f0u,0x13720u,0x137f8u,0x129f0u,0xc600u,0x120e0u,0x12110u,0x12a00u,0xc610u,0x12270u,0x12280u,0x12a10u,0xc620u,0x123c0u,0x12430u,0x12a20u,0xc630u,0x12550u,0x125a0u,0x12a30u,0xc640u,0x126a0u,0x12750u,0x12a40u,0xc650u,0x12830u,0x128c0u,0x12a50u,0xc660u,0x12980u,0x12a70u,0x12a60u,0xc670u,0x12b10u,0x12be0u,0x12a70u,0xc680u,0x12ce0u,0x12d10u,0x12a80u,0xc690u,0x12e70u,0x12e80u,0x12a90u,0xc6a0u,0x12fc0u,0x13030u,0x12aa0u,0xc6b0u,0x13150u,0x131a0u,0x12ab0u,0xc6c0u,0x132a0u,0x13350u,0x12ac0u,0xc6d0u,0x13430u,0x134c0u,0x12ad0u,0xc6e0u,0x13580u,0x13670u,0x12ae0u,0xc6f0u,0x13710u,0x137e0u,0x12af0u,0xc900u,0x120f0u,0x12118u,0x12b00u,0xc910u,0x12260u,0x12288u,0x12b10u,0xc920u,0x123d0u,0x12438u,0x12b20u,0xc930u,0x12540u,0x125a8u,0x12b30u,0xc940u,0x126b0u,0x12758u,0x12b40u,0xc950u,0x12820u,0x128c8u,0x12b50u,0xc960u,0x12990u,0x12a78u,0x12b60u,0xc970u,0x12b00u,0x12be8u,0x12b70u,0xc980u,0x12cf0u,0x12d18u,0x12b80u,0xc990u,0x12e60u,0x12e88u,0x12b90u,0xc9a0u,0x12fd0u,0x13038u,0x12ba0u,0xc9b0u,0x13140u,0x131a8u,0x12bb0u,0xc9c0u,0x132b0u,0x13358u,0x12bc0u,0xc9d0u,0x13420u,0x134c8u,0x12bd0u,0xc9e0u,0x13590u,0x13678u,0x12be0u,0xc9f0u,0x13700u,0x137e8u,0x12bf0u,0x7800u,0x1c300u,0x1e980u,0x1ebc0u,0x7880u,0x1c310u,0x100000u,0x1ebc2u,0x7910u,0x1c320u,0x100000u,0x1ebc4u,0x7990u,0x1c330u,0x100000u,0x1ebc6u,0x7a20u,0x1c340u,0x1e980u,0x1ebc8u,0x7aa0u,0x1c350u,0x100000u,0x1ebcau,0x7b30u,0x1c360u,0x100000u,0x1ebccu,0x7bb0u,0x1c370u,0x100000u,0x1ebceu,0x7c40u,0x1c380u,0x1e980u,0x1ebd0u,0x7cc0u,0x1c390u,0x100000u,0x1ebd2u,0x7d50u,0x1c3a0u,0x100000u,0x1ebd4u,0x7dd0u,0x1c3b0u,0x100000u,0x1ebd6u,0x7e60u,0x1c3c0u,0x1e980u,0x1ebd8u,0x7ee0u,0x1c3d0u,0x100000u,0x1ebdau,0x7f70u,0x1c3e0u,0x100000u,0x1ebdcu,0x7ff0u,0x1c3f0u,0x100000u,0x1ebdeu,0x7810u,0x1c400u,0x1e9b0u,0x1ebc0u,0x7890u,0x1c410u,0x100000u,0x1ebc2u,0x7900u,0x1c420u,0x100000u,0x1ebc4u,0x7980u,0x1c430u,0x100000u,0x1ebc6u,0x7a30u,0x1c440u,0x1e9b0u,0x1ebc8u,0x7ab0u,0x1c450u,0x100000u,0x1ebcau,0x7b20u,0x1c460u,0x100000u,0x1ebccu,0x7ba0u,0x1c470u,0x100000u,0x1ebceu,0x7c50u,0x1c480u,0x1e9b0u,0x1ebd0u,0x7cd0u,0x1c490u,0x100000u,0x1ebd2u,0x7d40u,0x1c4a0u,0x100000u,0x1ebd4u,0x7dc0u,0x1c4b0u,0x100000u,0x1ebd6u,0x7e70u,0x1c4c0u,0x1e9b0u,0x1ebd8u,0x7ef0u,0x1c4d0u,0x100000u,0x1ebdau,0x7f60u,0x1c4e0u,0x100000u,0x1ebdcu,0x7fe0u,0x1c4f0u,0x100000u,0x1ebdeu,0x7820u,0x1c500u,0x1e9e0u,0x1ebc0u,0x78a0u,0x1c510u,0x100000u,0x1ebc2u,0x7930u,0x1c520u,0x100000u,0x1ebc4u,0x79b0u,0x1c530u,0x100000u,0x1ebc6u,0x7a00u,0x1c540u,0x1e9e0u,0x1ebc8u,0x7a80u,0x1c550u,0x100000u,0x1ebcau,0x7b10u,0x1c560u,0x100000u,0x1ebccu,0x7b90u,0x1c570u,0x100000u,0x1ebceu,0x7c60u,0x1c580u,0x1e9e0u,0x1ebd0u,0x7ce0u,0x1c590u,0x100000u,0x1ebd2u,0x7d70u,0x1c5a0u,0x100000u,0x1ebd4u,0x7df0u,0x1c5b0u,0x100000u,0x1ebd6u,0x7e40u,0x1c5c0u,0x1e9e0u,0x1ebd8u,0x7ec0u,0x1c5d0u,0x100000u,0x1ebdau,0x7f50u,0x1c5e0u,0x100000u,0x1ebdcu,0x7fd0u,0x1c5f0u,0x100000u,0x1ebdeu,0x7830u,0x1c600u,0x1ea10u,0x1ebc0u,0x78b0u,0x1c610u,0x100000u,0x1ebc2u,0x7920u,0x1c620u,0x100000u,0x1ebc4u,0x79a0u,0x1c630u,0x100000u,0x1ebc6u,0x7a10u,0x1c640u,0x1ea10u,0x1ebc8u,0x7a90u,0x1c650u,0x100000u,0x1ebcau,0x7b00u,0x1c660u,0x100000u,0x1ebccu,0x7b80u,0x1c670u,0x100000u,0x1ebceu,0x7c70u,0x1c680u,0x1ea10u,0x1ebd0u,0x7cf0u,0x1c690u,0x100000u,0x1ebd2u,0x7d60u,0x1c6a0u,0x100000u,0x1ebd4u,0x7de0u,0x1c6b0u,0x100000u,0x1ebd6u,0x7e50u,0x1c6c0u,0x1ea10u,0x1ebd8u,0x7ed0u,0x1c6d0u,0x100000u,0x1ebdau,0x7f40u,0x1c6e0u,0x100000u,0x1ebdcu,0x7fc0u,0x1c6f0u,0x100000u,0x1ebdeu,0x26000u,0x1d400u,0x1d400u,0x80000000u,0x26002u,0x1d402u,0x100000u,0x80000000u,0x26004u,0x1d404u,0x100000u,0x80000000u,0x26006u,0x1d406u,0x100000u,0x80000000u,0x26200u,0x1d410u,0x1d400u,0x80000000u,0x26202u,0x1d412u,0x100000u,0x80000000u,0x26204u,0x1d414u,0x100000u,0x80000000u,0x26206u,0x1d416u,0x100000u,0x80000000u,0x26400u,0x1d420u,0x1d400u,0x80000000u,0x26402u,0x1d422u,0x100000u,0x80000000u,0x26404u,0x1d424u,0x100000u,0x80000000u,0x26406u,0x1d426u,0x100000u,0x80000000u,0x26600u,0x1d430u,0x1d400u,0x80000000u,0x26602u,0x1d432u,0x100000u,0x80000000u,0x26604u,0x1d434u,0x100000u,0x80000000u,0x26606u,0x1d436u,0x100000u,0x80000000u,0x26008u,0x1d408u,0x1d410u,0x80000000u,0x2600au,0x1d40au,0x100000u,0x80000000u,0x2600cu,0x1d40cu,0x100000u,0x80000000u,0x2600eu,0x1d40eu,0x100000u,0x80000000u,0x26208u,0x1d418u,0x1d410u,0x80000000u,0x2620au,0x1d41au,0x100000u,0x80000000u,0x2620cu,0x1d41cu,0x100000u,0x80000000u,0x2620eu,0x1d41eu,0x100000u,0x80000000u,0x26408u,0x1d428u,0x1d410u,0x80000000u,0x2640au,0x1d42au,0x100000u,0x80000000u,0x2640cu,0x1d42cu,0x100000u,0x80000000u,0x2640eu,0x1d42eu,0x100000u,0x80000000u,0x26608u,0x1d438u,0x1d410u,0x80000000u,0x2660au,0x1d43au,0x100000u,0x80000000u,0x2660cu,0x1d43cu,0x100000u,0x80000000u,0x2660eu,0x1d43eu,0x100000u,0x80000000u,0x26800u,0x1d440u,0x1d420u,0x80000000u,0x26802u,0x1d442u,0x100000u,0x80000000u,0x26804u,0x1d444u,0x100000u,0x80000000u,0x26806u,0x1d446u,0x100000u,0x80000000u,0x26a00u,0x1d450u,0x1d420u,0x80000000u,0x26a02u,0x1d452u,0x100000u,0x80000000u,0x26a04u,0x1d454u,0x100000u,0x80000000u,0x26a06u,0x1d456u,0x100000u,0x80000000u,0x26c00u,0x1d460u,0x1d420u,0x80000000u,0x26c02u,0x1d462u,0x100000u,0x80000000u,0x26c04u,0x1d464u,0x100000u,0x80000000u,0x26c06u,0x1d466u,0x100000u,0x80000000u,0x26e00u,0x1d470u,0x1d420u,0x80000000u,0x26e02u,0x1d472u,0x100000u,0x80000000u,0x26e04u,0x1d474u,0x100000u,0x80000000u,0x26e06u,0x1d476u,0x100000u,0x80000000u,0x100000u,0x1d448u,0x1d430u,0x80000000u,0x100000u,0x1d44au,0x100000u,0x80000000u,0x100000u,0x1d44cu,0x100000u,0x80000000u,0x100000u,0x1d44eu,0x100000u,0x80000000u,0x100000u,0x1d458u,0x1d430u,0x80000000u,0x100000u,0x1d45au,0x100000u,0x80000000u,0x100000u,0x1d45cu,0x100000u,0x80000000u,0x100000u,0x1d45eu,0x100000u,0x80000000u,0x100000u,0x1d468u,0x1d430u,0x80000000u,0x100000u,0x1d46au,0x100000u,0x80000000u,0x100000u,0x1d46cu,0x100000u,0x80000000u,0x100000u,0x1d46eu,0x100000u,0x80000000u,0x100000u,0x1d478u,0x1d430u,0x80000000u,0x100000u,0x1d47au,0x100000u,0x80000000u,0x100000u,0x1d47cu,0x100000u,0x80000000u,0x100000u,0x1d47eu,0x100000u,0x80000000u,0x850u,0x60000u,0x90420u,0xefc50u,0x840u,0x60010u,0x90430u,0xefc40u,0x870u,0x60020u,0x90400u,0xefc70u,0x860u,0x60030u,0x90410u,0xefc60u,0x810u,0x60040u,0x90460u,0xefc10u,0x800u,0x60050u,0x90470u,0xefc00u,0x830u,0x60060u,0x90440u,0xefc30u,0x820u,0x60070u,0x90450u,0xefc20u,0x8d0u,0x60080u,0x904b0u,0xefcd0u,0x8c0u,0x60090u,0x904a0u,0xefcc0u,0x8f0u,0x600a0u,0x90490u,0xefcf0u,0x8e0u,0x600b0u,0x90480u,0xefce0u,0x890u,0x600c0u,0x904f0u,0xefc90u,0x880u,0x600d0u,0x904e0u,0xefc80u,0x8b0u,0x600e0u,0x904d0u,0xefcb0u,0x8a0u,0x600f0u,0x904c0u,0xefca0u,0x960u,0x60100u,0x90530u,0xefd60u,0x970u,0x60110u,0x90520u,0xefd70u,0x940u,0x60120u,0x90510u,0xefd40u,0x950u,0x60130u,0x90500u,0xefd50u,0x920u,0x60140u,0x90570u,0xefd20u,0x930u,0x60150u,0x90560u,0xefd30u,0x900u,0x60160u,0x90550u,0xefd00u,0x910u,0x60170u,0x90540u,0xefd10u,0x9e0u,0x60190u,0x905b0u,0xefde0u,0x9f0u,0x60180u,0x905a0u,0xefdf0u,0x9c0u,0x601b0u,0x90590u,0xefdc0u,0x9d0u,0x601a0u,0x90580u,0xefdd0u,0x9a0u,0x601d0u,0x905f0u,0xefda0u,0x9b0u,0x601c0u,0x905e0u,0xefdb0u,0x980u,0x601f0u,0x905d0u,0xefd80u,0x990u,0x601e0u,0x905c0u,0xefd90u,0xa60u,0x60210u,0xbfa40u,0xefe60u,0xa70u,0x60200u,0xbfa50u,0xefe70u,0xa40u,0x60230u,0xbfa60u,0xefe40u,0xa50u,0x60220u,0xbfa70u,0xefe50u,0xa20u,0x60250u,0xbfa00u,0xefe20u,0xa30u,0x60240u,0xbfa10u,0xefe30u,0xa00u,0x60270u,0xbfa20u,0xefe00u,0xa10u,0x60260u,0xbfa30u,0xefe10u,0x2fef0u,0x60290u,0xbfac0u,0xefef0u,0x2fee0u,0x60280u,0xbfad0u,0xefee0u,0x2fed0u,0x602b0u,0xbfae0u,0xefed0u,0x2fec0u,0x602a0u,0xbfaf0u,0xefec0u,0x2feb0u,0x602d0u,0xbfa80u,0xefeb0u,0x2fea0u,0x602c0u,0xbfa90u,0xefea0u,0x2fe90u,0x602f0u,0xbfaa0u,0xefe90u,0x2fe80u,0x602e0u,0xbfab0u,0xefe80u,0x2ff70u,0x60320u,0xbfb40u,0xeff70u,0x2ff60u,0x60330u,0xbfb50u,0xeff60u,0x2ff50u,0x60300u,0xbfb60u,0xeff50u,0x2ff40u,0x60310u,0xbfb70u,0xeff40u,0x2ff30u,0x60360u,0xbfb00u,0xeff30u,0x2ff20u,0x60370u,0xbfb10u,0xeff20u,0x2ff10u,0x60340u,0xbfb20u,0xeff10u,0x2ff00u,0x60350u,0xbfb30u,0xeff00u,0x2fff0u,0x603a0u,0xbfbd0u,0xefff0u,0x2ffe0u,0x603b0u,0xbfbc0u,0xeffe0u,0x2ffd0u,0x60380u,0xbfbf0u,0xeffd0u,0x2ffc0u,0x60390u,0xbfbe0u,0xeffc0u,0x2ffb0u,0x603e0u,0xbfb90u,0xeffb0u,0x2ffa0u,0x603f0u,0xbfb80u,0xeffa0u,0x2ff90u,0x603c0u,0xbfbb0u,0xeff90u,0x2ff80u,0x603d0u,0xbfba0u,0xeff80u,0x0u,0x80000000u,0x0u,0x18830u,0x0u,0x80000000u,0x0u,0x18830u,0x0u,0x80000000u,0x0u,0x18830u,0x0u,0x80000000u,0x0u,0x18830u,0x0u,0x80000000u,0x0u,0x18830u,0x0u,0x80000000u,0x0u,0x18830u,0x0u,0x80000000u,0x0u,0x18830u,0x0u,0x80000000u,0x0u,0x18830u,0x0u,0x80000000u,0x0u,0x18830u,0x0u,0x80000000u,0x0u,0x18830u,0x0u,0x80000000u,0x0u,0x18830u,0x0u,0x80000000u,0x0u,0x18830u,0x0u,0x80000000u,0x0u,0x18830u,0x0u,0x80000000u,0x0u,0x18830u,0x0u,0x80000000u,0x0u,0x18830u,0x0u,0x80000000u,0x0u,0x18830u,0x0u,0x80000000u,0x0u,0x188b2u,0x0u,0x80000000u,0x0u,0x188b2u,0x0u,0x80000000u,0x0u,0x188b2u,0x0u,0x80000000u,0x0u,0x188b2u,0x0u,0x80000000u,0x0u,0x188b2u,0x0u,0x80000000u,0x0u,0x188b2u,0x0u,0x80000000u,0x0u,0x188b2u,0x0u,0x80000000u,0x0u,0x188b2u,0x0u,0x80000000u,0x0u,0x18892u,0x0u,0x80000000u,0x0u,0x18892u,0x0u,0x80000000u,0x0u,0x18892u,0x0u,0x80000000u,0x0u,0x18892u,0x0u,0x80000000u,0x0u,0x18892u,0x0u,0x80000000u,0x0u,0x18892u,0x0u,0x80000000u,0x0u,0x18892u,0x0u,0x80000000u,0x0u,0x18892u,0x0u,0x80000000u,0x0u,0x1489au,0x0u,0x80000000u,0x0u,0x1489au,0x0u,0x80000000u,0x0u,0x1489au,0x0u,0x80000000u,0x0u,0x1489au,0x0u,0x80000000u,0x0u,0x1489au,0x0u,0x80000000u,0x0u,0x1489au,0x0u,0x80000000u,0x0u,0x1489au,0x0u,0x80000000u,0x0u,0x1489au,0x0u,0x80000000u,0x0u,0x34018u,0x0u,0x80000000u,0x0u,0x34018u,0x0u,0x80000000u,0x0u,0x34018u,0x0u,0x80000000u,0x0u,0x34018u,0x0u,0x80000000u,0x0u,0x34018u,0x0u,0x80000000u,0x0u,0x34018u,0x0u,0x80000000u,0x0u,0x34018u,0x0u,0x80000000u,0x0u,0x34018u,0x0u,0x80000000u,0x0u,0x36018u,0x0u,0x80000000u,0x0u,0x36018u,0x0u,0x80000000u,0x0u,0x36018u,0x0u,0x80000000u,0x0u,0x36018u,0x0u,0x80000000u,0x0u,0x36018u,0x0u,0x80000000u,0x0u,0x36018u,0x0u,0x80000000u,0x0u,0x36018u,0x0u,0x80000000u,0x0u,0x36018u,0x0u,0x80000000u,0x0u,0x36030u,0x0u,0x80000000u,0x0u,0x36030u,0x0u,0x80000000u,0x0u,0x36030u,0x0u,0x80000000u,0x0u,0x36030u,0x0u,0x80000000u,0x0u,0x36030u,0x0u,0x80000000u,0x0u,0x36030u,0x0u,0x80000000u,0x0u,0x36030u,0x0u,0x80000000u,0x0u,0x36030u,0x6840u,0x6860u,0x6820u,0x6820u,0x6848u,0x6868u,0x6828u,0x6830u,0x6850u,0x6870u,0x6830u,0x6840u,0x6858u,0x6878u,0x6838u,0x6850u,0x68c0u,0x68e0u,0x68a0u,0x68a0u,0x68c8u,0x68e8u,0x68a8u,0x68b0u,0x68d0u,0x68f0u,0x68b0u,0x68c0u,0x68d8u,0x68f8u,0x68b8u,0x68d0u,0x6960u,0x6940u,0x6900u,0x6900u,0x6968u,0x6948u,0x6908u,0x6910u,0x6970u,0x6950u,0x6910u,0x6960u,0x6978u,0x6958u,0x6918u,0x6970u,0x69e0u,0x69c0u,0x6980u,0x6980u,0x69e8u,0x69c8u,0x6988u,0x6990u,0x69f0u,0x69d0u,0x6990u,0x69e0u,0x69f8u,0x69d8u,0x6998u,0x69f0u,0x6c00u,0x6c20u,0x6c60u,0x6a20u,0x6c08u,0x6c28u,0x6c68u,0x6a30u,0x6c10u,0x6c30u,0x6c70u,0x6a40u,0x6c18u,0x6c38u,0x6c78u,0x6a50u,0x6c80u,0x6ca0u,0x6ce0u,0x6aa0u,0x6c88u,0x6ca8u,0x6ce8u,0x6ab0u,0x6c90u,0x6cb0u,0x6cf0u,0x6ac0u,0x6c98u,0x6cb8u,0x6cf8u,0x6ad0u,0x6d20u,0x6d00u,0x6d40u,0x6b00u,0x6d28u,0x6d08u,0x6d48u,0x6b10u,0x6d30u,0x6d10u,0x6d50u,0x6b60u,0x6d38u,0x6d18u,0x6d58u,0x6b70u,0x6da0u,0x6d80u,0x6dc0u,0x6b80u,0x6da8u,0x6d88u,0x6dc8u,0x6b90u,0x6db0u,0x6d90u,0x6dd0u,0x6be0u,0x6db8u,0x6d98u,0x6dd8u,0x6bf0u,0x7040u,0x7060u,0x7020u,0x6c60u,0x7048u,0x7068u,0x7028u,0x6c70u,0x7050u,0x7070u,0x7030u,0x6c00u,0x7058u,0x7078u,0x7038u,0x6c10u,0x70c0u,0x70e0u,0x70a0u,0x6ce0u,0x70c8u,0x70e8u,0x70a8u,0x6cf0u,0x70d0u,0x70f0u,0x70b0u,0x6c80u,0x70d8u,0x70f8u,0x70b8u,0x6c90u,0x7160u,0x7140u,0x7100u,0x6d40u,0x7168u,0x7148u,0x7108u,0x6d50u,0x7170u,0x7150u,0x7110u,0x6d20u,0x7178u,0x7158u,0x7118u,0x6d30u,0x71e0u,0x71c0u,0x7180u,0x6dc0u,0x71e8u,0x71c8u,0x7188u,0x6dd0u,0x71f0u,0x71d0u,0x7190u,0x6da0u,0x71f8u,0x71d8u,0x7198u,0x6db0u,0x7400u,0x7420u,0x7460u,0x6e60u,0x7408u,0x7428u,0x7468u,0x6e70u,0x7410u,0x7430u,0x7470u,0x6e00u,0x7418u,0x7438u,0x7478u,0x6e10u,0x7480u,0x74a0u,0x74e0u,0x6ee0u,0x7488u,0x74a8u,0x74e8u,0x6ef0u,0x7490u,0x74b0u,0x74f0u,0x6e80u,0x7498u,0x74b8u,0x74f8u,0x6e90u,0x7520u,0x7500u,0x7540u,0x6f40u,0x7528u,0x7508u,0x7548u,0x6f50u,0x7530u,0x7510u,0x7550u,0x6f20u,0x7538u,0x7518u,0x7558u,0x6f30u,0x75a0u,0x7580u,0x75c0u,0x6fc0u,0x75a8u,0x7588u,0x75c8u,0x6fd0u,0x75b0u,0x7590u,0x75d0u,0x6fa0u,0x75b8u,0x7598u,0x75d8u,0x6fb0u,0x6860u,0x1ea40u,0x1ec20u,0x2440u,0x6868u,0x1eed0u,0x1f0b0u,0x2450u,0x6870u,0x1f360u,0x1f540u,0x2460u,0x6878u,0x1f7f0u,0x1f9d0u,0x2470u,0x68e0u,0x1fc80u,0x1fe60u,0x2400u,0x68e8u,0x20110u,0x202f0u,0x2410u,0x68f0u,0x205a0u,0x20780u,0x2420u,0x68f8u,0x20a30u,0x20c10u,0x2430u,0x6940u,0x20ec0u,0x210a0u,0x25c0u,0x6948u,0x21350u,0x21530u,0x25d0u,0x6950u,0x217e0u,0x219c0u,0x25e0u,0x6958u,0x21c70u,0x21e50u,0x25f0u,0x69c0u,0x22100u,0x222e0u,0x2580u,0x69c8u,0x22590u,0x22770u,0x2590u,0x69d0u,0x22a20u,0x22c00u,0x25a0u,0x69d8u,0x22eb0u,0x23090u,0x25b0u,0x6a60u,0x1ea70u,0x1ec28u,0x2760u,0x6a68u,0x1ef00u,0x1f0b8u,0x2770u,0x6a70u,0x1f390u,0x1f548u,0x2740u,0x6a78u,0x1f820u,0x1f9d8u,0x2750u,0x6ae0u,0x1fcb0u,0x1fe68u,0x2720u,0x6ae8u,0x20140u,0x202f8u,0x2730u,0x6af0u,0x205d0u,0x20788u,0x2700u,0x6af8u,0x20a60u,0x20c18u,0x2710u,0x6b40u,0x20ef0u,0x210a8u,0x28e0u,0x6b48u,0x21380u,0x21538u,0x28f0u,0x6b50u,0x21810u,0x219c8u,0x28c0u,0x6b58u,0x21ca0u,0x21e58u,0x28d0u,0x6bc0u,0x22130u,0x222e8u,0x28a0u,0x6bc8u,0x225c0u,0x22778u,0x28b0u,0x6bd0u,0x22a50u,0x22c08u,0x2880u,0x6bd8u,0x22ee0u,0x23098u,0x2890u,0x6c20u,0x1eaa0u,0x1ec30u,0x2a40u,0x6c28u,0x1ef30u,0x1f0c0u,0x2a50u,0x6c30u,0x1f3c0u,0x1f550u,0x2a60u,0x6c38u,0x1f850u,0x1f9e0u,0x2a70u,0x6ca0u,0x1fce0u,0x1fe70u,0x2a00u,0x6ca8u,0x20170u,0x20300u,0x2a10u,0x6cb0u,0x20600u,0x20790u,0x2a20u,0x6cb8u,0x20a90u,0x20c20u,0x2a30u,0x6d00u,0x20f20u,0x210b0u,0x2bc0u,0x6d08u,0x213b0u,0x21540u,0x2bd0u,0x6d10u,0x21840u,0x219d0u,0x2be0u,0x6d18u,0x21cd0u,0x21e60u,0x2bf0u,0x6d80u,0x22160u,0x222f0u,0x2b80u,0x6d88u,0x225f0u,0x22780u,0x2b90u,0x6d90u,0x22a80u,0x22c10u,0x2ba0u,0x6d98u,0x22f10u,0x230a0u,0x2bb0u,0x6e20u,0x1ead0u,0x1ec38u,0x2d60u,0x6e28u,0x1ef60u,0x1f0c8u,0x2d70u,0x6e30u,0x1f3f0u,0x1f558u,0x2d40u,0x6e38u,0x1f880u,0x1f9e8u,0x2d50u,0x6ea0u,0x1fd10u,0x1fe78u,0x2d20u,0x6ea8u,0x201a0u,0x20308u,0x2d30u,0x6eb0u,0x20630u,0x20798u,0x2d00u,0x6eb8u,0x20ac0u,0x20c28u,0x2d10u,0x6f00u,0x20f50u,0x210b8u,0x2ee0u,0x6f08u,0x213e0u,0x21548u,0x2ef0u,0x6f10u,0x21870u,0x219d8u,0x2ec0u,0x6f18u,0x21d00u,0x21e68u,0x2ed0u,0x6f80u,0x22190u,0x222f8u,0x2ea0u,0x6f88u,0x22620u,0x22788u,0x2eb0u,0x6f90u,0x22ab0u,0x22c18u,0x2e80u,0x6f98u,0x22f40u,0x230a8u,0x2e90u,0x0u,0x40u,0x9000u,0xcc00u,0x80u,0xc0u,0x9010u,0xcc10u,0x110u,0x150u,0x9020u,0xcc20u,0x190u,0x1d0u,0x9030u,0xcc30u,0x220u,0x260u,0x9040u,0xcc40u,0x2a0u,0x2e0u,0x9050u,0xcc50u,0x330u,0x370u,0x9060u,0xcc60u,0x3b0u,0x3f0u,0x9070u,0xcc70u,0x440u,0x400u,0x9080u,0xcc80u,0x4c0u,0x480u,0x9090u,0xcc90u,0x550u,0x510u,0x90a0u,0xcca0u,0x5d0u,0x590u,0x90b0u,0xccb0u,0x660u,0x620u,0x90c0u,0xccc0u,0x6e0u,0x6a0u,0x90d0u,0xccd0u,0x770u,0x730u,0x90e0u,0xcce0u,0x7f0u,0x7b0u,0x90f0u,0xccf0u,0x10u,0x50u,0x9300u,0xcf00u,0x90u,0xd0u,0x9310u,0xcf10u,0x100u,0x140u,0x9320u,0xcf20u,0x180u,0x1c0u,0x9330u,0xcf30u,0x230u,0x270u,0x9340u,0xcf40u,0x2b0u,0x2f0u,0x9350u,0xcf50u,0x320u,0x360u,0x9360u,0xcf60u,0x3a0u,0x3e0u,0x9370u,0xcf70u,0x450u,0x410u,0x9380u,0xcf80u,0x4d0u,0x490u,0x9390u,0xcf90u,0x540u,0x500u,0x93a0u,0xcfa0u,0x5c0u,0x580u,0x93b0u,0xcfb0u,0x670u,0x630u,0x93c0u,0xcfc0u,0x6f0u,0x6b0u,0x93d0u,0xcfd0u,0x760u,0x720u,0x93e0u,0xcfe0u,0x7e0u,0x7a0u,0x93f0u,0xcff0u,0x20u,0x60u,0x9600u,0xd200u,0xa0u,0xe0u,0x9610u,0xd210u,0x130u,0x170u,0x9620u,0xd220u,0x1b0u,0x1f0u,0x9630u,0xd230u,0x200u,0x240u,0x9640u,0xd240u,0x280u,0x2c0u,0x9650u,0xd250u,0x310u,0x350u,0x9660u,0xd260u,0x390u,0x3d0u,0x9670u,0xd270u,0x460u,0x420u,0x9680u,0xd280u,0x4e0u,0x4a0u,0x9690u,0xd290u,0x570u,0x530u,0x96a0u,0xd2a0u,0x5f0u,0x5b0u,0x96b0u,0xd2b0u,0x640u,0x600u,0x96c0u,0xd2c0u,0x6c0u,0x680u,0x96d0u,0xd2d0u,0x750u,0x710u,0x96e0u,0xd2e0u,0x7d0u,0x790u,0x96f0u,0xd2f0u,0x30u,0x70u,0x9900u,0xd500u,0xb0u,0xf0u,0x9910u,0xd510u,0x120u,0x160u,0x9920u,0xd520u,0x1a0u,0x1e0u,0x9930u,0xd530u,0x210u,0x250u,0x9940u,0xd540u,0x290u,0x2d0u,0x9950u,0xd550u,0x300u,0x340u,0x9960u,0xd560u,0x380u,0x3c0u,0x9970u,0xd570u,0x470u,0x430u,0x9980u,0xd580u,0x4f0u,0x4b0u,0x9990u,0xd590u,0x560u,0x520u,0x99a0u,0xd5a0u,0x5e0u,0x5a0u,0x99b0u,0xd5b0u,0x650u,0x610u,0x99c0u,0xd5c0u,0x6d0u,0x690u,0x99d0u,0xd5d0u,0x740u,0x700u,0x99e0u,0xd5e0u,0x7c0u,0x780u,0x99f0u,0xd5f0u,0xc008u,0x12120u,0x12160u,0x12c00u,0xc018u,0x122b0u,0x122f0u,0x12c10u,0xc028u,0x12400u,0x12440u,0x12c20u,0xc038u,0x12590u,0x125d0u,0x12c30u,0xc048u,0x12760u,0x12720u,0x12c40u,0xc058u,0x128f0u,0x128b0u,0x12c50u,0xc068u,0x12a40u,0x12a00u,0x12c60u,0xc078u,0x12bd0u,0x12b90u,0x12c70u,0xc088u,0x12d20u,0x12d60u,0x12c80u,0xc098u,0x12eb0u,0x12ef0u,0x12c90u,0xc0a8u,0x13000u,0x13040u,0x12ca0u,0xc0b8u,0x13190u,0x131d0u,0x12cb0u,0xc0c8u,0x13360u,0x13320u,0x12cc0u,0xc0d8u,0x134f0u,0x134b0u,0x12cd0u,0xc0e8u,0x13640u,0x13600u,0x12ce0u,0xc0f8u,0x137d0u,0x13790u,0x12cf0u,0xc308u,0x12130u,0x12168u,0x12d00u,0xc318u,0x122a0u,0x122f8u,0x12d10u,0xc328u,0x12410u,0x12448u,0x12d20u,0xc338u,0x12580u,0x125d8u,0x12d30u,0xc348u,0x12770u,0x12728u,0x12d40u,0xc358u,0x128e0u,0x128b8u,0x12d50u,0xc368u,0x12a50u,0x12a08u,0x12d60u,0xc378u,0x12bc0u,0x12b98u,0x12d70u,0xc388u,0x12d30u,0x12d68u,0x12d80u,0xc398u,0x12ea0u,0x12ef8u,0x12d90u,0xc3a8u,0x13010u,0x13048u,0x12da0u,0xc3b8u,0x13180u,0x131d8u,0x12db0u,0xc3c8u,0x13370u,0x13328u,0x12dc0u,0xc3d8u,0x134e0u,0x134b8u,0x12dd0u,0xc3e8u,0x13650u,0x13608u,0x12de0u,0xc3f8u,0x137c0u,0x13798u,0x12df0u,0xc608u,0x12140u,0x12170u,0x12e00u,0xc618u,0x122d0u,0x122e0u,0x12e10u,0xc628u,0x12460u,0x12450u,0x12e20u,0xc638u,0x125f0u,0x125c0u,0x12e30u,0xc648u,0x12700u,0x12730u,0x12e40u,0xc658u,0x12890u,0x128a0u,0x12e50u,0xc668u,0x12a20u,0x12a10u,0x12e60u,0xc678u,0x12bb0u,0x12b80u,0x12e70u,0xc688u,0x12d40u,0x12d70u,0x12e80u,0xc698u,0x12ed0u,0x12ee0u,0x12e90u,0xc6a8u,0x13060u,0x13050u,0x12ea0u,0xc6b8u,0x131f0u,0x131c0u,0x12eb0u,0xc6c8u,0x13300u,0x13330u,0x12ec0u,0xc6d8u,0x13490u,0x134a0u,0x12ed0u,0xc6e8u,0x13620u,0x13610u,0x12ee0u,0xc6f8u,0x137b0u,0x13780u,0x12ef0u,0xc908u,0x12150u,0x12178u,0x12f00u,0xc918u,0x122c0u,0x122e8u,0x12f10u,0xc928u,0x12470u,0x12458u,0x12f20u,0xc938u,0x125e0u,0x125c8u,0x12f30u,0xc948u,0x12710u,0x12738u,0x12f40u,0xc958u,0x12880u,0x128a8u,0x12f50u,0xc968u,0x12a30u,0x12a18u,0x12f60u,0xc978u,0x12ba0u,0x12b88u,0x12f70u,0xc988u,0x12d50u,0x12d78u,0x12f80u,0xc998u,0x12ec0u,0x12ee8u,0x12f90u,0xc9a8u,0x13070u,0x13058u,0x12fa0u,0xc9b8u,0x131e0u,0x131c8u,0x12fb0u,0xc9c8u,0x13310u,0x13338u,0x12fc0u,0xc9d8u,0x13480u,0x134a8u,0x12fd0u,0xc9e8u,0x13630u,0x13618u,0x12fe0u,0xc9f8u,0x137a0u,0x13788u,0x12ff0u,0x8000u,0x1c300u,0x1ea40u,0x1ec20u,0x8080u,0x1c310u,0x100000u,0x1ec22u,0x8110u,0x1c320u,0x100000u,0x1ec24u,0x8190u,0x1c330u,0x100000u,0x1ec26u,0x8220u,0x1c340u,0x1ea40u,0x1ec28u,0x82a0u,0x1c350u,0x100000u,0x1ec2au,0x8330u,0x1c360u,0x100000u,0x1ec2cu,0x83b0u,0x1c370u,0x100000u,0x1ec2eu,0x8440u,0x1c380u,0x1ea40u,0x1ec30u,0x84c0u,0x1c390u,0x100000u,0x1ec32u,0x8550u,0x1c3a0u,0x100000u,0x1ec34u,0x85d0u,0x1c3b0u,0x100000u,0x1ec36u,0x8660u,0x1c3c0u,0x1ea40u,0x1ec38u,0x86e0u,0x1c3d0u,0x100000u,0x1ec3au,0x8770u,0x1c3e0u,0x100000u,0x1ec3cu,0x87f0u,0x1c3f0u,0x100000u,0x1ec3eu,0x8010u,0x1c400u,0x1ea70u,0x1ec20u,0x8090u,0x1c410u,0x100000u,0x1ec22u,0x8100u,0x1c420u,0x100000u,0x1ec24u,0x8180u,0x1c430u,0x100000u,0x1ec26u,0x8230u,0x1c440u,0x1ea70u,0x1ec28u,0x82b0u,0x1c450u,0x100000u,0x1ec2au,0x8320u,0x1c460u,0x100000u,0x1ec2cu,0x83a0u,0x1c470u,0x100000u,0x1ec2eu,0x8450u,0x1c480u,0x1ea70u,0x1ec30u,0x84d0u,0x1c490u,0x100000u,0x1ec32u,0x8540u,0x1c4a0u,0x100000u,0x1ec34u,0x85c0u,0x1c4b0u,0x100000u,0x1ec36u,0x8670u,0x1c4c0u,0x1ea70u,0x1ec38u,0x86f0u,0x1c4d0u,0x100000u,0x1ec3au,0x8760u,0x1c4e0u,0x100000u,0x1ec3cu,0x87e0u,0x1c4f0u,0x100000u,0x1ec3eu,0x8020u,0x1c500u,0x1eaa0u,0x1ec20u,0x80a0u,0x1c510u,0x100000u,0x1ec22u,0x8130u,0x1c520u,0x100000u,0x1ec24u,0x81b0u,0x1c530u,0x100000u,0x1ec26u,0x8200u,0x1c540u,0x1eaa0u,0x1ec28u,0x8280u,0x1c550u,0x100000u,0x1ec2au,0x8310u,0x1c560u,0x100000u,0x1ec2cu,0x8390u,0x1c570u,0x100000u,0x1ec2eu,0x8460u,0x1c580u,0x1eaa0u,0x1ec30u,0x84e0u,0x1c590u,0x100000u,0x1ec32u,0x8570u,0x1c5a0u,0x100000u,0x1ec34u,0x85f0u,0x1c5b0u,0x100000u,0x1ec36u,0x8640u,0x1c5c0u,0x1eaa0u,0x1ec38u,0x86c0u,0x1c5d0u,0x100000u,0x1ec3au,0x8750u,0x1c5e0u,0x100000u,0x1ec3cu,0x87d0u,0x1c5f0u,0x100000u,0x1ec3eu,0x8030u,0x1c600u,0x1ead0u,0x1ec20u,0x80b0u,0x1c610u,0x100000u,0x1ec22u,0x8120u,0x1c620u,0x100000u,0x1ec24u,0x81a0u,0x1c630u,0x100000u,0x1ec26u,0x8210u,0x1c640u,0x1ead0u,0x1ec28u,0x8290u,0x1c650u,0x100000u,0x1ec2au,0x8300u,0x1c660u,0x100000u,0x1ec2cu,0x8380u,0x1c670u,0x100000u,0x1ec2eu,0x8470u,0x1c680u,0x1ead0u,0x1ec30u,0x84f0u,0x1c690u,0x100000u,0x1ec32u,0x8560u,0x1c6a0u,0x100000u,0x1ec34u,0x85e0u,0x1c6b0u,0x100000u,0x1ec36u,0x8650u,0x1c6c0u,0x1ead0u,0x1ec38u,0x86d0u,0x1c6d0u,0x100000u,0x1ec3au,0x8740u,0x1c6e0u,0x100000u,0x1ec3cu,0x87c0u,0x1c6f0u,0x100000u,0x1ec3eu,0x27000u,0x1d480u,0x1d480u,0x80000000u,0x27002u,0x1d482u,0x100000u,0x80000000u,0x27004u,0x1d484u,0x100000u,0x80000000u,0x27006u,0x1d486u,0x100000u,0x80000000u,0x27200u,0x1d490u,0x1d480u,0x80000000u,0x27202u,0x1d492u,0x100000u,0x80000000u,0x27204u,0x1d494u,0x100000u,0x80000000u,0x27206u,0x1d496u,0x100000u,0x80000000u,0x27400u,0x1d4a0u,0x1d480u,0x80000000u,0x27402u,0x1d4a2u,0x100000u,0x80000000u,0x27404u,0x1d4a4u,0x100000u,0x80000000u,0x27406u,0x1d4a6u,0x100000u,0x80000000u,0x27600u,0x1d4b0u,0x1d480u,0x80000000u,0x27602u,0x1d4b2u,0x100000u,0x80000000u,0x27604u,0x1d4b4u,0x100000u,0x80000000u,0x27606u,0x1d4b6u,0x100000u,0x80000000u,0x27008u,0x1d488u,0x1d490u,0x80000000u,0x2700au,0x1d48au,0x100000u,0x80000000u,0x2700cu,0x1d48cu,0x100000u,0x80000000u,0x2700eu,0x1d48eu,0x100000u,0x80000000u,0x27208u,0x1d498u,0x1d490u,0x80000000u,0x2720au,0x1d49au,0x100000u,0x80000000u,0x2720cu,0x1d49cu,0x100000u,0x80000000u,0x2720eu,0x1d49eu,0x100000u,0x80000000u,0x27408u,0x1d4a8u,0x1d490u,0x80000000u,0x2740au,0x1d4aau,0x100000u,0x80000000u,0x2740cu,0x1d4acu,0x100000u,0x80000000u,0x2740eu,0x1d4aeu,0x100000u,0x80000000u,0x27608u,0x1d4b8u,0x1d490u,0x80000000u,0x2760au,0x1d4bau,0x100000u,0x80000000u,0x2760cu,0x1d4bcu,0x100000u,0x80000000u,0x2760eu,0x1d4beu,0x100000u,0x80000000u,0x26808u,0x1d4c0u,0x1d4a0u,0x80000000u,0x2680au,0x1d4c2u,0x100000u,0x80000000u,0x2680cu,0x1d4c4u,0x100000u,0x80000000u,0x2680eu,0x1d4c6u,0x100000u,0x80000000u,0x26a08u,0x1d4d0u,0x1d4a0u,0x80000000u,0x26a0au,0x1d4d2u,0x100000u,0x80000000u,0x26a0cu,0x1d4d4u,0x100000u,0x80000000u,0x26a0eu,0x1d4d6u,0x100000u,0x80000000u,0x26c08u,0x1d4e0u,0x1d4a0u,0x80000000u,0x26c0au,0x1d4e2u,0x100000u,0x80000000u,0x26c0cu,0x1d4e4u,0x100000u,0x80000000u,0x26c0eu,0x1d4e6u,0x100000u,0x80000000u,0x26e08u,0x1d4f0u,0x1d4a0u,0x80000000u,0x26e0au,0x1d4f2u,0x100000u,0x80000000u,0x26e0cu,0x1d4f4u,0x100000u,0x80000000u,0x26e0eu,0x1d4f6u,0x100000u,0x80000000u,0x100000u,0x1d4c8u,0x1d4b0u,0x80000000u,0x100000u,0x1d4cau,0x100000u,0x80000000u,0x100000u,0x1d4ccu,0x100000u,0x80000000u,0x100000u,0x1d4ceu,0x100000u,0x80000000u,0x100000u,0x1d4d8u,0x1d4b0u,0x80000000u,0x100000u,0x1d4dau,0x100000u,0x80000000u,0x100000u,0x1d4dcu,0x100000u,0x80000000u,0x100000u,0x1d4deu,0x100000u,0x80000000u,0x100000u,0x1d4e8u,0x1d4b0u,0x80000000u,0x100000u,0x1d4eau,0x100000u,0x80000000u,0x100000u,0x1d4ecu,0x100000u,0x80000000u,0x100000u,0x1d4eeu,0x100000u,0x80000000u,0x100000u,0x1d4f8u,0x1d4b0u,0x80000000u,0x100000u,0x1d4fau,0x100000u,0x80000000u,0x100000u,0x1d4fcu,0x100000u,0x80000000u,0x100000u,0x1d4feu,0x100000u,0x80000000u,0x30000u,0x60420u,0xbfc50u,0xf0000u,0x30010u,0x60430u,0xbfc40u,0xf0010u,0x30020u,0x60400u,0xbfc70u,0xf0020u,0x30030u,0x60410u,0xbfc60u,0xf0030u,0x30040u,0x60460u,0xbfc10u,0xf0040u,0x30050u,0x60470u,0xbfc00u,0xf0050u,0x30060u,0x60440u,0xbfc30u,0xf0060u,0x30070u,0x60450u,0xbfc20u,0xf0070u,0x30080u,0x604b0u,0xbfcd0u,0xf0080u,0x30090u,0x604a0u,0xbfcc0u,0xf0090u,0x300a0u,0x60490u,0xbfcf0u,0xf00a0u,0x300b0u,0x60480u,0xbfce0u,0xf00b0u,0x300c0u,0x604f0u,0xbfc90u,0xf00c0u,0x300d0u,0x604e0u,0xbfc80u,0xf00d0u,0x300e0u,0x604d0u,0xbfcb0u,0xf00e0u,0x300f0u,0x604c0u,0xbfca0u,0xf00f0u,0x30100u,0x60530u,0xbfd60u,0xf0100u,0x30110u,0x60520u,0xbfd70u,0xf0110u,0x30120u,0x60510u,0xbfd40u,0xf0120u,0x30130u,0x60500u,0xbfd50u,0xf0130u,0x30140u,0x60570u,0xbfd20u,0xf0140u,0x30150u,0x60560u,0xbfd30u,0xf0150u,0x30160u,0x60550u,0xbfd00u,0xf0160u,0x30170u,0x60540u,0xbfd10u,0xf0170u,0x30190u,0x605b0u,0xbfde0u,0xf0190u,0x30180u,0x605a0u,0xbfdf0u,0xf0180u,0x301b0u,0x60590u,0xbfdc0u,0xf01b0u,0x301a0u,0x60580u,0xbfdd0u,0xf01a0u,0x301d0u,0x605f0u,0xbfda0u,0xf01d0u,0x301c0u,0x605e0u,0xbfdb0u,0xf01c0u,0x301f0u,0x605d0u,0xbfd80u,0xf01f0u,0x301e0u,0x605c0u,0xbfd90u,0xf01e0u,0x30210u,0x60640u,0xbfe60u,0xf0210u,0x30200u,0x60650u,0xbfe70u,0xf0200u,0x30230u,0x60660u,0xbfe40u,0xf0230u,0x30220u,0x60670u,0xbfe50u,0xf0220u,0x30250u,0x60600u,0xbfe20u,0xf0250u,0x30240u,0x60610u,0xbfe30u,0xf0240u,0x30270u,0x60620u,0xbfe00u,0xf0270u,0x30260u,0x60630u,0xbfe10u,0xf0260u,0x30290u,0x606c0u,0xbfef0u,0xf0290u,0x30280u,0x606d0u,0xbfee0u,0xf0280u,0x302b0u,0x606e0u,0xbfed0u,0xf02b0u,0x302a0u,0x606f0u,0xbfec0u,0xf02a0u,0x302d0u,0x60680u,0xbfeb0u,0xf02d0u,0x302c0u,0x60690u,0xbfea0u,0xf02c0u,0x302f0u,0x606a0u,0xbfe90u,0xf02f0u,0x302e0u,0x606b0u,0xbfe80u,0xf02e0u,0x30320u,0x60740u,0xbff70u,0x11f720u,0x30330u,0x60750u,0xbff60u,0x11f730u,0x30300u,0x60760u,0xbff50u,0x11f700u,0x30310u,0x60770u,0xbff40u,0x11f710u,0x30360u,0x60700u,0xbff30u,0x11f760u,0x30370u,0x60710u,0xbff20u,0x11f770u,0x30340u,0x60720u,0xbff10u,0x11f740u,0x30350u,0x60730u,0xbff00u,0x11f750u,0x303a0u,0x8fbd0u,0xbfff0u,0x11f7a0u,0x303b0u,0x8fbc0u,0xbffe0u,0x11f7b0u,0x30380u,0x8fbf0u,0xbffd0u,0x11f780u,0x30390u,0x8fbe0u,0xbffc0u,0x11f790u,0x303e0u,0x8fb90u,0xbffb0u,0x11f7e0u,0x303f0u,0x8fb80u,0xbffa0u,0x11f7f0u,0x303c0u,0x8fbb0u,0xbff90u,0x11f7c0u,0x303d0u,0x8fba0u,0xbff80u,0x11f7d0u,0x0u,0x80000000u,0x0u,0x36030u,0x0u,0x80000000u,0x0u,0x36030u,0x0u,0x80000000u,0x0u,0x36030u,0x0u,0x80000000u,0x0u,0x36030u,0x0u,0x80000000u,0x0u,0x36030u,0x0u,0x80000000u,0x0u,0x36030u,0x0u,0x80000000u,0x0u,0x36030u,0x0u,0x80000000u,0x0u,0x36030u,0x0u,0x80000000u,0x0u,0x360b2u,0x0u,0x80000000u,0x0u,0x360b2u,0x0u,0x80000000u,0x0u,0x360b2u,0x0u,0x80000000u,0x0u,0x360b2u,0x0u,0x80000000u,0x0u,0x360b2u,0x0u,0x80000000u,0x0u,0x360b2u,0x0u,0x80000000u,0x0u,0x360b2u,0x0u,0x80000000u,0x0u,0x360b2u,0x0u,0x80000000u,0x0u,0x36092u,0x0u,0x80000000u,0x0u,0x36092u,0x0u,0x80000000u,0x0u,0x36092u,0x0u,0x80000000u,0x0u,0x36092u,0x0u,0x80000000u,0x0u,0x36092u,0x0u,0x80000000u,0x0u,0x36092u,0x0u,0x80000000u,0x0u,0x36092u,0x0u,0x80000000u,0x0u,0x36092u,0x0u,0x80000000u,0x0u,0x3609au,0x0u,0x80000000u,0x0u,0x3609au,0x0u,0x80000000u,0x0u,0x3609au,0x0u,0x80000000u,0x0u,0x3609au,0x0u,0x80000000u,0x0u,0x3609au,0x0u,0x80000000u,0x0u,0x3609au,0x0u,0x80000000u,0x0u,0x3609au,0x0u,0x80000000u,0x0u,0x3609au,0x0u,0x80000000u,0x0u,0x36059u,0x0u,0x80000000u,0x0u,0x36059u,0x0u,0x80000000u,0x0u,0x36059u,0x0u,0x80000000u,0x0u,0x36059u,0x0u,0x80000000u,0x0u,0x36059u,0x0u,0x80000000u,0x0u,0x36059u,0x0u,0x80000000u,0x0u,0x36059u,0x0u,0x80000000u,0x0u,0x36059u,0x0u,0x80000000u,0x0u,0x36059u,0x0u,0x80000000u,0x0u,0x36059u,0x0u,0x80000000u,0x0u,0x36059u,0x0u,0x80000000u,0x0u,0x36059u,0x0u,0x80000000u,0x0u,0x36059u,0x0u,0x80000000u,0x0u,0x36059u,0x0u,0x80000000u,0x0u,0x36059u,0x0u,0x80000000u,0x0u,0x36059u,0x0u,0x80000000u,0x0u,0x16071u,0x0u,0x80000000u,0x0u,0x16071u,0x0u,0x80000000u,0x0u,0x16071u,0x0u,0x80000000u,0x0u,0x16071u,0x0u,0x80000000u,0x0u,0x16071u,0x0u,0x80000000u,0x0u,0x16071u,0x0u,0x80000000u,0x0u,0x16071u,0x0u,0x80000000u,0x0u,0x16071u,0x0u,0x80000000u,0x0u,0x14071u,0x0u,0x80000000u,0x0u,0x14071u,0x0u,0x80000000u,0x0u,0x14071u,0x0u,0x80000000u,0x0u,0x14071u,0x0u,0x80000000u,0x0u,0x14071u,0x0u,0x80000000u,0x0u,0x14071u,0x0u,0x80000000u,0x0u,0x14071u,0x0u,0x80000000u,0x0u,0x14071u};

__global__ __launch_bounds__(256, 1) void swin_main(const uint16_t* x, const uint16_t* mask, char* ws, uint16_t* out,
                                                    const uint16_t* wqkv, const uint16_t* wo, const uint16_t* ls,
                                                    const uint16_t* w1, const uint16_t* w2, const uint16_t* rc,
                                                    const int* ridx) {
  __shared__ __attribute__((aligned(16))) char smem[LDS_BYTES];
  const int wave = __builtin_amdgcn_readfirstlane(threadIdx.x >> 6);
  const unsigned lds0 = (unsigned)(size_t)smem;
  asm volatile(
      ".p2align 5\n"
      "s_mov_b64 s[92:93], %0\n"
      "s_mov_b32 s40, %1\n"
      "s_mov_b32 s41, %2\n"
      "s_mov_b64 s[88:89], %3\n"
      "s_load_dwordx2 s[16:17], s[92:93], 0\n"
      "s_load_dwordx2 s[24:25], s[92:93], 8\n"
      "s_load_dwordx2 s[34:35], s[92:93], 16\n"
      "s_load_dwordx2 s[20:21], s[92:93], 24\n"
      "s_load_dwordx2 s[28:29], s[92:93], 32\n"
      "s_load_dwordx2 s[36:37], s[92:93], 40\n"
      "s_load_dwordx8 s[80:87], s[92:93], 48\n"
      "s_load_dwordx2 s[90:91], s[92:93], 80\n"
      "s_getpc_b64 s[98:99]\n"
      "L_ipf0_%=:\n"
      "v_mbcnt_lo_u32_b32 v167, -1, 0\n"
      "v_mbcnt_hi_u32_b32 v167, -1, v167\n"
      "v_lshlrev_b32 v237, 2, v167\n"
      "v_lshlrev_b32 v41, 4, v167\n"
      "s_cmp_lt_u32 s41, 209\n"
      "s_cselect_b32 s44, 20, 19\n"
      "s_lshl_b32 s92, s40, 8\n"
      "v_add_u32 v164, s92, v237\n"
      "v_min_u32 v29, 704, v164\n"
      "v_add_u32 v29, 4096, v29\n"
      "v_min_u32 v164, 672, v164\n"
      "v_add_u32 v238, 122112, v164\n"
      "s_lshr_b32 s92, s41, 3\n"
      "s_lshl_b32 s92, s92, 7\n"
      "v_lshlrev_b32 v239, 7, v167\n"
      "s_mul_i32 s93, s92, 18\n"
      "v_min_u32 v234, 2176, v239\n"
      "v_add_u32 v234, s93, v234\n"
      "s_cmp_eq_u32 s40, 1\n"
      "s_cselect_b32 s93, 4096, 0\n"
      "s_add_u32 s92, s92, s93\n"
      "s_cmp_eq_u32 s40, 2\n"
      "s_cselect_b32 s93, 9, 8\n"
      "s_cselect_b64 s[72:73], s[98:99], s[88:89]\n"
      "s_cmp_lt_u32 s40, 2\n"
      "s_cselect_b32 s93, 27, s93\n"
      "s_mul_i32 s92, s92, s93\n"
      "s_sub_u32 s93, s93, 1\n"
      "s_lshl_b32 s93, s93, 7\n"
      "v_min_u32 v239, s93, v239\n"
      "v_add_u32 v239, s92, v239\n"
      "s_lshl_b32 s95, s40, 3\n"
      "v_lshrrev_b32 v8, 3, v167\n"
      "v_and_b32 v166, 7, v167\n"
      "v_add_u32_e64 v236, s95, v8\n"
      "v_bfe_u32 v48, v236, 1, 3\n"
      "v_cmp_lt_u32 s[70:71], s44, v236\n"
      "v_min_u32_e64 v236, s44, v236\n"
      "s_mov_b32 s18, 100270464\n"
      "s_mov_b32 s19, 0x20000\n"
      "s_mov_b32 s78, 9604\n"
      "s_mov_b32 s79, 0x20000\n"
      "s_lshl_b32 s64, s40, 10\n"
      "v_add_u32_e64 v36, s64, v41\n"
      "s_cmp_lt_u32 s40, 2\n"
      "s_cselect_b32 s65, s64, 163840\n"
      "s_waitcnt lgkmcnt(0)\n"
      "s_cselect_b64 s[72:73], s[28:29], s[72:73]\n"
      "global_load_dword v164, v164, s[86:87]\n"
      "global_load_dword a255, v239, s[72:73]\n"
      "s_mov_b32 s76, s90\n"
      "s_mov_b32 s77, s91\n"
      "s_add_u32 m0, s64, 92160\n"
      "s_movk_i32 s92, 0\n"
      "buffer_load_dwordx4 v36, s[76:79], s92 offen  lds\n"
      "s_add_u32 m0, s64, 96256\n"
      "s_movk_i32 s92, 4096\n"
      "buffer_load_dwordx4 v36, s[76:79], s92 offen  lds\n"
      "s_add_u32 m0, s64, 100352\n"
      "s_cmp_lt_u32 s40, 2\n"
      "s_cselect_b32 m0, m0, s65\n"
      "s_movk_i32 s92, 8192\n"
      "buffer_load_dwordx4 v36, s[76:79], s92 offen  lds\n"
      "s_load_dwordx16 s[0:15], s[82:83], 0\n"
      "s_load_dwordx16 s[48:63], s[84:85], 0\n"
      "s_load_dwordx2 s[66:67], s[80:81], 0\n"
      "s_movk_i32 s93, 4180\n"
      "s_cmp_lt_u32 s41, 209\n"
      "s_cselect_b32 s92, 209, 47\n"
      "s_cselect_b32 s93, 795, s93\n"
      "s_cmp_lt_u32 s41, 252\n"
      "s_cselect_b32 s94, 16, 15\n"
      "s_add_u32 s93, s93, s41\n"
      "v_lshl_add_u32 v239, v236, 8, s41\n"
      "v_mul_u32_u24 v40, s92, v236\n"
      "v_add_u32 v40, s93, v40\n"
      "v_cmp_lt_u32 vcc, s94, v236\n"
      "v_subrev_u32 v49, 5184, v40\n"
      "s_nop 0\n"
      "v_cndmask_b32 v239, v239, v40, vcc\n"
      "v_cmp_le_u32 vcc, 5184, v239\n"
      "v_mul_u32_u24 v50, 58255, v239\n"
      "v_lshrrev_b32 v50, 22, v50\n"
      "v_mul_u32_u24 v165, 72, v50\n"
      "v_sub_u32 v165, v239, v165\n"
      "v_min_u32 v40, 72, v49\n"
      "v_cndmask_b32 v50, v50, v40, vcc\n"
      "v_subrev_u32 v40, 72, v49\n"
      "v_min_u32 v40, 72, v40\n"
      "v_cndmask_b32 v165, v165, v40, vcc\n"
      "v_mad_u32_u24 v239, v50, 7, 9\n"
      "v_subrev_u32 v40, 0x1ff, v239\n"
      "v_min_u32 v239, v239, v40\n"
      "v_mad_u32_u24 v40, v165, 7, 9\n"
      "v_subrev_u32 v165, 0x1ff, v40\n"
      "v_min_u32 v40, v40, v165\n"
      "v_mul_u32_u24 v239, 0x1ff, v239\n"
      "v_add_u32 v239, v239, v40\n"
      "v_mul_u32_u24 v165, 0x180, v239\n"
      "v_bfrev_b32 v236, 1\n"
      "v_cndmask_b32 v165, v165, v236, s[70:71]\n"
      "v_xor_b32 v48, v48, v166\n"
      "v_lshl_add_u32 v165, v48, 4, v165\n"
      "s_add_u32 m0, s64, 149504\n"
      "s_nop 0\n"
      "buffer_load_dwordx4 v165, s[16:19], 0 offen  lds\n"
      "s_add_u32 m0, s64, 153472\n"
      "s_nop 0\n"
      "buffer_load_dwordx4 v165, s[16:19], 0 offen offset:128  lds\n"
      "s_add_u32 m0, s64, 157440\n"
      "s_nop 0\n"
      "buffer_load_dwordx4 v165, s[16:19], 0 offen offset:256  lds\n"
      "s_movk_i32 s95, 504\n"
      "s_add_u32 s92, s40, 0\n"
      "s_mul_i32 s93, s92, 43\n"
      "s_lshr_b32 s93, s93, 8\n"
      "s_mul_i32 s94, s93, 6\n"
      "s_sub_u32 s92, s92, s94\n"
      "s_lshl_b32 s92, s92, 3\n"
      "s_lshl_b32 s93, s93, 7\n"
      "v_add_u32 v165, s92, v8\n"
      "v_bfe_u32 v236, v165, 1, 3\n"
      "v_xor_b32 v236, v236, v166\n"
      "v_lshl_add_u32 v236, v236, 4, s93\n"
      "v_mul_u32_u24 v239, 37, v165\n"
      "v_lshrrev_b32_e64 v239, 8, v239\n"
      "v_mad_u32_u24 v16, v239, s95, v165\n"
      "v_mul_u32_u24 v16, 384, v16\n"
      "v_add_u32 v16, v16, v236\n"
      "s_add_u32 s92, s40, 4\n"
      "s_mul_i32 s93, s92, 43\n"
      "s_lshr_b32 s93, s93, 8\n"
      "s_mul_i32 s94, s93, 6\n"
      "s_sub_u32 s92, s92, s94\n"
      "s_lshl_b32 s92, s92, 3\n"
      "s_lshl_b32 s93, s93, 7\n"
      "v_add_u32 v236, s92, v8\n"
      "v_bfe_u32 v239, v236, 1, 3\n"
      "v_xor_b32 v239, v239, v166\n"
      "v_lshl_add_u32 v239, v239, 4, s93\n"
      "v_mul_u32_u24 v165, 37, v236\n"
      "v_lshrrev_b32 v165, 8, v165\n"
      "v_mad_u32_u24 v17, v165, s95, v236\n"
      "v_mul_u32_u24 v17, 384, v17\n"
      "v_add_u32 v17, v17, v239\n"
      "s_add_u32 s92, s40, 8\n"
      "s_mul_i32 s93, s92, 43\n"
      "s_lshr_b32 s93, s93, 8\n"
      "s_mul_i32 s94, s93, 6\n"
      "s_sub_u32 s92, s92, s94\n"
      "s_lshl_b32 s92, s92, 3\n"
      "s_lshl_b32 s93, s93, 7\n"
      "v_add_u32_e64 v236, s92, v8\n"
      "v_bfe_u32 v165, v236, 1, 3\n"
      "v_xor_b32 v165, v165, v166\n"
      "v_lshl_add_u32 v165, v165, 4, s93\n"
      "v_mul_u32_u24 v239, 37, v236\n"
      "v_lshrrev_b32_e64 v239, 8, v239\n"
      "v_mad_u32_u24 v18, v239, s95, v236\n"
      "v_mul_u32_u24 v18, 384, v18\n"
      "v_add_u32 v18, v18, v165\n"
      "s_add_u32 s92, s40, 12\n"
      "s_mul_i32 s93, s92, 43\n"
      "s_lshr_b32 s93, s93, 8\n"
      "s_mul_i32 s94, s93, 6\n"
      "s_sub_u32 s92, s92, s94\n"
      "s_lshl_b32 s92, s92, 3\n"
      "s_lshl_b32 s93, s93, 7\n"
      "v_add_u32 v236, s92, v8\n"
      "v_bfe_u32 v239, v236, 1, 3\n"
      "v_xor_b32 v239, v239, v166\n"
      "v_lshl_add_u32 v239, v239, 4, s93\n"
      "v_mul_u32_u24 v165, 37, v236\n"
      "v_lshrrev_b32 v165, 8, v165\n"
      "v_mad_u32_u24 v19, v165, s95, v236\n"
      "v_mul_u32_u24 v19, 384, v19\n"
      "v_add_u32 v19, v19, v239\n"
      "s_add_u32 s92, s40, 16\n"
      "s_mul_i32 s93, s92, 43\n"
      "s_lshr_b32 s93, s93, 8\n"
      "s_mul_i32 s94, s93, 6\n"
      "s_sub_u32 s92, s92, s94\n"
      "s_lshl_b32 s92, s92, 3\n"
      "s_lshl_b32 s93, s93, 7\n"
      "s_cmp_lt_u32 s40, 2\n"
      "s_cselect_b32 s93, s93, 0x80000000\n"
      "v_add_u32 v236, s92, v8\n"
      "v_bfe_u32 v239, v236, 1, 3\n"
      "v_xor_b32_e64 v239, v239, v166\n"
      "v_lshl_add_u32 v239, v239, 4, s93\n"
      "v_mul_u32_u24_e64 v165, 37, v236\n"
      "v_lshrrev_b32_e64 v165, 8, v165\n"
      "v_mad_u32_u24 v15, v165, s95, v236\n"
      "v_mul_u32_u24 v15, 384, v15\n"
      "v_add_u32 v15, v15, v239\n"
      "s_add_u32 s92, s41, 0\n"
      "s_mul_i32 s93, s92, 58255\n"
      "s_lshr_b32 s93, s93, 22\n"
      "s_add_u32 s47, s92, s93\n"
      "s_mul_i32 s47, s47, 4802\n"
      "s_and_b32 s47, s47, -4\n"
      "s_mul_i32 s93, s93, 439\n"
      "s_add_u32 s45, s92, s93\n"
      "s_mul_i32 s45, s45, 2688\n"
      "s_add_u32 s45, s45, 589824\n"
      "s_add_u32 s92, s41, 256\n"
      "s_mul_i32 s93, s92, 58255\n"
      "s_lshr_b32 s93, s93, 22\n"
      "s_add_u32 s74, s92, s93\n"
      "s_mul_i32 s74, s74, 4802\n"
      "s_and_b32 s74, s74, -4\n"
      "s_mul_i32 s93, s93, 439\n"
      "s_add_u32 s46, s92, s93\n"
      "s_mul_i32 s46, s46, 2688\n"
      "s_add_u32 s46, s46, 589824\n"
      "s_add_u32 m0, s64, 0\n"
      "s_nop 0\n"
      "buffer_load_dwordx4 v16, s[16:19], s45 offen  lds\n"
      "s_add_u32 m0, s64, 4096\n"
      "s_nop 0\n"
      "buffer_load_dwordx4 v17, s[16:19], s45 offen  lds\n"
      "s_add_u32 m0, s64, 8192\n"
      "s_nop 0\n"
      "buffer_load_dwordx4 v18, s[16:19], s45 offen  lds\n"
      "s_add_u32 m0, s64, 12288\n"
      "s_nop 0\n"
      "buffer_load_dwordx4 v19, s[16:19], s45 offen  lds\n"
      "s_add_u32 m0, s65, 16384\n"
      "s_nop 0\n"
      "buffer_load_dwordx4 v15, s[16:19], s45 offen  lds\n"
      "v_subrev_u32 v165, 2, v167\n"
      "v_cmp_ge_u32 s[72:73], s44, v165\n"
      "v_min_u32_e64 v165, s44, v165\n"
      "s_movk_i32 s93, 4180\n"
      "s_cmp_lt_u32 s41, 209\n"
      "s_cselect_b32 s92, 209, 47\n"
      "s_cselect_b32 s93, 795, s93\n"
      "s_cmp_lt_u32 s41, 252\n"
      "s_cselect_b32 s94, 16, 15\n"
      "s_add_u32 s93, s93, s41\n"
      "v_lshl_add_u32 v236, v165, 8, s41\n"
      "v_mul_u32_u24 v239, s92, v165\n"
      "v_add_u32 v239, s93, v239\n"
      "v_cmp_lt_u32 vcc, s94, v165\n"
      "v_subrev_u32 v40, 5184, v239\n"
      "s_nop 0\n"
      "v_cndmask_b32 v236, v236, v239, vcc\n"
      "v_cmp_le_u32 vcc, 5184, v236\n"
      "v_mul_u32_u24 v49, 58255, v236\n"
      "v_lshrrev_b32 v49, 22, v49\n"
      "v_mul_u32_u24 v48, 72, v49\n"
      "v_sub_u32 v48, v236, v48\n"
      "v_min_u32 v239, 72, v40\n"
      "v_cndmask_b32 v49, v49, v239, vcc\n"
      "v_subrev_u32 v239, 72, v40\n"
      "v_min_u32 v239, 72, v239\n"
      "v_cndmask_b32 v48, v48, v239, vcc\n"
      "v_mul_u32_u24 v38, 73, v49\n"
      "v_add_u32 v38, v38, v48\n"
      "v_mul_u32_u24 v38, 4802, v38\n"
      "v_mul_u32_u24 v165, 0x1ff, v49\n"
      "v_add_u32_e64 v165, v165, v48\n"
      "v_mul_u32_u24 v165, 2688, v165\n"
      "v_add_u32 v165, 589824, v165\n"
      "v_cmp_eq_u32 vcc, 72, v49\n"
      "v_or_b32 v239, 0x80000000, v165\n"
      "s_nop 0\n"
      "v_cndmask_b32 v165, v165, v239, vcc\n"
      "v_cmp_eq_u32 vcc, 72, v48\n"
      "v_or_b32_e64 v239, 0x40000000, v165\n"
      "s_nop 0\n"
      "v_cndmask_b32 v165, v165, v239, vcc\n"
      "v_mov_b32 v236, 0x20000000\n"
      "v_cndmask_b32 v165, v236, v165, s[72:73]\n"
      "v_cndmask_b32 v38, v236, v38, s[72:73]\n"
      "v_mov_b32 v37, v236\n"
      "s_nop 1\n"
      "v_mov_b32_dpp v37, v165 wave_shr:1 row_mask:0xf bank_mask:0xf\n"
      "v_mov_b32 v79, 0\n"
      "v_mov_b32 v83, 0\n"
      "v_mov_b32 v87, 0\n"
      "v_mov_b32 v91, 0\n"
      "v_mov_b32 v95, 0\n"
      "s_lshl_b32 s94, s40, 2\n"
      "s_add_u32 s94, s94, 115456\n"
      "v_add_u32 v31, s94, v41\n"
      "v_and_b32 v239, 3, v167\n"
      "v_cmp_ne_u32 vcc, 0, v239\n"
      "v_mov_b32 v239, 0x3f803f80\n"
      "s_add_u32 s33, s64, 18432\n"
      "v_cndmask_b32_e64 v32, v239, 0, vcc\n"
      "v_mov_b32 v33, v32\n"
      "v_mov_b32 v34, v32\n"
      "v_mov_b32 v35, v32\n"
      "s_mov_b32 s22, 100270464\n"
      "s_mov_b32 s23, 0x20000\n"
      "s_mov_b32 s38, 73728\n"
      "s_mov_b32 s39, 0x20000\n"
      "s_mov_b32 s26, 25589860\n"
      "s_mov_b32 s27, 0x20000\n"
      "s_mov_b32 s30, 221184\n"
      "s_mov_b32 s31, 0x20000\n"
      "v_and_b32 v56, 15, v167\n"
      "v_lshrrev_b32 v61, 4, v167\n"
      "v_mul_u32_u24 v56, 196, v56\n"
      "v_add_u32 v56, 92160, v56\n"
      "v_lshl_add_u32 v61, v61, 4, v56\n"
      "v_min_u32 v52, 192, v237\n"
      "v_add_u32 v52, 92160, v52\n"
      "s_and_b32 s92, s40, 1\n"
      "s_lshl_b32 s92, s92, 2\n"
      "s_mul_i32 s93, s40, 9216\n"
      "v_and_b32 v239, 2, v8\n"
      "v_xor_b32 v239, v239, v166\n"
      "v_xor_b32_e64 v239, s92, v239\n"
      "v_lshl_add_u32 v239, v239, 4, s93\n"
      "v_mul_u32_u24 v114, 1152, v8\n"
      "v_add_u32 v114, v114, v239\n"
      "s_mul_i32 s92, s40, 8192\n"
      "s_add_u32 s88, s88, s92\n"
      "s_addc_u32 s89, s89, 0\n"
      "s_waitcnt lgkmcnt(0)\n"
      "s_pack_ll_b32_b16 s92, s0, s8\n"
      "s_waitcnt vmcnt(12)\n"
      "v_dot2_f32_bf16 v8, s92, v164, 0\n"
      "s_pack_hh_b32_b16 s93, s0, s8\n"
      "v_dot2_f32_bf16 v166, s93, v164, 0\n"
      "s_pack_ll_b32_b16 s94, s1, s9\n"
      "v_dot2_f32_bf16 v167, s94, v164, 0\n"
      "s_pack_hh_b32_b16 s95, s1, s9\n"
      "v_dot2_f32_bf16 v236, s95, v164, 0\n"
      "s_pack_ll_b32_b16 s92, s2, s10\n"
      "v_dot2_f32_bf16 v237, s92, v164, 0\n"
      "s_pack_hh_b32_b16 s93, s2, s10\n"
      "v_dot2_f32_bf16 v165, s93, v164, 0\n"
      "s_pack_ll_b32_b16 s94, s3, s11\n"
      "v_dot2_f32_bf16 v48, s94, v164, 0\n"
      "s_pack_hh_b32_b16 s95, s3, s11\n"
      "v_dot2_f32_bf16 v40, s95, v164, 0\n"
      "s_pack_ll_b32_b16 s92, s4, s12\n"
      "v_dot2_f32_bf16 v49, s92, v164, 0\n"
      "s_pack_hh_b32_b16 s93, s4, s12\n"
      "v_dot2_f32_bf16 v50, s93, v164, 0\n"
      "s_pack_ll_b32_b16 s94, s5, s13\n"
      "v_dot2_f32_bf16 v45, s94, v164, 0\n"
      "s_pack_hh_b32_b16 s95, s5, s13\n"
      "v_dot2_f32_bf16 v51, s95, v164, 0\n"
      "s_pack_ll_b32_b16 s92, s6, s14\n"
      "v_dot2_f32_bf16 v46, s92, v164, 0\n"
      "s_pack_hh_b32_b16 s93, s6, s14\n"
      "v_dot2_f32_bf16 v42, s93, v164, 0\n"
      "s_pack_ll_b32_b16 s94, s7, s15\n"
      "v_dot2_f32_bf16 v57, s94, v164, 0\n"
      "s_pack_hh_b32_b16 s95, s7, s15\n"
      "v_dot2_f32_bf16 v43, s95, v164, 0\n"
      "s_load_dwordx16 s[0:15], s[84:85], 64\n"
      "s_lshl_b32 s92, s40, 4\n"
      "s_lshr_b64 s[66:67], s[66:67], s92\n"
      "s_lshl_b32 s66, s66, 16\n"
      "v_mov_b32_e64 v164, s66\n"
      "v_max_f32 v164, 0xc2c80000, v164\n"
      "v_min_f32 v164, 0x40930000, v164\n"
      "v_mul_f32 v164, 0x3fb8aa3b, v164\n"
      "v_exp_f32 v164, v164\n"
      "s_nop 0\n"
      "v_cvt_pk_bf16_f32 v164, v164, v164\n"
      "v_lshlrev_b32 v164, 16, v164\n"
      "v_max_f32 v8, 0, v8\n"
      "v_max_f32 v166, 0, v166\n"
      "v_cvt_pk_bf16_f32 v8, v8, v166\n"
      "v_max_f32 v167, 0, v167\n"
      "v_max_f32 v236, 0, v236\n"
      "v_cvt_pk_bf16_f32 v167, v167, v236\n"
      "v_max_f32 v237, 0, v237\n"
      "v_max_f32 v165, 0, v165\n"
      "v_cvt_pk_bf16_f32 v237, v237, v165\n"
      "v_max_f32 v48, 0, v48\n"
      "v_max_f32 v40, 0, v40\n"
      "v_cvt_pk_bf16_f32 v48, v48, v40\n"
      "v_max_f32 v49, 0, v49\n"
      "v_max_f32 v50, 0, v50\n"
      "v_cvt_pk_bf16_f32 v49, v49, v50\n"
      "v_max_f32 v45, 0, v45\n"
      "v_max_f32 v51, 0, v51\n"
      "v_cvt_pk_bf16_f32 v45, v45, v51\n"
      "v_max_f32 v46, 0, v46\n"
      "v_max_f32 v42, 0, v42\n"
      "v_cvt_pk_bf16_f32 v46, v46, v42\n"
      "v_max_f32 v57, 0, v57\n"
      "v_max_f32 v43, 0, v43\n"
      "v_cvt_pk_bf16_f32 v57, v57, v43\n"
      "s_pack_ll_b32_b16 s92, s48, s50\n"
      "v_dot2_f32_bf16 v165, s92, v8, 0\n"
      "s_pack_hh_b32_b16 s93, s48, s50\n"
      "v_dot2_f32_bf16 v236, s93, v8, 0\n"
      "s_pack_ll_b32_b16 s94, s49, s51\n"
      "v_dot2_f32_bf16 v166, s94, v8, 0\n"
      "s_pack_hh_b32_b16 s95, s49, s51\n"
      "v_dot2_f32_bf16 v43, s95, v8, 0\n"
      "s_pack_ll_b32_b16 s92, s52, s54\n"
      "v_dot2_f32_bf16 v165, s92, v167, v165\n"
      "s_pack_hh_b32_b16 s93, s52, s54\n"
      "v_dot2_f32_bf16 v236, s93, v167, v236\n"
      "s_pack_ll_b32_b16 s94, s53, s55\n"
      "v_dot2_f32_bf16 v166, s94, v167, v166\n"
      "s_pack_hh_b32_b16 s95, s53, s55\n"
      "v_dot2_f32_bf16 v43, s95, v167, v43\n"
      "s_pack_ll_b32_b16 s92, s56, s58\n"
      "v_dot2_f32_bf16 v165, s92, v237, v165\n"
      "s_pack_hh_b32_b16 s93, s56, s58\n"
      "v_dot2_f32_bf16 v236, s93, v237, v236\n"
      "s_pack_ll_b32_b16 s94, s57, s59\n"
      "v_dot2_f32_bf16 v166, s94, v237, v166\n"
      "s_pack_hh_b32_b16 s95, s57, s59\n"
      "v_dot2_f32_bf16 v43, s95, v237, v43\n"
      "s_pack_ll_b32_b16 s92, s60, s62\n"
      "v_dot2_f32_bf16 v165, s92, v48, v165\n"
      "s_pack_hh_b32_b16 s93, s60, s62\n"
      "v_dot2_f32_bf16 v236, s93, v48, v236\n"
      "s_pack_ll_b32_b16 s94, s61, s63\n"
      "v_dot2_f32_bf16 v166, s94, v48, v166\n"
      "s_pack_hh_b32_b16 s95, s61, s63\n"
      "v_dot2_f32_bf16 v43, s95, v48, v43\n"
      "s_waitcnt lgkmcnt(0)\n"
      "s_pack_ll_b32_b16 s92, s0, s2\n"
      "v_dot2_f32_bf16 v165, s92, v49, v165\n"
      "s_pack_hh_b32_b16 s93, s0, s2\n"
      "v_dot2_f32_bf16 v236, s93, v49, v236\n"
      "s_pack_ll_b32_b16 s94, s1, s3\n"
      "v_dot2_f32_bf16 v166, s94, v49, v166\n"
      "s_pack_hh_b32_b16 s95, s1, s3\n"
      "v_dot2_f32_bf16 v43, s95, v49, v43\n"
      "s_pack_ll_b32_b16 s92, s4, s6\n"
      "v_dot2_f32_bf16 v165, s92, v45, v165\n"
      "s_pack_hh_b32_b16 s93, s4, s6\n"
      "v_dot2_f32_bf16 v236, s93, v45, v236\n"
      "s_pack_ll_b32_b16 s94, s5, s7\n"
      "v_dot2_f32_bf16 v166, s94, v45, v166\n"
      "s_pack_hh_b32_b16 s95, s5, s7\n"
      "v_dot2_f32_bf16 v43, s95, v45, v43\n"
      "s_pack_ll_b32_b16 s92, s8, s10\n"
      "v_dot2_f32_bf16 v165, s92, v46, v165\n"
      "s_pack_hh_b32_b16 s93, s8, s10\n"
      "v_dot2_f32_bf16 v236, s93, v46, v236\n"
      "s_pack_ll_b32_b16 s94, s9, s11\n"
      "v_dot2_f32_bf16 v166, s94, v46, v166\n"
      "s_pack_hh_b32_b16 s95, s9, s11\n"
      "v_dot2_f32_bf16 v43, s95, v46, v43\n"
      "s_pack_ll_b32_b16 s92, s12, s14\n"
      "v_dot2_f32_bf16 v165, s92, v57, v165\n"
      "s_pack_hh_b32_b16 s93, s12, s14\n"
      "v_dot2_f32_bf16 v236, s93, v57, v236\n"
      "s_pack_ll_b32_b16 s94, s13, s15\n"
      "v_dot2_f32_bf16 v166, s94, v57, v166\n"
      "s_pack_hh_b32_b16 s95, s13, s15\n"
      "v_dot2_f32_bf16 v43, s95, v57, v43\n"
      "s_mov_b32 s53, 0x3fb8aa3b\n"
      "s_mov_b32 s51, 0x2a8cbccc\n"
      "s_mov_b32 s52, 0x38fba882\n"
      "s_mov_b32 s58, 0xffff0000\n"
      "s_mov_b32 s59, 0x0\n"
      "s_mov_b32 s60, 0x0\n"
      "s_mov_b32 s61, 0xffff\n"
      "s_mov_b32 s62, 0x0\n"
      "s_mov_b32 s63, 0xffff0000\n"
      "v_mov_b32 v239, 0x41f00000\n"
      "v_cmp_nle_f32 s[54:55], v164, v239\n"
      "v_mul_f32 v164, s53, v164\n"
      "s_nop 0\n"
      "v_readfirstlane_b32 s50, v164\n"
      "v_cvt_pk_bf16_f32 v165, v165, v165\n"
      "v_lshlrev_b32 v165, 16, v165\n"
      "v_mul_f32 v165, 0xbfb8aa3b, v165\n"
      "v_exp_f32 v165, v165\n"
      "s_nop 0\n"
      "v_add_f32 v165, 1.0, v165\n"
      "v_rcp_f32 v165, v165\n"
      "s_nop 0\n"
      "v_cvt_pk_bf16_f32 v165, v165, v165\n"
      "v_lshlrev_b32 v165, 16, v165\n"
      "v_add_f32 v165, -0.5, v165\n"
      "v_mul_f32 v165, 0x41b8aa3b, v165\n"
      "ds_write_b32 v238, v165\n"
      "v_cvt_pk_bf16_f32 v236, v236, v236\n"
      "v_lshlrev_b32_e64 v236, 16, v236\n"
      "v_mul_f32 v236, 0xbfb8aa3b, v236\n"
      "v_exp_f32 v236, v236\n"
      "s_nop 0\n"
      "v_add_f32 v236, 1.0, v236\n"
      "v_rcp_f32_e64 v236, v236\n"
      "s_nop 0\n"
      "v_cvt_pk_bf16_f32 v236, v236, v236\n"
      "v_lshlrev_b32 v236, 16, v236\n"
      "v_add_f32 v236, -0.5, v236\n"
      "v_mul_f32 v236, 0x41b8aa3b, v236\n"
      "ds_write_b32 v238, v236 offset:704\n"
      "v_cvt_pk_bf16_f32 v166, v166, v166\n"
      "v_lshlrev_b32 v166, 16, v166\n"
      "v_mul_f32 v166, 0xbfb8aa3b, v166\n"
      "v_exp_f32 v166, v166\n"
      "s_nop 0\n"
      "v_add_f32 v166, 1.0, v166\n"
      "v_rcp_f32 v166, v166\n"
      "s_nop 0\n"
      "v_cvt_pk_bf16_f32 v166, v166, v166\n"
      "v_lshlrev_b32 v166, 16, v166\n"
      "v_add_f32 v166, -0.5, v166\n"
      "v_mul_f32 v166, 0x41b8aa3b, v166\n"
      "ds_write_b32 v238, v166 offset:1408\n"
      "v_cvt_pk_bf16_f32 v43, v43, v43\n"
      "v_lshlrev_b32 v43, 16, v43\n"
      "v_mul_f32 v43, 0xbfb8aa3b, v43\n"
      "v_exp_f32 v43, v43\n"
      "s_nop 0\n"
      "v_add_f32 v43, 1.0, v43\n"
      "v_rcp_f32 v43, v43\n"
      "s_nop 0\n"
      "v_cvt_pk_bf16_f32 v43, v43, v43\n"
      "v_lshlrev_b32_e64 v43, 16, v43\n"
      "v_add_f32 v43, -0.5, v43\n"
      "v_mul_f32 v43, 0x41b8aa3b, v43\n"
      "ds_write_b32 v238, v43 offset:2112\n"
      "s_mul_i32 s76, s40, 704\n"
      "s_add_u32 s76, s76, 122112\n"
      "s_mov_b32 s78, 0\n"
      "s_add_u32 m0, s33, 0\n"
      "s_nop 0\n"
      "buffer_load_dwordx4 v114, s[28:31], s78 offen  lds\n"
      "s_add_u32 m0, s33, 3968\n"
      "s_nop 0\n"
      "buffer_load_dwordx4 v114, s[28:31], s78 offen offset:128  lds\n"
      "s_add_u32 m0, s33, 7936\n"
      "s_nop 0\n"
      "buffer_load_dwordx4 v114, s[28:31], s78 offen offset:256  lds\n"
      "s_add_u32 m0, s33, 11904\n"
      "s_nop 0\n"
      "buffer_load_dwordx4 v114, s[28:31], s78 offen offset:384  lds\n"
      "s_add_u32 m0, s33, 15872\n"
      "s_nop 0\n"
      "buffer_load_dwordx4 v114, s[28:31], s78 offen offset:512  lds\n"
      "s_add_u32 m0, s33, 19840\n"
      "s_nop 0\n"
      "buffer_load_dwordx4 v114, s[28:31], s78 offen offset:640  lds\n"
      "s_waitcnt vmcnt(14)\n"
      "s_waitcnt lgkmcnt(0)\n"
      "s_barrier\n"
      "global_load_dwordx4 v[236:239], v41, s[88:89]\n"
      "global_load_dwordx4 v[164:167], v41, s[88:89] offset:1024\n"
      "global_load_dwordx4 v[0:3], v41, s[88:89] offset:2048\n"
      "global_load_dwordx4 v[4:7], v41, s[88:89] offset:3072\n"
      "s_add_u32 s88, s88, 4096\n"
      "s_addc_u32 s89, s89, 0\n"
      "global_load_dwordx4 v[8:11], v41, s[88:89]\n"
      "global_load_dwordx4 v[12:15], v41, s[88:89] offset:1024\n"
      "global_load_dwordx4 v[20:23], v41, s[88:89] offset:2048\n"
      "global_load_dwordx4 v[24:27], v41, s[88:89] offset:3072\n"
      "s_mov_b32 s79, 36864\n"
      "s_add_u32 m0, s33, 24576\n"
      "s_nop 0\n"
      "buffer_load_dwordx4 v114, s[28:31], s79 offen  lds\n"
      "s_add_u32 m0, s33, 28544\n"
      "s_nop 0\n"
      "buffer_load_dwordx4 v114, s[28:31], s79 offen offset:128  lds\n"
      "s_add_u32 m0, s33, 32512\n"
      "s_nop 0\n"
      "buffer_load_dwordx4 v114, s[28:31], s79 offen offset:256  lds\n"
      "s_add_u32 m0, s33, 36480\n"
      "s_nop 0\n"
      "buffer_load_dwordx4 v114, s[28:31], s79 offen offset:384  lds\n"
      "s_add_u32 m0, s33, 40448\n"
      "s_nop 0\n"
      "buffer_load_dwordx4 v114, s[28:31], s79 offen offset:512  lds\n"
      "s_add_u32 m0, s33, 44416\n"
      "s_nop 0\n"
      "buffer_load_dwordx4 v114, s[28:31], s79 offen offset:640  lds\n"
      "ds_read2_b32 v[40:41], v61 offset0:0 offset1:1\n"
      "ds_read2_b32 v[42:43], v61 offset0:2 offset1:3\n"
      "ds_read2_b32 v[44:45], v61 offset0:16 offset1:17\n"
      "ds_read2_b32 v[46:47], v61 offset0:18 offset1:19\n"
      "ds_read2_b32 v[48:49], v61 offset0:32 offset1:33\n"
      "ds_read2_b32 v[50:51], v61 offset0:34 offset1:35\n"
      "ds_read_b32 v242, v56 offset:192\n"
      "ds_read_b32 v233, v56 offset:3328\n"
      "ds_read_b32 v241, v56 offset:6464\n"
      "ds_read_b32 v243, v52 offset:9408\n"
      "s_mov_b32 s72, 0xffff\n"
      "s_mov_b32 s73, 0x0\n"
      "s_mov_b32 s66, 0xffffffff\n"
      "s_mov_b32 s67, 0x1ffff\n"
      "v_add_u32 v64, 3136, v61\n"
      "v_add_u32 v240, 6272, v61\n"
      "ds_read2_b32 v[52:53], v64 offset0:0 offset1:1\n"
      "ds_read2_b32 v[54:55], v64 offset0:2 offset1:3\n"
      "ds_read2_b32 v[56:57], v64 offset0:16 offset1:17\n"
      "ds_read2_b32 v[58:59], v64 offset0:18 offset1:19\n"
      "ds_read2_b32 v[60:61], v64 offset0:32 offset1:33\n"
      "ds_read2_b32 v[62:63], v64 offset0:34 offset1:35\n"
      "ds_read2_b32 v[64:65], v240 offset0:0 offset1:1\n"
      "ds_read2_b32 v[66:67], v240 offset0:2 offset1:3\n"
      "ds_read2_b32 v[68:69], v240 offset0:16 offset1:17\n"
      "ds_read2_b32 v[70:71], v240 offset0:18 offset1:19\n"
      "ds_read2_b32 v[72:73], v240 offset0:32 offset1:33\n"
      "ds_read2_b32 v[74:75], v240 offset0:34 offset1:35\n"
      "s_waitcnt lgkmcnt(11)\n"
      "v_lshl_add_u32 v40, v40, 2, s76\n"
      "v_lshl_add_u32 v41, v41, 2, s76\n"
      "v_lshl_add_u32 v42, v42, 2, s76\n"
      "v_lshl_add_u32 v43, v43, 2, s76\n"
      "v_lshl_add_u32 v44, v44, 2, s76\n"
      "v_lshl_add_u32 v45, v45, 2, s76\n"
      "v_lshl_add_u32 v46, v46, 2, s76\n"
      "v_lshl_add_u32 v47, v47, 2, s76\n"
      "v_lshl_add_u32 v48, v48, 2, s76\n"
      "v_lshl_add_u32 v49, v49, 2, s76\n"
      "v_lshl_add_u32 v50, v50, 2, s76\n"
      "v_lshl_add_u32 v51, v51, 2, s76\n"
      "v_lshl_add_u32 v52, v52, 2, s76\n"
      "v_lshl_add_u32 v53, v53, 2, s76\n"
      "s_waitcnt lgkmcnt(4)\n"
      "v_lshl_add_u32 v54, v54, 2, s76\n"
      "v_lshl_add_u32 v55, v55, 2, s76\n"
      "v_lshl_add_u32 v56, v56, 2, s76\n"
      "v_lshl_add_u32 v57, v57, 2, s76\n"
      "v_lshl_add_u32 v58, v58, 2, s76\n"
      "v_lshl_add_u32 v59, v59, 2, s76\n"
      "v_lshl_add_u32 v60, v60, 2, s76\n"
      "v_lshl_add_u32 v61, v61, 2, s76\n"
      "v_lshl_add_u32 v62, v62, 2, s76\n"
      "v_lshl_add_u32 v63, v63, 2, s76\n"
      "v_lshl_add_u32 v64, v64, 2, s76\n"
      "v_lshl_add_u32 v65, v65, 2, s76\n"
      "v_lshl_add_u32 v66, v66, 2, s76\n"
      "v_lshl_add_u32 v67, v67, 2, s76\n"
      "s_waitcnt lgkmcnt(0)\n"
      "v_lshl_add_u32 v68, v68, 2, s76\n"
      "v_lshl_add_u32 v69, v69, 2, s76\n"
      "v_lshl_add_u32 v70, v70, 2, s76\n"
      "v_lshl_add_u32 v71, v71, 2, s76\n"
      "v_lshl_add_u32 v72, v72, 2, s76\n"
      "v_lshl_add_u32 v73, v73, 2, s76\n"
      "v_lshl_add_u32 v74, v74, 2, s76\n"
      "v_lshl_add_u32 v75, v75, 2, s76\n"
      "v_lshl_add_u32 v242, v242, 2, s76\n"
      "v_lshl_add_u32 v233, v233, 2, s76\n"
      "v_lshl_add_u32 v241, v241, 2, s76\n"
      "v_lshl_add_u32 v243, v243, 2, s76\n"
      "ds_read_b32 v40, v40\n"
      "ds_read_b32 v41, v41\n"
      "ds_read_b32 v42, v42\n"
      "ds_read_b32 v43, v43\n"
      "ds_read_b32 v44, v44\n"
      "ds_read_b32 v45, v45\n"
      "ds_read_b32 v46, v46\n"
      "ds_read_b32 v47, v47\n"
      "ds_read_b32 v48, v48\n"
      "ds_read_b32 v49, v49\n"
      "ds_read_b32 v50, v50\n"
      "ds_read_b32 v51, v51\n"
      "ds_read_b32 v52, v52\n"
      "ds_read_b32 v53, v53\n"
      "ds_read_b32 v54, v54\n"
      "ds_read_b32 v55, v55\n"
      "ds_read_b32 v56, v56\n"
      "ds_read_b32 v57, v57\n"
      "ds_read_b32 v58, v58\n"
      "ds_read_b32 v59, v59\n"
      "ds_read_b32 v60, v60\n"
      "ds_read_b32 v61, v61\n"
      "ds_read_b32 v62, v62\n"
      "ds_read_b32 v63, v63\n"
      "ds_read_b32 v64, v64\n"
      "ds_read_b32 v65, v65\n"
      "ds_read_b32 v66, v66\n"
      "ds_read_b32 v67, v67\n"
      "ds_read_b32 v68, v68\n"
      "ds_read_b32 v69, v69\n"
      "ds_read_b32 v70, v70\n"
      "ds_read_b32 v71, v71\n"
      "ds_read_b32 v72, v72\n"
      "ds_read_b32 v73, v73\n"
      "ds_read_b32 v74, v74\n"
      "ds_read_b32 v75, v75\n"
      "ds_read_b32 v242, v242\n"
      "ds_read_b32 v233, v233\n"
      "ds_read_b32 v241, v241\n"
      "ds_read_b32 v243, v243\n"
      "v_mov_b32 v240, 0xf149f2ca\n"
      "s_waitcnt lgkmcnt(3)\n"
      "v_cndmask_b32 v253, v240, v242, s[72:73]\n"
      "s_waitcnt lgkmcnt(2)\n"
      "v_cndmask_b32 v250, v240, v233, s[72:73]\n"
      "s_waitcnt lgkmcnt(1)\n"
      "v_cndmask_b32 v28, v240, v241, s[72:73]\n"
      "s_waitcnt lgkmcnt(0)\n"
      "v_cndmask_b32 v30, v240, v243, s[66:67]\n"
      "s_mov_b32 s78, 73728\n"
      "s_add_u32 m0, s33, 49152\n"
      "s_nop 0\n"
      "buffer_load_dwordx4 v114, s[28:31], s78 offen  lds\n"
      "s_add_u32 m0, s33, 53120\n"
      "s_nop 0\n"
      "buffer_load_dwordx4 v114, s[28:31], s78 offen offset:128  lds\n"
      "s_add_u32 m0, s33, 57088\n"
      "s_nop 0\n"
      "buffer_load_dwordx4 v114, s[28:31], s78 offen offset:256  lds\n"
      "s_add_u32 m0, s33, 61056\n"
      "s_nop 0\n"
      "buffer_load_dwordx4 v114, s[28:31], s78 offen offset:384  lds\n"
      "s_add_u32 m0, s33, 65024\n"
      "s_nop 0\n"
      "buffer_load_dwordx4 v114, s[28:31], s78 offen offset:512  lds\n"
      "s_add_u32 m0, s33, 68992\n"
      "s_nop 0\n"
      "buffer_load_dwordx4 v114, s[28:31], s78 offen offset:640  lds\n"
      "global_load_dword a253, v234, s[36:37]\n"
      "buffer_load_dwordx4 v[232:235], v36, s[24:27], s47 offen\n"
      "buffer_load_dword v115, v29, s[24:27], s47 offen\n"
      "buffer_load_dwordx4 v[240:243], v36, s[24:27], s74 offen\n"
      "buffer_load_dword v113, v29, s[24:27], s74 offen\n"
      "s_mov_b32 s45, s46\n"
      "s_mov_b32 s66, 0\n"
      "s_mov_b32 s67, 0\n"
      "s_mov_b32 s68, 0\n"
      "s_mov_b32 s69, 0\n"
      "s_mov_b32 s70, 0\n"
      "s_mov_b32 s71, 0\n"
      "s_mov_b32 s48, 0\n"
      "s_mov_b32 s42, 1\n"
      "s_mov_b32 s43, 5\n"
      "v_readlane_b32 s47, v38, 4\n"
      "s_nop 0\n"
      "s_and_b32 s47, s47, -4\n"
      "s_waitcnt vmcnt(22)\n"
      "v_add_u32 v134, 149504, v0\n"
      "v_add_u32 v135, 149504, v1\n"
      "v_add_u32 v248, 49152, v236\n"
      "v_add_u32 v181, 49152, v237\n"
      "v_add_u32 v249, 49152, v238\n"
      "v_add_u32 v76, 49152, v239\n"
      "v_add_u32 v84, 49152, v164\n"
      "s_waitcnt vmcnt(17)\n"
      "v_mov_b32_e64 v24, v253\n"
      "v_mov_b32_e64 v26, v250\n"
      "s_waitcnt lgkmcnt(0)\n"
      "s_barrier\n"
      "ds_read_b128 v[244:247], v134\n"
      "ds_read_b128 v[250:253], v0\n"
      "s_add_u32 m0, s33, 73728\n"
      "s_mov_b32 s79, 110592\n"
      "buffer_load_dwordx4 v114, s[28:31], s79 offen  lds\n"
      "s_add_u32 m0, s33, 77696\n"
      "ds_read_b64_tr_b16 a[0:1], v236\n"
      "ds_read_b64_tr_b16 a[2:3], v236 offset:512\n"
      "ds_read_b64_tr_b16 a[24:25], v237\n"
      "ds_read_b64_tr_b16 a[26:27], v237 offset:512\n"
      "ds_read_b64_tr_b16 a[48:49], v238\n"
      "ds_read_b64_tr_b16 a[50:51], v238 offset:512\n"
      "ds_read_b64_tr_b16 a[72:73], v236 offset:12288\n"
      "ds_read_b64_tr_b16 a[74:75], v236 offset:12800\n"
      "ds_read_b64_tr_b16 a[96:97], v237 offset:12288\n"
      "ds_read_b64_tr_b16 a[98:99], v237 offset:12800\n"
      "ds_read_b64_tr_b16 a[120:121], v238 offset:12288\n"
      "ds_read_b64_tr_b16 a[122:123], v238 offset:12800\n"
      "buffer_load_dwordx4 v114, s[28:31], s79 offen offset:128  lds\n"
      "s_waitcnt lgkmcnt(9)\n"
      "v_mfma_f32_16x16x32_bf16 v[226:229], a[0:3], v[244:247], 0\n"
      "s_waitcnt lgkmcnt(8)\n"
      "s_add_u32 m0, s33, 81664\n"
      "v_readlane_b32 s46, v37, s42\n"
      "v_mfma_f32_16x16x32_bf16 v[130:133], a[24:27], v[244:247], 0\n"
      "s_waitcnt lgkmcnt(6)\n"
      "buffer_load_dwordx4 v114, s[28:31], s79 offen offset:256  lds\n"
      "v_mfma_f32_16x16x32_bf16 v[214:217], a[48:51], v[244:247], 0\n"
      "s_waitcnt lgkmcnt(3)\n"
      "s_add_u32 m0, s33, 85632\n"
      "ds_read_b128 v[196:199], v0 offset:2048\n"
      "v_mfma_f32_16x16x32_bf16 v[210:213], a[72:75], v[244:247], 0\n"
      "s_waitcnt lgkmcnt(2)\n"
      "buffer_load_dwordx4 v114, s[28:31], s79 offen offset:384  lds\n"
      "v_mfma_f32_16x16x32_bf16 v[188:191], a[96:99], v[244:247], 0\n"
      "s_waitcnt lgkmcnt(1)\n"
      "s_add_u32 m0, s33, 89600\n"
      "ds_read_b128 v[140:143], v0 offset:4096\n"
      "v_mfma_f32_16x16x32_bf16 v[160:163], a[120:123], v[244:247], 0\n"
      "buffer_load_dwordx4 v114, s[28:31], s79 offen offset:512  lds\n"
      "v_mfma_f32_16x16x32_bf16 v[104:107], a[0:3], v[250:253], 0\n"
      "s_add_u32 m0, s33, 93568\n"
      "v_mfma_f32_16x16x32_bf16 v[184:187], a[24:27], v[250:253], 0\n"
      "buffer_load_dwordx4 v114, s[28:31], s79 offen offset:640  lds\n"
      "v_mfma_f32_16x16x32_bf16 v[152:155], a[48:51], v[250:253], 0\n"
      "s_waitcnt vmcnt(17)\n"
      "s_barrier\n"
      "s_add_u32 m0, s33, 0\n"
      "v_mfma_f32_16x16x32_bf16 v[96:99], a[72:75], v[250:253], 0\n"
      "s_waitcnt lgkmcnt(1)\n"
      "s_mov_b32 s78, 147456\n"
      "buffer_load_dwordx4 v114, s[28:31], s78 offen  lds\n"
      "v_mfma_f32_16x16x32_bf16 v[144:147], a[96:99], v[250:253], 0\n"
      "s_add_u32 m0, s33, 3968\n"
      "ds_read_b64_tr_b16 a[4:5], v236 offset:24576\n"
      "v_mfma_f32_16x16x32_bf16 v[100:103], a[120:123], v[250:253], 0\n"
      "buffer_load_dwordx4 v114, s[28:31], s78 offen offset:128  lds\n"
      "v_mfma_f32_16x16x32_bf16 v[108:111], a[0:3], v[196:199], 0\n"
      "ds_read_b64_tr_b16 a[6:7], v236 offset:25088\n"
      "ds_read_b64_tr_b16 a[28:29], v237 offset:24576\n"
      "v_mfma_f32_16x16x32_bf16 v[172:175], a[24:27], v[196:199], 0\n"
      "ds_read_b64_tr_b16 a[30:31], v237 offset:25088\n"
      "ds_read_b64_tr_b16 a[52:53], v238 offset:24576\n"
      "v_mfma_f32_16x16x32_bf16 v[124:127], a[48:51], v[196:199], 0\n"
      "ds_read_b64_tr_b16 a[54:55], v238 offset:25088\n"
      "ds_read_b64_tr_b16 a[76:77], v236 offset:36864\n"
      "v_mfma_f32_16x16x32_bf16 v[168:171], a[72:75], v[196:199], 0\n"
      "s_waitcnt lgkmcnt(7)\n"
      "ds_read_b64_tr_b16 a[78:79], v236 offset:37376\n"
      "v_mfma_f32_16x16x32_bf16 v[148:151], a[96:99], v[196:199], 0\n"
      "ds_read_b64_tr_b16 a[100:101], v237 offset:36864\n"
      "ds_read_b64_tr_b16 a[102:103], v237 offset:37376\n"
      "v_mfma_f32_16x16x32_bf16 v[206:209], a[120:123], v[196:199], 0\n"
      "ds_read_b64_tr_b16 a[124:125], v238 offset:36864\n"
      "ds_read_b64_tr_b16 a[126:127], v238 offset:37376\n"
      "v_mfma_f32_16x16x32_bf16 v[196:199], a[0:3], v[140:143], 0\n"
      "s_add_u32 m0, s33, 7936\n"
      "ds_read_b128 v[136:139], v135\n"
      "v_mfma_f32_16x16x32_bf16 v[218:221], a[24:27], v[140:143], 0\n"
      "buffer_load_dwordx4 v114, s[28:31], s78 offen offset:256  lds\n"
      "v_mfma_f32_16x16x32_bf16 v[176:179], a[48:51], v[140:143], 0\n"
      "s_add_u32 m0, s33, 11904\n"
      "ds_read_b128 v[120:123], v1\n"
      "v_mfma_f32_16x16x32_bf16 v[202:205], a[72:75], v[140:143], 0\n"
      "s_waitcnt lgkmcnt(6)\n"
      "buffer_load_dwordx4 v114, s[28:31], s78 offen offset:384  lds\n"
      "v_mfma_f32_16x16x32_bf16 v[116:119], a[96:99], v[140:143], 0\n"
      "s_add_u32 m0, s33, 15872\n"
      "ds_read_b128 v[156:159], v1 offset:2048\n"
      "v_mfma_f32_16x16x32_bf16 v[222:225], a[120:123], v[140:143], 0\n"
      "buffer_load_dwordx4 v114, s[28:31], s78 offen offset:512  lds\n"
      "s_waitcnt lgkmcnt(2)\n"
      "v_mfma_f32_16x16x32_bf16 v[226:229], a[4:7], v[136:139], v[226:229]\n"
      "s_add_u32 m0, s33, 19840\n"
      "s_waitcnt vmcnt(16)\n"
      "s_barrier\n"
      "v_mfma_f32_16x16x32_bf16 v[130:133], a[28:31], v[136:139], v[130:133]\n"
      "buffer_load_dwordx4 v114, s[28:31], s78 offen offset:640  lds\n"
      "v_mfma_f32_16x16x32_bf16 v[214:217], a[52:55], v[136:139], v[214:217]\n"
      "s_add_u32 m0, s33, 24576\n"
      "s_mov_b32 s79, 184320\n"
      "buffer_load_dwordx4 v114, s[28:31], s79 offen  lds\n"
      "v_mfma_f32_16x16x32_bf16 v[210:213], a[76:79], v[136:139], v[210:213]\n"
      "s_waitcnt lgkmcnt(0)\n"
      "s_add_u32 m0, s33, 28544\n"
      "ds_read_b64_tr_b16 a[8:9], v248\n"
      "v_mfma_f32_16x16x32_bf16 v[188:191], a[100:103], v[136:139], v[188:191]\n"
      "buffer_load_dwordx4 v114, s[28:31], s79 offen offset:128  lds\n"
      "v_mfma_f32_16x16x32_bf16 v[160:163], a[124:127], v[136:139], v[160:163]\n"
      "ds_read_b64_tr_b16 a[10:11], v248 offset:512\n"
      "ds_read_b64_tr_b16 a[32:33], v181\n"
      "v_mfma_f32_16x16x32_bf16 v[104:107], a[4:7], v[120:123], v[104:107]\n"
      "ds_read_b64_tr_b16 a[34:35], v181 offset:512\n"
      "ds_read_b64_tr_b16 a[56:57], v249\n"
      "v_mfma_f32_16x16x32_bf16 v[184:187], a[28:31], v[120:123], v[184:187]\n"
      "ds_read_b64_tr_b16 a[58:59], v249 offset:512\n"
      "ds_read_b64_tr_b16 a[80:81], v248 offset:12288\n"
      "v_mfma_f32_16x16x32_bf16 v[152:155], a[52:55], v[120:123], v[152:155]\n"
      "ds_read_b64_tr_b16 a[82:83], v248 offset:12800\n"
      "ds_read_b64_tr_b16 a[104:105], v181 offset:12288\n"
      "v_mfma_f32_16x16x32_bf16 v[96:99], a[76:79], v[120:123], v[96:99]\n"
      "ds_read_b64_tr_b16 a[106:107], v181 offset:12800\n"
      "ds_read_b64_tr_b16 a[128:129], v249 offset:12288\n"
      "v_mfma_f32_16x16x32_bf16 v[144:147], a[100:103], v[120:123], v[144:147]\n"
      "ds_read_b64_tr_b16 a[130:131], v249 offset:12800\n"
      "s_add_u32 m0, s33, 32512\n"
      "v_mfma_f32_16x16x32_bf16 v[100:103], a[124:127], v[120:123], v[100:103]\n"
      "buffer_load_dwordx4 v114, s[28:31], s79 offen offset:256  lds\n"
      "v_mfma_f32_16x16x32_bf16 v[108:111], a[4:7], v[156:159], v[108:111]\n"
      "s_add_u32 m0, s33, 36480\n"
      "ds_read_b128 v[140:143], v1 offset:4096\n"
      "v_mfma_f32_16x16x32_bf16 v[172:175], a[28:31], v[156:159], v[172:175]\n"
      "buffer_load_dwordx4 v114, s[28:31], s79 offen offset:384  lds\n"
      "v_mfma_f32_16x16x32_bf16 v[124:127], a[52:55], v[156:159], v[124:127]\n"
      "s_add_u32 m0, s33, 40448\n"
      "ds_read_b128 v[120:123], v134 offset:4096\n"
      "v_mfma_f32_16x16x32_bf16 v[168:171], a[76:79], v[156:159], v[168:171]\n"
      "buffer_load_dwordx4 v114, s[28:31], s79 offen offset:512  lds\n"
      "v_mfma_f32_16x16x32_bf16 v[148:151], a[100:103], v[156:159], v[148:151]\n"
      "s_add_u32 m0, s33, 44416\n"
      "ds_read_b128 v[136:139], v0 offset:6144\n"
      "v_mfma_f32_16x16x32_bf16 v[206:209], a[124:127], v[156:159], v[206:209]\n"
      "buffer_load_dwordx4 v114, s[28:31], s79 offen offset:640  lds\n"
      "s_waitcnt lgkmcnt(2)\n"
      "v_mfma_f32_16x16x32_bf16 v[196:199], a[4:7], v[140:143], v[196:199]\n"
      "s_add_u32 m0, s33, 48384\n"
      "v_mfma_f32_16x16x32_bf16 v[218:221], a[28:31], v[140:143], v[218:221]\n"
      "v_mfma_f32_16x16x32_bf16 v[176:179], a[52:55], v[140:143], v[176:179]\n"
      "s_waitcnt lgkmcnt(1)\n"
      "v_mfma_f32_16x16x32_bf16 v[202:205], a[76:79], v[140:143], v[202:205]\n"
      "v_mfma_f32_16x16x32_bf16 v[116:119], a[100:103], v[140:143], v[116:119]\n"
      "ds_read_b128 v[156:159], v0 offset:8192\n"
      "v_mfma_f32_16x16x32_bf16 v[222:225], a[124:127], v[140:143], v[222:225]\n"
      "s_waitcnt lgkmcnt(1)\n"
      "v_mfma_f32_16x16x32_bf16 v[226:229], a[8:11], v[120:123], v[226:229]\n"
      "v_mfma_f32_16x16x32_bf16 v[130:133], a[32:35], v[120:123], v[130:133]\n"
      "v_mfma_f32_16x16x32_bf16 v[214:217], a[56:59], v[120:123], v[214:217]\n"
      "v_mfma_f32_16x16x32_bf16 v[210:213], a[80:83], v[120:123], v[210:213]\n"
      "v_mfma_f32_16x16x32_bf16 v[188:191], a[104:107], v[120:123], v[188:191]\n"
      "ds_read_b128 v[140:143], v0 offset:10240\n"
      "v_mfma_f32_16x16x32_bf16 v[160:163], a[128:131], v[120:123], v[160:163]\n"
      "s_waitcnt lgkmcnt(1)\n"
      "v_mfma_f32_16x16x32_bf16 v[104:107], a[8:11], v[136:139], v[104:107]\n"
      "v_mfma_f32_16x16x32_bf16 v[184:187], a[32:35], v[136:139], v[184:187]\n"
      "s_waitcnt vmcnt(21)\n"
      "v_or3_b32 v232, v232, v233, v234\n"
      "s_waitcnt vmcnt(19)\n"
      "v_or3_b32 v240, v240, v241, v242\n"
      "v_mfma_f32_16x16x32_bf16 v[152:155], a[56:59], v[136:139], v[152:155]\n"
      "v_or3_b32 v232, v232, v235, v115\n"
      "s_waitcnt vmcnt(18)\n"
      "v_or3_b32 v240, v240, v243, v113\n"
      "v_mfma_f32_16x16x32_bf16 v[96:99], a[80:83], v[136:139], v[96:99]\n"
      "v_and_b32 v232, 0x7fff7fff, v232\n"
      "v_and_b32 v240, 0x7fff7fff, v240\n"
      "v_mfma_f32_16x16x32_bf16 v[144:147], a[104:107], v[136:139], v[144:147]\n"
      "ds_read_b128 v[242:245], v135 offset:4096\n"
      "ds_write_b32 v31, v232 offset:6656\n"
      "v_mfma_f32_16x16x32_bf16 v[100:103], a[128:131], v[136:139], v[100:103]\n"
      "ds_write_b32 v31, v240 offset:5632\n"
      "v_mfma_f32_16x16x32_bf16 v[108:111], a[8:11], v[156:159], v[108:111]\n"
      "s_waitcnt vmcnt(12)\n"
      "s_barrier\n"
      "s_mov_b32 s78, 0\n"
      "v_mfma_f32_16x16x32_bf16 v[172:175], a[32:35], v[156:159], v[172:175]\n"
      "buffer_load_dwordx4 v114, s[28:31], s78 offen offset:768  lds\n"
      "v_mfma_f32_16x16x32_bf16 v[124:127], a[56:59], v[156:159], v[124:127]\n"
      "s_add_u32 m0, s33, 52352\n"
      "ds_read_b64_tr_b16 a[12:13], v248 offset:24576\n"
      "v_mfma_f32_16x16x32_bf16 v[168:171], a[80:83], v[156:159], v[168:171]\n"
      "s_waitcnt lgkmcnt(4)\n"
      "buffer_load_dwordx4 v114, s[28:31], s78 offen offset:896  lds\n"
      "v_mfma_f32_16x16x32_bf16 v[148:151], a[104:107], v[156:159], v[148:151]\n"
      "ds_read_b64_tr_b16 a[14:15], v248 offset:25088\n"
      "ds_read_b64_tr_b16 a[36:37], v181 offset:24576\n"
      "v_mfma_f32_16x16x32_bf16 v[206:209], a[128:131], v[156:159], v[206:209]\n"
      "ds_read_b64_tr_b16 a[38:39], v181 offset:25088\n"
      "ds_read_b64_tr_b16 a[60:61], v249 offset:24576\n"
      "v_mfma_f32_16x16x32_bf16 v[196:199], a[8:11], v[140:143], v[196:199]\n"
      "ds_read_b64_tr_b16 a[62:63], v249 offset:25088\n"
      "ds_read_b64_tr_b16 a[84:85], v248 offset:36864\n"
      "v_mfma_f32_16x16x32_bf16 v[218:221], a[32:35], v[140:143], v[218:221]\n"
      "ds_read_b64_tr_b16 a[86:87], v248 offset:37376\n"
      "ds_read_b64_tr_b16 a[108:109], v181 offset:36864\n"
      "v_mfma_f32_16x16x32_bf16 v[176:179], a[56:59], v[140:143], v[176:179]\n"
      "ds_read_b64_tr_b16 a[110:111], v181 offset:37376\n"
      "ds_read_b64_tr_b16 a[132:133], v249 offset:36864\n"
      "v_mfma_f32_16x16x32_bf16 v[202:205], a[80:83], v[140:143], v[202:205]\n"
      "s_waitcnt lgkmcnt(9)\n"
      "ds_read_b64_tr_b16 a[134:135], v249 offset:37376\n"
      "v_mfma_f32_16x16x32_bf16 v[116:119], a[104:107], v[140:143], v[116:119]\n"
      "s_waitcnt lgkmcnt(8)\n"
      "s_add_u32 m0, s33, 56320\n"
      "ds_read_b128 v[232:235], v1 offset:6144\n"
      "v_mfma_f32_16x16x32_bf16 v[222:225], a[128:131], v[140:143], v[222:225]\n"
      "s_waitcnt lgkmcnt(7)\n"
      "buffer_load_dwordx4 v114, s[28:31], s78 offen offset:1024  lds\n"
      "v_mfma_f32_16x16x32_bf16 v[226:229], a[12:15], v[242:245], v[226:229]\n"
      "s_waitcnt lgkmcnt(5)\n"
      "s_add_u32 m0, s33, 60672\n"
      "s_mov_b32 s78, 36864\n"
      "v_mfma_f32_16x16x32_bf16 v[130:133], a[36:39], v[242:245], v[130:133]\n"
      "s_waitcnt lgkmcnt(3)\n"
      "buffer_load_dwordx4 v114, s[28:31], s78 offen offset:768  lds\n"
      "v_mfma_f32_16x16x32_bf16 v[214:217], a[60:63], v[242:245], v[214:217]\n"
      "s_waitcnt lgkmcnt(1)\n"
      "s_add_u32 m0, s33, 64640\n"
      "ds_read_b128 v[140:143], v1 offset:8192\n"
      "v_mfma_f32_16x16x32_bf16 v[210:213], a[84:87], v[242:245], v[210:213]\n"
      "s_waitcnt lgkmcnt(1)\n"
      "buffer_load_dwordx4 v114, s[28:31], s78 offen offset:896  lds\n"
      "v_mfma_f32_16x16x32_bf16 v[188:191], a[108:111], v[242:245], v[188:191]\n"
      "s_add_u32 m0, s33, 68608\n"
      "ds_read_b128 v[156:159], v1 offset:10240\n"
      "v_mfma_f32_16x16x32_bf16 v[160:163], a[132:135], v[242:245], v[160:163]\n"
      "buffer_load_dwordx4 v114, s[28:31], s78 offen offset:1024  lds\n"
      "v_mfma_f32_16x16x32_bf16 v[104:107], a[12:15], v[232:235], v[104:107]\n"
      "s_add_u32 m0, s33, 72960\n"
      "v_mfma_f32_16x16x32_bf16 v[184:187], a[36:39], v[232:235], v[184:187]\n"
      "v_mfma_f32_16x16x32_bf16 v[152:155], a[60:63], v[232:235], v[152:155]\n"
      "s_waitcnt lgkmcnt(1)\n"
      "v_mfma_f32_16x16x32_bf16 v[96:99], a[84:87], v[232:235], v[96:99]\n"
      "v_mfma_f32_16x16x32_bf16 v[144:147], a[108:111], v[232:235], v[144:147]\n"
      "ds_read_b128 v[242:245], v134 offset:8192\n"
      "v_mfma_f32_16x16x32_bf16 v[100:103], a[132:135], v[232:235], v[100:103]\n"
      "s_waitcnt lgkmcnt(1)\n"
      "v_mfma_f32_16x16x32_bf16 v[108:111], a[12:15], v[140:143], v[108:111]\n"
      "v_mfma_f32_16x16x32_bf16 v[172:175], a[36:39], v[140:143], v[172:175]\n"
      "v_mfma_f32_16x16x32_bf16 v[124:127], a[60:63], v[140:143], v[124:127]\n"
      "v_mfma_f32_16x16x32_bf16 v[168:171], a[84:87], v[140:143], v[168:171]\n"
      "v_mfma_f32_16x16x32_bf16 v[148:151], a[108:111], v[140:143], v[148:151]\n"
      "ds_read_b128 v[232:235], v0 offset:12288\n"
      "v_mfma_f32_16x16x32_bf16 v[206:209], a[132:135], v[140:143], v[206:209]\n"
      "s_waitcnt lgkmcnt(1)\n"
      "v_mfma_f32_16x16x32_bf16 v[196:199], a[12:15], v[156:159], v[196:199]\n"
      "v_mfma_f32_16x16x32_bf16 v[218:221], a[36:39], v[156:159], v[218:221]\n"
      "v_mfma_f32_16x16x32_bf16 v[176:179], a[60:63], v[156:159], v[176:179]\n"
      "v_mfma_f32_16x16x32_bf16 v[202:205], a[84:87], v[156:159], v[202:205]\n"
      "v_mfma_f32_16x16x32_bf16 v[116:119], a[108:111], v[156:159], v[116:119]\n"
      "ds_read_b128 v[140:143], v0 offset:14336\n"
      "s_waitcnt vmcnt(12)\n"
      "s_barrier\n"
      "v_mfma_f32_16x16x32_bf16 v[222:225], a[132:135], v[156:159], v[222:225]\n"
      "s_mov_b32 s79, 73728\n"
      "ds_read_b64_tr_b16 a[16:17], v236\n"
      "ds_read_b64_tr_b16 a[18:19], v236 offset:512\n"
      "ds_read_b64_tr_b16 a[40:41], v237\n"
      "ds_read_b64_tr_b16 a[42:43], v237 offset:512\n"
      "ds_read_b64_tr_b16 a[64:65], v238\n"
      "ds_read_b64_tr_b16 a[66:67], v238 offset:512\n"
      "ds_read_b64_tr_b16 a[88:89], v236 offset:12288\n"
      "ds_read_b64_tr_b16 a[90:91], v236 offset:12800\n"
      "ds_read_b64_tr_b16 a[112:113], v237 offset:12288\n"
      "ds_read_b64_tr_b16 a[114:115], v237 offset:12800\n"
      "ds_read_b64_tr_b16 a[136:137], v238 offset:12288\n"
      "ds_read_b64_tr_b16 a[138:139], v238 offset:12800\n"
      "buffer_load_dwordx4 v114, s[28:31], s79 offen offset:768  lds\n"
      "s_waitcnt lgkmcnt(9)\n"
      "v_mfma_f32_16x16x32_bf16 v[226:229], a[16:19], v[242:245], v[226:229]\n"
      "s_waitcnt lgkmcnt(8)\n"
      "s_add_u32 m0, s33, 76928\n"
      "v_mfma_f32_16x16x32_bf16 v[130:133], a[40:43], v[242:245], v[130:133]\n"
      "s_waitcnt lgkmcnt(6)\n"
      "buffer_load_dwordx4 v114, s[28:31], s79 offen offset:896  lds\n"
      "v_mfma_f32_16x16x32_bf16 v[214:217], a[64:67], v[242:245], v[214:217]\n"
      "s_waitcnt lgkmcnt(3)\n"
      "s_add_u32 m0, s33, 80896\n"
      "v_mfma_f32_16x16x32_bf16 v[210:213], a[88:91], v[242:245], v[210:213]\n"
      "s_waitcnt lgkmcnt(1)\n"
      "buffer_load_dwordx4 v114, s[28:31], s79 offen offset:1024  lds\n"
      "v_mfma_f32_16x16x32_bf16 v[188:191], a[112:115], v[242:245], v[188:191]\n"
      "s_waitcnt lgkmcnt(0)\n"
      "s_add_u32 m0, s33, 85248\n"
      "s_mov_b32 s79, 110592\n"
      "v_mfma_f32_16x16x32_bf16 v[160:163], a[136:139], v[242:245], v[160:163]\n"
      "buffer_load_dwordx4 v114, s[28:31], s79 offen offset:768  lds\n"
      "v_mfma_f32_16x16x32_bf16 v[104:107], a[16:19], v[232:235], v[104:107]\n"
      "s_add_u32 m0, s33, 89216\n"
      "ds_read_b128 v[242:245], v0 offset:16384\n"
      "v_mfma_f32_16x16x32_bf16 v[184:187], a[40:43], v[232:235], v[184:187]\n"
      "buffer_load_dwordx4 v114, s[28:31], s79 offen offset:896  lds\n"
      "v_mfma_f32_16x16x32_bf16 v[152:155], a[64:67], v[232:235], v[152:155]\n"
      "s_add_u32 m0, s33, 93184\n"
      "v_mfma_f32_16x16x32_bf16 v[96:99], a[88:91], v[232:235], v[96:99]\n"
      "buffer_load_dwordx4 v114, s[28:31], s79 offen offset:1024  lds\n"
      "v_mfma_f32_16x16x32_bf16 v[144:147], a[112:115], v[232:235], v[144:147]\n"
      "s_add_u32 m0, s33, -768\n"
      "ds_read_b128 v[156:159], v135 offset:8192\n"
      "v_mfma_f32_16x16x32_bf16 v[100:103], a[136:139], v[232:235], v[100:103]\n"
      "v_mfma_f32_16x16x32_bf16 v[108:111], a[16:19], v[140:143], v[108:111]\n"
      "s_waitcnt lgkmcnt(1)\n"
      "v_mfma_f32_16x16x32_bf16 v[172:175], a[40:43], v[140:143], v[172:175]\n"
      "v_mfma_f32_16x16x32_bf16 v[124:127], a[64:67], v[140:143], v[124:127]\n"
      "v_mfma_f32_16x16x32_bf16 v[168:171], a[88:91], v[140:143], v[168:171]\n"
      "v_mfma_f32_16x16x32_bf16 v[148:151], a[112:115], v[140:143], v[148:151]\n"
      "ds_read_b128 v[232:235], v1 offset:12288\n"
      "v_mfma_f32_16x16x32_bf16 v[206:209], a[136:139], v[140:143], v[206:209]\n"
      "s_waitcnt lgkmcnt(1)\n"
      "v_mfma_f32_16x16x32_bf16 v[196:199], a[16:19], v[242:245], v[196:199]\n"
      "v_mfma_f32_16x16x32_bf16 v[218:221], a[40:43], v[242:245], v[218:221]\n"
      "v_mfma_f32_16x16x32_bf16 v[176:179], a[64:67], v[242:245], v[176:179]\n"
      "v_mfma_f32_16x16x32_bf16 v[202:205], a[88:91], v[242:245], v[202:205]\n"
      "v_mfma_f32_16x16x32_bf16 v[116:119], a[112:115], v[242:245], v[116:119]\n"
      "ds_read_b128 v[140:143], v1 offset:14336\n"
      "v_mfma_f32_16x16x32_bf16 v[222:225], a[136:139], v[242:245], v[222:225]\n"
      "s_waitcnt lgkmcnt(1)\n"
      "s_waitcnt vmcnt(12)\n"
      "s_barrier\n"
      "ds_read_b64_tr_b16 a[20:21], v236 offset:24576\n"
      "ds_read_b64_tr_b16 a[22:23], v236 offset:25088\n"
      "ds_read_b64_tr_b16 a[44:45], v237 offset:24576\n"
      "ds_read_b64_tr_b16 a[46:47], v237 offset:25088\n"
      "ds_read_b64_tr_b16 a[68:69], v238 offset:24576\n"
      "ds_read_b64_tr_b16 a[70:71], v238 offset:25088\n"
      "ds_read_b64_tr_b16 a[92:93], v236 offset:36864\n"
      "ds_read_b64_tr_b16 a[94:95], v236 offset:37376\n"
      "ds_read_b64_tr_b16 a[116:117], v237 offset:36864\n"
      "ds_read_b64_tr_b16 a[118:119], v237 offset:37376\n"
      "ds_read_b64_tr_b16 a[140:141], v238 offset:36864\n"
      "ds_read_b64_tr_b16 a[142:143], v238 offset:37376\n"
      "s_mov_b32 s78, 147456\n"
      "buffer_load_dwordx4 v114, s[28:31], s78 offen offset:768  lds\n"
      "s_waitcnt lgkmcnt(9)\n"
      "v_mfma_f32_16x16x32_bf16 v[226:229], a[20:23], v[156:159], v[226:229]\n"
      "s_waitcnt lgkmcnt(8)\n"
      "s_add_u32 m0, s33, 3200\n"
      "v_mfma_f32_16x16x32_bf16 v[130:133], a[44:47], v[156:159], v[130:133]\n"
      "s_waitcnt lgkmcnt(6)\n"
      "buffer_load_dwordx4 v114, s[28:31], s78 offen offset:896  lds\n"
      "v_mfma_f32_16x16x32_bf16 v[214:217], a[68:71], v[156:159], v[214:217]\n"
      "s_waitcnt lgkmcnt(3)\n"
      "s_add_u32 m0, s33, 7168\n"
      "v_fma_f32 v250, v226, v226, s51\n"
      "v_mfma_f32_16x16x32_bf16 v[210:213], a[92:95], v[156:159], v[210:213]\n"
      "s_waitcnt lgkmcnt(1)\n"
      "buffer_load_dwordx4 v114, s[28:31], s78 offen offset:1024  lds\n"
      "v_mfma_f32_16x16x32_bf16 v[188:191], a[116:119], v[156:159], v[188:191]\n"
      "s_waitcnt lgkmcnt(0)\n"
      "s_add_u32 m0, s33, 11520\n"
      "s_mov_b32 s78, 184320\n"
      "v_mfma_f32_16x16x32_bf16 v[160:163], a[140:143], v[156:159], v[160:163]\n"
      "buffer_load_dwordx4 v114, s[28:31], s78 offen offset:768  lds\n"
      "v_mfma_f32_16x16x32_bf16 v[104:107], a[20:23], v[232:235], v[104:107]\n"
      "s_add_u32 m0, s33, 15488\n"
      "v_fma_f32 v241, v210, v210, s51\n"
      "buffer_load_dwordx4 v114, s[28:31], s78 offen offset:896  lds\n"
      "v_mfma_f32_16x16x32_bf16 v[184:187], a[44:47], v[232:235], v[184:187]\n"
      "s_add_u32 m0, s33, 19456\n"
      "v_fmac_f32 v250, v227, v227\n"
      "buffer_load_dwordx4 v114, s[28:31], s78 offen offset:1024  lds\n"
      "v_mfma_f32_16x16x32_bf16 v[152:155], a[68:71], v[232:235], v[152:155]\n"
      "s_add_u32 m0, s33, 24576\n"
      "v_fmac_f32 v241, v211, v211\n"
      "v_fma_f32 v240, v104, v104, s51\n"
      "v_mfma_f32_16x16x32_bf16 v[96:99], a[92:95], v[232:235], v[96:99]\n"
      "ds_read_b128 v[242:245], v1 offset:16384\n"
      "v_fmac_f32 v250, v228, v228\n"
      "v_mfma_f32_16x16x32_bf16 v[144:147], a[116:119], v[232:235], v[144:147]\n"
      "ds_read_b128 v[156:159], v134 offset:2048\n"
      "v_fmac_f32 v241, v212, v212\n"
      "v_mfma_f32_16x16x32_bf16 v[100:103], a[140:143], v[232:235], v[100:103]\n"
      "ds_read_b128 v[232:235], v135 offset:2048\n"
      "v_fmac_f32 v240, v105, v105\n"
      "v_mfma_f32_16x16x32_bf16 v[108:111], a[20:23], v[140:143], v[108:111]\n"
      "v_fma_f32 v246, v96, v96, s51\n"
      "v_fmac_f32 v246, v97, v97\n"
      "v_mfma_f32_16x16x32_bf16 v[172:175], a[44:47], v[140:143], v[172:175]\n"
      "v_fmac_f32 v250, v229, v229\n"
      "v_fmac_f32 v241, v213, v213\n"
      "v_mfma_f32_16x16x32_bf16 v[124:127], a[68:71], v[140:143], v[124:127]\n"
      "v_fmac_f32 v240, v106, v106\n"
      "v_fmac_f32 v246, v98, v98\n"
      "v_mfma_f32_16x16x32_bf16 v[168:171], a[92:95], v[140:143], v[168:171]\n"
      "s_waitcnt lgkmcnt(2)\n"
      "v_fma_f32 v193, v108, v108, s51\n"
      "v_fmac_f32 v193, v109, v109\n"
      "v_mfma_f32_16x16x32_bf16 v[148:151], a[116:119], v[140:143], v[148:151]\n"
      "s_waitcnt lgkmcnt(1)\n"
      "v_fmac_f32 v250, v130, v130\n"
      "v_fmac_f32 v241, v188, v188\n"
      "v_mfma_f32_16x16x32_bf16 v[206:209], a[140:143], v[140:143], v[206:209]\n"
      "s_waitcnt lgkmcnt(0)\n"
      "v_fma_f32 v140, v168, v168, s51\n"
      "v_fmac_f32 v140, v169, v169\n"
      "v_mfma_f32_16x16x32_bf16 v[196:199], a[20:23], v[242:245], v[196:199]\n"
      "v_fmac_f32 v240, v107, v107\n"
      "v_fmac_f32 v246, v99, v99\n"
      "v_mfma_f32_16x16x32_bf16 v[218:221], a[44:47], v[242:245], v[218:221]\n"
      "v_fmac_f32 v193, v110, v110\n"
      "v_fmac_f32 v140, v170, v170\n"
      "v_mfma_f32_16x16x32_bf16 v[176:179], a[68:71], v[242:245], v[176:179]\n"
      "v_fmac_f32 v250, v131, v131\n"
      "v_fmac_f32_e64 v241, v189, v189\n"
      "v_mfma_f32_16x16x32_bf16 v[202:205], a[92:95], v[242:245], v[202:205]\n"
      "v_fma_f32 v195, v196, v196, s51\n"
      "v_fmac_f32 v195, v197, v197\n"
      "v_mfma_f32_16x16x32_bf16 v[116:119], a[116:119], v[242:245], v[116:119]\n"
      "s_waitcnt vmcnt(12)\n"
      "s_barrier\n"
      "s_mov_b32 s79, 0\n"
      "v_mfma_f32_16x16x32_bf16 v[222:225], a[140:143], v[242:245], v[222:225]\n"
      "buffer_load_dwordx4 v167, s[36:39], s79 offen  lds\n"
      "v_mfma_f32_16x16x32_bf16 v[112:115], a[0:3], v[156:159], 0\n"
      "ds_read_b64_tr_b16 a[144:145], v248\n"
      "ds_read_b64_tr_b16 a[146:147], v248 offset:512\n"
      "v_mfma_f32_16x16x32_bf16 v[112:115], a[4:7], v[232:235], v[112:115]\n"
      "ds_read_b64_tr_b16 a[168:169], v181\n"
      "ds_read_b64_tr_b16 a[170:171], v181 offset:512\n"
      "v_mfma_f32_16x16x32_bf16 v[120:123], a[24:27], v[156:159], 0\n"
      "ds_read_b64_tr_b16 a[192:193], v249\n"
      "ds_read_b64_tr_b16 a[194:195], v249 offset:512\n"
      "v_mfma_f32_16x16x32_bf16 v[120:123], a[28:31], v[232:235], v[120:123]\n"
      "ds_read_b64_tr_b16 a[148:149], v248 offset:12288\n"
      "ds_read_b64_tr_b16 a[150:151], v248 offset:12800\n"
      "v_mfma_f32_16x16x32_bf16 v[136:139], a[48:51], v[156:159], 0\n"
      "ds_read_b64_tr_b16 a[172:173], v181 offset:12288\n"
      "ds_read_b64_tr_b16 a[174:175], v181 offset:12800\n"
      "v_mfma_f32_16x16x32_bf16 v[136:139], a[52:55], v[232:235], v[136:139]\n"
      "ds_read_b64_tr_b16 a[196:197], v249 offset:12288\n"
      "ds_read_b64_tr_b16 a[198:199], v249 offset:12800\n"
      "v_mfma_f32_16x16x32_bf16 v[252:255], a[72:75], v[156:159], 0\n"
      "s_add_u32 m0, s33, 28544\n"
      "v_fmac_f32_e64 v195, v198, v198\n"
      "buffer_load_dwordx4 v167, s[36:39], s79 offen offset:128  lds\n"
      "v_mfma_f32_16x16x32_bf16 v[252:255], a[76:79], v[232:235], v[252:255]\n"
      "s_add_u32 m0, s33, 32512\n"
      "v_fmac_f32_e64 v240, v184, v184\n"
      "buffer_load_dwordx4 v167, s[36:39], s79 offen offset:256  lds\n"
      "s_add_u32 m0, s33, 36864\n"
      "s_mov_b32 s79, 12288\n"
      "buffer_load_dwordx4 v167, s[36:39], s79 offen  lds\n"
      "s_add_u32 m0, s33, 40832\n"
      "v_fmac_f32 v246, v144, v144\n"
      "buffer_load_dwordx4 v167, s[36:39], s79 offen offset:128  lds\n"
      "s_add_u32 m0, s33, 44800\n"
      "v_fmac_f32 v193, v111, v111\n"
      "buffer_load_dwordx4 v167, s[36:39], s79 offen offset:256  lds\n"
      "s_add_u32 m0, s33, 49152\n"
      "v_fmac_f32 v140, v171, v171\n"
      "v_fmac_f32 v250, v132, v132\n"
      "v_fmac_f32 v241, v190, v190\n"
      "v_fmac_f32 v195, v199, v199\n"
      "v_fmac_f32 v240, v185, v185\n"
      "v_fmac_f32 v246, v145, v145\n"
      "v_fmac_f32 v193, v172, v172\n"
      "v_fmac_f32 v140, v148, v148\n"
      "v_fmac_f32 v250, v133, v133\n"
      "v_fmac_f32 v241, v191, v191\n"
      "v_fmac_f32 v195, v218, v218\n"
      "v_fmac_f32 v240, v186, v186\n"
      "v_fmac_f32 v246, v146, v146\n"
      "v_fmac_f32 v193, v173, v173\n"
      "v_fmac_f32 v140, v149, v149\n"
      "v_fmac_f32 v250, v214, v214\n"
      "v_fmac_f32 v241, v160, v160\n"
      "v_fmac_f32 v195, v219, v219\n"
      "v_fmac_f32 v240, v187, v187\n"
      "v_fmac_f32 v246, v147, v147\n"
      "v_fmac_f32 v193, v174, v174\n"
      "v_fmac_f32 v140, v150, v150\n"
      "v_fmac_f32 v250, v215, v215\n"
      "v_fmac_f32 v241, v161, v161\n"
      "v_fmac_f32 v195, v220, v220\n"
      "v_fmac_f32 v240, v152, v152\n"
      "v_fmac_f32 v246, v100, v100\n"
      "v_fmac_f32 v193, v175, v175\n"
      "v_fmac_f32 v140, v151, v151\n"
      "v_fmac_f32 v250, v216, v216\n"
      "v_fmac_f32 v241, v162, v162\n"
      "v_fmac_f32 v195, v221, v221\n"
      "v_fmac_f32 v240, v153, v153\n"
      "v_fmac_f32 v246, v101, v101\n"
      "v_fmac_f32 v193, v124, v124\n"
      "v_fmac_f32 v140, v206, v206\n"
      "v_fmac_f32 v250, v217, v217\n"
      "v_fmac_f32 v241, v163, v163\n"
      "v_fmac_f32 v195, v176, v176\n"
      "v_fmac_f32 v240, v154, v154\n"
      "v_fmac_f32 v246, v102, v102\n"
      "v_fmac_f32 v193, v125, v125\n"
      "v_fmac_f32 v140, v207, v207\n"
      "v_fmac_f32 v195, v177, v177\n"
      "v_fmac_f32 v240, v155, v155\n"
      "v_fmac_f32 v246, v103, v103\n"
      "v_fmac_f32 v193, v126, v126\n"
      "v_fmac_f32 v140, v208, v208\n"
      "v_fmac_f32 v195, v178, v178\n"
      "v_fmac_f32 v193, v127, v127\n"
      "v_fmac_f32 v140, v209, v209\n"
      "v_fmac_f32 v195, v179, v179\n"
      "v_permlane16_swap_b32 v250, v241\n"
      "v_permlane16_swap_b32 v240, v246\n"
      "v_add_f32 v250, v250, v241\n"
      "v_permlane16_swap_b32 v193, v140\n"
      "v_mov_b32 v241, v250\n"
      "v_add_f32 v240, v240, v246\n"
      "v_mov_b32 v246, v240\n"
      "v_add_f32 v193, v193, v140\n"
      "v_mov_b32 v140, v193\n"
      "v_permlane32_swap_b32 v250, v241\n"
      "v_permlane32_swap_b32 v240, v246\n"
      "v_add_f32 v250, v250, v241\n"
      "v_permlane32_swap_b32 v193, v140\n"
      "s_waitcnt lgkmcnt(0)\n"
      "s_waitcnt vmcnt(12)\n"
      "s_barrier\n"
      "ds_read_b64_tr_b16 a[152:153], v248 offset:24576\n"
      "ds_read_b64_tr_b16 a[154:155], v248 offset:25088\n"
      "ds_read_b64_tr_b16 a[176:177], v181 offset:24576\n"
      "ds_read_b64_tr_b16 a[178:179], v181 offset:25088\n"
      "ds_read_b64_tr_b16 a[200:201], v249 offset:24576\n"
      "ds_read_b64_tr_b16 a[202:203], v249 offset:25088\n"
      "ds_read_b64_tr_b16 a[156:157], v248 offset:36864\n"
      "ds_read_b64_tr_b16 a[158:159], v248 offset:37376\n"
      "ds_read_b64_tr_b16 a[180:181], v181 offset:36864\n"
      "ds_read_b64_tr_b16 a[182:183], v181 offset:37376\n"
      "ds_read_b64_tr_b16 a[204:205], v249 offset:36864\n"
      "ds_read_b64_tr_b16 a[206:207], v249 offset:37376\n"
      "s_mov_b32 s78, 24576\n"
      "buffer_load_dwordx4 v167, s[36:39], s78 offen  lds\n"
      "s_add_u32 m0, s33, 53120\n"
      "s_lshl_b32 s92, s40, 6\n"
      "buffer_load_dwordx4 v167, s[36:39], s78 offen offset:128  lds\n"
      "s_add_u32 m0, s33, 57088\n"
      "v_fma_f32 v194, v202, v202, s51\n"
      "buffer_load_dwordx4 v167, s[36:39], s78 offen offset:256  lds\n"
      "s_add_u32 m0, s33, 61440\n"
      "s_mov_b32 s78, 36864\n"
      "buffer_load_dwordx4 v167, s[36:39], s78 offen  lds\n"
      "s_add_u32 m0, s33, 65408\n"
      "v_fmac_f32 v194, v203, v203\n"
      "buffer_load_dwordx4 v167, s[36:39], s78 offen offset:128  lds\n"
      "s_add_u32 m0, s33, 69376\n"
      "v_fmac_f32 v194, v204, v204\n"
      "buffer_load_dwordx4 v167, s[36:39], s78 offen offset:256  lds\n"
      "s_add_u32 m0, s33, 73728\n"
      "v_fmac_f32 v194, v205, v205\n"
      "v_fmac_f32 v194, v116, v116\n"
      "v_fmac_f32 v194, v117, v117\n"
      "v_fmac_f32 v194, v118, v118\n"
      "v_fmac_f32 v194, v119, v119\n"
      "v_fmac_f32 v194, v222, v222\n"
      "v_fmac_f32 v194, v223, v223\n"
      "v_fmac_f32 v194, v224, v224\n"
      "v_fmac_f32 v194, v225, v225\n"
      "v_rsq_f32 v250, v250\n"
      "v_add_f32 v240, v240, v246\n"
      "v_permlane16_swap_b32 v195, v194\n"
      "v_add_f32 v195, v195, v194\n"
      "v_mov_b32 v194, v195\n"
      "v_rsq_f32 v240, v240\n"
      "v_add_f32 v193, v193, v140\n"
      "v_permlane32_swap_b32 v195, v194\n"
      "v_add_f32 v195, v195, v194\n"
      "v_rsq_f32 v193, v193\n"
      "v_mov_b32 v241, v250\n"
      "v_rsq_f32 v195, v195\n"
      "v_mov_b32 v246, v240\n"
      "v_mov_b32 v140, v193\n"
      "v_mov_b32 v194, v195\n"
      "v_permlane16_swap_b32 v250, v241\n"
      "v_permlane16_swap_b32 v240, v246\n"
      "v_mul_f32 v250, s50, v250\n"
      "v_permlane16_swap_b32 v193, v140\n"
      "v_mul_f32 v226, v250, v226\n"
      "v_mul_f32 v227, v250, v227\n"
      "v_mul_f32 v210, v241, v210\n"
      "v_mul_f32 v211, v241, v211\n"
      "v_permlane16_swap_b32 v195, v194\n"
      "v_mul_f32 v240, s50, v240\n"
      "v_cvt_pk_bf16_f32 v226, v226, v227\n"
      "v_mul_f32 v228, v250, v228\n"
      "v_mul_f32 v229, v250, v229\n"
      "v_mul_f32 v214, v250, v214\n"
      "v_mul_f32 v215, v250, v215\n"
      "v_mul_f32 v160, v241, v160\n"
      "v_mul_f32 v161, v241, v161\n"
      "v_cvt_pk_bf16_f32 v210, v210, v211\n"
      "v_mul_f32 v212, v241, v212\n"
      "v_mul_f32 v213, v241, v213\n"
      "v_mul_f32 v96, v246, v96\n"
      "v_mul_f32 v97, v246, v97\n"
      "v_mul_f32 v104, v240, v104\n"
      "v_mul_f32 v105, v240, v105\n"
      "v_mul_f32 v193, s50, v193\n"
      "v_cvt_pk_bf16_f32 v227, v228, v229\n"
      "v_mul_f32 v130, v250, v130\n"
      "v_mul_f32 v131, v250, v131\n"
      "v_mul_f32 v132, v250, v132\n"
      "v_mul_f32 v133, v250, v133\n"
      "v_cvt_pk_bf16_f32 v214, v214, v215\n"
      "v_mul_f32_e64 v216, v250, v216\n"
      "v_mul_f32_e64 v217, v250, v217\n"
      "v_cvt_pk_bf16_f32 v160, v160, v161\n"
      "v_mul_f32 v162, v241, v162\n"
      "s_waitcnt lgkmcnt(0)\n"
      "s_waitcnt vmcnt(12)\n"
      "s_barrier\n"
      "ds_read_b64_tr_b16 a[160:161], v236\n"
      "ds_read_b64_tr_b16 a[162:163], v236 offset:512\n"
      "ds_read_b64_tr_b16 a[184:185], v237\n"
      "ds_read_b64_tr_b16 a[186:187], v237 offset:512\n"
      "ds_read_b64_tr_b16 a[208:209], v238\n"
      "ds_read_b64_tr_b16 a[210:211], v238 offset:512\n"
      "ds_read_b64_tr_b16 a[164:165], v236 offset:12288\n"
      "ds_read_b64_tr_b16 a[166:167], v236 offset:12800\n"
      "v_mfma_f32_16x16x32_bf16 v[248:251], a[96:99], v[156:159], 0\n"
      "ds_read_b64_tr_b16 a[188:189], v237 offset:12288\n"
      "ds_read_b64_tr_b16 a[190:191], v237 offset:12800\n"
      "v_mfma_f32_16x16x32_bf16 v[248:251], a[100:103], v[232:235], v[248:251]\n"
      "ds_read_b64_tr_b16 a[212:213], v238 offset:12288\n"
      "ds_read_b64_tr_b16 a[214:215], v238 offset:12800\n"
      "v_mfma_f32_16x16x32_bf16 v[180:183], a[120:123], v[156:159], 0\n"
      "s_mov_b32 s79, 49152\n"
      "buffer_load_dwordx4 v167, s[36:39], s79 offen  lds\n"
      "v_mfma_f32_16x16x32_bf16 v[180:183], a[124:127], v[232:235], v[180:183]\n"
      "s_add_u32 m0, s33, 77696\n"
      "ds_read_b128 v[234:237], v134 offset:6144\n"
      "buffer_load_dwordx4 v167, s[36:39], s79 offen offset:128  lds\n"
      "s_add_u32 m0, s33, 81664\n"
      "v_mul_f32_e64 v163, v241, v163\n"
      "buffer_load_dwordx4 v167, s[36:39], s79 offen offset:256  lds\n"
      "s_add_u32 m0, s33, 86016\n"
      "s_mov_b32 s79, 61440\n"
      "buffer_load_dwordx4 v167, s[36:39], s79 offen  lds\n"
      "s_add_u32 m0, s33, 89984\n"
      "v_cvt_pk_bf16_f32 v211, v212, v213\n"
      "buffer_load_dwordx4 v167, s[36:39], s79 offen offset:128  lds\n"
      "s_waitcnt lgkmcnt(0)\n"
      "v_mfma_f32_16x16x32_bf16 v[112:115], a[8:11], v[234:237], v[112:115]\n"
      "s_add_u32 m0, s33, 93952\n"
      "v_mul_f32 v188, v241, v188\n"
      "buffer_load_dwordx4 v167, s[36:39], s79 offen offset:256  lds\n"
      "v_mul_f32 v189, v241, v189\n"
      "v_mul_f32 v190, v241, v190\n"
      "v_mul_f32 v191, v241, v191\n"
      "ds_read_b128 v[242:245], v135 offset:6144\n"
      "v_mul_f32 v195, s50, v195\n"
      "v_cvt_pk_bf16_f32 v96, v96, v97\n"
      "v_mul_f32 v98, v246, v98\n"
      "v_mul_f32 v99, v246, v99\n"
      "v_mul_f32 v100, v246, v100\n"
      "v_mul_f32 v101, v246, v101\n"
      "v_mul_f32 v168, v140, v168\n"
      "v_mul_f32 v169, v140, v169\n"
      "v_cvt_pk_bf16_f32 v104, v104, v105\n"
      "v_mul_f32 v106, v240, v106\n"
      "v_mul_f32 v107, v240, v107\n"
      "v_mul_f32 v152, v240, v152\n"
      "v_mul_f32 v153, v240, v153\n"
      "v_mul_f32 v108, v193, v108\n"
      "v_mul_f32 v109, v193, v109\n"
      "v_cvt_pk_bf16_f32 v228, v130, v131\n"
      "v_cvt_pk_bf16_f32 v229, v132, v133\n"
      "v_cvt_pk_bf16_f32 v215, v216, v217\n"
      "v_cvt_pk_bf16_f32 v161, v162, v163\n"
      "v_cvt_pk_bf16_f32 v212, v188, v189\n"
      "v_cvt_pk_bf16_f32 v213, v190, v191\n"
      "v_mul_f32 v202, v194, v202\n"
      "s_waitcnt lgkmcnt(0)\n"
      "v_mfma_f32_16x16x32_bf16 v[112:115], a[12:15], v[242:245], v[112:115]\n"
      "ds_write_b128 v165, v[226:229]\n"
      "v_mfma_f32_16x16x32_bf16 v[120:123], a[32:35], v[234:237], v[120:123]\n"
      "ds_write_b64 v165, v[214:215] offset:16\n"
      "v_mfma_f32_16x16x32_bf16 v[120:123], a[36:39], v[242:245], v[120:123]\n"
      "ds_write_b64 v165, v[160:161] offset:24\n"
      "v_mfma_f32_16x16x32_bf16 v[136:139], a[56:59], v[234:237], v[136:139]\n"
      "ds_write_b128 v165, v[210:213] offset:32\n"
      "v_mfma_f32_16x16x32_bf16 v[136:139], a[60:63], v[242:245], v[136:139]\n"
      "ds_read_b128 v[226:229], v134 offset:10240\n"
      "v_mul_f32 v203, v194, v203\n"
      "v_mfma_f32_16x16x32_bf16 v[252:255], a[80:83], v[234:237], v[252:255]\n"
      "ds_read_b128 v[130:133], v135 offset:10240\n"
      "v_mul_f32 v196, v195, v196\n"
      "v_mfma_f32_16x16x32_bf16 v[252:255], a[84:87], v[242:245], v[252:255]\n"
      "s_waitcnt vmcnt(12)\n"
      "s_barrier\n"
      "ds_read_b64_tr_b16 a[216:217], v239 offset:24576\n"
      "v_mfma_f32_16x16x32_bf16 v[248:251], a[104:107], v[234:237], v[248:251]\n"
      "ds_read_b64_tr_b16 a[218:219], v239 offset:26624\n"
      "ds_read_b64_tr_b16 a[240:241], v239 offset:24584\n"
      "v_mfma_f32_16x16x32_bf16 v[248:251], a[108:111], v[242:245], v[248:251]\n"
      "ds_read_b64_tr_b16 a[242:243], v239 offset:26632\n"
      "ds_read_b64_tr_b16 v[232:233], v164 offset:24576\n"
      "v_mfma_f32_16x16x32_bf16 v[180:183], a[128:131], v[234:237], v[180:183]\n"
      "ds_read_b64_tr_b16 v[234:235], v164 offset:26624\n"
      "ds_read_b64_tr_b16 a[220:221], v239 offset:36864\n"
      "v_mfma_f32_16x16x32_bf16 v[180:183], a[132:135], v[242:245], v[180:183]\n"
      "s_waitcnt lgkmcnt(8)\n"
      "ds_read_b64_tr_b16 a[222:223], v239 offset:38912\n"
      "v_mfma_f32_16x16x32_bf16 v[112:115], a[16:19], v[226:229], v[112:115]\n"
      "s_waitcnt lgkmcnt(8)\n"
      "ds_read_b64_tr_b16 a[244:245], v239 offset:36872\n"
      "v_mfma_f32_16x16x32_bf16 v[112:115], a[20:23], v[130:133], v[112:115]\n"
      "ds_read_b64_tr_b16 a[246:247], v239 offset:38920\n"
      "ds_read_b64_tr_b16 v[236:237], v164 offset:36864\n"
      "v_mfma_f32_16x16x32_bf16 v[120:123], a[40:43], v[226:229], v[120:123]\n"
      "ds_read_b64_tr_b16 v[238:239], v164 offset:38912\n"
      "s_add_u32 m0, s64, 18432\n"
      "v_mfma_f32_16x16x32_bf16 v[120:123], a[44:47], v[130:133], v[120:123]\n"
      "buffer_load_dwordx4 v16, s[16:19], s45 offen  lds\n"
      "v_mfma_f32_16x16x32_bf16 v[136:139], a[64:67], v[226:229], v[136:139]\n"
      "s_add_u32 m0, s64, 22528\n"
      "v_fma_f32 v156, v112, v112, s51\n"
      "v_fmac_f32_e64 v156, v113, v113\n"
      "v_mfma_f32_16x16x32_bf16 v[136:139], a[68:71], v[130:133], v[136:139]\n"
      "buffer_load_dwordx4 v17, s[16:19], s45 offen  lds\n"
      "v_mfma_f32_16x16x32_bf16 v[252:255], a[88:91], v[226:229], v[252:255]\n"
      "s_add_u32 m0, s64, 26624\n"
      "v_fmac_f32 v156, v114, v114\n"
      "v_fmac_f32 v156, v115, v115\n"
      "v_mfma_f32_16x16x32_bf16 v[252:255], a[92:95], v[130:133], v[252:255]\n"
      "buffer_load_dwordx4 v18, s[16:19], s45 offen  lds\n"
      "v_mfma_f32_16x16x32_bf16 v[248:251], a[112:115], v[226:229], v[248:251]\n"
      "s_add_u32 m0, s64, 30720\n"
      "v_fmac_f32 v156, v120, v120\n"
      "v_fmac_f32 v156, v121, v121\n"
      "v_mfma_f32_16x16x32_bf16 v[248:251], a[116:119], v[130:133], v[248:251]\n"
      "s_waitcnt lgkmcnt(0)\n"
      "v_fmac_f32 v156, v122, v122\n"
      "v_fma_f32 v157, v252, v252, s51\n"
      "v_mfma_f32_16x16x32_bf16 v[180:183], a[136:139], v[226:229], v[180:183]\n"
      "buffer_load_dwordx4 v19, s[16:19], s45 offen  lds\n"
      "v_mfma_f32_16x16x32_bf16 v[180:183], a[140:143], v[130:133], v[180:183]\n"
      "s_add_u32 m0, s65, 34816\n"
      "ds_read_b128 v[226:229], v134\n"
      "ds_read_b128 v[130:133], v134 offset:2048\n"
      "ds_read_b128 v[214:217], v135\n"
      "buffer_load_dwordx4 v15, s[16:19], s45 offen  lds\n"
      "buffer_load_dwordx4 v[210:213], v36, s[24:27], s47 offen\n"
      "buffer_load_dword v164, v29, s[24:27], s47 offen\n"
      "ds_read_b128 v[188:191], v135 offset:2048\n"
      "v_fmac_f32 v157, v253, v253\n"
      "v_fmac_f32 v157, v254, v254\n"
      "v_fmac_f32 v157, v255, v255\n"
      "v_fmac_f32 v157, v248, v248\n"
      "v_fmac_f32 v157, v249, v249\n"
      "v_fmac_f32 v157, v250, v250\n"
      "v_fmac_f32 v156, v123, v123\n"
      "v_fmac_f32 v157, v251, v251\n"
      "v_fmac_f32 v156, v136, v136\n"
      "v_fmac_f32 v157, v180, v180\n"
      "v_fmac_f32 v156, v137, v137\n"
      "v_fmac_f32 v157, v181, v181\n"
      "v_fmac_f32 v156, v138, v138\n"
      "v_fmac_f32 v157, v182, v182\n"
      "v_fmac_f32 v156, v139, v139\n"
      "v_fmac_f32 v157, v183, v183\n"
      "v_mul_f32 v197, v195, v197\n"
      "v_cvt_pk_bf16_f32 v97, v98, v99\n"
      "v_permlane16_swap_b32 v156, v157\n"
      "v_add_f32 v156, v156, v157\n"
      "v_mov_b32 v157, v156\n"
      "v_mul_f32 v144, v246, v144\n"
      "v_mul_f32 v145, v246, v145\n"
      "v_permlane32_swap_b32 v156, v157\n"
      "v_add_f32 v156, v156, v157\n"
      "v_rsq_f32 v156, v156\n"
      "v_mul_f32 v146, v246, v146\n"
      "v_mov_b32 v157, v156\n"
      "v_mul_f32 v147, v246, v147\n"
      "v_cvt_pk_bf16_f32 v100, v100, v101\n"
      "v_permlane16_swap_b32 v156, v157\n"
      "v_mul_f32 v156, s50, v156\n"
      "v_mul_f32 v252, v157, v252\n"
      "v_mul_f32 v253, v157, v253\n"
      "v_mul_f32 v112, v156, v112\n"
      "v_mul_f32 v113, v156, v113\n"
      "v_mul_f32 v102, v246, v102\n"
      "v_mul_f32 v103, v246, v103\n"
      "s_waitcnt lgkmcnt(0)\n"
      "v_mfma_f32_16x16x32_bf16 v[160:163], a[144:147], v[226:229], 0\n"
      "v_cvt_pk_bf16_f32 v168, v168, v169\n"
      "v_mul_f32 v170, v140, v170\n"
      "v_mul_f32_e64 v171, v140, v171\n"
      "v_cvt_pk_bf16_f32 v105, v106, v107\n"
      "s_waitcnt vmcnt(13)\n"
      "s_barrier\n"
      "ds_read_b64_tr_b16 a[224:225], v76\n"
      "ds_read_b64_tr_b16 a[226:227], v76 offset:2048\n"
      "ds_read_b64_tr_b16 a[248:249], v76 offset:8\n"
      "ds_read_b64_tr_b16 a[250:251], v76 offset:2056\n"
      "ds_read_b64_tr_b16 a[228:229], v76 offset:12288\n"
      "ds_read_b64_tr_b16 a[230:231], v76 offset:14336\n"
      "ds_read_b64_tr_b16 a[252:253], v76 offset:12296\n"
      "ds_read_b64_tr_b16 a[254:255], v76 offset:14344\n"
      "v_mul_f32 v184, v240, v184\n"
      "v_mul_f32 v185, v240, v185\n"
      "v_mul_f32 v206, v140, v206\n"
      "v_mul_f32 v207, v140, v207\n"
      "v_mul_f32 v186, v240, v186\n"
      "v_mul_f32 v187, v240, v187\n"
      "v_cvt_pk_bf16_f32 v152, v152, v153\n"
      "v_mul_f32 v154, v240, v154\n"
      "v_mul_f32 v155, v240, v155\n"
      "v_cvt_pk_bf16_f32 v108, v108, v109\n"
      "v_mul_f32 v110, v193, v110\n"
      "v_mul_f32 v111, v193, v111\n"
      "v_mul_f32 v124, v193, v124\n"
      "v_mul_f32 v125, v193, v125\n"
      "v_cvt_pk_bf16_f32 v252, v252, v253\n"
      "v_mul_f32 v254, v157, v254\n"
      "v_mul_f32 v255, v157, v255\n"
      "v_mul_f32 v180, v157, v180\n"
      "v_mul_f32 v181, v157, v181\n"
      "v_cvt_pk_bf16_f32 v112, v112, v113\n"
      "v_mul_f32 v114, v156, v114\n"
      "v_mul_f32 v115, v156, v115\n"
      "v_mul_f32 v136, v156, v136\n"
      "v_mul_f32 v137, v156, v137\n"
      "v_cvt_pk_bf16_f32 v202, v202, v203\n"
      "v_mul_f32 v204, v194, v204\n"
      "v_mul_f32 v205, v194, v205\n"
      "v_mul_f32 v222, v194, v222\n"
      "v_mul_f32 v223, v194, v223\n"
      "v_cvt_pk_bf16_f32 v196, v196, v197\n"
      "v_mul_f32 v198, v195, v198\n"
      "v_mul_f32 v199, v195, v199\n"
      "v_cvt_pk_bf16_f32 v98, v144, v145\n"
      "ds_read_b64_tr_b16 v[240:241], v84\n"
      "v_cvt_pk_bf16_f32 v99, v146, v147\n"
      "ds_read_b64_tr_b16 v[242:243], v84 offset:2048\n"
      "v_mul_f32 v176, v195, v176\n"
      "v_mul_f32 v177, v195, v177\n"
      "v_cvt_pk_bf16_f32 v101, v102, v103\n"
      "ds_read_b64_tr_b16 v[244:245], v84 offset:12288\n"
      "v_cvt_pk_bf16_f32 v169, v170, v171\n"
      "v_mul_f32 v148, v140, v148\n"
      "v_mul_f32 v149, v140, v149\n"
      "v_mul_f32 v150, v140, v150\n"
      "v_mul_f32 v151, v140, v151\n"
      "v_cvt_pk_bf16_f32 v106, v184, v185\n"
      "ds_read_b64_tr_b16 v[246:247], v84 offset:14336\n"
      "v_cvt_pk_bf16_f32 v206, v206, v207\n"
      "v_mul_f32 v208, v140, v208\n"
      "v_mul_f32 v209, v140, v209\n"
      "v_cvt_pk_bf16_f32 v107, v186, v187\n"
      "v_mfma_f32_16x16x32_bf16 v[144:147], a[168:171], v[226:229], 0\n"
      "s_waitcnt lgkmcnt(3)\n"
      "v_cvt_pk_bf16_f32 v153, v154, v155\n"
      "v_cvt_pk_bf16_f32 v109, v110, v111\n"
      "v_mfma_f32_16x16x32_bf16 v[140:143], a[192:195], v[226:229], 0\n"
      "s_waitcnt lgkmcnt(2)\n"
      "v_mul_f32 v172, v193, v172\n"
      "v_mul_f32 v173, v193, v173\n"
      "v_mul_f32 v174, v193, v174\n"
      "v_mul_f32 v175, v193, v175\n"
      "v_cvt_pk_bf16_f32 v124, v124, v125\n"
      "v_mul_f32 v126, v193, v126\n"
      "v_mul_f32 v127, v193, v127\n"
      "v_cvt_pk_bf16_f32 v253, v254, v255\n"
      "v_mul_f32 v248, v157, v248\n"
      "v_mul_f32 v249, v157, v249\n"
      "v_mul_f32 v250, v157, v250\n"
      "v_mul_f32 v251, v157, v251\n"
      "v_cvt_pk_bf16_f32 v180, v180, v181\n"
      "v_mul_f32 v182, v157, v182\n"
      "v_mul_f32_e64 v183, v157, v183\n"
      "v_mfma_f32_16x16x32_bf16 v[184:187], a[144:147], v[130:133], 0\n"
      "s_waitcnt lgkmcnt(0)\n"
      "v_cvt_pk_bf16_f32 v113, v114, v115\n"
      "v_mul_f32 v120, v156, v120\n"
      "v_mul_f32 v121, v156, v121\n"
      "v_mul_f32 v122, v156, v122\n"
      "v_mul_f32 v123, v156, v123\n"
      "v_cvt_pk_bf16_f32 v136, v136, v137\n"
      "v_mul_f32 v138, v156, v138\n"
      "v_mul_f32 v139, v156, v139\n"
      "v_cvt_pk_bf16_f32 v203, v204, v205\n"
      "v_mul_f32 v116, v194, v116\n"
      "v_mul_f32 v117, v194, v117\n"
      "v_mul_f32 v118, v194, v118\n"
      "v_mul_f32 v119, v194, v119\n"
      "v_cvt_pk_bf16_f32 v222, v222, v223\n"
      "v_mul_f32 v224, v194, v224\n"
      "v_mul_f32 v225, v194, v225\n"
      "v_cvt_pk_bf16_f32 v197, v198, v199\n"
      "v_mul_f32 v218, v195, v218\n"
      "v_mul_f32 v219, v195, v219\n"
      "v_mul_f32_e64 v220, v195, v220\n"
      "v_mul_f32_e64 v221, v195, v221\n"
      "v_cvt_pk_bf16_f32 v176, v176, v177\n"
      "v_mul_f32_e64 v178, v195, v178\n"
      "v_mul_f32 v179, v195, v179\n"
      "v_cvt_pk_bf16_f32 v170, v148, v149\n"
      "v_mfma_f32_16x16x32_bf16 v[192:195], a[168:171], v[130:133], 0\n"
      "v_cvt_pk_bf16_f32 v171, v150, v151\n"
      "v_cvt_pk_bf16_f32 v207, v208, v209\n"
      "v_mfma_f32_16x16x32_bf16 v[148:151], a[192:195], v[130:133], 0\n"
      "ds_read_b128 v[226:229], v134 offset:4096\n"
      "v_cvt_pk_bf16_f32 v110, v172, v173\n"
      "v_mfma_f32_16x16x32_bf16 v[160:163], a[148:151], v[214:217], v[160:163]\n"
      "v_cvt_pk_bf16_f32 v111, v174, v175\n"
      "v_cvt_pk_bf16_f32 v125, v126, v127\n"
      "v_mfma_f32_16x16x32_bf16 v[144:147], a[172:175], v[214:217], v[144:147]\n"
      "ds_read_b128 v[130:133], v134 offset:6144\n"
      "v_cvt_pk_bf16_f32 v254, v248, v249\n"
      "v_mfma_f32_16x16x32_bf16 v[140:143], a[196:199], v[214:217], v[140:143]\n"
      "ds_read_b128 v[214:217], v135 offset:4096\n"
      "v_cvt_pk_bf16_f32 v255, v250, v251\n"
      "v_mfma_f32_16x16x32_bf16 v[184:187], a[148:151], v[188:191], v[184:187]\n"
      "ds_read_b128 v[172:175], v135 offset:6144\n"
      "v_cvt_pk_bf16_f32 v181, v182, v183\n"
      "v_mfma_f32_16x16x32_bf16 v[192:195], a[172:175], v[188:191], v[192:195]\n"
      "ds_read_b128 v[126:129], v134 offset:8192\n"
      "v_cvt_pk_bf16_f32 v114, v120, v121\n"
      "v_mfma_f32_16x16x32_bf16 v[148:151], a[196:199], v[188:191], v[148:151]\n"
      "s_waitcnt lgkmcnt(4)\n"
      "v_cvt_pk_bf16_f32 v115, v122, v123\n"
      "v_cvt_pk_bf16_f32 v137, v138, v139\n"
      "v_mfma_f32_16x16x32_bf16 v[160:163], a[152:155], v[226:229], v[160:163]\n"
      "s_mov_b32 exec_lo, 0x1f001f\n"
      "s_mov_b32 exec_hi, 0x1f001f\n"
      "ds_write_b128 v165, v[112:115] offset:18688\n"
      "ds_write_b64 v165, v[136:137] offset:18704\n"
      "ds_write_b64 v165, v[180:181] offset:18712\n"
      "ds_write_b128 v165, v[252:255] offset:18720\n"
      "s_mov_b64 exec, -1\n"
      "v_mfma_f32_16x16x32_bf16 v[144:147], a[176:179], v[226:229], v[144:147]\n"
      "s_waitcnt lgkmcnt(4)\n"
      "v_cvt_pk_bf16_f32 v204, v116, v117\n"
      "v_cvt_pk_bf16_f32 v205, v118, v119\n"
      "v_mfma_f32_16x16x32_bf16 v[140:143], a[200:203], v[226:229], v[140:143]\n"
      "s_waitcnt vmcnt(7)\n"
      "s_barrier\n"
      "v_cvt_pk_bf16_f32 v223, v224, v225\n"
      "v_mfma_f32_16x16x32_bf16 v[184:187], a[152:155], v[130:133], v[184:187]\n"
      "ds_read_b64_tr_b16 a[232:233], v76 offset:24576\n"
      "v_cvt_pk_bf16_f32 v198, v218, v219\n"
      "v_mfma_f32_16x16x32_bf16 v[192:195], a[176:179], v[130:133], v[192:195]\n"
      "ds_read_b64_tr_b16 a[234:235], v76 offset:26624\n"
      "v_cvt_pk_bf16_f32 v199, v220, v221\n"
      "v_mfma_f32_16x16x32_bf16 v[148:151], a[200:203], v[130:133], v[148:151]\n"
      "ds_read_b64_tr_b16 v[224:225], v76 offset:24584\n"
      "v_cvt_pk_bf16_f32 v177, v178, v179\n"
      "v_mfma_f32_16x16x32_bf16 v[160:163], a[156:159], v[214:217], v[160:163]\n"
      "ds_read_b64_tr_b16 v[226:227], v76 offset:26632\n"
      "ds_read_b64_tr_b16 v[248:249], v84 offset:24576\n"
      "v_mfma_f32_16x16x32_bf16 v[144:147], a[180:183], v[214:217], v[144:147]\n"
      "ds_read_b64_tr_b16 v[250:251], v84 offset:26624\n"
      "ds_read_b64_tr_b16 a[236:237], v76 offset:36864\n"
      "v_mfma_f32_16x16x32_bf16 v[140:143], a[204:207], v[214:217], v[140:143]\n"
      "ds_read_b64_tr_b16 a[238:239], v76 offset:38912\n"
      "ds_read_b64_tr_b16 v[228:229], v76 offset:36872\n"
      "v_mfma_f32_16x16x32_bf16 v[184:187], a[156:159], v[172:175], v[184:187]\n"
      "ds_read_b64_tr_b16 v[230:231], v76 offset:38920\n"
      "ds_read_b64_tr_b16 v[252:253], v84 offset:36864\n"
      "v_mfma_f32_16x16x32_bf16 v[192:195], a[180:183], v[172:175], v[192:195]\n"
      "ds_read_b64_tr_b16 v[254:255], v84 offset:38912\n"
      "v_subrev_u32 v39, 115456, v9\n"
      "v_mfma_f32_16x16x32_bf16 v[148:151], a[204:207], v[172:175], v[148:151]\n"
      "ds_read_b128 v[130:133], v134 offset:10240\n"
      "v_lshrrev_b32_e64 v39, 4, v39\n"
      "v_mfma_f32_16x16x32_bf16 v[160:163], a[160:163], v[126:129], v[160:163]\n"
      "ds_read_b128 v[214:217], v135 offset:8192\n"
      "v_and_b32_e64 v39, 48, v39\n"
      "v_mfma_f32_16x16x32_bf16 v[144:147], a[184:187], v[126:129], v[144:147]\n"
      "ds_read_b128 v[188:191], v135 offset:10240\n"
      "v_add_u32 v39, 123136, v39\n"
      "v_mfma_f32_16x16x32_bf16 v[140:143], a[208:211], v[126:129], v[140:143]\n"
      "ds_read_b128 v[180:183], v8 offset:24576\n"
      "ds_read_b128 v[218:221], v8 offset:28672\n"
      "ds_read_b128 v[172:175], v8 offset:32768\n"
      "v_add_u32 v135, s92, v39\n"
      "v_xor_b32 v136, 64, v8\n"
      "ds_read_b128 v[126:129], v136 offset:24576\n"
      "ds_read_b128 v[116:119], v136 offset:28672\n"
      "s_waitcnt lgkmcnt(7)\n"
      "v_mfma_f32_16x16x32_bf16 v[184:187], a[160:163], v[130:133], v[184:187]\n"
      "ds_read_b128 v[112:115], v136 offset:32768\n"
      "v_mov_b32 v154, 0xbc80bc80\n"
      "v_mfma_f32_16x16x32_bf16 v[192:195], a[184:187], v[130:133], v[192:195]\n"
      "s_waitcnt lgkmcnt(7)\n"
      "v_mov_b32 v155, 0xbc80bc80\n"
      "v_mov_b32 v156, 0xbc80bc80\n"
      "v_mfma_f32_16x16x32_bf16 v[148:151], a[208:211], v[130:133], v[148:151]\n"
      "s_waitcnt lgkmcnt(6)\n"
      "v_mov_b32 v157, 0xbc80bc80\n"
      "v_add_u32 v134, 49152, v8\n"
      "v_mfma_f32_16x16x32_bf16 v[160:163], a[164:167], v[214:217], v[160:163]\n"
      "ds_read_b128 v[130:133], v134\n"
      "v_add_u32 v76, 49152, v136\n"
      "v_mfma_f32_16x16x32_bf16 v[144:147], a[188:191], v[214:217], v[144:147]\n"
      "s_waitcnt lgkmcnt(5)\n"
      "v_subrev_u32 v178, 115456, v9\n"
      "v_readlane_b32 s45, v37, s43\n"
      "v_mfma_f32_16x16x32_bf16 v[140:143], a[212:215], v[214:217], v[140:143]\n"
      "ds_read_b128 v[214:217], v76\n"
      "v_cvt_pk_bf16_f32 v160, v160, v161\n"
      "v_mfma_f32_16x16x32_bf16 v[184:187], a[164:167], v[188:191], v[184:187]\n"
      "s_or_b32 s95, s45, s46\n"
      "v_cvt_pk_bf16_f32 v144, v144, v145\n"
      "v_cvt_pk_bf16_f32 v161, v162, v163\n"
      "v_mfma_f32_16x16x32_bf16 v[192:195], a[188:191], v[188:191], v[192:195]\n"
      "s_waitcnt lgkmcnt(3)\n"
      "v_cvt_pk_bf16_f32 v140, v140, v141\n"
      "v_cvt_pk_bf16_f32 v145, v146, v147\n"
      "v_mfma_f32_16x16x32_bf16 v[148:151], a[212:215], v[188:191], v[148:151]\n"
      "ds_read_b128 v[188:191], v134 offset:4096\n"
      "ds_read_b128 v[136:139], v76 offset:4096\n"
      "v_mfma_f32_16x16x32_bf16 v[120:123], v[180:183], v[154:157], 0\n"
      "ds_write_b64 v166, v[160:161]\n"
      "v_mfma_f32_16x16x32_bf16 v[120:123], v[126:129], v[154:157], v[120:123]\n"
      "ds_read_b128 v[180:183], v134 offset:8192\n"
      "v_cvt_pk_bf16_f32 v141, v142, v143\n"
      "v_mfma_f32_16x16x32_bf16 v[120:123], v[218:221], v[154:157], v[120:123]\n"
      "s_waitcnt lgkmcnt(4)\n"
      "ds_read_b128 v[218:221], v76 offset:8192\n"
      "v_mfma_f32_16x16x32_bf16 v[120:123], v[116:119], v[154:157], v[120:123]\n"
      "ds_read_b128 v[116:119], v134 offset:24576\n"
      "ds_read_b128 v[158:161], v76 offset:24576\n"
      "v_mfma_f32_16x16x32_bf16 v[120:123], v[172:175], v[154:157], v[120:123]\n"
      "ds_read_b128 v[172:175], v134 offset:28672\n"
      "ds_write_b64 v166, v[144:145] offset:32\n"
      "v_mfma_f32_16x16x32_bf16 v[120:123], v[112:115], v[154:157], v[120:123]\n"
      "s_waitcnt lgkmcnt(7)\n"
      "ds_read_b128 v[144:147], v76 offset:28672\n"
      "v_mfma_f32_16x16x32_bf16 v[126:129], v[130:133], v[154:157], 0\n"
      "ds_write_b64 v166, v[140:141] offset:64\n"
      "v_mfma_f32_16x16x32_bf16 v[126:129], v[214:217], v[154:157], v[126:129]\n"
      "s_waitcnt lgkmcnt(7)\n"
      "ds_read_b128 v[130:133], v134 offset:32768\n"
      "v_mfma_f32_16x16x32_bf16 v[126:129], v[188:191], v[154:157], v[126:129]\n"
      "s_waitcnt lgkmcnt(7)\n"
      "v_mul_f32 v120, 0x3eaaaaab, v120\n"
      "v_mul_f32 v121, 0x3eaaaaab, v121\n"
      "v_mfma_f32_16x16x32_bf16 v[126:129], v[136:139], v[154:157], v[126:129]\n"
      "s_waitcnt lgkmcnt(6)\n"
      "v_mul_f32 v122, 0x3eaaaaab, v122\n"
      "v_mul_f32 v123, 0x3eaaaaab, v123\n"
      "v_mfma_f32_16x16x32_bf16 v[126:129], v[180:183], v[154:157], v[126:129]\n"
      "s_waitcnt lgkmcnt(5)\n"
      "ds_write_b128 v135, v[120:123]\n"
      "v_mfma_f32_16x16x32_bf16 v[126:129], v[218:221], v[154:157], v[126:129]\n"
      "s_waitcnt lgkmcnt(3)\n"
      "ds_read_b128 v[214:217], v76 offset:32768\n"
      "v_mfma_f32_16x16x32_bf16 v[188:191], v[116:119], v[154:157], 0\n"
      "ds_read_b128 v[116:119], v0 offset:4096\n"
      "ds_read_b128 v[180:183], v1 offset:4096\n"
      "v_mfma_f32_16x16x32_bf16 v[188:191], v[158:161], v[154:157], v[188:191]\n"
      "s_waitcnt lgkmcnt(4)\n"
      "ds_read_b128 v[138:141], v0 offset:10240\n"
      "v_mfma_f32_16x16x32_bf16 v[188:191], v[172:175], v[154:157], v[188:191]\n"
      "ds_read_b128 v[160:163], v1 offset:10240\n"
      "v_mul_f32 v126, 0x3eaaaaab, v126\n"
      "v_mfma_f32_16x16x32_bf16 v[188:191], v[144:147], v[154:157], v[188:191]\n"
      "ds_read_b128 v[120:123], v0 offset:16384\n"
      "v_mul_f32 v127, 0x3eaaaaab, v127\n"
      "v_mfma_f32_16x16x32_bf16 v[188:191], v[130:133], v[154:157], v[188:191]\n"
      "ds_read_b128 v[112:115], v1 offset:16384\n"
      "s_or_b32 s95, s95, s48\n"
      "s_cmp_ge_u32 s95, 0x40000000\n"
      "s_cselect_b32 s94, -1, -2\n"
      "s_cmp_gt_u32 s42, s44\n"
      "s_cselect_b32 s95, s94, s95\n"
      "s_waitcnt lgkmcnt(6)\n"
      "v_mfma_f32_16x16x32_bf16 v[188:191], v[214:217], v[154:157], v[188:191]\n"
      "v_mul_f32 v128, 0x3eaaaaab, v128\n"
      "v_mul_f32 v129, 0x3eaaaaab, v129\n"
      "s_waitcnt lgkmcnt(5)\n"
      "v_mfma_f32_16x16x32_bf16 v[172:175], v[116:119], a[144:147], 0\n"
      "s_waitcnt lgkmcnt(4)\n"
      "ds_write_b128 v135, v[126:129] offset:256\n"
      "v_mfma_f32_16x16x32_bf16 v[172:175], v[180:183], a[148:151], v[172:175]\n"
      "s_waitcnt lgkmcnt(3)\n"
      "v_mul_f32 v188, 0x3eaaaaab, v188\n"
      "v_mul_f32 v189, 0x3eaaaaab, v189\n"
      "v_mfma_f32_16x16x32_bf16 v[172:175], v[138:141], a[152:155], v[172:175]\n"
      "s_waitcnt lgkmcnt(2)\n"
      "v_mul_f32 v190, 0x3eaaaaab, v190\n"
      "v_mul_f32 v191, 0x3eaaaaab, v191\n"
      "v_mfma_f32_16x16x32_bf16 v[172:175], v[160:163], a[156:159], v[172:175]\n"
      "s_waitcnt lgkmcnt(1)\n"
      "ds_write_b128 v135, v[188:191] offset:512\n"
      "v_mfma_f32_16x16x32_bf16 v[172:175], v[120:123], a[160:163], v[172:175]\n"
      "s_waitcnt lgkmcnt(0)\n"
      "s_barrier\n"
      "ds_read_b128 v[134:137], v9 offset:5632\n"
      "v_mfma_f32_16x16x32_bf16 v[172:175], v[112:115], a[164:167], v[172:175]\n"
      "ds_read_b128 v[126:129], v0 offset:2048\n"
      "ds_read_b128 v[188:191], v1 offset:2048\n"
      "v_mfma_f32_16x16x32_bf16 v[218:221], v[116:119], a[168:171], 0\n"
      "ds_read_b128 v[156:159], v0 offset:8192\n"
      "v_bfe_u32 v154, v178, 8, 2\n"
      "v_mfma_f32_16x16x32_bf16 v[218:221], v[180:183], a[172:175], v[218:221]\n"
      "ds_read_b128 v[130:133], v1 offset:8192\n"
      "v_bfe_u32 v178, v178, 4, 4\n"
      "v_mfma_f32_16x16x32_bf16 v[218:221], v[138:141], a[176:179], v[218:221]\n"
      "v_lshlrev_b32 v154, 2, v154\n"
      "v_sub_u32_e64 v178, v178, v154\n"
      "v_mfma_f32_16x16x32_bf16 v[218:221], v[160:163], a[180:183], v[218:221]\n"
      "v_lshlrev_b32_e64 v178, 4, v178\n"
      "v_cmp_gt_u32 s[80:81], 64, v178\n"
      "v_mfma_f32_16x16x32_bf16 v[218:221], v[120:123], a[184:187], v[218:221]\n"
      "v_mov_b32 v154, 0x3f80\n"
      "v_mov_b32_e64 v155, 0\n"
      "v_mfma_f32_16x16x32_bf16 v[218:221], v[112:115], a[188:191], v[218:221]\n"
      "v_lshlrev_b64 v[154:155], v178, v[154:155]\n"
      "v_cndmask_b32_e64 v144, 0, v154, s[80:81]\n"
      "v_mfma_f32_16x16x32_bf16 v[214:217], v[116:119], a[192:195], 0\n"
      "s_waitcnt lgkmcnt(3)\n"
      "v_mov_b32 v146, 0\n"
      "v_cndmask_b32_e64 v142, 0, v154, s[80:81]\n"
      "v_mfma_f32_16x16x32_bf16 v[214:217], v[180:183], a[196:199], v[214:217]\n"
      "ds_read_b128 v[116:119], v0 offset:14336\n"
      "v_cvt_pk_bf16_f32 v76, v172, v173\n"
      "v_mfma_f32_16x16x32_bf16 v[214:217], v[138:141], a[200:203], v[214:217]\n"
      "ds_read_b128 v[178:181], v1 offset:14336\n"
      "v_cndmask_b32_e64 v145, 0, v155, s[80:81]\n"
      "v_mfma_f32_16x16x32_bf16 v[214:217], v[160:163], a[204:207], v[214:217]\n"
      "s_waitcnt lgkmcnt(2)\n"
      "v_mov_b32 v147, 0\n"
      "v_mov_b32_e64 v140, 0\n"
      "v_mfma_f32_16x16x32_bf16 v[214:217], v[120:123], a[208:211], v[214:217]\n"
      "v_cndmask_b32_e64 v143, 0, v155, s[80:81]\n"
      "v_mov_b32_e64 v141, 0\n"
      "v_mfma_f32_16x16x32_bf16 v[214:217], v[112:115], a[212:215], v[214:217]\n"
      "v_cvt_pk_bf16_f32 v77, v174, v175\n"
      "v_cvt_pk_bf16_f32 v80, v218, v219\n"
      "v_mfma_f32_16x16x32_bf16 v[112:115], v[126:129], a[144:147], 0\n"
      "v_cvt_pk_bf16_f32 v81, v220, v221\n"
      "v_cvt_pk_bf16_f32 v184, v184, v185\n"
      "v_mfma_f32_16x16x32_bf16 v[112:115], v[188:191], a[148:151], v[112:115]\n"
      "s_waitcnt lgkmcnt(1)\n"
      "v_cvt_pk_bf16_f32 v192, v192, v193\n"
      "v_cvt_pk_bf16_f32 v84, v214, v215\n"
      "v_mfma_f32_16x16x32_bf16 v[112:115], v[156:159], a[152:155], v[112:115]\n"
      "s_waitcnt lgkmcnt(0)\n"
      "v_cvt_pk_bf16_f32 v85, v216, v217\n"
      "v_cvt_pk_bf16_f32 v148, v148, v149\n"
      "v_mfma_f32_16x16x32_bf16 v[112:115], v[130:133], a[156:159], v[112:115]\n"
      "v_cvt_pk_bf16_f32 v185, v186, v187\n"
      "v_cvt_pk_bf16_f32 v193, v194, v195\n"
      "v_mfma_f32_16x16x32_bf16 v[112:115], v[116:119], a[160:163], v[112:115]\n"
      "v_cvt_pk_bf16_f32 v149, v150, v151\n"
      "s_mov_b32 exec_lo, 0x1f001f\n"
      "s_mov_b32 exec_hi, 0x1f001f\n"
      "ds_write_b64 v166, v[184:185] offset:18688\n"
      "ds_write_b64 v166, v[192:193] offset:18720\n"
      "ds_write_b64 v166, v[148:149] offset:18752\n"
      "s_mov_b64 exec, -1\n"
      "v_mfma_f32_16x16x32_bf16 v[112:115], v[178:181], a[164:167], v[112:115]\n"
      "s_waitcnt vmcnt(1)\n"
      "v_or3_b32 v210, v210, v211, v212\n"
      "s_waitcnt vmcnt(0)\n"
      "v_or3_b32 v210, v210, v213, v164\n"
      "v_mfma_f32_16x16x32_bf16 v[192:195], v[126:129], a[168:171], 0\n"
      "v_and_b32 v210, 0x7fff7fff, v210\n"
      "ds_write_b32 v31, v210 offset:4608\n"
      "v_mfma_f32_16x16x32_bf16 v[192:195], v[188:191], a[172:175], v[192:195]\n"
      "ds_read_b128 v[218:221], v10 offset:32\n"
      "v_or3_b32 v134, v134, v135, v136\n"
      "v_mfma_f32_16x16x32_bf16 v[192:195], v[156:159], a[176:179], v[192:195]\n"
      "v_cvt_pk_bf16_f32 v115, v114, v115\n"
      "v_cvt_pk_bf16_f32 v114, v112, v113\n"
      "v_mfma_f32_16x16x32_bf16 v[192:195], v[130:133], a[180:183], v[192:195]\n"
      "v_or_b32_e64 v134, v134, v137\n"
      "v_readlane_b32 s47, v38, s43\n"
      "v_mfma_f32_16x16x32_bf16 v[192:195], v[116:119], a[184:187], v[192:195]\n"
      "s_and_b32 s47, s47, -4\n"
      "s_add_u32 s43, s43, 1\n"
      "v_mfma_f32_16x16x32_bf16 v[192:195], v[178:181], a[188:191], v[192:195]\n"
      "ds_read_b64 v[216:217], v10 offset:24\n"
      "v_mfma_f32_16x16x32_bf16 v[164:167], v[126:129], a[192:195], 0\n"
      "ds_read_b128 v[210:213], v0\n"
      "v_mfma_f32_16x16x32_bf16 v[164:167], v[188:191], a[196:199], v[164:167]\n"
      "ds_read_b128 v[188:191], v1\n"
      "v_mfma_f32_16x16x32_bf16 v[164:167], v[156:159], a[200:203], v[164:167]\n"
      "ds_read_b128 v[136:139], v0 offset:6144\n"
      "v_mfma_f32_16x16x32_bf16 v[164:167], v[130:133], a[204:207], v[164:167]\n"
      "ds_read_b128 v[154:157], v1 offset:6144\n"
      "v_cvt_pk_bf16_f32 v195, v194, v195\n"
      "v_mfma_f32_16x16x32_bf16 v[164:167], v[116:119], a[208:211], v[164:167]\n"
      "ds_read_b128 v[120:123], v0 offset:12288\n"
      "v_cvt_pk_bf16_f32 v194, v192, v193\n"
      "v_mfma_f32_16x16x32_bf16 v[164:167], v[178:181], a[212:215], v[164:167]\n"
      "ds_read_b128 v[128:131], v1 offset:12288\n"
      "v_mfma_f32_16x16x32_bf16 v[116:119], v[96:99], v[104:107], v[40:43]\n"
      "s_waitcnt lgkmcnt(5)\n"
      "v_mfma_f32_16x16x32_bf16 v[172:175], v[168:171], v[104:107], v[44:47]\n"
      "s_waitcnt lgkmcnt(4)\n"
      "v_mfma_f32_16x16x32_bf16 v[178:181], v[202:205], v[104:107], v[48:51]\n"
      "s_waitcnt lgkmcnt(3)\n"
      "v_mfma_f32_16x16x32_bf16 v[158:161], v[218:221], v[104:107], v[24:27]\n"
      "s_waitcnt lgkmcnt(2)\n"
      "v_cvt_pk_bf16_f32 v167, v166, v167\n"
      "v_cvt_pk_bf16_f32 v166, v164, v165\n"
      "v_mfma_f32_16x16x16_bf16 v[116:119], v[100:101], v[152:153], v[116:119]\n"
      "v_mfma_f32_16x16x16_bf16 v[172:175], v[206:207], v[152:153], v[172:175]\n"
      "v_mfma_f32_16x16x16_bf16 v[178:181], v[222:223], v[152:153], v[178:181]\n"
      "v_mfma_f32_16x16x16_bf16 v[158:161], v[216:217], v[152:153], v[158:161]\n"
      "v_mfma_f32_16x16x32_bf16 v[150:153], v[210:213], a[144:147], 0\n"
      "s_waitcnt lgkmcnt(0)\n"
      "v_mfma_f32_16x16x32_bf16 v[150:153], v[188:191], a[148:151], v[150:153]\n"
      "v_mfma_f32_16x16x32_bf16 v[150:153], v[136:139], a[152:155], v[150:153]\n"
      "v_mfma_f32_16x16x32_bf16 v[150:153], v[154:157], a[156:159], v[150:153]\n"
      "v_mfma_f32_16x16x32_bf16 v[150:153], v[120:123], a[160:163], v[150:153]\n"
      "v_mfma_f32_16x16x32_bf16 v[150:153], v[128:131], a[164:167], v[150:153]\n"
      "v_mfma_f32_16x16x32_bf16 v[162:165], v[210:213], a[168:171], 0\n"
      "v_mfma_f32_16x16x32_bf16 v[162:165], v[188:191], a[172:175], v[162:165]\n"
      "v_mfma_f32_16x16x32_bf16 v[162:165], v[136:139], a[176:179], v[162:165]\n"
      "v_mfma_f32_16x16x32_bf16 v[162:165], v[154:157], a[180:183], v[162:165]\n"
      "v_mfma_f32_16x16x32_bf16 v[162:165], v[120:123], a[184:187], v[162:165]\n"
      "v_mfma_f32_16x16x32_bf16 v[162:165], v[128:131], a[188:191], v[162:165]\n"
      "v_mfma_f32_16x16x32_bf16 v[104:107], v[210:213], a[192:195], 0\n"
      "ds_read_b128 v[208:211], v39\n"
      "v_cvt_pk_bf16_f32 v112, v150, v151\n"
      "v_mfma_f32_16x16x32_bf16 v[104:107], v[188:191], a[196:199], v[104:107]\n"
      "ds_read_b128 v[188:191], v9 offset:6656\n"
      "ds_read_b128 v[212:215], v39 offset:64\n"
      "v_mfma_f32_16x16x32_bf16 v[104:107], v[136:139], a[200:203], v[104:107]\n"
      "v_cvt_pk_bf16_f32 v113, v152, v153\n"
      "v_cvt_pk_bf16_f32 v192, v162, v163\n"
      "v_mfma_f32_16x16x32_bf16 v[104:107], v[154:157], a[204:207], v[104:107]\n"
      "v_cvt_pk_bf16_f32 v193, v164, v165\n"
      "v_mfma_f32_16x16x32_bf16 v[104:107], v[120:123], a[208:211], v[104:107]\n"
      "v_mfma_f32_16x16x32_bf16 v[104:107], v[128:131], a[212:215], v[104:107]\n"
      "v_mfma_f32_16x16x32_bf16 v[148:151], v[96:99], v[108:111], v[52:55]\n"
      "s_waitcnt lgkmcnt(2)\n"
      "v_mfma_f32_16x16x32_bf16 v[120:123], v[168:171], v[108:111], v[56:59]\n"
      "s_waitcnt lgkmcnt(1)\n"
      "v_or3_b32 v188, v188, v189, v190\n"
      "v_or_b32 v188, v188, v191\n"
      "v_mfma_f32_16x16x32_bf16 v[136:139], v[202:205], v[108:111], v[60:63]\n"
      "s_waitcnt lgkmcnt(0)\n"
      "v_cmp_ne_u32 s[56:57], 0, v188\n"
      "v_cvt_pk_bf16_f32 v164, v104, v105\n"
      "v_mfma_f32_16x16x32_bf16 v[184:187], v[218:221], v[108:111], v[26:29]\n"
      "s_or_b64 vcc, s[56:57], s[54:55]\n"
      "s_cbranch_vccnz L_rare_1_%=\n"
      "L_back_2_%=:\n"
      "v_mfma_f32_16x16x16_bf16 v[148:151], v[100:101], v[124:125], v[148:151]\n"
      "ds_read_u16 v78, v11\n"
      "s_cbranch_vccnz L_rare_3_%=\n"
      "L_back_4_%=:\n"
      "v_mfma_f32_16x16x16_bf16 v[120:123], v[206:207], v[124:125], v[120:123]\n"
      "v_exp_f32 v116, v116\n"
      "v_exp_f32 v117, v117\n"
      "v_mfma_f32_16x16x16_bf16 v[136:139], v[222:223], v[124:125], v[136:139]\n"
      "v_exp_f32 v118, v118\n"
      "v_exp_f32 v119, v119\n"
      "v_mfma_f32_16x16x16_bf16 v[184:187], v[216:217], v[124:125], v[184:187]\n"
      "v_exp_f32 v172, v172\n"
      "v_exp_f32_e64 v173, v173\n"
      "v_mfma_f32_16x16x32_bf16 v[152:155], v[144:147], a[216:219], v[208:211]\n"
      "ds_read_u16 v82, v11 offset:32\n"
      "v_exp_f32 v174, v174\n"
      "v_mfma_f32_16x16x32_bf16 v[160:163], v[140:143], a[216:219], v[212:215]\n"
      "ds_read_u16 v86, v11 offset:64\n"
      "v_exp_f32 v175, v175\n"
      "v_mfma_f32_16x16x32_bf16 v[108:111], v[144:147], a[240:243], v[208:211]\n"
      "s_cbranch_vccnz L_rare_5_%=\n"
      "L_back_6_%=:\n"
      "s_add_u32 s42, s42, 1\n"
      "v_cvt_pk_bf16_f32 v116, v116, v117\n"
      "v_cvt_pk_bf16_f32 v117, v118, v119\n"
      "v_cvt_pk_bf16_f32 v118, v172, v173\n"
      "v_mfma_f32_16x16x32_bf16 v[126:129], v[140:143], a[240:243], v[212:215]\n"
      "s_waitcnt lgkmcnt(2)\n"
      "v_cvt_pk_bf16_f32 v119, v174, v175\n"
      "v_exp_f32 v178, v178\n"
      "v_mfma_f32_16x16x32_bf16 v[172:175], v[144:147], v[232:235], v[208:211]\n"
      "s_waitcnt lgkmcnt(1)\n"
      "v_exp_f32 v179, v179\n"
      "v_mfma_f32_16x16x32_bf16 v[102:105], v[140:143], v[232:235], v[212:215]\n"
      "v_exp_f32 v180, v180\n"
      "v_exp_f32 v181, v181\n"
      "v_exp_f32 v158, v158\n"
      "v_cvt_pk_bf16_f32 v88, v178, v179\n"
      "v_mfma_f32_16x16x32_bf16 v[210:213], v[32:35], v[116:119], 0\n"
      "s_waitcnt lgkmcnt(0)\n"
      "v_cvt_pk_bf16_f32 v89, v180, v181\n"
      "v_cvt_pk_bf16_f32 v90, v158, 0\n"
      "v_cvt_pk_bf16_f32 v165, v106, v107\n"
      "v_exp_f32 v148, v148\n"
      "v_mfma_f32_16x16x32_bf16 v[210:213], v[32:35], v[88:91], v[210:213]\n"
      "v_exp_f32 v149, v149\n"
      "v_mfma_f32_16x16x32_bf16 v[188:191], v[112:115], v[116:119], 0\n"
      "v_exp_f32 v150, v150\n"
      "v_mfma_f32_16x16x32_bf16 v[188:191], v[76:79], v[88:91], v[188:191]\n"
      "v_exp_f32 v151, v151\n"
      "v_exp_f32 v120, v120\n"
      "v_exp_f32 v121, v121\n"
      "v_exp_f32 v122, v122\n"
      "v_exp_f32 v123, v123\n"
      "v_cvt_pk_bf16_f32 v148, v148, v149\n"
      "v_cvt_pk_bf16_f32 v149, v150, v151\n"
      "v_cvt_pk_bf16_f32 v150, v120, v121\n"
      "v_cvt_pk_bf16_f32 v151, v122, v123\n"
      "v_mfma_f32_16x16x32_bf16 v[130:133], v[192:195], v[116:119], 0\n"
      "v_exp_f32 v136, v136\n"
      "v_mfma_f32_16x16x32_bf16 v[130:133], v[80:83], v[88:91], v[130:133]\n"
      "v_exp_f32 v137, v137\n"
      "v_mfma_f32_16x16x32_bf16 v[120:123], v[164:167], v[116:119], 0\n"
      "v_exp_f32 v138, v138\n"
      "v_mfma_f32_16x16x32_bf16 v[120:123], v[84:87], v[88:91], v[120:123]\n"
      "ds_read_b128 v[156:159], v39 offset:128\n"
      "v_exp_f32 v139, v139\n"
      "v_exp_f32 v184, v184\n"
      "v_cvt_pk_bf16_f32 v92, v136, v137\n"
      "v_cvt_pk_bf16_f32 v93, v138, v139\n"
      "v_cvt_pk_bf16_f32 v94, v184, 0\n"
      "v_rcp_f32 v210, v210\n"
      "v_cmp_ne_u32 s[56:57], 0, v134\n"
      "v_mul_f32 v188, v210, v188\n"
      "v_mul_f32 v189, v210, v189\n"
      "v_cvt_pk_bf16_f32 v188, v188, v189\n"
      "v_mul_f32 v190, v210, v190\n"
      "v_mul_f32 v191, v210, v191\n"
      "v_cvt_pk_bf16_f32 v189, v190, v191\n"
      "v_mul_f32 v130, v210, v130\n"
      "v_mul_f32 v131, v210, v131\n"
      "v_mul_f32 v132, v210, v132\n"
      "v_mul_f32 v133, v210, v133\n"
      "v_cvt_pk_bf16_f32 v190, v130, v131\n"
      "v_mfma_f32_16x16x32_bf16 v[180:183], v[96:99], v[196:199], v[64:67]\n"
      "s_waitcnt lgkmcnt(0)\n"
      "v_cvt_pk_bf16_f32 v191, v132, v133\n"
      "v_mul_f32 v120, v210, v120\n"
      "v_mul_f32_e64 v121, v210, v121\n"
      "ds_write_b128 v3, v[188:191]\n"
      "v_mfma_f32_16x16x32_bf16 v[212:215], v[168:171], v[196:199], v[68:71]\n"
      "v_cvt_pk_bf16_f32 v120, v120, v121\n"
      "v_mul_f32 v122, v210, v122\n"
      "v_mul_f32 v123, v210, v123\n"
      "v_mfma_f32_16x16x32_bf16 v[116:119], v[202:205], v[196:199], v[72:75]\n"
      "v_cvt_pk_bf16_f32 v121, v122, v123\n"
      "ds_write_b64 v4, v[120:121]\n"
      "v_mfma_f32_16x16x32_bf16 v[188:191], v[218:221], v[196:199], v[28:31]\n"
      "ds_read_b128 v[136:139], v39 offset:192\n"
      "v_cvt_pk_bf16_f32 v152, v152, v153\n"
      "v_mfma_f32_16x16x16_bf16 v[180:183], v[100:101], v[176:177], v[180:183]\n"
      "v_cvt_pk_bf16_f32 v154, v154, v155\n"
      "v_cvt_pk_bf16_f32 v160, v160, v161\n"
      "v_cvt_pk_bf16_f32 v162, v162, v163\n"
      "v_mfma_f32_16x16x16_bf16 v[212:215], v[206:207], v[176:177], v[212:215]\n"
      "v_cvt_pk_bf16_f32 v108, v108, v109\n"
      "v_cvt_pk_bf16_f32 v110, v110, v111\n"
      "v_cvt_pk_bf16_f32 v126, v126, v127\n"
      "v_mfma_f32_16x16x16_bf16 v[116:119], v[222:223], v[176:177], v[116:119]\n"
      "v_cvt_pk_bf16_f32 v128, v128, v129\n"
      "v_accvgpr_write_b32 a216, v152\n"
      "v_accvgpr_write_b32 a217, v154\n"
      "v_mfma_f32_16x16x16_bf16 v[188:191], v[216:217], v[176:177], v[188:191]\n"
      "v_accvgpr_write_b32 a218, v160\n"
      "v_accvgpr_write_b32 a219, v162\n"
      "v_accvgpr_write_b32 a240, v108\n"
      "v_mfma_f32_16x16x32_bf16 v[184:187], v[144:147], a[220:223], v[156:159]\n"
      "ds_read_b128 v[122:125], v10\n"
      "v_accvgpr_write_b32 a241, v110\n"
      "v_accvgpr_write_b32 a242, v126\n"
      "v_accvgpr_write_b32 a243, v128\n"
      "s_cbranch_vccnz L_rare_7_%=\n"
      "L_back_8_%=:\n"
      "s_waitcnt lgkmcnt(1)\n"
      "v_mfma_f32_16x16x32_bf16 v[132:135], v[140:143], a[220:223], v[136:139]\n"
      "v_exp_f32 v180, v180\n"
      "v_mfma_f32_16x16x32_bf16 v[160:163], v[144:147], a[244:247], v[156:159]\n"
      "v_exp_f32 v181, v181\n"
      "v_mfma_f32_16x16x32_bf16 v[108:111], v[140:143], a[244:247], v[136:139]\n"
      "v_exp_f32 v182, v182\n"
      "v_mfma_f32_16x16x32_bf16 v[128:131], v[144:147], v[236:239], v[156:159]\n"
      "v_exp_f32 v183, v183\n"
      "v_mfma_f32_16x16x32_bf16 v[152:155], v[140:143], v[236:239], v[136:139]\n"
      "s_waitcnt lgkmcnt(0)\n"
      "v_exp_f32 v212, v212\n"
      "v_exp_f32 v213, v213\n"
      "v_exp_f32 v214, v214\n"
      "v_exp_f32_e64 v215, v215\n"
      "v_cvt_pk_bf16_f32 v180, v180, v181\n"
      "v_cvt_pk_bf16_f32 v181, v182, v183\n"
      "v_cvt_pk_bf16_f32 v182, v212, v213\n"
      "ds_read_b64 v[126:127], v10 offset:16\n"
      "v_cvt_pk_bf16_f32 v183, v214, v215\n"
      "v_exp_f32 v116, v116\n"
      "v_exp_f32 v117, v117\n"
      "v_exp_f32 v118, v118\n"
      "v_exp_f32 v119, v119\n"
      "v_exp_f32_e64 v188, v188\n"
      "v_cvt_pk_bf16_f32 v88, v116, v117\n"
      "v_mfma_f32_16x16x32_bf16 v[136:139], v[32:35], v[148:151], 0\n"
      "v_cvt_pk_bf16_f32 v89, v118, v119\n"
      "v_cvt_pk_bf16_f32 v90, v188, 0\n"
      "v_mfma_f32_16x16x32_bf16 v[136:139], v[32:35], v[92:95], v[136:139]\n"
      "v_cvt_pk_bf16_f32 v184, v184, v185\n"
      "v_cvt_pk_bf16_f32 v186, v186, v187\n"
      "v_mfma_f32_16x16x32_bf16 v[116:119], v[112:115], v[148:151], 0\n"
      "v_cvt_pk_bf16_f32 v132, v132, v133\n"
      "v_cvt_pk_bf16_f32 v134, v134, v135\n"
      "v_mfma_f32_16x16x32_bf16 v[116:119], v[76:79], v[92:95], v[116:119]\n"
      "ds_read_b128 v[156:159], v39 offset:256\n"
      "v_cvt_pk_bf16_f32 v160, v160, v161\n"
      "v_rcp_f32_e64 v136, v136\n"
      "v_cvt_pk_bf16_f32 v162, v162, v163\n"
      "v_cvt_pk_bf16_f32 v108, v108, v109\n"
      "v_cvt_pk_bf16_f32 v110, v110, v111\n"
      "v_mfma_f32_16x16x32_bf16 v[176:179], v[192:195], v[148:151], 0\n"
      "s_waitcnt lgkmcnt(1)\n"
      "v_mul_f32 v116, v136, v116\n"
      "v_mul_f32 v117, v136, v117\n"
      "v_mfma_f32_16x16x32_bf16 v[176:179], v[80:83], v[92:95], v[176:179]\n"
      "v_cvt_pk_bf16_f32 v116, v116, v117\n"
      "v_mul_f32 v118, v136, v118\n"
      "v_mfma_f32_16x16x32_bf16 v[212:215], v[164:167], v[148:151], 0\n"
      "v_mul_f32_e64 v119, v136, v119\n"
      "v_cvt_pk_bf16_f32 v117, v118, v119\n"
      "v_mfma_f32_16x16x32_bf16 v[212:215], v[84:87], v[92:95], v[212:215]\n"
      "v_cvt_pk_bf16_f32 v232, v172, v173\n"
      "ds_read_b128 v[188:191], v39 offset:320\n"
      "v_mfma_f32_16x16x32_bf16 v[148:151], v[122:125], v[96:99], v[30:33]\n"
      "s_waitcnt lgkmcnt(1)\n"
      "v_mul_f32 v176, v136, v176\n"
      "v_mul_f32 v177, v136, v177\n"
      "v_mfma_f32_16x16x32_bf16 v[96:99], v[122:125], v[168:171], v[30:33]\n"
      "v_mul_f32 v178, v136, v178\n"
      "v_mul_f32_e64 v179, v136, v179\n"
      "v_mfma_f32_16x16x32_bf16 v[168:171], v[122:125], v[202:205], v[30:33]\n"
      "v_cvt_pk_bf16_f32 v118, v176, v177\n"
      "v_cvt_pk_bf16_f32 v119, v178, v179\n"
      "v_mfma_f32_16x16x32_bf16 v[176:179], v[122:125], v[218:221], v[30:33]\n"
      "ds_write_b128 v3, v[116:119] offset:256\n"
      "v_mfma_f32_16x16x16_bf16 v[148:151], v[126:127], v[100:101], v[148:151]\n"
      "v_mul_f32 v212, v136, v212\n"
      "v_mul_f32 v213, v136, v213\n"
      "v_cvt_pk_bf16_f32 v212, v212, v213\n"
      "v_mfma_f32_16x16x16_bf16 v[96:99], v[126:127], v[206:207], v[96:99]\n"
      "v_mul_f32 v214, v136, v214\n"
      "v_mul_f32 v215, v136, v215\n"
      "v_cvt_pk_bf16_f32 v213, v214, v215\n"
      "v_mfma_f32_16x16x16_bf16 v[168:171], v[126:127], v[222:223], v[168:171]\n"
      "ds_write_b64 v4, v[212:213] offset:256\n"
      "v_mfma_f32_16x16x16_bf16 v[176:179], v[126:127], v[216:217], v[176:179]\n"
      "v_cvt_pk_bf16_f32 v233, v174, v175\n"
      "v_cvt_pk_bf16_f32 v234, v102, v103\n"
      "v_cndmask_b32 v148, v148, v96, s[58:59]\n"
      "v_mfma_f32_16x16x32_bf16 v[200:203], v[144:147], a[224:227], v[156:159]\n"
      "s_waitcnt lgkmcnt(2)\n"
      "ds_read_b128 v[172:175], v39 offset:384\n"
      "v_mfma_f32_16x16x32_bf16 v[136:139], v[140:143], a[224:227], v[188:191]\n"
      "ds_read_b128 v[208:211], v39 offset:448\n"
      "v_cndmask_b32 v148, v148, v168, s[60:61]\n"
      "v_mfma_f32_16x16x32_bf16 v[96:99], v[144:147], a[248:251], v[156:159]\n"
      "v_cndmask_b32 v148, v148, v176, s[62:63]\n"
      "s_cbranch_vccnz L_rare_9_%=\n"
      "L_back_10_%=:\n"
      "v_mfma_f32_16x16x32_bf16 v[116:119], v[140:143], a[248:251], v[188:191]\n"
      "s_cmp_ge_u32 s95, 0x40000000\n"
      "s_cselect_b64 vcc, -1, s[56:57]\n"
      "v_exp_f32 v148, v148\n"
      "v_mfma_f32_16x16x32_bf16 v[124:127], v[144:147], v[240:243], v[156:159]\n"
      "v_cvt_pk_bf16_f32 v148, v148, v148\n"
      "ds_write_b16 v13, v148\n"
      "v_mfma_f32_16x16x32_bf16 v[176:179], v[140:143], v[240:243], v[188:191]\n"
      "s_waitcnt lgkmcnt(2)\n"
      "v_cvt_pk_bf16_f32 v200, v200, v201\n"
      "v_cvt_pk_bf16_f32 v202, v202, v203\n"
      "v_mfma_f32_16x16x32_bf16 v[196:199], v[32:35], v[180:183], 0\n"
      "ds_read_b128 v[148:151], v39 offset:512\n"
      "v_cvt_pk_bf16_f32 v136, v136, v137\n"
      "v_mfma_f32_16x16x32_bf16 v[196:199], v[32:35], v[88:91], v[196:199]\n"
      "ds_read_b128 v[156:159], v39 offset:576\n"
      "v_cvt_pk_bf16_f32 v138, v138, v139\n"
      "v_mfma_f32_16x16x32_bf16 v[220:223], v[112:115], v[180:183], 0\n"
      "s_waitcnt lgkmcnt(3)\n"
      "v_cvt_pk_bf16_f32 v96, v96, v97\n"
      "v_cvt_pk_bf16_f32 v98, v98, v99\n"
      "v_mfma_f32_16x16x32_bf16 v[220:223], v[76:79], v[88:91], v[220:223]\n"
      "v_cvt_pk_bf16_f32 v116, v116, v117\n"
      "v_rcp_f32 v196, v196\n"
      "v_mfma_f32_16x16x32_bf16 v[216:219], v[192:195], v[180:183], 0\n"
      "v_cvt_pk_bf16_f32 v118, v118, v119\n"
      "v_cvt_pk_bf16_f32 v235, v104, v105\n"
      "v_mfma_f32_16x16x32_bf16 v[216:219], v[80:83], v[88:91], v[216:219]\n"
      "v_accvgpr_write_b32 a220, v184\n"
      "v_accvgpr_write_b32 a221, v186\n"
      "v_mfma_f32_16x16x32_bf16 v[204:207], v[164:167], v[180:183], 0\n"
      "ds_read_b128 v[212:215], v39 offset:640\n"
      "v_mul_f32 v220, v196, v220\n"
      "v_mfma_f32_16x16x32_bf16 v[204:207], v[84:87], v[88:91], v[204:207]\n"
      "ds_read_b128 v[100:103], v39 offset:704\n"
      "v_mul_f32 v221, v196, v221\n"
      "v_mfma_f32_16x16x32_bf16 v[168:171], v[144:147], a[228:231], v[172:175]\n"
      "s_waitcnt lgkmcnt(2)\n"
      "v_cvt_pk_bf16_f32 v220, v220, v221\n"
      "v_mul_f32 v222, v196, v222\n"
      "v_mfma_f32_16x16x32_bf16 v[184:187], v[140:143], a[228:231], v[208:211]\n"
      "v_mul_f32 v223, v196, v223\n"
      "v_cvt_pk_bf16_f32 v221, v222, v223\n"
      "v_mfma_f32_16x16x32_bf16 v[188:191], v[144:147], a[252:255], v[172:175]\n"
      "v_mul_f32 v216, v196, v216\n"
      "v_mul_f32 v217, v196, v217\n"
      "v_mul_f32 v218, v196, v218\n"
      "v_mul_f32 v219, v196, v219\n"
      "v_cvt_pk_bf16_f32 v222, v216, v217\n"
      "v_mfma_f32_16x16x32_bf16 v[120:123], v[140:143], a[252:255], v[208:211]\n"
      "v_cvt_pk_bf16_f32 v223, v218, v219\n"
      "v_mul_f32 v204, v196, v204\n"
      "v_mfma_f32_16x16x32_bf16 v[216:219], v[144:147], v[244:247], v[172:175]\n"
      "ds_write_b128 v3, v[220:223] offset:512\n"
      "v_mfma_f32_16x16x32_bf16 v[180:183], v[140:143], v[244:247], v[208:211]\n"
      "ds_read_b128 v[104:107], v14\n"
      "v_mul_f32 v205, v196, v205\n"
      "v_cvt_pk_bf16_f32 v204, v204, v205\n"
      "v_mul_f32 v206, v196, v206\n"
      "v_mul_f32_e64 v207, v196, v207\n"
      "v_mfma_f32_16x16x32_bf16 v[208:211], v[144:147], a[232:235], v[148:151]\n"
      "s_waitcnt lgkmcnt(2)\n"
      "v_cvt_pk_bf16_f32 v205, v206, v207\n"
      "ds_write_b64 v4, v[204:205] offset:512\n"
      "v_mfma_f32_16x16x32_bf16 v[172:175], v[140:143], a[232:235], v[156:159]\n"
      "v_cvt_pk_bf16_f32 v168, v168, v169\n"
      "v_cvt_pk_bf16_f32 v170, v170, v171\n"
      "v_cvt_pk_bf16_f32 v184, v184, v185\n"
      "v_cvt_pk_bf16_f32 v186, v186, v187\n"
      "v_mfma_f32_16x16x32_bf16 v[196:199], v[144:147], v[224:227], v[148:151]\n"
      "v_cvt_pk_bf16_f32 v188, v188, v189\n"
      "v_cvt_pk_bf16_f32 v190, v190, v191\n"
      "v_cvt_pk_bf16_f32 v120, v120, v121\n"
      "v_cvt_pk_bf16_f32 v122, v122, v123\n"
      "v_mfma_f32_16x16x32_bf16 v[204:207], v[140:143], v[224:227], v[156:159]\n"
      "s_waitcnt lgkmcnt(1)\n"
      "v_cvt_pk_bf16_f32 v208, v208, v209\n"
      "v_cvt_pk_bf16_f32 v210, v210, v211\n"
      "v_mfma_f32_16x16x32_bf16 v[220:223], v[144:147], v[248:251], v[148:151]\n"
      "v_cvt_pk_bf16_f32 v172, v172, v173\n"
      "v_cvt_pk_bf16_f32 v174, v174, v175\n"
      "v_mfma_f32_16x16x32_bf16 v[148:151], v[140:143], v[248:251], v[156:159]\n"
      "s_nop 2\n"
      "ds_read_b128 v[156:159], v14 offset:64\n"
      "v_accvgpr_write_b32 a222, v132\n"
      "v_accvgpr_write_b32 a223, v134\n"
      "v_accvgpr_write_b32 a244, v160\n"
      "v_accvgpr_write_b32 a245, v162\n"
      "v_mfma_f32_16x16x32_bf16 v[132:135], v[144:147], a[236:239], v[212:215]\n"
      "v_accvgpr_write_b32 a246, v108\n"
      "v_accvgpr_write_b32 a247, v110\n"
      "v_cvt_pk_bf16_f32 v236, v128, v129\n"
      "v_mfma_f32_16x16x32_bf16 v[160:163], v[140:143], a[236:239], v[100:103]\n"
      "v_cvt_pk_bf16_f32 v237, v130, v131\n"
      "v_cvt_pk_bf16_f32 v238, v152, v153\n"
      "v_mfma_f32_16x16x32_bf16 v[128:131], v[144:147], v[228:231], v[212:215]\n"
      "v_cvt_pk_bf16_f32 v239, v154, v155\n"
      "v_cvt_pk_bf16_f32 v132, v132, v133\n"
      "v_cvt_pk_bf16_f32 v134, v134, v135\n"
      "v_mfma_f32_16x16x32_bf16 v[108:111], v[140:143], v[228:231], v[100:103]\n"
      "v_accvgpr_write_b32 a224, v200\n"
      "v_cvt_pk_bf16_f32 v160, v160, v161\n"
      "v_mfma_f32_16x16x32_bf16 v[152:155], v[144:147], v[252:255], v[212:215]\n"
      "s_waitcnt lgkmcnt(0)\n"
      "v_cvt_pk_bf16_f32 v162, v162, v163\n"
      "v_accvgpr_write_b32 a225, v202\n"
      "v_mfma_f32_16x16x32_bf16 v[212:215], v[140:143], v[252:255], v[100:103]\n"
      "v_accvgpr_write_b32 a226, v136\n"
      "v_accvgpr_write_b32 a227, v138\n"
      "v_mfma_f32_16x16x32_bf16 v[100:103], v[104:107], v[32:35], 0\n"
      "v_accvgpr_write_b32 a248, v96\n"
      "v_accvgpr_write_b32 a249, v98\n"
      "v_mfma_f32_16x16x32_bf16 v[100:103], v[156:159], v[32:35], v[100:103]\n"
      "v_accvgpr_write_b32 a250, v116\n"
      "v_accvgpr_write_b32 a251, v118\n"
      "v_cvt_pk_bf16_f32 v240, v124, v125\n"
      "v_cvt_pk_bf16_f32 v241, v126, v127\n"
      "v_cvt_pk_bf16_f32 v242, v176, v177\n"
      "v_cvt_pk_bf16_f32 v243, v178, v179\n"
      "v_accvgpr_write_b32 a228, v168\n"
      "v_accvgpr_write_b32 a229, v170\n"
      "v_accvgpr_write_b32 a230, v184\n"
      "v_accvgpr_write_b32 a231, v186\n"
      "v_accvgpr_write_b32 a252, v188\n"
      "v_accvgpr_write_b32 a253, v190\n"
      "v_accvgpr_write_b32 a254, v120\n"
      "v_accvgpr_write_b32 a255, v122\n"
      "v_cvt_pk_bf16_f32 v244, v216, v217\n"
      "v_cvt_pk_bf16_f32 v245, v218, v219\n"
      "v_cvt_pk_bf16_f32 v246, v180, v181\n"
      "v_cvt_pk_bf16_f32 v247, v182, v183\n"
      "v_accvgpr_write_b32 a232, v208\n"
      "v_accvgpr_write_b32 a233, v210\n"
      "v_accvgpr_write_b32 a234, v172\n"
      "v_accvgpr_write_b32 a235, v174\n"
      "v_cvt_pk_bf16_f32 v224, v196, v197\n"
      "v_cvt_pk_bf16_f32 v225, v198, v199\n"
      "v_cvt_pk_bf16_f32 v226, v204, v205\n"
      "v_cvt_pk_bf16_f32 v227, v206, v207\n"
      "v_cvt_pk_bf16_f32 v248, v220, v221\n"
      "v_cvt_pk_bf16_f32 v249, v222, v223\n"
      "v_cvt_pk_bf16_f32 v250, v148, v149\n"
      "v_cvt_pk_bf16_f32 v251, v150, v151\n"
      "v_accvgpr_write_b32 a236, v132\n"
      "v_accvgpr_write_b32 a237, v134\n"
      "v_accvgpr_write_b32 a238, v160\n"
      "v_accvgpr_write_b32 a239, v162\n"
      "v_cvt_pk_bf16_f32 v228, v128, v129\n"
      "v_cvt_pk_bf16_f32 v229, v130, v131\n"
      "v_cvt_pk_bf16_f32 v230, v108, v109\n"
      "v_cvt_pk_bf16_f32 v231, v110, v111\n"
      "v_cvt_pk_bf16_f32 v252, v152, v153\n"
      "v_cvt_pk_bf16_f32 v253, v154, v155\n"
      "v_cvt_pk_bf16_f32 v254, v212, v213\n"
      "v_cvt_pk_bf16_f32 v255, v214, v215\n"
      "s_waitcnt lgkmcnt(0)\n"
      "s_barrier\n"
      "s_or_b64 vcc, vcc, s[54:55]\n"
      "s_cbranch_vccnz L_disp1_%=\n"
      "s_nop 7\n"
      "s_nop 7\n"
      "L_loop1_%=:\n"
      "v_mfma_f32_16x16x32_bf16 v[140:143], v[104:107], v[112:115], 0\n"
      "ds_read_b128 v[182:185], v0 offset:18432\n"
      "ds_read_b128 v[132:135], v1 offset:18432\n"
      "v_mfma_f32_16x16x32_bf16 v[140:143], v[156:159], v[76:79], v[140:143]\n"
      "ds_read_b128 v[124:127], v0 offset:24576\n"
      "s_add_u32 m0, s64, 0\n"
      "v_mfma_f32_16x16x32_bf16 v[112:115], v[104:107], v[192:195], 0\n"
      "buffer_load_dwordx4 v16, s[16:19], s45 offen  lds\n"
      "v_mfma_f32_16x16x32_bf16 v[112:115], v[156:159], v[80:83], v[112:115]\n"
      "s_add_u32 m0, s64, 4096\n"
      "v_mfma_f32_16x16x32_bf16 v[190:193], v[104:107], v[164:167], 0\n"
      "ds_read_b128 v[104:107], v9\n"
      "v_mfma_f32_16x16x32_bf16 v[190:193], v[156:159], v[84:87], v[190:193]\n"
      "v_mov_b32_dpp v100, v100 quad_perm:[0,0,0,0] row_mask:0xf bank_mask:0xf\n"
      "v_rcp_f32_e64 v100, v100\n"
      "s_waitcnt lgkmcnt(3)\n"
      "v_mfma_f32_16x16x32_bf16 v[144:147], a[0:3], v[182:185], 0\n"
      "buffer_load_dwordx4 v17, s[16:19], s45 offen  lds\n"
      "v_mfma_f32_16x16x32_bf16 v[202:205], a[24:27], v[182:185], 0\n"
      "s_add_u32 m0, s64, 8192\n"
      "v_cndmask_b32 v140, v140, v112, s[58:59]\n"
      "v_cndmask_b32 v140, v140, v190, s[60:61]\n"
      "v_mfma_f32_16x16x32_bf16 v[186:189], a[48:51], v[182:185], 0\n"
      "s_waitcnt lgkmcnt(1)\n"
      "v_mul_f32 v140, v100, v140\n"
      "v_cvt_pk_bf16_f32 v140, v140, v140\n"
      "v_mfma_f32_16x16x32_bf16 v[156:159], a[72:75], v[182:185], 0\n"
      "ds_write_b16 v12, v140\n"
      "v_mfma_f32_16x16x32_bf16 v[100:103], a[96:99], v[182:185], 0\n"
      "buffer_load_dwordx4 v18, s[16:19], s45 offen  lds\n"
      "v_mfma_f32_16x16x32_bf16 v[116:119], a[120:123], v[182:185], 0\n"
      "ds_read_b128 v[160:163], v1 offset:24576\n"
      "s_waitcnt lgkmcnt(2)\n"
      "v_add_f32 v104, v104, v105\n"
      "v_mfma_f32_16x16x32_bf16 v[144:147], a[4:7], v[132:135], v[144:147]\n"
      "ds_read_b128 v[96:99], v0 offset:30720\n"
      "v_add_f32 v106, v106, v107\n"
      "v_mfma_f32_16x16x32_bf16 v[144:147], a[8:11], v[124:127], v[144:147]\n"
      "ds_read_b128 v[108:111], v1 offset:30720\n"
      "v_add_f32 v104, v104, v106\n"
      "v_mfma_f32_16x16x32_bf16 v[202:205], a[28:31], v[132:135], v[202:205]\n"
      "s_add_u32 m0, s64, 12288\n"
      "v_rsq_f32_e64 v104, v104\n"
      "v_mfma_f32_16x16x32_bf16 v[202:205], a[32:35], v[124:127], v[202:205]\n"
      "buffer_load_dwordx4 v19, s[16:19], s45 offen  lds\n"
      "v_mfma_f32_16x16x32_bf16 v[186:189], a[52:55], v[132:135], v[186:189]\n"
      "s_add_u32 m0, s65, 16384\n"
      "v_mul_f32 v104, 0x415db3d7, v104\n"
      "v_mov_b32_e64 v106, v104\n"
      "v_mfma_f32_16x16x32_bf16 v[186:189], a[56:59], v[124:127], v[186:189]\n"
      "s_waitcnt lgkmcnt(2)\n"
      "v_permlane32_swap_b32 v104, v106\n"
      "v_mfma_f32_16x16x32_bf16 v[156:159], a[76:79], v[132:135], v[156:159]\n"
      "s_waitcnt lgkmcnt(1)\n"
      "v_mov_b32 v105, v104\n"
      "v_mul_f32 v136, v106, v136\n"
      "v_mfma_f32_16x16x32_bf16 v[156:159], a[80:83], v[124:127], v[156:159]\n"
      "buffer_load_dwordx4 v15, s[16:19], s45 offen  lds\n"
      "v_mfma_f32_16x16x32_bf16 v[100:103], a[100:103], v[132:135], v[100:103]\n"
      "s_waitcnt lgkmcnt(0)\n"
      "v_permlane16_swap_b32 v104, v105\n"
      "v_mfma_f32_16x16x32_bf16 v[100:103], a[104:107], v[124:127], v[100:103]\n"
      "v_mul_f32 v220, v104, v220\n"
      "v_mul_f32_e64 v221, v104, v221\n"
      "v_mfma_f32_16x16x32_bf16 v[116:119], a[124:127], v[132:135], v[116:119]\n"
      "v_cvt_pk_bf16_f32 v220, v220, v221\n"
      "v_mul_f32_e64 v222, v104, v222\n"
      "v_mfma_f32_16x16x32_bf16 v[116:119], a[128:131], v[124:127], v[116:119]\n"
      "buffer_load_dwordx4 v[174:177], v36, s[24:27], s47 offen\n"
      "v_mfma_f32_16x16x32_bf16 v[144:147], a[12:15], v[160:163], v[144:147]\n"
      "v_mul_f32 v223, v104, v223\n"
      "v_mul_f32 v214, v104, v214\n"
      "v_mfma_f32_16x16x32_bf16 v[144:147], a[16:19], v[96:99], v[144:147]\n"
      "v_mul_f32_e64 v215, v104, v215\n"
      "v_cvt_pk_bf16_f32 v221, v222, v223\n"
      "v_mfma_f32_16x16x32_bf16 v[144:147], a[20:23], v[108:111], v[144:147]\n"
      "v_mul_f32 v208, v104, v208\n"
      "v_mul_f32 v209, v104, v209\n"
      "v_mfma_f32_16x16x32_bf16 v[202:205], a[36:39], v[160:163], v[202:205]\n"
      "buffer_load_dword v39, v29, s[24:27], s47 offen\n"
      "v_mfma_f32_16x16x32_bf16 v[202:205], a[40:43], v[96:99], v[202:205]\n"
      "v_mul_f32 v210, v104, v210\n"
      "v_mul_f32 v211, v104, v211\n"
      "v_mfma_f32_16x16x32_bf16 v[202:205], a[44:47], v[108:111], v[202:205]\n"
      "v_cvt_pk_bf16_f32 v214, v214, v215\n"
      "v_mul_f32 v216, v104, v216\n"
      "v_mfma_f32_16x16x32_bf16 v[186:189], a[60:63], v[160:163], v[186:189]\n"
      "v_mul_f32 v217, v104, v217\n"
      "v_cvt_pk_bf16_f32 v222, v208, v209\n"
      "v_mfma_f32_16x16x32_bf16 v[186:189], a[64:67], v[96:99], v[186:189]\n"
      "v_cvt_pk_bf16_f32 v223, v210, v211\n"
      "v_cvt_pk_bf16_f32 v215, v216, v217\n"
      "v_mfma_f32_16x16x32_bf16 v[186:189], a[68:71], v[108:111], v[186:189]\n"
      "ds_write_b128 v5, v[220:223] offset:18432\n"
      "v_mfma_f32_16x16x32_bf16 v[156:159], a[84:87], v[160:163], v[156:159]\n"
      "v_mul_f32 v148, v105, v148\n"
      "v_mul_f32 v149, v105, v149\n"
      "v_mfma_f32_16x16x32_bf16 v[156:159], a[88:91], v[96:99], v[156:159]\n"
      "v_cvt_pk_bf16_f32 v148, v148, v149\n"
      "v_mul_f32_e64 v150, v105, v150\n"
      "v_mfma_f32_16x16x32_bf16 v[156:159], a[92:95], v[108:111], v[156:159]\n"
      "ds_write_b64 v6, v[214:215] offset:18432\n"
      "v_mfma_f32_16x16x32_bf16 v[100:103], a[108:111], v[160:163], v[100:103]\n"
      "v_mul_f32 v151, v105, v151\n"
      "v_mul_f32 v120, v105, v120\n"
      "v_mfma_f32_16x16x32_bf16 v[100:103], a[112:115], v[96:99], v[100:103]\n"
      "v_mul_f32_e64 v121, v105, v121\n"
      "v_cvt_pk_bf16_f32 v149, v150, v151\n"
      "v_mfma_f32_16x16x32_bf16 v[100:103], a[116:119], v[108:111], v[100:103]\n"
      "v_mul_f32 v170, v105, v170\n"
      "v_mul_f32 v171, v105, v171\n"
      "v_mfma_f32_16x16x32_bf16 v[116:119], a[132:135], v[160:163], v[116:119]\n"
      "v_mul_f32 v172, v105, v172\n"
      "v_mul_f32 v173, v105, v173\n"
      "v_mfma_f32_16x16x32_bf16 v[116:119], a[136:139], v[96:99], v[116:119]\n"
      "v_cvt_pk_bf16_f32 v120, v120, v121\n"
      "v_mul_f32_e64 v122, v105, v122\n"
      "v_mfma_f32_16x16x32_bf16 v[116:119], a[140:143], v[108:111], v[116:119]\n"
      "v_mul_f32_e64 v123, v105, v123\n"
      "v_cvt_pk_bf16_f32 v150, v170, v171\n"
      "v_mfma_f32_16x16x32_bf16 v[112:115], v[182:185], a[144:147], 0\n"
      "v_cvt_pk_bf16_f32 v151, v172, v173\n"
      "v_cvt_pk_bf16_f32 v121, v122, v123\n"
      "v_mfma_f32_16x16x32_bf16 v[112:115], v[132:135], a[148:151], v[112:115]\n"
      "ds_write_b128 v5, v[148:151] offset:24576\n"
      "v_mfma_f32_16x16x32_bf16 v[112:115], v[124:127], a[152:155], v[112:115]\n"
      "v_mul_f32 v137, v106, v137\n"
      "v_cvt_pk_bf16_f32 v136, v136, v137\n"
      "v_mfma_f32_16x16x32_bf16 v[112:115], v[160:163], a[156:159], v[112:115]\n"
      "v_mul_f32 v138, v106, v138\n"
      "v_mul_f32_e64 v139, v106, v139\n"
      "v_mfma_f32_16x16x32_bf16 v[112:115], v[96:99], a[160:163], v[112:115]\n"
      "ds_write_b64 v6, v[120:121] offset:24576\n"
      "v_mfma_f32_16x16x32_bf16 v[112:115], v[108:111], a[164:167], v[112:115]\n"
      "ds_read_b128 v[208:211], v0 offset:20480\n"
      "v_mul_f32 v178, v106, v178\n"
      "v_mfma_f32_16x16x32_bf16 v[192:195], v[182:185], a[168:171], 0\n"
      "ds_read_b128 v[148:151], v1 offset:20480\n"
      "v_mul_f32 v179, v106, v179\n"
      "v_mfma_f32_16x16x32_bf16 v[192:195], v[132:135], a[172:175], v[192:195]\n"
      "ds_read_b128 v[170:173], v0 offset:26624\n"
      "v_cvt_pk_bf16_f32 v137, v138, v139\n"
      "v_mfma_f32_16x16x32_bf16 v[192:195], v[124:127], a[176:179], v[192:195]\n"
      "ds_read_b128 v[152:155], v1 offset:26624\n"
      "v_mul_f32 v196, v106, v196\n"
      "v_mfma_f32_16x16x32_bf16 v[192:195], v[160:163], a[180:183], v[192:195]\n"
      "ds_read_b128 v[128:131], v0 offset:32768\n"
      "v_mul_f32 v197, v106, v197\n"
      "v_mfma_f32_16x16x32_bf16 v[192:195], v[96:99], a[184:187], v[192:195]\n"
      "ds_read_b128 v[140:143], v1 offset:32768\n"
      "v_mul_f32 v198, v106, v198\n"
      "v_mfma_f32_16x16x32_bf16 v[192:195], v[108:111], a[188:191], v[192:195]\n"
      "v_mul_f32 v199, v106, v199\n"
      "v_cvt_pk_bf16_f32 v178, v178, v179\n"
      "v_mfma_f32_16x16x32_bf16 v[164:167], v[182:185], a[192:195], 0\n"
      "s_waitcnt lgkmcnt(5)\n"
      "v_mul_f32 v180, v106, v180\n"
      "v_mul_f32_e64 v181, v106, v181\n"
      "v_mfma_f32_16x16x32_bf16 v[164:167], v[132:135], a[196:199], v[164:167]\n"
      "s_waitcnt lgkmcnt(4)\n"
      "v_cvt_pk_bf16_f32 v138, v196, v197\n"
      "v_cvt_pk_bf16_f32 v139, v198, v199\n"
      "v_mfma_f32_16x16x32_bf16 v[164:167], v[124:127], a[200:203], v[164:167]\n"
      "ds_write_b128 v5, v[136:139] offset:30720\n"
      "v_mfma_f32_16x16x32_bf16 v[164:167], v[160:163], a[204:207], v[164:167]\n"
      "s_waitcnt lgkmcnt(2)\n"
      "v_cvt_pk_bf16_f32 v179, v180, v181\n"
      "v_fma_f32 v163, v144, v144, s51\n"
      "v_mfma_f32_16x16x32_bf16 v[164:167], v[96:99], a[208:211], v[164:167]\n"
      "s_waitcnt lgkmcnt(1)\n"
      "v_fma_f32 v160, v156, v156, s51\n"
      "v_fmac_f32 v163, v145, v145\n"
      "v_mfma_f32_16x16x32_bf16 v[164:167], v[108:111], a[212:215], v[164:167]\n"
      "ds_write_b64 v6, v[178:179] offset:30720\n"
      "v_mfma_f32_16x16x32_bf16 v[108:111], a[0:3], v[208:211], 0\n"
      "v_fmac_f32 v160, v157, v157\n"
      "v_fmac_f32 v163, v146, v146\n"
      "v_mfma_f32_16x16x32_bf16 v[108:111], a[4:7], v[148:151], v[108:111]\n"
      "v_fmac_f32 v160, v158, v158\n"
      "v_fmac_f32 v163, v147, v147\n"
      "v_mfma_f32_16x16x32_bf16 v[108:111], a[8:11], v[170:173], v[108:111]\n"
      "v_fmac_f32 v160, v159, v159\n"
      "v_fmac_f32 v163, v202, v202\n"
      "v_mfma_f32_16x16x32_bf16 v[108:111], a[12:15], v[152:155], v[108:111]\n"
      "v_fmac_f32 v160, v100, v100\n"
      "v_fmac_f32 v163, v203, v203\n"
      "v_mfma_f32_16x16x32_bf16 v[108:111], a[16:19], v[128:131], v[108:111]\n"
      "v_fmac_f32 v160, v101, v101\n"
      "v_fmac_f32 v163, v204, v204\n"
      "v_mfma_f32_16x16x32_bf16 v[108:111], a[20:23], v[140:143], v[108:111]\n"
      "v_fmac_f32 v160, v102, v102\n"
      "v_fmac_f32 v163, v205, v205\n"
      "v_mfma_f32_16x16x32_bf16 v[96:99], a[24:27], v[208:211], 0\n"
      "v_fmac_f32 v160, v103, v103\n"
      "v_fmac_f32 v163, v186, v186\n"
      "v_mfma_f32_16x16x32_bf16 v[96:99], a[28:31], v[148:151], v[96:99]\n"
      "v_fmac_f32 v160, v116, v116\n"
      "v_fmac_f32 v163, v187, v187\n"
      "v_mfma_f32_16x16x32_bf16 v[96:99], a[32:35], v[170:173], v[96:99]\n"
      "v_fmac_f32 v160, v117, v117\n"
      "v_fmac_f32 v163, v188, v188\n"
      "v_mfma_f32_16x16x32_bf16 v[96:99], a[36:39], v[152:155], v[96:99]\n"
      "v_fmac_f32 v160, v118, v118\n"
      "v_fmac_f32 v163, v189, v189\n"
      "v_mfma_f32_16x16x32_bf16 v[96:99], a[40:43], v[128:131], v[96:99]\n"
      "v_fmac_f32_e64 v160, v119, v119\n"
      "v_fma_f32 v126, v108, v108, s51\n"
      "v_mfma_f32_16x16x32_bf16 v[96:99], a[44:47], v[140:143], v[96:99]\n"
      "v_permlane16_swap_b32 v163, v160\n"
      "v_mfma_f32_16x16x32_bf16 v[104:107], a[48:51], v[208:211], 0\n"
      "v_add_f32 v163, v163, v160\n"
      "v_mov_b32 v160, v163\n"
      "v_mfma_f32_16x16x32_bf16 v[104:107], a[52:55], v[148:151], v[104:107]\n"
      "v_fmac_f32 v126, v109, v109\n"
      "v_permlane32_swap_b32 v163, v160\n"
      "v_mfma_f32_16x16x32_bf16 v[104:107], a[56:59], v[170:173], v[104:107]\n"
      "v_add_f32 v163, v163, v160\n"
      "v_rsq_f32 v163, v163\n"
      "v_mfma_f32_16x16x32_bf16 v[104:107], a[60:63], v[152:155], v[104:107]\n"
      "v_mov_b32 v160, v163\n"
      "v_fmac_f32 v126, v110, v110\n"
      "v_mfma_f32_16x16x32_bf16 v[104:107], a[64:67], v[128:131], v[104:107]\n"
      "v_permlane16_swap_b32 v163, v160\n"
      "v_mfma_f32_16x16x32_bf16 v[104:107], a[68:71], v[140:143], v[104:107]\n"
      "v_mul_f32 v163, s50, v163\n"
      "v_mul_f32 v156, v160, v156\n"
      "v_mfma_f32_16x16x32_bf16 v[178:181], a[72:75], v[208:211], 0\n"
      "v_mul_f32 v157, v160, v157\n"
      "v_mul_f32 v144, v163, v144\n"
      "v_mfma_f32_16x16x32_bf16 v[178:181], a[76:79], v[148:151], v[178:181]\n"
      "v_mul_f32 v145, v163, v145\n"
      "v_cvt_pk_bf16_f32 v156, v156, v157\n"
      "v_mfma_f32_16x16x32_bf16 v[178:181], a[80:83], v[170:173], v[178:181]\n"
      "v_mul_f32 v158, v160, v158\n"
      "v_mul_f32 v159, v160, v159\n"
      "v_mfma_f32_16x16x32_bf16 v[178:181], a[84:87], v[152:155], v[178:181]\n"
      "v_mul_f32 v116, v160, v116\n"
      "v_mul_f32_e64 v117, v160, v117\n"
      "v_mfma_f32_16x16x32_bf16 v[178:181], a[88:91], v[128:131], v[178:181]\n"
      "v_cvt_pk_bf16_f32 v144, v144, v145\n"
      "v_mul_f32 v146, v163, v146\n"
      "v_mfma_f32_16x16x32_bf16 v[178:181], a[92:95], v[140:143], v[178:181]\n"
      "v_mul_f32 v147, v163, v147\n"
      "v_mul_f32_e64 v186, v163, v186\n"
      "v_mfma_f32_16x16x32_bf16 v[136:139], a[96:99], v[208:211], 0\n"
      "v_mul_f32 v187, v163, v187\n"
      "v_cvt_pk_bf16_f32 v157, v158, v159\n"
      "v_mfma_f32_16x16x32_bf16 v[136:139], a[100:103], v[148:151], v[136:139]\n"
      "v_mul_f32 v100, v160, v100\n"
      "v_mul_f32 v101, v160, v101\n"
      "v_mfma_f32_16x16x32_bf16 v[136:139], a[104:107], v[170:173], v[136:139]\n"
      "v_mul_f32 v102, v160, v102\n"
      "v_mul_f32_e64 v103, v160, v103\n"
      "v_mfma_f32_16x16x32_bf16 v[136:139], a[108:111], v[152:155], v[136:139]\n"
      "v_cvt_pk_bf16_f32 v116, v116, v117\n"
      "v_mul_f32 v118, v160, v118\n"
      "v_mfma_f32_16x16x32_bf16 v[136:139], a[112:115], v[128:131], v[136:139]\n"
      "v_mul_f32 v119, v160, v119\n"
      "v_cvt_pk_bf16_f32 v145, v146, v147\n"
      "v_mfma_f32_16x16x32_bf16 v[136:139], a[116:119], v[140:143], v[136:139]\n"
      "v_mul_f32 v202, v163, v202\n"
      "v_mul_f32 v203, v163, v203\n"
      "v_mfma_f32_16x16x32_bf16 v[212:215], a[120:123], v[208:211], 0\n"
      "v_mul_f32 v204, v163, v204\n"
      "v_mul_f32 v205, v163, v205\n"
      "v_mfma_f32_16x16x32_bf16 v[212:215], a[124:127], v[148:151], v[212:215]\n"
      "v_cvt_pk_bf16_f32 v186, v186, v187\n"
      "v_mul_f32 v188, v163, v188\n"
      "v_mfma_f32_16x16x32_bf16 v[212:215], a[128:131], v[170:173], v[212:215]\n"
      "v_mul_f32 v189, v163, v189\n"
      "v_cvt_pk_bf16_f32 v158, v100, v101\n"
      "v_mfma_f32_16x16x32_bf16 v[212:215], a[132:135], v[152:155], v[212:215]\n"
      "ds_read_b128 v[160:163], v7\n"
      "v_cvt_pk_bf16_f32 v159, v102, v103\n"
      "v_mfma_f32_16x16x32_bf16 v[212:215], a[136:139], v[128:131], v[212:215]\n"
      "ds_read_b128 v[182:185], v7 offset:4096\n"
      "v_cvt_pk_bf16_f32 v117, v118, v119\n"
      "v_mfma_f32_16x16x32_bf16 v[212:215], a[140:143], v[140:143], v[212:215]\n"
      "v_cvt_pk_bf16_f32 v146, v202, v203\n"
      "v_cvt_pk_bf16_f32 v147, v204, v205\n"
      "v_mfma_f32_16x16x32_bf16 v[204:207], v[208:211], a[144:147], 0\n"
      "ds_read_b128 v[100:103], v7 offset:8192\n"
      "v_cvt_pk_bf16_f32 v187, v188, v189\n"
      "v_mfma_f32_16x16x32_bf16 v[204:207], v[148:151], a[148:151], v[204:207]\n"
      "ds_read_b128 v[118:121], v7 offset:12288\n"
      "v_fma_f32 v127, v178, v178, s51\n"
      "v_mfma_f32_16x16x32_bf16 v[204:207], v[170:173], a[152:155], v[204:207]\n"
      "v_fmac_f32 v127, v179, v179\n"
      "v_fmac_f32 v127, v180, v180\n"
      "v_mfma_f32_16x16x32_bf16 v[204:207], v[152:155], a[156:159], v[204:207]\n"
      "ds_read_b128 v[132:135], v7 offset:16384\n"
      "v_fmac_f32 v126, v111, v111\n"
      "v_mfma_f32_16x16x32_bf16 v[204:207], v[128:131], a[160:163], v[204:207]\n"
      "v_fmac_f32 v127, v181, v181\n"
      "v_fmac_f32_e64 v126, v96, v96\n"
      "v_mfma_f32_16x16x32_bf16 v[204:207], v[140:143], a[164:167], v[204:207]\n"
      "ds_read_b128 v[220:223], v0 offset:22528\n"
      "v_fmac_f32 v127, v136, v136\n"
      "v_mfma_f32_16x16x32_bf16 v[122:125], v[208:211], a[168:171], 0\n"
      "ds_read_b128 v[188:191], v1 offset:22528\n"
      "v_fmac_f32 v126, v97, v97\n"
      "v_mfma_f32_16x16x32_bf16 v[122:125], v[148:151], a[172:175], v[122:125]\n"
      "s_waitcnt lgkmcnt(3)\n"
      "v_fmac_f32 v127, v137, v137\n"
      "v_fmac_f32_e64 v126, v98, v98\n"
      "v_mfma_f32_16x16x32_bf16 v[122:125], v[170:173], a[176:179], v[122:125]\n"
      "buffer_store_dwordx4 v[160:163], v20, s[20:23], s46 offen sc1\n"
      "v_mfma_f32_16x16x32_bf16 v[122:125], v[152:155], a[180:183], v[122:125]\n"
      "ds_read_b128 v[200:203], v0 offset:28672\n"
      "v_fmac_f32_e64 v127, v138, v138\n"
      "v_mfma_f32_16x16x32_bf16 v[122:125], v[128:131], a[184:187], v[122:125]\n"
      "ds_read_b128 v[196:199], v1 offset:28672\n"
      "v_fmac_f32_e64 v126, v99, v99\n"
      "v_mfma_f32_16x16x32_bf16 v[122:125], v[140:143], a[188:191], v[122:125]\n"
      "ds_read_b128 v[216:219], v0 offset:34816\n"
      "v_fmac_f32 v127, v139, v139\n"
      "v_mfma_f32_16x16x32_bf16 v[160:163], v[208:211], a[192:195], 0\n"
      "s_waitcnt lgkmcnt(3)\n"
      "v_fmac_f32 v126, v104, v104\n"
      "v_fmac_f32 v127, v212, v212\n"
      "v_mfma_f32_16x16x32_bf16 v[160:163], v[148:151], a[196:199], v[160:163]\n"
      "ds_read_b128 v[208:211], v1 offset:34816\n"
      "v_fmac_f32 v126, v105, v105\n"
      "v_mfma_f32_16x16x32_bf16 v[160:163], v[170:173], a[200:203], v[160:163]\n"
      "v_fmac_f32 v127, v213, v213\n"
      "v_fmac_f32 v126, v106, v106\n"
      "v_mfma_f32_16x16x32_bf16 v[160:163], v[152:155], a[204:207], v[160:163]\n"
      "s_waitcnt lgkmcnt(3)\n"
      "v_fmac_f32 v127, v214, v214\n"
      "v_fmac_f32 v126, v107, v107\n"
      "v_mfma_f32_16x16x32_bf16 v[160:163], v[128:131], a[208:211], v[160:163]\n"
      "buffer_store_dwordx4 v[182:185], v21, s[20:23], s46 offen sc1\n"
      "v_mfma_f32_16x16x32_bf16 v[160:163], v[140:143], a[212:215], v[160:163]\n"
      "s_waitcnt lgkmcnt(1)\n"
      "v_fmac_f32 v127, v215, v215\n"
      "v_cvt_pk_bf16_f32 v112, v112, v113\n"
      "v_mfma_f32_16x16x32_bf16 v[182:185], a[0:3], v[220:223], 0\n"
      "s_waitcnt lgkmcnt(0)\n"
      "v_permlane16_swap_b32 v126, v127\n"
      "v_mfma_f32_16x16x32_bf16 v[182:185], a[4:7], v[188:191], v[182:185]\n"
      "v_add_f32 v126, v126, v127\n"
      "v_mov_b32 v127, v126\n"
      "v_mfma_f32_16x16x32_bf16 v[182:185], a[8:11], v[200:203], v[182:185]\n"
      "v_cvt_pk_bf16_f32 v113, v114, v115\n"
      "v_permlane32_swap_b32 v126, v127\n"
      "v_mfma_f32_16x16x32_bf16 v[182:185], a[12:15], v[196:199], v[182:185]\n"
      "v_add_f32 v126, v126, v127\n"
      "v_rsq_f32 v126, v126\n"
      "v_mfma_f32_16x16x32_bf16 v[182:185], a[16:19], v[216:219], v[182:185]\n"
      "v_mov_b32 v127, v126\n"
      "v_cvt_pk_bf16_f32 v114, v204, v205\n"
      "v_mfma_f32_16x16x32_bf16 v[182:185], a[20:23], v[208:211], v[182:185]\n"
      "v_permlane16_swap_b32 v126, v127\n"
      "v_mfma_f32_16x16x32_bf16 v[148:151], a[24:27], v[220:223], 0\n"
      "buffer_store_dwordx4 v[100:103], v22, s[20:23], s46 offen sc1\n"
      "v_mfma_f32_16x16x32_bf16 v[148:151], a[28:31], v[188:191], v[148:151]\n"
      "v_mul_f32 v126, s50, v126\n"
      "v_mul_f32 v178, v127, v178\n"
      "v_mfma_f32_16x16x32_bf16 v[148:151], a[32:35], v[200:203], v[148:151]\n"
      "v_mul_f32 v179, v127, v179\n"
      "v_mul_f32 v108, v126, v108\n"
      "v_mfma_f32_16x16x32_bf16 v[148:151], a[36:39], v[196:199], v[148:151]\n"
      "v_mul_f32 v109, v126, v109\n"
      "v_cvt_pk_bf16_f32 v178, v178, v179\n"
      "v_mfma_f32_16x16x32_bf16 v[148:151], a[40:43], v[216:219], v[148:151]\n"
      "v_mul_f32 v180, v127, v180\n"
      "v_mul_f32 v181, v127, v181\n"
      "v_mfma_f32_16x16x32_bf16 v[148:151], a[44:47], v[208:211], v[148:151]\n"
      "v_mul_f32 v212, v127, v212\n"
      "v_mul_f32 v213, v127, v213\n"
      "v_mfma_f32_16x16x32_bf16 v[168:171], a[48:51], v[220:223], 0\n"
      "v_cvt_pk_bf16_f32 v108, v108, v109\n"
      "v_mul_f32_e64 v110, v126, v110\n"
      "v_mfma_f32_16x16x32_bf16 v[168:171], a[52:55], v[188:191], v[168:171]\n"
      "v_mul_f32 v111, v126, v111\n"
      "v_mul_f32 v104, v126, v104\n"
      "v_mfma_f32_16x16x32_bf16 v[168:171], a[56:59], v[200:203], v[168:171]\n"
      "buffer_store_dwordx4 v[118:121], v23, s[20:23], s46 offen sc1\n"
      "v_mfma_f32_16x16x32_bf16 v[168:171], a[60:63], v[196:199], v[168:171]\n"
      "v_mul_f32 v105, v126, v105\n"
      "v_cvt_pk_bf16_f32 v179, v180, v181\n"
      "v_mfma_f32_16x16x32_bf16 v[168:171], a[64:67], v[216:219], v[168:171]\n"
      "v_mul_f32 v136, v127, v136\n"
      "v_mul_f32 v137, v127, v137\n"
      "v_mfma_f32_16x16x32_bf16 v[168:171], a[68:71], v[208:211], v[168:171]\n"
      "v_mul_f32 v138, v127, v138\n"
      "v_mul_f32_e64 v139, v127, v139\n"
      "v_mfma_f32_16x16x32_bf16 v[100:103], a[72:75], v[220:223], 0\n"
      "v_cvt_pk_bf16_f32 v212, v212, v213\n"
      "v_mul_f32 v214, v127, v214\n"
      "v_mfma_f32_16x16x32_bf16 v[100:103], a[76:79], v[188:191], v[100:103]\n"
      "ds_read_b128 v[140:143], v9 offset:4608\n"
      "v_mul_f32 v215, v127, v215\n"
      "v_mfma_f32_16x16x32_bf16 v[100:103], a[80:83], v[200:203], v[100:103]\n"
      "v_cvt_pk_bf16_f32 v109, v110, v111\n"
      "v_mul_f32 v96, v126, v96\n"
      "v_mfma_f32_16x16x32_bf16 v[100:103], a[84:87], v[196:199], v[100:103]\n"
      "v_mul_f32 v97, v126, v97\n"
      "v_mul_f32 v98, v126, v98\n"
      "v_mfma_f32_16x16x32_bf16 v[100:103], a[88:91], v[216:219], v[100:103]\n"
      "buffer_store_dwordx4 v[132:135], v25, s[20:23], s46 offen sc1\n"
      "v_mfma_f32_16x16x32_bf16 v[100:103], a[92:95], v[208:211], v[100:103]\n"
      "v_mul_f32 v99, v126, v99\n"
      "v_cvt_pk_bf16_f32 v104, v104, v105\n"
      "v_mfma_f32_16x16x32_bf16 v[130:133], a[96:99], v[220:223], 0\n"
      "v_mul_f32 v106, v126, v106\n"
      "v_mul_f32 v107, v126, v107\n"
      "v_mfma_f32_16x16x32_bf16 v[130:133], a[100:103], v[188:191], v[130:133]\n"
      "v_cvt_pk_bf16_f32 v180, v136, v137\n"
      "v_cvt_pk_bf16_f32 v181, v138, v139\n"
      "v_mfma_f32_16x16x32_bf16 v[130:133], a[104:107], v[200:203], v[130:133]\n"
      "v_cvt_pk_bf16_f32 v213, v214, v215\n"
      "v_cvt_pk_bf16_f32 v110, v96, v97\n"
      "v_mfma_f32_16x16x32_bf16 v[130:133], a[108:111], v[196:199], v[130:133]\n"
      "v_cvt_pk_bf16_f32 v111, v98, v99\n"
      "v_cvt_pk_bf16_f32 v105, v106, v107\n"
      "v_mfma_f32_16x16x32_bf16 v[130:133], a[112:115], v[216:219], v[130:133]\n"
      "v_fma_f32 v172, v182, v182, s51\n"
      "v_fma_f32 v214, v100, v100, s51\n"
      "v_mfma_f32_16x16x32_bf16 v[130:133], a[116:119], v[208:211], v[130:133]\n"
      "v_fmac_f32 v172, v183, v183\n"
      "v_fmac_f32 v214, v101, v101\n"
      "v_mfma_f32_16x16x32_bf16 v[96:99], a[120:123], v[220:223], 0\n"
      "v_fmac_f32 v172, v184, v184\n"
      "v_fmac_f32 v214, v102, v102\n"
      "v_mfma_f32_16x16x32_bf16 v[96:99], a[124:127], v[188:191], v[96:99]\n"
      "v_fmac_f32 v172, v185, v185\n"
      "v_fmac_f32 v214, v103, v103\n"
      "v_mfma_f32_16x16x32_bf16 v[96:99], a[128:131], v[200:203], v[96:99]\n"
      "v_fmac_f32 v172, v148, v148\n"
      "v_fmac_f32 v214, v130, v130\n"
      "v_mfma_f32_16x16x32_bf16 v[96:99], a[132:135], v[196:199], v[96:99]\n"
      "v_fmac_f32 v172, v149, v149\n"
      "v_fmac_f32 v214, v131, v131\n"
      "v_mfma_f32_16x16x32_bf16 v[96:99], a[136:139], v[216:219], v[96:99]\n"
      "v_fmac_f32 v172, v150, v150\n"
      "v_fmac_f32 v214, v132, v132\n"
      "v_mfma_f32_16x16x32_bf16 v[96:99], a[140:143], v[208:211], v[96:99]\n"
      "v_fmac_f32 v172, v151, v151\n"
      "v_fmac_f32 v214, v133, v133\n"
      "v_mfma_f32_16x16x32_bf16 v[136:139], v[220:223], a[144:147], 0\n"
      "v_fmac_f32 v172, v168, v168\n"
      "v_fmac_f32 v172, v169, v169\n"
      "v_mfma_f32_16x16x32_bf16 v[136:139], v[188:191], a[148:151], v[136:139]\n"
      "v_fmac_f32 v172, v170, v170\n"
      "v_fmac_f32 v172, v171, v171\n"
      "v_mfma_f32_16x16x32_bf16 v[136:139], v[200:203], a[152:155], v[136:139]\n"
      "v_fmac_f32 v214, v96, v96\n"
      "v_fmac_f32 v214, v97, v97\n"
      "v_mfma_f32_16x16x32_bf16 v[136:139], v[196:199], a[156:159], v[136:139]\n"
      "v_fmac_f32 v214, v98, v98\n"
      "v_fmac_f32 v214, v99, v99\n"
      "v_mfma_f32_16x16x32_bf16 v[136:139], v[216:219], a[160:163], v[136:139]\n"
      "v_cvt_pk_bf16_f32 v115, v206, v207\n"
      "v_permlane16_swap_b32_e64 v172, v214\n"
      "v_mfma_f32_16x16x32_bf16 v[136:139], v[208:211], a[164:167], v[136:139]\n"
      "ds_read_b128 v[126:129], v2\n"
      "v_add_f32_e64 v172, v172, v214\n"
      "v_mfma_f32_16x16x32_bf16 v[118:121], v[220:223], a[168:171], 0\n"
      "ds_read_b128 v[204:207], v2 offset:3072\n"
      "v_mov_b32 v214, v172\n"
      "v_mfma_f32_16x16x32_bf16 v[118:121], v[188:191], a[172:175], v[118:121]\n"
      "ds_read_b128 v[152:155], v2 offset:6144\n"
      "v_permlane32_swap_b32 v172, v214\n"
      "v_mfma_f32_16x16x32_bf16 v[118:121], v[200:203], a[176:179], v[118:121]\n"
      "v_add_f32 v172, v172, v214\n"
      "v_rsq_f32 v172, v172\n"
      "v_mfma_f32_16x16x32_bf16 v[118:121], v[196:199], a[180:183], v[118:121]\n"
      "v_mov_b32_e64 v214, v172\n"
      "v_cvt_pk_bf16_f32 v76, v136, v137\n"
      "v_mfma_f32_16x16x32_bf16 v[118:121], v[216:219], a[184:187], v[118:121]\n"
      "v_permlane16_swap_b32 v172, v214\n"
      "v_mfma_f32_16x16x32_bf16 v[118:121], v[208:211], a[188:191], v[118:121]\n"
      "s_waitcnt lgkmcnt(2)\n"
      "v_mul_f32 v172, s50, v172\n"
      "v_mul_f32 v100, v214, v100\n"
      "v_mfma_f32_16x16x32_bf16 v[134:137], v[220:223], a[192:195], 0\n"
      "s_waitcnt lgkmcnt(1)\n"
      "v_mul_f32 v101, v214, v101\n"
      "v_mul_f32_e64 v182, v172, v182\n"
      "v_mfma_f32_16x16x32_bf16 v[134:137], v[188:191], a[196:199], v[134:137]\n"
      "s_waitcnt lgkmcnt(0)\n"
      "v_mul_f32 v183, v172, v183\n"
      "v_cvt_pk_bf16_f32 v100, v100, v101\n"
      "v_mfma_f32_16x16x32_bf16 v[134:137], v[200:203], a[200:203], v[134:137]\n"
      "ds_read_b128 v[200:203], v2 offset:9216\n"
      "v_mul_f32 v102, v214, v102\n"
      "v_mfma_f32_16x16x32_bf16 v[134:137], v[196:199], a[204:207], v[134:137]\n"
      "ds_read_b128 v[196:199], v2 offset:12288\n"
      "v_mul_f32 v103, v214, v103\n"
      "v_mfma_f32_16x16x32_bf16 v[134:137], v[216:219], a[208:211], v[134:137]\n"
      "ds_read_b128 v[188:191], v2 offset:15360\n"
      "v_mul_f32 v96, v214, v96\n"
      "v_mfma_f32_16x16x32_bf16 v[134:137], v[208:211], a[212:215], v[134:137]\n"
      "v_mul_f32 v97, v214, v97\n"
      "v_cvt_pk_bf16_f32 v182, v182, v183\n"
      "v_mfma_f32_16x16x32_bf16 v[220:223], a[216:219], v[126:129], 0\n"
      "v_mul_f32 v184, v172, v184\n"
      "v_mul_f32 v185, v172, v185\n"
      "v_mfma_f32_16x16x32_bf16 v[220:223], a[220:223], v[204:207], v[220:223]\n"
      "v_mul_f32 v168, v172, v168\n"
      "v_mul_f32 v169, v172, v169\n"
      "v_mfma_f32_16x16x32_bf16 v[220:223], a[224:227], v[152:155], v[220:223]\n"
      "s_waitcnt lgkmcnt(2)\n"
      "v_cvt_pk_bf16_f32 v101, v102, v103\n"
      "v_mul_f32 v130, v214, v130\n"
      "v_mfma_f32_16x16x32_bf16 v[208:211], a[240:243], v[126:129], 0\n"
      "s_waitcnt lgkmcnt(1)\n"
      "v_mul_f32 v131, v214, v131\n"
      "v_mul_f32 v132, v214, v132\n"
      "v_mfma_f32_16x16x32_bf16 v[208:211], a[244:247], v[204:207], v[208:211]\n"
      "s_waitcnt lgkmcnt(0)\n"
      "v_mul_f32 v133, v214, v133\n"
      "v_cvt_pk_bf16_f32 v96, v96, v97\n"
      "v_mfma_f32_16x16x32_bf16 v[208:211], a[248:251], v[152:155], v[208:211]\n"
      "v_mul_f32 v98, v214, v98\n"
      "v_mul_f32_e64 v99, v214, v99\n"
      "v_mfma_f32_16x16x32_bf16 v[214:217], v[232:235], v[126:129], 0\n"
      "v_cvt_pk_bf16_f32 v183, v184, v185\n"
      "v_mul_f32 v148, v172, v148\n"
      "v_mfma_f32_16x16x32_bf16 v[214:217], v[236:239], v[204:207], v[214:217]\n"
      "v_mul_f32 v149, v172, v149\n"
      "v_mul_f32 v150, v172, v150\n"
      "v_mfma_f32_16x16x32_bf16 v[214:217], v[240:243], v[152:155], v[214:217]\n"
      "ds_read_b128 v[126:129], v10 offset:1200\n"
      "v_mul_f32 v151, v172, v151\n"
      "v_mfma_f32_16x16x32_bf16 v[220:223], a[228:231], v[200:203], v[220:223]\n"
      "v_cvt_pk_bf16_f32 v168, v168, v169\n"
      "v_mul_f32 v170, v172, v170\n"
      "v_mfma_f32_16x16x32_bf16 v[220:223], a[232:235], v[196:199], v[220:223]\n"
      "v_mul_f32 v171, v172, v171\n"
      "v_cvt_pk_bf16_f32 v102, v130, v131\n"
      "v_mfma_f32_16x16x32_bf16 v[220:223], a[236:239], v[188:191], v[220:223]\n"
      "v_cvt_pk_bf16_f32 v103, v132, v133\n"
      "v_cvt_pk_bf16_f32 v97, v98, v99\n"
      "v_mfma_f32_16x16x32_bf16 v[208:211], a[252:255], v[200:203], v[208:211]\n"
      "ds_read_b64 v[98:99], v10 offset:1192\n"
      "v_cvt_pk_bf16_f32 v184, v148, v149\n"
      "v_mfma_f32_16x16x32_bf16 v[208:211], v[224:227], v[196:199], v[208:211]\n"
      "ds_read_b128 v[130:133], v2 offset:256\n"
      "v_cvt_pk_bf16_f32 v185, v150, v151\n"
      "v_mfma_f32_16x16x32_bf16 v[208:211], v[228:231], v[188:191], v[208:211]\n"
      "ds_read_b128 v[204:207], v2 offset:3328\n"
      "v_cvt_pk_bf16_f32 v169, v170, v171\n"
      "v_mfma_f32_16x16x32_bf16 v[214:217], v[244:247], v[200:203], v[214:217]\n"
      "ds_read_b128 v[200:203], v2 offset:6400\n"
      "v_cvt_pk_bf16_f32 v77, v138, v139\n"
      "v_mfma_f32_16x16x32_bf16 v[214:217], v[248:251], v[196:199], v[214:217]\n"
      "s_waitcnt lgkmcnt(4)\n"
      "v_cvt_pk_bf16_f32 v192, v192, v193\n"
      "v_cvt_pk_bf16_f32 v193, v194, v195\n"
      "v_mfma_f32_16x16x32_bf16 v[214:217], v[252:255], v[188:191], v[214:217]\n"
      "v_cvt_pk_bf16_f32 v194, v122, v123\n"
      "v_cvt_pk_bf16_f32 v195, v124, v125\n"
      "v_mfma_f32_16x16x32_bf16 v[196:199], v[156:159], v[144:147], v[40:43]\n"
      "s_waitcnt lgkmcnt(3)\n"
      "v_cvt_pk_bf16_f32 v80, v118, v119\n"
      "v_cvt_pk_bf16_f32 v81, v120, v121\n"
      "v_mfma_f32_16x16x32_bf16 v[120:123], v[178:181], v[144:147], v[44:47]\n"
      "s_waitcnt lgkmcnt(2)\n"
      "v_cvt_pk_bf16_f32 v164, v164, v165\n"
      "v_cvt_pk_bf16_f32 v165, v166, v167\n"
      "v_mfma_f32_16x16x32_bf16 v[188:191], v[100:103], v[144:147], v[48:51]\n"
      "s_waitcnt lgkmcnt(1)\n"
      "v_cvt_pk_bf16_f32 v166, v160, v161\n"
      "v_cvt_pk_bf16_f32 v167, v162, v163\n"
      "v_mfma_f32_16x16x32_bf16 v[152:155], v[126:129], v[144:147], v[24:27]\n"
      "s_waitcnt lgkmcnt(0)\n"
      "v_cvt_pk_bf16_f32 v84, v134, v135\n"
      "v_cvt_pk_bf16_f32 v85, v136, v137\n"
      "v_mfma_f32_16x16x16_bf16 v[196:199], v[116:117], v[186:187], v[196:199]\n"
      "v_fma_f32 v125, v220, v220, s52\n"
      "v_fmac_f32 v125, v221, v221\n"
      "v_fmac_f32 v125, v222, v222\n"
      "v_mfma_f32_16x16x16_bf16 v[120:123], v[212:213], v[186:187], v[120:123]\n"
      "v_fmac_f32 v125, v223, v223\n"
      "v_fmac_f32 v125, v208, v208\n"
      "v_fmac_f32 v125, v209, v209\n"
      "v_mfma_f32_16x16x16_bf16 v[188:191], v[96:97], v[186:187], v[188:191]\n"
      "v_exp_f32 v196, v196\n"
      "v_exp_f32 v197, v197\n"
      "v_mfma_f32_16x16x16_bf16 v[152:155], v[98:99], v[186:187], v[152:155]\n"
      "v_exp_f32 v198, v198\n"
      "v_exp_f32 v199, v199\n"
      "v_mfma_f32_16x16x32_bf16 v[148:151], a[216:219], v[130:133], 0\n"
      "v_exp_f32 v120, v120\n"
      "v_mfma_f32_16x16x32_bf16 v[148:151], a[220:223], v[204:207], v[148:151]\n"
      "v_exp_f32 v121, v121\n"
      "v_mfma_f32_16x16x32_bf16 v[148:151], a[224:227], v[200:203], v[148:151]\n"
      "v_exp_f32 v122, v122\n"
      "v_mfma_f32_16x16x32_bf16 v[170:173], a[240:243], v[130:133], 0\n"
      "v_exp_f32 v123, v123\n"
      "v_mfma_f32_16x16x32_bf16 v[170:173], a[244:247], v[204:207], v[170:173]\n"
      "v_cvt_pk_bf16_f32 v196, v196, v197\n"
      "v_cvt_pk_bf16_f32 v197, v198, v199\n"
      "v_mfma_f32_16x16x32_bf16 v[170:173], a[248:251], v[200:203], v[170:173]\n"
      "v_cvt_pk_bf16_f32 v198, v120, v121\n"
      "v_cvt_pk_bf16_f32 v199, v122, v123\n"
      "v_mfma_f32_16x16x32_bf16 v[120:123], v[232:235], v[130:133], 0\n"
      "v_exp_f32_e64 v188, v188\n"
      "v_mfma_f32_16x16x32_bf16 v[120:123], v[236:239], v[204:207], v[120:123]\n"
      "ds_read_u16 v78, v11 offset:1168\n"
      "v_exp_f32 v189, v189\n"
      "v_mfma_f32_16x16x32_bf16 v[120:123], v[240:243], v[200:203], v[120:123]\n"
      "v_exp_f32 v190, v190\n"
      "v_mfma_f32_16x16x32_bf16 v[136:139], v[156:159], v[108:111], v[52:55]\n"
      "ds_read_u16 v82, v11 offset:1200\n"
      "v_exp_f32 v191, v191\n"
      "v_mfma_f32_16x16x32_bf16 v[200:203], v[178:181], v[108:111], v[56:59]\n"
      "v_exp_f32 v152, v152\n"
      "v_mfma_f32_16x16x32_bf16 v[204:207], v[100:103], v[108:111], v[60:63]\n"
      "ds_read_u16 v86, v11 offset:1232\n"
      "v_cvt_pk_bf16_f32 v88, v188, v189\n"
      "v_mfma_f32_16x16x32_bf16 v[144:147], v[126:129], v[108:111], v[26:29]\n"
      "v_cvt_pk_bf16_f32 v89, v190, v191\n"
      "v_cvt_pk_bf16_f32 v90, v152, 0\n"
      "v_mfma_f32_16x16x16_bf16 v[136:139], v[116:117], v[104:105], v[136:139]\n"
      "v_fmac_f32 v125, v210, v210\n"
      "v_fmac_f32 v125, v211, v211\n"
      "v_fmac_f32_e64 v125, v214, v214\n"
      "v_mfma_f32_16x16x16_bf16 v[200:203], v[212:213], v[104:105], v[200:203]\n"
      "v_fmac_f32 v125, v215, v215\n"
      "v_fmac_f32 v125, v216, v216\n"
      "v_fmac_f32 v125, v217, v217\n"
      "v_mfma_f32_16x16x16_bf16 v[204:207], v[96:97], v[104:105], v[204:207]\n"
      "v_exp_f32 v136, v136\n"
      "v_exp_f32 v137, v137\n"
      "v_mfma_f32_16x16x16_bf16 v[144:147], v[98:99], v[104:105], v[144:147]\n"
      "v_exp_f32 v138, v138\n"
      "v_exp_f32 v139, v139\n"
      "v_mfma_f32_16x16x32_bf16 v[104:107], v[32:35], v[196:199], 0\n"
      "s_waitcnt lgkmcnt(0)\n"
      "v_exp_f32 v200, v200\n"
      "v_mfma_f32_16x16x32_bf16 v[104:107], v[32:35], v[88:91], v[104:107]\n"
      "v_exp_f32 v201, v201\n"
      "v_mfma_f32_16x16x32_bf16 v[188:191], v[112:115], v[196:199], 0\n"
      "v_exp_f32_e64 v202, v202\n"
      "v_mfma_f32_16x16x32_bf16 v[188:191], v[76:79], v[88:91], v[188:191]\n"
      "ds_read_b128 v[152:155], v2 offset:9472\n"
      "v_exp_f32_e64 v203, v203\n"
      "v_mfma_f32_16x16x32_bf16 v[132:135], v[192:195], v[196:199], 0\n"
      "ds_read_b128 v[160:163], v2 offset:12544\n"
      "v_cvt_pk_bf16_f32 v136, v136, v137\n"
      "v_mfma_f32_16x16x32_bf16 v[132:135], v[80:83], v[88:91], v[132:135]\n"
      "v_cvt_pk_bf16_f32 v137, v138, v139\n"
      "v_cvt_pk_bf16_f32 v138, v200, v201\n"
      "v_mfma_f32_16x16x32_bf16 v[106:109], v[164:167], v[196:199], 0\n"
      "v_cvt_pk_bf16_f32 v139, v202, v203\n"
      "v_exp_f32_e64 v204, v204\n"
      "v_mfma_f32_16x16x32_bf16 v[106:109], v[84:87], v[88:91], v[106:109]\n"
      "ds_read_b128 v[196:199], v2 offset:15616\n"
      "v_exp_f32 v205, v205\n"
      "v_mfma_f32_16x16x32_bf16 v[200:203], v[156:159], v[182:185], v[64:67]\n"
      "v_exp_f32 v206, v206\n"
      "v_exp_f32 v207, v207\n"
      "v_exp_f32 v144, v144\n"
      "v_cvt_pk_bf16_f32 v92, v204, v205\n"
      "v_cvt_pk_bf16_f32 v93, v206, v207\n"
      "v_cvt_pk_bf16_f32 v94, v144, 0\n"
      "v_rcp_f32 v104, v104\n"
      "s_waitcnt vmcnt(6)\n"
      "v_or3_b32 v174, v174, v175, v176\n"
      "v_mfma_f32_16x16x32_bf16 v[204:207], v[178:181], v[182:185], v[68:71]\n"
      "s_waitcnt lgkmcnt(1)\n"
      "v_mul_f32 v188, v104, v188\n"
      "v_mul_f32 v189, v104, v189\n"
      "v_cvt_pk_bf16_f32 v188, v188, v189\n"
      "v_mul_f32 v190, v104, v190\n"
      "v_mul_f32 v191, v104, v191\n"
      "v_cvt_pk_bf16_f32 v189, v190, v191\n"
      "v_mul_f32 v132, v104, v132\n"
      "v_mul_f32 v133, v104, v133\n"
      "v_mul_f32 v134, v104, v134\n"
      "v_mul_f32_e64 v135, v104, v135\n"
      "v_cvt_pk_bf16_f32 v190, v132, v133\n"
      "v_cvt_pk_bf16_f32 v191, v134, v135\n"
      "v_mfma_f32_16x16x32_bf16 v[144:147], v[100:103], v[182:185], v[72:75]\n"
      "ds_write_b128 v3, v[188:191] offset:18432\n"
      "v_mfma_f32_16x16x32_bf16 v[188:191], v[126:129], v[182:185], v[28:31]\n"
      "s_waitcnt lgkmcnt(1)\n"
      "v_mul_f32 v106, v104, v106\n"
      "v_mul_f32_e64 v107, v104, v107\n"
      "v_mfma_f32_16x16x16_bf16 v[200:203], v[116:117], v[168:169], v[200:203]\n"
      "v_cvt_pk_bf16_f32 v106, v106, v107\n"
      "v_mul_f32_e64 v108, v104, v108\n"
      "v_mul_f32 v109, v104, v109\n"
      "v_mfma_f32_16x16x16_bf16 v[204:207], v[212:213], v[168:169], v[204:207]\n"
      "v_cvt_pk_bf16_f32 v107, v108, v109\n"
      "s_waitcnt vmcnt(5)\n"
      "v_or3_b32 v174, v174, v177, v39\n"
      "v_and_b32 v174, 0x7fff7fff, v174\n"
      "v_mfma_f32_16x16x16_bf16 v[144:147], v[96:97], v[168:169], v[144:147]\n"
      "v_exp_f32 v200, v200\n"
      "v_exp_f32 v201, v201\n"
      "v_mfma_f32_16x16x16_bf16 v[188:191], v[98:99], v[168:169], v[188:191]\n"
      "v_exp_f32 v202, v202\n"
      "v_exp_f32 v203, v203\n"
      "v_mfma_f32_16x16x32_bf16 v[148:151], a[228:231], v[152:155], v[148:151]\n"
      "ds_write_b64 v4, v[106:107] offset:18432\n"
      "v_mfma_f32_16x16x32_bf16 v[148:151], a[232:235], v[160:163], v[148:151]\n"
      "v_exp_f32 v204, v204\n"
      "v_mfma_f32_16x16x32_bf16 v[148:151], a[236:239], v[196:199], v[148:151]\n"
      "v_exp_f32 v205, v205\n"
      "v_mfma_f32_16x16x32_bf16 v[170:173], a[252:255], v[152:155], v[170:173]\n"
      "ds_write_b32 v31, v174 offset:5632\n"
      "v_mfma_f32_16x16x32_bf16 v[170:173], v[224:227], v[160:163], v[170:173]\n"
      "ds_read_b128 v[104:107], v10 offset:1168\n"
      "v_exp_f32 v206, v206\n"
      "v_mfma_f32_16x16x32_bf16 v[170:173], v[228:231], v[196:199], v[170:173]\n"
      "v_exp_f32 v207, v207\n"
      "v_mfma_f32_16x16x32_bf16 v[120:123], v[244:247], v[152:155], v[120:123]\n"
      "v_cvt_pk_bf16_f32 v200, v200, v201\n"
      "v_cvt_pk_bf16_f32 v201, v202, v203\n"
      "v_mfma_f32_16x16x32_bf16 v[120:123], v[248:251], v[160:163], v[120:123]\n"
      "v_cvt_pk_bf16_f32 v202, v204, v205\n"
      "v_cvt_pk_bf16_f32 v203, v206, v207\n"
      "v_mfma_f32_16x16x32_bf16 v[120:123], v[252:255], v[196:199], v[120:123]\n"
      "ds_read_b64 v[190:191], v10 offset:1184\n"
      "v_exp_f32 v144, v144\n"
      "v_mfma_f32_16x16x32_bf16 v[174:177], v[32:35], v[136:139], 0\n"
      "v_exp_f32 v145, v145\n"
      "v_mfma_f32_16x16x32_bf16 v[174:177], v[32:35], v[92:95], v[174:177]\n"
      "v_exp_f32_e64 v146, v146\n"
      "v_mfma_f32_16x16x32_bf16 v[152:155], v[112:115], v[136:139], 0\n"
      "s_waitcnt lgkmcnt(1)\n"
      "v_exp_f32 v147, v147\n"
      "v_mfma_f32_16x16x32_bf16 v[152:155], v[76:79], v[92:95], v[152:155]\n"
      "ds_read_b128 v[130:133], v2 offset:512\n"
      "v_exp_f32_e64 v188, v188\n"
      "v_mfma_f32_16x16x32_bf16 v[182:185], v[192:195], v[136:139], 0\n"
      "ds_read_b128 v[108:111], v2 offset:3584\n"
      "v_cvt_pk_bf16_f32 v88, v144, v145\n"
      "v_mfma_f32_16x16x32_bf16 v[182:185], v[80:83], v[92:95], v[182:185]\n"
      "ds_read_b128 v[160:163], v2 offset:6656\n"
      "v_cvt_pk_bf16_f32 v89, v146, v147\n"
      "v_mfma_f32_16x16x32_bf16 v[144:147], v[164:167], v[136:139], 0\n"
      "s_waitcnt lgkmcnt(3)\n"
      "v_cvt_pk_bf16_f32 v90, v188, 0\n"
      "v_fma_f32 v207, v148, v148, s52\n"
      "v_mfma_f32_16x16x32_bf16 v[144:147], v[84:87], v[92:95], v[144:147]\n"
      "v_fmac_f32 v207, v149, v149\n"
      "v_fmac_f32 v207, v150, v150\n"
      "v_mfma_f32_16x16x32_bf16 v[134:137], v[104:107], v[156:159], v[30:33]\n"
      "v_fmac_f32 v207, v151, v151\n"
      "v_fmac_f32 v207, v170, v170\n"
      "v_mfma_f32_16x16x32_bf16 v[196:199], v[104:107], v[178:181], v[30:33]\n"
      "s_waitcnt lgkmcnt(2)\n"
      "v_fmac_f32 v207, v171, v171\n"
      "v_fmac_f32 v207, v172, v172\n"
      "v_mfma_f32_16x16x32_bf16 v[178:181], v[104:107], v[100:103], v[30:33]\n"
      "s_waitcnt lgkmcnt(1)\n"
      "v_fmac_f32 v207, v173, v173\n"
      "v_fmac_f32_e64 v207, v120, v120\n"
      "v_mfma_f32_16x16x32_bf16 v[156:159], v[104:107], v[126:129], v[30:33]\n"
      "s_waitcnt lgkmcnt(0)\n"
      "v_fmac_f32 v207, v121, v121\n"
      "v_fmac_f32 v207, v122, v122\n"
      "v_mfma_f32_16x16x16_bf16 v[134:137], v[190:191], v[116:117], v[134:137]\n"
      "v_fmac_f32 v207, v123, v123\n"
      "v_rcp_f32 v174, v174\n"
      "v_mfma_f32_16x16x16_bf16 v[196:199], v[190:191], v[212:213], v[196:199]\n"
      "v_permlane16_swap_b32 v125, v207\n"
      "v_add_f32 v125, v125, v207\n"
      "v_mfma_f32_16x16x16_bf16 v[178:181], v[190:191], v[96:97], v[178:181]\n"
      "v_mul_f32 v152, v174, v152\n"
      "v_mul_f32 v153, v174, v153\n"
      "v_cvt_pk_bf16_f32 v152, v152, v153\n"
      "v_mfma_f32_16x16x16_bf16 v[156:159], v[190:191], v[98:99], v[156:159]\n"
      "v_mul_f32 v154, v174, v154\n"
      "v_cndmask_b32 v134, v134, v196, s[58:59]\n"
      "v_mul_f32_e64 v155, v174, v155\n"
      "v_mfma_f32_16x16x32_bf16 v[136:139], a[216:219], v[130:133], 0\n"
      "v_cndmask_b32 v134, v134, v178, s[60:61]\n"
      "v_cvt_pk_bf16_f32 v153, v154, v155\n"
      "v_mfma_f32_16x16x32_bf16 v[136:139], a[220:223], v[108:111], v[136:139]\n"
      "v_mul_f32 v182, v174, v182\n"
      "v_mul_f32 v183, v174, v183\n"
      "v_mfma_f32_16x16x32_bf16 v[136:139], a[224:227], v[160:163], v[136:139]\n"
      "v_mul_f32 v184, v174, v184\n"
      "v_mul_f32 v185, v174, v185\n"
      "v_mfma_f32_16x16x32_bf16 v[196:199], a[240:243], v[130:133], 0\n"
      "v_cndmask_b32 v134, v134, v156, s[62:63]\n"
      "v_cvt_pk_bf16_f32 v154, v182, v183\n"
      "v_mfma_f32_16x16x32_bf16 v[196:199], a[244:247], v[108:111], v[196:199]\n"
      "ds_read_b128 v[188:191], v2 offset:9728\n"
      "v_cvt_pk_bf16_f32 v155, v184, v185\n"
      "v_mfma_f32_16x16x32_bf16 v[196:199], a[248:251], v[160:163], v[196:199]\n"
      "ds_read_b128 v[126:129], v2 offset:12800\n"
      "v_mul_f32 v144, v174, v144\n"
      "v_mfma_f32_16x16x32_bf16 v[178:181], v[232:235], v[130:133], 0\n"
      "ds_read_b128 v[98:101], v2 offset:15872\n"
      "v_mul_f32 v145, v174, v145\n"
      "v_mfma_f32_16x16x32_bf16 v[178:181], v[236:239], v[108:111], v[178:181]\n"
      "ds_write_b128 v3, v[152:155] offset:18688\n"
      "v_mfma_f32_16x16x32_bf16 v[178:181], v[240:243], v[160:163], v[178:181]\n"
      "v_exp_f32 v134, v134\n"
      "v_mfma_f32_16x16x32_bf16 v[154:157], v[32:35], v[200:203], 0\n"
      "s_waitcnt lgkmcnt(3)\n"
      "v_cvt_pk_bf16_f32 v144, v144, v145\n"
      "v_mul_f32 v146, v174, v146\n"
      "v_mfma_f32_16x16x32_bf16 v[154:157], v[32:35], v[88:91], v[154:157]\n"
      "s_waitcnt lgkmcnt(2)\n"
      "v_mul_f32_e64 v147, v174, v147\n"
      "v_cvt_pk_bf16_f32 v145, v146, v147\n"
      "v_mfma_f32_16x16x32_bf16 v[116:119], v[112:115], v[200:203], 0\n"
      "ds_write_b64 v4, v[144:145] offset:18688\n"
      "v_mfma_f32_16x16x32_bf16 v[116:119], v[76:79], v[88:91], v[116:119]\n"
      "s_waitcnt lgkmcnt(2)\n"
      "v_cvt_pk_bf16_f32 v134, v134, v134\n"
      "v_rcp_f32 v154, v154\n"
      "v_mfma_f32_16x16x32_bf16 v[144:147], v[192:195], v[200:203], 0\n"
      "ds_write_b16 v13, v134\n"
      "v_mfma_f32_16x16x32_bf16 v[144:147], v[80:83], v[88:91], v[144:147]\n"
      "ds_read_b128 v[104:107], v14\n"
      "v_readlane_b32 s45, v37, s43\n"
      "v_mfma_f32_16x16x32_bf16 v[182:185], v[164:167], v[200:203], 0\n"
      "ds_read_b128 v[156:159], v14 offset:64\n"
      "v_mul_f32 v116, v154, v116\n"
      "v_mfma_f32_16x16x32_bf16 v[182:185], v[84:87], v[88:91], v[182:185]\n"
      "v_mul_f32 v117, v154, v117\n"
      "v_cvt_pk_bf16_f32 v116, v116, v117\n"
      "v_mfma_f32_16x16x32_bf16 v[136:139], a[228:231], v[188:191], v[136:139]\n"
      "v_mul_f32 v118, v154, v118\n"
      "v_mul_f32 v119, v154, v119\n"
      "v_mfma_f32_16x16x32_bf16 v[136:139], a[232:235], v[126:129], v[136:139]\n"
      "v_cvt_pk_bf16_f32 v117, v118, v119\n"
      "v_mul_f32 v144, v154, v144\n"
      "v_mfma_f32_16x16x32_bf16 v[136:139], a[236:239], v[98:101], v[136:139]\n"
      "v_mul_f32 v145, v154, v145\n"
      "v_mul_f32 v146, v154, v146\n"
      "v_mfma_f32_16x16x32_bf16 v[196:199], a[252:255], v[188:191], v[196:199]\n"
      "v_mul_f32 v147, v154, v147\n"
      "v_cvt_pk_bf16_f32 v118, v144, v145\n"
      "v_mfma_f32_16x16x32_bf16 v[196:199], v[224:227], v[126:129], v[196:199]\n"
      "s_waitcnt lgkmcnt(1)\n"
      "v_cvt_pk_bf16_f32 v119, v146, v147\n"
      "v_fma_f32 v174, v136, v136, s52\n"
      "v_mfma_f32_16x16x32_bf16 v[196:199], v[228:231], v[98:101], v[196:199]\n"
      "ds_write_b128 v3, v[116:119] offset:18944\n"
      "v_mfma_f32_16x16x32_bf16 v[178:181], v[244:247], v[188:191], v[178:181]\n"
      "s_waitcnt lgkmcnt(1)\n"
      "v_fmac_f32 v174, v137, v137\n"
      "v_fmac_f32 v174, v138, v138\n"
      "v_mfma_f32_16x16x32_bf16 v[178:181], v[248:251], v[126:129], v[178:181]\n"
      "v_fmac_f32 v174, v139, v139\n"
      "v_mul_f32 v182, v154, v182\n"
      "v_mfma_f32_16x16x32_bf16 v[178:181], v[252:255], v[98:101], v[178:181]\n"
      "v_fmac_f32 v174, v196, v196\n"
      "v_fmac_f32 v174, v197, v197\n"
      "v_mfma_f32_16x16x32_bf16 v[100:103], v[104:107], v[32:35], 0\n"
      "v_fmac_f32 v174, v198, v198\n"
      "v_fmac_f32 v174, v199, v199\n"
      "v_mfma_f32_16x16x32_bf16 v[100:103], v[156:159], v[32:35], v[100:103]\n"
      "v_mul_f32 v183, v154, v183\n"
      "v_cvt_pk_bf16_f32 v182, v182, v183\n"
      "v_fmac_f32 v174, v178, v178\n"
      "v_fmac_f32 v174, v179, v179\n"
      "v_fmac_f32 v174, v180, v180\n"
      "v_fmac_f32 v174, v181, v181\n"
      "v_mov_b32 v207, v174\n"
      "v_mul_f32 v184, v154, v184\n"
      "v_mul_f32 v185, v154, v185\n"
      "v_permlane16_swap_b32 v174, v207\n"
      "v_add_f32 v174, v174, v207\n"
      "v_cvt_pk_bf16_f32 v183, v184, v185\n"
      "ds_write_b64 v4, v[182:183] offset:18944\n"
      "v_permlane32_swap_b32 v125, v174\n"
      "v_add_f32 v125, v125, v174\n"
      "v_readlane_b32 s46, v37, s42\n"
      "v_or3_b32 v140, v140, v141, v142\n"
      "ds_write_b32 v31, v125 offset:1024\n"
      "v_or_b32_e64 v140, v140, v143\n"
      "v_cmp_ne_u32 s[56:57], 0, v140\n"
      "v_readlane_b32 s47, v38, s43\n"
      "v_add_u32 v10, 2336, v10\n"
      "v_add_u32 v11, 2336, v11\n"
      "s_or_b32 s95, s45, s46\n"
      "s_or_b32 s95, s95, s48\n"
      "s_cmp_ge_u32 s95, 0x40000000\n"
      "s_cselect_b32 s94, -1, -2\n"
      "s_cmp_gt_u32 s42, s44\n"
      "s_cselect_b32 s95, s94, s95\n"
      "s_cmp_ge_u32 s95, 0x40000000\n"
      "s_cselect_b64 vcc, -1, s[56:57]\n"
      "s_and_b32 s47, s47, -4\n"
      "s_add_u32 s42, s42, 1\n"
      "s_add_u32 s43, s43, 1\n"
      "s_waitcnt lgkmcnt(0)\n"
      "s_barrier\n"
      "s_cbranch_vccnz L_disp0_%=\n"
      "L_loop0_%=:\n"
      "v_mfma_f32_16x16x32_bf16 v[140:143], v[104:107], v[112:115], 0\n"
      "ds_read_b128 v[204:207], v0\n"
      "ds_read_b128 v[186:189], v1\n"
      "v_mfma_f32_16x16x32_bf16 v[140:143], v[156:159], v[76:79], v[140:143]\n"
      "ds_read_b128 v[174:177], v0 offset:6144\n"
      "s_add_u32 m0, s64, 18432\n"
      "v_mfma_f32_16x16x32_bf16 v[112:115], v[104:107], v[192:195], 0\n"
      "buffer_load_dwordx4 v16, s[16:19], s45 offen  lds\n"
      "v_mfma_f32_16x16x32_bf16 v[112:115], v[156:159], v[80:83], v[112:115]\n"
      "s_add_u32 m0, s64, 22528\n"
      "v_mfma_f32_16x16x32_bf16 v[190:193], v[104:107], v[164:167], 0\n"
      "ds_read_b128 v[104:107], v9 offset:1024\n"
      "v_mfma_f32_16x16x32_bf16 v[190:193], v[156:159], v[84:87], v[190:193]\n"
      "v_mov_b32_dpp v100, v100 quad_perm:[0,0,0,0] row_mask:0xf bank_mask:0xf\n"
      "v_rcp_f32 v100, v100\n"
      "s_waitcnt lgkmcnt(3)\n"
      "v_mfma_f32_16x16x32_bf16 v[144:147], a[0:3], v[204:207], 0\n"
      "buffer_load_dwordx4 v17, s[16:19], s45 offen  lds\n"
      "v_mfma_f32_16x16x32_bf16 v[128:131], a[24:27], v[204:207], 0\n"
      "s_add_u32 m0, s64, 26624\n"
      "v_cndmask_b32 v140, v140, v112, s[58:59]\n"
      "v_cndmask_b32 v140, v140, v190, s[60:61]\n"
      "v_mfma_f32_16x16x32_bf16 v[96:99], a[48:51], v[204:207], 0\n"
      "s_waitcnt lgkmcnt(1)\n"
      "v_mul_f32 v140, v100, v140\n"
      "v_cvt_pk_bf16_f32 v140, v140, v140\n"
      "v_mfma_f32_16x16x32_bf16 v[152:155], a[72:75], v[204:207], 0\n"
      "ds_write_b16 v12, v140 offset:16\n"
      "v_mfma_f32_16x16x32_bf16 v[116:119], a[96:99], v[204:207], 0\n"
      "buffer_load_dwordx4 v18, s[16:19], s45 offen  lds\n"
      "v_mfma_f32_16x16x32_bf16 v[124:127], a[120:123], v[204:207], 0\n"
      "ds_read_b128 v[182:185], v1 offset:6144\n"
      "s_waitcnt lgkmcnt(2)\n"
      "v_add_f32 v104, v104, v105\n"
      "v_mfma_f32_16x16x32_bf16 v[144:147], a[4:7], v[186:189], v[144:147]\n"
      "ds_read_b128 v[108:111], v0 offset:12288\n"
      "v_add_f32 v106, v106, v107\n"
      "v_mfma_f32_16x16x32_bf16 v[144:147], a[8:11], v[174:177], v[144:147]\n"
      "ds_read_b128 v[132:135], v1 offset:12288\n"
      "v_add_f32 v104, v104, v106\n"
      "v_mfma_f32_16x16x32_bf16 v[128:131], a[28:31], v[186:189], v[128:131]\n"
      "s_add_u32 m0, s64, 30720\n"
      "v_rsq_f32_e64 v104, v104\n"
      "v_mfma_f32_16x16x32_bf16 v[128:131], a[32:35], v[174:177], v[128:131]\n"
      "buffer_load_dwordx4 v19, s[16:19], s45 offen  lds\n"
      "v_mfma_f32_16x16x32_bf16 v[96:99], a[52:55], v[186:189], v[96:99]\n"
      "s_add_u32 m0, s65, 34816\n"
      "v_mul_f32 v104, 0x415db3d7, v104\n"
      "v_mov_b32 v106, v104\n"
      "v_mfma_f32_16x16x32_bf16 v[96:99], a[56:59], v[174:177], v[96:99]\n"
      "s_waitcnt lgkmcnt(2)\n"
      "v_permlane32_swap_b32 v104, v106\n"
      "v_mfma_f32_16x16x32_bf16 v[152:155], a[76:79], v[186:189], v[152:155]\n"
      "s_waitcnt lgkmcnt(1)\n"
      "v_mov_b32 v105, v104\n"
      "v_mul_f32 v136, v106, v136\n"
      "v_mfma_f32_16x16x32_bf16 v[152:155], a[80:83], v[174:177], v[152:155]\n"
      "buffer_load_dwordx4 v15, s[16:19], s45 offen  lds\n"
      "v_mfma_f32_16x16x32_bf16 v[116:119], a[100:103], v[186:189], v[116:119]\n"
      "s_waitcnt lgkmcnt(0)\n"
      "v_permlane16_swap_b32 v104, v105\n"
      "v_mfma_f32_16x16x32_bf16 v[116:119], a[104:107], v[174:177], v[116:119]\n"
      "v_mul_f32 v220, v104, v220\n"
      "v_mul_f32 v221, v104, v221\n"
      "v_mfma_f32_16x16x32_bf16 v[124:127], a[124:127], v[186:189], v[124:127]\n"
      "v_cvt_pk_bf16_f32 v220, v220, v221\n"
      "v_mul_f32_e64 v222, v104, v222\n"
      "v_mfma_f32_16x16x32_bf16 v[124:127], a[128:131], v[174:177], v[124:127]\n"
      "buffer_load_dwordx4 v[200:203], v36, s[24:27], s47 offen\n"
      "v_mfma_f32_16x16x32_bf16 v[144:147], a[12:15], v[182:185], v[144:147]\n"
      "v_mul_f32 v223, v104, v223\n"
      "v_mul_f32 v214, v104, v214\n"
      "v_mfma_f32_16x16x32_bf16 v[144:147], a[16:19], v[108:111], v[144:147]\n"
      "v_mul_f32_e64 v215, v104, v215\n"
      "v_cvt_pk_bf16_f32 v221, v222, v223\n"
      "v_mfma_f32_16x16x32_bf16 v[144:147], a[20:23], v[132:135], v[144:147]\n"
      "v_mul_f32 v208, v104, v208\n"
      "v_mul_f32 v209, v104, v209\n"
      "v_mfma_f32_16x16x32_bf16 v[128:131], a[36:39], v[182:185], v[128:131]\n"
      "buffer_load_dword v39, v29, s[24:27], s47 offen\n"
      "v_mfma_f32_16x16x32_bf16 v[128:131], a[40:43], v[108:111], v[128:131]\n"
      "v_mul_f32 v210, v104, v210\n"
      "v_mul_f32 v211, v104, v211\n"
      "v_mfma_f32_16x16x32_bf16 v[128:131], a[44:47], v[132:135], v[128:131]\n"
      "v_cvt_pk_bf16_f32 v214, v214, v215\n"
      "v_mul_f32 v216, v104, v216\n"
      "v_mfma_f32_16x16x32_bf16 v[96:99], a[60:63], v[182:185], v[96:99]\n"
      "v_mul_f32 v217, v104, v217\n"
      "v_cvt_pk_bf16_f32 v222, v208, v209\n"
      "v_mfma_f32_16x16x32_bf16 v[96:99], a[64:67], v[108:111], v[96:99]\n"
      "v_cvt_pk_bf16_f32 v223, v210, v211\n"
      "v_cvt_pk_bf16_f32 v215, v216, v217\n"
      "v_mfma_f32_16x16x32_bf16 v[96:99], a[68:71], v[132:135], v[96:99]\n"
      "ds_write_b128 v5, v[220:223]\n"
      "v_mfma_f32_16x16x32_bf16 v[152:155], a[84:87], v[182:185], v[152:155]\n"
      "v_mul_f32 v148, v105, v148\n"
      "v_mul_f32 v149, v105, v149\n"
      "v_mfma_f32_16x16x32_bf16 v[152:155], a[88:91], v[108:111], v[152:155]\n"
      "v_cvt_pk_bf16_f32 v148, v148, v149\n"
      "v_mul_f32_e64 v150, v105, v150\n"
      "v_mfma_f32_16x16x32_bf16 v[152:155], a[92:95], v[132:135], v[152:155]\n"
      "ds_write_b64 v6, v[214:215]\n"
      "v_mfma_f32_16x16x32_bf16 v[116:119], a[108:111], v[182:185], v[116:119]\n"
      "v_mul_f32 v151, v105, v151\n"
      "v_mul_f32 v120, v105, v120\n"
      "v_mfma_f32_16x16x32_bf16 v[116:119], a[112:115], v[108:111], v[116:119]\n"
      "v_mul_f32_e64 v121, v105, v121\n"
      "v_cvt_pk_bf16_f32 v149, v150, v151\n"
      "v_mfma_f32_16x16x32_bf16 v[116:119], a[116:119], v[132:135], v[116:119]\n"
      "v_mul_f32 v170, v105, v170\n"
      "v_mul_f32 v171, v105, v171\n"
      "v_mfma_f32_16x16x32_bf16 v[124:127], a[132:135], v[182:185], v[124:127]\n"
      "v_mul_f32 v172, v105, v172\n"
      "v_mul_f32 v173, v105, v173\n"
      "v_mfma_f32_16x16x32_bf16 v[124:127], a[136:139], v[108:111], v[124:127]\n"
      "v_cvt_pk_bf16_f32 v120, v120, v121\n"
      "v_mul_f32_e64 v122, v105, v122\n"
      "v_mfma_f32_16x16x32_bf16 v[124:127], a[140:143], v[132:135], v[124:127]\n"
      "v_mul_f32_e64 v123, v105, v123\n"
      "v_cvt_pk_bf16_f32 v150, v170, v171\n"
      "v_mfma_f32_16x16x32_bf16 v[112:115], v[204:207], a[144:147], 0\n"
      "v_cvt_pk_bf16_f32 v151, v172, v173\n"
      "v_cvt_pk_bf16_f32 v121, v122, v123\n"
      "v_mfma_f32_16x16x32_bf16 v[112:115], v[186:189], a[148:151], v[112:115]\n"
      "ds_write_b128 v5, v[148:151] offset:6144\n"
      "v_mfma_f32_16x16x32_bf16 v[112:115], v[174:177], a[152:155], v[112:115]\n"
      "v_mul_f32 v137, v106, v137\n"
      "v_cvt_pk_bf16_f32 v136, v136, v137\n"
      "v_mfma_f32_16x16x32_bf16 v[112:115], v[182:185], a[156:159], v[112:115]\n"
      "v_mul_f32 v138, v106, v138\n"
      "v_mul_f32_e64 v139, v106, v139\n"
      "v_mfma_f32_16x16x32_bf16 v[112:115], v[108:111], a[160:163], v[112:115]\n"
      "ds_write_b64 v6, v[120:121] offset:6144\n"
      "v_mfma_f32_16x16x32_bf16 v[112:115], v[132:135], a[164:167], v[112:115]\n"
      "ds_read_b128 v[156:159], v0 offset:2048\n"
      "v_mul_f32 v178, v106, v178\n"
      "v_mfma_f32_16x16x32_bf16 v[192:195], v[204:207], a[168:171], 0\n"
      "ds_read_b128 v[220:223], v1 offset:2048\n"
      "v_mul_f32 v179, v106, v179\n"
      "v_mfma_f32_16x16x32_bf16 v[192:195], v[186:189], a[172:175], v[192:195]\n"
      "ds_read_b128 v[148:151], v0 offset:8192\n"
      "v_cvt_pk_bf16_f32 v137, v138, v139\n"
      "v_mfma_f32_16x16x32_bf16 v[192:195], v[174:177], a[176:179], v[192:195]\n"
      "ds_read_b128 v[160:163], v1 offset:8192\n"
      "v_mul_f32 v196, v106, v196\n"
      "v_mfma_f32_16x16x32_bf16 v[192:195], v[182:185], a[180:183], v[192:195]\n"
      "ds_read_b128 v[168:171], v0 offset:14336\n"
      "v_mul_f32 v197, v106, v197\n"
      "v_mfma_f32_16x16x32_bf16 v[192:195], v[108:111], a[184:187], v[192:195]\n"
      "ds_read_b128 v[140:143], v1 offset:14336\n"
      "v_mul_f32 v198, v106, v198\n"
      "v_mfma_f32_16x16x32_bf16 v[192:195], v[132:135], a[188:191], v[192:195]\n"
      "v_mul_f32 v199, v106, v199\n"
      "v_cvt_pk_bf16_f32 v178, v178, v179\n"
      "v_mfma_f32_16x16x32_bf16 v[164:167], v[204:207], a[192:195], 0\n"
      "s_waitcnt lgkmcnt(5)\n"
      "v_mul_f32 v180, v106, v180\n"
      "v_mul_f32_e64 v181, v106, v181\n"
      "v_mfma_f32_16x16x32_bf16 v[164:167], v[186:189], a[196:199], v[164:167]\n"
      "s_waitcnt lgkmcnt(4)\n"
      "v_cvt_pk_bf16_f32 v138, v196, v197\n"
      "v_cvt_pk_bf16_f32 v139, v198, v199\n"
      "v_mfma_f32_16x16x32_bf16 v[164:167], v[174:177], a[200:203], v[164:167]\n"
      "ds_write_b128 v5, v[136:139] offset:12288\n"
      "v_mfma_f32_16x16x32_bf16 v[164:167], v[182:185], a[204:207], v[164:167]\n"
      "s_waitcnt lgkmcnt(2)\n"
      "v_cvt_pk_bf16_f32 v179, v180, v181\n"
      "v_fma_f32 v181, v144, v144, s51\n"
      "v_mfma_f32_16x16x32_bf16 v[164:167], v[108:111], a[208:211], v[164:167]\n"
      "s_waitcnt lgkmcnt(1)\n"
      "v_fma_f32 v182, v152, v152, s51\n"
      "v_fmac_f32 v181, v145, v145\n"
      "v_mfma_f32_16x16x32_bf16 v[164:167], v[132:135], a[212:215], v[164:167]\n"
      "ds_write_b64 v6, v[178:179] offset:12288\n"
      "v_mfma_f32_16x16x32_bf16 v[188:191], a[0:3], v[156:159], 0\n"
      "v_fmac_f32 v182, v153, v153\n"
      "v_fmac_f32 v181, v146, v146\n"
      "v_mfma_f32_16x16x32_bf16 v[188:191], a[4:7], v[220:223], v[188:191]\n"
      "v_fmac_f32 v182, v154, v154\n"
      "v_fmac_f32 v181, v147, v147\n"
      "v_mfma_f32_16x16x32_bf16 v[188:191], a[8:11], v[148:151], v[188:191]\n"
      "v_fmac_f32 v182, v155, v155\n"
      "v_fmac_f32 v181, v128, v128\n"
      "v_mfma_f32_16x16x32_bf16 v[188:191], a[12:15], v[160:163], v[188:191]\n"
      "v_fmac_f32 v182, v116, v116\n"
      "v_fmac_f32 v181, v129, v129\n"
      "v_mfma_f32_16x16x32_bf16 v[188:191], a[16:19], v[168:171], v[188:191]\n"
      "v_fmac_f32 v182, v117, v117\n"
      "v_fmac_f32 v181, v130, v130\n"
      "v_mfma_f32_16x16x32_bf16 v[188:191], a[20:23], v[140:143], v[188:191]\n"
      "v_fmac_f32 v182, v118, v118\n"
      "v_fmac_f32 v181, v131, v131\n"
      "v_mfma_f32_16x16x32_bf16 v[196:199], a[24:27], v[156:159], 0\n"
      "v_fmac_f32 v182, v119, v119\n"
      "v_fmac_f32 v181, v96, v96\n"
      "v_mfma_f32_16x16x32_bf16 v[196:199], a[28:31], v[220:223], v[196:199]\n"
      "v_fmac_f32 v182, v124, v124\n"
      "v_fmac_f32 v181, v97, v97\n"
      "v_mfma_f32_16x16x32_bf16 v[196:199], a[32:35], v[148:151], v[196:199]\n"
      "v_fmac_f32 v182, v125, v125\n"
      "v_fmac_f32 v181, v98, v98\n"
      "v_mfma_f32_16x16x32_bf16 v[196:199], a[36:39], v[160:163], v[196:199]\n"
      "v_fmac_f32 v182, v126, v126\n"
      "v_fmac_f32 v181, v99, v99\n"
      "v_mfma_f32_16x16x32_bf16 v[196:199], a[40:43], v[168:171], v[196:199]\n"
      "v_fmac_f32_e64 v182, v127, v127\n"
      "v_fma_f32 v106, v188, v188, s51\n"
      "v_mfma_f32_16x16x32_bf16 v[196:199], a[44:47], v[140:143], v[196:199]\n"
      "v_permlane16_swap_b32 v181, v182\n"
      "v_mfma_f32_16x16x32_bf16 v[102:105], a[48:51], v[156:159], 0\n"
      "v_add_f32 v181, v181, v182\n"
      "v_mov_b32 v182, v181\n"
      "v_mfma_f32_16x16x32_bf16 v[102:105], a[52:55], v[220:223], v[102:105]\n"
      "v_fmac_f32 v106, v189, v189\n"
      "v_permlane32_swap_b32 v181, v182\n"
      "v_mfma_f32_16x16x32_bf16 v[102:105], a[56:59], v[148:151], v[102:105]\n"
      "v_add_f32 v181, v181, v182\n"
      "v_rsq_f32 v181, v181\n"
      "v_mfma_f32_16x16x32_bf16 v[102:105], a[60:63], v[160:163], v[102:105]\n"
      "v_mov_b32 v182, v181\n"
      "v_fmac_f32 v106, v190, v190\n"
      "v_mfma_f32_16x16x32_bf16 v[102:105], a[64:67], v[168:171], v[102:105]\n"
      "v_permlane16_swap_b32 v181, v182\n"
      "v_mfma_f32_16x16x32_bf16 v[102:105], a[68:71], v[140:143], v[102:105]\n"
      "v_mul_f32 v181, s50, v181\n"
      "v_mul_f32 v152, v182, v152\n"
      "v_mfma_f32_16x16x32_bf16 v[134:137], a[72:75], v[156:159], 0\n"
      "v_mul_f32 v153, v182, v153\n"
      "v_mul_f32 v144, v181, v144\n"
      "v_mfma_f32_16x16x32_bf16 v[134:137], a[76:79], v[220:223], v[134:137]\n"
      "v_mul_f32 v145, v181, v145\n"
      "v_cvt_pk_bf16_f32 v152, v152, v153\n"
      "v_mfma_f32_16x16x32_bf16 v[134:137], a[80:83], v[148:151], v[134:137]\n"
      "v_mul_f32 v154, v182, v154\n"
      "v_mul_f32 v155, v182, v155\n"
      "v_mfma_f32_16x16x32_bf16 v[134:137], a[84:87], v[160:163], v[134:137]\n"
      "v_mul_f32 v124, v182, v124\n"
      "v_mul_f32_e64 v125, v182, v125\n"
      "v_mfma_f32_16x16x32_bf16 v[134:137], a[88:91], v[168:171], v[134:137]\n"
      "v_cvt_pk_bf16_f32 v144, v144, v145\n"
      "v_mul_f32 v146, v181, v146\n"
      "v_mfma_f32_16x16x32_bf16 v[134:137], a[92:95], v[140:143], v[134:137]\n"
      "v_mul_f32 v147, v181, v147\n"
      "v_mul_f32_e64 v96, v181, v96\n"
      "v_mfma_f32_16x16x32_bf16 v[204:207], a[96:99], v[156:159], 0\n"
      "v_mul_f32 v97, v181, v97\n"
      "v_cvt_pk_bf16_f32 v153, v154, v155\n"
      "v_mfma_f32_16x16x32_bf16 v[204:207], a[100:103], v[220:223], v[204:207]\n"
      "v_mul_f32 v116, v182, v116\n"
      "v_mul_f32 v117, v182, v117\n"
      "v_mfma_f32_16x16x32_bf16 v[204:207], a[104:107], v[148:151], v[204:207]\n"
      "v_mul_f32 v118, v182, v118\n"
      "v_mul_f32_e64 v119, v182, v119\n"
      "v_mfma_f32_16x16x32_bf16 v[204:207], a[108:111], v[160:163], v[204:207]\n"
      "v_cvt_pk_bf16_f32 v124, v124, v125\n"
      "v_mul_f32 v126, v182, v126\n"
      "v_mfma_f32_16x16x32_bf16 v[204:207], a[112:115], v[168:171], v[204:207]\n"
      "v_mul_f32 v127, v182, v127\n"
      "v_cvt_pk_bf16_f32 v145, v146, v147\n"
      "v_mfma_f32_16x16x32_bf16 v[204:207], a[116:119], v[140:143], v[204:207]\n"
      "v_mul_f32 v128, v181, v128\n"
      "v_mul_f32 v129, v181, v129\n"
      "v_mfma_f32_16x16x32_bf16 v[212:215], a[120:123], v[156:159], 0\n"
      "v_mul_f32 v130, v181, v130\n"
      "v_mul_f32 v131, v181, v131\n"
      "v_mfma_f32_16x16x32_bf16 v[212:215], a[124:127], v[220:223], v[212:215]\n"
      "v_cvt_pk_bf16_f32 v96, v96, v97\n"
      "v_mul_f32 v98, v181, v98\n"
      "v_mfma_f32_16x16x32_bf16 v[212:215], a[128:131], v[148:151], v[212:215]\n"
      "v_mul_f32 v99, v181, v99\n"
      "v_cvt_pk_bf16_f32 v154, v116, v117\n"
      "v_mfma_f32_16x16x32_bf16 v[212:215], a[132:135], v[160:163], v[212:215]\n"
      "ds_read_b128 v[120:123], v7 offset:18432\n"
      "v_cvt_pk_bf16_f32 v155, v118, v119\n"
      "v_mfma_f32_16x16x32_bf16 v[212:215], a[136:139], v[168:171], v[212:215]\n"
      "ds_read_b128 v[180:183], v7 offset:22528\n"
      "v_cvt_pk_bf16_f32 v125, v126, v127\n"
      "v_mfma_f32_16x16x32_bf16 v[212:215], a[140:143], v[140:143], v[212:215]\n"
      "v_cvt_pk_bf16_f32 v146, v128, v129\n"
      "v_cvt_pk_bf16_f32 v147, v130, v131\n"
      "v_mfma_f32_16x16x32_bf16 v[172:175], v[156:159], a[144:147], 0\n"
      "ds_read_b128 v[216:219], v7 offset:26624\n"
      "v_cvt_pk_bf16_f32 v97, v98, v99\n"
      "v_mfma_f32_16x16x32_bf16 v[172:175], v[220:223], a[148:151], v[172:175]\n"
      "ds_read_b128 v[184:187], v7 offset:30720\n"
      "v_fma_f32 v107, v134, v134, s51\n"
      "v_mfma_f32_16x16x32_bf16 v[172:175], v[148:151], a[152:155], v[172:175]\n"
      "v_fmac_f32 v107, v135, v135\n"
      "v_fmac_f32 v107, v136, v136\n"
      "v_mfma_f32_16x16x32_bf16 v[172:175], v[160:163], a[156:159], v[172:175]\n"
      "ds_read_b128 v[176:179], v7 offset:34816\n"
      "v_fmac_f32 v106, v191, v191\n"
      "v_mfma_f32_16x16x32_bf16 v[172:175], v[168:171], a[160:163], v[172:175]\n"
      "v_fmac_f32 v107, v137, v137\n"
      "v_fmac_f32_e64 v106, v196, v196\n"
      "v_mfma_f32_16x16x32_bf16 v[172:175], v[140:143], a[164:167], v[172:175]\n"
      "ds_read_b128 v[208:211], v0 offset:4096\n"
      "v_fmac_f32 v107, v204, v204\n"
      "v_mfma_f32_16x16x32_bf16 v[98:101], v[156:159], a[168:171], 0\n"
      "ds_read_b128 v[108:111], v1 offset:4096\n"
      "v_fmac_f32 v106, v197, v197\n"
      "v_mfma_f32_16x16x32_bf16 v[98:101], v[220:223], a[172:175], v[98:101]\n"
      "s_waitcnt lgkmcnt(3)\n"
      "v_fmac_f32 v107, v205, v205\n"
      "v_fmac_f32_e64 v106, v198, v198\n"
      "v_mfma_f32_16x16x32_bf16 v[98:101], v[148:151], a[176:179], v[98:101]\n"
      "buffer_store_dwordx4 v[120:123], v20, s[20:23], s46 offen sc1\n"
      "v_mfma_f32_16x16x32_bf16 v[98:101], v[160:163], a[180:183], v[98:101]\n"
      "ds_read_b128 v[116:119], v0 offset:10240\n"
      "v_fmac_f32_e64 v107, v206, v206\n"
      "v_mfma_f32_16x16x32_bf16 v[98:101], v[168:171], a[184:187], v[98:101]\n"
      "ds_read_b128 v[130:133], v1 offset:10240\n"
      "v_fmac_f32_e64 v106, v199, v199\n"
      "v_mfma_f32_16x16x32_bf16 v[98:101], v[140:143], a[188:191], v[98:101]\n"
      "ds_read_b128 v[126:129], v0 offset:16384\n"
      "v_fmac_f32 v107, v207, v207\n"
      "v_mfma_f32_16x16x32_bf16 v[120:123], v[156:159], a[192:195], 0\n"
      "s_waitcnt lgkmcnt(3)\n"
      "v_fmac_f32 v106, v102, v102\n"
      "v_fmac_f32 v107, v212, v212\n"
      "v_mfma_f32_16x16x32_bf16 v[120:123], v[220:223], a[196:199], v[120:123]\n"
      "ds_read_b128 v[220:223], v1 offset:16384\n"
      "v_fmac_f32 v106, v103, v103\n"
      "v_mfma_f32_16x16x32_bf16 v[120:123], v[148:151], a[200:203], v[120:123]\n"
      "v_fmac_f32 v107, v213, v213\n"
      "v_fmac_f32 v106, v104, v104\n"
      "v_mfma_f32_16x16x32_bf16 v[120:123], v[160:163], a[204:207], v[120:123]\n"
      "s_waitcnt lgkmcnt(3)\n"
      "v_fmac_f32 v107, v214, v214\n"
      "v_fmac_f32 v106, v105, v105\n"
      "v_mfma_f32_16x16x32_bf16 v[120:123], v[168:171], a[208:211], v[120:123]\n"
      "buffer_store_dwordx4 v[180:183], v21, s[20:23], s46 offen sc1\n"
      "v_mfma_f32_16x16x32_bf16 v[120:123], v[140:143], a[212:215], v[120:123]\n"
      "s_waitcnt lgkmcnt(1)\n"
      "v_fmac_f32 v107, v215, v215\n"
      "v_cvt_pk_bf16_f32 v112, v112, v113\n"
      "v_mfma_f32_16x16x32_bf16 v[180:183], a[0:3], v[208:211], 0\n"
      "s_waitcnt lgkmcnt(0)\n"
      "v_permlane16_swap_b32 v106, v107\n"
      "v_mfma_f32_16x16x32_bf16 v[180:183], a[4:7], v[108:111], v[180:183]\n"
      "v_add_f32 v106, v106, v107\n"
      "v_mov_b32 v107, v106\n"
      "v_mfma_f32_16x16x32_bf16 v[180:183], a[8:11], v[116:119], v[180:183]\n"
      "v_cvt_pk_bf16_f32 v113, v114, v115\n"
      "v_permlane32_swap_b32 v106, v107\n"
      "v_mfma_f32_16x16x32_bf16 v[180:183], a[12:15], v[130:133], v[180:183]\n"
      "v_add_f32 v106, v106, v107\n"
      "v_rsq_f32 v106, v106\n"
      "v_mfma_f32_16x16x32_bf16 v[180:183], a[16:19], v[126:129], v[180:183]\n"
      "v_mov_b32 v107, v106\n"
      "v_cvt_pk_bf16_f32 v114, v172, v173\n"
      "v_mfma_f32_16x16x32_bf16 v[180:183], a[20:23], v[220:223], v[180:183]\n"
      "v_permlane16_swap_b32 v106, v107\n"
      "v_mfma_f32_16x16x32_bf16 v[168:171], a[24:27], v[208:211], 0\n"
      "buffer_store_dwordx4 v[216:219], v22, s[20:23], s46 offen sc1\n"
      "v_mfma_f32_16x16x32_bf16 v[168:171], a[28:31], v[108:111], v[168:171]\n"
      "v_mul_f32 v106, s50, v106\n"
      "v_mul_f32 v134, v107, v134\n"
      "v_mfma_f32_16x16x32_bf16 v[168:171], a[32:35], v[116:119], v[168:171]\n"
      "v_mul_f32 v135, v107, v135\n"
      "v_mul_f32 v188, v106, v188\n"
      "v_mfma_f32_16x16x32_bf16 v[168:171], a[36:39], v[130:133], v[168:171]\n"
      "v_mul_f32 v189, v106, v189\n"
      "v_cvt_pk_bf16_f32 v134, v134, v135\n"
      "v_mfma_f32_16x16x32_bf16 v[168:171], a[40:43], v[126:129], v[168:171]\n"
      "v_mul_f32 v136, v107, v136\n"
      "v_mul_f32 v137, v107, v137\n"
      "v_mfma_f32_16x16x32_bf16 v[168:171], a[44:47], v[220:223], v[168:171]\n"
      "v_mul_f32 v212, v107, v212\n"
      "v_mul_f32 v213, v107, v213\n"
      "v_mfma_f32_16x16x32_bf16 v[140:143], a[48:51], v[208:211], 0\n"
      "v_cvt_pk_bf16_f32 v188, v188, v189\n"
      "v_mul_f32_e64 v190, v106, v190\n"
      "v_mfma_f32_16x16x32_bf16 v[140:143], a[52:55], v[108:111], v[140:143]\n"
      "v_mul_f32 v191, v106, v191\n"
      "v_mul_f32 v102, v106, v102\n"
      "v_mfma_f32_16x16x32_bf16 v[140:143], a[56:59], v[116:119], v[140:143]\n"
      "buffer_store_dwordx4 v[184:187], v23, s[20:23], s46 offen sc1\n"
      "v_mfma_f32_16x16x32_bf16 v[140:143], a[60:63], v[130:133], v[140:143]\n"
      "v_mul_f32 v103, v106, v103\n"
      "v_cvt_pk_bf16_f32 v135, v136, v137\n"
      "v_mfma_f32_16x16x32_bf16 v[140:143], a[64:67], v[126:129], v[140:143]\n"
      "v_mul_f32 v204, v107, v204\n"
      "v_mul_f32 v205, v107, v205\n"
      "v_mfma_f32_16x16x32_bf16 v[140:143], a[68:71], v[220:223], v[140:143]\n"
      "v_mul_f32 v206, v107, v206\n"
      "v_mul_f32_e64 v207, v107, v207\n"
      "v_mfma_f32_16x16x32_bf16 v[156:159], a[72:75], v[208:211], 0\n"
      "v_cvt_pk_bf16_f32 v212, v212, v213\n"
      "v_mul_f32 v214, v107, v214\n"
      "v_mfma_f32_16x16x32_bf16 v[156:159], a[76:79], v[108:111], v[156:159]\n"
      "ds_read_b128 v[160:163], v9 offset:5632\n"
      "v_mul_f32 v215, v107, v215\n"
      "v_mfma_f32_16x16x32_bf16 v[156:159], a[80:83], v[116:119], v[156:159]\n"
      "v_cvt_pk_bf16_f32 v189, v190, v191\n"
      "v_mul_f32 v196, v106, v196\n"
      "v_mfma_f32_16x16x32_bf16 v[156:159], a[84:87], v[130:133], v[156:159]\n"
      "v_mul_f32 v197, v106, v197\n"
      "v_mul_f32 v198, v106, v198\n"
      "v_mfma_f32_16x16x32_bf16 v[156:159], a[88:91], v[126:129], v[156:159]\n"
      "buffer_store_dwordx4 v[176:179], v25, s[20:23], s46 offen sc1\n"
      "v_mfma_f32_16x16x32_bf16 v[156:159], a[92:95], v[220:223], v[156:159]\n"
      "v_mul_f32 v199, v106, v199\n"
      "v_cvt_pk_bf16_f32 v102, v102, v103\n"
      "v_mfma_f32_16x16x32_bf16 v[184:187], a[96:99], v[208:211], 0\n"
      "v_mul_f32 v104, v106, v104\n"
      "v_mul_f32 v105, v106, v105\n"
      "v_mfma_f32_16x16x32_bf16 v[184:187], a[100:103], v[108:111], v[184:187]\n"
      "v_cvt_pk_bf16_f32 v136, v204, v205\n"
      "v_cvt_pk_bf16_f32 v137, v206, v207\n"
      "v_mfma_f32_16x16x32_bf16 v[184:187], a[104:107], v[116:119], v[184:187]\n"
      "v_cvt_pk_bf16_f32 v213, v214, v215\n"
      "v_cvt_pk_bf16_f32 v190, v196, v197\n"
      "v_mfma_f32_16x16x32_bf16 v[184:187], a[108:111], v[130:133], v[184:187]\n"
      "v_cvt_pk_bf16_f32 v191, v198, v199\n"
      "v_cvt_pk_bf16_f32 v103, v104, v105\n"
      "v_mfma_f32_16x16x32_bf16 v[184:187], a[112:115], v[126:129], v[184:187]\n"
      "v_fma_f32 v138, v180, v180, s51\n"
      "v_fma_f32 v214, v156, v156, s51\n"
      "v_mfma_f32_16x16x32_bf16 v[184:187], a[116:119], v[220:223], v[184:187]\n"
      "v_fmac_f32 v138, v181, v181\n"
      "v_fmac_f32 v214, v157, v157\n"
      "v_mfma_f32_16x16x32_bf16 v[104:107], a[120:123], v[208:211], 0\n"
      "v_fmac_f32 v138, v182, v182\n"
      "v_fmac_f32 v214, v158, v158\n"
      "v_mfma_f32_16x16x32_bf16 v[104:107], a[124:127], v[108:111], v[104:107]\n"
      "v_fmac_f32 v138, v183, v183\n"
      "v_fmac_f32 v214, v159, v159\n"
      "v_mfma_f32_16x16x32_bf16 v[104:107], a[128:131], v[116:119], v[104:107]\n"
      "v_fmac_f32 v138, v168, v168\n"
      "v_fmac_f32 v214, v184, v184\n"
      "v_mfma_f32_16x16x32_bf16 v[104:107], a[132:135], v[130:133], v[104:107]\n"
      "v_fmac_f32 v138, v169, v169\n"
      "v_fmac_f32 v214, v185, v185\n"
      "v_mfma_f32_16x16x32_bf16 v[104:107], a[136:139], v[126:129], v[104:107]\n"
      "v_fmac_f32 v138, v170, v170\n"
      "v_fmac_f32 v214, v186, v186\n"
      "v_mfma_f32_16x16x32_bf16 v[104:107], a[140:143], v[220:223], v[104:107]\n"
      "v_fmac_f32 v138, v171, v171\n"
      "v_fmac_f32 v214, v187, v187\n"
      "v_mfma_f32_16x16x32_bf16 v[216:219], v[208:211], a[144:147], 0\n"
      "v_fmac_f32 v138, v140, v140\n"
      "v_fmac_f32 v138, v141, v141\n"
      "v_mfma_f32_16x16x32_bf16 v[216:219], v[108:111], a[148:151], v[216:219]\n"
      "v_fmac_f32 v138, v142, v142\n"
      "v_fmac_f32 v138, v143, v143\n"
      "v_mfma_f32_16x16x32_bf16 v[216:219], v[116:119], a[152:155], v[216:219]\n"
      "v_fmac_f32 v214, v104, v104\n"
      "v_fmac_f32 v214, v105, v105\n"
      "v_mfma_f32_16x16x32_bf16 v[216:219], v[130:133], a[156:159], v[216:219]\n"
      "v_fmac_f32 v214, v106, v106\n"
      "v_fmac_f32 v214, v107, v107\n"
      "v_mfma_f32_16x16x32_bf16 v[216:219], v[126:129], a[160:163], v[216:219]\n"
      "v_cvt_pk_bf16_f32 v115, v174, v175\n"
      "v_permlane16_swap_b32_e64 v138, v214\n"
      "v_mfma_f32_16x16x32_bf16 v[216:219], v[220:223], a[164:167], v[216:219]\n"
      "ds_read_b128 v[196:199], v2 offset:18432\n"
      "v_add_f32_e64 v138, v138, v214\n"
      "v_mfma_f32_16x16x32_bf16 v[176:179], v[208:211], a[168:171], 0\n"
      "ds_read_b128 v[204:207], v2 offset:21504\n"
      "v_mov_b32 v214, v138\n"
      "v_mfma_f32_16x16x32_bf16 v[176:179], v[108:111], a[172:175], v[176:179]\n"
      "ds_read_b128 v[148:151], v2 offset:24576\n"
      "v_permlane32_swap_b32 v138, v214\n"
      "v_mfma_f32_16x16x32_bf16 v[176:179], v[116:119], a[176:179], v[176:179]\n"
      "v_add_f32 v138, v138, v214\n"
      "v_rsq_f32 v138, v138\n"
      "v_mfma_f32_16x16x32_bf16 v[176:179], v[130:133], a[180:183], v[176:179]\n"
      "v_mov_b32_e64 v214, v138\n"
      "v_cvt_pk_bf16_f32 v76, v216, v217\n"
      "v_mfma_f32_16x16x32_bf16 v[176:179], v[126:129], a[184:187], v[176:179]\n"
      "v_permlane16_swap_b32 v138, v214\n"
      "v_mfma_f32_16x16x32_bf16 v[176:179], v[220:223], a[188:191], v[176:179]\n"
      "s_waitcnt lgkmcnt(2)\n"
      "v_mul_f32 v138, s50, v138\n"
      "v_mul_f32 v156, v214, v156\n"
      "v_mfma_f32_16x16x32_bf16 v[172:175], v[208:211], a[192:195], 0\n"
      "s_waitcnt lgkmcnt(1)\n"
      "v_mul_f32 v157, v214, v157\n"
      "v_mul_f32_e64 v180, v138, v180\n"
      "v_mfma_f32_16x16x32_bf16 v[172:175], v[108:111], a[196:199], v[172:175]\n"
      "s_waitcnt lgkmcnt(0)\n"
      "v_mul_f32 v181, v138, v181\n"
      "v_cvt_pk_bf16_f32 v156, v156, v157\n"
      "v_mfma_f32_16x16x32_bf16 v[172:175], v[116:119], a[200:203], v[172:175]\n"
      "ds_read_b128 v[108:111], v2 offset:27648\n"
      "v_mul_f32 v158, v214, v158\n"
      "v_mfma_f32_16x16x32_bf16 v[172:175], v[130:133], a[204:207], v[172:175]\n"
      "ds_read_b128 v[116:119], v2 offset:30720\n"
      "v_mul_f32 v159, v214, v159\n"
      "v_mfma_f32_16x16x32_bf16 v[172:175], v[126:129], a[208:211], v[172:175]\n"
      "ds_read_b128 v[130:133], v2 offset:33792\n"
      "v_mul_f32 v104, v214, v104\n"
      "v_mfma_f32_16x16x32_bf16 v[172:175], v[220:223], a[212:215], v[172:175]\n"
      "v_mul_f32 v105, v214, v105\n"
      "v_cvt_pk_bf16_f32 v180, v180, v181\n"
      "v_mfma_f32_16x16x32_bf16 v[220:223], a[216:219], v[196:199], 0\n"
      "v_mul_f32 v182, v138, v182\n"
      "v_mul_f32 v183, v138, v183\n"
      "v_mfma_f32_16x16x32_bf16 v[220:223], a[220:223], v[204:207], v[220:223]\n"
      "v_mul_f32 v140, v138, v140\n"
      "v_mul_f32 v141, v138, v141\n"
      "v_mfma_f32_16x16x32_bf16 v[220:223], a[224:227], v[148:151], v[220:223]\n"
      "s_waitcnt lgkmcnt(2)\n"
      "v_cvt_pk_bf16_f32 v157, v158, v159\n"
      "v_mul_f32 v184, v214, v184\n"
      "v_mfma_f32_16x16x32_bf16 v[208:211], a[240:243], v[196:199], 0\n"
      "s_waitcnt lgkmcnt(1)\n"
      "v_mul_f32 v185, v214, v185\n"
      "v_mul_f32 v186, v214, v186\n"
      "v_mfma_f32_16x16x32_bf16 v[208:211], a[244:247], v[204:207], v[208:211]\n"
      "s_waitcnt lgkmcnt(0)\n"
      "v_mul_f32 v187, v214, v187\n"
      "v_cvt_pk_bf16_f32 v104, v104, v105\n"
      "v_mfma_f32_16x16x32_bf16 v[208:211], a[248:251], v[148:151], v[208:211]\n"
      "v_mul_f32 v106, v214, v106\n"
      "v_mul_f32_e64 v107, v214, v107\n"
      "v_mfma_f32_16x16x32_bf16 v[214:217], v[232:235], v[196:199], 0\n"
      "v_cvt_pk_bf16_f32 v181, v182, v183\n"
      "v_mul_f32 v168, v138, v168\n"
      "v_mfma_f32_16x16x32_bf16 v[214:217], v[236:239], v[204:207], v[214:217]\n"
      "v_mul_f32 v169, v138, v169\n"
      "v_mul_f32 v170, v138, v170\n"
      "v_mfma_f32_16x16x32_bf16 v[214:217], v[240:243], v[148:151], v[214:217]\n"
      "ds_read_b128 v[204:207], v10 offset:32\n"
      "v_mul_f32 v171, v138, v171\n"
      "v_mfma_f32_16x16x32_bf16 v[220:223], a[228:231], v[108:111], v[220:223]\n"
      "v_cvt_pk_bf16_f32 v140, v140, v141\n"
      "v_mul_f32 v142, v138, v142\n"
      "v_mfma_f32_16x16x32_bf16 v[220:223], a[232:235], v[116:119], v[220:223]\n"
      "v_mul_f32 v143, v138, v143\n"
      "v_cvt_pk_bf16_f32 v158, v184, v185\n"
      "v_mfma_f32_16x16x32_bf16 v[220:223], a[236:239], v[130:133], v[220:223]\n"
      "v_cvt_pk_bf16_f32 v159, v186, v187\n"
      "v_cvt_pk_bf16_f32 v105, v106, v107\n"
      "v_mfma_f32_16x16x32_bf16 v[208:211], a[252:255], v[108:111], v[208:211]\n"
      "ds_read_b64 v[138:139], v10 offset:24\n"
      "v_cvt_pk_bf16_f32 v182, v168, v169\n"
      "v_mfma_f32_16x16x32_bf16 v[208:211], v[224:227], v[116:119], v[208:211]\n"
      "ds_read_b128 v[184:187], v2 offset:18688\n"
      "v_cvt_pk_bf16_f32 v183, v170, v171\n"
      "v_mfma_f32_16x16x32_bf16 v[208:211], v[228:231], v[130:133], v[208:211]\n"
      "ds_read_b128 v[126:129], v2 offset:21760\n"
      "v_cvt_pk_bf16_f32 v141, v142, v143\n"
      "v_mfma_f32_16x16x32_bf16 v[214:217], v[244:247], v[108:111], v[214:217]\n"
      "ds_read_b128 v[106:109], v2 offset:24832\n"
      "v_cvt_pk_bf16_f32 v77, v218, v219\n"
      "v_mfma_f32_16x16x32_bf16 v[214:217], v[248:251], v[116:119], v[214:217]\n"
      "s_waitcnt lgkmcnt(4)\n"
      "v_cvt_pk_bf16_f32 v192, v192, v193\n"
      "v_cvt_pk_bf16_f32 v193, v194, v195\n"
      "v_mfma_f32_16x16x32_bf16 v[214:217], v[252:255], v[130:133], v[214:217]\n"
      "v_cvt_pk_bf16_f32 v194, v98, v99\n"
      "v_cvt_pk_bf16_f32 v195, v100, v101\n"
      "v_mfma_f32_16x16x32_bf16 v[196:199], v[152:155], v[144:147], v[40:43]\n"
      "s_waitcnt lgkmcnt(3)\n"
      "v_cvt_pk_bf16_f32 v80, v176, v177\n"
      "v_cvt_pk_bf16_f32 v81, v178, v179\n"
      "v_mfma_f32_16x16x32_bf16 v[176:179], v[134:137], v[144:147], v[44:47]\n"
      "s_waitcnt lgkmcnt(2)\n"
      "v_cvt_pk_bf16_f32 v164, v164, v165\n"
      "v_cvt_pk_bf16_f32 v165, v166, v167\n"
      "v_mfma_f32_16x16x32_bf16 v[116:119], v[156:159], v[144:147], v[48:51]\n"
      "s_waitcnt lgkmcnt(1)\n"
      "v_cvt_pk_bf16_f32 v166, v120, v121\n"
      "v_cvt_pk_bf16_f32 v167, v122, v123\n"
      "v_mfma_f32_16x16x32_bf16 v[168:171], v[204:207], v[144:147], v[24:27]\n"
      "s_waitcnt lgkmcnt(0)\n"
      "v_cvt_pk_bf16_f32 v84, v172, v173\n"
      "v_cvt_pk_bf16_f32 v85, v174, v175\n"
      "v_mfma_f32_16x16x16_bf16 v[196:199], v[124:125], v[96:97], v[196:199]\n"
      "v_fma_f32 v175, v220, v220, s52\n"
      "v_fmac_f32 v175, v221, v221\n"
      "v_fmac_f32 v175, v222, v222\n"
      "v_mfma_f32_16x16x16_bf16 v[176:179], v[212:213], v[96:97], v[176:179]\n"
      "v_fmac_f32 v175, v223, v223\n"
      "v_fmac_f32 v175, v208, v208\n"
      "v_fmac_f32 v175, v209, v209\n"
      "v_mfma_f32_16x16x16_bf16 v[116:119], v[104:105], v[96:97], v[116:119]\n"
      "v_exp_f32 v196, v196\n"
      "v_exp_f32 v197, v197\n"
      "v_mfma_f32_16x16x16_bf16 v[168:171], v[138:139], v[96:97], v[168:171]\n"
      "v_exp_f32 v198, v198\n"
      "v_exp_f32 v199, v199\n"
      "v_mfma_f32_16x16x32_bf16 v[148:151], a[216:219], v[184:187], 0\n"
      "v_exp_f32 v176, v176\n"
      "v_mfma_f32_16x16x32_bf16 v[148:151], a[220:223], v[126:129], v[148:151]\n"
      "v_exp_f32 v177, v177\n"
      "v_mfma_f32_16x16x32_bf16 v[148:151], a[224:227], v[106:109], v[148:151]\n"
      "v_exp_f32 v178, v178\n"
      "v_mfma_f32_16x16x32_bf16 v[170:173], a[240:243], v[184:187], 0\n"
      "v_exp_f32 v179, v179\n"
      "v_mfma_f32_16x16x32_bf16 v[170:173], a[244:247], v[126:129], v[170:173]\n"
      "v_cvt_pk_bf16_f32 v196, v196, v197\n"
      "v_cvt_pk_bf16_f32 v197, v198, v199\n"
      "v_mfma_f32_16x16x32_bf16 v[170:173], a[248:251], v[106:109], v[170:173]\n"
      "v_cvt_pk_bf16_f32 v198, v176, v177\n"
      "v_cvt_pk_bf16_f32 v199, v178, v179\n"
      "v_mfma_f32_16x16x32_bf16 v[120:123], v[232:235], v[184:187], 0\n"
      "v_exp_f32_e64 v116, v116\n"
      "v_mfma_f32_16x16x32_bf16 v[120:123], v[236:239], v[126:129], v[120:123]\n"
      "ds_read_u16 v78, v11\n"
      "v_exp_f32 v117, v117\n"
      "v_mfma_f32_16x16x32_bf16 v[120:123], v[240:243], v[106:109], v[120:123]\n"
      "v_exp_f32 v118, v118\n"
      "v_mfma_f32_16x16x32_bf16 v[176:179], v[152:155], v[188:191], v[52:55]\n"
      "ds_read_u16 v82, v11 offset:32\n"
      "v_exp_f32 v119, v119\n"
      "v_mfma_f32_16x16x32_bf16 v[106:109], v[134:137], v[188:191], v[56:59]\n"
      "v_exp_f32 v168, v168\n"
      "v_mfma_f32_16x16x32_bf16 v[142:145], v[156:159], v[188:191], v[60:63]\n"
      "ds_read_u16 v86, v11 offset:64\n"
      "v_cvt_pk_bf16_f32 v88, v116, v117\n"
      "v_mfma_f32_16x16x32_bf16 v[128:131], v[204:207], v[188:191], v[26:29]\n"
      "v_cvt_pk_bf16_f32 v89, v118, v119\n"
      "v_cvt_pk_bf16_f32 v90, v168, 0\n"
      "v_mfma_f32_16x16x16_bf16 v[176:179], v[124:125], v[102:103], v[176:179]\n"
      "v_fmac_f32 v175, v210, v210\n"
      "v_fmac_f32 v175, v211, v211\n"
      "v_fmac_f32_e64 v175, v214, v214\n"
      "v_mfma_f32_16x16x16_bf16 v[106:109], v[212:213], v[102:103], v[106:109]\n"
      "v_fmac_f32 v175, v215, v215\n"
      "v_fmac_f32 v175, v216, v216\n"
      "v_fmac_f32 v175, v217, v217\n"
      "v_mfma_f32_16x16x16_bf16 v[142:145], v[104:105], v[102:103], v[142:145]\n"
      "v_exp_f32 v176, v176\n"
      "v_exp_f32 v177, v177\n"
      "v_mfma_f32_16x16x16_bf16 v[128:131], v[138:139], v[102:103], v[128:131]\n"
      "v_exp_f32 v178, v178\n"
      "v_exp_f32 v179, v179\n"
      "v_mfma_f32_16x16x32_bf16 v[188:191], v[32:35], v[196:199], 0\n"
      "s_waitcnt lgkmcnt(0)\n"
      "v_exp_f32 v106, v106\n"
      "v_mfma_f32_16x16x32_bf16 v[188:191], v[32:35], v[88:91], v[188:191]\n"
      "v_exp_f32 v107, v107\n"
      "v_mfma_f32_16x16x32_bf16 v[116:119], v[112:115], v[196:199], 0\n"
      "v_exp_f32_e64 v108, v108\n"
      "v_mfma_f32_16x16x32_bf16 v[116:119], v[76:79], v[88:91], v[116:119]\n"
      "ds_read_b128 v[184:187], v2 offset:27904\n"
      "v_exp_f32_e64 v109, v109\n"
      "v_mfma_f32_16x16x32_bf16 v[130:133], v[192:195], v[196:199], 0\n"
      "ds_read_b128 v[96:99], v2 offset:30976\n"
      "v_cvt_pk_bf16_f32 v176, v176, v177\n"
      "v_mfma_f32_16x16x32_bf16 v[130:133], v[80:83], v[88:91], v[130:133]\n"
      "v_cvt_pk_bf16_f32 v177, v178, v179\n"
      "v_cvt_pk_bf16_f32 v178, v106, v107\n"
      "v_mfma_f32_16x16x32_bf16 v[100:103], v[164:167], v[196:199], 0\n"
      "v_cvt_pk_bf16_f32 v179, v108, v109\n"
      "v_exp_f32_e64 v142, v142\n"
      "v_mfma_f32_16x16x32_bf16 v[100:103], v[84:87], v[88:91], v[100:103]\n"
      "ds_read_b128 v[196:199], v2 offset:34048\n"
      "v_exp_f32 v143, v143\n"
      "v_mfma_f32_16x16x32_bf16 v[108:111], v[152:155], v[180:183], v[64:67]\n"
      "v_exp_f32 v144, v144\n"
      "v_exp_f32 v145, v145\n"
      "v_exp_f32 v128, v128\n"
      "v_cvt_pk_bf16_f32 v92, v142, v143\n"
      "v_cvt_pk_bf16_f32 v93, v144, v145\n"
      "v_cvt_pk_bf16_f32 v94, v128, 0\n"
      "v_rcp_f32 v188, v188\n"
      "s_waitcnt vmcnt(6)\n"
      "v_or3_b32 v200, v200, v201, v202\n"
      "v_mfma_f32_16x16x32_bf16 v[142:145], v[134:137], v[180:183], v[68:71]\n"
      "s_waitcnt lgkmcnt(1)\n"
      "v_mul_f32 v116, v188, v116\n"
      "v_mul_f32 v117, v188, v117\n"
      "v_cvt_pk_bf16_f32 v116, v116, v117\n"
      "v_mul_f32 v118, v188, v118\n"
      "v_mul_f32 v119, v188, v119\n"
      "v_cvt_pk_bf16_f32 v117, v118, v119\n"
      "v_mul_f32 v130, v188, v130\n"
      "v_mul_f32 v131, v188, v131\n"
      "v_mul_f32 v132, v188, v132\n"
      "v_mul_f32_e64 v133, v188, v133\n"
      "v_cvt_pk_bf16_f32 v118, v130, v131\n"
      "v_cvt_pk_bf16_f32 v119, v132, v133\n"
      "v_mfma_f32_16x16x32_bf16 v[130:133], v[156:159], v[180:183], v[72:75]\n"
      "ds_write_b128 v3, v[116:119]\n"
      "v_mfma_f32_16x16x32_bf16 v[116:119], v[204:207], v[180:183], v[28:31]\n"
      "s_waitcnt lgkmcnt(1)\n"
      "v_mul_f32 v100, v188, v100\n"
      "v_mul_f32_e64 v101, v188, v101\n"
      "v_mfma_f32_16x16x16_bf16 v[108:111], v[124:125], v[140:141], v[108:111]\n"
      "v_cvt_pk_bf16_f32 v100, v100, v101\n"
      "v_mul_f32_e64 v102, v188, v102\n"
      "v_mul_f32 v103, v188, v103\n"
      "v_mfma_f32_16x16x16_bf16 v[142:145], v[212:213], v[140:141], v[142:145]\n"
      "v_cvt_pk_bf16_f32 v101, v102, v103\n"
      "s_waitcnt vmcnt(5)\n"
      "v_or3_b32 v200, v200, v203, v39\n"
      "v_and_b32 v200, 0x7fff7fff, v200\n"
      "v_mfma_f32_16x16x16_bf16 v[130:133], v[104:105], v[140:141], v[130:133]\n"
      "v_exp_f32 v108, v108\n"
      "v_exp_f32 v109, v109\n"
      "v_mfma_f32_16x16x16_bf16 v[116:119], v[138:139], v[140:141], v[116:119]\n"
      "v_exp_f32 v110, v110\n"
      "v_exp_f32 v111, v111\n"
      "v_mfma_f32_16x16x32_bf16 v[148:151], a[228:231], v[184:187], v[148:151]\n"
      "ds_write_b64 v4, v[100:101]\n"
      "v_mfma_f32_16x16x32_bf16 v[148:151], a[232:235], v[96:99], v[148:151]\n"
      "v_exp_f32 v142, v142\n"
      "v_mfma_f32_16x16x32_bf16 v[148:151], a[236:239], v[196:199], v[148:151]\n"
      "v_exp_f32 v143, v143\n"
      "v_mfma_f32_16x16x32_bf16 v[170:173], a[252:255], v[184:187], v[170:173]\n"
      "ds_write_b32 v31, v200 offset:4608\n"
      "v_mfma_f32_16x16x32_bf16 v[170:173], v[224:227], v[96:99], v[170:173]\n"
      "ds_read_b128 v[100:103], v10\n"
      "v_exp_f32 v144, v144\n"
      "v_mfma_f32_16x16x32_bf16 v[170:173], v[228:231], v[196:199], v[170:173]\n"
      "v_exp_f32 v145, v145\n"
      "v_mfma_f32_16x16x32_bf16 v[120:123], v[244:247], v[184:187], v[120:123]\n"
      "v_cvt_pk_bf16_f32 v108, v108, v109\n"
      "v_cvt_pk_bf16_f32 v109, v110, v111\n"
      "v_mfma_f32_16x16x32_bf16 v[120:123], v[248:251], v[96:99], v[120:123]\n"
      "v_cvt_pk_bf16_f32 v110, v142, v143\n"
      "v_cvt_pk_bf16_f32 v111, v144, v145\n"
      "v_mfma_f32_16x16x32_bf16 v[120:123], v[252:255], v[196:199], v[120:123]\n"
      "ds_read_b64 v[202:203], v10 offset:16\n"
      "v_exp_f32 v130, v130\n"
      "v_mfma_f32_16x16x32_bf16 v[140:143], v[32:35], v[176:179], 0\n"
      "v_exp_f32 v131, v131\n"
      "v_mfma_f32_16x16x32_bf16 v[140:143], v[32:35], v[92:95], v[140:143]\n"
      "v_exp_f32_e64 v132, v132\n"
      "v_mfma_f32_16x16x32_bf16 v[188:191], v[112:115], v[176:179], 0\n"
      "s_waitcnt lgkmcnt(1)\n"
      "v_exp_f32 v133, v133\n"
      "v_mfma_f32_16x16x32_bf16 v[188:191], v[76:79], v[92:95], v[188:191]\n"
      "ds_read_b128 v[96:99], v2 offset:18944\n"
      "v_exp_f32_e64 v116, v116\n"
      "v_mfma_f32_16x16x32_bf16 v[180:183], v[192:195], v[176:179], 0\n"
      "ds_read_b128 v[142:145], v2 offset:22016\n"
      "v_cvt_pk_bf16_f32 v88, v130, v131\n"
      "v_mfma_f32_16x16x32_bf16 v[180:183], v[80:83], v[92:95], v[180:183]\n"
      "ds_read_b128 v[184:187], v2 offset:25088\n"
      "v_cvt_pk_bf16_f32 v89, v132, v133\n"
      "v_mfma_f32_16x16x32_bf16 v[130:133], v[164:167], v[176:179], 0\n"
      "s_waitcnt lgkmcnt(3)\n"
      "v_cvt_pk_bf16_f32 v90, v116, 0\n"
      "v_fma_f32 v168, v148, v148, s52\n"
      "v_mfma_f32_16x16x32_bf16 v[130:133], v[84:87], v[92:95], v[130:133]\n"
      "v_fmac_f32 v168, v149, v149\n"
      "v_fmac_f32 v168, v150, v150\n"
      "v_mfma_f32_16x16x32_bf16 v[176:179], v[100:103], v[152:155], v[30:33]\n"
      "v_fmac_f32 v168, v151, v151\n"
      "v_fmac_f32 v168, v170, v170\n"
      "v_mfma_f32_16x16x32_bf16 v[196:199], v[100:103], v[134:137], v[30:33]\n"
      "s_waitcnt lgkmcnt(2)\n"
      "v_fmac_f32 v168, v171, v171\n"
      "v_fmac_f32 v168, v172, v172\n"
      "v_mfma_f32_16x16x32_bf16 v[116:119], v[100:103], v[156:159], v[30:33]\n"
      "s_waitcnt lgkmcnt(1)\n"
      "v_fmac_f32 v168, v173, v173\n"
      "v_fmac_f32_e64 v168, v120, v120\n"
      "v_mfma_f32_16x16x32_bf16 v[156:159], v[100:103], v[204:207], v[30:33]\n"
      "s_waitcnt lgkmcnt(0)\n"
      "v_fmac_f32 v168, v121, v121\n"
      "v_fmac_f32 v168, v122, v122\n"
      "v_mfma_f32_16x16x16_bf16 v[176:179], v[202:203], v[124:125], v[176:179]\n"
      "v_fmac_f32 v168, v123, v123\n"
      "v_rcp_f32 v140, v140\n"
      "v_mfma_f32_16x16x16_bf16 v[196:199], v[202:203], v[212:213], v[196:199]\n"
      "v_permlane16_swap_b32 v175, v168\n"
      "v_add_f32 v175, v175, v168\n"
      "v_mfma_f32_16x16x16_bf16 v[116:119], v[202:203], v[104:105], v[116:119]\n"
      "v_mul_f32 v188, v140, v188\n"
      "v_mul_f32 v189, v140, v189\n"
      "v_cvt_pk_bf16_f32 v188, v188, v189\n"
      "v_mfma_f32_16x16x16_bf16 v[156:159], v[202:203], v[138:139], v[156:159]\n"
      "v_mul_f32 v190, v140, v190\n"
      "v_cndmask_b32 v176, v176, v196, s[58:59]\n"
      "v_mul_f32_e64 v191, v140, v191\n"
      "v_mfma_f32_16x16x32_bf16 v[136:139], a[216:219], v[96:99], 0\n"
      "v_cndmask_b32 v176, v176, v116, s[60:61]\n"
      "v_cvt_pk_bf16_f32 v189, v190, v191\n"
      "v_mfma_f32_16x16x32_bf16 v[136:139], a[220:223], v[142:145], v[136:139]\n"
      "v_mul_f32 v180, v140, v180\n"
      "v_mul_f32 v181, v140, v181\n"
      "v_mfma_f32_16x16x32_bf16 v[136:139], a[224:227], v[184:187], v[136:139]\n"
      "v_mul_f32 v182, v140, v182\n"
      "v_mul_f32 v183, v140, v183\n"
      "v_mfma_f32_16x16x32_bf16 v[196:199], a[240:243], v[96:99], 0\n"
      "v_cndmask_b32 v176, v176, v156, s[62:63]\n"
      "v_cvt_pk_bf16_f32 v190, v180, v181\n"
      "v_mfma_f32_16x16x32_bf16 v[196:199], a[244:247], v[142:145], v[196:199]\n"
      "ds_read_b128 v[100:103], v2 offset:28160\n"
      "v_cvt_pk_bf16_f32 v191, v182, v183\n"
      "v_mfma_f32_16x16x32_bf16 v[196:199], a[248:251], v[184:187], v[196:199]\n"
      "ds_read_b128 v[124:127], v2 offset:31232\n"
      "v_mul_f32 v130, v140, v130\n"
      "v_mfma_f32_16x16x32_bf16 v[178:181], v[232:235], v[96:99], 0\n"
      "ds_read_b128 v[202:205], v2 offset:34304\n"
      "v_mul_f32 v131, v140, v131\n"
      "v_mfma_f32_16x16x32_bf16 v[178:181], v[236:239], v[142:145], v[178:181]\n"
      "ds_write_b128 v3, v[188:191] offset:256\n"
      "v_mfma_f32_16x16x32_bf16 v[178:181], v[240:243], v[184:187], v[178:181]\n"
      "v_exp_f32 v176, v176\n"
      "v_mfma_f32_16x16x32_bf16 v[154:157], v[32:35], v[108:111], 0\n"
      "s_waitcnt lgkmcnt(3)\n"
      "v_cvt_pk_bf16_f32 v130, v130, v131\n"
      "v_mul_f32 v132, v140, v132\n"
      "v_mfma_f32_16x16x32_bf16 v[154:157], v[32:35], v[88:91], v[154:157]\n"
      "s_waitcnt lgkmcnt(2)\n"
      "v_mul_f32_e64 v133, v140, v133\n"
      "v_cvt_pk_bf16_f32 v131, v132, v133\n"
      "v_mfma_f32_16x16x32_bf16 v[140:143], v[112:115], v[108:111], 0\n"
      "ds_write_b64 v4, v[130:131] offset:256\n"
      "v_mfma_f32_16x16x32_bf16 v[140:143], v[76:79], v[88:91], v[140:143]\n"
      "s_waitcnt lgkmcnt(2)\n"
      "v_cvt_pk_bf16_f32 v176, v176, v176\n"
      "v_rcp_f32 v154, v154\n"
      "v_mfma_f32_16x16x32_bf16 v[188:191], v[192:195], v[108:111], 0\n"
      "ds_write_b16 v13, v176\n"
      "v_mfma_f32_16x16x32_bf16 v[188:191], v[80:83], v[88:91], v[188:191]\n"
      "ds_read_b128 v[104:107], v14\n"
      "v_readlane_b32 s45, v37, s43\n"
      "v_mfma_f32_16x16x32_bf16 v[116:119], v[164:167], v[108:111], 0\n"
      "ds_read_b128 v[156:159], v14 offset:64\n"
      "v_mul_f32 v140, v154, v140\n"
      "v_mfma_f32_16x16x32_bf16 v[116:119], v[84:87], v[88:91], v[116:119]\n"
      "v_mul_f32 v141, v154, v141\n"
      "v_cvt_pk_bf16_f32 v140, v140, v141\n"
      "v_mfma_f32_16x16x32_bf16 v[136:139], a[228:231], v[100:103], v[136:139]\n"
      "v_mul_f32 v142, v154, v142\n"
      "v_mul_f32 v143, v154, v143\n"
      "v_mfma_f32_16x16x32_bf16 v[136:139], a[232:235], v[124:127], v[136:139]\n"
      "v_cvt_pk_bf16_f32 v141, v142, v143\n"
      "v_mul_f32 v188, v154, v188\n"
      "v_mfma_f32_16x16x32_bf16 v[136:139], a[236:239], v[202:205], v[136:139]\n"
      "v_mul_f32 v189, v154, v189\n"
      "v_mul_f32_e64 v190, v154, v190\n"
      "v_mfma_f32_16x16x32_bf16 v[196:199], a[252:255], v[100:103], v[196:199]\n"
      "v_mul_f32 v191, v154, v191\n"
      "v_cvt_pk_bf16_f32 v142, v188, v189\n"
      "v_mfma_f32_16x16x32_bf16 v[196:199], v[224:227], v[124:127], v[196:199]\n"
      "s_waitcnt lgkmcnt(1)\n"
      "v_cvt_pk_bf16_f32 v143, v190, v191\n"
      "v_fma_f32 v96, v136, v136, s52\n"
      "v_mfma_f32_16x16x32_bf16 v[196:199], v[228:231], v[202:205], v[196:199]\n"
      "ds_write_b128 v3, v[140:143] offset:512\n"
      "v_mfma_f32_16x16x32_bf16 v[178:181], v[244:247], v[100:103], v[178:181]\n"
      "s_waitcnt lgkmcnt(1)\n"
      "v_fmac_f32 v96, v137, v137\n"
      "v_fmac_f32 v96, v138, v138\n"
      "v_mfma_f32_16x16x32_bf16 v[178:181], v[248:251], v[124:127], v[178:181]\n"
      "v_fmac_f32 v96, v139, v139\n"
      "v_mul_f32_e64 v116, v154, v116\n"
      "v_mfma_f32_16x16x32_bf16 v[178:181], v[252:255], v[202:205], v[178:181]\n"
      "v_fmac_f32 v96, v196, v196\n"
      "v_fmac_f32 v96, v197, v197\n"
      "v_mfma_f32_16x16x32_bf16 v[100:103], v[104:107], v[32:35], 0\n"
      "v_fmac_f32 v96, v198, v198\n"
      "v_fmac_f32 v96, v199, v199\n"
      "v_mfma_f32_16x16x32_bf16 v[100:103], v[156:159], v[32:35], v[100:103]\n"
      "v_mul_f32 v117, v154, v117\n"
      "v_cvt_pk_bf16_f32 v116, v116, v117\n"
      "v_fmac_f32 v96, v178, v178\n"
      "v_fmac_f32 v96, v179, v179\n"
      "v_fmac_f32 v96, v180, v180\n"
      "v_fmac_f32 v96, v181, v181\n"
      "v_mov_b32 v168, v96\n"
      "v_mul_f32 v118, v154, v118\n"
      "v_mul_f32 v119, v154, v119\n"
      "v_permlane16_swap_b32 v96, v168\n"
      "v_add_f32 v96, v96, v168\n"
      "v_cvt_pk_bf16_f32 v117, v118, v119\n"
      "ds_write_b64 v4, v[116:117] offset:512\n"
      "v_permlane32_swap_b32 v175, v96\n"
      "v_add_f32 v175, v175, v96\n"
      "v_readlane_b32 s46, v37, s42\n"
      "v_or3_b32 v160, v160, v161, v162\n"
      "ds_write_b32 v31, v175\n"
      "v_or_b32 v160, v160, v163\n"
      "v_cmp_ne_u32 s[56:57], 0, v160\n"
      "v_readlane_b32 s47, v38, s43\n"
      "v_add_u32 v12, 32, v12\n"
      "s_or_b32 s95, s45, s46\n"
      "s_or_b32 s95, s95, s48\n"
      "s_cmp_ge_u32 s95, 0x40000000\n"
      "s_cselect_b32 s94, -1, -2\n"
      "s_cmp_gt_u32 s42, s44\n"
      "s_cselect_b32 s95, s94, s95\n"
      "s_cmp_ge_u32 s95, 0x40000000\n"
      "s_cselect_b64 vcc, -1, s[56:57]\n"
      "s_and_b32 s47, s47, -4\n"
      "s_add_u32 s42, s42, 1\n"
      "s_add_u32 s43, s43, 1\n"
      "s_waitcnt lgkmcnt(0)\n"
      "s_barrier\n"
      "s_cbranch_vccz L_loop1_%=\n"
      "s_branch L_disp1_%=\n"
      "s_nop 7\n"
      "s_nop 7\n"
      "L_max1_%=:\n"
      "v_mfma_f32_16x16x32_bf16 v[140:143], v[104:107], v[112:115], 0\n"
      "ds_read_b128 v[96:99], v0 offset:18432\n"
      "ds_read_b128 v[116:119], v9\n"
      "v_mfma_f32_16x16x32_bf16 v[140:143], v[156:159], v[76:79], v[140:143]\n"
      "ds_read_b128 v[124:127], v1 offset:18432\n"
      "s_add_u32 m0, s64, 0\n"
      "v_mfma_f32_16x16x32_bf16 v[112:115], v[104:107], v[192:195], 0\n"
      "ds_read_b128 v[204:207], v0 offset:24576\n"
      "buffer_load_dwordx4 v16, s[16:19], s45 offen  lds\n"
      "v_mfma_f32_16x16x32_bf16 v[112:115], v[156:159], v[80:83], v[112:115]\n"
      "s_add_u32 m0, s64, 4096\n"
      "v_mfma_f32_16x16x32_bf16 v[190:193], v[104:107], v[164:167], 0\n"
      "v_mfma_f32_16x16x32_bf16 v[190:193], v[156:159], v[84:87], v[190:193]\n"
      "s_waitcnt lgkmcnt(3)\n"
      "v_mov_b32_dpp v100, v100 quad_perm:[0,0,0,0] row_mask:0xf bank_mask:0xf\n"
      "v_rcp_f32 v100, v100\n"
      "v_mfma_f32_16x16x32_bf16 v[108:111], a[0:3], v[96:99], 0\n"
      "buffer_load_dwordx4 v17, s[16:19], s45 offen  lds\n"
      "v_mfma_f32_16x16x32_bf16 v[156:159], a[24:27], v[96:99], 0\n"
      "s_add_u32 m0, s64, 8192\n"
      "v_cndmask_b32 v140, v140, v112, s[58:59]\n"
      "v_cndmask_b32 v140, v140, v190, s[60:61]\n"
      "v_mfma_f32_16x16x32_bf16 v[184:187], a[48:51], v[96:99], 0\n"
      "s_waitcnt lgkmcnt(0)\n"
      "v_mul_f32 v140, v100, v140\n"
      "v_cvt_pk_bf16_f32 v140, v140, v140\n"
      "v_mfma_f32_16x16x32_bf16 v[104:107], a[72:75], v[96:99], 0\n"
      "ds_write_b16 v12, v140\n"
      "v_mfma_f32_16x16x32_bf16 v[188:191], a[96:99], v[96:99], 0\n"
      "buffer_load_dwordx4 v18, s[16:19], s45 offen  lds\n"
      "v_mfma_f32_16x16x32_bf16 v[140:143], a[120:123], v[96:99], 0\n"
      "s_add_u32 m0, s64, 12288\n"
      "v_add_f32 v116, v116, v117\n"
      "v_add_f32 v118, v118, v119\n"
      "v_mfma_f32_16x16x32_bf16 v[108:111], a[4:7], v[124:127], v[108:111]\n"
      "v_add_f32 v116, v116, v118\n"
      "v_rsq_f32 v116, v116\n"
      "v_mfma_f32_16x16x32_bf16 v[108:111], a[8:11], v[204:207], v[108:111]\n"
      "ds_read_b128 v[152:155], v1 offset:24576\n"
      "v_mul_f32 v116, 0x415db3d7, v116\n"
      "v_mfma_f32_16x16x32_bf16 v[156:159], a[28:31], v[124:127], v[156:159]\n"
      "ds_read_b128 v[160:163], v0 offset:30720\n"
      "v_mov_b32_e64 v118, v116\n"
      "v_mfma_f32_16x16x32_bf16 v[156:159], a[32:35], v[204:207], v[156:159]\n"
      "ds_read_b128 v[130:133], v1 offset:30720\n"
      "v_permlane32_swap_b32_e64 v116, v118\n"
      "v_mfma_f32_16x16x32_bf16 v[184:187], a[52:55], v[124:127], v[184:187]\n"
      "buffer_load_dwordx4 v19, s[16:19], s45 offen  lds\n"
      "v_mfma_f32_16x16x32_bf16 v[184:187], a[56:59], v[204:207], v[184:187]\n"
      "s_add_u32 m0, s65, 16384\n"
      "v_mov_b32 v117, v116\n"
      "v_mul_f32 v136, v118, v136\n"
      "v_mfma_f32_16x16x32_bf16 v[104:107], a[76:79], v[124:127], v[104:107]\n"
      "v_permlane16_swap_b32 v116, v117\n"
      "v_mfma_f32_16x16x32_bf16 v[104:107], a[80:83], v[204:207], v[104:107]\n"
      "s_waitcnt lgkmcnt(2)\n"
      "v_mul_f32 v220, v116, v220\n"
      "v_mul_f32 v221, v116, v221\n"
      "v_mfma_f32_16x16x32_bf16 v[188:191], a[100:103], v[124:127], v[188:191]\n"
      "buffer_load_dwordx4 v15, s[16:19], s45 offen  lds\n"
      "v_mfma_f32_16x16x32_bf16 v[188:191], a[104:107], v[204:207], v[188:191]\n"
      "s_waitcnt lgkmcnt(0)\n"
      "v_cvt_pk_bf16_f32 v220, v220, v221\n"
      "v_mul_f32 v222, v116, v222\n"
      "v_mfma_f32_16x16x32_bf16 v[140:143], a[124:127], v[124:127], v[140:143]\n"
      "v_mul_f32 v223, v116, v223\n"
      "v_mul_f32 v214, v116, v214\n"
      "v_mfma_f32_16x16x32_bf16 v[140:143], a[128:131], v[204:207], v[140:143]\n"
      "v_mul_f32_e64 v215, v116, v215\n"
      "v_cvt_pk_bf16_f32 v221, v222, v223\n"
      "v_mfma_f32_16x16x32_bf16 v[108:111], a[12:15], v[152:155], v[108:111]\n"
      "buffer_load_dwordx4 v[100:103], v36, s[24:27], s47 offen\n"
      "v_mfma_f32_16x16x32_bf16 v[108:111], a[16:19], v[160:163], v[108:111]\n"
      "v_mul_f32 v208, v116, v208\n"
      "v_mul_f32 v209, v116, v209\n"
      "v_mfma_f32_16x16x32_bf16 v[108:111], a[20:23], v[130:133], v[108:111]\n"
      "v_mul_f32 v210, v116, v210\n"
      "v_mul_f32 v211, v116, v211\n"
      "v_mfma_f32_16x16x32_bf16 v[156:159], a[36:39], v[152:155], v[156:159]\n"
      "v_cvt_pk_bf16_f32 v214, v214, v215\n"
      "v_mul_f32_e64 v216, v116, v216\n"
      "v_mfma_f32_16x16x32_bf16 v[156:159], a[40:43], v[160:163], v[156:159]\n"
      "buffer_load_dword v39, v29, s[24:27], s47 offen\n"
      "v_mfma_f32_16x16x32_bf16 v[156:159], a[44:47], v[130:133], v[156:159]\n"
      "v_mul_f32_e64 v217, v116, v217\n"
      "v_cvt_pk_bf16_f32 v222, v208, v209\n"
      "v_mfma_f32_16x16x32_bf16 v[184:187], a[60:63], v[152:155], v[184:187]\n"
      "v_cvt_pk_bf16_f32 v223, v210, v211\n"
      "v_cvt_pk_bf16_f32 v215, v216, v217\n"
      "v_mfma_f32_16x16x32_bf16 v[184:187], a[64:67], v[160:163], v[184:187]\n"
      "ds_write_b128 v5, v[220:223] offset:18432\n"
      "v_mfma_f32_16x16x32_bf16 v[184:187], a[68:71], v[130:133], v[184:187]\n"
      "v_mul_f32 v148, v117, v148\n"
      "v_mul_f32 v149, v117, v149\n"
      "v_mfma_f32_16x16x32_bf16 v[104:107], a[84:87], v[152:155], v[104:107]\n"
      "v_cvt_pk_bf16_f32 v148, v148, v149\n"
      "v_mul_f32 v150, v117, v150\n"
      "v_mfma_f32_16x16x32_bf16 v[104:107], a[88:91], v[160:163], v[104:107]\n"
      "ds_write_b64 v6, v[214:215] offset:18432\n"
      "v_mfma_f32_16x16x32_bf16 v[104:107], a[92:95], v[130:133], v[104:107]\n"
      "v_mul_f32 v151, v117, v151\n"
      "v_mul_f32 v120, v117, v120\n"
      "v_mfma_f32_16x16x32_bf16 v[188:191], a[108:111], v[152:155], v[188:191]\n"
      "v_mul_f32 v121, v117, v121\n"
      "v_cvt_pk_bf16_f32 v149, v150, v151\n"
      "v_mfma_f32_16x16x32_bf16 v[188:191], a[112:115], v[160:163], v[188:191]\n"
      "v_mul_f32 v170, v117, v170\n"
      "v_mul_f32 v171, v117, v171\n"
      "v_mfma_f32_16x16x32_bf16 v[188:191], a[116:119], v[130:133], v[188:191]\n"
      "v_mul_f32 v172, v117, v172\n"
      "v_mul_f32 v173, v117, v173\n"
      "v_mfma_f32_16x16x32_bf16 v[140:143], a[132:135], v[152:155], v[140:143]\n"
      "v_cvt_pk_bf16_f32 v120, v120, v121\n"
      "v_mul_f32 v122, v117, v122\n"
      "v_mfma_f32_16x16x32_bf16 v[140:143], a[136:139], v[160:163], v[140:143]\n"
      "v_mul_f32 v123, v117, v123\n"
      "v_cvt_pk_bf16_f32 v150, v170, v171\n"
      "v_mfma_f32_16x16x32_bf16 v[140:143], a[140:143], v[130:133], v[140:143]\n"
      "v_cvt_pk_bf16_f32 v151, v172, v173\n"
      "v_cvt_pk_bf16_f32 v121, v122, v123\n"
      "v_mfma_f32_16x16x32_bf16 v[112:115], v[96:99], a[144:147], 0\n"
      "ds_write_b128 v5, v[148:151] offset:24576\n"
      "v_mfma_f32_16x16x32_bf16 v[112:115], v[124:127], a[148:151], v[112:115]\n"
      "v_mul_f32_e64 v137, v118, v137\n"
      "v_cvt_pk_bf16_f32 v136, v136, v137\n"
      "v_mfma_f32_16x16x32_bf16 v[112:115], v[204:207], a[152:155], v[112:115]\n"
      "v_mul_f32 v138, v118, v138\n"
      "v_mul_f32 v139, v118, v139\n"
      "v_mfma_f32_16x16x32_bf16 v[112:115], v[152:155], a[156:159], v[112:115]\n"
      "ds_write_b64 v6, v[120:121] offset:24576\n"
      "v_mfma_f32_16x16x32_bf16 v[112:115], v[160:163], a[160:163], v[112:115]\n"
      "v_mul_f32 v178, v118, v178\n"
      "v_mul_f32 v179, v118, v179\n"
      "v_mfma_f32_16x16x32_bf16 v[112:115], v[130:133], a[164:167], v[112:115]\n"
      "v_cvt_pk_bf16_f32 v137, v138, v139\n"
      "v_mul_f32 v196, v118, v196\n"
      "v_mfma_f32_16x16x32_bf16 v[192:195], v[96:99], a[168:171], 0\n"
      "v_mul_f32 v197, v118, v197\n"
      "v_mul_f32_e64 v198, v118, v198\n"
      "v_mfma_f32_16x16x32_bf16 v[192:195], v[124:127], a[172:175], v[192:195]\n"
      "ds_read_b128 v[220:223], v0 offset:20480\n"
      "v_mul_f32_e64 v199, v118, v199\n"
      "v_mfma_f32_16x16x32_bf16 v[192:195], v[204:207], a[176:179], v[192:195]\n"
      "ds_read_b128 v[168:171], v1 offset:20480\n"
      "v_cvt_pk_bf16_f32 v178, v178, v179\n"
      "v_mfma_f32_16x16x32_bf16 v[192:195], v[152:155], a[180:183], v[192:195]\n"
      "ds_read_b128 v[216:219], v0 offset:26624\n"
      "v_mul_f32_e64 v180, v118, v180\n"
      "v_mfma_f32_16x16x32_bf16 v[192:195], v[160:163], a[184:187], v[192:195]\n"
      "ds_read_b128 v[172:175], v1 offset:26624\n"
      "v_mul_f32_e64 v181, v118, v181\n"
      "v_mfma_f32_16x16x32_bf16 v[192:195], v[130:133], a[188:191], v[192:195]\n"
      "ds_read_b128 v[148:151], v0 offset:32768\n"
      "v_cvt_pk_bf16_f32 v138, v196, v197\n"
      "v_mfma_f32_16x16x32_bf16 v[164:167], v[96:99], a[192:195], 0\n"
      "ds_read_b128 v[208:211], v1 offset:32768\n"
      "v_cvt_pk_bf16_f32 v139, v198, v199\n"
      "v_mfma_f32_16x16x32_bf16 v[164:167], v[124:127], a[196:199], v[164:167]\n"
      "ds_write_b128 v5, v[136:139] offset:30720\n"
      "v_mfma_f32_16x16x32_bf16 v[164:167], v[204:207], a[200:203], v[164:167]\n"
      "s_waitcnt lgkmcnt(4)\n"
      "v_cvt_pk_bf16_f32 v179, v180, v181\n"
      "v_fma_f32 v124, v108, v108, s51\n"
      "v_mfma_f32_16x16x32_bf16 v[164:167], v[152:155], a[204:207], v[164:167]\n"
      "s_waitcnt lgkmcnt(3)\n"
      "v_fma_f32 v214, v104, v104, s51\n"
      "v_fmac_f32_e64 v124, v109, v109\n"
      "v_mfma_f32_16x16x32_bf16 v[164:167], v[160:163], a[208:211], v[164:167]\n"
      "ds_write_b64 v6, v[178:179] offset:30720\n"
      "v_mfma_f32_16x16x32_bf16 v[164:167], v[130:133], a[212:215], v[164:167]\n"
      "s_waitcnt lgkmcnt(2)\n"
      "v_fmac_f32 v214, v105, v105\n"
      "v_fmac_f32 v124, v110, v110\n"
      "v_mfma_f32_16x16x32_bf16 v[204:207], a[0:3], v[220:223], 0\n"
      "v_fmac_f32 v214, v106, v106\n"
      "v_fmac_f32 v124, v111, v111\n"
      "v_mfma_f32_16x16x32_bf16 v[204:207], a[4:7], v[168:171], v[204:207]\n"
      "v_fmac_f32 v214, v107, v107\n"
      "v_fmac_f32 v124, v156, v156\n"
      "v_mfma_f32_16x16x32_bf16 v[204:207], a[8:11], v[216:219], v[204:207]\n"
      "v_fmac_f32 v214, v188, v188\n"
      "v_fmac_f32 v124, v157, v157\n"
      "v_mfma_f32_16x16x32_bf16 v[204:207], a[12:15], v[172:175], v[204:207]\n"
      "v_fmac_f32 v214, v189, v189\n"
      "v_fmac_f32 v124, v158, v158\n"
      "v_mfma_f32_16x16x32_bf16 v[204:207], a[16:19], v[148:151], v[204:207]\n"
      "v_fmac_f32 v214, v190, v190\n"
      "v_fmac_f32 v124, v159, v159\n"
      "v_mfma_f32_16x16x32_bf16 v[204:207], a[20:23], v[208:211], v[204:207]\n"
      "v_fmac_f32 v214, v191, v191\n"
      "v_fmac_f32 v124, v184, v184\n"
      "v_mfma_f32_16x16x32_bf16 v[200:203], a[24:27], v[220:223], 0\n"
      "v_fmac_f32 v214, v140, v140\n"
      "v_fmac_f32 v124, v185, v185\n"
      "v_mfma_f32_16x16x32_bf16 v[200:203], a[28:31], v[168:171], v[200:203]\n"
      "v_fmac_f32 v214, v141, v141\n"
      "v_fmac_f32 v124, v186, v186\n"
      "v_mfma_f32_16x16x32_bf16 v[200:203], a[32:35], v[216:219], v[200:203]\n"
      "v_fmac_f32 v214, v142, v142\n"
      "v_fmac_f32 v124, v187, v187\n"
      "v_mfma_f32_16x16x32_bf16 v[200:203], a[36:39], v[172:175], v[200:203]\n"
      "v_fmac_f32 v214, v143, v143\n"
      "v_fma_f32 v152, v204, v204, s51\n"
      "v_mfma_f32_16x16x32_bf16 v[200:203], a[40:43], v[148:151], v[200:203]\n"
      "v_permlane16_swap_b32 v124, v214\n"
      "v_mfma_f32_16x16x32_bf16 v[200:203], a[44:47], v[208:211], v[200:203]\n"
      "v_add_f32 v124, v124, v214\n"
      "v_mov_b32 v214, v124\n"
      "v_mfma_f32_16x16x32_bf16 v[120:123], a[48:51], v[220:223], 0\n"
      "v_fmac_f32 v152, v205, v205\n"
      "v_permlane32_swap_b32 v124, v214\n"
      "v_mfma_f32_16x16x32_bf16 v[120:123], a[52:55], v[168:171], v[120:123]\n"
      "v_add_f32 v124, v124, v214\n"
      "v_rsq_f32 v124, v124\n"
      "v_mfma_f32_16x16x32_bf16 v[120:123], a[56:59], v[216:219], v[120:123]\n"
      "v_mov_b32 v214, v124\n"
      "v_fmac_f32 v152, v206, v206\n"
      "v_mfma_f32_16x16x32_bf16 v[120:123], a[60:63], v[172:175], v[120:123]\n"
      "v_permlane16_swap_b32 v124, v214\n"
      "v_mfma_f32_16x16x32_bf16 v[120:123], a[64:67], v[148:151], v[120:123]\n"
      "v_mul_f32 v124, s50, v124\n"
      "v_mul_f32 v104, v214, v104\n"
      "v_mfma_f32_16x16x32_bf16 v[120:123], a[68:71], v[208:211], v[120:123]\n"
      "v_mul_f32 v105, v214, v105\n"
      "v_mul_f32 v108, v124, v108\n"
      "v_mfma_f32_16x16x32_bf16 v[176:179], a[72:75], v[220:223], 0\n"
      "v_mul_f32_e64 v109, v124, v109\n"
      "v_cvt_pk_bf16_f32 v104, v104, v105\n"
      "v_mfma_f32_16x16x32_bf16 v[176:179], a[76:79], v[168:171], v[176:179]\n"
      "v_mul_f32 v106, v214, v106\n"
      "v_mul_f32 v107, v214, v107\n"
      "v_mfma_f32_16x16x32_bf16 v[176:179], a[80:83], v[216:219], v[176:179]\n"
      "v_mul_f32 v140, v214, v140\n"
      "v_mul_f32 v141, v214, v141\n"
      "v_mfma_f32_16x16x32_bf16 v[176:179], a[84:87], v[172:175], v[176:179]\n"
      "v_cvt_pk_bf16_f32 v108, v108, v109\n"
      "v_mul_f32_e64 v110, v124, v110\n"
      "v_mfma_f32_16x16x32_bf16 v[176:179], a[88:91], v[148:151], v[176:179]\n"
      "v_mul_f32 v111, v124, v111\n"
      "v_mul_f32 v184, v124, v184\n"
      "v_mfma_f32_16x16x32_bf16 v[176:179], a[92:95], v[208:211], v[176:179]\n"
      "v_mul_f32_e64 v185, v124, v185\n"
      "v_cvt_pk_bf16_f32 v105, v106, v107\n"
      "v_mfma_f32_16x16x32_bf16 v[116:119], a[96:99], v[220:223], 0\n"
      "v_mul_f32 v188, v214, v188\n"
      "v_mul_f32 v189, v214, v189\n"
      "v_mfma_f32_16x16x32_bf16 v[116:119], a[100:103], v[168:171], v[116:119]\n"
      "v_mul_f32 v190, v214, v190\n"
      "v_mul_f32 v191, v214, v191\n"
      "v_mfma_f32_16x16x32_bf16 v[116:119], a[104:107], v[216:219], v[116:119]\n"
      "v_cvt_pk_bf16_f32 v140, v140, v141\n"
      "v_mul_f32_e64 v142, v214, v142\n"
      "v_mfma_f32_16x16x32_bf16 v[116:119], a[108:111], v[172:175], v[116:119]\n"
      "v_mul_f32 v143, v214, v143\n"
      "v_cvt_pk_bf16_f32 v109, v110, v111\n"
      "v_mfma_f32_16x16x32_bf16 v[116:119], a[112:115], v[148:151], v[116:119]\n"
      "v_mul_f32 v156, v124, v156\n"
      "v_mul_f32 v157, v124, v157\n"
      "v_mfma_f32_16x16x32_bf16 v[116:119], a[116:119], v[208:211], v[116:119]\n"
      "v_mul_f32 v158, v124, v158\n"
      "v_mul_f32_e64 v159, v124, v159\n"
      "v_mfma_f32_16x16x32_bf16 v[212:215], a[120:123], v[220:223], 0\n"
      "v_cvt_pk_bf16_f32 v184, v184, v185\n"
      "v_mul_f32 v186, v124, v186\n"
      "v_mfma_f32_16x16x32_bf16 v[212:215], a[124:127], v[168:171], v[212:215]\n"
      "v_mul_f32 v187, v124, v187\n"
      "v_cvt_pk_bf16_f32 v106, v188, v189\n"
      "v_mfma_f32_16x16x32_bf16 v[212:215], a[128:131], v[216:219], v[212:215]\n"
      "v_cvt_pk_bf16_f32 v107, v190, v191\n"
      "v_cvt_pk_bf16_f32 v141, v142, v143\n"
      "v_mfma_f32_16x16x32_bf16 v[212:215], a[132:135], v[172:175], v[212:215]\n"
      "v_cvt_pk_bf16_f32 v110, v156, v157\n"
      "v_cvt_pk_bf16_f32 v111, v158, v159\n"
      "v_mfma_f32_16x16x32_bf16 v[212:215], a[136:139], v[148:151], v[212:215]\n"
      "v_cvt_pk_bf16_f32 v185, v186, v187\n"
      "v_fma_f32 v155, v176, v176, s51\n"
      "v_mfma_f32_16x16x32_bf16 v[212:215], a[140:143], v[208:211], v[212:215]\n"
      "v_fmac_f32 v155, v177, v177\n"
      "v_fmac_f32 v155, v178, v178\n"
      "v_mfma_f32_16x16x32_bf16 v[124:127], v[220:223], a[144:147], 0\n"
      "v_fmac_f32 v152, v207, v207\n"
      "v_fmac_f32 v155, v179, v179\n"
      "v_mfma_f32_16x16x32_bf16 v[124:127], v[168:171], a[148:151], v[124:127]\n"
      "v_fmac_f32 v152, v200, v200\n"
      "v_fmac_f32 v155, v116, v116\n"
      "v_mfma_f32_16x16x32_bf16 v[124:127], v[216:219], a[152:155], v[124:127]\n"
      "v_fmac_f32 v152, v201, v201\n"
      "v_fmac_f32 v155, v117, v117\n"
      "v_mfma_f32_16x16x32_bf16 v[124:127], v[172:175], a[156:159], v[124:127]\n"
      "v_fmac_f32 v152, v202, v202\n"
      "v_fmac_f32 v155, v118, v118\n"
      "v_mfma_f32_16x16x32_bf16 v[124:127], v[148:151], a[160:163], v[124:127]\n"
      "v_fmac_f32 v152, v203, v203\n"
      "v_fmac_f32 v155, v119, v119\n"
      "v_mfma_f32_16x16x32_bf16 v[124:127], v[208:211], a[164:167], v[124:127]\n"
      "v_fmac_f32 v152, v120, v120\n"
      "v_fmac_f32 v155, v212, v212\n"
      "v_mfma_f32_16x16x32_bf16 v[128:131], v[220:223], a[168:171], 0\n"
      "v_fmac_f32 v152, v121, v121\n"
      "v_fmac_f32 v155, v213, v213\n"
      "v_mfma_f32_16x16x32_bf16 v[128:131], v[168:171], a[172:175], v[128:131]\n"
      "ds_read_b128 v[136:139], v0 offset:22528\n"
      "v_fmac_f32_e64 v152, v122, v122\n"
      "v_mfma_f32_16x16x32_bf16 v[128:131], v[216:219], a[176:179], v[128:131]\n"
      "ds_read_b128 v[156:159], v1 offset:22528\n"
      "v_fmac_f32_e64 v155, v214, v214\n"
      "v_mfma_f32_16x16x32_bf16 v[128:131], v[172:175], a[180:183], v[128:131]\n"
      "ds_read_b128 v[160:163], v0 offset:28672\n"
      "v_fmac_f32 v152, v123, v123\n"
      "v_mfma_f32_16x16x32_bf16 v[128:131], v[148:151], a[184:187], v[128:131]\n"
      "ds_read_b128 v[96:99], v1 offset:28672\n"
      "v_fmac_f32 v155, v215, v215\n"
      "v_mfma_f32_16x16x32_bf16 v[128:131], v[208:211], a[188:191], v[128:131]\n"
      "ds_read_b128 v[132:135], v0 offset:34816\n"
      "v_permlane16_swap_b32_e64 v152, v155\n"
      "v_mfma_f32_16x16x32_bf16 v[186:189], v[220:223], a[192:195], 0\n"
      "ds_read_b128 v[220:223], v1 offset:34816\n"
      "v_add_f32 v152, v152, v155\n"
      "v_mfma_f32_16x16x32_bf16 v[186:189], v[168:171], a[196:199], v[186:189]\n"
      "s_waitcnt lgkmcnt(5)\n"
      "v_mov_b32_e64 v155, v152\n"
      "v_cvt_pk_bf16_f32 v112, v112, v113\n"
      "v_mfma_f32_16x16x32_bf16 v[186:189], v[216:219], a[200:203], v[186:189]\n"
      "s_waitcnt lgkmcnt(4)\n"
      "v_permlane32_swap_b32 v152, v155\n"
      "v_mfma_f32_16x16x32_bf16 v[186:189], v[172:175], a[204:207], v[186:189]\n"
      "s_waitcnt lgkmcnt(3)\n"
      "v_add_f32 v152, v152, v155\n"
      "v_rsq_f32 v152, v152\n"
      "v_mfma_f32_16x16x32_bf16 v[186:189], v[148:151], a[208:211], v[186:189]\n"
      "s_waitcnt lgkmcnt(2)\n"
      "v_mov_b32_e64 v155, v152\n"
      "v_cvt_pk_bf16_f32 v113, v114, v115\n"
      "v_mfma_f32_16x16x32_bf16 v[186:189], v[208:211], a[212:215], v[186:189]\n"
      "s_waitcnt lgkmcnt(1)\n"
      "v_permlane16_swap_b32 v152, v155\n"
      "v_mfma_f32_16x16x32_bf16 v[196:199], a[0:3], v[136:139], 0\n"
      "s_waitcnt lgkmcnt(0)\n"
      "v_mul_f32 v152, s50, v152\n"
      "v_mul_f32 v176, v155, v176\n"
      "v_mfma_f32_16x16x32_bf16 v[196:199], a[4:7], v[156:159], v[196:199]\n"
      "v_mul_f32 v177, v155, v177\n"
      "v_mul_f32_e64 v204, v152, v204\n"
      "v_mfma_f32_16x16x32_bf16 v[196:199], a[8:11], v[160:163], v[196:199]\n"
      "v_mul_f32 v205, v152, v205\n"
      "v_cvt_pk_bf16_f32 v176, v176, v177\n"
      "v_mfma_f32_16x16x32_bf16 v[196:199], a[12:15], v[96:99], v[196:199]\n"
      "v_mul_f32 v178, v155, v178\n"
      "v_mul_f32 v179, v155, v179\n"
      "v_mfma_f32_16x16x32_bf16 v[196:199], a[16:19], v[132:135], v[196:199]\n"
      "v_mul_f32 v212, v155, v212\n"
      "v_mul_f32_e64 v213, v155, v213\n"
      "v_mfma_f32_16x16x32_bf16 v[196:199], a[20:23], v[220:223], v[196:199]\n"
      "v_cvt_pk_bf16_f32 v204, v204, v205\n"
      "v_mul_f32 v206, v152, v206\n"
      "v_mfma_f32_16x16x32_bf16 v[142:145], a[24:27], v[136:139], 0\n"
      "v_mul_f32 v207, v152, v207\n"
      "v_mul_f32_e64 v120, v152, v120\n"
      "v_mfma_f32_16x16x32_bf16 v[142:145], a[28:31], v[156:159], v[142:145]\n"
      "v_mul_f32 v121, v152, v121\n"
      "v_cvt_pk_bf16_f32 v177, v178, v179\n"
      "v_mfma_f32_16x16x32_bf16 v[142:145], a[32:35], v[160:163], v[142:145]\n"
      "v_mul_f32 v116, v155, v116\n"
      "v_mul_f32 v117, v155, v117\n"
      "v_mfma_f32_16x16x32_bf16 v[142:145], a[36:39], v[96:99], v[142:145]\n"
      "v_mul_f32 v118, v155, v118\n"
      "v_mul_f32_e64 v119, v155, v119\n"
      "v_mfma_f32_16x16x32_bf16 v[142:145], a[40:43], v[132:135], v[142:145]\n"
      "ds_read_b128 v[208:211], v7\n"
      "v_cvt_pk_bf16_f32 v212, v212, v213\n"
      "v_mfma_f32_16x16x32_bf16 v[142:145], a[44:47], v[220:223], v[142:145]\n"
      "ds_read_b128 v[172:175], v7 offset:4096\n"
      "v_mul_f32 v214, v155, v214\n"
      "v_mfma_f32_16x16x32_bf16 v[180:183], a[48:51], v[136:139], 0\n"
      "ds_read_b128 v[148:151], v7 offset:8192\n"
      "v_mul_f32 v215, v155, v215\n"
      "v_mfma_f32_16x16x32_bf16 v[180:183], a[52:55], v[156:159], v[180:183]\n"
      "v_cvt_pk_bf16_f32 v205, v206, v207\n"
      "v_mul_f32 v200, v152, v200\n"
      "v_mfma_f32_16x16x32_bf16 v[180:183], a[56:59], v[160:163], v[180:183]\n"
      "ds_read_b128 v[216:219], v7 offset:12288\n"
      "v_mul_f32 v201, v152, v201\n"
      "v_mfma_f32_16x16x32_bf16 v[180:183], a[60:63], v[96:99], v[180:183]\n"
      "ds_read_b128 v[168:171], v7 offset:16384\n"
      "v_mul_f32 v202, v152, v202\n"
      "v_mfma_f32_16x16x32_bf16 v[180:183], a[64:67], v[132:135], v[180:183]\n"
      "v_mul_f32 v203, v152, v203\n"
      "v_cvt_pk_bf16_f32 v120, v120, v121\n"
      "v_mfma_f32_16x16x32_bf16 v[180:183], a[68:71], v[220:223], v[180:183]\n"
      "v_mul_f32 v122, v152, v122\n"
      "v_mul_f32 v123, v152, v123\n"
      "v_mfma_f32_16x16x32_bf16 v[152:155], a[72:75], v[136:139], 0\n"
      "s_waitcnt lgkmcnt(2)\n"
      "v_cvt_pk_bf16_f32 v178, v116, v117\n"
      "v_cvt_pk_bf16_f32 v179, v118, v119\n"
      "v_mfma_f32_16x16x32_bf16 v[152:155], a[76:79], v[156:159], v[152:155]\n"
      "buffer_store_dwordx4 v[208:211], v20, s[20:23], s46 offen sc1\n"
      "v_mfma_f32_16x16x32_bf16 v[152:155], a[80:83], v[160:163], v[152:155]\n"
      "s_waitcnt lgkmcnt(0)\n"
      "v_cvt_pk_bf16_f32 v213, v214, v215\n"
      "v_cvt_pk_bf16_f32 v206, v200, v201\n"
      "v_mfma_f32_16x16x32_bf16 v[152:155], a[84:87], v[96:99], v[152:155]\n"
      "v_cvt_pk_bf16_f32 v207, v202, v203\n"
      "v_cvt_pk_bf16_f32 v121, v122, v123\n"
      "v_mfma_f32_16x16x32_bf16 v[152:155], a[88:91], v[132:135], v[152:155]\n"
      "v_fma_f32 v122, v196, v196, s51\n"
      "v_fmac_f32 v122, v197, v197\n"
      "v_mfma_f32_16x16x32_bf16 v[152:155], a[92:95], v[220:223], v[152:155]\n"
      "v_fmac_f32 v122, v198, v198\n"
      "v_fmac_f32_e64 v122, v199, v199\n"
      "v_mfma_f32_16x16x32_bf16 v[208:211], a[96:99], v[136:139], 0\n"
      "v_fmac_f32 v122, v142, v142\n"
      "v_fmac_f32 v122, v143, v143\n"
      "v_mfma_f32_16x16x32_bf16 v[208:211], a[100:103], v[156:159], v[208:211]\n"
      "v_fmac_f32 v122, v144, v144\n"
      "v_fmac_f32 v122, v145, v145\n"
      "v_mfma_f32_16x16x32_bf16 v[208:211], a[104:107], v[160:163], v[208:211]\n"
      "v_fma_f32 v123, v152, v152, s51\n"
      "v_fmac_f32_e64 v123, v153, v153\n"
      "v_mfma_f32_16x16x32_bf16 v[208:211], a[108:111], v[96:99], v[208:211]\n"
      "buffer_store_dwordx4 v[172:175], v21, s[20:23], s46 offen sc1\n"
      "v_mfma_f32_16x16x32_bf16 v[208:211], a[112:115], v[132:135], v[208:211]\n"
      "v_fmac_f32 v123, v154, v154\n"
      "v_fmac_f32 v123, v155, v155\n"
      "v_mfma_f32_16x16x32_bf16 v[208:211], a[116:119], v[220:223], v[208:211]\n"
      "v_fmac_f32 v122, v180, v180\n"
      "v_fmac_f32 v122, v181, v181\n"
      "v_mfma_f32_16x16x32_bf16 v[200:203], a[120:123], v[136:139], 0\n"
      "v_fmac_f32 v122, v182, v182\n"
      "v_fmac_f32 v122, v183, v183\n"
      "v_mfma_f32_16x16x32_bf16 v[200:203], a[124:127], v[156:159], v[200:203]\n"
      "v_cvt_pk_bf16_f32 v114, v124, v125\n"
      "v_cvt_pk_bf16_f32 v115, v126, v127\n"
      "v_mfma_f32_16x16x32_bf16 v[200:203], a[128:131], v[160:163], v[200:203]\n"
      "v_fmac_f32 v123, v208, v208\n"
      "v_fmac_f32 v123, v209, v209\n"
      "v_mfma_f32_16x16x32_bf16 v[200:203], a[132:135], v[96:99], v[200:203]\n"
      "v_fmac_f32 v123, v210, v210\n"
      "v_fmac_f32_e64 v123, v211, v211\n"
      "v_mfma_f32_16x16x32_bf16 v[200:203], a[136:139], v[132:135], v[200:203]\n"
      "v_cvt_pk_bf16_f32 v192, v192, v193\n"
      "v_cvt_pk_bf16_f32 v193, v194, v195\n"
      "v_mfma_f32_16x16x32_bf16 v[200:203], a[140:143], v[220:223], v[200:203]\n"
      "buffer_store_dwordx4 v[148:151], v22, s[20:23], s46 offen sc1\n"
      "s_nop 1\n"
      "v_mfma_f32_16x16x32_bf16 v[148:151], v[136:139], a[144:147], 0\n"
      "v_cvt_pk_bf16_f32 v194, v128, v129\n"
      "v_cvt_pk_bf16_f32 v195, v130, v131\n"
      "v_mfma_f32_16x16x32_bf16 v[148:151], v[156:159], a[148:151], v[148:151]\n"
      "v_cvt_pk_bf16_f32 v164, v164, v165\n"
      "v_cvt_pk_bf16_f32 v165, v166, v167\n"
      "v_mfma_f32_16x16x32_bf16 v[148:151], v[160:163], a[152:155], v[148:151]\n"
      "v_fmac_f32 v123, v200, v200\n"
      "v_fmac_f32 v123, v201, v201\n"
      "v_mfma_f32_16x16x32_bf16 v[148:151], v[96:99], a[156:159], v[148:151]\n"
      "v_fmac_f32 v123, v202, v202\n"
      "v_fmac_f32 v123, v203, v203\n"
      "v_mfma_f32_16x16x32_bf16 v[148:151], v[132:135], a[160:163], v[148:151]\n"
      "v_cvt_pk_bf16_f32 v166, v186, v187\n"
      "v_permlane16_swap_b32 v122, v123\n"
      "v_mfma_f32_16x16x32_bf16 v[148:151], v[220:223], a[164:167], v[148:151]\n"
      "v_add_f32 v122, v122, v123\n"
      "v_mov_b32_e64 v123, v122\n"
      "v_mfma_f32_16x16x32_bf16 v[128:131], v[136:139], a[168:171], 0\n"
      "v_cvt_pk_bf16_f32 v167, v188, v189\n"
      "v_permlane32_swap_b32_e64 v122, v123\n"
      "v_mfma_f32_16x16x32_bf16 v[128:131], v[156:159], a[172:175], v[128:131]\n"
      "buffer_store_dwordx4 v[216:219], v23, s[20:23], s46 offen sc1\n"
      "v_mfma_f32_16x16x32_bf16 v[128:131], v[160:163], a[176:179], v[128:131]\n"
      "ds_read_b128 v[188:191], v2\n"
      "v_add_f32_e64 v122, v122, v123\n"
      "v_mfma_f32_16x16x32_bf16 v[128:131], v[96:99], a[180:183], v[128:131]\n"
      "ds_read_b128 v[172:175], v2 offset:3072\n"
      "v_rsq_f32 v122, v122\n"
      "v_mfma_f32_16x16x32_bf16 v[128:131], v[132:135], a[184:187], v[128:131]\n"
      "ds_read_b128 v[116:119], v2 offset:6144\n"
      "v_mov_b32 v123, v122\n"
      "v_mfma_f32_16x16x32_bf16 v[128:131], v[220:223], a[188:191], v[128:131]\n"
      "v_cvt_pk_bf16_f32 v76, v148, v149\n"
      "v_permlane16_swap_b32 v122, v123\n"
      "v_mfma_f32_16x16x32_bf16 v[124:127], v[136:139], a[192:195], 0\n"
      "v_mul_f32 v152, v123, v152\n"
      "v_mul_f32 v153, v123, v153\n"
      "v_mfma_f32_16x16x32_bf16 v[124:127], v[156:159], a[196:199], v[124:127]\n"
      "v_cvt_pk_bf16_f32 v152, v152, v153\n"
      "v_mul_f32 v154, v123, v154\n"
      "v_mfma_f32_16x16x32_bf16 v[124:127], v[160:163], a[200:203], v[124:127]\n"
      "s_waitcnt lgkmcnt(2)\n"
      "v_mul_f32_e64 v155, v123, v155\n"
      "buffer_store_dwordx4 v[168:171], v25, s[20:23], s46 offen sc1\n"
      "v_mfma_f32_16x16x32_bf16 v[124:127], v[96:99], a[204:207], v[124:127]\n"
      "s_waitcnt lgkmcnt(0)\n"
      "v_cvt_pk_bf16_f32 v153, v154, v155\n"
      "v_mul_f32 v208, v123, v208\n"
      "v_mfma_f32_16x16x32_bf16 v[124:127], v[132:135], a[208:211], v[124:127]\n"
      "v_mul_f32 v209, v123, v209\n"
      "v_mul_f32_e64 v210, v123, v210\n"
      "v_mfma_f32_16x16x32_bf16 v[124:127], v[220:223], a[212:215], v[124:127]\n"
      "v_mul_f32_e64 v211, v123, v211\n"
      "v_cvt_pk_bf16_f32 v154, v208, v209\n"
      "v_mfma_f32_16x16x32_bf16 v[220:223], a[216:219], v[188:191], 0\n"
      "v_cvt_pk_bf16_f32 v155, v210, v211\n"
      "v_mul_f32 v122, s50, v122\n"
      "v_mfma_f32_16x16x32_bf16 v[220:223], a[220:223], v[172:175], v[220:223]\n"
      "ds_read_b128 v[160:163], v10 offset:1200\n"
      "v_mul_f32 v196, v122, v196\n"
      "v_mfma_f32_16x16x32_bf16 v[220:223], a[224:227], v[116:119], v[220:223]\n"
      "v_mul_f32 v197, v122, v197\n"
      "v_mul_f32 v200, v123, v200\n"
      "v_mfma_f32_16x16x32_bf16 v[208:211], a[240:243], v[188:191], 0\n"
      "v_mul_f32_e64 v201, v123, v201\n"
      "v_cvt_pk_bf16_f32 v196, v196, v197\n"
      "v_mfma_f32_16x16x32_bf16 v[208:211], a[244:247], v[172:175], v[208:211]\n"
      "v_mul_f32 v198, v122, v198\n"
      "v_mul_f32 v199, v122, v199\n"
      "v_mfma_f32_16x16x32_bf16 v[208:211], a[248:251], v[116:119], v[208:211]\n"
      "ds_read_b64 v[218:219], v10 offset:1192\n"
      "v_mul_f32 v180, v122, v180\n"
      "v_mfma_f32_16x16x32_bf16 v[214:217], v[232:235], v[188:191], 0\n"
      "ds_read_b128 v[156:159], v2 offset:9216\n"
      "v_mul_f32 v181, v122, v181\n"
      "v_mfma_f32_16x16x32_bf16 v[214:217], v[236:239], v[172:175], v[214:217]\n"
      "ds_read_b128 v[146:149], v2 offset:12288\n"
      "v_cvt_pk_bf16_f32 v200, v200, v201\n"
      "v_mfma_f32_16x16x32_bf16 v[214:217], v[240:243], v[116:119], v[214:217]\n"
      "ds_read_b128 v[188:191], v2 offset:15360\n"
      "v_mul_f32 v202, v123, v202\n"
      "v_mfma_f32_16x16x32_bf16 v[136:139], v[104:107], v[108:111], v[40:43]\n"
      "s_waitcnt lgkmcnt(4)\n"
      "v_mul_f32_e64 v203, v123, v203\n"
      "v_cvt_pk_bf16_f32 v197, v198, v199\n"
      "v_mfma_f32_16x16x32_bf16 v[172:175], v[176:179], v[108:111], v[44:47]\n"
      "v_mul_f32 v142, v122, v142\n"
      "v_mul_f32 v143, v122, v143\n"
      "v_mfma_f32_16x16x32_bf16 v[96:99], v[152:155], v[108:111], v[48:51]\n"
      "s_waitcnt lgkmcnt(3)\n"
      "v_mul_f32 v144, v122, v144\n"
      "v_mul_f32 v145, v122, v145\n"
      "v_mfma_f32_16x16x32_bf16 v[116:119], v[160:163], v[108:111], v[24:27]\n"
      "s_waitcnt lgkmcnt(2)\n"
      "v_cvt_pk_bf16_f32 v180, v180, v181\n"
      "v_mul_f32 v182, v122, v182\n"
      "v_mfma_f32_16x16x16_bf16 v[136:139], v[140:141], v[184:185], v[136:139]\n"
      "v_mul_f32 v183, v122, v183\n"
      "v_cvt_pk_bf16_f32 v201, v202, v203\n"
      "v_cvt_pk_bf16_f32 v198, v142, v143\n"
      "v_mfma_f32_16x16x16_bf16 v[172:175], v[212:213], v[184:185], v[172:175]\n"
      "v_cvt_pk_bf16_f32 v199, v144, v145\n"
      "v_cvt_pk_bf16_f32 v181, v182, v183\n"
      "v_cvt_pk_bf16_f32 v77, v150, v151\n"
      "v_mfma_f32_16x16x16_bf16 v[96:99], v[200:201], v[184:185], v[96:99]\n"
      "v_max3_f32 v122, v136, v137, v138\n"
      "v_cvt_pk_bf16_f32 v80, v128, v129\n"
      "v_cvt_pk_bf16_f32 v81, v130, v131\n"
      "v_mfma_f32_16x16x16_bf16 v[116:119], v[218:219], v[184:185], v[116:119]\n"
      "v_max3_f32 v131, v139, v172, v173\n"
      "v_max3_f32 v122, v122, v174, v175\n"
      "v_cvt_pk_bf16_f32 v84, v124, v125\n"
      "v_mfma_f32_16x16x32_bf16 v[220:223], a[228:231], v[156:159], v[220:223]\n"
      "s_waitcnt lgkmcnt(0)\n"
      "v_max3_f32 v131, v131, v96, v97\n"
      "v_max3_f32 v122, v122, v98, v99\n"
      "v_mfma_f32_16x16x32_bf16 v[220:223], a[232:235], v[146:149], v[220:223]\n"
      "v_max3_f32 v122, v122, v131, v116\n"
      "v_mov_b32 v131, v122\n"
      "v_mfma_f32_16x16x32_bf16 v[220:223], a[236:239], v[188:191], v[220:223]\n"
      "v_cvt_pk_bf16_f32 v85, v126, v127\n"
      "v_permlane16_swap_b32 v122, v131\n"
      "v_mfma_f32_16x16x32_bf16 v[208:211], a[252:255], v[156:159], v[208:211]\n"
      "v_max_f32 v122, v122, v131\n"
      "v_mov_b32 v131, v122\n"
      "v_mfma_f32_16x16x32_bf16 v[208:211], v[224:227], v[146:149], v[208:211]\n"
      "s_waitcnt vmcnt(6)\n"
      "v_or3_b32 v100, v100, v101, v102\n"
      "v_permlane32_swap_b32 v122, v131\n"
      "v_mfma_f32_16x16x32_bf16 v[208:211], v[228:231], v[188:191], v[208:211]\n"
      "v_max_f32 v122, v122, v131\n"
      "v_sub_f32 v136, v136, v122\n"
      "v_mfma_f32_16x16x32_bf16 v[214:217], v[244:247], v[156:159], v[214:217]\n"
      "ds_read_b128 v[132:135], v9 offset:4608\n"
      "v_sub_f32 v137, v137, v122\n"
      "v_mfma_f32_16x16x32_bf16 v[214:217], v[248:251], v[146:149], v[214:217]\n"
      "ds_read_b128 v[128:131], v2 offset:256\n"
      "v_sub_f32 v138, v138, v122\n"
      "v_mfma_f32_16x16x32_bf16 v[214:217], v[252:255], v[188:191], v[214:217]\n"
      "ds_read_b128 v[156:159], v2 offset:3328\n"
      "v_sub_f32 v139, v139, v122\n"
      "v_mfma_f32_16x16x32_bf16 v[188:191], v[104:107], v[204:207], v[52:55]\n"
      "ds_read_b128 v[124:127], v2 offset:6400\n"
      "v_exp_f32 v136, v136\n"
      "v_mfma_f32_16x16x32_bf16 v[142:145], v[176:179], v[204:207], v[56:59]\n"
      "v_exp_f32 v137, v137\n"
      "v_mfma_f32_16x16x32_bf16 v[184:187], v[152:155], v[204:207], v[60:63]\n"
      "v_sub_f32 v172, v172, v122\n"
      "v_sub_f32 v173, v173, v122\n"
      "v_mfma_f32_16x16x32_bf16 v[168:171], v[160:163], v[204:207], v[26:29]\n"
      "v_sub_f32 v174, v174, v122\n"
      "v_sub_f32 v175, v175, v122\n"
      "v_mfma_f32_16x16x16_bf16 v[188:191], v[140:141], v[120:121], v[188:191]\n"
      "v_exp_f32 v138, v138\n"
      "v_exp_f32 v139, v139\n"
      "v_mfma_f32_16x16x16_bf16 v[142:145], v[212:213], v[120:121], v[142:145]\n"
      "v_exp_f32 v172, v172\n"
      "v_exp_f32 v173, v173\n"
      "v_mfma_f32_16x16x16_bf16 v[184:187], v[200:201], v[120:121], v[184:187]\n"
      "v_exp_f32 v174, v174\n"
      "v_exp_f32 v175, v175\n"
      "v_mfma_f32_16x16x16_bf16 v[168:171], v[218:219], v[120:121], v[168:171]\n"
      "v_cvt_pk_bf16_f32 v136, v136, v137\n"
      "v_cvt_pk_bf16_f32 v137, v138, v139\n"
      "v_cvt_pk_bf16_f32 v138, v172, v173\n"
      "s_waitcnt lgkmcnt(0)\n"
      "v_mfma_f32_16x16x32_bf16 v[148:151], a[216:219], v[128:131], 0\n"
      "v_cvt_pk_bf16_f32 v139, v174, v175\n"
      "v_sub_f32 v96, v96, v122\n"
      "v_mfma_f32_16x16x32_bf16 v[148:151], a[220:223], v[156:159], v[148:151]\n"
      "ds_read_u16 v78, v11 offset:1168\n"
      "v_sub_f32 v97, v97, v122\n"
      "v_mfma_f32_16x16x32_bf16 v[148:151], a[224:227], v[124:127], v[148:151]\n"
      "v_sub_f32 v98, v98, v122\n"
      "v_sub_f32 v99, v99, v122\n"
      "v_mfma_f32_16x16x32_bf16 v[170:173], a[240:243], v[128:131], 0\n"
      "ds_read_u16 v82, v11 offset:1200\n"
      "v_sub_f32 v116, v116, v122\n"
      "v_mfma_f32_16x16x32_bf16 v[170:173], a[244:247], v[156:159], v[170:173]\n"
      "v_exp_f32 v96, v96\n"
      "v_mfma_f32_16x16x32_bf16 v[170:173], a[248:251], v[124:127], v[170:173]\n"
      "ds_read_u16 v86, v11 offset:1232\n"
      "v_exp_f32 v97, v97\n"
      "v_mfma_f32_16x16x32_bf16 v[120:123], v[232:235], v[128:131], 0\n"
      "v_exp_f32 v98, v98\n"
      "v_mfma_f32_16x16x32_bf16 v[120:123], v[236:239], v[156:159], v[120:123]\n"
      "s_waitcnt lgkmcnt(2)\n"
      "v_exp_f32 v99, v99\n"
      "v_mfma_f32_16x16x32_bf16 v[120:123], v[240:243], v[124:127], v[120:123]\n"
      "v_exp_f32 v116, v116\n"
      "v_mfma_f32_16x16x32_bf16 v[108:111], v[32:35], v[136:139], 0\n"
      "s_waitcnt lgkmcnt(1)\n"
      "v_cvt_pk_bf16_f32 v88, v96, v97\n"
      "v_cvt_pk_bf16_f32 v89, v98, v99\n"
      "v_cvt_pk_bf16_f32 v90, v116, 0\n"
      "v_max3_f32 v131, v188, v189, v190\n"
      "v_max3_f32 v116, v191, v142, v143\n"
      "v_mfma_f32_16x16x32_bf16 v[108:111], v[32:35], v[88:91], v[108:111]\n"
      "s_waitcnt lgkmcnt(0)\n"
      "v_max3_f32 v131, v131, v144, v145\n"
      "v_max3_f32 v116, v116, v184, v185\n"
      "v_mfma_f32_16x16x32_bf16 v[156:159], v[112:115], v[136:139], 0\n"
      "v_max3_f32 v131, v131, v186, v187\n"
      "v_max3_f32 v131, v131, v116, v168\n"
      "v_mfma_f32_16x16x32_bf16 v[156:159], v[76:79], v[88:91], v[156:159]\n"
      "v_mov_b32_e64 v130, v131\n"
      "v_fma_f32 v174, v220, v220, s52\n"
      "v_mfma_f32_16x16x32_bf16 v[116:119], v[192:195], v[136:139], 0\n"
      "v_permlane16_swap_b32 v131, v130\n"
      "v_mfma_f32_16x16x32_bf16 v[116:119], v[80:83], v[88:91], v[116:119]\n"
      "ds_read_b128 v[96:99], v2 offset:9472\n"
      "v_max_f32 v131, v131, v130\n"
      "v_mfma_f32_16x16x32_bf16 v[204:207], v[164:167], v[136:139], 0\n"
      "ds_read_b128 v[124:127], v2 offset:12544\n"
      "v_mov_b32_e64 v130, v131\n"
      "v_mfma_f32_16x16x32_bf16 v[204:207], v[84:87], v[88:91], v[204:207]\n"
      "ds_read_b128 v[136:139], v2 offset:15616\n"
      "v_permlane32_swap_b32 v131, v130\n"
      "v_max_f32 v131, v131, v130\n"
      "v_sub_f32 v188, v188, v131\n"
      "v_sub_f32 v189, v189, v131\n"
      "v_sub_f32 v190, v190, v131\n"
      "v_sub_f32 v191, v191, v131\n"
      "v_exp_f32 v188, v188\n"
      "v_exp_f32 v189, v189\n"
      "v_sub_f32 v142, v142, v131\n"
      "v_sub_f32 v143, v143, v131\n"
      "v_sub_f32 v144, v144, v131\n"
      "v_sub_f32 v145, v145, v131\n"
      "v_exp_f32 v190, v190\n"
      "v_exp_f32 v191, v191\n"
      "v_exp_f32 v142, v142\n"
      "v_exp_f32 v143, v143\n"
      "v_exp_f32 v144, v144\n"
      "v_exp_f32 v145, v145\n"
      "v_cvt_pk_bf16_f32 v188, v188, v189\n"
      "v_cvt_pk_bf16_f32 v189, v190, v191\n"
      "v_cvt_pk_bf16_f32 v190, v142, v143\n"
      "v_cvt_pk_bf16_f32 v191, v144, v145\n"
      "v_mfma_f32_16x16x32_bf16 v[142:145], v[104:107], v[196:199], v[64:67]\n"
      "v_sub_f32 v184, v184, v131\n"
      "v_sub_f32 v185, v185, v131\n"
      "v_sub_f32 v186, v186, v131\n"
      "v_sub_f32 v187, v187, v131\n"
      "v_sub_f32 v168, v168, v131\n"
      "v_exp_f32 v184, v184\n"
      "v_exp_f32 v185, v185\n"
      "v_exp_f32 v186, v186\n"
      "v_exp_f32_e64 v187, v187\n"
      "v_exp_f32 v168, v168\n"
      "v_cvt_pk_bf16_f32 v92, v184, v185\n"
      "v_cvt_pk_bf16_f32 v93, v186, v187\n"
      "v_mfma_f32_16x16x32_bf16 v[128:131], v[176:179], v[196:199], v[68:71]\n"
      "s_waitcnt lgkmcnt(0)\n"
      "v_cvt_pk_bf16_f32 v94, v168, 0\n"
      "v_fmac_f32 v174, v221, v221\n"
      "v_fmac_f32 v174, v222, v222\n"
      "v_fmac_f32 v174, v223, v223\n"
      "v_fmac_f32 v174, v208, v208\n"
      "v_fmac_f32 v174, v209, v209\n"
      "v_fmac_f32 v174, v210, v210\n"
      "v_fmac_f32 v174, v211, v211\n"
      "v_fmac_f32 v174, v214, v214\n"
      "v_fmac_f32 v174, v215, v215\n"
      "v_fmac_f32 v174, v216, v216\n"
      "v_fmac_f32 v174, v217, v217\n"
      "v_rcp_f32_e64 v108, v108\n"
      "s_waitcnt vmcnt(5)\n"
      "v_or3_b32 v100, v100, v103, v39\n"
      "v_mul_f32 v156, v108, v156\n"
      "v_mul_f32 v157, v108, v157\n"
      "v_cvt_pk_bf16_f32 v156, v156, v157\n"
      "v_mul_f32 v158, v108, v158\n"
      "v_mul_f32 v159, v108, v159\n"
      "v_cvt_pk_bf16_f32 v157, v158, v159\n"
      "v_mul_f32 v116, v108, v116\n"
      "v_mul_f32 v117, v108, v117\n"
      "v_mul_f32 v118, v108, v118\n"
      "v_mul_f32 v119, v108, v119\n"
      "v_cvt_pk_bf16_f32 v158, v116, v117\n"
      "v_mfma_f32_16x16x32_bf16 v[184:187], v[152:155], v[196:199], v[72:75]\n"
      "v_cvt_pk_bf16_f32 v159, v118, v119\n"
      "v_mul_f32_e64 v204, v108, v204\n"
      "v_mfma_f32_16x16x32_bf16 v[116:119], v[160:163], v[196:199], v[28:31]\n"
      "ds_write_b128 v3, v[156:159] offset:18432\n"
      "v_mfma_f32_16x16x16_bf16 v[142:145], v[140:141], v[180:181], v[142:145]\n"
      "v_mul_f32_e64 v205, v108, v205\n"
      "v_cvt_pk_bf16_f32 v204, v204, v205\n"
      "v_mul_f32_e64 v206, v108, v206\n"
      "v_mfma_f32_16x16x16_bf16 v[128:131], v[212:213], v[180:181], v[128:131]\n"
      "v_mul_f32_e64 v207, v108, v207\n"
      "v_cvt_pk_bf16_f32 v205, v206, v207\n"
      "v_and_b32 v100, 0x7fff7fff, v100\n"
      "v_mfma_f32_16x16x16_bf16 v[184:187], v[200:201], v[180:181], v[184:187]\n"
      "v_max3_f32 v88, v142, v143, v144\n"
      "v_readlane_b32 s45, v37, s43\n"
      "v_readlane_b32 s46, v37, s42\n"
      "v_mfma_f32_16x16x16_bf16 v[116:119], v[218:219], v[180:181], v[116:119]\n"
      "v_max3_f32 v197, v145, v128, v129\n"
      "v_max3_f32 v88, v88, v130, v131\n"
      "v_or3_b32 v132, v132, v133, v134\n"
      "v_mfma_f32_16x16x32_bf16 v[148:151], a[228:231], v[96:99], v[148:151]\n"
      "ds_write_b64 v4, v[204:205] offset:18432\n"
      "v_mfma_f32_16x16x32_bf16 v[148:151], a[232:235], v[124:127], v[148:151]\n"
      "s_or_b32 s95, s45, s46\n"
      "v_max3_f32 v197, v197, v184, v185\n"
      "v_max3_f32 v88, v88, v186, v187\n"
      "v_mfma_f32_16x16x32_bf16 v[148:151], a[236:239], v[136:139], v[148:151]\n"
      "s_or_b32 s95, s95, s48\n"
      "v_max3_f32 v88, v88, v197, v116\n"
      "v_mov_b32_e64 v89, v88\n"
      "v_mfma_f32_16x16x32_bf16 v[170:173], a[252:255], v[96:99], v[170:173]\n"
      "ds_write_b32 v31, v100 offset:5632\n"
      "v_mfma_f32_16x16x32_bf16 v[170:173], v[224:227], v[124:127], v[170:173]\n"
      "s_cmp_ge_u32 s95, 0x40000000\n"
      "s_cselect_b32 s94, -1, -2\n"
      "v_fma_f32 v90, v148, v148, s52\n"
      "v_mfma_f32_16x16x32_bf16 v[170:173], v[228:231], v[136:139], v[170:173]\n"
      "s_cmp_gt_u32 s42, s44\n"
      "s_cselect_b32 s95, s94, s95\n"
      "v_fmac_f32 v90, v149, v149\n"
      "v_mfma_f32_16x16x32_bf16 v[120:123], v[244:247], v[96:99], v[120:123]\n"
      "ds_read_b128 v[196:199], v10 offset:1168\n"
      "v_fmac_f32 v90, v150, v150\n"
      "v_mfma_f32_16x16x32_bf16 v[120:123], v[248:251], v[124:127], v[120:123]\n"
      "s_add_u32 s42, s42, 1\n"
      "v_fmac_f32 v90, v151, v151\n"
      "v_fmac_f32 v90, v170, v170\n"
      "v_mfma_f32_16x16x32_bf16 v[120:123], v[252:255], v[136:139], v[120:123]\n"
      "v_fmac_f32 v90, v171, v171\n"
      "v_fmac_f32 v90, v172, v172\n"
      "v_mfma_f32_16x16x32_bf16 v[156:159], v[32:35], v[188:191], 0\n"
      "v_fmac_f32 v90, v173, v173\n"
      "v_permlane16_swap_b32_e64 v88, v89\n"
      "v_mfma_f32_16x16x32_bf16 v[156:159], v[32:35], v[92:95], v[156:159]\n"
      "ds_read_b64 v[202:203], v10 offset:1184\n"
      "v_max_f32 v88, v88, v89\n"
      "v_mfma_f32_16x16x32_bf16 v[100:103], v[112:115], v[188:191], 0\n"
      "v_fmac_f32 v90, v120, v120\n"
      "v_fmac_f32_e64 v90, v121, v121\n"
      "v_mfma_f32_16x16x32_bf16 v[100:103], v[76:79], v[92:95], v[100:103]\n"
      "s_waitcnt lgkmcnt(1)\n"
      "v_fmac_f32 v90, v122, v122\n"
      "v_fmac_f32 v90, v123, v123\n"
      "v_mfma_f32_16x16x32_bf16 v[180:183], v[192:195], v[188:191], 0\n"
      "v_mov_b32 v89, v88\n"
      "v_permlane16_swap_b32 v174, v90\n"
      "v_mfma_f32_16x16x32_bf16 v[180:183], v[80:83], v[92:95], v[180:183]\n"
      "ds_read_b128 v[204:207], v2 offset:512\n"
      "v_add_f32 v174, v174, v90\n"
      "v_mfma_f32_16x16x32_bf16 v[124:127], v[164:167], v[188:191], 0\n"
      "ds_read_b128 v[188:191], v2 offset:3584\n"
      "v_rcp_f32 v156, v156\n"
      "v_mfma_f32_16x16x32_bf16 v[124:127], v[84:87], v[92:95], v[124:127]\n"
      "ds_read_b128 v[96:99], v2 offset:6656\n"
      "v_permlane32_swap_b32 v88, v89\n"
      "v_mfma_f32_16x16x32_bf16 v[108:111], v[196:199], v[104:107], v[30:33]\n"
      "s_waitcnt lgkmcnt(3)\n"
      "v_mul_f32 v100, v156, v100\n"
      "v_mul_f32 v101, v156, v101\n"
      "v_mfma_f32_16x16x32_bf16 v[136:139], v[196:199], v[176:179], v[30:33]\n"
      "v_max_f32 v88, v88, v89\n"
      "v_cvt_pk_bf16_f32 v100, v100, v101\n"
      "v_mfma_f32_16x16x32_bf16 v[104:107], v[196:199], v[152:155], v[30:33]\n"
      "v_mul_f32 v102, v156, v102\n"
      "v_mul_f32 v103, v156, v103\n"
      "v_mfma_f32_16x16x32_bf16 v[176:179], v[196:199], v[160:163], v[30:33]\n"
      "s_waitcnt lgkmcnt(2)\n"
      "v_sub_f32 v142, v142, v88\n"
      "v_sub_f32 v143, v143, v88\n"
      "v_mfma_f32_16x16x16_bf16 v[108:111], v[202:203], v[140:141], v[108:111]\n"
      "v_cvt_pk_bf16_f32 v101, v102, v103\n"
      "v_mul_f32 v180, v156, v180\n"
      "v_mul_f32 v181, v156, v181\n"
      "v_mfma_f32_16x16x16_bf16 v[136:139], v[202:203], v[212:213], v[136:139]\n"
      "v_mul_f32 v182, v156, v182\n"
      "v_mul_f32 v183, v156, v183\n"
      "v_sub_f32 v144, v144, v88\n"
      "v_mfma_f32_16x16x16_bf16 v[104:107], v[202:203], v[200:201], v[104:107]\n"
      "v_sub_f32 v145, v145, v88\n"
      "v_exp_f32 v142, v142\n"
      "v_mfma_f32_16x16x16_bf16 v[176:179], v[202:203], v[218:219], v[176:179]\n"
      "v_exp_f32 v143, v143\n"
      "v_cndmask_b32 v108, v108, v136, s[58:59]\n"
      "v_mfma_f32_16x16x32_bf16 v[136:139], a[216:219], v[204:207], 0\n"
      "s_waitcnt lgkmcnt(0)\n"
      "v_cvt_pk_bf16_f32 v102, v180, v181\n"
      "v_cndmask_b32 v108, v108, v104, s[60:61]\n"
      "v_mfma_f32_16x16x32_bf16 v[136:139], a[220:223], v[188:191], v[136:139]\n"
      "ds_read_b128 v[104:107], v2 offset:9728\n"
      "v_cndmask_b32 v108, v108, v176, s[62:63]\n"
      "v_mfma_f32_16x16x32_bf16 v[136:139], a[224:227], v[96:99], v[136:139]\n"
      "ds_read_b128 v[158:161], v2 offset:12800\n"
      "v_max_f32_dpp v89, v108, v108 row_ror:8 row_mask:0xf bank_mask:0xf\n"
      "v_mfma_f32_16x16x32_bf16 v[196:199], a[240:243], v[204:207], 0\n"
      "ds_read_b128 v[200:203], v2 offset:15872\n"
      "v_max_f32_dpp v89, v89, v89 row_ror:4 row_mask:0xf bank_mask:0xf\n"
      "v_mfma_f32_16x16x32_bf16 v[196:199], a[244:247], v[188:191], v[196:199]\n"
      "v_cvt_pk_bf16_f32 v103, v182, v183\n"
      "v_max_f32_dpp v89, v89, v89 row_ror:2 row_mask:0xf bank_mask:0xf\n"
      "v_mfma_f32_16x16x32_bf16 v[196:199], a[248:251], v[96:99], v[196:199]\n"
      "ds_write_b128 v3, v[100:103] offset:18688\n"
      "v_mfma_f32_16x16x32_bf16 v[178:181], v[232:235], v[204:207], 0\n"
      "s_waitcnt lgkmcnt(3)\n"
      "v_max_f32_dpp v89, v89, v89 row_ror:1 row_mask:0xf bank_mask:0xf\n"
      "v_sub_f32 v128, v128, v88\n"
      "v_mfma_f32_16x16x32_bf16 v[178:181], v[236:239], v[188:191], v[178:181]\n"
      "s_waitcnt lgkmcnt(2)\n"
      "v_max_f32_dpp v89, v89, v89 row_bcast:15 row_mask:0xa bank_mask:0xf\n"
      "v_sub_f32 v129, v129, v88\n"
      "v_mfma_f32_16x16x32_bf16 v[178:181], v[240:243], v[96:99], v[178:181]\n"
      "s_waitcnt lgkmcnt(1)\n"
      "v_max_f32_dpp v89, v89, v89 row_bcast:31 row_mask:0xc bank_mask:0xf\n"
      "v_sub_f32 v130, v130, v88\n"
      "v_mfma_f32_16x16x32_bf16 v[136:139], a[228:231], v[104:107], v[136:139]\n"
      "v_readlane_b32 s72, v89, 63\n"
      "v_sub_f32 v131, v131, v88\n"
      "v_mfma_f32_16x16x32_bf16 v[136:139], a[232:235], v[158:161], v[136:139]\n"
      "v_subrev_f32 v108, s72, v108\n"
      "v_exp_f32 v144, v144\n"
      "v_mfma_f32_16x16x32_bf16 v[136:139], a[236:239], v[200:203], v[136:139]\n"
      "v_exp_f32 v145, v145\n"
      "v_mfma_f32_16x16x32_bf16 v[196:199], a[252:255], v[104:107], v[196:199]\n"
      "v_mul_f32 v124, v156, v124\n"
      "v_mul_f32 v125, v156, v125\n"
      "v_mfma_f32_16x16x32_bf16 v[196:199], v[224:227], v[158:161], v[196:199]\n"
      "v_exp_f32_e64 v108, v108\n"
      "v_mfma_f32_16x16x32_bf16 v[196:199], v[228:231], v[200:203], v[196:199]\n"
      "v_exp_f32 v128, v128\n"
      "v_mfma_f32_16x16x32_bf16 v[178:181], v[244:247], v[104:107], v[178:181]\n"
      "v_fma_f32 v98, v136, v136, s52\n"
      "v_fmac_f32 v98, v137, v137\n"
      "v_mfma_f32_16x16x32_bf16 v[178:181], v[248:251], v[158:161], v[178:181]\n"
      "v_fmac_f32 v98, v138, v138\n"
      "v_fmac_f32 v98, v139, v139\n"
      "v_mfma_f32_16x16x32_bf16 v[178:181], v[252:255], v[200:203], v[178:181]\n"
      "v_fmac_f32 v98, v196, v196\n"
      "v_fmac_f32 v98, v197, v197\n"
      "v_fmac_f32 v98, v198, v198\n"
      "v_fmac_f32 v98, v199, v199\n"
      "v_exp_f32 v129, v129\n"
      "v_exp_f32 v130, v130\n"
      "v_exp_f32 v131, v131\n"
      "v_cvt_pk_bf16_f32 v124, v124, v125\n"
      "v_fmac_f32 v98, v178, v178\n"
      "v_fmac_f32 v98, v179, v179\n"
      "v_fmac_f32 v98, v180, v180\n"
      "v_fmac_f32 v98, v181, v181\n"
      "v_mov_b32 v90, v98\n"
      "v_mul_f32 v126, v156, v126\n"
      "v_mul_f32 v127, v156, v127\n"
      "v_permlane16_swap_b32 v98, v90\n"
      "v_add_f32 v98, v98, v90\n"
      "v_cvt_pk_bf16_f32 v142, v142, v143\n"
      "v_cvt_pk_bf16_f32 v125, v126, v127\n"
      "v_permlane32_swap_b32_e64 v174, v98\n"
      "v_cvt_pk_bf16_f32 v143, v144, v145\n"
      "v_cvt_pk_bf16_f32 v108, v108, v108\n"
      "v_add_f32 v174, v174, v98\n"
      "v_cvt_pk_bf16_f32 v144, v128, v129\n"
      "v_cvt_pk_bf16_f32 v145, v130, v131\n"
      "v_sub_f32 v184, v184, v88\n"
      "v_sub_f32 v185, v185, v88\n"
      "v_mfma_f32_16x16x32_bf16 v[98:101], v[32:35], v[142:145], 0\n"
      "ds_write_b64 v4, v[124:125] offset:18688\n"
      "v_sub_f32 v186, v186, v88\n"
      "v_sub_f32 v187, v187, v88\n"
      "v_sub_f32 v116, v116, v88\n"
      "v_exp_f32 v184, v184\n"
      "v_exp_f32 v185, v185\n"
      "v_exp_f32 v186, v186\n"
      "v_exp_f32 v187, v187\n"
      "v_exp_f32_e64 v116, v116\n"
      "v_cvt_pk_bf16_f32 v88, v184, v185\n"
      "v_cvt_pk_bf16_f32 v89, v186, v187\n"
      "v_cvt_pk_bf16_f32 v90, v116, 0\n"
      "v_or_b32_e64 v132, v132, v135\n"
      "v_cmp_ne_u32 s[56:57], 0, v132\n"
      "v_mfma_f32_16x16x32_bf16 v[98:101], v[32:35], v[88:91], v[98:101]\n"
      "ds_write_b16 v13, v108\n"
      "v_mfma_f32_16x16x32_bf16 v[188:191], v[112:115], v[142:145], 0\n"
      "ds_read_b128 v[104:107], v14\n"
      "v_readlane_b32 s47, v38, s43\n"
      "v_mfma_f32_16x16x32_bf16 v[188:191], v[76:79], v[88:91], v[188:191]\n"
      "ds_write_b32 v31, v174 offset:1024\n"
      "v_mfma_f32_16x16x32_bf16 v[184:187], v[192:195], v[142:145], 0\n"
      "ds_read_b128 v[156:159], v14 offset:64\n"
      "v_rcp_f32_e64 v98, v98\n"
      "v_mfma_f32_16x16x32_bf16 v[184:187], v[80:83], v[88:91], v[184:187]\n"
      "s_cmp_ge_u32 s95, 0x40000000\n"
      "s_cselect_b64 vcc, -1, s[56:57]\n"
      "v_add_u32 v10, 2336, v10\n"
      "v_mfma_f32_16x16x32_bf16 v[204:207], v[164:167], v[142:145], 0\n"
      "s_and_b32 s47, s47, -4\n"
      "v_mul_f32 v188, v98, v188\n"
      "v_mul_f32_e64 v189, v98, v189\n"
      "v_mfma_f32_16x16x32_bf16 v[204:207], v[84:87], v[88:91], v[204:207]\n"
      "s_add_u32 s43, s43, 1\n"
      "v_cvt_pk_bf16_f32 v188, v188, v189\n"
      "v_mul_f32 v190, v98, v190\n"
      "v_mul_f32_e64 v191, v98, v191\n"
      "v_cvt_pk_bf16_f32 v189, v190, v191\n"
      "v_mul_f32 v184, v98, v184\n"
      "s_waitcnt lgkmcnt(2)\n"
      "v_mfma_f32_16x16x32_bf16 v[100:103], v[104:107], v[32:35], 0\n"
      "v_mul_f32 v185, v98, v185\n"
      "v_mul_f32 v186, v98, v186\n"
      "v_mul_f32 v187, v98, v187\n"
      "v_cvt_pk_bf16_f32 v190, v184, v185\n"
      "v_cvt_pk_bf16_f32 v191, v186, v187\n"
      "s_waitcnt lgkmcnt(0)\n"
      "v_mfma_f32_16x16x32_bf16 v[100:103], v[156:159], v[32:35], v[100:103]\n"
      "ds_write_b128 v3, v[188:191] offset:18944\n"
      "v_mul_f32 v204, v98, v204\n"
      "v_mul_f32 v205, v98, v205\n"
      "v_cvt_pk_bf16_f32 v204, v204, v205\n"
      "v_mul_f32_e64 v206, v98, v206\n"
      "v_mul_f32 v207, v98, v207\n"
      "v_cvt_pk_bf16_f32 v205, v206, v207\n"
      "v_add_u32 v11, 2336, v11\n"
      "ds_write_b64 v4, v[204:205] offset:18944\n"
      "s_waitcnt lgkmcnt(0)\n"
      "s_barrier\n"
      "s_cbranch_vccnz L_disp0_%=\n"
      "L_max0_%=:\n"
      "v_mfma_f32_16x16x32_bf16 v[140:143], v[104:107], v[112:115], 0\n"
      "ds_read_b128 v[124:127], v0\n"
      "ds_read_b128 v[108:111], v9 offset:1024\n"
      "v_mfma_f32_16x16x32_bf16 v[140:143], v[156:159], v[76:79], v[140:143]\n"
      "ds_read_b128 v[182:185], v1\n"
      "s_add_u32 m0, s64, 18432\n"
      "v_mfma_f32_16x16x32_bf16 v[112:115], v[104:107], v[192:195], 0\n"
      "ds_read_b128 v[160:163], v0 offset:6144\n"
      "buffer_load_dwordx4 v16, s[16:19], s45 offen  lds\n"
      "v_mfma_f32_16x16x32_bf16 v[112:115], v[156:159], v[80:83], v[112:115]\n"
      "s_add_u32 m0, s64, 22528\n"
      "v_mfma_f32_16x16x32_bf16 v[190:193], v[104:107], v[164:167], 0\n"
      "v_mfma_f32_16x16x32_bf16 v[190:193], v[156:159], v[84:87], v[190:193]\n"
      "s_waitcnt lgkmcnt(3)\n"
      "v_mov_b32_dpp v100, v100 quad_perm:[0,0,0,0] row_mask:0xf bank_mask:0xf\n"
      "v_rcp_f32 v100, v100\n"
      "v_mfma_f32_16x16x32_bf16 v[104:107], a[0:3], v[124:127], 0\n"
      "buffer_load_dwordx4 v17, s[16:19], s45 offen  lds\n"
      "v_mfma_f32_16x16x32_bf16 v[156:159], a[24:27], v[124:127], 0\n"
      "s_add_u32 m0, s64, 26624\n"
      "v_cndmask_b32 v140, v140, v112, s[58:59]\n"
      "v_cndmask_b32 v140, v140, v190, s[60:61]\n"
      "v_mfma_f32_16x16x32_bf16 v[142:145], a[48:51], v[124:127], 0\n"
      "s_waitcnt lgkmcnt(0)\n"
      "v_mul_f32 v140, v100, v140\n"
      "v_cvt_pk_bf16_f32 v140, v140, v140\n"
      "v_mfma_f32_16x16x32_bf16 v[100:103], a[72:75], v[124:127], 0\n"
      "ds_write_b16 v12, v140 offset:16\n"
      "v_mfma_f32_16x16x32_bf16 v[188:191], a[96:99], v[124:127], 0\n"
      "buffer_load_dwordx4 v18, s[16:19], s45 offen  lds\n"
      "v_mfma_f32_16x16x32_bf16 v[132:135], a[120:123], v[124:127], 0\n"
      "s_add_u32 m0, s64, 30720\n"
      "v_add_f32 v108, v108, v109\n"
      "v_add_f32 v110, v110, v111\n"
      "v_mfma_f32_16x16x32_bf16 v[104:107], a[4:7], v[182:185], v[104:107]\n"
      "v_add_f32 v108, v108, v110\n"
      "v_rsq_f32 v108, v108\n"
      "v_mfma_f32_16x16x32_bf16 v[104:107], a[8:11], v[160:163], v[104:107]\n"
      "ds_read_b128 v[204:207], v1 offset:6144\n"
      "v_mul_f32 v108, 0x415db3d7, v108\n"
      "v_mfma_f32_16x16x32_bf16 v[156:159], a[28:31], v[182:185], v[156:159]\n"
      "ds_read_b128 v[152:155], v0 offset:12288\n"
      "v_mov_b32 v110, v108\n"
      "v_mfma_f32_16x16x32_bf16 v[156:159], a[32:35], v[160:163], v[156:159]\n"
      "ds_read_b128 v[200:203], v1 offset:12288\n"
      "v_permlane32_swap_b32 v108, v110\n"
      "v_mfma_f32_16x16x32_bf16 v[142:145], a[52:55], v[182:185], v[142:145]\n"
      "buffer_load_dwordx4 v19, s[16:19], s45 offen  lds\n"
      "v_mfma_f32_16x16x32_bf16 v[142:145], a[56:59], v[160:163], v[142:145]\n"
      "s_add_u32 m0, s65, 34816\n"
      "v_mov_b32 v109, v108\n"
      "v_mul_f32 v136, v110, v136\n"
      "v_mfma_f32_16x16x32_bf16 v[100:103], a[76:79], v[182:185], v[100:103]\n"
      "v_permlane16_swap_b32 v108, v109\n"
      "v_mfma_f32_16x16x32_bf16 v[100:103], a[80:83], v[160:163], v[100:103]\n"
      "s_waitcnt lgkmcnt(2)\n"
      "v_mul_f32 v220, v108, v220\n"
      "v_mul_f32 v221, v108, v221\n"
      "v_mfma_f32_16x16x32_bf16 v[188:191], a[100:103], v[182:185], v[188:191]\n"
      "buffer_load_dwordx4 v15, s[16:19], s45 offen  lds\n"
      "v_mfma_f32_16x16x32_bf16 v[188:191], a[104:107], v[160:163], v[188:191]\n"
      "s_waitcnt lgkmcnt(0)\n"
      "v_cvt_pk_bf16_f32 v220, v220, v221\n"
      "v_mul_f32 v222, v108, v222\n"
      "v_mfma_f32_16x16x32_bf16 v[132:135], a[124:127], v[182:185], v[132:135]\n"
      "v_mul_f32 v223, v108, v223\n"
      "v_mul_f32 v214, v108, v214\n"
      "v_mfma_f32_16x16x32_bf16 v[132:135], a[128:131], v[160:163], v[132:135]\n"
      "v_mul_f32_e64 v215, v108, v215\n"
      "v_cvt_pk_bf16_f32 v221, v222, v223\n"
      "v_mfma_f32_16x16x32_bf16 v[104:107], a[12:15], v[204:207], v[104:107]\n"
      "buffer_load_dwordx4 v[116:119], v36, s[24:27], s47 offen\n"
      "v_mfma_f32_16x16x32_bf16 v[104:107], a[16:19], v[152:155], v[104:107]\n"
      "v_mul_f32 v208, v108, v208\n"
      "v_mul_f32 v209, v108, v209\n"
      "v_mfma_f32_16x16x32_bf16 v[104:107], a[20:23], v[200:203], v[104:107]\n"
      "v_mul_f32 v210, v108, v210\n"
      "v_mul_f32 v211, v108, v211\n"
      "v_mfma_f32_16x16x32_bf16 v[156:159], a[36:39], v[204:207], v[156:159]\n"
      "v_cvt_pk_bf16_f32 v214, v214, v215\n"
      "v_mul_f32_e64 v216, v108, v216\n"
      "v_mfma_f32_16x16x32_bf16 v[156:159], a[40:43], v[152:155], v[156:159]\n"
      "buffer_load_dword v39, v29, s[24:27], s47 offen\n"
      "v_mfma_f32_16x16x32_bf16 v[156:159], a[44:47], v[200:203], v[156:159]\n"
      "v_mul_f32_e64 v217, v108, v217\n"
      "v_cvt_pk_bf16_f32 v222, v208, v209\n"
      "v_mfma_f32_16x16x32_bf16 v[142:145], a[60:63], v[204:207], v[142:145]\n"
      "v_cvt_pk_bf16_f32 v223, v210, v211\n"
      "v_cvt_pk_bf16_f32 v215, v216, v217\n"
      "v_mfma_f32_16x16x32_bf16 v[142:145], a[64:67], v[152:155], v[142:145]\n"
      "ds_write_b128 v5, v[220:223]\n"
      "v_mfma_f32_16x16x32_bf16 v[142:145], a[68:71], v[200:203], v[142:145]\n"
      "v_mul_f32 v148, v109, v148\n"
      "v_mul_f32 v149, v109, v149\n"
      "v_mfma_f32_16x16x32_bf16 v[100:103], a[84:87], v[204:207], v[100:103]\n"
      "v_cvt_pk_bf16_f32 v148, v148, v149\n"
      "v_mul_f32 v150, v109, v150\n"
      "v_mfma_f32_16x16x32_bf16 v[100:103], a[88:91], v[152:155], v[100:103]\n"
      "ds_write_b64 v6, v[214:215]\n"
      "v_mfma_f32_16x16x32_bf16 v[100:103], a[92:95], v[200:203], v[100:103]\n"
      "v_mul_f32 v151, v109, v151\n"
      "v_mul_f32 v120, v109, v120\n"
      "v_mfma_f32_16x16x32_bf16 v[188:191], a[108:111], v[204:207], v[188:191]\n"
      "v_mul_f32 v121, v109, v121\n"
      "v_cvt_pk_bf16_f32 v149, v150, v151\n"
      "v_mfma_f32_16x16x32_bf16 v[188:191], a[112:115], v[152:155], v[188:191]\n"
      "v_mul_f32 v170, v109, v170\n"
      "v_mul_f32 v171, v109, v171\n"
      "v_mfma_f32_16x16x32_bf16 v[188:191], a[116:119], v[200:203], v[188:191]\n"
      "v_mul_f32 v172, v109, v172\n"
      "v_mul_f32 v173, v109, v173\n"
      "v_mfma_f32_16x16x32_bf16 v[132:135], a[132:135], v[204:207], v[132:135]\n"
      "v_cvt_pk_bf16_f32 v120, v120, v121\n"
      "v_mul_f32 v122, v109, v122\n"
      "v_mfma_f32_16x16x32_bf16 v[132:135], a[136:139], v[152:155], v[132:135]\n"
      "v_mul_f32 v123, v109, v123\n"
      "v_cvt_pk_bf16_f32 v150, v170, v171\n"
      "v_mfma_f32_16x16x32_bf16 v[132:135], a[140:143], v[200:203], v[132:135]\n"
      "v_cvt_pk_bf16_f32 v151, v172, v173\n"
      "v_cvt_pk_bf16_f32 v121, v122, v123\n"
      "v_mfma_f32_16x16x32_bf16 v[112:115], v[124:127], a[144:147], 0\n"
      "ds_write_b128 v5, v[148:151] offset:6144\n"
      "v_mfma_f32_16x16x32_bf16 v[112:115], v[182:185], a[148:151], v[112:115]\n"
      "v_mul_f32_e64 v137, v110, v137\n"
      "v_cvt_pk_bf16_f32 v136, v136, v137\n"
      "v_mfma_f32_16x16x32_bf16 v[112:115], v[160:163], a[152:155], v[112:115]\n"
      "v_mul_f32 v138, v110, v138\n"
      "v_mul_f32 v139, v110, v139\n"
      "v_mfma_f32_16x16x32_bf16 v[112:115], v[204:207], a[156:159], v[112:115]\n"
      "ds_write_b64 v6, v[120:121] offset:6144\n"
      "v_mfma_f32_16x16x32_bf16 v[112:115], v[152:155], a[160:163], v[112:115]\n"
      "v_mul_f32 v178, v110, v178\n"
      "v_mul_f32 v179, v110, v179\n"
      "v_mfma_f32_16x16x32_bf16 v[112:115], v[200:203], a[164:167], v[112:115]\n"
      "v_cvt_pk_bf16_f32 v137, v138, v139\n"
      "v_mul_f32 v196, v110, v196\n"
      "v_mfma_f32_16x16x32_bf16 v[192:195], v[124:127], a[168:171], 0\n"
      "v_mul_f32 v197, v110, v197\n"
      "v_mul_f32_e64 v198, v110, v198\n"
      "v_mfma_f32_16x16x32_bf16 v[192:195], v[182:185], a[172:175], v[192:195]\n"
      "ds_read_b128 v[214:217], v0 offset:2048\n"
      "v_mul_f32_e64 v199, v110, v199\n"
      "v_mfma_f32_16x16x32_bf16 v[192:195], v[160:163], a[176:179], v[192:195]\n"
      "ds_read_b128 v[208:211], v1 offset:2048\n"
      "v_cvt_pk_bf16_f32 v178, v178, v179\n"
      "v_mfma_f32_16x16x32_bf16 v[192:195], v[204:207], a[180:183], v[192:195]\n"
      "ds_read_b128 v[128:131], v0 offset:8192\n"
      "v_mul_f32_e64 v180, v110, v180\n"
      "v_mfma_f32_16x16x32_bf16 v[192:195], v[152:155], a[184:187], v[192:195]\n"
      "ds_read_b128 v[172:175], v1 offset:8192\n"
      "v_mul_f32_e64 v181, v110, v181\n"
      "v_mfma_f32_16x16x32_bf16 v[192:195], v[200:203], a[188:191], v[192:195]\n"
      "ds_read_b128 v[148:151], v0 offset:14336\n"
      "v_cvt_pk_bf16_f32 v138, v196, v197\n"
      "v_mfma_f32_16x16x32_bf16 v[164:167], v[124:127], a[192:195], 0\n"
      "ds_read_b128 v[120:123], v1 offset:14336\n"
      "v_cvt_pk_bf16_f32 v139, v198, v199\n"
      "v_mfma_f32_16x16x32_bf16 v[164:167], v[182:185], a[196:199], v[164:167]\n"
      "ds_write_b128 v5, v[136:139] offset:12288\n"
      "v_mfma_f32_16x16x32_bf16 v[164:167], v[160:163], a[200:203], v[164:167]\n"
      "s_waitcnt lgkmcnt(4)\n"
      "v_cvt_pk_bf16_f32 v179, v180, v181\n"
      "v_fma_f32 v125, v104, v104, s51\n"
      "v_mfma_f32_16x16x32_bf16 v[164:167], v[204:207], a[204:207], v[164:167]\n"
      "s_waitcnt lgkmcnt(3)\n"
      "v_fma_f32 v221, v100, v100, s51\n"
      "v_fmac_f32_e64 v125, v105, v105\n"
      "v_mfma_f32_16x16x32_bf16 v[164:167], v[152:155], a[208:211], v[164:167]\n"
      "ds_write_b64 v6, v[178:179] offset:12288\n"
      "v_mfma_f32_16x16x32_bf16 v[164:167], v[200:203], a[212:215], v[164:167]\n"
      "s_waitcnt lgkmcnt(2)\n"
      "v_fmac_f32 v221, v101, v101\n"
      "v_fmac_f32 v125, v106, v106\n"
      "v_mfma_f32_16x16x32_bf16 v[160:163], a[0:3], v[214:217], 0\n"
      "v_fmac_f32 v221, v102, v102\n"
      "v_fmac_f32 v125, v107, v107\n"
      "v_mfma_f32_16x16x32_bf16 v[160:163], a[4:7], v[208:211], v[160:163]\n"
      "v_fmac_f32 v221, v103, v103\n"
      "v_fmac_f32 v125, v156, v156\n"
      "v_mfma_f32_16x16x32_bf16 v[160:163], a[8:11], v[128:131], v[160:163]\n"
      "v_fmac_f32 v221, v188, v188\n"
      "v_fmac_f32 v125, v157, v157\n"
      "v_mfma_f32_16x16x32_bf16 v[160:163], a[12:15], v[172:175], v[160:163]\n"
      "v_fmac_f32 v221, v189, v189\n"
      "v_fmac_f32 v125, v158, v158\n"
      "v_mfma_f32_16x16x32_bf16 v[160:163], a[16:19], v[148:151], v[160:163]\n"
      "v_fmac_f32 v221, v190, v190\n"
      "v_fmac_f32 v125, v159, v159\n"
      "v_mfma_f32_16x16x32_bf16 v[160:163], a[20:23], v[120:123], v[160:163]\n"
      "v_fmac_f32 v221, v191, v191\n"
      "v_fmac_f32 v125, v142, v142\n"
      "v_mfma_f32_16x16x32_bf16 v[168:171], a[24:27], v[214:217], 0\n"
      "v_fmac_f32 v221, v132, v132\n"
      "v_fmac_f32 v125, v143, v143\n"
      "v_mfma_f32_16x16x32_bf16 v[168:171], a[28:31], v[208:211], v[168:171]\n"
      "v_fmac_f32 v221, v133, v133\n"
      "v_fmac_f32 v125, v144, v144\n"
      "v_mfma_f32_16x16x32_bf16 v[168:171], a[32:35], v[128:131], v[168:171]\n"
      "v_fmac_f32 v221, v134, v134\n"
      "v_fmac_f32 v125, v145, v145\n"
      "v_mfma_f32_16x16x32_bf16 v[168:171], a[36:39], v[172:175], v[168:171]\n"
      "v_fmac_f32 v221, v135, v135\n"
      "v_fma_f32 v222, v160, v160, s51\n"
      "v_mfma_f32_16x16x32_bf16 v[168:171], a[40:43], v[148:151], v[168:171]\n"
      "v_permlane16_swap_b32 v125, v221\n"
      "v_mfma_f32_16x16x32_bf16 v[168:171], a[44:47], v[120:123], v[168:171]\n"
      "v_add_f32 v125, v125, v221\n"
      "v_mov_b32 v221, v125\n"
      "v_mfma_f32_16x16x32_bf16 v[180:183], a[48:51], v[214:217], 0\n"
      "v_fmac_f32 v222, v161, v161\n"
      "v_permlane32_swap_b32 v125, v221\n"
      "v_mfma_f32_16x16x32_bf16 v[180:183], a[52:55], v[208:211], v[180:183]\n"
      "v_add_f32 v125, v125, v221\n"
      "v_rsq_f32 v125, v125\n"
      "v_mfma_f32_16x16x32_bf16 v[180:183], a[56:59], v[128:131], v[180:183]\n"
      "v_mov_b32 v221, v125\n"
      "v_fmac_f32 v222, v162, v162\n"
      "v_mfma_f32_16x16x32_bf16 v[180:183], a[60:63], v[172:175], v[180:183]\n"
      "v_permlane16_swap_b32 v125, v221\n"
      "v_mfma_f32_16x16x32_bf16 v[180:183], a[64:67], v[148:151], v[180:183]\n"
      "v_mul_f32 v125, s50, v125\n"
      "v_mul_f32 v100, v221, v100\n"
      "v_mfma_f32_16x16x32_bf16 v[180:183], a[68:71], v[120:123], v[180:183]\n"
      "v_mul_f32 v101, v221, v101\n"
      "v_mul_f32 v104, v125, v104\n"
      "v_mfma_f32_16x16x32_bf16 v[200:203], a[72:75], v[214:217], 0\n"
      "v_mul_f32_e64 v105, v125, v105\n"
      "v_cvt_pk_bf16_f32 v100, v100, v101\n"
      "v_mfma_f32_16x16x32_bf16 v[200:203], a[76:79], v[208:211], v[200:203]\n"
      "v_mul_f32 v102, v221, v102\n"
      "v_mul_f32 v103, v221, v103\n"
      "v_mfma_f32_16x16x32_bf16 v[200:203], a[80:83], v[128:131], v[200:203]\n"
      "v_mul_f32 v132, v221, v132\n"
      "v_mul_f32 v133, v221, v133\n"
      "v_mfma_f32_16x16x32_bf16 v[200:203], a[84:87], v[172:175], v[200:203]\n"
      "v_cvt_pk_bf16_f32 v104, v104, v105\n"
      "v_mul_f32_e64 v106, v125, v106\n"
      "v_mfma_f32_16x16x32_bf16 v[200:203], a[88:91], v[148:151], v[200:203]\n"
      "v_mul_f32 v107, v125, v107\n"
      "v_mul_f32 v142, v125, v142\n"
      "v_mfma_f32_16x16x32_bf16 v[200:203], a[92:95], v[120:123], v[200:203]\n"
      "v_mul_f32_e64 v143, v125, v143\n"
      "v_cvt_pk_bf16_f32 v101, v102, v103\n"
      "v_mfma_f32_16x16x32_bf16 v[152:155], a[96:99], v[214:217], 0\n"
      "v_mul_f32 v188, v221, v188\n"
      "v_mul_f32 v189, v221, v189\n"
      "v_mfma_f32_16x16x32_bf16 v[152:155], a[100:103], v[208:211], v[152:155]\n"
      "v_mul_f32 v190, v221, v190\n"
      "v_mul_f32 v191, v221, v191\n"
      "v_mfma_f32_16x16x32_bf16 v[152:155], a[104:107], v[128:131], v[152:155]\n"
      "v_cvt_pk_bf16_f32 v132, v132, v133\n"
      "v_mul_f32_e64 v134, v221, v134\n"
      "v_mfma_f32_16x16x32_bf16 v[152:155], a[108:111], v[172:175], v[152:155]\n"
      "v_mul_f32 v135, v221, v135\n"
      "v_cvt_pk_bf16_f32 v105, v106, v107\n"
      "v_mfma_f32_16x16x32_bf16 v[152:155], a[112:115], v[148:151], v[152:155]\n"
      "v_mul_f32 v156, v125, v156\n"
      "v_mul_f32 v157, v125, v157\n"
      "v_mfma_f32_16x16x32_bf16 v[152:155], a[116:119], v[120:123], v[152:155]\n"
      "v_mul_f32 v158, v125, v158\n"
      "v_mul_f32_e64 v159, v125, v159\n"
      "v_mfma_f32_16x16x32_bf16 v[218:221], a[120:123], v[214:217], 0\n"
      "v_cvt_pk_bf16_f32 v142, v142, v143\n"
      "v_mul_f32 v144, v125, v144\n"
      "v_mfma_f32_16x16x32_bf16 v[218:221], a[124:127], v[208:211], v[218:221]\n"
      "v_mul_f32 v145, v125, v145\n"
      "v_cvt_pk_bf16_f32 v102, v188, v189\n"
      "v_mfma_f32_16x16x32_bf16 v[218:221], a[128:131], v[128:131], v[218:221]\n"
      "v_cvt_pk_bf16_f32 v103, v190, v191\n"
      "v_cvt_pk_bf16_f32 v133, v134, v135\n"
      "v_mfma_f32_16x16x32_bf16 v[218:221], a[132:135], v[172:175], v[218:221]\n"
      "v_cvt_pk_bf16_f32 v106, v156, v157\n"
      "v_cvt_pk_bf16_f32 v107, v158, v159\n"
      "v_mfma_f32_16x16x32_bf16 v[218:221], a[136:139], v[148:151], v[218:221]\n"
      "v_cvt_pk_bf16_f32 v143, v144, v145\n"
      "v_fma_f32 v135, v200, v200, s51\n"
      "v_mfma_f32_16x16x32_bf16 v[218:221], a[140:143], v[120:123], v[218:221]\n"
      "v_fmac_f32 v135, v201, v201\n"
      "v_fmac_f32 v135, v202, v202\n"
      "v_mfma_f32_16x16x32_bf16 v[156:159], v[214:217], a[144:147], 0\n"
      "v_fmac_f32 v222, v163, v163\n"
      "v_fmac_f32 v135, v203, v203\n"
      "v_mfma_f32_16x16x32_bf16 v[156:159], v[208:211], a[148:151], v[156:159]\n"
      "v_fmac_f32 v222, v168, v168\n"
      "v_fmac_f32 v135, v152, v152\n"
      "v_mfma_f32_16x16x32_bf16 v[156:159], v[128:131], a[152:155], v[156:159]\n"
      "v_fmac_f32 v222, v169, v169\n"
      "v_fmac_f32 v135, v153, v153\n"
      "v_mfma_f32_16x16x32_bf16 v[156:159], v[172:175], a[156:159], v[156:159]\n"
      "v_fmac_f32 v222, v170, v170\n"
      "v_fmac_f32 v135, v154, v154\n"
      "v_mfma_f32_16x16x32_bf16 v[156:159], v[148:151], a[160:163], v[156:159]\n"
      "v_fmac_f32 v222, v171, v171\n"
      "v_fmac_f32 v135, v155, v155\n"
      "v_mfma_f32_16x16x32_bf16 v[156:159], v[120:123], a[164:167], v[156:159]\n"
      "v_fmac_f32 v222, v180, v180\n"
      "v_fmac_f32 v135, v218, v218\n"
      "v_mfma_f32_16x16x32_bf16 v[188:191], v[214:217], a[168:171], 0\n"
      "v_fmac_f32 v222, v181, v181\n"
      "v_fmac_f32 v135, v219, v219\n"
      "v_mfma_f32_16x16x32_bf16 v[188:191], v[208:211], a[172:175], v[188:191]\n"
      "ds_read_b128 v[204:207], v0 offset:4096\n"
      "v_fmac_f32_e64 v222, v182, v182\n"
      "v_mfma_f32_16x16x32_bf16 v[188:191], v[128:131], a[176:179], v[188:191]\n"
      "ds_read_b128 v[176:179], v1 offset:4096\n"
      "v_fmac_f32_e64 v135, v220, v220\n"
      "v_mfma_f32_16x16x32_bf16 v[188:191], v[172:175], a[180:183], v[188:191]\n"
      "ds_read_b128 v[124:127], v0 offset:10240\n"
      "v_fmac_f32 v222, v183, v183\n"
      "v_mfma_f32_16x16x32_bf16 v[188:191], v[148:151], a[184:187], v[188:191]\n"
      "ds_read_b128 v[196:199], v1 offset:10240\n"
      "v_fmac_f32 v135, v221, v221\n"
      "v_mfma_f32_16x16x32_bf16 v[188:191], v[120:123], a[188:191], v[188:191]\n"
      "ds_read_b128 v[96:99], v0 offset:16384\n"
      "v_permlane16_swap_b32_e64 v222, v135\n"
      "v_mfma_f32_16x16x32_bf16 v[184:187], v[214:217], a[192:195], 0\n"
      "ds_read_b128 v[108:111], v1 offset:16384\n"
      "v_add_f32 v222, v222, v135\n"
      "v_mfma_f32_16x16x32_bf16 v[184:187], v[208:211], a[196:199], v[184:187]\n"
      "s_waitcnt lgkmcnt(5)\n"
      "v_mov_b32_e64 v135, v222\n"
      "v_cvt_pk_bf16_f32 v112, v112, v113\n"
      "v_mfma_f32_16x16x32_bf16 v[184:187], v[128:131], a[200:203], v[184:187]\n"
      "s_waitcnt lgkmcnt(4)\n"
      "v_permlane32_swap_b32 v222, v135\n"
      "v_mfma_f32_16x16x32_bf16 v[184:187], v[172:175], a[204:207], v[184:187]\n"
      "s_waitcnt lgkmcnt(3)\n"
      "v_add_f32 v222, v222, v135\n"
      "v_rsq_f32 v222, v222\n"
      "v_mfma_f32_16x16x32_bf16 v[184:187], v[148:151], a[208:211], v[184:187]\n"
      "s_waitcnt lgkmcnt(2)\n"
      "v_mov_b32_e64 v135, v222\n"
      "v_cvt_pk_bf16_f32 v113, v114, v115\n"
      "v_mfma_f32_16x16x32_bf16 v[184:187], v[120:123], a[212:215], v[184:187]\n"
      "s_waitcnt lgkmcnt(1)\n"
      "v_permlane16_swap_b32 v222, v135\n"
      "v_mfma_f32_16x16x32_bf16 v[144:147], a[0:3], v[204:207], 0\n"
      "s_waitcnt lgkmcnt(0)\n"
      "v_mul_f32 v222, s50, v222\n"
      "v_mul_f32 v200, v135, v200\n"
      "v_mfma_f32_16x16x32_bf16 v[144:147], a[4:7], v[176:179], v[144:147]\n"
      "v_mul_f32 v201, v135, v201\n"
      "v_mul_f32_e64 v160, v222, v160\n"
      "v_mfma_f32_16x16x32_bf16 v[144:147], a[8:11], v[124:127], v[144:147]\n"
      "v_mul_f32 v161, v222, v161\n"
      "v_cvt_pk_bf16_f32 v200, v200, v201\n"
      "v_mfma_f32_16x16x32_bf16 v[144:147], a[12:15], v[196:199], v[144:147]\n"
      "v_mul_f32 v202, v135, v202\n"
      "v_mul_f32 v203, v135, v203\n"
      "v_mfma_f32_16x16x32_bf16 v[144:147], a[16:19], v[96:99], v[144:147]\n"
      "v_mul_f32 v218, v135, v218\n"
      "v_mul_f32_e64 v219, v135, v219\n"
      "v_mfma_f32_16x16x32_bf16 v[144:147], a[20:23], v[108:111], v[144:147]\n"
      "v_cvt_pk_bf16_f32 v160, v160, v161\n"
      "v_mul_f32 v162, v222, v162\n"
      "v_mfma_f32_16x16x32_bf16 v[148:151], a[24:27], v[204:207], 0\n"
      "v_mul_f32 v163, v222, v163\n"
      "v_mul_f32_e64 v180, v222, v180\n"
      "v_mfma_f32_16x16x32_bf16 v[148:151], a[28:31], v[176:179], v[148:151]\n"
      "v_mul_f32 v181, v222, v181\n"
      "v_cvt_pk_bf16_f32 v201, v202, v203\n"
      "v_mfma_f32_16x16x32_bf16 v[148:151], a[32:35], v[124:127], v[148:151]\n"
      "v_mul_f32 v152, v135, v152\n"
      "v_mul_f32 v153, v135, v153\n"
      "v_mfma_f32_16x16x32_bf16 v[148:151], a[36:39], v[196:199], v[148:151]\n"
      "v_mul_f32 v154, v135, v154\n"
      "v_mul_f32_e64 v155, v135, v155\n"
      "v_mfma_f32_16x16x32_bf16 v[148:151], a[40:43], v[96:99], v[148:151]\n"
      "ds_read_b128 v[208:211], v7 offset:18432\n"
      "v_cvt_pk_bf16_f32 v218, v218, v219\n"
      "v_mfma_f32_16x16x32_bf16 v[148:151], a[44:47], v[108:111], v[148:151]\n"
      "ds_read_b128 v[214:217], v7 offset:22528\n"
      "v_mul_f32 v220, v135, v220\n"
      "v_mfma_f32_16x16x32_bf16 v[138:141], a[48:51], v[204:207], 0\n"
      "ds_read_b128 v[120:123], v7 offset:26624\n"
      "v_mul_f32 v221, v135, v221\n"
      "v_mfma_f32_16x16x32_bf16 v[138:141], a[52:55], v[176:179], v[138:141]\n"
      "v_cvt_pk_bf16_f32 v161, v162, v163\n"
      "v_mul_f32 v168, v222, v168\n"
      "v_mfma_f32_16x16x32_bf16 v[138:141], a[56:59], v[124:127], v[138:141]\n"
      "ds_read_b128 v[172:175], v7 offset:30720\n"
      "v_mul_f32 v169, v222, v169\n"
      "v_mfma_f32_16x16x32_bf16 v[138:141], a[60:63], v[196:199], v[138:141]\n"
      "ds_read_b128 v[134:137], v7 offset:34816\n"
      "v_mul_f32 v170, v222, v170\n"
      "v_mfma_f32_16x16x32_bf16 v[138:141], a[64:67], v[96:99], v[138:141]\n"
      "v_mul_f32 v171, v222, v171\n"
      "v_cvt_pk_bf16_f32 v180, v180, v181\n"
      "v_mfma_f32_16x16x32_bf16 v[138:141], a[68:71], v[108:111], v[138:141]\n"
      "v_mul_f32 v182, v222, v182\n"
      "v_mul_f32 v183, v222, v183\n"
      "v_mfma_f32_16x16x32_bf16 v[128:131], a[72:75], v[204:207], 0\n"
      "s_waitcnt lgkmcnt(2)\n"
      "v_cvt_pk_bf16_f32 v202, v152, v153\n"
      "v_cvt_pk_bf16_f32 v203, v154, v155\n"
      "v_mfma_f32_16x16x32_bf16 v[128:131], a[76:79], v[176:179], v[128:131]\n"
      "buffer_store_dwordx4 v[208:211], v20, s[20:23], s46 offen sc1\n"
      "v_mfma_f32_16x16x32_bf16 v[128:131], a[80:83], v[124:127], v[128:131]\n"
      "s_waitcnt lgkmcnt(0)\n"
      "v_cvt_pk_bf16_f32 v219, v220, v221\n"
      "v_cvt_pk_bf16_f32 v162, v168, v169\n"
      "v_mfma_f32_16x16x32_bf16 v[128:131], a[84:87], v[196:199], v[128:131]\n"
      "v_cvt_pk_bf16_f32 v163, v170, v171\n"
      "v_cvt_pk_bf16_f32 v181, v182, v183\n"
      "v_mfma_f32_16x16x32_bf16 v[128:131], a[88:91], v[96:99], v[128:131]\n"
      "v_fma_f32 v77, v144, v144, s51\n"
      "v_fmac_f32 v77, v145, v145\n"
      "v_mfma_f32_16x16x32_bf16 v[128:131], a[92:95], v[108:111], v[128:131]\n"
      "v_fmac_f32 v77, v146, v146\n"
      "v_fmac_f32_e64 v77, v147, v147\n"
      "v_mfma_f32_16x16x32_bf16 v[208:211], a[96:99], v[204:207], 0\n"
      "v_fmac_f32 v77, v148, v148\n"
      "v_fmac_f32 v77, v149, v149\n"
      "v_mfma_f32_16x16x32_bf16 v[208:211], a[100:103], v[176:179], v[208:211]\n"
      "v_fmac_f32 v77, v150, v150\n"
      "v_fmac_f32 v77, v151, v151\n"
      "v_mfma_f32_16x16x32_bf16 v[208:211], a[104:107], v[124:127], v[208:211]\n"
      "v_fma_f32 v81, v128, v128, s51\n"
      "v_fmac_f32_e64 v81, v129, v129\n"
      "v_mfma_f32_16x16x32_bf16 v[208:211], a[108:111], v[196:199], v[208:211]\n"
      "buffer_store_dwordx4 v[214:217], v21, s[20:23], s46 offen sc1\n"
      "v_mfma_f32_16x16x32_bf16 v[208:211], a[112:115], v[96:99], v[208:211]\n"
      "v_fmac_f32 v81, v130, v130\n"
      "v_fmac_f32 v81, v131, v131\n"
      "v_mfma_f32_16x16x32_bf16 v[208:211], a[116:119], v[108:111], v[208:211]\n"
      "v_fmac_f32 v77, v138, v138\n"
      "v_fmac_f32 v77, v139, v139\n"
      "v_mfma_f32_16x16x32_bf16 v[168:171], a[120:123], v[204:207], 0\n"
      "v_fmac_f32 v77, v140, v140\n"
      "v_fmac_f32 v77, v141, v141\n"
      "v_mfma_f32_16x16x32_bf16 v[168:171], a[124:127], v[176:179], v[168:171]\n"
      "v_cvt_pk_bf16_f32 v114, v156, v157\n"
      "v_cvt_pk_bf16_f32 v115, v158, v159\n"
      "v_mfma_f32_16x16x32_bf16 v[168:171], a[128:131], v[124:127], v[168:171]\n"
      "v_fmac_f32 v81, v208, v208\n"
      "v_fmac_f32 v81, v209, v209\n"
      "v_mfma_f32_16x16x32_bf16 v[168:171], a[132:135], v[196:199], v[168:171]\n"
      "v_fmac_f32 v81, v210, v210\n"
      "v_fmac_f32_e64 v81, v211, v211\n"
      "v_mfma_f32_16x16x32_bf16 v[168:171], a[136:139], v[96:99], v[168:171]\n"
      "v_cvt_pk_bf16_f32 v192, v192, v193\n"
      "v_cvt_pk_bf16_f32 v193, v194, v195\n"
      "v_mfma_f32_16x16x32_bf16 v[168:171], a[140:143], v[108:111], v[168:171]\n"
      "buffer_store_dwordx4 v[120:123], v22, s[20:23], s46 offen sc1\n"
      "s_nop 1\n"
      "v_mfma_f32_16x16x32_bf16 v[120:123], v[204:207], a[144:147], 0\n"
      "v_cvt_pk_bf16_f32 v194, v188, v189\n"
      "v_cvt_pk_bf16_f32 v195, v190, v191\n"
      "v_mfma_f32_16x16x32_bf16 v[120:123], v[176:179], a[148:151], v[120:123]\n"
      "v_cvt_pk_bf16_f32 v164, v164, v165\n"
      "v_cvt_pk_bf16_f32 v165, v166, v167\n"
      "v_mfma_f32_16x16x32_bf16 v[120:123], v[124:127], a[152:155], v[120:123]\n"
      "v_fmac_f32 v81, v168, v168\n"
      "v_fmac_f32 v81, v169, v169\n"
      "v_mfma_f32_16x16x32_bf16 v[120:123], v[196:199], a[156:159], v[120:123]\n"
      "v_fmac_f32 v81, v170, v170\n"
      "v_fmac_f32 v81, v171, v171\n"
      "v_mfma_f32_16x16x32_bf16 v[120:123], v[96:99], a[160:163], v[120:123]\n"
      "v_cvt_pk_bf16_f32 v166, v184, v185\n"
      "v_permlane16_swap_b32 v77, v81\n"
      "v_mfma_f32_16x16x32_bf16 v[120:123], v[108:111], a[164:167], v[120:123]\n"
      "v_add_f32 v77, v77, v81\n"
      "v_mov_b32_e64 v81, v77\n"
      "v_mfma_f32_16x16x32_bf16 v[156:159], v[204:207], a[168:171], 0\n"
      "v_cvt_pk_bf16_f32 v167, v186, v187\n"
      "v_permlane32_swap_b32_e64 v77, v81\n"
      "v_mfma_f32_16x16x32_bf16 v[156:159], v[176:179], a[172:175], v[156:159]\n"
      "buffer_store_dwordx4 v[172:175], v23, s[20:23], s46 offen sc1\n"
      "v_mfma_f32_16x16x32_bf16 v[156:159], v[124:127], a[176:179], v[156:159]\n"
      "s_nop 0\n"
      "ds_read_b128 v[172:175], v2 offset:18432\n"
      "v_add_f32 v77, v77, v81\n"
      "v_mfma_f32_16x16x32_bf16 v[156:159], v[196:199], a[180:183], v[156:159]\n"
      "ds_read_b128 v[188:191], v2 offset:21504\n"
      "v_rsq_f32 v77, v77\n"
      "v_mfma_f32_16x16x32_bf16 v[156:159], v[96:99], a[184:187], v[156:159]\n"
      "ds_read_b128 v[184:187], v2 offset:24576\n"
      "v_mov_b32 v81, v77\n"
      "v_mfma_f32_16x16x32_bf16 v[156:159], v[108:111], a[188:191], v[156:159]\n"
      "v_cvt_pk_bf16_f32 v76, v120, v121\n"
      "v_permlane16_swap_b32 v77, v81\n"
      "v_mfma_f32_16x16x32_bf16 v[152:155], v[204:207], a[192:195], 0\n"
      "v_mul_f32 v128, v81, v128\n"
      "v_mul_f32 v129, v81, v129\n"
      "v_mfma_f32_16x16x32_bf16 v[152:155], v[176:179], a[196:199], v[152:155]\n"
      "v_cvt_pk_bf16_f32 v128, v128, v129\n"
      "v_mul_f32 v130, v81, v130\n"
      "v_mfma_f32_16x16x32_bf16 v[152:155], v[124:127], a[200:203], v[152:155]\n"
      "s_waitcnt lgkmcnt(2)\n"
      "v_mul_f32_e64 v131, v81, v131\n"
      "buffer_store_dwordx4 v[134:137], v25, s[20:23], s46 offen sc1\n"
      "v_mfma_f32_16x16x32_bf16 v[152:155], v[196:199], a[204:207], v[152:155]\n"
      "s_waitcnt lgkmcnt(0)\n"
      "v_cvt_pk_bf16_f32 v129, v130, v131\n"
      "v_mul_f32 v208, v81, v208\n"
      "v_mfma_f32_16x16x32_bf16 v[152:155], v[96:99], a[208:211], v[152:155]\n"
      "v_mul_f32 v209, v81, v209\n"
      "v_mul_f32_e64 v210, v81, v210\n"
      "v_mfma_f32_16x16x32_bf16 v[152:155], v[108:111], a[212:215], v[152:155]\n"
      "v_mul_f32_e64 v211, v81, v211\n"
      "v_cvt_pk_bf16_f32 v130, v208, v209\n"
      "v_mfma_f32_16x16x32_bf16 v[220:223], a[216:219], v[172:175], 0\n"
      "v_cvt_pk_bf16_f32 v131, v210, v211\n"
      "v_mul_f32 v77, s50, v77\n"
      "v_mfma_f32_16x16x32_bf16 v[220:223], a[220:223], v[188:191], v[220:223]\n"
      "ds_read_b128 v[196:199], v10 offset:32\n"
      "v_mul_f32 v144, v77, v144\n"
      "v_mfma_f32_16x16x32_bf16 v[220:223], a[224:227], v[184:187], v[220:223]\n"
      "v_mul_f32 v145, v77, v145\n"
      "v_mul_f32 v168, v81, v168\n"
      "v_mfma_f32_16x16x32_bf16 v[208:211], a[240:243], v[172:175], 0\n"
      "v_mul_f32_e64 v169, v81, v169\n"
      "v_cvt_pk_bf16_f32 v144, v144, v145\n"
      "v_mfma_f32_16x16x32_bf16 v[208:211], a[244:247], v[188:191], v[208:211]\n"
      "v_mul_f32 v146, v77, v146\n"
      "v_mul_f32 v147, v77, v147\n"
      "v_mfma_f32_16x16x32_bf16 v[208:211], a[248:251], v[184:187], v[208:211]\n"
      "ds_read_b64 v[212:213], v10 offset:24\n"
      "v_mul_f32 v138, v77, v138\n"
      "v_mfma_f32_16x16x32_bf16 v[214:217], v[232:235], v[172:175], 0\n"
      "ds_read_b128 v[172:175], v2 offset:27648\n"
      "v_mul_f32 v139, v77, v139\n"
      "v_mfma_f32_16x16x32_bf16 v[214:217], v[236:239], v[188:191], v[214:217]\n"
      "ds_read_b128 v[176:179], v2 offset:30720\n"
      "v_cvt_pk_bf16_f32 v168, v168, v169\n"
      "v_mfma_f32_16x16x32_bf16 v[214:217], v[240:243], v[184:187], v[214:217]\n"
      "ds_read_b128 v[188:191], v2 offset:33792\n"
      "v_mul_f32 v170, v81, v170\n"
      "v_mfma_f32_16x16x32_bf16 v[96:99], v[100:103], v[104:107], v[40:43]\n"
      "s_waitcnt lgkmcnt(4)\n"
      "v_mul_f32_e64 v171, v81, v171\n"
      "v_cvt_pk_bf16_f32 v145, v146, v147\n"
      "v_mfma_f32_16x16x32_bf16 v[134:137], v[200:203], v[104:107], v[44:47]\n"
      "v_mul_f32 v148, v77, v148\n"
      "v_mul_f32 v149, v77, v149\n"
      "v_mfma_f32_16x16x32_bf16 v[184:187], v[128:131], v[104:107], v[48:51]\n"
      "s_waitcnt lgkmcnt(3)\n"
      "v_mul_f32 v150, v77, v150\n"
      "v_mul_f32 v151, v77, v151\n"
      "v_mfma_f32_16x16x32_bf16 v[204:207], v[196:199], v[104:107], v[24:27]\n"
      "s_waitcnt lgkmcnt(2)\n"
      "v_cvt_pk_bf16_f32 v138, v138, v139\n"
      "v_mul_f32 v140, v77, v140\n"
      "v_mfma_f32_16x16x16_bf16 v[96:99], v[132:133], v[142:143], v[96:99]\n"
      "v_mul_f32 v141, v77, v141\n"
      "v_cvt_pk_bf16_f32 v169, v170, v171\n"
      "v_cvt_pk_bf16_f32 v146, v148, v149\n"
      "v_mfma_f32_16x16x16_bf16 v[134:137], v[218:219], v[142:143], v[134:137]\n"
      "v_cvt_pk_bf16_f32 v147, v150, v151\n"
      "v_cvt_pk_bf16_f32 v139, v140, v141\n"
      "v_cvt_pk_bf16_f32 v77, v122, v123\n"
      "v_mfma_f32_16x16x16_bf16 v[184:187], v[168:169], v[142:143], v[184:187]\n"
      "v_max3_f32 v123, v96, v97, v98\n"
      "v_cvt_pk_bf16_f32 v80, v156, v157\n"
      "v_cvt_pk_bf16_f32 v81, v158, v159\n"
      "v_mfma_f32_16x16x16_bf16 v[204:207], v[212:213], v[142:143], v[204:207]\n"
      "v_max3_f32 v107, v99, v134, v135\n"
      "v_max3_f32 v123, v123, v136, v137\n"
      "v_cvt_pk_bf16_f32 v84, v152, v153\n"
      "v_mfma_f32_16x16x32_bf16 v[220:223], a[228:231], v[172:175], v[220:223]\n"
      "s_waitcnt lgkmcnt(0)\n"
      "v_max3_f32 v107, v107, v184, v185\n"
      "v_max3_f32 v123, v123, v186, v187\n"
      "v_mfma_f32_16x16x32_bf16 v[220:223], a[232:235], v[176:179], v[220:223]\n"
      "v_max3_f32 v123, v123, v107, v204\n"
      "v_mov_b32 v107, v123\n"
      "v_mfma_f32_16x16x32_bf16 v[220:223], a[236:239], v[188:191], v[220:223]\n"
      "v_cvt_pk_bf16_f32 v85, v154, v155\n"
      "v_permlane16_swap_b32 v123, v107\n"
      "v_mfma_f32_16x16x32_bf16 v[208:211], a[252:255], v[172:175], v[208:211]\n"
      "v_max_f32 v123, v123, v107\n"
      "v_mov_b32 v107, v123\n"
      "v_mfma_f32_16x16x32_bf16 v[208:211], v[224:227], v[176:179], v[208:211]\n"
      "s_waitcnt vmcnt(6)\n"
      "v_or3_b32 v116, v116, v117, v118\n"
      "v_permlane32_swap_b32 v123, v107\n"
      "v_mfma_f32_16x16x32_bf16 v[208:211], v[228:231], v[188:191], v[208:211]\n"
      "v_max_f32 v123, v123, v107\n"
      "v_sub_f32 v96, v96, v123\n"
      "v_mfma_f32_16x16x32_bf16 v[214:217], v[244:247], v[172:175], v[214:217]\n"
      "ds_read_b128 v[104:107], v9 offset:5632\n"
      "v_sub_f32 v97, v97, v123\n"
      "v_mfma_f32_16x16x32_bf16 v[214:217], v[248:251], v[176:179], v[214:217]\n"
      "ds_read_b128 v[152:155], v2 offset:18688\n"
      "v_sub_f32 v98, v98, v123\n"
      "v_mfma_f32_16x16x32_bf16 v[214:217], v[252:255], v[188:191], v[214:217]\n"
      "ds_read_b128 v[176:179], v2 offset:21760\n"
      "v_sub_f32 v99, v99, v123\n"
      "v_mfma_f32_16x16x32_bf16 v[124:127], v[100:103], v[160:163], v[52:55]\n"
      "ds_read_b128 v[108:111], v2 offset:24832\n"
      "v_exp_f32 v96, v96\n"
      "v_mfma_f32_16x16x32_bf16 v[156:159], v[200:203], v[160:163], v[56:59]\n"
      "v_exp_f32 v97, v97\n"
      "v_mfma_f32_16x16x32_bf16 v[188:191], v[128:131], v[160:163], v[60:63]\n"
      "v_sub_f32 v134, v134, v123\n"
      "v_sub_f32 v135, v135, v123\n"
      "v_mfma_f32_16x16x32_bf16 v[140:143], v[196:199], v[160:163], v[26:29]\n"
      "v_sub_f32 v136, v136, v123\n"
      "v_sub_f32 v137, v137, v123\n"
      "v_mfma_f32_16x16x16_bf16 v[124:127], v[132:133], v[180:181], v[124:127]\n"
      "v_exp_f32 v98, v98\n"
      "v_exp_f32 v99, v99\n"
      "v_mfma_f32_16x16x16_bf16 v[156:159], v[218:219], v[180:181], v[156:159]\n"
      "v_exp_f32 v134, v134\n"
      "v_exp_f32 v135, v135\n"
      "v_mfma_f32_16x16x16_bf16 v[188:191], v[168:169], v[180:181], v[188:191]\n"
      "v_exp_f32 v136, v136\n"
      "v_exp_f32 v137, v137\n"
      "v_mfma_f32_16x16x16_bf16 v[140:143], v[212:213], v[180:181], v[140:143]\n"
      "v_cvt_pk_bf16_f32 v96, v96, v97\n"
      "v_cvt_pk_bf16_f32 v97, v98, v99\n"
      "v_cvt_pk_bf16_f32 v98, v134, v135\n"
      "s_waitcnt lgkmcnt(0)\n"
      "v_mfma_f32_16x16x32_bf16 v[148:151], a[216:219], v[152:155], 0\n"
      "v_cvt_pk_bf16_f32 v99, v136, v137\n"
      "v_sub_f32 v184, v184, v123\n"
      "v_mfma_f32_16x16x32_bf16 v[148:151], a[220:223], v[176:179], v[148:151]\n"
      "ds_read_u16 v78, v11\n"
      "v_sub_f32 v185, v185, v123\n"
      "v_mfma_f32_16x16x32_bf16 v[148:151], a[224:227], v[108:111], v[148:151]\n"
      "v_sub_f32 v186, v186, v123\n"
      "v_sub_f32 v187, v187, v123\n"
      "v_mfma_f32_16x16x32_bf16 v[170:173], a[240:243], v[152:155], 0\n"
      "ds_read_u16 v82, v11 offset:32\n"
      "v_sub_f32 v204, v204, v123\n"
      "v_mfma_f32_16x16x32_bf16 v[170:173], a[244:247], v[176:179], v[170:173]\n"
      "v_exp_f32 v184, v184\n"
      "v_mfma_f32_16x16x32_bf16 v[170:173], a[248:251], v[108:111], v[170:173]\n"
      "ds_read_u16 v86, v11 offset:64\n"
      "v_exp_f32 v185, v185\n"
      "v_mfma_f32_16x16x32_bf16 v[120:123], v[232:235], v[152:155], 0\n"
      "v_exp_f32 v186, v186\n"
      "v_mfma_f32_16x16x32_bf16 v[120:123], v[236:239], v[176:179], v[120:123]\n"
      "s_waitcnt lgkmcnt(2)\n"
      "v_exp_f32 v187, v187\n"
      "v_mfma_f32_16x16x32_bf16 v[120:123], v[240:243], v[108:111], v[120:123]\n"
      "v_exp_f32 v204, v204\n"
      "v_mfma_f32_16x16x32_bf16 v[178:181], v[32:35], v[96:99], 0\n"
      "s_waitcnt lgkmcnt(1)\n"
      "v_cvt_pk_bf16_f32 v88, v184, v185\n"
      "v_cvt_pk_bf16_f32 v89, v186, v187\n"
      "v_cvt_pk_bf16_f32 v90, v204, 0\n"
      "v_max3_f32 v143, v124, v125, v126\n"
      "v_max3_f32 v187, v127, v156, v157\n"
      "v_mfma_f32_16x16x32_bf16 v[178:181], v[32:35], v[88:91], v[178:181]\n"
      "s_waitcnt lgkmcnt(0)\n"
      "v_max3_f32 v143, v143, v158, v159\n"
      "v_max3_f32 v187, v187, v188, v189\n"
      "v_mfma_f32_16x16x32_bf16 v[134:137], v[112:115], v[96:99], 0\n"
      "v_max3_f32 v143, v143, v190, v191\n"
      "v_max3_f32 v143, v143, v187, v140\n"
      "v_mfma_f32_16x16x32_bf16 v[134:137], v[76:79], v[88:91], v[134:137]\n"
      "v_mov_b32_e64 v163, v143\n"
      "v_fma_f32 v8, v220, v220, s52\n"
      "v_mfma_f32_16x16x32_bf16 v[180:183], v[192:195], v[96:99], 0\n"
      "v_permlane16_swap_b32 v143, v163\n"
      "v_mfma_f32_16x16x32_bf16 v[180:183], v[80:83], v[88:91], v[180:183]\n"
      "ds_read_b128 v[184:187], v2 offset:27904\n"
      "v_max_f32 v143, v143, v163\n"
      "v_mfma_f32_16x16x32_bf16 v[152:155], v[164:167], v[96:99], 0\n"
      "ds_read_b128 v[96:99], v2 offset:30976\n"
      "v_mov_b32_e64 v163, v143\n"
      "v_mfma_f32_16x16x32_bf16 v[152:155], v[84:87], v[88:91], v[152:155]\n"
      "ds_read_b128 v[174:177], v2 offset:34048\n"
      "v_permlane32_swap_b32 v143, v163\n"
      "v_max_f32 v143, v143, v163\n"
      "v_sub_f32 v124, v124, v143\n"
      "v_sub_f32 v125, v125, v143\n"
      "v_sub_f32 v126, v126, v143\n"
      "v_sub_f32 v127, v127, v143\n"
      "v_exp_f32 v124, v124\n"
      "v_exp_f32 v125, v125\n"
      "v_sub_f32 v156, v156, v143\n"
      "v_sub_f32 v157, v157, v143\n"
      "v_sub_f32 v158, v158, v143\n"
      "v_sub_f32 v159, v159, v143\n"
      "v_exp_f32 v126, v126\n"
      "v_exp_f32 v127, v127\n"
      "v_exp_f32 v156, v156\n"
      "v_exp_f32 v157, v157\n"
      "v_exp_f32 v158, v158\n"
      "v_exp_f32 v159, v159\n"
      "v_cvt_pk_bf16_f32 v124, v124, v125\n"
      "v_cvt_pk_bf16_f32 v125, v126, v127\n"
      "v_cvt_pk_bf16_f32 v126, v156, v157\n"
      "v_cvt_pk_bf16_f32 v127, v158, v159\n"
      "v_mfma_f32_16x16x32_bf16 v[160:163], v[100:103], v[144:147], v[64:67]\n"
      "v_sub_f32 v188, v188, v143\n"
      "v_sub_f32 v189, v189, v143\n"
      "v_sub_f32 v190, v190, v143\n"
      "v_sub_f32 v191, v191, v143\n"
      "v_sub_f32 v140, v140, v143\n"
      "v_exp_f32 v188, v188\n"
      "v_exp_f32 v189, v189\n"
      "v_exp_f32 v190, v190\n"
      "v_exp_f32_e64 v191, v191\n"
      "v_exp_f32 v140, v140\n"
      "v_cvt_pk_bf16_f32 v92, v188, v189\n"
      "v_cvt_pk_bf16_f32 v93, v190, v191\n"
      "v_mfma_f32_16x16x32_bf16 v[156:159], v[200:203], v[144:147], v[68:71]\n"
      "s_waitcnt lgkmcnt(0)\n"
      "v_cvt_pk_bf16_f32 v94, v140, 0\n"
      "v_fmac_f32 v8, v221, v221\n"
      "v_fmac_f32 v8, v222, v222\n"
      "v_fmac_f32 v8, v223, v223\n"
      "v_fmac_f32 v8, v208, v208\n"
      "v_fmac_f32 v8, v209, v209\n"
      "v_fmac_f32 v8, v210, v210\n"
      "v_fmac_f32 v8, v211, v211\n"
      "v_fmac_f32 v8, v214, v214\n"
      "v_fmac_f32 v8, v215, v215\n"
      "v_fmac_f32 v8, v216, v216\n"
      "v_fmac_f32 v8, v217, v217\n"
      "v_rcp_f32_e64 v178, v178\n"
      "s_waitcnt vmcnt(5)\n"
      "v_or3_b32 v116, v116, v119, v39\n"
      "v_mul_f32 v134, v178, v134\n"
      "v_mul_f32 v135, v178, v135\n"
      "v_cvt_pk_bf16_f32 v134, v134, v135\n"
      "v_mul_f32 v136, v178, v136\n"
      "v_mul_f32 v137, v178, v137\n"
      "v_cvt_pk_bf16_f32 v135, v136, v137\n"
      "v_mul_f32 v180, v178, v180\n"
      "v_mul_f32 v181, v178, v181\n"
      "v_mul_f32 v182, v178, v182\n"
      "v_mul_f32 v183, v178, v183\n"
      "v_cvt_pk_bf16_f32 v136, v180, v181\n"
      "v_mfma_f32_16x16x32_bf16 v[140:143], v[128:131], v[144:147], v[72:75]\n"
      "v_cvt_pk_bf16_f32 v137, v182, v183\n"
      "v_mul_f32_e64 v152, v178, v152\n"
      "v_mfma_f32_16x16x32_bf16 v[188:191], v[196:199], v[144:147], v[28:31]\n"
      "ds_write_b128 v3, v[134:137]\n"
      "v_mfma_f32_16x16x16_bf16 v[160:163], v[132:133], v[138:139], v[160:163]\n"
      "v_mul_f32_e64 v153, v178, v153\n"
      "v_cvt_pk_bf16_f32 v152, v152, v153\n"
      "v_mul_f32_e64 v154, v178, v154\n"
      "v_mfma_f32_16x16x16_bf16 v[156:159], v[218:219], v[138:139], v[156:159]\n"
      "v_mul_f32_e64 v155, v178, v155\n"
      "v_cvt_pk_bf16_f32 v153, v154, v155\n"
      "v_and_b32 v116, 0x7fff7fff, v116\n"
      "v_mfma_f32_16x16x16_bf16 v[140:143], v[168:169], v[138:139], v[140:143]\n"
      "v_max3_f32 v88, v160, v161, v162\n"
      "v_readlane_b32 s45, v37, s43\n"
      "v_readlane_b32 s46, v37, s42\n"
      "v_mfma_f32_16x16x16_bf16 v[188:191], v[212:213], v[138:139], v[188:191]\n"
      "v_max3_f32 v139, v163, v156, v157\n"
      "v_max3_f32 v88, v88, v158, v159\n"
      "v_or3_b32 v104, v104, v105, v106\n"
      "v_mfma_f32_16x16x32_bf16 v[148:151], a[228:231], v[184:187], v[148:151]\n"
      "ds_write_b64 v4, v[152:153]\n"
      "v_mfma_f32_16x16x32_bf16 v[148:151], a[232:235], v[96:99], v[148:151]\n"
      "s_or_b32 s95, s45, s46\n"
      "v_max3_f32 v139, v139, v140, v141\n"
      "v_max3_f32 v88, v88, v142, v143\n"
      "v_mfma_f32_16x16x32_bf16 v[148:151], a[236:239], v[174:177], v[148:151]\n"
      "s_or_b32 s95, s95, s48\n"
      "v_max3_f32 v88, v88, v139, v188\n"
      "v_mov_b32_e64 v109, v88\n"
      "v_mfma_f32_16x16x32_bf16 v[170:173], a[252:255], v[184:187], v[170:173]\n"
      "ds_write_b32 v31, v116 offset:4608\n"
      "v_mfma_f32_16x16x32_bf16 v[170:173], v[224:227], v[96:99], v[170:173]\n"
      "s_cmp_ge_u32 s95, 0x40000000\n"
      "s_cselect_b32 s94, -1, -2\n"
      "v_fma_f32 v105, v148, v148, s52\n"
      "v_mfma_f32_16x16x32_bf16 v[170:173], v[228:231], v[174:177], v[170:173]\n"
      "s_cmp_gt_u32 s42, s44\n"
      "s_cselect_b32 s95, s94, s95\n"
      "v_fmac_f32 v105, v149, v149\n"
      "v_mfma_f32_16x16x32_bf16 v[120:123], v[244:247], v[184:187], v[120:123]\n"
      "ds_read_b128 v[136:139], v10\n"
      "v_fmac_f32 v105, v150, v150\n"
      "v_mfma_f32_16x16x32_bf16 v[120:123], v[248:251], v[96:99], v[120:123]\n"
      "s_add_u32 s42, s42, 1\n"
      "v_fmac_f32 v105, v151, v151\n"
      "v_fmac_f32 v105, v170, v170\n"
      "v_mfma_f32_16x16x32_bf16 v[120:123], v[252:255], v[174:177], v[120:123]\n"
      "v_fmac_f32 v105, v171, v171\n"
      "v_fmac_f32 v105, v172, v172\n"
      "v_mfma_f32_16x16x32_bf16 v[176:179], v[32:35], v[124:127], 0\n"
      "v_fmac_f32 v105, v173, v173\n"
      "v_permlane16_swap_b32_e64 v88, v109\n"
      "v_mfma_f32_16x16x32_bf16 v[176:179], v[32:35], v[92:95], v[176:179]\n"
      "ds_read_b64 v[110:111], v10 offset:16\n"
      "v_max_f32 v88, v88, v109\n"
      "v_mfma_f32_16x16x32_bf16 v[180:183], v[112:115], v[124:127], 0\n"
      "v_fmac_f32 v105, v120, v120\n"
      "v_fmac_f32_e64 v105, v121, v121\n"
      "v_mfma_f32_16x16x32_bf16 v[180:183], v[76:79], v[92:95], v[180:183]\n"
      "s_waitcnt lgkmcnt(1)\n"
      "v_fmac_f32 v105, v122, v122\n"
      "v_fmac_f32 v105, v123, v123\n"
      "v_mfma_f32_16x16x32_bf16 v[152:155], v[192:195], v[124:127], 0\n"
      "v_mov_b32 v109, v88\n"
      "v_permlane16_swap_b32 v8, v105\n"
      "v_mfma_f32_16x16x32_bf16 v[152:155], v[80:83], v[92:95], v[152:155]\n"
      "ds_read_b128 v[116:119], v2 offset:18944\n"
      "v_add_f32 v8, v8, v105\n"
      "v_mfma_f32_16x16x32_bf16 v[144:147], v[164:167], v[124:127], 0\n"
      "ds_read_b128 v[96:99], v2 offset:22016\n"
      "v_rcp_f32 v176, v176\n"
      "v_mfma_f32_16x16x32_bf16 v[144:147], v[84:87], v[92:95], v[144:147]\n"
      "ds_read_b128 v[124:127], v2 offset:25088\n"
      "v_permlane32_swap_b32 v88, v109\n"
      "v_mfma_f32_16x16x32_bf16 v[204:207], v[136:139], v[100:103], v[30:33]\n"
      "s_waitcnt lgkmcnt(3)\n"
      "v_mul_f32 v180, v176, v180\n"
      "v_mul_f32 v181, v176, v181\n"
      "v_mfma_f32_16x16x32_bf16 v[100:103], v[136:139], v[200:203], v[30:33]\n"
      "v_max_f32 v88, v88, v109\n"
      "v_cvt_pk_bf16_f32 v180, v180, v181\n"
      "v_mfma_f32_16x16x32_bf16 v[184:187], v[136:139], v[128:131], v[30:33]\n"
      "v_mul_f32 v182, v176, v182\n"
      "v_mul_f32 v183, v176, v183\n"
      "v_mfma_f32_16x16x32_bf16 v[200:203], v[136:139], v[196:199], v[30:33]\n"
      "s_waitcnt lgkmcnt(2)\n"
      "v_sub_f32 v160, v160, v88\n"
      "v_sub_f32 v161, v161, v88\n"
      "v_mfma_f32_16x16x16_bf16 v[204:207], v[110:111], v[132:133], v[204:207]\n"
      "v_cvt_pk_bf16_f32 v181, v182, v183\n"
      "v_mul_f32 v152, v176, v152\n"
      "v_mul_f32 v153, v176, v153\n"
      "v_mfma_f32_16x16x16_bf16 v[100:103], v[110:111], v[218:219], v[100:103]\n"
      "v_mul_f32 v154, v176, v154\n"
      "v_mul_f32_e64 v155, v176, v155\n"
      "v_sub_f32_e64 v162, v162, v88\n"
      "v_mfma_f32_16x16x16_bf16 v[184:187], v[110:111], v[168:169], v[184:187]\n"
      "v_sub_f32_e64 v163, v163, v88\n"
      "v_exp_f32_e64 v160, v160\n"
      "v_mfma_f32_16x16x16_bf16 v[200:203], v[110:111], v[212:213], v[200:203]\n"
      "v_exp_f32 v161, v161\n"
      "v_cndmask_b32 v204, v204, v100, s[58:59]\n"
      "v_mfma_f32_16x16x32_bf16 v[136:139], a[216:219], v[116:119], 0\n"
      "s_waitcnt lgkmcnt(0)\n"
      "v_cvt_pk_bf16_f32 v182, v152, v153\n"
      "v_cndmask_b32 v204, v204, v184, s[60:61]\n"
      "v_mfma_f32_16x16x32_bf16 v[136:139], a[220:223], v[96:99], v[136:139]\n"
      "ds_read_b128 v[100:103], v2 offset:28160\n"
      "v_cndmask_b32 v204, v204, v200, s[62:63]\n"
      "v_mfma_f32_16x16x32_bf16 v[136:139], a[224:227], v[124:127], v[136:139]\n"
      "ds_read_b128 v[130:133], v2 offset:31232\n"
      "v_max_f32_dpp v129, v204, v204 row_ror:8 row_mask:0xf bank_mask:0xf\n"
      "v_mfma_f32_16x16x32_bf16 v[196:199], a[240:243], v[116:119], 0\n"
      "ds_read_b128 v[108:111], v2 offset:34304\n"
      "v_max_f32_dpp v129, v129, v129 row_ror:4 row_mask:0xf bank_mask:0xf\n"
      "v_mfma_f32_16x16x32_bf16 v[196:199], a[244:247], v[96:99], v[196:199]\n"
      "v_cvt_pk_bf16_f32 v183, v154, v155\n"
      "v_max_f32_dpp v129, v129, v129 row_ror:2 row_mask:0xf bank_mask:0xf\n"
      "v_mfma_f32_16x16x32_bf16 v[196:199], a[248:251], v[124:127], v[196:199]\n"
      "ds_write_b128 v3, v[180:183] offset:256\n"
      "v_mfma_f32_16x16x32_bf16 v[178:181], v[232:235], v[116:119], 0\n"
      "s_waitcnt lgkmcnt(3)\n"
      "v_max_f32_dpp v129, v129, v129 row_ror:1 row_mask:0xf bank_mask:0xf\n"
      "v_sub_f32_e64 v156, v156, v88\n"
      "v_mfma_f32_16x16x32_bf16 v[178:181], v[236:239], v[96:99], v[178:181]\n"
      "s_waitcnt lgkmcnt(2)\n"
      "v_max_f32_dpp v129, v129, v129 row_bcast:15 row_mask:0xa bank_mask:0xf\n"
      "v_sub_f32 v157, v157, v88\n"
      "v_mfma_f32_16x16x32_bf16 v[178:181], v[240:243], v[124:127], v[178:181]\n"
      "s_waitcnt lgkmcnt(1)\n"
      "v_max_f32_dpp v129, v129, v129 row_bcast:31 row_mask:0xc bank_mask:0xf\n"
      "v_sub_f32 v158, v158, v88\n"
      "v_mfma_f32_16x16x32_bf16 v[136:139], a[228:231], v[100:103], v[136:139]\n"
      "v_readlane_b32 s72, v129, 63\n"
      "v_sub_f32 v159, v159, v88\n"
      "v_mfma_f32_16x16x32_bf16 v[136:139], a[232:235], v[130:133], v[136:139]\n"
      "v_subrev_f32 v204, s72, v204\n"
      "v_exp_f32 v162, v162\n"
      "v_mfma_f32_16x16x32_bf16 v[136:139], a[236:239], v[108:111], v[136:139]\n"
      "v_exp_f32 v163, v163\n"
      "v_mfma_f32_16x16x32_bf16 v[196:199], a[252:255], v[100:103], v[196:199]\n"
      "v_mul_f32 v144, v176, v144\n"
      "v_mul_f32 v145, v176, v145\n"
      "v_mfma_f32_16x16x32_bf16 v[196:199], v[224:227], v[130:133], v[196:199]\n"
      "v_exp_f32 v204, v204\n"
      "v_mfma_f32_16x16x32_bf16 v[196:199], v[228:231], v[108:111], v[196:199]\n"
      "v_exp_f32_e64 v156, v156\n"
      "v_mfma_f32_16x16x32_bf16 v[178:181], v[244:247], v[100:103], v[178:181]\n"
      "v_fma_f32 v100, v136, v136, s52\n"
      "v_fmac_f32 v100, v137, v137\n"
      "v_mfma_f32_16x16x32_bf16 v[178:181], v[248:251], v[130:133], v[178:181]\n"
      "v_fmac_f32 v100, v138, v138\n"
      "v_fmac_f32 v100, v139, v139\n"
      "v_mfma_f32_16x16x32_bf16 v[178:181], v[252:255], v[108:111], v[178:181]\n"
      "v_fmac_f32 v100, v196, v196\n"
      "v_fmac_f32 v100, v197, v197\n"
      "v_fmac_f32 v100, v198, v198\n"
      "v_fmac_f32 v100, v199, v199\n"
      "v_exp_f32 v157, v157\n"
      "v_exp_f32 v158, v158\n"
      "v_exp_f32 v159, v159\n"
      "v_cvt_pk_bf16_f32 v144, v144, v145\n"
      "v_fmac_f32 v100, v178, v178\n"
      "v_fmac_f32 v100, v179, v179\n"
      "v_fmac_f32 v100, v180, v180\n"
      "v_fmac_f32 v100, v181, v181\n"
      "v_mov_b32 v105, v100\n"
      "v_mul_f32 v146, v176, v146\n"
      "v_mul_f32 v147, v176, v147\n"
      "v_permlane16_swap_b32 v100, v105\n"
      "v_add_f32_e64 v100, v100, v105\n"
      "v_cvt_pk_bf16_f32 v160, v160, v161\n"
      "v_cvt_pk_bf16_f32 v145, v146, v147\n"
      "v_permlane32_swap_b32 v8, v100\n"
      "v_cvt_pk_bf16_f32 v161, v162, v163\n"
      "v_cvt_pk_bf16_f32 v204, v204, v204\n"
      "v_add_f32 v8, v8, v100\n"
      "v_cvt_pk_bf16_f32 v162, v156, v157\n"
      "v_cvt_pk_bf16_f32 v163, v158, v159\n"
      "v_sub_f32 v140, v140, v88\n"
      "v_sub_f32 v141, v141, v88\n"
      "v_mfma_f32_16x16x32_bf16 v[98:101], v[32:35], v[160:163], 0\n"
      "ds_write_b64 v4, v[144:145] offset:256\n"
      "v_sub_f32 v142, v142, v88\n"
      "v_sub_f32 v143, v143, v88\n"
      "v_sub_f32 v188, v188, v88\n"
      "v_exp_f32 v140, v140\n"
      "v_exp_f32 v141, v141\n"
      "v_exp_f32 v142, v142\n"
      "v_exp_f32 v143, v143\n"
      "v_exp_f32 v188, v188\n"
      "v_cvt_pk_bf16_f32 v88, v140, v141\n"
      "v_cvt_pk_bf16_f32 v89, v142, v143\n"
      "v_cvt_pk_bf16_f32 v90, v188, 0\n"
      "v_or_b32_e64 v104, v104, v107\n"
      "v_cmp_ne_u32 s[56:57], 0, v104\n"
      "v_mfma_f32_16x16x32_bf16 v[98:101], v[32:35], v[88:91], v[98:101]\n"
      "ds_write_b16 v13, v204\n"
      "v_mfma_f32_16x16x32_bf16 v[140:143], v[112:115], v[160:163], 0\n"
      "ds_read_b128 v[104:107], v14\n"
      "v_readlane_b32 s47, v38, s43\n"
      "v_mfma_f32_16x16x32_bf16 v[140:143], v[76:79], v[88:91], v[140:143]\n"
      "ds_write_b32 v31, v8\n"
      "v_mfma_f32_16x16x32_bf16 v[188:191], v[192:195], v[160:163], 0\n"
      "ds_read_b128 v[156:159], v14 offset:64\n"
      "v_rcp_f32_e64 v98, v98\n"
      "v_mfma_f32_16x16x32_bf16 v[188:191], v[80:83], v[88:91], v[188:191]\n"
      "s_cmp_ge_u32 s95, 0x40000000\n"
      "s_cselect_b64 vcc, -1, s[56:57]\n"
      "v_add_u32 v12, 32, v12\n"
      "v_mfma_f32_16x16x32_bf16 v[126:129], v[164:167], v[160:163], 0\n"
      "s_and_b32 s47, s47, -4\n"
      "v_mul_f32 v140, v98, v140\n"
      "v_mul_f32 v141, v98, v141\n"
      "v_mfma_f32_16x16x32_bf16 v[126:129], v[84:87], v[88:91], v[126:129]\n"
      "s_add_u32 s43, s43, 1\n"
      "v_cvt_pk_bf16_f32 v140, v140, v141\n"
      "v_mul_f32 v142, v98, v142\n"
      "v_mul_f32 v143, v98, v143\n"
      "v_cvt_pk_bf16_f32 v141, v142, v143\n"
      "v_mul_f32 v188, v98, v188\n"
      "s_waitcnt lgkmcnt(2)\n"
      "v_mfma_f32_16x16x32_bf16 v[100:103], v[104:107], v[32:35], 0\n"
      "v_mul_f32 v189, v98, v189\n"
      "v_mul_f32 v190, v98, v190\n"
      "v_mul_f32_e64 v191, v98, v191\n"
      "v_cvt_pk_bf16_f32 v142, v188, v189\n"
      "v_cvt_pk_bf16_f32 v143, v190, v191\n"
      "s_waitcnt lgkmcnt(0)\n"
      "v_mfma_f32_16x16x32_bf16 v[100:103], v[156:159], v[32:35], v[100:103]\n"
      "ds_write_b128 v3, v[140:143] offset:512\n"
      "v_mul_f32 v126, v98, v126\n"
      "v_mul_f32 v127, v98, v127\n"
      "v_cvt_pk_bf16_f32 v126, v126, v127\n"
      "v_mul_f32_e64 v128, v98, v128\n"
      "v_mul_f32_e64 v129, v98, v129\n"
      "v_cvt_pk_bf16_f32 v127, v128, v129\n"
      "ds_write_b64 v4, v[126:127] offset:512\n"
      "s_waitcnt lgkmcnt(0)\n"
      "s_barrier\n"
      "s_cbranch_vccz L_max1_%=\n"
      "L_cold_%=:\n"
      "L_disp1_%=:\n"
      "s_cmp_lt_u32 s95, 0x40000000\n"
      "s_cbranch_scc1 L_nofix_11_%=\n"
      "s_cmp_eq_u32 s95, -2\n"
      "s_cbranch_scc1 L_tail1_%=\n"
      "s_nop 2\n"
      "v_lshlrev_b32 v126, 3, v36\n"
      "s_add_u32 s72, s98, L_cold_%=-L_ipf0_%=\n"
      "s_addc_u32 s73, s99, 0\n"
      "s_mov_b32 s74, L_cend_%=-L_cold_%=\n"
      "s_mov_b32 s75, 0x20000\n"
      "s_mov_b32 m0, 161792\n"
      "s_nop 0\n"
      "buffer_load_dwordx4 v126, s[72:75], 0 offen  lds\n"
      "s_lshr_b32 s92, s45, 30\n"
      "s_or_b32 s92, s92, s66\n"
      "s_cbranch_scc0 L_nf_12_%=\n"
      "s_bitcmp1_b32 s45, 31\n"
      "s_cselect_b32 s75, 100270464, 0\n"
      "s_bitcmp1_b32 s45, 30\n"
      "s_cselect_b32 s76, 196224, 0\n"
      "s_and_b32 s74, s45, 0x3fffffff\n"
      "s_cmp_ge_u32 s45, 0x40000000\n"
      "s_cselect_b32 s74, s74, 0\n"
      "s_cselect_b32 s45, 0, s45\n"
      "s_sub_u32 s74, s74, s66\n"
      "s_add_u32 s66, s66, s74\n"
      "s_sub_u32 s75, s75, s67\n"
      "s_add_u32 s67, s67, s75\n"
      "s_sub_u32 s76, s76, s68\n"
      "s_add_u32 s68, s68, s76\n"
      "v_add_u32 v16, s74, v16\n"
      "v_add_u32 v17, s74, v17\n"
      "v_add_u32 v18, s74, v18\n"
      "v_add_u32 v19, s74, v19\n"
      "v_add_u32 v15, s74, v15\n"
      "s_cmp_eq_u32 s75, 0\n"
      "s_cbranch_scc1 L_ax_13_%=\n"
      "v_bfe_i32 v126, v27, 0, 1\n"
      "v_and_b32 v126, s75, v126\n"
      "v_sub_u32 v16, v16, v126\n"
      "v_bfe_i32 v126, v27, 2, 1\n"
      "v_and_b32 v126, s75, v126\n"
      "v_sub_u32 v17, v17, v126\n"
      "v_bfe_i32 v126, v27, 4, 1\n"
      "v_and_b32 v126, s75, v126\n"
      "v_sub_u32 v18, v18, v126\n"
      "v_bfe_i32 v126, v27, 6, 1\n"
      "v_and_b32 v126, s75, v126\n"
      "v_sub_u32 v19, v19, v126\n"
      "v_bfe_i32 v126, v27, 8, 1\n"
      "v_and_b32 v126, s75, v126\n"
      "v_sub_u32 v15, v15, v126\n"
      "L_ax_13_%=:\n"
      "s_cmp_eq_u32 s76, 0\n"
      "s_cbranch_scc1 L_ax_14_%=\n"
      "v_bfe_i32 v126, v27, 1, 1\n"
      "v_and_b32 v126, s76, v126\n"
      "v_sub_u32 v16, v16, v126\n"
      "v_bfe_i32 v126, v27, 3, 1\n"
      "v_and_b32 v126, s76, v126\n"
      "v_sub_u32 v17, v17, v126\n"
      "v_bfe_i32 v126, v27, 5, 1\n"
      "v_and_b32 v126, s76, v126\n"
      "v_sub_u32 v18, v18, v126\n"
      "v_bfe_i32 v126, v27, 7, 1\n"
      "v_and_b32 v126, s76, v126\n"
      "v_sub_u32 v19, v19, v126\n"
      "v_bfe_i32 v126, v27, 9, 1\n"
      "v_and_b32 v126, s76, v126\n"
      "v_sub_u32 v15, v15, v126\n"
      "L_ax_14_%=:\n"
      "s_or_b32 s48, s66, s69\n"
      "s_cmp_lg_u32 s48, 0\n"
      "s_cselect_b32 s48, 0x40000000, 0\n"
      "L_nf_12_%=:\n"
      "s_lshr_b32 s92, s46, 30\n"
      "s_or_b32 s92, s92, s69\n"
      "s_cbranch_scc0 L_nf_15_%=\n"
      "s_bitcmp1_b32 s46, 31\n"
      "s_cselect_b32 s75, 100270464, 0\n"
      "s_bitcmp1_b32 s46, 30\n"
      "s_cselect_b32 s76, 196224, 0\n"
      "s_and_b32 s74, s46, 0x3fffffff\n"
      "s_cmp_ge_u32 s46, 0x40000000\n"
      "s_cselect_b32 s74, s74, 0\n"
      "s_cselect_b32 s46, 0, s46\n"
      "s_sub_u32 s74, s74, s69\n"
      "s_add_u32 s69, s69, s74\n"
      "s_sub_u32 s75, s75, s70\n"
      "s_add_u32 s70, s70, s75\n"
      "s_sub_u32 s76, s76, s71\n"
      "s_add_u32 s71, s71, s76\n"
      "v_add_u32 v20, s74, v20\n"
      "v_add_u32 v21, s74, v21\n"
      "v_add_u32 v22, s74, v22\n"
      "v_add_u32 v23, s74, v23\n"
      "v_add_u32 v25, s74, v25\n"
      "s_cmp_eq_u32 s75, 0\n"
      "s_cbranch_scc1 L_ax_16_%=\n"
      "v_bfe_i32 v126, v27, 10, 1\n"
      "v_and_b32 v126, s75, v126\n"
      "v_sub_u32 v20, v20, v126\n"
      "v_bfe_i32 v126, v27, 12, 1\n"
      "v_and_b32 v126, s75, v126\n"
      "v_sub_u32 v21, v21, v126\n"
      "v_bfe_i32 v126, v27, 14, 1\n"
      "v_and_b32 v126, s75, v126\n"
      "v_sub_u32 v22, v22, v126\n"
      "v_bfe_i32 v126, v27, 16, 1\n"
      "v_and_b32 v126, s75, v126\n"
      "v_sub_u32 v23, v23, v126\n"
      "v_bfe_i32 v126, v27, 18, 1\n"
      "v_and_b32 v126, s75, v126\n"
      "v_sub_u32 v25, v25, v126\n"
      "L_ax_16_%=:\n"
      "s_cmp_eq_u32 s76, 0\n"
      "s_cbranch_scc1 L_ax_17_%=\n"
      "v_bfe_i32 v126, v27, 11, 1\n"
      "v_and_b32 v126, s76, v126\n"
      "v_sub_u32 v20, v20, v126\n"
      "v_bfe_i32 v126, v27, 13, 1\n"
      "v_and_b32 v126, s76, v126\n"
      "v_sub_u32 v21, v21, v126\n"
      "v_bfe_i32 v126, v27, 15, 1\n"
      "v_and_b32 v126, s76, v126\n"
      "v_sub_u32 v22, v22, v126\n"
      "v_bfe_i32 v126, v27, 17, 1\n"
      "v_and_b32 v126, s76, v126\n"
      "v_sub_u32 v23, v23, v126\n"
      "v_bfe_i32 v126, v27, 19, 1\n"
      "v_and_b32 v126, s76, v126\n"
      "v_sub_u32 v25, v25, v126\n"
      "L_ax_17_%=:\n"
      "s_or_b32 s48, s66, s69\n"
      "s_cmp_lg_u32 s48, 0\n"
      "s_cselect_b32 s48, 0x40000000, 0\n"
      "L_nf_15_%=:\n"
      "s_sub_u32 s92, s42, 1\n"
      "s_cmp_gt_u32 s92, s44\n"
      "s_cbranch_scc1 L_tail1_%=\n"
      "L_nofix_11_%=:\n"
      "s_cmp_lg_u64 s[56:57], 0\n"
      "s_cbranch_scc1 L_slow1_%=\n"
      "L_sel1_%=:\n"
      "s_cmp_lg_u64 s[54:55], 0\n"
      "s_cbranch_scc1 L_max1_%=\n"
      "s_branch L_loop1_%=\n"
      "L_slow1_%=:\n"
      "s_nop 4\n"
      "v_mfma_f32_16x16x32_bf16 v[140:143], v[104:107], v[112:115], 0\n"
      "ds_read_b128 v[126:129], v0 offset:18432\n"
      "v_subrev_u32 v204, 115456, v9\n"
      "v_mfma_f32_16x16x32_bf16 v[140:143], v[156:159], v[76:79], v[140:143]\n"
      "ds_read_b128 v[160:163], v1 offset:18432\n"
      "v_subrev_u32 v76, 115456, v9\n"
      "v_mfma_f32_16x16x32_bf16 v[112:115], v[104:107], v[192:195], 0\n"
      "ds_read_b128 v[182:185], v0 offset:24576\n"
      "v_bfe_u32 v77, v76, 4, 4\n"
      "v_mfma_f32_16x16x32_bf16 v[112:115], v[156:159], v[80:83], v[112:115]\n"
      "ds_read_b128 v[96:99], v9\n"
      "v_mul_u32_u24 v88, 98, v77\n"
      "v_mfma_f32_16x16x32_bf16 v[190:193], v[104:107], v[164:167], 0\n"
      "s_add_u32 s72, s42, 1\n"
      "v_readlane_b32 s77, v38, s72\n"
      "v_lshrrev_b32 v76, 8, v76\n"
      "v_mfma_f32_16x16x32_bf16 v[190:193], v[156:159], v[84:87], v[190:193]\n"
      "s_mov_b32 m0, 110592\n"
      "v_mov_b32_dpp v100, v100 quad_perm:[0,0,0,0] row_mask:0xf bank_mask:0xf\n"
      "v_rcp_f32_e64 v100, v100\n"
      "s_waitcnt lgkmcnt(3)\n"
      "v_mfma_f32_16x16x32_bf16 v[104:107], a[0:3], v[126:129], 0\n"
      "s_and_b32 s73, s77, -16\n"
      "v_cndmask_b32 v140, v140, v112, s[58:59]\n"
      "v_lshl_add_u32 v88, v76, 3, v88\n"
      "v_mfma_f32_16x16x32_bf16 v[156:159], a[24:27], v[126:129], 0\n"
      "buffer_load_dwordx4 v204, s[24:27], s73 offen  lds\n"
      "v_mfma_f32_16x16x32_bf16 v[174:177], a[48:51], v[126:129], 0\n"
      "s_mov_b32 m0, 111616\n"
      "s_waitcnt lgkmcnt(0)\n"
      "v_add_f32 v96, v96, v97\n"
      "v_mfma_f32_16x16x32_bf16 v[144:147], a[72:75], v[126:129], 0\n"
      "ds_read_b128 v[116:119], v1 offset:24576\n"
      "v_add_f32 v98, v98, v99\n"
      "v_mfma_f32_16x16x32_bf16 v[108:111], a[96:99], v[126:129], 0\n"
      "ds_read_b128 v[130:133], v0 offset:30720\n"
      "v_add_f32 v96, v96, v98\n"
      "v_mfma_f32_16x16x32_bf16 v[186:189], a[120:123], v[126:129], 0\n"
      "ds_read_b128 v[152:155], v1 offset:30720\n"
      "v_rsq_f32_e64 v96, v96\n"
      "v_mfma_f32_16x16x32_bf16 v[104:107], a[4:7], v[160:163], v[104:107]\n"
      "s_add_u32 s73, s73, 1024\n"
      "v_mul_f32 v96, 0x415db3d7, v96\n"
      "v_mov_b32_e64 v98, v96\n"
      "v_mfma_f32_16x16x32_bf16 v[104:107], a[8:11], v[182:185], v[104:107]\n"
      "buffer_load_dwordx4 v204, s[24:27], s73 offen  lds\n"
      "v_mfma_f32_16x16x32_bf16 v[156:159], a[28:31], v[160:163], v[156:159]\n"
      "s_mov_b32 m0, 112640\n"
      "v_permlane32_swap_b32_e64 v96, v98\n"
      "v_mfma_f32_16x16x32_bf16 v[156:159], a[32:35], v[182:185], v[156:159]\n"
      "s_add_u32 s73, s73, 1024\n"
      "v_mov_b32 v97, v96\n"
      "v_mul_f32 v136, v98, v136\n"
      "v_mfma_f32_16x16x32_bf16 v[174:177], a[52:55], v[160:163], v[174:177]\n"
      "s_and_b32 s77, s77, 15\n"
      "v_permlane16_swap_b32 v96, v97\n"
      "v_mfma_f32_16x16x32_bf16 v[174:177], a[56:59], v[182:185], v[174:177]\n"
      "buffer_load_dwordx4 v204, s[24:27], s73 offen  lds\n"
      "v_mfma_f32_16x16x32_bf16 v[144:147], a[76:79], v[160:163], v[144:147]\n"
      "s_mov_b32 m0, 113664\n"
      "v_mul_f32 v220, v96, v220\n"
      "v_mul_f32 v221, v96, v221\n"
      "v_mfma_f32_16x16x32_bf16 v[144:147], a[80:83], v[182:185], v[144:147]\n"
      "s_add_u32 s73, s73, 1024\n"
      "v_cvt_pk_bf16_f32 v220, v220, v221\n"
      "v_mul_f32 v222, v96, v222\n"
      "v_mfma_f32_16x16x32_bf16 v[108:111], a[100:103], v[160:163], v[108:111]\n"
      "s_waitcnt lgkmcnt(0)\n"
      "v_mul_f32 v223, v96, v223\n"
      "v_mul_f32 v214, v96, v214\n"
      "v_mfma_f32_16x16x32_bf16 v[108:111], a[104:107], v[182:185], v[108:111]\n"
      "buffer_load_dwordx4 v204, s[24:27], s73 offen  lds\n"
      "v_mfma_f32_16x16x32_bf16 v[186:189], a[124:127], v[160:163], v[186:189]\n"
      "s_mov_b32 m0, 114688\n"
      "v_mul_f32 v215, v96, v215\n"
      "v_cvt_pk_bf16_f32 v221, v222, v223\n"
      "v_mfma_f32_16x16x32_bf16 v[186:189], a[128:131], v[182:185], v[186:189]\n"
      "s_add_u32 s73, s73, 1024\n"
      "v_mul_f32 v208, v96, v208\n"
      "v_mul_f32 v209, v96, v209\n"
      "v_mfma_f32_16x16x32_bf16 v[104:107], a[12:15], v[116:119], v[104:107]\n"
      "v_mul_f32 v210, v96, v210\n"
      "v_mul_f32_e64 v211, v96, v211\n"
      "v_mfma_f32_16x16x32_bf16 v[104:107], a[16:19], v[130:133], v[104:107]\n"
      "s_mov_b32 exec_hi, 0xffff\n"
      "buffer_load_dwordx4 v204, s[24:27], s73 offen  lds\n"
      "s_mov_b32 exec_hi, -1\n"
      "v_mfma_f32_16x16x32_bf16 v[104:107], a[20:23], v[152:155], v[104:107]\n"
      "s_add_u32 m0, s64, 0\n"
      "v_cvt_pk_bf16_f32 v214, v214, v215\n"
      "v_mul_f32 v216, v96, v216\n"
      "v_mfma_f32_16x16x32_bf16 v[156:159], a[36:39], v[116:119], v[156:159]\n"
      "s_add_u32 s73, s73, 1024\n"
      "v_mul_f32 v217, v96, v217\n"
      "v_cvt_pk_bf16_f32 v222, v208, v209\n"
      "v_mfma_f32_16x16x32_bf16 v[156:159], a[40:43], v[130:133], v[156:159]\n"
      "v_cvt_pk_bf16_f32 v223, v210, v211\n"
      "v_cvt_pk_bf16_f32 v215, v216, v217\n"
      "v_mfma_f32_16x16x32_bf16 v[156:159], a[44:47], v[152:155], v[156:159]\n"
      "ds_write_b128 v5, v[220:223] offset:18432\n"
      "v_mfma_f32_16x16x32_bf16 v[174:177], a[60:63], v[116:119], v[174:177]\n"
      "buffer_load_dwordx4 v16, s[16:19], s45 offen  lds\n"
      "v_mfma_f32_16x16x32_bf16 v[174:177], a[64:67], v[130:133], v[174:177]\n"
      "s_add_u32 m0, s64, 4096\n"
      "v_mul_f32 v148, v97, v148\n"
      "v_mul_f32 v149, v97, v149\n"
      "v_mfma_f32_16x16x32_bf16 v[174:177], a[68:71], v[152:155], v[174:177]\n"
      "ds_write_b64 v6, v[214:215] offset:18432\n"
      "v_mfma_f32_16x16x32_bf16 v[144:147], a[84:87], v[116:119], v[144:147]\n"
      "v_cvt_pk_bf16_f32 v148, v148, v149\n"
      "v_mul_f32_e64 v150, v97, v150\n"
      "v_mfma_f32_16x16x32_bf16 v[144:147], a[88:91], v[130:133], v[144:147]\n"
      "buffer_load_dwordx4 v17, s[16:19], s45 offen  lds\n"
      "v_mfma_f32_16x16x32_bf16 v[144:147], a[92:95], v[152:155], v[144:147]\n"
      "s_add_u32 m0, s64, 8192\n"
      "v_mul_f32 v151, v97, v151\n"
      "v_mul_f32 v120, v97, v120\n"
      "v_mfma_f32_16x16x32_bf16 v[108:111], a[108:111], v[116:119], v[108:111]\n"
      "v_mul_f32_e64 v121, v97, v121\n"
      "v_cvt_pk_bf16_f32 v149, v150, v151\n"
      "v_mfma_f32_16x16x32_bf16 v[108:111], a[112:115], v[130:133], v[108:111]\n"
      "v_mul_f32 v170, v97, v170\n"
      "v_mul_f32 v171, v97, v171\n"
      "v_mfma_f32_16x16x32_bf16 v[108:111], a[116:119], v[152:155], v[108:111]\n"
      "buffer_load_dwordx4 v18, s[16:19], s45 offen  lds\n"
      "v_mfma_f32_16x16x32_bf16 v[186:189], a[132:135], v[116:119], v[186:189]\n"
      "s_add_u32 m0, s64, 12288\n"
      "v_mul_f32 v172, v97, v172\n"
      "v_mul_f32 v173, v97, v173\n"
      "v_mfma_f32_16x16x32_bf16 v[186:189], a[136:139], v[130:133], v[186:189]\n"
      "v_cvt_pk_bf16_f32 v120, v120, v121\n"
      "v_mul_f32 v122, v97, v122\n"
      "v_mfma_f32_16x16x32_bf16 v[186:189], a[140:143], v[152:155], v[186:189]\n"
      "v_mul_f32 v123, v97, v123\n"
      "v_cvt_pk_bf16_f32 v150, v170, v171\n"
      "v_mfma_f32_16x16x32_bf16 v[112:115], v[126:129], a[144:147], 0\n"
      "buffer_load_dwordx4 v19, s[16:19], s45 offen  lds\n"
      "v_mfma_f32_16x16x32_bf16 v[112:115], v[160:163], a[148:151], v[112:115]\n"
      "s_add_u32 m0, s65, 16384\n"
      "v_cvt_pk_bf16_f32 v151, v172, v173\n"
      "v_cvt_pk_bf16_f32 v121, v122, v123\n"
      "v_mfma_f32_16x16x32_bf16 v[112:115], v[182:185], a[152:155], v[112:115]\n"
      "ds_write_b128 v5, v[148:151] offset:24576\n"
      "v_mfma_f32_16x16x32_bf16 v[112:115], v[116:119], a[156:159], v[112:115]\n"
      "buffer_load_dwordx4 v15, s[16:19], s45 offen  lds\n"
      "v_mfma_f32_16x16x32_bf16 v[112:115], v[130:133], a[160:163], v[112:115]\n"
      "ds_read_b128 v[220:223], v0 offset:20480\n"
      "v_mul_f32_e64 v137, v98, v137\n"
      "v_mfma_f32_16x16x32_bf16 v[112:115], v[152:155], a[164:167], v[112:115]\n"
      "ds_write_b64 v6, v[120:121] offset:24576\n"
      "v_mfma_f32_16x16x32_bf16 v[192:195], v[126:129], a[168:171], 0\n"
      "ds_read_b128 v[212:215], v1 offset:20480\n"
      "v_cvt_pk_bf16_f32 v136, v136, v137\n"
      "v_mfma_f32_16x16x32_bf16 v[192:195], v[160:163], a[172:175], v[192:195]\n"
      "ds_read_b128 v[216:219], v0 offset:26624\n"
      "v_mul_f32 v138, v98, v138\n"
      "v_mfma_f32_16x16x32_bf16 v[192:195], v[182:185], a[176:179], v[192:195]\n"
      "ds_read_b128 v[148:151], v1 offset:26624\n"
      "v_mul_f32 v139, v98, v139\n"
      "v_mfma_f32_16x16x32_bf16 v[192:195], v[116:119], a[180:183], v[192:195]\n"
      "ds_read_b128 v[170:173], v0 offset:32768\n"
      "v_mul_f32 v178, v98, v178\n"
      "v_mfma_f32_16x16x32_bf16 v[192:195], v[130:133], a[184:187], v[192:195]\n"
      "ds_read_b128 v[204:207], v1 offset:32768\n"
      "v_mul_f32 v179, v98, v179\n"
      "v_mfma_f32_16x16x32_bf16 v[192:195], v[152:155], a[188:191], v[192:195]\n"
      "buffer_load_dwordx4 v[120:123], v36, s[24:27], s47 offen\n"
      "v_mfma_f32_16x16x32_bf16 v[164:167], v[126:129], a[192:195], 0\n"
      "v_cvt_pk_bf16_f32 v137, v138, v139\n"
      "v_mul_f32 v196, v98, v196\n"
      "v_mfma_f32_16x16x32_bf16 v[164:167], v[160:163], a[196:199], v[164:167]\n"
      "v_mul_f32 v197, v98, v197\n"
      "v_mul_f32 v198, v98, v198\n"
      "v_mfma_f32_16x16x32_bf16 v[164:167], v[182:185], a[200:203], v[164:167]\n"
      "s_waitcnt lgkmcnt(3)\n"
      "v_mul_f32_e64 v199, v98, v199\n"
      "v_cvt_pk_bf16_f32 v178, v178, v179\n"
      "v_mfma_f32_16x16x32_bf16 v[164:167], v[116:119], a[204:207], v[164:167]\n"
      "buffer_load_dword v89, v29, s[24:27], s47 offen\n"
      "v_mfma_f32_16x16x32_bf16 v[164:167], v[130:133], a[208:211], v[164:167]\n"
      "v_mul_f32 v180, v98, v180\n"
      "v_mul_f32_e64 v181, v98, v181\n"
      "v_mfma_f32_16x16x32_bf16 v[164:167], v[152:155], a[212:215], v[164:167]\n"
      "s_waitcnt lgkmcnt(0)\n"
      "v_cvt_pk_bf16_f32 v138, v196, v197\n"
      "v_cvt_pk_bf16_f32 v139, v198, v199\n"
      "v_mfma_f32_16x16x32_bf16 v[128:131], a[0:3], v[220:223], 0\n"
      "ds_write_b128 v5, v[136:139] offset:30720\n"
      "v_mfma_f32_16x16x32_bf16 v[128:131], a[4:7], v[212:215], v[128:131]\n"
      "v_cvt_pk_bf16_f32 v179, v180, v181\n"
      "v_fma_f32 v185, v104, v104, s51\n"
      "v_mfma_f32_16x16x32_bf16 v[128:131], a[8:11], v[216:219], v[128:131]\n"
      "v_fma_f32 v127, v144, v144, s51\n"
      "v_fmac_f32_e64 v185, v105, v105\n"
      "v_mfma_f32_16x16x32_bf16 v[128:131], a[12:15], v[148:151], v[128:131]\n"
      "ds_write_b64 v6, v[178:179] offset:30720\n"
      "v_mfma_f32_16x16x32_bf16 v[128:131], a[16:19], v[170:173], v[128:131]\n"
      "v_fmac_f32 v127, v145, v145\n"
      "v_fmac_f32 v185, v106, v106\n"
      "v_mfma_f32_16x16x32_bf16 v[128:131], a[20:23], v[204:207], v[128:131]\n"
      "v_fmac_f32 v127, v146, v146\n"
      "v_fmac_f32 v185, v107, v107\n"
      "v_mfma_f32_16x16x32_bf16 v[96:99], a[24:27], v[220:223], 0\n"
      "v_fmac_f32 v127, v147, v147\n"
      "v_fmac_f32 v185, v156, v156\n"
      "v_mfma_f32_16x16x32_bf16 v[96:99], a[28:31], v[212:215], v[96:99]\n"
      "v_fmac_f32 v127, v108, v108\n"
      "v_fmac_f32 v185, v157, v157\n"
      "v_mfma_f32_16x16x32_bf16 v[96:99], a[32:35], v[216:219], v[96:99]\n"
      "v_fmac_f32 v127, v109, v109\n"
      "v_fmac_f32 v185, v158, v158\n"
      "v_mfma_f32_16x16x32_bf16 v[96:99], a[36:39], v[148:151], v[96:99]\n"
      "v_fmac_f32 v127, v110, v110\n"
      "v_fmac_f32 v185, v159, v159\n"
      "v_mfma_f32_16x16x32_bf16 v[96:99], a[40:43], v[170:173], v[96:99]\n"
      "v_fmac_f32 v127, v111, v111\n"
      "v_fmac_f32 v185, v174, v174\n"
      "v_mfma_f32_16x16x32_bf16 v[96:99], a[44:47], v[204:207], v[96:99]\n"
      "v_fmac_f32 v127, v186, v186\n"
      "v_fmac_f32 v185, v175, v175\n"
      "v_mfma_f32_16x16x32_bf16 v[116:119], a[48:51], v[220:223], 0\n"
      "v_fmac_f32 v127, v187, v187\n"
      "v_fmac_f32 v185, v176, v176\n"
      "v_mfma_f32_16x16x32_bf16 v[116:119], a[52:55], v[212:215], v[116:119]\n"
      "v_fmac_f32 v127, v188, v188\n"
      "v_fmac_f32 v185, v177, v177\n"
      "v_mfma_f32_16x16x32_bf16 v[116:119], a[56:59], v[216:219], v[116:119]\n"
      "v_fmac_f32 v127, v189, v189\n"
      "v_cndmask_b32 v140, v140, v190, s[60:61]\n"
      "v_mfma_f32_16x16x32_bf16 v[116:119], a[60:63], v[148:151], v[116:119]\n"
      "v_permlane16_swap_b32 v185, v127\n"
      "v_mfma_f32_16x16x32_bf16 v[116:119], a[64:67], v[170:173], v[116:119]\n"
      "v_add_f32 v185, v185, v127\n"
      "v_mov_b32 v127, v185\n"
      "v_mfma_f32_16x16x32_bf16 v[116:119], a[68:71], v[204:207], v[116:119]\n"
      "v_mul_f32 v140, v100, v140\n"
      "v_permlane32_swap_b32 v185, v127\n"
      "v_mfma_f32_16x16x32_bf16 v[152:155], a[72:75], v[220:223], 0\n"
      "v_add_f32 v185, v185, v127\n"
      "v_rsq_f32 v185, v185\n"
      "v_mfma_f32_16x16x32_bf16 v[152:155], a[76:79], v[212:215], v[152:155]\n"
      "v_mov_b32_e64 v127, v185\n"
      "v_cvt_pk_bf16_f32 v140, v140, v140\n"
      "v_mfma_f32_16x16x32_bf16 v[152:155], a[80:83], v[216:219], v[152:155]\n"
      "ds_write_b16 v12, v140\n"
      "v_mfma_f32_16x16x32_bf16 v[152:155], a[84:87], v[148:151], v[152:155]\n"
      "v_permlane16_swap_b32 v185, v127\n"
      "v_mfma_f32_16x16x32_bf16 v[152:155], a[88:91], v[170:173], v[152:155]\n"
      "v_mul_f32 v185, s50, v185\n"
      "v_mul_f32 v144, v127, v144\n"
      "v_mfma_f32_16x16x32_bf16 v[152:155], a[92:95], v[204:207], v[152:155]\n"
      "v_mul_f32 v145, v127, v145\n"
      "v_mul_f32 v104, v185, v104\n"
      "v_mfma_f32_16x16x32_bf16 v[178:181], a[96:99], v[220:223], 0\n"
      "v_mul_f32 v105, v185, v105\n"
      "v_cvt_pk_bf16_f32 v144, v144, v145\n"
      "v_mfma_f32_16x16x32_bf16 v[178:181], a[100:103], v[212:215], v[178:181]\n"
      "v_mul_f32 v146, v127, v146\n"
      "v_mul_f32 v147, v127, v147\n"
      "v_mfma_f32_16x16x32_bf16 v[178:181], a[104:107], v[216:219], v[178:181]\n"
      "v_mul_f32 v186, v127, v186\n"
      "v_mul_f32 v187, v127, v187\n"
      "v_mfma_f32_16x16x32_bf16 v[178:181], a[108:111], v[148:151], v[178:181]\n"
      "v_cvt_pk_bf16_f32 v104, v104, v105\n"
      "v_mul_f32 v106, v185, v106\n"
      "v_mfma_f32_16x16x32_bf16 v[178:181], a[112:115], v[170:173], v[178:181]\n"
      "v_mul_f32 v107, v185, v107\n"
      "v_mul_f32_e64 v174, v185, v174\n"
      "v_mfma_f32_16x16x32_bf16 v[178:181], a[116:119], v[204:207], v[178:181]\n"
      "v_mul_f32 v175, v185, v175\n"
      "v_cvt_pk_bf16_f32 v145, v146, v147\n"
      "v_mfma_f32_16x16x32_bf16 v[200:203], a[120:123], v[220:223], 0\n"
      "v_mul_f32 v108, v127, v108\n"
      "v_mul_f32 v109, v127, v109\n"
      "v_mfma_f32_16x16x32_bf16 v[200:203], a[124:127], v[212:215], v[200:203]\n"
      "v_mul_f32 v110, v127, v110\n"
      "v_mul_f32_e64 v111, v127, v111\n"
      "v_mfma_f32_16x16x32_bf16 v[200:203], a[128:131], v[216:219], v[200:203]\n"
      "v_cvt_pk_bf16_f32 v186, v186, v187\n"
      "v_mul_f32 v188, v127, v188\n"
      "v_mfma_f32_16x16x32_bf16 v[200:203], a[132:135], v[148:151], v[200:203]\n"
      "v_mul_f32 v189, v127, v189\n"
      "v_cvt_pk_bf16_f32 v105, v106, v107\n"
      "v_mfma_f32_16x16x32_bf16 v[200:203], a[136:139], v[170:173], v[200:203]\n"
      "v_mul_f32 v156, v185, v156\n"
      "v_mul_f32 v157, v185, v157\n"
      "v_mfma_f32_16x16x32_bf16 v[200:203], a[140:143], v[204:207], v[200:203]\n"
      "v_mul_f32 v158, v185, v158\n"
      "v_mul_f32 v159, v185, v159\n"
      "v_mfma_f32_16x16x32_bf16 v[124:127], v[220:223], a[144:147], 0\n"
      "v_cvt_pk_bf16_f32 v174, v174, v175\n"
      "v_mul_f32 v176, v185, v176\n"
      "v_mfma_f32_16x16x32_bf16 v[124:127], v[212:215], a[148:151], v[124:127]\n"
      "v_mul_f32 v177, v185, v177\n"
      "v_cvt_pk_bf16_f32 v146, v108, v109\n"
      "v_mfma_f32_16x16x32_bf16 v[124:127], v[216:219], a[152:155], v[124:127]\n"
      "v_cvt_pk_bf16_f32 v147, v110, v111\n"
      "v_cvt_pk_bf16_f32 v187, v188, v189\n"
      "v_mfma_f32_16x16x32_bf16 v[124:127], v[148:151], a[156:159], v[124:127]\n"
      "ds_read_b128 v[182:185], v0 offset:22528\n"
      "v_cvt_pk_bf16_f32 v106, v156, v157\n"
      "v_mfma_f32_16x16x32_bf16 v[124:127], v[170:173], a[160:163], v[124:127]\n"
      "ds_read_b128 v[132:135], v1 offset:22528\n"
      "v_cvt_pk_bf16_f32 v107, v158, v159\n"
      "v_mfma_f32_16x16x32_bf16 v[124:127], v[204:207], a[164:167], v[124:127]\n"
      "ds_read_b128 v[160:163], v0 offset:28672\n"
      "v_cvt_pk_bf16_f32 v175, v176, v177\n"
      "v_mfma_f32_16x16x32_bf16 v[196:199], v[220:223], a[168:171], 0\n"
      "ds_read_b128 v[140:143], v1 offset:28672\n"
      "v_fma_f32 v176, v128, v128, s51\n"
      "v_mfma_f32_16x16x32_bf16 v[196:199], v[212:215], a[172:175], v[196:199]\n"
      "ds_read_b128 v[136:139], v0 offset:34816\n"
      "v_fma_f32 v177, v152, v152, s51\n"
      "v_mfma_f32_16x16x32_bf16 v[196:199], v[216:219], a[176:179], v[196:199]\n"
      "ds_read_b128 v[208:211], v1 offset:34816\n"
      "v_fmac_f32 v176, v129, v129\n"
      "v_mfma_f32_16x16x32_bf16 v[196:199], v[148:151], a[180:183], v[196:199]\n"
      "s_waitcnt vmcnt(7)\n"
      "v_fmac_f32 v177, v153, v153\n"
      "v_fmac_f32 v176, v130, v130\n"
      "v_mfma_f32_16x16x32_bf16 v[196:199], v[170:173], a[184:187], v[196:199]\n"
      "v_fmac_f32 v177, v154, v154\n"
      "v_fmac_f32 v176, v131, v131\n"
      "v_mfma_f32_16x16x32_bf16 v[196:199], v[204:207], a[188:191], v[196:199]\n"
      "v_fmac_f32 v177, v155, v155\n"
      "v_fmac_f32 v176, v96, v96\n"
      "v_mfma_f32_16x16x32_bf16 v[188:191], v[220:223], a[192:195], 0\n"
      "v_fmac_f32 v177, v178, v178\n"
      "v_fmac_f32 v176, v97, v97\n"
      "v_mfma_f32_16x16x32_bf16 v[188:191], v[212:215], a[196:199], v[188:191]\n"
      "v_fmac_f32 v177, v179, v179\n"
      "v_fmac_f32 v176, v98, v98\n"
      "v_mfma_f32_16x16x32_bf16 v[188:191], v[216:219], a[200:203], v[188:191]\n"
      "s_waitcnt lgkmcnt(0)\n"
      "v_fmac_f32 v177, v180, v180\n"
      "v_fmac_f32 v176, v99, v99\n"
      "v_mfma_f32_16x16x32_bf16 v[188:191], v[148:151], a[204:207], v[188:191]\n"
      "v_fmac_f32 v177, v181, v181\n"
      "v_fmac_f32 v176, v116, v116\n"
      "v_mfma_f32_16x16x32_bf16 v[188:191], v[170:173], a[208:211], v[188:191]\n"
      "v_fmac_f32 v177, v200, v200\n"
      "v_fmac_f32 v176, v117, v117\n"
      "v_mfma_f32_16x16x32_bf16 v[188:191], v[204:207], a[212:215], v[188:191]\n"
      "v_fmac_f32 v177, v201, v201\n"
      "v_fmac_f32 v176, v118, v118\n"
      "v_mfma_f32_16x16x32_bf16 v[108:111], a[0:3], v[182:185], 0\n"
      "v_fmac_f32 v177, v202, v202\n"
      "v_fmac_f32 v176, v119, v119\n"
      "v_mfma_f32_16x16x32_bf16 v[108:111], a[4:7], v[132:135], v[108:111]\n"
      "v_fmac_f32 v177, v203, v203\n"
      "v_add_u32 v88, s77, v88\n"
      "v_mfma_f32_16x16x32_bf16 v[108:111], a[8:11], v[160:163], v[108:111]\n"
      "v_permlane16_swap_b32 v176, v177\n"
      "v_mfma_f32_16x16x32_bf16 v[108:111], a[12:15], v[140:143], v[108:111]\n"
      "v_add_f32 v176, v176, v177\n"
      "v_mov_b32 v177, v176\n"
      "v_mfma_f32_16x16x32_bf16 v[108:111], a[16:19], v[136:139], v[108:111]\n"
      "v_and_b32 v92, 2, v88\n"
      "v_permlane32_swap_b32 v176, v177\n"
      "v_mfma_f32_16x16x32_bf16 v[108:111], a[20:23], v[208:211], v[108:111]\n"
      "v_add_f32 v176, v176, v177\n"
      "v_rsq_f32 v176, v176\n"
      "v_mfma_f32_16x16x32_bf16 v[212:215], a[24:27], v[182:185], 0\n"
      "v_mov_b32 v177, v176\n"
      "v_and_b32 v88, -4, v88\n"
      "v_mfma_f32_16x16x32_bf16 v[212:215], a[28:31], v[132:135], v[212:215]\n"
      "v_permlane16_swap_b32_e64 v176, v177\n"
      "v_mfma_f32_16x16x32_bf16 v[212:215], a[32:35], v[160:163], v[212:215]\n"
      "v_mul_f32 v176, s50, v176\n"
      "v_mul_f32 v152, v177, v152\n"
      "v_mfma_f32_16x16x32_bf16 v[212:215], a[36:39], v[140:143], v[212:215]\n"
      "v_mul_f32 v153, v177, v153\n"
      "v_mul_f32 v128, v176, v128\n"
      "v_mfma_f32_16x16x32_bf16 v[212:215], a[40:43], v[136:139], v[212:215]\n"
      "v_mul_f32 v129, v176, v129\n"
      "v_cvt_pk_bf16_f32 v152, v152, v153\n"
      "v_mfma_f32_16x16x32_bf16 v[212:215], a[44:47], v[208:211], v[212:215]\n"
      "v_mul_f32 v154, v177, v154\n"
      "v_mul_f32 v155, v177, v155\n"
      "v_mfma_f32_16x16x32_bf16 v[100:103], a[48:51], v[182:185], 0\n"
      "v_mul_f32 v200, v177, v200\n"
      "v_mul_f32_e64 v201, v177, v201\n"
      "v_mfma_f32_16x16x32_bf16 v[100:103], a[52:55], v[132:135], v[100:103]\n"
      "v_cvt_pk_bf16_f32 v128, v128, v129\n"
      "v_mul_f32 v130, v176, v130\n"
      "v_mfma_f32_16x16x32_bf16 v[100:103], a[56:59], v[160:163], v[100:103]\n"
      "v_mul_f32 v131, v176, v131\n"
      "v_mul_f32_e64 v116, v176, v116\n"
      "v_mfma_f32_16x16x32_bf16 v[100:103], a[60:63], v[140:143], v[100:103]\n"
      "v_mul_f32 v117, v176, v117\n"
      "v_cvt_pk_bf16_f32 v153, v154, v155\n"
      "v_mfma_f32_16x16x32_bf16 v[100:103], a[64:67], v[136:139], v[100:103]\n"
      "v_mul_f32 v178, v177, v178\n"
      "v_mul_f32 v179, v177, v179\n"
      "v_mfma_f32_16x16x32_bf16 v[100:103], a[68:71], v[208:211], v[100:103]\n"
      "v_mul_f32 v180, v177, v180\n"
      "v_mul_f32_e64 v181, v177, v181\n"
      "v_mfma_f32_16x16x32_bf16 v[204:207], a[72:75], v[182:185], 0\n"
      "v_cvt_pk_bf16_f32 v200, v200, v201\n"
      "v_mul_f32 v202, v177, v202\n"
      "v_mfma_f32_16x16x32_bf16 v[204:207], a[76:79], v[132:135], v[204:207]\n"
      "v_mul_f32 v203, v177, v203\n"
      "v_cvt_pk_bf16_f32 v129, v130, v131\n"
      "v_mfma_f32_16x16x32_bf16 v[204:207], a[80:83], v[160:163], v[204:207]\n"
      "v_mul_f32 v96, v176, v96\n"
      "v_mul_f32 v97, v176, v97\n"
      "v_mfma_f32_16x16x32_bf16 v[204:207], a[84:87], v[140:143], v[204:207]\n"
      "v_mul_f32 v98, v176, v98\n"
      "v_mul_f32 v99, v176, v99\n"
      "v_mfma_f32_16x16x32_bf16 v[204:207], a[88:91], v[136:139], v[204:207]\n"
      "v_cvt_pk_bf16_f32 v116, v116, v117\n"
      "v_mul_f32 v118, v176, v118\n"
      "v_mfma_f32_16x16x32_bf16 v[204:207], a[92:95], v[208:211], v[204:207]\n"
      "v_mul_f32 v119, v176, v119\n"
      "v_cvt_pk_bf16_f32 v154, v178, v179\n"
      "v_mfma_f32_16x16x32_bf16 v[220:223], a[96:99], v[182:185], 0\n"
      "v_cvt_pk_bf16_f32 v155, v180, v181\n"
      "v_cvt_pk_bf16_f32 v201, v202, v203\n"
      "v_mfma_f32_16x16x32_bf16 v[220:223], a[100:103], v[132:135], v[220:223]\n"
      "v_cvt_pk_bf16_f32 v130, v96, v97\n"
      "v_cvt_pk_bf16_f32 v131, v98, v99\n"
      "v_mfma_f32_16x16x32_bf16 v[220:223], a[104:107], v[160:163], v[220:223]\n"
      "v_cvt_pk_bf16_f32 v117, v118, v119\n"
      "v_fma_f32 v203, v108, v108, s51\n"
      "v_mfma_f32_16x16x32_bf16 v[220:223], a[108:111], v[140:143], v[220:223]\n"
      "v_fma_f32 v181, v204, v204, s51\n"
      "v_fmac_f32 v203, v109, v109\n"
      "v_mfma_f32_16x16x32_bf16 v[220:223], a[112:115], v[136:139], v[220:223]\n"
      "v_fmac_f32 v181, v205, v205\n"
      "v_fmac_f32 v203, v110, v110\n"
      "v_mfma_f32_16x16x32_bf16 v[220:223], a[116:119], v[208:211], v[220:223]\n"
      "v_fmac_f32 v181, v206, v206\n"
      "v_fmac_f32 v203, v111, v111\n"
      "v_mfma_f32_16x16x32_bf16 v[168:171], a[120:123], v[182:185], 0\n"
      "v_fmac_f32 v181, v207, v207\n"
      "v_fmac_f32 v203, v212, v212\n"
      "v_mfma_f32_16x16x32_bf16 v[168:171], a[124:127], v[132:135], v[168:171]\n"
      "v_fmac_f32 v203, v213, v213\n"
      "v_fmac_f32 v203, v214, v214\n"
      "v_mfma_f32_16x16x32_bf16 v[168:171], a[128:131], v[160:163], v[168:171]\n"
      "v_fmac_f32 v181, v220, v220\n"
      "v_fmac_f32 v181, v221, v221\n"
      "v_mfma_f32_16x16x32_bf16 v[168:171], a[132:135], v[140:143], v[168:171]\n"
      "v_fmac_f32 v181, v222, v222\n"
      "v_fmac_f32 v203, v215, v215\n"
      "v_mfma_f32_16x16x32_bf16 v[168:171], a[136:139], v[136:139], v[168:171]\n"
      "v_fmac_f32 v181, v223, v223\n"
      "v_fmac_f32 v203, v100, v100\n"
      "v_mfma_f32_16x16x32_bf16 v[168:171], a[140:143], v[208:211], v[168:171]\n"
      "v_fmac_f32 v203, v101, v101\n"
      "v_fmac_f32 v203, v102, v102\n"
      "v_mfma_f32_16x16x32_bf16 v[216:219], v[182:185], a[144:147], 0\n"
      "v_fmac_f32 v203, v103, v103\n"
      "v_add_u32 v82, 110592, v88\n"
      "v_mfma_f32_16x16x32_bf16 v[216:219], v[132:135], a[148:151], v[216:219]\n"
      "v_lshlrev_b32 v76, 2, v76\n"
      "v_sub_u32 v77, v77, v76\n"
      "v_mfma_f32_16x16x32_bf16 v[216:219], v[160:163], a[152:155], v[216:219]\n"
      "v_fmac_f32 v181, v168, v168\n"
      "v_fmac_f32 v181, v169, v169\n"
      "v_mfma_f32_16x16x32_bf16 v[216:219], v[140:143], a[156:159], v[216:219]\n"
      "ds_read_b128 v[96:99], v2\n"
      "v_fmac_f32 v181, v170, v170\n"
      "v_mfma_f32_16x16x32_bf16 v[216:219], v[136:139], a[160:163], v[216:219]\n"
      "ds_read_b128 v[176:179], v2 offset:3072\n"
      "v_fmac_f32 v181, v171, v171\n"
      "v_mfma_f32_16x16x32_bf16 v[216:219], v[208:211], a[164:167], v[216:219]\n"
      "ds_read_b128 v[148:151], v2 offset:6144\n"
      "v_permlane16_swap_b32 v203, v181\n"
      "v_add_f32 v203, v203, v181\n"
      "v_mov_b32 v181, v203\n"
      "v_lshlrev_b32 v77, 4, v77\n"
      "v_cmp_gt_u32 s[72:73], 64, v77\n"
      "v_permlane32_swap_b32 v203, v181\n"
      "v_add_f32 v203, v203, v181\n"
      "v_rsq_f32 v203, v203\n"
      "v_cvt_pk_bf16_f32 v112, v112, v113\n"
      "v_mov_b32 v181, v203\n"
      "v_cvt_pk_bf16_f32 v113, v114, v115\n"
      "v_mov_b32 v202, 0x3fb9\n"
      "v_permlane16_swap_b32 v203, v181\n"
      "v_mul_f32 v203, s50, v203\n"
      "v_mul_f32 v204, v181, v204\n"
      "v_mul_f32 v205, v181, v205\n"
      "v_mul_f32 v108, v203, v108\n"
      "v_mul_f32 v109, v203, v109\n"
      "v_cvt_pk_bf16_f32 v204, v204, v205\n"
      "v_mul_f32 v206, v181, v206\n"
      "v_mul_f32 v207, v181, v207\n"
      "v_mul_f32 v168, v181, v168\n"
      "v_mul_f32 v169, v181, v169\n"
      "v_cvt_pk_bf16_f32 v108, v108, v109\n"
      "v_mul_f32 v110, v203, v110\n"
      "v_mul_f32 v111, v203, v111\n"
      "v_mul_f32 v100, v203, v100\n"
      "v_mul_f32 v101, v203, v101\n"
      "v_cvt_pk_bf16_f32 v205, v206, v207\n"
      "v_mul_f32 v220, v181, v220\n"
      "v_mul_f32 v221, v181, v221\n"
      "v_mul_f32 v222, v181, v222\n"
      "v_mul_f32 v223, v181, v223\n"
      "v_cvt_pk_bf16_f32 v168, v168, v169\n"
      "v_mul_f32 v170, v181, v170\n"
      "v_mul_f32 v171, v181, v171\n"
      "v_cvt_pk_bf16_f32 v109, v110, v111\n"
      "v_mul_f32 v212, v203, v212\n"
      "v_mul_f32 v213, v203, v213\n"
      "v_mul_f32 v214, v203, v214\n"
      "v_mul_f32 v215, v203, v215\n"
      "v_cvt_pk_bf16_f32 v100, v100, v101\n"
      "v_mul_f32 v102, v203, v102\n"
      "v_mul_f32 v103, v203, v103\n"
      "v_cvt_pk_bf16_f32 v206, v220, v221\n"
      "v_mfma_f32_16x16x32_bf16 v[156:159], v[182:185], a[168:171], 0\n"
      "v_cvt_pk_bf16_f32 v207, v222, v223\n"
      "v_cvt_pk_bf16_f32 v169, v170, v171\n"
      "v_mfma_f32_16x16x32_bf16 v[156:159], v[132:135], a[172:175], v[156:159]\n"
      "v_cvt_pk_bf16_f32 v110, v212, v213\n"
      "v_cvt_pk_bf16_f32 v111, v214, v215\n"
      "v_mfma_f32_16x16x32_bf16 v[156:159], v[160:163], a[176:179], v[156:159]\n"
      "v_cvt_pk_bf16_f32 v101, v102, v103\n"
      "v_mov_b32_e64 v203, 0\n"
      "v_mfma_f32_16x16x32_bf16 v[156:159], v[140:143], a[180:183], v[156:159]\n"
      "v_lshlrev_b64 v[202:203], v77, v[202:203]\n"
      "v_cndmask_b32_e64 v202, 0, v202, s[72:73]\n"
      "v_mfma_f32_16x16x32_bf16 v[156:159], v[136:139], a[184:187], v[156:159]\n"
      "v_cndmask_b32_e64 v203, 0, v203, s[72:73]\n"
      "v_cmp_gt_u32 s[72:73], 64, v77\n"
      "v_mfma_f32_16x16x32_bf16 v[156:159], v[208:211], a[188:191], v[156:159]\n"
      "ds_read_b128 v[212:215], v7\n"
      "v_mov_b32 v102, 0xbb2b\n"
      "v_mfma_f32_16x16x32_bf16 v[170:173], v[182:185], a[192:195], 0\n"
      "ds_read_b128 v[180:183], v2 offset:9216\n"
      "v_mov_b32 v103, 0\n"
      "v_mfma_f32_16x16x32_bf16 v[170:173], v[132:135], a[196:199], v[170:173]\n"
      "ds_read_b128 v[132:135], v2 offset:12288\n"
      "v_lshlrev_b64 v[102:103], v77, v[102:103]\n"
      "v_mfma_f32_16x16x32_bf16 v[170:173], v[160:163], a[200:203], v[170:173]\n"
      "ds_read_b128 v[160:163], v7 offset:4096\n"
      "v_cndmask_b32_e64 v102, 0, v102, s[72:73]\n"
      "v_mfma_f32_16x16x32_bf16 v[170:173], v[140:143], a[204:207], v[170:173]\n"
      "ds_read_b128 v[140:143], v2 offset:15360\n"
      "v_cndmask_b32_e64 v103, 0, v103, s[72:73]\n"
      "v_mfma_f32_16x16x32_bf16 v[170:173], v[136:139], a[208:211], v[170:173]\n"
      "ds_read_b128 v[136:139], v7 offset:8192\n"
      "v_add_u32 v78, 112160, v88\n"
      "v_mfma_f32_16x16x32_bf16 v[170:173], v[208:211], a[212:215], v[170:173]\n"
      "s_waitcnt lgkmcnt(5)\n"
      "v_cvt_pk_bf16_f32 v114, v124, v125\n"
      "v_cvt_pk_bf16_f32 v115, v126, v127\n"
      "v_mfma_f32_16x16x32_bf16 v[220:223], a[216:219], v[96:99], 0\n"
      "buffer_store_dwordx4 v[212:215], v20, s[20:23], s46 offen sc1\n"
      "v_mfma_f32_16x16x32_bf16 v[220:223], a[220:223], v[176:179], v[220:223]\n"
      "ds_read_b128 v[124:127], v7 offset:12288\n"
      "v_cvt_pk_bf16_f32 v76, v216, v217\n"
      "v_mfma_f32_16x16x32_bf16 v[220:223], a[224:227], v[148:151], v[220:223]\n"
      "v_cvt_pk_bf16_f32 v77, v218, v219\n"
      "v_cvt_pk_bf16_f32 v192, v192, v193\n"
      "v_mfma_f32_16x16x32_bf16 v[208:211], a[240:243], v[96:99], 0\n"
      "v_cvt_pk_bf16_f32 v193, v194, v195\n"
      "v_cvt_pk_bf16_f32 v194, v196, v197\n"
      "v_mfma_f32_16x16x32_bf16 v[208:211], a[244:247], v[176:179], v[208:211]\n"
      "v_cvt_pk_bf16_f32 v195, v198, v199\n"
      "v_cvt_pk_bf16_f32 v80, v156, v157\n"
      "v_mfma_f32_16x16x32_bf16 v[208:211], a[248:251], v[148:151], v[208:211]\n"
      "ds_read_b128 v[196:199], v7 offset:16384\n"
      "v_cvt_pk_bf16_f32 v81, v158, v159\n"
      "v_mfma_f32_16x16x32_bf16 v[214:217], v[232:235], v[96:99], 0\n"
      "s_waitcnt lgkmcnt(2)\n"
      "v_cvt_pk_bf16_f32 v164, v164, v165\n"
      "v_cvt_pk_bf16_f32 v165, v166, v167\n"
      "v_mfma_f32_16x16x32_bf16 v[214:217], v[236:239], v[176:179], v[214:217]\n"
      "ds_read_b128 v[156:159], v10 offset:1200\n"
      "v_cvt_pk_bf16_f32 v166, v188, v189\n"
      "v_mfma_f32_16x16x32_bf16 v[214:217], v[240:243], v[148:151], v[214:217]\n"
      "buffer_store_dwordx4 v[160:163], v21, s[20:23], s46 offen sc1\n"
      "v_mfma_f32_16x16x32_bf16 v[220:223], a[228:231], v[180:183], v[220:223]\n"
      "s_waitcnt lgkmcnt(2)\n"
      "v_cvt_pk_bf16_f32 v167, v190, v191\n"
      "v_cvt_pk_bf16_f32 v84, v170, v171\n"
      "v_mfma_f32_16x16x32_bf16 v[220:223], a[232:235], v[132:135], v[220:223]\n"
      "ds_read_b64 v[98:99], v10 offset:1192\n"
      "v_cvt_pk_bf16_f32 v85, v172, v173\n"
      "v_mfma_f32_16x16x32_bf16 v[220:223], a[236:239], v[140:143], v[220:223]\n"
      "ds_read2_b32 v[150:151], v82 offset0:0 offset1:1\n"
      "v_add_u32 v93, 113728, v88\n"
      "v_mfma_f32_16x16x32_bf16 v[208:211], a[252:255], v[180:183], v[208:211]\n"
      "ds_read2_b32 v[148:149], v82 offset0:2 offset1:10\n"
      "v_subrev_u32 v213, 115456, v9\n"
      "v_mfma_f32_16x16x32_bf16 v[208:211], v[224:227], v[132:135], v[208:211]\n"
      "s_waitcnt lgkmcnt(3)\n"
      "v_lshrrev_b32 v213, 3, v213\n"
      "v_min_u32 v213, 96, v213\n"
      "v_mfma_f32_16x16x32_bf16 v[208:211], v[228:231], v[140:143], v[208:211]\n"
      "ds_read2_b32 v[172:173], v82 offset0:8 offset1:9\n"
      "v_add_u32_e64 v213, s77, v213\n"
      "v_mfma_f32_16x16x32_bf16 v[214:217], v[244:247], v[180:183], v[214:217]\n"
      "ds_read2_b32 v[170:171], v82 offset0:16 offset1:17\n"
      "v_add_u32 v213, 115296, v213\n"
      "v_mfma_f32_16x16x32_bf16 v[214:217], v[248:251], v[132:135], v[214:217]\n"
      "buffer_store_dwordx4 v[136:139], v22, s[20:23], s46 offen sc1\n"
      "v_mfma_f32_16x16x32_bf16 v[214:217], v[252:255], v[140:143], v[214:217]\n"
      "ds_read2_b32 v[176:177], v82 offset0:24 offset1:18\n"
      "s_waitcnt lgkmcnt(3)\n"
      "v_alignbyte_b32 v150, v151, v150, v92\n"
      "v_mfma_f32_16x16x32_bf16 v[136:139], v[144:147], v[104:107], v[40:43]\n"
      "v_alignbyte_b32 v151, v148, v151, v92\n"
      "v_fma_f32 v212, v220, v220, s52\n"
      "v_mfma_f32_16x16x32_bf16 v[178:181], v[152:155], v[104:107], v[44:47]\n"
      "ds_read_b128 v[140:143], v2 offset:256\n"
      "s_waitcnt lgkmcnt(3)\n"
      "v_alignbyte_b32 v172, v173, v172, v92\n"
      "v_mfma_f32_16x16x32_bf16 v[188:191], v[204:207], v[104:107], v[48:51]\n"
      "ds_read_b128 v[160:163], v2 offset:3328\n"
      "v_alignbyte_b32 v173, v149, v173, v92\n"
      "v_mfma_f32_16x16x32_bf16 v[132:135], v[156:159], v[104:107], v[24:27]\n"
      "s_waitcnt lgkmcnt(3)\n"
      "v_alignbyte_b32 v170, v171, v170, v92\n"
      "v_fmac_f32 v212, v221, v221\n"
      "v_mfma_f32_16x16x16_bf16 v[136:139], v[186:187], v[174:175], v[136:139]\n"
      "v_fmac_f32 v212, v222, v222\n"
      "v_fmac_f32_e64 v212, v223, v223\n"
      "s_waitcnt lgkmcnt(2)\n"
      "v_alignbyte_b32 v171, v177, v171, v92\n"
      "v_mfma_f32_16x16x16_bf16 v[178:181], v[200:201], v[174:175], v[178:181]\n"
      "v_alignbyte_b32 v176, v176, v176, v92\n"
      "buffer_store_dwordx4 v[124:127], v23, s[20:23], s46 offen sc1\n"
      "v_mfma_f32_16x16x16_bf16 v[188:191], v[168:169], v[174:175], v[188:191]\n"
      "v_fmac_f32 v212, v208, v208\n"
      "v_fmac_f32 v212, v209, v209\n"
      "v_fmac_f32 v212, v210, v210\n"
      "v_mfma_f32_16x16x16_bf16 v[132:135], v[98:99], v[174:175], v[132:135]\n"
      "v_fmac_f32 v212, v211, v211\n"
      "v_fmac_f32_e64 v212, v214, v214\n"
      "v_fmac_f32_e64 v212, v215, v215\n"
      "v_mfma_f32_16x16x16_bf16 v[136:139], v[202:203], v[150:151], v[136:139]\n"
      "v_fmac_f32_e64 v212, v216, v216\n"
      "v_fmac_f32_e64 v212, v217, v217\n"
      "v_readlane_b32 s45, v37, s43\n"
      "v_mfma_f32_16x16x16_bf16 v[178:181], v[202:203], v[172:173], v[178:181]\n"
      "v_readlane_b32 s47, v38, s43\n"
      "ds_read_b128 v[104:107], v2 offset:6400\n"
      "v_mfma_f32_16x16x16_bf16 v[188:191], v[202:203], v[170:171], v[188:191]\n"
      "s_waitcnt vmcnt(5)\n"
      "v_or3_b32 v120, v120, v121, v122\n"
      "s_and_b32 s47, s47, -4\n"
      "s_add_u32 s43, s43, 1\n"
      "v_mfma_f32_16x16x16_bf16 v[132:135], v[202:203], v[176:177], v[132:135]\n"
      "v_mfma_f32_16x16x16_bf16 v[136:139], v[102:103], v[150:151], v[136:139]\n"
      "v_mfma_f32_16x16x16_bf16 v[178:181], v[102:103], v[172:173], v[178:181]\n"
      "buffer_store_dwordx4 v[196:199], v25, s[20:23], s46 offen sc1\n"
      "v_mfma_f32_16x16x16_bf16 v[188:191], v[102:103], v[170:171], v[188:191]\n"
      "s_waitcnt vmcnt(5)\n"
      "v_or3_b32 v120, v120, v123, v89\n"
      "v_and_b32 v120, 0x7fff7fff, v120\n"
      "v_readlane_b32 s46, v37, s42\n"
      "v_mfma_f32_16x16x16_bf16 v[132:135], v[102:103], v[176:177], v[132:135]\n"
      "ds_write_b32 v31, v120 offset:5632\n"
      "s_waitcnt lgkmcnt(1)\n"
      "v_mfma_f32_16x16x32_bf16 v[148:151], a[216:219], v[140:143], 0\n"
      "s_or_b32 s95, s45, s46\n"
      "v_max3_f32 v82, v136, v137, v138\n"
      "v_max3_f32 v120, v139, v178, v179\n"
      "v_mfma_f32_16x16x32_bf16 v[148:151], a[220:223], v[160:163], v[148:151]\n"
      "ds_read2_b32 v[182:183], v78 offset0:0 offset1:1\n"
      "v_max3_f32 v82, v82, v180, v181\n"
      "v_mfma_f32_16x16x32_bf16 v[148:151], a[224:227], v[104:107], v[148:151]\n"
      "ds_read2_b32 v[176:177], v78 offset0:2 offset1:10\n"
      "v_max3_f32 v120, v120, v188, v189\n"
      "v_mfma_f32_16x16x32_bf16 v[170:173], a[240:243], v[140:143], 0\n"
      "ds_read_b128 v[196:199], v9 offset:4608\n"
      "v_max3_f32 v82, v82, v190, v191\n"
      "v_mfma_f32_16x16x32_bf16 v[170:173], a[244:247], v[160:163], v[170:173]\n"
      "ds_read2_b32 v[184:185], v78 offset0:8 offset1:9\n"
      "v_max3_f32 v82, v82, v120, v132\n"
      "v_mfma_f32_16x16x32_bf16 v[170:173], a[248:251], v[104:107], v[170:173]\n"
      "ds_read2_b32 v[134:135], v78 offset0:16 offset1:17\n"
      "v_mov_b32_e64 v133, v82\n"
      "v_mfma_f32_16x16x32_bf16 v[120:123], v[232:235], v[140:143], 0\n"
      "ds_read2_b32 v[88:89], v78 offset0:24 offset1:18\n"
      "v_permlane16_swap_b32_e64 v82, v133\n"
      "v_mfma_f32_16x16x32_bf16 v[120:123], v[236:239], v[160:163], v[120:123]\n"
      "s_or_b32 s95, s95, s48\n"
      "s_waitcnt lgkmcnt(5)\n"
      "v_alignbyte_b32 v182, v183, v182, v92\n"
      "v_mfma_f32_16x16x32_bf16 v[120:123], v[240:243], v[104:107], v[120:123]\n"
      "s_cmp_ge_u32 s95, 0x40000000\n"
      "s_cselect_b32 s94, -1, -2\n"
      "s_waitcnt lgkmcnt(4)\n"
      "v_alignbyte_b32 v183, v176, v183, v92\n"
      "v_mfma_f32_16x16x32_bf16 v[104:107], v[144:147], v[128:131], v[52:55]\n"
      "s_cmp_gt_u32 s42, s44\n"
      "s_cselect_b32 s95, s94, s95\n"
      "s_waitcnt lgkmcnt(2)\n"
      "v_alignbyte_b32 v184, v185, v184, v92\n"
      "v_mfma_f32_16x16x32_bf16 v[140:143], v[152:155], v[128:131], v[56:59]\n"
      "s_add_u32 s42, s42, 1\n"
      "v_alignbyte_b32 v185, v177, v185, v92\n"
      "s_waitcnt lgkmcnt(1)\n"
      "v_alignbyte_b32 v134, v135, v134, v92\n"
      "v_max_f32 v82, v82, v133\n"
      "s_waitcnt lgkmcnt(0)\n"
      "v_alignbyte_b32 v135, v89, v135, v92\n"
      "v_alignbyte_b32 v88, v88, v88, v92\n"
      "v_mov_b32 v133, v82\n"
      "v_or3_b32 v196, v196, v197, v198\n"
      "v_mfma_f32_16x16x32_bf16 v[124:127], v[204:207], v[128:131], v[60:63]\n"
      "v_permlane32_swap_b32 v82, v133\n"
      "v_mfma_f32_16x16x32_bf16 v[174:177], v[156:159], v[128:131], v[26:29]\n"
      "v_max_f32 v82, v82, v133\n"
      "v_sub_f32 v136, v136, v82\n"
      "v_mfma_f32_16x16x16_bf16 v[104:107], v[186:187], v[116:117], v[104:107]\n"
      "v_sub_f32 v137, v137, v82\n"
      "v_sub_f32 v138, v138, v82\n"
      "v_sub_f32_e64 v139, v139, v82\n"
      "v_mfma_f32_16x16x16_bf16 v[140:143], v[200:201], v[116:117], v[140:143]\n"
      "v_exp_f32 v136, v136\n"
      "v_exp_f32 v137, v137\n"
      "v_mfma_f32_16x16x16_bf16 v[124:127], v[168:169], v[116:117], v[124:127]\n"
      "v_sub_f32 v178, v178, v82\n"
      "v_sub_f32 v179, v179, v82\n"
      "v_sub_f32 v180, v180, v82\n"
      "v_mfma_f32_16x16x16_bf16 v[174:177], v[98:99], v[116:117], v[174:177]\n"
      "v_sub_f32 v181, v181, v82\n"
      "v_exp_f32 v138, v138\n"
      "v_mfma_f32_16x16x16_bf16 v[104:107], v[202:203], v[182:183], v[104:107]\n"
      "v_exp_f32 v139, v139\n"
      "v_exp_f32 v178, v178\n"
      "v_mfma_f32_16x16x16_bf16 v[140:143], v[202:203], v[184:185], v[140:143]\n"
      "v_exp_f32 v179, v179\n"
      "v_exp_f32 v180, v180\n"
      "v_mfma_f32_16x16x16_bf16 v[124:127], v[202:203], v[134:135], v[124:127]\n"
      "v_exp_f32 v181, v181\n"
      "v_cvt_pk_bf16_f32 v136, v136, v137\n"
      "v_mfma_f32_16x16x16_bf16 v[174:177], v[202:203], v[88:89], v[174:177]\n"
      "v_cvt_pk_bf16_f32 v137, v138, v139\n"
      "v_cvt_pk_bf16_f32 v138, v178, v179\n"
      "v_cvt_pk_bf16_f32 v139, v180, v181\n"
      "v_mfma_f32_16x16x16_bf16 v[104:107], v[102:103], v[182:183], v[104:107]\n"
      "v_sub_f32 v188, v188, v82\n"
      "v_sub_f32 v189, v189, v82\n"
      "v_sub_f32_e64 v190, v190, v82\n"
      "v_mfma_f32_16x16x16_bf16 v[140:143], v[102:103], v[184:185], v[140:143]\n"
      "v_sub_f32 v191, v191, v82\n"
      "v_sub_f32 v132, v132, v82\n"
      "v_exp_f32 v188, v188\n"
      "v_mfma_f32_16x16x16_bf16 v[124:127], v[102:103], v[134:135], v[124:127]\n"
      "v_exp_f32 v189, v189\n"
      "v_exp_f32_e64 v190, v190\n"
      "v_mfma_f32_16x16x16_bf16 v[174:177], v[102:103], v[88:89], v[174:177]\n"
      "ds_read_b128 v[178:181], v2 offset:9472\n"
      "ds_read_b128 v[160:163], v2 offset:12544\n"
      "ds_read_b128 v[182:185], v2 offset:15616\n"
      "v_exp_f32 v191, v191\n"
      "v_exp_f32 v132, v132\n"
      "v_cvt_pk_bf16_f32 v88, v188, v189\n"
      "v_cvt_pk_bf16_f32 v89, v190, v191\n"
      "v_cvt_pk_bf16_f32 v90, v132, 0\n"
      "v_max3_f32 v134, v104, v105, v106\n"
      "v_max3_f32 v82, v107, v140, v141\n"
      "v_max3_f32 v134, v134, v142, v143\n"
      "v_max3_f32 v82, v82, v124, v125\n"
      "v_max3_f32 v134, v134, v126, v127\n"
      "v_max3_f32 v134, v134, v82, v174\n"
      "v_mov_b32 v177, v134\n"
      "v_or_b32 v196, v196, v199\n"
      "v_cmp_ne_u32 s[56:57], 0, v196\n"
      "v_permlane16_swap_b32 v134, v177\n"
      "s_waitcnt lgkmcnt(2)\n"
      "v_mfma_f32_16x16x32_bf16 v[148:151], a[228:231], v[178:181], v[148:151]\n"
      "ds_read_u16 v78, v11 offset:1168\n"
      "v_max_f32 v134, v134, v177\n"
      "s_waitcnt lgkmcnt(2)\n"
      "v_mfma_f32_16x16x32_bf16 v[148:151], a[232:235], v[160:163], v[148:151]\n"
      "ds_read_u16 v82, v11 offset:1200\n"
      "v_mov_b32 v177, v134\n"
      "s_waitcnt lgkmcnt(2)\n"
      "v_mfma_f32_16x16x32_bf16 v[148:151], a[236:239], v[182:185], v[148:151]\n"
      "ds_read_u16 v86, v11 offset:1232\n"
      "v_permlane32_swap_b32 v134, v177\n"
      "v_mfma_f32_16x16x32_bf16 v[170:173], a[252:255], v[178:181], v[170:173]\n"
      "s_cmp_ge_u32 s95, 0x40000000\n"
      "s_cselect_b64 vcc, -1, s[56:57]\n"
      "v_max_f32 v134, v134, v177\n"
      "v_mfma_f32_16x16x32_bf16 v[170:173], v[224:227], v[160:163], v[170:173]\n"
      "v_sub_f32 v104, v104, v134\n"
      "v_sub_f32 v105, v105, v134\n"
      "v_mfma_f32_16x16x32_bf16 v[170:173], v[228:231], v[182:185], v[170:173]\n"
      "v_sub_f32 v106, v106, v134\n"
      "v_sub_f32 v107, v107, v134\n"
      "v_mfma_f32_16x16x32_bf16 v[120:123], v[244:247], v[178:181], v[120:123]\n"
      "v_exp_f32 v104, v104\n"
      "v_mfma_f32_16x16x32_bf16 v[120:123], v[248:251], v[160:163], v[120:123]\n"
      "v_exp_f32 v105, v105\n"
      "v_mfma_f32_16x16x32_bf16 v[120:123], v[252:255], v[182:185], v[120:123]\n"
      "s_waitcnt lgkmcnt(0)\n"
      "v_sub_f32 v140, v140, v134\n"
      "v_sub_f32_e64 v141, v141, v134\n"
      "v_mfma_f32_16x16x32_bf16 v[176:179], v[32:35], v[136:139], 0\n"
      "ds_read2_b32 v[160:161], v93 offset0:0 offset1:1\n"
      "v_sub_f32 v142, v142, v134\n"
      "v_mfma_f32_16x16x32_bf16 v[176:179], v[32:35], v[88:91], v[176:179]\n"
      "ds_read2_b32 v[184:185], v93 offset0:2 offset1:10\n"
      "v_sub_f32 v143, v143, v134\n"
      "v_mfma_f32_16x16x32_bf16 v[196:199], v[112:115], v[136:139], 0\n"
      "ds_read2_b32 v[162:163], v93 offset0:8 offset1:9\n"
      "v_exp_f32_e64 v106, v106\n"
      "v_mfma_f32_16x16x32_bf16 v[196:199], v[76:79], v[88:91], v[196:199]\n"
      "ds_read2_b32 v[132:133], v93 offset0:16 offset1:17\n"
      "v_exp_f32 v107, v107\n"
      "v_mfma_f32_16x16x32_bf16 v[178:181], v[192:195], v[136:139], 0\n"
      "ds_read2_b32 v[96:97], v93 offset0:24 offset1:18\n"
      "v_exp_f32 v140, v140\n"
      "v_mfma_f32_16x16x32_bf16 v[178:181], v[80:83], v[88:91], v[178:181]\n"
      "v_exp_f32 v141, v141\n"
      "v_mfma_f32_16x16x32_bf16 v[188:191], v[164:167], v[136:139], 0\n"
      "s_waitcnt lgkmcnt(4)\n"
      "v_alignbyte_b32 v160, v161, v160, v92\n"
      "s_waitcnt lgkmcnt(3)\n"
      "v_alignbyte_b32 v161, v184, v161, v92\n"
      "v_mfma_f32_16x16x32_bf16 v[188:191], v[84:87], v[88:91], v[188:191]\n"
      "s_waitcnt lgkmcnt(2)\n"
      "v_alignbyte_b32 v162, v163, v162, v92\n"
      "v_alignbyte_b32 v163, v185, v163, v92\n"
      "v_mfma_f32_16x16x32_bf16 v[128:131], v[144:147], v[108:111], v[64:67]\n"
      "v_exp_f32 v142, v142\n"
      "s_waitcnt lgkmcnt(1)\n"
      "v_alignbyte_b32 v132, v133, v132, v92\n"
      "v_exp_f32 v143, v143\n"
      "s_waitcnt lgkmcnt(0)\n"
      "v_alignbyte_b32 v133, v97, v133, v92\n"
      "v_alignbyte_b32 v96, v96, v96, v92\n"
      "v_cvt_pk_bf16_f32 v104, v104, v105\n"
      "v_cvt_pk_bf16_f32 v105, v106, v107\n"
      "v_cvt_pk_bf16_f32 v106, v140, v141\n"
      "v_mfma_f32_16x16x32_bf16 v[182:185], v[152:155], v[108:111], v[68:71]\n"
      "v_cvt_pk_bf16_f32 v107, v142, v143\n"
      "v_sub_f32 v124, v124, v134\n"
      "v_sub_f32 v125, v125, v134\n"
      "v_sub_f32 v126, v126, v134\n"
      "v_sub_f32 v127, v127, v134\n"
      "v_sub_f32 v174, v174, v134\n"
      "v_mfma_f32_16x16x32_bf16 v[140:143], v[204:207], v[108:111], v[72:75]\n"
      "v_exp_f32 v124, v124\n"
      "v_mfma_f32_16x16x32_bf16 v[134:137], v[156:159], v[108:111], v[28:31]\n"
      "v_exp_f32 v125, v125\n"
      "v_mfma_f32_16x16x16_bf16 v[128:131], v[186:187], v[100:101], v[128:131]\n"
      "v_exp_f32 v126, v126\n"
      "v_exp_f32_e64 v127, v127\n"
      "v_mfma_f32_16x16x16_bf16 v[182:185], v[200:201], v[100:101], v[182:185]\n"
      "v_exp_f32_e64 v174, v174\n"
      "v_cvt_pk_bf16_f32 v92, v124, v125\n"
      "v_mfma_f32_16x16x16_bf16 v[140:143], v[168:169], v[100:101], v[140:143]\n"
      "v_cvt_pk_bf16_f32 v93, v126, v127\n"
      "v_cvt_pk_bf16_f32 v94, v174, 0\n"
      "v_fma_f32 v174, v148, v148, s52\n"
      "v_mfma_f32_16x16x16_bf16 v[134:137], v[98:99], v[100:101], v[134:137]\n"
      "v_fmac_f32 v174, v149, v149\n"
      "v_fmac_f32 v174, v150, v150\n"
      "v_fmac_f32 v174, v151, v151\n"
      "v_mfma_f32_16x16x16_bf16 v[128:131], v[202:203], v[160:161], v[128:131]\n"
      "v_fmac_f32 v174, v170, v170\n"
      "v_fmac_f32 v174, v171, v171\n"
      "v_fmac_f32 v174, v172, v172\n"
      "v_mfma_f32_16x16x16_bf16 v[182:185], v[202:203], v[162:163], v[182:185]\n"
      "v_fmac_f32 v174, v173, v173\n"
      "v_fmac_f32 v174, v120, v120\n"
      "v_fmac_f32 v174, v121, v121\n"
      "v_mfma_f32_16x16x16_bf16 v[140:143], v[202:203], v[132:133], v[140:143]\n"
      "v_fmac_f32 v174, v122, v122\n"
      "v_fmac_f32 v174, v123, v123\n"
      "v_rcp_f32 v176, v176\n"
      "v_mfma_f32_16x16x16_bf16 v[134:137], v[202:203], v[96:97], v[134:137]\n"
      "v_permlane16_swap_b32 v212, v174\n"
      "v_add_f32 v212, v212, v174\n"
      "v_mfma_f32_16x16x16_bf16 v[128:131], v[102:103], v[160:161], v[128:131]\n"
      "v_mul_f32 v196, v176, v196\n"
      "v_mul_f32 v197, v176, v197\n"
      "v_cvt_pk_bf16_f32 v196, v196, v197\n"
      "v_mfma_f32_16x16x16_bf16 v[182:185], v[102:103], v[162:163], v[182:185]\n"
      "v_mul_f32 v198, v176, v198\n"
      "v_mul_f32 v199, v176, v199\n"
      "v_cvt_pk_bf16_f32 v197, v198, v199\n"
      "v_mfma_f32_16x16x16_bf16 v[140:143], v[102:103], v[132:133], v[140:143]\n"
      "v_max3_f32 v133, v128, v129, v130\n"
      "v_mul_f32 v178, v176, v178\n"
      "v_mul_f32 v179, v176, v179\n"
      "v_mfma_f32_16x16x16_bf16 v[134:137], v[102:103], v[96:97], v[134:137]\n"
      "ds_read_b128 v[100:103], v2 offset:512\n"
      "ds_read_b128 v[108:111], v2 offset:3584\n"
      "ds_read_b128 v[124:127], v2 offset:6656\n"
      "v_max3_f32 v163, v131, v182, v183\n"
      "v_max3_f32 v133, v133, v184, v185\n"
      "v_max3_f32 v163, v163, v140, v141\n"
      "v_max3_f32 v133, v133, v142, v143\n"
      "v_mul_f32 v180, v176, v180\n"
      "v_max3_f32 v133, v133, v163, v134\n"
      "v_mov_b32 v137, v133\n"
      "v_mul_f32 v181, v176, v181\n"
      "v_cvt_pk_bf16_f32 v198, v178, v179\n"
      "v_permlane16_swap_b32 v133, v137\n"
      "v_max_f32 v133, v133, v137\n"
      "v_mov_b32 v137, v133\n"
      "v_cvt_pk_bf16_f32 v199, v180, v181\n"
      "v_mul_f32 v188, v176, v188\n"
      "v_permlane32_swap_b32 v133, v137\n"
      "v_max_f32 v133, v133, v137\n"
      "s_waitcnt lgkmcnt(2)\n"
      "v_mfma_f32_16x16x32_bf16 v[136:139], a[216:219], v[100:103], 0\n"
      "ds_write_b128 v3, v[196:199] offset:18432\n"
      "s_waitcnt lgkmcnt(1)\n"
      "v_mfma_f32_16x16x32_bf16 v[136:139], a[220:223], v[108:111], v[136:139]\n"
      "v_sub_f32 v128, v128, v133\n"
      "v_sub_f32 v129, v129, v133\n"
      "v_mfma_f32_16x16x32_bf16 v[136:139], a[224:227], v[124:127], v[136:139]\n"
      "ds_read_b128 v[160:163], v10 offset:1168\n"
      "v_sub_f32 v130, v130, v133\n"
      "v_mfma_f32_16x16x32_bf16 v[196:199], a[240:243], v[100:103], 0\n"
      "v_sub_f32 v131, v131, v133\n"
      "v_exp_f32 v128, v128\n"
      "v_mfma_f32_16x16x32_bf16 v[196:199], a[244:247], v[108:111], v[196:199]\n"
      "v_exp_f32 v129, v129\n"
      "v_mfma_f32_16x16x32_bf16 v[196:199], a[248:251], v[124:127], v[196:199]\n"
      "v_sub_f32 v182, v182, v133\n"
      "v_sub_f32 v183, v183, v133\n"
      "v_mfma_f32_16x16x32_bf16 v[178:181], v[232:235], v[100:103], 0\n"
      "v_sub_f32 v184, v184, v133\n"
      "v_sub_f32_e64 v185, v185, v133\n"
      "v_mfma_f32_16x16x32_bf16 v[178:181], v[236:239], v[108:111], v[178:181]\n"
      "ds_read_b64 v[202:203], v10 offset:1184\n"
      "v_exp_f32 v130, v130\n"
      "v_mfma_f32_16x16x32_bf16 v[178:181], v[240:243], v[124:127], v[178:181]\n"
      "v_exp_f32 v131, v131\n"
      "v_mfma_f32_16x16x32_bf16 v[100:103], v[32:35], v[104:107], 0\n"
      "v_exp_f32 v182, v182\n"
      "v_mfma_f32_16x16x32_bf16 v[100:103], v[32:35], v[92:95], v[100:103]\n"
      "ds_read_b128 v[108:111], v2 offset:9728\n"
      "v_exp_f32 v183, v183\n"
      "v_exp_f32 v184, v184\n"
      "v_exp_f32 v185, v185\n"
      "v_cvt_pk_bf16_f32 v128, v128, v129\n"
      "v_cvt_pk_bf16_f32 v129, v130, v131\n"
      "v_cvt_pk_bf16_f32 v130, v182, v183\n"
      "v_mfma_f32_16x16x32_bf16 v[124:127], v[112:115], v[104:107], 0\n"
      "v_cvt_pk_bf16_f32 v131, v184, v185\n"
      "v_sub_f32 v140, v140, v133\n"
      "v_mfma_f32_16x16x32_bf16 v[124:127], v[76:79], v[92:95], v[124:127]\n"
      "v_sub_f32 v141, v141, v133\n"
      "v_sub_f32 v142, v142, v133\n"
      "v_sub_f32 v143, v143, v133\n"
      "v_sub_f32 v134, v134, v133\n"
      "v_exp_f32 v140, v140\n"
      "v_exp_f32_e64 v141, v141\n"
      "v_exp_f32_e64 v142, v142\n"
      "v_exp_f32_e64 v143, v143\n"
      "v_exp_f32 v134, v134\n"
      "v_cvt_pk_bf16_f32 v88, v140, v141\n"
      "v_mfma_f32_16x16x32_bf16 v[182:185], v[192:195], v[104:107], 0\n"
      "s_waitcnt lgkmcnt(0)\n"
      "v_cvt_pk_bf16_f32 v89, v142, v143\n"
      "v_cvt_pk_bf16_f32 v90, v134, 0\n"
      "v_mfma_f32_16x16x32_bf16 v[182:185], v[80:83], v[92:95], v[182:185]\n"
      "ds_read_u16 v213, v213\n"
      "v_rcp_f32_e64 v100, v100\n"
      "v_mfma_f32_16x16x32_bf16 v[140:143], v[164:167], v[104:107], 0\n"
      "v_mul_f32 v124, v100, v124\n"
      "v_mul_f32 v125, v100, v125\n"
      "v_mfma_f32_16x16x32_bf16 v[140:143], v[84:87], v[92:95], v[140:143]\n"
      "ds_read_b128 v[104:107], v2 offset:12800\n"
      "v_cvt_pk_bf16_f32 v124, v124, v125\n"
      "v_mfma_f32_16x16x32_bf16 v[132:135], v[160:163], v[144:147], v[30:33]\n"
      "v_mul_f32 v126, v100, v126\n"
      "v_mul_f32 v127, v100, v127\n"
      "v_mfma_f32_16x16x32_bf16 v[144:147], v[160:163], v[152:155], v[30:33]\n"
      "v_cvt_pk_bf16_f32 v125, v126, v127\n"
      "v_mul_f32 v182, v100, v182\n"
      "v_mfma_f32_16x16x32_bf16 v[152:155], v[160:163], v[204:207], v[30:33]\n"
      "v_mul_f32 v183, v100, v183\n"
      "v_mul_f32_e64 v184, v100, v184\n"
      "v_mfma_f32_16x16x32_bf16 v[204:207], v[160:163], v[156:159], v[30:33]\n"
      "ds_read_b128 v[156:159], v2 offset:15872\n"
      "s_waitcnt lgkmcnt(2)\n"
      "v_lshlrev_b32 v213, 16, v213\n"
      "v_mfma_f32_16x16x16_bf16 v[132:135], v[202:203], v[186:187], v[132:135]\n"
      "v_mul_f32 v185, v100, v185\n"
      "v_cvt_pk_bf16_f32 v126, v182, v183\n"
      "v_cvt_pk_bf16_f32 v127, v184, v185\n"
      "v_mfma_f32_16x16x16_bf16 v[144:147], v[202:203], v[200:201], v[144:147]\n"
      "v_mul_f32 v189, v176, v189\n"
      "v_mul_f32 v140, v100, v140\n"
      "v_mul_f32 v141, v100, v141\n"
      "v_mfma_f32_16x16x16_bf16 v[152:155], v[202:203], v[168:169], v[152:155]\n"
      "v_cvt_pk_bf16_f32 v188, v188, v189\n"
      "v_mul_f32 v190, v176, v190\n"
      "v_mul_f32 v191, v176, v191\n"
      "v_mfma_f32_16x16x16_bf16 v[204:207], v[202:203], v[98:99], v[204:207]\n"
      "v_cndmask_b32 v132, v132, v144, s[58:59]\n"
      "v_cvt_pk_bf16_f32 v140, v140, v141\n"
      "v_mul_f32_e64 v142, v100, v142\n"
      "v_mfma_f32_16x16x32_bf16 v[136:139], a[228:231], v[108:111], v[136:139]\n"
      "s_waitcnt lgkmcnt(1)\n"
      "v_cndmask_b32 v132, v132, v152, s[60:61]\n"
      "v_mul_f32 v143, v100, v143\n"
      "v_mfma_f32_16x16x32_bf16 v[136:139], a[232:235], v[104:107], v[136:139]\n"
      "s_waitcnt lgkmcnt(0)\n"
      "v_cndmask_b32 v132, v132, v204, s[62:63]\n"
      "v_fmac_f32 v132, s53, v213\n"
      "v_mfma_f32_16x16x32_bf16 v[136:139], a[236:239], v[156:159], v[136:139]\n"
      "ds_write_b128 v3, v[124:127] offset:18688\n"
      "v_mfma_f32_16x16x32_bf16 v[196:199], a[252:255], v[108:111], v[196:199]\n"
      "v_max_f32_dpp v176, v132, v132 row_ror:8 row_mask:0xf bank_mask:0xf\n"
      "v_cvt_pk_bf16_f32 v189, v190, v191\n"
      "v_mfma_f32_16x16x32_bf16 v[196:199], v[224:227], v[104:107], v[196:199]\n"
      "v_max_f32_dpp v176, v176, v176 row_ror:4 row_mask:0xf bank_mask:0xf\n"
      "v_cvt_pk_bf16_f32 v141, v142, v143\n"
      "v_mfma_f32_16x16x32_bf16 v[196:199], v[228:231], v[156:159], v[196:199]\n"
      "ds_write_b64 v4, v[188:189] offset:18432\n"
      "v_mfma_f32_16x16x32_bf16 v[178:181], v[244:247], v[108:111], v[178:181]\n"
      "v_max_f32_dpp v176, v176, v176 row_ror:2 row_mask:0xf bank_mask:0xf\n"
      "v_fma_f32 v204, v136, v136, s52\n"
      "v_mfma_f32_16x16x32_bf16 v[178:181], v[248:251], v[104:107], v[178:181]\n"
      "v_max_f32_dpp v176, v176, v176 row_ror:1 row_mask:0xf bank_mask:0xf\n"
      "v_fmac_f32_e64 v204, v137, v137\n"
      "v_mfma_f32_16x16x32_bf16 v[178:181], v[252:255], v[156:159], v[178:181]\n"
      "ds_write_b64 v4, v[140:141] offset:18688\n"
      "v_mfma_f32_16x16x32_bf16 v[102:105], v[32:35], v[128:131], 0\n"
      "v_fmac_f32 v204, v138, v138\n"
      "v_max_f32_dpp v176, v176, v176 row_bcast:15 row_mask:0xa bank_mask:0xf\n"
      "v_mfma_f32_16x16x32_bf16 v[102:105], v[32:35], v[88:91], v[102:105]\n"
      "v_fmac_f32 v204, v139, v139\n"
      "v_fmac_f32 v204, v196, v196\n"
      "v_mfma_f32_16x16x32_bf16 v[140:143], v[112:115], v[128:131], 0\n"
      "v_fmac_f32 v204, v197, v197\n"
      "v_fmac_f32_e64 v204, v198, v198\n"
      "v_mfma_f32_16x16x32_bf16 v[140:143], v[76:79], v[88:91], v[140:143]\n"
      "v_max_f32_dpp v176, v176, v176 row_bcast:31 row_mask:0xc bank_mask:0xf\n"
      "v_fmac_f32 v204, v199, v199\n"
      "v_mfma_f32_16x16x32_bf16 v[98:101], v[192:195], v[128:131], 0\n"
      "v_fmac_f32 v204, v178, v178\n"
      "v_readlane_b32 s72, v176, 63\n"
      "v_mfma_f32_16x16x32_bf16 v[98:101], v[80:83], v[88:91], v[98:101]\n"
      "v_fmac_f32 v204, v179, v179\n"
      "v_fmac_f32 v204, v180, v180\n"
      "v_mfma_f32_16x16x32_bf16 v[188:191], v[164:167], v[128:131], 0\n"
      "v_fmac_f32 v204, v181, v181\n"
      "v_mov_b32 v174, v204\n"
      "v_mfma_f32_16x16x32_bf16 v[188:191], v[84:87], v[88:91], v[188:191]\n"
      "v_subrev_f32 v132, s72, v132\n"
      "v_rcp_f32 v102, v102\n"
      "v_exp_f32 v132, v132\n"
      "v_permlane16_swap_b32 v204, v174\n"
      "v_mul_f32 v140, v102, v140\n"
      "v_mul_f32 v141, v102, v141\n"
      "v_cvt_pk_bf16_f32 v132, v132, v132\n"
      "v_cvt_pk_bf16_f32 v140, v140, v141\n"
      "v_mul_f32 v142, v102, v142\n"
      "v_mul_f32 v143, v102, v143\n"
      "ds_write_b16 v13, v132\n"
      "ds_read_b128 v[104:107], v14\n"
      "ds_read_b128 v[156:159], v14 offset:64\n"
      "v_add_f32 v204, v204, v174\n"
      "v_cvt_pk_bf16_f32 v141, v142, v143\n"
      "v_mul_f32 v98, v102, v98\n"
      "v_mul_f32 v99, v102, v99\n"
      "v_mul_f32 v100, v102, v100\n"
      "v_mul_f32 v101, v102, v101\n"
      "v_cvt_pk_bf16_f32 v142, v98, v99\n"
      "v_cvt_pk_bf16_f32 v143, v100, v101\n"
      "v_mul_f32 v188, v102, v188\n"
      "v_mul_f32 v189, v102, v189\n"
      "ds_write_b128 v3, v[140:143] offset:18944\n"
      "v_cvt_pk_bf16_f32 v188, v188, v189\n"
      "v_mul_f32 v190, v102, v190\n"
      "v_mul_f32 v191, v102, v191\n"
      "v_permlane32_swap_b32 v212, v204\n"
      "v_cvt_pk_bf16_f32 v189, v190, v191\n"
      "s_waitcnt lgkmcnt(2)\n"
      "v_mfma_f32_16x16x32_bf16 v[100:103], v[104:107], v[32:35], 0\n"
      "ds_write_b64 v4, v[188:189] offset:18944\n"
      "s_waitcnt lgkmcnt(2)\n"
      "v_mfma_f32_16x16x32_bf16 v[100:103], v[156:159], v[32:35], v[100:103]\n"
      "v_add_f32_e64 v212, v212, v204\n"
      "v_add_u32 v10, 2336, v10\n"
      "v_add_u32 v11, 2336, v11\n"
      "ds_write_b32 v31, v212 offset:1024\n"
      "s_waitcnt lgkmcnt(0)\n"
      "s_barrier\n"
      "s_cbranch_vccz L_sel0_%=\n"
      "L_disp0_%=:\n"
      "s_cmp_lt_u32 s95, 0x40000000\n"
      "s_cbranch_scc1 L_nofix_18_%=\n"
      "s_cmp_eq_u32 s95, -2\n"
      "s_cbranch_scc1 L_tail0_%=\n"
      "s_nop 2\n"
      "v_lshlrev_b32 v140, 3, v36\n"
      "s_add_u32 s72, s98, L_cold_%=-L_ipf0_%=\n"
      "s_addc_u32 s73, s99, 0\n"
      "s_mov_b32 s74, L_cend_%=-L_cold_%=\n"
      "s_mov_b32 s75, 0x20000\n"
      "s_mov_b32 m0, 161792\n"
      "s_nop 0\n"
      "buffer_load_dwordx4 v140, s[72:75], 0 offen  lds\n"
      "s_lshr_b32 s92, s45, 30\n"
      "s_or_b32 s92, s92, s66\n"
      "s_cbranch_scc0 L_nf_19_%=\n"
      "s_bitcmp1_b32 s45, 31\n"
      "s_cselect_b32 s75, 100270464, 0\n"
      "s_bitcmp1_b32 s45, 30\n"
      "s_cselect_b32 s76, 196224, 0\n"
      "s_and_b32 s74, s45, 0x3fffffff\n"
      "s_cmp_ge_u32 s45, 0x40000000\n"
      "s_cselect_b32 s74, s74, 0\n"
      "s_cselect_b32 s45, 0, s45\n"
      "s_sub_u32 s74, s74, s66\n"
      "s_add_u32 s66, s66, s74\n"
      "s_sub_u32 s75, s75, s67\n"
      "s_add_u32 s67, s67, s75\n"
      "s_sub_u32 s76, s76, s68\n"
      "s_add_u32 s68, s68, s76\n"
      "v_add_u32 v16, s74, v16\n"
      "v_add_u32 v17, s74, v17\n"
      "v_add_u32 v18, s74, v18\n"
      "v_add_u32 v19, s74, v19\n"
      "v_add_u32 v15, s74, v15\n"
      "s_cmp_eq_u32 s75, 0\n"
      "s_cbranch_scc1 L_ax_20_%=\n"
      "v_bfe_i32 v140, v27, 0, 1\n"
      "v_and_b32 v140, s75, v140\n"
      "v_sub_u32 v16, v16, v140\n"
      "v_bfe_i32 v140, v27, 2, 1\n"
      "v_and_b32 v140, s75, v140\n"
      "v_sub_u32 v17, v17, v140\n"
      "v_bfe_i32 v140, v27, 4, 1\n"
      "v_and_b32 v140, s75, v140\n"
      "v_sub_u32 v18, v18, v140\n"
      "v_bfe_i32 v140, v27, 6, 1\n"
      "v_and_b32 v140, s75, v140\n"
      "v_sub_u32 v19, v19, v140\n"
      "v_bfe_i32 v140, v27, 8, 1\n"
      "v_and_b32 v140, s75, v140\n"
      "v_sub_u32 v15, v15, v140\n"
      "L_ax_20_%=:\n"
      "s_cmp_eq_u32 s76, 0\n"
      "s_cbranch_scc1 L_ax_21_%=\n"
      "v_bfe_i32 v140, v27, 1, 1\n"
      "v_and_b32 v140, s76, v140\n"
      "v_sub_u32 v16, v16, v140\n"
      "v_bfe_i32 v140, v27, 3, 1\n"
      "v_and_b32 v140, s76, v140\n"
      "v_sub_u32 v17, v17, v140\n"
      "v_bfe_i32 v140, v27, 5, 1\n"
      "v_and_b32 v140, s76, v140\n"
      "v_sub_u32 v18, v18, v140\n"
      "v_bfe_i32 v140, v27, 7, 1\n"
      "v_and_b32 v140, s76, v140\n"
      "v_sub_u32 v19, v19, v140\n"
      "v_bfe_i32 v140, v27, 9, 1\n"
      "v_and_b32 v140, s76, v140\n"
      "v_sub_u32 v15, v15, v140\n"
      "L_ax_21_%=:\n"
      "s_or_b32 s48, s66, s69\n"
      "s_cmp_lg_u32 s48, 0\n"
      "s_cselect_b32 s48, 0x40000000, 0\n"
      "L_nf_19_%=:\n"
      "s_lshr_b32 s92, s46, 30\n"
      "s_or_b32 s92, s92, s69\n"
      "s_cbranch_scc0 L_nf_22_%=\n"
      "s_bitcmp1_b32 s46, 31\n"
      "s_cselect_b32 s75, 100270464, 0\n"
      "s_bitcmp1_b32 s46, 30\n"
      "s_cselect_b32 s76, 196224, 0\n"
      "s_and_b32 s74, s46, 0x3fffffff\n"
      "s_cmp_ge_u32 s46, 0x40000000\n"
      "s_cselect_b32 s74, s74, 0\n"
      "s_cselect_b32 s46, 0, s46\n"
      "s_sub_u32 s74, s74, s69\n"
      "s_add_u32 s69, s69, s74\n"
      "s_sub_u32 s75, s75, s70\n"
      "s_add_u32 s70, s70, s75\n"
      "s_sub_u32 s76, s76, s71\n"
      "s_add_u32 s71, s71, s76\n"
      "v_add_u32 v20, s74, v20\n"
      "v_add_u32 v21, s74, v21\n"
      "v_add_u32 v22, s74, v22\n"
      "v_add_u32 v23, s74, v23\n"
      "v_add_u32 v25, s74, v25\n"
      "s_cmp_eq_u32 s75, 0\n"
      "s_cbranch_scc1 L_ax_23_%=\n"
      "v_bfe_i32 v140, v27, 10, 1\n"
      "v_and_b32 v140, s75, v140\n"
      "v_sub_u32 v20, v20, v140\n"
      "v_bfe_i32 v140, v27, 12, 1\n"
      "v_and_b32 v140, s75, v140\n"
      "v_sub_u32 v21, v21, v140\n"
      "v_bfe_i32 v140, v27, 14, 1\n"
      "v_and_b32 v140, s75, v140\n"
      "v_sub_u32 v22, v22, v140\n"
      "v_bfe_i32 v140, v27, 16, 1\n"
      "v_and_b32 v140, s75, v140\n"
      "v_sub_u32 v23, v23, v140\n"
      "v_bfe_i32 v140, v27, 18, 1\n"
      "v_and_b32 v140, s75, v140\n"
      "v_sub_u32 v25, v25, v140\n"
      "L_ax_23_%=:\n"
      "s_cmp_eq_u32 s76, 0\n"
      "s_cbranch_scc1 L_ax_24_%=\n"
      "v_bfe_i32 v140, v27, 11, 1\n"
      "v_and_b32 v140, s76, v140\n"
      "v_sub_u32 v20, v20, v140\n"
      "v_bfe_i32 v140, v27, 13, 1\n"
      "v_and_b32 v140, s76, v140\n"
      "v_sub_u32 v21, v21, v140\n"
      "v_bfe_i32 v140, v27, 15, 1\n"
      "v_and_b32 v140, s76, v140\n"
      "v_sub_u32 v22, v22, v140\n"
      "v_bfe_i32 v140, v27, 17, 1\n"
      "v_and_b32 v140, s76, v140\n"
      "v_sub_u32 v23, v23, v140\n"
      "v_bfe_i32 v140, v27, 19, 1\n"
      "v_and_b32 v140, s76, v140\n"
      "v_sub_u32 v25, v25, v140\n"
      "L_ax_24_%=:\n"
      "s_or_b32 s48, s66, s69\n"
      "s_cmp_lg_u32 s48, 0\n"
      "s_cselect_b32 s48, 0x40000000, 0\n"
      "L_nf_22_%=:\n"
      "s_sub_u32 s92, s42, 1\n"
      "s_cmp_gt_u32 s92, s44\n"
      "s_cbranch_scc1 L_tail0_%=\n"
      "L_nofix_18_%=:\n"
      "s_cmp_lg_u64 s[56:57], 0\n"
      "s_cbranch_scc1 L_slow0_%=\n"
      "L_sel0_%=:\n"
      "s_cmp_lg_u64 s[54:55], 0\n"
      "s_cbranch_scc1 L_max0_%=\n"
      "s_branch L_loop0_%=\n"
      "L_slow0_%=:\n"
      "s_nop 4\n"
      "v_mfma_f32_16x16x32_bf16 v[140:143], v[104:107], v[112:115], 0\n"
      "ds_read_b128 v[174:177], v0\n"
      "v_subrev_u32 v212, 115456, v9\n"
      "v_mfma_f32_16x16x32_bf16 v[140:143], v[156:159], v[76:79], v[140:143]\n"
      "ds_read_b128 v[204:207], v1\n"
      "v_subrev_u32 v76, 115456, v9\n"
      "v_mfma_f32_16x16x32_bf16 v[112:115], v[104:107], v[192:195], 0\n"
      "ds_read_b128 v[124:127], v0 offset:6144\n"
      "v_bfe_u32 v77, v76, 4, 4\n"
      "v_mfma_f32_16x16x32_bf16 v[112:115], v[156:159], v[80:83], v[112:115]\n"
      "ds_read_b128 v[186:189], v9 offset:1024\n"
      "v_mul_u32_u24 v78, 98, v77\n"
      "v_mfma_f32_16x16x32_bf16 v[190:193], v[104:107], v[164:167], 0\n"
      "s_add_u32 s72, s42, 1\n"
      "v_readlane_b32 s77, v38, s72\n"
      "v_lshrrev_b32 v76, 8, v76\n"
      "v_mfma_f32_16x16x32_bf16 v[190:193], v[156:159], v[84:87], v[190:193]\n"
      "s_mov_b32 m0, 110592\n"
      "v_mov_b32_dpp v100, v100 quad_perm:[0,0,0,0] row_mask:0xf bank_mask:0xf\n"
      "v_rcp_f32 v100, v100\n"
      "s_waitcnt lgkmcnt(3)\n"
      "v_mfma_f32_16x16x32_bf16 v[128:131], a[0:3], v[174:177], 0\n"
      "s_and_b32 s73, s77, -16\n"
      "v_cndmask_b32 v140, v140, v112, s[58:59]\n"
      "v_lshl_add_u32 v78, v76, 3, v78\n"
      "v_mfma_f32_16x16x32_bf16 v[182:185], a[24:27], v[174:177], 0\n"
      "buffer_load_dwordx4 v212, s[24:27], s73 offen  lds\n"
      "v_mfma_f32_16x16x32_bf16 v[142:145], a[48:51], v[174:177], 0\n"
      "s_mov_b32 m0, 111616\n"
      "s_waitcnt lgkmcnt(0)\n"
      "v_add_f32_e64 v186, v186, v187\n"
      "v_mfma_f32_16x16x32_bf16 v[108:111], a[72:75], v[174:177], 0\n"
      "ds_read_b128 v[132:135], v1 offset:6144\n"
      "v_add_f32_e64 v188, v188, v189\n"
      "v_mfma_f32_16x16x32_bf16 v[156:159], a[96:99], v[174:177], 0\n"
      "ds_read_b128 v[160:163], v0 offset:12288\n"
      "v_add_f32 v186, v186, v188\n"
      "v_mfma_f32_16x16x32_bf16 v[96:99], a[120:123], v[174:177], 0\n"
      "ds_read_b128 v[102:105], v1 offset:12288\n"
      "v_rsq_f32 v186, v186\n"
      "v_mfma_f32_16x16x32_bf16 v[128:131], a[4:7], v[204:207], v[128:131]\n"
      "s_add_u32 s73, s73, 1024\n"
      "v_mul_f32 v186, 0x415db3d7, v186\n"
      "v_mov_b32_e64 v188, v186\n"
      "v_mfma_f32_16x16x32_bf16 v[128:131], a[8:11], v[124:127], v[128:131]\n"
      "buffer_load_dwordx4 v212, s[24:27], s73 offen  lds\n"
      "v_mfma_f32_16x16x32_bf16 v[182:185], a[28:31], v[204:207], v[182:185]\n"
      "s_mov_b32 m0, 112640\n"
      "v_permlane32_swap_b32 v186, v188\n"
      "v_mfma_f32_16x16x32_bf16 v[182:185], a[32:35], v[124:127], v[182:185]\n"
      "s_add_u32 s73, s73, 1024\n"
      "v_mov_b32 v187, v186\n"
      "v_mul_f32_e64 v136, v188, v136\n"
      "v_mfma_f32_16x16x32_bf16 v[142:145], a[52:55], v[204:207], v[142:145]\n"
      "s_and_b32 s77, s77, 15\n"
      "v_permlane16_swap_b32 v186, v187\n"
      "v_mfma_f32_16x16x32_bf16 v[142:145], a[56:59], v[124:127], v[142:145]\n"
      "buffer_load_dwordx4 v212, s[24:27], s73 offen  lds\n"
      "v_mfma_f32_16x16x32_bf16 v[108:111], a[76:79], v[204:207], v[108:111]\n"
      "s_mov_b32 m0, 113664\n"
      "v_mul_f32 v220, v186, v220\n"
      "v_mul_f32 v221, v186, v221\n"
      "v_mfma_f32_16x16x32_bf16 v[108:111], a[80:83], v[124:127], v[108:111]\n"
      "s_add_u32 s73, s73, 1024\n"
      "v_cvt_pk_bf16_f32 v220, v220, v221\n"
      "v_mul_f32 v222, v186, v222\n"
      "v_mfma_f32_16x16x32_bf16 v[156:159], a[100:103], v[204:207], v[156:159]\n"
      "s_waitcnt lgkmcnt(0)\n"
      "v_mul_f32 v223, v186, v223\n"
      "v_mul_f32 v214, v186, v214\n"
      "v_mfma_f32_16x16x32_bf16 v[156:159], a[104:107], v[124:127], v[156:159]\n"
      "buffer_load_dwordx4 v212, s[24:27], s73 offen  lds\n"
      "v_mfma_f32_16x16x32_bf16 v[96:99], a[124:127], v[204:207], v[96:99]\n"
      "s_mov_b32 m0, 114688\n"
      "v_mul_f32_e64 v215, v186, v215\n"
      "v_cvt_pk_bf16_f32 v221, v222, v223\n"
      "v_mfma_f32_16x16x32_bf16 v[96:99], a[128:131], v[124:127], v[96:99]\n"
      "s_add_u32 s73, s73, 1024\n"
      "v_mul_f32 v208, v186, v208\n"
      "v_mul_f32 v209, v186, v209\n"
      "v_mfma_f32_16x16x32_bf16 v[128:131], a[12:15], v[132:135], v[128:131]\n"
      "v_mul_f32_e64 v210, v186, v210\n"
      "v_mul_f32 v211, v186, v211\n"
      "v_mfma_f32_16x16x32_bf16 v[128:131], a[16:19], v[160:163], v[128:131]\n"
      "s_mov_b32 exec_hi, 0xffff\n"
      "buffer_load_dwordx4 v212, s[24:27], s73 offen  lds\n"
      "s_mov_b32 exec_hi, -1\n"
      "v_mfma_f32_16x16x32_bf16 v[128:131], a[20:23], v[102:105], v[128:131]\n"
      "s_add_u32 m0, s64, 18432\n"
      "v_cvt_pk_bf16_f32 v214, v214, v215\n"
      "v_mul_f32_e64 v216, v186, v216\n"
      "v_mfma_f32_16x16x32_bf16 v[182:185], a[36:39], v[132:135], v[182:185]\n"
      "s_add_u32 s73, s73, 1024\n"
      "v_mul_f32_e64 v217, v186, v217\n"
      "v_cvt_pk_bf16_f32 v222, v208, v209\n"
      "v_mfma_f32_16x16x32_bf16 v[182:185], a[40:43], v[160:163], v[182:185]\n"
      "v_cvt_pk_bf16_f32 v223, v210, v211\n"
      "v_cvt_pk_bf16_f32 v215, v216, v217\n"
      "v_mfma_f32_16x16x32_bf16 v[182:185], a[44:47], v[102:105], v[182:185]\n"
      "ds_write_b128 v5, v[220:223]\n"
      "v_mfma_f32_16x16x32_bf16 v[142:145], a[60:63], v[132:135], v[142:145]\n"
      "buffer_load_dwordx4 v16, s[16:19], s45 offen  lds\n"
      "v_mfma_f32_16x16x32_bf16 v[142:145], a[64:67], v[160:163], v[142:145]\n"
      "s_add_u32 m0, s64, 22528\n"
      "v_mul_f32 v148, v187, v148\n"
      "v_mul_f32 v149, v187, v149\n"
      "v_mfma_f32_16x16x32_bf16 v[142:145], a[68:71], v[102:105], v[142:145]\n"
      "ds_write_b64 v6, v[214:215]\n"
      "v_mfma_f32_16x16x32_bf16 v[108:111], a[84:87], v[132:135], v[108:111]\n"
      "v_cvt_pk_bf16_f32 v148, v148, v149\n"
      "v_mul_f32_e64 v150, v187, v150\n"
      "v_mfma_f32_16x16x32_bf16 v[108:111], a[88:91], v[160:163], v[108:111]\n"
      "buffer_load_dwordx4 v17, s[16:19], s45 offen  lds\n"
      "v_mfma_f32_16x16x32_bf16 v[108:111], a[92:95], v[102:105], v[108:111]\n"
      "s_add_u32 m0, s64, 26624\n"
      "v_mul_f32 v151, v187, v151\n"
      "v_mul_f32 v120, v187, v120\n"
      "v_mfma_f32_16x16x32_bf16 v[156:159], a[108:111], v[132:135], v[156:159]\n"
      "v_mul_f32 v121, v187, v121\n"
      "v_cvt_pk_bf16_f32 v149, v150, v151\n"
      "v_mfma_f32_16x16x32_bf16 v[156:159], a[112:115], v[160:163], v[156:159]\n"
      "v_mul_f32 v170, v187, v170\n"
      "v_mul_f32_e64 v171, v187, v171\n"
      "v_mfma_f32_16x16x32_bf16 v[156:159], a[116:119], v[102:105], v[156:159]\n"
      "buffer_load_dwordx4 v18, s[16:19], s45 offen  lds\n"
      "v_mfma_f32_16x16x32_bf16 v[96:99], a[132:135], v[132:135], v[96:99]\n"
      "s_add_u32 m0, s64, 30720\n"
      "v_mul_f32 v172, v187, v172\n"
      "v_mul_f32 v173, v187, v173\n"
      "v_mfma_f32_16x16x32_bf16 v[96:99], a[136:139], v[160:163], v[96:99]\n"
      "v_cvt_pk_bf16_f32 v120, v120, v121\n"
      "v_mul_f32 v122, v187, v122\n"
      "v_mfma_f32_16x16x32_bf16 v[96:99], a[140:143], v[102:105], v[96:99]\n"
      "v_mul_f32 v123, v187, v123\n"
      "v_cvt_pk_bf16_f32 v150, v170, v171\n"
      "v_mfma_f32_16x16x32_bf16 v[112:115], v[174:177], a[144:147], 0\n"
      "buffer_load_dwordx4 v19, s[16:19], s45 offen  lds\n"
      "v_mfma_f32_16x16x32_bf16 v[112:115], v[204:207], a[148:151], v[112:115]\n"
      "s_add_u32 m0, s65, 34816\n"
      "v_cvt_pk_bf16_f32 v151, v172, v173\n"
      "v_cvt_pk_bf16_f32 v121, v122, v123\n"
      "v_mfma_f32_16x16x32_bf16 v[112:115], v[124:127], a[152:155], v[112:115]\n"
      "ds_write_b128 v5, v[148:151] offset:6144\n"
      "v_mfma_f32_16x16x32_bf16 v[112:115], v[132:135], a[156:159], v[112:115]\n"
      "buffer_load_dwordx4 v15, s[16:19], s45 offen  lds\n"
      "v_mfma_f32_16x16x32_bf16 v[112:115], v[160:163], a[160:163], v[112:115]\n"
      "ds_read_b128 v[148:151], v0 offset:2048\n"
      "v_mul_f32_e64 v137, v188, v137\n"
      "v_mfma_f32_16x16x32_bf16 v[112:115], v[102:105], a[164:167], v[112:115]\n"
      "ds_write_b64 v6, v[120:121] offset:6144\n"
      "v_mfma_f32_16x16x32_bf16 v[192:195], v[174:177], a[168:171], 0\n"
      "ds_read_b128 v[116:119], v1 offset:2048\n"
      "v_cvt_pk_bf16_f32 v136, v136, v137\n"
      "v_mfma_f32_16x16x32_bf16 v[192:195], v[204:207], a[172:175], v[192:195]\n"
      "ds_read_b128 v[212:215], v0 offset:8192\n"
      "v_mul_f32_e64 v138, v188, v138\n"
      "v_mfma_f32_16x16x32_bf16 v[192:195], v[124:127], a[176:179], v[192:195]\n"
      "ds_read_b128 v[208:211], v1 offset:8192\n"
      "v_mul_f32_e64 v139, v188, v139\n"
      "v_mfma_f32_16x16x32_bf16 v[192:195], v[132:135], a[180:183], v[192:195]\n"
      "ds_read_b128 v[152:155], v0 offset:14336\n"
      "v_mul_f32 v178, v188, v178\n"
      "v_mfma_f32_16x16x32_bf16 v[192:195], v[160:163], a[184:187], v[192:195]\n"
      "ds_read_b128 v[216:219], v1 offset:14336\n"
      "v_mul_f32 v179, v188, v179\n"
      "v_mfma_f32_16x16x32_bf16 v[192:195], v[102:105], a[188:191], v[192:195]\n"
      "buffer_load_dwordx4 v[200:203], v36, s[24:27], s47 offen\n"
      "v_mfma_f32_16x16x32_bf16 v[164:167], v[174:177], a[192:195], 0\n"
      "v_cvt_pk_bf16_f32 v137, v138, v139\n"
      "v_mul_f32 v196, v188, v196\n"
      "v_mfma_f32_16x16x32_bf16 v[164:167], v[204:207], a[196:199], v[164:167]\n"
      "v_mul_f32 v197, v188, v197\n"
      "v_mul_f32 v198, v188, v198\n"
      "v_mfma_f32_16x16x32_bf16 v[164:167], v[124:127], a[200:203], v[164:167]\n"
      "s_waitcnt lgkmcnt(3)\n"
      "v_mul_f32_e64 v199, v188, v199\n"
      "v_cvt_pk_bf16_f32 v178, v178, v179\n"
      "v_mfma_f32_16x16x32_bf16 v[164:167], v[132:135], a[204:207], v[164:167]\n"
      "buffer_load_dword v172, v29, s[24:27], s47 offen\n"
      "v_mfma_f32_16x16x32_bf16 v[164:167], v[160:163], a[208:211], v[164:167]\n"
      "v_mul_f32 v180, v188, v180\n"
      "v_mul_f32_e64 v181, v188, v181\n"
      "v_mfma_f32_16x16x32_bf16 v[164:167], v[102:105], a[212:215], v[164:167]\n"
      "s_waitcnt lgkmcnt(0)\n"
      "v_cvt_pk_bf16_f32 v138, v196, v197\n"
      "v_cvt_pk_bf16_f32 v139, v198, v199\n"
      "v_mfma_f32_16x16x32_bf16 v[204:207], a[0:3], v[148:151], 0\n"
      "ds_write_b128 v5, v[136:139] offset:12288\n"
      "v_mfma_f32_16x16x32_bf16 v[204:207], a[4:7], v[116:119], v[204:207]\n"
      "v_cvt_pk_bf16_f32 v179, v180, v181\n"
      "v_fma_f32 v132, v128, v128, s51\n"
      "v_mfma_f32_16x16x32_bf16 v[204:207], a[8:11], v[212:215], v[204:207]\n"
      "v_fma_f32 v198, v108, v108, s51\n"
      "v_fmac_f32_e64 v132, v129, v129\n"
      "v_mfma_f32_16x16x32_bf16 v[204:207], a[12:15], v[208:211], v[204:207]\n"
      "ds_write_b64 v6, v[178:179] offset:12288\n"
      "v_mfma_f32_16x16x32_bf16 v[204:207], a[16:19], v[152:155], v[204:207]\n"
      "v_fmac_f32 v198, v109, v109\n"
      "v_fmac_f32 v132, v130, v130\n"
      "v_mfma_f32_16x16x32_bf16 v[204:207], a[20:23], v[216:219], v[204:207]\n"
      "v_fmac_f32 v198, v110, v110\n"
      "v_fmac_f32 v132, v131, v131\n"
      "v_mfma_f32_16x16x32_bf16 v[168:171], a[24:27], v[148:151], 0\n"
      "v_fmac_f32 v198, v111, v111\n"
      "v_fmac_f32 v132, v182, v182\n"
      "v_mfma_f32_16x16x32_bf16 v[168:171], a[28:31], v[116:119], v[168:171]\n"
      "v_fmac_f32 v198, v156, v156\n"
      "v_fmac_f32 v132, v183, v183\n"
      "v_mfma_f32_16x16x32_bf16 v[168:171], a[32:35], v[212:215], v[168:171]\n"
      "v_fmac_f32 v198, v157, v157\n"
      "v_fmac_f32 v132, v184, v184\n"
      "v_mfma_f32_16x16x32_bf16 v[168:171], a[36:39], v[208:211], v[168:171]\n"
      "v_fmac_f32 v198, v158, v158\n"
      "v_fmac_f32 v132, v185, v185\n"
      "v_mfma_f32_16x16x32_bf16 v[168:171], a[40:43], v[152:155], v[168:171]\n"
      "v_fmac_f32 v198, v159, v159\n"
      "v_fmac_f32 v132, v142, v142\n"
      "v_mfma_f32_16x16x32_bf16 v[168:171], a[44:47], v[216:219], v[168:171]\n"
      "v_fmac_f32 v198, v96, v96\n"
      "v_fmac_f32 v132, v143, v143\n"
      "v_mfma_f32_16x16x32_bf16 v[178:181], a[48:51], v[148:151], 0\n"
      "v_fmac_f32 v198, v97, v97\n"
      "v_fmac_f32 v132, v144, v144\n"
      "v_mfma_f32_16x16x32_bf16 v[178:181], a[52:55], v[116:119], v[178:181]\n"
      "v_fmac_f32 v198, v98, v98\n"
      "v_fmac_f32 v132, v145, v145\n"
      "v_mfma_f32_16x16x32_bf16 v[178:181], a[56:59], v[212:215], v[178:181]\n"
      "v_fmac_f32 v198, v99, v99\n"
      "v_cndmask_b32 v140, v140, v190, s[60:61]\n"
      "v_mfma_f32_16x16x32_bf16 v[178:181], a[60:63], v[208:211], v[178:181]\n"
      "v_permlane16_swap_b32 v132, v198\n"
      "v_mfma_f32_16x16x32_bf16 v[178:181], a[64:67], v[152:155], v[178:181]\n"
      "v_add_f32 v132, v132, v198\n"
      "v_mov_b32 v198, v132\n"
      "v_mfma_f32_16x16x32_bf16 v[178:181], a[68:71], v[216:219], v[178:181]\n"
      "v_mul_f32 v140, v100, v140\n"
      "v_permlane32_swap_b32 v132, v198\n"
      "v_mfma_f32_16x16x32_bf16 v[160:163], a[72:75], v[148:151], 0\n"
      "v_add_f32 v132, v132, v198\n"
      "v_rsq_f32 v132, v132\n"
      "v_mfma_f32_16x16x32_bf16 v[160:163], a[76:79], v[116:119], v[160:163]\n"
      "v_mov_b32_e64 v198, v132\n"
      "v_cvt_pk_bf16_f32 v140, v140, v140\n"
      "v_mfma_f32_16x16x32_bf16 v[160:163], a[80:83], v[212:215], v[160:163]\n"
      "ds_write_b16 v12, v140 offset:16\n"
      "v_mfma_f32_16x16x32_bf16 v[160:163], a[84:87], v[208:211], v[160:163]\n"
      "v_permlane16_swap_b32 v132, v198\n"
      "v_mfma_f32_16x16x32_bf16 v[160:163], a[88:91], v[152:155], v[160:163]\n"
      "v_mul_f32 v132, s50, v132\n"
      "v_mul_f32 v108, v198, v108\n"
      "v_mfma_f32_16x16x32_bf16 v[160:163], a[92:95], v[216:219], v[160:163]\n"
      "v_mul_f32 v109, v198, v109\n"
      "v_mul_f32 v128, v132, v128\n"
      "v_mfma_f32_16x16x32_bf16 v[188:191], a[96:99], v[148:151], 0\n"
      "v_mul_f32 v129, v132, v129\n"
      "v_cvt_pk_bf16_f32 v108, v108, v109\n"
      "v_mfma_f32_16x16x32_bf16 v[188:191], a[100:103], v[116:119], v[188:191]\n"
      "v_mul_f32 v110, v198, v110\n"
      "v_mul_f32 v111, v198, v111\n"
      "v_mfma_f32_16x16x32_bf16 v[188:191], a[104:107], v[212:215], v[188:191]\n"
      "v_mul_f32 v96, v198, v96\n"
      "v_mul_f32 v97, v198, v97\n"
      "v_mfma_f32_16x16x32_bf16 v[188:191], a[108:111], v[208:211], v[188:191]\n"
      "v_cvt_pk_bf16_f32 v128, v128, v129\n"
      "v_mul_f32 v130, v132, v130\n"
      "v_mfma_f32_16x16x32_bf16 v[188:191], a[112:115], v[152:155], v[188:191]\n"
      "v_mul_f32 v131, v132, v131\n"
      "v_mul_f32_e64 v142, v132, v142\n"
      "v_mfma_f32_16x16x32_bf16 v[188:191], a[116:119], v[216:219], v[188:191]\n"
      "v_mul_f32 v143, v132, v143\n"
      "v_cvt_pk_bf16_f32 v109, v110, v111\n"
      "v_mfma_f32_16x16x32_bf16 v[102:105], a[120:123], v[148:151], 0\n"
      "v_mul_f32 v156, v198, v156\n"
      "v_mul_f32 v157, v198, v157\n"
      "v_mfma_f32_16x16x32_bf16 v[102:105], a[124:127], v[116:119], v[102:105]\n"
      "v_mul_f32 v158, v198, v158\n"
      "v_mul_f32_e64 v159, v198, v159\n"
      "v_mfma_f32_16x16x32_bf16 v[102:105], a[128:131], v[212:215], v[102:105]\n"
      "v_cvt_pk_bf16_f32 v96, v96, v97\n"
      "v_mul_f32 v98, v198, v98\n"
      "v_mfma_f32_16x16x32_bf16 v[102:105], a[132:135], v[208:211], v[102:105]\n"
      "v_mul_f32 v99, v198, v99\n"
      "v_cvt_pk_bf16_f32 v129, v130, v131\n"
      "v_mfma_f32_16x16x32_bf16 v[102:105], a[136:139], v[152:155], v[102:105]\n"
      "v_mul_f32 v182, v132, v182\n"
      "v_mul_f32 v183, v132, v183\n"
      "v_mfma_f32_16x16x32_bf16 v[102:105], a[140:143], v[216:219], v[102:105]\n"
      "v_mul_f32 v184, v132, v184\n"
      "v_mul_f32 v185, v132, v185\n"
      "v_mfma_f32_16x16x32_bf16 v[196:199], v[148:151], a[144:147], 0\n"
      "v_cvt_pk_bf16_f32 v142, v142, v143\n"
      "v_mul_f32 v144, v132, v144\n"
      "v_mfma_f32_16x16x32_bf16 v[196:199], v[116:119], a[148:151], v[196:199]\n"
      "v_mul_f32 v145, v132, v145\n"
      "v_cvt_pk_bf16_f32 v110, v156, v157\n"
      "v_mfma_f32_16x16x32_bf16 v[196:199], v[212:215], a[152:155], v[196:199]\n"
      "v_cvt_pk_bf16_f32 v111, v158, v159\n"
      "v_cvt_pk_bf16_f32 v97, v98, v99\n"
      "v_mfma_f32_16x16x32_bf16 v[196:199], v[208:211], a[156:159], v[196:199]\n"
      "ds_read_b128 v[120:123], v0 offset:4096\n"
      "v_cvt_pk_bf16_f32 v130, v182, v183\n"
      "v_mfma_f32_16x16x32_bf16 v[196:199], v[152:155], a[160:163], v[196:199]\n"
      "ds_read_b128 v[132:135], v1 offset:4096\n"
      "v_cvt_pk_bf16_f32 v131, v184, v185\n"
      "v_mfma_f32_16x16x32_bf16 v[196:199], v[216:219], a[164:167], v[196:199]\n"
      "ds_read_b128 v[124:127], v0 offset:10240\n"
      "v_cvt_pk_bf16_f32 v143, v144, v145\n"
      "v_mfma_f32_16x16x32_bf16 v[98:101], v[148:151], a[168:171], 0\n"
      "ds_read_b128 v[174:177], v1 offset:10240\n"
      "v_fma_f32 v144, v204, v204, s51\n"
      "v_mfma_f32_16x16x32_bf16 v[98:101], v[116:119], a[172:175], v[98:101]\n"
      "ds_read_b128 v[156:159], v0 offset:16384\n"
      "v_fma_f32 v145, v160, v160, s51\n"
      "v_mfma_f32_16x16x32_bf16 v[98:101], v[212:215], a[176:179], v[98:101]\n"
      "ds_read_b128 v[220:223], v1 offset:16384\n"
      "v_fmac_f32 v144, v205, v205\n"
      "v_mfma_f32_16x16x32_bf16 v[98:101], v[208:211], a[180:183], v[98:101]\n"
      "s_waitcnt vmcnt(7)\n"
      "v_fmac_f32 v145, v161, v161\n"
      "v_fmac_f32 v144, v206, v206\n"
      "v_mfma_f32_16x16x32_bf16 v[98:101], v[152:155], a[184:187], v[98:101]\n"
      "v_fmac_f32 v145, v162, v162\n"
      "v_fmac_f32 v144, v207, v207\n"
      "v_mfma_f32_16x16x32_bf16 v[98:101], v[216:219], a[188:191], v[98:101]\n"
      "v_fmac_f32 v145, v163, v163\n"
      "v_fmac_f32 v144, v168, v168\n"
      "v_mfma_f32_16x16x32_bf16 v[136:139], v[148:151], a[192:195], 0\n"
      "v_fmac_f32 v145, v188, v188\n"
      "v_fmac_f32 v144, v169, v169\n"
      "v_mfma_f32_16x16x32_bf16 v[136:139], v[116:119], a[196:199], v[136:139]\n"
      "v_fmac_f32 v145, v189, v189\n"
      "v_fmac_f32 v144, v170, v170\n"
      "v_mfma_f32_16x16x32_bf16 v[136:139], v[212:215], a[200:203], v[136:139]\n"
      "s_waitcnt lgkmcnt(0)\n"
      "v_fmac_f32 v145, v190, v190\n"
      "v_fmac_f32 v144, v171, v171\n"
      "v_mfma_f32_16x16x32_bf16 v[136:139], v[208:211], a[204:207], v[136:139]\n"
      "v_fmac_f32 v145, v191, v191\n"
      "v_fmac_f32 v144, v178, v178\n"
      "v_mfma_f32_16x16x32_bf16 v[136:139], v[152:155], a[208:211], v[136:139]\n"
      "v_fmac_f32 v145, v102, v102\n"
      "v_fmac_f32 v144, v179, v179\n"
      "v_mfma_f32_16x16x32_bf16 v[136:139], v[216:219], a[212:215], v[136:139]\n"
      "v_fmac_f32 v145, v103, v103\n"
      "v_fmac_f32 v144, v180, v180\n"
      "v_mfma_f32_16x16x32_bf16 v[152:155], a[0:3], v[120:123], 0\n"
      "v_fmac_f32 v145, v104, v104\n"
      "v_fmac_f32 v144, v181, v181\n"
      "v_mfma_f32_16x16x32_bf16 v[152:155], a[4:7], v[132:135], v[152:155]\n"
      "v_fmac_f32 v145, v105, v105\n"
      "v_add_u32 v78, s77, v78\n"
      "v_mfma_f32_16x16x32_bf16 v[152:155], a[8:11], v[124:127], v[152:155]\n"
      "v_permlane16_swap_b32 v144, v145\n"
      "v_mfma_f32_16x16x32_bf16 v[152:155], a[12:15], v[174:177], v[152:155]\n"
      "v_add_f32 v144, v144, v145\n"
      "v_mov_b32 v145, v144\n"
      "v_mfma_f32_16x16x32_bf16 v[152:155], a[16:19], v[156:159], v[152:155]\n"
      "v_and_b32 v93, 2, v78\n"
      "v_permlane32_swap_b32 v144, v145\n"
      "v_mfma_f32_16x16x32_bf16 v[152:155], a[20:23], v[220:223], v[152:155]\n"
      "v_add_f32 v144, v144, v145\n"
      "v_rsq_f32 v144, v144\n"
      "v_mfma_f32_16x16x32_bf16 v[184:187], a[24:27], v[120:123], 0\n"
      "v_mov_b32 v145, v144\n"
      "v_and_b32 v78, -4, v78\n"
      "v_mfma_f32_16x16x32_bf16 v[184:187], a[28:31], v[132:135], v[184:187]\n"
      "v_permlane16_swap_b32_e64 v144, v145\n"
      "v_mfma_f32_16x16x32_bf16 v[184:187], a[32:35], v[124:127], v[184:187]\n"
      "v_mul_f32 v144, s50, v144\n"
      "v_mul_f32 v160, v145, v160\n"
      "v_mfma_f32_16x16x32_bf16 v[184:187], a[36:39], v[174:177], v[184:187]\n"
      "v_mul_f32 v161, v145, v161\n"
      "v_mul_f32 v204, v144, v204\n"
      "v_mfma_f32_16x16x32_bf16 v[184:187], a[40:43], v[156:159], v[184:187]\n"
      "v_mul_f32 v205, v144, v205\n"
      "v_cvt_pk_bf16_f32 v160, v160, v161\n"
      "v_mfma_f32_16x16x32_bf16 v[184:187], a[44:47], v[220:223], v[184:187]\n"
      "v_mul_f32 v162, v145, v162\n"
      "v_mul_f32 v163, v145, v163\n"
      "v_mfma_f32_16x16x32_bf16 v[212:215], a[48:51], v[120:123], 0\n"
      "v_mul_f32 v102, v145, v102\n"
      "v_mul_f32_e64 v103, v145, v103\n"
      "v_mfma_f32_16x16x32_bf16 v[212:215], a[52:55], v[132:135], v[212:215]\n"
      "v_cvt_pk_bf16_f32 v204, v204, v205\n"
      "v_mul_f32 v206, v144, v206\n"
      "v_mfma_f32_16x16x32_bf16 v[212:215], a[56:59], v[124:127], v[212:215]\n"
      "v_mul_f32 v207, v144, v207\n"
      "v_mul_f32_e64 v178, v144, v178\n"
      "v_mfma_f32_16x16x32_bf16 v[212:215], a[60:63], v[174:177], v[212:215]\n"
      "v_mul_f32 v179, v144, v179\n"
      "v_cvt_pk_bf16_f32 v161, v162, v163\n"
      "v_mfma_f32_16x16x32_bf16 v[212:215], a[64:67], v[156:159], v[212:215]\n"
      "v_mul_f32 v188, v145, v188\n"
      "v_mul_f32 v189, v145, v189\n"
      "v_mfma_f32_16x16x32_bf16 v[212:215], a[68:71], v[220:223], v[212:215]\n"
      "v_mul_f32 v190, v145, v190\n"
      "v_mul_f32_e64 v191, v145, v191\n"
      "v_mfma_f32_16x16x32_bf16 v[116:119], a[72:75], v[120:123], 0\n"
      "v_cvt_pk_bf16_f32 v102, v102, v103\n"
      "v_mul_f32 v104, v145, v104\n"
      "v_mfma_f32_16x16x32_bf16 v[116:119], a[76:79], v[132:135], v[116:119]\n"
      "v_mul_f32 v105, v145, v105\n"
      "v_cvt_pk_bf16_f32 v205, v206, v207\n"
      "v_mfma_f32_16x16x32_bf16 v[116:119], a[80:83], v[124:127], v[116:119]\n"
      "v_mul_f32 v168, v144, v168\n"
      "v_mul_f32 v169, v144, v169\n"
      "v_mfma_f32_16x16x32_bf16 v[116:119], a[84:87], v[174:177], v[116:119]\n"
      "v_mul_f32 v170, v144, v170\n"
      "v_mul_f32 v171, v144, v171\n"
      "v_mfma_f32_16x16x32_bf16 v[116:119], a[88:91], v[156:159], v[116:119]\n"
      "v_cvt_pk_bf16_f32 v178, v178, v179\n"
      "v_mul_f32 v180, v144, v180\n"
      "v_mfma_f32_16x16x32_bf16 v[116:119], a[92:95], v[220:223], v[116:119]\n"
      "v_mul_f32 v181, v144, v181\n"
      "v_cvt_pk_bf16_f32 v162, v188, v189\n"
      "v_mfma_f32_16x16x32_bf16 v[144:147], a[96:99], v[120:123], 0\n"
      "v_cvt_pk_bf16_f32 v163, v190, v191\n"
      "v_cvt_pk_bf16_f32 v103, v104, v105\n"
      "v_mfma_f32_16x16x32_bf16 v[144:147], a[100:103], v[132:135], v[144:147]\n"
      "v_cvt_pk_bf16_f32 v206, v168, v169\n"
      "v_cvt_pk_bf16_f32 v207, v170, v171\n"
      "v_mfma_f32_16x16x32_bf16 v[144:147], a[104:107], v[124:127], v[144:147]\n"
      "v_cvt_pk_bf16_f32 v179, v180, v181\n"
      "v_fma_f32 v104, v152, v152, s51\n"
      "v_mfma_f32_16x16x32_bf16 v[144:147], a[108:111], v[174:177], v[144:147]\n"
      "v_fma_f32 v105, v116, v116, s51\n"
      "v_fmac_f32 v104, v153, v153\n"
      "v_mfma_f32_16x16x32_bf16 v[144:147], a[112:115], v[156:159], v[144:147]\n"
      "v_fmac_f32 v105, v117, v117\n"
      "v_fmac_f32 v104, v154, v154\n"
      "v_mfma_f32_16x16x32_bf16 v[144:147], a[116:119], v[220:223], v[144:147]\n"
      "v_fmac_f32 v105, v118, v118\n"
      "v_fmac_f32 v104, v155, v155\n"
      "v_mfma_f32_16x16x32_bf16 v[188:191], a[120:123], v[120:123], 0\n"
      "v_fmac_f32 v105, v119, v119\n"
      "v_fmac_f32 v104, v184, v184\n"
      "v_mfma_f32_16x16x32_bf16 v[188:191], a[124:127], v[132:135], v[188:191]\n"
      "v_fmac_f32 v104, v185, v185\n"
      "v_fmac_f32 v104, v186, v186\n"
      "v_mfma_f32_16x16x32_bf16 v[188:191], a[128:131], v[124:127], v[188:191]\n"
      "v_fmac_f32 v105, v144, v144\n"
      "v_fmac_f32 v105, v145, v145\n"
      "v_mfma_f32_16x16x32_bf16 v[188:191], a[132:135], v[174:177], v[188:191]\n"
      "v_fmac_f32 v105, v146, v146\n"
      "v_fmac_f32 v104, v187, v187\n"
      "v_mfma_f32_16x16x32_bf16 v[188:191], a[136:139], v[156:159], v[188:191]\n"
      "v_fmac_f32 v105, v147, v147\n"
      "v_fmac_f32 v104, v212, v212\n"
      "v_mfma_f32_16x16x32_bf16 v[188:191], a[140:143], v[220:223], v[188:191]\n"
      "v_fmac_f32 v104, v213, v213\n"
      "v_fmac_f32 v104, v214, v214\n"
      "v_mfma_f32_16x16x32_bf16 v[208:211], v[120:123], a[144:147], 0\n"
      "v_fmac_f32 v104, v215, v215\n"
      "v_add_u32 v140, 110592, v78\n"
      "v_mfma_f32_16x16x32_bf16 v[208:211], v[132:135], a[148:151], v[208:211]\n"
      "v_lshlrev_b32 v76, 2, v76\n"
      "v_sub_u32 v77, v77, v76\n"
      "v_mfma_f32_16x16x32_bf16 v[208:211], v[124:127], a[152:155], v[208:211]\n"
      "v_fmac_f32 v105, v188, v188\n"
      "v_fmac_f32 v105, v189, v189\n"
      "v_mfma_f32_16x16x32_bf16 v[208:211], v[174:177], a[156:159], v[208:211]\n"
      "ds_read_b128 v[168:171], v2 offset:18432\n"
      "v_fmac_f32 v105, v190, v190\n"
      "v_mfma_f32_16x16x32_bf16 v[208:211], v[156:159], a[160:163], v[208:211]\n"
      "ds_read_b128 v[148:151], v2 offset:21504\n"
      "v_fmac_f32 v105, v191, v191\n"
      "v_mfma_f32_16x16x32_bf16 v[208:211], v[220:223], a[164:167], v[208:211]\n"
      "ds_read_b128 v[180:183], v2 offset:24576\n"
      "v_permlane16_swap_b32 v104, v105\n"
      "v_add_f32 v104, v104, v105\n"
      "v_mov_b32 v105, v104\n"
      "v_lshlrev_b32 v77, 4, v77\n"
      "v_cmp_gt_u32 s[72:73], 64, v77\n"
      "v_permlane32_swap_b32 v104, v105\n"
      "v_add_f32 v104, v104, v105\n"
      "v_rsq_f32 v104, v104\n"
      "v_cvt_pk_bf16_f32 v112, v112, v113\n"
      "v_mov_b32 v105, v104\n"
      "v_cvt_pk_bf16_f32 v113, v114, v115\n"
      "v_mov_b32 v218, 0x3fb9\n"
      "v_permlane16_swap_b32 v104, v105\n"
      "v_mul_f32 v104, s50, v104\n"
      "v_mul_f32 v116, v105, v116\n"
      "v_mul_f32 v117, v105, v117\n"
      "v_mul_f32 v152, v104, v152\n"
      "v_mul_f32 v153, v104, v153\n"
      "v_cvt_pk_bf16_f32 v116, v116, v117\n"
      "v_mul_f32 v118, v105, v118\n"
      "v_mul_f32 v119, v105, v119\n"
      "v_mul_f32 v188, v105, v188\n"
      "v_mul_f32 v189, v105, v189\n"
      "v_cvt_pk_bf16_f32 v152, v152, v153\n"
      "v_mul_f32 v154, v104, v154\n"
      "v_mul_f32 v155, v104, v155\n"
      "v_mul_f32 v212, v104, v212\n"
      "v_mul_f32 v213, v104, v213\n"
      "v_cvt_pk_bf16_f32 v117, v118, v119\n"
      "v_mul_f32 v144, v105, v144\n"
      "v_mul_f32 v145, v105, v145\n"
      "v_mul_f32 v146, v105, v146\n"
      "v_mul_f32 v147, v105, v147\n"
      "v_cvt_pk_bf16_f32 v188, v188, v189\n"
      "v_mul_f32 v190, v105, v190\n"
      "v_mul_f32 v191, v105, v191\n"
      "v_cvt_pk_bf16_f32 v153, v154, v155\n"
      "v_mul_f32 v184, v104, v184\n"
      "v_mul_f32 v185, v104, v185\n"
      "v_mul_f32 v186, v104, v186\n"
      "v_mul_f32 v187, v104, v187\n"
      "v_cvt_pk_bf16_f32 v212, v212, v213\n"
      "v_mul_f32 v214, v104, v214\n"
      "v_mul_f32 v215, v104, v215\n"
      "v_cvt_pk_bf16_f32 v118, v144, v145\n"
      "v_mfma_f32_16x16x32_bf16 v[104:107], v[120:123], a[168:171], 0\n"
      "v_cvt_pk_bf16_f32 v119, v146, v147\n"
      "v_cvt_pk_bf16_f32 v189, v190, v191\n"
      "v_mfma_f32_16x16x32_bf16 v[104:107], v[132:135], a[172:175], v[104:107]\n"
      "v_cvt_pk_bf16_f32 v154, v184, v185\n"
      "v_cvt_pk_bf16_f32 v155, v186, v187\n"
      "v_mfma_f32_16x16x32_bf16 v[104:107], v[124:127], a[176:179], v[104:107]\n"
      "v_cvt_pk_bf16_f32 v213, v214, v215\n"
      "v_mov_b32_e64 v219, 0\n"
      "v_mfma_f32_16x16x32_bf16 v[104:107], v[174:177], a[180:183], v[104:107]\n"
      "v_lshlrev_b64 v[218:219], v77, v[218:219]\n"
      "v_cndmask_b32_e64 v218, 0, v218, s[72:73]\n"
      "v_mfma_f32_16x16x32_bf16 v[104:107], v[156:159], a[184:187], v[104:107]\n"
      "v_cndmask_b32_e64 v219, 0, v219, s[72:73]\n"
      "v_cmp_gt_u32 s[72:73], 64, v77\n"
      "v_mfma_f32_16x16x32_bf16 v[104:107], v[220:223], a[188:191], v[104:107]\n"
      "ds_read_b128 v[214:217], v7 offset:18432\n"
      "v_mov_b32 v190, 0xbb2b\n"
      "v_mfma_f32_16x16x32_bf16 v[144:147], v[120:123], a[192:195], 0\n"
      "ds_read_b128 v[184:187], v2 offset:27648\n"
      "v_mov_b32 v191, 0\n"
      "v_mfma_f32_16x16x32_bf16 v[144:147], v[132:135], a[196:199], v[144:147]\n"
      "ds_read_b128 v[132:135], v2 offset:30720\n"
      "v_lshlrev_b64 v[190:191], v77, v[190:191]\n"
      "v_mfma_f32_16x16x32_bf16 v[144:147], v[124:127], a[200:203], v[144:147]\n"
      "ds_read_b128 v[124:127], v7 offset:22528\n"
      "v_cndmask_b32_e64 v190, 0, v190, s[72:73]\n"
      "v_mfma_f32_16x16x32_bf16 v[144:147], v[174:177], a[204:207], v[144:147]\n"
      "ds_read_b128 v[120:123], v2 offset:33792\n"
      "v_cndmask_b32_e64 v191, 0, v191, s[72:73]\n"
      "v_mfma_f32_16x16x32_bf16 v[144:147], v[156:159], a[208:211], v[144:147]\n"
      "ds_read_b128 v[156:159], v7 offset:26624\n"
      "v_add_u32 v82, 112160, v78\n"
      "v_mfma_f32_16x16x32_bf16 v[144:147], v[220:223], a[212:215], v[144:147]\n"
      "s_waitcnt lgkmcnt(5)\n"
      "v_cvt_pk_bf16_f32 v114, v196, v197\n"
      "v_cvt_pk_bf16_f32 v115, v198, v199\n"
      "v_mfma_f32_16x16x32_bf16 v[220:223], a[216:219], v[168:171], 0\n"
      "buffer_store_dwordx4 v[214:217], v20, s[20:23], s46 offen sc1\n"
      "v_mfma_f32_16x16x32_bf16 v[220:223], a[220:223], v[148:151], v[220:223]\n"
      "ds_read_b128 v[196:199], v7 offset:30720\n"
      "v_cvt_pk_bf16_f32 v76, v208, v209\n"
      "v_mfma_f32_16x16x32_bf16 v[220:223], a[224:227], v[180:183], v[220:223]\n"
      "v_cvt_pk_bf16_f32 v77, v210, v211\n"
      "v_cvt_pk_bf16_f32 v192, v192, v193\n"
      "v_mfma_f32_16x16x32_bf16 v[208:211], a[240:243], v[168:171], 0\n"
      "v_cvt_pk_bf16_f32 v193, v194, v195\n"
      "v_cvt_pk_bf16_f32 v194, v98, v99\n"
      "v_mfma_f32_16x16x32_bf16 v[208:211], a[244:247], v[148:151], v[208:211]\n"
      "v_cvt_pk_bf16_f32 v195, v100, v101\n"
      "v_cvt_pk_bf16_f32 v80, v104, v105\n"
      "v_mfma_f32_16x16x32_bf16 v[208:211], a[248:251], v[180:183], v[208:211]\n"
      "ds_read_b128 v[98:101], v7 offset:34816\n"
      "v_cvt_pk_bf16_f32 v81, v106, v107\n"
      "v_mfma_f32_16x16x32_bf16 v[214:217], v[232:235], v[168:171], 0\n"
      "s_waitcnt lgkmcnt(2)\n"
      "v_cvt_pk_bf16_f32 v164, v164, v165\n"
      "v_cvt_pk_bf16_f32 v165, v166, v167\n"
      "v_mfma_f32_16x16x32_bf16 v[214:217], v[236:239], v[148:151], v[214:217]\n"
      "ds_read_b128 v[104:107], v10 offset:32\n"
      "v_cvt_pk_bf16_f32 v166, v136, v137\n"
      "v_mfma_f32_16x16x32_bf16 v[214:217], v[240:243], v[180:183], v[214:217]\n"
      "buffer_store_dwordx4 v[124:127], v21, s[20:23], s46 offen sc1\n"
      "v_mfma_f32_16x16x32_bf16 v[220:223], a[228:231], v[184:187], v[220:223]\n"
      "s_waitcnt lgkmcnt(2)\n"
      "v_cvt_pk_bf16_f32 v167, v138, v139\n"
      "v_cvt_pk_bf16_f32 v84, v144, v145\n"
      "v_mfma_f32_16x16x32_bf16 v[220:223], a[232:235], v[132:135], v[220:223]\n"
      "ds_read_b64 v[168:169], v10 offset:24\n"
      "v_cvt_pk_bf16_f32 v85, v146, v147\n"
      "v_mfma_f32_16x16x32_bf16 v[220:223], a[236:239], v[120:123], v[220:223]\n"
      "ds_read2_b32 v[148:149], v140 offset0:0 offset1:1\n"
      "v_add_u32 v94, 113728, v78\n"
      "v_mfma_f32_16x16x32_bf16 v[208:211], a[252:255], v[184:187], v[208:211]\n"
      "ds_read2_b32 v[176:177], v140 offset0:2 offset1:10\n"
      "v_subrev_u32 v8, 115456, v9\n"
      "v_mfma_f32_16x16x32_bf16 v[208:211], v[224:227], v[132:135], v[208:211]\n"
      "s_waitcnt lgkmcnt(3)\n"
      "v_lshrrev_b32 v8, 3, v8\n"
      "v_min_u32 v8, 96, v8\n"
      "v_mfma_f32_16x16x32_bf16 v[208:211], v[228:231], v[120:123], v[208:211]\n"
      "ds_read2_b32 v[150:151], v140 offset0:8 offset1:9\n"
      "v_add_u32_e64 v8, s77, v8\n"
      "v_mfma_f32_16x16x32_bf16 v[214:217], v[244:247], v[184:187], v[214:217]\n"
      "ds_read2_b32 v[170:171], v140 offset0:16 offset1:17\n"
      "v_add_u32 v8, 115296, v8\n"
      "v_mfma_f32_16x16x32_bf16 v[214:217], v[248:251], v[132:135], v[214:217]\n"
      "buffer_store_dwordx4 v[156:159], v22, s[20:23], s46 offen sc1\n"
      "v_mfma_f32_16x16x32_bf16 v[214:217], v[252:255], v[120:123], v[214:217]\n"
      "ds_read2_b32 v[120:121], v140 offset0:24 offset1:18\n"
      "s_waitcnt lgkmcnt(3)\n"
      "v_alignbyte_b32 v148, v149, v148, v93\n"
      "v_mfma_f32_16x16x32_bf16 v[136:139], v[108:111], v[128:131], v[40:43]\n"
      "v_alignbyte_b32 v149, v176, v149, v93\n"
      "v_fma_f32 v39, v220, v220, s52\n"
      "v_mfma_f32_16x16x32_bf16 v[156:159], v[160:163], v[128:131], v[44:47]\n"
      "ds_read_b128 v[180:183], v2 offset:18688\n"
      "s_waitcnt lgkmcnt(3)\n"
      "v_alignbyte_b32 v150, v151, v150, v93\n"
      "v_mfma_f32_16x16x32_bf16 v[144:147], v[116:119], v[128:131], v[48:51]\n"
      "ds_read_b128 v[132:135], v2 offset:21760\n"
      "v_alignbyte_b32 v151, v177, v151, v93\n"
      "v_mfma_f32_16x16x32_bf16 v[174:177], v[104:107], v[128:131], v[24:27]\n"
      "s_waitcnt lgkmcnt(3)\n"
      "v_alignbyte_b32 v170, v171, v170, v93\n"
      "v_fmac_f32 v39, v221, v221\n"
      "v_mfma_f32_16x16x16_bf16 v[136:139], v[96:97], v[142:143], v[136:139]\n"
      "v_fmac_f32 v39, v222, v222\n"
      "v_fmac_f32_e64 v39, v223, v223\n"
      "s_waitcnt lgkmcnt(2)\n"
      "v_alignbyte_b32 v171, v121, v171, v93\n"
      "v_mfma_f32_16x16x16_bf16 v[156:159], v[102:103], v[142:143], v[156:159]\n"
      "v_alignbyte_b32 v120, v120, v120, v93\n"
      "buffer_store_dwordx4 v[196:199], v23, s[20:23], s46 offen sc1\n"
      "v_mfma_f32_16x16x16_bf16 v[144:147], v[188:189], v[142:143], v[144:147]\n"
      "v_fmac_f32 v39, v208, v208\n"
      "v_fmac_f32 v39, v209, v209\n"
      "v_fmac_f32 v39, v210, v210\n"
      "v_mfma_f32_16x16x16_bf16 v[174:177], v[168:169], v[142:143], v[174:177]\n"
      "v_fmac_f32 v39, v211, v211\n"
      "v_fmac_f32 v39, v214, v214\n"
      "v_fmac_f32 v39, v215, v215\n"
      "v_mfma_f32_16x16x16_bf16 v[136:139], v[218:219], v[148:149], v[136:139]\n"
      "v_fmac_f32_e64 v39, v216, v216\n"
      "v_fmac_f32_e64 v39, v217, v217\n"
      "v_readlane_b32 s45, v37, s43\n"
      "v_mfma_f32_16x16x16_bf16 v[156:159], v[218:219], v[150:151], v[156:159]\n"
      "v_readlane_b32 s47, v38, s43\n"
      "v_add_u32_e64 v12, 32, v12\n"
      "s_waitcnt vmcnt(5)\n"
      "v_or3_b32 v200, v200, v201, v202\n"
      "v_mfma_f32_16x16x16_bf16 v[144:147], v[218:219], v[170:171], v[144:147]\n"
      "ds_read_b128 v[196:199], v2 offset:24832\n"
      "s_and_b32 s47, s47, -4\n"
      "v_mfma_f32_16x16x16_bf16 v[174:177], v[218:219], v[120:121], v[174:177]\n"
      "s_add_u32 s43, s43, 1\n"
      "v_mfma_f32_16x16x16_bf16 v[136:139], v[190:191], v[148:149], v[136:139]\n"
      "v_mfma_f32_16x16x16_bf16 v[156:159], v[190:191], v[150:151], v[156:159]\n"
      "buffer_store_dwordx4 v[98:101], v25, s[20:23], s46 offen sc1\n"
      "v_mfma_f32_16x16x16_bf16 v[144:147], v[190:191], v[170:171], v[144:147]\n"
      "s_waitcnt vmcnt(5)\n"
      "v_or3_b32 v200, v200, v203, v172\n"
      "v_and_b32 v200, 0x7fff7fff, v200\n"
      "v_readlane_b32 s46, v37, s42\n"
      "v_mfma_f32_16x16x16_bf16 v[174:177], v[190:191], v[120:121], v[174:177]\n"
      "ds_write_b32 v31, v200 offset:4608\n"
      "s_waitcnt lgkmcnt(1)\n"
      "v_mfma_f32_16x16x32_bf16 v[148:151], a[216:219], v[180:183], 0\n"
      "s_or_b32 s95, s45, s46\n"
      "v_max3_f32 v78, v136, v137, v138\n"
      "v_max3_f32 v121, v139, v156, v157\n"
      "v_mfma_f32_16x16x32_bf16 v[148:151], a[220:223], v[132:135], v[148:151]\n"
      "ds_read2_b32 v[184:185], v82 offset0:0 offset1:1\n"
      "v_max3_f32 v78, v78, v158, v159\n"
      "v_mfma_f32_16x16x32_bf16 v[148:151], a[224:227], v[196:199], v[148:151]\n"
      "ds_read2_b32 v[176:177], v82 offset0:2 offset1:10\n"
      "v_max3_f32 v121, v121, v144, v145\n"
      "v_mfma_f32_16x16x32_bf16 v[170:173], a[240:243], v[180:183], 0\n"
      "ds_read_b128 v[140:143], v9 offset:5632\n"
      "v_max3_f32 v78, v78, v146, v147\n"
      "v_mfma_f32_16x16x32_bf16 v[170:173], a[244:247], v[132:135], v[170:173]\n"
      "ds_read2_b32 v[186:187], v82 offset0:8 offset1:9\n"
      "v_max3_f32 v78, v78, v121, v174\n"
      "v_mfma_f32_16x16x32_bf16 v[170:173], a[248:251], v[196:199], v[170:173]\n"
      "ds_read2_b32 v[130:131], v82 offset0:16 offset1:17\n"
      "v_mov_b32_e64 v127, v78\n"
      "v_mfma_f32_16x16x32_bf16 v[120:123], v[232:235], v[180:183], 0\n"
      "ds_read2_b32 v[128:129], v82 offset0:24 offset1:18\n"
      "v_permlane16_swap_b32_e64 v78, v127\n"
      "v_mfma_f32_16x16x32_bf16 v[120:123], v[236:239], v[132:135], v[120:123]\n"
      "s_or_b32 s95, s95, s48\n"
      "s_waitcnt lgkmcnt(5)\n"
      "v_alignbyte_b32 v184, v185, v184, v93\n"
      "v_mfma_f32_16x16x32_bf16 v[120:123], v[240:243], v[196:199], v[120:123]\n"
      "s_cmp_ge_u32 s95, 0x40000000\n"
      "s_cselect_b32 s94, -1, -2\n"
      "s_waitcnt lgkmcnt(4)\n"
      "v_alignbyte_b32 v185, v176, v185, v93\n"
      "v_mfma_f32_16x16x32_bf16 v[98:101], v[108:111], v[204:207], v[52:55]\n"
      "s_cmp_gt_u32 s42, s44\n"
      "s_cselect_b32 s95, s94, s95\n"
      "s_waitcnt lgkmcnt(2)\n"
      "v_alignbyte_b32 v186, v187, v186, v93\n"
      "v_mfma_f32_16x16x32_bf16 v[180:183], v[160:163], v[204:207], v[56:59]\n"
      "s_add_u32 s42, s42, 1\n"
      "v_alignbyte_b32 v187, v177, v187, v93\n"
      "s_waitcnt lgkmcnt(1)\n"
      "v_alignbyte_b32 v130, v131, v130, v93\n"
      "v_max_f32 v78, v78, v127\n"
      "s_waitcnt lgkmcnt(0)\n"
      "v_alignbyte_b32 v131, v129, v131, v93\n"
      "v_alignbyte_b32 v128, v128, v128, v93\n"
      "v_mov_b32 v127, v78\n"
      "v_or3_b32 v140, v140, v141, v142\n"
      "v_mfma_f32_16x16x32_bf16 v[196:199], v[116:119], v[204:207], v[60:63]\n"
      "v_permlane32_swap_b32 v78, v127\n"
      "v_mfma_f32_16x16x32_bf16 v[132:135], v[104:107], v[204:207], v[26:29]\n"
      "v_max_f32 v78, v78, v127\n"
      "v_sub_f32 v136, v136, v78\n"
      "v_mfma_f32_16x16x16_bf16 v[98:101], v[96:97], v[178:179], v[98:101]\n"
      "v_sub_f32 v137, v137, v78\n"
      "v_sub_f32 v138, v138, v78\n"
      "v_sub_f32_e64 v139, v139, v78\n"
      "v_mfma_f32_16x16x16_bf16 v[180:183], v[102:103], v[178:179], v[180:183]\n"
      "v_exp_f32 v136, v136\n"
      "v_exp_f32 v137, v137\n"
      "v_mfma_f32_16x16x16_bf16 v[196:199], v[188:189], v[178:179], v[196:199]\n"
      "v_sub_f32 v156, v156, v78\n"
      "v_sub_f32 v157, v157, v78\n"
      "v_sub_f32 v158, v158, v78\n"
      "v_mfma_f32_16x16x16_bf16 v[132:135], v[168:169], v[178:179], v[132:135]\n"
      "v_sub_f32 v159, v159, v78\n"
      "v_exp_f32 v138, v138\n"
      "v_mfma_f32_16x16x16_bf16 v[98:101], v[218:219], v[184:185], v[98:101]\n"
      "v_exp_f32 v139, v139\n"
      "v_exp_f32 v156, v156\n"
      "v_mfma_f32_16x16x16_bf16 v[180:183], v[218:219], v[186:187], v[180:183]\n"
      "v_exp_f32 v157, v157\n"
      "v_exp_f32 v158, v158\n"
      "v_mfma_f32_16x16x16_bf16 v[196:199], v[218:219], v[130:131], v[196:199]\n"
      "v_exp_f32 v159, v159\n"
      "v_cvt_pk_bf16_f32 v136, v136, v137\n"
      "v_mfma_f32_16x16x16_bf16 v[132:135], v[218:219], v[128:129], v[132:135]\n"
      "v_cvt_pk_bf16_f32 v137, v138, v139\n"
      "v_cvt_pk_bf16_f32 v138, v156, v157\n"
      "v_cvt_pk_bf16_f32 v139, v158, v159\n"
      "v_mfma_f32_16x16x16_bf16 v[98:101], v[190:191], v[184:185], v[98:101]\n"
      "v_sub_f32 v144, v144, v78\n"
      "v_sub_f32 v145, v145, v78\n"
      "v_sub_f32_e64 v146, v146, v78\n"
      "v_mfma_f32_16x16x16_bf16 v[180:183], v[190:191], v[186:187], v[180:183]\n"
      "v_sub_f32 v147, v147, v78\n"
      "v_sub_f32 v174, v174, v78\n"
      "v_exp_f32 v144, v144\n"
      "v_mfma_f32_16x16x16_bf16 v[196:199], v[190:191], v[130:131], v[196:199]\n"
      "v_exp_f32 v145, v145\n"
      "v_exp_f32_e64 v146, v146\n"
      "v_mfma_f32_16x16x16_bf16 v[132:135], v[190:191], v[128:129], v[132:135]\n"
      "ds_read_b128 v[156:159], v2 offset:27904\n"
      "ds_read_b128 v[176:179], v2 offset:30976\n"
      "ds_read_b128 v[184:187], v2 offset:34048\n"
      "v_exp_f32 v147, v147\n"
      "v_exp_f32 v174, v174\n"
      "v_cvt_pk_bf16_f32 v88, v144, v145\n"
      "v_cvt_pk_bf16_f32 v89, v146, v147\n"
      "v_cvt_pk_bf16_f32 v90, v174, 0\n"
      "v_max3_f32 v135, v98, v99, v100\n"
      "v_max3_f32 v78, v101, v180, v181\n"
      "v_max3_f32 v135, v135, v182, v183\n"
      "v_max3_f32 v78, v78, v196, v197\n"
      "v_max3_f32 v135, v135, v198, v199\n"
      "v_max3_f32 v135, v135, v78, v132\n"
      "v_mov_b32 v146, v135\n"
      "v_or_b32 v140, v140, v143\n"
      "v_cmp_ne_u32 s[56:57], 0, v140\n"
      "v_permlane16_swap_b32 v135, v146\n"
      "s_waitcnt lgkmcnt(2)\n"
      "v_mfma_f32_16x16x32_bf16 v[148:151], a[228:231], v[156:159], v[148:151]\n"
      "ds_read_u16 v78, v11\n"
      "v_max_f32 v135, v135, v146\n"
      "s_waitcnt lgkmcnt(2)\n"
      "v_mfma_f32_16x16x32_bf16 v[148:151], a[232:235], v[176:179], v[148:151]\n"
      "ds_read_u16 v82, v11 offset:32\n"
      "v_mov_b32 v146, v135\n"
      "s_waitcnt lgkmcnt(2)\n"
      "v_mfma_f32_16x16x32_bf16 v[148:151], a[236:239], v[184:187], v[148:151]\n"
      "ds_read_u16 v86, v11 offset:64\n"
      "v_permlane32_swap_b32 v135, v146\n"
      "v_mfma_f32_16x16x32_bf16 v[170:173], a[252:255], v[156:159], v[170:173]\n"
      "s_cmp_ge_u32 s95, 0x40000000\n"
      "s_cselect_b64 vcc, -1, s[56:57]\n"
      "v_max_f32 v135, v135, v146\n"
      "v_mfma_f32_16x16x32_bf16 v[170:173], v[224:227], v[176:179], v[170:173]\n"
      "v_sub_f32 v98, v98, v135\n"
      "v_sub_f32 v99, v99, v135\n"
      "v_mfma_f32_16x16x32_bf16 v[170:173], v[228:231], v[184:187], v[170:173]\n"
      "v_sub_f32 v100, v100, v135\n"
      "v_sub_f32 v101, v101, v135\n"
      "v_mfma_f32_16x16x32_bf16 v[120:123], v[244:247], v[156:159], v[120:123]\n"
      "v_exp_f32 v98, v98\n"
      "v_mfma_f32_16x16x32_bf16 v[120:123], v[248:251], v[176:179], v[120:123]\n"
      "v_exp_f32 v99, v99\n"
      "v_mfma_f32_16x16x32_bf16 v[120:123], v[252:255], v[184:187], v[120:123]\n"
      "s_waitcnt lgkmcnt(0)\n"
      "v_sub_f32 v180, v180, v135\n"
      "v_sub_f32_e64 v181, v181, v135\n"
      "v_mfma_f32_16x16x32_bf16 v[156:159], v[32:35], v[136:139], 0\n"
      "ds_read2_b32 v[204:205], v94 offset0:0 offset1:1\n"
      "v_sub_f32 v182, v182, v135\n"
      "v_mfma_f32_16x16x32_bf16 v[156:159], v[32:35], v[88:91], v[156:159]\n"
      "ds_read2_b32 v[146:147], v94 offset0:2 offset1:10\n"
      "v_sub_f32 v183, v183, v135\n"
      "v_mfma_f32_16x16x32_bf16 v[140:143], v[112:115], v[136:139], 0\n"
      "ds_read2_b32 v[206:207], v94 offset0:8 offset1:9\n"
      "v_exp_f32_e64 v100, v100\n"
      "v_mfma_f32_16x16x32_bf16 v[140:143], v[76:79], v[88:91], v[140:143]\n"
      "ds_read2_b32 v[126:127], v94 offset0:16 offset1:17\n"
      "v_exp_f32 v101, v101\n"
      "v_mfma_f32_16x16x32_bf16 v[176:179], v[192:195], v[136:139], 0\n"
      "ds_read2_b32 v[124:125], v94 offset0:24 offset1:18\n"
      "v_exp_f32 v180, v180\n"
      "v_mfma_f32_16x16x32_bf16 v[176:179], v[80:83], v[88:91], v[176:179]\n"
      "v_exp_f32 v181, v181\n"
      "v_mfma_f32_16x16x32_bf16 v[128:131], v[164:167], v[136:139], 0\n"
      "s_waitcnt lgkmcnt(4)\n"
      "v_alignbyte_b32 v204, v205, v204, v93\n"
      "s_waitcnt lgkmcnt(3)\n"
      "v_alignbyte_b32 v205, v146, v205, v93\n"
      "v_mfma_f32_16x16x32_bf16 v[128:131], v[84:87], v[88:91], v[128:131]\n"
      "s_waitcnt lgkmcnt(2)\n"
      "v_alignbyte_b32 v206, v207, v206, v93\n"
      "v_alignbyte_b32 v207, v147, v207, v93\n"
      "v_mfma_f32_16x16x32_bf16 v[144:147], v[108:111], v[152:155], v[64:67]\n"
      "v_exp_f32 v182, v182\n"
      "s_waitcnt lgkmcnt(1)\n"
      "v_alignbyte_b32 v126, v127, v126, v93\n"
      "v_exp_f32 v183, v183\n"
      "s_waitcnt lgkmcnt(0)\n"
      "v_alignbyte_b32 v127, v125, v127, v93\n"
      "v_alignbyte_b32 v124, v124, v124, v93\n"
      "v_cvt_pk_bf16_f32 v98, v98, v99\n"
      "v_cvt_pk_bf16_f32 v99, v100, v101\n"
      "v_cvt_pk_bf16_f32 v100, v180, v181\n"
      "v_mfma_f32_16x16x32_bf16 v[200:203], v[160:163], v[152:155], v[68:71]\n"
      "v_cvt_pk_bf16_f32 v101, v182, v183\n"
      "v_sub_f32 v196, v196, v135\n"
      "v_sub_f32 v197, v197, v135\n"
      "v_sub_f32 v198, v198, v135\n"
      "v_sub_f32 v199, v199, v135\n"
      "v_sub_f32 v132, v132, v135\n"
      "v_mfma_f32_16x16x32_bf16 v[182:185], v[116:119], v[152:155], v[72:75]\n"
      "v_exp_f32 v196, v196\n"
      "v_mfma_f32_16x16x32_bf16 v[134:137], v[104:107], v[152:155], v[28:31]\n"
      "v_exp_f32 v197, v197\n"
      "v_mfma_f32_16x16x16_bf16 v[144:147], v[96:97], v[212:213], v[144:147]\n"
      "v_exp_f32 v198, v198\n"
      "v_exp_f32_e64 v199, v199\n"
      "v_mfma_f32_16x16x16_bf16 v[200:203], v[102:103], v[212:213], v[200:203]\n"
      "v_exp_f32_e64 v132, v132\n"
      "v_cvt_pk_bf16_f32 v92, v196, v197\n"
      "v_mfma_f32_16x16x16_bf16 v[182:185], v[188:189], v[212:213], v[182:185]\n"
      "v_cvt_pk_bf16_f32 v93, v198, v199\n"
      "v_cvt_pk_bf16_f32 v94, v132, 0\n"
      "v_fma_f32 v186, v148, v148, s52\n"
      "v_mfma_f32_16x16x16_bf16 v[134:137], v[168:169], v[212:213], v[134:137]\n"
      "v_fmac_f32 v186, v149, v149\n"
      "v_fmac_f32 v186, v150, v150\n"
      "v_fmac_f32 v186, v151, v151\n"
      "v_mfma_f32_16x16x16_bf16 v[144:147], v[218:219], v[204:205], v[144:147]\n"
      "v_fmac_f32 v186, v170, v170\n"
      "v_fmac_f32 v186, v171, v171\n"
      "v_fmac_f32 v186, v172, v172\n"
      "v_mfma_f32_16x16x16_bf16 v[200:203], v[218:219], v[206:207], v[200:203]\n"
      "v_fmac_f32 v186, v173, v173\n"
      "v_fmac_f32 v186, v120, v120\n"
      "v_fmac_f32 v186, v121, v121\n"
      "v_mfma_f32_16x16x16_bf16 v[182:185], v[218:219], v[126:127], v[182:185]\n"
      "v_fmac_f32 v186, v122, v122\n"
      "v_fmac_f32 v186, v123, v123\n"
      "v_rcp_f32 v156, v156\n"
      "v_mfma_f32_16x16x16_bf16 v[134:137], v[218:219], v[124:125], v[134:137]\n"
      "v_permlane16_swap_b32 v39, v186\n"
      "v_add_f32 v39, v39, v186\n"
      "v_mfma_f32_16x16x16_bf16 v[144:147], v[190:191], v[204:205], v[144:147]\n"
      "v_mul_f32 v140, v156, v140\n"
      "v_mul_f32 v141, v156, v141\n"
      "v_cvt_pk_bf16_f32 v140, v140, v141\n"
      "v_mfma_f32_16x16x16_bf16 v[200:203], v[190:191], v[206:207], v[200:203]\n"
      "v_mul_f32 v142, v156, v142\n"
      "v_mul_f32 v143, v156, v143\n"
      "v_cvt_pk_bf16_f32 v141, v142, v143\n"
      "v_mfma_f32_16x16x16_bf16 v[182:185], v[190:191], v[126:127], v[182:185]\n"
      "v_max3_f32 v132, v144, v145, v146\n"
      "v_mul_f32 v176, v156, v176\n"
      "v_mul_f32 v177, v156, v177\n"
      "v_mfma_f32_16x16x16_bf16 v[134:137], v[190:191], v[124:125], v[134:137]\n"
      "ds_read_b128 v[152:155], v2 offset:18944\n"
      "ds_read_b128 v[124:127], v2 offset:22016\n"
      "ds_read_b128 v[204:207], v2 offset:25088\n"
      "v_max3_f32 v198, v147, v200, v201\n"
      "v_max3_f32 v132, v132, v202, v203\n"
      "v_max3_f32 v198, v198, v182, v183\n"
      "v_max3_f32 v132, v132, v184, v185\n"
      "v_mul_f32 v178, v156, v178\n"
      "v_max3_f32 v132, v132, v198, v134\n"
      "v_mov_b32 v136, v132\n"
      "v_mul_f32 v179, v156, v179\n"
      "v_cvt_pk_bf16_f32 v142, v176, v177\n"
      "v_permlane16_swap_b32 v132, v136\n"
      "v_max_f32 v132, v132, v136\n"
      "v_mov_b32 v136, v132\n"
      "v_cvt_pk_bf16_f32 v143, v178, v179\n"
      "v_mul_f32 v128, v156, v128\n"
      "v_permlane32_swap_b32 v132, v136\n"
      "v_max_f32 v132, v132, v136\n"
      "s_waitcnt lgkmcnt(2)\n"
      "v_mfma_f32_16x16x32_bf16 v[136:139], a[216:219], v[152:155], 0\n"
      "ds_write_b128 v3, v[140:143]\n"
      "s_waitcnt lgkmcnt(1)\n"
      "v_mfma_f32_16x16x32_bf16 v[136:139], a[220:223], v[124:127], v[136:139]\n"
      "v_sub_f32 v144, v144, v132\n"
      "v_sub_f32 v145, v145, v132\n"
      "v_mfma_f32_16x16x32_bf16 v[136:139], a[224:227], v[204:207], v[136:139]\n"
      "ds_read_b128 v[140:143], v10\n"
      "v_sub_f32 v146, v146, v132\n"
      "v_mfma_f32_16x16x32_bf16 v[196:199], a[240:243], v[152:155], 0\n"
      "v_sub_f32 v147, v147, v132\n"
      "v_exp_f32 v144, v144\n"
      "v_mfma_f32_16x16x32_bf16 v[196:199], a[244:247], v[124:127], v[196:199]\n"
      "v_exp_f32 v145, v145\n"
      "v_mfma_f32_16x16x32_bf16 v[196:199], a[248:251], v[204:207], v[196:199]\n"
      "v_sub_f32 v200, v200, v132\n"
      "v_sub_f32 v201, v201, v132\n"
      "v_mfma_f32_16x16x32_bf16 v[178:181], v[232:235], v[152:155], 0\n"
      "v_sub_f32 v202, v202, v132\n"
      "v_sub_f32_e64 v203, v203, v132\n"
      "v_mfma_f32_16x16x32_bf16 v[178:181], v[236:239], v[124:127], v[178:181]\n"
      "ds_read_b64 v[190:191], v10 offset:16\n"
      "v_exp_f32 v146, v146\n"
      "v_mfma_f32_16x16x32_bf16 v[178:181], v[240:243], v[204:207], v[178:181]\n"
      "v_exp_f32 v147, v147\n"
      "v_mfma_f32_16x16x32_bf16 v[204:207], v[32:35], v[98:101], 0\n"
      "v_exp_f32 v200, v200\n"
      "v_mfma_f32_16x16x32_bf16 v[204:207], v[32:35], v[92:95], v[204:207]\n"
      "ds_read_b128 v[174:177], v2 offset:28160\n"
      "v_exp_f32 v201, v201\n"
      "v_exp_f32 v202, v202\n"
      "v_exp_f32 v203, v203\n"
      "v_cvt_pk_bf16_f32 v144, v144, v145\n"
      "v_cvt_pk_bf16_f32 v145, v146, v147\n"
      "v_cvt_pk_bf16_f32 v146, v200, v201\n"
      "v_mfma_f32_16x16x32_bf16 v[152:155], v[112:115], v[98:101], 0\n"
      "v_cvt_pk_bf16_f32 v147, v202, v203\n"
      "v_sub_f32 v182, v182, v132\n"
      "v_mfma_f32_16x16x32_bf16 v[152:155], v[76:79], v[92:95], v[152:155]\n"
      "v_sub_f32 v183, v183, v132\n"
      "v_sub_f32 v184, v184, v132\n"
      "v_sub_f32 v185, v185, v132\n"
      "v_sub_f32 v134, v134, v132\n"
      "v_exp_f32 v182, v182\n"
      "v_exp_f32_e64 v183, v183\n"
      "v_exp_f32_e64 v184, v184\n"
      "v_exp_f32_e64 v185, v185\n"
      "v_exp_f32 v134, v134\n"
      "v_cvt_pk_bf16_f32 v88, v182, v183\n"
      "v_mfma_f32_16x16x32_bf16 v[200:203], v[192:195], v[98:101], 0\n"
      "s_waitcnt lgkmcnt(0)\n"
      "v_cvt_pk_bf16_f32 v89, v184, v185\n"
      "v_cvt_pk_bf16_f32 v90, v134, 0\n"
      "v_mfma_f32_16x16x32_bf16 v[200:203], v[80:83], v[92:95], v[200:203]\n"
      "ds_read_u16 v8, v8\n"
      "v_rcp_f32_e64 v204, v204\n"
      "v_mfma_f32_16x16x32_bf16 v[132:135], v[164:167], v[98:101], 0\n"
      "v_mul_f32 v152, v204, v152\n"
      "v_mul_f32 v153, v204, v153\n"
      "v_mfma_f32_16x16x32_bf16 v[132:135], v[84:87], v[92:95], v[132:135]\n"
      "ds_read_b128 v[182:185], v2 offset:31232\n"
      "v_cvt_pk_bf16_f32 v152, v152, v153\n"
      "v_mfma_f32_16x16x32_bf16 v[124:127], v[140:143], v[108:111], v[30:33]\n"
      "v_mul_f32 v154, v204, v154\n"
      "v_mul_f32 v155, v204, v155\n"
      "v_mfma_f32_16x16x32_bf16 v[98:101], v[140:143], v[160:163], v[30:33]\n"
      "v_cvt_pk_bf16_f32 v153, v154, v155\n"
      "v_mul_f32 v200, v204, v200\n"
      "v_mfma_f32_16x16x32_bf16 v[108:111], v[140:143], v[116:119], v[30:33]\n"
      "v_mul_f32 v201, v204, v201\n"
      "v_mul_f32_e64 v202, v204, v202\n"
      "v_mfma_f32_16x16x32_bf16 v[158:161], v[140:143], v[104:107], v[30:33]\n"
      "ds_read_b128 v[104:107], v2 offset:34304\n"
      "s_waitcnt lgkmcnt(2)\n"
      "v_lshlrev_b32 v8, 16, v8\n"
      "v_mfma_f32_16x16x16_bf16 v[124:127], v[190:191], v[96:97], v[124:127]\n"
      "v_mul_f32 v203, v204, v203\n"
      "v_cvt_pk_bf16_f32 v154, v200, v201\n"
      "v_cvt_pk_bf16_f32 v155, v202, v203\n"
      "v_mfma_f32_16x16x16_bf16 v[98:101], v[190:191], v[102:103], v[98:101]\n"
      "v_mul_f32 v129, v156, v129\n"
      "v_mul_f32 v132, v204, v132\n"
      "v_mul_f32 v133, v204, v133\n"
      "v_mfma_f32_16x16x16_bf16 v[108:111], v[190:191], v[188:189], v[108:111]\n"
      "v_cvt_pk_bf16_f32 v128, v128, v129\n"
      "v_mul_f32 v130, v156, v130\n"
      "v_mul_f32 v131, v156, v131\n"
      "v_mfma_f32_16x16x16_bf16 v[158:161], v[190:191], v[168:169], v[158:161]\n"
      "v_cndmask_b32 v124, v124, v98, s[58:59]\n"
      "v_cvt_pk_bf16_f32 v132, v132, v133\n"
      "v_mul_f32_e64 v134, v204, v134\n"
      "v_mfma_f32_16x16x32_bf16 v[136:139], a[228:231], v[174:177], v[136:139]\n"
      "s_waitcnt lgkmcnt(1)\n"
      "v_cndmask_b32 v124, v124, v108, s[60:61]\n"
      "v_mul_f32 v135, v204, v135\n"
      "v_mfma_f32_16x16x32_bf16 v[136:139], a[232:235], v[182:185], v[136:139]\n"
      "s_waitcnt lgkmcnt(0)\n"
      "v_cndmask_b32 v124, v124, v158, s[62:63]\n"
      "v_fmac_f32 v124, s53, v8\n"
      "v_mfma_f32_16x16x32_bf16 v[136:139], a[236:239], v[104:107], v[136:139]\n"
      "ds_write_b128 v3, v[152:155] offset:256\n"
      "v_mfma_f32_16x16x32_bf16 v[196:199], a[252:255], v[174:177], v[196:199]\n"
      "v_max_f32_dpp v190, v124, v124 row_ror:8 row_mask:0xf bank_mask:0xf\n"
      "v_cvt_pk_bf16_f32 v129, v130, v131\n"
      "v_mfma_f32_16x16x32_bf16 v[196:199], v[224:227], v[182:185], v[196:199]\n"
      "v_max_f32_dpp v190, v190, v190 row_ror:4 row_mask:0xf bank_mask:0xf\n"
      "v_cvt_pk_bf16_f32 v133, v134, v135\n"
      "v_mfma_f32_16x16x32_bf16 v[196:199], v[228:231], v[104:107], v[196:199]\n"
      "ds_write_b64 v4, v[128:129]\n"
      "v_mfma_f32_16x16x32_bf16 v[178:181], v[244:247], v[174:177], v[178:181]\n"
      "v_max_f32_dpp v190, v190, v190 row_ror:2 row_mask:0xf bank_mask:0xf\n"
      "v_fma_f32 v176, v136, v136, s52\n"
      "v_mfma_f32_16x16x32_bf16 v[178:181], v[248:251], v[182:185], v[178:181]\n"
      "v_max_f32_dpp v190, v190, v190 row_ror:1 row_mask:0xf bank_mask:0xf\n"
      "v_fmac_f32_e64 v176, v137, v137\n"
      "v_mfma_f32_16x16x32_bf16 v[178:181], v[252:255], v[104:107], v[178:181]\n"
      "ds_write_b64 v4, v[132:133] offset:256\n"
      "v_mfma_f32_16x16x32_bf16 v[102:105], v[32:35], v[144:147], 0\n"
      "v_fmac_f32 v176, v138, v138\n"
      "v_max_f32_dpp v190, v190, v190 row_bcast:15 row_mask:0xa bank_mask:0xf\n"
      "v_mfma_f32_16x16x32_bf16 v[102:105], v[32:35], v[88:91], v[102:105]\n"
      "v_fmac_f32 v176, v139, v139\n"
      "v_fmac_f32 v176, v196, v196\n"
      "v_mfma_f32_16x16x32_bf16 v[140:143], v[112:115], v[144:147], 0\n"
      "v_fmac_f32 v176, v197, v197\n"
      "v_fmac_f32_e64 v176, v198, v198\n"
      "v_mfma_f32_16x16x32_bf16 v[140:143], v[76:79], v[88:91], v[140:143]\n"
      "v_max_f32_dpp v190, v190, v190 row_bcast:31 row_mask:0xc bank_mask:0xf\n"
      "v_fmac_f32 v176, v199, v199\n"
      "v_mfma_f32_16x16x32_bf16 v[98:101], v[192:195], v[144:147], 0\n"
      "v_fmac_f32 v176, v178, v178\n"
      "v_readlane_b32 s72, v190, 63\n"
      "v_mfma_f32_16x16x32_bf16 v[98:101], v[80:83], v[88:91], v[98:101]\n"
      "v_fmac_f32 v176, v179, v179\n"
      "v_fmac_f32 v176, v180, v180\n"
      "v_mfma_f32_16x16x32_bf16 v[188:191], v[164:167], v[144:147], 0\n"
      "v_fmac_f32 v176, v181, v181\n"
      "v_mov_b32 v186, v176\n"
      "v_mfma_f32_16x16x32_bf16 v[188:191], v[84:87], v[88:91], v[188:191]\n"
      "v_subrev_f32 v124, s72, v124\n"
      "v_rcp_f32 v102, v102\n"
      "v_exp_f32 v124, v124\n"
      "v_permlane16_swap_b32 v176, v186\n"
      "v_mul_f32 v140, v102, v140\n"
      "v_mul_f32 v141, v102, v141\n"
      "v_cvt_pk_bf16_f32 v124, v124, v124\n"
      "v_cvt_pk_bf16_f32 v140, v140, v141\n"
      "v_mul_f32 v142, v102, v142\n"
      "v_mul_f32 v143, v102, v143\n"
      "ds_write_b16 v13, v124\n"
      "ds_read_b128 v[104:107], v14\n"
      "ds_read_b128 v[156:159], v14 offset:64\n"
      "v_add_f32 v176, v176, v186\n"
      "v_cvt_pk_bf16_f32 v141, v142, v143\n"
      "v_mul_f32 v98, v102, v98\n"
      "v_mul_f32 v99, v102, v99\n"
      "v_mul_f32 v100, v102, v100\n"
      "v_mul_f32 v101, v102, v101\n"
      "v_cvt_pk_bf16_f32 v142, v98, v99\n"
      "v_cvt_pk_bf16_f32 v143, v100, v101\n"
      "v_mul_f32 v188, v102, v188\n"
      "v_mul_f32 v189, v102, v189\n"
      "ds_write_b128 v3, v[140:143] offset:512\n"
      "v_cvt_pk_bf16_f32 v188, v188, v189\n"
      "v_mul_f32 v190, v102, v190\n"
      "v_mul_f32 v191, v102, v191\n"
      "v_permlane32_swap_b32 v39, v176\n"
      "v_cvt_pk_bf16_f32 v189, v190, v191\n"
      "s_waitcnt lgkmcnt(2)\n"
      "v_mfma_f32_16x16x32_bf16 v[100:103], v[104:107], v[32:35], 0\n"
      "ds_write_b64 v4, v[188:189] offset:512\n"
      "s_waitcnt lgkmcnt(2)\n"
      "v_mfma_f32_16x16x32_bf16 v[100:103], v[156:159], v[32:35], v[100:103]\n"
      "v_add_f32 v39, v39, v176\n"
      "ds_write_b32 v31, v39\n"
      "s_waitcnt lgkmcnt(0)\n"
      "s_barrier\n"
      "s_cbranch_vccz L_sel1_%=\n"
      "s_branch L_disp1_%=\n"
      "L_tail1_%=:\n"
      "s_nop 4\n"
      "v_mfma_f32_16x16x32_bf16 v[140:143], v[104:107], v[112:115], 0\n"
      "ds_read_b128 v[130:133], v9\n"
      "ds_read_b128 v[174:177], v2\n"
      "v_mfma_f32_16x16x32_bf16 v[140:143], v[156:159], v[76:79], v[140:143]\n"
      "ds_read_b128 v[96:99], v2 offset:3072\n"
      "ds_read_b128 v[144:147], v2 offset:6144\n"
      "v_mfma_f32_16x16x32_bf16 v[112:115], v[104:107], v[192:195], 0\n"
      "v_mfma_f32_16x16x32_bf16 v[112:115], v[156:159], v[80:83], v[112:115]\n"
      "v_mfma_f32_16x16x32_bf16 v[190:193], v[104:107], v[164:167], 0\n"
      "v_mfma_f32_16x16x32_bf16 v[190:193], v[156:159], v[84:87], v[190:193]\n"
      "ds_read_b128 v[156:159], v2 offset:9216\n"
      "s_waitcnt lgkmcnt(4)\n"
      "v_add_f32 v130, v130, v131\n"
      "v_add_f32 v132, v132, v133\n"
      "v_add_f32 v130, v130, v132\n"
      "v_rsq_f32_e64 v130, v130\n"
      "v_mov_b32_dpp v100, v100 quad_perm:[0,0,0,0] row_mask:0xf bank_mask:0xf\n"
      "v_mul_f32 v130, 0x415db3d7, v130\n"
      "v_mov_b32 v132, v130\n"
      "v_rcp_f32 v100, v100\n"
      "v_cndmask_b32 v140, v140, v112, s[58:59]\n"
      "v_permlane32_swap_b32 v130, v132\n"
      "v_mov_b32 v131, v130\n"
      "v_cndmask_b32 v140, v140, v190, s[60:61]\n"
      "v_mul_f32 v140, v100, v140\n"
      "v_permlane16_swap_b32 v130, v131\n"
      "v_mul_f32 v220, v130, v220\n"
      "v_mul_f32 v221, v130, v221\n"
      "v_cvt_pk_bf16_f32 v220, v220, v221\n"
      "v_mul_f32 v222, v130, v222\n"
      "v_mul_f32 v223, v130, v223\n"
      "v_mul_f32 v214, v130, v214\n"
      "v_mul_f32 v215, v130, v215\n"
      "v_cvt_pk_bf16_f32 v221, v222, v223\n"
      "v_mul_f32 v208, v130, v208\n"
      "v_mul_f32 v209, v130, v209\n"
      "v_mul_f32 v210, v130, v210\n"
      "v_mul_f32 v211, v130, v211\n"
      "v_cvt_pk_bf16_f32 v214, v214, v215\n"
      "v_mul_f32 v216, v130, v216\n"
      "v_mul_f32 v217, v130, v217\n"
      "v_cvt_pk_bf16_f32 v140, v140, v140\n"
      "v_cvt_pk_bf16_f32 v222, v208, v209\n"
      "v_cvt_pk_bf16_f32 v223, v210, v211\n"
      "v_cvt_pk_bf16_f32 v215, v216, v217\n"
      "ds_write_b16 v12, v140\n"
      "ds_write_b128 v5, v[220:223] offset:18432\n"
      "ds_write_b64 v6, v[214:215] offset:18432\n"
      "ds_read_b128 v[190:193], v2 offset:12288\n"
      "ds_read_b128 v[100:103], v2 offset:15360\n"
      "v_mul_f32 v148, v131, v148\n"
      "v_mul_f32 v149, v131, v149\n"
      "v_cvt_pk_bf16_f32 v148, v148, v149\n"
      "v_mul_f32 v150, v131, v150\n"
      "v_mul_f32 v151, v131, v151\n"
      "v_mul_f32 v120, v131, v120\n"
      "v_mul_f32 v121, v131, v121\n"
      "v_cvt_pk_bf16_f32 v149, v150, v151\n"
      "v_mul_f32 v170, v131, v170\n"
      "v_mul_f32 v171, v131, v171\n"
      "v_mul_f32 v172, v131, v172\n"
      "v_mul_f32 v173, v131, v173\n"
      "v_cvt_pk_bf16_f32 v120, v120, v121\n"
      "v_mul_f32 v122, v131, v122\n"
      "v_mul_f32 v123, v131, v123\n"
      "v_cvt_pk_bf16_f32 v150, v170, v171\n"
      "v_cvt_pk_bf16_f32 v151, v172, v173\n"
      "v_cvt_pk_bf16_f32 v121, v122, v123\n"
      "v_mul_f32 v136, v132, v136\n"
      "v_mul_f32 v137, v132, v137\n"
      "ds_write_b128 v5, v[148:151] offset:24576\n"
      "ds_write_b64 v6, v[120:121] offset:24576\n"
      "v_cvt_pk_bf16_f32 v136, v136, v137\n"
      "v_mul_f32 v138, v132, v138\n"
      "v_mul_f32 v139, v132, v139\n"
      "v_cvt_pk_bf16_f32 v137, v138, v139\n"
      "v_mul_f32 v196, v132, v196\n"
      "v_mul_f32 v197, v132, v197\n"
      "v_mul_f32 v198, v132, v198\n"
      "v_mul_f32 v199, v132, v199\n"
      "v_cvt_pk_bf16_f32 v138, v196, v197\n"
      "v_cvt_pk_bf16_f32 v139, v198, v199\n"
      "v_mul_f32 v178, v132, v178\n"
      "v_mul_f32 v179, v132, v179\n"
      "ds_write_b128 v5, v[136:139] offset:30720\n"
      "v_cvt_pk_bf16_f32 v178, v178, v179\n"
      "v_mul_f32 v180, v132, v180\n"
      "v_mul_f32 v181, v132, v181\n"
      "v_cvt_pk_bf16_f32 v179, v180, v181\n"
      "ds_write_b64 v6, v[178:179] offset:30720\n"
      "s_waitcnt lgkmcnt(2)\n"
      "v_mfma_f32_16x16x32_bf16 v[220:223], a[216:219], v[174:177], 0\n"
      "v_mfma_f32_16x16x32_bf16 v[220:223], a[220:223], v[96:99], v[220:223]\n"
      "v_mfma_f32_16x16x32_bf16 v[220:223], a[224:227], v[144:147], v[220:223]\n"
      "v_mfma_f32_16x16x32_bf16 v[208:211], a[240:243], v[174:177], 0\n"
      "v_mfma_f32_16x16x32_bf16 v[208:211], a[244:247], v[96:99], v[208:211]\n"
      "v_mfma_f32_16x16x32_bf16 v[208:211], a[248:251], v[144:147], v[208:211]\n"
      "ds_read_b128 v[136:139], v2 offset:256\n"
      "v_mfma_f32_16x16x32_bf16 v[214:217], v[232:235], v[174:177], 0\n"
      "ds_read_b128 v[196:199], v2 offset:3328\n"
      "v_mfma_f32_16x16x32_bf16 v[214:217], v[236:239], v[96:99], v[214:217]\n"
      "ds_read_b128 v[178:181], v2 offset:6400\n"
      "v_mfma_f32_16x16x32_bf16 v[214:217], v[240:243], v[144:147], v[214:217]\n"
      "v_mfma_f32_16x16x32_bf16 v[220:223], a[228:231], v[156:159], v[220:223]\n"
      "v_mfma_f32_16x16x32_bf16 v[220:223], a[232:235], v[190:193], v[220:223]\n"
      "v_mfma_f32_16x16x32_bf16 v[220:223], a[236:239], v[100:103], v[220:223]\n"
      "s_waitcnt lgkmcnt(2)\n"
      "v_mfma_f32_16x16x32_bf16 v[208:211], a[252:255], v[156:159], v[208:211]\n"
      "s_waitcnt lgkmcnt(1)\n"
      "v_mfma_f32_16x16x32_bf16 v[208:211], v[224:227], v[190:193], v[208:211]\n"
      "s_waitcnt lgkmcnt(0)\n"
      "v_mfma_f32_16x16x32_bf16 v[208:211], v[228:231], v[100:103], v[208:211]\n"
      "ds_read_b128 v[140:143], v2 offset:9472\n"
      "v_mfma_f32_16x16x32_bf16 v[214:217], v[244:247], v[156:159], v[214:217]\n"
      "ds_read_b128 v[156:159], v2 offset:12544\n"
      "v_fma_f32 v195, v220, v220, s52\n"
      "v_mfma_f32_16x16x32_bf16 v[214:217], v[248:251], v[190:193], v[214:217]\n"
      "ds_read_b128 v[190:193], v2 offset:15616\n"
      "v_fmac_f32 v195, v221, v221\n"
      "v_mfma_f32_16x16x32_bf16 v[214:217], v[252:255], v[100:103], v[214:217]\n"
      "v_fmac_f32 v195, v222, v222\n"
      "v_fmac_f32 v195, v223, v223\n"
      "v_mfma_f32_16x16x32_bf16 v[148:151], a[216:219], v[136:139], 0\n"
      "v_fmac_f32 v195, v208, v208\n"
      "v_fmac_f32 v195, v209, v209\n"
      "v_mfma_f32_16x16x32_bf16 v[148:151], a[220:223], v[196:199], v[148:151]\n"
      "v_fmac_f32 v195, v210, v210\n"
      "v_fmac_f32 v195, v211, v211\n"
      "v_mfma_f32_16x16x32_bf16 v[148:151], a[224:227], v[178:181], v[148:151]\n"
      "s_waitcnt lgkmcnt(2)\n"
      "v_fmac_f32 v195, v214, v214\n"
      "v_fmac_f32 v195, v215, v215\n"
      "v_mfma_f32_16x16x32_bf16 v[170:173], a[240:243], v[136:139], 0\n"
      "s_waitcnt lgkmcnt(1)\n"
      "v_fmac_f32 v195, v216, v216\n"
      "v_fmac_f32 v195, v217, v217\n"
      "v_mfma_f32_16x16x32_bf16 v[170:173], a[244:247], v[196:199], v[170:173]\n"
      "s_waitcnt lgkmcnt(0)\n"
      "v_mfma_f32_16x16x32_bf16 v[170:173], a[248:251], v[178:181], v[170:173]\n"
      "ds_read_b128 v[100:103], v2 offset:512\n"
      "v_mfma_f32_16x16x32_bf16 v[120:123], v[232:235], v[136:139], 0\n"
      "ds_read_b128 v[112:115], v2 offset:3584\n"
      "v_mfma_f32_16x16x32_bf16 v[120:123], v[236:239], v[196:199], v[120:123]\n"
      "ds_read_b128 v[104:107], v2 offset:6656\n"
      "v_mfma_f32_16x16x32_bf16 v[120:123], v[240:243], v[178:181], v[120:123]\n"
      "v_mfma_f32_16x16x32_bf16 v[148:151], a[228:231], v[140:143], v[148:151]\n"
      "v_mfma_f32_16x16x32_bf16 v[148:151], a[232:235], v[156:159], v[148:151]\n"
      "v_mfma_f32_16x16x32_bf16 v[148:151], a[236:239], v[190:193], v[148:151]\n"
      "s_waitcnt lgkmcnt(2)\n"
      "v_mfma_f32_16x16x32_bf16 v[170:173], a[252:255], v[140:143], v[170:173]\n"
      "s_waitcnt lgkmcnt(1)\n"
      "v_mfma_f32_16x16x32_bf16 v[170:173], v[224:227], v[156:159], v[170:173]\n"
      "ds_read_b128 v[178:181], v7\n"
      "v_mfma_f32_16x16x32_bf16 v[170:173], v[228:231], v[190:193], v[170:173]\n"
      "ds_read_b128 v[164:167], v2 offset:9728\n"
      "ds_read_b128 v[130:133], v7 offset:4096\n"
      "v_mfma_f32_16x16x32_bf16 v[120:123], v[244:247], v[140:143], v[120:123]\n"
      "ds_read_b128 v[140:143], v2 offset:12800\n"
      "v_fma_f32 v80, v148, v148, s52\n"
      "v_mfma_f32_16x16x32_bf16 v[120:123], v[248:251], v[156:159], v[120:123]\n"
      "ds_read_b128 v[156:159], v2 offset:15872\n"
      "v_fmac_f32 v80, v149, v149\n"
      "v_mfma_f32_16x16x32_bf16 v[120:123], v[252:255], v[190:193], v[120:123]\n"
      "s_waitcnt lgkmcnt(5)\n"
      "v_fmac_f32 v80, v150, v150\n"
      "v_fmac_f32 v80, v151, v151\n"
      "v_mfma_f32_16x16x32_bf16 v[136:139], a[216:219], v[100:103], 0\n"
      "ds_read_b128 v[190:193], v7 offset:8192\n"
      "v_fmac_f32_e64 v80, v170, v170\n"
      "v_mfma_f32_16x16x32_bf16 v[136:139], a[220:223], v[112:115], v[136:139]\n"
      "ds_read_b128 v[96:99], v7 offset:12288\n"
      "v_fmac_f32 v80, v171, v171\n"
      "v_mfma_f32_16x16x32_bf16 v[136:139], a[224:227], v[104:107], v[136:139]\n"
      "ds_read_b128 v[124:127], v7 offset:16384\n"
      "v_fmac_f32 v80, v172, v172\n"
      "v_mfma_f32_16x16x32_bf16 v[196:199], a[240:243], v[100:103], 0\n"
      "v_fmac_f32 v80, v173, v173\n"
      "v_fmac_f32 v80, v120, v120\n"
      "v_mfma_f32_16x16x32_bf16 v[196:199], a[244:247], v[112:115], v[196:199]\n"
      "v_fmac_f32 v80, v121, v121\n"
      "v_fmac_f32 v80, v122, v122\n"
      "v_mfma_f32_16x16x32_bf16 v[196:199], a[248:251], v[104:107], v[196:199]\n"
      "s_waitcnt lgkmcnt(3)\n"
      "v_fmac_f32_e64 v80, v123, v123\n"
      "buffer_store_dwordx4 v[178:181], v20, s[20:23], s46 offen sc1\n"
      "s_nop 1\n"
      "v_mfma_f32_16x16x32_bf16 v[178:181], v[232:235], v[100:103], 0\n"
      "buffer_store_dwordx4 v[130:133], v21, s[20:23], s46 offen sc1\n"
      "v_mfma_f32_16x16x32_bf16 v[178:181], v[236:239], v[112:115], v[178:181]\n"
      "v_permlane16_swap_b32_e64 v195, v80\n"
      "v_mfma_f32_16x16x32_bf16 v[178:181], v[240:243], v[104:107], v[178:181]\n"
      "s_waitcnt lgkmcnt(0)\n"
      "v_add_f32 v195, v195, v80\n"
      "buffer_store_dwordx4 v[190:193], v22, s[20:23], s46 offen sc1\n"
      "v_mfma_f32_16x16x32_bf16 v[136:139], a[228:231], v[164:167], v[136:139]\n"
      "buffer_store_dwordx4 v[96:99], v23, s[20:23], s46 offen sc1\n"
      "v_mfma_f32_16x16x32_bf16 v[136:139], a[232:235], v[140:143], v[136:139]\n"
      "buffer_store_dwordx4 v[124:127], v25, s[20:23], s46 offen sc1\n"
      "v_mfma_f32_16x16x32_bf16 v[136:139], a[236:239], v[156:159], v[136:139]\n"
      "v_mfma_f32_16x16x32_bf16 v[196:199], a[252:255], v[164:167], v[196:199]\n"
      "v_readlane_b32 s46, v37, s42\n"
      "s_add_u32 s42, s42, 1\n"
      "v_mfma_f32_16x16x32_bf16 v[196:199], v[224:227], v[140:143], v[196:199]\n"
      "s_or_b32 s95, s46, s48\n"
      "s_cmp_ge_u32 s95, 0x40000000\n"
      "s_cbranch_scc1 L_rare_27_%=\n"
      "L_back_28_%=:\n"
      "v_mfma_f32_16x16x32_bf16 v[196:199], v[228:231], v[156:159], v[196:199]\n"
      "v_fma_f32 v103, v136, v136, s52\n"
      "v_fmac_f32_e64 v103, v137, v137\n"
      "v_mfma_f32_16x16x32_bf16 v[178:181], v[244:247], v[164:167], v[178:181]\n"
      "v_fmac_f32 v103, v138, v138\n"
      "v_fmac_f32 v103, v139, v139\n"
      "v_mfma_f32_16x16x32_bf16 v[178:181], v[248:251], v[140:143], v[178:181]\n"
      "v_mfma_f32_16x16x32_bf16 v[178:181], v[252:255], v[156:159], v[178:181]\n"
      "s_nop 0\n"
      "v_fmac_f32 v103, v196, v196\n"
      "v_fmac_f32 v103, v197, v197\n"
      "v_fmac_f32 v103, v198, v198\n"
      "v_fmac_f32 v103, v199, v199\n"
      "s_nop 0\n"
      "s_nop 0\n"
      "s_nop 0\n"
      "v_fmac_f32 v103, v178, v178\n"
      "v_fmac_f32 v103, v179, v179\n"
      "v_fmac_f32 v103, v180, v180\n"
      "v_fmac_f32 v103, v181, v181\n"
      "v_mov_b32 v80, v103\n"
      "s_nop 0\n"
      "s_nop 0\n"
      "v_permlane16_swap_b32 v103, v80\n"
      "v_add_f32 v103, v103, v80\n"
      "s_nop 0\n"
      "s_nop 0\n"
      "v_permlane32_swap_b32 v195, v103\n"
      "v_add_f32 v195, v195, v103\n"
      "ds_write_b32 v31, v195 offset:1024\n"
      "s_waitcnt lgkmcnt(0)\n"
      "s_barrier\n"
      "ds_read_b128 v[156:159], v9 offset:1024\n"
      "v_subrev_u32 v80, 115456, v9\n"
      "v_and_b32 v141, 0x300, v80\n"
      "v_add_u32 v80, v80, v141\n"
      "v_add_u32 v80, 149504, v80\n"
      "ds_read_b128 v[140:143], v80\n"
      "ds_read_b128 v[164:167], v80 offset:2048\n"
      "v_subrev_u32 v195, 115456, v9\n"
      "v_and_b32 v107, 0x3fffffff, v37\n"
      "v_ashrrev_i32 v103, 31, v37\n"
      "v_add_u32 v107, 1179648, v107\n"
      "v_and_b32 v103, 100270464, v103\n"
      "v_sub_u32 v107, v107, v103\n"
      "v_bfe_i32 v103, v37, 30, 1\n"
      "v_and_b32 v103, 196224, v103\n"
      "v_sub_u32_e64 v107, v107, v103\n"
      "v_subrev_u32 v106, 73728, v5\n"
      "v_subrev_u32 v101, 73728, v6\n"
      "s_mul_i32 s92, s40, 3072\n"
      "v_add_u32 v100, s92, v195\n"
      "s_waitcnt lgkmcnt(2)\n"
      "v_add_f32 v156, v156, v157\n"
      "v_add_f32 v158, v158, v159\n"
      "v_add_f32 v156, v156, v158\n"
      "v_rsq_f32 v156, v156\n"
      "v_lshrrev_b32 v195, 4, v100\n"
      "s_waitcnt lgkmcnt(1)\n"
      "v_mfma_f32_16x16x32_bf16 v[102:105], a[216:219], v[140:143], 0\n"
      "v_mul_f32 v156, 0x415db3d7, v156\n"
      "v_mov_b32 v158, v156\n"
      "s_waitcnt lgkmcnt(0)\n"
      "v_mfma_f32_16x16x32_bf16 v[102:105], a[220:223], v[164:167], v[102:105]\n"
      "v_permlane32_swap_b32 v156, v158\n"
      "v_mov_b32 v157, v156\n"
      "v_mul_f32 v136, v158, v136\n"
      "v_mul_f32 v137, v158, v137\n"
      "v_permlane16_swap_b32 v156, v157\n"
      "v_mul_f32 v220, v156, v220\n"
      "v_mul_f32 v221, v156, v221\n"
      "v_cvt_pk_bf16_f32 v220, v220, v221\n"
      "v_mul_f32 v222, v156, v222\n"
      "v_mul_f32 v223, v156, v223\n"
      "v_mul_f32 v214, v156, v214\n"
      "v_mul_f32 v215, v156, v215\n"
      "v_cvt_pk_bf16_f32 v221, v222, v223\n"
      "v_mul_f32 v208, v156, v208\n"
      "v_mul_f32 v209, v156, v209\n"
      "v_mul_f32 v210, v156, v210\n"
      "v_mul_f32_e64 v211, v156, v211\n"
      "v_cvt_pk_bf16_f32 v214, v214, v215\n"
      "v_mul_f32 v216, v156, v216\n"
      "v_mul_f32 v217, v156, v217\n"
      "v_cvt_pk_bf16_f32 v222, v208, v209\n"
      "v_cvt_pk_bf16_f32 v223, v210, v211\n"
      "v_mfma_f32_16x16x32_bf16 v[208:211], a[240:243], v[140:143], 0\n"
      "ds_write_b128 v5, v[220:223]\n"
      "v_mfma_f32_16x16x32_bf16 v[208:211], a[244:247], v[164:167], v[208:211]\n"
      "ds_read_b128 v[220:223], v80 offset:4096\n"
      "v_cvt_pk_bf16_f32 v215, v216, v217\n"
      "v_mfma_f32_16x16x32_bf16 v[112:115], v[232:235], v[140:143], 0\n"
      "ds_write_b64 v6, v[214:215]\n"
      "v_mfma_f32_16x16x32_bf16 v[112:115], v[236:239], v[164:167], v[112:115]\n"
      "ds_read_b128 v[164:167], v80 offset:6144\n"
      "ds_read_b128 v[214:217], v80 offset:8192\n"
      "v_mul_f32 v148, v157, v148\n"
      "v_mul_f32 v149, v157, v149\n"
      "v_cvt_pk_bf16_f32 v148, v148, v149\n"
      "v_mul_f32 v150, v157, v150\n"
      "v_mul_f32 v151, v157, v151\n"
      "v_mul_f32 v120, v157, v120\n"
      "v_mul_f32 v121, v157, v121\n"
      "v_cvt_pk_bf16_f32 v149, v150, v151\n"
      "v_mul_f32 v170, v157, v170\n"
      "v_mul_f32 v171, v157, v171\n"
      "v_mul_f32 v172, v157, v172\n"
      "v_mul_f32 v173, v157, v173\n"
      "v_cvt_pk_bf16_f32 v120, v120, v121\n"
      "v_mul_f32 v122, v157, v122\n"
      "s_waitcnt lgkmcnt(3)\n"
      "v_mfma_f32_16x16x32_bf16 v[102:105], a[224:227], v[220:223], v[102:105]\n"
      "v_mul_f32 v123, v157, v123\n"
      "v_cvt_pk_bf16_f32 v150, v170, v171\n"
      "v_cvt_pk_bf16_f32 v151, v172, v173\n"
      "v_cvt_pk_bf16_f32 v121, v122, v123\n"
      "ds_write_b128 v5, v[148:151] offset:6144\n"
      "s_waitcnt lgkmcnt(1)\n"
      "v_mfma_f32_16x16x32_bf16 v[102:105], a[228:231], v[164:167], v[102:105]\n"
      "ds_write_b64 v6, v[120:121] offset:6144\n"
      "v_mfma_f32_16x16x32_bf16 v[208:211], a[248:251], v[220:223], v[208:211]\n"
      "ds_read_b128 v[120:123], v80 offset:10240\n"
      "v_cvt_pk_bf16_f32 v136, v136, v137\n"
      "v_mfma_f32_16x16x32_bf16 v[208:211], a[252:255], v[164:167], v[208:211]\n"
      "ds_read_b128 v[148:151], v80 offset:256\n"
      "v_mul_f32 v138, v158, v138\n"
      "v_mfma_f32_16x16x32_bf16 v[112:115], v[240:243], v[220:223], v[112:115]\n"
      "ds_read_b128 v[220:223], v80 offset:2304\n"
      "v_mul_f32 v139, v158, v139\n"
      "v_mfma_f32_16x16x32_bf16 v[112:115], v[244:247], v[164:167], v[112:115]\n"
      "ds_read_b128 v[164:167], v80 offset:4352\n"
      "v_mul_f32 v178, v158, v178\n"
      "v_mfma_f32_16x16x32_bf16 v[102:105], a[232:235], v[214:217], v[102:105]\n"
      "ds_read_b128 v[170:173], v80 offset:6400\n"
      "v_mul_f32 v179, v158, v179\n"
      "v_cvt_pk_bf16_f32 v137, v138, v139\n"
      "v_mul_f32 v196, v158, v196\n"
      "v_mul_f32 v197, v158, v197\n"
      "v_mul_f32 v198, v158, v198\n"
      "v_mul_f32 v199, v158, v199\n"
      "v_cvt_pk_bf16_f32 v178, v178, v179\n"
      "v_mul_f32 v180, v158, v180\n"
      "v_mul_f32 v181, v158, v181\n"
      "s_waitcnt lgkmcnt(4)\n"
      "v_mfma_f32_16x16x32_bf16 v[102:105], a[236:239], v[120:123], v[102:105]\n"
      "v_cvt_pk_bf16_f32 v138, v196, v197\n"
      "v_cvt_pk_bf16_f32 v139, v198, v199\n"
      "v_mfma_f32_16x16x32_bf16 v[208:211], v[224:227], v[214:217], v[208:211]\n"
      "ds_write_b128 v5, v[136:139] offset:12288\n"
      "v_mfma_f32_16x16x32_bf16 v[208:211], v[228:231], v[120:123], v[208:211]\n"
      "s_waitcnt lgkmcnt(2)\n"
      "v_cvt_pk_bf16_f32 v179, v180, v181\n"
      "ds_write_b64 v6, v[178:179] offset:12288\n"
      "v_mfma_f32_16x16x32_bf16 v[112:115], v[248:251], v[214:217], v[112:115]\n"
      "ds_read_b128 v[136:139], v7 offset:18432\n"
      "v_fma_f32 v126, v102, v102, s52\n"
      "v_mfma_f32_16x16x32_bf16 v[112:115], v[252:255], v[120:123], v[112:115]\n"
      "ds_read_b128 v[214:217], v7 offset:22528\n"
      "v_fmac_f32_e64 v126, v103, v103\n"
      "v_mfma_f32_16x16x32_bf16 v[120:123], a[216:219], v[148:151], 0\n"
      "ds_read_b128 v[178:181], v7 offset:26624\n"
      "v_fmac_f32_e64 v126, v104, v104\n"
      "v_mfma_f32_16x16x32_bf16 v[120:123], a[220:223], v[220:223], v[120:123]\n"
      "ds_read_b128 v[196:199], v80 offset:8448\n"
      "v_fmac_f32 v126, v105, v105\n"
      "v_mfma_f32_16x16x32_bf16 v[156:159], a[240:243], v[148:151], 0\n"
      "s_waitcnt lgkmcnt(5)\n"
      "v_fmac_f32 v126, v208, v208\n"
      "v_fmac_f32 v126, v209, v209\n"
      "v_mfma_f32_16x16x32_bf16 v[156:159], a[244:247], v[220:223], v[156:159]\n"
      "ds_read_b128 v[140:143], v7 offset:30720\n"
      "v_fmac_f32 v126, v210, v210\n"
      "v_mfma_f32_16x16x32_bf16 v[190:193], v[232:235], v[148:151], 0\n"
      "v_fmac_f32 v126, v211, v211\n"
      "v_fmac_f32 v126, v112, v112\n"
      "v_mfma_f32_16x16x32_bf16 v[190:193], v[236:239], v[220:223], v[190:193]\n"
      "s_waitcnt lgkmcnt(4)\n"
      "v_fmac_f32 v126, v113, v113\n"
      "v_fmac_f32 v126, v114, v114\n"
      "v_mfma_f32_16x16x32_bf16 v[120:123], a[224:227], v[164:167], v[120:123]\n"
      "s_waitcnt lgkmcnt(3)\n"
      "v_fmac_f32 v126, v115, v115\n"
      "ds_read_b128 v[220:223], v80 offset:10496\n"
      "v_mfma_f32_16x16x32_bf16 v[120:123], a[228:231], v[170:173], v[120:123]\n"
      "buffer_store_dwordx4 v[136:139], v20, s[20:23], s46 offen sc1\n"
      "v_mfma_f32_16x16x32_bf16 v[156:159], a[248:251], v[164:167], v[156:159]\n"
      "s_waitcnt lgkmcnt(1)\n"
      "ds_read_b128 v[136:139], v7 offset:34816\n"
      "v_mfma_f32_16x16x32_bf16 v[156:159], a[252:255], v[170:173], v[156:159]\n"
      "buffer_store_dwordx4 v[214:217], v21, s[20:23], s46 offen sc1\n"
      "v_mfma_f32_16x16x32_bf16 v[190:193], v[240:243], v[164:167], v[190:193]\n"
      "buffer_store_dwordx4 v[178:181], v22, s[20:23], s46 offen sc1\n"
      "v_mfma_f32_16x16x32_bf16 v[190:193], v[244:247], v[170:173], v[190:193]\n"
      "s_waitcnt lgkmcnt(1)\n"
      "v_add_u32 v167, 0, v195\n"
      "v_add_u32 v166, 64, v195\n"
      "v_mfma_f32_16x16x32_bf16 v[120:123], a[232:235], v[196:199], v[120:123]\n"
      "buffer_store_dwordx4 v[140:143], v23, s[20:23], s46 offen sc1\n"
      "v_mfma_f32_16x16x32_bf16 v[120:123], a[236:239], v[220:223], v[120:123]\n"
      "s_waitcnt lgkmcnt(0)\n"
      "v_add_u32 v165, 128, v195\n"
      "v_mul_u32_u24 v178, 2731, v167\n"
      "v_mfma_f32_16x16x32_bf16 v[156:159], v[224:227], v[196:199], v[156:159]\n"
      "buffer_store_dwordx4 v[136:139], v25, s[20:23], s46 offen sc1\n"
      "v_mfma_f32_16x16x32_bf16 v[156:159], v[228:231], v[220:223], v[156:159]\n"
      "s_nop 0\n"
      "v_mul_u32_u24 v139, 2731, v166\n"
      "v_mul_u32_u24 v137, 2731, v165\n"
      "v_mfma_f32_16x16x32_bf16 v[190:193], v[248:251], v[196:199], v[190:193]\n"
      "v_fma_f32 v198, v120, v120, s52\n"
      "v_fmac_f32 v198, v121, v121\n"
      "v_mfma_f32_16x16x32_bf16 v[190:193], v[252:255], v[220:223], v[190:193]\n"
      "v_fmac_f32 v198, v122, v122\n"
      "v_fmac_f32 v198, v123, v123\n"
      "v_fmac_f32 v198, v156, v156\n"
      "v_fmac_f32 v198, v157, v157\n"
      "v_fmac_f32 v198, v158, v158\n"
      "v_fmac_f32 v198, v159, v159\n"
      "v_lshrrev_b32 v178, 16, v178\n"
      "v_lshrrev_b32 v139, 16, v139\n"
      "v_fmac_f32 v198, v190, v190\n"
      "v_fmac_f32 v198, v191, v191\n"
      "v_fmac_f32 v198, v192, v192\n"
      "v_fmac_f32 v198, v193, v193\n"
      "v_lshrrev_b32 v137, 16, v137\n"
      "v_mul_u32_u24 v221, 24, v178\n"
      "v_permlane16_swap_b32 v126, v198\n"
      "v_add_f32 v126, v126, v198\n"
      "v_mov_b32 v198, v126\n"
      "v_mul_u32_u24 v222, 24, v139\n"
      "v_mul_u32_u24 v223, 24, v137\n"
      "v_permlane32_swap_b32 v126, v198\n"
      "v_add_f32 v126, v126, v198\n"
      "ds_write_b32 v31, v126 offset:4608\n"
      "v_sub_u32 v221, v167, v221\n"
      "v_sub_u32 v222, v166, v222\n"
      "v_sub_u32 v223, v165, v223\n"
      "v_and_b32 v167, 7, v178\n"
      "v_and_b32 v166, 7, v139\n"
      "v_and_b32 v165, 7, v137\n"
      "v_lshl_add_u32 v178, v178, 2, 12\n"
      "v_lshl_add_u32 v139, v139, 2, 12\n"
      "v_lshl_add_u32 v137, v137, 2, 12\n"
      "ds_bpermute_b32 v126, v178, v107\n"
      "ds_bpermute_b32 v195, v139, v107\n"
      "ds_bpermute_b32 v217, v137, v107\n"
      "v_readlane_b32 s46, v37, s42\n"
      "v_xor_b32 v221, v221, v167\n"
      "s_or_b32 s95, s46, s48\n"
      "s_cmp_ge_u32 s95, 0x40000000\n"
      "s_cbranch_scc1 L_rare_31_%=\n"
      "L_back_32_%=:\n"
      "s_add_u32 s42, s42, 1\n"
      "v_xor_b32 v222, v222, v166\n"
      "v_xor_b32 v223, v223, v165\n"
      "s_waitcnt lgkmcnt(2)\n"
      "v_lshl_add_u32 v126, v221, 4, v126\n"
      "s_waitcnt lgkmcnt(1)\n"
      "v_lshl_add_u32 v195, v222, 4, v195\n"
      "s_waitcnt lgkmcnt(0)\n"
      "v_lshl_add_u32 v217, v223, 4, v217\n"
      "s_waitcnt lgkmcnt(0)\n"
      "s_barrier\n"
      "ds_read_b128 v[220:223], v9 offset:4608\n"
      "ds_read_b128 v[136:139], v7\n"
      "ds_read_b128 v[196:199], v7 offset:4096\n"
      "ds_read_b128 v[164:167], v7 offset:8192\n"
      "ds_read_b128 v[178:181], v7 offset:12288\n"
      "ds_read_b128 v[170:173], v7 offset:16384\n"
      "s_waitcnt lgkmcnt(5)\n"
      "v_add_f32 v220, v220, v221\n"
      "v_add_f32 v222, v222, v223\n"
      "v_add_f32 v220, v220, v222\n"
      "v_rsq_f32 v220, v220\n"
      "s_waitcnt lgkmcnt(4)\n"
      "buffer_store_dwordx4 v[136:139], v20, s[20:23], s46 offen sc1\n"
      "v_mul_f32 v220, 0x415db3d7, v220\n"
      "v_mov_b32 v222, v220\n"
      "s_waitcnt lgkmcnt(1)\n"
      "buffer_store_dwordx4 v[196:199], v21, s[20:23], s46 offen sc1\n"
      "v_permlane32_swap_b32 v220, v222\n"
      "v_mov_b32 v221, v220\n"
      "buffer_store_dwordx4 v[164:167], v22, s[20:23], s46 offen sc1\n"
      "buffer_store_dwordx4 v[178:181], v23, s[20:23], s46 offen sc1\n"
      "v_permlane16_swap_b32 v220, v221\n"
      "v_mul_f32 v102, v220, v102\n"
      "v_mul_f32 v103, v220, v103\n"
      "v_mul_f32 v120, v221, v120\n"
      "v_mul_f32 v121, v221, v121\n"
      "v_cvt_pk_bf16_f32 v102, v102, v103\n"
      "v_mul_f32 v104, v220, v104\n"
      "v_mul_f32 v105, v220, v105\n"
      "v_mul_f32 v112, v220, v112\n"
      "v_mul_f32_e64 v113, v220, v113\n"
      "v_cvt_pk_bf16_f32 v120, v120, v121\n"
      "v_mul_f32 v122, v221, v122\n"
      "v_mul_f32 v123, v221, v123\n"
      "v_mul_f32 v190, v221, v190\n"
      "v_mul_f32 v191, v221, v191\n"
      "v_cvt_pk_bf16_f32 v103, v104, v105\n"
      "v_mul_f32 v208, v220, v208\n"
      "v_mul_f32 v209, v220, v209\n"
      "v_mul_f32 v210, v220, v210\n"
      "v_mul_f32 v211, v220, v211\n"
      "v_cvt_pk_bf16_f32 v112, v112, v113\n"
      "v_mul_f32 v114, v220, v114\n"
      "v_mul_f32 v115, v220, v115\n"
      "v_cvt_pk_bf16_f32 v121, v122, v123\n"
      "v_mul_f32_e64 v156, v221, v156\n"
      "v_mul_f32_e64 v157, v221, v157\n"
      "v_mul_f32_e64 v158, v221, v158\n"
      "v_mul_f32_e64 v159, v221, v159\n"
      "v_cvt_pk_bf16_f32 v190, v190, v191\n"
      "v_mul_f32_e64 v192, v221, v192\n"
      "v_mul_f32_e64 v193, v221, v193\n"
      "v_cvt_pk_bf16_f32 v104, v208, v209\n"
      "v_cvt_pk_bf16_f32 v105, v210, v211\n"
      "v_cvt_pk_bf16_f32 v113, v114, v115\n"
      "v_cvt_pk_bf16_f32 v122, v156, v157\n"
      "v_cvt_pk_bf16_f32 v123, v158, v159\n"
      "v_cvt_pk_bf16_f32 v191, v192, v193\n"
      "ds_write_b128 v106, v[102:105]\n"
      "ds_write_b64 v101, v[112:113]\n"
      "ds_write_b128 v106, v[120:123] offset:6144\n"
      "ds_write_b64 v101, v[190:191] offset:6144\n"
      "s_waitcnt lgkmcnt(4)\n"
      "buffer_store_dwordx4 v[170:173], v25, s[20:23], s46 offen sc1\n"
      "s_waitcnt lgkmcnt(0)\n"
      "s_barrier\n"
      "ds_read_b128 v[170:173], v100\n"
      "ds_read_b128 v[120:123], v100 offset:1024\n"
      "ds_read_b128 v[102:105], v100 offset:2048\n"
      "s_waitcnt lgkmcnt(2)\n"
      "buffer_store_dwordx4 v[170:173], v126, s[20:23], 0 offen sc1\n"
      "s_waitcnt lgkmcnt(1)\n"
      "buffer_store_dwordx4 v[120:123], v195, s[20:23], 0 offen sc1\n"
      "s_waitcnt lgkmcnt(0)\n"
      "buffer_store_dwordx4 v[102:105], v217, s[20:23], 0 offen sc1\n"
      "s_endpgm\n"
      "L_tail0_%=:\n"
      "s_nop 4\n"
      "v_mfma_f32_16x16x32_bf16 v[140:143], v[104:107], v[112:115], 0\n"
      "ds_read_b128 v[128:131], v9 offset:1024\n"
      "ds_read_b128 v[124:127], v2 offset:18432\n"
      "v_mfma_f32_16x16x32_bf16 v[140:143], v[156:159], v[76:79], v[140:143]\n"
      "ds_read_b128 v[132:135], v2 offset:21504\n"
      "ds_read_b128 v[96:99], v2 offset:24576\n"
      "v_mfma_f32_16x16x32_bf16 v[112:115], v[104:107], v[192:195], 0\n"
      "v_mfma_f32_16x16x32_bf16 v[112:115], v[156:159], v[80:83], v[112:115]\n"
      "v_mfma_f32_16x16x32_bf16 v[190:193], v[104:107], v[164:167], 0\n"
      "v_mfma_f32_16x16x32_bf16 v[190:193], v[156:159], v[84:87], v[190:193]\n"
      "ds_read_b128 v[156:159], v2 offset:27648\n"
      "s_waitcnt lgkmcnt(4)\n"
      "v_add_f32 v128, v128, v129\n"
      "v_add_f32 v130, v130, v131\n"
      "v_add_f32 v128, v128, v130\n"
      "v_rsq_f32_e64 v128, v128\n"
      "v_mov_b32_dpp v100, v100 quad_perm:[0,0,0,0] row_mask:0xf bank_mask:0xf\n"
      "v_mul_f32 v128, 0x415db3d7, v128\n"
      "v_mov_b32 v130, v128\n"
      "v_rcp_f32 v100, v100\n"
      "v_cndmask_b32 v140, v140, v112, s[58:59]\n"
      "v_permlane32_swap_b32 v128, v130\n"
      "v_mov_b32 v129, v128\n"
      "v_cndmask_b32 v140, v140, v190, s[60:61]\n"
      "v_mul_f32 v140, v100, v140\n"
      "v_permlane16_swap_b32 v128, v129\n"
      "v_mul_f32 v220, v128, v220\n"
      "v_mul_f32 v221, v128, v221\n"
      "v_cvt_pk_bf16_f32 v220, v220, v221\n"
      "v_mul_f32 v222, v128, v222\n"
      "v_mul_f32 v223, v128, v223\n"
      "v_mul_f32 v214, v128, v214\n"
      "v_mul_f32 v215, v128, v215\n"
      "v_cvt_pk_bf16_f32 v221, v222, v223\n"
      "v_mul_f32 v208, v128, v208\n"
      "v_mul_f32 v209, v128, v209\n"
      "v_mul_f32 v210, v128, v210\n"
      "v_mul_f32 v211, v128, v211\n"
      "v_cvt_pk_bf16_f32 v214, v214, v215\n"
      "v_mul_f32 v216, v128, v216\n"
      "v_mul_f32 v217, v128, v217\n"
      "v_cvt_pk_bf16_f32 v140, v140, v140\n"
      "v_cvt_pk_bf16_f32 v222, v208, v209\n"
      "v_cvt_pk_bf16_f32 v223, v210, v211\n"
      "v_cvt_pk_bf16_f32 v215, v216, v217\n"
      "ds_write_b16 v12, v140 offset:16\n"
      "ds_write_b128 v5, v[220:223]\n"
      "ds_write_b64 v6, v[214:215]\n"
      "ds_read_b128 v[104:107], v2 offset:30720\n"
      "ds_read_b128 v[112:115], v2 offset:33792\n"
      "v_mul_f32 v148, v129, v148\n"
      "v_mul_f32 v149, v129, v149\n"
      "v_cvt_pk_bf16_f32 v148, v148, v149\n"
      "v_mul_f32 v150, v129, v150\n"
      "v_mul_f32 v151, v129, v151\n"
      "v_mul_f32 v120, v129, v120\n"
      "v_mul_f32 v121, v129, v121\n"
      "v_cvt_pk_bf16_f32 v149, v150, v151\n"
      "v_mul_f32 v170, v129, v170\n"
      "v_mul_f32 v171, v129, v171\n"
      "v_mul_f32 v172, v129, v172\n"
      "v_mul_f32 v173, v129, v173\n"
      "v_cvt_pk_bf16_f32 v120, v120, v121\n"
      "v_mul_f32 v122, v129, v122\n"
      "v_mul_f32 v123, v129, v123\n"
      "v_cvt_pk_bf16_f32 v150, v170, v171\n"
      "v_cvt_pk_bf16_f32 v151, v172, v173\n"
      "v_cvt_pk_bf16_f32 v121, v122, v123\n"
      "v_mul_f32 v136, v130, v136\n"
      "v_mul_f32 v137, v130, v137\n"
      "ds_write_b128 v5, v[148:151] offset:6144\n"
      "ds_write_b64 v6, v[120:121] offset:6144\n"
      "v_cvt_pk_bf16_f32 v136, v136, v137\n"
      "v_mul_f32 v138, v130, v138\n"
      "v_mul_f32 v139, v130, v139\n"
      "v_cvt_pk_bf16_f32 v137, v138, v139\n"
      "v_mul_f32 v196, v130, v196\n"
      "v_mul_f32 v197, v130, v197\n"
      "v_mul_f32 v198, v130, v198\n"
      "v_mul_f32 v199, v130, v199\n"
      "v_cvt_pk_bf16_f32 v138, v196, v197\n"
      "v_cvt_pk_bf16_f32 v139, v198, v199\n"
      "v_mul_f32 v178, v130, v178\n"
      "v_mul_f32 v179, v130, v179\n"
      "ds_write_b128 v5, v[136:139] offset:12288\n"
      "v_cvt_pk_bf16_f32 v178, v178, v179\n"
      "v_mul_f32_e64 v180, v130, v180\n"
      "v_mul_f32_e64 v181, v130, v181\n"
      "v_cvt_pk_bf16_f32 v179, v180, v181\n"
      "ds_write_b64 v6, v[178:179] offset:12288\n"
      "s_waitcnt lgkmcnt(2)\n"
      "v_mfma_f32_16x16x32_bf16 v[220:223], a[216:219], v[124:127], 0\n"
      "v_add_u32 v12, 32, v12\n"
      "v_mfma_f32_16x16x32_bf16 v[220:223], a[220:223], v[132:135], v[220:223]\n"
      "v_mfma_f32_16x16x32_bf16 v[220:223], a[224:227], v[96:99], v[220:223]\n"
      "v_mfma_f32_16x16x32_bf16 v[208:211], a[240:243], v[124:127], 0\n"
      "v_mfma_f32_16x16x32_bf16 v[208:211], a[244:247], v[132:135], v[208:211]\n"
      "v_mfma_f32_16x16x32_bf16 v[208:211], a[248:251], v[96:99], v[208:211]\n"
      "ds_read_b128 v[136:139], v2 offset:18688\n"
      "v_mfma_f32_16x16x32_bf16 v[214:217], v[232:235], v[124:127], 0\n"
      "ds_read_b128 v[196:199], v2 offset:21760\n"
      "v_mfma_f32_16x16x32_bf16 v[214:217], v[236:239], v[132:135], v[214:217]\n"
      "ds_read_b128 v[178:181], v2 offset:24832\n"
      "v_mfma_f32_16x16x32_bf16 v[214:217], v[240:243], v[96:99], v[214:217]\n"
      "v_mfma_f32_16x16x32_bf16 v[220:223], a[228:231], v[156:159], v[220:223]\n"
      "v_mfma_f32_16x16x32_bf16 v[220:223], a[232:235], v[104:107], v[220:223]\n"
      "v_mfma_f32_16x16x32_bf16 v[220:223], a[236:239], v[112:115], v[220:223]\n"
      "s_waitcnt lgkmcnt(2)\n"
      "v_mfma_f32_16x16x32_bf16 v[208:211], a[252:255], v[156:159], v[208:211]\n"
      "s_waitcnt lgkmcnt(1)\n"
      "v_mfma_f32_16x16x32_bf16 v[208:211], v[224:227], v[104:107], v[208:211]\n"
      "s_waitcnt lgkmcnt(0)\n"
      "v_mfma_f32_16x16x32_bf16 v[208:211], v[228:231], v[112:115], v[208:211]\n"
      "ds_read_b128 v[190:193], v2 offset:27904\n"
      "v_mfma_f32_16x16x32_bf16 v[214:217], v[244:247], v[156:159], v[214:217]\n"
      "ds_read_b128 v[124:127], v2 offset:30976\n"
      "v_fma_f32 v100, v220, v220, s52\n"
      "v_mfma_f32_16x16x32_bf16 v[214:217], v[248:251], v[104:107], v[214:217]\n"
      "ds_read_b128 v[156:159], v2 offset:34048\n"
      "v_fmac_f32 v100, v221, v221\n"
      "v_mfma_f32_16x16x32_bf16 v[214:217], v[252:255], v[112:115], v[214:217]\n"
      "v_fmac_f32 v100, v222, v222\n"
      "v_fmac_f32 v100, v223, v223\n"
      "v_mfma_f32_16x16x32_bf16 v[148:151], a[216:219], v[136:139], 0\n"
      "v_fmac_f32 v100, v208, v208\n"
      "v_fmac_f32 v100, v209, v209\n"
      "v_mfma_f32_16x16x32_bf16 v[148:151], a[220:223], v[196:199], v[148:151]\n"
      "v_fmac_f32 v100, v210, v210\n"
      "v_fmac_f32_e64 v100, v211, v211\n"
      "v_mfma_f32_16x16x32_bf16 v[148:151], a[224:227], v[178:181], v[148:151]\n"
      "s_waitcnt lgkmcnt(2)\n"
      "v_fmac_f32 v100, v214, v214\n"
      "v_fmac_f32_e64 v100, v215, v215\n"
      "v_mfma_f32_16x16x32_bf16 v[170:173], a[240:243], v[136:139], 0\n"
      "s_waitcnt lgkmcnt(1)\n"
      "v_fmac_f32_e64 v100, v216, v216\n"
      "v_fmac_f32 v100, v217, v217\n"
      "v_mfma_f32_16x16x32_bf16 v[170:173], a[244:247], v[196:199], v[170:173]\n"
      "s_waitcnt lgkmcnt(0)\n"
      "v_mfma_f32_16x16x32_bf16 v[170:173], a[248:251], v[178:181], v[170:173]\n"
      "ds_read_b128 v[104:107], v2 offset:18944\n"
      "v_mfma_f32_16x16x32_bf16 v[120:123], v[232:235], v[136:139], 0\n"
      "ds_read_b128 v[112:115], v2 offset:22016\n"
      "v_mfma_f32_16x16x32_bf16 v[120:123], v[236:239], v[196:199], v[120:123]\n"
      "ds_read_b128 v[132:135], v2 offset:25088\n"
      "v_mfma_f32_16x16x32_bf16 v[120:123], v[240:243], v[178:181], v[120:123]\n"
      "v_mfma_f32_16x16x32_bf16 v[148:151], a[228:231], v[190:193], v[148:151]\n"
      "v_mfma_f32_16x16x32_bf16 v[148:151], a[232:235], v[124:127], v[148:151]\n"
      "v_mfma_f32_16x16x32_bf16 v[148:151], a[236:239], v[156:159], v[148:151]\n"
      "s_waitcnt lgkmcnt(2)\n"
      "v_mfma_f32_16x16x32_bf16 v[170:173], a[252:255], v[190:193], v[170:173]\n"
      "s_waitcnt lgkmcnt(1)\n"
      "v_mfma_f32_16x16x32_bf16 v[170:173], v[224:227], v[124:127], v[170:173]\n"
      "ds_read_b128 v[178:181], v7 offset:18432\n"
      "v_mfma_f32_16x16x32_bf16 v[170:173], v[228:231], v[156:159], v[170:173]\n"
      "ds_read_b128 v[128:131], v2 offset:28160\n"
      "ds_read_b128 v[164:167], v7 offset:22528\n"
      "v_mfma_f32_16x16x32_bf16 v[120:123], v[244:247], v[190:193], v[120:123]\n"
      "ds_read_b128 v[190:193], v2 offset:31232\n"
      "v_fma_f32 v101, v148, v148, s52\n"
      "v_mfma_f32_16x16x32_bf16 v[120:123], v[248:251], v[124:127], v[120:123]\n"
      "ds_read_b128 v[140:143], v2 offset:34304\n"
      "v_fmac_f32 v101, v149, v149\n"
      "v_mfma_f32_16x16x32_bf16 v[120:123], v[252:255], v[156:159], v[120:123]\n"
      "s_waitcnt lgkmcnt(5)\n"
      "v_fmac_f32 v101, v150, v150\n"
      "v_fmac_f32 v101, v151, v151\n"
      "v_mfma_f32_16x16x32_bf16 v[136:139], a[216:219], v[104:107], 0\n"
      "ds_read_b128 v[96:99], v7 offset:26624\n"
      "v_fmac_f32 v101, v170, v170\n"
      "v_mfma_f32_16x16x32_bf16 v[136:139], a[220:223], v[112:115], v[136:139]\n"
      "ds_read_b128 v[156:159], v7 offset:30720\n"
      "v_fmac_f32 v101, v171, v171\n"
      "v_mfma_f32_16x16x32_bf16 v[136:139], a[224:227], v[132:135], v[136:139]\n"
      "ds_read_b128 v[124:127], v7 offset:34816\n"
      "v_fmac_f32 v101, v172, v172\n"
      "v_mfma_f32_16x16x32_bf16 v[196:199], a[240:243], v[104:107], 0\n"
      "v_fmac_f32 v101, v173, v173\n"
      "v_fmac_f32 v101, v120, v120\n"
      "v_mfma_f32_16x16x32_bf16 v[196:199], a[244:247], v[112:115], v[196:199]\n"
      "v_fmac_f32 v101, v121, v121\n"
      "v_fmac_f32 v101, v122, v122\n"
      "v_mfma_f32_16x16x32_bf16 v[196:199], a[248:251], v[132:135], v[196:199]\n"
      "s_waitcnt lgkmcnt(3)\n"
      "v_fmac_f32 v101, v123, v123\n"
      "buffer_store_dwordx4 v[178:181], v20, s[20:23], s46 offen sc1\n"
      "s_nop 1\n"
      "v_mfma_f32_16x16x32_bf16 v[178:181], v[232:235], v[104:107], 0\n"
      "buffer_store_dwordx4 v[164:167], v21, s[20:23], s46 offen sc1\n"
      "v_mfma_f32_16x16x32_bf16 v[178:181], v[236:239], v[112:115], v[178:181]\n"
      "v_permlane16_swap_b32 v100, v101\n"
      "v_mfma_f32_16x16x32_bf16 v[178:181], v[240:243], v[132:135], v[178:181]\n"
      "s_waitcnt lgkmcnt(0)\n"
      "v_add_f32_e64 v100, v100, v101\n"
      "buffer_store_dwordx4 v[96:99], v22, s[20:23], s46 offen sc1\n"
      "v_mfma_f32_16x16x32_bf16 v[136:139], a[228:231], v[128:131], v[136:139]\n"
      "buffer_store_dwordx4 v[156:159], v23, s[20:23], s46 offen sc1\n"
      "v_mfma_f32_16x16x32_bf16 v[136:139], a[232:235], v[190:193], v[136:139]\n"
      "buffer_store_dwordx4 v[124:127], v25, s[20:23], s46 offen sc1\n"
      "v_mfma_f32_16x16x32_bf16 v[136:139], a[236:239], v[140:143], v[136:139]\n"
      "v_mfma_f32_16x16x32_bf16 v[196:199], a[252:255], v[128:131], v[196:199]\n"
      "v_readlane_b32 s46, v37, s42\n"
      "s_add_u32 s42, s42, 1\n"
      "v_mfma_f32_16x16x32_bf16 v[196:199], v[224:227], v[190:193], v[196:199]\n"
      "s_or_b32 s95, s46, s48\n"
      "s_cmp_ge_u32 s95, 0x40000000\n"
      "s_cbranch_scc1 L_rare_35_%=\n"
      "L_back_36_%=:\n"
      "v_mfma_f32_16x16x32_bf16 v[196:199], v[228:231], v[140:143], v[196:199]\n"
      "v_fma_f32 v157, v136, v136, s52\n"
      "v_fmac_f32 v157, v137, v137\n"
      "v_mfma_f32_16x16x32_bf16 v[178:181], v[244:247], v[128:131], v[178:181]\n"
      "v_fmac_f32 v157, v138, v138\n"
      "v_fmac_f32_e64 v157, v139, v139\n"
      "v_mfma_f32_16x16x32_bf16 v[178:181], v[248:251], v[190:193], v[178:181]\n"
      "v_mfma_f32_16x16x32_bf16 v[178:181], v[252:255], v[140:143], v[178:181]\n"
      "s_nop 0\n"
      "v_fmac_f32 v157, v196, v196\n"
      "v_fmac_f32 v157, v197, v197\n"
      "v_fmac_f32 v157, v198, v198\n"
      "v_fmac_f32 v157, v199, v199\n"
      "s_nop 0\n"
      "s_nop 0\n"
      "s_nop 0\n"
      "v_fmac_f32 v157, v178, v178\n"
      "v_fmac_f32 v157, v179, v179\n"
      "v_fmac_f32 v157, v180, v180\n"
      "v_fmac_f32 v157, v181, v181\n"
      "v_mov_b32 v101, v157\n"
      "s_nop 0\n"
      "s_nop 0\n"
      "v_permlane16_swap_b32 v157, v101\n"
      "v_add_f32 v157, v157, v101\n"
      "s_nop 0\n"
      "s_nop 0\n"
      "v_permlane32_swap_b32 v100, v157\n"
      "v_add_f32_e64 v100, v100, v157\n"
      "ds_write_b32 v31, v100\n"
      "s_waitcnt lgkmcnt(0)\n"
      "s_barrier\n"
      "ds_read_b128 v[128:131], v9\n"
      "v_subrev_u32 v165, 115456, v9\n"
      "v_and_b32 v157, 0x300, v165\n"
      "v_add_u32_e64 v165, v165, v157\n"
      "v_add_u32 v165, 149504, v165\n"
      "ds_read_b128 v[104:107], v165\n"
      "ds_read_b128 v[156:159], v165 offset:2048\n"
      "v_subrev_u32 v167, 115456, v9\n"
      "v_and_b32 v134, 0x3fffffff, v37\n"
      "v_ashrrev_i32 v140, 31, v37\n"
      "v_add_u32 v134, 1179648, v134\n"
      "v_and_b32 v140, 100270464, v140\n"
      "v_sub_u32 v134, v134, v140\n"
      "v_bfe_i32 v140, v37, 30, 1\n"
      "v_and_b32 v140, 196224, v140\n"
      "v_sub_u32_e64 v134, v134, v140\n"
      "v_subrev_u32 v132, 73728, v5\n"
      "v_subrev_u32 v133, 73728, v6\n"
      "s_mul_i32 s92, s40, 3072\n"
      "v_add_u32 v135, s92, v167\n"
      "s_waitcnt lgkmcnt(2)\n"
      "v_add_f32 v128, v128, v129\n"
      "v_add_f32 v130, v130, v131\n"
      "v_add_f32 v128, v128, v130\n"
      "v_rsq_f32 v128, v128\n"
      "v_lshrrev_b32 v167, 4, v135\n"
      "s_waitcnt lgkmcnt(1)\n"
      "v_mfma_f32_16x16x32_bf16 v[140:143], a[216:219], v[104:107], 0\n"
      "v_mul_f32 v128, 0x415db3d7, v128\n"
      "v_mov_b32 v130, v128\n"
      "s_waitcnt lgkmcnt(0)\n"
      "v_mfma_f32_16x16x32_bf16 v[140:143], a[220:223], v[156:159], v[140:143]\n"
      "v_permlane32_swap_b32 v128, v130\n"
      "v_mov_b32 v129, v128\n"
      "v_mul_f32 v136, v130, v136\n"
      "v_mul_f32 v137, v130, v137\n"
      "v_permlane16_swap_b32 v128, v129\n"
      "v_mul_f32 v220, v128, v220\n"
      "v_mul_f32 v221, v128, v221\n"
      "v_cvt_pk_bf16_f32 v220, v220, v221\n"
      "v_mul_f32 v222, v128, v222\n"
      "v_mul_f32 v223, v128, v223\n"
      "v_mul_f32 v214, v128, v214\n"
      "v_mul_f32_e64 v215, v128, v215\n"
      "v_cvt_pk_bf16_f32 v221, v222, v223\n"
      "v_mul_f32 v208, v128, v208\n"
      "v_mul_f32 v209, v128, v209\n"
      "v_mul_f32 v210, v128, v210\n"
      "v_mul_f32 v211, v128, v211\n"
      "v_cvt_pk_bf16_f32 v214, v214, v215\n"
      "v_mul_f32 v216, v128, v216\n"
      "v_mul_f32 v217, v128, v217\n"
      "v_cvt_pk_bf16_f32 v222, v208, v209\n"
      "v_cvt_pk_bf16_f32 v223, v210, v211\n"
      "v_mfma_f32_16x16x32_bf16 v[124:127], a[240:243], v[104:107], 0\n"
      "ds_write_b128 v5, v[220:223] offset:18432\n"
      "v_mfma_f32_16x16x32_bf16 v[124:127], a[244:247], v[156:159], v[124:127]\n"
      "ds_read_b128 v[190:193], v165 offset:4096\n"
      "v_cvt_pk_bf16_f32 v215, v216, v217\n"
      "v_mfma_f32_16x16x32_bf16 v[208:211], v[232:235], v[104:107], 0\n"
      "ds_write_b64 v6, v[214:215] offset:18432\n"
      "v_mfma_f32_16x16x32_bf16 v[208:211], v[236:239], v[156:159], v[208:211]\n"
      "ds_read_b128 v[96:99], v165 offset:6144\n"
      "ds_read_b128 v[112:115], v165 offset:8192\n"
      "v_mul_f32 v148, v129, v148\n"
      "v_mul_f32 v149, v129, v149\n"
      "v_cvt_pk_bf16_f32 v148, v148, v149\n"
      "v_mul_f32 v150, v129, v150\n"
      "v_mul_f32 v151, v129, v151\n"
      "v_mul_f32 v120, v129, v120\n"
      "v_mul_f32 v121, v129, v121\n"
      "v_cvt_pk_bf16_f32 v149, v150, v151\n"
      "v_mul_f32 v170, v129, v170\n"
      "v_mul_f32 v171, v129, v171\n"
      "v_mul_f32 v172, v129, v172\n"
      "v_mul_f32 v173, v129, v173\n"
      "v_cvt_pk_bf16_f32 v120, v120, v121\n"
      "v_mul_f32 v122, v129, v122\n"
      "s_waitcnt lgkmcnt(3)\n"
      "v_mfma_f32_16x16x32_bf16 v[140:143], a[224:227], v[190:193], v[140:143]\n"
      "v_mul_f32 v123, v129, v123\n"
      "v_cvt_pk_bf16_f32 v150, v170, v171\n"
      "v_cvt_pk_bf16_f32 v151, v172, v173\n"
      "v_cvt_pk_bf16_f32 v121, v122, v123\n"
      "ds_write_b128 v5, v[148:151] offset:24576\n"
      "s_waitcnt lgkmcnt(1)\n"
      "v_mfma_f32_16x16x32_bf16 v[140:143], a[228:231], v[96:99], v[140:143]\n"
      "ds_write_b64 v6, v[120:121] offset:24576\n"
      "v_mfma_f32_16x16x32_bf16 v[124:127], a[248:251], v[190:193], v[124:127]\n"
      "ds_read_b128 v[214:217], v165 offset:10240\n"
      "v_cvt_pk_bf16_f32 v136, v136, v137\n"
      "v_mfma_f32_16x16x32_bf16 v[124:127], a[252:255], v[96:99], v[124:127]\n"
      "ds_read_b128 v[170:173], v165 offset:256\n"
      "v_mul_f32_e64 v138, v130, v138\n"
      "v_mfma_f32_16x16x32_bf16 v[208:211], v[240:243], v[190:193], v[208:211]\n"
      "ds_read_b128 v[220:223], v165 offset:2304\n"
      "v_mul_f32_e64 v139, v130, v139\n"
      "v_mfma_f32_16x16x32_bf16 v[208:211], v[244:247], v[96:99], v[208:211]\n"
      "ds_read_b128 v[104:107], v165 offset:4352\n"
      "v_mul_f32 v178, v130, v178\n"
      "v_mfma_f32_16x16x32_bf16 v[140:143], a[232:235], v[112:115], v[140:143]\n"
      "ds_read_b128 v[98:101], v165 offset:6400\n"
      "v_mul_f32 v179, v130, v179\n"
      "v_cvt_pk_bf16_f32 v137, v138, v139\n"
      "v_mul_f32 v196, v130, v196\n"
      "v_mul_f32 v197, v130, v197\n"
      "v_mul_f32 v198, v130, v198\n"
      "v_mul_f32 v199, v130, v199\n"
      "v_cvt_pk_bf16_f32 v178, v178, v179\n"
      "v_mul_f32 v180, v130, v180\n"
      "v_mul_f32 v181, v130, v181\n"
      "s_waitcnt lgkmcnt(4)\n"
      "v_mfma_f32_16x16x32_bf16 v[140:143], a[236:239], v[214:217], v[140:143]\n"
      "v_cvt_pk_bf16_f32 v138, v196, v197\n"
      "v_cvt_pk_bf16_f32 v139, v198, v199\n"
      "v_mfma_f32_16x16x32_bf16 v[124:127], v[224:227], v[112:115], v[124:127]\n"
      "ds_write_b128 v5, v[136:139] offset:30720\n"
      "v_mfma_f32_16x16x32_bf16 v[124:127], v[228:231], v[214:217], v[124:127]\n"
      "s_waitcnt lgkmcnt(2)\n"
      "v_cvt_pk_bf16_f32 v179, v180, v181\n"
      "ds_write_b64 v6, v[178:179] offset:30720\n"
      "v_mfma_f32_16x16x32_bf16 v[208:211], v[248:251], v[112:115], v[208:211]\n"
      "ds_read_b128 v[136:139], v7\n"
      "v_fma_f32 v96, v140, v140, s52\n"
      "v_mfma_f32_16x16x32_bf16 v[208:211], v[252:255], v[214:217], v[208:211]\n"
      "ds_read_b128 v[196:199], v7 offset:4096\n"
      "v_fmac_f32_e64 v96, v141, v141\n"
      "v_mfma_f32_16x16x32_bf16 v[178:181], a[216:219], v[170:173], 0\n"
      "ds_read_b128 v[190:193], v7 offset:8192\n"
      "v_fmac_f32_e64 v96, v142, v142\n"
      "v_mfma_f32_16x16x32_bf16 v[178:181], a[220:223], v[220:223], v[178:181]\n"
      "ds_read_b128 v[148:151], v165 offset:8448\n"
      "v_fmac_f32 v96, v143, v143\n"
      "v_mfma_f32_16x16x32_bf16 v[112:115], a[240:243], v[170:173], 0\n"
      "s_waitcnt lgkmcnt(5)\n"
      "v_fmac_f32 v96, v124, v124\n"
      "v_fmac_f32 v96, v125, v125\n"
      "v_mfma_f32_16x16x32_bf16 v[112:115], a[244:247], v[220:223], v[112:115]\n"
      "ds_read_b128 v[128:131], v7 offset:12288\n"
      "v_fmac_f32 v96, v126, v126\n"
      "v_mfma_f32_16x16x32_bf16 v[214:217], v[232:235], v[170:173], 0\n"
      "v_fmac_f32 v96, v127, v127\n"
      "v_fmac_f32 v96, v208, v208\n"
      "v_mfma_f32_16x16x32_bf16 v[214:217], v[236:239], v[220:223], v[214:217]\n"
      "s_waitcnt lgkmcnt(4)\n"
      "v_fmac_f32 v96, v209, v209\n"
      "v_fmac_f32 v96, v210, v210\n"
      "v_mfma_f32_16x16x32_bf16 v[178:181], a[224:227], v[104:107], v[178:181]\n"
      "s_waitcnt lgkmcnt(3)\n"
      "v_fmac_f32 v96, v211, v211\n"
      "ds_read_b128 v[170:173], v165 offset:10496\n"
      "v_mfma_f32_16x16x32_bf16 v[178:181], a[228:231], v[98:101], v[178:181]\n"
      "buffer_store_dwordx4 v[136:139], v20, s[20:23], s46 offen sc1\n"
      "v_mfma_f32_16x16x32_bf16 v[112:115], a[248:251], v[104:107], v[112:115]\n"
      "s_waitcnt lgkmcnt(1)\n"
      "ds_read_b128 v[120:123], v7 offset:16384\n"
      "v_mfma_f32_16x16x32_bf16 v[112:115], a[252:255], v[98:101], v[112:115]\n"
      "buffer_store_dwordx4 v[196:199], v21, s[20:23], s46 offen sc1\n"
      "v_mfma_f32_16x16x32_bf16 v[214:217], v[240:243], v[104:107], v[214:217]\n"
      "buffer_store_dwordx4 v[190:193], v22, s[20:23], s46 offen sc1\n"
      "v_mfma_f32_16x16x32_bf16 v[214:217], v[244:247], v[98:101], v[214:217]\n"
      "s_waitcnt lgkmcnt(1)\n"
      "v_add_u32 v99, 0, v167\n"
      "v_add_u32 v98, 64, v167\n"
      "v_mfma_f32_16x16x32_bf16 v[178:181], a[232:235], v[148:151], v[178:181]\n"
      "buffer_store_dwordx4 v[128:131], v23, s[20:23], s46 offen sc1\n"
      "v_mfma_f32_16x16x32_bf16 v[178:181], a[236:239], v[170:173], v[178:181]\n"
      "s_waitcnt lgkmcnt(0)\n"
      "v_add_u32 v197, 128, v167\n"
      "v_mul_u32_u24 v198, 2731, v99\n"
      "v_mfma_f32_16x16x32_bf16 v[112:115], v[224:227], v[148:151], v[112:115]\n"
      "buffer_store_dwordx4 v[120:123], v25, s[20:23], s46 offen sc1\n"
      "v_mfma_f32_16x16x32_bf16 v[112:115], v[228:231], v[170:173], v[112:115]\n"
      "s_nop 0\n"
      "v_mul_u32_u24 v120, 2731, v98\n"
      "v_mul_u32_u24 v123, 2731, v197\n"
      "v_mfma_f32_16x16x32_bf16 v[214:217], v[248:251], v[148:151], v[214:217]\n"
      "v_fma_f32 v151, v178, v178, s52\n"
      "v_fmac_f32 v151, v179, v179\n"
      "v_mfma_f32_16x16x32_bf16 v[214:217], v[252:255], v[170:173], v[214:217]\n"
      "v_fmac_f32 v151, v180, v180\n"
      "v_fmac_f32 v151, v181, v181\n"
      "v_fmac_f32 v151, v112, v112\n"
      "v_fmac_f32 v151, v113, v113\n"
      "v_fmac_f32 v151, v114, v114\n"
      "v_fmac_f32 v151, v115, v115\n"
      "v_lshrrev_b32 v198, 16, v198\n"
      "v_lshrrev_b32 v120, 16, v120\n"
      "v_fmac_f32 v151, v214, v214\n"
      "v_fmac_f32 v151, v215, v215\n"
      "v_fmac_f32 v151, v216, v216\n"
      "v_fmac_f32 v151, v217, v217\n"
      "v_lshrrev_b32 v123, 16, v123\n"
      "v_mul_u32_u24 v150, 24, v198\n"
      "v_permlane16_swap_b32 v96, v151\n"
      "v_add_f32 v96, v96, v151\n"
      "v_mov_b32 v151, v96\n"
      "v_mul_u32_u24 v148, 24, v120\n"
      "v_mul_u32_u24 v149, 24, v123\n"
      "v_permlane32_swap_b32 v96, v151\n"
      "v_add_f32 v96, v96, v151\n"
      "ds_write_b32 v31, v96 offset:4608\n"
      "v_sub_u32 v150, v99, v150\n"
      "v_sub_u32 v148, v98, v148\n"
      "v_sub_u32 v149, v197, v149\n"
      "v_and_b32 v99, 7, v198\n"
      "v_and_b32 v98, 7, v120\n"
      "v_and_b32 v197, 7, v123\n"
      "v_lshl_add_u32 v198, v198, 2, 12\n"
      "v_lshl_add_u32 v120, v120, 2, 12\n"
      "v_lshl_add_u32 v123, v123, 2, 12\n"
      "ds_bpermute_b32 v131, v198, v134\n"
      "ds_bpermute_b32 v139, v120, v134\n"
      "ds_bpermute_b32 v129, v123, v134\n"
      "v_readlane_b32 s46, v37, s42\n"
      "v_xor_b32 v150, v150, v99\n"
      "s_or_b32 s95, s46, s48\n"
      "s_cmp_ge_u32 s95, 0x40000000\n"
      "s_cbranch_scc1 L_rare_39_%=\n"
      "L_back_40_%=:\n"
      "s_add_u32 s42, s42, 1\n"
      "v_xor_b32 v148, v148, v98\n"
      "v_xor_b32 v149, v149, v197\n"
      "s_waitcnt lgkmcnt(2)\n"
      "v_lshl_add_u32 v131, v150, 4, v131\n"
      "s_waitcnt lgkmcnt(1)\n"
      "v_lshl_add_u32 v139, v148, 4, v139\n"
      "s_waitcnt lgkmcnt(0)\n"
      "v_lshl_add_u32 v129, v149, 4, v129\n"
      "s_waitcnt lgkmcnt(0)\n"
      "s_barrier\n"
      "ds_read_b128 v[96:99], v9 offset:4608\n"
      "ds_read_b128 v[148:151], v7 offset:18432\n"
      "ds_read_b128 v[196:199], v7 offset:22528\n"
      "ds_read_b128 v[104:107], v7 offset:26624\n"
      "ds_read_b128 v[190:193], v7 offset:30720\n"
      "ds_read_b128 v[120:123], v7 offset:34816\n"
      "s_waitcnt lgkmcnt(5)\n"
      "v_add_f32 v96, v96, v97\n"
      "v_add_f32 v98, v98, v99\n"
      "v_add_f32 v96, v96, v98\n"
      "v_rsq_f32 v96, v96\n"
      "s_waitcnt lgkmcnt(4)\n"
      "buffer_store_dwordx4 v[148:151], v20, s[20:23], s46 offen sc1\n"
      "v_mul_f32 v96, 0x415db3d7, v96\n"
      "v_mov_b32 v98, v96\n"
      "s_waitcnt lgkmcnt(1)\n"
      "buffer_store_dwordx4 v[196:199], v21, s[20:23], s46 offen sc1\n"
      "v_permlane32_swap_b32 v96, v98\n"
      "v_mov_b32 v97, v96\n"
      "buffer_store_dwordx4 v[104:107], v22, s[20:23], s46 offen sc1\n"
      "buffer_store_dwordx4 v[190:193], v23, s[20:23], s46 offen sc1\n"
      "v_permlane16_swap_b32 v96, v97\n"
      "v_mul_f32 v140, v96, v140\n"
      "v_mul_f32 v141, v96, v141\n"
      "v_mul_f32 v178, v97, v178\n"
      "v_mul_f32 v179, v97, v179\n"
      "v_cvt_pk_bf16_f32 v140, v140, v141\n"
      "v_mul_f32 v142, v96, v142\n"
      "v_mul_f32 v143, v96, v143\n"
      "v_mul_f32 v208, v96, v208\n"
      "v_mul_f32_e64 v209, v96, v209\n"
      "v_cvt_pk_bf16_f32 v178, v178, v179\n"
      "v_mul_f32 v180, v97, v180\n"
      "v_mul_f32 v181, v97, v181\n"
      "v_mul_f32 v214, v97, v214\n"
      "v_mul_f32 v215, v97, v215\n"
      "v_cvt_pk_bf16_f32 v141, v142, v143\n"
      "v_mul_f32 v124, v96, v124\n"
      "v_mul_f32 v125, v96, v125\n"
      "v_mul_f32 v126, v96, v126\n"
      "v_mul_f32 v127, v96, v127\n"
      "v_cvt_pk_bf16_f32 v208, v208, v209\n"
      "v_mul_f32 v210, v96, v210\n"
      "v_mul_f32 v211, v96, v211\n"
      "v_cvt_pk_bf16_f32 v179, v180, v181\n"
      "v_mul_f32 v112, v97, v112\n"
      "v_mul_f32 v113, v97, v113\n"
      "v_mul_f32 v114, v97, v114\n"
      "v_mul_f32 v115, v97, v115\n"
      "v_cvt_pk_bf16_f32 v214, v214, v215\n"
      "v_mul_f32_e64 v216, v97, v216\n"
      "v_mul_f32_e64 v217, v97, v217\n"
      "v_cvt_pk_bf16_f32 v142, v124, v125\n"
      "v_cvt_pk_bf16_f32 v143, v126, v127\n"
      "v_cvt_pk_bf16_f32 v209, v210, v211\n"
      "v_cvt_pk_bf16_f32 v180, v112, v113\n"
      "v_cvt_pk_bf16_f32 v181, v114, v115\n"
      "v_cvt_pk_bf16_f32 v215, v216, v217\n"
      "ds_write_b128 v132, v[140:143]\n"
      "ds_write_b64 v133, v[208:209]\n"
      "ds_write_b128 v132, v[178:181] offset:6144\n"
      "ds_write_b64 v133, v[214:215] offset:6144\n"
      "s_waitcnt lgkmcnt(4)\n"
      "buffer_store_dwordx4 v[120:123], v25, s[20:23], s46 offen sc1\n"
      "s_waitcnt lgkmcnt(0)\n"
      "s_barrier\n"
      "ds_read_b128 v[124:127], v135\n"
      "ds_read_b128 v[120:123], v135 offset:1024\n"
      "ds_read_b128 v[98:101], v135 offset:2048\n"
      "s_waitcnt lgkmcnt(2)\n"
      "buffer_store_dwordx4 v[124:127], v131, s[20:23], 0 offen sc1\n"
      "s_waitcnt lgkmcnt(1)\n"
      "buffer_store_dwordx4 v[120:123], v139, s[20:23], 0 offen sc1\n"
      "s_waitcnt lgkmcnt(0)\n"
      "buffer_store_dwordx4 v[98:101], v129, s[20:23], 0 offen sc1\n"
      "s_endpgm\n"
      "L_rare_1_%=:\n"
      "s_nop 7\n"
      "s_waitcnt lgkmcnt(0)\n"
      "s_add_u32 s72, s42, 1\n"
      "v_readlane_b32 s77, v38, s72\n"
      "v_subrev_u32 v110, 115456, v9\n"
      "s_and_b32 s73, s77, -16\n"
      "s_mov_b32 m0, 110592\n"
      "s_nop 0\n"
      "buffer_load_dwordx4 v110, s[24:27], s73 offen  lds\n"
      "s_add_u32 s73, s73, 1024\n"
      "s_mov_b32 m0, 111616\n"
      "s_nop 0\n"
      "buffer_load_dwordx4 v110, s[24:27], s73 offen  lds\n"
      "s_add_u32 s73, s73, 1024\n"
      "s_mov_b32 m0, 112640\n"
      "s_nop 0\n"
      "buffer_load_dwordx4 v110, s[24:27], s73 offen  lds\n"
      "s_add_u32 s73, s73, 1024\n"
      "s_mov_b32 m0, 113664\n"
      "s_nop 0\n"
      "buffer_load_dwordx4 v110, s[24:27], s73 offen  lds\n"
      "s_add_u32 s73, s73, 1024\n"
      "s_mov_b32 m0, 114688\n"
      "s_mov_b32 exec_hi, 0xffff\n"
      "buffer_load_dwordx4 v110, s[24:27], s73 offen  lds\n"
      "s_mov_b32 exec_hi, -1\n"
      "s_add_u32 s73, s73, 1024\n"
      "s_and_b32 s77, s77, 15\n"
      "s_waitcnt lgkmcnt(0)\n"
      "s_nop 4\n"
      "s_branch L_back_2_%=\n"
      "L_rare_3_%=:\n"
      "s_nop 7\n"
      "s_waitcnt lgkmcnt(0)\n"
      "s_waitcnt vmcnt(0)\n"
      "v_subrev_u32 v188, 115456, v9\n"
      "v_bfe_u32 v104, v188, 4, 4\n"
      "v_mul_u32_u24 v104, 98, v104\n"
      "v_lshrrev_b32 v188, 8, v188\n"
      "v_lshl_add_u32 v104, v188, 3, v104\n"
      "v_add_u32 v104, s77, v104\n"
      "v_and_b32 v105, 2, v104\n"
      "v_and_b32_e64 v104, -4, v104\n"
      "v_add_u32 v104, 110592, v104\n"
      "ds_read2_b32 v[154:155], v104 offset0:0 offset1:1\n"
      "ds_read2_b32 v[152:153], v104 offset0:8 offset1:9\n"
      "ds_read2_b32 v[110:111], v104 offset0:16 offset1:17\n"
      "ds_read2_b32 v[108:109], v104 offset0:2 offset1:10\n"
      "ds_read2_b32 v[162:163], v104 offset0:18 offset1:24\n"
      "s_waitcnt lgkmcnt(4)\n"
      "v_alignbyte_b32 v154, v155, v154, v105\n"
      "s_waitcnt lgkmcnt(1)\n"
      "v_alignbyte_b32 v155, v108, v155, v105\n"
      "v_lshlrev_b32 v188, 16, v154\n"
      "v_fmac_f32 v116, s53, v188\n"
      "v_and_b32 v188, 0xffff0000, v154\n"
      "v_fmac_f32 v117, s53, v188\n"
      "v_lshlrev_b32 v188, 16, v155\n"
      "v_fmac_f32_e64 v118, s53, v188\n"
      "v_and_b32 v188, 0xffff0000, v155\n"
      "v_fmac_f32 v119, s53, v188\n"
      "v_alignbyte_b32 v152, v153, v152, v105\n"
      "v_alignbyte_b32 v153, v109, v153, v105\n"
      "v_lshlrev_b32 v188, 16, v152\n"
      "v_fmac_f32 v172, s53, v188\n"
      "v_and_b32 v188, 0xffff0000, v152\n"
      "v_fmac_f32 v173, s53, v188\n"
      "v_lshlrev_b32 v188, 16, v153\n"
      "v_fmac_f32 v174, s53, v188\n"
      "v_and_b32 v188, 0xffff0000, v153\n"
      "v_fmac_f32 v175, s53, v188\n"
      "v_alignbyte_b32 v110, v111, v110, v105\n"
      "s_waitcnt lgkmcnt(0)\n"
      "v_alignbyte_b32 v111, v162, v111, v105\n"
      "v_lshlrev_b32 v188, 16, v110\n"
      "v_fmac_f32 v178, s53, v188\n"
      "v_and_b32 v188, 0xffff0000, v110\n"
      "v_fmac_f32 v179, s53, v188\n"
      "v_lshlrev_b32 v188, 16, v111\n"
      "v_fmac_f32 v180, s53, v188\n"
      "v_and_b32 v188, 0xffff0000, v111\n"
      "v_fmac_f32 v181, s53, v188\n"
      "v_alignbyte_b32 v188, v163, v163, v105\n"
      "v_lshlrev_b32 v188, 16, v188\n"
      "v_fmac_f32 v158, s53, v188\n"
      "v_max3_f32 v104, v116, v117, v118\n"
      "v_max3_f32 v105, v119, v172, v173\n"
      "v_max3_f32 v104, v104, v174, v175\n"
      "v_max3_f32 v105, v105, v178, v179\n"
      "v_max3_f32 v104, v104, v180, v181\n"
      "v_max3_f32 v104, v104, v105, v158\n"
      "v_mov_b32 v188, v104\n"
      "s_nop 1\n"
      "v_permlane16_swap_b32 v104, v188\n"
      "v_max_f32 v104, v104, v188\n"
      "v_mov_b32 v188, v104\n"
      "s_nop 1\n"
      "v_permlane32_swap_b32 v104, v188\n"
      "v_max_f32 v104, v104, v188\n"
      "v_sub_f32 v116, v116, v104\n"
      "v_sub_f32 v117, v117, v104\n"
      "v_sub_f32 v118, v118, v104\n"
      "v_sub_f32 v119, v119, v104\n"
      "v_sub_f32 v172, v172, v104\n"
      "v_sub_f32 v173, v173, v104\n"
      "v_sub_f32 v174, v174, v104\n"
      "v_sub_f32 v175, v175, v104\n"
      "v_sub_f32 v178, v178, v104\n"
      "v_sub_f32 v179, v179, v104\n"
      "v_sub_f32 v180, v180, v104\n"
      "v_sub_f32 v181, v181, v104\n"
      "v_sub_f32_e64 v158, v158, v104\n"
      "s_waitcnt lgkmcnt(0)\n"
      "s_nop 4\n"
      "s_branch L_back_4_%=\n"
      "L_rare_5_%=:\n"
      "s_nop 7\n"
      "s_waitcnt lgkmcnt(0)\n"
      "s_waitcnt vmcnt(0)\n"
      "v_subrev_u32 v130, 115456, v9\n"
      "v_bfe_u32 v165, v130, 4, 4\n"
      "v_mul_u32_u24 v165, 98, v165\n"
      "v_lshrrev_b32 v130, 8, v130\n"
      "v_lshl_add_u32 v165, v130, 3, v165\n"
      "v_add_u32 v165, s77, v165\n"
      "v_and_b32 v131, 2, v165\n"
      "v_and_b32 v165, -4, v165\n"
      "v_add_u32 v165, 112160, v165\n"
      "ds_read2_b32 v[128:129], v165 offset0:0 offset1:1\n"
      "ds_read2_b32 v[104:105], v165 offset0:8 offset1:9\n"
      "ds_read2_b32 v[126:127], v165 offset0:16 offset1:17\n"
      "ds_read2_b32 v[190:191], v165 offset0:2 offset1:10\n"
      "ds_read2_b32 v[188:189], v165 offset0:18 offset1:24\n"
      "s_waitcnt lgkmcnt(4)\n"
      "v_alignbyte_b32 v128, v129, v128, v131\n"
      "s_waitcnt lgkmcnt(1)\n"
      "v_alignbyte_b32 v129, v190, v129, v131\n"
      "v_lshlrev_b32 v130, 16, v128\n"
      "v_fmac_f32 v148, s53, v130\n"
      "v_and_b32 v130, 0xffff0000, v128\n"
      "v_fmac_f32 v149, s53, v130\n"
      "v_lshlrev_b32 v130, 16, v129\n"
      "v_fmac_f32 v150, s53, v130\n"
      "v_and_b32 v130, 0xffff0000, v129\n"
      "v_fmac_f32 v151, s53, v130\n"
      "v_alignbyte_b32 v104, v105, v104, v131\n"
      "v_alignbyte_b32 v105, v191, v105, v131\n"
      "v_lshlrev_b32 v130, 16, v104\n"
      "v_fmac_f32 v120, s53, v130\n"
      "v_and_b32 v130, 0xffff0000, v104\n"
      "v_fmac_f32 v121, s53, v130\n"
      "v_lshlrev_b32 v130, 16, v105\n"
      "v_fmac_f32_e64 v122, s53, v130\n"
      "v_and_b32 v130, 0xffff0000, v105\n"
      "v_fmac_f32 v123, s53, v130\n"
      "v_alignbyte_b32 v126, v127, v126, v131\n"
      "s_waitcnt lgkmcnt(0)\n"
      "v_alignbyte_b32 v127, v188, v127, v131\n"
      "v_lshlrev_b32 v130, 16, v126\n"
      "v_fmac_f32 v136, s53, v130\n"
      "v_and_b32 v130, 0xffff0000, v126\n"
      "v_fmac_f32 v137, s53, v130\n"
      "v_lshlrev_b32 v130, 16, v127\n"
      "v_fmac_f32_e64 v138, s53, v130\n"
      "v_and_b32 v130, 0xffff0000, v127\n"
      "v_fmac_f32 v139, s53, v130\n"
      "v_alignbyte_b32 v130, v189, v189, v131\n"
      "v_lshlrev_b32 v130, 16, v130\n"
      "v_fmac_f32_e64 v184, s53, v130\n"
      "v_max3_f32 v165, v148, v149, v150\n"
      "v_max3_f32 v131, v151, v120, v121\n"
      "v_max3_f32 v165, v165, v122, v123\n"
      "v_max3_f32 v131, v131, v136, v137\n"
      "v_max3_f32 v165, v165, v138, v139\n"
      "v_max3_f32 v165, v165, v131, v184\n"
      "v_mov_b32 v130, v165\n"
      "s_nop 1\n"
      "v_permlane16_swap_b32 v165, v130\n"
      "v_max_f32 v165, v165, v130\n"
      "v_mov_b32 v130, v165\n"
      "s_nop 1\n"
      "v_permlane32_swap_b32 v165, v130\n"
      "v_max_f32 v165, v165, v130\n"
      "v_sub_f32 v148, v148, v165\n"
      "v_sub_f32 v149, v149, v165\n"
      "v_sub_f32 v150, v150, v165\n"
      "v_sub_f32 v151, v151, v165\n"
      "v_sub_f32 v120, v120, v165\n"
      "v_sub_f32 v121, v121, v165\n"
      "v_sub_f32 v122, v122, v165\n"
      "v_sub_f32 v123, v123, v165\n"
      "v_sub_f32 v136, v136, v165\n"
      "v_sub_f32 v137, v137, v165\n"
      "v_sub_f32 v138, v138, v165\n"
      "v_sub_f32 v139, v139, v165\n"
      "v_sub_f32_e64 v184, v184, v165\n"
      "s_waitcnt lgkmcnt(0)\n"
      "s_nop 4\n"
      "s_branch L_back_6_%=\n"
      "L_rare_7_%=:\n"
      "s_nop 7\n"
      "s_waitcnt lgkmcnt(0)\n"
      "s_waitcnt vmcnt(0)\n"
      "v_subrev_u32 v154, 115456, v9\n"
      "v_bfe_u32 v132, v154, 4, 4\n"
      "v_mul_u32_u24 v132, 98, v132\n"
      "v_lshrrev_b32_e64 v154, 8, v154\n"
      "v_lshl_add_u32 v132, v154, 3, v132\n"
      "v_add_u32 v132, s77, v132\n"
      "v_and_b32 v133, 2, v132\n"
      "v_and_b32_e64 v132, -4, v132\n"
      "v_add_u32 v132, 113728, v132\n"
      "ds_read2_b32 v[162:163], v132 offset0:0 offset1:1\n"
      "ds_read2_b32 v[160:161], v132 offset0:8 offset1:9\n"
      "ds_read2_b32 v[110:111], v132 offset0:16 offset1:17\n"
      "ds_read2_b32 v[108:109], v132 offset0:2 offset1:10\n"
      "ds_read2_b32 v[128:129], v132 offset0:18 offset1:24\n"
      "s_waitcnt lgkmcnt(4)\n"
      "v_alignbyte_b32 v162, v163, v162, v133\n"
      "s_waitcnt lgkmcnt(1)\n"
      "v_alignbyte_b32 v163, v108, v163, v133\n"
      "v_lshlrev_b32 v154, 16, v162\n"
      "v_fmac_f32 v180, s53, v154\n"
      "v_and_b32 v154, 0xffff0000, v162\n"
      "v_fmac_f32 v181, s53, v154\n"
      "v_lshlrev_b32 v154, 16, v163\n"
      "v_fmac_f32_e64 v182, s53, v154\n"
      "v_and_b32 v154, 0xffff0000, v163\n"
      "v_fmac_f32 v183, s53, v154\n"
      "v_alignbyte_b32 v160, v161, v160, v133\n"
      "v_alignbyte_b32 v161, v109, v161, v133\n"
      "v_lshlrev_b32 v154, 16, v160\n"
      "v_fmac_f32 v212, s53, v154\n"
      "v_and_b32 v154, 0xffff0000, v160\n"
      "v_fmac_f32 v213, s53, v154\n"
      "v_lshlrev_b32 v154, 16, v161\n"
      "v_fmac_f32 v214, s53, v154\n"
      "v_and_b32 v154, 0xffff0000, v161\n"
      "v_fmac_f32 v215, s53, v154\n"
      "v_alignbyte_b32 v110, v111, v110, v133\n"
      "s_waitcnt lgkmcnt(0)\n"
      "v_alignbyte_b32 v111, v128, v111, v133\n"
      "v_lshlrev_b32 v154, 16, v110\n"
      "v_fmac_f32 v116, s53, v154\n"
      "v_and_b32 v154, 0xffff0000, v110\n"
      "v_fmac_f32 v117, s53, v154\n"
      "v_lshlrev_b32 v154, 16, v111\n"
      "v_fmac_f32 v118, s53, v154\n"
      "v_and_b32 v154, 0xffff0000, v111\n"
      "v_fmac_f32 v119, s53, v154\n"
      "v_alignbyte_b32 v154, v129, v129, v133\n"
      "v_lshlrev_b32 v154, 16, v154\n"
      "v_fmac_f32 v188, s53, v154\n"
      "v_max3_f32 v132, v180, v181, v182\n"
      "v_max3_f32 v133, v183, v212, v213\n"
      "v_max3_f32 v132, v132, v214, v215\n"
      "v_max3_f32 v133, v133, v116, v117\n"
      "v_max3_f32 v132, v132, v118, v119\n"
      "v_max3_f32 v132, v132, v133, v188\n"
      "v_mov_b32 v154, v132\n"
      "s_nop 1\n"
      "v_permlane16_swap_b32 v132, v154\n"
      "v_max_f32 v132, v132, v154\n"
      "v_mov_b32 v154, v132\n"
      "s_nop 1\n"
      "v_permlane32_swap_b32 v132, v154\n"
      "v_max_f32 v132, v132, v154\n"
      "v_sub_f32 v180, v180, v132\n"
      "v_sub_f32 v181, v181, v132\n"
      "v_sub_f32 v182, v182, v132\n"
      "v_sub_f32 v183, v183, v132\n"
      "v_sub_f32 v212, v212, v132\n"
      "v_sub_f32 v213, v213, v132\n"
      "v_sub_f32 v214, v214, v132\n"
      "v_sub_f32 v215, v215, v132\n"
      "v_sub_f32 v116, v116, v132\n"
      "v_sub_f32 v117, v117, v132\n"
      "v_sub_f32 v118, v118, v132\n"
      "v_sub_f32 v119, v119, v132\n"
      "v_sub_f32 v188, v188, v132\n"
      "s_waitcnt lgkmcnt(0)\n"
      "s_nop 4\n"
      "s_branch L_back_8_%=\n"
      "L_rare_9_%=:\n"
      "s_nop 7\n"
      "s_waitcnt lgkmcnt(0)\n"
      "s_waitcnt vmcnt(0)\n"
      "v_subrev_u32 v117, 115456, v9\n"
      "v_lshrrev_b32 v117, 3, v117\n"
      "v_min_u32 v117, 96, v117\n"
      "v_add_u32 v117, s77, v117\n"
      "v_add_u32 v117, 115296, v117\n"
      "ds_read_u16 v117, v117\n"
      "s_waitcnt lgkmcnt(0)\n"
      "v_lshlrev_b32_e64 v117, 16, v117\n"
      "v_fmac_f32 v148, s53, v117\n"
      "s_nop 1\n"
      "v_max_f32_dpp v119, v148, v148 row_ror:8 row_mask:0xf bank_mask:0xf\n"
      "s_nop 1\n"
      "v_max_f32_dpp v119, v119, v119 row_ror:4 row_mask:0xf bank_mask:0xf\n"
      "s_nop 1\n"
      "v_max_f32_dpp v119, v119, v119 row_ror:2 row_mask:0xf bank_mask:0xf\n"
      "s_nop 1\n"
      "v_max_f32_dpp v119, v119, v119 row_ror:1 row_mask:0xf bank_mask:0xf\n"
      "s_nop 1\n"
      "v_max_f32_dpp v119, v119, v119 row_bcast:15 row_mask:0xa bank_mask:0xf\n"
      "s_nop 1\n"
      "v_max_f32_dpp v119, v119, v119 row_bcast:31 row_mask:0xc bank_mask:0xf\n"
      "s_nop 0\n"
      "v_readlane_b32 s72, v119, 63\n"
      "s_nop 1\n"
      "v_subrev_f32 v148, s72, v148\n"
      "s_waitcnt lgkmcnt(0)\n"
      "s_nop 4\n"
      "s_branch L_back_10_%=\n"
      "L_rare_27_%=:\n"
      "s_nop 7\n"
      "s_waitcnt lgkmcnt(0)\n"
      "s_bitcmp1_b32 s46, 31\n"
      "s_cselect_b32 s75, 100270464, 0\n"
      "s_bitcmp1_b32 s46, 30\n"
      "s_cselect_b32 s76, 196224, 0\n"
      "s_and_b32 s74, s46, 0x3fffffff\n"
      "s_cmp_ge_u32 s46, 0x40000000\n"
      "s_cselect_b32 s74, s74, 0\n"
      "s_cselect_b32 s46, 0, s46\n"
      "s_sub_u32 s74, s74, s69\n"
      "s_add_u32 s69, s69, s74\n"
      "s_sub_u32 s75, s75, s70\n"
      "s_add_u32 s70, s70, s75\n"
      "s_sub_u32 s76, s76, s71\n"
      "s_add_u32 s71, s71, s76\n"
      "v_add_u32 v20, s74, v20\n"
      "v_add_u32 v21, s74, v21\n"
      "v_add_u32 v22, s74, v22\n"
      "v_add_u32 v23, s74, v23\n"
      "v_add_u32 v25, s74, v25\n"
      "s_cmp_eq_u32 s75, 0\n"
      "s_cbranch_scc1 L_ax_25_%=\n"
      "v_bfe_i32 v103, v27, 10, 1\n"
      "v_and_b32 v103, s75, v103\n"
      "v_sub_u32 v20, v20, v103\n"
      "v_bfe_i32 v103, v27, 12, 1\n"
      "v_and_b32 v103, s75, v103\n"
      "v_sub_u32 v21, v21, v103\n"
      "v_bfe_i32 v103, v27, 14, 1\n"
      "v_and_b32 v103, s75, v103\n"
      "v_sub_u32 v22, v22, v103\n"
      "v_bfe_i32 v103, v27, 16, 1\n"
      "v_and_b32 v103, s75, v103\n"
      "v_sub_u32 v23, v23, v103\n"
      "v_bfe_i32 v103, v27, 18, 1\n"
      "v_and_b32 v103, s75, v103\n"
      "v_sub_u32 v25, v25, v103\n"
      "L_ax_25_%=:\n"
      "s_cmp_eq_u32 s76, 0\n"
      "s_cbranch_scc1 L_ax_26_%=\n"
      "v_bfe_i32 v103, v27, 11, 1\n"
      "v_and_b32 v103, s76, v103\n"
      "v_sub_u32_e64 v20, v20, v103\n"
      "v_bfe_i32 v103, v27, 13, 1\n"
      "v_and_b32 v103, s76, v103\n"
      "v_sub_u32 v21, v21, v103\n"
      "v_bfe_i32 v103, v27, 15, 1\n"
      "v_and_b32 v103, s76, v103\n"
      "v_sub_u32 v22, v22, v103\n"
      "v_bfe_i32 v103, v27, 17, 1\n"
      "v_and_b32 v103, s76, v103\n"
      "v_sub_u32 v23, v23, v103\n"
      "v_bfe_i32 v103, v27, 19, 1\n"
      "v_and_b32 v103, s76, v103\n"
      "v_sub_u32 v25, v25, v103\n"
      "L_ax_26_%=:\n"
      "s_or_b32 s48, s66, s69\n"
      "s_cmp_lg_u32 s48, 0\n"
      "s_cselect_b32 s48, 0x40000000, 0\n"
      "s_waitcnt lgkmcnt(0)\n"
      "s_nop 4\n"
      "s_branch L_back_28_%=\n"
      "L_rare_31_%=:\n"
      "s_nop 7\n"
      "s_waitcnt lgkmcnt(0)\n"
      "s_bitcmp1_b32 s46, 31\n"
      "s_cselect_b32 s75, 100270464, 0\n"
      "s_bitcmp1_b32 s46, 30\n"
      "s_cselect_b32 s76, 196224, 0\n"
      "s_and_b32 s74, s46, 0x3fffffff\n"
      "s_cmp_ge_u32 s46, 0x40000000\n"
      "s_cselect_b32 s74, s74, 0\n"
      "s_cselect_b32 s46, 0, s46\n"
      "s_sub_u32 s74, s74, s69\n"
      "s_add_u32 s69, s69, s74\n"
      "s_sub_u32 s75, s75, s70\n"
      "s_add_u32 s70, s70, s75\n"
      "s_sub_u32 s76, s76, s71\n"
      "s_add_u32 s71, s71, s76\n"
      "v_add_u32 v20, s74, v20\n"
      "v_add_u32 v21, s74, v21\n"
      "v_add_u32 v22, s74, v22\n"
      "v_add_u32 v23, s74, v23\n"
      "v_add_u32 v25, s74, v25\n"
      "s_cmp_eq_u32 s75, 0\n"
      "s_cbranch_scc1 L_ax_29_%=\n"
      "v_bfe_i32 v139, v27, 10, 1\n"
      "v_and_b32 v139, s75, v139\n"
      "v_sub_u32 v20, v20, v139\n"
      "v_bfe_i32 v139, v27, 12, 1\n"
      "v_and_b32 v139, s75, v139\n"
      "v_sub_u32 v21, v21, v139\n"
      "v_bfe_i32 v139, v27, 14, 1\n"
      "v_and_b32 v139, s75, v139\n"
      "v_sub_u32 v22, v22, v139\n"
      "v_bfe_i32 v139, v27, 16, 1\n"
      "v_and_b32 v139, s75, v139\n"
      "v_sub_u32 v23, v23, v139\n"
      "v_bfe_i32 v139, v27, 18, 1\n"
      "v_and_b32 v139, s75, v139\n"
      "v_sub_u32 v25, v25, v139\n"
      "L_ax_29_%=:\n"
      "s_cmp_eq_u32 s76, 0\n"
      "s_cbranch_scc1 L_ax_30_%=\n"
      "v_bfe_i32 v139, v27, 11, 1\n"
      "v_and_b32 v139, s76, v139\n"
      "v_sub_u32 v20, v20, v139\n"
      "v_bfe_i32 v139, v27, 13, 1\n"
      "v_and_b32 v139, s76, v139\n"
      "v_sub_u32 v21, v21, v139\n"
      "v_bfe_i32 v139, v27, 15, 1\n"
      "v_and_b32 v139, s76, v139\n"
      "v_sub_u32 v22, v22, v139\n"
      "v_bfe_i32 v139, v27, 17, 1\n"
      "v_and_b32 v139, s76, v139\n"
      "v_sub_u32 v23, v23, v139\n"
      "v_bfe_i32 v139, v27, 19, 1\n"
      "v_and_b32 v139, s76, v139\n"
      "v_sub_u32 v25, v25, v139\n"
      "L_ax_30_%=:\n"
      "s_or_b32 s48, s66, s69\n"
      "s_cmp_lg_u32 s48, 0\n"
      "s_cselect_b32 s48, 0x40000000, 0\n"
      "s_waitcnt lgkmcnt(0)\n"
      "s_nop 4\n"
      "s_branch L_back_32_%=\n"
      "L_rare_35_%=:\n"
      "s_nop 7\n"
      "s_waitcnt lgkmcnt(0)\n"
      "s_bitcmp1_b32 s46, 31\n"
      "s_cselect_b32 s75, 100270464, 0\n"
      "s_bitcmp1_b32 s46, 30\n"
      "s_cselect_b32 s76, 196224, 0\n"
      "s_and_b32 s74, s46, 0x3fffffff\n"
      "s_cmp_ge_u32 s46, 0x40000000\n"
      "s_cselect_b32 s74, s74, 0\n"
      "s_cselect_b32 s46, 0, s46\n"
      "s_sub_u32 s74, s74, s69\n"
      "s_add_u32 s69, s69, s74\n"
      "s_sub_u32 s75, s75, s70\n"
      "s_add_u32 s70, s70, s75\n"
      "s_sub_u32 s76, s76, s71\n"
      "s_add_u32 s71, s71, s76\n"
      "v_add_u32 v20, s74, v20\n"
      "v_add_u32 v21, s74, v21\n"
      "v_add_u32 v22, s74, v22\n"
      "v_add_u32 v23, s74, v23\n"
      "v_add_u32 v25, s74, v25\n"
      "s_cmp_eq_u32 s75, 0\n"
      "s_cbranch_scc1 L_ax_33_%=\n"
      "v_bfe_i32 v157, v27, 10, 1\n"
      "v_and_b32 v157, s75, v157\n"
      "v_sub_u32 v20, v20, v157\n"
      "v_bfe_i32 v157, v27, 12, 1\n"
      "v_and_b32 v157, s75, v157\n"
      "v_sub_u32 v21, v21, v157\n"
      "v_bfe_i32 v157, v27, 14, 1\n"
      "v_and_b32 v157, s75, v157\n"
      "v_sub_u32 v22, v22, v157\n"
      "v_bfe_i32 v157, v27, 16, 1\n"
      "v_and_b32 v157, s75, v157\n"
      "v_sub_u32 v23, v23, v157\n"
      "v_bfe_i32 v157, v27, 18, 1\n"
      "v_and_b32 v157, s75, v157\n"
      "v_sub_u32 v25, v25, v157\n"
      "L_ax_33_%=:\n"
      "s_cmp_eq_u32 s76, 0\n"
      "s_cbranch_scc1 L_ax_34_%=\n"
      "v_bfe_i32 v157, v27, 11, 1\n"
      "v_and_b32 v157, s76, v157\n"
      "v_sub_u32 v20, v20, v157\n"
      "v_bfe_i32 v157, v27, 13, 1\n"
      "v_and_b32 v157, s76, v157\n"
      "v_sub_u32 v21, v21, v157\n"
      "v_bfe_i32 v157, v27, 15, 1\n"
      "v_and_b32 v157, s76, v157\n"
      "v_sub_u32 v22, v22, v157\n"
      "v_bfe_i32 v157, v27, 17, 1\n"
      "v_and_b32 v157, s76, v157\n"
      "v_sub_u32 v23, v23, v157\n"
      "v_bfe_i32 v157, v27, 19, 1\n"
      "v_and_b32 v157, s76, v157\n"
      "v_sub_u32 v25, v25, v157\n"
      "L_ax_34_%=:\n"
      "s_or_b32 s48, s66, s69\n"
      "s_cmp_lg_u32 s48, 0\n"
      "s_cselect_b32 s48, 0x40000000, 0\n"
      "s_waitcnt lgkmcnt(0)\n"
      "s_nop 4\n"
      "s_branch L_back_36_%=\n"
      "L_rare_39_%=:\n"
      "s_nop 7\n"
      "s_waitcnt lgkmcnt(0)\n"
      "s_bitcmp1_b32 s46, 31\n"
      "s_cselect_b32 s75, 100270464, 0\n"
      "s_bitcmp1_b32 s46, 30\n"
      "s_cselect_b32 s76, 196224, 0\n"
      "s_and_b32 s74, s46, 0x3fffffff\n"
      "s_cmp_ge_u32 s46, 0x40000000\n"
      "s_cselect_b32 s74, s74, 0\n"
      "s_cselect_b32 s46, 0, s46\n"
      "s_sub_u32 s74, s74, s69\n"
      "s_add_u32 s69, s69, s74\n"
      "s_sub_u32 s75, s75, s70\n"
      "s_add_u32 s70, s70, s75\n"
      "s_sub_u32 s76, s76, s71\n"
      "s_add_u32 s71, s71, s76\n"
      "v_add_u32 v20, s74, v20\n"
      "v_add_u32 v21, s74, v21\n"
      "v_add_u32 v22, s74, v22\n"
      "v_add_u32 v23, s74, v23\n"
      "v_add_u32 v25, s74, v25\n"
      "s_cmp_eq_u32 s75, 0\n"
      "s_cbranch_scc1 L_ax_37_%=\n"
      "v_bfe_i32 v99, v27, 10, 1\n"
      "v_and_b32 v99, s75, v99\n"
      "v_sub_u32 v20, v20, v99\n"
      "v_bfe_i32 v99, v27, 12, 1\n"
      "v_and_b32 v99, s75, v99\n"
      "v_sub_u32 v21, v21, v99\n"
      "v_bfe_i32 v99, v27, 14, 1\n"
      "v_and_b32 v99, s75, v99\n"
      "v_sub_u32 v22, v22, v99\n"
      "v_bfe_i32 v99, v27, 16, 1\n"
      "v_and_b32 v99, s75, v99\n"
      "v_sub_u32 v23, v23, v99\n"
      "v_bfe_i32 v99, v27, 18, 1\n"
      "v_and_b32 v99, s75, v99\n"
      "v_sub_u32 v25, v25, v99\n"
      "L_ax_37_%=:\n"
      "s_cmp_eq_u32 s76, 0\n"
      "s_cbranch_scc1 L_ax_38_%=\n"
      "v_bfe_i32 v99, v27, 11, 1\n"
      "v_and_b32 v99, s76, v99\n"
      "v_sub_u32 v20, v20, v99\n"
      "v_bfe_i32 v99, v27, 13, 1\n"
      "v_and_b32 v99, s76, v99\n"
      "v_sub_u32 v21, v21, v99\n"
      "v_bfe_i32 v99, v27, 15, 1\n"
      "v_and_b32 v99, s76, v99\n"
      "v_sub_u32 v22, v22, v99\n"
      "v_bfe_i32 v99, v27, 17, 1\n"
      "v_and_b32 v99, s76, v99\n"
      "v_sub_u32 v23, v23, v99\n"
      "v_bfe_i32 v99, v27, 19, 1\n"
      "v_and_b32 v99, s76, v99\n"
      "v_sub_u32 v25, v25, v99\n"
      "L_ax_38_%=:\n"
      "s_or_b32 s48, s66, s69\n"
      "s_cmp_lg_u32 s48, 0\n"
      "s_cselect_b32 s48, 0x40000000, 0\n"
      "s_waitcnt lgkmcnt(0)\n"
      "s_nop 4\n"
      "s_branch L_back_40_%=\n"
      "L_cend_%=:\n"
      :
      : "s"(__builtin_amdgcn_kernarg_segment_ptr()), "s"(wave), "s"(blockIdx.x), "s"(CT_TAB), "s"(lds0)
      : "v0","v1","v2","v3","v4","v5","v6","v7","v8","v9","v10","v11","v12","v13","v14","v15","v16","v17","v18","v19","v20","v21","v22","v23","v24","v25","v26","v27","v28","v29","v30","v31","v32","v33","v34","v35","v36","v37","v38","v39","v40","v41","v42","v43","v44","v45","v46","v47","v48","v49","v50","v51","v52","v53","v54","v55","v56","v57","v58","v59","v60","v61","v62","v63","v64","v65","v66","v67","v68","v69","v70","v71","v72","v73","v74","v75","v76","v77","v78","v79","v80","v81","v82","v83","v84","v85","v86","v87","v88","v89","v90","v91","v92","v93","v94","v95","v96","v97","v98","v99","v100","v101","v102","v103","v104","v105","v106","v107","v108","v109","v110","v111","v112","v113","v114","v115","v116","v117","v118","v119","v120","v121","v122","v123","v124","v125","v126","v127","v128","v129","v130","v131","v132","v133","v134","v135","v136","v137","v138","v139","v140","v141","v142","v143","v144","v145","v146","v147","v148","v149","v150","v151","v152","v153","v154","v155","v156","v157","v158","v159","v160","v161","v162","v163","v164","v165","v166","v167","v168","v169","v170","v171","v172","v173","v174","v175","v176","v177","v178","v179","v180","v181","v182","v183","v184","v185","v186","v187","v188","v189","v190","v191","v192","v193","v194","v195","v196","v197","v198","v199","v200","v201","v202","v203","v204","v205","v206","v207","v208","v209","v210","v211","v212","v213","v214","v215","v216","v217","v218","v219","v220","v221","v222","v223","v224","v225","v226","v227","v228","v229","v230","v231","v232","v233","v234","v235","v236","v237","v238","v239","v240","v241","v242","v243","v244","v245","v246","v247","v248","v249","v250","v251","v252","v253","v254","v255","a0","a1","a2","a3","a4","a5","a6","a7","a8","a9","a10","a11","a12","a13","a14","a15","a16","a17","a18","a19","a20","a21","a22","a23","a24","a25","a26","a27","a28","a29","a30","a31","a32","a33","a34","a35","a36","a37","a38","a39","a40","a41","a42","a43","a44","a45","a46","a47","a48","a49","a50","a51","a52","a53","a54","a55","a56","a57","a58","a59","a60","a61","a62","a63","a64","a65","a66","a67","a68","a69","a70","a71","a72","a73","a74","a75","a76","a77","a78","a79","a80","a81","a82","a83","a84","a85","a86","a87","a88","a89","a90","a91","a92","a93","a94","a95","a96","a97","a98","a99","a100","a101","a102","a103","a104","a105","a106","a107","a108","a109","a110","a111","a112","a113","a114","a115","a116","a117","a118","a119","a120","a121","a122","a123","a124","a125","a126","a127","a128","a129","a130","a131","a132","a133","a134","a135","a136","a137","a138","a139","a140","a141","a142","a143","a144","a145","a146","a147","a148","a149","a150","a151","a152","a153","a154","a155","a156","a157","a158","a159","a160","a161","a162","a163","a164","a165","a166","a167","a168","a169","a170","a171","a172","a173","a174","a175","a176","a177","a178","a179","a180","a181","a182","a183","a184","a185","a186","a187","a188","a189","a190","a191","a192","a193","a194","a195","a196","a197","a198","a199","a200","a201","a202","a203","a204","a205","a206","a207","a208","a209","a210","a211","a212","a213","a214","a215","a216","a217","a218","a219","a220","a221","a222","a223","a224","a225","a226","a227","a228","a229","a230","a231","a232","a233","a234","a235","a236","a237","a238","a239","a240","a241","a242","a243","a244","a245","a246","a247","a248","a249","a250","a251","a252","a253","a254","a255","s16","s17","s18","s19","s20","s21","s22","s23","s24","s25","s26","s27","s28","s29","s30","s31","s33","s34","s35","s36","s37","s38","s39","s40","s41","s42","s43","s44","s45","s46","s47","s48","s49","s50","s51","s52","s53","s54","s55","s56","s57","s58","s59","s60","s61","s62","s63","s64","s65","s66","s67","s68","s69","s70","s71","s72","s73","s74","s75","s76","s77","s78","s79","s80","s81","s82","s83","s84","s85","s86","s87","s88","s89","s90","s91","s92","s93","s94","s95","s96","s97","s98","s99","s100","s101", "vcc", "scc", "memory");
}

}

extern "C" void kernel_launch(void* const* d_in, const int* in_sizes, int n_in, void* d_out, int out_size, void* d_ws,
                              size_t ws_size, hipStream_t stream) {
  const uint16_t* x = (const uint16_t*)d_in[0];
  const uint16_t* wqkv = (const uint16_t*)d_in[1];
  const uint16_t* wo = (const uint16_t*)d_in[2];
  const uint16_t* ls = (const uint16_t*)d_in[3];
  const uint16_t* w1 = (const uint16_t*)d_in[4];
  const uint16_t* w2 = (const uint16_t*)d_in[5];
  const uint16_t* rc = (const uint16_t*)d_in[6];
  const int* ridx = (const int*)d_in[7];
  const uint16_t* mask = (const uint16_t*)d_in[8];
  swin::swin_main<<<256, 256, 0, stream>>>(x, mask, (char*)d_ws, (uint16_t*)d_out, wqkv, wo, ls, w1, w2, rc, ridx);
}
```

```cpp
#include <hip/hip_runtime.h>
#include <hip/hip_bf16.h>
#include <stdint.h>

namespace swin {

constexpr unsigned CT_WORDS = 8192u;
constexpr unsigned LDS_BYTES = 163840u;

__device__ __attribute__((aligned(256))) const unsigned CT_TAB[CT_WORDS] = {0x4800u,0x4820u,0x4840u,0x4800u,0x4808u,0x4828u,0x4848u,0x4810u,0x4810u,0x4830u,0x4850u,0x4820u,0x4818u,0x4838u,0x4858u,0x4830u,0x4880u,0x48a0u,0x48c0u,0x4880u,0x4888u,0x48a8u,0x48c8u,0x4890u,0x4890u,0x48b0u,0x48d0u,0x48a0u,0x4898u,0x48b8u,0x48d8u,0x48b0u,0x4920u,0x4900u,0x4960u,0x4920u,0x4928u,0x4908u,0x4968u,0x4930u,0x4930u,0x4910u,0x4970u,0x4900u,0x4938u,0x4918u,0x4978u,0x4910u,0x49a0u,0x4980u,0x49e0u,0x49a0u,0x49a8u,0x4988u,0x49e8u,0x49b0u,0x49b0u,0x4990u,0x49f0u,0x4980u,0x49b8u,0x4998u,0x49f8u,0x4990u,0x4c40u,0x4c60u,0x4c00u,0x4a00u,0x4c48u,0x4c68u,0x4c08u,0x4a10u,0x4c50u,0x4c70u,0x4c10u,0x4a20u,0x4c58u,0x4c78u,0x4c18u,0x4a30u,0x4cc0u,0x4ce0u,0x4c80u,0x4a80u,0x4cc8u,0x4ce8u,0x4c88u,0x4a90u,0x4cd0u,0x4cf0u,0x4c90u,0x4aa0u,0x4cd8u,0x4cf8u,0x4c98u,0x4ab0u,0x4d60u,0x4d40u,0x4d20u,0x4b20u,0x4d68u,0x4d48u,0x4d28u,0x4b30u,0x4d70u,0x4d50u,0x4d30u,0x4b00u,0x4d78u,0x4d58u,0x4d38u,0x4b10u,0x4de0u,0x4dc0u,0x4da0u,0x4ba0u,0x4de8u,0x4dc8u,0x4da8u,0x4bb0u,0x4df0u,0x4dd0u,0x4db0u,0x4b80u,0x4df8u,0x4dd8u,0x4db8u,0x4b90u,0x5000u,0x5020u,0x5040u,0x4c40u,0x5008u,0x5028u,0x5048u,0x4c50u,0x5010u,0x5030u,0x5050u,0x4c60u,0x5018u,0x5038u,0x5058u,0x4c70u,0x5080u,0x50a0u,0x50c0u,0x4cc0u,0x5088u,0x50a8u,0x50c8u,0x4cd0u,0x5090u,0x50b0u,0x50d0u,0x4ce0u,0x5098u,0x50b8u,0x50d8u,0x4cf0u,0x5120u,0x5100u,0x5160u,0x4d60u,0x5128u,0x5108u,0x5168u,0x4d70u,0x5130u,0x5110u,0x5170u,0x4d40u,0x5138u,0x5118u,0x5178u,0x4d50u,0x51a0u,0x5180u,0x51e0u,0x4de0u,0x51a8u,0x5188u,0x51e8u,0x4df0u,0x51b0u,0x5190u,0x51f0u,0x4dc0u,0x51b8u,0x5198u,0x51f8u,0x4dd0u,0x5440u,0x5460u,0x5400u,0x4e40u,0x5448u,0x5468u,0x5408u,0x4e50u,0x5450u,0x5470u,0x5410u,0x4e60u,0x5458u,0x5478u,0x5418u,0x4e70u,0x54c0u,0x54e0u,0x5480u,0x4ec0u,0x54c8u,0x54e8u,0x5488u,0x4ed0u,0x54d0u,0x54f0u,0x5490u,0x4ee0u,0x54d8u,0x54f8u,0x5498u,0x4ef0u,0x5560u,0x5540u,0x5520u,0x4f60u,0x5568u,0x5548u,0x5528u,0x4f70u,0x5570u,0x5550u,0x5530u,0x4f40u,0x5578u,0x5558u,0x5538u,0x4f50u,0x55e0u,0x55c0u,0x55a0u,0x4fe0u,0x55e8u,0x55c8u,0x55a8u,0x4ff0u,0x55f0u,0x55d0u,0x55b0u,0x4fc0u,0x55f8u,0x55d8u,0x55b8u,0x4fd0u,0x4840u,0x1e800u,0x1eb00u,0x0u,0x4848u,0x1ec90u,0x1ef90u,0x10u,0x4850u,0x1f120u,0x1f420u,0x20u,0x4858u,0x1f5b0u,0x1f8b0u,0x30u,0x48c0u,0x1fa40u,0x1fd40u,0x40u,0x48c8u,0x1fed0u,0x201d0u,0x50u,0x48d0u,0x20360u,0x20660u,0x60u,0x48d8u,0x207f0u,0x20af0u,0x70u,0x4960u,0x20c80u,0x20f80u,0x180u,0x4968u,0x21110u,0x21410u,0x190u,0x4970u,0x215a0u,0x218a0u,0x1a0u,0x4978u,0x21a30u,0x21d30u,0x1b0u,0x49e0u,0x21ec0u,0x221c0u,0x1c0u,0x49e8u,0x22350u,0x22650u,0x1d0u,0x49f0u,0x227e0u,0x22ae0u,0x1e0u,0x49f8u,0x22c70u,0x22f70u,0x1f0u,0x4a40u,0x1e830u,0x1eb08u,0x320u,0x4a48u,0x1ecc0u,0x1ef98u,0x330u,0x4a50u,0x1f150u,0x1f428u,0x300u,0x4a58u,0x1f5e0u,0x1f8b8u,0x310u,0x4ac0u,0x1fa70u,0x1fd48u,0x360u,0x4ac8u,0x1ff00u,0x201d8u,0x370u,0x4ad0u,0x20390u,0x20668u,0x340u,0x4ad8u,0x20820u,0x20af8u,0x350u,0x4b60u,0x20cb0u,0x20f88u,0x4a0u,0x4b68u,0x21140u,0x21418u,0x4b0u,0x4b70u,0x215d0u,0x218a8u,0x480u,0x4b78u,0x21a60u,0x21d38u,0x490u,0x4be0u,0x21ef0u,0x221c8u,0x4e0u,0x4be8u,0x22380u,0x22658u,0x4f0u,0x4bf0u,0x22810u,0x22ae8u,0x4c0u,0x4bf8u,0x22ca0u,0x22f78u,0x4d0u,0x4c00u,0x1e860u,0x1eb10u,0x600u,0x4c08u,0x1ecf0u,0x1efa0u,0x610u,0x4c10u,0x1f180u,0x1f430u,0x620u,0x4c18u,0x1f610u,0x1f8c0u,0x630u,0x4c80u,0x1faa0u,0x1fd50u,0x640u,0x4c88u,0x1ff30u,0x201e0u,0x650u,0x4c90u,0x203c0u,0x20670u,0x660u,0x4c98u,0x20850u,0x20b00u,0x670u,0x4d20u,0x20ce0u,0x20f90u,0x780u,0x4d28u,0x21170u,0x21420u,0x790u,0x4d30u,0x21600u,0x218b0u,0x7a0u,0x4d38u,0x21a90u,0x21d40u,0x7b0u,0x4da0u,0x21f20u,0x221d0u,0x7c0u,0x4da8u,0x223b0u,0x22660u,0x7d0u,0x4db0u,0x22840u,0x22af0u,0x7e0u,0x4db8u,0x22cd0u,0x22f80u,0x7f0u,0x4e00u,0x1e890u,0x1eb18u,0x920u,0x4e08u,0x1ed20u,0x1efa8u,0x930u,0x4e10u,0x1f1b0u,0x1f438u,0x900u,0x4e18u,0x1f640u,0x1f8c8u,0x910u,0x4e80u,0x1fad0u,0x1fd58u,0x960u,0x4e88u,0x1ff60u,0x201e8u,0x970u,0x4e90u,0x203f0u,0x20678u,0x940u,0x4e98u,0x20880u,0x20b08u,0x950u,0x4f20u,0x20d10u,0x20f98u,0xaa0u,0x4f28u,0x211a0u,0x21428u,0xab0u,0x4f30u,0x21630u,0x218b8u,0xa80u,0x4f38u,0x21ac0u,0x21d48u,0xa90u,0x4fa0u,0x21f50u,0x221d8u,0xae0u,0x4fa8u,0x223e0u,0x22668u,0xaf0u,0x4fb0u,0x22870u,0x22af8u,0xac0u,0x4fb8u,0x22d00u,0x22f88u,0xad0u,0x0u,0x40u,0x9000u,0x9000u,0x80u,0xc0u,0x9010u,0x9010u,0x110u,0x150u,0x9020u,0x9020u,0x190u,0x1d0u,0x9030u,0x9030u,0x220u,0x260u,0x9040u,0x9040u,0x2a0u,0x2e0u,0x9050u,0x9050u,0x330u,0x370u,0x9060u,0x9060u,0x3b0u,0x3f0u,0x9070u,0x9070u,0x440u,0x400u,0x9080u,0x9080u,0x4c0u,0x480u,0x9090u,0x9090u,0x550u,0x510u,0x90a0u,0x90a0u,0x5d0u,0x590u,0x90b0u,0x90b0u,0x660u,0x620u,0x90c0u,0x90c0u,0x6e0u,0x6a0u,0x90d0u,0x90d0u,0x770u,0x730u,0x90e0u,0x90e0u,0x7f0u,0x7b0u,0x90f0u,0x90f0u,0x10u,0x50u,0x9300u,0x9300u,0x90u,0xd0u,0x9310u,0x9310u,0x100u,0x140u,0x9320u,0x9320u,0x180u,0x1c0u,0x9330u,0x9330u,0x230u,0x270u,0x9340u,0x9340u,0x2b0u,0x2f0u,0x9350u,0x9350u,0x320u,0x360u,0x9360u,0x9360u,0x3a0u,0x3e0u,0x9370u,0x9370u,0x450u,0x410u,0x9380u,0x9380u,0x4d0u,0x490u,0x9390u,0x9390u,0x540u,0x500u,0x93a0u,0x93a0u,0x5c0u,0x580u,0x93b0u,0x93b0u,0x670u,0x630u,0x93c0u,0x93c0u,0x6f0u,0x6b0u,0x93d0u,0x93d0u,0x760u,0x720u,0x93e0u,0x93e0u,0x7e0u,0x7a0u,0x93f0u,0x93f0u,0x20u,0x60u,0x9600u,0x9600u,0xa0u,0xe0u,0x9610u,0x9610u,0x130u,0x170u,0x9620u,0x9620u,0x1b0u,0x1f0u,0x9630u,0x9630u,0x200u,0x240u,0x9640u,0x9640u,0x280u,0x2c0u,0x9650u,0x9650u,0x310u,0x350u,0x9660u,0x9660u,0x390u,0x3d0u,0x9670u,0x9670u,0x460u,0x420u,0x9680u,0x9680u,0x4e0u,0x4a0u,0x9690u,0x9690u,0x570u,0x530u,0x96a0u,0x96a0u,0x5f0u,0x5b0u,0x96b0u,0x96b0u,0x640u,0x600u,0x96c0u,0x96c0u,0x6c0u,0x680u,0x96d0u,0x96d0u,0x750u,0x710u,0x96e0u,0x96e0u,0x7d0u,0x790u,0x96f0u,0x96f0u,0x30u,0x70u,0x9900u,0x9900u,0xb0u,0xf0u,0x9910u,0x9910u,0x120u,0x160u,0x9920u,0x9920u,0x1a0u,0x1e0u,0x9930u,0x9930u,0x210u,0x250u,0x9940u,0x9940u,0x290u,0x2d0u,0x9950u,0x9950u,0x300u,0x340u,0x9960u,0x9960u,0x380u,0x3c0u,0x9970u,0x9970u,0x470u,0x430u,0x9980u,0x9980u,0x4f0u,0x4b0u,0x9990u,0x9990u,0x560u,0x520u,0x99a0u,0x99a0u,0x5e0u,0x5a0u,0x99b0u,0x99b0u,0x650u,0x610u,0x99c0u,0x99c0u,0x6d0u,0x690u,0x99d0u,0x99d0u,0x740u,0x700u,0x99e0u,0x99e0u,0x7c0u,0x780u,0x99f0u,0x99f0u,0x9c00u,0x12000u,0x12040u,0x12000u,0x9c10u,0x12190u,0x121d0u,0x12010u,0x9c20u,0x12320u,0x12360u,0x12020u,0x9c30u,0x124b0u,0x124f0u,0x12030u,0x9c40u,0x12640u,0x12600u,0x12040u,0x9c50u,0x127d0u,0x12790u,0x12050u,0x9c60u,0x12960u,0x12920u,0x12060u,0x9c70u,0x12af0u,0x12ab0u,0x12070u,0x9c80u,0x12c00u,0x12c40u,0x12080u,0x9c90u,0x12d90u,0x12dd0u,0x12090u,0x9ca0u,0x12f20u,0x12f60u,0x120a0u,0x9cb0u,0x130b0u,0x130f0u,0x120b0u,0x9cc0u,0x13240u,0x13200u,0x120c0u,0x9cd0u,0x133d0u,0x13390u,0x120d0u,0x9ce0u,0x13560u,0x13520u,0x120e0u,0x9cf0u,0x136f0u,0x136b0u,0x120f0u,0x9f00u,0x12010u,0x12048u,0x12100u,0x9f10u,0x12180u,0x121d8u,0x12110u,0x9f20u,0x12330u,0x12368u,0x12120u,0x9f30u,0x124a0u,0x124f8u,0x12130u,0x9f40u,0x12650u,0x12608u,0x12140u,0x9f50u,0x127c0u,0x12798u,0x12150u,0x9f60u,0x12970u,0x12928u,0x12160u,0x9f70u,0x12ae0u,0x12ab8u,0x12170u,0x9f80u,0x12c10u,0x12c48u,0x12180u,0x9f90u,0x12d80u,0x12dd8u,0x12190u,0x9fa0u,0x12f30u,0x12f68u,0x121a0u,0x9fb0u,0x130a0u,0x130f8u,0x121b0u,0x9fc0u,0x13250u,0x13208u,0x121c0u,0x9fd0u,0x133c0u,0x13398u,0x121d0u,0x9fe0u,0x13570u,0x13528u,0x121e0u,0x9ff0u,0x136e0u,0x136b8u,0x121f0u,0xa200u,0x12020u,0x12050u,0x12200u,0xa210u,0x121b0u,0x121c0u,0x12210u,0xa220u,0x12300u,0x12370u,0x12220u,0xa230u,0x12490u,0x124e0u,0x12230u,0xa240u,0x12660u,0x12610u,0x12240u,0xa250u,0x127f0u,0x12780u,0x12250u,0xa260u,0x12940u,0x12930u,0x12260u,0xa270u,0x12ad0u,0x12aa0u,0x12270u,0xa280u,0x12c20u,0x12c50u,0x12280u,0xa290u,0x12db0u,0x12dc0u,0x12290u,0xa2a0u,0x12f00u,0x12f70u,0x122a0u,0xa2b0u,0x13090u,0x130e0u,0x122b0u,0xa2c0u,0x13260u,0x13210u,0x122c0u,0xa2d0u,0x133f0u,0x13380u,0x122d0u,0xa2e0u,0x13540u,0x13530u,0x122e0u,0xa2f0u,0x136d0u,0x136a0u,0x122f0u,0xa500u,0x12030u,0x12058u,0x12300u,0xa510u,0x121a0u,0x121c8u,0x12310u,0xa520u,0x12310u,0x12378u,0x12320u,0xa530u,0x12480u,0x124e8u,0x12330u,0xa540u,0x12670u,0x12618u,0x12340u,0xa550u,0x127e0u,0x12788u,0x12350u,0xa560u,0x12950u,0x12938u,0x12360u,0xa570u,0x12ac0u,0x12aa8u,0x12370u,0xa580u,0x12c30u,0x12c58u,0x12380u,0xa590u,0x12da0u,0x12dc8u,0x12390u,0xa5a0u,0x12f10u,0x12f78u,0x123a0u,0xa5b0u,0x13080u,0x130e8u,0x123b0u,0xa5c0u,0x13270u,0x13218u,0x123c0u,0xa5d0u,0x133e0u,0x13388u,0x123d0u,0xa5e0u,0x13550u,0x13538u,0x123e0u,0xa5f0u,0x136c0u,0x136a8u,0x123f0u,0x4800u,0x1c300u,0x1e800u,0x1eb00u,0x4880u,0x1c310u,0x100000u,0x1eb02u,0x4910u,0x1c320u,0x100000u,0x1eb04u,0x4990u,0x1c330u,0x100000u,0x1eb06u,0x4a20u,0x1c340u,0x1e800u,0x1eb08u,0x4aa0u,0x1c350u,0x100000u,0x1eb0au,0x4b30u,0x1c360u,0x100000u,0x1eb0cu,0x4bb0u,0x1c370u,0x100000u,0x1eb0eu,0x4c40u,0x1c380u,0x1e800u,0x1eb10u,0x4cc0u,0x1c390u,0x100000u,0x1eb12u,0x4d50u,0x1c3a0u,0x100000u,0x1eb14u,0x4dd0u,0x1c3b0u,0x100000u,0x1eb16u,0x4e60u,0x1c3c0u,0x1e800u,0x1eb18u,0x4ee0u,0x1c3d0u,0x100000u,0x1eb1au,0x4f70u,0x1c3e0u,0x100000u,0x1eb1cu,0x4ff0u,0x1c3f0u,0x100000u,0x1eb1eu,0x4810u,0x1c400u,0x1e830u,0x1eb00u,0x4890u,0x1c410u,0x100000u,0x1eb02u,0x4900u,0x1c420u,0x100000u,0x1eb04u,0x4980u,0x1c430u,0x100000u,0x1eb06u,0x4a30u,0x1c440u,0x1e830u,0x1eb08u,0x4ab0u,0x1c450u,0x100000u,0x1eb0au,0x4b20u,0x1c460u,0x100000u,0x1eb0cu,0x4ba0u,0x1c470u,0x100000u,0x1eb0eu,0x4c50u,0x1c480u,0x1e830u,0x1eb10u,0x4cd0u,0x1c490u,0x100000u,0x1eb12u,0x4d40u,0x1c4a0u,0x100000u,0x1eb14u,0x4dc0u,0x1c4b0u,0x100000u,0x1eb16u,0x4e70u,0x1c4c0u,0x1e830u,0x1eb18u,0x4ef0u,0x1c4d0u,0x100000u,0x1eb1au,0x4f60u,0x1c4e0u,0x100000u,0x1eb1cu,0x4fe0u,0x1c4f0u,0x100000u,0x1eb1eu,0x4820u,0x1c500u,0x1e860u,0x1eb00u,0x48a0u,0x1c510u,0x100000u,0x1eb02u,0x4930u,0x1c520u,0x100000u,0x1eb04u,0x49b0u,0x1c530u,0x100000u,0x1eb06u,0x4a00u,0x1c540u,0x1e860u,0x1eb08u,0x4a80u,0x1c550u,0x100000u,0x1eb0au,0x4b10u,0x1c560u,0x100000u,0x1eb0cu,0x4b90u,0x1c570u,0x100000u,0x1eb0eu,0x4c60u,0x1c580u,0x1e860u,0x1eb10u,0x4ce0u,0x1c590u,0x100000u,0x1eb12u,0x4d70u,0x1c5a0u,0x100000u,0x1eb14u,0x4df0u,0x1c5b0u,0x100000u,0x1eb16u,0x4e40u,0x1c5c0u,0x1e860u,0x1eb18u,0x4ec0u,0x1c5d0u,0x100000u,0x1eb1au,0x4f50u,0x1c5e0u,0x100000u,0x1eb1cu,0x4fd0u,0x1c5f0u,0x100000u,0x1eb1eu,0x4830u,0x1c600u,0x1e890u,0x1eb00u,0x48b0u,0x1c610u,0x100000u,0x1eb02u,0x4920u,0x1c620u,0x100000u,0x1eb04u,0x49a0u,0x1c630u,0x100000u,0x1eb06u,0x4a10u,0x1c640u,0x1e890u,0x1eb08u,0x4a90u,0x1c650u,0x100000u,0x1eb0au,0x4b00u,0x1c660u,0x100000u,0x1eb0cu,0x4b80u,0x1c670u,0x100000u,0x1eb0eu,0x4c70u,0x1c680u,0x1e890u,0x1eb10u,0x4cf0u,0x1c690u,0x100000u,0x1eb12u,0x4d60u,0x1c6a0u,0x100000u,0x1eb14u,0x4de0u,0x1c6b0u,0x100000u,0x1eb16u,0x4e50u,0x1c6c0u,0x1e890u,0x1eb18u,0x4ed0u,0x1c6d0u,0x100000u,0x1eb1au,0x4f40u,0x1c6e0u,0x100000u,0x1eb1cu,0x4fc0u,0x1c6f0u,0x100000u,0x1eb1eu,0x24800u,0x1d300u,0x1d300u,0xc0100u,0x24802u,0x1d302u,0x100000u,0xc0110u,0x24804u,0x1d304u,0x100000u,0xc0120u,0x24806u,0x1d306u,0x100000u,0xc0130u,0x24a00u,0x1d310u,0x1d300u,0xc0140u,0x24a02u,0x1d312u,0x100000u,0xc0150u,0x24a04u,0x1d314u,0x100000u,0xc0160u,0x24a06u,0x1d316u,0x100000u,0xc0170u,0x24c00u,0x1d320u,0x1d300u,0xc0280u,0x24c02u,0x1d322u,0x100000u,0xc0290u,0x24c04u,0x1d324u,0x100000u,0xc02a0u,0x24c06u,0x1d326u,0x100000u,0xc02b0u,0x24e00u,0x1d330u,0x1d300u,0xc02c0u,0x24e02u,0x1d332u,0x100000u,0xc02d0u,0x24e04u,0x1d334u,0x100000u,0xc02e0u,0x24e06u,0x1d336u,0x100000u,0xc02f0u,0x24808u,0x1d308u,0x1d310u,0xc0410u,0x2480au,0x1d30au,0x100000u,0xc0400u,0x2480cu,0x1d30cu,0x100000u,0xc0430u,0x2480eu,0x1d30eu,0x100000u,0xc0420u,0x24a08u,0x1d318u,0x1d310u,0xc0450u,0x24a0au,0x1d31au,0x100000u,0xc0440u,0x24a0cu,0x1d31cu,0x100000u,0xc0470u,0x24a0eu,0x1d31eu,0x100000u,0xc0460u,0x24c08u,0x1d328u,0x1d310u,0xef990u,0x24c0au,0x1d32au,0x100000u,0xef980u,0x24c0cu,0x1d32cu,0x100000u,0xef9b0u,0x24c0eu,0x1d32eu,0x100000u,0xef9a0u,0x24e08u,0x1d338u,0x1d310u,0xef9d0u,0x24e0au,0x1d33au,0x100000u,0xef9c0u,0x24e0cu,0x1d33cu,0x100000u,0xef9f0u,0x24e0eu,0x1d33eu,0x100000u,0xef9e0u,0x25000u,0x1d340u,0x1d320u,0xefb20u,0x25002u,0x1d342u,0x100000u,0xefb30u,0x25004u,0x1d344u,0x100000u,0xefb00u,0x25006u,0x1d346u,0x100000u,0xefb10u,0x25200u,0x1d350u,0x1d320u,0xefb60u,0x25202u,0x1d352u,0x100000u,0xefb70u,0x25204u,0x1d354u,0x100000u,0xefb40u,0x25206u,0x1d356u,0x100000u,0xefb50u,0x25400u,0x1d360u,0x1d320u,0xefca0u,0x25402u,0x1d362u,0x100000u,0xefcb0u,0x25404u,0x1d364u,0x100000u,0xefc80u,0x25406u,0x1d366u,0x100000u,0xefc90u,0x25600u,0x1d370u,0x1d320u,0xefce0u,0x25602u,0x1d372u,0x100000u,0xefcf0u,0x25604u,0x1d374u,0x100000u,0xefcc0u,0x25606u,0x1d376u,0x100000u,0xefcd0u,0x100000u,0x1d348u,0x1d330u,0xefe30u,0x100000u,0x1d34au,0x100000u,0xefe20u,0x100000u,0x1d34cu,0x100000u,0xefe10u,0x100000u,0x1d34eu,0x100000u,0xefe00u,0x100000u,0x1d358u,0x1d330u,0xefe70u,0x100000u,0x1d35au,0x100000u,0xefe60u,0x100000u,0x1d35cu,0x100000u,0xefe50u,0x100000u,0x1d35eu,0x100000u,0xefe40u,0x100000u,0x1d368u,0x1d330u,0xeffb0u,0x100000u,0x1d36au,0x100000u,0xeffa0u,0x100000u,0x1d36cu,0x100000u,0xeff90u,0x100000u,0x1d36eu,0x100000u,0xeff80u,0x100000u,0x1d378u,0x1d330u,0xefff0u,0x100000u,0x1d37au,0x100000u,0xeffe0u,0x100000u,0x1d37cu,0x100000u,0xeffd0u,0x100000u,0x1d37eu,0x100000u,0xeffc0u,0x0u,0x30420u,0x8fc50u,0xc0000u,0x10u,0x30430u,0x8fc40u,0xc0010u,0x20u,0x30400u,0x8fc70u,0xc0020u,0x30u,0x30410u,0x8fc60u,0xc0030u,0x40u,0x30460u,0x8fc10u,0xc0040u,0x50u,0x30470u,0x8fc00u,0xc0050u,0x60u,0x30440u,0x8fc30u,0xc0060u,0x70u,0x30450u,0x8fc20u,0xc0070u,0x80u,0x304b0u,0x8fcd0u,0xc0080u,0x90u,0x304a0u,0x8fcc0u,0xc0090u,0xa0u,0x30490u,0x8fcf0u,0xc00a0u,0xb0u,0x30480u,0x8fce0u,0xc00b0u,0xc0u,0x304f0u,0x8fc90u,0xc00c0u,0xd0u,0x304e0u,0x8fc80u,0xc00d0u,0xe0u,0x304d0u,0x8fcb0u,0xc00e0u,0xf0u,0x304c0u,0x8fca0u,0xc00f0u,0x100u,0x30530u,0x8fd60u,0xc0100u,0x110u,0x30520u,0x8fd70u,0xc0110u,0x120u,0x30510u,0x8fd40u,0xc0120u,0x130u,0x30500u,0x8fd50u,0xc0130u,0x140u,0x30570u,0x8fd20u,0xc0140u,0x150u,0x30560u,0x8fd30u,0xc0150u,0x160u,0x30550u,0x8fd00u,0xc0160u,0x170u,0x30540u,0x8fd10u,0xc0170u,0x190u,0x305b0u,0x8fde0u,0xc0190u,0x180u,0x305a0u,0x8fdf0u,0xc0180u,0x1b0u,0x30590u,0x8fdc0u,0xc01b0u,0x1a0u,0x30580u,0x8fdd0u,0xc01a0u,0x1d0u,0x305f0u,0x8fda0u,0xc01d0u,0x1c0u,0x305e0u,0x8fdb0u,0xc01c0u,0x1f0u,0x305d0u,0x8fd80u,0xc01f0u,0x1e0u,0x305c0u,0x8fd90u,0xc01e0u,0x210u,0x30640u,0x8fe60u,0xc0210u,0x200u,0x30650u,0x8fe70u,0xc0200u,0x230u,0x30660u,0x8fe40u,0xc0230u,0x220u,0x30670u,0x8fe50u,0xc0220u,0x250u,0x30600u,0x8fe20u,0xc0250u,0x240u,0x30610u,0x8fe30u,0xc0240u,0x270u,0x30620u,0x8fe00u,0xc0270u,0x260u,0x30630u,0x8fe10u,0xc0260u,0x290u,0x306c0u,0x8fef0u,0xc0290u,0x280u,0x306d0u,0x8fee0u,0xc0280u,0x2b0u,0x306e0u,0x8fed0u,0xc02b0u,0x2a0u,0x306f0u,0x8fec0u,0xc02a0u,0x2d0u,0x30680u,0x8feb0u,0xc02d0u,0x2c0u,0x30690u,0x8fea0u,0xc02c0u,0x2f0u,0x306a0u,0x8fe90u,0xc02f0u,0x2e0u,0x306b0u,0x8fe80u,0xc02e0u,0x320u,0x30740u,0x8ff70u,0xc0320u,0x330u,0x30750u,0x8ff60u,0xc0330u,0x300u,0x30760u,0x8ff50u,0xc0300u,0x310u,0x30770u,0x8ff40u,0xc0310u,0x360u,0x30700u,0x8ff30u,0xc0360u,0x370u,0x30710u,0x8ff20u,0xc0370u,0x340u,0x30720u,0x8ff10u,0xc0340u,0x350u,0x30730u,0x8ff00u,0xc0350u,0x3a0u,0x307d0u,0x8fff0u,0xc03a0u,0x3b0u,0x307c0u,0x8ffe0u,0xc03b0u,0x380u,0x307f0u,0x8ffd0u,0xc0380u,0x390u,0x307e0u,0x8ffc0u,0xc0390u,0x3e0u,0x30790u,0x8ffb0u,0xc03e0u,0x3f0u,0x30780u,0x8ffa0u,0xc03f0u,0x3c0u,0x307b0u,0x8ff90u,0xc03c0u,0x3d0u,0x307a0u,0x8ff80u,0xc03d0u,0x0u,0x11f820u,0x0u,0x7030cu,0x0u,0x11f830u,0x0u,0x7030cu,0x0u,0x11f800u,0x0u,0x7030cu,0x0u,0x11f810u,0x0u,0x7030cu,0x0u,0x11f860u,0x0u,0x7030cu,0x0u,0x11f870u,0x0u,0x7030cu,0x0u,0x11f840u,0x0u,0x7030cu,0x0u,0x11f850u,0x0u,0x7030cu,0x0u,0x11f8b0u,0x0u,0x7230cu,0x0u,0x11f8a0u,0x0u,0x7230cu,0x0u,0x11f890u,0x0u,0x7230cu,0x0u,0x11f880u,0x0u,0x7230cu,0x0u,0x11f8f0u,0x0u,0x7230cu,0x0u,0x11f8e0u,0x0u,0x7230cu,0x0u,0x11f8d0u,0x0u,0x7230cu,0x0u,0x11f8c0u,0x0u,0x7230cu,0x0u,0x11f930u,0x0u,0x7232cu,0x0u,0x11f920u,0x0u,0x7232cu,0x0u,0x11f910u,0x0u,0x7232cu,0x0u,0x11f900u,0x0u,0x7232cu,0x0u,0x11f970u,0x0u,0x7232cu,0x0u,0x11f960u,0x0u,0x7232cu,0x0u,0x11f950u,0x0u,0x7232cu,0x0u,0x11f940u,0x0u,0x7232cu,0x0u,0x11f9b0u,0x0u,0x72124u,0x0u,0x11f9a0u,0x0u,0x72124u,0x0u,0x11f990u,0x0u,0x72124u,0x0u,0x11f980u,0x0u,0x72124u,0x0u,0x11f9f0u,0x0u,0x72124u,0x0u,0x11f9e0u,0x0u,0x72124u,0x0u,0x11f9d0u,0x0u,0x72124u,0x0u,0x11f9c0u,0x0u,0x72124u,0x0u,0x11fa40u,0x0u,0x721a6u,0x0u,0x11fa50u,0x0u,0x721a6u,0x0u,0x11fa60u,0x0u,0x721a6u,0x0u,0x11fa70u,0x0u,0x721a6u,0x0u,0x11fa00u,0x0u,0x721a6u,0x0u,0x11fa10u,0x0u,0x721a6u,0x0u,0x11fa20u,0x0u,0x721a6u,0x0u,0x11fa30u,0x0u,0x721a6u,0x0u,0x11fac0u,0x0u,0x72186u,0x0u,0x11fad0u,0x0u,0x72186u,0x0u,0x11fae0u,0x0u,0x72186u,0x0u,0x11faf0u,0x0u,0x72186u,0x0u,0x11fa80u,0x0u,0x72186u,0x0u,0x11fa90u,0x0u,0x72186u,0x0u,0x11faa0u,0x0u,0x72186u,0x0u,0x11fab0u,0x0u,0x72186u,0x0u,0x11fb40u,0x0u,0x72186u,0x0u,0x11fb50u,0x0u,0x72186u,0x0u,0x11fb60u,0x0u,0x72186u,0x0u,0x11fb70u,0x0u,0x72186u,0x0u,0x11fb00u,0x0u,0x72186u,0x0u,0x11fb10u,0x0u,0x72186u,0x0u,0x11fb20u,0x0u,0x72186u,0x0u,0x11fb30u,0x0u,0x72186u,0x0u,0x11fbd0u,0x0u,0x7230cu,0x0u,0x11fbc0u,0x0u,0x7230cu,0x0u,0x11fbf0u,0x0u,0x7230cu,0x0u,0x11fbe0u,0x0u,0x7230cu,0x0u,0x11fb90u,0x0u,0x7230cu,0x0u,0x11fb80u,0x0u,0x7230cu,0x0u,0x11fbb0u,0x0u,0x7230cu,0x0u,0x11fba0u,0x0u,0x7230cu,0x5800u,0x5820u,0x4860u,0x4860u,0x5808u,0x5828u,0x4868u,0x4870u,0x5810u,0x5830u,0x4870u,0x5800u,0x5818u,0x5838u,0x4878u,0x5810u,0x5880u,0x58a0u,0x48e0u,0x48e0u,0x5888u,0x58a8u,0x48e8u,0x48f0u,0x5890u,0x58b0u,0x48f0u,0x5880u,0x5898u,0x58b8u,0x48f8u,0x5890u,0x5920u,0x5900u,0x4940u,0x4940u,0x5928u,0x5908u,0x4948u,0x4950u,0x5930u,0x5910u,0x4950u,0x5920u,0x5938u,0x5918u,0x4958u,0x5930u,0x59a0u,0x5980u,0x49c0u,0x49c0u,0x59a8u,0x5988u,0x49c8u,0x49d0u,0x59b0u,0x5990u,0x49d0u,0x59a0u,0x59b8u,0x5998u,0x49d8u,0x59b0u,0x5c40u,0x5c60u,0x4c20u,0x4a60u,0x5c48u,0x5c68u,0x4c28u,0x4a70u,0x5c50u,0x5c70u,0x4c30u,0x5a00u,0x5c58u,0x5c78u,0x4c38u,0x5a10u,0x5cc0u,0x5ce0u,0x4ca0u,0x4ae0u,0x5cc8u,0x5ce8u,0x4ca8u,0x4af0u,0x5cd0u,0x5cf0u,0x4cb0u,0x5a80u,0x5cd8u,0x5cf8u,0x4cb8u,0x5a90u,0x5d60u,0x5d40u,0x4d00u,0x4b40u,0x5d68u,0x5d48u,0x4d08u,0x4b50u,0x5d70u,0x5d50u,0x4d10u,0x5b20u,0x5d78u,0x5d58u,0x4d18u,0x5b30u,0x5de0u,0x5dc0u,0x4d80u,0x4bc0u,0x5de8u,0x5dc8u,0x4d88u,0x4bd0u,0x5df0u,0x5dd0u,0x4d90u,0x5ba0u,0x5df8u,0x5dd8u,0x4d98u,0x5bb0u,0x6000u,0x6020u,0x5060u,0x4c20u,0x6008u,0x6028u,0x5068u,0x4c30u,0x6010u,0x6030u,0x5070u,0x5c40u,0x6018u,0x6038u,0x5078u,0x5c50u,0x6080u,0x60a0u,0x50e0u,0x4ca0u,0x6088u,0x60a8u,0x50e8u,0x4cb0u,0x6090u,0x60b0u,0x50f0u,0x5cc0u,0x6098u,0x60b8u,0x50f8u,0x5cd0u,0x6120u,0x6100u,0x5140u,0x4d00u,0x6128u,0x6108u,0x5148u,0x4d10u,0x6130u,0x6110u,0x5150u,0x5d60u,0x6138u,0x6118u,0x5158u,0x5d70u,0x61a0u,0x6180u,0x51c0u,0x4d80u,0x61a8u,0x6188u,0x51c8u,0x4d90u,0x61b0u,0x6190u,0x51d0u,0x5de0u,0x61b8u,0x6198u,0x51d8u,0x5df0u,0x6440u,0x6460u,0x5420u,0x4e20u,0x6448u,0x6468u,0x5428u,0x4e30u,0x6450u,0x6470u,0x5430u,0x5e40u,0x6458u,0x6478u,0x5438u,0x5e50u,0x64c0u,0x64e0u,0x54a0u,0x4ea0u,0x64c8u,0x64e8u,0x54a8u,0x4eb0u,0x64d0u,0x64f0u,0x54b0u,0x5ec0u,0x64d8u,0x64f8u,0x54b8u,0x5ed0u,0x6560u,0x6540u,0x5500u,0x4f00u,0x6568u,0x6548u,0x5508u,0x4f10u,0x6570u,0x6550u,0x5510u,0x5f60u,0x6578u,0x6558u,0x5518u,0x5f70u,0x65e0u,0x65c0u,0x5580u,0x4f80u,0x65e8u,0x65c8u,0x5588u,0x4f90u,0x65f0u,0x65d0u,0x5590u,0x5fe0u,0x65f8u,0x65d8u,0x5598u,0x5ff0u,0x5820u,0x1e8c0u,0x1eb60u,0xc40u,0x5828u,0x1ed50u,0x1eff0u,0xc50u,0x5830u,0x1f1e0u,0x1f480u,0xc60u,0x5838u,0x1f670u,0x1f910u,0xc70u,0x58a0u,0x1fb00u,0x1fda0u,0xc00u,0x58a8u,0x1ff90u,0x20230u,0xc10u,0x58b0u,0x20420u,0x206c0u,0xc20u,0x58b8u,0x208b0u,0x20b50u,0xc30u,0x5900u,0x20d40u,0x20fe0u,0xdc0u,0x5908u,0x211d0u,0x21470u,0xdd0u,0x5910u,0x21660u,0x21900u,0xde0u,0x5918u,0x21af0u,0x21d90u,0xdf0u,0x5980u,0x21f80u,0x22220u,0xd80u,0x5988u,0x22410u,0x226b0u,0xd90u,0x5990u,0x228a0u,0x22b40u,0xda0u,0x5998u,0x22d30u,0x22fd0u,0xdb0u,0x5a20u,0x1e8f0u,0x1eb68u,0xf60u,0x5a28u,0x1ed80u,0x1eff8u,0xf70u,0x5a30u,0x1f210u,0x1f488u,0xf40u,0x5a38u,0x1f6a0u,0x1f918u,0xf50u,0x5aa0u,0x1fb30u,0x1fda8u,0xf20u,0x5aa8u,0x1ffc0u,0x20238u,0xf30u,0x5ab0u,0x20450u,0x206c8u,0xf00u,0x5ab8u,0x208e0u,0x20b58u,0xf10u,0x5b00u,0x20d70u,0x20fe8u,0x10e0u,0x5b08u,0x21200u,0x21478u,0x10f0u,0x5b10u,0x21690u,0x21908u,0x10c0u,0x5b18u,0x21b20u,0x21d98u,0x10d0u,0x5b80u,0x21fb0u,0x22228u,0x10a0u,0x5b88u,0x22440u,0x226b8u,0x10b0u,0x5b90u,0x228d0u,0x22b48u,0x1080u,0x5b98u,0x22d60u,0x22fd8u,0x1090u,0x5c60u,0x1e920u,0x1eb70u,0x1240u,0x5c68u,0x1edb0u,0x1f000u,0x1250u,0x5c70u,0x1f240u,0x1f490u,0x1260u,0x5c78u,0x1f6d0u,0x1f920u,0x1270u,0x5ce0u,0x1fb60u,0x1fdb0u,0x1200u,0x5ce8u,0x1fff0u,0x20240u,0x1210u,0x5cf0u,0x20480u,0x206d0u,0x1220u,0x5cf8u,0x20910u,0x20b60u,0x1230u,0x5d40u,0x20da0u,0x20ff0u,0x13c0u,0x5d48u,0x21230u,0x21480u,0x13d0u,0x5d50u,0x216c0u,0x21910u,0x13e0u,0x5d58u,0x21b50u,0x21da0u,0x13f0u,0x5dc0u,0x21fe0u,0x22230u,0x1380u,0x5dc8u,0x22470u,0x226c0u,0x1390u,0x5dd0u,0x22900u,0x22b50u,0x13a0u,0x5dd8u,0x22d90u,0x22fe0u,0x13b0u,0x5e60u,0x1e950u,0x1eb78u,0x1560u,0x5e68u,0x1ede0u,0x1f008u,0x1570u,0x5e70u,0x1f270u,0x1f498u,0x1540u,0x5e78u,0x1f700u,0x1f928u,0x1550u,0x5ee0u,0x1fb90u,0x1fdb8u,0x1520u,0x5ee8u,0x20020u,0x20248u,0x1530u,0x5ef0u,0x204b0u,0x206d8u,0x1500u,0x5ef8u,0x20940u,0x20b68u,0x1510u,0x5f40u,0x20dd0u,0x20ff8u,0x16e0u,0x5f48u,0x21260u,0x21488u,0x16f0u,0x5f50u,0x216f0u,0x21918u,0x16c0u,0x5f58u,0x21b80u,0x21da8u,0x16d0u,0x5fc0u,0x22010u,0x22238u,0x16a0u,0x5fc8u,0x224a0u,0x226c8u,0x16b0u,0x5fd0u,0x22930u,0x22b58u,0x1680u,0x5fd8u,0x22dc0u,0x22fe8u,0x1690u,0x0u,0x40u,0x9000u,0xa800u,0x80u,0xc0u,0x9010u,0xa810u,0x110u,0x150u,0x9020u,0xa820u,0x190u,0x1d0u,0x9030u,0xa830u,0x220u,0x260u,0x9040u,0xa840u,0x2a0u,0x2e0u,0x9050u,0xa850u,0x330u,0x370u,0x9060u,0xa860u,0x3b0u,0x3f0u,0x9070u,0xa870u,0x440u,0x400u,0x9080u,0xa880u,0x4c0u,0x480u,0x9090u,0xa890u,0x550u,0x510u,0x90a0u,0xa8a0u,0x5d0u,0x590u,0x90b0u,0xa8b0u,0x660u,0x620u,0x90c0u,0xa8c0u,0x6e0u,0x6a0u,0x90d0u,0xa8d0u,0x770u,0x730u,0x90e0u,0xa8e0u,0x7f0u,0x7b0u,0x90f0u,0xa8f0u,0x10u,0x50u,0x9300u,0xab00u,0x90u,0xd0u,0x9310u,0xab10u,0x100u,0x140u,0x9320u,0xab20u,0x180u,0x1c0u,0x9330u,0xab30u,0x230u,0x270u,0x9340u,0xab40u,0x2b0u,0x2f0u,0x9350u,0xab50u,0x320u,0x360u,0x9360u,0xab60u,0x3a0u,0x3e0u,0x9370u,0xab70u,0x450u,0x410u,0x9380u,0xab80u,0x4d0u,0x490u,0x9390u,0xab90u,0x540u,0x500u,0x93a0u,0xaba0u,0x5c0u,0x580u,0x93b0u,0xabb0u,0x670u,0x630u,0x93c0u,0xabc0u,0x6f0u,0x6b0u,0x93d0u,0xabd0u,0x760u,0x720u,0x93e0u,0xabe0u,0x7e0u,0x7a0u,0x93f0u,0xabf0u,0x20u,0x60u,0x9600u,0xae00u,0xa0u,0xe0u,0x9610u,0xae10u,0x130u,0x170u,0x9620u,0xae20u,0x1b0u,0x1f0u,0x9630u,0xae30u,0x200u,0x240u,0x9640u,0xae40u,0x280u,0x2c0u,0x9650u,0xae50u,0x310u,0x350u,0x9660u,0xae60u,0x390u,0x3d0u,0x9670u,0xae70u,0x460u,0x420u,0x9680u,0xae80u,0x4e0u,0x4a0u,0x9690u,0xae90u,0x570u,0x530u,0x96a0u,0xaea0u,0x5f0u,0x5b0u,0x96b0u,0xaeb0u,0x640u,0x600u,0x96c0u,0xaec0u,0x6c0u,0x680u,0x96d0u,0xaed0u,0x750u,0x710u,0x96e0u,0xaee0u,0x7d0u,0x790u,0x96f0u,0xaef0u,0x30u,0x70u,0x9900u,0xb100u,0xb0u,0xf0u,0x9910u,0xb110u,0x120u,0x160u,0x9920u,0xb120u,0x1a0u,0x1e0u,0x9930u,0xb130u,0x210u,0x250u,0x9940u,0xb140u,0x290u,0x2d0u,0x9950u,0xb150u,0x300u,0x340u,0x9960u,0xb160u,0x380u,0x3c0u,0x9970u,0xb170u,0x470u,0x430u,0x9980u,0xb180u,0x4f0u,0x4b0u,0x9990u,0xb190u,0x560u,0x520u,0x99a0u,0xb1a0u,0x5e0u,0x5a0u,0x99b0u,0xb1b0u,0x650u,0x610u,0x99c0u,0xb1c0u,0x6d0u,0x690u,0x99d0u,0xb1d0u,0x740u,0x700u,0x99e0u,0xb1e0u,0x7c0u,0x780u,0x99f0u,0xb1f0u,0x9c08u,0x12060u,0x120a0u,0x12400u,0x9c18u,0x121f0u,0x12230u,0x12410u,0x9c28u,0x12340u,0x12380u,0x12420u,0x9c38u,0x124d0u,0x12510u,0x12430u,0x9c48u,0x12620u,0x126e0u,0x12440u,0x9c58u,0x127b0u,0x12870u,0x12450u,0x9c68u,0x12900u,0x129c0u,0x12460u,0x9c78u,0x12a90u,0x12b50u,0x12470u,0x9c88u,0x12c60u,0x12ca0u,0x12480u,0x9c98u,0x12df0u,0x12e30u,0x12490u,0x9ca8u,0x12f40u,0x12f80u,0x124a0u,0x9cb8u,0x130d0u,0x13110u,0x124b0u,0x9cc8u,0x13220u,0x132e0u,0x124c0u,0x9cd8u,0x133b0u,0x13470u,0x124d0u,0x9ce8u,0x13500u,0x135c0u,0x124e0u,0x9cf8u,0x13690u,0x13750u,0x124f0u,0x9f08u,0x12070u,0x120a8u,0x12500u,0x9f18u,0x121e0u,0x12238u,0x12510u,0x9f28u,0x12350u,0x12388u,0x12520u,0x9f38u,0x124c0u,0x12518u,0x12530u,0x9f48u,0x12630u,0x126e8u,0x12540u,0x9f58u,0x127a0u,0x12878u,0x12550u,0x9f68u,0x12910u,0x129c8u,0x12560u,0x9f78u,0x12a80u,0x12b58u,0x12570u,0x9f88u,0x12c70u,0x12ca8u,0x12580u,0x9f98u,0x12de0u,0x12e38u,0x12590u,0x9fa8u,0x12f50u,0x12f88u,0x125a0u,0x9fb8u,0x130c0u,0x13118u,0x125b0u,0x9fc8u,0x13230u,0x132e8u,0x125c0u,0x9fd8u,0x133a0u,0x13478u,0x125d0u,0x9fe8u,0x13510u,0x135c8u,0x125e0u,0x9ff8u,0x13680u,0x13758u,0x125f0u,0xa208u,0x12080u,0x120b0u,0x12600u,0xa218u,0x12210u,0x12220u,0x12610u,0xa228u,0x123a0u,0x12390u,0x12620u,0xa238u,0x12530u,0x12500u,0x12630u,0xa248u,0x126c0u,0x126f0u,0x12640u,0xa258u,0x12850u,0x12860u,0x12650u,0xa268u,0x129e0u,0x129d0u,0x12660u,0xa278u,0x12b70u,0x12b40u,0x12670u,0xa288u,0x12c80u,0x12cb0u,0x12680u,0xa298u,0x12e10u,0x12e20u,0x12690u,0xa2a8u,0x12fa0u,0x12f90u,0x126a0u,0xa2b8u,0x13130u,0x13100u,0x126b0u,0xa2c8u,0x132c0u,0x132f0u,0x126c0u,0xa2d8u,0x13450u,0x13460u,0x126d0u,0xa2e8u,0x135e0u,0x135d0u,0x126e0u,0xa2f8u,0x13770u,0x13740u,0x126f0u,0xa508u,0x12090u,0x120b8u,0x12700u,0xa518u,0x12200u,0x12228u,0x12710u,0xa528u,0x123b0u,0x12398u,0x12720u,0xa538u,0x12520u,0x12508u,0x12730u,0xa548u,0x126d0u,0x126f8u,0x12740u,0xa558u,0x12840u,0x12868u,0x12750u,0xa568u,0x129f0u,0x129d8u,0x12760u,0xa578u,0x12b60u,0x12b48u,0x12770u,0xa588u,0x12c90u,0x12cb8u,0x12780u,0xa598u,0x12e00u,0x12e28u,0x12790u,0xa5a8u,0x12fb0u,0x12f98u,0x127a0u,0xa5b8u,0x13120u,0x13108u,0x127b0u,0xa5c8u,0x132d0u,0x132f8u,0x127c0u,0xa5d8u,0x13440u,0x13468u,0x127d0u,0xa5e8u,0x135f0u,0x135d8u,0x127e0u,0xa5f8u,0x13760u,0x13748u,0x127f0u,0x5000u,0x1c300u,0x1e8c0u,0x1eb60u,0x5080u,0x1c310u,0x100000u,0x1eb62u,0x5110u,0x1c320u,0x100000u,0x1eb64u,0x5190u,0x1c330u,0x100000u,0x1eb66u,0x5220u,0x1c340u,0x1e8c0u,0x1eb68u,0x52a0u,0x1c350u,0x100000u,0x1eb6au,0x5330u,0x1c360u,0x100000u,0x1eb6cu,0x53b0u,0x1c370u,0x100000u,0x1eb6eu,0x5440u,0x1c380u,0x1e8c0u,0x1eb70u,0x54c0u,0x1c390u,0x100000u,0x1eb72u,0x5550u,0x1c3a0u,0x100000u,0x1eb74u,0x55d0u,0x1c3b0u,0x100000u,0x1eb76u,0x5660u,0x1c3c0u,0x1e8c0u,0x1eb78u,0x56e0u,0x1c3d0u,0x100000u,0x1eb7au,0x5770u,0x1c3e0u,0x100000u,0x1eb7cu,0x57f0u,0x1c3f0u,0x100000u,0x1eb7eu,0x5010u,0x1c400u,0x1e8f0u,0x1eb60u,0x5090u,0x1c410u,0x100000u,0x1eb62u,0x5100u,0x1c420u,0x100000u,0x1eb64u,0x5180u,0x1c430u,0x100000u,0x1eb66u,0x5230u,0x1c440u,0x1e8f0u,0x1eb68u,0x52b0u,0x1c450u,0x100000u,0x1eb6au,0x5320u,0x1c460u,0x100000u,0x1eb6cu,0x53a0u,0x1c470u,0x100000u,0x1eb6eu,0x5450u,0x1c480u,0x1e8f0u,0x1eb70u,0x54d0u,0x1c490u,0x100000u,0x1eb72u,0x5540u,0x1c4a0u,0x100000u,0x1eb74u,0x55c0u,0x1c4b0u,0x100000u,0x1eb76u,0x5670u,0x1c4c0u,0x1e8f0u,0x1eb78u,0x56f0u,0x1c4d0u,0x100000u,0x1eb7au,0x5760u,0x1c4e0u,0x100000u,0x1eb7cu,0x57e0u,0x1c4f0u,0x100000u,0x1eb7eu,0x5020u,0x1c500u,0x1e920u,0x1eb60u,0x50a0u,0x1c510u,0x100000u,0x1eb62u,0x5130u,0x1c520u,0x100000u,0x1eb64u,0x51b0u,0x1c530u,0x100000u,0x1eb66u,0x5200u,0x1c540u,0x1e920u,0x1eb68u,0x5280u,0x1c550u,0x100000u,0x1eb6au,0x5310u,0x1c560u,0x100000u,0x1eb6cu,0x5390u,0x1c570u,0x100000u,0x1eb6eu,0x5460u,0x1c580u,0x1e920u,0x1eb70u,0x54e0u,0x1c590u,0x100000u,0x1eb72u,0x5570u,0x1c5a0u,0x100000u,0x1eb74u,0x55f0u,0x1c5b0u,0x100000u,0x1eb76u,0x5640u,0x1c5c0u,0x1e920u,0x1eb78u,0x56c0u,0x1c5d0u,0x100000u,0x1eb7au,0x5750u,0x1c5e0u,0x100000u,0x1eb7cu,0x57d0u,0x1c5f0u,0x100000u,0x1eb7eu,0x5030u,0x1c600u,0x1e950u,0x1eb60u,0x50b0u,0x1c610u,0x100000u,0x1eb62u,0x5120u,0x1c620u,0x100000u,0x1eb64u,0x51a0u,0x1c630u,0x100000u,0x1eb66u,0x5210u,0x1c640u,0x1e950u,0x1eb68u,0x5290u,0x1c650u,0x100000u,0x1eb6au,0x5300u,0x1c660u,0x100000u,0x1eb6cu,0x5380u,0x1c670u,0x100000u,0x1eb6eu,0x5470u,0x1c680u,0x1e950u,0x1eb70u,0x54f0u,0x1c690u,0x100000u,0x1eb72u,0x5560u,0x1c6a0u,0x100000u,0x1eb74u,0x55e0u,0x1c6b0u,0x100000u,0x1eb76u,0x5650u,0x1c6c0u,0x1e950u,0x1eb78u,0x56d0u,0x1c6d0u,0x100000u,0x1eb7au,0x5740u,0x1c6e0u,0x100000u,0x1eb7cu,0x57c0u,0x1c6f0u,0x100000u,0x1eb7eu,0x25800u,0x1d380u,0x1d380u,0xf0140u,0x25802u,0x1d382u,0x100000u,0xf0150u,0x25804u,0x1d384u,0x100000u,0xf0160u,0x25806u,0x1d386u,0x100000u,0xf0170u,0x25a00u,0x1d390u,0x1d380u,0xf0100u,0x25a02u,0x1d392u,0x100000u,0xf0110u,0x25a04u,0x1d394u,0x100000u,0xf0120u,0x25a06u,0x1d396u,0x100000u,0xf0130u,0x25c00u,0x1d3a0u,0x1d380u,0xf02c0u,0x25c02u,0x1d3a2u,0x100000u,0xf02d0u,0x25c04u,0x1d3a4u,0x100000u,0xf02e0u,0x25c06u,0x1d3a6u,0x100000u,0xf02f0u,0x25e00u,0x1d3b0u,0x1d380u,0xf0280u,0x25e02u,0x1d3b2u,0x100000u,0xf0290u,0x25e04u,0x1d3b4u,0x100000u,0xf02a0u,0x25e06u,0x1d3b6u,0x100000u,0xf02b0u,0x25808u,0x1d388u,0x1d390u,0x11f850u,0x2580au,0x1d38au,0x100000u,0x11f840u,0x2580cu,0x1d38cu,0x100000u,0x11f870u,0x2580eu,0x1d38eu,0x100000u,0x11f860u,0x25a08u,0x1d398u,0x1d390u,0x11f810u,0x25a0au,0x1d39au,0x100000u,0x11f800u,0x25a0cu,0x1d39cu,0x100000u,0x11f830u,0x25a0eu,0x1d39eu,0x100000u,0x11f820u,0x25c08u,0x1d3a8u,0x1d390u,0x11f9d0u,0x25c0au,0x1d3aau,0x100000u,0x11f9c0u,0x25c0cu,0x1d3acu,0x100000u,0x11f9f0u,0x25c0eu,0x1d3aeu,0x100000u,0x11f9e0u,0x25e08u,0x1d3b8u,0x1d390u,0x11f990u,0x25e0au,0x1d3bau,0x100000u,0x11f980u,0x25e0cu,0x1d3bcu,0x100000u,0x11f9b0u,0x25e0eu,0x1d3beu,0x100000u,0x11f9a0u,0x25008u,0x1d3c0u,0x1d3a0u,0x11fb60u,0x2500au,0x1d3c2u,0x100000u,0x11fb70u,0x2500cu,0x1d3c4u,0x100000u,0x11fb40u,0x2500eu,0x1d3c6u,0x100000u,0x11fb50u,0x25208u,0x1d3d0u,0x1d3a0u,0x11fb20u,0x2520au,0x1d3d2u,0x100000u,0x11fb30u,0x2520cu,0x1d3d4u,0x100000u,0x11fb00u,0x2520eu,0x1d3d6u,0x100000u,0x11fb10u,0x25408u,0x1d3e0u,0x1d3a0u,0x11fce0u,0x2540au,0x1d3e2u,0x100000u,0x11fcf0u,0x2540cu,0x1d3e4u,0x100000u,0x11fcc0u,0x2540eu,0x1d3e6u,0x100000u,0x11fcd0u,0x25608u,0x1d3f0u,0x1d3a0u,0x11fca0u,0x2560au,0x1d3f2u,0x100000u,0x11fcb0u,0x2560cu,0x1d3f4u,0x100000u,0x11fc80u,0x2560eu,0x1d3f6u,0x100000u,0x11fc90u,0x100000u,0x1d3c8u,0x1d3b0u,0x11fe70u,0x100000u,0x1d3cau,0x100000u,0x11fe60u,0x100000u,0x1d3ccu,0x100000u,0x11fe50u,0x100000u,0x1d3ceu,0x100000u,0x11fe40u,0x100000u,0x1d3d8u,0x1d3b0u,0x11fe30u,0x100000u,0x1d3dau,0x100000u,0x11fe20u,0x100000u,0x1d3dcu,0x100000u,0x11fe10u,0x100000u,0x1d3deu,0x100000u,0x11fe00u,0x100000u,0x1d3e8u,0x1d3b0u,0x11fff0u,0x100000u,0x1d3eau,0x100000u,0x11ffe0u,0x100000u,0x1d3ecu,0x100000u,0x11ffd0u,0x100000u,0x1d3eeu,0x100000u,0x11ffc0u,0x100000u,0x1d3f8u,0x1d3b0u,0x11ffb0u,0x100000u,0x1d3fau,0x100000u,0x11ffa0u,0x100000u,0x1d3fcu,0x100000u,0x11ff90u,0x100000u,0x1d3feu,0x100000u,0x11ff80u,0x420u,0x30850u,0x90000u,0xc0420u,0x430u,0x30840u,0x90010u,0xc0430u,0x400u,0x30870u,0x90020u,0xc0400u,0x410u,0x30860u,0x90030u,0xc0410u,0x460u,0x30810u,0x90040u,0xc0460u,0x470u,0x30800u,0x90050u,0xc0470u,0x440u,0x30830u,0x90060u,0xc0440u,0x450u,0x30820u,0x90070u,0xc0450u,0x4b0u,0x308d0u,0x90080u,0xef8b0u,0x4a0u,0x308c0u,0x90090u,0xef8a0u,0x490u,0x308f0u,0x900a0u,0xef890u,0x480u,0x308e0u,0x900b0u,0xef880u,0x4f0u,0x30890u,0x900c0u,0xef8f0u,0x4e0u,0x30880u,0x900d0u,0xef8e0u,0x4d0u,0x308b0u,0x900e0u,0xef8d0u,0x4c0u,0x308a0u,0x900f0u,0xef8c0u,0x530u,0x5fd60u,0x90100u,0xef930u,0x520u,0x5fd70u,0x90110u,0xef920u,0x510u,0x5fd40u,0x90120u,0xef910u,0x500u,0x5fd50u,0x90130u,0xef900u,0x570u,0x5fd20u,0x90140u,0xef970u,0x560u,0x5fd30u,0x90150u,0xef960u,0x550u,0x5fd00u,0x90160u,0xef950u,0x540u,0x5fd10u,0x90170u,0xef940u,0x5b0u,0x5fde0u,0x90190u,0xef9b0u,0x5a0u,0x5fdf0u,0x90180u,0xef9a0u,0x590u,0x5fdc0u,0x901b0u,0xef990u,0x580u,0x5fdd0u,0x901a0u,0xef980u,0x5f0u,0x5fda0u,0x901d0u,0xef9f0u,0x5e0u,0x5fdb0u,0x901c0u,0xef9e0u,0x5d0u,0x5fd80u,0x901f0u,0xef9d0u,0x5c0u,0x5fd90u,0x901e0u,0xef9c0u,0x640u,0x5fe60u,0x90210u,0xefa40u,0x650u,0x5fe70u,0x90200u,0xefa50u,0x660u,0x5fe40u,0x90230u,0xefa60u,0x670u,0x5fe50u,0x90220u,0xefa70u,0x600u,0x5fe20u,0x90250u,0xefa00u,0x610u,0x5fe30u,0x90240u,0xefa10u,0x620u,0x5fe00u,0x90270u,0xefa20u,0x630u,0x5fe10u,0x90260u,0xefa30u,0x6c0u,0x5fef0u,0x90290u,0xefac0u,0x6d0u,0x5fee0u,0x90280u,0xefad0u,0x6e0u,0x5fed0u,0x902b0u,0xefae0u,0x6f0u,0x5fec0u,0x902a0u,0xefaf0u,0x680u,0x5feb0u,0x902d0u,0xefa80u,0x690u,0x5fea0u,0x902c0u,0xefa90u,0x6a0u,0x5fe90u,0x902f0u,0xefaa0u,0x6b0u,0x5fe80u,0x902e0u,0xefab0u,0x740u,0x5ff70u,0x90320u,0xefb40u,0x750u,0x5ff60u,0x90330u,0xefb50u,0x760u,0x5ff50u,0x90300u,0xefb60u,0x770u,0x5ff40u,0x90310u,0xefb70u,0x700u,0x5ff30u,0x90360u,0xefb00u,0x710u,0x5ff20u,0x90370u,0xefb10u,0x720u,0x5ff10u,0x90340u,0xefb20u,0x730u,0x5ff00u,0x90350u,0xefb30u,0x7d0u,0x5fff0u,0x903a0u,0xefbd0u,0x7c0u,0x5ffe0u,0x903b0u,0xefbc0u,0x7f0u,0x5ffd0u,0x90380u,0xefbf0u,0x7e0u,0x5ffc0u,0x90390u,0xefbe0u,0x790u,0x5ffb0u,0x903e0u,0xefb90u,0x780u,0x5ffa0u,0x903f0u,0xefb80u,0x7b0u,0x5ff90u,0x903c0u,0xefbb0u,0x7a0u,0x5ff80u,0x903d0u,0xefba0u,0x0u,0x11fc50u,0x0u,0x7230cu,0x0u,0x11fc40u,0x0u,0x7230cu,0x0u,0x11fc70u,0x0u,0x7230cu,0x0u,0x11fc60u,0x0u,0x7230cu,0x0u,0x11fc10u,0x0u,0x7230cu,0x0u,0x11fc00u,0x0u,0x7230cu,0x0u,0x11fc30u,0x0u,0x7230cu,0x0u,0x11fc20u,0x0u,0x7230cu,0x0u,0x11fcd0u,0x0u,0x5232cu,0x0u,0x11fcc0u,0x0u,0x5232cu,0x0u,0x11fcf0u,0x0u,0x5232cu,0x0u,0x11fce0u,0x0u,0x5232cu,0x0u,0x11fc90u,0x0u,0x5232cu,0x0u,0x11fc80u,0x0u,0x5232cu,0x0u,0x11fcb0u,0x0u,0x5232cu,0x0u,0x11fca0u,0x0u,0x5232cu,0x0u,0x11fd60u,0x0u,0xd0124u,0x0u,0x11fd70u,0x0u,0xd0124u,0x0u,0x11fd40u,0x0u,0xd0124u,0x0u,0x11fd50u,0x0u,0xd0124u,0x0u,0x11fd20u,0x0u,0xd0124u,0x0u,0x11fd30u,0x0u,0xd0124u,0x0u,0x11fd00u,0x0u,0xd0124u,0x0u,0x11fd10u,0x0u,0xd0124u,0x0u,0x11fde0u,0x0u,0xd81a6u,0x0u,0x11fdf0u,0x0u,0xd81a6u,0x0u,0x11fdc0u,0x0u,0xd81a6u,0x0u,0x11fdd0u,0x0u,0xd81a6u,0x0u,0x11fda0u,0x0u,0xd81a6u,0x0u,0x11fdb0u,0x0u,0xd81a6u,0x0u,0x11fd80u,0x0u,0xd81a6u,0x0u,0x11fd90u,0x0u,0xd81a6u,0x0u,0x11fe60u,0x0u,0xd8996u,0x0u,0x11fe70u,0x0u,0xd8996u,0x0u,0x11fe40u,0x0u,0xd8996u,0x0u,0x11fe50u,0x0u,0xd8996u,0x0u,0x11fe20u,0x0u,0xd8996u,0x0u,0x11fe30u,0x0u,0xd8996u,0x0u,0x11fe00u,0x0u,0xd8996u,0x0u,0x11fe10u,0x0u,0xd8996u,0x0u,0x11fef0u,0x0u,0xd8996u,0x0u,0x11fee0u,0x0u,0xd8996u,0x0u,0x11fed0u,0x0u,0xd8996u,0x0u,0x11fec0u,0x0u,0xd8996u,0x0u,0x11feb0u,0x0u,0xd8996u,0x0u,0x11fea0u,0x0u,0xd8996u,0x0u,0x11fe90u,0x0u,0xd8996u,0x0u,0x11fe80u,0x0u,0xd8996u,0x0u,0x11ff70u,0x0u,0xd8b1cu,0x0u,0x11ff60u,0x0u,0xd8b1cu,0x0u,0x11ff50u,0x0u,0xd8b1cu,0x0u,0x11ff40u,0x0u,0xd8b1cu,0x0u,0x11ff30u,0x0u,0xd8b1cu,0x0u,0x11ff20u,0x0u,0xd8b1cu,0x0u,0x11ff10u,0x0u,0xd8b1cu,0x0u,0x11ff00u,0x0u,0xd8b1cu,0x0u,0x11fff0u,0x0u,0xd8b1cu,0x0u,0x11ffe0u,0x0u,0xd8b1cu,0x0u,0x11ffd0u,0x0u,0xd8b1cu,0x0u,0x11ffc0u,0x0u,0xd8b1cu,0x0u,0x11ffb0u,0x0u,0xd8b1cu,0x0u,0x11ffa0u,0x0u,0xd8b1cu,0x0u,0x11ff90u,0x0u,0xd8b1cu,0x0u,0x11ff80u,0x0u,0xd8b1cu,0x5840u,0x5860u,0x6800u,0x5840u,0x5848u,0x5868u,0x6808u,0x5850u,0x5850u,0x5870u,0x6810u,0x5860u,0x5858u,0x5878u,0x6818u,0x5870u,0x58c0u,0x58e0u,0x6880u,0x58c0u,0x58c8u,0x58e8u,0x6888u,0x58d0u,0x58d0u,0x58f0u,0x6890u,0x58e0u,0x58d8u,0x58f8u,0x6898u,0x58f0u,0x5960u,0x5940u,0x6920u,0x5960u,0x5968u,0x5948u,0x6928u,0x5970u,0x5970u,0x5950u,0x6930u,0x5940u,0x5978u,0x5958u,0x6938u,0x5950u,0x59e0u,0x59c0u,0x69a0u,0x59e0u,0x59e8u,0x59c8u,0x69a8u,0x59f0u,0x59f0u,0x59d0u,0x69b0u,0x59c0u,0x59f8u,0x59d8u,0x69b8u,0x59d0u,0x5c00u,0x5c20u,0x6c40u,0x5a40u,0x5c08u,0x5c28u,0x6c48u,0x5a50u,0x5c10u,0x5c30u,0x6c50u,0x5a60u,0x5c18u,0x5c38u,0x6c58u,0x5a70u,0x5c80u,0x5ca0u,0x6cc0u,0x5ac0u,0x5c88u,0x5ca8u,0x6cc8u,0x5ad0u,0x5c90u,0x5cb0u,0x6cd0u,0x5ae0u,0x5c98u,0x5cb8u,0x6cd8u,0x5af0u,0x5d20u,0x5d00u,0x6d60u,0x5b60u,0x5d28u,0x5d08u,0x6d68u,0x5b70u,0x5d30u,0x5d10u,0x6d70u,0x5b40u,0x5d38u,0x5d18u,0x6d78u,0x5b50u,0x5da0u,0x5d80u,0x6de0u,0x5be0u,0x5da8u,0x5d88u,0x6de8u,0x5bf0u,0x5db0u,0x5d90u,0x6df0u,0x5bc0u,0x5db8u,0x5d98u,0x6df8u,0x5bd0u,0x6040u,0x6060u,0x7000u,0x5c00u,0x6048u,0x6068u,0x7008u,0x5c10u,0x6050u,0x6070u,0x7010u,0x5c20u,0x6058u,0x6078u,0x7018u,0x5c30u,0x60c0u,0x60e0u,0x7080u,0x5c80u,0x60c8u,0x60e8u,0x7088u,0x5c90u,0x60d0u,0x60f0u,0x7090u,0x5ca0u,0x60d8u,0x60f8u,0x7098u,0x5cb0u,0x6160u,0x6140u,0x7120u,0x5d20u,0x6168u,0x6148u,0x7128u,0x5d30u,0x6170u,0x6150u,0x7130u,0x5d00u,0x6178u,0x6158u,0x7138u,0x5d10u,0x61e0u,0x61c0u,0x71a0u,0x5da0u,0x61e8u,0x61c8u,0x71a8u,0x5db0u,0x61f0u,0x61d0u,0x71b0u,0x5d80u,0x61f8u,0x61d8u,0x71b8u,0x5d90u,0x6400u,0x6420u,0x7440u,0x5e00u,0x6408u,0x6428u,0x7448u,0x5e10u,0x6410u,0x6430u,0x7450u,0x5e20u,0x6418u,0x6438u,0x7458u,0x5e30u,0x6480u,0x64a0u,0x74c0u,0x5e80u,0x6488u,0x64a8u,0x74c8u,0x5e90u,0x6490u,0x64b0u,0x74d0u,0x5ea0u,0x6498u,0x64b8u,0x74d8u,0x5eb0u,0x6520u,0x6500u,0x7560u,0x5f20u,0x6528u,0x6508u,0x7568u,0x5f30u,0x6530u,0x6510u,0x7570u,0x5f00u,0x6538u,0x6518u,0x7578u,0x5f10u,0x65a0u,0x6580u,0x75e0u,0x5fa0u,0x65a8u,0x6588u,0x75e8u,0x5fb0u,0x65b0u,0x6590u,0x75f0u,0x5f80u,0x65b8u,0x6598u,0x75f8u,0x5f90u,0x6800u,0x1e980u,0x1ebc0u,0x1800u,0x6808u,0x1ee10u,0x1f050u,0x1810u,0x6810u,0x1f2a0u,0x1f4e0u,0x1820u,0x6818u,0x1f730u,0x1f970u,0x1830u,0x6880u,0x1fbc0u,0x1fe00u,0x1840u,0x6888u,0x20050u,0x20290u,0x1850u,0x6890u,0x204e0u,0x20720u,0x1860u,0x6898u,0x20970u,0x20bb0u,0x1870u,0x6920u,0x20e00u,0x21040u,0x1980u,0x6928u,0x21290u,0x214d0u,0x1990u,0x6930u,0x21720u,0x21960u,0x19a0u,0x6938u,0x21bb0u,0x21df0u,0x19b0u,0x69a0u,0x22040u,0x22280u,0x19c0u,0x69a8u,0x224d0u,0x22710u,0x19d0u,0x69b0u,0x22960u,0x22ba0u,0x19e0u,0x69b8u,0x22df0u,0x23030u,0x19f0u,0x6a00u,0x1e9b0u,0x1ebc8u,0x1b20u,0x6a08u,0x1ee40u,0x1f058u,0x1b30u,0x6a10u,0x1f2d0u,0x1f4e8u,0x1b00u,0x6a18u,0x1f760u,0x1f978u,0x1b10u,0x6a80u,0x1fbf0u,0x1fe08u,0x1b60u,0x6a88u,0x20080u,0x20298u,0x1b70u,0x6a90u,0x20510u,0x20728u,0x1b40u,0x6a98u,0x209a0u,0x20bb8u,0x1b50u,0x6b20u,0x20e30u,0x21048u,0x1ca0u,0x6b28u,0x212c0u,0x214d8u,0x1cb0u,0x6b30u,0x21750u,0x21968u,0x1c80u,0x6b38u,0x21be0u,0x21df8u,0x1c90u,0x6ba0u,0x22070u,0x22288u,0x1ce0u,0x6ba8u,0x22500u,0x22718u,0x1cf0u,0x6bb0u,0x22990u,0x22ba8u,0x1cc0u,0x6bb8u,0x22e20u,0x23038u,0x1cd0u,0x6c40u,0x1e9e0u,0x1ebd0u,0x1e00u,0x6c48u,0x1ee70u,0x1f060u,0x1e10u,0x6c50u,0x1f300u,0x1f4f0u,0x1e20u,0x6c58u,0x1f790u,0x1f980u,0x1e30u,0x6cc0u,0x1fc20u,0x1fe10u,0x1e40u,0x6cc8u,0x200b0u,0x202a0u,0x1e50u,0x6cd0u,0x20540u,0x20730u,0x1e60u,0x6cd8u,0x209d0u,0x20bc0u,0x1e70u,0x6d60u,0x20e60u,0x21050u,0x1f80u,0x6d68u,0x212f0u,0x214e0u,0x1f90u,0x6d70u,0x21780u,0x21970u,0x1fa0u,0x6d78u,0x21c10u,0x21e00u,0x1fb0u,0x6de0u,0x220a0u,0x22290u,0x1fc0u,0x6de8u,0x22530u,0x22720u,0x1fd0u,0x6df0u,0x229c0u,0x22bb0u,0x1fe0u,0x6df8u,0x22e50u,0x23040u,0x1ff0u,0x6e40u,0x1ea10u,0x1ebd8u,0x2120u,0x6e48u,0x1eea0u,0x1f068u,0x2130u,0x6e50u,0x1f330u,0x1f4f8u,0x2100u,0x6e58u,0x1f7c0u,0x1f988u,0x2110u,0x6ec0u,0x1fc50u,0x1fe18u,0x2160u,0x6ec8u,0x200e0u,0x202a8u,0x2170u,0x6ed0u,0x20570u,0x20738u,0x2140u,0x6ed8u,0x20a00u,0x20bc8u,0x2150u,0x6f60u,0x20e90u,0x21058u,0x22a0u,0x6f68u,0x21320u,0x214e8u,0x22b0u,0x6f70u,0x217b0u,0x21978u,0x2280u,0x6f78u,0x21c40u,0x21e08u,0x2290u,0x6fe0u,0x220d0u,0x22298u,0x22e0u,0x6fe8u,0x22560u,0x22728u,0x22f0u,0x6ff0u,0x229f0u,0x22bb8u,0x22c0u,0x6ff8u,0x22e80u,0x23048u,0x22d0u,0x0u,0x40u,0x9000u,0xb400u,0x80u,0xc0u,0x9010u,0xb410u,0x110u,0x150u,0x9020u,0xb420u,0x190u,0x1d0u,0x9030u,0xb430u,0x220u,0x260u,0x9040u,0xb440u,0x2a0u,0x2e0u,0x9050u,0xb450u,0x330u,0x370u,0x9060u,0xb460u,0x3b0u,0x3f0u,0x9070u,0xb470u,0x440u,0x400u,0x9080u,0xb480u,0x4c0u,0x480u,0x9090u,0xb490u,0x550u,0x510u,0x90a0u,0xb4a0u,0x5d0u,0x590u,0x90b0u,0xb4b0u,0x660u,0x620u,0x90c0u,0xb4c0u,0x6e0u,0x6a0u,0x90d0u,0xb4d0u,0x770u,0x730u,0x90e0u,0xb4e0u,0x7f0u,0x7b0u,0x90f0u,0xb4f0u,0x10u,0x50u,0x9300u,0xb700u,0x90u,0xd0u,0x9310u,0xb710u,0x100u,0x140u,0x9320u,0xb720u,0x180u,0x1c0u,0x9330u,0xb730u,0x230u,0x270u,0x9340u,0xb740u,0x2b0u,0x2f0u,0x9350u,0xb750u,0x320u,0x360u,0x9360u,0xb760u,0x3a0u,0x3e0u,0x9370u,0xb770u,0x450u,0x410u,0x9380u,0xb780u,0x4d0u,0x490u,0x9390u,0xb790u,0x540u,0x500u,0x93a0u,0xb7a0u,0x5c0u,0x580u,0x93b0u,0xb7b0u,0x670u,0x630u,0x93c0u,0xb7c0u,0x6f0u,0x6b0u,0x93d0u,0xb7d0u,0x760u,0x720u,0x93e0u,0xb7e0u,0x7e0u,0x7a0u,0x93f0u,0xb7f0u,0x20u,0x60u,0x9600u,0xba00u,0xa0u,0xe0u,0x9610u,0xba10u,0x130u,0x170u,0x9620u,0xba20u,0x1b0u,0x1f0u,0x9630u,0xba30u,0x200u,0x240u,0x9640u,0xba40u,0x280u,0x2c0u,0x9650u,0xba50u,0x310u,0x350u,0x9660u,0xba60u,0x390u,0x3d0u,0x9670u,0xba70u,0x460u,0x420u,0x9680u,0xba80u,0x4e0u,0x4a0u,0x9690u,0xba90u,0x570u,0x530u,0x96a0u,0xbaa0u,0x5f0u,0x5b0u,0x96b0u,0xbab0u,0x640u,0x600u,0x96c0u,0xbac0u,0x6c0u,0x680u,0x96d0u,0xbad0u,0x750u,0x710u,0x96e0u,0xbae0u,0x7d0u,0x790u,0x96f0u,0xbaf0u,0x30u,0x70u,0x9900u,0xbd00u,0xb0u,0xf0u,0x9910u,0xbd10u,0x120u,0x160u,0x9920u,0xbd20u,0x1a0u,0x1e0u,0x9930u,0xbd30u,0x210u,0x250u,0x9940u,0xbd40u,0x290u,0x2d0u,0x9950u,0xbd50u,0x300u,0x340u,0x9960u,0xbd60u,0x380u,0x3c0u,0x9970u,0xbd70u,0x470u,0x430u,0x9980u,0xbd80u,0x4f0u,0x4b0u,0x9990u,0xbd90u,0x560u,0x520u,0x99a0u,0xbda0u,0x5e0u,0x5a0u,0x99b0u,0xbdb0u,0x650u,0x610u,0x99c0u,0xbdc0u,0x6d0u,0x690u,0x99d0u,0xbdd0u,0x740u,0x700u,0x99e0u,0xbde0u,0x7c0u,0x780u,0x99f0u,0xbdf0u,0xc000u,0x120c0u,0x12100u,0x12800u,0xc010u,0x12250u,0x12290u,0x12810u,0xc020u,0x123e0u,0x12420u,0x12820u,0xc030u,0x12570u,0x125b0u,0x12830u,0xc040u,0x12680u,0x12740u,0x12840u,0xc050u,0x12810u,0x128d0u,0x12850u,0xc060u,0x129a0u,0x12a60u,0x12860u,0xc070u,0x12b30u,0x12bf0u,0x12870u,0xc080u,0x12cc0u,0x12d00u,0x12880u,0xc090u,0x12e50u,0x12e90u,0x12890u,0xc0a0u,0x12fe0u,0x13020u,0x128a0u,0xc0b0u,0x13170u,0x131b0u,0x128b0u,0xc0c0u,0x13280u,0x13340u,0x128c0u,0xc0d0u,0x13410u,0x134d0u,0x128d0u,0xc0e0u,0x135a0u,0x13660u,0x128e0u,0xc0f0u,0x13730u,0x137f0u,0x128f0u,0xc300u,0x120d0u,0x12108u,0x12900u,0xc310u,0x12240u,0x12298u,0x12910u,0xc320u,0x123f0u,0x12428u,0x12920u,0xc330u,0x12560u,0x125b8u,0x12930u,0xc340u,0x12690u,0x12748u,0x12940u,0xc350u,0x12800u,0x128d8u,0x12950u,0xc360u,0x129b0u,0x12a68u,0x12960u,0xc370u,0x12b20u,0x12bf8u,0x12970u,0xc380u,0x12cd0u,0x12d08u,0x12980u,0xc390u,0x12e40u,0x12e98u,0x12990u,0xc3a0u,0x12ff0u,0x13028u,0x129a0u,0xc3b0u,0x13160u,0x131b8u,0x129b0u,0xc3c0u,0x13290u,0x13348u,0x129c0u,0xc3d0u,0x13400u,0x134d8u,0x129d0u,0xc3e0u,0x135b0u,0x13668u,0x129e0u,0xc3f0u,0x13720u,0x137f8u,0x129f0u,0xc600u,0x120e0u,0x12110u,0x12a00u,0xc610u,0x12270u,0x12280u,0x12a10u,0xc620u,0x123c0u,0x12430u,0x12a20u,0xc630u,0x12550u,0x125a0u,0x12a30u,0xc640u,0x126a0u,0x12750u,0x12a40u,0xc650u,0x12830u,0x128c0u,0x12a50u,0xc660u,0x12980u,0x12a70u,0x12a60u,0xc670u,0x12b10u,0x12be0u,0x12a70u,0xc680u,0x12ce0u,0x12d10u,0x12a80u,0xc690u,0x12e70u,0x12e80u,0x12a90u,0xc6a0u,0x12fc0u,0x13030u,0x12aa0u,0xc6b0u,0x13150u,0x131a0u,0x12ab0u,0xc6c0u,0x132a0u,0x13350u,0x12ac0u,0xc6d0u,0x13430u,0x134c0u,0x12ad0u,0xc6e0u,0x13580u,0x13670u,0x12ae0u,0xc6f0u,0x13710u,0x137e0u,0x12af0u,0xc900u,0x120f0u,0x12118u,0x12b00u,0xc910u,0x12260u,0x12288u,0x12b10u,0xc920u,0x123d0u,0x12438u,0x12b20u,0xc930u,0x12540u,0x125a8u,0x12b30u,0xc940u,0x126b0u,0x12758u,0x12b40u,0xc950u,0x12820u,0x128c8u,0x12b50u,0xc960u,0x12990u,0x12a78u,0x12b60u,0xc970u,0x12b00u,0x12be8u,0x12b70u,0xc980u,0x12cf0u,0x12d18u,0x12b80u,0xc990u,0x12e60u,0x12e88u,0x12b90u,0xc9a0u,0x12fd0u,0x13038u,0x12ba0u,0xc9b0u,0x13140u,0x131a8u,0x12bb0u,0xc9c0u,0x132b0u,0x13358u,0x12bc0u,0xc9d0u,0x13420u,0x134c8u,0x12bd0u,0xc9e0u,0x13590u,0x13678u,0x12be0u,0xc9f0u,0x13700u,0x137e8u,0x12bf0u,0x7800u,0x1c300u,0x1e980u,0x1ebc0u,0x7880u,0x1c310u,0x100000u,0x1ebc2u,0x7910u,0x1c320u,0x100000u,0x1ebc4u,0x7990u,0x1c330u,0x100000u,0x1ebc6u,0x7a20u,0x1c340u,0x1e980u,0x1ebc8u,0x7aa0u,0x1c350u,0x100000u,0x1ebcau,0x7b30u,0x1c360u,0x100000u,0x1ebccu,0x7bb0u,0x1c370u,0x100000u,0x1ebceu,0x7c40u,0x1c380u,0x1e980u,0x1ebd0u,0x7cc0u,0x1c390u,0x100000u,0x1ebd2u,0x7d50u,0x1c3a0u,0x100000u,0x1ebd4u,0x7dd0u,0x1c3b0u,0x100000u,0x1ebd6u,0x7e60u,0x1c3c0u,0x1e980u,0x1ebd8u,0x7ee0u,0x1c3d0u,0x100000u,0x1ebdau,0x7f70u,0x1c3e0u,0x100000u,0x1ebdcu,0x7ff0u,0x1c3f0u,0x100000u,0x1ebdeu,0x7810u,0x1c400u,0x1e9b0u,0x1ebc0u,0x7890u,0x1c410u,0x100000u,0x1ebc2u,0x7900u,0x1c420u,0x100000u,0x1ebc4u,0x7980u,0x1c430u,0x100000u,0x1ebc6u,0x7a30u,0x1c440u,0x1e9b0u,0x1ebc8u,0x7ab0u,0x1c450u,0x100000u,0x1ebcau,0x7b20u,0x1c460u,0x100000u,0x1ebccu,0x7ba0u,0x1c470u,0x100000u,0x1ebceu,0x7c50u,0x1c480u,0x1e9b0u,0x1ebd0u,0x7cd0u,0x1c490u,0x100000u,0x1ebd2u,0x7d40u,0x1c4a0u,0x100000u,0x1ebd4u,0x7dc0u,0x1c4b0u,0x100000u,0x1ebd6u,0x7e70u,0x1c4c0u,0x1e9b0u,0x1ebd8u,0x7ef0u,0x1c4d0u,0x100000u,0x1ebdau,0x7f60u,0x1c4e0u,0x100000u,0x1ebdcu,0x7fe0u,0x1c4f0u,0x100000u,0x1ebdeu,0x7820u,0x1c500u,0x1e9e0u,0x1ebc0u,0x78a0u,0x1c510u,0x100000u,0x1ebc2u,0x7930u,0x1c520u,0x100000u,0x1ebc4u,0x79b0u,0x1c530u,0x100000u,0x1ebc6u,0x7a00u,0x1c540u,0x1e9e0u,0x1ebc8u,0x7a80u,0x1c550u,0x100000u,0x1ebcau,0x7b10u,0x1c560u,0x100000u,0x1ebccu,0x7b90u,0x1c570u,0x100000u,0x1ebceu,0x7c60u,0x1c580u,0x1e9e0u,0x1ebd0u,0x7ce0u,0x1c590u,0x100000u,0x1ebd2u,0x7d70u,0x1c5a0u,0x100000u,0x1ebd4u,0x7df0u,0x1c5b0u,0x100000u,0x1ebd6u,0x7e40u,0x1c5c0u,0x1e9e0u,0x1ebd8u,0x7ec0u,0x1c5d0u,0x100000u,0x1ebdau,0x7f50u,0x1c5e0u,0x100000u,0x1ebdcu,0x7fd0u,0x1c5f0u,0x100000u,0x1ebdeu,0x7830u,0x1c600u,0x1ea10u,0x1ebc0u,0x78b0u,0x1c610u,0x100000u,0x1ebc2u,0x7920u,0x1c620u,0x100000u,0x1ebc4u,0x79a0u,0x1c630u,0x100000u,0x1ebc6u,0x7a10u,0x1c640u,0x1ea10u,0x1ebc8u,0x7a90u,0x1c650u,0x100000u,0x1ebcau,0x7b00u,0x1c660u,0x100000u,0x1ebccu,0x7b80u,0x1c670u,0x100000u,0x1ebceu,0x7c70u,0x1c680u,0x1ea10u,0x1ebd0u,0x7cf0u,0x1c690u,0x100000u,0x1ebd2u,0x7d60u,0x1c6a0u,0x100000u,0x1ebd4u,0x7de0u,0x1c6b0u,0x100000u,0x1ebd6u,0x7e50u,0x1c6c0u,0x1ea10u,0x1ebd8u,0x7ed0u,0x1c6d0u,0x100000u,0x1ebdau,0x7f40u,0x1c6e0u,0x100000u,0x1ebdcu,0x7fc0u,0x1c6f0u,0x100000u,0x1ebdeu,0x26000u,0x1d400u,0x1d400u,0x80000000u,0x26002u,0x1d402u,0x100000u,0x80000000u,0x26004u,0x1d404u,0x100000u,0x80000000u,0x26006u,0x1d406u,0x100000u,0x80000000u,0x26200u,0x1d410u,0x1d400u,0x80000000u,0x26202u,0x1d412u,0x100000u,0x80000000u,0x26204u,0x1d414u,0x100000u,0x80000000u,0x26206u,0x1d416u,0x100000u,0x80000000u,0x26400u,0x1d420u,0x1d400u,0x80000000u,0x26402u,0x1d422u,0x100000u,0x80000000u,0x26404u,0x1d424u,0x100000u,0x80000000u,0x26406u,0x1d426u,0x100000u,0x80000000u,0x26600u,0x1d430u,0x1d400u,0x80000000u,0x26602u,0x1d432u,0x100000u,0x80000000u,0x26604u,0x1d434u,0x100000u,0x80000000u,0x26606u,0x1d436u,0x100000u,0x80000000u,0x26008u,0x1d408u,0x1d410u,0x80000000u,0x2600au,0x1d40au,0x100000u,0x80000000u,0x2600cu,0x1d40cu,0x100000u,0x80000000u,0x2600eu,0x1d40eu,0x100000u,0x80000000u,0x26208u,0x1d418u,0x1d410u,0x80000000u,0x2620au,0x1d41au,0x100000u,0x80000000u,0x2620cu,0x1d41cu,0x100000u,0x80000000u,0x2620eu,0x1d41eu,0x100000u,0x80000000u,0x26408u,0x1d428u,0x1d410u,0x80000000u,0x2640au,0x1d42au,0x100000u,0x80000000u,0x2640cu,0x1d42cu,0x100000u,0x80000000u,0x2640eu,0x1d42eu,0x100000u,0x80000000u,0x26608u,0x1d438u,0x1d410u,0x80000000u,0x2660au,0x1d43au,0x100000u,0x80000000u,0x2660cu,0x1d43cu,0x100000u,0x80000000u,0x2660eu,0x1d43eu,0x100000u,0x80000000u,0x26800u,0x1d440u,0x1d420u,0x80000000u,0x26802u,0x1d442u,0x100000u,0x80000000u,0x26804u,0x1d444u,0x100000u,0x80000000u,0x26806u,0x1d446u,0x100000u,0x80000000u,0x26a00u,0x1d450u,0x1d420u,0x80000000u,0x26a02u,0x1d452u,0x100000u,0x80000000u,0x26a04u,0x1d454u,0x100000u,0x80000000u,0x26a06u,0x1d456u,0x100000u,0x80000000u,0x26c00u,0x1d460u,0x1d420u,0x80000000u,0x26c02u,0x1d462u,0x100000u,0x80000000u,0x26c04u,0x1d464u,0x100000u,0x80000000u,0x26c06u,0x1d466u,0x100000u,0x80000000u,0x26e00u,0x1d470u,0x1d420u,0x80000000u,0x26e02u,0x1d472u,0x100000u,0x80000000u,0x26e04u,0x1d474u,0x100000u,0x80000000u,0x26e06u,0x1d476u,0x100000u,0x80000000u,0x100000u,0x1d448u,0x1d430u,0x80000000u,0x100000u,0x1d44au,0x100000u,0x80000000u,0x100000u,0x1d44cu,0x100000u,0x80000000u,0x100000u,0x1d44eu,0x100000u,0x80000000u,0x100000u,0x1d458u,0x1d430u,0x80000000u,0x100000u,0x1d45au,0x100000u,0x80000000u,0x100000u,0x1d45cu,0x100000u,0x80000000u,0x100000u,0x1d45eu,0x100000u,0x80000000u,0x100000u,0x1d468u,0x1d430u,0x80000000u,0x100000u,0x1d46au,0x100000u,0x80000000u,0x100000u,0x1d46cu,0x100000u,0x80000000u,0x100000u,0x1d46eu,0x100000u,0x80000000u,0x100000u,0x1d478u,0x1d430u,0x80000000u,0x100000u,0x1d47au,0x100000u,0x80000000u,0x100000u,0x1d47cu,0x100000u,0x80000000u,0x100000u,0x1d47eu,0x100000u,0x80000000u,0x850u,0x60000u,0x90420u,0xefc50u,0x840u,0x60010u,0x90430u,0xefc40u,0x870u,0x60020u,0x90400u,0xefc70u,0x860u,0x60030u,0x90410u,0xefc60u,0x810u,0x60040u,0x90460u,0xefc10u,0x800u,0x60050u,0x90470u,0xefc00u,0x830u,0x60060u,0x90440u,0xefc30u,0x820u,0x60070u,0x90450u,0xefc20u,0x8d0u,0x60080u,0x904b0u,0xefcd0u,0x8c0u,0x60090u,0x904a0u,0xefcc0u,0x8f0u,0x600a0u,0x90490u,0xefcf0u,0x8e0u,0x600b0u,0x90480u,0xefce0u,0x890u,0x600c0u,0x904f0u,0xefc90u,0x880u,0x600d0u,0x904e0u,0xefc80u,0x8b0u,0x600e0u,0x904d0u,0xefcb0u,0x8a0u,0x600f0u,0x904c0u,0xefca0u,0x960u,0x60100u,0x90530u,0xefd60u,0x970u,0x60110u,0x90520u,0xefd70u,0x940u,0x60120u,0x90510u,0xefd40u,0x950u,0x60130u,0x90500u,0xefd50u,0x920u,0x60140u,0x90570u,0xefd20u,0x930u,0x60150u,0x90560u,0xefd30u,0x900u,0x60160u,0x90550u,0xefd00u,0x910u,0x60170u,0x90540u,0xefd10u,0x9e0u,0x60190u,0x905b0u,0xefde0u,0x9f0u,0x60180u,0x905a0u,0xefdf0u,0x9c0u,0x601b0u,0x90590u,0xefdc0u,0x9d0u,0x601a0u,0x90580u,0xefdd0u,0x9a0u,0x601d0u,0x905f0u,0xefda0u,0x9b0u,0x601c0u,0x905e0u,0xefdb0u,0x980u,0x601f0u,0x905d0u,0xefd80u,0x990u,0x601e0u,0x905c0u,0xefd90u,0xa60u,0x60210u,0xbfa40u,0xefe60u,0xa70u,0x60200u,0xbfa50u,0xefe70u,0xa40u,0x60230u,0xbfa60u,0xefe40u,0xa50u,0x60220u,0xbfa70u,0xefe50u,0xa20u,0x60250u,0xbfa00u,0xefe20u,0xa30u,0x60240u,0xbfa10u,0xefe30u,0xa00u,0x60270u,0xbfa20u,0xefe00u,0xa10u,0x60260u,0xbfa30u,0xefe10u,0x2fef0u,0x60290u,0xbfac0u,0xefef0u,0x2fee0u,0x60280u,0xbfad0u,0xefee0u,0x2fed0u,0x602b0u,0xbfae0u,0xefed0u,0x2fec0u,0x602a0u,0xbfaf0u,0xefec0u,0x2feb0u,0x602d0u,0xbfa80u,0xefeb0u,0x2fea0u,0x602c0u,0xbfa90u,0xefea0u,0x2fe90u,0x602f0u,0xbfaa0u,0xefe90u,0x2fe80u,0x602e0u,0xbfab0u,0xefe80u,0x2ff70u,0x60320u,0xbfb40u,0xeff70u,0x2ff60u,0x60330u,0xbfb50u,0xeff60u,0x2ff50u,0x60300u,0xbfb60u,0xeff50u,0x2ff40u,0x60310u,0xbfb70u,0xeff40u,0x2ff30u,0x60360u,0xbfb00u,0xeff30u,0x2ff20u,0x60370u,0xbfb10u,0xeff20u,0x2ff10u,0x60340u,0xbfb20u,0xeff10u,0x2ff00u,0x60350u,0xbfb30u,0xeff00u,0x2fff0u,0x603a0u,0xbfbd0u,0xefff0u,0x2ffe0u,0x603b0u,0xbfbc0u,0xeffe0u,0x2ffd0u,0x60380u,0xbfbf0u,0xeffd0u,0x2ffc0u,0x60390u,0xbfbe0u,0xeffc0u,0x2ffb0u,0x603e0u,0xbfb90u,0xeffb0u,0x2ffa0u,0x603f0u,0xbfb80u,0xeffa0u,0x2ff90u,0x603c0u,0xbfbb0u,0xeff90u,0x2ff80u,0x603d0u,0xbfba0u,0xeff80u,0x0u,0x80000000u,0x0u,0x18830u,0x0u,0x80000000u,0x0u,0x18830u,0x0u,0x80000000u,0x0u,0x18830u,0x0u,0x80000000u,0x0u,0x18830u,0x0u,0x80000000u,0x0u,0x18830u,0x0u,0x80000000u,0x0u,0x18830u,0x0u,0x80000000u,0x0u,0x18830u,0x0u,0x80000000u,0x0u,0x18830u,0x0u,0x80000000u,0x0u,0x18830u,0x0u,0x80000000u,0x0u,0x18830u,0x0u,0x80000000u,0x0u,0x18830u,0x0u,0x80000000u,0x0u,0x18830u,0x0u,0x80000000u,0x0u,0x18830u,0x0u,0x80000000u,0x0u,0x18830u,0x0u,0x80000000u,0x0u,0x18830u,0x0u,0x80000000u,0x0u,0x18830u,0x0u,0x80000000u,0x0u,0x188b2u,0x0u,0x80000000u,0x0u,0x188b2u,0x0u,0x80000000u,0x0u,0x188b2u,0x0u,0x80000000u,0x0u,0x188b2u,0x0u,0x80000000u,0x0u,0x188b2u,0x0u,0x80000000u,0x0u,0x188b2u,0x0u,0x80000000u,0x0u,0x188b2u,0x0u,0x80000000u,0x0u,0x188b2u,0x0u,0x80000000u,0x0u,0x18892u,0x0u,0x80000000u,0x0u,0x18892u,0x0u,0x80000000u,0x0u,0x18892u,0x0u,0x80000000u,0x0u,0x18892u,0x0u,0x80000000u,0x0u,0x18892u,0x0u,0x80000000u,0x0u,0x18892u,0x0u,0x80000000u,0x0u,0x18892u,0x0u,0x80000000u,0x0u,0x18892u,0x0u,0x80000000u,0x0u,0x1489au,0x0u,0x80000000u,0x0u,0x1489au,0x0u,0x80000000u,0x0u,0x1489au,0x0u,0x80000000u,0x0u,0x1489au,0x0u,0x80000000u,0x0u,0x1489au,0x0u,0x80000000u,0x0u,0x1489au,0x0u,0x80000000u,0x0u,0x1489au,0x0u,0x80000000u,0x0u,0x1489au,0x0u,0x80000000u,0x0u,0x34018u,0x0u,0x80000000u,0x0u,0x34018u,0x0u,0x80000000u,0x0u,0x34018u,0x0u,0x80000000u,0x0u,0x34018u,0x0u,0x80000000u,0x0u,0x34018u,0x0u,0x80000000u,0x0u,0x34018u,0x0u,0x80000000u,0x0u,0x34018u,0x0u,0x80000000u,0x0u,0x34018u,0x0u,0x80000000u,0x0u,0x36018u,0x0u,0x80000000u,0x0u,0x36018u,0x0u,0x80000000u,0x0u,0x36018u,0x0u,0x80000000u,0x0u,0x36018u,0x0u,0x80000000u,0x0u,0x36018u,0x0u,0x80000000u,0x0u,0x36018u,0x0u,0x80000000u,0x0u,0x36018u,0x0u,0x80000000u,0x0u,0x36018u,0x0u,0x80000000u,0x0u,0x36030u,0x0u,0x80000000u,0x0u,0x36030u,0x0u,0x80000000u,0x0u,0x36030u,0x0u,0x80000000u,0x0u,0x36030u,0x0u,0x80000000u,0x0u,0x36030u,0x0u,0x80000000u,0x0u,0x36030u,0x0u,0x80000000u,0x0u,0x36030u,0x0u,0x80000000u,0x0u,0x36030u,0x6840u,0x6860u,0x6820u,0x6820u,0x6848u,0x6868u,0x6828u,0x6830u,0x6850u,0x6870u,0x6830u,0x6840u,0x6858u,0x6878u,0x6838u,0x6850u,0x68c0u,0x68e0u,0x68a0u,0x68a0u,0x68c8u,0x68e8u,0x68a8u,0x68b0u,0x68d0u,0x68f0u,0x68b0u,0x68c0u,0x68d8u,0x68f8u,0x68b8u,0x68d0u,0x6960u,0x6940u,0x6900u,0x6900u,0x6968u,0x6948u,0x6908u,0x6910u,0x6970u,0x6950u,0x6910u,0x6960u,0x6978u,0x6958u,0x6918u,0x6970u,0x69e0u,0x69c0u,0x6980u,0x6980u,0x69e8u,0x69c8u,0x6988u,0x6990u,0x69f0u,0x69d0u,0x6990u,0x69e0u,0x69f8u,0x69d8u,0x6998u,0x69f0u,0x6c00u,0x6c20u,0x6c60u,0x6a20u,0x6c08u,0x6c28u,0x6c68u,0x6a30u,0x6c10u,0x6c30u,0x6c70u,0x6a40u,0x6c18u,0x6c38u,0x6c78u,0x6a50u,0x6c80u,0x6ca0u,0x6ce0u,0x6aa0u,0x6c88u,0x6ca8u,0x6ce8u,0x6ab0u,0x6c90u,0x6cb0u,0x6cf0u,0x6ac0u,0x6c98u,0x6cb8u,0x6cf8u,0x6ad0u,0x6d20u,0x6d00u,0x6d40u,0x6b00u,0x6d28u,0x6d08u,0x6d48u,0x6b10u,0x6d30u,0x6d10u,0x6d50u,0x6b60u,0x6d38u,0x6d18u,0x6d58u,0x6b70u,0x6da0u,0x6d80u,0x6dc0u,0x6b80u,0x6da8u,0x6d88u,0x6dc8u,0x6b90u,0x6db0u,0x6d90u,0x6dd0u,0x6be0u,0x6db8u,0x6d98u,0x6dd8u,0x6bf0u,0x7040u,0x7060u,0x7020u,0x6c60u,0x7048u,0x7068u,0x7028u,0x6c70u,0x7050u,0x7070u,0x7030u,0x6c00u,0x7058u,0x7078u,0x7038u,0x6c10u,0x70c0u,0x70e0u,0x70a0u,0x6ce0u,0x70c8u,0x70e8u,0x70a8u,0x6cf0u,0x70d0u,0x70f0u,0x70b0u,0x6c80u,0x70d8u,0x70f8u,0x70b8u,0x6c90u,0x7160u,0x7140u,0x7100u,0x6d40u,0x7168u,0x7148u,0x7108u,0x6d50u,0x7170u,0x7150u,0x7110u,0x6d20u,0x7178u,0x7158u,0x7118u,0x6d30u,0x71e0u,0x71c0u,0x7180u,0x6dc0u,0x71e8u,0x71c8u,0x7188u,0x6dd0u,0x71f0u,0x71d0u,0x7190u,0x6da0u,0x71f8u,0x71d8u,0x7198u,0x6db0u,0x7400u,0x7420u,0x7460u,0x6e60u,0x7408u,0x7428u,0x7468u,0x6e70u,0x7410u,0x7430u,0x7470u,0x6e00u,0x7418u,0x7438u,0x7478u,0x6e10u,0x7480u,0x74a0u,0x74e0u,0x6ee0u,0x7488u,0x74a8u,0x74e8u,0x6ef0u,0x7490u,0x74b0u,0x74f0u,0x6e80u,0x7498u,0x74b8u,0x74f8u,0x6e90u,0x7520u,0x7500u,0x7540u,0x6f40u,0x7528u,0x7508u,0x7548u,0x6f50u,0x7530u,0x7510u,0x7550u,0x6f20u,0x7538u,0x7518u,0x7558u,0x6f30u,0x75a0u,0x7580u,0x75c0u,0x6fc0u,0x75a8u,0x7588u,0x75c8u,0x6fd0u,0x75b0u,0x7590u,0x75d0u,0x6fa0u,0x75b8u,0x7598u,0x75d8u,0x6fb0u,0x6860u,0x1ea40u,0x1ec20u,0x2440u,0x6868u,0x1eed0u,0x1f0b0u,0x2450u,0x6870u,0x1f360u,0x1f540u,0x2460u,0x6878u,0x1f7f0u,0x1f9d0u,0x2470u,0x68e0u,0x1fc80u,0x1fe60u,0x2400u,0x68e8u,0x20110u,0x202f0u,0x2410u,0x68f0u,0x205a0u,0x20780u,0x2420u,0x68f8u,0x20a30u,0x20c10u,0x2430u,0x6940u,0x20ec0u,0x210a0u,0x25c0u,0x6948u,0x21350u,0x21530u,0x25d0u,0x6950u,0x217e0u,0x219c0u,0x25e0u,0x6958u,0x21c70u,0x21e50u,0x25f0u,0x69c0u,0x22100u,0x222e0u,0x2580u,0x69c8u,0x22590u,0x22770u,0x2590u,0x69d0u,0x22a20u,0x22c00u,0x25a0u,0x69d8u,0x22eb0u,0x23090u,0x25b0u,0x6a60u,0x1ea70u,0x1ec28u,0x2760u,0x6a68u,0x1ef00u,0x1f0b8u,0x2770u,0x6a70u,0x1f390u,0x1f548u,0x2740u,0x6a78u,0x1f820u,0x1f9d8u,0x2750u,0x6ae0u,0x1fcb0u,0x1fe68u,0x2720u,0x6ae8u,0x20140u,0x202f8u,0x2730u,0x6af0u,0x205d0u,0x20788u,0x2700u,0x6af8u,0x20a60u,0x20c18u,0x2710u,0x6b40u,0x20ef0u,0x210a8u,0x28e0u,0x6b48u,0x21380u,0x21538u,0x28f0u,0x6b50u,0x21810u,0x219c8u,0x28c0u,0x6b58u,0x21ca0u,0x21e58u,0x28d0u,0x6bc0u,0x22130u,0x222e8u,0x28a0u,0x6bc8u,0x225c0u,0x22778u,0x28b0u,0x6bd0u,0x22a50u,0x22c08u,0x2880u,0x6bd8u,0x22ee0u,0x23098u,0x2890u,0x6c20u,0x1eaa0u,0x1ec30u,0x2a40u,0x6c28u,0x1ef30u,0x1f0c0u,0x2a50u,0x6c30u,0x1f3c0u,0x1f550u,0x2a60u,0x6c38u,0x1f850u,0x1f9e0u,0x2a70u,0x6ca0u,0x1fce0u,0x1fe70u,0x2a00u,0x6ca8u,0x20170u,0x20300u,0x2a10u,0x6cb0u,0x20600u,0x20790u,0x2a20u,0x6cb8u,0x20a90u,0x20c20u,0x2a30u,0x6d00u,0x20f20u,0x210b0u,0x2bc0u,0x6d08u,0x213b0u,0x21540u,0x2bd0u,0x6d10u,0x21840u,0x219d0u,0x2be0u,0x6d18u,0x21cd0u,0x21e60u,0x2bf0u,0x6d80u,0x22160u,0x222f0u,0x2b80u,0x6d88u,0x225f0u,0x22780u,0x2b90u,0x6d90u,0x22a80u,0x22c10u,0x2ba0u,0x6d98u,0x22f10u,0x230a0u,0x2bb0u,0x6e20u,0x1ead0u,0x1ec38u,0x2d60u,0x6e28u,0x1ef60u,0x1f0c8u,0x2d70u,0x6e30u,0x1f3f0u,0x1f558u,0x2d40u,0x6e38u,0x1f880u,0x1f9e8u,0x2d50u,0x6ea0u,0x1fd10u,0x1fe78u,0x2d20u,0x6ea8u,0x201a0u,0x20308u,0x2d30u,0x6eb0u,0x20630u,0x20798u,0x2d00u,0x6eb8u,0x20ac0u,0x20c28u,0x2d10u,0x6f00u,0x20f50u,0x210b8u,0x2ee0u,0x6f08u,0x213e0u,0x21548u,0x2ef0u,0x6f10u,0x21870u,0x219d8u,0x2ec0u,0x6f18u,0x21d00u,0x21e68u,0x2ed0u,0x6f80u,0x22190u,0x222f8u,0x2ea0u,0x6f88u,0x22620u,0x22788u,0x2eb0u,0x6f90u,0x22ab0u,0x22c18u,0x2e80u,0x6f98u,0x22f40u,0x230a8u,0x2e90u,0x0u,0x40u,0x9000u,0xcc00u,0x80u,0xc0u,0x9010u,0xcc10u,0x110u,0x150u,0x9020u,0xcc20u,0x190u,0x1d0u,0x9030u,0xcc30u,0x220u,0x260u,0x9040u,0xcc40u,0x2a0u,0x2e0u,0x9050u,0xcc50u,0x330u,0x370u,0x9060u,0xcc60u,0x3b0u,0x3f0u,0x9070u,0xcc70u,0x440u,0x400u,0x9080u,0xcc80u,0x4c0u,0x480u,0x9090u,0xcc90u,0x550u,0x510u,0x90a0u,0xcca0u,0x5d0u,0x590u,0x90b0u,0xccb0u,0x660u,0x620u,0x90c0u,0xccc0u,0x6e0u,0x6a0u,0x90d0u,0xccd0u,0x770u,0x730u,0x90e0u,0xcce0u,0x7f0u,0x7b0u,0x90f0u,0xccf0u,0x10u,0x50u,0x9300u,0xcf00u,0x90u,0xd0u,0x9310u,0xcf10u,0x100u,0x140u,0x9320u,0xcf20u,0x180u,0x1c0u,0x9330u,0xcf30u,0x230u,0x270u,0x9340u,0xcf40u,0x2b0u,0x2f0u,0x9350u,0xcf50u,0x320u,0x360u,0x9360u,0xcf60u,0x3a0u,0x3e0u,0x9370u,0xcf70u,0x450u,0x410u,0x9380u,0xcf80u,0x4d0u,0x490u,0x9390u,0xcf90u,0x540u,0x500u,0x93a0u,0xcfa0u,0x5c0u,0x580u,0x93b0u,0xcfb0u,0x670u,0x630u,0x93c0u,0xcfc0u,0x6f0u,0x6b0u,0x93d0u,0xcfd0u,0x760u,0x720u,0x93e0u,0xcfe0u,0x7e0u,0x7a0u,0x93f0u,0xcff0u,0x20u,0x60u,0x9600u,0xd200u,0xa0u,0xe0u,0x9610u,0xd210u,0x130u,0x170u,0x9620u,0xd220u,0x1b0u,0x1f0u,0x9630u,0xd230u,0x200u,0x240u,0x9640u,0xd240u,0x280u,0x2c0u,0x9650u,0xd250u,0x310u,0x350u,0x9660u,0xd260u,0x390u,0x3d0u,0x9670u,0xd270u,0x460u,0x420u,0x9680u,0xd280u,0x4e0u,0x4a0u,0x9690u,0xd290u,0x570u,0x530u,0x96a0u,0xd2a0u,0x5f0u,0x5b0u,0x96b0u,0xd2b0u,0x640u,0x600u,0x96c0u,0xd2c0u,0x6c0u,0x680u,0x96d0u,0xd2d0u,0x750u,0x710u,0x96e0u,0xd2e0u,0x7d0u,0x790u,0x96f0u,0xd2f0u,0x30u,0x70u,0x9900u,0xd500u,0xb0u,0xf0u,0x9910u,0xd510u,0x120u,0x160u,0x9920u,0xd520u,0x1a0u,0x1e0u,0x9930u,0xd530u,0x210u,0x250u,0x9940u,0xd540u,0x290u,0x2d0u,0x9950u,0xd550u,0x300u,0x340u,0x9960u,0xd560u,0x380u,0x3c0u,0x9970u,0xd570u,0x470u,0x430u,0x9980u,0xd580u,0x4f0u,0x4b0u,0x9990u,0xd590u,0x560u,0x520u,0x99a0u,0xd5a0u,0x5e0u,0x5a0u,0x99b0u,0xd5b0u,0x650u,0x610u,0x99c0u,0xd5c0u,0x6d0u,0x690u,0x99d0u,0xd5d0u,0x740u,0x700u,0x99e0u,0xd5e0u,0x7c0u,0x780u,0x99f0u,0xd5f0u,0xc008u,0x12120u,0x12160u,0x12c00u,0xc018u,0x122b0u,0x122f0u,0x12c10u,0xc028u,0x12400u,0x12440u,0x12c20u,0xc038u,0x12590u,0x125d0u,0x12c30u,0xc048u,0x12760u,0x12720u,0x12c40u,0xc058u,0x128f0u,0x128b0u,0x12c50u,0xc068u,0x12a40u,0x12a00u,0x12c60u,0xc078u,0x12bd0u,0x12b90u,0x12c70u,0xc088u,0x12d20u,0x12d60u,0x12c80u,0xc098u,0x12eb0u,0x12ef0u,0x12c90u,0xc0a8u,0x13000u,0x13040u,0x12ca0u,0xc0b8u,0x13190u,0x131d0u,0x12cb0u,0xc0c8u,0x13360u,0x13320u,0x12cc0u,0xc0d8u,0x134f0u,0x134b0u,0x12cd0u,0xc0e8u,0x13640u,0x13600u,0x12ce0u,0xc0f8u,0x137d0u,0x13790u,0x12cf0u,0xc308u,0x12130u,0x12168u,0x12d00u,0xc318u,0x122a0u,0x122f8u,0x12d10u,0xc328u,0x12410u,0x12448u,0x12d20u,0xc338u,0x12580u,0x125d8u,0x12d30u,0xc348u,0x12770u,0x12728u,0x12d40u,0xc358u,0x128e0u,0x128b8u,0x12d50u,0xc368u,0x12a50u,0x12a08u,0x12d60u,0xc378u,0x12bc0u,0x12b98u,0x12d70u,0xc388u,0x12d30u,0x12d68u,0x12d80u,0xc398u,0x12ea0u,0x12ef8u,0x12d90u,0xc3a8u,0x13010u,0x13048u,0x12da0u,0xc3b8u,0x13180u,0x131d8u,0x12db0u,0xc3c8u,0x13370u,0x13328u,0x12dc0u,0xc3d8u,0x134e0u,0x134b8u,0x12dd0u,0xc3e8u,0x13650u,0x13608u,0x12de0u,0xc3f8u,0x137c0u,0x13798u,0x12df0u,0xc608u,0x12140u,0x12170u,0x12e00u,0xc618u,0x122d0u,0x122e0u,0x12e10u,0xc628u,0x12460u,0x12450u,0x12e20u,0xc638u,0x125f0u,0x125c0u,0x12e30u,0xc648u,0x12700u,0x12730u,0x12e40u,0xc658u,0x12890u,0x128a0u,0x12e50u,0xc668u,0x12a20u,0x12a10u,0x12e60u,0xc678u,0x12bb0u,0x12b80u,0x12e70u,0xc688u,0x12d40u,0x12d70u,0x12e80u,0xc698u,0x12ed0u,0x12ee0u,0x12e90u,0xc6a8u,0x13060u,0x13050u,0x12ea0u,0xc6b8u,0x131f0u,0x131c0u,0x12eb0u,0xc6c8u,0x13300u,0x13330u,0x12ec0u,0xc6d8u,0x13490u,0x134a0u,0x12ed0u,0xc6e8u,0x13620u,0x13610u,0x12ee0u,0xc6f8u,0x137b0u,0x13780u,0x12ef0u,0xc908u,0x12150u,0x12178u,0x12f00u,0xc918u,0x122c0u,0x122e8u,0x12f10u,0xc928u,0x12470u,0x12458u,0x12f20u,0xc938u,0x125e0u,0x125c8u,0x12f30u,0xc948u,0x12710u,0x12738u,0x12f40u,0xc958u,0x12880u,0x128a8u,0x12f50u,0xc968u,0x12a30u,0x12a18u,0x12f60u,0xc978u,0x12ba0u,0x12b88u,0x12f70u,0xc988u,0x12d50u,0x12d78u,0x12f80u,0xc998u,0x12ec0u,0x12ee8u,0x12f90u,0xc9a8u,0x13070u,0x13058u,0x12fa0u,0xc9b8u,0x131e0u,0x131c8u,0x12fb0u,0xc9c8u,0x13310u,0x13338u,0x12fc0u,0xc9d8u,0x13480u,0x134a8u,0x12fd0u,0xc9e8u,0x13630u,0x13618u,0x12fe0u,0xc9f8u,0x137a0u,0x13788u,0x12ff0u,0x8000u,0x1c300u,0x1ea40u,0x1ec20u,0x8080u,0x1c310u,0x100000u,0x1ec22u,0x8110u,0x1c320u,0x100000u,0x1ec24u,0x8190u,0x1c330u,0x100000u,0x1ec26u,0x8220u,0x1c340u,0x1ea40u,0x1ec28u,0x82a0u,0x1c350u,0x100000u,0x1ec2au,0x8330u,0x1c360u,0x100000u,0x1ec2cu,0x83b0u,0x1c370u,0x100000u,0x1ec2eu,0x8440u,0x1c380u,0x1ea40u,0x1ec30u,0x84c0u,0x1c390u,0x100000u,0x1ec32u,0x8550u,0x1c3a0u,0x100000u,0x1ec34u,0x85d0u,0x1c3b0u,0x100000u,0x1ec36u,0x8660u,0x1c3c0u,0x1ea40u,0x1ec38u,0x86e0u,0x1c3d0u,0x100000u,0x1ec3au,0x8770u,0x1c3e0u,0x100000u,0x1ec3cu,0x87f0u,0x1c3f0u,0x100000u,0x1ec3eu,0x8010u,0x1c400u,0x1ea70u,0x1ec20u,0x8090u,0x1c410u,0x100000u,0x1ec22u,0x8100u,0x1c420u,0x100000u,0x1ec24u,0x8180u,0x1c430u,0x100000u,0x1ec26u,0x8230u,0x1c440u,0x1ea70u,0x1ec28u,0x82b0u,0x1c450u,0x100000u,0x1ec2au,0x8320u,0x1c460u,0x100000u,0x1ec2cu,0x83a0u,0x1c470u,0x100000u,0x1ec2eu,0x8450u,0x1c480u,0x1ea70u,0x1ec30u,0x84d0u,0x1c490u,0x100000u,0x1ec32u,0x8540u,0x1c4a0u,0x100000u,0x1ec34u,0x85c0u,0x1c4b0u,0x100000u,0x1ec36u,0x8670u,0x1c4c0u,0x1ea70u,0x1ec38u,0x86f0u,0x1c4d0u,0x100000u,0x1ec3au,0x8760u,0x1c4e0u,0x100000u,0x1ec3cu,0x87e0u,0x1c4f0u,0x100000u,0x1ec3eu,0x8020u,0x1c500u,0x1eaa0u,0x1ec20u,0x80a0u,0x1c510u,0x100000u,0x1ec22u,0x8130u,0x1c520u,0x100000u,0x1ec24u,0x81b0u,0x1c530u,0x100000u,0x1ec26u,0x8200u,0x1c540u,0x1eaa0u,0x1ec28u,0x8280u,0x1c550u,0x100000u,0x1ec2au,0x8310u,0x1c560u,0x100000u,0x1ec2cu,0x8390u,0x1c570u,0x100000u,0x1ec2eu,0x8460u,0x1c580u,0x1eaa0u,0x1ec30u,0x84e0u,0x1c590u,0x100000u,0x1ec32u,0x8570u,0x1c5a0u,0x100000u,0x1ec34u,0x85f0u,0x1c5b0u,0x100000u,0x1ec36u,0x8640u,0x1c5c0u,0x1eaa0u,0x1ec38u,0x86c0u,0x1c5d0u,0x100000u,0x1ec3au,0x8750u,0x1c5e0u,0x100000u,0x1ec3cu,0x87d0u,0x1c5f0u,0x100000u,0x1ec3eu,0x8030u,0x1c600u,0x1ead0u,0x1ec20u,0x80b0u,0x1c610u,0x100000u,0x1ec22u,0x8120u,0x1c620u,0x100000u,0x1ec24u,0x81a0u,0x1c630u,0x100000u,0x1ec26u,0x8210u,0x1c640u,0x1ead0u,0x1ec28u,0x8290u,0x1c650u,0x100000u,0x1ec2au,0x8300u,0x1c660u,0x100000u,0x1ec2cu,0x8380u,0x1c670u,0x100000u,0x1ec2eu,0x8470u,0x1c680u,0x1ead0u,0x1ec30u,0x84f0u,0x1c690u,0x100000u,0x1ec32u,0x8560u,0x1c6a0u,0x100000u,0x1ec34u,0x85e0u,0x1c6b0u,0x100000u,0x1ec36u,0x8650u,0x1c6c0u,0x1ead0u,0x1ec38u,0x86d0u,0x1c6d0u,0x100000u,0x1ec3au,0x8740u,0x1c6e0u,0x100000u,0x1ec3cu,0x87c0u,0x1c6f0u,0x100000u,0x1ec3eu,0x27000u,0x1d480u,0x1d480u,0x80000000u,0x27002u,0x1d482u,0x100000u,0x80000000u,0x27004u,0x1d484u,0x100000u,0x80000000u,0x27006u,0x1d486u,0x100000u,0x80000000u,0x27200u,0x1d490u,0x1d480u,0x80000000u,0x27202u,0x1d492u,0x100000u,0x80000000u,0x27204u,0x1d494u,0x100000u,0x80000000u,0x27206u,0x1d496u,0x100000u,0x80000000u,0x27400u,0x1d4a0u,0x1d480u,0x80000000u,0x27402u,0x1d4a2u,0x100000u,0x80000000u,0x27404u,0x1d4a4u,0x100000u,0x80000000u,0x27406u,0x1d4a6u,0x100000u,0x80000000u,0x27600u,0x1d4b0u,0x1d480u,0x80000000u,0x27602u,0x1d4b2u,0x100000u,0x80000000u,0x27604u,0x1d4b4u,0x100000u,0x80000000u,0x27606u,0x1d4b6u,0x100000u,0x80000000u,0x27008u,0x1d488u,0x1d490u,0x80000000u,0x2700au,0x1d48au,0x100000u,0x80000000u,0x2700cu,0x1d48cu,0x100000u,0x80000000u,0x2700eu,0x1d48eu,0x100000u,0x80000000u,0x27208u,0x1d498u,0x1d490u,0x80000000u,0x2720au,0x1d49au,0x100000u,0x80000000u,0x2720cu,0x1d49cu,0x100000u,0x80000000u,0x2720eu,0x1d49eu,0x100000u,0x80000000u,0x27408u,0x1d4a8u,0x1d490u,0x80000000u,0x2740au,0x1d4aau,0x100000u,0x80000000u,0x2740cu,0x1d4acu,0x100000u,0x80000000u,0x2740eu,0x1d4aeu,0x100000u,0x80000000u,0x27608u,0x1d4b8u,0x1d490u,0x80000000u,0x2760au,0x1d4bau,0x100000u,0x80000000u,0x2760cu,0x1d4bcu,0x100000u,0x80000000u,0x2760eu,0x1d4beu,0x100000u,0x80000000u,0x26808u,0x1d4c0u,0x1d4a0u,0x80000000u,0x2680au,0x1d4c2u,0x100000u,0x80000000u,0x2680cu,0x1d4c4u,0x100000u,0x80000000u,0x2680eu,0x1d4c6u,0x100000u,0x80000000u,0x26a08u,0x1d4d0u,0x1d4a0u,0x80000000u,0x26a0au,0x1d4d2u,0x100000u,0x80000000u,0x26a0cu,0x1d4d4u,0x100000u,0x80000000u,0x26a0eu,0x1d4d6u,0x100000u,0x80000000u,0x26c08u,0x1d4e0u,0x1d4a0u,0x80000000u,0x26c0au,0x1d4e2u,0x100000u,0x80000000u,0x26c0cu,0x1d4e4u,0x100000u,0x80000000u,0x26c0eu,0x1d4e6u,0x100000u,0x80000000u,0x26e08u,0x1d4f0u,0x1d4a0u,0x80000000u,0x26e0au,0x1d4f2u,0x100000u,0x80000000u,0x26e0cu,0x1d4f4u,0x100000u,0x80000000u,0x26e0eu,0x1d4f6u,0x100000u,0x80000000u,0x100000u,0x1d4c8u,0x1d4b0u,0x80000000u,0x100000u,0x1d4cau,0x100000u,0x80000000u,0x100000u,0x1d4ccu,0x100000u,0x80000000u,0x100000u,0x1d4ceu,0x100000u,0x80000000u,0x100000u,0x1d4d8u,0x1d4b0u,0x80000000u,0x100000u,0x1d4dau,0x100000u,0x80000000u,0x100000u,0x1d4dcu,0x100000u,0x80000000u,0x100000u,0x1d4deu,0x100000u,0x80000000u,0x100000u,0x1d4e8u,0x1d4b0u,0x80000000u,0x100000u,0x1d4eau,0x100000u,0x80000000u,0x100000u,0x1d4ecu,0x100000u,0x80000000u,0x100000u,0x1d4eeu,0x100000u,0x80000000u,0x100000u,0x1d4f8u,0x1d4b0u,0x80000000u,0x100000u,0x1d4fau,0x100000u,0x80000000u,0x100000u,0x1d4fcu,0x100000u,0x80000000u,0x100000u,0x1d4feu,0x100000u,0x80000000u,0x30000u,0x60420u,0xbfc50u,0xf0000u,0x30010u,0x60430u,0xbfc40u,0xf0010u,0x30020u,0x60400u,0xbfc70u,0xf0020u,0x30030u,0x60410u,0xbfc60u,0xf0030u,0x30040u,0x60460u,0xbfc10u,0xf0040u,0x30050u,0x60470u,0xbfc00u,0xf0050u,0x30060u,0x60440u,0xbfc30u,0xf0060u,0x30070u,0x60450u,0xbfc20u,0xf0070u,0x30080u,0x604b0u,0xbfcd0u,0xf0080u,0x30090u,0x604a0u,0xbfcc0u,0xf0090u,0x300a0u,0x60490u,0xbfcf0u,0xf00a0u,0x300b0u,0x60480u,0xbfce0u,0xf00b0u,0x300c0u,0x604f0u,0xbfc90u,0xf00c0u,0x300d0u,0x604e0u,0xbfc80u,0xf00d0u,0x300e0u,0x604d0u,0xbfcb0u,0xf00e0u,0x300f0u,0x604c0u,0xbfca0u,0xf00f0u,0x30100u,0x60530u,0xbfd60u,0xf0100u,0x30110u,0x60520u,0xbfd70u,0xf0110u,0x30120u,0x60510u,0xbfd40u,0xf0120u,0x30130u,0x60500u,0xbfd50u,0xf0130u,0x30140u,0x60570u,0xbfd20u,0xf0140u,0x30150u,0x60560u,0xbfd30u,0xf0150u,0x30160u,0x60550u,0xbfd00u,0xf0160u,0x30170u,0x60540u,0xbfd10u,0xf0170u,0x30190u,0x605b0u,0xbfde0u,0xf0190u,0x30180u,0x605a0u,0xbfdf0u,0xf0180u,0x301b0u,0x60590u,0xbfdc0u,0xf01b0u,0x301a0u,0x60580u,0xbfdd0u,0xf01a0u,0x301d0u,0x605f0u,0xbfda0u,0xf01d0u,0x301c0u,0x605e0u,0xbfdb0u,0xf01c0u,0x301f0u,0x605d0u,0xbfd80u,0xf01f0u,0x301e0u,0x605c0u,0xbfd90u,0xf01e0u,0x30210u,0x60640u,0xbfe60u,0xf0210u,0x30200u,0x60650u,0xbfe70u,0xf0200u,0x30230u,0x60660u,0xbfe40u,0xf0230u,0x30220u,0x60670u,0xbfe50u,0xf0220u,0x30250u,0x60600u,0xbfe20u,0xf0250u,0x30240u,0x60610u,0xbfe30u,0xf0240u,0x30270u,0x60620u,0xbfe00u,0xf0270u,0x30260u,0x60630u,0xbfe10u,0xf0260u,0x30290u,0x606c0u,0xbfef0u,0xf0290u,0x30280u,0x606d0u,0xbfee0u,0xf0280u,0x302b0u,0x606e0u,0xbfed0u,0xf02b0u,0x302a0u,0x606f0u,0xbfec0u,0xf02a0u,0x302d0u,0x60680u,0xbfeb0u,0xf02d0u,0x302c0u,0x60690u,0xbfea0u,0xf02c0u,0x302f0u,0x606a0u,0xbfe90u,0xf02f0u,0x302e0u,0x606b0u,0xbfe80u,0xf02e0u,0x30320u,0x60740u,0xbff70u,0x11f720u,0x30330u,0x60750u,0xbff60u,0x11f730u,0x30300u,0x60760u,0xbff50u,0x11f700u,0x30310u,0x60770u,0xbff40u,0x11f710u,0x30360u,0x60700u,0xbff30u,0x11f760u,0x30370u,0x60710u,0xbff20u,0x11f770u,0x30340u,0x60720u,0xbff10u,0x11f740u,0x30350u,0x60730u,0xbff00u,0x11f750u,0x303a0u,0x8fbd0u,0xbfff0u,0x11f7a0u,0x303b0u,0x8fbc0u,0xbffe0u,0x11f7b0u,0x30380u,0x8fbf0u,0xbffd0u,0x11f780u,0x30390u,0x8fbe0u,0xbffc0u,0x11f790u,0x303e0u,0x8fb90u,0xbffb0u,0x11f7e0u,0x303f0u,0x8fb80u,0xbffa0u,0x11f7f0u,0x303c0u,0x8fbb0u,0xbff90u,0x11f7c0u,0x303d0u,0x8fba0u,0xbff80u,0x11f7d0u,0x0u,0x80000000u,0x0u,0x36030u,0x0u,0x80000000u,0x0u,0x36030u,0x0u,0x80000000u,0x0u,0x36030u,0x0u,0x80000000u,0x0u,0x36030u,0x0u,0x80000000u,0x0u,0x36030u,0x0u,0x80000000u,0x0u,0x36030u,0x0u,0x80000000u,0x0u,0x36030u,0x0u,0x80000000u,0x0u,0x36030u,0x0u,0x80000000u,0x0u,0x360b2u,0x0u,0x80000000u,0x0u,0x360b2u,0x0u,0x80000000u,0x0u,0x360b2u,0x0u,0x80000000u,0x0u,0x360b2u,0x0u,0x80000000u,0x0u,0x360b2u,0x0u,0x80000000u,0x0u,0x360b2u,0x0u,0x80000000u,0x0u,0x360b2u,0x0u,0x80000000u,0x0u,0x360b2u,0x0u,0x80000000u,0x0u,0x36092u,0x0u,0x80000000u,0x0u,0x36092u,0x0u,0x80000000u,0x0u,0x36092u,0x0u,0x80000000u,0x0u,0x36092u,0x0u,0x80000000u,0x0u,0x36092u,0x0u,0x80000000u,0x0u,0x36092u,0x0u,0x80000000u,0x0u,0x36092u,0x0u,0x80000000u,0x0u,0x36092u,0x0u,0x80000000u,0x0u,0x3609au,0x0u,0x80000000u,0x0u,0x3609au,0x0u,0x80000000u,0x0u,0x3609au,0x0u,0x80000000u,0x0u,0x3609au,0x0u,0x80000000u,0x0u,0x3609au,0x0u,0x80000000u,0x0u,0x3609au,0x0u,0x80000000u,0x0u,0x3609au,0x0u,0x80000000u,0x0u,0x3609au,0x0u,0x80000000u,0x0u,0x36059u,0x0u,0x80000000u,0x0u,0x36059u,0x0u,0x80000000u,0x0u,0x36059u,0x0u,0x80000000u,0x0u,0x36059u,0x0u,0x80000000u,0x0u,0x36059u,0x0u,0x80000000u,0x0u,0x36059u,0x0u,0x80000000u,0x0u,0x36059u,0x0u,0x80000000u,0x0u,0x36059u,0x0u,0x80000000u,0x0u,0x36059u,0x0u,0x80000000u,0x0u,0x36059u,0x0u,0x80000000u,0x0u,0x36059u,0x0u,0x80000000u,0x0u,0x36059u,0x0u,0x80000000u,0x0u,0x36059u,0x0u,0x80000000u,0x0u,0x36059u,0x0u,0x80000000u,0x0u,0x36059u,0x0u,0x80000000u,0x0u,0x36059u,0x0u,0x80000000u,0x0u,0x16071u,0x0u,0x80000000u,0x0u,0x16071u,0x0u,0x80000000u,0x0u,0x16071u,0x0u,0x80000000u,0x0u,0x16071u,0x0u,0x80000000u,0x0u,0x16071u,0x0u,0x80000000u,0x0u,0x16071u,0x0u,0x80000000u,0x0u,0x16071u,0x0u,0x80000000u,0x0u,0x16071u,0x0u,0x80000000u,0x0u,0x14071u,0x0u,0x80000000u,0x0u,0x14071u,0x0u,0x80000000u,0x0u,0x14071u,0x0u,0x80000000u,0x0u,0x14071u,0x0u,0x80000000u,0x0u,0x14071u,0x0u,0x80000000u,0x0u,0x14071u,0x0u,0x80000000u,0x0u,0x14071u,0x0u,0x80000000u,0x0u,0x14071u};

__global__ __launch_bounds__(256, 1) void swin_main(const uint16_t* x, const uint16_t* mask, char* ws, uint16_t* out,
                                                    const uint16_t* wqkv, const uint16_t* wo, const uint16_t* ls,
                                                    const uint16_t* w1, const uint16_t* w2, const uint16_t* rc,
                                                    const int* ridx) {
  __shared__ __attribute__((aligned(16))) char smem[LDS_BYTES];
  const int wave = __builtin_amdgcn_readfirstlane(threadIdx.x >> 6);
  const unsigned lds0 = (unsigned)(size_t)smem;
  asm volatile(
      ".p2align 5\n"
      "s_mov_b64 s[92:93], %0\n"
      "s_mov_b32 s40, %1\n"
      "s_mov_b32 s41, %2\n"
      "s_mov_b64 s[88:89], %3\n"
      "s_load_dwordx2 s[16:17], s[92:93], 0\n"
      "s_load_dwordx2 s[20:21], s[92:93], 32\n"
      "s_load_dwordx2 s[46:47], s[92:93], 40\n"
      "s_load_dwordx8 s[80:87], s[92:93], 48\n"
      "s_load_dwordx2 s[76:77], s[92:93], 80\n"
      "s_mov_b64 s[90:91], s[92:93]\n"
      "s_getpc_b64 s[98:99]\n"
      "L_ipf0_%=:\n"
      "v_mbcnt_lo_u32_b32 v239, -1, 0\n"
      "v_mbcnt_hi_u32_b32 v239, -1, v239\n"
      "v_lshlrev_b32 v238, 2, v239\n"
      "v_lshlrev_b32 v235, 4, v239\n"
      "s_cmp_lt_u32 s41, 209\n"
      "s_cselect_b32 s44, 20, 19\n"
      "s_lshl_b32 s92, s40, 8\n"
      "v_add_u32_e64 v49, s92, v238\n"
      "v_min_u32 v29, 704, v49\n"
      "v_add_u32 v29, 4096, v29\n"
      "v_min_u32 v49, 672, v49\n"
      "v_add_u32 v47, 122112, v49\n"
      "s_lshr_b32 s92, s41, 3\n"
      "s_lshl_b32 s92, s92, 7\n"
      "v_lshlrev_b32 v236, 7, v239\n"
      "s_mul_i32 s93, s92, 18\n"
      "s_mul_i32 s94, s40, 640\n"
      "v_min_u32 v237, 512, v236\n"
      "v_add_u32 v237, s94, v237\n"
      "v_min_u32 v237, 2176, v237\n"
      "v_add_u32 v237, s93, v237\n"
      "s_cmp_eq_u32 s40, 1\n"
      "s_cselect_b32 s93, 4096, 0\n"
      "s_add_u32 s92, s92, s93\n"
      "s_cmp_eq_u32 s40, 2\n"
      "s_cselect_b32 s93, 9, 8\n"
      "s_cselect_b64 s[72:73], s[98:99], s[88:89]\n"
      "s_cmp_lt_u32 s40, 2\n"
      "s_cselect_b32 s93, 27, s93\n"
      "s_mul_i32 s92, s92, s93\n"
      "s_sub_u32 s93, s93, 1\n"
      "s_lshl_b32 s93, s93, 7\n"
      "v_min_u32 v236, s93, v236\n"
      "v_add_u32 v236, s92, v236\n"
      "s_lshl_b32 s95, s40, 3\n"
      "v_lshrrev_b32 v8, 3, v239\n"
      "v_and_b32 v147, 7, v239\n"
      "v_add_u32 v149, s95, v8\n"
      "v_bfe_u32 v148, v149, 1, 3\n"
      "v_cmp_lt_u32 s[70:71], s44, v149\n"
      "v_min_u32_e64 v149, s44, v149\n"
      "s_mov_b32 s18, 100270464\n"
      "s_mov_b32 s19, 0x20000\n"
      "s_mov_b32 s78, 9604\n"
      "s_mov_b32 s79, 0x20000\n"
      "s_mov_b32 s22, 221184\n"
      "s_mov_b32 s23, 0x20000\n"
      "s_lshl_b32 s64, s40, 10\n"
      "v_add_u32 v36, s64, v235\n"
      "s_add_u32 s75, s64, 18432\n"
      "s_and_b32 s92, s40, 1\n"
      "s_lshl_b32 s92, s92, 2\n"
      "s_mul_i32 s93, s40, 9216\n"
      "v_and_b32 v146, 2, v8\n"
      "v_xor_b32 v146, v146, v147\n"
      "v_xor_b32_e64 v146, s92, v146\n"
      "v_lshl_add_u32 v146, v146, 4, s93\n"
      "v_mul_u32_u24 v106, 1152, v8\n"
      "v_add_u32 v106, v106, v146\n"
      "s_cmp_lt_u32 s40, 2\n"
      "s_cselect_b32 s65, s64, 163840\n"
      "s_waitcnt lgkmcnt(0)\n"
      "s_cselect_b64 s[72:73], s[20:21], s[72:73]\n"
      "global_load_dword v49, v49, s[86:87]\n"
      "global_load_dword a255, v236, s[72:73]\n"
      "global_load_dword a253, v237, s[46:47]\n"
      "s_mul_i32 s92, s40, 8192\n"
      "s_add_u32 s88, s88, s92\n"
      "s_addc_u32 s89, s89, 0\n"
      "s_add_u32 m0, s64, 124928\n"
      "s_movk_i32 s92, 0\n"
      "buffer_load_dwordx4 v36, s[76:79], s92 offen  lds\n"
      "s_add_u32 m0, s64, 129024\n"
      "s_movk_i32 s92, 4096\n"
      "buffer_load_dwordx4 v36, s[76:79], s92 offen  lds\n"
      "s_add_u32 m0, s64, 133120\n"
      "s_cmp_lt_u32 s40, 2\n"
      "s_cselect_b32 m0, m0, s65\n"
      "s_movk_i32 s92, 8192\n"
      "buffer_load_dwordx4 v36, s[76:79], s92 offen  lds\n"
      "s_load_dwordx16 s[0:15], s[82:83], 0\n"
      "s_load_dwordx16 s[48:63], s[84:85], 0\n"
      "s_load_dwordx16 s[24:39], s[84:85], 64\n"
      "s_load_dwordx2 s[66:67], s[80:81], 0\n"
      "s_movk_i32 s93, 4180\n"
      "s_cmp_lt_u32 s41, 209\n"
      "s_cselect_b32 s92, 209, 47\n"
      "s_cselect_b32 s93, 795, s93\n"
      "s_cmp_lt_u32 s41, 252\n"
      "s_cselect_b32 s94, 16, 15\n"
      "s_add_u32 s93, s93, s41\n"
      "v_lshl_add_u32 v236, v149, 8, s41\n"
      "v_mul_u32_u24 v237, s92, v149\n"
      "v_add_u32 v237, s93, v237\n"
      "v_cmp_lt_u32 vcc, s94, v149\n"
      "v_subrev_u32 v146, 5184, v237\n"
      "s_nop 0\n"
      "v_cndmask_b32 v236, v236, v237, vcc\n"
      "v_cmp_le_u32 vcc, 5184, v236\n"
      "v_mul_u32_u24 v44, 58255, v236\n"
      "v_lshrrev_b32 v44, 22, v44\n"
      "v_mul_u32_u24 v42, 72, v44\n"
      "v_sub_u32 v42, v236, v42\n"
      "v_min_u32 v237, 72, v146\n"
      "v_cndmask_b32 v44, v44, v237, vcc\n"
      "v_subrev_u32 v237, 72, v146\n"
      "v_min_u32 v237, 72, v237\n"
      "v_cndmask_b32 v42, v42, v237, vcc\n"
      "v_mad_u32_u24 v237, v44, 7, 9\n"
      "v_subrev_u32 v236, 0x1ff, v237\n"
      "v_min_u32 v237, v237, v236\n"
      "v_mad_u32_u24 v236, v42, 7, 9\n"
      "v_subrev_u32 v146, 0x1ff, v236\n"
      "v_min_u32 v236, v236, v146\n"
      "v_mul_u32_u24 v237, 0x1ff, v237\n"
      "v_add_u32 v237, v237, v236\n"
      "v_mul_u32_u24 v236, 0x180, v237\n"
      "v_bfrev_b32 v149, 1\n"
      "v_cndmask_b32 v236, v236, v149, s[70:71]\n"
      "v_xor_b32 v148, v148, v147\n"
      "v_lshl_add_u32 v236, v148, 4, v236\n"
      "s_add_u32 m0, s64, 149504\n"
      "s_nop 0\n"
      "buffer_load_dwordx4 v236, s[16:19], 0 offen  lds\n"
      "s_add_u32 m0, s64, 153472\n"
      "s_nop 0\n"
      "buffer_load_dwordx4 v236, s[16:19], 0 offen offset:128  lds\n"
      "s_add_u32 m0, s64, 157440\n"
      "s_nop 0\n"
      "buffer_load_dwordx4 v236, s[16:19], 0 offen offset:256  lds\n"
      "s_movk_i32 s95, 504\n"
      "s_add_u32 s92, s40, 0\n"
      "s_mul_i32 s93, s92, 43\n"
      "s_lshr_b32 s93, s93, 8\n"
      "s_mul_i32 s94, s93, 6\n"
      "s_sub_u32 s92, s92, s94\n"
      "s_lshl_b32 s92, s92, 3\n"
      "s_lshl_b32 s93, s93, 7\n"
      "v_add_u32 v237, s92, v8\n"
      "v_bfe_u32 v236, v237, 1, 3\n"
      "v_xor_b32 v236, v236, v147\n"
      "v_lshl_add_u32 v236, v236, 4, s93\n"
      "v_mul_u32_u24 v148, 37, v237\n"
      "v_lshrrev_b32_e64 v148, 8, v148\n"
      "v_mad_u32_u24 v16, v148, s95, v237\n"
      "v_mul_u32_u24 v16, 384, v16\n"
      "v_add_u32 v16, v16, v236\n"
      "s_add_u32 s92, s40, 4\n"
      "s_mul_i32 s93, s92, 43\n"
      "s_lshr_b32 s93, s93, 8\n"
      "s_mul_i32 s94, s93, 6\n"
      "s_sub_u32 s92, s92, s94\n"
      "s_lshl_b32 s92, s92, 3\n"
      "s_lshl_b32 s93, s93, 7\n"
      "v_add_u32 v237, s92, v8\n"
      "v_bfe_u32 v236, v237, 1, 3\n"
      "v_xor_b32 v236, v236, v147\n"
      "v_lshl_add_u32 v236, v236, 4, s93\n"
      "v_mul_u32_u24 v148, 37, v237\n"
      "v_lshrrev_b32 v148, 8, v148\n"
      "v_mad_u32_u24 v17, v148, s95, v237\n"
      "v_mul_u32_u24 v17, 384, v17\n"
      "v_add_u32 v17, v17, v236\n"
      "s_add_u32 s92, s40, 8\n"
      "s_mul_i32 s93, s92, 43\n"
      "s_lshr_b32 s93, s93, 8\n"
      "s_mul_i32 s94, s93, 6\n"
      "s_sub_u32 s92, s92, s94\n"
      "s_lshl_b32 s92, s92, 3\n"
      "s_lshl_b32 s93, s93, 7\n"
      "v_add_u32_e64 v236, s92, v8\n"
      "v_bfe_u32 v237, v236, 1, 3\n"
      "v_xor_b32 v237, v237, v147\n"
      "v_lshl_add_u32 v237, v237, 4, s93\n"
      "v_mul_u32_u24 v148, 37, v236\n"
      "v_lshrrev_b32_e64 v148, 8, v148\n"
      "v_mad_u32_u24 v18, v148, s95, v236\n"
      "v_mul_u32_u24 v18, 384, v18\n"
      "v_add_u32 v18, v18, v237\n"
      "s_add_u32 s92, s40, 12\n"
      "s_mul_i32 s93, s92, 43\n"
      "s_lshr_b32 s93, s93, 8\n"
      "s_mul_i32 s94, s93, 6\n"
      "s_sub_u32 s92, s92, s94\n"
      "s_lshl_b32 s92, s92, 3\n"
      "s_lshl_b32 s93, s93, 7\n"
      "v_add_u32 v236, s92, v8\n"
      "v_bfe_u32 v237, v236, 1, 3\n"
      "v_xor_b32 v237, v237, v147\n"
      "v_lshl_add_u32 v237, v237, 4, s93\n"
      "v_mul_u32_u24 v148, 37, v236\n"
      "v_lshrrev_b32 v148, 8, v148\n"
      "v_mad_u32_u24 v19, v148, s95, v236\n"
      "v_mul_u32_u24 v19, 384, v19\n"
      "v_add_u32 v19, v19, v237\n"
      "s_add_u32 s92, s40, 16\n"
      "s_mul_i32 s93, s92, 43\n"
      "s_lshr_b32 s93, s93, 8\n"
      "s_mul_i32 s94, s93, 6\n"
      "s_sub_u32 s92, s92, s94\n"
      "s_lshl_b32 s92, s92, 3\n"
      "s_lshl_b32 s93, s93, 7\n"
      "s_cmp_lt_u32 s40, 2\n"
      "s_cselect_b32 s93, s93, 0x80000000\n"
      "v_add_u32 v237, s92, v8\n"
      "v_bfe_u32 v8, v237, 1, 3\n"
      "v_xor_b32_e64 v8, v8, v147\n"
      "v_lshl_add_u32 v8, v8, 4, s93\n"
      "v_mul_u32_u24_e64 v236, 37, v237\n"
      "v_lshrrev_b32_e64 v236, 8, v236\n"
      "v_mad_u32_u24 v15, v236, s95, v237\n"
      "v_mul_u32_u24 v15, 384, v15\n"
      "v_add_u32 v15, v15, v8\n"
      "s_add_u32 s92, s41, 0\n"
      "s_mul_i32 s93, s92, 58255\n"
      "s_lshr_b32 s93, s93, 22\n"
      "s_add_u32 s47, s92, s93\n"
      "s_mul_i32 s47, s47, 4802\n"
      "s_and_b32 s47, s47, -4\n"
      "s_mul_i32 s93, s93, 439\n"
      "s_add_u32 s45, s92, s93\n"
      "s_mul_i32 s45, s45, 2688\n"
      "s_add_u32 s45, s45, 589824\n"
      "s_add_u32 s92, s41, 256\n"
      "s_mul_i32 s93, s92, 58255\n"
      "s_lshr_b32 s93, s93, 22\n"
      "s_add_u32 s74, s92, s93\n"
      "s_mul_i32 s74, s74, 4802\n"
      "s_and_b32 s74, s74, -4\n"
      "s_mul_i32 s93, s93, 439\n"
      "s_add_u32 s46, s92, s93\n"
      "s_mul_i32 s46, s46, 2688\n"
      "s_add_u32 s46, s46, 589824\n"
      "s_add_u32 m0, s64, 0\n"
      "s_nop 0\n"
      "buffer_load_dwordx4 v16, s[16:19], s45 offen  lds\n"
      "s_add_u32 m0, s64, 4096\n"
      "s_nop 0\n"
      "buffer_load_dwordx4 v17, s[16:19], s45 offen  lds\n"
      "s_add_u32 m0, s64, 8192\n"
      "s_nop 0\n"
      "buffer_load_dwordx4 v18, s[16:19], s45 offen  lds\n"
      "s_add_u32 m0, s64, 12288\n"
      "s_nop 0\n"
      "buffer_load_dwordx4 v19, s[16:19], s45 offen  lds\n"
      "s_add_u32 m0, s65, 16384\n"
      "s_nop 0\n"
      "buffer_load_dwordx4 v15, s[16:19], s45 offen  lds\n"
      "v_subrev_u32 v8, 2, v239\n"
      "v_cmp_ge_u32 s[72:73], s44, v8\n"
      "v_min_u32_e64 v8, s44, v8\n"
      "s_movk_i32 s93, 4180\n"
      "s_cmp_lt_u32 s41, 209\n"
      "s_cselect_b32 s92, 209, 47\n"
      "s_cselect_b32 s93, 795, s93\n"
      "s_cmp_lt_u32 s41, 252\n"
      "s_cselect_b32 s94, 16, 15\n"
      "s_add_u32 s93, s93, s41\n"
      "v_lshl_add_u32 v237, v8, 8, s41\n"
      "v_mul_u32_u24 v236, s92, v8\n"
      "v_add_u32_e64 v236, s93, v236\n"
      "v_cmp_lt_u32 vcc, s94, v8\n"
      "v_subrev_u32 v147, 5184, v236\n"
      "s_nop 0\n"
      "v_cndmask_b32 v237, v237, v236, vcc\n"
      "v_cmp_le_u32 vcc, 5184, v237\n"
      "v_mul_u32_u24 v148, 58255, v237\n"
      "v_lshrrev_b32 v148, 22, v148\n"
      "v_mul_u32_u24 v149, 72, v148\n"
      "v_sub_u32_e64 v149, v237, v149\n"
      "v_min_u32 v236, 72, v147\n"
      "v_cndmask_b32 v148, v148, v236, vcc\n"
      "v_subrev_u32 v236, 72, v147\n"
      "v_min_u32 v236, 72, v236\n"
      "v_cndmask_b32 v149, v149, v236, vcc\n"
      "v_mul_u32_u24 v38, 73, v148\n"
      "v_add_u32 v38, v38, v149\n"
      "v_mul_u32_u24 v38, 4802, v38\n"
      "v_mul_u32_u24 v8, 0x1ff, v148\n"
      "v_add_u32_e64 v8, v8, v149\n"
      "v_mul_u32_u24 v8, 2688, v8\n"
      "v_add_u32 v8, 589824, v8\n"
      "v_cmp_eq_u32 vcc, 72, v148\n"
      "v_or_b32 v42, 0x80000000, v8\n"
      "s_nop 0\n"
      "v_cndmask_b32 v8, v8, v42, vcc\n"
      "v_cmp_eq_u32 vcc, 72, v149\n"
      "v_or_b32_e64 v42, 0x40000000, v8\n"
      "s_nop 0\n"
      "v_cndmask_b32 v8, v8, v42, vcc\n"
      "v_mov_b32 v236, 0x20000000\n"
      "v_cndmask_b32 v8, v236, v8, s[72:73]\n"
      "v_cndmask_b32 v38, v236, v38, s[72:73]\n"
      "v_mov_b32 v37, v236\n"
      "s_nop 1\n"
      "v_mov_b32_dpp v37, v8 wave_shr:1 row_mask:0xf bank_mask:0xf\n"
      "v_mov_b32 v79, 0\n"
      "v_mov_b32 v83, 0\n"
      "v_mov_b32 v87, 0\n"
      "v_mov_b32 v91, 0\n"
      "v_mov_b32 v95, 0\n"
      "s_lshl_b32 s94, s40, 2\n"
      "s_add_u32 s94, s94, 115456\n"
      "v_add_u32 v31, s94, v235\n"
      "v_and_b32 v42, 3, v239\n"
      "v_cmp_ne_u32 vcc, 0, v42\n"
      "v_mov_b32 v42, 0x3f803f80\n"
      "s_nop 0\n"
      "v_cndmask_b32_e64 v32, v42, 0, vcc\n"
      "v_mov_b32 v33, v32\n"
      "v_mov_b32 v34, v32\n"
      "v_mov_b32 v35, v32\n"
      "v_and_b32 v65, 15, v239\n"
      "v_lshrrev_b32_e64 v58, 4, v239\n"
      "v_mul_u32_u24 v65, 196, v65\n"
      "v_add_u32 v65, 124928, v65\n"
      "v_lshl_add_u32 v58, v58, 4, v65\n"
      "v_min_u32 v68, 192, v238\n"
      "v_add_u32 v68, 124928, v68\n"
      "global_load_dwordx4 v[236:239], v235, s[88:89]\n"
      "s_waitcnt lgkmcnt(0)\n"
      "s_pack_ll_b32_b16 s92, s0, s8\n"
      "s_waitcnt vmcnt(14)\n"
      "v_dot2_f32_bf16 v44, s92, v49, 0\n"
      "s_pack_hh_b32_b16 s93, s0, s8\n"
      "v_dot2_f32_bf16 v46, s93, v49, 0\n"
      "s_pack_ll_b32_b16 s94, s1, s9\n"
      "v_dot2_f32_bf16 v40, s94, v49, 0\n"
      "s_pack_hh_b32_b16 s95, s1, s9\n"
      "v_dot2_f32_bf16 v48, s95, v49, 0\n"
      "s_pack_ll_b32_b16 s92, s2, s10\n"
      "v_dot2_f32_bf16 v45, s92, v49, 0\n"
      "s_pack_hh_b32_b16 s93, s2, s10\n"
      "v_dot2_f32_bf16 v43, s93, v49, 0\n"
      "s_pack_ll_b32_b16 s94, s3, s11\n"
      "v_dot2_f32_bf16 v50, s94, v49, 0\n"
      "s_pack_hh_b32_b16 s95, s3, s11\n"
      "v_dot2_f32_bf16 v41, s95, v49, 0\n"
      "s_pack_ll_b32_b16 s92, s4, s12\n"
      "v_dot2_f32_bf16 v51, s92, v49, 0\n"
      "s_pack_hh_b32_b16 s93, s4, s12\n"
      "v_dot2_f32_bf16 v54, s93, v49, 0\n"
      "s_pack_ll_b32_b16 s94, s5, s13\n"
      "v_dot2_f32_bf16 v56, s94, v49, 0\n"
      "global_load_dwordx4 v[146:149], v235, s[88:89] offset:1024\n"
      "s_pack_hh_b32_b16 s95, s5, s13\n"
      "v_dot2_f32_bf16 v63, s95, v49, 0\n"
      "s_pack_ll_b32_b16 s92, s6, s14\n"
      "v_dot2_f32_bf16 v53, s92, v49, 0\n"
      "s_pack_hh_b32_b16 s93, s6, s14\n"
      "v_dot2_f32_bf16 v57, s93, v49, 0\n"
      "s_pack_ll_b32_b16 s94, s7, s15\n"
      "v_dot2_f32_bf16 v55, s94, v49, 0\n"
      "s_pack_hh_b32_b16 s95, s7, s15\n"
      "v_dot2_f32_bf16 v70, s95, v49, 0\n"
      "s_lshl_b32 s92, s40, 4\n"
      "s_lshr_b64 s[66:67], s[66:67], s92\n"
      "s_lshl_b32 s66, s66, 16\n"
      "v_mov_b32 v49, s66\n"
      "v_max_f32 v49, 0xc2c80000, v49\n"
      "v_min_f32 v49, 0x40930000, v49\n"
      "v_mul_f32 v49, 0x3fb8aa3b, v49\n"
      "v_exp_f32 v49, v49\n"
      "s_nop 0\n"
      "v_cvt_pk_bf16_f32 v49, v49, v49\n"
      "v_lshlrev_b32 v49, 16, v49\n"
      "v_max_f32 v44, 0, v44\n"
      "v_max_f32_e64 v46, 0, v46\n"
      "global_load_dwordx4 v[0:3], v235, s[88:89] offset:2048\n"
      "v_cvt_pk_bf16_f32 v44, v44, v46\n"
      "v_max_f32 v40, 0, v40\n"
      "v_max_f32 v48, 0, v48\n"
      "v_cvt_pk_bf16_f32 v40, v40, v48\n"
      "v_max_f32 v45, 0, v45\n"
      "v_max_f32 v43, 0, v43\n"
      "v_cvt_pk_bf16_f32 v45, v45, v43\n"
      "v_max_f32 v50, 0, v50\n"
      "v_max_f32 v41, 0, v41\n"
      "v_cvt_pk_bf16_f32 v50, v50, v41\n"
      "v_max_f32 v51, 0, v51\n"
      "v_max_f32 v54, 0, v54\n"
      "v_cvt_pk_bf16_f32 v51, v51, v54\n"
      "v_max_f32 v56, 0, v56\n"
      "v_max_f32 v63, 0, v63\n"
      "v_cvt_pk_bf16_f32 v56, v56, v63\n"
      "v_max_f32 v53, 0, v53\n"
      "v_max_f32 v57, 0, v57\n"
      "v_cvt_pk_bf16_f32 v53, v53, v57\n"
      "v_max_f32 v55, 0, v55\n"
      "v_max_f32 v70, 0, v70\n"
      "v_cvt_pk_bf16_f32 v55, v55, v70\n"
      "s_mov_b32 s42, 0\n"
      "s_add_u32 m0, s75, 0\n"
      "s_nop 0\n"
      "buffer_load_dwordx4 v106, s[20:23], s42 offen  lds\n"
      "s_pack_ll_b32_b16 s92, s48, s50\n"
      "v_dot2_f32_bf16 v41, s92, v44, 0\n"
      "s_pack_hh_b32_b16 s93, s48, s50\n"
      "v_dot2_f32_bf16 v43, s93, v44, 0\n"
      "s_pack_ll_b32_b16 s94, s49, s51\n"
      "v_dot2_f32_bf16 v46, s94, v44, 0\n"
      "s_pack_hh_b32_b16 s95, s49, s51\n"
      "v_dot2_f32_bf16 v48, s95, v44, 0\n"
      "s_pack_ll_b32_b16 s92, s52, s54\n"
      "v_dot2_f32_bf16 v41, s92, v40, v41\n"
      "s_pack_hh_b32_b16 s93, s52, s54\n"
      "v_dot2_f32_bf16 v43, s93, v40, v43\n"
      "s_pack_ll_b32_b16 s94, s53, s55\n"
      "v_dot2_f32_bf16 v46, s94, v40, v46\n"
      "s_pack_hh_b32_b16 s95, s53, s55\n"
      "v_dot2_f32_bf16 v48, s95, v40, v48\n"
      "s_pack_ll_b32_b16 s92, s56, s58\n"
      "v_dot2_f32_bf16 v41, s92, v45, v41\n"
      "s_pack_hh_b32_b16 s93, s56, s58\n"
      "v_dot2_f32_bf16 v43, s93, v45, v43\n"
      "s_pack_ll_b32_b16 s94, s57, s59\n"
      "v_dot2_f32_bf16 v46, s94, v45, v46\n"
      "s_add_u32 m0, s75, 3968\n"
      "s_nop 0\n"
      "buffer_load_dwordx4 v106, s[20:23], s42 offen offset:128  lds\n"
      "s_pack_hh_b32_b16 s95, s57, s59\n"
      "v_dot2_f32_bf16 v48, s95, v45, v48\n"
      "s_pack_ll_b32_b16 s92, s60, s62\n"
      "v_dot2_f32_bf16 v41, s92, v50, v41\n"
      "s_pack_hh_b32_b16 s93, s60, s62\n"
      "v_dot2_f32_bf16 v43, s93, v50, v43\n"
      "s_pack_ll_b32_b16 s94, s61, s63\n"
      "v_dot2_f32_bf16 v46, s94, v50, v46\n"
      "s_pack_hh_b32_b16 s95, s61, s63\n"
      "v_dot2_f32_bf16 v48, s95, v50, v48\n"
      "s_pack_ll_b32_b16 s92, s24, s26\n"
      "v_dot2_f32_bf16 v41, s92, v51, v41\n"
      "s_pack_hh_b32_b16 s93, s24, s26\n"
      "v_dot2_f32_bf16 v43, s93, v51, v43\n"
      "s_pack_ll_b32_b16 s94, s25, s27\n"
      "v_dot2_f32_bf16 v46, s94, v51, v46\n"
      "s_pack_hh_b32_b16 s95, s25, s27\n"
      "v_dot2_f32_bf16 v48, s95, v51, v48\n"
      "s_pack_ll_b32_b16 s92, s28, s30\n"
      "v_dot2_f32_bf16 v41, s92, v56, v41\n"
      "s_pack_hh_b32_b16 s93, s28, s30\n"
      "v_dot2_f32_bf16 v43, s93, v56, v43\n"
      "s_pack_ll_b32_b16 s94, s29, s31\n"
      "s_add_u32 m0, s75, 7936\n"
      "s_nop 0\n"
      "buffer_load_dwordx4 v106, s[20:23], s42 offen offset:256  lds\n"
      "v_dot2_f32_bf16 v46, s94, v56, v46\n"
      "s_pack_hh_b32_b16 s95, s29, s31\n"
      "v_dot2_f32_bf16 v48, s95, v56, v48\n"
      "s_pack_ll_b32_b16 s92, s32, s34\n"
      "v_dot2_f32_bf16 v41, s92, v53, v41\n"
      "s_pack_hh_b32_b16 s93, s32, s34\n"
      "v_dot2_f32_bf16 v43, s93, v53, v43\n"
      "s_pack_ll_b32_b16 s94, s33, s35\n"
      "v_dot2_f32_bf16 v46, s94, v53, v46\n"
      "s_pack_hh_b32_b16 s95, s33, s35\n"
      "v_dot2_f32_bf16 v48, s95, v53, v48\n"
      "s_pack_ll_b32_b16 s92, s36, s38\n"
      "v_dot2_f32_bf16 v41, s92, v55, v41\n"
      "s_pack_hh_b32_b16 s93, s36, s38\n"
      "v_dot2_f32_bf16 v43, s93, v55, v43\n"
      "s_pack_ll_b32_b16 s94, s37, s39\n"
      "v_dot2_f32_bf16 v46, s94, v55, v46\n"
      "s_pack_hh_b32_b16 s95, s37, s39\n"
      "v_dot2_f32_bf16 v48, s95, v55, v48\n"
      "s_load_dwordx2 s[28:29], s[90:91], 8\n"
      "s_load_dwordx2 s[34:35], s[90:91], 16\n"
      "s_load_dwordx2 s[24:25], s[90:91], 24\n"
      "s_add_u32 m0, s75, 11904\n"
      "s_nop 0\n"
      "buffer_load_dwordx4 v106, s[20:23], s42 offen offset:384  lds\n"
      "s_load_dwordx2 s[36:37], s[90:91], 40\n"
      "s_mov_b32 s26, 100270464\n"
      "s_mov_b32 s27, 0x20000\n"
      "s_mov_b32 s38, 73728\n"
      "s_mov_b32 s39, 0x20000\n"
      "s_mov_b32 s30, 25589860\n"
      "s_mov_b32 s31, 0x20000\n"
      "s_mov_b32 s53, 0x3fb8aa3b\n"
      "s_mov_b32 s51, 0x2a8cbccc\n"
      "s_mov_b32 s52, 0x38fba882\n"
      "s_mov_b32 s58, 0xffff0000\n"
      "s_mov_b32 s59, 0x0\n"
      "s_mov_b32 s60, 0x0\n"
      "s_mov_b32 s61, 0xffff\n"
      "s_mov_b32 s62, 0x0\n"
      "s_mov_b32 s63, 0xffff0000\n"
      "v_mov_b32 v42, 0x41f00000\n"
      "v_cmp_nle_f32 s[54:55], v49, v42\n"
      "v_mul_f32 v49, s53, v49\n"
      "s_nop 0\n"
      "v_readfirstlane_b32 s50, v49\n"
      "v_cvt_pk_bf16_f32 v41, v41, v41\n"
      "v_lshlrev_b32 v41, 16, v41\n"
      "s_add_u32 m0, s75, 15872\n"
      "s_nop 0\n"
      "buffer_load_dwordx4 v106, s[20:23], s42 offen offset:512  lds\n"
      "v_mul_f32 v41, 0xbfb8aa3b, v41\n"
      "v_exp_f32 v41, v41\n"
      "s_nop 0\n"
      "v_add_f32 v41, 1.0, v41\n"
      "v_rcp_f32 v41, v41\n"
      "s_nop 0\n"
      "v_cvt_pk_bf16_f32 v41, v41, v41\n"
      "v_lshlrev_b32 v41, 16, v41\n"
      "v_add_f32 v41, -0.5, v41\n"
      "v_mul_f32 v41, 0x41b8aa3b, v41\n"
      "ds_write_b32 v47, v41\n"
      "v_cvt_pk_bf16_f32 v43, v43, v43\n"
      "v_lshlrev_b32 v43, 16, v43\n"
      "v_mul_f32 v43, 0xbfb8aa3b, v43\n"
      "v_exp_f32 v43, v43\n"
      "s_nop 0\n"
      "v_add_f32 v43, 1.0, v43\n"
      "v_rcp_f32_e64 v43, v43\n"
      "s_nop 0\n"
      "v_cvt_pk_bf16_f32 v43, v43, v43\n"
      "v_lshlrev_b32 v43, 16, v43\n"
      "v_add_f32 v43, -0.5, v43\n"
      "v_mul_f32 v43, 0x41b8aa3b, v43\n"
      "ds_write_b32 v47, v43 offset:704\n"
      "v_cvt_pk_bf16_f32 v46, v46, v46\n"
      "v_lshlrev_b32 v46, 16, v46\n"
      "s_add_u32 m0, s75, 19840\n"
      "s_nop 0\n"
      "buffer_load_dwordx4 v106, s[20:23], s42 offen offset:640  lds\n"
      "v_mul_f32 v46, 0xbfb8aa3b, v46\n"
      "v_exp_f32 v46, v46\n"
      "s_nop 0\n"
      "v_add_f32 v46, 1.0, v46\n"
      "v_rcp_f32_e64 v46, v46\n"
      "s_nop 0\n"
      "v_cvt_pk_bf16_f32 v46, v46, v46\n"
      "v_lshlrev_b32 v46, 16, v46\n"
      "v_add_f32 v46, -0.5, v46\n"
      "v_mul_f32 v46, 0x41b8aa3b, v46\n"
      "ds_write_b32 v47, v46 offset:1408\n"
      "v_cvt_pk_bf16_f32 v48, v48, v48\n"
      "v_lshlrev_b32 v48, 16, v48\n"
      "v_mul_f32 v48, 0xbfb8aa3b, v48\n"
      "v_exp_f32 v48, v48\n"
      "s_nop 0\n"
      "v_add_f32 v48, 1.0, v48\n"
      "v_rcp_f32 v48, v48\n"
      "s_nop 0\n"
      "v_cvt_pk_bf16_f32 v48, v48, v48\n"
      "v_lshlrev_b32 v48, 16, v48\n"
      "v_add_f32 v48, -0.5, v48\n"
      "v_mul_f32 v48, 0x41b8aa3b, v48\n"
      "ds_write_b32 v47, v48 offset:2112\n"
      "s_mul_i32 s76, s40, 704\n"
      "s_add_u32 s76, s76, 122112\n"
      "s_waitcnt vmcnt(17)\n"
      "s_waitcnt lgkmcnt(0)\n"
      "s_barrier\n"
      "global_load_dwordx4 v[4:7], v235, s[88:89] offset:3072\n"
      "ds_read2_b32 v[40:41], v58 offset0:0 offset1:1\n"
      "ds_read2_b32 v[42:43], v58 offset0:2 offset1:3\n"
      "ds_read2_b32 v[44:45], v58 offset0:16 offset1:17\n"
      "ds_read2_b32 v[46:47], v58 offset0:18 offset1:19\n"
      "ds_read2_b32 v[48:49], v58 offset0:32 offset1:33\n"
      "ds_read2_b32 v[50:51], v58 offset0:34 offset1:35\n"
      "ds_read_b32 v232, v65 offset:192\n"
      "ds_read_b32 v242, v65 offset:3328\n"
      "s_add_u32 s88, s88, 4096\n"
      "s_addc_u32 s89, s89, 0\n"
      "global_load_dwordx4 v[8:11], v235, s[88:89]\n"
      "ds_read_b32 v246, v65 offset:6464\n"
      "ds_read_b32 v233, v68 offset:9408\n"
      "s_mov_b32 s72, 0xffff\n"
      "s_mov_b32 s73, 0x0\n"
      "s_mov_b32 s66, 0xffffffff\n"
      "s_mov_b32 s67, 0x1ffff\n"
      "v_add_u32 v65, 3136, v58\n"
      "global_load_dwordx4 v[12:15], v235, s[88:89] offset:1024\n"
      "v_add_u32 v234, 6272, v58\n"
      "ds_read2_b32 v[52:53], v65 offset0:0 offset1:1\n"
      "ds_read2_b32 v[54:55], v65 offset0:2 offset1:3\n"
      "ds_read2_b32 v[56:57], v65 offset0:16 offset1:17\n"
      "ds_read2_b32 v[58:59], v65 offset0:18 offset1:19\n"
      "ds_read2_b32 v[60:61], v65 offset0:32 offset1:33\n"
      "ds_read2_b32 v[62:63], v65 offset0:34 offset1:35\n"
      "global_load_dwordx4 v[20:23], v235, s[88:89] offset:2048\n"
      "ds_read2_b32 v[64:65], v234 offset0:0 offset1:1\n"
      "ds_read2_b32 v[66:67], v234 offset0:2 offset1:3\n"
      "ds_read2_b32 v[68:69], v234 offset0:16 offset1:17\n"
      "ds_read2_b32 v[70:71], v234 offset0:18 offset1:19\n"
      "ds_read2_b32 v[72:73], v234 offset0:32 offset1:33\n"
      "ds_read2_b32 v[74:75], v234 offset0:34 offset1:35\n"
      "s_waitcnt lgkmcnt(11)\n"
      "global_load_dwordx4 v[24:27], v235, s[88:89] offset:3072\n"
      "v_lshl_add_u32 v40, v40, 2, s76\n"
      "v_lshl_add_u32 v41, v41, 2, s76\n"
      "v_lshl_add_u32 v42, v42, 2, s76\n"
      "v_lshl_add_u32 v43, v43, 2, s76\n"
      "v_lshl_add_u32 v44, v44, 2, s76\n"
      "v_lshl_add_u32 v45, v45, 2, s76\n"
      "v_lshl_add_u32 v46, v46, 2, s76\n"
      "s_mov_b32 s43, 36864\n"
      "s_add_u32 m0, s75, 24576\n"
      "s_nop 0\n"
      "buffer_load_dwordx4 v106, s[20:23], s43 offen  lds\n"
      "v_lshl_add_u32 v47, v47, 2, s76\n"
      "v_lshl_add_u32 v48, v48, 2, s76\n"
      "v_lshl_add_u32 v49, v49, 2, s76\n"
      "v_lshl_add_u32 v50, v50, 2, s76\n"
      "v_lshl_add_u32 v51, v51, 2, s76\n"
      "v_lshl_add_u32 v52, v52, 2, s76\n"
      "v_lshl_add_u32 v53, v53, 2, s76\n"
      "s_add_u32 m0, s75, 28544\n"
      "s_nop 0\n"
      "buffer_load_dwordx4 v106, s[20:23], s43 offen offset:128  lds\n"
      "s_waitcnt lgkmcnt(4)\n"
      "v_lshl_add_u32 v54, v54, 2, s76\n"
      "v_lshl_add_u32 v55, v55, 2, s76\n"
      "v_lshl_add_u32 v56, v56, 2, s76\n"
      "v_lshl_add_u32 v57, v57, 2, s76\n"
      "v_lshl_add_u32 v58, v58, 2, s76\n"
      "v_lshl_add_u32 v59, v59, 2, s76\n"
      "s_add_u32 m0, s75, 32512\n"
      "s_nop 0\n"
      "buffer_load_dwordx4 v106, s[20:23], s43 offen offset:256  lds\n"
      "v_lshl_add_u32 v60, v60, 2, s76\n"
      "v_lshl_add_u32 v61, v61, 2, s76\n"
      "v_lshl_add_u32 v62, v62, 2, s76\n"
      "v_lshl_add_u32 v63, v63, 2, s76\n"
      "v_lshl_add_u32 v64, v64, 2, s76\n"
      "v_lshl_add_u32 v65, v65, 2, s76\n"
      "v_lshl_add_u32 v66, v66, 2, s76\n"
      "s_add_u32 m0, s75, 36480\n"
      "s_nop 0\n"
      "buffer_load_dwordx4 v106, s[20:23], s43 offen offset:384  lds\n"
      "v_lshl_add_u32 v67, v67, 2, s76\n"
      "s_waitcnt lgkmcnt(0)\n"
      "v_lshl_add_u32 v68, v68, 2, s76\n"
      "v_lshl_add_u32 v69, v69, 2, s76\n"
      "v_lshl_add_u32 v70, v70, 2, s76\n"
      "v_lshl_add_u32 v71, v71, 2, s76\n"
      "v_lshl_add_u32 v72, v72, 2, s76\n"
      "s_add_u32 m0, s75, 40448\n"
      "s_nop 0\n"
      "buffer_load_dwordx4 v106, s[20:23], s43 offen offset:512  lds\n"
      "v_lshl_add_u32 v73, v73, 2, s76\n"
      "v_lshl_add_u32 v74, v74, 2, s76\n"
      "v_lshl_add_u32 v75, v75, 2, s76\n"
      "v_lshl_add_u32 v232, v232, 2, s76\n"
      "v_lshl_add_u32 v242, v242, 2, s76\n"
      "v_lshl_add_u32 v246, v246, 2, s76\n"
      "v_lshl_add_u32 v233, v233, 2, s76\n"
      "s_add_u32 m0, s75, 44416\n"
      "s_nop 0\n"
      "buffer_load_dwordx4 v106, s[20:23], s43 offen offset:640  lds\n"
      "ds_read_b32 v40, v40\n"
      "ds_read_b32 v41, v41\n"
      "ds_read_b32 v42, v42\n"
      "ds_read_b32 v43, v43\n"
      "ds_read_b32 v44, v44\n"
      "ds_read_b32 v45, v45\n"
      "ds_read_b32 v46, v46\n"
      "s_mov_b32 s42, 73728\n"
      "s_add_u32 m0, s75, 49152\n"
      "s_nop 0\n"
      "buffer_load_dwordx4 v106, s[20:23], s42 offen  lds\n"
      "ds_read_b32 v47, v47\n"
      "ds_read_b32 v48, v48\n"
      "ds_read_b32 v49, v49\n"
      "ds_read_b32 v50, v50\n"
      "ds_read_b32 v51, v51\n"
      "ds_read_b32 v52, v52\n"
      "ds_read_b32 v53, v53\n"
      "s_add_u32 m0, s75, 53120\n"
      "s_nop 0\n"
      "buffer_load_dwordx4 v106, s[20:23], s42 offen offset:128  lds\n"
      "ds_read_b32 v54, v54\n"
      "ds_read_b32 v55, v55\n"
      "ds_read_b32 v56, v56\n"
      "ds_read_b32 v57, v57\n"
      "ds_read_b32 v58, v58\n"
      "ds_read_b32 v59, v59\n"
      "ds_read_b32 v60, v60\n"
      "s_add_u32 m0, s75, 57088\n"
      "s_nop 0\n"
      "buffer_load_dwordx4 v106, s[20:23], s42 offen offset:256  lds\n"
      "ds_read_b32 v61, v61\n"
      "ds_read_b32 v62, v62\n"
      "ds_read_b32 v63, v63\n"
      "ds_read_b32 v64, v64\n"
      "ds_read_b32 v65, v65\n"
      "ds_read_b32 v66, v66\n"
      "ds_read_b32 v67, v67\n"
      "s_add_u32 m0, s75, 61056\n"
      "s_nop 0\n"
      "buffer_load_dwordx4 v106, s[20:23], s42 offen offset:384  lds\n"
      "ds_read_b32 v68, v68\n"
      "ds_read_b32 v69, v69\n"
      "ds_read_b32 v70, v70\n"
      "ds_read_b32 v71, v71\n"
      "ds_read_b32 v72, v72\n"
      "ds_read_b32 v73, v73\n"
      "ds_read_b32 v74, v74\n"
      "s_add_u32 m0, s75, 65024\n"
      "s_nop 0\n"
      "buffer_load_dwordx4 v106, s[20:23], s42 offen offset:512  lds\n"
      "ds_read_b32 v75, v75\n"
      "ds_read_b32 v232, v232\n"
      "ds_read_b32 v242, v242\n"
      "ds_read_b32 v246, v246\n"
      "ds_read_b32 v233, v233\n"
      "v_mov_b32 v234, 0xf149f2ca\n"
      "s_waitcnt lgkmcnt(3)\n"
      "s_add_u32 m0, s75, 68992\n"
      "s_nop 0\n"
      "buffer_load_dwordx4 v106, s[20:23], s42 offen offset:640  lds\n"
      "v_cndmask_b32 v235, v234, v232, s[72:73]\n"
      "s_waitcnt lgkmcnt(2)\n"
      "v_cndmask_b32 v232, v234, v242, s[72:73]\n"
      "s_waitcnt lgkmcnt(1)\n"
      "v_cndmask_b32 v28, v234, v246, s[72:73]\n"
      "s_waitcnt lgkmcnt(0)\n"
      "v_cndmask_b32 v30, v234, v233, s[66:67]\n"
      "s_mov_b32 s45, s46\n"
      "s_mov_b32 s49, s47\n"
      "s_mov_b32 s66, 0\n"
      "s_mov_b32 s67, 0\n"
      "s_mov_b32 s68, 0\n"
      "s_mov_b32 s69, 0\n"
      "s_mov_b32 s70, 0\n"
      "s_mov_b32 s71, 0\n"
      "s_mov_b32 s48, 0\n"
      "s_mov_b32 s42, 1\n"
      "s_mov_b32 s43, 5\n"
      "v_readlane_b32 s47, v38, 4\n"
      "s_nop 0\n"
      "s_and_b32 s47, s47, -4\n"
      "s_waitcnt vmcnt(23)\n"
      "v_add_u32 v189, 149504, v0\n"
      "v_add_u32 v188, 149504, v1\n"
      "v_add_u32 v196, 49152, v236\n"
      "v_add_u32 v197, 49152, v237\n"
      "v_add_u32 v194, 49152, v238\n"
      "v_add_u32 v132, 49152, v239\n"
      "v_add_u32 v133, 49152, v146\n"
      "s_waitcnt vmcnt(12)\n"
      "v_mov_b32_e64 v24, v235\n"
      "v_mov_b32_e64 v26, v232\n"
      "s_waitcnt lgkmcnt(0)\n"
      "s_barrier\n"
      "ds_read_b128 a[216:219], v189\n"
      "ds_read_b128 a[220:223], v0\n"
      "s_add_u32 m0, s75, 73728\n"
      "s_mov_b32 s79, 110592\n"
      "buffer_load_dwordx4 v106, s[20:23], s79 offen  lds\n"
      "s_add_u32 m0, s75, 77696\n"
      "ds_read_b64_tr_b16 a[0:1], v236\n"
      "ds_read_b64_tr_b16 a[2:3], v236 offset:512\n"
      "ds_read_b64_tr_b16 a[24:25], v237\n"
      "ds_read_b64_tr_b16 a[26:27], v237 offset:512\n"
      "ds_read_b64_tr_b16 a[48:49], v238\n"
      "ds_read_b64_tr_b16 a[50:51], v238 offset:512\n"
      "ds_read_b64_tr_b16 a[72:73], v236 offset:12288\n"
      "ds_read_b64_tr_b16 a[74:75], v236 offset:12800\n"
      "ds_read_b64_tr_b16 a[96:97], v237 offset:12288\n"
      "ds_read_b64_tr_b16 a[98:99], v237 offset:12800\n"
      "ds_read_b64_tr_b16 a[120:121], v238 offset:12288\n"
      "ds_read_b64_tr_b16 a[122:123], v238 offset:12800\n"
      "buffer_load_dwordx4 v106, s[20:23], s79 offen offset:128  lds\n"
      "s_waitcnt lgkmcnt(9)\n"
      "v_mfma_f32_16x16x32_bf16 v[232:235], a[0:3], a[216:219], 0\n"
      "s_waitcnt lgkmcnt(8)\n"
      "s_add_u32 m0, s75, 81664\n"
      "ds_read_b128 a[224:227], v0 offset:2048\n"
      "v_mfma_f32_16x16x32_bf16 v[240:243], a[24:27], a[216:219], 0\n"
      "s_waitcnt lgkmcnt(7)\n"
      "v_readlane_b32 s46, v37, s42\n"
      "v_mfma_f32_16x16x32_bf16 v[244:247], a[48:51], a[216:219], 0\n"
      "s_waitcnt lgkmcnt(5)\n"
      "buffer_load_dwordx4 v106, s[20:23], s79 offen offset:256  lds\n"
      "v_mfma_f32_16x16x32_bf16 v[134:137], a[72:75], a[216:219], 0\n"
      "s_waitcnt lgkmcnt(2)\n"
      "s_add_u32 m0, s75, 85632\n"
      "v_mfma_f32_16x16x32_bf16 v[210:213], a[96:99], a[216:219], 0\n"
      "s_waitcnt lgkmcnt(1)\n"
      "v_mfma_f32_16x16x32_bf16 v[252:255], a[120:123], a[216:219], 0\n"
      "v_mfma_f32_16x16x32_bf16 v[96:99], a[0:3], a[220:223], 0\n"
      "buffer_load_dwordx4 v106, s[20:23], s79 offen offset:384  lds\n"
      "v_mfma_f32_16x16x32_bf16 v[124:127], a[24:27], a[220:223], 0\n"
      "s_add_u32 m0, s75, 89600\n"
      "v_mfma_f32_16x16x32_bf16 v[202:205], a[48:51], a[220:223], 0\n"
      "s_waitcnt lgkmcnt(0)\n"
      "v_mfma_f32_16x16x32_bf16 v[138:141], a[72:75], a[220:223], 0\n"
      "ds_read_b128 a[228:231], v188\n"
      "v_mfma_f32_16x16x32_bf16 v[206:209], a[96:99], a[220:223], 0\n"
      "buffer_load_dwordx4 v106, s[20:23], s79 offen offset:512  lds\n"
      "v_mfma_f32_16x16x32_bf16 v[174:177], a[120:123], a[220:223], 0\n"
      "s_add_u32 m0, s75, 93568\n"
      "v_mfma_f32_16x16x32_bf16 v[120:123], a[0:3], a[224:227], 0\n"
      "v_mfma_f32_16x16x32_bf16 v[166:169], a[24:27], a[224:227], 0\n"
      "v_mfma_f32_16x16x32_bf16 v[222:225], a[48:51], a[224:227], 0\n"
      "buffer_load_dwordx4 v106, s[20:23], s79 offen offset:640  lds\n"
      "v_mfma_f32_16x16x32_bf16 v[108:111], a[72:75], a[224:227], 0\n"
      "s_waitcnt vmcnt(12)\n"
      "s_barrier\n"
      "s_add_u32 m0, s75, 0\n"
      "v_mfma_f32_16x16x32_bf16 v[190:193], a[96:99], a[224:227], 0\n"
      "s_mov_b32 s78, 147456\n"
      "ds_read_b64_tr_b16 a[4:5], v236 offset:24576\n"
      "v_mfma_f32_16x16x32_bf16 v[112:115], a[120:123], a[224:227], 0\n"
      "s_waitcnt lgkmcnt(1)\n"
      "ds_read_b64_tr_b16 a[6:7], v236 offset:25088\n"
      "ds_read_b64_tr_b16 a[28:29], v237 offset:24576\n"
      "ds_read_b64_tr_b16 a[30:31], v237 offset:25088\n"
      "ds_read_b64_tr_b16 a[52:53], v238 offset:24576\n"
      "ds_read_b64_tr_b16 a[54:55], v238 offset:25088\n"
      "ds_read_b64_tr_b16 a[76:77], v236 offset:36864\n"
      "ds_read_b64_tr_b16 a[78:79], v236 offset:37376\n"
      "ds_read_b64_tr_b16 a[100:101], v237 offset:36864\n"
      "ds_read_b64_tr_b16 a[102:103], v237 offset:37376\n"
      "ds_read_b64_tr_b16 a[124:125], v238 offset:36864\n"
      "ds_read_b64_tr_b16 a[126:127], v238 offset:37376\n"
      "ds_read_b128 a[232:235], v1\n"
      "s_waitcnt lgkmcnt(11)\n"
      "v_mfma_f32_16x16x32_bf16 v[232:235], a[4:7], a[228:231], v[232:235]\n"
      "s_waitcnt lgkmcnt(10)\n"
      "buffer_load_dwordx4 v106, s[20:23], s78 offen  lds\n"
      "s_waitcnt lgkmcnt(8)\n"
      "v_mfma_f32_16x16x32_bf16 v[240:243], a[28:31], a[228:231], v[240:243]\n"
      "s_waitcnt lgkmcnt(7)\n"
      "s_add_u32 m0, s75, 3968\n"
      "ds_read_b128 a[236:239], v1 offset:2048\n"
      "v_mfma_f32_16x16x32_bf16 v[244:247], a[52:55], a[228:231], v[244:247]\n"
      "s_waitcnt lgkmcnt(5)\n"
      "v_mfma_f32_16x16x32_bf16 v[134:137], a[76:79], a[228:231], v[134:137]\n"
      "s_waitcnt lgkmcnt(3)\n"
      "buffer_load_dwordx4 v106, s[20:23], s78 offen offset:128  lds\n"
      "v_mfma_f32_16x16x32_bf16 v[210:213], a[100:103], a[228:231], v[210:213]\n"
      "s_waitcnt lgkmcnt(1)\n"
      "s_add_u32 m0, s75, 7936\n"
      "v_mfma_f32_16x16x32_bf16 v[252:255], a[124:127], a[228:231], v[252:255]\n"
      "v_mfma_f32_16x16x32_bf16 v[96:99], a[4:7], a[232:235], v[96:99]\n"
      "v_mfma_f32_16x16x32_bf16 v[124:127], a[28:31], a[232:235], v[124:127]\n"
      "buffer_load_dwordx4 v106, s[20:23], s78 offen offset:256  lds\n"
      "v_mfma_f32_16x16x32_bf16 v[202:205], a[52:55], a[232:235], v[202:205]\n"
      "s_add_u32 m0, s75, 11904\n"
      "v_mfma_f32_16x16x32_bf16 v[138:141], a[76:79], a[232:235], v[138:141]\n"
      "ds_read_b128 a[240:243], v189 offset:4096\n"
      "v_mfma_f32_16x16x32_bf16 v[206:209], a[100:103], a[232:235], v[206:209]\n"
      "s_waitcnt lgkmcnt(1)\n"
      "v_mfma_f32_16x16x32_bf16 v[174:177], a[124:127], a[232:235], v[174:177]\n"
      "buffer_load_dwordx4 v106, s[20:23], s78 offen offset:384  lds\n"
      "v_mfma_f32_16x16x32_bf16 v[120:123], a[4:7], a[236:239], v[120:123]\n"
      "s_add_u32 m0, s75, 15872\n"
      "s_waitcnt vmcnt(10)\n"
      "s_barrier\n"
      "v_mfma_f32_16x16x32_bf16 v[166:169], a[28:31], a[236:239], v[166:169]\n"
      "s_mov_b32 s79, 184320\n"
      "ds_read_b64_tr_b16 a[8:9], v196\n"
      "v_mfma_f32_16x16x32_bf16 v[222:225], a[52:55], a[236:239], v[222:225]\n"
      "ds_read_b64_tr_b16 a[10:11], v196 offset:512\n"
      "ds_read_b64_tr_b16 a[32:33], v197\n"
      "v_mfma_f32_16x16x32_bf16 v[108:111], a[76:79], a[236:239], v[108:111]\n"
      "buffer_load_dwordx4 v106, s[20:23], s78 offen offset:512  lds\n"
      "v_mfma_f32_16x16x32_bf16 v[190:193], a[100:103], a[236:239], v[190:193]\n"
      "s_add_u32 m0, s75, 19840\n"
      "ds_read_b64_tr_b16 a[34:35], v197 offset:512\n"
      "v_mfma_f32_16x16x32_bf16 v[112:115], a[124:127], a[236:239], v[112:115]\n"
      "s_waitcnt lgkmcnt(4)\n"
      "ds_read_b64_tr_b16 a[56:57], v194\n"
      "ds_read_b64_tr_b16 a[58:59], v194 offset:512\n"
      "ds_read_b64_tr_b16 a[80:81], v196 offset:12288\n"
      "ds_read_b64_tr_b16 a[82:83], v196 offset:12800\n"
      "ds_read_b64_tr_b16 a[104:105], v197 offset:12288\n"
      "s_waitcnt lgkmcnt(7)\n"
      "v_mfma_f32_16x16x32_bf16 v[232:235], a[8:11], a[240:243], v[232:235]\n"
      "s_waitcnt lgkmcnt(6)\n"
      "ds_read_b64_tr_b16 a[106:107], v197 offset:12800\n"
      "ds_read_b64_tr_b16 a[128:129], v194 offset:12288\n"
      "ds_read_b64_tr_b16 a[130:131], v194 offset:12800\n"
      "ds_read_b128 a[244:247], v0 offset:6144\n"
      "s_waitcnt lgkmcnt(9)\n"
      "v_mfma_f32_16x16x32_bf16 v[240:243], a[32:35], a[240:243], v[240:243]\n"
      "buffer_load_dwordx4 v106, s[20:23], s78 offen offset:640  lds\n"
      "s_waitcnt lgkmcnt(7)\n"
      "v_mfma_f32_16x16x32_bf16 v[244:247], a[56:59], a[240:243], v[244:247]\n"
      "s_waitcnt lgkmcnt(6)\n"
      "s_add_u32 m0, s75, 24576\n"
      "ds_read_b128 a[248:251], v0 offset:8192\n"
      "s_waitcnt lgkmcnt(5)\n"
      "v_mfma_f32_16x16x32_bf16 v[134:137], a[80:83], a[240:243], v[134:137]\n"
      "s_waitcnt lgkmcnt(4)\n"
      "v_mfma_f32_16x16x32_bf16 v[210:213], a[104:107], a[240:243], v[210:213]\n"
      "s_waitcnt lgkmcnt(3)\n"
      "buffer_load_dwordx4 v106, s[20:23], s79 offen  lds\n"
      "s_waitcnt lgkmcnt(1)\n"
      "v_mfma_f32_16x16x32_bf16 v[252:255], a[128:131], a[240:243], v[252:255]\n"
      "s_add_u32 m0, s75, 28544\n"
      "v_mfma_f32_16x16x32_bf16 v[96:99], a[8:11], a[244:247], v[96:99]\n"
      "v_mfma_f32_16x16x32_bf16 v[124:127], a[32:35], a[244:247], v[124:127]\n"
      "v_mfma_f32_16x16x32_bf16 v[202:205], a[56:59], a[244:247], v[202:205]\n"
      "buffer_load_dwordx4 v106, s[20:23], s79 offen offset:128  lds\n"
      "v_mfma_f32_16x16x32_bf16 v[138:141], a[80:83], a[244:247], v[138:141]\n"
      "s_add_u32 m0, s75, 32512\n"
      "ds_read_b128 a[252:255], v188 offset:4096\n"
      "v_mfma_f32_16x16x32_bf16 v[206:209], a[104:107], a[244:247], v[206:209]\n"
      "s_waitcnt lgkmcnt(1)\n"
      "v_mfma_f32_16x16x32_bf16 v[174:177], a[128:131], a[244:247], v[174:177]\n"
      "v_mfma_f32_16x16x32_bf16 v[120:123], a[8:11], a[248:251], v[120:123]\n"
      "buffer_load_dwordx4 v106, s[20:23], s79 offen offset:256  lds\n"
      "v_mfma_f32_16x16x32_bf16 v[166:169], a[32:35], a[248:251], v[166:169]\n"
      "s_add_u32 m0, s75, 36480\n"
      "v_mfma_f32_16x16x32_bf16 v[222:225], a[56:59], a[248:251], v[222:225]\n"
      "v_mfma_f32_16x16x32_bf16 v[108:111], a[80:83], a[248:251], v[108:111]\n"
      "ds_read_b128 a[216:219], v1 offset:6144\n"
      "v_mfma_f32_16x16x32_bf16 v[190:193], a[104:107], a[248:251], v[190:193]\n"
      "buffer_load_dwordx4 v106, s[20:23], s79 offen offset:384  lds\n"
      "v_mfma_f32_16x16x32_bf16 v[112:115], a[128:131], a[248:251], v[112:115]\n"
      "s_waitcnt lgkmcnt(1)\n"
      "s_add_u32 m0, s75, 40448\n"
      "ds_read_b128 a[220:223], v1 offset:8192\n"
      "buffer_load_dwordx4 v106, s[20:23], s79 offen offset:512  lds\n"
      "s_add_u32 m0, s75, 44416\n"
      "s_nop 0\n"
      "buffer_load_dwordx4 v106, s[20:23], s79 offen offset:640  lds\n"
      "s_waitcnt vmcnt(12)\n"
      "s_barrier\n"
      "ds_read_b64_tr_b16 a[12:13], v196 offset:24576\n"
      "ds_read_b64_tr_b16 a[14:15], v196 offset:25088\n"
      "ds_read_b64_tr_b16 a[36:37], v197 offset:24576\n"
      "ds_read_b64_tr_b16 a[38:39], v197 offset:25088\n"
      "ds_read_b64_tr_b16 a[60:61], v194 offset:24576\n"
      "ds_read_b64_tr_b16 a[62:63], v194 offset:25088\n"
      "ds_read_b64_tr_b16 a[84:85], v196 offset:36864\n"
      "ds_read_b64_tr_b16 a[86:87], v196 offset:37376\n"
      "ds_read_b64_tr_b16 a[108:109], v197 offset:36864\n"
      "ds_read_b64_tr_b16 a[110:111], v197 offset:37376\n"
      "ds_read_b64_tr_b16 a[132:133], v194 offset:36864\n"
      "ds_read_b64_tr_b16 a[134:135], v194 offset:37376\n"
      "s_add_u32 m0, s75, 48384\n"
      "s_mov_b32 s78, 0\n"
      "s_waitcnt lgkmcnt(10)\n"
      "v_mfma_f32_16x16x32_bf16 v[232:235], a[12:15], a[252:255], v[232:235]\n"
      "s_waitcnt lgkmcnt(9)\n"
      "buffer_load_dwordx4 v106, s[20:23], s78 offen offset:768  lds\n"
      "s_waitcnt lgkmcnt(7)\n"
      "v_mfma_f32_16x16x32_bf16 v[240:243], a[36:39], a[252:255], v[240:243]\n"
      "s_waitcnt lgkmcnt(6)\n"
      "s_add_u32 m0, s75, 52352\n"
      "v_mfma_f32_16x16x32_bf16 v[244:247], a[60:63], a[252:255], v[244:247]\n"
      "s_waitcnt lgkmcnt(4)\n"
      "v_mfma_f32_16x16x32_bf16 v[134:137], a[84:87], a[252:255], v[134:137]\n"
      "s_waitcnt lgkmcnt(2)\n"
      "v_mfma_f32_16x16x32_bf16 v[210:213], a[108:111], a[252:255], v[210:213]\n"
      "s_waitcnt lgkmcnt(0)\n"
      "buffer_load_dwordx4 v106, s[20:23], s78 offen offset:896  lds\n"
      "v_mfma_f32_16x16x32_bf16 v[252:255], a[132:135], a[252:255], v[252:255]\n"
      "s_add_u32 m0, s75, 56320\n"
      "v_mfma_f32_16x16x32_bf16 v[96:99], a[12:15], a[216:219], v[96:99]\n"
      "v_mfma_f32_16x16x32_bf16 v[124:127], a[36:39], a[216:219], v[124:127]\n"
      "v_mfma_f32_16x16x32_bf16 v[202:205], a[60:63], a[216:219], v[202:205]\n"
      "buffer_load_dwordx4 v106, s[20:23], s78 offen offset:1024  lds\n"
      "v_mfma_f32_16x16x32_bf16 v[138:141], a[84:87], a[216:219], v[138:141]\n"
      "s_add_u32 m0, s75, 60672\n"
      "s_mov_b32 s78, 36864\n"
      "ds_read_b128 a[224:227], v189 offset:8192\n"
      "v_mfma_f32_16x16x32_bf16 v[206:209], a[108:111], a[216:219], v[206:209]\n"
      "v_mfma_f32_16x16x32_bf16 v[174:177], a[132:135], a[216:219], v[174:177]\n"
      "v_mfma_f32_16x16x32_bf16 v[120:123], a[12:15], a[220:223], v[120:123]\n"
      "buffer_load_dwordx4 v106, s[20:23], s78 offen offset:768  lds\n"
      "v_mfma_f32_16x16x32_bf16 v[166:169], a[36:39], a[220:223], v[166:169]\n"
      "s_add_u32 m0, s75, 64640\n"
      "v_mfma_f32_16x16x32_bf16 v[222:225], a[60:63], a[220:223], v[222:225]\n"
      "v_mfma_f32_16x16x32_bf16 v[108:111], a[84:87], a[220:223], v[108:111]\n"
      "ds_read_b128 a[228:231], v0 offset:12288\n"
      "v_mfma_f32_16x16x32_bf16 v[190:193], a[108:111], a[220:223], v[190:193]\n"
      "buffer_load_dwordx4 v106, s[20:23], s78 offen offset:896  lds\n"
      "v_mfma_f32_16x16x32_bf16 v[112:115], a[132:135], a[220:223], v[112:115]\n"
      "s_waitcnt lgkmcnt(1)\n"
      "s_add_u32 m0, s75, 68608\n"
      "ds_read_b128 a[232:235], v0 offset:14336\n"
      "buffer_load_dwordx4 v106, s[20:23], s78 offen offset:1024  lds\n"
      "s_add_u32 m0, s75, 72960\n"
      "s_waitcnt vmcnt(12)\n"
      "s_barrier\n"
      "ds_read_b64_tr_b16 a[16:17], v236\n"
      "ds_read_b64_tr_b16 a[18:19], v236 offset:512\n"
      "ds_read_b64_tr_b16 a[40:41], v237\n"
      "ds_read_b64_tr_b16 a[42:43], v237 offset:512\n"
      "ds_read_b64_tr_b16 a[64:65], v238\n"
      "ds_read_b64_tr_b16 a[66:67], v238 offset:512\n"
      "ds_read_b64_tr_b16 a[88:89], v236 offset:12288\n"
      "ds_read_b64_tr_b16 a[90:91], v236 offset:12800\n"
      "ds_read_b64_tr_b16 a[112:113], v237 offset:12288\n"
      "ds_read_b64_tr_b16 a[114:115], v237 offset:12800\n"
      "ds_read_b64_tr_b16 a[136:137], v238 offset:12288\n"
      "ds_read_b64_tr_b16 a[138:139], v238 offset:12800\n"
      "s_mov_b32 s79, 73728\n"
      "buffer_load_dwordx4 v106, s[20:23], s79 offen offset:768  lds\n"
      "s_waitcnt lgkmcnt(9)\n"
      "v_mfma_f32_16x16x32_bf16 v[232:235], a[16:19], a[224:227], v[232:235]\n"
      "s_waitcnt lgkmcnt(8)\n"
      "s_add_u32 m0, s75, 76928\n"
      "v_mfma_f32_16x16x32_bf16 v[240:243], a[40:43], a[224:227], v[240:243]\n"
      "s_waitcnt lgkmcnt(6)\n"
      "v_mfma_f32_16x16x32_bf16 v[244:247], a[64:67], a[224:227], v[244:247]\n"
      "s_waitcnt lgkmcnt(4)\n"
      "v_mfma_f32_16x16x32_bf16 v[134:137], a[88:91], a[224:227], v[134:137]\n"
      "s_waitcnt lgkmcnt(2)\n"
      "buffer_load_dwordx4 v106, s[20:23], s79 offen offset:896  lds\n"
      "v_mfma_f32_16x16x32_bf16 v[210:213], a[112:115], a[224:227], v[210:213]\n"
      "s_waitcnt lgkmcnt(0)\n"
      "s_add_u32 m0, s75, 80896\n"
      "v_mfma_f32_16x16x32_bf16 v[252:255], a[136:139], a[224:227], v[252:255]\n"
      "v_mfma_f32_16x16x32_bf16 v[96:99], a[16:19], a[228:231], v[96:99]\n"
      "v_mfma_f32_16x16x32_bf16 v[124:127], a[40:43], a[228:231], v[124:127]\n"
      "buffer_load_dwordx4 v106, s[20:23], s79 offen offset:1024  lds\n"
      "v_mfma_f32_16x16x32_bf16 v[202:205], a[64:67], a[228:231], v[202:205]\n"
      "s_add_u32 m0, s75, 85248\n"
      "s_mov_b32 s79, 110592\n"
      "v_mfma_f32_16x16x32_bf16 v[138:141], a[88:91], a[228:231], v[138:141]\n"
      "ds_read_b128 a[236:239], v188 offset:8192\n"
      "v_mfma_f32_16x16x32_bf16 v[206:209], a[112:115], a[228:231], v[206:209]\n"
      "v_mfma_f32_16x16x32_bf16 v[174:177], a[136:139], a[228:231], v[174:177]\n"
      "buffer_load_dwordx4 v106, s[20:23], s79 offen offset:768  lds\n"
      "v_mfma_f32_16x16x32_bf16 v[120:123], a[16:19], a[232:235], v[120:123]\n"
      "s_add_u32 m0, s75, 89216\n"
      "v_mfma_f32_16x16x32_bf16 v[166:169], a[40:43], a[232:235], v[166:169]\n"
      "v_mfma_f32_16x16x32_bf16 v[222:225], a[64:67], a[232:235], v[222:225]\n"
      "v_mfma_f32_16x16x32_bf16 v[108:111], a[88:91], a[232:235], v[108:111]\n"
      "buffer_load_dwordx4 v106, s[20:23], s79 offen offset:896  lds\n"
      "v_mfma_f32_16x16x32_bf16 v[190:193], a[112:115], a[232:235], v[190:193]\n"
      "s_add_u32 m0, s75, 93184\n"
      "ds_read_b128 a[240:243], v1 offset:12288\n"
      "v_mfma_f32_16x16x32_bf16 v[112:115], a[136:139], a[232:235], v[112:115]\n"
      "s_waitcnt lgkmcnt(1)\n"
      "ds_read_b128 a[244:247], v1 offset:14336\n"
      "buffer_load_dwordx4 v106, s[20:23], s79 offen offset:1024  lds\n"
      "s_add_u32 m0, s75, -768\n"
      "s_waitcnt vmcnt(12)\n"
      "s_barrier\n"
      "ds_read_b64_tr_b16 a[20:21], v236 offset:24576\n"
      "ds_read_b64_tr_b16 a[22:23], v236 offset:25088\n"
      "ds_read_b64_tr_b16 a[44:45], v237 offset:24576\n"
      "ds_read_b64_tr_b16 a[46:47], v237 offset:25088\n"
      "ds_read_b64_tr_b16 a[68:69], v238 offset:24576\n"
      "ds_read_b64_tr_b16 a[70:71], v238 offset:25088\n"
      "ds_read_b64_tr_b16 a[92:93], v236 offset:36864\n"
      "ds_read_b64_tr_b16 a[94:95], v236 offset:37376\n"
      "ds_read_b64_tr_b16 a[116:117], v237 offset:36864\n"
      "ds_read_b64_tr_b16 a[118:119], v237 offset:37376\n"
      "ds_read_b64_tr_b16 a[140:141], v238 offset:36864\n"
      "ds_read_b64_tr_b16 a[142:143], v238 offset:37376\n"
      "s_mov_b32 s78, 147456\n"
      "buffer_load_dwordx4 v106, s[20:23], s78 offen offset:768  lds\n"
      "s_waitcnt lgkmcnt(9)\n"
      "v_mfma_f32_16x16x32_bf16 v[232:235], a[20:23], a[236:239], v[232:235]\n"
      "s_waitcnt lgkmcnt(8)\n"
      "s_add_u32 m0, s75, 3200\n"
      "v_mfma_f32_16x16x32_bf16 v[240:243], a[44:47], a[236:239], v[240:243]\n"
      "s_waitcnt lgkmcnt(6)\n"
      "v_mfma_f32_16x16x32_bf16 v[244:247], a[68:71], a[236:239], v[244:247]\n"
      "s_waitcnt lgkmcnt(4)\n"
      "v_mfma_f32_16x16x32_bf16 v[134:137], a[92:95], a[236:239], v[134:137]\n"
      "s_waitcnt lgkmcnt(2)\n"
      "buffer_load_dwordx4 v106, s[20:23], s78 offen offset:896  lds\n"
      "v_mfma_f32_16x16x32_bf16 v[210:213], a[116:119], a[236:239], v[210:213]\n"
      "s_waitcnt lgkmcnt(0)\n"
      "s_add_u32 m0, s75, 7168\n"
      "ds_read_b128 a[248:251], v0 offset:4096\n"
      "v_mfma_f32_16x16x32_bf16 v[252:255], a[140:143], a[236:239], v[252:255]\n"
      "ds_read_b128 a[252:255], v1 offset:4096\n"
      "v_fma_f32 v199, v232, v232, s51\n"
      "v_mfma_f32_16x16x32_bf16 v[96:99], a[20:23], a[240:243], v[96:99]\n"
      "ds_read_b128 a[216:219], v0 offset:10240\n"
      "buffer_load_dwordx4 v106, s[20:23], s78 offen offset:1024  lds\n"
      "v_mfma_f32_16x16x32_bf16 v[124:127], a[44:47], a[240:243], v[124:127]\n"
      "s_add_u32 m0, s75, 11520\n"
      "s_mov_b32 s78, 184320\n"
      "ds_read_b128 a[220:223], v1 offset:10240\n"
      "v_mfma_f32_16x16x32_bf16 v[202:205], a[68:71], a[240:243], v[202:205]\n"
      "ds_read_b128 a[224:227], v0 offset:16384\n"
      "v_fma_f32 v187, v134, v134, s51\n"
      "v_mfma_f32_16x16x32_bf16 v[138:141], a[92:95], a[240:243], v[138:141]\n"
      "ds_read_b128 a[228:231], v1 offset:16384\n"
      "buffer_load_dwordx4 v106, s[20:23], s78 offen offset:768  lds\n"
      "v_mfma_f32_16x16x32_bf16 v[206:209], a[116:119], a[240:243], v[206:209]\n"
      "s_add_u32 m0, s75, 15488\n"
      "v_fmac_f32 v199, v233, v233\n"
      "v_fmac_f32 v187, v135, v135\n"
      "v_mfma_f32_16x16x32_bf16 v[174:177], a[140:143], a[240:243], v[174:177]\n"
      "s_lshl_b32 s92, s40, 6\n"
      "v_fmac_f32 v199, v234, v234\n"
      "v_fmac_f32_e64 v187, v136, v136\n"
      "v_mfma_f32_16x16x32_bf16 v[120:123], a[20:23], a[244:247], v[120:123]\n"
      "v_fmac_f32 v199, v235, v235\n"
      "v_fmac_f32 v187, v137, v137\n"
      "v_mfma_f32_16x16x32_bf16 v[166:169], a[44:47], a[244:247], v[166:169]\n"
      "buffer_load_dwordx4 v106, s[20:23], s78 offen offset:896  lds\n"
      "v_mfma_f32_16x16x32_bf16 v[222:225], a[68:71], a[244:247], v[222:225]\n"
      "s_add_u32 m0, s75, 19456\n"
      "v_fmac_f32 v199, v240, v240\n"
      "v_fmac_f32 v187, v210, v210\n"
      "v_mfma_f32_16x16x32_bf16 v[108:111], a[92:95], a[244:247], v[108:111]\n"
      "s_waitcnt lgkmcnt(0)\n"
      "v_fmac_f32 v199, v241, v241\n"
      "v_fmac_f32 v187, v211, v211\n"
      "v_mfma_f32_16x16x32_bf16 v[190:193], a[116:119], a[244:247], v[190:193]\n"
      "v_fmac_f32 v199, v242, v242\n"
      "v_fmac_f32_e64 v187, v212, v212\n"
      "v_mfma_f32_16x16x32_bf16 v[112:115], a[140:143], a[244:247], v[112:115]\n"
      "buffer_load_dwordx4 v106, s[20:23], s78 offen offset:1024  lds\n"
      "v_mfma_f32_16x16x32_bf16 v[104:107], a[0:3], a[248:251], 0\n"
      "s_add_u32 m0, s75, 24576\n"
      "v_fmac_f32 v199, v243, v243\n"
      "v_fmac_f32 v187, v213, v213\n"
      "v_mfma_f32_16x16x32_bf16 v[104:107], a[4:7], a[252:255], v[104:107]\n"
      "v_fmac_f32 v199, v244, v244\n"
      "v_fmac_f32 v187, v252, v252\n"
      "v_mfma_f32_16x16x32_bf16 v[104:107], a[8:11], a[216:219], v[104:107]\n"
      "v_fmac_f32 v199, v245, v245\n"
      "v_fmac_f32 v187, v253, v253\n"
      "v_mfma_f32_16x16x32_bf16 v[104:107], a[12:15], a[220:223], v[104:107]\n"
      "v_fmac_f32 v199, v246, v246\n"
      "v_fmac_f32 v187, v254, v254\n"
      "v_mfma_f32_16x16x32_bf16 v[104:107], a[16:19], a[224:227], v[104:107]\n"
      "v_fmac_f32 v199, v247, v247\n"
      "s_waitcnt vmcnt(12)\n"
      "s_barrier\n"
      "v_mfma_f32_16x16x32_bf16 v[104:107], a[20:23], a[228:231], v[104:107]\n"
      "s_mov_b32 s79, 0\n"
      "buffer_load_dwordx4 v149, s[36:39], s79 offen  lds\n"
      "v_mfma_f32_16x16x32_bf16 v[226:229], a[24:27], a[248:251], 0\n"
      "s_add_u32 m0, s75, 28544\n"
      "ds_read_b64_tr_b16 a[144:145], v196\n"
      "v_mfma_f32_16x16x32_bf16 v[226:229], a[28:31], a[252:255], v[226:229]\n"
      "ds_read_b64_tr_b16 a[146:147], v196 offset:512\n"
      "ds_read_b64_tr_b16 a[168:169], v197\n"
      "v_mfma_f32_16x16x32_bf16 v[226:229], a[32:35], a[216:219], v[226:229]\n"
      "ds_read_b64_tr_b16 a[170:171], v197 offset:512\n"
      "buffer_load_dwordx4 v149, s[36:39], s79 offen offset:128  lds\n"
      "v_mfma_f32_16x16x32_bf16 v[226:229], a[36:39], a[220:223], v[226:229]\n"
      "ds_read_b64_tr_b16 a[192:193], v194\n"
      "ds_read_b64_tr_b16 a[194:195], v194 offset:512\n"
      "v_mfma_f32_16x16x32_bf16 v[226:229], a[40:43], a[224:227], v[226:229]\n"
      "ds_read_b64_tr_b16 a[148:149], v196 offset:12288\n"
      "ds_read_b64_tr_b16 a[150:151], v196 offset:12800\n"
      "v_mfma_f32_16x16x32_bf16 v[226:229], a[44:47], a[228:231], v[226:229]\n"
      "s_waitcnt lgkmcnt(7)\n"
      "ds_read_b64_tr_b16 a[172:173], v197 offset:12288\n"
      "v_mfma_f32_16x16x32_bf16 v[142:145], a[48:51], a[248:251], 0\n"
      "s_waitcnt lgkmcnt(6)\n"
      "ds_read_b64_tr_b16 a[174:175], v197 offset:12800\n"
      "v_mfma_f32_16x16x32_bf16 v[142:145], a[52:55], a[252:255], v[142:145]\n"
      "s_waitcnt lgkmcnt(6)\n"
      "ds_read_b64_tr_b16 a[196:197], v194 offset:12288\n"
      "v_mfma_f32_16x16x32_bf16 v[142:145], a[56:59], a[216:219], v[142:145]\n"
      "ds_read_b64_tr_b16 a[198:199], v194 offset:12800\n"
      "s_add_u32 m0, s75, 32512\n"
      "v_mfma_f32_16x16x32_bf16 v[142:145], a[60:63], a[220:223], v[142:145]\n"
      "s_waitcnt lgkmcnt(6)\n"
      "buffer_load_dwordx4 v149, s[36:39], s79 offen offset:256  lds\n"
      "v_mfma_f32_16x16x32_bf16 v[142:145], a[64:67], a[224:227], v[142:145]\n"
      "s_waitcnt lgkmcnt(4)\n"
      "s_add_u32 m0, s75, 36864\n"
      "s_mov_b32 s79, 12288\n"
      "v_mfma_f32_16x16x32_bf16 v[142:145], a[68:71], a[228:231], v[142:145]\n"
      "s_waitcnt lgkmcnt(3)\n"
      "v_fmac_f32 v187, v255, v255\n"
      "v_fma_f32 v183, v96, v96, s51\n"
      "v_mfma_f32_16x16x32_bf16 v[158:161], a[72:75], a[248:251], 0\n"
      "s_waitcnt lgkmcnt(2)\n"
      "v_permlane16_swap_b32 v199, v187\n"
      "v_mfma_f32_16x16x32_bf16 v[158:161], a[76:79], a[252:255], v[158:161]\n"
      "s_waitcnt lgkmcnt(1)\n"
      "buffer_load_dwordx4 v149, s[36:39], s79 offen  lds\n"
      "v_mfma_f32_16x16x32_bf16 v[158:161], a[80:83], a[216:219], v[158:161]\n"
      "s_waitcnt lgkmcnt(0)\n"
      "s_add_u32 m0, s75, 40832\n"
      "v_add_f32 v199, v199, v187\n"
      "v_mfma_f32_16x16x32_bf16 v[158:161], a[84:87], a[220:223], v[158:161]\n"
      "v_mov_b32_e64 v187, v199\n"
      "v_fma_f32 v182, v138, v138, s51\n"
      "v_mfma_f32_16x16x32_bf16 v[158:161], a[88:91], a[224:227], v[158:161]\n"
      "ds_read_b128 a[232:235], v189 offset:2048\n"
      "v_permlane32_swap_b32_e64 v199, v187\n"
      "v_mfma_f32_16x16x32_bf16 v[158:161], a[92:95], a[228:231], v[158:161]\n"
      "buffer_load_dwordx4 v149, s[36:39], s79 offen offset:128  lds\n"
      "v_mfma_f32_16x16x32_bf16 v[218:221], a[96:99], a[248:251], 0\n"
      "s_add_u32 m0, s75, 44800\n"
      "ds_read_b128 a[240:243], v189 offset:6144\n"
      "v_mfma_f32_16x16x32_bf16 v[218:221], a[100:103], a[252:255], v[218:221]\n"
      "ds_read_b128 a[244:247], v188 offset:6144\n"
      "ds_read_b128 a[236:239], v188 offset:2048\n"
      "v_mfma_f32_16x16x32_bf16 v[218:221], a[104:107], a[216:219], v[218:221]\n"
      "v_add_f32_e64 v199, v199, v187\n"
      "buffer_load_dwordx4 v149, s[36:39], s79 offen offset:256  lds\n"
      "v_mfma_f32_16x16x32_bf16 v[218:221], a[108:111], a[220:223], v[218:221]\n"
      "s_add_u32 m0, s75, 49152\n"
      "v_rsq_f32 v199, v199\n"
      "v_mfma_f32_16x16x32_bf16 v[218:221], a[112:115], a[224:227], v[218:221]\n"
      "v_mov_b32 v187, v199\n"
      "v_fmac_f32_e64 v183, v97, v97\n"
      "v_mfma_f32_16x16x32_bf16 v[218:221], a[116:119], a[228:231], v[218:221]\n"
      "v_permlane16_swap_b32 v199, v187\n"
      "v_mfma_f32_16x16x32_bf16 v[116:119], a[120:123], a[248:251], 0\n"
      "ds_read_b128 a[248:251], v189 offset:10240\n"
      "v_mul_f32 v199, s50, v199\n"
      "v_mfma_f32_16x16x32_bf16 v[116:119], a[124:127], a[252:255], v[116:119]\n"
      "ds_read_b128 a[252:255], v188 offset:10240\n"
      "v_mul_f32 v232, v199, v232\n"
      "v_mfma_f32_16x16x32_bf16 v[116:119], a[128:131], a[216:219], v[116:119]\n"
      "s_waitcnt lgkmcnt(2)\n"
      "v_mul_f32 v233, v199, v233\n"
      "v_mul_f32 v134, v187, v134\n"
      "v_mfma_f32_16x16x32_bf16 v[116:119], a[132:135], a[220:223], v[116:119]\n"
      "v_mul_f32_e64 v135, v187, v135\n"
      "v_cvt_pk_bf16_f32 v232, v232, v233\n"
      "v_mfma_f32_16x16x32_bf16 v[116:119], a[136:139], a[224:227], v[116:119]\n"
      "v_mul_f32 v234, v199, v234\n"
      "v_mul_f32 v235, v199, v235\n"
      "v_mfma_f32_16x16x32_bf16 v[116:119], a[140:143], a[228:231], v[116:119]\n"
      "v_mul_f32 v244, v199, v244\n"
      "v_mul_f32 v245, v199, v245\n"
      "v_mfma_f32_16x16x32_bf16 v[128:131], a[0:3], a[232:235], 0\n"
      "s_waitcnt lgkmcnt(1)\n"
      "v_mul_f32 v252, v187, v252\n"
      "v_mul_f32 v253, v187, v253\n"
      "v_mfma_f32_16x16x32_bf16 v[128:131], a[4:7], a[236:239], v[128:131]\n"
      "s_waitcnt lgkmcnt(0)\n"
      "v_cvt_pk_bf16_f32 v134, v134, v135\n"
      "v_mul_f32 v136, v187, v136\n"
      "v_mfma_f32_16x16x32_bf16 v[128:131], a[8:11], a[240:243], v[128:131]\n"
      "v_mul_f32 v137, v187, v137\n"
      "v_cvt_pk_bf16_f32 v233, v234, v235\n"
      "v_mfma_f32_16x16x32_bf16 v[128:131], a[12:15], a[244:247], v[128:131]\n"
      "s_waitcnt vmcnt(12)\n"
      "s_barrier\n"
      "s_mov_b32 s78, 24576\n"
      "v_mfma_f32_16x16x32_bf16 v[128:131], a[16:19], a[248:251], v[128:131]\n"
      "buffer_load_dwordx4 v149, s[36:39], s78 offen  lds\n"
      "v_mfma_f32_16x16x32_bf16 v[128:131], a[20:23], a[252:255], v[128:131]\n"
      "s_add_u32 m0, s75, 53120\n"
      "ds_read_b64_tr_b16 a[152:153], v196 offset:24576\n"
      "v_mfma_f32_16x16x32_bf16 v[162:165], a[24:27], a[232:235], 0\n"
      "ds_read_b64_tr_b16 a[154:155], v196 offset:25088\n"
      "ds_read_b64_tr_b16 a[176:177], v197 offset:24576\n"
      "v_mfma_f32_16x16x32_bf16 v[162:165], a[28:31], a[236:239], v[162:165]\n"
      "ds_read_b64_tr_b16 a[178:179], v197 offset:25088\n"
      "ds_read_b64_tr_b16 a[200:201], v194 offset:24576\n"
      "v_mfma_f32_16x16x32_bf16 v[162:165], a[32:35], a[240:243], v[162:165]\n"
      "ds_read_b64_tr_b16 a[202:203], v194 offset:25088\n"
      "ds_read_b64_tr_b16 a[156:157], v196 offset:36864\n"
      "v_mfma_f32_16x16x32_bf16 v[162:165], a[36:39], a[244:247], v[162:165]\n"
      "ds_read_b64_tr_b16 a[158:159], v196 offset:37376\n"
      "ds_read_b64_tr_b16 a[180:181], v197 offset:36864\n"
      "v_mfma_f32_16x16x32_bf16 v[162:165], a[40:43], a[248:251], v[162:165]\n"
      "ds_read_b64_tr_b16 a[182:183], v197 offset:37376\n"
      "ds_read_b64_tr_b16 a[204:205], v194 offset:36864\n"
      "v_mfma_f32_16x16x32_bf16 v[162:165], a[44:47], a[252:255], v[162:165]\n"
      "s_waitcnt lgkmcnt(9)\n"
      "ds_read_b64_tr_b16 a[206:207], v194 offset:37376\n"
      "v_mfma_f32_16x16x32_bf16 v[194:197], a[48:51], a[232:235], 0\n"
      "s_waitcnt lgkmcnt(8)\n"
      "buffer_load_dwordx4 v149, s[36:39], s78 offen offset:128  lds\n"
      "v_mfma_f32_16x16x32_bf16 v[194:197], a[52:55], a[236:239], v[194:197]\n"
      "s_waitcnt lgkmcnt(6)\n"
      "s_add_u32 m0, s75, 57088\n"
      "v_mul_f32 v240, v199, v240\n"
      "v_mfma_f32_16x16x32_bf16 v[194:197], a[56:59], a[240:243], v[194:197]\n"
      "s_waitcnt lgkmcnt(4)\n"
      "v_mul_f32 v241, v199, v241\n"
      "v_mul_f32 v242, v199, v242\n"
      "v_mfma_f32_16x16x32_bf16 v[194:197], a[60:63], a[244:247], v[194:197]\n"
      "s_waitcnt lgkmcnt(3)\n"
      "v_mul_f32 v243, v199, v243\n"
      "v_cvt_pk_bf16_f32 v244, v244, v245\n"
      "v_mfma_f32_16x16x32_bf16 v[194:197], a[64:67], a[248:251], v[194:197]\n"
      "s_waitcnt lgkmcnt(1)\n"
      "buffer_load_dwordx4 v149, s[36:39], s78 offen offset:256  lds\n"
      "v_mfma_f32_16x16x32_bf16 v[194:197], a[68:71], a[252:255], v[194:197]\n"
      "s_waitcnt lgkmcnt(0)\n"
      "s_add_u32 m0, s75, 61440\n"
      "s_mov_b32 s78, 36864\n"
      "v_mfma_f32_16x16x32_bf16 v[100:103], a[72:75], a[232:235], 0\n"
      "v_mul_f32 v246, v199, v246\n"
      "v_mul_f32 v247, v199, v247\n"
      "v_mfma_f32_16x16x32_bf16 v[100:103], a[76:79], a[236:239], v[100:103]\n"
      "v_cvt_pk_bf16_f32 v252, v252, v253\n"
      "v_mul_f32 v254, v187, v254\n"
      "v_mfma_f32_16x16x32_bf16 v[100:103], a[80:83], a[240:243], v[100:103]\n"
      "buffer_load_dwordx4 v149, s[36:39], s78 offen  lds\n"
      "v_mfma_f32_16x16x32_bf16 v[100:103], a[84:87], a[244:247], v[100:103]\n"
      "s_add_u32 m0, s75, 65408\n"
      "v_mul_f32 v255, v187, v255\n"
      "v_cvt_pk_bf16_f32 v135, v136, v137\n"
      "v_mfma_f32_16x16x32_bf16 v[100:103], a[88:91], a[248:251], v[100:103]\n"
      "ds_read_b128 v[198:201], v189\n"
      "v_mul_f32 v210, v187, v210\n"
      "v_mfma_f32_16x16x32_bf16 v[100:103], a[92:95], a[252:255], v[100:103]\n"
      "v_mul_f32 v211, v187, v211\n"
      "v_mul_f32 v212, v187, v212\n"
      "v_mfma_f32_16x16x32_bf16 v[154:157], a[96:99], a[232:235], 0\n"
      "buffer_load_dwordx4 v149, s[36:39], s78 offen offset:128  lds\n"
      "v_mfma_f32_16x16x32_bf16 v[154:157], a[100:103], a[236:239], v[154:157]\n"
      "s_add_u32 m0, s75, 69376\n"
      "v_mul_f32_e64 v213, v187, v213\n"
      "v_cvt_pk_bf16_f32 v234, v240, v241\n"
      "v_mfma_f32_16x16x32_bf16 v[154:157], a[104:107], a[240:243], v[154:157]\n"
      "ds_read_b128 v[184:187], v189 offset:2048\n"
      "v_cvt_pk_bf16_f32 v235, v242, v243\n"
      "v_mfma_f32_16x16x32_bf16 v[154:157], a[108:111], a[244:247], v[154:157]\n"
      "ds_write_b128 v147, v[232:235]\n"
      "v_mfma_f32_16x16x32_bf16 v[154:157], a[112:115], a[248:251], v[154:157]\n"
      "buffer_load_dwordx4 v149, s[36:39], s78 offen offset:256  lds\n"
      "v_mfma_f32_16x16x32_bf16 v[154:157], a[116:119], a[252:255], v[154:157]\n"
      "s_add_u32 m0, s75, 73728\n"
      "v_cvt_pk_bf16_f32 v245, v246, v247\n"
      "v_cvt_pk_bf16_f32 v253, v254, v255\n"
      "v_mfma_f32_16x16x32_bf16 v[214:217], a[120:123], a[232:235], 0\n"
      "ds_write_b64 v147, v[244:245] offset:16\n"
      "v_mfma_f32_16x16x32_bf16 v[214:217], a[124:127], a[236:239], v[214:217]\n"
      "ds_read_b128 v[232:235], v188\n"
      "v_cvt_pk_bf16_f32 v136, v210, v211\n"
      "v_mfma_f32_16x16x32_bf16 v[214:217], a[128:131], a[240:243], v[214:217]\n"
      "ds_read_b128 v[244:247], v188 offset:2048\n"
      "v_cvt_pk_bf16_f32 v137, v212, v213\n"
      "v_mfma_f32_16x16x32_bf16 v[214:217], a[132:135], a[244:247], v[214:217]\n"
      "ds_write_b64 v147, v[252:253] offset:24\n"
      "v_mfma_f32_16x16x32_bf16 v[214:217], a[136:139], a[248:251], v[214:217]\n"
      "ds_read_b128 v[240:243], v189 offset:4096\n"
      "v_fmac_f32 v182, v139, v139\n"
      "v_mfma_f32_16x16x32_bf16 v[214:217], a[140:143], a[252:255], v[214:217]\n"
      "s_waitcnt lgkmcnt(5)\n"
      "s_waitcnt vmcnt(12)\n"
      "s_barrier\n"
      "v_mfma_f32_16x16x32_bf16 v[248:251], a[144:147], v[198:201], 0\n"
      "ds_read_b64_tr_b16 a[160:161], v236\n"
      "ds_read_b64_tr_b16 a[162:163], v236 offset:512\n"
      "v_mfma_f32_16x16x32_bf16 v[252:255], a[168:171], v[198:201], 0\n"
      "ds_read_b64_tr_b16 a[184:185], v237\n"
      "ds_read_b64_tr_b16 a[186:187], v237 offset:512\n"
      "v_mfma_f32_16x16x32_bf16 v[210:213], a[192:195], v[198:201], 0\n"
      "ds_read_b64_tr_b16 a[208:209], v238\n"
      "ds_read_b64_tr_b16 a[210:211], v238 offset:512\n"
      "ds_read_b64_tr_b16 a[164:165], v236 offset:12288\n"
      "ds_read_b64_tr_b16 a[166:167], v236 offset:12800\n"
      "ds_read_b64_tr_b16 a[188:189], v237 offset:12288\n"
      "ds_read_b64_tr_b16 a[190:191], v237 offset:12800\n"
      "v_mfma_f32_16x16x32_bf16 v[150:153], a[144:147], v[184:187], 0\n"
      "ds_read_b64_tr_b16 a[212:213], v238 offset:12288\n"
      "ds_read_b64_tr_b16 a[214:215], v238 offset:12800\n"
      "s_mov_b32 s79, 49152\n"
      "buffer_load_dwordx4 v149, s[36:39], s79 offen  lds\n"
      "s_add_u32 m0, s75, 77696\n"
      "ds_write_b128 v147, v[134:137] offset:32\n"
      "v_mfma_f32_16x16x32_bf16 v[198:201], a[168:171], v[184:187], 0\n"
      "s_waitcnt lgkmcnt(5)\n"
      "v_fmac_f32 v183, v98, v98\n"
      "v_fmac_f32_e64 v182, v140, v140\n"
      "v_mfma_f32_16x16x32_bf16 v[178:181], a[192:195], v[184:187], 0\n"
      "s_waitcnt lgkmcnt(3)\n"
      "buffer_load_dwordx4 v149, s[36:39], s79 offen offset:128  lds\n"
      "v_mfma_f32_16x16x32_bf16 v[248:251], a[148:151], v[232:235], v[248:251]\n"
      "s_waitcnt lgkmcnt(1)\n"
      "s_add_u32 m0, s75, 81664\n"
      "v_fmac_f32 v183, v99, v99\n"
      "v_mfma_f32_16x16x32_bf16 v[252:255], a[172:175], v[232:235], v[252:255]\n"
      "v_fmac_f32 v182, v141, v141\n"
      "v_fmac_f32_e64 v183, v124, v124\n"
      "v_mfma_f32_16x16x32_bf16 v[210:213], a[196:199], v[232:235], v[210:213]\n"
      "ds_read_b128 v[232:235], v189 offset:6144\n"
      "v_fmac_f32_e64 v182, v206, v206\n"
      "v_mfma_f32_16x16x32_bf16 v[150:153], a[148:151], v[244:247], v[150:153]\n"
      "buffer_load_dwordx4 v149, s[36:39], s79 offen offset:256  lds\n"
      "v_mfma_f32_16x16x32_bf16 v[198:201], a[172:175], v[244:247], v[198:201]\n"
      "s_add_u32 m0, s75, 86016\n"
      "s_mov_b32 s79, 61440\n"
      "v_fmac_f32 v183, v125, v125\n"
      "v_mfma_f32_16x16x32_bf16 v[178:181], a[196:199], v[244:247], v[178:181]\n"
      "ds_read_b128 v[244:247], v188 offset:4096\n"
      "v_fmac_f32 v182, v207, v207\n"
      "v_mfma_f32_16x16x32_bf16 v[248:251], a[152:155], v[240:243], v[248:251]\n"
      "v_fmac_f32 v183, v126, v126\n"
      "v_fmac_f32 v182, v208, v208\n"
      "v_mfma_f32_16x16x32_bf16 v[252:255], a[176:179], v[240:243], v[252:255]\n"
      "buffer_load_dwordx4 v149, s[36:39], s79 offen  lds\n"
      "v_mfma_f32_16x16x32_bf16 v[210:213], a[200:203], v[240:243], v[210:213]\n"
      "s_waitcnt lgkmcnt(1)\n"
      "s_add_u32 m0, s75, 89984\n"
      "v_fmac_f32 v183, v127, v127\n"
      "v_mfma_f32_16x16x32_bf16 v[150:153], a[152:155], v[232:235], v[150:153]\n"
      "ds_read_b128 v[240:243], v188 offset:6144\n"
      "v_fmac_f32 v182, v209, v209\n"
      "v_mfma_f32_16x16x32_bf16 v[198:201], a[176:179], v[232:235], v[198:201]\n"
      "v_fmac_f32 v183, v202, v202\n"
      "v_fmac_f32 v182, v174, v174\n"
      "v_mfma_f32_16x16x32_bf16 v[178:181], a[200:203], v[232:235], v[178:181]\n"
      "s_waitcnt lgkmcnt(1)\n"
      "buffer_load_dwordx4 v149, s[36:39], s79 offen offset:128  lds\n"
      "v_mfma_f32_16x16x32_bf16 v[248:251], a[156:159], v[244:247], v[248:251]\n"
      "s_add_u32 m0, s75, 93952\n"
      "v_fmac_f32 v183, v203, v203\n"
      "v_fmac_f32 v182, v175, v175\n"
      "v_mfma_f32_16x16x32_bf16 v[252:255], a[180:183], v[244:247], v[252:255]\n"
      "ds_read_b128 v[232:235], v189 offset:8192\n"
      "v_fmac_f32_e64 v183, v204, v204\n"
      "v_mfma_f32_16x16x32_bf16 v[210:213], a[204:207], v[244:247], v[210:213]\n"
      "ds_read_b128 v[244:247], v189 offset:10240\n"
      "v_fmac_f32 v182, v176, v176\n"
      "s_waitcnt lgkmcnt(2)\n"
      "v_mfma_f32_16x16x32_bf16 v[150:153], a[156:159], v[240:243], v[150:153]\n"
      "buffer_load_dwordx4 v149, s[36:39], s79 offen offset:256  lds\n"
      "v_mfma_f32_16x16x32_bf16 v[198:201], a[180:183], v[240:243], v[198:201]\n"
      "v_fmac_f32 v183, v205, v205\n"
      "v_fmac_f32 v182, v177, v177\n"
      "v_mfma_f32_16x16x32_bf16 v[178:181], a[204:207], v[240:243], v[178:181]\n"
      "ds_read_b128 v[240:243], v188 offset:8192\n"
      "ds_read_b128 v[184:187], v188 offset:10240\n"
      "v_permlane16_swap_b32 v183, v182\n"
      "s_waitcnt lgkmcnt(3)\n"
      "v_mfma_f32_16x16x32_bf16 v[248:251], a[160:163], v[232:235], v[248:251]\n"
      "v_add_f32 v183, v183, v182\n"
      "s_waitcnt vmcnt(12)\n"
      "s_barrier\n"
      "v_mfma_f32_16x16x32_bf16 v[252:255], a[184:187], v[232:235], v[252:255]\n"
      "ds_read_b64_tr_b16 a[216:217], v239 offset:24576\n"
      "ds_read_b64_tr_b16 a[218:219], v239 offset:26624\n"
      "v_mfma_f32_16x16x32_bf16 v[210:213], a[208:211], v[232:235], v[210:213]\n"
      "s_waitcnt lgkmcnt(4)\n"
      "ds_read_b64_tr_b16 a[240:241], v239 offset:24584\n"
      "v_mfma_f32_16x16x32_bf16 v[150:153], a[160:163], v[244:247], v[150:153]\n"
      "ds_read_b64_tr_b16 a[242:243], v239 offset:26632\n"
      "ds_read_b64_tr_b16 v[232:233], v146 offset:24576\n"
      "v_mfma_f32_16x16x32_bf16 v[198:201], a[184:187], v[244:247], v[198:201]\n"
      "ds_read_b64_tr_b16 v[234:235], v146 offset:26624\n"
      "ds_read_b64_tr_b16 a[220:221], v239 offset:36864\n"
      "v_mfma_f32_16x16x32_bf16 v[178:181], a[208:211], v[244:247], v[178:181]\n"
      "s_waitcnt lgkmcnt(7)\n"
      "ds_read_b64_tr_b16 a[222:223], v239 offset:38912\n"
      "v_mfma_f32_16x16x32_bf16 v[248:251], a[164:167], v[240:243], v[248:251]\n"
      "ds_read_b64_tr_b16 a[244:245], v239 offset:36872\n"
      "ds_read_b64_tr_b16 a[246:247], v239 offset:38920\n"
      "v_mfma_f32_16x16x32_bf16 v[252:255], a[188:191], v[240:243], v[252:255]\n"
      "s_waitcnt lgkmcnt(8)\n"
      "ds_read_b64_tr_b16 v[236:237], v146 offset:36864\n"
      "v_mfma_f32_16x16x32_bf16 v[210:213], a[212:215], v[240:243], v[210:213]\n"
      "s_waitcnt lgkmcnt(8)\n"
      "ds_read_b64_tr_b16 v[238:239], v146 offset:38912\n"
      "v_mfma_f32_16x16x32_bf16 v[150:153], a[164:167], v[184:187], v[150:153]\n"
      "s_waitcnt lgkmcnt(7)\n"
      "buffer_load_dwordx4 v[134:137], v36, s[28:31], s49 offen\n"
      "v_mfma_f32_16x16x32_bf16 v[198:201], a[188:191], v[184:187], v[198:201]\n"
      "s_waitcnt lgkmcnt(5)\n"
      "s_add_u32 m0, s64, 18432\n"
      "v_mov_b32 v182, v183\n"
      "v_mfma_f32_16x16x32_bf16 v[178:181], a[212:215], v[184:187], v[178:181]\n"
      "s_waitcnt lgkmcnt(4)\n"
      "ds_read_b128 v[186:189], v0 offset:4096\n"
      "v_permlane32_swap_b32 v183, v182\n"
      "v_add_f32 v183, v183, v182\n"
      "v_rsq_f32 v183, v183\n"
      "buffer_load_dword v149, v29, s[28:31], s49 offen\n"
      "v_mov_b32 v182, v183\n"
      "v_fma_f32 v241, v120, v120, s51\n"
      "v_fmac_f32 v241, v121, v121\n"
      "v_permlane16_swap_b32 v183, v182\n"
      "v_mul_f32 v183, s50, v183\n"
      "v_mul_f32 v138, v182, v138\n"
      "v_mul_f32 v139, v182, v139\n"
      "v_mul_f32 v96, v183, v96\n"
      "v_mul_f32 v97, v183, v97\n"
      "v_cvt_pk_bf16_f32 v138, v138, v139\n"
      "v_mul_f32 v140, v182, v140\n"
      "v_mul_f32 v141, v182, v141\n"
      "v_mul_f32 v174, v182, v174\n"
      "v_mul_f32 v175, v182, v175\n"
      "v_cvt_pk_bf16_f32 v96, v96, v97\n"
      "v_mul_f32 v98, v183, v98\n"
      "v_mul_f32 v99, v183, v99\n"
      "v_mul_f32 v202, v183, v202\n"
      "v_mul_f32 v203, v183, v203\n"
      "v_cvt_pk_bf16_f32 v139, v140, v141\n"
      "v_mul_f32 v206, v182, v206\n"
      "v_mul_f32 v207, v182, v207\n"
      "v_mul_f32 v208, v182, v208\n"
      "v_mul_f32 v209, v182, v209\n"
      "v_cvt_pk_bf16_f32 v174, v174, v175\n"
      "v_mul_f32 v176, v182, v176\n"
      "v_mul_f32 v177, v182, v177\n"
      "v_cvt_pk_bf16_f32 v97, v98, v99\n"
      "v_mul_f32 v124, v183, v124\n"
      "v_mul_f32 v125, v183, v125\n"
      "v_mul_f32 v126, v183, v126\n"
      "v_mul_f32 v127, v183, v127\n"
      "v_cvt_pk_bf16_f32 v202, v202, v203\n"
      "v_mul_f32 v204, v183, v204\n"
      "v_mul_f32_e64 v205, v183, v205\n"
      "v_cvt_pk_bf16_f32 v140, v206, v207\n"
      "s_waitcnt lgkmcnt(0)\n"
      "v_mfma_f32_16x16x32_bf16 v[182:185], v[186:189], a[144:147], 0\n"
      "v_cvt_pk_bf16_f32 v141, v208, v209\n"
      "v_cvt_pk_bf16_f32 v175, v176, v177\n"
      "ds_read_b128 v[206:209], v1 offset:4096\n"
      "v_cvt_pk_bf16_f32 v98, v124, v125\n"
      "v_cvt_pk_bf16_f32 v99, v126, v127\n"
      "ds_read_b128 v[124:127], v0 offset:10240\n"
      "v_cvt_pk_bf16_f32 v203, v204, v205\n"
      "v_fma_f32 v240, v108, v108, s51\n"
      "v_fmac_f32 v240, v109, v109\n"
      "v_fmac_f32 v241, v122, v122\n"
      "v_fmac_f32 v240, v110, v110\n"
      "v_fmac_f32 v241, v123, v123\n"
      "v_fmac_f32 v240, v111, v111\n"
      "v_fmac_f32 v241, v166, v166\n"
      "v_fmac_f32 v240, v190, v190\n"
      "v_fmac_f32 v241, v167, v167\n"
      "v_fmac_f32 v240, v191, v191\n"
      "v_fmac_f32 v241, v168, v168\n"
      "v_fmac_f32 v240, v192, v192\n"
      "v_fmac_f32 v241, v169, v169\n"
      "v_fmac_f32 v240, v193, v193\n"
      "v_fmac_f32 v241, v222, v222\n"
      "v_fmac_f32 v240, v112, v112\n"
      "s_waitcnt lgkmcnt(1)\n"
      "v_mfma_f32_16x16x32_bf16 v[182:185], v[206:209], a[148:151], v[182:185]\n"
      "v_fmac_f32 v241, v223, v223\n"
      "v_fmac_f32_e64 v240, v113, v113\n"
      "s_waitcnt lgkmcnt(0)\n"
      "v_mfma_f32_16x16x32_bf16 v[182:185], v[124:127], a[152:155], v[182:185]\n"
      "v_fmac_f32 v241, v224, v224\n"
      "v_fmac_f32 v240, v114, v114\n"
      "v_fmac_f32 v241, v225, v225\n"
      "v_fmac_f32 v240, v115, v115\n"
      "v_fma_f32 v146, v104, v104, s51\n"
      "v_fmac_f32 v146, v105, v105\n"
      "v_permlane16_swap_b32 v241, v240\n"
      "v_add_f32_e64 v241, v241, v240\n"
      "s_waitcnt vmcnt(8)\n"
      "s_barrier\n"
      "ds_read_b64_tr_b16 a[224:225], v132\n"
      "ds_read_b64_tr_b16 a[226:227], v132 offset:2048\n"
      "ds_read_b64_tr_b16 a[248:249], v132 offset:8\n"
      "ds_read_b64_tr_b16 a[250:251], v132 offset:2056\n"
      "ds_read_b64_tr_b16 a[228:229], v132 offset:12288\n"
      "ds_read_b64_tr_b16 a[230:231], v132 offset:14336\n"
      "ds_read_b64_tr_b16 a[252:253], v132 offset:12296\n"
      "ds_read_b64_tr_b16 a[254:255], v132 offset:14344\n"
      "v_mov_b32 v240, v241\n"
      "v_fmac_f32 v146, v106, v106\n"
      "v_fmac_f32 v146, v107, v107\n"
      "v_permlane32_swap_b32 v241, v240\n"
      "v_add_f32 v241, v241, v240\n"
      "v_rsq_f32 v241, v241\n"
      "v_fmac_f32 v146, v226, v226\n"
      "v_mov_b32 v240, v241\n"
      "v_fmac_f32 v146, v227, v227\n"
      "v_fmac_f32 v146, v228, v228\n"
      "v_permlane16_swap_b32 v241, v240\n"
      "v_mul_f32 v241, s50, v241\n"
      "v_mul_f32 v108, v240, v108\n"
      "v_mul_f32 v109, v240, v109\n"
      "v_mul_f32 v120, v241, v120\n"
      "v_mul_f32 v121, v241, v121\n"
      "v_cvt_pk_bf16_f32 v108, v108, v109\n"
      "v_mul_f32 v110, v240, v110\n"
      "v_mul_f32 v111, v240, v111\n"
      "v_mul_f32 v112, v240, v112\n"
      "v_mul_f32 v113, v240, v113\n"
      "v_cvt_pk_bf16_f32 v120, v120, v121\n"
      "v_mul_f32 v122, v241, v122\n"
      "v_mul_f32 v123, v241, v123\n"
      "v_mul_f32 v222, v241, v222\n"
      "v_mul_f32 v223, v241, v223\n"
      "v_cvt_pk_bf16_f32 v109, v110, v111\n"
      "v_mul_f32 v190, v240, v190\n"
      "v_mul_f32 v191, v240, v191\n"
      "v_mul_f32 v192, v240, v192\n"
      "v_mul_f32 v193, v240, v193\n"
      "v_cvt_pk_bf16_f32 v112, v112, v113\n"
      "v_mul_f32 v114, v240, v114\n"
      "v_mul_f32 v115, v240, v115\n"
      "v_cvt_pk_bf16_f32 v121, v122, v123\n"
      "v_mul_f32 v166, v241, v166\n"
      "v_mul_f32 v167, v241, v167\n"
      "v_mul_f32 v168, v241, v168\n"
      "v_mul_f32 v169, v241, v169\n"
      "v_cvt_pk_bf16_f32 v222, v222, v223\n"
      "v_mul_f32 v224, v241, v224\n"
      "v_mul_f32 v225, v241, v225\n"
      "ds_read_b64_tr_b16 v[240:241], v133\n"
      "v_cvt_pk_bf16_f32 v110, v190, v191\n"
      "ds_read_b64_tr_b16 v[242:243], v133 offset:2048\n"
      "v_cvt_pk_bf16_f32 v111, v192, v193\n"
      "ds_read_b64_tr_b16 v[244:245], v133 offset:12288\n"
      "v_cvt_pk_bf16_f32 v113, v114, v115\n"
      "ds_read_b64_tr_b16 v[246:247], v133 offset:14336\n"
      "v_cvt_pk_bf16_f32 v122, v166, v167\n"
      "v_cvt_pk_bf16_f32 v123, v168, v169\n"
      "ds_read_b128 v[166:169], v1 offset:10240\n"
      "v_cvt_pk_bf16_f32 v223, v224, v225\n"
      "v_fma_f32 v193, v158, v158, s51\n"
      "v_fmac_f32 v193, v159, v159\n"
      "v_fmac_f32 v193, v160, v160\n"
      "v_fmac_f32 v193, v161, v161\n"
      "v_fmac_f32 v193, v218, v218\n"
      "v_fmac_f32 v193, v219, v219\n"
      "v_fmac_f32 v193, v220, v220\n"
      "v_fmac_f32 v146, v229, v229\n"
      "v_fmac_f32 v193, v221, v221\n"
      "v_fmac_f32 v146, v142, v142\n"
      "v_fmac_f32 v193, v116, v116\n"
      "v_fmac_f32 v146, v143, v143\n"
      "v_fmac_f32 v193, v117, v117\n"
      "v_fmac_f32 v146, v144, v144\n"
      "v_fmac_f32 v193, v118, v118\n"
      "v_fmac_f32 v146, v145, v145\n"
      "v_fmac_f32 v193, v119, v119\n"
      "v_fma_f32 v177, v128, v128, s51\n"
      "v_fmac_f32 v177, v129, v129\n"
      "v_permlane16_swap_b32 v146, v193\n"
      "s_waitcnt lgkmcnt(0)\n"
      "v_mfma_f32_16x16x32_bf16 v[182:185], v[166:169], a[156:159], v[182:185]\n"
      "v_add_f32 v146, v146, v193\n"
      "v_mov_b32 v193, v146\n"
      "v_fmac_f32 v177, v130, v130\n"
      "v_fmac_f32 v177, v131, v131\n"
      "v_permlane32_swap_b32 v146, v193\n"
      "v_add_f32 v146, v146, v193\n"
      "v_rsq_f32 v146, v146\n"
      "v_fmac_f32 v177, v162, v162\n"
      "v_mov_b32 v193, v146\n"
      "v_fmac_f32 v177, v163, v163\n"
      "v_fmac_f32 v177, v164, v164\n"
      "v_permlane16_swap_b32 v146, v193\n"
      "v_mul_f32 v146, s50, v146\n"
      "v_mul_f32 v158, v193, v158\n"
      "v_mul_f32 v159, v193, v159\n"
      "v_mul_f32 v104, v146, v104\n"
      "v_mul_f32 v105, v146, v105\n"
      "v_cvt_pk_bf16_f32 v158, v158, v159\n"
      "v_mul_f32 v160, v193, v160\n"
      "v_mul_f32 v161, v193, v161\n"
      "v_mul_f32 v116, v193, v116\n"
      "v_mul_f32 v117, v193, v117\n"
      "v_cvt_pk_bf16_f32 v104, v104, v105\n"
      "v_mul_f32 v106, v146, v106\n"
      "v_mul_f32 v107, v146, v107\n"
      "v_mul_f32 v142, v146, v142\n"
      "v_mul_f32 v143, v146, v143\n"
      "v_cvt_pk_bf16_f32 v159, v160, v161\n"
      "v_mul_f32 v218, v193, v218\n"
      "v_mul_f32 v219, v193, v219\n"
      "v_mul_f32 v220, v193, v220\n"
      "v_mul_f32 v221, v193, v221\n"
      "v_cvt_pk_bf16_f32 v116, v116, v117\n"
      "v_mul_f32 v118, v193, v118\n"
      "v_mul_f32 v119, v193, v119\n"
      "v_cvt_pk_bf16_f32 v105, v106, v107\n"
      "v_mul_f32 v226, v146, v226\n"
      "v_mul_f32 v227, v146, v227\n"
      "v_mul_f32 v228, v146, v228\n"
      "v_mul_f32 v229, v146, v229\n"
      "v_cvt_pk_bf16_f32 v142, v142, v143\n"
      "v_mul_f32 v144, v146, v144\n"
      "v_mul_f32 v145, v146, v145\n"
      "v_cvt_pk_bf16_f32 v160, v218, v219\n"
      "ds_read_b128 v[170:173], v0 offset:16384\n"
      "v_cvt_pk_bf16_f32 v161, v220, v221\n"
      "v_cvt_pk_bf16_f32 v117, v118, v119\n"
      "ds_read_b128 v[190:193], v1 offset:16384\n"
      "v_cvt_pk_bf16_f32 v106, v226, v227\n"
      "v_cvt_pk_bf16_f32 v107, v228, v229\n"
      "buffer_load_dwordx4 v[218:221], v36, s[28:31], s74 offen\n"
      "v_cvt_pk_bf16_f32 v143, v144, v145\n"
      "v_fma_f32 v144, v100, v100, s51\n"
      "v_fmac_f32 v144, v101, v101\n"
      "v_fmac_f32 v144, v102, v102\n"
      "v_fmac_f32 v144, v103, v103\n"
      "v_fmac_f32 v144, v154, v154\n"
      "v_fmac_f32 v144, v155, v155\n"
      "v_fmac_f32 v144, v156, v156\n"
      "v_fmac_f32 v177, v165, v165\n"
      "v_fmac_f32 v144, v157, v157\n"
      "s_waitcnt lgkmcnt(1)\n"
      "v_mfma_f32_16x16x32_bf16 v[182:185], v[170:173], a[160:163], v[182:185]\n"
      "buffer_load_dword v119, v29, s[28:31], s74 offen\n"
      "s_waitcnt lgkmcnt(0)\n"
      "v_mfma_f32_16x16x32_bf16 v[182:185], v[190:193], a[164:167], v[182:185]\n"
      "s_waitcnt vmcnt(4)\n"
      "s_barrier\n"
      "ds_read_b64_tr_b16 a[232:233], v132 offset:24576\n"
      "ds_read_b64_tr_b16 a[234:235], v132 offset:26624\n"
      "ds_read_b64_tr_b16 a[236:237], v132 offset:36864\n"
      "ds_read_b64_tr_b16 a[238:239], v132 offset:38912\n"
      "buffer_load_dwordx4 v16, s[16:19], s45 offen  lds\n"
      "s_add_u32 m0, s64, 22528\n"
      "v_fmac_f32 v177, v194, v194\n"
      "v_fmac_f32 v144, v214, v214\n"
      "v_fmac_f32 v177, v195, v195\n"
      "v_fmac_f32 v144, v215, v215\n"
      "v_fmac_f32 v177, v196, v196\n"
      "v_fmac_f32 v144, v216, v216\n"
      "v_fmac_f32 v177, v197, v197\n"
      "v_fmac_f32 v144, v217, v217\n"
      "v_cvt_pk_bf16_f32 v248, v248, v249\n"
      "v_cvt_pk_bf16_f32 v252, v252, v253\n"
      "v_permlane16_swap_b32_e64 v177, v144\n"
      "buffer_load_dwordx4 v17, s[16:19], s45 offen  lds\n"
      "s_add_u32 m0, s64, 26624\n"
      "v_add_f32 v177, v177, v144\n"
      "v_mov_b32 v144, v177\n"
      "v_cvt_pk_bf16_f32 v210, v210, v211\n"
      "v_cvt_pk_bf16_f32 v249, v250, v251\n"
      "ds_read_b64_tr_b16 v[224:225], v132 offset:24584\n"
      "ds_write_b64 v148, v[248:249]\n"
      "ds_read_b64_tr_b16 v[226:227], v132 offset:26632\n"
      "buffer_load_dwordx4 v18, s[16:19], s45 offen  lds\n"
      "s_add_u32 m0, s64, 30720\n"
      "v_permlane32_swap_b32 v177, v144\n"
      "v_add_f32 v177, v177, v144\n"
      "v_rsq_f32 v177, v177\n"
      "s_waitcnt vmcnt(6)\n"
      "v_or3_b32 v134, v134, v135, v136\n"
      "ds_read_b64_tr_b16 v[248:249], v133 offset:24576\n"
      "v_mov_b32_e64 v144, v177\n"
      "v_cvt_pk_bf16_f32 v253, v254, v255\n"
      "ds_read_b64_tr_b16 v[250:251], v133 offset:26624\n"
      "buffer_load_dwordx4 v19, s[16:19], s45 offen  lds\n"
      "s_add_u32 m0, s65, 34816\n"
      "ds_write_b64 v148, v[252:253] offset:32\n"
      "ds_read_b64_tr_b16 v[228:229], v132 offset:36872\n"
      "v_permlane16_swap_b32 v177, v144\n"
      "v_mul_f32 v177, s50, v177\n"
      "v_mul_f32 v100, v144, v100\n"
      "v_mul_f32 v101, v144, v101\n"
      "buffer_load_dwordx4 v15, s[16:19], s45 offen  lds\n"
      "v_mul_f32 v128, v177, v128\n"
      "v_mul_f32 v129, v177, v129\n"
      "v_cvt_pk_bf16_f32 v100, v100, v101\n"
      "v_mul_f32 v102, v144, v102\n"
      "v_mul_f32 v103, v144, v103\n"
      "v_mul_f32 v214, v144, v214\n"
      "v_mul_f32 v215, v144, v215\n"
      "v_cvt_pk_bf16_f32 v128, v128, v129\n"
      "v_mul_f32 v130, v177, v130\n"
      "v_mul_f32 v131, v177, v131\n"
      "v_mul_f32 v194, v177, v194\n"
      "v_mul_f32 v195, v177, v195\n"
      "v_cvt_pk_bf16_f32 v101, v102, v103\n"
      "v_mul_f32 v154, v144, v154\n"
      "v_mul_f32 v155, v144, v155\n"
      "v_mul_f32 v156, v144, v156\n"
      "v_mul_f32 v157, v144, v157\n"
      "v_cvt_pk_bf16_f32 v214, v214, v215\n"
      "v_mul_f32 v216, v144, v216\n"
      "v_mul_f32 v217, v144, v217\n"
      "ds_read_b64_tr_b16 v[230:231], v132 offset:38920\n"
      "v_cvt_pk_bf16_f32 v129, v130, v131\n"
      "v_mul_f32 v162, v177, v162\n"
      "v_mul_f32 v163, v177, v163\n"
      "v_mul_f32 v164, v177, v164\n"
      "v_mul_f32 v165, v177, v165\n"
      "v_cvt_pk_bf16_f32 v194, v194, v195\n"
      "v_mul_f32_e64 v196, v177, v196\n"
      "v_mul_f32_e64 v197, v177, v197\n"
      "v_cvt_pk_bf16_f32 v102, v154, v155\n"
      "ds_read_b64_tr_b16 v[252:253], v133 offset:36864\n"
      "ds_read_b64_tr_b16 v[254:255], v133 offset:38912\n"
      "v_cvt_pk_bf16_f32 v103, v156, v157\n"
      "v_cvt_pk_bf16_f32 v215, v216, v217\n"
      "v_mfma_f32_16x16x32_bf16 v[154:157], v[186:189], a[168:171], 0\n"
      "v_cvt_pk_bf16_f32 v130, v162, v163\n"
      "v_cvt_pk_bf16_f32 v131, v164, v165\n"
      "v_mfma_f32_16x16x32_bf16 v[154:157], v[206:209], a[172:175], v[154:157]\n"
      "buffer_load_dwordx4 v[162:165], v36, s[28:31], s47 offen\n"
      "v_mfma_f32_16x16x32_bf16 v[154:157], v[124:127], a[176:179], v[154:157]\n"
      "v_cvt_pk_bf16_f32 v195, v196, v197\n"
      "s_mov_b32 exec_lo, 0x1f001f\n"
      "s_mov_b32 exec_hi, 0x1f001f\n"
      "ds_write_b128 v147, v[128:131] offset:18688\n"
      "ds_write_b64 v147, v[194:195] offset:18704\n"
      "ds_write_b64 v147, v[214:215] offset:18712\n"
      "ds_write_b128 v147, v[100:103] offset:18720\n"
      "s_mov_b64 exec, -1\n"
      "v_mfma_f32_16x16x32_bf16 v[154:157], v[166:169], a[180:183], v[154:157]\n"
      "buffer_load_dword v204, v29, s[28:31], s47 offen\n"
      "v_mfma_f32_16x16x32_bf16 v[154:157], v[170:173], a[184:187], v[154:157]\n"
      "ds_read_b128 v[194:197], v8 offset:24576\n"
      "v_subrev_u32 v114, 115456, v9\n"
      "v_mfma_f32_16x16x32_bf16 v[154:157], v[190:193], a[188:191], v[154:157]\n"
      "ds_read_b128 v[214:217], v8 offset:28672\n"
      "v_lshrrev_b32 v114, 4, v114\n"
      "v_mfma_f32_16x16x32_bf16 v[144:147], v[186:189], a[192:195], 0\n"
      "ds_read_b128 v[130:133], v8 offset:32768\n"
      "v_and_b32_e64 v114, 48, v114\n"
      "v_mfma_f32_16x16x32_bf16 v[144:147], v[206:209], a[196:199], v[144:147]\n"
      "v_add_u32 v114, 123136, v114\n"
      "v_add_u32 v118, s92, v114\n"
      "v_mfma_f32_16x16x32_bf16 v[144:147], v[124:127], a[200:203], v[144:147]\n"
      "s_waitcnt vmcnt(9)\n"
      "v_or3_b32 v134, v134, v137, v149\n"
      "v_and_b32 v134, 0x7fff7fff, v134\n"
      "v_mfma_f32_16x16x32_bf16 v[144:147], v[166:169], a[204:207], v[144:147]\n"
      "v_xor_b32_e64 v128, 64, v8\n"
      "ds_read_b128 v[206:209], v128 offset:24576\n"
      "v_mfma_f32_16x16x32_bf16 v[144:147], v[170:173], a[208:211], v[144:147]\n"
      "ds_read_b128 v[166:169], v128 offset:28672\n"
      "ds_write_b32 v31, v134 offset:6656\n"
      "v_mfma_f32_16x16x32_bf16 v[144:147], v[190:193], a[212:215], v[144:147]\n"
      "s_waitcnt lgkmcnt(3)\n"
      "ds_read_b128 v[188:191], v128 offset:32768\n"
      "ds_read_b128 v[100:103], v0 offset:2048\n"
      "ds_read_b128 v[170:173], v1 offset:2048\n"
      "v_mov_b32 v126, 0xbc80bc80\n"
      "v_mov_b32 v127, 0xbc80bc80\n"
      "v_add_u32 v205, 49152, v8\n"
      "v_add_u32 v149, 49152, v128\n"
      "v_cvt_pk_bf16_f32 v211, v212, v213\n"
      "v_mov_b32 v128, 0xbc80bc80\n"
      "v_mov_b32 v129, 0xbc80bc80\n"
      "ds_write_b64 v148, v[210:211] offset:64\n"
      "v_subrev_u32 v134, 115456, v9\n"
      "v_mfma_f32_16x16x32_bf16 v[210:213], v[194:197], v[126:129], 0\n"
      "s_waitcnt lgkmcnt(5)\n"
      "v_bfe_u32 v124, v134, 8, 2\n"
      "v_bfe_u32 v134, v134, 4, 4\n"
      "v_mfma_f32_16x16x32_bf16 v[210:213], v[206:209], v[126:129], v[210:213]\n"
      "ds_read_b128 v[194:197], v205\n"
      "v_lshlrev_b32_e64 v124, 2, v124\n"
      "v_mfma_f32_16x16x32_bf16 v[210:213], v[214:217], v[126:129], v[210:213]\n"
      "ds_read_b128 v[206:209], v149\n"
      "v_sub_u32_e64 v134, v134, v124\n"
      "v_mfma_f32_16x16x32_bf16 v[210:213], v[166:169], v[126:129], v[210:213]\n"
      "ds_read_b128 v[166:169], v205 offset:4096\n"
      "v_lshlrev_b32 v134, 4, v134\n"
      "v_mfma_f32_16x16x32_bf16 v[210:213], v[130:133], v[126:129], v[210:213]\n"
      "s_waitcnt lgkmcnt(4)\n"
      "ds_read_b128 v[130:133], v149 offset:4096\n"
      "v_mfma_f32_16x16x32_bf16 v[210:213], v[188:191], v[126:129], v[210:213]\n"
      "ds_read_b128 v[188:191], v205 offset:8192\n"
      "v_cmp_gt_u32 s[80:81], 64, v134\n"
      "v_mov_b32 v124, 0x3f80\n"
      "v_cvt_pk_bf16_f32 v76, v182, v183\n"
      "v_mov_b32 v125, 0\n"
      "v_lshlrev_b64 v[124:125], v134, v[124:125]\n"
      "v_cndmask_b32_e64 v134, 0, v124, s[80:81]\n"
      "s_waitcnt lgkmcnt(4)\n"
      "v_mfma_f32_16x16x32_bf16 v[214:217], v[194:197], v[126:129], 0\n"
      "v_mul_f32 v210, 0x3eaaaaab, v210\n"
      "v_mul_f32 v211, 0x3eaaaaab, v211\n"
      "s_waitcnt lgkmcnt(3)\n"
      "v_mfma_f32_16x16x32_bf16 v[214:217], v[206:209], v[126:129], v[214:217]\n"
      "ds_read_b128 v[194:197], v149 offset:8192\n"
      "v_mul_f32 v212, 0x3eaaaaab, v212\n"
      "s_waitcnt lgkmcnt(3)\n"
      "v_mfma_f32_16x16x32_bf16 v[214:217], v[166:169], v[126:129], v[214:217]\n"
      "ds_read_b128 v[166:169], v0 offset:8192\n"
      "v_mul_f32 v213, 0x3eaaaaab, v213\n"
      "s_waitcnt lgkmcnt(3)\n"
      "v_mfma_f32_16x16x32_bf16 v[214:217], v[130:133], v[126:129], v[214:217]\n"
      "s_waitcnt lgkmcnt(2)\n"
      "ds_write_b128 v118, v[210:213]\n"
      "v_mfma_f32_16x16x32_bf16 v[214:217], v[188:191], v[126:129], v[214:217]\n"
      "ds_read_b128 v[210:213], v1 offset:8192\n"
      "ds_read_b128 v[186:189], v0 offset:14336\n"
      "ds_read_b128 v[190:193], v1 offset:14336\n"
      "s_waitcnt lgkmcnt(5)\n"
      "v_mfma_f32_16x16x32_bf16 v[214:217], v[194:197], v[126:129], v[214:217]\n"
      "v_mov_b32 v136, 0\n"
      "v_cndmask_b32_e64 v135, 0, v125, s[80:81]\n"
      "v_mov_b32_e64 v137, 0\n"
      "v_cndmask_b32_e64 v208, 0, v124, s[80:81]\n"
      "v_mov_b32 v206, 0\n"
      "v_cndmask_b32_e64 v209, 0, v125, s[80:81]\n"
      "v_cvt_pk_bf16_f32 v77, v184, v185\n"
      "v_mov_b32 v207, 0\n"
      "v_mul_f32 v214, 0x3eaaaaab, v214\n"
      "v_mul_f32 v215, 0x3eaaaaab, v215\n"
      "v_mul_f32 v216, 0x3eaaaaab, v216\n"
      "v_mul_f32 v217, 0x3eaaaaab, v217\n"
      "ds_write_b128 v118, v[214:217] offset:256\n"
      "v_mfma_f32_16x16x32_bf16 v[194:197], v[100:103], a[144:147], 0\n"
      "s_waitcnt lgkmcnt(3)\n"
      "v_cvt_pk_bf16_f32 v80, v154, v155\n"
      "v_cvt_pk_bf16_f32 v81, v156, v157\n"
      "v_mfma_f32_16x16x32_bf16 v[194:197], v[170:173], a[148:151], v[194:197]\n"
      "s_waitcnt lgkmcnt(2)\n"
      "v_cvt_pk_bf16_f32 v84, v144, v145\n"
      "v_cvt_pk_bf16_f32 v85, v146, v147\n"
      "v_mfma_f32_16x16x32_bf16 v[194:197], v[166:169], a[152:155], v[194:197]\n"
      "s_waitcnt lgkmcnt(1)\n"
      "v_cvt_pk_bf16_f32 v150, v150, v151\n"
      "v_cvt_pk_bf16_f32 v198, v198, v199\n"
      "v_mfma_f32_16x16x32_bf16 v[194:197], v[210:213], a[156:159], v[194:197]\n"
      "v_cvt_pk_bf16_f32 v178, v178, v179\n"
      "v_cvt_pk_bf16_f32 v151, v152, v153\n"
      "v_mfma_f32_16x16x32_bf16 v[194:197], v[186:189], a[160:163], v[194:197]\n"
      "ds_read_b128 v[144:147], v205 offset:24576\n"
      "s_waitcnt vmcnt(8)\n"
      "v_or3_b32 v218, v218, v219, v220\n"
      "v_mfma_f32_16x16x32_bf16 v[194:197], v[190:193], a[164:167], v[194:197]\n"
      "ds_read_b128 v[182:185], v149 offset:24576\n"
      "v_cvt_pk_bf16_f32 v199, v200, v201\n"
      "v_cvt_pk_bf16_f32 v179, v180, v181\n"
      "v_mfma_f32_16x16x32_bf16 v[130:133], v[100:103], a[168:171], 0\n"
      "s_mov_b32 exec_lo, 0x1f001f\n"
      "s_mov_b32 exec_hi, 0x1f001f\n"
      "ds_write_b64 v148, v[150:151] offset:18688\n"
      "ds_write_b64 v148, v[198:199] offset:18720\n"
      "ds_write_b64 v148, v[178:179] offset:18752\n"
      "s_mov_b64 exec, -1\n"
      "v_mfma_f32_16x16x32_bf16 v[130:133], v[170:173], a[172:175], v[130:133]\n"
      "ds_read_b128 v[176:179], v205 offset:28672\n"
      "s_waitcnt vmcnt(7)\n"
      "v_or3_b32 v218, v218, v221, v119\n"
      "v_mfma_f32_16x16x32_bf16 v[130:133], v[166:169], a[176:179], v[130:133]\n"
      "ds_read_b128 v[152:155], v149 offset:28672\n"
      "v_and_b32 v218, 0x7fff7fff, v218\n"
      "v_mfma_f32_16x16x32_bf16 v[130:133], v[210:213], a[180:183], v[130:133]\n"
      "v_cvt_pk_bf16_f32 v197, v196, v197\n"
      "v_cvt_pk_bf16_f32 v196, v194, v195\n"
      "v_mfma_f32_16x16x32_bf16 v[130:133], v[186:189], a[184:187], v[130:133]\n"
      "ds_read_b128 v[198:201], v205 offset:32768\n"
      "v_readlane_b32 s45, v37, s43\n"
      "v_mfma_f32_16x16x32_bf16 v[130:133], v[190:193], a[188:191], v[130:133]\n"
      "s_or_b32 s95, s45, s46\n"
      "s_or_b32 s95, s95, s48\n"
      "s_cmp_ge_u32 s95, 0x40000000\n"
      "s_cselect_b32 s94, -1, -2\n"
      "v_mfma_f32_16x16x32_bf16 v[214:217], v[100:103], a[192:195], 0\n"
      "ds_write_b32 v31, v218 offset:5632\n"
      "v_mfma_f32_16x16x32_bf16 v[214:217], v[170:173], a[196:199], v[214:217]\n"
      "ds_read_b128 v[218:221], v149 offset:32768\n"
      "v_cvt_pk_bf16_f32 v133, v132, v133\n"
      "v_mfma_f32_16x16x32_bf16 v[214:217], v[166:169], a[200:203], v[214:217]\n"
      "ds_read_b128 v[166:169], v0\n"
      "v_cvt_pk_bf16_f32 v132, v130, v131\n"
      "v_mfma_f32_16x16x32_bf16 v[214:217], v[210:213], a[204:207], v[214:217]\n"
      "ds_read_b128 v[210:213], v1\n"
      "ds_read_b128 v[170:173], v0 offset:6144\n"
      "v_mfma_f32_16x16x32_bf16 v[214:217], v[186:189], a[208:211], v[214:217]\n"
      "ds_read_b128 v[186:189], v1 offset:6144\n"
      "s_cmp_gt_u32 s42, s44\n"
      "s_cselect_b32 s95, s94, s95\n"
      "v_mfma_f32_16x16x32_bf16 v[214:217], v[190:193], a[212:215], v[214:217]\n"
      "s_waitcnt lgkmcnt(6)\n"
      "ds_read_b128 v[190:193], v0 offset:12288\n"
      "v_mfma_f32_16x16x32_bf16 v[100:103], v[144:147], v[126:129], 0\n"
      "v_readlane_b32 s47, v38, s43\n"
      "s_add_u32 s43, s43, 1\n"
      "v_mfma_f32_16x16x32_bf16 v[100:103], v[182:185], v[126:129], v[100:103]\n"
      "ds_read_b128 v[144:147], v1 offset:12288\n"
      "s_and_b32 s47, s47, -4\n"
      "v_mfma_f32_16x16x32_bf16 v[100:103], v[176:179], v[126:129], v[100:103]\n"
      "s_waitcnt lgkmcnt(6)\n"
      "v_cvt_pk_bf16_f32 v217, v216, v217\n"
      "v_cvt_pk_bf16_f32 v216, v214, v215\n"
      "v_mfma_f32_16x16x32_bf16 v[100:103], v[152:155], v[126:129], v[100:103]\n"
      "s_waitcnt lgkmcnt(5)\n"
      "v_mfma_f32_16x16x32_bf16 v[100:103], v[198:201], v[126:129], v[100:103]\n"
      "s_waitcnt lgkmcnt(4)\n"
      "v_mfma_f32_16x16x32_bf16 v[100:103], v[218:221], v[126:129], v[100:103]\n"
      "s_waitcnt lgkmcnt(3)\n"
      "v_mfma_f32_16x16x32_bf16 v[218:221], v[166:169], a[144:147], 0\n"
      "s_waitcnt lgkmcnt(2)\n"
      "v_mfma_f32_16x16x32_bf16 v[218:221], v[210:213], a[148:151], v[218:221]\n"
      "s_waitcnt lgkmcnt(1)\n"
      "v_mfma_f32_16x16x32_bf16 v[218:221], v[170:173], a[152:155], v[218:221]\n"
      "s_waitcnt lgkmcnt(0)\n"
      "v_mfma_f32_16x16x32_bf16 v[218:221], v[186:189], a[156:159], v[218:221]\n"
      "v_mul_f32 v100, 0x3eaaaaab, v100\n"
      "v_mul_f32 v101, 0x3eaaaaab, v101\n"
      "v_mfma_f32_16x16x32_bf16 v[218:221], v[190:193], a[160:163], v[218:221]\n"
      "v_mul_f32 v102, 0x3eaaaaab, v102\n"
      "v_mul_f32 v103, 0x3eaaaaab, v103\n"
      "v_mfma_f32_16x16x32_bf16 v[218:221], v[144:147], a[164:167], v[218:221]\n"
      "ds_write_b128 v118, v[100:103] offset:512\n"
      "v_mfma_f32_16x16x32_bf16 v[180:183], v[166:169], a[168:171], 0\n"
      "s_waitcnt lgkmcnt(0)\n"
      "s_barrier\n"
      "ds_read_b128 v[124:127], v9 offset:5632\n"
      "v_mfma_f32_16x16x32_bf16 v[180:183], v[210:213], a[172:175], v[180:183]\n"
      "ds_read_b128 v[176:179], v10 offset:32\n"
      "v_mfma_f32_16x16x32_bf16 v[180:183], v[170:173], a[176:179], v[180:183]\n"
      "v_cvt_pk_bf16_f32 v194, v218, v219\n"
      "v_cvt_pk_bf16_f32 v195, v220, v221\n"
      "v_mfma_f32_16x16x32_bf16 v[180:183], v[186:189], a[180:183], v[180:183]\n"
      "v_mfma_f32_16x16x32_bf16 v[180:183], v[190:193], a[184:187], v[180:183]\n"
      "v_mfma_f32_16x16x32_bf16 v[180:183], v[144:147], a[188:191], v[180:183]\n"
      "ds_read_b64 v[118:119], v10 offset:24\n"
      "v_mfma_f32_16x16x32_bf16 v[218:221], v[166:169], a[192:195], 0\n"
      "ds_read_b128 v[166:169], v114\n"
      "s_waitcnt lgkmcnt(3)\n"
      "v_or3_b32 v124, v124, v125, v126\n"
      "v_mfma_f32_16x16x32_bf16 v[218:221], v[210:213], a[196:199], v[218:221]\n"
      "ds_read_b128 v[210:213], v114 offset:64\n"
      "s_waitcnt vmcnt(1)\n"
      "v_or3_b32 v162, v162, v163, v164\n"
      "v_mfma_f32_16x16x32_bf16 v[218:221], v[170:173], a[200:203], v[218:221]\n"
      "v_cvt_pk_bf16_f32 v130, v180, v181\n"
      "v_cvt_pk_bf16_f32 v131, v182, v183\n"
      "v_mfma_f32_16x16x32_bf16 v[218:221], v[186:189], a[204:207], v[218:221]\n"
      "v_or_b32 v124, v124, v127\n"
      "v_mfma_f32_16x16x32_bf16 v[218:221], v[190:193], a[208:211], v[218:221]\n"
      "v_mfma_f32_16x16x32_bf16 v[218:221], v[144:147], a[212:215], v[218:221]\n"
      "s_waitcnt lgkmcnt(2)\n"
      "v_mfma_f32_16x16x32_bf16 v[188:191], v[138:141], v[96:99], v[40:43]\n"
      "v_mfma_f32_16x16x32_bf16 v[180:183], v[108:111], v[96:99], v[44:47]\n"
      "v_mfma_f32_16x16x32_bf16 v[144:147], v[158:161], v[96:99], v[48:51]\n"
      "v_mfma_f32_16x16x32_bf16 v[126:129], v[176:179], v[96:99], v[24:27]\n"
      "ds_read_b128 v[98:101], v9 offset:6656\n"
      "s_nop 0\n"
      "s_nop 0\n"
      "v_mfma_f32_16x16x16_bf16 v[188:191], v[174:175], v[202:203], v[188:191]\n"
      "v_cvt_pk_bf16_f32 v214, v218, v219\n"
      "v_cvt_pk_bf16_f32 v215, v220, v221\n"
      "s_waitcnt vmcnt(0)\n"
      "v_or3_b32 v162, v162, v165, v204\n"
      "v_mfma_f32_16x16x16_bf16 v[180:183], v[112:113], v[202:203], v[180:183]\n"
      "v_and_b32 v162, 0x7fff7fff, v162\n"
      "ds_write_b32 v31, v162 offset:4608\n"
      "v_mfma_f32_16x16x16_bf16 v[144:147], v[116:117], v[202:203], v[144:147]\n"
      "v_mfma_f32_16x16x16_bf16 v[126:129], v[118:119], v[202:203], v[126:129]\n"
      "s_waitcnt lgkmcnt(2)\n"
      "v_mfma_f32_16x16x32_bf16 v[218:221], v[134:137], a[216:219], v[166:169]\n"
      "s_waitcnt lgkmcnt(1)\n"
      "v_or3_b32 v98, v98, v99, v100\n"
      "v_or_b32 v98, v98, v101\n"
      "v_mfma_f32_16x16x32_bf16 v[202:205], v[206:209], a[216:219], v[210:213]\n"
      "v_cmp_ne_u32 s[56:57], 0, v98\n"
      "v_mfma_f32_16x16x32_bf16 v[162:165], v[134:137], a[240:243], v[166:169]\n"
      "s_or_b64 vcc, s[56:57], s[54:55]\n"
      "s_cbranch_vccnz L_rare_1_%=\n"
      "L_back_2_%=:\n"
      "v_mfma_f32_16x16x32_bf16 v[96:99], v[206:209], a[240:243], v[210:213]\n"
      "ds_read_u16 v78, v11\n"
      "s_cbranch_vccnz L_rare_3_%=\n"
      "L_back_4_%=:\n"
      "v_mfma_f32_16x16x32_bf16 v[152:155], v[134:137], v[232:235], v[166:169]\n"
      "s_add_u32 s42, s42, 1\n"
      "v_exp_f32_e64 v188, v188\n"
      "v_mfma_f32_16x16x32_bf16 v[166:169], v[206:209], v[232:235], v[210:213]\n"
      "ds_read_u16 v82, v11 offset:32\n"
      "v_exp_f32 v189, v189\n"
      "v_mfma_f32_16x16x32_bf16 v[210:213], v[138:141], v[120:123], v[52:55]\n"
      "v_exp_f32 v190, v190\n"
      "v_mfma_f32_16x16x32_bf16 v[198:201], v[108:111], v[120:123], v[56:59]\n"
      "ds_read_u16 v86, v11 offset:64\n"
      "v_exp_f32 v191, v191\n"
      "v_exp_f32 v180, v180\n"
      "v_exp_f32 v181, v181\n"
      "v_exp_f32 v182, v182\n"
      "v_exp_f32_e64 v183, v183\n"
      "v_cvt_pk_bf16_f32 v188, v188, v189\n"
      "v_cvt_pk_bf16_f32 v189, v190, v191\n"
      "v_cvt_pk_bf16_f32 v190, v180, v181\n"
      "v_cvt_pk_bf16_f32 v191, v182, v183\n"
      "v_mfma_f32_16x16x32_bf16 v[180:183], v[158:161], v[120:123], v[60:63]\n"
      "v_exp_f32 v144, v144\n"
      "v_mfma_f32_16x16x32_bf16 v[170:173], v[176:179], v[120:123], v[26:29]\n"
      "v_exp_f32 v145, v145\n"
      "v_mfma_f32_16x16x16_bf16 v[210:213], v[174:175], v[222:223], v[210:213]\n"
      "v_exp_f32 v146, v146\n"
      "v_exp_f32 v147, v147\n"
      "v_mfma_f32_16x16x16_bf16 v[198:201], v[112:113], v[222:223], v[198:201]\n"
      "v_exp_f32_e64 v126, v126\n"
      "v_cvt_pk_bf16_f32 v88, v144, v145\n"
      "v_mfma_f32_16x16x16_bf16 v[180:183], v[116:117], v[222:223], v[180:183]\n"
      "v_cvt_pk_bf16_f32 v89, v146, v147\n"
      "v_cvt_pk_bf16_f32 v90, v126, 0\n"
      "v_cmp_ne_u32 s[56:57], 0, v124\n"
      "v_mfma_f32_16x16x16_bf16 v[170:173], v[118:119], v[222:223], v[170:173]\n"
      "ds_read_b128 v[144:147], v114 offset:128\n"
      "ds_read_b128 v[124:127], v114 offset:192\n"
      "v_cvt_pk_bf16_f32 v218, v218, v219\n"
      "v_cvt_pk_bf16_f32 v220, v220, v221\n"
      "v_cvt_pk_bf16_f32 v202, v202, v203\n"
      "v_mfma_f32_16x16x32_bf16 v[120:123], v[32:35], v[188:191], 0\n"
      "s_waitcnt lgkmcnt(2)\n"
      "v_cvt_pk_bf16_f32 v204, v204, v205\n"
      "s_cbranch_vccnz L_rare_5_%=\n"
      "L_back_6_%=:\n"
      "v_mfma_f32_16x16x32_bf16 v[120:123], v[32:35], v[88:91], v[120:123]\n"
      "v_exp_f32_e64 v210, v210\n"
      "v_mfma_f32_16x16x32_bf16 v[100:103], v[194:197], v[188:191], 0\n"
      "v_exp_f32 v211, v211\n"
      "v_mfma_f32_16x16x32_bf16 v[100:103], v[76:79], v[88:91], v[100:103]\n"
      "v_exp_f32 v212, v212\n"
      "v_exp_f32 v213, v213\n"
      "v_exp_f32 v198, v198\n"
      "v_exp_f32 v199, v199\n"
      "v_exp_f32 v200, v200\n"
      "v_exp_f32_e64 v201, v201\n"
      "v_cvt_pk_bf16_f32 v210, v210, v211\n"
      "v_cvt_pk_bf16_f32 v211, v212, v213\n"
      "v_cvt_pk_bf16_f32 v212, v198, v199\n"
      "v_cvt_pk_bf16_f32 v213, v200, v201\n"
      "v_mfma_f32_16x16x32_bf16 v[184:187], v[130:133], v[188:191], 0\n"
      "s_waitcnt lgkmcnt(0)\n"
      "v_exp_f32 v180, v180\n"
      "v_mfma_f32_16x16x32_bf16 v[184:187], v[80:83], v[88:91], v[184:187]\n"
      "v_exp_f32 v181, v181\n"
      "v_mfma_f32_16x16x32_bf16 v[198:201], v[214:217], v[188:191], 0\n"
      "v_exp_f32 v182, v182\n"
      "v_mfma_f32_16x16x32_bf16 v[198:201], v[84:87], v[88:91], v[198:201]\n"
      "v_exp_f32 v183, v183\n"
      "v_exp_f32 v170, v170\n"
      "v_cvt_pk_bf16_f32 v92, v180, v181\n"
      "v_mfma_f32_16x16x32_bf16 v[188:191], v[134:137], a[220:223], v[144:147]\n"
      "v_cvt_pk_bf16_f32 v93, v182, v183\n"
      "v_cvt_pk_bf16_f32 v94, v170, 0\n"
      "v_rcp_f32 v120, v120\n"
      "v_cvt_pk_bf16_f32 v162, v162, v163\n"
      "v_mul_f32 v100, v120, v100\n"
      "v_mul_f32_e64 v101, v120, v101\n"
      "v_cvt_pk_bf16_f32 v100, v100, v101\n"
      "v_mul_f32 v102, v120, v102\n"
      "v_mul_f32 v103, v120, v103\n"
      "v_cvt_pk_bf16_f32 v101, v102, v103\n"
      "v_mul_f32 v184, v120, v184\n"
      "v_mul_f32 v185, v120, v185\n"
      "v_mul_f32 v186, v120, v186\n"
      "v_mul_f32 v187, v120, v187\n"
      "v_cvt_pk_bf16_f32 v102, v184, v185\n"
      "v_mfma_f32_16x16x32_bf16 v[180:183], v[206:209], a[220:223], v[124:127]\n"
      "v_cvt_pk_bf16_f32 v103, v186, v187\n"
      "v_mul_f32 v198, v120, v198\n"
      "v_mul_f32 v199, v120, v199\n"
      "ds_write_b128 v3, v[100:103]\n"
      "v_mfma_f32_16x16x32_bf16 v[100:103], v[134:137], a[244:247], v[144:147]\n"
      "v_cvt_pk_bf16_f32 v198, v198, v199\n"
      "v_mul_f32 v200, v120, v200\n"
      "v_mul_f32_e64 v201, v120, v201\n"
      "v_cvt_pk_bf16_f32 v199, v200, v201\n"
      "v_mfma_f32_16x16x32_bf16 v[170:173], v[206:209], a[244:247], v[124:127]\n"
      "v_cvt_pk_bf16_f32 v164, v164, v165\n"
      "ds_write_b64 v4, v[198:199]\n"
      "v_mfma_f32_16x16x32_bf16 v[184:187], v[134:137], v[236:239], v[144:147]\n"
      "s_nop 2\n"
      "ds_read_b128 v[144:147], v114 offset:256\n"
      "v_cvt_pk_bf16_f32 v96, v96, v97\n"
      "v_mfma_f32_16x16x32_bf16 v[198:201], v[206:209], v[236:239], v[124:127]\n"
      "v_cvt_pk_bf16_f32 v98, v98, v99\n"
      "v_cvt_pk_bf16_f32 v188, v188, v189\n"
      "v_cvt_pk_bf16_f32 v190, v190, v191\n"
      "v_mfma_f32_16x16x32_bf16 v[124:127], v[138:141], v[104:107], v[64:67]\n"
      "v_cvt_pk_bf16_f32 v180, v180, v181\n"
      "v_cvt_pk_bf16_f32 v182, v182, v183\n"
      "v_cvt_pk_bf16_f32 v100, v100, v101\n"
      "v_cvt_pk_bf16_f32 v102, v102, v103\n"
      "v_mfma_f32_16x16x32_bf16 v[120:123], v[108:111], v[104:107], v[68:71]\n"
      "v_cvt_pk_bf16_f32 v170, v170, v171\n"
      "v_cvt_pk_bf16_f32 v172, v172, v173\n"
      "v_accvgpr_write_b32 a216, v218\n"
      "v_accvgpr_write_b32 a217, v220\n"
      "v_mfma_f32_16x16x32_bf16 v[218:221], v[158:161], v[104:107], v[72:75]\n"
      "v_accvgpr_write_b32 a218, v202\n"
      "v_accvgpr_write_b32 a219, v204\n"
      "v_mfma_f32_16x16x32_bf16 v[202:205], v[176:179], v[104:107], v[28:31]\n"
      "v_accvgpr_write_b32 a240, v162\n"
      "v_accvgpr_write_b32 a241, v164\n"
      "v_mfma_f32_16x16x16_bf16 v[124:127], v[174:175], v[142:143], v[124:127]\n"
      "ds_read_b128 v[162:165], v114 offset:320\n"
      "v_accvgpr_write_b32 a242, v96\n"
      "v_mfma_f32_16x16x16_bf16 v[120:123], v[112:113], v[142:143], v[120:123]\n"
      "v_accvgpr_write_b32 a243, v98\n"
      "v_cvt_pk_bf16_f32 v232, v152, v153\n"
      "v_cvt_pk_bf16_f32 v233, v154, v155\n"
      "v_mfma_f32_16x16x16_bf16 v[218:221], v[116:117], v[142:143], v[218:221]\n"
      "v_cvt_pk_bf16_f32 v234, v166, v167\n"
      "v_cvt_pk_bf16_f32 v235, v168, v169\n"
      "v_accvgpr_write_b32 a220, v188\n"
      "v_mfma_f32_16x16x16_bf16 v[202:205], v[118:119], v[142:143], v[202:205]\n"
      "v_accvgpr_write_b32 a221, v190\n"
      "v_accvgpr_write_b32 a222, v180\n"
      "v_accvgpr_write_b32 a223, v182\n"
      "s_waitcnt lgkmcnt(1)\n"
      "v_mfma_f32_16x16x32_bf16 v[188:191], v[134:137], a[224:227], v[144:147]\n"
      "ds_read_b128 v[166:169], v114 offset:384\n"
      "v_accvgpr_write_b32 a244, v100\n"
      "v_accvgpr_write_b32 a245, v102\n"
      "s_cbranch_vccnz L_rare_7_%=\n"
      "L_back_8_%=:\n"
      "s_waitcnt lgkmcnt(1)\n"
      "v_mfma_f32_16x16x32_bf16 v[180:183], v[206:209], a[224:227], v[162:165]\n"
      "ds_read_b128 v[102:105], v114 offset:448\n"
      "v_exp_f32 v124, v124\n"
      "v_mfma_f32_16x16x32_bf16 v[152:155], v[134:137], a[248:251], v[144:147]\n"
      "v_exp_f32 v125, v125\n"
      "v_exp_f32 v126, v126\n"
      "v_exp_f32 v127, v127\n"
      "v_exp_f32 v120, v120\n"
      "v_exp_f32 v121, v121\n"
      "v_exp_f32 v122, v122\n"
      "v_exp_f32_e64 v123, v123\n"
      "v_cvt_pk_bf16_f32 v124, v124, v125\n"
      "v_cvt_pk_bf16_f32 v125, v126, v127\n"
      "v_cvt_pk_bf16_f32 v126, v120, v121\n"
      "v_cvt_pk_bf16_f32 v127, v122, v123\n"
      "v_mfma_f32_16x16x32_bf16 v[98:101], v[206:209], a[248:251], v[162:165]\n"
      "v_exp_f32 v218, v218\n"
      "v_mfma_f32_16x16x32_bf16 v[120:123], v[134:137], v[240:243], v[144:147]\n"
      "v_exp_f32 v219, v219\n"
      "v_mfma_f32_16x16x32_bf16 v[144:147], v[206:209], v[240:243], v[162:165]\n"
      "v_exp_f32_e64 v220, v220\n"
      "v_exp_f32_e64 v221, v221\n"
      "v_exp_f32_e64 v202, v202\n"
      "v_cvt_pk_bf16_f32 v88, v218, v219\n"
      "v_cvt_pk_bf16_f32 v89, v220, v221\n"
      "v_cvt_pk_bf16_f32 v90, v202, 0\n"
      "v_cvt_pk_bf16_f32 v188, v188, v189\n"
      "v_mfma_f32_16x16x32_bf16 v[218:221], v[32:35], v[210:213], 0\n"
      "v_cvt_pk_bf16_f32 v190, v190, v191\n"
      "v_cvt_pk_bf16_f32 v180, v180, v181\n"
      "v_mfma_f32_16x16x32_bf16 v[218:221], v[32:35], v[92:95], v[218:221]\n"
      "ds_read_b128 v[202:205], v10\n"
      "v_cvt_pk_bf16_f32 v182, v182, v183\n"
      "v_cvt_pk_bf16_f32 v152, v152, v153\n"
      "v_cvt_pk_bf16_f32 v154, v154, v155\n"
      "v_mfma_f32_16x16x32_bf16 v[162:165], v[194:197], v[210:213], 0\n"
      "v_cvt_pk_bf16_f32 v98, v98, v99\n"
      "v_cvt_pk_bf16_f32 v100, v100, v101\n"
      "v_mfma_f32_16x16x32_bf16 v[162:165], v[76:79], v[92:95], v[162:165]\n"
      "v_rcp_f32 v218, v218\n"
      "v_accvgpr_write_b32 a246, v170\n"
      "v_accvgpr_write_b32 a247, v172\n"
      "v_mfma_f32_16x16x32_bf16 v[220:223], v[130:133], v[210:213], 0\n"
      "s_waitcnt lgkmcnt(1)\n"
      "v_cvt_pk_bf16_f32 v236, v184, v185\n"
      "ds_read_b64 v[96:97], v10 offset:16\n"
      "v_mfma_f32_16x16x32_bf16 v[220:223], v[80:83], v[92:95], v[220:223]\n"
      "v_mul_f32 v162, v218, v162\n"
      "v_mul_f32 v163, v218, v163\n"
      "v_mfma_f32_16x16x32_bf16 v[170:173], v[214:217], v[210:213], 0\n"
      "v_cvt_pk_bf16_f32 v162, v162, v163\n"
      "v_mul_f32 v164, v218, v164\n"
      "v_mfma_f32_16x16x32_bf16 v[170:173], v[84:87], v[92:95], v[170:173]\n"
      "v_mul_f32 v165, v218, v165\n"
      "v_cvt_pk_bf16_f32 v163, v164, v165\n"
      "v_mul_f32 v220, v218, v220\n"
      "v_mul_f32 v221, v218, v221\n"
      "v_mul_f32 v222, v218, v222\n"
      "v_mul_f32 v223, v218, v223\n"
      "v_cvt_pk_bf16_f32 v164, v220, v221\n"
      "v_mfma_f32_16x16x32_bf16 v[210:213], v[134:137], a[228:231], v[166:169]\n"
      "v_cvt_pk_bf16_f32 v165, v222, v223\n"
      "v_mul_f32_e64 v170, v218, v170\n"
      "v_mul_f32_e64 v171, v218, v171\n"
      "ds_write_b128 v3, v[162:165] offset:256\n"
      "v_mfma_f32_16x16x32_bf16 v[148:151], v[206:209], a[228:231], v[102:105]\n"
      "v_cvt_pk_bf16_f32 v170, v170, v171\n"
      "v_mul_f32_e64 v172, v218, v172\n"
      "v_mul_f32 v173, v218, v173\n"
      "v_mfma_f32_16x16x32_bf16 v[162:165], v[134:137], a[252:255], v[166:169]\n"
      "s_waitcnt lgkmcnt(1)\n"
      "v_cvt_pk_bf16_f32 v171, v172, v173\n"
      "ds_write_b64 v4, v[170:171] offset:256\n"
      "v_mfma_f32_16x16x32_bf16 v[218:221], v[206:209], a[252:255], v[102:105]\n"
      "v_cvt_pk_bf16_f32 v210, v210, v211\n"
      "v_cvt_pk_bf16_f32 v212, v212, v213\n"
      "v_mfma_f32_16x16x32_bf16 v[170:173], v[134:137], v[244:247], v[166:169]\n"
      "v_cvt_pk_bf16_f32 v148, v148, v149\n"
      "v_cvt_pk_bf16_f32 v150, v150, v151\n"
      "v_mfma_f32_16x16x32_bf16 v[166:169], v[206:209], v[244:247], v[102:105]\n"
      "v_cvt_pk_bf16_f32 v162, v162, v163\n"
      "v_cvt_pk_bf16_f32 v164, v164, v165\n"
      "v_mfma_f32_16x16x32_bf16 v[102:105], v[202:205], v[138:141], v[30:33]\n"
      "v_cvt_pk_bf16_f32 v218, v218, v219\n"
      "v_cvt_pk_bf16_f32 v220, v220, v221\n"
      "v_mfma_f32_16x16x32_bf16 v[140:143], v[202:205], v[108:111], v[30:33]\n"
      "v_cvt_pk_bf16_f32 v237, v186, v187\n"
      "v_cvt_pk_bf16_f32 v238, v198, v199\n"
      "v_mfma_f32_16x16x32_bf16 v[108:111], v[202:205], v[158:161], v[30:33]\n"
      "ds_read_b128 v[158:161], v114 offset:512\n"
      "v_cvt_pk_bf16_f32 v239, v200, v201\n"
      "v_mfma_f32_16x16x32_bf16 v[184:187], v[202:205], v[176:179], v[30:33]\n"
      "ds_read_b128 v[202:205], v114 offset:576\n"
      "v_accvgpr_write_b32 a224, v188\n"
      "v_mfma_f32_16x16x16_bf16 v[102:105], v[96:97], v[174:175], v[102:105]\n"
      "v_accvgpr_write_b32 a225, v190\n"
      "v_accvgpr_write_b32 a226, v180\n"
      "v_accvgpr_write_b32 a227, v182\n"
      "v_mfma_f32_16x16x16_bf16 v[140:143], v[96:97], v[112:113], v[140:143]\n"
      "v_accvgpr_write_b32 a248, v152\n"
      "v_accvgpr_write_b32 a249, v154\n"
      "v_accvgpr_write_b32 a250, v98\n"
      "v_mfma_f32_16x16x16_bf16 v[108:111], v[96:97], v[116:117], v[108:111]\n"
      "v_accvgpr_write_b32 a251, v100\n"
      "v_cvt_pk_bf16_f32 v240, v120, v121\n"
      "v_cvt_pk_bf16_f32 v241, v122, v123\n"
      "v_mfma_f32_16x16x16_bf16 v[184:187], v[96:97], v[118:119], v[184:187]\n"
      "v_cndmask_b32 v102, v102, v140, s[58:59]\n"
      "v_cvt_pk_bf16_f32 v242, v144, v145\n"
      "v_cvt_pk_bf16_f32 v243, v146, v147\n"
      "v_mfma_f32_16x16x32_bf16 v[142:145], v[32:35], v[124:127], 0\n"
      "v_cndmask_b32 v102, v102, v108, s[60:61]\n"
      "v_accvgpr_write_b32 a228, v210\n"
      "v_mfma_f32_16x16x32_bf16 v[142:145], v[32:35], v[88:91], v[142:145]\n"
      "v_accvgpr_write_b32 a229, v212\n"
      "v_cndmask_b32 v102, v102, v184, s[62:63]\n"
      "v_mfma_f32_16x16x32_bf16 v[108:111], v[194:197], v[124:127], 0\n"
      "s_cbranch_vccnz L_rare_9_%=\n"
      "L_back_10_%=:\n"
      "v_mfma_f32_16x16x32_bf16 v[108:111], v[76:79], v[88:91], v[108:111]\n"
      "ds_read_b128 v[180:183], v114 offset:640\n"
      "v_exp_f32 v102, v102\n"
      "v_mfma_f32_16x16x32_bf16 v[188:191], v[130:133], v[124:127], 0\n"
      "ds_read_b128 v[144:147], v114 offset:704\n"
      "v_cvt_pk_bf16_f32 v102, v102, v102\n"
      "v_mfma_f32_16x16x32_bf16 v[188:191], v[80:83], v[88:91], v[188:191]\n"
      "ds_write_b16 v13, v102\n"
      "v_mfma_f32_16x16x32_bf16 v[138:141], v[214:217], v[124:127], 0\n"
      "ds_read_b128 v[210:213], v14\n"
      "v_rcp_f32 v142, v142\n"
      "v_mfma_f32_16x16x32_bf16 v[138:141], v[84:87], v[88:91], v[138:141]\n"
      "s_waitcnt lgkmcnt(4)\n"
      "v_mul_f32 v108, v142, v108\n"
      "v_mul_f32 v109, v142, v109\n"
      "v_mfma_f32_16x16x32_bf16 v[124:127], v[134:137], a[232:235], v[158:161]\n"
      "ds_read_b128 v[118:121], v14 offset:64\n"
      "v_cvt_pk_bf16_f32 v108, v108, v109\n"
      "v_mfma_f32_16x16x32_bf16 v[100:103], v[206:209], a[232:235], v[202:205]\n"
      "s_cmp_ge_u32 s95, 0x40000000\n"
      "s_cselect_b64 vcc, -1, s[56:57]\n"
      "v_mul_f32 v110, v142, v110\n"
      "v_mfma_f32_16x16x32_bf16 v[152:155], v[134:137], v[224:227], v[158:161]\n"
      "v_mul_f32 v111, v142, v111\n"
      "v_cvt_pk_bf16_f32 v109, v110, v111\n"
      "v_mfma_f32_16x16x32_bf16 v[96:99], v[206:209], v[224:227], v[202:205]\n"
      "v_mul_f32 v188, v142, v188\n"
      "v_mul_f32 v189, v142, v189\n"
      "v_mfma_f32_16x16x32_bf16 v[198:201], v[134:137], v[248:251], v[158:161]\n"
      "s_waitcnt lgkmcnt(2)\n"
      "v_mul_f32 v190, v142, v190\n"
      "v_mul_f32 v191, v142, v191\n"
      "v_mfma_f32_16x16x32_bf16 v[158:161], v[206:209], v[248:251], v[202:205]\n"
      "s_waitcnt lgkmcnt(1)\n"
      "v_cvt_pk_bf16_f32 v110, v188, v189\n"
      "v_cvt_pk_bf16_f32 v111, v190, v191\n"
      "v_mfma_f32_16x16x32_bf16 v[188:191], v[210:213], v[32:35], 0\n"
      "s_waitcnt lgkmcnt(0)\n"
      "v_mul_f32 v138, v142, v138\n"
      "v_mul_f32_e64 v139, v142, v139\n"
      "v_mfma_f32_16x16x32_bf16 v[188:191], v[118:121], v[32:35], v[188:191]\n"
      "ds_write_b128 v3, v[108:111] offset:512\n"
      "v_mfma_f32_16x16x32_bf16 v[108:111], v[134:137], a[236:239], v[180:183]\n"
      "v_cvt_pk_bf16_f32 v138, v138, v139\n"
      "v_mul_f32 v140, v142, v140\n"
      "v_mfma_f32_16x16x32_bf16 v[202:205], v[206:209], a[236:239], v[144:147]\n"
      "v_mul_f32 v141, v142, v141\n"
      "v_cvt_pk_bf16_f32 v139, v140, v141\n"
      "v_mfma_f32_16x16x32_bf16 v[140:143], v[134:137], v[228:231], v[180:183]\n"
      "ds_write_b64 v4, v[138:139] offset:512\n"
      "v_mfma_f32_16x16x32_bf16 v[104:107], v[206:209], v[228:231], v[144:147]\n"
      "v_cvt_pk_bf16_f32 v124, v124, v125\n"
      "v_cvt_pk_bf16_f32 v126, v126, v127\n"
      "v_mfma_f32_16x16x32_bf16 v[184:187], v[134:137], v[252:255], v[180:183]\n"
      "v_cvt_pk_bf16_f32 v100, v100, v101\n"
      "v_cvt_pk_bf16_f32 v102, v102, v103\n"
      "v_mfma_f32_16x16x32_bf16 v[180:183], v[206:209], v[252:255], v[144:147]\n"
      "v_cvt_pk_bf16_f32 v108, v108, v109\n"
      "v_cvt_pk_bf16_f32 v110, v110, v111\n"
      "v_cvt_pk_bf16_f32 v202, v202, v203\n"
      "v_cvt_pk_bf16_f32 v204, v204, v205\n"
      "v_accvgpr_write_b32 a230, v148\n"
      "v_accvgpr_write_b32 a231, v150\n"
      "v_accvgpr_write_b32 a252, v162\n"
      "v_accvgpr_write_b32 a253, v164\n"
      "v_accvgpr_write_b32 a254, v218\n"
      "v_accvgpr_write_b32 a255, v220\n"
      "v_cvt_pk_bf16_f32 v244, v170, v171\n"
      "v_cvt_pk_bf16_f32 v245, v172, v173\n"
      "v_cvt_pk_bf16_f32 v246, v166, v167\n"
      "v_cvt_pk_bf16_f32 v247, v168, v169\n"
      "v_accvgpr_write_b32 a232, v124\n"
      "v_accvgpr_write_b32 a233, v126\n"
      "v_accvgpr_write_b32 a234, v100\n"
      "v_accvgpr_write_b32 a235, v102\n"
      "v_cvt_pk_bf16_f32 v224, v152, v153\n"
      "v_cvt_pk_bf16_f32 v225, v154, v155\n"
      "v_cvt_pk_bf16_f32 v226, v96, v97\n"
      "v_cvt_pk_bf16_f32 v227, v98, v99\n"
      "v_cvt_pk_bf16_f32 v248, v198, v199\n"
      "v_cvt_pk_bf16_f32 v249, v200, v201\n"
      "v_cvt_pk_bf16_f32 v250, v158, v159\n"
      "v_cvt_pk_bf16_f32 v251, v160, v161\n"
      "v_accvgpr_write_b32 a236, v108\n"
      "v_accvgpr_write_b32 a237, v110\n"
      "v_accvgpr_write_b32 a238, v202\n"
      "v_accvgpr_write_b32 a239, v204\n"
      "v_cvt_pk_bf16_f32 v228, v140, v141\n"
      "v_cvt_pk_bf16_f32 v229, v142, v143\n"
      "v_cvt_pk_bf16_f32 v230, v104, v105\n"
      "v_cvt_pk_bf16_f32 v231, v106, v107\n"
      "v_cvt_pk_bf16_f32 v252, v184, v185\n"
      "v_cvt_pk_bf16_f32 v253, v186, v187\n"
      "v_cvt_pk_bf16_f32 v254, v180, v181\n"
      "v_cvt_pk_bf16_f32 v255, v182, v183\n"
      "s_waitcnt lgkmcnt(0)\n"
      "s_barrier\n"
      "s_or_b64 vcc, vcc, s[54:55]\n"
      "s_cbranch_vccnz L_disp1_%=\n"
      "s_nop 7\n"
      "s_nop 7\n"
      "L_loop1_%=:\n"
      "v_mfma_f32_16x16x32_bf16 v[202:205], v[210:213], v[194:197], 0\n"
      "ds_read_b128 v[206:209], v0 offset:18432\n"
      "ds_read_b128 v[162:165], v1 offset:18432\n"
      "v_mfma_f32_16x16x32_bf16 v[202:205], v[118:121], v[76:79], v[202:205]\n"
      "ds_read_b128 v[220:223], v0 offset:24576\n"
      "s_add_u32 m0, s64, 0\n"
      "v_mfma_f32_16x16x32_bf16 v[104:107], v[210:213], v[130:133], 0\n"
      "buffer_load_dwordx4 v16, s[16:19], s45 offen  lds\n"
      "v_mfma_f32_16x16x32_bf16 v[104:107], v[118:121], v[80:83], v[104:107]\n"
      "s_add_u32 m0, s64, 4096\n"
      "v_mfma_f32_16x16x32_bf16 v[174:177], v[210:213], v[214:217], 0\n"
      "ds_read_b128 v[216:219], v9\n"
      "v_mfma_f32_16x16x32_bf16 v[174:177], v[118:121], v[84:87], v[174:177]\n"
      "v_mov_b32_dpp v188, v188 quad_perm:[0,0,0,0] row_mask:0xf bank_mask:0xf\n"
      "v_rcp_f32_e64 v188, v188\n"
      "s_waitcnt lgkmcnt(3)\n"
      "v_mfma_f32_16x16x32_bf16 v[100:103], a[0:3], v[206:209], 0\n"
      "buffer_load_dwordx4 v17, s[16:19], s45 offen  lds\n"
      "v_mfma_f32_16x16x32_bf16 v[148:151], a[24:27], v[206:209], 0\n"
      "s_add_u32 m0, s64, 8192\n"
      "v_cndmask_b32 v202, v202, v104, s[58:59]\n"
      "v_cndmask_b32 v202, v202, v174, s[60:61]\n"
      "v_mfma_f32_16x16x32_bf16 v[184:187], a[48:51], v[206:209], 0\n"
      "s_waitcnt lgkmcnt(1)\n"
      "v_mul_f32 v202, v188, v202\n"
      "v_cvt_pk_bf16_f32 v202, v202, v202\n"
      "v_mfma_f32_16x16x32_bf16 v[170:173], a[72:75], v[206:209], 0\n"
      "ds_write_b16 v12, v202\n"
      "v_mfma_f32_16x16x32_bf16 v[198:201], a[96:99], v[206:209], 0\n"
      "buffer_load_dwordx4 v18, s[16:19], s45 offen  lds\n"
      "v_mfma_f32_16x16x32_bf16 v[174:177], a[120:123], v[206:209], 0\n"
      "ds_read_b128 v[118:121], v1 offset:24576\n"
      "s_waitcnt lgkmcnt(2)\n"
      "v_add_f32 v216, v216, v217\n"
      "v_mfma_f32_16x16x32_bf16 v[100:103], a[4:7], v[162:165], v[100:103]\n"
      "ds_read_b128 v[152:155], v0 offset:30720\n"
      "v_add_f32_e64 v218, v218, v219\n"
      "v_mfma_f32_16x16x32_bf16 v[100:103], a[8:11], v[220:223], v[100:103]\n"
      "ds_read_b128 v[202:205], v1 offset:30720\n"
      "v_add_f32 v216, v216, v218\n"
      "v_mfma_f32_16x16x32_bf16 v[148:151], a[28:31], v[162:165], v[148:151]\n"
      "s_add_u32 m0, s64, 12288\n"
      "v_rsq_f32 v216, v216\n"
      "v_mfma_f32_16x16x32_bf16 v[148:151], a[32:35], v[220:223], v[148:151]\n"
      "buffer_load_dwordx4 v19, s[16:19], s45 offen  lds\n"
      "v_mfma_f32_16x16x32_bf16 v[184:187], a[52:55], v[162:165], v[184:187]\n"
      "s_add_u32 m0, s65, 16384\n"
      "v_mul_f32 v216, 0x415db3d7, v216\n"
      "v_mov_b32 v218, v216\n"
      "v_mfma_f32_16x16x32_bf16 v[184:187], a[56:59], v[220:223], v[184:187]\n"
      "s_waitcnt lgkmcnt(2)\n"
      "v_permlane32_swap_b32 v216, v218\n"
      "v_mfma_f32_16x16x32_bf16 v[170:173], a[76:79], v[162:165], v[170:173]\n"
      "s_waitcnt lgkmcnt(1)\n"
      "v_mov_b32 v217, v216\n"
      "v_mul_f32 v144, v218, v144\n"
      "v_mfma_f32_16x16x32_bf16 v[170:173], a[80:83], v[220:223], v[170:173]\n"
      "buffer_load_dwordx4 v15, s[16:19], s45 offen  lds\n"
      "v_mfma_f32_16x16x32_bf16 v[198:201], a[100:103], v[162:165], v[198:201]\n"
      "s_waitcnt lgkmcnt(0)\n"
      "v_permlane16_swap_b32 v216, v217\n"
      "v_mfma_f32_16x16x32_bf16 v[198:201], a[104:107], v[220:223], v[198:201]\n"
      "v_mul_f32 v158, v216, v158\n"
      "v_mul_f32 v159, v216, v159\n"
      "v_mfma_f32_16x16x32_bf16 v[174:177], a[124:127], v[162:165], v[174:177]\n"
      "v_cvt_pk_bf16_f32 v158, v158, v159\n"
      "v_mul_f32_e64 v160, v216, v160\n"
      "v_mfma_f32_16x16x32_bf16 v[174:177], a[128:131], v[220:223], v[174:177]\n"
      "buffer_load_dwordx4 v[122:125], v36, s[28:31], s47 offen\n"
      "v_mfma_f32_16x16x32_bf16 v[100:103], a[12:15], v[118:121], v[100:103]\n"
      "v_mul_f32 v161, v216, v161\n"
      "v_mul_f32 v114, v216, v114\n"
      "v_mfma_f32_16x16x32_bf16 v[100:103], a[16:19], v[152:155], v[100:103]\n"
      "v_mul_f32 v115, v216, v115\n"
      "v_cvt_pk_bf16_f32 v159, v160, v161\n"
      "v_mfma_f32_16x16x32_bf16 v[100:103], a[20:23], v[202:205], v[100:103]\n"
      "v_mul_f32 v96, v216, v96\n"
      "v_mul_f32_e64 v97, v216, v97\n"
      "v_mfma_f32_16x16x32_bf16 v[148:151], a[36:39], v[118:121], v[148:151]\n"
      "buffer_load_dword v8, v29, s[28:31], s47 offen\n"
      "v_mfma_f32_16x16x32_bf16 v[148:151], a[40:43], v[152:155], v[148:151]\n"
      "v_mul_f32 v98, v216, v98\n"
      "v_mul_f32 v99, v216, v99\n"
      "v_mfma_f32_16x16x32_bf16 v[148:151], a[44:47], v[202:205], v[148:151]\n"
      "v_cvt_pk_bf16_f32 v114, v114, v115\n"
      "v_mul_f32 v116, v216, v116\n"
      "v_mfma_f32_16x16x32_bf16 v[184:187], a[60:63], v[118:121], v[184:187]\n"
      "v_mul_f32 v117, v216, v117\n"
      "v_cvt_pk_bf16_f32 v160, v96, v97\n"
      "v_mfma_f32_16x16x32_bf16 v[184:187], a[64:67], v[152:155], v[184:187]\n"
      "v_cvt_pk_bf16_f32 v161, v98, v99\n"
      "v_cvt_pk_bf16_f32 v115, v116, v117\n"
      "v_mfma_f32_16x16x32_bf16 v[184:187], a[68:71], v[202:205], v[184:187]\n"
      "ds_write_b128 v5, v[158:161] offset:18432\n"
      "v_mfma_f32_16x16x32_bf16 v[170:173], a[84:87], v[118:121], v[170:173]\n"
      "v_mul_f32 v178, v217, v178\n"
      "v_mul_f32 v179, v217, v179\n"
      "v_mfma_f32_16x16x32_bf16 v[170:173], a[88:91], v[152:155], v[170:173]\n"
      "v_cvt_pk_bf16_f32 v178, v178, v179\n"
      "v_mul_f32_e64 v180, v217, v180\n"
      "v_mfma_f32_16x16x32_bf16 v[170:173], a[92:95], v[202:205], v[170:173]\n"
      "ds_write_b64 v6, v[114:115] offset:18432\n"
      "v_mfma_f32_16x16x32_bf16 v[198:201], a[108:111], v[118:121], v[198:201]\n"
      "v_mul_f32 v181, v217, v181\n"
      "v_mul_f32 v126, v217, v126\n"
      "v_mfma_f32_16x16x32_bf16 v[198:201], a[112:115], v[152:155], v[198:201]\n"
      "v_mul_f32 v127, v217, v127\n"
      "v_cvt_pk_bf16_f32 v179, v180, v181\n"
      "v_mfma_f32_16x16x32_bf16 v[198:201], a[116:119], v[202:205], v[198:201]\n"
      "v_mul_f32 v166, v217, v166\n"
      "v_mul_f32_e64 v167, v217, v167\n"
      "v_mfma_f32_16x16x32_bf16 v[174:177], a[132:135], v[118:121], v[174:177]\n"
      "v_mul_f32 v168, v217, v168\n"
      "v_mul_f32 v169, v217, v169\n"
      "v_mfma_f32_16x16x32_bf16 v[174:177], a[136:139], v[152:155], v[174:177]\n"
      "v_cvt_pk_bf16_f32 v126, v126, v127\n"
      "v_mul_f32 v128, v217, v128\n"
      "v_mfma_f32_16x16x32_bf16 v[174:177], a[140:143], v[202:205], v[174:177]\n"
      "v_mul_f32 v129, v217, v129\n"
      "v_cvt_pk_bf16_f32 v180, v166, v167\n"
      "v_mfma_f32_16x16x32_bf16 v[194:197], v[206:209], a[144:147], 0\n"
      "v_cvt_pk_bf16_f32 v181, v168, v169\n"
      "v_cvt_pk_bf16_f32 v127, v128, v129\n"
      "v_mfma_f32_16x16x32_bf16 v[194:197], v[162:165], a[148:151], v[194:197]\n"
      "ds_write_b128 v5, v[178:181] offset:24576\n"
      "v_mfma_f32_16x16x32_bf16 v[194:197], v[220:223], a[152:155], v[194:197]\n"
      "v_mul_f32 v145, v218, v145\n"
      "v_cvt_pk_bf16_f32 v144, v144, v145\n"
      "v_mfma_f32_16x16x32_bf16 v[194:197], v[118:121], a[156:159], v[194:197]\n"
      "v_mul_f32 v146, v218, v146\n"
      "v_mul_f32_e64 v147, v218, v147\n"
      "v_mfma_f32_16x16x32_bf16 v[194:197], v[152:155], a[160:163], v[194:197]\n"
      "ds_write_b64 v6, v[126:127] offset:24576\n"
      "v_mfma_f32_16x16x32_bf16 v[194:197], v[202:205], a[164:167], v[194:197]\n"
      "ds_read_b128 v[158:161], v0 offset:20480\n"
      "v_mul_f32 v138, v218, v138\n"
      "v_mfma_f32_16x16x32_bf16 v[130:133], v[206:209], a[168:171], 0\n"
      "ds_read_b128 v[180:183], v1 offset:20480\n"
      "v_mul_f32 v139, v218, v139\n"
      "v_mfma_f32_16x16x32_bf16 v[130:133], v[162:165], a[172:175], v[130:133]\n"
      "ds_read_b128 v[210:213], v0 offset:26624\n"
      "v_cvt_pk_bf16_f32 v145, v146, v147\n"
      "v_mfma_f32_16x16x32_bf16 v[130:133], v[220:223], a[176:179], v[130:133]\n"
      "ds_read_b128 v[126:129], v1 offset:26624\n"
      "v_mul_f32 v108, v218, v108\n"
      "v_mfma_f32_16x16x32_bf16 v[130:133], v[118:121], a[180:183], v[130:133]\n"
      "ds_read_b128 v[104:107], v0 offset:32768\n"
      "v_mul_f32 v109, v218, v109\n"
      "v_mfma_f32_16x16x32_bf16 v[130:133], v[152:155], a[184:187], v[130:133]\n"
      "ds_read_b128 v[134:137], v1 offset:32768\n"
      "v_mul_f32 v110, v218, v110\n"
      "v_mfma_f32_16x16x32_bf16 v[130:133], v[202:205], a[188:191], v[130:133]\n"
      "v_mul_f32 v111, v218, v111\n"
      "v_cvt_pk_bf16_f32 v138, v138, v139\n"
      "v_mfma_f32_16x16x32_bf16 v[214:217], v[206:209], a[192:195], 0\n"
      "s_waitcnt lgkmcnt(5)\n"
      "v_mul_f32 v140, v218, v140\n"
      "v_mul_f32_e64 v141, v218, v141\n"
      "v_mfma_f32_16x16x32_bf16 v[214:217], v[162:165], a[196:199], v[214:217]\n"
      "s_waitcnt lgkmcnt(4)\n"
      "v_cvt_pk_bf16_f32 v146, v108, v109\n"
      "v_cvt_pk_bf16_f32 v147, v110, v111\n"
      "v_mfma_f32_16x16x32_bf16 v[214:217], v[220:223], a[200:203], v[214:217]\n"
      "ds_write_b128 v5, v[144:147] offset:30720\n"
      "v_mfma_f32_16x16x32_bf16 v[214:217], v[118:121], a[204:207], v[214:217]\n"
      "s_waitcnt lgkmcnt(2)\n"
      "v_cvt_pk_bf16_f32 v139, v140, v141\n"
      "v_fma_f32 v110, v100, v100, s51\n"
      "v_mfma_f32_16x16x32_bf16 v[214:217], v[152:155], a[208:211], v[214:217]\n"
      "s_waitcnt lgkmcnt(1)\n"
      "v_fma_f32 v115, v170, v170, s51\n"
      "v_fmac_f32 v110, v101, v101\n"
      "v_mfma_f32_16x16x32_bf16 v[214:217], v[202:205], a[212:215], v[214:217]\n"
      "ds_write_b64 v6, v[138:139] offset:30720\n"
      "v_mfma_f32_16x16x32_bf16 v[118:121], a[0:3], v[158:161], 0\n"
      "v_fmac_f32 v115, v171, v171\n"
      "v_fmac_f32 v110, v102, v102\n"
      "v_mfma_f32_16x16x32_bf16 v[118:121], a[4:7], v[180:183], v[118:121]\n"
      "v_fmac_f32 v115, v172, v172\n"
      "v_fmac_f32 v110, v103, v103\n"
      "v_mfma_f32_16x16x32_bf16 v[118:121], a[8:11], v[210:213], v[118:121]\n"
      "v_fmac_f32 v115, v173, v173\n"
      "v_fmac_f32 v110, v148, v148\n"
      "v_mfma_f32_16x16x32_bf16 v[118:121], a[12:15], v[126:129], v[118:121]\n"
      "v_fmac_f32 v115, v198, v198\n"
      "v_fmac_f32 v110, v149, v149\n"
      "v_mfma_f32_16x16x32_bf16 v[118:121], a[16:19], v[104:107], v[118:121]\n"
      "v_fmac_f32 v115, v199, v199\n"
      "v_fmac_f32 v110, v150, v150\n"
      "v_mfma_f32_16x16x32_bf16 v[118:121], a[20:23], v[134:137], v[118:121]\n"
      "v_fmac_f32 v115, v200, v200\n"
      "v_fmac_f32 v110, v151, v151\n"
      "v_mfma_f32_16x16x32_bf16 v[154:157], a[24:27], v[158:161], 0\n"
      "v_fmac_f32 v115, v201, v201\n"
      "v_fmac_f32 v110, v184, v184\n"
      "v_mfma_f32_16x16x32_bf16 v[154:157], a[28:31], v[180:183], v[154:157]\n"
      "v_fmac_f32 v115, v174, v174\n"
      "v_fmac_f32 v110, v185, v185\n"
      "v_mfma_f32_16x16x32_bf16 v[154:157], a[32:35], v[210:213], v[154:157]\n"
      "v_fmac_f32 v115, v175, v175\n"
      "v_fmac_f32 v110, v186, v186\n"
      "v_mfma_f32_16x16x32_bf16 v[154:157], a[36:39], v[126:129], v[154:157]\n"
      "v_fmac_f32 v115, v176, v176\n"
      "v_fmac_f32 v110, v187, v187\n"
      "v_mfma_f32_16x16x32_bf16 v[154:157], a[40:43], v[104:107], v[154:157]\n"
      "v_fmac_f32_e64 v115, v177, v177\n"
      "v_fma_f32 v222, v118, v118, s51\n"
      "v_mfma_f32_16x16x32_bf16 v[154:157], a[44:47], v[134:137], v[154:157]\n"
      "v_permlane16_swap_b32 v110, v115\n"
      "v_mfma_f32_16x16x32_bf16 v[218:221], a[48:51], v[158:161], 0\n"
      "v_add_f32 v110, v110, v115\n"
      "v_mov_b32 v115, v110\n"
      "v_mfma_f32_16x16x32_bf16 v[218:221], a[52:55], v[180:183], v[218:221]\n"
      "v_fmac_f32 v222, v119, v119\n"
      "v_permlane32_swap_b32 v110, v115\n"
      "v_mfma_f32_16x16x32_bf16 v[218:221], a[56:59], v[210:213], v[218:221]\n"
      "v_add_f32 v110, v110, v115\n"
      "v_rsq_f32 v110, v110\n"
      "v_mfma_f32_16x16x32_bf16 v[218:221], a[60:63], v[126:129], v[218:221]\n"
      "v_mov_b32 v115, v110\n"
      "v_fmac_f32 v222, v120, v120\n"
      "v_mfma_f32_16x16x32_bf16 v[218:221], a[64:67], v[104:107], v[218:221]\n"
      "v_permlane16_swap_b32 v110, v115\n"
      "v_mfma_f32_16x16x32_bf16 v[218:221], a[68:71], v[134:137], v[218:221]\n"
      "v_mul_f32 v110, s50, v110\n"
      "v_mul_f32 v170, v115, v170\n"
      "v_mfma_f32_16x16x32_bf16 v[138:141], a[72:75], v[158:161], 0\n"
      "v_mul_f32 v171, v115, v171\n"
      "v_mul_f32 v100, v110, v100\n"
      "v_mfma_f32_16x16x32_bf16 v[138:141], a[76:79], v[180:183], v[138:141]\n"
      "v_mul_f32 v101, v110, v101\n"
      "v_cvt_pk_bf16_f32 v170, v170, v171\n"
      "v_mfma_f32_16x16x32_bf16 v[138:141], a[80:83], v[210:213], v[138:141]\n"
      "v_mul_f32 v172, v115, v172\n"
      "v_mul_f32 v173, v115, v173\n"
      "v_mfma_f32_16x16x32_bf16 v[138:141], a[84:87], v[126:129], v[138:141]\n"
      "v_mul_f32 v174, v115, v174\n"
      "v_mul_f32_e64 v175, v115, v175\n"
      "v_mfma_f32_16x16x32_bf16 v[138:141], a[88:91], v[104:107], v[138:141]\n"
      "v_cvt_pk_bf16_f32 v100, v100, v101\n"
      "v_mul_f32 v102, v110, v102\n"
      "v_mfma_f32_16x16x32_bf16 v[138:141], a[92:95], v[134:137], v[138:141]\n"
      "v_mul_f32 v103, v110, v103\n"
      "v_mul_f32_e64 v184, v110, v184\n"
      "v_mfma_f32_16x16x32_bf16 v[162:165], a[96:99], v[158:161], 0\n"
      "v_mul_f32 v185, v110, v185\n"
      "v_cvt_pk_bf16_f32 v171, v172, v173\n"
      "v_mfma_f32_16x16x32_bf16 v[162:165], a[100:103], v[180:183], v[162:165]\n"
      "v_mul_f32 v198, v115, v198\n"
      "v_mul_f32 v199, v115, v199\n"
      "v_mfma_f32_16x16x32_bf16 v[162:165], a[104:107], v[210:213], v[162:165]\n"
      "v_mul_f32 v200, v115, v200\n"
      "v_mul_f32_e64 v201, v115, v201\n"
      "v_mfma_f32_16x16x32_bf16 v[162:165], a[108:111], v[126:129], v[162:165]\n"
      "v_cvt_pk_bf16_f32 v174, v174, v175\n"
      "v_mul_f32 v176, v115, v176\n"
      "v_mfma_f32_16x16x32_bf16 v[162:165], a[112:115], v[104:107], v[162:165]\n"
      "v_mul_f32 v177, v115, v177\n"
      "v_cvt_pk_bf16_f32 v101, v102, v103\n"
      "v_mfma_f32_16x16x32_bf16 v[162:165], a[116:119], v[134:137], v[162:165]\n"
      "v_mul_f32 v148, v110, v148\n"
      "v_mul_f32 v149, v110, v149\n"
      "v_mfma_f32_16x16x32_bf16 v[112:115], a[120:123], v[158:161], 0\n"
      "v_mul_f32 v150, v110, v150\n"
      "v_mul_f32 v151, v110, v151\n"
      "v_mfma_f32_16x16x32_bf16 v[112:115], a[124:127], v[180:183], v[112:115]\n"
      "v_cvt_pk_bf16_f32 v184, v184, v185\n"
      "v_mul_f32 v186, v110, v186\n"
      "v_mfma_f32_16x16x32_bf16 v[112:115], a[128:131], v[210:213], v[112:115]\n"
      "v_mul_f32 v187, v110, v187\n"
      "v_cvt_pk_bf16_f32 v172, v198, v199\n"
      "v_mfma_f32_16x16x32_bf16 v[112:115], a[132:135], v[126:129], v[112:115]\n"
      "ds_read_b128 v[202:205], v7\n"
      "v_cvt_pk_bf16_f32 v173, v200, v201\n"
      "v_mfma_f32_16x16x32_bf16 v[112:115], a[136:139], v[104:107], v[112:115]\n"
      "ds_read_b128 v[198:201], v7 offset:4096\n"
      "v_cvt_pk_bf16_f32 v175, v176, v177\n"
      "v_mfma_f32_16x16x32_bf16 v[112:115], a[140:143], v[134:137], v[112:115]\n"
      "v_cvt_pk_bf16_f32 v102, v148, v149\n"
      "v_cvt_pk_bf16_f32 v103, v150, v151\n"
      "v_mfma_f32_16x16x32_bf16 v[206:209], v[158:161], a[144:147], 0\n"
      "ds_read_b128 v[108:111], v7 offset:8192\n"
      "v_cvt_pk_bf16_f32 v185, v186, v187\n"
      "v_mfma_f32_16x16x32_bf16 v[206:209], v[180:183], a[148:151], v[206:209]\n"
      "ds_read_b128 v[190:193], v7 offset:12288\n"
      "v_fma_f32 v223, v138, v138, s51\n"
      "v_mfma_f32_16x16x32_bf16 v[206:209], v[210:213], a[152:155], v[206:209]\n"
      "v_fmac_f32 v223, v139, v139\n"
      "v_fmac_f32 v223, v140, v140\n"
      "v_mfma_f32_16x16x32_bf16 v[206:209], v[126:129], a[156:159], v[206:209]\n"
      "ds_read_b128 v[176:179], v7 offset:16384\n"
      "v_fmac_f32 v222, v121, v121\n"
      "v_mfma_f32_16x16x32_bf16 v[206:209], v[104:107], a[160:163], v[206:209]\n"
      "v_fmac_f32 v223, v141, v141\n"
      "v_fmac_f32_e64 v222, v154, v154\n"
      "v_mfma_f32_16x16x32_bf16 v[206:209], v[134:137], a[164:167], v[206:209]\n"
      "ds_read_b128 v[166:169], v0 offset:22528\n"
      "v_fmac_f32 v223, v162, v162\n"
      "v_mfma_f32_16x16x32_bf16 v[186:189], v[158:161], a[168:171], 0\n"
      "ds_read_b128 v[150:153], v1 offset:22528\n"
      "v_fmac_f32 v222, v155, v155\n"
      "v_mfma_f32_16x16x32_bf16 v[186:189], v[180:183], a[172:175], v[186:189]\n"
      "s_waitcnt lgkmcnt(3)\n"
      "v_fmac_f32 v223, v163, v163\n"
      "v_fmac_f32_e64 v222, v156, v156\n"
      "v_mfma_f32_16x16x32_bf16 v[186:189], v[210:213], a[176:179], v[186:189]\n"
      "buffer_store_dwordx4 v[202:205], v20, s[24:27], s46 offen sc1\n"
      "v_mfma_f32_16x16x32_bf16 v[186:189], v[126:129], a[180:183], v[186:189]\n"
      "ds_read_b128 v[96:99], v0 offset:28672\n"
      "v_fmac_f32_e64 v223, v164, v164\n"
      "v_mfma_f32_16x16x32_bf16 v[186:189], v[104:107], a[184:187], v[186:189]\n"
      "ds_read_b128 v[146:149], v1 offset:28672\n"
      "v_fmac_f32_e64 v222, v157, v157\n"
      "v_mfma_f32_16x16x32_bf16 v[186:189], v[134:137], a[188:191], v[186:189]\n"
      "ds_read_b128 v[202:205], v0 offset:34816\n"
      "v_fmac_f32 v223, v165, v165\n"
      "v_mfma_f32_16x16x32_bf16 v[142:145], v[158:161], a[192:195], 0\n"
      "s_waitcnt lgkmcnt(3)\n"
      "v_fmac_f32 v222, v218, v218\n"
      "v_fmac_f32 v223, v112, v112\n"
      "v_mfma_f32_16x16x32_bf16 v[142:145], v[180:183], a[196:199], v[142:145]\n"
      "ds_read_b128 v[158:161], v1 offset:34816\n"
      "v_fmac_f32 v222, v219, v219\n"
      "v_mfma_f32_16x16x32_bf16 v[142:145], v[210:213], a[200:203], v[142:145]\n"
      "v_fmac_f32 v223, v113, v113\n"
      "v_fmac_f32 v222, v220, v220\n"
      "v_mfma_f32_16x16x32_bf16 v[142:145], v[126:129], a[204:207], v[142:145]\n"
      "s_waitcnt lgkmcnt(3)\n"
      "v_fmac_f32 v223, v114, v114\n"
      "v_fmac_f32 v222, v221, v221\n"
      "v_mfma_f32_16x16x32_bf16 v[142:145], v[104:107], a[208:211], v[142:145]\n"
      "buffer_store_dwordx4 v[198:201], v21, s[24:27], s46 offen sc1\n"
      "v_mfma_f32_16x16x32_bf16 v[142:145], v[134:137], a[212:215], v[142:145]\n"
      "s_waitcnt lgkmcnt(1)\n"
      "v_fmac_f32 v223, v115, v115\n"
      "v_cvt_pk_bf16_f32 v194, v194, v195\n"
      "v_mfma_f32_16x16x32_bf16 v[134:137], a[0:3], v[166:169], 0\n"
      "s_waitcnt lgkmcnt(0)\n"
      "v_permlane16_swap_b32 v222, v223\n"
      "v_mfma_f32_16x16x32_bf16 v[134:137], a[4:7], v[150:153], v[134:137]\n"
      "v_add_f32 v222, v222, v223\n"
      "v_mov_b32 v223, v222\n"
      "v_mfma_f32_16x16x32_bf16 v[134:137], a[8:11], v[96:99], v[134:137]\n"
      "v_cvt_pk_bf16_f32 v195, v196, v197\n"
      "v_permlane32_swap_b32 v222, v223\n"
      "v_mfma_f32_16x16x32_bf16 v[134:137], a[12:15], v[146:149], v[134:137]\n"
      "v_add_f32 v222, v222, v223\n"
      "v_rsq_f32 v222, v222\n"
      "v_mfma_f32_16x16x32_bf16 v[134:137], a[16:19], v[202:205], v[134:137]\n"
      "v_mov_b32 v223, v222\n"
      "v_cvt_pk_bf16_f32 v196, v206, v207\n"
      "v_mfma_f32_16x16x32_bf16 v[134:137], a[20:23], v[158:161], v[134:137]\n"
      "v_permlane16_swap_b32 v222, v223\n"
      "v_mfma_f32_16x16x32_bf16 v[210:213], a[24:27], v[166:169], 0\n"
      "buffer_store_dwordx4 v[108:111], v22, s[24:27], s46 offen sc1\n"
      "v_mfma_f32_16x16x32_bf16 v[210:213], a[28:31], v[150:153], v[210:213]\n"
      "v_mul_f32 v222, s50, v222\n"
      "v_mul_f32 v138, v223, v138\n"
      "v_mfma_f32_16x16x32_bf16 v[210:213], a[32:35], v[96:99], v[210:213]\n"
      "v_mul_f32 v139, v223, v139\n"
      "v_mul_f32 v118, v222, v118\n"
      "v_mfma_f32_16x16x32_bf16 v[210:213], a[36:39], v[146:149], v[210:213]\n"
      "v_mul_f32 v119, v222, v119\n"
      "v_cvt_pk_bf16_f32 v138, v138, v139\n"
      "v_mfma_f32_16x16x32_bf16 v[210:213], a[40:43], v[202:205], v[210:213]\n"
      "v_mul_f32 v140, v223, v140\n"
      "v_mul_f32 v141, v223, v141\n"
      "v_mfma_f32_16x16x32_bf16 v[210:213], a[44:47], v[158:161], v[210:213]\n"
      "v_mul_f32 v112, v223, v112\n"
      "v_mul_f32 v113, v223, v113\n"
      "v_mfma_f32_16x16x32_bf16 v[108:111], a[48:51], v[166:169], 0\n"
      "v_cvt_pk_bf16_f32 v118, v118, v119\n"
      "v_mul_f32_e64 v120, v222, v120\n"
      "v_mfma_f32_16x16x32_bf16 v[108:111], a[52:55], v[150:153], v[108:111]\n"
      "v_mul_f32 v121, v222, v121\n"
      "v_mul_f32 v218, v222, v218\n"
      "v_mfma_f32_16x16x32_bf16 v[108:111], a[56:59], v[96:99], v[108:111]\n"
      "buffer_store_dwordx4 v[190:193], v23, s[24:27], s46 offen sc1\n"
      "v_mfma_f32_16x16x32_bf16 v[108:111], a[60:63], v[146:149], v[108:111]\n"
      "v_mul_f32 v219, v222, v219\n"
      "v_cvt_pk_bf16_f32 v139, v140, v141\n"
      "v_mfma_f32_16x16x32_bf16 v[108:111], a[64:67], v[202:205], v[108:111]\n"
      "v_mul_f32 v162, v223, v162\n"
      "v_mul_f32 v163, v223, v163\n"
      "v_mfma_f32_16x16x32_bf16 v[108:111], a[68:71], v[158:161], v[108:111]\n"
      "v_mul_f32 v164, v223, v164\n"
      "v_mul_f32_e64 v165, v223, v165\n"
      "v_mfma_f32_16x16x32_bf16 v[190:193], a[72:75], v[166:169], 0\n"
      "v_cvt_pk_bf16_f32 v112, v112, v113\n"
      "v_mul_f32 v114, v223, v114\n"
      "v_mfma_f32_16x16x32_bf16 v[190:193], a[76:79], v[150:153], v[190:193]\n"
      "ds_read_b128 v[198:201], v9 offset:4608\n"
      "v_mul_f32 v115, v223, v115\n"
      "v_mfma_f32_16x16x32_bf16 v[190:193], a[80:83], v[96:99], v[190:193]\n"
      "v_cvt_pk_bf16_f32 v119, v120, v121\n"
      "v_mul_f32 v154, v222, v154\n"
      "v_mfma_f32_16x16x32_bf16 v[190:193], a[84:87], v[146:149], v[190:193]\n"
      "v_mul_f32 v155, v222, v155\n"
      "v_mul_f32 v156, v222, v156\n"
      "v_mfma_f32_16x16x32_bf16 v[190:193], a[88:91], v[202:205], v[190:193]\n"
      "buffer_store_dwordx4 v[176:179], v25, s[24:27], s46 offen sc1\n"
      "v_mfma_f32_16x16x32_bf16 v[190:193], a[92:95], v[158:161], v[190:193]\n"
      "v_mul_f32 v157, v222, v157\n"
      "v_cvt_pk_bf16_f32 v218, v218, v219\n"
      "v_mfma_f32_16x16x32_bf16 v[176:179], a[96:99], v[166:169], 0\n"
      "v_mul_f32 v220, v222, v220\n"
      "v_mul_f32 v221, v222, v221\n"
      "v_mfma_f32_16x16x32_bf16 v[176:179], a[100:103], v[150:153], v[176:179]\n"
      "v_cvt_pk_bf16_f32 v140, v162, v163\n"
      "v_cvt_pk_bf16_f32 v141, v164, v165\n"
      "v_mfma_f32_16x16x32_bf16 v[176:179], a[104:107], v[96:99], v[176:179]\n"
      "v_cvt_pk_bf16_f32 v113, v114, v115\n"
      "v_cvt_pk_bf16_f32 v120, v154, v155\n"
      "v_mfma_f32_16x16x32_bf16 v[176:179], a[108:111], v[146:149], v[176:179]\n"
      "v_cvt_pk_bf16_f32 v121, v156, v157\n"
      "v_cvt_pk_bf16_f32 v219, v220, v221\n"
      "v_mfma_f32_16x16x32_bf16 v[176:179], a[112:115], v[202:205], v[176:179]\n"
      "v_fma_f32 v77, v134, v134, s51\n"
      "v_fma_f32 v114, v190, v190, s51\n"
      "v_mfma_f32_16x16x32_bf16 v[176:179], a[116:119], v[158:161], v[176:179]\n"
      "v_fmac_f32 v77, v135, v135\n"
      "v_fmac_f32 v114, v191, v191\n"
      "v_mfma_f32_16x16x32_bf16 v[162:165], a[120:123], v[166:169], 0\n"
      "v_fmac_f32 v77, v136, v136\n"
      "v_fmac_f32 v114, v192, v192\n"
      "v_mfma_f32_16x16x32_bf16 v[162:165], a[124:127], v[150:153], v[162:165]\n"
      "v_fmac_f32 v77, v137, v137\n"
      "v_fmac_f32 v114, v193, v193\n"
      "v_mfma_f32_16x16x32_bf16 v[162:165], a[128:131], v[96:99], v[162:165]\n"
      "v_fmac_f32 v77, v210, v210\n"
      "v_fmac_f32 v114, v176, v176\n"
      "v_mfma_f32_16x16x32_bf16 v[162:165], a[132:135], v[146:149], v[162:165]\n"
      "v_fmac_f32 v77, v211, v211\n"
      "v_fmac_f32 v114, v177, v177\n"
      "v_mfma_f32_16x16x32_bf16 v[162:165], a[136:139], v[202:205], v[162:165]\n"
      "v_fmac_f32 v77, v212, v212\n"
      "v_fmac_f32 v114, v178, v178\n"
      "v_mfma_f32_16x16x32_bf16 v[162:165], a[140:143], v[158:161], v[162:165]\n"
      "v_fmac_f32 v77, v213, v213\n"
      "v_fmac_f32 v114, v179, v179\n"
      "v_mfma_f32_16x16x32_bf16 v[180:183], v[166:169], a[144:147], 0\n"
      "v_fmac_f32 v77, v108, v108\n"
      "v_fmac_f32 v77, v109, v109\n"
      "v_mfma_f32_16x16x32_bf16 v[180:183], v[150:153], a[148:151], v[180:183]\n"
      "v_fmac_f32 v77, v110, v110\n"
      "v_fmac_f32 v77, v111, v111\n"
      "v_mfma_f32_16x16x32_bf16 v[180:183], v[96:99], a[152:155], v[180:183]\n"
      "v_fmac_f32 v114, v162, v162\n"
      "v_fmac_f32 v114, v163, v163\n"
      "v_mfma_f32_16x16x32_bf16 v[180:183], v[146:149], a[156:159], v[180:183]\n"
      "v_fmac_f32 v114, v164, v164\n"
      "v_fmac_f32 v114, v165, v165\n"
      "v_mfma_f32_16x16x32_bf16 v[180:183], v[202:205], a[160:163], v[180:183]\n"
      "v_cvt_pk_bf16_f32 v197, v208, v209\n"
      "v_permlane16_swap_b32_e64 v77, v114\n"
      "v_mfma_f32_16x16x32_bf16 v[180:183], v[158:161], a[164:167], v[180:183]\n"
      "ds_read_b128 v[220:223], v2\n"
      "v_add_f32_e64 v77, v77, v114\n"
      "v_mfma_f32_16x16x32_bf16 v[104:107], v[166:169], a[168:171], 0\n"
      "ds_read_b128 v[206:209], v2 offset:3072\n"
      "v_mov_b32 v114, v77\n"
      "v_mfma_f32_16x16x32_bf16 v[104:107], v[150:153], a[172:175], v[104:107]\n"
      "ds_read_b128 v[154:157], v2 offset:6144\n"
      "v_permlane32_swap_b32 v77, v114\n"
      "v_mfma_f32_16x16x32_bf16 v[104:107], v[96:99], a[176:179], v[104:107]\n"
      "v_add_f32 v77, v77, v114\n"
      "v_rsq_f32 v77, v77\n"
      "v_mfma_f32_16x16x32_bf16 v[104:107], v[146:149], a[180:183], v[104:107]\n"
      "v_mov_b32_e64 v114, v77\n"
      "v_cvt_pk_bf16_f32 v76, v180, v181\n"
      "v_mfma_f32_16x16x32_bf16 v[104:107], v[202:205], a[184:187], v[104:107]\n"
      "v_permlane16_swap_b32 v77, v114\n"
      "v_mfma_f32_16x16x32_bf16 v[104:107], v[158:161], a[188:191], v[104:107]\n"
      "s_waitcnt lgkmcnt(2)\n"
      "v_mul_f32 v77, s50, v77\n"
      "v_mul_f32 v190, v114, v190\n"
      "v_mfma_f32_16x16x32_bf16 v[126:129], v[166:169], a[192:195], 0\n"
      "s_waitcnt lgkmcnt(1)\n"
      "v_mul_f32 v191, v114, v191\n"
      "v_mul_f32_e64 v134, v77, v134\n"
      "v_mfma_f32_16x16x32_bf16 v[126:129], v[150:153], a[196:199], v[126:129]\n"
      "s_waitcnt lgkmcnt(0)\n"
      "v_mul_f32 v135, v77, v135\n"
      "v_cvt_pk_bf16_f32 v190, v190, v191\n"
      "v_mfma_f32_16x16x32_bf16 v[126:129], v[96:99], a[200:203], v[126:129]\n"
      "ds_read_b128 v[166:169], v2 offset:9216\n"
      "v_mul_f32 v192, v114, v192\n"
      "v_mfma_f32_16x16x32_bf16 v[126:129], v[146:149], a[204:207], v[126:129]\n"
      "ds_read_b128 v[146:149], v2 offset:12288\n"
      "v_mul_f32 v193, v114, v193\n"
      "v_mfma_f32_16x16x32_bf16 v[126:129], v[202:205], a[208:211], v[126:129]\n"
      "ds_read_b128 v[150:153], v2 offset:15360\n"
      "v_mul_f32 v162, v114, v162\n"
      "v_mfma_f32_16x16x32_bf16 v[126:129], v[158:161], a[212:215], v[126:129]\n"
      "v_mul_f32 v163, v114, v163\n"
      "v_cvt_pk_bf16_f32 v134, v134, v135\n"
      "v_mfma_f32_16x16x32_bf16 v[158:161], a[216:219], v[220:223], 0\n"
      "v_mul_f32 v136, v77, v136\n"
      "v_mul_f32 v137, v77, v137\n"
      "v_mfma_f32_16x16x32_bf16 v[158:161], a[220:223], v[206:209], v[158:161]\n"
      "v_mul_f32 v108, v77, v108\n"
      "v_mul_f32 v109, v77, v109\n"
      "v_mfma_f32_16x16x32_bf16 v[158:161], a[224:227], v[154:157], v[158:161]\n"
      "s_waitcnt lgkmcnt(2)\n"
      "v_cvt_pk_bf16_f32 v191, v192, v193\n"
      "v_mul_f32 v176, v114, v176\n"
      "v_mfma_f32_16x16x32_bf16 v[96:99], a[240:243], v[220:223], 0\n"
      "s_waitcnt lgkmcnt(1)\n"
      "v_mul_f32 v177, v114, v177\n"
      "v_mul_f32 v178, v114, v178\n"
      "v_mfma_f32_16x16x32_bf16 v[96:99], a[244:247], v[206:209], v[96:99]\n"
      "s_waitcnt lgkmcnt(0)\n"
      "v_mul_f32 v179, v114, v179\n"
      "v_cvt_pk_bf16_f32 v162, v162, v163\n"
      "v_mfma_f32_16x16x32_bf16 v[96:99], a[248:251], v[154:157], v[96:99]\n"
      "v_mul_f32 v164, v114, v164\n"
      "v_mul_f32_e64 v165, v114, v165\n"
      "v_mfma_f32_16x16x32_bf16 v[114:117], v[232:235], v[220:223], 0\n"
      "v_cvt_pk_bf16_f32 v135, v136, v137\n"
      "v_mul_f32 v210, v77, v210\n"
      "v_mfma_f32_16x16x32_bf16 v[114:117], v[236:239], v[206:209], v[114:117]\n"
      "v_mul_f32 v211, v77, v211\n"
      "v_mul_f32 v212, v77, v212\n"
      "v_mfma_f32_16x16x32_bf16 v[114:117], v[240:243], v[154:157], v[114:117]\n"
      "ds_read_b128 v[206:209], v10 offset:1200\n"
      "v_mul_f32 v213, v77, v213\n"
      "v_mfma_f32_16x16x32_bf16 v[158:161], a[228:231], v[166:169], v[158:161]\n"
      "v_cvt_pk_bf16_f32 v108, v108, v109\n"
      "v_mul_f32 v110, v77, v110\n"
      "v_mfma_f32_16x16x32_bf16 v[158:161], a[232:235], v[146:149], v[158:161]\n"
      "v_mul_f32 v111, v77, v111\n"
      "v_cvt_pk_bf16_f32 v192, v176, v177\n"
      "v_mfma_f32_16x16x32_bf16 v[158:161], a[236:239], v[150:153], v[158:161]\n"
      "v_cvt_pk_bf16_f32 v193, v178, v179\n"
      "v_cvt_pk_bf16_f32 v163, v164, v165\n"
      "v_mfma_f32_16x16x32_bf16 v[96:99], a[252:255], v[166:169], v[96:99]\n"
      "ds_read_b64 v[176:177], v10 offset:1192\n"
      "v_cvt_pk_bf16_f32 v136, v210, v211\n"
      "v_mfma_f32_16x16x32_bf16 v[96:99], v[224:227], v[146:149], v[96:99]\n"
      "ds_read_b128 v[220:223], v2 offset:256\n"
      "v_cvt_pk_bf16_f32 v137, v212, v213\n"
      "v_mfma_f32_16x16x32_bf16 v[96:99], v[228:231], v[150:153], v[96:99]\n"
      "ds_read_b128 v[202:205], v2 offset:3328\n"
      "v_cvt_pk_bf16_f32 v109, v110, v111\n"
      "v_mfma_f32_16x16x32_bf16 v[114:117], v[244:247], v[166:169], v[114:117]\n"
      "ds_read_b128 v[210:213], v2 offset:6400\n"
      "v_cvt_pk_bf16_f32 v77, v182, v183\n"
      "v_mfma_f32_16x16x32_bf16 v[114:117], v[248:251], v[146:149], v[114:117]\n"
      "s_waitcnt lgkmcnt(4)\n"
      "v_cvt_pk_bf16_f32 v130, v130, v131\n"
      "v_cvt_pk_bf16_f32 v131, v132, v133\n"
      "v_mfma_f32_16x16x32_bf16 v[114:117], v[252:255], v[150:153], v[114:117]\n"
      "v_cvt_pk_bf16_f32 v132, v186, v187\n"
      "v_cvt_pk_bf16_f32 v133, v188, v189\n"
      "v_mfma_f32_16x16x32_bf16 v[186:189], v[170:173], v[100:103], v[40:43]\n"
      "s_waitcnt lgkmcnt(3)\n"
      "v_cvt_pk_bf16_f32 v80, v104, v105\n"
      "v_cvt_pk_bf16_f32 v81, v106, v107\n"
      "v_mfma_f32_16x16x32_bf16 v[104:107], v[138:141], v[100:103], v[44:47]\n"
      "s_waitcnt lgkmcnt(2)\n"
      "v_cvt_pk_bf16_f32 v214, v214, v215\n"
      "v_cvt_pk_bf16_f32 v215, v216, v217\n"
      "v_mfma_f32_16x16x32_bf16 v[146:149], v[190:193], v[100:103], v[48:51]\n"
      "s_waitcnt lgkmcnt(1)\n"
      "v_cvt_pk_bf16_f32 v216, v142, v143\n"
      "v_cvt_pk_bf16_f32 v217, v144, v145\n"
      "v_mfma_f32_16x16x32_bf16 v[150:153], v[206:209], v[100:103], v[24:27]\n"
      "s_waitcnt lgkmcnt(0)\n"
      "v_cvt_pk_bf16_f32 v84, v126, v127\n"
      "v_cvt_pk_bf16_f32 v85, v128, v129\n"
      "v_mfma_f32_16x16x16_bf16 v[186:189], v[174:175], v[184:185], v[186:189]\n"
      "v_fma_f32 v165, v158, v158, s52\n"
      "v_fmac_f32 v165, v159, v159\n"
      "v_fmac_f32 v165, v160, v160\n"
      "v_mfma_f32_16x16x16_bf16 v[104:107], v[112:113], v[184:185], v[104:107]\n"
      "v_fmac_f32 v165, v161, v161\n"
      "v_fmac_f32 v165, v96, v96\n"
      "v_fmac_f32 v165, v97, v97\n"
      "v_mfma_f32_16x16x16_bf16 v[146:149], v[162:163], v[184:185], v[146:149]\n"
      "v_exp_f32 v186, v186\n"
      "v_exp_f32 v187, v187\n"
      "v_mfma_f32_16x16x16_bf16 v[150:153], v[176:177], v[184:185], v[150:153]\n"
      "v_exp_f32 v188, v188\n"
      "v_exp_f32 v189, v189\n"
      "v_mfma_f32_16x16x32_bf16 v[178:181], a[216:219], v[220:223], 0\n"
      "v_exp_f32 v104, v104\n"
      "v_mfma_f32_16x16x32_bf16 v[178:181], a[220:223], v[202:205], v[178:181]\n"
      "v_exp_f32 v105, v105\n"
      "v_mfma_f32_16x16x32_bf16 v[178:181], a[224:227], v[210:213], v[178:181]\n"
      "v_exp_f32 v106, v106\n"
      "v_mfma_f32_16x16x32_bf16 v[166:169], a[240:243], v[220:223], 0\n"
      "v_exp_f32 v107, v107\n"
      "v_mfma_f32_16x16x32_bf16 v[166:169], a[244:247], v[202:205], v[166:169]\n"
      "v_cvt_pk_bf16_f32 v186, v186, v187\n"
      "v_cvt_pk_bf16_f32 v187, v188, v189\n"
      "v_mfma_f32_16x16x32_bf16 v[166:169], a[248:251], v[210:213], v[166:169]\n"
      "v_cvt_pk_bf16_f32 v188, v104, v105\n"
      "v_cvt_pk_bf16_f32 v189, v106, v107\n"
      "v_mfma_f32_16x16x32_bf16 v[126:129], v[232:235], v[220:223], 0\n"
      "v_exp_f32_e64 v146, v146\n"
      "v_mfma_f32_16x16x32_bf16 v[126:129], v[236:239], v[202:205], v[126:129]\n"
      "ds_read_u16 v78, v11 offset:1168\n"
      "v_exp_f32 v147, v147\n"
      "v_mfma_f32_16x16x32_bf16 v[126:129], v[240:243], v[210:213], v[126:129]\n"
      "v_exp_f32 v148, v148\n"
      "v_mfma_f32_16x16x32_bf16 v[142:145], v[170:173], v[118:121], v[52:55]\n"
      "ds_read_u16 v82, v11 offset:1200\n"
      "v_exp_f32 v149, v149\n"
      "v_mfma_f32_16x16x32_bf16 v[202:205], v[138:141], v[118:121], v[56:59]\n"
      "v_exp_f32 v150, v150\n"
      "v_mfma_f32_16x16x32_bf16 v[104:107], v[190:193], v[118:121], v[60:63]\n"
      "ds_read_u16 v86, v11 offset:1232\n"
      "v_cvt_pk_bf16_f32 v88, v146, v147\n"
      "v_mfma_f32_16x16x32_bf16 v[220:223], v[206:209], v[118:121], v[26:29]\n"
      "v_cvt_pk_bf16_f32 v89, v148, v149\n"
      "v_cvt_pk_bf16_f32 v90, v150, 0\n"
      "v_mfma_f32_16x16x16_bf16 v[142:145], v[174:175], v[218:219], v[142:145]\n"
      "v_fmac_f32 v165, v98, v98\n"
      "v_fmac_f32 v165, v99, v99\n"
      "v_fmac_f32_e64 v165, v114, v114\n"
      "v_mfma_f32_16x16x16_bf16 v[202:205], v[112:113], v[218:219], v[202:205]\n"
      "v_fmac_f32 v165, v115, v115\n"
      "v_fmac_f32 v165, v116, v116\n"
      "v_fmac_f32 v165, v117, v117\n"
      "v_mfma_f32_16x16x16_bf16 v[104:107], v[162:163], v[218:219], v[104:107]\n"
      "v_exp_f32 v142, v142\n"
      "v_exp_f32 v143, v143\n"
      "v_mfma_f32_16x16x16_bf16 v[220:223], v[176:177], v[218:219], v[220:223]\n"
      "v_exp_f32 v144, v144\n"
      "v_exp_f32 v145, v145\n"
      "v_mfma_f32_16x16x32_bf16 v[154:157], v[32:35], v[186:189], 0\n"
      "s_waitcnt lgkmcnt(0)\n"
      "v_exp_f32 v202, v202\n"
      "v_mfma_f32_16x16x32_bf16 v[154:157], v[32:35], v[88:91], v[154:157]\n"
      "v_exp_f32 v203, v203\n"
      "v_mfma_f32_16x16x32_bf16 v[150:153], v[194:197], v[186:189], 0\n"
      "v_exp_f32_e64 v204, v204\n"
      "v_mfma_f32_16x16x32_bf16 v[150:153], v[76:79], v[88:91], v[150:153]\n"
      "ds_read_b128 v[118:121], v2 offset:9472\n"
      "v_exp_f32_e64 v205, v205\n"
      "v_mfma_f32_16x16x32_bf16 v[210:213], v[130:133], v[186:189], 0\n"
      "ds_read_b128 v[100:103], v2 offset:12544\n"
      "v_cvt_pk_bf16_f32 v142, v142, v143\n"
      "v_mfma_f32_16x16x32_bf16 v[210:213], v[80:83], v[88:91], v[210:213]\n"
      "v_cvt_pk_bf16_f32 v143, v144, v145\n"
      "v_cvt_pk_bf16_f32 v144, v202, v203\n"
      "v_mfma_f32_16x16x32_bf16 v[146:149], v[214:217], v[186:189], 0\n"
      "v_cvt_pk_bf16_f32 v145, v204, v205\n"
      "v_exp_f32_e64 v104, v104\n"
      "v_mfma_f32_16x16x32_bf16 v[146:149], v[84:87], v[88:91], v[146:149]\n"
      "ds_read_b128 v[202:205], v2 offset:15616\n"
      "v_exp_f32 v105, v105\n"
      "v_mfma_f32_16x16x32_bf16 v[184:187], v[170:173], v[134:137], v[64:67]\n"
      "v_exp_f32 v106, v106\n"
      "v_exp_f32 v107, v107\n"
      "v_exp_f32 v220, v220\n"
      "v_cvt_pk_bf16_f32 v92, v104, v105\n"
      "v_cvt_pk_bf16_f32 v93, v106, v107\n"
      "v_cvt_pk_bf16_f32 v94, v220, 0\n"
      "v_rcp_f32 v154, v154\n"
      "s_waitcnt vmcnt(6)\n"
      "v_or3_b32 v122, v122, v123, v124\n"
      "v_mfma_f32_16x16x32_bf16 v[104:107], v[138:141], v[134:137], v[68:71]\n"
      "s_waitcnt lgkmcnt(1)\n"
      "v_mul_f32 v150, v154, v150\n"
      "v_mul_f32 v151, v154, v151\n"
      "v_cvt_pk_bf16_f32 v150, v150, v151\n"
      "v_mul_f32 v152, v154, v152\n"
      "v_mul_f32 v153, v154, v153\n"
      "v_cvt_pk_bf16_f32 v151, v152, v153\n"
      "v_mul_f32 v210, v154, v210\n"
      "v_mul_f32 v211, v154, v211\n"
      "v_mul_f32 v212, v154, v212\n"
      "v_mul_f32_e64 v213, v154, v213\n"
      "v_cvt_pk_bf16_f32 v152, v210, v211\n"
      "v_cvt_pk_bf16_f32 v153, v212, v213\n"
      "v_mfma_f32_16x16x32_bf16 v[218:221], v[190:193], v[134:137], v[72:75]\n"
      "ds_write_b128 v3, v[150:153] offset:18432\n"
      "v_mfma_f32_16x16x32_bf16 v[210:213], v[206:209], v[134:137], v[28:31]\n"
      "s_waitcnt lgkmcnt(1)\n"
      "v_mul_f32 v146, v154, v146\n"
      "v_mul_f32_e64 v147, v154, v147\n"
      "v_mfma_f32_16x16x16_bf16 v[184:187], v[174:175], v[108:109], v[184:187]\n"
      "v_cvt_pk_bf16_f32 v146, v146, v147\n"
      "v_mul_f32_e64 v148, v154, v148\n"
      "v_mul_f32 v149, v154, v149\n"
      "v_mfma_f32_16x16x16_bf16 v[104:107], v[112:113], v[108:109], v[104:107]\n"
      "v_cvt_pk_bf16_f32 v147, v148, v149\n"
      "s_waitcnt vmcnt(5)\n"
      "v_or3_b32 v122, v122, v125, v8\n"
      "v_and_b32 v122, 0x7fff7fff, v122\n"
      "v_mfma_f32_16x16x16_bf16 v[218:221], v[162:163], v[108:109], v[218:221]\n"
      "v_exp_f32 v184, v184\n"
      "v_exp_f32 v185, v185\n"
      "v_mfma_f32_16x16x16_bf16 v[210:213], v[176:177], v[108:109], v[210:213]\n"
      "v_exp_f32 v186, v186\n"
      "v_exp_f32 v187, v187\n"
      "v_mfma_f32_16x16x32_bf16 v[178:181], a[228:231], v[118:121], v[178:181]\n"
      "ds_write_b64 v4, v[146:147] offset:18432\n"
      "v_mfma_f32_16x16x32_bf16 v[178:181], a[232:235], v[100:103], v[178:181]\n"
      "v_exp_f32 v104, v104\n"
      "v_mfma_f32_16x16x32_bf16 v[178:181], a[236:239], v[202:205], v[178:181]\n"
      "v_exp_f32 v105, v105\n"
      "v_mfma_f32_16x16x32_bf16 v[166:169], a[252:255], v[118:121], v[166:169]\n"
      "ds_write_b32 v31, v122 offset:5632\n"
      "v_mfma_f32_16x16x32_bf16 v[166:169], v[224:227], v[100:103], v[166:169]\n"
      "ds_read_b128 v[108:111], v10 offset:1168\n"
      "v_exp_f32 v106, v106\n"
      "v_mfma_f32_16x16x32_bf16 v[166:169], v[228:231], v[202:205], v[166:169]\n"
      "v_exp_f32 v107, v107\n"
      "v_mfma_f32_16x16x32_bf16 v[126:129], v[244:247], v[118:121], v[126:129]\n"
      "v_cvt_pk_bf16_f32 v184, v184, v185\n"
      "v_cvt_pk_bf16_f32 v185, v186, v187\n"
      "v_mfma_f32_16x16x32_bf16 v[126:129], v[248:251], v[100:103], v[126:129]\n"
      "v_cvt_pk_bf16_f32 v186, v104, v105\n"
      "v_cvt_pk_bf16_f32 v187, v106, v107\n"
      "v_mfma_f32_16x16x32_bf16 v[126:129], v[252:255], v[202:205], v[126:129]\n"
      "ds_read_b64 v[104:105], v10 offset:1184\n"
      "v_exp_f32 v218, v218\n"
      "v_mfma_f32_16x16x32_bf16 v[202:205], v[32:35], v[142:145], 0\n"
      "v_exp_f32 v219, v219\n"
      "v_mfma_f32_16x16x32_bf16 v[202:205], v[32:35], v[92:95], v[202:205]\n"
      "v_exp_f32_e64 v220, v220\n"
      "v_mfma_f32_16x16x32_bf16 v[134:137], v[194:197], v[142:145], 0\n"
      "s_waitcnt lgkmcnt(1)\n"
      "v_exp_f32 v221, v221\n"
      "v_mfma_f32_16x16x32_bf16 v[134:137], v[76:79], v[92:95], v[134:137]\n"
      "ds_read_b128 v[118:121], v2 offset:512\n"
      "v_exp_f32_e64 v210, v210\n"
      "v_mfma_f32_16x16x32_bf16 v[100:103], v[130:133], v[142:145], 0\n"
      "ds_read_b128 v[150:153], v2 offset:3584\n"
      "v_cvt_pk_bf16_f32 v88, v218, v219\n"
      "v_mfma_f32_16x16x32_bf16 v[100:103], v[80:83], v[92:95], v[100:103]\n"
      "ds_read_b128 v[154:157], v2 offset:6656\n"
      "v_cvt_pk_bf16_f32 v89, v220, v221\n"
      "v_mfma_f32_16x16x32_bf16 v[218:221], v[214:217], v[142:145], 0\n"
      "s_waitcnt lgkmcnt(3)\n"
      "v_cvt_pk_bf16_f32 v90, v210, 0\n"
      "v_fma_f32 v148, v178, v178, s52\n"
      "v_mfma_f32_16x16x32_bf16 v[218:221], v[84:87], v[92:95], v[218:221]\n"
      "v_fmac_f32 v148, v179, v179\n"
      "v_fmac_f32 v148, v180, v180\n"
      "v_mfma_f32_16x16x32_bf16 v[210:213], v[108:111], v[170:173], v[30:33]\n"
      "v_fmac_f32 v148, v181, v181\n"
      "v_fmac_f32 v148, v166, v166\n"
      "v_mfma_f32_16x16x32_bf16 v[144:147], v[108:111], v[138:141], v[30:33]\n"
      "s_waitcnt lgkmcnt(2)\n"
      "v_fmac_f32 v148, v167, v167\n"
      "v_fmac_f32 v148, v168, v168\n"
      "v_mfma_f32_16x16x32_bf16 v[140:143], v[108:111], v[190:193], v[30:33]\n"
      "s_waitcnt lgkmcnt(1)\n"
      "v_fmac_f32 v148, v169, v169\n"
      "v_fmac_f32_e64 v148, v126, v126\n"
      "v_mfma_f32_16x16x32_bf16 v[188:191], v[108:111], v[206:209], v[30:33]\n"
      "s_waitcnt lgkmcnt(0)\n"
      "v_fmac_f32 v148, v127, v127\n"
      "v_fmac_f32 v148, v128, v128\n"
      "v_mfma_f32_16x16x16_bf16 v[210:213], v[104:105], v[174:175], v[210:213]\n"
      "v_fmac_f32 v148, v129, v129\n"
      "v_rcp_f32 v202, v202\n"
      "v_mfma_f32_16x16x16_bf16 v[144:147], v[104:105], v[112:113], v[144:147]\n"
      "v_permlane16_swap_b32 v165, v148\n"
      "v_add_f32 v165, v165, v148\n"
      "v_mfma_f32_16x16x16_bf16 v[140:143], v[104:105], v[162:163], v[140:143]\n"
      "v_mul_f32 v134, v202, v134\n"
      "v_mul_f32 v135, v202, v135\n"
      "v_cvt_pk_bf16_f32 v134, v134, v135\n"
      "v_mfma_f32_16x16x16_bf16 v[188:191], v[104:105], v[176:177], v[188:191]\n"
      "v_mul_f32 v136, v202, v136\n"
      "v_cndmask_b32 v210, v210, v144, s[58:59]\n"
      "v_mul_f32_e64 v137, v202, v137\n"
      "v_mfma_f32_16x16x32_bf16 v[144:147], a[216:219], v[118:121], 0\n"
      "v_cndmask_b32 v210, v210, v140, s[60:61]\n"
      "v_cvt_pk_bf16_f32 v135, v136, v137\n"
      "v_mfma_f32_16x16x32_bf16 v[144:147], a[220:223], v[150:153], v[144:147]\n"
      "v_mul_f32 v100, v202, v100\n"
      "v_mul_f32 v101, v202, v101\n"
      "v_mfma_f32_16x16x32_bf16 v[144:147], a[224:227], v[154:157], v[144:147]\n"
      "v_mul_f32 v102, v202, v102\n"
      "v_mul_f32 v103, v202, v103\n"
      "v_mfma_f32_16x16x32_bf16 v[108:111], a[240:243], v[118:121], 0\n"
      "v_cndmask_b32 v210, v210, v188, s[62:63]\n"
      "v_cvt_pk_bf16_f32 v136, v100, v101\n"
      "v_mfma_f32_16x16x32_bf16 v[108:111], a[244:247], v[150:153], v[108:111]\n"
      "ds_read_b128 v[188:191], v2 offset:9728\n"
      "v_cvt_pk_bf16_f32 v137, v102, v103\n"
      "v_mfma_f32_16x16x32_bf16 v[108:111], a[248:251], v[154:157], v[108:111]\n"
      "ds_read_b128 v[104:107], v2 offset:12800\n"
      "v_mul_f32 v218, v202, v218\n"
      "v_mfma_f32_16x16x32_bf16 v[138:141], v[232:235], v[118:121], 0\n"
      "ds_read_b128 v[100:103], v2 offset:15872\n"
      "v_mul_f32 v219, v202, v219\n"
      "v_mfma_f32_16x16x32_bf16 v[138:141], v[236:239], v[150:153], v[138:141]\n"
      "ds_write_b128 v3, v[134:137] offset:18688\n"
      "v_mfma_f32_16x16x32_bf16 v[138:141], v[240:243], v[154:157], v[138:141]\n"
      "v_exp_f32 v210, v210\n"
      "v_mfma_f32_16x16x32_bf16 v[172:175], v[32:35], v[184:187], 0\n"
      "s_waitcnt lgkmcnt(3)\n"
      "v_cvt_pk_bf16_f32 v218, v218, v219\n"
      "v_mul_f32 v220, v202, v220\n"
      "v_mfma_f32_16x16x32_bf16 v[172:175], v[32:35], v[88:91], v[172:175]\n"
      "s_waitcnt lgkmcnt(2)\n"
      "v_mul_f32_e64 v221, v202, v221\n"
      "v_cvt_pk_bf16_f32 v219, v220, v221\n"
      "v_mfma_f32_16x16x32_bf16 v[202:205], v[194:197], v[184:187], 0\n"
      "ds_write_b64 v4, v[218:219] offset:18688\n"
      "v_mfma_f32_16x16x32_bf16 v[202:205], v[76:79], v[88:91], v[202:205]\n"
      "s_waitcnt lgkmcnt(2)\n"
      "v_cvt_pk_bf16_f32 v210, v210, v210\n"
      "v_rcp_f32 v172, v172\n"
      "v_mfma_f32_16x16x32_bf16 v[150:153], v[130:133], v[184:187], 0\n"
      "ds_write_b16 v13, v210\n"
      "v_mfma_f32_16x16x32_bf16 v[150:153], v[80:83], v[88:91], v[150:153]\n"
      "ds_read_b128 v[210:213], v14\n"
      "v_readlane_b32 s45, v37, s43\n"
      "v_mfma_f32_16x16x32_bf16 v[154:157], v[214:217], v[184:187], 0\n"
      "ds_read_b128 v[118:121], v14 offset:64\n"
      "v_mul_f32 v202, v172, v202\n"
      "v_mfma_f32_16x16x32_bf16 v[154:157], v[84:87], v[88:91], v[154:157]\n"
      "v_mul_f32 v203, v172, v203\n"
      "v_cvt_pk_bf16_f32 v202, v202, v203\n"
      "v_mfma_f32_16x16x32_bf16 v[144:147], a[228:231], v[188:191], v[144:147]\n"
      "v_mul_f32 v204, v172, v204\n"
      "v_mul_f32 v205, v172, v205\n"
      "v_mfma_f32_16x16x32_bf16 v[144:147], a[232:235], v[104:107], v[144:147]\n"
      "v_cvt_pk_bf16_f32 v203, v204, v205\n"
      "v_mul_f32 v150, v172, v150\n"
      "v_mfma_f32_16x16x32_bf16 v[144:147], a[236:239], v[100:103], v[144:147]\n"
      "v_mul_f32 v151, v172, v151\n"
      "v_mul_f32 v152, v172, v152\n"
      "v_mfma_f32_16x16x32_bf16 v[108:111], a[252:255], v[188:191], v[108:111]\n"
      "v_mul_f32 v153, v172, v153\n"
      "v_cvt_pk_bf16_f32 v204, v150, v151\n"
      "v_mfma_f32_16x16x32_bf16 v[108:111], v[224:227], v[104:107], v[108:111]\n"
      "s_waitcnt lgkmcnt(1)\n"
      "v_cvt_pk_bf16_f32 v205, v152, v153\n"
      "v_fma_f32 v150, v144, v144, s52\n"
      "v_mfma_f32_16x16x32_bf16 v[108:111], v[228:231], v[100:103], v[108:111]\n"
      "ds_write_b128 v3, v[202:205] offset:18944\n"
      "v_mfma_f32_16x16x32_bf16 v[138:141], v[244:247], v[188:191], v[138:141]\n"
      "s_waitcnt lgkmcnt(1)\n"
      "v_fmac_f32 v150, v145, v145\n"
      "v_fmac_f32 v150, v146, v146\n"
      "v_mfma_f32_16x16x32_bf16 v[138:141], v[248:251], v[104:107], v[138:141]\n"
      "v_fmac_f32 v150, v147, v147\n"
      "v_mul_f32 v154, v172, v154\n"
      "v_mfma_f32_16x16x32_bf16 v[138:141], v[252:255], v[100:103], v[138:141]\n"
      "v_fmac_f32 v150, v108, v108\n"
      "v_fmac_f32 v150, v109, v109\n"
      "v_mfma_f32_16x16x32_bf16 v[188:191], v[210:213], v[32:35], 0\n"
      "v_fmac_f32 v150, v110, v110\n"
      "v_fmac_f32 v150, v111, v111\n"
      "v_mfma_f32_16x16x32_bf16 v[188:191], v[118:121], v[32:35], v[188:191]\n"
      "v_mul_f32 v155, v172, v155\n"
      "v_cvt_pk_bf16_f32 v154, v154, v155\n"
      "v_fmac_f32 v150, v138, v138\n"
      "v_fmac_f32 v150, v139, v139\n"
      "v_fmac_f32 v150, v140, v140\n"
      "v_fmac_f32 v150, v141, v141\n"
      "v_mov_b32 v148, v150\n"
      "v_mul_f32 v156, v172, v156\n"
      "v_mul_f32 v157, v172, v157\n"
      "v_permlane16_swap_b32 v150, v148\n"
      "v_add_f32 v150, v150, v148\n"
      "v_cvt_pk_bf16_f32 v155, v156, v157\n"
      "ds_write_b64 v4, v[154:155] offset:18944\n"
      "v_permlane32_swap_b32 v165, v150\n"
      "v_add_f32 v165, v165, v150\n"
      "v_readlane_b32 s46, v37, s42\n"
      "v_or3_b32 v198, v198, v199, v200\n"
      "ds_write_b32 v31, v165 offset:1024\n"
      "v_or_b32_e64 v198, v198, v201\n"
      "v_cmp_ne_u32 s[56:57], 0, v198\n"
      "v_readlane_b32 s47, v38, s43\n"
      "v_add_u32 v10, 2336, v10\n"
      "v_add_u32 v11, 2336, v11\n"
      "s_or_b32 s95, s45, s46\n"
      "s_or_b32 s95, s95, s48\n"
      "s_cmp_ge_u32 s95, 0x40000000\n"
      "s_cselect_b32 s94, -1, -2\n"
      "s_cmp_gt_u32 s42, s44\n"
      "s_cselect_b32 s95, s94, s95\n"
      "s_cmp_ge_u32 s95, 0x40000000\n"
      "s_cselect_b64 vcc, -1, s[56:57]\n"
      "s_and_b32 s47, s47, -4\n"
      "s_add_u32 s42, s42, 1\n"
      "s_add_u32 s43, s43, 1\n"
      "s_waitcnt lgkmcnt(0)\n"
      "s_barrier\n"
      "s_cbranch_vccnz L_disp0_%=\n"
      "L_loop0_%=:\n"
      "v_mfma_f32_16x16x32_bf16 v[202:205], v[210:213], v[194:197], 0\n"
      "ds_read_b128 v[148:151], v0\n"
      "ds_read_b128 v[184:187], v1\n"
      "v_mfma_f32_16x16x32_bf16 v[202:205], v[118:121], v[76:79], v[202:205]\n"
      "ds_read_b128 v[134:137], v0 offset:6144\n"
      "s_add_u32 m0, s64, 18432\n"
      "v_mfma_f32_16x16x32_bf16 v[104:107], v[210:213], v[130:133], 0\n"
      "buffer_load_dwordx4 v16, s[16:19], s45 offen  lds\n"
      "v_mfma_f32_16x16x32_bf16 v[104:107], v[118:121], v[80:83], v[104:107]\n"
      "s_add_u32 m0, s64, 22528\n"
      "v_mfma_f32_16x16x32_bf16 v[174:177], v[210:213], v[214:217], 0\n"
      "ds_read_b128 v[216:219], v9 offset:1024\n"
      "v_mfma_f32_16x16x32_bf16 v[174:177], v[118:121], v[84:87], v[174:177]\n"
      "v_mov_b32_dpp v188, v188 quad_perm:[0,0,0,0] row_mask:0xf bank_mask:0xf\n"
      "v_rcp_f32 v188, v188\n"
      "s_waitcnt lgkmcnt(3)\n"
      "v_mfma_f32_16x16x32_bf16 v[100:103], a[0:3], v[148:151], 0\n"
      "buffer_load_dwordx4 v17, s[16:19], s45 offen  lds\n"
      "v_mfma_f32_16x16x32_bf16 v[170:173], a[24:27], v[148:151], 0\n"
      "s_add_u32 m0, s64, 26624\n"
      "v_cndmask_b32 v202, v202, v104, s[58:59]\n"
      "v_cndmask_b32 v202, v202, v174, s[60:61]\n"
      "v_mfma_f32_16x16x32_bf16 v[152:155], a[48:51], v[148:151], 0\n"
      "s_waitcnt lgkmcnt(1)\n"
      "v_mul_f32 v202, v188, v202\n"
      "v_cvt_pk_bf16_f32 v202, v202, v202\n"
      "v_mfma_f32_16x16x32_bf16 v[118:121], a[72:75], v[148:151], 0\n"
      "ds_write_b16 v12, v202 offset:16\n"
      "v_mfma_f32_16x16x32_bf16 v[202:205], a[96:99], v[148:151], 0\n"
      "buffer_load_dwordx4 v18, s[16:19], s45 offen  lds\n"
      "v_mfma_f32_16x16x32_bf16 v[104:107], a[120:123], v[148:151], 0\n"
      "ds_read_b128 v[206:209], v1 offset:6144\n"
      "s_waitcnt lgkmcnt(2)\n"
      "v_add_f32 v216, v216, v217\n"
      "v_mfma_f32_16x16x32_bf16 v[100:103], a[4:7], v[184:187], v[100:103]\n"
      "ds_read_b128 v[190:193], v0 offset:12288\n"
      "v_add_f32 v218, v218, v219\n"
      "v_mfma_f32_16x16x32_bf16 v[100:103], a[8:11], v[134:137], v[100:103]\n"
      "ds_read_b128 v[210:213], v1 offset:12288\n"
      "v_add_f32 v216, v216, v218\n"
      "v_mfma_f32_16x16x32_bf16 v[170:173], a[28:31], v[184:187], v[170:173]\n"
      "s_add_u32 m0, s64, 30720\n"
      "v_rsq_f32_e64 v216, v216\n"
      "v_mfma_f32_16x16x32_bf16 v[170:173], a[32:35], v[134:137], v[170:173]\n"
      "buffer_load_dwordx4 v19, s[16:19], s45 offen  lds\n"
      "v_mfma_f32_16x16x32_bf16 v[152:155], a[52:55], v[184:187], v[152:155]\n"
      "s_add_u32 m0, s65, 34816\n"
      "v_mul_f32 v216, 0x415db3d7, v216\n"
      "v_mov_b32 v218, v216\n"
      "v_mfma_f32_16x16x32_bf16 v[152:155], a[56:59], v[134:137], v[152:155]\n"
      "s_waitcnt lgkmcnt(2)\n"
      "v_permlane32_swap_b32 v216, v218\n"
      "v_mfma_f32_16x16x32_bf16 v[118:121], a[76:79], v[184:187], v[118:121]\n"
      "s_waitcnt lgkmcnt(1)\n"
      "v_mov_b32 v217, v216\n"
      "v_mul_f32 v144, v218, v144\n"
      "v_mfma_f32_16x16x32_bf16 v[118:121], a[80:83], v[134:137], v[118:121]\n"
      "buffer_load_dwordx4 v15, s[16:19], s45 offen  lds\n"
      "v_mfma_f32_16x16x32_bf16 v[202:205], a[100:103], v[184:187], v[202:205]\n"
      "s_waitcnt lgkmcnt(0)\n"
      "v_permlane16_swap_b32 v216, v217\n"
      "v_mfma_f32_16x16x32_bf16 v[202:205], a[104:107], v[134:137], v[202:205]\n"
      "v_mul_f32 v158, v216, v158\n"
      "v_mul_f32 v159, v216, v159\n"
      "v_mfma_f32_16x16x32_bf16 v[104:107], a[124:127], v[184:187], v[104:107]\n"
      "v_cvt_pk_bf16_f32 v158, v158, v159\n"
      "v_mul_f32_e64 v160, v216, v160\n"
      "v_mfma_f32_16x16x32_bf16 v[104:107], a[128:131], v[134:137], v[104:107]\n"
      "buffer_load_dwordx4 v[174:177], v36, s[28:31], s47 offen\n"
      "v_mfma_f32_16x16x32_bf16 v[100:103], a[12:15], v[206:209], v[100:103]\n"
      "v_mul_f32 v161, v216, v161\n"
      "v_mul_f32 v114, v216, v114\n"
      "v_mfma_f32_16x16x32_bf16 v[100:103], a[16:19], v[190:193], v[100:103]\n"
      "v_mul_f32_e64 v115, v216, v115\n"
      "v_cvt_pk_bf16_f32 v159, v160, v161\n"
      "v_mfma_f32_16x16x32_bf16 v[100:103], a[20:23], v[210:213], v[100:103]\n"
      "v_mul_f32 v96, v216, v96\n"
      "v_mul_f32 v97, v216, v97\n"
      "v_mfma_f32_16x16x32_bf16 v[170:173], a[36:39], v[206:209], v[170:173]\n"
      "buffer_load_dword v8, v29, s[28:31], s47 offen\n"
      "v_mfma_f32_16x16x32_bf16 v[170:173], a[40:43], v[190:193], v[170:173]\n"
      "v_mul_f32 v98, v216, v98\n"
      "v_mul_f32 v99, v216, v99\n"
      "v_mfma_f32_16x16x32_bf16 v[170:173], a[44:47], v[210:213], v[170:173]\n"
      "v_cvt_pk_bf16_f32 v114, v114, v115\n"
      "v_mul_f32 v116, v216, v116\n"
      "v_mfma_f32_16x16x32_bf16 v[152:155], a[60:63], v[206:209], v[152:155]\n"
      "v_mul_f32 v117, v216, v117\n"
      "v_cvt_pk_bf16_f32 v160, v96, v97\n"
      "v_mfma_f32_16x16x32_bf16 v[152:155], a[64:67], v[190:193], v[152:155]\n"
      "v_cvt_pk_bf16_f32 v161, v98, v99\n"
      "v_cvt_pk_bf16_f32 v115, v116, v117\n"
      "v_mfma_f32_16x16x32_bf16 v[152:155], a[68:71], v[210:213], v[152:155]\n"
      "ds_write_b128 v5, v[158:161]\n"
      "v_mfma_f32_16x16x32_bf16 v[118:121], a[84:87], v[206:209], v[118:121]\n"
      "v_mul_f32 v178, v217, v178\n"
      "v_mul_f32 v179, v217, v179\n"
      "v_mfma_f32_16x16x32_bf16 v[118:121], a[88:91], v[190:193], v[118:121]\n"
      "v_cvt_pk_bf16_f32 v178, v178, v179\n"
      "v_mul_f32_e64 v180, v217, v180\n"
      "v_mfma_f32_16x16x32_bf16 v[118:121], a[92:95], v[210:213], v[118:121]\n"
      "ds_write_b64 v6, v[114:115]\n"
      "v_mfma_f32_16x16x32_bf16 v[202:205], a[108:111], v[206:209], v[202:205]\n"
      "v_mul_f32 v181, v217, v181\n"
      "v_mul_f32 v126, v217, v126\n"
      "v_mfma_f32_16x16x32_bf16 v[202:205], a[112:115], v[190:193], v[202:205]\n"
      "v_mul_f32_e64 v127, v217, v127\n"
      "v_cvt_pk_bf16_f32 v179, v180, v181\n"
      "v_mfma_f32_16x16x32_bf16 v[202:205], a[116:119], v[210:213], v[202:205]\n"
      "v_mul_f32 v166, v217, v166\n"
      "v_mul_f32 v167, v217, v167\n"
      "v_mfma_f32_16x16x32_bf16 v[104:107], a[132:135], v[206:209], v[104:107]\n"
      "v_mul_f32 v168, v217, v168\n"
      "v_mul_f32 v169, v217, v169\n"
      "v_mfma_f32_16x16x32_bf16 v[104:107], a[136:139], v[190:193], v[104:107]\n"
      "v_cvt_pk_bf16_f32 v126, v126, v127\n"
      "v_mul_f32_e64 v128, v217, v128\n"
      "v_mfma_f32_16x16x32_bf16 v[104:107], a[140:143], v[210:213], v[104:107]\n"
      "v_mul_f32_e64 v129, v217, v129\n"
      "v_cvt_pk_bf16_f32 v180, v166, v167\n"
      "v_mfma_f32_16x16x32_bf16 v[194:197], v[148:151], a[144:147], 0\n"
      "v_cvt_pk_bf16_f32 v181, v168, v169\n"
      "v_cvt_pk_bf16_f32 v127, v128, v129\n"
      "v_mfma_f32_16x16x32_bf16 v[194:197], v[184:187], a[148:151], v[194:197]\n"
      "ds_write_b128 v5, v[178:181] offset:6144\n"
      "v_mfma_f32_16x16x32_bf16 v[194:197], v[134:137], a[152:155], v[194:197]\n"
      "v_mul_f32 v145, v218, v145\n"
      "v_cvt_pk_bf16_f32 v144, v144, v145\n"
      "v_mfma_f32_16x16x32_bf16 v[194:197], v[206:209], a[156:159], v[194:197]\n"
      "v_mul_f32 v146, v218, v146\n"
      "v_mul_f32_e64 v147, v218, v147\n"
      "v_mfma_f32_16x16x32_bf16 v[194:197], v[190:193], a[160:163], v[194:197]\n"
      "ds_write_b64 v6, v[126:127] offset:6144\n"
      "v_mfma_f32_16x16x32_bf16 v[194:197], v[210:213], a[164:167], v[194:197]\n"
      "ds_read_b128 v[114:117], v0 offset:2048\n"
      "v_mul_f32 v138, v218, v138\n"
      "v_mfma_f32_16x16x32_bf16 v[130:133], v[148:151], a[168:171], 0\n"
      "ds_read_b128 v[198:201], v1 offset:2048\n"
      "v_mul_f32 v139, v218, v139\n"
      "v_mfma_f32_16x16x32_bf16 v[130:133], v[184:187], a[172:175], v[130:133]\n"
      "ds_read_b128 v[166:169], v0 offset:8192\n"
      "v_cvt_pk_bf16_f32 v145, v146, v147\n"
      "v_mfma_f32_16x16x32_bf16 v[130:133], v[134:137], a[176:179], v[130:133]\n"
      "ds_read_b128 v[162:165], v1 offset:8192\n"
      "v_mul_f32 v108, v218, v108\n"
      "v_mfma_f32_16x16x32_bf16 v[130:133], v[206:209], a[180:183], v[130:133]\n"
      "ds_read_b128 v[220:223], v0 offset:14336\n"
      "v_mul_f32 v109, v218, v109\n"
      "v_mfma_f32_16x16x32_bf16 v[130:133], v[190:193], a[184:187], v[130:133]\n"
      "ds_read_b128 v[126:129], v1 offset:14336\n"
      "v_mul_f32 v110, v218, v110\n"
      "v_mfma_f32_16x16x32_bf16 v[130:133], v[210:213], a[188:191], v[130:133]\n"
      "v_mul_f32 v111, v218, v111\n"
      "v_cvt_pk_bf16_f32 v138, v138, v139\n"
      "v_mfma_f32_16x16x32_bf16 v[214:217], v[148:151], a[192:195], 0\n"
      "s_waitcnt lgkmcnt(5)\n"
      "v_mul_f32 v140, v218, v140\n"
      "v_mul_f32_e64 v141, v218, v141\n"
      "v_mfma_f32_16x16x32_bf16 v[214:217], v[184:187], a[196:199], v[214:217]\n"
      "s_waitcnt lgkmcnt(4)\n"
      "v_cvt_pk_bf16_f32 v146, v108, v109\n"
      "v_cvt_pk_bf16_f32 v147, v110, v111\n"
      "v_mfma_f32_16x16x32_bf16 v[214:217], v[134:137], a[200:203], v[214:217]\n"
      "ds_write_b128 v5, v[144:147] offset:12288\n"
      "v_mfma_f32_16x16x32_bf16 v[214:217], v[206:209], a[204:207], v[214:217]\n"
      "s_waitcnt lgkmcnt(2)\n"
      "v_cvt_pk_bf16_f32 v139, v140, v141\n"
      "v_fma_f32 v207, v100, v100, s51\n"
      "v_mfma_f32_16x16x32_bf16 v[214:217], v[190:193], a[208:211], v[214:217]\n"
      "s_waitcnt lgkmcnt(1)\n"
      "v_fma_f32 v187, v118, v118, s51\n"
      "v_fmac_f32 v207, v101, v101\n"
      "v_mfma_f32_16x16x32_bf16 v[214:217], v[210:213], a[212:215], v[214:217]\n"
      "ds_write_b64 v6, v[138:139] offset:12288\n"
      "v_mfma_f32_16x16x32_bf16 v[110:113], a[0:3], v[114:117], 0\n"
      "v_fmac_f32 v187, v119, v119\n"
      "v_fmac_f32 v207, v102, v102\n"
      "v_mfma_f32_16x16x32_bf16 v[110:113], a[4:7], v[198:201], v[110:113]\n"
      "v_fmac_f32 v187, v120, v120\n"
      "v_fmac_f32 v207, v103, v103\n"
      "v_mfma_f32_16x16x32_bf16 v[110:113], a[8:11], v[166:169], v[110:113]\n"
      "v_fmac_f32 v187, v121, v121\n"
      "v_fmac_f32 v207, v170, v170\n"
      "v_mfma_f32_16x16x32_bf16 v[110:113], a[12:15], v[162:165], v[110:113]\n"
      "v_fmac_f32 v187, v202, v202\n"
      "v_fmac_f32 v207, v171, v171\n"
      "v_mfma_f32_16x16x32_bf16 v[110:113], a[16:19], v[220:223], v[110:113]\n"
      "v_fmac_f32 v187, v203, v203\n"
      "v_fmac_f32 v207, v172, v172\n"
      "v_mfma_f32_16x16x32_bf16 v[110:113], a[20:23], v[126:129], v[110:113]\n"
      "v_fmac_f32 v187, v204, v204\n"
      "v_fmac_f32 v207, v173, v173\n"
      "v_mfma_f32_16x16x32_bf16 v[122:125], a[24:27], v[114:117], 0\n"
      "v_fmac_f32 v187, v205, v205\n"
      "v_fmac_f32 v207, v152, v152\n"
      "v_mfma_f32_16x16x32_bf16 v[122:125], a[28:31], v[198:201], v[122:125]\n"
      "v_fmac_f32 v187, v104, v104\n"
      "v_fmac_f32 v207, v153, v153\n"
      "v_mfma_f32_16x16x32_bf16 v[122:125], a[32:35], v[166:169], v[122:125]\n"
      "v_fmac_f32 v187, v105, v105\n"
      "v_fmac_f32 v207, v154, v154\n"
      "v_mfma_f32_16x16x32_bf16 v[122:125], a[36:39], v[162:165], v[122:125]\n"
      "v_fmac_f32 v187, v106, v106\n"
      "v_fmac_f32 v207, v155, v155\n"
      "v_mfma_f32_16x16x32_bf16 v[122:125], a[40:43], v[220:223], v[122:125]\n"
      "v_fmac_f32_e64 v187, v107, v107\n"
      "v_fma_f32 v150, v110, v110, s51\n"
      "v_mfma_f32_16x16x32_bf16 v[122:125], a[44:47], v[126:129], v[122:125]\n"
      "v_permlane16_swap_b32 v207, v187\n"
      "v_mfma_f32_16x16x32_bf16 v[146:149], a[48:51], v[114:117], 0\n"
      "v_add_f32 v207, v207, v187\n"
      "v_mov_b32 v187, v207\n"
      "v_mfma_f32_16x16x32_bf16 v[146:149], a[52:55], v[198:201], v[146:149]\n"
      "v_fmac_f32 v150, v111, v111\n"
      "v_permlane32_swap_b32 v207, v187\n"
      "v_mfma_f32_16x16x32_bf16 v[146:149], a[56:59], v[166:169], v[146:149]\n"
      "v_add_f32 v207, v207, v187\n"
      "v_rsq_f32 v207, v207\n"
      "v_mfma_f32_16x16x32_bf16 v[146:149], a[60:63], v[162:165], v[146:149]\n"
      "v_mov_b32 v187, v207\n"
      "v_fmac_f32 v150, v112, v112\n"
      "v_mfma_f32_16x16x32_bf16 v[146:149], a[64:67], v[220:223], v[146:149]\n"
      "v_permlane16_swap_b32 v207, v187\n"
      "v_mfma_f32_16x16x32_bf16 v[146:149], a[68:71], v[126:129], v[146:149]\n"
      "v_mul_f32 v207, s50, v207\n"
      "v_mul_f32 v118, v187, v118\n"
      "v_mfma_f32_16x16x32_bf16 v[142:145], a[72:75], v[114:117], 0\n"
      "v_mul_f32 v119, v187, v119\n"
      "v_mul_f32 v100, v207, v100\n"
      "v_mfma_f32_16x16x32_bf16 v[142:145], a[76:79], v[198:201], v[142:145]\n"
      "v_mul_f32 v101, v207, v101\n"
      "v_cvt_pk_bf16_f32 v118, v118, v119\n"
      "v_mfma_f32_16x16x32_bf16 v[142:145], a[80:83], v[166:169], v[142:145]\n"
      "v_mul_f32 v120, v187, v120\n"
      "v_mul_f32 v121, v187, v121\n"
      "v_mfma_f32_16x16x32_bf16 v[142:145], a[84:87], v[162:165], v[142:145]\n"
      "v_mul_f32 v104, v187, v104\n"
      "v_mul_f32_e64 v105, v187, v105\n"
      "v_mfma_f32_16x16x32_bf16 v[142:145], a[88:91], v[220:223], v[142:145]\n"
      "v_cvt_pk_bf16_f32 v100, v100, v101\n"
      "v_mul_f32 v102, v207, v102\n"
      "v_mfma_f32_16x16x32_bf16 v[142:145], a[92:95], v[126:129], v[142:145]\n"
      "v_mul_f32 v103, v207, v103\n"
      "v_mul_f32_e64 v152, v207, v152\n"
      "v_mfma_f32_16x16x32_bf16 v[178:181], a[96:99], v[114:117], 0\n"
      "v_mul_f32 v153, v207, v153\n"
      "v_cvt_pk_bf16_f32 v119, v120, v121\n"
      "v_mfma_f32_16x16x32_bf16 v[178:181], a[100:103], v[198:201], v[178:181]\n"
      "v_mul_f32 v202, v187, v202\n"
      "v_mul_f32 v203, v187, v203\n"
      "v_mfma_f32_16x16x32_bf16 v[178:181], a[104:107], v[166:169], v[178:181]\n"
      "v_mul_f32 v204, v187, v204\n"
      "v_mul_f32_e64 v205, v187, v205\n"
      "v_mfma_f32_16x16x32_bf16 v[178:181], a[108:111], v[162:165], v[178:181]\n"
      "v_cvt_pk_bf16_f32 v104, v104, v105\n"
      "v_mul_f32 v106, v187, v106\n"
      "v_mfma_f32_16x16x32_bf16 v[178:181], a[112:115], v[220:223], v[178:181]\n"
      "v_mul_f32 v107, v187, v107\n"
      "v_cvt_pk_bf16_f32 v101, v102, v103\n"
      "v_mfma_f32_16x16x32_bf16 v[178:181], a[116:119], v[126:129], v[178:181]\n"
      "v_mul_f32 v170, v207, v170\n"
      "v_mul_f32 v171, v207, v171\n"
      "v_mfma_f32_16x16x32_bf16 v[186:189], a[120:123], v[114:117], 0\n"
      "v_mul_f32 v172, v207, v172\n"
      "v_mul_f32 v173, v207, v173\n"
      "v_mfma_f32_16x16x32_bf16 v[186:189], a[124:127], v[198:201], v[186:189]\n"
      "v_cvt_pk_bf16_f32 v152, v152, v153\n"
      "v_mul_f32 v154, v207, v154\n"
      "v_mfma_f32_16x16x32_bf16 v[186:189], a[128:131], v[166:169], v[186:189]\n"
      "v_mul_f32 v155, v207, v155\n"
      "v_cvt_pk_bf16_f32 v120, v202, v203\n"
      "v_mfma_f32_16x16x32_bf16 v[186:189], a[132:135], v[162:165], v[186:189]\n"
      "ds_read_b128 v[206:209], v7 offset:18432\n"
      "v_cvt_pk_bf16_f32 v121, v204, v205\n"
      "v_mfma_f32_16x16x32_bf16 v[186:189], a[136:139], v[220:223], v[186:189]\n"
      "ds_read_b128 v[190:193], v7 offset:22528\n"
      "v_cvt_pk_bf16_f32 v105, v106, v107\n"
      "v_mfma_f32_16x16x32_bf16 v[186:189], a[140:143], v[126:129], v[186:189]\n"
      "v_cvt_pk_bf16_f32 v102, v170, v171\n"
      "v_cvt_pk_bf16_f32 v103, v172, v173\n"
      "v_mfma_f32_16x16x32_bf16 v[182:185], v[114:117], a[144:147], 0\n"
      "ds_read_b128 v[106:109], v7 offset:26624\n"
      "v_cvt_pk_bf16_f32 v153, v154, v155\n"
      "v_mfma_f32_16x16x32_bf16 v[182:185], v[198:201], a[148:151], v[182:185]\n"
      "ds_read_b128 v[154:157], v7 offset:30720\n"
      "v_fma_f32 v151, v142, v142, s51\n"
      "v_mfma_f32_16x16x32_bf16 v[182:185], v[166:169], a[152:155], v[182:185]\n"
      "v_fmac_f32 v151, v143, v143\n"
      "v_fmac_f32 v151, v144, v144\n"
      "v_mfma_f32_16x16x32_bf16 v[182:185], v[162:165], a[156:159], v[182:185]\n"
      "ds_read_b128 v[210:213], v7 offset:34816\n"
      "v_fmac_f32 v150, v113, v113\n"
      "v_mfma_f32_16x16x32_bf16 v[182:185], v[220:223], a[160:163], v[182:185]\n"
      "v_fmac_f32 v151, v145, v145\n"
      "v_fmac_f32_e64 v150, v122, v122\n"
      "v_mfma_f32_16x16x32_bf16 v[182:185], v[126:129], a[164:167], v[182:185]\n"
      "ds_read_b128 v[134:137], v0 offset:4096\n"
      "v_fmac_f32 v151, v178, v178\n"
      "v_mfma_f32_16x16x32_bf16 v[202:205], v[114:117], a[168:171], 0\n"
      "ds_read_b128 v[138:141], v1 offset:4096\n"
      "v_fmac_f32 v150, v123, v123\n"
      "v_mfma_f32_16x16x32_bf16 v[202:205], v[198:201], a[172:175], v[202:205]\n"
      "s_waitcnt lgkmcnt(3)\n"
      "v_fmac_f32 v151, v179, v179\n"
      "v_fmac_f32_e64 v150, v124, v124\n"
      "v_mfma_f32_16x16x32_bf16 v[202:205], v[166:169], a[176:179], v[202:205]\n"
      "buffer_store_dwordx4 v[206:209], v20, s[24:27], s46 offen sc1\n"
      "v_mfma_f32_16x16x32_bf16 v[202:205], v[162:165], a[180:183], v[202:205]\n"
      "s_nop 0\n"
      "ds_read_b128 v[206:209], v0 offset:10240\n"
      "v_fmac_f32 v151, v180, v180\n"
      "v_mfma_f32_16x16x32_bf16 v[202:205], v[220:223], a[184:187], v[202:205]\n"
      "ds_read_b128 v[96:99], v1 offset:10240\n"
      "v_fmac_f32_e64 v150, v125, v125\n"
      "v_mfma_f32_16x16x32_bf16 v[202:205], v[126:129], a[188:191], v[202:205]\n"
      "ds_read_b128 v[158:161], v0 offset:16384\n"
      "v_fmac_f32 v151, v181, v181\n"
      "v_mfma_f32_16x16x32_bf16 v[170:173], v[114:117], a[192:195], 0\n"
      "s_waitcnt lgkmcnt(3)\n"
      "v_fmac_f32 v150, v146, v146\n"
      "v_fmac_f32 v151, v186, v186\n"
      "v_mfma_f32_16x16x32_bf16 v[170:173], v[198:201], a[196:199], v[170:173]\n"
      "ds_read_b128 v[114:117], v1 offset:16384\n"
      "v_fmac_f32 v150, v147, v147\n"
      "v_mfma_f32_16x16x32_bf16 v[170:173], v[166:169], a[200:203], v[170:173]\n"
      "v_fmac_f32 v151, v187, v187\n"
      "v_fmac_f32 v150, v148, v148\n"
      "v_mfma_f32_16x16x32_bf16 v[170:173], v[162:165], a[204:207], v[170:173]\n"
      "s_waitcnt lgkmcnt(3)\n"
      "v_fmac_f32 v151, v188, v188\n"
      "v_fmac_f32 v150, v149, v149\n"
      "v_mfma_f32_16x16x32_bf16 v[170:173], v[220:223], a[208:211], v[170:173]\n"
      "buffer_store_dwordx4 v[190:193], v21, s[24:27], s46 offen sc1\n"
      "v_mfma_f32_16x16x32_bf16 v[170:173], v[126:129], a[212:215], v[170:173]\n"
      "s_waitcnt lgkmcnt(1)\n"
      "v_fmac_f32 v151, v189, v189\n"
      "v_cvt_pk_bf16_f32 v194, v194, v195\n"
      "v_mfma_f32_16x16x32_bf16 v[218:221], a[0:3], v[134:137], 0\n"
      "s_waitcnt lgkmcnt(0)\n"
      "v_permlane16_swap_b32 v150, v151\n"
      "v_mfma_f32_16x16x32_bf16 v[218:221], a[4:7], v[138:141], v[218:221]\n"
      "v_add_f32 v150, v150, v151\n"
      "v_mov_b32 v151, v150\n"
      "v_mfma_f32_16x16x32_bf16 v[218:221], a[8:11], v[206:209], v[218:221]\n"
      "v_cvt_pk_bf16_f32 v195, v196, v197\n"
      "v_permlane32_swap_b32 v150, v151\n"
      "v_mfma_f32_16x16x32_bf16 v[218:221], a[12:15], v[96:99], v[218:221]\n"
      "v_add_f32 v150, v150, v151\n"
      "v_rsq_f32 v150, v150\n"
      "v_mfma_f32_16x16x32_bf16 v[218:221], a[16:19], v[158:161], v[218:221]\n"
      "v_mov_b32 v151, v150\n"
      "v_cvt_pk_bf16_f32 v196, v182, v183\n"
      "v_mfma_f32_16x16x32_bf16 v[218:221], a[20:23], v[114:117], v[218:221]\n"
      "v_permlane16_swap_b32 v150, v151\n"
      "v_mfma_f32_16x16x32_bf16 v[126:129], a[24:27], v[134:137], 0\n"
      "buffer_store_dwordx4 v[106:109], v22, s[24:27], s46 offen sc1\n"
      "v_mfma_f32_16x16x32_bf16 v[126:129], a[28:31], v[138:141], v[126:129]\n"
      "v_mul_f32 v150, s50, v150\n"
      "v_mul_f32 v142, v151, v142\n"
      "v_mfma_f32_16x16x32_bf16 v[126:129], a[32:35], v[206:209], v[126:129]\n"
      "v_mul_f32 v143, v151, v143\n"
      "v_mul_f32 v110, v150, v110\n"
      "v_mfma_f32_16x16x32_bf16 v[126:129], a[36:39], v[96:99], v[126:129]\n"
      "v_mul_f32 v111, v150, v111\n"
      "v_cvt_pk_bf16_f32 v142, v142, v143\n"
      "v_mfma_f32_16x16x32_bf16 v[126:129], a[40:43], v[158:161], v[126:129]\n"
      "v_mul_f32 v144, v151, v144\n"
      "v_mul_f32 v145, v151, v145\n"
      "v_mfma_f32_16x16x32_bf16 v[126:129], a[44:47], v[114:117], v[126:129]\n"
      "v_mul_f32 v186, v151, v186\n"
      "v_mul_f32 v187, v151, v187\n"
      "v_mfma_f32_16x16x32_bf16 v[190:193], a[48:51], v[134:137], 0\n"
      "v_cvt_pk_bf16_f32 v110, v110, v111\n"
      "v_mul_f32_e64 v112, v150, v112\n"
      "v_mfma_f32_16x16x32_bf16 v[190:193], a[52:55], v[138:141], v[190:193]\n"
      "v_mul_f32 v113, v150, v113\n"
      "v_mul_f32 v146, v150, v146\n"
      "v_mfma_f32_16x16x32_bf16 v[190:193], a[56:59], v[206:209], v[190:193]\n"
      "buffer_store_dwordx4 v[154:157], v23, s[24:27], s46 offen sc1\n"
      "v_mfma_f32_16x16x32_bf16 v[190:193], a[60:63], v[96:99], v[190:193]\n"
      "v_mul_f32 v147, v150, v147\n"
      "v_cvt_pk_bf16_f32 v143, v144, v145\n"
      "v_mfma_f32_16x16x32_bf16 v[190:193], a[64:67], v[158:161], v[190:193]\n"
      "v_mul_f32 v178, v151, v178\n"
      "v_mul_f32 v179, v151, v179\n"
      "v_mfma_f32_16x16x32_bf16 v[190:193], a[68:71], v[114:117], v[190:193]\n"
      "v_mul_f32 v180, v151, v180\n"
      "v_mul_f32_e64 v181, v151, v181\n"
      "v_mfma_f32_16x16x32_bf16 v[162:165], a[72:75], v[134:137], 0\n"
      "v_cvt_pk_bf16_f32 v186, v186, v187\n"
      "v_mul_f32 v188, v151, v188\n"
      "v_mfma_f32_16x16x32_bf16 v[162:165], a[76:79], v[138:141], v[162:165]\n"
      "ds_read_b128 v[198:201], v9 offset:5632\n"
      "v_mul_f32 v189, v151, v189\n"
      "v_mfma_f32_16x16x32_bf16 v[162:165], a[80:83], v[206:209], v[162:165]\n"
      "v_cvt_pk_bf16_f32 v111, v112, v113\n"
      "v_mul_f32 v122, v150, v122\n"
      "v_mfma_f32_16x16x32_bf16 v[162:165], a[84:87], v[96:99], v[162:165]\n"
      "v_mul_f32 v123, v150, v123\n"
      "v_mul_f32 v124, v150, v124\n"
      "v_mfma_f32_16x16x32_bf16 v[162:165], a[88:91], v[158:161], v[162:165]\n"
      "buffer_store_dwordx4 v[210:213], v25, s[24:27], s46 offen sc1\n"
      "v_mfma_f32_16x16x32_bf16 v[162:165], a[92:95], v[114:117], v[162:165]\n"
      "v_mul_f32 v125, v150, v125\n"
      "v_cvt_pk_bf16_f32 v146, v146, v147\n"
      "v_mfma_f32_16x16x32_bf16 v[154:157], a[96:99], v[134:137], 0\n"
      "v_mul_f32 v148, v150, v148\n"
      "v_mul_f32 v149, v150, v149\n"
      "v_mfma_f32_16x16x32_bf16 v[154:157], a[100:103], v[138:141], v[154:157]\n"
      "v_cvt_pk_bf16_f32 v144, v178, v179\n"
      "v_cvt_pk_bf16_f32 v145, v180, v181\n"
      "v_mfma_f32_16x16x32_bf16 v[154:157], a[104:107], v[206:209], v[154:157]\n"
      "v_cvt_pk_bf16_f32 v187, v188, v189\n"
      "v_cvt_pk_bf16_f32 v112, v122, v123\n"
      "v_mfma_f32_16x16x32_bf16 v[154:157], a[108:111], v[96:99], v[154:157]\n"
      "v_cvt_pk_bf16_f32 v113, v124, v125\n"
      "v_cvt_pk_bf16_f32 v147, v148, v149\n"
      "v_mfma_f32_16x16x32_bf16 v[154:157], a[112:115], v[158:161], v[154:157]\n"
      "v_fma_f32 v77, v218, v218, s51\n"
      "v_fma_f32 v80, v162, v162, s51\n"
      "v_mfma_f32_16x16x32_bf16 v[154:157], a[116:119], v[114:117], v[154:157]\n"
      "v_fmac_f32 v77, v219, v219\n"
      "v_fmac_f32 v80, v163, v163\n"
      "v_mfma_f32_16x16x32_bf16 v[122:125], a[120:123], v[134:137], 0\n"
      "v_fmac_f32 v77, v220, v220\n"
      "v_fmac_f32 v80, v164, v164\n"
      "v_mfma_f32_16x16x32_bf16 v[122:125], a[124:127], v[138:141], v[122:125]\n"
      "v_fmac_f32 v77, v221, v221\n"
      "v_fmac_f32 v80, v165, v165\n"
      "v_mfma_f32_16x16x32_bf16 v[122:125], a[128:131], v[206:209], v[122:125]\n"
      "v_fmac_f32 v77, v126, v126\n"
      "v_fmac_f32 v80, v154, v154\n"
      "v_mfma_f32_16x16x32_bf16 v[122:125], a[132:135], v[96:99], v[122:125]\n"
      "v_fmac_f32 v77, v127, v127\n"
      "v_fmac_f32 v80, v155, v155\n"
      "v_mfma_f32_16x16x32_bf16 v[122:125], a[136:139], v[158:161], v[122:125]\n"
      "v_fmac_f32 v77, v128, v128\n"
      "v_fmac_f32 v80, v156, v156\n"
      "v_mfma_f32_16x16x32_bf16 v[122:125], a[140:143], v[114:117], v[122:125]\n"
      "v_fmac_f32 v77, v129, v129\n"
      "v_fmac_f32 v80, v157, v157\n"
      "v_mfma_f32_16x16x32_bf16 v[166:169], v[134:137], a[144:147], 0\n"
      "v_fmac_f32 v77, v190, v190\n"
      "v_fmac_f32 v77, v191, v191\n"
      "v_mfma_f32_16x16x32_bf16 v[166:169], v[138:141], a[148:151], v[166:169]\n"
      "v_fmac_f32 v77, v192, v192\n"
      "v_fmac_f32 v77, v193, v193\n"
      "v_mfma_f32_16x16x32_bf16 v[166:169], v[206:209], a[152:155], v[166:169]\n"
      "v_fmac_f32 v80, v122, v122\n"
      "v_fmac_f32 v80, v123, v123\n"
      "v_mfma_f32_16x16x32_bf16 v[166:169], v[96:99], a[156:159], v[166:169]\n"
      "v_fmac_f32 v80, v124, v124\n"
      "v_fmac_f32 v80, v125, v125\n"
      "v_mfma_f32_16x16x32_bf16 v[166:169], v[158:161], a[160:163], v[166:169]\n"
      "v_cvt_pk_bf16_f32 v197, v184, v185\n"
      "v_permlane16_swap_b32_e64 v77, v80\n"
      "v_mfma_f32_16x16x32_bf16 v[166:169], v[114:117], a[164:167], v[166:169]\n"
      "ds_read_b128 v[178:181], v2 offset:18432\n"
      "v_add_f32_e64 v77, v77, v80\n"
      "v_mfma_f32_16x16x32_bf16 v[148:151], v[134:137], a[168:171], 0\n"
      "ds_read_b128 v[182:185], v2 offset:21504\n"
      "v_mov_b32 v80, v77\n"
      "v_mfma_f32_16x16x32_bf16 v[148:151], v[138:141], a[172:175], v[148:151]\n"
      "ds_read_b128 v[106:109], v2 offset:24576\n"
      "v_permlane32_swap_b32 v77, v80\n"
      "v_mfma_f32_16x16x32_bf16 v[148:151], v[206:209], a[176:179], v[148:151]\n"
      "v_add_f32 v77, v77, v80\n"
      "v_rsq_f32 v77, v77\n"
      "v_mfma_f32_16x16x32_bf16 v[148:151], v[96:99], a[180:183], v[148:151]\n"
      "v_mov_b32_e64 v80, v77\n"
      "v_cvt_pk_bf16_f32 v76, v166, v167\n"
      "v_mfma_f32_16x16x32_bf16 v[148:151], v[158:161], a[184:187], v[148:151]\n"
      "v_permlane16_swap_b32 v77, v80\n"
      "v_mfma_f32_16x16x32_bf16 v[148:151], v[114:117], a[188:191], v[148:151]\n"
      "s_waitcnt lgkmcnt(2)\n"
      "v_mul_f32 v77, s50, v77\n"
      "v_mul_f32 v162, v80, v162\n"
      "v_mfma_f32_16x16x32_bf16 v[210:213], v[134:137], a[192:195], 0\n"
      "s_waitcnt lgkmcnt(1)\n"
      "v_mul_f32 v163, v80, v163\n"
      "v_mul_f32_e64 v218, v77, v218\n"
      "v_mfma_f32_16x16x32_bf16 v[210:213], v[138:141], a[196:199], v[210:213]\n"
      "s_waitcnt lgkmcnt(0)\n"
      "v_mul_f32 v219, v77, v219\n"
      "v_cvt_pk_bf16_f32 v162, v162, v163\n"
      "v_mfma_f32_16x16x32_bf16 v[210:213], v[206:209], a[200:203], v[210:213]\n"
      "ds_read_b128 v[138:141], v2 offset:27648\n"
      "v_mul_f32 v164, v80, v164\n"
      "v_mfma_f32_16x16x32_bf16 v[210:213], v[96:99], a[204:207], v[210:213]\n"
      "ds_read_b128 v[134:137], v2 offset:30720\n"
      "v_mul_f32 v165, v80, v165\n"
      "v_mfma_f32_16x16x32_bf16 v[210:213], v[158:161], a[208:211], v[210:213]\n"
      "ds_read_b128 v[206:209], v2 offset:33792\n"
      "v_mul_f32 v122, v80, v122\n"
      "v_mfma_f32_16x16x32_bf16 v[210:213], v[114:117], a[212:215], v[210:213]\n"
      "v_mul_f32 v123, v80, v123\n"
      "v_cvt_pk_bf16_f32 v218, v218, v219\n"
      "v_mfma_f32_16x16x32_bf16 v[158:161], a[216:219], v[178:181], 0\n"
      "v_mul_f32 v220, v77, v220\n"
      "v_mul_f32 v221, v77, v221\n"
      "v_mfma_f32_16x16x32_bf16 v[158:161], a[220:223], v[182:185], v[158:161]\n"
      "v_mul_f32 v190, v77, v190\n"
      "v_mul_f32 v191, v77, v191\n"
      "v_mfma_f32_16x16x32_bf16 v[158:161], a[224:227], v[106:109], v[158:161]\n"
      "s_waitcnt lgkmcnt(2)\n"
      "v_cvt_pk_bf16_f32 v163, v164, v165\n"
      "v_mul_f32 v154, v80, v154\n"
      "v_mfma_f32_16x16x32_bf16 v[96:99], a[240:243], v[178:181], 0\n"
      "s_waitcnt lgkmcnt(1)\n"
      "v_mul_f32 v155, v80, v155\n"
      "v_mul_f32 v156, v80, v156\n"
      "v_mfma_f32_16x16x32_bf16 v[96:99], a[244:247], v[182:185], v[96:99]\n"
      "s_waitcnt lgkmcnt(0)\n"
      "v_mul_f32 v157, v80, v157\n"
      "v_cvt_pk_bf16_f32 v122, v122, v123\n"
      "v_mfma_f32_16x16x32_bf16 v[96:99], a[248:251], v[106:109], v[96:99]\n"
      "v_mul_f32 v124, v80, v124\n"
      "v_mul_f32_e64 v125, v80, v125\n"
      "v_mfma_f32_16x16x32_bf16 v[114:117], v[232:235], v[178:181], 0\n"
      "v_cvt_pk_bf16_f32 v219, v220, v221\n"
      "v_mul_f32 v126, v77, v126\n"
      "v_mfma_f32_16x16x32_bf16 v[114:117], v[236:239], v[182:185], v[114:117]\n"
      "v_mul_f32 v127, v77, v127\n"
      "v_mul_f32 v128, v77, v128\n"
      "v_mfma_f32_16x16x32_bf16 v[114:117], v[240:243], v[106:109], v[114:117]\n"
      "ds_read_b128 v[106:109], v10 offset:32\n"
      "v_mul_f32 v129, v77, v129\n"
      "v_mfma_f32_16x16x32_bf16 v[158:161], a[228:231], v[138:141], v[158:161]\n"
      "v_cvt_pk_bf16_f32 v190, v190, v191\n"
      "v_mul_f32 v192, v77, v192\n"
      "v_mfma_f32_16x16x32_bf16 v[158:161], a[232:235], v[134:137], v[158:161]\n"
      "v_mul_f32 v193, v77, v193\n"
      "v_cvt_pk_bf16_f32 v164, v154, v155\n"
      "v_mfma_f32_16x16x32_bf16 v[158:161], a[236:239], v[206:209], v[158:161]\n"
      "v_cvt_pk_bf16_f32 v165, v156, v157\n"
      "v_cvt_pk_bf16_f32 v123, v124, v125\n"
      "v_mfma_f32_16x16x32_bf16 v[96:99], a[252:255], v[138:141], v[96:99]\n"
      "ds_read_b64 v[124:125], v10 offset:24\n"
      "v_cvt_pk_bf16_f32 v220, v126, v127\n"
      "v_mfma_f32_16x16x32_bf16 v[96:99], v[224:227], v[134:137], v[96:99]\n"
      "ds_read_b128 v[182:185], v2 offset:18688\n"
      "v_cvt_pk_bf16_f32 v221, v128, v129\n"
      "v_mfma_f32_16x16x32_bf16 v[96:99], v[228:231], v[206:209], v[96:99]\n"
      "ds_read_b128 v[154:157], v2 offset:21760\n"
      "v_cvt_pk_bf16_f32 v191, v192, v193\n"
      "v_mfma_f32_16x16x32_bf16 v[114:117], v[244:247], v[138:141], v[114:117]\n"
      "ds_read_b128 v[138:141], v2 offset:24832\n"
      "v_cvt_pk_bf16_f32 v77, v168, v169\n"
      "v_mfma_f32_16x16x32_bf16 v[114:117], v[248:251], v[134:137], v[114:117]\n"
      "s_waitcnt lgkmcnt(4)\n"
      "v_cvt_pk_bf16_f32 v130, v130, v131\n"
      "v_cvt_pk_bf16_f32 v131, v132, v133\n"
      "v_mfma_f32_16x16x32_bf16 v[114:117], v[252:255], v[206:209], v[114:117]\n"
      "v_cvt_pk_bf16_f32 v132, v202, v203\n"
      "v_cvt_pk_bf16_f32 v133, v204, v205\n"
      "v_mfma_f32_16x16x32_bf16 v[202:205], v[118:121], v[100:103], v[40:43]\n"
      "s_waitcnt lgkmcnt(3)\n"
      "v_cvt_pk_bf16_f32 v80, v148, v149\n"
      "v_cvt_pk_bf16_f32 v81, v150, v151\n"
      "v_mfma_f32_16x16x32_bf16 v[126:129], v[142:145], v[100:103], v[44:47]\n"
      "s_waitcnt lgkmcnt(2)\n"
      "v_cvt_pk_bf16_f32 v214, v214, v215\n"
      "v_cvt_pk_bf16_f32 v215, v216, v217\n"
      "v_mfma_f32_16x16x32_bf16 v[134:137], v[162:165], v[100:103], v[48:51]\n"
      "s_waitcnt lgkmcnt(1)\n"
      "v_cvt_pk_bf16_f32 v216, v170, v171\n"
      "v_cvt_pk_bf16_f32 v217, v172, v173\n"
      "v_mfma_f32_16x16x32_bf16 v[170:173], v[106:109], v[100:103], v[24:27]\n"
      "s_waitcnt lgkmcnt(0)\n"
      "v_cvt_pk_bf16_f32 v84, v210, v211\n"
      "v_cvt_pk_bf16_f32 v85, v212, v213\n"
      "v_mfma_f32_16x16x16_bf16 v[202:205], v[104:105], v[152:153], v[202:205]\n"
      "v_fma_f32 v222, v158, v158, s52\n"
      "v_fmac_f32 v222, v159, v159\n"
      "v_fmac_f32 v222, v160, v160\n"
      "v_mfma_f32_16x16x16_bf16 v[126:129], v[186:187], v[152:153], v[126:129]\n"
      "v_fmac_f32 v222, v161, v161\n"
      "v_fmac_f32 v222, v96, v96\n"
      "v_fmac_f32 v222, v97, v97\n"
      "v_mfma_f32_16x16x16_bf16 v[134:137], v[122:123], v[152:153], v[134:137]\n"
      "v_exp_f32 v202, v202\n"
      "v_exp_f32 v203, v203\n"
      "v_mfma_f32_16x16x16_bf16 v[170:173], v[124:125], v[152:153], v[170:173]\n"
      "v_exp_f32 v204, v204\n"
      "v_exp_f32 v205, v205\n"
      "v_mfma_f32_16x16x32_bf16 v[178:181], a[216:219], v[182:185], 0\n"
      "v_exp_f32 v126, v126\n"
      "v_mfma_f32_16x16x32_bf16 v[178:181], a[220:223], v[154:157], v[178:181]\n"
      "v_exp_f32 v127, v127\n"
      "v_mfma_f32_16x16x32_bf16 v[178:181], a[224:227], v[138:141], v[178:181]\n"
      "v_exp_f32 v128, v128\n"
      "v_mfma_f32_16x16x32_bf16 v[166:169], a[240:243], v[182:185], 0\n"
      "v_exp_f32 v129, v129\n"
      "v_mfma_f32_16x16x32_bf16 v[166:169], a[244:247], v[154:157], v[166:169]\n"
      "v_cvt_pk_bf16_f32 v202, v202, v203\n"
      "v_cvt_pk_bf16_f32 v203, v204, v205\n"
      "v_mfma_f32_16x16x32_bf16 v[166:169], a[248:251], v[138:141], v[166:169]\n"
      "v_cvt_pk_bf16_f32 v204, v126, v127\n"
      "v_cvt_pk_bf16_f32 v205, v128, v129\n"
      "v_mfma_f32_16x16x32_bf16 v[126:129], v[232:235], v[182:185], 0\n"
      "v_exp_f32_e64 v134, v134\n"
      "v_mfma_f32_16x16x32_bf16 v[126:129], v[236:239], v[154:157], v[126:129]\n"
      "ds_read_u16 v78, v11\n"
      "v_exp_f32 v135, v135\n"
      "v_mfma_f32_16x16x32_bf16 v[126:129], v[240:243], v[138:141], v[126:129]\n"
      "v_exp_f32 v136, v136\n"
      "v_mfma_f32_16x16x32_bf16 v[138:141], v[118:121], v[110:113], v[52:55]\n"
      "ds_read_u16 v82, v11 offset:32\n"
      "v_exp_f32 v137, v137\n"
      "v_mfma_f32_16x16x32_bf16 v[210:213], v[142:145], v[110:113], v[56:59]\n"
      "v_exp_f32 v170, v170\n"
      "v_mfma_f32_16x16x32_bf16 v[150:153], v[162:165], v[110:113], v[60:63]\n"
      "ds_read_u16 v86, v11 offset:64\n"
      "v_cvt_pk_bf16_f32 v88, v134, v135\n"
      "v_mfma_f32_16x16x32_bf16 v[182:185], v[106:109], v[110:113], v[26:29]\n"
      "v_cvt_pk_bf16_f32 v89, v136, v137\n"
      "v_cvt_pk_bf16_f32 v90, v170, 0\n"
      "v_mfma_f32_16x16x16_bf16 v[138:141], v[104:105], v[146:147], v[138:141]\n"
      "v_fmac_f32 v222, v98, v98\n"
      "v_fmac_f32 v222, v99, v99\n"
      "v_fmac_f32_e64 v222, v114, v114\n"
      "v_mfma_f32_16x16x16_bf16 v[210:213], v[186:187], v[146:147], v[210:213]\n"
      "v_fmac_f32 v222, v115, v115\n"
      "v_fmac_f32 v222, v116, v116\n"
      "v_fmac_f32 v222, v117, v117\n"
      "v_mfma_f32_16x16x16_bf16 v[150:153], v[122:123], v[146:147], v[150:153]\n"
      "v_exp_f32 v138, v138\n"
      "v_exp_f32 v139, v139\n"
      "v_mfma_f32_16x16x16_bf16 v[182:185], v[124:125], v[146:147], v[182:185]\n"
      "v_exp_f32 v140, v140\n"
      "v_exp_f32 v141, v141\n"
      "v_mfma_f32_16x16x32_bf16 v[110:113], v[32:35], v[202:205], 0\n"
      "s_waitcnt lgkmcnt(0)\n"
      "v_exp_f32 v210, v210\n"
      "v_mfma_f32_16x16x32_bf16 v[110:113], v[32:35], v[88:91], v[110:113]\n"
      "v_exp_f32 v211, v211\n"
      "v_mfma_f32_16x16x32_bf16 v[146:149], v[194:197], v[202:205], 0\n"
      "v_exp_f32_e64 v212, v212\n"
      "v_mfma_f32_16x16x32_bf16 v[146:149], v[76:79], v[88:91], v[146:149]\n"
      "ds_read_b128 v[134:137], v2 offset:27904\n"
      "v_exp_f32_e64 v213, v213\n"
      "v_mfma_f32_16x16x32_bf16 v[170:173], v[130:133], v[202:205], 0\n"
      "ds_read_b128 v[100:103], v2 offset:30976\n"
      "v_cvt_pk_bf16_f32 v138, v138, v139\n"
      "v_mfma_f32_16x16x32_bf16 v[170:173], v[80:83], v[88:91], v[170:173]\n"
      "v_cvt_pk_bf16_f32 v139, v140, v141\n"
      "v_cvt_pk_bf16_f32 v140, v210, v211\n"
      "v_mfma_f32_16x16x32_bf16 v[208:211], v[214:217], v[202:205], 0\n"
      "v_cvt_pk_bf16_f32 v141, v212, v213\n"
      "v_exp_f32_e64 v150, v150\n"
      "v_mfma_f32_16x16x32_bf16 v[208:211], v[84:87], v[88:91], v[208:211]\n"
      "ds_read_b128 v[202:205], v2 offset:34048\n"
      "v_exp_f32 v151, v151\n"
      "v_mfma_f32_16x16x32_bf16 v[154:157], v[118:121], v[218:221], v[64:67]\n"
      "v_exp_f32 v152, v152\n"
      "v_exp_f32 v153, v153\n"
      "v_exp_f32 v182, v182\n"
      "v_cvt_pk_bf16_f32 v92, v150, v151\n"
      "v_cvt_pk_bf16_f32 v93, v152, v153\n"
      "v_cvt_pk_bf16_f32 v94, v182, 0\n"
      "v_rcp_f32 v110, v110\n"
      "s_waitcnt vmcnt(6)\n"
      "v_or3_b32 v174, v174, v175, v176\n"
      "v_mfma_f32_16x16x32_bf16 v[182:185], v[142:145], v[218:221], v[68:71]\n"
      "s_waitcnt lgkmcnt(1)\n"
      "v_mul_f32 v146, v110, v146\n"
      "v_mul_f32 v147, v110, v147\n"
      "v_cvt_pk_bf16_f32 v146, v146, v147\n"
      "v_mul_f32 v148, v110, v148\n"
      "v_mul_f32 v149, v110, v149\n"
      "v_cvt_pk_bf16_f32 v147, v148, v149\n"
      "v_mul_f32 v170, v110, v170\n"
      "v_mul_f32 v171, v110, v171\n"
      "v_mul_f32 v172, v110, v172\n"
      "v_mul_f32_e64 v173, v110, v173\n"
      "v_cvt_pk_bf16_f32 v148, v170, v171\n"
      "v_cvt_pk_bf16_f32 v149, v172, v173\n"
      "v_mfma_f32_16x16x32_bf16 v[150:153], v[162:165], v[218:221], v[72:75]\n"
      "ds_write_b128 v3, v[146:149]\n"
      "v_mfma_f32_16x16x32_bf16 v[146:149], v[106:109], v[218:221], v[28:31]\n"
      "s_waitcnt lgkmcnt(1)\n"
      "v_mul_f32 v208, v110, v208\n"
      "v_mul_f32_e64 v209, v110, v209\n"
      "v_mfma_f32_16x16x16_bf16 v[154:157], v[104:105], v[190:191], v[154:157]\n"
      "v_cvt_pk_bf16_f32 v208, v208, v209\n"
      "v_mul_f32_e64 v210, v110, v210\n"
      "v_mul_f32 v211, v110, v211\n"
      "v_mfma_f32_16x16x16_bf16 v[182:185], v[186:187], v[190:191], v[182:185]\n"
      "v_cvt_pk_bf16_f32 v209, v210, v211\n"
      "s_waitcnt vmcnt(5)\n"
      "v_or3_b32 v174, v174, v177, v8\n"
      "v_and_b32 v174, 0x7fff7fff, v174\n"
      "v_mfma_f32_16x16x16_bf16 v[150:153], v[122:123], v[190:191], v[150:153]\n"
      "v_exp_f32 v154, v154\n"
      "v_exp_f32 v155, v155\n"
      "v_mfma_f32_16x16x16_bf16 v[146:149], v[124:125], v[190:191], v[146:149]\n"
      "v_exp_f32 v156, v156\n"
      "v_exp_f32 v157, v157\n"
      "v_mfma_f32_16x16x32_bf16 v[178:181], a[228:231], v[134:137], v[178:181]\n"
      "ds_write_b64 v4, v[208:209]\n"
      "v_mfma_f32_16x16x32_bf16 v[178:181], a[232:235], v[100:103], v[178:181]\n"
      "v_exp_f32 v182, v182\n"
      "v_mfma_f32_16x16x32_bf16 v[178:181], a[236:239], v[202:205], v[178:181]\n"
      "v_exp_f32 v183, v183\n"
      "v_mfma_f32_16x16x32_bf16 v[166:169], a[252:255], v[134:137], v[166:169]\n"
      "ds_write_b32 v31, v174 offset:4608\n"
      "v_mfma_f32_16x16x32_bf16 v[166:169], v[224:227], v[100:103], v[166:169]\n"
      "ds_read_b128 v[210:213], v10\n"
      "v_exp_f32 v184, v184\n"
      "v_mfma_f32_16x16x32_bf16 v[166:169], v[228:231], v[202:205], v[166:169]\n"
      "v_exp_f32 v185, v185\n"
      "v_mfma_f32_16x16x32_bf16 v[126:129], v[244:247], v[134:137], v[126:129]\n"
      "v_cvt_pk_bf16_f32 v154, v154, v155\n"
      "v_cvt_pk_bf16_f32 v155, v156, v157\n"
      "v_mfma_f32_16x16x32_bf16 v[126:129], v[248:251], v[100:103], v[126:129]\n"
      "v_cvt_pk_bf16_f32 v156, v182, v183\n"
      "v_cvt_pk_bf16_f32 v157, v184, v185\n"
      "v_mfma_f32_16x16x32_bf16 v[126:129], v[252:255], v[202:205], v[126:129]\n"
      "ds_read_b64 v[110:111], v10 offset:16\n"
      "v_exp_f32 v150, v150\n"
      "v_mfma_f32_16x16x32_bf16 v[188:191], v[32:35], v[138:141], 0\n"
      "v_exp_f32 v151, v151\n"
      "v_mfma_f32_16x16x32_bf16 v[188:191], v[32:35], v[92:95], v[188:191]\n"
      "v_exp_f32_e64 v152, v152\n"
      "v_mfma_f32_16x16x32_bf16 v[202:205], v[194:197], v[138:141], 0\n"
      "s_waitcnt lgkmcnt(1)\n"
      "v_exp_f32 v153, v153\n"
      "v_mfma_f32_16x16x32_bf16 v[202:205], v[76:79], v[92:95], v[202:205]\n"
      "ds_read_b128 v[172:175], v2 offset:18944\n"
      "v_exp_f32_e64 v146, v146\n"
      "v_mfma_f32_16x16x32_bf16 v[134:137], v[130:133], v[138:141], 0\n"
      "ds_read_b128 v[190:193], v2 offset:22016\n"
      "v_cvt_pk_bf16_f32 v88, v150, v151\n"
      "v_mfma_f32_16x16x32_bf16 v[134:137], v[80:83], v[92:95], v[134:137]\n"
      "ds_read_b128 v[218:221], v2 offset:25088\n"
      "v_cvt_pk_bf16_f32 v89, v152, v153\n"
      "v_mfma_f32_16x16x32_bf16 v[148:151], v[214:217], v[138:141], 0\n"
      "s_waitcnt lgkmcnt(3)\n"
      "v_cvt_pk_bf16_f32 v90, v146, 0\n"
      "v_fma_f32 v185, v178, v178, s52\n"
      "v_mfma_f32_16x16x32_bf16 v[148:151], v[84:87], v[92:95], v[148:151]\n"
      "v_fmac_f32 v185, v179, v179\n"
      "v_fmac_f32 v185, v180, v180\n"
      "v_mfma_f32_16x16x32_bf16 v[206:209], v[210:213], v[118:121], v[30:33]\n"
      "v_fmac_f32 v185, v181, v181\n"
      "v_fmac_f32 v185, v166, v166\n"
      "v_mfma_f32_16x16x32_bf16 v[138:141], v[210:213], v[142:145], v[30:33]\n"
      "s_waitcnt lgkmcnt(2)\n"
      "v_fmac_f32 v185, v167, v167\n"
      "v_fmac_f32 v185, v168, v168\n"
      "v_mfma_f32_16x16x32_bf16 v[118:121], v[210:213], v[162:165], v[30:33]\n"
      "s_waitcnt lgkmcnt(1)\n"
      "v_fmac_f32 v185, v169, v169\n"
      "v_fmac_f32_e64 v185, v126, v126\n"
      "v_mfma_f32_16x16x32_bf16 v[162:165], v[210:213], v[106:109], v[30:33]\n"
      "s_waitcnt lgkmcnt(0)\n"
      "v_fmac_f32 v185, v127, v127\n"
      "v_fmac_f32 v185, v128, v128\n"
      "v_mfma_f32_16x16x16_bf16 v[206:209], v[110:111], v[104:105], v[206:209]\n"
      "v_fmac_f32 v185, v129, v129\n"
      "v_rcp_f32 v188, v188\n"
      "v_mfma_f32_16x16x16_bf16 v[138:141], v[110:111], v[186:187], v[138:141]\n"
      "v_permlane16_swap_b32 v222, v185\n"
      "v_add_f32 v222, v222, v185\n"
      "v_mfma_f32_16x16x16_bf16 v[118:121], v[110:111], v[122:123], v[118:121]\n"
      "v_mul_f32 v202, v188, v202\n"
      "v_mul_f32 v203, v188, v203\n"
      "v_cvt_pk_bf16_f32 v202, v202, v203\n"
      "v_mfma_f32_16x16x16_bf16 v[162:165], v[110:111], v[124:125], v[162:165]\n"
      "v_mul_f32 v204, v188, v204\n"
      "v_cndmask_b32 v206, v206, v138, s[58:59]\n"
      "v_mul_f32_e64 v205, v188, v205\n"
      "v_mfma_f32_16x16x32_bf16 v[144:147], a[216:219], v[172:175], 0\n"
      "v_cndmask_b32 v206, v206, v118, s[60:61]\n"
      "v_cvt_pk_bf16_f32 v203, v204, v205\n"
      "v_mfma_f32_16x16x32_bf16 v[144:147], a[220:223], v[190:193], v[144:147]\n"
      "v_mul_f32 v134, v188, v134\n"
      "v_mul_f32 v135, v188, v135\n"
      "v_mfma_f32_16x16x32_bf16 v[144:147], a[224:227], v[218:221], v[144:147]\n"
      "v_mul_f32 v136, v188, v136\n"
      "v_mul_f32 v137, v188, v137\n"
      "v_mfma_f32_16x16x32_bf16 v[108:111], a[240:243], v[172:175], 0\n"
      "v_cndmask_b32 v206, v206, v162, s[62:63]\n"
      "v_cvt_pk_bf16_f32 v204, v134, v135\n"
      "v_mfma_f32_16x16x32_bf16 v[108:111], a[244:247], v[190:193], v[108:111]\n"
      "ds_read_b128 v[104:107], v2 offset:28160\n"
      "v_cvt_pk_bf16_f32 v205, v136, v137\n"
      "v_mfma_f32_16x16x32_bf16 v[108:111], a[248:251], v[218:221], v[108:111]\n"
      "ds_read_b128 v[162:165], v2 offset:31232\n"
      "v_mul_f32 v148, v188, v148\n"
      "v_mfma_f32_16x16x32_bf16 v[138:141], v[232:235], v[172:175], 0\n"
      "ds_read_b128 v[134:137], v2 offset:34304\n"
      "v_mul_f32 v149, v188, v149\n"
      "v_mfma_f32_16x16x32_bf16 v[138:141], v[236:239], v[190:193], v[138:141]\n"
      "ds_write_b128 v3, v[202:205] offset:256\n"
      "v_mfma_f32_16x16x32_bf16 v[138:141], v[240:243], v[218:221], v[138:141]\n"
      "v_exp_f32 v206, v206\n"
      "v_mfma_f32_16x16x32_bf16 v[202:205], v[32:35], v[154:157], 0\n"
      "s_waitcnt lgkmcnt(3)\n"
      "v_cvt_pk_bf16_f32 v148, v148, v149\n"
      "v_mul_f32 v150, v188, v150\n"
      "v_mfma_f32_16x16x32_bf16 v[202:205], v[32:35], v[88:91], v[202:205]\n"
      "s_waitcnt lgkmcnt(2)\n"
      "v_mul_f32_e64 v151, v188, v151\n"
      "v_cvt_pk_bf16_f32 v149, v150, v151\n"
      "v_mfma_f32_16x16x32_bf16 v[188:191], v[194:197], v[154:157], 0\n"
      "ds_write_b64 v4, v[148:149] offset:256\n"
      "v_mfma_f32_16x16x32_bf16 v[188:191], v[76:79], v[88:91], v[188:191]\n"
      "s_waitcnt lgkmcnt(2)\n"
      "v_cvt_pk_bf16_f32 v206, v206, v206\n"
      "v_rcp_f32 v202, v202\n"
      "v_mfma_f32_16x16x32_bf16 v[174:177], v[130:133], v[154:157], 0\n"
      "ds_write_b16 v13, v206\n"
      "v_mfma_f32_16x16x32_bf16 v[174:177], v[80:83], v[88:91], v[174:177]\n"
      "ds_read_b128 v[210:213], v14\n"
      "v_readlane_b32 s45, v37, s43\n"
      "v_mfma_f32_16x16x32_bf16 v[204:207], v[214:217], v[154:157], 0\n"
      "ds_read_b128 v[118:121], v14 offset:64\n"
      "v_mul_f32 v188, v202, v188\n"
      "v_mfma_f32_16x16x32_bf16 v[204:207], v[84:87], v[88:91], v[204:207]\n"
      "v_mul_f32 v189, v202, v189\n"
      "v_cvt_pk_bf16_f32 v188, v188, v189\n"
      "v_mfma_f32_16x16x32_bf16 v[144:147], a[228:231], v[104:107], v[144:147]\n"
      "v_mul_f32 v190, v202, v190\n"
      "v_mul_f32 v191, v202, v191\n"
      "v_mfma_f32_16x16x32_bf16 v[144:147], a[232:235], v[162:165], v[144:147]\n"
      "v_cvt_pk_bf16_f32 v189, v190, v191\n"
      "v_mul_f32 v174, v202, v174\n"
      "v_mfma_f32_16x16x32_bf16 v[144:147], a[236:239], v[134:137], v[144:147]\n"
      "v_mul_f32 v175, v202, v175\n"
      "v_mul_f32_e64 v176, v202, v176\n"
      "v_mfma_f32_16x16x32_bf16 v[108:111], a[252:255], v[104:107], v[108:111]\n"
      "v_mul_f32 v177, v202, v177\n"
      "v_cvt_pk_bf16_f32 v190, v174, v175\n"
      "v_mfma_f32_16x16x32_bf16 v[108:111], v[224:227], v[162:165], v[108:111]\n"
      "s_waitcnt lgkmcnt(1)\n"
      "v_cvt_pk_bf16_f32 v191, v176, v177\n"
      "v_fma_f32 v203, v144, v144, s52\n"
      "v_mfma_f32_16x16x32_bf16 v[108:111], v[228:231], v[134:137], v[108:111]\n"
      "ds_write_b128 v3, v[188:191] offset:512\n"
      "v_mfma_f32_16x16x32_bf16 v[138:141], v[244:247], v[104:107], v[138:141]\n"
      "s_waitcnt lgkmcnt(1)\n"
      "v_fmac_f32 v203, v145, v145\n"
      "v_fmac_f32 v203, v146, v146\n"
      "v_mfma_f32_16x16x32_bf16 v[138:141], v[248:251], v[162:165], v[138:141]\n"
      "v_fmac_f32 v203, v147, v147\n"
      "v_mul_f32_e64 v204, v202, v204\n"
      "v_mfma_f32_16x16x32_bf16 v[138:141], v[252:255], v[134:137], v[138:141]\n"
      "v_fmac_f32 v203, v108, v108\n"
      "v_fmac_f32 v203, v109, v109\n"
      "v_mfma_f32_16x16x32_bf16 v[188:191], v[210:213], v[32:35], 0\n"
      "v_fmac_f32 v203, v110, v110\n"
      "v_fmac_f32 v203, v111, v111\n"
      "v_mfma_f32_16x16x32_bf16 v[188:191], v[118:121], v[32:35], v[188:191]\n"
      "v_mul_f32 v205, v202, v205\n"
      "v_cvt_pk_bf16_f32 v204, v204, v205\n"
      "v_fmac_f32 v203, v138, v138\n"
      "v_fmac_f32 v203, v139, v139\n"
      "v_fmac_f32 v203, v140, v140\n"
      "v_fmac_f32 v203, v141, v141\n"
      "v_mov_b32 v185, v203\n"
      "v_mul_f32 v206, v202, v206\n"
      "v_mul_f32 v207, v202, v207\n"
      "v_permlane16_swap_b32 v203, v185\n"
      "v_add_f32 v203, v203, v185\n"
      "v_cvt_pk_bf16_f32 v205, v206, v207\n"
      "ds_write_b64 v4, v[204:205] offset:512\n"
      "v_permlane32_swap_b32 v222, v203\n"
      "v_add_f32 v222, v222, v203\n"
      "v_readlane_b32 s46, v37, s42\n"
      "v_or3_b32 v198, v198, v199, v200\n"
      "ds_write_b32 v31, v222\n"
      "v_or_b32 v198, v198, v201\n"
      "v_cmp_ne_u32 s[56:57], 0, v198\n"
      "v_readlane_b32 s47, v38, s43\n"
      "v_add_u32 v12, 32, v12\n"
      "s_or_b32 s95, s45, s46\n"
      "s_or_b32 s95, s95, s48\n"
      "s_cmp_ge_u32 s95, 0x40000000\n"
      "s_cselect_b32 s94, -1, -2\n"
      "s_cmp_gt_u32 s42, s44\n"
      "s_cselect_b32 s95, s94, s95\n"
      "s_cmp_ge_u32 s95, 0x40000000\n"
      "s_cselect_b64 vcc, -1, s[56:57]\n"
      "s_and_b32 s47, s47, -4\n"
      "s_add_u32 s42, s42, 1\n"
      "s_add_u32 s43, s43, 1\n"
      "s_waitcnt lgkmcnt(0)\n"
      "s_barrier\n"
      "s_cbranch_vccz L_loop1_%=\n"
      "s_branch L_disp1_%=\n"
      "s_nop 7\n"
      "s_nop 7\n"
      "L_max1_%=:\n"
      "v_mfma_f32_16x16x32_bf16 v[202:205], v[210:213], v[194:197], 0\n"
      "ds_read_b128 v[134:137], v0 offset:18432\n"
      "ds_read_b128 v[196:199], v9\n"
      "v_mfma_f32_16x16x32_bf16 v[202:205], v[118:121], v[76:79], v[202:205]\n"
      "ds_read_b128 v[218:221], v1 offset:18432\n"
      "s_add_u32 m0, s64, 0\n"
      "v_mfma_f32_16x16x32_bf16 v[104:107], v[210:213], v[130:133], 0\n"
      "ds_read_b128 v[154:157], v0 offset:24576\n"
      "buffer_load_dwordx4 v16, s[16:19], s45 offen  lds\n"
      "v_mfma_f32_16x16x32_bf16 v[104:107], v[118:121], v[80:83], v[104:107]\n"
      "s_add_u32 m0, s64, 4096\n"
      "v_mfma_f32_16x16x32_bf16 v[174:177], v[210:213], v[214:217], 0\n"
      "v_mfma_f32_16x16x32_bf16 v[174:177], v[118:121], v[84:87], v[174:177]\n"
      "s_waitcnt lgkmcnt(3)\n"
      "v_mov_b32_dpp v188, v188 quad_perm:[0,0,0,0] row_mask:0xf bank_mask:0xf\n"
      "v_rcp_f32 v188, v188\n"
      "v_mfma_f32_16x16x32_bf16 v[182:185], a[0:3], v[134:137], 0\n"
      "buffer_load_dwordx4 v17, s[16:19], s45 offen  lds\n"
      "v_mfma_f32_16x16x32_bf16 v[118:121], a[24:27], v[134:137], 0\n"
      "s_add_u32 m0, s64, 8192\n"
      "v_cndmask_b32 v202, v202, v104, s[58:59]\n"
      "v_cndmask_b32 v202, v202, v174, s[60:61]\n"
      "v_mfma_f32_16x16x32_bf16 v[100:103], a[48:51], v[134:137], 0\n"
      "s_waitcnt lgkmcnt(0)\n"
      "v_mul_f32 v202, v188, v202\n"
      "v_cvt_pk_bf16_f32 v202, v202, v202\n"
      "v_mfma_f32_16x16x32_bf16 v[186:189], a[72:75], v[134:137], 0\n"
      "ds_write_b16 v12, v202\n"
      "v_mfma_f32_16x16x32_bf16 v[202:205], a[96:99], v[134:137], 0\n"
      "buffer_load_dwordx4 v18, s[16:19], s45 offen  lds\n"
      "v_mfma_f32_16x16x32_bf16 v[174:177], a[120:123], v[134:137], 0\n"
      "s_add_u32 m0, s64, 12288\n"
      "v_add_f32 v196, v196, v197\n"
      "v_add_f32 v198, v198, v199\n"
      "v_mfma_f32_16x16x32_bf16 v[182:185], a[4:7], v[218:221], v[182:185]\n"
      "v_add_f32 v196, v196, v198\n"
      "v_rsq_f32 v196, v196\n"
      "v_mfma_f32_16x16x32_bf16 v[182:185], a[8:11], v[154:157], v[182:185]\n"
      "ds_read_b128 v[162:165], v1 offset:24576\n"
      "v_mul_f32 v196, 0x415db3d7, v196\n"
      "v_mfma_f32_16x16x32_bf16 v[118:121], a[28:31], v[218:221], v[118:121]\n"
      "ds_read_b128 v[122:125], v0 offset:30720\n"
      "v_mov_b32_e64 v198, v196\n"
      "v_mfma_f32_16x16x32_bf16 v[118:121], a[32:35], v[154:157], v[118:121]\n"
      "ds_read_b128 v[150:153], v1 offset:30720\n"
      "v_permlane32_swap_b32_e64 v196, v198\n"
      "v_mfma_f32_16x16x32_bf16 v[100:103], a[52:55], v[218:221], v[100:103]\n"
      "buffer_load_dwordx4 v19, s[16:19], s45 offen  lds\n"
      "v_mfma_f32_16x16x32_bf16 v[100:103], a[56:59], v[154:157], v[100:103]\n"
      "s_add_u32 m0, s65, 16384\n"
      "v_mov_b32 v197, v196\n"
      "v_mul_f32 v144, v198, v144\n"
      "v_mfma_f32_16x16x32_bf16 v[186:189], a[76:79], v[218:221], v[186:189]\n"
      "v_permlane16_swap_b32 v196, v197\n"
      "v_mfma_f32_16x16x32_bf16 v[186:189], a[80:83], v[154:157], v[186:189]\n"
      "s_waitcnt lgkmcnt(2)\n"
      "v_mul_f32 v158, v196, v158\n"
      "v_mul_f32 v159, v196, v159\n"
      "v_mfma_f32_16x16x32_bf16 v[202:205], a[100:103], v[218:221], v[202:205]\n"
      "buffer_load_dwordx4 v15, s[16:19], s45 offen  lds\n"
      "v_mfma_f32_16x16x32_bf16 v[202:205], a[104:107], v[154:157], v[202:205]\n"
      "s_waitcnt lgkmcnt(0)\n"
      "v_cvt_pk_bf16_f32 v158, v158, v159\n"
      "v_mul_f32 v160, v196, v160\n"
      "v_mfma_f32_16x16x32_bf16 v[174:177], a[124:127], v[218:221], v[174:177]\n"
      "v_mul_f32 v161, v196, v161\n"
      "v_mul_f32 v114, v196, v114\n"
      "v_mfma_f32_16x16x32_bf16 v[174:177], a[128:131], v[154:157], v[174:177]\n"
      "v_mul_f32_e64 v115, v196, v115\n"
      "v_cvt_pk_bf16_f32 v159, v160, v161\n"
      "v_mfma_f32_16x16x32_bf16 v[182:185], a[12:15], v[162:165], v[182:185]\n"
      "buffer_load_dwordx4 v[190:193], v36, s[28:31], s47 offen\n"
      "v_mfma_f32_16x16x32_bf16 v[182:185], a[16:19], v[122:125], v[182:185]\n"
      "v_mul_f32 v96, v196, v96\n"
      "v_mul_f32 v97, v196, v97\n"
      "v_mfma_f32_16x16x32_bf16 v[182:185], a[20:23], v[150:153], v[182:185]\n"
      "v_mul_f32 v98, v196, v98\n"
      "v_mul_f32 v99, v196, v99\n"
      "v_mfma_f32_16x16x32_bf16 v[118:121], a[36:39], v[162:165], v[118:121]\n"
      "v_cvt_pk_bf16_f32 v114, v114, v115\n"
      "v_mul_f32_e64 v116, v196, v116\n"
      "v_mfma_f32_16x16x32_bf16 v[118:121], a[40:43], v[122:125], v[118:121]\n"
      "buffer_load_dword v8, v29, s[28:31], s47 offen\n"
      "v_mfma_f32_16x16x32_bf16 v[118:121], a[44:47], v[150:153], v[118:121]\n"
      "v_mul_f32_e64 v117, v196, v117\n"
      "v_cvt_pk_bf16_f32 v160, v96, v97\n"
      "v_mfma_f32_16x16x32_bf16 v[100:103], a[60:63], v[162:165], v[100:103]\n"
      "v_cvt_pk_bf16_f32 v161, v98, v99\n"
      "v_cvt_pk_bf16_f32 v115, v116, v117\n"
      "v_mfma_f32_16x16x32_bf16 v[100:103], a[64:67], v[122:125], v[100:103]\n"
      "ds_write_b128 v5, v[158:161] offset:18432\n"
      "v_mfma_f32_16x16x32_bf16 v[100:103], a[68:71], v[150:153], v[100:103]\n"
      "v_mul_f32 v178, v197, v178\n"
      "v_mul_f32 v179, v197, v179\n"
      "v_mfma_f32_16x16x32_bf16 v[186:189], a[84:87], v[162:165], v[186:189]\n"
      "v_cvt_pk_bf16_f32 v178, v178, v179\n"
      "v_mul_f32 v180, v197, v180\n"
      "v_mfma_f32_16x16x32_bf16 v[186:189], a[88:91], v[122:125], v[186:189]\n"
      "ds_write_b64 v6, v[114:115] offset:18432\n"
      "v_mfma_f32_16x16x32_bf16 v[186:189], a[92:95], v[150:153], v[186:189]\n"
      "v_mul_f32 v181, v197, v181\n"
      "v_mul_f32 v126, v197, v126\n"
      "v_mfma_f32_16x16x32_bf16 v[202:205], a[108:111], v[162:165], v[202:205]\n"
      "v_mul_f32 v127, v197, v127\n"
      "v_cvt_pk_bf16_f32 v179, v180, v181\n"
      "v_mfma_f32_16x16x32_bf16 v[202:205], a[112:115], v[122:125], v[202:205]\n"
      "v_mul_f32 v166, v197, v166\n"
      "v_mul_f32 v167, v197, v167\n"
      "v_mfma_f32_16x16x32_bf16 v[202:205], a[116:119], v[150:153], v[202:205]\n"
      "v_mul_f32 v168, v197, v168\n"
      "v_mul_f32 v169, v197, v169\n"
      "v_mfma_f32_16x16x32_bf16 v[174:177], a[132:135], v[162:165], v[174:177]\n"
      "v_cvt_pk_bf16_f32 v126, v126, v127\n"
      "v_mul_f32 v128, v197, v128\n"
      "v_mfma_f32_16x16x32_bf16 v[174:177], a[136:139], v[122:125], v[174:177]\n"
      "v_mul_f32 v129, v197, v129\n"
      "v_cvt_pk_bf16_f32 v180, v166, v167\n"
      "v_mfma_f32_16x16x32_bf16 v[174:177], a[140:143], v[150:153], v[174:177]\n"
      "v_cvt_pk_bf16_f32 v181, v168, v169\n"
      "v_cvt_pk_bf16_f32 v127, v128, v129\n"
      "v_mfma_f32_16x16x32_bf16 v[194:197], v[134:137], a[144:147], 0\n"
      "ds_write_b128 v5, v[178:181] offset:24576\n"
      "v_mfma_f32_16x16x32_bf16 v[194:197], v[218:221], a[148:151], v[194:197]\n"
      "v_mul_f32_e64 v145, v198, v145\n"
      "v_cvt_pk_bf16_f32 v144, v144, v145\n"
      "v_mfma_f32_16x16x32_bf16 v[194:197], v[154:157], a[152:155], v[194:197]\n"
      "v_mul_f32 v146, v198, v146\n"
      "v_mul_f32 v147, v198, v147\n"
      "v_mfma_f32_16x16x32_bf16 v[194:197], v[162:165], a[156:159], v[194:197]\n"
      "ds_write_b64 v6, v[126:127] offset:24576\n"
      "v_mfma_f32_16x16x32_bf16 v[194:197], v[122:125], a[160:163], v[194:197]\n"
      "v_mul_f32 v138, v198, v138\n"
      "v_mul_f32 v139, v198, v139\n"
      "v_mfma_f32_16x16x32_bf16 v[194:197], v[150:153], a[164:167], v[194:197]\n"
      "v_cvt_pk_bf16_f32 v145, v146, v147\n"
      "v_mul_f32 v108, v198, v108\n"
      "v_mfma_f32_16x16x32_bf16 v[130:133], v[134:137], a[168:171], 0\n"
      "v_mul_f32 v109, v198, v109\n"
      "v_mul_f32_e64 v110, v198, v110\n"
      "v_mfma_f32_16x16x32_bf16 v[130:133], v[218:221], a[172:175], v[130:133]\n"
      "ds_read_b128 v[158:161], v0 offset:20480\n"
      "v_mul_f32_e64 v111, v198, v111\n"
      "v_mfma_f32_16x16x32_bf16 v[130:133], v[154:157], a[176:179], v[130:133]\n"
      "ds_read_b128 v[96:99], v1 offset:20480\n"
      "v_cvt_pk_bf16_f32 v138, v138, v139\n"
      "v_mfma_f32_16x16x32_bf16 v[130:133], v[162:165], a[180:183], v[130:133]\n"
      "ds_read_b128 v[114:117], v0 offset:26624\n"
      "v_mul_f32_e64 v140, v198, v140\n"
      "v_mfma_f32_16x16x32_bf16 v[130:133], v[122:125], a[184:187], v[130:133]\n"
      "ds_read_b128 v[178:181], v1 offset:26624\n"
      "v_mul_f32_e64 v141, v198, v141\n"
      "v_mfma_f32_16x16x32_bf16 v[130:133], v[150:153], a[188:191], v[130:133]\n"
      "ds_read_b128 v[166:169], v0 offset:32768\n"
      "v_cvt_pk_bf16_f32 v146, v108, v109\n"
      "v_mfma_f32_16x16x32_bf16 v[214:217], v[134:137], a[192:195], 0\n"
      "ds_read_b128 v[126:129], v1 offset:32768\n"
      "v_cvt_pk_bf16_f32 v147, v110, v111\n"
      "v_mfma_f32_16x16x32_bf16 v[214:217], v[218:221], a[196:199], v[214:217]\n"
      "ds_write_b128 v5, v[144:147] offset:30720\n"
      "v_mfma_f32_16x16x32_bf16 v[214:217], v[154:157], a[200:203], v[214:217]\n"
      "s_waitcnt lgkmcnt(4)\n"
      "v_cvt_pk_bf16_f32 v139, v140, v141\n"
      "v_fma_f32 v145, v182, v182, s51\n"
      "v_mfma_f32_16x16x32_bf16 v[214:217], v[162:165], a[204:207], v[214:217]\n"
      "s_waitcnt lgkmcnt(3)\n"
      "v_fma_f32 v147, v186, v186, s51\n"
      "v_fmac_f32_e64 v145, v183, v183\n"
      "v_mfma_f32_16x16x32_bf16 v[214:217], v[122:125], a[208:211], v[214:217]\n"
      "ds_write_b64 v6, v[138:139] offset:30720\n"
      "v_mfma_f32_16x16x32_bf16 v[214:217], v[150:153], a[212:215], v[214:217]\n"
      "s_waitcnt lgkmcnt(2)\n"
      "v_fmac_f32 v147, v187, v187\n"
      "v_fmac_f32 v145, v184, v184\n"
      "v_mfma_f32_16x16x32_bf16 v[198:201], a[0:3], v[158:161], 0\n"
      "v_fmac_f32 v147, v188, v188\n"
      "v_fmac_f32 v145, v185, v185\n"
      "v_mfma_f32_16x16x32_bf16 v[198:201], a[4:7], v[96:99], v[198:201]\n"
      "v_fmac_f32 v147, v189, v189\n"
      "v_fmac_f32 v145, v118, v118\n"
      "v_mfma_f32_16x16x32_bf16 v[198:201], a[8:11], v[114:117], v[198:201]\n"
      "v_fmac_f32 v147, v202, v202\n"
      "v_fmac_f32 v145, v119, v119\n"
      "v_mfma_f32_16x16x32_bf16 v[198:201], a[12:15], v[178:181], v[198:201]\n"
      "v_fmac_f32 v147, v203, v203\n"
      "v_fmac_f32 v145, v120, v120\n"
      "v_mfma_f32_16x16x32_bf16 v[198:201], a[16:19], v[166:169], v[198:201]\n"
      "v_fmac_f32 v147, v204, v204\n"
      "v_fmac_f32 v145, v121, v121\n"
      "v_mfma_f32_16x16x32_bf16 v[198:201], a[20:23], v[126:129], v[198:201]\n"
      "v_fmac_f32 v147, v205, v205\n"
      "v_fmac_f32 v145, v100, v100\n"
      "v_mfma_f32_16x16x32_bf16 v[150:153], a[24:27], v[158:161], 0\n"
      "v_fmac_f32 v147, v174, v174\n"
      "v_fmac_f32 v145, v101, v101\n"
      "v_mfma_f32_16x16x32_bf16 v[150:153], a[28:31], v[96:99], v[150:153]\n"
      "v_fmac_f32 v147, v175, v175\n"
      "v_fmac_f32 v145, v102, v102\n"
      "v_mfma_f32_16x16x32_bf16 v[150:153], a[32:35], v[114:117], v[150:153]\n"
      "v_fmac_f32 v147, v176, v176\n"
      "v_fmac_f32 v145, v103, v103\n"
      "v_mfma_f32_16x16x32_bf16 v[150:153], a[36:39], v[178:181], v[150:153]\n"
      "v_fmac_f32 v147, v177, v177\n"
      "v_fma_f32 v156, v198, v198, s51\n"
      "v_mfma_f32_16x16x32_bf16 v[150:153], a[40:43], v[166:169], v[150:153]\n"
      "v_permlane16_swap_b32 v145, v147\n"
      "v_mfma_f32_16x16x32_bf16 v[150:153], a[44:47], v[126:129], v[150:153]\n"
      "v_add_f32 v145, v145, v147\n"
      "v_mov_b32 v147, v145\n"
      "v_mfma_f32_16x16x32_bf16 v[106:109], a[48:51], v[158:161], 0\n"
      "v_fmac_f32 v156, v199, v199\n"
      "v_permlane32_swap_b32 v145, v147\n"
      "v_mfma_f32_16x16x32_bf16 v[106:109], a[52:55], v[96:99], v[106:109]\n"
      "v_add_f32 v145, v145, v147\n"
      "v_rsq_f32 v145, v145\n"
      "v_mfma_f32_16x16x32_bf16 v[106:109], a[56:59], v[114:117], v[106:109]\n"
      "v_mov_b32 v147, v145\n"
      "v_fmac_f32 v156, v200, v200\n"
      "v_mfma_f32_16x16x32_bf16 v[106:109], a[60:63], v[178:181], v[106:109]\n"
      "v_permlane16_swap_b32 v145, v147\n"
      "v_mfma_f32_16x16x32_bf16 v[106:109], a[64:67], v[166:169], v[106:109]\n"
      "v_mul_f32 v145, s50, v145\n"
      "v_mul_f32 v186, v147, v186\n"
      "v_mfma_f32_16x16x32_bf16 v[106:109], a[68:71], v[126:129], v[106:109]\n"
      "v_mul_f32 v187, v147, v187\n"
      "v_mul_f32 v182, v145, v182\n"
      "v_mfma_f32_16x16x32_bf16 v[138:141], a[72:75], v[158:161], 0\n"
      "v_mul_f32_e64 v183, v145, v183\n"
      "v_cvt_pk_bf16_f32 v186, v186, v187\n"
      "v_mfma_f32_16x16x32_bf16 v[138:141], a[76:79], v[96:99], v[138:141]\n"
      "v_mul_f32 v188, v147, v188\n"
      "v_mul_f32 v189, v147, v189\n"
      "v_mfma_f32_16x16x32_bf16 v[138:141], a[80:83], v[114:117], v[138:141]\n"
      "v_mul_f32 v174, v147, v174\n"
      "v_mul_f32 v175, v147, v175\n"
      "v_mfma_f32_16x16x32_bf16 v[138:141], a[84:87], v[178:181], v[138:141]\n"
      "v_cvt_pk_bf16_f32 v182, v182, v183\n"
      "v_mul_f32_e64 v184, v145, v184\n"
      "v_mfma_f32_16x16x32_bf16 v[138:141], a[88:91], v[166:169], v[138:141]\n"
      "v_mul_f32 v185, v145, v185\n"
      "v_mul_f32 v100, v145, v100\n"
      "v_mfma_f32_16x16x32_bf16 v[138:141], a[92:95], v[126:129], v[138:141]\n"
      "v_mul_f32_e64 v101, v145, v101\n"
      "v_cvt_pk_bf16_f32 v187, v188, v189\n"
      "v_mfma_f32_16x16x32_bf16 v[134:137], a[96:99], v[158:161], 0\n"
      "v_mul_f32 v202, v147, v202\n"
      "v_mul_f32 v203, v147, v203\n"
      "v_mfma_f32_16x16x32_bf16 v[134:137], a[100:103], v[96:99], v[134:137]\n"
      "v_mul_f32 v204, v147, v204\n"
      "v_mul_f32 v205, v147, v205\n"
      "v_mfma_f32_16x16x32_bf16 v[134:137], a[104:107], v[114:117], v[134:137]\n"
      "v_cvt_pk_bf16_f32 v174, v174, v175\n"
      "v_mul_f32_e64 v176, v147, v176\n"
      "v_mfma_f32_16x16x32_bf16 v[134:137], a[108:111], v[178:181], v[134:137]\n"
      "v_mul_f32 v177, v147, v177\n"
      "v_cvt_pk_bf16_f32 v183, v184, v185\n"
      "v_mfma_f32_16x16x32_bf16 v[134:137], a[112:115], v[166:169], v[134:137]\n"
      "v_mul_f32 v118, v145, v118\n"
      "v_mul_f32 v119, v145, v119\n"
      "v_mfma_f32_16x16x32_bf16 v[134:137], a[116:119], v[126:129], v[134:137]\n"
      "v_mul_f32 v120, v145, v120\n"
      "v_mul_f32_e64 v121, v145, v121\n"
      "v_mfma_f32_16x16x32_bf16 v[146:149], a[120:123], v[158:161], 0\n"
      "v_cvt_pk_bf16_f32 v100, v100, v101\n"
      "v_mul_f32 v102, v145, v102\n"
      "v_mfma_f32_16x16x32_bf16 v[146:149], a[124:127], v[96:99], v[146:149]\n"
      "v_mul_f32 v103, v145, v103\n"
      "v_cvt_pk_bf16_f32 v188, v202, v203\n"
      "v_mfma_f32_16x16x32_bf16 v[146:149], a[128:131], v[114:117], v[146:149]\n"
      "v_cvt_pk_bf16_f32 v189, v204, v205\n"
      "v_cvt_pk_bf16_f32 v175, v176, v177\n"
      "v_mfma_f32_16x16x32_bf16 v[146:149], a[132:135], v[178:181], v[146:149]\n"
      "v_cvt_pk_bf16_f32 v184, v118, v119\n"
      "v_cvt_pk_bf16_f32 v185, v120, v121\n"
      "v_mfma_f32_16x16x32_bf16 v[146:149], a[136:139], v[166:169], v[146:149]\n"
      "v_cvt_pk_bf16_f32 v101, v102, v103\n"
      "v_fma_f32 v102, v138, v138, s51\n"
      "v_mfma_f32_16x16x32_bf16 v[146:149], a[140:143], v[126:129], v[146:149]\n"
      "v_fmac_f32 v102, v139, v139\n"
      "v_fmac_f32 v102, v140, v140\n"
      "v_mfma_f32_16x16x32_bf16 v[118:121], v[158:161], a[144:147], 0\n"
      "v_fmac_f32 v156, v201, v201\n"
      "v_fmac_f32 v102, v141, v141\n"
      "v_mfma_f32_16x16x32_bf16 v[118:121], v[96:99], a[148:151], v[118:121]\n"
      "v_fmac_f32 v156, v150, v150\n"
      "v_fmac_f32 v102, v134, v134\n"
      "v_mfma_f32_16x16x32_bf16 v[118:121], v[114:117], a[152:155], v[118:121]\n"
      "v_fmac_f32 v156, v151, v151\n"
      "v_fmac_f32 v102, v135, v135\n"
      "v_mfma_f32_16x16x32_bf16 v[118:121], v[178:181], a[156:159], v[118:121]\n"
      "v_fmac_f32 v156, v152, v152\n"
      "v_fmac_f32 v102, v136, v136\n"
      "v_mfma_f32_16x16x32_bf16 v[118:121], v[166:169], a[160:163], v[118:121]\n"
      "v_fmac_f32 v156, v153, v153\n"
      "v_fmac_f32 v102, v137, v137\n"
      "v_mfma_f32_16x16x32_bf16 v[118:121], v[126:129], a[164:167], v[118:121]\n"
      "v_fmac_f32 v156, v106, v106\n"
      "v_fmac_f32 v102, v146, v146\n"
      "v_mfma_f32_16x16x32_bf16 v[202:205], v[158:161], a[168:171], 0\n"
      "v_fmac_f32 v156, v107, v107\n"
      "v_fmac_f32 v102, v147, v147\n"
      "v_mfma_f32_16x16x32_bf16 v[202:205], v[96:99], a[172:175], v[202:205]\n"
      "ds_read_b128 v[142:145], v0 offset:22528\n"
      "v_fmac_f32_e64 v156, v108, v108\n"
      "v_mfma_f32_16x16x32_bf16 v[202:205], v[114:117], a[176:179], v[202:205]\n"
      "ds_read_b128 v[218:221], v1 offset:22528\n"
      "v_fmac_f32_e64 v102, v148, v148\n"
      "v_mfma_f32_16x16x32_bf16 v[202:205], v[178:181], a[180:183], v[202:205]\n"
      "ds_read_b128 v[122:125], v0 offset:28672\n"
      "v_fmac_f32 v156, v109, v109\n"
      "v_mfma_f32_16x16x32_bf16 v[202:205], v[166:169], a[184:187], v[202:205]\n"
      "ds_read_b128 v[210:213], v1 offset:28672\n"
      "v_fmac_f32 v102, v149, v149\n"
      "v_mfma_f32_16x16x32_bf16 v[202:205], v[126:129], a[188:191], v[202:205]\n"
      "ds_read_b128 v[110:113], v0 offset:34816\n"
      "v_permlane16_swap_b32_e64 v156, v102\n"
      "v_mfma_f32_16x16x32_bf16 v[170:173], v[158:161], a[192:195], 0\n"
      "ds_read_b128 v[158:161], v1 offset:34816\n"
      "v_add_f32 v156, v156, v102\n"
      "v_mfma_f32_16x16x32_bf16 v[170:173], v[96:99], a[196:199], v[170:173]\n"
      "s_waitcnt lgkmcnt(5)\n"
      "v_mov_b32_e64 v102, v156\n"
      "v_cvt_pk_bf16_f32 v194, v194, v195\n"
      "v_mfma_f32_16x16x32_bf16 v[170:173], v[114:117], a[200:203], v[170:173]\n"
      "s_waitcnt lgkmcnt(4)\n"
      "v_permlane32_swap_b32 v156, v102\n"
      "v_mfma_f32_16x16x32_bf16 v[170:173], v[178:181], a[204:207], v[170:173]\n"
      "s_waitcnt lgkmcnt(3)\n"
      "v_add_f32 v156, v156, v102\n"
      "v_rsq_f32 v156, v156\n"
      "v_mfma_f32_16x16x32_bf16 v[170:173], v[166:169], a[208:211], v[170:173]\n"
      "s_waitcnt lgkmcnt(2)\n"
      "v_mov_b32_e64 v102, v156\n"
      "v_cvt_pk_bf16_f32 v195, v196, v197\n"
      "v_mfma_f32_16x16x32_bf16 v[170:173], v[126:129], a[212:215], v[170:173]\n"
      "s_waitcnt lgkmcnt(1)\n"
      "v_permlane16_swap_b32 v156, v102\n"
      "v_mfma_f32_16x16x32_bf16 v[162:165], a[0:3], v[142:145], 0\n"
      "s_waitcnt lgkmcnt(0)\n"
      "v_mul_f32 v156, s50, v156\n"
      "v_mul_f32 v138, v102, v138\n"
      "v_mfma_f32_16x16x32_bf16 v[162:165], a[4:7], v[218:221], v[162:165]\n"
      "v_mul_f32 v139, v102, v139\n"
      "v_mul_f32_e64 v198, v156, v198\n"
      "v_mfma_f32_16x16x32_bf16 v[162:165], a[8:11], v[122:125], v[162:165]\n"
      "v_mul_f32 v199, v156, v199\n"
      "v_cvt_pk_bf16_f32 v138, v138, v139\n"
      "v_mfma_f32_16x16x32_bf16 v[162:165], a[12:15], v[210:213], v[162:165]\n"
      "v_mul_f32 v140, v102, v140\n"
      "v_mul_f32 v141, v102, v141\n"
      "v_mfma_f32_16x16x32_bf16 v[162:165], a[16:19], v[110:113], v[162:165]\n"
      "v_mul_f32 v146, v102, v146\n"
      "v_mul_f32_e64 v147, v102, v147\n"
      "v_mfma_f32_16x16x32_bf16 v[162:165], a[20:23], v[158:161], v[162:165]\n"
      "v_cvt_pk_bf16_f32 v198, v198, v199\n"
      "v_mul_f32 v200, v156, v200\n"
      "v_mfma_f32_16x16x32_bf16 v[166:169], a[24:27], v[142:145], 0\n"
      "v_mul_f32 v201, v156, v201\n"
      "v_mul_f32_e64 v106, v156, v106\n"
      "v_mfma_f32_16x16x32_bf16 v[166:169], a[28:31], v[218:221], v[166:169]\n"
      "v_mul_f32 v107, v156, v107\n"
      "v_cvt_pk_bf16_f32 v139, v140, v141\n"
      "v_mfma_f32_16x16x32_bf16 v[166:169], a[32:35], v[122:125], v[166:169]\n"
      "v_mul_f32 v134, v102, v134\n"
      "v_mul_f32 v135, v102, v135\n"
      "v_mfma_f32_16x16x32_bf16 v[166:169], a[36:39], v[210:213], v[166:169]\n"
      "v_mul_f32 v136, v102, v136\n"
      "v_mul_f32_e64 v137, v102, v137\n"
      "v_mfma_f32_16x16x32_bf16 v[166:169], a[40:43], v[110:113], v[166:169]\n"
      "ds_read_b128 v[96:99], v7\n"
      "v_cvt_pk_bf16_f32 v146, v146, v147\n"
      "v_mfma_f32_16x16x32_bf16 v[166:169], a[44:47], v[158:161], v[166:169]\n"
      "ds_read_b128 v[114:117], v7 offset:4096\n"
      "v_mul_f32 v148, v102, v148\n"
      "v_mfma_f32_16x16x32_bf16 v[176:179], a[48:51], v[142:145], 0\n"
      "ds_read_b128 v[206:209], v7 offset:8192\n"
      "v_mul_f32 v149, v102, v149\n"
      "v_mfma_f32_16x16x32_bf16 v[176:179], a[52:55], v[218:221], v[176:179]\n"
      "v_cvt_pk_bf16_f32 v199, v200, v201\n"
      "v_mul_f32 v150, v156, v150\n"
      "v_mfma_f32_16x16x32_bf16 v[176:179], a[56:59], v[122:125], v[176:179]\n"
      "ds_read_b128 v[126:129], v7 offset:12288\n"
      "v_mul_f32 v151, v156, v151\n"
      "v_mfma_f32_16x16x32_bf16 v[176:179], a[60:63], v[210:213], v[176:179]\n"
      "ds_read_b128 v[102:105], v7 offset:16384\n"
      "v_mul_f32 v152, v156, v152\n"
      "v_mfma_f32_16x16x32_bf16 v[176:179], a[64:67], v[110:113], v[176:179]\n"
      "v_mul_f32 v153, v156, v153\n"
      "v_cvt_pk_bf16_f32 v106, v106, v107\n"
      "v_mfma_f32_16x16x32_bf16 v[176:179], a[68:71], v[158:161], v[176:179]\n"
      "v_mul_f32 v108, v156, v108\n"
      "v_mul_f32 v109, v156, v109\n"
      "v_mfma_f32_16x16x32_bf16 v[154:157], a[72:75], v[142:145], 0\n"
      "s_waitcnt lgkmcnt(2)\n"
      "v_cvt_pk_bf16_f32 v140, v134, v135\n"
      "v_cvt_pk_bf16_f32 v141, v136, v137\n"
      "v_mfma_f32_16x16x32_bf16 v[154:157], a[76:79], v[218:221], v[154:157]\n"
      "buffer_store_dwordx4 v[96:99], v20, s[24:27], s46 offen sc1\n"
      "v_mfma_f32_16x16x32_bf16 v[154:157], a[80:83], v[122:125], v[154:157]\n"
      "s_waitcnt lgkmcnt(0)\n"
      "v_cvt_pk_bf16_f32 v147, v148, v149\n"
      "v_cvt_pk_bf16_f32 v200, v150, v151\n"
      "v_mfma_f32_16x16x32_bf16 v[154:157], a[84:87], v[210:213], v[154:157]\n"
      "v_cvt_pk_bf16_f32 v201, v152, v153\n"
      "v_cvt_pk_bf16_f32 v107, v108, v109\n"
      "v_mfma_f32_16x16x32_bf16 v[154:157], a[88:91], v[110:113], v[154:157]\n"
      "v_fma_f32 v153, v162, v162, s51\n"
      "v_fmac_f32 v153, v163, v163\n"
      "v_mfma_f32_16x16x32_bf16 v[154:157], a[92:95], v[158:161], v[154:157]\n"
      "v_fmac_f32 v153, v164, v164\n"
      "v_fmac_f32_e64 v153, v165, v165\n"
      "v_mfma_f32_16x16x32_bf16 v[96:99], a[96:99], v[142:145], 0\n"
      "v_fmac_f32 v153, v166, v166\n"
      "v_fmac_f32 v153, v167, v167\n"
      "v_mfma_f32_16x16x32_bf16 v[96:99], a[100:103], v[218:221], v[96:99]\n"
      "v_fmac_f32 v153, v168, v168\n"
      "v_fmac_f32 v153, v169, v169\n"
      "v_mfma_f32_16x16x32_bf16 v[96:99], a[104:107], v[122:125], v[96:99]\n"
      "v_fma_f32 v152, v154, v154, s51\n"
      "v_fmac_f32_e64 v152, v155, v155\n"
      "v_mfma_f32_16x16x32_bf16 v[96:99], a[108:111], v[210:213], v[96:99]\n"
      "buffer_store_dwordx4 v[114:117], v21, s[24:27], s46 offen sc1\n"
      "v_mfma_f32_16x16x32_bf16 v[96:99], a[112:115], v[110:113], v[96:99]\n"
      "v_fmac_f32 v152, v156, v156\n"
      "v_fmac_f32 v152, v157, v157\n"
      "v_mfma_f32_16x16x32_bf16 v[96:99], a[116:119], v[158:161], v[96:99]\n"
      "v_fmac_f32 v153, v176, v176\n"
      "v_fmac_f32 v153, v177, v177\n"
      "v_mfma_f32_16x16x32_bf16 v[148:151], a[120:123], v[142:145], 0\n"
      "v_fmac_f32 v153, v178, v178\n"
      "v_fmac_f32 v153, v179, v179\n"
      "v_mfma_f32_16x16x32_bf16 v[148:151], a[124:127], v[218:221], v[148:151]\n"
      "v_cvt_pk_bf16_f32 v196, v118, v119\n"
      "v_cvt_pk_bf16_f32 v197, v120, v121\n"
      "v_mfma_f32_16x16x32_bf16 v[148:151], a[128:131], v[122:125], v[148:151]\n"
      "v_fmac_f32 v152, v96, v96\n"
      "v_fmac_f32 v152, v97, v97\n"
      "v_mfma_f32_16x16x32_bf16 v[148:151], a[132:135], v[210:213], v[148:151]\n"
      "v_fmac_f32 v152, v98, v98\n"
      "v_fmac_f32 v152, v99, v99\n"
      "v_mfma_f32_16x16x32_bf16 v[148:151], a[136:139], v[110:113], v[148:151]\n"
      "v_cvt_pk_bf16_f32 v130, v130, v131\n"
      "v_cvt_pk_bf16_f32 v131, v132, v133\n"
      "v_mfma_f32_16x16x32_bf16 v[148:151], a[140:143], v[158:161], v[148:151]\n"
      "buffer_store_dwordx4 v[206:209], v22, s[24:27], s46 offen sc1\n"
      "v_mfma_f32_16x16x32_bf16 v[116:119], v[142:145], a[144:147], 0\n"
      "v_cvt_pk_bf16_f32 v132, v202, v203\n"
      "v_cvt_pk_bf16_f32 v133, v204, v205\n"
      "v_mfma_f32_16x16x32_bf16 v[116:119], v[218:221], a[148:151], v[116:119]\n"
      "v_cvt_pk_bf16_f32 v214, v214, v215\n"
      "v_cvt_pk_bf16_f32 v215, v216, v217\n"
      "v_mfma_f32_16x16x32_bf16 v[116:119], v[122:125], a[152:155], v[116:119]\n"
      "v_fmac_f32 v152, v148, v148\n"
      "v_fmac_f32 v152, v149, v149\n"
      "v_mfma_f32_16x16x32_bf16 v[116:119], v[210:213], a[156:159], v[116:119]\n"
      "v_fmac_f32 v152, v150, v150\n"
      "v_fmac_f32 v152, v151, v151\n"
      "v_mfma_f32_16x16x32_bf16 v[116:119], v[110:113], a[160:163], v[116:119]\n"
      "v_cvt_pk_bf16_f32 v216, v170, v171\n"
      "v_permlane16_swap_b32_e64 v153, v152\n"
      "v_mfma_f32_16x16x32_bf16 v[116:119], v[158:161], a[164:167], v[116:119]\n"
      "v_add_f32 v153, v153, v152\n"
      "v_mov_b32 v152, v153\n"
      "v_mfma_f32_16x16x32_bf16 v[202:205], v[142:145], a[168:171], 0\n"
      "v_cvt_pk_bf16_f32 v217, v172, v173\n"
      "v_permlane32_swap_b32 v153, v152\n"
      "v_mfma_f32_16x16x32_bf16 v[202:205], v[218:221], a[172:175], v[202:205]\n"
      "buffer_store_dwordx4 v[126:129], v23, s[24:27], s46 offen sc1\n"
      "v_mfma_f32_16x16x32_bf16 v[202:205], v[122:125], a[176:179], v[202:205]\n"
      "s_nop 0\n"
      "ds_read_b128 v[126:129], v2\n"
      "v_add_f32 v153, v153, v152\n"
      "v_mfma_f32_16x16x32_bf16 v[202:205], v[210:213], a[180:183], v[202:205]\n"
      "ds_read_b128 v[170:173], v2 offset:3072\n"
      "v_rsq_f32 v153, v153\n"
      "v_mfma_f32_16x16x32_bf16 v[202:205], v[110:113], a[184:187], v[202:205]\n"
      "ds_read_b128 v[206:209], v2 offset:6144\n"
      "v_mov_b32_e64 v152, v153\n"
      "v_mfma_f32_16x16x32_bf16 v[202:205], v[158:161], a[188:191], v[202:205]\n"
      "v_cvt_pk_bf16_f32 v76, v116, v117\n"
      "v_permlane16_swap_b32 v153, v152\n"
      "v_mfma_f32_16x16x32_bf16 v[134:137], v[142:145], a[192:195], 0\n"
      "v_mul_f32 v154, v152, v154\n"
      "v_mul_f32_e64 v155, v152, v155\n"
      "v_mfma_f32_16x16x32_bf16 v[134:137], v[218:221], a[196:199], v[134:137]\n"
      "v_cvt_pk_bf16_f32 v154, v154, v155\n"
      "v_mul_f32 v156, v152, v156\n"
      "v_mfma_f32_16x16x32_bf16 v[134:137], v[122:125], a[200:203], v[134:137]\n"
      "s_waitcnt lgkmcnt(2)\n"
      "v_mul_f32 v157, v152, v157\n"
      "buffer_store_dwordx4 v[102:105], v25, s[24:27], s46 offen sc1\n"
      "v_mfma_f32_16x16x32_bf16 v[134:137], v[210:213], a[204:207], v[134:137]\n"
      "s_waitcnt lgkmcnt(0)\n"
      "v_cvt_pk_bf16_f32 v155, v156, v157\n"
      "v_mul_f32 v96, v152, v96\n"
      "v_mfma_f32_16x16x32_bf16 v[134:137], v[110:113], a[208:211], v[134:137]\n"
      "v_mul_f32 v97, v152, v97\n"
      "v_mul_f32 v98, v152, v98\n"
      "v_mfma_f32_16x16x32_bf16 v[134:137], v[158:161], a[212:215], v[134:137]\n"
      "v_mul_f32 v99, v152, v99\n"
      "v_cvt_pk_bf16_f32 v156, v96, v97\n"
      "v_mfma_f32_16x16x32_bf16 v[158:161], a[216:219], v[126:129], 0\n"
      "v_cvt_pk_bf16_f32 v157, v98, v99\n"
      "v_mul_f32_e64 v153, s50, v153\n"
      "v_mfma_f32_16x16x32_bf16 v[158:161], a[220:223], v[170:173], v[158:161]\n"
      "ds_read_b128 v[210:213], v10 offset:1200\n"
      "v_mul_f32 v162, v153, v162\n"
      "v_mfma_f32_16x16x32_bf16 v[158:161], a[224:227], v[206:209], v[158:161]\n"
      "v_mul_f32 v163, v153, v163\n"
      "v_mul_f32 v148, v152, v148\n"
      "v_mfma_f32_16x16x32_bf16 v[96:99], a[240:243], v[126:129], 0\n"
      "v_mul_f32 v149, v152, v149\n"
      "v_cvt_pk_bf16_f32 v162, v162, v163\n"
      "v_mfma_f32_16x16x32_bf16 v[96:99], a[244:247], v[170:173], v[96:99]\n"
      "v_mul_f32 v164, v153, v164\n"
      "v_mul_f32 v165, v153, v165\n"
      "v_mfma_f32_16x16x32_bf16 v[96:99], a[248:251], v[206:209], v[96:99]\n"
      "ds_read_b64 v[222:223], v10 offset:1192\n"
      "v_mul_f32_e64 v176, v153, v176\n"
      "v_mfma_f32_16x16x32_bf16 v[114:117], v[232:235], v[126:129], 0\n"
      "ds_read_b128 v[126:129], v2 offset:9216\n"
      "v_mul_f32_e64 v177, v153, v177\n"
      "v_mfma_f32_16x16x32_bf16 v[114:117], v[236:239], v[170:173], v[114:117]\n"
      "ds_read_b128 v[142:145], v2 offset:12288\n"
      "v_cvt_pk_bf16_f32 v148, v148, v149\n"
      "v_mfma_f32_16x16x32_bf16 v[114:117], v[240:243], v[206:209], v[114:117]\n"
      "ds_read_b128 v[110:113], v2 offset:15360\n"
      "v_mul_f32 v150, v152, v150\n"
      "v_mfma_f32_16x16x32_bf16 v[102:105], v[186:189], v[182:185], v[40:43]\n"
      "s_waitcnt lgkmcnt(4)\n"
      "v_mul_f32_e64 v151, v152, v151\n"
      "v_cvt_pk_bf16_f32 v163, v164, v165\n"
      "v_mfma_f32_16x16x32_bf16 v[120:123], v[138:141], v[182:185], v[44:47]\n"
      "v_mul_f32 v166, v153, v166\n"
      "v_mul_f32 v167, v153, v167\n"
      "v_mfma_f32_16x16x32_bf16 v[206:209], v[154:157], v[182:185], v[48:51]\n"
      "s_waitcnt lgkmcnt(3)\n"
      "v_mul_f32 v168, v153, v168\n"
      "v_mul_f32 v169, v153, v169\n"
      "v_mfma_f32_16x16x32_bf16 v[170:173], v[210:213], v[182:185], v[24:27]\n"
      "s_waitcnt lgkmcnt(2)\n"
      "v_cvt_pk_bf16_f32 v176, v176, v177\n"
      "v_mul_f32_e64 v178, v153, v178\n"
      "v_mfma_f32_16x16x16_bf16 v[102:105], v[174:175], v[100:101], v[102:105]\n"
      "v_mul_f32_e64 v179, v153, v179\n"
      "v_cvt_pk_bf16_f32 v149, v150, v151\n"
      "v_cvt_pk_bf16_f32 v164, v166, v167\n"
      "v_mfma_f32_16x16x16_bf16 v[120:123], v[146:147], v[100:101], v[120:123]\n"
      "v_cvt_pk_bf16_f32 v165, v168, v169\n"
      "v_cvt_pk_bf16_f32 v177, v178, v179\n"
      "v_cvt_pk_bf16_f32 v77, v118, v119\n"
      "v_mfma_f32_16x16x16_bf16 v[206:209], v[148:149], v[100:101], v[206:209]\n"
      "v_max3_f32 v119, v102, v103, v104\n"
      "v_cvt_pk_bf16_f32 v80, v202, v203\n"
      "v_cvt_pk_bf16_f32 v81, v204, v205\n"
      "v_mfma_f32_16x16x16_bf16 v[170:173], v[222:223], v[100:101], v[170:173]\n"
      "v_max3_f32 v150, v105, v120, v121\n"
      "v_max3_f32 v119, v119, v122, v123\n"
      "v_cvt_pk_bf16_f32 v84, v134, v135\n"
      "v_mfma_f32_16x16x32_bf16 v[158:161], a[228:231], v[126:129], v[158:161]\n"
      "s_waitcnt lgkmcnt(0)\n"
      "v_max3_f32 v150, v150, v206, v207\n"
      "v_max3_f32 v119, v119, v208, v209\n"
      "v_mfma_f32_16x16x32_bf16 v[158:161], a[232:235], v[142:145], v[158:161]\n"
      "v_max3_f32 v119, v119, v150, v170\n"
      "v_mov_b32 v150, v119\n"
      "v_mfma_f32_16x16x32_bf16 v[158:161], a[236:239], v[110:113], v[158:161]\n"
      "v_cvt_pk_bf16_f32 v85, v136, v137\n"
      "v_permlane16_swap_b32 v119, v150\n"
      "v_mfma_f32_16x16x32_bf16 v[96:99], a[252:255], v[126:129], v[96:99]\n"
      "v_max_f32 v119, v119, v150\n"
      "v_mov_b32 v150, v119\n"
      "v_mfma_f32_16x16x32_bf16 v[96:99], v[224:227], v[142:145], v[96:99]\n"
      "s_waitcnt vmcnt(6)\n"
      "v_or3_b32 v190, v190, v191, v192\n"
      "v_permlane32_swap_b32 v119, v150\n"
      "v_mfma_f32_16x16x32_bf16 v[96:99], v[228:231], v[110:113], v[96:99]\n"
      "v_max_f32 v119, v119, v150\n"
      "v_sub_f32 v102, v102, v119\n"
      "v_mfma_f32_16x16x32_bf16 v[114:117], v[244:247], v[126:129], v[114:117]\n"
      "ds_read_b128 v[150:153], v9 offset:4608\n"
      "v_sub_f32 v103, v103, v119\n"
      "v_mfma_f32_16x16x32_bf16 v[114:117], v[248:251], v[142:145], v[114:117]\n"
      "ds_read_b128 v[142:145], v2 offset:256\n"
      "v_sub_f32 v104, v104, v119\n"
      "v_mfma_f32_16x16x32_bf16 v[114:117], v[252:255], v[110:113], v[114:117]\n"
      "ds_read_b128 v[110:113], v2 offset:3328\n"
      "v_sub_f32 v105, v105, v119\n"
      "v_mfma_f32_16x16x32_bf16 v[182:185], v[186:189], v[198:201], v[52:55]\n"
      "ds_read_b128 v[134:137], v2 offset:6400\n"
      "v_exp_f32 v102, v102\n"
      "v_mfma_f32_16x16x32_bf16 v[218:221], v[138:141], v[198:201], v[56:59]\n"
      "v_exp_f32 v103, v103\n"
      "v_mfma_f32_16x16x32_bf16 v[202:205], v[154:157], v[198:201], v[60:63]\n"
      "v_sub_f32 v120, v120, v119\n"
      "v_sub_f32 v121, v121, v119\n"
      "v_mfma_f32_16x16x32_bf16 v[124:127], v[210:213], v[198:201], v[26:29]\n"
      "v_sub_f32 v122, v122, v119\n"
      "v_sub_f32 v123, v123, v119\n"
      "v_mfma_f32_16x16x16_bf16 v[182:185], v[174:175], v[106:107], v[182:185]\n"
      "v_exp_f32 v104, v104\n"
      "v_exp_f32 v105, v105\n"
      "v_mfma_f32_16x16x16_bf16 v[218:221], v[146:147], v[106:107], v[218:221]\n"
      "v_exp_f32 v120, v120\n"
      "v_exp_f32 v121, v121\n"
      "v_mfma_f32_16x16x16_bf16 v[202:205], v[148:149], v[106:107], v[202:205]\n"
      "v_exp_f32 v122, v122\n"
      "v_exp_f32 v123, v123\n"
      "v_mfma_f32_16x16x16_bf16 v[124:127], v[222:223], v[106:107], v[124:127]\n"
      "v_cvt_pk_bf16_f32 v102, v102, v103\n"
      "v_cvt_pk_bf16_f32 v103, v104, v105\n"
      "v_cvt_pk_bf16_f32 v104, v120, v121\n"
      "s_waitcnt lgkmcnt(0)\n"
      "v_mfma_f32_16x16x32_bf16 v[178:181], a[216:219], v[142:145], 0\n"
      "v_cvt_pk_bf16_f32 v105, v122, v123\n"
      "v_sub_f32 v206, v206, v119\n"
      "v_mfma_f32_16x16x32_bf16 v[178:181], a[220:223], v[110:113], v[178:181]\n"
      "ds_read_u16 v78, v11 offset:1168\n"
      "v_sub_f32 v207, v207, v119\n"
      "v_mfma_f32_16x16x32_bf16 v[178:181], a[224:227], v[134:137], v[178:181]\n"
      "v_sub_f32 v208, v208, v119\n"
      "v_sub_f32 v209, v209, v119\n"
      "v_mfma_f32_16x16x32_bf16 v[166:169], a[240:243], v[142:145], 0\n"
      "ds_read_u16 v82, v11 offset:1200\n"
      "v_sub_f32 v170, v170, v119\n"
      "v_mfma_f32_16x16x32_bf16 v[166:169], a[244:247], v[110:113], v[166:169]\n"
      "v_exp_f32 v206, v206\n"
      "v_mfma_f32_16x16x32_bf16 v[166:169], a[248:251], v[134:137], v[166:169]\n"
      "ds_read_u16 v86, v11 offset:1232\n"
      "v_exp_f32 v207, v207\n"
      "v_mfma_f32_16x16x32_bf16 v[126:129], v[232:235], v[142:145], 0\n"
      "v_exp_f32 v208, v208\n"
      "v_mfma_f32_16x16x32_bf16 v[126:129], v[236:239], v[110:113], v[126:129]\n"
      "s_waitcnt lgkmcnt(2)\n"
      "v_exp_f32 v209, v209\n"
      "v_mfma_f32_16x16x32_bf16 v[126:129], v[240:243], v[134:137], v[126:129]\n"
      "v_exp_f32 v170, v170\n"
      "v_mfma_f32_16x16x32_bf16 v[108:111], v[32:35], v[102:105], 0\n"
      "s_waitcnt lgkmcnt(1)\n"
      "v_cvt_pk_bf16_f32 v88, v206, v207\n"
      "v_cvt_pk_bf16_f32 v89, v208, v209\n"
      "v_cvt_pk_bf16_f32 v90, v170, 0\n"
      "v_max3_f32 v123, v182, v183, v184\n"
      "v_max3_f32 v145, v185, v218, v219\n"
      "v_mfma_f32_16x16x32_bf16 v[108:111], v[32:35], v[88:91], v[108:111]\n"
      "s_waitcnt lgkmcnt(0)\n"
      "v_max3_f32 v123, v123, v220, v221\n"
      "v_max3_f32 v145, v145, v202, v203\n"
      "v_mfma_f32_16x16x32_bf16 v[118:121], v[194:197], v[102:105], 0\n"
      "v_max3_f32 v123, v123, v204, v205\n"
      "v_max3_f32 v123, v123, v145, v124\n"
      "v_mfma_f32_16x16x32_bf16 v[118:121], v[76:79], v[88:91], v[118:121]\n"
      "v_mov_b32_e64 v101, v123\n"
      "v_fma_f32 v170, v158, v158, s52\n"
      "v_mfma_f32_16x16x32_bf16 v[142:145], v[130:133], v[102:105], 0\n"
      "v_permlane16_swap_b32 v123, v101\n"
      "v_mfma_f32_16x16x32_bf16 v[142:145], v[80:83], v[88:91], v[142:145]\n"
      "ds_read_b128 v[110:113], v2 offset:9472\n"
      "v_max_f32 v123, v123, v101\n"
      "v_mfma_f32_16x16x32_bf16 v[134:137], v[214:217], v[102:105], 0\n"
      "ds_read_b128 v[104:107], v2 offset:12544\n"
      "v_mov_b32_e64 v101, v123\n"
      "v_mfma_f32_16x16x32_bf16 v[134:137], v[84:87], v[88:91], v[134:137]\n"
      "ds_read_b128 v[206:209], v2 offset:15616\n"
      "v_permlane32_swap_b32 v123, v101\n"
      "v_max_f32 v123, v123, v101\n"
      "v_sub_f32 v182, v182, v123\n"
      "v_sub_f32 v183, v183, v123\n"
      "v_sub_f32 v184, v184, v123\n"
      "v_sub_f32 v185, v185, v123\n"
      "v_exp_f32 v182, v182\n"
      "v_exp_f32 v183, v183\n"
      "v_sub_f32 v218, v218, v123\n"
      "v_sub_f32 v219, v219, v123\n"
      "v_sub_f32 v220, v220, v123\n"
      "v_sub_f32 v221, v221, v123\n"
      "v_exp_f32 v184, v184\n"
      "v_exp_f32 v185, v185\n"
      "v_exp_f32 v218, v218\n"
      "v_exp_f32 v219, v219\n"
      "v_exp_f32 v220, v220\n"
      "v_exp_f32 v221, v221\n"
      "v_cvt_pk_bf16_f32 v182, v182, v183\n"
      "v_cvt_pk_bf16_f32 v183, v184, v185\n"
      "v_cvt_pk_bf16_f32 v184, v218, v219\n"
      "v_cvt_pk_bf16_f32 v185, v220, v221\n"
      "v_mfma_f32_16x16x32_bf16 v[100:103], v[186:189], v[162:165], v[64:67]\n"
      "v_sub_f32 v202, v202, v123\n"
      "v_sub_f32 v203, v203, v123\n"
      "v_sub_f32 v204, v204, v123\n"
      "v_sub_f32 v205, v205, v123\n"
      "v_sub_f32 v124, v124, v123\n"
      "v_exp_f32 v202, v202\n"
      "v_exp_f32 v203, v203\n"
      "v_exp_f32 v204, v204\n"
      "v_exp_f32_e64 v205, v205\n"
      "v_exp_f32 v124, v124\n"
      "v_cvt_pk_bf16_f32 v92, v202, v203\n"
      "v_cvt_pk_bf16_f32 v93, v204, v205\n"
      "v_mfma_f32_16x16x32_bf16 v[202:205], v[138:141], v[162:165], v[68:71]\n"
      "s_waitcnt lgkmcnt(0)\n"
      "v_cvt_pk_bf16_f32 v94, v124, 0\n"
      "v_fmac_f32 v170, v159, v159\n"
      "v_fmac_f32 v170, v160, v160\n"
      "v_fmac_f32 v170, v161, v161\n"
      "v_fmac_f32 v170, v96, v96\n"
      "v_fmac_f32 v170, v97, v97\n"
      "v_fmac_f32 v170, v98, v98\n"
      "v_fmac_f32 v170, v99, v99\n"
      "v_fmac_f32 v170, v114, v114\n"
      "v_fmac_f32 v170, v115, v115\n"
      "v_fmac_f32 v170, v116, v116\n"
      "v_fmac_f32 v170, v117, v117\n"
      "v_rcp_f32_e64 v108, v108\n"
      "s_waitcnt vmcnt(5)\n"
      "v_or3_b32 v190, v190, v193, v8\n"
      "v_mul_f32 v118, v108, v118\n"
      "v_mul_f32 v119, v108, v119\n"
      "v_cvt_pk_bf16_f32 v118, v118, v119\n"
      "v_mul_f32 v120, v108, v120\n"
      "v_mul_f32 v121, v108, v121\n"
      "v_cvt_pk_bf16_f32 v119, v120, v121\n"
      "v_mul_f32 v142, v108, v142\n"
      "v_mul_f32 v143, v108, v143\n"
      "v_mul_f32 v144, v108, v144\n"
      "v_mul_f32 v145, v108, v145\n"
      "v_cvt_pk_bf16_f32 v120, v142, v143\n"
      "v_mfma_f32_16x16x32_bf16 v[218:221], v[154:157], v[162:165], v[72:75]\n"
      "v_cvt_pk_bf16_f32 v121, v144, v145\n"
      "v_mul_f32_e64 v134, v108, v134\n"
      "v_mfma_f32_16x16x32_bf16 v[142:145], v[210:213], v[162:165], v[28:31]\n"
      "ds_write_b128 v3, v[118:121] offset:18432\n"
      "v_mfma_f32_16x16x16_bf16 v[100:103], v[174:175], v[176:177], v[100:103]\n"
      "v_mul_f32_e64 v135, v108, v135\n"
      "v_cvt_pk_bf16_f32 v134, v134, v135\n"
      "v_mul_f32_e64 v136, v108, v136\n"
      "v_mfma_f32_16x16x16_bf16 v[202:205], v[146:147], v[176:177], v[202:205]\n"
      "v_mul_f32_e64 v137, v108, v137\n"
      "v_cvt_pk_bf16_f32 v135, v136, v137\n"
      "v_and_b32 v190, 0x7fff7fff, v190\n"
      "v_mfma_f32_16x16x16_bf16 v[218:221], v[148:149], v[176:177], v[218:221]\n"
      "v_max3_f32 v88, v100, v101, v102\n"
      "v_readlane_b32 s45, v37, s43\n"
      "v_readlane_b32 s46, v37, s42\n"
      "v_mfma_f32_16x16x16_bf16 v[142:145], v[222:223], v[176:177], v[142:145]\n"
      "v_max3_f32 v108, v103, v202, v203\n"
      "v_max3_f32 v88, v88, v204, v205\n"
      "v_or3_b32 v150, v150, v151, v152\n"
      "v_mfma_f32_16x16x32_bf16 v[178:181], a[228:231], v[110:113], v[178:181]\n"
      "ds_write_b64 v4, v[134:135] offset:18432\n"
      "v_mfma_f32_16x16x32_bf16 v[178:181], a[232:235], v[104:107], v[178:181]\n"
      "s_or_b32 s95, s45, s46\n"
      "v_max3_f32 v108, v108, v218, v219\n"
      "v_max3_f32 v88, v88, v220, v221\n"
      "v_mfma_f32_16x16x32_bf16 v[178:181], a[236:239], v[206:209], v[178:181]\n"
      "s_or_b32 s95, s95, s48\n"
      "v_max3_f32 v88, v88, v108, v142\n"
      "v_mov_b32_e64 v201, v88\n"
      "v_mfma_f32_16x16x32_bf16 v[166:169], a[252:255], v[110:113], v[166:169]\n"
      "ds_write_b32 v31, v190 offset:5632\n"
      "v_mfma_f32_16x16x32_bf16 v[166:169], v[224:227], v[104:107], v[166:169]\n"
      "s_cmp_ge_u32 s95, 0x40000000\n"
      "s_cselect_b32 s94, -1, -2\n"
      "v_fma_f32 v89, v178, v178, s52\n"
      "v_mfma_f32_16x16x32_bf16 v[166:169], v[228:231], v[206:209], v[166:169]\n"
      "s_cmp_gt_u32 s42, s44\n"
      "s_cselect_b32 s95, s94, s95\n"
      "v_fmac_f32 v89, v179, v179\n"
      "v_mfma_f32_16x16x32_bf16 v[126:129], v[244:247], v[110:113], v[126:129]\n"
      "ds_read_b128 v[108:111], v10 offset:1168\n"
      "v_fmac_f32 v89, v180, v180\n"
      "v_mfma_f32_16x16x32_bf16 v[126:129], v[248:251], v[104:107], v[126:129]\n"
      "s_add_u32 s42, s42, 1\n"
      "v_fmac_f32 v89, v181, v181\n"
      "v_fmac_f32 v89, v166, v166\n"
      "v_mfma_f32_16x16x32_bf16 v[126:129], v[252:255], v[206:209], v[126:129]\n"
      "v_fmac_f32 v89, v167, v167\n"
      "v_fmac_f32 v89, v168, v168\n"
      "v_mfma_f32_16x16x32_bf16 v[118:121], v[32:35], v[182:185], 0\n"
      "v_fmac_f32 v89, v169, v169\n"
      "v_permlane16_swap_b32_e64 v88, v201\n"
      "v_mfma_f32_16x16x32_bf16 v[118:121], v[32:35], v[92:95], v[118:121]\n"
      "ds_read_b64 v[144:145], v10 offset:1184\n"
      "v_max_f32 v88, v88, v201\n"
      "v_mfma_f32_16x16x32_bf16 v[104:107], v[194:197], v[182:185], 0\n"
      "v_fmac_f32 v89, v126, v126\n"
      "v_fmac_f32_e64 v89, v127, v127\n"
      "v_mfma_f32_16x16x32_bf16 v[104:107], v[76:79], v[92:95], v[104:107]\n"
      "s_waitcnt lgkmcnt(1)\n"
      "v_fmac_f32 v89, v128, v128\n"
      "v_fmac_f32 v89, v129, v129\n"
      "v_mfma_f32_16x16x32_bf16 v[190:193], v[130:133], v[182:185], 0\n"
      "v_mov_b32 v201, v88\n"
      "v_permlane16_swap_b32 v170, v89\n"
      "v_mfma_f32_16x16x32_bf16 v[190:193], v[80:83], v[92:95], v[190:193]\n"
      "ds_read_b128 v[120:123], v2 offset:512\n"
      "v_add_f32 v170, v170, v89\n"
      "v_mfma_f32_16x16x32_bf16 v[206:209], v[214:217], v[182:185], 0\n"
      "ds_read_b128 v[182:185], v2 offset:3584\n"
      "v_rcp_f32 v118, v118\n"
      "v_mfma_f32_16x16x32_bf16 v[206:209], v[84:87], v[92:95], v[206:209]\n"
      "ds_read_b128 v[134:137], v2 offset:6656\n"
      "v_permlane32_swap_b32 v88, v201\n"
      "v_mfma_f32_16x16x32_bf16 v[162:165], v[108:111], v[186:189], v[30:33]\n"
      "s_waitcnt lgkmcnt(3)\n"
      "v_mul_f32 v104, v118, v104\n"
      "v_mul_f32 v105, v118, v105\n"
      "v_mfma_f32_16x16x32_bf16 v[186:189], v[108:111], v[138:141], v[30:33]\n"
      "v_max_f32 v88, v88, v201\n"
      "v_cvt_pk_bf16_f32 v104, v104, v105\n"
      "v_mfma_f32_16x16x32_bf16 v[138:141], v[108:111], v[154:157], v[30:33]\n"
      "v_mul_f32 v106, v118, v106\n"
      "v_mul_f32 v107, v118, v107\n"
      "v_mfma_f32_16x16x32_bf16 v[198:201], v[108:111], v[210:213], v[30:33]\n"
      "s_waitcnt lgkmcnt(2)\n"
      "v_sub_f32 v100, v100, v88\n"
      "v_sub_f32 v101, v101, v88\n"
      "v_mfma_f32_16x16x16_bf16 v[162:165], v[144:145], v[174:175], v[162:165]\n"
      "v_cvt_pk_bf16_f32 v105, v106, v107\n"
      "v_mul_f32 v190, v118, v190\n"
      "v_mul_f32 v191, v118, v191\n"
      "v_mfma_f32_16x16x16_bf16 v[186:189], v[144:145], v[146:147], v[186:189]\n"
      "v_mul_f32 v192, v118, v192\n"
      "v_mul_f32 v193, v118, v193\n"
      "v_sub_f32 v102, v102, v88\n"
      "v_mfma_f32_16x16x16_bf16 v[138:141], v[144:145], v[148:149], v[138:141]\n"
      "v_sub_f32 v103, v103, v88\n"
      "v_exp_f32 v100, v100\n"
      "v_mfma_f32_16x16x16_bf16 v[198:201], v[144:145], v[222:223], v[198:201]\n"
      "v_exp_f32 v101, v101\n"
      "v_cndmask_b32 v162, v162, v186, s[58:59]\n"
      "v_mfma_f32_16x16x32_bf16 v[144:147], a[216:219], v[120:123], 0\n"
      "s_waitcnt lgkmcnt(0)\n"
      "v_cvt_pk_bf16_f32 v106, v190, v191\n"
      "v_cndmask_b32 v162, v162, v138, s[60:61]\n"
      "v_mfma_f32_16x16x32_bf16 v[144:147], a[220:223], v[182:185], v[144:147]\n"
      "ds_read_b128 v[210:213], v2 offset:9728\n"
      "v_cndmask_b32 v162, v162, v198, s[62:63]\n"
      "v_mfma_f32_16x16x32_bf16 v[144:147], a[224:227], v[134:137], v[144:147]\n"
      "ds_read_b128 v[188:191], v2 offset:12800\n"
      "v_max_f32_dpp v186, v162, v162 row_ror:8 row_mask:0xf bank_mask:0xf\n"
      "v_mfma_f32_16x16x32_bf16 v[108:111], a[240:243], v[120:123], 0\n"
      "ds_read_b128 v[172:175], v2 offset:15872\n"
      "v_max_f32_dpp v186, v186, v186 row_ror:4 row_mask:0xf bank_mask:0xf\n"
      "v_mfma_f32_16x16x32_bf16 v[108:111], a[244:247], v[182:185], v[108:111]\n"
      "v_cvt_pk_bf16_f32 v107, v192, v193\n"
      "v_max_f32_dpp v186, v186, v186 row_ror:2 row_mask:0xf bank_mask:0xf\n"
      "v_mfma_f32_16x16x32_bf16 v[108:111], a[248:251], v[134:137], v[108:111]\n"
      "ds_write_b128 v3, v[104:107] offset:18688\n"
      "v_mfma_f32_16x16x32_bf16 v[138:141], v[232:235], v[120:123], 0\n"
      "s_waitcnt lgkmcnt(3)\n"
      "v_max_f32_dpp v186, v186, v186 row_ror:1 row_mask:0xf bank_mask:0xf\n"
      "v_sub_f32 v202, v202, v88\n"
      "v_mfma_f32_16x16x32_bf16 v[138:141], v[236:239], v[182:185], v[138:141]\n"
      "s_waitcnt lgkmcnt(2)\n"
      "v_max_f32_dpp v186, v186, v186 row_bcast:15 row_mask:0xa bank_mask:0xf\n"
      "v_sub_f32 v203, v203, v88\n"
      "v_mfma_f32_16x16x32_bf16 v[138:141], v[240:243], v[134:137], v[138:141]\n"
      "s_waitcnt lgkmcnt(1)\n"
      "v_max_f32_dpp v186, v186, v186 row_bcast:31 row_mask:0xc bank_mask:0xf\n"
      "v_sub_f32 v204, v204, v88\n"
      "v_mfma_f32_16x16x32_bf16 v[144:147], a[228:231], v[210:213], v[144:147]\n"
      "v_readlane_b32 s72, v186, 63\n"
      "v_sub_f32 v205, v205, v88\n"
      "v_mfma_f32_16x16x32_bf16 v[144:147], a[232:235], v[188:191], v[144:147]\n"
      "v_subrev_f32 v162, s72, v162\n"
      "v_exp_f32 v102, v102\n"
      "v_mfma_f32_16x16x32_bf16 v[144:147], a[236:239], v[172:175], v[144:147]\n"
      "v_exp_f32 v103, v103\n"
      "v_mfma_f32_16x16x32_bf16 v[108:111], a[252:255], v[210:213], v[108:111]\n"
      "v_mul_f32 v206, v118, v206\n"
      "v_mul_f32 v207, v118, v207\n"
      "v_mfma_f32_16x16x32_bf16 v[108:111], v[224:227], v[188:191], v[108:111]\n"
      "v_exp_f32_e64 v162, v162\n"
      "v_mfma_f32_16x16x32_bf16 v[108:111], v[228:231], v[172:175], v[108:111]\n"
      "v_exp_f32 v202, v202\n"
      "v_mfma_f32_16x16x32_bf16 v[138:141], v[244:247], v[210:213], v[138:141]\n"
      "v_fma_f32 v212, v144, v144, s52\n"
      "v_fmac_f32 v212, v145, v145\n"
      "v_mfma_f32_16x16x32_bf16 v[138:141], v[248:251], v[188:191], v[138:141]\n"
      "v_fmac_f32 v212, v146, v146\n"
      "v_fmac_f32 v212, v147, v147\n"
      "v_mfma_f32_16x16x32_bf16 v[138:141], v[252:255], v[172:175], v[138:141]\n"
      "v_fmac_f32 v212, v108, v108\n"
      "v_fmac_f32 v212, v109, v109\n"
      "v_fmac_f32 v212, v110, v110\n"
      "v_fmac_f32 v212, v111, v111\n"
      "v_exp_f32 v203, v203\n"
      "v_exp_f32 v204, v204\n"
      "v_exp_f32 v205, v205\n"
      "v_cvt_pk_bf16_f32 v206, v206, v207\n"
      "v_fmac_f32 v212, v138, v138\n"
      "v_fmac_f32 v212, v139, v139\n"
      "v_fmac_f32 v212, v140, v140\n"
      "v_fmac_f32 v212, v141, v141\n"
      "v_mov_b32 v89, v212\n"
      "v_mul_f32 v208, v118, v208\n"
      "v_mul_f32 v209, v118, v209\n"
      "v_permlane16_swap_b32 v212, v89\n"
      "v_add_f32 v212, v212, v89\n"
      "v_cvt_pk_bf16_f32 v100, v100, v101\n"
      "v_cvt_pk_bf16_f32 v207, v208, v209\n"
      "v_permlane32_swap_b32_e64 v170, v212\n"
      "v_cvt_pk_bf16_f32 v101, v102, v103\n"
      "v_cvt_pk_bf16_f32 v162, v162, v162\n"
      "v_add_f32 v170, v170, v212\n"
      "v_cvt_pk_bf16_f32 v102, v202, v203\n"
      "v_cvt_pk_bf16_f32 v103, v204, v205\n"
      "v_sub_f32 v218, v218, v88\n"
      "v_sub_f32 v219, v219, v88\n"
      "v_mfma_f32_16x16x32_bf16 v[202:205], v[32:35], v[100:103], 0\n"
      "ds_write_b64 v4, v[206:207] offset:18688\n"
      "v_sub_f32 v220, v220, v88\n"
      "v_sub_f32 v221, v221, v88\n"
      "v_sub_f32 v142, v142, v88\n"
      "v_exp_f32 v218, v218\n"
      "v_exp_f32 v219, v219\n"
      "v_exp_f32 v220, v220\n"
      "v_exp_f32 v221, v221\n"
      "v_exp_f32_e64 v142, v142\n"
      "v_cvt_pk_bf16_f32 v88, v218, v219\n"
      "v_cvt_pk_bf16_f32 v89, v220, v221\n"
      "v_cvt_pk_bf16_f32 v90, v142, 0\n"
      "v_or_b32_e64 v150, v150, v153\n"
      "v_cmp_ne_u32 s[56:57], 0, v150\n"
      "v_mfma_f32_16x16x32_bf16 v[202:205], v[32:35], v[88:91], v[202:205]\n"
      "ds_write_b16 v13, v162\n"
      "v_mfma_f32_16x16x32_bf16 v[104:107], v[194:197], v[100:103], 0\n"
      "ds_read_b128 v[210:213], v14\n"
      "v_readlane_b32 s47, v38, s43\n"
      "v_mfma_f32_16x16x32_bf16 v[104:107], v[76:79], v[88:91], v[104:107]\n"
      "ds_write_b32 v31, v170 offset:1024\n"
      "v_mfma_f32_16x16x32_bf16 v[184:187], v[130:133], v[100:103], 0\n"
      "ds_read_b128 v[118:121], v14 offset:64\n"
      "v_rcp_f32_e64 v202, v202\n"
      "v_mfma_f32_16x16x32_bf16 v[184:187], v[80:83], v[88:91], v[184:187]\n"
      "s_cmp_ge_u32 s95, 0x40000000\n"
      "s_cselect_b64 vcc, -1, s[56:57]\n"
      "v_add_u32 v10, 2336, v10\n"
      "v_mfma_f32_16x16x32_bf16 v[204:207], v[214:217], v[100:103], 0\n"
      "s_and_b32 s47, s47, -4\n"
      "v_mul_f32 v104, v202, v104\n"
      "v_mul_f32_e64 v105, v202, v105\n"
      "v_mfma_f32_16x16x32_bf16 v[204:207], v[84:87], v[88:91], v[204:207]\n"
      "s_add_u32 s43, s43, 1\n"
      "v_cvt_pk_bf16_f32 v104, v104, v105\n"
      "v_mul_f32 v106, v202, v106\n"
      "v_mul_f32_e64 v107, v202, v107\n"
      "v_cvt_pk_bf16_f32 v105, v106, v107\n"
      "v_mul_f32 v184, v202, v184\n"
      "s_waitcnt lgkmcnt(2)\n"
      "v_mfma_f32_16x16x32_bf16 v[188:191], v[210:213], v[32:35], 0\n"
      "v_mul_f32 v185, v202, v185\n"
      "v_mul_f32 v186, v202, v186\n"
      "v_mul_f32 v187, v202, v187\n"
      "v_cvt_pk_bf16_f32 v106, v184, v185\n"
      "v_cvt_pk_bf16_f32 v107, v186, v187\n"
      "s_waitcnt lgkmcnt(0)\n"
      "v_mfma_f32_16x16x32_bf16 v[188:191], v[118:121], v[32:35], v[188:191]\n"
      "ds_write_b128 v3, v[104:107] offset:18944\n"
      "v_mul_f32 v204, v202, v204\n"
      "v_mul_f32 v205, v202, v205\n"
      "v_cvt_pk_bf16_f32 v204, v204, v205\n"
      "v_mul_f32_e64 v206, v202, v206\n"
      "v_mul_f32 v207, v202, v207\n"
      "v_cvt_pk_bf16_f32 v205, v206, v207\n"
      "v_add_u32 v11, 2336, v11\n"
      "ds_write_b64 v4, v[204:205] offset:18944\n"
      "s_waitcnt lgkmcnt(0)\n"
      "s_barrier\n"
      "s_cbranch_vccnz L_disp0_%=\n"
      "L_max0_%=:\n"
      "v_mfma_f32_16x16x32_bf16 v[202:205], v[210:213], v[194:197], 0\n"
      "ds_read_b128 v[184:187], v0\n"
      "ds_read_b128 v[196:199], v9 offset:1024\n"
      "v_mfma_f32_16x16x32_bf16 v[202:205], v[118:121], v[76:79], v[202:205]\n"
      "ds_read_b128 v[162:165], v1\n"
      "s_add_u32 m0, s64, 18432\n"
      "v_mfma_f32_16x16x32_bf16 v[104:107], v[210:213], v[130:133], 0\n"
      "ds_read_b128 v[218:221], v0 offset:6144\n"
      "buffer_load_dwordx4 v16, s[16:19], s45 offen  lds\n"
      "v_mfma_f32_16x16x32_bf16 v[104:107], v[118:121], v[80:83], v[104:107]\n"
      "s_add_u32 m0, s64, 22528\n"
      "v_mfma_f32_16x16x32_bf16 v[174:177], v[210:213], v[214:217], 0\n"
      "v_mfma_f32_16x16x32_bf16 v[174:177], v[118:121], v[84:87], v[174:177]\n"
      "s_waitcnt lgkmcnt(3)\n"
      "v_mov_b32_dpp v188, v188 quad_perm:[0,0,0,0] row_mask:0xf bank_mask:0xf\n"
      "v_rcp_f32 v188, v188\n"
      "v_mfma_f32_16x16x32_bf16 v[170:173], a[0:3], v[184:187], 0\n"
      "buffer_load_dwordx4 v17, s[16:19], s45 offen  lds\n"
      "v_mfma_f32_16x16x32_bf16 v[210:213], a[24:27], v[184:187], 0\n"
      "s_add_u32 m0, s64, 26624\n"
      "v_cndmask_b32 v202, v202, v104, s[58:59]\n"
      "v_cndmask_b32 v202, v202, v174, s[60:61]\n"
      "v_mfma_f32_16x16x32_bf16 v[100:103], a[48:51], v[184:187], 0\n"
      "s_waitcnt lgkmcnt(0)\n"
      "v_mul_f32 v202, v188, v202\n"
      "v_cvt_pk_bf16_f32 v202, v202, v202\n"
      "v_mfma_f32_16x16x32_bf16 v[150:153], a[72:75], v[184:187], 0\n"
      "ds_write_b16 v12, v202 offset:16\n"
      "v_mfma_f32_16x16x32_bf16 v[202:205], a[96:99], v[184:187], 0\n"
      "buffer_load_dwordx4 v18, s[16:19], s45 offen  lds\n"
      "v_mfma_f32_16x16x32_bf16 v[118:121], a[120:123], v[184:187], 0\n"
      "s_add_u32 m0, s64, 30720\n"
      "v_add_f32 v196, v196, v197\n"
      "v_add_f32 v198, v198, v199\n"
      "v_mfma_f32_16x16x32_bf16 v[170:173], a[4:7], v[162:165], v[170:173]\n"
      "v_add_f32 v196, v196, v198\n"
      "v_rsq_f32 v196, v196\n"
      "v_mfma_f32_16x16x32_bf16 v[170:173], a[8:11], v[218:221], v[170:173]\n"
      "ds_read_b128 v[174:177], v1 offset:6144\n"
      "v_mul_f32 v196, 0x415db3d7, v196\n"
      "v_mfma_f32_16x16x32_bf16 v[210:213], a[28:31], v[162:165], v[210:213]\n"
      "ds_read_b128 v[206:209], v0 offset:12288\n"
      "v_mov_b32 v198, v196\n"
      "v_mfma_f32_16x16x32_bf16 v[210:213], a[32:35], v[218:221], v[210:213]\n"
      "ds_read_b128 v[154:157], v1 offset:12288\n"
      "v_permlane32_swap_b32 v196, v198\n"
      "v_mfma_f32_16x16x32_bf16 v[100:103], a[52:55], v[162:165], v[100:103]\n"
      "buffer_load_dwordx4 v19, s[16:19], s45 offen  lds\n"
      "v_mfma_f32_16x16x32_bf16 v[100:103], a[56:59], v[218:221], v[100:103]\n"
      "s_add_u32 m0, s65, 34816\n"
      "v_mov_b32 v197, v196\n"
      "v_mul_f32 v144, v198, v144\n"
      "v_mfma_f32_16x16x32_bf16 v[150:153], a[76:79], v[162:165], v[150:153]\n"
      "v_permlane16_swap_b32 v196, v197\n"
      "v_mfma_f32_16x16x32_bf16 v[150:153], a[80:83], v[218:221], v[150:153]\n"
      "s_waitcnt lgkmcnt(2)\n"
      "v_mul_f32 v158, v196, v158\n"
      "v_mul_f32 v159, v196, v159\n"
      "v_mfma_f32_16x16x32_bf16 v[202:205], a[100:103], v[162:165], v[202:205]\n"
      "buffer_load_dwordx4 v15, s[16:19], s45 offen  lds\n"
      "v_mfma_f32_16x16x32_bf16 v[202:205], a[104:107], v[218:221], v[202:205]\n"
      "s_waitcnt lgkmcnt(0)\n"
      "v_cvt_pk_bf16_f32 v158, v158, v159\n"
      "v_mul_f32 v160, v196, v160\n"
      "v_mfma_f32_16x16x32_bf16 v[118:121], a[124:127], v[162:165], v[118:121]\n"
      "v_mul_f32 v161, v196, v161\n"
      "v_mul_f32 v114, v196, v114\n"
      "v_mfma_f32_16x16x32_bf16 v[118:121], a[128:131], v[218:221], v[118:121]\n"
      "v_mul_f32_e64 v115, v196, v115\n"
      "v_cvt_pk_bf16_f32 v159, v160, v161\n"
      "v_mfma_f32_16x16x32_bf16 v[170:173], a[12:15], v[174:177], v[170:173]\n"
      "buffer_load_dwordx4 v[190:193], v36, s[28:31], s47 offen\n"
      "v_mfma_f32_16x16x32_bf16 v[170:173], a[16:19], v[206:209], v[170:173]\n"
      "v_mul_f32 v96, v196, v96\n"
      "v_mul_f32 v97, v196, v97\n"
      "v_mfma_f32_16x16x32_bf16 v[170:173], a[20:23], v[154:157], v[170:173]\n"
      "v_mul_f32 v98, v196, v98\n"
      "v_mul_f32 v99, v196, v99\n"
      "v_mfma_f32_16x16x32_bf16 v[210:213], a[36:39], v[174:177], v[210:213]\n"
      "v_cvt_pk_bf16_f32 v114, v114, v115\n"
      "v_mul_f32_e64 v116, v196, v116\n"
      "v_mfma_f32_16x16x32_bf16 v[210:213], a[40:43], v[206:209], v[210:213]\n"
      "buffer_load_dword v8, v29, s[28:31], s47 offen\n"
      "v_mfma_f32_16x16x32_bf16 v[210:213], a[44:47], v[154:157], v[210:213]\n"
      "v_mul_f32_e64 v117, v196, v117\n"
      "v_cvt_pk_bf16_f32 v160, v96, v97\n"
      "v_mfma_f32_16x16x32_bf16 v[100:103], a[60:63], v[174:177], v[100:103]\n"
      "v_cvt_pk_bf16_f32 v161, v98, v99\n"
      "v_cvt_pk_bf16_f32 v115, v116, v117\n"
      "v_mfma_f32_16x16x32_bf16 v[100:103], a[64:67], v[206:209], v[100:103]\n"
      "ds_write_b128 v5, v[158:161]\n"
      "v_mfma_f32_16x16x32_bf16 v[100:103], a[68:71], v[154:157], v[100:103]\n"
      "v_mul_f32 v178, v197, v178\n"
      "v_mul_f32 v179, v197, v179\n"
      "v_mfma_f32_16x16x32_bf16 v[150:153], a[84:87], v[174:177], v[150:153]\n"
      "v_cvt_pk_bf16_f32 v178, v178, v179\n"
      "v_mul_f32 v180, v197, v180\n"
      "v_mfma_f32_16x16x32_bf16 v[150:153], a[88:91], v[206:209], v[150:153]\n"
      "ds_write_b64 v6, v[114:115]\n"
      "v_mfma_f32_16x16x32_bf16 v[150:153], a[92:95], v[154:157], v[150:153]\n"
      "v_mul_f32 v181, v197, v181\n"
      "v_mul_f32 v126, v197, v126\n"
      "v_mfma_f32_16x16x32_bf16 v[202:205], a[108:111], v[174:177], v[202:205]\n"
      "v_mul_f32 v127, v197, v127\n"
      "v_cvt_pk_bf16_f32 v179, v180, v181\n"
      "v_mfma_f32_16x16x32_bf16 v[202:205], a[112:115], v[206:209], v[202:205]\n"
      "v_mul_f32 v166, v197, v166\n"
      "v_mul_f32 v167, v197, v167\n"
      "v_mfma_f32_16x16x32_bf16 v[202:205], a[116:119], v[154:157], v[202:205]\n"
      "v_mul_f32 v168, v197, v168\n"
      "v_mul_f32 v169, v197, v169\n"
      "v_mfma_f32_16x16x32_bf16 v[118:121], a[132:135], v[174:177], v[118:121]\n"
      "v_cvt_pk_bf16_f32 v126, v126, v127\n"
      "v_mul_f32 v128, v197, v128\n"
      "v_mfma_f32_16x16x32_bf16 v[118:121], a[136:139], v[206:209], v[118:121]\n"
      "v_mul_f32 v129, v197, v129\n"
      "v_cvt_pk_bf16_f32 v180, v166, v167\n"
      "v_mfma_f32_16x16x32_bf16 v[118:121], a[140:143], v[154:157], v[118:121]\n"
      "v_cvt_pk_bf16_f32 v181, v168, v169\n"
      "v_cvt_pk_bf16_f32 v127, v128, v129\n"
      "v_mfma_f32_16x16x32_bf16 v[194:197], v[184:187], a[144:147], 0\n"
      "ds_write_b128 v5, v[178:181] offset:6144\n"
      "v_mfma_f32_16x16x32_bf16 v[194:197], v[162:165], a[148:151], v[194:197]\n"
      "v_mul_f32_e64 v145, v198, v145\n"
      "v_cvt_pk_bf16_f32 v144, v144, v145\n"
      "v_mfma_f32_16x16x32_bf16 v[194:197], v[218:221], a[152:155], v[194:197]\n"
      "v_mul_f32 v146, v198, v146\n"
      "v_mul_f32 v147, v198, v147\n"
      "v_mfma_f32_16x16x32_bf16 v[194:197], v[174:177], a[156:159], v[194:197]\n"
      "ds_write_b64 v6, v[126:127] offset:6144\n"
      "v_mfma_f32_16x16x32_bf16 v[194:197], v[206:209], a[160:163], v[194:197]\n"
      "v_mul_f32 v138, v198, v138\n"
      "v_mul_f32 v139, v198, v139\n"
      "v_mfma_f32_16x16x32_bf16 v[194:197], v[154:157], a[164:167], v[194:197]\n"
      "v_cvt_pk_bf16_f32 v145, v146, v147\n"
      "v_mul_f32 v108, v198, v108\n"
      "v_mfma_f32_16x16x32_bf16 v[130:133], v[184:187], a[168:171], 0\n"
      "v_mul_f32 v109, v198, v109\n"
      "v_mul_f32_e64 v110, v198, v110\n"
      "v_mfma_f32_16x16x32_bf16 v[130:133], v[162:165], a[172:175], v[130:133]\n"
      "ds_read_b128 v[158:161], v0 offset:2048\n"
      "v_mul_f32_e64 v111, v198, v111\n"
      "v_mfma_f32_16x16x32_bf16 v[130:133], v[218:221], a[176:179], v[130:133]\n"
      "ds_read_b128 v[96:99], v1 offset:2048\n"
      "v_cvt_pk_bf16_f32 v138, v138, v139\n"
      "v_mfma_f32_16x16x32_bf16 v[130:133], v[174:177], a[180:183], v[130:133]\n"
      "ds_read_b128 v[114:117], v0 offset:8192\n"
      "v_mul_f32_e64 v140, v198, v140\n"
      "v_mfma_f32_16x16x32_bf16 v[130:133], v[206:209], a[184:187], v[130:133]\n"
      "ds_read_b128 v[178:181], v1 offset:8192\n"
      "v_mul_f32_e64 v141, v198, v141\n"
      "v_mfma_f32_16x16x32_bf16 v[130:133], v[154:157], a[188:191], v[130:133]\n"
      "ds_read_b128 v[166:169], v0 offset:14336\n"
      "v_cvt_pk_bf16_f32 v146, v108, v109\n"
      "v_mfma_f32_16x16x32_bf16 v[214:217], v[184:187], a[192:195], 0\n"
      "ds_read_b128 v[126:129], v1 offset:14336\n"
      "v_cvt_pk_bf16_f32 v147, v110, v111\n"
      "v_mfma_f32_16x16x32_bf16 v[214:217], v[162:165], a[196:199], v[214:217]\n"
      "ds_write_b128 v5, v[144:147] offset:12288\n"
      "v_mfma_f32_16x16x32_bf16 v[214:217], v[218:221], a[200:203], v[214:217]\n"
      "s_waitcnt lgkmcnt(4)\n"
      "v_cvt_pk_bf16_f32 v139, v140, v141\n"
      "v_fma_f32 v147, v170, v170, s51\n"
      "v_mfma_f32_16x16x32_bf16 v[214:217], v[174:177], a[204:207], v[214:217]\n"
      "s_waitcnt lgkmcnt(3)\n"
      "v_fma_f32 v144, v150, v150, s51\n"
      "v_fmac_f32_e64 v147, v171, v171\n"
      "v_mfma_f32_16x16x32_bf16 v[214:217], v[206:209], a[208:211], v[214:217]\n"
      "ds_write_b64 v6, v[138:139] offset:12288\n"
      "v_mfma_f32_16x16x32_bf16 v[214:217], v[154:157], a[212:215], v[214:217]\n"
      "s_waitcnt lgkmcnt(2)\n"
      "v_fmac_f32 v144, v151, v151\n"
      "v_fmac_f32 v147, v172, v172\n"
      "v_mfma_f32_16x16x32_bf16 v[106:109], a[0:3], v[158:161], 0\n"
      "v_fmac_f32 v144, v152, v152\n"
      "v_fmac_f32 v147, v173, v173\n"
      "v_mfma_f32_16x16x32_bf16 v[106:109], a[4:7], v[96:99], v[106:109]\n"
      "v_fmac_f32 v144, v153, v153\n"
      "v_fmac_f32 v147, v210, v210\n"
      "v_mfma_f32_16x16x32_bf16 v[106:109], a[8:11], v[114:117], v[106:109]\n"
      "v_fmac_f32 v144, v202, v202\n"
      "v_fmac_f32 v147, v211, v211\n"
      "v_mfma_f32_16x16x32_bf16 v[106:109], a[12:15], v[178:181], v[106:109]\n"
      "v_fmac_f32 v144, v203, v203\n"
      "v_fmac_f32 v147, v212, v212\n"
      "v_mfma_f32_16x16x32_bf16 v[106:109], a[16:19], v[166:169], v[106:109]\n"
      "v_fmac_f32 v144, v204, v204\n"
      "v_fmac_f32 v147, v213, v213\n"
      "v_mfma_f32_16x16x32_bf16 v[106:109], a[20:23], v[126:129], v[106:109]\n"
      "v_fmac_f32 v144, v205, v205\n"
      "v_fmac_f32 v147, v100, v100\n"
      "v_mfma_f32_16x16x32_bf16 v[154:157], a[24:27], v[158:161], 0\n"
      "v_fmac_f32 v144, v118, v118\n"
      "v_fmac_f32 v147, v101, v101\n"
      "v_mfma_f32_16x16x32_bf16 v[154:157], a[28:31], v[96:99], v[154:157]\n"
      "v_fmac_f32 v144, v119, v119\n"
      "v_fmac_f32 v147, v102, v102\n"
      "v_mfma_f32_16x16x32_bf16 v[154:157], a[32:35], v[114:117], v[154:157]\n"
      "v_fmac_f32 v144, v120, v120\n"
      "v_fmac_f32 v147, v103, v103\n"
      "v_mfma_f32_16x16x32_bf16 v[154:157], a[36:39], v[178:181], v[154:157]\n"
      "v_fmac_f32 v144, v121, v121\n"
      "v_fma_f32 v185, v106, v106, s51\n"
      "v_mfma_f32_16x16x32_bf16 v[154:157], a[40:43], v[166:169], v[154:157]\n"
      "v_permlane16_swap_b32 v147, v144\n"
      "v_mfma_f32_16x16x32_bf16 v[154:157], a[44:47], v[126:129], v[154:157]\n"
      "v_add_f32 v147, v147, v144\n"
      "v_mov_b32 v144, v147\n"
      "v_mfma_f32_16x16x32_bf16 v[110:113], a[48:51], v[158:161], 0\n"
      "v_fmac_f32 v185, v107, v107\n"
      "v_permlane32_swap_b32 v147, v144\n"
      "v_mfma_f32_16x16x32_bf16 v[110:113], a[52:55], v[96:99], v[110:113]\n"
      "v_add_f32 v147, v147, v144\n"
      "v_rsq_f32 v147, v147\n"
      "v_mfma_f32_16x16x32_bf16 v[110:113], a[56:59], v[114:117], v[110:113]\n"
      "v_mov_b32 v144, v147\n"
      "v_fmac_f32 v185, v108, v108\n"
      "v_mfma_f32_16x16x32_bf16 v[110:113], a[60:63], v[178:181], v[110:113]\n"
      "v_permlane16_swap_b32 v147, v144\n"
      "v_mfma_f32_16x16x32_bf16 v[110:113], a[64:67], v[166:169], v[110:113]\n"
      "v_mul_f32 v147, s50, v147\n"
      "v_mul_f32 v150, v144, v150\n"
      "v_mfma_f32_16x16x32_bf16 v[110:113], a[68:71], v[126:129], v[110:113]\n"
      "v_mul_f32 v151, v144, v151\n"
      "v_mul_f32 v170, v147, v170\n"
      "v_mfma_f32_16x16x32_bf16 v[198:201], a[72:75], v[158:161], 0\n"
      "v_mul_f32_e64 v171, v147, v171\n"
      "v_cvt_pk_bf16_f32 v150, v150, v151\n"
      "v_mfma_f32_16x16x32_bf16 v[198:201], a[76:79], v[96:99], v[198:201]\n"
      "v_mul_f32 v152, v144, v152\n"
      "v_mul_f32 v153, v144, v153\n"
      "v_mfma_f32_16x16x32_bf16 v[198:201], a[80:83], v[114:117], v[198:201]\n"
      "v_mul_f32 v118, v144, v118\n"
      "v_mul_f32 v119, v144, v119\n"
      "v_mfma_f32_16x16x32_bf16 v[198:201], a[84:87], v[178:181], v[198:201]\n"
      "v_cvt_pk_bf16_f32 v170, v170, v171\n"
      "v_mul_f32_e64 v172, v147, v172\n"
      "v_mfma_f32_16x16x32_bf16 v[198:201], a[88:91], v[166:169], v[198:201]\n"
      "v_mul_f32 v173, v147, v173\n"
      "v_mul_f32 v100, v147, v100\n"
      "v_mfma_f32_16x16x32_bf16 v[198:201], a[92:95], v[126:129], v[198:201]\n"
      "v_mul_f32_e64 v101, v147, v101\n"
      "v_cvt_pk_bf16_f32 v151, v152, v153\n"
      "v_mfma_f32_16x16x32_bf16 v[174:177], a[96:99], v[158:161], 0\n"
      "v_mul_f32 v202, v144, v202\n"
      "v_mul_f32 v203, v144, v203\n"
      "v_mfma_f32_16x16x32_bf16 v[174:177], a[100:103], v[96:99], v[174:177]\n"
      "v_mul_f32 v204, v144, v204\n"
      "v_mul_f32 v205, v144, v205\n"
      "v_mfma_f32_16x16x32_bf16 v[174:177], a[104:107], v[114:117], v[174:177]\n"
      "v_cvt_pk_bf16_f32 v118, v118, v119\n"
      "v_mul_f32_e64 v120, v144, v120\n"
      "v_mfma_f32_16x16x32_bf16 v[174:177], a[108:111], v[178:181], v[174:177]\n"
      "v_mul_f32 v121, v144, v121\n"
      "v_cvt_pk_bf16_f32 v171, v172, v173\n"
      "v_mfma_f32_16x16x32_bf16 v[174:177], a[112:115], v[166:169], v[174:177]\n"
      "v_mul_f32 v210, v147, v210\n"
      "v_mul_f32 v211, v147, v211\n"
      "v_mfma_f32_16x16x32_bf16 v[174:177], a[116:119], v[126:129], v[174:177]\n"
      "v_mul_f32 v212, v147, v212\n"
      "v_mul_f32_e64 v213, v147, v213\n"
      "v_mfma_f32_16x16x32_bf16 v[142:145], a[120:123], v[158:161], 0\n"
      "v_cvt_pk_bf16_f32 v100, v100, v101\n"
      "v_mul_f32 v102, v147, v102\n"
      "v_mfma_f32_16x16x32_bf16 v[142:145], a[124:127], v[96:99], v[142:145]\n"
      "v_mul_f32 v103, v147, v103\n"
      "v_cvt_pk_bf16_f32 v152, v202, v203\n"
      "v_mfma_f32_16x16x32_bf16 v[142:145], a[128:131], v[114:117], v[142:145]\n"
      "v_cvt_pk_bf16_f32 v153, v204, v205\n"
      "v_cvt_pk_bf16_f32 v119, v120, v121\n"
      "v_mfma_f32_16x16x32_bf16 v[142:145], a[132:135], v[178:181], v[142:145]\n"
      "v_cvt_pk_bf16_f32 v172, v210, v211\n"
      "v_cvt_pk_bf16_f32 v173, v212, v213\n"
      "v_mfma_f32_16x16x32_bf16 v[142:145], a[136:139], v[166:169], v[142:145]\n"
      "v_cvt_pk_bf16_f32 v101, v102, v103\n"
      "v_fma_f32 v186, v198, v198, s51\n"
      "v_mfma_f32_16x16x32_bf16 v[142:145], a[140:143], v[126:129], v[142:145]\n"
      "v_fmac_f32 v186, v199, v199\n"
      "v_fmac_f32 v186, v200, v200\n"
      "v_mfma_f32_16x16x32_bf16 v[146:149], v[158:161], a[144:147], 0\n"
      "v_fmac_f32 v185, v109, v109\n"
      "v_fmac_f32 v186, v201, v201\n"
      "v_mfma_f32_16x16x32_bf16 v[146:149], v[96:99], a[148:151], v[146:149]\n"
      "v_fmac_f32 v185, v154, v154\n"
      "v_fmac_f32 v186, v174, v174\n"
      "v_mfma_f32_16x16x32_bf16 v[146:149], v[114:117], a[152:155], v[146:149]\n"
      "v_fmac_f32 v185, v155, v155\n"
      "v_fmac_f32 v186, v175, v175\n"
      "v_mfma_f32_16x16x32_bf16 v[146:149], v[178:181], a[156:159], v[146:149]\n"
      "v_fmac_f32 v185, v156, v156\n"
      "v_fmac_f32 v186, v176, v176\n"
      "v_mfma_f32_16x16x32_bf16 v[146:149], v[166:169], a[160:163], v[146:149]\n"
      "v_fmac_f32 v185, v157, v157\n"
      "v_fmac_f32 v186, v177, v177\n"
      "v_mfma_f32_16x16x32_bf16 v[146:149], v[126:129], a[164:167], v[146:149]\n"
      "v_fmac_f32 v185, v110, v110\n"
      "v_fmac_f32 v186, v142, v142\n"
      "v_mfma_f32_16x16x32_bf16 v[102:105], v[158:161], a[168:171], 0\n"
      "v_fmac_f32 v185, v111, v111\n"
      "v_fmac_f32 v186, v143, v143\n"
      "v_mfma_f32_16x16x32_bf16 v[102:105], v[96:99], a[172:175], v[102:105]\n"
      "ds_read_b128 v[206:209], v0 offset:4096\n"
      "v_fmac_f32_e64 v185, v112, v112\n"
      "v_mfma_f32_16x16x32_bf16 v[102:105], v[114:117], a[176:179], v[102:105]\n"
      "ds_read_b128 v[134:137], v1 offset:4096\n"
      "v_fmac_f32_e64 v186, v144, v144\n"
      "v_mfma_f32_16x16x32_bf16 v[102:105], v[178:181], a[180:183], v[102:105]\n"
      "ds_read_b128 v[138:141], v0 offset:10240\n"
      "v_fmac_f32 v185, v113, v113\n"
      "v_mfma_f32_16x16x32_bf16 v[102:105], v[166:169], a[184:187], v[102:105]\n"
      "ds_read_b128 v[218:221], v1 offset:10240\n"
      "v_fmac_f32 v186, v145, v145\n"
      "v_mfma_f32_16x16x32_bf16 v[102:105], v[126:129], a[188:191], v[102:105]\n"
      "ds_read_b128 v[162:165], v0 offset:16384\n"
      "v_permlane16_swap_b32_e64 v185, v186\n"
      "v_mfma_f32_16x16x32_bf16 v[210:213], v[158:161], a[192:195], 0\n"
      "ds_read_b128 v[158:161], v1 offset:16384\n"
      "v_add_f32 v185, v185, v186\n"
      "v_mfma_f32_16x16x32_bf16 v[210:213], v[96:99], a[196:199], v[210:213]\n"
      "s_waitcnt lgkmcnt(5)\n"
      "v_mov_b32_e64 v186, v185\n"
      "v_cvt_pk_bf16_f32 v194, v194, v195\n"
      "v_mfma_f32_16x16x32_bf16 v[210:213], v[114:117], a[200:203], v[210:213]\n"
      "s_waitcnt lgkmcnt(4)\n"
      "v_permlane32_swap_b32 v185, v186\n"
      "v_mfma_f32_16x16x32_bf16 v[210:213], v[178:181], a[204:207], v[210:213]\n"
      "s_waitcnt lgkmcnt(3)\n"
      "v_add_f32 v185, v185, v186\n"
      "v_rsq_f32 v185, v185\n"
      "v_mfma_f32_16x16x32_bf16 v[210:213], v[166:169], a[208:211], v[210:213]\n"
      "s_waitcnt lgkmcnt(2)\n"
      "v_mov_b32_e64 v186, v185\n"
      "v_cvt_pk_bf16_f32 v195, v196, v197\n"
      "v_mfma_f32_16x16x32_bf16 v[210:213], v[126:129], a[212:215], v[210:213]\n"
      "s_waitcnt lgkmcnt(1)\n"
      "v_permlane16_swap_b32 v185, v186\n"
      "v_mfma_f32_16x16x32_bf16 v[120:123], a[0:3], v[206:209], 0\n"
      "s_waitcnt lgkmcnt(0)\n"
      "v_mul_f32 v185, s50, v185\n"
      "v_mul_f32 v198, v186, v198\n"
      "v_mfma_f32_16x16x32_bf16 v[120:123], a[4:7], v[134:137], v[120:123]\n"
      "v_mul_f32 v199, v186, v199\n"
      "v_mul_f32_e64 v106, v185, v106\n"
      "v_mfma_f32_16x16x32_bf16 v[120:123], a[8:11], v[138:141], v[120:123]\n"
      "v_mul_f32 v107, v185, v107\n"
      "v_cvt_pk_bf16_f32 v198, v198, v199\n"
      "v_mfma_f32_16x16x32_bf16 v[120:123], a[12:15], v[218:221], v[120:123]\n"
      "v_mul_f32 v200, v186, v200\n"
      "v_mul_f32 v201, v186, v201\n"
      "v_mfma_f32_16x16x32_bf16 v[120:123], a[16:19], v[162:165], v[120:123]\n"
      "v_mul_f32 v142, v186, v142\n"
      "v_mul_f32_e64 v143, v186, v143\n"
      "v_mfma_f32_16x16x32_bf16 v[120:123], a[20:23], v[158:161], v[120:123]\n"
      "v_cvt_pk_bf16_f32 v106, v106, v107\n"
      "v_mul_f32 v108, v185, v108\n"
      "v_mfma_f32_16x16x32_bf16 v[178:181], a[24:27], v[206:209], 0\n"
      "v_mul_f32 v109, v185, v109\n"
      "v_mul_f32_e64 v110, v185, v110\n"
      "v_mfma_f32_16x16x32_bf16 v[178:181], a[28:31], v[134:137], v[178:181]\n"
      "v_mul_f32 v111, v185, v111\n"
      "v_cvt_pk_bf16_f32 v199, v200, v201\n"
      "v_mfma_f32_16x16x32_bf16 v[178:181], a[32:35], v[138:141], v[178:181]\n"
      "v_mul_f32 v174, v186, v174\n"
      "v_mul_f32 v175, v186, v175\n"
      "v_mfma_f32_16x16x32_bf16 v[178:181], a[36:39], v[218:221], v[178:181]\n"
      "v_mul_f32 v176, v186, v176\n"
      "v_mul_f32_e64 v177, v186, v177\n"
      "v_mfma_f32_16x16x32_bf16 v[178:181], a[40:43], v[162:165], v[178:181]\n"
      "ds_read_b128 v[166:169], v7 offset:18432\n"
      "v_cvt_pk_bf16_f32 v142, v142, v143\n"
      "v_mfma_f32_16x16x32_bf16 v[178:181], a[44:47], v[158:161], v[178:181]\n"
      "ds_read_b128 v[96:99], v7 offset:22528\n"
      "v_mul_f32 v144, v186, v144\n"
      "v_mfma_f32_16x16x32_bf16 v[202:205], a[48:51], v[206:209], 0\n"
      "ds_read_b128 v[114:117], v7 offset:26624\n"
      "v_mul_f32 v145, v186, v145\n"
      "v_mfma_f32_16x16x32_bf16 v[202:205], a[52:55], v[134:137], v[202:205]\n"
      "v_cvt_pk_bf16_f32 v107, v108, v109\n"
      "v_mul_f32 v154, v185, v154\n"
      "v_mfma_f32_16x16x32_bf16 v[202:205], a[56:59], v[138:141], v[202:205]\n"
      "ds_read_b128 v[186:189], v7 offset:30720\n"
      "v_mul_f32 v155, v185, v155\n"
      "v_mfma_f32_16x16x32_bf16 v[202:205], a[60:63], v[218:221], v[202:205]\n"
      "ds_read_b128 v[124:127], v7 offset:34816\n"
      "v_mul_f32 v156, v185, v156\n"
      "v_mfma_f32_16x16x32_bf16 v[202:205], a[64:67], v[162:165], v[202:205]\n"
      "v_mul_f32 v157, v185, v157\n"
      "v_cvt_pk_bf16_f32 v110, v110, v111\n"
      "v_mfma_f32_16x16x32_bf16 v[202:205], a[68:71], v[158:161], v[202:205]\n"
      "v_mul_f32 v112, v185, v112\n"
      "v_mul_f32 v113, v185, v113\n"
      "v_mfma_f32_16x16x32_bf16 v[182:185], a[72:75], v[206:209], 0\n"
      "s_waitcnt lgkmcnt(2)\n"
      "v_cvt_pk_bf16_f32 v200, v174, v175\n"
      "v_cvt_pk_bf16_f32 v201, v176, v177\n"
      "v_mfma_f32_16x16x32_bf16 v[182:185], a[76:79], v[134:137], v[182:185]\n"
      "buffer_store_dwordx4 v[166:169], v20, s[24:27], s46 offen sc1\n"
      "v_mfma_f32_16x16x32_bf16 v[182:185], a[80:83], v[138:141], v[182:185]\n"
      "s_waitcnt lgkmcnt(0)\n"
      "v_cvt_pk_bf16_f32 v143, v144, v145\n"
      "v_cvt_pk_bf16_f32 v108, v154, v155\n"
      "v_mfma_f32_16x16x32_bf16 v[182:185], a[84:87], v[218:221], v[182:185]\n"
      "v_cvt_pk_bf16_f32 v109, v156, v157\n"
      "v_cvt_pk_bf16_f32 v111, v112, v113\n"
      "v_mfma_f32_16x16x32_bf16 v[182:185], a[88:91], v[162:165], v[182:185]\n"
      "v_fma_f32 v129, v120, v120, s51\n"
      "v_fmac_f32 v129, v121, v121\n"
      "v_mfma_f32_16x16x32_bf16 v[182:185], a[92:95], v[158:161], v[182:185]\n"
      "v_fmac_f32 v129, v122, v122\n"
      "v_fmac_f32_e64 v129, v123, v123\n"
      "v_mfma_f32_16x16x32_bf16 v[166:169], a[96:99], v[206:209], 0\n"
      "v_fmac_f32 v129, v178, v178\n"
      "v_fmac_f32 v129, v179, v179\n"
      "v_mfma_f32_16x16x32_bf16 v[166:169], a[100:103], v[134:137], v[166:169]\n"
      "v_fmac_f32 v129, v180, v180\n"
      "v_fmac_f32 v129, v181, v181\n"
      "v_mfma_f32_16x16x32_bf16 v[166:169], a[104:107], v[138:141], v[166:169]\n"
      "v_fma_f32 v128, v182, v182, s51\n"
      "v_fmac_f32_e64 v128, v183, v183\n"
      "v_mfma_f32_16x16x32_bf16 v[166:169], a[108:111], v[218:221], v[166:169]\n"
      "buffer_store_dwordx4 v[96:99], v21, s[24:27], s46 offen sc1\n"
      "v_mfma_f32_16x16x32_bf16 v[166:169], a[112:115], v[162:165], v[166:169]\n"
      "v_fmac_f32 v128, v184, v184\n"
      "v_fmac_f32 v128, v185, v185\n"
      "v_mfma_f32_16x16x32_bf16 v[166:169], a[116:119], v[158:161], v[166:169]\n"
      "v_fmac_f32 v129, v202, v202\n"
      "v_fmac_f32 v129, v203, v203\n"
      "v_mfma_f32_16x16x32_bf16 v[154:157], a[120:123], v[206:209], 0\n"
      "v_fmac_f32 v129, v204, v204\n"
      "v_fmac_f32 v129, v205, v205\n"
      "v_mfma_f32_16x16x32_bf16 v[154:157], a[124:127], v[134:137], v[154:157]\n"
      "v_cvt_pk_bf16_f32 v196, v146, v147\n"
      "v_cvt_pk_bf16_f32 v197, v148, v149\n"
      "v_mfma_f32_16x16x32_bf16 v[154:157], a[128:131], v[138:141], v[154:157]\n"
      "v_fmac_f32 v128, v166, v166\n"
      "v_fmac_f32 v128, v167, v167\n"
      "v_mfma_f32_16x16x32_bf16 v[154:157], a[132:135], v[218:221], v[154:157]\n"
      "v_fmac_f32 v128, v168, v168\n"
      "v_fmac_f32 v128, v169, v169\n"
      "v_mfma_f32_16x16x32_bf16 v[154:157], a[136:139], v[162:165], v[154:157]\n"
      "v_cvt_pk_bf16_f32 v130, v130, v131\n"
      "v_cvt_pk_bf16_f32 v131, v132, v133\n"
      "v_mfma_f32_16x16x32_bf16 v[154:157], a[140:143], v[158:161], v[154:157]\n"
      "buffer_store_dwordx4 v[114:117], v22, s[24:27], s46 offen sc1\n"
      "v_mfma_f32_16x16x32_bf16 v[146:149], v[206:209], a[144:147], 0\n"
      "v_cvt_pk_bf16_f32 v132, v102, v103\n"
      "v_cvt_pk_bf16_f32 v133, v104, v105\n"
      "v_mfma_f32_16x16x32_bf16 v[146:149], v[134:137], a[148:151], v[146:149]\n"
      "v_cvt_pk_bf16_f32 v214, v214, v215\n"
      "v_cvt_pk_bf16_f32 v215, v216, v217\n"
      "v_mfma_f32_16x16x32_bf16 v[146:149], v[138:141], a[152:155], v[146:149]\n"
      "v_fmac_f32 v128, v154, v154\n"
      "v_fmac_f32 v128, v155, v155\n"
      "v_mfma_f32_16x16x32_bf16 v[146:149], v[218:221], a[156:159], v[146:149]\n"
      "v_fmac_f32 v128, v156, v156\n"
      "v_fmac_f32 v128, v157, v157\n"
      "v_mfma_f32_16x16x32_bf16 v[146:149], v[162:165], a[160:163], v[146:149]\n"
      "v_cvt_pk_bf16_f32 v216, v210, v211\n"
      "v_permlane16_swap_b32_e64 v129, v128\n"
      "v_mfma_f32_16x16x32_bf16 v[146:149], v[158:161], a[164:167], v[146:149]\n"
      "v_add_f32 v129, v129, v128\n"
      "v_mov_b32 v128, v129\n"
      "v_mfma_f32_16x16x32_bf16 v[174:177], v[206:209], a[168:171], 0\n"
      "v_cvt_pk_bf16_f32 v217, v212, v213\n"
      "v_permlane32_swap_b32_e64 v129, v128\n"
      "v_mfma_f32_16x16x32_bf16 v[174:177], v[134:137], a[172:175], v[174:177]\n"
      "buffer_store_dwordx4 v[186:189], v23, s[24:27], s46 offen sc1\n"
      "v_mfma_f32_16x16x32_bf16 v[174:177], v[138:141], a[176:179], v[174:177]\n"
      "ds_read_b128 v[102:105], v2 offset:18432\n"
      "v_add_f32 v129, v129, v128\n"
      "v_mfma_f32_16x16x32_bf16 v[174:177], v[218:221], a[180:183], v[174:177]\n"
      "ds_read_b128 v[210:213], v2 offset:21504\n"
      "v_rsq_f32 v129, v129\n"
      "v_mfma_f32_16x16x32_bf16 v[174:177], v[162:165], a[184:187], v[174:177]\n"
      "ds_read_b128 v[186:189], v2 offset:24576\n"
      "v_mov_b32_e64 v128, v129\n"
      "v_mfma_f32_16x16x32_bf16 v[174:177], v[158:161], a[188:191], v[174:177]\n"
      "v_cvt_pk_bf16_f32 v76, v146, v147\n"
      "v_permlane16_swap_b32 v129, v128\n"
      "v_mfma_f32_16x16x32_bf16 v[144:147], v[206:209], a[192:195], 0\n"
      "v_mul_f32 v182, v128, v182\n"
      "v_mul_f32_e64 v183, v128, v183\n"
      "v_mfma_f32_16x16x32_bf16 v[144:147], v[134:137], a[196:199], v[144:147]\n"
      "v_cvt_pk_bf16_f32 v182, v182, v183\n"
      "v_mul_f32 v184, v128, v184\n"
      "v_mfma_f32_16x16x32_bf16 v[144:147], v[138:141], a[200:203], v[144:147]\n"
      "s_waitcnt lgkmcnt(2)\n"
      "v_mul_f32 v185, v128, v185\n"
      "buffer_store_dwordx4 v[124:127], v25, s[24:27], s46 offen sc1\n"
      "v_mfma_f32_16x16x32_bf16 v[144:147], v[218:221], a[204:207], v[144:147]\n"
      "s_waitcnt lgkmcnt(0)\n"
      "v_cvt_pk_bf16_f32 v183, v184, v185\n"
      "v_mul_f32 v166, v128, v166\n"
      "v_mfma_f32_16x16x32_bf16 v[144:147], v[162:165], a[208:211], v[144:147]\n"
      "v_mul_f32 v167, v128, v167\n"
      "v_mul_f32 v168, v128, v168\n"
      "v_mfma_f32_16x16x32_bf16 v[144:147], v[158:161], a[212:215], v[144:147]\n"
      "v_mul_f32 v169, v128, v169\n"
      "v_cvt_pk_bf16_f32 v184, v166, v167\n"
      "v_mfma_f32_16x16x32_bf16 v[158:161], a[216:219], v[102:105], 0\n"
      "v_cvt_pk_bf16_f32 v185, v168, v169\n"
      "v_mul_f32_e64 v129, s50, v129\n"
      "v_mfma_f32_16x16x32_bf16 v[158:161], a[220:223], v[210:213], v[158:161]\n"
      "ds_read_b128 v[134:137], v10 offset:32\n"
      "v_mul_f32 v120, v129, v120\n"
      "v_mfma_f32_16x16x32_bf16 v[158:161], a[224:227], v[186:189], v[158:161]\n"
      "v_mul_f32 v121, v129, v121\n"
      "v_mul_f32 v154, v128, v154\n"
      "v_mfma_f32_16x16x32_bf16 v[96:99], a[240:243], v[102:105], 0\n"
      "v_mul_f32 v155, v128, v155\n"
      "v_cvt_pk_bf16_f32 v120, v120, v121\n"
      "v_mfma_f32_16x16x32_bf16 v[96:99], a[244:247], v[210:213], v[96:99]\n"
      "v_mul_f32 v122, v129, v122\n"
      "v_mul_f32 v123, v129, v123\n"
      "v_mfma_f32_16x16x32_bf16 v[96:99], a[248:251], v[186:189], v[96:99]\n"
      "ds_read_b64 v[218:219], v10 offset:24\n"
      "v_mul_f32_e64 v202, v129, v202\n"
      "v_mfma_f32_16x16x32_bf16 v[114:117], v[232:235], v[102:105], 0\n"
      "ds_read_b128 v[102:105], v2 offset:27648\n"
      "v_mul_f32_e64 v203, v129, v203\n"
      "v_mfma_f32_16x16x32_bf16 v[114:117], v[236:239], v[210:213], v[114:117]\n"
      "ds_read_b128 v[206:209], v2 offset:30720\n"
      "v_cvt_pk_bf16_f32 v154, v154, v155\n"
      "v_mfma_f32_16x16x32_bf16 v[114:117], v[240:243], v[186:189], v[114:117]\n"
      "ds_read_b128 v[210:213], v2 offset:33792\n"
      "v_mul_f32 v156, v128, v156\n"
      "v_mfma_f32_16x16x32_bf16 v[186:189], v[150:153], v[170:173], v[40:43]\n"
      "s_waitcnt lgkmcnt(4)\n"
      "v_mul_f32_e64 v157, v128, v157\n"
      "v_cvt_pk_bf16_f32 v121, v122, v123\n"
      "v_mfma_f32_16x16x32_bf16 v[166:169], v[198:201], v[170:173], v[44:47]\n"
      "v_mul_f32 v178, v129, v178\n"
      "v_mul_f32 v179, v129, v179\n"
      "v_mfma_f32_16x16x32_bf16 v[162:165], v[182:185], v[170:173], v[48:51]\n"
      "s_waitcnt lgkmcnt(3)\n"
      "v_mul_f32 v180, v129, v180\n"
      "v_mul_f32 v181, v129, v181\n"
      "v_mfma_f32_16x16x32_bf16 v[138:141], v[134:137], v[170:173], v[24:27]\n"
      "s_waitcnt lgkmcnt(2)\n"
      "v_cvt_pk_bf16_f32 v202, v202, v203\n"
      "v_mul_f32_e64 v204, v129, v204\n"
      "v_mfma_f32_16x16x16_bf16 v[186:189], v[118:119], v[100:101], v[186:189]\n"
      "v_mul_f32_e64 v205, v129, v205\n"
      "v_cvt_pk_bf16_f32 v155, v156, v157\n"
      "v_cvt_pk_bf16_f32 v122, v178, v179\n"
      "v_mfma_f32_16x16x16_bf16 v[166:169], v[142:143], v[100:101], v[166:169]\n"
      "v_cvt_pk_bf16_f32 v123, v180, v181\n"
      "v_cvt_pk_bf16_f32 v203, v204, v205\n"
      "v_cvt_pk_bf16_f32 v77, v148, v149\n"
      "v_mfma_f32_16x16x16_bf16 v[162:165], v[154:155], v[100:101], v[162:165]\n"
      "v_max3_f32 v129, v186, v187, v188\n"
      "v_cvt_pk_bf16_f32 v80, v174, v175\n"
      "v_cvt_pk_bf16_f32 v81, v176, v177\n"
      "v_mfma_f32_16x16x16_bf16 v[138:141], v[218:219], v[100:101], v[138:141]\n"
      "v_max3_f32 v173, v189, v166, v167\n"
      "v_max3_f32 v129, v129, v168, v169\n"
      "v_cvt_pk_bf16_f32 v84, v144, v145\n"
      "v_mfma_f32_16x16x32_bf16 v[158:161], a[228:231], v[102:105], v[158:161]\n"
      "s_waitcnt lgkmcnt(0)\n"
      "v_max3_f32 v173, v173, v162, v163\n"
      "v_max3_f32 v129, v129, v164, v165\n"
      "v_mfma_f32_16x16x32_bf16 v[158:161], a[232:235], v[206:209], v[158:161]\n"
      "v_max3_f32 v129, v129, v173, v138\n"
      "v_mov_b32 v173, v129\n"
      "v_mfma_f32_16x16x32_bf16 v[158:161], a[236:239], v[210:213], v[158:161]\n"
      "v_cvt_pk_bf16_f32 v85, v146, v147\n"
      "v_permlane16_swap_b32 v129, v173\n"
      "v_mfma_f32_16x16x32_bf16 v[96:99], a[252:255], v[102:105], v[96:99]\n"
      "v_max_f32 v129, v129, v173\n"
      "v_mov_b32 v173, v129\n"
      "v_mfma_f32_16x16x32_bf16 v[96:99], v[224:227], v[206:209], v[96:99]\n"
      "s_waitcnt vmcnt(6)\n"
      "v_or3_b32 v190, v190, v191, v192\n"
      "v_permlane32_swap_b32 v129, v173\n"
      "v_mfma_f32_16x16x32_bf16 v[96:99], v[228:231], v[210:213], v[96:99]\n"
      "v_max_f32 v129, v129, v173\n"
      "v_sub_f32 v186, v186, v129\n"
      "v_mfma_f32_16x16x32_bf16 v[114:117], v[244:247], v[102:105], v[114:117]\n"
      "ds_read_b128 v[170:173], v9 offset:5632\n"
      "v_sub_f32 v187, v187, v129\n"
      "v_mfma_f32_16x16x32_bf16 v[114:117], v[248:251], v[206:209], v[114:117]\n"
      "ds_read_b128 v[144:147], v2 offset:18688\n"
      "v_sub_f32 v188, v188, v129\n"
      "v_mfma_f32_16x16x32_bf16 v[114:117], v[252:255], v[210:213], v[114:117]\n"
      "ds_read_b128 v[174:177], v2 offset:21760\n"
      "v_sub_f32 v189, v189, v129\n"
      "v_mfma_f32_16x16x32_bf16 v[210:213], v[150:153], v[106:109], v[52:55]\n"
      "ds_read_b128 v[220:223], v2 offset:24832\n"
      "v_exp_f32 v186, v186\n"
      "v_mfma_f32_16x16x32_bf16 v[204:207], v[198:201], v[106:109], v[56:59]\n"
      "v_exp_f32 v187, v187\n"
      "v_mfma_f32_16x16x32_bf16 v[102:105], v[182:185], v[106:109], v[60:63]\n"
      "v_sub_f32 v166, v166, v129\n"
      "v_sub_f32 v167, v167, v129\n"
      "v_mfma_f32_16x16x32_bf16 v[124:127], v[134:137], v[106:109], v[26:29]\n"
      "v_sub_f32 v168, v168, v129\n"
      "v_sub_f32 v169, v169, v129\n"
      "v_mfma_f32_16x16x16_bf16 v[210:213], v[118:119], v[110:111], v[210:213]\n"
      "v_exp_f32 v188, v188\n"
      "v_exp_f32 v189, v189\n"
      "v_mfma_f32_16x16x16_bf16 v[204:207], v[142:143], v[110:111], v[204:207]\n"
      "v_exp_f32 v166, v166\n"
      "v_exp_f32 v167, v167\n"
      "v_mfma_f32_16x16x16_bf16 v[102:105], v[154:155], v[110:111], v[102:105]\n"
      "v_exp_f32 v168, v168\n"
      "v_exp_f32 v169, v169\n"
      "v_mfma_f32_16x16x16_bf16 v[124:127], v[218:219], v[110:111], v[124:127]\n"
      "v_cvt_pk_bf16_f32 v186, v186, v187\n"
      "v_cvt_pk_bf16_f32 v187, v188, v189\n"
      "v_cvt_pk_bf16_f32 v188, v166, v167\n"
      "s_waitcnt lgkmcnt(0)\n"
      "v_mfma_f32_16x16x32_bf16 v[178:181], a[216:219], v[144:147], 0\n"
      "v_cvt_pk_bf16_f32 v189, v168, v169\n"
      "v_sub_f32 v162, v162, v129\n"
      "v_mfma_f32_16x16x32_bf16 v[178:181], a[220:223], v[174:177], v[178:181]\n"
      "ds_read_u16 v78, v11\n"
      "v_sub_f32 v163, v163, v129\n"
      "v_mfma_f32_16x16x32_bf16 v[178:181], a[224:227], v[220:223], v[178:181]\n"
      "v_sub_f32 v164, v164, v129\n"
      "v_sub_f32 v165, v165, v129\n"
      "v_mfma_f32_16x16x32_bf16 v[166:169], a[240:243], v[144:147], 0\n"
      "ds_read_u16 v82, v11 offset:32\n"
      "v_sub_f32 v138, v138, v129\n"
      "v_mfma_f32_16x16x32_bf16 v[166:169], a[244:247], v[174:177], v[166:169]\n"
      "v_exp_f32 v162, v162\n"
      "v_mfma_f32_16x16x32_bf16 v[166:169], a[248:251], v[220:223], v[166:169]\n"
      "ds_read_u16 v86, v11 offset:64\n"
      "v_exp_f32 v163, v163\n"
      "v_mfma_f32_16x16x32_bf16 v[126:129], v[232:235], v[144:147], 0\n"
      "v_exp_f32 v164, v164\n"
      "v_mfma_f32_16x16x32_bf16 v[126:129], v[236:239], v[174:177], v[126:129]\n"
      "s_waitcnt lgkmcnt(2)\n"
      "v_exp_f32 v165, v165\n"
      "v_mfma_f32_16x16x32_bf16 v[126:129], v[240:243], v[220:223], v[126:129]\n"
      "v_exp_f32 v138, v138\n"
      "v_mfma_f32_16x16x32_bf16 v[108:111], v[32:35], v[186:189], 0\n"
      "s_waitcnt lgkmcnt(1)\n"
      "v_cvt_pk_bf16_f32 v88, v162, v163\n"
      "v_cvt_pk_bf16_f32 v89, v164, v165\n"
      "v_cvt_pk_bf16_f32 v90, v138, 0\n"
      "v_max3_f32 v209, v210, v211, v212\n"
      "v_max3_f32 v165, v213, v204, v205\n"
      "v_mfma_f32_16x16x32_bf16 v[108:111], v[32:35], v[88:91], v[108:111]\n"
      "s_waitcnt lgkmcnt(0)\n"
      "v_max3_f32 v209, v209, v206, v207\n"
      "v_max3_f32 v165, v165, v102, v103\n"
      "v_mfma_f32_16x16x32_bf16 v[138:141], v[194:197], v[186:189], 0\n"
      "v_max3_f32 v209, v209, v104, v105\n"
      "v_max3_f32 v209, v209, v165, v124\n"
      "v_mfma_f32_16x16x32_bf16 v[138:141], v[76:79], v[88:91], v[138:141]\n"
      "v_mov_b32_e64 v220, v209\n"
      "v_fma_f32 v125, v158, v158, s52\n"
      "v_mfma_f32_16x16x32_bf16 v[110:113], v[130:133], v[186:189], 0\n"
      "v_permlane16_swap_b32 v209, v220\n"
      "v_mfma_f32_16x16x32_bf16 v[110:113], v[80:83], v[88:91], v[110:113]\n"
      "ds_read_b128 v[144:147], v2 offset:27904\n"
      "v_max_f32 v209, v209, v220\n"
      "v_mfma_f32_16x16x32_bf16 v[162:165], v[214:217], v[186:189], 0\n"
      "ds_read_b128 v[186:189], v2 offset:30976\n"
      "v_mov_b32_e64 v220, v209\n"
      "v_mfma_f32_16x16x32_bf16 v[162:165], v[84:87], v[88:91], v[162:165]\n"
      "ds_read_b128 v[174:177], v2 offset:34048\n"
      "v_permlane32_swap_b32 v209, v220\n"
      "v_max_f32 v209, v209, v220\n"
      "v_sub_f32 v210, v210, v209\n"
      "v_sub_f32 v211, v211, v209\n"
      "v_sub_f32 v212, v212, v209\n"
      "v_sub_f32 v213, v213, v209\n"
      "v_exp_f32 v210, v210\n"
      "v_exp_f32 v211, v211\n"
      "v_sub_f32 v204, v204, v209\n"
      "v_sub_f32 v205, v205, v209\n"
      "v_sub_f32 v206, v206, v209\n"
      "v_sub_f32 v207, v207, v209\n"
      "v_exp_f32 v212, v212\n"
      "v_exp_f32 v213, v213\n"
      "v_exp_f32 v204, v204\n"
      "v_exp_f32 v205, v205\n"
      "v_exp_f32 v206, v206\n"
      "v_exp_f32 v207, v207\n"
      "v_cvt_pk_bf16_f32 v210, v210, v211\n"
      "v_cvt_pk_bf16_f32 v211, v212, v213\n"
      "v_cvt_pk_bf16_f32 v212, v204, v205\n"
      "v_cvt_pk_bf16_f32 v213, v206, v207\n"
      "v_mfma_f32_16x16x32_bf16 v[220:223], v[150:153], v[120:123], v[64:67]\n"
      "v_sub_f32 v102, v102, v209\n"
      "v_sub_f32 v103, v103, v209\n"
      "v_sub_f32 v104, v104, v209\n"
      "v_sub_f32 v105, v105, v209\n"
      "v_sub_f32 v124, v124, v209\n"
      "v_exp_f32 v102, v102\n"
      "v_exp_f32 v103, v103\n"
      "v_exp_f32 v104, v104\n"
      "v_exp_f32_e64 v105, v105\n"
      "v_exp_f32 v124, v124\n"
      "v_cvt_pk_bf16_f32 v92, v102, v103\n"
      "v_cvt_pk_bf16_f32 v93, v104, v105\n"
      "v_mfma_f32_16x16x32_bf16 v[206:209], v[198:201], v[120:123], v[68:71]\n"
      "s_waitcnt lgkmcnt(0)\n"
      "v_cvt_pk_bf16_f32 v94, v124, 0\n"
      "v_fmac_f32 v125, v159, v159\n"
      "v_fmac_f32 v125, v160, v160\n"
      "v_fmac_f32 v125, v161, v161\n"
      "v_fmac_f32 v125, v96, v96\n"
      "v_fmac_f32 v125, v97, v97\n"
      "v_fmac_f32 v125, v98, v98\n"
      "v_fmac_f32 v125, v99, v99\n"
      "v_fmac_f32 v125, v114, v114\n"
      "v_fmac_f32 v125, v115, v115\n"
      "v_fmac_f32 v125, v116, v116\n"
      "v_fmac_f32 v125, v117, v117\n"
      "v_rcp_f32_e64 v108, v108\n"
      "s_waitcnt vmcnt(5)\n"
      "v_or3_b32 v190, v190, v193, v8\n"
      "v_mul_f32 v138, v108, v138\n"
      "v_mul_f32 v139, v108, v139\n"
      "v_cvt_pk_bf16_f32 v138, v138, v139\n"
      "v_mul_f32 v140, v108, v140\n"
      "v_mul_f32 v141, v108, v141\n"
      "v_cvt_pk_bf16_f32 v139, v140, v141\n"
      "v_mul_f32 v110, v108, v110\n"
      "v_mul_f32 v111, v108, v111\n"
      "v_mul_f32 v112, v108, v112\n"
      "v_mul_f32 v113, v108, v113\n"
      "v_cvt_pk_bf16_f32 v140, v110, v111\n"
      "v_mfma_f32_16x16x32_bf16 v[104:107], v[182:185], v[120:123], v[72:75]\n"
      "v_cvt_pk_bf16_f32 v141, v112, v113\n"
      "v_mul_f32_e64 v162, v108, v162\n"
      "v_mfma_f32_16x16x32_bf16 v[100:103], v[134:137], v[120:123], v[28:31]\n"
      "ds_write_b128 v3, v[138:141]\n"
      "v_mfma_f32_16x16x16_bf16 v[220:223], v[118:119], v[202:203], v[220:223]\n"
      "v_mul_f32_e64 v163, v108, v163\n"
      "v_cvt_pk_bf16_f32 v162, v162, v163\n"
      "v_mul_f32_e64 v164, v108, v164\n"
      "v_mfma_f32_16x16x16_bf16 v[206:209], v[142:143], v[202:203], v[206:209]\n"
      "v_mul_f32_e64 v165, v108, v165\n"
      "v_cvt_pk_bf16_f32 v163, v164, v165\n"
      "v_and_b32 v190, 0x7fff7fff, v190\n"
      "v_mfma_f32_16x16x16_bf16 v[104:107], v[154:155], v[202:203], v[104:107]\n"
      "v_max3_f32 v90, v220, v221, v222\n"
      "v_readlane_b32 s45, v37, s43\n"
      "v_readlane_b32 s46, v37, s42\n"
      "v_mfma_f32_16x16x16_bf16 v[100:103], v[218:219], v[202:203], v[100:103]\n"
      "v_max3_f32 v141, v223, v206, v207\n"
      "v_max3_f32 v90, v90, v208, v209\n"
      "v_or3_b32 v170, v170, v171, v172\n"
      "v_mfma_f32_16x16x32_bf16 v[178:181], a[228:231], v[144:147], v[178:181]\n"
      "ds_write_b64 v4, v[162:163]\n"
      "v_mfma_f32_16x16x32_bf16 v[178:181], a[232:235], v[186:189], v[178:181]\n"
      "s_or_b32 s95, s45, s46\n"
      "v_max3_f32 v141, v141, v104, v105\n"
      "v_max3_f32 v90, v90, v106, v107\n"
      "v_mfma_f32_16x16x32_bf16 v[178:181], a[236:239], v[174:177], v[178:181]\n"
      "s_or_b32 s95, s95, s48\n"
      "v_max3_f32 v90, v90, v141, v100\n"
      "v_mov_b32_e64 v103, v90\n"
      "v_mfma_f32_16x16x32_bf16 v[166:169], a[252:255], v[144:147], v[166:169]\n"
      "ds_write_b32 v31, v190 offset:4608\n"
      "v_mfma_f32_16x16x32_bf16 v[166:169], v[224:227], v[186:189], v[166:169]\n"
      "s_cmp_ge_u32 s95, 0x40000000\n"
      "s_cselect_b32 s94, -1, -2\n"
      "v_fma_f32 v172, v178, v178, s52\n"
      "v_mfma_f32_16x16x32_bf16 v[166:169], v[228:231], v[174:177], v[166:169]\n"
      "s_cmp_gt_u32 s42, s44\n"
      "s_cselect_b32 s95, s94, s95\n"
      "v_fmac_f32 v172, v179, v179\n"
      "v_mfma_f32_16x16x32_bf16 v[126:129], v[244:247], v[144:147], v[126:129]\n"
      "ds_read_b128 v[144:147], v10\n"
      "v_fmac_f32 v172, v180, v180\n"
      "v_mfma_f32_16x16x32_bf16 v[126:129], v[248:251], v[186:189], v[126:129]\n"
      "s_add_u32 s42, s42, 1\n"
      "v_fmac_f32 v172, v181, v181\n"
      "v_fmac_f32 v172, v166, v166\n"
      "v_mfma_f32_16x16x32_bf16 v[126:129], v[252:255], v[174:177], v[126:129]\n"
      "v_fmac_f32 v172, v167, v167\n"
      "v_fmac_f32 v172, v168, v168\n"
      "v_mfma_f32_16x16x32_bf16 v[188:191], v[32:35], v[210:213], 0\n"
      "v_fmac_f32 v172, v169, v169\n"
      "v_permlane16_swap_b32_e64 v90, v103\n"
      "v_mfma_f32_16x16x32_bf16 v[188:191], v[32:35], v[92:95], v[188:191]\n"
      "ds_read_b64 v[88:89], v10 offset:16\n"
      "v_max_f32 v90, v90, v103\n"
      "v_mfma_f32_16x16x32_bf16 v[138:141], v[194:197], v[210:213], 0\n"
      "v_fmac_f32 v172, v126, v126\n"
      "v_fmac_f32_e64 v172, v127, v127\n"
      "v_mfma_f32_16x16x32_bf16 v[138:141], v[76:79], v[92:95], v[138:141]\n"
      "s_waitcnt lgkmcnt(1)\n"
      "v_fmac_f32 v172, v128, v128\n"
      "v_fmac_f32 v172, v129, v129\n"
      "v_mfma_f32_16x16x32_bf16 v[190:193], v[130:133], v[210:213], 0\n"
      "v_mov_b32 v103, v90\n"
      "v_permlane16_swap_b32 v125, v172\n"
      "v_mfma_f32_16x16x32_bf16 v[190:193], v[80:83], v[92:95], v[190:193]\n"
      "ds_read_b128 v[202:205], v2 offset:18944\n"
      "v_add_f32 v125, v125, v172\n"
      "v_mfma_f32_16x16x32_bf16 v[120:123], v[214:217], v[210:213], 0\n"
      "ds_read_b128 v[210:213], v2 offset:22016\n"
      "v_rcp_f32 v188, v188\n"
      "v_mfma_f32_16x16x32_bf16 v[120:123], v[84:87], v[92:95], v[120:123]\n"
      "ds_read_b128 v[174:177], v2 offset:25088\n"
      "v_permlane32_swap_b32 v90, v103\n"
      "v_mfma_f32_16x16x32_bf16 v[162:165], v[144:147], v[150:153], v[30:33]\n"
      "s_waitcnt lgkmcnt(3)\n"
      "v_mul_f32 v138, v188, v138\n"
      "v_mul_f32 v139, v188, v139\n"
      "v_mfma_f32_16x16x32_bf16 v[108:111], v[144:147], v[198:201], v[30:33]\n"
      "v_max_f32 v90, v90, v103\n"
      "v_cvt_pk_bf16_f32 v138, v138, v139\n"
      "v_mfma_f32_16x16x32_bf16 v[150:153], v[144:147], v[182:185], v[30:33]\n"
      "v_mul_f32 v140, v188, v140\n"
      "v_mul_f32 v141, v188, v141\n"
      "v_mfma_f32_16x16x32_bf16 v[184:187], v[144:147], v[134:137], v[30:33]\n"
      "s_waitcnt lgkmcnt(2)\n"
      "v_sub_f32 v220, v220, v90\n"
      "v_sub_f32 v221, v221, v90\n"
      "v_mfma_f32_16x16x16_bf16 v[162:165], v[88:89], v[118:119], v[162:165]\n"
      "v_cvt_pk_bf16_f32 v139, v140, v141\n"
      "v_mul_f32 v190, v188, v190\n"
      "v_mul_f32 v191, v188, v191\n"
      "v_mfma_f32_16x16x16_bf16 v[108:111], v[88:89], v[142:143], v[108:111]\n"
      "v_mul_f32 v192, v188, v192\n"
      "v_mul_f32_e64 v193, v188, v193\n"
      "v_sub_f32_e64 v222, v222, v90\n"
      "v_mfma_f32_16x16x16_bf16 v[150:153], v[88:89], v[154:155], v[150:153]\n"
      "v_sub_f32_e64 v223, v223, v90\n"
      "v_exp_f32_e64 v220, v220\n"
      "v_mfma_f32_16x16x16_bf16 v[184:187], v[88:89], v[218:219], v[184:187]\n"
      "v_exp_f32 v221, v221\n"
      "v_cndmask_b32 v162, v162, v108, s[58:59]\n"
      "v_mfma_f32_16x16x32_bf16 v[144:147], a[216:219], v[202:205], 0\n"
      "s_waitcnt lgkmcnt(0)\n"
      "v_cvt_pk_bf16_f32 v140, v190, v191\n"
      "v_cndmask_b32 v162, v162, v150, s[60:61]\n"
      "v_mfma_f32_16x16x32_bf16 v[144:147], a[220:223], v[210:213], v[144:147]\n"
      "ds_read_b128 v[152:155], v2 offset:28160\n"
      "v_cndmask_b32 v162, v162, v184, s[62:63]\n"
      "v_mfma_f32_16x16x32_bf16 v[144:147], a[224:227], v[174:177], v[144:147]\n"
      "ds_read_b128 v[182:185], v2 offset:31232\n"
      "v_max_f32_dpp v88, v162, v162 row_ror:8 row_mask:0xf bank_mask:0xf\n"
      "v_mfma_f32_16x16x32_bf16 v[108:111], a[240:243], v[202:205], 0\n"
      "ds_read_b128 v[134:137], v2 offset:34304\n"
      "v_max_f32_dpp v88, v88, v88 row_ror:4 row_mask:0xf bank_mask:0xf\n"
      "v_mfma_f32_16x16x32_bf16 v[108:111], a[244:247], v[210:213], v[108:111]\n"
      "v_cvt_pk_bf16_f32 v141, v192, v193\n"
      "v_max_f32_dpp v88, v88, v88 row_ror:2 row_mask:0xf bank_mask:0xf\n"
      "v_mfma_f32_16x16x32_bf16 v[108:111], a[248:251], v[174:177], v[108:111]\n"
      "ds_write_b128 v3, v[138:141] offset:256\n"
      "v_mfma_f32_16x16x32_bf16 v[138:141], v[232:235], v[202:205], 0\n"
      "s_waitcnt lgkmcnt(3)\n"
      "v_max_f32_dpp v88, v88, v88 row_ror:1 row_mask:0xf bank_mask:0xf\n"
      "v_sub_f32_e64 v206, v206, v90\n"
      "v_mfma_f32_16x16x32_bf16 v[138:141], v[236:239], v[210:213], v[138:141]\n"
      "s_waitcnt lgkmcnt(2)\n"
      "v_max_f32_dpp v88, v88, v88 row_bcast:15 row_mask:0xa bank_mask:0xf\n"
      "v_sub_f32 v207, v207, v90\n"
      "v_mfma_f32_16x16x32_bf16 v[138:141], v[240:243], v[174:177], v[138:141]\n"
      "s_waitcnt lgkmcnt(1)\n"
      "v_max_f32_dpp v88, v88, v88 row_bcast:31 row_mask:0xc bank_mask:0xf\n"
      "v_sub_f32 v208, v208, v90\n"
      "v_mfma_f32_16x16x32_bf16 v[144:147], a[228:231], v[152:155], v[144:147]\n"
      "v_readlane_b32 s72, v88, 63\n"
      "v_sub_f32 v209, v209, v90\n"
      "v_mfma_f32_16x16x32_bf16 v[144:147], a[232:235], v[182:185], v[144:147]\n"
      "v_subrev_f32 v162, s72, v162\n"
      "v_exp_f32 v222, v222\n"
      "v_mfma_f32_16x16x32_bf16 v[144:147], a[236:239], v[134:137], v[144:147]\n"
      "v_exp_f32 v223, v223\n"
      "v_mfma_f32_16x16x32_bf16 v[108:111], a[252:255], v[152:155], v[108:111]\n"
      "v_mul_f32 v120, v188, v120\n"
      "v_mul_f32 v121, v188, v121\n"
      "v_mfma_f32_16x16x32_bf16 v[108:111], v[224:227], v[182:185], v[108:111]\n"
      "v_exp_f32 v162, v162\n"
      "v_mfma_f32_16x16x32_bf16 v[108:111], v[228:231], v[134:137], v[108:111]\n"
      "v_exp_f32_e64 v206, v206\n"
      "v_mfma_f32_16x16x32_bf16 v[138:141], v[244:247], v[152:155], v[138:141]\n"
      "v_fma_f32 v204, v144, v144, s52\n"
      "v_fmac_f32 v204, v145, v145\n"
      "v_mfma_f32_16x16x32_bf16 v[138:141], v[248:251], v[182:185], v[138:141]\n"
      "v_fmac_f32 v204, v146, v146\n"
      "v_fmac_f32 v204, v147, v147\n"
      "v_mfma_f32_16x16x32_bf16 v[138:141], v[252:255], v[134:137], v[138:141]\n"
      "v_fmac_f32 v204, v108, v108\n"
      "v_fmac_f32 v204, v109, v109\n"
      "v_fmac_f32 v204, v110, v110\n"
      "v_fmac_f32 v204, v111, v111\n"
      "v_exp_f32 v207, v207\n"
      "v_exp_f32 v208, v208\n"
      "v_exp_f32 v209, v209\n"
      "v_cvt_pk_bf16_f32 v120, v120, v121\n"
      "v_fmac_f32 v204, v138, v138\n"
      "v_fmac_f32 v204, v139, v139\n"
      "v_fmac_f32 v204, v140, v140\n"
      "v_fmac_f32 v204, v141, v141\n"
      "v_mov_b32 v172, v204\n"
      "v_mul_f32 v122, v188, v122\n"
      "v_mul_f32 v123, v188, v123\n"
      "v_permlane16_swap_b32 v204, v172\n"
      "v_add_f32_e64 v204, v204, v172\n"
      "v_cvt_pk_bf16_f32 v220, v220, v221\n"
      "v_cvt_pk_bf16_f32 v121, v122, v123\n"
      "v_permlane32_swap_b32 v125, v204\n"
      "v_cvt_pk_bf16_f32 v221, v222, v223\n"
      "v_cvt_pk_bf16_f32 v162, v162, v162\n"
      "v_add_f32 v125, v125, v204\n"
      "v_cvt_pk_bf16_f32 v222, v206, v207\n"
      "v_cvt_pk_bf16_f32 v223, v208, v209\n"
      "v_sub_f32 v104, v104, v90\n"
      "v_sub_f32 v105, v105, v90\n"
      "v_mfma_f32_16x16x32_bf16 v[202:205], v[32:35], v[220:223], 0\n"
      "ds_write_b64 v4, v[120:121] offset:256\n"
      "v_sub_f32 v106, v106, v90\n"
      "v_sub_f32 v107, v107, v90\n"
      "v_sub_f32 v100, v100, v90\n"
      "v_exp_f32 v104, v104\n"
      "v_exp_f32 v105, v105\n"
      "v_exp_f32 v106, v106\n"
      "v_exp_f32 v107, v107\n"
      "v_exp_f32 v100, v100\n"
      "v_cvt_pk_bf16_f32 v88, v104, v105\n"
      "v_cvt_pk_bf16_f32 v89, v106, v107\n"
      "v_cvt_pk_bf16_f32 v90, v100, 0\n"
      "v_or_b32_e64 v170, v170, v173\n"
      "v_cmp_ne_u32 s[56:57], 0, v170\n"
      "v_mfma_f32_16x16x32_bf16 v[202:205], v[32:35], v[88:91], v[202:205]\n"
      "ds_write_b16 v13, v162\n"
      "v_mfma_f32_16x16x32_bf16 v[104:107], v[194:197], v[220:223], 0\n"
      "ds_read_b128 v[210:213], v14\n"
      "v_readlane_b32 s47, v38, s43\n"
      "v_mfma_f32_16x16x32_bf16 v[104:107], v[76:79], v[88:91], v[104:107]\n"
      "ds_write_b32 v31, v125\n"
      "v_mfma_f32_16x16x32_bf16 v[206:209], v[130:133], v[220:223], 0\n"
      "ds_read_b128 v[118:121], v14 offset:64\n"
      "v_rcp_f32_e64 v202, v202\n"
      "v_mfma_f32_16x16x32_bf16 v[206:209], v[80:83], v[88:91], v[206:209]\n"
      "s_cmp_ge_u32 s95, 0x40000000\n"
      "s_cselect_b64 vcc, -1, s[56:57]\n"
      "v_add_u32 v12, 32, v12\n"
      "v_mfma_f32_16x16x32_bf16 v[152:155], v[214:217], v[220:223], 0\n"
      "s_and_b32 s47, s47, -4\n"
      "v_mul_f32 v104, v202, v104\n"
      "v_mul_f32 v105, v202, v105\n"
      "v_mfma_f32_16x16x32_bf16 v[152:155], v[84:87], v[88:91], v[152:155]\n"
      "s_add_u32 s43, s43, 1\n"
      "v_cvt_pk_bf16_f32 v104, v104, v105\n"
      "v_mul_f32 v106, v202, v106\n"
      "v_mul_f32 v107, v202, v107\n"
      "v_cvt_pk_bf16_f32 v105, v106, v107\n"
      "v_mul_f32 v206, v202, v206\n"
      "s_waitcnt lgkmcnt(2)\n"
      "v_mfma_f32_16x16x32_bf16 v[188:191], v[210:213], v[32:35], 0\n"
      "v_mul_f32 v207, v202, v207\n"
      "v_mul_f32 v208, v202, v208\n"
      "v_mul_f32_e64 v209, v202, v209\n"
      "v_cvt_pk_bf16_f32 v106, v206, v207\n"
      "v_cvt_pk_bf16_f32 v107, v208, v209\n"
      "s_waitcnt lgkmcnt(0)\n"
      "v_mfma_f32_16x16x32_bf16 v[188:191], v[118:121], v[32:35], v[188:191]\n"
      "ds_write_b128 v3, v[104:107] offset:512\n"
      "v_mul_f32 v152, v202, v152\n"
      "v_mul_f32 v153, v202, v153\n"
      "v_cvt_pk_bf16_f32 v152, v152, v153\n"
      "v_mul_f32_e64 v154, v202, v154\n"
      "v_mul_f32_e64 v155, v202, v155\n"
      "v_cvt_pk_bf16_f32 v153, v154, v155\n"
      "ds_write_b64 v4, v[152:153] offset:512\n"
      "s_waitcnt lgkmcnt(0)\n"
      "s_barrier\n"
      "s_cbranch_vccz L_max1_%=\n"
      "L_cold_%=:\n"
      "L_disp1_%=:\n"
      "s_cmp_lt_u32 s95, 0x40000000\n"
      "s_cbranch_scc1 L_nofix_11_%=\n"
      "s_cmp_eq_u32 s95, -2\n"
      "s_cbranch_scc1 L_tail1_%=\n"
      "s_nop 2\n"
      "v_lshlrev_b32 v202, 3, v36\n"
      "s_add_u32 s72, s98, L_cold_%=-L_ipf0_%=\n"
      "s_addc_u32 s73, s99, 0\n"
      "s_mov_b32 s74, L_cend_%=-L_cold_%=\n"
      "s_mov_b32 s75, 0x20000\n"
      "s_mov_b32 m0, 161792\n"
      "s_nop 0\n"
      "buffer_load_dwordx4 v202, s[72:75], 0 offen  lds\n"
      "s_lshr_b32 s92, s45, 30\n"
      "s_or_b32 s92, s92, s66\n"
      "s_cbranch_scc0 L_nf_12_%=\n"
      "s_bitcmp1_b32 s45, 31\n"
      "s_cselect_b32 s75, 100270464, 0\n"
      "s_bitcmp1_b32 s45, 30\n"
      "s_cselect_b32 s76, 196224, 0\n"
      "s_and_b32 s74, s45, 0x3fffffff\n"
      "s_cmp_ge_u32 s45, 0x40000000\n"
      "s_cselect_b32 s74, s74, 0\n"
      "s_cselect_b32 s45, 0, s45\n"
      "s_sub_u32 s74, s74, s66\n"
      "s_add_u32 s66, s66, s74\n"
      "s_sub_u32 s75, s75, s67\n"
      "s_add_u32 s67, s67, s75\n"
      "s_sub_u32 s76, s76, s68\n"
      "s_add_u32 s68, s68, s76\n"
      "v_add_u32 v16, s74, v16\n"
      "v_add_u32 v17, s74, v17\n"
      "v_add_u32 v18, s74, v18\n"
      "v_add_u32 v19, s74, v19\n"
      "v_add_u32 v15, s74, v15\n"
      "s_cmp_eq_u32 s75, 0\n"
      "s_cbranch_scc1 L_ax_13_%=\n"
      "v_bfe_i32 v202, v27, 0, 1\n"
      "v_and_b32 v202, s75, v202\n"
      "v_sub_u32 v16, v16, v202\n"
      "v_bfe_i32 v202, v27, 2, 1\n"
      "v_and_b32 v202, s75, v202\n"
      "v_sub_u32 v17, v17, v202\n"
      "v_bfe_i32 v202, v27, 4, 1\n"
      "v_and_b32 v202, s75, v202\n"
      "v_sub_u32 v18, v18, v202\n"
      "v_bfe_i32 v202, v27, 6, 1\n"
      "v_and_b32 v202, s75, v202\n"
      "v_sub_u32 v19, v19, v202\n"
      "v_bfe_i32 v202, v27, 8, 1\n"
      "v_and_b32 v202, s75, v202\n"
      "v_sub_u32 v15, v15, v202\n"
      "L_ax_13_%=:\n"
      "s_cmp_eq_u32 s76, 0\n"
      "s_cbranch_scc1 L_ax_14_%=\n"
      "v_bfe_i32 v202, v27, 1, 1\n"
      "v_and_b32 v202, s76, v202\n"
      "v_sub_u32 v16, v16, v202\n"
      "v_bfe_i32 v202, v27, 3, 1\n"
      "v_and_b32 v202, s76, v202\n"
      "v_sub_u32 v17, v17, v202\n"
      "v_bfe_i32 v202, v27, 5, 1\n"
      "v_and_b32 v202, s76, v202\n"
      "v_sub_u32 v18, v18, v202\n"
      "v_bfe_i32 v202, v27, 7, 1\n"
      "v_and_b32 v202, s76, v202\n"
      "v_sub_u32 v19, v19, v202\n"
      "v_bfe_i32 v202, v27, 9, 1\n"
      "v_and_b32 v202, s76, v202\n"
      "v_sub_u32 v15, v15, v202\n"
      "L_ax_14_%=:\n"
      "s_or_b32 s48, s66, s69\n"
      "s_cmp_lg_u32 s48, 0\n"
      "s_cselect_b32 s48, 0x40000000, 0\n"
      "L_nf_12_%=:\n"
      "s_lshr_b32 s92, s46, 30\n"
      "s_or_b32 s92, s92, s69\n"
      "s_cbranch_scc0 L_nf_15_%=\n"
      "s_bitcmp1_b32 s46, 31\n"
      "s_cselect_b32 s75, 100270464, 0\n"
      "s_bitcmp1_b32 s46, 30\n"
      "s_cselect_b32 s76, 196224, 0\n"
      "s_and_b32 s74, s46, 0x3fffffff\n"
      "s_cmp_ge_u32 s46, 0x40000000\n"
      "s_cselect_b32 s74, s74, 0\n"
      "s_cselect_b32 s46, 0, s46\n"
      "s_sub_u32 s74, s74, s69\n"
      "s_add_u32 s69, s69, s74\n"
      "s_sub_u32 s75, s75, s70\n"
      "s_add_u32 s70, s70, s75\n"
      "s_sub_u32 s76, s76, s71\n"
      "s_add_u32 s71, s71, s76\n"
      "v_add_u32 v20, s74, v20\n"
      "v_add_u32 v21, s74, v21\n"
      "v_add_u32 v22, s74, v22\n"
      "v_add_u32 v23, s74, v23\n"
      "v_add_u32 v25, s74, v25\n"
      "s_cmp_eq_u32 s75, 0\n"
      "s_cbranch_scc1 L_ax_16_%=\n"
      "v_bfe_i32 v202, v27, 10, 1\n"
      "v_and_b32 v202, s75, v202\n"
      "v_sub_u32 v20, v20, v202\n"
      "v_bfe_i32 v202, v27, 12, 1\n"
      "v_and_b32 v202, s75, v202\n"
      "v_sub_u32 v21, v21, v202\n"
      "v_bfe_i32 v202, v27, 14, 1\n"
      "v_and_b32 v202, s75, v202\n"
      "v_sub_u32 v22, v22, v202\n"
      "v_bfe_i32 v202, v27, 16, 1\n"
      "v_and_b32 v202, s75, v202\n"
      "v_sub_u32 v23, v23, v202\n"
      "v_bfe_i32 v202, v27, 18, 1\n"
      "v_and_b32 v202, s75, v202\n"
      "v_sub_u32 v25, v25, v202\n"
      "L_ax_16_%=:\n"
      "s_cmp_eq_u32 s76, 0\n"
      "s_cbranch_scc1 L_ax_17_%=\n"
      "v_bfe_i32 v202, v27, 11, 1\n"
      "v_and_b32 v202, s76, v202\n"
      "v_sub_u32 v20, v20, v202\n"
      "v_bfe_i32 v202, v27, 13, 1\n"
      "v_and_b32 v202, s76, v202\n"
      "v_sub_u32 v21, v21, v202\n"
      "v_bfe_i32 v202, v27, 15, 1\n"
      "v_and_b32 v202, s76, v202\n"
      "v_sub_u32 v22, v22, v202\n"
      "v_bfe_i32 v202, v27, 17, 1\n"
      "v_and_b32 v202, s76, v202\n"
      "v_sub_u32 v23, v23, v202\n"
      "v_bfe_i32 v202, v27, 19, 1\n"
      "v_and_b32 v202, s76, v202\n"
      "v_sub_u32 v25, v25, v202\n"
      "L_ax_17_%=:\n"
      "s_or_b32 s48, s66, s69\n"
      "s_cmp_lg_u32 s48, 0\n"
      "s_cselect_b32 s48, 0x40000000, 0\n"
      "L_nf_15_%=:\n"
      "s_sub_u32 s92, s42, 1\n"
      "s_cmp_gt_u32 s92, s44\n"
      "s_cbranch_scc1 L_tail1_%=\n"
      "L_nofix_11_%=:\n"
      "s_cmp_lg_u64 s[56:57], 0\n"
      "s_cbranch_scc1 L_slow1_%=\n"
      "L_sel1_%=:\n"
      "s_cmp_lg_u64 s[54:55], 0\n"
      "s_cbranch_scc1 L_max1_%=\n"
      "s_branch L_loop1_%=\n"
      "L_slow1_%=:\n"
      "s_nop 4\n"
      "v_mfma_f32_16x16x32_bf16 v[202:205], v[210:213], v[194:197], 0\n"
      "ds_read_b128 v[134:137], v0 offset:18432\n"
      "v_subrev_u32 v194, 115456, v9\n"
      "v_mfma_f32_16x16x32_bf16 v[202:205], v[118:121], v[76:79], v[202:205]\n"
      "ds_read_b128 v[198:201], v1 offset:18432\n"
      "v_subrev_u32 v76, 115456, v9\n"
      "v_mfma_f32_16x16x32_bf16 v[104:107], v[210:213], v[130:133], 0\n"
      "ds_read_b128 v[162:165], v0 offset:24576\n"
      "v_bfe_u32 v77, v76, 4, 4\n"
      "v_mfma_f32_16x16x32_bf16 v[104:107], v[118:121], v[80:83], v[104:107]\n"
      "ds_read_b128 v[206:209], v9\n"
      "v_mul_u32_u24 v78, 98, v77\n"
      "v_mfma_f32_16x16x32_bf16 v[174:177], v[210:213], v[214:217], 0\n"
      "s_add_u32 s72, s42, 1\n"
      "v_readlane_b32 s77, v38, s72\n"
      "v_lshrrev_b32 v76, 8, v76\n"
      "v_mfma_f32_16x16x32_bf16 v[174:177], v[118:121], v[84:87], v[174:177]\n"
      "s_mov_b32 m0, 110592\n"
      "v_mov_b32_dpp v188, v188 quad_perm:[0,0,0,0] row_mask:0xf bank_mask:0xf\n"
      "v_rcp_f32_e64 v188, v188\n"
      "s_waitcnt lgkmcnt(3)\n"
      "v_mfma_f32_16x16x32_bf16 v[210:213], a[0:3], v[134:137], 0\n"
      "s_and_b32 s73, s77, -16\n"
      "v_cndmask_b32 v202, v202, v104, s[58:59]\n"
      "v_lshl_add_u32 v78, v76, 3, v78\n"
      "v_mfma_f32_16x16x32_bf16 v[150:153], a[24:27], v[134:137], 0\n"
      "buffer_load_dwordx4 v194, s[28:31], s73 offen  lds\n"
      "v_mfma_f32_16x16x32_bf16 v[104:107], a[48:51], v[134:137], 0\n"
      "s_mov_b32 m0, 111616\n"
      "s_waitcnt lgkmcnt(0)\n"
      "v_add_f32 v206, v206, v207\n"
      "v_mfma_f32_16x16x32_bf16 v[118:121], a[72:75], v[134:137], 0\n"
      "ds_read_b128 v[184:187], v1 offset:24576\n"
      "v_add_f32 v208, v208, v209\n"
      "v_mfma_f32_16x16x32_bf16 v[154:157], a[96:99], v[134:137], 0\n"
      "ds_read_b128 v[100:103], v0 offset:30720\n"
      "v_add_f32 v206, v206, v208\n"
      "v_mfma_f32_16x16x32_bf16 v[170:173], a[120:123], v[134:137], 0\n"
      "ds_read_b128 v[122:125], v1 offset:30720\n"
      "v_rsq_f32_e64 v206, v206\n"
      "v_mfma_f32_16x16x32_bf16 v[210:213], a[4:7], v[198:201], v[210:213]\n"
      "s_add_u32 s73, s73, 1024\n"
      "v_mul_f32 v206, 0x415db3d7, v206\n"
      "v_mov_b32_e64 v208, v206\n"
      "v_mfma_f32_16x16x32_bf16 v[210:213], a[8:11], v[162:165], v[210:213]\n"
      "buffer_load_dwordx4 v194, s[28:31], s73 offen  lds\n"
      "v_mfma_f32_16x16x32_bf16 v[150:153], a[28:31], v[198:201], v[150:153]\n"
      "s_mov_b32 m0, 112640\n"
      "v_permlane32_swap_b32_e64 v206, v208\n"
      "v_mfma_f32_16x16x32_bf16 v[150:153], a[32:35], v[162:165], v[150:153]\n"
      "s_add_u32 s73, s73, 1024\n"
      "v_mov_b32 v207, v206\n"
      "v_mul_f32 v144, v208, v144\n"
      "v_mfma_f32_16x16x32_bf16 v[104:107], a[52:55], v[198:201], v[104:107]\n"
      "s_and_b32 s77, s77, 15\n"
      "v_permlane16_swap_b32 v206, v207\n"
      "v_mfma_f32_16x16x32_bf16 v[104:107], a[56:59], v[162:165], v[104:107]\n"
      "buffer_load_dwordx4 v194, s[28:31], s73 offen  lds\n"
      "v_mfma_f32_16x16x32_bf16 v[118:121], a[76:79], v[198:201], v[118:121]\n"
      "s_mov_b32 m0, 113664\n"
      "v_mul_f32 v158, v206, v158\n"
      "v_mul_f32 v159, v206, v159\n"
      "v_mfma_f32_16x16x32_bf16 v[118:121], a[80:83], v[162:165], v[118:121]\n"
      "s_add_u32 s73, s73, 1024\n"
      "v_cvt_pk_bf16_f32 v158, v158, v159\n"
      "v_mul_f32 v160, v206, v160\n"
      "v_mfma_f32_16x16x32_bf16 v[154:157], a[100:103], v[198:201], v[154:157]\n"
      "s_waitcnt lgkmcnt(0)\n"
      "v_mul_f32 v161, v206, v161\n"
      "v_mul_f32 v114, v206, v114\n"
      "v_mfma_f32_16x16x32_bf16 v[154:157], a[104:107], v[162:165], v[154:157]\n"
      "buffer_load_dwordx4 v194, s[28:31], s73 offen  lds\n"
      "v_mfma_f32_16x16x32_bf16 v[170:173], a[124:127], v[198:201], v[170:173]\n"
      "s_mov_b32 m0, 114688\n"
      "v_mul_f32 v115, v206, v115\n"
      "v_cvt_pk_bf16_f32 v159, v160, v161\n"
      "v_mfma_f32_16x16x32_bf16 v[170:173], a[128:131], v[162:165], v[170:173]\n"
      "s_add_u32 s73, s73, 1024\n"
      "v_mul_f32 v96, v206, v96\n"
      "v_mul_f32 v97, v206, v97\n"
      "v_mfma_f32_16x16x32_bf16 v[210:213], a[12:15], v[184:187], v[210:213]\n"
      "v_mul_f32 v98, v206, v98\n"
      "v_mul_f32_e64 v99, v206, v99\n"
      "v_mfma_f32_16x16x32_bf16 v[210:213], a[16:19], v[100:103], v[210:213]\n"
      "s_mov_b32 exec_hi, 0xffff\n"
      "buffer_load_dwordx4 v194, s[28:31], s73 offen  lds\n"
      "s_mov_b32 exec_hi, -1\n"
      "v_mfma_f32_16x16x32_bf16 v[210:213], a[20:23], v[122:125], v[210:213]\n"
      "s_add_u32 m0, s64, 0\n"
      "v_cvt_pk_bf16_f32 v114, v114, v115\n"
      "v_mul_f32 v116, v206, v116\n"
      "v_mfma_f32_16x16x32_bf16 v[150:153], a[36:39], v[184:187], v[150:153]\n"
      "s_add_u32 s73, s73, 1024\n"
      "v_mul_f32 v117, v206, v117\n"
      "v_cvt_pk_bf16_f32 v160, v96, v97\n"
      "v_mfma_f32_16x16x32_bf16 v[150:153], a[40:43], v[100:103], v[150:153]\n"
      "v_cvt_pk_bf16_f32 v161, v98, v99\n"
      "v_cvt_pk_bf16_f32 v115, v116, v117\n"
      "v_mfma_f32_16x16x32_bf16 v[150:153], a[44:47], v[122:125], v[150:153]\n"
      "ds_write_b128 v5, v[158:161] offset:18432\n"
      "v_mfma_f32_16x16x32_bf16 v[104:107], a[60:63], v[184:187], v[104:107]\n"
      "buffer_load_dwordx4 v16, s[16:19], s45 offen  lds\n"
      "v_mfma_f32_16x16x32_bf16 v[104:107], a[64:67], v[100:103], v[104:107]\n"
      "s_add_u32 m0, s64, 4096\n"
      "v_mul_f32 v178, v207, v178\n"
      "v_mul_f32 v179, v207, v179\n"
      "v_mfma_f32_16x16x32_bf16 v[104:107], a[68:71], v[122:125], v[104:107]\n"
      "ds_write_b64 v6, v[114:115] offset:18432\n"
      "v_mfma_f32_16x16x32_bf16 v[118:121], a[84:87], v[184:187], v[118:121]\n"
      "v_cvt_pk_bf16_f32 v178, v178, v179\n"
      "v_mul_f32_e64 v180, v207, v180\n"
      "v_mfma_f32_16x16x32_bf16 v[118:121], a[88:91], v[100:103], v[118:121]\n"
      "buffer_load_dwordx4 v17, s[16:19], s45 offen  lds\n"
      "v_mfma_f32_16x16x32_bf16 v[118:121], a[92:95], v[122:125], v[118:121]\n"
      "s_add_u32 m0, s64, 8192\n"
      "v_mul_f32 v181, v207, v181\n"
      "v_mul_f32 v126, v207, v126\n"
      "v_mfma_f32_16x16x32_bf16 v[154:157], a[108:111], v[184:187], v[154:157]\n"
      "v_mul_f32_e64 v127, v207, v127\n"
      "v_cvt_pk_bf16_f32 v179, v180, v181\n"
      "v_mfma_f32_16x16x32_bf16 v[154:157], a[112:115], v[100:103], v[154:157]\n"
      "v_mul_f32 v166, v207, v166\n"
      "v_mul_f32 v167, v207, v167\n"
      "v_mfma_f32_16x16x32_bf16 v[154:157], a[116:119], v[122:125], v[154:157]\n"
      "buffer_load_dwordx4 v18, s[16:19], s45 offen  lds\n"
      "v_mfma_f32_16x16x32_bf16 v[170:173], a[132:135], v[184:187], v[170:173]\n"
      "s_add_u32 m0, s64, 12288\n"
      "v_mul_f32 v168, v207, v168\n"
      "v_mul_f32 v169, v207, v169\n"
      "v_mfma_f32_16x16x32_bf16 v[170:173], a[136:139], v[100:103], v[170:173]\n"
      "v_cvt_pk_bf16_f32 v126, v126, v127\n"
      "v_mul_f32 v128, v207, v128\n"
      "v_mfma_f32_16x16x32_bf16 v[170:173], a[140:143], v[122:125], v[170:173]\n"
      "v_mul_f32 v129, v207, v129\n"
      "v_cvt_pk_bf16_f32 v180, v166, v167\n"
      "v_mfma_f32_16x16x32_bf16 v[194:197], v[134:137], a[144:147], 0\n"
      "buffer_load_dwordx4 v19, s[16:19], s45 offen  lds\n"
      "v_mfma_f32_16x16x32_bf16 v[194:197], v[198:201], a[148:151], v[194:197]\n"
      "s_add_u32 m0, s65, 16384\n"
      "v_cvt_pk_bf16_f32 v181, v168, v169\n"
      "v_cvt_pk_bf16_f32 v127, v128, v129\n"
      "v_mfma_f32_16x16x32_bf16 v[194:197], v[162:165], a[152:155], v[194:197]\n"
      "ds_write_b128 v5, v[178:181] offset:24576\n"
      "v_mfma_f32_16x16x32_bf16 v[194:197], v[184:187], a[156:159], v[194:197]\n"
      "buffer_load_dwordx4 v15, s[16:19], s45 offen  lds\n"
      "v_mfma_f32_16x16x32_bf16 v[194:197], v[100:103], a[160:163], v[194:197]\n"
      "ds_read_b128 v[190:193], v0 offset:20480\n"
      "v_mul_f32_e64 v145, v208, v145\n"
      "v_mfma_f32_16x16x32_bf16 v[194:197], v[122:125], a[164:167], v[194:197]\n"
      "ds_write_b64 v6, v[126:127] offset:24576\n"
      "v_mfma_f32_16x16x32_bf16 v[130:133], v[134:137], a[168:171], 0\n"
      "ds_read_b128 v[126:129], v1 offset:20480\n"
      "v_cvt_pk_bf16_f32 v144, v144, v145\n"
      "v_mfma_f32_16x16x32_bf16 v[130:133], v[198:201], a[172:175], v[130:133]\n"
      "ds_read_b128 v[180:183], v0 offset:26624\n"
      "v_mul_f32 v146, v208, v146\n"
      "v_mfma_f32_16x16x32_bf16 v[130:133], v[162:165], a[176:179], v[130:133]\n"
      "ds_read_b128 v[220:223], v1 offset:26624\n"
      "v_mul_f32 v147, v208, v147\n"
      "v_mfma_f32_16x16x32_bf16 v[130:133], v[184:187], a[180:183], v[130:133]\n"
      "ds_read_b128 v[176:179], v0 offset:32768\n"
      "v_mul_f32 v138, v208, v138\n"
      "v_mfma_f32_16x16x32_bf16 v[130:133], v[100:103], a[184:187], v[130:133]\n"
      "ds_read_b128 v[114:117], v1 offset:32768\n"
      "v_mul_f32 v139, v208, v139\n"
      "v_mfma_f32_16x16x32_bf16 v[130:133], v[122:125], a[188:191], v[130:133]\n"
      "buffer_load_dwordx4 v[204:207], v36, s[28:31], s47 offen\n"
      "v_mfma_f32_16x16x32_bf16 v[214:217], v[134:137], a[192:195], 0\n"
      "v_cvt_pk_bf16_f32 v145, v146, v147\n"
      "v_mul_f32 v108, v208, v108\n"
      "v_mfma_f32_16x16x32_bf16 v[214:217], v[198:201], a[196:199], v[214:217]\n"
      "v_mul_f32 v109, v208, v109\n"
      "v_mul_f32 v110, v208, v110\n"
      "v_mfma_f32_16x16x32_bf16 v[214:217], v[162:165], a[200:203], v[214:217]\n"
      "s_waitcnt lgkmcnt(3)\n"
      "v_mul_f32_e64 v111, v208, v111\n"
      "v_cvt_pk_bf16_f32 v138, v138, v139\n"
      "v_mfma_f32_16x16x32_bf16 v[214:217], v[184:187], a[204:207], v[214:217]\n"
      "buffer_load_dword v82, v29, s[28:31], s47 offen\n"
      "v_mfma_f32_16x16x32_bf16 v[214:217], v[100:103], a[208:211], v[214:217]\n"
      "v_mul_f32 v140, v208, v140\n"
      "v_mul_f32_e64 v141, v208, v141\n"
      "v_mfma_f32_16x16x32_bf16 v[214:217], v[122:125], a[212:215], v[214:217]\n"
      "s_waitcnt lgkmcnt(0)\n"
      "v_cvt_pk_bf16_f32 v146, v108, v109\n"
      "v_cvt_pk_bf16_f32 v147, v110, v111\n"
      "v_mfma_f32_16x16x32_bf16 v[134:137], a[0:3], v[190:193], 0\n"
      "ds_write_b128 v5, v[144:147] offset:30720\n"
      "v_mfma_f32_16x16x32_bf16 v[134:137], a[4:7], v[126:129], v[134:137]\n"
      "v_cvt_pk_bf16_f32 v139, v140, v141\n"
      "v_fma_f32 v100, v210, v210, s51\n"
      "v_mfma_f32_16x16x32_bf16 v[134:137], a[8:11], v[180:183], v[134:137]\n"
      "v_fma_f32 v101, v118, v118, s51\n"
      "v_fmac_f32_e64 v100, v211, v211\n"
      "v_mfma_f32_16x16x32_bf16 v[134:137], a[12:15], v[220:223], v[134:137]\n"
      "ds_write_b64 v6, v[138:139] offset:30720\n"
      "v_mfma_f32_16x16x32_bf16 v[134:137], a[16:19], v[176:179], v[134:137]\n"
      "v_fmac_f32 v101, v119, v119\n"
      "v_fmac_f32 v100, v212, v212\n"
      "v_mfma_f32_16x16x32_bf16 v[134:137], a[20:23], v[114:117], v[134:137]\n"
      "v_fmac_f32 v101, v120, v120\n"
      "v_fmac_f32 v100, v213, v213\n"
      "v_mfma_f32_16x16x32_bf16 v[142:145], a[24:27], v[190:193], 0\n"
      "v_fmac_f32 v101, v121, v121\n"
      "v_fmac_f32 v100, v150, v150\n"
      "v_mfma_f32_16x16x32_bf16 v[142:145], a[28:31], v[126:129], v[142:145]\n"
      "v_fmac_f32 v101, v154, v154\n"
      "v_fmac_f32 v100, v151, v151\n"
      "v_mfma_f32_16x16x32_bf16 v[142:145], a[32:35], v[180:183], v[142:145]\n"
      "v_fmac_f32 v101, v155, v155\n"
      "v_fmac_f32 v100, v152, v152\n"
      "v_mfma_f32_16x16x32_bf16 v[142:145], a[36:39], v[220:223], v[142:145]\n"
      "v_fmac_f32 v101, v156, v156\n"
      "v_fmac_f32 v100, v153, v153\n"
      "v_mfma_f32_16x16x32_bf16 v[142:145], a[40:43], v[176:179], v[142:145]\n"
      "v_fmac_f32 v101, v157, v157\n"
      "v_fmac_f32 v100, v104, v104\n"
      "v_mfma_f32_16x16x32_bf16 v[142:145], a[44:47], v[114:117], v[142:145]\n"
      "v_fmac_f32 v101, v170, v170\n"
      "v_fmac_f32 v100, v105, v105\n"
      "v_mfma_f32_16x16x32_bf16 v[110:113], a[48:51], v[190:193], 0\n"
      "v_fmac_f32 v101, v171, v171\n"
      "v_fmac_f32 v100, v106, v106\n"
      "v_mfma_f32_16x16x32_bf16 v[110:113], a[52:55], v[126:129], v[110:113]\n"
      "v_fmac_f32 v101, v172, v172\n"
      "v_fmac_f32 v100, v107, v107\n"
      "v_mfma_f32_16x16x32_bf16 v[110:113], a[56:59], v[180:183], v[110:113]\n"
      "v_fmac_f32 v101, v173, v173\n"
      "v_cndmask_b32 v202, v202, v174, s[60:61]\n"
      "v_mfma_f32_16x16x32_bf16 v[110:113], a[60:63], v[220:223], v[110:113]\n"
      "v_permlane16_swap_b32 v100, v101\n"
      "v_mfma_f32_16x16x32_bf16 v[110:113], a[64:67], v[176:179], v[110:113]\n"
      "v_add_f32 v100, v100, v101\n"
      "v_mov_b32 v101, v100\n"
      "v_mfma_f32_16x16x32_bf16 v[110:113], a[68:71], v[114:117], v[110:113]\n"
      "v_mul_f32 v202, v188, v202\n"
      "v_permlane32_swap_b32 v100, v101\n"
      "v_mfma_f32_16x16x32_bf16 v[122:125], a[72:75], v[190:193], 0\n"
      "v_add_f32 v100, v100, v101\n"
      "v_rsq_f32 v100, v100\n"
      "v_mfma_f32_16x16x32_bf16 v[122:125], a[76:79], v[126:129], v[122:125]\n"
      "v_mov_b32_e64 v101, v100\n"
      "v_cvt_pk_bf16_f32 v202, v202, v202\n"
      "v_mfma_f32_16x16x32_bf16 v[122:125], a[80:83], v[180:183], v[122:125]\n"
      "ds_write_b16 v12, v202\n"
      "v_mfma_f32_16x16x32_bf16 v[122:125], a[84:87], v[220:223], v[122:125]\n"
      "v_permlane16_swap_b32 v100, v101\n"
      "v_mfma_f32_16x16x32_bf16 v[122:125], a[88:91], v[176:179], v[122:125]\n"
      "v_mul_f32 v100, s50, v100\n"
      "v_mul_f32 v118, v101, v118\n"
      "v_mfma_f32_16x16x32_bf16 v[122:125], a[92:95], v[114:117], v[122:125]\n"
      "v_mul_f32 v119, v101, v119\n"
      "v_mul_f32 v210, v100, v210\n"
      "v_mfma_f32_16x16x32_bf16 v[146:149], a[96:99], v[190:193], 0\n"
      "v_mul_f32 v211, v100, v211\n"
      "v_cvt_pk_bf16_f32 v118, v118, v119\n"
      "v_mfma_f32_16x16x32_bf16 v[146:149], a[100:103], v[126:129], v[146:149]\n"
      "v_mul_f32 v120, v101, v120\n"
      "v_mul_f32 v121, v101, v121\n"
      "v_mfma_f32_16x16x32_bf16 v[146:149], a[104:107], v[180:183], v[146:149]\n"
      "v_mul_f32 v170, v101, v170\n"
      "v_mul_f32 v171, v101, v171\n"
      "v_mfma_f32_16x16x32_bf16 v[146:149], a[108:111], v[220:223], v[146:149]\n"
      "v_cvt_pk_bf16_f32 v210, v210, v211\n"
      "v_mul_f32 v212, v100, v212\n"
      "v_mfma_f32_16x16x32_bf16 v[146:149], a[112:115], v[176:179], v[146:149]\n"
      "v_mul_f32 v213, v100, v213\n"
      "v_mul_f32_e64 v104, v100, v104\n"
      "v_mfma_f32_16x16x32_bf16 v[146:149], a[116:119], v[114:117], v[146:149]\n"
      "v_mul_f32 v105, v100, v105\n"
      "v_cvt_pk_bf16_f32 v119, v120, v121\n"
      "v_mfma_f32_16x16x32_bf16 v[198:201], a[120:123], v[190:193], 0\n"
      "v_mul_f32 v154, v101, v154\n"
      "v_mul_f32 v155, v101, v155\n"
      "v_mfma_f32_16x16x32_bf16 v[198:201], a[124:127], v[126:129], v[198:201]\n"
      "v_mul_f32 v156, v101, v156\n"
      "v_mul_f32_e64 v157, v101, v157\n"
      "v_mfma_f32_16x16x32_bf16 v[198:201], a[128:131], v[180:183], v[198:201]\n"
      "v_cvt_pk_bf16_f32 v170, v170, v171\n"
      "v_mul_f32 v172, v101, v172\n"
      "v_mfma_f32_16x16x32_bf16 v[198:201], a[132:135], v[220:223], v[198:201]\n"
      "v_mul_f32 v173, v101, v173\n"
      "v_cvt_pk_bf16_f32 v211, v212, v213\n"
      "v_mfma_f32_16x16x32_bf16 v[198:201], a[136:139], v[176:179], v[198:201]\n"
      "v_mul_f32 v150, v100, v150\n"
      "v_mul_f32 v151, v100, v151\n"
      "v_mfma_f32_16x16x32_bf16 v[198:201], a[140:143], v[114:117], v[198:201]\n"
      "v_mul_f32 v152, v100, v152\n"
      "v_mul_f32 v153, v100, v153\n"
      "v_mfma_f32_16x16x32_bf16 v[96:99], v[190:193], a[144:147], 0\n"
      "v_cvt_pk_bf16_f32 v104, v104, v105\n"
      "v_mul_f32 v106, v100, v106\n"
      "v_mfma_f32_16x16x32_bf16 v[96:99], v[126:129], a[148:151], v[96:99]\n"
      "v_mul_f32 v107, v100, v107\n"
      "v_cvt_pk_bf16_f32 v120, v154, v155\n"
      "v_mfma_f32_16x16x32_bf16 v[96:99], v[180:183], a[152:155], v[96:99]\n"
      "v_cvt_pk_bf16_f32 v121, v156, v157\n"
      "v_cvt_pk_bf16_f32 v171, v172, v173\n"
      "v_mfma_f32_16x16x32_bf16 v[96:99], v[220:223], a[156:159], v[96:99]\n"
      "ds_read_b128 v[100:103], v0 offset:22528\n"
      "v_cvt_pk_bf16_f32 v212, v150, v151\n"
      "v_mfma_f32_16x16x32_bf16 v[96:99], v[176:179], a[160:163], v[96:99]\n"
      "ds_read_b128 v[166:169], v1 offset:22528\n"
      "v_cvt_pk_bf16_f32 v213, v152, v153\n"
      "v_mfma_f32_16x16x32_bf16 v[96:99], v[114:117], a[164:167], v[96:99]\n"
      "ds_read_b128 v[172:175], v0 offset:28672\n"
      "v_cvt_pk_bf16_f32 v105, v106, v107\n"
      "v_mfma_f32_16x16x32_bf16 v[154:157], v[190:193], a[168:171], 0\n"
      "ds_read_b128 v[106:109], v1 offset:28672\n"
      "v_fma_f32 v202, v134, v134, s51\n"
      "v_mfma_f32_16x16x32_bf16 v[154:157], v[126:129], a[172:175], v[154:157]\n"
      "ds_read_b128 v[158:161], v0 offset:34816\n"
      "v_fma_f32 v188, v122, v122, s51\n"
      "v_mfma_f32_16x16x32_bf16 v[154:157], v[180:183], a[176:179], v[154:157]\n"
      "ds_read_b128 v[138:141], v1 offset:34816\n"
      "v_fmac_f32 v202, v135, v135\n"
      "v_mfma_f32_16x16x32_bf16 v[154:157], v[220:223], a[180:183], v[154:157]\n"
      "s_waitcnt vmcnt(7)\n"
      "v_fmac_f32 v188, v123, v123\n"
      "v_fmac_f32 v202, v136, v136\n"
      "v_mfma_f32_16x16x32_bf16 v[154:157], v[176:179], a[184:187], v[154:157]\n"
      "v_fmac_f32 v188, v124, v124\n"
      "v_fmac_f32 v202, v137, v137\n"
      "v_mfma_f32_16x16x32_bf16 v[154:157], v[114:117], a[188:191], v[154:157]\n"
      "v_fmac_f32 v188, v125, v125\n"
      "v_fmac_f32 v202, v142, v142\n"
      "v_mfma_f32_16x16x32_bf16 v[150:153], v[190:193], a[192:195], 0\n"
      "v_fmac_f32 v188, v146, v146\n"
      "v_fmac_f32 v202, v143, v143\n"
      "v_mfma_f32_16x16x32_bf16 v[150:153], v[126:129], a[196:199], v[150:153]\n"
      "v_fmac_f32 v188, v147, v147\n"
      "v_fmac_f32 v202, v144, v144\n"
      "v_mfma_f32_16x16x32_bf16 v[150:153], v[180:183], a[200:203], v[150:153]\n"
      "s_waitcnt lgkmcnt(0)\n"
      "v_fmac_f32 v188, v148, v148\n"
      "v_fmac_f32 v202, v145, v145\n"
      "v_mfma_f32_16x16x32_bf16 v[150:153], v[220:223], a[204:207], v[150:153]\n"
      "v_fmac_f32 v188, v149, v149\n"
      "v_fmac_f32 v202, v110, v110\n"
      "v_mfma_f32_16x16x32_bf16 v[150:153], v[176:179], a[208:211], v[150:153]\n"
      "v_fmac_f32 v188, v198, v198\n"
      "v_fmac_f32 v202, v111, v111\n"
      "v_mfma_f32_16x16x32_bf16 v[150:153], v[114:117], a[212:215], v[150:153]\n"
      "v_fmac_f32 v188, v199, v199\n"
      "v_fmac_f32 v202, v112, v112\n"
      "v_mfma_f32_16x16x32_bf16 v[162:165], a[0:3], v[100:103], 0\n"
      "v_fmac_f32 v188, v200, v200\n"
      "v_fmac_f32 v202, v113, v113\n"
      "v_mfma_f32_16x16x32_bf16 v[162:165], a[4:7], v[166:169], v[162:165]\n"
      "v_fmac_f32 v188, v201, v201\n"
      "v_add_u32 v78, s77, v78\n"
      "v_mfma_f32_16x16x32_bf16 v[162:165], a[8:11], v[172:175], v[162:165]\n"
      "v_permlane16_swap_b32 v202, v188\n"
      "v_mfma_f32_16x16x32_bf16 v[162:165], a[12:15], v[106:109], v[162:165]\n"
      "v_add_f32 v202, v202, v188\n"
      "v_mov_b32 v188, v202\n"
      "v_mfma_f32_16x16x32_bf16 v[162:165], a[16:19], v[158:161], v[162:165]\n"
      "v_and_b32 v93, 2, v78\n"
      "v_permlane32_swap_b32 v202, v188\n"
      "v_mfma_f32_16x16x32_bf16 v[162:165], a[20:23], v[138:141], v[162:165]\n"
      "v_add_f32 v202, v202, v188\n"
      "v_rsq_f32 v202, v202\n"
      "v_mfma_f32_16x16x32_bf16 v[180:183], a[24:27], v[100:103], 0\n"
      "v_mov_b32 v188, v202\n"
      "v_and_b32 v78, -4, v78\n"
      "v_mfma_f32_16x16x32_bf16 v[180:183], a[28:31], v[166:169], v[180:183]\n"
      "v_permlane16_swap_b32_e64 v202, v188\n"
      "v_mfma_f32_16x16x32_bf16 v[180:183], a[32:35], v[172:175], v[180:183]\n"
      "v_mul_f32 v202, s50, v202\n"
      "v_mul_f32 v122, v188, v122\n"
      "v_mfma_f32_16x16x32_bf16 v[180:183], a[36:39], v[106:109], v[180:183]\n"
      "v_mul_f32 v123, v188, v123\n"
      "v_mul_f32 v134, v202, v134\n"
      "v_mfma_f32_16x16x32_bf16 v[180:183], a[40:43], v[158:161], v[180:183]\n"
      "v_mul_f32 v135, v202, v135\n"
      "v_cvt_pk_bf16_f32 v122, v122, v123\n"
      "v_mfma_f32_16x16x32_bf16 v[180:183], a[44:47], v[138:141], v[180:183]\n"
      "v_mul_f32 v124, v188, v124\n"
      "v_mul_f32 v125, v188, v125\n"
      "v_mfma_f32_16x16x32_bf16 v[176:179], a[48:51], v[100:103], 0\n"
      "v_mul_f32 v198, v188, v198\n"
      "v_mul_f32_e64 v199, v188, v199\n"
      "v_mfma_f32_16x16x32_bf16 v[176:179], a[52:55], v[166:169], v[176:179]\n"
      "v_cvt_pk_bf16_f32 v134, v134, v135\n"
      "v_mul_f32 v136, v202, v136\n"
      "v_mfma_f32_16x16x32_bf16 v[176:179], a[56:59], v[172:175], v[176:179]\n"
      "v_mul_f32 v137, v202, v137\n"
      "v_mul_f32_e64 v110, v202, v110\n"
      "v_mfma_f32_16x16x32_bf16 v[176:179], a[60:63], v[106:109], v[176:179]\n"
      "v_mul_f32 v111, v202, v111\n"
      "v_cvt_pk_bf16_f32 v123, v124, v125\n"
      "v_mfma_f32_16x16x32_bf16 v[176:179], a[64:67], v[158:161], v[176:179]\n"
      "v_mul_f32 v146, v188, v146\n"
      "v_mul_f32 v147, v188, v147\n"
      "v_mfma_f32_16x16x32_bf16 v[176:179], a[68:71], v[138:141], v[176:179]\n"
      "v_mul_f32 v148, v188, v148\n"
      "v_mul_f32_e64 v149, v188, v149\n"
      "v_mfma_f32_16x16x32_bf16 v[190:193], a[72:75], v[100:103], 0\n"
      "v_cvt_pk_bf16_f32 v198, v198, v199\n"
      "v_mul_f32 v200, v188, v200\n"
      "v_mfma_f32_16x16x32_bf16 v[190:193], a[76:79], v[166:169], v[190:193]\n"
      "v_mul_f32 v201, v188, v201\n"
      "v_cvt_pk_bf16_f32 v135, v136, v137\n"
      "v_mfma_f32_16x16x32_bf16 v[190:193], a[80:83], v[172:175], v[190:193]\n"
      "v_mul_f32 v142, v202, v142\n"
      "v_mul_f32 v143, v202, v143\n"
      "v_mfma_f32_16x16x32_bf16 v[190:193], a[84:87], v[106:109], v[190:193]\n"
      "v_mul_f32 v144, v202, v144\n"
      "v_mul_f32 v145, v202, v145\n"
      "v_mfma_f32_16x16x32_bf16 v[190:193], a[88:91], v[158:161], v[190:193]\n"
      "v_cvt_pk_bf16_f32 v110, v110, v111\n"
      "v_mul_f32 v112, v202, v112\n"
      "v_mfma_f32_16x16x32_bf16 v[190:193], a[92:95], v[138:141], v[190:193]\n"
      "v_mul_f32 v113, v202, v113\n"
      "v_cvt_pk_bf16_f32 v124, v146, v147\n"
      "v_mfma_f32_16x16x32_bf16 v[114:117], a[96:99], v[100:103], 0\n"
      "v_cvt_pk_bf16_f32 v125, v148, v149\n"
      "v_cvt_pk_bf16_f32 v199, v200, v201\n"
      "v_mfma_f32_16x16x32_bf16 v[114:117], a[100:103], v[166:169], v[114:117]\n"
      "v_cvt_pk_bf16_f32 v136, v142, v143\n"
      "v_cvt_pk_bf16_f32 v137, v144, v145\n"
      "v_mfma_f32_16x16x32_bf16 v[114:117], a[104:107], v[172:175], v[114:117]\n"
      "v_cvt_pk_bf16_f32 v111, v112, v113\n"
      "v_fma_f32 v113, v162, v162, s51\n"
      "v_mfma_f32_16x16x32_bf16 v[114:117], a[108:111], v[106:109], v[114:117]\n"
      "v_fma_f32 v112, v190, v190, s51\n"
      "v_fmac_f32 v113, v163, v163\n"
      "v_mfma_f32_16x16x32_bf16 v[114:117], a[112:115], v[158:161], v[114:117]\n"
      "v_fmac_f32 v112, v191, v191\n"
      "v_fmac_f32 v113, v164, v164\n"
      "v_mfma_f32_16x16x32_bf16 v[114:117], a[116:119], v[138:141], v[114:117]\n"
      "v_fmac_f32 v112, v192, v192\n"
      "v_fmac_f32 v113, v165, v165\n"
      "v_mfma_f32_16x16x32_bf16 v[218:221], a[120:123], v[100:103], 0\n"
      "v_fmac_f32 v112, v193, v193\n"
      "v_fmac_f32 v113, v180, v180\n"
      "v_mfma_f32_16x16x32_bf16 v[218:221], a[124:127], v[166:169], v[218:221]\n"
      "v_fmac_f32 v113, v181, v181\n"
      "v_fmac_f32 v113, v182, v182\n"
      "v_mfma_f32_16x16x32_bf16 v[218:221], a[128:131], v[172:175], v[218:221]\n"
      "v_fmac_f32 v112, v114, v114\n"
      "v_fmac_f32 v112, v115, v115\n"
      "v_mfma_f32_16x16x32_bf16 v[218:221], a[132:135], v[106:109], v[218:221]\n"
      "v_fmac_f32 v112, v116, v116\n"
      "v_fmac_f32 v113, v183, v183\n"
      "v_mfma_f32_16x16x32_bf16 v[218:221], a[136:139], v[158:161], v[218:221]\n"
      "v_fmac_f32 v112, v117, v117\n"
      "v_fmac_f32 v113, v176, v176\n"
      "v_mfma_f32_16x16x32_bf16 v[218:221], a[140:143], v[138:141], v[218:221]\n"
      "v_fmac_f32 v113, v177, v177\n"
      "v_fmac_f32 v113, v178, v178\n"
      "v_mfma_f32_16x16x32_bf16 v[126:129], v[100:103], a[144:147], 0\n"
      "v_fmac_f32 v113, v179, v179\n"
      "v_add_u32 v86, 110592, v78\n"
      "v_mfma_f32_16x16x32_bf16 v[126:129], v[166:169], a[148:151], v[126:129]\n"
      "v_lshlrev_b32 v76, 2, v76\n"
      "v_sub_u32 v77, v77, v76\n"
      "v_mfma_f32_16x16x32_bf16 v[126:129], v[172:175], a[152:155], v[126:129]\n"
      "v_fmac_f32 v112, v218, v218\n"
      "v_fmac_f32 v112, v219, v219\n"
      "v_mfma_f32_16x16x32_bf16 v[126:129], v[106:109], a[156:159], v[126:129]\n"
      "ds_read_b128 v[200:203], v2\n"
      "v_fmac_f32 v112, v220, v220\n"
      "v_mfma_f32_16x16x32_bf16 v[126:129], v[158:161], a[160:163], v[126:129]\n"
      "ds_read_b128 v[186:189], v2 offset:3072\n"
      "v_fmac_f32 v112, v221, v221\n"
      "v_mfma_f32_16x16x32_bf16 v[126:129], v[138:141], a[164:167], v[126:129]\n"
      "ds_read_b128 v[144:147], v2 offset:6144\n"
      "v_permlane16_swap_b32 v113, v112\n"
      "v_add_f32 v113, v113, v112\n"
      "v_mov_b32 v112, v113\n"
      "v_lshlrev_b32 v77, 4, v77\n"
      "v_cmp_gt_u32 s[72:73], 64, v77\n"
      "v_permlane32_swap_b32 v113, v112\n"
      "v_add_f32 v113, v113, v112\n"
      "v_rsq_f32 v113, v113\n"
      "v_cvt_pk_bf16_f32 v194, v194, v195\n"
      "v_mov_b32 v112, v113\n"
      "v_cvt_pk_bf16_f32 v195, v196, v197\n"
      "v_mov_b32 v208, 0x3fb9\n"
      "v_permlane16_swap_b32 v113, v112\n"
      "v_mul_f32 v113, s50, v113\n"
      "v_mul_f32 v190, v112, v190\n"
      "v_mul_f32 v191, v112, v191\n"
      "v_mul_f32 v162, v113, v162\n"
      "v_mul_f32 v163, v113, v163\n"
      "v_cvt_pk_bf16_f32 v190, v190, v191\n"
      "v_mul_f32 v192, v112, v192\n"
      "v_mul_f32 v193, v112, v193\n"
      "v_mul_f32 v218, v112, v218\n"
      "v_mul_f32 v219, v112, v219\n"
      "v_cvt_pk_bf16_f32 v162, v162, v163\n"
      "v_mul_f32 v164, v113, v164\n"
      "v_mul_f32 v165, v113, v165\n"
      "v_mul_f32 v176, v113, v176\n"
      "v_mul_f32 v177, v113, v177\n"
      "v_cvt_pk_bf16_f32 v191, v192, v193\n"
      "v_mul_f32 v114, v112, v114\n"
      "v_mul_f32 v115, v112, v115\n"
      "v_mul_f32 v116, v112, v116\n"
      "v_mul_f32 v117, v112, v117\n"
      "v_cvt_pk_bf16_f32 v218, v218, v219\n"
      "v_mul_f32 v220, v112, v220\n"
      "v_mul_f32 v221, v112, v221\n"
      "v_cvt_pk_bf16_f32 v163, v164, v165\n"
      "v_mul_f32 v180, v113, v180\n"
      "v_mul_f32 v181, v113, v181\n"
      "v_mul_f32 v182, v113, v182\n"
      "v_mul_f32 v183, v113, v183\n"
      "v_cvt_pk_bf16_f32 v176, v176, v177\n"
      "v_mul_f32 v178, v113, v178\n"
      "v_mul_f32 v179, v113, v179\n"
      "v_cvt_pk_bf16_f32 v192, v114, v115\n"
      "v_mfma_f32_16x16x32_bf16 v[112:115], v[100:103], a[168:171], 0\n"
      "v_cvt_pk_bf16_f32 v193, v116, v117\n"
      "v_cvt_pk_bf16_f32 v219, v220, v221\n"
      "v_mfma_f32_16x16x32_bf16 v[112:115], v[166:169], a[172:175], v[112:115]\n"
      "v_cvt_pk_bf16_f32 v164, v180, v181\n"
      "v_cvt_pk_bf16_f32 v165, v182, v183\n"
      "v_mfma_f32_16x16x32_bf16 v[112:115], v[172:175], a[176:179], v[112:115]\n"
      "v_cvt_pk_bf16_f32 v177, v178, v179\n"
      "v_mov_b32_e64 v209, 0\n"
      "v_mfma_f32_16x16x32_bf16 v[112:115], v[106:109], a[180:183], v[112:115]\n"
      "v_lshlrev_b64 v[208:209], v77, v[208:209]\n"
      "v_cndmask_b32_e64 v208, 0, v208, s[72:73]\n"
      "v_mfma_f32_16x16x32_bf16 v[112:115], v[158:161], a[184:187], v[112:115]\n"
      "v_cndmask_b32_e64 v209, 0, v209, s[72:73]\n"
      "v_cmp_gt_u32 s[72:73], 64, v77\n"
      "v_mfma_f32_16x16x32_bf16 v[112:115], v[138:141], a[188:191], v[112:115]\n"
      "ds_read_b128 v[178:181], v7\n"
      "v_mov_b32 v142, 0xbb2b\n"
      "v_mfma_f32_16x16x32_bf16 v[182:185], v[100:103], a[192:195], 0\n"
      "ds_read_b128 v[220:223], v2 offset:9216\n"
      "v_mov_b32_e64 v143, 0\n"
      "v_mfma_f32_16x16x32_bf16 v[182:185], v[166:169], a[196:199], v[182:185]\n"
      "ds_read_b128 v[166:169], v2 offset:12288\n"
      "v_lshlrev_b64 v[142:143], v77, v[142:143]\n"
      "v_mfma_f32_16x16x32_bf16 v[182:185], v[172:175], a[200:203], v[182:185]\n"
      "ds_read_b128 v[100:103], v7 offset:4096\n"
      "v_cndmask_b32_e64 v142, 0, v142, s[72:73]\n"
      "v_mfma_f32_16x16x32_bf16 v[182:185], v[106:109], a[204:207], v[182:185]\n"
      "ds_read_b128 v[106:109], v2 offset:15360\n"
      "v_cndmask_b32_e64 v143, 0, v143, s[72:73]\n"
      "v_mfma_f32_16x16x32_bf16 v[182:185], v[158:161], a[208:211], v[182:185]\n"
      "ds_read_b128 v[172:175], v7 offset:8192\n"
      "v_add_u32 v89, 112160, v78\n"
      "v_mfma_f32_16x16x32_bf16 v[182:185], v[138:141], a[212:215], v[182:185]\n"
      "s_waitcnt lgkmcnt(5)\n"
      "v_cvt_pk_bf16_f32 v196, v96, v97\n"
      "v_cvt_pk_bf16_f32 v197, v98, v99\n"
      "v_mfma_f32_16x16x32_bf16 v[158:161], a[216:219], v[200:203], 0\n"
      "buffer_store_dwordx4 v[178:181], v20, s[24:27], s46 offen sc1\n"
      "v_mfma_f32_16x16x32_bf16 v[158:161], a[220:223], v[186:189], v[158:161]\n"
      "s_nop 0\n"
      "ds_read_b128 v[178:181], v7 offset:12288\n"
      "v_cvt_pk_bf16_f32 v76, v126, v127\n"
      "v_mfma_f32_16x16x32_bf16 v[158:161], a[224:227], v[144:147], v[158:161]\n"
      "v_cvt_pk_bf16_f32 v77, v128, v129\n"
      "v_cvt_pk_bf16_f32 v130, v130, v131\n"
      "v_mfma_f32_16x16x32_bf16 v[96:99], a[240:243], v[200:203], 0\n"
      "v_cvt_pk_bf16_f32 v131, v132, v133\n"
      "v_cvt_pk_bf16_f32 v132, v154, v155\n"
      "v_mfma_f32_16x16x32_bf16 v[96:99], a[244:247], v[186:189], v[96:99]\n"
      "v_cvt_pk_bf16_f32 v133, v156, v157\n"
      "v_cvt_pk_bf16_f32 v80, v112, v113\n"
      "v_mfma_f32_16x16x32_bf16 v[96:99], a[248:251], v[144:147], v[96:99]\n"
      "ds_read_b128 v[126:129], v7 offset:16384\n"
      "v_cvt_pk_bf16_f32 v81, v114, v115\n"
      "v_mfma_f32_16x16x32_bf16 v[114:117], v[232:235], v[200:203], 0\n"
      "s_waitcnt lgkmcnt(2)\n"
      "v_cvt_pk_bf16_f32 v214, v214, v215\n"
      "v_cvt_pk_bf16_f32 v215, v216, v217\n"
      "v_mfma_f32_16x16x32_bf16 v[114:117], v[236:239], v[186:189], v[114:117]\n"
      "ds_read_b128 v[186:189], v10 offset:1200\n"
      "v_cvt_pk_bf16_f32 v216, v150, v151\n"
      "v_mfma_f32_16x16x32_bf16 v[114:117], v[240:243], v[144:147], v[114:117]\n"
      "buffer_store_dwordx4 v[100:103], v21, s[24:27], s46 offen sc1\n"
      "v_mfma_f32_16x16x32_bf16 v[158:161], a[228:231], v[220:223], v[158:161]\n"
      "s_waitcnt lgkmcnt(2)\n"
      "v_cvt_pk_bf16_f32 v217, v152, v153\n"
      "v_cvt_pk_bf16_f32 v84, v182, v183\n"
      "v_mfma_f32_16x16x32_bf16 v[158:161], a[232:235], v[166:169], v[158:161]\n"
      "ds_read_b64 v[100:101], v10 offset:1192\n"
      "v_cvt_pk_bf16_f32 v85, v184, v185\n"
      "v_mfma_f32_16x16x32_bf16 v[158:161], a[236:239], v[106:109], v[158:161]\n"
      "ds_read2_b32 v[102:103], v86 offset0:0 offset1:1\n"
      "v_add_u32 v184, 113728, v78\n"
      "v_mfma_f32_16x16x32_bf16 v[96:99], a[252:255], v[220:223], v[96:99]\n"
      "ds_read2_b32 v[112:113], v86 offset0:2 offset1:10\n"
      "v_subrev_u32 v149, 115456, v9\n"
      "v_mfma_f32_16x16x32_bf16 v[96:99], v[224:227], v[166:169], v[96:99]\n"
      "s_waitcnt lgkmcnt(3)\n"
      "v_lshrrev_b32 v149, 3, v149\n"
      "v_min_u32 v149, 96, v149\n"
      "v_mfma_f32_16x16x32_bf16 v[96:99], v[228:231], v[106:109], v[96:99]\n"
      "ds_read2_b32 v[156:157], v86 offset0:8 offset1:9\n"
      "v_add_u32_e64 v149, s77, v149\n"
      "v_mfma_f32_16x16x32_bf16 v[114:117], v[244:247], v[220:223], v[114:117]\n"
      "ds_read2_b32 v[182:183], v86 offset0:16 offset1:17\n"
      "v_add_u32 v149, 115296, v149\n"
      "v_mfma_f32_16x16x32_bf16 v[114:117], v[248:251], v[166:169], v[114:117]\n"
      "buffer_store_dwordx4 v[172:175], v22, s[24:27], s46 offen sc1\n"
      "v_mfma_f32_16x16x32_bf16 v[114:117], v[252:255], v[106:109], v[114:117]\n"
      "ds_read2_b32 v[168:169], v86 offset0:24 offset1:18\n"
      "s_waitcnt lgkmcnt(3)\n"
      "v_alignbyte_b32 v102, v103, v102, v93\n"
      "v_mfma_f32_16x16x32_bf16 v[144:147], v[118:121], v[210:213], v[40:43]\n"
      "v_alignbyte_b32 v103, v112, v103, v93\n"
      "v_fma_f32 v148, v158, v158, s52\n"
      "v_mfma_f32_16x16x32_bf16 v[138:141], v[122:125], v[210:213], v[44:47]\n"
      "ds_read_b128 v[106:109], v2 offset:256\n"
      "s_waitcnt lgkmcnt(3)\n"
      "v_alignbyte_b32 v156, v157, v156, v93\n"
      "v_mfma_f32_16x16x32_bf16 v[200:203], v[190:193], v[210:213], v[48:51]\n"
      "ds_read_b128 v[152:155], v2 offset:3328\n"
      "v_alignbyte_b32 v157, v113, v157, v93\n"
      "v_mfma_f32_16x16x32_bf16 v[220:223], v[186:189], v[210:213], v[24:27]\n"
      "s_waitcnt lgkmcnt(3)\n"
      "v_alignbyte_b32 v182, v183, v182, v93\n"
      "v_fmac_f32 v148, v159, v159\n"
      "v_mfma_f32_16x16x16_bf16 v[144:147], v[170:171], v[104:105], v[144:147]\n"
      "v_fmac_f32 v148, v160, v160\n"
      "v_fmac_f32_e64 v148, v161, v161\n"
      "s_waitcnt lgkmcnt(2)\n"
      "v_alignbyte_b32 v183, v169, v183, v93\n"
      "v_mfma_f32_16x16x16_bf16 v[138:141], v[198:199], v[104:105], v[138:141]\n"
      "v_alignbyte_b32 v168, v168, v168, v93\n"
      "buffer_store_dwordx4 v[178:181], v23, s[24:27], s46 offen sc1\n"
      "v_mfma_f32_16x16x16_bf16 v[200:203], v[218:219], v[104:105], v[200:203]\n"
      "v_fmac_f32 v148, v96, v96\n"
      "v_fmac_f32 v148, v97, v97\n"
      "v_fmac_f32 v148, v98, v98\n"
      "v_mfma_f32_16x16x16_bf16 v[220:223], v[100:101], v[104:105], v[220:223]\n"
      "v_fmac_f32 v148, v99, v99\n"
      "v_fmac_f32 v148, v114, v114\n"
      "v_fmac_f32 v148, v115, v115\n"
      "v_mfma_f32_16x16x16_bf16 v[144:147], v[208:209], v[102:103], v[144:147]\n"
      "v_fmac_f32_e64 v148, v116, v116\n"
      "v_fmac_f32_e64 v148, v117, v117\n"
      "v_readlane_b32 s45, v37, s43\n"
      "v_mfma_f32_16x16x16_bf16 v[138:141], v[208:209], v[156:157], v[138:141]\n"
      "v_readlane_b32 s47, v38, s43\n"
      "ds_read_b128 v[210:213], v2 offset:6400\n"
      "v_mfma_f32_16x16x16_bf16 v[200:203], v[208:209], v[182:183], v[200:203]\n"
      "s_waitcnt vmcnt(5)\n"
      "v_or3_b32 v204, v204, v205, v206\n"
      "s_and_b32 s47, s47, -4\n"
      "s_add_u32 s43, s43, 1\n"
      "v_mfma_f32_16x16x16_bf16 v[220:223], v[208:209], v[168:169], v[220:223]\n"
      "v_mfma_f32_16x16x16_bf16 v[144:147], v[142:143], v[102:103], v[144:147]\n"
      "v_mfma_f32_16x16x16_bf16 v[138:141], v[142:143], v[156:157], v[138:141]\n"
      "buffer_store_dwordx4 v[126:129], v25, s[24:27], s46 offen sc1\n"
      "v_mfma_f32_16x16x16_bf16 v[200:203], v[142:143], v[182:183], v[200:203]\n"
      "s_waitcnt vmcnt(5)\n"
      "v_or3_b32 v204, v204, v207, v82\n"
      "v_and_b32 v204, 0x7fff7fff, v204\n"
      "v_readlane_b32 s46, v37, s42\n"
      "v_mfma_f32_16x16x16_bf16 v[220:223], v[142:143], v[168:169], v[220:223]\n"
      "ds_write_b32 v31, v204 offset:5632\n"
      "s_waitcnt lgkmcnt(1)\n"
      "v_mfma_f32_16x16x32_bf16 v[178:181], a[216:219], v[106:109], 0\n"
      "s_or_b32 s95, s45, s46\n"
      "v_max3_f32 v82, v144, v145, v146\n"
      "v_max3_f32 v127, v147, v138, v139\n"
      "v_mfma_f32_16x16x32_bf16 v[178:181], a[220:223], v[152:155], v[178:181]\n"
      "ds_read2_b32 v[112:113], v89 offset0:0 offset1:1\n"
      "v_max3_f32 v82, v82, v140, v141\n"
      "v_mfma_f32_16x16x32_bf16 v[178:181], a[224:227], v[210:213], v[178:181]\n"
      "ds_read2_b32 v[204:205], v89 offset0:2 offset1:10\n"
      "v_max3_f32 v127, v127, v200, v201\n"
      "v_mfma_f32_16x16x32_bf16 v[166:169], a[240:243], v[106:109], 0\n"
      "ds_read_b128 v[102:105], v9 offset:4608\n"
      "v_max3_f32 v82, v82, v202, v203\n"
      "v_mfma_f32_16x16x32_bf16 v[166:169], a[244:247], v[152:155], v[166:169]\n"
      "ds_read2_b32 v[172:173], v89 offset0:8 offset1:9\n"
      "v_max3_f32 v82, v82, v127, v220\n"
      "v_mfma_f32_16x16x32_bf16 v[166:169], a[248:251], v[210:213], v[166:169]\n"
      "ds_read2_b32 v[174:175], v89 offset0:16 offset1:17\n"
      "v_mov_b32_e64 v78, v82\n"
      "v_mfma_f32_16x16x32_bf16 v[126:129], v[232:235], v[106:109], 0\n"
      "ds_read2_b32 v[222:223], v89 offset0:24 offset1:18\n"
      "v_permlane16_swap_b32_e64 v82, v78\n"
      "v_mfma_f32_16x16x32_bf16 v[126:129], v[236:239], v[152:155], v[126:129]\n"
      "s_or_b32 s95, s95, s48\n"
      "s_waitcnt lgkmcnt(5)\n"
      "v_alignbyte_b32 v112, v113, v112, v93\n"
      "v_mfma_f32_16x16x32_bf16 v[126:129], v[240:243], v[210:213], v[126:129]\n"
      "s_cmp_ge_u32 s95, 0x40000000\n"
      "s_cselect_b32 s94, -1, -2\n"
      "s_waitcnt lgkmcnt(4)\n"
      "v_alignbyte_b32 v113, v204, v113, v93\n"
      "v_mfma_f32_16x16x32_bf16 v[210:213], v[118:121], v[134:137], v[52:55]\n"
      "s_cmp_gt_u32 s42, s44\n"
      "s_cselect_b32 s95, s94, s95\n"
      "s_waitcnt lgkmcnt(2)\n"
      "v_alignbyte_b32 v172, v173, v172, v93\n"
      "v_mfma_f32_16x16x32_bf16 v[106:109], v[122:125], v[134:137], v[56:59]\n"
      "s_add_u32 s42, s42, 1\n"
      "v_alignbyte_b32 v173, v205, v173, v93\n"
      "s_waitcnt lgkmcnt(1)\n"
      "v_alignbyte_b32 v174, v175, v174, v93\n"
      "v_max_f32 v82, v82, v78\n"
      "s_waitcnt lgkmcnt(0)\n"
      "v_alignbyte_b32 v175, v223, v175, v93\n"
      "v_alignbyte_b32 v222, v222, v222, v93\n"
      "v_mov_b32 v78, v82\n"
      "v_or3_b32 v102, v102, v103, v104\n"
      "v_mfma_f32_16x16x32_bf16 v[204:207], v[190:193], v[134:137], v[60:63]\n"
      "v_permlane32_swap_b32 v82, v78\n"
      "v_mfma_f32_16x16x32_bf16 v[152:155], v[186:189], v[134:137], v[26:29]\n"
      "v_max_f32 v82, v82, v78\n"
      "v_sub_f32 v144, v144, v82\n"
      "v_mfma_f32_16x16x16_bf16 v[210:213], v[170:171], v[110:111], v[210:213]\n"
      "v_sub_f32 v145, v145, v82\n"
      "v_sub_f32 v146, v146, v82\n"
      "v_sub_f32_e64 v147, v147, v82\n"
      "v_mfma_f32_16x16x16_bf16 v[106:109], v[198:199], v[110:111], v[106:109]\n"
      "v_exp_f32 v144, v144\n"
      "v_exp_f32 v145, v145\n"
      "v_mfma_f32_16x16x16_bf16 v[204:207], v[218:219], v[110:111], v[204:207]\n"
      "v_sub_f32 v138, v138, v82\n"
      "v_sub_f32 v139, v139, v82\n"
      "v_sub_f32 v140, v140, v82\n"
      "v_mfma_f32_16x16x16_bf16 v[152:155], v[100:101], v[110:111], v[152:155]\n"
      "v_sub_f32 v141, v141, v82\n"
      "v_exp_f32 v146, v146\n"
      "v_mfma_f32_16x16x16_bf16 v[210:213], v[208:209], v[112:113], v[210:213]\n"
      "v_exp_f32 v147, v147\n"
      "v_exp_f32 v138, v138\n"
      "v_mfma_f32_16x16x16_bf16 v[106:109], v[208:209], v[172:173], v[106:109]\n"
      "v_exp_f32 v139, v139\n"
      "v_exp_f32 v140, v140\n"
      "v_mfma_f32_16x16x16_bf16 v[204:207], v[208:209], v[174:175], v[204:207]\n"
      "v_exp_f32 v141, v141\n"
      "v_cvt_pk_bf16_f32 v144, v144, v145\n"
      "v_mfma_f32_16x16x16_bf16 v[152:155], v[208:209], v[222:223], v[152:155]\n"
      "v_cvt_pk_bf16_f32 v145, v146, v147\n"
      "v_cvt_pk_bf16_f32 v146, v138, v139\n"
      "v_cvt_pk_bf16_f32 v147, v140, v141\n"
      "v_mfma_f32_16x16x16_bf16 v[210:213], v[142:143], v[112:113], v[210:213]\n"
      "v_sub_f32 v200, v200, v82\n"
      "v_sub_f32 v201, v201, v82\n"
      "v_sub_f32_e64 v202, v202, v82\n"
      "v_mfma_f32_16x16x16_bf16 v[106:109], v[142:143], v[172:173], v[106:109]\n"
      "v_sub_f32 v203, v203, v82\n"
      "v_sub_f32 v220, v220, v82\n"
      "v_exp_f32 v200, v200\n"
      "v_mfma_f32_16x16x16_bf16 v[204:207], v[142:143], v[174:175], v[204:207]\n"
      "v_exp_f32 v201, v201\n"
      "v_exp_f32_e64 v202, v202\n"
      "v_mfma_f32_16x16x16_bf16 v[152:155], v[142:143], v[222:223], v[152:155]\n"
      "ds_read_b128 v[138:141], v2 offset:9472\n"
      "ds_read_b128 v[110:113], v2 offset:12544\n"
      "ds_read_b128 v[172:175], v2 offset:15616\n"
      "v_exp_f32 v203, v203\n"
      "v_exp_f32 v220, v220\n"
      "v_cvt_pk_bf16_f32 v88, v200, v201\n"
      "v_cvt_pk_bf16_f32 v89, v202, v203\n"
      "v_cvt_pk_bf16_f32 v90, v220, 0\n"
      "v_max3_f32 v220, v210, v211, v212\n"
      "v_max3_f32 v82, v213, v106, v107\n"
      "v_max3_f32 v220, v220, v108, v109\n"
      "v_max3_f32 v82, v82, v204, v205\n"
      "v_max3_f32 v220, v220, v206, v207\n"
      "v_max3_f32 v220, v220, v82, v152\n"
      "v_mov_b32 v203, v220\n"
      "v_or_b32 v102, v102, v105\n"
      "v_cmp_ne_u32 s[56:57], 0, v102\n"
      "v_permlane16_swap_b32 v220, v203\n"
      "s_waitcnt lgkmcnt(2)\n"
      "v_mfma_f32_16x16x32_bf16 v[178:181], a[228:231], v[138:141], v[178:181]\n"
      "ds_read_u16 v78, v11 offset:1168\n"
      "v_max_f32 v220, v220, v203\n"
      "s_waitcnt lgkmcnt(2)\n"
      "v_mfma_f32_16x16x32_bf16 v[178:181], a[232:235], v[110:113], v[178:181]\n"
      "ds_read_u16 v82, v11 offset:1200\n"
      "v_mov_b32 v203, v220\n"
      "s_waitcnt lgkmcnt(2)\n"
      "v_mfma_f32_16x16x32_bf16 v[178:181], a[236:239], v[172:175], v[178:181]\n"
      "ds_read_u16 v86, v11 offset:1232\n"
      "v_permlane32_swap_b32 v220, v203\n"
      "v_mfma_f32_16x16x32_bf16 v[166:169], a[252:255], v[138:141], v[166:169]\n"
      "s_cmp_ge_u32 s95, 0x40000000\n"
      "s_cselect_b64 vcc, -1, s[56:57]\n"
      "v_max_f32 v220, v220, v203\n"
      "v_mfma_f32_16x16x32_bf16 v[166:169], v[224:227], v[110:113], v[166:169]\n"
      "v_sub_f32 v210, v210, v220\n"
      "v_sub_f32 v211, v211, v220\n"
      "v_mfma_f32_16x16x32_bf16 v[166:169], v[228:231], v[172:175], v[166:169]\n"
      "v_sub_f32 v212, v212, v220\n"
      "v_sub_f32 v213, v213, v220\n"
      "v_mfma_f32_16x16x32_bf16 v[126:129], v[244:247], v[138:141], v[126:129]\n"
      "v_exp_f32 v210, v210\n"
      "v_mfma_f32_16x16x32_bf16 v[126:129], v[248:251], v[110:113], v[126:129]\n"
      "v_exp_f32 v211, v211\n"
      "v_mfma_f32_16x16x32_bf16 v[126:129], v[252:255], v[172:175], v[126:129]\n"
      "s_waitcnt lgkmcnt(0)\n"
      "v_sub_f32 v106, v106, v220\n"
      "v_sub_f32_e64 v107, v107, v220\n"
      "v_mfma_f32_16x16x32_bf16 v[200:203], v[32:35], v[144:147], 0\n"
      "ds_read2_b32 v[136:137], v184 offset0:0 offset1:1\n"
      "v_sub_f32 v108, v108, v220\n"
      "v_mfma_f32_16x16x32_bf16 v[200:203], v[32:35], v[88:91], v[200:203]\n"
      "ds_read2_b32 v[174:175], v184 offset0:2 offset1:10\n"
      "v_sub_f32 v109, v109, v220\n"
      "v_mfma_f32_16x16x32_bf16 v[138:141], v[194:197], v[144:147], 0\n"
      "ds_read2_b32 v[134:135], v184 offset0:8 offset1:9\n"
      "v_exp_f32_e64 v212, v212\n"
      "v_mfma_f32_16x16x32_bf16 v[138:141], v[76:79], v[88:91], v[138:141]\n"
      "ds_read2_b32 v[182:183], v184 offset0:16 offset1:17\n"
      "v_exp_f32 v213, v213\n"
      "v_mfma_f32_16x16x32_bf16 v[110:113], v[130:133], v[144:147], 0\n"
      "ds_read2_b32 v[202:203], v184 offset0:24 offset1:18\n"
      "v_exp_f32 v106, v106\n"
      "v_mfma_f32_16x16x32_bf16 v[110:113], v[80:83], v[88:91], v[110:113]\n"
      "v_exp_f32 v107, v107\n"
      "v_mfma_f32_16x16x32_bf16 v[102:105], v[214:217], v[144:147], 0\n"
      "s_waitcnt lgkmcnt(4)\n"
      "v_alignbyte_b32 v136, v137, v136, v93\n"
      "s_waitcnt lgkmcnt(3)\n"
      "v_alignbyte_b32 v137, v174, v137, v93\n"
      "v_mfma_f32_16x16x32_bf16 v[102:105], v[84:87], v[88:91], v[102:105]\n"
      "s_waitcnt lgkmcnt(2)\n"
      "v_alignbyte_b32 v134, v135, v134, v93\n"
      "v_alignbyte_b32 v135, v175, v135, v93\n"
      "v_mfma_f32_16x16x32_bf16 v[172:175], v[118:121], v[162:165], v[64:67]\n"
      "v_exp_f32 v108, v108\n"
      "s_waitcnt lgkmcnt(1)\n"
      "v_alignbyte_b32 v182, v183, v182, v93\n"
      "v_exp_f32 v109, v109\n"
      "s_waitcnt lgkmcnt(0)\n"
      "v_alignbyte_b32 v183, v203, v183, v93\n"
      "v_alignbyte_b32 v202, v202, v202, v93\n"
      "v_cvt_pk_bf16_f32 v210, v210, v211\n"
      "v_cvt_pk_bf16_f32 v211, v212, v213\n"
      "v_cvt_pk_bf16_f32 v212, v106, v107\n"
      "v_mfma_f32_16x16x32_bf16 v[154:157], v[122:125], v[162:165], v[68:71]\n"
      "v_cvt_pk_bf16_f32 v213, v108, v109\n"
      "v_sub_f32 v204, v204, v220\n"
      "v_sub_f32 v205, v205, v220\n"
      "v_sub_f32 v206, v206, v220\n"
      "v_sub_f32 v207, v207, v220\n"
      "v_sub_f32 v152, v152, v220\n"
      "v_mfma_f32_16x16x32_bf16 v[220:223], v[190:193], v[162:165], v[72:75]\n"
      "v_exp_f32 v204, v204\n"
      "v_mfma_f32_16x16x32_bf16 v[106:109], v[186:189], v[162:165], v[28:31]\n"
      "v_exp_f32 v205, v205\n"
      "v_mfma_f32_16x16x16_bf16 v[172:175], v[170:171], v[176:177], v[172:175]\n"
      "v_exp_f32 v206, v206\n"
      "v_exp_f32_e64 v207, v207\n"
      "v_mfma_f32_16x16x16_bf16 v[154:157], v[198:199], v[176:177], v[154:157]\n"
      "v_exp_f32_e64 v152, v152\n"
      "v_cvt_pk_bf16_f32 v92, v204, v205\n"
      "v_mfma_f32_16x16x16_bf16 v[220:223], v[218:219], v[176:177], v[220:223]\n"
      "v_cvt_pk_bf16_f32 v93, v206, v207\n"
      "v_cvt_pk_bf16_f32 v94, v152, 0\n"
      "v_fma_f32 v201, v178, v178, s52\n"
      "v_mfma_f32_16x16x16_bf16 v[106:109], v[100:101], v[176:177], v[106:109]\n"
      "v_fmac_f32 v201, v179, v179\n"
      "v_fmac_f32 v201, v180, v180\n"
      "v_fmac_f32 v201, v181, v181\n"
      "v_mfma_f32_16x16x16_bf16 v[172:175], v[208:209], v[136:137], v[172:175]\n"
      "v_fmac_f32 v201, v166, v166\n"
      "v_fmac_f32 v201, v167, v167\n"
      "v_fmac_f32 v201, v168, v168\n"
      "v_mfma_f32_16x16x16_bf16 v[154:157], v[208:209], v[134:135], v[154:157]\n"
      "v_fmac_f32 v201, v169, v169\n"
      "v_fmac_f32 v201, v126, v126\n"
      "v_fmac_f32 v201, v127, v127\n"
      "v_mfma_f32_16x16x16_bf16 v[220:223], v[208:209], v[182:183], v[220:223]\n"
      "v_fmac_f32 v201, v128, v128\n"
      "v_fmac_f32 v201, v129, v129\n"
      "v_rcp_f32 v200, v200\n"
      "v_mfma_f32_16x16x16_bf16 v[106:109], v[208:209], v[202:203], v[106:109]\n"
      "v_permlane16_swap_b32 v148, v201\n"
      "v_add_f32 v148, v148, v201\n"
      "v_mfma_f32_16x16x16_bf16 v[172:175], v[142:143], v[136:137], v[172:175]\n"
      "v_mul_f32 v138, v200, v138\n"
      "v_mul_f32 v139, v200, v139\n"
      "v_cvt_pk_bf16_f32 v138, v138, v139\n"
      "v_mfma_f32_16x16x16_bf16 v[154:157], v[142:143], v[134:135], v[154:157]\n"
      "v_mul_f32 v140, v200, v140\n"
      "v_mul_f32 v141, v200, v141\n"
      "v_cvt_pk_bf16_f32 v139, v140, v141\n"
      "v_mfma_f32_16x16x16_bf16 v[220:223], v[142:143], v[182:183], v[220:223]\n"
      "v_max3_f32 v207, v172, v173, v174\n"
      "v_mul_f32 v110, v200, v110\n"
      "v_mul_f32 v111, v200, v111\n"
      "v_mfma_f32_16x16x16_bf16 v[106:109], v[142:143], v[202:203], v[106:109]\n"
      "ds_read_b128 v[202:205], v2 offset:512\n"
      "ds_read_b128 v[134:137], v2 offset:3584\n"
      "ds_read_b128 v[162:165], v2 offset:6656\n"
      "v_max3_f32 v183, v175, v154, v155\n"
      "v_max3_f32 v207, v207, v156, v157\n"
      "v_max3_f32 v183, v183, v220, v221\n"
      "v_max3_f32 v207, v207, v222, v223\n"
      "v_mul_f32 v112, v200, v112\n"
      "v_max3_f32 v207, v207, v183, v106\n"
      "v_mov_b32 v183, v207\n"
      "v_mul_f32 v113, v200, v113\n"
      "v_cvt_pk_bf16_f32 v140, v110, v111\n"
      "v_permlane16_swap_b32 v207, v183\n"
      "v_max_f32 v207, v207, v183\n"
      "v_mov_b32 v183, v207\n"
      "v_cvt_pk_bf16_f32 v141, v112, v113\n"
      "v_mul_f32 v102, v200, v102\n"
      "v_permlane32_swap_b32 v207, v183\n"
      "v_max_f32 v207, v207, v183\n"
      "s_waitcnt lgkmcnt(2)\n"
      "v_mfma_f32_16x16x32_bf16 v[144:147], a[216:219], v[202:205], 0\n"
      "ds_write_b128 v3, v[138:141] offset:18432\n"
      "s_waitcnt lgkmcnt(1)\n"
      "v_mfma_f32_16x16x32_bf16 v[144:147], a[220:223], v[134:137], v[144:147]\n"
      "v_sub_f32 v172, v172, v207\n"
      "v_sub_f32 v173, v173, v207\n"
      "v_mfma_f32_16x16x32_bf16 v[144:147], a[224:227], v[162:165], v[144:147]\n"
      "ds_read_b128 v[182:185], v10 offset:1168\n"
      "v_sub_f32 v174, v174, v207\n"
      "v_mfma_f32_16x16x32_bf16 v[108:111], a[240:243], v[202:205], 0\n"
      "v_sub_f32 v175, v175, v207\n"
      "v_exp_f32 v172, v172\n"
      "v_mfma_f32_16x16x32_bf16 v[108:111], a[244:247], v[134:137], v[108:111]\n"
      "v_exp_f32 v173, v173\n"
      "v_mfma_f32_16x16x32_bf16 v[108:111], a[248:251], v[162:165], v[108:111]\n"
      "v_sub_f32 v154, v154, v207\n"
      "v_sub_f32 v155, v155, v207\n"
      "v_mfma_f32_16x16x32_bf16 v[138:141], v[232:235], v[202:205], 0\n"
      "v_sub_f32 v156, v156, v207\n"
      "v_sub_f32_e64 v157, v157, v207\n"
      "v_mfma_f32_16x16x32_bf16 v[138:141], v[236:239], v[134:137], v[138:141]\n"
      "ds_read_b64 v[176:177], v10 offset:1184\n"
      "v_exp_f32 v174, v174\n"
      "v_mfma_f32_16x16x32_bf16 v[138:141], v[240:243], v[162:165], v[138:141]\n"
      "v_exp_f32 v175, v175\n"
      "v_mfma_f32_16x16x32_bf16 v[202:205], v[32:35], v[210:213], 0\n"
      "v_exp_f32 v154, v154\n"
      "v_mfma_f32_16x16x32_bf16 v[202:205], v[32:35], v[92:95], v[202:205]\n"
      "ds_read_b128 v[134:137], v2 offset:9728\n"
      "v_exp_f32 v155, v155\n"
      "v_exp_f32 v156, v156\n"
      "v_exp_f32 v157, v157\n"
      "v_cvt_pk_bf16_f32 v172, v172, v173\n"
      "v_cvt_pk_bf16_f32 v173, v174, v175\n"
      "v_cvt_pk_bf16_f32 v174, v154, v155\n"
      "v_mfma_f32_16x16x32_bf16 v[150:153], v[194:197], v[210:213], 0\n"
      "v_cvt_pk_bf16_f32 v175, v156, v157\n"
      "v_sub_f32 v220, v220, v207\n"
      "v_mfma_f32_16x16x32_bf16 v[150:153], v[76:79], v[92:95], v[150:153]\n"
      "v_sub_f32 v221, v221, v207\n"
      "v_sub_f32 v222, v222, v207\n"
      "v_sub_f32 v223, v223, v207\n"
      "v_sub_f32 v106, v106, v207\n"
      "v_exp_f32 v220, v220\n"
      "v_exp_f32_e64 v221, v221\n"
      "v_exp_f32_e64 v222, v222\n"
      "v_exp_f32_e64 v223, v223\n"
      "v_exp_f32 v106, v106\n"
      "v_cvt_pk_bf16_f32 v88, v220, v221\n"
      "v_mfma_f32_16x16x32_bf16 v[204:207], v[130:133], v[210:213], 0\n"
      "s_waitcnt lgkmcnt(0)\n"
      "v_cvt_pk_bf16_f32 v89, v222, v223\n"
      "v_cvt_pk_bf16_f32 v90, v106, 0\n"
      "v_mfma_f32_16x16x32_bf16 v[204:207], v[80:83], v[92:95], v[204:207]\n"
      "ds_read_u16 v149, v149\n"
      "v_rcp_f32_e64 v202, v202\n"
      "v_mfma_f32_16x16x32_bf16 v[154:157], v[214:217], v[210:213], 0\n"
      "v_mul_f32 v150, v202, v150\n"
      "v_mul_f32 v151, v202, v151\n"
      "v_mfma_f32_16x16x32_bf16 v[154:157], v[84:87], v[92:95], v[154:157]\n"
      "ds_read_b128 v[210:213], v2 offset:12800\n"
      "v_cvt_pk_bf16_f32 v150, v150, v151\n"
      "v_mfma_f32_16x16x32_bf16 v[162:165], v[182:185], v[118:121], v[30:33]\n"
      "v_mul_f32 v152, v202, v152\n"
      "v_mul_f32 v153, v202, v153\n"
      "v_mfma_f32_16x16x32_bf16 v[118:121], v[182:185], v[122:125], v[30:33]\n"
      "v_cvt_pk_bf16_f32 v151, v152, v153\n"
      "v_mul_f32 v204, v202, v204\n"
      "v_mfma_f32_16x16x32_bf16 v[122:125], v[182:185], v[190:193], v[30:33]\n"
      "v_mul_f32 v205, v202, v205\n"
      "v_mul_f32_e64 v206, v202, v206\n"
      "v_mfma_f32_16x16x32_bf16 v[190:193], v[182:185], v[186:189], v[30:33]\n"
      "ds_read_b128 v[186:189], v2 offset:15872\n"
      "s_waitcnt lgkmcnt(2)\n"
      "v_lshlrev_b32 v149, 16, v149\n"
      "v_mfma_f32_16x16x16_bf16 v[162:165], v[176:177], v[170:171], v[162:165]\n"
      "v_mul_f32 v207, v202, v207\n"
      "v_cvt_pk_bf16_f32 v152, v204, v205\n"
      "v_cvt_pk_bf16_f32 v153, v206, v207\n"
      "v_mfma_f32_16x16x16_bf16 v[118:121], v[176:177], v[198:199], v[118:121]\n"
      "v_mul_f32 v103, v200, v103\n"
      "v_mul_f32 v154, v202, v154\n"
      "v_mul_f32 v155, v202, v155\n"
      "v_mfma_f32_16x16x16_bf16 v[122:125], v[176:177], v[218:219], v[122:125]\n"
      "v_cvt_pk_bf16_f32 v102, v102, v103\n"
      "v_mul_f32 v104, v200, v104\n"
      "v_mul_f32 v105, v200, v105\n"
      "v_mfma_f32_16x16x16_bf16 v[190:193], v[176:177], v[100:101], v[190:193]\n"
      "v_cndmask_b32 v162, v162, v118, s[58:59]\n"
      "v_cvt_pk_bf16_f32 v154, v154, v155\n"
      "v_mul_f32_e64 v156, v202, v156\n"
      "v_mfma_f32_16x16x32_bf16 v[144:147], a[228:231], v[134:137], v[144:147]\n"
      "s_waitcnt lgkmcnt(1)\n"
      "v_cndmask_b32 v162, v162, v122, s[60:61]\n"
      "v_mul_f32 v157, v202, v157\n"
      "v_mfma_f32_16x16x32_bf16 v[144:147], a[232:235], v[210:213], v[144:147]\n"
      "s_waitcnt lgkmcnt(0)\n"
      "v_cndmask_b32 v162, v162, v190, s[62:63]\n"
      "v_fmac_f32 v162, s53, v149\n"
      "v_mfma_f32_16x16x32_bf16 v[144:147], a[236:239], v[186:189], v[144:147]\n"
      "ds_write_b128 v3, v[150:153] offset:18688\n"
      "v_mfma_f32_16x16x32_bf16 v[108:111], a[252:255], v[134:137], v[108:111]\n"
      "v_max_f32_dpp v106, v162, v162 row_ror:8 row_mask:0xf bank_mask:0xf\n"
      "v_cvt_pk_bf16_f32 v103, v104, v105\n"
      "v_mfma_f32_16x16x32_bf16 v[108:111], v[224:227], v[210:213], v[108:111]\n"
      "v_max_f32_dpp v106, v106, v106 row_ror:4 row_mask:0xf bank_mask:0xf\n"
      "v_cvt_pk_bf16_f32 v155, v156, v157\n"
      "v_mfma_f32_16x16x32_bf16 v[108:111], v[228:231], v[186:189], v[108:111]\n"
      "ds_write_b64 v4, v[102:103] offset:18432\n"
      "v_mfma_f32_16x16x32_bf16 v[138:141], v[244:247], v[134:137], v[138:141]\n"
      "v_max_f32_dpp v106, v106, v106 row_ror:2 row_mask:0xf bank_mask:0xf\n"
      "v_fma_f32 v157, v144, v144, s52\n"
      "v_mfma_f32_16x16x32_bf16 v[138:141], v[248:251], v[210:213], v[138:141]\n"
      "v_max_f32_dpp v106, v106, v106 row_ror:1 row_mask:0xf bank_mask:0xf\n"
      "v_fmac_f32_e64 v157, v145, v145\n"
      "v_mfma_f32_16x16x32_bf16 v[138:141], v[252:255], v[186:189], v[138:141]\n"
      "ds_write_b64 v4, v[154:155] offset:18688\n"
      "v_mfma_f32_16x16x32_bf16 v[188:191], v[32:35], v[172:175], 0\n"
      "v_fmac_f32 v157, v146, v146\n"
      "v_max_f32_dpp v106, v106, v106 row_bcast:15 row_mask:0xa bank_mask:0xf\n"
      "v_mfma_f32_16x16x32_bf16 v[188:191], v[32:35], v[88:91], v[188:191]\n"
      "v_fmac_f32 v157, v147, v147\n"
      "v_fmac_f32 v157, v108, v108\n"
      "v_mfma_f32_16x16x32_bf16 v[202:205], v[194:197], v[172:175], 0\n"
      "v_fmac_f32 v157, v109, v109\n"
      "v_fmac_f32_e64 v157, v110, v110\n"
      "v_mfma_f32_16x16x32_bf16 v[202:205], v[76:79], v[88:91], v[202:205]\n"
      "v_max_f32_dpp v106, v106, v106 row_bcast:31 row_mask:0xc bank_mask:0xf\n"
      "v_fmac_f32 v157, v111, v111\n"
      "v_mfma_f32_16x16x32_bf16 v[122:125], v[130:133], v[172:175], 0\n"
      "v_fmac_f32 v157, v138, v138\n"
      "v_readlane_b32 s72, v106, 63\n"
      "v_mfma_f32_16x16x32_bf16 v[122:125], v[80:83], v[88:91], v[122:125]\n"
      "v_fmac_f32 v157, v139, v139\n"
      "v_fmac_f32 v157, v140, v140\n"
      "v_mfma_f32_16x16x32_bf16 v[104:107], v[214:217], v[172:175], 0\n"
      "v_fmac_f32 v157, v141, v141\n"
      "v_mov_b32 v201, v157\n"
      "v_mfma_f32_16x16x32_bf16 v[104:107], v[84:87], v[88:91], v[104:107]\n"
      "v_subrev_f32 v162, s72, v162\n"
      "v_rcp_f32 v188, v188\n"
      "v_exp_f32 v162, v162\n"
      "v_permlane16_swap_b32 v157, v201\n"
      "v_mul_f32 v202, v188, v202\n"
      "v_mul_f32 v203, v188, v203\n"
      "v_cvt_pk_bf16_f32 v162, v162, v162\n"
      "v_cvt_pk_bf16_f32 v202, v202, v203\n"
      "v_mul_f32 v204, v188, v204\n"
      "v_mul_f32 v205, v188, v205\n"
      "ds_write_b16 v13, v162\n"
      "ds_read_b128 v[210:213], v14\n"
      "ds_read_b128 v[118:121], v14 offset:64\n"
      "v_add_f32 v157, v157, v201\n"
      "v_cvt_pk_bf16_f32 v203, v204, v205\n"
      "v_mul_f32 v122, v188, v122\n"
      "v_mul_f32 v123, v188, v123\n"
      "v_mul_f32 v124, v188, v124\n"
      "v_mul_f32 v125, v188, v125\n"
      "v_cvt_pk_bf16_f32 v204, v122, v123\n"
      "v_cvt_pk_bf16_f32 v205, v124, v125\n"
      "v_mul_f32 v104, v188, v104\n"
      "v_mul_f32 v105, v188, v105\n"
      "ds_write_b128 v3, v[202:205] offset:18944\n"
      "v_cvt_pk_bf16_f32 v104, v104, v105\n"
      "v_mul_f32 v106, v188, v106\n"
      "v_mul_f32 v107, v188, v107\n"
      "v_permlane32_swap_b32 v148, v157\n"
      "v_cvt_pk_bf16_f32 v105, v106, v107\n"
      "s_waitcnt lgkmcnt(2)\n"
      "v_mfma_f32_16x16x32_bf16 v[188:191], v[210:213], v[32:35], 0\n"
      "ds_write_b64 v4, v[104:105] offset:18944\n"
      "s_waitcnt lgkmcnt(2)\n"
      "v_mfma_f32_16x16x32_bf16 v[188:191], v[118:121], v[32:35], v[188:191]\n"
      "v_add_f32_e64 v148, v148, v157\n"
      "v_add_u32 v10, 2336, v10\n"
      "v_add_u32 v11, 2336, v11\n"
      "ds_write_b32 v31, v148 offset:1024\n"
      "s_waitcnt lgkmcnt(0)\n"
      "s_barrier\n"
      "s_cbranch_vccz L_sel0_%=\n"
      "L_disp0_%=:\n"
      "s_cmp_lt_u32 s95, 0x40000000\n"
      "s_cbranch_scc1 L_nofix_18_%=\n"
      "s_cmp_eq_u32 s95, -2\n"
      "s_cbranch_scc1 L_tail0_%=\n"
      "s_nop 2\n"
      "v_lshlrev_b32 v204, 3, v36\n"
      "s_add_u32 s72, s98, L_cold_%=-L_ipf0_%=\n"
      "s_addc_u32 s73, s99, 0\n"
      "s_mov_b32 s74, L_cend_%=-L_cold_%=\n"
      "s_mov_b32 s75, 0x20000\n"
      "s_mov_b32 m0, 161792\n"
      "s_nop 0\n"
      "buffer_load_dwordx4 v204, s[72:75], 0 offen  lds\n"
      "s_lshr_b32 s92, s45, 30\n"
      "s_or_b32 s92, s92, s66\n"
      "s_cbranch_scc0 L_nf_19_%=\n"
      "s_bitcmp1_b32 s45, 31\n"
      "s_cselect_b32 s75, 100270464, 0\n"
      "s_bitcmp1_b32 s45, 30\n"
      "s_cselect_b32 s76, 196224, 0\n"
      "s_and_b32 s74, s45, 0x3fffffff\n"
      "s_cmp_ge_u32 s45, 0x40000000\n"
      "s_cselect_b32 s74, s74, 0\n"
      "s_cselect_b32 s45, 0, s45\n"
      "s_sub_u32 s74, s74, s66\n"
      "s_add_u32 s66, s66, s74\n"
      "s_sub_u32 s75, s75, s67\n"
      "s_add_u32 s67, s67, s75\n"
      "s_sub_u32 s76, s76, s68\n"
      "s_add_u32 s68, s68, s76\n"
      "v_add_u32 v16, s74, v16\n"
      "v_add_u32 v17, s74, v17\n"
      "v_add_u32 v18, s74, v18\n"
      "v_add_u32 v19, s74, v19\n"
      "v_add_u32 v15, s74, v15\n"
      "s_cmp_eq_u32 s75, 0\n"
      "s_cbranch_scc1 L_ax_20_%=\n"
      "v_bfe_i32 v204, v27, 0, 1\n"
      "v_and_b32 v204, s75, v204\n"
      "v_sub_u32 v16, v16, v204\n"
      "v_bfe_i32 v204, v27, 2, 1\n"
      "v_and_b32 v204, s75, v204\n"
      "v_sub_u32 v17, v17, v204\n"
      "v_bfe_i32 v204, v27, 4, 1\n"
      "v_and_b32 v204, s75, v204\n"
      "v_sub_u32 v18, v18, v204\n"
      "v_bfe_i32 v204, v27, 6, 1\n"
      "v_and_b32 v204, s75, v204\n"
      "v_sub_u32 v19, v19, v204\n"
      "v_bfe_i32 v204, v27, 8, 1\n"
      "v_and_b32 v204, s75, v204\n"
      "v_sub_u32 v15, v15, v204\n"
      "L_ax_20_%=:\n"
      "s_cmp_eq_u32 s76, 0\n"
      "s_cbranch_scc1 L_ax_21_%=\n"
      "v_bfe_i32 v204, v27, 1, 1\n"
      "v_and_b32 v204, s76, v204\n"
      "v_sub_u32 v16, v16, v204\n"
      "v_bfe_i32 v204, v27, 3, 1\n"
      "v_and_b32 v204, s76, v204\n"
      "v_sub_u32 v17, v17, v204\n"
      "v_bfe_i32 v204, v27, 5, 1\n"
      "v_and_b32 v204, s76, v204\n"
      "v_sub_u32 v18, v18, v204\n"
      "v_bfe_i32 v204, v27, 7, 1\n"
      "v_and_b32 v204, s76, v204\n"
      "v_sub_u32 v19, v19, v204\n"
      "v_bfe_i32 v204, v27, 9, 1\n"
      "v_and_b32 v204, s76, v204\n"
      "v_sub_u32 v15, v15, v204\n"
      "L_ax_21_%=:\n"
      "s_or_b32 s48, s66, s69\n"
      "s_cmp_lg_u32 s48, 0\n"
      "s_cselect_b32 s48, 0x40000000, 0\n"
      "L_nf_19_%=:\n"
      "s_lshr_b32 s92, s46, 30\n"
      "s_or_b32 s92, s92, s69\n"
      "s_cbranch_scc0 L_nf_22_%=\n"
      "s_bitcmp1_b32 s46, 31\n"
      "s_cselect_b32 s75, 100270464, 0\n"
      "s_bitcmp1_b32 s46, 30\n"
      "s_cselect_b32 s76, 196224, 0\n"
      "s_and_b32 s74, s46, 0x3fffffff\n"
      "s_cmp_ge_u32 s46, 0x40000000\n"
      "s_cselect_b32 s74, s74, 0\n"
      "s_cselect_b32 s46, 0, s46\n"
      "s_sub_u32 s74, s74, s69\n"
      "s_add_u32 s69, s69, s74\n"
      "s_sub_u32 s75, s75, s70\n"
      "s_add_u32 s70, s70, s75\n"
      "s_sub_u32 s76, s76, s71\n"
      "s_add_u32 s71, s71, s76\n"
      "v_add_u32 v20, s74, v20\n"
      "v_add_u32 v21, s74, v21\n"
      "v_add_u32 v22, s74, v22\n"
      "v_add_u32 v23, s74, v23\n"
      "v_add_u32 v25, s74, v25\n"
      "s_cmp_eq_u32 s75, 0\n"
      "s_cbranch_scc1 L_ax_23_%=\n"
      "v_bfe_i32 v204, v27, 10, 1\n"
      "v_and_b32 v204, s75, v204\n"
      "v_sub_u32 v20, v20, v204\n"
      "v_bfe_i32 v204, v27, 12, 1\n"
      "v_and_b32 v204, s75, v204\n"
      "v_sub_u32 v21, v21, v204\n"
      "v_bfe_i32 v204, v27, 14, 1\n"
      "v_and_b32 v204, s75, v204\n"
      "v_sub_u32 v22, v22, v204\n"
      "v_bfe_i32 v204, v27, 16, 1\n"
      "v_and_b32 v204, s75, v204\n"
      "v_sub_u32 v23, v23, v204\n"
      "v_bfe_i32 v204, v27, 18, 1\n"
      "v_and_b32 v204, s75, v204\n"
      "v_sub_u32 v25, v25, v204\n"
      "L_ax_23_%=:\n"
      "s_cmp_eq_u32 s76, 0\n"
      "s_cbranch_scc1 L_ax_24_%=\n"
      "v_bfe_i32 v204, v27, 11, 1\n"
      "v_and_b32 v204, s76, v204\n"
      "v_sub_u32 v20, v20, v204\n"
      "v_bfe_i32 v204, v27, 13, 1\n"
      "v_and_b32 v204, s76, v204\n"
      "v_sub_u32 v21, v21, v204\n"
      "v_bfe_i32 v204, v27, 15, 1\n"
      "v_and_b32 v204, s76, v204\n"
      "v_sub_u32 v22, v22, v204\n"
      "v_bfe_i32 v204, v27, 17, 1\n"
      "v_and_b32 v204, s76, v204\n"
      "v_sub_u32 v23, v23, v204\n"
      "v_bfe_i32 v204, v27, 19, 1\n"
      "v_and_b32 v204, s76, v204\n"
      "v_sub_u32 v25, v25, v204\n"
      "L_ax_24_%=:\n"
      "s_or_b32 s48, s66, s69\n"
      "s_cmp_lg_u32 s48, 0\n"
      "s_cselect_b32 s48, 0x40000000, 0\n"
      "L_nf_22_%=:\n"
      "s_sub_u32 s92, s42, 1\n"
      "s_cmp_gt_u32 s92, s44\n"
      "s_cbranch_scc1 L_tail0_%=\n"
      "L_nofix_18_%=:\n"
      "s_cmp_lg_u64 s[56:57], 0\n"
      "s_cbranch_scc1 L_slow0_%=\n"
      "L_sel0_%=:\n"
      "s_cmp_lg_u64 s[54:55], 0\n"
      "s_cbranch_scc1 L_max0_%=\n"
      "s_branch L_loop0_%=\n"
      "L_slow0_%=:\n"
      "s_nop 4\n"
      "v_mfma_f32_16x16x32_bf16 v[202:205], v[210:213], v[194:197], 0\n"
      "ds_read_b128 v[154:157], v0\n"
      "v_subrev_u32 v195, 115456, v9\n"
      "v_mfma_f32_16x16x32_bf16 v[202:205], v[118:121], v[76:79], v[202:205]\n"
      "ds_read_b128 v[170:173], v1\n"
      "v_subrev_u32 v76, 115456, v9\n"
      "v_mfma_f32_16x16x32_bf16 v[104:107], v[210:213], v[130:133], 0\n"
      "ds_read_b128 v[184:187], v0 offset:6144\n"
      "v_bfe_u32 v77, v76, 4, 4\n"
      "v_mfma_f32_16x16x32_bf16 v[104:107], v[118:121], v[80:83], v[104:107]\n"
      "ds_read_b128 v[196:199], v9 offset:1024\n"
      "v_mul_u32_u24 v82, 98, v77\n"
      "v_mfma_f32_16x16x32_bf16 v[174:177], v[210:213], v[214:217], 0\n"
      "s_add_u32 s72, s42, 1\n"
      "v_readlane_b32 s77, v38, s72\n"
      "v_lshrrev_b32 v76, 8, v76\n"
      "v_mfma_f32_16x16x32_bf16 v[174:177], v[118:121], v[84:87], v[174:177]\n"
      "s_mov_b32 m0, 110592\n"
      "v_mov_b32_dpp v188, v188 quad_perm:[0,0,0,0] row_mask:0xf bank_mask:0xf\n"
      "v_rcp_f32 v188, v188\n"
      "s_waitcnt lgkmcnt(3)\n"
      "v_mfma_f32_16x16x32_bf16 v[150:153], a[0:3], v[154:157], 0\n"
      "s_and_b32 s73, s77, -16\n"
      "v_cndmask_b32 v202, v202, v104, s[58:59]\n"
      "v_lshl_add_u32 v82, v76, 3, v82\n"
      "v_mfma_f32_16x16x32_bf16 v[210:213], a[24:27], v[154:157], 0\n"
      "buffer_load_dwordx4 v195, s[28:31], s73 offen  lds\n"
      "v_mfma_f32_16x16x32_bf16 v[134:137], a[48:51], v[154:157], 0\n"
      "s_mov_b32 m0, 111616\n"
      "s_waitcnt lgkmcnt(0)\n"
      "v_add_f32_e64 v196, v196, v197\n"
      "v_mfma_f32_16x16x32_bf16 v[104:107], a[72:75], v[154:157], 0\n"
      "ds_read_b128 v[204:207], v1 offset:6144\n"
      "v_add_f32_e64 v198, v198, v199\n"
      "v_mfma_f32_16x16x32_bf16 v[122:125], a[96:99], v[154:157], 0\n"
      "ds_read_b128 v[118:121], v0 offset:12288\n"
      "v_add_f32 v196, v196, v198\n"
      "v_mfma_f32_16x16x32_bf16 v[218:221], a[120:123], v[154:157], 0\n"
      "ds_read_b128 v[162:165], v1 offset:12288\n"
      "v_rsq_f32 v196, v196\n"
      "v_mfma_f32_16x16x32_bf16 v[150:153], a[4:7], v[170:173], v[150:153]\n"
      "s_add_u32 s73, s73, 1024\n"
      "v_mul_f32 v196, 0x415db3d7, v196\n"
      "v_mov_b32_e64 v198, v196\n"
      "v_mfma_f32_16x16x32_bf16 v[150:153], a[8:11], v[184:187], v[150:153]\n"
      "buffer_load_dwordx4 v195, s[28:31], s73 offen  lds\n"
      "v_mfma_f32_16x16x32_bf16 v[210:213], a[28:31], v[170:173], v[210:213]\n"
      "s_mov_b32 m0, 112640\n"
      "v_permlane32_swap_b32 v196, v198\n"
      "v_mfma_f32_16x16x32_bf16 v[210:213], a[32:35], v[184:187], v[210:213]\n"
      "s_add_u32 s73, s73, 1024\n"
      "v_mov_b32 v197, v196\n"
      "v_mul_f32_e64 v144, v198, v144\n"
      "v_mfma_f32_16x16x32_bf16 v[134:137], a[52:55], v[170:173], v[134:137]\n"
      "s_and_b32 s77, s77, 15\n"
      "v_permlane16_swap_b32 v196, v197\n"
      "v_mfma_f32_16x16x32_bf16 v[134:137], a[56:59], v[184:187], v[134:137]\n"
      "buffer_load_dwordx4 v195, s[28:31], s73 offen  lds\n"
      "v_mfma_f32_16x16x32_bf16 v[104:107], a[76:79], v[170:173], v[104:107]\n"
      "s_mov_b32 m0, 113664\n"
      "v_mul_f32 v158, v196, v158\n"
      "v_mul_f32 v159, v196, v159\n"
      "v_mfma_f32_16x16x32_bf16 v[104:107], a[80:83], v[184:187], v[104:107]\n"
      "s_add_u32 s73, s73, 1024\n"
      "v_cvt_pk_bf16_f32 v158, v158, v159\n"
      "v_mul_f32 v160, v196, v160\n"
      "v_mfma_f32_16x16x32_bf16 v[122:125], a[100:103], v[170:173], v[122:125]\n"
      "s_waitcnt lgkmcnt(0)\n"
      "v_mul_f32 v161, v196, v161\n"
      "v_mul_f32 v114, v196, v114\n"
      "v_mfma_f32_16x16x32_bf16 v[122:125], a[104:107], v[184:187], v[122:125]\n"
      "buffer_load_dwordx4 v195, s[28:31], s73 offen  lds\n"
      "v_mfma_f32_16x16x32_bf16 v[218:221], a[124:127], v[170:173], v[218:221]\n"
      "s_mov_b32 m0, 114688\n"
      "v_mul_f32_e64 v115, v196, v115\n"
      "v_cvt_pk_bf16_f32 v159, v160, v161\n"
      "v_mfma_f32_16x16x32_bf16 v[218:221], a[128:131], v[184:187], v[218:221]\n"
      "s_add_u32 s73, s73, 1024\n"
      "v_mul_f32 v96, v196, v96\n"
      "v_mul_f32 v97, v196, v97\n"
      "v_mfma_f32_16x16x32_bf16 v[150:153], a[12:15], v[204:207], v[150:153]\n"
      "v_mul_f32_e64 v98, v196, v98\n"
      "v_mul_f32 v99, v196, v99\n"
      "v_mfma_f32_16x16x32_bf16 v[150:153], a[16:19], v[118:121], v[150:153]\n"
      "s_mov_b32 exec_hi, 0xffff\n"
      "buffer_load_dwordx4 v195, s[28:31], s73 offen  lds\n"
      "s_mov_b32 exec_hi, -1\n"
      "v_mfma_f32_16x16x32_bf16 v[150:153], a[20:23], v[162:165], v[150:153]\n"
      "s_add_u32 m0, s64, 18432\n"
      "v_cvt_pk_bf16_f32 v114, v114, v115\n"
      "v_mul_f32_e64 v116, v196, v116\n"
      "v_mfma_f32_16x16x32_bf16 v[210:213], a[36:39], v[204:207], v[210:213]\n"
      "s_add_u32 s73, s73, 1024\n"
      "v_mul_f32_e64 v117, v196, v117\n"
      "v_cvt_pk_bf16_f32 v160, v96, v97\n"
      "v_mfma_f32_16x16x32_bf16 v[210:213], a[40:43], v[118:121], v[210:213]\n"
      "v_cvt_pk_bf16_f32 v161, v98, v99\n"
      "v_cvt_pk_bf16_f32 v115, v116, v117\n"
      "v_mfma_f32_16x16x32_bf16 v[210:213], a[44:47], v[162:165], v[210:213]\n"
      "ds_write_b128 v5, v[158:161]\n"
      "v_mfma_f32_16x16x32_bf16 v[134:137], a[60:63], v[204:207], v[134:137]\n"
      "buffer_load_dwordx4 v16, s[16:19], s45 offen  lds\n"
      "v_mfma_f32_16x16x32_bf16 v[134:137], a[64:67], v[118:121], v[134:137]\n"
      "s_add_u32 m0, s64, 22528\n"
      "v_mul_f32 v178, v197, v178\n"
      "v_mul_f32 v179, v197, v179\n"
      "v_mfma_f32_16x16x32_bf16 v[134:137], a[68:71], v[162:165], v[134:137]\n"
      "ds_write_b64 v6, v[114:115]\n"
      "v_mfma_f32_16x16x32_bf16 v[104:107], a[84:87], v[204:207], v[104:107]\n"
      "v_cvt_pk_bf16_f32 v178, v178, v179\n"
      "v_mul_f32_e64 v180, v197, v180\n"
      "v_mfma_f32_16x16x32_bf16 v[104:107], a[88:91], v[118:121], v[104:107]\n"
      "buffer_load_dwordx4 v17, s[16:19], s45 offen  lds\n"
      "v_mfma_f32_16x16x32_bf16 v[104:107], a[92:95], v[162:165], v[104:107]\n"
      "s_add_u32 m0, s64, 26624\n"
      "v_mul_f32 v181, v197, v181\n"
      "v_mul_f32 v126, v197, v126\n"
      "v_mfma_f32_16x16x32_bf16 v[122:125], a[108:111], v[204:207], v[122:125]\n"
      "v_mul_f32 v127, v197, v127\n"
      "v_cvt_pk_bf16_f32 v179, v180, v181\n"
      "v_mfma_f32_16x16x32_bf16 v[122:125], a[112:115], v[118:121], v[122:125]\n"
      "v_mul_f32 v166, v197, v166\n"
      "v_mul_f32_e64 v167, v197, v167\n"
      "v_mfma_f32_16x16x32_bf16 v[122:125], a[116:119], v[162:165], v[122:125]\n"
      "buffer_load_dwordx4 v18, s[16:19], s45 offen  lds\n"
      "v_mfma_f32_16x16x32_bf16 v[218:221], a[132:135], v[204:207], v[218:221]\n"
      "s_add_u32 m0, s64, 30720\n"
      "v_mul_f32 v168, v197, v168\n"
      "v_mul_f32 v169, v197, v169\n"
      "v_mfma_f32_16x16x32_bf16 v[218:221], a[136:139], v[118:121], v[218:221]\n"
      "v_cvt_pk_bf16_f32 v126, v126, v127\n"
      "v_mul_f32 v128, v197, v128\n"
      "v_mfma_f32_16x16x32_bf16 v[218:221], a[140:143], v[162:165], v[218:221]\n"
      "v_mul_f32 v129, v197, v129\n"
      "v_cvt_pk_bf16_f32 v180, v166, v167\n"
      "v_mfma_f32_16x16x32_bf16 v[194:197], v[154:157], a[144:147], 0\n"
      "buffer_load_dwordx4 v19, s[16:19], s45 offen  lds\n"
      "v_mfma_f32_16x16x32_bf16 v[194:197], v[170:173], a[148:151], v[194:197]\n"
      "s_add_u32 m0, s65, 34816\n"
      "v_cvt_pk_bf16_f32 v181, v168, v169\n"
      "v_cvt_pk_bf16_f32 v127, v128, v129\n"
      "v_mfma_f32_16x16x32_bf16 v[194:197], v[184:187], a[152:155], v[194:197]\n"
      "ds_write_b128 v5, v[178:181] offset:6144\n"
      "v_mfma_f32_16x16x32_bf16 v[194:197], v[204:207], a[156:159], v[194:197]\n"
      "buffer_load_dwordx4 v15, s[16:19], s45 offen  lds\n"
      "v_mfma_f32_16x16x32_bf16 v[194:197], v[118:121], a[160:163], v[194:197]\n"
      "ds_read_b128 v[158:161], v0 offset:2048\n"
      "v_mul_f32_e64 v145, v198, v145\n"
      "v_mfma_f32_16x16x32_bf16 v[194:197], v[162:165], a[164:167], v[194:197]\n"
      "ds_write_b64 v6, v[126:127] offset:6144\n"
      "v_mfma_f32_16x16x32_bf16 v[130:133], v[154:157], a[168:171], 0\n"
      "ds_read_b128 v[96:99], v1 offset:2048\n"
      "v_cvt_pk_bf16_f32 v144, v144, v145\n"
      "v_mfma_f32_16x16x32_bf16 v[130:133], v[170:173], a[172:175], v[130:133]\n"
      "ds_read_b128 v[114:117], v0 offset:8192\n"
      "v_mul_f32_e64 v146, v198, v146\n"
      "v_mfma_f32_16x16x32_bf16 v[130:133], v[184:187], a[176:179], v[130:133]\n"
      "ds_read_b128 v[178:181], v1 offset:8192\n"
      "v_mul_f32_e64 v147, v198, v147\n"
      "v_mfma_f32_16x16x32_bf16 v[130:133], v[204:207], a[180:183], v[130:133]\n"
      "ds_read_b128 v[126:129], v0 offset:14336\n"
      "v_mul_f32 v138, v198, v138\n"
      "v_mfma_f32_16x16x32_bf16 v[130:133], v[118:121], a[184:187], v[130:133]\n"
      "ds_read_b128 v[166:169], v1 offset:14336\n"
      "v_mul_f32 v139, v198, v139\n"
      "v_mfma_f32_16x16x32_bf16 v[130:133], v[162:165], a[188:191], v[130:133]\n"
      "buffer_load_dwordx4 v[190:193], v36, s[28:31], s47 offen\n"
      "v_mfma_f32_16x16x32_bf16 v[214:217], v[154:157], a[192:195], 0\n"
      "v_cvt_pk_bf16_f32 v145, v146, v147\n"
      "v_mul_f32 v108, v198, v108\n"
      "v_mfma_f32_16x16x32_bf16 v[214:217], v[170:173], a[196:199], v[214:217]\n"
      "v_mul_f32 v109, v198, v109\n"
      "v_mul_f32 v110, v198, v110\n"
      "v_mfma_f32_16x16x32_bf16 v[214:217], v[184:187], a[200:203], v[214:217]\n"
      "s_waitcnt lgkmcnt(3)\n"
      "v_mul_f32_e64 v111, v198, v111\n"
      "v_cvt_pk_bf16_f32 v138, v138, v139\n"
      "v_mfma_f32_16x16x32_bf16 v[214:217], v[204:207], a[204:207], v[214:217]\n"
      "buffer_load_dword v78, v29, s[28:31], s47 offen\n"
      "v_mfma_f32_16x16x32_bf16 v[214:217], v[118:121], a[208:211], v[214:217]\n"
      "v_mul_f32 v140, v198, v140\n"
      "v_mul_f32_e64 v141, v198, v141\n"
      "v_mfma_f32_16x16x32_bf16 v[214:217], v[162:165], a[212:215], v[214:217]\n"
      "s_waitcnt lgkmcnt(0)\n"
      "v_cvt_pk_bf16_f32 v146, v108, v109\n"
      "v_cvt_pk_bf16_f32 v147, v110, v111\n"
      "v_mfma_f32_16x16x32_bf16 v[162:165], a[0:3], v[158:161], 0\n"
      "ds_write_b128 v5, v[144:147] offset:12288\n"
      "v_mfma_f32_16x16x32_bf16 v[162:165], a[4:7], v[96:99], v[162:165]\n"
      "v_cvt_pk_bf16_f32 v139, v140, v141\n"
      "v_fma_f32 v155, v150, v150, s51\n"
      "v_mfma_f32_16x16x32_bf16 v[162:165], a[8:11], v[114:117], v[162:165]\n"
      "v_fma_f32 v120, v104, v104, s51\n"
      "v_fmac_f32_e64 v155, v151, v151\n"
      "v_mfma_f32_16x16x32_bf16 v[162:165], a[12:15], v[178:181], v[162:165]\n"
      "ds_write_b64 v6, v[138:139] offset:12288\n"
      "v_mfma_f32_16x16x32_bf16 v[162:165], a[16:19], v[126:129], v[162:165]\n"
      "v_fmac_f32 v120, v105, v105\n"
      "v_fmac_f32 v155, v152, v152\n"
      "v_mfma_f32_16x16x32_bf16 v[162:165], a[20:23], v[166:169], v[162:165]\n"
      "v_fmac_f32 v120, v106, v106\n"
      "v_fmac_f32 v155, v153, v153\n"
      "v_mfma_f32_16x16x32_bf16 v[108:111], a[24:27], v[158:161], 0\n"
      "v_fmac_f32 v120, v107, v107\n"
      "v_fmac_f32 v155, v210, v210\n"
      "v_mfma_f32_16x16x32_bf16 v[108:111], a[28:31], v[96:99], v[108:111]\n"
      "v_fmac_f32 v120, v122, v122\n"
      "v_fmac_f32 v155, v211, v211\n"
      "v_mfma_f32_16x16x32_bf16 v[108:111], a[32:35], v[114:117], v[108:111]\n"
      "v_fmac_f32 v120, v123, v123\n"
      "v_fmac_f32 v155, v212, v212\n"
      "v_mfma_f32_16x16x32_bf16 v[108:111], a[36:39], v[178:181], v[108:111]\n"
      "v_fmac_f32 v120, v124, v124\n"
      "v_fmac_f32 v155, v213, v213\n"
      "v_mfma_f32_16x16x32_bf16 v[108:111], a[40:43], v[126:129], v[108:111]\n"
      "v_fmac_f32 v120, v125, v125\n"
      "v_fmac_f32 v155, v134, v134\n"
      "v_mfma_f32_16x16x32_bf16 v[108:111], a[44:47], v[166:169], v[108:111]\n"
      "v_fmac_f32 v120, v218, v218\n"
      "v_fmac_f32 v155, v135, v135\n"
      "v_mfma_f32_16x16x32_bf16 v[140:143], a[48:51], v[158:161], 0\n"
      "v_fmac_f32 v120, v219, v219\n"
      "v_fmac_f32 v155, v136, v136\n"
      "v_mfma_f32_16x16x32_bf16 v[140:143], a[52:55], v[96:99], v[140:143]\n"
      "v_fmac_f32 v120, v220, v220\n"
      "v_fmac_f32 v155, v137, v137\n"
      "v_mfma_f32_16x16x32_bf16 v[140:143], a[56:59], v[114:117], v[140:143]\n"
      "v_fmac_f32 v120, v221, v221\n"
      "v_cndmask_b32 v202, v202, v174, s[60:61]\n"
      "v_mfma_f32_16x16x32_bf16 v[140:143], a[60:63], v[178:181], v[140:143]\n"
      "v_permlane16_swap_b32 v155, v120\n"
      "v_mfma_f32_16x16x32_bf16 v[140:143], a[64:67], v[126:129], v[140:143]\n"
      "v_add_f32 v155, v155, v120\n"
      "v_mov_b32 v120, v155\n"
      "v_mfma_f32_16x16x32_bf16 v[140:143], a[68:71], v[166:169], v[140:143]\n"
      "v_mul_f32 v202, v188, v202\n"
      "v_permlane32_swap_b32 v155, v120\n"
      "v_mfma_f32_16x16x32_bf16 v[182:185], a[72:75], v[158:161], 0\n"
      "v_add_f32 v155, v155, v120\n"
      "v_rsq_f32 v155, v155\n"
      "v_mfma_f32_16x16x32_bf16 v[182:185], a[76:79], v[96:99], v[182:185]\n"
      "v_mov_b32_e64 v120, v155\n"
      "v_cvt_pk_bf16_f32 v202, v202, v202\n"
      "v_mfma_f32_16x16x32_bf16 v[182:185], a[80:83], v[114:117], v[182:185]\n"
      "ds_write_b16 v12, v202 offset:16\n"
      "v_mfma_f32_16x16x32_bf16 v[182:185], a[84:87], v[178:181], v[182:185]\n"
      "v_permlane16_swap_b32 v155, v120\n"
      "v_mfma_f32_16x16x32_bf16 v[182:185], a[88:91], v[126:129], v[182:185]\n"
      "v_mul_f32 v155, s50, v155\n"
      "v_mul_f32 v104, v120, v104\n"
      "v_mfma_f32_16x16x32_bf16 v[182:185], a[92:95], v[166:169], v[182:185]\n"
      "v_mul_f32 v105, v120, v105\n"
      "v_mul_f32 v150, v155, v150\n"
      "v_mfma_f32_16x16x32_bf16 v[100:103], a[96:99], v[158:161], 0\n"
      "v_mul_f32 v151, v155, v151\n"
      "v_cvt_pk_bf16_f32 v104, v104, v105\n"
      "v_mfma_f32_16x16x32_bf16 v[100:103], a[100:103], v[96:99], v[100:103]\n"
      "v_mul_f32 v106, v120, v106\n"
      "v_mul_f32 v107, v120, v107\n"
      "v_mfma_f32_16x16x32_bf16 v[100:103], a[104:107], v[114:117], v[100:103]\n"
      "v_mul_f32 v218, v120, v218\n"
      "v_mul_f32 v219, v120, v219\n"
      "v_mfma_f32_16x16x32_bf16 v[100:103], a[108:111], v[178:181], v[100:103]\n"
      "v_cvt_pk_bf16_f32 v150, v150, v151\n"
      "v_mul_f32 v152, v155, v152\n"
      "v_mfma_f32_16x16x32_bf16 v[100:103], a[112:115], v[126:129], v[100:103]\n"
      "v_mul_f32 v153, v155, v153\n"
      "v_mul_f32_e64 v134, v155, v134\n"
      "v_mfma_f32_16x16x32_bf16 v[100:103], a[116:119], v[166:169], v[100:103]\n"
      "v_mul_f32 v135, v155, v135\n"
      "v_cvt_pk_bf16_f32 v105, v106, v107\n"
      "v_mfma_f32_16x16x32_bf16 v[198:201], a[120:123], v[158:161], 0\n"
      "v_mul_f32 v122, v120, v122\n"
      "v_mul_f32 v123, v120, v123\n"
      "v_mfma_f32_16x16x32_bf16 v[198:201], a[124:127], v[96:99], v[198:201]\n"
      "v_mul_f32 v124, v120, v124\n"
      "v_mul_f32_e64 v125, v120, v125\n"
      "v_mfma_f32_16x16x32_bf16 v[198:201], a[128:131], v[114:117], v[198:201]\n"
      "v_cvt_pk_bf16_f32 v218, v218, v219\n"
      "v_mul_f32 v220, v120, v220\n"
      "v_mfma_f32_16x16x32_bf16 v[198:201], a[132:135], v[178:181], v[198:201]\n"
      "v_mul_f32 v221, v120, v221\n"
      "v_cvt_pk_bf16_f32 v151, v152, v153\n"
      "v_mfma_f32_16x16x32_bf16 v[198:201], a[136:139], v[126:129], v[198:201]\n"
      "v_mul_f32 v210, v155, v210\n"
      "v_mul_f32 v211, v155, v211\n"
      "v_mfma_f32_16x16x32_bf16 v[198:201], a[140:143], v[166:169], v[198:201]\n"
      "v_mul_f32 v212, v155, v212\n"
      "v_mul_f32 v213, v155, v213\n"
      "v_mfma_f32_16x16x32_bf16 v[118:121], v[158:161], a[144:147], 0\n"
      "v_cvt_pk_bf16_f32 v134, v134, v135\n"
      "v_mul_f32 v136, v155, v136\n"
      "v_mfma_f32_16x16x32_bf16 v[118:121], v[96:99], a[148:151], v[118:121]\n"
      "v_mul_f32 v137, v155, v137\n"
      "v_cvt_pk_bf16_f32 v106, v122, v123\n"
      "v_mfma_f32_16x16x32_bf16 v[118:121], v[114:117], a[152:155], v[118:121]\n"
      "v_cvt_pk_bf16_f32 v107, v124, v125\n"
      "v_cvt_pk_bf16_f32 v219, v220, v221\n"
      "v_mfma_f32_16x16x32_bf16 v[118:121], v[178:181], a[156:159], v[118:121]\n"
      "ds_read_b128 v[220:223], v0 offset:4096\n"
      "v_cvt_pk_bf16_f32 v152, v210, v211\n"
      "v_mfma_f32_16x16x32_bf16 v[118:121], v[126:129], a[160:163], v[118:121]\n"
      "ds_read_b128 v[154:157], v1 offset:4096\n"
      "v_cvt_pk_bf16_f32 v153, v212, v213\n"
      "v_mfma_f32_16x16x32_bf16 v[118:121], v[166:169], a[164:167], v[118:121]\n"
      "ds_read_b128 v[186:189], v0 offset:10240\n"
      "v_cvt_pk_bf16_f32 v135, v136, v137\n"
      "v_mfma_f32_16x16x32_bf16 v[136:139], v[158:161], a[168:171], 0\n"
      "ds_read_b128 v[210:213], v1 offset:10240\n"
      "v_fma_f32 v124, v162, v162, s51\n"
      "v_mfma_f32_16x16x32_bf16 v[136:139], v[96:99], a[172:175], v[136:139]\n"
      "ds_read_b128 v[170:173], v0 offset:16384\n"
      "v_fma_f32 v122, v182, v182, s51\n"
      "v_mfma_f32_16x16x32_bf16 v[136:139], v[114:117], a[176:179], v[136:139]\n"
      "ds_read_b128 v[206:209], v1 offset:16384\n"
      "v_fmac_f32 v124, v163, v163\n"
      "v_mfma_f32_16x16x32_bf16 v[136:139], v[178:181], a[180:183], v[136:139]\n"
      "s_waitcnt vmcnt(7)\n"
      "v_fmac_f32 v122, v183, v183\n"
      "v_fmac_f32 v124, v164, v164\n"
      "v_mfma_f32_16x16x32_bf16 v[136:139], v[126:129], a[184:187], v[136:139]\n"
      "v_fmac_f32 v122, v184, v184\n"
      "v_fmac_f32 v124, v165, v165\n"
      "v_mfma_f32_16x16x32_bf16 v[136:139], v[166:169], a[188:191], v[136:139]\n"
      "v_fmac_f32 v122, v185, v185\n"
      "v_fmac_f32 v124, v108, v108\n"
      "v_mfma_f32_16x16x32_bf16 v[202:205], v[158:161], a[192:195], 0\n"
      "v_fmac_f32 v122, v100, v100\n"
      "v_fmac_f32 v124, v109, v109\n"
      "v_mfma_f32_16x16x32_bf16 v[202:205], v[96:99], a[196:199], v[202:205]\n"
      "v_fmac_f32 v122, v101, v101\n"
      "v_fmac_f32 v124, v110, v110\n"
      "v_mfma_f32_16x16x32_bf16 v[202:205], v[114:117], a[200:203], v[202:205]\n"
      "s_waitcnt lgkmcnt(0)\n"
      "v_fmac_f32 v122, v102, v102\n"
      "v_fmac_f32 v124, v111, v111\n"
      "v_mfma_f32_16x16x32_bf16 v[202:205], v[178:181], a[204:207], v[202:205]\n"
      "v_fmac_f32 v122, v103, v103\n"
      "v_fmac_f32 v124, v140, v140\n"
      "v_mfma_f32_16x16x32_bf16 v[202:205], v[126:129], a[208:211], v[202:205]\n"
      "v_fmac_f32 v122, v198, v198\n"
      "v_fmac_f32 v124, v141, v141\n"
      "v_mfma_f32_16x16x32_bf16 v[202:205], v[166:169], a[212:215], v[202:205]\n"
      "v_fmac_f32 v122, v199, v199\n"
      "v_fmac_f32 v124, v142, v142\n"
      "v_mfma_f32_16x16x32_bf16 v[146:149], a[0:3], v[220:223], 0\n"
      "v_fmac_f32 v122, v200, v200\n"
      "v_fmac_f32 v124, v143, v143\n"
      "v_mfma_f32_16x16x32_bf16 v[146:149], a[4:7], v[154:157], v[146:149]\n"
      "v_fmac_f32 v122, v201, v201\n"
      "v_add_u32 v82, s77, v82\n"
      "v_mfma_f32_16x16x32_bf16 v[146:149], a[8:11], v[186:189], v[146:149]\n"
      "v_permlane16_swap_b32 v124, v122\n"
      "v_mfma_f32_16x16x32_bf16 v[146:149], a[12:15], v[210:213], v[146:149]\n"
      "v_add_f32 v124, v124, v122\n"
      "v_mov_b32 v122, v124\n"
      "v_mfma_f32_16x16x32_bf16 v[146:149], a[16:19], v[170:173], v[146:149]\n"
      "v_and_b32 v92, 2, v82\n"
      "v_permlane32_swap_b32 v124, v122\n"
      "v_mfma_f32_16x16x32_bf16 v[146:149], a[20:23], v[206:209], v[146:149]\n"
      "v_add_f32 v124, v124, v122\n"
      "v_rsq_f32 v124, v124\n"
      "v_mfma_f32_16x16x32_bf16 v[158:161], a[24:27], v[220:223], 0\n"
      "v_mov_b32 v122, v124\n"
      "v_and_b32 v82, -4, v82\n"
      "v_mfma_f32_16x16x32_bf16 v[158:161], a[28:31], v[154:157], v[158:161]\n"
      "v_permlane16_swap_b32_e64 v124, v122\n"
      "v_mfma_f32_16x16x32_bf16 v[158:161], a[32:35], v[186:189], v[158:161]\n"
      "v_mul_f32 v124, s50, v124\n"
      "v_mul_f32 v182, v122, v182\n"
      "v_mfma_f32_16x16x32_bf16 v[158:161], a[36:39], v[210:213], v[158:161]\n"
      "v_mul_f32 v183, v122, v183\n"
      "v_mul_f32 v162, v124, v162\n"
      "v_mfma_f32_16x16x32_bf16 v[158:161], a[40:43], v[170:173], v[158:161]\n"
      "v_mul_f32 v163, v124, v163\n"
      "v_cvt_pk_bf16_f32 v182, v182, v183\n"
      "v_mfma_f32_16x16x32_bf16 v[158:161], a[44:47], v[206:209], v[158:161]\n"
      "v_mul_f32 v184, v122, v184\n"
      "v_mul_f32 v185, v122, v185\n"
      "v_mfma_f32_16x16x32_bf16 v[112:115], a[48:51], v[220:223], 0\n"
      "v_mul_f32 v198, v122, v198\n"
      "v_mul_f32_e64 v199, v122, v199\n"
      "v_mfma_f32_16x16x32_bf16 v[112:115], a[52:55], v[154:157], v[112:115]\n"
      "v_cvt_pk_bf16_f32 v162, v162, v163\n"
      "v_mul_f32 v164, v124, v164\n"
      "v_mfma_f32_16x16x32_bf16 v[112:115], a[56:59], v[186:189], v[112:115]\n"
      "v_mul_f32 v165, v124, v165\n"
      "v_mul_f32_e64 v140, v124, v140\n"
      "v_mfma_f32_16x16x32_bf16 v[112:115], a[60:63], v[210:213], v[112:115]\n"
      "v_mul_f32 v141, v124, v141\n"
      "v_cvt_pk_bf16_f32 v183, v184, v185\n"
      "v_mfma_f32_16x16x32_bf16 v[112:115], a[64:67], v[170:173], v[112:115]\n"
      "v_mul_f32 v100, v122, v100\n"
      "v_mul_f32 v101, v122, v101\n"
      "v_mfma_f32_16x16x32_bf16 v[112:115], a[68:71], v[206:209], v[112:115]\n"
      "v_mul_f32 v102, v122, v102\n"
      "v_mul_f32_e64 v103, v122, v103\n"
      "v_mfma_f32_16x16x32_bf16 v[174:177], a[72:75], v[220:223], 0\n"
      "v_cvt_pk_bf16_f32 v198, v198, v199\n"
      "v_mul_f32 v200, v122, v200\n"
      "v_mfma_f32_16x16x32_bf16 v[174:177], a[76:79], v[154:157], v[174:177]\n"
      "v_mul_f32 v201, v122, v201\n"
      "v_cvt_pk_bf16_f32 v163, v164, v165\n"
      "v_mfma_f32_16x16x32_bf16 v[174:177], a[80:83], v[186:189], v[174:177]\n"
      "v_mul_f32 v108, v124, v108\n"
      "v_mul_f32 v109, v124, v109\n"
      "v_mfma_f32_16x16x32_bf16 v[174:177], a[84:87], v[210:213], v[174:177]\n"
      "v_mul_f32 v110, v124, v110\n"
      "v_mul_f32 v111, v124, v111\n"
      "v_mfma_f32_16x16x32_bf16 v[174:177], a[88:91], v[170:173], v[174:177]\n"
      "v_cvt_pk_bf16_f32 v140, v140, v141\n"
      "v_mul_f32 v142, v124, v142\n"
      "v_mfma_f32_16x16x32_bf16 v[174:177], a[92:95], v[206:209], v[174:177]\n"
      "v_mul_f32 v143, v124, v143\n"
      "v_cvt_pk_bf16_f32 v184, v100, v101\n"
      "v_mfma_f32_16x16x32_bf16 v[96:99], a[96:99], v[220:223], 0\n"
      "v_cvt_pk_bf16_f32 v185, v102, v103\n"
      "v_cvt_pk_bf16_f32 v199, v200, v201\n"
      "v_mfma_f32_16x16x32_bf16 v[96:99], a[100:103], v[154:157], v[96:99]\n"
      "v_cvt_pk_bf16_f32 v164, v108, v109\n"
      "v_cvt_pk_bf16_f32 v165, v110, v111\n"
      "v_mfma_f32_16x16x32_bf16 v[96:99], a[104:107], v[186:189], v[96:99]\n"
      "v_cvt_pk_bf16_f32 v141, v142, v143\n"
      "v_fma_f32 v103, v146, v146, s51\n"
      "v_mfma_f32_16x16x32_bf16 v[96:99], a[108:111], v[210:213], v[96:99]\n"
      "v_fma_f32 v102, v174, v174, s51\n"
      "v_fmac_f32 v103, v147, v147\n"
      "v_mfma_f32_16x16x32_bf16 v[96:99], a[112:115], v[170:173], v[96:99]\n"
      "v_fmac_f32 v102, v175, v175\n"
      "v_fmac_f32 v103, v148, v148\n"
      "v_mfma_f32_16x16x32_bf16 v[96:99], a[116:119], v[206:209], v[96:99]\n"
      "v_fmac_f32 v102, v176, v176\n"
      "v_fmac_f32 v103, v149, v149\n"
      "v_mfma_f32_16x16x32_bf16 v[122:125], a[120:123], v[220:223], 0\n"
      "v_fmac_f32 v102, v177, v177\n"
      "v_fmac_f32 v103, v158, v158\n"
      "v_mfma_f32_16x16x32_bf16 v[122:125], a[124:127], v[154:157], v[122:125]\n"
      "v_fmac_f32 v103, v159, v159\n"
      "v_fmac_f32 v103, v160, v160\n"
      "v_mfma_f32_16x16x32_bf16 v[122:125], a[128:131], v[186:189], v[122:125]\n"
      "v_fmac_f32 v102, v96, v96\n"
      "v_fmac_f32 v102, v97, v97\n"
      "v_mfma_f32_16x16x32_bf16 v[122:125], a[132:135], v[210:213], v[122:125]\n"
      "v_fmac_f32 v102, v98, v98\n"
      "v_fmac_f32 v103, v161, v161\n"
      "v_mfma_f32_16x16x32_bf16 v[122:125], a[136:139], v[170:173], v[122:125]\n"
      "v_fmac_f32 v102, v99, v99\n"
      "v_fmac_f32 v103, v112, v112\n"
      "v_mfma_f32_16x16x32_bf16 v[122:125], a[140:143], v[206:209], v[122:125]\n"
      "v_fmac_f32 v103, v113, v113\n"
      "v_fmac_f32 v103, v114, v114\n"
      "v_mfma_f32_16x16x32_bf16 v[178:181], v[220:223], a[144:147], 0\n"
      "v_fmac_f32 v103, v115, v115\n"
      "v_add_u32 v86, 110592, v82\n"
      "v_mfma_f32_16x16x32_bf16 v[178:181], v[154:157], a[148:151], v[178:181]\n"
      "v_lshlrev_b32 v76, 2, v76\n"
      "v_sub_u32 v77, v77, v76\n"
      "v_mfma_f32_16x16x32_bf16 v[178:181], v[186:189], a[152:155], v[178:181]\n"
      "v_fmac_f32 v102, v122, v122\n"
      "v_fmac_f32 v102, v123, v123\n"
      "v_mfma_f32_16x16x32_bf16 v[178:181], v[210:213], a[156:159], v[178:181]\n"
      "ds_read_b128 v[166:169], v2 offset:18432\n"
      "v_fmac_f32 v102, v124, v124\n"
      "v_mfma_f32_16x16x32_bf16 v[178:181], v[170:173], a[160:163], v[178:181]\n"
      "ds_read_b128 v[126:129], v2 offset:21504\n"
      "v_fmac_f32 v102, v125, v125\n"
      "v_mfma_f32_16x16x32_bf16 v[178:181], v[206:209], a[164:167], v[178:181]\n"
      "ds_read_b128 v[108:111], v2 offset:24576\n"
      "v_permlane16_swap_b32 v103, v102\n"
      "v_add_f32 v103, v103, v102\n"
      "v_mov_b32 v102, v103\n"
      "v_lshlrev_b32 v77, 4, v77\n"
      "v_cmp_gt_u32 s[72:73], 64, v77\n"
      "v_permlane32_swap_b32 v103, v102\n"
      "v_add_f32 v103, v103, v102\n"
      "v_rsq_f32 v103, v103\n"
      "v_cvt_pk_bf16_f32 v194, v194, v195\n"
      "v_mov_b32 v102, v103\n"
      "v_cvt_pk_bf16_f32 v195, v196, v197\n"
      "v_mov_b32 v200, 0x3fb9\n"
      "v_permlane16_swap_b32 v103, v102\n"
      "v_mul_f32 v103, s50, v103\n"
      "v_mul_f32 v174, v102, v174\n"
      "v_mul_f32 v175, v102, v175\n"
      "v_mul_f32 v146, v103, v146\n"
      "v_mul_f32 v147, v103, v147\n"
      "v_cvt_pk_bf16_f32 v174, v174, v175\n"
      "v_mul_f32 v176, v102, v176\n"
      "v_mul_f32 v177, v102, v177\n"
      "v_mul_f32 v122, v102, v122\n"
      "v_mul_f32 v123, v102, v123\n"
      "v_cvt_pk_bf16_f32 v146, v146, v147\n"
      "v_mul_f32 v148, v103, v148\n"
      "v_mul_f32 v149, v103, v149\n"
      "v_mul_f32 v112, v103, v112\n"
      "v_mul_f32 v113, v103, v113\n"
      "v_cvt_pk_bf16_f32 v175, v176, v177\n"
      "v_mul_f32 v96, v102, v96\n"
      "v_mul_f32 v97, v102, v97\n"
      "v_mul_f32 v98, v102, v98\n"
      "v_mul_f32 v99, v102, v99\n"
      "v_cvt_pk_bf16_f32 v122, v122, v123\n"
      "v_mul_f32 v124, v102, v124\n"
      "v_mul_f32 v125, v102, v125\n"
      "v_cvt_pk_bf16_f32 v147, v148, v149\n"
      "v_mul_f32 v158, v103, v158\n"
      "v_mul_f32 v159, v103, v159\n"
      "v_mul_f32 v160, v103, v160\n"
      "v_mul_f32 v161, v103, v161\n"
      "v_cvt_pk_bf16_f32 v112, v112, v113\n"
      "v_mul_f32 v114, v103, v114\n"
      "v_mul_f32 v115, v103, v115\n"
      "v_cvt_pk_bf16_f32 v176, v96, v97\n"
      "v_mfma_f32_16x16x32_bf16 v[100:103], v[220:223], a[168:171], 0\n"
      "v_cvt_pk_bf16_f32 v177, v98, v99\n"
      "v_cvt_pk_bf16_f32 v123, v124, v125\n"
      "v_mfma_f32_16x16x32_bf16 v[100:103], v[154:157], a[172:175], v[100:103]\n"
      "v_cvt_pk_bf16_f32 v148, v158, v159\n"
      "v_cvt_pk_bf16_f32 v149, v160, v161\n"
      "v_mfma_f32_16x16x32_bf16 v[100:103], v[186:189], a[176:179], v[100:103]\n"
      "v_cvt_pk_bf16_f32 v113, v114, v115\n"
      "v_mov_b32_e64 v201, 0\n"
      "v_mfma_f32_16x16x32_bf16 v[100:103], v[210:213], a[180:183], v[100:103]\n"
      "v_lshlrev_b64 v[200:201], v77, v[200:201]\n"
      "v_cndmask_b32_e64 v200, 0, v200, s[72:73]\n"
      "v_mfma_f32_16x16x32_bf16 v[100:103], v[170:173], a[184:187], v[100:103]\n"
      "v_cndmask_b32_e64 v201, 0, v201, s[72:73]\n"
      "v_cmp_gt_u32 s[72:73], 64, v77\n"
      "v_mfma_f32_16x16x32_bf16 v[100:103], v[206:209], a[188:191], v[100:103]\n"
      "ds_read_b128 v[96:99], v7 offset:18432\n"
      "v_mov_b32 v124, 0xbb2b\n"
      "v_mfma_f32_16x16x32_bf16 v[142:145], v[220:223], a[192:195], 0\n"
      "ds_read_b128 v[220:223], v2 offset:27648\n"
      "v_mov_b32 v125, 0\n"
      "v_mfma_f32_16x16x32_bf16 v[142:145], v[154:157], a[196:199], v[142:145]\n"
      "ds_read_b128 v[154:157], v2 offset:30720\n"
      "v_lshlrev_b64 v[124:125], v77, v[124:125]\n"
      "v_mfma_f32_16x16x32_bf16 v[142:145], v[186:189], a[200:203], v[142:145]\n"
      "ds_read_b128 v[186:189], v7 offset:22528\n"
      "v_cndmask_b32_e64 v124, 0, v124, s[72:73]\n"
      "v_mfma_f32_16x16x32_bf16 v[142:145], v[210:213], a[204:207], v[142:145]\n"
      "ds_read_b128 v[210:213], v2 offset:33792\n"
      "v_cndmask_b32_e64 v125, 0, v125, s[72:73]\n"
      "v_mfma_f32_16x16x32_bf16 v[142:145], v[170:173], a[208:211], v[142:145]\n"
      "ds_read_b128 v[170:173], v7 offset:26624\n"
      "v_add_u32 v90, 112160, v82\n"
      "v_mfma_f32_16x16x32_bf16 v[142:145], v[206:209], a[212:215], v[142:145]\n"
      "s_waitcnt lgkmcnt(5)\n"
      "v_cvt_pk_bf16_f32 v196, v118, v119\n"
      "v_cvt_pk_bf16_f32 v197, v120, v121\n"
      "v_mfma_f32_16x16x32_bf16 v[158:161], a[216:219], v[166:169], 0\n"
      "buffer_store_dwordx4 v[96:99], v20, s[24:27], s46 offen sc1\n"
      "v_mfma_f32_16x16x32_bf16 v[158:161], a[220:223], v[126:129], v[158:161]\n"
      "ds_read_b128 v[118:121], v7 offset:30720\n"
      "v_cvt_pk_bf16_f32 v76, v178, v179\n"
      "v_mfma_f32_16x16x32_bf16 v[158:161], a[224:227], v[108:111], v[158:161]\n"
      "v_cvt_pk_bf16_f32 v77, v180, v181\n"
      "v_cvt_pk_bf16_f32 v130, v130, v131\n"
      "v_mfma_f32_16x16x32_bf16 v[96:99], a[240:243], v[166:169], 0\n"
      "v_cvt_pk_bf16_f32 v131, v132, v133\n"
      "v_cvt_pk_bf16_f32 v132, v136, v137\n"
      "v_mfma_f32_16x16x32_bf16 v[96:99], a[244:247], v[126:129], v[96:99]\n"
      "v_cvt_pk_bf16_f32 v133, v138, v139\n"
      "v_cvt_pk_bf16_f32 v80, v100, v101\n"
      "v_mfma_f32_16x16x32_bf16 v[96:99], a[248:251], v[108:111], v[96:99]\n"
      "ds_read_b128 v[178:181], v7 offset:34816\n"
      "v_cvt_pk_bf16_f32 v81, v102, v103\n"
      "v_mfma_f32_16x16x32_bf16 v[114:117], v[232:235], v[166:169], 0\n"
      "s_waitcnt lgkmcnt(2)\n"
      "v_cvt_pk_bf16_f32 v214, v214, v215\n"
      "v_cvt_pk_bf16_f32 v215, v216, v217\n"
      "v_mfma_f32_16x16x32_bf16 v[114:117], v[236:239], v[126:129], v[114:117]\n"
      "ds_read_b128 v[100:103], v10 offset:32\n"
      "v_cvt_pk_bf16_f32 v216, v202, v203\n"
      "v_mfma_f32_16x16x32_bf16 v[114:117], v[240:243], v[108:111], v[114:117]\n"
      "buffer_store_dwordx4 v[186:189], v21, s[24:27], s46 offen sc1\n"
      "v_mfma_f32_16x16x32_bf16 v[158:161], a[228:231], v[220:223], v[158:161]\n"
      "s_waitcnt lgkmcnt(2)\n"
      "v_cvt_pk_bf16_f32 v217, v204, v205\n"
      "v_cvt_pk_bf16_f32 v84, v142, v143\n"
      "v_mfma_f32_16x16x32_bf16 v[158:161], a[232:235], v[154:157], v[158:161]\n"
      "ds_read_b64 v[208:209], v10 offset:24\n"
      "v_cvt_pk_bf16_f32 v85, v144, v145\n"
      "v_mfma_f32_16x16x32_bf16 v[158:161], a[236:239], v[210:213], v[158:161]\n"
      "ds_read2_b32 v[168:169], v86 offset0:0 offset1:1\n"
      "v_add_u32 v207, 113728, v82\n"
      "v_mfma_f32_16x16x32_bf16 v[96:99], a[252:255], v[220:223], v[96:99]\n"
      "ds_read2_b32 v[186:187], v86 offset0:2 offset1:10\n"
      "v_subrev_u32 v8, 115456, v9\n"
      "v_mfma_f32_16x16x32_bf16 v[96:99], v[224:227], v[154:157], v[96:99]\n"
      "s_waitcnt lgkmcnt(3)\n"
      "v_lshrrev_b32 v8, 3, v8\n"
      "v_min_u32 v8, 96, v8\n"
      "v_mfma_f32_16x16x32_bf16 v[96:99], v[228:231], v[210:213], v[96:99]\n"
      "ds_read2_b32 v[166:167], v86 offset0:8 offset1:9\n"
      "v_add_u32_e64 v8, s77, v8\n"
      "v_mfma_f32_16x16x32_bf16 v[114:117], v[244:247], v[220:223], v[114:117]\n"
      "ds_read2_b32 v[126:127], v86 offset0:16 offset1:17\n"
      "v_add_u32 v8, 115296, v8\n"
      "v_mfma_f32_16x16x32_bf16 v[114:117], v[248:251], v[154:157], v[114:117]\n"
      "buffer_store_dwordx4 v[170:173], v22, s[24:27], s46 offen sc1\n"
      "v_mfma_f32_16x16x32_bf16 v[114:117], v[252:255], v[210:213], v[114:117]\n"
      "ds_read2_b32 v[128:129], v86 offset0:24 offset1:18\n"
      "s_waitcnt lgkmcnt(3)\n"
      "v_alignbyte_b32 v168, v169, v168, v92\n"
      "v_mfma_f32_16x16x32_bf16 v[108:111], v[104:107], v[150:153], v[40:43]\n"
      "v_alignbyte_b32 v169, v186, v169, v92\n"
      "v_fma_f32 v39, v158, v158, s52\n"
      "v_mfma_f32_16x16x32_bf16 v[210:213], v[182:185], v[150:153], v[44:47]\n"
      "ds_read_b128 v[202:205], v2 offset:18688\n"
      "s_waitcnt lgkmcnt(3)\n"
      "v_alignbyte_b32 v166, v167, v166, v92\n"
      "v_mfma_f32_16x16x32_bf16 v[136:139], v[174:177], v[150:153], v[48:51]\n"
      "ds_read_b128 v[142:145], v2 offset:21760\n"
      "v_alignbyte_b32 v167, v187, v167, v92\n"
      "v_mfma_f32_16x16x32_bf16 v[186:189], v[100:103], v[150:153], v[24:27]\n"
      "s_waitcnt lgkmcnt(3)\n"
      "v_alignbyte_b32 v126, v127, v126, v92\n"
      "v_fmac_f32 v39, v159, v159\n"
      "v_mfma_f32_16x16x16_bf16 v[108:111], v[218:219], v[134:135], v[108:111]\n"
      "v_fmac_f32 v39, v160, v160\n"
      "v_fmac_f32_e64 v39, v161, v161\n"
      "s_waitcnt lgkmcnt(2)\n"
      "v_alignbyte_b32 v127, v129, v127, v92\n"
      "v_mfma_f32_16x16x16_bf16 v[210:213], v[198:199], v[134:135], v[210:213]\n"
      "v_alignbyte_b32 v128, v128, v128, v92\n"
      "buffer_store_dwordx4 v[118:121], v23, s[24:27], s46 offen sc1\n"
      "v_mfma_f32_16x16x16_bf16 v[136:139], v[122:123], v[134:135], v[136:139]\n"
      "v_fmac_f32 v39, v96, v96\n"
      "v_fmac_f32 v39, v97, v97\n"
      "v_fmac_f32 v39, v98, v98\n"
      "v_mfma_f32_16x16x16_bf16 v[186:189], v[208:209], v[134:135], v[186:189]\n"
      "v_fmac_f32 v39, v99, v99\n"
      "v_fmac_f32 v39, v114, v114\n"
      "v_fmac_f32 v39, v115, v115\n"
      "v_mfma_f32_16x16x16_bf16 v[108:111], v[200:201], v[168:169], v[108:111]\n"
      "v_fmac_f32_e64 v39, v116, v116\n"
      "v_fmac_f32_e64 v39, v117, v117\n"
      "v_readlane_b32 s45, v37, s43\n"
      "v_mfma_f32_16x16x16_bf16 v[210:213], v[200:201], v[166:167], v[210:213]\n"
      "v_readlane_b32 s47, v38, s43\n"
      "v_add_u32_e64 v12, 32, v12\n"
      "s_waitcnt vmcnt(5)\n"
      "v_or3_b32 v190, v190, v191, v192\n"
      "v_mfma_f32_16x16x16_bf16 v[136:139], v[200:201], v[126:127], v[136:139]\n"
      "ds_read_b128 v[118:121], v2 offset:24832\n"
      "s_and_b32 s47, s47, -4\n"
      "v_mfma_f32_16x16x16_bf16 v[186:189], v[200:201], v[128:129], v[186:189]\n"
      "s_add_u32 s43, s43, 1\n"
      "v_mfma_f32_16x16x16_bf16 v[108:111], v[124:125], v[168:169], v[108:111]\n"
      "v_mfma_f32_16x16x16_bf16 v[210:213], v[124:125], v[166:167], v[210:213]\n"
      "buffer_store_dwordx4 v[178:181], v25, s[24:27], s46 offen sc1\n"
      "v_mfma_f32_16x16x16_bf16 v[136:139], v[124:125], v[126:127], v[136:139]\n"
      "s_waitcnt vmcnt(5)\n"
      "v_or3_b32 v190, v190, v193, v78\n"
      "v_and_b32 v190, 0x7fff7fff, v190\n"
      "v_readlane_b32 s46, v37, s42\n"
      "v_mfma_f32_16x16x16_bf16 v[186:189], v[124:125], v[128:129], v[186:189]\n"
      "ds_write_b32 v31, v190 offset:4608\n"
      "s_waitcnt lgkmcnt(1)\n"
      "v_mfma_f32_16x16x32_bf16 v[178:181], a[216:219], v[202:205], 0\n"
      "s_or_b32 s95, s45, s46\n"
      "v_max3_f32 v78, v108, v109, v110\n"
      "v_max3_f32 v128, v111, v210, v211\n"
      "v_mfma_f32_16x16x32_bf16 v[178:181], a[220:223], v[142:145], v[178:181]\n"
      "ds_read2_b32 v[152:153], v90 offset0:0 offset1:1\n"
      "v_max3_f32 v78, v78, v212, v213\n"
      "v_mfma_f32_16x16x32_bf16 v[178:181], a[224:227], v[118:121], v[178:181]\n"
      "ds_read2_b32 v[170:171], v90 offset0:2 offset1:10\n"
      "v_max3_f32 v128, v128, v136, v137\n"
      "v_mfma_f32_16x16x32_bf16 v[166:169], a[240:243], v[202:205], 0\n"
      "ds_read_b128 v[188:191], v9 offset:5632\n"
      "v_max3_f32 v78, v78, v138, v139\n"
      "v_mfma_f32_16x16x32_bf16 v[166:169], a[244:247], v[142:145], v[166:169]\n"
      "ds_read2_b32 v[150:151], v90 offset0:8 offset1:9\n"
      "v_max3_f32 v78, v78, v128, v186\n"
      "v_mfma_f32_16x16x32_bf16 v[166:169], a[248:251], v[118:121], v[166:169]\n"
      "ds_read2_b32 v[154:155], v90 offset0:16 offset1:17\n"
      "v_mov_b32_e64 v86, v78\n"
      "v_mfma_f32_16x16x32_bf16 v[126:129], v[232:235], v[202:205], 0\n"
      "ds_read2_b32 v[156:157], v90 offset0:24 offset1:18\n"
      "v_permlane16_swap_b32_e64 v78, v86\n"
      "v_mfma_f32_16x16x32_bf16 v[126:129], v[236:239], v[142:145], v[126:129]\n"
      "s_or_b32 s95, s95, s48\n"
      "s_waitcnt lgkmcnt(5)\n"
      "v_alignbyte_b32 v152, v153, v152, v92\n"
      "v_mfma_f32_16x16x32_bf16 v[126:129], v[240:243], v[118:121], v[126:129]\n"
      "s_cmp_ge_u32 s95, 0x40000000\n"
      "s_cselect_b32 s94, -1, -2\n"
      "s_waitcnt lgkmcnt(4)\n"
      "v_alignbyte_b32 v153, v170, v153, v92\n"
      "v_mfma_f32_16x16x32_bf16 v[118:121], v[104:107], v[162:165], v[52:55]\n"
      "s_cmp_gt_u32 s42, s44\n"
      "s_cselect_b32 s95, s94, s95\n"
      "s_waitcnt lgkmcnt(2)\n"
      "v_alignbyte_b32 v150, v151, v150, v92\n"
      "v_mfma_f32_16x16x32_bf16 v[202:205], v[182:185], v[162:165], v[56:59]\n"
      "s_add_u32 s42, s42, 1\n"
      "v_alignbyte_b32 v151, v171, v151, v92\n"
      "s_waitcnt lgkmcnt(1)\n"
      "v_alignbyte_b32 v154, v155, v154, v92\n"
      "v_max_f32 v78, v78, v86\n"
      "s_waitcnt lgkmcnt(0)\n"
      "v_alignbyte_b32 v155, v157, v155, v92\n"
      "v_alignbyte_b32 v156, v156, v156, v92\n"
      "v_mov_b32 v86, v78\n"
      "v_or3_b32 v188, v188, v189, v190\n"
      "v_mfma_f32_16x16x32_bf16 v[142:145], v[174:177], v[162:165], v[60:63]\n"
      "v_permlane32_swap_b32 v78, v86\n"
      "v_mfma_f32_16x16x32_bf16 v[170:173], v[100:103], v[162:165], v[26:29]\n"
      "v_max_f32 v78, v78, v86\n"
      "v_sub_f32 v108, v108, v78\n"
      "v_mfma_f32_16x16x16_bf16 v[118:121], v[218:219], v[140:141], v[118:121]\n"
      "v_sub_f32 v109, v109, v78\n"
      "v_sub_f32 v110, v110, v78\n"
      "v_sub_f32_e64 v111, v111, v78\n"
      "v_mfma_f32_16x16x16_bf16 v[202:205], v[198:199], v[140:141], v[202:205]\n"
      "v_exp_f32 v108, v108\n"
      "v_exp_f32 v109, v109\n"
      "v_mfma_f32_16x16x16_bf16 v[142:145], v[122:123], v[140:141], v[142:145]\n"
      "v_sub_f32 v210, v210, v78\n"
      "v_sub_f32 v211, v211, v78\n"
      "v_sub_f32 v212, v212, v78\n"
      "v_mfma_f32_16x16x16_bf16 v[170:173], v[208:209], v[140:141], v[170:173]\n"
      "v_sub_f32 v213, v213, v78\n"
      "v_exp_f32 v110, v110\n"
      "v_mfma_f32_16x16x16_bf16 v[118:121], v[200:201], v[152:153], v[118:121]\n"
      "v_exp_f32 v111, v111\n"
      "v_exp_f32 v210, v210\n"
      "v_mfma_f32_16x16x16_bf16 v[202:205], v[200:201], v[150:151], v[202:205]\n"
      "v_exp_f32 v211, v211\n"
      "v_exp_f32 v212, v212\n"
      "v_mfma_f32_16x16x16_bf16 v[142:145], v[200:201], v[154:155], v[142:145]\n"
      "v_exp_f32 v213, v213\n"
      "v_cvt_pk_bf16_f32 v108, v108, v109\n"
      "v_mfma_f32_16x16x16_bf16 v[170:173], v[200:201], v[156:157], v[170:173]\n"
      "v_cvt_pk_bf16_f32 v109, v110, v111\n"
      "v_cvt_pk_bf16_f32 v110, v210, v211\n"
      "v_cvt_pk_bf16_f32 v111, v212, v213\n"
      "v_mfma_f32_16x16x16_bf16 v[118:121], v[124:125], v[152:153], v[118:121]\n"
      "v_sub_f32 v136, v136, v78\n"
      "v_sub_f32 v137, v137, v78\n"
      "v_sub_f32_e64 v138, v138, v78\n"
      "v_mfma_f32_16x16x16_bf16 v[202:205], v[124:125], v[150:151], v[202:205]\n"
      "v_sub_f32 v139, v139, v78\n"
      "v_sub_f32 v186, v186, v78\n"
      "v_exp_f32 v136, v136\n"
      "v_mfma_f32_16x16x16_bf16 v[142:145], v[124:125], v[154:155], v[142:145]\n"
      "v_exp_f32 v137, v137\n"
      "v_exp_f32_e64 v138, v138\n"
      "v_mfma_f32_16x16x16_bf16 v[170:173], v[124:125], v[156:157], v[170:173]\n"
      "ds_read_b128 v[210:213], v2 offset:27904\n"
      "ds_read_b128 v[154:157], v2 offset:30976\n"
      "ds_read_b128 v[150:153], v2 offset:34048\n"
      "v_exp_f32 v139, v139\n"
      "v_exp_f32 v186, v186\n"
      "v_cvt_pk_bf16_f32 v88, v136, v137\n"
      "v_cvt_pk_bf16_f32 v89, v138, v139\n"
      "v_cvt_pk_bf16_f32 v90, v186, 0\n"
      "v_max3_f32 v136, v118, v119, v120\n"
      "v_max3_f32 v78, v121, v202, v203\n"
      "v_max3_f32 v136, v136, v204, v205\n"
      "v_max3_f32 v78, v78, v142, v143\n"
      "v_max3_f32 v136, v136, v144, v145\n"
      "v_max3_f32 v136, v136, v78, v170\n"
      "v_mov_b32 v186, v136\n"
      "v_or_b32 v188, v188, v191\n"
      "v_cmp_ne_u32 s[56:57], 0, v188\n"
      "v_permlane16_swap_b32 v136, v186\n"
      "s_waitcnt lgkmcnt(2)\n"
      "v_mfma_f32_16x16x32_bf16 v[178:181], a[228:231], v[210:213], v[178:181]\n"
      "ds_read_u16 v78, v11\n"
      "v_max_f32 v136, v136, v186\n"
      "s_waitcnt lgkmcnt(2)\n"
      "v_mfma_f32_16x16x32_bf16 v[178:181], a[232:235], v[154:157], v[178:181]\n"
      "ds_read_u16 v82, v11 offset:32\n"
      "v_mov_b32 v186, v136\n"
      "s_waitcnt lgkmcnt(2)\n"
      "v_mfma_f32_16x16x32_bf16 v[178:181], a[236:239], v[150:153], v[178:181]\n"
      "ds_read_u16 v86, v11 offset:64\n"
      "v_permlane32_swap_b32 v136, v186\n"
      "v_mfma_f32_16x16x32_bf16 v[166:169], a[252:255], v[210:213], v[166:169]\n"
      "s_cmp_ge_u32 s95, 0x40000000\n"
      "s_cselect_b64 vcc, -1, s[56:57]\n"
      "v_max_f32 v136, v136, v186\n"
      "v_mfma_f32_16x16x32_bf16 v[166:169], v[224:227], v[154:157], v[166:169]\n"
      "v_sub_f32 v118, v118, v136\n"
      "v_sub_f32 v119, v119, v136\n"
      "v_mfma_f32_16x16x32_bf16 v[166:169], v[228:231], v[150:153], v[166:169]\n"
      "v_sub_f32 v120, v120, v136\n"
      "v_sub_f32 v121, v121, v136\n"
      "v_mfma_f32_16x16x32_bf16 v[126:129], v[244:247], v[210:213], v[126:129]\n"
      "v_exp_f32 v118, v118\n"
      "v_mfma_f32_16x16x32_bf16 v[126:129], v[248:251], v[154:157], v[126:129]\n"
      "v_exp_f32 v119, v119\n"
      "v_mfma_f32_16x16x32_bf16 v[126:129], v[252:255], v[150:153], v[126:129]\n"
      "s_waitcnt lgkmcnt(0)\n"
      "v_sub_f32 v202, v202, v136\n"
      "v_sub_f32_e64 v203, v203, v136\n"
      "v_mfma_f32_16x16x32_bf16 v[186:189], v[32:35], v[108:111], 0\n"
      "ds_read2_b32 v[172:173], v207 offset0:0 offset1:1\n"
      "v_sub_f32 v204, v204, v136\n"
      "v_mfma_f32_16x16x32_bf16 v[186:189], v[32:35], v[88:91], v[186:189]\n"
      "ds_read2_b32 v[150:151], v207 offset0:2 offset1:10\n"
      "v_sub_f32 v205, v205, v136\n"
      "v_mfma_f32_16x16x32_bf16 v[138:141], v[194:197], v[108:111], 0\n"
      "ds_read2_b32 v[164:165], v207 offset0:8 offset1:9\n"
      "v_exp_f32_e64 v120, v120\n"
      "v_mfma_f32_16x16x32_bf16 v[138:141], v[76:79], v[88:91], v[138:141]\n"
      "ds_read2_b32 v[162:163], v207 offset0:16 offset1:17\n"
      "v_exp_f32 v121, v121\n"
      "v_mfma_f32_16x16x32_bf16 v[188:191], v[130:133], v[108:111], 0\n"
      "ds_read2_b32 v[220:221], v207 offset0:24 offset1:18\n"
      "v_exp_f32 v202, v202\n"
      "v_mfma_f32_16x16x32_bf16 v[188:191], v[80:83], v[88:91], v[188:191]\n"
      "v_exp_f32 v203, v203\n"
      "v_mfma_f32_16x16x32_bf16 v[210:213], v[214:217], v[108:111], 0\n"
      "s_waitcnt lgkmcnt(4)\n"
      "v_alignbyte_b32 v172, v173, v172, v92\n"
      "s_waitcnt lgkmcnt(3)\n"
      "v_alignbyte_b32 v173, v150, v173, v92\n"
      "v_mfma_f32_16x16x32_bf16 v[210:213], v[84:87], v[88:91], v[210:213]\n"
      "s_waitcnt lgkmcnt(2)\n"
      "v_alignbyte_b32 v164, v165, v164, v92\n"
      "v_alignbyte_b32 v165, v151, v165, v92\n"
      "v_mfma_f32_16x16x32_bf16 v[150:153], v[104:107], v[146:149], v[64:67]\n"
      "v_exp_f32 v204, v204\n"
      "s_waitcnt lgkmcnt(1)\n"
      "v_alignbyte_b32 v162, v163, v162, v92\n"
      "v_exp_f32 v205, v205\n"
      "s_waitcnt lgkmcnt(0)\n"
      "v_alignbyte_b32 v163, v221, v163, v92\n"
      "v_alignbyte_b32 v220, v220, v220, v92\n"
      "v_cvt_pk_bf16_f32 v118, v118, v119\n"
      "v_cvt_pk_bf16_f32 v119, v120, v121\n"
      "v_cvt_pk_bf16_f32 v120, v202, v203\n"
      "v_mfma_f32_16x16x32_bf16 v[154:157], v[182:185], v[146:149], v[68:71]\n"
      "v_cvt_pk_bf16_f32 v121, v204, v205\n"
      "v_sub_f32 v142, v142, v136\n"
      "v_sub_f32 v143, v143, v136\n"
      "v_sub_f32 v144, v144, v136\n"
      "v_sub_f32 v145, v145, v136\n"
      "v_sub_f32 v170, v170, v136\n"
      "v_mfma_f32_16x16x32_bf16 v[204:207], v[174:177], v[146:149], v[72:75]\n"
      "v_exp_f32 v142, v142\n"
      "v_mfma_f32_16x16x32_bf16 v[134:137], v[100:103], v[146:149], v[28:31]\n"
      "v_exp_f32 v143, v143\n"
      "v_mfma_f32_16x16x16_bf16 v[150:153], v[218:219], v[112:113], v[150:153]\n"
      "v_exp_f32 v144, v144\n"
      "v_exp_f32_e64 v145, v145\n"
      "v_mfma_f32_16x16x16_bf16 v[154:157], v[198:199], v[112:113], v[154:157]\n"
      "v_exp_f32_e64 v170, v170\n"
      "v_cvt_pk_bf16_f32 v92, v142, v143\n"
      "v_mfma_f32_16x16x16_bf16 v[204:207], v[122:123], v[112:113], v[204:207]\n"
      "v_cvt_pk_bf16_f32 v93, v144, v145\n"
      "v_cvt_pk_bf16_f32 v94, v170, 0\n"
      "v_fma_f32 v149, v178, v178, s52\n"
      "v_mfma_f32_16x16x16_bf16 v[134:137], v[208:209], v[112:113], v[134:137]\n"
      "v_fmac_f32 v149, v179, v179\n"
      "v_fmac_f32 v149, v180, v180\n"
      "v_fmac_f32 v149, v181, v181\n"
      "v_mfma_f32_16x16x16_bf16 v[150:153], v[200:201], v[172:173], v[150:153]\n"
      "v_fmac_f32 v149, v166, v166\n"
      "v_fmac_f32 v149, v167, v167\n"
      "v_fmac_f32 v149, v168, v168\n"
      "v_mfma_f32_16x16x16_bf16 v[154:157], v[200:201], v[164:165], v[154:157]\n"
      "v_fmac_f32 v149, v169, v169\n"
      "v_fmac_f32 v149, v126, v126\n"
      "v_fmac_f32 v149, v127, v127\n"
      "v_mfma_f32_16x16x16_bf16 v[204:207], v[200:201], v[162:163], v[204:207]\n"
      "v_fmac_f32 v149, v128, v128\n"
      "v_fmac_f32 v149, v129, v129\n"
      "v_rcp_f32 v186, v186\n"
      "v_mfma_f32_16x16x16_bf16 v[134:137], v[200:201], v[220:221], v[134:137]\n"
      "v_permlane16_swap_b32 v39, v149\n"
      "v_add_f32 v39, v39, v149\n"
      "v_mfma_f32_16x16x16_bf16 v[150:153], v[124:125], v[172:173], v[150:153]\n"
      "v_mul_f32 v138, v186, v138\n"
      "v_mul_f32 v139, v186, v139\n"
      "v_cvt_pk_bf16_f32 v138, v138, v139\n"
      "v_mfma_f32_16x16x16_bf16 v[154:157], v[124:125], v[164:165], v[154:157]\n"
      "v_mul_f32 v140, v186, v140\n"
      "v_mul_f32 v141, v186, v141\n"
      "v_cvt_pk_bf16_f32 v139, v140, v141\n"
      "v_mfma_f32_16x16x16_bf16 v[204:207], v[124:125], v[162:163], v[204:207]\n"
      "v_max3_f32 v202, v150, v151, v152\n"
      "v_mul_f32 v188, v186, v188\n"
      "v_mul_f32 v189, v186, v189\n"
      "v_mfma_f32_16x16x16_bf16 v[134:137], v[124:125], v[220:221], v[134:137]\n"
      "ds_read_b128 v[220:223], v2 offset:18944\n"
      "ds_read_b128 v[170:173], v2 offset:22016\n"
      "ds_read_b128 v[162:165], v2 offset:25088\n"
      "v_max3_f32 v144, v153, v154, v155\n"
      "v_max3_f32 v202, v202, v156, v157\n"
      "v_max3_f32 v144, v144, v204, v205\n"
      "v_max3_f32 v202, v202, v206, v207\n"
      "v_mul_f32 v190, v186, v190\n"
      "v_max3_f32 v202, v202, v144, v134\n"
      "v_mov_b32 v144, v202\n"
      "v_mul_f32 v191, v186, v191\n"
      "v_cvt_pk_bf16_f32 v140, v188, v189\n"
      "v_permlane16_swap_b32 v202, v144\n"
      "v_max_f32 v202, v202, v144\n"
      "v_mov_b32 v144, v202\n"
      "v_cvt_pk_bf16_f32 v141, v190, v191\n"
      "v_mul_f32 v210, v186, v210\n"
      "v_permlane32_swap_b32 v202, v144\n"
      "v_max_f32 v202, v202, v144\n"
      "s_waitcnt lgkmcnt(2)\n"
      "v_mfma_f32_16x16x32_bf16 v[144:147], a[216:219], v[220:223], 0\n"
      "ds_write_b128 v3, v[138:141]\n"
      "s_waitcnt lgkmcnt(1)\n"
      "v_mfma_f32_16x16x32_bf16 v[144:147], a[220:223], v[170:173], v[144:147]\n"
      "v_sub_f32 v150, v150, v202\n"
      "v_sub_f32 v151, v151, v202\n"
      "v_mfma_f32_16x16x32_bf16 v[144:147], a[224:227], v[162:165], v[144:147]\n"
      "ds_read_b128 v[188:191], v10\n"
      "v_sub_f32 v152, v152, v202\n"
      "v_mfma_f32_16x16x32_bf16 v[108:111], a[240:243], v[220:223], 0\n"
      "v_sub_f32 v153, v153, v202\n"
      "v_exp_f32 v150, v150\n"
      "v_mfma_f32_16x16x32_bf16 v[108:111], a[244:247], v[170:173], v[108:111]\n"
      "v_exp_f32 v151, v151\n"
      "v_mfma_f32_16x16x32_bf16 v[108:111], a[248:251], v[162:165], v[108:111]\n"
      "v_sub_f32 v154, v154, v202\n"
      "v_sub_f32 v155, v155, v202\n"
      "v_mfma_f32_16x16x32_bf16 v[138:141], v[232:235], v[220:223], 0\n"
      "v_sub_f32 v156, v156, v202\n"
      "v_sub_f32_e64 v157, v157, v202\n"
      "v_mfma_f32_16x16x32_bf16 v[138:141], v[236:239], v[170:173], v[138:141]\n"
      "ds_read_b64 v[124:125], v10 offset:16\n"
      "v_exp_f32 v152, v152\n"
      "v_mfma_f32_16x16x32_bf16 v[138:141], v[240:243], v[162:165], v[138:141]\n"
      "v_exp_f32 v153, v153\n"
      "v_mfma_f32_16x16x32_bf16 v[220:223], v[32:35], v[118:121], 0\n"
      "v_exp_f32 v154, v154\n"
      "v_mfma_f32_16x16x32_bf16 v[220:223], v[32:35], v[92:95], v[220:223]\n"
      "ds_read_b128 v[170:173], v2 offset:28160\n"
      "v_exp_f32 v155, v155\n"
      "v_exp_f32 v156, v156\n"
      "v_exp_f32 v157, v157\n"
      "v_cvt_pk_bf16_f32 v150, v150, v151\n"
      "v_cvt_pk_bf16_f32 v151, v152, v153\n"
      "v_cvt_pk_bf16_f32 v152, v154, v155\n"
      "v_mfma_f32_16x16x32_bf16 v[162:165], v[194:197], v[118:121], 0\n"
      "v_cvt_pk_bf16_f32 v153, v156, v157\n"
      "v_sub_f32 v204, v204, v202\n"
      "v_mfma_f32_16x16x32_bf16 v[162:165], v[76:79], v[92:95], v[162:165]\n"
      "v_sub_f32 v205, v205, v202\n"
      "v_sub_f32 v206, v206, v202\n"
      "v_sub_f32 v207, v207, v202\n"
      "v_sub_f32 v134, v134, v202\n"
      "v_exp_f32 v204, v204\n"
      "v_exp_f32_e64 v205, v205\n"
      "v_exp_f32_e64 v206, v206\n"
      "v_exp_f32_e64 v207, v207\n"
      "v_exp_f32 v134, v134\n"
      "v_cvt_pk_bf16_f32 v88, v204, v205\n"
      "v_mfma_f32_16x16x32_bf16 v[202:205], v[130:133], v[118:121], 0\n"
      "s_waitcnt lgkmcnt(0)\n"
      "v_cvt_pk_bf16_f32 v89, v206, v207\n"
      "v_cvt_pk_bf16_f32 v90, v134, 0\n"
      "v_mfma_f32_16x16x32_bf16 v[202:205], v[80:83], v[92:95], v[202:205]\n"
      "ds_read_u16 v8, v8\n"
      "v_rcp_f32_e64 v220, v220\n"
      "v_mfma_f32_16x16x32_bf16 v[134:137], v[214:217], v[118:121], 0\n"
      "v_mul_f32 v162, v220, v162\n"
      "v_mul_f32 v163, v220, v163\n"
      "v_mfma_f32_16x16x32_bf16 v[134:137], v[84:87], v[92:95], v[134:137]\n"
      "ds_read_b128 v[118:121], v2 offset:31232\n"
      "v_cvt_pk_bf16_f32 v162, v162, v163\n"
      "v_mfma_f32_16x16x32_bf16 v[154:157], v[188:191], v[104:107], v[30:33]\n"
      "v_mul_f32 v164, v220, v164\n"
      "v_mul_f32 v165, v220, v165\n"
      "v_mfma_f32_16x16x32_bf16 v[104:107], v[188:191], v[182:185], v[30:33]\n"
      "v_cvt_pk_bf16_f32 v163, v164, v165\n"
      "v_mul_f32 v202, v220, v202\n"
      "v_mfma_f32_16x16x32_bf16 v[182:185], v[188:191], v[174:177], v[30:33]\n"
      "v_mul_f32 v203, v220, v203\n"
      "v_mul_f32_e64 v204, v220, v204\n"
      "v_mfma_f32_16x16x32_bf16 v[174:177], v[188:191], v[100:103], v[30:33]\n"
      "ds_read_b128 v[188:191], v2 offset:34304\n"
      "s_waitcnt lgkmcnt(2)\n"
      "v_lshlrev_b32 v8, 16, v8\n"
      "v_mfma_f32_16x16x16_bf16 v[154:157], v[124:125], v[218:219], v[154:157]\n"
      "v_mul_f32 v205, v220, v205\n"
      "v_cvt_pk_bf16_f32 v164, v202, v203\n"
      "v_cvt_pk_bf16_f32 v165, v204, v205\n"
      "v_mfma_f32_16x16x16_bf16 v[104:107], v[124:125], v[198:199], v[104:107]\n"
      "v_mul_f32 v211, v186, v211\n"
      "v_mul_f32 v134, v220, v134\n"
      "v_mul_f32 v135, v220, v135\n"
      "v_mfma_f32_16x16x16_bf16 v[182:185], v[124:125], v[122:123], v[182:185]\n"
      "v_cvt_pk_bf16_f32 v210, v210, v211\n"
      "v_mul_f32 v212, v186, v212\n"
      "v_mul_f32 v213, v186, v213\n"
      "v_mfma_f32_16x16x16_bf16 v[174:177], v[124:125], v[208:209], v[174:177]\n"
      "v_cndmask_b32 v154, v154, v104, s[58:59]\n"
      "v_cvt_pk_bf16_f32 v134, v134, v135\n"
      "v_mul_f32_e64 v136, v220, v136\n"
      "v_mfma_f32_16x16x32_bf16 v[144:147], a[228:231], v[170:173], v[144:147]\n"
      "s_waitcnt lgkmcnt(1)\n"
      "v_cndmask_b32 v154, v154, v182, s[60:61]\n"
      "v_mul_f32 v137, v220, v137\n"
      "v_mfma_f32_16x16x32_bf16 v[144:147], a[232:235], v[118:121], v[144:147]\n"
      "s_waitcnt lgkmcnt(0)\n"
      "v_cndmask_b32 v154, v154, v174, s[62:63]\n"
      "v_fmac_f32 v154, s53, v8\n"
      "v_mfma_f32_16x16x32_bf16 v[144:147], a[236:239], v[188:191], v[144:147]\n"
      "ds_write_b128 v3, v[162:165] offset:256\n"
      "v_mfma_f32_16x16x32_bf16 v[108:111], a[252:255], v[170:173], v[108:111]\n"
      "v_max_f32_dpp v125, v154, v154 row_ror:8 row_mask:0xf bank_mask:0xf\n"
      "v_cvt_pk_bf16_f32 v211, v212, v213\n"
      "v_mfma_f32_16x16x32_bf16 v[108:111], v[224:227], v[118:121], v[108:111]\n"
      "v_max_f32_dpp v125, v125, v125 row_ror:4 row_mask:0xf bank_mask:0xf\n"
      "v_cvt_pk_bf16_f32 v135, v136, v137\n"
      "v_mfma_f32_16x16x32_bf16 v[108:111], v[228:231], v[188:191], v[108:111]\n"
      "ds_write_b64 v4, v[210:211]\n"
      "v_mfma_f32_16x16x32_bf16 v[138:141], v[244:247], v[170:173], v[138:141]\n"
      "v_max_f32_dpp v125, v125, v125 row_ror:2 row_mask:0xf bank_mask:0xf\n"
      "v_fma_f32 v176, v144, v144, s52\n"
      "v_mfma_f32_16x16x32_bf16 v[138:141], v[248:251], v[118:121], v[138:141]\n"
      "v_max_f32_dpp v125, v125, v125 row_ror:1 row_mask:0xf bank_mask:0xf\n"
      "v_fmac_f32_e64 v176, v145, v145\n"
      "v_mfma_f32_16x16x32_bf16 v[138:141], v[252:255], v[188:191], v[138:141]\n"
      "ds_write_b64 v4, v[134:135] offset:256\n"
      "v_mfma_f32_16x16x32_bf16 v[188:191], v[32:35], v[150:153], 0\n"
      "v_fmac_f32 v176, v146, v146\n"
      "v_max_f32_dpp v125, v125, v125 row_bcast:15 row_mask:0xa bank_mask:0xf\n"
      "v_mfma_f32_16x16x32_bf16 v[188:191], v[32:35], v[88:91], v[188:191]\n"
      "v_fmac_f32 v176, v147, v147\n"
      "v_fmac_f32 v176, v108, v108\n"
      "v_mfma_f32_16x16x32_bf16 v[202:205], v[194:197], v[150:153], 0\n"
      "v_fmac_f32 v176, v109, v109\n"
      "v_fmac_f32_e64 v176, v110, v110\n"
      "v_mfma_f32_16x16x32_bf16 v[202:205], v[76:79], v[88:91], v[202:205]\n"
      "v_max_f32_dpp v125, v125, v125 row_bcast:31 row_mask:0xc bank_mask:0xf\n"
      "v_fmac_f32 v176, v111, v111\n"
      "v_mfma_f32_16x16x32_bf16 v[104:107], v[130:133], v[150:153], 0\n"
      "v_fmac_f32 v176, v138, v138\n"
      "v_readlane_b32 s72, v125, 63\n"
      "v_mfma_f32_16x16x32_bf16 v[104:107], v[80:83], v[88:91], v[104:107]\n"
      "v_fmac_f32 v176, v139, v139\n"
      "v_fmac_f32 v176, v140, v140\n"
      "v_mfma_f32_16x16x32_bf16 v[122:125], v[214:217], v[150:153], 0\n"
      "v_fmac_f32 v176, v141, v141\n"
      "v_mov_b32 v149, v176\n"
      "v_mfma_f32_16x16x32_bf16 v[122:125], v[84:87], v[88:91], v[122:125]\n"
      "v_subrev_f32 v154, s72, v154\n"
      "v_rcp_f32 v188, v188\n"
      "v_exp_f32 v154, v154\n"
      "v_permlane16_swap_b32 v176, v149\n"
      "v_mul_f32 v202, v188, v202\n"
      "v_mul_f32 v203, v188, v203\n"
      "v_cvt_pk_bf16_f32 v154, v154, v154\n"
      "v_cvt_pk_bf16_f32 v202, v202, v203\n"
      "v_mul_f32 v204, v188, v204\n"
      "v_mul_f32 v205, v188, v205\n"
      "ds_write_b16 v13, v154\n"
      "ds_read_b128 v[210:213], v14\n"
      "ds_read_b128 v[118:121], v14 offset:64\n"
      "v_add_f32 v176, v176, v149\n"
      "v_cvt_pk_bf16_f32 v203, v204, v205\n"
      "v_mul_f32 v104, v188, v104\n"
      "v_mul_f32 v105, v188, v105\n"
      "v_mul_f32 v106, v188, v106\n"
      "v_mul_f32 v107, v188, v107\n"
      "v_cvt_pk_bf16_f32 v204, v104, v105\n"
      "v_cvt_pk_bf16_f32 v205, v106, v107\n"
      "v_mul_f32 v122, v188, v122\n"
      "v_mul_f32 v123, v188, v123\n"
      "ds_write_b128 v3, v[202:205] offset:512\n"
      "v_cvt_pk_bf16_f32 v122, v122, v123\n"
      "v_mul_f32 v124, v188, v124\n"
      "v_mul_f32 v125, v188, v125\n"
      "v_permlane32_swap_b32 v39, v176\n"
      "v_cvt_pk_bf16_f32 v123, v124, v125\n"
      "s_waitcnt lgkmcnt(2)\n"
      "v_mfma_f32_16x16x32_bf16 v[188:191], v[210:213], v[32:35], 0\n"
      "ds_write_b64 v4, v[122:123] offset:512\n"
      "s_waitcnt lgkmcnt(2)\n"
      "v_mfma_f32_16x16x32_bf16 v[188:191], v[118:121], v[32:35], v[188:191]\n"
      "v_add_f32 v39, v39, v176\n"
      "ds_write_b32 v31, v39\n"
      "s_waitcnt lgkmcnt(0)\n"
      "s_barrier\n"
      "s_cbranch_vccz L_sel1_%=\n"
      "s_branch L_disp1_%=\n"
      "L_tail1_%=:\n"
      "s_nop 4\n"
      "v_mfma_f32_16x16x32_bf16 v[202:205], v[210:213], v[194:197], 0\n"
      "ds_read_b128 v[194:197], v9\n"
      "v_subrev_u32 v135, 115456, v9\n"
      "v_mfma_f32_16x16x32_bf16 v[202:205], v[118:121], v[76:79], v[202:205]\n"
      "s_mul_i32 s92, s40, 2048\n"
      "v_and_b32 v106, 0x300, v135\n"
      "v_add_u32_e64 v135, v135, v106\n"
      "v_mfma_f32_16x16x32_bf16 v[104:107], v[210:213], v[130:133], 0\n"
      "v_add_u32 v135, 149504, v135\n"
      "ds_read_b128 v[130:133], v135\n"
      "v_mfma_f32_16x16x32_bf16 v[104:107], v[118:121], v[80:83], v[104:107]\n"
      "ds_read_b128 v[198:201], v135 offset:2048\n"
      "v_subrev_u32 v171, 115456, v9\n"
      "v_mfma_f32_16x16x32_bf16 v[174:177], v[210:213], v[214:217], 0\n"
      "ds_read_b128 v[212:215], v135 offset:4096\n"
      "v_add_u32 v82, s92, v171\n"
      "v_mfma_f32_16x16x32_bf16 v[174:177], v[118:121], v[84:87], v[174:177]\n"
      "ds_read_b128 v[118:121], v135 offset:6144\n"
      "v_mov_b32_dpp v188, v188 quad_perm:[0,0,0,0] row_mask:0xf bank_mask:0xf\n"
      "v_rcp_f32 v188, v188\n"
      "v_cndmask_b32 v202, v202, v104, s[58:59]\n"
      "s_waitcnt lgkmcnt(4)\n"
      "v_add_f32 v194, v194, v195\n"
      "v_add_f32 v196, v196, v197\n"
      "v_add_f32 v194, v194, v196\n"
      "v_cndmask_b32 v202, v202, v174, s[60:61]\n"
      "v_mul_f32 v202, v188, v202\n"
      "v_cvt_pk_bf16_f32 v202, v202, v202\n"
      "s_waitcnt lgkmcnt(3)\n"
      "v_mfma_f32_16x16x32_bf16 v[188:191], a[216:219], v[130:133], 0\n"
      "ds_write_b16 v12, v202\n"
      "s_waitcnt lgkmcnt(3)\n"
      "v_mfma_f32_16x16x32_bf16 v[188:191], a[220:223], v[198:201], v[188:191]\n"
      "s_waitcnt lgkmcnt(0)\n"
      "s_barrier\n"
      "v_rsq_f32 v194, v194\n"
      "v_mfma_f32_16x16x32_bf16 v[202:205], a[240:243], v[130:133], 0\n"
      "ds_read_b128 v[104:107], v135 offset:8192\n"
      "v_mul_f32 v194, 0x415db3d7, v194\n"
      "v_mfma_f32_16x16x32_bf16 v[202:205], a[244:247], v[198:201], v[202:205]\n"
      "s_waitcnt lgkmcnt(2)\n"
      "v_mov_b32 v196, v194\n"
      "v_lshrrev_b32 v171, 4, v82\n"
      "v_mfma_f32_16x16x32_bf16 v[122:125], v[232:235], v[130:133], 0\n"
      "ds_read_b128 v[130:133], v135 offset:10240\n"
      "v_permlane32_swap_b32 v194, v196\n"
      "v_mfma_f32_16x16x32_bf16 v[122:125], v[236:239], v[198:201], v[122:125]\n"
      "ds_read_b128 v[208:211], v2\n"
      "v_mov_b32 v195, v194\n"
      "v_mfma_f32_16x16x32_bf16 v[188:191], a[224:227], v[212:215], v[188:191]\n"
      "ds_read_b128 v[216:219], v2 offset:3072\n"
      "v_permlane16_swap_b32 v194, v195\n"
      "v_mfma_f32_16x16x32_bf16 v[188:191], a[228:231], v[118:121], v[188:191]\n"
      "v_mul_f32 v158, v194, v158\n"
      "v_mul_f32_e64 v159, v194, v159\n"
      "v_mfma_f32_16x16x32_bf16 v[202:205], a[248:251], v[212:215], v[202:205]\n"
      "s_waitcnt lgkmcnt(3)\n"
      "v_cvt_pk_bf16_f32 v158, v158, v159\n"
      "v_mul_f32 v160, v194, v160\n"
      "v_mfma_f32_16x16x32_bf16 v[202:205], a[252:255], v[118:121], v[202:205]\n"
      "v_mul_f32 v161, v194, v161\n"
      "v_mul_f32_e64 v114, v194, v114\n"
      "v_mfma_f32_16x16x32_bf16 v[122:125], v[240:243], v[212:215], v[122:125]\n"
      "s_waitcnt lgkmcnt(2)\n"
      "v_mul_f32 v115, v194, v115\n"
      "v_cvt_pk_bf16_f32 v159, v160, v161\n"
      "v_mfma_f32_16x16x32_bf16 v[122:125], v[244:247], v[118:121], v[122:125]\n"
      "ds_read_b128 v[118:121], v2 offset:6144\n"
      "v_mul_f32 v96, v194, v96\n"
      "v_mfma_f32_16x16x32_bf16 v[188:191], a[232:235], v[104:107], v[188:191]\n"
      "ds_read_b128 v[212:215], v2 offset:9216\n"
      "v_mul_f32 v97, v194, v97\n"
      "v_mfma_f32_16x16x32_bf16 v[188:191], a[236:239], v[130:133], v[188:191]\n"
      "s_waitcnt lgkmcnt(2)\n"
      "v_mul_f32 v98, v194, v98\n"
      "v_mul_f32_e64 v99, v194, v99\n"
      "v_mfma_f32_16x16x32_bf16 v[202:205], v[224:227], v[104:107], v[202:205]\n"
      "v_cvt_pk_bf16_f32 v114, v114, v115\n"
      "v_mul_f32 v116, v194, v116\n"
      "v_mfma_f32_16x16x32_bf16 v[202:205], v[228:231], v[130:133], v[202:205]\n"
      "v_mul_f32 v117, v194, v117\n"
      "v_cvt_pk_bf16_f32 v160, v96, v97\n"
      "v_mfma_f32_16x16x32_bf16 v[122:125], v[248:251], v[104:107], v[122:125]\n"
      "v_cvt_pk_bf16_f32 v161, v98, v99\n"
      "v_cvt_pk_bf16_f32 v115, v116, v117\n"
      "v_mfma_f32_16x16x32_bf16 v[122:125], v[252:255], v[130:133], v[122:125]\n"
      "ds_write_b128 v5, v[158:161] offset:18432\n"
      "ds_write_b64 v6, v[114:115] offset:18432\n"
      "v_mul_f32 v178, v195, v178\n"
      "v_mul_f32 v179, v195, v179\n"
      "v_cvt_pk_bf16_f32 v178, v178, v179\n"
      "v_mul_f32 v180, v195, v180\n"
      "v_mul_f32 v181, v195, v181\n"
      "v_mul_f32 v126, v195, v126\n"
      "v_mul_f32 v127, v195, v127\n"
      "v_cvt_pk_bf16_f32 v179, v180, v181\n"
      "v_mul_f32 v166, v195, v166\n"
      "v_mul_f32 v167, v195, v167\n"
      "v_mul_f32 v168, v195, v168\n"
      "v_mul_f32 v169, v195, v169\n"
      "v_cvt_pk_bf16_f32 v126, v126, v127\n"
      "v_mul_f32 v128, v195, v128\n"
      "v_mul_f32 v129, v195, v129\n"
      "v_cvt_pk_bf16_f32 v180, v166, v167\n"
      "v_cvt_pk_bf16_f32 v181, v168, v169\n"
      "v_cvt_pk_bf16_f32 v127, v128, v129\n"
      "ds_write_b128 v5, v[178:181] offset:24576\n"
      "ds_write_b64 v6, v[126:127] offset:24576\n"
      "ds_read_b128 v[178:181], v2 offset:12288\n"
      "v_mul_f32 v144, v196, v144\n"
      "v_mul_f32 v145, v196, v145\n"
      "v_cvt_pk_bf16_f32 v144, v144, v145\n"
      "v_mul_f32 v146, v196, v146\n"
      "v_mul_f32 v147, v196, v147\n"
      "v_mul_f32 v138, v196, v138\n"
      "v_mul_f32 v139, v196, v139\n"
      "v_cvt_pk_bf16_f32 v145, v146, v147\n"
      "v_mul_f32 v108, v196, v108\n"
      "v_mul_f32 v109, v196, v109\n"
      "v_mul_f32 v110, v196, v110\n"
      "v_mul_f32 v111, v196, v111\n"
      "v_cvt_pk_bf16_f32 v138, v138, v139\n"
      "v_mul_f32 v140, v196, v140\n"
      "v_mul_f32 v141, v196, v141\n"
      "v_cvt_pk_bf16_f32 v146, v108, v109\n"
      "v_cvt_pk_bf16_f32 v147, v110, v111\n"
      "v_cvt_pk_bf16_f32 v139, v140, v141\n"
      "ds_write_b128 v5, v[144:147] offset:30720\n"
      "ds_write_b64 v6, v[138:139] offset:30720\n"
      "v_mfma_f32_16x16x32_bf16 v[158:161], a[216:219], v[208:211], 0\n"
      "ds_read_b128 v[166:169], v2 offset:15360\n"
      "v_fma_f32 v84, v188, v188, s52\n"
      "v_mfma_f32_16x16x32_bf16 v[158:161], a[220:223], v[216:219], v[158:161]\n"
      "s_waitcnt lgkmcnt(3)\n"
      "v_fmac_f32 v84, v189, v189\n"
      "v_fmac_f32 v84, v190, v190\n"
      "v_mfma_f32_16x16x32_bf16 v[96:99], a[240:243], v[208:211], 0\n"
      "v_fmac_f32 v84, v191, v191\n"
      "v_fmac_f32 v84, v202, v202\n"
      "v_mfma_f32_16x16x32_bf16 v[96:99], a[244:247], v[216:219], v[96:99]\n"
      "v_fmac_f32 v84, v203, v203\n"
      "v_fmac_f32 v84, v204, v204\n"
      "v_mfma_f32_16x16x32_bf16 v[114:117], v[232:235], v[208:211], 0\n"
      "v_fmac_f32 v84, v205, v205\n"
      "v_fmac_f32_e64 v84, v122, v122\n"
      "v_mfma_f32_16x16x32_bf16 v[114:117], v[236:239], v[216:219], v[114:117]\n"
      "ds_read_b128 v[144:147], v2 offset:256\n"
      "v_fmac_f32 v84, v123, v123\n"
      "v_mfma_f32_16x16x32_bf16 v[158:161], a[224:227], v[118:121], v[158:161]\n"
      "ds_read_b128 v[108:111], v2 offset:3328\n"
      "v_fmac_f32 v84, v124, v124\n"
      "v_mfma_f32_16x16x32_bf16 v[158:161], a[228:231], v[212:215], v[158:161]\n"
      "s_waitcnt lgkmcnt(2)\n"
      "v_fmac_f32 v84, v125, v125\n"
      "v_add_u32_e64 v210, 0, v171\n"
      "v_mfma_f32_16x16x32_bf16 v[96:99], a[248:251], v[118:121], v[96:99]\n"
      "v_add_u32 v211, 64, v171\n"
      "v_and_b32 v86, 0x3fffffff, v37\n"
      "v_mfma_f32_16x16x32_bf16 v[96:99], a[252:255], v[212:215], v[96:99]\n"
      "v_ashrrev_i32 v209, 31, v37\n"
      "v_mul_u32_u24 v80, 2731, v210\n"
      "v_mfma_f32_16x16x32_bf16 v[114:117], v[240:243], v[118:121], v[114:117]\n"
      "v_mul_u32_u24 v81, 2731, v211\n"
      "v_add_u32 v86, 1179648, v86\n"
      "v_mfma_f32_16x16x32_bf16 v[114:117], v[244:247], v[212:215], v[114:117]\n"
      "ds_read_b128 v[138:141], v2 offset:6400\n"
      "v_and_b32 v209, 100270464, v209\n"
      "v_mfma_f32_16x16x32_bf16 v[158:161], a[232:235], v[178:181], v[158:161]\n"
      "ds_read_b128 v[212:215], v2 offset:9472\n"
      "v_lshrrev_b32 v80, 16, v80\n"
      "v_mfma_f32_16x16x32_bf16 v[158:161], a[236:239], v[166:169], v[158:161]\n"
      "s_waitcnt lgkmcnt(2)\n"
      "v_lshrrev_b32 v81, 16, v81\n"
      "v_sub_u32 v86, v86, v209\n"
      "v_mfma_f32_16x16x32_bf16 v[96:99], v[224:227], v[178:181], v[96:99]\n"
      "v_mul_u32_u24 v85, 24, v80\n"
      "v_mul_u32_u24 v76, 24, v81\n"
      "v_mfma_f32_16x16x32_bf16 v[96:99], v[228:231], v[166:169], v[96:99]\n"
      "v_bfe_i32 v209, v37, 30, 1\n"
      "v_fma_f32 v77, v158, v158, s52\n"
      "v_mfma_f32_16x16x32_bf16 v[114:117], v[248:251], v[178:181], v[114:117]\n"
      "v_fmac_f32 v77, v159, v159\n"
      "v_fmac_f32 v77, v160, v160\n"
      "v_mfma_f32_16x16x32_bf16 v[114:117], v[252:255], v[166:169], v[114:117]\n"
      "ds_read_b128 v[118:121], v2 offset:12544\n"
      "v_fmac_f32 v77, v161, v161\n"
      "v_mfma_f32_16x16x32_bf16 v[178:181], a[216:219], v[144:147], 0\n"
      "ds_read_b128 v[130:133], v2 offset:15616\n"
      "v_fmac_f32 v77, v96, v96\n"
      "v_mfma_f32_16x16x32_bf16 v[178:181], a[220:223], v[108:111], v[178:181]\n"
      "s_waitcnt lgkmcnt(2)\n"
      "v_fmac_f32 v77, v97, v97\n"
      "v_fmac_f32 v77, v98, v98\n"
      "v_mfma_f32_16x16x32_bf16 v[166:169], a[240:243], v[144:147], 0\n"
      "v_fmac_f32 v77, v99, v99\n"
      "v_fmac_f32_e64 v77, v114, v114\n"
      "v_mfma_f32_16x16x32_bf16 v[166:169], a[244:247], v[108:111], v[166:169]\n"
      "v_fmac_f32 v77, v115, v115\n"
      "v_fmac_f32 v77, v116, v116\n"
      "v_mfma_f32_16x16x32_bf16 v[126:129], v[232:235], v[144:147], 0\n"
      "v_fmac_f32 v77, v117, v117\n"
      "v_sub_u32 v85, v210, v85\n"
      "v_mfma_f32_16x16x32_bf16 v[126:129], v[236:239], v[108:111], v[126:129]\n"
      "ds_read_b128 v[194:197], v2 offset:512\n"
      "v_sub_u32_e64 v76, v211, v76\n"
      "v_mfma_f32_16x16x32_bf16 v[178:181], a[224:227], v[138:141], v[178:181]\n"
      "ds_read_b128 v[104:107], v2 offset:3584\n"
      "v_and_b32 v209, 196224, v209\n"
      "v_mfma_f32_16x16x32_bf16 v[178:181], a[228:231], v[212:215], v[178:181]\n"
      "s_waitcnt lgkmcnt(2)\n"
      "v_and_b32 v210, 7, v80\n"
      "v_and_b32 v211, 7, v81\n"
      "v_mfma_f32_16x16x32_bf16 v[166:169], a[248:251], v[138:141], v[166:169]\n"
      "v_sub_u32 v86, v86, v209\n"
      "v_lshl_add_u32 v80, v80, 2, 12\n"
      "v_mfma_f32_16x16x32_bf16 v[166:169], a[252:255], v[212:215], v[166:169]\n"
      "v_lshl_add_u32 v81, v81, 2, 12\n"
      "v_xor_b32 v85, v85, v210\n"
      "v_mfma_f32_16x16x32_bf16 v[126:129], v[240:243], v[138:141], v[126:129]\n"
      "v_xor_b32 v76, v76, v211\n"
      "v_subrev_u32 v78, 73728, v5\n"
      "v_mfma_f32_16x16x32_bf16 v[126:129], v[244:247], v[212:215], v[126:129]\n"
      "ds_read_b128 v[212:215], v2 offset:6656\n"
      "v_subrev_u32 v154, 73728, v6\n"
      "v_mfma_f32_16x16x32_bf16 v[178:181], a[232:235], v[118:121], v[178:181]\n"
      "ds_read_b128 v[208:211], v2 offset:9728\n"
      "v_mfma_f32_16x16x32_bf16 v[178:181], a[236:239], v[130:133], v[178:181]\n"
      "s_waitcnt lgkmcnt(2)\n"
      "v_mfma_f32_16x16x32_bf16 v[166:169], v[224:227], v[118:121], v[166:169]\n"
      "v_mfma_f32_16x16x32_bf16 v[166:169], v[228:231], v[130:133], v[166:169]\n"
      "v_mfma_f32_16x16x32_bf16 v[126:129], v[248:251], v[118:121], v[126:129]\n"
      "v_mfma_f32_16x16x32_bf16 v[126:129], v[252:255], v[130:133], v[126:129]\n"
      "ds_read_b128 v[130:133], v2 offset:12800\n"
      "v_mfma_f32_16x16x32_bf16 v[144:147], a[216:219], v[194:197], 0\n"
      "ds_read_b128 v[118:121], v2 offset:15872\n"
      "v_fma_f32 v156, v178, v178, s52\n"
      "v_mfma_f32_16x16x32_bf16 v[144:147], a[220:223], v[104:107], v[144:147]\n"
      "s_waitcnt lgkmcnt(2)\n"
      "v_fmac_f32 v156, v179, v179\n"
      "v_fmac_f32 v156, v180, v180\n"
      "v_mfma_f32_16x16x32_bf16 v[108:111], a[240:243], v[194:197], 0\n"
      "v_fmac_f32 v156, v181, v181\n"
      "v_fmac_f32 v156, v166, v166\n"
      "v_mfma_f32_16x16x32_bf16 v[108:111], a[244:247], v[104:107], v[108:111]\n"
      "v_fmac_f32 v156, v167, v167\n"
      "v_fmac_f32 v156, v168, v168\n"
      "v_mfma_f32_16x16x32_bf16 v[138:141], v[232:235], v[194:197], 0\n"
      "v_fmac_f32 v156, v169, v169\n"
      "v_fmac_f32 v156, v126, v126\n"
      "v_mfma_f32_16x16x32_bf16 v[138:141], v[236:239], v[104:107], v[138:141]\n"
      "ds_read_b128 v[194:197], v135 offset:256\n"
      "v_fmac_f32 v156, v127, v127\n"
      "v_mfma_f32_16x16x32_bf16 v[144:147], a[224:227], v[212:215], v[144:147]\n"
      "ds_read_b128 v[104:107], v135 offset:2304\n"
      "v_fmac_f32 v156, v128, v128\n"
      "v_mfma_f32_16x16x32_bf16 v[144:147], a[228:231], v[208:211], v[144:147]\n"
      "s_waitcnt lgkmcnt(2)\n"
      "v_fmac_f32 v156, v129, v129\n"
      "v_mfma_f32_16x16x32_bf16 v[108:111], a[248:251], v[212:215], v[108:111]\n"
      "v_mfma_f32_16x16x32_bf16 v[108:111], a[252:255], v[208:211], v[108:111]\n"
      "v_permlane16_swap_b32_e64 v77, v156\n"
      "v_mfma_f32_16x16x32_bf16 v[138:141], v[240:243], v[212:215], v[138:141]\n"
      "v_add_f32_e64 v77, v77, v156\n"
      "v_mfma_f32_16x16x32_bf16 v[138:141], v[244:247], v[208:211], v[138:141]\n"
      "ds_read_b128 v[210:213], v135 offset:4352\n"
      "v_mfma_f32_16x16x32_bf16 v[144:147], a[232:235], v[130:133], v[144:147]\n"
      "ds_read_b128 v[214:217], v135 offset:6400\n"
      "v_mfma_f32_16x16x32_bf16 v[144:147], a[236:239], v[118:121], v[144:147]\n"
      "ds_read_b128 v[174:177], v7\n"
      "v_mfma_f32_16x16x32_bf16 v[108:111], v[224:227], v[130:133], v[108:111]\n"
      "ds_read_b128 v[170:173], v7 offset:4096\n"
      "v_mfma_f32_16x16x32_bf16 v[108:111], v[228:231], v[118:121], v[108:111]\n"
      "s_waitcnt lgkmcnt(4)\n"
      "v_mfma_f32_16x16x32_bf16 v[138:141], v[248:251], v[130:133], v[138:141]\n"
      "ds_read_b128 v[130:133], v7 offset:8192\n"
      "v_mfma_f32_16x16x32_bf16 v[138:141], v[252:255], v[118:121], v[138:141]\n"
      "ds_read_b128 v[118:121], v135 offset:8448\n"
      "v_fma_f32 v222, v144, v144, s52\n"
      "v_mfma_f32_16x16x32_bf16 v[198:201], a[216:219], v[194:197], 0\n"
      "ds_read_b128 v[206:209], v7 offset:12288\n"
      "v_fmac_f32 v222, v145, v145\n"
      "v_mfma_f32_16x16x32_bf16 v[198:201], a[220:223], v[104:107], v[198:201]\n"
      "ds_read_b128 v[162:165], v135 offset:10496\n"
      "v_fmac_f32_e64 v222, v146, v146\n"
      "v_mfma_f32_16x16x32_bf16 v[134:137], a[240:243], v[194:197], 0\n"
      "ds_read_b128 v[218:221], v7 offset:16384\n"
      "v_fmac_f32 v222, v147, v147\n"
      "v_mfma_f32_16x16x32_bf16 v[134:137], a[244:247], v[104:107], v[134:137]\n"
      "s_waitcnt lgkmcnt(5)\n"
      "v_fmac_f32_e64 v222, v108, v108\n"
      "v_fmac_f32 v222, v109, v109\n"
      "v_mfma_f32_16x16x32_bf16 v[182:185], v[232:235], v[194:197], 0\n"
      "buffer_store_dwordx4 v[174:177], v20, s[24:27], s46 offen sc1\n"
      "v_mfma_f32_16x16x32_bf16 v[182:185], v[236:239], v[104:107], v[182:185]\n"
      "s_nop 0\n"
      "ds_bpermute_b32 v177, v80, v86\n"
      "v_fmac_f32 v222, v110, v110\n"
      "v_mfma_f32_16x16x32_bf16 v[198:201], a[224:227], v[210:213], v[198:201]\n"
      "ds_bpermute_b32 v80, v81, v86\n"
      "v_fmac_f32 v222, v111, v111\n"
      "v_mfma_f32_16x16x32_bf16 v[198:201], a[228:231], v[214:217], v[198:201]\n"
      "s_waitcnt lgkmcnt(2)\n"
      "v_fmac_f32 v222, v138, v138\n"
      "v_fmac_f32 v222, v139, v139\n"
      "v_mfma_f32_16x16x32_bf16 v[134:137], a[248:251], v[210:213], v[134:137]\n"
      "v_fmac_f32 v222, v140, v140\n"
      "v_fmac_f32 v222, v141, v141\n"
      "v_mfma_f32_16x16x32_bf16 v[134:137], a[252:255], v[214:217], v[134:137]\n"
      "v_mov_b32 v156, v222\n"
      "v_mfma_f32_16x16x32_bf16 v[182:185], v[240:243], v[210:213], v[182:185]\n"
      "v_mfma_f32_16x16x32_bf16 v[182:185], v[244:247], v[214:217], v[182:185]\n"
      "buffer_store_dwordx4 v[170:173], v21, s[24:27], s46 offen sc1\n"
      "v_mfma_f32_16x16x32_bf16 v[198:201], a[232:235], v[118:121], v[198:201]\n"
      "s_waitcnt lgkmcnt(0)\n"
      "v_permlane16_swap_b32 v222, v156\n"
      "v_mfma_f32_16x16x32_bf16 v[198:201], a[236:239], v[162:165], v[198:201]\n"
      "v_add_f32 v222, v222, v156\n"
      "v_lshl_add_u32 v177, v85, 4, v177\n"
      "v_mfma_f32_16x16x32_bf16 v[134:137], v[224:227], v[118:121], v[134:137]\n"
      "v_permlane32_swap_b32 v77, v222\n"
      "v_mfma_f32_16x16x32_bf16 v[134:137], v[228:231], v[162:165], v[134:137]\n"
      "v_add_f32 v77, v77, v222\n"
      "ds_write_b32 v31, v77 offset:1024\n"
      "v_mfma_f32_16x16x32_bf16 v[182:185], v[248:251], v[118:121], v[182:185]\n"
      "v_fma_f32 v118, v198, v198, s52\n"
      "v_fmac_f32 v118, v199, v199\n"
      "v_mfma_f32_16x16x32_bf16 v[182:185], v[252:255], v[162:165], v[182:185]\n"
      "buffer_store_dwordx4 v[130:133], v22, s[24:27], s46 offen sc1\n"
      "v_fmac_f32 v118, v200, v200\n"
      "v_fmac_f32 v118, v201, v201\n"
      "v_fmac_f32 v118, v134, v134\n"
      "v_fmac_f32 v118, v135, v135\n"
      "v_fmac_f32 v118, v136, v136\n"
      "v_fmac_f32 v118, v137, v137\n"
      "v_lshl_add_u32 v80, v76, 4, v80\n"
      "v_fmac_f32 v118, v182, v182\n"
      "v_fmac_f32 v118, v183, v183\n"
      "v_fmac_f32 v118, v184, v184\n"
      "v_fmac_f32 v118, v185, v185\n"
      "s_nop 0\n"
      "s_nop 0\n"
      "v_permlane16_swap_b32 v84, v118\n"
      "v_add_f32 v84, v84, v118\n"
      "v_mov_b32 v118, v84\n"
      "s_nop 0\n"
      "s_nop 0\n"
      "v_permlane32_swap_b32 v84, v118\n"
      "v_add_f32_e64 v84, v84, v118\n"
      "ds_write_b32 v31, v84 offset:4608\n"
      "buffer_store_dwordx4 v[206:209], v23, s[24:27], s46 offen sc1\n"
      "buffer_store_dwordx4 v[218:221], v25, s[24:27], s46 offen sc1\n"
      "s_nop 0\n"
      "s_nop 0\n"
      "v_readlane_b32 s46, v37, s42\n"
      "s_add_u32 s42, s42, 1\n"
      "s_or_b32 s95, s46, s48\n"
      "s_cmp_ge_u32 s95, 0x40000000\n"
      "s_cbranch_scc1 L_rare_27_%=\n"
      "L_back_28_%=:\n"
      "s_waitcnt lgkmcnt(0)\n"
      "s_barrier\n"
      "ds_read_b128 v[130:133], v7 offset:18432\n"
      "ds_read_b128 v[118:121], v7 offset:22528\n"
      "ds_read_b128 v[212:215], v7 offset:26624\n"
      "ds_read_b128 v[194:197], v7 offset:30720\n"
      "ds_read_b128 v[104:107], v9 offset:1024\n"
      "ds_read_b128 v[216:219], v7 offset:34816\n"
      "ds_read_b128 v[208:211], v9 offset:4608\n"
      "s_waitcnt lgkmcnt(6)\n"
      "buffer_store_dwordx4 v[130:133], v20, s[24:27], s46 offen sc1\n"
      "s_waitcnt lgkmcnt(4)\n"
      "buffer_store_dwordx4 v[118:121], v21, s[24:27], s46 offen sc1\n"
      "buffer_store_dwordx4 v[212:215], v22, s[24:27], s46 offen sc1\n"
      "s_waitcnt lgkmcnt(1)\n"
      "buffer_store_dwordx4 v[194:197], v23, s[24:27], s46 offen sc1\n"
      "buffer_store_dwordx4 v[216:219], v25, s[24:27], s46 offen sc1\n"
      "v_add_f32 v104, v104, v105\n"
      "v_add_f32 v106, v106, v107\n"
      "v_add_f32 v104, v104, v106\n"
      "v_rsq_f32 v104, v104\n"
      "s_waitcnt lgkmcnt(0)\n"
      "v_add_f32 v208, v208, v209\n"
      "v_mul_f32 v104, 0x415db3d7, v104\n"
      "v_mov_b32 v106, v104\n"
      "v_add_f32 v210, v210, v211\n"
      "v_add_f32 v208, v208, v210\n"
      "v_permlane32_swap_b32 v104, v106\n"
      "v_mov_b32 v105, v104\n"
      "v_mul_f32 v144, v106, v144\n"
      "v_mul_f32 v145, v106, v145\n"
      "v_permlane16_swap_b32 v104, v105\n"
      "v_mul_f32 v158, v104, v158\n"
      "v_mul_f32_e64 v159, v104, v159\n"
      "v_cvt_pk_bf16_f32 v158, v158, v159\n"
      "v_mul_f32 v160, v104, v160\n"
      "v_mul_f32 v161, v104, v161\n"
      "v_mul_f32 v114, v104, v114\n"
      "v_mul_f32 v115, v104, v115\n"
      "v_cvt_pk_bf16_f32 v159, v160, v161\n"
      "v_mul_f32 v96, v104, v96\n"
      "v_mul_f32 v97, v104, v97\n"
      "v_mul_f32 v98, v104, v98\n"
      "v_mul_f32 v99, v104, v99\n"
      "v_cvt_pk_bf16_f32 v114, v114, v115\n"
      "v_mul_f32 v116, v104, v116\n"
      "v_mul_f32 v117, v104, v117\n"
      "v_cvt_pk_bf16_f32 v160, v96, v97\n"
      "v_cvt_pk_bf16_f32 v161, v98, v99\n"
      "v_cvt_pk_bf16_f32 v115, v116, v117\n"
      "ds_write_b128 v5, v[158:161]\n"
      "ds_write_b64 v6, v[114:115]\n"
      "v_mul_f32 v178, v105, v178\n"
      "v_mul_f32 v179, v105, v179\n"
      "v_cvt_pk_bf16_f32 v178, v178, v179\n"
      "v_mul_f32 v180, v105, v180\n"
      "v_mul_f32 v181, v105, v181\n"
      "v_mul_f32 v126, v105, v126\n"
      "v_mul_f32 v127, v105, v127\n"
      "v_cvt_pk_bf16_f32 v179, v180, v181\n"
      "v_mul_f32 v166, v105, v166\n"
      "v_mul_f32 v167, v105, v167\n"
      "v_mul_f32 v168, v105, v168\n"
      "v_mul_f32 v169, v105, v169\n"
      "v_cvt_pk_bf16_f32 v126, v126, v127\n"
      "v_mul_f32 v128, v105, v128\n"
      "v_mul_f32 v129, v105, v129\n"
      "v_cvt_pk_bf16_f32 v180, v166, v167\n"
      "v_cvt_pk_bf16_f32 v181, v168, v169\n"
      "v_cvt_pk_bf16_f32 v127, v128, v129\n"
      "ds_write_b128 v5, v[178:181] offset:6144\n"
      "ds_write_b64 v6, v[126:127] offset:6144\n"
      "v_cvt_pk_bf16_f32 v144, v144, v145\n"
      "v_mul_f32 v146, v106, v146\n"
      "v_mul_f32 v147, v106, v147\n"
      "v_mul_f32 v138, v106, v138\n"
      "v_mul_f32 v139, v106, v139\n"
      "v_cvt_pk_bf16_f32 v145, v146, v147\n"
      "v_mul_f32 v108, v106, v108\n"
      "v_mul_f32 v109, v106, v109\n"
      "v_mul_f32 v110, v106, v110\n"
      "v_mul_f32 v111, v106, v111\n"
      "v_cvt_pk_bf16_f32 v138, v138, v139\n"
      "v_mul_f32 v140, v106, v140\n"
      "v_mul_f32 v141, v106, v141\n"
      "v_cvt_pk_bf16_f32 v146, v108, v109\n"
      "v_cvt_pk_bf16_f32 v147, v110, v111\n"
      "v_cvt_pk_bf16_f32 v139, v140, v141\n"
      "ds_write_b128 v5, v[144:147] offset:12288\n"
      "ds_write_b64 v6, v[138:139] offset:12288\n"
      "v_rsq_f32 v208, v208\n"
      "v_readlane_b32 s46, v37, s42\n"
      "v_mul_f32 v208, 0x415db3d7, v208\n"
      "v_mov_b32 v210, v208\n"
      "s_or_b32 s95, s46, s48\n"
      "s_cmp_ge_u32 s95, 0x40000000\n"
      "s_cbranch_scc1 L_rare_31_%=\n"
      "L_back_32_%=:\n"
      "s_add_u32 s42, s42, 1\n"
      "v_permlane32_swap_b32 v208, v210\n"
      "v_mov_b32 v209, v208\n"
      "s_nop 0\n"
      "s_nop 0\n"
      "v_permlane16_swap_b32 v208, v209\n"
      "v_mul_f32 v188, v208, v188\n"
      "v_mul_f32 v189, v208, v189\n"
      "v_mul_f32 v198, v209, v198\n"
      "v_mul_f32 v199, v209, v199\n"
      "v_cvt_pk_bf16_f32 v188, v188, v189\n"
      "v_mul_f32 v190, v208, v190\n"
      "v_mul_f32_e64 v191, v208, v191\n"
      "v_cvt_pk_bf16_f32 v198, v198, v199\n"
      "v_mul_f32 v200, v209, v200\n"
      "v_mul_f32 v201, v209, v201\n"
      "v_cvt_pk_bf16_f32 v189, v190, v191\n"
      "v_mul_f32 v202, v208, v202\n"
      "v_mul_f32 v203, v208, v203\n"
      "v_mul_f32 v204, v208, v204\n"
      "v_mul_f32 v205, v208, v205\n"
      "v_cvt_pk_bf16_f32 v199, v200, v201\n"
      "v_mul_f32 v134, v209, v134\n"
      "v_mul_f32 v135, v209, v135\n"
      "v_mul_f32 v136, v209, v136\n"
      "v_mul_f32 v137, v209, v137\n"
      "v_cvt_pk_bf16_f32 v190, v202, v203\n"
      "v_cvt_pk_bf16_f32 v191, v204, v205\n"
      "v_cvt_pk_bf16_f32 v200, v134, v135\n"
      "v_cvt_pk_bf16_f32 v201, v136, v137\n"
      "v_mul_f32 v122, v208, v122\n"
      "v_mul_f32 v123, v208, v123\n"
      "v_mul_f32 v182, v209, v182\n"
      "v_mul_f32 v183, v209, v183\n"
      "ds_write_b128 v78, v[188:191]\n"
      "ds_write_b128 v78, v[198:201] offset:6144\n"
      "v_cvt_pk_bf16_f32 v122, v122, v123\n"
      "v_mul_f32 v124, v208, v124\n"
      "v_mul_f32 v125, v208, v125\n"
      "v_cvt_pk_bf16_f32 v182, v182, v183\n"
      "v_mul_f32_e64 v184, v209, v184\n"
      "v_mul_f32_e64 v185, v209, v185\n"
      "v_cvt_pk_bf16_f32 v123, v124, v125\n"
      "v_cvt_pk_bf16_f32 v183, v184, v185\n"
      "ds_write_b64 v154, v[122:123]\n"
      "ds_write_b64 v154, v[182:183] offset:6144\n"
      "s_waitcnt lgkmcnt(0)\n"
      "s_barrier\n"
      "ds_read_b128 v[188:191], v7\n"
      "ds_read_b128 v[202:205], v7 offset:4096\n"
      "ds_read_b128 v[138:141], v7 offset:8192\n"
      "ds_read_b128 v[144:147], v7 offset:12288\n"
      "ds_read_b128 v[108:111], v7 offset:16384\n"
      "ds_read_b128 v[178:181], v82\n"
      "ds_read_b128 v[126:129], v82 offset:1024\n"
      "s_waitcnt lgkmcnt(6)\n"
      "buffer_store_dwordx4 v[188:191], v20, s[24:27], s46 offen sc1\n"
      "s_waitcnt lgkmcnt(5)\n"
      "buffer_store_dwordx4 v[202:205], v21, s[24:27], s46 offen sc1\n"
      "s_waitcnt lgkmcnt(4)\n"
      "buffer_store_dwordx4 v[138:141], v22, s[24:27], s46 offen sc1\n"
      "s_waitcnt lgkmcnt(3)\n"
      "buffer_store_dwordx4 v[144:147], v23, s[24:27], s46 offen sc1\n"
      "s_waitcnt lgkmcnt(2)\n"
      "buffer_store_dwordx4 v[108:111], v25, s[24:27], s46 offen sc1\n"
      "s_waitcnt lgkmcnt(1)\n"
      "buffer_store_dwordx4 v[178:181], v177, s[24:27], 0 offen sc1\n"
      "s_waitcnt lgkmcnt(0)\n"
      "buffer_store_dwordx4 v[126:129], v80, s[24:27], 0 offen sc1\n"
      "s_endpgm\n"
      "L_tail0_%=:\n"
      "s_nop 4\n"
      "v_mfma_f32_16x16x32_bf16 v[202:205], v[210:213], v[194:197], 0\n"
      "ds_read_b128 v[198:201], v9 offset:1024\n"
      "v_subrev_u32 v154, 115456, v9\n"
      "v_mfma_f32_16x16x32_bf16 v[202:205], v[118:121], v[76:79], v[202:205]\n"
      "s_mul_i32 s92, s40, 2048\n"
      "v_and_b32 v194, 0x300, v154\n"
      "v_add_u32 v154, v154, v194\n"
      "v_mfma_f32_16x16x32_bf16 v[104:107], v[210:213], v[130:133], 0\n"
      "v_add_u32 v154, 149504, v154\n"
      "ds_read_b128 v[194:197], v154\n"
      "v_mfma_f32_16x16x32_bf16 v[104:107], v[118:121], v[80:83], v[104:107]\n"
      "ds_read_b128 v[134:137], v154 offset:2048\n"
      "v_subrev_u32 v203, 115456, v9\n"
      "v_mfma_f32_16x16x32_bf16 v[174:177], v[210:213], v[214:217], 0\n"
      "ds_read_b128 v[204:207], v154 offset:4096\n"
      "v_add_u32_e64 v77, s92, v203\n"
      "v_mfma_f32_16x16x32_bf16 v[174:177], v[118:121], v[84:87], v[174:177]\n"
      "ds_read_b128 v[216:219], v154 offset:6144\n"
      "v_mov_b32_dpp v188, v188 quad_perm:[0,0,0,0] row_mask:0xf bank_mask:0xf\n"
      "v_rcp_f32_e64 v188, v188\n"
      "v_cndmask_b32 v202, v202, v104, s[58:59]\n"
      "s_waitcnt lgkmcnt(4)\n"
      "v_add_f32 v198, v198, v199\n"
      "v_add_f32 v200, v200, v201\n"
      "v_add_f32 v198, v198, v200\n"
      "v_cndmask_b32 v202, v202, v174, s[60:61]\n"
      "v_mul_f32 v202, v188, v202\n"
      "v_cvt_pk_bf16_f32 v202, v202, v202\n"
      "s_waitcnt lgkmcnt(3)\n"
      "v_mfma_f32_16x16x32_bf16 v[120:123], a[216:219], v[194:197], 0\n"
      "ds_write_b16 v12, v202 offset:16\n"
      "s_waitcnt lgkmcnt(3)\n"
      "v_mfma_f32_16x16x32_bf16 v[120:123], a[220:223], v[134:137], v[120:123]\n"
      "s_waitcnt lgkmcnt(0)\n"
      "s_barrier\n"
      "v_add_u32 v12, 32, v12\n"
      "v_mfma_f32_16x16x32_bf16 v[188:191], a[240:243], v[194:197], 0\n"
      "ds_read_b128 v[212:215], v154 offset:8192\n"
      "v_rsq_f32 v198, v198\n"
      "v_mfma_f32_16x16x32_bf16 v[188:191], a[244:247], v[134:137], v[188:191]\n"
      "s_waitcnt lgkmcnt(2)\n"
      "v_mul_f32 v198, 0x415db3d7, v198\n"
      "v_mov_b32 v200, v198\n"
      "v_mfma_f32_16x16x32_bf16 v[130:133], v[232:235], v[194:197], 0\n"
      "ds_read_b128 v[194:197], v154 offset:10240\n"
      "v_permlane32_swap_b32 v198, v200\n"
      "v_mfma_f32_16x16x32_bf16 v[130:133], v[236:239], v[134:137], v[130:133]\n"
      "ds_read_b128 v[104:107], v2 offset:18432\n"
      "v_mov_b32 v199, v198\n"
      "v_mfma_f32_16x16x32_bf16 v[120:123], a[224:227], v[204:207], v[120:123]\n"
      "ds_read_b128 v[208:211], v2 offset:21504\n"
      "v_permlane16_swap_b32 v198, v199\n"
      "v_mfma_f32_16x16x32_bf16 v[120:123], a[228:231], v[216:219], v[120:123]\n"
      "v_mul_f32 v158, v198, v158\n"
      "v_mul_f32_e64 v159, v198, v159\n"
      "v_mfma_f32_16x16x32_bf16 v[188:191], a[248:251], v[204:207], v[188:191]\n"
      "s_waitcnt lgkmcnt(3)\n"
      "v_cvt_pk_bf16_f32 v158, v158, v159\n"
      "v_mul_f32 v160, v198, v160\n"
      "v_mfma_f32_16x16x32_bf16 v[188:191], a[252:255], v[216:219], v[188:191]\n"
      "v_mul_f32 v161, v198, v161\n"
      "v_mul_f32_e64 v114, v198, v114\n"
      "v_mfma_f32_16x16x32_bf16 v[130:133], v[240:243], v[204:207], v[130:133]\n"
      "s_waitcnt lgkmcnt(2)\n"
      "v_mul_f32 v115, v198, v115\n"
      "v_cvt_pk_bf16_f32 v159, v160, v161\n"
      "v_mfma_f32_16x16x32_bf16 v[130:133], v[244:247], v[216:219], v[130:133]\n"
      "ds_read_b128 v[204:207], v2 offset:24576\n"
      "v_mul_f32 v96, v198, v96\n"
      "v_mfma_f32_16x16x32_bf16 v[120:123], a[232:235], v[212:215], v[120:123]\n"
      "ds_read_b128 v[134:137], v2 offset:27648\n"
      "v_mul_f32 v97, v198, v97\n"
      "v_mfma_f32_16x16x32_bf16 v[120:123], a[236:239], v[194:197], v[120:123]\n"
      "s_waitcnt lgkmcnt(2)\n"
      "v_mul_f32 v98, v198, v98\n"
      "v_mul_f32_e64 v99, v198, v99\n"
      "v_mfma_f32_16x16x32_bf16 v[188:191], v[224:227], v[212:215], v[188:191]\n"
      "v_cvt_pk_bf16_f32 v114, v114, v115\n"
      "v_mul_f32 v116, v198, v116\n"
      "v_mfma_f32_16x16x32_bf16 v[188:191], v[228:231], v[194:197], v[188:191]\n"
      "v_mul_f32 v117, v198, v117\n"
      "v_cvt_pk_bf16_f32 v160, v96, v97\n"
      "v_mfma_f32_16x16x32_bf16 v[130:133], v[248:251], v[212:215], v[130:133]\n"
      "v_cvt_pk_bf16_f32 v161, v98, v99\n"
      "v_cvt_pk_bf16_f32 v115, v116, v117\n"
      "v_mfma_f32_16x16x32_bf16 v[130:133], v[252:255], v[194:197], v[130:133]\n"
      "ds_write_b128 v5, v[158:161]\n"
      "ds_write_b64 v6, v[114:115]\n"
      "v_mul_f32 v178, v199, v178\n"
      "v_mul_f32 v179, v199, v179\n"
      "v_cvt_pk_bf16_f32 v178, v178, v179\n"
      "v_mul_f32 v180, v199, v180\n"
      "v_mul_f32 v181, v199, v181\n"
      "v_mul_f32 v126, v199, v126\n"
      "v_mul_f32 v127, v199, v127\n"
      "v_cvt_pk_bf16_f32 v179, v180, v181\n"
      "v_mul_f32 v166, v199, v166\n"
      "v_mul_f32 v167, v199, v167\n"
      "v_mul_f32 v168, v199, v168\n"
      "v_mul_f32 v169, v199, v169\n"
      "v_cvt_pk_bf16_f32 v126, v126, v127\n"
      "v_mul_f32 v128, v199, v128\n"
      "v_mul_f32 v129, v199, v129\n"
      "v_cvt_pk_bf16_f32 v180, v166, v167\n"
      "v_cvt_pk_bf16_f32 v181, v168, v169\n"
      "v_cvt_pk_bf16_f32 v127, v128, v129\n"
      "ds_write_b128 v5, v[178:181] offset:6144\n"
      "ds_write_b64 v6, v[126:127] offset:6144\n"
      "ds_read_b128 v[178:181], v2 offset:30720\n"
      "v_mul_f32 v144, v200, v144\n"
      "v_mul_f32 v145, v200, v145\n"
      "v_cvt_pk_bf16_f32 v144, v144, v145\n"
      "v_mul_f32 v146, v200, v146\n"
      "v_mul_f32 v147, v200, v147\n"
      "v_mul_f32 v138, v200, v138\n"
      "v_mul_f32 v139, v200, v139\n"
      "v_cvt_pk_bf16_f32 v145, v146, v147\n"
      "v_mul_f32 v108, v200, v108\n"
      "v_mul_f32 v109, v200, v109\n"
      "v_mul_f32 v110, v200, v110\n"
      "v_mul_f32 v111, v200, v111\n"
      "v_cvt_pk_bf16_f32 v138, v138, v139\n"
      "v_mul_f32 v140, v200, v140\n"
      "v_mul_f32 v141, v200, v141\n"
      "v_cvt_pk_bf16_f32 v146, v108, v109\n"
      "v_cvt_pk_bf16_f32 v147, v110, v111\n"
      "v_cvt_pk_bf16_f32 v139, v140, v141\n"
      "ds_write_b128 v5, v[144:147] offset:12288\n"
      "ds_write_b64 v6, v[138:139] offset:12288\n"
      "v_mfma_f32_16x16x32_bf16 v[158:161], a[216:219], v[104:107], 0\n"
      "ds_read_b128 v[166:169], v2 offset:33792\n"
      "v_fma_f32 v119, v120, v120, s52\n"
      "v_mfma_f32_16x16x32_bf16 v[158:161], a[220:223], v[208:211], v[158:161]\n"
      "s_waitcnt lgkmcnt(3)\n"
      "v_fmac_f32 v119, v121, v121\n"
      "v_fmac_f32 v119, v122, v122\n"
      "v_mfma_f32_16x16x32_bf16 v[96:99], a[240:243], v[104:107], 0\n"
      "v_fmac_f32 v119, v123, v123\n"
      "v_fmac_f32 v119, v188, v188\n"
      "v_mfma_f32_16x16x32_bf16 v[96:99], a[244:247], v[208:211], v[96:99]\n"
      "v_fmac_f32 v119, v189, v189\n"
      "v_fmac_f32 v119, v190, v190\n"
      "v_mfma_f32_16x16x32_bf16 v[114:117], v[232:235], v[104:107], 0\n"
      "v_fmac_f32 v119, v191, v191\n"
      "v_fmac_f32_e64 v119, v130, v130\n"
      "v_mfma_f32_16x16x32_bf16 v[114:117], v[236:239], v[208:211], v[114:117]\n"
      "ds_read_b128 v[144:147], v2 offset:18688\n"
      "v_fmac_f32 v119, v131, v131\n"
      "v_mfma_f32_16x16x32_bf16 v[158:161], a[224:227], v[204:207], v[158:161]\n"
      "ds_read_b128 v[108:111], v2 offset:21760\n"
      "v_fmac_f32 v119, v132, v132\n"
      "v_mfma_f32_16x16x32_bf16 v[158:161], a[228:231], v[134:137], v[158:161]\n"
      "s_waitcnt lgkmcnt(2)\n"
      "v_fmac_f32 v119, v133, v133\n"
      "v_lshrrev_b32 v203, 4, v77\n"
      "v_mfma_f32_16x16x32_bf16 v[96:99], a[248:251], v[204:207], v[96:99]\n"
      "v_add_u32 v210, 0, v203\n"
      "v_add_u32 v211, 64, v203\n"
      "v_mfma_f32_16x16x32_bf16 v[96:99], a[252:255], v[134:137], v[96:99]\n"
      "v_and_b32 v86, 0x3fffffff, v37\n"
      "v_ashrrev_i32 v197, 31, v37\n"
      "v_mfma_f32_16x16x32_bf16 v[114:117], v[240:243], v[204:207], v[114:117]\n"
      "v_mul_u32_u24 v81, 2731, v210\n"
      "v_mul_u32_u24 v84, 2731, v211\n"
      "v_mfma_f32_16x16x32_bf16 v[114:117], v[244:247], v[134:137], v[114:117]\n"
      "ds_read_b128 v[138:141], v2 offset:24832\n"
      "v_add_u32 v86, 1179648, v86\n"
      "v_mfma_f32_16x16x32_bf16 v[158:161], a[232:235], v[178:181], v[158:161]\n"
      "ds_read_b128 v[206:209], v2 offset:27904\n"
      "v_and_b32 v197, 100270464, v197\n"
      "v_mfma_f32_16x16x32_bf16 v[158:161], a[236:239], v[166:169], v[158:161]\n"
      "s_waitcnt lgkmcnt(2)\n"
      "v_lshrrev_b32 v81, 16, v81\n"
      "v_lshrrev_b32 v84, 16, v84\n"
      "v_mfma_f32_16x16x32_bf16 v[96:99], v[224:227], v[178:181], v[96:99]\n"
      "v_sub_u32 v86, v86, v197\n"
      "v_mul_u32_u24 v80, 24, v81\n"
      "v_mfma_f32_16x16x32_bf16 v[96:99], v[228:231], v[166:169], v[96:99]\n"
      "v_mul_u32_u24 v82, 24, v84\n"
      "v_fma_f32 v85, v158, v158, s52\n"
      "v_mfma_f32_16x16x32_bf16 v[114:117], v[248:251], v[178:181], v[114:117]\n"
      "v_fmac_f32 v85, v159, v159\n"
      "v_fmac_f32 v85, v160, v160\n"
      "v_mfma_f32_16x16x32_bf16 v[114:117], v[252:255], v[166:169], v[114:117]\n"
      "ds_read_b128 v[198:201], v2 offset:30976\n"
      "v_fmac_f32_e64 v85, v161, v161\n"
      "v_mfma_f32_16x16x32_bf16 v[178:181], a[216:219], v[144:147], 0\n"
      "ds_read_b128 v[202:205], v2 offset:34048\n"
      "v_fmac_f32 v85, v96, v96\n"
      "v_mfma_f32_16x16x32_bf16 v[178:181], a[220:223], v[108:111], v[178:181]\n"
      "s_waitcnt lgkmcnt(2)\n"
      "v_fmac_f32 v85, v97, v97\n"
      "v_fmac_f32 v85, v98, v98\n"
      "v_mfma_f32_16x16x32_bf16 v[166:169], a[240:243], v[144:147], 0\n"
      "v_fmac_f32 v85, v99, v99\n"
      "v_fmac_f32 v85, v114, v114\n"
      "v_mfma_f32_16x16x32_bf16 v[166:169], a[244:247], v[108:111], v[166:169]\n"
      "v_fmac_f32 v85, v115, v115\n"
      "v_fmac_f32 v85, v116, v116\n"
      "v_mfma_f32_16x16x32_bf16 v[126:129], v[232:235], v[144:147], 0\n"
      "v_fmac_f32_e64 v85, v117, v117\n"
      "v_bfe_i32 v197, v37, 30, 1\n"
      "v_mfma_f32_16x16x32_bf16 v[126:129], v[236:239], v[108:111], v[126:129]\n"
      "ds_read_b128 v[134:137], v2 offset:18944\n"
      "v_sub_u32_e64 v80, v210, v80\n"
      "v_mfma_f32_16x16x32_bf16 v[178:181], a[224:227], v[138:141], v[178:181]\n"
      "ds_read_b128 v[104:107], v2 offset:22016\n"
      "v_sub_u32 v82, v211, v82\n"
      "v_mfma_f32_16x16x32_bf16 v[178:181], a[228:231], v[206:209], v[178:181]\n"
      "s_waitcnt lgkmcnt(2)\n"
      "v_and_b32 v197, 196224, v197\n"
      "v_and_b32 v210, 7, v81\n"
      "v_mfma_f32_16x16x32_bf16 v[166:169], a[248:251], v[138:141], v[166:169]\n"
      "v_and_b32 v211, 7, v84\n"
      "v_sub_u32_e64 v86, v86, v197\n"
      "v_mfma_f32_16x16x32_bf16 v[166:169], a[252:255], v[206:209], v[166:169]\n"
      "v_lshl_add_u32 v81, v81, 2, 12\n"
      "v_lshl_add_u32 v84, v84, 2, 12\n"
      "v_mfma_f32_16x16x32_bf16 v[126:129], v[240:243], v[138:141], v[126:129]\n"
      "v_xor_b32_e64 v80, v80, v210\n"
      "v_xor_b32 v82, v82, v211\n"
      "v_mfma_f32_16x16x32_bf16 v[126:129], v[244:247], v[206:209], v[126:129]\n"
      "ds_read_b128 v[194:197], v2 offset:25088\n"
      "v_subrev_u32 v78, 73728, v5\n"
      "v_mfma_f32_16x16x32_bf16 v[178:181], a[232:235], v[198:201], v[178:181]\n"
      "ds_read_b128 v[208:211], v2 offset:28160\n"
      "v_subrev_u32 v118, 73728, v6\n"
      "v_mfma_f32_16x16x32_bf16 v[178:181], a[236:239], v[202:205], v[178:181]\n"
      "s_waitcnt lgkmcnt(2)\n"
      "v_mfma_f32_16x16x32_bf16 v[166:169], v[224:227], v[198:201], v[166:169]\n"
      "v_mfma_f32_16x16x32_bf16 v[166:169], v[228:231], v[202:205], v[166:169]\n"
      "v_mfma_f32_16x16x32_bf16 v[126:129], v[248:251], v[198:201], v[126:129]\n"
      "v_mfma_f32_16x16x32_bf16 v[126:129], v[252:255], v[202:205], v[126:129]\n"
      "ds_read_b128 v[198:201], v2 offset:31232\n"
      "v_mfma_f32_16x16x32_bf16 v[144:147], a[216:219], v[134:137], 0\n"
      "ds_read_b128 v[204:207], v2 offset:34304\n"
      "v_fma_f32 v125, v178, v178, s52\n"
      "v_mfma_f32_16x16x32_bf16 v[144:147], a[220:223], v[104:107], v[144:147]\n"
      "s_waitcnt lgkmcnt(2)\n"
      "v_fmac_f32 v125, v179, v179\n"
      "v_fmac_f32 v125, v180, v180\n"
      "v_mfma_f32_16x16x32_bf16 v[108:111], a[240:243], v[134:137], 0\n"
      "v_fmac_f32 v125, v181, v181\n"
      "v_fmac_f32 v125, v166, v166\n"
      "v_mfma_f32_16x16x32_bf16 v[108:111], a[244:247], v[104:107], v[108:111]\n"
      "v_fmac_f32 v125, v167, v167\n"
      "v_fmac_f32 v125, v168, v168\n"
      "v_mfma_f32_16x16x32_bf16 v[138:141], v[232:235], v[134:137], 0\n"
      "v_fmac_f32 v125, v169, v169\n"
      "v_fmac_f32 v125, v126, v126\n"
      "v_mfma_f32_16x16x32_bf16 v[138:141], v[236:239], v[104:107], v[138:141]\n"
      "ds_read_b128 v[134:137], v154 offset:256\n"
      "v_fmac_f32 v125, v127, v127\n"
      "v_mfma_f32_16x16x32_bf16 v[144:147], a[224:227], v[194:197], v[144:147]\n"
      "ds_read_b128 v[212:215], v154 offset:2304\n"
      "v_fmac_f32 v125, v128, v128\n"
      "v_mfma_f32_16x16x32_bf16 v[144:147], a[228:231], v[208:211], v[144:147]\n"
      "s_waitcnt lgkmcnt(2)\n"
      "v_fmac_f32_e64 v125, v129, v129\n"
      "v_mfma_f32_16x16x32_bf16 v[108:111], a[248:251], v[194:197], v[108:111]\n"
      "v_mfma_f32_16x16x32_bf16 v[108:111], a[252:255], v[208:211], v[108:111]\n"
      "v_permlane16_swap_b32 v85, v125\n"
      "v_mfma_f32_16x16x32_bf16 v[138:141], v[240:243], v[194:197], v[138:141]\n"
      "v_add_f32 v85, v85, v125\n"
      "v_mfma_f32_16x16x32_bf16 v[138:141], v[244:247], v[208:211], v[138:141]\n"
      "ds_read_b128 v[104:107], v154 offset:4352\n"
      "v_mfma_f32_16x16x32_bf16 v[144:147], a[232:235], v[198:201], v[144:147]\n"
      "ds_read_b128 v[208:211], v154 offset:6400\n"
      "v_mfma_f32_16x16x32_bf16 v[144:147], a[236:239], v[204:207], v[144:147]\n"
      "ds_read_b128 v[194:197], v7 offset:18432\n"
      "v_mfma_f32_16x16x32_bf16 v[108:111], v[224:227], v[198:201], v[108:111]\n"
      "ds_read_b128 v[216:219], v7 offset:22528\n"
      "v_mfma_f32_16x16x32_bf16 v[108:111], v[228:231], v[204:207], v[108:111]\n"
      "s_waitcnt lgkmcnt(4)\n"
      "v_mfma_f32_16x16x32_bf16 v[138:141], v[248:251], v[198:201], v[138:141]\n"
      "ds_read_b128 v[200:203], v7 offset:26624\n"
      "v_mfma_f32_16x16x32_bf16 v[138:141], v[252:255], v[204:207], v[138:141]\n"
      "ds_read_b128 v[204:207], v154 offset:8448\n"
      "v_fma_f32 v198, v144, v144, s52\n"
      "v_mfma_f32_16x16x32_bf16 v[182:185], a[216:219], v[134:137], 0\n"
      "ds_read_b128 v[220:223], v7 offset:30720\n"
      "v_fmac_f32 v198, v145, v145\n"
      "v_mfma_f32_16x16x32_bf16 v[182:185], a[220:223], v[212:215], v[182:185]\n"
      "ds_read_b128 v[170:173], v154 offset:10496\n"
      "v_fmac_f32_e64 v198, v146, v146\n"
      "v_mfma_f32_16x16x32_bf16 v[162:165], a[240:243], v[134:137], 0\n"
      "ds_read_b128 v[174:177], v7 offset:34816\n"
      "v_fmac_f32 v198, v147, v147\n"
      "v_mfma_f32_16x16x32_bf16 v[162:165], a[244:247], v[212:215], v[162:165]\n"
      "s_waitcnt lgkmcnt(5)\n"
      "v_fmac_f32 v198, v108, v108\n"
      "v_fmac_f32 v198, v109, v109\n"
      "v_mfma_f32_16x16x32_bf16 v[154:157], v[232:235], v[134:137], 0\n"
      "buffer_store_dwordx4 v[194:197], v20, s[24:27], s46 offen sc1\n"
      "v_mfma_f32_16x16x32_bf16 v[154:157], v[236:239], v[212:215], v[154:157]\n"
      "ds_bpermute_b32 v136, v81, v86\n"
      "v_fmac_f32 v198, v110, v110\n"
      "v_mfma_f32_16x16x32_bf16 v[182:185], a[224:227], v[104:107], v[182:185]\n"
      "ds_bpermute_b32 v194, v84, v86\n"
      "v_fmac_f32 v198, v111, v111\n"
      "v_mfma_f32_16x16x32_bf16 v[182:185], a[228:231], v[208:211], v[182:185]\n"
      "s_waitcnt lgkmcnt(2)\n"
      "v_fmac_f32 v198, v138, v138\n"
      "v_fmac_f32 v198, v139, v139\n"
      "v_mfma_f32_16x16x32_bf16 v[162:165], a[248:251], v[104:107], v[162:165]\n"
      "v_fmac_f32 v198, v140, v140\n"
      "v_fmac_f32 v198, v141, v141\n"
      "v_mfma_f32_16x16x32_bf16 v[162:165], a[252:255], v[208:211], v[162:165]\n"
      "v_mov_b32 v125, v198\n"
      "v_mfma_f32_16x16x32_bf16 v[154:157], v[240:243], v[104:107], v[154:157]\n"
      "v_mfma_f32_16x16x32_bf16 v[154:157], v[244:247], v[208:211], v[154:157]\n"
      "buffer_store_dwordx4 v[216:219], v21, s[24:27], s46 offen sc1\n"
      "v_mfma_f32_16x16x32_bf16 v[182:185], a[232:235], v[204:207], v[182:185]\n"
      "s_waitcnt lgkmcnt(0)\n"
      "v_permlane16_swap_b32 v198, v125\n"
      "v_mfma_f32_16x16x32_bf16 v[182:185], a[236:239], v[170:173], v[182:185]\n"
      "v_add_f32 v198, v198, v125\n"
      "v_lshl_add_u32 v136, v80, 4, v136\n"
      "v_mfma_f32_16x16x32_bf16 v[162:165], v[224:227], v[204:207], v[162:165]\n"
      "v_permlane32_swap_b32 v85, v198\n"
      "v_mfma_f32_16x16x32_bf16 v[162:165], v[228:231], v[170:173], v[162:165]\n"
      "v_add_f32 v85, v85, v198\n"
      "ds_write_b32 v31, v85\n"
      "v_mfma_f32_16x16x32_bf16 v[154:157], v[248:251], v[204:207], v[154:157]\n"
      "v_fma_f32 v210, v182, v182, s52\n"
      "v_fmac_f32 v210, v183, v183\n"
      "v_mfma_f32_16x16x32_bf16 v[154:157], v[252:255], v[170:173], v[154:157]\n"
      "buffer_store_dwordx4 v[200:203], v22, s[24:27], s46 offen sc1\n"
      "v_fmac_f32 v210, v184, v184\n"
      "v_fmac_f32 v210, v185, v185\n"
      "v_fmac_f32 v210, v162, v162\n"
      "v_fmac_f32 v210, v163, v163\n"
      "v_fmac_f32 v210, v164, v164\n"
      "v_fmac_f32 v210, v165, v165\n"
      "v_lshl_add_u32 v194, v82, 4, v194\n"
      "v_fmac_f32 v210, v154, v154\n"
      "v_fmac_f32 v210, v155, v155\n"
      "v_fmac_f32 v210, v156, v156\n"
      "v_fmac_f32 v210, v157, v157\n"
      "s_nop 0\n"
      "s_nop 0\n"
      "v_permlane16_swap_b32 v119, v210\n"
      "v_add_f32 v119, v119, v210\n"
      "v_mov_b32 v210, v119\n"
      "s_nop 0\n"
      "s_nop 0\n"
      "v_permlane32_swap_b32 v119, v210\n"
      "v_add_f32_e64 v119, v119, v210\n"
      "ds_write_b32 v31, v119 offset:4608\n"
      "buffer_store_dwordx4 v[220:223], v23, s[24:27], s46 offen sc1\n"
      "buffer_store_dwordx4 v[174:177], v25, s[24:27], s46 offen sc1\n"
      "s_nop 0\n"
      "s_nop 0\n"
      "v_readlane_b32 s46, v37, s42\n"
      "s_add_u32 s42, s42, 1\n"
      "s_or_b32 s95, s46, s48\n"
      "s_cmp_ge_u32 s95, 0x40000000\n"
      "s_cbranch_scc1 L_rare_35_%=\n"
      "L_back_36_%=:\n"
      "s_waitcnt lgkmcnt(0)\n"
      "s_barrier\n"
      "ds_read_b128 v[208:211], v7\n"
      "ds_read_b128 v[218:221], v7 offset:4096\n"
      "ds_read_b128 v[214:217], v7 offset:8192\n"
      "ds_read_b128 v[170:173], v7 offset:12288\n"
      "ds_read_b128 v[202:205], v9\n"
      "ds_read_b128 v[196:199], v7 offset:16384\n"
      "ds_read_b128 v[174:177], v9 offset:4608\n"
      "s_waitcnt lgkmcnt(6)\n"
      "buffer_store_dwordx4 v[208:211], v20, s[24:27], s46 offen sc1\n"
      "s_waitcnt lgkmcnt(4)\n"
      "buffer_store_dwordx4 v[218:221], v21, s[24:27], s46 offen sc1\n"
      "buffer_store_dwordx4 v[214:217], v22, s[24:27], s46 offen sc1\n"
      "s_waitcnt lgkmcnt(1)\n"
      "buffer_store_dwordx4 v[170:173], v23, s[24:27], s46 offen sc1\n"
      "buffer_store_dwordx4 v[196:199], v25, s[24:27], s46 offen sc1\n"
      "v_add_f32 v202, v202, v203\n"
      "v_add_f32 v204, v204, v205\n"
      "v_add_f32 v202, v202, v204\n"
      "v_rsq_f32 v202, v202\n"
      "s_waitcnt lgkmcnt(0)\n"
      "v_add_f32 v174, v174, v175\n"
      "v_mul_f32 v202, 0x415db3d7, v202\n"
      "v_mov_b32 v204, v202\n"
      "v_add_f32 v176, v176, v177\n"
      "v_add_f32 v174, v174, v176\n"
      "v_permlane32_swap_b32 v202, v204\n"
      "v_mov_b32 v203, v202\n"
      "v_mul_f32 v144, v204, v144\n"
      "v_mul_f32 v145, v204, v145\n"
      "v_permlane16_swap_b32 v202, v203\n"
      "v_mul_f32 v158, v202, v158\n"
      "v_mul_f32_e64 v159, v202, v159\n"
      "v_cvt_pk_bf16_f32 v158, v158, v159\n"
      "v_mul_f32 v160, v202, v160\n"
      "v_mul_f32 v161, v202, v161\n"
      "v_mul_f32 v114, v202, v114\n"
      "v_mul_f32 v115, v202, v115\n"
      "v_cvt_pk_bf16_f32 v159, v160, v161\n"
      "v_mul_f32 v96, v202, v96\n"
      "v_mul_f32 v97, v202, v97\n"
      "v_mul_f32 v98, v202, v98\n"
      "v_mul_f32 v99, v202, v99\n"
      "v_cvt_pk_bf16_f32 v114, v114, v115\n"
      "v_mul_f32 v116, v202, v116\n"
      "v_mul_f32 v117, v202, v117\n"
      "v_cvt_pk_bf16_f32 v160, v96, v97\n"
      "v_cvt_pk_bf16_f32 v161, v98, v99\n"
      "v_cvt_pk_bf16_f32 v115, v116, v117\n"
      "ds_write_b128 v5, v[158:161] offset:18432\n"
      "ds_write_b64 v6, v[114:115] offset:18432\n"
      "v_mul_f32 v178, v203, v178\n"
      "v_mul_f32 v179, v203, v179\n"
      "v_cvt_pk_bf16_f32 v178, v178, v179\n"
      "v_mul_f32 v180, v203, v180\n"
      "v_mul_f32 v181, v203, v181\n"
      "v_mul_f32 v126, v203, v126\n"
      "v_mul_f32 v127, v203, v127\n"
      "v_cvt_pk_bf16_f32 v179, v180, v181\n"
      "v_mul_f32 v166, v203, v166\n"
      "v_mul_f32 v167, v203, v167\n"
      "v_mul_f32 v168, v203, v168\n"
      "v_mul_f32 v169, v203, v169\n"
      "v_cvt_pk_bf16_f32 v126, v126, v127\n"
      "v_mul_f32 v128, v203, v128\n"
      "v_mul_f32 v129, v203, v129\n"
      "v_cvt_pk_bf16_f32 v180, v166, v167\n"
      "v_cvt_pk_bf16_f32 v181, v168, v169\n"
      "v_cvt_pk_bf16_f32 v127, v128, v129\n"
      "ds_write_b128 v5, v[178:181] offset:24576\n"
      "ds_write_b64 v6, v[126:127] offset:24576\n"
      "v_cvt_pk_bf16_f32 v144, v144, v145\n"
      "v_mul_f32 v146, v204, v146\n"
      "v_mul_f32 v147, v204, v147\n"
      "v_mul_f32 v138, v204, v138\n"
      "v_mul_f32 v139, v204, v139\n"
      "v_cvt_pk_bf16_f32 v145, v146, v147\n"
      "v_mul_f32 v108, v204, v108\n"
      "v_mul_f32 v109, v204, v109\n"
      "v_mul_f32 v110, v204, v110\n"
      "v_mul_f32 v111, v204, v111\n"
      "v_cvt_pk_bf16_f32 v138, v138, v139\n"
      "v_mul_f32 v140, v204, v140\n"
      "v_mul_f32 v141, v204, v141\n"
      "v_cvt_pk_bf16_f32 v146, v108, v109\n"
      "v_cvt_pk_bf16_f32 v147, v110, v111\n"
      "v_cvt_pk_bf16_f32 v139, v140, v141\n"
      "ds_write_b128 v5, v[144:147] offset:30720\n"
      "ds_write_b64 v6, v[138:139] offset:30720\n"
      "v_rsq_f32 v174, v174\n"
      "v_readlane_b32 s46, v37, s42\n"
      "v_mul_f32 v174, 0x415db3d7, v174\n"
      "v_mov_b32 v176, v174\n"
      "s_or_b32 s95, s46, s48\n"
      "s_cmp_ge_u32 s95, 0x40000000\n"
      "s_cbranch_scc1 L_rare_39_%=\n"
      "L_back_40_%=:\n"
      "s_add_u32 s42, s42, 1\n"
      "v_permlane32_swap_b32 v174, v176\n"
      "v_mov_b32 v175, v174\n"
      "s_nop 0\n"
      "s_nop 0\n"
      "v_permlane16_swap_b32 v174, v175\n"
      "v_mul_f32 v120, v174, v120\n"
      "v_mul_f32 v121, v174, v121\n"
      "v_mul_f32 v182, v175, v182\n"
      "v_mul_f32 v183, v175, v183\n"
      "v_cvt_pk_bf16_f32 v120, v120, v121\n"
      "v_mul_f32 v122, v174, v122\n"
      "v_mul_f32_e64 v123, v174, v123\n"
      "v_cvt_pk_bf16_f32 v182, v182, v183\n"
      "v_mul_f32 v184, v175, v184\n"
      "v_mul_f32 v185, v175, v185\n"
      "v_cvt_pk_bf16_f32 v121, v122, v123\n"
      "v_mul_f32 v188, v174, v188\n"
      "v_mul_f32 v189, v174, v189\n"
      "v_mul_f32 v190, v174, v190\n"
      "v_mul_f32 v191, v174, v191\n"
      "v_cvt_pk_bf16_f32 v183, v184, v185\n"
      "v_mul_f32 v162, v175, v162\n"
      "v_mul_f32 v163, v175, v163\n"
      "v_mul_f32 v164, v175, v164\n"
      "v_mul_f32 v165, v175, v165\n"
      "v_cvt_pk_bf16_f32 v122, v188, v189\n"
      "v_cvt_pk_bf16_f32 v123, v190, v191\n"
      "v_cvt_pk_bf16_f32 v184, v162, v163\n"
      "v_cvt_pk_bf16_f32 v185, v164, v165\n"
      "v_mul_f32 v130, v174, v130\n"
      "v_mul_f32 v131, v174, v131\n"
      "v_mul_f32 v154, v175, v154\n"
      "v_mul_f32 v155, v175, v155\n"
      "ds_write_b128 v78, v[120:123]\n"
      "ds_write_b128 v78, v[182:185] offset:6144\n"
      "v_cvt_pk_bf16_f32 v130, v130, v131\n"
      "v_mul_f32 v132, v174, v132\n"
      "v_mul_f32 v133, v174, v133\n"
      "v_cvt_pk_bf16_f32 v154, v154, v155\n"
      "v_mul_f32 v156, v175, v156\n"
      "v_mul_f32 v157, v175, v157\n"
      "v_cvt_pk_bf16_f32 v131, v132, v133\n"
      "v_cvt_pk_bf16_f32 v155, v156, v157\n"
      "ds_write_b64 v118, v[130:131]\n"
      "ds_write_b64 v118, v[154:155] offset:6144\n"
      "s_waitcnt lgkmcnt(0)\n"
      "s_barrier\n"
      "ds_read_b128 v[180:183], v7 offset:18432\n"
      "ds_read_b128 v[174:177], v7 offset:22528\n"
      "ds_read_b128 v[96:99], v7 offset:26624\n"
      "ds_read_b128 v[120:123], v7 offset:30720\n"
      "ds_read_b128 v[138:141], v7 offset:34816\n"
      "ds_read_b128 v[164:167], v77\n"
      "ds_read_b128 v[144:147], v77 offset:1024\n"
      "s_waitcnt lgkmcnt(6)\n"
      "buffer_store_dwordx4 v[180:183], v20, s[24:27], s46 offen sc1\n"
      "s_waitcnt lgkmcnt(5)\n"
      "buffer_store_dwordx4 v[174:177], v21, s[24:27], s46 offen sc1\n"
      "s_waitcnt lgkmcnt(4)\n"
      "buffer_store_dwordx4 v[96:99], v22, s[24:27], s46 offen sc1\n"
      "s_waitcnt lgkmcnt(3)\n"
      "buffer_store_dwordx4 v[120:123], v23, s[24:27], s46 offen sc1\n"
      "s_waitcnt lgkmcnt(2)\n"
      "buffer_store_dwordx4 v[138:141], v25, s[24:27], s46 offen sc1\n"
      "s_waitcnt lgkmcnt(1)\n"
      "buffer_store_dwordx4 v[164:167], v136, s[24:27], 0 offen sc1\n"
      "s_waitcnt lgkmcnt(0)\n"
      "buffer_store_dwordx4 v[144:147], v194, s[24:27], 0 offen sc1\n"
      "s_endpgm\n"
      "L_rare_1_%=:\n"
      "s_nop 7\n"
      "s_waitcnt lgkmcnt(0)\n"
      "s_add_u32 s72, s42, 1\n"
      "v_readlane_b32 s77, v38, s72\n"
      "v_subrev_u32 v98, 115456, v9\n"
      "s_and_b32 s73, s77, -16\n"
      "s_mov_b32 m0, 110592\n"
      "s_nop 0\n"
      "buffer_load_dwordx4 v98, s[28:31], s73 offen  lds\n"
      "s_add_u32 s73, s73, 1024\n"
      "s_mov_b32 m0, 111616\n"
      "s_nop 0\n"
      "buffer_load_dwordx4 v98, s[28:31], s73 offen  lds\n"
      "s_add_u32 s73, s73, 1024\n"
      "s_mov_b32 m0, 112640\n"
      "s_nop 0\n"
      "buffer_load_dwordx4 v98, s[28:31], s73 offen  lds\n"
      "s_add_u32 s73, s73, 1024\n"
      "s_mov_b32 m0, 113664\n"
      "s_nop 0\n"
      "buffer_load_dwordx4 v98, s[28:31], s73 offen  lds\n"
      "s_add_u32 s73, s73, 1024\n"
      "s_mov_b32 m0, 114688\n"
      "s_mov_b32 exec_hi, 0xffff\n"
      "buffer_load_dwordx4 v98, s[28:31], s73 offen  lds\n"
      "s_mov_b32 exec_hi, -1\n"
      "s_add_u32 s73, s73, 1024\n"
      "s_and_b32 s77, s77, 15\n"
      "s_waitcnt lgkmcnt(0)\n"
      "s_nop 4\n"
      "s_branch L_back_2_%=\n"
      "L_rare_3_%=:\n"
      "s_nop 7\n"
      "s_waitcnt lgkmcnt(0)\n"
      "s_waitcnt vmcnt(0)\n"
      "v_subrev_u32 v127, 115456, v9\n"
      "v_bfe_u32 v170, v127, 4, 4\n"
      "v_mul_u32_u24 v170, 98, v170\n"
      "v_lshrrev_b32 v127, 8, v127\n"
      "v_lshl_add_u32 v170, v127, 3, v170\n"
      "v_add_u32 v170, s77, v170\n"
      "v_and_b32 v171, 2, v170\n"
      "v_and_b32 v170, -4, v170\n"
      "v_add_u32 v170, 110592, v170\n"
      "ds_read2_b32 v[152:153], v170 offset0:0 offset1:1\n"
      "ds_read2_b32 v[154:155], v170 offset0:8 offset1:9\n"
      "ds_read2_b32 v[198:199], v170 offset0:16 offset1:17\n"
      "ds_read2_b32 v[200:201], v170 offset0:2 offset1:10\n"
      "ds_read2_b32 v[172:173], v170 offset0:18 offset1:24\n"
      "s_waitcnt lgkmcnt(4)\n"
      "v_alignbyte_b32 v152, v153, v152, v171\n"
      "s_waitcnt lgkmcnt(1)\n"
      "v_alignbyte_b32 v153, v200, v153, v171\n"
      "v_lshlrev_b32 v127, 16, v152\n"
      "v_fmac_f32 v188, s53, v127\n"
      "v_and_b32 v127, 0xffff0000, v152\n"
      "v_fmac_f32 v189, s53, v127\n"
      "v_lshlrev_b32 v127, 16, v153\n"
      "v_fmac_f32_e64 v190, s53, v127\n"
      "v_and_b32 v127, 0xffff0000, v153\n"
      "v_fmac_f32 v191, s53, v127\n"
      "v_alignbyte_b32 v154, v155, v154, v171\n"
      "v_alignbyte_b32 v155, v201, v155, v171\n"
      "v_lshlrev_b32 v127, 16, v154\n"
      "v_fmac_f32 v180, s53, v127\n"
      "v_and_b32 v127, 0xffff0000, v154\n"
      "v_fmac_f32 v181, s53, v127\n"
      "v_lshlrev_b32 v127, 16, v155\n"
      "v_fmac_f32 v182, s53, v127\n"
      "v_and_b32 v127, 0xffff0000, v155\n"
      "v_fmac_f32 v183, s53, v127\n"
      "v_alignbyte_b32 v198, v199, v198, v171\n"
      "s_waitcnt lgkmcnt(0)\n"
      "v_alignbyte_b32 v199, v172, v199, v171\n"
      "v_lshlrev_b32 v127, 16, v198\n"
      "v_fmac_f32 v144, s53, v127\n"
      "v_and_b32 v127, 0xffff0000, v198\n"
      "v_fmac_f32 v145, s53, v127\n"
      "v_lshlrev_b32 v127, 16, v199\n"
      "v_fmac_f32 v146, s53, v127\n"
      "v_and_b32 v127, 0xffff0000, v199\n"
      "v_fmac_f32 v147, s53, v127\n"
      "v_alignbyte_b32 v127, v173, v173, v171\n"
      "v_lshlrev_b32 v127, 16, v127\n"
      "v_fmac_f32 v126, s53, v127\n"
      "v_max3_f32 v170, v188, v189, v190\n"
      "v_max3_f32 v171, v191, v180, v181\n"
      "v_max3_f32 v170, v170, v182, v183\n"
      "v_max3_f32 v171, v171, v144, v145\n"
      "v_max3_f32 v170, v170, v146, v147\n"
      "v_max3_f32 v170, v170, v171, v126\n"
      "v_mov_b32 v127, v170\n"
      "s_nop 1\n"
      "v_permlane16_swap_b32 v170, v127\n"
      "v_max_f32 v170, v170, v127\n"
      "v_mov_b32 v127, v170\n"
      "s_nop 1\n"
      "v_permlane32_swap_b32 v170, v127\n"
      "v_max_f32 v170, v170, v127\n"
      "v_sub_f32 v188, v188, v170\n"
      "v_sub_f32 v189, v189, v170\n"
      "v_sub_f32 v190, v190, v170\n"
      "v_sub_f32 v191, v191, v170\n"
      "v_sub_f32 v180, v180, v170\n"
      "v_sub_f32 v181, v181, v170\n"
      "v_sub_f32 v182, v182, v170\n"
      "v_sub_f32 v183, v183, v170\n"
      "v_sub_f32 v144, v144, v170\n"
      "v_sub_f32 v145, v145, v170\n"
      "v_sub_f32 v146, v146, v170\n"
      "v_sub_f32 v147, v147, v170\n"
      "v_sub_f32_e64 v126, v126, v170\n"
      "s_waitcnt lgkmcnt(0)\n"
      "s_nop 4\n"
      "s_branch L_back_4_%=\n"
      "L_rare_5_%=:\n"
      "s_nop 7\n"
      "s_waitcnt lgkmcnt(0)\n"
      "s_waitcnt vmcnt(0)\n"
      "v_subrev_u32 v221, 115456, v9\n"
      "v_bfe_u32 v171, v221, 4, 4\n"
      "v_mul_u32_u24 v171, 98, v171\n"
      "v_lshrrev_b32 v221, 8, v221\n"
      "v_lshl_add_u32 v171, v221, 3, v171\n"
      "v_add_u32 v171, s77, v171\n"
      "v_and_b32 v219, 2, v171\n"
      "v_and_b32 v171, -4, v171\n"
      "v_add_u32 v171, 112160, v171\n"
      "ds_read2_b32 v[100:101], v171 offset0:0 offset1:1\n"
      "ds_read2_b32 v[102:103], v171 offset0:8 offset1:9\n"
      "ds_read2_b32 v[186:187], v171 offset0:16 offset1:17\n"
      "ds_read2_b32 v[184:185], v171 offset0:2 offset1:10\n"
      "ds_read2_b32 v[172:173], v171 offset0:18 offset1:24\n"
      "s_waitcnt lgkmcnt(4)\n"
      "v_alignbyte_b32 v100, v101, v100, v219\n"
      "s_waitcnt lgkmcnt(1)\n"
      "v_alignbyte_b32 v101, v184, v101, v219\n"
      "v_lshlrev_b32 v221, 16, v100\n"
      "v_fmac_f32 v210, s53, v221\n"
      "v_and_b32 v221, 0xffff0000, v100\n"
      "v_fmac_f32 v211, s53, v221\n"
      "v_lshlrev_b32 v221, 16, v101\n"
      "v_fmac_f32 v212, s53, v221\n"
      "v_and_b32 v221, 0xffff0000, v101\n"
      "v_fmac_f32 v213, s53, v221\n"
      "v_alignbyte_b32 v102, v103, v102, v219\n"
      "v_alignbyte_b32 v103, v185, v103, v219\n"
      "v_lshlrev_b32 v221, 16, v102\n"
      "v_fmac_f32 v198, s53, v221\n"
      "v_and_b32 v221, 0xffff0000, v102\n"
      "v_fmac_f32 v199, s53, v221\n"
      "v_lshlrev_b32 v221, 16, v103\n"
      "v_fmac_f32_e64 v200, s53, v221\n"
      "v_and_b32 v221, 0xffff0000, v103\n"
      "v_fmac_f32 v201, s53, v221\n"
      "v_alignbyte_b32 v186, v187, v186, v219\n"
      "s_waitcnt lgkmcnt(0)\n"
      "v_alignbyte_b32 v187, v172, v187, v219\n"
      "v_lshlrev_b32 v221, 16, v186\n"
      "v_fmac_f32 v180, s53, v221\n"
      "v_and_b32 v221, 0xffff0000, v186\n"
      "v_fmac_f32 v181, s53, v221\n"
      "v_lshlrev_b32 v221, 16, v187\n"
      "v_fmac_f32_e64 v182, s53, v221\n"
      "v_and_b32 v221, 0xffff0000, v187\n"
      "v_fmac_f32 v183, s53, v221\n"
      "v_alignbyte_b32 v221, v173, v173, v219\n"
      "v_lshlrev_b32 v221, 16, v221\n"
      "v_fmac_f32_e64 v170, s53, v221\n"
      "v_max3_f32 v171, v210, v211, v212\n"
      "v_max3_f32 v219, v213, v198, v199\n"
      "v_max3_f32 v171, v171, v200, v201\n"
      "v_max3_f32 v219, v219, v180, v181\n"
      "v_max3_f32 v171, v171, v182, v183\n"
      "v_max3_f32 v171, v171, v219, v170\n"
      "v_mov_b32 v221, v171\n"
      "s_nop 1\n"
      "v_permlane16_swap_b32 v171, v221\n"
      "v_max_f32 v171, v171, v221\n"
      "v_mov_b32 v221, v171\n"
      "s_nop 1\n"
      "v_permlane32_swap_b32 v171, v221\n"
      "v_max_f32 v171, v171, v221\n"
      "v_sub_f32 v210, v210, v171\n"
      "v_sub_f32 v211, v211, v171\n"
      "v_sub_f32 v212, v212, v171\n"
      "v_sub_f32 v213, v213, v171\n"
      "v_sub_f32 v198, v198, v171\n"
      "v_sub_f32 v199, v199, v171\n"
      "v_sub_f32 v200, v200, v171\n"
      "v_sub_f32 v201, v201, v171\n"
      "v_sub_f32 v180, v180, v171\n"
      "v_sub_f32 v181, v181, v171\n"
      "v_sub_f32 v182, v182, v171\n"
      "v_sub_f32 v183, v183, v171\n"
      "v_sub_f32_e64 v170, v170, v171\n"
      "s_waitcnt lgkmcnt(0)\n"
      "s_nop 4\n"
      "s_branch L_back_6_%=\n"
      "L_rare_7_%=:\n"
      "s_nop 7\n"
      "s_waitcnt lgkmcnt(0)\n"
      "s_waitcnt vmcnt(0)\n"
      "v_subrev_u32 v100, 115456, v9\n"
      "v_bfe_u32 v105, v100, 4, 4\n"
      "v_mul_u32_u24 v105, 98, v105\n"
      "v_lshrrev_b32_e64 v100, 8, v100\n"
      "v_lshl_add_u32 v105, v100, 3, v105\n"
      "v_add_u32 v105, s77, v105\n"
      "v_and_b32 v104, 2, v105\n"
      "v_and_b32_e64 v105, -4, v105\n"
      "v_add_u32 v105, 113728, v105\n"
      "ds_read2_b32 v[182:183], v105 offset0:0 offset1:1\n"
      "ds_read2_b32 v[102:103], v105 offset0:8 offset1:9\n"
      "ds_read2_b32 v[180:181], v105 offset0:16 offset1:17\n"
      "ds_read2_b32 v[154:155], v105 offset0:2 offset1:10\n"
      "ds_read2_b32 v[152:153], v105 offset0:18 offset1:24\n"
      "s_waitcnt lgkmcnt(4)\n"
      "v_alignbyte_b32 v182, v183, v182, v104\n"
      "s_waitcnt lgkmcnt(1)\n"
      "v_alignbyte_b32 v183, v154, v183, v104\n"
      "v_lshlrev_b32 v100, 16, v182\n"
      "v_fmac_f32 v124, s53, v100\n"
      "v_and_b32 v100, 0xffff0000, v182\n"
      "v_fmac_f32 v125, s53, v100\n"
      "v_lshlrev_b32 v100, 16, v183\n"
      "v_fmac_f32_e64 v126, s53, v100\n"
      "v_and_b32 v100, 0xffff0000, v183\n"
      "v_fmac_f32 v127, s53, v100\n"
      "v_alignbyte_b32 v102, v103, v102, v104\n"
      "v_alignbyte_b32 v103, v155, v103, v104\n"
      "v_lshlrev_b32 v100, 16, v102\n"
      "v_fmac_f32 v120, s53, v100\n"
      "v_and_b32 v100, 0xffff0000, v102\n"
      "v_fmac_f32 v121, s53, v100\n"
      "v_lshlrev_b32 v100, 16, v103\n"
      "v_fmac_f32 v122, s53, v100\n"
      "v_and_b32 v100, 0xffff0000, v103\n"
      "v_fmac_f32 v123, s53, v100\n"
      "v_alignbyte_b32 v180, v181, v180, v104\n"
      "s_waitcnt lgkmcnt(0)\n"
      "v_alignbyte_b32 v181, v152, v181, v104\n"
      "v_lshlrev_b32 v100, 16, v180\n"
      "v_fmac_f32 v218, s53, v100\n"
      "v_and_b32 v100, 0xffff0000, v180\n"
      "v_fmac_f32 v219, s53, v100\n"
      "v_lshlrev_b32 v100, 16, v181\n"
      "v_fmac_f32 v220, s53, v100\n"
      "v_and_b32 v100, 0xffff0000, v181\n"
      "v_fmac_f32 v221, s53, v100\n"
      "v_alignbyte_b32 v100, v153, v153, v104\n"
      "v_lshlrev_b32 v100, 16, v100\n"
      "v_fmac_f32 v202, s53, v100\n"
      "v_max3_f32 v105, v124, v125, v126\n"
      "v_max3_f32 v104, v127, v120, v121\n"
      "v_max3_f32 v105, v105, v122, v123\n"
      "v_max3_f32 v104, v104, v218, v219\n"
      "v_max3_f32 v105, v105, v220, v221\n"
      "v_max3_f32 v105, v105, v104, v202\n"
      "v_mov_b32 v100, v105\n"
      "s_nop 1\n"
      "v_permlane16_swap_b32 v105, v100\n"
      "v_max_f32 v105, v105, v100\n"
      "v_mov_b32 v100, v105\n"
      "s_nop 1\n"
      "v_permlane32_swap_b32 v105, v100\n"
      "v_max_f32 v105, v105, v100\n"
      "v_sub_f32 v124, v124, v105\n"
      "v_sub_f32 v125, v125, v105\n"
      "v_sub_f32 v126, v126, v105\n"
      "v_sub_f32 v127, v127, v105\n"
      "v_sub_f32 v120, v120, v105\n"
      "v_sub_f32 v121, v121, v105\n"
      "v_sub_f32 v122, v122, v105\n"
      "v_sub_f32 v123, v123, v105\n"
      "v_sub_f32 v218, v218, v105\n"
      "v_sub_f32 v219, v219, v105\n"
      "v_sub_f32 v220, v220, v105\n"
      "v_sub_f32 v221, v221, v105\n"
      "v_sub_f32 v202, v202, v105\n"
      "s_waitcnt lgkmcnt(0)\n"
      "s_nop 4\n"
      "s_branch L_back_8_%=\n"
      "L_rare_9_%=:\n"
      "s_nop 7\n"
      "s_waitcnt lgkmcnt(0)\n"
      "s_waitcnt vmcnt(0)\n"
      "v_subrev_u32 v147, 115456, v9\n"
      "v_lshrrev_b32 v147, 3, v147\n"
      "v_min_u32 v147, 96, v147\n"
      "v_add_u32 v147, s77, v147\n"
      "v_add_u32 v147, 115296, v147\n"
      "ds_read_u16 v147, v147\n"
      "s_waitcnt lgkmcnt(0)\n"
      "v_lshlrev_b32_e64 v147, 16, v147\n"
      "v_fmac_f32 v102, s53, v147\n"
      "s_nop 1\n"
      "v_max_f32_dpp v146, v102, v102 row_ror:8 row_mask:0xf bank_mask:0xf\n"
      "s_nop 1\n"
      "v_max_f32_dpp v146, v146, v146 row_ror:4 row_mask:0xf bank_mask:0xf\n"
      "s_nop 1\n"
      "v_max_f32_dpp v146, v146, v146 row_ror:2 row_mask:0xf bank_mask:0xf\n"
      "s_nop 1\n"
      "v_max_f32_dpp v146, v146, v146 row_ror:1 row_mask:0xf bank_mask:0xf\n"
      "s_nop 1\n"
      "v_max_f32_dpp v146, v146, v146 row_bcast:15 row_mask:0xa bank_mask:0xf\n"
      "s_nop 1\n"
      "v_max_f32_dpp v146, v146, v146 row_bcast:31 row_mask:0xc bank_mask:0xf\n"
      "s_nop 0\n"
      "v_readlane_b32 s72, v146, 63\n"
      "s_nop 1\n"
      "v_subrev_f32 v102, s72, v102\n"
      "s_waitcnt lgkmcnt(0)\n"
      "s_nop 4\n"
      "s_branch L_back_10_%=\n"
      "L_rare_27_%=:\n"
      "s_nop 7\n"
      "s_waitcnt lgkmcnt(0)\n"
      "s_bitcmp1_b32 s46, 31\n"
      "s_cselect_b32 s75, 100270464, 0\n"
      "s_bitcmp1_b32 s46, 30\n"
      "s_cselect_b32 s76, 196224, 0\n"
      "s_and_b32 s74, s46, 0x3fffffff\n"
      "s_cmp_ge_u32 s46, 0x40000000\n"
      "s_cselect_b32 s74, s74, 0\n"
      "s_cselect_b32 s46, 0, s46\n"
      "s_sub_u32 s74, s74, s69\n"
      "s_add_u32 s69, s69, s74\n"
      "s_sub_u32 s75, s75, s70\n"
      "s_add_u32 s70, s70, s75\n"
      "s_sub_u32 s76, s76, s71\n"
      "s_add_u32 s71, s71, s76\n"
      "v_add_u32 v20, s74, v20\n"
      "v_add_u32 v21, s74, v21\n"
      "v_add_u32 v22, s74, v22\n"
      "v_add_u32 v23, s74, v23\n"
      "v_add_u32 v25, s74, v25\n"
      "s_cmp_eq_u32 s75, 0\n"
      "s_cbranch_scc1 L_ax_25_%=\n"
      "v_bfe_i32 v118, v27, 10, 1\n"
      "v_and_b32 v118, s75, v118\n"
      "v_sub_u32 v20, v20, v118\n"
      "v_bfe_i32 v118, v27, 12, 1\n"
      "v_and_b32 v118, s75, v118\n"
      "v_sub_u32 v21, v21, v118\n"
      "v_bfe_i32 v118, v27, 14, 1\n"
      "v_and_b32 v118, s75, v118\n"
      "v_sub_u32 v22, v22, v118\n"
      "v_bfe_i32 v118, v27, 16, 1\n"
      "v_and_b32 v118, s75, v118\n"
      "v_sub_u32 v23, v23, v118\n"
      "v_bfe_i32 v118, v27, 18, 1\n"
      "v_and_b32 v118, s75, v118\n"
      "v_sub_u32 v25, v25, v118\n"
      "L_ax_25_%=:\n"
      "s_cmp_eq_u32 s76, 0\n"
      "s_cbranch_scc1 L_ax_26_%=\n"
      "v_bfe_i32 v118, v27, 11, 1\n"
      "v_and_b32 v118, s76, v118\n"
      "v_sub_u32_e64 v20, v20, v118\n"
      "v_bfe_i32 v118, v27, 13, 1\n"
      "v_and_b32 v118, s76, v118\n"
      "v_sub_u32 v21, v21, v118\n"
      "v_bfe_i32 v118, v27, 15, 1\n"
      "v_and_b32 v118, s76, v118\n"
      "v_sub_u32 v22, v22, v118\n"
      "v_bfe_i32 v118, v27, 17, 1\n"
      "v_and_b32 v118, s76, v118\n"
      "v_sub_u32 v23, v23, v118\n"
      "v_bfe_i32 v118, v27, 19, 1\n"
      "v_and_b32 v118, s76, v118\n"
      "v_sub_u32 v25, v25, v118\n"
      "L_ax_26_%=:\n"
      "s_or_b32 s48, s66, s69\n"
      "s_cmp_lg_u32 s48, 0\n"
      "s_cselect_b32 s48, 0x40000000, 0\n"
      "s_waitcnt lgkmcnt(0)\n"
      "s_nop 4\n"
      "s_branch L_back_28_%=\n"
      "L_rare_31_%=:\n"
      "s_nop 7\n"
      "s_waitcnt lgkmcnt(0)\n"
      "s_bitcmp1_b32 s46, 31\n"
      "s_cselect_b32 s75, 100270464, 0\n"
      "s_bitcmp1_b32 s46, 30\n"
      "s_cselect_b32 s76, 196224, 0\n"
      "s_and_b32 s74, s46, 0x3fffffff\n"
      "s_cmp_ge_u32 s46, 0x40000000\n"
      "s_cselect_b32 s74, s74, 0\n"
      "s_cselect_b32 s46, 0, s46\n"
      "s_sub_u32 s74, s74, s69\n"
      "s_add_u32 s69, s69, s74\n"
      "s_sub_u32 s75, s75, s70\n"
      "s_add_u32 s70, s70, s75\n"
      "s_sub_u32 s76, s76, s71\n"
      "s_add_u32 s71, s71, s76\n"
      "v_add_u32 v20, s74, v20\n"
      "v_add_u32 v21, s74, v21\n"
      "v_add_u32 v22, s74, v22\n"
      "v_add_u32 v23, s74, v23\n"
      "v_add_u32 v25, s74, v25\n"
      "s_cmp_eq_u32 s75, 0\n"
      "s_cbranch_scc1 L_ax_29_%=\n"
      "v_bfe_i32 v141, v27, 10, 1\n"
      "v_and_b32 v141, s75, v141\n"
      "v_sub_u32 v20, v20, v141\n"
      "v_bfe_i32 v141, v27, 12, 1\n"
      "v_and_b32 v141, s75, v141\n"
      "v_sub_u32 v21, v21, v141\n"
      "v_bfe_i32 v141, v27, 14, 1\n"
      "v_and_b32 v141, s75, v141\n"
      "v_sub_u32 v22, v22, v141\n"
      "v_bfe_i32 v141, v27, 16, 1\n"
      "v_and_b32 v141, s75, v141\n"
      "v_sub_u32 v23, v23, v141\n"
      "v_bfe_i32 v141, v27, 18, 1\n"
      "v_and_b32 v141, s75, v141\n"
      "v_sub_u32 v25, v25, v141\n"
      "L_ax_29_%=:\n"
      "s_cmp_eq_u32 s76, 0\n"
      "s_cbranch_scc1 L_ax_30_%=\n"
      "v_bfe_i32 v141, v27, 11, 1\n"
      "v_and_b32 v141, s76, v141\n"
      "v_sub_u32 v20, v20, v141\n"
      "v_bfe_i32 v141, v27, 13, 1\n"
      "v_and_b32 v141, s76, v141\n"
      "v_sub_u32 v21, v21, v141\n"
      "v_bfe_i32 v141, v27, 15, 1\n"
      "v_and_b32 v141, s76, v141\n"
      "v_sub_u32 v22, v22, v141\n"
      "v_bfe_i32 v141, v27, 17, 1\n"
      "v_and_b32 v141, s76, v141\n"
      "v_sub_u32 v23, v23, v141\n"
      "v_bfe_i32 v141, v27, 19, 1\n"
      "v_and_b32 v141, s76, v141\n"
      "v_sub_u32 v25, v25, v141\n"
      "L_ax_30_%=:\n"
      "s_or_b32 s48, s66, s69\n"
      "s_cmp_lg_u32 s48, 0\n"
      "s_cselect_b32 s48, 0x40000000, 0\n"
      "s_waitcnt lgkmcnt(0)\n"
      "s_nop 4\n"
      "s_branch L_back_32_%=\n"
      "L_rare_35_%=:\n"
      "s_nop 7\n"
      "s_waitcnt lgkmcnt(0)\n"
      "s_bitcmp1_b32 s46, 31\n"
      "s_cselect_b32 s75, 100270464, 0\n"
      "s_bitcmp1_b32 s46, 30\n"
      "s_cselect_b32 s76, 196224, 0\n"
      "s_and_b32 s74, s46, 0x3fffffff\n"
      "s_cmp_ge_u32 s46, 0x40000000\n"
      "s_cselect_b32 s74, s74, 0\n"
      "s_cselect_b32 s46, 0, s46\n"
      "s_sub_u32 s74, s74, s69\n"
      "s_add_u32 s69, s69, s74\n"
      "s_sub_u32 s75, s75, s70\n"
      "s_add_u32 s70, s70, s75\n"
      "s_sub_u32 s76, s76, s71\n"
      "s_add_u32 s71, s71, s76\n"
      "v_add_u32 v20, s74, v20\n"
      "v_add_u32 v21, s74, v21\n"
      "v_add_u32 v22, s74, v22\n"
      "v_add_u32 v23, s74, v23\n"
      "v_add_u32 v25, s74, v25\n"
      "s_cmp_eq_u32 s75, 0\n"
      "s_cbranch_scc1 L_ax_33_%=\n"
      "v_bfe_i32 v210, v27, 10, 1\n"
      "v_and_b32 v210, s75, v210\n"
      "v_sub_u32 v20, v20, v210\n"
      "v_bfe_i32 v210, v27, 12, 1\n"
      "v_and_b32 v210, s75, v210\n"
      "v_sub_u32 v21, v21, v210\n"
      "v_bfe_i32 v210, v27, 14, 1\n"
      "v_and_b32 v210, s75, v210\n"
      "v_sub_u32 v22, v22, v210\n"
      "v_bfe_i32 v210, v27, 16, 1\n"
      "v_and_b32 v210, s75, v210\n"
      "v_sub_u32 v23, v23, v210\n"
      "v_bfe_i32 v210, v27, 18, 1\n"
      "v_and_b32 v210, s75, v210\n"
      "v_sub_u32 v25, v25, v210\n"
      "L_ax_33_%=:\n"
      "s_cmp_eq_u32 s76, 0\n"
      "s_cbranch_scc1 L_ax_34_%=\n"
      "v_bfe_i32 v210, v27, 11, 1\n"
      "v_and_b32 v210, s76, v210\n"
      "v_sub_u32 v20, v20, v210\n"
      "v_bfe_i32 v210, v27, 13, 1\n"
      "v_and_b32 v210, s76, v210\n"
      "v_sub_u32 v21, v21, v210\n"
      "v_bfe_i32 v210, v27, 15, 1\n"
      "v_and_b32 v210, s76, v210\n"
      "v_sub_u32 v22, v22, v210\n"
      "v_bfe_i32 v210, v27, 17, 1\n"
      "v_and_b32 v210, s76, v210\n"
      "v_sub_u32 v23, v23, v210\n"
      "v_bfe_i32 v210, v27, 19, 1\n"
      "v_and_b32 v210, s76, v210\n"
      "v_sub_u32 v25, v25, v210\n"
      "L_ax_34_%=:\n"
      "s_or_b32 s48, s66, s69\n"
      "s_cmp_lg_u32 s48, 0\n"
      "s_cselect_b32 s48, 0x40000000, 0\n"
      "s_waitcnt lgkmcnt(0)\n"
      "s_nop 4\n"
      "s_branch L_back_36_%=\n"
      "L_rare_39_%=:\n"
      "s_nop 7\n"
      "s_waitcnt lgkmcnt(0)\n"
      "s_bitcmp1_b32 s46, 31\n"
      "s_cselect_b32 s75, 100270464, 0\n"
      "s_bitcmp1_b32 s46, 30\n"
      "s_cselect_b32 s76, 196224, 0\n"
      "s_and_b32 s74, s46, 0x3fffffff\n"
      "s_cmp_ge_u32 s46, 0x40000000\n"
      "s_cselect_b32 s74, s74, 0\n"
      "s_cselect_b32 s46, 0, s46\n"
      "s_sub_u32 s74, s74, s69\n"
      "s_add_u32 s69, s69, s74\n"
      "s_sub_u32 s75, s75, s70\n"
      "s_add_u32 s70, s70, s75\n"
      "s_sub_u32 s76, s76, s71\n"
      "s_add_u32 s71, s71, s76\n"
      "v_add_u32 v20, s74, v20\n"
      "v_add_u32 v21, s74, v21\n"
      "v_add_u32 v22, s74, v22\n"
      "v_add_u32 v23, s74, v23\n"
      "v_add_u32 v25, s74, v25\n"
      "s_cmp_eq_u32 s75, 0\n"
      "s_cbranch_scc1 L_ax_37_%=\n"
      "v_bfe_i32 v181, v27, 10, 1\n"
      "v_and_b32 v181, s75, v181\n"
      "v_sub_u32 v20, v20, v181\n"
      "v_bfe_i32 v181, v27, 12, 1\n"
      "v_and_b32 v181, s75, v181\n"
      "v_sub_u32 v21, v21, v181\n"
      "v_bfe_i32 v181, v27, 14, 1\n"
      "v_and_b32 v181, s75, v181\n"
      "v_sub_u32 v22, v22, v181\n"
      "v_bfe_i32 v181, v27, 16, 1\n"
      "v_and_b32 v181, s75, v181\n"
      "v_sub_u32 v23, v23, v181\n"
      "v_bfe_i32 v181, v27, 18, 1\n"
      "v_and_b32 v181, s75, v181\n"
      "v_sub_u32 v25, v25, v181\n"
      "L_ax_37_%=:\n"
      "s_cmp_eq_u32 s76, 0\n"
      "s_cbranch_scc1 L_ax_38_%=\n"
      "v_bfe_i32 v181, v27, 11, 1\n"
      "v_and_b32 v181, s76, v181\n"
      "v_sub_u32 v20, v20, v181\n"
      "v_bfe_i32 v181, v27, 13, 1\n"
      "v_and_b32 v181, s76, v181\n"
      "v_sub_u32 v21, v21, v181\n"
      "v_bfe_i32 v181, v27, 15, 1\n"
      "v_and_b32 v181, s76, v181\n"
      "v_sub_u32 v22, v22, v181\n"
      "v_bfe_i32 v181, v27, 17, 1\n"
      "v_and_b32 v181, s76, v181\n"
      "v_sub_u32 v23, v23, v181\n"
      "v_bfe_i32 v181, v27, 19, 1\n"
      "v_and_b32 v181, s76, v181\n"
      "v_sub_u32 v25, v25, v181\n"
      "L_ax_38_%=:\n"
      "s_or_b32 s48, s66, s69\n"
      "s_cmp_lg_u32 s48, 0\n"
      "s_cselect_b32 s48, 0x40000000, 0\n"
      "s_waitcnt lgkmcnt(0)\n"
      "s_nop 4\n"
      "s_branch L_back_40_%=\n"
      "L_cend_%=:\n"
      :
      : "s"(__builtin_amdgcn_kernarg_segment_ptr()), "s"(wave), "s"(blockIdx.x), "s"(CT_TAB), "s"(lds0)
      : "v0","v1","v2","v3","v4","v5","v6","v7","v8","v9","v10","v11","v12","v13","v14","v15","v16","v17","v18","v19","v20","v21","v22","v23","v24","v25","v26","v27","v28","v29","v30","v31","v32","v33","v34","v35","v36","v37","v38","v39","v40","v41","v42","v43","v44","v45","v46","v47","v48","v49","v50","v51","v52","v53","v54","v55","v56","v57","v58","v59","v60","v61","v62","v63","v64","v65","v66","v67","v68","v69","v70","v71","v72","v73","v74","v75","v76","v77","v78","v79","v80","v81","v82","v83","v84","v85","v86","v87","v88","v89","v90","v91","v92","v93","v94","v95","v96","v97","v98","v99","v100","v101","v102","v103","v104","v105","v106","v107","v108","v109","v110","v111","v112","v113","v114","v115","v116","v117","v118","v119","v120","v121","v122","v123","v124","v125","v126","v127","v128","v129","v130","v131","v132","v133","v134","v135","v136","v137","v138","v139","v140","v141","v142","v143","v144","v145","v146","v147","v148","v149","v150","v151","v152","v153","v154","v155","v156","v157","v158","v159","v160","v161","v162","v163","v164","v165","v166","v167","v168","v169","v170","v171","v172","v173","v174","v175","v176","v177","v178","v179","v180","v181","v182","v183","v184","v185","v186","v187","v188","v189","v190","v191","v192","v193","v194","v195","v196","v197","v198","v199","v200","v201","v202","v203","v204","v205","v206","v207","v208","v209","v210","v211","v212","v213","v214","v215","v216","v217","v218","v219","v220","v221","v222","v223","v224","v225","v226","v227","v228","v229","v230","v231","v232","v233","v234","v235","v236","v237","v238","v239","v240","v241","v242","v243","v244","v245","v246","v247","v248","v249","v250","v251","v252","v253","v254","v255","a0","a1","a2","a3","a4","a5","a6","a7","a8","a9","a10","a11","a12","a13","a14","a15","a16","a17","a18","a19","a20","a21","a22","a23","a24","a25","a26","a27","a28","a29","a30","a31","a32","a33","a34","a35","a36","a37","a38","a39","a40","a41","a42","a43","a44","a45","a46","a47","a48","a49","a50","a51","a52","a53","a54","a55","a56","a57","a58","a59","a60","a61","a62","a63","a64","a65","a66","a67","a68","a69","a70","a71","a72","a73","a74","a75","a76","a77","a78","a79","a80","a81","a82","a83","a84","a85","a86","a87","a88","a89","a90","a91","a92","a93","a94","a95","a96","a97","a98","a99","a100","a101","a102","a103","a104","a105","a106","a107","a108","a109","a110","a111","a112","a113","a114","a115","a116","a117","a118","a119","a120","a121","a122","a123","a124","a125","a126","a127","a128","a129","a130","a131","a132","a133","a134","a135","a136","a137","a138","a139","a140","a141","a142","a143","a144","a145","a146","a147","a148","a149","a150","a151","a152","a153","a154","a155","a156","a157","a158","a159","a160","a161","a162","a163","a164","a165","a166","a167","a168","a169","a170","a171","a172","a173","a174","a175","a176","a177","a178","a179","a180","a181","a182","a183","a184","a185","a186","a187","a188","a189","a190","a191","a192","a193","a194","a195","a196","a197","a198","a199","a200","a201","a202","a203","a204","a205","a206","a207","a208","a209","a210","a211","a212","a213","a214","a215","a216","a217","a218","a219","a220","a221","a222","a223","a224","a225","a226","a227","a228","a229","a230","a231","a232","a233","a234","a235","a236","a237","a238","a239","a240","a241","a242","a243","a244","a245","a246","a247","a248","a249","a250","a251","a252","a253","a254","a255","s16","s17","s18","s19","s20","s21","s22","s23","s24","s25","s26","s27","s28","s29","s30","s31","s33","s34","s35","s36","s37","s38","s39","s40","s41","s42","s43","s44","s45","s46","s47","s48","s49","s50","s51","s52","s53","s54","s55","s56","s57","s58","s59","s60","s61","s62","s63","s64","s65","s66","s67","s68","s69","s70","s71","s72","s73","s74","s75","s76","s77","s78","s79","s80","s81","s82","s83","s84","s85","s86","s87","s88","s89","s90","s91","s92","s93","s94","s95","s96","s97","s98","s99","s100","s101", "vcc", "scc", "memory");
}

}

extern "C" void kernel_launch(void* const* d_in, const int* in_sizes, int n_in, void* d_out, int out_size, void* d_ws,
                              size_t ws_size, hipStream_t stream) {
  const uint16_t* x = (const uint16_t*)d_in[0];
  const uint16_t* wqkv = (const uint16_t*)d_in[1];
  const uint16_t* wo = (const uint16_t*)d_in[2];
  const uint16_t* ls = (const uint16_t*)d_in[3];
  const uint16_t* w1 = (const uint16_t*)d_in[4];
  const uint16_t* w2 = (const uint16_t*)d_in[5];
  const uint16_t* rc = (const uint16_t*)d_in[6];
  const int* ridx = (const int*)d_in[7];
  const uint16_t* mask = (const uint16_t*)d_in[8];
  swin::swin_main<<<256, 256, 0, stream>>>(x, mask, (char*)d_ws, (uint16_t*)d_out, wqkv, wo, ls, w1, w2, rc, ridx);
}
```

```cpp
#include <hip/hip_runtime.h>
#include <hip/hip_bf16.h>
#include <stdint.h>

namespace swin {

constexpr unsigned CT_WORDS = 8192u;
constexpr unsigned LDS_BYTES = 163840u;

__device__ __attribute__((aligned(256))) const unsigned CT_TAB[CT_WORDS] = {0x4800u,0x4820u,0x4840u,0x4800u,0x4808u,0x4828u,0x4848u,0x4810u,0x4810u,0x4830u,0x4850u,0x4820u,0x4818u,0x4838u,0x4858u,0x4830u,0x4880u,0x48a0u,0x48c0u,0x4880u,0x4888u,0x48a8u,0x48c8u,0x4890u,0x4890u,0x48b0u,0x48d0u,0x48a0u,0x4898u,0x48b8u,0x48d8u,0x48b0u,0x4920u,0x4900u,0x4960u,0x4920u,0x4928u,0x4908u,0x4968u,0x4930u,0x4930u,0x4910u,0x4970u,0x4900u,0x4938u,0x4918u,0x4978u,0x4910u,0x49a0u,0x4980u,0x49e0u,0x49a0u,0x49a8u,0x4988u,0x49e8u,0x49b0u,0x49b0u,0x4990u,0x49f0u,0x4980u,0x49b8u,0x4998u,0x49f8u,0x4990u,0x4c40u,0x4c60u,0x4c00u,0x4a00u,0x4c48u,0x4c68u,0x4c08u,0x4a10u,0x4c50u,0x4c70u,0x4c10u,0x4a20u,0x4c58u,0x4c78u,0x4c18u,0x4a30u,0x4cc0u,0x4ce0u,0x4c80u,0x4a80u,0x4cc8u,0x4ce8u,0x4c88u,0x4a90u,0x4cd0u,0x4cf0u,0x4c90u,0x4aa0u,0x4cd8u,0x4cf8u,0x4c98u,0x4ab0u,0x4d60u,0x4d40u,0x4d20u,0x4b20u,0x4d68u,0x4d48u,0x4d28u,0x4b30u,0x4d70u,0x4d50u,0x4d30u,0x4b00u,0x4d78u,0x4d58u,0x4d38u,0x4b10u,0x4de0u,0x4dc0u,0x4da0u,0x4ba0u,0x4de8u,0x4dc8u,0x4da8u,0x4bb0u,0x4df0u,0x4dd0u,0x4db0u,0x4b80u,0x4df8u,0x4dd8u,0x4db8u,0x4b90u,0x5000u,0x5020u,0x5040u,0x4c40u,0x5008u,0x5028u,0x5048u,0x4c50u,0x5010u,0x5030u,0x5050u,0x4c60u,0x5018u,0x5038u,0x5058u,0x4c70u,0x5080u,0x50a0u,0x50c0u,0x4cc0u,0x5088u,0x50a8u,0x50c8u,0x4cd0u,0x5090u,0x50b0u,0x50d0u,0x4ce0u,0x5098u,0x50b8u,0x50d8u,0x4cf0u,0x5120u,0x5100u,0x5160u,0x4d60u,0x5128u,0x5108u,0x5168u,0x4d70u,0x5130u,0x5110u,0x5170u,0x4d40u,0x5138u,0x5118u,0x5178u,0x4d50u,0x51a0u,0x5180u,0x51e0u,0x4de0u,0x51a8u,0x5188u,0x51e8u,0x4df0u,0x51b0u,0x5190u,0x51f0u,0x4dc0u,0x51b8u,0x5198u,0x51f8u,0x4dd0u,0x5440u,0x5460u,0x5400u,0x4e40u,0x5448u,0x5468u,0x5408u,0x4e50u,0x5450u,0x5470u,0x5410u,0x4e60u,0x5458u,0x5478u,0x5418u,0x4e70u,0x54c0u,0x54e0u,0x5480u,0x4ec0u,0x54c8u,0x54e8u,0x5488u,0x4ed0u,0x54d0u,0x54f0u,0x5490u,0x4ee0u,0x54d8u,0x54f8u,0x5498u,0x4ef0u,0x5560u,0x5540u,0x5520u,0x4f60u,0x5568u,0x5548u,0x5528u,0x4f70u,0x5570u,0x5550u,0x5530u,0x4f40u,0x5578u,0x5558u,0x5538u,0x4f50u,0x55e0u,0x55c0u,0x55a0u,0x4fe0u,0x55e8u,0x55c8u,0x55a8u,0x4ff0u,0x55f0u,0x55d0u,0x55b0u,0x4fc0u,0x55f8u,0x55d8u,0x55b8u,0x4fd0u,0x4840u,0x1e800u,0x1eb00u,0x0u,0x4848u,0x1ec90u,0x1ef90u,0x10u,0x4850u,0x1f120u,0x1f420u,0x20u,0x4858u,0x1f5b0u,0x1f8b0u,0x30u,0x48c0u,0x1fa40u,0x1fd40u,0x40u,0x48c8u,0x1fed0u,0x201d0u,0x50u,0x48d0u,0x20360u,0x20660u,0x60u,0x48d8u,0x207f0u,0x20af0u,0x70u,0x4960u,0x20c80u,0x20f80u,0x180u,0x4968u,0x21110u,0x21410u,0x190u,0x4970u,0x215a0u,0x218a0u,0x1a0u,0x4978u,0x21a30u,0x21d30u,0x1b0u,0x49e0u,0x21ec0u,0x221c0u,0x1c0u,0x49e8u,0x22350u,0x22650u,0x1d0u,0x49f0u,0x227e0u,0x22ae0u,0x1e0u,0x49f8u,0x22c70u,0x22f70u,0x1f0u,0x4a40u,0x1e830u,0x1eb08u,0x320u,0x4a48u,0x1ecc0u,0x1ef98u,0x330u,0x4a50u,0x1f150u,0x1f428u,0x300u,0x4a58u,0x1f5e0u,0x1f8b8u,0x310u,0x4ac0u,0x1fa70u,0x1fd48u,0x360u,0x4ac8u,0x1ff00u,0x201d8u,0x370u,0x4ad0u,0x20390u,0x20668u,0x340u,0x4ad8u,0x20820u,0x20af8u,0x350u,0x4b60u,0x20cb0u,0x20f88u,0x4a0u,0x4b68u,0x21140u,0x21418u,0x4b0u,0x4b70u,0x215d0u,0x218a8u,0x480u,0x4b78u,0x21a60u,0x21d38u,0x490u,0x4be0u,0x21ef0u,0x221c8u,0x4e0u,0x4be8u,0x22380u,0x22658u,0x4f0u,0x4bf0u,0x22810u,0x22ae8u,0x4c0u,0x4bf8u,0x22ca0u,0x22f78u,0x4d0u,0x4c00u,0x1e860u,0x1eb10u,0x600u,0x4c08u,0x1ecf0u,0x1efa0u,0x610u,0x4c10u,0x1f180u,0x1f430u,0x620u,0x4c18u,0x1f610u,0x1f8c0u,0x630u,0x4c80u,0x1faa0u,0x1fd50u,0x640u,0x4c88u,0x1ff30u,0x201e0u,0x650u,0x4c90u,0x203c0u,0x20670u,0x660u,0x4c98u,0x20850u,0x20b00u,0x670u,0x4d20u,0x20ce0u,0x20f90u,0x780u,0x4d28u,0x21170u,0x21420u,0x790u,0x4d30u,0x21600u,0x218b0u,0x7a0u,0x4d38u,0x21a90u,0x21d40u,0x7b0u,0x4da0u,0x21f20u,0x221d0u,0x7c0u,0x4da8u,0x223b0u,0x22660u,0x7d0u,0x4db0u,0x22840u,0x22af0u,0x7e0u,0x4db8u,0x22cd0u,0x22f80u,0x7f0u,0x4e00u,0x1e890u,0x1eb18u,0x920u,0x4e08u,0x1ed20u,0x1efa8u,0x930u,0x4e10u,0x1f1b0u,0x1f438u,0x900u,0x4e18u,0x1f640u,0x1f8c8u,0x910u,0x4e80u,0x1fad0u,0x1fd58u,0x960u,0x4e88u,0x1ff60u,0x201e8u,0x970u,0x4e90u,0x203f0u,0x20678u,0x940u,0x4e98u,0x20880u,0x20b08u,0x950u,0x4f20u,0x20d10u,0x20f98u,0xaa0u,0x4f28u,0x211a0u,0x21428u,0xab0u,0x4f30u,0x21630u,0x218b8u,0xa80u,0x4f38u,0x21ac0u,0x21d48u,0xa90u,0x4fa0u,0x21f50u,0x221d8u,0xae0u,0x4fa8u,0x223e0u,0x22668u,0xaf0u,0x4fb0u,0x22870u,0x22af8u,0xac0u,0x4fb8u,0x22d00u,0x22f88u,0xad0u,0x0u,0x40u,0x9000u,0x9000u,0x80u,0xc0u,0x9010u,0x9010u,0x110u,0x150u,0x9020u,0x9020u,0x190u,0x1d0u,0x9030u,0x9030u,0x220u,0x260u,0x9040u,0x9040u,0x2a0u,0x2e0u,0x9050u,0x9050u,0x330u,0x370u,0x9060u,0x9060u,0x3b0u,0x3f0u,0x9070u,0x9070u,0x440u,0x400u,0x9080u,0x9080u,0x4c0u,0x480u,0x9090u,0x9090u,0x550u,0x510u,0x90a0u,0x90a0u,0x5d0u,0x590u,0x90b0u,0x90b0u,0x660u,0x620u,0x90c0u,0x90c0u,0x6e0u,0x6a0u,0x90d0u,0x90d0u,0x770u,0x730u,0x90e0u,0x90e0u,0x7f0u,0x7b0u,0x90f0u,0x90f0u,0x10u,0x50u,0x9300u,0x9300u,0x90u,0xd0u,0x9310u,0x9310u,0x100u,0x140u,0x9320u,0x9320u,0x180u,0x1c0u,0x9330u,0x9330u,0x230u,0x270u,0x9340u,0x9340u,0x2b0u,0x2f0u,0x9350u,0x9350u,0x320u,0x360u,0x9360u,0x9360u,0x3a0u,0x3e0u,0x9370u,0x9370u,0x450u,0x410u,0x9380u,0x9380u,0x4d0u,0x490u,0x9390u,0x9390u,0x540u,0x500u,0x93a0u,0x93a0u,0x5c0u,0x580u,0x93b0u,0x93b0u,0x670u,0x630u,0x93c0u,0x93c0u,0x6f0u,0x6b0u,0x93d0u,0x93d0u,0x760u,0x720u,0x93e0u,0x93e0u,0x7e0u,0x7a0u,0x93f0u,0x93f0u,0x20u,0x60u,0x9600u,0x9600u,0xa0u,0xe0u,0x9610u,0x9610u,0x130u,0x170u,0x9620u,0x9620u,0x1b0u,0x1f0u,0x9630u,0x9630u,0x200u,0x240u,0x9640u,0x9640u,0x280u,0x2c0u,0x9650u,0x9650u,0x310u,0x350u,0x9660u,0x9660u,0x390u,0x3d0u,0x9670u,0x9670u,0x460u,0x420u,0x9680u,0x9680u,0x4e0u,0x4a0u,0x9690u,0x9690u,0x570u,0x530u,0x96a0u,0x96a0u,0x5f0u,0x5b0u,0x96b0u,0x96b0u,0x640u,0x600u,0x96c0u,0x96c0u,0x6c0u,0x680u,0x96d0u,0x96d0u,0x750u,0x710u,0x96e0u,0x96e0u,0x7d0u,0x790u,0x96f0u,0x96f0u,0x30u,0x70u,0x9900u,0x9900u,0xb0u,0xf0u,0x9910u,0x9910u,0x120u,0x160u,0x9920u,0x9920u,0x1a0u,0x1e0u,0x9930u,0x9930u,0x210u,0x250u,0x9940u,0x9940u,0x290u,0x2d0u,0x9950u,0x9950u,0x300u,0x340u,0x9960u,0x9960u,0x380u,0x3c0u,0x9970u,0x9970u,0x470u,0x430u,0x9980u,0x9980u,0x4f0u,0x4b0u,0x9990u,0x9990u,0x560u,0x520u,0x99a0u,0x99a0u,0x5e0u,0x5a0u,0x99b0u,0x99b0u,0x650u,0x610u,0x99c0u,0x99c0u,0x6d0u,0x690u,0x99d0u,0x99d0u,0x740u,0x700u,0x99e0u,0x99e0u,0x7c0u,0x780u,0x99f0u,0x99f0u,0x9c00u,0x12000u,0x12040u,0x12000u,0x9c10u,0x12190u,0x121d0u,0x12010u,0x9c20u,0x12320u,0x12360u,0x12020u,0x9c30u,0x124b0u,0x124f0u,0x12030u,0x9c40u,0x12640u,0x12600u,0x12040u,0x9c50u,0x127d0u,0x12790u,0x12050u,0x9c60u,0x12960u,0x12920u,0x12060u,0x9c70u,0x12af0u,0x12ab0u,0x12070u,0x9c80u,0x12c00u,0x12c40u,0x12080u,0x9c90u,0x12d90u,0x12dd0u,0x12090u,0x9ca0u,0x12f20u,0x12f60u,0x120a0u,0x9cb0u,0x130b0u,0x130f0u,0x120b0u,0x9cc0u,0x13240u,0x13200u,0x120c0u,0x9cd0u,0x133d0u,0x13390u,0x120d0u,0x9ce0u,0x13560u,0x13520u,0x120e0u,0x9cf0u,0x136f0u,0x136b0u,0x120f0u,0x9f00u,0x12010u,0x12048u,0x12100u,0x9f10u,0x12180u,0x121d8u,0x12110u,0x9f20u,0x12330u,0x12368u,0x12120u,0x9f30u,0x124a0u,0x124f8u,0x12130u,0x9f40u,0x12650u,0x12608u,0x12140u,0x9f50u,0x127c0u,0x12798u,0x12150u,0x9f60u,0x12970u,0x12928u,0x12160u,0x9f70u,0x12ae0u,0x12ab8u,0x12170u,0x9f80u,0x12c10u,0x12c48u,0x12180u,0x9f90u,0x12d80u,0x12dd8u,0x12190u,0x9fa0u,0x12f30u,0x12f68u,0x121a0u,0x9fb0u,0x130a0u,0x130f8u,0x121b0u,0x9fc0u,0x13250u,0x13208u,0x121c0u,0x9fd0u,0x133c0u,0x13398u,0x121d0u,0x9fe0u,0x13570u,0x13528u,0x121e0u,0x9ff0u,0x136e0u,0x136b8u,0x121f0u,0xa200u,0x12020u,0x12050u,0x12200u,0xa210u,0x121b0u,0x121c0u,0x12210u,0xa220u,0x12300u,0x12370u,0x12220u,0xa230u,0x12490u,0x124e0u,0x12230u,0xa240u,0x12660u,0x12610u,0x12240u,0xa250u,0x127f0u,0x12780u,0x12250u,0xa260u,0x12940u,0x12930u,0x12260u,0xa270u,0x12ad0u,0x12aa0u,0x12270u,0xa280u,0x12c20u,0x12c50u,0x12280u,0xa290u,0x12db0u,0x12dc0u,0x12290u,0xa2a0u,0x12f00u,0x12f70u,0x122a0u,0xa2b0u,0x13090u,0x130e0u,0x122b0u,0xa2c0u,0x13260u,0x13210u,0x122c0u,0xa2d0u,0x133f0u,0x13380u,0x122d0u,0xa2e0u,0x13540u,0x13530u,0x122e0u,0xa2f0u,0x136d0u,0x136a0u,0x122f0u,0xa500u,0x12030u,0x12058u,0x12300u,0xa510u,0x121a0u,0x121c8u,0x12310u,0xa520u,0x12310u,0x12378u,0x12320u,0xa530u,0x12480u,0x124e8u,0x12330u,0xa540u,0x12670u,0x12618u,0x12340u,0xa550u,0x127e0u,0x12788u,0x12350u,0xa560u,0x12950u,0x12938u,0x12360u,0xa570u,0x12ac0u,0x12aa8u,0x12370u,0xa580u,0x12c30u,0x12c58u,0x12380u,0xa590u,0x12da0u,0x12dc8u,0x12390u,0xa5a0u,0x12f10u,0x12f78u,0x123a0u,0xa5b0u,0x13080u,0x130e8u,0x123b0u,0xa5c0u,0x13270u,0x13218u,0x123c0u,0xa5d0u,0x133e0u,0x13388u,0x123d0u,0xa5e0u,0x13550u,0x13538u,0x123e0u,0xa5f0u,0x136c0u,0x136a8u,0x123f0u,0x4800u,0x1c300u,0x1e800u,0x1eb00u,0x4880u,0x1c310u,0x100000u,0x1eb02u,0x4910u,0x1c320u,0x100000u,0x1eb04u,0x4990u,0x1c330u,0x100000u,0x1eb06u,0x4a20u,0x1c340u,0x1e800u,0x1eb08u,0x4aa0u,0x1c350u,0x100000u,0x1eb0au,0x4b30u,0x1c360u,0x100000u,0x1eb0cu,0x4bb0u,0x1c370u,0x100000u,0x1eb0eu,0x4c40u,0x1c380u,0x1e800u,0x1eb10u,0x4cc0u,0x1c390u,0x100000u,0x1eb12u,0x4d50u,0x1c3a0u,0x100000u,0x1eb14u,0x4dd0u,0x1c3b0u,0x100000u,0x1eb16u,0x4e60u,0x1c3c0u,0x1e800u,0x1eb18u,0x4ee0u,0x1c3d0u,0x100000u,0x1eb1au,0x4f70u,0x1c3e0u,0x100000u,0x1eb1cu,0x4ff0u,0x1c3f0u,0x100000u,0x1eb1eu,0x4810u,0x1c400u,0x1e830u,0x1eb00u,0x4890u,0x1c410u,0x100000u,0x1eb02u,0x4900u,0x1c420u,0x100000u,0x1eb04u,0x4980u,0x1c430u,0x100000u,0x1eb06u,0x4a30u,0x1c440u,0x1e830u,0x1eb08u,0x4ab0u,0x1c450u,0x100000u,0x1eb0au,0x4b20u,0x1c460u,0x100000u,0x1eb0cu,0x4ba0u,0x1c470u,0x100000u,0x1eb0eu,0x4c50u,0x1c480u,0x1e830u,0x1eb10u,0x4cd0u,0x1c490u,0x100000u,0x1eb12u,0x4d40u,0x1c4a0u,0x100000u,0x1eb14u,0x4dc0u,0x1c4b0u,0x100000u,0x1eb16u,0x4e70u,0x1c4c0u,0x1e830u,0x1eb18u,0x4ef0u,0x1c4d0u,0x100000u,0x1eb1au,0x4f60u,0x1c4e0u,0x100000u,0x1eb1cu,0x4fe0u,0x1c4f0u,0x100000u,0x1eb1eu,0x4820u,0x1c500u,0x1e860u,0x1eb00u,0x48a0u,0x1c510u,0x100000u,0x1eb02u,0x4930u,0x1c520u,0x100000u,0x1eb04u,0x49b0u,0x1c530u,0x100000u,0x1eb06u,0x4a00u,0x1c540u,0x1e860u,0x1eb08u,0x4a80u,0x1c550u,0x100000u,0x1eb0au,0x4b10u,0x1c560u,0x100000u,0x1eb0cu,0x4b90u,0x1c570u,0x100000u,0x1eb0eu,0x4c60u,0x1c580u,0x1e860u,0x1eb10u,0x4ce0u,0x1c590u,0x100000u,0x1eb12u,0x4d70u,0x1c5a0u,0x100000u,0x1eb14u,0x4df0u,0x1c5b0u,0x100000u,0x1eb16u,0x4e40u,0x1c5c0u,0x1e860u,0x1eb18u,0x4ec0u,0x1c5d0u,0x100000u,0x1eb1au,0x4f50u,0x1c5e0u,0x100000u,0x1eb1cu,0x4fd0u,0x1c5f0u,0x100000u,0x1eb1eu,0x4830u,0x1c600u,0x1e890u,0x1eb00u,0x48b0u,0x1c610u,0x100000u,0x1eb02u,0x4920u,0x1c620u,0x100000u,0x1eb04u,0x49a0u,0x1c630u,0x100000u,0x1eb06u,0x4a10u,0x1c640u,0x1e890u,0x1eb08u,0x4a90u,0x1c650u,0x100000u,0x1eb0au,0x4b00u,0x1c660u,0x100000u,0x1eb0cu,0x4b80u,0x1c670u,0x100000u,0x1eb0eu,0x4c70u,0x1c680u,0x1e890u,0x1eb10u,0x4cf0u,0x1c690u,0x100000u,0x1eb12u,0x4d60u,0x1c6a0u,0x100000u,0x1eb14u,0x4de0u,0x1c6b0u,0x100000u,0x1eb16u,0x4e50u,0x1c6c0u,0x1e890u,0x1eb18u,0x4ed0u,0x1c6d0u,0x100000u,0x1eb1au,0x4f40u,0x1c6e0u,0x100000u,0x1eb1cu,0x4fc0u,0x1c6f0u,0x100000u,0x1eb1eu,0x24800u,0x1d300u,0x1d300u,0xc0100u,0x24802u,0x1d302u,0x100000u,0xc0110u,0x24804u,0x1d304u,0x100000u,0xc0120u,0x24806u,0x1d306u,0x100000u,0xc0130u,0x24a00u,0x1d310u,0x1d300u,0xc0140u,0x24a02u,0x1d312u,0x100000u,0xc0150u,0x24a04u,0x1d314u,0x100000u,0xc0160u,0x24a06u,0x1d316u,0x100000u,0xc0170u,0x24c00u,0x1d320u,0x1d300u,0xc0280u,0x24c02u,0x1d322u,0x100000u,0xc0290u,0x24c04u,0x1d324u,0x100000u,0xc02a0u,0x24c06u,0x1d326u,0x100000u,0xc02b0u,0x24e00u,0x1d330u,0x1d300u,0xc02c0u,0x24e02u,0x1d332u,0x100000u,0xc02d0u,0x24e04u,0x1d334u,0x100000u,0xc02e0u,0x24e06u,0x1d336u,0x100000u,0xc02f0u,0x24808u,0x1d308u,0x1d310u,0xc0410u,0x2480au,0x1d30au,0x100000u,0xc0400u,0x2480cu,0x1d30cu,0x100000u,0xc0430u,0x2480eu,0x1d30eu,0x100000u,0xc0420u,0x24a08u,0x1d318u,0x1d310u,0xc0450u,0x24a0au,0x1d31au,0x100000u,0xc0440u,0x24a0cu,0x1d31cu,0x100000u,0xc0470u,0x24a0eu,0x1d31eu,0x100000u,0xc0460u,0x24c08u,0x1d328u,0x1d310u,0xef990u,0x24c0au,0x1d32au,0x100000u,0xef980u,0x24c0cu,0x1d32cu,0x100000u,0xef9b0u,0x24c0eu,0x1d32eu,0x100000u,0xef9a0u,0x24e08u,0x1d338u,0x1d310u,0xef9d0u,0x24e0au,0x1d33au,0x100000u,0xef9c0u,0x24e0cu,0x1d33cu,0x100000u,0xef9f0u,0x24e0eu,0x1d33eu,0x100000u,0xef9e0u,0x25000u,0x1d340u,0x1d320u,0xefb20u,0x25002u,0x1d342u,0x100000u,0xefb30u,0x25004u,0x1d344u,0x100000u,0xefb00u,0x25006u,0x1d346u,0x100000u,0xefb10u,0x25200u,0x1d350u,0x1d320u,0xefb60u,0x25202u,0x1d352u,0x100000u,0xefb70u,0x25204u,0x1d354u,0x100000u,0xefb40u,0x25206u,0x1d356u,0x100000u,0xefb50u,0x25400u,0x1d360u,0x1d320u,0xefca0u,0x25402u,0x1d362u,0x100000u,0xefcb0u,0x25404u,0x1d364u,0x100000u,0xefc80u,0x25406u,0x1d366u,0x100000u,0xefc90u,0x25600u,0x1d370u,0x1d320u,0xefce0u,0x25602u,0x1d372u,0x100000u,0xefcf0u,0x25604u,0x1d374u,0x100000u,0xefcc0u,0x25606u,0x1d376u,0x100000u,0xefcd0u,0x100000u,0x1d348u,0x1d330u,0xefe30u,0x100000u,0x1d34au,0x100000u,0xefe20u,0x100000u,0x1d34cu,0x100000u,0xefe10u,0x100000u,0x1d34eu,0x100000u,0xefe00u,0x100000u,0x1d358u,0x1d330u,0xefe70u,0x100000u,0x1d35au,0x100000u,0xefe60u,0x100000u,0x1d35cu,0x100000u,0xefe50u,0x100000u,0x1d35eu,0x100000u,0xefe40u,0x100000u,0x1d368u,0x1d330u,0xeffb0u,0x100000u,0x1d36au,0x100000u,0xeffa0u,0x100000u,0x1d36cu,0x100000u,0xeff90u,0x100000u,0x1d36eu,0x100000u,0xeff80u,0x100000u,0x1d378u,0x1d330u,0xefff0u,0x100000u,0x1d37au,0x100000u,0xeffe0u,0x100000u,0x1d37cu,0x100000u,0xeffd0u,0x100000u,0x1d37eu,0x100000u,0xeffc0u,0x0u,0x30420u,0x8fc50u,0xc0000u,0x10u,0x30430u,0x8fc40u,0xc0010u,0x20u,0x30400u,0x8fc70u,0xc0020u,0x30u,0x30410u,0x8fc60u,0xc0030u,0x40u,0x30460u,0x8fc10u,0xc0040u,0x50u,0x30470u,0x8fc00u,0xc0050u,0x60u,0x30440u,0x8fc30u,0xc0060u,0x70u,0x30450u,0x8fc20u,0xc0070u,0x80u,0x304b0u,0x8fcd0u,0xc0080u,0x90u,0x304a0u,0x8fcc0u,0xc0090u,0xa0u,0x30490u,0x8fcf0u,0xc00a0u,0xb0u,0x30480u,0x8fce0u,0xc00b0u,0xc0u,0x304f0u,0x8fc90u,0xc00c0u,0xd0u,0x304e0u,0x8fc80u,0xc00d0u,0xe0u,0x304d0u,0x8fcb0u,0xc00e0u,0xf0u,0x304c0u,0x8fca0u,0xc00f0u,0x100u,0x30530u,0x8fd60u,0xc0100u,0x110u,0x30520u,0x8fd70u,0xc0110u,0x120u,0x30510u,0x8fd40u,0xc0120u,0x130u,0x30500u,0x8fd50u,0xc0130u,0x140u,0x30570u,0x8fd20u,0xc0140u,0x150u,0x30560u,0x8fd30u,0xc0150u,0x160u,0x30550u,0x8fd00u,0xc0160u,0x170u,0x30540u,0x8fd10u,0xc0170u,0x190u,0x305b0u,0x8fde0u,0xc0190u,0x180u,0x305a0u,0x8fdf0u,0xc0180u,0x1b0u,0x30590u,0x8fdc0u,0xc01b0u,0x1a0u,0x30580u,0x8fdd0u,0xc01a0u,0x1d0u,0x305f0u,0x8fda0u,0xc01d0u,0x1c0u,0x305e0u,0x8fdb0u,0xc01c0u,0x1f0u,0x305d0u,0x8fd80u,0xc01f0u,0x1e0u,0x305c0u,0x8fd90u,0xc01e0u,0x210u,0x30640u,0x8fe60u,0xc0210u,0x200u,0x30650u,0x8fe70u,0xc0200u,0x230u,0x30660u,0x8fe40u,0xc0230u,0x220u,0x30670u,0x8fe50u,0xc0220u,0x250u,0x30600u,0x8fe20u,0xc0250u,0x240u,0x30610u,0x8fe30u,0xc0240u,0x270u,0x30620u,0x8fe00u,0xc0270u,0x260u,0x30630u,0x8fe10u,0xc0260u,0x290u,0x306c0u,0x8fef0u,0xc0290u,0x280u,0x306d0u,0x8fee0u,0xc0280u,0x2b0u,0x306e0u,0x8fed0u,0xc02b0u,0x2a0u,0x306f0u,0x8fec0u,0xc02a0u,0x2d0u,0x30680u,0x8feb0u,0xc02d0u,0x2c0u,0x30690u,0x8fea0u,0xc02c0u,0x2f0u,0x306a0u,0x8fe90u,0xc02f0u,0x2e0u,0x306b0u,0x8fe80u,0xc02e0u,0x320u,0x30740u,0x8ff70u,0xc0320u,0x330u,0x30750u,0x8ff60u,0xc0330u,0x300u,0x30760u,0x8ff50u,0xc0300u,0x310u,0x30770u,0x8ff40u,0xc0310u,0x360u,0x30700u,0x8ff30u,0xc0360u,0x370u,0x30710u,0x8ff20u,0xc0370u,0x340u,0x30720u,0x8ff10u,0xc0340u,0x350u,0x30730u,0x8ff00u,0xc0350u,0x3a0u,0x307d0u,0x8fff0u,0xc03a0u,0x3b0u,0x307c0u,0x8ffe0u,0xc03b0u,0x380u,0x307f0u,0x8ffd0u,0xc0380u,0x390u,0x307e0u,0x8ffc0u,0xc0390u,0x3e0u,0x30790u,0x8ffb0u,0xc03e0u,0x3f0u,0x30780u,0x8ffa0u,0xc03f0u,0x3c0u,0x307b0u,0x8ff90u,0xc03c0u,0x3d0u,0x307a0u,0x8ff80u,0xc03d0u,0x0u,0x11f820u,0x0u,0x7030cu,0x0u,0x11f830u,0x0u,0x7030cu,0x0u,0x11f800u,0x0u,0x7030cu,0x0u,0x11f810u,0x0u,0x7030cu,0x0u,0x11f860u,0x0u,0x7030cu,0x0u,0x11f870u,0x0u,0x7030cu,0x0u,0x11f840u,0x0u,0x7030cu,0x0u,0x11f850u,0x0u,0x7030cu,0x0u,0x11f8b0u,0x0u,0x7230cu,0x0u,0x11f8a0u,0x0u,0x7230cu,0x0u,0x11f890u,0x0u,0x7230cu,0x0u,0x11f880u,0x0u,0x7230cu,0x0u,0x11f8f0u,0x0u,0x7230cu,0x0u,0x11f8e0u,0x0u,0x7230cu,0x0u,0x11f8d0u,0x0u,0x7230cu,0x0u,0x11f8c0u,0x0u,0x7230cu,0x0u,0x11f930u,0x0u,0x7232cu,0x0u,0x11f920u,0x0u,0x7232cu,0x0u,0x11f910u,0x0u,0x7232cu,0x0u,0x11f900u,0x0u,0x7232cu,0x0u,0x11f970u,0x0u,0x7232cu,0x0u,0x11f960u,0x0u,0x7232cu,0x0u,0x11f950u,0x0u,0x7232cu,0x0u,0x11f940u,0x0u,0x7232cu,0x0u,0x11f9b0u,0x0u,0x72124u,0x0u,0x11f9a0u,0x0u,0x72124u,0x0u,0x11f990u,0x0u,0x72124u,0x0u,0x11f980u,0x0u,0x72124u,0x0u,0x11f9f0u,0x0u,0x72124u,0x0u,0x11f9e0u,0x0u,0x72124u,0x0u,0x11f9d0u,0x0u,0x72124u,0x0u,0x11f9c0u,0x0u,0x72124u,0x0u,0x11fa40u,0x0u,0x721a6u,0x0u,0x11fa50u,0x0u,0x721a6u,0x0u,0x11fa60u,0x0u,0x721a6u,0x0u,0x11fa70u,0x0u,0x721a6u,0x0u,0x11fa00u,0x0u,0x721a6u,0x0u,0x11fa10u,0x0u,0x721a6u,0x0u,0x11fa20u,0x0u,0x721a6u,0x0u,0x11fa30u,0x0u,0x721a6u,0x0u,0x11fac0u,0x0u,0x72186u,0x0u,0x11fad0u,0x0u,0x72186u,0x0u,0x11fae0u,0x0u,0x72186u,0x0u,0x11faf0u,0x0u,0x72186u,0x0u,0x11fa80u,0x0u,0x72186u,0x0u,0x11fa90u,0x0u,0x72186u,0x0u,0x11faa0u,0x0u,0x72186u,0x0u,0x11fab0u,0x0u,0x72186u,0x0u,0x11fb40u,0x0u,0x72186u,0x0u,0x11fb50u,0x0u,0x72186u,0x0u,0x11fb60u,0x0u,0x72186u,0x0u,0x11fb70u,0x0u,0x72186u,0x0u,0x11fb00u,0x0u,0x72186u,0x0u,0x11fb10u,0x0u,0x72186u,0x0u,0x11fb20u,0x0u,0x72186u,0x0u,0x11fb30u,0x0u,0x72186u,0x0u,0x11fbd0u,0x0u,0x7230cu,0x0u,0x11fbc0u,0x0u,0x7230cu,0x0u,0x11fbf0u,0x0u,0x7230cu,0x0u,0x11fbe0u,0x0u,0x7230cu,0x0u,0x11fb90u,0x0u,0x7230cu,0x0u,0x11fb80u,0x0u,0x7230cu,0x0u,0x11fbb0u,0x0u,0x7230cu,0x0u,0x11fba0u,0x0u,0x7230cu,0x5800u,0x5820u,0x4860u,0x4860u,0x5808u,0x5828u,0x4868u,0x4870u,0x5810u,0x5830u,0x4870u,0x5800u,0x5818u,0x5838u,0x4878u,0x5810u,0x5880u,0x58a0u,0x48e0u,0x48e0u,0x5888u,0x58a8u,0x48e8u,0x48f0u,0x5890u,0x58b0u,0x48f0u,0x5880u,0x5898u,0x58b8u,0x48f8u,0x5890u,0x5920u,0x5900u,0x4940u,0x4940u,0x5928u,0x5908u,0x4948u,0x4950u,0x5930u,0x5910u,0x4950u,0x5920u,0x5938u,0x5918u,0x4958u,0x5930u,0x59a0u,0x5980u,0x49c0u,0x49c0u,0x59a8u,0x5988u,0x49c8u,0x49d0u,0x59b0u,0x5990u,0x49d0u,0x59a0u,0x59b8u,0x5998u,0x49d8u,0x59b0u,0x5c40u,0x5c60u,0x4c20u,0x4a60u,0x5c48u,0x5c68u,0x4c28u,0x4a70u,0x5c50u,0x5c70u,0x4c30u,0x5a00u,0x5c58u,0x5c78u,0x4c38u,0x5a10u,0x5cc0u,0x5ce0u,0x4ca0u,0x4ae0u,0x5cc8u,0x5ce8u,0x4ca8u,0x4af0u,0x5cd0u,0x5cf0u,0x4cb0u,0x5a80u,0x5cd8u,0x5cf8u,0x4cb8u,0x5a90u,0x5d60u,0x5d40u,0x4d00u,0x4b40u,0x5d68u,0x5d48u,0x4d08u,0x4b50u,0x5d70u,0x5d50u,0x4d10u,0x5b20u,0x5d78u,0x5d58u,0x4d18u,0x5b30u,0x5de0u,0x5dc0u,0x4d80u,0x4bc0u,0x5de8u,0x5dc8u,0x4d88u,0x4bd0u,0x5df0u,0x5dd0u,0x4d90u,0x5ba0u,0x5df8u,0x5dd8u,0x4d98u,0x5bb0u,0x6000u,0x6020u,0x5060u,0x4c20u,0x6008u,0x6028u,0x5068u,0x4c30u,0x6010u,0x6030u,0x5070u,0x5c40u,0x6018u,0x6038u,0x5078u,0x5c50u,0x6080u,0x60a0u,0x50e0u,0x4ca0u,0x6088u,0x60a8u,0x50e8u,0x4cb0u,0x6090u,0x60b0u,0x50f0u,0x5cc0u,0x6098u,0x60b8u,0x50f8u,0x5cd0u,0x6120u,0x6100u,0x5140u,0x4d00u,0x6128u,0x6108u,0x5148u,0x4d10u,0x6130u,0x6110u,0x5150u,0x5d60u,0x6138u,0x6118u,0x5158u,0x5d70u,0x61a0u,0x6180u,0x51c0u,0x4d80u,0x61a8u,0x6188u,0x51c8u,0x4d90u,0x61b0u,0x6190u,0x51d0u,0x5de0u,0x61b8u,0x6198u,0x51d8u,0x5df0u,0x6440u,0x6460u,0x5420u,0x4e20u,0x6448u,0x6468u,0x5428u,0x4e30u,0x6450u,0x6470u,0x5430u,0x5e40u,0x6458u,0x6478u,0x5438u,0x5e50u,0x64c0u,0x64e0u,0x54a0u,0x4ea0u,0x64c8u,0x64e8u,0x54a8u,0x4eb0u,0x64d0u,0x64f0u,0x54b0u,0x5ec0u,0x64d8u,0x64f8u,0x54b8u,0x5ed0u,0x6560u,0x6540u,0x5500u,0x4f00u,0x6568u,0x6548u,0x5508u,0x4f10u,0x6570u,0x6550u,0x5510u,0x5f60u,0x6578u,0x6558u,0x5518u,0x5f70u,0x65e0u,0x65c0u,0x5580u,0x4f80u,0x65e8u,0x65c8u,0x5588u,0x4f90u,0x65f0u,0x65d0u,0x5590u,0x5fe0u,0x65f8u,0x65d8u,0x5598u,0x5ff0u,0x5820u,0x1e8c0u,0x1eb60u,0xc40u,0x5828u,0x1ed50u,0x1eff0u,0xc50u,0x5830u,0x1f1e0u,0x1f480u,0xc60u,0x5838u,0x1f670u,0x1f910u,0xc70u,0x58a0u,0x1fb00u,0x1fda0u,0xc00u,0x58a8u,0x1ff90u,0x20230u,0xc10u,0x58b0u,0x20420u,0x206c0u,0xc20u,0x58b8u,0x208b0u,0x20b50u,0xc30u,0x5900u,0x20d40u,0x20fe0u,0xdc0u,0x5908u,0x211d0u,0x21470u,0xdd0u,0x5910u,0x21660u,0x21900u,0xde0u,0x5918u,0x21af0u,0x21d90u,0xdf0u,0x5980u,0x21f80u,0x22220u,0xd80u,0x5988u,0x22410u,0x226b0u,0xd90u,0x5990u,0x228a0u,0x22b40u,0xda0u,0x5998u,0x22d30u,0x22fd0u,0xdb0u,0x5a20u,0x1e8f0u,0x1eb68u,0xf60u,0x5a28u,0x1ed80u,0x1eff8u,0xf70u,0x5a30u,0x1f210u,0x1f488u,0xf40u,0x5a38u,0x1f6a0u,0x1f918u,0xf50u,0x5aa0u,0x1fb30u,0x1fda8u,0xf20u,0x5aa8u,0x1ffc0u,0x20238u,0xf30u,0x5ab0u,0x20450u,0x206c8u,0xf00u,0x5ab8u,0x208e0u,0x20b58u,0xf10u,0x5b00u,0x20d70u,0x20fe8u,0x10e0u,0x5b08u,0x21200u,0x21478u,0x10f0u,0x5b10u,0x21690u,0x21908u,0x10c0u,0x5b18u,0x21b20u,0x21d98u,0x10d0u,0x5b80u,0x21fb0u,0x22228u,0x10a0u,0x5b88u,0x22440u,0x226b8u,0x10b0u,0x5b90u,0x228d0u,0x22b48u,0x1080u,0x5b98u,0x22d60u,0x22fd8u,0x1090u,0x5c60u,0x1e920u,0x1eb70u,0x1240u,0x5c68u,0x1edb0u,0x1f000u,0x1250u,0x5c70u,0x1f240u,0x1f490u,0x1260u,0x5c78u,0x1f6d0u,0x1f920u,0x1270u,0x5ce0u,0x1fb60u,0x1fdb0u,0x1200u,0x5ce8u,0x1fff0u,0x20240u,0x1210u,0x5cf0u,0x20480u,0x206d0u,0x1220u,0x5cf8u,0x20910u,0x20b60u,0x1230u,0x5d40u,0x20da0u,0x20ff0u,0x13c0u,0x5d48u,0x21230u,0x21480u,0x13d0u,0x5d50u,0x216c0u,0x21910u,0x13e0u,0x5d58u,0x21b50u,0x21da0u,0x13f0u,0x5dc0u,0x21fe0u,0x22230u,0x1380u,0x5dc8u,0x22470u,0x226c0u,0x1390u,0x5dd0u,0x22900u,0x22b50u,0x13a0u,0x5dd8u,0x22d90u,0x22fe0u,0x13b0u,0x5e60u,0x1e950u,0x1eb78u,0x1560u,0x5e68u,0x1ede0u,0x1f008u,0x1570u,0x5e70u,0x1f270u,0x1f498u,0x1540u,0x5e78u,0x1f700u,0x1f928u,0x1550u,0x5ee0u,0x1fb90u,0x1fdb8u,0x1520u,0x5ee8u,0x20020u,0x20248u,0x1530u,0x5ef0u,0x204b0u,0x206d8u,0x1500u,0x5ef8u,0x20940u,0x20b68u,0x1510u,0x5f40u,0x20dd0u,0x20ff8u,0x16e0u,0x5f48u,0x21260u,0x21488u,0x16f0u,0x5f50u,0x216f0u,0x21918u,0x16c0u,0x5f58u,0x21b80u,0x21da8u,0x16d0u,0x5fc0u,0x22010u,0x22238u,0x16a0u,0x5fc8u,0x224a0u,0x226c8u,0x16b0u,0x5fd0u,0x22930u,0x22b58u,0x1680u,0x5fd8u,0x22dc0u,0x22fe8u,0x1690u,0x0u,0x40u,0x9000u,0xa800u,0x80u,0xc0u,0x9010u,0xa810u,0x110u,0x150u,0x9020u,0xa820u,0x190u,0x1d0u,0x9030u,0xa830u,0x220u,0x260u,0x9040u,0xa840u,0x2a0u,0x2e0u,0x9050u,0xa850u,0x330u,0x370u,0x9060u,0xa860u,0x3b0u,0x3f0u,0x9070u,0xa870u,0x440u,0x400u,0x9080u,0xa880u,0x4c0u,0x480u,0x9090u,0xa890u,0x550u,0x510u,0x90a0u,0xa8a0u,0x5d0u,0x590u,0x90b0u,0xa8b0u,0x660u,0x620u,0x90c0u,0xa8c0u,0x6e0u,0x6a0u,0x90d0u,0xa8d0u,0x770u,0x730u,0x90e0u,0xa8e0u,0x7f0u,0x7b0u,0x90f0u,0xa8f0u,0x10u,0x50u,0x9300u,0xab00u,0x90u,0xd0u,0x9310u,0xab10u,0x100u,0x140u,0x9320u,0xab20u,0x180u,0x1c0u,0x9330u,0xab30u,0x230u,0x270u,0x9340u,0xab40u,0x2b0u,0x2f0u,0x9350u,0xab50u,0x320u,0x360u,0x9360u,0xab60u,0x3a0u,0x3e0u,0x9370u,0xab70u,0x450u,0x410u,0x9380u,0xab80u,0x4d0u,0x490u,0x9390u,0xab90u,0x540u,0x500u,0x93a0u,0xaba0u,0x5c0u,0x580u,0x93b0u,0xabb0u,0x670u,0x630u,0x93c0u,0xabc0u,0x6f0u,0x6b0u,0x93d0u,0xabd0u,0x760u,0x720u,0x93e0u,0xabe0u,0x7e0u,0x7a0u,0x93f0u,0xabf0u,0x20u,0x60u,0x9600u,0xae00u,0xa0u,0xe0u,0x9610u,0xae10u,0x130u,0x170u,0x9620u,0xae20u,0x1b0u,0x1f0u,0x9630u,0xae30u,0x200u,0x240u,0x9640u,0xae40u,0x280u,0x2c0u,0x9650u,0xae50u,0x310u,0x350u,0x9660u,0xae60u,0x390u,0x3d0u,0x9670u,0xae70u,0x460u,0x420u,0x9680u,0xae80u,0x4e0u,0x4a0u,0x9690u,0xae90u,0x570u,0x530u,0x96a0u,0xaea0u,0x5f0u,0x5b0u,0x96b0u,0xaeb0u,0x640u,0x600u,0x96c0u,0xaec0u,0x6c0u,0x680u,0x96d0u,0xaed0u,0x750u,0x710u,0x96e0u,0xaee0u,0x7d0u,0x790u,0x96f0u,0xaef0u,0x30u,0x70u,0x9900u,0xb100u,0xb0u,0xf0u,0x9910u,0xb110u,0x120u,0x160u,0x9920u,0xb120u,0x1a0u,0x1e0u,0x9930u,0xb130u,0x210u,0x250u,0x9940u,0xb140u,0x290u,0x2d0u,0x9950u,0xb150u,0x300u,0x340u,0x9960u,0xb160u,0x380u,0x3c0u,0x9970u,0xb170u,0x470u,0x430u,0x9980u,0xb180u,0x4f0u,0x4b0u,0x9990u,0xb190u,0x560u,0x520u,0x99a0u,0xb1a0u,0x5e0u,0x5a0u,0x99b0u,0xb1b0u,0x650u,0x610u,0x99c0u,0xb1c0u,0x6d0u,0x690u,0x99d0u,0xb1d0u,0x740u,0x700u,0x99e0u,0xb1e0u,0x7c0u,0x780u,0x99f0u,0xb1f0u,0x9c08u,0x12060u,0x120a0u,0x12400u,0x9c18u,0x121f0u,0x12230u,0x12410u,0x9c28u,0x12340u,0x12380u,0x12420u,0x9c38u,0x124d0u,0x12510u,0x12430u,0x9c48u,0x12620u,0x126e0u,0x12440u,0x9c58u,0x127b0u,0x12870u,0x12450u,0x9c68u,0x12900u,0x129c0u,0x12460u,0x9c78u,0x12a90u,0x12b50u,0x12470u,0x9c88u,0x12c60u,0x12ca0u,0x12480u,0x9c98u,0x12df0u,0x12e30u,0x12490u,0x9ca8u,0x12f40u,0x12f80u,0x124a0u,0x9cb8u,0x130d0u,0x13110u,0x124b0u,0x9cc8u,0x13220u,0x132e0u,0x124c0u,0x9cd8u,0x133b0u,0x13470u,0x124d0u,0x9ce8u,0x13500u,0x135c0u,0x124e0u,0x9cf8u,0x13690u,0x13750u,0x124f0u,0x9f08u,0x12070u,0x120a8u,0x12500u,0x9f18u,0x121e0u,0x12238u,0x12510u,0x9f28u,0x12350u,0x12388u,0x12520u,0x9f38u,0x124c0u,0x12518u,0x12530u,0x9f48u,0x12630u,0x126e8u,0x12540u,0x9f58u,0x127a0u,0x12878u,0x12550u,0x9f68u,0x12910u,0x129c8u,0x12560u,0x9f78u,0x12a80u,0x12b58u,0x12570u,0x9f88u,0x12c70u,0x12ca8u,0x12580u,0x9f98u,0x12de0u,0x12e38u,0x12590u,0x9fa8u,0x12f50u,0x12f88u,0x125a0u,0x9fb8u,0x130c0u,0x13118u,0x125b0u,0x9fc8u,0x13230u,0x132e8u,0x125c0u,0x9fd8u,0x133a0u,0x13478u,0x125d0u,0x9fe8u,0x13510u,0x135c8u,0x125e0u,0x9ff8u,0x13680u,0x13758u,0x125f0u,0xa208u,0x12080u,0x120b0u,0x12600u,0xa218u,0x12210u,0x12220u,0x12610u,0xa228u,0x123a0u,0x12390u,0x12620u,0xa238u,0x12530u,0x12500u,0x12630u,0xa248u,0x126c0u,0x126f0u,0x12640u,0xa258u,0x12850u,0x12860u,0x12650u,0xa268u,0x129e0u,0x129d0u,0x12660u,0xa278u,0x12b70u,0x12b40u,0x12670u,0xa288u,0x12c80u,0x12cb0u,0x12680u,0xa298u,0x12e10u,0x12e20u,0x12690u,0xa2a8u,0x12fa0u,0x12f90u,0x126a0u,0xa2b8u,0x13130u,0x13100u,0x126b0u,0xa2c8u,0x132c0u,0x132f0u,0x126c0u,0xa2d8u,0x13450u,0x13460u,0x126d0u,0xa2e8u,0x135e0u,0x135d0u,0x126e0u,0xa2f8u,0x13770u,0x13740u,0x126f0u,0xa508u,0x12090u,0x120b8u,0x12700u,0xa518u,0x12200u,0x12228u,0x12710u,0xa528u,0x123b0u,0x12398u,0x12720u,0xa538u,0x12520u,0x12508u,0x12730u,0xa548u,0x126d0u,0x126f8u,0x12740u,0xa558u,0x12840u,0x12868u,0x12750u,0xa568u,0x129f0u,0x129d8u,0x12760u,0xa578u,0x12b60u,0x12b48u,0x12770u,0xa588u,0x12c90u,0x12cb8u,0x12780u,0xa598u,0x12e00u,0x12e28u,0x12790u,0xa5a8u,0x12fb0u,0x12f98u,0x127a0u,0xa5b8u,0x13120u,0x13108u,0x127b0u,0xa5c8u,0x132d0u,0x132f8u,0x127c0u,0xa5d8u,0x13440u,0x13468u,0x127d0u,0xa5e8u,0x135f0u,0x135d8u,0x127e0u,0xa5f8u,0x13760u,0x13748u,0x127f0u,0x5000u,0x1c300u,0x1e8c0u,0x1eb60u,0x5080u,0x1c310u,0x100000u,0x1eb62u,0x5110u,0x1c320u,0x100000u,0x1eb64u,0x5190u,0x1c330u,0x100000u,0x1eb66u,0x5220u,0x1c340u,0x1e8c0u,0x1eb68u,0x52a0u,0x1c350u,0x100000u,0x1eb6au,0x5330u,0x1c360u,0x100000u,0x1eb6cu,0x53b0u,0x1c370u,0x100000u,0x1eb6eu,0x5440u,0x1c380u,0x1e8c0u,0x1eb70u,0x54c0u,0x1c390u,0x100000u,0x1eb72u,0x5550u,0x1c3a0u,0x100000u,0x1eb74u,0x55d0u,0x1c3b0u,0x100000u,0x1eb76u,0x5660u,0x1c3c0u,0x1e8c0u,0x1eb78u,0x56e0u,0x1c3d0u,0x100000u,0x1eb7au,0x5770u,0x1c3e0u,0x100000u,0x1eb7cu,0x57f0u,0x1c3f0u,0x100000u,0x1eb7eu,0x5010u,0x1c400u,0x1e8f0u,0x1eb60u,0x5090u,0x1c410u,0x100000u,0x1eb62u,0x5100u,0x1c420u,0x100000u,0x1eb64u,0x5180u,0x1c430u,0x100000u,0x1eb66u,0x5230u,0x1c440u,0x1e8f0u,0x1eb68u,0x52b0u,0x1c450u,0x100000u,0x1eb6au,0x5320u,0x1c460u,0x100000u,0x1eb6cu,0x53a0u,0x1c470u,0x100000u,0x1eb6eu,0x5450u,0x1c480u,0x1e8f0u,0x1eb70u,0x54d0u,0x1c490u,0x100000u,0x1eb72u,0x5540u,0x1c4a0u,0x100000u,0x1eb74u,0x55c0u,0x1c4b0u,0x100000u,0x1eb76u,0x5670u,0x1c4c0u,0x1e8f0u,0x1eb78u,0x56f0u,0x1c4d0u,0x100000u,0x1eb7au,0x5760u,0x1c4e0u,0x100000u,0x1eb7cu,0x57e0u,0x1c4f0u,0x100000u,0x1eb7eu,0x5020u,0x1c500u,0x1e920u,0x1eb60u,0x50a0u,0x1c510u,0x100000u,0x1eb62u,0x5130u,0x1c520u,0x100000u,0x1eb64u,0x51b0u,0x1c530u,0x100000u,0x1eb66u,0x5200u,0x1c540u,0x1e920u,0x1eb68u,0x5280u,0x1c550u,0x100000u,0x1eb6au,0x5310u,0x1c560u,0x100000u,0x1eb6cu,0x5390u,0x1c570u,0x100000u,0x1eb6eu,0x5460u,0x1c580u,0x1e920u,0x1eb70u,0x54e0u,0x1c590u,0x100000u,0x1eb72u,0x5570u,0x1c5a0u,0x100000u,0x1eb74u,0x55f0u,0x1c5b0u,0x100000u,0x1eb76u,0x5640u,0x1c5c0u,0x1e920u,0x1eb78u,0x56c0u,0x1c5d0u,0x100000u,0x1eb7au,0x5750u,0x1c5e0u,0x100000u,0x1eb7cu,0x57d0u,0x1c5f0u,0x100000u,0x1eb7eu,0x5030u,0x1c600u,0x1e950u,0x1eb60u,0x50b0u,0x1c610u,0x100000u,0x1eb62u,0x5120u,0x1c620u,0x100000u,0x1eb64u,0x51a0u,0x1c630u,0x100000u,0x1eb66u,0x5210u,0x1c640u,0x1e950u,0x1eb68u,0x5290u,0x1c650u,0x100000u,0x1eb6au,0x5300u,0x1c660u,0x100000u,0x1eb6cu,0x5380u,0x1c670u,0x100000u,0x1eb6eu,0x5470u,0x1c680u,0x1e950u,0x1eb70u,0x54f0u,0x1c690u,0x100000u,0x1eb72u,0x5560u,0x1c6a0u,0x100000u,0x1eb74u,0x55e0u,0x1c6b0u,0x100000u,0x1eb76u,0x5650u,0x1c6c0u,0x1e950u,0x1eb78u,0x56d0u,0x1c6d0u,0x100000u,0x1eb7au,0x5740u,0x1c6e0u,0x100000u,0x1eb7cu,0x57c0u,0x1c6f0u,0x100000u,0x1eb7eu,0x25800u,0x1d380u,0x1d380u,0xf0140u,0x25802u,0x1d382u,0x100000u,0xf0150u,0x25804u,0x1d384u,0x100000u,0xf0160u,0x25806u,0x1d386u,0x100000u,0xf0170u,0x25a00u,0x1d390u,0x1d380u,0xf0100u,0x25a02u,0x1d392u,0x100000u,0xf0110u,0x25a04u,0x1d394u,0x100000u,0xf0120u,0x25a06u,0x1d396u,0x100000u,0xf0130u,0x25c00u,0x1d3a0u,0x1d380u,0xf02c0u,0x25c02u,0x1d3a2u,0x100000u,0xf02d0u,0x25c04u,0x1d3a4u,0x100000u,0xf02e0u,0x25c06u,0x1d3a6u,0x100000u,0xf02f0u,0x25e00u,0x1d3b0u,0x1d380u,0xf0280u,0x25e02u,0x1d3b2u,0x100000u,0xf0290u,0x25e04u,0x1d3b4u,0x100000u,0xf02a0u,0x25e06u,0x1d3b6u,0x100000u,0xf02b0u,0x25808u,0x1d388u,0x1d390u,0x11f850u,0x2580au,0x1d38au,0x100000u,0x11f840u,0x2580cu,0x1d38cu,0x100000u,0x11f870u,0x2580eu,0x1d38eu,0x100000u,0x11f860u,0x25a08u,0x1d398u,0x1d390u,0x11f810u,0x25a0au,0x1d39au,0x100000u,0x11f800u,0x25a0cu,0x1d39cu,0x100000u,0x11f830u,0x25a0eu,0x1d39eu,0x100000u,0x11f820u,0x25c08u,0x1d3a8u,0x1d390u,0x11f9d0u,0x25c0au,0x1d3aau,0x100000u,0x11f9c0u,0x25c0cu,0x1d3acu,0x100000u,0x11f9f0u,0x25c0eu,0x1d3aeu,0x100000u,0x11f9e0u,0x25e08u,0x1d3b8u,0x1d390u,0x11f990u,0x25e0au,0x1d3bau,0x100000u,0x11f980u,0x25e0cu,0x1d3bcu,0x100000u,0x11f9b0u,0x25e0eu,0x1d3beu,0x100000u,0x11f9a0u,0x25008u,0x1d3c0u,0x1d3a0u,0x11fb60u,0x2500au,0x1d3c2u,0x100000u,0x11fb70u,0x2500cu,0x1d3c4u,0x100000u,0x11fb40u,0x2500eu,0x1d3c6u,0x100000u,0x11fb50u,0x25208u,0x1d3d0u,0x1d3a0u,0x11fb20u,0x2520au,0x1d3d2u,0x100000u,0x11fb30u,0x2520cu,0x1d3d4u,0x100000u,0x11fb00u,0x2520eu,0x1d3d6u,0x100000u,0x11fb10u,0x25408u,0x1d3e0u,0x1d3a0u,0x11fce0u,0x2540au,0x1d3e2u,0x100000u,0x11fcf0u,0x2540cu,0x1d3e4u,0x100000u,0x11fcc0u,0x2540eu,0x1d3e6u,0x100000u,0x11fcd0u,0x25608u,0x1d3f0u,0x1d3a0u,0x11fca0u,0x2560au,0x1d3f2u,0x100000u,0x11fcb0u,0x2560cu,0x1d3f4u,0x100000u,0x11fc80u,0x2560eu,0x1d3f6u,0x100000u,0x11fc90u,0x100000u,0x1d3c8u,0x1d3b0u,0x11fe70u,0x100000u,0x1d3cau,0x100000u,0x11fe60u,0x100000u,0x1d3ccu,0x100000u,0x11fe50u,0x100000u,0x1d3ceu,0x100000u,0x11fe40u,0x100000u,0x1d3d8u,0x1d3b0u,0x11fe30u,0x100000u,0x1d3dau,0x100000u,0x11fe20u,0x100000u,0x1d3dcu,0x100000u,0x11fe10u,0x100000u,0x1d3deu,0x100000u,0x11fe00u,0x100000u,0x1d3e8u,0x1d3b0u,0x11fff0u,0x100000u,0x1d3eau,0x100000u,0x11ffe0u,0x100000u,0x1d3ecu,0x100000u,0x11ffd0u,0x100000u,0x1d3eeu,0x100000u,0x11ffc0u,0x100000u,0x1d3f8u,0x1d3b0u,0x11ffb0u,0x100000u,0x1d3fau,0x100000u,0x11ffa0u,0x100000u,0x1d3fcu,0x100000u,0x11ff90u,0x100000u,0x1d3feu,0x100000u,0x11ff80u,0x420u,0x30850u,0x90000u,0xc0420u,0x430u,0x30840u,0x90010u,0xc0430u,0x400u,0x30870u,0x90020u,0xc0400u,0x410u,0x30860u,0x90030u,0xc0410u,0x460u,0x30810u,0x90040u,0xc0460u,0x470u,0x30800u,0x90050u,0xc0470u,0x440u,0x30830u,0x90060u,0xc0440u,0x450u,0x30820u,0x90070u,0xc0450u,0x4b0u,0x308d0u,0x90080u,0xef8b0u,0x4a0u,0x308c0u,0x90090u,0xef8a0u,0x490u,0x308f0u,0x900a0u,0xef890u,0x480u,0x308e0u,0x900b0u,0xef880u,0x4f0u,0x30890u,0x900c0u,0xef8f0u,0x4e0u,0x30880u,0x900d0u,0xef8e0u,0x4d0u,0x308b0u,0x900e0u,0xef8d0u,0x4c0u,0x308a0u,0x900f0u,0xef8c0u,0x530u,0x5fd60u,0x90100u,0xef930u,0x520u,0x5fd70u,0x90110u,0xef920u,0x510u,0x5fd40u,0x90120u,0xef910u,0x500u,0x5fd50u,0x90130u,0xef900u,0x570u,0x5fd20u,0x90140u,0xef970u,0x560u,0x5fd30u,0x90150u,0xef960u,0x550u,0x5fd00u,0x90160u,0xef950u,0x540u,0x5fd10u,0x90170u,0xef940u,0x5b0u,0x5fde0u,0x90190u,0xef9b0u,0x5a0u,0x5fdf0u,0x90180u,0xef9a0u,0x590u,0x5fdc0u,0x901b0u,0xef990u,0x580u,0x5fdd0u,0x901a0u,0xef980u,0x5f0u,0x5fda0u,0x901d0u,0xef9f0u,0x5e0u,0x5fdb0u,0x901c0u,0xef9e0u,0x5d0u,0x5fd80u,0x901f0u,0xef9d0u,0x5c0u,0x5fd90u,0x901e0u,0xef9c0u,0x640u,0x5fe60u,0x90210u,0xefa40u,0x650u,0x5fe70u,0x90200u,0xefa50u,0x660u,0x5fe40u,0x90230u,0xefa60u,0x670u,0x5fe50u,0x90220u,0xefa70u,0x600u,0x5fe20u,0x90250u,0xefa00u,0x610u,0x5fe30u,0x90240u,0xefa10u,0x620u,0x5fe00u,0x90270u,0xefa20u,0x630u,0x5fe10u,0x90260u,0xefa30u,0x6c0u,0x5fef0u,0x90290u,0xefac0u,0x6d0u,0x5fee0u,0x90280u,0xefad0u,0x6e0u,0x5fed0u,0x902b0u,0xefae0u,0x6f0u,0x5fec0u,0x902a0u,0xefaf0u,0x680u,0x5feb0u,0x902d0u,0xefa80u,0x690u,0x5fea0u,0x902c0u,0xefa90u,0x6a0u,0x5fe90u,0x902f0u,0xefaa0u,0x6b0u,0x5fe80u,0x902e0u,0xefab0u,0x740u,0x5ff70u,0x90320u,0xefb40u,0x750u,0x5ff60u,0x90330u,0xefb50u,0x760u,0x5ff50u,0x90300u,0xefb60u,0x770u,0x5ff40u,0x90310u,0xefb70u,0x700u,0x5ff30u,0x90360u,0xefb00u,0x710u,0x5ff20u,0x90370u,0xefb10u,0x720u,0x5ff10u,0x90340u,0xefb20u,0x730u,0x5ff00u,0x90350u,0xefb30u,0x7d0u,0x5fff0u,0x903a0u,0xefbd0u,0x7c0u,0x5ffe0u,0x903b0u,0xefbc0u,0x7f0u,0x5ffd0u,0x90380u,0xefbf0u,0x7e0u,0x5ffc0u,0x90390u,0xefbe0u,0x790u,0x5ffb0u,0x903e0u,0xefb90u,0x780u,0x5ffa0u,0x903f0u,0xefb80u,0x7b0u,0x5ff90u,0x903c0u,0xefbb0u,0x7a0u,0x5ff80u,0x903d0u,0xefba0u,0x0u,0x11fc50u,0x0u,0x7230cu,0x0u,0x11fc40u,0x0u,0x7230cu,0x0u,0x11fc70u,0x0u,0x7230cu,0x0u,0x11fc60u,0x0u,0x7230cu,0x0u,0x11fc10u,0x0u,0x7230cu,0x0u,0x11fc00u,0x0u,0x7230cu,0x0u,0x11fc30u,0x0u,0x7230cu,0x0u,0x11fc20u,0x0u,0x7230cu,0x0u,0x11fcd0u,0x0u,0x5232cu,0x0u,0x11fcc0u,0x0u,0x5232cu,0x0u,0x11fcf0u,0x0u,0x5232cu,0x0u,0x11fce0u,0x0u,0x5232cu,0x0u,0x11fc90u,0x0u,0x5232cu,0x0u,0x11fc80u,0x0u,0x5232cu,0x0u,0x11fcb0u,0x0u,0x5232cu,0x0u,0x11fca0u,0x0u,0x5232cu,0x0u,0x11fd60u,0x0u,0xd0124u,0x0u,0x11fd70u,0x0u,0xd0124u,0x0u,0x11fd40u,0x0u,0xd0124u,0x0u,0x11fd50u,0x0u,0xd0124u,0x0u,0x11fd20u,0x0u,0xd0124u,0x0u,0x11fd30u,0x0u,0xd0124u,0x0u,0x11fd00u,0x0u,0xd0124u,0x0u,0x11fd10u,0x0u,0xd0124u,0x0u,0x11fde0u,0x0u,0xd81a6u,0x0u,0x11fdf0u,0x0u,0xd81a6u,0x0u,0x11fdc0u,0x0u,0xd81a6u,0x0u,0x11fdd0u,0x0u,0xd81a6u,0x0u,0x11fda0u,0x0u,0xd81a6u,0x0u,0x11fdb0u,0x0u,0xd81a6u,0x0u,0x11fd80u,0x0u,0xd81a6u,0x0u,0x11fd90u,0x0u,0xd81a6u,0x0u,0x11fe60u,0x0u,0xd8996u,0x0u,0x11fe70u,0x0u,0xd8996u,0x0u,0x11fe40u,0x0u,0xd8996u,0x0u,0x11fe50u,0x0u,0xd8996u,0x0u,0x11fe20u,0x0u,0xd8996u,0x0u,0x11fe30u,0x0u,0xd8996u,0x0u,0x11fe00u,0x0u,0xd8996u,0x0u,0x11fe10u,0x0u,0xd8996u,0x0u,0x11fef0u,0x0u,0xd8996u,0x0u,0x11fee0u,0x0u,0xd8996u,0x0u,0x11fed0u,0x0u,0xd8996u,0x0u,0x11fec0u,0x0u,0xd8996u,0x0u,0x11feb0u,0x0u,0xd8996u,0x0u,0x11fea0u,0x0u,0xd8996u,0x0u,0x11fe90u,0x0u,0xd8996u,0x0u,0x11fe80u,0x0u,0xd8996u,0x0u,0x11ff70u,0x0u,0xd8b1cu,0x0u,0x11ff60u,0x0u,0xd8b1cu,0x0u,0x11ff50u,0x0u,0xd8b1cu,0x0u,0x11ff40u,0x0u,0xd8b1cu,0x0u,0x11ff30u,0x0u,0xd8b1cu,0x0u,0x11ff20u,0x0u,0xd8b1cu,0x0u,0x11ff10u,0x0u,0xd8b1cu,0x0u,0x11ff00u,0x0u,0xd8b1cu,0x0u,0x11fff0u,0x0u,0xd8b1cu,0x0u,0x11ffe0u,0x0u,0xd8b1cu,0x0u,0x11ffd0u,0x0u,0xd8b1cu,0x0u,0x11ffc0u,0x0u,0xd8b1cu,0x0u,0x11ffb0u,0x0u,0xd8b1cu,0x0u,0x11ffa0u,0x0u,0xd8b1cu,0x0u,0x11ff90u,0x0u,0xd8b1cu,0x0u,0x11ff80u,0x0u,0xd8b1cu,0x5840u,0x5860u,0x6800u,0x5840u,0x5848u,0x5868u,0x6808u,0x5850u,0x5850u,0x5870u,0x6810u,0x5860u,0x5858u,0x5878u,0x6818u,0x5870u,0x58c0u,0x58e0u,0x6880u,0x58c0u,0x58c8u,0x58e8u,0x6888u,0x58d0u,0x58d0u,0x58f0u,0x6890u,0x58e0u,0x58d8u,0x58f8u,0x6898u,0x58f0u,0x5960u,0x5940u,0x6920u,0x5960u,0x5968u,0x5948u,0x6928u,0x5970u,0x5970u,0x5950u,0x6930u,0x5940u,0x5978u,0x5958u,0x6938u,0x5950u,0x59e0u,0x59c0u,0x69a0u,0x59e0u,0x59e8u,0x59c8u,0x69a8u,0x59f0u,0x59f0u,0x59d0u,0x69b0u,0x59c0u,0x59f8u,0x59d8u,0x69b8u,0x59d0u,0x5c00u,0x5c20u,0x6c40u,0x5a40u,0x5c08u,0x5c28u,0x6c48u,0x5a50u,0x5c10u,0x5c30u,0x6c50u,0x5a60u,0x5c18u,0x5c38u,0x6c58u,0x5a70u,0x5c80u,0x5ca0u,0x6cc0u,0x5ac0u,0x5c88u,0x5ca8u,0x6cc8u,0x5ad0u,0x5c90u,0x5cb0u,0x6cd0u,0x5ae0u,0x5c98u,0x5cb8u,0x6cd8u,0x5af0u,0x5d20u,0x5d00u,0x6d60u,0x5b60u,0x5d28u,0x5d08u,0x6d68u,0x5b70u,0x5d30u,0x5d10u,0x6d70u,0x5b40u,0x5d38u,0x5d18u,0x6d78u,0x5b50u,0x5da0u,0x5d80u,0x6de0u,0x5be0u,0x5da8u,0x5d88u,0x6de8u,0x5bf0u,0x5db0u,0x5d90u,0x6df0u,0x5bc0u,0x5db8u,0x5d98u,0x6df8u,0x5bd0u,0x6040u,0x6060u,0x7000u,0x5c00u,0x6048u,0x6068u,0x7008u,0x5c10u,0x6050u,0x6070u,0x7010u,0x5c20u,0x6058u,0x6078u,0x7018u,0x5c30u,0x60c0u,0x60e0u,0x7080u,0x5c80u,0x60c8u,0x60e8u,0x7088u,0x5c90u,0x60d0u,0x60f0u,0x7090u,0x5ca0u,0x60d8u,0x60f8u,0x7098u,0x5cb0u,0x6160u,0x6140u,0x7120u,0x5d20u,0x6168u,0x6148u,0x7128u,0x5d30u,0x6170u,0x6150u,0x7130u,0x5d00u,0x6178u,0x6158u,0x7138u,0x5d10u,0x61e0u,0x61c0u,0x71a0u,0x5da0u,0x61e8u,0x61c8u,0x71a8u,0x5db0u,0x61f0u,0x61d0u,0x71b0u,0x5d80u,0x61f8u,0x61d8u,0x71b8u,0x5d90u,0x6400u,0x6420u,0x7440u,0x5e00u,0x6408u,0x6428u,0x7448u,0x5e10u,0x6410u,0x6430u,0x7450u,0x5e20u,0x6418u,0x6438u,0x7458u,0x5e30u,0x6480u,0x64a0u,0x74c0u,0x5e80u,0x6488u,0x64a8u,0x74c8u,0x5e90u,0x6490u,0x64b0u,0x74d0u,0x5ea0u,0x6498u,0x64b8u,0x74d8u,0x5eb0u,0x6520u,0x6500u,0x7560u,0x5f20u,0x6528u,0x6508u,0x7568u,0x5f30u,0x6530u,0x6510u,0x7570u,0x5f00u,0x6538u,0x6518u,0x7578u,0x5f10u,0x65a0u,0x6580u,0x75e0u,0x5fa0u,0x65a8u,0x6588u,0x75e8u,0x5fb0u,0x65b0u,0x6590u,0x75f0u,0x5f80u,0x65b8u,0x6598u,0x75f8u,0x5f90u,0x6800u,0x1e980u,0x1ebc0u,0x1800u,0x6808u,0x1ee10u,0x1f050u,0x1810u,0x6810u,0x1f2a0u,0x1f4e0u,0x1820u,0x6818u,0x1f730u,0x1f970u,0x1830u,0x6880u,0x1fbc0u,0x1fe00u,0x1840u,0x6888u,0x20050u,0x20290u,0x1850u,0x6890u,0x204e0u,0x20720u,0x1860u,0x6898u,0x20970u,0x20bb0u,0x1870u,0x6920u,0x20e00u,0x21040u,0x1980u,0x6928u,0x21290u,0x214d0u,0x1990u,0x6930u,0x21720u,0x21960u,0x19a0u,0x6938u,0x21bb0u,0x21df0u,0x19b0u,0x69a0u,0x22040u,0x22280u,0x19c0u,0x69a8u,0x224d0u,0x22710u,0x19d0u,0x69b0u,0x22960u,0x22ba0u,0x19e0u,0x69b8u,0x22df0u,0x23030u,0x19f0u,0x6a00u,0x1e9b0u,0x1ebc8u,0x1b20u,0x6a08u,0x1ee40u,0x1f058u,0x1b30u,0x6a10u,0x1f2d0u,0x1f4e8u,0x1b00u,0x6a18u,0x1f760u,0x1f978u,0x1b10u,0x6a80u,0x1fbf0u,0x1fe08u,0x1b60u,0x6a88u,0x20080u,0x20298u,0x1b70u,0x6a90u,0x20510u,0x20728u,0x1b40u,0x6a98u,0x209a0u,0x20bb8u,0x1b50u,0x6b20u,0x20e30u,0x21048u,0x1ca0u,0x6b28u,0x212c0u,0x214d8u,0x1cb0u,0x6b30u,0x21750u,0x21968u,0x1c80u,0x6b38u,0x21be0u,0x21df8u,0x1c90u,0x6ba0u,0x22070u,0x22288u,0x1ce0u,0x6ba8u,0x22500u,0x22718u,0x1cf0u,0x6bb0u,0x22990u,0x22ba8u,0x1cc0u,0x6bb8u,0x22e20u,0x23038u,0x1cd0u,0x6c40u,0x1e9e0u,0x1ebd0u,0x1e00u,0x6c48u,0x1ee70u,0x1f060u,0x1e10u,0x6c50u,0x1f300u,0x1f4f0u,0x1e20u,0x6c58u,0x1f790u,0x1f980u,0x1e30u,0x6cc0u,0x1fc20u,0x1fe10u,0x1e40u,0x6cc8u,0x200b0u,0x202a0u,0x1e50u,0x6cd0u,0x20540u,0x20730u,0x1e60u,0x6cd8u,0x209d0u,0x20bc0u,0x1e70u,0x6d60u,0x20e60u,0x21050u,0x1f80u,0x6d68u,0x212f0u,0x214e0u,0x1f90u,0x6d70u,0x21780u,0x21970u,0x1fa0u,0x6d78u,0x21c10u,0x21e00u,0x1fb0u,0x6de0u,0x220a0u,0x22290u,0x1fc0u,0x6de8u,0x22530u,0x22720u,0x1fd0u,0x6df0u,0x229c0u,0x22bb0u,0x1fe0u,0x6df8u,0x22e50u,0x23040u,0x1ff0u,0x6e40u,0x1ea10u,0x1ebd8u,0x2120u,0x6e48u,0x1eea0u,0x1f068u,0x2130u,0x6e50u,0x1f330u,0x1f4f8u,0x2100u,0x6e58u,0x1f7c0u,0x1f988u,0x2110u,0x6ec0u,0x1fc50u,0x1fe18u,0x2160u,0x6ec8u,0x200e0u,0x202a8u,0x2170u,0x6ed0u,0x20570u,0x20738u,0x2140u,0x6ed8u,0x20a00u,0x20bc8u,0x2150u,0x6f60u,0x20e90u,0x21058u,0x22a0u,0x6f68u,0x21320u,0x214e8u,0x22b0u,0x6f70u,0x217b0u,0x21978u,0x2280u,0x6f78u,0x21c40u,0x21e08u,0x2290u,0x6fe0u,0x220d0u,0x22298u,0x22e0u,0x6fe8u,0x22560u,0x22728u,0x22f0u,0x6ff0u,0x229f0u,0x22bb8u,0x22c0u,0x6ff8u,0x22e80u,0x23048u,0x22d0u,0x0u,0x40u,0x9000u,0xb400u,0x80u,0xc0u,0x9010u,0xb410u,0x110u,0x150u,0x9020u,0xb420u,0x190u,0x1d0u,0x9030u,0xb430u,0x220u,0x260u,0x9040u,0xb440u,0x2a0u,0x2e0u,0x9050u,0xb450u,0x330u,0x370u,0x9060u,0xb460u,0x3b0u,0x3f0u,0x9070u,0xb470u,0x440u,0x400u,0x9080u,0xb480u,0x4c0u,0x480u,0x9090u,0xb490u,0x550u,0x510u,0x90a0u,0xb4a0u,0x5d0u,0x590u,0x90b0u,0xb4b0u,0x660u,0x620u,0x90c0u,0xb4c0u,0x6e0u,0x6a0u,0x90d0u,0xb4d0u,0x770u,0x730u,0x90e0u,0xb4e0u,0x7f0u,0x7b0u,0x90f0u,0xb4f0u,0x10u,0x50u,0x9300u,0xb700u,0x90u,0xd0u,0x9310u,0xb710u,0x100u,0x140u,0x9320u,0xb720u,0x180u,0x1c0u,0x9330u,0xb730u,0x230u,0x270u,0x9340u,0xb740u,0x2b0u,0x2f0u,0x9350u,0xb750u,0x320u,0x360u,0x9360u,0xb760u,0x3a0u,0x3e0u,0x9370u,0xb770u,0x450u,0x410u,0x9380u,0xb780u,0x4d0u,0x490u,0x9390u,0xb790u,0x540u,0x500u,0x93a0u,0xb7a0u,0x5c0u,0x580u,0x93b0u,0xb7b0u,0x670u,0x630u,0x93c0u,0xb7c0u,0x6f0u,0x6b0u,0x93d0u,0xb7d0u,0x760u,0x720u,0x93e0u,0xb7e0u,0x7e0u,0x7a0u,0x93f0u,0xb7f0u,0x20u,0x60u,0x9600u,0xba00u,0xa0u,0xe0u,0x9610u,0xba10u,0x130u,0x170u,0x9620u,0xba20u,0x1b0u,0x1f0u,0x9630u,0xba30u,0x200u,0x240u,0x9640u,0xba40u,0x280u,0x2c0u,0x9650u,0xba50u,0x310u,0x350u,0x9660u,0xba60u,0x390u,0x3d0u,0x9670u,0xba70u,0x460u,0x420u,0x9680u,0xba80u,0x4e0u,0x4a0u,0x9690u,0xba90u,0x570u,0x530u,0x96a0u,0xbaa0u,0x5f0u,0x5b0u,0x96b0u,0xbab0u,0x640u,0x600u,0x96c0u,0xbac0u,0x6c0u,0x680u,0x96d0u,0xbad0u,0x750u,0x710u,0x96e0u,0xbae0u,0x7d0u,0x790u,0x96f0u,0xbaf0u,0x30u,0x70u,0x9900u,0xbd00u,0xb0u,0xf0u,0x9910u,0xbd10u,0x120u,0x160u,0x9920u,0xbd20u,0x1a0u,0x1e0u,0x9930u,0xbd30u,0x210u,0x250u,0x9940u,0xbd40u,0x290u,0x2d0u,0x9950u,0xbd50u,0x300u,0x340u,0x9960u,0xbd60u,0x380u,0x3c0u,0x9970u,0xbd70u,0x470u,0x430u,0x9980u,0xbd80u,0x4f0u,0x4b0u,0x9990u,0xbd90u,0x560u,0x520u,0x99a0u,0xbda0u,0x5e0u,0x5a0u,0x99b0u,0xbdb0u,0x650u,0x610u,0x99c0u,0xbdc0u,0x6d0u,0x690u,0x99d0u,0xbdd0u,0x740u,0x700u,0x99e0u,0xbde0u,0x7c0u,0x780u,0x99f0u,0xbdf0u,0xc000u,0x120c0u,0x12100u,0x12800u,0xc010u,0x12250u,0x12290u,0x12810u,0xc020u,0x123e0u,0x12420u,0x12820u,0xc030u,0x12570u,0x125b0u,0x12830u,0xc040u,0x12680u,0x12740u,0x12840u,0xc050u,0x12810u,0x128d0u,0x12850u,0xc060u,0x129a0u,0x12a60u,0x12860u,0xc070u,0x12b30u,0x12bf0u,0x12870u,0xc080u,0x12cc0u,0x12d00u,0x12880u,0xc090u,0x12e50u,0x12e90u,0x12890u,0xc0a0u,0x12fe0u,0x13020u,0x128a0u,0xc0b0u,0x13170u,0x131b0u,0x128b0u,0xc0c0u,0x13280u,0x13340u,0x128c0u,0xc0d0u,0x13410u,0x134d0u,0x128d0u,0xc0e0u,0x135a0u,0x13660u,0x128e0u,0xc0f0u,0x13730u,0x137f0u,0x128f0u,0xc300u,0x120d0u,0x12108u,0x12900u,0xc310u,0x12240u,0x12298u,0x12910u,0xc320u,0x123f0u,0x12428u,0x12920u,0xc330u,0x12560u,0x125b8u,0x12930u,0xc340u,0x12690u,0x12748u,0x12940u,0xc350u,0x12800u,0x128d8u,0x12950u,0xc360u,0x129b0u,0x12a68u,0x12960u,0xc370u,0x12b20u,0x12bf8u,0x12970u,0xc380u,0x12cd0u,0x12d08u,0x12980u,0xc390u,0x12e40u,0x12e98u,0x12990u,0xc3a0u,0x12ff0u,0x13028u,0x129a0u,0xc3b0u,0x13160u,0x131b8u,0x129b0u,0xc3c0u,0x13290u,0x13348u,0x129c0u,0xc3d0u,0x13400u,0x134d8u,0x129d0u,0xc3e0u,0x135b0u,0x13668u,0x129e0u,0xc3f0u,0x13720u,0x137f8u,0x129f0u,0xc600u,0x120e0u,0x12110u,0x12a00u,0xc610u,0x12270u,0x12280u,0x12a10u,0xc620u,0x123c0u,0x12430u,0x12a20u,0xc630u,0x12550u,0x125a0u,0x12a30u,0xc640u,0x126a0u,0x12750u,0x12a40u,0xc650u,0x12830u,0x128c0u,0x12a50u,0xc660u,0x12980u,0x12a70u,0x12a60u,0xc670u,0x12b10u,0x12be0u,0x12a70u,0xc680u,0x12ce0u,0x12d10u,0x12a80u,0xc690u,0x12e70u,0x12e80u,0x12a90u,0xc6a0u,0x12fc0u,0x13030u,0x12aa0u,0xc6b0u,0x13150u,0x131a0u,0x12ab0u,0xc6c0u,0x132a0u,0x13350u,0x12ac0u,0xc6d0u,0x13430u,0x134c0u,0x12ad0u,0xc6e0u,0x13580u,0x13670u,0x12ae0u,0xc6f0u,0x13710u,0x137e0u,0x12af0u,0xc900u,0x120f0u,0x12118u,0x12b00u,0xc910u,0x12260u,0x12288u,0x12b10u,0xc920u,0x123d0u,0x12438u,0x12b20u,0xc930u,0x12540u,0x125a8u,0x12b30u,0xc940u,0x126b0u,0x12758u,0x12b40u,0xc950u,0x12820u,0x128c8u,0x12b50u,0xc960u,0x12990u,0x12a78u,0x12b60u,0xc970u,0x12b00u,0x12be8u,0x12b70u,0xc980u,0x12cf0u,0x12d18u,0x12b80u,0xc990u,0x12e60u,0x12e88u,0x12b90u,0xc9a0u,0x12fd0u,0x13038u,0x12ba0u,0xc9b0u,0x13140u,0x131a8u,0x12bb0u,0xc9c0u,0x132b0u,0x13358u,0x12bc0u,0xc9d0u,0x13420u,0x134c8u,0x12bd0u,0xc9e0u,0x13590u,0x13678u,0x12be0u,0xc9f0u,0x13700u,0x137e8u,0x12bf0u,0x7800u,0x1c300u,0x1e980u,0x1ebc0u,0x7880u,0x1c310u,0x100000u,0x1ebc2u,0x7910u,0x1c320u,0x100000u,0x1ebc4u,0x7990u,0x1c330u,0x100000u,0x1ebc6u,0x7a20u,0x1c340u,0x1e980u,0x1ebc8u,0x7aa0u,0x1c350u,0x100000u,0x1ebcau,0x7b30u,0x1c360u,0x100000u,0x1ebccu,0x7bb0u,0x1c370u,0x100000u,0x1ebceu,0x7c40u,0x1c380u,0x1e980u,0x1ebd0u,0x7cc0u,0x1c390u,0x100000u,0x1ebd2u,0x7d50u,0x1c3a0u,0x100000u,0x1ebd4u,0x7dd0u,0x1c3b0u,0x100000u,0x1ebd6u,0x7e60u,0x1c3c0u,0x1e980u,0x1ebd8u,0x7ee0u,0x1c3d0u,0x100000u,0x1ebdau,0x7f70u,0x1c3e0u,0x100000u,0x1ebdcu,0x7ff0u,0x1c3f0u,0x100000u,0x1ebdeu,0x7810u,0x1c400u,0x1e9b0u,0x1ebc0u,0x7890u,0x1c410u,0x100000u,0x1ebc2u,0x7900u,0x1c420u,0x100000u,0x1ebc4u,0x7980u,0x1c430u,0x100000u,0x1ebc6u,0x7a30u,0x1c440u,0x1e9b0u,0x1ebc8u,0x7ab0u,0x1c450u,0x100000u,0x1ebcau,0x7b20u,0x1c460u,0x100000u,0x1ebccu,0x7ba0u,0x1c470u,0x100000u,0x1ebceu,0x7c50u,0x1c480u,0x1e9b0u,0x1ebd0u,0x7cd0u,0x1c490u,0x100000u,0x1ebd2u,0x7d40u,0x1c4a0u,0x100000u,0x1ebd4u,0x7dc0u,0x1c4b0u,0x100000u,0x1ebd6u,0x7e70u,0x1c4c0u,0x1e9b0u,0x1ebd8u,0x7ef0u,0x1c4d0u,0x100000u,0x1ebdau,0x7f60u,0x1c4e0u,0x100000u,0x1ebdcu,0x7fe0u,0x1c4f0u,0x100000u,0x1ebdeu,0x7820u,0x1c500u,0x1e9e0u,0x1ebc0u,0x78a0u,0x1c510u,0x100000u,0x1ebc2u,0x7930u,0x1c520u,0x100000u,0x1ebc4u,0x79b0u,0x1c530u,0x100000u,0x1ebc6u,0x7a00u,0x1c540u,0x1e9e0u,0x1ebc8u,0x7a80u,0x1c550u,0x100000u,0x1ebcau,0x7b10u,0x1c560u,0x100000u,0x1ebccu,0x7b90u,0x1c570u,0x100000u,0x1ebceu,0x7c60u,0x1c580u,0x1e9e0u,0x1ebd0u,0x7ce0u,0x1c590u,0x100000u,0x1ebd2u,0x7d70u,0x1c5a0u,0x100000u,0x1ebd4u,0x7df0u,0x1c5b0u,0x100000u,0x1ebd6u,0x7e40u,0x1c5c0u,0x1e9e0u,0x1ebd8u,0x7ec0u,0x1c5d0u,0x100000u,0x1ebdau,0x7f50u,0x1c5e0u,0x100000u,0x1ebdcu,0x7fd0u,0x1c5f0u,0x100000u,0x1ebdeu,0x7830u,0x1c600u,0x1ea10u,0x1ebc0u,0x78b0u,0x1c610u,0x100000u,0x1ebc2u,0x7920u,0x1c620u,0x100000u,0x1ebc4u,0x79a0u,0x1c630u,0x100000u,0x1ebc6u,0x7a10u,0x1c640u,0x1ea10u,0x1ebc8u,0x7a90u,0x1c650u,0x100000u,0x1ebcau,0x7b00u,0x1c660u,0x100000u,0x1ebccu,0x7b80u,0x1c670u,0x100000u,0x1ebceu,0x7c70u,0x1c680u,0x1ea10u,0x1ebd0u,0x7cf0u,0x1c690u,0x100000u,0x1ebd2u,0x7d60u,0x1c6a0u,0x100000u,0x1ebd4u,0x7de0u,0x1c6b0u,0x100000u,0x1ebd6u,0x7e50u,0x1c6c0u,0x1ea10u,0x1ebd8u,0x7ed0u,0x1c6d0u,0x100000u,0x1ebdau,0x7f40u,0x1c6e0u,0x100000u,0x1ebdcu,0x7fc0u,0x1c6f0u,0x100000u,0x1ebdeu,0x26000u,0x1d400u,0x1d400u,0x80000000u,0x26002u,0x1d402u,0x100000u,0x80000000u,0x26004u,0x1d404u,0x100000u,0x80000000u,0x26006u,0x1d406u,0x100000u,0x80000000u,0x26200u,0x1d410u,0x1d400u,0x80000000u,0x26202u,0x1d412u,0x100000u,0x80000000u,0x26204u,0x1d414u,0x100000u,0x80000000u,0x26206u,0x1d416u,0x100000u,0x80000000u,0x26400u,0x1d420u,0x1d400u,0x80000000u,0x26402u,0x1d422u,0x100000u,0x80000000u,0x26404u,0x1d424u,0x100000u,0x80000000u,0x26406u,0x1d426u,0x100000u,0x80000000u,0x26600u,0x1d430u,0x1d400u,0x80000000u,0x26602u,0x1d432u,0x100000u,0x80000000u,0x26604u,0x1d434u,0x100000u,0x80000000u,0x26606u,0x1d436u,0x100000u,0x80000000u,0x26008u,0x1d408u,0x1d410u,0x80000000u,0x2600au,0x1d40au,0x100000u,0x80000000u,0x2600cu,0x1d40cu,0x100000u,0x80000000u,0x2600eu,0x1d40eu,0x100000u,0x80000000u,0x26208u,0x1d418u,0x1d410u,0x80000000u,0x2620au,0x1d41au,0x100000u,0x80000000u,0x2620cu,0x1d41cu,0x100000u,0x80000000u,0x2620eu,0x1d41eu,0x100000u,0x80000000u,0x26408u,0x1d428u,0x1d410u,0x80000000u,0x2640au,0x1d42au,0x100000u,0x80000000u,0x2640cu,0x1d42cu,0x100000u,0x80000000u,0x2640eu,0x1d42eu,0x100000u,0x80000000u,0x26608u,0x1d438u,0x1d410u,0x80000000u,0x2660au,0x1d43au,0x100000u,0x80000000u,0x2660cu,0x1d43cu,0x100000u,0x80000000u,0x2660eu,0x1d43eu,0x100000u,0x80000000u,0x26800u,0x1d440u,0x1d420u,0x80000000u,0x26802u,0x1d442u,0x100000u,0x80000000u,0x26804u,0x1d444u,0x100000u,0x80000000u,0x26806u,0x1d446u,0x100000u,0x80000000u,0x26a00u,0x1d450u,0x1d420u,0x80000000u,0x26a02u,0x1d452u,0x100000u,0x80000000u,0x26a04u,0x1d454u,0x100000u,0x80000000u,0x26a06u,0x1d456u,0x100000u,0x80000000u,0x26c00u,0x1d460u,0x1d420u,0x80000000u,0x26c02u,0x1d462u,0x100000u,0x80000000u,0x26c04u,0x1d464u,0x100000u,0x80000000u,0x26c06u,0x1d466u,0x100000u,0x80000000u,0x26e00u,0x1d470u,0x1d420u,0x80000000u,0x26e02u,0x1d472u,0x100000u,0x80000000u,0x26e04u,0x1d474u,0x100000u,0x80000000u,0x26e06u,0x1d476u,0x100000u,0x80000000u,0x100000u,0x1d448u,0x1d430u,0x80000000u,0x100000u,0x1d44au,0x100000u,0x80000000u,0x100000u,0x1d44cu,0x100000u,0x80000000u,0x100000u,0x1d44eu,0x100000u,0x80000000u,0x100000u,0x1d458u,0x1d430u,0x80000000u,0x100000u,0x1d45au,0x100000u,0x80000000u,0x100000u,0x1d45cu,0x100000u,0x80000000u,0x100000u,0x1d45eu,0x100000u,0x80000000u,0x100000u,0x1d468u,0x1d430u,0x80000000u,0x100000u,0x1d46au,0x100000u,0x80000000u,0x100000u,0x1d46cu,0x100000u,0x80000000u,0x100000u,0x1d46eu,0x100000u,0x80000000u,0x100000u,0x1d478u,0x1d430u,0x80000000u,0x100000u,0x1d47au,0x100000u,0x80000000u,0x100000u,0x1d47cu,0x100000u,0x80000000u,0x100000u,0x1d47eu,0x100000u,0x80000000u,0x850u,0x60000u,0x90420u,0xefc50u,0x840u,0x60010u,0x90430u,0xefc40u,0x870u,0x60020u,0x90400u,0xefc70u,0x860u,0x60030u,0x90410u,0xefc60u,0x810u,0x60040u,0x90460u,0xefc10u,0x800u,0x60050u,0x90470u,0xefc00u,0x830u,0x60060u,0x90440u,0xefc30u,0x820u,0x60070u,0x90450u,0xefc20u,0x8d0u,0x60080u,0x904b0u,0xefcd0u,0x8c0u,0x60090u,0x904a0u,0xefcc0u,0x8f0u,0x600a0u,0x90490u,0xefcf0u,0x8e0u,0x600b0u,0x90480u,0xefce0u,0x890u,0x600c0u,0x904f0u,0xefc90u,0x880u,0x600d0u,0x904e0u,0xefc80u,0x8b0u,0x600e0u,0x904d0u,0xefcb0u,0x8a0u,0x600f0u,0x904c0u,0xefca0u,0x960u,0x60100u,0x90530u,0xefd60u,0x970u,0x60110u,0x90520u,0xefd70u,0x940u,0x60120u,0x90510u,0xefd40u,0x950u,0x60130u,0x90500u,0xefd50u,0x920u,0x60140u,0x90570u,0xefd20u,0x930u,0x60150u,0x90560u,0xefd30u,0x900u,0x60160u,0x90550u,0xefd00u,0x910u,0x60170u,0x90540u,0xefd10u,0x9e0u,0x60190u,0x905b0u,0xefde0u,0x9f0u,0x60180u,0x905a0u,0xefdf0u,0x9c0u,0x601b0u,0x90590u,0xefdc0u,0x9d0u,0x601a0u,0x90580u,0xefdd0u,0x9a0u,0x601d0u,0x905f0u,0xefda0u,0x9b0u,0x601c0u,0x905e0u,0xefdb0u,0x980u,0x601f0u,0x905d0u,0xefd80u,0x990u,0x601e0u,0x905c0u,0xefd90u,0xa60u,0x60210u,0xbfa40u,0xefe60u,0xa70u,0x60200u,0xbfa50u,0xefe70u,0xa40u,0x60230u,0xbfa60u,0xefe40u,0xa50u,0x60220u,0xbfa70u,0xefe50u,0xa20u,0x60250u,0xbfa00u,0xefe20u,0xa30u,0x60240u,0xbfa10u,0xefe30u,0xa00u,0x60270u,0xbfa20u,0xefe00u,0xa10u,0x60260u,0xbfa30u,0xefe10u,0x2fef0u,0x60290u,0xbfac0u,0xefef0u,0x2fee0u,0x60280u,0xbfad0u,0xefee0u,0x2fed0u,0x602b0u,0xbfae0u,0xefed0u,0x2fec0u,0x602a0u,0xbfaf0u,0xefec0u,0x2feb0u,0x602d0u,0xbfa80u,0xefeb0u,0x2fea0u,0x602c0u,0xbfa90u,0xefea0u,0x2fe90u,0x602f0u,0xbfaa0u,0xefe90u,0x2fe80u,0x602e0u,0xbfab0u,0xefe80u,0x2ff70u,0x60320u,0xbfb40u,0xeff70u,0x2ff60u,0x60330u,0xbfb50u,0xeff60u,0x2ff50u,0x60300u,0xbfb60u,0xeff50u,0x2ff40u,0x60310u,0xbfb70u,0xeff40u,0x2ff30u,0x60360u,0xbfb00u,0xeff30u,0x2ff20u,0x60370u,0xbfb10u,0xeff20u,0x2ff10u,0x60340u,0xbfb20u,0xeff10u,0x2ff00u,0x60350u,0xbfb30u,0xeff00u,0x2fff0u,0x603a0u,0xbfbd0u,0xefff0u,0x2ffe0u,0x603b0u,0xbfbc0u,0xeffe0u,0x2ffd0u,0x60380u,0xbfbf0u,0xeffd0u,0x2ffc0u,0x60390u,0xbfbe0u,0xeffc0u,0x2ffb0u,0x603e0u,0xbfb90u,0xeffb0u,0x2ffa0u,0x603f0u,0xbfb80u,0xeffa0u,0x2ff90u,0x603c0u,0xbfbb0u,0xeff90u,0x2ff80u,0x603d0u,0xbfba0u,0xeff80u,0x0u,0x80000000u,0x0u,0x18830u,0x0u,0x80000000u,0x0u,0x18830u,0x0u,0x80000000u,0x0u,0x18830u,0x0u,0x80000000u,0x0u,0x18830u,0x0u,0x80000000u,0x0u,0x18830u,0x0u,0x80000000u,0x0u,0x18830u,0x0u,0x80000000u,0x0u,0x18830u,0x0u,0x80000000u,0x0u,0x18830u,0x0u,0x80000000u,0x0u,0x18830u,0x0u,0x80000000u,0x0u,0x18830u,0x0u,0x80000000u,0x0u,0x18830u,0x0u,0x80000000u,0x0u,0x18830u,0x0u,0x80000000u,0x0u,0x18830u,0x0u,0x80000000u,0x0u,0x18830u,0x0u,0x80000000u,0x0u,0x18830u,0x0u,0x80000000u,0x0u,0x18830u,0x0u,0x80000000u,0x0u,0x188b2u,0x0u,0x80000000u,0x0u,0x188b2u,0x0u,0x80000000u,0x0u,0x188b2u,0x0u,0x80000000u,0x0u,0x188b2u,0x0u,0x80000000u,0x0u,0x188b2u,0x0u,0x80000000u,0x0u,0x188b2u,0x0u,0x80000000u,0x0u,0x188b2u,0x0u,0x80000000u,0x0u,0x188b2u,0x0u,0x80000000u,0x0u,0x18892u,0x0u,0x80000000u,0x0u,0x18892u,0x0u,0x80000000u,0x0u,0x18892u,0x0u,0x80000000u,0x0u,0x18892u,0x0u,0x80000000u,0x0u,0x18892u,0x0u,0x80000000u,0x0u,0x18892u,0x0u,0x80000000u,0x0u,0x18892u,0x0u,0x80000000u,0x0u,0x18892u,0x0u,0x80000000u,0x0u,0x1489au,0x0u,0x80000000u,0x0u,0x1489au,0x0u,0x80000000u,0x0u,0x1489au,0x0u,0x80000000u,0x0u,0x1489au,0x0u,0x80000000u,0x0u,0x1489au,0x0u,0x80000000u,0x0u,0x1489au,0x0u,0x80000000u,0x0u,0x1489au,0x0u,0x80000000u,0x0u,0x1489au,0x0u,0x80000000u,0x0u,0x34018u,0x0u,0x80000000u,0x0u,0x34018u,0x0u,0x80000000u,0x0u,0x34018u,0x0u,0x80000000u,0x0u,0x34018u,0x0u,0x80000000u,0x0u,0x34018u,0x0u,0x80000000u,0x0u,0x34018u,0x0u,0x80000000u,0x0u,0x34018u,0x0u,0x80000000u,0x0u,0x34018u,0x0u,0x80000000u,0x0u,0x36018u,0x0u,0x80000000u,0x0u,0x36018u,0x0u,0x80000000u,0x0u,0x36018u,0x0u,0x80000000u,0x0u,0x36018u,0x0u,0x80000000u,0x0u,0x36018u,0x0u,0x80000000u,0x0u,0x36018u,0x0u,0x80000000u,0x0u,0x36018u,0x0u,0x80000000u,0x0u,0x36018u,0x0u,0x80000000u,0x0u,0x36030u,0x0u,0x80000000u,0x0u,0x36030u,0x0u,0x80000000u,0x0u,0x36030u,0x0u,0x80000000u,0x0u,0x36030u,0x0u,0x80000000u,0x0u,0x36030u,0x0u,0x80000000u,0x0u,0x36030u,0x0u,0x80000000u,0x0u,0x36030u,0x0u,0x80000000u,0x0u,0x36030u,0x6840u,0x6860u,0x6820u,0x6820u,0x6848u,0x6868u,0x6828u,0x6830u,0x6850u,0x6870u,0x6830u,0x6840u,0x6858u,0x6878u,0x6838u,0x6850u,0x68c0u,0x68e0u,0x68a0u,0x68a0u,0x68c8u,0x68e8u,0x68a8u,0x68b0u,0x68d0u,0x68f0u,0x68b0u,0x68c0u,0x68d8u,0x68f8u,0x68b8u,0x68d0u,0x6960u,0x6940u,0x6900u,0x6900u,0x6968u,0x6948u,0x6908u,0x6910u,0x6970u,0x6950u,0x6910u,0x6960u,0x6978u,0x6958u,0x6918u,0x6970u,0x69e0u,0x69c0u,0x6980u,0x6980u,0x69e8u,0x69c8u,0x6988u,0x6990u,0x69f0u,0x69d0u,0x6990u,0x69e0u,0x69f8u,0x69d8u,0x6998u,0x69f0u,0x6c00u,0x6c20u,0x6c60u,0x6a20u,0x6c08u,0x6c28u,0x6c68u,0x6a30u,0x6c10u,0x6c30u,0x6c70u,0x6a40u,0x6c18u,0x6c38u,0x6c78u,0x6a50u,0x6c80u,0x6ca0u,0x6ce0u,0x6aa0u,0x6c88u,0x6ca8u,0x6ce8u,0x6ab0u,0x6c90u,0x6cb0u,0x6cf0u,0x6ac0u,0x6c98u,0x6cb8u,0x6cf8u,0x6ad0u,0x6d20u,0x6d00u,0x6d40u,0x6b00u,0x6d28u,0x6d08u,0x6d48u,0x6b10u,0x6d30u,0x6d10u,0x6d50u,0x6b60u,0x6d38u,0x6d18u,0x6d58u,0x6b70u,0x6da0u,0x6d80u,0x6dc0u,0x6b80u,0x6da8u,0x6d88u,0x6dc8u,0x6b90u,0x6db0u,0x6d90u,0x6dd0u,0x6be0u,0x6db8u,0x6d98u,0x6dd8u,0x6bf0u,0x7040u,0x7060u,0x7020u,0x6c60u,0x7048u,0x7068u,0x7028u,0x6c70u,0x7050u,0x7070u,0x7030u,0x6c00u,0x7058u,0x7078u,0x7038u,0x6c10u,0x70c0u,0x70e0u,0x70a0u,0x6ce0u,0x70c8u,0x70e8u,0x70a8u,0x6cf0u,0x70d0u,0x70f0u,0x70b0u,0x6c80u,0x70d8u,0x70f8u,0x70b8u,0x6c90u,0x7160u,0x7140u,0x7100u,0x6d40u,0x7168u,0x7148u,0x7108u,0x6d50u,0x7170u,0x7150u,0x7110u,0x6d20u,0x7178u,0x7158u,0x7118u,0x6d30u,0x71e0u,0x71c0u,0x7180u,0x6dc0u,0x71e8u,0x71c8u,0x7188u,0x6dd0u,0x71f0u,0x71d0u,0x7190u,0x6da0u,0x71f8u,0x71d8u,0x7198u,0x6db0u,0x7400u,0x7420u,0x7460u,0x6e60u,0x7408u,0x7428u,0x7468u,0x6e70u,0x7410u,0x7430u,0x7470u,0x6e00u,0x7418u,0x7438u,0x7478u,0x6e10u,0x7480u,0x74a0u,0x74e0u,0x6ee0u,0x7488u,0x74a8u,0x74e8u,0x6ef0u,0x7490u,0x74b0u,0x74f0u,0x6e80u,0x7498u,0x74b8u,0x74f8u,0x6e90u,0x7520u,0x7500u,0x7540u,0x6f40u,0x7528u,0x7508u,0x7548u,0x6f50u,0x7530u,0x7510u,0x7550u,0x6f20u,0x7538u,0x7518u,0x7558u,0x6f30u,0x75a0u,0x7580u,0x75c0u,0x6fc0u,0x75a8u,0x7588u,0x75c8u,0x6fd0u,0x75b0u,0x7590u,0x75d0u,0x6fa0u,0x75b8u,0x7598u,0x75d8u,0x6fb0u,0x6860u,0x1ea40u,0x1ec20u,0x2440u,0x6868u,0x1eed0u,0x1f0b0u,0x2450u,0x6870u,0x1f360u,0x1f540u,0x2460u,0x6878u,0x1f7f0u,0x1f9d0u,0x2470u,0x68e0u,0x1fc80u,0x1fe60u,0x2400u,0x68e8u,0x20110u,0x202f0u,0x2410u,0x68f0u,0x205a0u,0x20780u,0x2420u,0x68f8u,0x20a30u,0x20c10u,0x2430u,0x6940u,0x20ec0u,0x210a0u,0x25c0u,0x6948u,0x21350u,0x21530u,0x25d0u,0x6950u,0x217e0u,0x219c0u,0x25e0u,0x6958u,0x21c70u,0x21e50u,0x25f0u,0x69c0u,0x22100u,0x222e0u,0x2580u,0x69c8u,0x22590u,0x22770u,0x2590u,0x69d0u,0x22a20u,0x22c00u,0x25a0u,0x69d8u,0x22eb0u,0x23090u,0x25b0u,0x6a60u,0x1ea70u,0x1ec28u,0x2760u,0x6a68u,0x1ef00u,0x1f0b8u,0x2770u,0x6a70u,0x1f390u,0x1f548u,0x2740u,0x6a78u,0x1f820u,0x1f9d8u,0x2750u,0x6ae0u,0x1fcb0u,0x1fe68u,0x2720u,0x6ae8u,0x20140u,0x202f8u,0x2730u,0x6af0u,0x205d0u,0x20788u,0x2700u,0x6af8u,0x20a60u,0x20c18u,0x2710u,0x6b40u,0x20ef0u,0x210a8u,0x28e0u,0x6b48u,0x21380u,0x21538u,0x28f0u,0x6b50u,0x21810u,0x219c8u,0x28c0u,0x6b58u,0x21ca0u,0x21e58u,0x28d0u,0x6bc0u,0x22130u,0x222e8u,0x28a0u,0x6bc8u,0x225c0u,0x22778u,0x28b0u,0x6bd0u,0x22a50u,0x22c08u,0x2880u,0x6bd8u,0x22ee0u,0x23098u,0x2890u,0x6c20u,0x1eaa0u,0x1ec30u,0x2a40u,0x6c28u,0x1ef30u,0x1f0c0u,0x2a50u,0x6c30u,0x1f3c0u,0x1f550u,0x2a60u,0x6c38u,0x1f850u,0x1f9e0u,0x2a70u,0x6ca0u,0x1fce0u,0x1fe70u,0x2a00u,0x6ca8u,0x20170u,0x20300u,0x2a10u,0x6cb0u,0x20600u,0x20790u,0x2a20u,0x6cb8u,0x20a90u,0x20c20u,0x2a30u,0x6d00u,0x20f20u,0x210b0u,0x2bc0u,0x6d08u,0x213b0u,0x21540u,0x2bd0u,0x6d10u,0x21840u,0x219d0u,0x2be0u,0x6d18u,0x21cd0u,0x21e60u,0x2bf0u,0x6d80u,0x22160u,0x222f0u,0x2b80u,0x6d88u,0x225f0u,0x22780u,0x2b90u,0x6d90u,0x22a80u,0x22c10u,0x2ba0u,0x6d98u,0x22f10u,0x230a0u,0x2bb0u,0x6e20u,0x1ead0u,0x1ec38u,0x2d60u,0x6e28u,0x1ef60u,0x1f0c8u,0x2d70u,0x6e30u,0x1f3f0u,0x1f558u,0x2d40u,0x6e38u,0x1f880u,0x1f9e8u,0x2d50u,0x6ea0u,0x1fd10u,0x1fe78u,0x2d20u,0x6ea8u,0x201a0u,0x20308u,0x2d30u,0x6eb0u,0x20630u,0x20798u,0x2d00u,0x6eb8u,0x20ac0u,0x20c28u,0x2d10u,0x6f00u,0x20f50u,0x210b8u,0x2ee0u,0x6f08u,0x213e0u,0x21548u,0x2ef0u,0x6f10u,0x21870u,0x219d8u,0x2ec0u,0x6f18u,0x21d00u,0x21e68u,0x2ed0u,0x6f80u,0x22190u,0x222f8u,0x2ea0u,0x6f88u,0x22620u,0x22788u,0x2eb0u,0x6f90u,0x22ab0u,0x22c18u,0x2e80u,0x6f98u,0x22f40u,0x230a8u,0x2e90u,0x0u,0x40u,0x9000u,0xcc00u,0x80u,0xc0u,0x9010u,0xcc10u,0x110u,0x150u,0x9020u,0xcc20u,0x190u,0x1d0u,0x9030u,0xcc30u,0x220u,0x260u,0x9040u,0xcc40u,0x2a0u,0x2e0u,0x9050u,0xcc50u,0x330u,0x370u,0x9060u,0xcc60u,0x3b0u,0x3f0u,0x9070u,0xcc70u,0x440u,0x400u,0x9080u,0xcc80u,0x4c0u,0x480u,0x9090u,0xcc90u,0x550u,0x510u,0x90a0u,0xcca0u,0x5d0u,0x590u,0x90b0u,0xccb0u,0x660u,0x620u,0x90c0u,0xccc0u,0x6e0u,0x6a0u,0x90d0u,0xccd0u,0x770u,0x730u,0x90e0u,0xcce0u,0x7f0u,0x7b0u,0x90f0u,0xccf0u,0x10u,0x50u,0x9300u,0xcf00u,0x90u,0xd0u,0x9310u,0xcf10u,0x100u,0x140u,0x9320u,0xcf20u,0x180u,0x1c0u,0x9330u,0xcf30u,0x230u,0x270u,0x9340u,0xcf40u,0x2b0u,0x2f0u,0x9350u,0xcf50u,0x320u,0x360u,0x9360u,0xcf60u,0x3a0u,0x3e0u,0x9370u,0xcf70u,0x450u,0x410u,0x9380u,0xcf80u,0x4d0u,0x490u,0x9390u,0xcf90u,0x540u,0x500u,0x93a0u,0xcfa0u,0x5c0u,0x580u,0x93b0u,0xcfb0u,0x670u,0x630u,0x93c0u,0xcfc0u,0x6f0u,0x6b0u,0x93d0u,0xcfd0u,0x760u,0x720u,0x93e0u,0xcfe0u,0x7e0u,0x7a0u,0x93f0u,0xcff0u,0x20u,0x60u,0x9600u,0xd200u,0xa0u,0xe0u,0x9610u,0xd210u,0x130u,0x170u,0x9620u,0xd220u,0x1b0u,0x1f0u,0x9630u,0xd230u,0x200u,0x240u,0x9640u,0xd240u,0x280u,0x2c0u,0x9650u,0xd250u,0x310u,0x350u,0x9660u,0xd260u,0x390u,0x3d0u,0x9670u,0xd270u,0x460u,0x420u,0x9680u,0xd280u,0x4e0u,0x4a0u,0x9690u,0xd290u,0x570u,0x530u,0x96a0u,0xd2a0u,0x5f0u,0x5b0u,0x96b0u,0xd2b0u,0x640u,0x600u,0x96c0u,0xd2c0u,0x6c0u,0x680u,0x96d0u,0xd2d0u,0x750u,0x710u,0x96e0u,0xd2e0u,0x7d0u,0x790u,0x96f0u,0xd2f0u,0x30u,0x70u,0x9900u,0xd500u,0xb0u,0xf0u,0x9910u,0xd510u,0x120u,0x160u,0x9920u,0xd520u,0x1a0u,0x1e0u,0x9930u,0xd530u,0x210u,0x250u,0x9940u,0xd540u,0x290u,0x2d0u,0x9950u,0xd550u,0x300u,0x340u,0x9960u,0xd560u,0x380u,0x3c0u,0x9970u,0xd570u,0x470u,0x430u,0x9980u,0xd580u,0x4f0u,0x4b0u,0x9990u,0xd590u,0x560u,0x520u,0x99a0u,0xd5a0u,0x5e0u,0x5a0u,0x99b0u,0xd5b0u,0x650u,0x610u,0x99c0u,0xd5c0u,0x6d0u,0x690u,0x99d0u,0xd5d0u,0x740u,0x700u,0x99e0u,0xd5e0u,0x7c0u,0x780u,0x99f0u,0xd5f0u,0xc008u,0x12120u,0x12160u,0x12c00u,0xc018u,0x122b0u,0x122f0u,0x12c10u,0xc028u,0x12400u,0x12440u,0x12c20u,0xc038u,0x12590u,0x125d0u,0x12c30u,0xc048u,0x12760u,0x12720u,0x12c40u,0xc058u,0x128f0u,0x128b0u,0x12c50u,0xc068u,0x12a40u,0x12a00u,0x12c60u,0xc078u,0x12bd0u,0x12b90u,0x12c70u,0xc088u,0x12d20u,0x12d60u,0x12c80u,0xc098u,0x12eb0u,0x12ef0u,0x12c90u,0xc0a8u,0x13000u,0x13040u,0x12ca0u,0xc0b8u,0x13190u,0x131d0u,0x12cb0u,0xc0c8u,0x13360u,0x13320u,0x12cc0u,0xc0d8u,0x134f0u,0x134b0u,0x12cd0u,0xc0e8u,0x13640u,0x13600u,0x12ce0u,0xc0f8u,0x137d0u,0x13790u,0x12cf0u,0xc308u,0x12130u,0x12168u,0x12d00u,0xc318u,0x122a0u,0x122f8u,0x12d10u,0xc328u,0x12410u,0x12448u,0x12d20u,0xc338u,0x12580u,0x125d8u,0x12d30u,0xc348u,0x12770u,0x12728u,0x12d40u,0xc358u,0x128e0u,0x128b8u,0x12d50u,0xc368u,0x12a50u,0x12a08u,0x12d60u,0xc378u,0x12bc0u,0x12b98u,0x12d70u,0xc388u,0x12d30u,0x12d68u,0x12d80u,0xc398u,0x12ea0u,0x12ef8u,0x12d90u,0xc3a8u,0x13010u,0x13048u,0x12da0u,0xc3b8u,0x13180u,0x131d8u,0x12db0u,0xc3c8u,0x13370u,0x13328u,0x12dc0u,0xc3d8u,0x134e0u,0x134b8u,0x12dd0u,0xc3e8u,0x13650u,0x13608u,0x12de0u,0xc3f8u,0x137c0u,0x13798u,0x12df0u,0xc608u,0x12140u,0x12170u,0x12e00u,0xc618u,0x122d0u,0x122e0u,0x12e10u,0xc628u,0x12460u,0x12450u,0x12e20u,0xc638u,0x125f0u,0x125c0u,0x12e30u,0xc648u,0x12700u,0x12730u,0x12e40u,0xc658u,0x12890u,0x128a0u,0x12e50u,0xc668u,0x12a20u,0x12a10u,0x12e60u,0xc678u,0x12bb0u,0x12b80u,0x12e70u,0xc688u,0x12d40u,0x12d70u,0x12e80u,0xc698u,0x12ed0u,0x12ee0u,0x12e90u,0xc6a8u,0x13060u,0x13050u,0x12ea0u,0xc6b8u,0x131f0u,0x131c0u,0x12eb0u,0xc6c8u,0x13300u,0x13330u,0x12ec0u,0xc6d8u,0x13490u,0x134a0u,0x12ed0u,0xc6e8u,0x13620u,0x13610u,0x12ee0u,0xc6f8u,0x137b0u,0x13780u,0x12ef0u,0xc908u,0x12150u,0x12178u,0x12f00u,0xc918u,0x122c0u,0x122e8u,0x12f10u,0xc928u,0x12470u,0x12458u,0x12f20u,0xc938u,0x125e0u,0x125c8u,0x12f30u,0xc948u,0x12710u,0x12738u,0x12f40u,0xc958u,0x12880u,0x128a8u,0x12f50u,0xc968u,0x12a30u,0x12a18u,0x12f60u,0xc978u,0x12ba0u,0x12b88u,0x12f70u,0xc988u,0x12d50u,0x12d78u,0x12f80u,0xc998u,0x12ec0u,0x12ee8u,0x12f90u,0xc9a8u,0x13070u,0x13058u,0x12fa0u,0xc9b8u,0x131e0u,0x131c8u,0x12fb0u,0xc9c8u,0x13310u,0x13338u,0x12fc0u,0xc9d8u,0x13480u,0x134a8u,0x12fd0u,0xc9e8u,0x13630u,0x13618u,0x12fe0u,0xc9f8u,0x137a0u,0x13788u,0x12ff0u,0x8000u,0x1c300u,0x1ea40u,0x1ec20u,0x8080u,0x1c310u,0x100000u,0x1ec22u,0x8110u,0x1c320u,0x100000u,0x1ec24u,0x8190u,0x1c330u,0x100000u,0x1ec26u,0x8220u,0x1c340u,0x1ea40u,0x1ec28u,0x82a0u,0x1c350u,0x100000u,0x1ec2au,0x8330u,0x1c360u,0x100000u,0x1ec2cu,0x83b0u,0x1c370u,0x100000u,0x1ec2eu,0x8440u,0x1c380u,0x1ea40u,0x1ec30u,0x84c0u,0x1c390u,0x100000u,0x1ec32u,0x8550u,0x1c3a0u,0x100000u,0x1ec34u,0x85d0u,0x1c3b0u,0x100000u,0x1ec36u,0x8660u,0x1c3c0u,0x1ea40u,0x1ec38u,0x86e0u,0x1c3d0u,0x100000u,0x1ec3au,0x8770u,0x1c3e0u,0x100000u,0x1ec3cu,0x87f0u,0x1c3f0u,0x100000u,0x1ec3eu,0x8010u,0x1c400u,0x1ea70u,0x1ec20u,0x8090u,0x1c410u,0x100000u,0x1ec22u,0x8100u,0x1c420u,0x100000u,0x1ec24u,0x8180u,0x1c430u,0x100000u,0x1ec26u,0x8230u,0x1c440u,0x1ea70u,0x1ec28u,0x82b0u,0x1c450u,0x100000u,0x1ec2au,0x8320u,0x1c460u,0x100000u,0x1ec2cu,0x83a0u,0x1c470u,0x100000u,0x1ec2eu,0x8450u,0x1c480u,0x1ea70u,0x1ec30u,0x84d0u,0x1c490u,0x100000u,0x1ec32u,0x8540u,0x1c4a0u,0x100000u,0x1ec34u,0x85c0u,0x1c4b0u,0x100000u,0x1ec36u,0x8670u,0x1c4c0u,0x1ea70u,0x1ec38u,0x86f0u,0x1c4d0u,0x100000u,0x1ec3au,0x8760u,0x1c4e0u,0x100000u,0x1ec3cu,0x87e0u,0x1c4f0u,0x100000u,0x1ec3eu,0x8020u,0x1c500u,0x1eaa0u,0x1ec20u,0x80a0u,0x1c510u,0x100000u,0x1ec22u,0x8130u,0x1c520u,0x100000u,0x1ec24u,0x81b0u,0x1c530u,0x100000u,0x1ec26u,0x8200u,0x1c540u,0x1eaa0u,0x1ec28u,0x8280u,0x1c550u,0x100000u,0x1ec2au,0x8310u,0x1c560u,0x100000u,0x1ec2cu,0x8390u,0x1c570u,0x100000u,0x1ec2eu,0x8460u,0x1c580u,0x1eaa0u,0x1ec30u,0x84e0u,0x1c590u,0x100000u,0x1ec32u,0x8570u,0x1c5a0u,0x100000u,0x1ec34u,0x85f0u,0x1c5b0u,0x100000u,0x1ec36u,0x8640u,0x1c5c0u,0x1eaa0u,0x1ec38u,0x86c0u,0x1c5d0u,0x100000u,0x1ec3au,0x8750u,0x1c5e0u,0x100000u,0x1ec3cu,0x87d0u,0x1c5f0u,0x100000u,0x1ec3eu,0x8030u,0x1c600u,0x1ead0u,0x1ec20u,0x80b0u,0x1c610u,0x100000u,0x1ec22u,0x8120u,0x1c620u,0x100000u,0x1ec24u,0x81a0u,0x1c630u,0x100000u,0x1ec26u,0x8210u,0x1c640u,0x1ead0u,0x1ec28u,0x8290u,0x1c650u,0x100000u,0x1ec2au,0x8300u,0x1c660u,0x100000u,0x1ec2cu,0x8380u,0x1c670u,0x100000u,0x1ec2eu,0x8470u,0x1c680u,0x1ead0u,0x1ec30u,0x84f0u,0x1c690u,0x100000u,0x1ec32u,0x8560u,0x1c6a0u,0x100000u,0x1ec34u,0x85e0u,0x1c6b0u,0x100000u,0x1ec36u,0x8650u,0x1c6c0u,0x1ead0u,0x1ec38u,0x86d0u,0x1c6d0u,0x100000u,0x1ec3au,0x8740u,0x1c6e0u,0x100000u,0x1ec3cu,0x87c0u,0x1c6f0u,0x100000u,0x1ec3eu,0x27000u,0x1d480u,0x1d480u,0x80000000u,0x27002u,0x1d482u,0x100000u,0x80000000u,0x27004u,0x1d484u,0x100000u,0x80000000u,0x27006u,0x1d486u,0x100000u,0x80000000u,0x27200u,0x1d490u,0x1d480u,0x80000000u,0x27202u,0x1d492u,0x100000u,0x80000000u,0x27204u,0x1d494u,0x100000u,0x80000000u,0x27206u,0x1d496u,0x100000u,0x80000000u,0x27400u,0x1d4a0u,0x1d480u,0x80000000u,0x27402u,0x1d4a2u,0x100000u,0x80000000u,0x27404u,0x1d4a4u,0x100000u,0x80000000u,0x27406u,0x1d4a6u,0x100000u,0x80000000u,0x27600u,0x1d4b0u,0x1d480u,0x80000000u,0x27602u,0x1d4b2u,0x100000u,0x80000000u,0x27604u,0x1d4b4u,0x100000u,0x80000000u,0x27606u,0x1d4b6u,0x100000u,0x80000000u,0x27008u,0x1d488u,0x1d490u,0x80000000u,0x2700au,0x1d48au,0x100000u,0x80000000u,0x2700cu,0x1d48cu,0x100000u,0x80000000u,0x2700eu,0x1d48eu,0x100000u,0x80000000u,0x27208u,0x1d498u,0x1d490u,0x80000000u,0x2720au,0x1d49au,0x100000u,0x80000000u,0x2720cu,0x1d49cu,0x100000u,0x80000000u,0x2720eu,0x1d49eu,0x100000u,0x80000000u,0x27408u,0x1d4a8u,0x1d490u,0x80000000u,0x2740au,0x1d4aau,0x100000u,0x80000000u,0x2740cu,0x1d4acu,0x100000u,0x80000000u,0x2740eu,0x1d4aeu,0x100000u,0x80000000u,0x27608u,0x1d4b8u,0x1d490u,0x80000000u,0x2760au,0x1d4bau,0x100000u,0x80000000u,0x2760cu,0x1d4bcu,0x100000u,0x80000000u,0x2760eu,0x1d4beu,0x100000u,0x80000000u,0x26808u,0x1d4c0u,0x1d4a0u,0x80000000u,0x2680au,0x1d4c2u,0x100000u,0x80000000u,0x2680cu,0x1d4c4u,0x100000u,0x80000000u,0x2680eu,0x1d4c6u,0x100000u,0x80000000u,0x26a08u,0x1d4d0u,0x1d4a0u,0x80000000u,0x26a0au,0x1d4d2u,0x100000u,0x80000000u,0x26a0cu,0x1d4d4u,0x100000u,0x80000000u,0x26a0eu,0x1d4d6u,0x100000u,0x80000000u,0x26c08u,0x1d4e0u,0x1d4a0u,0x80000000u,0x26c0au,0x1d4e2u,0x100000u,0x80000000u,0x26c0cu,0x1d4e4u,0x100000u,0x80000000u,0x26c0eu,0x1d4e6u,0x100000u,0x80000000u,0x26e08u,0x1d4f0u,0x1d4a0u,0x80000000u,0x26e0au,0x1d4f2u,0x100000u,0x80000000u,0x26e0cu,0x1d4f4u,0x100000u,0x80000000u,0x26e0eu,0x1d4f6u,0x100000u,0x80000000u,0x100000u,0x1d4c8u,0x1d4b0u,0x80000000u,0x100000u,0x1d4cau,0x100000u,0x80000000u,0x100000u,0x1d4ccu,0x100000u,0x80000000u,0x100000u,0x1d4ceu,0x100000u,0x80000000u,0x100000u,0x1d4d8u,0x1d4b0u,0x80000000u,0x100000u,0x1d4dau,0x100000u,0x80000000u,0x100000u,0x1d4dcu,0x100000u,0x80000000u,0x100000u,0x1d4deu,0x100000u,0x80000000u,0x100000u,0x1d4e8u,0x1d4b0u,0x80000000u,0x100000u,0x1d4eau,0x100000u,0x80000000u,0x100000u,0x1d4ecu,0x100000u,0x80000000u,0x100000u,0x1d4eeu,0x100000u,0x80000000u,0x100000u,0x1d4f8u,0x1d4b0u,0x80000000u,0x100000u,0x1d4fau,0x100000u,0x80000000u,0x100000u,0x1d4fcu,0x100000u,0x80000000u,0x100000u,0x1d4feu,0x100000u,0x80000000u,0x30000u,0x60420u,0xbfc50u,0xf0000u,0x30010u,0x60430u,0xbfc40u,0xf0010u,0x30020u,0x60400u,0xbfc70u,0xf0020u,0x30030u,0x60410u,0xbfc60u,0xf0030u,0x30040u,0x60460u,0xbfc10u,0xf0040u,0x30050u,0x60470u,0xbfc00u,0xf0050u,0x30060u,0x60440u,0xbfc30u,0xf0060u,0x30070u,0x60450u,0xbfc20u,0xf0070u,0x30080u,0x604b0u,0xbfcd0u,0xf0080u,0x30090u,0x604a0u,0xbfcc0u,0xf0090u,0x300a0u,0x60490u,0xbfcf0u,0xf00a0u,0x300b0u,0x60480u,0xbfce0u,0xf00b0u,0x300c0u,0x604f0u,0xbfc90u,0xf00c0u,0x300d0u,0x604e0u,0xbfc80u,0xf00d0u,0x300e0u,0x604d0u,0xbfcb0u,0xf00e0u,0x300f0u,0x604c0u,0xbfca0u,0xf00f0u,0x30100u,0x60530u,0xbfd60u,0xf0100u,0x30110u,0x60520u,0xbfd70u,0xf0110u,0x30120u,0x60510u,0xbfd40u,0xf0120u,0x30130u,0x60500u,0xbfd50u,0xf0130u,0x30140u,0x60570u,0xbfd20u,0xf0140u,0x30150u,0x60560u,0xbfd30u,0xf0150u,0x30160u,0x60550u,0xbfd00u,0xf0160u,0x30170u,0x60540u,0xbfd10u,0xf0170u,0x30190u,0x605b0u,0xbfde0u,0xf0190u,0x30180u,0x605a0u,0xbfdf0u,0xf0180u,0x301b0u,0x60590u,0xbfdc0u,0xf01b0u,0x301a0u,0x60580u,0xbfdd0u,0xf01a0u,0x301d0u,0x605f0u,0xbfda0u,0xf01d0u,0x301c0u,0x605e0u,0xbfdb0u,0xf01c0u,0x301f0u,0x605d0u,0xbfd80u,0xf01f0u,0x301e0u,0x605c0u,0xbfd90u,0xf01e0u,0x30210u,0x60640u,0xbfe60u,0xf0210u,0x30200u,0x60650u,0xbfe70u,0xf0200u,0x30230u,0x60660u,0xbfe40u,0xf0230u,0x30220u,0x60670u,0xbfe50u,0xf0220u,0x30250u,0x60600u,0xbfe20u,0xf0250u,0x30240u,0x60610u,0xbfe30u,0xf0240u,0x30270u,0x60620u,0xbfe00u,0xf0270u,0x30260u,0x60630u,0xbfe10u,0xf0260u,0x30290u,0x606c0u,0xbfef0u,0xf0290u,0x30280u,0x606d0u,0xbfee0u,0xf0280u,0x302b0u,0x606e0u,0xbfed0u,0xf02b0u,0x302a0u,0x606f0u,0xbfec0u,0xf02a0u,0x302d0u,0x60680u,0xbfeb0u,0xf02d0u,0x302c0u,0x60690u,0xbfea0u,0xf02c0u,0x302f0u,0x606a0u,0xbfe90u,0xf02f0u,0x302e0u,0x606b0u,0xbfe80u,0xf02e0u,0x30320u,0x60740u,0xbff70u,0x11f720u,0x30330u,0x60750u,0xbff60u,0x11f730u,0x30300u,0x60760u,0xbff50u,0x11f700u,0x30310u,0x60770u,0xbff40u,0x11f710u,0x30360u,0x60700u,0xbff30u,0x11f760u,0x30370u,0x60710u,0xbff20u,0x11f770u,0x30340u,0x60720u,0xbff10u,0x11f740u,0x30350u,0x60730u,0xbff00u,0x11f750u,0x303a0u,0x8fbd0u,0xbfff0u,0x11f7a0u,0x303b0u,0x8fbc0u,0xbffe0u,0x11f7b0u,0x30380u,0x8fbf0u,0xbffd0u,0x11f780u,0x30390u,0x8fbe0u,0xbffc0u,0x11f790u,0x303e0u,0x8fb90u,0xbffb0u,0x11f7e0u,0x303f0u,0x8fb80u,0xbffa0u,0x11f7f0u,0x303c0u,0x8fbb0u,0xbff90u,0x11f7c0u,0x303d0u,0x8fba0u,0xbff80u,0x11f7d0u,0x0u,0x80000000u,0x0u,0x36030u,0x0u,0x80000000u,0x0u,0x36030u,0x0u,0x80000000u,0x0u,0x36030u,0x0u,0x80000000u,0x0u,0x36030u,0x0u,0x80000000u,0x0u,0x36030u,0x0u,0x80000000u,0x0u,0x36030u,0x0u,0x80000000u,0x0u,0x36030u,0x0u,0x80000000u,0x0u,0x36030u,0x0u,0x80000000u,0x0u,0x360b2u,0x0u,0x80000000u,0x0u,0x360b2u,0x0u,0x80000000u,0x0u,0x360b2u,0x0u,0x80000000u,0x0u,0x360b2u,0x0u,0x80000000u,0x0u,0x360b2u,0x0u,0x80000000u,0x0u,0x360b2u,0x0u,0x80000000u,0x0u,0x360b2u,0x0u,0x80000000u,0x0u,0x360b2u,0x0u,0x80000000u,0x0u,0x36092u,0x0u,0x80000000u,0x0u,0x36092u,0x0u,0x80000000u,0x0u,0x36092u,0x0u,0x80000000u,0x0u,0x36092u,0x0u,0x80000000u,0x0u,0x36092u,0x0u,0x80000000u,0x0u,0x36092u,0x0u,0x80000000u,0x0u,0x36092u,0x0u,0x80000000u,0x0u,0x36092u,0x0u,0x80000000u,0x0u,0x3609au,0x0u,0x80000000u,0x0u,0x3609au,0x0u,0x80000000u,0x0u,0x3609au,0x0u,0x80000000u,0x0u,0x3609au,0x0u,0x80000000u,0x0u,0x3609au,0x0u,0x80000000u,0x0u,0x3609au,0x0u,0x80000000u,0x0u,0x3609au,0x0u,0x80000000u,0x0u,0x3609au,0x0u,0x80000000u,0x0u,0x36059u,0x0u,0x80000000u,0x0u,0x36059u,0x0u,0x80000000u,0x0u,0x36059u,0x0u,0x80000000u,0x0u,0x36059u,0x0u,0x80000000u,0x0u,0x36059u,0x0u,0x80000000u,0x0u,0x36059u,0x0u,0x80000000u,0x0u,0x36059u,0x0u,0x80000000u,0x0u,0x36059u,0x0u,0x80000000u,0x0u,0x36059u,0x0u,0x80000000u,0x0u,0x36059u,0x0u,0x80000000u,0x0u,0x36059u,0x0u,0x80000000u,0x0u,0x36059u,0x0u,0x80000000u,0x0u,0x36059u,0x0u,0x80000000u,0x0u,0x36059u,0x0u,0x80000000u,0x0u,0x36059u,0x0u,0x80000000u,0x0u,0x36059u,0x0u,0x80000000u,0x0u,0x16071u,0x0u,0x80000000u,0x0u,0x16071u,0x0u,0x80000000u,0x0u,0x16071u,0x0u,0x80000000u,0x0u,0x16071u,0x0u,0x80000000u,0x0u,0x16071u,0x0u,0x80000000u,0x0u,0x16071u,0x0u,0x80000000u,0x0u,0x16071u,0x0u,0x80000000u,0x0u,0x16071u,0x0u,0x80000000u,0x0u,0x14071u,0x0u,0x80000000u,0x0u,0x14071u,0x0u,0x80000000u,0x0u,0x14071u,0x0u,0x80000000u,0x0u,0x14071u,0x0u,0x80000000u,0x0u,0x14071u,0x0u,0x80000000u,0x0u,0x14071u,0x0u,0x80000000u,0x0u,0x14071u,0x0u,0x80000000u,0x0u,0x14071u};

__global__ __launch_bounds__(256, 1) void swin_main(const uint16_t* x, const uint16_t* mask, char* ws, uint16_t* out,
                                                    const uint16_t* wqkv, const uint16_t* wo, const uint16_t* ls,
                                                    const uint16_t* w1, const uint16_t* w2, const uint16_t* rc,
                                                    const int* ridx) {
  __shared__ __attribute__((aligned(16))) char smem[LDS_BYTES];
  const int wave = __builtin_amdgcn_readfirstlane(threadIdx.x >> 6);
  const unsigned lds0 = (unsigned)(size_t)smem;
  asm volatile(
      ".p2align 5\n"
      "s_mov_b64 s[92:93], %0\n"
      "s_mov_b32 s40, %1\n"
      "s_mov_b32 s41, %2\n"
      "s_mov_b64 s[88:89], %3\n"
      "s_load_dwordx2 s[16:17], s[92:93], 0\n"
      "s_load_dwordx2 s[20:21], s[92:93], 32\n"
      "s_load_dwordx2 s[46:47], s[92:93], 40\n"
      "s_load_dwordx8 s[80:87], s[92:93], 48\n"
      "s_load_dwordx2 s[76:77], s[92:93], 80\n"
      "s_mov_b64 s[90:91], s[92:93]\n"
      "s_getpc_b64 s[98:99]\n"
      "L_ipf0_%=:\n"
      "v_mbcnt_lo_u32_b32 v133, -1, 0\n"
      "v_mbcnt_hi_u32_b32 v133, -1, v133\n"
      "v_lshlrev_b32 v239, 2, v133\n"
      "v_lshlrev_b32 v245, 4, v133\n"
      "s_cmp_lt_u32 s41, 209\n"
      "s_cselect_b32 s44, 20, 19\n"
      "s_lshl_b32 s92, s40, 8\n"
      "v_add_u32_e64 v50, s92, v239\n"
      "v_min_u32 v29, 704, v50\n"
      "v_add_u32 v29, 4096, v29\n"
      "v_min_u32 v50, 672, v50\n"
      "v_add_u32 v43, 122112, v50\n"
      "s_lshr_b32 s92, s41, 3\n"
      "s_lshl_b32 s92, s92, 7\n"
      "v_lshlrev_b32 v238, 7, v133\n"
      "s_mul_i32 s93, s92, 18\n"
      "s_mul_i32 s94, s40, 640\n"
      "v_min_u32 v8, 512, v238\n"
      "v_add_u32 v8, s94, v8\n"
      "v_min_u32 v8, 2176, v8\n"
      "v_add_u32 v8, s93, v8\n"
      "s_cmp_eq_u32 s40, 1\n"
      "s_cselect_b32 s93, 4096, 0\n"
      "s_add_u32 s92, s92, s93\n"
      "s_cmp_eq_u32 s40, 2\n"
      "s_cselect_b32 s93, 9, 8\n"
      "s_cselect_b64 s[72:73], s[98:99], s[88:89]\n"
      "s_cmp_lt_u32 s40, 2\n"
      "s_cselect_b32 s93, 27, s93\n"
      "s_mul_i32 s92, s92, s93\n"
      "s_sub_u32 s93, s93, 1\n"
      "s_lshl_b32 s93, s93, 7\n"
      "v_min_u32 v238, s93, v238\n"
      "v_add_u32 v238, s92, v238\n"
      "s_lshl_b32 s95, s40, 3\n"
      "v_lshrrev_b32 v236, 3, v133\n"
      "v_and_b32 v237, 7, v133\n"
      "v_add_u32 v135, s95, v236\n"
      "v_bfe_u32 v134, v135, 1, 3\n"
      "v_cmp_lt_u32 s[70:71], s44, v135\n"
      "v_min_u32_e64 v135, s44, v135\n"
      "s_mov_b32 s18, 100270464\n"
      "s_mov_b32 s19, 0x20000\n"
      "s_mov_b32 s78, 9604\n"
      "s_mov_b32 s79, 0x20000\n"
      "s_mov_b32 s22, 221184\n"
      "s_mov_b32 s23, 0x20000\n"
      "s_lshl_b32 s64, s40, 10\n"
      "v_add_u32 v36, s64, v245\n"
      "s_add_u32 s75, s64, 18432\n"
      "s_and_b32 s92, s40, 1\n"
      "s_lshl_b32 s92, s92, 2\n"
      "s_mul_i32 s93, s40, 9216\n"
      "v_and_b32 v132, 2, v236\n"
      "v_xor_b32 v132, v132, v237\n"
      "v_xor_b32_e64 v132, s92, v132\n"
      "v_lshl_add_u32 v132, v132, 4, s93\n"
      "v_mul_u32_u24 v179, 1152, v236\n"
      "v_add_u32 v179, v179, v132\n"
      "s_cmp_lt_u32 s40, 2\n"
      "s_cselect_b32 s65, s64, 163840\n"
      "s_waitcnt lgkmcnt(0)\n"
      "s_cselect_b64 s[72:73], s[20:21], s[72:73]\n"
      "global_load_dword v50, v50, s[86:87]\n"
      "global_load_dword a255, v238, s[72:73]\n"
      "global_load_dword a253, v8, s[46:47]\n"
      "s_mul_i32 s92, s40, 8192\n"
      "s_add_u32 s88, s88, s92\n"
      "s_addc_u32 s89, s89, 0\n"
      "s_add_u32 m0, s64, 124928\n"
      "s_movk_i32 s92, 0\n"
      "buffer_load_dwordx4 v36, s[76:79], s92 offen  lds\n"
      "s_add_u32 m0, s64, 129024\n"
      "s_movk_i32 s92, 4096\n"
      "buffer_load_dwordx4 v36, s[76:79], s92 offen  lds\n"
      "s_add_u32 m0, s64, 133120\n"
      "s_cmp_lt_u32 s40, 2\n"
      "s_cselect_b32 m0, m0, s65\n"
      "s_movk_i32 s92, 8192\n"
      "buffer_load_dwordx4 v36, s[76:79], s92 offen  lds\n"
      "s_load_dwordx16 s[0:15], s[82:83], 0\n"
      "s_load_dwordx16 s[48:63], s[84:85], 0\n"
      "s_load_dwordx16 s[24:39], s[84:85], 64\n"
      "s_load_dwordx2 s[66:67], s[80:81], 0\n"
      "s_movk_i32 s93, 4180\n"
      "s_cmp_lt_u32 s41, 209\n"
      "s_cselect_b32 s92, 209, 47\n"
      "s_cselect_b32 s93, 795, s93\n"
      "s_cmp_lt_u32 s41, 252\n"
      "s_cselect_b32 s94, 16, 15\n"
      "s_add_u32 s93, s93, s41\n"
      "v_lshl_add_u32 v8, v135, 8, s41\n"
      "v_mul_u32_u24 v238, s92, v135\n"
      "v_add_u32 v238, s93, v238\n"
      "v_cmp_lt_u32 vcc, s94, v135\n"
      "v_subrev_u32 v132, 5184, v238\n"
      "s_nop 0\n"
      "v_cndmask_b32 v8, v8, v238, vcc\n"
      "v_cmp_le_u32 vcc, 5184, v8\n"
      "v_mul_u32_u24 v42, 58255, v8\n"
      "v_lshrrev_b32 v42, 22, v42\n"
      "v_mul_u32_u24 v70, 72, v42\n"
      "v_sub_u32 v70, v8, v70\n"
      "v_min_u32 v238, 72, v132\n"
      "v_cndmask_b32 v42, v42, v238, vcc\n"
      "v_subrev_u32 v238, 72, v132\n"
      "v_min_u32 v238, 72, v238\n"
      "v_cndmask_b32 v70, v70, v238, vcc\n"
      "v_mad_u32_u24 v238, v42, 7, 9\n"
      "v_subrev_u32 v8, 0x1ff, v238\n"
      "v_min_u32 v238, v238, v8\n"
      "v_mad_u32_u24 v8, v70, 7, 9\n"
      "v_subrev_u32 v132, 0x1ff, v8\n"
      "v_min_u32 v8, v8, v132\n"
      "v_mul_u32_u24 v238, 0x1ff, v238\n"
      "v_add_u32 v238, v238, v8\n"
      "v_mul_u32_u24 v8, 0x180, v238\n"
      "v_bfrev_b32 v135, 1\n"
      "v_cndmask_b32 v8, v8, v135, s[70:71]\n"
      "v_xor_b32 v134, v134, v237\n"
      "v_lshl_add_u32 v8, v134, 4, v8\n"
      "s_add_u32 m0, s64, 149504\n"
      "s_nop 0\n"
      "buffer_load_dwordx4 v8, s[16:19], 0 offen  lds\n"
      "s_add_u32 m0, s64, 153472\n"
      "s_nop 0\n"
      "buffer_load_dwordx4 v8, s[16:19], 0 offen offset:128  lds\n"
      "s_add_u32 m0, s64, 157440\n"
      "s_nop 0\n"
      "buffer_load_dwordx4 v8, s[16:19], 0 offen offset:256  lds\n"
      "s_movk_i32 s95, 504\n"
      "s_add_u32 s92, s40, 0\n"
      "s_mul_i32 s93, s92, 43\n"
      "s_lshr_b32 s93, s93, 8\n"
      "s_mul_i32 s94, s93, 6\n"
      "s_sub_u32 s92, s92, s94\n"
      "s_lshl_b32 s92, s92, 3\n"
      "s_lshl_b32 s93, s93, 7\n"
      "v_add_u32 v8, s92, v236\n"
      "v_bfe_u32 v238, v8, 1, 3\n"
      "v_xor_b32 v238, v238, v237\n"
      "v_lshl_add_u32 v238, v238, 4, s93\n"
      "v_mul_u32_u24 v132, 37, v8\n"
      "v_lshrrev_b32_e64 v132, 8, v132\n"
      "v_mad_u32_u24 v16, v132, s95, v8\n"
      "v_mul_u32_u24 v16, 384, v16\n"
      "v_add_u32 v16, v16, v238\n"
      "s_add_u32 s92, s40, 4\n"
      "s_mul_i32 s93, s92, 43\n"
      "s_lshr_b32 s93, s93, 8\n"
      "s_mul_i32 s94, s93, 6\n"
      "s_sub_u32 s92, s92, s94\n"
      "s_lshl_b32 s92, s92, 3\n"
      "s_lshl_b32 s93, s93, 7\n"
      "v_add_u32 v8, s92, v236\n"
      "v_bfe_u32 v238, v8, 1, 3\n"
      "v_xor_b32 v238, v238, v237\n"
      "v_lshl_add_u32 v238, v238, 4, s93\n"
      "v_mul_u32_u24 v134, 37, v8\n"
      "v_lshrrev_b32 v134, 8, v134\n"
      "v_mad_u32_u24 v17, v134, s95, v8\n"
      "v_mul_u32_u24 v17, 384, v17\n"
      "v_add_u32 v17, v17, v238\n"
      "s_add_u32 s92, s40, 8\n"
      "s_mul_i32 s93, s92, 43\n"
      "s_lshr_b32 s93, s93, 8\n"
      "s_mul_i32 s94, s93, 6\n"
      "s_sub_u32 s92, s92, s94\n"
      "s_lshl_b32 s92, s92, 3\n"
      "s_lshl_b32 s93, s93, 7\n"
      "v_add_u32_e64 v238, s92, v236\n"
      "v_bfe_u32 v8, v238, 1, 3\n"
      "v_xor_b32 v8, v8, v237\n"
      "v_lshl_add_u32 v8, v8, 4, s93\n"
      "v_mul_u32_u24 v134, 37, v238\n"
      "v_lshrrev_b32_e64 v134, 8, v134\n"
      "v_mad_u32_u24 v18, v134, s95, v238\n"
      "v_mul_u32_u24 v18, 384, v18\n"
      "v_add_u32 v18, v18, v8\n"
      "s_add_u32 s92, s40, 12\n"
      "s_mul_i32 s93, s92, 43\n"
      "s_lshr_b32 s93, s93, 8\n"
      "s_mul_i32 s94, s93, 6\n"
      "s_sub_u32 s92, s92, s94\n"
      "s_lshl_b32 s92, s92, 3\n"
      "s_lshl_b32 s93, s93, 7\n"
      "v_add_u32 v238, s92, v236\n"
      "v_bfe_u32 v8, v238, 1, 3\n"
      "v_xor_b32 v8, v8, v237\n"
      "v_lshl_add_u32 v8, v8, 4, s93\n"
      "v_mul_u32_u24 v134, 37, v238\n"
      "v_lshrrev_b32 v134, 8, v134\n"
      "v_mad_u32_u24 v19, v134, s95, v238\n"
      "v_mul_u32_u24 v19, 384, v19\n"
      "v_add_u32 v19, v19, v8\n"
      "s_add_u32 s92, s40, 16\n"
      "s_mul_i32 s93, s92, 43\n"
      "s_lshr_b32 s93, s93, 8\n"
      "s_mul_i32 s94, s93, 6\n"
      "s_sub_u32 s92, s92, s94\n"
      "s_lshl_b32 s92, s92, 3\n"
      "s_lshl_b32 s93, s93, 7\n"
      "s_cmp_lt_u32 s40, 2\n"
      "s_cselect_b32 s93, s93, 0x80000000\n"
      "v_add_u32 v238, s92, v236\n"
      "v_bfe_u32 v236, v238, 1, 3\n"
      "v_xor_b32_e64 v236, v236, v237\n"
      "v_lshl_add_u32 v236, v236, 4, s93\n"
      "v_mul_u32_u24_e64 v237, 37, v238\n"
      "v_lshrrev_b32_e64 v237, 8, v237\n"
      "v_mad_u32_u24 v15, v237, s95, v238\n"
      "v_mul_u32_u24 v15, 384, v15\n"
      "v_add_u32 v15, v15, v236\n"
      "s_add_u32 s92, s41, 0\n"
      "s_mul_i32 s93, s92, 58255\n"
      "s_lshr_b32 s93, s93, 22\n"
      "s_add_u32 s47, s92, s93\n"
      "s_mul_i32 s47, s47, 4802\n"
      "s_and_b32 s47, s47, -4\n"
      "s_mul_i32 s93, s93, 439\n"
      "s_add_u32 s45, s92, s93\n"
      "s_mul_i32 s45, s45, 2688\n"
      "s_add_u32 s45, s45, 589824\n"
      "s_add_u32 s92, s41, 256\n"
      "s_mul_i32 s93, s92, 58255\n"
      "s_lshr_b32 s93, s93, 22\n"
      "s_add_u32 s74, s92, s93\n"
      "s_mul_i32 s74, s74, 4802\n"
      "s_and_b32 s74, s74, -4\n"
      "s_mul_i32 s93, s93, 439\n"
      "s_add_u32 s46, s92, s93\n"
      "s_mul_i32 s46, s46, 2688\n"
      "s_add_u32 s46, s46, 589824\n"
      "s_add_u32 m0, s64, 0\n"
      "s_nop 0\n"
      "buffer_load_dwordx4 v16, s[16:19], s45 offen  lds\n"
      "s_add_u32 m0, s64, 4096\n"
      "s_nop 0\n"
      "buffer_load_dwordx4 v17, s[16:19], s45 offen  lds\n"
      "s_add_u32 m0, s64, 8192\n"
      "s_nop 0\n"
      "buffer_load_dwordx4 v18, s[16:19], s45 offen  lds\n"
      "s_add_u32 m0, s64, 12288\n"
      "s_nop 0\n"
      "buffer_load_dwordx4 v19, s[16:19], s45 offen  lds\n"
      "s_add_u32 m0, s65, 16384\n"
      "s_nop 0\n"
      "buffer_load_dwordx4 v15, s[16:19], s45 offen  lds\n"
      "v_subrev_u32 v236, 2, v133\n"
      "v_cmp_ge_u32 s[72:73], s44, v236\n"
      "v_min_u32_e64 v236, s44, v236\n"
      "s_movk_i32 s93, 4180\n"
      "s_cmp_lt_u32 s41, 209\n"
      "s_cselect_b32 s92, 209, 47\n"
      "s_cselect_b32 s93, 795, s93\n"
      "s_cmp_lt_u32 s41, 252\n"
      "s_cselect_b32 s94, 16, 15\n"
      "s_add_u32 s93, s93, s41\n"
      "v_lshl_add_u32 v238, v236, 8, s41\n"
      "v_mul_u32_u24 v237, s92, v236\n"
      "v_add_u32_e64 v237, s93, v237\n"
      "v_cmp_lt_u32 vcc, s94, v236\n"
      "v_subrev_u32 v8, 5184, v237\n"
      "s_nop 0\n"
      "v_cndmask_b32 v238, v238, v237, vcc\n"
      "v_cmp_le_u32 vcc, 5184, v238\n"
      "v_mul_u32_u24 v134, 58255, v238\n"
      "v_lshrrev_b32 v134, 22, v134\n"
      "v_mul_u32_u24 v132, 72, v134\n"
      "v_sub_u32_e64 v132, v238, v132\n"
      "v_min_u32 v237, 72, v8\n"
      "v_cndmask_b32 v134, v134, v237, vcc\n"
      "v_subrev_u32 v237, 72, v8\n"
      "v_min_u32 v237, 72, v237\n"
      "v_cndmask_b32 v132, v132, v237, vcc\n"
      "v_mul_u32_u24 v38, 73, v134\n"
      "v_add_u32 v38, v38, v132\n"
      "v_mul_u32_u24 v38, 4802, v38\n"
      "v_mul_u32_u24 v236, 0x1ff, v134\n"
      "v_add_u32_e64 v236, v236, v132\n"
      "v_mul_u32_u24 v236, 2688, v236\n"
      "v_add_u32 v236, 589824, v236\n"
      "v_cmp_eq_u32 vcc, 72, v134\n"
      "v_or_b32 v42, 0x80000000, v236\n"
      "s_nop 0\n"
      "v_cndmask_b32 v236, v236, v42, vcc\n"
      "v_cmp_eq_u32 vcc, 72, v132\n"
      "v_or_b32_e64 v42, 0x40000000, v236\n"
      "s_nop 0\n"
      "v_cndmask_b32 v236, v236, v42, vcc\n"
      "v_mov_b32 v8, 0x20000000\n"
      "v_cndmask_b32 v236, v8, v236, s[72:73]\n"
      "v_cndmask_b32 v38, v8, v38, s[72:73]\n"
      "v_mov_b32 v37, v8\n"
      "s_nop 1\n"
      "v_mov_b32_dpp v37, v236 wave_shr:1 row_mask:0xf bank_mask:0xf\n"
      "v_mov_b32 v79, 0\n"
      "v_mov_b32 v83, 0\n"
      "v_mov_b32 v87, 0\n"
      "v_mov_b32 v91, 0\n"
      "v_mov_b32 v95, 0\n"
      "s_lshl_b32 s94, s40, 2\n"
      "s_add_u32 s94, s94, 115456\n"
      "v_add_u32 v31, s94, v245\n"
      "v_and_b32 v42, 3, v133\n"
      "v_cmp_ne_u32 vcc, 0, v42\n"
      "v_mov_b32 v42, 0x3f803f80\n"
      "s_nop 0\n"
      "v_cndmask_b32_e64 v32, v42, 0, vcc\n"
      "v_mov_b32 v33, v32\n"
      "v_mov_b32 v34, v32\n"
      "v_mov_b32 v35, v32\n"
      "v_and_b32 v242, 15, v133\n"
      "v_lshrrev_b32_e64 v66, 4, v133\n"
      "v_mul_u32_u24 v242, 196, v242\n"
      "v_add_u32 v242, 124928, v242\n"
      "v_lshl_add_u32 v66, v66, 4, v242\n"
      "v_min_u32 v72, 192, v239\n"
      "v_add_u32 v72, 124928, v72\n"
      "global_load_dwordx4 v[236:239], v245, s[88:89]\n"
      "s_waitcnt lgkmcnt(0)\n"
      "s_pack_ll_b32_b16 s92, s0, s8\n"
      "s_waitcnt vmcnt(14)\n"
      "v_dot2_f32_bf16 v70, s92, v50, 0\n"
      "s_pack_hh_b32_b16 s93, s0, s8\n"
      "v_dot2_f32_bf16 v41, s93, v50, 0\n"
      "s_pack_ll_b32_b16 s94, s1, s9\n"
      "v_dot2_f32_bf16 v58, s94, v50, 0\n"
      "s_pack_hh_b32_b16 s95, s1, s9\n"
      "v_dot2_f32_bf16 v46, s95, v50, 0\n"
      "s_pack_ll_b32_b16 s92, s2, s10\n"
      "v_dot2_f32_bf16 v59, s92, v50, 0\n"
      "s_pack_hh_b32_b16 s93, s2, s10\n"
      "v_dot2_f32_bf16 v47, s93, v50, 0\n"
      "s_pack_ll_b32_b16 s94, s3, s11\n"
      "v_dot2_f32_bf16 v74, s94, v50, 0\n"
      "s_pack_hh_b32_b16 s95, s3, s11\n"
      "v_dot2_f32_bf16 v71, s95, v50, 0\n"
      "s_pack_ll_b32_b16 s92, s4, s12\n"
      "v_dot2_f32_bf16 v40, s92, v50, 0\n"
      "s_pack_hh_b32_b16 s93, s4, s12\n"
      "v_dot2_f32_bf16 v45, s93, v50, 0\n"
      "s_pack_ll_b32_b16 s94, s5, s13\n"
      "v_dot2_f32_bf16 v75, s94, v50, 0\n"
      "global_load_dwordx4 v[132:135], v245, s[88:89] offset:1024\n"
      "s_pack_hh_b32_b16 s95, s5, s13\n"
      "v_dot2_f32_bf16 v69, s95, v50, 0\n"
      "s_pack_ll_b32_b16 s92, s6, s14\n"
      "v_dot2_f32_bf16 v51, s92, v50, 0\n"
      "s_pack_hh_b32_b16 s93, s6, s14\n"
      "v_dot2_f32_bf16 v61, s93, v50, 0\n"
      "s_pack_ll_b32_b16 s94, s7, s15\n"
      "v_dot2_f32_bf16 v55, s94, v50, 0\n"
      "s_pack_hh_b32_b16 s95, s7, s15\n"
      "v_dot2_f32_bf16 v73, s95, v50, 0\n"
      "s_lshl_b32 s92, s40, 4\n"
      "s_lshr_b64 s[66:67], s[66:67], s92\n"
      "s_lshl_b32 s66, s66, 16\n"
      "v_mov_b32 v50, s66\n"
      "v_max_f32 v50, 0xc2c80000, v50\n"
      "v_min_f32 v50, 0x40930000, v50\n"
      "v_mul_f32 v50, 0x3fb8aa3b, v50\n"
      "v_exp_f32 v50, v50\n"
      "s_nop 0\n"
      "v_cvt_pk_bf16_f32 v50, v50, v50\n"
      "v_lshlrev_b32 v50, 16, v50\n"
      "v_max_f32 v70, 0, v70\n"
      "v_max_f32_e64 v41, 0, v41\n"
      "global_load_dwordx4 v[0:3], v245, s[88:89] offset:2048\n"
      "v_cvt_pk_bf16_f32 v70, v70, v41\n"
      "v_max_f32 v58, 0, v58\n"
      "v_max_f32 v46, 0, v46\n"
      "v_cvt_pk_bf16_f32 v58, v58, v46\n"
      "v_max_f32 v59, 0, v59\n"
      "v_max_f32 v47, 0, v47\n"
      "v_cvt_pk_bf16_f32 v59, v59, v47\n"
      "v_max_f32 v74, 0, v74\n"
      "v_max_f32 v71, 0, v71\n"
      "v_cvt_pk_bf16_f32 v74, v74, v71\n"
      "v_max_f32 v40, 0, v40\n"
      "v_max_f32 v45, 0, v45\n"
      "v_cvt_pk_bf16_f32 v40, v40, v45\n"
      "v_max_f32 v75, 0, v75\n"
      "v_max_f32 v69, 0, v69\n"
      "v_cvt_pk_bf16_f32 v75, v75, v69\n"
      "v_max_f32 v51, 0, v51\n"
      "v_max_f32 v61, 0, v61\n"
      "v_cvt_pk_bf16_f32 v51, v51, v61\n"
      "v_max_f32 v55, 0, v55\n"
      "v_max_f32 v73, 0, v73\n"
      "v_cvt_pk_bf16_f32 v55, v55, v73\n"
      "s_mov_b32 s42, 0\n"
      "s_add_u32 m0, s75, 0\n"
      "s_nop 0\n"
      "buffer_load_dwordx4 v179, s[20:23], s42 offen  lds\n"
      "s_pack_ll_b32_b16 s92, s48, s50\n"
      "v_dot2_f32_bf16 v71, s92, v70, 0\n"
      "s_pack_hh_b32_b16 s93, s48, s50\n"
      "v_dot2_f32_bf16 v47, s93, v70, 0\n"
      "s_pack_ll_b32_b16 s94, s49, s51\n"
      "v_dot2_f32_bf16 v46, s94, v70, 0\n"
      "s_pack_hh_b32_b16 s95, s49, s51\n"
      "v_dot2_f32_bf16 v41, s95, v70, 0\n"
      "s_pack_ll_b32_b16 s92, s52, s54\n"
      "v_dot2_f32_bf16 v71, s92, v58, v71\n"
      "s_pack_hh_b32_b16 s93, s52, s54\n"
      "v_dot2_f32_bf16 v47, s93, v58, v47\n"
      "s_pack_ll_b32_b16 s94, s53, s55\n"
      "v_dot2_f32_bf16 v46, s94, v58, v46\n"
      "s_pack_hh_b32_b16 s95, s53, s55\n"
      "v_dot2_f32_bf16 v41, s95, v58, v41\n"
      "s_pack_ll_b32_b16 s92, s56, s58\n"
      "v_dot2_f32_bf16 v71, s92, v59, v71\n"
      "s_pack_hh_b32_b16 s93, s56, s58\n"
      "v_dot2_f32_bf16 v47, s93, v59, v47\n"
      "s_pack_ll_b32_b16 s94, s57, s59\n"
      "v_dot2_f32_bf16 v46, s94, v59, v46\n"
      "s_add_u32 m0, s75, 3968\n"
      "s_nop 0\n"
      "buffer_load_dwordx4 v179, s[20:23], s42 offen offset:128  lds\n"
      "s_pack_hh_b32_b16 s95, s57, s59\n"
      "v_dot2_f32_bf16 v41, s95, v59, v41\n"
      "s_pack_ll_b32_b16 s92, s60, s62\n"
      "v_dot2_f32_bf16 v71, s92, v74, v71\n"
      "s_pack_hh_b32_b16 s93, s60, s62\n"
      "v_dot2_f32_bf16 v47, s93, v74, v47\n"
      "s_pack_ll_b32_b16 s94, s61, s63\n"
      "v_dot2_f32_bf16 v46, s94, v74, v46\n"
      "s_pack_hh_b32_b16 s95, s61, s63\n"
      "v_dot2_f32_bf16 v41, s95, v74, v41\n"
      "s_pack_ll_b32_b16 s92, s24, s26\n"
      "v_dot2_f32_bf16 v71, s92, v40, v71\n"
      "s_pack_hh_b32_b16 s93, s24, s26\n"
      "v_dot2_f32_bf16 v47, s93, v40, v47\n"
      "s_pack_ll_b32_b16 s94, s25, s27\n"
      "v_dot2_f32_bf16 v46, s94, v40, v46\n"
      "s_pack_hh_b32_b16 s95, s25, s27\n"
      "v_dot2_f32_bf16 v41, s95, v40, v41\n"
      "s_pack_ll_b32_b16 s92, s28, s30\n"
      "v_dot2_f32_bf16 v71, s92, v75, v71\n"
      "s_pack_hh_b32_b16 s93, s28, s30\n"
      "v_dot2_f32_bf16 v47, s93, v75, v47\n"
      "s_pack_ll_b32_b16 s94, s29, s31\n"
      "s_add_u32 m0, s75, 7936\n"
      "s_nop 0\n"
      "buffer_load_dwordx4 v179, s[20:23], s42 offen offset:256  lds\n"
      "v_dot2_f32_bf16 v46, s94, v75, v46\n"
      "s_pack_hh_b32_b16 s95, s29, s31\n"
      "v_dot2_f32_bf16 v41, s95, v75, v41\n"
      "s_pack_ll_b32_b16 s92, s32, s34\n"
      "v_dot2_f32_bf16 v71, s92, v51, v71\n"
      "s_pack_hh_b32_b16 s93, s32, s34\n"
      "v_dot2_f32_bf16 v47, s93, v51, v47\n"
      "s_pack_ll_b32_b16 s94, s33, s35\n"
      "v_dot2_f32_bf16 v46, s94, v51, v46\n"
      "s_pack_hh_b32_b16 s95, s33, s35\n"
      "v_dot2_f32_bf16 v41, s95, v51, v41\n"
      "s_pack_ll_b32_b16 s92, s36, s38\n"
      "v_dot2_f32_bf16 v71, s92, v55, v71\n"
      "s_pack_hh_b32_b16 s93, s36, s38\n"
      "v_dot2_f32_bf16 v47, s93, v55, v47\n"
      "s_pack_ll_b32_b16 s94, s37, s39\n"
      "v_dot2_f32_bf16 v46, s94, v55, v46\n"
      "s_pack_hh_b32_b16 s95, s37, s39\n"
      "v_dot2_f32_bf16 v41, s95, v55, v41\n"
      "s_load_dwordx2 s[28:29], s[90:91], 8\n"
      "s_load_dwordx2 s[34:35], s[90:91], 16\n"
      "s_load_dwordx2 s[24:25], s[90:91], 24\n"
      "s_add_u32 m0, s75, 11904\n"
      "s_nop 0\n"
      "buffer_load_dwordx4 v179, s[20:23], s42 offen offset:384  lds\n"
      "s_load_dwordx2 s[36:37], s[90:91], 40\n"
      "s_mov_b32 s26, 100270464\n"
      "s_mov_b32 s27, 0x20000\n"
      "s_mov_b32 s38, 73728\n"
      "s_mov_b32 s39, 0x20000\n"
      "s_mov_b32 s30, 25589860\n"
      "s_mov_b32 s31, 0x20000\n"
      "s_mov_b32 s53, 0x3fb8aa3b\n"
      "s_mov_b32 s51, 0x2a8cbccc\n"
      "s_mov_b32 s52, 0x38fba882\n"
      "s_mov_b32 s58, 0xffff0000\n"
      "s_mov_b32 s59, 0x0\n"
      "s_mov_b32 s60, 0x0\n"
      "s_mov_b32 s61, 0xffff\n"
      "s_mov_b32 s62, 0x0\n"
      "s_mov_b32 s63, 0xffff0000\n"
      "v_mov_b32 v42, 0x41f00000\n"
      "v_cmp_nle_f32 s[54:55], v50, v42\n"
      "v_mul_f32 v50, s53, v50\n"
      "s_nop 0\n"
      "v_readfirstlane_b32 s50, v50\n"
      "v_cvt_pk_bf16_f32 v71, v71, v71\n"
      "v_lshlrev_b32 v71, 16, v71\n"
      "s_add_u32 m0, s75, 15872\n"
      "s_nop 0\n"
      "buffer_load_dwordx4 v179, s[20:23], s42 offen offset:512  lds\n"
      "v_mul_f32 v71, 0xbfb8aa3b, v71\n"
      "v_exp_f32 v71, v71\n"
      "s_nop 0\n"
      "v_add_f32 v71, 1.0, v71\n"
      "v_rcp_f32 v71, v71\n"
      "s_nop 0\n"
      "v_cvt_pk_bf16_f32 v71, v71, v71\n"
      "v_lshlrev_b32 v71, 16, v71\n"
      "v_add_f32 v71, -0.5, v71\n"
      "v_mul_f32 v71, 0x41b8aa3b, v71\n"
      "ds_write_b32 v43, v71\n"
      "v_cvt_pk_bf16_f32 v47, v47, v47\n"
      "v_lshlrev_b32 v47, 16, v47\n"
      "v_mul_f32 v47, 0xbfb8aa3b, v47\n"
      "v_exp_f32 v47, v47\n"
      "s_nop 0\n"
      "v_add_f32 v47, 1.0, v47\n"
      "v_rcp_f32_e64 v47, v47\n"
      "s_nop 0\n"
      "v_cvt_pk_bf16_f32 v47, v47, v47\n"
      "v_lshlrev_b32 v47, 16, v47\n"
      "v_add_f32 v47, -0.5, v47\n"
      "v_mul_f32 v47, 0x41b8aa3b, v47\n"
      "ds_write_b32 v43, v47 offset:704\n"
      "v_cvt_pk_bf16_f32 v46, v46, v46\n"
      "v_lshlrev_b32 v46, 16, v46\n"
      "s_add_u32 m0, s75, 19840\n"
      "s_nop 0\n"
      "buffer_load_dwordx4 v179, s[20:23], s42 offen offset:640  lds\n"
      "v_mul_f32 v46, 0xbfb8aa3b, v46\n"
      "v_exp_f32 v46, v46\n"
      "s_nop 0\n"
      "v_add_f32 v46, 1.0, v46\n"
      "v_rcp_f32_e64 v46, v46\n"
      "s_nop 0\n"
      "v_cvt_pk_bf16_f32 v46, v46, v46\n"
      "v_lshlrev_b32 v46, 16, v46\n"
      "v_add_f32 v46, -0.5, v46\n"
      "v_mul_f32 v46, 0x41b8aa3b, v46\n"
      "ds_write_b32 v43, v46 offset:1408\n"
      "v_cvt_pk_bf16_f32 v41, v41, v41\n"
      "v_lshlrev_b32 v41, 16, v41\n"
      "v_mul_f32 v41, 0xbfb8aa3b, v41\n"
      "v_exp_f32 v41, v41\n"
      "s_nop 0\n"
      "v_add_f32 v41, 1.0, v41\n"
      "v_rcp_f32 v41, v41\n"
      "s_nop 0\n"
      "v_cvt_pk_bf16_f32 v41, v41, v41\n"
      "v_lshlrev_b32 v41, 16, v41\n"
      "v_add_f32_e64 v41, -0.5, v41\n"
      "v_mul_f32 v41, 0x41b8aa3b, v41\n"
      "ds_write_b32 v43, v41 offset:2112\n"
      "s_mul_i32 s76, s40, 704\n"
      "s_add_u32 s76, s76, 122112\n"
      "s_waitcnt vmcnt(17)\n"
      "s_waitcnt lgkmcnt(0)\n"
      "s_barrier\n"
      "global_load_dwordx4 v[4:7], v245, s[88:89] offset:3072\n"
      "v_add_u32 v243, 6272, v66\n"
      "v_add_u32 v64, 3136, v66\n"
      "s_mov_b32 s73, 0x0\n"
      "ds_read2_b32 v[58:59], v64 offset0:18 offset1:19\n"
      "ds_read2_b32 v[70:71], v243 offset0:18 offset1:19\n"
      "ds_read2_b32 v[42:43], v66 offset0:2 offset1:3\n"
      "ds_read2_b32 v[46:47], v66 offset0:18 offset1:19\n"
      "s_add_u32 s88, s88, 4096\n"
      "s_addc_u32 s89, s89, 0\n"
      "global_load_dwordx4 v[8:11], v245, s[88:89]\n"
      "ds_read2_b32 v[50:51], v66 offset0:34 offset1:35\n"
      "ds_read2_b32 v[40:41], v66 offset0:0 offset1:1\n"
      "ds_read2_b32 v[74:75], v243 offset0:34 offset1:35\n"
      "ds_read2_b32 v[52:53], v64 offset0:0 offset1:1\n"
      "ds_read2_b32 v[54:55], v64 offset0:2 offset1:3\n"
      "ds_read2_b32 v[68:69], v243 offset0:16 offset1:17\n"
      "ds_read2_b32 v[60:61], v64 offset0:32 offset1:33\n"
      "global_load_dwordx4 v[12:15], v245, s[88:89] offset:1024\n"
      "ds_read_b32 v194, v242 offset:192\n"
      "ds_read_b32 v215, v242 offset:6464\n"
      "ds_read2_b32 v[44:45], v66 offset0:16 offset1:17\n"
      "ds_read_b32 v234, v72 offset:9408\n"
      "ds_read2_b32 v[62:63], v64 offset0:34 offset1:35\n"
      "ds_read2_b32 v[72:73], v243 offset0:32 offset1:33\n"
      "ds_read2_b32 v[48:49], v66 offset0:32 offset1:33\n"
      "global_load_dwordx4 v[20:23], v245, s[88:89] offset:2048\n"
      "ds_read2_b32 v[66:67], v243 offset0:2 offset1:3\n"
      "ds_read2_b32 v[56:57], v64 offset0:16 offset1:17\n"
      "ds_read2_b32 v[64:65], v243 offset0:0 offset1:1\n"
      "ds_read_b32 v243, v242 offset:3328\n"
      "s_waitcnt lgkmcnt(11)\n"
      "v_lshl_add_u32 v59, v59, 2, s76\n"
      "v_lshl_add_u32 v46, v46, 2, s76\n"
      "global_load_dwordx4 v[24:27], v245, s[88:89] offset:3072\n"
      "v_lshl_add_u32 v61, v61, 2, s76\n"
      "v_lshl_add_u32 v47, v47, 2, s76\n"
      "v_lshl_add_u32 v51, v51, 2, s76\n"
      "v_lshl_add_u32 v41, v41, 2, s76\n"
      "v_lshl_add_u32 v68, v68, 2, s76\n"
      "v_lshl_add_u32 v71, v71, 2, s76\n"
      "v_lshl_add_u32 v50, v50, 2, s76\n"
      "s_mov_b32 s43, 36864\n"
      "s_add_u32 m0, s75, 24576\n"
      "s_nop 0\n"
      "buffer_load_dwordx4 v179, s[20:23], s43 offen  lds\n"
      "v_lshl_add_u32 v70, v70, 2, s76\n"
      "v_lshl_add_u32 v74, v74, 2, s76\n"
      "v_lshl_add_u32 v60, v60, 2, s76\n"
      "v_lshl_add_u32 v58, v58, 2, s76\n"
      "v_lshl_add_u32 v54, v54, 2, s76\n"
      "v_lshl_add_u32 v52, v52, 2, s76\n"
      "s_add_u32 m0, s75, 28544\n"
      "s_nop 0\n"
      "buffer_load_dwordx4 v179, s[20:23], s43 offen offset:128  lds\n"
      "v_lshl_add_u32 v42, v42, 2, s76\n"
      "v_lshl_add_u32 v69, v69, 2, s76\n"
      "v_lshl_add_u32 v43, v43, 2, s76\n"
      "v_lshl_add_u32 v53, v53, 2, s76\n"
      "v_lshl_add_u32 v40, v40, 2, s76\n"
      "v_lshl_add_u32 v55, v55, 2, s76\n"
      "s_waitcnt lgkmcnt(0)\n"
      "s_add_u32 m0, s75, 32512\n"
      "s_nop 0\n"
      "buffer_load_dwordx4 v179, s[20:23], s43 offen offset:256  lds\n"
      "v_lshl_add_u32 v56, v56, 2, s76\n"
      "v_lshl_add_u32 v62, v62, 2, s76\n"
      "v_lshl_add_u32 v44, v44, 2, s76\n"
      "v_lshl_add_u32 v194, v194, 2, s76\n"
      "v_lshl_add_u32 v64, v64, 2, s76\n"
      "v_lshl_add_u32 v234, v234, 2, s76\n"
      "v_lshl_add_u32 v215, v215, 2, s76\n"
      "s_add_u32 m0, s75, 36480\n"
      "s_nop 0\n"
      "buffer_load_dwordx4 v179, s[20:23], s43 offen offset:384  lds\n"
      "v_lshl_add_u32 v73, v73, 2, s76\n"
      "v_lshl_add_u32 v48, v48, 2, s76\n"
      "v_lshl_add_u32 v49, v49, 2, s76\n"
      "v_lshl_add_u32 v72, v72, 2, s76\n"
      "v_lshl_add_u32 v243, v243, 2, s76\n"
      "v_lshl_add_u32 v66, v66, 2, s76\n"
      "v_lshl_add_u32 v75, v75, 2, s76\n"
      "s_add_u32 m0, s75, 40448\n"
      "s_nop 0\n"
      "buffer_load_dwordx4 v179, s[20:23], s43 offen offset:512  lds\n"
      "v_lshl_add_u32 v45, v45, 2, s76\n"
      "v_lshl_add_u32 v63, v63, 2, s76\n"
      "v_lshl_add_u32 v65, v65, 2, s76\n"
      "v_lshl_add_u32 v67, v67, 2, s76\n"
      "v_lshl_add_u32 v57, v57, 2, s76\n"
      "s_mov_b32 s67, 0x1ffff\n"
      "ds_read_b32 v53, v53\n"
      "s_add_u32 m0, s75, 44416\n"
      "s_nop 0\n"
      "buffer_load_dwordx4 v179, s[20:23], s43 offen offset:640  lds\n"
      "ds_read_b32 v234, v234\n"
      "ds_read_b32 v40, v40\n"
      "ds_read_b32 v57, v57\n"
      "ds_read_b32 v51, v51\n"
      "ds_read_b32 v55, v55\n"
      "ds_read_b32 v60, v60\n"
      "ds_read_b32 v59, v59\n"
      "s_mov_b32 s42, 73728\n"
      "s_add_u32 m0, s75, 49152\n"
      "s_nop 0\n"
      "buffer_load_dwordx4 v179, s[20:23], s42 offen  lds\n"
      "ds_read_b32 v68, v68\n"
      "ds_read_b32 v71, v71\n"
      "ds_read_b32 v243, v243\n"
      "ds_read_b32 v47, v47\n"
      "ds_read_b32 v50, v50\n"
      "ds_read_b32 v45, v45\n"
      "s_add_u32 m0, s75, 53120\n"
      "s_nop 0\n"
      "buffer_load_dwordx4 v179, s[20:23], s42 offen offset:128  lds\n"
      "ds_read_b32 v49, v49\n"
      "ds_read_b32 v41, v41\n"
      "ds_read_b32 v75, v75\n"
      "ds_read_b32 v194, v194\n"
      "ds_read_b32 v67, v67\n"
      "ds_read_b32 v215, v215\n"
      "ds_read_b32 v46, v46\n"
      "s_add_u32 m0, s75, 57088\n"
      "s_nop 0\n"
      "buffer_load_dwordx4 v179, s[20:23], s42 offen offset:256  lds\n"
      "ds_read_b32 v42, v42\n"
      "ds_read_b32 v66, v66\n"
      "ds_read_b32 v54, v54\n"
      "ds_read_b32 v62, v62\n"
      "ds_read_b32 v61, v61\n"
      "ds_read_b32 v69, v69\n"
      "ds_read_b32 v64, v64\n"
      "s_add_u32 m0, s75, 61056\n"
      "s_nop 0\n"
      "buffer_load_dwordx4 v179, s[20:23], s42 offen offset:384  lds\n"
      "ds_read_b32 v48, v48\n"
      "ds_read_b32 v73, v73\n"
      "ds_read_b32 v44, v44\n"
      "ds_read_b32 v58, v58\n"
      "ds_read_b32 v56, v56\n"
      "ds_read_b32 v63, v63\n"
      "ds_read_b32 v52, v52\n"
      "s_add_u32 m0, s75, 65024\n"
      "s_nop 0\n"
      "buffer_load_dwordx4 v179, s[20:23], s42 offen offset:512  lds\n"
      "ds_read_b32 v65, v65\n"
      "ds_read_b32 v74, v74\n"
      "ds_read_b32 v72, v72\n"
      "ds_read_b32 v70, v70\n"
      "ds_read_b32 v43, v43\n"
      "s_mov_b32 s66, 0xffffffff\n"
      "v_mov_b32 v245, 0xf149f2ca\n"
      "s_add_u32 m0, s75, 68992\n"
      "s_nop 0\n"
      "buffer_load_dwordx4 v179, s[20:23], s42 offen offset:640  lds\n"
      "s_waitcnt lgkmcnt(10)\n"
      "v_cndmask_b32 v30, v245, v234, s[66:67]\n"
      "s_mov_b32 s72, 0xffff\n"
      "v_cndmask_b32 v234, v245, v194, s[72:73]\n"
      "v_cndmask_b32 v28, v245, v215, s[72:73]\n"
      "v_cndmask_b32 v215, v245, v243, s[72:73]\n"
      "s_mov_b32 s45, s46\n"
      "s_mov_b32 s49, s47\n"
      "s_mov_b32 s66, 0\n"
      "s_mov_b32 s67, 0\n"
      "s_mov_b32 s68, 0\n"
      "s_mov_b32 s69, 0\n"
      "s_mov_b32 s70, 0\n"
      "s_mov_b32 s71, 0\n"
      "s_mov_b32 s48, 0\n"
      "s_mov_b32 s42, 1\n"
      "s_mov_b32 s43, 5\n"
      "v_readlane_b32 s47, v38, 4\n"
      "s_nop 0\n"
      "s_and_b32 s47, s47, -4\n"
      "s_waitcnt vmcnt(23)\n"
      "v_add_u32 v158, 149504, v0\n"
      "v_add_u32 v156, 149504, v1\n"
      "v_add_u32 v101, 49152, v236\n"
      "v_add_u32 v103, 49152, v237\n"
      "v_add_u32 v98, 49152, v238\n"
      "v_add_u32 v78, 49152, v239\n"
      "v_add_u32 v86, 49152, v132\n"
      "s_waitcnt vmcnt(12)\n"
      "v_mov_b32 v24, v234\n"
      "v_mov_b32_e64 v26, v215\n"
      "s_waitcnt lgkmcnt(0)\n"
      "s_barrier\n"
      "s_add_u32 m0, s75, 73728\n"
      "s_mov_b32 s79, 110592\n"
      "ds_read_b128 a[220:223], v0\n"
      "ds_read_b64_tr_b16 a[0:1], v236\n"
      "buffer_load_dwordx4 v179, s[20:23], s79 offen  lds\n"
      "s_add_u32 m0, s75, 77696\n"
      "ds_read_b128 a[240:243], v158 offset:4096\n"
      "ds_read_b64_tr_b16 a[48:49], v238\n"
      "ds_read_b64_tr_b16 a[122:123], v238 offset:12800\n"
      "ds_read_b64_tr_b16 a[98:99], v237 offset:12800\n"
      "ds_read_b64_tr_b16 a[50:51], v238 offset:512\n"
      "buffer_load_dwordx4 v179, s[20:23], s79 offen offset:128  lds\n"
      "ds_read_b128 a[224:227], v0 offset:2048\n"
      "ds_read_b128 a[216:219], v158\n"
      "ds_read_b64_tr_b16 a[96:97], v237 offset:12288\n"
      "s_add_u32 m0, s75, 81664\n"
      "ds_read_b64_tr_b16 a[74:75], v236 offset:12800\n"
      "ds_read_b128 a[232:235], v1\n"
      "ds_read_b64_tr_b16 a[24:25], v237\n"
      "buffer_load_dwordx4 v179, s[20:23], s79 offen offset:256  lds\n"
      "ds_read_b64_tr_b16 a[72:73], v236 offset:12288\n"
      "ds_read_b64_tr_b16 a[120:121], v238 offset:12288\n"
      "s_add_u32 m0, s75, 85632\n"
      "s_waitcnt lgkmcnt(5)\n"
      "v_mfma_f32_16x16x32_bf16 v[242:245], a[96:99], a[220:223], 0\n"
      "ds_read_b64_tr_b16 a[26:27], v237 offset:512\n"
      "buffer_load_dwordx4 v179, s[20:23], s79 offen offset:384  lds\n"
      "s_add_u32 m0, s75, 89600\n"
      "ds_read_b64_tr_b16 a[2:3], v236 offset:512\n"
      "v_mfma_f32_16x16x32_bf16 v[212:215], a[48:51], a[220:223], 0\n"
      "buffer_load_dwordx4 v179, s[20:23], s79 offen offset:512  lds\n"
      "s_add_u32 m0, s75, 93568\n"
      "s_waitcnt lgkmcnt(2)\n"
      "v_mfma_f32_16x16x32_bf16 v[232:235], a[120:123], a[216:219], 0\n"
      "ds_read_b128 a[228:231], v156\n"
      "v_mfma_f32_16x16x32_bf16 v[192:195], a[48:51], a[224:227], 0\n"
      "ds_read_b128 a[248:251], v0 offset:8192\n"
      "buffer_load_dwordx4 v179, s[20:23], s79 offen offset:640  lds\n"
      "s_add_u32 m0, s75, 0\n"
      "v_mfma_f32_16x16x32_bf16 v[144:147], a[72:75], a[220:223], 0\n"
      "s_waitcnt lgkmcnt(2)\n"
      "s_waitcnt vmcnt(12)\n"
      "s_barrier\n"
      "v_mfma_f32_16x16x32_bf16 v[246:249], a[24:27], a[220:223], 0\n"
      "ds_read_b64_tr_b16 a[124:125], v238 offset:36864\n"
      "ds_read_b64_tr_b16 a[126:127], v238 offset:37376\n"
      "v_mfma_f32_16x16x32_bf16 v[250:253], a[24:27], a[216:219], 0\n"
      "ds_read_b64_tr_b16 a[102:103], v237 offset:37376\n"
      "ds_read_b64_tr_b16 a[78:79], v236 offset:37376\n"
      "s_mov_b32 s78, 147456\n"
      "ds_read_b64_tr_b16 a[30:31], v237 offset:25088\n"
      "buffer_load_dwordx4 v179, s[20:23], s78 offen  lds\n"
      "s_add_u32 m0, s75, 3968\n"
      "ds_read_b64_tr_b16 a[28:29], v237 offset:24576\n"
      "ds_read_b64_tr_b16 a[76:77], v236 offset:36864\n"
      "v_mfma_f32_16x16x32_bf16 v[224:227], a[96:99], a[216:219], 0\n"
      "ds_read_b64_tr_b16 a[52:53], v238 offset:24576\n"
      "v_mfma_f32_16x16x32_bf16 v[228:231], a[48:51], a[216:219], 0\n"
      "ds_read_b64_tr_b16 a[100:101], v237 offset:36864\n"
      "ds_read_b64_tr_b16 a[6:7], v236 offset:25088\n"
      "buffer_load_dwordx4 v179, s[20:23], s78 offen offset:128  lds\n"
      "ds_read_b64_tr_b16 a[4:5], v236 offset:24576\n"
      "v_mfma_f32_16x16x32_bf16 v[208:211], a[0:3], a[220:223], 0\n"
      "s_add_u32 m0, s75, 7936\n"
      "ds_read_b128 v[216:219], v156\n"
      "v_mfma_f32_16x16x32_bf16 v[184:187], a[120:123], a[220:223], 0\n"
      "ds_read_b64_tr_b16 a[54:55], v238 offset:25088\n"
      "ds_read_b128 v[160:163], v158 offset:2048\n"
      "s_waitcnt lgkmcnt(7)\n"
      "v_mfma_f32_16x16x32_bf16 v[184:187], a[124:127], a[232:235], v[184:187]\n"
      "ds_read_b128 a[236:239], v1 offset:2048\n"
      "ds_read_b128 a[244:247], v0 offset:6144\n"
      "buffer_load_dwordx4 v179, s[20:23], s78 offen offset:256  lds\n"
      "s_add_u32 m0, s75, 11904\n"
      "v_mfma_f32_16x16x32_bf16 v[188:191], a[72:75], a[216:219], 0\n"
      "v_mfma_f32_16x16x32_bf16 v[188:191], a[76:79], a[228:231], v[188:191]\n"
      "s_waitcnt lgkmcnt(5)\n"
      "v_mfma_f32_16x16x32_bf16 v[166:169], a[120:123], a[224:227], 0\n"
      "buffer_load_dwordx4 v179, s[20:23], s78 offen offset:384  lds\n"
      "v_mfma_f32_16x16x32_bf16 v[204:207], a[0:3], a[216:219], 0\n"
      "v_mfma_f32_16x16x32_bf16 v[204:207], a[4:7], a[228:231], v[204:207]\n"
      "s_add_u32 m0, s75, 15872\n"
      "v_mfma_f32_16x16x32_bf16 v[108:111], a[72:75], a[224:227], 0\n"
      "s_waitcnt lgkmcnt(0)\n"
      "v_mfma_f32_16x16x32_bf16 v[108:111], a[76:79], a[236:239], v[108:111]\n"
      "buffer_load_dwordx4 v179, s[20:23], s78 offen offset:512  lds\n"
      "v_mfma_f32_16x16x32_bf16 v[172:175], a[0:3], a[224:227], 0\n"
      "s_add_u32 m0, s75, 19840\n"
      "v_mfma_f32_16x16x32_bf16 v[172:175], a[4:7], a[236:239], v[172:175]\n"
      "s_waitcnt vmcnt(11)\n"
      "s_barrier\n"
      "ds_read_b64_tr_b16 a[34:35], v103 offset:512\n"
      "ds_read_b64_tr_b16 a[106:107], v103 offset:12800\n"
      "ds_read_b64_tr_b16 a[104:105], v103 offset:12288\n"
      "ds_read_b64_tr_b16 a[80:81], v101 offset:12288\n"
      "ds_read_b64_tr_b16 a[32:33], v103\n"
      "ds_read_b64_tr_b16 a[10:11], v101 offset:512\n"
      "ds_read_b64_tr_b16 a[8:9], v101\n"
      "ds_read_b64_tr_b16 a[82:83], v101 offset:12800\n"
      "ds_read_b64_tr_b16 a[128:129], v98 offset:12288\n"
      "ds_read_b64_tr_b16 a[58:59], v98 offset:512\n"
      "ds_read_b64_tr_b16 a[56:57], v98\n"
      "ds_read_b64_tr_b16 a[130:131], v98 offset:12800\n"
      "v_mfma_f32_16x16x32_bf16 v[124:127], a[96:99], a[224:227], 0\n"
      "buffer_load_dwordx4 v179, s[20:23], s78 offen offset:640  lds\n"
      "s_mov_b32 s79, 184320\n"
      "v_mfma_f32_16x16x32_bf16 v[124:127], a[100:103], a[236:239], v[124:127]\n"
      "s_waitcnt lgkmcnt(8)\n"
      "v_mfma_f32_16x16x32_bf16 v[224:227], a[100:103], a[228:231], v[224:227]\n"
      "s_waitcnt lgkmcnt(6)\n"
      "v_mfma_f32_16x16x32_bf16 v[224:227], a[104:107], a[240:243], v[224:227]\n"
      "s_add_u32 m0, s75, 24576\n"
      "v_mfma_f32_16x16x32_bf16 v[212:215], a[52:55], a[232:235], v[212:215]\n"
      "buffer_load_dwordx4 v179, s[20:23], s79 offen  lds\n"
      "v_mfma_f32_16x16x32_bf16 v[246:249], a[28:31], a[232:235], v[246:249]\n"
      "s_waitcnt lgkmcnt(0)\n"
      "v_mfma_f32_16x16x32_bf16 v[246:249], a[32:35], a[244:247], v[246:249]\n"
      "s_add_u32 m0, s75, 28544\n"
      "v_mfma_f32_16x16x32_bf16 v[180:183], a[24:27], a[224:227], 0\n"
      "ds_read_b128 a[216:219], v1 offset:6144\n"
      "v_mfma_f32_16x16x32_bf16 v[180:183], a[28:31], a[236:239], v[180:183]\n"
      "buffer_load_dwordx4 v179, s[20:23], s79 offen offset:128  lds\n"
      "v_mfma_f32_16x16x32_bf16 v[180:183], a[32:35], a[248:251], v[180:183]\n"
      "ds_read_b128 a[220:223], v1 offset:8192\n"
      "v_mfma_f32_16x16x32_bf16 v[208:211], a[4:7], a[232:235], v[208:211]\n"
      "v_mfma_f32_16x16x32_bf16 v[208:211], a[8:11], a[244:247], v[208:211]\n"
      "ds_read_b128 a[252:255], v156 offset:4096\n"
      "s_add_u32 m0, s75, 32512\n"
      "v_mfma_f32_16x16x32_bf16 v[184:187], a[128:131], a[244:247], v[184:187]\n"
      "buffer_load_dwordx4 v179, s[20:23], s79 offen offset:256  lds\n"
      "v_mfma_f32_16x16x32_bf16 v[166:169], a[124:127], a[236:239], v[166:169]\n"
      "v_mfma_f32_16x16x32_bf16 v[166:169], a[128:131], a[248:251], v[166:169]\n"
      "s_add_u32 m0, s75, 36480\n"
      "v_mfma_f32_16x16x32_bf16 v[232:235], a[124:127], a[228:231], v[232:235]\n"
      "v_mfma_f32_16x16x32_bf16 v[232:235], a[128:131], a[240:243], v[232:235]\n"
      "buffer_load_dwordx4 v179, s[20:23], s79 offen offset:384  lds\n"
      "v_mfma_f32_16x16x32_bf16 v[228:231], a[52:55], a[228:231], v[228:231]\n"
      "s_add_u32 m0, s75, 40448\n"
      "v_mfma_f32_16x16x32_bf16 v[228:231], a[56:59], a[240:243], v[228:231]\n"
      "buffer_load_dwordx4 v179, s[20:23], s79 offen offset:512  lds\n"
      "v_mfma_f32_16x16x32_bf16 v[250:253], a[28:31], a[228:231], v[250:253]\n"
      "v_mfma_f32_16x16x32_bf16 v[250:253], a[32:35], a[240:243], v[250:253]\n"
      "s_add_u32 m0, s75, 44416\n"
      "v_mfma_f32_16x16x32_bf16 v[242:245], a[100:103], a[232:235], v[242:245]\n"
      "v_mfma_f32_16x16x32_bf16 v[242:245], a[104:107], a[244:247], v[242:245]\n"
      "buffer_load_dwordx4 v179, s[20:23], s79 offen offset:640  lds\n"
      "v_mfma_f32_16x16x32_bf16 v[204:207], a[8:11], a[240:243], v[204:207]\n"
      "s_waitcnt vmcnt(12)\n"
      "s_barrier\n"
      "ds_read_b64_tr_b16 a[84:85], v101 offset:36864\n"
      "ds_read_b64_tr_b16 a[12:13], v101 offset:24576\n"
      "ds_read_b64_tr_b16 a[36:37], v103 offset:24576\n"
      "ds_read_b64_tr_b16 a[108:109], v103 offset:36864\n"
      "ds_read_b64_tr_b16 a[60:61], v98 offset:24576\n"
      "ds_read_b64_tr_b16 a[110:111], v103 offset:37376\n"
      "ds_read_b64_tr_b16 a[62:63], v98 offset:25088\n"
      "ds_read_b64_tr_b16 a[134:135], v98 offset:37376\n"
      "ds_read_b64_tr_b16 a[38:39], v103 offset:25088\n"
      "ds_read_b64_tr_b16 a[86:87], v101 offset:37376\n"
      "ds_read_b64_tr_b16 a[132:133], v98 offset:36864\n"
      "ds_read_b64_tr_b16 a[14:15], v101 offset:25088\n"
      "v_mfma_f32_16x16x32_bf16 v[192:195], a[52:55], a[236:239], v[192:195]\n"
      "ds_read_b128 v[196:199], v158 offset:4096\n"
      "s_mov_b32 s78, 0\n"
      "s_add_u32 m0, s75, 48384\n"
      "v_mfma_f32_16x16x32_bf16 v[192:195], a[56:59], a[248:251], v[192:195]\n"
      "buffer_load_dwordx4 v179, s[20:23], s78 offen offset:768  lds\n"
      "s_add_u32 m0, s75, 52352\n"
      "s_waitcnt lgkmcnt(6)\n"
      "v_mfma_f32_16x16x32_bf16 v[192:195], a[60:63], a[220:223], v[192:195]\n"
      "s_waitcnt lgkmcnt(4)\n"
      "v_mfma_f32_16x16x32_bf16 v[246:249], a[36:39], a[216:219], v[246:249]\n"
      "v_mfma_f32_16x16x32_bf16 v[224:227], a[108:111], a[252:255], v[224:227]\n"
      "buffer_load_dwordx4 v179, s[20:23], s78 offen offset:896  lds\n"
      "v_mfma_f32_16x16x32_bf16 v[124:127], a[104:107], a[248:251], v[124:127]\n"
      "v_mfma_f32_16x16x32_bf16 v[124:127], a[108:111], a[220:223], v[124:127]\n"
      "s_waitcnt lgkmcnt(0)\n"
      "v_mfma_f32_16x16x32_bf16 v[188:191], a[80:83], a[240:243], v[188:191]\n"
      "s_add_u32 m0, s75, 56320\n"
      "ds_read_b128 a[224:227], v158 offset:8192\n"
      "v_mfma_f32_16x16x32_bf16 v[188:191], a[84:87], a[252:255], v[188:191]\n"
      "buffer_load_dwordx4 v179, s[20:23], s78 offen offset:1024  lds\n"
      "v_mfma_f32_16x16x32_bf16 v[204:207], a[12:15], a[252:255], v[204:207]\n"
      "v_mfma_f32_16x16x32_bf16 v[180:183], a[36:39], a[220:223], v[180:183]\n"
      "v_mfma_f32_16x16x32_bf16 v[212:215], a[56:59], a[244:247], v[212:215]\n"
      "s_mov_b32 s78, 36864\n"
      "s_add_u32 m0, s75, 60672\n"
      "v_mfma_f32_16x16x32_bf16 v[208:211], a[12:15], a[216:219], v[208:211]\n"
      "buffer_load_dwordx4 v179, s[20:23], s78 offen offset:768  lds\n"
      "v_mfma_f32_16x16x32_bf16 v[108:111], a[80:83], a[248:251], v[108:111]\n"
      "s_add_u32 m0, s75, 64640\n"
      "v_mfma_f32_16x16x32_bf16 v[108:111], a[84:87], a[220:223], v[108:111]\n"
      "v_mfma_f32_16x16x32_bf16 v[172:175], a[8:11], a[248:251], v[172:175]\n"
      "buffer_load_dwordx4 v179, s[20:23], s78 offen offset:896  lds\n"
      "v_mfma_f32_16x16x32_bf16 v[172:175], a[12:15], a[220:223], v[172:175]\n"
      "ds_read_b128 a[248:251], v0 offset:4096\n"
      "v_mfma_f32_16x16x32_bf16 v[166:169], a[132:135], a[220:223], v[166:169]\n"
      "ds_read_b128 a[228:231], v0 offset:12288\n"
      "v_mfma_f32_16x16x32_bf16 v[144:147], a[76:79], a[232:235], v[144:147]\n"
      "v_mfma_f32_16x16x32_bf16 v[144:147], a[80:83], a[244:247], v[144:147]\n"
      "v_readlane_b32 s46, v37, s42\n"
      "s_add_u32 m0, s75, 68608\n"
      "ds_read_b128 a[244:247], v1 offset:14336\n"
      "v_mfma_f32_16x16x32_bf16 v[144:147], a[84:87], a[216:219], v[144:147]\n"
      "buffer_load_dwordx4 v179, s[20:23], s78 offen offset:1024  lds\n"
      "v_mfma_f32_16x16x32_bf16 v[242:245], a[108:111], a[216:219], v[242:245]\n"
      "s_waitcnt lgkmcnt(2)\n"
      "v_mfma_f32_16x16x32_bf16 v[200:203], a[24:27], a[248:251], 0\n"
      "s_add_u32 m0, s75, 72960\n"
      "ds_read_b128 a[232:235], v0 offset:14336\n"
      "v_mfma_f32_16x16x32_bf16 v[220:223], a[120:123], a[248:251], 0\n"
      "s_waitcnt vmcnt(12)\n"
      "s_barrier\n"
      "ds_read_b64_tr_b16 a[42:43], v237 offset:512\n"
      "ds_read_b64_tr_b16 a[138:139], v238 offset:12800\n"
      "ds_read_b64_tr_b16 a[64:65], v238\n"
      "ds_read_b64_tr_b16 a[40:41], v237\n"
      "ds_read_b64_tr_b16 a[16:17], v236\n"
      "ds_read_b64_tr_b16 a[18:19], v236 offset:512\n"
      "ds_read_b64_tr_b16 a[136:137], v238 offset:12288\n"
      "ds_read_b64_tr_b16 a[88:89], v236 offset:12288\n"
      "ds_read_b64_tr_b16 a[112:113], v237 offset:12288\n"
      "ds_read_b64_tr_b16 a[114:115], v237 offset:12800\n"
      "ds_read_b64_tr_b16 a[90:91], v236 offset:12800\n"
      "ds_read_b64_tr_b16 a[66:67], v238 offset:512\n"
      "v_mfma_f32_16x16x32_bf16 v[212:215], a[60:63], a[216:219], v[212:215]\n"
      "s_mov_b32 s79, 73728\n"
      "v_mfma_f32_16x16x32_bf16 v[250:253], a[36:39], a[252:255], v[250:253]\n"
      "buffer_load_dwordx4 v179, s[20:23], s79 offen offset:768  lds\n"
      "s_waitcnt lgkmcnt(7)\n"
      "v_mfma_f32_16x16x32_bf16 v[250:253], a[40:43], a[224:227], v[250:253]\n"
      "s_add_u32 m0, s75, 76928\n"
      "v_mfma_f32_16x16x32_bf16 v[246:249], a[40:43], a[228:231], v[246:249]\n"
      "ds_read_b128 a[240:243], v1 offset:12288\n"
      "v_mfma_f32_16x16x32_bf16 v[184:187], a[132:135], a[216:219], v[184:187]\n"
      "s_waitcnt lgkmcnt(3)\n"
      "v_mfma_f32_16x16x32_bf16 v[184:187], a[136:139], a[228:231], v[184:187]\n"
      "buffer_load_dwordx4 v179, s[20:23], s79 offen offset:896  lds\n"
      "v_mfma_f32_16x16x32_bf16 v[232:235], a[132:135], a[252:255], v[232:235]\n"
      "v_mfma_f32_16x16x32_bf16 v[232:235], a[136:139], a[224:227], v[232:235]\n"
      "ds_read_b128 a[236:239], v156 offset:8192\n"
      "v_mfma_f32_16x16x32_bf16 v[124:127], a[112:115], a[232:235], v[124:127]\n"
      "s_add_u32 m0, s75, 80896\n"
      "ds_read_b128 a[216:219], v0 offset:10240\n"
      "v_mfma_f32_16x16x32_bf16 v[242:245], a[112:115], a[228:231], v[242:245]\n"
      "buffer_load_dwordx4 v179, s[20:23], s79 offen offset:1024  lds\n"
      "v_mfma_f32_16x16x32_bf16 v[172:175], a[16:19], a[232:235], v[172:175]\n"
      "s_add_u32 m0, s75, 85248\n"
      "v_mfma_f32_16x16x32_bf16 v[204:207], a[16:19], a[224:227], v[204:207]\n"
      "s_mov_b32 s79, 110592\n"
      "v_mfma_f32_16x16x32_bf16 v[166:169], a[136:139], a[232:235], v[166:169]\n"
      "s_waitcnt lgkmcnt(2)\n"
      "v_mfma_f32_16x16x32_bf16 v[228:231], a[60:63], a[252:255], v[228:231]\n"
      "buffer_load_dwordx4 v179, s[20:23], s79 offen offset:768  lds\n"
      "v_mfma_f32_16x16x32_bf16 v[228:231], a[64:67], a[224:227], v[228:231]\n"
      "ds_read_b128 a[252:255], v1 offset:4096\n"
      "v_mfma_f32_16x16x32_bf16 v[144:147], a[88:91], a[228:231], v[144:147]\n"
      "s_add_u32 m0, s75, 89216\n"
      "v_mfma_f32_16x16x32_bf16 v[208:211], a[16:19], a[228:231], v[208:211]\n"
      "v_mfma_f32_16x16x32_bf16 v[192:195], a[64:67], a[232:235], v[192:195]\n"
      "buffer_load_dwordx4 v179, s[20:23], s79 offen offset:896  lds\n"
      "v_mfma_f32_16x16x32_bf16 v[224:227], a[112:115], a[224:227], v[224:227]\n"
      "ds_read_b128 a[220:223], v1 offset:10240\n"
      "v_mfma_f32_16x16x32_bf16 v[180:183], a[40:43], a[232:235], v[180:183]\n"
      "s_add_u32 m0, s75, 93184\n"
      "v_mfma_f32_16x16x32_bf16 v[212:215], a[64:67], a[228:231], v[212:215]\n"
      "s_waitcnt lgkmcnt(1)\n"
      "v_mfma_f32_16x16x32_bf16 v[112:115], a[0:3], a[248:251], 0\n"
      "buffer_load_dwordx4 v179, s[20:23], s79 offen offset:1024  lds\n"
      "v_mfma_f32_16x16x32_bf16 v[112:115], a[4:7], a[252:255], v[112:115]\n"
      "v_mfma_f32_16x16x32_bf16 v[188:191], a[88:91], a[224:227], v[188:191]\n"
      "s_add_u32 m0, s75, -768\n"
      "v_mfma_f32_16x16x32_bf16 v[108:111], a[88:91], a[232:235], v[108:111]\n"
      "ds_read_b128 a[228:231], v1 offset:16384\n"
      "v_mfma_f32_16x16x32_bf16 v[140:143], a[48:51], a[248:251], 0\n"
      "s_waitcnt vmcnt(12)\n"
      "s_barrier\n"
      "ds_read_b64_tr_b16 a[92:93], v236 offset:36864\n"
      "ds_read_b64_tr_b16 a[22:23], v236 offset:25088\n"
      "ds_read_b64_tr_b16 a[70:71], v238 offset:25088\n"
      "ds_read_b64_tr_b16 a[94:95], v236 offset:37376\n"
      "ds_read_b64_tr_b16 a[46:47], v237 offset:25088\n"
      "ds_read_b64_tr_b16 a[44:45], v237 offset:24576\n"
      "ds_read_b64_tr_b16 a[142:143], v238 offset:37376\n"
      "ds_read_b64_tr_b16 a[20:21], v236 offset:24576\n"
      "ds_read_b64_tr_b16 a[140:141], v238 offset:36864\n"
      "ds_read_b64_tr_b16 a[118:119], v237 offset:37376\n"
      "ds_read_b64_tr_b16 a[68:69], v238 offset:24576\n"
      "ds_read_b64_tr_b16 a[116:117], v237 offset:36864\n"
      "v_mfma_f32_16x16x32_bf16 v[140:143], a[52:55], a[252:255], v[140:143]\n"
      "s_mov_b32 s78, 147456\n"
      "buffer_load_dwordx4 v179, s[20:23], s78 offen offset:768  lds\n"
      "s_waitcnt lgkmcnt(8)\n"
      "v_mfma_f32_16x16x32_bf16 v[188:191], a[92:95], a[236:239], v[188:191]\n"
      "ds_read_b128 a[232:235], v158 offset:2048\n"
      "s_waitcnt lgkmcnt(7)\n"
      "v_mfma_f32_16x16x32_bf16 v[250:253], a[44:47], a[236:239], v[250:253]\n"
      "s_add_u32 m0, s75, 3200\n"
      "s_waitcnt lgkmcnt(5)\n"
      "v_mfma_f32_16x16x32_bf16 v[204:207], a[20:23], a[236:239], v[204:207]\n"
      "s_nop 1\n"
      "v_fma_f32 v255, v188, v188, s51\n"
      "v_mfma_f32_16x16x32_bf16 v[172:175], a[20:23], a[244:247], v[172:175]\n"
      "buffer_load_dwordx4 v179, s[20:23], s78 offen offset:896  lds\n"
      "v_fmac_f32 v255, v189, v189\n"
      "s_add_u32 m0, s75, 7168\n"
      "v_mfma_f32_16x16x32_bf16 v[144:147], a[92:95], a[240:243], v[144:147]\n"
      "s_waitcnt lgkmcnt(1)\n"
      "v_fmac_f32 v255, v190, v190\n"
      "v_fma_f32 v97, v204, v204, s51\n"
      "v_mfma_f32_16x16x32_bf16 v[108:111], a[92:95], a[244:247], v[108:111]\n"
      "ds_read_b128 a[224:227], v0 offset:16384\n"
      "v_fmac_f32 v255, v191, v191\n"
      "v_mfma_f32_16x16x32_bf16 v[224:227], a[116:119], a[236:239], v[224:227]\n"
      "v_fmac_f32 v97, v205, v205\n"
      "v_mfma_f32_16x16x32_bf16 v[180:183], a[44:47], a[244:247], v[180:183]\n"
      "v_fmac_f32 v97, v206, v206\n"
      "v_fmac_f32 v97, v207, v207\n"
      "v_mfma_f32_16x16x32_bf16 v[192:195], a[68:71], a[244:247], v[192:195]\n"
      "buffer_load_dwordx4 v179, s[20:23], s78 offen offset:1024  lds\n"
      "v_mfma_f32_16x16x32_bf16 v[232:235], a[140:143], a[236:239], v[232:235]\n"
      "s_add_u32 m0, s75, 11520\n"
      "v_fma_f32 v157, v172, v172, s51\n"
      "v_fmac_f32_e64 v255, v224, v224\n"
      "v_mfma_f32_16x16x32_bf16 v[228:231], a[68:71], a[236:239], v[228:231]\n"
      "s_mov_b32 s78, 184320\n"
      "v_fmac_f32 v255, v225, v225\n"
      "v_fmac_f32 v157, v173, v173\n"
      "v_mfma_f32_16x16x32_bf16 v[242:245], a[116:119], a[240:243], v[242:245]\n"
      "v_fmac_f32 v97, v250, v250\n"
      "v_fmac_f32 v97, v251, v251\n"
      "v_mfma_f32_16x16x32_bf16 v[200:203], a[28:31], a[252:255], v[200:203]\n"
      "buffer_load_dwordx4 v179, s[20:23], s78 offen offset:768  lds\n"
      "v_mfma_f32_16x16x32_bf16 v[166:169], a[140:143], a[244:247], v[166:169]\n"
      "ds_read_b128 a[236:239], v156 offset:2048\n"
      "v_fmac_f32 v255, v226, v226\n"
      "v_mfma_f32_16x16x32_bf16 v[212:215], a[68:71], a[240:243], v[212:215]\n"
      "s_waitcnt lgkmcnt(1)\n"
      "v_fmac_f32 v97, v252, v252\n"
      "v_fmac_f32 v255, v227, v227\n"
      "v_fmac_f32_e64 v157, v174, v174\n"
      "v_mfma_f32_16x16x32_bf16 v[184:187], a[140:143], a[240:243], v[184:187]\n"
      "s_add_u32 m0, s75, 15488\n"
      "v_fma_f32 v159, v144, v144, s51\n"
      "v_fmac_f32_e64 v97, v253, v253\n"
      "v_mfma_f32_16x16x32_bf16 v[124:127], a[116:119], a[244:247], v[124:127]\n"
      "buffer_load_dwordx4 v179, s[20:23], s78 offen offset:896  lds\n"
      "v_mfma_f32_16x16x32_bf16 v[120:123], a[96:99], a[232:235], 0\n"
      "s_add_u32 m0, s75, 19456\n"
      "v_fmac_f32 v255, v232, v232\n"
      "v_mfma_f32_16x16x32_bf16 v[112:115], a[8:11], a[216:219], v[112:115]\n"
      "v_fmac_f32 v255, v233, v233\n"
      "v_fmac_f32 v97, v228, v228\n"
      "v_mfma_f32_16x16x32_bf16 v[112:115], a[12:15], a[220:223], v[112:115]\n"
      "s_lshl_b32 s92, s40, 6\n"
      "v_fmac_f32 v97, v229, v229\n"
      "v_fmac_f32 v97, v230, v230\n"
      "v_mfma_f32_16x16x32_bf16 v[112:115], a[16:19], a[224:227], v[112:115]\n"
      "buffer_load_dwordx4 v179, s[20:23], s78 offen offset:1024  lds\n"
      "v_mfma_f32_16x16x32_bf16 v[112:115], a[20:23], a[228:231], v[112:115]\n"
      "s_add_u32 m0, s75, 24576\n"
      "v_fmac_f32 v97, v231, v231\n"
      "v_fmac_f32 v255, v234, v234\n"
      "v_mfma_f32_16x16x32_bf16 v[246:249], a[44:47], a[240:243], v[246:249]\n"
      "ds_read_b128 a[244:247], v156 offset:6144\n"
      "v_fmac_f32 v255, v235, v235\n"
      "v_mfma_f32_16x16x32_bf16 v[200:203], a[32:35], a[216:219], v[200:203]\n"
      "s_waitcnt vmcnt(12)\n"
      "s_barrier\n"
      "s_mov_b32 s79, 0\n"
      "v_mfma_f32_16x16x32_bf16 v[200:203], a[36:39], a[220:223], v[200:203]\n"
      "ds_read_b64_tr_b16 a[170:171], v103 offset:512\n"
      "ds_read_b64_tr_b16 a[174:175], v103 offset:12800\n"
      "ds_read_b64_tr_b16 a[196:197], v98 offset:12288\n"
      "v_mfma_f32_16x16x32_bf16 v[200:203], a[40:43], a[224:227], v[200:203]\n"
      "ds_read_b64_tr_b16 a[146:147], v101 offset:512\n"
      "ds_read_b64_tr_b16 a[172:173], v103 offset:12288\n"
      "v_mfma_f32_16x16x32_bf16 v[200:203], a[44:47], a[228:231], v[200:203]\n"
      "ds_read_b64_tr_b16 a[168:169], v103\n"
      "ds_read_b64_tr_b16 a[144:145], v101\n"
      "ds_read_b64_tr_b16 a[198:199], v98 offset:12800\n"
      "ds_read_b64_tr_b16 a[150:151], v101 offset:12800\n"
      "v_mfma_f32_16x16x32_bf16 v[208:211], a[20:23], a[240:243], v[208:211]\n"
      "buffer_load_dwordx4 v135, s[36:39], s79 offen  lds\n"
      "ds_read_b64_tr_b16 a[192:193], v98\n"
      "v_mfma_f32_16x16x32_bf16 v[140:143], a[56:59], a[216:219], v[140:143]\n"
      "ds_read_b64_tr_b16 a[148:149], v101 offset:12288\n"
      "ds_read_b64_tr_b16 a[194:195], v98 offset:512\n"
      "v_mfma_f32_16x16x32_bf16 v[140:143], a[60:63], a[220:223], v[140:143]\n"
      "v_permlane16_swap_b32 v97, v255\n"
      "v_fmac_f32 v159, v145, v145\n"
      "v_mfma_f32_16x16x32_bf16 v[176:179], a[72:75], a[248:251], 0\n"
      "s_add_u32 m0, s75, 28544\n"
      "v_add_f32 v97, v97, v255\n"
      "v_fmac_f32 v157, v175, v175\n"
      "v_mfma_f32_16x16x32_bf16 v[176:179], a[76:79], a[252:255], v[176:179]\n"
      "buffer_load_dwordx4 v135, s[36:39], s79 offen offset:128  lds\n"
      "v_mfma_f32_16x16x32_bf16 v[176:179], a[80:83], a[216:219], v[176:179]\n"
      "s_add_u32 m0, s75, 32512\n"
      "v_mov_b32 v255, v97\n"
      "v_fmac_f32 v157, v180, v180\n"
      "v_mfma_f32_16x16x32_bf16 v[104:107], a[96:99], a[248:251], 0\n"
      "v_permlane32_swap_b32 v97, v255\n"
      "v_add_f32 v97, v97, v255\n"
      "v_fma_f32 v130, v208, v208, s51\n"
      "v_rsq_f32 v97, v97\n"
      "v_mfma_f32_16x16x32_bf16 v[104:107], a[100:103], a[252:255], v[104:107]\n"
      "buffer_load_dwordx4 v135, s[36:39], s79 offen offset:256  lds\n"
      "v_mov_b32 v255, v97\n"
      "v_mfma_f32_16x16x32_bf16 v[140:143], a[64:67], a[224:227], v[140:143]\n"
      "s_nop 0\n"
      "v_permlane16_swap_b32 v97, v255\n"
      "v_mfma_f32_16x16x32_bf16 v[176:179], a[84:87], a[220:223], v[176:179]\n"
      "ds_read_b128 a[248:251], v158 offset:10240\n"
      "v_mul_f32 v232, v255, v232\n"
      "v_fmac_f32 v130, v209, v209\n"
      "v_mfma_f32_16x16x32_bf16 v[176:179], a[88:91], a[224:227], v[176:179]\n"
      "ds_read_b128 a[240:243], v158 offset:6144\n"
      "v_mul_f32 v97, s50, v97\n"
      "v_fmac_f32 v130, v210, v210\n"
      "v_mfma_f32_16x16x32_bf16 v[176:179], a[92:95], a[228:231], v[176:179]\n"
      "s_add_u32 m0, s75, 36864\n"
      "v_fmac_f32_e64 v130, v211, v211\n"
      "s_mov_b32 s79, 12288\n"
      "v_mul_f32 v188, v255, v188\n"
      "v_mul_f32 v206, v97, v206\n"
      "v_mul_f32 v224, v255, v224\n"
      "v_mfma_f32_16x16x32_bf16 v[140:143], a[68:71], a[228:231], v[140:143]\n"
      "v_mul_f32 v235, v255, v235\n"
      "v_fmac_f32 v159, v146, v146\n"
      "v_fmac_f32 v157, v181, v181\n"
      "v_mfma_f32_16x16x32_bf16 v[220:223], a[124:127], a[252:255], v[220:223]\n"
      "buffer_load_dwordx4 v135, s[36:39], s79 offen  lds\n"
      "v_fmac_f32 v157, v182, v182\n"
      "v_mul_f32 v229, v97, v229\n"
      "v_mfma_f32_16x16x32_bf16 v[220:223], a[128:131], a[216:219], v[220:223]\n"
      "s_add_u32 m0, s75, 40832\n"
      "v_mul_f32 v233, v255, v233\n"
      "v_mul_f32 v253, v97, v253\n"
      "v_mfma_f32_16x16x32_bf16 v[220:223], a[132:135], a[220:223], v[220:223]\n"
      "v_mul_f32 v207, v97, v207\n"
      "v_mul_f32 v189, v255, v189\n"
      "v_fmac_f32 v159, v147, v147\n"
      "v_mul_f32 v228, v97, v228\n"
      "v_mfma_f32_16x16x32_bf16 v[220:223], a[136:139], a[224:227], v[220:223]\n"
      "buffer_load_dwordx4 v135, s[36:39], s79 offen offset:128  lds\n"
      "v_mfma_f32_16x16x32_bf16 v[220:223], a[140:143], a[228:231], v[220:223]\n"
      "s_add_u32 m0, s75, 44800\n"
      "v_mul_f32 v191, v255, v191\n"
      "v_mul_f32 v230, v97, v230\n"
      "v_mfma_f32_16x16x32_bf16 v[104:107], a[104:107], a[216:219], v[104:107]\n"
      "v_cvt_pk_bf16_f32 v228, v228, v229\n"
      "v_mul_f32 v231, v97, v231\n"
      "v_mul_f32 v204, v97, v204\n"
      "v_mul_f32 v250, v97, v250\n"
      "v_mul_f32 v190, v255, v190\n"
      "v_mul_f32_e64 v205, v97, v205\n"
      "v_mfma_f32_16x16x32_bf16 v[104:107], a[108:111], a[220:223], v[104:107]\n"
      "buffer_load_dwordx4 v135, s[36:39], s79 offen offset:256  lds\n"
      "v_mfma_f32_16x16x32_bf16 v[104:107], a[112:115], a[224:227], v[104:107]\n"
      "s_waitcnt lgkmcnt(0)\n"
      "v_mul_f32 v227, v255, v227\n"
      "v_cvt_pk_bf16_f32 v232, v232, v233\n"
      "v_cvt_pk_bf16_f32 v188, v188, v189\n"
      "v_mfma_f32_16x16x32_bf16 v[104:107], a[116:119], a[228:231], v[104:107]\n"
      "s_add_u32 m0, s75, 49152\n"
      "v_cvt_pk_bf16_f32 v229, v230, v231\n"
      "v_cvt_pk_bf16_f32 v204, v204, v205\n"
      "v_mul_f32_e64 v225, v255, v225\n"
      "v_mfma_f32_16x16x32_bf16 v[152:155], a[0:3], a[232:235], 0\n"
      "s_waitcnt vmcnt(12)\n"
      "s_barrier\n"
      "ds_read_b64_tr_b16 a[176:177], v103 offset:24576\n"
      "v_mfma_f32_16x16x32_bf16 v[152:155], a[4:7], a[236:239], v[152:155]\n"
      "ds_read_b64_tr_b16 a[182:183], v103 offset:37376\n"
      "ds_read_b64_tr_b16 a[178:179], v103 offset:25088\n"
      "v_mfma_f32_16x16x32_bf16 v[152:155], a[8:11], a[240:243], v[152:155]\n"
      "ds_read_b64_tr_b16 a[202:203], v98 offset:25088\n"
      "ds_read_b64_tr_b16 a[156:157], v101 offset:36864\n"
      "v_mfma_f32_16x16x32_bf16 v[152:155], a[12:15], a[244:247], v[152:155]\n"
      "ds_read_b64_tr_b16 a[154:155], v101 offset:25088\n"
      "ds_read_b64_tr_b16 a[180:181], v103 offset:36864\n"
      "ds_read_b64_tr_b16 a[204:205], v98 offset:36864\n"
      "ds_read_b64_tr_b16 a[152:153], v101 offset:24576\n"
      "v_mfma_f32_16x16x32_bf16 v[152:155], a[16:19], a[248:251], v[152:155]\n"
      "ds_read_b64_tr_b16 a[158:159], v101 offset:37376\n"
      "ds_read_b64_tr_b16 a[206:207], v98 offset:37376\n"
      "v_mfma_f32_16x16x32_bf16 v[100:103], a[24:27], a[232:235], 0\n"
      "ds_read_b64_tr_b16 a[200:201], v98 offset:24576\n"
      "v_cvt_pk_bf16_f32 v189, v190, v191\n"
      "v_mfma_f32_16x16x32_bf16 v[100:103], a[28:31], a[236:239], v[100:103]\n"
      "ds_write_b64 v133, v[228:229] offset:16\n"
      "v_mfma_f32_16x16x32_bf16 v[100:103], a[32:35], a[240:243], v[100:103]\n"
      "s_mov_b32 s78, 24576\n"
      "v_mul_f32 v234, v255, v234\n"
      "v_mul_f32 v251, v97, v251\n"
      "v_mfma_f32_16x16x32_bf16 v[100:103], a[36:39], a[244:247], v[100:103]\n"
      "buffer_load_dwordx4 v135, s[36:39], s78 offen  lds\n"
      "v_mul_f32 v252, v97, v252\n"
      "v_fmac_f32 v130, v246, v246\n"
      "v_mfma_f32_16x16x32_bf16 v[228:231], a[72:75], a[232:235], 0\n"
      "s_add_u32 m0, s75, 53120\n"
      "v_cvt_pk_bf16_f32 v205, v206, v207\n"
      "v_cvt_pk_bf16_f32 v233, v234, v235\n"
      "v_mfma_f32_16x16x32_bf16 v[228:231], a[76:79], a[236:239], v[228:231]\n"
      "ds_read_b128 a[252:255], v156 offset:10240\n"
      "v_fmac_f32 v157, v183, v183\n"
      "v_fmac_f32 v130, v247, v247\n"
      "v_mfma_f32_16x16x32_bf16 v[96:99], a[48:51], a[232:235], 0\n"
      "buffer_load_dwordx4 v135, s[36:39], s78 offen offset:128  lds\n"
      "v_mfma_f32_16x16x32_bf16 v[96:99], a[52:55], a[236:239], v[96:99]\n"
      "s_add_u32 m0, s75, 57088\n"
      "v_mul_f32 v226, v255, v226\n"
      "v_fmac_f32 v130, v248, v248\n"
      "v_mfma_f32_16x16x32_bf16 v[96:99], a[56:59], a[240:243], v[96:99]\n"
      "v_cvt_pk_bf16_f32 v207, v252, v253\n"
      "v_fmac_f32 v159, v242, v242\n"
      "v_fmac_f32 v157, v192, v192\n"
      "v_mfma_f32_16x16x32_bf16 v[96:99], a[60:63], a[244:247], v[96:99]\n"
      "ds_read_b128 v[252:255], v158\n"
      "v_cvt_pk_bf16_f32 v191, v226, v227\n"
      "v_mfma_f32_16x16x32_bf16 v[96:99], a[64:67], a[248:251], v[96:99]\n"
      "buffer_load_dwordx4 v135, s[36:39], s78 offen offset:256  lds\n"
      "v_mfma_f32_16x16x32_bf16 v[100:103], a[40:43], a[248:251], v[100:103]\n"
      "v_fmac_f32 v159, v243, v243\n"
      "v_cvt_pk_bf16_f32 v190, v224, v225\n"
      "v_mfma_f32_16x16x32_bf16 v[228:231], a[80:83], a[240:243], v[228:231]\n"
      "s_waitcnt lgkmcnt(1)\n"
      "v_fmac_f32_e64 v159, v244, v244\n"
      "s_mov_b32 s78, 36864\n"
      "v_mfma_f32_16x16x32_bf16 v[228:231], a[84:87], a[244:247], v[228:231]\n"
      "s_add_u32 m0, s75, 61440\n"
      "v_fmac_f32 v159, v245, v245\n"
      "v_fmac_f32 v130, v249, v249\n"
      "v_mfma_f32_16x16x32_bf16 v[228:231], a[88:91], a[248:251], v[228:231]\n"
      "buffer_load_dwordx4 v135, s[36:39], s78 offen  lds\n"
      "v_mfma_f32_16x16x32_bf16 v[228:231], a[92:95], a[252:255], v[228:231]\n"
      "s_waitcnt lgkmcnt(0)\n"
      "v_fmac_f32 v159, v184, v184\n"
      "v_mfma_f32_16x16x32_bf16 v[120:123], a[100:103], a[236:239], v[120:123]\n"
      "ds_write_b64 v133, v[232:233] offset:24\n"
      "v_mfma_f32_16x16x32_bf16 v[120:123], a[104:107], a[240:243], v[120:123]\n"
      "s_add_u32 m0, s75, 65408\n"
      "v_cvt_pk_bf16_f32 v206, v250, v251\n"
      "v_fmac_f32 v130, v212, v212\n"
      "v_mfma_f32_16x16x32_bf16 v[224:227], a[120:123], a[232:235], 0\n"
      "buffer_load_dwordx4 v135, s[36:39], s78 offen offset:128  lds\n"
      "v_fmac_f32 v157, v193, v193\n"
      "v_mfma_f32_16x16x32_bf16 v[224:227], a[124:127], a[236:239], v[224:227]\n"
      "ds_read_b128 v[232:235], v156 offset:2048\n"
      "v_fmac_f32_e64 v130, v213, v213\n"
      "v_mfma_f32_16x16x32_bf16 v[224:227], a[128:131], a[240:243], v[224:227]\n"
      "s_add_u32 m0, s75, 69376\n"
      "v_fmac_f32 v157, v194, v194\n"
      "v_fmac_f32 v159, v185, v185\n"
      "v_mfma_f32_16x16x32_bf16 v[224:227], a[132:135], a[244:247], v[224:227]\n"
      "ds_write_b128 v133, v[204:207]\n"
      "v_mfma_f32_16x16x32_bf16 v[120:123], a[108:111], a[244:247], v[120:123]\n"
      "buffer_load_dwordx4 v135, s[36:39], s78 offen offset:256  lds\n"
      "v_mfma_f32_16x16x32_bf16 v[120:123], a[112:115], a[248:251], v[120:123]\n"
      "s_waitcnt vmcnt(12)\n"
      "s_barrier\n"
      "ds_read_b64_tr_b16 a[184:185], v237\n"
      "v_mfma_f32_16x16x32_bf16 v[204:207], a[144:147], v[252:255], 0\n"
      "ds_read_b64_tr_b16 a[186:187], v237 offset:512\n"
      "ds_read_b64_tr_b16 a[210:211], v238 offset:512\n"
      "v_mfma_f32_16x16x32_bf16 v[204:207], a[148:151], v[216:219], v[204:207]\n"
      "ds_read_b64_tr_b16 a[162:163], v236 offset:512\n"
      "ds_read_b64_tr_b16 a[208:209], v238\n"
      "s_add_u32 m0, s75, 73728\n"
      "v_mfma_f32_16x16x32_bf16 v[204:207], a[152:155], v[196:199], v[204:207]\n"
      "ds_read_b64_tr_b16 a[212:213], v238 offset:12288\n"
      "ds_read_b64_tr_b16 a[190:191], v237 offset:12800\n"
      "v_mfma_f32_16x16x32_bf16 v[96:99], a[68:71], a[252:255], v[96:99]\n"
      "ds_read_b64_tr_b16 a[214:215], v238 offset:12800\n"
      "s_mov_b32 s79, 49152\n"
      "ds_read_b64_tr_b16 a[188:189], v237 offset:12288\n"
      "v_mfma_f32_16x16x32_bf16 v[136:139], a[192:195], v[252:255], 0\n"
      "ds_read_b64_tr_b16 a[166:167], v236 offset:12800\n"
      "ds_read_b64_tr_b16 a[160:161], v236\n"
      "v_mfma_f32_16x16x32_bf16 v[136:139], a[196:199], v[216:219], v[136:139]\n"
      "buffer_load_dwordx4 v135, s[36:39], s79 offen  lds\n"
      "v_mfma_f32_16x16x32_bf16 v[148:151], a[168:171], v[160:163], 0\n"
      "ds_read_b64_tr_b16 a[164:165], v236 offset:12288\n"
      "s_add_u32 m0, s75, 77696\n"
      "v_mfma_f32_16x16x32_bf16 v[116:119], a[168:171], v[252:255], 0\n"
      "ds_write_b128 v133, v[188:191] offset:32\n"
      "v_mfma_f32_16x16x32_bf16 v[252:255], a[192:195], v[160:163], 0\n"
      "buffer_load_dwordx4 v135, s[36:39], s79 offen offset:128  lds\n"
      "v_mfma_f32_16x16x32_bf16 v[188:191], a[144:147], v[160:163], 0\n"
      "ds_read_b128 v[160:163], v158 offset:6144\n"
      "v_fmac_f32 v130, v214, v214\n"
      "v_mfma_f32_16x16x32_bf16 v[116:119], a[172:175], v[216:219], v[116:119]\n"
      "ds_read_b128 v[216:219], v156 offset:4096\n"
      "v_fmac_f32 v130, v215, v215\n"
      "v_mfma_f32_16x16x32_bf16 v[136:139], a[200:203], v[196:199], v[136:139]\n"
      "s_waitcnt lgkmcnt(3)\n"
      "v_fmac_f32 v159, v186, v186\n"
      "s_add_u32 m0, s75, 81664\n"
      "v_mfma_f32_16x16x32_bf16 v[252:255], a[196:199], v[232:235], v[252:255]\n"
      "buffer_load_dwordx4 v135, s[36:39], s79 offen offset:256  lds\n"
      "v_mfma_f32_16x16x32_bf16 v[188:191], a[148:151], v[232:235], v[188:191]\n"
      "s_mov_b32 s79, 61440\n"
      "v_fmac_f32 v159, v187, v187\n"
      "v_fmac_f32 v157, v195, v195\n"
      "v_mfma_f32_16x16x32_bf16 v[116:119], a[176:179], v[196:199], v[116:119]\n"
      "s_add_u32 m0, s75, 86016\n"
      "ds_read_b128 v[196:199], v158 offset:8192\n"
      "v_mfma_f32_16x16x32_bf16 v[148:151], a[172:175], v[232:235], v[148:151]\n"
      "ds_read_b128 v[232:235], v156 offset:6144\n"
      "s_waitcnt lgkmcnt(3)\n"
      "v_mfma_f32_16x16x32_bf16 v[188:191], a[152:155], v[160:163], v[188:191]\n"
      "buffer_load_dwordx4 v135, s[36:39], s79 offen  lds\n"
      "v_mfma_f32_16x16x32_bf16 v[148:151], a[176:179], v[160:163], v[148:151]\n"
      "s_add_u32 m0, s75, 89984\n"
      "v_permlane16_swap_b32 v130, v159\n"
      "v_mfma_f32_16x16x32_bf16 v[252:255], a[200:203], v[160:163], v[252:255]\n"
      "s_waitcnt lgkmcnt(2)\n"
      "v_add_f32 v130, v130, v159\n"
      "v_mov_b32 v159, v130\n"
      "v_mfma_f32_16x16x32_bf16 v[204:207], a[156:159], v[216:219], v[204:207]\n"
      "ds_read_b128 v[160:163], v158 offset:10240\n"
      "v_permlane32_swap_b32 v130, v159\n"
      "v_mfma_f32_16x16x32_bf16 v[136:139], a[204:207], v[216:219], v[136:139]\n"
      "buffer_load_dwordx4 v135, s[36:39], s79 offen offset:128  lds\n"
      "v_mfma_f32_16x16x32_bf16 v[152:155], a[20:23], a[252:255], v[152:155]\n"
      "s_waitcnt lgkmcnt(1)\n"
      "v_fma_f32 v158, v108, v108, s51\n"
      "v_fmac_f32 v158, v109, v109\n"
      "v_add_f32 v130, v130, v159\n"
      "v_mfma_f32_16x16x32_bf16 v[116:119], a[180:183], v[216:219], v[116:119]\n"
      "ds_read_b128 v[216:219], v156 offset:8192\n"
      "v_fmac_f32 v158, v110, v110\n"
      "v_rsq_f32_e64 v130, v130\n"
      "v_mfma_f32_16x16x32_bf16 v[148:151], a[180:183], v[232:235], v[148:151]\n"
      "s_add_u32 m0, s75, 93952\n"
      "v_fmac_f32 v158, v111, v111\n"
      "v_fmac_f32 v158, v124, v124\n"
      "v_fmac_f32 v158, v125, v125\n"
      "v_mov_b32 v159, v130\n"
      "v_mfma_f32_16x16x32_bf16 v[188:191], a[156:159], v[232:235], v[188:191]\n"
      "buffer_load_dwordx4 v135, s[36:39], s79 offen offset:256  lds\n"
      "v_fmac_f32_e64 v158, v126, v126\n"
      "v_mfma_f32_16x16x32_bf16 v[252:255], a[204:207], v[232:235], v[252:255]\n"
      "ds_read_b128 v[234:237], v156 offset:10240\n"
      "v_permlane16_swap_b32 v130, v159\n"
      "v_mul_f32 v130, s50, v130\n"
      "v_fmac_f32 v158, v127, v127\n"
      "v_fmac_f32 v158, v166, v166\n"
      "v_fmac_f32 v158, v167, v167\n"
      "v_mul_f32 v246, v130, v246\n"
      "v_mul_f32 v214, v130, v214\n"
      "v_fmac_f32 v158, v168, v168\n"
      "v_fmac_f32 v158, v169, v169\n"
      "v_fma_f32 v82, v176, v176, s51\n"
      "v_mfma_f32_16x16x32_bf16 v[204:207], a[160:163], v[196:199], v[204:207]\n"
      "s_waitcnt lgkmcnt(1)\n"
      "v_mul_f32 v184, v159, v184\n"
      "v_fmac_f32 v82, v177, v177\n"
      "v_mfma_f32_16x16x32_bf16 v[204:207], a[164:167], v[216:219], v[204:207]\n"
      "s_waitcnt vmcnt(12)\n"
      "s_barrier\n"
      "v_mul_f32_e64 v249, v130, v249\n"
      "v_mfma_f32_16x16x32_bf16 v[116:119], a[184:187], v[196:199], v[116:119]\n"
      "ds_read_b64_tr_b16 a[216:217], v239 offset:24576\n"
      "v_permlane16_swap_b32_e64 v157, v158\n"
      "v_mfma_f32_16x16x32_bf16 v[116:119], a[188:191], v[216:219], v[116:119]\n"
      "ds_read_b64_tr_b16 a[240:241], v239 offset:24584\n"
      "v_cvt_pk_bf16_f32 v204, v204, v205\n"
      "v_cvt_pk_bf16_f32 v205, v206, v207\n"
      "v_mfma_f32_16x16x32_bf16 v[136:139], a[208:211], v[196:199], v[136:139]\n"
      "ds_write_b64 v134, v[204:205]\n"
      "v_mfma_f32_16x16x32_bf16 v[136:139], a[212:215], v[216:219], v[136:139]\n"
      "s_waitcnt lgkmcnt(3)\n"
      "v_mul_f32 v185, v159, v185\n"
      "v_mul_f32_e64 v186, v159, v186\n"
      "v_cvt_pk_bf16_f32 v184, v184, v185\n"
      "v_fma_f32 v135, v228, v228, s51\n"
      "v_mul_f32 v187, v159, v187\n"
      "v_add_f32 v157, v157, v158\n"
      "v_mov_b32_e64 v158, v157\n"
      "v_fma_f32 v77, v152, v152, s51\n"
      "v_cvt_pk_bf16_f32 v116, v116, v117\n"
      "v_cvt_pk_bf16_f32 v117, v118, v119\n"
      "v_cvt_pk_bf16_f32 v136, v136, v137\n"
      "v_cvt_pk_bf16_f32 v137, v138, v139\n"
      "v_permlane32_swap_b32 v157, v158\n"
      "v_mul_f32 v209, v130, v209\n"
      "v_add_f32 v157, v157, v158\n"
      "v_mul_f32 v244, v159, v244\n"
      "v_mfma_f32_16x16x32_bf16 v[148:151], a[184:187], v[160:163], v[148:151]\n"
      "buffer_load_dwordx4 v[216:219], v36, s[28:31], s49 offen\n"
      "v_rsq_f32_e64 v157, v157\n"
      "v_mfma_f32_16x16x32_bf16 v[148:151], a[188:191], v[234:237], v[148:151]\n"
      "ds_write_b64 v134, v[116:117] offset:32\n"
      "ds_read_b64_tr_b16 a[218:219], v239 offset:26624\n"
      "v_cvt_pk_bf16_f32 v185, v186, v187\n"
      "v_mov_b32 v158, v157\n"
      "v_mul_f32 v213, v130, v213\n"
      "v_mul_f32_e64 v211, v130, v211\n"
      "v_mfma_f32_16x16x32_bf16 v[188:191], a[160:163], v[160:163], v[188:191]\n"
      "buffer_load_dword v138, v29, s[28:31], s49 offen\n"
      "v_permlane16_swap_b32 v157, v158\n"
      "v_cvt_pk_bf16_f32 v148, v148, v149\n"
      "v_fmac_f32 v82, v178, v178\n"
      "v_fmac_f32 v77, v153, v153\n"
      "v_fmac_f32 v135, v229, v229\n"
      "v_fma_f32 v187, v112, v112, s51\n"
      "v_mul_f32 v242, v159, v242\n"
      "v_mul_f32 v210, v130, v210\n"
      "v_mul_f32 v157, s50, v157\n"
      "v_mul_f32 v172, v157, v172\n"
      "v_mfma_f32_16x16x32_bf16 v[252:255], a[208:211], v[160:163], v[252:255]\n"
      "ds_read_b64_tr_b16 a[222:223], v239 offset:38912\n"
      "v_fmac_f32 v82, v179, v179\n"
      "v_mul_f32 v208, v130, v208\n"
      "v_mul_f32 v109, v158, v109\n"
      "v_fmac_f32 v82, v104, v104\n"
      "v_mfma_f32_16x16x32_bf16 v[252:255], a[212:215], v[234:237], v[252:255]\n"
      "ds_read_b64_tr_b16 v[232:233], v132 offset:24576\n"
      "v_mul_f32_e64 v168, v158, v168\n"
      "v_mfma_f32_16x16x32_bf16 v[188:191], a[164:167], v[234:237], v[188:191]\n"
      "ds_read_b64_tr_b16 v[236:237], v132 offset:36864\n"
      "ds_read_b64_tr_b16 a[220:221], v239 offset:36864\n"
      "ds_read_b64_tr_b16 a[246:247], v239 offset:38920\n"
      "v_mul_f32 v126, v158, v126\n"
      "v_mul_f32 v195, v157, v195\n"
      "v_mul_f32 v145, v159, v145\n"
      "v_mul_f32 v144, v159, v144\n"
      "v_mul_f32 v108, v158, v108\n"
      "v_fmac_f32 v135, v230, v230\n"
      "v_mfma_f32_16x16x32_bf16 v[100:103], a[44:47], a[252:255], v[100:103]\n"
      "ds_read_b64_tr_b16 a[242:243], v239 offset:26632\n"
      "v_fmac_f32 v187, v113, v113\n"
      "v_mul_f32 v175, v157, v175\n"
      "v_cvt_pk_bf16_f32 v208, v208, v209\n"
      "v_mul_f32 v194, v157, v194\n"
      "v_mul_f32 v174, v157, v174\n"
      "v_mul_f32 v147, v159, v147\n"
      "v_fmac_f32 v77, v154, v154\n"
      "v_fmac_f32_e64 v82, v105, v105\n"
      "v_mfma_f32_16x16x32_bf16 v[120:123], a[116:119], a[252:255], v[120:123]\n"
      "ds_read_b64_tr_b16 a[244:245], v239 offset:36872\n"
      "ds_read_b64_tr_b16 v[238:239], v132 offset:38912\n"
      "ds_read_b64_tr_b16 v[234:235], v132 offset:26624\n"
      "v_mul_f32 v181, v157, v181\n"
      "v_mul_f32 v192, v157, v192\n"
      "v_mul_f32 v193, v157, v193\n"
      "v_mul_f32 v125, v158, v125\n"
      "v_mul_f32 v166, v158, v166\n"
      "v_mul_f32 v173, v157, v173\n"
      "v_mul_f32 v247, v130, v247\n"
      "v_cvt_pk_bf16_f32 v172, v172, v173\n"
      "v_fmac_f32 v135, v231, v231\n"
      "v_mul_f32 v146, v159, v146\n"
      "v_fmac_f32 v187, v114, v114\n"
      "v_cvt_pk_bf16_f32 v188, v188, v189\n"
      "v_fmac_f32 v77, v155, v155\n"
      "v_mul_f32 v167, v158, v167\n"
      "v_fmac_f32 v82, v106, v106\n"
      "v_cvt_pk_bf16_f32 v209, v210, v211\n"
      "v_fmac_f32 v135, v120, v120\n"
      "v_mul_f32 v169, v158, v169\n"
      "v_mul_f32 v111, v158, v111\n"
      "v_mul_f32 v245, v159, v245\n"
      "v_mul_f32 v110, v158, v110\n"
      "v_cvt_pk_bf16_f32 v252, v252, v253\n"
      "v_mul_f32_e64 v243, v159, v243\n"
      "v_subrev_u32 v186, 115456, v9\n"
      "v_cvt_pk_bf16_f32 v166, v166, v167\n"
      "v_fmac_f32 v187, v115, v115\n"
      "v_cvt_pk_bf16_f32 v189, v190, v191\n"
      "v_cvt_pk_bf16_f32 v144, v144, v145\n"
      "v_fmac_f32 v77, v100, v100\n"
      "v_mul_f32 v182, v157, v182\n"
      "v_mul_f32 v180, v157, v180\n"
      "v_mul_f32 v248, v130, v248\n"
      "v_fmac_f32 v187, v200, v200\n"
      "v_cvt_pk_bf16_f32 v145, v146, v147\n"
      "v_fmac_f32 v135, v121, v121\n"
      "v_fmac_f32 v135, v122, v122\n"
      "v_fmac_f32 v187, v201, v201\n"
      "v_cvt_pk_bf16_f32 v173, v174, v175\n"
      "v_cvt_pk_bf16_f32 v149, v150, v151\n"
      "v_cvt_pk_bf16_f32 v211, v248, v249\n"
      "v_fmac_f32 v135, v123, v123\n"
      "v_mul_f32 v212, v130, v212\n"
      "v_mfma_f32_16x16x32_bf16 v[224:227], a[136:139], a[248:251], v[224:227]\n"
      "ds_read_b128 v[248:251], v0 offset:4096\n"
      "ds_read_b128 v[160:163], v1 offset:4096\n"
      "v_fmac_f32 v77, v101, v101\n"
      "v_cvt_pk_bf16_f32 v108, v108, v109\n"
      "v_fmac_f32 v187, v202, v202\n"
      "v_mul_f32 v183, v157, v183\n"
      "v_cvt_pk_bf16_f32 v192, v192, v193\n"
      "v_fmac_f32 v82, v107, v107\n"
      "v_fmac_f32 v77, v102, v102\n"
      "v_cvt_pk_bf16_f32 v253, v254, v255\n"
      "v_mfma_f32_16x16x32_bf16 v[224:227], a[140:143], a[252:255], v[224:227]\n"
      "ds_read_b128 v[204:207], v0 offset:16384\n"
      "v_fmac_f32_e64 v77, v103, v103\n"
      "v_cvt_pk_bf16_f32 v212, v212, v213\n"
      "v_mul_f32 v127, v158, v127\n"
      "v_fmac_f32 v82, v220, v220\n"
      "v_fmac_f32 v77, v96, v96\n"
      "v_fmac_f32 v77, v97, v97\n"
      "v_fmac_f32_e64 v77, v98, v98\n"
      "v_cvt_pk_bf16_f32 v175, v182, v183\n"
      "v_fmac_f32 v135, v224, v224\n"
      "v_fmac_f32 v135, v225, v225\n"
      "v_cvt_pk_bf16_f32 v109, v110, v111\n"
      "v_fmac_f32_e64 v187, v203, v203\n"
      "v_cvt_pk_bf16_f32 v146, v242, v243\n"
      "v_cvt_pk_bf16_f32 v147, v244, v245\n"
      "v_fmac_f32 v82, v221, v221\n"
      "v_fmac_f32 v135, v226, v226\n"
      "v_mul_f32 v215, v130, v215\n"
      "v_fmac_f32 v77, v99, v99\n"
      "v_fmac_f32 v135, v227, v227\n"
      "v_fmac_f32 v187, v140, v140\n"
      "s_add_u32 m0, s64, 18432\n"
      "buffer_load_dwordx4 v[116:119], v36, s[28:31], s74 offen\n"
      "v_fmac_f32 v82, v222, v222\n"
      "v_mul_f32 v124, v158, v124\n"
      "v_permlane16_swap_b32 v77, v135\n"
      "v_cvt_pk_bf16_f32 v111, v126, v127\n"
      "v_add_f32 v77, v77, v135\n"
      "v_fmac_f32 v187, v141, v141\n"
      "v_cvt_pk_bf16_f32 v110, v124, v125\n"
      "v_mov_b32 v135, v77\n"
      "s_waitcnt lgkmcnt(0)\n"
      "v_mfma_f32_16x16x32_bf16 v[124:127], v[248:251], a[144:147], 0\n"
      "ds_read_b128 v[196:199], v0 offset:10240\n"
      "v_permlane32_swap_b32 v77, v135\n"
      "v_cvt_pk_bf16_f32 v167, v168, v169\n"
      "v_fmac_f32 v187, v142, v142\n"
      "v_add_f32 v77, v77, v135\n"
      "v_fmac_f32 v82, v223, v223\n"
      "v_rsq_f32 v77, v77\n"
      "v_cvt_pk_bf16_f32 v213, v214, v215\n"
      "v_fmac_f32_e64 v187, v143, v143\n"
      "v_cvt_pk_bf16_f32 v193, v194, v195\n"
      "v_mov_b32_e64 v135, v77\n"
      "v_cvt_pk_bf16_f32 v174, v180, v181\n"
      "v_cvt_pk_bf16_f32 v210, v246, v247\n"
      "v_mfma_f32_16x16x32_bf16 v[124:127], v[160:163], a[148:151], v[124:127]\n"
      "buffer_load_dword v254, v29, s[28:31], s74 offen\n"
      "ds_read_b128 v[180:183], v1 offset:10240\n"
      "ds_read_b128 v[156:159], v1 offset:16384\n"
      "v_permlane16_swap_b32 v77, v135\n"
      "v_mul_f32 v77, s50, v77\n"
      "v_permlane16_swap_b32 v187, v82\n"
      "v_mul_f32 v98, v77, v98\n"
      "s_waitcnt vmcnt(10)\n"
      "s_barrier\n"
      "buffer_load_dwordx4 v16, s[16:19], s45 offen  lds\n"
      "v_mul_f32 v229, v135, v229\n"
      "v_mul_f32 v121, v135, v121\n"
      "v_mul_f32 v154, v77, v154\n"
      "v_mul_f32 v153, v77, v153\n"
      "v_xor_b32 v169, 64, v8\n"
      "v_mul_f32 v152, v77, v152\n"
      "v_add_f32 v187, v187, v82\n"
      "v_mul_f32 v123, v135, v123\n"
      "v_mov_b32 v82, v187\n"
      "v_mul_f32 v96, v77, v96\n"
      "v_mul_f32 v228, v135, v228\n"
      "v_mul_f32_e64 v102, v77, v102\n"
      "v_cvt_pk_bf16_f32 v152, v152, v153\n"
      "s_waitcnt lgkmcnt(0)\n"
      "v_mfma_f32_16x16x32_bf16 v[124:127], v[196:199], a[152:155], v[124:127]\n"
      "s_add_u32 m0, s64, 22528\n"
      "v_cvt_pk_bf16_f32 v228, v228, v229\n"
      "v_mul_f32_e64 v231, v135, v231\n"
      "v_mfma_f32_16x16x32_bf16 v[124:127], v[180:183], a[156:159], v[124:127]\n"
      "buffer_load_dwordx4 v17, s[16:19], s45 offen  lds\n"
      "v_mul_f32 v122, v135, v122\n"
      "v_mul_f32 v230, v135, v230\n"
      "v_mul_f32 v99, v77, v99\n"
      "v_mul_f32 v224, v135, v224\n"
      "v_mul_f32 v155, v77, v155\n"
      "v_mul_f32 v100, v77, v100\n"
      "v_cvt_pk_bf16_f32 v153, v154, v155\n"
      "v_permlane32_swap_b32 v187, v82\n"
      "s_add_u32 m0, s64, 26624\n"
      "v_mul_f32 v225, v135, v225\n"
      "v_mfma_f32_16x16x32_bf16 v[124:127], v[204:207], a[160:163], v[124:127]\n"
      "buffer_load_dwordx4 v18, s[16:19], s45 offen  lds\n"
      "v_mul_f32 v101, v77, v101\n"
      "v_mul_f32 v226, v135, v226\n"
      "v_cvt_pk_bf16_f32 v229, v230, v231\n"
      "v_mfma_f32_16x16x32_bf16 v[124:127], v[156:159], a[164:167], v[124:127]\n"
      "ds_read_b64_tr_b16 a[250:251], v78 offset:2056\n"
      "s_add_u32 m0, s64, 30720\n"
      "v_mul_f32_e64 v120, v135, v120\n"
      "v_cvt_pk_bf16_f32 v154, v100, v101\n"
      "v_mul_f32 v97, v77, v97\n"
      "v_add_f32 v187, v187, v82\n"
      "v_cvt_pk_bf16_f32 v96, v96, v97\n"
      "v_mul_f32_e64 v103, v77, v103\n"
      "v_cvt_pk_bf16_f32 v230, v120, v121\n"
      "v_mfma_f32_16x16x32_bf16 v[128:131], v[248:251], a[192:195], 0\n"
      "buffer_load_dwordx4 v19, s[16:19], s45 offen  lds\n"
      "ds_read_b64_tr_b16 a[254:255], v78 offset:14344\n"
      "v_cvt_pk_bf16_f32 v97, v98, v99\n"
      "v_rsq_f32 v187, v187\n"
      "v_add_u32 v255, 49152, v169\n"
      "v_cvt_pk_bf16_f32 v77, v126, v127\n"
      "v_mul_f32 v227, v135, v227\n"
      "v_mov_b32_e64 v82, v187\n"
      "s_add_u32 m0, s65, 34816\n"
      "v_mfma_f32_16x16x32_bf16 v[128:131], v[160:163], a[196:199], v[128:131]\n"
      "ds_read_b64_tr_b16 a[226:227], v78 offset:2048\n"
      "v_cvt_pk_bf16_f32 v224, v224, v225\n"
      "v_cvt_pk_bf16_f32 v155, v102, v103\n"
      "v_cvt_pk_bf16_f32 v231, v122, v123\n"
      "v_permlane16_swap_b32_e64 v187, v82\n"
      "v_mfma_f32_16x16x32_bf16 v[120:123], v[248:251], a[168:171], 0\n"
      "buffer_load_dwordx4 v15, s[16:19], s45 offen  lds\n"
      "ds_read_b64_tr_b16 a[228:229], v78 offset:12288\n"
      "ds_read_b128 v[248:251], v8 offset:28672\n"
      "v_cvt_pk_bf16_f32 v225, v226, v227\n"
      "v_mfma_f32_16x16x32_bf16 v[120:123], v[160:163], a[172:175], v[120:123]\n"
      "ds_read_b64_tr_b16 v[240:241], v86\n"
      "ds_read_b64_tr_b16 v[246:247], v86 offset:14336\n"
      "ds_read_b64_tr_b16 v[242:243], v86 offset:2048\n"
      "v_mfma_f32_16x16x32_bf16 v[120:123], v[196:199], a[176:179], v[120:123]\n"
      "buffer_load_dwordx4 v[160:163], v36, s[28:31], s47 offen\n"
      "s_mov_b32 exec_lo, 0x1f001f\n"
      "s_mov_b32 exec_hi, 0x1f001f\n"
      "ds_write_b128 v133, v[152:155] offset:18688\n"
      "ds_write_b64 v133, v[96:97] offset:18704\n"
      "ds_write_b64 v133, v[224:225] offset:18712\n"
      "ds_write_b128 v133, v[228:231] offset:18720\n"
      "s_mov_b64 exec, -1\n"
      "ds_read_b64_tr_b16 a[252:253], v78 offset:12296\n"
      "ds_read_b64_tr_b16 a[224:225], v78\n"
      "v_mul_f32 v187, s50, v187\n"
      "v_mfma_f32_16x16x32_bf16 v[120:123], v[180:183], a[180:183], v[120:123]\n"
      "buffer_load_dword v89, v29, s[28:31], s47 offen\n"
      "ds_read_b128 v[228:231], v169 offset:24576\n"
      "v_mov_b32 v99, 0xbc80bc80\n"
      "v_mfma_f32_16x16x32_bf16 v[120:123], v[204:207], a[184:187], v[120:123]\n"
      "ds_read_b64_tr_b16 v[244:245], v86 offset:12288\n"
      "ds_read_b64_tr_b16 a[248:249], v78 offset:8\n"
      "v_mfma_f32_16x16x32_bf16 v[120:123], v[156:159], a[188:191], v[120:123]\n"
      "ds_read_b128 v[224:227], v8 offset:24576\n"
      "ds_read_b64_tr_b16 a[230:231], v78 offset:14336\n"
      "v_mfma_f32_16x16x32_bf16 v[128:131], v[196:199], a[200:203], v[128:131]\n"
      "ds_read_b128 v[194:197], v8 offset:32768\n"
      "v_mul_f32 v178, v82, v178\n"
      "v_add_u32 v152, 49152, v8\n"
      "v_mov_b32 v96, 0xbc80bc80\n"
      "v_mul_f32_e64 v106, v82, v106\n"
      "v_mfma_f32_16x16x32_bf16 v[128:131], v[180:183], a[204:207], v[128:131]\n"
      "ds_read_b128 v[180:183], v152\n"
      "s_waitcnt vmcnt(10)\n"
      "v_or3_b32 v216, v216, v217, v218\n"
      "v_mfma_f32_16x16x32_bf16 v[128:131], v[204:207], a[208:211], v[128:131]\n"
      "ds_read_b128 v[204:207], v169 offset:28672\n"
      "v_mov_b32 v98, 0xbc80bc80\n"
      "v_mfma_f32_16x16x32_bf16 v[128:131], v[156:159], a[212:215], v[128:131]\n"
      "ds_read_b128 v[154:157], v169 offset:32768\n"
      "v_lshrrev_b32 v186, 4, v186\n"
      "v_mov_b32 v97, 0xbc80bc80\n"
      "v_mul_f32_e64 v223, v82, v223\n"
      "v_mul_f32_e64 v142, v187, v142\n"
      "s_waitcnt vmcnt(9)\n"
      "v_or3_b32 v216, v216, v219, v138\n"
      "v_mul_f32 v141, v187, v141\n"
      "s_waitcnt lgkmcnt(4)\n"
      "v_mfma_f32_16x16x32_bf16 v[100:103], v[224:227], v[96:99], 0\n"
      "ds_write_b64 v134, v[136:137] offset:64\n"
      "ds_read_b128 v[224:227], v1 offset:2048\n"
      "v_mul_f32_e64 v176, v82, v176\n"
      "v_mfma_f32_16x16x32_bf16 v[100:103], v[228:231], v[96:99], v[100:103]\n"
      "ds_read_b128 v[228:231], v255 offset:4096\n"
      "v_and_b32 v186, 48, v186\n"
      "v_mfma_f32_16x16x32_bf16 v[100:103], v[248:251], v[96:99], v[100:103]\n"
      "ds_read_b128 v[248:251], v255\n"
      "s_waitcnt lgkmcnt(4)\n"
      "v_mfma_f32_16x16x32_bf16 v[100:103], v[204:207], v[96:99], v[100:103]\n"
      "ds_read_b128 v[204:207], v255 offset:8192\n"
      "v_cvt_pk_bf16_f32 v76, v124, v125\n"
      "v_mfma_f32_16x16x32_bf16 v[100:103], v[194:197], v[96:99], v[100:103]\n"
      "ds_read_b128 v[194:197], v152 offset:4096\n"
      "v_mul_f32 v177, v82, v177\n"
      "v_mfma_f32_16x16x32_bf16 v[100:103], v[154:157], v[96:99], v[100:103]\n"
      "ds_read_b128 v[124:127], v152 offset:8192\n"
      "ds_read_b128 v[156:159], v0 offset:8192\n"
      "v_mul_f32 v201, v187, v201\n"
      "s_mov_b32 exec_lo, 0x1f001f\n"
      "s_mov_b32 exec_hi, 0x1f001f\n"
      "ds_write_b64 v134, v[188:189] offset:18688\n"
      "ds_write_b64 v134, v[148:149] offset:18720\n"
      "ds_write_b64 v134, v[252:253] offset:18752\n"
      "s_mov_b64 exec, -1\n"
      "v_mfma_f32_16x16x32_bf16 v[188:191], v[180:183], v[96:99], 0\n"
      "ds_read_b128 v[180:183], v0 offset:2048\n"
      "v_mul_f32 v115, v187, v115\n"
      "v_cvt_pk_bf16_f32 v84, v128, v129\n"
      "v_cvt_pk_bf16_f32 v85, v130, v131\n"
      "v_mul_f32 v112, v187, v112\n"
      "v_readlane_b32 s45, v37, s43\n"
      "s_waitcnt lgkmcnt(5)\n"
      "v_mfma_f32_16x16x32_bf16 v[188:191], v[248:251], v[96:99], v[188:191]\n"
      "ds_read_b128 v[248:251], v1 offset:14336\n"
      "v_mul_f32 v140, v187, v140\n"
      "v_mul_f32 v114, v187, v114\n"
      "v_mul_f32_e64 v107, v82, v107\n"
      "v_mfma_f32_16x16x32_bf16 v[188:191], v[194:197], v[96:99], v[188:191]\n"
      "ds_read_b128 v[168:171], v1 offset:8192\n"
      "v_mul_f32 v179, v82, v179\n"
      "v_mul_f32 v105, v82, v105\n"
      "v_mfma_f32_16x16x32_bf16 v[188:191], v[228:231], v[96:99], v[188:191]\n"
      "s_or_b32 s95, s45, s46\n"
      "v_mul_f32 v104, v82, v104\n"
      "v_add_u32 v186, 123136, v186\n"
      "v_mul_f32 v113, v187, v113\n"
      "v_mul_f32 v220, v82, v220\n"
      "v_mfma_f32_16x16x32_bf16 v[188:191], v[124:127], v[96:99], v[188:191]\n"
      "s_waitcnt lgkmcnt(2)\n"
      "v_cvt_pk_bf16_f32 v140, v140, v141\n"
      "v_mul_f32 v102, 0x3eaaaaab, v102\n"
      "v_mul_f32 v100, 0x3eaaaaab, v100\n"
      "v_add_u32 v149, s92, v186\n"
      "v_mfma_f32_16x16x32_bf16 v[188:191], v[204:207], v[96:99], v[188:191]\n"
      "ds_read_b128 v[204:207], v0 offset:14336\n"
      "v_cvt_pk_bf16_f32 v81, v122, v123\n"
      "v_mfma_f32_16x16x32_bf16 v[194:197], v[180:183], a[144:147], 0\n"
      "s_barrier\n"
      "v_mul_f32_e64 v143, v187, v143\n"
      "v_cvt_pk_bf16_f32 v80, v120, v121\n"
      "v_mfma_f32_16x16x32_bf16 v[194:197], v[224:227], a[148:151], v[194:197]\n"
      "s_waitcnt lgkmcnt(1)\n"
      "v_mul_f32 v188, 0x3eaaaaab, v188\n"
      "v_mul_f32 v190, 0x3eaaaaab, v190\n"
      "v_mfma_f32_16x16x32_bf16 v[194:197], v[156:159], a[152:155], v[194:197]\n"
      "ds_read_b128 v[124:127], v152 offset:24576\n"
      "v_and_b32 v216, 0x7fff7fff, v216\n"
      "v_mfma_f32_16x16x32_bf16 v[132:135], v[180:183], a[168:171], 0\n"
      "ds_read_b64_tr_b16 v[252:253], v86 offset:36864\n"
      "v_cvt_pk_bf16_f32 v141, v142, v143\n"
      "v_mfma_f32_16x16x32_bf16 v[132:135], v[224:227], a[172:175], v[132:135]\n"
      "s_or_b32 s95, s95, s48\n"
      "v_cvt_pk_bf16_f32 v112, v112, v113\n"
      "v_mul_f32 v200, v187, v200\n"
      "v_cvt_pk_bf16_f32 v176, v176, v177\n"
      "v_mfma_f32_16x16x32_bf16 v[194:197], v[168:171], a[156:159], v[194:197]\n"
      "ds_write_b32 v31, v216 offset:6656\n"
      "v_cvt_pk_bf16_f32 v113, v114, v115\n"
      "v_mul_f32_e64 v203, v187, v203\n"
      "v_mfma_f32_16x16x32_bf16 v[214:217], v[180:183], a[192:195], 0\n"
      "ds_read_b64_tr_b16 a[234:235], v78 offset:26624\n"
      "v_cvt_pk_bf16_f32 v114, v200, v201\n"
      "v_mul_f32 v103, 0x3eaaaaab, v103\n"
      "v_mfma_f32_16x16x32_bf16 v[214:217], v[224:227], a[196:199], v[214:217]\n"
      "s_waitcnt lgkmcnt(3)\n"
      "v_mul_f32 v189, 0x3eaaaaab, v189\n"
      "v_cvt_pk_bf16_f32 v177, v178, v179\n"
      "v_mfma_f32_16x16x32_bf16 v[194:197], v[204:207], a[160:163], v[194:197]\n"
      "ds_read_b128 v[226:229], v0\n"
      "v_mul_f32 v221, v82, v221\n"
      "v_mul_f32 v191, 0x3eaaaaab, v191\n"
      "v_mfma_f32_16x16x32_bf16 v[194:197], v[248:251], a[164:167], v[194:197]\n"
      "ds_write_b128 v149, v[188:191] offset:256\n"
      "v_mul_f32 v222, v82, v222\n"
      "v_mfma_f32_16x16x32_bf16 v[214:217], v[156:159], a[200:203], v[214:217]\n"
      "ds_read_b128 v[188:191], v255 offset:28672\n"
      "s_waitcnt vmcnt(8)\n"
      "v_or3_b32 v116, v116, v117, v118\n"
      "v_mfma_f32_16x16x32_bf16 v[214:217], v[168:171], a[204:207], v[214:217]\n"
      "ds_read_b128 v[180:183], v152 offset:28672\n"
      "v_cvt_pk_bf16_f32 v178, v104, v105\n"
      "v_subrev_u32 v118, 115456, v9\n"
      "v_cvt_pk_bf16_f32 v197, v196, v197\n"
      "v_mfma_f32_16x16x32_bf16 v[214:217], v[204:207], a[208:211], v[214:217]\n"
      "ds_read_b128 v[136:139], v255 offset:24576\n"
      "v_cvt_pk_bf16_f32 v196, v194, v195\n"
      "v_mfma_f32_16x16x32_bf16 v[132:135], v[156:159], a[176:179], v[132:135]\n"
      "ds_read_b128 v[128:131], v152 offset:32768\n"
      "v_cvt_pk_bf16_f32 v220, v220, v221\n"
      "v_mfma_f32_16x16x32_bf16 v[132:135], v[168:171], a[180:183], v[132:135]\n"
      "ds_read_b64_tr_b16 v[230:231], v78 offset:38920\n"
      "v_bfe_u32 v152, v118, 8, 2\n"
      "v_mfma_f32_16x16x32_bf16 v[132:135], v[204:207], a[184:187], v[132:135]\n"
      "ds_read_b128 v[204:207], v1\n"
      "v_mul_f32 v101, 0x3eaaaaab, v101\n"
      "v_mfma_f32_16x16x32_bf16 v[214:217], v[248:251], a[212:215], v[214:217]\n"
      "ds_read_b128 v[168:171], v0 offset:6144\n"
      "v_cvt_pk_bf16_f32 v179, v106, v107\n"
      "s_waitcnt vmcnt(7)\n"
      "v_or3_b32 v116, v116, v119, v254\n"
      "v_mfma_f32_16x16x32_bf16 v[132:135], v[248:251], a[188:191], v[132:135]\n"
      "ds_read_b128 v[198:201], v1 offset:6144\n"
      "ds_read_b64_tr_b16 a[232:233], v78 offset:24576\n"
      "v_mfma_f32_16x16x32_bf16 v[154:157], v[124:127], v[96:99], 0\n"
      "ds_read_b128 v[124:127], v255 offset:32768\n"
      "s_waitcnt lgkmcnt(6)\n"
      "v_mfma_f32_16x16x32_bf16 v[104:107], v[226:229], a[168:171], 0\n"
      "ds_write_b128 v149, v[100:103]\n"
      "v_mfma_f32_16x16x32_bf16 v[120:123], v[226:229], a[144:147], 0\n"
      "ds_read_b64_tr_b16 a[236:237], v78 offset:36864\n"
      "v_mul_f32 v202, v187, v202\n"
      "v_mfma_f32_16x16x32_bf16 v[154:157], v[136:139], v[96:99], v[154:157]\n"
      "ds_read_b64_tr_b16 v[224:225], v78 offset:24584\n"
      "v_cvt_pk_bf16_f32 v135, v134, v135\n"
      "v_mfma_f32_16x16x32_bf16 v[154:157], v[180:183], v[96:99], v[154:157]\n"
      "ds_read_b128 v[180:183], v1 offset:12288\n"
      "v_bfe_u32 v118, v118, 4, 4\n"
      "v_lshlrev_b32 v152, 2, v152\n"
      "v_mfma_f32_16x16x32_bf16 v[154:157], v[188:191], v[96:99], v[154:157]\n"
      "s_waitcnt lgkmcnt(4)\n"
      "v_sub_u32 v118, v118, v152\n"
      "v_cvt_pk_bf16_f32 v134, v132, v133\n"
      "v_mfma_f32_16x16x32_bf16 v[154:157], v[128:131], v[96:99], v[154:157]\n"
      "ds_read_b64_tr_b16 v[248:249], v86 offset:24576\n"
      "v_mov_b32 v188, 0\n"
      "v_mfma_f32_16x16x32_bf16 v[100:103], v[226:229], a[192:195], 0\n"
      "ds_read_b128 v[128:131], v0 offset:12288\n"
      "v_lshlrev_b32 v118, 4, v118\n"
      "v_mfma_f32_16x16x32_bf16 v[154:157], v[124:127], v[96:99], v[154:157]\n"
      "s_cmp_ge_u32 s95, 0x40000000\n"
      "s_cselect_b32 s94, -1, -2\n"
      "v_mov_b32 v153, 0\n"
      "v_mfma_f32_16x16x32_bf16 v[124:127], v[108:111], v[172:175], v[56:59]\n"
      "ds_read_b64_tr_b16 v[254:255], v86 offset:38912\n"
      "v_and_b32 v116, 0x7fff7fff, v116\n"
      "v_mfma_f32_16x16x32_bf16 v[120:123], v[204:207], a[148:151], v[120:123]\n"
      "ds_read_b64_tr_b16 v[226:227], v78 offset:26632\n"
      "v_mul_f32 v155, 0x3eaaaaab, v155\n"
      "v_mfma_f32_16x16x32_bf16 v[120:123], v[168:171], a[152:155], v[120:123]\n"
      "ds_read_u16 v82, v11 offset:32\n"
      "v_mul_f32 v154, 0x3eaaaaab, v154\n"
      "v_mfma_f32_16x16x32_bf16 v[120:123], v[198:201], a[156:159], v[120:123]\n"
      "ds_write_b32 v31, v116 offset:5632\n"
      "v_mfma_f32_16x16x32_bf16 v[100:103], v[204:207], a[196:199], v[100:103]\n"
      "ds_read_b64_tr_b16 v[228:229], v78 offset:36872\n"
      "v_mul_f32 v157, 0x3eaaaaab, v157\n"
      "v_mfma_f32_16x16x32_bf16 v[100:103], v[168:171], a[200:203], v[100:103]\n"
      "s_waitcnt lgkmcnt(5)\n"
      "v_mov_b32 v152, 0x3f80\n"
      "v_mul_f32 v156, 0x3eaaaaab, v156\n"
      "v_mfma_f32_16x16x32_bf16 v[120:123], v[128:131], a[160:163], v[120:123]\n"
      "ds_read_b64_tr_b16 a[238:239], v78 offset:38912\n"
      "v_lshlrev_b64 v[152:153], v118, v[152:153]\n"
      "v_mfma_f32_16x16x32_bf16 v[100:103], v[198:201], a[204:207], v[100:103]\n"
      "ds_read_b64_tr_b16 v[250:251], v86 offset:26624\n"
      "v_cmp_gt_u32 s[80:81], 64, v118\n"
      "v_mfma_f32_16x16x32_bf16 v[120:123], v[180:183], a[164:167], v[120:123]\n"
      "ds_write_b128 v149, v[154:157] offset:512\n"
      "v_mfma_f32_16x16x32_bf16 v[136:139], v[144:147], v[208:211], v[40:43]\n"
      "s_waitcnt lgkmcnt(0)\n"
      "s_barrier\n"
      "v_cvt_pk_bf16_f32 v217, v216, v217\n"
      "v_mfma_f32_16x16x32_bf16 v[104:107], v[204:207], a[172:175], v[104:107]\n"
      "ds_read_b128 v[148:151], v10 offset:32\n"
      "v_mov_b32 v159, 0\n"
      "v_mfma_f32_16x16x32_bf16 v[104:107], v[168:171], a[176:179], v[104:107]\n"
      "ds_read_b128 v[168:171], v9 offset:5632\n"
      "v_cvt_pk_bf16_f32 v216, v214, v215\n"
      "v_mfma_f32_16x16x32_bf16 v[104:107], v[198:201], a[180:183], v[104:107]\n"
      "ds_read_b128 v[96:99], v9 offset:6656\n"
      "v_cndmask_b32_e64 v191, 0, v153, s[80:81]\n"
      "v_mfma_f32_16x16x32_bf16 v[100:103], v[128:131], a[208:211], v[100:103]\n"
      "ds_read_b128 v[116:119], v186 offset:64\n"
      "v_cndmask_b32_e64 v190, 0, v152, s[80:81]\n"
      "v_mfma_f32_16x16x32_bf16 v[104:107], v[128:131], a[184:187], v[104:107]\n"
      "ds_read_b64 v[128:129], v10 offset:24\n"
      "s_waitcnt vmcnt(1)\n"
      "v_or3_b32 v160, v160, v161, v162\n"
      "v_mfma_f32_16x16x16_bf16 v[136:139], v[184:185], v[212:213], v[136:139]\n"
      "v_readlane_b32 s47, v38, s43\n"
      "v_cvt_pk_bf16_f32 v115, v202, v203\n"
      "v_cvt_pk_bf16_f32 v195, v122, v123\n"
      "v_mfma_f32_16x16x32_bf16 v[100:103], v[180:183], a[212:215], v[100:103]\n"
      "ds_read_u16 v78, v11\n"
      "v_cndmask_b32_e64 v157, 0, v153, s[80:81]\n"
      "v_mfma_f32_16x16x32_bf16 v[104:107], v[180:183], a[188:191], v[104:107]\n"
      "s_cmp_gt_u32 s42, s44\n"
      "s_cselect_b32 s95, s94, s95\n"
      "v_mov_b32 v189, 0\n"
      "v_mfma_f32_16x16x32_bf16 v[180:183], v[108:111], v[208:211], v[44:47]\n"
      "s_waitcnt lgkmcnt(3)\n"
      "v_cvt_pk_bf16_f32 v194, v120, v121\n"
      "v_or3_b32 v96, v96, v97, v98\n"
      "v_mfma_f32_16x16x32_bf16 v[200:203], v[148:151], v[208:211], v[24:27]\n"
      "ds_read_u16 v86, v11 offset:64\n"
      "v_cndmask_b32_e64 v156, 0, v152, s[80:81]\n"
      "v_cvt_pk_bf16_f32 v221, v222, v223\n"
      "v_mfma_f32_16x16x16_bf16 v[180:183], v[166:167], v[212:213], v[180:183]\n"
      "v_or_b32 v96, v96, v99\n"
      "v_cvt_pk_bf16_f32 v214, v100, v101\n"
      "v_cmp_ne_u32 s[56:57], 0, v96\n"
      "v_mfma_f32_16x16x32_bf16 v[96:99], v[176:179], v[208:211], v[48:51]\n"
      "s_waitcnt lgkmcnt(2)\n"
      "v_or3_b32 v168, v168, v169, v170\n"
      "v_cvt_pk_bf16_f32 v215, v102, v103\n"
      "v_mfma_f32_16x16x32_bf16 v[208:211], v[144:147], v[172:175], v[52:55]\n"
      "ds_read_b128 v[152:155], v186\n"
      "v_cvt_pk_bf16_f32 v132, v104, v105\n"
      "v_mfma_f32_16x16x16_bf16 v[200:203], v[128:129], v[212:213], v[200:203]\n"
      "s_waitcnt vmcnt(0)\n"
      "v_or3_b32 v160, v160, v163, v89\n"
      "v_and_b32 v160, 0x7fff7fff, v160\n"
      "v_or_b32 v168, v168, v171\n"
      "v_mfma_f32_16x16x16_bf16 v[96:99], v[220:221], v[212:213], v[96:99]\n"
      "ds_write_b32 v31, v160 offset:4608\n"
      "v_mfma_f32_16x16x32_bf16 v[160:163], v[188:191], v[232:235], v[116:119]\n"
      "s_or_b64 vcc, s[56:57], s[54:55]\n"
      "v_cmp_ne_u32 s[56:57], 0, v168\n"
      "v_cvt_pk_bf16_f32 v133, v106, v107\n"
      "v_mfma_f32_16x16x32_bf16 v[168:171], v[188:191], a[216:219], v[116:119]\n"
      "s_cbranch_vccnz L_rare_1_%=\n"
      "L_back_2_%=:\n"
      "v_mfma_f32_16x16x32_bf16 v[102:105], v[188:191], a[240:243], v[116:119]\n"
      "s_nop 2\n"
      "s_cbranch_vccnz L_rare_3_%=\n"
      "L_back_4_%=:\n"
      "v_exp_f32 v200, v200\n"
      "v_exp_f32 v99, v99\n"
      "v_exp_f32_e64 v181, v181\n"
      "v_mfma_f32_16x16x32_bf16 v[118:121], v[176:179], v[172:175], v[60:63]\n"
      "v_cvt_pk_bf16_f32 v170, v170, v171\n"
      "v_exp_f32 v182, v182\n"
      "v_exp_f32 v98, v98\n"
      "v_mfma_f32_16x16x16_bf16 v[208:211], v[184:185], v[192:193], v[208:211]\n"
      "v_exp_f32 v96, v96\n"
      "v_exp_f32 v137, v137\n"
      "v_mfma_f32_16x16x32_bf16 v[202:205], v[148:151], v[172:175], v[26:29]\n"
      "s_waitcnt lgkmcnt(0)\n"
      "v_exp_f32 v139, v139\n"
      "v_mfma_f32_16x16x16_bf16 v[124:127], v[166:167], v[192:193], v[124:127]\n"
      "v_cvt_pk_bf16_f32 v104, v104, v105\n"
      "v_exp_f32 v97, v97\n"
      "v_mfma_f32_16x16x16_bf16 v[118:121], v[220:221], v[192:193], v[118:121]\n"
      "v_exp_f32 v138, v138\n"
      "v_mov_b32 v158, 0\n"
      "v_mfma_f32_16x16x16_bf16 v[202:205], v[128:129], v[192:193], v[202:205]\n"
      "v_exp_f32 v136, v136\n"
      "v_cvt_pk_bf16_f32 v88, v96, v97\n"
      "v_cvt_pk_bf16_f32 v102, v102, v103\n"
      "v_exp_f32 v180, v180\n"
      "v_exp_f32 v183, v183\n"
      "v_mfma_f32_16x16x32_bf16 v[172:175], v[156:159], v[232:235], v[152:155]\n"
      "v_cvt_pk_bf16_f32 v136, v136, v137\n"
      "v_cvt_pk_bf16_f32 v137, v138, v139\n"
      "v_cvt_pk_bf16_f32 v138, v180, v181\n"
      "v_cvt_pk_bf16_f32 v235, v162, v163\n"
      "v_mfma_f32_16x16x32_bf16 v[204:207], v[156:159], a[216:219], v[152:155]\n"
      "v_cvt_pk_bf16_f32 v139, v182, v183\n"
      "v_cvt_pk_bf16_f32 v90, v200, 0\n"
      "v_mfma_f32_16x16x32_bf16 v[180:183], v[156:159], a[240:243], v[152:155]\n"
      "v_cvt_pk_bf16_f32 v168, v168, v169\n"
      "v_accvgpr_write_b32 a242, v102\n"
      "v_cvt_pk_bf16_f32 v89, v98, v99\n"
      "v_accvgpr_write_b32 a219, v170\n"
      "v_cvt_pk_bf16_f32 v233, v174, v175\n"
      "v_cvt_pk_bf16_f32 v204, v204, v205\n"
      "v_accvgpr_write_b32 a243, v104\n"
      "v_mfma_f32_16x16x32_bf16 v[162:165], v[194:197], v[136:139], 0\n"
      "s_cbranch_vccnz L_rare_5_%=\n"
      "L_back_6_%=:\n"
      "v_mfma_f32_16x16x32_bf16 v[162:165], v[76:79], v[88:91], v[162:165]\n"
      "v_exp_f32 v211, v211\n"
      "v_mfma_f32_16x16x32_bf16 v[152:155], v[32:35], v[136:139], 0\n"
      "v_cvt_pk_bf16_f32 v232, v172, v173\n"
      "v_cvt_pk_bf16_f32 v206, v206, v207\n"
      "v_mfma_f32_16x16x32_bf16 v[152:155], v[32:35], v[88:91], v[152:155]\n"
      "v_exp_f32 v125, v125\n"
      "v_cvt_pk_bf16_f32 v182, v182, v183\n"
      "v_exp_f32 v209, v209\n"
      "v_exp_f32 v210, v210\n"
      "v_accvgpr_write_b32 a217, v206\n"
      "v_mfma_f32_16x16x32_bf16 v[172:175], v[132:135], v[136:139], 0\n"
      "v_exp_f32 v208, v208\n"
      "v_mfma_f32_16x16x32_bf16 v[172:175], v[80:83], v[88:91], v[172:175]\n"
      "ds_read_b128 v[198:201], v186 offset:128\n"
      "v_exp_f32 v118, v118\n"
      "v_mfma_f32_16x16x32_bf16 v[98:101], v[148:151], v[112:115], v[28:31]\n"
      "v_accvgpr_write_b32 a218, v168\n"
      "v_rcp_f32 v152, v152\n"
      "v_exp_f32_e64 v119, v119\n"
      "v_accvgpr_write_b32 a216, v204\n"
      "v_mfma_f32_16x16x32_bf16 v[204:207], v[214:217], v[136:139], 0\n"
      "ds_read_b128 v[168:171], v186 offset:192\n"
      "v_cvt_pk_bf16_f32 v180, v180, v181\n"
      "v_mfma_f32_16x16x32_bf16 v[204:207], v[84:87], v[88:91], v[204:207]\n"
      "s_add_u32 s43, s43, 1\n"
      "v_cvt_pk_bf16_f32 v208, v208, v209\n"
      "v_mul_f32 v162, v152, v162\n"
      "v_mul_f32 v165, v152, v165\n"
      "v_accvgpr_write_b32 a241, v182\n"
      "v_mfma_f32_16x16x16_bf16 v[98:101], v[128:129], v[140:141], v[98:101]\n"
      "v_exp_f32 v127, v127\n"
      "v_cvt_pk_bf16_f32 v92, v118, v119\n"
      "v_mul_f32 v163, v152, v163\n"
      "v_exp_f32 v202, v202\n"
      "v_cvt_pk_bf16_f32 v209, v210, v211\n"
      "v_mul_f32 v175, v152, v175\n"
      "v_mul_f32 v164, v152, v164\n"
      "v_mfma_f32_16x16x32_bf16 v[136:139], v[176:179], v[112:115], v[72:75]\n"
      "s_waitcnt lgkmcnt(1)\n"
      "v_mul_f32 v206, v152, v206\n"
      "v_mul_f32_e64 v204, v152, v204\n"
      "v_mul_f32_e64 v172, v152, v172\n"
      "v_exp_f32_e64 v126, v126\n"
      "v_cvt_pk_bf16_f32 v234, v160, v161\n"
      "v_mfma_f32_16x16x32_bf16 v[100:103], v[156:159], a[220:223], v[198:201]\n"
      "v_cvt_pk_bf16_f32 v162, v162, v163\n"
      "v_mul_f32_e64 v205, v152, v205\n"
      "v_accvgpr_write_b32 a240, v180\n"
      "v_mfma_f32_16x16x32_bf16 v[180:183], v[156:159], a[244:247], v[198:201]\n"
      "v_cvt_pk_bf16_f32 v204, v204, v205\n"
      "v_mul_f32_e64 v174, v152, v174\n"
      "v_cvt_pk_bf16_f32 v163, v164, v165\n"
      "v_cvt_pk_bf16_f32 v165, v174, v175\n"
      "v_mul_f32_e64 v173, v152, v173\n"
      "v_mul_f32_e64 v207, v152, v207\n"
      "v_mfma_f32_16x16x32_bf16 v[104:107], v[156:159], v[236:239], v[198:201]\n"
      "s_waitcnt lgkmcnt(0)\n"
      "v_exp_f32 v121, v121\n"
      "v_cvt_pk_bf16_f32 v182, v182, v183\n"
      "v_cvt_pk_bf16_f32 v164, v172, v173\n"
      "v_mfma_f32_16x16x32_bf16 v[172:175], v[144:147], v[112:115], v[64:67]\n"
      "ds_write_b128 v3, v[162:165]\n"
      "v_mfma_f32_16x16x16_bf16 v[136:139], v[220:221], v[140:141], v[136:139]\n"
      "v_cvt_pk_bf16_f32 v205, v206, v207\n"
      "v_exp_f32_e64 v124, v124\n"
      "v_mfma_f32_16x16x32_bf16 v[160:163], v[188:191], a[244:247], v[168:171]\n"
      "ds_read_b128 v[152:155], v186 offset:256\n"
      "v_cvt_pk_bf16_f32 v210, v124, v125\n"
      "v_mfma_f32_16x16x16_bf16 v[172:175], v[184:185], v[140:141], v[172:175]\n"
      "v_cvt_pk_bf16_f32 v102, v102, v103\n"
      "v_cvt_pk_bf16_f32 v211, v126, v127\n"
      "v_exp_f32_e64 v120, v120\n"
      "v_mfma_f32_16x16x32_bf16 v[124:127], v[108:111], v[112:115], v[68:71]\n"
      "ds_read_b128 v[198:201], v186 offset:320\n"
      "v_cvt_pk_bf16_f32 v162, v162, v163\n"
      "v_cvt_pk_bf16_f32 v160, v160, v161\n"
      "v_mfma_f32_16x16x32_bf16 v[112:115], v[188:191], v[236:239], v[168:171]\n"
      "ds_write_b64 v4, v[204:205]\n"
      "v_cvt_pk_bf16_f32 v100, v100, v101\n"
      "v_mfma_f32_16x16x16_bf16 v[124:127], v[166:167], v[140:141], v[124:127]\n"
      "v_cvt_pk_bf16_f32 v180, v180, v181\n"
      "v_accvgpr_write_b32 a246, v160\n"
      "v_accvgpr_write_b32 a244, v180\n"
      "v_mfma_f32_16x16x32_bf16 v[204:207], v[188:191], a[220:223], v[168:171]\n"
      "s_waitcnt lgkmcnt(2)\n"
      "v_cvt_pk_bf16_f32 v94, v202, 0\n"
      "v_accvgpr_write_b32 a247, v162\n"
      "v_cvt_pk_bf16_f32 v236, v104, v105\n"
      "v_cvt_pk_bf16_f32 v239, v114, v115\n"
      "v_mfma_f32_16x16x32_bf16 v[160:163], v[156:159], a[248:251], v[152:155]\n"
      "s_cbranch_vccnz L_rare_7_%=\n"
      "L_back_8_%=:\n"
      "v_mfma_f32_16x16x32_bf16 v[140:143], v[156:159], a[224:227], v[152:155]\n"
      "v_exp_f32_e64 v172, v172\n"
      "v_cvt_pk_bf16_f32 v237, v106, v107\n"
      "v_cvt_pk_bf16_f32 v93, v120, v121\n"
      "v_mfma_f32_16x16x32_bf16 v[168:171], v[156:159], v[240:243], v[152:155]\n"
      "ds_read_b128 v[118:121], v10\n"
      "v_cvt_pk_bf16_f32 v238, v112, v113\n"
      "v_exp_f32_e64 v98, v98\n"
      "v_mfma_f32_16x16x32_bf16 v[112:115], v[32:35], v[208:211], 0\n"
      "v_accvgpr_write_b32 a220, v100\n"
      "v_cvt_pk_bf16_f32 v162, v162, v163\n"
      "v_exp_f32 v139, v139\n"
      "v_mfma_f32_16x16x32_bf16 v[112:115], v[32:35], v[92:95], v[112:115]\n"
      "s_waitcnt lgkmcnt(1)\n"
      "v_accvgpr_write_b32 a245, v182\n"
      "v_cvt_pk_bf16_f32 v142, v142, v143\n"
      "v_mfma_f32_16x16x32_bf16 v[180:183], v[188:191], v[240:243], v[198:201]\n"
      "v_cvt_pk_bf16_f32 v204, v204, v205\n"
      "v_cvt_pk_bf16_f32 v241, v170, v171\n"
      "v_mfma_f32_16x16x32_bf16 v[152:155], v[188:191], a[248:251], v[198:201]\n"
      "v_exp_f32_e64 v175, v175\n"
      "v_cvt_pk_bf16_f32 v206, v206, v207\n"
      "v_mfma_f32_16x16x32_bf16 v[104:107], v[188:191], a[224:227], v[198:201]\n"
      "v_cvt_pk_bf16_f32 v160, v160, v161\n"
      "v_exp_f32 v137, v137\n"
      "v_mfma_f32_16x16x32_bf16 v[200:203], v[132:135], v[208:211], 0\n"
      "s_add_u32 s42, s42, 1\n"
      "v_exp_f32_e64 v174, v174\n"
      "v_cvt_pk_bf16_f32 v243, v182, v183\n"
      "v_cvt_pk_bf16_f32 v240, v168, v169\n"
      "v_mfma_f32_16x16x32_bf16 v[200:203], v[80:83], v[92:95], v[200:203]\n"
      "v_cvt_pk_bf16_f32 v152, v152, v153\n"
      "v_exp_f32_e64 v173, v173\n"
      "v_accvgpr_write_b32 a221, v102\n"
      "v_mfma_f32_16x16x32_bf16 v[168:171], v[194:197], v[208:211], 0\n"
      "ds_read_b64 v[96:97], v10 offset:16\n"
      "v_cvt_pk_bf16_f32 v172, v172, v173\n"
      "v_mfma_f32_16x16x32_bf16 v[168:171], v[76:79], v[92:95], v[168:171]\n"
      "s_waitcnt lgkmcnt(1)\n"
      "v_exp_f32 v126, v126\n"
      "v_accvgpr_write_b32 a248, v160\n"
      "v_mfma_f32_16x16x32_bf16 v[114:117], v[118:121], v[108:111], v[30:33]\n"
      "v_exp_f32 v138, v138\n"
      "v_cvt_pk_bf16_f32 v104, v104, v105\n"
      "v_rcp_f32 v112, v112\n"
      "v_mfma_f32_16x16x32_bf16 v[108:111], v[118:121], v[148:151], v[30:33]\n"
      "v_mul_f32_e64 v203, v112, v203\n"
      "v_cvt_pk_bf16_f32 v106, v106, v107\n"
      "v_mfma_f32_16x16x32_bf16 v[100:103], v[214:217], v[208:211], 0\n"
      "ds_read_b128 v[210:213], v186 offset:384\n"
      "v_cvt_pk_bf16_f32 v154, v154, v155\n"
      "v_mfma_f32_16x16x32_bf16 v[100:103], v[84:87], v[92:95], v[100:103]\n"
      "v_mul_f32 v171, v112, v171\n"
      "v_mul_f32 v200, v112, v200\n"
      "v_mfma_f32_16x16x32_bf16 v[148:151], v[118:121], v[176:179], v[30:33]\n"
      "s_waitcnt lgkmcnt(1)\n"
      "v_cvt_pk_bf16_f32 v173, v174, v175\n"
      "v_exp_f32 v127, v127\n"
      "v_mfma_f32_16x16x16_bf16 v[114:117], v[96:97], v[166:167], v[114:117]\n"
      "v_mul_f32_e64 v202, v112, v202\n"
      "v_cvt_pk_bf16_f32 v89, v138, v139\n"
      "v_exp_f32 v125, v125\n"
      "v_mfma_f32_16x16x16_bf16 v[148:151], v[96:97], v[220:221], v[148:151]\n"
      "v_cvt_pk_bf16_f32 v90, v98, 0\n"
      "v_mul_f32 v201, v112, v201\n"
      "v_accvgpr_write_b32 a222, v204\n"
      "v_mfma_f32_16x16x16_bf16 v[108:111], v[96:97], v[128:129], v[108:111]\n"
      "v_cvt_pk_bf16_f32 v175, v126, v127\n"
      "v_mul_f32 v102, v112, v102\n"
      "v_exp_f32 v136, v136\n"
      "v_mfma_f32_16x16x32_bf16 v[218:221], v[118:121], v[144:147], v[30:33]\n"
      "s_waitcnt lgkmcnt(0)\n"
      "v_mul_f32 v170, v112, v170\n"
      "v_mul_f32 v101, v112, v101\n"
      "v_cvt_pk_bf16_f32 v88, v136, v137\n"
      "v_exp_f32 v124, v124\n"
      "v_mfma_f32_16x16x32_bf16 v[144:147], v[156:159], a[228:231], v[210:213]\n"
      "ds_read_b128 v[176:179], v186 offset:448\n"
      "v_cvt_pk_bf16_f32 v174, v124, v125\n"
      "v_mfma_f32_16x16x32_bf16 v[124:127], v[156:159], a[252:255], v[210:213]\n"
      "ds_read_b128 v[164:167], v186 offset:576\n"
      "v_mul_f32 v168, v112, v168\n"
      "v_mfma_f32_16x16x16_bf16 v[218:221], v[96:97], v[184:185], v[218:221]\n"
      "v_mul_f32 v100, v112, v100\n"
      "v_mul_f32 v169, v112, v169\n"
      "v_mul_f32 v103, v112, v103\n"
      "v_cvt_pk_bf16_f32 v168, v168, v169\n"
      "v_mfma_f32_16x16x32_bf16 v[110:113], v[194:197], v[172:175], 0\n"
      "ds_read_b128 v[128:131], v186 offset:704\n"
      "v_accvgpr_write_b32 a227, v106\n"
      "v_mfma_f32_16x16x32_bf16 v[110:113], v[76:79], v[88:91], v[110:113]\n"
      "ds_read_b128 v[220:223], v186 offset:512\n"
      "v_cndmask_b32 v218, v218, v114, s[58:59]\n"
      "v_cvt_pk_bf16_f32 v100, v100, v101\n"
      "v_mfma_f32_16x16x32_bf16 v[96:99], v[32:35], v[172:175], 0\n"
      "v_cndmask_b32 v218, v218, v148, s[60:61]\n"
      "v_cvt_pk_bf16_f32 v169, v170, v171\n"
      "v_mfma_f32_16x16x32_bf16 v[96:99], v[32:35], v[88:91], v[96:99]\n"
      "v_cvt_pk_bf16_f32 v101, v102, v103\n"
      "v_accvgpr_write_b32 a225, v142\n"
      "v_cndmask_b32 v218, v218, v108, s[62:63]\n"
      "v_mfma_f32_16x16x32_bf16 v[106:109], v[156:159], v[244:247], v[210:213]\n"
      "ds_read_b128 v[114:117], v186 offset:640\n"
      "v_accvgpr_write_b32 a223, v206\n"
      "v_mfma_f32_16x16x32_bf16 v[206:209], v[132:135], v[172:175], 0\n"
      "s_cbranch_vccnz L_rare_9_%=\n"
      "L_back_10_%=:\n"
      "v_rcp_f32 v96, v96\n"
      "v_mfma_f32_16x16x32_bf16 v[206:209], v[80:83], v[88:91], v[206:209]\n"
      "s_cmp_ge_u32 s95, 0x40000000\n"
      "s_cselect_b64 vcc, -1, s[56:57]\n"
      "v_accvgpr_write_b32 a226, v104\n"
      "v_mfma_f32_16x16x32_bf16 v[118:121], v[214:217], v[172:175], 0\n"
      "s_waitcnt lgkmcnt(1)\n"
      "v_cvt_pk_bf16_f32 v146, v146, v147\n"
      "v_exp_f32 v218, v218\n"
      "v_accvgpr_write_b32 a251, v154\n"
      "v_mfma_f32_16x16x32_bf16 v[118:121], v[84:87], v[88:91], v[118:121]\n"
      "ds_write_b64 v4, v[100:101] offset:256\n"
      "v_cvt_pk_bf16_f32 v218, v218, v218\n"
      "v_mfma_f32_16x16x32_bf16 v[98:101], v[156:159], a[232:235], v[220:223]\n"
      "s_and_b32 s47, s47, -4\n"
      "v_mul_f32 v206, v96, v206\n"
      "v_cvt_pk_bf16_f32 v124, v124, v125\n"
      "v_mul_f32 v110, v96, v110\n"
      "v_mul_f32 v209, v96, v209\n"
      "v_mul_f32_e64 v118, v96, v118\n"
      "v_cvt_pk_bf16_f32 v171, v202, v203\n"
      "v_cvt_pk_bf16_f32 v170, v200, v201\n"
      "v_mul_f32 v111, v96, v111\n"
      "v_cvt_pk_bf16_f32 v98, v98, v99\n"
      "v_mfma_f32_16x16x32_bf16 v[202:205], v[188:191], a[228:231], v[176:179]\n"
      "s_waitcnt lgkmcnt(1)\n"
      "v_cvt_pk_bf16_f32 v140, v140, v141\n"
      "v_cvt_pk_bf16_f32 v110, v110, v111\n"
      "v_mfma_f32_16x16x32_bf16 v[136:139], v[156:159], a[236:239], v[114:117]\n"
      "ds_write_b16 v13, v218\n"
      "v_mul_f32 v113, v96, v113\n"
      "v_mul_f32 v207, v96, v207\n"
      "v_mfma_f32_16x16x32_bf16 v[172:175], v[156:159], v[252:255], v[114:117]\n"
      "ds_read_b128 v[210:213], v14\n"
      "v_accvgpr_write_b32 a229, v146\n"
      "v_mfma_f32_16x16x32_bf16 v[146:149], v[188:191], a[252:255], v[176:179]\n"
      "v_cvt_pk_bf16_f32 v126, v126, v127\n"
      "v_accvgpr_write_b32 a249, v162\n"
      "v_mul_f32_e64 v112, v96, v112\n"
      "v_mul_f32_e64 v121, v96, v121\n"
      "v_mfma_f32_16x16x32_bf16 v[160:163], v[188:191], v[252:255], v[128:131]\n"
      "ds_write_b128 v3, v[168:171] offset:256\n"
      "v_mfma_f32_16x16x32_bf16 v[168:171], v[188:191], v[244:247], v[176:179]\n"
      "v_mul_f32_e64 v119, v96, v119\n"
      "v_cvt_pk_bf16_f32 v148, v148, v149\n"
      "v_cvt_pk_bf16_f32 v242, v180, v181\n"
      "v_mul_f32_e64 v208, v96, v208\n"
      "v_mfma_f32_16x16x32_bf16 v[178:181], v[188:191], a[236:239], v[128:131]\n"
      "v_cvt_pk_bf16_f32 v118, v118, v119\n"
      "v_cvt_pk_bf16_f32 v111, v112, v113\n"
      "v_cvt_pk_bf16_f32 v244, v106, v107\n"
      "v_mfma_f32_16x16x32_bf16 v[198:201], v[188:191], v[224:227], v[164:167]\n"
      "v_cvt_pk_bf16_f32 v204, v204, v205\n"
      "v_mul_f32_e64 v120, v96, v120\n"
      "v_accvgpr_write_b32 a250, v152\n"
      "v_cvt_pk_bf16_f32 v246, v168, v169\n"
      "v_accvgpr_write_b32 a231, v204\n"
      "v_cvt_pk_bf16_f32 v119, v120, v121\n"
      "v_cvt_pk_bf16_f32 v136, v136, v137\n"
      "v_cvt_pk_bf16_f32 v202, v202, v203\n"
      "v_cvt_pk_bf16_f32 v138, v138, v139\n"
      "v_cvt_pk_bf16_f32 v253, v174, v175\n"
      "v_cvt_pk_bf16_f32 v144, v144, v145\n"
      "v_mfma_f32_16x16x32_bf16 v[150:153], v[188:191], a[232:235], v[164:167]\n"
      "v_cvt_pk_bf16_f32 v178, v178, v179\n"
      "v_accvgpr_write_b32 a224, v140\n"
      "v_mfma_f32_16x16x32_bf16 v[140:143], v[156:159], v[228:231], v[114:117]\n"
      "ds_read_b128 v[184:187], v14 offset:64\n"
      "v_accvgpr_write_b32 a236, v136\n"
      "v_mfma_f32_16x16x32_bf16 v[114:117], v[156:159], v[224:227], v[220:223]\n"
      "v_cvt_pk_bf16_f32 v180, v180, v181\n"
      "v_cvt_pk_bf16_f32 v113, v208, v209\n"
      "v_cvt_pk_bf16_f32 v255, v162, v163\n"
      "v_accvgpr_write_b32 a238, v178\n"
      "v_cvt_pk_bf16_f32 v112, v206, v207\n"
      "v_mfma_f32_16x16x32_bf16 v[206:209], v[188:191], v[228:231], v[128:131]\n"
      "ds_write_b64 v4, v[118:119] offset:512\n"
      "v_accvgpr_write_b32 a239, v180\n"
      "v_mfma_f32_16x16x32_bf16 v[180:183], v[188:191], v[248:251], v[164:167]\n"
      "s_waitcnt lgkmcnt(2)\n"
      "v_accvgpr_write_b32 a252, v124\n"
      "v_accvgpr_write_b32 a253, v126\n"
      "v_mfma_f32_16x16x32_bf16 v[166:169], v[156:159], v[248:251], v[220:223]\n"
      "ds_write_b128 v3, v[110:113] offset:512\n"
      "v_cvt_pk_bf16_f32 v247, v170, v171\n"
      "v_cvt_pk_bf16_f32 v224, v114, v115\n"
      "v_cvt_pk_bf16_f32 v228, v140, v141\n"
      "v_cvt_pk_bf16_f32 v230, v206, v207\n"
      "v_cvt_pk_bf16_f32 v226, v198, v199\n"
      "v_cvt_pk_bf16_f32 v150, v150, v151\n"
      "v_cvt_pk_bf16_f32 v245, v108, v109\n"
      "v_cvt_pk_bf16_f32 v100, v100, v101\n"
      "v_accvgpr_write_b32 a237, v138\n"
      "v_cvt_pk_bf16_f32 v254, v160, v161\n"
      "v_mfma_f32_16x16x32_bf16 v[188:191], v[210:213], v[32:35], 0\n"
      "s_waitcnt lgkmcnt(1)\n"
      "v_accvgpr_write_b32 a232, v98\n"
      "v_cvt_pk_bf16_f32 v250, v180, v181\n"
      "v_cvt_pk_bf16_f32 v248, v166, v167\n"
      "v_accvgpr_write_b32 a228, v144\n"
      "v_accvgpr_write_b32 a230, v202\n"
      "v_cvt_pk_bf16_f32 v231, v208, v209\n"
      "v_accvgpr_write_b32 a233, v100\n"
      "v_accvgpr_write_b32 a255, v148\n"
      "v_cvt_pk_bf16_f32 v252, v172, v173\n"
      "v_cvt_pk_bf16_f32 v227, v200, v201\n"
      "v_cvt_pk_bf16_f32 v152, v152, v153\n"
      "v_accvgpr_write_b32 a234, v150\n"
      "v_accvgpr_write_b32 a235, v152\n"
      "v_cvt_pk_bf16_f32 v249, v168, v169\n"
      "v_cvt_pk_bf16_f32 v251, v182, v183\n"
      "v_cvt_pk_bf16_f32 v146, v146, v147\n"
      "v_accvgpr_write_b32 a254, v146\n"
      "v_cvt_pk_bf16_f32 v225, v116, v117\n"
      "v_cvt_pk_bf16_f32 v229, v142, v143\n"
      "v_mfma_f32_16x16x32_bf16 v[188:191], v[184:187], v[32:35], v[188:191]\n"
      "s_waitcnt lgkmcnt(0)\n"
      "s_barrier\n"
      "s_or_b64 vcc, vcc, s[54:55]\n"
      "s_cbranch_vccnz L_disp1_%=\n"
      "s_nop 7\n"
      "s_nop 7\n"
      "L_loop1_%=:\n"
      "v_mfma_f32_16x16x32_bf16 v[202:205], v[210:213], v[194:197], 0\n"
      "ds_read_b128 v[156:159], v0 offset:18432\n"
      "ds_read_b128 v[136:139], v1 offset:18432\n"
      "v_mfma_f32_16x16x32_bf16 v[202:205], v[184:187], v[76:79], v[202:205]\n"
      "ds_read_b128 v[170:173], v0 offset:24576\n"
      "s_add_u32 m0, s64, 0\n"
      "v_mfma_f32_16x16x32_bf16 v[102:105], v[210:213], v[132:135], 0\n"
      "buffer_load_dwordx4 v16, s[16:19], s45 offen  lds\n"
      "v_mfma_f32_16x16x32_bf16 v[102:105], v[184:187], v[80:83], v[102:105]\n"
      "s_add_u32 m0, s64, 4096\n"
      "v_mfma_f32_16x16x32_bf16 v[192:195], v[210:213], v[214:217], 0\n"
      "ds_read_b128 v[210:213], v9\n"
      "v_mfma_f32_16x16x32_bf16 v[192:195], v[184:187], v[84:87], v[192:195]\n"
      "v_mov_b32_dpp v188, v188 quad_perm:[0,0,0,0] row_mask:0xf bank_mask:0xf\n"
      "v_rcp_f32_e64 v188, v188\n"
      "s_waitcnt lgkmcnt(3)\n"
      "v_mfma_f32_16x16x32_bf16 v[148:151], a[0:3], v[156:159], 0\n"
      "buffer_load_dwordx4 v17, s[16:19], s45 offen  lds\n"
      "v_mfma_f32_16x16x32_bf16 v[198:201], a[24:27], v[156:159], 0\n"
      "s_add_u32 m0, s64, 8192\n"
      "v_cndmask_b32 v202, v202, v102, s[58:59]\n"
      "v_cndmask_b32 v202, v202, v192, s[60:61]\n"
      "v_mfma_f32_16x16x32_bf16 v[100:103], a[48:51], v[156:159], 0\n"
      "s_waitcnt lgkmcnt(1)\n"
      "v_mul_f32 v202, v188, v202\n"
      "v_cvt_pk_bf16_f32 v202, v202, v202\n"
      "v_mfma_f32_16x16x32_bf16 v[176:179], a[72:75], v[156:159], 0\n"
      "ds_write_b16 v12, v202\n"
      "v_mfma_f32_16x16x32_bf16 v[202:205], a[96:99], v[156:159], 0\n"
      "buffer_load_dwordx4 v18, s[16:19], s45 offen  lds\n"
      "v_mfma_f32_16x16x32_bf16 v[184:187], a[120:123], v[156:159], 0\n"
      "ds_read_b128 v[96:99], v1 offset:24576\n"
      "s_waitcnt lgkmcnt(2)\n"
      "v_add_f32 v210, v210, v211\n"
      "v_mfma_f32_16x16x32_bf16 v[148:151], a[4:7], v[136:139], v[148:151]\n"
      "ds_read_b128 v[120:123], v0 offset:30720\n"
      "v_add_f32 v212, v212, v213\n"
      "v_mfma_f32_16x16x32_bf16 v[148:151], a[8:11], v[170:173], v[148:151]\n"
      "ds_read_b128 v[104:107], v1 offset:30720\n"
      "v_add_f32 v210, v210, v212\n"
      "v_mfma_f32_16x16x32_bf16 v[198:201], a[28:31], v[136:139], v[198:201]\n"
      "s_add_u32 m0, s64, 12288\n"
      "v_rsq_f32_e64 v210, v210\n"
      "v_mfma_f32_16x16x32_bf16 v[198:201], a[32:35], v[170:173], v[198:201]\n"
      "buffer_load_dwordx4 v19, s[16:19], s45 offen  lds\n"
      "v_mfma_f32_16x16x32_bf16 v[100:103], a[52:55], v[136:139], v[100:103]\n"
      "s_add_u32 m0, s65, 16384\n"
      "v_mul_f32 v210, 0x415db3d7, v210\n"
      "v_mov_b32_e64 v212, v210\n"
      "v_mfma_f32_16x16x32_bf16 v[100:103], a[56:59], v[170:173], v[100:103]\n"
      "s_waitcnt lgkmcnt(2)\n"
      "v_permlane32_swap_b32 v210, v212\n"
      "v_mfma_f32_16x16x32_bf16 v[176:179], a[76:79], v[136:139], v[176:179]\n"
      "s_waitcnt lgkmcnt(1)\n"
      "v_mov_b32 v211, v210\n"
      "v_mul_f32 v144, v212, v144\n"
      "v_mfma_f32_16x16x32_bf16 v[176:179], a[80:83], v[170:173], v[176:179]\n"
      "buffer_load_dwordx4 v15, s[16:19], s45 offen  lds\n"
      "v_mfma_f32_16x16x32_bf16 v[202:205], a[100:103], v[136:139], v[202:205]\n"
      "s_waitcnt lgkmcnt(0)\n"
      "v_permlane16_swap_b32 v210, v211\n"
      "v_mfma_f32_16x16x32_bf16 v[202:205], a[104:107], v[170:173], v[202:205]\n"
      "v_mul_f32 v114, v210, v114\n"
      "v_mul_f32_e64 v115, v210, v115\n"
      "v_mfma_f32_16x16x32_bf16 v[184:187], a[124:127], v[136:139], v[184:187]\n"
      "v_cvt_pk_bf16_f32 v114, v114, v115\n"
      "v_mul_f32_e64 v116, v210, v116\n"
      "v_mfma_f32_16x16x32_bf16 v[184:187], a[128:131], v[170:173], v[184:187]\n"
      "buffer_load_dwordx4 v[190:193], v36, s[28:31], s47 offen\n"
      "v_mfma_f32_16x16x32_bf16 v[148:151], a[12:15], v[96:99], v[148:151]\n"
      "v_mul_f32 v117, v210, v117\n"
      "v_mul_f32 v220, v210, v220\n"
      "v_mfma_f32_16x16x32_bf16 v[148:151], a[16:19], v[120:123], v[148:151]\n"
      "v_mul_f32_e64 v221, v210, v221\n"
      "v_cvt_pk_bf16_f32 v115, v116, v117\n"
      "v_mfma_f32_16x16x32_bf16 v[148:151], a[20:23], v[104:107], v[148:151]\n"
      "v_mul_f32 v206, v210, v206\n"
      "v_mul_f32 v207, v210, v207\n"
      "v_mfma_f32_16x16x32_bf16 v[198:201], a[36:39], v[96:99], v[198:201]\n"
      "buffer_load_dword v8, v29, s[28:31], s47 offen\n"
      "v_mfma_f32_16x16x32_bf16 v[198:201], a[40:43], v[120:123], v[198:201]\n"
      "v_mul_f32 v208, v210, v208\n"
      "v_mul_f32 v209, v210, v209\n"
      "v_mfma_f32_16x16x32_bf16 v[198:201], a[44:47], v[104:107], v[198:201]\n"
      "v_cvt_pk_bf16_f32 v220, v220, v221\n"
      "v_mul_f32 v222, v210, v222\n"
      "v_mfma_f32_16x16x32_bf16 v[100:103], a[60:63], v[96:99], v[100:103]\n"
      "v_mul_f32 v223, v210, v223\n"
      "v_cvt_pk_bf16_f32 v116, v206, v207\n"
      "v_mfma_f32_16x16x32_bf16 v[100:103], a[64:67], v[120:123], v[100:103]\n"
      "v_cvt_pk_bf16_f32 v117, v208, v209\n"
      "v_cvt_pk_bf16_f32 v221, v222, v223\n"
      "v_mfma_f32_16x16x32_bf16 v[100:103], a[68:71], v[104:107], v[100:103]\n"
      "ds_write_b128 v5, v[114:117] offset:18432\n"
      "v_mfma_f32_16x16x32_bf16 v[176:179], a[84:87], v[96:99], v[176:179]\n"
      "v_mul_f32 v180, v211, v180\n"
      "v_mul_f32 v181, v211, v181\n"
      "v_mfma_f32_16x16x32_bf16 v[176:179], a[88:91], v[120:123], v[176:179]\n"
      "v_cvt_pk_bf16_f32 v180, v180, v181\n"
      "v_mul_f32_e64 v182, v211, v182\n"
      "v_mfma_f32_16x16x32_bf16 v[176:179], a[92:95], v[104:107], v[176:179]\n"
      "ds_write_b64 v6, v[220:221] offset:18432\n"
      "v_mfma_f32_16x16x32_bf16 v[202:205], a[108:111], v[96:99], v[202:205]\n"
      "v_mul_f32 v183, v211, v183\n"
      "v_mul_f32 v166, v211, v166\n"
      "v_mfma_f32_16x16x32_bf16 v[202:205], a[112:115], v[120:123], v[202:205]\n"
      "v_mul_f32_e64 v167, v211, v167\n"
      "v_cvt_pk_bf16_f32 v181, v182, v183\n"
      "v_mfma_f32_16x16x32_bf16 v[202:205], a[116:119], v[104:107], v[202:205]\n"
      "v_mul_f32 v124, v211, v124\n"
      "v_mul_f32 v125, v211, v125\n"
      "v_mfma_f32_16x16x32_bf16 v[184:187], a[132:135], v[96:99], v[184:187]\n"
      "v_mul_f32 v126, v211, v126\n"
      "v_mul_f32 v127, v211, v127\n"
      "v_mfma_f32_16x16x32_bf16 v[184:187], a[136:139], v[120:123], v[184:187]\n"
      "v_cvt_pk_bf16_f32 v166, v166, v167\n"
      "v_mul_f32_e64 v168, v211, v168\n"
      "v_mfma_f32_16x16x32_bf16 v[184:187], a[140:143], v[104:107], v[184:187]\n"
      "v_mul_f32_e64 v169, v211, v169\n"
      "v_cvt_pk_bf16_f32 v182, v124, v125\n"
      "v_mfma_f32_16x16x32_bf16 v[194:197], v[156:159], a[144:147], 0\n"
      "v_cvt_pk_bf16_f32 v183, v126, v127\n"
      "v_cvt_pk_bf16_f32 v167, v168, v169\n"
      "v_mfma_f32_16x16x32_bf16 v[194:197], v[136:139], a[148:151], v[194:197]\n"
      "ds_write_b128 v5, v[180:183] offset:24576\n"
      "v_mfma_f32_16x16x32_bf16 v[194:197], v[170:173], a[152:155], v[194:197]\n"
      "v_mul_f32 v145, v212, v145\n"
      "v_cvt_pk_bf16_f32 v144, v144, v145\n"
      "v_mfma_f32_16x16x32_bf16 v[194:197], v[96:99], a[156:159], v[194:197]\n"
      "v_mul_f32 v146, v212, v146\n"
      "v_mul_f32_e64 v147, v212, v147\n"
      "v_mfma_f32_16x16x32_bf16 v[194:197], v[120:123], a[160:163], v[194:197]\n"
      "ds_write_b64 v6, v[166:167] offset:24576\n"
      "v_mfma_f32_16x16x32_bf16 v[194:197], v[104:107], a[164:167], v[194:197]\n"
      "ds_read_b128 v[152:155], v0 offset:20480\n"
      "v_mul_f32 v140, v212, v140\n"
      "v_mfma_f32_16x16x32_bf16 v[132:135], v[156:159], a[168:171], 0\n"
      "ds_read_b128 v[218:221], v1 offset:20480\n"
      "v_mul_f32 v141, v212, v141\n"
      "v_mfma_f32_16x16x32_bf16 v[132:135], v[136:139], a[172:175], v[132:135]\n"
      "ds_read_b128 v[180:183], v0 offset:26624\n"
      "v_cvt_pk_bf16_f32 v145, v146, v147\n"
      "v_mfma_f32_16x16x32_bf16 v[132:135], v[170:173], a[176:179], v[132:135]\n"
      "ds_read_b128 v[160:163], v1 offset:26624\n"
      "v_mul_f32 v108, v212, v108\n"
      "v_mfma_f32_16x16x32_bf16 v[132:135], v[96:99], a[180:183], v[132:135]\n"
      "ds_read_b128 v[128:131], v0 offset:32768\n"
      "v_mul_f32 v109, v212, v109\n"
      "v_mfma_f32_16x16x32_bf16 v[132:135], v[120:123], a[184:187], v[132:135]\n"
      "ds_read_b128 v[164:167], v1 offset:32768\n"
      "v_mul_f32 v110, v212, v110\n"
      "v_mfma_f32_16x16x32_bf16 v[132:135], v[104:107], a[188:191], v[132:135]\n"
      "v_mul_f32 v111, v212, v111\n"
      "v_cvt_pk_bf16_f32 v140, v140, v141\n"
      "v_mfma_f32_16x16x32_bf16 v[214:217], v[156:159], a[192:195], 0\n"
      "s_waitcnt lgkmcnt(5)\n"
      "v_mul_f32 v142, v212, v142\n"
      "v_mul_f32_e64 v143, v212, v143\n"
      "v_mfma_f32_16x16x32_bf16 v[214:217], v[136:139], a[196:199], v[214:217]\n"
      "s_waitcnt lgkmcnt(4)\n"
      "v_cvt_pk_bf16_f32 v146, v108, v109\n"
      "v_cvt_pk_bf16_f32 v147, v110, v111\n"
      "v_mfma_f32_16x16x32_bf16 v[214:217], v[170:173], a[200:203], v[214:217]\n"
      "ds_write_b128 v5, v[144:147] offset:30720\n"
      "v_mfma_f32_16x16x32_bf16 v[214:217], v[96:99], a[204:207], v[214:217]\n"
      "s_waitcnt lgkmcnt(2)\n"
      "v_cvt_pk_bf16_f32 v141, v142, v143\n"
      "v_fma_f32 v98, v148, v148, s51\n"
      "v_mfma_f32_16x16x32_bf16 v[214:217], v[120:123], a[208:211], v[214:217]\n"
      "s_waitcnt lgkmcnt(1)\n"
      "v_fma_f32 v123, v176, v176, s51\n"
      "v_fmac_f32 v98, v149, v149\n"
      "v_mfma_f32_16x16x32_bf16 v[214:217], v[104:107], a[212:215], v[214:217]\n"
      "ds_write_b64 v6, v[140:141] offset:30720\n"
      "v_mfma_f32_16x16x32_bf16 v[118:121], a[0:3], v[152:155], 0\n"
      "v_fmac_f32 v123, v177, v177\n"
      "v_fmac_f32 v98, v150, v150\n"
      "v_mfma_f32_16x16x32_bf16 v[118:121], a[4:7], v[218:221], v[118:121]\n"
      "v_fmac_f32 v123, v178, v178\n"
      "v_fmac_f32 v98, v151, v151\n"
      "v_mfma_f32_16x16x32_bf16 v[118:121], a[8:11], v[180:183], v[118:121]\n"
      "v_fmac_f32 v123, v179, v179\n"
      "v_fmac_f32 v98, v198, v198\n"
      "v_mfma_f32_16x16x32_bf16 v[118:121], a[12:15], v[160:163], v[118:121]\n"
      "v_fmac_f32 v123, v202, v202\n"
      "v_fmac_f32 v98, v199, v199\n"
      "v_mfma_f32_16x16x32_bf16 v[118:121], a[16:19], v[128:131], v[118:121]\n"
      "v_fmac_f32 v123, v203, v203\n"
      "v_fmac_f32 v98, v200, v200\n"
      "v_mfma_f32_16x16x32_bf16 v[118:121], a[20:23], v[164:167], v[118:121]\n"
      "v_fmac_f32 v123, v204, v204\n"
      "v_fmac_f32 v98, v201, v201\n"
      "v_mfma_f32_16x16x32_bf16 v[106:109], a[24:27], v[152:155], 0\n"
      "v_fmac_f32 v123, v205, v205\n"
      "v_fmac_f32 v98, v100, v100\n"
      "v_mfma_f32_16x16x32_bf16 v[106:109], a[28:31], v[218:221], v[106:109]\n"
      "v_fmac_f32 v123, v184, v184\n"
      "v_fmac_f32 v98, v101, v101\n"
      "v_mfma_f32_16x16x32_bf16 v[106:109], a[32:35], v[180:183], v[106:109]\n"
      "v_fmac_f32 v123, v185, v185\n"
      "v_fmac_f32 v98, v102, v102\n"
      "v_mfma_f32_16x16x32_bf16 v[106:109], a[36:39], v[160:163], v[106:109]\n"
      "v_fmac_f32 v123, v186, v186\n"
      "v_fmac_f32 v98, v103, v103\n"
      "v_mfma_f32_16x16x32_bf16 v[106:109], a[40:43], v[128:131], v[106:109]\n"
      "v_fmac_f32_e64 v123, v187, v187\n"
      "v_fma_f32 v126, v118, v118, s51\n"
      "v_mfma_f32_16x16x32_bf16 v[106:109], a[44:47], v[164:167], v[106:109]\n"
      "v_permlane16_swap_b32 v98, v123\n"
      "v_mfma_f32_16x16x32_bf16 v[136:139], a[48:51], v[152:155], 0\n"
      "v_add_f32 v98, v98, v123\n"
      "v_mov_b32 v123, v98\n"
      "v_mfma_f32_16x16x32_bf16 v[136:139], a[52:55], v[218:221], v[136:139]\n"
      "v_fmac_f32 v126, v119, v119\n"
      "v_permlane32_swap_b32 v98, v123\n"
      "v_mfma_f32_16x16x32_bf16 v[136:139], a[56:59], v[180:183], v[136:139]\n"
      "v_add_f32 v98, v98, v123\n"
      "v_rsq_f32 v98, v98\n"
      "v_mfma_f32_16x16x32_bf16 v[136:139], a[60:63], v[160:163], v[136:139]\n"
      "v_mov_b32 v123, v98\n"
      "v_fmac_f32 v126, v120, v120\n"
      "v_mfma_f32_16x16x32_bf16 v[136:139], a[64:67], v[128:131], v[136:139]\n"
      "v_permlane16_swap_b32 v98, v123\n"
      "v_mfma_f32_16x16x32_bf16 v[136:139], a[68:71], v[164:167], v[136:139]\n"
      "v_mul_f32 v98, s50, v98\n"
      "v_mul_f32 v176, v123, v176\n"
      "v_mfma_f32_16x16x32_bf16 v[140:143], a[72:75], v[152:155], 0\n"
      "v_mul_f32 v177, v123, v177\n"
      "v_mul_f32 v148, v98, v148\n"
      "v_mfma_f32_16x16x32_bf16 v[140:143], a[76:79], v[218:221], v[140:143]\n"
      "v_mul_f32 v149, v98, v149\n"
      "v_cvt_pk_bf16_f32 v176, v176, v177\n"
      "v_mfma_f32_16x16x32_bf16 v[140:143], a[80:83], v[180:183], v[140:143]\n"
      "v_mul_f32 v178, v123, v178\n"
      "v_mul_f32 v179, v123, v179\n"
      "v_mfma_f32_16x16x32_bf16 v[140:143], a[84:87], v[160:163], v[140:143]\n"
      "v_mul_f32 v184, v123, v184\n"
      "v_mul_f32_e64 v185, v123, v185\n"
      "v_mfma_f32_16x16x32_bf16 v[140:143], a[88:91], v[128:131], v[140:143]\n"
      "v_cvt_pk_bf16_f32 v148, v148, v149\n"
      "v_mul_f32 v150, v98, v150\n"
      "v_mfma_f32_16x16x32_bf16 v[140:143], a[92:95], v[164:167], v[140:143]\n"
      "v_mul_f32 v151, v98, v151\n"
      "v_mul_f32_e64 v100, v98, v100\n"
      "v_mfma_f32_16x16x32_bf16 v[172:175], a[96:99], v[152:155], 0\n"
      "v_mul_f32 v101, v98, v101\n"
      "v_cvt_pk_bf16_f32 v177, v178, v179\n"
      "v_mfma_f32_16x16x32_bf16 v[172:175], a[100:103], v[218:221], v[172:175]\n"
      "v_mul_f32 v202, v123, v202\n"
      "v_mul_f32 v203, v123, v203\n"
      "v_mfma_f32_16x16x32_bf16 v[172:175], a[104:107], v[180:183], v[172:175]\n"
      "v_mul_f32 v204, v123, v204\n"
      "v_mul_f32_e64 v205, v123, v205\n"
      "v_mfma_f32_16x16x32_bf16 v[172:175], a[108:111], v[160:163], v[172:175]\n"
      "v_cvt_pk_bf16_f32 v184, v184, v185\n"
      "v_mul_f32 v186, v123, v186\n"
      "v_mfma_f32_16x16x32_bf16 v[172:175], a[112:115], v[128:131], v[172:175]\n"
      "v_mul_f32 v187, v123, v187\n"
      "v_cvt_pk_bf16_f32 v149, v150, v151\n"
      "v_mfma_f32_16x16x32_bf16 v[172:175], a[116:119], v[164:167], v[172:175]\n"
      "v_mul_f32 v198, v98, v198\n"
      "v_mul_f32 v199, v98, v199\n"
      "v_mfma_f32_16x16x32_bf16 v[122:125], a[120:123], v[152:155], 0\n"
      "v_mul_f32 v200, v98, v200\n"
      "v_mul_f32 v201, v98, v201\n"
      "v_mfma_f32_16x16x32_bf16 v[122:125], a[124:127], v[218:221], v[122:125]\n"
      "v_cvt_pk_bf16_f32 v100, v100, v101\n"
      "v_mul_f32 v102, v98, v102\n"
      "v_mfma_f32_16x16x32_bf16 v[122:125], a[128:131], v[180:183], v[122:125]\n"
      "v_mul_f32 v103, v98, v103\n"
      "v_cvt_pk_bf16_f32 v178, v202, v203\n"
      "v_mfma_f32_16x16x32_bf16 v[122:125], a[132:135], v[160:163], v[122:125]\n"
      "ds_read_b128 v[114:117], v7\n"
      "v_cvt_pk_bf16_f32 v179, v204, v205\n"
      "v_mfma_f32_16x16x32_bf16 v[122:125], a[136:139], v[128:131], v[122:125]\n"
      "ds_read_b128 v[202:205], v7 offset:4096\n"
      "v_cvt_pk_bf16_f32 v185, v186, v187\n"
      "v_mfma_f32_16x16x32_bf16 v[122:125], a[140:143], v[164:167], v[122:125]\n"
      "v_cvt_pk_bf16_f32 v150, v198, v199\n"
      "v_cvt_pk_bf16_f32 v151, v200, v201\n"
      "v_mfma_f32_16x16x32_bf16 v[96:99], v[152:155], a[144:147], 0\n"
      "ds_read_b128 v[168:171], v7 offset:8192\n"
      "v_cvt_pk_bf16_f32 v101, v102, v103\n"
      "v_mfma_f32_16x16x32_bf16 v[96:99], v[218:221], a[148:151], v[96:99]\n"
      "ds_read_b128 v[156:159], v7 offset:12288\n"
      "v_fma_f32 v127, v140, v140, s51\n"
      "v_mfma_f32_16x16x32_bf16 v[96:99], v[180:183], a[152:155], v[96:99]\n"
      "v_fmac_f32 v127, v141, v141\n"
      "v_fmac_f32 v127, v142, v142\n"
      "v_mfma_f32_16x16x32_bf16 v[96:99], v[160:163], a[156:159], v[96:99]\n"
      "ds_read_b128 v[198:201], v7 offset:16384\n"
      "v_fmac_f32 v126, v121, v121\n"
      "v_mfma_f32_16x16x32_bf16 v[96:99], v[128:131], a[160:163], v[96:99]\n"
      "v_fmac_f32 v127, v143, v143\n"
      "v_fmac_f32_e64 v126, v106, v106\n"
      "v_mfma_f32_16x16x32_bf16 v[96:99], v[164:167], a[164:167], v[96:99]\n"
      "ds_read_b128 v[110:113], v0 offset:22528\n"
      "v_fmac_f32 v127, v172, v172\n"
      "v_mfma_f32_16x16x32_bf16 v[186:189], v[152:155], a[168:171], 0\n"
      "ds_read_b128 v[210:213], v1 offset:22528\n"
      "v_fmac_f32 v126, v107, v107\n"
      "v_mfma_f32_16x16x32_bf16 v[186:189], v[218:221], a[172:175], v[186:189]\n"
      "s_waitcnt lgkmcnt(3)\n"
      "v_fmac_f32 v127, v173, v173\n"
      "v_fmac_f32_e64 v126, v108, v108\n"
      "v_mfma_f32_16x16x32_bf16 v[186:189], v[180:183], a[176:179], v[186:189]\n"
      "buffer_store_dwordx4 v[114:117], v20, s[24:27], s46 offen sc1\n"
      "v_mfma_f32_16x16x32_bf16 v[186:189], v[160:163], a[180:183], v[186:189]\n"
      "s_nop 0\n"
      "ds_read_b128 v[114:117], v0 offset:28672\n"
      "v_fmac_f32 v127, v174, v174\n"
      "v_mfma_f32_16x16x32_bf16 v[186:189], v[128:131], a[184:187], v[186:189]\n"
      "ds_read_b128 v[102:105], v1 offset:28672\n"
      "v_fmac_f32_e64 v126, v109, v109\n"
      "v_mfma_f32_16x16x32_bf16 v[186:189], v[164:167], a[188:191], v[186:189]\n"
      "ds_read_b128 v[206:209], v0 offset:34816\n"
      "v_fmac_f32 v127, v175, v175\n"
      "v_mfma_f32_16x16x32_bf16 v[144:147], v[152:155], a[192:195], 0\n"
      "s_waitcnt lgkmcnt(3)\n"
      "v_fmac_f32 v126, v136, v136\n"
      "v_fmac_f32 v127, v122, v122\n"
      "v_mfma_f32_16x16x32_bf16 v[144:147], v[218:221], a[196:199], v[144:147]\n"
      "ds_read_b128 v[218:221], v1 offset:34816\n"
      "v_fmac_f32 v126, v137, v137\n"
      "v_mfma_f32_16x16x32_bf16 v[144:147], v[180:183], a[200:203], v[144:147]\n"
      "v_fmac_f32 v127, v123, v123\n"
      "v_fmac_f32 v126, v138, v138\n"
      "v_mfma_f32_16x16x32_bf16 v[144:147], v[160:163], a[204:207], v[144:147]\n"
      "s_waitcnt lgkmcnt(3)\n"
      "v_fmac_f32 v127, v124, v124\n"
      "v_fmac_f32 v126, v139, v139\n"
      "v_mfma_f32_16x16x32_bf16 v[144:147], v[128:131], a[208:211], v[144:147]\n"
      "buffer_store_dwordx4 v[202:205], v21, s[24:27], s46 offen sc1\n"
      "v_mfma_f32_16x16x32_bf16 v[144:147], v[164:167], a[212:215], v[144:147]\n"
      "s_waitcnt lgkmcnt(1)\n"
      "v_fmac_f32 v127, v125, v125\n"
      "v_cvt_pk_bf16_f32 v194, v194, v195\n"
      "v_mfma_f32_16x16x32_bf16 v[202:205], a[0:3], v[110:113], 0\n"
      "s_waitcnt lgkmcnt(0)\n"
      "v_permlane16_swap_b32 v126, v127\n"
      "v_mfma_f32_16x16x32_bf16 v[202:205], a[4:7], v[210:213], v[202:205]\n"
      "v_add_f32 v126, v126, v127\n"
      "v_mov_b32 v127, v126\n"
      "v_mfma_f32_16x16x32_bf16 v[202:205], a[8:11], v[114:117], v[202:205]\n"
      "v_cvt_pk_bf16_f32 v195, v196, v197\n"
      "v_permlane32_swap_b32 v126, v127\n"
      "v_mfma_f32_16x16x32_bf16 v[202:205], a[12:15], v[102:105], v[202:205]\n"
      "v_add_f32 v126, v126, v127\n"
      "v_rsq_f32 v126, v126\n"
      "v_mfma_f32_16x16x32_bf16 v[202:205], a[16:19], v[206:209], v[202:205]\n"
      "v_mov_b32 v127, v126\n"
      "v_cvt_pk_bf16_f32 v196, v96, v97\n"
      "v_mfma_f32_16x16x32_bf16 v[202:205], a[20:23], v[218:221], v[202:205]\n"
      "v_permlane16_swap_b32 v126, v127\n"
      "v_mfma_f32_16x16x32_bf16 v[160:163], a[24:27], v[110:113], 0\n"
      "buffer_store_dwordx4 v[168:171], v22, s[24:27], s46 offen sc1\n"
      "v_mfma_f32_16x16x32_bf16 v[160:163], a[28:31], v[210:213], v[160:163]\n"
      "v_mul_f32 v126, s50, v126\n"
      "v_mul_f32 v140, v127, v140\n"
      "v_mfma_f32_16x16x32_bf16 v[160:163], a[32:35], v[114:117], v[160:163]\n"
      "v_mul_f32 v141, v127, v141\n"
      "v_mul_f32 v118, v126, v118\n"
      "v_mfma_f32_16x16x32_bf16 v[160:163], a[36:39], v[102:105], v[160:163]\n"
      "v_mul_f32 v119, v126, v119\n"
      "v_cvt_pk_bf16_f32 v140, v140, v141\n"
      "v_mfma_f32_16x16x32_bf16 v[160:163], a[40:43], v[206:209], v[160:163]\n"
      "v_mul_f32 v142, v127, v142\n"
      "v_mul_f32 v143, v127, v143\n"
      "v_mfma_f32_16x16x32_bf16 v[160:163], a[44:47], v[218:221], v[160:163]\n"
      "v_mul_f32 v122, v127, v122\n"
      "v_mul_f32 v123, v127, v123\n"
      "v_mfma_f32_16x16x32_bf16 v[164:167], a[48:51], v[110:113], 0\n"
      "v_cvt_pk_bf16_f32 v118, v118, v119\n"
      "v_mul_f32_e64 v120, v126, v120\n"
      "v_mfma_f32_16x16x32_bf16 v[164:167], a[52:55], v[210:213], v[164:167]\n"
      "v_mul_f32 v121, v126, v121\n"
      "v_mul_f32 v136, v126, v136\n"
      "v_mfma_f32_16x16x32_bf16 v[164:167], a[56:59], v[114:117], v[164:167]\n"
      "buffer_store_dwordx4 v[156:159], v23, s[24:27], s46 offen sc1\n"
      "v_mfma_f32_16x16x32_bf16 v[164:167], a[60:63], v[102:105], v[164:167]\n"
      "v_mul_f32 v137, v126, v137\n"
      "v_cvt_pk_bf16_f32 v141, v142, v143\n"
      "v_mfma_f32_16x16x32_bf16 v[164:167], a[64:67], v[206:209], v[164:167]\n"
      "v_mul_f32 v172, v127, v172\n"
      "v_mul_f32 v173, v127, v173\n"
      "v_mfma_f32_16x16x32_bf16 v[164:167], a[68:71], v[218:221], v[164:167]\n"
      "v_mul_f32 v174, v127, v174\n"
      "v_mul_f32_e64 v175, v127, v175\n"
      "v_mfma_f32_16x16x32_bf16 v[152:155], a[72:75], v[110:113], 0\n"
      "v_cvt_pk_bf16_f32 v122, v122, v123\n"
      "v_mul_f32 v124, v127, v124\n"
      "v_mfma_f32_16x16x32_bf16 v[152:155], a[76:79], v[210:213], v[152:155]\n"
      "ds_read_b128 v[156:159], v9 offset:4608\n"
      "v_mul_f32 v125, v127, v125\n"
      "v_mfma_f32_16x16x32_bf16 v[152:155], a[80:83], v[114:117], v[152:155]\n"
      "v_cvt_pk_bf16_f32 v119, v120, v121\n"
      "v_mul_f32 v106, v126, v106\n"
      "v_mfma_f32_16x16x32_bf16 v[152:155], a[84:87], v[102:105], v[152:155]\n"
      "v_mul_f32 v107, v126, v107\n"
      "v_mul_f32 v108, v126, v108\n"
      "v_mfma_f32_16x16x32_bf16 v[152:155], a[88:91], v[206:209], v[152:155]\n"
      "buffer_store_dwordx4 v[198:201], v25, s[24:27], s46 offen sc1\n"
      "v_mfma_f32_16x16x32_bf16 v[152:155], a[92:95], v[218:221], v[152:155]\n"
      "v_mul_f32 v109, v126, v109\n"
      "v_cvt_pk_bf16_f32 v136, v136, v137\n"
      "v_mfma_f32_16x16x32_bf16 v[168:171], a[96:99], v[110:113], 0\n"
      "v_mul_f32 v138, v126, v138\n"
      "v_mul_f32 v139, v126, v139\n"
      "v_mfma_f32_16x16x32_bf16 v[168:171], a[100:103], v[210:213], v[168:171]\n"
      "v_cvt_pk_bf16_f32 v142, v172, v173\n"
      "v_cvt_pk_bf16_f32 v143, v174, v175\n"
      "v_mfma_f32_16x16x32_bf16 v[168:171], a[104:107], v[114:117], v[168:171]\n"
      "v_cvt_pk_bf16_f32 v123, v124, v125\n"
      "v_cvt_pk_bf16_f32 v120, v106, v107\n"
      "v_mfma_f32_16x16x32_bf16 v[168:171], a[108:111], v[102:105], v[168:171]\n"
      "v_cvt_pk_bf16_f32 v121, v108, v109\n"
      "v_cvt_pk_bf16_f32 v137, v138, v139\n"
      "v_mfma_f32_16x16x32_bf16 v[168:171], a[112:115], v[206:209], v[168:171]\n"
      "v_fma_f32 v138, v202, v202, s51\n"
      "v_fma_f32 v222, v152, v152, s51\n"
      "v_mfma_f32_16x16x32_bf16 v[168:171], a[116:119], v[218:221], v[168:171]\n"
      "v_fmac_f32 v138, v203, v203\n"
      "v_fmac_f32 v222, v153, v153\n"
      "v_mfma_f32_16x16x32_bf16 v[172:175], a[120:123], v[110:113], 0\n"
      "v_fmac_f32 v138, v204, v204\n"
      "v_fmac_f32 v222, v154, v154\n"
      "v_mfma_f32_16x16x32_bf16 v[172:175], a[124:127], v[210:213], v[172:175]\n"
      "v_fmac_f32 v138, v205, v205\n"
      "v_fmac_f32 v222, v155, v155\n"
      "v_mfma_f32_16x16x32_bf16 v[172:175], a[128:131], v[114:117], v[172:175]\n"
      "v_fmac_f32 v138, v160, v160\n"
      "v_fmac_f32 v222, v168, v168\n"
      "v_mfma_f32_16x16x32_bf16 v[172:175], a[132:135], v[102:105], v[172:175]\n"
      "v_fmac_f32 v138, v161, v161\n"
      "v_fmac_f32 v222, v169, v169\n"
      "v_mfma_f32_16x16x32_bf16 v[172:175], a[136:139], v[206:209], v[172:175]\n"
      "v_fmac_f32 v138, v162, v162\n"
      "v_fmac_f32 v222, v170, v170\n"
      "v_mfma_f32_16x16x32_bf16 v[172:175], a[140:143], v[218:221], v[172:175]\n"
      "v_fmac_f32 v138, v163, v163\n"
      "v_fmac_f32 v222, v171, v171\n"
      "v_mfma_f32_16x16x32_bf16 v[106:109], v[110:113], a[144:147], 0\n"
      "v_fmac_f32 v138, v164, v164\n"
      "v_fmac_f32 v138, v165, v165\n"
      "v_mfma_f32_16x16x32_bf16 v[106:109], v[210:213], a[148:151], v[106:109]\n"
      "v_fmac_f32 v138, v166, v166\n"
      "v_fmac_f32 v138, v167, v167\n"
      "v_mfma_f32_16x16x32_bf16 v[106:109], v[114:117], a[152:155], v[106:109]\n"
      "v_fmac_f32 v222, v172, v172\n"
      "v_fmac_f32 v222, v173, v173\n"
      "v_mfma_f32_16x16x32_bf16 v[106:109], v[102:105], a[156:159], v[106:109]\n"
      "v_fmac_f32 v222, v174, v174\n"
      "v_fmac_f32 v222, v175, v175\n"
      "v_mfma_f32_16x16x32_bf16 v[106:109], v[206:209], a[160:163], v[106:109]\n"
      "v_cvt_pk_bf16_f32 v197, v98, v99\n"
      "v_permlane16_swap_b32_e64 v138, v222\n"
      "v_mfma_f32_16x16x32_bf16 v[106:109], v[218:221], a[164:167], v[106:109]\n"
      "ds_read_b128 v[198:201], v2\n"
      "v_add_f32_e64 v138, v138, v222\n"
      "v_mfma_f32_16x16x32_bf16 v[124:127], v[110:113], a[168:171], 0\n"
      "ds_read_b128 v[180:183], v2 offset:3072\n"
      "v_mov_b32 v222, v138\n"
      "v_mfma_f32_16x16x32_bf16 v[124:127], v[210:213], a[172:175], v[124:127]\n"
      "ds_read_b128 v[96:99], v2 offset:6144\n"
      "v_permlane32_swap_b32 v138, v222\n"
      "v_mfma_f32_16x16x32_bf16 v[124:127], v[114:117], a[176:179], v[124:127]\n"
      "v_add_f32 v138, v138, v222\n"
      "v_rsq_f32 v138, v138\n"
      "v_mfma_f32_16x16x32_bf16 v[124:127], v[102:105], a[180:183], v[124:127]\n"
      "v_mov_b32_e64 v222, v138\n"
      "v_cvt_pk_bf16_f32 v76, v106, v107\n"
      "v_mfma_f32_16x16x32_bf16 v[124:127], v[206:209], a[184:187], v[124:127]\n"
      "v_permlane16_swap_b32 v138, v222\n"
      "v_mfma_f32_16x16x32_bf16 v[124:127], v[218:221], a[188:191], v[124:127]\n"
      "s_waitcnt lgkmcnt(2)\n"
      "v_mul_f32 v138, s50, v138\n"
      "v_mul_f32 v152, v222, v152\n"
      "v_mfma_f32_16x16x32_bf16 v[128:131], v[110:113], a[192:195], 0\n"
      "s_waitcnt lgkmcnt(1)\n"
      "v_mul_f32 v153, v222, v153\n"
      "v_mul_f32_e64 v202, v138, v202\n"
      "v_mfma_f32_16x16x32_bf16 v[128:131], v[210:213], a[196:199], v[128:131]\n"
      "s_waitcnt lgkmcnt(0)\n"
      "v_mul_f32 v203, v138, v203\n"
      "v_cvt_pk_bf16_f32 v152, v152, v153\n"
      "v_mfma_f32_16x16x32_bf16 v[128:131], v[114:117], a[200:203], v[128:131]\n"
      "ds_read_b128 v[210:213], v2 offset:9216\n"
      "v_mul_f32 v154, v222, v154\n"
      "v_mfma_f32_16x16x32_bf16 v[128:131], v[102:105], a[204:207], v[128:131]\n"
      "ds_read_b128 v[110:113], v2 offset:12288\n"
      "v_mul_f32 v155, v222, v155\n"
      "v_mfma_f32_16x16x32_bf16 v[128:131], v[206:209], a[208:211], v[128:131]\n"
      "ds_read_b128 v[102:105], v2 offset:15360\n"
      "v_mul_f32 v172, v222, v172\n"
      "v_mfma_f32_16x16x32_bf16 v[128:131], v[218:221], a[212:215], v[128:131]\n"
      "v_mul_f32 v173, v222, v173\n"
      "v_cvt_pk_bf16_f32 v202, v202, v203\n"
      "v_mfma_f32_16x16x32_bf16 v[114:117], a[216:219], v[198:201], 0\n"
      "v_mul_f32 v204, v138, v204\n"
      "v_mul_f32 v205, v138, v205\n"
      "v_mfma_f32_16x16x32_bf16 v[114:117], a[220:223], v[180:183], v[114:117]\n"
      "v_mul_f32 v164, v138, v164\n"
      "v_mul_f32 v165, v138, v165\n"
      "v_mfma_f32_16x16x32_bf16 v[114:117], a[224:227], v[96:99], v[114:117]\n"
      "s_waitcnt lgkmcnt(2)\n"
      "v_cvt_pk_bf16_f32 v153, v154, v155\n"
      "v_mul_f32 v168, v222, v168\n"
      "v_mfma_f32_16x16x32_bf16 v[206:209], a[240:243], v[198:201], 0\n"
      "s_waitcnt lgkmcnt(1)\n"
      "v_mul_f32 v169, v222, v169\n"
      "v_mul_f32 v170, v222, v170\n"
      "v_mfma_f32_16x16x32_bf16 v[206:209], a[244:247], v[180:183], v[206:209]\n"
      "s_waitcnt lgkmcnt(0)\n"
      "v_mul_f32 v171, v222, v171\n"
      "v_cvt_pk_bf16_f32 v172, v172, v173\n"
      "v_mfma_f32_16x16x32_bf16 v[206:209], a[248:251], v[96:99], v[206:209]\n"
      "v_mul_f32 v174, v222, v174\n"
      "v_mul_f32_e64 v175, v222, v175\n"
      "v_mfma_f32_16x16x32_bf16 v[220:223], v[232:235], v[198:201], 0\n"
      "v_cvt_pk_bf16_f32 v203, v204, v205\n"
      "v_mul_f32 v160, v138, v160\n"
      "v_mfma_f32_16x16x32_bf16 v[220:223], v[236:239], v[180:183], v[220:223]\n"
      "v_mul_f32 v161, v138, v161\n"
      "v_mul_f32 v162, v138, v162\n"
      "v_mfma_f32_16x16x32_bf16 v[220:223], v[240:243], v[96:99], v[220:223]\n"
      "ds_read_b128 v[198:201], v10 offset:1200\n"
      "v_mul_f32 v163, v138, v163\n"
      "v_mfma_f32_16x16x32_bf16 v[114:117], a[228:231], v[210:213], v[114:117]\n"
      "v_cvt_pk_bf16_f32 v164, v164, v165\n"
      "v_mul_f32 v166, v138, v166\n"
      "v_mfma_f32_16x16x32_bf16 v[114:117], a[232:235], v[110:113], v[114:117]\n"
      "v_mul_f32 v167, v138, v167\n"
      "v_cvt_pk_bf16_f32 v154, v168, v169\n"
      "v_mfma_f32_16x16x32_bf16 v[114:117], a[236:239], v[102:105], v[114:117]\n"
      "v_cvt_pk_bf16_f32 v155, v170, v171\n"
      "v_cvt_pk_bf16_f32 v173, v174, v175\n"
      "v_mfma_f32_16x16x32_bf16 v[206:209], a[252:255], v[210:213], v[206:209]\n"
      "ds_read_b64 v[218:219], v10 offset:1192\n"
      "v_cvt_pk_bf16_f32 v204, v160, v161\n"
      "v_mfma_f32_16x16x32_bf16 v[206:209], v[224:227], v[110:113], v[206:209]\n"
      "ds_read_b128 v[96:99], v2 offset:256\n"
      "v_cvt_pk_bf16_f32 v205, v162, v163\n"
      "v_mfma_f32_16x16x32_bf16 v[206:209], v[228:231], v[102:105], v[206:209]\n"
      "ds_read_b128 v[160:163], v2 offset:3328\n"
      "v_cvt_pk_bf16_f32 v165, v166, v167\n"
      "v_mfma_f32_16x16x32_bf16 v[220:223], v[244:247], v[210:213], v[220:223]\n"
      "ds_read_b128 v[210:213], v2 offset:6400\n"
      "v_cvt_pk_bf16_f32 v77, v108, v109\n"
      "v_mfma_f32_16x16x32_bf16 v[220:223], v[248:251], v[110:113], v[220:223]\n"
      "s_waitcnt lgkmcnt(4)\n"
      "v_cvt_pk_bf16_f32 v132, v132, v133\n"
      "v_cvt_pk_bf16_f32 v133, v134, v135\n"
      "v_mfma_f32_16x16x32_bf16 v[220:223], v[252:255], v[102:105], v[220:223]\n"
      "v_cvt_pk_bf16_f32 v134, v186, v187\n"
      "v_cvt_pk_bf16_f32 v135, v188, v189\n"
      "v_mfma_f32_16x16x32_bf16 v[108:111], v[176:179], v[148:151], v[40:43]\n"
      "s_waitcnt lgkmcnt(3)\n"
      "v_cvt_pk_bf16_f32 v80, v124, v125\n"
      "v_cvt_pk_bf16_f32 v81, v126, v127\n"
      "v_mfma_f32_16x16x32_bf16 v[166:169], v[140:143], v[148:151], v[44:47]\n"
      "s_waitcnt lgkmcnt(2)\n"
      "v_cvt_pk_bf16_f32 v214, v214, v215\n"
      "v_cvt_pk_bf16_f32 v215, v216, v217\n"
      "v_mfma_f32_16x16x32_bf16 v[186:189], v[152:155], v[148:151], v[48:51]\n"
      "s_waitcnt lgkmcnt(1)\n"
      "v_cvt_pk_bf16_f32 v216, v144, v145\n"
      "v_cvt_pk_bf16_f32 v217, v146, v147\n"
      "v_mfma_f32_16x16x32_bf16 v[144:147], v[198:201], v[148:151], v[24:27]\n"
      "s_waitcnt lgkmcnt(0)\n"
      "v_cvt_pk_bf16_f32 v84, v128, v129\n"
      "v_cvt_pk_bf16_f32 v85, v130, v131\n"
      "v_mfma_f32_16x16x16_bf16 v[108:111], v[184:185], v[100:101], v[108:111]\n"
      "v_fma_f32 v171, v114, v114, s52\n"
      "v_fmac_f32 v171, v115, v115\n"
      "v_fmac_f32 v171, v116, v116\n"
      "v_mfma_f32_16x16x16_bf16 v[166:169], v[122:123], v[100:101], v[166:169]\n"
      "v_fmac_f32 v171, v117, v117\n"
      "v_fmac_f32 v171, v206, v206\n"
      "v_fmac_f32 v171, v207, v207\n"
      "v_mfma_f32_16x16x16_bf16 v[186:189], v[172:173], v[100:101], v[186:189]\n"
      "v_exp_f32 v108, v108\n"
      "v_exp_f32 v109, v109\n"
      "v_mfma_f32_16x16x16_bf16 v[144:147], v[218:219], v[100:101], v[144:147]\n"
      "v_exp_f32 v110, v110\n"
      "v_exp_f32 v111, v111\n"
      "v_mfma_f32_16x16x32_bf16 v[180:183], a[216:219], v[96:99], 0\n"
      "v_exp_f32 v166, v166\n"
      "v_mfma_f32_16x16x32_bf16 v[180:183], a[220:223], v[160:163], v[180:183]\n"
      "v_exp_f32 v167, v167\n"
      "v_mfma_f32_16x16x32_bf16 v[180:183], a[224:227], v[210:213], v[180:183]\n"
      "v_exp_f32 v168, v168\n"
      "v_mfma_f32_16x16x32_bf16 v[124:127], a[240:243], v[96:99], 0\n"
      "v_exp_f32 v169, v169\n"
      "v_mfma_f32_16x16x32_bf16 v[124:127], a[244:247], v[160:163], v[124:127]\n"
      "v_cvt_pk_bf16_f32 v108, v108, v109\n"
      "v_cvt_pk_bf16_f32 v109, v110, v111\n"
      "v_mfma_f32_16x16x32_bf16 v[124:127], a[248:251], v[210:213], v[124:127]\n"
      "v_cvt_pk_bf16_f32 v110, v166, v167\n"
      "v_cvt_pk_bf16_f32 v111, v168, v169\n"
      "v_mfma_f32_16x16x32_bf16 v[166:169], v[232:235], v[96:99], 0\n"
      "v_exp_f32_e64 v186, v186\n"
      "v_mfma_f32_16x16x32_bf16 v[166:169], v[236:239], v[160:163], v[166:169]\n"
      "ds_read_u16 v78, v11 offset:1168\n"
      "v_exp_f32 v187, v187\n"
      "v_mfma_f32_16x16x32_bf16 v[166:169], v[240:243], v[210:213], v[166:169]\n"
      "v_exp_f32 v188, v188\n"
      "v_mfma_f32_16x16x32_bf16 v[210:213], v[176:179], v[118:121], v[52:55]\n"
      "ds_read_u16 v82, v11 offset:1200\n"
      "v_exp_f32 v189, v189\n"
      "v_mfma_f32_16x16x32_bf16 v[146:149], v[140:143], v[118:121], v[56:59]\n"
      "v_exp_f32 v144, v144\n"
      "v_mfma_f32_16x16x32_bf16 v[98:101], v[152:155], v[118:121], v[60:63]\n"
      "ds_read_u16 v86, v11 offset:1232\n"
      "v_cvt_pk_bf16_f32 v88, v186, v187\n"
      "v_mfma_f32_16x16x32_bf16 v[128:131], v[198:201], v[118:121], v[26:29]\n"
      "v_cvt_pk_bf16_f32 v89, v188, v189\n"
      "v_cvt_pk_bf16_f32 v90, v144, 0\n"
      "v_mfma_f32_16x16x16_bf16 v[210:213], v[184:185], v[136:137], v[210:213]\n"
      "v_fmac_f32 v171, v208, v208\n"
      "v_fmac_f32 v171, v209, v209\n"
      "v_fmac_f32_e64 v171, v220, v220\n"
      "v_mfma_f32_16x16x16_bf16 v[146:149], v[122:123], v[136:137], v[146:149]\n"
      "v_fmac_f32 v171, v221, v221\n"
      "v_fmac_f32 v171, v222, v222\n"
      "v_fmac_f32 v171, v223, v223\n"
      "v_mfma_f32_16x16x16_bf16 v[98:101], v[172:173], v[136:137], v[98:101]\n"
      "v_exp_f32 v210, v210\n"
      "v_exp_f32 v211, v211\n"
      "v_mfma_f32_16x16x16_bf16 v[128:131], v[218:219], v[136:137], v[128:131]\n"
      "v_exp_f32 v212, v212\n"
      "v_exp_f32 v213, v213\n"
      "v_mfma_f32_16x16x32_bf16 v[186:189], v[32:35], v[108:111], 0\n"
      "s_waitcnt lgkmcnt(0)\n"
      "v_exp_f32 v146, v146\n"
      "v_mfma_f32_16x16x32_bf16 v[186:189], v[32:35], v[88:91], v[186:189]\n"
      "v_exp_f32 v147, v147\n"
      "v_mfma_f32_16x16x32_bf16 v[102:105], v[194:197], v[108:111], 0\n"
      "v_exp_f32_e64 v148, v148\n"
      "v_mfma_f32_16x16x32_bf16 v[102:105], v[76:79], v[88:91], v[102:105]\n"
      "ds_read_b128 v[118:121], v2 offset:9472\n"
      "v_exp_f32_e64 v149, v149\n"
      "v_mfma_f32_16x16x32_bf16 v[160:163], v[132:135], v[108:111], 0\n"
      "ds_read_b128 v[136:139], v2 offset:12544\n"
      "v_cvt_pk_bf16_f32 v210, v210, v211\n"
      "v_mfma_f32_16x16x32_bf16 v[160:163], v[80:83], v[88:91], v[160:163]\n"
      "v_cvt_pk_bf16_f32 v211, v212, v213\n"
      "v_cvt_pk_bf16_f32 v212, v146, v147\n"
      "v_mfma_f32_16x16x32_bf16 v[144:147], v[214:217], v[108:111], 0\n"
      "v_cvt_pk_bf16_f32 v213, v148, v149\n"
      "v_exp_f32_e64 v98, v98\n"
      "v_mfma_f32_16x16x32_bf16 v[144:147], v[84:87], v[88:91], v[144:147]\n"
      "ds_read_b128 v[108:111], v2 offset:15616\n"
      "v_exp_f32 v99, v99\n"
      "v_mfma_f32_16x16x32_bf16 v[148:151], v[176:179], v[202:205], v[64:67]\n"
      "v_exp_f32 v100, v100\n"
      "v_exp_f32 v101, v101\n"
      "v_exp_f32 v128, v128\n"
      "v_cvt_pk_bf16_f32 v92, v98, v99\n"
      "v_cvt_pk_bf16_f32 v93, v100, v101\n"
      "v_cvt_pk_bf16_f32 v94, v128, 0\n"
      "v_rcp_f32 v186, v186\n"
      "s_waitcnt vmcnt(6)\n"
      "v_or3_b32 v190, v190, v191, v192\n"
      "v_mfma_f32_16x16x32_bf16 v[98:101], v[140:143], v[202:205], v[68:71]\n"
      "s_waitcnt lgkmcnt(1)\n"
      "v_mul_f32 v102, v186, v102\n"
      "v_mul_f32 v103, v186, v103\n"
      "v_cvt_pk_bf16_f32 v102, v102, v103\n"
      "v_mul_f32 v104, v186, v104\n"
      "v_mul_f32 v105, v186, v105\n"
      "v_cvt_pk_bf16_f32 v103, v104, v105\n"
      "v_mul_f32 v160, v186, v160\n"
      "v_mul_f32 v161, v186, v161\n"
      "v_mul_f32 v162, v186, v162\n"
      "v_mul_f32_e64 v163, v186, v163\n"
      "v_cvt_pk_bf16_f32 v104, v160, v161\n"
      "v_cvt_pk_bf16_f32 v105, v162, v163\n"
      "v_mfma_f32_16x16x32_bf16 v[160:163], v[152:155], v[202:205], v[72:75]\n"
      "ds_write_b128 v3, v[102:105] offset:18432\n"
      "v_mfma_f32_16x16x32_bf16 v[104:107], v[198:201], v[202:205], v[28:31]\n"
      "s_waitcnt lgkmcnt(1)\n"
      "v_mul_f32 v144, v186, v144\n"
      "v_mul_f32_e64 v145, v186, v145\n"
      "v_mfma_f32_16x16x16_bf16 v[148:151], v[184:185], v[164:165], v[148:151]\n"
      "v_cvt_pk_bf16_f32 v144, v144, v145\n"
      "v_mul_f32_e64 v146, v186, v146\n"
      "v_mul_f32 v147, v186, v147\n"
      "v_mfma_f32_16x16x16_bf16 v[98:101], v[122:123], v[164:165], v[98:101]\n"
      "v_cvt_pk_bf16_f32 v145, v146, v147\n"
      "s_waitcnt vmcnt(5)\n"
      "v_or3_b32 v190, v190, v193, v8\n"
      "v_and_b32 v190, 0x7fff7fff, v190\n"
      "v_mfma_f32_16x16x16_bf16 v[160:163], v[172:173], v[164:165], v[160:163]\n"
      "v_exp_f32 v148, v148\n"
      "v_exp_f32 v149, v149\n"
      "v_mfma_f32_16x16x16_bf16 v[104:107], v[218:219], v[164:165], v[104:107]\n"
      "v_exp_f32 v150, v150\n"
      "v_exp_f32 v151, v151\n"
      "v_mfma_f32_16x16x32_bf16 v[180:183], a[228:231], v[118:121], v[180:183]\n"
      "ds_write_b64 v4, v[144:145] offset:18432\n"
      "v_mfma_f32_16x16x32_bf16 v[180:183], a[232:235], v[136:139], v[180:183]\n"
      "v_exp_f32 v98, v98\n"
      "v_mfma_f32_16x16x32_bf16 v[180:183], a[236:239], v[108:111], v[180:183]\n"
      "v_exp_f32 v99, v99\n"
      "v_mfma_f32_16x16x32_bf16 v[124:127], a[252:255], v[118:121], v[124:127]\n"
      "ds_write_b32 v31, v190 offset:5632\n"
      "v_mfma_f32_16x16x32_bf16 v[124:127], v[224:227], v[136:139], v[124:127]\n"
      "ds_read_b128 v[144:147], v10 offset:1168\n"
      "v_exp_f32 v100, v100\n"
      "v_mfma_f32_16x16x32_bf16 v[124:127], v[228:231], v[108:111], v[124:127]\n"
      "v_exp_f32 v101, v101\n"
      "v_mfma_f32_16x16x32_bf16 v[166:169], v[244:247], v[118:121], v[166:169]\n"
      "v_cvt_pk_bf16_f32 v148, v148, v149\n"
      "v_cvt_pk_bf16_f32 v149, v150, v151\n"
      "v_mfma_f32_16x16x32_bf16 v[166:169], v[248:251], v[136:139], v[166:169]\n"
      "v_cvt_pk_bf16_f32 v150, v98, v99\n"
      "v_cvt_pk_bf16_f32 v151, v100, v101\n"
      "v_mfma_f32_16x16x32_bf16 v[166:169], v[252:255], v[108:111], v[166:169]\n"
      "ds_read_b64 v[106:107], v10 offset:1184\n"
      "v_exp_f32 v160, v160\n"
      "v_mfma_f32_16x16x32_bf16 v[188:191], v[32:35], v[210:213], 0\n"
      "v_exp_f32 v161, v161\n"
      "v_mfma_f32_16x16x32_bf16 v[188:191], v[32:35], v[92:95], v[188:191]\n"
      "v_exp_f32_e64 v162, v162\n"
      "v_mfma_f32_16x16x32_bf16 v[202:205], v[194:197], v[210:213], 0\n"
      "s_waitcnt lgkmcnt(1)\n"
      "v_exp_f32 v163, v163\n"
      "v_mfma_f32_16x16x32_bf16 v[202:205], v[76:79], v[92:95], v[202:205]\n"
      "ds_read_b128 v[98:101], v2 offset:512\n"
      "v_exp_f32_e64 v104, v104\n"
      "v_mfma_f32_16x16x32_bf16 v[118:121], v[132:135], v[210:213], 0\n"
      "ds_read_b128 v[190:193], v2 offset:3584\n"
      "v_cvt_pk_bf16_f32 v88, v160, v161\n"
      "v_mfma_f32_16x16x32_bf16 v[118:121], v[80:83], v[92:95], v[118:121]\n"
      "ds_read_b128 v[128:131], v2 offset:6656\n"
      "v_cvt_pk_bf16_f32 v89, v162, v163\n"
      "v_mfma_f32_16x16x32_bf16 v[160:163], v[214:217], v[210:213], 0\n"
      "s_waitcnt lgkmcnt(3)\n"
      "v_cvt_pk_bf16_f32 v90, v104, 0\n"
      "v_fma_f32 v102, v180, v180, s52\n"
      "v_mfma_f32_16x16x32_bf16 v[160:163], v[84:87], v[92:95], v[160:163]\n"
      "v_fmac_f32 v102, v181, v181\n"
      "v_fmac_f32 v102, v182, v182\n"
      "v_mfma_f32_16x16x32_bf16 v[210:213], v[144:147], v[176:179], v[30:33]\n"
      "v_fmac_f32 v102, v183, v183\n"
      "v_fmac_f32 v102, v124, v124\n"
      "v_mfma_f32_16x16x32_bf16 v[108:111], v[144:147], v[140:143], v[30:33]\n"
      "s_waitcnt lgkmcnt(2)\n"
      "v_fmac_f32 v102, v125, v125\n"
      "v_fmac_f32 v102, v126, v126\n"
      "v_mfma_f32_16x16x32_bf16 v[140:143], v[144:147], v[152:155], v[30:33]\n"
      "s_waitcnt lgkmcnt(1)\n"
      "v_fmac_f32 v102, v127, v127\n"
      "v_fmac_f32_e64 v102, v166, v166\n"
      "v_mfma_f32_16x16x32_bf16 v[174:177], v[144:147], v[198:201], v[30:33]\n"
      "s_waitcnt lgkmcnt(0)\n"
      "v_fmac_f32 v102, v167, v167\n"
      "v_fmac_f32 v102, v168, v168\n"
      "v_mfma_f32_16x16x16_bf16 v[210:213], v[106:107], v[184:185], v[210:213]\n"
      "v_fmac_f32 v102, v169, v169\n"
      "v_rcp_f32 v188, v188\n"
      "v_mfma_f32_16x16x16_bf16 v[108:111], v[106:107], v[122:123], v[108:111]\n"
      "v_permlane16_swap_b32 v171, v102\n"
      "v_add_f32 v171, v171, v102\n"
      "v_mfma_f32_16x16x16_bf16 v[140:143], v[106:107], v[172:173], v[140:143]\n"
      "v_mul_f32 v202, v188, v202\n"
      "v_mul_f32 v203, v188, v203\n"
      "v_cvt_pk_bf16_f32 v202, v202, v203\n"
      "v_mfma_f32_16x16x16_bf16 v[174:177], v[106:107], v[218:219], v[174:177]\n"
      "v_mul_f32 v204, v188, v204\n"
      "v_cndmask_b32 v210, v210, v108, s[58:59]\n"
      "v_mul_f32_e64 v205, v188, v205\n"
      "v_mfma_f32_16x16x32_bf16 v[144:147], a[216:219], v[98:101], 0\n"
      "v_cndmask_b32 v210, v210, v140, s[60:61]\n"
      "v_cvt_pk_bf16_f32 v203, v204, v205\n"
      "v_mfma_f32_16x16x32_bf16 v[144:147], a[220:223], v[190:193], v[144:147]\n"
      "v_mul_f32 v118, v188, v118\n"
      "v_mul_f32 v119, v188, v119\n"
      "v_mfma_f32_16x16x32_bf16 v[144:147], a[224:227], v[128:131], v[144:147]\n"
      "v_mul_f32 v120, v188, v120\n"
      "v_mul_f32 v121, v188, v121\n"
      "v_mfma_f32_16x16x32_bf16 v[108:111], a[240:243], v[98:101], 0\n"
      "v_cndmask_b32 v210, v210, v174, s[62:63]\n"
      "v_cvt_pk_bf16_f32 v204, v118, v119\n"
      "v_mfma_f32_16x16x32_bf16 v[108:111], a[244:247], v[190:193], v[108:111]\n"
      "ds_read_b128 v[198:201], v2 offset:9728\n"
      "v_cvt_pk_bf16_f32 v205, v120, v121\n"
      "v_mfma_f32_16x16x32_bf16 v[108:111], a[248:251], v[128:131], v[108:111]\n"
      "ds_read_b128 v[104:107], v2 offset:12800\n"
      "v_mul_f32 v160, v188, v160\n"
      "v_mfma_f32_16x16x32_bf16 v[140:143], v[232:235], v[98:101], 0\n"
      "ds_read_b128 v[98:101], v2 offset:15872\n"
      "v_mul_f32 v161, v188, v161\n"
      "v_mfma_f32_16x16x32_bf16 v[140:143], v[236:239], v[190:193], v[140:143]\n"
      "ds_write_b128 v3, v[202:205] offset:18688\n"
      "v_mfma_f32_16x16x32_bf16 v[140:143], v[240:243], v[128:131], v[140:143]\n"
      "v_exp_f32 v210, v210\n"
      "v_mfma_f32_16x16x32_bf16 v[202:205], v[32:35], v[148:151], 0\n"
      "s_waitcnt lgkmcnt(3)\n"
      "v_cvt_pk_bf16_f32 v160, v160, v161\n"
      "v_mul_f32 v162, v188, v162\n"
      "v_mfma_f32_16x16x32_bf16 v[202:205], v[32:35], v[88:91], v[202:205]\n"
      "s_waitcnt lgkmcnt(2)\n"
      "v_mul_f32_e64 v163, v188, v163\n"
      "v_cvt_pk_bf16_f32 v161, v162, v163\n"
      "v_mfma_f32_16x16x32_bf16 v[188:191], v[194:197], v[148:151], 0\n"
      "ds_write_b64 v4, v[160:161] offset:18688\n"
      "v_mfma_f32_16x16x32_bf16 v[188:191], v[76:79], v[88:91], v[188:191]\n"
      "s_waitcnt lgkmcnt(2)\n"
      "v_cvt_pk_bf16_f32 v210, v210, v210\n"
      "v_rcp_f32 v202, v202\n"
      "v_mfma_f32_16x16x32_bf16 v[118:121], v[132:135], v[148:151], 0\n"
      "ds_write_b16 v13, v210\n"
      "v_mfma_f32_16x16x32_bf16 v[118:121], v[80:83], v[88:91], v[118:121]\n"
      "ds_read_b128 v[210:213], v14\n"
      "v_readlane_b32 s45, v37, s43\n"
      "v_mfma_f32_16x16x32_bf16 v[152:155], v[214:217], v[148:151], 0\n"
      "ds_read_b128 v[184:187], v14 offset:64\n"
      "v_mul_f32 v188, v202, v188\n"
      "v_mfma_f32_16x16x32_bf16 v[152:155], v[84:87], v[88:91], v[152:155]\n"
      "v_mul_f32 v189, v202, v189\n"
      "v_cvt_pk_bf16_f32 v188, v188, v189\n"
      "v_mfma_f32_16x16x32_bf16 v[144:147], a[228:231], v[198:201], v[144:147]\n"
      "v_mul_f32 v190, v202, v190\n"
      "v_mul_f32 v191, v202, v191\n"
      "v_mfma_f32_16x16x32_bf16 v[144:147], a[232:235], v[104:107], v[144:147]\n"
      "v_cvt_pk_bf16_f32 v189, v190, v191\n"
      "v_mul_f32 v118, v202, v118\n"
      "v_mfma_f32_16x16x32_bf16 v[144:147], a[236:239], v[98:101], v[144:147]\n"
      "v_mul_f32 v119, v202, v119\n"
      "v_mul_f32 v120, v202, v120\n"
      "v_mfma_f32_16x16x32_bf16 v[108:111], a[252:255], v[198:201], v[108:111]\n"
      "v_mul_f32 v121, v202, v121\n"
      "v_cvt_pk_bf16_f32 v190, v118, v119\n"
      "v_mfma_f32_16x16x32_bf16 v[108:111], v[224:227], v[104:107], v[108:111]\n"
      "s_waitcnt lgkmcnt(1)\n"
      "v_cvt_pk_bf16_f32 v191, v120, v121\n"
      "v_fma_f32 v204, v144, v144, s52\n"
      "v_mfma_f32_16x16x32_bf16 v[108:111], v[228:231], v[98:101], v[108:111]\n"
      "ds_write_b128 v3, v[188:191] offset:18944\n"
      "v_mfma_f32_16x16x32_bf16 v[140:143], v[244:247], v[198:201], v[140:143]\n"
      "s_waitcnt lgkmcnt(1)\n"
      "v_fmac_f32 v204, v145, v145\n"
      "v_fmac_f32 v204, v146, v146\n"
      "v_mfma_f32_16x16x32_bf16 v[140:143], v[248:251], v[104:107], v[140:143]\n"
      "v_fmac_f32 v204, v147, v147\n"
      "v_mul_f32 v152, v202, v152\n"
      "v_mfma_f32_16x16x32_bf16 v[140:143], v[252:255], v[98:101], v[140:143]\n"
      "v_fmac_f32 v204, v108, v108\n"
      "v_fmac_f32 v204, v109, v109\n"
      "v_mfma_f32_16x16x32_bf16 v[188:191], v[210:213], v[32:35], 0\n"
      "v_fmac_f32 v204, v110, v110\n"
      "v_fmac_f32 v204, v111, v111\n"
      "v_mfma_f32_16x16x32_bf16 v[188:191], v[184:187], v[32:35], v[188:191]\n"
      "v_mul_f32 v153, v202, v153\n"
      "v_cvt_pk_bf16_f32 v152, v152, v153\n"
      "v_fmac_f32 v204, v140, v140\n"
      "v_fmac_f32 v204, v141, v141\n"
      "v_fmac_f32 v204, v142, v142\n"
      "v_fmac_f32 v204, v143, v143\n"
      "v_mov_b32 v102, v204\n"
      "v_mul_f32 v154, v202, v154\n"
      "v_mul_f32 v155, v202, v155\n"
      "v_permlane16_swap_b32 v204, v102\n"
      "v_add_f32 v204, v204, v102\n"
      "v_cvt_pk_bf16_f32 v153, v154, v155\n"
      "ds_write_b64 v4, v[152:153] offset:18944\n"
      "v_permlane32_swap_b32 v171, v204\n"
      "v_add_f32 v171, v171, v204\n"
      "v_readlane_b32 s46, v37, s42\n"
      "v_or3_b32 v156, v156, v157, v158\n"
      "ds_write_b32 v31, v171 offset:1024\n"
      "v_or_b32_e64 v156, v156, v159\n"
      "v_cmp_ne_u32 s[56:57], 0, v156\n"
      "v_readlane_b32 s47, v38, s43\n"
      "v_add_u32 v10, 2336, v10\n"
      "v_add_u32 v11, 2336, v11\n"
      "s_or_b32 s95, s45, s46\n"
      "s_or_b32 s95, s95, s48\n"
      "s_cmp_ge_u32 s95, 0x40000000\n"
      "s_cselect_b32 s94, -1, -2\n"
      "s_cmp_gt_u32 s42, s44\n"
      "s_cselect_b32 s95, s94, s95\n"
      "s_cmp_ge_u32 s95, 0x40000000\n"
      "s_cselect_b64 vcc, -1, s[56:57]\n"
      "s_and_b32 s47, s47, -4\n"
      "s_add_u32 s42, s42, 1\n"
      "s_add_u32 s43, s43, 1\n"
      "s_waitcnt lgkmcnt(0)\n"
      "s_barrier\n"
      "s_cbranch_vccnz L_disp0_%=\n"
      "L_loop0_%=:\n"
      "v_mfma_f32_16x16x32_bf16 v[202:205], v[210:213], v[194:197], 0\n"
      "ds_read_b128 v[128:131], v0\n"
      "ds_read_b128 v[176:179], v1\n"
      "v_mfma_f32_16x16x32_bf16 v[202:205], v[184:187], v[76:79], v[202:205]\n"
      "ds_read_b128 v[198:201], v0 offset:6144\n"
      "s_add_u32 m0, s64, 18432\n"
      "v_mfma_f32_16x16x32_bf16 v[102:105], v[210:213], v[132:135], 0\n"
      "buffer_load_dwordx4 v16, s[16:19], s45 offen  lds\n"
      "v_mfma_f32_16x16x32_bf16 v[102:105], v[184:187], v[80:83], v[102:105]\n"
      "s_add_u32 m0, s64, 22528\n"
      "v_mfma_f32_16x16x32_bf16 v[192:195], v[210:213], v[214:217], 0\n"
      "ds_read_b128 v[216:219], v9 offset:1024\n"
      "v_mfma_f32_16x16x32_bf16 v[192:195], v[184:187], v[84:87], v[192:195]\n"
      "v_mov_b32_dpp v188, v188 quad_perm:[0,0,0,0] row_mask:0xf bank_mask:0xf\n"
      "v_rcp_f32 v188, v188\n"
      "s_waitcnt lgkmcnt(3)\n"
      "v_mfma_f32_16x16x32_bf16 v[170:173], a[0:3], v[128:131], 0\n"
      "buffer_load_dwordx4 v17, s[16:19], s45 offen  lds\n"
      "v_mfma_f32_16x16x32_bf16 v[148:151], a[24:27], v[128:131], 0\n"
      "s_add_u32 m0, s64, 26624\n"
      "v_cndmask_b32 v202, v202, v102, s[58:59]\n"
      "v_cndmask_b32 v202, v202, v192, s[60:61]\n"
      "v_mfma_f32_16x16x32_bf16 v[96:99], a[48:51], v[128:131], 0\n"
      "s_waitcnt lgkmcnt(1)\n"
      "v_mul_f32 v202, v188, v202\n"
      "v_cvt_pk_bf16_f32 v202, v202, v202\n"
      "v_mfma_f32_16x16x32_bf16 v[136:139], a[72:75], v[128:131], 0\n"
      "ds_write_b16 v12, v202 offset:16\n"
      "v_mfma_f32_16x16x32_bf16 v[202:205], a[96:99], v[128:131], 0\n"
      "buffer_load_dwordx4 v18, s[16:19], s45 offen  lds\n"
      "v_mfma_f32_16x16x32_bf16 v[102:105], a[120:123], v[128:131], 0\n"
      "ds_read_b128 v[210:213], v1 offset:6144\n"
      "s_waitcnt lgkmcnt(2)\n"
      "v_add_f32 v216, v216, v217\n"
      "v_mfma_f32_16x16x32_bf16 v[170:173], a[4:7], v[176:179], v[170:173]\n"
      "ds_read_b128 v[120:123], v0 offset:12288\n"
      "v_add_f32 v218, v218, v219\n"
      "v_mfma_f32_16x16x32_bf16 v[170:173], a[8:11], v[198:201], v[170:173]\n"
      "ds_read_b128 v[152:155], v1 offset:12288\n"
      "v_add_f32 v216, v216, v218\n"
      "v_mfma_f32_16x16x32_bf16 v[148:151], a[28:31], v[176:179], v[148:151]\n"
      "s_add_u32 m0, s64, 30720\n"
      "v_rsq_f32_e64 v216, v216\n"
      "v_mfma_f32_16x16x32_bf16 v[148:151], a[32:35], v[198:201], v[148:151]\n"
      "buffer_load_dwordx4 v19, s[16:19], s45 offen  lds\n"
      "v_mfma_f32_16x16x32_bf16 v[96:99], a[52:55], v[176:179], v[96:99]\n"
      "s_add_u32 m0, s65, 34816\n"
      "v_mul_f32 v216, 0x415db3d7, v216\n"
      "v_mov_b32 v218, v216\n"
      "v_mfma_f32_16x16x32_bf16 v[96:99], a[56:59], v[198:201], v[96:99]\n"
      "s_waitcnt lgkmcnt(2)\n"
      "v_permlane32_swap_b32 v216, v218\n"
      "v_mfma_f32_16x16x32_bf16 v[136:139], a[76:79], v[176:179], v[136:139]\n"
      "s_waitcnt lgkmcnt(1)\n"
      "v_mov_b32 v217, v216\n"
      "v_mul_f32 v144, v218, v144\n"
      "v_mfma_f32_16x16x32_bf16 v[136:139], a[80:83], v[198:201], v[136:139]\n"
      "buffer_load_dwordx4 v15, s[16:19], s45 offen  lds\n"
      "v_mfma_f32_16x16x32_bf16 v[202:205], a[100:103], v[176:179], v[202:205]\n"
      "s_waitcnt lgkmcnt(0)\n"
      "v_permlane16_swap_b32 v216, v217\n"
      "v_mfma_f32_16x16x32_bf16 v[202:205], a[104:107], v[198:201], v[202:205]\n"
      "v_mul_f32 v114, v216, v114\n"
      "v_mul_f32 v115, v216, v115\n"
      "v_mfma_f32_16x16x32_bf16 v[102:105], a[124:127], v[176:179], v[102:105]\n"
      "v_cvt_pk_bf16_f32 v114, v114, v115\n"
      "v_mul_f32_e64 v116, v216, v116\n"
      "v_mfma_f32_16x16x32_bf16 v[102:105], a[128:131], v[198:201], v[102:105]\n"
      "buffer_load_dwordx4 v[162:165], v36, s[28:31], s47 offen\n"
      "v_mfma_f32_16x16x32_bf16 v[170:173], a[12:15], v[210:213], v[170:173]\n"
      "v_mul_f32 v117, v216, v117\n"
      "v_mul_f32 v220, v216, v220\n"
      "v_mfma_f32_16x16x32_bf16 v[170:173], a[16:19], v[120:123], v[170:173]\n"
      "v_mul_f32_e64 v221, v216, v221\n"
      "v_cvt_pk_bf16_f32 v115, v116, v117\n"
      "v_mfma_f32_16x16x32_bf16 v[170:173], a[20:23], v[152:155], v[170:173]\n"
      "v_mul_f32 v206, v216, v206\n"
      "v_mul_f32 v207, v216, v207\n"
      "v_mfma_f32_16x16x32_bf16 v[148:151], a[36:39], v[210:213], v[148:151]\n"
      "buffer_load_dword v8, v29, s[28:31], s47 offen\n"
      "v_mfma_f32_16x16x32_bf16 v[148:151], a[40:43], v[120:123], v[148:151]\n"
      "v_mul_f32 v208, v216, v208\n"
      "v_mul_f32 v209, v216, v209\n"
      "v_mfma_f32_16x16x32_bf16 v[148:151], a[44:47], v[152:155], v[148:151]\n"
      "v_cvt_pk_bf16_f32 v220, v220, v221\n"
      "v_mul_f32 v222, v216, v222\n"
      "v_mfma_f32_16x16x32_bf16 v[96:99], a[60:63], v[210:213], v[96:99]\n"
      "v_mul_f32 v223, v216, v223\n"
      "v_cvt_pk_bf16_f32 v116, v206, v207\n"
      "v_mfma_f32_16x16x32_bf16 v[96:99], a[64:67], v[120:123], v[96:99]\n"
      "v_cvt_pk_bf16_f32 v117, v208, v209\n"
      "v_cvt_pk_bf16_f32 v221, v222, v223\n"
      "v_mfma_f32_16x16x32_bf16 v[96:99], a[68:71], v[152:155], v[96:99]\n"
      "ds_write_b128 v5, v[114:117]\n"
      "v_mfma_f32_16x16x32_bf16 v[136:139], a[84:87], v[210:213], v[136:139]\n"
      "v_mul_f32 v180, v217, v180\n"
      "v_mul_f32 v181, v217, v181\n"
      "v_mfma_f32_16x16x32_bf16 v[136:139], a[88:91], v[120:123], v[136:139]\n"
      "v_cvt_pk_bf16_f32 v180, v180, v181\n"
      "v_mul_f32_e64 v182, v217, v182\n"
      "v_mfma_f32_16x16x32_bf16 v[136:139], a[92:95], v[152:155], v[136:139]\n"
      "ds_write_b64 v6, v[220:221]\n"
      "v_mfma_f32_16x16x32_bf16 v[202:205], a[108:111], v[210:213], v[202:205]\n"
      "v_mul_f32 v183, v217, v183\n"
      "v_mul_f32 v166, v217, v166\n"
      "v_mfma_f32_16x16x32_bf16 v[202:205], a[112:115], v[120:123], v[202:205]\n"
      "v_mul_f32_e64 v167, v217, v167\n"
      "v_cvt_pk_bf16_f32 v181, v182, v183\n"
      "v_mfma_f32_16x16x32_bf16 v[202:205], a[116:119], v[152:155], v[202:205]\n"
      "v_mul_f32 v124, v217, v124\n"
      "v_mul_f32 v125, v217, v125\n"
      "v_mfma_f32_16x16x32_bf16 v[102:105], a[132:135], v[210:213], v[102:105]\n"
      "v_mul_f32 v126, v217, v126\n"
      "v_mul_f32 v127, v217, v127\n"
      "v_mfma_f32_16x16x32_bf16 v[102:105], a[136:139], v[120:123], v[102:105]\n"
      "v_cvt_pk_bf16_f32 v166, v166, v167\n"
      "v_mul_f32_e64 v168, v217, v168\n"
      "v_mfma_f32_16x16x32_bf16 v[102:105], a[140:143], v[152:155], v[102:105]\n"
      "v_mul_f32_e64 v169, v217, v169\n"
      "v_cvt_pk_bf16_f32 v182, v124, v125\n"
      "v_mfma_f32_16x16x32_bf16 v[194:197], v[128:131], a[144:147], 0\n"
      "v_cvt_pk_bf16_f32 v183, v126, v127\n"
      "v_cvt_pk_bf16_f32 v167, v168, v169\n"
      "v_mfma_f32_16x16x32_bf16 v[194:197], v[176:179], a[148:151], v[194:197]\n"
      "ds_write_b128 v5, v[180:183] offset:6144\n"
      "v_mfma_f32_16x16x32_bf16 v[194:197], v[198:201], a[152:155], v[194:197]\n"
      "v_mul_f32 v145, v218, v145\n"
      "v_cvt_pk_bf16_f32 v144, v144, v145\n"
      "v_mfma_f32_16x16x32_bf16 v[194:197], v[210:213], a[156:159], v[194:197]\n"
      "v_mul_f32 v146, v218, v146\n"
      "v_mul_f32_e64 v147, v218, v147\n"
      "v_mfma_f32_16x16x32_bf16 v[194:197], v[120:123], a[160:163], v[194:197]\n"
      "ds_write_b64 v6, v[166:167] offset:6144\n"
      "v_mfma_f32_16x16x32_bf16 v[194:197], v[152:155], a[164:167], v[194:197]\n"
      "ds_read_b128 v[182:185], v0 offset:2048\n"
      "v_mul_f32 v140, v218, v140\n"
      "v_mfma_f32_16x16x32_bf16 v[132:135], v[128:131], a[168:171], 0\n"
      "ds_read_b128 v[220:223], v1 offset:2048\n"
      "v_mul_f32 v141, v218, v141\n"
      "v_mfma_f32_16x16x32_bf16 v[132:135], v[176:179], a[172:175], v[132:135]\n"
      "ds_read_b128 v[124:127], v0 offset:8192\n"
      "v_cvt_pk_bf16_f32 v145, v146, v147\n"
      "v_mfma_f32_16x16x32_bf16 v[132:135], v[198:201], a[176:179], v[132:135]\n"
      "ds_read_b128 v[158:161], v1 offset:8192\n"
      "v_mul_f32 v108, v218, v108\n"
      "v_mfma_f32_16x16x32_bf16 v[132:135], v[210:213], a[180:183], v[132:135]\n"
      "ds_read_b128 v[112:115], v0 offset:14336\n"
      "v_mul_f32 v109, v218, v109\n"
      "v_mfma_f32_16x16x32_bf16 v[132:135], v[120:123], a[184:187], v[132:135]\n"
      "ds_read_b128 v[190:193], v1 offset:14336\n"
      "v_mul_f32 v110, v218, v110\n"
      "v_mfma_f32_16x16x32_bf16 v[132:135], v[152:155], a[188:191], v[132:135]\n"
      "v_mul_f32 v111, v218, v111\n"
      "v_cvt_pk_bf16_f32 v140, v140, v141\n"
      "v_mfma_f32_16x16x32_bf16 v[214:217], v[128:131], a[192:195], 0\n"
      "s_waitcnt lgkmcnt(5)\n"
      "v_mul_f32 v142, v218, v142\n"
      "v_mul_f32_e64 v143, v218, v143\n"
      "v_mfma_f32_16x16x32_bf16 v[214:217], v[176:179], a[196:199], v[214:217]\n"
      "s_waitcnt lgkmcnt(4)\n"
      "v_cvt_pk_bf16_f32 v146, v108, v109\n"
      "v_cvt_pk_bf16_f32 v147, v110, v111\n"
      "v_mfma_f32_16x16x32_bf16 v[214:217], v[198:201], a[200:203], v[214:217]\n"
      "ds_write_b128 v5, v[144:147] offset:12288\n"
      "v_mfma_f32_16x16x32_bf16 v[214:217], v[210:213], a[204:207], v[214:217]\n"
      "s_waitcnt lgkmcnt(2)\n"
      "v_cvt_pk_bf16_f32 v141, v142, v143\n"
      "v_fma_f32 v131, v170, v170, s51\n"
      "v_mfma_f32_16x16x32_bf16 v[214:217], v[120:123], a[208:211], v[214:217]\n"
      "s_waitcnt lgkmcnt(1)\n"
      "v_fma_f32 v110, v136, v136, s51\n"
      "v_fmac_f32 v131, v171, v171\n"
      "v_mfma_f32_16x16x32_bf16 v[214:217], v[152:155], a[212:215], v[214:217]\n"
      "ds_write_b64 v6, v[140:141] offset:12288\n"
      "v_mfma_f32_16x16x32_bf16 v[120:123], a[0:3], v[182:185], 0\n"
      "v_fmac_f32 v110, v137, v137\n"
      "v_fmac_f32 v131, v172, v172\n"
      "v_mfma_f32_16x16x32_bf16 v[120:123], a[4:7], v[220:223], v[120:123]\n"
      "v_fmac_f32 v110, v138, v138\n"
      "v_fmac_f32 v131, v173, v173\n"
      "v_mfma_f32_16x16x32_bf16 v[120:123], a[8:11], v[124:127], v[120:123]\n"
      "v_fmac_f32 v110, v139, v139\n"
      "v_fmac_f32 v131, v148, v148\n"
      "v_mfma_f32_16x16x32_bf16 v[120:123], a[12:15], v[158:161], v[120:123]\n"
      "v_fmac_f32 v110, v202, v202\n"
      "v_fmac_f32 v131, v149, v149\n"
      "v_mfma_f32_16x16x32_bf16 v[120:123], a[16:19], v[112:115], v[120:123]\n"
      "v_fmac_f32 v110, v203, v203\n"
      "v_fmac_f32 v131, v150, v150\n"
      "v_mfma_f32_16x16x32_bf16 v[120:123], a[20:23], v[190:193], v[120:123]\n"
      "v_fmac_f32 v110, v204, v204\n"
      "v_fmac_f32 v131, v151, v151\n"
      "v_mfma_f32_16x16x32_bf16 v[140:143], a[24:27], v[182:185], 0\n"
      "v_fmac_f32 v110, v205, v205\n"
      "v_fmac_f32 v131, v96, v96\n"
      "v_mfma_f32_16x16x32_bf16 v[140:143], a[28:31], v[220:223], v[140:143]\n"
      "v_fmac_f32 v110, v102, v102\n"
      "v_fmac_f32 v131, v97, v97\n"
      "v_mfma_f32_16x16x32_bf16 v[140:143], a[32:35], v[124:127], v[140:143]\n"
      "v_fmac_f32 v110, v103, v103\n"
      "v_fmac_f32 v131, v98, v98\n"
      "v_mfma_f32_16x16x32_bf16 v[140:143], a[36:39], v[158:161], v[140:143]\n"
      "v_fmac_f32 v110, v104, v104\n"
      "v_fmac_f32 v131, v99, v99\n"
      "v_mfma_f32_16x16x32_bf16 v[140:143], a[40:43], v[112:115], v[140:143]\n"
      "v_fmac_f32_e64 v110, v105, v105\n"
      "v_fma_f32 v153, v120, v120, s51\n"
      "v_mfma_f32_16x16x32_bf16 v[140:143], a[44:47], v[190:193], v[140:143]\n"
      "v_permlane16_swap_b32 v131, v110\n"
      "v_mfma_f32_16x16x32_bf16 v[144:147], a[48:51], v[182:185], 0\n"
      "v_add_f32 v131, v131, v110\n"
      "v_mov_b32 v110, v131\n"
      "v_mfma_f32_16x16x32_bf16 v[144:147], a[52:55], v[220:223], v[144:147]\n"
      "v_fmac_f32 v153, v121, v121\n"
      "v_permlane32_swap_b32 v131, v110\n"
      "v_mfma_f32_16x16x32_bf16 v[144:147], a[56:59], v[124:127], v[144:147]\n"
      "v_add_f32 v131, v131, v110\n"
      "v_rsq_f32 v131, v131\n"
      "v_mfma_f32_16x16x32_bf16 v[144:147], a[60:63], v[158:161], v[144:147]\n"
      "v_mov_b32 v110, v131\n"
      "v_fmac_f32 v153, v122, v122\n"
      "v_mfma_f32_16x16x32_bf16 v[144:147], a[64:67], v[112:115], v[144:147]\n"
      "v_permlane16_swap_b32 v131, v110\n"
      "v_mfma_f32_16x16x32_bf16 v[144:147], a[68:71], v[190:193], v[144:147]\n"
      "v_mul_f32 v131, s50, v131\n"
      "v_mul_f32 v136, v110, v136\n"
      "v_mfma_f32_16x16x32_bf16 v[198:201], a[72:75], v[182:185], 0\n"
      "v_mul_f32 v137, v110, v137\n"
      "v_mul_f32 v170, v131, v170\n"
      "v_mfma_f32_16x16x32_bf16 v[198:201], a[76:79], v[220:223], v[198:201]\n"
      "v_mul_f32 v171, v131, v171\n"
      "v_cvt_pk_bf16_f32 v136, v136, v137\n"
      "v_mfma_f32_16x16x32_bf16 v[198:201], a[80:83], v[124:127], v[198:201]\n"
      "v_mul_f32 v138, v110, v138\n"
      "v_mul_f32 v139, v110, v139\n"
      "v_mfma_f32_16x16x32_bf16 v[198:201], a[84:87], v[158:161], v[198:201]\n"
      "v_mul_f32 v102, v110, v102\n"
      "v_mul_f32_e64 v103, v110, v103\n"
      "v_mfma_f32_16x16x32_bf16 v[198:201], a[88:91], v[112:115], v[198:201]\n"
      "v_cvt_pk_bf16_f32 v170, v170, v171\n"
      "v_mul_f32 v172, v131, v172\n"
      "v_mfma_f32_16x16x32_bf16 v[198:201], a[92:95], v[190:193], v[198:201]\n"
      "v_mul_f32 v173, v131, v173\n"
      "v_mul_f32_e64 v96, v131, v96\n"
      "v_mfma_f32_16x16x32_bf16 v[174:177], a[96:99], v[182:185], 0\n"
      "v_mul_f32 v97, v131, v97\n"
      "v_cvt_pk_bf16_f32 v137, v138, v139\n"
      "v_mfma_f32_16x16x32_bf16 v[174:177], a[100:103], v[220:223], v[174:177]\n"
      "v_mul_f32 v202, v110, v202\n"
      "v_mul_f32 v203, v110, v203\n"
      "v_mfma_f32_16x16x32_bf16 v[174:177], a[104:107], v[124:127], v[174:177]\n"
      "v_mul_f32 v204, v110, v204\n"
      "v_mul_f32_e64 v205, v110, v205\n"
      "v_mfma_f32_16x16x32_bf16 v[174:177], a[108:111], v[158:161], v[174:177]\n"
      "v_cvt_pk_bf16_f32 v102, v102, v103\n"
      "v_mul_f32 v104, v110, v104\n"
      "v_mfma_f32_16x16x32_bf16 v[174:177], a[112:115], v[112:115], v[174:177]\n"
      "v_mul_f32 v105, v110, v105\n"
      "v_cvt_pk_bf16_f32 v171, v172, v173\n"
      "v_mfma_f32_16x16x32_bf16 v[174:177], a[116:119], v[190:193], v[174:177]\n"
      "v_mul_f32 v148, v131, v148\n"
      "v_mul_f32 v149, v131, v149\n"
      "v_mfma_f32_16x16x32_bf16 v[108:111], a[120:123], v[182:185], 0\n"
      "v_mul_f32 v150, v131, v150\n"
      "v_mul_f32 v151, v131, v151\n"
      "v_mfma_f32_16x16x32_bf16 v[108:111], a[124:127], v[220:223], v[108:111]\n"
      "v_cvt_pk_bf16_f32 v96, v96, v97\n"
      "v_mul_f32 v98, v131, v98\n"
      "v_mfma_f32_16x16x32_bf16 v[108:111], a[128:131], v[124:127], v[108:111]\n"
      "v_mul_f32 v99, v131, v99\n"
      "v_cvt_pk_bf16_f32 v138, v202, v203\n"
      "v_mfma_f32_16x16x32_bf16 v[108:111], a[132:135], v[158:161], v[108:111]\n"
      "ds_read_b128 v[128:131], v7 offset:18432\n"
      "v_cvt_pk_bf16_f32 v139, v204, v205\n"
      "v_mfma_f32_16x16x32_bf16 v[108:111], a[136:139], v[112:115], v[108:111]\n"
      "ds_read_b128 v[178:181], v7 offset:22528\n"
      "v_cvt_pk_bf16_f32 v103, v104, v105\n"
      "v_mfma_f32_16x16x32_bf16 v[108:111], a[140:143], v[190:193], v[108:111]\n"
      "v_cvt_pk_bf16_f32 v172, v148, v149\n"
      "v_cvt_pk_bf16_f32 v173, v150, v151\n"
      "v_mfma_f32_16x16x32_bf16 v[166:169], v[182:185], a[144:147], 0\n"
      "ds_read_b128 v[202:205], v7 offset:26624\n"
      "v_cvt_pk_bf16_f32 v97, v98, v99\n"
      "v_mfma_f32_16x16x32_bf16 v[166:169], v[220:223], a[148:151], v[166:169]\n"
      "ds_read_b128 v[186:189], v7 offset:30720\n"
      "v_fma_f32 v152, v198, v198, s51\n"
      "v_mfma_f32_16x16x32_bf16 v[166:169], v[124:127], a[152:155], v[166:169]\n"
      "v_fmac_f32 v152, v199, v199\n"
      "v_fmac_f32 v152, v200, v200\n"
      "v_mfma_f32_16x16x32_bf16 v[166:169], v[158:161], a[156:159], v[166:169]\n"
      "ds_read_b128 v[148:151], v7 offset:34816\n"
      "v_fmac_f32 v153, v123, v123\n"
      "v_mfma_f32_16x16x32_bf16 v[166:169], v[112:115], a[160:163], v[166:169]\n"
      "v_fmac_f32 v152, v201, v201\n"
      "v_fmac_f32_e64 v153, v140, v140\n"
      "v_mfma_f32_16x16x32_bf16 v[166:169], v[190:193], a[164:167], v[166:169]\n"
      "ds_read_b128 v[154:157], v0 offset:4096\n"
      "v_fmac_f32 v152, v174, v174\n"
      "v_mfma_f32_16x16x32_bf16 v[210:213], v[182:185], a[168:171], 0\n"
      "ds_read_b128 v[104:107], v1 offset:4096\n"
      "v_fmac_f32 v153, v141, v141\n"
      "v_mfma_f32_16x16x32_bf16 v[210:213], v[220:223], a[172:175], v[210:213]\n"
      "s_waitcnt lgkmcnt(3)\n"
      "v_fmac_f32 v152, v175, v175\n"
      "v_fmac_f32_e64 v153, v142, v142\n"
      "v_mfma_f32_16x16x32_bf16 v[210:213], v[124:127], a[176:179], v[210:213]\n"
      "buffer_store_dwordx4 v[128:131], v20, s[24:27], s46 offen sc1\n"
      "v_mfma_f32_16x16x32_bf16 v[210:213], v[158:161], a[180:183], v[210:213]\n"
      "ds_read_b128 v[206:209], v0 offset:10240\n"
      "v_fmac_f32_e64 v152, v176, v176\n"
      "v_mfma_f32_16x16x32_bf16 v[210:213], v[112:115], a[184:187], v[210:213]\n"
      "ds_read_b128 v[98:101], v1 offset:10240\n"
      "v_fmac_f32_e64 v153, v143, v143\n"
      "v_mfma_f32_16x16x32_bf16 v[210:213], v[190:193], a[188:191], v[210:213]\n"
      "ds_read_b128 v[116:119], v0 offset:16384\n"
      "v_fmac_f32 v152, v177, v177\n"
      "v_mfma_f32_16x16x32_bf16 v[128:131], v[182:185], a[192:195], 0\n"
      "s_waitcnt lgkmcnt(3)\n"
      "v_fmac_f32 v153, v144, v144\n"
      "v_fmac_f32 v152, v108, v108\n"
      "v_mfma_f32_16x16x32_bf16 v[128:131], v[220:223], a[196:199], v[128:131]\n"
      "ds_read_b128 v[220:223], v1 offset:16384\n"
      "v_fmac_f32 v153, v145, v145\n"
      "v_mfma_f32_16x16x32_bf16 v[128:131], v[124:127], a[200:203], v[128:131]\n"
      "v_fmac_f32 v152, v109, v109\n"
      "v_fmac_f32 v153, v146, v146\n"
      "v_mfma_f32_16x16x32_bf16 v[128:131], v[158:161], a[204:207], v[128:131]\n"
      "s_waitcnt lgkmcnt(3)\n"
      "v_fmac_f32 v152, v110, v110\n"
      "v_fmac_f32 v153, v147, v147\n"
      "v_mfma_f32_16x16x32_bf16 v[128:131], v[112:115], a[208:211], v[128:131]\n"
      "buffer_store_dwordx4 v[178:181], v21, s[24:27], s46 offen sc1\n"
      "v_mfma_f32_16x16x32_bf16 v[128:131], v[190:193], a[212:215], v[128:131]\n"
      "s_waitcnt lgkmcnt(1)\n"
      "v_fmac_f32 v152, v111, v111\n"
      "v_cvt_pk_bf16_f32 v194, v194, v195\n"
      "v_mfma_f32_16x16x32_bf16 v[190:193], a[0:3], v[154:157], 0\n"
      "s_waitcnt lgkmcnt(0)\n"
      "v_permlane16_swap_b32 v153, v152\n"
      "v_mfma_f32_16x16x32_bf16 v[190:193], a[4:7], v[104:107], v[190:193]\n"
      "v_add_f32 v153, v153, v152\n"
      "v_mov_b32 v152, v153\n"
      "v_mfma_f32_16x16x32_bf16 v[190:193], a[8:11], v[206:209], v[190:193]\n"
      "v_cvt_pk_bf16_f32 v195, v196, v197\n"
      "v_permlane32_swap_b32 v153, v152\n"
      "v_mfma_f32_16x16x32_bf16 v[190:193], a[12:15], v[98:101], v[190:193]\n"
      "v_add_f32 v153, v153, v152\n"
      "v_rsq_f32 v153, v153\n"
      "v_mfma_f32_16x16x32_bf16 v[190:193], a[16:19], v[116:119], v[190:193]\n"
      "v_mov_b32 v152, v153\n"
      "v_cvt_pk_bf16_f32 v196, v166, v167\n"
      "v_mfma_f32_16x16x32_bf16 v[190:193], a[20:23], v[220:223], v[190:193]\n"
      "v_permlane16_swap_b32 v153, v152\n"
      "v_mfma_f32_16x16x32_bf16 v[182:185], a[24:27], v[154:157], 0\n"
      "buffer_store_dwordx4 v[202:205], v22, s[24:27], s46 offen sc1\n"
      "v_mfma_f32_16x16x32_bf16 v[182:185], a[28:31], v[104:107], v[182:185]\n"
      "v_mul_f32 v153, s50, v153\n"
      "v_mul_f32 v198, v152, v198\n"
      "v_mfma_f32_16x16x32_bf16 v[182:185], a[32:35], v[206:209], v[182:185]\n"
      "v_mul_f32 v199, v152, v199\n"
      "v_mul_f32 v120, v153, v120\n"
      "v_mfma_f32_16x16x32_bf16 v[182:185], a[36:39], v[98:101], v[182:185]\n"
      "v_mul_f32 v121, v153, v121\n"
      "v_cvt_pk_bf16_f32 v198, v198, v199\n"
      "v_mfma_f32_16x16x32_bf16 v[182:185], a[40:43], v[116:119], v[182:185]\n"
      "v_mul_f32 v200, v152, v200\n"
      "v_mul_f32 v201, v152, v201\n"
      "v_mfma_f32_16x16x32_bf16 v[182:185], a[44:47], v[220:223], v[182:185]\n"
      "v_mul_f32 v108, v152, v108\n"
      "v_mul_f32 v109, v152, v109\n"
      "v_mfma_f32_16x16x32_bf16 v[158:161], a[48:51], v[154:157], 0\n"
      "v_cvt_pk_bf16_f32 v120, v120, v121\n"
      "v_mul_f32_e64 v122, v153, v122\n"
      "v_mfma_f32_16x16x32_bf16 v[158:161], a[52:55], v[104:107], v[158:161]\n"
      "v_mul_f32 v123, v153, v123\n"
      "v_mul_f32 v144, v153, v144\n"
      "v_mfma_f32_16x16x32_bf16 v[158:161], a[56:59], v[206:209], v[158:161]\n"
      "buffer_store_dwordx4 v[186:189], v23, s[24:27], s46 offen sc1\n"
      "v_mfma_f32_16x16x32_bf16 v[158:161], a[60:63], v[98:101], v[158:161]\n"
      "v_mul_f32 v145, v153, v145\n"
      "v_cvt_pk_bf16_f32 v199, v200, v201\n"
      "v_mfma_f32_16x16x32_bf16 v[158:161], a[64:67], v[116:119], v[158:161]\n"
      "v_mul_f32 v174, v152, v174\n"
      "v_mul_f32 v175, v152, v175\n"
      "v_mfma_f32_16x16x32_bf16 v[158:161], a[68:71], v[220:223], v[158:161]\n"
      "v_mul_f32 v176, v152, v176\n"
      "v_mul_f32_e64 v177, v152, v177\n"
      "v_mfma_f32_16x16x32_bf16 v[186:189], a[72:75], v[154:157], 0\n"
      "v_cvt_pk_bf16_f32 v108, v108, v109\n"
      "v_mul_f32 v110, v152, v110\n"
      "v_mfma_f32_16x16x32_bf16 v[186:189], a[76:79], v[104:107], v[186:189]\n"
      "ds_read_b128 v[202:205], v9 offset:5632\n"
      "v_mul_f32 v111, v152, v111\n"
      "v_mfma_f32_16x16x32_bf16 v[186:189], a[80:83], v[206:209], v[186:189]\n"
      "v_cvt_pk_bf16_f32 v121, v122, v123\n"
      "v_mul_f32 v140, v153, v140\n"
      "v_mfma_f32_16x16x32_bf16 v[186:189], a[84:87], v[98:101], v[186:189]\n"
      "v_mul_f32 v141, v153, v141\n"
      "v_mul_f32 v142, v153, v142\n"
      "v_mfma_f32_16x16x32_bf16 v[186:189], a[88:91], v[116:119], v[186:189]\n"
      "buffer_store_dwordx4 v[148:151], v25, s[24:27], s46 offen sc1\n"
      "v_mfma_f32_16x16x32_bf16 v[186:189], a[92:95], v[220:223], v[186:189]\n"
      "v_mul_f32 v143, v153, v143\n"
      "v_cvt_pk_bf16_f32 v144, v144, v145\n"
      "v_mfma_f32_16x16x32_bf16 v[124:127], a[96:99], v[154:157], 0\n"
      "v_mul_f32 v146, v153, v146\n"
      "v_mul_f32 v147, v153, v147\n"
      "v_mfma_f32_16x16x32_bf16 v[124:127], a[100:103], v[104:107], v[124:127]\n"
      "v_cvt_pk_bf16_f32 v200, v174, v175\n"
      "v_cvt_pk_bf16_f32 v201, v176, v177\n"
      "v_mfma_f32_16x16x32_bf16 v[124:127], a[104:107], v[206:209], v[124:127]\n"
      "v_cvt_pk_bf16_f32 v109, v110, v111\n"
      "v_cvt_pk_bf16_f32 v122, v140, v141\n"
      "v_mfma_f32_16x16x32_bf16 v[124:127], a[108:111], v[98:101], v[124:127]\n"
      "v_cvt_pk_bf16_f32 v123, v142, v143\n"
      "v_cvt_pk_bf16_f32 v145, v146, v147\n"
      "v_mfma_f32_16x16x32_bf16 v[124:127], a[112:115], v[116:119], v[124:127]\n"
      "v_fma_f32 v218, v190, v190, s51\n"
      "v_fma_f32 v219, v186, v186, s51\n"
      "v_mfma_f32_16x16x32_bf16 v[124:127], a[116:119], v[220:223], v[124:127]\n"
      "v_fmac_f32 v218, v191, v191\n"
      "v_fmac_f32 v219, v187, v187\n"
      "v_mfma_f32_16x16x32_bf16 v[140:143], a[120:123], v[154:157], 0\n"
      "v_fmac_f32 v218, v192, v192\n"
      "v_fmac_f32 v219, v188, v188\n"
      "v_mfma_f32_16x16x32_bf16 v[140:143], a[124:127], v[104:107], v[140:143]\n"
      "v_fmac_f32 v218, v193, v193\n"
      "v_fmac_f32 v219, v189, v189\n"
      "v_mfma_f32_16x16x32_bf16 v[140:143], a[128:131], v[206:209], v[140:143]\n"
      "v_fmac_f32 v218, v182, v182\n"
      "v_fmac_f32 v219, v124, v124\n"
      "v_mfma_f32_16x16x32_bf16 v[140:143], a[132:135], v[98:101], v[140:143]\n"
      "v_fmac_f32 v218, v183, v183\n"
      "v_fmac_f32 v219, v125, v125\n"
      "v_mfma_f32_16x16x32_bf16 v[140:143], a[136:139], v[116:119], v[140:143]\n"
      "v_fmac_f32 v218, v184, v184\n"
      "v_fmac_f32 v219, v126, v126\n"
      "v_mfma_f32_16x16x32_bf16 v[140:143], a[140:143], v[220:223], v[140:143]\n"
      "v_fmac_f32 v218, v185, v185\n"
      "v_fmac_f32 v219, v127, v127\n"
      "v_mfma_f32_16x16x32_bf16 v[178:181], v[154:157], a[144:147], 0\n"
      "v_fmac_f32 v218, v158, v158\n"
      "v_fmac_f32 v218, v159, v159\n"
      "v_mfma_f32_16x16x32_bf16 v[178:181], v[104:107], a[148:151], v[178:181]\n"
      "v_fmac_f32 v218, v160, v160\n"
      "v_fmac_f32 v218, v161, v161\n"
      "v_mfma_f32_16x16x32_bf16 v[178:181], v[206:209], a[152:155], v[178:181]\n"
      "v_fmac_f32 v219, v140, v140\n"
      "v_fmac_f32 v219, v141, v141\n"
      "v_mfma_f32_16x16x32_bf16 v[178:181], v[98:101], a[156:159], v[178:181]\n"
      "v_fmac_f32 v219, v142, v142\n"
      "v_fmac_f32 v219, v143, v143\n"
      "v_mfma_f32_16x16x32_bf16 v[178:181], v[116:119], a[160:163], v[178:181]\n"
      "v_cvt_pk_bf16_f32 v197, v168, v169\n"
      "v_permlane16_swap_b32_e64 v218, v219\n"
      "v_mfma_f32_16x16x32_bf16 v[178:181], v[220:223], a[164:167], v[178:181]\n"
      "ds_read_b128 v[174:177], v2 offset:18432\n"
      "v_add_f32_e64 v218, v218, v219\n"
      "v_mfma_f32_16x16x32_bf16 v[150:153], v[154:157], a[168:171], 0\n"
      "ds_read_b128 v[110:113], v2 offset:21504\n"
      "v_mov_b32 v219, v218\n"
      "v_mfma_f32_16x16x32_bf16 v[150:153], v[104:107], a[172:175], v[150:153]\n"
      "ds_read_b128 v[146:149], v2 offset:24576\n"
      "v_permlane32_swap_b32 v218, v219\n"
      "v_mfma_f32_16x16x32_bf16 v[150:153], v[206:209], a[176:179], v[150:153]\n"
      "v_add_f32 v218, v218, v219\n"
      "v_rsq_f32 v218, v218\n"
      "v_mfma_f32_16x16x32_bf16 v[150:153], v[98:101], a[180:183], v[150:153]\n"
      "v_mov_b32_e64 v219, v218\n"
      "v_cvt_pk_bf16_f32 v76, v178, v179\n"
      "v_mfma_f32_16x16x32_bf16 v[150:153], v[116:119], a[184:187], v[150:153]\n"
      "v_permlane16_swap_b32 v218, v219\n"
      "v_mfma_f32_16x16x32_bf16 v[150:153], v[220:223], a[188:191], v[150:153]\n"
      "s_waitcnt lgkmcnt(2)\n"
      "v_mul_f32 v218, s50, v218\n"
      "v_mul_f32 v186, v219, v186\n"
      "v_mfma_f32_16x16x32_bf16 v[166:169], v[154:157], a[192:195], 0\n"
      "s_waitcnt lgkmcnt(1)\n"
      "v_mul_f32 v187, v219, v187\n"
      "v_mul_f32_e64 v190, v218, v190\n"
      "v_mfma_f32_16x16x32_bf16 v[166:169], v[104:107], a[196:199], v[166:169]\n"
      "s_waitcnt lgkmcnt(0)\n"
      "v_mul_f32 v191, v218, v191\n"
      "v_cvt_pk_bf16_f32 v186, v186, v187\n"
      "v_mfma_f32_16x16x32_bf16 v[166:169], v[206:209], a[200:203], v[166:169]\n"
      "ds_read_b128 v[154:157], v2 offset:27648\n"
      "v_mul_f32 v188, v219, v188\n"
      "v_mfma_f32_16x16x32_bf16 v[166:169], v[98:101], a[204:207], v[166:169]\n"
      "ds_read_b128 v[98:101], v2 offset:30720\n"
      "v_mul_f32 v189, v219, v189\n"
      "v_mfma_f32_16x16x32_bf16 v[166:169], v[116:119], a[208:211], v[166:169]\n"
      "ds_read_b128 v[104:107], v2 offset:33792\n"
      "v_mul_f32 v140, v219, v140\n"
      "v_mfma_f32_16x16x32_bf16 v[166:169], v[220:223], a[212:215], v[166:169]\n"
      "v_mul_f32 v141, v219, v141\n"
      "v_cvt_pk_bf16_f32 v190, v190, v191\n"
      "v_mfma_f32_16x16x32_bf16 v[114:117], a[216:219], v[174:177], 0\n"
      "v_mul_f32 v192, v218, v192\n"
      "v_mul_f32 v193, v218, v193\n"
      "v_mfma_f32_16x16x32_bf16 v[114:117], a[220:223], v[110:113], v[114:117]\n"
      "v_mul_f32 v158, v218, v158\n"
      "v_mul_f32 v159, v218, v159\n"
      "v_mfma_f32_16x16x32_bf16 v[114:117], a[224:227], v[146:149], v[114:117]\n"
      "s_waitcnt lgkmcnt(2)\n"
      "v_cvt_pk_bf16_f32 v187, v188, v189\n"
      "v_mul_f32 v124, v219, v124\n"
      "v_mfma_f32_16x16x32_bf16 v[206:209], a[240:243], v[174:177], 0\n"
      "s_waitcnt lgkmcnt(1)\n"
      "v_mul_f32 v125, v219, v125\n"
      "v_mul_f32 v126, v219, v126\n"
      "v_mfma_f32_16x16x32_bf16 v[206:209], a[244:247], v[110:113], v[206:209]\n"
      "s_waitcnt lgkmcnt(0)\n"
      "v_mul_f32 v127, v219, v127\n"
      "v_cvt_pk_bf16_f32 v140, v140, v141\n"
      "v_mfma_f32_16x16x32_bf16 v[206:209], a[248:251], v[146:149], v[206:209]\n"
      "v_mul_f32 v142, v219, v142\n"
      "v_mul_f32_e64 v143, v219, v143\n"
      "v_mfma_f32_16x16x32_bf16 v[220:223], v[232:235], v[174:177], 0\n"
      "v_cvt_pk_bf16_f32 v191, v192, v193\n"
      "v_mul_f32 v182, v218, v182\n"
      "v_mfma_f32_16x16x32_bf16 v[220:223], v[236:239], v[110:113], v[220:223]\n"
      "v_mul_f32 v183, v218, v183\n"
      "v_mul_f32 v184, v218, v184\n"
      "v_mfma_f32_16x16x32_bf16 v[220:223], v[240:243], v[146:149], v[220:223]\n"
      "ds_read_b128 v[146:149], v10 offset:32\n"
      "v_mul_f32 v185, v218, v185\n"
      "v_mfma_f32_16x16x32_bf16 v[114:117], a[228:231], v[154:157], v[114:117]\n"
      "v_cvt_pk_bf16_f32 v158, v158, v159\n"
      "v_mul_f32 v160, v218, v160\n"
      "v_mfma_f32_16x16x32_bf16 v[114:117], a[232:235], v[98:101], v[114:117]\n"
      "v_mul_f32 v161, v218, v161\n"
      "v_cvt_pk_bf16_f32 v188, v124, v125\n"
      "v_mfma_f32_16x16x32_bf16 v[114:117], a[236:239], v[104:107], v[114:117]\n"
      "v_cvt_pk_bf16_f32 v189, v126, v127\n"
      "v_cvt_pk_bf16_f32 v141, v142, v143\n"
      "v_mfma_f32_16x16x32_bf16 v[206:209], a[252:255], v[154:157], v[206:209]\n"
      "ds_read_b64 v[218:219], v10 offset:24\n"
      "v_cvt_pk_bf16_f32 v192, v182, v183\n"
      "v_mfma_f32_16x16x32_bf16 v[206:209], v[224:227], v[98:101], v[206:209]\n"
      "ds_read_b128 v[110:113], v2 offset:18688\n"
      "v_cvt_pk_bf16_f32 v193, v184, v185\n"
      "v_mfma_f32_16x16x32_bf16 v[206:209], v[228:231], v[104:107], v[206:209]\n"
      "ds_read_b128 v[176:179], v2 offset:21760\n"
      "v_cvt_pk_bf16_f32 v159, v160, v161\n"
      "v_mfma_f32_16x16x32_bf16 v[220:223], v[244:247], v[154:157], v[220:223]\n"
      "ds_read_b128 v[154:157], v2 offset:24832\n"
      "v_cvt_pk_bf16_f32 v77, v180, v181\n"
      "v_mfma_f32_16x16x32_bf16 v[220:223], v[248:251], v[98:101], v[220:223]\n"
      "s_waitcnt lgkmcnt(4)\n"
      "v_cvt_pk_bf16_f32 v132, v132, v133\n"
      "v_cvt_pk_bf16_f32 v133, v134, v135\n"
      "v_mfma_f32_16x16x32_bf16 v[220:223], v[252:255], v[104:107], v[220:223]\n"
      "v_cvt_pk_bf16_f32 v134, v210, v211\n"
      "v_cvt_pk_bf16_f32 v135, v212, v213\n"
      "v_mfma_f32_16x16x32_bf16 v[210:213], v[136:139], v[170:173], v[40:43]\n"
      "s_waitcnt lgkmcnt(3)\n"
      "v_cvt_pk_bf16_f32 v80, v150, v151\n"
      "v_cvt_pk_bf16_f32 v81, v152, v153\n"
      "v_mfma_f32_16x16x32_bf16 v[104:107], v[198:201], v[170:173], v[44:47]\n"
      "s_waitcnt lgkmcnt(2)\n"
      "v_cvt_pk_bf16_f32 v214, v214, v215\n"
      "v_cvt_pk_bf16_f32 v215, v216, v217\n"
      "v_mfma_f32_16x16x32_bf16 v[98:101], v[186:189], v[170:173], v[48:51]\n"
      "s_waitcnt lgkmcnt(1)\n"
      "v_cvt_pk_bf16_f32 v216, v128, v129\n"
      "v_cvt_pk_bf16_f32 v217, v130, v131\n"
      "v_mfma_f32_16x16x32_bf16 v[150:153], v[146:149], v[170:173], v[24:27]\n"
      "s_waitcnt lgkmcnt(0)\n"
      "v_cvt_pk_bf16_f32 v84, v166, v167\n"
      "v_cvt_pk_bf16_f32 v85, v168, v169\n"
      "v_mfma_f32_16x16x16_bf16 v[210:213], v[102:103], v[96:97], v[210:213]\n"
      "v_fma_f32 v161, v114, v114, s52\n"
      "v_fmac_f32 v161, v115, v115\n"
      "v_fmac_f32 v161, v116, v116\n"
      "v_mfma_f32_16x16x16_bf16 v[104:107], v[108:109], v[96:97], v[104:107]\n"
      "v_fmac_f32 v161, v117, v117\n"
      "v_fmac_f32 v161, v206, v206\n"
      "v_fmac_f32 v161, v207, v207\n"
      "v_mfma_f32_16x16x16_bf16 v[98:101], v[140:141], v[96:97], v[98:101]\n"
      "v_exp_f32 v210, v210\n"
      "v_exp_f32 v211, v211\n"
      "v_mfma_f32_16x16x16_bf16 v[150:153], v[218:219], v[96:97], v[150:153]\n"
      "v_exp_f32 v212, v212\n"
      "v_exp_f32 v213, v213\n"
      "v_mfma_f32_16x16x32_bf16 v[180:183], a[216:219], v[110:113], 0\n"
      "v_exp_f32 v104, v104\n"
      "v_mfma_f32_16x16x32_bf16 v[180:183], a[220:223], v[176:179], v[180:183]\n"
      "v_exp_f32 v105, v105\n"
      "v_mfma_f32_16x16x32_bf16 v[180:183], a[224:227], v[154:157], v[180:183]\n"
      "v_exp_f32 v106, v106\n"
      "v_mfma_f32_16x16x32_bf16 v[124:127], a[240:243], v[110:113], 0\n"
      "v_exp_f32 v107, v107\n"
      "v_mfma_f32_16x16x32_bf16 v[124:127], a[244:247], v[176:179], v[124:127]\n"
      "v_cvt_pk_bf16_f32 v210, v210, v211\n"
      "v_cvt_pk_bf16_f32 v211, v212, v213\n"
      "v_mfma_f32_16x16x32_bf16 v[124:127], a[248:251], v[154:157], v[124:127]\n"
      "v_cvt_pk_bf16_f32 v212, v104, v105\n"
      "v_cvt_pk_bf16_f32 v213, v106, v107\n"
      "v_mfma_f32_16x16x32_bf16 v[166:169], v[232:235], v[110:113], 0\n"
      "v_exp_f32_e64 v98, v98\n"
      "v_mfma_f32_16x16x32_bf16 v[166:169], v[236:239], v[176:179], v[166:169]\n"
      "ds_read_u16 v78, v11\n"
      "v_exp_f32 v99, v99\n"
      "v_mfma_f32_16x16x32_bf16 v[166:169], v[240:243], v[154:157], v[166:169]\n"
      "v_exp_f32 v100, v100\n"
      "v_mfma_f32_16x16x32_bf16 v[110:113], v[136:139], v[120:123], v[52:55]\n"
      "ds_read_u16 v82, v11 offset:32\n"
      "v_exp_f32 v101, v101\n"
      "v_mfma_f32_16x16x32_bf16 v[104:107], v[198:201], v[120:123], v[56:59]\n"
      "v_exp_f32 v150, v150\n"
      "v_mfma_f32_16x16x32_bf16 v[154:157], v[186:189], v[120:123], v[60:63]\n"
      "ds_read_u16 v86, v11 offset:64\n"
      "v_cvt_pk_bf16_f32 v88, v98, v99\n"
      "v_mfma_f32_16x16x32_bf16 v[176:179], v[146:149], v[120:123], v[26:29]\n"
      "v_cvt_pk_bf16_f32 v89, v100, v101\n"
      "v_cvt_pk_bf16_f32 v90, v150, 0\n"
      "v_mfma_f32_16x16x16_bf16 v[110:113], v[102:103], v[144:145], v[110:113]\n"
      "v_fmac_f32 v161, v208, v208\n"
      "v_fmac_f32 v161, v209, v209\n"
      "v_fmac_f32_e64 v161, v220, v220\n"
      "v_mfma_f32_16x16x16_bf16 v[104:107], v[108:109], v[144:145], v[104:107]\n"
      "v_fmac_f32 v161, v221, v221\n"
      "v_fmac_f32 v161, v222, v222\n"
      "v_fmac_f32 v161, v223, v223\n"
      "v_mfma_f32_16x16x16_bf16 v[154:157], v[140:141], v[144:145], v[154:157]\n"
      "v_exp_f32 v110, v110\n"
      "v_exp_f32 v111, v111\n"
      "v_mfma_f32_16x16x16_bf16 v[176:179], v[218:219], v[144:145], v[176:179]\n"
      "v_exp_f32 v112, v112\n"
      "v_exp_f32 v113, v113\n"
      "v_mfma_f32_16x16x32_bf16 v[142:145], v[32:35], v[210:213], 0\n"
      "s_waitcnt lgkmcnt(0)\n"
      "v_exp_f32 v104, v104\n"
      "v_mfma_f32_16x16x32_bf16 v[142:145], v[32:35], v[88:91], v[142:145]\n"
      "v_exp_f32 v105, v105\n"
      "v_mfma_f32_16x16x32_bf16 v[120:123], v[194:197], v[210:213], 0\n"
      "v_exp_f32_e64 v106, v106\n"
      "v_mfma_f32_16x16x32_bf16 v[120:123], v[76:79], v[88:91], v[120:123]\n"
      "ds_read_b128 v[98:101], v2 offset:27904\n"
      "v_exp_f32_e64 v107, v107\n"
      "v_mfma_f32_16x16x32_bf16 v[150:153], v[132:135], v[210:213], 0\n"
      "ds_read_b128 v[170:173], v2 offset:30976\n"
      "v_cvt_pk_bf16_f32 v110, v110, v111\n"
      "v_mfma_f32_16x16x32_bf16 v[150:153], v[80:83], v[88:91], v[150:153]\n"
      "v_cvt_pk_bf16_f32 v111, v112, v113\n"
      "v_cvt_pk_bf16_f32 v112, v104, v105\n"
      "v_mfma_f32_16x16x32_bf16 v[128:131], v[214:217], v[210:213], 0\n"
      "v_cvt_pk_bf16_f32 v113, v106, v107\n"
      "v_exp_f32_e64 v154, v154\n"
      "v_mfma_f32_16x16x32_bf16 v[128:131], v[84:87], v[88:91], v[128:131]\n"
      "ds_read_b128 v[210:213], v2 offset:34048\n"
      "v_exp_f32 v155, v155\n"
      "v_mfma_f32_16x16x32_bf16 v[104:107], v[136:139], v[190:193], v[64:67]\n"
      "v_exp_f32 v156, v156\n"
      "v_exp_f32 v157, v157\n"
      "v_exp_f32 v176, v176\n"
      "v_cvt_pk_bf16_f32 v92, v154, v155\n"
      "v_cvt_pk_bf16_f32 v93, v156, v157\n"
      "v_cvt_pk_bf16_f32 v94, v176, 0\n"
      "v_rcp_f32 v142, v142\n"
      "s_waitcnt vmcnt(6)\n"
      "v_or3_b32 v162, v162, v163, v164\n"
      "v_mfma_f32_16x16x32_bf16 v[154:157], v[198:201], v[190:193], v[68:71]\n"
      "s_waitcnt lgkmcnt(1)\n"
      "v_mul_f32 v120, v142, v120\n"
      "v_mul_f32 v121, v142, v121\n"
      "v_cvt_pk_bf16_f32 v120, v120, v121\n"
      "v_mul_f32 v122, v142, v122\n"
      "v_mul_f32 v123, v142, v123\n"
      "v_cvt_pk_bf16_f32 v121, v122, v123\n"
      "v_mul_f32 v150, v142, v150\n"
      "v_mul_f32 v151, v142, v151\n"
      "v_mul_f32 v152, v142, v152\n"
      "v_mul_f32_e64 v153, v142, v153\n"
      "v_cvt_pk_bf16_f32 v122, v150, v151\n"
      "v_cvt_pk_bf16_f32 v123, v152, v153\n"
      "v_mfma_f32_16x16x32_bf16 v[150:153], v[186:189], v[190:193], v[72:75]\n"
      "ds_write_b128 v3, v[120:123]\n"
      "v_mfma_f32_16x16x32_bf16 v[118:121], v[146:149], v[190:193], v[28:31]\n"
      "s_waitcnt lgkmcnt(1)\n"
      "v_mul_f32 v128, v142, v128\n"
      "v_mul_f32_e64 v129, v142, v129\n"
      "v_mfma_f32_16x16x16_bf16 v[104:107], v[102:103], v[158:159], v[104:107]\n"
      "v_cvt_pk_bf16_f32 v128, v128, v129\n"
      "v_mul_f32_e64 v130, v142, v130\n"
      "v_mul_f32 v131, v142, v131\n"
      "v_mfma_f32_16x16x16_bf16 v[154:157], v[108:109], v[158:159], v[154:157]\n"
      "v_cvt_pk_bf16_f32 v129, v130, v131\n"
      "s_waitcnt vmcnt(5)\n"
      "v_or3_b32 v162, v162, v165, v8\n"
      "v_and_b32 v162, 0x7fff7fff, v162\n"
      "v_mfma_f32_16x16x16_bf16 v[150:153], v[140:141], v[158:159], v[150:153]\n"
      "v_exp_f32 v104, v104\n"
      "v_exp_f32 v105, v105\n"
      "v_mfma_f32_16x16x16_bf16 v[118:121], v[218:219], v[158:159], v[118:121]\n"
      "v_exp_f32 v106, v106\n"
      "v_exp_f32 v107, v107\n"
      "v_mfma_f32_16x16x32_bf16 v[180:183], a[228:231], v[98:101], v[180:183]\n"
      "ds_write_b64 v4, v[128:129]\n"
      "v_mfma_f32_16x16x32_bf16 v[180:183], a[232:235], v[170:173], v[180:183]\n"
      "v_exp_f32 v154, v154\n"
      "v_mfma_f32_16x16x32_bf16 v[180:183], a[236:239], v[210:213], v[180:183]\n"
      "v_exp_f32 v155, v155\n"
      "v_mfma_f32_16x16x32_bf16 v[124:127], a[252:255], v[98:101], v[124:127]\n"
      "ds_write_b32 v31, v162 offset:4608\n"
      "v_mfma_f32_16x16x32_bf16 v[124:127], v[224:227], v[170:173], v[124:127]\n"
      "ds_read_b128 v[142:145], v10\n"
      "v_exp_f32 v156, v156\n"
      "v_mfma_f32_16x16x32_bf16 v[124:127], v[228:231], v[210:213], v[124:127]\n"
      "v_exp_f32 v157, v157\n"
      "v_mfma_f32_16x16x32_bf16 v[166:169], v[244:247], v[98:101], v[166:169]\n"
      "v_cvt_pk_bf16_f32 v104, v104, v105\n"
      "v_cvt_pk_bf16_f32 v105, v106, v107\n"
      "v_mfma_f32_16x16x32_bf16 v[166:169], v[248:251], v[170:173], v[166:169]\n"
      "v_cvt_pk_bf16_f32 v106, v154, v155\n"
      "v_cvt_pk_bf16_f32 v107, v156, v157\n"
      "v_mfma_f32_16x16x32_bf16 v[166:169], v[252:255], v[210:213], v[166:169]\n"
      "ds_read_b64 v[100:101], v10 offset:16\n"
      "v_exp_f32 v150, v150\n"
      "v_mfma_f32_16x16x32_bf16 v[210:213], v[32:35], v[110:113], 0\n"
      "v_exp_f32 v151, v151\n"
      "v_mfma_f32_16x16x32_bf16 v[210:213], v[32:35], v[92:95], v[210:213]\n"
      "v_exp_f32_e64 v152, v152\n"
      "v_mfma_f32_16x16x32_bf16 v[190:193], v[194:197], v[110:113], 0\n"
      "s_waitcnt lgkmcnt(1)\n"
      "v_exp_f32 v153, v153\n"
      "v_mfma_f32_16x16x32_bf16 v[190:193], v[76:79], v[92:95], v[190:193]\n"
      "ds_read_b128 v[172:175], v2 offset:18944\n"
      "v_exp_f32_e64 v118, v118\n"
      "v_mfma_f32_16x16x32_bf16 v[154:157], v[132:135], v[110:113], 0\n"
      "ds_read_b128 v[176:179], v2 offset:22016\n"
      "v_cvt_pk_bf16_f32 v88, v150, v151\n"
      "v_mfma_f32_16x16x32_bf16 v[154:157], v[80:83], v[92:95], v[154:157]\n"
      "ds_read_b128 v[128:131], v2 offset:25088\n"
      "v_cvt_pk_bf16_f32 v89, v152, v153\n"
      "v_mfma_f32_16x16x32_bf16 v[96:99], v[214:217], v[110:113], 0\n"
      "s_waitcnt lgkmcnt(3)\n"
      "v_cvt_pk_bf16_f32 v90, v118, 0\n"
      "v_fma_f32 v170, v180, v180, s52\n"
      "v_mfma_f32_16x16x32_bf16 v[96:99], v[84:87], v[92:95], v[96:99]\n"
      "v_fmac_f32 v170, v181, v181\n"
      "v_fmac_f32 v170, v182, v182\n"
      "v_mfma_f32_16x16x32_bf16 v[162:165], v[142:145], v[136:139], v[30:33]\n"
      "v_fmac_f32 v170, v183, v183\n"
      "v_fmac_f32 v170, v124, v124\n"
      "v_mfma_f32_16x16x32_bf16 v[110:113], v[142:145], v[198:201], v[30:33]\n"
      "s_waitcnt lgkmcnt(2)\n"
      "v_fmac_f32 v170, v125, v125\n"
      "v_fmac_f32 v170, v126, v126\n"
      "v_mfma_f32_16x16x32_bf16 v[136:139], v[142:145], v[186:189], v[30:33]\n"
      "s_waitcnt lgkmcnt(1)\n"
      "v_fmac_f32 v170, v127, v127\n"
      "v_fmac_f32_e64 v170, v166, v166\n"
      "v_mfma_f32_16x16x32_bf16 v[186:189], v[142:145], v[146:149], v[30:33]\n"
      "s_waitcnt lgkmcnt(0)\n"
      "v_fmac_f32 v170, v167, v167\n"
      "v_fmac_f32 v170, v168, v168\n"
      "v_mfma_f32_16x16x16_bf16 v[162:165], v[100:101], v[102:103], v[162:165]\n"
      "v_fmac_f32 v170, v169, v169\n"
      "v_rcp_f32 v210, v210\n"
      "v_mfma_f32_16x16x16_bf16 v[110:113], v[100:101], v[108:109], v[110:113]\n"
      "v_permlane16_swap_b32 v161, v170\n"
      "v_add_f32 v161, v161, v170\n"
      "v_mfma_f32_16x16x16_bf16 v[136:139], v[100:101], v[140:141], v[136:139]\n"
      "v_mul_f32 v190, v210, v190\n"
      "v_mul_f32 v191, v210, v191\n"
      "v_cvt_pk_bf16_f32 v190, v190, v191\n"
      "v_mfma_f32_16x16x16_bf16 v[186:189], v[100:101], v[218:219], v[186:189]\n"
      "v_mul_f32 v192, v210, v192\n"
      "v_cndmask_b32 v162, v162, v110, s[58:59]\n"
      "v_mul_f32_e64 v193, v210, v193\n"
      "v_mfma_f32_16x16x32_bf16 v[144:147], a[216:219], v[172:175], 0\n"
      "v_cndmask_b32 v162, v162, v136, s[60:61]\n"
      "v_cvt_pk_bf16_f32 v191, v192, v193\n"
      "v_mfma_f32_16x16x32_bf16 v[144:147], a[220:223], v[176:179], v[144:147]\n"
      "v_mul_f32 v154, v210, v154\n"
      "v_mul_f32 v155, v210, v155\n"
      "v_mfma_f32_16x16x32_bf16 v[144:147], a[224:227], v[128:131], v[144:147]\n"
      "v_mul_f32 v156, v210, v156\n"
      "v_mul_f32 v157, v210, v157\n"
      "v_mfma_f32_16x16x32_bf16 v[108:111], a[240:243], v[172:175], 0\n"
      "v_cndmask_b32 v162, v162, v186, s[62:63]\n"
      "v_cvt_pk_bf16_f32 v192, v154, v155\n"
      "v_mfma_f32_16x16x32_bf16 v[108:111], a[244:247], v[176:179], v[108:111]\n"
      "ds_read_b128 v[100:103], v2 offset:28160\n"
      "v_cvt_pk_bf16_f32 v193, v156, v157\n"
      "v_mfma_f32_16x16x32_bf16 v[108:111], a[248:251], v[128:131], v[108:111]\n"
      "ds_read_b128 v[152:155], v2 offset:31232\n"
      "v_mul_f32 v96, v210, v96\n"
      "v_mfma_f32_16x16x32_bf16 v[140:143], v[232:235], v[172:175], 0\n"
      "ds_read_b128 v[136:139], v2 offset:34304\n"
      "v_mul_f32 v97, v210, v97\n"
      "v_mfma_f32_16x16x32_bf16 v[140:143], v[236:239], v[176:179], v[140:143]\n"
      "ds_write_b128 v3, v[190:193] offset:256\n"
      "v_mfma_f32_16x16x32_bf16 v[140:143], v[240:243], v[128:131], v[140:143]\n"
      "v_exp_f32 v162, v162\n"
      "v_mfma_f32_16x16x32_bf16 v[128:131], v[32:35], v[104:107], 0\n"
      "s_waitcnt lgkmcnt(3)\n"
      "v_cvt_pk_bf16_f32 v96, v96, v97\n"
      "v_mul_f32 v98, v210, v98\n"
      "v_mfma_f32_16x16x32_bf16 v[128:131], v[32:35], v[88:91], v[128:131]\n"
      "s_waitcnt lgkmcnt(2)\n"
      "v_mul_f32_e64 v99, v210, v99\n"
      "v_cvt_pk_bf16_f32 v97, v98, v99\n"
      "v_mfma_f32_16x16x32_bf16 v[188:191], v[194:197], v[104:107], 0\n"
      "ds_write_b64 v4, v[96:97] offset:256\n"
      "v_mfma_f32_16x16x32_bf16 v[188:191], v[76:79], v[88:91], v[188:191]\n"
      "s_waitcnt lgkmcnt(2)\n"
      "v_cvt_pk_bf16_f32 v162, v162, v162\n"
      "v_rcp_f32 v128, v128\n"
      "v_mfma_f32_16x16x32_bf16 v[148:151], v[132:135], v[104:107], 0\n"
      "ds_write_b16 v13, v162\n"
      "v_mfma_f32_16x16x32_bf16 v[148:151], v[80:83], v[88:91], v[148:151]\n"
      "ds_read_b128 v[210:213], v14\n"
      "v_readlane_b32 s45, v37, s43\n"
      "v_mfma_f32_16x16x32_bf16 v[174:177], v[214:217], v[104:107], 0\n"
      "ds_read_b128 v[184:187], v14 offset:64\n"
      "v_mul_f32 v188, v128, v188\n"
      "v_mfma_f32_16x16x32_bf16 v[174:177], v[84:87], v[88:91], v[174:177]\n"
      "v_mul_f32 v189, v128, v189\n"
      "v_cvt_pk_bf16_f32 v188, v188, v189\n"
      "v_mfma_f32_16x16x32_bf16 v[144:147], a[228:231], v[100:103], v[144:147]\n"
      "v_mul_f32 v190, v128, v190\n"
      "v_mul_f32 v191, v128, v191\n"
      "v_mfma_f32_16x16x32_bf16 v[144:147], a[232:235], v[152:155], v[144:147]\n"
      "v_cvt_pk_bf16_f32 v189, v190, v191\n"
      "v_mul_f32 v148, v128, v148\n"
      "v_mfma_f32_16x16x32_bf16 v[144:147], a[236:239], v[136:139], v[144:147]\n"
      "v_mul_f32 v149, v128, v149\n"
      "v_mul_f32_e64 v150, v128, v150\n"
      "v_mfma_f32_16x16x32_bf16 v[108:111], a[252:255], v[100:103], v[108:111]\n"
      "v_mul_f32 v151, v128, v151\n"
      "v_cvt_pk_bf16_f32 v190, v148, v149\n"
      "v_mfma_f32_16x16x32_bf16 v[108:111], v[224:227], v[152:155], v[108:111]\n"
      "s_waitcnt lgkmcnt(1)\n"
      "v_cvt_pk_bf16_f32 v191, v150, v151\n"
      "v_fma_f32 v150, v144, v144, s52\n"
      "v_mfma_f32_16x16x32_bf16 v[108:111], v[228:231], v[136:139], v[108:111]\n"
      "ds_write_b128 v3, v[188:191] offset:512\n"
      "v_mfma_f32_16x16x32_bf16 v[140:143], v[244:247], v[100:103], v[140:143]\n"
      "s_waitcnt lgkmcnt(1)\n"
      "v_fmac_f32 v150, v145, v145\n"
      "v_fmac_f32 v150, v146, v146\n"
      "v_mfma_f32_16x16x32_bf16 v[140:143], v[248:251], v[152:155], v[140:143]\n"
      "v_fmac_f32 v150, v147, v147\n"
      "v_mul_f32_e64 v174, v128, v174\n"
      "v_mfma_f32_16x16x32_bf16 v[140:143], v[252:255], v[136:139], v[140:143]\n"
      "v_fmac_f32 v150, v108, v108\n"
      "v_fmac_f32 v150, v109, v109\n"
      "v_mfma_f32_16x16x32_bf16 v[188:191], v[210:213], v[32:35], 0\n"
      "v_fmac_f32 v150, v110, v110\n"
      "v_fmac_f32 v150, v111, v111\n"
      "v_mfma_f32_16x16x32_bf16 v[188:191], v[184:187], v[32:35], v[188:191]\n"
      "v_mul_f32 v175, v128, v175\n"
      "v_cvt_pk_bf16_f32 v174, v174, v175\n"
      "v_fmac_f32 v150, v140, v140\n"
      "v_fmac_f32 v150, v141, v141\n"
      "v_fmac_f32 v150, v142, v142\n"
      "v_fmac_f32 v150, v143, v143\n"
      "v_mov_b32 v170, v150\n"
      "v_mul_f32 v176, v128, v176\n"
      "v_mul_f32 v177, v128, v177\n"
      "v_permlane16_swap_b32 v150, v170\n"
      "v_add_f32 v150, v150, v170\n"
      "v_cvt_pk_bf16_f32 v175, v176, v177\n"
      "ds_write_b64 v4, v[174:175] offset:512\n"
      "v_permlane32_swap_b32 v161, v150\n"
      "v_add_f32 v161, v161, v150\n"
      "v_readlane_b32 s46, v37, s42\n"
      "v_or3_b32 v202, v202, v203, v204\n"
      "ds_write_b32 v31, v161\n"
      "v_or_b32 v202, v202, v205\n"
      "v_cmp_ne_u32 s[56:57], 0, v202\n"
      "v_readlane_b32 s47, v38, s43\n"
      "v_add_u32 v12, 32, v12\n"
      "s_or_b32 s95, s45, s46\n"
      "s_or_b32 s95, s95, s48\n"
      "s_cmp_ge_u32 s95, 0x40000000\n"
      "s_cselect_b32 s94, -1, -2\n"
      "s_cmp_gt_u32 s42, s44\n"
      "s_cselect_b32 s95, s94, s95\n"
      "s_cmp_ge_u32 s95, 0x40000000\n"
      "s_cselect_b64 vcc, -1, s[56:57]\n"
      "s_and_b32 s47, s47, -4\n"
      "s_add_u32 s42, s42, 1\n"
      "s_add_u32 s43, s43, 1\n"
      "s_waitcnt lgkmcnt(0)\n"
      "s_barrier\n"
      "s_cbranch_vccz L_loop1_%=\n"
      "s_branch L_disp1_%=\n"
      "s_nop 7\n"
      "s_nop 7\n"
      "L_max1_%=:\n"
      "v_mfma_f32_16x16x32_bf16 v[202:205], v[210:213], v[194:197], 0\n"
      "ds_read_b128 v[98:101], v0 offset:18432\n"
      "v_mfma_f32_16x16x32_bf16 v[202:205], v[184:187], v[76:79], v[202:205]\n"
      "s_add_u32 m0, s64, 0\n"
      "ds_read_b128 v[196:199], v9\n"
      "v_mfma_f32_16x16x32_bf16 v[102:105], v[210:213], v[132:135], 0\n"
      "buffer_load_dwordx4 v16, s[16:19], s45 offen  lds\n"
      "v_mfma_f32_16x16x32_bf16 v[102:105], v[184:187], v[80:83], v[102:105]\n"
      "ds_read_b128 v[148:151], v0 offset:24576\n"
      "ds_read_b128 v[170:173], v1 offset:18432\n"
      "v_mfma_f32_16x16x32_bf16 v[192:195], v[210:213], v[214:217], 0\n"
      "ds_read_b128 v[210:213], v1 offset:24576\n"
      "v_mfma_f32_16x16x32_bf16 v[192:195], v[184:187], v[84:87], v[192:195]\n"
      "s_add_u32 m0, s64, 4096\n"
      "v_mov_b32_dpp v188, v188 quad_perm:[0,0,0,0] row_mask:0xf bank_mask:0xf\n"
      "s_waitcnt lgkmcnt(4)\n"
      "v_mfma_f32_16x16x32_bf16 v[184:187], a[48:51], v[98:101], 0\n"
      "buffer_load_dwordx4 v17, s[16:19], s45 offen  lds\n"
      "v_mfma_f32_16x16x32_bf16 v[158:161], a[120:123], v[98:101], 0\n"
      "s_add_u32 m0, s64, 8192\n"
      "v_rcp_f32 v188, v188\n"
      "v_mfma_f32_16x16x32_bf16 v[162:165], a[24:27], v[98:101], 0\n"
      "ds_read_b128 v[152:155], v0 offset:30720\n"
      "v_cndmask_b32 v202, v202, v102, s[58:59]\n"
      "v_mfma_f32_16x16x32_bf16 v[120:123], a[96:99], v[98:101], 0\n"
      "s_waitcnt lgkmcnt(3)\n"
      "v_add_f32 v198, v198, v199\n"
      "v_add_f32 v196, v196, v197\n"
      "v_mfma_f32_16x16x32_bf16 v[102:105], a[72:75], v[98:101], 0\n"
      "v_add_f32 v196, v196, v198\n"
      "v_cndmask_b32 v202, v202, v192, s[60:61]\n"
      "v_mfma_f32_16x16x32_bf16 v[190:193], a[0:3], v[98:101], 0\n"
      "s_waitcnt lgkmcnt(1)\n"
      "v_rsq_f32 v196, v196\n"
      "v_mfma_f32_16x16x32_bf16 v[190:193], a[4:7], v[170:173], v[190:193]\n"
      "buffer_load_dwordx4 v18, s[16:19], s45 offen  lds\n"
      "v_mfma_f32_16x16x32_bf16 v[190:193], a[8:11], v[148:151], v[190:193]\n"
      "v_mul_f32_e64 v202, v188, v202\n"
      "v_mul_f32 v196, 0x415db3d7, v196\n"
      "v_mfma_f32_16x16x32_bf16 v[162:165], a[28:31], v[170:173], v[162:165]\n"
      "s_add_u32 m0, s64, 12288\n"
      "v_mov_b32 v198, v196\n"
      "v_cvt_pk_bf16_f32 v202, v202, v202\n"
      "v_mfma_f32_16x16x32_bf16 v[162:165], a[32:35], v[148:151], v[162:165]\n"
      "v_permlane32_swap_b32 v196, v198\n"
      "v_mfma_f32_16x16x32_bf16 v[184:187], a[52:55], v[170:173], v[184:187]\n"
      "buffer_load_dwordx4 v19, s[16:19], s45 offen  lds\n"
      "v_mfma_f32_16x16x32_bf16 v[184:187], a[56:59], v[148:151], v[184:187]\n"
      "s_add_u32 m0, s65, 16384\n"
      "v_mov_b32 v197, v196\n"
      "v_mul_f32 v143, v198, v143\n"
      "v_mfma_f32_16x16x32_bf16 v[102:105], a[76:79], v[170:173], v[102:105]\n"
      "v_permlane16_swap_b32 v196, v197\n"
      "v_mfma_f32_16x16x32_bf16 v[102:105], a[80:83], v[148:151], v[102:105]\n"
      "v_mul_f32 v117, v196, v117\n"
      "v_mul_f32 v180, v197, v180\n"
      "v_mul_f32 v209, v196, v209\n"
      "v_mfma_f32_16x16x32_bf16 v[120:123], a[100:103], v[170:173], v[120:123]\n"
      "ds_read_b128 v[128:131], v1 offset:30720\n"
      "v_mul_f32_e64 v181, v197, v181\n"
      "v_mfma_f32_16x16x32_bf16 v[120:123], a[104:107], v[148:151], v[120:123]\n"
      "buffer_load_dwordx4 v15, s[16:19], s45 offen  lds\n"
      "v_mul_f32 v114, v196, v114\n"
      "v_mfma_f32_16x16x32_bf16 v[120:123], a[108:111], v[210:213], v[120:123]\n"
      "v_mul_f32 v168, v197, v168\n"
      "v_mul_f32 v115, v196, v115\n"
      "v_mul_f32 v221, v196, v221\n"
      "v_mfma_f32_16x16x32_bf16 v[158:161], a[124:127], v[170:173], v[158:161]\n"
      "s_waitcnt lgkmcnt(1)\n"
      "v_cvt_pk_bf16_f32 v114, v114, v115\n"
      "v_mul_f32 v208, v196, v208\n"
      "v_mfma_f32_16x16x32_bf16 v[158:161], a[128:131], v[148:151], v[158:161]\n"
      "ds_write_b16 v12, v202\n"
      "v_mfma_f32_16x16x32_bf16 v[162:165], a[36:39], v[210:213], v[162:165]\n"
      "buffer_load_dwordx4 v[174:177], v36, s[28:31], s47 offen\n"
      "v_mfma_f32_16x16x32_bf16 v[162:165], a[40:43], v[152:155], v[162:165]\n"
      "s_waitcnt lgkmcnt(1)\n"
      "v_mul_f32 v167, v197, v167\n"
      "v_mul_f32 v220, v196, v220\n"
      "v_mul_f32 v166, v197, v166\n"
      "v_mul_f32_e64 v116, v196, v116\n"
      "v_mfma_f32_16x16x32_bf16 v[162:165], a[44:47], v[128:131], v[162:165]\n"
      "v_mul_f32 v207, v196, v207\n"
      "v_cvt_pk_bf16_f32 v115, v116, v117\n"
      "v_mul_f32 v206, v196, v206\n"
      "v_mfma_f32_16x16x32_bf16 v[190:193], a[12:15], v[210:213], v[190:193]\n"
      "v_mul_f32 v147, v198, v147\n"
      "v_cvt_pk_bf16_f32 v220, v220, v221\n"
      "v_mfma_f32_16x16x32_bf16 v[190:193], a[16:19], v[152:155], v[190:193]\n"
      "v_mul_f32 v222, v196, v222\n"
      "v_cvt_pk_bf16_f32 v180, v180, v181\n"
      "v_mfma_f32_16x16x32_bf16 v[190:193], a[20:23], v[128:131], v[190:193]\n"
      "buffer_load_dword v85, v29, s[28:31], s47 offen\n"
      "v_mfma_f32_16x16x32_bf16 v[184:187], a[60:63], v[210:213], v[184:187]\n"
      "ds_read_b128 v[136:139], v1 offset:32768\n"
      "v_cvt_pk_bf16_f32 v116, v206, v207\n"
      "v_mul_f32 v126, v197, v126\n"
      "v_mul_f32 v146, v198, v146\n"
      "v_mfma_f32_16x16x32_bf16 v[184:187], a[64:67], v[152:155], v[184:187]\n"
      "v_mul_f32_e64 v124, v197, v124\n"
      "v_cvt_pk_bf16_f32 v117, v208, v209\n"
      "v_mfma_f32_16x16x32_bf16 v[184:187], a[68:71], v[128:131], v[184:187]\n"
      "ds_write_b128 v5, v[114:117] offset:18432\n"
      "v_mul_f32_e64 v182, v197, v182\n"
      "v_mfma_f32_16x16x32_bf16 v[102:105], a[84:87], v[210:213], v[102:105]\n"
      "v_mul_f32 v144, v198, v144\n"
      "v_mul_f32 v140, v198, v140\n"
      "v_mfma_f32_16x16x32_bf16 v[102:105], a[88:91], v[152:155], v[102:105]\n"
      "v_mul_f32 v127, v197, v127\n"
      "v_mul_f32 v223, v196, v223\n"
      "v_mfma_f32_16x16x32_bf16 v[102:105], a[92:95], v[128:131], v[102:105]\n"
      "v_mul_f32 v183, v197, v183\n"
      "v_mul_f32 v125, v197, v125\n"
      "v_mfma_f32_16x16x32_bf16 v[120:123], a[112:115], v[152:155], v[120:123]\n"
      "v_cvt_pk_bf16_f32 v221, v222, v223\n"
      "v_cvt_pk_bf16_f32 v181, v182, v183\n"
      "v_mfma_f32_16x16x32_bf16 v[120:123], a[116:119], v[128:131], v[120:123]\n"
      "ds_write_b64 v6, v[220:221] offset:18432\n"
      "v_mfma_f32_16x16x32_bf16 v[158:161], a[132:135], v[210:213], v[158:161]\n"
      "v_fma_f32 v199, v190, v190, s51\n"
      "v_cvt_pk_bf16_f32 v182, v124, v125\n"
      "v_mfma_f32_16x16x32_bf16 v[158:161], a[136:139], v[152:155], v[158:161]\n"
      "v_mul_f32_e64 v145, v198, v145\n"
      "v_cvt_pk_bf16_f32 v183, v126, v127\n"
      "v_mfma_f32_16x16x32_bf16 v[158:161], a[140:143], v[128:131], v[158:161]\n"
      "ds_read_b128 v[114:117], v1 offset:26624\n"
      "v_mul_f32_e64 v169, v197, v169\n"
      "v_mfma_f32_16x16x32_bf16 v[194:197], v[98:101], a[144:147], 0\n"
      "ds_write_b128 v5, v[180:183] offset:24576\n"
      "v_mfma_f32_16x16x32_bf16 v[194:197], v[170:173], a[148:151], v[194:197]\n"
      "v_cvt_pk_bf16_f32 v166, v166, v167\n"
      "v_fma_f32 v118, v102, v102, s51\n"
      "v_mfma_f32_16x16x32_bf16 v[194:197], v[148:151], a[152:155], v[194:197]\n"
      "v_cvt_pk_bf16_f32 v144, v144, v145\n"
      "v_cvt_pk_bf16_f32 v167, v168, v169\n"
      "v_mfma_f32_16x16x32_bf16 v[194:197], v[210:213], a[156:159], v[194:197]\n"
      "ds_write_b64 v6, v[166:167] offset:24576\n"
      "v_mfma_f32_16x16x32_bf16 v[194:197], v[152:155], a[160:163], v[194:197]\n"
      "v_mul_f32 v110, v198, v110\n"
      "v_mul_f32 v108, v198, v108\n"
      "v_mfma_f32_16x16x32_bf16 v[194:197], v[128:131], a[164:167], v[194:197]\n"
      "v_mul_f32 v141, v198, v141\n"
      "v_fmac_f32 v199, v191, v191\n"
      "v_mfma_f32_16x16x32_bf16 v[132:135], v[98:101], a[168:171], 0\n"
      "v_fmac_f32 v199, v192, v192\n"
      "v_mul_f32 v111, v198, v111\n"
      "v_mfma_f32_16x16x32_bf16 v[132:135], v[170:173], a[172:175], v[132:135]\n"
      "v_fmac_f32 v118, v103, v103\n"
      "v_mul_f32 v142, v198, v142\n"
      "v_mfma_f32_16x16x32_bf16 v[132:135], v[148:151], a[176:179], v[132:135]\n"
      "v_cvt_pk_bf16_f32 v145, v146, v147\n"
      "v_mul_f32 v109, v198, v109\n"
      "v_mfma_f32_16x16x32_bf16 v[132:135], v[210:213], a[180:183], v[132:135]\n"
      "v_fmac_f32 v199, v193, v193\n"
      "v_cvt_pk_bf16_f32 v140, v140, v141\n"
      "v_mfma_f32_16x16x32_bf16 v[132:135], v[152:155], a[184:187], v[132:135]\n"
      "ds_read_b128 v[206:209], v0 offset:20480\n"
      "v_cvt_pk_bf16_f32 v147, v110, v111\n"
      "v_mfma_f32_16x16x32_bf16 v[132:135], v[128:131], a[188:191], v[132:135]\n"
      "ds_read_b128 v[220:223], v1 offset:20480\n"
      "v_fmac_f32_e64 v199, v162, v162\n"
      "v_mfma_f32_16x16x32_bf16 v[214:217], v[98:101], a[192:195], 0\n"
      "ds_read_b128 v[180:183], v0 offset:26624\n"
      "v_cvt_pk_bf16_f32 v146, v108, v109\n"
      "v_mfma_f32_16x16x32_bf16 v[214:217], v[170:173], a[196:199], v[214:217]\n"
      "ds_write_b128 v5, v[144:147] offset:30720\n"
      "v_mfma_f32_16x16x32_bf16 v[214:217], v[148:151], a[200:203], v[214:217]\n"
      "ds_read_b128 v[166:169], v0 offset:32768\n"
      "v_cvt_pk_bf16_f32 v141, v142, v143\n"
      "v_mfma_f32_16x16x32_bf16 v[214:217], v[210:213], a[204:207], v[214:217]\n"
      "ds_read_b128 v[124:127], v7 offset:8192\n"
      "v_fmac_f32_e64 v118, v104, v104\n"
      "v_mfma_f32_16x16x32_bf16 v[214:217], v[152:155], a[208:211], v[214:217]\n"
      "ds_write_b64 v6, v[140:141] offset:30720\n"
      "v_mfma_f32_16x16x32_bf16 v[214:217], v[128:131], a[212:215], v[214:217]\n"
      "s_waitcnt lgkmcnt(4)\n"
      "v_fmac_f32 v199, v163, v163\n"
      "v_cvt_pk_bf16_f32 v194, v194, v195\n"
      "v_mfma_f32_16x16x32_bf16 v[106:109], a[0:3], v[206:209], 0\n"
      "v_fmac_f32 v118, v105, v105\n"
      "v_fmac_f32 v118, v120, v120\n"
      "v_mfma_f32_16x16x32_bf16 v[106:109], a[4:7], v[220:223], v[106:109]\n"
      "ds_read_b128 v[144:147], v1 offset:28672\n"
      "v_fmac_f32 v118, v121, v121\n"
      "v_mfma_f32_16x16x32_bf16 v[106:109], a[8:11], v[180:183], v[106:109]\n"
      "v_cvt_pk_bf16_f32 v132, v132, v133\n"
      "v_fmac_f32 v118, v122, v122\n"
      "v_mfma_f32_16x16x32_bf16 v[106:109], a[12:15], v[114:117], v[106:109]\n"
      "s_waitcnt lgkmcnt(3)\n"
      "v_fmac_f32 v118, v123, v123\n"
      "v_fmac_f32_e64 v199, v164, v164\n"
      "v_mfma_f32_16x16x32_bf16 v[106:109], a[16:19], v[166:169], v[106:109]\n"
      "ds_read_b128 v[140:143], v0 offset:28672\n"
      "v_fmac_f32 v199, v165, v165\n"
      "v_mfma_f32_16x16x32_bf16 v[106:109], a[20:23], v[136:139], v[106:109]\n"
      "v_cvt_pk_bf16_f32 v195, v196, v197\n"
      "v_fmac_f32 v118, v158, v158\n"
      "v_mfma_f32_16x16x32_bf16 v[210:213], a[24:27], v[206:209], 0\n"
      "v_fmac_f32 v199, v184, v184\n"
      "v_fmac_f32 v199, v185, v185\n"
      "v_mfma_f32_16x16x32_bf16 v[210:213], a[28:31], v[220:223], v[210:213]\n"
      "v_fmac_f32 v199, v186, v186\n"
      "v_fmac_f32 v199, v187, v187\n"
      "v_fmac_f32 v118, v159, v159\n"
      "v_mfma_f32_16x16x32_bf16 v[210:213], a[32:35], v[180:183], v[210:213]\n"
      "v_fmac_f32 v118, v160, v160\n"
      "v_readlane_b32 s47, v38, s43\n"
      "v_mfma_f32_16x16x32_bf16 v[210:213], a[36:39], v[114:117], v[210:213]\n"
      "v_fmac_f32 v118, v161, v161\n"
      "v_fma_f32 v76, v106, v106, s51\n"
      "v_mfma_f32_16x16x32_bf16 v[210:213], a[40:43], v[166:169], v[210:213]\n"
      "v_permlane16_swap_b32 v199, v118\n"
      "v_mfma_f32_16x16x32_bf16 v[210:213], a[44:47], v[136:139], v[210:213]\n"
      "v_add_f32 v199, v199, v118\n"
      "v_mov_b32 v118, v199\n"
      "v_mfma_f32_16x16x32_bf16 v[202:205], a[48:51], v[206:209], 0\n"
      "s_nop 0\n"
      "v_permlane32_swap_b32 v199, v118\n"
      "v_mfma_f32_16x16x32_bf16 v[202:205], a[52:55], v[220:223], v[202:205]\n"
      "v_add_f32 v199, v199, v118\n"
      "v_rsq_f32 v199, v199\n"
      "v_mfma_f32_16x16x32_bf16 v[202:205], a[56:59], v[180:183], v[202:205]\n"
      "v_mov_b32 v118, v199\n"
      "v_fmac_f32 v76, v107, v107\n"
      "v_mfma_f32_16x16x32_bf16 v[202:205], a[60:63], v[114:117], v[202:205]\n"
      "v_permlane16_swap_b32 v199, v118\n"
      "v_mfma_f32_16x16x32_bf16 v[202:205], a[64:67], v[166:169], v[202:205]\n"
      "v_mul_f32 v158, v118, v158\n"
      "v_mul_f32 v199, s50, v199\n"
      "v_mfma_f32_16x16x32_bf16 v[202:205], a[68:71], v[136:139], v[202:205]\n"
      "v_fmac_f32 v76, v108, v108\n"
      "v_mul_f32 v193, v199, v193\n"
      "v_mfma_f32_16x16x32_bf16 v[128:131], a[72:75], v[206:209], 0\n"
      "v_fmac_f32 v76, v109, v109\n"
      "v_mul_f32 v185, v199, v185\n"
      "v_mfma_f32_16x16x32_bf16 v[128:131], a[76:79], v[220:223], v[128:131]\n"
      "v_mul_f32 v102, v118, v102\n"
      "v_mul_f32 v105, v118, v105\n"
      "v_mfma_f32_16x16x32_bf16 v[128:131], a[80:83], v[180:183], v[128:131]\n"
      "v_mul_f32 v120, v118, v120\n"
      "v_cvt_pk_bf16_f32 v133, v134, v135\n"
      "v_mfma_f32_16x16x32_bf16 v[128:131], a[84:87], v[114:117], v[128:131]\n"
      "v_mul_f32 v121, v118, v121\n"
      "v_fmac_f32 v76, v210, v210\n"
      "v_mfma_f32_16x16x32_bf16 v[128:131], a[88:91], v[166:169], v[128:131]\n"
      "v_mul_f32 v103, v118, v103\n"
      "v_mul_f32 v191, v199, v191\n"
      "v_mfma_f32_16x16x32_bf16 v[128:131], a[92:95], v[136:139], v[128:131]\n"
      "v_fmac_f32 v76, v211, v211\n"
      "v_cvt_pk_bf16_f32 v102, v102, v103\n"
      "v_mfma_f32_16x16x32_bf16 v[150:153], a[96:99], v[206:209], 0\n"
      "v_fmac_f32 v76, v212, v212\n"
      "v_mul_f32_e64 v159, v118, v159\n"
      "v_mfma_f32_16x16x32_bf16 v[150:153], a[100:103], v[220:223], v[150:153]\n"
      "v_mul_f32 v161, v118, v161\n"
      "v_mul_f32 v190, v199, v190\n"
      "v_mfma_f32_16x16x32_bf16 v[150:153], a[104:107], v[180:183], v[150:153]\n"
      "v_readlane_b32 s45, v37, s43\n"
      "v_mul_f32 v165, v199, v165\n"
      "v_mfma_f32_16x16x32_bf16 v[150:153], a[108:111], v[114:117], v[150:153]\n"
      "ds_read_b128 v[96:99], v1 offset:22528\n"
      "v_mul_f32 v184, v199, v184\n"
      "v_mfma_f32_16x16x32_bf16 v[150:153], a[112:115], v[166:169], v[150:153]\n"
      "v_fma_f32 v111, v128, v128, s51\n"
      "v_fmac_f32 v76, v213, v213\n"
      "v_mfma_f32_16x16x32_bf16 v[150:153], a[116:119], v[136:139], v[150:153]\n"
      "v_cvt_pk_bf16_f32 v214, v214, v215\n"
      "v_mul_f32 v122, v118, v122\n"
      "v_mfma_f32_16x16x32_bf16 v[170:173], a[120:123], v[206:209], 0\n"
      "v_fmac_f32 v76, v202, v202\n"
      "v_mul_f32 v187, v199, v187\n"
      "v_mfma_f32_16x16x32_bf16 v[170:173], a[124:127], v[220:223], v[170:173]\n"
      "v_fmac_f32 v111, v129, v129\n"
      "v_fmac_f32 v111, v130, v130\n"
      "v_mfma_f32_16x16x32_bf16 v[170:173], a[128:131], v[180:183], v[170:173]\n"
      "v_mul_f32 v104, v118, v104\n"
      "v_cvt_pk_bf16_f32 v103, v104, v105\n"
      "v_mfma_f32_16x16x32_bf16 v[170:173], a[132:135], v[114:117], v[170:173]\n"
      "v_fmac_f32 v111, v131, v131\n"
      "v_mul_f32 v164, v199, v164\n"
      "v_cvt_pk_bf16_f32 v104, v120, v121\n"
      "v_mfma_f32_16x16x32_bf16 v[170:173], a[136:139], v[166:169], v[170:173]\n"
      "v_mul_f32 v162, v199, v162\n"
      "v_fmac_f32_e64 v111, v150, v150\n"
      "v_mfma_f32_16x16x32_bf16 v[170:173], a[140:143], v[136:139], v[170:173]\n"
      "v_cvt_pk_bf16_f32 v215, v216, v217\n"
      "v_fmac_f32_e64 v111, v151, v151\n"
      "v_mfma_f32_16x16x32_bf16 v[216:219], v[206:209], a[144:147], 0\n"
      "v_cvt_pk_bf16_f32 v190, v190, v191\n"
      "v_fmac_f32 v76, v203, v203\n"
      "v_mfma_f32_16x16x32_bf16 v[216:219], v[220:223], a[148:151], v[216:219]\n"
      "v_mul_f32 v160, v118, v160\n"
      "v_mul_f32_e64 v186, v199, v186\n"
      "v_mfma_f32_16x16x32_bf16 v[216:219], v[180:183], a[152:155], v[216:219]\n"
      "v_fmac_f32 v111, v152, v152\n"
      "v_cvt_pk_bf16_f32 v184, v184, v185\n"
      "v_mfma_f32_16x16x32_bf16 v[216:219], v[114:117], a[156:159], v[216:219]\n"
      "v_mul_f32 v163, v199, v163\n"
      "v_fmac_f32 v76, v204, v204\n"
      "v_mfma_f32_16x16x32_bf16 v[216:219], v[166:169], a[160:163], v[216:219]\n"
      "v_mul_f32 v192, v199, v192\n"
      "v_fmac_f32_e64 v111, v153, v153\n"
      "v_mfma_f32_16x16x32_bf16 v[216:219], v[136:139], a[164:167], v[216:219]\n"
      "v_cvt_pk_bf16_f32 v158, v158, v159\n"
      "v_fmac_f32 v111, v170, v170\n"
      "v_mfma_f32_16x16x32_bf16 v[198:201], v[206:209], a[168:171], 0\n"
      "v_fmac_f32 v76, v205, v205\n"
      "v_fmac_f32_e64 v111, v171, v171\n"
      "v_mfma_f32_16x16x32_bf16 v[198:201], v[220:223], a[172:175], v[198:201]\n"
      "ds_read_b128 v[154:157], v0 offset:34816\n"
      "v_cvt_pk_bf16_f32 v159, v160, v161\n"
      "v_mfma_f32_16x16x32_bf16 v[198:201], v[180:183], a[176:179], v[198:201]\n"
      "v_cvt_pk_bf16_f32 v191, v192, v193\n"
      "v_fmac_f32 v111, v172, v172\n"
      "v_mfma_f32_16x16x32_bf16 v[198:201], v[114:117], a[180:183], v[198:201]\n"
      "v_fmac_f32 v111, v173, v173\n"
      "v_cvt_pk_bf16_f32 v185, v186, v187\n"
      "v_mfma_f32_16x16x32_bf16 v[198:201], v[166:169], a[184:187], v[198:201]\n"
      "v_permlane16_swap_b32 v76, v111\n"
      "v_mfma_f32_16x16x32_bf16 v[198:201], v[136:139], a[188:191], v[198:201]\n"
      "ds_read_b128 v[186:189], v0 offset:22528\n"
      "v_mul_f32 v123, v118, v123\n"
      "v_mfma_f32_16x16x32_bf16 v[118:121], v[206:209], a[192:195], 0\n"
      "v_add_f32 v76, v76, v111\n"
      "v_mov_b32 v111, v76\n"
      "v_mfma_f32_16x16x32_bf16 v[118:121], v[220:223], a[196:199], v[118:121]\n"
      "s_nop 0\n"
      "v_permlane32_swap_b32 v76, v111\n"
      "v_mfma_f32_16x16x32_bf16 v[118:121], v[180:183], a[200:203], v[118:121]\n"
      "ds_read_b128 v[206:209], v1 offset:34816\n"
      "v_add_f32 v76, v76, v111\n"
      "v_mfma_f32_16x16x32_bf16 v[118:121], v[114:117], a[204:207], v[118:121]\n"
      "v_rsq_f32 v76, v76\n"
      "v_mfma_f32_16x16x32_bf16 v[118:121], v[166:169], a[208:211], v[118:121]\n"
      "v_mov_b32 v111, v76\n"
      "v_cvt_pk_bf16_f32 v192, v162, v163\n"
      "v_mfma_f32_16x16x32_bf16 v[118:121], v[136:139], a[212:215], v[118:121]\n"
      "s_waitcnt lgkmcnt(1)\n"
      "v_permlane16_swap_b32 v76, v111\n"
      "v_mfma_f32_16x16x32_bf16 v[136:139], a[0:3], v[186:189], 0\n"
      "v_mul_f32 v171, v111, v171\n"
      "v_cvt_pk_bf16_f32 v135, v200, v201\n"
      "v_mfma_f32_16x16x32_bf16 v[136:139], a[4:7], v[96:99], v[136:139]\n"
      "v_mul_f32 v130, v111, v130\n"
      "v_mul_f32 v152, v111, v152\n"
      "v_mfma_f32_16x16x32_bf16 v[136:139], a[8:11], v[140:143], v[136:139]\n"
      "v_mul_f32 v170, v111, v170\n"
      "v_mul_f32 v128, v111, v128\n"
      "v_mfma_f32_16x16x32_bf16 v[136:139], a[12:15], v[144:147], v[136:139]\n"
      "v_mul_f32 v129, v111, v129\n"
      "v_mul_f32_e64 v172, v111, v172\n"
      "v_mfma_f32_16x16x32_bf16 v[136:139], a[16:19], v[154:157], v[136:139]\n"
      "s_waitcnt lgkmcnt(0)\n"
      "v_cvt_pk_bf16_f32 v193, v164, v165\n"
      "v_cvt_pk_bf16_f32 v196, v216, v217\n"
      "v_mfma_f32_16x16x32_bf16 v[136:139], a[20:23], v[206:209], v[136:139]\n"
      "v_cvt_pk_bf16_f32 v105, v122, v123\n"
      "v_cvt_pk_bf16_f32 v170, v170, v171\n"
      "v_mfma_f32_16x16x32_bf16 v[180:183], a[24:27], v[186:189], 0\n"
      "v_cvt_pk_bf16_f32 v197, v218, v219\n"
      "v_mul_f32 v76, s50, v76\n"
      "v_mfma_f32_16x16x32_bf16 v[180:183], a[28:31], v[96:99], v[180:183]\n"
      "v_mul_f32 v204, v76, v204\n"
      "v_cvt_pk_bf16_f32 v128, v128, v129\n"
      "v_mfma_f32_16x16x32_bf16 v[180:183], a[32:35], v[140:143], v[180:183]\n"
      "v_mul_f32 v150, v111, v150\n"
      "v_mul_f32 v173, v111, v173\n"
      "v_mfma_f32_16x16x32_bf16 v[180:183], a[36:39], v[144:147], v[180:183]\n"
      "ds_read_b128 v[114:117], v7 offset:4096\n"
      "v_fma_f32 v80, v136, v136, s51\n"
      "v_mfma_f32_16x16x32_bf16 v[180:183], a[40:43], v[154:157], v[180:183]\n"
      "v_mul_f32 v205, v76, v205\n"
      "v_mul_f32 v107, v76, v107\n"
      "v_mfma_f32_16x16x32_bf16 v[180:183], a[44:47], v[206:209], v[180:183]\n"
      "ds_read_b128 v[220:223], v7\n"
      "v_mul_f32 v212, v76, v212\n"
      "v_mfma_f32_16x16x32_bf16 v[164:167], a[48:51], v[186:189], 0\n"
      "v_fmac_f32 v80, v137, v137\n"
      "v_mul_f32 v213, v76, v213\n"
      "v_mfma_f32_16x16x32_bf16 v[164:167], a[52:55], v[96:99], v[164:167]\n"
      "v_mul_f32 v108, v76, v108\n"
      "v_mul_f32 v202, v76, v202\n"
      "v_mfma_f32_16x16x32_bf16 v[164:167], a[56:59], v[140:143], v[164:167]\n"
      "v_fmac_f32 v80, v138, v138\n"
      "v_cvt_pk_bf16_f32 v134, v198, v199\n"
      "v_mfma_f32_16x16x32_bf16 v[164:167], a[60:63], v[144:147], v[164:167]\n"
      "v_fmac_f32 v80, v139, v139\n"
      "v_fmac_f32 v80, v180, v180\n"
      "v_mfma_f32_16x16x32_bf16 v[164:167], a[64:67], v[154:157], v[164:167]\n"
      "v_mul_f32 v203, v76, v203\n"
      "v_mul_f32 v109, v76, v109\n"
      "v_mfma_f32_16x16x32_bf16 v[164:167], a[68:71], v[206:209], v[164:167]\n"
      "v_fmac_f32 v80, v181, v181\n"
      "v_mul_f32 v210, v76, v210\n"
      "v_mfma_f32_16x16x32_bf16 v[198:201], a[72:75], v[186:189], 0\n"
      "s_waitcnt lgkmcnt(0)\n"
      "v_mul_f32 v131, v111, v131\n"
      "v_cvt_pk_bf16_f32 v129, v130, v131\n"
      "v_mfma_f32_16x16x32_bf16 v[198:201], a[76:79], v[96:99], v[198:201]\n"
      "buffer_store_dwordx4 v[220:223], v20, s[24:27], s46 offen sc1\n"
      "v_mfma_f32_16x16x32_bf16 v[198:201], a[80:83], v[140:143], v[198:201]\n"
      "v_mul_f32 v106, v76, v106\n"
      "v_fmac_f32 v80, v182, v182\n"
      "v_mfma_f32_16x16x32_bf16 v[198:201], a[84:87], v[144:147], v[198:201]\n"
      "v_mul_f32 v151, v111, v151\n"
      "v_mul_f32 v153, v111, v153\n"
      "v_mfma_f32_16x16x32_bf16 v[198:201], a[88:91], v[154:157], v[198:201]\n"
      "v_fmac_f32 v80, v183, v183\n"
      "v_cvt_pk_bf16_f32 v171, v172, v173\n"
      "v_mfma_f32_16x16x32_bf16 v[198:201], a[92:95], v[206:209], v[198:201]\n"
      "ds_read_b128 v[220:223], v7 offset:12288\n"
      "v_fmac_f32 v80, v164, v164\n"
      "v_mfma_f32_16x16x32_bf16 v[160:163], a[96:99], v[186:189], 0\n"
      "v_cvt_pk_bf16_f32 v217, v120, v121\n"
      "v_cvt_pk_bf16_f32 v130, v150, v151\n"
      "v_mfma_f32_16x16x32_bf16 v[160:163], a[100:103], v[96:99], v[160:163]\n"
      "ds_read_b128 v[110:113], v2 offset:3072\n"
      "v_mul_f32_e64 v211, v76, v211\n"
      "v_mfma_f32_16x16x32_bf16 v[160:163], a[104:107], v[140:143], v[160:163]\n"
      "v_cvt_pk_bf16_f32 v131, v152, v153\n"
      "v_fma_f32 v149, v198, v198, s51\n"
      "v_mfma_f32_16x16x32_bf16 v[160:163], a[108:111], v[144:147], v[160:163]\n"
      "buffer_store_dwordx4 v[114:117], v21, s[24:27], s46 offen sc1\n"
      "v_mfma_f32_16x16x32_bf16 v[160:163], a[112:115], v[154:157], v[160:163]\n"
      "v_fmac_f32 v80, v165, v165\n"
      "v_cvt_pk_bf16_f32 v216, v118, v119\n"
      "v_mfma_f32_16x16x32_bf16 v[160:163], a[116:119], v[206:209], v[160:163]\n"
      "v_fmac_f32 v149, v199, v199\n"
      "v_fmac_f32 v80, v166, v166\n"
      "v_mfma_f32_16x16x32_bf16 v[118:121], a[120:123], v[186:189], 0\n"
      "v_fmac_f32 v149, v200, v200\n"
      "v_fmac_f32 v149, v201, v201\n"
      "v_mfma_f32_16x16x32_bf16 v[118:121], a[124:127], v[96:99], v[118:121]\n"
      "s_and_b32 s47, s47, -4\n"
      "v_cvt_pk_bf16_f32 v106, v106, v107\n"
      "v_fmac_f32 v149, v160, v160\n"
      "v_mfma_f32_16x16x32_bf16 v[118:121], a[128:131], v[140:143], v[118:121]\n"
      "v_fmac_f32 v149, v161, v161\n"
      "v_fmac_f32_e64 v149, v162, v162\n"
      "v_mfma_f32_16x16x32_bf16 v[118:121], a[132:135], v[144:147], v[118:121]\n"
      "v_fmac_f32_e64 v149, v163, v163\n"
      "v_cvt_pk_bf16_f32 v202, v202, v203\n"
      "v_mfma_f32_16x16x32_bf16 v[118:121], a[136:139], v[154:157], v[118:121]\n"
      "v_cvt_pk_bf16_f32 v107, v108, v109\n"
      "v_fmac_f32 v80, v167, v167\n"
      "v_mfma_f32_16x16x32_bf16 v[118:121], a[140:143], v[206:209], v[118:121]\n"
      "buffer_store_dwordx4 v[124:127], v22, s[24:27], s46 offen sc1\n"
      "s_nop 1\n"
      "v_mfma_f32_16x16x32_bf16 v[124:127], v[186:189], a[144:147], 0\n"
      "ds_read_b128 v[114:117], v7 offset:16384\n"
      "v_cvt_pk_bf16_f32 v203, v204, v205\n"
      "v_mfma_f32_16x16x32_bf16 v[124:127], v[96:99], a[148:151], v[124:127]\n"
      "v_cvt_pk_bf16_f32 v108, v210, v211\n"
      "v_cvt_pk_bf16_f32 v109, v212, v213\n"
      "v_mfma_f32_16x16x32_bf16 v[124:127], v[140:143], a[152:155], v[124:127]\n"
      "v_fmac_f32 v149, v118, v118\n"
      "v_fmac_f32 v149, v119, v119\n"
      "v_mfma_f32_16x16x32_bf16 v[124:127], v[144:147], a[156:159], v[124:127]\n"
      "v_fmac_f32 v149, v120, v120\n"
      "v_fmac_f32 v149, v121, v121\n"
      "v_mfma_f32_16x16x32_bf16 v[124:127], v[154:157], a[160:163], v[124:127]\n"
      "s_nop 0\n"
      "v_permlane16_swap_b32 v80, v149\n"
      "v_mfma_f32_16x16x32_bf16 v[124:127], v[206:209], a[164:167], v[124:127]\n"
      "s_waitcnt lgkmcnt(1)\n"
      "v_add_f32 v80, v80, v149\n"
      "v_mov_b32 v149, v80\n"
      "v_mfma_f32_16x16x32_bf16 v[210:213], v[186:189], a[168:171], 0\n"
      "s_nop 0\n"
      "v_permlane32_swap_b32 v80, v149\n"
      "v_mfma_f32_16x16x32_bf16 v[210:213], v[96:99], a[172:175], v[210:213]\n"
      "buffer_store_dwordx4 v[220:223], v23, s[24:27], s46 offen sc1\n"
      "v_mfma_f32_16x16x32_bf16 v[210:213], v[140:143], a[176:179], v[210:213]\n"
      "v_add_f32 v80, v80, v149\n"
      "v_cvt_pk_bf16_f32 v76, v124, v125\n"
      "v_mfma_f32_16x16x32_bf16 v[210:213], v[144:147], a[180:183], v[210:213]\n"
      "v_rsq_f32_e64 v80, v80\n"
      "v_mfma_f32_16x16x32_bf16 v[210:213], v[154:157], a[184:187], v[210:213]\n"
      "ds_read_b128 v[122:125], v2\n"
      "v_mov_b32 v149, v80\n"
      "v_mfma_f32_16x16x32_bf16 v[150:153], v[186:189], a[192:195], 0\n"
      "s_nop 0\n"
      "v_permlane16_swap_b32_e64 v80, v149\n"
      "v_mfma_f32_16x16x32_bf16 v[210:213], v[206:209], a[188:191], v[210:213]\n"
      "v_mul_f32 v201, v149, v201\n"
      "v_mul_f32 v80, s50, v80\n"
      "v_mul_f32 v162, v149, v162\n"
      "v_mfma_f32_16x16x32_bf16 v[150:153], v[96:99], a[196:199], v[150:153]\n"
      "s_waitcnt lgkmcnt(1)\n"
      "v_mul_f32 v199, v149, v199\n"
      "v_mul_f32 v120, v149, v120\n"
      "v_mfma_f32_16x16x32_bf16 v[150:153], v[140:143], a[200:203], v[150:153]\n"
      "ds_read_b128 v[140:143], v10 offset:1200\n"
      "v_mul_f32_e64 v198, v149, v198\n"
      "v_mfma_f32_16x16x32_bf16 v[150:153], v[144:147], a[204:207], v[150:153]\n"
      "ds_read_b128 v[144:147], v2 offset:6144\n"
      "v_mul_f32_e64 v118, v149, v118\n"
      "v_mfma_f32_16x16x32_bf16 v[150:153], v[154:157], a[208:211], v[150:153]\n"
      "buffer_store_dwordx4 v[114:117], v25, s[24:27], s46 offen sc1\n"
      "v_cvt_pk_bf16_f32 v81, v212, v213\n"
      "v_mfma_f32_16x16x32_bf16 v[150:153], v[206:209], a[212:215], v[150:153]\n"
      "ds_read_b128 v[186:189], v2 offset:9216\n"
      "v_mul_f32 v160, v149, v160\n"
      "v_mul_f32 v119, v149, v119\n"
      "v_mfma_f32_16x16x32_bf16 v[154:157], v[128:131], v[190:193], v[44:47]\n"
      "s_waitcnt lgkmcnt(3)\n"
      "v_mul_f32 v139, v80, v139\n"
      "v_mul_f32_e64 v167, v80, v167\n"
      "v_mfma_f32_16x16x32_bf16 v[114:117], a[216:219], v[122:125], 0\n"
      "ds_read_b64 v[172:173], v10 offset:1192\n"
      "v_mul_f32 v164, v80, v164\n"
      "v_mfma_f32_16x16x32_bf16 v[114:117], a[220:223], v[110:113], v[114:117]\n"
      "s_waitcnt lgkmcnt(2)\n"
      "v_mul_f32 v163, v149, v163\n"
      "v_cvt_pk_bf16_f32 v77, v126, v127\n"
      "v_mfma_f32_16x16x32_bf16 v[114:117], a[224:227], v[144:147], v[114:117]\n"
      "v_mul_f32 v181, v80, v181\n"
      "v_mul_f32_e64 v180, v80, v180\n"
      "v_mfma_f32_16x16x32_bf16 v[206:209], a[240:243], v[122:125], 0\n"
      "v_mul_f32 v121, v149, v121\n"
      "v_cvt_pk_bf16_f32 v84, v150, v151\n"
      "v_mfma_f32_16x16x32_bf16 v[206:209], a[244:247], v[110:113], v[206:209]\n"
      "v_mul_f32 v182, v80, v182\n"
      "v_mul_f32 v200, v149, v200\n"
      "v_mfma_f32_16x16x32_bf16 v[206:209], a[248:251], v[144:147], v[206:209]\n"
      "v_mul_f32_e64 v161, v149, v161\n"
      "v_cvt_pk_bf16_f32 v198, v198, v199\n"
      "v_cvt_pk_bf16_f32 v199, v200, v201\n"
      "v_mfma_f32_16x16x32_bf16 v[220:223], v[232:235], v[122:125], 0\n"
      "v_cvt_pk_bf16_f32 v200, v160, v161\n"
      "s_waitcnt vmcnt(6)\n"
      "v_or3_b32 v174, v174, v175, v176\n"
      "v_mfma_f32_16x16x32_bf16 v[220:223], v[236:239], v[110:113], v[220:223]\n"
      "ds_read_b128 v[124:127], v2 offset:12288\n"
      "v_cvt_pk_bf16_f32 v201, v162, v163\n"
      "v_mfma_f32_16x16x32_bf16 v[220:223], v[240:243], v[144:147], v[220:223]\n"
      "ds_read_b128 v[144:147], v2 offset:15360\n"
      "v_cvt_pk_bf16_f32 v118, v118, v119\n"
      "v_mul_f32 v137, v80, v137\n"
      "v_mfma_f32_16x16x32_bf16 v[110:113], v[102:105], v[190:193], v[40:43]\n"
      "s_waitcnt lgkmcnt(2)\n"
      "v_mul_f32 v166, v80, v166\n"
      "v_mul_f32 v165, v80, v165\n"
      "v_cvt_pk_bf16_f32 v119, v120, v121\n"
      "v_mfma_f32_16x16x32_bf16 v[120:123], v[198:201], v[190:193], v[48:51]\n"
      "ds_read_b128 v[160:163], v2 offset:3328\n"
      "v_mul_f32_e64 v136, v80, v136\n"
      "v_mfma_f32_16x16x32_bf16 v[148:151], v[140:143], v[190:193], v[24:27]\n"
      "ds_read_u16 v78, v11 offset:1168\n"
      "v_cvt_pk_bf16_f32 v136, v136, v137\n"
      "v_mfma_f32_16x16x16_bf16 v[110:113], v[158:159], v[184:185], v[110:113]\n"
      "s_waitcnt vmcnt(5)\n"
      "v_or3_b32 v174, v174, v177, v85\n"
      "v_mul_f32 v138, v80, v138\n"
      "v_mul_f32_e64 v183, v80, v183\n"
      "v_mfma_f32_16x16x16_bf16 v[154:157], v[170:171], v[184:185], v[154:157]\n"
      "v_cvt_pk_bf16_f32 v137, v138, v139\n"
      "v_cvt_pk_bf16_f32 v80, v210, v211\n"
      "v_cvt_pk_bf16_f32 v85, v152, v153\n"
      "v_cvt_pk_bf16_f32 v164, v164, v165\n"
      "v_mfma_f32_16x16x16_bf16 v[120:123], v[118:119], v[184:185], v[120:123]\n"
      "v_readlane_b32 s46, v37, s42\n"
      "v_max3_f32 v177, v110, v111, v112\n"
      "v_cvt_pk_bf16_f32 v165, v166, v167\n"
      "v_mfma_f32_16x16x16_bf16 v[148:151], v[172:173], v[184:185], v[148:151]\n"
      "v_and_b32 v174, 0x7fff7fff, v174\n"
      "v_max3_f32 v177, v177, v156, v157\n"
      "v_cvt_pk_bf16_f32 v138, v180, v181\n"
      "v_mfma_f32_16x16x32_bf16 v[114:117], a[228:231], v[186:189], v[114:117]\n"
      "s_waitcnt lgkmcnt(1)\n"
      "v_max3_f32 v177, v177, v122, v123\n"
      "v_cvt_pk_bf16_f32 v139, v182, v183\n"
      "v_mfma_f32_16x16x32_bf16 v[114:117], a[232:235], v[124:127], v[114:117]\n"
      "ds_read_u16 v82, v11 offset:1200\n"
      "v_max3_f32 v211, v113, v154, v155\n"
      "v_mfma_f32_16x16x32_bf16 v[114:117], a[236:239], v[144:147], v[114:117]\n"
      "ds_read_b128 v[180:183], v9 offset:4608\n"
      "v_max3_f32 v211, v211, v120, v121\n"
      "v_max3_f32 v177, v177, v211, v148\n"
      "v_mov_b32 v191, v177\n"
      "v_mfma_f32_16x16x32_bf16 v[210:213], v[198:201], v[106:109], v[60:63]\n"
      "s_nop 0\n"
      "v_permlane16_swap_b32 v177, v191\n"
      "v_mfma_f32_16x16x32_bf16 v[206:209], a[252:255], v[186:189], v[206:209]\n"
      "v_max_f32 v177, v177, v191\n"
      "v_mov_b32 v191, v177\n"
      "v_mfma_f32_16x16x32_bf16 v[206:209], v[224:227], v[124:127], v[206:209]\n"
      "s_nop 0\n"
      "v_permlane32_swap_b32 v177, v191\n"
      "v_mfma_f32_16x16x32_bf16 v[206:209], v[228:231], v[144:147], v[206:209]\n"
      "v_max_f32 v177, v177, v191\n"
      "v_sub_f32 v148, v148, v177\n"
      "v_mfma_f32_16x16x32_bf16 v[190:193], v[102:105], v[106:109], v[52:55]\n"
      "v_sub_f32 v121, v121, v177\n"
      "v_sub_f32 v120, v120, v177\n"
      "v_mfma_f32_16x16x32_bf16 v[220:223], v[244:247], v[186:189], v[220:223]\n"
      "v_fma_f32 v178, v114, v114, s52\n"
      "v_sub_f32 v123, v123, v177\n"
      "v_sub_f32 v156, v156, v177\n"
      "v_mfma_f32_16x16x32_bf16 v[220:223], v[248:251], v[124:127], v[220:223]\n"
      "ds_read_b128 v[186:189], v2 offset:256\n"
      "v_fmac_f32 v178, v115, v115\n"
      "v_exp_f32 v121, v121\n"
      "v_sub_f32 v122, v122, v177\n"
      "v_mfma_f32_16x16x32_bf16 v[220:223], v[252:255], v[144:147], v[220:223]\n"
      "v_sub_f32 v154, v154, v177\n"
      "v_exp_f32_e64 v122, v122\n"
      "v_mfma_f32_16x16x32_bf16 v[144:147], v[128:131], v[106:109], v[56:59]\n"
      "ds_read_u16 v86, v11 offset:1232\n"
      "v_exp_f32 v120, v120\n"
      "v_mfma_f32_16x16x16_bf16 v[190:193], v[158:159], v[202:203], v[190:193]\n"
      "v_exp_f32 v154, v154\n"
      "v_exp_f32 v148, v148\n"
      "v_sub_f32 v110, v110, v177\n"
      "v_sub_f32 v112, v112, v177\n"
      "v_mfma_f32_16x16x16_bf16 v[144:147], v[170:171], v[202:203], v[144:147]\n"
      "v_exp_f32 v112, v112\n"
      "v_sub_f32 v111, v111, v177\n"
      "v_mfma_f32_16x16x32_bf16 v[150:153], v[140:143], v[106:109], v[26:29]\n"
      "s_waitcnt lgkmcnt(1)\n"
      "v_fmac_f32 v178, v116, v116\n"
      "v_fmac_f32 v178, v117, v117\n"
      "v_or3_b32 v180, v180, v181, v182\n"
      "v_sub_f32 v113, v113, v177\n"
      "v_mfma_f32_16x16x16_bf16 v[210:213], v[118:119], v[202:203], v[210:213]\n"
      "v_fmac_f32 v178, v206, v206\n"
      "v_exp_f32 v156, v156\n"
      "v_or_b32 v180, v180, v183\n"
      "v_exp_f32 v111, v111\n"
      "v_sub_f32 v155, v155, v177\n"
      "v_cmp_ne_u32 s[56:57], 0, v180\n"
      "v_mfma_f32_16x16x16_bf16 v[150:153], v[172:173], v[202:203], v[150:153]\n"
      "v_max3_f32 v202, v193, v144, v145\n"
      "v_fmac_f32 v178, v207, v207\n"
      "v_exp_f32 v110, v110\n"
      "v_max3_f32 v202, v202, v210, v211\n"
      "v_mfma_f32_16x16x32_bf16 v[180:183], a[216:219], v[186:189], 0\n"
      "v_max3_f32 v96, v190, v191, v192\n"
      "v_max3_f32 v96, v96, v146, v147\n"
      "v_exp_f32 v155, v155\n"
      "v_sub_f32 v157, v157, v177\n"
      "v_mfma_f32_16x16x32_bf16 v[180:183], a[220:223], v[160:163], v[180:183]\n"
      "v_exp_f32 v157, v157\n"
      "v_max3_f32 v96, v96, v212, v213\n"
      "v_exp_f32 v123, v123\n"
      "v_max3_f32 v96, v96, v202, v150\n"
      "v_mfma_f32_16x16x32_bf16 v[166:169], v[232:235], v[186:189], 0\n"
      "v_exp_f32 v113, v113\n"
      "v_cvt_pk_bf16_f32 v90, v148, 0\n"
      "v_cvt_pk_bf16_f32 v88, v120, v121\n"
      "v_cvt_pk_bf16_f32 v110, v110, v111\n"
      "v_mfma_f32_16x16x32_bf16 v[166:169], v[236:239], v[160:163], v[166:169]\n"
      "v_mov_b32 v152, v96\n"
      "v_cvt_pk_bf16_f32 v89, v122, v123\n"
      "v_cvt_pk_bf16_f32 v111, v112, v113\n"
      "v_cvt_pk_bf16_f32 v112, v154, v155\n"
      "v_mfma_f32_16x16x32_bf16 v[124:127], a[240:243], v[186:189], 0\n"
      "ds_read_b128 v[186:189], v2 offset:6400\n"
      "v_cvt_pk_bf16_f32 v113, v156, v157\n"
      "v_mfma_f32_16x16x32_bf16 v[124:127], a[244:247], v[160:163], v[124:127]\n"
      "v_permlane16_swap_b32 v96, v152\n"
      "v_mfma_f32_16x16x32_bf16 v[202:205], v[32:35], v[110:113], 0\n"
      "v_max_f32 v96, v96, v152\n"
      "v_mov_b32 v152, v96\n"
      "v_mfma_f32_16x16x32_bf16 v[202:205], v[32:35], v[88:91], v[202:205]\n"
      "s_or_b32 s95, s45, s46\n"
      "v_permlane32_swap_b32 v96, v152\n"
      "v_mfma_f32_16x16x32_bf16 v[106:109], v[194:197], v[110:113], 0\n"
      "ds_write_b32 v31, v174 offset:5632\n"
      "v_mfma_f32_16x16x32_bf16 v[106:109], v[76:79], v[88:91], v[106:109]\n"
      "v_max_f32 v96, v96, v152\n"
      "v_sub_f32 v210, v210, v96\n"
      "v_mfma_f32_16x16x32_bf16 v[152:155], v[132:135], v[110:113], 0\n"
      "s_or_b32 s95, s95, s48\n"
      "v_rcp_f32 v202, v202\n"
      "v_mfma_f32_16x16x32_bf16 v[152:155], v[80:83], v[88:91], v[152:155]\n"
      "v_sub_f32 v213, v213, v96\n"
      "v_sub_f32 v193, v193, v96\n"
      "v_mfma_f32_16x16x32_bf16 v[174:177], v[140:143], v[136:139], v[28:31]\n"
      "v_mul_f32 v107, v202, v107\n"
      "v_sub_f32 v190, v190, v96\n"
      "v_mfma_f32_16x16x32_bf16 v[160:163], v[214:217], v[110:113], 0\n"
      "s_waitcnt lgkmcnt(1)\n"
      "v_mul_f32 v106, v202, v106\n"
      "v_sub_f32 v192, v192, v96\n"
      "v_mfma_f32_16x16x32_bf16 v[160:163], v[84:87], v[88:91], v[160:163]\n"
      "ds_read_b128 v[110:113], v2 offset:9472\n"
      "v_mul_f32_e64 v154, v202, v154\n"
      "v_mfma_f32_16x16x16_bf16 v[174:177], v[172:173], v[164:165], v[174:177]\n"
      "v_sub_f32 v144, v144, v96\n"
      "v_sub_f32 v211, v211, v96\n"
      "v_mul_f32 v152, v202, v152\n"
      "v_mul_f32 v108, v202, v108\n"
      "v_mfma_f32_16x16x32_bf16 v[124:127], a[248:251], v[186:189], v[124:127]\n"
      "s_cmp_ge_u32 s95, 0x40000000\n"
      "s_cselect_b32 s94, -1, -2\n"
      "v_sub_f32 v191, v191, v96\n"
      "v_mul_f32 v155, v202, v155\n"
      "v_cvt_pk_bf16_f32 v106, v106, v107\n"
      "v_sub_f32 v147, v147, v96\n"
      "v_sub_f32 v212, v212, v96\n"
      "v_mul_f32 v109, v202, v109\n"
      "v_sub_f32 v145, v145, v96\n"
      "v_sub_f32 v150, v150, v96\n"
      "v_mul_f32 v161, v202, v161\n"
      "v_mul_f32_e64 v163, v202, v163\n"
      "v_cvt_pk_bf16_f32 v107, v108, v109\n"
      "v_cvt_pk_bf16_f32 v109, v154, v155\n"
      "v_mfma_f32_16x16x32_bf16 v[180:183], a[224:227], v[186:189], v[180:183]\n"
      "s_waitcnt lgkmcnt(0)\n"
      "v_exp_f32 v190, v190\n"
      "v_mfma_f32_16x16x32_bf16 v[166:169], v[240:243], v[186:189], v[166:169]\n"
      "ds_read_b128 v[186:189], v2 offset:12544\n"
      "v_mul_f32 v162, v202, v162\n"
      "v_mul_f32 v153, v202, v153\n"
      "v_exp_f32 v191, v191\n"
      "v_exp_f32 v147, v147\n"
      "v_cvt_pk_bf16_f32 v108, v152, v153\n"
      "v_sub_f32 v146, v146, v96\n"
      "v_exp_f32 v193, v193\n"
      "v_mfma_f32_16x16x32_bf16 v[166:169], v[244:247], v[110:113], v[166:169]\n"
      "v_exp_f32 v146, v146\n"
      "v_mul_f32 v160, v202, v160\n"
      "v_cvt_pk_bf16_f32 v160, v160, v161\n"
      "v_mfma_f32_16x16x32_bf16 v[202:205], v[102:105], v[136:139], v[64:67]\n"
      "ds_read_b128 v[120:123], v2 offset:15616\n"
      "v_cvt_pk_bf16_f32 v161, v162, v163\n"
      "v_exp_f32_e64 v150, v150\n"
      "v_mfma_f32_16x16x32_bf16 v[152:155], v[128:131], v[136:139], v[68:71]\n"
      "s_waitcnt lgkmcnt(1)\n"
      "v_exp_f32 v212, v212\n"
      "v_mfma_f32_16x16x32_bf16 v[166:169], v[248:251], v[186:189], v[166:169]\n"
      "ds_write_b128 v3, v[106:109] offset:18432\n"
      "v_mfma_f32_16x16x32_bf16 v[96:99], v[198:201], v[136:139], v[72:75]\n"
      "s_cmp_gt_u32 s42, s44\n"
      "s_cselect_b32 s95, s94, s95\n"
      "v_exp_f32 v213, v213\n"
      "v_mfma_f32_16x16x16_bf16 v[202:205], v[158:159], v[164:165], v[202:205]\n"
      "v_exp_f32 v211, v211\n"
      "v_exp_f32 v210, v210\n"
      "v_mfma_f32_16x16x16_bf16 v[152:155], v[170:171], v[164:165], v[152:155]\n"
      "v_cvt_pk_bf16_f32 v190, v190, v191\n"
      "v_exp_f32 v192, v192\n"
      "v_cvt_pk_bf16_f32 v92, v210, v211\n"
      "v_mfma_f32_16x16x16_bf16 v[96:99], v[118:119], v[164:165], v[96:99]\n"
      "v_exp_f32 v145, v145\n"
      "v_fmac_f32 v178, v208, v208\n"
      "v_mfma_f32_16x16x32_bf16 v[180:183], a[228:231], v[110:113], v[180:183]\n"
      "ds_read_b128 v[106:109], v10 offset:1168\n"
      "v_cvt_pk_bf16_f32 v191, v192, v193\n"
      "v_mfma_f32_16x16x32_bf16 v[180:183], a[232:235], v[186:189], v[180:183]\n"
      "s_waitcnt lgkmcnt(1)\n"
      "v_exp_f32 v144, v144\n"
      "v_mfma_f32_16x16x32_bf16 v[180:183], a[236:239], v[120:123], v[180:183]\n"
      "v_cvt_pk_bf16_f32 v94, v150, 0\n"
      "v_cvt_pk_bf16_f32 v193, v146, v147\n"
      "v_mfma_f32_16x16x32_bf16 v[166:169], v[252:255], v[120:123], v[166:169]\n"
      "v_cvt_pk_bf16_f32 v192, v144, v145\n"
      "v_max3_f32 v89, v202, v203, v204\n"
      "v_max3_f32 v89, v89, v154, v155\n"
      "v_mfma_f32_16x16x32_bf16 v[124:127], a[252:255], v[110:113], v[124:127]\n"
      "v_cvt_pk_bf16_f32 v93, v212, v213\n"
      "v_max3_f32 v163, v205, v152, v153\n"
      "v_mfma_f32_16x16x32_bf16 v[124:127], v[224:227], v[186:189], v[124:127]\n"
      "ds_read_b64 v[110:111], v10 offset:1184\n"
      "v_max3_f32 v89, v89, v98, v99\n"
      "v_mfma_f32_16x16x32_bf16 v[124:127], v[228:231], v[120:123], v[124:127]\n"
      "s_waitcnt lgkmcnt(1)\n"
      "v_max3_f32 v163, v163, v96, v97\n"
      "v_fma_f32 v177, v180, v180, s52\n"
      "v_mfma_f32_16x16x32_bf16 v[144:147], v[106:109], v[198:201], v[30:33]\n"
      "ds_read_b128 v[210:213], v2 offset:3584\n"
      "v_max3_f32 v89, v89, v163, v174\n"
      "v_mov_b32 v201, v89\n"
      "v_mfma_f32_16x16x32_bf16 v[186:189], v[32:35], v[190:193], 0\n"
      "s_nop 0\n"
      "v_permlane16_swap_b32 v89, v201\n"
      "v_fmac_f32_e64 v177, v181, v181\n"
      "v_mfma_f32_16x16x32_bf16 v[186:189], v[32:35], v[92:95], v[186:189]\n"
      "ds_write_b64 v4, v[160:161] offset:18432\n"
      "v_mfma_f32_16x16x32_bf16 v[160:163], v[106:109], v[102:105], v[30:33]\n"
      "v_max_f32 v89, v89, v201\n"
      "v_fmac_f32 v177, v182, v182\n"
      "v_mov_b32 v201, v89\n"
      "v_mfma_f32_16x16x32_bf16 v[102:105], v[194:197], v[190:193], 0\n"
      "v_fmac_f32 v177, v183, v183\n"
      "v_fmac_f32 v178, v209, v209\n"
      "v_mfma_f32_16x16x32_bf16 v[102:105], v[76:79], v[92:95], v[102:105]\n"
      "s_waitcnt lgkmcnt(1)\n"
      "v_rcp_f32 v186, v186\n"
      "v_mfma_f32_16x16x16_bf16 v[160:163], v[110:111], v[158:159], v[160:163]\n"
      "v_permlane32_swap_b32 v89, v201\n"
      "v_max_f32 v89, v89, v201\n"
      "v_mfma_f32_16x16x32_bf16 v[198:201], v[106:109], v[128:131], v[30:33]\n"
      "ds_read_b128 v[128:131], v2 offset:512\n"
      "v_sub_f32 v98, v98, v89\n"
      "v_sub_f32_e64 v155, v155, v89\n"
      "v_mfma_f32_16x16x16_bf16 v[144:147], v[110:111], v[118:119], v[144:147]\n"
      "v_sub_f32 v152, v152, v89\n"
      "v_sub_f32 v204, v204, v89\n"
      "v_exp_f32 v155, v155\n"
      "v_sub_f32 v154, v154, v89\n"
      "v_mfma_f32_16x16x32_bf16 v[120:123], v[132:135], v[190:193], 0\n"
      "v_exp_f32 v154, v154\n"
      "v_sub_f32 v202, v202, v89\n"
      "v_sub_f32 v205, v205, v89\n"
      "v_mfma_f32_16x16x32_bf16 v[120:123], v[80:83], v[92:95], v[120:123]\n"
      "ds_read_b128 v[136:139], v2 offset:9728\n"
      "v_fmac_f32 v177, v124, v124\n"
      "v_mfma_f32_16x16x32_bf16 v[156:159], v[214:217], v[190:193], 0\n"
      "ds_read_b128 v[188:191], v2 offset:12800\n"
      "v_sub_f32_e64 v203, v203, v89\n"
      "v_mfma_f32_16x16x32_bf16 v[156:159], v[84:87], v[92:95], v[156:159]\n"
      "ds_read_b128 v[148:151], v2 offset:6656\n"
      "v_mul_f32 v104, v186, v104\n"
      "v_mfma_f32_16x16x32_bf16 v[162:165], v[106:109], v[140:143], v[30:33]\n"
      "s_waitcnt lgkmcnt(3)\n"
      "v_sub_f32 v174, v174, v89\n"
      "v_sub_f32 v96, v96, v89\n"
      "v_mfma_f32_16x16x16_bf16 v[198:201], v[110:111], v[170:171], v[198:201]\n"
      "v_fmac_f32 v178, v220, v220\n"
      "v_sub_f32 v97, v97, v89\n"
      "v_mul_f32 v157, v186, v157\n"
      "v_mfma_f32_16x16x16_bf16 v[162:165], v[110:111], v[172:173], v[162:165]\n"
      "v_fmac_f32 v177, v125, v125\n"
      "v_exp_f32 v202, v202\n"
      "v_mfma_f32_16x16x32_bf16 v[108:111], a[240:243], v[128:131], 0\n"
      "v_exp_f32 v203, v203\n"
      "v_cndmask_b32 v160, v160, v198, s[58:59]\n"
      "v_fmac_f32 v177, v126, v126\n"
      "v_exp_f32 v205, v205\n"
      "v_mfma_f32_16x16x32_bf16 v[108:111], a[244:247], v[210:213], v[108:111]\n"
      "s_waitcnt lgkmcnt(0)\n"
      "v_cndmask_b32 v160, v160, v144, s[60:61]\n"
      "v_mul_f32 v102, v186, v102\n"
      "v_cndmask_b32 v160, v160, v162, s[62:63]\n"
      "v_fmac_f32 v177, v127, v127\n"
      "v_exp_f32 v152, v152\n"
      "v_mfma_f32_16x16x32_bf16 v[108:111], a[248:251], v[148:151], v[108:111]\n"
      "v_mul_f32 v105, v186, v105\n"
      "v_sub_f32 v153, v153, v89\n"
      "v_mul_f32 v158, v186, v158\n"
      "v_mul_f32 v156, v186, v156\n"
      "v_mul_f32 v121, v186, v121\n"
      "v_max_f32_dpp v187, v160, v160 row_ror:8 row_mask:0xf bank_mask:0xf\n"
      "v_mfma_f32_16x16x32_bf16 v[108:111], a[252:255], v[136:139], v[108:111]\n"
      "ds_read_b128 v[162:165], v2 offset:15872\n"
      "v_mul_f32 v159, v186, v159\n"
      "v_fmac_f32 v177, v166, v166\n"
      "v_exp_f32 v97, v97\n"
      "v_mul_f32 v103, v186, v103\n"
      "v_mfma_f32_16x16x32_bf16 v[144:147], a[216:219], v[128:131], 0\n"
      "v_fmac_f32 v178, v221, v221\n"
      "v_max_f32_dpp v187, v187, v187 row_ror:4 row_mask:0xf bank_mask:0xf\n"
      "v_mfma_f32_16x16x32_bf16 v[144:147], a[220:223], v[210:213], v[144:147]\n"
      "v_exp_f32 v153, v153\n"
      "v_sub_f32 v99, v99, v89\n"
      "v_cvt_pk_bf16_f32 v156, v156, v157\n"
      "v_fmac_f32_e64 v178, v222, v222\n"
      "v_mfma_f32_16x16x32_bf16 v[144:147], a[224:227], v[148:151], v[144:147]\n"
      "v_mul_f32 v123, v186, v123\n"
      "v_fmac_f32 v177, v167, v167\n"
      "v_exp_f32 v99, v99\n"
      "v_max_f32_dpp v187, v187, v187 row_ror:2 row_mask:0xf bank_mask:0xf\n"
      "v_fmac_f32 v178, v223, v223\n"
      "v_mfma_f32_16x16x32_bf16 v[144:147], a[228:231], v[136:139], v[144:147]\n"
      "v_exp_f32 v204, v204\n"
      "v_max_f32_dpp v187, v187, v187 row_ror:1 row_mask:0xf bank_mask:0xf\n"
      "v_mfma_f32_16x16x32_bf16 v[144:147], a[232:235], v[188:191], v[144:147]\n"
      "s_waitcnt lgkmcnt(0)\n"
      "v_exp_f32 v174, v174\n"
      "v_mfma_f32_16x16x32_bf16 v[144:147], a[236:239], v[162:165], v[144:147]\n"
      "v_mul_f32 v122, v186, v122\n"
      "v_exp_f32_e64 v98, v98\n"
      "v_cvt_pk_bf16_f32 v102, v102, v103\n"
      "v_max_f32_dpp v187, v187, v187 row_bcast:15 row_mask:0xa bank_mask:0xf\n"
      "v_mfma_f32_16x16x32_bf16 v[108:111], v[224:227], v[188:191], v[108:111]\n"
      "v_fmac_f32 v177, v168, v168\n"
      "v_mul_f32 v120, v186, v120\n"
      "v_cvt_pk_bf16_f32 v103, v104, v105\n"
      "v_mfma_f32_16x16x32_bf16 v[108:111], v[228:231], v[162:165], v[108:111]\n"
      "v_max_f32_dpp v187, v187, v187 row_bcast:31 row_mask:0xc bank_mask:0xf\n"
      "v_cvt_pk_bf16_f32 v202, v202, v203\n"
      "v_mfma_f32_16x16x32_bf16 v[140:143], v[232:235], v[128:131], 0\n"
      "v_exp_f32_e64 v96, v96\n"
      "v_cvt_pk_bf16_f32 v89, v98, v99\n"
      "v_mfma_f32_16x16x32_bf16 v[140:143], v[236:239], v[210:213], v[140:143]\n"
      "v_cvt_pk_bf16_f32 v88, v96, v97\n"
      "v_fma_f32 v129, v144, v144, s52\n"
      "v_cvt_pk_bf16_f32 v203, v204, v205\n"
      "v_mfma_f32_16x16x32_bf16 v[140:143], v[240:243], v[148:151], v[140:143]\n"
      "v_cvt_pk_bf16_f32 v104, v120, v121\n"
      "v_fmac_f32 v129, v145, v145\n"
      "v_cvt_pk_bf16_f32 v105, v122, v123\n"
      "v_cvt_pk_bf16_f32 v204, v152, v153\n"
      "v_mfma_f32_16x16x32_bf16 v[140:143], v[244:247], v[136:139], v[140:143]\n"
      "s_add_u32 s43, s43, 1\n"
      "v_cvt_pk_bf16_f32 v90, v174, 0\n"
      "v_readlane_b32 s72, v187, 63\n"
      "v_mfma_f32_16x16x32_bf16 v[140:143], v[248:251], v[188:191], v[140:143]\n"
      "s_nop 0\n"
      "v_subrev_f32 v160, s72, v160\n"
      "v_fmac_f32 v129, v146, v146\n"
      "v_fmac_f32 v129, v147, v147\n"
      "v_exp_f32 v160, v160\n"
      "v_mfma_f32_16x16x32_bf16 v[140:143], v[252:255], v[162:165], v[140:143]\n"
      "v_cvt_pk_bf16_f32 v205, v154, v155\n"
      "v_fmac_f32 v177, v169, v169\n"
      "v_cvt_pk_bf16_f32 v160, v160, v160\n"
      "v_mfma_f32_16x16x32_bf16 v[152:155], v[32:35], v[202:205], 0\n"
      "ds_write_b16 v13, v160\n"
      "v_fmac_f32 v129, v108, v108\n"
      "v_fmac_f32 v129, v109, v109\n"
      "v_mfma_f32_16x16x32_bf16 v[152:155], v[32:35], v[88:91], v[152:155]\n"
      "ds_read_b128 v[210:213], v14\n"
      "v_fmac_f32 v129, v110, v110\n"
      "v_fmac_f32 v129, v111, v111\n"
      "v_fmac_f32_e64 v129, v140, v140\n"
      "v_mfma_f32_16x16x32_bf16 v[184:187], v[194:197], v[202:205], 0\n"
      "v_fmac_f32 v129, v141, v141\n"
      "v_fmac_f32 v129, v142, v142\n"
      "v_fmac_f32 v129, v143, v143\n"
      "v_permlane16_swap_b32 v178, v177\n"
      "v_cvt_pk_bf16_f32 v157, v158, v159\n"
      "v_mfma_f32_16x16x32_bf16 v[184:187], v[76:79], v[88:91], v[184:187]\n"
      "ds_write_b128 v3, v[102:105] offset:18688\n"
      "v_rcp_f32 v152, v152\n"
      "v_add_f32 v178, v178, v177\n"
      "v_mov_b32_e64 v177, v129\n"
      "v_mfma_f32_16x16x32_bf16 v[188:191], v[132:135], v[202:205], 0\n"
      "s_nop 0\n"
      "v_permlane16_swap_b32 v129, v177\n"
      "v_mfma_f32_16x16x32_bf16 v[188:191], v[80:83], v[88:91], v[188:191]\n"
      "ds_write_b64 v4, v[156:157] offset:18688\n"
      "v_add_f32 v129, v129, v177\n"
      "v_mul_f32 v184, v152, v184\n"
      "v_mul_f32 v186, v152, v186\n"
      "v_permlane32_swap_b32 v178, v129\n"
      "v_mfma_f32_16x16x32_bf16 v[102:105], v[214:217], v[202:205], 0\n"
      "s_waitcnt lgkmcnt(2)\n"
      "v_mul_f32 v185, v152, v185\n"
      "v_add_f32 v178, v178, v129\n"
      "v_mul_f32 v187, v152, v187\n"
      "v_cvt_pk_bf16_f32 v184, v184, v185\n"
      "v_mul_f32 v189, v152, v189\n"
      "v_mul_f32 v188, v152, v188\n"
      "v_mfma_f32_16x16x32_bf16 v[102:105], v[84:87], v[88:91], v[102:105]\n"
      "ds_write_b32 v31, v178 offset:1024\n"
      "v_cvt_pk_bf16_f32 v185, v186, v187\n"
      "v_mul_f32 v190, v152, v190\n"
      "v_cvt_pk_bf16_f32 v186, v188, v189\n"
      "v_mul_f32 v191, v152, v191\n"
      "v_cvt_pk_bf16_f32 v187, v190, v191\n"
      "v_mfma_f32_16x16x32_bf16 v[188:191], v[210:213], v[32:35], 0\n"
      "ds_write_b128 v3, v[184:187] offset:18944\n"
      "ds_read_b128 v[184:187], v14 offset:64\n"
      "v_mul_f32_e64 v103, v152, v103\n"
      "v_mul_f32_e64 v102, v152, v102\n"
      "v_mul_f32_e64 v105, v152, v105\n"
      "v_cvt_pk_bf16_f32 v102, v102, v103\n"
      "v_mul_f32 v104, v152, v104\n"
      "v_cvt_pk_bf16_f32 v103, v104, v105\n"
      "s_add_u32 s42, s42, 1\n"
      "ds_write_b64 v4, v[102:103] offset:18944\n"
      "v_add_u32 v10, 2336, v10\n"
      "s_waitcnt lgkmcnt(1)\n"
      "v_mfma_f32_16x16x32_bf16 v[188:191], v[184:187], v[32:35], v[188:191]\n"
      "v_add_u32 v11, 2336, v11\n"
      "s_cmp_ge_u32 s95, 0x40000000\n"
      "s_cselect_b64 vcc, -1, s[56:57]\n"
      "s_waitcnt lgkmcnt(0)\n"
      "s_barrier\n"
      "s_cbranch_vccnz L_disp0_%=\n"
      "L_max0_%=:\n"
      "v_mfma_f32_16x16x32_bf16 v[202:205], v[210:213], v[194:197], 0\n"
      "ds_read_b128 v[162:165], v0\n"
      "ds_read_b128 v[156:159], v9 offset:1024\n"
      "v_mfma_f32_16x16x32_bf16 v[202:205], v[184:187], v[76:79], v[202:205]\n"
      "s_add_u32 m0, s64, 18432\n"
      "ds_read_b128 v[152:155], v1\n"
      "v_mfma_f32_16x16x32_bf16 v[102:105], v[210:213], v[132:135], 0\n"
      "buffer_load_dwordx4 v16, s[16:19], s45 offen  lds\n"
      "v_mfma_f32_16x16x32_bf16 v[192:195], v[210:213], v[214:217], 0\n"
      "s_add_u32 m0, s64, 22528\n"
      "ds_read_b128 v[210:213], v0 offset:12288\n"
      "v_mfma_f32_16x16x32_bf16 v[102:105], v[184:187], v[80:83], v[102:105]\n"
      "ds_read_b128 v[120:123], v0 offset:6144\n"
      "v_mfma_f32_16x16x32_bf16 v[192:195], v[184:187], v[84:87], v[192:195]\n"
      "ds_read_b128 v[184:187], v1 offset:6144\n"
      "s_waitcnt lgkmcnt(5)\n"
      "v_mfma_f32_16x16x32_bf16 v[198:201], a[0:3], v[162:165], 0\n"
      "buffer_load_dwordx4 v17, s[16:19], s45 offen  lds\n"
      "v_mfma_f32_16x16x32_bf16 v[176:179], a[96:99], v[162:165], 0\n"
      "s_add_u32 m0, s64, 26624\n"
      "v_cndmask_b32 v202, v202, v102, s[58:59]\n"
      "v_mov_b32_dpp v188, v188 quad_perm:[0,0,0,0] row_mask:0xf bank_mask:0xf\n"
      "v_mfma_f32_16x16x32_bf16 v[102:105], a[24:27], v[162:165], 0\n"
      "s_waitcnt lgkmcnt(3)\n"
      "v_rcp_f32 v188, v188\n"
      "v_mfma_f32_16x16x32_bf16 v[128:131], a[48:51], v[162:165], 0\n"
      "v_add_f32 v156, v156, v157\n"
      "v_add_f32_e64 v158, v158, v159\n"
      "v_mfma_f32_16x16x32_bf16 v[170:173], a[120:123], v[162:165], 0\n"
      "buffer_load_dwordx4 v18, s[16:19], s45 offen  lds\n"
      "v_mfma_f32_16x16x32_bf16 v[170:173], a[124:127], v[152:155], v[170:173]\n"
      "v_cndmask_b32 v202, v202, v192, s[60:61]\n"
      "v_add_f32_e64 v156, v156, v158\n"
      "v_mfma_f32_16x16x32_bf16 v[190:193], a[72:75], v[162:165], 0\n"
      "ds_read_b128 v[96:99], v0 offset:14336\n"
      "v_mul_f32_e64 v202, v188, v202\n"
      "v_mfma_f32_16x16x32_bf16 v[198:201], a[4:7], v[152:155], v[198:201]\n"
      "s_add_u32 m0, s64, 30720\n"
      "v_rsq_f32 v156, v156\n"
      "v_mfma_f32_16x16x32_bf16 v[102:105], a[28:31], v[152:155], v[102:105]\n"
      "s_waitcnt lgkmcnt(1)\n"
      "v_mul_f32 v156, 0x415db3d7, v156\n"
      "v_mov_b32 v158, v156\n"
      "v_mfma_f32_16x16x32_bf16 v[102:105], a[32:35], v[120:123], v[102:105]\n"
      "s_nop 0\n"
      "v_permlane32_swap_b32_e64 v156, v158\n"
      "v_mfma_f32_16x16x32_bf16 v[128:131], a[52:55], v[152:155], v[128:131]\n"
      "buffer_load_dwordx4 v19, s[16:19], s45 offen  lds\n"
      "v_mfma_f32_16x16x32_bf16 v[128:131], a[56:59], v[120:123], v[128:131]\n"
      "ds_read_b128 v[148:151], v1 offset:12288\n"
      "v_mov_b32_e64 v157, v156\n"
      "v_mfma_f32_16x16x32_bf16 v[190:193], a[76:79], v[152:155], v[190:193]\n"
      "s_nop 0\n"
      "v_permlane16_swap_b32 v156, v157\n"
      "v_mfma_f32_16x16x32_bf16 v[190:193], a[80:83], v[120:123], v[190:193]\n"
      "s_add_u32 m0, s65, 34816\n"
      "v_mul_f32 v183, v157, v183\n"
      "v_cvt_pk_bf16_f32 v202, v202, v202\n"
      "v_mfma_f32_16x16x32_bf16 v[176:179], a[100:103], v[152:155], v[176:179]\n"
      "v_mul_f32 v180, v157, v180\n"
      "v_mul_f32_e64 v181, v157, v181\n"
      "v_mfma_f32_16x16x32_bf16 v[176:179], a[104:107], v[120:123], v[176:179]\n"
      "v_mul_f32 v207, v156, v207\n"
      "v_mul_f32 v143, v158, v143\n"
      "v_mfma_f32_16x16x32_bf16 v[198:201], a[8:11], v[120:123], v[198:201]\n"
      "buffer_load_dwordx4 v15, s[16:19], s45 offen  lds\n"
      "v_mfma_f32_16x16x32_bf16 v[198:201], a[12:15], v[184:187], v[198:201]\n"
      "v_cvt_pk_bf16_f32 v180, v180, v181\n"
      "v_mul_f32 v208, v156, v208\n"
      "v_mfma_f32_16x16x32_bf16 v[198:201], a[16:19], v[210:213], v[198:201]\n"
      "s_waitcnt lgkmcnt(0)\n"
      "v_mul_f32 v125, v157, v125\n"
      "v_mul_f32 v115, v156, v115\n"
      "v_mfma_f32_16x16x32_bf16 v[198:201], a[20:23], v[148:151], v[198:201]\n"
      "v_mul_f32 v209, v156, v209\n"
      "v_mul_f32 v114, v156, v114\n"
      "v_mfma_f32_16x16x32_bf16 v[102:105], a[36:39], v[184:187], v[102:105]\n"
      "v_mul_f32 v182, v157, v182\n"
      "v_mul_f32 v116, v156, v116\n"
      "v_mfma_f32_16x16x32_bf16 v[102:105], a[40:43], v[210:213], v[102:105]\n"
      "v_mul_f32_e64 v117, v156, v117\n"
      "v_cvt_pk_bf16_f32 v114, v114, v115\n"
      "v_mfma_f32_16x16x32_bf16 v[102:105], a[44:47], v[148:151], v[102:105]\n"
      "v_mul_f32_e64 v124, v157, v124\n"
      "v_cvt_pk_bf16_f32 v115, v116, v117\n"
      "v_mfma_f32_16x16x32_bf16 v[170:173], a[128:131], v[120:123], v[170:173]\n"
      "v_cvt_pk_bf16_f32 v117, v208, v209\n"
      "v_mul_f32 v147, v158, v147\n"
      "v_mfma_f32_16x16x32_bf16 v[128:131], a[60:63], v[184:187], v[128:131]\n"
      "v_mul_f32 v206, v156, v206\n"
      "v_cvt_pk_bf16_f32 v116, v206, v207\n"
      "v_mfma_f32_16x16x32_bf16 v[128:131], a[64:67], v[210:213], v[128:131]\n"
      "ds_write_b16 v12, v202 offset:16\n"
      "v_mul_f32_e64 v220, v156, v220\n"
      "v_mfma_f32_16x16x32_bf16 v[128:131], a[68:71], v[148:151], v[128:131]\n"
      "buffer_load_dwordx4 v[202:205], v36, s[28:31], s47 offen\n"
      "v_mfma_f32_16x16x32_bf16 v[190:193], a[84:87], v[184:187], v[190:193]\n"
      "ds_write_b128 v5, v[114:117]\n"
      "v_mfma_f32_16x16x32_bf16 v[190:193], a[88:91], v[210:213], v[190:193]\n"
      "ds_read_b128 v[112:115], v1 offset:2048\n"
      "v_mul_f32_e64 v221, v156, v221\n"
      "v_mfma_f32_16x16x32_bf16 v[190:193], a[92:95], v[148:151], v[190:193]\n"
      "buffer_load_dword v78, v29, s[28:31], s47 offen\n"
      "v_mfma_f32_16x16x32_bf16 v[176:179], a[108:111], v[184:187], v[176:179]\n"
      "v_mul_f32 v166, v157, v166\n"
      "v_mul_f32 v144, v158, v144\n"
      "v_mul_f32_e64 v142, v158, v142\n"
      "v_mfma_f32_16x16x32_bf16 v[176:179], a[112:115], v[210:213], v[176:179]\n"
      "v_cvt_pk_bf16_f32 v181, v182, v183\n"
      "v_fma_f32 v107, v198, v198, s51\n"
      "v_mul_f32 v141, v158, v141\n"
      "v_mul_f32 v167, v157, v167\n"
      "v_mfma_f32_16x16x32_bf16 v[176:179], a[116:119], v[148:151], v[176:179]\n"
      "v_mul_f32 v145, v158, v145\n"
      "v_mul_f32 v127, v157, v127\n"
      "v_mfma_f32_16x16x32_bf16 v[170:173], a[132:135], v[184:187], v[170:173]\n"
      "v_cvt_pk_bf16_f32 v220, v220, v221\n"
      "v_fma_f32 v188, v190, v190, s51\n"
      "v_mfma_f32_16x16x32_bf16 v[170:173], a[136:139], v[210:213], v[170:173]\n"
      "ds_read_b128 v[206:209], v0 offset:8192\n"
      "v_mul_f32_e64 v126, v157, v126\n"
      "v_mfma_f32_16x16x32_bf16 v[170:173], a[140:143], v[148:151], v[170:173]\n"
      "v_cvt_pk_bf16_f32 v182, v124, v125\n"
      "v_cvt_pk_bf16_f32 v183, v126, v127\n"
      "v_mfma_f32_16x16x32_bf16 v[194:197], v[162:165], a[144:147], 0\n"
      "ds_write_b128 v5, v[180:183] offset:6144\n"
      "v_mul_f32 v168, v157, v168\n"
      "v_mfma_f32_16x16x32_bf16 v[194:197], v[152:155], a[148:151], v[194:197]\n"
      "v_mul_f32 v108, v158, v108\n"
      "v_fmac_f32 v188, v191, v191\n"
      "v_mfma_f32_16x16x32_bf16 v[194:197], v[120:123], a[152:155], v[194:197]\n"
      "v_mul_f32 v169, v157, v169\n"
      "v_fmac_f32 v107, v199, v199\n"
      "v_mfma_f32_16x16x32_bf16 v[194:197], v[184:187], a[156:159], v[194:197]\n"
      "v_mul_f32 v111, v158, v111\n"
      "v_readlane_b32 s45, v37, s43\n"
      "v_mfma_f32_16x16x32_bf16 v[194:197], v[210:213], a[160:163], v[194:197]\n"
      "v_cvt_pk_bf16_f32 v166, v166, v167\n"
      "v_mul_f32 v110, v158, v110\n"
      "v_mfma_f32_16x16x32_bf16 v[194:197], v[148:151], a[164:167], v[194:197]\n"
      "v_mul_f32 v146, v158, v146\n"
      "v_cvt_pk_bf16_f32 v167, v168, v169\n"
      "v_fmac_f32 v188, v192, v192\n"
      "v_mfma_f32_16x16x32_bf16 v[132:135], v[162:165], a[168:171], 0\n"
      "ds_write_b64 v6, v[166:167] offset:6144\n"
      "v_mfma_f32_16x16x32_bf16 v[132:135], v[152:155], a[172:175], v[132:135]\n"
      "v_mul_f32 v222, v156, v222\n"
      "v_fmac_f32 v188, v193, v193\n"
      "v_mfma_f32_16x16x32_bf16 v[132:135], v[120:123], a[176:179], v[132:135]\n"
      "v_fmac_f32 v188, v176, v176\n"
      "v_mul_f32_e64 v109, v158, v109\n"
      "v_mfma_f32_16x16x32_bf16 v[132:135], v[184:187], a[180:183], v[132:135]\n"
      "ds_read_b128 v[124:127], v0 offset:2048\n"
      "v_cvt_pk_bf16_f32 v144, v144, v145\n"
      "v_mfma_f32_16x16x32_bf16 v[132:135], v[210:213], a[184:187], v[132:135]\n"
      "v_fmac_f32_e64 v107, v200, v200\n"
      "v_cvt_pk_bf16_f32 v145, v146, v147\n"
      "v_mfma_f32_16x16x32_bf16 v[132:135], v[148:151], a[188:191], v[132:135]\n"
      "ds_read_b128 v[166:169], v1 offset:8192\n"
      "v_cvt_pk_bf16_f32 v146, v108, v109\n"
      "v_mfma_f32_16x16x32_bf16 v[214:217], v[162:165], a[192:195], 0\n"
      "ds_read_b128 v[180:183], v1 offset:14336\n"
      "v_cvt_pk_bf16_f32 v147, v110, v111\n"
      "v_mfma_f32_16x16x32_bf16 v[214:217], v[152:155], a[196:199], v[214:217]\n"
      "ds_write_b128 v5, v[144:147] offset:12288\n"
      "v_mul_f32 v140, v158, v140\n"
      "v_mfma_f32_16x16x32_bf16 v[214:217], v[120:123], a[200:203], v[214:217]\n"
      "v_fmac_f32 v188, v177, v177\n"
      "v_cvt_pk_bf16_f32 v140, v140, v141\n"
      "v_mfma_f32_16x16x32_bf16 v[214:217], v[184:187], a[204:207], v[214:217]\n"
      "v_mul_f32_e64 v223, v156, v223\n"
      "v_cvt_pk_bf16_f32 v141, v142, v143\n"
      "v_mfma_f32_16x16x32_bf16 v[214:217], v[210:213], a[208:211], v[214:217]\n"
      "ds_write_b64 v6, v[140:141] offset:12288\n"
      "v_mfma_f32_16x16x32_bf16 v[214:217], v[148:151], a[212:215], v[214:217]\n"
      "s_waitcnt lgkmcnt(2)\n"
      "v_fmac_f32 v107, v201, v201\n"
      "v_fmac_f32_e64 v107, v102, v102\n"
      "v_mfma_f32_16x16x32_bf16 v[108:111], a[0:3], v[124:127], 0\n"
      "v_fmac_f32 v188, v178, v178\n"
      "v_fmac_f32 v107, v103, v103\n"
      "v_mfma_f32_16x16x32_bf16 v[108:111], a[4:7], v[112:115], v[108:111]\n"
      "v_add_u32 v12, 32, v12\n"
      "v_fmac_f32 v188, v179, v179\n"
      "v_mfma_f32_16x16x32_bf16 v[108:111], a[8:11], v[206:209], v[108:111]\n"
      "v_fmac_f32 v107, v104, v104\n"
      "v_fmac_f32 v188, v170, v170\n"
      "v_mfma_f32_16x16x32_bf16 v[108:111], a[12:15], v[166:169], v[108:111]\n"
      "v_fmac_f32 v188, v171, v171\n"
      "v_fmac_f32 v107, v105, v105\n"
      "v_mfma_f32_16x16x32_bf16 v[108:111], a[16:19], v[96:99], v[108:111]\n"
      "v_fmac_f32 v107, v128, v128\n"
      "v_readlane_b32 s47, v38, s43\n"
      "v_mfma_f32_16x16x32_bf16 v[108:111], a[20:23], v[180:183], v[108:111]\n"
      "v_fmac_f32 v107, v129, v129\n"
      "v_cvt_pk_bf16_f32 v194, v194, v195\n"
      "v_mfma_f32_16x16x32_bf16 v[140:143], a[24:27], v[124:127], 0\n"
      "ds_read_u16 v82, v11 offset:32\n"
      "v_fmac_f32_e64 v107, v130, v130\n"
      "v_mfma_f32_16x16x32_bf16 v[140:143], a[28:31], v[112:115], v[140:143]\n"
      "ds_read_b128 v[144:147], v0 offset:4096\n"
      "v_fmac_f32 v188, v172, v172\n"
      "v_mfma_f32_16x16x32_bf16 v[140:143], a[32:35], v[206:209], v[140:143]\n"
      "v_fmac_f32 v107, v131, v131\n"
      "v_fmac_f32 v188, v173, v173\n"
      "v_mfma_f32_16x16x32_bf16 v[140:143], a[36:39], v[166:169], v[140:143]\n"
      "s_and_b32 s47, s47, -4\n"
      "v_fma_f32 v106, v108, v108, s51\n"
      "v_cvt_pk_bf16_f32 v214, v214, v215\n"
      "v_mfma_f32_16x16x32_bf16 v[140:143], a[40:43], v[96:99], v[140:143]\n"
      "v_permlane16_swap_b32 v107, v188\n"
      "v_mfma_f32_16x16x32_bf16 v[140:143], a[44:47], v[180:183], v[140:143]\n"
      "v_add_f32 v107, v107, v188\n"
      "v_mov_b32_e64 v188, v107\n"
      "v_mfma_f32_16x16x32_bf16 v[210:213], a[48:51], v[124:127], 0\n"
      "s_nop 0\n"
      "v_permlane32_swap_b32 v107, v188\n"
      "v_mfma_f32_16x16x32_bf16 v[210:213], a[52:55], v[112:115], v[210:213]\n"
      "v_add_f32 v107, v107, v188\n"
      "v_rsq_f32 v107, v107\n"
      "v_mfma_f32_16x16x32_bf16 v[210:213], a[56:59], v[206:209], v[210:213]\n"
      "ds_read_b128 v[136:139], v1 offset:16384\n"
      "v_mov_b32 v188, v107\n"
      "v_mfma_f32_16x16x32_bf16 v[210:213], a[60:63], v[166:169], v[210:213]\n"
      "s_nop 0\n"
      "v_permlane16_swap_b32 v107, v188\n"
      "v_mfma_f32_16x16x32_bf16 v[210:213], a[64:67], v[96:99], v[210:213]\n"
      "v_cvt_pk_bf16_f32 v215, v216, v217\n"
      "v_fmac_f32 v106, v109, v109\n"
      "v_mfma_f32_16x16x32_bf16 v[210:213], a[68:71], v[180:183], v[210:213]\n"
      "v_mul_f32 v107, s50, v107\n"
      "v_fmac_f32 v106, v110, v110\n"
      "v_mfma_f32_16x16x32_bf16 v[120:123], a[72:75], v[124:127], 0\n"
      "ds_read_b128 v[148:151], v1 offset:10240\n"
      "v_mul_f32_e64 v129, v107, v129\n"
      "v_mfma_f32_16x16x32_bf16 v[120:123], a[76:79], v[112:115], v[120:123]\n"
      "v_mul_f32 v200, v107, v200\n"
      "v_fmac_f32 v106, v111, v111\n"
      "v_mfma_f32_16x16x32_bf16 v[120:123], a[80:83], v[206:209], v[120:123]\n"
      "v_cvt_pk_bf16_f32 v195, v196, v197\n"
      "v_mul_f32 v105, v107, v105\n"
      "v_mfma_f32_16x16x32_bf16 v[120:123], a[84:87], v[166:169], v[120:123]\n"
      "v_cvt_pk_bf16_f32 v221, v222, v223\n"
      "v_mul_f32 v198, v107, v198\n"
      "v_mfma_f32_16x16x32_bf16 v[120:123], a[88:91], v[96:99], v[120:123]\n"
      "v_mul_f32 v199, v107, v199\n"
      "v_cvt_pk_bf16_f32 v132, v132, v133\n"
      "v_mfma_f32_16x16x32_bf16 v[120:123], a[92:95], v[180:183], v[120:123]\n"
      "v_mul_f32 v177, v188, v177\n"
      "v_mul_f32_e64 v128, v107, v128\n"
      "v_mfma_f32_16x16x32_bf16 v[152:155], a[96:99], v[124:127], 0\n"
      "v_mul_f32 v170, v188, v170\n"
      "v_mul_f32 v103, v107, v103\n"
      "v_mfma_f32_16x16x32_bf16 v[152:155], a[100:103], v[112:115], v[152:155]\n"
      "ds_write_b64 v6, v[220:221]\n"
      "v_mfma_f32_16x16x32_bf16 v[152:155], a[104:107], v[206:209], v[152:155]\n"
      "v_fma_f32 v222, v120, v120, s51\n"
      "v_fmac_f32_e64 v222, v121, v121\n"
      "v_mfma_f32_16x16x32_bf16 v[152:155], a[108:111], v[166:169], v[152:155]\n"
      "v_cvt_pk_bf16_f32 v133, v134, v135\n"
      "v_mul_f32 v176, v188, v176\n"
      "v_mfma_f32_16x16x32_bf16 v[152:155], a[112:115], v[96:99], v[152:155]\n"
      "v_mul_f32 v190, v188, v190\n"
      "v_fmac_f32_e64 v106, v140, v140\n"
      "v_mfma_f32_16x16x32_bf16 v[152:155], a[116:119], v[180:183], v[152:155]\n"
      "v_mul_f32 v201, v107, v201\n"
      "v_fmac_f32 v106, v141, v141\n"
      "v_mfma_f32_16x16x32_bf16 v[218:221], a[120:123], v[124:127], 0\n"
      "v_mul_f32 v191, v188, v191\n"
      "v_fmac_f32 v106, v142, v142\n"
      "v_cvt_pk_bf16_f32 v198, v198, v199\n"
      "v_mfma_f32_16x16x32_bf16 v[218:221], a[124:127], v[112:115], v[218:221]\n"
      "ds_read_b128 v[156:159], v0 offset:10240\n"
      "v_mul_f32_e64 v102, v107, v102\n"
      "v_mfma_f32_16x16x32_bf16 v[218:221], a[128:131], v[206:209], v[218:221]\n"
      "v_mul_f32 v172, v188, v172\n"
      "v_fmac_f32 v106, v143, v143\n"
      "v_mfma_f32_16x16x32_bf16 v[218:221], a[132:135], v[166:169], v[218:221]\n"
      "v_fmac_f32 v222, v122, v122\n"
      "v_fmac_f32 v106, v210, v210\n"
      "v_mfma_f32_16x16x32_bf16 v[218:221], a[136:139], v[96:99], v[218:221]\n"
      "v_fmac_f32 v222, v123, v123\n"
      "v_cvt_pk_bf16_f32 v199, v200, v201\n"
      "v_mfma_f32_16x16x32_bf16 v[218:221], a[140:143], v[180:183], v[218:221]\n"
      "v_mul_f32 v171, v188, v171\n"
      "v_fmac_f32 v222, v152, v152\n"
      "v_mfma_f32_16x16x32_bf16 v[162:165], v[124:127], a[144:147], 0\n"
      "v_cvt_pk_bf16_f32 v190, v190, v191\n"
      "v_fmac_f32 v106, v211, v211\n"
      "v_mfma_f32_16x16x32_bf16 v[162:165], v[112:115], a[148:151], v[162:165]\n"
      "v_mul_f32 v192, v188, v192\n"
      "v_fmac_f32 v222, v153, v153\n"
      "v_mfma_f32_16x16x32_bf16 v[162:165], v[206:209], a[152:155], v[162:165]\n"
      "v_fmac_f32_e64 v106, v212, v212\n"
      "v_cvt_pk_bf16_f32 v200, v102, v103\n"
      "v_mfma_f32_16x16x32_bf16 v[162:165], v[166:169], a[156:159], v[162:165]\n"
      "v_fmac_f32 v222, v154, v154\n"
      "v_fmac_f32 v222, v155, v155\n"
      "v_mfma_f32_16x16x32_bf16 v[162:165], v[96:99], a[160:163], v[162:165]\n"
      "v_mul_f32 v193, v188, v193\n"
      "v_mul_f32 v104, v107, v104\n"
      "v_mfma_f32_16x16x32_bf16 v[162:165], v[180:183], a[164:167], v[162:165]\n"
      "v_cvt_pk_bf16_f32 v191, v192, v193\n"
      "v_cvt_pk_bf16_f32 v192, v176, v177\n"
      "v_cvt_pk_bf16_f32 v128, v128, v129\n"
      "v_mul_f32 v179, v188, v179\n"
      "v_mfma_f32_16x16x32_bf16 v[100:103], v[124:127], a[168:171], 0\n"
      "v_mul_f32 v173, v188, v173\n"
      "v_fmac_f32_e64 v222, v218, v218\n"
      "v_mfma_f32_16x16x32_bf16 v[100:103], v[112:115], a[172:175], v[100:103]\n"
      "ds_read_b128 v[116:119], v1 offset:4096\n"
      "v_cvt_pk_bf16_f32 v196, v162, v163\n"
      "v_mfma_f32_16x16x32_bf16 v[100:103], v[206:209], a[176:179], v[100:103]\n"
      "ds_read_b128 v[160:163], v7 offset:22528\n"
      "v_fmac_f32 v222, v219, v219\n"
      "v_mfma_f32_16x16x32_bf16 v[100:103], v[166:169], a[180:183], v[100:103]\n"
      "v_fmac_f32 v222, v220, v220\n"
      "v_fmac_f32 v222, v221, v221\n"
      "v_mfma_f32_16x16x32_bf16 v[100:103], v[96:99], a[184:187], v[100:103]\n"
      "v_mul_f32 v178, v188, v178\n"
      "v_fmac_f32 v106, v213, v213\n"
      "v_mfma_f32_16x16x32_bf16 v[100:103], v[180:183], a[188:191], v[100:103]\n"
      "ds_read_b128 v[174:177], v0 offset:16384\n"
      "v_mul_f32 v131, v107, v131\n"
      "v_mfma_f32_16x16x32_bf16 v[186:189], v[124:127], a[192:195], 0\n"
      "v_permlane16_swap_b32 v106, v222\n"
      "v_mfma_f32_16x16x32_bf16 v[186:189], v[112:115], a[196:199], v[186:189]\n"
      "v_add_f32 v106, v106, v222\n"
      "v_mov_b32 v222, v106\n"
      "v_mfma_f32_16x16x32_bf16 v[186:189], v[206:209], a[200:203], v[186:189]\n"
      "s_nop 0\n"
      "v_permlane32_swap_b32 v106, v222\n"
      "v_mfma_f32_16x16x32_bf16 v[186:189], v[166:169], a[204:207], v[186:189]\n"
      "v_add_f32 v106, v106, v222\n"
      "v_rsq_f32 v106, v106\n"
      "v_mfma_f32_16x16x32_bf16 v[186:189], v[96:99], a[208:211], v[186:189]\n"
      "s_waitcnt lgkmcnt(1)\n"
      "v_cvt_pk_bf16_f32 v170, v170, v171\n"
      "v_mov_b32 v222, v106\n"
      "v_mfma_f32_16x16x32_bf16 v[186:189], v[180:183], a[212:215], v[186:189]\n"
      "s_nop 0\n"
      "v_permlane16_swap_b32_e64 v106, v222\n"
      "v_mfma_f32_16x16x32_bf16 v[96:99], a[0:3], v[144:147], 0\n"
      "v_mul_f32 v120, v222, v120\n"
      "v_mul_f32 v106, s50, v106\n"
      "v_mfma_f32_16x16x32_bf16 v[96:99], a[4:7], v[116:119], v[96:99]\n"
      "v_mul_f32 v210, v106, v210\n"
      "v_mul_f32 v211, v106, v211\n"
      "v_mfma_f32_16x16x32_bf16 v[96:99], a[8:11], v[156:159], v[96:99]\n"
      "v_mul_f32 v219, v222, v219\n"
      "v_mul_f32 v140, v106, v140\n"
      "v_mfma_f32_16x16x32_bf16 v[96:99], a[12:15], v[148:151], v[96:99]\n"
      "s_waitcnt lgkmcnt(0)\n"
      "v_mul_f32 v212, v106, v212\n"
      "v_mul_f32 v141, v106, v141\n"
      "v_mul_f32 v130, v107, v130\n"
      "v_mfma_f32_16x16x32_bf16 v[96:99], a[16:19], v[174:177], v[96:99]\n"
      "v_cvt_pk_bf16_f32 v210, v210, v211\n"
      "v_mul_f32_e64 v111, v106, v111\n"
      "v_mfma_f32_16x16x32_bf16 v[96:99], a[20:23], v[136:139], v[96:99]\n"
      "v_mul_f32 v218, v222, v218\n"
      "v_cvt_pk_bf16_f32 v193, v178, v179\n"
      "v_mfma_f32_16x16x32_bf16 v[124:127], a[24:27], v[144:147], 0\n"
      "v_mul_f32 v213, v106, v213\n"
      "v_mul_f32 v109, v106, v109\n"
      "v_mfma_f32_16x16x32_bf16 v[124:127], a[28:31], v[116:119], v[124:127]\n"
      "v_cvt_pk_bf16_f32 v211, v212, v213\n"
      "v_mul_f32 v108, v106, v108\n"
      "v_mfma_f32_16x16x32_bf16 v[124:127], a[32:35], v[156:159], v[124:127]\n"
      "ds_read_b128 v[206:209], v7 offset:18432\n"
      "v_cvt_pk_bf16_f32 v171, v172, v173\n"
      "v_mfma_f32_16x16x32_bf16 v[124:127], a[36:39], v[148:151], v[124:127]\n"
      "v_cvt_pk_bf16_f32 v129, v130, v131\n"
      "v_cvt_pk_bf16_f32 v216, v186, v187\n"
      "v_mfma_f32_16x16x32_bf16 v[124:127], a[40:43], v[174:177], v[124:127]\n"
      "v_cvt_pk_bf16_f32 v201, v104, v105\n"
      "v_mul_f32_e64 v121, v222, v121\n"
      "v_mfma_f32_16x16x32_bf16 v[124:127], a[44:47], v[136:139], v[124:127]\n"
      "v_fma_f32 v173, v96, v96, s51\n"
      "v_mul_f32 v155, v222, v155\n"
      "v_mfma_f32_16x16x32_bf16 v[184:187], a[48:51], v[144:147], 0\n"
      "v_mul_f32 v143, v106, v143\n"
      "v_fmac_f32 v173, v97, v97\n"
      "v_mfma_f32_16x16x32_bf16 v[184:187], a[52:55], v[116:119], v[184:187]\n"
      "v_fmac_f32 v173, v98, v98\n"
      "v_fmac_f32 v173, v99, v99\n"
      "v_mfma_f32_16x16x32_bf16 v[184:187], a[56:59], v[156:159], v[184:187]\n"
      "v_fmac_f32 v173, v124, v124\n"
      "v_mul_f32 v142, v106, v142\n"
      "v_mfma_f32_16x16x32_bf16 v[184:187], a[60:63], v[148:151], v[184:187]\n"
      "v_cvt_pk_bf16_f32 v217, v188, v189\n"
      "v_mul_f32 v110, v106, v110\n"
      "v_mfma_f32_16x16x32_bf16 v[184:187], a[64:67], v[174:177], v[184:187]\n"
      "ds_read_b128 v[180:183], v7 offset:30720\n"
      "v_fmac_f32 v173, v125, v125\n"
      "v_mfma_f32_16x16x32_bf16 v[184:187], a[68:71], v[136:139], v[184:187]\n"
      "v_mul_f32 v122, v222, v122\n"
      "v_cvt_pk_bf16_f32 v108, v108, v109\n"
      "v_mfma_f32_16x16x32_bf16 v[104:107], a[72:75], v[144:147], 0\n"
      "s_waitcnt lgkmcnt(1)\n"
      "v_cvt_pk_bf16_f32 v134, v100, v101\n"
      "v_mul_f32 v220, v222, v220\n"
      "v_mfma_f32_16x16x32_bf16 v[104:107], a[76:79], v[116:119], v[104:107]\n"
      "buffer_store_dwordx4 v[206:209], v20, s[24:27], s46 offen sc1\n"
      "v_mfma_f32_16x16x32_bf16 v[104:107], a[80:83], v[156:159], v[104:107]\n"
      "v_fmac_f32 v173, v126, v126\n"
      "v_fmac_f32 v173, v127, v127\n"
      "v_mfma_f32_16x16x32_bf16 v[104:107], a[84:87], v[148:151], v[104:107]\n"
      "ds_read_b128 v[206:209], v7 offset:26624\n"
      "v_cvt_pk_bf16_f32 v135, v102, v103\n"
      "v_mfma_f32_16x16x32_bf16 v[104:107], a[88:91], v[174:177], v[104:107]\n"
      "v_cvt_pk_bf16_f32 v109, v110, v111\n"
      "v_fmac_f32 v173, v184, v184\n"
      "v_mfma_f32_16x16x32_bf16 v[104:107], a[92:95], v[136:139], v[104:107]\n"
      "v_mul_f32 v123, v222, v123\n"
      "v_mul_f32_e64 v154, v222, v154\n"
      "v_mfma_f32_16x16x32_bf16 v[166:169], a[96:99], v[144:147], 0\n"
      "v_cvt_pk_bf16_f32 v110, v140, v141\n"
      "v_fmac_f32 v173, v185, v185\n"
      "v_mfma_f32_16x16x32_bf16 v[166:169], a[100:103], v[116:119], v[166:169]\n"
      "v_cvt_pk_bf16_f32 v120, v120, v121\n"
      "v_fmac_f32 v173, v186, v186\n"
      "v_mfma_f32_16x16x32_bf16 v[166:169], a[104:107], v[156:159], v[166:169]\n"
      "v_fma_f32 v179, v104, v104, s51\n"
      "v_fmac_f32_e64 v179, v105, v105\n"
      "v_mfma_f32_16x16x32_bf16 v[166:169], a[108:111], v[148:151], v[166:169]\n"
      "buffer_store_dwordx4 v[160:163], v21, s[24:27], s46 offen sc1\n"
      "v_mfma_f32_16x16x32_bf16 v[166:169], a[112:115], v[174:177], v[166:169]\n"
      "v_fmac_f32 v179, v106, v106\n"
      "v_mul_f32 v152, v222, v152\n"
      "v_mfma_f32_16x16x32_bf16 v[166:169], a[116:119], v[136:139], v[166:169]\n"
      "v_mul_f32 v221, v222, v221\n"
      "v_cvt_pk_bf16_f32 v121, v122, v123\n"
      "v_cvt_pk_bf16_f32 v197, v164, v165\n"
      "v_mfma_f32_16x16x32_bf16 v[100:103], a[120:123], v[144:147], 0\n"
      "v_fmac_f32 v173, v187, v187\n"
      "v_fmac_f32 v179, v107, v107\n"
      "v_mfma_f32_16x16x32_bf16 v[100:103], a[124:127], v[116:119], v[100:103]\n"
      "v_cvt_pk_bf16_f32 v123, v154, v155\n"
      "v_mul_f32 v153, v222, v153\n"
      "v_mfma_f32_16x16x32_bf16 v[100:103], a[128:131], v[156:159], v[100:103]\n"
      "v_fmac_f32 v179, v166, v166\n"
      "v_cvt_pk_bf16_f32 v218, v218, v219\n"
      "v_mfma_f32_16x16x32_bf16 v[100:103], a[132:135], v[148:151], v[100:103]\n"
      "s_waitcnt lgkmcnt(0)\n"
      "v_cvt_pk_bf16_f32 v111, v142, v143\n"
      "v_cvt_pk_bf16_f32 v219, v220, v221\n"
      "v_mfma_f32_16x16x32_bf16 v[100:103], a[136:139], v[174:177], v[100:103]\n"
      "ds_read_b128 v[140:143], v2 offset:18432\n"
      "v_fmac_f32_e64 v179, v167, v167\n"
      "v_mfma_f32_16x16x32_bf16 v[100:103], a[140:143], v[136:139], v[100:103]\n"
      "buffer_store_dwordx4 v[206:209], v22, s[24:27], s46 offen sc1\n"
      "s_nop 1\n"
      "v_mfma_f32_16x16x32_bf16 v[206:209], v[144:147], a[144:147], 0\n"
      "v_fmac_f32 v179, v168, v168\n"
      "v_cvt_pk_bf16_f32 v122, v152, v153\n"
      "v_mfma_f32_16x16x32_bf16 v[206:209], v[116:119], a[148:151], v[206:209]\n"
      "ds_read_b128 v[220:223], v7 offset:34816\n"
      "v_fmac_f32_e64 v179, v169, v169\n"
      "v_mfma_f32_16x16x32_bf16 v[206:209], v[156:159], a[152:155], v[206:209]\n"
      "v_fmac_f32 v179, v100, v100\n"
      "v_fmac_f32 v179, v101, v101\n"
      "v_mfma_f32_16x16x32_bf16 v[206:209], v[148:151], a[156:159], v[206:209]\n"
      "v_fmac_f32 v179, v102, v102\n"
      "v_fmac_f32 v179, v103, v103\n"
      "v_mfma_f32_16x16x32_bf16 v[206:209], v[174:177], a[160:163], v[206:209]\n"
      "s_nop 0\n"
      "v_permlane16_swap_b32 v173, v179\n"
      "v_mfma_f32_16x16x32_bf16 v[206:209], v[136:139], a[164:167], v[206:209]\n"
      "v_add_f32 v173, v173, v179\n"
      "v_mov_b32 v179, v173\n"
      "v_mfma_f32_16x16x32_bf16 v[160:163], v[144:147], a[168:171], 0\n"
      "s_nop 0\n"
      "v_permlane32_swap_b32 v173, v179\n"
      "v_mfma_f32_16x16x32_bf16 v[160:163], v[116:119], a[172:175], v[160:163]\n"
      "buffer_store_dwordx4 v[180:183], v23, s[24:27], s46 offen sc1\n"
      "v_add_f32 v173, v173, v179\n"
      "v_mfma_f32_16x16x32_bf16 v[160:163], v[156:159], a[176:179], v[160:163]\n"
      "v_rsq_f32 v173, v173\n"
      "v_mfma_f32_16x16x32_bf16 v[160:163], v[148:151], a[180:183], v[160:163]\n"
      "ds_read_b128 v[180:183], v2 offset:21504\n"
      "v_mov_b32 v179, v173\n"
      "v_mfma_f32_16x16x32_bf16 v[160:163], v[174:177], a[184:187], v[160:163]\n"
      "s_nop 0\n"
      "v_permlane16_swap_b32_e64 v173, v179\n"
      "v_mfma_f32_16x16x32_bf16 v[160:163], v[136:139], a[188:191], v[160:163]\n"
      "v_cvt_pk_bf16_f32 v77, v208, v209\n"
      "v_mul_f32 v166, v179, v166\n"
      "v_mul_f32 v100, v179, v100\n"
      "v_mfma_f32_16x16x32_bf16 v[152:155], v[144:147], a[192:195], 0\n"
      "v_mul_f32 v104, v179, v104\n"
      "v_mul_f32 v101, v179, v101\n"
      "v_mul_f32 v107, v179, v107\n"
      "v_mfma_f32_16x16x32_bf16 v[152:155], v[116:119], a[196:199], v[152:155]\n"
      "ds_read_b128 v[144:147], v2 offset:24576\n"
      "v_mul_f32 v105, v179, v105\n"
      "v_mfma_f32_16x16x32_bf16 v[152:155], v[156:159], a[200:203], v[152:155]\n"
      "ds_read_b128 v[156:159], v10 offset:32\n"
      "v_mul_f32_e64 v173, s50, v173\n"
      "v_mfma_f32_16x16x32_bf16 v[152:155], v[148:151], a[204:207], v[152:155]\n"
      "ds_read_b128 v[148:151], v9 offset:5632\n"
      "v_cvt_pk_bf16_f32 v104, v104, v105\n"
      "v_mfma_f32_16x16x32_bf16 v[152:155], v[174:177], a[208:211], v[152:155]\n"
      "s_waitcnt lgkmcnt(3)\n"
      "v_mul_f32 v106, v179, v106\n"
      "v_cvt_pk_bf16_f32 v76, v206, v207\n"
      "v_mfma_f32_16x16x32_bf16 v[152:155], v[136:139], a[212:215], v[152:155]\n"
      "buffer_store_dwordx4 v[220:223], v25, s[24:27], s46 offen sc1\n"
      "v_mfma_f32_16x16x32_bf16 v[114:117], a[216:219], v[140:143], 0\n"
      "ds_read_b128 v[136:139], v2 offset:27648\n"
      "v_mul_f32 v169, v179, v169\n"
      "v_mfma_f32_16x16x32_bf16 v[114:117], a[220:223], v[180:183], v[114:117]\n"
      "s_waitcnt lgkmcnt(2)\n"
      "v_cvt_pk_bf16_f32 v100, v100, v101\n"
      "v_mul_f32_e64 v125, v173, v125\n"
      "v_mfma_f32_16x16x32_bf16 v[114:117], a[224:227], v[144:147], v[114:117]\n"
      "v_mul_f32 v168, v179, v168\n"
      "v_cvt_pk_bf16_f32 v105, v106, v107\n"
      "v_mfma_f32_16x16x32_bf16 v[206:209], a[240:243], v[140:143], 0\n"
      "s_waitcnt lgkmcnt(1)\n"
      "v_mul_f32 v187, v173, v187\n"
      "v_mul_f32 v167, v179, v167\n"
      "v_mfma_f32_16x16x32_bf16 v[206:209], a[244:247], v[180:183], v[206:209]\n"
      "v_or3_b32 v148, v148, v149, v150\n"
      "v_mul_f32_e64 v124, v173, v124\n"
      "v_mfma_f32_16x16x32_bf16 v[206:209], a[248:251], v[144:147], v[206:209]\n"
      "ds_read_b64 v[112:113], v10 offset:24\n"
      "v_cvt_pk_bf16_f32 v106, v166, v167\n"
      "v_mul_f32_e64 v99, v173, v99\n"
      "v_mfma_f32_16x16x32_bf16 v[220:223], v[232:235], v[140:143], 0\n"
      "v_cvt_pk_bf16_f32 v107, v168, v169\n"
      "v_or_b32 v148, v148, v151\n"
      "v_mfma_f32_16x16x32_bf16 v[220:223], v[236:239], v[180:183], v[220:223]\n"
      "v_mul_f32 v102, v179, v102\n"
      "v_mul_f32 v103, v179, v103\n"
      "v_mfma_f32_16x16x32_bf16 v[220:223], v[240:243], v[144:147], v[220:223]\n"
      "v_mul_f32 v184, v173, v184\n"
      "v_cvt_pk_bf16_f32 v81, v162, v163\n"
      "v_mfma_f32_16x16x32_bf16 v[180:183], v[104:107], v[198:201], v[48:51]\n"
      "ds_read_b128 v[166:169], v2 offset:30720\n"
      "v_cvt_pk_bf16_f32 v85, v154, v155\n"
      "v_mfma_f32_16x16x32_bf16 v[142:145], v[190:193], v[198:201], v[40:43]\n"
      "ds_read_b128 v[176:179], v2 offset:33792\n"
      "v_cvt_pk_bf16_f32 v101, v102, v103\n"
      "v_mfma_f32_16x16x32_bf16 v[162:165], v[156:159], v[198:201], v[24:27]\n"
      "v_cmp_ne_u32 s[56:57], 0, v148\n"
      "v_mul_f32_e64 v96, v173, v96\n"
      "v_mfma_f32_16x16x32_bf16 v[146:149], v[120:123], v[198:201], v[44:47]\n"
      "s_waitcnt lgkmcnt(2)\n"
      "v_mul_f32 v97, v173, v97\n"
      "v_readlane_b32 s46, v37, s42\n"
      "v_mfma_f32_16x16x16_bf16 v[142:145], v[170:171], v[128:129], v[142:145]\n"
      "v_cvt_pk_bf16_f32 v96, v96, v97\n"
      "v_mul_f32 v126, v173, v126\n"
      "v_mul_f32 v185, v173, v185\n"
      "v_mfma_f32_16x16x16_bf16 v[146:149], v[218:219], v[128:129], v[146:149]\n"
      "v_mul_f32_e64 v98, v173, v98\n"
      "v_cvt_pk_bf16_f32 v184, v184, v185\n"
      "v_cvt_pk_bf16_f32 v84, v152, v153\n"
      "v_mfma_f32_16x16x16_bf16 v[180:183], v[100:101], v[128:129], v[180:183]\n"
      "v_cvt_pk_bf16_f32 v80, v160, v161\n"
      "v_cvt_pk_bf16_f32 v97, v98, v99\n"
      "s_waitcnt vmcnt(6)\n"
      "v_or3_b32 v202, v202, v203, v204\n"
      "v_mfma_f32_16x16x16_bf16 v[162:165], v[112:113], v[128:129], v[162:165]\n"
      "v_max3_f32 v203, v142, v143, v144\n"
      "v_max3_f32 v203, v203, v148, v149\n"
      "v_max3_f32 v198, v145, v146, v147\n"
      "v_mfma_f32_16x16x32_bf16 v[114:117], a[228:231], v[136:139], v[114:117]\n"
      "s_waitcnt lgkmcnt(0)\n"
      "v_mul_f32 v186, v173, v186\n"
      "v_max3_f32 v203, v203, v182, v183\n"
      "v_mfma_f32_16x16x32_bf16 v[114:117], a[232:235], v[166:169], v[114:117]\n"
      "v_mul_f32 v127, v173, v127\n"
      "v_max3_f32 v198, v198, v180, v181\n"
      "v_max3_f32 v203, v203, v198, v162\n"
      "v_mfma_f32_16x16x32_bf16 v[114:117], a[236:239], v[176:179], v[114:117]\n"
      "v_mov_b32 v140, v203\n"
      "s_waitcnt vmcnt(5)\n"
      "v_or3_b32 v202, v202, v205, v78\n"
      "v_mfma_f32_16x16x32_bf16 v[206:209], a[252:255], v[136:139], v[206:209]\n"
      "v_permlane16_swap_b32_e64 v203, v140\n"
      "v_mfma_f32_16x16x32_bf16 v[206:209], v[224:227], v[166:169], v[206:209]\n"
      "ds_read_b128 v[198:201], v2 offset:18688\n"
      "v_cvt_pk_bf16_f32 v98, v124, v125\n"
      "v_max_f32 v203, v203, v140\n"
      "v_mfma_f32_16x16x32_bf16 v[206:209], v[228:231], v[176:179], v[206:209]\n"
      "v_cvt_pk_bf16_f32 v185, v186, v187\n"
      "v_mov_b32 v140, v203\n"
      "v_mfma_f32_16x16x32_bf16 v[220:223], v[244:247], v[136:139], v[220:223]\n"
      "ds_read_b128 v[186:189], v2 offset:21760\n"
      "v_permlane32_swap_b32 v203, v140\n"
      "v_max_f32 v203, v203, v140\n"
      "v_mfma_f32_16x16x32_bf16 v[220:223], v[248:251], v[166:169], v[220:223]\n"
      "ds_read_b128 v[138:141], v2 offset:24832\n"
      "v_sub_f32 v181, v181, v203\n"
      "v_sub_f32 v182, v182, v203\n"
      "v_sub_f32 v148, v148, v203\n"
      "v_sub_f32 v162, v162, v203\n"
      "v_sub_f32_e64 v183, v183, v203\n"
      "v_mfma_f32_16x16x32_bf16 v[220:223], v[252:255], v[176:179], v[220:223]\n"
      "ds_read_u16 v78, v11\n"
      "v_exp_f32 v183, v183\n"
      "v_mfma_f32_16x16x32_bf16 v[152:155], v[190:193], v[108:111], v[52:55]\n"
      "v_exp_f32 v181, v181\n"
      "v_mfma_f32_16x16x32_bf16 v[164:167], v[156:159], v[108:111], v[26:29]\n"
      "v_sub_f32_e64 v144, v144, v203\n"
      "v_cvt_pk_bf16_f32 v99, v126, v127\n"
      "v_mfma_f32_16x16x32_bf16 v[172:175], v[120:123], v[108:111], v[56:59]\n"
      "ds_read_b128 v[128:131], v2 offset:27904\n"
      "v_sub_f32 v145, v145, v203\n"
      "v_exp_f32 v162, v162\n"
      "v_mfma_f32_16x16x32_bf16 v[176:179], v[104:107], v[108:111], v[60:63]\n"
      "s_waitcnt lgkmcnt(2)\n"
      "v_sub_f32 v180, v180, v203\n"
      "v_cvt_pk_bf16_f32 v90, v162, 0\n"
      "v_mfma_f32_16x16x16_bf16 v[164:167], v[112:113], v[210:211], v[164:167]\n"
      "v_sub_f32 v149, v149, v203\n"
      "v_exp_f32 v180, v180\n"
      "v_sub_f32_e64 v146, v146, v203\n"
      "v_mfma_f32_16x16x16_bf16 v[152:155], v[170:171], v[210:211], v[152:155]\n"
      "v_sub_f32 v147, v147, v203\n"
      "v_exp_f32 v145, v145\n"
      "v_and_b32 v202, 0x7fff7fff, v202\n"
      "v_mfma_f32_16x16x16_bf16 v[172:175], v[218:219], v[210:211], v[172:175]\n"
      "v_exp_f32 v182, v182\n"
      "v_exp_f32 v148, v148\n"
      "v_cvt_pk_bf16_f32 v89, v182, v183\n"
      "v_sub_f32 v143, v143, v203\n"
      "v_exp_f32 v149, v149\n"
      "v_exp_f32_e64 v143, v143\n"
      "v_max3_f32 v151, v152, v153, v154\n"
      "v_mfma_f32_16x16x16_bf16 v[176:179], v[100:101], v[210:211], v[176:179]\n"
      "v_exp_f32_e64 v144, v144\n"
      "v_max3_f32 v210, v155, v172, v173\n"
      "v_mfma_f32_16x16x32_bf16 v[124:127], a[240:243], v[198:201], 0\n"
      "v_sub_f32 v142, v142, v203\n"
      "v_exp_f32 v147, v147\n"
      "v_mfma_f32_16x16x32_bf16 v[124:127], a[244:247], v[186:189], v[124:127]\n"
      "v_max3_f32 v151, v151, v174, v175\n"
      "v_exp_f32 v142, v142\n"
      "v_mfma_f32_16x16x32_bf16 v[124:127], a[248:251], v[138:141], v[124:127]\n"
      "s_waitcnt lgkmcnt(0)\n"
      "v_exp_f32_e64 v146, v146\n"
      "v_mfma_f32_16x16x32_bf16 v[124:127], a[252:255], v[128:131], v[124:127]\n"
      "v_cvt_pk_bf16_f32 v142, v142, v143\n"
      "v_cvt_pk_bf16_f32 v88, v180, v181\n"
      "v_mfma_f32_16x16x32_bf16 v[180:183], a[216:219], v[198:201], 0\n"
      "v_cvt_pk_bf16_f32 v143, v144, v145\n"
      "v_cvt_pk_bf16_f32 v144, v146, v147\n"
      "v_mfma_f32_16x16x32_bf16 v[180:183], a[220:223], v[186:189], v[180:183]\n"
      "v_cvt_pk_bf16_f32 v145, v148, v149\n"
      "v_max3_f32 v151, v151, v178, v179\n"
      "v_mfma_f32_16x16x32_bf16 v[166:169], v[232:235], v[198:201], 0\n"
      "s_or_b32 s95, s45, s46\n"
      "v_max3_f32 v210, v210, v176, v177\n"
      "v_max3_f32 v151, v151, v210, v164\n"
      "v_mfma_f32_16x16x32_bf16 v[166:169], v[236:239], v[186:189], v[166:169]\n"
      "ds_read_u16 v86, v11 offset:64\n"
      "v_mov_b32 v189, v151\n"
      "v_mfma_f32_16x16x32_bf16 v[108:111], v[32:35], v[142:145], 0\n"
      "s_nop 0\n"
      "v_permlane16_swap_b32 v151, v189\n"
      "v_mfma_f32_16x16x32_bf16 v[108:111], v[32:35], v[88:91], v[108:111]\n"
      "v_max_f32 v151, v151, v189\n"
      "v_mov_b32 v189, v151\n"
      "v_mfma_f32_16x16x32_bf16 v[210:213], v[194:197], v[142:145], 0\n"
      "s_nop 0\n"
      "v_permlane32_swap_b32 v151, v189\n"
      "v_mfma_f32_16x16x32_bf16 v[210:213], v[76:79], v[88:91], v[210:213]\n"
      "v_max_f32 v151, v151, v189\n"
      "v_sub_f32 v164, v164, v151\n"
      "v_mfma_f32_16x16x32_bf16 v[186:189], v[132:135], v[142:145], 0\n"
      "ds_write_b32 v31, v202 offset:4608\n"
      "v_mfma_f32_16x16x32_bf16 v[186:189], v[80:83], v[88:91], v[186:189]\n"
      "v_sub_f32 v176, v176, v151\n"
      "v_rcp_f32 v108, v108\n"
      "v_mfma_f32_16x16x32_bf16 v[180:183], a[224:227], v[138:141], v[180:183]\n"
      "v_mul_f32 v211, v108, v211\n"
      "v_sub_f32 v154, v154, v151\n"
      "v_mfma_f32_16x16x32_bf16 v[180:183], a[228:231], v[128:131], v[180:183]\n"
      "v_mul_f32 v210, v108, v210\n"
      "v_sub_f32 v152, v152, v151\n"
      "v_mfma_f32_16x16x32_bf16 v[202:205], v[214:217], v[142:145], 0\n"
      "s_waitcnt lgkmcnt(1)\n"
      "v_exp_f32 v152, v152\n"
      "v_mfma_f32_16x16x32_bf16 v[202:205], v[84:87], v[88:91], v[202:205]\n"
      "v_exp_f32 v176, v176\n"
      "v_mul_f32 v189, v108, v189\n"
      "v_mul_f32 v213, v108, v213\n"
      "v_sub_f32 v173, v173, v151\n"
      "v_sub_f32 v172, v172, v151\n"
      "v_mul_f32 v188, v108, v188\n"
      "v_mfma_f32_16x16x32_bf16 v[166:169], v[240:243], v[138:141], v[166:169]\n"
      "v_cvt_pk_bf16_f32 v210, v210, v211\n"
      "v_mul_f32 v202, v108, v202\n"
      "v_mul_f32 v186, v108, v186\n"
      "v_mfma_f32_16x16x32_bf16 v[166:169], v[244:247], v[128:131], v[166:169]\n"
      "ds_read_b128 v[140:143], v2 offset:30976\n"
      "v_sub_f32 v155, v155, v151\n"
      "v_mfma_f32_16x16x32_bf16 v[128:131], v[156:159], v[96:99], v[28:31]\n"
      "s_or_b32 s95, s95, s48\n"
      "v_sub_f32 v175, v175, v151\n"
      "v_mul_f32 v212, v108, v212\n"
      "v_sub_f32 v179, v179, v151\n"
      "v_sub_f32 v177, v177, v151\n"
      "v_sub_f32 v174, v174, v151\n"
      "v_mul_f32 v205, v108, v205\n"
      "v_exp_f32 v172, v172\n"
      "v_cvt_pk_bf16_f32 v211, v212, v213\n"
      "v_mul_f32 v203, v108, v203\n"
      "v_sub_f32 v178, v178, v151\n"
      "v_sub_f32 v153, v153, v151\n"
      "v_mfma_f32_16x16x16_bf16 v[128:131], v[112:113], v[184:185], v[128:131]\n"
      "v_exp_f32 v155, v155\n"
      "v_mul_f32 v187, v108, v187\n"
      "v_cvt_pk_bf16_f32 v212, v186, v187\n"
      "v_exp_f32 v154, v154\n"
      "v_mul_f32 v204, v108, v204\n"
      "v_mfma_f32_16x16x32_bf16 v[148:151], v[104:107], v[96:99], v[72:75]\n"
      "v_exp_f32 v178, v178\n"
      "v_exp_f32 v153, v153\n"
      "v_cvt_pk_bf16_f32 v213, v188, v189\n"
      "v_mfma_f32_16x16x32_bf16 v[186:189], v[190:193], v[96:99], v[64:67]\n"
      "ds_read_b128 v[108:111], v2 offset:34048\n"
      "v_exp_f32 v177, v177\n"
      "v_cvt_pk_bf16_f32 v152, v152, v153\n"
      "v_cvt_pk_bf16_f32 v202, v202, v203\n"
      "v_mfma_f32_16x16x32_bf16 v[198:201], v[120:123], v[96:99], v[68:71]\n"
      "s_waitcnt lgkmcnt(1)\n"
      "v_cvt_pk_bf16_f32 v92, v176, v177\n"
      "v_cvt_pk_bf16_f32 v203, v204, v205\n"
      "v_mfma_f32_16x16x16_bf16 v[186:189], v[170:171], v[184:185], v[186:189]\n"
      "v_exp_f32 v173, v173\n"
      "v_fma_f32 v129, v114, v114, s52\n"
      "v_mfma_f32_16x16x16_bf16 v[148:151], v[100:101], v[184:185], v[148:151]\n"
      "v_fmac_f32 v129, v115, v115\n"
      "v_exp_f32 v175, v175\n"
      "v_mfma_f32_16x16x16_bf16 v[198:201], v[218:219], v[184:185], v[198:201]\n"
      "v_cvt_pk_bf16_f32 v153, v154, v155\n"
      "v_exp_f32 v179, v179\n"
      "v_exp_f32_e64 v164, v164\n"
      "v_max3_f32 v90, v186, v187, v188\n"
      "v_mfma_f32_16x16x32_bf16 v[180:183], a[232:235], v[140:143], v[180:183]\n"
      "s_waitcnt lgkmcnt(0)\n"
      "v_exp_f32 v174, v174\n"
      "v_mfma_f32_16x16x32_bf16 v[180:183], a[236:239], v[108:111], v[180:183]\n"
      "v_max3_f32 v90, v90, v200, v201\n"
      "v_max3_f32 v89, v189, v198, v199\n"
      "v_max3_f32 v90, v90, v150, v151\n"
      "v_mfma_f32_16x16x32_bf16 v[166:169], v[248:251], v[140:143], v[166:169]\n"
      "v_max3_f32 v89, v89, v148, v149\n"
      "v_max3_f32 v90, v90, v89, v128\n"
      "v_cvt_pk_bf16_f32 v154, v172, v173\n"
      "v_mfma_f32_16x16x32_bf16 v[166:169], v[252:255], v[108:111], v[166:169]\n"
      "ds_read_b64 v[88:89], v10 offset:16\n"
      "v_cvt_pk_bf16_f32 v155, v174, v175\n"
      "v_mfma_f32_16x16x32_bf16 v[124:127], v[224:227], v[140:143], v[124:127]\n"
      "ds_read_b128 v[140:143], v10\n"
      "v_cvt_pk_bf16_f32 v93, v178, v179\n"
      "v_cvt_pk_bf16_f32 v94, v164, 0\n"
      "v_fmac_f32 v129, v116, v116\n"
      "v_mov_b32 v138, v90\n"
      "v_mfma_f32_16x16x32_bf16 v[124:127], v[228:231], v[108:111], v[124:127]\n"
      "ds_write_b128 v3, v[210:213]\n"
      "v_mfma_f32_16x16x32_bf16 v[210:213], v[32:35], v[152:155], 0\n"
      "v_permlane16_swap_b32 v90, v138\n"
      "v_fmac_f32 v129, v117, v117\n"
      "v_mfma_f32_16x16x32_bf16 v[210:213], v[32:35], v[92:95], v[210:213]\n"
      "v_max_f32 v90, v90, v138\n"
      "v_mov_b32 v138, v90\n"
      "v_mfma_f32_16x16x32_bf16 v[174:177], v[194:197], v[152:155], 0\n"
      "s_nop 0\n"
      "v_permlane32_swap_b32 v90, v138\n"
      "v_max_f32_e64 v90, v90, v138\n"
      "v_fma_f32 v185, v180, v180, s52\n"
      "v_mfma_f32_16x16x32_bf16 v[174:177], v[76:79], v[92:95], v[174:177]\n"
      "s_waitcnt lgkmcnt(1)\n"
      "v_fmac_f32 v185, v181, v181\n"
      "v_fmac_f32_e64 v129, v206, v206\n"
      "v_mfma_f32_16x16x32_bf16 v[160:163], v[140:143], v[190:193], v[30:33]\n"
      "v_fmac_f32 v129, v207, v207\n"
      "v_rcp_f32 v210, v210\n"
      "v_mfma_f32_16x16x32_bf16 v[190:193], v[214:217], v[152:155], 0\n"
      "ds_read_b128 v[136:139], v2 offset:18944\n"
      "v_fmac_f32 v129, v208, v208\n"
      "v_fmac_f32 v129, v209, v209\n"
      "v_mfma_f32_16x16x32_bf16 v[190:193], v[84:87], v[92:95], v[190:193]\n"
      "ds_write_b64 v4, v[202:203]\n"
      "v_mfma_f32_16x16x16_bf16 v[160:163], v[88:89], v[170:171], v[160:163]\n"
      "v_sub_f32 v188, v188, v90\n"
      "v_sub_f32 v189, v189, v90\n"
      "v_sub_f32 v148, v148, v90\n"
      "v_mfma_f32_16x16x32_bf16 v[108:111], v[140:143], v[120:123], v[30:33]\n"
      "v_sub_f32 v150, v150, v90\n"
      "v_sub_f32 v200, v200, v90\n"
      "v_sub_f32 v201, v201, v90\n"
      "v_sub_f32 v198, v198, v90\n"
      "v_fmac_f32 v185, v182, v182\n"
      "v_mfma_f32_16x16x32_bf16 v[202:205], v[140:143], v[156:159], v[30:33]\n"
      "ds_read_b128 v[156:159], v2 offset:22016\n"
      "v_sub_f32_e64 v151, v151, v90\n"
      "v_mfma_f32_16x16x16_bf16 v[108:111], v[88:89], v[218:219], v[108:111]\n"
      "v_sub_f32 v128, v128, v90\n"
      "v_sub_f32 v186, v186, v90\n"
      "v_fmac_f32 v185, v183, v183\n"
      "v_mfma_f32_16x16x32_bf16 v[144:147], v[140:143], v[104:107], v[30:33]\n"
      "s_waitcnt lgkmcnt(2)\n"
      "v_sub_f32 v187, v187, v90\n"
      "v_mul_f32 v191, v210, v191\n"
      "v_mul_f32 v190, v210, v190\n"
      "v_exp_f32 v187, v187\n"
      "v_mfma_f32_16x16x32_bf16 v[102:105], v[132:135], v[152:155], 0\n"
      "ds_read_b128 v[152:155], v2 offset:25088\n"
      "v_cndmask_b32 v160, v160, v108, s[58:59]\n"
      "v_mfma_f32_16x16x32_bf16 v[102:105], v[80:83], v[92:95], v[102:105]\n"
      "ds_read_b128 v[96:99], v2 offset:28160\n"
      "v_fmac_f32 v185, v124, v124\n"
      "v_fmac_f32 v185, v125, v125\n"
      "v_mfma_f32_16x16x16_bf16 v[144:147], v[88:89], v[100:101], v[144:147]\n"
      "v_mul_f32 v175, v210, v175\n"
      "v_sub_f32 v199, v199, v90\n"
      "v_cvt_pk_bf16_f32 v190, v190, v191\n"
      "v_sub_f32_e64 v149, v149, v90\n"
      "v_mfma_f32_16x16x32_bf16 v[108:111], a[240:243], v[136:139], 0\n"
      "ds_read_b128 v[170:173], v2 offset:31232\n"
      "v_exp_f32 v150, v150\n"
      "v_mfma_f32_16x16x16_bf16 v[202:205], v[88:89], v[112:113], v[202:205]\n"
      "v_exp_f32 v188, v188\n"
      "v_mul_f32 v176, v210, v176\n"
      "v_mfma_f32_16x16x32_bf16 v[140:143], v[232:235], v[136:139], 0\n"
      "s_waitcnt lgkmcnt(3)\n"
      "v_exp_f32 v198, v198\n"
      "v_cndmask_b32 v160, v160, v144, s[60:61]\n"
      "v_fmac_f32 v185, v126, v126\n"
      "v_mfma_f32_16x16x32_bf16 v[144:147], a[216:219], v[136:139], 0\n"
      "ds_read_b128 v[120:123], v2 offset:34304\n"
      "v_cndmask_b32 v160, v160, v202, s[62:63]\n"
      "v_mfma_f32_16x16x32_bf16 v[144:147], a[220:223], v[156:159], v[144:147]\n"
      "s_waitcnt lgkmcnt(2)\n"
      "v_fmac_f32 v129, v220, v220\n"
      "v_mul_f32 v177, v210, v177\n"
      "v_exp_f32 v148, v148\n"
      "v_mul_f32 v174, v210, v174\n"
      "v_mfma_f32_16x16x32_bf16 v[144:147], a[224:227], v[152:155], v[144:147]\n"
      "s_waitcnt lgkmcnt(1)\n"
      "v_exp_f32 v199, v199\n"
      "v_mfma_f32_16x16x32_bf16 v[144:147], a[228:231], v[96:99], v[144:147]\n"
      "s_add_u32 s43, s43, 1\n"
      "v_max_f32_dpp v88, v160, v160 row_ror:8 row_mask:0xf bank_mask:0xf\n"
      "v_fmac_f32 v129, v221, v221\n"
      "v_mul_f32 v192, v210, v192\n"
      "v_exp_f32 v151, v151\n"
      "v_exp_f32 v201, v201\n"
      "v_mfma_f32_16x16x32_bf16 v[144:147], a[232:235], v[170:173], v[144:147]\n"
      "s_waitcnt lgkmcnt(0)\n"
      "v_mul_f32 v193, v210, v193\n"
      "v_cvt_pk_bf16_f32 v174, v174, v175\n"
      "v_mfma_f32_16x16x32_bf16 v[144:147], a[236:239], v[120:123], v[144:147]\n"
      "s_cmp_ge_u32 s95, 0x40000000\n"
      "s_cselect_b32 s94, -1, -2\n"
      "v_exp_f32 v149, v149\n"
      "v_mfma_f32_16x16x32_bf16 v[108:111], a[244:247], v[156:159], v[108:111]\n"
      "v_fmac_f32 v185, v127, v127\n"
      "v_max_f32_dpp v88, v88, v88 row_ror:4 row_mask:0xf bank_mask:0xf\n"
      "v_mfma_f32_16x16x32_bf16 v[108:111], a[248:251], v[152:155], v[108:111]\n"
      "s_nop 0\n"
      "v_max_f32_dpp v88, v88, v88 row_ror:2 row_mask:0xf bank_mask:0xf\n"
      "v_exp_f32 v186, v186\n"
      "v_mul_f32 v105, v210, v105\n"
      "v_fmac_f32 v129, v222, v222\n"
      "v_mul_f32_e64 v104, v210, v104\n"
      "v_fma_f32 v184, v144, v144, s52\n"
      "v_cvt_pk_bf16_f32 v175, v176, v177\n"
      "v_max_f32_dpp v88, v88, v88 row_ror:1 row_mask:0xf bank_mask:0xf\n"
      "v_mfma_f32_16x16x32_bf16 v[108:111], a[252:255], v[96:99], v[108:111]\n"
      "v_exp_f32 v189, v189\n"
      "v_mul_f32 v102, v210, v102\n"
      "v_exp_f32_e64 v128, v128\n"
      "v_mfma_f32_16x16x32_bf16 v[108:111], v[224:227], v[170:173], v[108:111]\n"
      "v_mul_f32 v103, v210, v103\n"
      "v_fmac_f32 v129, v223, v223\n"
      "v_fmac_f32_e64 v185, v166, v166\n"
      "v_mfma_f32_16x16x32_bf16 v[108:111], v[228:231], v[120:123], v[108:111]\n"
      "v_cvt_pk_bf16_f32 v186, v186, v187\n"
      "v_exp_f32_e64 v200, v200\n"
      "v_mfma_f32_16x16x32_bf16 v[140:143], v[236:239], v[156:159], v[140:143]\n"
      "v_cvt_pk_bf16_f32 v187, v188, v189\n"
      "v_cvt_pk_bf16_f32 v90, v128, 0\n"
      "v_cvt_pk_bf16_f32 v177, v104, v105\n"
      "v_max_f32_dpp v88, v88, v88 row_bcast:15 row_mask:0xa bank_mask:0xf\n"
      "v_fmac_f32_e64 v185, v167, v167\n"
      "v_cvt_pk_bf16_f32 v176, v102, v103\n"
      "v_fmac_f32 v184, v145, v145\n"
      "v_mfma_f32_16x16x32_bf16 v[140:143], v[240:243], v[152:155], v[140:143]\n"
      "v_fmac_f32 v184, v146, v146\n"
      "v_max_f32_dpp v88, v88, v88 row_bcast:31 row_mask:0xc bank_mask:0xf\n"
      "v_fmac_f32 v184, v147, v147\n"
      "v_fmac_f32 v184, v108, v108\n"
      "v_cvt_pk_bf16_f32 v89, v150, v151\n"
      "v_fmac_f32 v185, v168, v168\n"
      "v_fmac_f32 v184, v109, v109\n"
      "v_readlane_b32 s72, v88, 63\n"
      "v_cvt_pk_bf16_f32 v191, v192, v193\n"
      "v_mfma_f32_16x16x32_bf16 v[140:143], v[244:247], v[96:99], v[140:143]\n"
      "ds_write_b64 v4, v[190:191] offset:256\n"
      "v_cvt_pk_bf16_f32 v188, v198, v199\n"
      "v_mfma_f32_16x16x32_bf16 v[140:143], v[248:251], v[170:173], v[140:143]\n"
      "v_fmac_f32 v185, v169, v169\n"
      "v_subrev_f32 v160, s72, v160\n"
      "v_cvt_pk_bf16_f32 v189, v200, v201\n"
      "v_mfma_f32_16x16x32_bf16 v[140:143], v[252:255], v[120:123], v[140:143]\n"
      "v_exp_f32 v160, v160\n"
      "v_mfma_f32_16x16x32_bf16 v[190:193], v[32:35], v[186:189], 0\n"
      "v_cvt_pk_bf16_f32 v88, v148, v149\n"
      "v_fmac_f32 v184, v110, v110\n"
      "v_cvt_pk_bf16_f32 v160, v160, v160\n"
      "v_mfma_f32_16x16x32_bf16 v[190:193], v[32:35], v[88:91], v[190:193]\n"
      "ds_write_b16 v13, v160\n"
      "v_mfma_f32_16x16x32_bf16 v[102:105], v[194:197], v[186:189], 0\n"
      "ds_read_b128 v[210:213], v14\n"
      "v_fmac_f32 v184, v111, v111\n"
      "v_permlane16_swap_b32 v129, v185\n"
      "v_add_f32 v129, v129, v185\n"
      "v_mfma_f32_16x16x32_bf16 v[102:105], v[76:79], v[88:91], v[102:105]\n"
      "ds_write_b128 v3, v[174:177] offset:256\n"
      "v_rcp_f32 v190, v190\n"
      "v_fmac_f32 v184, v140, v140\n"
      "v_mfma_f32_16x16x32_bf16 v[202:205], v[132:135], v[186:189], 0\n"
      "v_fmac_f32 v184, v141, v141\n"
      "v_fmac_f32 v184, v142, v142\n"
      "v_fmac_f32 v184, v143, v143\n"
      "v_mfma_f32_16x16x32_bf16 v[202:205], v[80:83], v[88:91], v[202:205]\n"
      "s_cmp_gt_u32 s42, s44\n"
      "s_cselect_b32 s95, s94, s95\n"
      "v_mul_f32 v102, v190, v102\n"
      "v_mov_b32 v185, v184\n"
      "v_mfma_f32_16x16x32_bf16 v[174:177], v[214:217], v[186:189], 0\n"
      "s_waitcnt lgkmcnt(1)\n"
      "v_mul_f32 v105, v190, v105\n"
      "v_permlane16_swap_b32 v184, v185\n"
      "v_add_f32 v184, v184, v185\n"
      "v_mul_f32 v103, v190, v103\n"
      "v_mul_f32 v205, v190, v205\n"
      "v_mul_f32 v204, v190, v204\n"
      "v_cvt_pk_bf16_f32 v102, v102, v103\n"
      "v_mul_f32 v104, v190, v104\n"
      "v_permlane32_swap_b32 v129, v184\n"
      "v_mul_f32 v202, v190, v202\n"
      "v_add_f32_e64 v129, v129, v184\n"
      "v_mfma_f32_16x16x32_bf16 v[174:177], v[84:87], v[88:91], v[174:177]\n"
      "ds_write_b32 v31, v129\n"
      "v_cvt_pk_bf16_f32 v103, v104, v105\n"
      "v_mul_f32 v203, v190, v203\n"
      "v_cvt_pk_bf16_f32 v104, v202, v203\n"
      "v_cvt_pk_bf16_f32 v105, v204, v205\n"
      "s_add_u32 s42, s42, 1\n"
      "ds_write_b128 v3, v[102:105] offset:512\n"
      "ds_read_b128 v[184:187], v14 offset:64\n"
      "v_mul_f32 v174, v190, v174\n"
      "v_mul_f32 v177, v190, v177\n"
      "v_mul_f32 v175, v190, v175\n"
      "v_mul_f32 v176, v190, v176\n"
      "v_cvt_pk_bf16_f32 v174, v174, v175\n"
      "v_cvt_pk_bf16_f32 v175, v176, v177\n"
      "v_mfma_f32_16x16x32_bf16 v[188:191], v[210:213], v[32:35], 0\n"
      "ds_write_b64 v4, v[174:175] offset:512\n"
      "s_waitcnt lgkmcnt(1)\n"
      "v_mfma_f32_16x16x32_bf16 v[188:191], v[184:187], v[32:35], v[188:191]\n"
      "s_cmp_ge_u32 s95, 0x40000000\n"
      "s_cselect_b64 vcc, -1, s[56:57]\n"
      "s_waitcnt lgkmcnt(0)\n"
      "s_barrier\n"
      "s_cbranch_vccz L_max1_%=\n"
      "L_cold_%=:\n"
      "L_disp1_%=:\n"
      "s_cmp_lt_u32 s95, 0x40000000\n"
      "s_cbranch_scc1 L_nofix_11_%=\n"
      "s_cmp_eq_u32 s95, -2\n"
      "s_cbranch_scc1 L_tail1_%=\n"
      "s_nop 2\n"
      "v_lshlrev_b32 v202, 3, v36\n"
      "s_add_u32 s72, s98, L_cold_%=-L_ipf0_%=\n"
      "s_addc_u32 s73, s99, 0\n"
      "s_mov_b32 s74, L_cend_%=-L_cold_%=\n"
      "s_mov_b32 s75, 0x20000\n"
      "s_mov_b32 m0, 161792\n"
      "s_nop 0\n"
      "buffer_load_dwordx4 v202, s[72:75], 0 offen  lds\n"
      "s_lshr_b32 s92, s45, 30\n"
      "s_or_b32 s92, s92, s66\n"
      "s_cbranch_scc0 L_nf_12_%=\n"
      "s_bitcmp1_b32 s45, 31\n"
      "s_cselect_b32 s75, 100270464, 0\n"
      "s_bitcmp1_b32 s45, 30\n"
      "s_cselect_b32 s76, 196224, 0\n"
      "s_and_b32 s74, s45, 0x3fffffff\n"
      "s_cmp_ge_u32 s45, 0x40000000\n"
      "s_cselect_b32 s74, s74, 0\n"
      "s_cselect_b32 s45, 0, s45\n"
      "s_sub_u32 s74, s74, s66\n"
      "s_add_u32 s66, s66, s74\n"
      "s_sub_u32 s75, s75, s67\n"
      "s_add_u32 s67, s67, s75\n"
      "s_sub_u32 s76, s76, s68\n"
      "s_add_u32 s68, s68, s76\n"
      "v_add_u32 v16, s74, v16\n"
      "v_add_u32 v17, s74, v17\n"
      "v_add_u32 v18, s74, v18\n"
      "v_add_u32 v19, s74, v19\n"
      "v_add_u32 v15, s74, v15\n"
      "s_cmp_eq_u32 s75, 0\n"
      "s_cbranch_scc1 L_ax_13_%=\n"
      "v_bfe_i32 v202, v27, 0, 1\n"
      "v_and_b32 v202, s75, v202\n"
      "v_sub_u32 v16, v16, v202\n"
      "v_bfe_i32 v202, v27, 2, 1\n"
      "v_and_b32 v202, s75, v202\n"
      "v_sub_u32 v17, v17, v202\n"
      "v_bfe_i32 v202, v27, 4, 1\n"
      "v_and_b32 v202, s75, v202\n"
      "v_sub_u32 v18, v18, v202\n"
      "v_bfe_i32 v202, v27, 6, 1\n"
      "v_and_b32 v202, s75, v202\n"
      "v_sub_u32 v19, v19, v202\n"
      "v_bfe_i32 v202, v27, 8, 1\n"
      "v_and_b32 v202, s75, v202\n"
      "v_sub_u32 v15, v15, v202\n"
      "L_ax_13_%=:\n"
      "s_cmp_eq_u32 s76, 0\n"
      "s_cbranch_scc1 L_ax_14_%=\n"
      "v_bfe_i32 v202, v27, 1, 1\n"
      "v_and_b32 v202, s76, v202\n"
      "v_sub_u32_e64 v16, v16, v202\n"
      "v_bfe_i32 v202, v27, 3, 1\n"
      "v_and_b32 v202, s76, v202\n"
      "v_sub_u32 v17, v17, v202\n"
      "v_bfe_i32 v202, v27, 5, 1\n"
      "v_and_b32 v202, s76, v202\n"
      "v_sub_u32 v18, v18, v202\n"
      "v_bfe_i32 v202, v27, 7, 1\n"
      "v_and_b32 v202, s76, v202\n"
      "v_sub_u32 v19, v19, v202\n"
      "v_bfe_i32 v202, v27, 9, 1\n"
      "v_and_b32_e64 v202, s76, v202\n"
      "v_sub_u32 v15, v15, v202\n"
      "L_ax_14_%=:\n"
      "s_or_b32 s48, s66, s69\n"
      "s_cmp_lg_u32 s48, 0\n"
      "s_cselect_b32 s48, 0x40000000, 0\n"
      "L_nf_12_%=:\n"
      "s_lshr_b32 s92, s46, 30\n"
      "s_or_b32 s92, s92, s69\n"
      "s_cbranch_scc0 L_nf_15_%=\n"
      "s_bitcmp1_b32 s46, 31\n"
      "s_cselect_b32 s75, 100270464, 0\n"
      "s_bitcmp1_b32 s46, 30\n"
      "s_cselect_b32 s76, 196224, 0\n"
      "s_and_b32 s74, s46, 0x3fffffff\n"
      "s_cmp_ge_u32 s46, 0x40000000\n"
      "s_cselect_b32 s74, s74, 0\n"
      "s_cselect_b32 s46, 0, s46\n"
      "s_sub_u32 s74, s74, s69\n"
      "s_add_u32 s69, s69, s74\n"
      "s_sub_u32 s75, s75, s70\n"
      "s_add_u32 s70, s70, s75\n"
      "s_sub_u32 s76, s76, s71\n"
      "s_add_u32 s71, s71, s76\n"
      "v_add_u32 v20, s74, v20\n"
      "v_add_u32 v21, s74, v21\n"
      "v_add_u32 v22, s74, v22\n"
      "v_add_u32 v23, s74, v23\n"
      "v_add_u32_e64 v25, s74, v25\n"
      "s_cmp_eq_u32 s75, 0\n"
      "s_cbranch_scc1 L_ax_16_%=\n"
      "v_bfe_i32 v202, v27, 10, 1\n"
      "v_and_b32 v202, s75, v202\n"
      "v_sub_u32 v20, v20, v202\n"
      "v_bfe_i32 v202, v27, 12, 1\n"
      "v_and_b32 v202, s75, v202\n"
      "v_sub_u32 v21, v21, v202\n"
      "v_bfe_i32 v202, v27, 14, 1\n"
      "v_and_b32 v202, s75, v202\n"
      "v_sub_u32 v22, v22, v202\n"
      "v_bfe_i32 v202, v27, 16, 1\n"
      "v_and_b32 v202, s75, v202\n"
      "v_sub_u32 v23, v23, v202\n"
      "v_bfe_i32 v202, v27, 18, 1\n"
      "v_and_b32 v202, s75, v202\n"
      "v_sub_u32 v25, v25, v202\n"
      "L_ax_16_%=:\n"
      "s_cmp_eq_u32 s76, 0\n"
      "s_cbranch_scc1 L_ax_17_%=\n"
      "v_bfe_i32 v202, v27, 11, 1\n"
      "v_and_b32 v202, s76, v202\n"
      "v_sub_u32 v20, v20, v202\n"
      "v_bfe_i32 v202, v27, 13, 1\n"
      "v_and_b32 v202, s76, v202\n"
      "v_sub_u32 v21, v21, v202\n"
      "v_bfe_i32 v202, v27, 15, 1\n"
      "v_and_b32 v202, s76, v202\n"
      "v_sub_u32 v22, v22, v202\n"
      "v_bfe_i32 v202, v27, 17, 1\n"
      "v_and_b32 v202, s76, v202\n"
      "v_sub_u32 v23, v23, v202\n"
      "v_bfe_i32 v202, v27, 19, 1\n"
      "v_and_b32 v202, s76, v202\n"
      "v_sub_u32 v25, v25, v202\n"
      "L_ax_17_%=:\n"
      "s_or_b32 s48, s66, s69\n"
      "s_cmp_lg_u32 s48, 0\n"
      "s_cselect_b32 s48, 0x40000000, 0\n"
      "L_nf_15_%=:\n"
      "s_sub_u32 s92, s42, 1\n"
      "s_cmp_gt_u32 s92, s44\n"
      "s_cbranch_scc1 L_tail1_%=\n"
      "L_nofix_11_%=:\n"
      "s_cmp_lg_u64 s[56:57], 0\n"
      "s_cbranch_scc1 L_slow1_%=\n"
      "L_sel1_%=:\n"
      "s_cmp_lg_u64 s[54:55], 0\n"
      "s_cbranch_scc1 L_max1_%=\n"
      "s_branch L_loop1_%=\n"
      "L_slow1_%=:\n"
      "s_nop 4\n"
      "v_mfma_f32_16x16x32_bf16 v[202:205], v[210:213], v[194:197], 0\n"
      "ds_read_b128 v[152:155], v0 offset:18432\n"
      "v_subrev_u32 v196, 115456, v9\n"
      "v_mfma_f32_16x16x32_bf16 v[202:205], v[184:187], v[76:79], v[202:205]\n"
      "ds_read_b128 v[128:131], v1 offset:18432\n"
      "v_subrev_u32 v178, 115456, v9\n"
      "v_mfma_f32_16x16x32_bf16 v[102:105], v[210:213], v[132:135], 0\n"
      "ds_read_b128 v[120:123], v0 offset:24576\n"
      "v_bfe_u32 v78, v178, 4, 4\n"
      "v_mfma_f32_16x16x32_bf16 v[102:105], v[184:187], v[80:83], v[102:105]\n"
      "ds_read_b128 v[174:177], v9\n"
      "v_mul_u32_u24 v82, 98, v78\n"
      "v_mfma_f32_16x16x32_bf16 v[192:195], v[210:213], v[214:217], 0\n"
      "s_add_u32 s72, s42, 1\n"
      "v_lshrrev_b32 v178, 8, v178\n"
      "v_readlane_b32 s77, v38, s72\n"
      "v_mfma_f32_16x16x32_bf16 v[192:195], v[184:187], v[84:87], v[192:195]\n"
      "s_mov_b32 m0, 110592\n"
      "v_mov_b32_dpp v188, v188 quad_perm:[0,0,0,0] row_mask:0xf bank_mask:0xf\n"
      "v_lshl_add_u32 v82, v178, 3, v82\n"
      "v_lshlrev_b32 v178, 2, v178\n"
      "s_waitcnt lgkmcnt(3)\n"
      "v_mfma_f32_16x16x32_bf16 v[136:139], a[0:3], v[152:155], 0\n"
      "s_and_b32 s73, s77, -16\n"
      "v_rcp_f32 v188, v188\n"
      "v_mfma_f32_16x16x32_bf16 v[184:187], a[24:27], v[152:155], 0\n"
      "buffer_load_dwordx4 v196, s[28:31], s73 offen  lds\n"
      "v_mfma_f32_16x16x32_bf16 v[104:107], a[48:51], v[152:155], 0\n"
      "s_mov_b32 m0, 111616\n"
      "s_waitcnt lgkmcnt(0)\n"
      "v_add_f32 v176, v176, v177\n"
      "v_mfma_f32_16x16x32_bf16 v[148:151], a[72:75], v[152:155], 0\n"
      "ds_read_b128 v[170:173], v1 offset:24576\n"
      "v_add_f32 v174, v174, v175\n"
      "v_mfma_f32_16x16x32_bf16 v[160:163], a[96:99], v[152:155], 0\n"
      "ds_read_b128 v[198:201], v0 offset:30720\n"
      "v_add_f32 v174, v174, v176\n"
      "v_mfma_f32_16x16x32_bf16 v[96:99], a[120:123], v[152:155], 0\n"
      "v_rsq_f32 v174, v174\n"
      "v_mfma_f32_16x16x32_bf16 v[136:139], a[4:7], v[128:131], v[136:139]\n"
      "s_add_u32 s73, s73, 1024\n"
      "v_mul_f32 v174, 0x415db3d7, v174\n"
      "v_mov_b32 v176, v174\n"
      "v_mfma_f32_16x16x32_bf16 v[136:139], a[8:11], v[120:123], v[136:139]\n"
      "buffer_load_dwordx4 v196, s[28:31], s73 offen  lds\n"
      "v_mfma_f32_16x16x32_bf16 v[184:187], a[28:31], v[128:131], v[184:187]\n"
      "s_mov_b32 m0, 112640\n"
      "v_permlane32_swap_b32 v174, v176\n"
      "v_mul_f32 v147, v176, v147\n"
      "v_mfma_f32_16x16x32_bf16 v[184:187], a[32:35], v[120:123], v[184:187]\n"
      "ds_read_b128 v[156:159], v1 offset:30720\n"
      "v_mov_b32_e64 v175, v174\n"
      "v_mfma_f32_16x16x32_bf16 v[104:107], a[52:55], v[128:131], v[104:107]\n"
      "s_add_u32 s73, s73, 1024\n"
      "v_permlane16_swap_b32_e64 v174, v175\n"
      "v_mfma_f32_16x16x32_bf16 v[104:107], a[56:59], v[120:123], v[104:107]\n"
      "buffer_load_dwordx4 v196, s[28:31], s73 offen  lds\n"
      "v_mfma_f32_16x16x32_bf16 v[148:151], a[76:79], v[128:131], v[148:151]\n"
      "s_mov_b32 m0, 113664\n"
      "v_mul_f32 v208, v174, v208\n"
      "v_mul_f32 v125, v175, v125\n"
      "v_mfma_f32_16x16x32_bf16 v[148:151], a[80:83], v[120:123], v[148:151]\n"
      "s_add_u32 s73, s73, 1024\n"
      "v_mul_f32 v116, v174, v116\n"
      "v_mul_f32 v220, v174, v220\n"
      "v_mfma_f32_16x16x32_bf16 v[160:163], a[100:103], v[128:131], v[160:163]\n"
      "v_cndmask_b32 v202, v202, v102, s[58:59]\n"
      "v_mul_f32 v115, v174, v115\n"
      "v_mfma_f32_16x16x32_bf16 v[160:163], a[104:107], v[120:123], v[160:163]\n"
      "buffer_load_dwordx4 v196, s[28:31], s73 offen  lds\n"
      "v_mul_f32 v124, v175, v124\n"
      "v_mfma_f32_16x16x32_bf16 v[96:99], a[124:127], v[128:131], v[96:99]\n"
      "s_add_u32 s73, s73, 1024\n"
      "v_mul_f32 v114, v174, v114\n"
      "v_mul_f32 v221, v174, v221\n"
      "v_mfma_f32_16x16x32_bf16 v[96:99], a[128:131], v[120:123], v[96:99]\n"
      "s_waitcnt lgkmcnt(0)\n"
      "v_mul_f32 v206, v174, v206\n"
      "v_mul_f32 v183, v175, v183\n"
      "v_mfma_f32_16x16x32_bf16 v[136:139], a[12:15], v[170:173], v[136:139]\n"
      "s_mov_b32 m0, 114688\n"
      "v_mul_f32 v209, v174, v209\n"
      "v_cvt_pk_bf16_f32 v114, v114, v115\n"
      "v_mfma_f32_16x16x32_bf16 v[136:139], a[16:19], v[198:201], v[136:139]\n"
      "v_mul_f32 v222, v174, v222\n"
      "v_mul_f32 v117, v174, v117\n"
      "v_mfma_f32_16x16x32_bf16 v[136:139], a[20:23], v[156:159], v[136:139]\n"
      "s_mov_b32 exec_hi, 0xffff\n"
      "buffer_load_dwordx4 v196, s[28:31], s73 offen  lds\n"
      "s_mov_b32 exec_hi, -1\n"
      "v_mfma_f32_16x16x32_bf16 v[184:187], a[36:39], v[170:173], v[184:187]\n"
      "s_add_u32 m0, s64, 0\n"
      "v_mul_f32 v207, v174, v207\n"
      "v_cvt_pk_bf16_f32 v115, v116, v117\n"
      "v_mfma_f32_16x16x32_bf16 v[184:187], a[40:43], v[198:201], v[184:187]\n"
      "s_and_b32 s77, s77, 15\n"
      "v_cvt_pk_bf16_f32 v116, v206, v207\n"
      "v_cvt_pk_bf16_f32 v220, v220, v221\n"
      "v_cvt_pk_bf16_f32 v117, v208, v209\n"
      "v_mfma_f32_16x16x32_bf16 v[184:187], a[44:47], v[156:159], v[184:187]\n"
      "ds_write_b128 v5, v[114:117] offset:18432\n"
      "v_mfma_f32_16x16x32_bf16 v[104:107], a[60:63], v[170:173], v[104:107]\n"
      "buffer_load_dwordx4 v16, s[16:19], s45 offen  lds\n"
      "v_mfma_f32_16x16x32_bf16 v[104:107], a[64:67], v[198:201], v[104:107]\n"
      "v_mul_f32 v181, v175, v181\n"
      "v_mul_f32 v223, v174, v223\n"
      "v_cvt_pk_bf16_f32 v221, v222, v223\n"
      "v_mfma_f32_16x16x32_bf16 v[104:107], a[68:71], v[156:159], v[104:107]\n"
      "ds_write_b64 v6, v[220:221] offset:18432\n"
      "v_mfma_f32_16x16x32_bf16 v[148:151], a[84:87], v[170:173], v[148:151]\n"
      "s_add_u32 m0, s64, 4096\n"
      "v_mul_f32 v126, v175, v126\n"
      "v_mul_f32 v127, v175, v127\n"
      "v_mfma_f32_16x16x32_bf16 v[148:151], a[88:91], v[198:201], v[148:151]\n"
      "buffer_load_dwordx4 v17, s[16:19], s45 offen  lds\n"
      "v_mfma_f32_16x16x32_bf16 v[148:151], a[92:95], v[156:159], v[148:151]\n"
      "ds_read_b128 v[114:117], v1 offset:26624\n"
      "v_mul_f32_e64 v167, v175, v167\n"
      "v_mfma_f32_16x16x32_bf16 v[160:163], a[108:111], v[170:173], v[160:163]\n"
      "s_add_u32 m0, s64, 8192\n"
      "v_mul_f32 v166, v175, v166\n"
      "v_mul_f32 v180, v175, v180\n"
      "v_mfma_f32_16x16x32_bf16 v[160:163], a[112:115], v[198:201], v[160:163]\n"
      "v_mul_f32_e64 v169, v175, v169\n"
      "v_cvt_pk_bf16_f32 v180, v180, v181\n"
      "v_mfma_f32_16x16x32_bf16 v[160:163], a[116:119], v[156:159], v[160:163]\n"
      "buffer_load_dwordx4 v18, s[16:19], s45 offen  lds\n"
      "v_mfma_f32_16x16x32_bf16 v[96:99], a[132:135], v[170:173], v[96:99]\n"
      "v_mul_f32_e64 v182, v175, v182\n"
      "v_cvt_pk_bf16_f32 v181, v182, v183\n"
      "v_mfma_f32_16x16x32_bf16 v[96:99], a[136:139], v[198:201], v[96:99]\n"
      "s_add_u32 m0, s64, 12288\n"
      "v_cvt_pk_bf16_f32 v183, v126, v127\n"
      "v_mul_f32_e64 v168, v175, v168\n"
      "v_mfma_f32_16x16x32_bf16 v[96:99], a[140:143], v[156:159], v[96:99]\n"
      "v_cvt_pk_bf16_f32 v166, v166, v167\n"
      "v_cvt_pk_bf16_f32 v182, v124, v125\n"
      "v_mfma_f32_16x16x32_bf16 v[194:197], v[152:155], a[144:147], 0\n"
      "buffer_load_dwordx4 v19, s[16:19], s45 offen  lds\n"
      "v_mfma_f32_16x16x32_bf16 v[194:197], v[128:131], a[148:151], v[194:197]\n"
      "s_add_u32 m0, s65, 16384\n"
      "v_cvt_pk_bf16_f32 v167, v168, v169\n"
      "v_mul_f32_e64 v145, v176, v145\n"
      "v_mfma_f32_16x16x32_bf16 v[194:197], v[120:123], a[152:155], v[194:197]\n"
      "ds_write_b128 v5, v[180:183] offset:24576\n"
      "v_mfma_f32_16x16x32_bf16 v[194:197], v[170:173], a[156:159], v[194:197]\n"
      "buffer_load_dwordx4 v15, s[16:19], s45 offen  lds\n"
      "v_mfma_f32_16x16x32_bf16 v[194:197], v[198:201], a[160:163], v[194:197]\n"
      "ds_read_b128 v[206:209], v0 offset:20480\n"
      "v_mul_f32_e64 v144, v176, v144\n"
      "v_mfma_f32_16x16x32_bf16 v[194:197], v[156:159], a[164:167], v[194:197]\n"
      "ds_write_b64 v6, v[166:167] offset:24576\n"
      "v_mfma_f32_16x16x32_bf16 v[132:135], v[152:155], a[168:171], 0\n"
      "ds_read_b128 v[220:223], v1 offset:20480\n"
      "v_mul_f32_e64 v146, v176, v146\n"
      "v_mfma_f32_16x16x32_bf16 v[132:135], v[128:131], a[172:175], v[132:135]\n"
      "ds_read_b128 v[166:169], v0 offset:26624\n"
      "v_mul_f32_e64 v111, v176, v111\n"
      "v_mfma_f32_16x16x32_bf16 v[132:135], v[120:123], a[176:179], v[132:135]\n"
      "v_cvt_pk_bf16_f32 v144, v144, v145\n"
      "v_fma_f32 v179, v136, v136, s51\n"
      "v_mfma_f32_16x16x32_bf16 v[132:135], v[170:173], a[180:183], v[132:135]\n"
      "ds_read_b128 v[180:183], v0 offset:32768\n"
      "v_mul_f32_e64 v140, v176, v140\n"
      "v_mfma_f32_16x16x32_bf16 v[132:135], v[198:201], a[184:187], v[132:135]\n"
      "ds_read_b128 v[124:127], v1 offset:32768\n"
      "v_cvt_pk_bf16_f32 v194, v194, v195\n"
      "v_mfma_f32_16x16x32_bf16 v[132:135], v[156:159], a[188:191], v[132:135]\n"
      "buffer_load_dwordx4 v[210:213], v36, s[28:31], s47 offen\n"
      "v_mfma_f32_16x16x32_bf16 v[214:217], v[152:155], a[192:195], 0\n"
      "v_fmac_f32_e64 v179, v137, v137\n"
      "v_fma_f32 v175, v148, v148, s51\n"
      "v_mfma_f32_16x16x32_bf16 v[214:217], v[128:131], a[196:199], v[214:217]\n"
      "v_mul_f32 v109, v176, v109\n"
      "v_cvt_pk_bf16_f32 v145, v146, v147\n"
      "v_mfma_f32_16x16x32_bf16 v[214:217], v[120:123], a[200:203], v[214:217]\n"
      "v_fmac_f32 v179, v138, v138\n"
      "v_fmac_f32 v175, v149, v149\n"
      "v_mfma_f32_16x16x32_bf16 v[214:217], v[170:173], a[204:207], v[214:217]\n"
      "buffer_load_dword v86, v29, s[28:31], s47 offen\n"
      "v_mfma_f32_16x16x32_bf16 v[214:217], v[198:201], a[208:211], v[214:217]\n"
      "v_fmac_f32 v179, v139, v139\n"
      "v_mul_f32 v108, v176, v108\n"
      "v_mfma_f32_16x16x32_bf16 v[214:217], v[156:159], a[212:215], v[214:217]\n"
      "s_waitcnt lgkmcnt(0)\n"
      "v_fmac_f32 v179, v184, v184\n"
      "v_fmac_f32 v175, v150, v150\n"
      "v_fmac_f32_e64 v175, v151, v151\n"
      "v_mfma_f32_16x16x32_bf16 v[100:103], a[0:3], v[206:209], 0\n"
      "v_fmac_f32 v175, v160, v160\n"
      "v_cndmask_b32 v202, v202, v192, s[60:61]\n"
      "v_mfma_f32_16x16x32_bf16 v[100:103], a[4:7], v[220:223], v[100:103]\n"
      "v_fmac_f32 v175, v161, v161\n"
      "v_mul_f32 v141, v176, v141\n"
      "v_mfma_f32_16x16x32_bf16 v[100:103], a[8:11], v[166:169], v[100:103]\n"
      "v_fmac_f32 v175, v162, v162\n"
      "v_fmac_f32_e64 v175, v163, v163\n"
      "v_mfma_f32_16x16x32_bf16 v[100:103], a[12:15], v[114:117], v[100:103]\n"
      "ds_read_b128 v[190:193], v1 offset:28672\n"
      "v_cvt_pk_bf16_f32 v132, v132, v133\n"
      "v_mul_f32 v110, v176, v110\n"
      "v_mfma_f32_16x16x32_bf16 v[100:103], a[16:19], v[180:183], v[100:103]\n"
      "v_fmac_f32 v179, v185, v185\n"
      "v_mul_f32 v142, v176, v142\n"
      "v_mfma_f32_16x16x32_bf16 v[100:103], a[20:23], v[124:127], v[100:103]\n"
      "v_fmac_f32 v175, v96, v96\n"
      "v_cvt_pk_bf16_f32 v195, v196, v197\n"
      "v_sub_u32_e64 v78, v78, v178\n"
      "v_mfma_f32_16x16x32_bf16 v[128:131], a[24:27], v[206:209], 0\n"
      "s_add_u32 s73, s73, 1024\n"
      "v_fmac_f32_e64 v175, v97, v97\n"
      "v_cvt_pk_bf16_f32 v133, v134, v135\n"
      "v_cvt_pk_bf16_f32 v147, v110, v111\n"
      "v_mfma_f32_16x16x32_bf16 v[128:131], a[28:31], v[220:223], v[128:131]\n"
      "v_fmac_f32_e64 v179, v186, v186\n"
      "v_cvt_pk_bf16_f32 v146, v108, v109\n"
      "v_mfma_f32_16x16x32_bf16 v[128:131], a[32:35], v[166:169], v[128:131]\n"
      "ds_write_b128 v5, v[144:147] offset:30720\n"
      "v_mfma_f32_16x16x32_bf16 v[128:131], a[36:39], v[114:117], v[128:131]\n"
      "v_fmac_f32 v179, v187, v187\n"
      "v_fmac_f32 v179, v104, v104\n"
      "v_fmac_f32 v175, v98, v98\n"
      "v_mfma_f32_16x16x32_bf16 v[128:131], a[40:43], v[180:183], v[128:131]\n"
      "v_fmac_f32 v179, v105, v105\n"
      "v_fmac_f32 v179, v106, v106\n"
      "v_mfma_f32_16x16x32_bf16 v[128:131], a[44:47], v[124:127], v[128:131]\n"
      "v_fmac_f32 v175, v99, v99\n"
      "v_fmac_f32 v179, v107, v107\n"
      "v_mul_f32 v143, v176, v143\n"
      "v_mul_f32 v202, v188, v202\n"
      "v_mfma_f32_16x16x32_bf16 v[144:147], a[48:51], v[206:209], 0\n"
      "v_permlane16_swap_b32 v179, v175\n"
      "v_mfma_f32_16x16x32_bf16 v[144:147], a[52:55], v[220:223], v[144:147]\n"
      "v_fma_f32 v188, v100, v100, s51\n"
      "v_fmac_f32 v188, v101, v101\n"
      "v_mfma_f32_16x16x32_bf16 v[144:147], a[56:59], v[166:169], v[144:147]\n"
      "v_add_f32 v179, v179, v175\n"
      "v_cvt_pk_bf16_f32 v140, v140, v141\n"
      "v_mfma_f32_16x16x32_bf16 v[144:147], a[60:63], v[114:117], v[144:147]\n"
      "v_lshlrev_b32 v78, 4, v78\n"
      "v_mov_b32 v175, v179\n"
      "v_mfma_f32_16x16x32_bf16 v[144:147], a[64:67], v[180:183], v[144:147]\n"
      "s_nop 0\n"
      "v_permlane32_swap_b32 v179, v175\n"
      "v_mfma_f32_16x16x32_bf16 v[144:147], a[68:71], v[124:127], v[144:147]\n"
      "v_cvt_pk_bf16_f32 v141, v142, v143\n"
      "v_add_f32 v179, v179, v175\n"
      "v_mfma_f32_16x16x32_bf16 v[152:155], a[72:75], v[206:209], 0\n"
      "v_rsq_f32 v179, v179\n"
      "v_fmac_f32 v188, v102, v102\n"
      "v_mfma_f32_16x16x32_bf16 v[152:155], a[76:79], v[220:223], v[152:155]\n"
      "v_cvt_pk_bf16_f32 v202, v202, v202\n"
      "v_mov_b32 v175, v179\n"
      "v_mfma_f32_16x16x32_bf16 v[152:155], a[80:83], v[166:169], v[152:155]\n"
      "ds_write_b64 v6, v[140:141] offset:30720\n"
      "v_mfma_f32_16x16x32_bf16 v[152:155], a[84:87], v[114:117], v[152:155]\n"
      "v_permlane16_swap_b32 v179, v175\n"
      "v_mfma_f32_16x16x32_bf16 v[152:155], a[88:91], v[180:183], v[152:155]\n"
      "v_mul_f32 v99, v175, v99\n"
      "v_fmac_f32_e64 v188, v103, v103\n"
      "v_mfma_f32_16x16x32_bf16 v[152:155], a[92:95], v[124:127], v[152:155]\n"
      "ds_write_b16 v12, v202\n"
      "v_mfma_f32_16x16x32_bf16 v[108:111], a[96:99], v[206:209], 0\n"
      "v_cvt_pk_bf16_f32 v214, v214, v215\n"
      "v_cvt_pk_bf16_f32 v215, v216, v217\n"
      "v_mfma_f32_16x16x32_bf16 v[108:111], a[100:103], v[220:223], v[108:111]\n"
      "v_mul_f32 v148, v175, v148\n"
      "v_mul_f32 v98, v175, v98\n"
      "v_mfma_f32_16x16x32_bf16 v[108:111], a[104:107], v[166:169], v[108:111]\n"
      "v_mul_f32 v149, v175, v149\n"
      "v_mul_f32 v96, v175, v96\n"
      "v_mfma_f32_16x16x32_bf16 v[108:111], a[108:111], v[114:117], v[108:111]\n"
      "v_fma_f32 v76, v152, v152, s51\n"
      "v_add_u32 v82, s77, v82\n"
      "v_mfma_f32_16x16x32_bf16 v[108:111], a[112:115], v[180:183], v[108:111]\n"
      "v_fmac_f32 v188, v128, v128\n"
      "v_fmac_f32 v188, v129, v129\n"
      "v_mfma_f32_16x16x32_bf16 v[108:111], a[116:119], v[124:127], v[108:111]\n"
      "v_fmac_f32 v76, v153, v153\n"
      "v_mul_f32 v161, v175, v161\n"
      "v_mfma_f32_16x16x32_bf16 v[202:205], a[120:123], v[206:209], 0\n"
      "v_mul_f32 v97, v175, v97\n"
      "v_mov_b32 v178, 0x3fb9\n"
      "v_mfma_f32_16x16x32_bf16 v[202:205], a[124:127], v[220:223], v[202:205]\n"
      "v_mul_f32 v162, v175, v162\n"
      "v_fmac_f32 v188, v130, v130\n"
      "v_mfma_f32_16x16x32_bf16 v[202:205], a[128:131], v[166:169], v[202:205]\n"
      "v_and_b32 v93, 2, v82\n"
      "v_fmac_f32 v188, v131, v131\n"
      "v_mfma_f32_16x16x32_bf16 v[202:205], a[132:135], v[114:117], v[202:205]\n"
      "v_mul_f32 v179, s50, v179\n"
      "v_mul_f32 v106, v179, v106\n"
      "v_mfma_f32_16x16x32_bf16 v[202:205], a[136:139], v[180:183], v[202:205]\n"
      "v_mul_f32 v105, v179, v105\n"
      "v_fmac_f32 v76, v154, v154\n"
      "v_mfma_f32_16x16x32_bf16 v[202:205], a[140:143], v[124:127], v[202:205]\n"
      "v_mul_f32 v160, v175, v160\n"
      "v_fmac_f32 v76, v155, v155\n"
      "v_mfma_f32_16x16x32_bf16 v[140:143], v[206:209], a[144:147], 0\n"
      "ds_read_b128 v[156:159], v0 offset:28672\n"
      "v_mul_f32_e64 v107, v179, v107\n"
      "v_mfma_f32_16x16x32_bf16 v[140:143], v[220:223], a[148:151], v[140:143]\n"
      "v_mul_f32 v151, v175, v151\n"
      "v_fmac_f32 v188, v144, v144\n"
      "v_mfma_f32_16x16x32_bf16 v[140:143], v[166:169], a[152:155], v[140:143]\n"
      "v_mul_f32 v104, v179, v104\n"
      "v_fmac_f32 v76, v108, v108\n"
      "v_mfma_f32_16x16x32_bf16 v[140:143], v[114:117], a[156:159], v[140:143]\n"
      "ds_read_b128 v[198:201], v1 offset:22528\n"
      "v_cvt_pk_bf16_f32 v96, v96, v97\n"
      "v_mfma_f32_16x16x32_bf16 v[140:143], v[180:183], a[160:163], v[140:143]\n"
      "v_cvt_pk_bf16_f32 v104, v104, v105\n"
      "v_mul_f32 v137, v179, v137\n"
      "v_mfma_f32_16x16x32_bf16 v[140:143], v[124:127], a[164:167], v[140:143]\n"
      "v_cvt_pk_bf16_f32 v97, v98, v99\n"
      "v_fmac_f32 v76, v109, v109\n"
      "v_fmac_f32 v76, v110, v110\n"
      "v_mfma_f32_16x16x32_bf16 v[216:219], v[206:209], a[168:171], 0\n"
      "v_fmac_f32 v76, v111, v111\n"
      "v_mul_f32 v136, v179, v136\n"
      "v_fmac_f32 v188, v145, v145\n"
      "v_mfma_f32_16x16x32_bf16 v[216:219], v[220:223], a[172:175], v[216:219]\n"
      "v_mul_f32 v184, v179, v184\n"
      "v_fmac_f32 v76, v202, v202\n"
      "v_mfma_f32_16x16x32_bf16 v[216:219], v[166:169], a[176:179], v[216:219]\n"
      "v_mul_f32 v150, v175, v150\n"
      "v_fmac_f32 v76, v203, v203\n"
      "v_fmac_f32_e64 v76, v204, v204\n"
      "v_mfma_f32_16x16x32_bf16 v[216:219], v[114:117], a[180:183], v[216:219]\n"
      "v_cvt_pk_bf16_f32 v148, v148, v149\n"
      "v_mul_f32 v139, v179, v139\n"
      "v_mfma_f32_16x16x32_bf16 v[216:219], v[180:183], a[184:187], v[216:219]\n"
      "v_fmac_f32 v188, v146, v146\n"
      "v_mul_f32_e64 v163, v175, v163\n"
      "v_mfma_f32_16x16x32_bf16 v[216:219], v[124:127], a[188:191], v[216:219]\n"
      "ds_read_b128 v[120:123], v0 offset:22528\n"
      "v_cvt_pk_bf16_f32 v136, v136, v137\n"
      "v_mfma_f32_16x16x32_bf16 v[170:173], v[206:209], a[192:195], 0\n"
      "ds_read_b128 v[206:209], v1 offset:34816\n"
      "v_fmac_f32 v76, v205, v205\n"
      "v_mfma_f32_16x16x32_bf16 v[170:173], v[220:223], a[196:199], v[170:173]\n"
      "v_fmac_f32 v188, v147, v147\n"
      "v_mul_f32 v187, v179, v187\n"
      "v_mfma_f32_16x16x32_bf16 v[170:173], v[166:169], a[200:203], v[170:173]\n"
      "v_permlane16_swap_b32 v188, v76\n"
      "v_mfma_f32_16x16x32_bf16 v[170:173], v[114:117], a[204:207], v[170:173]\n"
      "ds_read_b128 v[114:117], v0 offset:34816\n"
      "v_cmp_gt_u32 s[72:73], 64, v78\n"
      "v_mfma_f32_16x16x32_bf16 v[170:173], v[180:183], a[208:211], v[170:173]\n"
      "v_mul_f32_e64 v138, v179, v138\n"
      "v_cvt_pk_bf16_f32 v134, v216, v217\n"
      "v_mfma_f32_16x16x32_bf16 v[170:173], v[124:127], a[212:215], v[170:173]\n"
      "s_waitcnt lgkmcnt(2)\n"
      "v_add_f32 v188, v188, v76\n"
      "v_and_b32 v82, -4, v82\n"
      "v_mfma_f32_16x16x32_bf16 v[174:177], a[0:3], v[120:123], 0\n"
      "v_mov_b32 v76, v188\n"
      "v_mul_f32_e64 v185, v179, v185\n"
      "v_mfma_f32_16x16x32_bf16 v[174:177], a[4:7], v[198:201], v[174:177]\n"
      "v_cvt_pk_bf16_f32 v149, v150, v151\n"
      "v_cvt_pk_bf16_f32 v196, v140, v141\n"
      "v_mfma_f32_16x16x32_bf16 v[174:177], a[8:11], v[156:159], v[174:177]\n"
      "v_permlane32_swap_b32 v188, v76\n"
      "v_mfma_f32_16x16x32_bf16 v[174:177], a[12:15], v[190:193], v[174:177]\n"
      "s_waitcnt lgkmcnt(0)\n"
      "v_add_f32_e64 v188, v188, v76\n"
      "v_cvt_pk_bf16_f32 v137, v138, v139\n"
      "v_mfma_f32_16x16x32_bf16 v[174:177], a[16:19], v[114:117], v[174:177]\n"
      "v_cvt_pk_bf16_f32 v151, v162, v163\n"
      "v_cvt_pk_bf16_f32 v105, v106, v107\n"
      "v_mfma_f32_16x16x32_bf16 v[174:177], a[20:23], v[206:209], v[174:177]\n"
      "s_waitcnt vmcnt(7)\n"
      "v_rsq_f32 v188, v188\n"
      "v_mfma_f32_16x16x32_bf16 v[166:169], a[24:27], v[120:123], 0\n"
      "v_mov_b32_e64 v76, v188\n"
      "v_cvt_pk_bf16_f32 v197, v142, v143\n"
      "v_mfma_f32_16x16x32_bf16 v[166:169], a[28:31], v[198:201], v[166:169]\n"
      "v_permlane16_swap_b32 v188, v76\n"
      "v_mfma_f32_16x16x32_bf16 v[166:169], a[32:35], v[156:159], v[166:169]\n"
      "v_mul_f32 v202, v76, v202\n"
      "v_mul_f32 v186, v179, v186\n"
      "v_mfma_f32_16x16x32_bf16 v[166:169], a[36:39], v[190:193], v[166:169]\n"
      "v_mul_f32 v203, v76, v203\n"
      "v_fma_f32 v88, v174, v174, s51\n"
      "v_mfma_f32_16x16x32_bf16 v[166:169], a[40:43], v[114:117], v[166:169]\n"
      "v_mul_f32 v188, s50, v188\n"
      "v_mul_f32 v109, v76, v109\n"
      "v_mfma_f32_16x16x32_bf16 v[166:169], a[44:47], v[206:209], v[166:169]\n"
      "v_fmac_f32 v88, v175, v175\n"
      "v_fmac_f32 v88, v176, v176\n"
      "v_mfma_f32_16x16x32_bf16 v[140:143], a[48:51], v[120:123], 0\n"
      "v_mul_f32 v144, v188, v144\n"
      "v_fmac_f32 v88, v177, v177\n"
      "v_mfma_f32_16x16x32_bf16 v[140:143], a[52:55], v[198:201], v[140:143]\n"
      "v_cvt_pk_bf16_f32 v202, v202, v203\n"
      "v_mul_f32_e64 v147, v188, v147\n"
      "v_mfma_f32_16x16x32_bf16 v[140:143], a[56:59], v[156:159], v[140:143]\n"
      "v_fmac_f32 v88, v166, v166\n"
      "v_mul_f32 v155, v76, v155\n"
      "v_mfma_f32_16x16x32_bf16 v[140:143], a[60:63], v[190:193], v[140:143]\n"
      "v_mul_f32_e64 v128, v188, v128\n"
      "v_cvt_pk_bf16_f32 v138, v184, v185\n"
      "v_mul_f32 v111, v76, v111\n"
      "v_mul_f32 v154, v76, v154\n"
      "v_mfma_f32_16x16x32_bf16 v[140:143], a[64:67], v[114:117], v[140:143]\n"
      "v_mov_b32 v179, 0\n"
      "v_mul_f32 v102, v188, v102\n"
      "v_mfma_f32_16x16x32_bf16 v[140:143], a[68:71], v[206:209], v[140:143]\n"
      "v_mul_f32 v108, v76, v108\n"
      "v_cvt_pk_bf16_f32 v135, v218, v219\n"
      "v_mfma_f32_16x16x32_bf16 v[162:165], a[72:75], v[120:123], 0\n"
      "v_mul_f32 v129, v188, v129\n"
      "v_lshlrev_b64 v[178:179], v78, v[178:179]\n"
      "v_fmac_f32 v88, v167, v167\n"
      "v_mul_f32 v100, v188, v100\n"
      "v_mfma_f32_16x16x32_bf16 v[162:165], a[76:79], v[198:201], v[162:165]\n"
      "v_mul_f32 v145, v188, v145\n"
      "v_mul_f32 v153, v76, v153\n"
      "v_mfma_f32_16x16x32_bf16 v[162:165], a[80:83], v[156:159], v[162:165]\n"
      "v_mul_f32 v110, v76, v110\n"
      "v_mul_f32 v152, v76, v152\n"
      "v_mfma_f32_16x16x32_bf16 v[162:165], a[84:87], v[190:193], v[162:165]\n"
      "ds_read_b128 v[180:183], v2 offset:6144\n"
      "v_cvt_pk_bf16_f32 v150, v160, v161\n"
      "v_mfma_f32_16x16x32_bf16 v[162:165], a[88:91], v[114:117], v[162:165]\n"
      "v_cvt_pk_bf16_f32 v139, v186, v187\n"
      "v_mul_f32 v131, v188, v131\n"
      "v_mul_f32 v204, v76, v204\n"
      "v_cndmask_b32_e64 v179, 0, v179, s[72:73]\n"
      "v_mfma_f32_16x16x32_bf16 v[162:165], a[92:95], v[206:209], v[162:165]\n"
      "v_mul_f32 v205, v76, v205\n"
      "v_mul_f32 v103, v188, v103\n"
      "v_mfma_f32_16x16x32_bf16 v[220:223], a[96:99], v[120:123], 0\n"
      "v_mul_f32 v130, v188, v130\n"
      "v_cvt_pk_bf16_f32 v216, v170, v171\n"
      "v_cvt_pk_bf16_f32 v152, v152, v153\n"
      "v_mfma_f32_16x16x32_bf16 v[220:223], a[100:103], v[198:201], v[220:223]\n"
      "v_mul_f32 v101, v188, v101\n"
      "v_cvt_pk_bf16_f32 v100, v100, v101\n"
      "v_mfma_f32_16x16x32_bf16 v[220:223], a[104:107], v[156:159], v[220:223]\n"
      "v_fmac_f32_e64 v88, v168, v168\n"
      "v_fma_f32 v160, v162, v162, s51\n"
      "v_mfma_f32_16x16x32_bf16 v[220:223], a[108:111], v[190:193], v[220:223]\n"
      "v_cvt_pk_bf16_f32 v101, v102, v103\n"
      "v_fmac_f32 v88, v169, v169\n"
      "v_mfma_f32_16x16x32_bf16 v[220:223], a[112:115], v[114:117], v[220:223]\n"
      "v_fmac_f32 v88, v140, v140\n"
      "v_mul_f32 v146, v188, v146\n"
      "v_mfma_f32_16x16x32_bf16 v[220:223], a[116:119], v[206:209], v[220:223]\n"
      "v_fmac_f32 v88, v141, v141\n"
      "v_fmac_f32 v160, v163, v163\n"
      "v_mfma_f32_16x16x32_bf16 v[184:187], a[120:123], v[120:123], 0\n"
      "v_fmac_f32 v88, v142, v142\n"
      "v_cvt_pk_bf16_f32 v203, v204, v205\n"
      "v_fmac_f32 v160, v164, v164\n"
      "v_mfma_f32_16x16x32_bf16 v[184:187], a[124:127], v[198:201], v[184:187]\n"
      "v_fmac_f32 v88, v143, v143\n"
      "v_fmac_f32_e64 v160, v165, v165\n"
      "v_mfma_f32_16x16x32_bf16 v[184:187], a[128:131], v[156:159], v[184:187]\n"
      "v_cndmask_b32_e64 v178, 0, v178, s[72:73]\n"
      "v_cvt_pk_bf16_f32 v102, v128, v129\n"
      "v_mfma_f32_16x16x32_bf16 v[184:187], a[132:135], v[190:193], v[184:187]\n"
      "v_cvt_pk_bf16_f32 v153, v154, v155\n"
      "v_fmac_f32_e64 v160, v220, v220\n"
      "v_mfma_f32_16x16x32_bf16 v[184:187], a[136:139], v[114:117], v[184:187]\n"
      "v_cvt_pk_bf16_f32 v154, v108, v109\n"
      "v_add_u32 v98, 110592, v82\n"
      "v_mfma_f32_16x16x32_bf16 v[184:187], a[140:143], v[206:209], v[184:187]\n"
      "v_fmac_f32_e64 v160, v221, v221\n"
      "v_cvt_pk_bf16_f32 v217, v172, v173\n"
      "v_mfma_f32_16x16x32_bf16 v[124:127], v[120:123], a[144:147], 0\n"
      "v_cvt_pk_bf16_f32 v155, v110, v111\n"
      "v_fmac_f32_e64 v160, v222, v222\n"
      "v_mfma_f32_16x16x32_bf16 v[124:127], v[198:201], a[148:151], v[124:127]\n"
      "v_cvt_pk_bf16_f32 v103, v130, v131\n"
      "v_fmac_f32 v160, v223, v223\n"
      "v_mfma_f32_16x16x32_bf16 v[124:127], v[156:159], a[152:155], v[124:127]\n"
      "v_cmp_gt_u32 s[72:73], 64, v78\n"
      "v_fmac_f32 v160, v184, v184\n"
      "v_fmac_f32 v160, v185, v185\n"
      "v_mfma_f32_16x16x32_bf16 v[124:127], v[190:193], a[156:159], v[124:127]\n"
      "v_fmac_f32 v160, v186, v186\n"
      "v_fmac_f32 v160, v187, v187\n"
      "v_mfma_f32_16x16x32_bf16 v[124:127], v[114:117], a[160:163], v[124:127]\n"
      "s_nop 0\n"
      "v_permlane16_swap_b32 v88, v160\n"
      "v_mfma_f32_16x16x32_bf16 v[124:127], v[206:209], a[164:167], v[124:127]\n"
      "v_add_f32 v88, v88, v160\n"
      "v_mov_b32 v160, v88\n"
      "v_mfma_f32_16x16x32_bf16 v[108:111], v[120:123], a[168:171], 0\n"
      "s_nop 0\n"
      "v_permlane32_swap_b32 v88, v160\n"
      "v_mfma_f32_16x16x32_bf16 v[108:111], v[198:201], a[172:175], v[108:111]\n"
      "v_add_f32 v88, v88, v160\n"
      "v_rsq_f32 v88, v88\n"
      "v_cvt_pk_bf16_f32 v144, v144, v145\n"
      "v_mov_b32 v160, v88\n"
      "v_mfma_f32_16x16x32_bf16 v[108:111], v[156:159], a[176:179], v[108:111]\n"
      "s_nop 0\n"
      "v_permlane16_swap_b32 v88, v160\n"
      "v_mfma_f32_16x16x32_bf16 v[108:111], v[190:193], a[180:183], v[108:111]\n"
      "v_mul_f32 v162, v160, v162\n"
      "v_mul_f32 v223, v160, v223\n"
      "v_mul_f32 v185, v160, v185\n"
      "v_mfma_f32_16x16x32_bf16 v[108:111], v[114:117], a[184:187], v[108:111]\n"
      "v_cvt_pk_bf16_f32 v145, v146, v147\n"
      "v_mul_f32 v165, v160, v165\n"
      "v_cvt_pk_bf16_f32 v76, v124, v125\n"
      "v_mul_f32 v186, v160, v186\n"
      "v_mfma_f32_16x16x32_bf16 v[108:111], v[206:209], a[188:191], v[108:111]\n"
      "ds_read_b128 v[128:131], v2\n"
      "v_mul_f32 v88, s50, v88\n"
      "v_cvt_pk_bf16_f32 v77, v126, v127\n"
      "v_mfma_f32_16x16x32_bf16 v[124:127], v[120:123], a[192:195], 0\n"
      "ds_read_b128 v[120:123], v2 offset:3072\n"
      "v_mul_f32_e64 v184, v160, v184\n"
      "v_mfma_f32_16x16x32_bf16 v[124:127], v[198:201], a[196:199], v[124:127]\n"
      "ds_read_b128 v[170:173], v7\n"
      "v_readlane_b32 s45, v37, s43\n"
      "v_mul_f32 v174, v88, v174\n"
      "v_mul_f32 v177, v88, v177\n"
      "v_cvt_pk_bf16_f32 v80, v108, v109\n"
      "v_mfma_f32_16x16x32_bf16 v[124:127], v[156:159], a[200:203], v[124:127]\n"
      "ds_read_b128 v[106:109], v2 offset:9216\n"
      "v_mul_f32_e64 v163, v160, v163\n"
      "v_mfma_f32_16x16x32_bf16 v[124:127], v[190:193], a[204:207], v[124:127]\n"
      "ds_read_b128 v[188:191], v7 offset:4096\n"
      "v_cvt_pk_bf16_f32 v162, v162, v163\n"
      "v_mfma_f32_16x16x32_bf16 v[124:127], v[114:117], a[208:211], v[124:127]\n"
      "s_waitcnt lgkmcnt(4)\n"
      "v_mul_f32 v164, v160, v164\n"
      "v_cvt_pk_bf16_f32 v81, v110, v111\n"
      "v_cvt_pk_bf16_f32 v163, v164, v165\n"
      "v_mfma_f32_16x16x32_bf16 v[124:127], v[206:209], a[212:215], v[124:127]\n"
      "ds_read2_b32 v[146:147], v98 offset0:24 offset1:18\n"
      "v_mul_f32 v175, v88, v175\n"
      "v_mfma_f32_16x16x32_bf16 v[114:117], a[216:219], v[128:131], 0\n"
      "s_waitcnt lgkmcnt(3)\n"
      "v_mul_f32 v221, v160, v221\n"
      "v_cvt_pk_bf16_f32 v184, v184, v185\n"
      "v_mul_f32 v176, v88, v176\n"
      "v_mul_f32 v187, v160, v187\n"
      "v_mfma_f32_16x16x32_bf16 v[114:117], a[220:223], v[120:123], v[114:117]\n"
      "buffer_store_dwordx4 v[170:173], v20, s[24:27], s46 offen sc1\n"
      "v_mul_f32 v167, v88, v167\n"
      "v_cvt_pk_bf16_f32 v85, v126, v127\n"
      "v_cvt_pk_bf16_f32 v185, v186, v187\n"
      "v_mfma_f32_16x16x32_bf16 v[114:117], a[224:227], v[180:183], v[114:117]\n"
      "ds_read_b128 v[110:113], v2 offset:15360\n"
      "v_mul_f32_e64 v222, v160, v222\n"
      "v_mul_f32_e64 v220, v160, v220\n"
      "v_mfma_f32_16x16x32_bf16 v[206:209], a[240:243], v[128:131], 0\n"
      "ds_read_b128 v[170:173], v7 offset:8192\n"
      "v_cvt_pk_bf16_f32 v165, v222, v223\n"
      "v_mfma_f32_16x16x32_bf16 v[206:209], a[244:247], v[120:123], v[206:209]\n"
      "ds_read_b128 v[156:159], v2 offset:12288\n"
      "v_cvt_pk_bf16_f32 v164, v220, v221\n"
      "v_mfma_f32_16x16x32_bf16 v[206:209], a[248:251], v[180:183], v[206:209]\n"
      "s_waitcnt lgkmcnt(3)\n"
      "v_cvt_pk_bf16_f32 v174, v174, v175\n"
      "v_mul_f32 v168, v88, v168\n"
      "v_mfma_f32_16x16x32_bf16 v[220:223], v[232:235], v[128:131], 0\n"
      "ds_read_b128 v[126:129], v7 offset:16384\n"
      "v_mul_f32_e64 v166, v88, v166\n"
      "v_mfma_f32_16x16x32_bf16 v[220:223], v[236:239], v[120:123], v[220:223]\n"
      "ds_read_b128 v[198:201], v10 offset:1200\n"
      "v_cvt_pk_bf16_f32 v175, v176, v177\n"
      "v_mfma_f32_16x16x32_bf16 v[220:223], v[240:243], v[180:183], v[220:223]\n"
      "buffer_store_dwordx4 v[188:191], v21, s[24:27], s46 offen sc1\n"
      "v_mfma_f32_16x16x32_bf16 v[114:117], a[228:231], v[106:109], v[114:117]\n"
      "s_waitcnt lgkmcnt(2)\n"
      "v_mul_f32 v169, v88, v169\n"
      "v_cvt_pk_bf16_f32 v84, v124, v125\n"
      "v_cvt_pk_bf16_f32 v177, v168, v169\n"
      "v_mfma_f32_16x16x32_bf16 v[114:117], a[232:235], v[156:159], v[114:117]\n"
      "ds_read_b64 v[204:205], v10 offset:1192\n"
      "v_cvt_pk_bf16_f32 v176, v166, v167\n"
      "v_mfma_f32_16x16x32_bf16 v[114:117], a[236:239], v[110:113], v[114:117]\n"
      "ds_read2_b32 v[180:181], v98 offset0:0 offset1:1\n"
      "v_mul_f32_e64 v140, v88, v140\n"
      "v_mfma_f32_16x16x32_bf16 v[206:209], a[252:255], v[106:109], v[206:209]\n"
      "ds_read2_b32 v[182:183], v98 offset0:2 offset1:10\n"
      "v_mul_f32_e64 v141, v88, v141\n"
      "v_mfma_f32_16x16x32_bf16 v[206:209], v[224:227], v[156:159], v[206:209]\n"
      "ds_read2_b32 v[130:131], v98 offset0:16 offset1:17\n"
      "v_mul_f32_e64 v142, v88, v142\n"
      "v_mfma_f32_16x16x32_bf16 v[206:209], v[228:231], v[110:113], v[206:209]\n"
      "ds_read2_b32 v[160:161], v98 offset0:8 offset1:9\n"
      "v_mov_b32 v98, 0xbb2b\n"
      "v_mfma_f32_16x16x32_bf16 v[220:223], v[244:247], v[106:109], v[220:223]\n"
      "ds_read_b128 v[166:169], v7 offset:12288\n"
      "v_fma_f32 v218, v114, v114, s52\n"
      "v_mfma_f32_16x16x32_bf16 v[220:223], v[248:251], v[156:159], v[220:223]\n"
      "buffer_store_dwordx4 v[170:173], v22, s[24:27], s46 offen sc1\n"
      "v_cvt_pk_bf16_f32 v140, v140, v141\n"
      "v_fmac_f32_e64 v218, v115, v115\n"
      "v_mfma_f32_16x16x32_bf16 v[220:223], v[252:255], v[110:113], v[220:223]\n"
      "v_readlane_b32 s47, v38, s43\n"
      "v_alignbyte_b32 v146, v146, v146, v93\n"
      "v_mfma_f32_16x16x32_bf16 v[108:111], v[148:151], v[136:139], v[40:43]\n"
      "s_waitcnt lgkmcnt(2)\n"
      "v_mov_b32 v99, 0\n"
      "v_lshlrev_b64 v[98:99], v78, v[98:99]\n"
      "v_mul_f32_e64 v143, v88, v143\n"
      "v_mfma_f32_16x16x32_bf16 v[186:189], v[152:155], v[136:139], v[44:47]\n"
      "ds_read_b128 v[190:193], v2 offset:3328\n"
      "v_alignbyte_b32 v130, v131, v130, v93\n"
      "v_mfma_f32_16x16x16_bf16 v[108:111], v[96:97], v[104:105], v[108:111]\n"
      "v_fmac_f32_e64 v218, v116, v116\n"
      "v_fmac_f32 v218, v117, v117\n"
      "v_cvt_pk_bf16_f32 v141, v142, v143\n"
      "v_alignbyte_b32 v180, v181, v180, v93\n"
      "v_mfma_f32_16x16x32_bf16 v[170:173], v[162:165], v[136:139], v[48:51]\n"
      "s_waitcnt lgkmcnt(1)\n"
      "v_alignbyte_b32 v181, v182, v181, v93\n"
      "v_cndmask_b32_e64 v99, 0, v99, s[72:73]\n"
      "v_mfma_f32_16x16x32_bf16 v[122:125], v[198:201], v[136:139], v[24:27]\n"
      "ds_read_b128 v[136:139], v2 offset:256\n"
      "v_subrev_u32 v219, 115456, v9\n"
      "v_mfma_f32_16x16x16_bf16 v[186:189], v[202:203], v[104:105], v[186:189]\n"
      "buffer_store_dwordx4 v[166:169], v23, s[24:27], s46 offen sc1\n"
      "v_mfma_f32_16x16x16_bf16 v[170:173], v[184:185], v[104:105], v[170:173]\n"
      "ds_read_b128 v[156:159], v2 offset:6400\n"
      "v_alignbyte_b32 v131, v147, v131, v93\n"
      "v_mfma_f32_16x16x16_bf16 v[122:125], v[204:205], v[104:105], v[122:125]\n"
      "v_lshrrev_b32_e64 v219, 3, v219\n"
      "v_cndmask_b32_e64 v98, 0, v98, s[72:73]\n"
      "v_fmac_f32 v218, v206, v206\n"
      "v_mfma_f32_16x16x16_bf16 v[108:111], v[178:179], v[180:181], v[108:111]\n"
      "v_fmac_f32 v218, v207, v207\n"
      "v_alignbyte_b32 v160, v161, v160, v93\n"
      "v_alignbyte_b32 v161, v183, v161, v93\n"
      "v_mfma_f32_16x16x16_bf16 v[108:111], v[98:99], v[180:181], v[108:111]\n"
      "ds_read_b128 v[166:169], v9 offset:4608\n"
      "v_add_u32 v92, 113728, v82\n"
      "v_mfma_f32_16x16x16_bf16 v[170:173], v[178:179], v[130:131], v[170:173]\n"
      "v_min_u32 v219, 96, v219\n"
      "v_add_u32 v104, 112160, v82\n"
      "v_fmac_f32_e64 v218, v208, v208\n"
      "v_mfma_f32_16x16x16_bf16 v[186:189], v[178:179], v[160:161], v[186:189]\n"
      "v_fmac_f32 v218, v209, v209\n"
      "v_fmac_f32 v218, v220, v220\n"
      "v_fmac_f32 v218, v221, v221\n"
      "v_mfma_f32_16x16x16_bf16 v[186:189], v[98:99], v[160:161], v[186:189]\n"
      "v_fmac_f32 v218, v222, v222\n"
      "v_add_u32_e64 v219, s77, v219\n"
      "v_max3_f32 v88, v108, v109, v110\n"
      "v_mfma_f32_16x16x16_bf16 v[122:125], v[178:179], v[146:147], v[122:125]\n"
      "buffer_store_dwordx4 v[126:129], v25, s[24:27], s46 offen sc1\n"
      "v_mfma_f32_16x16x16_bf16 v[122:125], v[98:99], v[146:147], v[122:125]\n"
      "v_fmac_f32_e64 v218, v223, v223\n"
      "s_waitcnt lgkmcnt(0)\n"
      "v_or3_b32 v166, v166, v167, v168\n"
      "v_readlane_b32 s46, v37, s42\n"
      "v_mfma_f32_16x16x16_bf16 v[170:173], v[98:99], v[130:131], v[170:173]\n"
      "s_waitcnt vmcnt(6)\n"
      "v_or3_b32 v210, v210, v211, v212\n"
      "v_max3_f32 v211, v111, v186, v187\n"
      "v_or_b32 v166, v166, v169\n"
      "v_cmp_ne_u32 s[56:57], 0, v166\n"
      "v_max3_f32 v88, v88, v188, v189\n"
      "s_waitcnt vmcnt(5)\n"
      "v_or3_b32 v210, v210, v213, v86\n"
      "v_and_b32 v210, 0x7fff7fff, v210\n"
      "v_mfma_f32_16x16x32_bf16 v[166:169], v[232:235], v[136:139], 0\n"
      "ds_write_b32 v31, v210 offset:5632\n"
      "v_mfma_f32_16x16x32_bf16 v[180:183], a[216:219], v[136:139], 0\n"
      "ds_read2_b32 v[146:147], v104 offset0:16 offset1:17\n"
      "v_add_u32 v219, 115296, v219\n"
      "v_mfma_f32_16x16x32_bf16 v[180:183], a[220:223], v[190:193], v[180:183]\n"
      "ds_read2_b32 v[142:143], v104 offset0:0 offset1:1\n"
      "v_max3_f32 v88, v88, v172, v173\n"
      "v_mfma_f32_16x16x32_bf16 v[180:183], a[224:227], v[156:159], v[180:183]\n"
      "ds_read2_b32 v[160:161], v104 offset0:8 offset1:9\n"
      "v_max3_f32 v211, v211, v170, v171\n"
      "v_mfma_f32_16x16x32_bf16 v[124:127], a[240:243], v[136:139], 0\n"
      "ds_read2_b32 v[130:131], v104 offset0:2 offset1:10\n"
      "v_max3_f32 v88, v88, v211, v122\n"
      "v_mfma_f32_16x16x32_bf16 v[124:127], a[244:247], v[190:193], v[124:127]\n"
      "ds_read2_b32 v[136:137], v104 offset0:24 offset1:18\n"
      "v_mov_b32 v104, v88\n"
      "v_mfma_f32_16x16x32_bf16 v[124:127], a[248:251], v[156:159], v[124:127]\n"
      "s_nop 0\n"
      "v_permlane16_swap_b32_e64 v88, v104\n"
      "v_mfma_f32_16x16x32_bf16 v[166:169], v[236:239], v[190:193], v[166:169]\n"
      "v_max_f32 v88, v88, v104\n"
      "v_mov_b32 v104, v88\n"
      "v_mfma_f32_16x16x32_bf16 v[166:169], v[240:243], v[156:159], v[166:169]\n"
      "s_nop 0\n"
      "v_permlane32_swap_b32 v88, v104\n"
      "v_mfma_f32_16x16x32_bf16 v[210:213], v[198:201], v[100:103], v[26:29]\n"
      "s_waitcnt lgkmcnt(2)\n"
      "v_max_f32 v88, v88, v104\n"
      "v_alignbyte_b32 v160, v161, v160, v93\n"
      "v_mfma_f32_16x16x32_bf16 v[190:193], v[152:155], v[100:103], v[56:59]\n"
      "s_waitcnt lgkmcnt(1)\n"
      "v_sub_f32_e64 v110, v110, v88\n"
      "v_alignbyte_b32 v142, v143, v142, v93\n"
      "v_mfma_f32_16x16x32_bf16 v[104:107], v[148:151], v[100:103], v[52:55]\n"
      "s_waitcnt lgkmcnt(0)\n"
      "v_alignbyte_b32 v143, v130, v143, v93\n"
      "v_alignbyte_b32 v161, v131, v161, v93\n"
      "v_mfma_f32_16x16x32_bf16 v[128:131], v[162:165], v[100:103], v[60:63]\n"
      "ds_read_u16 v219, v219\n"
      "v_sub_f32 v108, v108, v88\n"
      "v_mfma_f32_16x16x16_bf16 v[190:193], v[202:203], v[144:145], v[190:193]\n"
      "v_sub_f32 v111, v111, v88\n"
      "v_sub_f32 v173, v173, v88\n"
      "v_sub_f32 v188, v188, v88\n"
      "v_sub_f32_e64 v186, v186, v88\n"
      "v_mfma_f32_16x16x16_bf16 v[128:131], v[184:185], v[144:145], v[128:131]\n"
      "v_sub_f32 v189, v189, v88\n"
      "v_alignbyte_b32 v146, v147, v146, v93\n"
      "v_sub_f32 v122, v122, v88\n"
      "v_alignbyte_b32 v147, v137, v147, v93\n"
      "v_mfma_f32_16x16x16_bf16 v[104:107], v[96:97], v[144:145], v[104:107]\n"
      "v_sub_f32 v109, v109, v88\n"
      "v_exp_f32 v173, v173\n"
      "v_mfma_f32_16x16x16_bf16 v[104:107], v[178:179], v[142:143], v[104:107]\n"
      "v_exp_f32 v122, v122\n"
      "v_sub_f32 v171, v171, v88\n"
      "v_mfma_f32_16x16x16_bf16 v[104:107], v[98:99], v[142:143], v[104:107]\n"
      "v_sub_f32 v187, v187, v88\n"
      "v_exp_f32 v186, v186\n"
      "v_mfma_f32_16x16x16_bf16 v[190:193], v[178:179], v[160:161], v[190:193]\n"
      "v_exp_f32 v188, v188\n"
      "v_exp_f32 v189, v189\n"
      "v_exp_f32 v171, v171\n"
      "v_mfma_f32_16x16x16_bf16 v[190:193], v[98:99], v[160:161], v[190:193]\n"
      "v_exp_f32 v111, v111\n"
      "v_alignbyte_b32 v136, v136, v136, v93\n"
      "v_mfma_f32_16x16x16_bf16 v[210:213], v[204:205], v[144:145], v[210:213]\n"
      "v_sub_f32 v170, v170, v88\n"
      "v_exp_f32 v170, v170\n"
      "v_mfma_f32_16x16x16_bf16 v[210:213], v[178:179], v[136:137], v[210:213]\n"
      "v_sub_f32 v172, v172, v88\n"
      "v_exp_f32 v110, v110\n"
      "v_mfma_f32_16x16x16_bf16 v[210:213], v[98:99], v[136:137], v[210:213]\n"
      "ds_read_b128 v[142:145], v2 offset:9472\n"
      "ds_read_b128 v[100:103], v2 offset:12544\n"
      "v_mfma_f32_16x16x16_bf16 v[128:131], v[178:179], v[146:147], v[128:131]\n"
      "v_exp_f32_e64 v109, v109\n"
      "v_cvt_pk_bf16_f32 v90, v122, 0\n"
      "v_mfma_f32_16x16x16_bf16 v[128:131], v[98:99], v[146:147], v[128:131]\n"
      "ds_read_b128 v[156:159], v2 offset:15616\n"
      "v_max3_f32 v147, v104, v105, v106\n"
      "v_exp_f32 v187, v187\n"
      "v_exp_f32 v108, v108\n"
      "v_cvt_pk_bf16_f32 v88, v170, v171\n"
      "v_cvt_pk_bf16_f32 v108, v108, v109\n"
      "v_cvt_pk_bf16_f32 v109, v110, v111\n"
      "v_max3_f32 v147, v147, v192, v193\n"
      "v_cvt_pk_bf16_f32 v110, v186, v187\n"
      "v_exp_f32_e64 v172, v172\n"
      "v_max3_f32 v147, v147, v130, v131\n"
      "v_cvt_pk_bf16_f32 v111, v188, v189\n"
      "v_max3_f32 v213, v107, v190, v191\n"
      "s_waitcnt lgkmcnt(2)\n"
      "v_mfma_f32_16x16x32_bf16 v[180:183], a[228:231], v[142:145], v[180:183]\n"
      "s_waitcnt lgkmcnt(1)\n"
      "v_max3_f32 v213, v213, v128, v129\n"
      "v_max3_f32 v147, v147, v213, v210\n"
      "v_mov_b32 v188, v147\n"
      "v_mfma_f32_16x16x32_bf16 v[180:183], a[232:235], v[100:103], v[180:183]\n"
      "s_waitcnt lgkmcnt(0)\n"
      "v_cvt_pk_bf16_f32 v89, v172, v173\n"
      "v_permlane16_swap_b32 v147, v188\n"
      "v_mfma_f32_16x16x32_bf16 v[180:183], a[236:239], v[156:159], v[180:183]\n"
      "v_max_f32 v147, v147, v188\n"
      "v_mov_b32_e64 v188, v147\n"
      "v_mfma_f32_16x16x32_bf16 v[124:127], a[252:255], v[142:145], v[124:127]\n"
      "ds_read2_b32 v[212:213], v92 offset0:8 offset1:9\n"
      "v_permlane32_swap_b32_e64 v147, v188\n"
      "v_mfma_f32_16x16x32_bf16 v[124:127], v[224:227], v[100:103], v[124:127]\n"
      "ds_read2_b32 v[112:113], v92 offset0:16 offset1:17\n"
      "v_max_f32 v147, v147, v188\n"
      "v_sub_f32 v107, v107, v147\n"
      "v_mfma_f32_16x16x32_bf16 v[124:127], v[228:231], v[156:159], v[124:127]\n"
      "ds_read2_b32 v[172:173], v92 offset0:2 offset1:10\n"
      "v_sub_f32 v105, v105, v147\n"
      "v_mfma_f32_16x16x32_bf16 v[166:169], v[244:247], v[142:145], v[166:169]\n"
      "ds_read_u16 v78, v11 offset:1168\n"
      "v_sub_f32 v104, v104, v147\n"
      "v_mfma_f32_16x16x32_bf16 v[166:169], v[248:251], v[100:103], v[166:169]\n"
      "ds_read_u16 v82, v11 offset:1200\n"
      "v_sub_f32 v191, v191, v147\n"
      "v_sub_f32 v192, v192, v147\n"
      "v_sub_f32_e64 v106, v106, v147\n"
      "v_mfma_f32_16x16x32_bf16 v[166:169], v[252:255], v[156:159], v[166:169]\n"
      "ds_read_u16 v86, v11 offset:1232\n"
      "v_exp_f32 v104, v104\n"
      "v_exp_f32 v107, v107\n"
      "v_sub_f32 v129, v129, v147\n"
      "v_exp_f32 v192, v192\n"
      "v_exp_f32 v105, v105\n"
      "v_sub_f32 v190, v190, v147\n"
      "v_sub_f32 v130, v130, v147\n"
      "v_cvt_pk_bf16_f32 v104, v104, v105\n"
      "v_exp_f32 v106, v106\n"
      "v_sub_f32 v128, v128, v147\n"
      "v_mfma_f32_16x16x32_bf16 v[142:145], v[32:35], v[108:111], 0\n"
      "s_waitcnt lgkmcnt(1)\n"
      "v_alignbyte_b32 v112, v113, v112, v93\n"
      "v_sub_f32 v193, v193, v147\n"
      "v_sub_f32 v131, v131, v147\n"
      "v_alignbyte_b32 v212, v213, v212, v93\n"
      "v_mfma_f32_16x16x32_bf16 v[142:145], v[32:35], v[88:91], v[142:145]\n"
      "ds_read2_b32 v[122:123], v92 offset0:0 offset1:1\n"
      "v_alignbyte_b32 v213, v173, v213, v93\n"
      "v_sub_f32_e64 v210, v210, v147\n"
      "v_mfma_f32_16x16x32_bf16 v[186:189], v[194:197], v[108:111], 0\n"
      "ds_read2_b32 v[120:121], v92 offset0:24 offset1:18\n"
      "v_cvt_pk_bf16_f32 v105, v106, v107\n"
      "v_exp_f32 v191, v191\n"
      "v_mfma_f32_16x16x32_bf16 v[186:189], v[76:79], v[88:91], v[186:189]\n"
      "v_exp_f32 v193, v193\n"
      "v_mfma_f32_16x16x32_bf16 v[144:147], v[132:135], v[108:111], 0\n"
      "v_exp_f32_e64 v190, v190\n"
      "v_mfma_f32_16x16x32_bf16 v[144:147], v[80:83], v[88:91], v[144:147]\n"
      "v_cvt_pk_bf16_f32 v107, v192, v193\n"
      "v_cvt_pk_bf16_f32 v106, v190, v191\n"
      "v_mfma_f32_16x16x32_bf16 v[190:193], v[214:217], v[108:111], 0\n"
      "s_waitcnt lgkmcnt(2)\n"
      "v_rcp_f32 v142, v142\n"
      "v_mfma_f32_16x16x32_bf16 v[190:193], v[84:87], v[88:91], v[190:193]\n"
      "s_waitcnt lgkmcnt(1)\n"
      "v_alignbyte_b32 v122, v123, v122, v93\n"
      "v_alignbyte_b32 v123, v172, v123, v93\n"
      "v_mul_f32 v187, v142, v187\n"
      "v_mfma_f32_16x16x32_bf16 v[100:103], v[152:155], v[174:177], v[68:71]\n"
      "s_waitcnt lgkmcnt(0)\n"
      "v_alignbyte_b32 v113, v121, v113, v93\n"
      "v_alignbyte_b32 v120, v120, v120, v93\n"
      "v_mfma_f32_16x16x32_bf16 v[158:161], v[148:151], v[174:177], v[64:67]\n"
      "v_mul_f32 v186, v142, v186\n"
      "v_mul_f32 v147, v142, v147\n"
      "v_mfma_f32_16x16x32_bf16 v[136:139], v[198:201], v[174:177], v[28:31]\n"
      "v_mul_f32 v192, v142, v192\n"
      "v_mul_f32 v191, v142, v191\n"
      "v_mfma_f32_16x16x32_bf16 v[170:173], v[162:165], v[174:177], v[72:75]\n"
      "v_exp_f32 v131, v131\n"
      "v_mfma_f32_16x16x16_bf16 v[158:161], v[96:97], v[140:141], v[158:161]\n"
      "v_exp_f32 v129, v129\n"
      "v_mul_f32 v193, v142, v193\n"
      "v_mfma_f32_16x16x16_bf16 v[100:103], v[202:203], v[140:141], v[100:103]\n"
      "v_mul_f32 v189, v142, v189\n"
      "v_mul_f32 v190, v142, v190\n"
      "v_mul_f32 v144, v142, v144\n"
      "v_mfma_f32_16x16x16_bf16 v[170:173], v[184:185], v[140:141], v[170:173]\n"
      "v_exp_f32 v210, v210\n"
      "v_mul_f32 v145, v142, v145\n"
      "v_mfma_f32_16x16x16_bf16 v[136:139], v[204:205], v[140:141], v[136:139]\n"
      "v_exp_f32 v130, v130\n"
      "v_mul_f32 v146, v142, v146\n"
      "v_mfma_f32_16x16x16_bf16 v[158:161], v[178:179], v[122:123], v[158:161]\n"
      "v_mul_f32 v188, v142, v188\n"
      "v_cvt_pk_bf16_f32 v93, v130, v131\n"
      "v_cvt_pk_bf16_f32 v190, v190, v191\n"
      "v_mfma_f32_16x16x16_bf16 v[100:103], v[178:179], v[212:213], v[100:103]\n"
      "v_exp_f32_e64 v128, v128\n"
      "v_cvt_pk_bf16_f32 v186, v186, v187\n"
      "v_mfma_f32_16x16x16_bf16 v[170:173], v[178:179], v[112:113], v[170:173]\n"
      "v_cvt_pk_bf16_f32 v92, v128, v129\n"
      "v_cvt_pk_bf16_f32 v191, v192, v193\n"
      "v_cvt_pk_bf16_f32 v187, v188, v189\n"
      "v_mfma_f32_16x16x16_bf16 v[136:139], v[178:179], v[120:121], v[136:139]\n"
      "v_cvt_pk_bf16_f32 v188, v144, v145\n"
      "v_cvt_pk_bf16_f32 v189, v146, v147\n"
      "v_fma_f32 v157, v180, v180, s52\n"
      "v_mfma_f32_16x16x16_bf16 v[158:161], v[98:99], v[122:123], v[158:161]\n"
      "ds_read_b128 v[128:131], v2 offset:3584\n"
      "v_fmac_f32 v157, v181, v181\n"
      "v_fmac_f32 v157, v182, v182\n"
      "v_mfma_f32_16x16x16_bf16 v[100:103], v[98:99], v[212:213], v[100:103]\n"
      "ds_read_b128 v[174:177], v2 offset:512\n"
      "v_fmac_f32 v157, v183, v183\n"
      "v_mfma_f32_16x16x16_bf16 v[136:139], v[98:99], v[120:121], v[136:139]\n"
      "ds_read_b128 v[120:123], v2 offset:6656\n"
      "v_cvt_pk_bf16_f32 v94, v210, 0\n"
      "v_mfma_f32_16x16x16_bf16 v[170:173], v[98:99], v[112:113], v[170:173]\n"
      "v_fmac_f32 v157, v124, v124\n"
      "v_fmac_f32 v157, v125, v125\n"
      "v_fmac_f32 v157, v126, v126\n"
      "v_mfma_f32_16x16x32_bf16 v[210:213], v[32:35], v[104:107], 0\n"
      "ds_write_b128 v3, v[186:189] offset:18432\n"
      "v_max3_f32 v98, v158, v159, v160\n"
      "v_max3_f32 v98, v98, v102, v103\n"
      "v_max3_f32 v146, v161, v100, v101\n"
      "v_max3_f32 v146, v146, v170, v171\n"
      "v_mfma_f32_16x16x32_bf16 v[210:213], v[32:35], v[92:95], v[210:213]\n"
      "s_waitcnt lgkmcnt(2)\n"
      "v_max3_f32 v98, v98, v172, v173\n"
      "v_max3_f32 v98, v98, v146, v136\n"
      "v_mov_b32 v187, v98\n"
      "v_mfma_f32_16x16x32_bf16 v[144:147], a[216:219], v[174:177], 0\n"
      "s_or_b32 s95, s45, s46\n"
      "v_permlane16_swap_b32 v98, v187\n"
      "v_mfma_f32_16x16x32_bf16 v[144:147], a[220:223], v[128:131], v[144:147]\n"
      "s_waitcnt lgkmcnt(1)\n"
      "v_max_f32 v98, v98, v187\n"
      "v_mov_b32 v187, v98\n"
      "v_mfma_f32_16x16x32_bf16 v[144:147], a[224:227], v[120:123], v[144:147]\n"
      "ds_write_b64 v4, v[190:191] offset:18432\n"
      "v_mfma_f32_16x16x32_bf16 v[108:111], a[240:243], v[174:177], 0\n"
      "v_permlane32_swap_b32 v98, v187\n"
      "v_max_f32 v98, v98, v187\n"
      "v_mfma_f32_16x16x32_bf16 v[108:111], a[244:247], v[128:131], v[108:111]\n"
      "v_sub_f32 v170, v170, v98\n"
      "v_sub_f32 v102, v102, v98\n"
      "v_mfma_f32_16x16x32_bf16 v[108:111], a[248:251], v[120:123], v[108:111]\n"
      "v_lshlrev_b32 v219, 16, v219\n"
      "v_sub_f32 v172, v172, v98\n"
      "v_mfma_f32_16x16x32_bf16 v[140:143], v[232:235], v[174:177], 0\n"
      "ds_read_b128 v[186:189], v10 offset:1168\n"
      "v_sub_f32 v101, v101, v98\n"
      "v_mfma_f32_16x16x32_bf16 v[140:143], v[236:239], v[128:131], v[140:143]\n"
      "v_sub_f32 v100, v100, v98\n"
      "v_rcp_f32 v210, v210\n"
      "v_sub_f32 v161, v161, v98\n"
      "v_sub_f32 v160, v160, v98\n"
      "v_mfma_f32_16x16x32_bf16 v[140:143], v[240:243], v[120:123], v[140:143]\n"
      "ds_read_b64 v[88:89], v10 offset:1184\n"
      "v_sub_f32 v171, v171, v98\n"
      "v_exp_f32 v160, v160\n"
      "v_sub_f32 v136, v136, v98\n"
      "v_exp_f32 v161, v161\n"
      "v_sub_f32 v158, v158, v98\n"
      "v_exp_f32 v100, v100\n"
      "v_mfma_f32_16x16x32_bf16 v[190:193], v[194:197], v[104:107], 0\n"
      "v_sub_f32 v173, v173, v98\n"
      "v_sub_f32 v159, v159, v98\n"
      "v_mfma_f32_16x16x32_bf16 v[190:193], v[76:79], v[92:95], v[190:193]\n"
      "s_waitcnt lgkmcnt(1)\n"
      "v_exp_f32 v158, v158\n"
      "v_sub_f32_e64 v103, v103, v98\n"
      "v_mfma_f32_16x16x32_bf16 v[120:123], v[186:189], v[148:151], v[30:33]\n"
      "v_exp_f32 v102, v102\n"
      "v_mfma_f32_16x16x32_bf16 v[148:151], v[186:189], v[162:165], v[30:33]\n"
      "v_exp_f32 v101, v101\n"
      "v_mfma_f32_16x16x32_bf16 v[162:165], v[132:135], v[104:107], 0\n"
      "v_exp_f32 v173, v173\n"
      "v_mfma_f32_16x16x32_bf16 v[162:165], v[80:83], v[92:95], v[162:165]\n"
      "s_waitcnt lgkmcnt(0)\n"
      "v_exp_f32 v159, v159\n"
      "v_mfma_f32_16x16x16_bf16 v[120:123], v[88:89], v[96:97], v[120:123]\n"
      "v_exp_f32 v103, v103\n"
      "v_fmac_f32_e64 v157, v127, v127\n"
      "v_mfma_f32_16x16x16_bf16 v[148:151], v[88:89], v[184:185], v[148:151]\n"
      "v_cvt_pk_bf16_f32 v158, v158, v159\n"
      "v_cvt_pk_bf16_f32 v159, v160, v161\n"
      "v_cvt_pk_bf16_f32 v160, v100, v101\n"
      "v_mfma_f32_16x16x32_bf16 v[174:177], v[214:217], v[104:107], 0\n"
      "v_exp_f32_e64 v172, v172\n"
      "v_cvt_pk_bf16_f32 v161, v102, v103\n"
      "v_mfma_f32_16x16x32_bf16 v[174:177], v[84:87], v[92:95], v[174:177]\n"
      "ds_read_b128 v[102:105], v2 offset:9728\n"
      "v_exp_f32_e64 v171, v171\n"
      "v_mfma_f32_16x16x32_bf16 v[128:131], v[186:189], v[152:155], v[30:33]\n"
      "ds_read_b128 v[152:155], v2 offset:12800\n"
      "v_exp_f32 v136, v136\n"
      "v_exp_f32 v170, v170\n"
      "v_mfma_f32_16x16x32_bf16 v[96:99], v[186:189], v[198:201], v[30:33]\n"
      "ds_read_b128 v[184:187], v2 offset:15872\n"
      "v_cvt_pk_bf16_f32 v90, v136, 0\n"
      "v_mfma_f32_16x16x16_bf16 v[128:131], v[88:89], v[202:203], v[128:131]\n"
      "v_mul_f32 v174, v210, v174\n"
      "v_fmac_f32 v157, v166, v166\n"
      "v_mul_f32_e64 v163, v210, v163\n"
      "v_mfma_f32_16x16x16_bf16 v[96:99], v[88:89], v[204:205], v[96:99]\n"
      "v_mul_f32 v193, v210, v193\n"
      "v_mul_f32 v191, v210, v191\n"
      "v_mul_f32 v175, v210, v175\n"
      "v_fmac_f32 v157, v167, v167\n"
      "v_cvt_pk_bf16_f32 v88, v170, v171\n"
      "v_cndmask_b32 v120, v120, v128, s[58:59]\n"
      "v_mfma_f32_16x16x32_bf16 v[202:205], v[32:35], v[158:161], 0\n"
      "s_waitcnt lgkmcnt(2)\n"
      "v_mul_f32 v190, v210, v190\n"
      "v_mul_f32_e64 v176, v210, v176\n"
      "v_mfma_f32_16x16x32_bf16 v[144:147], a[228:231], v[102:105], v[144:147]\n"
      "s_waitcnt lgkmcnt(1)\n"
      "v_mul_f32 v177, v210, v177\n"
      "v_fmac_f32_e64 v157, v168, v168\n"
      "v_cndmask_b32 v120, v120, v148, s[60:61]\n"
      "v_mfma_f32_16x16x32_bf16 v[144:147], a[232:235], v[152:155], v[144:147]\n"
      "s_waitcnt lgkmcnt(0)\n"
      "v_cndmask_b32 v120, v120, v96, s[62:63]\n"
      "v_fmac_f32 v157, v169, v169\n"
      "v_mfma_f32_16x16x32_bf16 v[144:147], a[236:239], v[184:187], v[144:147]\n"
      "v_fmac_f32_e64 v120, s53, v219\n"
      "v_mul_f32_e64 v165, v210, v165\n"
      "v_mfma_f32_16x16x32_bf16 v[108:111], a[252:255], v[102:105], v[108:111]\n"
      "v_max_f32_dpp v96, v120, v120 row_ror:8 row_mask:0xf bank_mask:0xf\n"
      "v_mul_f32_e64 v164, v210, v164\n"
      "v_cvt_pk_bf16_f32 v174, v174, v175\n"
      "v_max_f32_dpp v96, v96, v96 row_ror:4 row_mask:0xf bank_mask:0xf\n"
      "v_mfma_f32_16x16x32_bf16 v[108:111], v[224:227], v[152:155], v[108:111]\n"
      "v_fma_f32 v188, v144, v144, s52\n"
      "v_fmac_f32_e64 v188, v145, v145\n"
      "v_cvt_pk_bf16_f32 v190, v190, v191\n"
      "v_cvt_pk_bf16_f32 v89, v172, v173\n"
      "v_mfma_f32_16x16x32_bf16 v[108:111], v[228:231], v[184:187], v[108:111]\n"
      "v_cvt_pk_bf16_f32 v175, v176, v177\n"
      "v_max_f32_dpp v96, v96, v96 row_ror:2 row_mask:0xf bank_mask:0xf\n"
      "v_mfma_f32_16x16x32_bf16 v[202:205], v[32:35], v[88:91], v[202:205]\n"
      "s_or_b32 s95, s95, s48\n"
      "v_max_f32_dpp v96, v96, v96 row_ror:1 row_mask:0xf bank_mask:0xf\n"
      "v_mul_f32 v162, v210, v162\n"
      "v_mfma_f32_16x16x32_bf16 v[140:143], v[244:247], v[102:105], v[140:143]\n"
      "v_fmac_f32 v188, v146, v146\n"
      "v_mul_f32 v192, v210, v192\n"
      "v_fmac_f32_e64 v188, v147, v147\n"
      "v_max_f32_dpp v96, v96, v96 row_bcast:15 row_mask:0xa bank_mask:0xf\n"
      "v_mfma_f32_16x16x32_bf16 v[140:143], v[248:251], v[152:155], v[140:143]\n"
      "ds_write_b64 v4, v[174:175] offset:18688\n"
      "v_max_f32_dpp v96, v96, v96 row_bcast:31 row_mask:0xc bank_mask:0xf\n"
      "v_mfma_f32_16x16x32_bf16 v[140:143], v[252:255], v[184:187], v[140:143]\n"
      "s_cmp_ge_u32 s95, 0x40000000\n"
      "s_cselect_b32 s94, -1, -2\n"
      "v_readlane_b32 s72, v96, 63\n"
      "v_mfma_f32_16x16x32_bf16 v[102:105], v[194:197], v[158:161], 0\n"
      "v_cvt_pk_bf16_f32 v191, v192, v193\n"
      "v_subrev_f32 v120, s72, v120\n"
      "v_fmac_f32 v188, v108, v108\n"
      "v_rcp_f32 v202, v202\n"
      "v_exp_f32 v120, v120\n"
      "v_permlane16_swap_b32_e64 v218, v157\n"
      "v_cvt_pk_bf16_f32 v120, v120, v120\n"
      "v_mfma_f32_16x16x32_bf16 v[102:105], v[76:79], v[88:91], v[102:105]\n"
      "ds_write_b16 v13, v120\n"
      "v_mfma_f32_16x16x32_bf16 v[96:99], v[132:135], v[158:161], 0\n"
      "ds_read_b128 v[210:213], v14\n"
      "v_fmac_f32_e64 v188, v109, v109\n"
      "v_cvt_pk_bf16_f32 v193, v164, v165\n"
      "v_mfma_f32_16x16x32_bf16 v[96:99], v[80:83], v[88:91], v[96:99]\n"
      "ds_read_b128 v[184:187], v14 offset:64\n"
      "v_fmac_f32 v188, v110, v110\n"
      "v_mul_f32 v105, v202, v105\n"
      "v_fmac_f32_e64 v188, v111, v111\n"
      "v_cvt_pk_bf16_f32 v192, v162, v163\n"
      "v_mul_f32 v102, v202, v102\n"
      "v_add_f32 v218, v218, v157\n"
      "v_mul_f32 v103, v202, v103\n"
      "v_mul_f32 v96, v202, v96\n"
      "v_fmac_f32 v188, v140, v140\n"
      "v_mul_f32 v99, v202, v99\n"
      "v_mul_f32 v97, v202, v97\n"
      "v_mfma_f32_16x16x32_bf16 v[152:155], v[214:217], v[158:161], 0\n"
      "ds_write_b128 v3, v[190:193] offset:18688\n"
      "v_fmac_f32 v188, v141, v141\n"
      "v_cvt_pk_bf16_f32 v102, v102, v103\n"
      "v_mul_f32 v98, v202, v98\n"
      "v_fmac_f32 v188, v142, v142\n"
      "v_mfma_f32_16x16x32_bf16 v[152:155], v[84:87], v[88:91], v[152:155]\n"
      "s_waitcnt lgkmcnt(1)\n"
      "v_mul_f32 v104, v202, v104\n"
      "v_cvt_pk_bf16_f32 v103, v104, v105\n"
      "v_fmac_f32 v188, v143, v143\n"
      "v_cvt_pk_bf16_f32 v105, v98, v99\n"
      "v_mov_b32_e64 v157, v188\n"
      "v_cvt_pk_bf16_f32 v104, v96, v97\n"
      "s_add_u32 s43, s43, 1\n"
      "ds_write_b128 v3, v[102:105] offset:18944\n"
      "v_permlane16_swap_b32 v188, v157\n"
      "v_add_f32 v188, v188, v157\n"
      "v_mul_f32 v155, v202, v155\n"
      "v_mul_f32 v152, v202, v152\n"
      "v_mul_f32 v153, v202, v153\n"
      "v_permlane32_swap_b32 v218, v188\n"
      "v_add_f32 v218, v218, v188\n"
      "v_mfma_f32_16x16x32_bf16 v[188:191], v[210:213], v[32:35], 0\n"
      "ds_write_b32 v31, v218 offset:1024\n"
      "v_mul_f32 v154, v202, v154\n"
      "v_cvt_pk_bf16_f32 v152, v152, v153\n"
      "v_cvt_pk_bf16_f32 v153, v154, v155\n"
      "s_and_b32 s47, s47, -4\n"
      "ds_write_b64 v4, v[152:153] offset:18944\n"
      "s_cmp_gt_u32 s42, s44\n"
      "s_cselect_b32 s95, s94, s95\n"
      "s_cmp_ge_u32 s95, 0x40000000\n"
      "s_cselect_b64 vcc, -1, s[56:57]\n"
      "v_add_u32 v11, 2336, v11\n"
      "v_add_u32 v10, 2336, v10\n"
      "v_mfma_f32_16x16x32_bf16 v[188:191], v[184:187], v[32:35], v[188:191]\n"
      "s_add_u32 s42, s42, 1\n"
      "s_waitcnt lgkmcnt(0)\n"
      "s_barrier\n"
      "s_cbranch_vccz L_sel0_%=\n"
      "L_disp0_%=:\n"
      "s_cmp_lt_u32 s95, 0x40000000\n"
      "s_cbranch_scc1 L_nofix_18_%=\n"
      "s_cmp_eq_u32 s95, -2\n"
      "s_cbranch_scc1 L_tail0_%=\n"
      "s_nop 2\n"
      "v_lshlrev_b32 v202, 3, v36\n"
      "s_add_u32 s72, s98, L_cold_%=-L_ipf0_%=\n"
      "s_addc_u32 s73, s99, 0\n"
      "s_mov_b32 s74, L_cend_%=-L_cold_%=\n"
      "s_mov_b32 s75, 0x20000\n"
      "s_mov_b32 m0, 161792\n"
      "s_nop 0\n"
      "buffer_load_dwordx4 v202, s[72:75], 0 offen  lds\n"
      "s_lshr_b32 s92, s45, 30\n"
      "s_or_b32 s92, s92, s66\n"
      "s_cbranch_scc0 L_nf_19_%=\n"
      "s_bitcmp1_b32 s45, 31\n"
      "s_cselect_b32 s75, 100270464, 0\n"
      "s_bitcmp1_b32 s45, 30\n"
      "s_cselect_b32 s76, 196224, 0\n"
      "s_and_b32 s74, s45, 0x3fffffff\n"
      "s_cmp_ge_u32 s45, 0x40000000\n"
      "s_cselect_b32 s74, s74, 0\n"
      "s_cselect_b32 s45, 0, s45\n"
      "s_sub_u32 s74, s74, s66\n"
      "s_add_u32 s66, s66, s74\n"
      "s_sub_u32 s75, s75, s67\n"
      "s_add_u32 s67, s67, s75\n"
      "s_sub_u32 s76, s76, s68\n"
      "s_add_u32 s68, s68, s76\n"
      "v_add_u32 v16, s74, v16\n"
      "v_add_u32 v17, s74, v17\n"
      "v_add_u32 v18, s74, v18\n"
      "v_add_u32 v19, s74, v19\n"
      "v_add_u32 v15, s74, v15\n"
      "s_cmp_eq_u32 s75, 0\n"
      "s_cbranch_scc1 L_ax_20_%=\n"
      "v_bfe_i32 v202, v27, 0, 1\n"
      "v_and_b32 v202, s75, v202\n"
      "v_sub_u32 v16, v16, v202\n"
      "v_bfe_i32 v202, v27, 2, 1\n"
      "v_and_b32 v202, s75, v202\n"
      "v_sub_u32 v17, v17, v202\n"
      "v_bfe_i32 v202, v27, 4, 1\n"
      "v_and_b32 v202, s75, v202\n"
      "v_sub_u32 v18, v18, v202\n"
      "v_bfe_i32 v202, v27, 6, 1\n"
      "v_and_b32 v202, s75, v202\n"
      "v_sub_u32 v19, v19, v202\n"
      "v_bfe_i32 v202, v27, 8, 1\n"
      "v_and_b32 v202, s75, v202\n"
      "v_sub_u32 v15, v15, v202\n"
      "L_ax_20_%=:\n"
      "s_cmp_eq_u32 s76, 0\n"
      "s_cbranch_scc1 L_ax_21_%=\n"
      "v_bfe_i32 v202, v27, 1, 1\n"
      "v_and_b32 v202, s76, v202\n"
      "v_sub_u32 v16, v16, v202\n"
      "v_bfe_i32 v202, v27, 3, 1\n"
      "v_and_b32 v202, s76, v202\n"
      "v_sub_u32 v17, v17, v202\n"
      "v_bfe_i32 v202, v27, 5, 1\n"
      "v_and_b32 v202, s76, v202\n"
      "v_sub_u32 v18, v18, v202\n"
      "v_bfe_i32 v202, v27, 7, 1\n"
      "v_and_b32 v202, s76, v202\n"
      "v_sub_u32 v19, v19, v202\n"
      "v_bfe_i32 v202, v27, 9, 1\n"
      "v_and_b32 v202, s76, v202\n"
      "v_sub_u32 v15, v15, v202\n"
      "L_ax_21_%=:\n"
      "s_or_b32 s48, s66, s69\n"
      "s_cmp_lg_u32 s48, 0\n"
      "s_cselect_b32 s48, 0x40000000, 0\n"
      "L_nf_19_%=:\n"
      "s_lshr_b32 s92, s46, 30\n"
      "s_or_b32 s92, s92, s69\n"
      "s_cbranch_scc0 L_nf_22_%=\n"
      "s_bitcmp1_b32 s46, 31\n"
      "s_cselect_b32 s75, 100270464, 0\n"
      "s_bitcmp1_b32 s46, 30\n"
      "s_cselect_b32 s76, 196224, 0\n"
      "s_and_b32 s74, s46, 0x3fffffff\n"
      "s_cmp_ge_u32 s46, 0x40000000\n"
      "s_cselect_b32 s74, s74, 0\n"
      "s_cselect_b32 s46, 0, s46\n"
      "s_sub_u32 s74, s74, s69\n"
      "s_add_u32 s69, s69, s74\n"
      "s_sub_u32 s75, s75, s70\n"
      "s_add_u32 s70, s70, s75\n"
      "s_sub_u32 s76, s76, s71\n"
      "s_add_u32 s71, s71, s76\n"
      "v_add_u32 v20, s74, v20\n"
      "v_add_u32 v21, s74, v21\n"
      "v_add_u32 v22, s74, v22\n"
      "v_add_u32 v23, s74, v23\n"
      "v_add_u32 v25, s74, v25\n"
      "s_cmp_eq_u32 s75, 0\n"
      "s_cbranch_scc1 L_ax_23_%=\n"
      "v_bfe_i32 v202, v27, 10, 1\n"
      "v_and_b32 v202, s75, v202\n"
      "v_sub_u32 v20, v20, v202\n"
      "v_bfe_i32 v202, v27, 12, 1\n"
      "v_and_b32 v202, s75, v202\n"
      "v_sub_u32 v21, v21, v202\n"
      "v_bfe_i32 v202, v27, 14, 1\n"
      "v_and_b32 v202, s75, v202\n"
      "v_sub_u32 v22, v22, v202\n"
      "v_bfe_i32 v202, v27, 16, 1\n"
      "v_and_b32 v202, s75, v202\n"
      "v_sub_u32 v23, v23, v202\n"
      "v_bfe_i32 v202, v27, 18, 1\n"
      "v_and_b32 v202, s75, v202\n"
      "v_sub_u32 v25, v25, v202\n"
      "L_ax_23_%=:\n"
      "s_cmp_eq_u32 s76, 0\n"
      "s_cbranch_scc1 L_ax_24_%=\n"
      "v_bfe_i32 v202, v27, 11, 1\n"
      "v_and_b32 v202, s76, v202\n"
      "v_sub_u32 v20, v20, v202\n"
      "v_bfe_i32 v202, v27, 13, 1\n"
      "v_and_b32 v202, s76, v202\n"
      "v_sub_u32 v21, v21, v202\n"
      "v_bfe_i32 v202, v27, 15, 1\n"
      "v_and_b32 v202, s76, v202\n"
      "v_sub_u32 v22, v22, v202\n"
      "v_bfe_i32 v202, v27, 17, 1\n"
      "v_and_b32 v202, s76, v202\n"
      "v_sub_u32 v23, v23, v202\n"
      "v_bfe_i32 v202, v27, 19, 1\n"
      "v_and_b32 v202, s76, v202\n"
      "v_sub_u32 v25, v25, v202\n"
      "L_ax_24_%=:\n"
      "s_or_b32 s48, s66, s69\n"
      "s_cmp_lg_u32 s48, 0\n"
      "s_cselect_b32 s48, 0x40000000, 0\n"
      "L_nf_22_%=:\n"
      "s_sub_u32 s92, s42, 1\n"
      "s_cmp_gt_u32 s92, s44\n"
      "s_cbranch_scc1 L_tail0_%=\n"
      "L_nofix_18_%=:\n"
      "s_cmp_lg_u64 s[56:57], 0\n"
      "s_cbranch_scc1 L_slow0_%=\n"
      "L_sel0_%=:\n"
      "s_cmp_lg_u64 s[54:55], 0\n"
      "s_cbranch_scc1 L_max0_%=\n"
      "s_branch L_loop0_%=\n"
      "L_slow0_%=:\n"
      "s_nop 4\n"
      "v_mfma_f32_16x16x32_bf16 v[202:205], v[210:213], v[194:197], 0\n"
      "ds_read_b128 v[174:177], v0\n"
      "v_subrev_u32 v106, 115456, v9\n"
      "v_mfma_f32_16x16x32_bf16 v[202:205], v[184:187], v[76:79], v[202:205]\n"
      "ds_read_b128 v[98:101], v1\n"
      "v_subrev_u32 v218, 115456, v9\n"
      "v_mfma_f32_16x16x32_bf16 v[102:105], v[210:213], v[132:135], 0\n"
      "ds_read_b128 v[162:165], v0 offset:6144\n"
      "v_bfe_u32 v78, v218, 4, 4\n"
      "v_mfma_f32_16x16x32_bf16 v[102:105], v[184:187], v[80:83], v[102:105]\n"
      "ds_read_b128 v[196:199], v9 offset:1024\n"
      "v_mul_u32_u24 v82, 98, v78\n"
      "v_mfma_f32_16x16x32_bf16 v[192:195], v[210:213], v[214:217], 0\n"
      "s_add_u32 s72, s42, 1\n"
      "v_lshrrev_b32 v218, 8, v218\n"
      "v_readlane_b32 s77, v38, s72\n"
      "v_mfma_f32_16x16x32_bf16 v[192:195], v[184:187], v[84:87], v[192:195]\n"
      "s_mov_b32 m0, 110592\n"
      "v_mov_b32_dpp v188, v188 quad_perm:[0,0,0,0] row_mask:0xf bank_mask:0xf\n"
      "v_rcp_f32_e64 v188, v188\n"
      "s_waitcnt lgkmcnt(3)\n"
      "v_mfma_f32_16x16x32_bf16 v[184:187], a[0:3], v[174:177], 0\n"
      "s_and_b32 s73, s77, -16\n"
      "v_cndmask_b32 v202, v202, v102, s[58:59]\n"
      "v_lshl_add_u32 v82, v218, 3, v82\n"
      "v_mfma_f32_16x16x32_bf16 v[210:213], a[24:27], v[174:177], 0\n"
      "buffer_load_dwordx4 v106, s[28:31], s73 offen  lds\n"
      "v_mfma_f32_16x16x32_bf16 v[102:105], a[48:51], v[174:177], 0\n"
      "s_mov_b32 m0, 111616\n"
      "s_waitcnt lgkmcnt(0)\n"
      "v_add_f32 v196, v196, v197\n"
      "v_mfma_f32_16x16x32_bf16 v[128:131], a[72:75], v[174:177], 0\n"
      "ds_read_b128 v[158:161], v1 offset:6144\n"
      "v_add_f32 v198, v198, v199\n"
      "v_mfma_f32_16x16x32_bf16 v[154:157], a[96:99], v[174:177], 0\n"
      "ds_read_b128 v[120:123], v0 offset:12288\n"
      "v_add_f32 v196, v196, v198\n"
      "v_mfma_f32_16x16x32_bf16 v[148:151], a[120:123], v[174:177], 0\n"
      "v_rsq_f32_e64 v196, v196\n"
      "v_mfma_f32_16x16x32_bf16 v[184:187], a[4:7], v[98:101], v[184:187]\n"
      "s_add_u32 s73, s73, 1024\n"
      "v_mul_f32 v196, 0x415db3d7, v196\n"
      "v_mov_b32_e64 v198, v196\n"
      "v_mfma_f32_16x16x32_bf16 v[184:187], a[8:11], v[162:165], v[184:187]\n"
      "buffer_load_dwordx4 v106, s[28:31], s73 offen  lds\n"
      "v_mfma_f32_16x16x32_bf16 v[210:213], a[28:31], v[98:101], v[210:213]\n"
      "s_mov_b32 m0, 112640\n"
      "v_permlane32_swap_b32_e64 v196, v198\n"
      "v_mfma_f32_16x16x32_bf16 v[210:213], a[32:35], v[162:165], v[210:213]\n"
      "ds_read_b128 v[136:139], v1 offset:12288\n"
      "v_mov_b32 v197, v196\n"
      "v_mfma_f32_16x16x32_bf16 v[102:105], a[52:55], v[98:101], v[102:105]\n"
      "s_add_u32 s73, s73, 1024\n"
      "v_permlane16_swap_b32 v196, v197\n"
      "v_mfma_f32_16x16x32_bf16 v[102:105], a[56:59], v[162:165], v[102:105]\n"
      "buffer_load_dwordx4 v106, s[28:31], s73 offen  lds\n"
      "v_mul_f32_e64 v206, v196, v206\n"
      "v_mfma_f32_16x16x32_bf16 v[128:131], a[76:79], v[98:101], v[128:131]\n"
      "s_add_u32 s73, s73, 1024\n"
      "v_mul_f32 v169, v197, v169\n"
      "v_mul_f32 v124, v197, v124\n"
      "v_mfma_f32_16x16x32_bf16 v[128:131], a[80:83], v[162:165], v[128:131]\n"
      "v_mul_f32 v209, v196, v209\n"
      "v_mul_f32 v180, v197, v180\n"
      "v_mfma_f32_16x16x32_bf16 v[154:157], a[100:103], v[98:101], v[154:157]\n"
      "s_mov_b32 m0, 113664\n"
      "v_mul_f32 v114, v196, v114\n"
      "v_mul_f32 v116, v196, v116\n"
      "v_mfma_f32_16x16x32_bf16 v[154:157], a[104:107], v[162:165], v[154:157]\n"
      "buffer_load_dwordx4 v106, s[28:31], s73 offen  lds\n"
      "v_mfma_f32_16x16x32_bf16 v[148:151], a[124:127], v[98:101], v[148:151]\n"
      "v_mul_f32 v115, v196, v115\n"
      "v_mul_f32 v208, v196, v208\n"
      "v_mfma_f32_16x16x32_bf16 v[148:151], a[128:131], v[162:165], v[148:151]\n"
      "s_waitcnt lgkmcnt(0)\n"
      "v_mul_f32 v222, v196, v222\n"
      "v_cvt_pk_bf16_f32 v114, v114, v115\n"
      "v_mfma_f32_16x16x32_bf16 v[184:187], a[12:15], v[158:161], v[184:187]\n"
      "s_add_u32 s73, s73, 1024\n"
      "v_mul_f32 v207, v196, v207\n"
      "v_mul_f32 v223, v196, v223\n"
      "v_mfma_f32_16x16x32_bf16 v[184:187], a[16:19], v[120:123], v[184:187]\n"
      "s_mov_b32 m0, 114688\n"
      "v_mul_f32_e64 v117, v196, v117\n"
      "v_mul_f32 v220, v196, v220\n"
      "v_mfma_f32_16x16x32_bf16 v[184:187], a[20:23], v[136:139], v[184:187]\n"
      "s_mov_b32 exec_hi, 0xffff\n"
      "buffer_load_dwordx4 v106, s[28:31], s73 offen  lds\n"
      "s_mov_b32 exec_hi, -1\n"
      "v_mfma_f32_16x16x32_bf16 v[210:213], a[36:39], v[158:161], v[210:213]\n"
      "v_cvt_pk_bf16_f32 v115, v116, v117\n"
      "v_cvt_pk_bf16_f32 v116, v206, v207\n"
      "v_mfma_f32_16x16x32_bf16 v[210:213], a[40:43], v[120:123], v[210:213]\n"
      "s_add_u32 m0, s64, 18432\n"
      "v_mul_f32_e64 v221, v196, v221\n"
      "v_cvt_pk_bf16_f32 v117, v208, v209\n"
      "v_mfma_f32_16x16x32_bf16 v[210:213], a[44:47], v[136:139], v[210:213]\n"
      "ds_write_b128 v5, v[114:117]\n"
      "v_mfma_f32_16x16x32_bf16 v[102:105], a[60:63], v[158:161], v[102:105]\n"
      "buffer_load_dwordx4 v16, s[16:19], s45 offen  lds\n"
      "v_mfma_f32_16x16x32_bf16 v[102:105], a[64:67], v[120:123], v[102:105]\n"
      "v_cvt_pk_bf16_f32 v220, v220, v221\n"
      "v_cvt_pk_bf16_f32 v221, v222, v223\n"
      "v_mfma_f32_16x16x32_bf16 v[102:105], a[68:71], v[136:139], v[102:105]\n"
      "ds_write_b64 v6, v[220:221]\n"
      "v_mfma_f32_16x16x32_bf16 v[128:131], a[84:87], v[158:161], v[128:131]\n"
      "s_add_u32 m0, s64, 22528\n"
      "v_mul_f32 v125, v197, v125\n"
      "v_mul_f32 v168, v197, v168\n"
      "v_mfma_f32_16x16x32_bf16 v[128:131], a[88:91], v[120:123], v[128:131]\n"
      "buffer_load_dwordx4 v17, s[16:19], s45 offen  lds\n"
      "v_mfma_f32_16x16x32_bf16 v[128:131], a[92:95], v[136:139], v[128:131]\n"
      "s_add_u32 m0, s64, 26624\n"
      "v_mul_f32 v167, v197, v167\n"
      "v_mul_f32 v181, v197, v181\n"
      "v_mfma_f32_16x16x32_bf16 v[154:157], a[108:111], v[158:161], v[154:157]\n"
      "v_mul_f32 v166, v197, v166\n"
      "v_cvt_pk_bf16_f32 v180, v180, v181\n"
      "v_mfma_f32_16x16x32_bf16 v[154:157], a[112:115], v[120:123], v[154:157]\n"
      "v_mul_f32 v183, v197, v183\n"
      "v_mul_f32 v182, v197, v182\n"
      "v_mfma_f32_16x16x32_bf16 v[154:157], a[116:119], v[136:139], v[154:157]\n"
      "buffer_load_dwordx4 v18, s[16:19], s45 offen  lds\n"
      "v_mfma_f32_16x16x32_bf16 v[148:151], a[132:135], v[158:161], v[148:151]\n"
      "v_mul_f32 v126, v197, v126\n"
      "v_mul_f32_e64 v141, v198, v141\n"
      "v_mfma_f32_16x16x32_bf16 v[148:151], a[136:139], v[120:123], v[148:151]\n"
      "v_cvt_pk_bf16_f32 v181, v182, v183\n"
      "v_cvt_pk_bf16_f32 v182, v124, v125\n"
      "v_mfma_f32_16x16x32_bf16 v[148:151], a[140:143], v[136:139], v[148:151]\n"
      "s_add_u32 m0, s64, 30720\n"
      "v_cvt_pk_bf16_f32 v166, v166, v167\n"
      "v_mul_f32_e64 v127, v197, v127\n"
      "v_mfma_f32_16x16x32_bf16 v[194:197], v[174:177], a[144:147], 0\n"
      "buffer_load_dwordx4 v19, s[16:19], s45 offen  lds\n"
      "v_mfma_f32_16x16x32_bf16 v[194:197], v[98:101], a[148:151], v[194:197]\n"
      "s_add_u32 m0, s65, 34816\n"
      "v_cvt_pk_bf16_f32 v183, v126, v127\n"
      "v_cvt_pk_bf16_f32 v167, v168, v169\n"
      "v_mfma_f32_16x16x32_bf16 v[194:197], v[162:165], a[152:155], v[194:197]\n"
      "ds_write_b128 v5, v[180:183] offset:6144\n"
      "v_mfma_f32_16x16x32_bf16 v[194:197], v[158:161], a[156:159], v[194:197]\n"
      "buffer_load_dwordx4 v15, s[16:19], s45 offen  lds\n"
      "v_mfma_f32_16x16x32_bf16 v[194:197], v[120:123], a[160:163], v[194:197]\n"
      "ds_read_b128 v[114:117], v0 offset:2048\n"
      "v_mul_f32_e64 v147, v198, v147\n"
      "v_mfma_f32_16x16x32_bf16 v[194:197], v[136:139], a[164:167], v[194:197]\n"
      "ds_write_b64 v6, v[166:167] offset:6144\n"
      "v_mfma_f32_16x16x32_bf16 v[132:135], v[174:177], a[168:171], 0\n"
      "ds_read_b128 v[206:209], v0 offset:14336\n"
      "v_fma_f32 v203, v184, v184, s51\n"
      "v_mfma_f32_16x16x32_bf16 v[132:135], v[98:101], a[172:175], v[132:135]\n"
      "ds_read_b128 v[220:223], v1 offset:14336\n"
      "v_fma_f32 v219, v128, v128, s51\n"
      "v_mfma_f32_16x16x32_bf16 v[132:135], v[162:165], a[176:179], v[132:135]\n"
      "v_fmac_f32 v219, v129, v129\n"
      "v_mul_f32 v145, v198, v145\n"
      "v_mfma_f32_16x16x32_bf16 v[132:135], v[158:161], a[180:183], v[132:135]\n"
      "ds_read_b128 v[180:183], v0 offset:8192\n"
      "v_mul_f32_e64 v140, v198, v140\n"
      "v_mfma_f32_16x16x32_bf16 v[132:135], v[120:123], a[184:187], v[132:135]\n"
      "buffer_load_dwordx4 v[124:127], v36, s[28:31], s47 offen\n"
      "v_mfma_f32_16x16x32_bf16 v[132:135], v[136:139], a[188:191], v[132:135]\n"
      "ds_read_b128 v[166:169], v1 offset:2048\n"
      "v_mul_f32_e64 v142, v198, v142\n"
      "v_mfma_f32_16x16x32_bf16 v[214:217], v[174:177], a[192:195], 0\n"
      "ds_read_b128 v[174:177], v1 offset:8192\n"
      "v_cndmask_b32 v202, v202, v192, s[60:61]\n"
      "v_mfma_f32_16x16x32_bf16 v[214:217], v[98:101], a[196:199], v[214:217]\n"
      "v_subrev_u32 v119, 115456, v9\n"
      "v_fmac_f32 v219, v130, v130\n"
      "v_mfma_f32_16x16x32_bf16 v[214:217], v[162:165], a[200:203], v[214:217]\n"
      "buffer_load_dword v86, v29, s[28:31], s47 offen\n"
      "v_mfma_f32_16x16x32_bf16 v[214:217], v[158:161], a[204:207], v[214:217]\n"
      "v_fmac_f32 v203, v185, v185\n"
      "v_mul_f32 v143, v198, v143\n"
      "v_mfma_f32_16x16x32_bf16 v[214:217], v[120:123], a[208:211], v[214:217]\n"
      "v_cvt_pk_bf16_f32 v194, v194, v195\n"
      "v_mul_f32 v110, v198, v110\n"
      "v_mfma_f32_16x16x32_bf16 v[214:217], v[136:139], a[212:215], v[214:217]\n"
      "s_waitcnt lgkmcnt(1)\n"
      "v_fmac_f32 v203, v186, v186\n"
      "v_lshrrev_b32 v119, 3, v119\n"
      "v_lshlrev_b32 v218, 2, v218\n"
      "v_cvt_pk_bf16_f32 v195, v196, v197\n"
      "v_mfma_f32_16x16x32_bf16 v[190:193], a[0:3], v[114:117], 0\n"
      "v_fmac_f32 v203, v187, v187\n"
      "v_min_u32 v119, 96, v119\n"
      "v_mfma_f32_16x16x32_bf16 v[190:193], a[4:7], v[166:169], v[190:193]\n"
      "v_mul_f32 v202, v188, v202\n"
      "v_cvt_pk_bf16_f32 v132, v132, v133\n"
      "v_mfma_f32_16x16x32_bf16 v[190:193], a[8:11], v[180:183], v[190:193]\n"
      "s_waitcnt lgkmcnt(0)\n"
      "v_mul_f32 v108, v198, v108\n"
      "v_fmac_f32 v219, v131, v131\n"
      "v_mfma_f32_16x16x32_bf16 v[190:193], a[12:15], v[174:177], v[190:193]\n"
      "v_fmac_f32 v203, v210, v210\n"
      "v_mul_f32 v146, v198, v146\n"
      "v_mfma_f32_16x16x32_bf16 v[190:193], a[16:19], v[206:209], v[190:193]\n"
      "v_fmac_f32 v219, v154, v154\n"
      "v_mul_f32 v111, v198, v111\n"
      "v_fmac_f32 v219, v155, v155\n"
      "v_mfma_f32_16x16x32_bf16 v[190:193], a[20:23], v[220:223], v[190:193]\n"
      "v_fmac_f32 v203, v211, v211\n"
      "v_mul_f32 v144, v198, v144\n"
      "v_mfma_f32_16x16x32_bf16 v[158:161], a[24:27], v[114:117], 0\n"
      "s_and_b32 s77, s77, 15\n"
      "v_cvt_pk_bf16_f32 v140, v140, v141\n"
      "v_fmac_f32 v203, v212, v212\n"
      "v_mfma_f32_16x16x32_bf16 v[158:161], a[28:31], v[166:169], v[158:161]\n"
      "v_cvt_pk_bf16_f32 v144, v144, v145\n"
      "v_fma_f32 v80, v190, v190, s51\n"
      "v_mfma_f32_16x16x32_bf16 v[158:161], a[32:35], v[180:183], v[158:161]\n"
      "v_mul_f32 v109, v198, v109\n"
      "v_fmac_f32 v203, v213, v213\n"
      "v_mfma_f32_16x16x32_bf16 v[158:161], a[36:39], v[174:177], v[158:161]\n"
      "v_cvt_pk_bf16_f32 v145, v146, v147\n"
      "v_fmac_f32_e64 v219, v156, v156\n"
      "v_mfma_f32_16x16x32_bf16 v[158:161], a[40:43], v[206:209], v[158:161]\n"
      "v_fmac_f32 v219, v157, v157\n"
      "v_fmac_f32 v219, v148, v148\n"
      "v_mfma_f32_16x16x32_bf16 v[158:161], a[44:47], v[220:223], v[158:161]\n"
      "v_fmac_f32 v203, v102, v102\n"
      "v_fmac_f32 v80, v191, v191\n"
      "v_mfma_f32_16x16x32_bf16 v[98:101], a[48:51], v[114:117], 0\n"
      "v_fmac_f32 v203, v103, v103\n"
      "v_cvt_pk_bf16_f32 v146, v108, v109\n"
      "v_mfma_f32_16x16x32_bf16 v[98:101], a[52:55], v[166:169], v[98:101]\n"
      "v_fmac_f32 v219, v149, v149\n"
      "v_fmac_f32 v203, v104, v104\n"
      "v_mfma_f32_16x16x32_bf16 v[98:101], a[56:59], v[180:183], v[98:101]\n"
      "ds_read_b128 v[106:109], v1 offset:10240\n"
      "v_fmac_f32 v203, v105, v105\n"
      "v_mfma_f32_16x16x32_bf16 v[98:101], a[60:63], v[174:177], v[98:101]\n"
      "ds_read_b128 v[120:123], v0 offset:4096\n"
      "v_fmac_f32 v219, v150, v150\n"
      "v_mfma_f32_16x16x32_bf16 v[98:101], a[64:67], v[206:209], v[98:101]\n"
      "v_cvt_pk_bf16_f32 v133, v134, v135\n"
      "v_fmac_f32 v219, v151, v151\n"
      "v_mfma_f32_16x16x32_bf16 v[98:101], a[68:71], v[220:223], v[98:101]\n"
      "s_nop 0\n"
      "v_permlane16_swap_b32 v203, v219\n"
      "v_mfma_f32_16x16x32_bf16 v[198:201], a[72:75], v[114:117], 0\n"
      "v_add_f32 v203, v203, v219\n"
      "v_mov_b32 v219, v203\n"
      "v_mfma_f32_16x16x32_bf16 v[198:201], a[76:79], v[166:169], v[198:201]\n"
      "s_nop 0\n"
      "v_permlane32_swap_b32 v203, v219\n"
      "v_mfma_f32_16x16x32_bf16 v[198:201], a[80:83], v[180:183], v[198:201]\n"
      "v_fmac_f32 v80, v192, v192\n"
      "v_add_f32 v203, v203, v219\n"
      "v_mfma_f32_16x16x32_bf16 v[198:201], a[84:87], v[174:177], v[198:201]\n"
      "v_rsq_f32 v203, v203\n"
      "v_mfma_f32_16x16x32_bf16 v[198:201], a[88:91], v[206:209], v[198:201]\n"
      "v_mov_b32 v219, v203\n"
      "v_cvt_pk_bf16_f32 v141, v142, v143\n"
      "v_mfma_f32_16x16x32_bf16 v[198:201], a[92:95], v[220:223], v[198:201]\n"
      "v_permlane16_swap_b32 v203, v219\n"
      "v_mfma_f32_16x16x32_bf16 v[136:139], a[96:99], v[114:117], 0\n"
      "ds_write_b64 v6, v[140:141] offset:12288\n"
      "v_mul_f32 v149, v219, v149\n"
      "v_mfma_f32_16x16x32_bf16 v[136:139], a[100:103], v[166:169], v[136:139]\n"
      "v_fmac_f32 v80, v193, v193\n"
      "v_mul_f32 v154, v219, v154\n"
      "v_mfma_f32_16x16x32_bf16 v[136:139], a[104:107], v[180:183], v[136:139]\n"
      "v_fmac_f32 v80, v158, v158\n"
      "v_mul_f32 v128, v219, v128\n"
      "v_mfma_f32_16x16x32_bf16 v[136:139], a[108:111], v[174:177], v[136:139]\n"
      "v_fma_f32 v77, v198, v198, s51\n"
      "v_fmac_f32 v77, v199, v199\n"
      "v_mul_f32 v203, s50, v203\n"
      "v_sub_u32_e64 v78, v78, v218\n"
      "v_mfma_f32_16x16x32_bf16 v[136:139], a[112:115], v[206:209], v[136:139]\n"
      "v_fmac_f32 v80, v159, v159\n"
      "v_mul_f32 v130, v219, v130\n"
      "v_mfma_f32_16x16x32_bf16 v[136:139], a[116:119], v[220:223], v[136:139]\n"
      "v_mul_f32 v157, v219, v157\n"
      "v_fmac_f32 v80, v160, v160\n"
      "v_mfma_f32_16x16x32_bf16 v[162:165], a[120:123], v[114:117], 0\n"
      "v_mul_f32 v102, v203, v102\n"
      "v_cvt_pk_bf16_f32 v214, v214, v215\n"
      "v_mul_f32 v155, v219, v155\n"
      "v_mfma_f32_16x16x32_bf16 v[162:165], a[124:127], v[166:169], v[162:165]\n"
      "v_fmac_f32 v77, v200, v200\n"
      "v_cvt_pk_bf16_f32 v147, v110, v111\n"
      "v_mfma_f32_16x16x32_bf16 v[162:165], a[128:131], v[180:183], v[162:165]\n"
      "v_mul_f32 v185, v203, v185\n"
      "v_fmac_f32_e64 v77, v201, v201\n"
      "v_mfma_f32_16x16x32_bf16 v[162:165], a[132:135], v[174:177], v[162:165]\n"
      "v_fmac_f32 v77, v136, v136\n"
      "v_fmac_f32 v80, v161, v161\n"
      "v_mul_f32 v131, v219, v131\n"
      "v_fmac_f32 v77, v137, v137\n"
      "v_mfma_f32_16x16x32_bf16 v[162:165], a[136:139], v[206:209], v[162:165]\n"
      "v_fmac_f32 v80, v98, v98\n"
      "v_fmac_f32 v77, v138, v138\n"
      "v_mul_f32 v151, v219, v151\n"
      "v_mfma_f32_16x16x32_bf16 v[162:165], a[140:143], v[220:223], v[162:165]\n"
      "v_mul_f32 v187, v203, v187\n"
      "v_fmac_f32_e64 v80, v99, v99\n"
      "v_mfma_f32_16x16x32_bf16 v[140:143], v[114:117], a[144:147], 0\n"
      "v_mul_f32 v148, v219, v148\n"
      "v_mul_f32 v213, v203, v213\n"
      "v_mfma_f32_16x16x32_bf16 v[140:143], v[166:169], a[148:151], v[140:143]\n"
      "v_add_u32 v82, s77, v82\n"
      "v_fmac_f32 v77, v139, v139\n"
      "v_mfma_f32_16x16x32_bf16 v[140:143], v[180:183], a[152:155], v[140:143]\n"
      "v_cvt_pk_bf16_f32 v215, v216, v217\n"
      "v_mul_f32_e64 v156, v219, v156\n"
      "v_mfma_f32_16x16x32_bf16 v[140:143], v[174:177], a[156:159], v[140:143]\n"
      "s_add_u32 s73, s73, 1024\n"
      "v_fmac_f32 v77, v162, v162\n"
      "v_mul_f32 v129, v219, v129\n"
      "v_mfma_f32_16x16x32_bf16 v[140:143], v[206:209], a[160:163], v[140:143]\n"
      "v_mul_f32 v150, v219, v150\n"
      "v_mul_f32 v211, v203, v211\n"
      "v_mfma_f32_16x16x32_bf16 v[140:143], v[220:223], a[164:167], v[140:143]\n"
      "v_fmac_f32 v77, v163, v163\n"
      "v_cvt_pk_bf16_f32 v128, v128, v129\n"
      "v_mfma_f32_16x16x32_bf16 v[216:219], v[114:117], a[168:171], 0\n"
      "v_fmac_f32 v80, v100, v100\n"
      "v_cvt_pk_bf16_f32 v129, v130, v131\n"
      "v_mfma_f32_16x16x32_bf16 v[216:219], v[166:169], a[172:175], v[216:219]\n"
      "ds_write_b128 v5, v[144:147] offset:12288\n"
      "v_mfma_f32_16x16x32_bf16 v[216:219], v[180:183], a[176:179], v[216:219]\n"
      "v_cvt_pk_bf16_f32 v131, v156, v157\n"
      "v_cvt_pk_bf16_f32 v196, v140, v141\n"
      "v_mul_f32 v184, v203, v184\n"
      "v_mfma_f32_16x16x32_bf16 v[216:219], v[174:177], a[180:183], v[216:219]\n"
      "v_mul_f32 v103, v203, v103\n"
      "v_fmac_f32 v77, v164, v164\n"
      "v_mfma_f32_16x16x32_bf16 v[216:219], v[206:209], a[184:187], v[216:219]\n"
      "v_fmac_f32 v80, v101, v101\n"
      "v_add_u32_e64 v119, s77, v119\n"
      "v_mfma_f32_16x16x32_bf16 v[216:219], v[220:223], a[188:191], v[216:219]\n"
      "ds_read_b128 v[144:147], v0 offset:10240\n"
      "v_cvt_pk_bf16_f32 v148, v148, v149\n"
      "v_mfma_f32_16x16x32_bf16 v[110:113], v[114:117], a[192:195], 0\n"
      "ds_read_b128 v[114:117], v1 offset:4096\n"
      "v_mul_f32 v186, v203, v186\n"
      "v_mfma_f32_16x16x32_bf16 v[110:113], v[166:169], a[196:199], v[110:113]\n"
      "ds_read_b128 v[166:169], v0 offset:16384\n"
      "v_mul_f32 v210, v203, v210\n"
      "v_mfma_f32_16x16x32_bf16 v[110:113], v[180:183], a[200:203], v[110:113]\n"
      "ds_read_b128 v[180:183], v1 offset:16384\n"
      "v_fmac_f32 v77, v165, v165\n"
      "v_mfma_f32_16x16x32_bf16 v[110:113], v[174:177], a[204:207], v[110:113]\n"
      "s_nop 0\n"
      "v_permlane16_swap_b32 v80, v77\n"
      "v_mfma_f32_16x16x32_bf16 v[110:113], v[206:209], a[208:211], v[110:113]\n"
      "s_waitcnt lgkmcnt(4)\n"
      "v_add_f32 v80, v80, v77\n"
      "v_mov_b32 v77, v80\n"
      "v_mfma_f32_16x16x32_bf16 v[110:113], v[220:223], a[212:215], v[110:113]\n"
      "s_nop 0\n"
      "v_permlane32_swap_b32 v80, v77\n"
      "v_mfma_f32_16x16x32_bf16 v[174:177], a[0:3], v[120:123], 0\n"
      "s_waitcnt lgkmcnt(2)\n"
      "v_mul_f32 v104, v203, v104\n"
      "v_cvt_pk_bf16_f32 v184, v184, v185\n"
      "v_mfma_f32_16x16x32_bf16 v[174:177], a[4:7], v[114:117], v[174:177]\n"
      "v_add_f32 v80, v80, v77\n"
      "v_rsq_f32 v80, v80\n"
      "v_mfma_f32_16x16x32_bf16 v[174:177], a[8:11], v[144:147], v[174:177]\n"
      "ds_read_b128 v[206:209], v7 offset:18432\n"
      "v_cvt_pk_bf16_f32 v149, v150, v151\n"
      "v_mfma_f32_16x16x32_bf16 v[174:177], a[12:15], v[106:109], v[174:177]\n"
      "s_waitcnt lgkmcnt(1)\n"
      "v_mov_b32 v77, v80\n"
      "v_cvt_pk_bf16_f32 v185, v186, v187\n"
      "v_cvt_pk_bf16_f32 v102, v102, v103\n"
      "v_mfma_f32_16x16x32_bf16 v[174:177], a[16:19], v[166:169], v[174:177]\n"
      "v_permlane16_swap_b32 v80, v77\n"
      "v_cvt_pk_bf16_f32 v135, v218, v219\n"
      "v_mfma_f32_16x16x32_bf16 v[174:177], a[20:23], v[180:183], v[174:177]\n"
      "v_mul_f32 v139, v77, v139\n"
      "v_cvt_pk_bf16_f32 v202, v202, v202\n"
      "v_mfma_f32_16x16x32_bf16 v[170:173], a[24:27], v[120:123], 0\n"
      "ds_write_b16 v12, v202 offset:16\n"
      "v_mfma_f32_16x16x32_bf16 v[170:173], a[28:31], v[114:117], v[170:173]\n"
      "v_cvt_pk_bf16_f32 v134, v216, v217\n"
      "v_mul_f32 v80, s50, v80\n"
      "v_mfma_f32_16x16x32_bf16 v[170:173], a[32:35], v[144:147], v[170:173]\n"
      "v_mul_f32 v191, v80, v191\n"
      "v_fma_f32 v151, v174, v174, s51\n"
      "v_mfma_f32_16x16x32_bf16 v[170:173], a[36:39], v[106:109], v[170:173]\n"
      "v_cvt_pk_bf16_f32 v186, v210, v211\n"
      "v_cvt_pk_bf16_f32 v217, v112, v113\n"
      "v_mul_f32_e64 v160, v80, v160\n"
      "v_mfma_f32_16x16x32_bf16 v[170:173], a[40:43], v[166:169], v[170:173]\n"
      "v_fmac_f32 v151, v175, v175\n"
      "v_mul_f32 v212, v203, v212\n"
      "v_mfma_f32_16x16x32_bf16 v[170:173], a[44:47], v[180:183], v[170:173]\n"
      "v_cvt_pk_bf16_f32 v197, v142, v143\n"
      "v_cvt_pk_bf16_f32 v216, v110, v111\n"
      "v_mfma_f32_16x16x32_bf16 v[140:143], a[48:51], v[120:123], 0\n"
      "v_mul_f32 v136, v77, v136\n"
      "v_mul_f32 v198, v77, v198\n"
      "v_mfma_f32_16x16x32_bf16 v[140:143], a[52:55], v[114:117], v[140:143]\n"
      "v_mul_f32 v162, v77, v162\n"
      "v_mul_f32 v105, v203, v105\n"
      "v_mfma_f32_16x16x32_bf16 v[140:143], a[56:59], v[144:147], v[140:143]\n"
      "s_waitcnt lgkmcnt(0)\n"
      "v_mul_f32 v190, v80, v190\n"
      "v_fmac_f32_e64 v151, v176, v176\n"
      "v_mfma_f32_16x16x32_bf16 v[140:143], a[60:63], v[106:109], v[140:143]\n"
      "buffer_store_dwordx4 v[206:209], v20, s[24:27], s46 offen sc1\n"
      "v_mul_f32 v200, v77, v200\n"
      "v_mfma_f32_16x16x32_bf16 v[140:143], a[64:67], v[166:169], v[140:143]\n"
      "v_fmac_f32 v151, v177, v177\n"
      "v_fmac_f32 v151, v170, v170\n"
      "v_mfma_f32_16x16x32_bf16 v[140:143], a[68:71], v[180:183], v[140:143]\n"
      "v_fmac_f32 v151, v171, v171\n"
      "v_add_u32 v119, 115296, v119\n"
      "v_mfma_f32_16x16x32_bf16 v[202:205], a[72:75], v[120:123], 0\n"
      "s_waitcnt vmcnt(8)\n"
      "v_mul_f32 v98, v80, v98\n"
      "v_mul_f32 v201, v77, v201\n"
      "v_mfma_f32_16x16x32_bf16 v[202:205], a[76:79], v[114:117], v[202:205]\n"
      "v_cvt_pk_bf16_f32 v187, v212, v213\n"
      "v_mul_f32 v161, v80, v161\n"
      "v_mfma_f32_16x16x32_bf16 v[202:205], a[80:83], v[144:147], v[202:205]\n"
      "v_and_b32 v93, 2, v82\n"
      "v_mul_f32 v199, v77, v199\n"
      "v_mfma_f32_16x16x32_bf16 v[202:205], a[84:87], v[106:109], v[202:205]\n"
      "v_fmac_f32 v151, v172, v172\n"
      "v_mul_f32 v163, v77, v163\n"
      "v_mul_f32 v101, v80, v101\n"
      "v_mul_f32 v138, v77, v138\n"
      "v_mfma_f32_16x16x32_bf16 v[202:205], a[88:91], v[166:169], v[202:205]\n"
      "v_mul_f32 v193, v80, v193\n"
      "v_mul_f32 v99, v80, v99\n"
      "v_mfma_f32_16x16x32_bf16 v[202:205], a[92:95], v[180:183], v[202:205]\n"
      "v_mul_f32 v165, v77, v165\n"
      "v_mul_f32 v159, v80, v159\n"
      "v_readlane_b32 s47, v38, s43\n"
      "v_mfma_f32_16x16x32_bf16 v[210:213], a[96:99], v[120:123], 0\n"
      "v_mul_f32 v100, v80, v100\n"
      "v_cvt_pk_bf16_f32 v103, v104, v105\n"
      "v_mfma_f32_16x16x32_bf16 v[210:213], a[100:103], v[114:117], v[210:213]\n"
      "v_cvt_pk_bf16_f32 v198, v198, v199\n"
      "v_mul_f32 v192, v80, v192\n"
      "v_mfma_f32_16x16x32_bf16 v[210:213], a[104:107], v[144:147], v[210:213]\n"
      "v_fmac_f32 v151, v173, v173\n"
      "v_fma_f32 v104, v202, v202, s51\n"
      "v_fmac_f32 v104, v203, v203\n"
      "v_mfma_f32_16x16x32_bf16 v[210:213], a[108:111], v[106:109], v[210:213]\n"
      "v_mov_b32_e64 v189, 0\n"
      "v_cvt_pk_bf16_f32 v98, v98, v99\n"
      "v_mfma_f32_16x16x32_bf16 v[210:213], a[112:115], v[166:169], v[210:213]\n"
      "ds_read_u16 v119, v119\n"
      "v_cvt_pk_bf16_f32 v190, v190, v191\n"
      "v_mul_f32 v164, v77, v164\n"
      "v_fmac_f32 v104, v204, v204\n"
      "v_mfma_f32_16x16x32_bf16 v[210:213], a[116:119], v[180:183], v[210:213]\n"
      "v_cvt_pk_bf16_f32 v162, v162, v163\n"
      "v_cvt_pk_bf16_f32 v130, v154, v155\n"
      "v_mfma_f32_16x16x32_bf16 v[154:157], a[120:123], v[120:123], 0\n"
      "v_fmac_f32 v104, v205, v205\n"
      "v_cvt_pk_bf16_f32 v163, v164, v165\n"
      "v_mfma_f32_16x16x32_bf16 v[154:157], a[124:127], v[114:117], v[154:157]\n"
      "v_fmac_f32 v151, v140, v140\n"
      "v_mul_f32 v158, v80, v158\n"
      "v_mfma_f32_16x16x32_bf16 v[154:157], a[128:131], v[144:147], v[154:157]\n"
      "v_cvt_pk_bf16_f32 v191, v192, v193\n"
      "v_fmac_f32 v104, v210, v210\n"
      "v_mfma_f32_16x16x32_bf16 v[154:157], a[132:135], v[106:109], v[154:157]\n"
      "v_fmac_f32 v151, v141, v141\n"
      "v_cvt_pk_bf16_f32 v192, v158, v159\n"
      "v_mfma_f32_16x16x32_bf16 v[154:157], a[136:139], v[166:169], v[154:157]\n"
      "v_fmac_f32 v104, v211, v211\n"
      "v_fmac_f32_e64 v151, v142, v142\n"
      "v_mfma_f32_16x16x32_bf16 v[154:157], a[140:143], v[180:183], v[154:157]\n"
      "v_and_b32 v82, -4, v82\n"
      "v_cvt_pk_bf16_f32 v199, v200, v201\n"
      "v_mfma_f32_16x16x32_bf16 v[206:209], v[120:123], a[168:171], 0\n"
      "v_fmac_f32 v151, v143, v143\n"
      "v_fmac_f32 v104, v212, v212\n"
      "v_mfma_f32_16x16x32_bf16 v[206:209], v[114:117], a[172:175], v[206:209]\n"
      "s_waitcnt lgkmcnt(0)\n"
      "v_mul_f32 v137, v77, v137\n"
      "v_lshlrev_b32 v119, 16, v119\n"
      "v_fmac_f32 v104, v213, v213\n"
      "v_fmac_f32 v104, v154, v154\n"
      "v_fmac_f32 v104, v155, v155\n"
      "v_mfma_f32_16x16x32_bf16 v[206:209], v[144:147], a[176:179], v[206:209]\n"
      "v_fmac_f32 v104, v156, v156\n"
      "v_fmac_f32_e64 v104, v157, v157\n"
      "v_mfma_f32_16x16x32_bf16 v[220:223], v[120:123], a[144:147], 0\n"
      "v_cvt_pk_bf16_f32 v201, v138, v139\n"
      "v_cvt_pk_bf16_f32 v200, v136, v137\n"
      "v_lshlrev_b32 v78, 4, v78\n"
      "v_mfma_f32_16x16x32_bf16 v[220:223], v[114:117], a[148:151], v[220:223]\n"
      "v_permlane16_swap_b32 v151, v104\n"
      "v_mfma_f32_16x16x32_bf16 v[220:223], v[144:147], a[152:155], v[220:223]\n"
      "v_add_f32 v151, v151, v104\n"
      "v_cvt_pk_bf16_f32 v99, v100, v101\n"
      "v_mfma_f32_16x16x32_bf16 v[220:223], v[106:109], a[156:159], v[220:223]\n"
      "v_mov_b32 v104, v151\n"
      "v_cvt_pk_bf16_f32 v193, v160, v161\n"
      "v_mfma_f32_16x16x32_bf16 v[220:223], v[166:169], a[160:163], v[220:223]\n"
      "v_permlane32_swap_b32 v151, v104\n"
      "v_add_f32 v151, v151, v104\n"
      "v_mfma_f32_16x16x32_bf16 v[220:223], v[180:183], a[164:167], v[220:223]\n"
      "v_rsq_f32 v151, v151\n"
      "v_mfma_f32_16x16x32_bf16 v[206:209], v[106:109], a[180:183], v[206:209]\n"
      "ds_read_b128 v[110:113], v7 offset:22528\n"
      "v_mov_b32 v104, v151\n"
      "v_mfma_f32_16x16x32_bf16 v[206:209], v[166:169], a[184:187], v[206:209]\n"
      "s_nop 0\n"
      "v_permlane16_swap_b32 v151, v104\n"
      "v_mfma_f32_16x16x32_bf16 v[206:209], v[180:183], a[188:191], v[206:209]\n"
      "ds_read_b128 v[136:139], v2 offset:18432\n"
      "v_mul_f32_e64 v151, s50, v151\n"
      "v_mfma_f32_16x16x32_bf16 v[158:161], v[120:123], a[192:195], 0\n"
      "v_mul_f32 v175, v151, v175\n"
      "v_mul_f32 v205, v104, v205\n"
      "v_mfma_f32_16x16x32_bf16 v[158:161], v[114:117], a[196:199], v[158:161]\n"
      "ds_read_b128 v[120:123], v2 offset:21504\n"
      "v_mul_f32 v210, v104, v210\n"
      "v_cvt_pk_bf16_f32 v80, v206, v207\n"
      "v_mfma_f32_16x16x32_bf16 v[158:161], v[144:147], a[200:203], v[158:161]\n"
      "v_mul_f32 v204, v104, v204\n"
      "v_mul_f32 v170, v151, v170\n"
      "v_mul_f32 v202, v104, v202\n"
      "v_mul_f32 v172, v151, v172\n"
      "v_mfma_f32_16x16x32_bf16 v[158:161], v[106:109], a[204:207], v[158:161]\n"
      "ds_read_b128 v[144:147], v2 offset:24576\n"
      "v_mul_f32 v203, v104, v203\n"
      "v_mul_f32_e64 v174, v151, v174\n"
      "v_mfma_f32_16x16x32_bf16 v[158:161], v[166:169], a[208:211], v[158:161]\n"
      "ds_read_b128 v[166:169], v2 offset:27648\n"
      "v_cvt_pk_bf16_f32 v77, v222, v223\n"
      "v_mfma_f32_16x16x32_bf16 v[158:161], v[180:183], a[212:215], v[158:161]\n"
      "s_waitcnt lgkmcnt(3)\n"
      "v_mul_f32 v213, v104, v213\n"
      "v_cvt_pk_bf16_f32 v202, v202, v203\n"
      "v_mul_f32 v156, v104, v156\n"
      "v_mul_f32 v177, v151, v177\n"
      "v_mul_f32_e64 v155, v104, v155\n"
      "v_cvt_pk_bf16_f32 v203, v204, v205\n"
      "v_mfma_f32_16x16x32_bf16 v[114:117], a[216:219], v[136:139], 0\n"
      "s_waitcnt lgkmcnt(2)\n"
      "v_cvt_pk_bf16_f32 v174, v174, v175\n"
      "v_mul_f32 v211, v104, v211\n"
      "v_mfma_f32_16x16x32_bf16 v[114:117], a[220:223], v[120:123], v[114:117]\n"
      "ds_read_b128 v[180:183], v2 offset:30720\n"
      "v_cvt_pk_bf16_f32 v84, v158, v159\n"
      "v_cvt_pk_bf16_f32 v76, v220, v221\n"
      "v_cvt_pk_bf16_f32 v81, v208, v209\n"
      "v_mul_f32 v173, v151, v173\n"
      "v_cvt_pk_bf16_f32 v85, v160, v161\n"
      "v_mul_f32 v171, v151, v171\n"
      "v_mfma_f32_16x16x32_bf16 v[206:209], a[240:243], v[136:139], 0\n"
      "s_waitcnt lgkmcnt(1)\n"
      "v_cvt_pk_bf16_f32 v204, v210, v211\n"
      "v_mul_f32 v140, v151, v140\n"
      "v_mul_f32 v157, v104, v157\n"
      "v_mul_f32 v154, v104, v154\n"
      "v_mul_f32 v176, v151, v176\n"
      "v_mul_f32 v143, v151, v143\n"
      "v_mfma_f32_16x16x32_bf16 v[206:209], a[244:247], v[120:123], v[206:209]\n"
      "ds_read_b128 v[106:109], v2 offset:33792\n"
      "v_mul_f32_e64 v141, v151, v141\n"
      "v_mfma_f32_16x16x32_bf16 v[206:209], a[248:251], v[144:147], v[206:209]\n"
      "buffer_store_dwordx4 v[110:113], v21, s[24:27], s46 offen sc1\n"
      "v_add_u32 v161, 110592, v82\n"
      "v_mul_f32_e64 v142, v151, v142\n"
      "v_mfma_f32_16x16x32_bf16 v[206:209], a[252:255], v[166:169], v[206:209]\n"
      "ds_read_b128 v[110:113], v7 offset:26624\n"
      "v_cmp_gt_u32 s[72:73], 64, v78\n"
      "v_mfma_f32_16x16x32_bf16 v[220:223], v[232:235], v[136:139], 0\n"
      "ds_read2_b32 v[100:101], v161 offset0:2 offset1:10\n"
      "v_cvt_pk_bf16_f32 v140, v140, v141\n"
      "v_mfma_f32_16x16x32_bf16 v[220:223], v[236:239], v[120:123], v[220:223]\n"
      "ds_read2_b32 v[150:151], v161 offset0:0 offset1:1\n"
      "v_mul_f32_e64 v212, v104, v212\n"
      "v_mfma_f32_16x16x32_bf16 v[220:223], v[240:243], v[144:147], v[220:223]\n"
      "ds_read2_b32 v[96:97], v161 offset0:16 offset1:17\n"
      "v_cvt_pk_bf16_f32 v154, v154, v155\n"
      "v_mfma_f32_16x16x32_bf16 v[114:117], a[224:227], v[144:147], v[114:117]\n"
      "s_waitcnt lgkmcnt(4)\n"
      "v_cvt_pk_bf16_f32 v175, v176, v177\n"
      "v_cvt_pk_bf16_f32 v155, v156, v157\n"
      "v_mfma_f32_16x16x32_bf16 v[114:117], a[228:231], v[166:169], v[114:117]\n"
      "ds_read2_b32 v[146:147], v161 offset0:8 offset1:9\n"
      "v_mov_b32 v188, 0x3fb9\n"
      "v_mfma_f32_16x16x32_bf16 v[114:117], a[232:235], v[180:183], v[114:117]\n"
      "ds_read2_b32 v[88:89], v161 offset0:24 offset1:18\n"
      "v_readlane_b32 s45, v37, s43\n"
      "v_mfma_f32_16x16x32_bf16 v[114:117], a[236:239], v[106:109], v[114:117]\n"
      "ds_read_b128 v[158:161], v10 offset:32\n"
      "v_cvt_pk_bf16_f32 v141, v142, v143\n"
      "v_mfma_f32_16x16x32_bf16 v[206:209], v[224:227], v[180:183], v[206:209]\n"
      "s_waitcnt lgkmcnt(4)\n"
      "v_cvt_pk_bf16_f32 v177, v172, v173\n"
      "v_lshlrev_b64 v[188:189], v78, v[188:189]\n"
      "v_cndmask_b32_e64 v189, 0, v189, s[72:73]\n"
      "v_mfma_f32_16x16x32_bf16 v[206:209], v[228:231], v[106:109], v[206:209]\n"
      "ds_read_b128 v[142:145], v7 offset:30720\n"
      "v_cvt_pk_bf16_f32 v176, v170, v171\n"
      "v_mfma_f32_16x16x32_bf16 v[220:223], v[244:247], v[166:169], v[220:223]\n"
      "buffer_store_dwordx4 v[110:113], v22, s[24:27], s46 offen sc1\n"
      "v_mfma_f32_16x16x32_bf16 v[220:223], v[248:251], v[180:183], v[220:223]\n"
      "v_alignbyte_b32 v150, v151, v150, v93\n"
      "v_cvt_pk_bf16_f32 v205, v212, v213\n"
      "v_mfma_f32_16x16x32_bf16 v[220:223], v[252:255], v[106:109], v[220:223]\n"
      "ds_read_b128 v[108:111], v2 offset:18688\n"
      "v_fma_f32 v112, v114, v114, s52\n"
      "v_mfma_f32_16x16x32_bf16 v[210:213], v[128:131], v[184:187], v[40:43]\n"
      "ds_read_b128 v[180:183], v7 offset:34816\n"
      "v_fmac_f32 v112, v115, v115\n"
      "v_mov_b32 v156, 0xbb2b\n"
      "v_mfma_f32_16x16x32_bf16 v[104:107], v[202:205], v[184:187], v[48:51]\n"
      "s_waitcnt lgkmcnt(2)\n"
      "v_mov_b32 v157, 0\n"
      "v_fmac_f32 v112, v116, v116\n"
      "v_mfma_f32_16x16x16_bf16 v[210:213], v[148:149], v[102:103], v[210:213]\n"
      "v_fmac_f32_e64 v112, v117, v117\n"
      "v_cndmask_b32_e64 v188, 0, v188, s[72:73]\n"
      "v_alignbyte_b32 v146, v147, v146, v93\n"
      "v_mfma_f32_16x16x32_bf16 v[120:123], v[198:201], v[184:187], v[44:47]\n"
      "ds_read_b64 v[218:219], v10 offset:24\n"
      "v_alignbyte_b32 v151, v100, v151, v93\n"
      "v_mfma_f32_16x16x16_bf16 v[104:107], v[154:155], v[102:103], v[104:107]\n"
      "v_cmp_gt_u32 s[72:73], 64, v78\n"
      "v_lshlrev_b64 v[156:157], v78, v[156:157]\n"
      "v_alignbyte_b32 v96, v97, v96, v93\n"
      "v_mfma_f32_16x16x32_bf16 v[136:139], v[158:161], v[184:187], v[24:27]\n"
      "buffer_store_dwordx4 v[142:145], v23, s[24:27], s46 offen sc1\n"
      "v_mfma_f32_16x16x16_bf16 v[120:123], v[162:163], v[102:103], v[120:123]\n"
      "v_fmac_f32 v112, v206, v206\n"
      "v_fmac_f32 v112, v207, v207\n"
      "v_fmac_f32_e64 v112, v208, v208\n"
      "v_mfma_f32_16x16x16_bf16 v[210:213], v[188:189], v[150:151], v[210:213]\n"
      "v_alignbyte_b32 v97, v89, v97, v93\n"
      "v_alignbyte_b32 v147, v101, v147, v93\n"
      "v_cndmask_b32_e64 v156, 0, v156, s[72:73]\n"
      "v_mfma_f32_16x16x16_bf16 v[104:107], v[188:189], v[96:97], v[104:107]\n"
      "v_fmac_f32_e64 v112, v209, v209\n"
      "v_fmac_f32_e64 v112, v220, v220\n"
      "v_fmac_f32 v112, v221, v221\n"
      "v_mfma_f32_16x16x16_bf16 v[120:123], v[188:189], v[146:147], v[120:123]\n"
      "v_cndmask_b32_e64 v157, 0, v157, s[72:73]\n"
      "v_alignbyte_b32 v88, v88, v88, v93\n"
      "s_waitcnt lgkmcnt(0)\n"
      "v_mfma_f32_16x16x16_bf16 v[136:139], v[218:219], v[102:103], v[136:139]\n"
      "ds_read_b128 v[142:145], v2 offset:24832\n"
      "v_fmac_f32_e64 v112, v222, v222\n"
      "v_mfma_f32_16x16x16_bf16 v[136:139], v[188:189], v[88:89], v[136:139]\n"
      "ds_read_b128 v[184:187], v2 offset:21760\n"
      "s_waitcnt vmcnt(5)\n"
      "v_or3_b32 v124, v124, v125, v126\n"
      "v_mfma_f32_16x16x16_bf16 v[210:213], v[156:157], v[150:151], v[210:213]\n"
      "v_add_u32 v12, 32, v12\n"
      "v_fmac_f32_e64 v112, v223, v223\n"
      "v_add_u32 v173, 112160, v82\n"
      "v_mfma_f32_16x16x16_bf16 v[120:123], v[156:157], v[146:147], v[120:123]\n"
      "buffer_store_dwordx4 v[180:183], v25, s[24:27], s46 offen sc1\n"
      "v_mfma_f32_16x16x16_bf16 v[104:107], v[156:157], v[96:97], v[104:107]\n"
      "v_add_u32 v94, 113728, v82\n"
      "s_waitcnt vmcnt(5)\n"
      "v_or3_b32 v124, v124, v127, v86\n"
      "v_and_b32 v124, 0x7fff7fff, v124\n"
      "v_mfma_f32_16x16x16_bf16 v[136:139], v[156:157], v[88:89], v[136:139]\n"
      "ds_write_b32 v31, v124 offset:4608\n"
      "v_mfma_f32_16x16x32_bf16 v[180:183], a[216:219], v[108:111], 0\n"
      "s_waitcnt lgkmcnt(1)\n"
      "v_max3_f32 v82, v210, v211, v212\n"
      "v_max3_f32 v82, v82, v122, v123\n"
      "v_mfma_f32_16x16x32_bf16 v[180:183], a[220:223], v[184:187], v[180:183]\n"
      "ds_read2_b32 v[96:97], v173 offset0:0 offset1:1\n"
      "v_max3_f32 v169, v213, v120, v121\n"
      "v_mfma_f32_16x16x32_bf16 v[180:183], a[224:227], v[142:145], v[180:183]\n"
      "ds_read2_b32 v[88:89], v173 offset0:2 offset1:10\n"
      "v_readlane_b32 s46, v37, s42\n"
      "v_mfma_f32_16x16x32_bf16 v[124:127], a[240:243], v[108:111], 0\n"
      "ds_read_b128 v[100:103], v9 offset:5632\n"
      "v_max3_f32 v169, v169, v104, v105\n"
      "v_mfma_f32_16x16x32_bf16 v[124:127], a[244:247], v[184:187], v[124:127]\n"
      "ds_read2_b32 v[150:151], v173 offset0:16 offset1:17\n"
      "v_max3_f32 v82, v82, v106, v107\n"
      "v_mfma_f32_16x16x32_bf16 v[124:127], a[248:251], v[142:145], v[124:127]\n"
      "ds_read2_b32 v[170:171], v173 offset0:8 offset1:9\n"
      "v_max3_f32 v82, v82, v169, v136\n"
      "v_mfma_f32_16x16x32_bf16 v[166:169], v[232:235], v[108:111], 0\n"
      "ds_read2_b32 v[138:139], v173 offset0:24 offset1:18\n"
      "v_mov_b32_e64 v111, v82\n"
      "v_mfma_f32_16x16x32_bf16 v[166:169], v[236:239], v[184:187], v[166:169]\n"
      "s_nop 0\n"
      "v_permlane16_swap_b32 v82, v111\n"
      "v_mfma_f32_16x16x32_bf16 v[166:169], v[240:243], v[142:145], v[166:169]\n"
      "v_max_f32 v82, v82, v111\n"
      "v_mov_b32 v111, v82\n"
      "v_mfma_f32_16x16x32_bf16 v[184:187], v[128:131], v[190:193], v[52:55]\n"
      "s_waitcnt lgkmcnt(3)\n"
      "v_or3_b32 v100, v100, v101, v102\n"
      "v_permlane32_swap_b32 v82, v111\n"
      "v_or_b32 v100, v100, v103\n"
      "v_max_f32 v82, v82, v111\n"
      "v_sub_f32 v213, v213, v82\n"
      "v_sub_f32 v212, v212, v82\n"
      "v_sub_f32 v104, v104, v82\n"
      "v_alignbyte_b32 v96, v97, v96, v93\n"
      "v_alignbyte_b32 v97, v88, v97, v93\n"
      "v_sub_f32 v122, v122, v82\n"
      "v_sub_f32 v107, v107, v82\n"
      "v_mfma_f32_16x16x16_bf16 v[184:187], v[148:149], v[98:99], v[184:187]\n"
      "v_cmp_ne_u32 s[56:57], 0, v100\n"
      "v_sub_f32 v121, v121, v82\n"
      "v_sub_f32 v106, v106, v82\n"
      "v_sub_f32 v211, v211, v82\n"
      "v_mfma_f32_16x16x16_bf16 v[184:187], v[188:189], v[96:97], v[184:187]\n"
      "v_sub_f32 v120, v120, v82\n"
      "v_exp_f32 v107, v107\n"
      "v_sub_f32 v123, v123, v82\n"
      "v_mfma_f32_16x16x32_bf16 v[108:111], v[202:205], v[190:193], v[60:63]\n"
      "s_waitcnt lgkmcnt(0)\n"
      "v_alignbyte_b32 v170, v171, v170, v93\n"
      "v_alignbyte_b32 v171, v89, v171, v93\n"
      "v_mfma_f32_16x16x32_bf16 v[142:145], v[198:201], v[190:193], v[56:59]\n"
      "v_exp_f32 v106, v106\n"
      "v_sub_f32 v210, v210, v82\n"
      "v_mfma_f32_16x16x16_bf16 v[108:111], v[154:155], v[98:99], v[108:111]\n"
      "v_cvt_pk_bf16_f32 v89, v106, v107\n"
      "v_exp_f32_e64 v104, v104\n"
      "v_mfma_f32_16x16x32_bf16 v[100:103], v[158:161], v[190:193], v[26:29]\n"
      "ds_read_b128 v[190:193], v2 offset:27904\n"
      "v_alignbyte_b32 v150, v151, v150, v93\n"
      "v_mfma_f32_16x16x16_bf16 v[142:145], v[162:163], v[98:99], v[142:145]\n"
      "v_alignbyte_b32 v138, v138, v138, v93\n"
      "v_exp_f32 v211, v211\n"
      "v_mfma_f32_16x16x16_bf16 v[142:145], v[188:189], v[170:171], v[142:145]\n"
      "v_alignbyte_b32 v151, v139, v151, v93\n"
      "v_exp_f32 v210, v210\n"
      "v_mfma_f32_16x16x16_bf16 v[142:145], v[156:157], v[170:171], v[142:145]\n"
      "v_exp_f32 v213, v213\n"
      "v_cvt_pk_bf16_f32 v210, v210, v211\n"
      "v_mfma_f32_16x16x16_bf16 v[100:103], v[218:219], v[98:99], v[100:103]\n"
      "v_exp_f32 v212, v212\n"
      "v_exp_f32_e64 v123, v123\n"
      "v_mfma_f32_16x16x16_bf16 v[108:111], v[188:189], v[150:151], v[108:111]\n"
      "ds_read_b128 v[170:173], v2 offset:30976\n"
      "v_sub_f32 v105, v105, v82\n"
      "v_sub_f32 v136, v136, v82\n"
      "v_mfma_f32_16x16x16_bf16 v[108:111], v[156:157], v[150:151], v[108:111]\n"
      "v_exp_f32 v122, v122\n"
      "v_cvt_pk_bf16_f32 v211, v212, v213\n"
      "v_mfma_f32_16x16x16_bf16 v[184:187], v[156:157], v[96:97], v[184:187]\n"
      "ds_read_b128 v[96:99], v2 offset:34048\n"
      "v_exp_f32 v105, v105\n"
      "v_mfma_f32_16x16x16_bf16 v[100:103], v[188:189], v[138:139], v[100:103]\n"
      "v_exp_f32_e64 v120, v120\n"
      "v_cvt_pk_bf16_f32 v213, v122, v123\n"
      "v_exp_f32_e64 v121, v121\n"
      "v_mfma_f32_16x16x16_bf16 v[100:103], v[156:157], v[138:139], v[100:103]\n"
      "v_cvt_pk_bf16_f32 v212, v120, v121\n"
      "v_cvt_pk_bf16_f32 v88, v104, v105\n"
      "v_max3_f32 v106, v184, v185, v186\n"
      "s_waitcnt lgkmcnt(2)\n"
      "v_mfma_f32_16x16x32_bf16 v[180:183], a[228:231], v[190:193], v[180:183]\n"
      "s_waitcnt lgkmcnt(1)\n"
      "v_max3_f32 v106, v106, v144, v145\n"
      "v_max3_f32 v82, v187, v142, v143\n"
      "v_exp_f32 v136, v136\n"
      "v_max3_f32 v82, v82, v108, v109\n"
      "v_max3_f32 v106, v106, v110, v111\n"
      "v_mfma_f32_16x16x32_bf16 v[180:183], a[232:235], v[170:173], v[180:183]\n"
      "s_waitcnt lgkmcnt(0)\n"
      "v_max3_f32 v106, v106, v82, v100\n"
      "v_cvt_pk_bf16_f32 v90, v136, 0\n"
      "v_mov_b32 v120, v106\n"
      "v_mfma_f32_16x16x32_bf16 v[180:183], a[236:239], v[96:99], v[180:183]\n"
      "s_nop 0\n"
      "v_permlane16_swap_b32 v106, v120\n"
      "v_mfma_f32_16x16x32_bf16 v[124:127], a[252:255], v[190:193], v[124:127]\n"
      "ds_read_u16 v78, v11\n"
      "v_max_f32 v106, v106, v120\n"
      "v_mfma_f32_16x16x32_bf16 v[124:127], v[224:227], v[170:173], v[124:127]\n"
      "ds_read_u16 v82, v11 offset:32\n"
      "v_mov_b32 v120, v106\n"
      "v_mfma_f32_16x16x32_bf16 v[124:127], v[228:231], v[96:99], v[124:127]\n"
      "s_nop 0\n"
      "v_permlane32_swap_b32_e64 v106, v120\n"
      "v_mfma_f32_16x16x32_bf16 v[166:169], v[244:247], v[190:193], v[166:169]\n"
      "ds_read_u16 v86, v11 offset:64\n"
      "v_max_f32_e64 v106, v106, v120\n"
      "v_mfma_f32_16x16x32_bf16 v[166:169], v[248:251], v[170:173], v[166:169]\n"
      "ds_read2_b32 v[146:147], v94 offset0:16 offset1:17\n"
      "v_sub_f32 v142, v142, v106\n"
      "v_sub_f32 v187, v187, v106\n"
      "v_sub_f32 v108, v108, v106\n"
      "v_sub_f32 v109, v109, v106\n"
      "v_mfma_f32_16x16x32_bf16 v[166:169], v[252:255], v[96:99], v[166:169]\n"
      "ds_read2_b32 v[120:121], v94 offset0:0 offset1:1\n"
      "v_sub_f32 v185, v185, v106\n"
      "v_sub_f32 v110, v110, v106\n"
      "v_sub_f32 v144, v144, v106\n"
      "v_sub_f32 v145, v145, v106\n"
      "v_exp_f32 v185, v185\n"
      "v_sub_f32 v143, v143, v106\n"
      "v_exp_f32 v109, v109\n"
      "v_exp_f32 v108, v108\n"
      "v_exp_f32_e64 v145, v145\n"
      "v_mfma_f32_16x16x32_bf16 v[190:193], v[32:35], v[210:213], 0\n"
      "ds_read2_b32 v[96:97], v94 offset0:2 offset1:10\n"
      "v_cvt_pk_bf16_f32 v92, v108, v109\n"
      "v_sub_f32 v184, v184, v106\n"
      "v_exp_f32 v144, v144\n"
      "v_mfma_f32_16x16x32_bf16 v[190:193], v[32:35], v[88:91], v[190:193]\n"
      "v_exp_f32 v184, v184\n"
      "v_exp_f32 v142, v142\n"
      "v_sub_f32 v100, v100, v106\n"
      "v_sub_f32 v111, v111, v106\n"
      "v_mfma_f32_16x16x32_bf16 v[150:153], v[194:197], v[210:213], 0\n"
      "s_waitcnt lgkmcnt(1)\n"
      "v_sub_f32 v186, v186, v106\n"
      "v_alignbyte_b32 v120, v121, v120, v93\n"
      "v_alignbyte_b32 v146, v147, v146, v93\n"
      "v_mfma_f32_16x16x32_bf16 v[150:153], v[76:79], v[88:91], v[150:153]\n"
      "ds_read2_b32 v[192:193], v94 offset0:8 offset1:9\n"
      "v_cvt_pk_bf16_f32 v184, v184, v185\n"
      "v_mfma_f32_16x16x32_bf16 v[106:109], v[214:217], v[210:213], 0\n"
      "s_waitcnt lgkmcnt(1)\n"
      "v_exp_f32 v187, v187\n"
      "v_mfma_f32_16x16x32_bf16 v[106:109], v[84:87], v[88:91], v[106:109]\n"
      "ds_read2_b32 v[164:165], v94 offset0:24 offset1:18\n"
      "v_alignbyte_b32 v121, v96, v121, v93\n"
      "v_mfma_f32_16x16x32_bf16 v[170:173], v[132:135], v[210:213], 0\n"
      "v_exp_f32 v143, v143\n"
      "v_exp_f32 v186, v186\n"
      "v_mfma_f32_16x16x32_bf16 v[170:173], v[80:83], v[88:91], v[170:173]\n"
      "s_or_b32 s95, s45, s46\n"
      "v_cvt_pk_bf16_f32 v185, v186, v187\n"
      "v_cvt_pk_bf16_f32 v186, v142, v143\n"
      "v_rcp_f32 v190, v190\n"
      "v_mfma_f32_16x16x32_bf16 v[102:105], v[128:131], v[174:177], v[64:67]\n"
      "s_waitcnt lgkmcnt(1)\n"
      "v_alignbyte_b32 v192, v193, v192, v93\n"
      "v_cvt_pk_bf16_f32 v187, v144, v145\n"
      "v_mfma_f32_16x16x32_bf16 v[136:139], v[202:205], v[174:177], v[72:75]\n"
      "v_mul_f32 v150, v190, v150\n"
      "v_alignbyte_b32 v193, v97, v193, v93\n"
      "v_mul_f32 v151, v190, v151\n"
      "v_mfma_f32_16x16x32_bf16 v[210:213], v[158:161], v[174:177], v[28:31]\n"
      "s_waitcnt lgkmcnt(0)\n"
      "v_alignbyte_b32 v147, v165, v147, v93\n"
      "v_alignbyte_b32 v164, v164, v164, v93\n"
      "v_mfma_f32_16x16x32_bf16 v[96:99], v[198:201], v[174:177], v[68:71]\n"
      "v_mul_f32 v171, v190, v171\n"
      "v_mul_f32 v108, v190, v108\n"
      "v_mfma_f32_16x16x16_bf16 v[102:105], v[148:149], v[140:141], v[102:105]\n"
      "v_cvt_pk_bf16_f32 v150, v150, v151\n"
      "v_mul_f32 v153, v190, v153\n"
      "v_mul_f32 v152, v190, v152\n"
      "v_mfma_f32_16x16x16_bf16 v[96:99], v[162:163], v[140:141], v[96:99]\n"
      "v_mul_f32_e64 v173, v190, v173\n"
      "v_fma_f32 v113, v180, v180, s52\n"
      "v_fmac_f32 v113, v181, v181\n"
      "v_mfma_f32_16x16x16_bf16 v[136:139], v[154:155], v[140:141], v[136:139]\n"
      "v_mul_f32 v170, v190, v170\n"
      "v_cvt_pk_bf16_f32 v151, v152, v153\n"
      "v_cvt_pk_bf16_f32 v152, v170, v171\n"
      "v_mfma_f32_16x16x16_bf16 v[210:213], v[218:219], v[140:141], v[210:213]\n"
      "v_exp_f32 v110, v110\n"
      "v_mul_f32 v172, v190, v172\n"
      "v_mfma_f32_16x16x16_bf16 v[102:105], v[188:189], v[120:121], v[102:105]\n"
      "v_fmac_f32 v113, v182, v182\n"
      "v_cvt_pk_bf16_f32 v153, v172, v173\n"
      "v_mul_f32 v109, v190, v109\n"
      "v_mfma_f32_16x16x16_bf16 v[96:99], v[188:189], v[192:193], v[96:99]\n"
      "v_exp_f32 v111, v111\n"
      "v_fmac_f32 v113, v183, v183\n"
      "v_mfma_f32_16x16x16_bf16 v[102:105], v[156:157], v[120:121], v[102:105]\n"
      "v_exp_f32 v100, v100\n"
      "v_cvt_pk_bf16_f32 v93, v110, v111\n"
      "v_mfma_f32_16x16x16_bf16 v[136:139], v[188:189], v[146:147], v[136:139]\n"
      "v_fmac_f32 v113, v124, v124\n"
      "v_mul_f32 v107, v190, v107\n"
      "v_mul_f32 v106, v190, v106\n"
      "v_mfma_f32_16x16x16_bf16 v[210:213], v[188:189], v[164:165], v[210:213]\n"
      "ds_read_b128 v[120:123], v2 offset:25088\n"
      "ds_read_b128 v[172:175], v2 offset:18944\n"
      "v_mfma_f32_16x16x16_bf16 v[96:99], v[156:157], v[192:193], v[96:99]\n"
      "v_fmac_f32_e64 v113, v125, v125\n"
      "v_cvt_pk_bf16_f32 v94, v100, 0\n"
      "v_max3_f32 v171, v102, v103, v104\n"
      "v_mfma_f32_16x16x16_bf16 v[136:139], v[156:157], v[146:147], v[136:139]\n"
      "ds_read_b128 v[176:179], v2 offset:22016\n"
      "v_cvt_pk_bf16_f32 v106, v106, v107\n"
      "v_mfma_f32_16x16x16_bf16 v[210:213], v[156:157], v[164:165], v[210:213]\n"
      "v_cvt_pk_bf16_f32 v107, v108, v109\n"
      "v_max3_f32 v108, v105, v96, v97\n"
      "v_fmac_f32_e64 v113, v126, v126\n"
      "v_mfma_f32_16x16x32_bf16 v[188:191], v[32:35], v[184:187], 0\n"
      "ds_write_b128 v3, v[150:153]\n"
      "v_max3_f32 v171, v171, v98, v99\n"
      "v_max3_f32 v108, v108, v136, v137\n"
      "v_max3_f32 v171, v171, v138, v139\n"
      "v_max3_f32 v171, v171, v108, v210\n"
      "v_mov_b32 v147, v171\n"
      "v_mfma_f32_16x16x32_bf16 v[188:191], v[32:35], v[92:95], v[188:191]\n"
      "s_waitcnt lgkmcnt(2)\n"
      "v_fmac_f32 v113, v127, v127\n"
      "v_permlane16_swap_b32 v171, v147\n"
      "v_max_f32 v171, v171, v147\n"
      "v_mov_b32 v147, v171\n"
      "v_fmac_f32 v113, v166, v166\n"
      "v_mfma_f32_16x16x32_bf16 v[108:111], a[240:243], v[172:175], 0\n"
      "s_waitcnt lgkmcnt(1)\n"
      "v_permlane32_swap_b32_e64 v171, v147\n"
      "v_mfma_f32_16x16x32_bf16 v[108:111], a[244:247], v[176:179], v[108:111]\n"
      "ds_write_b64 v4, v[106:107]\n"
      "v_mfma_f32_16x16x32_bf16 v[108:111], a[248:251], v[120:123], v[108:111]\n"
      "v_max_f32 v171, v171, v147\n"
      "v_sub_f32 v137, v137, v171\n"
      "v_sub_f32 v102, v102, v171\n"
      "v_sub_f32 v98, v98, v171\n"
      "v_exp_f32 v102, v102\n"
      "v_sub_f32 v136, v136, v171\n"
      "v_sub_f32 v96, v96, v171\n"
      "v_sub_f32 v104, v104, v171\n"
      "v_mfma_f32_16x16x32_bf16 v[144:147], a[216:219], v[172:175], 0\n"
      "s_or_b32 s95, s95, s48\n"
      "v_sub_f32 v103, v103, v171\n"
      "v_exp_f32 v137, v137\n"
      "v_mfma_f32_16x16x32_bf16 v[144:147], a[220:223], v[176:179], v[144:147]\n"
      "v_exp_f32 v98, v98\n"
      "v_sub_f32 v138, v138, v171\n"
      "v_sub_f32 v210, v210, v171\n"
      "v_mfma_f32_16x16x32_bf16 v[144:147], a[224:227], v[120:123], v[144:147]\n"
      "v_sub_f32 v139, v139, v171\n"
      "v_sub_f32 v105, v105, v171\n"
      "v_mfma_f32_16x16x32_bf16 v[140:143], v[232:235], v[172:175], 0\n"
      "v_exp_f32_e64 v104, v104\n"
      "v_mfma_f32_16x16x32_bf16 v[140:143], v[236:239], v[176:179], v[140:143]\n"
      "v_sub_f32 v99, v99, v171\n"
      "v_exp_f32 v136, v136\n"
      "v_exp_f32 v103, v103\n"
      "v_mfma_f32_16x16x32_bf16 v[140:143], v[240:243], v[120:123], v[140:143]\n"
      "s_cmp_ge_u32 s95, 0x40000000\n"
      "s_cselect_b32 s94, -1, -2\n"
      "v_cvt_pk_bf16_f32 v102, v102, v103\n"
      "v_mfma_f32_16x16x32_bf16 v[190:193], v[194:197], v[184:187], 0\n"
      "s_and_b32 s47, s47, -4\n"
      "v_exp_f32_e64 v105, v105\n"
      "v_mfma_f32_16x16x32_bf16 v[190:193], v[76:79], v[92:95], v[190:193]\n"
      "ds_read_b128 v[176:179], v10\n"
      "v_exp_f32 v96, v96\n"
      "v_sub_f32 v97, v97, v171\n"
      "v_cvt_pk_bf16_f32 v103, v104, v105\n"
      "v_exp_f32 v99, v99\n"
      "v_mfma_f32_16x16x32_bf16 v[170:173], v[214:217], v[184:187], 0\n"
      "ds_read_b64 v[212:213], v10 offset:16\n"
      "v_exp_f32 v97, v97\n"
      "v_exp_f32_e64 v138, v138\n"
      "v_mfma_f32_16x16x32_bf16 v[170:173], v[84:87], v[92:95], v[170:173]\n"
      "v_rcp_f32_e64 v188, v188\n"
      "v_mfma_f32_16x16x32_bf16 v[120:123], v[132:135], v[184:187], 0\n"
      "v_cvt_pk_bf16_f32 v105, v98, v99\n"
      "v_cvt_pk_bf16_f32 v104, v96, v97\n"
      "v_mfma_f32_16x16x32_bf16 v[120:123], v[80:83], v[92:95], v[120:123]\n"
      "s_waitcnt lgkmcnt(1)\n"
      "v_exp_f32 v139, v139\n"
      "v_mfma_f32_16x16x32_bf16 v[184:187], v[176:179], v[158:161], v[30:33]\n"
      "ds_read_b128 v[96:99], v2 offset:31232\n"
      "v_cvt_pk_bf16_f32 v89, v138, v139\n"
      "v_mfma_f32_16x16x32_bf16 v[158:161], v[176:179], v[128:131], v[30:33]\n"
      "ds_read_b128 v[150:153], v2 offset:28160\n"
      "v_mul_f32 v173, v188, v173\n"
      "v_mfma_f32_16x16x32_bf16 v[128:131], v[176:179], v[198:201], v[30:33]\n"
      "s_waitcnt lgkmcnt(2)\n"
      "v_cvt_pk_bf16_f32 v88, v136, v137\n"
      "v_mul_f32_e64 v192, v188, v192\n"
      "v_mfma_f32_16x16x32_bf16 v[198:201], v[176:179], v[202:205], v[30:33]\n"
      "ds_read_b128 v[202:205], v2 offset:34304\n"
      "v_mul_f32 v193, v188, v193\n"
      "v_mfma_f32_16x16x16_bf16 v[158:161], v[212:213], v[148:149], v[158:161]\n"
      "v_mul_f32 v170, v188, v170\n"
      "v_exp_f32 v210, v210\n"
      "v_mfma_f32_16x16x16_bf16 v[128:131], v[212:213], v[162:163], v[128:131]\n"
      "v_mul_f32 v191, v188, v191\n"
      "v_mul_f32 v190, v188, v190\n"
      "v_mul_f32 v123, v188, v123\n"
      "v_fmac_f32_e64 v113, v167, v167\n"
      "v_mfma_f32_16x16x16_bf16 v[198:201], v[212:213], v[154:155], v[198:201]\n"
      "v_mul_f32_e64 v171, v188, v171\n"
      "v_cvt_pk_bf16_f32 v90, v210, 0\n"
      "v_cvt_pk_bf16_f32 v170, v170, v171\n"
      "v_mfma_f32_16x16x16_bf16 v[184:187], v[212:213], v[218:219], v[184:187]\n"
      "v_fmac_f32_e64 v113, v168, v168\n"
      "v_cvt_pk_bf16_f32 v190, v190, v191\n"
      "v_cndmask_b32 v158, v158, v128, s[58:59]\n"
      "v_mfma_f32_16x16x32_bf16 v[136:139], v[32:35], v[102:105], 0\n"
      "s_waitcnt lgkmcnt(1)\n"
      "v_mul_f32 v122, v188, v122\n"
      "v_fmac_f32_e64 v113, v169, v169\n"
      "v_mfma_f32_16x16x32_bf16 v[144:147], a[228:231], v[150:153], v[144:147]\n"
      "v_cndmask_b32 v158, v158, v198, s[60:61]\n"
      "v_cndmask_b32 v158, v158, v184, s[62:63]\n"
      "v_mfma_f32_16x16x32_bf16 v[144:147], a[232:235], v[96:99], v[144:147]\n"
      "s_waitcnt lgkmcnt(0)\n"
      "v_mul_f32 v172, v188, v172\n"
      "v_fmac_f32 v158, s53, v119\n"
      "v_mfma_f32_16x16x32_bf16 v[144:147], a[236:239], v[202:205], v[144:147]\n"
      "s_cmp_gt_u32 s42, s44\n"
      "s_cselect_b32 s95, s94, s95\n"
      "v_max_f32_dpp v128, v158, v158 row_ror:8 row_mask:0xf bank_mask:0xf\n"
      "v_mul_f32 v121, v188, v121\n"
      "v_mfma_f32_16x16x32_bf16 v[108:111], a[252:255], v[150:153], v[108:111]\n"
      "v_cvt_pk_bf16_f32 v171, v172, v173\n"
      "v_mul_f32_e64 v120, v188, v120\n"
      "v_max_f32_dpp v128, v128, v128 row_ror:4 row_mask:0xf bank_mask:0xf\n"
      "v_mfma_f32_16x16x32_bf16 v[108:111], v[224:227], v[96:99], v[108:111]\n"
      "s_nop 0\n"
      "v_max_f32_dpp v128, v128, v128 row_ror:2 row_mask:0xf bank_mask:0xf\n"
      "v_cvt_pk_bf16_f32 v191, v192, v193\n"
      "v_mfma_f32_16x16x32_bf16 v[108:111], v[228:231], v[202:205], v[108:111]\n"
      "v_cvt_pk_bf16_f32 v192, v120, v121\n"
      "v_max_f32_dpp v128, v128, v128 row_ror:1 row_mask:0xf bank_mask:0xf\n"
      "v_mfma_f32_16x16x32_bf16 v[176:179], v[194:197], v[102:105], 0\n"
      "s_nop 0\n"
      "v_max_f32_dpp v128, v128, v128 row_bcast:15 row_mask:0xa bank_mask:0xf\n"
      "v_fma_f32 v198, v144, v144, s52\n"
      "v_mfma_f32_16x16x32_bf16 v[176:179], v[76:79], v[88:91], v[176:179]\n"
      "ds_write_b64 v4, v[170:171] offset:256\n"
      "v_fmac_f32 v198, v145, v145\n"
      "v_mfma_f32_16x16x32_bf16 v[136:139], v[32:35], v[88:91], v[136:139]\n"
      "v_max_f32_dpp v128, v128, v128 row_bcast:31 row_mask:0xc bank_mask:0xf\n"
      "v_fmac_f32 v198, v146, v146\n"
      "v_readlane_b32 s72, v128, 63\n"
      "v_mfma_f32_16x16x32_bf16 v[128:131], v[132:135], v[102:105], 0\n"
      "s_nop 0\n"
      "v_subrev_f32 v158, s72, v158\n"
      "v_permlane16_swap_b32 v112, v113\n"
      "v_mfma_f32_16x16x32_bf16 v[128:131], v[80:83], v[88:91], v[128:131]\n"
      "v_fmac_f32 v198, v147, v147\n"
      "v_exp_f32 v158, v158\n"
      "v_cvt_pk_bf16_f32 v193, v122, v123\n"
      "v_mfma_f32_16x16x32_bf16 v[140:143], v[244:247], v[150:153], v[140:143]\n"
      "ds_write_b128 v3, v[190:193] offset:256\n"
      "v_fmac_f32 v198, v108, v108\n"
      "v_rcp_f32 v136, v136\n"
      "v_cvt_pk_bf16_f32 v158, v158, v158\n"
      "v_mul_f32 v178, v136, v178\n"
      "v_fmac_f32 v198, v109, v109\n"
      "v_mul_f32 v130, v136, v130\n"
      "v_mul_f32_e64 v179, v136, v179\n"
      "v_mfma_f32_16x16x32_bf16 v[140:143], v[248:251], v[96:99], v[140:143]\n"
      "ds_write_b16 v13, v158\n"
      "v_mul_f32 v131, v136, v131\n"
      "v_mfma_f32_16x16x32_bf16 v[140:143], v[252:255], v[202:205], v[140:143]\n"
      "v_mul_f32 v176, v136, v176\n"
      "v_mul_f32 v177, v136, v177\n"
      "v_cvt_pk_bf16_f32 v176, v176, v177\n"
      "v_mfma_f32_16x16x32_bf16 v[188:191], v[214:217], v[102:105], 0\n"
      "ds_read_b128 v[184:187], v14 offset:64\n"
      "v_fmac_f32 v198, v110, v110\n"
      "v_cvt_pk_bf16_f32 v177, v178, v179\n"
      "v_fmac_f32 v198, v111, v111\n"
      "v_mfma_f32_16x16x32_bf16 v[188:191], v[84:87], v[88:91], v[188:191]\n"
      "ds_read_b128 v[210:213], v14\n"
      "v_mul_f32 v129, v136, v129\n"
      "v_fmac_f32 v198, v140, v140\n"
      "v_fmac_f32 v198, v141, v141\n"
      "v_fmac_f32 v198, v142, v142\n"
      "v_mul_f32 v128, v136, v128\n"
      "v_add_f32_e64 v112, v112, v113\n"
      "v_cvt_pk_bf16_f32 v179, v130, v131\n"
      "v_cvt_pk_bf16_f32 v178, v128, v129\n"
      "v_fmac_f32 v198, v143, v143\n"
      "v_mov_b32 v113, v198\n"
      "s_add_u32 s43, s43, 1\n"
      "ds_write_b128 v3, v[176:179] offset:512\n"
      "v_permlane16_swap_b32 v198, v113\n"
      "v_add_f32 v198, v198, v113\n"
      "v_mul_f32 v189, v136, v189\n"
      "v_mul_f32 v190, v136, v190\n"
      "v_permlane32_swap_b32 v112, v198\n"
      "v_add_f32 v112, v112, v198\n"
      "v_mul_f32 v188, v136, v188\n"
      "ds_write_b32 v31, v112\n"
      "v_mul_f32 v191, v136, v191\n"
      "v_cvt_pk_bf16_f32 v188, v188, v189\n"
      "v_cvt_pk_bf16_f32 v189, v190, v191\n"
      "s_add_u32 s42, s42, 1\n"
      "ds_write_b64 v4, v[188:189] offset:512\n"
      "s_waitcnt lgkmcnt(3)\n"
      "v_mfma_f32_16x16x32_bf16 v[188:191], v[210:213], v[32:35], 0\n"
      "v_mfma_f32_16x16x32_bf16 v[188:191], v[184:187], v[32:35], v[188:191]\n"
      "s_cmp_ge_u32 s95, 0x40000000\n"
      "s_cselect_b64 vcc, -1, s[56:57]\n"
      "s_waitcnt lgkmcnt(0)\n"
      "s_barrier\n"
      "s_cbranch_vccz L_sel1_%=\n"
      "s_branch L_disp1_%=\n"
      "L_tail1_%=:\n"
      "s_nop 4\n"
      "v_mfma_f32_16x16x32_bf16 v[202:205], v[210:213], v[194:197], 0\n"
      "ds_read_b128 v[196:199], v9\n"
      "v_subrev_u32 v96, 115456, v9\n"
      "v_and_b32 v195, 0x300, v96\n"
      "v_mfma_f32_16x16x32_bf16 v[202:205], v[184:187], v[76:79], v[202:205]\n"
      "v_add_u32_e64 v96, v96, v195\n"
      "v_add_u32 v96, 149504, v96\n"
      "v_mfma_f32_16x16x32_bf16 v[102:105], v[210:213], v[132:135], 0\n"
      "ds_read_b128 v[132:135], v96\n"
      "ds_read_b128 v[176:179], v96 offset:4096\n"
      "v_mfma_f32_16x16x32_bf16 v[102:105], v[184:187], v[80:83], v[102:105]\n"
      "ds_read_b128 v[136:139], v96 offset:2048\n"
      "ds_read_b128 v[148:151], v2 offset:3072\n"
      "v_mfma_f32_16x16x32_bf16 v[192:195], v[210:213], v[214:217], 0\n"
      "ds_read_b128 v[210:213], v96 offset:6144\n"
      "ds_read_b128 v[214:217], v96 offset:8192\n"
      "v_mfma_f32_16x16x32_bf16 v[192:195], v[184:187], v[84:87], v[192:195]\n"
      "s_waitcnt lgkmcnt(6)\n"
      "v_add_f32 v196, v196, v197\n"
      "v_cndmask_b32 v202, v202, v102, s[58:59]\n"
      "v_add_f32 v198, v198, v199\n"
      "v_mov_b32_dpp v188, v188 quad_perm:[0,0,0,0] row_mask:0xf bank_mask:0xf\n"
      "v_rcp_f32 v188, v188\n"
      "v_add_f32 v196, v196, v198\n"
      "v_rsq_f32 v196, v196\n"
      "v_cndmask_b32 v202, v202, v192, s[60:61]\n"
      "v_mul_f32 v202, v188, v202\n"
      "v_cvt_pk_bf16_f32 v202, v202, v202\n"
      "s_waitcnt lgkmcnt(4)\n"
      "v_mfma_f32_16x16x32_bf16 v[188:191], a[216:219], v[132:135], 0\n"
      "ds_write_b16 v12, v202\n"
      "v_mfma_f32_16x16x32_bf16 v[184:187], a[240:243], v[132:135], 0\n"
      "s_waitcnt lgkmcnt(4)\n"
      "v_mul_f32 v196, 0x415db3d7, v196\n"
      "v_mov_b32 v198, v196\n"
      "v_mfma_f32_16x16x32_bf16 v[184:187], a[244:247], v[136:139], v[184:187]\n"
      "s_nop 0\n"
      "v_permlane32_swap_b32 v196, v198\n"
      "v_mov_b32 v197, v196\n"
      "v_mul_f32 v111, v198, v111\n"
      "v_mul_f32 v147, v198, v147\n"
      "v_permlane16_swap_b32 v196, v197\n"
      "v_mul_f32 v223, v196, v223\n"
      "v_mul_f32 v209, v196, v209\n"
      "v_mfma_f32_16x16x32_bf16 v[184:187], a[248:251], v[176:179], v[184:187]\n"
      "s_waitcnt lgkmcnt(1)\n"
      "v_mul_f32 v221, v196, v221\n"
      "v_mul_f32 v125, v197, v125\n"
      "v_mul_f32 v127, v197, v127\n"
      "v_mfma_f32_16x16x32_bf16 v[184:187], a[252:255], v[210:213], v[184:187]\n"
      "s_mul_i32 s92, s40, 2048\n"
      "v_mul_f32 v124, v197, v124\n"
      "v_mul_f32 v145, v198, v145\n"
      "v_mul_f32 v207, v196, v207\n"
      "v_mul_f32 v181, v197, v181\n"
      "v_mul_f32 v183, v197, v183\n"
      "v_mul_f32 v114, v196, v114\n"
      "v_mfma_f32_16x16x32_bf16 v[184:187], v[224:227], v[214:217], v[184:187]\n"
      "ds_read_b128 v[202:205], v2\n"
      "v_mul_f32 v117, v196, v117\n"
      "v_mul_f32 v115, v196, v115\n"
      "v_mul_f32 v116, v196, v116\n"
      "v_mfma_f32_16x16x32_bf16 v[192:195], v[232:235], v[132:135], 0\n"
      "v_cvt_pk_bf16_f32 v114, v114, v115\n"
      "v_mul_f32 v182, v197, v182\n"
      "v_mul_f32_e64 v109, v198, v109\n"
      "v_mfma_f32_16x16x32_bf16 v[192:195], v[236:239], v[136:139], v[192:195]\n"
      "ds_read_b128 v[132:135], v2 offset:6144\n"
      "v_mul_f32 v180, v197, v180\n"
      "v_mul_f32 v141, v198, v141\n"
      "v_mul_f32 v166, v197, v166\n"
      "v_mul_f32 v140, v198, v140\n"
      "v_mul_f32 v208, v196, v208\n"
      "v_mul_f32 v144, v198, v144\n"
      "v_mul_f32 v220, v196, v220\n"
      "v_mfma_f32_16x16x32_bf16 v[192:195], v[240:243], v[176:179], v[192:195]\n"
      "ds_read_b128 v[102:105], v96 offset:10240\n"
      "v_mul_f32 v167, v197, v167\n"
      "v_cvt_pk_bf16_f32 v180, v180, v181\n"
      "v_cvt_pk_bf16_f32 v181, v182, v183\n"
      "v_cvt_pk_bf16_f32 v166, v166, v167\n"
      "v_mul_f32 v168, v197, v168\n"
      "v_mul_f32 v142, v198, v142\n"
      "v_cvt_pk_bf16_f32 v220, v220, v221\n"
      "v_mfma_f32_16x16x32_bf16 v[192:195], v[244:247], v[210:213], v[192:195]\n"
      "v_cvt_pk_bf16_f32 v182, v124, v125\n"
      "v_mul_f32 v108, v198, v108\n"
      "v_mul_f32 v126, v197, v126\n"
      "v_mul_f32 v169, v197, v169\n"
      "v_cvt_pk_bf16_f32 v144, v144, v145\n"
      "v_mul_f32 v146, v198, v146\n"
      "v_mul_f32_e64 v222, v196, v222\n"
      "v_cvt_pk_bf16_f32 v167, v168, v169\n"
      "v_cvt_pk_bf16_f32 v115, v116, v117\n"
      "v_mfma_f32_16x16x32_bf16 v[192:195], v[248:251], v[214:217], v[192:195]\n"
      "v_cvt_pk_bf16_f32 v140, v140, v141\n"
      "v_cvt_pk_bf16_f32 v117, v208, v209\n"
      "v_cvt_pk_bf16_f32 v221, v222, v223\n"
      "v_cvt_pk_bf16_f32 v145, v146, v147\n"
      "v_mul_f32 v143, v198, v143\n"
      "v_mul_f32 v206, v196, v206\n"
      "v_cvt_pk_bf16_f32 v116, v206, v207\n"
      "v_cvt_pk_bf16_f32 v146, v108, v109\n"
      "v_mul_f32 v110, v198, v110\n"
      "v_cvt_pk_bf16_f32 v141, v142, v143\n"
      "v_mfma_f32_16x16x32_bf16 v[188:191], a[220:223], v[136:139], v[188:191]\n"
      "ds_write_b64 v6, v[220:221] offset:18432\n"
      "v_mfma_f32_16x16x32_bf16 v[188:191], a[224:227], v[176:179], v[188:191]\n"
      "ds_write_b128 v5, v[114:117] offset:18432\n"
      "v_mfma_f32_16x16x32_bf16 v[188:191], a[228:231], v[210:213], v[188:191]\n"
      "ds_write_b64 v6, v[166:167] offset:24576\n"
      "v_mfma_f32_16x16x32_bf16 v[188:191], a[232:235], v[214:217], v[188:191]\n"
      "s_waitcnt lgkmcnt(3)\n"
      "v_cvt_pk_bf16_f32 v147, v110, v111\n"
      "v_cvt_pk_bf16_f32 v183, v126, v127\n"
      "v_mfma_f32_16x16x32_bf16 v[188:191], a[236:239], v[102:105], v[188:191]\n"
      "ds_write_b128 v5, v[144:147] offset:30720\n"
      "v_mfma_f32_16x16x32_bf16 v[184:187], v[228:231], v[102:105], v[184:187]\n"
      "ds_write_b64 v6, v[140:141] offset:30720\n"
      "v_mfma_f32_16x16x32_bf16 v[192:195], v[252:255], v[102:105], v[192:195]\n"
      "ds_write_b128 v5, v[180:183] offset:24576\n"
      "v_mfma_f32_16x16x32_bf16 v[206:209], a[240:243], v[202:205], 0\n"
      "ds_read_b128 v[180:183], v2 offset:9216\n"
      "v_mfma_f32_16x16x32_bf16 v[206:209], a[244:247], v[148:151], v[206:209]\n"
      "ds_read_b128 v[166:169], v2 offset:12288\n"
      "v_subrev_u32 v80, 73728, v6\n"
      "v_mfma_f32_16x16x32_bf16 v[206:209], a[248:251], v[132:135], v[206:209]\n"
      "ds_read_b128 v[124:127], v2 offset:15360\n"
      "v_mfma_f32_16x16x32_bf16 v[220:223], v[232:235], v[202:205], 0\n"
      "v_and_b32 v82, 0x3fffffff, v37\n"
      "v_mfma_f32_16x16x32_bf16 v[220:223], v[236:239], v[148:151], v[220:223]\n"
      "ds_read_b128 v[144:147], v2 offset:6400\n"
      "v_ashrrev_i32 v85, 31, v37\n"
      "v_mfma_f32_16x16x32_bf16 v[220:223], v[240:243], v[132:135], v[220:223]\n"
      "ds_read_b128 v[214:217], v2 offset:12800\n"
      "v_subrev_u32 v198, 115456, v9\n"
      "v_mfma_f32_16x16x32_bf16 v[114:117], a[216:219], v[202:205], 0\n"
      "ds_read_b128 v[140:143], v2 offset:256\n"
      "v_add_u32 v86, s92, v198\n"
      "v_mfma_f32_16x16x32_bf16 v[114:117], a[220:223], v[148:151], v[114:117]\n"
      "ds_read_b128 v[202:205], v2 offset:15872\n"
      "v_mfma_f32_16x16x32_bf16 v[114:117], a[224:227], v[132:135], v[114:117]\n"
      "s_waitcnt lgkmcnt(0)\n"
      "s_barrier\n"
      "s_waitcnt lgkmcnt(5)\n"
      "v_mfma_f32_16x16x32_bf16 v[114:117], a[228:231], v[180:183], v[114:117]\n"
      "ds_read_b128 v[108:111], v2 offset:3328\n"
      "v_add_u32 v82, 1179648, v82\n"
      "v_mfma_f32_16x16x32_bf16 v[114:117], a[232:235], v[166:169], v[114:117]\n"
      "s_waitcnt lgkmcnt(4)\n"
      "v_lshrrev_b32 v198, 4, v86\n"
      "v_add_u32 v84, 64, v198\n"
      "v_mfma_f32_16x16x32_bf16 v[114:117], a[236:239], v[124:127], v[114:117]\n"
      "v_and_b32 v85, 100270464, v85\n"
      "v_fma_f32 v81, v188, v188, s52\n"
      "v_mfma_f32_16x16x32_bf16 v[220:223], v[244:247], v[180:183], v[220:223]\n"
      "ds_read_b128 v[132:135], v2 offset:12544\n"
      "v_fmac_f32_e64 v81, v189, v189\n"
      "v_mfma_f32_16x16x32_bf16 v[220:223], v[248:251], v[166:169], v[220:223]\n"
      "ds_read_b128 v[210:213], v2 offset:9472\n"
      "v_mul_u32_u24 v77, 2731, v84\n"
      "v_mfma_f32_16x16x32_bf16 v[220:223], v[252:255], v[124:127], v[220:223]\n"
      "v_mfma_f32_16x16x32_bf16 v[206:209], a[252:255], v[180:183], v[206:209]\n"
      "s_waitcnt lgkmcnt(2)\n"
      "v_sub_u32 v82, v82, v85\n"
      "v_fma_f32 v78, v114, v114, s52\n"
      "v_mfma_f32_16x16x32_bf16 v[206:209], v[224:227], v[166:169], v[206:209]\n"
      "ds_read_b128 v[102:105], v2 offset:15616\n"
      "v_add_u32 v76, 0, v198\n"
      "v_mfma_f32_16x16x32_bf16 v[206:209], v[228:231], v[124:127], v[206:209]\n"
      "ds_read_b128 v[196:199], v7 offset:4096\n"
      "v_lshrrev_b32 v77, 16, v77\n"
      "v_mfma_f32_16x16x32_bf16 v[180:183], a[216:219], v[140:143], 0\n"
      "ds_read_b128 v[136:139], v2 offset:512\n"
      "v_fmac_f32 v81, v190, v190\n"
      "v_mfma_f32_16x16x32_bf16 v[180:183], a[220:223], v[108:111], v[180:183]\n"
      "ds_read_b128 v[148:151], v2 offset:3584\n"
      "v_fmac_f32 v81, v191, v191\n"
      "v_mfma_f32_16x16x32_bf16 v[180:183], a[224:227], v[144:147], v[180:183]\n"
      "s_waitcnt lgkmcnt(4)\n"
      "v_fmac_f32 v78, v115, v115\n"
      "v_mfma_f32_16x16x32_bf16 v[180:183], a[228:231], v[210:213], v[180:183]\n"
      "v_fmac_f32_e64 v78, v116, v116\n"
      "v_fmac_f32 v81, v184, v184\n"
      "v_mfma_f32_16x16x32_bf16 v[180:183], a[232:235], v[132:135], v[180:183]\n"
      "s_waitcnt lgkmcnt(3)\n"
      "v_mul_u32_u24 v98, 2731, v76\n"
      "v_bfe_i32 v85, v37, 30, 1\n"
      "v_mfma_f32_16x16x32_bf16 v[180:183], a[236:239], v[102:105], v[180:183]\n"
      "ds_read_b128 v[174:177], v96 offset:4352\n"
      "v_lshrrev_b32_e64 v98, 16, v98\n"
      "v_mfma_f32_16x16x32_bf16 v[166:169], v[232:235], v[140:143], 0\n"
      "ds_read_b128 v[152:155], v96 offset:6400\n"
      "v_mfma_f32_16x16x32_bf16 v[166:169], v[236:239], v[108:111], v[166:169]\n"
      "v_fmac_f32 v81, v185, v185\n"
      "v_fmac_f32 v81, v186, v186\n"
      "v_mfma_f32_16x16x32_bf16 v[166:169], v[240:243], v[144:147], v[166:169]\n"
      "ds_read_b128 v[120:123], v7\n"
      "v_mul_u32_u24_e64 v97, 24, v98\n"
      "v_mfma_f32_16x16x32_bf16 v[166:169], v[244:247], v[210:213], v[166:169]\n"
      "ds_read_b128 v[128:131], v2 offset:6656\n"
      "v_fma_f32 v99, v180, v180, s52\n"
      "v_mfma_f32_16x16x32_bf16 v[166:169], v[248:251], v[132:135], v[166:169]\n"
      "v_fmac_f32 v78, v117, v117\n"
      "v_fmac_f32 v78, v206, v206\n"
      "v_mfma_f32_16x16x32_bf16 v[166:169], v[252:255], v[102:105], v[166:169]\n"
      "v_mfma_f32_16x16x32_bf16 v[124:127], a[240:243], v[140:143], 0\n"
      "ds_read_b128 v[170:173], v96 offset:2304\n"
      "v_mul_u32_u24_e64 v106, 24, v77\n"
      "v_mfma_f32_16x16x32_bf16 v[124:127], a[244:247], v[108:111], v[124:127]\n"
      "ds_read_b128 v[160:163], v2 offset:9728\n"
      "v_fmac_f32 v78, v207, v207\n"
      "v_mfma_f32_16x16x32_bf16 v[124:127], a[248:251], v[144:147], v[124:127]\n"
      "v_fmac_f32 v99, v181, v181\n"
      "v_sub_u32_e64 v97, v76, v97\n"
      "v_mfma_f32_16x16x32_bf16 v[124:127], a[252:255], v[210:213], v[124:127]\n"
      "v_fmac_f32 v78, v208, v208\n"
      "v_mfma_f32_16x16x32_bf16 v[124:127], v[224:227], v[132:135], v[124:127]\n"
      "s_waitcnt lgkmcnt(2)\n"
      "v_fmac_f32 v78, v209, v209\n"
      "v_fmac_f32 v99, v182, v182\n"
      "v_mfma_f32_16x16x32_bf16 v[124:127], v[228:231], v[102:105], v[124:127]\n"
      "buffer_store_dwordx4 v[120:123], v20, s[24:27], s46 offen sc1\n"
      "v_mfma_f32_16x16x32_bf16 v[144:147], a[216:219], v[136:139], 0\n"
      "v_fmac_f32 v99, v183, v183\n"
      "v_sub_u32 v106, v84, v106\n"
      "v_mfma_f32_16x16x32_bf16 v[144:147], a[220:223], v[148:151], v[144:147]\n"
      "s_waitcnt lgkmcnt(0)\n"
      "v_fmac_f32 v81, v187, v187\n"
      "v_mfma_f32_16x16x32_bf16 v[144:147], a[224:227], v[128:131], v[144:147]\n"
      "ds_read_b128 v[132:135], v7 offset:8192\n"
      "v_fmac_f32_e64 v99, v124, v124\n"
      "v_mfma_f32_16x16x32_bf16 v[144:147], a[228:231], v[160:163], v[144:147]\n"
      "v_mfma_f32_16x16x32_bf16 v[144:147], a[232:235], v[214:217], v[144:147]\n"
      "ds_read_b128 v[210:213], v96 offset:8448\n"
      "v_subrev_u32 v107, 73728, v5\n"
      "v_mfma_f32_16x16x32_bf16 v[144:147], a[236:239], v[202:205], v[144:147]\n"
      "v_and_b32 v84, 7, v77\n"
      "v_fmac_f32 v81, v192, v192\n"
      "v_mfma_f32_16x16x32_bf16 v[140:143], v[232:235], v[136:139], 0\n"
      "v_lshl_add_u32 v77, v77, 2, 12\n"
      "v_and_b32 v85, 196224, v85\n"
      "v_mfma_f32_16x16x32_bf16 v[140:143], v[236:239], v[148:151], v[140:143]\n"
      "v_fmac_f32 v99, v125, v125\n"
      "v_fmac_f32 v78, v220, v220\n"
      "v_mfma_f32_16x16x32_bf16 v[140:143], v[240:243], v[128:131], v[140:143]\n"
      "buffer_store_dwordx4 v[196:199], v21, s[24:27], s46 offen sc1\n"
      "v_mfma_f32_16x16x32_bf16 v[140:143], v[244:247], v[160:163], v[140:143]\n"
      "v_fma_f32 v123, v144, v144, s52\n"
      "v_fmac_f32 v99, v126, v126\n"
      "v_mfma_f32_16x16x32_bf16 v[140:143], v[248:251], v[214:217], v[140:143]\n"
      "ds_read_b128 v[102:105], v96 offset:256\n"
      "v_fmac_f32 v123, v145, v145\n"
      "v_mfma_f32_16x16x32_bf16 v[140:143], v[252:255], v[202:205], v[140:143]\n"
      "v_xor_b32 v106, v106, v84\n"
      "v_fmac_f32 v99, v127, v127\n"
      "v_mfma_f32_16x16x32_bf16 v[108:111], a[240:243], v[136:139], 0\n"
      "v_fmac_f32 v78, v221, v221\n"
      "v_fmac_f32 v123, v146, v146\n"
      "v_mfma_f32_16x16x32_bf16 v[108:111], a[244:247], v[148:151], v[108:111]\n"
      "ds_read_b128 v[196:199], v96 offset:10496\n"
      "v_fmac_f32 v99, v166, v166\n"
      "v_mfma_f32_16x16x32_bf16 v[108:111], a[248:251], v[128:131], v[108:111]\n"
      "v_mfma_f32_16x16x32_bf16 v[108:111], a[252:255], v[160:163], v[108:111]\n"
      "v_fmac_f32 v78, v222, v222\n"
      "v_fmac_f32 v99, v167, v167\n"
      "v_mfma_f32_16x16x32_bf16 v[108:111], v[224:227], v[214:217], v[108:111]\n"
      "s_waitcnt lgkmcnt(1)\n"
      "v_fmac_f32 v99, v168, v168\n"
      "v_fmac_f32 v123, v147, v147\n"
      "v_mfma_f32_16x16x32_bf16 v[108:111], v[228:231], v[202:205], v[108:111]\n"
      "ds_read_b128 v[214:217], v7 offset:16384\n"
      "v_sub_u32 v82, v82, v85\n"
      "v_mfma_f32_16x16x32_bf16 v[202:205], a[240:243], v[102:105], 0\n"
      "v_and_b32 v76, 7, v98\n"
      "v_fmac_f32 v99, v169, v169\n"
      "v_mfma_f32_16x16x32_bf16 v[202:205], a[244:247], v[170:173], v[202:205]\n"
      "ds_read_b128 v[160:163], v7 offset:12288\n"
      "v_fmac_f32 v78, v223, v223\n"
      "v_mfma_f32_16x16x32_bf16 v[202:205], a[248:251], v[174:177], v[202:205]\n"
      "v_lshl_add_u32 v98, v98, 2, 12\n"
      "v_fmac_f32 v123, v108, v108\n"
      "v_mfma_f32_16x16x32_bf16 v[128:131], v[232:235], v[102:105], 0\n"
      "v_fmac_f32 v123, v109, v109\n"
      "v_fmac_f32 v123, v110, v110\n"
      "v_mfma_f32_16x16x32_bf16 v[148:151], a[216:219], v[102:105], 0\n"
      "v_permlane16_swap_b32 v78, v99\n"
      "v_mfma_f32_16x16x32_bf16 v[148:151], a[220:223], v[170:173], v[148:151]\n"
      "buffer_store_dwordx4 v[132:135], v22, s[24:27], s46 offen sc1\n"
      "v_mfma_f32_16x16x32_bf16 v[148:151], a[224:227], v[174:177], v[148:151]\n"
      "v_fmac_f32 v123, v111, v111\n"
      "v_fmac_f32 v123, v140, v140\n"
      "v_mfma_f32_16x16x32_bf16 v[148:151], a[228:231], v[152:155], v[148:151]\n"
      "ds_bpermute_b32 v85, v77, v82\n"
      "v_fmac_f32 v81, v193, v193\n"
      "v_mfma_f32_16x16x32_bf16 v[148:151], a[232:235], v[210:213], v[148:151]\n"
      "s_waitcnt lgkmcnt(1)\n"
      "v_fmac_f32 v123, v141, v141\n"
      "v_fmac_f32 v123, v142, v142\n"
      "v_mfma_f32_16x16x32_bf16 v[148:151], a[236:239], v[196:199], v[148:151]\n"
      "ds_bpermute_b32 v77, v98, v82\n"
      "v_add_f32_e64 v78, v78, v99\n"
      "v_mfma_f32_16x16x32_bf16 v[128:131], v[236:239], v[170:173], v[128:131]\n"
      "v_fmac_f32 v123, v143, v143\n"
      "v_mov_b32 v99, v123\n"
      "v_mfma_f32_16x16x32_bf16 v[128:131], v[240:243], v[174:177], v[128:131]\n"
      "s_nop 0\n"
      "v_permlane16_swap_b32 v123, v99\n"
      "v_mfma_f32_16x16x32_bf16 v[202:205], a[252:255], v[152:155], v[202:205]\n"
      "v_fma_f32 v132, v148, v148, s52\n"
      "v_add_f32 v123, v123, v99\n"
      "v_mfma_f32_16x16x32_bf16 v[202:205], v[224:227], v[210:213], v[202:205]\n"
      "s_nop 0\n"
      "v_permlane32_swap_b32 v78, v123\n"
      "v_mfma_f32_16x16x32_bf16 v[202:205], v[228:231], v[196:199], v[202:205]\n"
      "v_fmac_f32 v132, v149, v149\n"
      "v_add_f32_e64 v78, v78, v123\n"
      "v_mfma_f32_16x16x32_bf16 v[128:131], v[244:247], v[152:155], v[128:131]\n"
      "ds_write_b32 v31, v78 offset:1024\n"
      "v_mfma_f32_16x16x32_bf16 v[128:131], v[248:251], v[210:213], v[128:131]\n"
      "buffer_store_dwordx4 v[160:163], v23, s[24:27], s46 offen sc1\n"
      "v_mfma_f32_16x16x32_bf16 v[128:131], v[252:255], v[196:199], v[128:131]\n"
      "s_waitcnt lgkmcnt(1)\n"
      "v_fmac_f32 v132, v150, v150\n"
      "v_fmac_f32 v132, v151, v151\n"
      "v_fmac_f32 v81, v194, v194\n"
      "v_fmac_f32 v132, v202, v202\n"
      "v_fmac_f32 v132, v203, v203\n"
      "v_fmac_f32 v81, v195, v195\n"
      "v_fmac_f32 v132, v204, v204\n"
      "v_fmac_f32 v132, v205, v205\n"
      "v_fmac_f32 v132, v128, v128\n"
      "v_fmac_f32 v132, v129, v129\n"
      "v_fmac_f32 v132, v130, v130\n"
      "v_fmac_f32 v132, v131, v131\n"
      "s_nop 1\n"
      "v_permlane16_swap_b32 v81, v132\n"
      "v_add_f32 v81, v81, v132\n"
      "v_mov_b32 v132, v81\n"
      "s_nop 1\n"
      "v_permlane32_swap_b32 v81, v132\n"
      "v_add_f32 v81, v81, v132\n"
      "ds_write_b32 v31, v81 offset:4608\n"
      "buffer_store_dwordx4 v[214:217], v25, s[24:27], s46 offen sc1\n"
      "v_xor_b32 v97, v97, v76\n"
      "v_lshl_add_u32 v85, v106, 4, v85\n"
      "v_readlane_b32 s46, v37, s42\n"
      "s_nop 0\n"
      "s_or_b32 s95, s46, s48\n"
      "s_add_u32 s42, s42, 1\n"
      "s_cmp_ge_u32 s95, 0x40000000\n"
      "s_cbranch_scc1 L_rare_27_%=\n"
      "L_back_28_%=:\n"
      "v_lshl_add_u32 v77, v97, 4, v77\n"
      "s_waitcnt lgkmcnt(0)\n"
      "s_barrier\n"
      "ds_read_b128 v[210:213], v7 offset:18432\n"
      "ds_read_b128 v[214:217], v9 offset:4608\n"
      "ds_read_b128 v[132:135], v7 offset:22528\n"
      "ds_read_b128 v[102:105], v9 offset:1024\n"
      "ds_read_b128 v[196:199], v7 offset:34816\n"
      "ds_read_b128 v[170:173], v7 offset:26624\n"
      "ds_read_b128 v[120:123], v7 offset:30720\n"
      "s_waitcnt lgkmcnt(6)\n"
      "buffer_store_dwordx4 v[210:213], v20, s[24:27], s46 offen sc1\n"
      "s_waitcnt lgkmcnt(4)\n"
      "buffer_store_dwordx4 v[132:135], v21, s[24:27], s46 offen sc1\n"
      "v_add_f32 v216, v216, v217\n"
      "v_add_f32 v214, v214, v215\n"
      "s_waitcnt lgkmcnt(2)\n"
      "v_add_f32 v102, v102, v103\n"
      "v_add_f32 v104, v104, v105\n"
      "v_add_f32 v102, v102, v104\n"
      "v_rsq_f32 v102, v102\n"
      "v_add_f32 v214, v214, v216\n"
      "v_rsq_f32 v214, v214\n"
      "v_mul_f32 v102, 0x415db3d7, v102\n"
      "v_mov_b32 v104, v102\n"
      "v_mul_f32 v214, 0x415db3d7, v214\n"
      "v_mov_b32 v216, v214\n"
      "v_permlane32_swap_b32 v102, v104\n"
      "v_mul_f32 v111, v104, v111\n"
      "v_mov_b32 v103, v102\n"
      "v_mul_f32 v142, v104, v142\n"
      "v_permlane32_swap_b32 v214, v216\n"
      "v_permlane16_swap_b32 v102, v103\n"
      "v_mul_f32 v144, v104, v144\n"
      "v_mul_f32 v207, v102, v207\n"
      "v_mov_b32 v215, v214\n"
      "v_mul_f32 v146, v104, v146\n"
      "v_mul_f32 v108, v104, v108\n"
      "v_mul_f32 v140, v104, v140\n"
      "v_mul_f32 v208, v102, v208\n"
      "v_mul_f32 v145, v104, v145\n"
      "v_permlane16_swap_b32 v214, v215\n"
      "v_mul_f32 v167, v103, v167\n"
      "v_mul_f32 v125, v103, v125\n"
      "v_mul_f32 v203, v215, v203\n"
      "v_mul_f32 v130, v215, v130\n"
      "v_mul_f32 v115, v102, v115\n"
      "v_mul_f32 v148, v215, v148\n"
      "v_mul_f32 v222, v102, v222\n"
      "v_mul_f32 v147, v104, v147\n"
      "v_mul_f32 v202, v215, v202\n"
      "v_mul_f32 v109, v104, v109\n"
      "v_mul_f32 v180, v103, v180\n"
      "v_mul_f32 v186, v214, v186\n"
      "v_mul_f32 v114, v102, v114\n"
      "v_mul_f32 v181, v103, v181\n"
      "v_mul_f32_e64 v205, v215, v205\n"
      "v_cvt_pk_bf16_f32 v144, v144, v145\n"
      "v_mul_f32 v221, v102, v221\n"
      "v_mul_f32 v128, v215, v128\n"
      "v_mul_f32 v188, v214, v188\n"
      "v_mul_f32 v183, v103, v183\n"
      "v_mul_f32 v166, v103, v166\n"
      "v_mul_f32 v110, v104, v110\n"
      "v_mul_f32 v209, v102, v209\n"
      "v_mul_f32 v206, v102, v206\n"
      "v_mul_f32 v141, v104, v141\n"
      "v_mul_f32 v204, v215, v204\n"
      "v_cvt_pk_bf16_f32 v114, v114, v115\n"
      "v_cvt_pk_bf16_f32 v145, v146, v147\n"
      "v_mul_f32 v124, v103, v124\n"
      "v_mul_f32 v129, v215, v129\n"
      "v_mul_f32 v220, v102, v220\n"
      "v_cvt_pk_bf16_f32 v147, v110, v111\n"
      "v_mul_f32 v116, v102, v116\n"
      "v_mul_f32 v190, v214, v190\n"
      "v_mul_f32 v189, v214, v189\n"
      "v_mul_f32 v127, v103, v127\n"
      "v_cvt_pk_bf16_f32 v220, v220, v221\n"
      "v_mul_f32 v182, v103, v182\n"
      "v_mul_f32 v168, v103, v168\n"
      "v_mul_f32 v191, v214, v191\n"
      "v_mul_f32 v223, v102, v223\n"
      "v_mul_f32 v194, v214, v194\n"
      "v_mul_f32 v187, v214, v187\n"
      "v_cvt_pk_bf16_f32 v188, v188, v189\n"
      "v_cvt_pk_bf16_f32 v189, v190, v191\n"
      "v_mul_f32 v143, v104, v143\n"
      "v_cvt_pk_bf16_f32 v166, v166, v167\n"
      "v_cvt_pk_bf16_f32 v191, v186, v187\n"
      "v_cvt_pk_bf16_f32 v180, v180, v181\n"
      "v_mul_f32 v126, v103, v126\n"
      "v_cvt_pk_bf16_f32 v128, v128, v129\n"
      "v_mul_f32 v184, v214, v184\n"
      "v_cvt_pk_bf16_f32 v181, v182, v183\n"
      "v_mul_f32 v117, v102, v117\n"
      "v_mul_f32 v149, v215, v149\n"
      "v_cvt_pk_bf16_f32 v115, v116, v117\n"
      "v_cvt_pk_bf16_f32 v140, v140, v141\n"
      "v_mul_f32 v150, v215, v150\n"
      "v_cvt_pk_bf16_f32 v117, v208, v209\n"
      "v_mul_f32 v131, v215, v131\n"
      "v_mul_f32_e64 v151, v215, v151\n"
      "v_cvt_pk_bf16_f32 v148, v148, v149\n"
      "v_cvt_pk_bf16_f32 v146, v108, v109\n"
      "v_mul_f32 v169, v103, v169\n"
      "v_mul_f32 v185, v214, v185\n"
      "v_mul_f32_e64 v195, v214, v195\n"
      "v_cvt_pk_bf16_f32 v149, v150, v151\n"
      "v_cvt_pk_bf16_f32 v116, v206, v207\n"
      "v_mul_f32_e64 v193, v214, v193\n"
      "v_cvt_pk_bf16_f32 v141, v142, v143\n"
      "v_cvt_pk_bf16_f32 v167, v168, v169\n"
      "v_mul_f32 v192, v214, v192\n"
      "v_cvt_pk_bf16_f32 v192, v192, v193\n"
      "v_cvt_pk_bf16_f32 v221, v222, v223\n"
      "v_cvt_pk_bf16_f32 v190, v184, v185\n"
      "v_cvt_pk_bf16_f32 v129, v130, v131\n"
      "v_cvt_pk_bf16_f32 v193, v194, v195\n"
      "v_cvt_pk_bf16_f32 v150, v202, v203\n"
      "v_cvt_pk_bf16_f32 v151, v204, v205\n"
      "v_cvt_pk_bf16_f32 v183, v126, v127\n"
      "v_cvt_pk_bf16_f32 v182, v124, v125\n"
      "s_waitcnt lgkmcnt(0)\n"
      "buffer_store_dwordx4 v[170:173], v22, s[24:27], s46 offen sc1\n"
      "ds_write_b128 v5, v[144:147] offset:12288\n"
      "ds_write_b64 v6, v[166:167] offset:6144\n"
      "ds_write_b64 v80, v[128:129] offset:6144\n"
      "ds_write_b64 v6, v[140:141] offset:12288\n"
      "ds_write_b64 v80, v[192:193]\n"
      "ds_write_b64 v6, v[220:221]\n"
      "ds_write_b128 v5, v[180:183] offset:6144\n"
      "ds_write_b128 v107, v[188:191]\n"
      "ds_write_b128 v5, v[114:117]\n"
      "ds_write_b128 v107, v[148:151] offset:6144\n"
      "buffer_store_dwordx4 v[120:123], v23, s[24:27], s46 offen sc1\n"
      "buffer_store_dwordx4 v[196:199], v25, s[24:27], s46 offen sc1\n"
      "v_readlane_b32 s46, v37, s42\n"
      "s_add_u32 s42, s42, 1\n"
      "s_or_b32 s95, s46, s48\n"
      "s_cmp_ge_u32 s95, 0x40000000\n"
      "s_cbranch_scc1 L_rare_31_%=\n"
      "L_back_32_%=:\n"
      "s_waitcnt lgkmcnt(0)\n"
      "s_barrier\n"
      "ds_read_b128 v[114:117], v7\n"
      "ds_read_b128 v[188:191], v7 offset:4096\n"
      "ds_read_b128 v[180:183], v7 offset:8192\n"
      "ds_read_b128 v[194:197], v7 offset:12288\n"
      "ds_read_b128 v[144:147], v7 offset:16384\n"
      "ds_read_b128 v[220:223], v86\n"
      "ds_read_b128 v[124:127], v86 offset:1024\n"
      "s_waitcnt lgkmcnt(6)\n"
      "buffer_store_dwordx4 v[114:117], v20, s[24:27], s46 offen sc1\n"
      "s_waitcnt lgkmcnt(5)\n"
      "buffer_store_dwordx4 v[188:191], v21, s[24:27], s46 offen sc1\n"
      "s_waitcnt lgkmcnt(4)\n"
      "buffer_store_dwordx4 v[180:183], v22, s[24:27], s46 offen sc1\n"
      "s_waitcnt lgkmcnt(3)\n"
      "buffer_store_dwordx4 v[194:197], v23, s[24:27], s46 offen sc1\n"
      "s_waitcnt lgkmcnt(2)\n"
      "buffer_store_dwordx4 v[144:147], v25, s[24:27], s46 offen sc1\n"
      "s_waitcnt lgkmcnt(1)\n"
      "buffer_store_dwordx4 v[220:223], v77, s[24:27], 0 offen sc1\n"
      "s_waitcnt lgkmcnt(0)\n"
      "buffer_store_dwordx4 v[124:127], v85, s[24:27], 0 offen sc1\n"
      "s_endpgm\n"
      "L_tail0_%=:\n"
      "s_nop 4\n"
      "v_mfma_f32_16x16x32_bf16 v[202:205], v[210:213], v[194:197], 0\n"
      "ds_read_b128 v[196:199], v9 offset:1024\n"
      "v_subrev_u32 v139, 115456, v9\n"
      "v_mfma_f32_16x16x32_bf16 v[202:205], v[184:187], v[76:79], v[202:205]\n"
      "v_and_b32 v195, 0x300, v139\n"
      "v_add_u32 v139, v139, v195\n"
      "v_add_u32 v139, 149504, v139\n"
      "v_mfma_f32_16x16x32_bf16 v[102:105], v[210:213], v[132:135], 0\n"
      "ds_read_b128 v[170:173], v139\n"
      "ds_read_b128 v[134:137], v139 offset:2048\n"
      "v_mfma_f32_16x16x32_bf16 v[102:105], v[184:187], v[80:83], v[102:105]\n"
      "ds_read_b128 v[130:133], v139 offset:8192\n"
      "ds_read_b128 v[120:123], v139 offset:4096\n"
      "v_mfma_f32_16x16x32_bf16 v[192:195], v[210:213], v[214:217], 0\n"
      "ds_read_b128 v[212:215], v139 offset:6144\n"
      "ds_read_b128 v[152:155], v2 offset:24576\n"
      "v_mfma_f32_16x16x32_bf16 v[192:195], v[184:187], v[84:87], v[192:195]\n"
      "s_waitcnt lgkmcnt(6)\n"
      "v_add_f32 v198, v198, v199\n"
      "v_add_f32 v196, v196, v197\n"
      "v_add_f32 v196, v196, v198\n"
      "v_rsq_f32 v196, v196\n"
      "s_mul_i32 s92, s40, 2048\n"
      "v_mul_f32 v196, 0x415db3d7, v196\n"
      "v_mov_b32_e64 v198, v196\n"
      "s_waitcnt lgkmcnt(5)\n"
      "v_mfma_f32_16x16x32_bf16 v[184:187], a[216:219], v[170:173], 0\n"
      "v_permlane32_swap_b32 v196, v198\n"
      "v_mov_b32 v197, v196\n"
      "v_mfma_f32_16x16x32_bf16 v[160:163], v[232:235], v[170:173], 0\n"
      "s_waitcnt lgkmcnt(4)\n"
      "v_mul_f32 v111, v198, v111\n"
      "v_mul_f32 v144, v198, v144\n"
      "v_permlane16_swap_b32 v196, v197\n"
      "v_mul_f32 v147, v198, v147\n"
      "v_mul_f32 v221, v196, v221\n"
      "v_mul_f32 v114, v196, v114\n"
      "v_mfma_f32_16x16x32_bf16 v[160:163], v[236:239], v[134:137], v[160:163]\n"
      "s_waitcnt lgkmcnt(1)\n"
      "v_mul_f32 v167, v197, v167\n"
      "v_mul_f32 v126, v197, v126\n"
      "v_mfma_f32_16x16x32_bf16 v[160:163], v[240:243], v[120:123], v[160:163]\n"
      "v_mul_f32 v180, v197, v180\n"
      "v_mul_f32 v222, v196, v222\n"
      "v_mfma_f32_16x16x32_bf16 v[160:163], v[244:247], v[212:215], v[160:163]\n"
      "ds_read_b128 v[148:151], v2 offset:18432\n"
      "v_mul_f32 v206, v196, v206\n"
      "v_mul_f32 v140, v198, v140\n"
      "v_mul_f32 v146, v198, v146\n"
      "v_mul_f32 v145, v198, v145\n"
      "v_mfma_f32_16x16x32_bf16 v[160:163], v[248:251], v[130:133], v[160:163]\n"
      "ds_read_b128 v[174:177], v2 offset:27648\n"
      "v_cvt_pk_bf16_f32 v144, v144, v145\n"
      "v_mul_f32 v223, v196, v223\n"
      "v_mul_f32 v115, v196, v115\n"
      "v_mfma_f32_16x16x32_bf16 v[184:187], a[220:223], v[134:137], v[184:187]\n"
      "ds_read_b128 v[96:99], v139 offset:10240\n"
      "v_mul_f32 v124, v197, v124\n"
      "v_mul_f32 v110, v198, v110\n"
      "v_mul_f32 v181, v197, v181\n"
      "v_mul_f32 v220, v196, v220\n"
      "v_mul_f32 v141, v198, v141\n"
      "v_mul_f32 v207, v196, v207\n"
      "v_mul_f32 v117, v196, v117\n"
      "v_mul_f32 v108, v198, v108\n"
      "v_cvt_pk_bf16_f32 v114, v114, v115\n"
      "v_mul_f32 v168, v197, v168\n"
      "v_mul_f32 v182, v197, v182\n"
      "v_cvt_pk_bf16_f32 v220, v220, v221\n"
      "v_mul_f32 v116, v196, v116\n"
      "v_mul_f32 v169, v197, v169\n"
      "v_mul_f32 v142, v198, v142\n"
      "v_cvt_pk_bf16_f32 v221, v222, v223\n"
      "v_cvt_pk_bf16_f32 v180, v180, v181\n"
      "v_mul_f32 v127, v197, v127\n"
      "v_mfma_f32_16x16x32_bf16 v[184:187], a[224:227], v[120:123], v[184:187]\n"
      "ds_read_b128 v[104:107], v2 offset:21504\n"
      "v_mul_f32 v125, v197, v125\n"
      "v_mul_f32 v109, v198, v109\n"
      "v_cvt_pk_bf16_f32 v115, v116, v117\n"
      "v_mfma_f32_16x16x32_bf16 v[184:187], a[228:231], v[212:215], v[184:187]\n"
      "ds_write_b64 v6, v[220:221]\n"
      "v_mul_f32 v166, v197, v166\n"
      "v_mul_f32 v208, v196, v208\n"
      "v_mul_f32_e64 v183, v197, v183\n"
      "v_cvt_pk_bf16_f32 v116, v206, v207\n"
      "v_mfma_f32_16x16x32_bf16 v[184:187], a[232:235], v[130:133], v[184:187]\n"
      "s_waitcnt lgkmcnt(2)\n"
      "v_cvt_pk_bf16_f32 v145, v146, v147\n"
      "v_mul_f32 v209, v196, v209\n"
      "v_cvt_pk_bf16_f32 v181, v182, v183\n"
      "v_cvt_pk_bf16_f32 v117, v208, v209\n"
      "v_mfma_f32_16x16x32_bf16 v[184:187], a[236:239], v[96:99], v[184:187]\n"
      "v_cvt_pk_bf16_f32 v166, v166, v167\n"
      "v_cvt_pk_bf16_f32 v147, v110, v111\n"
      "v_mfma_f32_16x16x32_bf16 v[194:197], a[240:243], v[170:173], 0\n"
      "v_cvt_pk_bf16_f32 v146, v108, v109\n"
      "v_cvt_pk_bf16_f32 v182, v124, v125\n"
      "v_cvt_pk_bf16_f32 v167, v168, v169\n"
      "v_mul_f32 v143, v198, v143\n"
      "v_mfma_f32_16x16x32_bf16 v[194:197], a[244:247], v[134:137], v[194:197]\n"
      "ds_write_b128 v5, v[114:117]\n"
      "v_mfma_f32_16x16x32_bf16 v[194:197], a[248:251], v[120:123], v[194:197]\n"
      "s_waitcnt lgkmcnt(1)\n"
      "v_cvt_pk_bf16_f32 v183, v126, v127\n"
      "v_cvt_pk_bf16_f32 v140, v140, v141\n"
      "v_cvt_pk_bf16_f32 v141, v142, v143\n"
      "v_mfma_f32_16x16x32_bf16 v[194:197], a[252:255], v[212:215], v[194:197]\n"
      "ds_write_b128 v5, v[180:183] offset:6144\n"
      "v_mfma_f32_16x16x32_bf16 v[194:197], v[224:227], v[130:133], v[194:197]\n"
      "ds_write_b128 v5, v[144:147] offset:12288\n"
      "v_mfma_f32_16x16x32_bf16 v[194:197], v[228:231], v[96:99], v[194:197]\n"
      "ds_write_b64 v6, v[166:167] offset:6144\n"
      "v_mfma_f32_16x16x32_bf16 v[160:163], v[252:255], v[96:99], v[160:163]\n"
      "ds_write_b64 v6, v[140:141] offset:12288\n"
      "v_mfma_f32_16x16x32_bf16 v[220:223], v[232:235], v[148:151], 0\n"
      "ds_read_b128 v[120:123], v2 offset:22016\n"
      "v_and_b32 v86, 0x3fffffff, v37\n"
      "v_mfma_f32_16x16x32_bf16 v[220:223], v[236:239], v[104:107], v[220:223]\n"
      "ds_read_b128 v[140:143], v2 offset:30720\n"
      "v_mfma_f32_16x16x32_bf16 v[114:117], a[216:219], v[148:151], 0\n"
      "v_fma_f32 v193, v184, v184, s52\n"
      "v_subrev_u32 v167, 115456, v9\n"
      "v_mfma_f32_16x16x32_bf16 v[114:117], a[220:223], v[104:107], v[114:117]\n"
      "ds_read_b128 v[180:183], v2 offset:33792\n"
      "v_add_u32_e64 v198, s92, v167\n"
      "v_mfma_f32_16x16x32_bf16 v[114:117], a[224:227], v[152:155], v[114:117]\n"
      "ds_read_b128 v[144:147], v2 offset:27904\n"
      "v_cndmask_b32 v202, v202, v102, s[58:59]\n"
      "v_mfma_f32_16x16x32_bf16 v[114:117], a[228:231], v[174:177], v[114:117]\n"
      "ds_read_b128 v[108:111], v2 offset:24832\n"
      "v_mfma_f32_16x16x32_bf16 v[206:209], a[240:243], v[148:151], 0\n"
      "ds_read_b128 v[96:99], v2 offset:18688\n"
      "v_lshrrev_b32_e64 v167, 4, v198\n"
      "v_mfma_f32_16x16x32_bf16 v[206:209], a[244:247], v[104:107], v[206:209]\n"
      "ds_read_b128 v[102:105], v2 offset:21760\n"
      "v_cndmask_b32 v202, v202, v192, s[60:61]\n"
      "v_mfma_f32_16x16x32_bf16 v[206:209], a[248:251], v[152:155], v[206:209]\n"
      "ds_read_b128 v[170:173], v2 offset:18944\n"
      "v_fmac_f32_e64 v193, v185, v185\n"
      "v_mfma_f32_16x16x32_bf16 v[206:209], a[252:255], v[174:177], v[206:209]\n"
      "s_waitcnt lgkmcnt(5)\n"
      "v_ashrrev_i32 v76, 31, v37\n"
      "v_mov_b32_dpp v188, v188 quad_perm:[0,0,0,0] row_mask:0xf bank_mask:0xf\n"
      "v_mfma_f32_16x16x32_bf16 v[206:209], v[224:227], v[140:143], v[206:209]\n"
      "ds_read_b128 v[148:151], v2 offset:30976\n"
      "v_fmac_f32_e64 v193, v186, v186\n"
      "v_mfma_f32_16x16x32_bf16 v[206:209], v[228:231], v[180:183], v[206:209]\n"
      "ds_read_b128 v[210:213], v2 offset:25088\n"
      "v_fmac_f32 v193, v187, v187\n"
      "v_mfma_f32_16x16x32_bf16 v[114:117], a[232:235], v[140:143], v[114:117]\n"
      "v_rcp_f32 v188, v188\n"
      "v_mfma_f32_16x16x32_bf16 v[114:117], a[236:239], v[180:183], v[114:117]\n"
      "v_add_u32 v86, 1179648, v86\n"
      "v_add_u32_e64 v204, 64, v167\n"
      "v_mfma_f32_16x16x32_bf16 v[220:223], v[240:243], v[152:155], v[220:223]\n"
      "ds_read_b128 v[130:133], v2 offset:34048\n"
      "v_mul_u32_u24 v78, 2731, v204\n"
      "v_mfma_f32_16x16x32_bf16 v[220:223], v[244:247], v[174:177], v[220:223]\n"
      "ds_read_b128 v[134:137], v2 offset:31232\n"
      "v_fmac_f32_e64 v193, v194, v194\n"
      "v_mfma_f32_16x16x32_bf16 v[220:223], v[248:251], v[140:143], v[220:223]\n"
      "v_fma_f32 v199, v114, v114, s52\n"
      "v_and_b32 v76, 100270464, v76\n"
      "v_mfma_f32_16x16x32_bf16 v[220:223], v[252:255], v[180:183], v[220:223]\n"
      "s_waitcnt lgkmcnt(2)\n"
      "v_fmac_f32 v199, v115, v115\n"
      "v_fmac_f32 v199, v116, v116\n"
      "v_mfma_f32_16x16x32_bf16 v[140:143], v[232:235], v[170:173], 0\n"
      "ds_read_b128 v[152:155], v7 offset:34816\n"
      "v_fmac_f32 v199, v117, v117\n"
      "v_mfma_f32_16x16x32_bf16 v[140:143], v[236:239], v[120:123], v[140:143]\n"
      "v_mul_f32 v202, v188, v202\n"
      "v_fmac_f32 v199, v206, v206\n"
      "v_mfma_f32_16x16x32_bf16 v[140:143], v[240:243], v[210:213], v[140:143]\n"
      "ds_read_b128 v[174:177], v7 offset:22528\n"
      "v_lshrrev_b32 v78, 16, v78\n"
      "v_mfma_f32_16x16x32_bf16 v[180:183], a[216:219], v[96:99], 0\n"
      "v_fmac_f32 v199, v207, v207\n"
      "v_mfma_f32_16x16x32_bf16 v[180:183], a[220:223], v[102:105], v[180:183]\n"
      "v_sub_u32 v86, v86, v76\n"
      "v_mul_u32_u24 v82, 24, v78\n"
      "v_mfma_f32_16x16x32_bf16 v[180:183], a[224:227], v[108:111], v[180:183]\n"
      "ds_read_b128 v[188:191], v2 offset:28160\n"
      "v_mfma_f32_16x16x32_bf16 v[180:183], a[228:231], v[144:147], v[180:183]\n"
      "v_fmac_f32 v199, v208, v208\n"
      "v_sub_u32 v82, v204, v82\n"
      "v_mfma_f32_16x16x32_bf16 v[180:183], a[232:235], v[148:151], v[180:183]\n"
      "s_waitcnt lgkmcnt(2)\n"
      "v_fmac_f32 v199, v209, v209\n"
      "v_fmac_f32 v193, v195, v195\n"
      "v_mfma_f32_16x16x32_bf16 v[180:183], a[236:239], v[130:133], v[180:183]\n"
      "v_and_b32 v204, 7, v78\n"
      "v_cvt_pk_bf16_f32 v202, v202, v202\n"
      "v_mfma_f32_16x16x32_bf16 v[124:127], a[240:243], v[96:99], 0\n"
      "ds_read_b128 v[214:217], v2 offset:34304\n"
      "v_add_u32_e64 v81, 0, v167\n"
      "v_mfma_f32_16x16x32_bf16 v[124:127], a[244:247], v[102:105], v[124:127]\n"
      "ds_write_b16 v12, v202 offset:16\n"
      "v_mfma_f32_16x16x32_bf16 v[124:127], a[248:251], v[108:111], v[124:127]\n"
      "v_mul_u32_u24 v84, 2731, v81\n"
      "v_mfma_f32_16x16x32_bf16 v[124:127], a[252:255], v[144:147], v[124:127]\n"
      "v_fmac_f32 v199, v220, v220\n"
      "v_lshrrev_b32 v84, 16, v84\n"
      "v_mfma_f32_16x16x32_bf16 v[124:127], v[224:227], v[148:151], v[124:127]\n"
      "v_add_u32 v12, 32, v12\n"
      "v_mul_u32_u24 v192, 24, v84\n"
      "v_mfma_f32_16x16x32_bf16 v[124:127], v[228:231], v[130:133], v[124:127]\n"
      "v_fma_f32 v85, v180, v180, s52\n"
      "v_fmac_f32 v199, v221, v221\n"
      "v_mfma_f32_16x16x32_bf16 v[166:169], v[232:235], v[96:99], 0\n"
      "s_waitcnt lgkmcnt(0)\n"
      "s_barrier\n"
      "v_fmac_f32 v193, v196, v196\n"
      "v_mfma_f32_16x16x32_bf16 v[166:169], v[236:239], v[102:105], v[166:169]\n"
      "ds_read_b128 v[96:99], v139 offset:8448\n"
      "v_sub_u32_e64 v192, v81, v192\n"
      "v_mfma_f32_16x16x32_bf16 v[166:169], v[240:243], v[108:111], v[166:169]\n"
      "v_fmac_f32 v85, v181, v181\n"
      "v_mfma_f32_16x16x32_bf16 v[166:169], v[244:247], v[144:147], v[166:169]\n"
      "ds_read_b128 v[108:111], v7 offset:18432\n"
      "v_fmac_f32 v85, v182, v182\n"
      "v_mfma_f32_16x16x32_bf16 v[166:169], v[248:251], v[148:151], v[166:169]\n"
      "ds_read_b128 v[104:107], v139 offset:256\n"
      "v_fmac_f32 v199, v222, v222\n"
      "v_mfma_f32_16x16x32_bf16 v[166:169], v[252:255], v[130:133], v[166:169]\n"
      "v_fmac_f32 v85, v183, v183\n"
      "v_fmac_f32 v85, v124, v124\n"
      "v_mfma_f32_16x16x32_bf16 v[144:147], a[216:219], v[170:173], 0\n"
      "ds_read_b128 v[128:131], v139 offset:4352\n"
      "v_fmac_f32 v85, v125, v125\n"
      "v_mfma_f32_16x16x32_bf16 v[144:147], a[220:223], v[120:123], v[144:147]\n"
      "v_fmac_f32 v85, v126, v126\n"
      "v_mfma_f32_16x16x32_bf16 v[144:147], a[224:227], v[210:213], v[144:147]\n"
      "s_waitcnt lgkmcnt(3)\n"
      "v_bfe_i32 v76, v37, 30, 1\n"
      "v_mfma_f32_16x16x32_bf16 v[144:147], a[228:231], v[188:191], v[144:147]\n"
      "s_waitcnt lgkmcnt(2)\n"
      "v_xor_b32 v82, v82, v204\n"
      "v_fmac_f32 v193, v197, v197\n"
      "v_mfma_f32_16x16x32_bf16 v[144:147], a[232:235], v[134:137], v[144:147]\n"
      "ds_read_b128 v[202:205], v139 offset:2304\n"
      "v_lshl_add_u32 v78, v78, 2, 12\n"
      "v_mfma_f32_16x16x32_bf16 v[144:147], a[236:239], v[214:217], v[144:147]\n"
      "buffer_store_dwordx4 v[108:111], v20, s[24:27], s46 offen sc1\n"
      "s_nop 1\n"
      "v_mfma_f32_16x16x32_bf16 v[108:111], a[240:243], v[170:173], 0\n"
      "ds_read_b128 v[148:151], v139 offset:6400\n"
      "v_and_b32 v76, 196224, v76\n"
      "v_mfma_f32_16x16x32_bf16 v[108:111], a[244:247], v[120:123], v[108:111]\n"
      "v_and_b32 v81, 7, v84\n"
      "v_fmac_f32 v193, v160, v160\n"
      "v_mfma_f32_16x16x32_bf16 v[108:111], a[248:251], v[210:213], v[108:111]\n"
      "ds_read_b128 v[210:213], v139 offset:10496\n"
      "v_fma_f32 v139, v144, v144, s52\n"
      "v_mfma_f32_16x16x32_bf16 v[108:111], a[252:255], v[188:191], v[108:111]\n"
      "v_fmac_f32 v85, v127, v127\n"
      "v_fmac_f32 v139, v145, v145\n"
      "v_mfma_f32_16x16x32_bf16 v[108:111], v[224:227], v[134:137], v[108:111]\n"
      "v_sub_u32 v86, v86, v76\n"
      "v_fmac_f32 v85, v166, v166\n"
      "v_mfma_f32_16x16x32_bf16 v[108:111], v[228:231], v[214:217], v[108:111]\n"
      "v_fmac_f32 v193, v161, v161\n"
      "v_fmac_f32 v85, v167, v167\n"
      "v_mfma_f32_16x16x32_bf16 v[140:143], v[244:247], v[188:191], v[140:143]\n"
      "v_fmac_f32 v139, v146, v146\n"
      "v_mfma_f32_16x16x32_bf16 v[140:143], v[248:251], v[134:137], v[140:143]\n"
      "s_waitcnt lgkmcnt(1)\n"
      "v_fmac_f32 v85, v168, v168\n"
      "v_fmac_f32_e64 v85, v169, v169\n"
      "v_mfma_f32_16x16x32_bf16 v[140:143], v[252:255], v[214:217], v[140:143]\n"
      "buffer_store_dwordx4 v[174:177], v21, s[24:27], s46 offen sc1\n"
      "s_nop 1\n"
      "v_mfma_f32_16x16x32_bf16 v[174:177], v[232:235], v[104:107], 0\n"
      "ds_read_b128 v[170:173], v7 offset:26624\n"
      "v_fmac_f32 v139, v147, v147\n"
      "v_mfma_f32_16x16x32_bf16 v[174:177], v[236:239], v[202:205], v[174:177]\n"
      "v_fmac_f32 v139, v108, v108\n"
      "v_fmac_f32 v199, v223, v223\n"
      "v_mfma_f32_16x16x32_bf16 v[174:177], v[240:243], v[128:131], v[174:177]\n"
      "v_fmac_f32 v139, v109, v109\n"
      "v_mfma_f32_16x16x32_bf16 v[134:137], a[216:219], v[104:107], 0\n"
      "v_permlane16_swap_b32_e64 v199, v85\n"
      "v_mfma_f32_16x16x32_bf16 v[134:137], a[220:223], v[202:205], v[134:137]\n"
      "ds_read_b128 v[120:123], v7 offset:30720\n"
      "v_lshl_add_u32 v84, v84, 2, 12\n"
      "v_mfma_f32_16x16x32_bf16 v[134:137], a[224:227], v[128:131], v[134:137]\n"
      "v_fmac_f32 v139, v110, v110\n"
      "v_fmac_f32 v139, v111, v111\n"
      "v_mfma_f32_16x16x32_bf16 v[134:137], a[228:231], v[148:151], v[134:137]\n"
      "s_waitcnt lgkmcnt(1)\n"
      "v_fmac_f32 v139, v140, v140\n"
      "v_add_f32_e64 v199, v199, v85\n"
      "v_mfma_f32_16x16x32_bf16 v[134:137], a[232:235], v[96:99], v[134:137]\n"
      "ds_bpermute_b32 v216, v84, v86\n"
      "v_fmac_f32_e64 v139, v141, v141\n"
      "v_mfma_f32_16x16x32_bf16 v[134:137], a[236:239], v[210:213], v[134:137]\n"
      "buffer_store_dwordx4 v[170:173], v22, s[24:27], s46 offen sc1\n"
      "s_nop 1\n"
      "v_mfma_f32_16x16x32_bf16 v[170:173], a[240:243], v[104:107], 0\n"
      "v_fmac_f32 v139, v142, v142\n"
      "v_fmac_f32_e64 v139, v143, v143\n"
      "v_mfma_f32_16x16x32_bf16 v[170:173], a[244:247], v[202:205], v[170:173]\n"
      "ds_bpermute_b32 v76, v78, v86\n"
      "v_mov_b32_e64 v85, v139\n"
      "v_mfma_f32_16x16x32_bf16 v[170:173], a[248:251], v[128:131], v[170:173]\n"
      "v_fma_f32 v131, v134, v134, s52\n"
      "v_fmac_f32 v193, v162, v162\n"
      "v_mfma_f32_16x16x32_bf16 v[170:173], a[252:255], v[148:151], v[170:173]\n"
      "v_permlane16_swap_b32 v139, v85\n"
      "v_mfma_f32_16x16x32_bf16 v[170:173], v[224:227], v[96:99], v[170:173]\n"
      "v_add_f32 v139, v139, v85\n"
      "v_fmac_f32 v131, v135, v135\n"
      "v_mfma_f32_16x16x32_bf16 v[170:173], v[228:231], v[210:213], v[170:173]\n"
      "v_fmac_f32 v131, v136, v136\n"
      "v_fmac_f32 v131, v137, v137\n"
      "v_mfma_f32_16x16x32_bf16 v[174:177], v[244:247], v[148:151], v[174:177]\n"
      "s_waitcnt lgkmcnt(1)\n"
      "v_permlane32_swap_b32 v199, v139\n"
      "v_mfma_f32_16x16x32_bf16 v[174:177], v[248:251], v[96:99], v[174:177]\n"
      "buffer_store_dwordx4 v[120:123], v23, s[24:27], s46 offen sc1\n"
      "v_mfma_f32_16x16x32_bf16 v[174:177], v[252:255], v[210:213], v[174:177]\n"
      "v_fmac_f32 v131, v170, v170\n"
      "v_fmac_f32 v131, v171, v171\n"
      "v_add_f32 v199, v199, v139\n"
      "v_fmac_f32 v131, v172, v172\n"
      "v_fmac_f32 v193, v163, v163\n"
      "v_fmac_f32 v131, v173, v173\n"
      "s_nop 1\n"
      "v_fmac_f32 v131, v174, v174\n"
      "v_fmac_f32 v131, v175, v175\n"
      "v_fmac_f32 v131, v176, v176\n"
      "v_fmac_f32 v131, v177, v177\n"
      "v_xor_b32 v192, v192, v81\n"
      "s_nop 0\n"
      "v_permlane16_swap_b32 v193, v131\n"
      "v_add_f32 v193, v193, v131\n"
      "v_mov_b32 v131, v193\n"
      "s_nop 1\n"
      "v_permlane32_swap_b32 v193, v131\n"
      "v_add_f32 v193, v193, v131\n"
      "ds_write_b32 v31, v199\n"
      "ds_write_b32 v31, v193 offset:4608\n"
      "buffer_store_dwordx4 v[152:155], v25, s[24:27], s46 offen sc1\n"
      "v_readlane_b32 s46, v37, s42\n"
      "s_add_u32 s42, s42, 1\n"
      "v_subrev_u32 v139, 73728, v6\n"
      "v_lshl_add_u32 v216, v192, 4, v216\n"
      "v_subrev_u32 v81, 73728, v5\n"
      "s_waitcnt lgkmcnt(2)\n"
      "v_lshl_add_u32 v76, v82, 4, v76\n"
      "s_or_b32 s95, s46, s48\n"
      "s_cmp_ge_u32 s95, 0x40000000\n"
      "s_cbranch_scc1 L_rare_35_%=\n"
      "L_back_36_%=:\n"
      "s_waitcnt lgkmcnt(0)\n"
      "s_barrier\n"
      "ds_read_b128 v[128:131], v7\n"
      "ds_read_b128 v[202:205], v9 offset:4608\n"
      "ds_read_b128 v[148:151], v7 offset:4096\n"
      "ds_read_b128 v[210:213], v9\n"
      "ds_read_b128 v[120:123], v7 offset:16384\n"
      "ds_read_b128 v[96:99], v7 offset:8192\n"
      "ds_read_b128 v[104:107], v7 offset:12288\n"
      "s_waitcnt lgkmcnt(6)\n"
      "buffer_store_dwordx4 v[128:131], v20, s[24:27], s46 offen sc1\n"
      "s_waitcnt lgkmcnt(4)\n"
      "buffer_store_dwordx4 v[148:151], v21, s[24:27], s46 offen sc1\n"
      "v_add_f32 v204, v204, v205\n"
      "v_add_f32 v202, v202, v203\n"
      "s_waitcnt lgkmcnt(2)\n"
      "v_add_f32 v210, v210, v211\n"
      "v_add_f32 v212, v212, v213\n"
      "v_add_f32 v210, v210, v212\n"
      "v_rsq_f32 v210, v210\n"
      "v_add_f32 v202, v202, v204\n"
      "v_rsq_f32_e64 v202, v202\n"
      "v_mul_f32 v210, 0x415db3d7, v210\n"
      "v_mov_b32 v212, v210\n"
      "v_mul_f32 v202, 0x415db3d7, v202\n"
      "v_mov_b32 v204, v202\n"
      "v_permlane32_swap_b32 v210, v212\n"
      "v_mul_f32 v111, v212, v111\n"
      "v_mov_b32 v211, v210\n"
      "v_mul_f32 v142, v212, v142\n"
      "v_permlane32_swap_b32 v202, v204\n"
      "v_permlane16_swap_b32 v210, v211\n"
      "v_mul_f32 v144, v212, v144\n"
      "v_mul_f32 v207, v210, v207\n"
      "v_mov_b32 v203, v202\n"
      "v_mul_f32 v146, v212, v146\n"
      "v_mul_f32 v108, v212, v108\n"
      "v_mul_f32 v140, v212, v140\n"
      "v_mul_f32 v208, v210, v208\n"
      "v_mul_f32 v145, v212, v145\n"
      "v_permlane16_swap_b32 v202, v203\n"
      "v_mul_f32 v167, v211, v167\n"
      "v_mul_f32 v125, v211, v125\n"
      "v_mul_f32 v171, v203, v171\n"
      "v_mul_f32 v176, v203, v176\n"
      "v_mul_f32 v115, v210, v115\n"
      "v_mul_f32 v134, v203, v134\n"
      "v_mul_f32 v222, v210, v222\n"
      "v_mul_f32 v147, v212, v147\n"
      "v_mul_f32 v170, v203, v170\n"
      "v_mul_f32 v109, v212, v109\n"
      "v_mul_f32 v180, v211, v180\n"
      "v_mul_f32 v196, v202, v196\n"
      "v_mul_f32 v114, v210, v114\n"
      "v_mul_f32 v181, v211, v181\n"
      "v_mul_f32 v173, v203, v173\n"
      "v_cvt_pk_bf16_f32 v144, v144, v145\n"
      "v_mul_f32 v221, v210, v221\n"
      "v_mul_f32 v174, v203, v174\n"
      "v_mul_f32 v184, v202, v184\n"
      "v_mul_f32 v183, v211, v183\n"
      "v_mul_f32 v166, v211, v166\n"
      "v_mul_f32 v110, v212, v110\n"
      "v_mul_f32 v209, v210, v209\n"
      "v_mul_f32 v206, v210, v206\n"
      "v_mul_f32 v141, v212, v141\n"
      "v_mul_f32 v172, v203, v172\n"
      "v_cvt_pk_bf16_f32 v114, v114, v115\n"
      "v_cvt_pk_bf16_f32 v145, v146, v147\n"
      "v_mul_f32 v124, v211, v124\n"
      "v_mul_f32 v175, v203, v175\n"
      "v_mul_f32_e64 v220, v210, v220\n"
      "v_cvt_pk_bf16_f32 v147, v110, v111\n"
      "v_mul_f32 v116, v210, v116\n"
      "v_mul_f32 v186, v202, v186\n"
      "v_mul_f32 v185, v202, v185\n"
      "v_mul_f32 v127, v211, v127\n"
      "v_cvt_pk_bf16_f32 v220, v220, v221\n"
      "v_mul_f32 v182, v211, v182\n"
      "v_mul_f32 v168, v211, v168\n"
      "v_mul_f32 v187, v202, v187\n"
      "v_mul_f32 v223, v210, v223\n"
      "v_mul_f32 v162, v202, v162\n"
      "v_mul_f32 v197, v202, v197\n"
      "v_cvt_pk_bf16_f32 v184, v184, v185\n"
      "v_cvt_pk_bf16_f32 v185, v186, v187\n"
      "v_mul_f32_e64 v143, v212, v143\n"
      "v_cvt_pk_bf16_f32 v166, v166, v167\n"
      "v_cvt_pk_bf16_f32 v187, v196, v197\n"
      "v_cvt_pk_bf16_f32 v180, v180, v181\n"
      "v_mul_f32 v126, v211, v126\n"
      "v_cvt_pk_bf16_f32 v174, v174, v175\n"
      "v_mul_f32 v194, v202, v194\n"
      "v_cvt_pk_bf16_f32 v181, v182, v183\n"
      "v_mul_f32 v117, v210, v117\n"
      "v_mul_f32 v135, v203, v135\n"
      "v_cvt_pk_bf16_f32 v115, v116, v117\n"
      "v_cvt_pk_bf16_f32 v140, v140, v141\n"
      "v_mul_f32_e64 v136, v203, v136\n"
      "v_cvt_pk_bf16_f32 v117, v208, v209\n"
      "v_mul_f32 v177, v203, v177\n"
      "v_mul_f32 v137, v203, v137\n"
      "v_cvt_pk_bf16_f32 v134, v134, v135\n"
      "v_cvt_pk_bf16_f32 v146, v108, v109\n"
      "v_mul_f32 v169, v211, v169\n"
      "v_mul_f32 v195, v202, v195\n"
      "v_mul_f32 v163, v202, v163\n"
      "v_cvt_pk_bf16_f32 v135, v136, v137\n"
      "v_cvt_pk_bf16_f32 v116, v206, v207\n"
      "v_mul_f32 v161, v202, v161\n"
      "v_cvt_pk_bf16_f32 v141, v142, v143\n"
      "v_cvt_pk_bf16_f32 v167, v168, v169\n"
      "v_mul_f32 v160, v202, v160\n"
      "v_cvt_pk_bf16_f32 v160, v160, v161\n"
      "v_cvt_pk_bf16_f32 v221, v222, v223\n"
      "v_cvt_pk_bf16_f32 v186, v194, v195\n"
      "v_cvt_pk_bf16_f32 v175, v176, v177\n"
      "v_cvt_pk_bf16_f32 v161, v162, v163\n"
      "v_cvt_pk_bf16_f32 v136, v170, v171\n"
      "v_cvt_pk_bf16_f32 v137, v172, v173\n"
      "v_cvt_pk_bf16_f32 v183, v126, v127\n"
      "v_cvt_pk_bf16_f32 v182, v124, v125\n"
      "s_waitcnt lgkmcnt(0)\n"
      "buffer_store_dwordx4 v[96:99], v22, s[24:27], s46 offen sc1\n"
      "ds_write_b128 v5, v[144:147] offset:30720\n"
      "ds_write_b64 v6, v[166:167] offset:24576\n"
      "ds_write_b64 v139, v[174:175] offset:6144\n"
      "ds_write_b64 v6, v[140:141] offset:30720\n"
      "ds_write_b64 v139, v[160:161]\n"
      "ds_write_b64 v6, v[220:221] offset:18432\n"
      "ds_write_b128 v5, v[180:183] offset:24576\n"
      "ds_write_b128 v81, v[184:187]\n"
      "ds_write_b128 v5, v[114:117] offset:18432\n"
      "ds_write_b128 v81, v[134:137] offset:6144\n"
      "buffer_store_dwordx4 v[104:107], v23, s[24:27], s46 offen sc1\n"
      "buffer_store_dwordx4 v[120:123], v25, s[24:27], s46 offen sc1\n"
      "v_readlane_b32 s46, v37, s42\n"
      "s_add_u32 s42, s42, 1\n"
      "s_or_b32 s95, s46, s48\n"
      "s_cmp_ge_u32 s95, 0x40000000\n"
      "s_cbranch_scc1 L_rare_39_%=\n"
      "L_back_40_%=:\n"
      "s_waitcnt lgkmcnt(0)\n"
      "s_barrier\n"
      "ds_read_b128 v[172:175], v7 offset:18432\n"
      "ds_read_b128 v[166:169], v7 offset:22528\n"
      "ds_read_b128 v[140:143], v7 offset:26624\n"
      "ds_read_b128 v[120:123], v7 offset:30720\n"
      "ds_read_b128 v[96:99], v7 offset:34816\n"
      "ds_read_b128 v[104:107], v198\n"
      "ds_read_b128 v[114:117], v198 offset:1024\n"
      "s_waitcnt lgkmcnt(6)\n"
      "buffer_store_dwordx4 v[172:175], v20, s[24:27], s46 offen sc1\n"
      "s_waitcnt lgkmcnt(5)\n"
      "buffer_store_dwordx4 v[166:169], v21, s[24:27], s46 offen sc1\n"
      "s_waitcnt lgkmcnt(4)\n"
      "buffer_store_dwordx4 v[140:143], v22, s[24:27], s46 offen sc1\n"
      "s_waitcnt lgkmcnt(3)\n"
      "buffer_store_dwordx4 v[120:123], v23, s[24:27], s46 offen sc1\n"
      "s_waitcnt lgkmcnt(2)\n"
      "buffer_store_dwordx4 v[96:99], v25, s[24:27], s46 offen sc1\n"
      "s_waitcnt lgkmcnt(1)\n"
      "buffer_store_dwordx4 v[104:107], v216, s[24:27], 0 offen sc1\n"
      "s_waitcnt lgkmcnt(0)\n"
      "buffer_store_dwordx4 v[114:117], v76, s[24:27], 0 offen sc1\n"
      "s_endpgm\n"
      "L_rare_1_%=:\n"
      "s_nop 7\n"
      "s_waitcnt lgkmcnt(0)\n"
      "s_add_u32 s72, s42, 1\n"
      "v_readlane_b32 s77, v38, s72\n"
      "v_subrev_u32 v202, 115456, v9\n"
      "s_and_b32 s73, s77, -16\n"
      "s_mov_b32 m0, 110592\n"
      "s_nop 0\n"
      "buffer_load_dwordx4 v202, s[28:31], s73 offen  lds\n"
      "s_add_u32 s73, s73, 1024\n"
      "s_mov_b32 m0, 111616\n"
      "s_nop 0\n"
      "buffer_load_dwordx4 v202, s[28:31], s73 offen  lds\n"
      "s_add_u32 s73, s73, 1024\n"
      "s_mov_b32 m0, 112640\n"
      "s_nop 0\n"
      "buffer_load_dwordx4 v202, s[28:31], s73 offen  lds\n"
      "s_add_u32 s73, s73, 1024\n"
      "s_mov_b32 m0, 113664\n"
      "s_nop 0\n"
      "buffer_load_dwordx4 v202, s[28:31], s73 offen  lds\n"
      "s_add_u32 s73, s73, 1024\n"
      "s_mov_b32 m0, 114688\n"
      "s_mov_b32 exec_hi, 0xffff\n"
      "buffer_load_dwordx4 v202, s[28:31], s73 offen  lds\n"
      "s_mov_b32 exec_hi, -1\n"
      "s_add_u32 s73, s73, 1024\n"
      "s_and_b32 s77, s77, 15\n"
      "s_waitcnt lgkmcnt(0)\n"
      "s_nop 4\n"
      "s_branch L_back_2_%=\n"
      "L_rare_3_%=:\n"
      "s_nop 7\n"
      "s_waitcnt lgkmcnt(0)\n"
      "s_waitcnt vmcnt(0)\n"
      "v_subrev_u32 v158, 115456, v9\n"
      "v_bfe_u32 v89, v158, 4, 4\n"
      "v_mul_u32_u24 v89, 98, v89\n"
      "v_lshrrev_b32 v158, 8, v158\n"
      "v_lshl_add_u32 v89, v158, 3, v89\n"
      "v_add_u32 v89, s77, v89\n"
      "v_and_b32 v201, 2, v89\n"
      "v_and_b32 v89, -4, v89\n"
      "v_add_u32 v89, 110592, v89\n"
      "ds_read2_b32 v[118:119], v89 offset0:0 offset1:1\n"
      "ds_read2_b32 v[120:121], v89 offset0:8 offset1:9\n"
      "ds_read2_b32 v[202:203], v89 offset0:16 offset1:17\n"
      "ds_read2_b32 v[204:205], v89 offset0:2 offset1:10\n"
      "ds_read2_b32 v[206:207], v89 offset0:18 offset1:24\n"
      "s_waitcnt lgkmcnt(4)\n"
      "v_alignbyte_b32 v118, v119, v118, v201\n"
      "s_waitcnt lgkmcnt(1)\n"
      "v_alignbyte_b32 v119, v204, v119, v201\n"
      "v_lshlrev_b32 v158, 16, v118\n"
      "v_fmac_f32 v136, s53, v158\n"
      "v_and_b32 v158, 0xffff0000, v118\n"
      "v_fmac_f32 v137, s53, v158\n"
      "v_lshlrev_b32 v158, 16, v119\n"
      "v_fmac_f32_e64 v138, s53, v158\n"
      "v_and_b32 v158, 0xffff0000, v119\n"
      "v_fmac_f32 v139, s53, v158\n"
      "v_alignbyte_b32 v120, v121, v120, v201\n"
      "v_alignbyte_b32 v121, v205, v121, v201\n"
      "v_lshlrev_b32 v158, 16, v120\n"
      "v_fmac_f32 v180, s53, v158\n"
      "v_and_b32 v158, 0xffff0000, v120\n"
      "v_fmac_f32 v181, s53, v158\n"
      "v_lshlrev_b32 v158, 16, v121\n"
      "v_fmac_f32 v182, s53, v158\n"
      "v_and_b32 v158, 0xffff0000, v121\n"
      "v_fmac_f32 v183, s53, v158\n"
      "v_alignbyte_b32 v202, v203, v202, v201\n"
      "s_waitcnt lgkmcnt(0)\n"
      "v_alignbyte_b32 v203, v206, v203, v201\n"
      "v_lshlrev_b32 v158, 16, v202\n"
      "v_fmac_f32 v96, s53, v158\n"
      "v_and_b32 v158, 0xffff0000, v202\n"
      "v_fmac_f32 v97, s53, v158\n"
      "v_lshlrev_b32 v158, 16, v203\n"
      "v_fmac_f32 v98, s53, v158\n"
      "v_and_b32 v158, 0xffff0000, v203\n"
      "v_fmac_f32 v99, s53, v158\n"
      "v_alignbyte_b32 v158, v207, v207, v201\n"
      "v_lshlrev_b32 v158, 16, v158\n"
      "v_fmac_f32 v200, s53, v158\n"
      "v_max3_f32 v89, v136, v137, v138\n"
      "v_max3_f32 v201, v139, v180, v181\n"
      "v_max3_f32 v89, v89, v182, v183\n"
      "v_max3_f32 v201, v201, v96, v97\n"
      "v_max3_f32 v89, v89, v98, v99\n"
      "v_max3_f32 v89, v89, v201, v200\n"
      "v_mov_b32 v158, v89\n"
      "s_nop 1\n"
      "v_permlane16_swap_b32 v89, v158\n"
      "v_max_f32 v89, v89, v158\n"
      "v_mov_b32 v158, v89\n"
      "s_nop 1\n"
      "v_permlane32_swap_b32 v89, v158\n"
      "v_max_f32 v89, v89, v158\n"
      "v_sub_f32 v136, v136, v89\n"
      "v_sub_f32 v137, v137, v89\n"
      "v_sub_f32 v138, v138, v89\n"
      "v_sub_f32 v139, v139, v89\n"
      "v_sub_f32 v180, v180, v89\n"
      "v_sub_f32 v181, v181, v89\n"
      "v_sub_f32 v182, v182, v89\n"
      "v_sub_f32 v183, v183, v89\n"
      "v_sub_f32 v96, v96, v89\n"
      "v_sub_f32 v97, v97, v89\n"
      "v_sub_f32 v98, v98, v89\n"
      "v_sub_f32 v99, v99, v89\n"
      "v_sub_f32_e64 v200, v200, v89\n"
      "s_waitcnt lgkmcnt(0)\n"
      "s_nop 4\n"
      "s_branch L_back_4_%=\n"
      "L_rare_5_%=:\n"
      "s_nop 7\n"
      "s_waitcnt lgkmcnt(0)\n"
      "s_waitcnt vmcnt(0)\n"
      "v_subrev_u32 v169, 115456, v9\n"
      "v_bfe_u32 v205, v169, 4, 4\n"
      "v_mul_u32_u24 v205, 98, v205\n"
      "v_lshrrev_b32 v169, 8, v169\n"
      "v_lshl_add_u32 v205, v169, 3, v205\n"
      "v_add_u32 v205, s77, v205\n"
      "v_and_b32 v170, 2, v205\n"
      "v_and_b32 v205, -4, v205\n"
      "v_add_u32 v205, 112160, v205\n"
      "ds_read2_b32 v[154:155], v205 offset0:0 offset1:1\n"
      "ds_read2_b32 v[152:153], v205 offset0:8 offset1:9\n"
      "ds_read2_b32 v[174:175], v205 offset0:16 offset1:17\n"
      "ds_read2_b32 v[98:99], v205 offset0:2 offset1:10\n"
      "ds_read2_b32 v[200:201], v205 offset0:18 offset1:24\n"
      "s_waitcnt lgkmcnt(4)\n"
      "v_alignbyte_b32 v154, v155, v154, v170\n"
      "s_waitcnt lgkmcnt(1)\n"
      "v_alignbyte_b32 v155, v98, v155, v170\n"
      "v_lshlrev_b32 v169, 16, v154\n"
      "v_fmac_f32 v208, s53, v169\n"
      "v_and_b32 v169, 0xffff0000, v154\n"
      "v_fmac_f32 v209, s53, v169\n"
      "v_lshlrev_b32 v169, 16, v155\n"
      "v_fmac_f32 v210, s53, v169\n"
      "v_and_b32 v169, 0xffff0000, v155\n"
      "v_fmac_f32 v211, s53, v169\n"
      "v_alignbyte_b32 v152, v153, v152, v170\n"
      "v_alignbyte_b32 v153, v99, v153, v170\n"
      "v_lshlrev_b32 v169, 16, v152\n"
      "v_fmac_f32 v124, s53, v169\n"
      "v_and_b32 v169, 0xffff0000, v152\n"
      "v_fmac_f32 v125, s53, v169\n"
      "v_lshlrev_b32 v169, 16, v153\n"
      "v_fmac_f32_e64 v126, s53, v169\n"
      "v_and_b32 v169, 0xffff0000, v153\n"
      "v_fmac_f32 v127, s53, v169\n"
      "v_alignbyte_b32 v174, v175, v174, v170\n"
      "s_waitcnt lgkmcnt(0)\n"
      "v_alignbyte_b32 v175, v200, v175, v170\n"
      "v_lshlrev_b32 v169, 16, v174\n"
      "v_fmac_f32 v118, s53, v169\n"
      "v_and_b32 v169, 0xffff0000, v174\n"
      "v_fmac_f32 v119, s53, v169\n"
      "v_lshlrev_b32 v169, 16, v175\n"
      "v_fmac_f32_e64 v120, s53, v169\n"
      "v_and_b32 v169, 0xffff0000, v175\n"
      "v_fmac_f32 v121, s53, v169\n"
      "v_alignbyte_b32 v169, v201, v201, v170\n"
      "v_lshlrev_b32 v169, 16, v169\n"
      "v_fmac_f32_e64 v202, s53, v169\n"
      "v_max3_f32 v205, v208, v209, v210\n"
      "v_max3_f32 v170, v211, v124, v125\n"
      "v_max3_f32 v205, v205, v126, v127\n"
      "v_max3_f32 v170, v170, v118, v119\n"
      "v_max3_f32 v205, v205, v120, v121\n"
      "v_max3_f32 v205, v205, v170, v202\n"
      "v_mov_b32 v169, v205\n"
      "s_nop 1\n"
      "v_permlane16_swap_b32 v205, v169\n"
      "v_max_f32 v205, v205, v169\n"
      "v_mov_b32 v169, v205\n"
      "s_nop 1\n"
      "v_permlane32_swap_b32 v205, v169\n"
      "v_max_f32 v205, v205, v169\n"
      "v_sub_f32 v208, v208, v205\n"
      "v_sub_f32 v209, v209, v205\n"
      "v_sub_f32 v210, v210, v205\n"
      "v_sub_f32 v211, v211, v205\n"
      "v_sub_f32 v124, v124, v205\n"
      "v_sub_f32 v125, v125, v205\n"
      "v_sub_f32 v126, v126, v205\n"
      "v_sub_f32 v127, v127, v205\n"
      "v_sub_f32 v118, v118, v205\n"
      "v_sub_f32 v119, v119, v205\n"
      "v_sub_f32 v120, v120, v205\n"
      "v_sub_f32 v121, v121, v205\n"
      "v_sub_f32_e64 v202, v202, v205\n"
      "s_waitcnt lgkmcnt(0)\n"
      "s_nop 4\n"
      "s_branch L_back_6_%=\n"
      "L_rare_7_%=:\n"
      "s_nop 7\n"
      "s_waitcnt lgkmcnt(0)\n"
      "s_waitcnt vmcnt(0)\n"
      "v_subrev_u32 v105, 115456, v9\n"
      "v_bfe_u32 v119, v105, 4, 4\n"
      "v_mul_u32_u24 v119, 98, v119\n"
      "v_lshrrev_b32_e64 v105, 8, v105\n"
      "v_lshl_add_u32 v119, v105, 3, v119\n"
      "v_add_u32 v119, s77, v119\n"
      "v_and_b32 v118, 2, v119\n"
      "v_and_b32_e64 v119, -4, v119\n"
      "v_add_u32 v119, 113728, v119\n"
      "ds_read2_b32 v[140:141], v119 offset0:0 offset1:1\n"
      "ds_read2_b32 v[168:169], v119 offset0:8 offset1:9\n"
      "ds_read2_b32 v[170:171], v119 offset0:16 offset1:17\n"
      "ds_read2_b32 v[114:115], v119 offset0:2 offset1:10\n"
      "ds_read2_b32 v[180:181], v119 offset0:18 offset1:24\n"
      "s_waitcnt lgkmcnt(4)\n"
      "v_alignbyte_b32 v140, v141, v140, v118\n"
      "s_waitcnt lgkmcnt(1)\n"
      "v_alignbyte_b32 v141, v114, v141, v118\n"
      "v_lshlrev_b32 v105, 16, v140\n"
      "v_fmac_f32 v172, s53, v105\n"
      "v_and_b32 v105, 0xffff0000, v140\n"
      "v_fmac_f32 v173, s53, v105\n"
      "v_lshlrev_b32 v105, 16, v141\n"
      "v_fmac_f32_e64 v174, s53, v105\n"
      "v_and_b32 v105, 0xffff0000, v141\n"
      "v_fmac_f32 v175, s53, v105\n"
      "v_alignbyte_b32 v168, v169, v168, v118\n"
      "v_alignbyte_b32 v169, v115, v169, v118\n"
      "v_lshlrev_b32 v105, 16, v168\n"
      "v_fmac_f32 v124, s53, v105\n"
      "v_and_b32 v105, 0xffff0000, v168\n"
      "v_fmac_f32 v125, s53, v105\n"
      "v_lshlrev_b32 v105, 16, v169\n"
      "v_fmac_f32 v126, s53, v105\n"
      "v_and_b32 v105, 0xffff0000, v169\n"
      "v_fmac_f32 v127, s53, v105\n"
      "v_alignbyte_b32 v170, v171, v170, v118\n"
      "s_waitcnt lgkmcnt(0)\n"
      "v_alignbyte_b32 v171, v180, v171, v118\n"
      "v_lshlrev_b32 v105, 16, v170\n"
      "v_fmac_f32 v136, s53, v105\n"
      "v_and_b32 v105, 0xffff0000, v170\n"
      "v_fmac_f32 v137, s53, v105\n"
      "v_lshlrev_b32 v105, 16, v171\n"
      "v_fmac_f32 v138, s53, v105\n"
      "v_and_b32 v105, 0xffff0000, v171\n"
      "v_fmac_f32 v139, s53, v105\n"
      "v_alignbyte_b32 v105, v181, v181, v118\n"
      "v_lshlrev_b32 v105, 16, v105\n"
      "v_fmac_f32 v98, s53, v105\n"
      "v_max3_f32 v119, v172, v173, v174\n"
      "v_max3_f32 v118, v175, v124, v125\n"
      "v_max3_f32 v119, v119, v126, v127\n"
      "v_max3_f32 v118, v118, v136, v137\n"
      "v_max3_f32 v119, v119, v138, v139\n"
      "v_max3_f32 v119, v119, v118, v98\n"
      "v_mov_b32 v105, v119\n"
      "s_nop 1\n"
      "v_permlane16_swap_b32 v119, v105\n"
      "v_max_f32 v119, v119, v105\n"
      "v_mov_b32 v105, v119\n"
      "s_nop 1\n"
      "v_permlane32_swap_b32 v119, v105\n"
      "v_max_f32 v119, v119, v105\n"
      "v_sub_f32 v172, v172, v119\n"
      "v_sub_f32 v173, v173, v119\n"
      "v_sub_f32 v174, v174, v119\n"
      "v_sub_f32 v175, v175, v119\n"
      "v_sub_f32 v124, v124, v119\n"
      "v_sub_f32 v125, v125, v119\n"
      "v_sub_f32 v126, v126, v119\n"
      "v_sub_f32 v127, v127, v119\n"
      "v_sub_f32 v136, v136, v119\n"
      "v_sub_f32 v137, v137, v119\n"
      "v_sub_f32 v138, v138, v119\n"
      "v_sub_f32 v139, v139, v119\n"
      "v_sub_f32 v98, v98, v119\n"
      "s_waitcnt lgkmcnt(0)\n"
      "s_nop 4\n"
      "s_branch L_back_8_%=\n"
      "L_rare_9_%=:\n"
      "s_nop 7\n"
      "s_waitcnt lgkmcnt(0)\n"
      "s_waitcnt vmcnt(0)\n"
      "v_subrev_u32 v120, 115456, v9\n"
      "v_lshrrev_b32 v120, 3, v120\n"
      "v_min_u32 v120, 96, v120\n"
      "v_add_u32 v120, s77, v120\n"
      "v_add_u32 v120, 115296, v120\n"
      "ds_read_u16 v120, v120\n"
      "s_waitcnt lgkmcnt(0)\n"
      "v_lshlrev_b32_e64 v120, 16, v120\n"
      "v_fmac_f32 v218, s53, v120\n"
      "s_nop 1\n"
      "v_max_f32_dpp v118, v218, v218 row_ror:8 row_mask:0xf bank_mask:0xf\n"
      "s_nop 1\n"
      "v_max_f32_dpp v118, v118, v118 row_ror:4 row_mask:0xf bank_mask:0xf\n"
      "s_nop 1\n"
      "v_max_f32_dpp v118, v118, v118 row_ror:2 row_mask:0xf bank_mask:0xf\n"
      "s_nop 1\n"
      "v_max_f32_dpp v118, v118, v118 row_ror:1 row_mask:0xf bank_mask:0xf\n"
      "s_nop 1\n"
      "v_max_f32_dpp v118, v118, v118 row_bcast:15 row_mask:0xa bank_mask:0xf\n"
      "s_nop 1\n"
      "v_max_f32_dpp v118, v118, v118 row_bcast:31 row_mask:0xc bank_mask:0xf\n"
      "s_nop 0\n"
      "v_readlane_b32 s72, v118, 63\n"
      "s_nop 1\n"
      "v_subrev_f32 v218, s72, v218\n"
      "s_waitcnt lgkmcnt(0)\n"
      "s_nop 4\n"
      "s_branch L_back_10_%=\n"
      "L_rare_27_%=:\n"
      "s_nop 7\n"
      "s_waitcnt lgkmcnt(0)\n"
      "s_bitcmp1_b32 s46, 31\n"
      "s_cselect_b32 s75, 100270464, 0\n"
      "s_bitcmp1_b32 s46, 30\n"
      "s_cselect_b32 s76, 196224, 0\n"
      "s_and_b32 s74, s46, 0x3fffffff\n"
      "s_cmp_ge_u32 s46, 0x40000000\n"
      "s_cselect_b32 s74, s74, 0\n"
      "s_cselect_b32 s46, 0, s46\n"
      "s_sub_u32 s74, s74, s69\n"
      "s_add_u32 s69, s69, s74\n"
      "s_sub_u32 s75, s75, s70\n"
      "s_add_u32 s70, s70, s75\n"
      "s_sub_u32 s76, s76, s71\n"
      "s_add_u32 s71, s71, s76\n"
      "v_add_u32 v20, s74, v20\n"
      "v_add_u32 v21, s74, v21\n"
      "v_add_u32 v22, s74, v22\n"
      "v_add_u32 v23, s74, v23\n"
      "v_add_u32 v25, s74, v25\n"
      "s_cmp_eq_u32 s75, 0\n"
      "s_cbranch_scc1 L_ax_25_%=\n"
      "v_bfe_i32 v217, v27, 10, 1\n"
      "v_and_b32 v217, s75, v217\n"
      "v_sub_u32 v20, v20, v217\n"
      "v_bfe_i32 v217, v27, 12, 1\n"
      "v_and_b32 v217, s75, v217\n"
      "v_sub_u32 v21, v21, v217\n"
      "v_bfe_i32 v217, v27, 14, 1\n"
      "v_and_b32 v217, s75, v217\n"
      "v_sub_u32 v22, v22, v217\n"
      "v_bfe_i32 v217, v27, 16, 1\n"
      "v_and_b32 v217, s75, v217\n"
      "v_sub_u32 v23, v23, v217\n"
      "v_bfe_i32 v217, v27, 18, 1\n"
      "v_and_b32 v217, s75, v217\n"
      "v_sub_u32 v25, v25, v217\n"
      "L_ax_25_%=:\n"
      "s_cmp_eq_u32 s76, 0\n"
      "s_cbranch_scc1 L_ax_26_%=\n"
      "v_bfe_i32 v217, v27, 11, 1\n"
      "v_and_b32 v217, s76, v217\n"
      "v_sub_u32_e64 v20, v20, v217\n"
      "v_bfe_i32 v217, v27, 13, 1\n"
      "v_and_b32 v217, s76, v217\n"
      "v_sub_u32 v21, v21, v217\n"
      "v_bfe_i32 v217, v27, 15, 1\n"
      "v_and_b32 v217, s76, v217\n"
      "v_sub_u32 v22, v22, v217\n"
      "v_bfe_i32 v217, v27, 17, 1\n"
      "v_and_b32 v217, s76, v217\n"
      "v_sub_u32 v23, v23, v217\n"
      "v_bfe_i32 v217, v27, 19, 1\n"
      "v_and_b32 v217, s76, v217\n"
      "v_sub_u32 v25, v25, v217\n"
      "L_ax_26_%=:\n"
      "s_or_b32 s48, s66, s69\n"
      "s_cmp_lg_u32 s48, 0\n"
      "s_cselect_b32 s48, 0x40000000, 0\n"
      "s_waitcnt lgkmcnt(0)\n"
      "s_nop 4\n"
      "s_branch L_back_28_%=\n"
      "L_rare_31_%=:\n"
      "s_nop 7\n"
      "s_waitcnt lgkmcnt(0)\n"
      "s_bitcmp1_b32 s46, 31\n"
      "s_cselect_b32 s75, 100270464, 0\n"
      "s_bitcmp1_b32 s46, 30\n"
      "s_cselect_b32 s76, 196224, 0\n"
      "s_and_b32 s74, s46, 0x3fffffff\n"
      "s_cmp_ge_u32 s46, 0x40000000\n"
      "s_cselect_b32 s74, s74, 0\n"
      "s_cselect_b32 s46, 0, s46\n"
      "s_sub_u32 s74, s74, s69\n"
      "s_add_u32 s69, s69, s74\n"
      "s_sub_u32 s75, s75, s70\n"
      "s_add_u32 s70, s70, s75\n"
      "s_sub_u32 s76, s76, s71\n"
      "s_add_u32 s71, s71, s76\n"
      "v_add_u32 v20, s74, v20\n"
      "v_add_u32 v21, s74, v21\n"
      "v_add_u32 v22, s74, v22\n"
      "v_add_u32 v23, s74, v23\n"
      "v_add_u32 v25, s74, v25\n"
      "s_cmp_eq_u32 s75, 0\n"
      "s_cbranch_scc1 L_ax_29_%=\n"
      "v_bfe_i32 v117, v27, 10, 1\n"
      "v_and_b32 v117, s75, v117\n"
      "v_sub_u32 v20, v20, v117\n"
      "v_bfe_i32 v117, v27, 12, 1\n"
      "v_and_b32 v117, s75, v117\n"
      "v_sub_u32 v21, v21, v117\n"
      "v_bfe_i32 v117, v27, 14, 1\n"
      "v_and_b32 v117, s75, v117\n"
      "v_sub_u32 v22, v22, v117\n"
      "v_bfe_i32 v117, v27, 16, 1\n"
      "v_and_b32 v117, s75, v117\n"
      "v_sub_u32 v23, v23, v117\n"
      "v_bfe_i32 v117, v27, 18, 1\n"
      "v_and_b32 v117, s75, v117\n"
      "v_sub_u32 v25, v25, v117\n"
      "L_ax_29_%=:\n"
      "s_cmp_eq_u32 s76, 0\n"
      "s_cbranch_scc1 L_ax_30_%=\n"
      "v_bfe_i32 v117, v27, 11, 1\n"
      "v_and_b32 v117, s76, v117\n"
      "v_sub_u32 v20, v20, v117\n"
      "v_bfe_i32 v117, v27, 13, 1\n"
      "v_and_b32 v117, s76, v117\n"
      "v_sub_u32 v21, v21, v117\n"
      "v_bfe_i32 v117, v27, 15, 1\n"
      "v_and_b32 v117, s76, v117\n"
      "v_sub_u32 v22, v22, v117\n"
      "v_bfe_i32 v117, v27, 17, 1\n"
      "v_and_b32 v117, s76, v117\n"
      "v_sub_u32 v23, v23, v117\n"
      "v_bfe_i32 v117, v27, 19, 1\n"
      "v_and_b32 v117, s76, v117\n"
      "v_sub_u32 v25, v25, v117\n"
      "L_ax_30_%=:\n"
      "s_or_b32 s48, s66, s69\n"
      "s_cmp_lg_u32 s48, 0\n"
      "s_cselect_b32 s48, 0x40000000, 0\n"
      "s_waitcnt lgkmcnt(0)\n"
      "s_nop 4\n"
      "s_branch L_back_32_%=\n"
      "L_rare_35_%=:\n"
      "s_nop 7\n"
      "s_waitcnt lgkmcnt(0)\n"
      "s_bitcmp1_b32 s46, 31\n"
      "s_cselect_b32 s75, 100270464, 0\n"
      "s_bitcmp1_b32 s46, 30\n"
      "s_cselect_b32 s76, 196224, 0\n"
      "s_and_b32 s74, s46, 0x3fffffff\n"
      "s_cmp_ge_u32 s46, 0x40000000\n"
      "s_cselect_b32 s74, s74, 0\n"
      "s_cselect_b32 s46, 0, s46\n"
      "s_sub_u32 s74, s74, s69\n"
      "s_add_u32 s69, s69, s74\n"
      "s_sub_u32 s75, s75, s70\n"
      "s_add_u32 s70, s70, s75\n"
      "s_sub_u32 s76, s76, s71\n"
      "s_add_u32 s71, s71, s76\n"
      "v_add_u32 v20, s74, v20\n"
      "v_add_u32 v21, s74, v21\n"
      "v_add_u32 v22, s74, v22\n"
      "v_add_u32 v23, s74, v23\n"
      "v_add_u32 v25, s74, v25\n"
      "s_cmp_eq_u32 s75, 0\n"
      "s_cbranch_scc1 L_ax_33_%=\n"
      "v_bfe_i32 v131, v27, 10, 1\n"
      "v_and_b32 v131, s75, v131\n"
      "v_sub_u32 v20, v20, v131\n"
      "v_bfe_i32 v131, v27, 12, 1\n"
      "v_and_b32 v131, s75, v131\n"
      "v_sub_u32 v21, v21, v131\n"
      "v_bfe_i32 v131, v27, 14, 1\n"
      "v_and_b32 v131, s75, v131\n"
      "v_sub_u32 v22, v22, v131\n"
      "v_bfe_i32 v131, v27, 16, 1\n"
      "v_and_b32 v131, s75, v131\n"
      "v_sub_u32 v23, v23, v131\n"
      "v_bfe_i32 v131, v27, 18, 1\n"
      "v_and_b32 v131, s75, v131\n"
      "v_sub_u32 v25, v25, v131\n"
      "L_ax_33_%=:\n"
      "s_cmp_eq_u32 s76, 0\n"
      "s_cbranch_scc1 L_ax_34_%=\n"
      "v_bfe_i32 v131, v27, 11, 1\n"
      "v_and_b32 v131, s76, v131\n"
      "v_sub_u32 v20, v20, v131\n"
      "v_bfe_i32 v131, v27, 13, 1\n"
      "v_and_b32 v131, s76, v131\n"
      "v_sub_u32 v21, v21, v131\n"
      "v_bfe_i32 v131, v27, 15, 1\n"
      "v_and_b32 v131, s76, v131\n"
      "v_sub_u32 v22, v22, v131\n"
      "v_bfe_i32 v131, v27, 17, 1\n"
      "v_and_b32 v131, s76, v131\n"
      "v_sub_u32 v23, v23, v131\n"
      "v_bfe_i32 v131, v27, 19, 1\n"
      "v_and_b32 v131, s76, v131\n"
      "v_sub_u32 v25, v25, v131\n"
      "L_ax_34_%=:\n"
      "s_or_b32 s48, s66, s69\n"
      "s_cmp_lg_u32 s48, 0\n"
      "s_cselect_b32 s48, 0x40000000, 0\n"
      "s_waitcnt lgkmcnt(0)\n"
      "s_nop 4\n"
      "s_branch L_back_36_%=\n"
      "L_rare_39_%=:\n"
      "s_nop 7\n"
      "s_waitcnt lgkmcnt(0)\n"
      "s_bitcmp1_b32 s46, 31\n"
      "s_cselect_b32 s75, 100270464, 0\n"
      "s_bitcmp1_b32 s46, 30\n"
      "s_cselect_b32 s76, 196224, 0\n"
      "s_and_b32 s74, s46, 0x3fffffff\n"
      "s_cmp_ge_u32 s46, 0x40000000\n"
      "s_cselect_b32 s74, s74, 0\n"
      "s_cselect_b32 s46, 0, s46\n"
      "s_sub_u32 s74, s74, s69\n"
      "s_add_u32 s69, s69, s74\n"
      "s_sub_u32 s75, s75, s70\n"
      "s_add_u32 s70, s70, s75\n"
      "s_sub_u32 s76, s76, s71\n"
      "s_add_u32 s71, s71, s76\n"
      "v_add_u32 v20, s74, v20\n"
      "v_add_u32 v21, s74, v21\n"
      "v_add_u32 v22, s74, v22\n"
      "v_add_u32 v23, s74, v23\n"
      "v_add_u32 v25, s74, v25\n"
      "s_cmp_eq_u32 s75, 0\n"
      "s_cbranch_scc1 L_ax_37_%=\n"
      "v_bfe_i32 v175, v27, 10, 1\n"
      "v_and_b32 v175, s75, v175\n"
      "v_sub_u32 v20, v20, v175\n"
      "v_bfe_i32 v175, v27, 12, 1\n"
      "v_and_b32 v175, s75, v175\n"
      "v_sub_u32 v21, v21, v175\n"
      "v_bfe_i32 v175, v27, 14, 1\n"
      "v_and_b32 v175, s75, v175\n"
      "v_sub_u32 v22, v22, v175\n"
      "v_bfe_i32 v175, v27, 16, 1\n"
      "v_and_b32 v175, s75, v175\n"
      "v_sub_u32 v23, v23, v175\n"
      "v_bfe_i32 v175, v27, 18, 1\n"
      "v_and_b32 v175, s75, v175\n"
      "v_sub_u32 v25, v25, v175\n"
      "L_ax_37_%=:\n"
      "s_cmp_eq_u32 s76, 0\n"
      "s_cbranch_scc1 L_ax_38_%=\n"
      "v_bfe_i32 v175, v27, 11, 1\n"
      "v_and_b32 v175, s76, v175\n"
      "v_sub_u32 v20, v20, v175\n"
      "v_bfe_i32 v175, v27, 13, 1\n"
      "v_and_b32 v175, s76, v175\n"
      "v_sub_u32 v21, v21, v175\n"
      "v_bfe_i32 v175, v27, 15, 1\n"
      "v_and_b32 v175, s76, v175\n"
      "v_sub_u32 v22, v22, v175\n"
      "v_bfe_i32 v175, v27, 17, 1\n"
      "v_and_b32 v175, s76, v175\n"
      "v_sub_u32 v23, v23, v175\n"
      "v_bfe_i32 v175, v27, 19, 1\n"
      "v_and_b32 v175, s76, v175\n"
      "v_sub_u32 v25, v25, v175\n"
      "L_ax_38_%=:\n"
      "s_or_b32 s48, s66, s69\n"
      "s_cmp_lg_u32 s48, 0\n"
      "s_cselect_b32 s48, 0x40000000, 0\n"
      "s_waitcnt lgkmcnt(0)\n"
      "s_nop 4\n"
      "s_branch L_back_40_%=\n"
      "L_cend_%=:\n"
      :
      : "s"(__builtin_amdgcn_kernarg_segment_ptr()), "s"(wave), "s"(blockIdx.x), "s"(CT_TAB), "s"(lds0)
      : "v0","v1","v2","v3","v4","v5","v6","v7","v8","v9","v10","v11","v12","v13","v14","v15","v16","v17","v18","v19","v20","v21","v22","v23","v24","v25","v26","v27","v28","v29","v30","v31","v32","v33","v34","v35","v36","v37","v38","v39","v40","v41","v42","v43","v44","v45","v46","v47","v48","v49","v50","v51","v52","v53","v54","v55","v56","v57","v58","v59","v60","v61","v62","v63","v64","v65","v66","v67","v68","v69","v70","v71","v72","v73","v74","v75","v76","v77","v78","v79","v80","v81","v82","v83","v84","v85","v86","v87","v88","v89","v90","v91","v92","v93","v94","v95","v96","v97","v98","v99","v100","v101","v102","v103","v104","v105","v106","v107","v108","v109","v110","v111","v112","v113","v114","v115","v116","v117","v118","v119","v120","v121","v122","v123","v124","v125","v126","v127","v128","v129","v130","v131","v132","v133","v134","v135","v136","v137","v138","v139","v140","v141","v142","v143","v144","v145","v146","v147","v148","v149","v150","v151","v152","v153","v154","v155","v156","v157","v158","v159","v160","v161","v162","v163","v164","v165","v166","v167","v168","v169","v170","v171","v172","v173","v174","v175","v176","v177","v178","v179","v180","v181","v182","v183","v184","v185","v186","v187","v188","v189","v190","v191","v192","v193","v194","v195","v196","v197","v198","v199","v200","v201","v202","v203","v204","v205","v206","v207","v208","v209","v210","v211","v212","v213","v214","v215","v216","v217","v218","v219","v220","v221","v222","v223","v224","v225","v226","v227","v228","v229","v230","v231","v232","v233","v234","v235","v236","v237","v238","v239","v240","v241","v242","v243","v244","v245","v246","v247","v248","v249","v250","v251","v252","v253","v254","v255","a0","a1","a2","a3","a4","a5","a6","a7","a8","a9","a10","a11","a12","a13","a14","a15","a16","a17","a18","a19","a20","a21","a22","a23","a24","a25","a26","a27","a28","a29","a30","a31","a32","a33","a34","a35","a36","a37","a38","a39","a40","a41","a42","a43","a44","a45","a46","a47","a48","a49","a50","a51","a52","a53","a54","a55","a56","a57","a58","a59","a60","a61","a62","a63","a64","a65","a66","a67","a68","a69","a70","a71","a72","a73","a74","a75","a76","a77","a78","a79","a80","a81","a82","a83","a84","a85","a86","a87","a88","a89","a90","a91","a92","a93","a94","a95","a96","a97","a98","a99","a100","a101","a102","a103","a104","a105","a106","a107","a108","a109","a110","a111","a112","a113","a114","a115","a116","a117","a118","a119","a120","a121","a122","a123","a124","a125","a126","a127","a128","a129","a130","a131","a132","a133","a134","a135","a136","a137","a138","a139","a140","a141","a142","a143","a144","a145","a146","a147","a148","a149","a150","a151","a152","a153","a154","a155","a156","a157","a158","a159","a160","a161","a162","a163","a164","a165","a166","a167","a168","a169","a170","a171","a172","a173","a174","a175","a176","a177","a178","a179","a180","a181","a182","a183","a184","a185","a186","a187","a188","a189","a190","a191","a192","a193","a194","a195","a196","a197","a198","a199","a200","a201","a202","a203","a204","a205","a206","a207","a208","a209","a210","a211","a212","a213","a214","a215","a216","a217","a218","a219","a220","a221","a222","a223","a224","a225","a226","a227","a228","a229","a230","a231","a232","a233","a234","a235","a236","a237","a238","a239","a240","a241","a242","a243","a244","a245","a246","a247","a248","a249","a250","a251","a252","a253","a254","a255","s16","s17","s18","s19","s20","s21","s22","s23","s24","s25","s26","s27","s28","s29","s30","s31","s33","s34","s35","s36","s37","s38","s39","s40","s41","s42","s43","s44","s45","s46","s47","s48","s49","s50","s51","s52","s53","s54","s55","s56","s57","s58","s59","s60","s61","s62","s63","s64","s65","s66","s67","s68","s69","s70","s71","s72","s73","s74","s75","s76","s77","s78","s79","s80","s81","s82","s83","s84","s85","s86","s87","s88","s89","s90","s91","s92","s93","s94","s95","s96","s97","s98","s99","s100","s101", "vcc", "scc", "memory");
}

}

extern "C" void kernel_launch(void* const* d_in, const int* in_sizes, int n_in, void* d_out, int out_size, void* d_ws,
                              size_t ws_size, hipStream_t stream) {
  const uint16_t* x = (const uint16_t*)d_in[0];
  const uint16_t* wqkv = (const uint16_t*)d_in[1];
  const uint16_t* wo = (const uint16_t*)d_in[2];
  const uint16_t* ls = (const uint16_t*)d_in[3];
  const uint16_t* w1 = (const uint16_t*)d_in[4];
  const uint16_t* w2 = (const uint16_t*)d_in[5];
  const uint16_t* rc = (const uint16_t*)d_in[6];
  const int* ridx = (const int*)d_in[7];
  const uint16_t* mask = (const uint16_t*)d_in[8];
  swin::swin_main<<<256, 256, 0, stream>>>(x, mask, (char*)d_ws, (uint16_t*)d_out, wqkv, wo, ls, w1, w2, rc, ridx);
}
```

```cpp
#include <hip/hip_runtime.h>
#include <hip/hip_bf16.h>
#include <stdint.h>

namespace swin {

constexpr unsigned CT_WORDS = 8192u;
constexpr unsigned LDS_BYTES = 163840u;

__device__ __attribute__((aligned(256))) const unsigned CT_TAB[CT_WORDS] = {0x4800u,0x4820u,0x4840u,0x4800u,0x4808u,0x4828u,0x4848u,0x4810u,0x4810u,0x4830u,0x4850u,0x4820u,0x4818u,0x4838u,0x4858u,0x4830u,0x4880u,0x48a0u,0x48c0u,0x4880u,0x4888u,0x48a8u,0x48c8u,0x4890u,0x4890u,0x48b0u,0x48d0u,0x48a0u,0x4898u,0x48b8u,0x48d8u,0x48b0u,0x4920u,0x4900u,0x4960u,0x4920u,0x4928u,0x4908u,0x4968u,0x4930u,0x4930u,0x4910u,0x4970u,0x4900u,0x4938u,0x4918u,0x4978u,0x4910u,0x49a0u,0x4980u,0x49e0u,0x49a0u,0x49a8u,0x4988u,0x49e8u,0x49b0u,0x49b0u,0x4990u,0x49f0u,0x4980u,0x49b8u,0x4998u,0x49f8u,0x4990u,0x4c40u,0x4c60u,0x4c00u,0x4a00u,0x4c48u,0x4c68u,0x4c08u,0x4a10u,0x4c50u,0x4c70u,0x4c10u,0x4a20u,0x4c58u,0x4c78u,0x4c18u,0x4a30u,0x4cc0u,0x4ce0u,0x4c80u,0x4a80u,0x4cc8u,0x4ce8u,0x4c88u,0x4a90u,0x4cd0u,0x4cf0u,0x4c90u,0x4aa0u,0x4cd8u,0x4cf8u,0x4c98u,0x4ab0u,0x4d60u,0x4d40u,0x4d20u,0x4b20u,0x4d68u,0x4d48u,0x4d28u,0x4b30u,0x4d70u,0x4d50u,0x4d30u,0x4b00u,0x4d78u,0x4d58u,0x4d38u,0x4b10u,0x4de0u,0x4dc0u,0x4da0u,0x4ba0u,0x4de8u,0x4dc8u,0x4da8u,0x4bb0u,0x4df0u,0x4dd0u,0x4db0u,0x4b80u,0x4df8u,0x4dd8u,0x4db8u,0x4b90u,0x5000u,0x5020u,0x5040u,0x4c40u,0x5008u,0x5028u,0x5048u,0x4c50u,0x5010u,0x5030u,0x5050u,0x4c60u,0x5018u,0x5038u,0x5058u,0x4c70u,0x5080u,0x50a0u,0x50c0u,0x4cc0u,0x5088u,0x50a8u,0x50c8u,0x4cd0u,0x5090u,0x50b0u,0x50d0u,0x4ce0u,0x5098u,0x50b8u,0x50d8u,0x4cf0u,0x5120u,0x5100u,0x5160u,0x4d60u,0x5128u,0x5108u,0x5168u,0x4d70u,0x5130u,0x5110u,0x5170u,0x4d40u,0x5138u,0x5118u,0x5178u,0x4d50u,0x51a0u,0x5180u,0x51e0u,0x4de0u,0x51a8u,0x5188u,0x51e8u,0x4df0u,0x51b0u,0x5190u,0x51f0u,0x4dc0u,0x51b8u,0x5198u,0x51f8u,0x4dd0u,0x5440u,0x5460u,0x5400u,0x4e40u,0x5448u,0x5468u,0x5408u,0x4e50u,0x5450u,0x5470u,0x5410u,0x4e60u,0x5458u,0x5478u,0x5418u,0x4e70u,0x54c0u,0x54e0u,0x5480u,0x4ec0u,0x54c8u,0x54e8u,0x5488u,0x4ed0u,0x54d0u,0x54f0u,0x5490u,0x4ee0u,0x54d8u,0x54f8u,0x5498u,0x4ef0u,0x5560u,0x5540u,0x5520u,0x4f60u,0x5568u,0x5548u,0x5528u,0x4f70u,0x5570u,0x5550u,0x5530u,0x4f40u,0x5578u,0x5558u,0x5538u,0x4f50u,0x55e0u,0x55c0u,0x55a0u,0x4fe0u,0x55e8u,0x55c8u,0x55a8u,0x4ff0u,0x55f0u,0x55d0u,0x55b0u,0x4fc0u,0x55f8u,0x55d8u,0x55b8u,0x4fd0u,0x4840u,0x1e800u,0x1eb00u,0x0u,0x4848u,0x1ec90u,0x1ef90u,0x10u,0x4850u,0x1f120u,0x1f420u,0x20u,0x4858u,0x1f5b0u,0x1f8b0u,0x30u,0x48c0u,0x1fa40u,0x1fd40u,0x40u,0x48c8u,0x1fed0u,0x201d0u,0x50u,0x48d0u,0x20360u,0x20660u,0x60u,0x48d8u,0x207f0u,0x20af0u,0x70u,0x4960u,0x20c80u,0x20f80u,0x180u,0x4968u,0x21110u,0x21410u,0x190u,0x4970u,0x215a0u,0x218a0u,0x1a0u,0x4978u,0x21a30u,0x21d30u,0x1b0u,0x49e0u,0x21ec0u,0x221c0u,0x1c0u,0x49e8u,0x22350u,0x22650u,0x1d0u,0x49f0u,0x227e0u,0x22ae0u,0x1e0u,0x49f8u,0x22c70u,0x22f70u,0x1f0u,0x4a40u,0x1e830u,0x1eb08u,0x320u,0x4a48u,0x1ecc0u,0x1ef98u,0x330u,0x4a50u,0x1f150u,0x1f428u,0x300u,0x4a58u,0x1f5e0u,0x1f8b8u,0x310u,0x4ac0u,0x1fa70u,0x1fd48u,0x360u,0x4ac8u,0x1ff00u,0x201d8u,0x370u,0x4ad0u,0x20390u,0x20668u,0x340u,0x4ad8u,0x20820u,0x20af8u,0x350u,0x4b60u,0x20cb0u,0x20f88u,0x4a0u,0x4b68u,0x21140u,0x21418u,0x4b0u,0x4b70u,0x215d0u,0x218a8u,0x480u,0x4b78u,0x21a60u,0x21d38u,0x490u,0x4be0u,0x21ef0u,0x221c8u,0x4e0u,0x4be8u,0x22380u,0x22658u,0x4f0u,0x4bf0u,0x22810u,0x22ae8u,0x4c0u,0x4bf8u,0x22ca0u,0x22f78u,0x4d0u,0x4c00u,0x1e860u,0x1eb10u,0x600u,0x4c08u,0x1ecf0u,0x1efa0u,0x610u,0x4c10u,0x1f180u,0x1f430u,0x620u,0x4c18u,0x1f610u,0x1f8c0u,0x630u,0x4c80u,0x1faa0u,0x1fd50u,0x640u,0x4c88u,0x1ff30u,0x201e0u,0x650u,0x4c90u,0x203c0u,0x20670u,0x660u,0x4c98u,0x20850u,0x20b00u,0x670u,0x4d20u,0x20ce0u,0x20f90u,0x780u,0x4d28u,0x21170u,0x21420u,0x790u,0x4d30u,0x21600u,0x218b0u,0x7a0u,0x4d38u,0x21a90u,0x21d40u,0x7b0u,0x4da0u,0x21f20u,0x221d0u,0x7c0u,0x4da8u,0x223b0u,0x22660u,0x7d0u,0x4db0u,0x22840u,0x22af0u,0x7e0u,0x4db8u,0x22cd0u,0x22f80u,0x7f0u,0x4e00u,0x1e890u,0x1eb18u,0x920u,0x4e08u,0x1ed20u,0x1efa8u,0x930u,0x4e10u,0x1f1b0u,0x1f438u,0x900u,0x4e18u,0x1f640u,0x1f8c8u,0x910u,0x4e80u,0x1fad0u,0x1fd58u,0x960u,0x4e88u,0x1ff60u,0x201e8u,0x970u,0x4e90u,0x203f0u,0x20678u,0x940u,0x4e98u,0x20880u,0x20b08u,0x950u,0x4f20u,0x20d10u,0x20f98u,0xaa0u,0x4f28u,0x211a0u,0x21428u,0xab0u,0x4f30u,0x21630u,0x218b8u,0xa80u,0x4f38u,0x21ac0u,0x21d48u,0xa90u,0x4fa0u,0x21f50u,0x221d8u,0xae0u,0x4fa8u,0x223e0u,0x22668u,0xaf0u,0x4fb0u,0x22870u,0x22af8u,0xac0u,0x4fb8u,0x22d00u,0x22f88u,0xad0u,0x0u,0x40u,0x9000u,0x9000u,0x80u,0xc0u,0x9010u,0x9010u,0x110u,0x150u,0x9020u,0x9020u,0x190u,0x1d0u,0x9030u,0x9030u,0x220u,0x260u,0x9040u,0x9040u,0x2a0u,0x2e0u,0x9050u,0x9050u,0x330u,0x370u,0x9060u,0x9060u,0x3b0u,0x3f0u,0x9070u,0x9070u,0x440u,0x400u,0x9080u,0x9080u,0x4c0u,0x480u,0x9090u,0x9090u,0x550u,0x510u,0x90a0u,0x90a0u,0x5d0u,0x590u,0x90b0u,0x90b0u,0x660u,0x620u,0x90c0u,0x90c0u,0x6e0u,0x6a0u,0x90d0u,0x90d0u,0x770u,0x730u,0x90e0u,0x90e0u,0x7f0u,0x7b0u,0x90f0u,0x90f0u,0x10u,0x50u,0x9300u,0x9300u,0x90u,0xd0u,0x9310u,0x9310u,0x100u,0x140u,0x9320u,0x9320u,0x180u,0x1c0u,0x9330u,0x9330u,0x230u,0x270u,0x9340u,0x9340u,0x2b0u,0x2f0u,0x9350u,0x9350u,0x320u,0x360u,0x9360u,0x9360u,0x3a0u,0x3e0u,0x9370u,0x9370u,0x450u,0x410u,0x9380u,0x9380u,0x4d0u,0x490u,0x9390u,0x9390u,0x540u,0x500u,0x93a0u,0x93a0u,0x5c0u,0x580u,0x93b0u,0x93b0u,0x670u,0x630u,0x93c0u,0x93c0u,0x6f0u,0x6b0u,0x93d0u,0x93d0u,0x760u,0x720u,0x93e0u,0x93e0u,0x7e0u,0x7a0u,0x93f0u,0x93f0u,0x20u,0x60u,0x9600u,0x9600u,0xa0u,0xe0u,0x9610u,0x9610u,0x130u,0x170u,0x9620u,0x9620u,0x1b0u,0x1f0u,0x9630u,0x9630u,0x200u,0x240u,0x9640u,0x9640u,0x280u,0x2c0u,0x9650u,0x9650u,0x310u,0x350u,0x9660u,0x9660u,0x390u,0x3d0u,0x9670u,0x9670u,0x460u,0x420u,0x9680u,0x9680u,0x4e0u,0x4a0u,0x9690u,0x9690u,0x570u,0x530u,0x96a0u,0x96a0u,0x5f0u,0x5b0u,0x96b0u,0x96b0u,0x640u,0x600u,0x96c0u,0x96c0u,0x6c0u,0x680u,0x96d0u,0x96d0u,0x750u,0x710u,0x96e0u,0x96e0u,0x7d0u,0x790u,0x96f0u,0x96f0u,0x30u,0x70u,0x9900u,0x9900u,0xb0u,0xf0u,0x9910u,0x9910u,0x120u,0x160u,0x9920u,0x9920u,0x1a0u,0x1e0u,0x9930u,0x9930u,0x210u,0x250u,0x9940u,0x9940u,0x290u,0x2d0u,0x9950u,0x9950u,0x300u,0x340u,0x9960u,0x9960u,0x380u,0x3c0u,0x9970u,0x9970u,0x470u,0x430u,0x9980u,0x9980u,0x4f0u,0x4b0u,0x9990u,0x9990u,0x560u,0x520u,0x99a0u,0x99a0u,0x5e0u,0x5a0u,0x99b0u,0x99b0u,0x650u,0x610u,0x99c0u,0x99c0u,0x6d0u,0x690u,0x99d0u,0x99d0u,0x740u,0x700u,0x99e0u,0x99e0u,0x7c0u,0x780u,0x99f0u,0x99f0u,0x9c00u,0x12000u,0x12040u,0x12000u,0x9c10u,0x12190u,0x121d0u,0x12010u,0x9c20u,0x12320u,0x12360u,0x12020u,0x9c30u,0x124b0u,0x124f0u,0x12030u,0x9c40u,0x12640u,0x12600u,0x12040u,0x9c50u,0x127d0u,0x12790u,0x12050u,0x9c60u,0x12960u,0x12920u,0x12060u,0x9c70u,0x12af0u,0x12ab0u,0x12070u,0x9c80u,0x12c00u,0x12c40u,0x12080u,0x9c90u,0x12d90u,0x12dd0u,0x12090u,0x9ca0u,0x12f20u,0x12f60u,0x120a0u,0x9cb0u,0x130b0u,0x130f0u,0x120b0u,0x9cc0u,0x13240u,0x13200u,0x120c0u,0x9cd0u,0x133d0u,0x13390u,0x120d0u,0x9ce0u,0x13560u,0x13520u,0x120e0u,0x9cf0u,0x136f0u,0x136b0u,0x120f0u,0x9f00u,0x12010u,0x12048u,0x12100u,0x9f10u,0x12180u,0x121d8u,0x12110u,0x9f20u,0x12330u,0x12368u,0x12120u,0x9f30u,0x124a0u,0x124f8u,0x12130u,0x9f40u,0x12650u,0x12608u,0x12140u,0x9f50u,0x127c0u,0x12798u,0x12150u,0x9f60u,0x12970u,0x12928u,0x12160u,0x9f70u,0x12ae0u,0x12ab8u,0x12170u,0x9f80u,0x12c10u,0x12c48u,0x12180u,0x9f90u,0x12d80u,0x12dd8u,0x12190u,0x9fa0u,0x12f30u,0x12f68u,0x121a0u,0x9fb0u,0x130a0u,0x130f8u,0x121b0u,0x9fc0u,0x13250u,0x13208u,0x121c0u,0x9fd0u,0x133c0u,0x13398u,0x121d0u,0x9fe0u,0x13570u,0x13528u,0x121e0u,0x9ff0u,0x136e0u,0x136b8u,0x121f0u,0xa200u,0x12020u,0x12050u,0x12200u,0xa210u,0x121b0u,0x121c0u,0x12210u,0xa220u,0x12300u,0x12370u,0x12220u,0xa230u,0x12490u,0x124e0u,0x12230u,0xa240u,0x12660u,0x12610u,0x12240u,0xa250u,0x127f0u,0x12780u,0x12250u,0xa260u,0x12940u,0x12930u,0x12260u,0xa270u,0x12ad0u,0x12aa0u,0x12270u,0xa280u,0x12c20u,0x12c50u,0x12280u,0xa290u,0x12db0u,0x12dc0u,0x12290u,0xa2a0u,0x12f00u,0x12f70u,0x122a0u,0xa2b0u,0x13090u,0x130e0u,0x122b0u,0xa2c0u,0x13260u,0x13210u,0x122c0u,0xa2d0u,0x133f0u,0x13380u,0x122d0u,0xa2e0u,0x13540u,0x13530u,0x122e0u,0xa2f0u,0x136d0u,0x136a0u,0x122f0u,0xa500u,0x12030u,0x12058u,0x12300u,0xa510u,0x121a0u,0x121c8u,0x12310u,0xa520u,0x12310u,0x12378u,0x12320u,0xa530u,0x12480u,0x124e8u,0x12330u,0xa540u,0x12670u,0x12618u,0x12340u,0xa550u,0x127e0u,0x12788u,0x12350u,0xa560u,0x12950u,0x12938u,0x12360u,0xa570u,0x12ac0u,0x12aa8u,0x12370u,0xa580u,0x12c30u,0x12c58u,0x12380u,0xa590u,0x12da0u,0x12dc8u,0x12390u,0xa5a0u,0x12f10u,0x12f78u,0x123a0u,0xa5b0u,0x13080u,0x130e8u,0x123b0u,0xa5c0u,0x13270u,0x13218u,0x123c0u,0xa5d0u,0x133e0u,0x13388u,0x123d0u,0xa5e0u,0x13550u,0x13538u,0x123e0u,0xa5f0u,0x136c0u,0x136a8u,0x123f0u,0x4800u,0x1c300u,0x1e800u,0x1eb00u,0x4880u,0x1c310u,0x100000u,0x1eb02u,0x4910u,0x1c320u,0x100000u,0x1eb04u,0x4990u,0x1c330u,0x100000u,0x1eb06u,0x4a20u,0x1c340u,0x1e800u,0x1eb08u,0x4aa0u,0x1c350u,0x100000u,0x1eb0au,0x4b30u,0x1c360u,0x100000u,0x1eb0cu,0x4bb0u,0x1c370u,0x100000u,0x1eb0eu,0x4c40u,0x1c380u,0x1e800u,0x1eb10u,0x4cc0u,0x1c390u,0x100000u,0x1eb12u,0x4d50u,0x1c3a0u,0x100000u,0x1eb14u,0x4dd0u,0x1c3b0u,0x100000u,0x1eb16u,0x4e60u,0x1c3c0u,0x1e800u,0x1eb18u,0x4ee0u,0x1c3d0u,0x100000u,0x1eb1au,0x4f70u,0x1c3e0u,0x100000u,0x1eb1cu,0x4ff0u,0x1c3f0u,0x100000u,0x1eb1eu,0x4810u,0x1c400u,0x1e830u,0x1eb00u,0x4890u,0x1c410u,0x100000u,0x1eb02u,0x4900u,0x1c420u,0x100000u,0x1eb04u,0x4980u,0x1c430u,0x100000u,0x1eb06u,0x4a30u,0x1c440u,0x1e830u,0x1eb08u,0x4ab0u,0x1c450u,0x100000u,0x1eb0au,0x4b20u,0x1c460u,0x100000u,0x1eb0cu,0x4ba0u,0x1c470u,0x100000u,0x1eb0eu,0x4c50u,0x1c480u,0x1e830u,0x1eb10u,0x4cd0u,0x1c490u,0x100000u,0x1eb12u,0x4d40u,0x1c4a0u,0x100000u,0x1eb14u,0x4dc0u,0x1c4b0u,0x100000u,0x1eb16u,0x4e70u,0x1c4c0u,0x1e830u,0x1eb18u,0x4ef0u,0x1c4d0u,0x100000u,0x1eb1au,0x4f60u,0x1c4e0u,0x100000u,0x1eb1cu,0x4fe0u,0x1c4f0u,0x100000u,0x1eb1eu,0x4820u,0x1c500u,0x1e860u,0x1eb00u,0x48a0u,0x1c510u,0x100000u,0x1eb02u,0x4930u,0x1c520u,0x100000u,0x1eb04u,0x49b0u,0x1c530u,0x100000u,0x1eb06u,0x4a00u,0x1c540u,0x1e860u,0x1eb08u,0x4a80u,0x1c550u,0x100000u,0x1eb0au,0x4b10u,0x1c560u,0x100000u,0x1eb0cu,0x4b90u,0x1c570u,0x100000u,0x1eb0eu,0x4c60u,0x1c580u,0x1e860u,0x1eb10u,0x4ce0u,0x1c590u,0x100000u,0x1eb12u,0x4d70u,0x1c5a0u,0x100000u,0x1eb14u,0x4df0u,0x1c5b0u,0x100000u,0x1eb16u,0x4e40u,0x1c5c0u,0x1e860u,0x1eb18u,0x4ec0u,0x1c5d0u,0x100000u,0x1eb1au,0x4f50u,0x1c5e0u,0x100000u,0x1eb1cu,0x4fd0u,0x1c5f0u,0x100000u,0x1eb1eu,0x4830u,0x1c600u,0x1e890u,0x1eb00u,0x48b0u,0x1c610u,0x100000u,0x1eb02u,0x4920u,0x1c620u,0x100000u,0x1eb04u,0x49a0u,0x1c630u,0x100000u,0x1eb06u,0x4a10u,0x1c640u,0x1e890u,0x1eb08u,0x4a90u,0x1c650u,0x100000u,0x1eb0au,0x4b00u,0x1c660u,0x100000u,0x1eb0cu,0x4b80u,0x1c670u,0x100000u,0x1eb0eu,0x4c70u,0x1c680u,0x1e890u,0x1eb10u,0x4cf0u,0x1c690u,0x100000u,0x1eb12u,0x4d60u,0x1c6a0u,0x100000u,0x1eb14u,0x4de0u,0x1c6b0u,0x100000u,0x1eb16u,0x4e50u,0x1c6c0u,0x1e890u,0x1eb18u,0x4ed0u,0x1c6d0u,0x100000u,0x1eb1au,0x4f40u,0x1c6e0u,0x100000u,0x1eb1cu,0x4fc0u,0x1c6f0u,0x100000u,0x1eb1eu,0x24800u,0x1d300u,0x1d300u,0xc0100u,0x24802u,0x1d302u,0x100000u,0xc0110u,0x24804u,0x1d304u,0x100000u,0xc0120u,0x24806u,0x1d306u,0x100000u,0xc0130u,0x24a00u,0x1d310u,0x1d300u,0xc0140u,0x24a02u,0x1d312u,0x100000u,0xc0150u,0x24a04u,0x1d314u,0x100000u,0xc0160u,0x24a06u,0x1d316u,0x100000u,0xc0170u,0x24c00u,0x1d320u,0x1d300u,0xc0280u,0x24c02u,0x1d322u,0x100000u,0xc0290u,0x24c04u,0x1d324u,0x100000u,0xc02a0u,0x24c06u,0x1d326u,0x100000u,0xc02b0u,0x24e00u,0x1d330u,0x1d300u,0xc02c0u,0x24e02u,0x1d332u,0x100000u,0xc02d0u,0x24e04u,0x1d334u,0x100000u,0xc02e0u,0x24e06u,0x1d336u,0x100000u,0xc02f0u,0x24808u,0x1d308u,0x1d310u,0xc0410u,0x2480au,0x1d30au,0x100000u,0xc0400u,0x2480cu,0x1d30cu,0x100000u,0xc0430u,0x2480eu,0x1d30eu,0x100000u,0xc0420u,0x24a08u,0x1d318u,0x1d310u,0xc0450u,0x24a0au,0x1d31au,0x100000u,0xc0440u,0x24a0cu,0x1d31cu,0x100000u,0xc0470u,0x24a0eu,0x1d31eu,0x100000u,0xc0460u,0x24c08u,0x1d328u,0x1d310u,0xef990u,0x24c0au,0x1d32au,0x100000u,0xef980u,0x24c0cu,0x1d32cu,0x100000u,0xef9b0u,0x24c0eu,0x1d32eu,0x100000u,0xef9a0u,0x24e08u,0x1d338u,0x1d310u,0xef9d0u,0x24e0au,0x1d33au,0x100000u,0xef9c0u,0x24e0cu,0x1d33cu,0x100000u,0xef9f0u,0x24e0eu,0x1d33eu,0x100000u,0xef9e0u,0x25000u,0x1d340u,0x1d320u,0xefb20u,0x25002u,0x1d342u,0x100000u,0xefb30u,0x25004u,0x1d344u,0x100000u,0xefb00u,0x25006u,0x1d346u,0x100000u,0xefb10u,0x25200u,0x1d350u,0x1d320u,0xefb60u,0x25202u,0x1d352u,0x100000u,0xefb70u,0x25204u,0x1d354u,0x100000u,0xefb40u,0x25206u,0x1d356u,0x100000u,0xefb50u,0x25400u,0x1d360u,0x1d320u,0xefca0u,0x25402u,0x1d362u,0x100000u,0xefcb0u,0x25404u,0x1d364u,0x100000u,0xefc80u,0x25406u,0x1d366u,0x100000u,0xefc90u,0x25600u,0x1d370u,0x1d320u,0xefce0u,0x25602u,0x1d372u,0x100000u,0xefcf0u,0x25604u,0x1d374u,0x100000u,0xefcc0u,0x25606u,0x1d376u,0x100000u,0xefcd0u,0x100000u,0x1d348u,0x1d330u,0xefe30u,0x100000u,0x1d34au,0x100000u,0xefe20u,0x100000u,0x1d34cu,0x100000u,0xefe10u,0x100000u,0x1d34eu,0x100000u,0xefe00u,0x100000u,0x1d358u,0x1d330u,0xefe70u,0x100000u,0x1d35au,0x100000u,0xefe60u,0x100000u,0x1d35cu,0x100000u,0xefe50u,0x100000u,0x1d35eu,0x100000u,0xefe40u,0x100000u,0x1d368u,0x1d330u,0xeffb0u,0x100000u,0x1d36au,0x100000u,0xeffa0u,0x100000u,0x1d36cu,0x100000u,0xeff90u,0x100000u,0x1d36eu,0x100000u,0xeff80u,0x100000u,0x1d378u,0x1d330u,0xefff0u,0x100000u,0x1d37au,0x100000u,0xeffe0u,0x100000u,0x1d37cu,0x100000u,0xeffd0u,0x100000u,0x1d37eu,0x100000u,0xeffc0u,0x0u,0x30420u,0x8fc50u,0xc0000u,0x10u,0x30430u,0x8fc40u,0xc0010u,0x20u,0x30400u,0x8fc70u,0xc0020u,0x30u,0x30410u,0x8fc60u,0xc0030u,0x40u,0x30460u,0x8fc10u,0xc0040u,0x50u,0x30470u,0x8fc00u,0xc0050u,0x60u,0x30440u,0x8fc30u,0xc0060u,0x70u,0x30450u,0x8fc20u,0xc0070u,0x80u,0x304b0u,0x8fcd0u,0xc0080u,0x90u,0x304a0u,0x8fcc0u,0xc0090u,0xa0u,0x30490u,0x8fcf0u,0xc00a0u,0xb0u,0x30480u,0x8fce0u,0xc00b0u,0xc0u,0x304f0u,0x8fc90u,0xc00c0u,0xd0u,0x304e0u,0x8fc80u,0xc00d0u,0xe0u,0x304d0u,0x8fcb0u,0xc00e0u,0xf0u,0x304c0u,0x8fca0u,0xc00f0u,0x100u,0x30530u,0x8fd60u,0xc0100u,0x110u,0x30520u,0x8fd70u,0xc0110u,0x120u,0x30510u,0x8fd40u,0xc0120u,0x130u,0x30500u,0x8fd50u,0xc0130u,0x140u,0x30570u,0x8fd20u,0xc0140u,0x150u,0x30560u,0x8fd30u,0xc0150u,0x160u,0x30550u,0x8fd00u,0xc0160u,0x170u,0x30540u,0x8fd10u,0xc0170u,0x190u,0x305b0u,0x8fde0u,0xc0190u,0x180u,0x305a0u,0x8fdf0u,0xc0180u,0x1b0u,0x30590u,0x8fdc0u,0xc01b0u,0x1a0u,0x30580u,0x8fdd0u,0xc01a0u,0x1d0u,0x305f0u,0x8fda0u,0xc01d0u,0x1c0u,0x305e0u,0x8fdb0u,0xc01c0u,0x1f0u,0x305d0u,0x8fd80u,0xc01f0u,0x1e0u,0x305c0u,0x8fd90u,0xc01e0u,0x210u,0x30640u,0x8fe60u,0xc0210u,0x200u,0x30650u,0x8fe70u,0xc0200u,0x230u,0x30660u,0x8fe40u,0xc0230u,0x220u,0x30670u,0x8fe50u,0xc0220u,0x250u,0x30600u,0x8fe20u,0xc0250u,0x240u,0x30610u,0x8fe30u,0xc0240u,0x270u,0x30620u,0x8fe00u,0xc0270u,0x260u,0x30630u,0x8fe10u,0xc0260u,0x290u,0x306c0u,0x8fef0u,0xc0290u,0x280u,0x306d0u,0x8fee0u,0xc0280u,0x2b0u,0x306e0u,0x8fed0u,0xc02b0u,0x2a0u,0x306f0u,0x8fec0u,0xc02a0u,0x2d0u,0x30680u,0x8feb0u,0xc02d0u,0x2c0u,0x30690u,0x8fea0u,0xc02c0u,0x2f0u,0x306a0u,0x8fe90u,0xc02f0u,0x2e0u,0x306b0u,0x8fe80u,0xc02e0u,0x320u,0x30740u,0x8ff70u,0xc0320u,0x330u,0x30750u,0x8ff60u,0xc0330u,0x300u,0x30760u,0x8ff50u,0xc0300u,0x310u,0x30770u,0x8ff40u,0xc0310u,0x360u,0x30700u,0x8ff30u,0xc0360u,0x370u,0x30710u,0x8ff20u,0xc0370u,0x340u,0x30720u,0x8ff10u,0xc0340u,0x350u,0x30730u,0x8ff00u,0xc0350u,0x3a0u,0x307d0u,0x8fff0u,0xc03a0u,0x3b0u,0x307c0u,0x8ffe0u,0xc03b0u,0x380u,0x307f0u,0x8ffd0u,0xc0380u,0x390u,0x307e0u,0x8ffc0u,0xc0390u,0x3e0u,0x30790u,0x8ffb0u,0xc03e0u,0x3f0u,0x30780u,0x8ffa0u,0xc03f0u,0x3c0u,0x307b0u,0x8ff90u,0xc03c0u,0x3d0u,0x307a0u,0x8ff80u,0xc03d0u,0x0u,0x11f820u,0x0u,0x7030cu,0x0u,0x11f830u,0x0u,0x7030cu,0x0u,0x11f800u,0x0u,0x7030cu,0x0u,0x11f810u,0x0u,0x7030cu,0x0u,0x11f860u,0x0u,0x7030cu,0x0u,0x11f870u,0x0u,0x7030cu,0x0u,0x11f840u,0x0u,0x7030cu,0x0u,0x11f850u,0x0u,0x7030cu,0x0u,0x11f8b0u,0x0u,0x7230cu,0x0u,0x11f8a0u,0x0u,0x7230cu,0x0u,0x11f890u,0x0u,0x7230cu,0x0u,0x11f880u,0x0u,0x7230cu,0x0u,0x11f8f0u,0x0u,0x7230cu,0x0u,0x11f8e0u,0x0u,0x7230cu,0x0u,0x11f8d0u,0x0u,0x7230cu,0x0u,0x11f8c0u,0x0u,0x7230cu,0x0u,0x11f930u,0x0u,0x7232cu,0x0u,0x11f920u,0x0u,0x7232cu,0x0u,0x11f910u,0x0u,0x7232cu,0x0u,0x11f900u,0x0u,0x7232cu,0x0u,0x11f970u,0x0u,0x7232cu,0x0u,0x11f960u,0x0u,0x7232cu,0x0u,0x11f950u,0x0u,0x7232cu,0x0u,0x11f940u,0x0u,0x7232cu,0x0u,0x11f9b0u,0x0u,0x72124u,0x0u,0x11f9a0u,0x0u,0x72124u,0x0u,0x11f990u,0x0u,0x72124u,0x0u,0x11f980u,0x0u,0x72124u,0x0u,0x11f9f0u,0x0u,0x72124u,0x0u,0x11f9e0u,0x0u,0x72124u,0x0u,0x11f9d0u,0x0u,0x72124u,0x0u,0x11f9c0u,0x0u,0x72124u,0x0u,0x11fa40u,0x0u,0x721a6u,0x0u,0x11fa50u,0x0u,0x721a6u,0x0u,0x11fa60u,0x0u,0x721a6u,0x0u,0x11fa70u,0x0u,0x721a6u,0x0u,0x11fa00u,0x0u,0x721a6u,0x0u,0x11fa10u,0x0u,0x721a6u,0x0u,0x11fa20u,0x0u,0x721a6u,0x0u,0x11fa30u,0x0u,0x721a6u,0x0u,0x11fac0u,0x0u,0x72186u,0x0u,0x11fad0u,0x0u,0x72186u,0x0u,0x11fae0u,0x0u,0x72186u,0x0u,0x11faf0u,0x0u,0x72186u,0x0u,0x11fa80u,0x0u,0x72186u,0x0u,0x11fa90u,0x0u,0x72186u,0x0u,0x11faa0u,0x0u,0x72186u,0x0u,0x11fab0u,0x0u,0x72186u,0x0u,0x11fb40u,0x0u,0x72186u,0x0u,0x11fb50u,0x0u,0x72186u,0x0u,0x11fb60u,0x0u,0x72186u,0x0u,0x11fb70u,0x0u,0x72186u,0x0u,0x11fb00u,0x0u,0x72186u,0x0u,0x11fb10u,0x0u,0x72186u,0x0u,0x11fb20u,0x0u,0x72186u,0x0u,0x11fb30u,0x0u,0x72186u,0x0u,0x11fbd0u,0x0u,0x7230cu,0x0u,0x11fbc0u,0x0u,0x7230cu,0x0u,0x11fbf0u,0x0u,0x7230cu,0x0u,0x11fbe0u,0x0u,0x7230cu,0x0u,0x11fb90u,0x0u,0x7230cu,0x0u,0x11fb80u,0x0u,0x7230cu,0x0u,0x11fbb0u,0x0u,0x7230cu,0x0u,0x11fba0u,0x0u,0x7230cu,0x5800u,0x5820u,0x4860u,0x4860u,0x5808u,0x5828u,0x4868u,0x4870u,0x5810u,0x5830u,0x4870u,0x5800u,0x5818u,0x5838u,0x4878u,0x5810u,0x5880u,0x58a0u,0x48e0u,0x48e0u,0x5888u,0x58a8u,0x48e8u,0x48f0u,0x5890u,0x58b0u,0x48f0u,0x5880u,0x5898u,0x58b8u,0x48f8u,0x5890u,0x5920u,0x5900u,0x4940u,0x4940u,0x5928u,0x5908u,0x4948u,0x4950u,0x5930u,0x5910u,0x4950u,0x5920u,0x5938u,0x5918u,0x4958u,0x5930u,0x59a0u,0x5980u,0x49c0u,0x49c0u,0x59a8u,0x5988u,0x49c8u,0x49d0u,0x59b0u,0x5990u,0x49d0u,0x59a0u,0x59b8u,0x5998u,0x49d8u,0x59b0u,0x5c40u,0x5c60u,0x4c20u,0x4a60u,0x5c48u,0x5c68u,0x4c28u,0x4a70u,0x5c50u,0x5c70u,0x4c30u,0x5a00u,0x5c58u,0x5c78u,0x4c38u,0x5a10u,0x5cc0u,0x5ce0u,0x4ca0u,0x4ae0u,0x5cc8u,0x5ce8u,0x4ca8u,0x4af0u,0x5cd0u,0x5cf0u,0x4cb0u,0x5a80u,0x5cd8u,0x5cf8u,0x4cb8u,0x5a90u,0x5d60u,0x5d40u,0x4d00u,0x4b40u,0x5d68u,0x5d48u,0x4d08u,0x4b50u,0x5d70u,0x5d50u,0x4d10u,0x5b20u,0x5d78u,0x5d58u,0x4d18u,0x5b30u,0x5de0u,0x5dc0u,0x4d80u,0x4bc0u,0x5de8u,0x5dc8u,0x4d88u,0x4bd0u,0x5df0u,0x5dd0u,0x4d90u,0x5ba0u,0x5df8u,0x5dd8u,0x4d98u,0x5bb0u,0x6000u,0x6020u,0x5060u,0x4c20u,0x6008u,0x6028u,0x5068u,0x4c30u,0x6010u,0x6030u,0x5070u,0x5c40u,0x6018u,0x6038u,0x5078u,0x5c50u,0x6080u,0x60a0u,0x50e0u,0x4ca0u,0x6088u,0x60a8u,0x50e8u,0x4cb0u,0x6090u,0x60b0u,0x50f0u,0x5cc0u,0x6098u,0x60b8u,0x50f8u,0x5cd0u,0x6120u,0x6100u,0x5140u,0x4d00u,0x6128u,0x6108u,0x5148u,0x4d10u,0x6130u,0x6110u,0x5150u,0x5d60u,0x6138u,0x6118u,0x5158u,0x5d70u,0x61a0u,0x6180u,0x51c0u,0x4d80u,0x61a8u,0x6188u,0x51c8u,0x4d90u,0x61b0u,0x6190u,0x51d0u,0x5de0u,0x61b8u,0x6198u,0x51d8u,0x5df0u,0x6440u,0x6460u,0x5420u,0x4e20u,0x6448u,0x6468u,0x5428u,0x4e30u,0x6450u,0x6470u,0x5430u,0x5e40u,0x6458u,0x6478u,0x5438u,0x5e50u,0x64c0u,0x64e0u,0x54a0u,0x4ea0u,0x64c8u,0x64e8u,0x54a8u,0x4eb0u,0x64d0u,0x64f0u,0x54b0u,0x5ec0u,0x64d8u,0x64f8u,0x54b8u,0x5ed0u,0x6560u,0x6540u,0x5500u,0x4f00u,0x6568u,0x6548u,0x5508u,0x4f10u,0x6570u,0x6550u,0x5510u,0x5f60u,0x6578u,0x6558u,0x5518u,0x5f70u,0x65e0u,0x65c0u,0x5580u,0x4f80u,0x65e8u,0x65c8u,0x5588u,0x4f90u,0x65f0u,0x65d0u,0x5590u,0x5fe0u,0x65f8u,0x65d8u,0x5598u,0x5ff0u,0x5820u,0x1e8c0u,0x1eb60u,0xc40u,0x5828u,0x1ed50u,0x1eff0u,0xc50u,0x5830u,0x1f1e0u,0x1f480u,0xc60u,0x5838u,0x1f670u,0x1f910u,0xc70u,0x58a0u,0x1fb00u,0x1fda0u,0xc00u,0x58a8u,0x1ff90u,0x20230u,0xc10u,0x58b0u,0x20420u,0x206c0u,0xc20u,0x58b8u,0x208b0u,0x20b50u,0xc30u,0x5900u,0x20d40u,0x20fe0u,0xdc0u,0x5908u,0x211d0u,0x21470u,0xdd0u,0x5910u,0x21660u,0x21900u,0xde0u,0x5918u,0x21af0u,0x21d90u,0xdf0u,0x5980u,0x21f80u,0x22220u,0xd80u,0x5988u,0x22410u,0x226b0u,0xd90u,0x5990u,0x228a0u,0x22b40u,0xda0u,0x5998u,0x22d30u,0x22fd0u,0xdb0u,0x5a20u,0x1e8f0u,0x1eb68u,0xf60u,0x5a28u,0x1ed80u,0x1eff8u,0xf70u,0x5a30u,0x1f210u,0x1f488u,0xf40u,0x5a38u,0x1f6a0u,0x1f918u,0xf50u,0x5aa0u,0x1fb30u,0x1fda8u,0xf20u,0x5aa8u,0x1ffc0u,0x20238u,0xf30u,0x5ab0u,0x20450u,0x206c8u,0xf00u,0x5ab8u,0x208e0u,0x20b58u,0xf10u,0x5b00u,0x20d70u,0x20fe8u,0x10e0u,0x5b08u,0x21200u,0x21478u,0x10f0u,0x5b10u,0x21690u,0x21908u,0x10c0u,0x5b18u,0x21b20u,0x21d98u,0x10d0u,0x5b80u,0x21fb0u,0x22228u,0x10a0u,0x5b88u,0x22440u,0x226b8u,0x10b0u,0x5b90u,0x228d0u,0x22b48u,0x1080u,0x5b98u,0x22d60u,0x22fd8u,0x1090u,0x5c60u,0x1e920u,0x1eb70u,0x1240u,0x5c68u,0x1edb0u,0x1f000u,0x1250u,0x5c70u,0x1f240u,0x1f490u,0x1260u,0x5c78u,0x1f6d0u,0x1f920u,0x1270u,0x5ce0u,0x1fb60u,0x1fdb0u,0x1200u,0x5ce8u,0x1fff0u,0x20240u,0x1210u,0x5cf0u,0x20480u,0x206d0u,0x1220u,0x5cf8u,0x20910u,0x20b60u,0x1230u,0x5d40u,0x20da0u,0x20ff0u,0x13c0u,0x5d48u,0x21230u,0x21480u,0x13d0u,0x5d50u,0x216c0u,0x21910u,0x13e0u,0x5d58u,0x21b50u,0x21da0u,0x13f0u,0x5dc0u,0x21fe0u,0x22230u,0x1380u,0x5dc8u,0x22470u,0x226c0u,0x1390u,0x5dd0u,0x22900u,0x22b50u,0x13a0u,0x5dd8u,0x22d90u,0x22fe0u,0x13b0u,0x5e60u,0x1e950u,0x1eb78u,0x1560u,0x5e68u,0x1ede0u,0x1f008u,0x1570u,0x5e70u,0x1f270u,0x1f498u,0x1540u,0x5e78u,0x1f700u,0x1f928u,0x1550u,0x5ee0u,0x1fb90u,0x1fdb8u,0x1520u,0x5ee8u,0x20020u,0x20248u,0x1530u,0x5ef0u,0x204b0u,0x206d8u,0x1500u,0x5ef8u,0x20940u,0x20b68u,0x1510u,0x5f40u,0x20dd0u,0x20ff8u,0x16e0u,0x5f48u,0x21260u,0x21488u,0x16f0u,0x5f50u,0x216f0u,0x21918u,0x16c0u,0x5f58u,0x21b80u,0x21da8u,0x16d0u,0x5fc0u,0x22010u,0x22238u,0x16a0u,0x5fc8u,0x224a0u,0x226c8u,0x16b0u,0x5fd0u,0x22930u,0x22b58u,0x1680u,0x5fd8u,0x22dc0u,0x22fe8u,0x1690u,0x0u,0x40u,0x9000u,0xa800u,0x80u,0xc0u,0x9010u,0xa810u,0x110u,0x150u,0x9020u,0xa820u,0x190u,0x1d0u,0x9030u,0xa830u,0x220u,0x260u,0x9040u,0xa840u,0x2a0u,0x2e0u,0x9050u,0xa850u,0x330u,0x370u,0x9060u,0xa860u,0x3b0u,0x3f0u,0x9070u,0xa870u,0x440u,0x400u,0x9080u,0xa880u,0x4c0u,0x480u,0x9090u,0xa890u,0x550u,0x510u,0x90a0u,0xa8a0u,0x5d0u,0x590u,0x90b0u,0xa8b0u,0x660u,0x620u,0x90c0u,0xa8c0u,0x6e0u,0x6a0u,0x90d0u,0xa8d0u,0x770u,0x730u,0x90e0u,0xa8e0u,0x7f0u,0x7b0u,0x90f0u,0xa8f0u,0x10u,0x50u,0x9300u,0xab00u,0x90u,0xd0u,0x9310u,0xab10u,0x100u,0x140u,0x9320u,0xab20u,0x180u,0x1c0u,0x9330u,0xab30u,0x230u,0x270u,0x9340u,0xab40u,0x2b0u,0x2f0u,0x9350u,0xab50u,0x320u,0x360u,0x9360u,0xab60u,0x3a0u,0x3e0u,0x9370u,0xab70u,0x450u,0x410u,0x9380u,0xab80u,0x4d0u,0x490u,0x9390u,0xab90u,0x540u,0x500u,0x93a0u,0xaba0u,0x5c0u,0x580u,0x93b0u,0xabb0u,0x670u,0x630u,0x93c0u,0xabc0u,0x6f0u,0x6b0u,0x93d0u,0xabd0u,0x760u,0x720u,0x93e0u,0xabe0u,0x7e0u,0x7a0u,0x93f0u,0xabf0u,0x20u,0x60u,0x9600u,0xae00u,0xa0u,0xe0u,0x9610u,0xae10u,0x130u,0x170u,0x9620u,0xae20u,0x1b0u,0x1f0u,0x9630u,0xae30u,0x200u,0x240u,0x9640u,0xae40u,0x280u,0x2c0u,0x9650u,0xae50u,0x310u,0x350u,0x9660u,0xae60u,0x390u,0x3d0u,0x9670u,0xae70u,0x460u,0x420u,0x9680u,0xae80u,0x4e0u,0x4a0u,0x9690u,0xae90u,0x570u,0x530u,0x96a0u,0xaea0u,0x5f0u,0x5b0u,0x96b0u,0xaeb0u,0x640u,0x600u,0x96c0u,0xaec0u,0x6c0u,0x680u,0x96d0u,0xaed0u,0x750u,0x710u,0x96e0u,0xaee0u,0x7d0u,0x790u,0x96f0u,0xaef0u,0x30u,0x70u,0x9900u,0xb100u,0xb0u,0xf0u,0x9910u,0xb110u,0x120u,0x160u,0x9920u,0xb120u,0x1a0u,0x1e0u,0x9930u,0xb130u,0x210u,0x250u,0x9940u,0xb140u,0x290u,0x2d0u,0x9950u,0xb150u,0x300u,0x340u,0x9960u,0xb160u,0x380u,0x3c0u,0x9970u,0xb170u,0x470u,0x430u,0x9980u,0xb180u,0x4f0u,0x4b0u,0x9990u,0xb190u,0x560u,0x520u,0x99a0u,0xb1a0u,0x5e0u,0x5a0u,0x99b0u,0xb1b0u,0x650u,0x610u,0x99c0u,0xb1c0u,0x6d0u,0x690u,0x99d0u,0xb1d0u,0x740u,0x700u,0x99e0u,0xb1e0u,0x7c0u,0x780u,0x99f0u,0xb1f0u,0x9c08u,0x12060u,0x120a0u,0x12400u,0x9c18u,0x121f0u,0x12230u,0x12410u,0x9c28u,0x12340u,0x12380u,0x12420u,0x9c38u,0x124d0u,0x12510u,0x12430u,0x9c48u,0x12620u,0x126e0u,0x12440u,0x9c58u,0x127b0u,0x12870u,0x12450u,0x9c68u,0x12900u,0x129c0u,0x12460u,0x9c78u,0x12a90u,0x12b50u,0x12470u,0x9c88u,0x12c60u,0x12ca0u,0x12480u,0x9c98u,0x12df0u,0x12e30u,0x12490u,0x9ca8u,0x12f40u,0x12f80u,0x124a0u,0x9cb8u,0x130d0u,0x13110u,0x124b0u,0x9cc8u,0x13220u,0x132e0u,0x124c0u,0x9cd8u,0x133b0u,0x13470u,0x124d0u,0x9ce8u,0x13500u,0x135c0u,0x124e0u,0x9cf8u,0x13690u,0x13750u,0x124f0u,0x9f08u,0x12070u,0x120a8u,0x12500u,0x9f18u,0x121e0u,0x12238u,0x12510u,0x9f28u,0x12350u,0x12388u,0x12520u,0x9f38u,0x124c0u,0x12518u,0x12530u,0x9f48u,0x12630u,0x126e8u,0x12540u,0x9f58u,0x127a0u,0x12878u,0x12550u,0x9f68u,0x12910u,0x129c8u,0x12560u,0x9f78u,0x12a80u,0x12b58u,0x12570u,0x9f88u,0x12c70u,0x12ca8u,0x12580u,0x9f98u,0x12de0u,0x12e38u,0x12590u,0x9fa8u,0x12f50u,0x12f88u,0x125a0u,0x9fb8u,0x130c0u,0x13118u,0x125b0u,0x9fc8u,0x13230u,0x132e8u,0x125c0u,0x9fd8u,0x133a0u,0x13478u,0x125d0u,0x9fe8u,0x13510u,0x135c8u,0x125e0u,0x9ff8u,0x13680u,0x13758u,0x125f0u,0xa208u,0x12080u,0x120b0u,0x12600u,0xa218u,0x12210u,0x12220u,0x12610u,0xa228u,0x123a0u,0x12390u,0x12620u,0xa238u,0x12530u,0x12500u,0x12630u,0xa248u,0x126c0u,0x126f0u,0x12640u,0xa258u,0x12850u,0x12860u,0x12650u,0xa268u,0x129e0u,0x129d0u,0x12660u,0xa278u,0x12b70u,0x12b40u,0x12670u,0xa288u,0x12c80u,0x12cb0u,0x12680u,0xa298u,0x12e10u,0x12e20u,0x12690u,0xa2a8u,0x12fa0u,0x12f90u,0x126a0u,0xa2b8u,0x13130u,0x13100u,0x126b0u,0xa2c8u,0x132c0u,0x132f0u,0x126c0u,0xa2d8u,0x13450u,0x13460u,0x126d0u,0xa2e8u,0x135e0u,0x135d0u,0x126e0u,0xa2f8u,0x13770u,0x13740u,0x126f0u,0xa508u,0x12090u,0x120b8u,0x12700u,0xa518u,0x12200u,0x12228u,0x12710u,0xa528u,0x123b0u,0x12398u,0x12720u,0xa538u,0x12520u,0x12508u,0x12730u,0xa548u,0x126d0u,0x126f8u,0x12740u,0xa558u,0x12840u,0x12868u,0x12750u,0xa568u,0x129f0u,0x129d8u,0x12760u,0xa578u,0x12b60u,0x12b48u,0x12770u,0xa588u,0x12c90u,0x12cb8u,0x12780u,0xa598u,0x12e00u,0x12e28u,0x12790u,0xa5a8u,0x12fb0u,0x12f98u,0x127a0u,0xa5b8u,0x13120u,0x13108u,0x127b0u,0xa5c8u,0x132d0u,0x132f8u,0x127c0u,0xa5d8u,0x13440u,0x13468u,0x127d0u,0xa5e8u,0x135f0u,0x135d8u,0x127e0u,0xa5f8u,0x13760u,0x13748u,0x127f0u,0x5000u,0x1c300u,0x1e8c0u,0x1eb60u,0x5080u,0x1c310u,0x100000u,0x1eb62u,0x5110u,0x1c320u,0x100000u,0x1eb64u,0x5190u,0x1c330u,0x100000u,0x1eb66u,0x5220u,0x1c340u,0x1e8c0u,0x1eb68u,0x52a0u,0x1c350u,0x100000u,0x1eb6au,0x5330u,0x1c360u,0x100000u,0x1eb6cu,0x53b0u,0x1c370u,0x100000u,0x1eb6eu,0x5440u,0x1c380u,0x1e8c0u,0x1eb70u,0x54c0u,0x1c390u,0x100000u,0x1eb72u,0x5550u,0x1c3a0u,0x100000u,0x1eb74u,0x55d0u,0x1c3b0u,0x100000u,0x1eb76u,0x5660u,0x1c3c0u,0x1e8c0u,0x1eb78u,0x56e0u,0x1c3d0u,0x100000u,0x1eb7au,0x5770u,0x1c3e0u,0x100000u,0x1eb7cu,0x57f0u,0x1c3f0u,0x100000u,0x1eb7eu,0x5010u,0x1c400u,0x1e8f0u,0x1eb60u,0x5090u,0x1c410u,0x100000u,0x1eb62u,0x5100u,0x1c420u,0x100000u,0x1eb64u,0x5180u,0x1c430u,0x100000u,0x1eb66u,0x5230u,0x1c440u,0x1e8f0u,0x1eb68u,0x52b0u,0x1c450u,0x100000u,0x1eb6au,0x5320u,0x1c460u,0x100000u,0x1eb6cu,0x53a0u,0x1c470u,0x100000u,0x1eb6eu,0x5450u,0x1c480u,0x1e8f0u,0x1eb70u,0x54d0u,0x1c490u,0x100000u,0x1eb72u,0x5540u,0x1c4a0u,0x100000u,0x1eb74u,0x55c0u,0x1c4b0u,0x100000u,0x1eb76u,0x5670u,0x1c4c0u,0x1e8f0u,0x1eb78u,0x56f0u,0x1c4d0u,0x100000u,0x1eb7au,0x5760u,0x1c4e0u,0x100000u,0x1eb7cu,0x57e0u,0x1c4f0u,0x100000u,0x1eb7eu,0x5020u,0x1c500u,0x1e920u,0x1eb60u,0x50a0u,0x1c510u,0x100000u,0x1eb62u,0x5130u,0x1c520u,0x100000u,0x1eb64u,0x51b0u,0x1c530u,0x100000u,0x1eb66u,0x5200u,0x1c540u,0x1e920u,0x1eb68u,0x5280u,0x1c550u,0x100000u,0x1eb6au,0x5310u,0x1c560u,0x100000u,0x1eb6cu,0x5390u,0x1c570u,0x100000u,0x1eb6eu,0x5460u,0x1c580u,0x1e920u,0x1eb70u,0x54e0u,0x1c590u,0x100000u,0x1eb72u,0x5570u,0x1c5a0u,0x100000u,0x1eb74u,0x55f0u,0x1c5b0u,0x100000u,0x1eb76u,0x5640u,0x1c5c0u,0x1e920u,0x1eb78u,0x56c0u,0x1c5d0u,0x100000u,0x1eb7au,0x5750u,0x1c5e0u,0x100000u,0x1eb7cu,0x57d0u,0x1c5f0u,0x100000u,0x1eb7eu,0x5030u,0x1c600u,0x1e950u,0x1eb60u,0x50b0u,0x1c610u,0x100000u,0x1eb62u,0x5120u,0x1c620u,0x100000u,0x1eb64u,0x51a0u,0x1c630u,0x100000u,0x1eb66u,0x5210u,0x1c640u,0x1e950u,0x1eb68u,0x5290u,0x1c650u,0x100000u,0x1eb6au,0x5300u,0x1c660u,0x100000u,0x1eb6cu,0x5380u,0x1c670u,0x100000u,0x1eb6eu,0x5470u,0x1c680u,0x1e950u,0x1eb70u,0x54f0u,0x1c690u,0x100000u,0x1eb72u,0x5560u,0x1c6a0u,0x100000u,0x1eb74u,0x55e0u,0x1c6b0u,0x100000u,0x1eb76u,0x5650u,0x1c6c0u,0x1e950u,0x1eb78u,0x56d0u,0x1c6d0u,0x100000u,0x1eb7au,0x5740u,0x1c6e0u,0x100000u,0x1eb7cu,0x57c0u,0x1c6f0u,0x100000u,0x1eb7eu,0x25800u,0x1d380u,0x1d380u,0xf0140u,0x25802u,0x1d382u,0x100000u,0xf0150u,0x25804u,0x1d384u,0x100000u,0xf0160u,0x25806u,0x1d386u,0x100000u,0xf0170u,0x25a00u,0x1d390u,0x1d380u,0xf0100u,0x25a02u,0x1d392u,0x100000u,0xf0110u,0x25a04u,0x1d394u,0x100000u,0xf0120u,0x25a06u,0x1d396u,0x100000u,0xf0130u,0x25c00u,0x1d3a0u,0x1d380u,0xf02c0u,0x25c02u,0x1d3a2u,0x100000u,0xf02d0u,0x25c04u,0x1d3a4u,0x100000u,0xf02e0u,0x25c06u,0x1d3a6u,0x100000u,0xf02f0u,0x25e00u,0x1d3b0u,0x1d380u,0xf0280u,0x25e02u,0x1d3b2u,0x100000u,0xf0290u,0x25e04u,0x1d3b4u,0x100000u,0xf02a0u,0x25e06u,0x1d3b6u,0x100000u,0xf02b0u,0x25808u,0x1d388u,0x1d390u,0x11f850u,0x2580au,0x1d38au,0x100000u,0x11f840u,0x2580cu,0x1d38cu,0x100000u,0x11f870u,0x2580eu,0x1d38eu,0x100000u,0x11f860u,0x25a08u,0x1d398u,0x1d390u,0x11f810u,0x25a0au,0x1d39au,0x100000u,0x11f800u,0x25a0cu,0x1d39cu,0x100000u,0x11f830u,0x25a0eu,0x1d39eu,0x100000u,0x11f820u,0x25c08u,0x1d3a8u,0x1d390u,0x11f9d0u,0x25c0au,0x1d3aau,0x100000u,0x11f9c0u,0x25c0cu,0x1d3acu,0x100000u,0x11f9f0u,0x25c0eu,0x1d3aeu,0x100000u,0x11f9e0u,0x25e08u,0x1d3b8u,0x1d390u,0x11f990u,0x25e0au,0x1d3bau,0x100000u,0x11f980u,0x25e0cu,0x1d3bcu,0x100000u,0x11f9b0u,0x25e0eu,0x1d3beu,0x100000u,0x11f9a0u,0x25008u,0x1d3c0u,0x1d3a0u,0x11fb60u,0x2500au,0x1d3c2u,0x100000u,0x11fb70u,0x2500cu,0x1d3c4u,0x100000u,0x11fb40u,0x2500eu,0x1d3c6u,0x100000u,0x11fb50u,0x25208u,0x1d3d0u,0x1d3a0u,0x11fb20u,0x2520au,0x1d3d2u,0x100000u,0x11fb30u,0x2520cu,0x1d3d4u,0x100000u,0x11fb00u,0x2520eu,0x1d3d6u,0x100000u,0x11fb10u,0x25408u,0x1d3e0u,0x1d3a0u,0x11fce0u,0x2540au,0x1d3e2u,0x100000u,0x11fcf0u,0x2540cu,0x1d3e4u,0x100000u,0x11fcc0u,0x2540eu,0x1d3e6u,0x100000u,0x11fcd0u,0x25608u,0x1d3f0u,0x1d3a0u,0x11fca0u,0x2560au,0x1d3f2u,0x100000u,0x11fcb0u,0x2560cu,0x1d3f4u,0x100000u,0x11fc80u,0x2560eu,0x1d3f6u,0x100000u,0x11fc90u,0x100000u,0x1d3c8u,0x1d3b0u,0x11fe70u,0x100000u,0x1d3cau,0x100000u,0x11fe60u,0x100000u,0x1d3ccu,0x100000u,0x11fe50u,0x100000u,0x1d3ceu,0x100000u,0x11fe40u,0x100000u,0x1d3d8u,0x1d3b0u,0x11fe30u,0x100000u,0x1d3dau,0x100000u,0x11fe20u,0x100000u,0x1d3dcu,0x100000u,0x11fe10u,0x100000u,0x1d3deu,0x100000u,0x11fe00u,0x100000u,0x1d3e8u,0x1d3b0u,0x11fff0u,0x100000u,0x1d3eau,0x100000u,0x11ffe0u,0x100000u,0x1d3ecu,0x100000u,0x11ffd0u,0x100000u,0x1d3eeu,0x100000u,0x11ffc0u,0x100000u,0x1d3f8u,0x1d3b0u,0x11ffb0u,0x100000u,0x1d3fau,0x100000u,0x11ffa0u,0x100000u,0x1d3fcu,0x100000u,0x11ff90u,0x100000u,0x1d3feu,0x100000u,0x11ff80u,0x420u,0x30850u,0x90000u,0xc0420u,0x430u,0x30840u,0x90010u,0xc0430u,0x400u,0x30870u,0x90020u,0xc0400u,0x410u,0x30860u,0x90030u,0xc0410u,0x460u,0x30810u,0x90040u,0xc0460u,0x470u,0x30800u,0x90050u,0xc0470u,0x440u,0x30830u,0x90060u,0xc0440u,0x450u,0x30820u,0x90070u,0xc0450u,0x4b0u,0x308d0u,0x90080u,0xef8b0u,0x4a0u,0x308c0u,0x90090u,0xef8a0u,0x490u,0x308f0u,0x900a0u,0xef890u,0x480u,0x308e0u,0x900b0u,0xef880u,0x4f0u,0x30890u,0x900c0u,0xef8f0u,0x4e0u,0x30880u,0x900d0u,0xef8e0u,0x4d0u,0x308b0u,0x900e0u,0xef8d0u,0x4c0u,0x308a0u,0x900f0u,0xef8c0u,0x530u,0x5fd60u,0x90100u,0xef930u,0x520u,0x5fd70u,0x90110u,0xef920u,0x510u,0x5fd40u,0x90120u,0xef910u,0x500u,0x5fd50u,0x90130u,0xef900u,0x570u,0x5fd20u,0x90140u,0xef970u,0x560u,0x5fd30u,0x90150u,0xef960u,0x550u,0x5fd00u,0x90160u,0xef950u,0x540u,0x5fd10u,0x90170u,0xef940u,0x5b0u,0x5fde0u,0x90190u,0xef9b0u,0x5a0u,0x5fdf0u,0x90180u,0xef9a0u,0x590u,0x5fdc0u,0x901b0u,0xef990u,0x580u,0x5fdd0u,0x901a0u,0xef980u,0x5f0u,0x5fda0u,0x901d0u,0xef9f0u,0x5e0u,0x5fdb0u,0x901c0u,0xef9e0u,0x5d0u,0x5fd80u,0x901f0u,0xef9d0u,0x5c0u,0x5fd90u,0x901e0u,0xef9c0u,0x640u,0x5fe60u,0x90210u,0xefa40u,0x650u,0x5fe70u,0x90200u,0xefa50u,0x660u,0x5fe40u,0x90230u,0xefa60u,0x670u,0x5fe50u,0x90220u,0xefa70u,0x600u,0x5fe20u,0x90250u,0xefa00u,0x610u,0x5fe30u,0x90240u,0xefa10u,0x620u,0x5fe00u,0x90270u,0xefa20u,0x630u,0x5fe10u,0x90260u,0xefa30u,0x6c0u,0x5fef0u,0x90290u,0xefac0u,0x6d0u,0x5fee0u,0x90280u,0xefad0u,0x6e0u,0x5fed0u,0x902b0u,0xefae0u,0x6f0u,0x5fec0u,0x902a0u,0xefaf0u,0x680u,0x5feb0u,0x902d0u,0xefa80u,0x690u,0x5fea0u,0x902c0u,0xefa90u,0x6a0u,0x5fe90u,0x902f0u,0xefaa0u,0x6b0u,0x5fe80u,0x902e0u,0xefab0u,0x740u,0x5ff70u,0x90320u,0xefb40u,0x750u,0x5ff60u,0x90330u,0xefb50u,0x760u,0x5ff50u,0x90300u,0xefb60u,0x770u,0x5ff40u,0x90310u,0xefb70u,0x700u,0x5ff30u,0x90360u,0xefb00u,0x710u,0x5ff20u,0x90370u,0xefb10u,0x720u,0x5ff10u,0x90340u,0xefb20u,0x730u,0x5ff00u,0x90350u,0xefb30u,0x7d0u,0x5fff0u,0x903a0u,0xefbd0u,0x7c0u,0x5ffe0u,0x903b0u,0xefbc0u,0x7f0u,0x5ffd0u,0x90380u,0xefbf0u,0x7e0u,0x5ffc0u,0x90390u,0xefbe0u,0x790u,0x5ffb0u,0x903e0u,0xefb90u,0x780u,0x5ffa0u,0x903f0u,0xefb80u,0x7b0u,0x5ff90u,0x903c0u,0xefbb0u,0x7a0u,0x5ff80u,0x903d0u,0xefba0u,0x0u,0x11fc50u,0x0u,0x7230cu,0x0u,0x11fc40u,0x0u,0x7230cu,0x0u,0x11fc70u,0x0u,0x7230cu,0x0u,0x11fc60u,0x0u,0x7230cu,0x0u,0x11fc10u,0x0u,0x7230cu,0x0u,0x11fc00u,0x0u,0x7230cu,0x0u,0x11fc30u,0x0u,0x7230cu,0x0u,0x11fc20u,0x0u,0x7230cu,0x0u,0x11fcd0u,0x0u,0x5232cu,0x0u,0x11fcc0u,0x0u,0x5232cu,0x0u,0x11fcf0u,0x0u,0x5232cu,0x0u,0x11fce0u,0x0u,0x5232cu,0x0u,0x11fc90u,0x0u,0x5232cu,0x0u,0x11fc80u,0x0u,0x5232cu,0x0u,0x11fcb0u,0x0u,0x5232cu,0x0u,0x11fca0u,0x0u,0x5232cu,0x0u,0x11fd60u,0x0u,0xd0124u,0x0u,0x11fd70u,0x0u,0xd0124u,0x0u,0x11fd40u,0x0u,0xd0124u,0x0u,0x11fd50u,0x0u,0xd0124u,0x0u,0x11fd20u,0x0u,0xd0124u,0x0u,0x11fd30u,0x0u,0xd0124u,0x0u,0x11fd00u,0x0u,0xd0124u,0x0u,0x11fd10u,0x0u,0xd0124u,0x0u,0x11fde0u,0x0u,0xd81a6u,0x0u,0x11fdf0u,0x0u,0xd81a6u,0x0u,0x11fdc0u,0x0u,0xd81a6u,0x0u,0x11fdd0u,0x0u,0xd81a6u,0x0u,0x11fda0u,0x0u,0xd81a6u,0x0u,0x11fdb0u,0x0u,0xd81a6u,0x0u,0x11fd80u,0x0u,0xd81a6u,0x0u,0x11fd90u,0x0u,0xd81a6u,0x0u,0x11fe60u,0x0u,0xd8996u,0x0u,0x11fe70u,0x0u,0xd8996u,0x0u,0x11fe40u,0x0u,0xd8996u,0x0u,0x11fe50u,0x0u,0xd8996u,0x0u,0x11fe20u,0x0u,0xd8996u,0x0u,0x11fe30u,0x0u,0xd8996u,0x0u,0x11fe00u,0x0u,0xd8996u,0x0u,0x11fe10u,0x0u,0xd8996u,0x0u,0x11fef0u,0x0u,0xd8996u,0x0u,0x11fee0u,0x0u,0xd8996u,0x0u,0x11fed0u,0x0u,0xd8996u,0x0u,0x11fec0u,0x0u,0xd8996u,0x0u,0x11feb0u,0x0u,0xd8996u,0x0u,0x11fea0u,0x0u,0xd8996u,0x0u,0x11fe90u,0x0u,0xd8996u,0x0u,0x11fe80u,0x0u,0xd8996u,0x0u,0x11ff70u,0x0u,0xd8b1cu,0x0u,0x11ff60u,0x0u,0xd8b1cu,0x0u,0x11ff50u,0x0u,0xd8b1cu,0x0u,0x11ff40u,0x0u,0xd8b1cu,0x0u,0x11ff30u,0x0u,0xd8b1cu,0x0u,0x11ff20u,0x0u,0xd8b1cu,0x0u,0x11ff10u,0x0u,0xd8b1cu,0x0u,0x11ff00u,0x0u,0xd8b1cu,0x0u,0x11fff0u,0x0u,0xd8b1cu,0x0u,0x11ffe0u,0x0u,0xd8b1cu,0x0u,0x11ffd0u,0x0u,0xd8b1cu,0x0u,0x11ffc0u,0x0u,0xd8b1cu,0x0u,0x11ffb0u,0x0u,0xd8b1cu,0x0u,0x11ffa0u,0x0u,0xd8b1cu,0x0u,0x11ff90u,0x0u,0xd8b1cu,0x0u,0x11ff80u,0x0u,0xd8b1cu,0x5840u,0x5860u,0x6800u,0x5840u,0x5848u,0x5868u,0x6808u,0x5850u,0x5850u,0x5870u,0x6810u,0x5860u,0x5858u,0x5878u,0x6818u,0x5870u,0x58c0u,0x58e0u,0x6880u,0x58c0u,0x58c8u,0x58e8u,0x6888u,0x58d0u,0x58d0u,0x58f0u,0x6890u,0x58e0u,0x58d8u,0x58f8u,0x6898u,0x58f0u,0x5960u,0x5940u,0x6920u,0x5960u,0x5968u,0x5948u,0x6928u,0x5970u,0x5970u,0x5950u,0x6930u,0x5940u,0x5978u,0x5958u,0x6938u,0x5950u,0x59e0u,0x59c0u,0x69a0u,0x59e0u,0x59e8u,0x59c8u,0x69a8u,0x59f0u,0x59f0u,0x59d0u,0x69b0u,0x59c0u,0x59f8u,0x59d8u,0x69b8u,0x59d0u,0x5c00u,0x5c20u,0x6c40u,0x5a40u,0x5c08u,0x5c28u,0x6c48u,0x5a50u,0x5c10u,0x5c30u,0x6c50u,0x5a60u,0x5c18u,0x5c38u,0x6c58u,0x5a70u,0x5c80u,0x5ca0u,0x6cc0u,0x5ac0u,0x5c88u,0x5ca8u,0x6cc8u,0x5ad0u,0x5c90u,0x5cb0u,0x6cd0u,0x5ae0u,0x5c98u,0x5cb8u,0x6cd8u,0x5af0u,0x5d20u,0x5d00u,0x6d60u,0x5b60u,0x5d28u,0x5d08u,0x6d68u,0x5b70u,0x5d30u,0x5d10u,0x6d70u,0x5b40u,0x5d38u,0x5d18u,0x6d78u,0x5b50u,0x5da0u,0x5d80u,0x6de0u,0x5be0u,0x5da8u,0x5d88u,0x6de8u,0x5bf0u,0x5db0u,0x5d90u,0x6df0u,0x5bc0u,0x5db8u,0x5d98u,0x6df8u,0x5bd0u,0x6040u,0x6060u,0x7000u,0x5c00u,0x6048u,0x6068u,0x7008u,0x5c10u,0x6050u,0x6070u,0x7010u,0x5c20u,0x6058u,0x6078u,0x7018u,0x5c30u,0x60c0u,0x60e0u,0x7080u,0x5c80u,0x60c8u,0x60e8u,0x7088u,0x5c90u,0x60d0u,0x60f0u,0x7090u,0x5ca0u,0x60d8u,0x60f8u,0x7098u,0x5cb0u,0x6160u,0x6140u,0x7120u,0x5d20u,0x6168u,0x6148u,0x7128u,0x5d30u,0x6170u,0x6150u,0x7130u,0x5d00u,0x6178u,0x6158u,0x7138u,0x5d10u,0x61e0u,0x61c0u,0x71a0u,0x5da0u,0x61e8u,0x61c8u,0x71a8u,0x5db0u,0x61f0u,0x61d0u,0x71b0u,0x5d80u,0x61f8u,0x61d8u,0x71b8u,0x5d90u,0x6400u,0x6420u,0x7440u,0x5e00u,0x6408u,0x6428u,0x7448u,0x5e10u,0x6410u,0x6430u,0x7450u,0x5e20u,0x6418u,0x6438u,0x7458u,0x5e30u,0x6480u,0x64a0u,0x74c0u,0x5e80u,0x6488u,0x64a8u,0x74c8u,0x5e90u,0x6490u,0x64b0u,0x74d0u,0x5ea0u,0x6498u,0x64b8u,0x74d8u,0x5eb0u,0x6520u,0x6500u,0x7560u,0x5f20u,0x6528u,0x6508u,0x7568u,0x5f30u,0x6530u,0x6510u,0x7570u,0x5f00u,0x6538u,0x6518u,0x7578u,0x5f10u,0x65a0u,0x6580u,0x75e0u,0x5fa0u,0x65a8u,0x6588u,0x75e8u,0x5fb0u,0x65b0u,0x6590u,0x75f0u,0x5f80u,0x65b8u,0x6598u,0x75f8u,0x5f90u,0x6800u,0x1e980u,0x1ebc0u,0x1800u,0x6808u,0x1ee10u,0x1f050u,0x1810u,0x6810u,0x1f2a0u,0x1f4e0u,0x1820u,0x6818u,0x1f730u,0x1f970u,0x1830u,0x6880u,0x1fbc0u,0x1fe00u,0x1840u,0x6888u,0x20050u,0x20290u,0x1850u,0x6890u,0x204e0u,0x20720u,0x1860u,0x6898u,0x20970u,0x20bb0u,0x1870u,0x6920u,0x20e00u,0x21040u,0x1980u,0x6928u,0x21290u,0x214d0u,0x1990u,0x6930u,0x21720u,0x21960u,0x19a0u,0x6938u,0x21bb0u,0x21df0u,0x19b0u,0x69a0u,0x22040u,0x22280u,0x19c0u,0x69a8u,0x224d0u,0x22710u,0x19d0u,0x69b0u,0x22960u,0x22ba0u,0x19e0u,0x69b8u,0x22df0u,0x23030u,0x19f0u,0x6a00u,0x1e9b0u,0x1ebc8u,0x1b20u,0x6a08u,0x1ee40u,0x1f058u,0x1b30u,0x6a10u,0x1f2d0u,0x1f4e8u,0x1b00u,0x6a18u,0x1f760u,0x1f978u,0x1b10u,0x6a80u,0x1fbf0u,0x1fe08u,0x1b60u,0x6a88u,0x20080u,0x20298u,0x1b70u,0x6a90u,0x20510u,0x20728u,0x1b40u,0x6a98u,0x209a0u,0x20bb8u,0x1b50u,0x6b20u,0x20e30u,0x21048u,0x1ca0u,0x6b28u,0x212c0u,0x214d8u,0x1cb0u,0x6b30u,0x21750u,0x21968u,0x1c80u,0x6b38u,0x21be0u,0x21df8u,0x1c90u,0x6ba0u,0x22070u,0x22288u,0x1ce0u,0x6ba8u,0x22500u,0x22718u,0x1cf0u,0x6bb0u,0x22990u,0x22ba8u,0x1cc0u,0x6bb8u,0x22e20u,0x23038u,0x1cd0u,0x6c40u,0x1e9e0u,0x1ebd0u,0x1e00u,0x6c48u,0x1ee70u,0x1f060u,0x1e10u,0x6c50u,0x1f300u,0x1f4f0u,0x1e20u,0x6c58u,0x1f790u,0x1f980u,0x1e30u,0x6cc0u,0x1fc20u,0x1fe10u,0x1e40u,0x6cc8u,0x200b0u,0x202a0u,0x1e50u,0x6cd0u,0x20540u,0x20730u,0x1e60u,0x6cd8u,0x209d0u,0x20bc0u,0x1e70u,0x6d60u,0x20e60u,0x21050u,0x1f80u,0x6d68u,0x212f0u,0x214e0u,0x1f90u,0x6d70u,0x21780u,0x21970u,0x1fa0u,0x6d78u,0x21c10u,0x21e00u,0x1fb0u,0x6de0u,0x220a0u,0x22290u,0x1fc0u,0x6de8u,0x22530u,0x22720u,0x1fd0u,0x6df0u,0x229c0u,0x22bb0u,0x1fe0u,0x6df8u,0x22e50u,0x23040u,0x1ff0u,0x6e40u,0x1ea10u,0x1ebd8u,0x2120u,0x6e48u,0x1eea0u,0x1f068u,0x2130u,0x6e50u,0x1f330u,0x1f4f8u,0x2100u,0x6e58u,0x1f7c0u,0x1f988u,0x2110u,0x6ec0u,0x1fc50u,0x1fe18u,0x2160u,0x6ec8u,0x200e0u,0x202a8u,0x2170u,0x6ed0u,0x20570u,0x20738u,0x2140u,0x6ed8u,0x20a00u,0x20bc8u,0x2150u,0x6f60u,0x20e90u,0x21058u,0x22a0u,0x6f68u,0x21320u,0x214e8u,0x22b0u,0x6f70u,0x217b0u,0x21978u,0x2280u,0x6f78u,0x21c40u,0x21e08u,0x2290u,0x6fe0u,0x220d0u,0x22298u,0x22e0u,0x6fe8u,0x22560u,0x22728u,0x22f0u,0x6ff0u,0x229f0u,0x22bb8u,0x22c0u,0x6ff8u,0x22e80u,0x23048u,0x22d0u,0x0u,0x40u,0x9000u,0xb400u,0x80u,0xc0u,0x9010u,0xb410u,0x110u,0x150u,0x9020u,0xb420u,0x190u,0x1d0u,0x9030u,0xb430u,0x220u,0x260u,0x9040u,0xb440u,0x2a0u,0x2e0u,0x9050u,0xb450u,0x330u,0x370u,0x9060u,0xb460u,0x3b0u,0x3f0u,0x9070u,0xb470u,0x440u,0x400u,0x9080u,0xb480u,0x4c0u,0x480u,0x9090u,0xb490u,0x550u,0x510u,0x90a0u,0xb4a0u,0x5d0u,0x590u,0x90b0u,0xb4b0u,0x660u,0x620u,0x90c0u,0xb4c0u,0x6e0u,0x6a0u,0x90d0u,0xb4d0u,0x770u,0x730u,0x90e0u,0xb4e0u,0x7f0u,0x7b0u,0x90f0u,0xb4f0u,0x10u,0x50u,0x9300u,0xb700u,0x90u,0xd0u,0x9310u,0xb710u,0x100u,0x140u,0x9320u,0xb720u,0x180u,0x1c0u,0x9330u,0xb730u,0x230u,0x270u,0x9340u,0xb740u,0x2b0u,0x2f0u,0x9350u,0xb750u,0x320u,0x360u,0x9360u,0xb760u,0x3a0u,0x3e0u,0x9370u,0xb770u,0x450u,0x410u,0x9380u,0xb780u,0x4d0u,0x490u,0x9390u,0xb790u,0x540u,0x500u,0x93a0u,0xb7a0u,0x5c0u,0x580u,0x93b0u,0xb7b0u,0x670u,0x630u,0x93c0u,0xb7c0u,0x6f0u,0x6b0u,0x93d0u,0xb7d0u,0x760u,0x720u,0x93e0u,0xb7e0u,0x7e0u,0x7a0u,0x93f0u,0xb7f0u,0x20u,0x60u,0x9600u,0xba00u,0xa0u,0xe0u,0x9610u,0xba10u,0x130u,0x170u,0x9620u,0xba20u,0x1b0u,0x1f0u,0x9630u,0xba30u,0x200u,0x240u,0x9640u,0xba40u,0x280u,0x2c0u,0x9650u,0xba50u,0x310u,0x350u,0x9660u,0xba60u,0x390u,0x3d0u,0x9670u,0xba70u,0x460u,0x420u,0x9680u,0xba80u,0x4e0u,0x4a0u,0x9690u,0xba90u,0x570u,0x530u,0x96a0u,0xbaa0u,0x5f0u,0x5b0u,0x96b0u,0xbab0u,0x640u,0x600u,0x96c0u,0xbac0u,0x6c0u,0x680u,0x96d0u,0xbad0u,0x750u,0x710u,0x96e0u,0xbae0u,0x7d0u,0x790u,0x96f0u,0xbaf0u,0x30u,0x70u,0x9900u,0xbd00u,0xb0u,0xf0u,0x9910u,0xbd10u,0x120u,0x160u,0x9920u,0xbd20u,0x1a0u,0x1e0u,0x9930u,0xbd30u,0x210u,0x250u,0x9940u,0xbd40u,0x290u,0x2d0u,0x9950u,0xbd50u,0x300u,0x340u,0x9960u,0xbd60u,0x380u,0x3c0u,0x9970u,0xbd70u,0x470u,0x430u,0x9980u,0xbd80u,0x4f0u,0x4b0u,0x9990u,0xbd90u,0x560u,0x520u,0x99a0u,0xbda0u,0x5e0u,0x5a0u,0x99b0u,0xbdb0u,0x650u,0x610u,0x99c0u,0xbdc0u,0x6d0u,0x690u,0x99d0u,0xbdd0u,0x740u,0x700u,0x99e0u,0xbde0u,0x7c0u,0x780u,0x99f0u,0xbdf0u,0xc000u,0x120c0u,0x12100u,0x12800u,0xc010u,0x12250u,0x12290u,0x12810u,0xc020u,0x123e0u,0x12420u,0x12820u,0xc030u,0x12570u,0x125b0u,0x12830u,0xc040u,0x12680u,0x12740u,0x12840u,0xc050u,0x12810u,0x128d0u,0x12850u,0xc060u,0x129a0u,0x12a60u,0x12860u,0xc070u,0x12b30u,0x12bf0u,0x12870u,0xc080u,0x12cc0u,0x12d00u,0x12880u,0xc090u,0x12e50u,0x12e90u,0x12890u,0xc0a0u,0x12fe0u,0x13020u,0x128a0u,0xc0b0u,0x13170u,0x131b0u,0x128b0u,0xc0c0u,0x13280u,0x13340u,0x128c0u,0xc0d0u,0x13410u,0x134d0u,0x128d0u,0xc0e0u,0x135a0u,0x13660u,0x128e0u,0xc0f0u,0x13730u,0x137f0u,0x128f0u,0xc300u,0x120d0u,0x12108u,0x12900u,0xc310u,0x12240u,0x12298u,0x12910u,0xc320u,0x123f0u,0x12428u,0x12920u,0xc330u,0x12560u,0x125b8u,0x12930u,0xc340u,0x12690u,0x12748u,0x12940u,0xc350u,0x12800u,0x128d8u,0x12950u,0xc360u,0x129b0u,0x12a68u,0x12960u,0xc370u,0x12b20u,0x12bf8u,0x12970u,0xc380u,0x12cd0u,0x12d08u,0x12980u,0xc390u,0x12e40u,0x12e98u,0x12990u,0xc3a0u,0x12ff0u,0x13028u,0x129a0u,0xc3b0u,0x13160u,0x131b8u,0x129b0u,0xc3c0u,0x13290u,0x13348u,0x129c0u,0xc3d0u,0x13400u,0x134d8u,0x129d0u,0xc3e0u,0x135b0u,0x13668u,0x129e0u,0xc3f0u,0x13720u,0x137f8u,0x129f0u,0xc600u,0x120e0u,0x12110u,0x12a00u,0xc610u,0x12270u,0x12280u,0x12a10u,0xc620u,0x123c0u,0x12430u,0x12a20u,0xc630u,0x12550u,0x125a0u,0x12a30u,0xc640u,0x126a0u,0x12750u,0x12a40u,0xc650u,0x12830u,0x128c0u,0x12a50u,0xc660u,0x12980u,0x12a70u,0x12a60u,0xc670u,0x12b10u,0x12be0u,0x12a70u,0xc680u,0x12ce0u,0x12d10u,0x12a80u,0xc690u,0x12e70u,0x12e80u,0x12a90u,0xc6a0u,0x12fc0u,0x13030u,0x12aa0u,0xc6b0u,0x13150u,0x131a0u,0x12ab0u,0xc6c0u,0x132a0u,0x13350u,0x12ac0u,0xc6d0u,0x13430u,0x134c0u,0x12ad0u,0xc6e0u,0x13580u,0x13670u,0x12ae0u,0xc6f0u,0x13710u,0x137e0u,0x12af0u,0xc900u,0x120f0u,0x12118u,0x12b00u,0xc910u,0x12260u,0x12288u,0x12b10u,0xc920u,0x123d0u,0x12438u,0x12b20u,0xc930u,0x12540u,0x125a8u,0x12b30u,0xc940u,0x126b0u,0x12758u,0x12b40u,0xc950u,0x12820u,0x128c8u,0x12b50u,0xc960u,0x12990u,0x12a78u,0x12b60u,0xc970u,0x12b00u,0x12be8u,0x12b70u,0xc980u,0x12cf0u,0x12d18u,0x12b80u,0xc990u,0x12e60u,0x12e88u,0x12b90u,0xc9a0u,0x12fd0u,0x13038u,0x12ba0u,0xc9b0u,0x13140u,0x131a8u,0x12bb0u,0xc9c0u,0x132b0u,0x13358u,0x12bc0u,0xc9d0u,0x13420u,0x134c8u,0x12bd0u,0xc9e0u,0x13590u,0x13678u,0x12be0u,0xc9f0u,0x13700u,0x137e8u,0x12bf0u,0x7800u,0x1c300u,0x1e980u,0x1ebc0u,0x7880u,0x1c310u,0x100000u,0x1ebc2u,0x7910u,0x1c320u,0x100000u,0x1ebc4u,0x7990u,0x1c330u,0x100000u,0x1ebc6u,0x7a20u,0x1c340u,0x1e980u,0x1ebc8u,0x7aa0u,0x1c350u,0x100000u,0x1ebcau,0x7b30u,0x1c360u,0x100000u,0x1ebccu,0x7bb0u,0x1c370u,0x100000u,0x1ebceu,0x7c40u,0x1c380u,0x1e980u,0x1ebd0u,0x7cc0u,0x1c390u,0x100000u,0x1ebd2u,0x7d50u,0x1c3a0u,0x100000u,0x1ebd4u,0x7dd0u,0x1c3b0u,0x100000u,0x1ebd6u,0x7e60u,0x1c3c0u,0x1e980u,0x1ebd8u,0x7ee0u,0x1c3d0u,0x100000u,0x1ebdau,0x7f70u,0x1c3e0u,0x100000u,0x1ebdcu,0x7ff0u,0x1c3f0u,0x100000u,0x1ebdeu,0x7810u,0x1c400u,0x1e9b0u,0x1ebc0u,0x7890u,0x1c410u,0x100000u,0x1ebc2u,0x7900u,0x1c420u,0x100000u,0x1ebc4u,0x7980u,0x1c430u,0x100000u,0x1ebc6u,0x7a30u,0x1c440u,0x1e9b0u,0x1ebc8u,0x7ab0u,0x1c450u,0x100000u,0x1ebcau,0x7b20u,0x1c460u,0x100000u,0x1ebccu,0x7ba0u,0x1c470u,0x100000u,0x1ebceu,0x7c50u,0x1c480u,0x1e9b0u,0x1ebd0u,0x7cd0u,0x1c490u,0x100000u,0x1ebd2u,0x7d40u,0x1c4a0u,0x100000u,0x1ebd4u,0x7dc0u,0x1c4b0u,0x100000u,0x1ebd6u,0x7e70u,0x1c4c0u,0x1e9b0u,0x1ebd8u,0x7ef0u,0x1c4d0u,0x100000u,0x1ebdau,0x7f60u,0x1c4e0u,0x100000u,0x1ebdcu,0x7fe0u,0x1c4f0u,0x100000u,0x1ebdeu,0x7820u,0x1c500u,0x1e9e0u,0x1ebc0u,0x78a0u,0x1c510u,0x100000u,0x1ebc2u,0x7930u,0x1c520u,0x100000u,0x1ebc4u,0x79b0u,0x1c530u,0x100000u,0x1ebc6u,0x7a00u,0x1c540u,0x1e9e0u,0x1ebc8u,0x7a80u,0x1c550u,0x100000u,0x1ebcau,0x7b10u,0x1c560u,0x100000u,0x1ebccu,0x7b90u,0x1c570u,0x100000u,0x1ebceu,0x7c60u,0x1c580u,0x1e9e0u,0x1ebd0u,0x7ce0u,0x1c590u,0x100000u,0x1ebd2u,0x7d70u,0x1c5a0u,0x100000u,0x1ebd4u,0x7df0u,0x1c5b0u,0x100000u,0x1ebd6u,0x7e40u,0x1c5c0u,0x1e9e0u,0x1ebd8u,0x7ec0u,0x1c5d0u,0x100000u,0x1ebdau,0x7f50u,0x1c5e0u,0x100000u,0x1ebdcu,0x7fd0u,0x1c5f0u,0x100000u,0x1ebdeu,0x7830u,0x1c600u,0x1ea10u,0x1ebc0u,0x78b0u,0x1c610u,0x100000u,0x1ebc2u,0x7920u,0x1c620u,0x100000u,0x1ebc4u,0x79a0u,0x1c630u,0x100000u,0x1ebc6u,0x7a10u,0x1c640u,0x1ea10u,0x1ebc8u,0x7a90u,0x1c650u,0x100000u,0x1ebcau,0x7b00u,0x1c660u,0x100000u,0x1ebccu,0x7b80u,0x1c670u,0x100000u,0x1ebceu,0x7c70u,0x1c680u,0x1ea10u,0x1ebd0u,0x7cf0u,0x1c690u,0x100000u,0x1ebd2u,0x7d60u,0x1c6a0u,0x100000u,0x1ebd4u,0x7de0u,0x1c6b0u,0x100000u,0x1ebd6u,0x7e50u,0x1c6c0u,0x1ea10u,0x1ebd8u,0x7ed0u,0x1c6d0u,0x100000u,0x1ebdau,0x7f40u,0x1c6e0u,0x100000u,0x1ebdcu,0x7fc0u,0x1c6f0u,0x100000u,0x1ebdeu,0x26000u,0x1d400u,0x1d400u,0x80000000u,0x26002u,0x1d402u,0x100000u,0x80000000u,0x26004u,0x1d404u,0x100000u,0x80000000u,0x26006u,0x1d406u,0x100000u,0x80000000u,0x26200u,0x1d410u,0x1d400u,0x80000000u,0x26202u,0x1d412u,0x100000u,0x80000000u,0x26204u,0x1d414u,0x100000u,0x80000000u,0x26206u,0x1d416u,0x100000u,0x80000000u,0x26400u,0x1d420u,0x1d400u,0x80000000u,0x26402u,0x1d422u,0x100000u,0x80000000u,0x26404u,0x1d424u,0x100000u,0x80000000u,0x26406u,0x1d426u,0x100000u,0x80000000u,0x26600u,0x1d430u,0x1d400u,0x80000000u,0x26602u,0x1d432u,0x100000u,0x80000000u,0x26604u,0x1d434u,0x100000u,0x80000000u,0x26606u,0x1d436u,0x100000u,0x80000000u,0x26008u,0x1d408u,0x1d410u,0x80000000u,0x2600au,0x1d40au,0x100000u,0x80000000u,0x2600cu,0x1d40cu,0x100000u,0x80000000u,0x2600eu,0x1d40eu,0x100000u,0x80000000u,0x26208u,0x1d418u,0x1d410u,0x80000000u,0x2620au,0x1d41au,0x100000u,0x80000000u,0x2620cu,0x1d41cu,0x100000u,0x80000000u,0x2620eu,0x1d41eu,0x100000u,0x80000000u,0x26408u,0x1d428u,0x1d410u,0x80000000u,0x2640au,0x1d42au,0x100000u,0x80000000u,0x2640cu,0x1d42cu,0x100000u,0x80000000u,0x2640eu,0x1d42eu,0x100000u,0x80000000u,0x26608u,0x1d438u,0x1d410u,0x80000000u,0x2660au,0x1d43au,0x100000u,0x80000000u,0x2660cu,0x1d43cu,0x100000u,0x80000000u,0x2660eu,0x1d43eu,0x100000u,0x80000000u,0x26800u,0x1d440u,0x1d420u,0x80000000u,0x26802u,0x1d442u,0x100000u,0x80000000u,0x26804u,0x1d444u,0x100000u,0x80000000u,0x26806u,0x1d446u,0x100000u,0x80000000u,0x26a00u,0x1d450u,0x1d420u,0x80000000u,0x26a02u,0x1d452u,0x100000u,0x80000000u,0x26a04u,0x1d454u,0x100000u,0x80000000u,0x26a06u,0x1d456u,0x100000u,0x80000000u,0x26c00u,0x1d460u,0x1d420u,0x80000000u,0x26c02u,0x1d462u,0x100000u,0x80000000u,0x26c04u,0x1d464u,0x100000u,0x80000000u,0x26c06u,0x1d466u,0x100000u,0x80000000u,0x26e00u,0x1d470u,0x1d420u,0x80000000u,0x26e02u,0x1d472u,0x100000u,0x80000000u,0x26e04u,0x1d474u,0x100000u,0x80000000u,0x26e06u,0x1d476u,0x100000u,0x80000000u,0x100000u,0x1d448u,0x1d430u,0x80000000u,0x100000u,0x1d44au,0x100000u,0x80000000u,0x100000u,0x1d44cu,0x100000u,0x80000000u,0x100000u,0x1d44eu,0x100000u,0x80000000u,0x100000u,0x1d458u,0x1d430u,0x80000000u,0x100000u,0x1d45au,0x100000u,0x80000000u,0x100000u,0x1d45cu,0x100000u,0x80000000u,0x100000u,0x1d45eu,0x100000u,0x80000000u,0x100000u,0x1d468u,0x1d430u,0x80000000u,0x100000u,0x1d46au,0x100000u,0x80000000u,0x100000u,0x1d46cu,0x100000u,0x80000000u,0x100000u,0x1d46eu,0x100000u,0x80000000u,0x100000u,0x1d478u,0x1d430u,0x80000000u,0x100000u,0x1d47au,0x100000u,0x80000000u,0x100000u,0x1d47cu,0x100000u,0x80000000u,0x100000u,0x1d47eu,0x100000u,0x80000000u,0x850u,0x60000u,0x90420u,0xefc50u,0x840u,0x60010u,0x90430u,0xefc40u,0x870u,0x60020u,0x90400u,0xefc70u,0x860u,0x60030u,0x90410u,0xefc60u,0x810u,0x60040u,0x90460u,0xefc10u,0x800u,0x60050u,0x90470u,0xefc00u,0x830u,0x60060u,0x90440u,0xefc30u,0x820u,0x60070u,0x90450u,0xefc20u,0x8d0u,0x60080u,0x904b0u,0xefcd0u,0x8c0u,0x60090u,0x904a0u,0xefcc0u,0x8f0u,0x600a0u,0x90490u,0xefcf0u,0x8e0u,0x600b0u,0x90480u,0xefce0u,0x890u,0x600c0u,0x904f0u,0xefc90u,0x880u,0x600d0u,0x904e0u,0xefc80u,0x8b0u,0x600e0u,0x904d0u,0xefcb0u,0x8a0u,0x600f0u,0x904c0u,0xefca0u,0x960u,0x60100u,0x90530u,0xefd60u,0x970u,0x60110u,0x90520u,0xefd70u,0x940u,0x60120u,0x90510u,0xefd40u,0x950u,0x60130u,0x90500u,0xefd50u,0x920u,0x60140u,0x90570u,0xefd20u,0x930u,0x60150u,0x90560u,0xefd30u,0x900u,0x60160u,0x90550u,0xefd00u,0x910u,0x60170u,0x90540u,0xefd10u,0x9e0u,0x60190u,0x905b0u,0xefde0u,0x9f0u,0x60180u,0x905a0u,0xefdf0u,0x9c0u,0x601b0u,0x90590u,0xefdc0u,0x9d0u,0x601a0u,0x90580u,0xefdd0u,0x9a0u,0x601d0u,0x905f0u,0xefda0u,0x9b0u,0x601c0u,0x905e0u,0xefdb0u,0x980u,0x601f0u,0x905d0u,0xefd80u,0x990u,0x601e0u,0x905c0u,0xefd90u,0xa60u,0x60210u,0xbfa40u,0xefe60u,0xa70u,0x60200u,0xbfa50u,0xefe70u,0xa40u,0x60230u,0xbfa60u,0xefe40u,0xa50u,0x60220u,0xbfa70u,0xefe50u,0xa20u,0x60250u,0xbfa00u,0xefe20u,0xa30u,0x60240u,0xbfa10u,0xefe30u,0xa00u,0x60270u,0xbfa20u,0xefe00u,0xa10u,0x60260u,0xbfa30u,0xefe10u,0x2fef0u,0x60290u,0xbfac0u,0xefef0u,0x2fee0u,0x60280u,0xbfad0u,0xefee0u,0x2fed0u,0x602b0u,0xbfae0u,0xefed0u,0x2fec0u,0x602a0u,0xbfaf0u,0xefec0u,0x2feb0u,0x602d0u,0xbfa80u,0xefeb0u,0x2fea0u,0x602c0u,0xbfa90u,0xefea0u,0x2fe90u,0x602f0u,0xbfaa0u,0xefe90u,0x2fe80u,0x602e0u,0xbfab0u,0xefe80u,0x2ff70u,0x60320u,0xbfb40u,0xeff70u,0x2ff60u,0x60330u,0xbfb50u,0xeff60u,0x2ff50u,0x60300u,0xbfb60u,0xeff50u,0x2ff40u,0x60310u,0xbfb70u,0xeff40u,0x2ff30u,0x60360u,0xbfb00u,0xeff30u,0x2ff20u,0x60370u,0xbfb10u,0xeff20u,0x2ff10u,0x60340u,0xbfb20u,0xeff10u,0x2ff00u,0x60350u,0xbfb30u,0xeff00u,0x2fff0u,0x603a0u,0xbfbd0u,0xefff0u,0x2ffe0u,0x603b0u,0xbfbc0u,0xeffe0u,0x2ffd0u,0x60380u,0xbfbf0u,0xeffd0u,0x2ffc0u,0x60390u,0xbfbe0u,0xeffc0u,0x2ffb0u,0x603e0u,0xbfb90u,0xeffb0u,0x2ffa0u,0x603f0u,0xbfb80u,0xeffa0u,0x2ff90u,0x603c0u,0xbfbb0u,0xeff90u,0x2ff80u,0x603d0u,0xbfba0u,0xeff80u,0x0u,0x80000000u,0x0u,0x18830u,0x0u,0x80000000u,0x0u,0x18830u,0x0u,0x80000000u,0x0u,0x18830u,0x0u,0x80000000u,0x0u,0x18830u,0x0u,0x80000000u,0x0u,0x18830u,0x0u,0x80000000u,0x0u,0x18830u,0x0u,0x80000000u,0x0u,0x18830u,0x0u,0x80000000u,0x0u,0x18830u,0x0u,0x80000000u,0x0u,0x18830u,0x0u,0x80000000u,0x0u,0x18830u,0x0u,0x80000000u,0x0u,0x18830u,0x0u,0x80000000u,0x0u,0x18830u,0x0u,0x80000000u,0x0u,0x18830u,0x0u,0x80000000u,0x0u,0x18830u,0x0u,0x80000000u,0x0u,0x18830u,0x0u,0x80000000u,0x0u,0x18830u,0x0u,0x80000000u,0x0u,0x188b2u,0x0u,0x80000000u,0x0u,0x188b2u,0x0u,0x80000000u,0x0u,0x188b2u,0x0u,0x80000000u,0x0u,0x188b2u,0x0u,0x80000000u,0x0u,0x188b2u,0x0u,0x80000000u,0x0u,0x188b2u,0x0u,0x80000000u,0x0u,0x188b2u,0x0u,0x80000000u,0x0u,0x188b2u,0x0u,0x80000000u,0x0u,0x18892u,0x0u,0x80000000u,0x0u,0x18892u,0x0u,0x80000000u,0x0u,0x18892u,0x0u,0x80000000u,0x0u,0x18892u,0x0u,0x80000000u,0x0u,0x18892u,0x0u,0x80000000u,0x0u,0x18892u,0x0u,0x80000000u,0x0u,0x18892u,0x0u,0x80000000u,0x0u,0x18892u,0x0u,0x80000000u,0x0u,0x1489au,0x0u,0x80000000u,0x0u,0x1489au,0x0u,0x80000000u,0x0u,0x1489au,0x0u,0x80000000u,0x0u,0x1489au,0x0u,0x80000000u,0x0u,0x1489au,0x0u,0x80000000u,0x0u,0x1489au,0x0u,0x80000000u,0x0u,0x1489au,0x0u,0x80000000u,0x0u,0x1489au,0x0u,0x80000000u,0x0u,0x34018u,0x0u,0x80000000u,0x0u,0x34018u,0x0u,0x80000000u,0x0u,0x34018u,0x0u,0x80000000u,0x0u,0x34018u,0x0u,0x80000000u,0x0u,0x34018u,0x0u,0x80000000u,0x0u,0x34018u,0x0u,0x80000000u,0x0u,0x34018u,0x0u,0x80000000u,0x0u,0x34018u,0x0u,0x80000000u,0x0u,0x36018u,0x0u,0x80000000u,0x0u,0x36018u,0x0u,0x80000000u,0x0u,0x36018u,0x0u,0x80000000u,0x0u,0x36018u,0x0u,0x80000000u,0x0u,0x36018u,0x0u,0x80000000u,0x0u,0x36018u,0x0u,0x80000000u,0x0u,0x36018u,0x0u,0x80000000u,0x0u,0x36018u,0x0u,0x80000000u,0x0u,0x36030u,0x0u,0x80000000u,0x0u,0x36030u,0x0u,0x80000000u,0x0u,0x36030u,0x0u,0x80000000u,0x0u,0x36030u,0x0u,0x80000000u,0x0u,0x36030u,0x0u,0x80000000u,0x0u,0x36030u,0x0u,0x80000000u,0x0u,0x36030u,0x0u,0x80000000u,0x0u,0x36030u,0x6840u,0x6860u,0x6820u,0x6820u,0x6848u,0x6868u,0x6828u,0x6830u,0x6850u,0x6870u,0x6830u,0x6840u,0x6858u,0x6878u,0x6838u,0x6850u,0x68c0u,0x68e0u,0x68a0u,0x68a0u,0x68c8u,0x68e8u,0x68a8u,0x68b0u,0x68d0u,0x68f0u,0x68b0u,0x68c0u,0x68d8u,0x68f8u,0x68b8u,0x68d0u,0x6960u,0x6940u,0x6900u,0x6900u,0x6968u,0x6948u,0x6908u,0x6910u,0x6970u,0x6950u,0x6910u,0x6960u,0x6978u,0x6958u,0x6918u,0x6970u,0x69e0u,0x69c0u,0x6980u,0x6980u,0x69e8u,0x69c8u,0x6988u,0x6990u,0x69f0u,0x69d0u,0x6990u,0x69e0u,0x69f8u,0x69d8u,0x6998u,0x69f0u,0x6c00u,0x6c20u,0x6c60u,0x6a20u,0x6c08u,0x6c28u,0x6c68u,0x6a30u,0x6c10u,0x6c30u,0x6c70u,0x6a40u,0x6c18u,0x6c38u,0x6c78u,0x6a50u,0x6c80u,0x6ca0u,0x6ce0u,0x6aa0u,0x6c88u,0x6ca8u,0x6ce8u,0x6ab0u,0x6c90u,0x6cb0u,0x6cf0u,0x6ac0u,0x6c98u,0x6cb8u,0x6cf8u,0x6ad0u,0x6d20u,0x6d00u,0x6d40u,0x6b00u,0x6d28u,0x6d08u,0x6d48u,0x6b10u,0x6d30u,0x6d10u,0x6d50u,0x6b60u,0x6d38u,0x6d18u,0x6d58u,0x6b70u,0x6da0u,0x6d80u,0x6dc0u,0x6b80u,0x6da8u,0x6d88u,0x6dc8u,0x6b90u,0x6db0u,0x6d90u,0x6dd0u,0x6be0u,0x6db8u,0x6d98u,0x6dd8u,0x6bf0u,0x7040u,0x7060u,0x7020u,0x6c60u,0x7048u,0x7068u,0x7028u,0x6c70u,0x7050u,0x7070u,0x7030u,0x6c00u,0x7058u,0x7078u,0x7038u,0x6c10u,0x70c0u,0x70e0u,0x70a0u,0x6ce0u,0x70c8u,0x70e8u,0x70a8u,0x6cf0u,0x70d0u,0x70f0u,0x70b0u,0x6c80u,0x70d8u,0x70f8u,0x70b8u,0x6c90u,0x7160u,0x7140u,0x7100u,0x6d40u,0x7168u,0x7148u,0x7108u,0x6d50u,0x7170u,0x7150u,0x7110u,0x6d20u,0x7178u,0x7158u,0x7118u,0x6d30u,0x71e0u,0x71c0u,0x7180u,0x6dc0u,0x71e8u,0x71c8u,0x7188u,0x6dd0u,0x71f0u,0x71d0u,0x7190u,0x6da0u,0x71f8u,0x71d8u,0x7198u,0x6db0u,0x7400u,0x7420u,0x7460u,0x6e60u,0x7408u,0x7428u,0x7468u,0x6e70u,0x7410u,0x7430u,0x7470u,0x6e00u,0x7418u,0x7438u,0x7478u,0x6e10u,0x7480u,0x74a0u,0x74e0u,0x6ee0u,0x7488u,0x74a8u,0x74e8u,0x6ef0u,0x7490u,0x74b0u,0x74f0u,0x6e80u,0x7498u,0x74b8u,0x74f8u,0x6e90u,0x7520u,0x7500u,0x7540u,0x6f40u,0x7528u,0x7508u,0x7548u,0x6f50u,0x7530u,0x7510u,0x7550u,0x6f20u,0x7538u,0x7518u,0x7558u,0x6f30u,0x75a0u,0x7580u,0x75c0u,0x6fc0u,0x75a8u,0x7588u,0x75c8u,0x6fd0u,0x75b0u,0x7590u,0x75d0u,0x6fa0u,0x75b8u,0x7598u,0x75d8u,0x6fb0u,0x6860u,0x1ea40u,0x1ec20u,0x2440u,0x6868u,0x1eed0u,0x1f0b0u,0x2450u,0x6870u,0x1f360u,0x1f540u,0x2460u,0x6878u,0x1f7f0u,0x1f9d0u,0x2470u,0x68e0u,0x1fc80u,0x1fe60u,0x2400u,0x68e8u,0x20110u,0x202f0u,0x2410u,0x68f0u,0x205a0u,0x20780u,0x2420u,0x68f8u,0x20a30u,0x20c10u,0x2430u,0x6940u,0x20ec0u,0x210a0u,0x25c0u,0x6948u,0x21350u,0x21530u,0x25d0u,0x6950u,0x217e0u,0x219c0u,0x25e0u,0x6958u,0x21c70u,0x21e50u,0x25f0u,0x69c0u,0x22100u,0x222e0u,0x2580u,0x69c8u,0x22590u,0x22770u,0x2590u,0x69d0u,0x22a20u,0x22c00u,0x25a0u,0x69d8u,0x22eb0u,0x23090u,0x25b0u,0x6a60u,0x1ea70u,0x1ec28u,0x2760u,0x6a68u,0x1ef00u,0x1f0b8u,0x2770u,0x6a70u,0x1f390u,0x1f548u,0x2740u,0x6a78u,0x1f820u,0x1f9d8u,0x2750u,0x6ae0u,0x1fcb0u,0x1fe68u,0x2720u,0x6ae8u,0x20140u,0x202f8u,0x2730u,0x6af0u,0x205d0u,0x20788u,0x2700u,0x6af8u,0x20a60u,0x20c18u,0x2710u,0x6b40u,0x20ef0u,0x210a8u,0x28e0u,0x6b48u,0x21380u,0x21538u,0x28f0u,0x6b50u,0x21810u,0x219c8u,0x28c0u,0x6b58u,0x21ca0u,0x21e58u,0x28d0u,0x6bc0u,0x22130u,0x222e8u,0x28a0u,0x6bc8u,0x225c0u,0x22778u,0x28b0u,0x6bd0u,0x22a50u,0x22c08u,0x2880u,0x6bd8u,0x22ee0u,0x23098u,0x2890u,0x6c20u,0x1eaa0u,0x1ec30u,0x2a40u,0x6c28u,0x1ef30u,0x1f0c0u,0x2a50u,0x6c30u,0x1f3c0u,0x1f550u,0x2a60u,0x6c38u,0x1f850u,0x1f9e0u,0x2a70u,0x6ca0u,0x1fce0u,0x1fe70u,0x2a00u,0x6ca8u,0x20170u,0x20300u,0x2a10u,0x6cb0u,0x20600u,0x20790u,0x2a20u,0x6cb8u,0x20a90u,0x20c20u,0x2a30u,0x6d00u,0x20f20u,0x210b0u,0x2bc0u,0x6d08u,0x213b0u,0x21540u,0x2bd0u,0x6d10u,0x21840u,0x219d0u,0x2be0u,0x6d18u,0x21cd0u,0x21e60u,0x2bf0u,0x6d80u,0x22160u,0x222f0u,0x2b80u,0x6d88u,0x225f0u,0x22780u,0x2b90u,0x6d90u,0x22a80u,0x22c10u,0x2ba0u,0x6d98u,0x22f10u,0x230a0u,0x2bb0u,0x6e20u,0x1ead0u,0x1ec38u,0x2d60u,0x6e28u,0x1ef60u,0x1f0c8u,0x2d70u,0x6e30u,0x1f3f0u,0x1f558u,0x2d40u,0x6e38u,0x1f880u,0x1f9e8u,0x2d50u,0x6ea0u,0x1fd10u,0x1fe78u,0x2d20u,0x6ea8u,0x201a0u,0x20308u,0x2d30u,0x6eb0u,0x20630u,0x20798u,0x2d00u,0x6eb8u,0x20ac0u,0x20c28u,0x2d10u,0x6f00u,0x20f50u,0x210b8u,0x2ee0u,0x6f08u,0x213e0u,0x21548u,0x2ef0u,0x6f10u,0x21870u,0x219d8u,0x2ec0u,0x6f18u,0x21d00u,0x21e68u,0x2ed0u,0x6f80u,0x22190u,0x222f8u,0x2ea0u,0x6f88u,0x22620u,0x22788u,0x2eb0u,0x6f90u,0x22ab0u,0x22c18u,0x2e80u,0x6f98u,0x22f40u,0x230a8u,0x2e90u,0x0u,0x40u,0x9000u,0xcc00u,0x80u,0xc0u,0x9010u,0xcc10u,0x110u,0x150u,0x9020u,0xcc20u,0x190u,0x1d0u,0x9030u,0xcc30u,0x220u,0x260u,0x9040u,0xcc40u,0x2a0u,0x2e0u,0x9050u,0xcc50u,0x330u,0x370u,0x9060u,0xcc60u,0x3b0u,0x3f0u,0x9070u,0xcc70u,0x440u,0x400u,0x9080u,0xcc80u,0x4c0u,0x480u,0x9090u,0xcc90u,0x550u,0x510u,0x90a0u,0xcca0u,0x5d0u,0x590u,0x90b0u,0xccb0u,0x660u,0x620u,0x90c0u,0xccc0u,0x6e0u,0x6a0u,0x90d0u,0xccd0u,0x770u,0x730u,0x90e0u,0xcce0u,0x7f0u,0x7b0u,0x90f0u,0xccf0u,0x10u,0x50u,0x9300u,0xcf00u,0x90u,0xd0u,0x9310u,0xcf10u,0x100u,0x140u,0x9320u,0xcf20u,0x180u,0x1c0u,0x9330u,0xcf30u,0x230u,0x270u,0x9340u,0xcf40u,0x2b0u,0x2f0u,0x9350u,0xcf50u,0x320u,0x360u,0x9360u,0xcf60u,0x3a0u,0x3e0u,0x9370u,0xcf70u,0x450u,0x410u,0x9380u,0xcf80u,0x4d0u,0x490u,0x9390u,0xcf90u,0x540u,0x500u,0x93a0u,0xcfa0u,0x5c0u,0x580u,0x93b0u,0xcfb0u,0x670u,0x630u,0x93c0u,0xcfc0u,0x6f0u,0x6b0u,0x93d0u,0xcfd0u,0x760u,0x720u,0x93e0u,0xcfe0u,0x7e0u,0x7a0u,0x93f0u,0xcff0u,0x20u,0x60u,0x9600u,0xd200u,0xa0u,0xe0u,0x9610u,0xd210u,0x130u,0x170u,0x9620u,0xd220u,0x1b0u,0x1f0u,0x9630u,0xd230u,0x200u,0x240u,0x9640u,0xd240u,0x280u,0x2c0u,0x9650u,0xd250u,0x310u,0x350u,0x9660u,0xd260u,0x390u,0x3d0u,0x9670u,0xd270u,0x460u,0x420u,0x9680u,0xd280u,0x4e0u,0x4a0u,0x9690u,0xd290u,0x570u,0x530u,0x96a0u,0xd2a0u,0x5f0u,0x5b0u,0x96b0u,0xd2b0u,0x640u,0x600u,0x96c0u,0xd2c0u,0x6c0u,0x680u,0x96d0u,0xd2d0u,0x750u,0x710u,0x96e0u,0xd2e0u,0x7d0u,0x790u,0x96f0u,0xd2f0u,0x30u,0x70u,0x9900u,0xd500u,0xb0u,0xf0u,0x9910u,0xd510u,0x120u,0x160u,0x9920u,0xd520u,0x1a0u,0x1e0u,0x9930u,0xd530u,0x210u,0x250u,0x9940u,0xd540u,0x290u,0x2d0u,0x9950u,0xd550u,0x300u,0x340u,0x9960u,0xd560u,0x380u,0x3c0u,0x9970u,0xd570u,0x470u,0x430u,0x9980u,0xd580u,0x4f0u,0x4b0u,0x9990u,0xd590u,0x560u,0x520u,0x99a0u,0xd5a0u,0x5e0u,0x5a0u,0x99b0u,0xd5b0u,0x650u,0x610u,0x99c0u,0xd5c0u,0x6d0u,0x690u,0x99d0u,0xd5d0u,0x740u,0x700u,0x99e0u,0xd5e0u,0x7c0u,0x780u,0x99f0u,0xd5f0u,0xc008u,0x12120u,0x12160u,0x12c00u,0xc018u,0x122b0u,0x122f0u,0x12c10u,0xc028u,0x12400u,0x12440u,0x12c20u,0xc038u,0x12590u,0x125d0u,0x12c30u,0xc048u,0x12760u,0x12720u,0x12c40u,0xc058u,0x128f0u,0x128b0u,0x12c50u,0xc068u,0x12a40u,0x12a00u,0x12c60u,0xc078u,0x12bd0u,0x12b90u,0x12c70u,0xc088u,0x12d20u,0x12d60u,0x12c80u,0xc098u,0x12eb0u,0x12ef0u,0x12c90u,0xc0a8u,0x13000u,0x13040u,0x12ca0u,0xc0b8u,0x13190u,0x131d0u,0x12cb0u,0xc0c8u,0x13360u,0x13320u,0x12cc0u,0xc0d8u,0x134f0u,0x134b0u,0x12cd0u,0xc0e8u,0x13640u,0x13600u,0x12ce0u,0xc0f8u,0x137d0u,0x13790u,0x12cf0u,0xc308u,0x12130u,0x12168u,0x12d00u,0xc318u,0x122a0u,0x122f8u,0x12d10u,0xc328u,0x12410u,0x12448u,0x12d20u,0xc338u,0x12580u,0x125d8u,0x12d30u,0xc348u,0x12770u,0x12728u,0x12d40u,0xc358u,0x128e0u,0x128b8u,0x12d50u,0xc368u,0x12a50u,0x12a08u,0x12d60u,0xc378u,0x12bc0u,0x12b98u,0x12d70u,0xc388u,0x12d30u,0x12d68u,0x12d80u,0xc398u,0x12ea0u,0x12ef8u,0x12d90u,0xc3a8u,0x13010u,0x13048u,0x12da0u,0xc3b8u,0x13180u,0x131d8u,0x12db0u,0xc3c8u,0x13370u,0x13328u,0x12dc0u,0xc3d8u,0x134e0u,0x134b8u,0x12dd0u,0xc3e8u,0x13650u,0x13608u,0x12de0u,0xc3f8u,0x137c0u,0x13798u,0x12df0u,0xc608u,0x12140u,0x12170u,0x12e00u,0xc618u,0x122d0u,0x122e0u,0x12e10u,0xc628u,0x12460u,0x12450u,0x12e20u,0xc638u,0x125f0u,0x125c0u,0x12e30u,0xc648u,0x12700u,0x12730u,0x12e40u,0xc658u,0x12890u,0x128a0u,0x12e50u,0xc668u,0x12a20u,0x12a10u,0x12e60u,0xc678u,0x12bb0u,0x12b80u,0x12e70u,0xc688u,0x12d40u,0x12d70u,0x12e80u,0xc698u,0x12ed0u,0x12ee0u,0x12e90u,0xc6a8u,0x13060u,0x13050u,0x12ea0u,0xc6b8u,0x131f0u,0x131c0u,0x12eb0u,0xc6c8u,0x13300u,0x13330u,0x12ec0u,0xc6d8u,0x13490u,0x134a0u,0x12ed0u,0xc6e8u,0x13620u,0x13610u,0x12ee0u,0xc6f8u,0x137b0u,0x13780u,0x12ef0u,0xc908u,0x12150u,0x12178u,0x12f00u,0xc918u,0x122c0u,0x122e8u,0x12f10u,0xc928u,0x12470u,0x12458u,0x12f20u,0xc938u,0x125e0u,0x125c8u,0x12f30u,0xc948u,0x12710u,0x12738u,0x12f40u,0xc958u,0x12880u,0x128a8u,0x12f50u,0xc968u,0x12a30u,0x12a18u,0x12f60u,0xc978u,0x12ba0u,0x12b88u,0x12f70u,0xc988u,0x12d50u,0x12d78u,0x12f80u,0xc998u,0x12ec0u,0x12ee8u,0x12f90u,0xc9a8u,0x13070u,0x13058u,0x12fa0u,0xc9b8u,0x131e0u,0x131c8u,0x12fb0u,0xc9c8u,0x13310u,0x13338u,0x12fc0u,0xc9d8u,0x13480u,0x134a8u,0x12fd0u,0xc9e8u,0x13630u,0x13618u,0x12fe0u,0xc9f8u,0x137a0u,0x13788u,0x12ff0u,0x8000u,0x1c300u,0x1ea40u,0x1ec20u,0x8080u,0x1c310u,0x100000u,0x1ec22u,0x8110u,0x1c320u,0x100000u,0x1ec24u,0x8190u,0x1c330u,0x100000u,0x1ec26u,0x8220u,0x1c340u,0x1ea40u,0x1ec28u,0x82a0u,0x1c350u,0x100000u,0x1ec2au,0x8330u,0x1c360u,0x100000u,0x1ec2cu,0x83b0u,0x1c370u,0x100000u,0x1ec2eu,0x8440u,0x1c380u,0x1ea40u,0x1ec30u,0x84c0u,0x1c390u,0x100000u,0x1ec32u,0x8550u,0x1c3a0u,0x100000u,0x1ec34u,0x85d0u,0x1c3b0u,0x100000u,0x1ec36u,0x8660u,0x1c3c0u,0x1ea40u,0x1ec38u,0x86e0u,0x1c3d0u,0x100000u,0x1ec3au,0x8770u,0x1c3e0u,0x100000u,0x1ec3cu,0x87f0u,0x1c3f0u,0x100000u,0x1ec3eu,0x8010u,0x1c400u,0x1ea70u,0x1ec20u,0x8090u,0x1c410u,0x100000u,0x1ec22u,0x8100u,0x1c420u,0x100000u,0x1ec24u,0x8180u,0x1c430u,0x100000u,0x1ec26u,0x8230u,0x1c440u,0x1ea70u,0x1ec28u,0x82b0u,0x1c450u,0x100000u,0x1ec2au,0x8320u,0x1c460u,0x100000u,0x1ec2cu,0x83a0u,0x1c470u,0x100000u,0x1ec2eu,0x8450u,0x1c480u,0x1ea70u,0x1ec30u,0x84d0u,0x1c490u,0x100000u,0x1ec32u,0x8540u,0x1c4a0u,0x100000u,0x1ec34u,0x85c0u,0x1c4b0u,0x100000u,0x1ec36u,0x8670u,0x1c4c0u,0x1ea70u,0x1ec38u,0x86f0u,0x1c4d0u,0x100000u,0x1ec3au,0x8760u,0x1c4e0u,0x100000u,0x1ec3cu,0x87e0u,0x1c4f0u,0x100000u,0x1ec3eu,0x8020u,0x1c500u,0x1eaa0u,0x1ec20u,0x80a0u,0x1c510u,0x100000u,0x1ec22u,0x8130u,0x1c520u,0x100000u,0x1ec24u,0x81b0u,0x1c530u,0x100000u,0x1ec26u,0x8200u,0x1c540u,0x1eaa0u,0x1ec28u,0x8280u,0x1c550u,0x100000u,0x1ec2au,0x8310u,0x1c560u,0x100000u,0x1ec2cu,0x8390u,0x1c570u,0x100000u,0x1ec2eu,0x8460u,0x1c580u,0x1eaa0u,0x1ec30u,0x84e0u,0x1c590u,0x100000u,0x1ec32u,0x8570u,0x1c5a0u,0x100000u,0x1ec34u,0x85f0u,0x1c5b0u,0x100000u,0x1ec36u,0x8640u,0x1c5c0u,0x1eaa0u,0x1ec38u,0x86c0u,0x1c5d0u,0x100000u,0x1ec3au,0x8750u,0x1c5e0u,0x100000u,0x1ec3cu,0x87d0u,0x1c5f0u,0x100000u,0x1ec3eu,0x8030u,0x1c600u,0x1ead0u,0x1ec20u,0x80b0u,0x1c610u,0x100000u,0x1ec22u,0x8120u,0x1c620u,0x100000u,0x1ec24u,0x81a0u,0x1c630u,0x100000u,0x1ec26u,0x8210u,0x1c640u,0x1ead0u,0x1ec28u,0x8290u,0x1c650u,0x100000u,0x1ec2au,0x8300u,0x1c660u,0x100000u,0x1ec2cu,0x8380u,0x1c670u,0x100000u,0x1ec2eu,0x8470u,0x1c680u,0x1ead0u,0x1ec30u,0x84f0u,0x1c690u,0x100000u,0x1ec32u,0x8560u,0x1c6a0u,0x100000u,0x1ec34u,0x85e0u,0x1c6b0u,0x100000u,0x1ec36u,0x8650u,0x1c6c0u,0x1ead0u,0x1ec38u,0x86d0u,0x1c6d0u,0x100000u,0x1ec3au,0x8740u,0x1c6e0u,0x100000u,0x1ec3cu,0x87c0u,0x1c6f0u,0x100000u,0x1ec3eu,0x27000u,0x1d480u,0x1d480u,0x80000000u,0x27002u,0x1d482u,0x100000u,0x80000000u,0x27004u,0x1d484u,0x100000u,0x80000000u,0x27006u,0x1d486u,0x100000u,0x80000000u,0x27200u,0x1d490u,0x1d480u,0x80000000u,0x27202u,0x1d492u,0x100000u,0x80000000u,0x27204u,0x1d494u,0x100000u,0x80000000u,0x27206u,0x1d496u,0x100000u,0x80000000u,0x27400u,0x1d4a0u,0x1d480u,0x80000000u,0x27402u,0x1d4a2u,0x100000u,0x80000000u,0x27404u,0x1d4a4u,0x100000u,0x80000000u,0x27406u,0x1d4a6u,0x100000u,0x80000000u,0x27600u,0x1d4b0u,0x1d480u,0x80000000u,0x27602u,0x1d4b2u,0x100000u,0x80000000u,0x27604u,0x1d4b4u,0x100000u,0x80000000u,0x27606u,0x1d4b6u,0x100000u,0x80000000u,0x27008u,0x1d488u,0x1d490u,0x80000000u,0x2700au,0x1d48au,0x100000u,0x80000000u,0x2700cu,0x1d48cu,0x100000u,0x80000000u,0x2700eu,0x1d48eu,0x100000u,0x80000000u,0x27208u,0x1d498u,0x1d490u,0x80000000u,0x2720au,0x1d49au,0x100000u,0x80000000u,0x2720cu,0x1d49cu,0x100000u,0x80000000u,0x2720eu,0x1d49eu,0x100000u,0x80000000u,0x27408u,0x1d4a8u,0x1d490u,0x80000000u,0x2740au,0x1d4aau,0x100000u,0x80000000u,0x2740cu,0x1d4acu,0x100000u,0x80000000u,0x2740eu,0x1d4aeu,0x100000u,0x80000000u,0x27608u,0x1d4b8u,0x1d490u,0x80000000u,0x2760au,0x1d4bau,0x100000u,0x80000000u,0x2760cu,0x1d4bcu,0x100000u,0x80000000u,0x2760eu,0x1d4beu,0x100000u,0x80000000u,0x26808u,0x1d4c0u,0x1d4a0u,0x80000000u,0x2680au,0x1d4c2u,0x100000u,0x80000000u,0x2680cu,0x1d4c4u,0x100000u,0x80000000u,0x2680eu,0x1d4c6u,0x100000u,0x80000000u,0x26a08u,0x1d4d0u,0x1d4a0u,0x80000000u,0x26a0au,0x1d4d2u,0x100000u,0x80000000u,0x26a0cu,0x1d4d4u,0x100000u,0x80000000u,0x26a0eu,0x1d4d6u,0x100000u,0x80000000u,0x26c08u,0x1d4e0u,0x1d4a0u,0x80000000u,0x26c0au,0x1d4e2u,0x100000u,0x80000000u,0x26c0cu,0x1d4e4u,0x100000u,0x80000000u,0x26c0eu,0x1d4e6u,0x100000u,0x80000000u,0x26e08u,0x1d4f0u,0x1d4a0u,0x80000000u,0x26e0au,0x1d4f2u,0x100000u,0x80000000u,0x26e0cu,0x1d4f4u,0x100000u,0x80000000u,0x26e0eu,0x1d4f6u,0x100000u,0x80000000u,0x100000u,0x1d4c8u,0x1d4b0u,0x80000000u,0x100000u,0x1d4cau,0x100000u,0x80000000u,0x100000u,0x1d4ccu,0x100000u,0x80000000u,0x100000u,0x1d4ceu,0x100000u,0x80000000u,0x100000u,0x1d4d8u,0x1d4b0u,0x80000000u,0x100000u,0x1d4dau,0x100000u,0x80000000u,0x100000u,0x1d4dcu,0x100000u,0x80000000u,0x100000u,0x1d4deu,0x100000u,0x80000000u,0x100000u,0x1d4e8u,0x1d4b0u,0x80000000u,0x100000u,0x1d4eau,0x100000u,0x80000000u,0x100000u,0x1d4ecu,0x100000u,0x80000000u,0x100000u,0x1d4eeu,0x100000u,0x80000000u,0x100000u,0x1d4f8u,0x1d4b0u,0x80000000u,0x100000u,0x1d4fau,0x100000u,0x80000000u,0x100000u,0x1d4fcu,0x100000u,0x80000000u,0x100000u,0x1d4feu,0x100000u,0x80000000u,0x30000u,0x60420u,0xbfc50u,0xf0000u,0x30010u,0x60430u,0xbfc40u,0xf0010u,0x30020u,0x60400u,0xbfc70u,0xf0020u,0x30030u,0x60410u,0xbfc60u,0xf0030u,0x30040u,0x60460u,0xbfc10u,0xf0040u,0x30050u,0x60470u,0xbfc00u,0xf0050u,0x30060u,0x60440u,0xbfc30u,0xf0060u,0x30070u,0x60450u,0xbfc20u,0xf0070u,0x30080u,0x604b0u,0xbfcd0u,0xf0080u,0x30090u,0x604a0u,0xbfcc0u,0xf0090u,0x300a0u,0x60490u,0xbfcf0u,0xf00a0u,0x300b0u,0x60480u,0xbfce0u,0xf00b0u,0x300c0u,0x604f0u,0xbfc90u,0xf00c0u,0x300d0u,0x604e0u,0xbfc80u,0xf00d0u,0x300e0u,0x604d0u,0xbfcb0u,0xf00e0u,0x300f0u,0x604c0u,0xbfca0u,0xf00f0u,0x30100u,0x60530u,0xbfd60u,0xf0100u,0x30110u,0x60520u,0xbfd70u,0xf0110u,0x30120u,0x60510u,0xbfd40u,0xf0120u,0x30130u,0x60500u,0xbfd50u,0xf0130u,0x30140u,0x60570u,0xbfd20u,0xf0140u,0x30150u,0x60560u,0xbfd30u,0xf0150u,0x30160u,0x60550u,0xbfd00u,0xf0160u,0x30170u,0x60540u,0xbfd10u,0xf0170u,0x30190u,0x605b0u,0xbfde0u,0xf0190u,0x30180u,0x605a0u,0xbfdf0u,0xf0180u,0x301b0u,0x60590u,0xbfdc0u,0xf01b0u,0x301a0u,0x60580u,0xbfdd0u,0xf01a0u,0x301d0u,0x605f0u,0xbfda0u,0xf01d0u,0x301c0u,0x605e0u,0xbfdb0u,0xf01c0u,0x301f0u,0x605d0u,0xbfd80u,0xf01f0u,0x301e0u,0x605c0u,0xbfd90u,0xf01e0u,0x30210u,0x60640u,0xbfe60u,0xf0210u,0x30200u,0x60650u,0xbfe70u,0xf0200u,0x30230u,0x60660u,0xbfe40u,0xf0230u,0x30220u,0x60670u,0xbfe50u,0xf0220u,0x30250u,0x60600u,0xbfe20u,0xf0250u,0x30240u,0x60610u,0xbfe30u,0xf0240u,0x30270u,0x60620u,0xbfe00u,0xf0270u,0x30260u,0x60630u,0xbfe10u,0xf0260u,0x30290u,0x606c0u,0xbfef0u,0xf0290u,0x30280u,0x606d0u,0xbfee0u,0xf0280u,0x302b0u,0x606e0u,0xbfed0u,0xf02b0u,0x302a0u,0x606f0u,0xbfec0u,0xf02a0u,0x302d0u,0x60680u,0xbfeb0u,0xf02d0u,0x302c0u,0x60690u,0xbfea0u,0xf02c0u,0x302f0u,0x606a0u,0xbfe90u,0xf02f0u,0x302e0u,0x606b0u,0xbfe80u,0xf02e0u,0x30320u,0x60740u,0xbff70u,0x11f720u,0x30330u,0x60750u,0xbff60u,0x11f730u,0x30300u,0x60760u,0xbff50u,0x11f700u,0x30310u,0x60770u,0xbff40u,0x11f710u,0x30360u,0x60700u,0xbff30u,0x11f760u,0x30370u,0x60710u,0xbff20u,0x11f770u,0x30340u,0x60720u,0xbff10u,0x11f740u,0x30350u,0x60730u,0xbff00u,0x11f750u,0x303a0u,0x8fbd0u,0xbfff0u,0x11f7a0u,0x303b0u,0x8fbc0u,0xbffe0u,0x11f7b0u,0x30380u,0x8fbf0u,0xbffd0u,0x11f780u,0x30390u,0x8fbe0u,0xbffc0u,0x11f790u,0x303e0u,0x8fb90u,0xbffb0u,0x11f7e0u,0x303f0u,0x8fb80u,0xbffa0u,0x11f7f0u,0x303c0u,0x8fbb0u,0xbff90u,0x11f7c0u,0x303d0u,0x8fba0u,0xbff80u,0x11f7d0u,0x0u,0x80000000u,0x0u,0x36030u,0x0u,0x80000000u,0x0u,0x36030u,0x0u,0x80000000u,0x0u,0x36030u,0x0u,0x80000000u,0x0u,0x36030u,0x0u,0x80000000u,0x0u,0x36030u,0x0u,0x80000000u,0x0u,0x36030u,0x0u,0x80000000u,0x0u,0x36030u,0x0u,0x80000000u,0x0u,0x36030u,0x0u,0x80000000u,0x0u,0x360b2u,0x0u,0x80000000u,0x0u,0x360b2u,0x0u,0x80000000u,0x0u,0x360b2u,0x0u,0x80000000u,0x0u,0x360b2u,0x0u,0x80000000u,0x0u,0x360b2u,0x0u,0x80000000u,0x0u,0x360b2u,0x0u,0x80000000u,0x0u,0x360b2u,0x0u,0x80000000u,0x0u,0x360b2u,0x0u,0x80000000u,0x0u,0x36092u,0x0u,0x80000000u,0x0u,0x36092u,0x0u,0x80000000u,0x0u,0x36092u,0x0u,0x80000000u,0x0u,0x36092u,0x0u,0x80000000u,0x0u,0x36092u,0x0u,0x80000000u,0x0u,0x36092u,0x0u,0x80000000u,0x0u,0x36092u,0x0u,0x80000000u,0x0u,0x36092u,0x0u,0x80000000u,0x0u,0x3609au,0x0u,0x80000000u,0x0u,0x3609au,0x0u,0x80000000u,0x0u,0x3609au,0x0u,0x80000000u,0x0u,0x3609au,0x0u,0x80000000u,0x0u,0x3609au,0x0u,0x80000000u,0x0u,0x3609au,0x0u,0x80000000u,0x0u,0x3609au,0x0u,0x80000000u,0x0u,0x3609au,0x0u,0x80000000u,0x0u,0x36059u,0x0u,0x80000000u,0x0u,0x36059u,0x0u,0x80000000u,0x0u,0x36059u,0x0u,0x80000000u,0x0u,0x36059u,0x0u,0x80000000u,0x0u,0x36059u,0x0u,0x80000000u,0x0u,0x36059u,0x0u,0x80000000u,0x0u,0x36059u,0x0u,0x80000000u,0x0u,0x36059u,0x0u,0x80000000u,0x0u,0x36059u,0x0u,0x80000000u,0x0u,0x36059u,0x0u,0x80000000u,0x0u,0x36059u,0x0u,0x80000000u,0x0u,0x36059u,0x0u,0x80000000u,0x0u,0x36059u,0x0u,0x80000000u,0x0u,0x36059u,0x0u,0x80000000u,0x0u,0x36059u,0x0u,0x80000000u,0x0u,0x36059u,0x0u,0x80000000u,0x0u,0x16071u,0x0u,0x80000000u,0x0u,0x16071u,0x0u,0x80000000u,0x0u,0x16071u,0x0u,0x80000000u,0x0u,0x16071u,0x0u,0x80000000u,0x0u,0x16071u,0x0u,0x80000000u,0x0u,0x16071u,0x0u,0x80000000u,0x0u,0x16071u,0x0u,0x80000000u,0x0u,0x16071u,0x0u,0x80000000u,0x0u,0x14071u,0x0u,0x80000000u,0x0u,0x14071u,0x0u,0x80000000u,0x0u,0x14071u,0x0u,0x80000000u,0x0u,0x14071u,0x0u,0x80000000u,0x0u,0x14071u,0x0u,0x80000000u,0x0u,0x14071u,0x0u,0x80000000u,0x0u,0x14071u,0x0u,0x80000000u,0x0u,0x14071u};

__global__ __launch_bounds__(256, 1) void swin_main(const uint16_t* x, const uint16_t* mask, char* ws, uint16_t* out,
                                                    const uint16_t* wqkv, const uint16_t* wo, const uint16_t* ls,
                                                    const uint16_t* w1, const uint16_t* w2, const uint16_t* rc,
                                                    const int* ridx) {
  __shared__ __attribute__((aligned(16))) char smem[LDS_BYTES];
  const int wave = __builtin_amdgcn_readfirstlane(threadIdx.x >> 6);
  const unsigned lds0 = (unsigned)(size_t)smem;
  asm volatile(
      ".p2align 5\n"
      "s_mov_b64 s[92:93], %0\n"
      "s_mov_b32 s40, %1\n"
      "s_mov_b32 s41, %2\n"
      "s_mov_b64 s[88:89], %3\n"
      "s_load_dwordx2 s[16:17], s[92:93], 0\n"
      "s_load_dwordx2 s[20:21], s[92:93], 32\n"
      "s_load_dwordx2 s[46:47], s[92:93], 40\n"
      "s_load_dwordx8 s[80:87], s[92:93], 48\n"
      "s_load_dwordx2 s[76:77], s[92:93], 80\n"
      "s_mov_b64 s[90:91], s[92:93]\n"
      "s_getpc_b64 s[98:99]\n"
      "L_ipf0_%=:\n"
      "v_mbcnt_lo_u32_b32 v240, -1, 0\n"
      "v_mbcnt_hi_u32_b32 v240, -1, v240\n"
      "v_lshlrev_b32 v8, 2, v240\n"
      "v_lshlrev_b32 v154, 4, v240\n"
      "s_cmp_lt_u32 s41, 209\n"
      "s_cselect_b32 s44, 20, 19\n"
      "s_lshl_b32 s92, s40, 8\n"
      "v_add_u32_e64 v53, s92, v8\n"
      "v_min_u32 v29, 704, v53\n"
      "v_add_u32 v29, 4096, v29\n"
      "v_min_u32 v53, 672, v53\n"
      "v_add_u32 v61, 122112, v53\n"
      "s_lshr_b32 s92, s41, 3\n"
      "s_lshl_b32 s92, s92, 7\n"
      "v_lshlrev_b32 v239, 7, v240\n"
      "s_mul_i32 s93, s92, 18\n"
      "s_mul_i32 s94, s40, 640\n"
      "v_min_u32 v241, 512, v239\n"
      "v_add_u32 v241, s94, v241\n"
      "v_min_u32 v241, 2176, v241\n"
      "v_add_u32 v241, s93, v241\n"
      "s_cmp_eq_u32 s40, 1\n"
      "s_cselect_b32 s93, 4096, 0\n"
      "s_add_u32 s92, s92, s93\n"
      "s_cmp_eq_u32 s40, 2\n"
      "s_cselect_b32 s93, 9, 8\n"
      "s_cselect_b64 s[72:73], s[98:99], s[88:89]\n"
      "s_cmp_lt_u32 s40, 2\n"
      "s_cselect_b32 s93, 27, s93\n"
      "s_mul_i32 s92, s92, s93\n"
      "s_sub_u32 s93, s93, 1\n"
      "s_lshl_b32 s93, s93, 7\n"
      "v_min_u32 v239, s93, v239\n"
      "v_add_u32 v239, s92, v239\n"
      "s_lshl_b32 s95, s40, 3\n"
      "v_lshrrev_b32 v238, 3, v240\n"
      "v_and_b32 v194, 7, v240\n"
      "v_add_u32 v197, s95, v238\n"
      "v_bfe_u32 v195, v197, 1, 3\n"
      "v_cmp_lt_u32 s[70:71], s44, v197\n"
      "v_min_u32_e64 v197, s44, v197\n"
      "s_mov_b32 s18, 100270464\n"
      "s_mov_b32 s19, 0x20000\n"
      "s_mov_b32 s78, 9604\n"
      "s_mov_b32 s79, 0x20000\n"
      "s_mov_b32 s22, 221184\n"
      "s_mov_b32 s23, 0x20000\n"
      "s_lshl_b32 s64, s40, 10\n"
      "v_add_u32 v36, s64, v154\n"
      "s_add_u32 s75, s64, 18432\n"
      "s_and_b32 s92, s40, 1\n"
      "s_lshl_b32 s92, s92, 2\n"
      "s_mul_i32 s93, s40, 9216\n"
      "v_and_b32 v196, 2, v238\n"
      "v_xor_b32 v196, v196, v194\n"
      "v_xor_b32_e64 v196, s92, v196\n"
      "v_lshl_add_u32 v196, v196, 4, s93\n"
      "v_mul_u32_u24 v120, 1152, v238\n"
      "v_add_u32 v120, v120, v196\n"
      "s_cmp_lt_u32 s40, 2\n"
      "s_cselect_b32 s65, s64, 163840\n"
      "s_waitcnt lgkmcnt(0)\n"
      "s_cselect_b64 s[72:73], s[20:21], s[72:73]\n"
      "global_load_dword v53, v53, s[86:87]\n"
      "global_load_dword a255, v239, s[72:73]\n"
      "global_load_dword a253, v241, s[46:47]\n"
      "s_mul_i32 s92, s40, 8192\n"
      "s_add_u32 s88, s88, s92\n"
      "s_addc_u32 s89, s89, 0\n"
      "s_add_u32 m0, s64, 124928\n"
      "s_movk_i32 s92, 0\n"
      "buffer_load_dwordx4 v36, s[76:79], s92 offen  lds\n"
      "s_add_u32 m0, s64, 129024\n"
      "s_movk_i32 s92, 4096\n"
      "buffer_load_dwordx4 v36, s[76:79], s92 offen  lds\n"
      "s_add_u32 m0, s64, 133120\n"
      "s_cmp_lt_u32 s40, 2\n"
      "s_cselect_b32 m0, m0, s65\n"
      "s_movk_i32 s92, 8192\n"
      "buffer_load_dwordx4 v36, s[76:79], s92 offen  lds\n"
      "s_load_dwordx16 s[0:15], s[82:83], 0\n"
      "s_load_dwordx16 s[48:63], s[84:85], 0\n"
      "s_load_dwordx16 s[24:39], s[84:85], 64\n"
      "s_load_dwordx2 s[66:67], s[80:81], 0\n"
      "s_movk_i32 s93, 4180\n"
      "s_cmp_lt_u32 s41, 209\n"
      "s_cselect_b32 s92, 209, 47\n"
      "s_cselect_b32 s93, 795, s93\n"
      "s_cmp_lt_u32 s41, 252\n"
      "s_cselect_b32 s94, 16, 15\n"
      "s_add_u32 s93, s93, s41\n"
      "v_lshl_add_u32 v241, v197, 8, s41\n"
      "v_mul_u32_u24 v239, s92, v197\n"
      "v_add_u32 v239, s93, v239\n"
      "v_cmp_lt_u32 vcc, s94, v197\n"
      "v_subrev_u32 v196, 5184, v239\n"
      "s_nop 0\n"
      "v_cndmask_b32 v241, v241, v239, vcc\n"
      "v_cmp_le_u32 vcc, 5184, v241\n"
      "v_mul_u32_u24 v52, 58255, v241\n"
      "v_lshrrev_b32 v52, 22, v52\n"
      "v_mul_u32_u24 v44, 72, v52\n"
      "v_sub_u32 v44, v241, v44\n"
      "v_min_u32 v239, 72, v196\n"
      "v_cndmask_b32 v52, v52, v239, vcc\n"
      "v_subrev_u32 v239, 72, v196\n"
      "v_min_u32 v239, 72, v239\n"
      "v_cndmask_b32 v44, v44, v239, vcc\n"
      "v_mad_u32_u24 v239, v52, 7, 9\n"
      "v_subrev_u32 v241, 0x1ff, v239\n"
      "v_min_u32 v239, v239, v241\n"
      "v_mad_u32_u24 v241, v44, 7, 9\n"
      "v_subrev_u32 v196, 0x1ff, v241\n"
      "v_min_u32 v241, v241, v196\n"
      "v_mul_u32_u24 v239, 0x1ff, v239\n"
      "v_add_u32 v239, v239, v241\n"
      "v_mul_u32_u24 v241, 0x180, v239\n"
      "v_bfrev_b32 v197, 1\n"
      "v_cndmask_b32 v241, v241, v197, s[70:71]\n"
      "v_xor_b32 v195, v195, v194\n"
      "v_lshl_add_u32 v241, v195, 4, v241\n"
      "s_add_u32 m0, s64, 149504\n"
      "s_nop 0\n"
      "buffer_load_dwordx4 v241, s[16:19], 0 offen  lds\n"
      "s_add_u32 m0, s64, 153472\n"
      "s_nop 0\n"
      "buffer_load_dwordx4 v241, s[16:19], 0 offen offset:128  lds\n"
      "s_add_u32 m0, s64, 157440\n"
      "s_nop 0\n"
      "buffer_load_dwordx4 v241, s[16:19], 0 offen offset:256  lds\n"
      "s_movk_i32 s95, 504\n"
      "s_add_u32 s92, s40, 0\n"
      "s_mul_i32 s93, s92, 43\n"
      "s_lshr_b32 s93, s93, 8\n"
      "s_mul_i32 s94, s93, 6\n"
      "s_sub_u32 s92, s92, s94\n"
      "s_lshl_b32 s92, s92, 3\n"
      "s_lshl_b32 s93, s93, 7\n"
      "v_add_u32 v239, s92, v238\n"
      "v_bfe_u32 v241, v239, 1, 3\n"
      "v_xor_b32 v241, v241, v194\n"
      "v_lshl_add_u32 v241, v241, 4, s93\n"
      "v_mul_u32_u24 v197, 37, v239\n"
      "v_lshrrev_b32_e64 v197, 8, v197\n"
      "v_mad_u32_u24 v16, v197, s95, v239\n"
      "v_mul_u32_u24 v16, 384, v16\n"
      "v_add_u32 v16, v16, v241\n"
      "s_add_u32 s92, s40, 4\n"
      "s_mul_i32 s93, s92, 43\n"
      "s_lshr_b32 s93, s93, 8\n"
      "s_mul_i32 s94, s93, 6\n"
      "s_sub_u32 s92, s92, s94\n"
      "s_lshl_b32 s92, s92, 3\n"
      "s_lshl_b32 s93, s93, 7\n"
      "v_add_u32 v241, s92, v238\n"
      "v_bfe_u32 v239, v241, 1, 3\n"
      "v_xor_b32 v239, v239, v194\n"
      "v_lshl_add_u32 v239, v239, 4, s93\n"
      "v_mul_u32_u24 v197, 37, v241\n"
      "v_lshrrev_b32 v197, 8, v197\n"
      "v_mad_u32_u24 v17, v197, s95, v241\n"
      "v_mul_u32_u24 v17, 384, v17\n"
      "v_add_u32 v17, v17, v239\n"
      "s_add_u32 s92, s40, 8\n"
      "s_mul_i32 s93, s92, 43\n"
      "s_lshr_b32 s93, s93, 8\n"
      "s_mul_i32 s94, s93, 6\n"
      "s_sub_u32 s92, s92, s94\n"
      "s_lshl_b32 s92, s92, 3\n"
      "s_lshl_b32 s93, s93, 7\n"
      "v_add_u32_e64 v239, s92, v238\n"
      "v_bfe_u32 v241, v239, 1, 3\n"
      "v_xor_b32 v241, v241, v194\n"
      "v_lshl_add_u32 v241, v241, 4, s93\n"
      "v_mul_u32_u24 v197, 37, v239\n"
      "v_lshrrev_b32_e64 v197, 8, v197\n"
      "v_mad_u32_u24 v18, v197, s95, v239\n"
      "v_mul_u32_u24 v18, 384, v18\n"
      "v_add_u32 v18, v18, v241\n"
      "s_add_u32 s92, s40, 12\n"
      "s_mul_i32 s93, s92, 43\n"
      "s_lshr_b32 s93, s93, 8\n"
      "s_mul_i32 s94, s93, 6\n"
      "s_sub_u32 s92, s92, s94\n"
      "s_lshl_b32 s92, s92, 3\n"
      "s_lshl_b32 s93, s93, 7\n"
      "v_add_u32 v241, s92, v238\n"
      "v_bfe_u32 v239, v241, 1, 3\n"
      "v_xor_b32 v239, v239, v194\n"
      "v_lshl_add_u32 v239, v239, 4, s93\n"
      "v_mul_u32_u24 v197, 37, v241\n"
      "v_lshrrev_b32 v197, 8, v197\n"
      "v_mad_u32_u24 v19, v197, s95, v241\n"
      "v_mul_u32_u24 v19, 384, v19\n"
      "v_add_u32 v19, v19, v239\n"
      "s_add_u32 s92, s40, 16\n"
      "s_mul_i32 s93, s92, 43\n"
      "s_lshr_b32 s93, s93, 8\n"
      "s_mul_i32 s94, s93, 6\n"
      "s_sub_u32 s92, s92, s94\n"
      "s_lshl_b32 s92, s92, 3\n"
      "s_lshl_b32 s93, s93, 7\n"
      "s_cmp_lt_u32 s40, 2\n"
      "s_cselect_b32 s93, s93, 0x80000000\n"
      "v_add_u32 v239, s92, v238\n"
      "v_bfe_u32 v238, v239, 1, 3\n"
      "v_xor_b32_e64 v238, v238, v194\n"
      "v_lshl_add_u32 v238, v238, 4, s93\n"
      "v_mul_u32_u24_e64 v241, 37, v239\n"
      "v_lshrrev_b32_e64 v241, 8, v241\n"
      "v_mad_u32_u24 v15, v241, s95, v239\n"
      "v_mul_u32_u24 v15, 384, v15\n"
      "v_add_u32 v15, v15, v238\n"
      "s_add_u32 s92, s41, 0\n"
      "s_mul_i32 s93, s92, 58255\n"
      "s_lshr_b32 s93, s93, 22\n"
      "s_add_u32 s47, s92, s93\n"
      "s_mul_i32 s47, s47, 4802\n"
      "s_and_b32 s47, s47, -4\n"
      "s_mul_i32 s93, s93, 439\n"
      "s_add_u32 s45, s92, s93\n"
      "s_mul_i32 s45, s45, 2688\n"
      "s_add_u32 s45, s45, 589824\n"
      "s_add_u32 s92, s41, 256\n"
      "s_mul_i32 s93, s92, 58255\n"
      "s_lshr_b32 s93, s93, 22\n"
      "s_add_u32 s74, s92, s93\n"
      "s_mul_i32 s74, s74, 4802\n"
      "s_and_b32 s74, s74, -4\n"
      "s_mul_i32 s93, s93, 439\n"
      "s_add_u32 s46, s92, s93\n"
      "s_mul_i32 s46, s46, 2688\n"
      "s_add_u32 s46, s46, 589824\n"
      "s_add_u32 m0, s64, 0\n"
      "s_nop 0\n"
      "buffer_load_dwordx4 v16, s[16:19], s45 offen  lds\n"
      "s_add_u32 m0, s64, 4096\n"
      "s_nop 0\n"
      "buffer_load_dwordx4 v17, s[16:19], s45 offen  lds\n"
      "s_add_u32 m0, s64, 8192\n"
      "s_nop 0\n"
      "buffer_load_dwordx4 v18, s[16:19], s45 offen  lds\n"
      "s_add_u32 m0, s64, 12288\n"
      "s_nop 0\n"
      "buffer_load_dwordx4 v19, s[16:19], s45 offen  lds\n"
      "s_add_u32 m0, s65, 16384\n"
      "s_nop 0\n"
      "buffer_load_dwordx4 v15, s[16:19], s45 offen  lds\n"
      "v_subrev_u32 v241, 2, v240\n"
      "v_cmp_ge_u32 s[72:73], s44, v241\n"
      "v_min_u32_e64 v241, s44, v241\n"
      "s_movk_i32 s93, 4180\n"
      "s_cmp_lt_u32 s41, 209\n"
      "s_cselect_b32 s92, 209, 47\n"
      "s_cselect_b32 s93, 795, s93\n"
      "s_cmp_lt_u32 s41, 252\n"
      "s_cselect_b32 s94, 16, 15\n"
      "s_add_u32 s93, s93, s41\n"
      "v_lshl_add_u32 v239, v241, 8, s41\n"
      "v_mul_u32_u24 v238, s92, v241\n"
      "v_add_u32_e64 v238, s93, v238\n"
      "v_cmp_lt_u32 vcc, s94, v241\n"
      "v_subrev_u32 v194, 5184, v238\n"
      "s_nop 0\n"
      "v_cndmask_b32 v239, v239, v238, vcc\n"
      "v_cmp_le_u32 vcc, 5184, v239\n"
      "v_mul_u32_u24 v197, 58255, v239\n"
      "v_lshrrev_b32 v197, 22, v197\n"
      "v_mul_u32_u24 v195, 72, v197\n"
      "v_sub_u32_e64 v195, v239, v195\n"
      "v_min_u32 v238, 72, v194\n"
      "v_cndmask_b32 v197, v197, v238, vcc\n"
      "v_subrev_u32 v238, 72, v194\n"
      "v_min_u32 v238, 72, v238\n"
      "v_cndmask_b32 v195, v195, v238, vcc\n"
      "v_mul_u32_u24 v38, 73, v197\n"
      "v_add_u32 v38, v38, v195\n"
      "v_mul_u32_u24 v38, 4802, v38\n"
      "v_mul_u32_u24 v241, 0x1ff, v197\n"
      "v_add_u32_e64 v241, v241, v195\n"
      "v_mul_u32_u24 v241, 2688, v241\n"
      "v_add_u32 v241, 589824, v241\n"
      "v_cmp_eq_u32 vcc, 72, v197\n"
      "v_or_b32 v52, 0x80000000, v241\n"
      "s_nop 0\n"
      "v_cndmask_b32 v241, v241, v52, vcc\n"
      "v_cmp_eq_u32 vcc, 72, v195\n"
      "v_or_b32_e64 v52, 0x40000000, v241\n"
      "s_nop 0\n"
      "v_cndmask_b32 v241, v241, v52, vcc\n"
      "v_mov_b32 v238, 0x20000000\n"
      "v_cndmask_b32 v241, v238, v241, s[72:73]\n"
      "v_cndmask_b32 v38, v238, v38, s[72:73]\n"
      "v_mov_b32 v37, v238\n"
      "s_nop 1\n"
      "v_mov_b32_dpp v37, v241 wave_shr:1 row_mask:0xf bank_mask:0xf\n"
      "v_mov_b32 v79, 0\n"
      "v_mov_b32 v83, 0\n"
      "v_mov_b32 v87, 0\n"
      "v_mov_b32 v91, 0\n"
      "v_mov_b32 v95, 0\n"
      "s_lshl_b32 s94, s40, 2\n"
      "s_add_u32 s94, s94, 115456\n"
      "v_add_u32 v31, s94, v154\n"
      "v_and_b32 v52, 3, v240\n"
      "v_cmp_ne_u32 vcc, 0, v52\n"
      "v_mov_b32 v52, 0x3f803f80\n"
      "s_nop 0\n"
      "v_cndmask_b32_e64 v32, v52, 0, vcc\n"
      "v_mov_b32 v33, v32\n"
      "v_mov_b32 v34, v32\n"
      "v_mov_b32 v35, v32\n"
      "v_and_b32 v43, 15, v240\n"
      "v_lshrrev_b32_e64 v69, 4, v240\n"
      "v_mul_u32_u24 v43, 196, v43\n"
      "v_add_u32 v43, 124928, v43\n"
      "v_lshl_add_u32 v69, v69, 4, v43\n"
      "v_min_u32 v50, 192, v8\n"
      "v_add_u32 v50, 124928, v50\n"
      "global_load_dwordx4 v[238:241], v154, s[88:89]\n"
      "s_waitcnt lgkmcnt(0)\n"
      "s_pack_ll_b32_b16 s92, s0, s8\n"
      "s_waitcnt vmcnt(14)\n"
      "v_dot2_f32_bf16 v44, s92, v53, 0\n"
      "s_pack_hh_b32_b16 s93, s0, s8\n"
      "v_dot2_f32_bf16 v56, s93, v53, 0\n"
      "s_pack_ll_b32_b16 s94, s1, s9\n"
      "v_dot2_f32_bf16 v63, s94, v53, 0\n"
      "s_pack_hh_b32_b16 s95, s1, s9\n"
      "v_dot2_f32_bf16 v57, s95, v53, 0\n"
      "s_pack_ll_b32_b16 s92, s2, s10\n"
      "v_dot2_f32_bf16 v54, s92, v53, 0\n"
      "s_pack_hh_b32_b16 s93, s2, s10\n"
      "v_dot2_f32_bf16 v74, s93, v53, 0\n"
      "s_pack_ll_b32_b16 s94, s3, s11\n"
      "v_dot2_f32_bf16 v71, s94, v53, 0\n"
      "s_pack_hh_b32_b16 s95, s3, s11\n"
      "v_dot2_f32_bf16 v60, s95, v53, 0\n"
      "s_pack_ll_b32_b16 s92, s4, s12\n"
      "v_dot2_f32_bf16 v62, s92, v53, 0\n"
      "s_pack_hh_b32_b16 s93, s4, s12\n"
      "v_dot2_f32_bf16 v58, s93, v53, 0\n"
      "s_pack_ll_b32_b16 s94, s5, s13\n"
      "v_dot2_f32_bf16 v55, s94, v53, 0\n"
      "global_load_dwordx4 v[194:197], v154, s[88:89] offset:1024\n"
      "s_pack_hh_b32_b16 s95, s5, s13\n"
      "v_dot2_f32_bf16 v45, s95, v53, 0\n"
      "s_pack_ll_b32_b16 s92, s6, s14\n"
      "v_dot2_f32_bf16 v75, s92, v53, 0\n"
      "s_pack_hh_b32_b16 s93, s6, s14\n"
      "v_dot2_f32_bf16 v46, s93, v53, 0\n"
      "s_pack_ll_b32_b16 s94, s7, s15\n"
      "v_dot2_f32_bf16 v66, s94, v53, 0\n"
      "s_pack_hh_b32_b16 s95, s7, s15\n"
      "v_dot2_f32_bf16 v64, s95, v53, 0\n"
      "s_lshl_b32 s92, s40, 4\n"
      "s_lshr_b64 s[66:67], s[66:67], s92\n"
      "s_lshl_b32 s66, s66, 16\n"
      "v_mov_b32 v53, s66\n"
      "v_max_f32 v53, 0xc2c80000, v53\n"
      "v_min_f32 v53, 0x40930000, v53\n"
      "v_mul_f32 v53, 0x3fb8aa3b, v53\n"
      "v_exp_f32 v53, v53\n"
      "s_nop 0\n"
      "v_cvt_pk_bf16_f32 v53, v53, v53\n"
      "v_lshlrev_b32 v53, 16, v53\n"
      "v_max_f32 v44, 0, v44\n"
      "v_max_f32_e64 v56, 0, v56\n"
      "global_load_dwordx4 v[0:3], v154, s[88:89] offset:2048\n"
      "v_cvt_pk_bf16_f32 v44, v44, v56\n"
      "v_max_f32 v63, 0, v63\n"
      "v_max_f32 v57, 0, v57\n"
      "v_cvt_pk_bf16_f32 v63, v63, v57\n"
      "v_max_f32 v54, 0, v54\n"
      "v_max_f32 v74, 0, v74\n"
      "v_cvt_pk_bf16_f32 v54, v54, v74\n"
      "v_max_f32 v71, 0, v71\n"
      "v_max_f32 v60, 0, v60\n"
      "v_cvt_pk_bf16_f32 v71, v71, v60\n"
      "v_max_f32 v62, 0, v62\n"
      "v_max_f32 v58, 0, v58\n"
      "v_cvt_pk_bf16_f32 v62, v62, v58\n"
      "v_max_f32 v55, 0, v55\n"
      "v_max_f32 v45, 0, v45\n"
      "v_cvt_pk_bf16_f32 v55, v55, v45\n"
      "v_max_f32 v75, 0, v75\n"
      "v_max_f32 v46, 0, v46\n"
      "v_cvt_pk_bf16_f32 v75, v75, v46\n"
      "v_max_f32 v66, 0, v66\n"
      "v_max_f32 v64, 0, v64\n"
      "v_cvt_pk_bf16_f32 v66, v66, v64\n"
      "s_mov_b32 s42, 0\n"
      "s_add_u32 m0, s75, 0\n"
      "s_nop 0\n"
      "buffer_load_dwordx4 v120, s[20:23], s42 offen  lds\n"
      "s_pack_ll_b32_b16 s92, s48, s50\n"
      "v_dot2_f32_bf16 v60, s92, v44, 0\n"
      "s_pack_hh_b32_b16 s93, s48, s50\n"
      "v_dot2_f32_bf16 v45, s93, v44, 0\n"
      "s_pack_ll_b32_b16 s94, s49, s51\n"
      "v_dot2_f32_bf16 v46, s94, v44, 0\n"
      "s_pack_hh_b32_b16 s95, s49, s51\n"
      "v_dot2_f32_bf16 v74, s95, v44, 0\n"
      "s_pack_ll_b32_b16 s92, s52, s54\n"
      "v_dot2_f32_bf16 v60, s92, v63, v60\n"
      "s_pack_hh_b32_b16 s93, s52, s54\n"
      "v_dot2_f32_bf16 v45, s93, v63, v45\n"
      "s_pack_ll_b32_b16 s94, s53, s55\n"
      "v_dot2_f32_bf16 v46, s94, v63, v46\n"
      "s_pack_hh_b32_b16 s95, s53, s55\n"
      "v_dot2_f32_bf16 v74, s95, v63, v74\n"
      "s_pack_ll_b32_b16 s92, s56, s58\n"
      "v_dot2_f32_bf16 v60, s92, v54, v60\n"
      "s_pack_hh_b32_b16 s93, s56, s58\n"
      "v_dot2_f32_bf16 v45, s93, v54, v45\n"
      "s_pack_ll_b32_b16 s94, s57, s59\n"
      "v_dot2_f32_bf16 v46, s94, v54, v46\n"
      "s_add_u32 m0, s75, 3968\n"
      "s_nop 0\n"
      "buffer_load_dwordx4 v120, s[20:23], s42 offen offset:128  lds\n"
      "s_pack_hh_b32_b16 s95, s57, s59\n"
      "v_dot2_f32_bf16 v74, s95, v54, v74\n"
      "s_pack_ll_b32_b16 s92, s60, s62\n"
      "v_dot2_f32_bf16 v60, s92, v71, v60\n"
      "s_pack_hh_b32_b16 s93, s60, s62\n"
      "v_dot2_f32_bf16 v45, s93, v71, v45\n"
      "s_pack_ll_b32_b16 s94, s61, s63\n"
      "v_dot2_f32_bf16 v46, s94, v71, v46\n"
      "s_pack_hh_b32_b16 s95, s61, s63\n"
      "v_dot2_f32_bf16 v74, s95, v71, v74\n"
      "s_pack_ll_b32_b16 s92, s24, s26\n"
      "v_dot2_f32_bf16 v60, s92, v62, v60\n"
      "s_pack_hh_b32_b16 s93, s24, s26\n"
      "v_dot2_f32_bf16 v45, s93, v62, v45\n"
      "s_pack_ll_b32_b16 s94, s25, s27\n"
      "v_dot2_f32_bf16 v46, s94, v62, v46\n"
      "s_pack_hh_b32_b16 s95, s25, s27\n"
      "v_dot2_f32_bf16 v74, s95, v62, v74\n"
      "s_pack_ll_b32_b16 s92, s28, s30\n"
      "v_dot2_f32_bf16 v60, s92, v55, v60\n"
      "s_pack_hh_b32_b16 s93, s28, s30\n"
      "v_dot2_f32_bf16 v45, s93, v55, v45\n"
      "s_pack_ll_b32_b16 s94, s29, s31\n"
      "s_add_u32 m0, s75, 7936\n"
      "s_nop 0\n"
      "buffer_load_dwordx4 v120, s[20:23], s42 offen offset:256  lds\n"
      "v_dot2_f32_bf16 v46, s94, v55, v46\n"
      "s_pack_hh_b32_b16 s95, s29, s31\n"
      "v_dot2_f32_bf16 v74, s95, v55, v74\n"
      "s_pack_ll_b32_b16 s92, s32, s34\n"
      "v_dot2_f32_bf16 v60, s92, v75, v60\n"
      "s_pack_hh_b32_b16 s93, s32, s34\n"
      "v_dot2_f32_bf16 v45, s93, v75, v45\n"
      "s_pack_ll_b32_b16 s94, s33, s35\n"
      "v_dot2_f32_bf16 v46, s94, v75, v46\n"
      "s_pack_hh_b32_b16 s95, s33, s35\n"
      "v_dot2_f32_bf16 v74, s95, v75, v74\n"
      "s_pack_ll_b32_b16 s92, s36, s38\n"
      "v_dot2_f32_bf16 v60, s92, v66, v60\n"
      "s_pack_hh_b32_b16 s93, s36, s38\n"
      "v_dot2_f32_bf16 v45, s93, v66, v45\n"
      "s_pack_ll_b32_b16 s94, s37, s39\n"
      "v_dot2_f32_bf16 v46, s94, v66, v46\n"
      "s_pack_hh_b32_b16 s95, s37, s39\n"
      "v_dot2_f32_bf16 v74, s95, v66, v74\n"
      "s_load_dwordx2 s[28:29], s[90:91], 8\n"
      "s_load_dwordx2 s[34:35], s[90:91], 16\n"
      "s_load_dwordx2 s[24:25], s[90:91], 24\n"
      "s_add_u32 m0, s75, 11904\n"
      "s_nop 0\n"
      "buffer_load_dwordx4 v120, s[20:23], s42 offen offset:384  lds\n"
      "s_load_dwordx2 s[36:37], s[90:91], 40\n"
      "s_mov_b32 s26, 100270464\n"
      "s_mov_b32 s27, 0x20000\n"
      "s_mov_b32 s38, 73728\n"
      "s_mov_b32 s39, 0x20000\n"
      "s_mov_b32 s30, 25589860\n"
      "s_mov_b32 s31, 0x20000\n"
      "s_mov_b32 s53, 0x3fb8aa3b\n"
      "s_mov_b32 s51, 0x2a8cbccc\n"
      "s_mov_b32 s52, 0x38fba882\n"
      "s_mov_b32 s58, 0xffff0000\n"
      "s_mov_b32 s59, 0x0\n"
      "s_mov_b32 s60, 0x0\n"
      "s_mov_b32 s61, 0xffff\n"
      "s_mov_b32 s62, 0x0\n"
      "s_mov_b32 s63, 0xffff0000\n"
      "v_mov_b32 v52, 0x41f00000\n"
      "v_cmp_nle_f32 s[54:55], v53, v52\n"
      "v_mul_f32 v53, s53, v53\n"
      "s_nop 0\n"
      "v_readfirstlane_b32 s50, v53\n"
      "v_cvt_pk_bf16_f32 v60, v60, v60\n"
      "v_lshlrev_b32 v60, 16, v60\n"
      "s_add_u32 m0, s75, 15872\n"
      "s_nop 0\n"
      "buffer_load_dwordx4 v120, s[20:23], s42 offen offset:512  lds\n"
      "v_mul_f32 v60, 0xbfb8aa3b, v60\n"
      "v_exp_f32 v60, v60\n"
      "s_nop 0\n"
      "v_add_f32 v60, 1.0, v60\n"
      "v_rcp_f32 v60, v60\n"
      "s_nop 0\n"
      "v_cvt_pk_bf16_f32 v60, v60, v60\n"
      "v_lshlrev_b32 v60, 16, v60\n"
      "v_add_f32 v60, -0.5, v60\n"
      "v_mul_f32 v60, 0x41b8aa3b, v60\n"
      "ds_write_b32 v61, v60\n"
      "v_cvt_pk_bf16_f32 v45, v45, v45\n"
      "v_lshlrev_b32 v45, 16, v45\n"
      "v_mul_f32 v45, 0xbfb8aa3b, v45\n"
      "v_exp_f32 v45, v45\n"
      "s_nop 0\n"
      "v_add_f32 v45, 1.0, v45\n"
      "v_rcp_f32_e64 v45, v45\n"
      "s_nop 0\n"
      "v_cvt_pk_bf16_f32 v45, v45, v45\n"
      "v_lshlrev_b32 v45, 16, v45\n"
      "v_add_f32 v45, -0.5, v45\n"
      "v_mul_f32 v45, 0x41b8aa3b, v45\n"
      "ds_write_b32 v61, v45 offset:704\n"
      "v_cvt_pk_bf16_f32 v46, v46, v46\n"
      "v_lshlrev_b32 v46, 16, v46\n"
      "s_add_u32 m0, s75, 19840\n"
      "s_nop 0\n"
      "buffer_load_dwordx4 v120, s[20:23], s42 offen offset:640  lds\n"
      "v_mul_f32 v46, 0xbfb8aa3b, v46\n"
      "v_exp_f32 v46, v46\n"
      "s_nop 0\n"
      "v_add_f32 v46, 1.0, v46\n"
      "v_rcp_f32_e64 v46, v46\n"
      "s_nop 0\n"
      "v_cvt_pk_bf16_f32 v46, v46, v46\n"
      "v_lshlrev_b32 v46, 16, v46\n"
      "v_add_f32 v46, -0.5, v46\n"
      "v_mul_f32 v46, 0x41b8aa3b, v46\n"
      "ds_write_b32 v61, v46 offset:1408\n"
      "v_cvt_pk_bf16_f32 v74, v74, v74\n"
      "v_lshlrev_b32 v74, 16, v74\n"
      "v_mul_f32 v74, 0xbfb8aa3b, v74\n"
      "v_exp_f32 v74, v74\n"
      "s_nop 0\n"
      "v_add_f32 v74, 1.0, v74\n"
      "v_rcp_f32 v74, v74\n"
      "s_nop 0\n"
      "v_cvt_pk_bf16_f32 v74, v74, v74\n"
      "v_lshlrev_b32 v74, 16, v74\n"
      "v_add_f32 v74, -0.5, v74\n"
      "v_mul_f32 v74, 0x41b8aa3b, v74\n"
      "ds_write_b32 v61, v74 offset:2112\n"
      "s_mul_i32 s76, s40, 704\n"
      "s_add_u32 s76, s76, 122112\n"
      "s_waitcnt vmcnt(17)\n"
      "s_waitcnt lgkmcnt(0)\n"
      "s_barrier\n"
      "global_load_dwordx4 v[4:7], v154, s[88:89] offset:3072\n"
      "v_add_u32 v153, 6272, v69\n"
      "v_add_u32 v71, 3136, v69\n"
      "s_mov_b32 s72, 0xffff\n"
      "ds_read2_b32 v[54:55], v71 offset0:2 offset1:3\n"
      "ds_read2_b32 v[52:53], v71 offset0:0 offset1:1\n"
      "ds_read2_b32 v[60:61], v71 offset0:32 offset1:33\n"
      "ds_read2_b32 v[62:63], v71 offset0:34 offset1:35\n"
      "s_add_u32 s88, s88, 4096\n"
      "s_addc_u32 s89, s89, 0\n"
      "global_load_dwordx4 v[8:11], v154, s[88:89]\n"
      "ds_read_b32 v244, v43 offset:6464\n"
      "ds_read_b32 v237, v43 offset:192\n"
      "ds_read2_b32 v[56:57], v71 offset0:16 offset1:17\n"
      "ds_read2_b32 v[44:45], v69 offset0:16 offset1:17\n"
      "ds_read2_b32 v[74:75], v153 offset0:34 offset1:35\n"
      "ds_read2_b32 v[46:47], v69 offset0:18 offset1:19\n"
      "ds_read_b32 v155, v50 offset:9408\n"
      "global_load_dwordx4 v[12:15], v154, s[88:89] offset:1024\n"
      "ds_read2_b32 v[50:51], v69 offset0:34 offset1:35\n"
      "ds_read2_b32 v[66:67], v153 offset0:2 offset1:3\n"
      "ds_read2_b32 v[58:59], v71 offset0:18 offset1:19\n"
      "ds_read2_b32 v[72:73], v153 offset0:32 offset1:33\n"
      "ds_read2_b32 v[70:71], v153 offset0:18 offset1:19\n"
      "ds_read2_b32 v[48:49], v69 offset0:32 offset1:33\n"
      "ds_read2_b32 v[64:65], v153 offset0:0 offset1:1\n"
      "global_load_dwordx4 v[20:23], v154, s[88:89] offset:2048\n"
      "ds_read2_b32 v[40:41], v69 offset0:0 offset1:1\n"
      "ds_read_b32 v234, v43 offset:3328\n"
      "ds_read2_b32 v[42:43], v69 offset0:2 offset1:3\n"
      "ds_read2_b32 v[68:69], v153 offset0:16 offset1:17\n"
      "s_waitcnt lgkmcnt(11)\n"
      "v_lshl_add_u32 v46, v46, 2, s76\n"
      "v_lshl_add_u32 v44, v44, 2, s76\n"
      "global_load_dwordx4 v[24:27], v154, s[88:89] offset:3072\n"
      "v_lshl_add_u32 v61, v61, 2, s76\n"
      "v_lshl_add_u32 v54, v54, 2, s76\n"
      "v_lshl_add_u32 v55, v55, 2, s76\n"
      "v_lshl_add_u32 v53, v53, 2, s76\n"
      "v_lshl_add_u32 v45, v45, 2, s76\n"
      "v_lshl_add_u32 v244, v244, 2, s76\n"
      "v_lshl_add_u32 v62, v62, 2, s76\n"
      "s_mov_b32 s43, 36864\n"
      "s_add_u32 m0, s75, 24576\n"
      "s_nop 0\n"
      "buffer_load_dwordx4 v120, s[20:23], s43 offen  lds\n"
      "v_lshl_add_u32 v74, v74, 2, s76\n"
      "v_lshl_add_u32 v237, v237, 2, s76\n"
      "v_lshl_add_u32 v75, v75, 2, s76\n"
      "s_mov_b32 s66, 0xffffffff\n"
      "ds_read_b32 v54, v54\n"
      "ds_read_b32 v237, v237\n"
      "s_add_u32 m0, s75, 28544\n"
      "s_nop 0\n"
      "buffer_load_dwordx4 v120, s[20:23], s43 offen offset:128  lds\n"
      "ds_read_b32 v75, v75\n"
      "ds_read_b32 v244, v244\n"
      "v_lshl_add_u32 v155, v155, 2, s76\n"
      "s_waitcnt lgkmcnt(4)\n"
      "v_lshl_add_u32 v70, v70, 2, s76\n"
      "v_lshl_add_u32 v41, v41, 2, s76\n"
      "v_lshl_add_u32 v56, v56, 2, s76\n"
      "s_add_u32 m0, s75, 32512\n"
      "s_nop 0\n"
      "buffer_load_dwordx4 v120, s[20:23], s43 offen offset:256  lds\n"
      "v_lshl_add_u32 v64, v64, 2, s76\n"
      "v_lshl_add_u32 v58, v58, 2, s76\n"
      "v_lshl_add_u32 v48, v48, 2, s76\n"
      "v_lshl_add_u32 v52, v52, 2, s76\n"
      "v_lshl_add_u32 v234, v234, 2, s76\n"
      "v_lshl_add_u32 v66, v66, 2, s76\n"
      "v_lshl_add_u32 v68, v68, 2, s76\n"
      "s_add_u32 m0, s75, 36480\n"
      "s_nop 0\n"
      "buffer_load_dwordx4 v120, s[20:23], s43 offen offset:384  lds\n"
      "v_lshl_add_u32 v51, v51, 2, s76\n"
      "v_lshl_add_u32 v67, v67, 2, s76\n"
      "v_lshl_add_u32 v65, v65, 2, s76\n"
      "v_lshl_add_u32 v49, v49, 2, s76\n"
      "v_lshl_add_u32 v42, v42, 2, s76\n"
      "v_lshl_add_u32 v47, v47, 2, s76\n"
      "v_lshl_add_u32 v59, v59, 2, s76\n"
      "s_add_u32 m0, s75, 40448\n"
      "s_nop 0\n"
      "buffer_load_dwordx4 v120, s[20:23], s43 offen offset:512  lds\n"
      "v_lshl_add_u32 v50, v50, 2, s76\n"
      "v_lshl_add_u32 v72, v72, 2, s76\n"
      "v_lshl_add_u32 v71, v71, 2, s76\n"
      "v_lshl_add_u32 v63, v63, 2, s76\n"
      "v_lshl_add_u32 v60, v60, 2, s76\n"
      "v_lshl_add_u32 v40, v40, 2, s76\n"
      "v_lshl_add_u32 v57, v57, 2, s76\n"
      "s_add_u32 m0, s75, 44416\n"
      "s_nop 0\n"
      "buffer_load_dwordx4 v120, s[20:23], s43 offen offset:640  lds\n"
      "v_lshl_add_u32 v73, v73, 2, s76\n"
      "v_lshl_add_u32 v43, v43, 2, s76\n"
      "v_lshl_add_u32 v69, v69, 2, s76\n"
      "s_mov_b32 s67, 0x1ffff\n"
      "ds_read_b32 v59, v59\n"
      "ds_read_b32 v41, v41\n"
      "ds_read_b32 v61, v61\n"
      "s_mov_b32 s42, 73728\n"
      "s_add_u32 m0, s75, 49152\n"
      "s_nop 0\n"
      "buffer_load_dwordx4 v120, s[20:23], s42 offen  lds\n"
      "ds_read_b32 v49, v49\n"
      "ds_read_b32 v48, v48\n"
      "ds_read_b32 v73, v73\n"
      "ds_read_b32 v47, v47\n"
      "ds_read_b32 v155, v155\n"
      "ds_read_b32 v46, v46\n"
      "s_add_u32 m0, s75, 53120\n"
      "s_nop 0\n"
      "buffer_load_dwordx4 v120, s[20:23], s42 offen offset:128  lds\n"
      "ds_read_b32 v55, v55\n"
      "ds_read_b32 v74, v74\n"
      "ds_read_b32 v234, v234\n"
      "ds_read_b32 v62, v62\n"
      "ds_read_b32 v65, v65\n"
      "ds_read_b32 v44, v44\n"
      "ds_read_b32 v63, v63\n"
      "s_add_u32 m0, s75, 57088\n"
      "s_nop 0\n"
      "buffer_load_dwordx4 v120, s[20:23], s42 offen offset:256  lds\n"
      "ds_read_b32 v53, v53\n"
      "ds_read_b32 v50, v50\n"
      "ds_read_b32 v68, v68\n"
      "ds_read_b32 v70, v70\n"
      "ds_read_b32 v64, v64\n"
      "ds_read_b32 v57, v57\n"
      "ds_read_b32 v45, v45\n"
      "s_add_u32 m0, s75, 61056\n"
      "s_nop 0\n"
      "buffer_load_dwordx4 v120, s[20:23], s42 offen offset:384  lds\n"
      "ds_read_b32 v71, v71\n"
      "ds_read_b32 v69, v69\n"
      "ds_read_b32 v67, v67\n"
      "ds_read_b32 v58, v58\n"
      "ds_read_b32 v52, v52\n"
      "ds_read_b32 v40, v40\n"
      "ds_read_b32 v51, v51\n"
      "s_add_u32 m0, s75, 65024\n"
      "s_nop 0\n"
      "buffer_load_dwordx4 v120, s[20:23], s42 offen offset:512  lds\n"
      "ds_read_b32 v60, v60\n"
      "ds_read_b32 v43, v43\n"
      "ds_read_b32 v72, v72\n"
      "ds_read_b32 v56, v56\n"
      "ds_read_b32 v42, v42\n"
      "ds_read_b32 v66, v66\n"
      "v_mov_b32 v154, 0xf149f2ca\n"
      "s_add_u32 m0, s75, 68992\n"
      "s_nop 0\n"
      "buffer_load_dwordx4 v120, s[20:23], s42 offen offset:640  lds\n"
      "s_mov_b32 s73, 0x0\n"
      "s_waitcnt lgkmcnt(10)\n"
      "v_cndmask_b32 v30, v154, v155, s[66:67]\n"
      "v_cndmask_b32 v28, v154, v244, s[72:73]\n"
      "v_cndmask_b32 v155, v154, v237, s[72:73]\n"
      "v_cndmask_b32 v237, v154, v234, s[72:73]\n"
      "s_mov_b32 s45, s46\n"
      "s_mov_b32 s49, s47\n"
      "s_mov_b32 s66, 0\n"
      "s_mov_b32 s67, 0\n"
      "s_mov_b32 s68, 0\n"
      "s_mov_b32 s69, 0\n"
      "s_mov_b32 s70, 0\n"
      "s_mov_b32 s71, 0\n"
      "s_mov_b32 s48, 0\n"
      "s_mov_b32 s33, 0x7fff7fff\n"
      "s_mov_b32 s42, 1\n"
      "s_mov_b32 s43, 5\n"
      "v_readlane_b32 s47, v38, 4\n"
      "s_nop 0\n"
      "s_and_b32 s47, s47, -4\n"
      "s_waitcnt vmcnt(23)\n"
      "v_add_u32 v85, 149504, v0\n"
      "v_add_u32 v254, 149504, v1\n"
      "v_add_u32 v138, 49152, v238\n"
      "v_add_u32 v136, 49152, v239\n"
      "v_add_u32 v139, 49152, v240\n"
      "v_add_u32 v78, 49152, v241\n"
      "v_add_u32 v82, 49152, v194\n"
      "s_waitcnt vmcnt(12)\n"
      "v_mov_b32 v24, v155\n"
      "v_mov_b32_e64 v26, v237\n"
      "s_waitcnt lgkmcnt(0)\n"
      "s_barrier\n"
      "s_add_u32 m0, s75, 73728\n"
      "ds_read_b128 a[228:231], v0 offset:4096\n"
      "s_mov_b32 s79, 110592\n"
      "buffer_load_dwordx4 v120, s[20:23], s79 offen  lds\n"
      "ds_read_b64_tr_b16 a[122:123], v240 offset:12800\n"
      "ds_read_b64_tr_b16 a[120:121], v240 offset:12288\n"
      "ds_read_b64_tr_b16 a[50:51], v240 offset:512\n"
      "ds_read_b128 a[216:219], v85\n"
      "ds_read_b64_tr_b16 a[72:73], v238 offset:12288\n"
      "ds_read_b64_tr_b16 a[74:75], v238 offset:12800\n"
      "s_add_u32 m0, s75, 77696\n"
      "ds_read_b128 a[220:223], v0\n"
      "ds_read_b128 a[240:243], v1 offset:2048\n"
      "buffer_load_dwordx4 v120, s[20:23], s79 offen offset:128  lds\n"
      "s_add_u32 m0, s75, 81664\n"
      "ds_read_b64_tr_b16 a[98:99], v239 offset:12800\n"
      "ds_read_b64_tr_b16 a[48:49], v240\n"
      "ds_read_b128 a[224:227], v0 offset:2048\n"
      "ds_read_b64_tr_b16 a[24:25], v239\n"
      "ds_read_b64_tr_b16 a[96:97], v239 offset:12288\n"
      "s_waitcnt lgkmcnt(9)\n"
      "v_mfma_f32_16x16x32_bf16 v[234:237], a[120:123], a[216:219], 0\n"
      "buffer_load_dwordx4 v120, s[20:23], s79 offen offset:256  lds\n"
      "s_waitcnt lgkmcnt(7)\n"
      "v_mfma_f32_16x16x32_bf16 v[152:155], a[72:75], a[228:231], 0\n"
      "ds_read_b64_tr_b16 a[26:27], v239 offset:512\n"
      "s_add_u32 m0, s75, 85632\n"
      "v_mfma_f32_16x16x32_bf16 v[242:245], a[72:75], a[216:219], 0\n"
      "ds_read_b128 a[252:255], v0 offset:6144\n"
      "v_mfma_f32_16x16x32_bf16 v[128:131], a[120:123], a[228:231], 0\n"
      "ds_read_b128 a[236:239], v1\n"
      "s_waitcnt lgkmcnt(6)\n"
      "v_mfma_f32_16x16x32_bf16 v[174:177], a[120:123], a[220:223], 0\n"
      "buffer_load_dwordx4 v120, s[20:23], s79 offen offset:384  lds\n"
      "ds_read_b64_tr_b16 a[0:1], v238\n"
      "v_mfma_f32_16x16x32_bf16 v[188:191], a[48:51], a[220:223], 0\n"
      "s_add_u32 m0, s75, 89600\n"
      "ds_read_b64_tr_b16 a[2:3], v238 offset:512\n"
      "v_mfma_f32_16x16x32_bf16 v[202:205], a[72:75], a[220:223], 0\n"
      "ds_read_b128 a[232:235], v254\n"
      "s_waitcnt lgkmcnt(5)\n"
      "v_mfma_f32_16x16x32_bf16 v[108:111], a[120:123], a[224:227], 0\n"
      "buffer_load_dwordx4 v120, s[20:23], s79 offen offset:512  lds\n"
      "v_mfma_f32_16x16x32_bf16 v[140:143], a[48:51], a[224:227], 0\n"
      "s_add_u32 m0, s75, 93568\n"
      "v_mfma_f32_16x16x32_bf16 v[230:233], a[48:51], a[216:219], 0\n"
      "ds_read_b128 a[248:251], v85 offset:4096\n"
      "v_mfma_f32_16x16x32_bf16 v[214:217], a[96:99], a[228:231], 0\n"
      "ds_read_b128 a[244:247], v1 offset:4096\n"
      "v_readlane_b32 s85, v37, s42\n"
      "s_waitcnt lgkmcnt(3)\n"
      "v_mfma_f32_16x16x32_bf16 v[144:147], a[0:3], a[220:223], 0\n"
      "buffer_load_dwordx4 v120, s[20:23], s79 offen offset:640  lds\n"
      "v_mfma_f32_16x16x32_bf16 v[124:127], a[0:3], a[228:231], 0\n"
      "s_waitcnt vmcnt(12)\n"
      "s_barrier\n"
      "ds_read_b64_tr_b16 a[30:31], v239 offset:25088\n"
      "v_mfma_f32_16x16x32_bf16 v[246:249], a[0:3], a[216:219], 0\n"
      "s_add_u32 m0, s75, 0\n"
      "ds_read_b64_tr_b16 a[124:125], v240 offset:36864\n"
      "ds_read_b64_tr_b16 a[28:29], v239 offset:24576\n"
      "s_mov_b32 s78, 147456\n"
      "v_mfma_f32_16x16x32_bf16 v[206:209], a[72:75], a[224:227], 0\n"
      "buffer_load_dwordx4 v120, s[20:23], s78 offen  lds\n"
      "ds_read_b64_tr_b16 a[78:79], v238 offset:37376\n"
      "v_mfma_f32_16x16x32_bf16 v[226:229], a[24:27], a[224:227], 0\n"
      "ds_read_b64_tr_b16 a[4:5], v238 offset:24576\n"
      "v_mfma_f32_16x16x32_bf16 v[250:253], a[96:99], a[220:223], 0\n"
      "ds_read_b64_tr_b16 a[54:55], v240 offset:25088\n"
      "ds_read_b64_tr_b16 a[6:7], v238 offset:25088\n"
      "v_mfma_f32_16x16x32_bf16 v[160:163], a[96:99], a[216:219], 0\n"
      "s_add_u32 m0, s75, 3968\n"
      "ds_read_b64_tr_b16 a[126:127], v240 offset:37376\n"
      "v_mfma_f32_16x16x32_bf16 v[218:221], a[48:51], a[228:231], 0\n"
      "buffer_load_dwordx4 v120, s[20:23], s78 offen offset:128  lds\n"
      "v_mfma_f32_16x16x32_bf16 v[180:183], a[0:3], a[224:227], 0\n"
      "s_add_u32 m0, s75, 7936\n"
      "v_mfma_f32_16x16x32_bf16 v[166:169], a[24:27], a[220:223], 0\n"
      "ds_read_b64_tr_b16 a[76:77], v238 offset:36864\n"
      "s_waitcnt lgkmcnt(4)\n"
      "v_mfma_f32_16x16x32_bf16 v[166:169], a[28:31], a[236:239], v[166:169]\n"
      "v_mfma_f32_16x16x32_bf16 v[226:229], a[28:31], a[240:243], v[226:229]\n"
      "ds_read_b64_tr_b16 a[52:53], v240 offset:24576\n"
      "v_mfma_f32_16x16x32_bf16 v[132:135], a[24:27], a[216:219], 0\n"
      "buffer_load_dwordx4 v120, s[20:23], s78 offen offset:256  lds\n"
      "v_mfma_f32_16x16x32_bf16 v[132:135], a[28:31], a[232:235], v[132:135]\n"
      "ds_read_b64_tr_b16 a[102:103], v239 offset:37376\n"
      "v_mfma_f32_16x16x32_bf16 v[222:225], a[24:27], a[228:231], 0\n"
      "s_add_u32 m0, s75, 11904\n"
      "v_mfma_f32_16x16x32_bf16 v[222:225], a[28:31], a[244:247], v[222:225]\n"
      "s_waitcnt lgkmcnt(2)\n"
      "v_mfma_f32_16x16x32_bf16 v[108:111], a[124:127], a[240:243], v[108:111]\n"
      "ds_read_b64_tr_b16 a[100:101], v239 offset:36864\n"
      "v_mfma_f32_16x16x32_bf16 v[156:159], a[96:99], a[224:227], 0\n"
      "buffer_load_dwordx4 v120, s[20:23], s78 offen offset:384  lds\n"
      "v_mfma_f32_16x16x32_bf16 v[124:127], a[4:7], a[244:247], v[124:127]\n"
      "v_mfma_f32_16x16x32_bf16 v[152:155], a[76:79], a[244:247], v[152:155]\n"
      "ds_read_b128 a[216:219], v0 offset:8192\n"
      "s_add_u32 m0, s75, 15872\n"
      "v_mfma_f32_16x16x32_bf16 v[144:147], a[4:7], a[236:239], v[144:147]\n"
      "v_mfma_f32_16x16x32_bf16 v[174:177], a[124:127], a[236:239], v[174:177]\n"
      "buffer_load_dwordx4 v120, s[20:23], s78 offen offset:512  lds\n"
      "v_mfma_f32_16x16x32_bf16 v[234:237], a[124:127], a[232:235], v[234:237]\n"
      "s_waitcnt lgkmcnt(1)\n"
      "v_readlane_b32 s84, v37, s43\n"
      "v_mfma_f32_16x16x32_bf16 v[214:217], a[100:103], a[244:247], v[214:217]\n"
      "s_waitcnt vmcnt(11)\n"
      "s_barrier\n"
      "ds_read_b64_tr_b16 a[10:11], v138 offset:512\n"
      "ds_read_b64_tr_b16 a[128:129], v139 offset:12288\n"
      "ds_read_b64_tr_b16 a[80:81], v138 offset:12288\n"
      "ds_read_b64_tr_b16 a[104:105], v136 offset:12288\n"
      "ds_read_b64_tr_b16 a[32:33], v136\n"
      "ds_read_b64_tr_b16 a[58:59], v139 offset:512\n"
      "ds_read_b64_tr_b16 a[56:57], v139\n"
      "ds_read_b64_tr_b16 a[34:35], v136 offset:512\n"
      "ds_read_b64_tr_b16 a[130:131], v139 offset:12800\n"
      "ds_read_b64_tr_b16 a[106:107], v136 offset:12800\n"
      "ds_read_b64_tr_b16 a[82:83], v138 offset:12800\n"
      "ds_read_b64_tr_b16 a[8:9], v138\n"
      "v_mfma_f32_16x16x32_bf16 v[230:233], a[52:55], a[232:235], v[230:233]\n"
      "s_add_u32 m0, s75, 19840\n"
      "s_mov_b32 s79, 184320\n"
      "ds_read_b128 a[220:223], v0 offset:10240\n"
      "v_mfma_f32_16x16x32_bf16 v[156:159], a[100:103], a[240:243], v[156:159]\n"
      "buffer_load_dwordx4 v120, s[20:23], s78 offen offset:640  lds\n"
      "v_mfma_f32_16x16x32_bf16 v[246:249], a[4:7], a[232:235], v[246:249]\n"
      "v_mfma_f32_16x16x32_bf16 v[128:131], a[124:127], a[244:247], v[128:131]\n"
      "v_mfma_f32_16x16x32_bf16 v[202:205], a[76:79], a[236:239], v[202:205]\n"
      "s_add_u32 m0, s75, 24576\n"
      "v_mfma_f32_16x16x32_bf16 v[180:183], a[4:7], a[240:243], v[180:183]\n"
      "buffer_load_dwordx4 v120, s[20:23], s79 offen  lds\n"
      "v_mfma_f32_16x16x32_bf16 v[250:253], a[100:103], a[236:239], v[250:253]\n"
      "s_waitcnt lgkmcnt(0)\n"
      "v_mfma_f32_16x16x32_bf16 v[250:253], a[104:107], a[252:255], v[250:253]\n"
      "s_add_u32 m0, s75, 28544\n"
      "v_mfma_f32_16x16x32_bf16 v[174:177], a[128:131], a[252:255], v[174:177]\n"
      "v_mfma_f32_16x16x32_bf16 v[124:127], a[8:11], a[220:223], v[124:127]\n"
      "buffer_load_dwordx4 v120, s[20:23], s79 offen offset:128  lds\n"
      "v_mfma_f32_16x16x32_bf16 v[242:245], a[76:79], a[232:235], v[242:245]\n"
      "s_add_u32 m0, s75, 32512\n"
      "ds_read_b128 a[224:227], v254 offset:4096\n"
      "v_mfma_f32_16x16x32_bf16 v[242:245], a[80:83], a[248:251], v[242:245]\n"
      "v_mfma_f32_16x16x32_bf16 v[140:143], a[52:55], a[240:243], v[140:143]\n"
      "v_mfma_f32_16x16x32_bf16 v[188:191], a[52:55], a[236:239], v[188:191]\n"
      "v_mfma_f32_16x16x32_bf16 v[188:191], a[56:59], a[252:255], v[188:191]\n"
      "buffer_load_dwordx4 v120, s[20:23], s79 offen offset:256  lds\n"
      "v_mfma_f32_16x16x32_bf16 v[160:163], a[100:103], a[232:235], v[160:163]\n"
      "v_mfma_f32_16x16x32_bf16 v[160:163], a[104:107], a[248:251], v[160:163]\n"
      "v_mfma_f32_16x16x32_bf16 v[206:209], a[76:79], a[240:243], v[206:209]\n"
      "ds_read_b128 a[232:235], v1 offset:8192\n"
      "v_mfma_f32_16x16x32_bf16 v[218:221], a[52:55], a[244:247], v[218:221]\n"
      "ds_read_b128 v[100:103], v85\n"
      "s_add_u32 m0, s75, 36480\n"
      "v_mfma_f32_16x16x32_bf16 v[218:221], a[56:59], a[220:223], v[218:221]\n"
      "buffer_load_dwordx4 v120, s[20:23], s79 offen offset:384  lds\n"
      "v_mfma_f32_16x16x32_bf16 v[166:169], a[32:35], a[252:255], v[166:169]\n"
      "ds_read_b128 a[228:231], v1 offset:6144\n"
      "v_mfma_f32_16x16x32_bf16 v[144:147], a[8:11], a[252:255], v[144:147]\n"
      "v_mfma_f32_16x16x32_bf16 v[152:155], a[80:83], a[220:223], v[152:155]\n"
      "v_mfma_f32_16x16x32_bf16 v[132:135], a[32:35], a[248:251], v[132:135]\n"
      "s_add_u32 m0, s75, 40448\n"
      "s_or_b32 s95, s84, s85\n"
      "v_mfma_f32_16x16x32_bf16 v[108:111], a[128:131], a[216:219], v[108:111]\n"
      "buffer_load_dwordx4 v120, s[20:23], s79 offen offset:512  lds\n"
      "v_mfma_f32_16x16x32_bf16 v[214:217], a[104:107], a[220:223], v[214:217]\n"
      "s_waitcnt vmcnt(11)\n"
      "s_barrier\n"
      "ds_read_b64_tr_b16 a[108:109], v136 offset:36864\n"
      "ds_read_b64_tr_b16 a[84:85], v138 offset:36864\n"
      "ds_read_b64_tr_b16 a[86:87], v138 offset:37376\n"
      "ds_read_b64_tr_b16 a[60:61], v139 offset:24576\n"
      "ds_read_b64_tr_b16 a[14:15], v138 offset:25088\n"
      "ds_read_b64_tr_b16 a[110:111], v136 offset:37376\n"
      "ds_read_b64_tr_b16 a[38:39], v136 offset:25088\n"
      "ds_read_b64_tr_b16 a[134:135], v139 offset:37376\n"
      "ds_read_b64_tr_b16 a[132:133], v139 offset:36864\n"
      "ds_read_b64_tr_b16 a[62:63], v139 offset:25088\n"
      "ds_read_b64_tr_b16 a[12:13], v138 offset:24576\n"
      "ds_read_b64_tr_b16 a[36:37], v136 offset:24576\n"
      "s_add_u32 m0, s75, 44416\n"
      "v_mfma_f32_16x16x32_bf16 v[140:143], a[56:59], a[216:219], v[140:143]\n"
      "buffer_load_dwordx4 v120, s[20:23], s79 offen offset:640  lds\n"
      "v_mfma_f32_16x16x32_bf16 v[202:205], a[80:83], a[252:255], v[202:205]\n"
      "s_add_u32 m0, s75, 48384\n"
      "s_mov_b32 s78, 0\n"
      "s_waitcnt lgkmcnt(7)\n"
      "v_mfma_f32_16x16x32_bf16 v[202:205], a[84:87], a[228:231], v[202:205]\n"
      "ds_read_b128 a[236:239], v1 offset:10240\n"
      "v_mfma_f32_16x16x32_bf16 v[234:237], a[128:131], a[248:251], v[234:237]\n"
      "v_mfma_f32_16x16x32_bf16 v[230:233], a[56:59], a[248:251], v[230:233]\n"
      "buffer_load_dwordx4 v120, s[20:23], s78 offen offset:768  lds\n"
      "v_mfma_f32_16x16x32_bf16 v[226:229], a[32:35], a[216:219], v[226:229]\n"
      "s_add_u32 m0, s75, 52352\n"
      "s_waitcnt lgkmcnt(1)\n"
      "v_mfma_f32_16x16x32_bf16 v[226:229], a[36:39], a[232:235], v[226:229]\n"
      "v_mfma_f32_16x16x32_bf16 v[108:111], a[132:135], a[232:235], v[108:111]\n"
      "v_mfma_f32_16x16x32_bf16 v[160:163], a[108:111], a[224:227], v[160:163]\n"
      "buffer_load_dwordx4 v120, s[20:23], s78 offen offset:896  lds\n"
      "v_mfma_f32_16x16x32_bf16 v[246:249], a[8:11], a[248:251], v[246:249]\n"
      "s_waitcnt lgkmcnt(0)\n"
      "v_mfma_f32_16x16x32_bf16 v[246:249], a[12:15], a[224:227], v[246:249]\n"
      "v_mfma_f32_16x16x32_bf16 v[214:217], a[108:111], a[236:239], v[214:217]\n"
      "ds_read_b128 a[244:247], v0 offset:12288\n"
      "v_mfma_f32_16x16x32_bf16 v[152:155], a[84:87], a[236:239], v[152:155]\n"
      "v_mfma_f32_16x16x32_bf16 v[156:159], a[104:107], a[216:219], v[156:159]\n"
      "s_add_u32 m0, s75, 56320\n"
      "v_mfma_f32_16x16x32_bf16 v[156:159], a[108:111], a[232:235], v[156:159]\n"
      "buffer_load_dwordx4 v120, s[20:23], s78 offen offset:1024  lds\n"
      "v_mfma_f32_16x16x32_bf16 v[250:253], a[108:111], a[228:231], v[250:253]\n"
      "ds_read_b128 a[240:243], v85 offset:8192\n"
      "s_mov_b32 s78, 36864\n"
      "v_mfma_f32_16x16x32_bf16 v[128:131], a[128:131], a[220:223], v[128:131]\n"
      "v_mfma_f32_16x16x32_bf16 v[206:209], a[80:83], a[216:219], v[206:209]\n"
      "s_add_u32 m0, s75, 60672\n"
      "v_mfma_f32_16x16x32_bf16 v[206:209], a[84:87], a[232:235], v[206:209]\n"
      "buffer_load_dwordx4 v120, s[20:23], s78 offen offset:768  lds\n"
      "v_mfma_f32_16x16x32_bf16 v[242:245], a[84:87], a[224:227], v[242:245]\n"
      "s_add_u32 m0, s75, 64640\n"
      "v_mfma_f32_16x16x32_bf16 v[144:147], a[12:15], a[228:231], v[144:147]\n"
      "ds_read_b128 a[248:251], v0 offset:14336\n"
      "v_mfma_f32_16x16x32_bf16 v[166:169], a[36:39], a[228:231], v[166:169]\n"
      "ds_read_b128 a[252:255], v0 offset:16384\n"
      "v_mfma_f32_16x16x32_bf16 v[222:225], a[32:35], a[220:223], v[222:225]\n"
      "buffer_load_dwordx4 v120, s[20:23], s78 offen offset:896  lds\n"
      "v_mfma_f32_16x16x32_bf16 v[222:225], a[36:39], a[236:239], v[222:225]\n"
      "v_readlane_b32 s86, v38, s43\n"
      "v_mfma_f32_16x16x32_bf16 v[174:177], a[132:135], a[228:231], v[174:177]\n"
      "s_add_u32 m0, s75, 68608\n"
      "v_mfma_f32_16x16x32_bf16 v[132:135], a[36:39], a[224:227], v[132:135]\n"
      "s_waitcnt vmcnt(11)\n"
      "s_barrier\n"
      "ds_read_b64_tr_b16 a[88:89], v238 offset:12288\n"
      "v_mfma_f32_16x16x32_bf16 v[230:233], a[60:63], a[224:227], v[230:233]\n"
      "ds_read_b64_tr_b16 a[64:65], v240\n"
      "ds_read_b64_tr_b16 a[136:137], v240 offset:12288\n"
      "ds_read_b64_tr_b16 a[42:43], v239 offset:512\n"
      "ds_read_b64_tr_b16 a[138:139], v240 offset:12800\n"
      "v_mfma_f32_16x16x32_bf16 v[188:191], a[60:63], a[228:231], v[188:191]\n"
      "ds_read_b64_tr_b16 a[16:17], v238\n"
      "ds_read_b64_tr_b16 a[90:91], v238 offset:12800\n"
      "ds_read_b64_tr_b16 a[66:67], v240 offset:512\n"
      "v_mfma_f32_16x16x32_bf16 v[180:183], a[8:11], a[216:219], v[180:183]\n"
      "ds_read_b64_tr_b16 a[40:41], v239\n"
      "ds_read_b64_tr_b16 a[18:19], v238 offset:512\n"
      "v_mfma_f32_16x16x32_bf16 v[180:183], a[12:15], a[232:235], v[180:183]\n"
      "ds_read_b64_tr_b16 a[114:115], v239 offset:12800\n"
      "ds_read_b64_tr_b16 a[112:113], v239 offset:12288\n"
      "v_mfma_f32_16x16x32_bf16 v[234:237], a[132:135], a[224:227], v[234:237]\n"
      "buffer_load_dwordx4 v120, s[20:23], s78 offen offset:1024  lds\n"
      "v_mfma_f32_16x16x32_bf16 v[128:131], a[132:135], a[236:239], v[128:131]\n"
      "s_add_u32 s43, s43, 1\n"
      "s_mov_b32 s79, 73728\n"
      "v_mfma_f32_16x16x32_bf16 v[218:221], a[60:63], a[236:239], v[218:221]\n"
      "ds_read_b128 a[220:223], v1 offset:12288\n"
      "v_mfma_f32_16x16x32_bf16 v[140:143], a[60:63], a[232:235], v[140:143]\n"
      "s_add_u32 m0, s75, 72960\n"
      "s_waitcnt lgkmcnt(4)\n"
      "v_mfma_f32_16x16x32_bf16 v[140:143], a[64:67], a[248:251], v[140:143]\n"
      "buffer_load_dwordx4 v120, s[20:23], s79 offen offset:768  lds\n"
      "v_mfma_f32_16x16x32_bf16 v[152:155], a[88:91], a[252:255], v[152:155]\n"
      "s_waitcnt lgkmcnt(1)\n"
      "v_mfma_f32_16x16x32_bf16 v[246:249], a[16:19], a[240:243], v[246:249]\n"
      "v_mfma_f32_16x16x32_bf16 v[230:233], a[64:67], a[240:243], v[230:233]\n"
      "s_add_u32 m0, s75, 76928\n"
      "ds_read_b128 a[232:235], v85 offset:2048\n"
      "v_mfma_f32_16x16x32_bf16 v[222:225], a[40:43], a[252:255], v[222:225]\n"
      "buffer_load_dwordx4 v120, s[20:23], s79 offen offset:896  lds\n"
      "v_mfma_f32_16x16x32_bf16 v[202:205], a[88:91], a[244:247], v[202:205]\n"
      "ds_read_b128 a[216:219], v254 offset:8192\n"
      "v_mfma_f32_16x16x32_bf16 v[108:111], a[136:139], a[248:251], v[108:111]\n"
      "s_add_u32 m0, s75, 80896\n"
      "ds_read_b128 a[228:231], v1 offset:16384\n"
      "v_mfma_f32_16x16x32_bf16 v[132:135], a[40:43], a[240:243], v[132:135]\n"
      "v_mfma_f32_16x16x32_bf16 v[174:177], a[136:139], a[244:247], v[174:177]\n"
      "buffer_load_dwordx4 v120, s[20:23], s79 offen offset:1024  lds\n"
      "v_mfma_f32_16x16x32_bf16 v[166:169], a[40:43], a[244:247], v[166:169]\n"
      "s_mov_b32 s79, 110592\n"
      "s_add_u32 m0, s75, 85248\n"
      "v_mfma_f32_16x16x32_bf16 v[160:163], a[112:115], a[240:243], v[160:163]\n"
      "s_waitcnt lgkmcnt(2)\n"
      "v_mfma_f32_16x16x32_bf16 v[124:127], a[12:15], a[236:239], v[124:127]\n"
      "ds_read_b128 a[236:239], v254 offset:2048\n"
      "v_mfma_f32_16x16x32_bf16 v[250:253], a[112:115], a[244:247], v[250:253]\n"
      "buffer_load_dwordx4 v120, s[20:23], s79 offen offset:768  lds\n"
      "v_mfma_f32_16x16x32_bf16 v[234:237], a[136:139], a[240:243], v[234:237]\n"
      "v_mfma_f32_16x16x32_bf16 v[104:107], a[72:75], a[232:235], 0\n"
      "ds_read_b128 a[224:227], v1 offset:14336\n"
      "v_mfma_f32_16x16x32_bf16 v[242:245], a[88:91], a[240:243], v[242:245]\n"
      "s_add_u32 m0, s75, 89216\n"
      "v_mfma_f32_16x16x32_bf16 v[128:131], a[136:139], a[252:255], v[128:131]\n"
      "buffer_load_dwordx4 v120, s[20:23], s79 offen offset:896  lds\n"
      "v_mfma_f32_16x16x32_bf16 v[214:217], a[112:115], a[252:255], v[214:217]\n"
      "v_mfma_f32_16x16x32_bf16 v[180:183], a[16:19], a[248:251], v[180:183]\n"
      "s_waitcnt lgkmcnt(1)\n"
      "v_mfma_f32_16x16x32_bf16 v[112:115], a[48:51], a[232:235], 0\n"
      "s_add_u32 m0, s75, 93184\n"
      "v_mfma_f32_16x16x32_bf16 v[226:229], a[40:43], a[248:251], v[226:229]\n"
      "buffer_load_dwordx4 v120, s[20:23], s79 offen offset:1024  lds\n"
      "v_mfma_f32_16x16x32_bf16 v[124:127], a[16:19], a[252:255], v[124:127]\n"
      "s_waitcnt vmcnt(12)\n"
      "s_barrier\n"
      "ds_read_b64_tr_b16 a[92:93], v238 offset:36864\n"
      "v_mfma_f32_16x16x32_bf16 v[144:147], a[16:19], a[244:247], v[144:147]\n"
      "ds_read_b64_tr_b16 a[68:69], v240 offset:24576\n"
      "ds_read_b64_tr_b16 a[94:95], v238 offset:37376\n"
      "v_mfma_f32_16x16x32_bf16 v[206:209], a[88:91], a[248:251], v[206:209]\n"
      "ds_read_b64_tr_b16 a[20:21], v238 offset:24576\n"
      "ds_read_b64_tr_b16 a[22:23], v238 offset:25088\n"
      "v_mfma_f32_16x16x32_bf16 v[156:159], a[112:115], a[248:251], v[156:159]\n"
      "ds_read_b64_tr_b16 a[70:71], v240 offset:25088\n"
      "ds_read_b64_tr_b16 a[44:45], v239 offset:24576\n"
      "ds_read_b64_tr_b16 a[140:141], v240 offset:36864\n"
      "ds_read_b64_tr_b16 a[142:143], v240 offset:37376\n"
      "v_mfma_f32_16x16x32_bf16 v[188:191], a[64:67], a[244:247], v[188:191]\n"
      "ds_read_b64_tr_b16 a[116:117], v239 offset:36864\n"
      "ds_read_b64_tr_b16 a[118:119], v239 offset:37376\n"
      "ds_read_b64_tr_b16 a[46:47], v239 offset:25088\n"
      "v_mfma_f32_16x16x32_bf16 v[112:115], a[52:55], a[236:239], v[112:115]\n"
      "s_mov_b32 s78, 147456\n"
      "s_add_u32 m0, s75, -768\n"
      "s_waitcnt lgkmcnt(9)\n"
      "v_mfma_f32_16x16x32_bf16 v[202:205], a[92:95], a[220:223], v[202:205]\n"
      "buffer_load_dwordx4 v120, s[20:23], s78 offen offset:768  lds\n"
      "v_mfma_f32_16x16x32_bf16 v[242:245], a[92:95], a[216:219], v[242:245]\n"
      "s_and_b32 s86, s86, -4\n"
      "s_add_u32 m0, s75, 3200\n"
      "s_waitcnt lgkmcnt(5)\n"
      "v_mfma_f32_16x16x32_bf16 v[144:147], a[20:23], a[220:223], v[144:147]\n"
      "ds_read_b128 a[244:247], v254 offset:6144\n"
      "ds_read_b128 a[248:251], v85 offset:10240\n"
      "v_mfma_f32_16x16x32_bf16 v[246:249], a[20:23], a[216:219], v[246:249]\n"
      "s_waitcnt lgkmcnt(4)\n"
      "v_mul_f32 v193, v203, v203\n"
      "v_fma_f32 v184, v242, v242, s51\n"
      "v_mfma_f32_16x16x32_bf16 v[230:233], a[68:71], a[216:219], v[230:233]\n"
      "buffer_load_dwordx4 v120, s[20:23], s78 offen offset:896  lds\n"
      "v_mfma_f32_16x16x32_bf16 v[234:237], a[140:143], a[216:219], v[234:237]\n"
      "s_waitcnt lgkmcnt(2)\n"
      "v_mul_f32 v185, v243, v243\n"
      "v_fmac_f32 v185, v245, v245\n"
      "v_mfma_f32_16x16x32_bf16 v[124:127], a[20:23], a[228:231], v[124:127]\n"
      "s_add_u32 m0, s75, 7168\n"
      "v_mul_f32 v187, v247, v247\n"
      "v_fma_f32 v186, v246, v246, s51\n"
      "v_mfma_f32_16x16x32_bf16 v[174:177], a[140:143], a[220:223], v[174:177]\n"
      "ds_read_b128 a[240:243], v85 offset:6144\n"
      "v_fma_f32 v192, v202, v202, s51\n"
      "v_mfma_f32_16x16x32_bf16 v[218:221], a[64:67], a[252:255], v[218:221]\n"
      "buffer_load_dwordx4 v120, s[20:23], s78 offen offset:1024  lds\n"
      "v_mfma_f32_16x16x32_bf16 v[218:221], a[68:71], a[228:231], v[218:221]\n"
      "s_mov_b32 s78, 184320\n"
      "v_mul_f32 v179, v145, v145\n"
      "v_fmac_f32 v187, v249, v249\n"
      "v_mfma_f32_16x16x32_bf16 v[166:169], a[44:47], a[220:223], v[166:169]\n"
      "v_fma_f32 v178, v144, v144, s51\n"
      "v_mul_f32_e64 v81, v125, v125\n"
      "v_mfma_f32_16x16x32_bf16 v[132:135], a[44:47], a[216:219], v[132:135]\n"
      "s_add_u32 m0, s75, 11520\n"
      "v_fma_f32 v80, v124, v124, s51\n"
      "v_fmac_f32_e64 v186, v248, v248\n"
      "v_mfma_f32_16x16x32_bf16 v[250:253], a[116:119], a[220:223], v[250:253]\n"
      "buffer_load_dwordx4 v120, s[20:23], s78 offen offset:768  lds\n"
      "v_mfma_f32_16x16x32_bf16 v[104:107], a[76:79], a[236:239], v[104:107]\n"
      "s_add_u32 m0, s75, 15488\n"
      "s_or_b32 s95, s95, s48\n"
      "v_fmac_f32 v186, v132, v132\n"
      "v_mfma_f32_16x16x32_bf16 v[160:163], a[116:119], a[216:219], v[160:163]\n"
      "v_fmac_f32 v187, v133, v133\n"
      "v_fmac_f32 v179, v147, v147\n"
      "v_mfma_f32_16x16x32_bf16 v[206:209], a[92:95], a[224:227], v[206:209]\n"
      "ds_read_b128 a[252:255], v254 offset:10240\n"
      "v_fmac_f32 v184, v244, v244\n"
      "v_mfma_f32_16x16x32_bf16 v[188:191], a[68:71], a[220:223], v[188:191]\n"
      "buffer_load_dwordx4 v120, s[20:23], s78 offen offset:896  lds\n"
      "v_fmac_f32 v178, v146, v146\n"
      "v_pk_fma_f32 v[178:179], v[166:167], v[166:167], v[178:179]\n"
      "v_pk_fma_f32 v[186:187], v[134:135], v[134:135], v[186:187]\n"
      "v_pk_fma_f32 v[186:187], v[230:231], v[230:231], v[186:187]\n"
      "v_pk_fma_f32 v[192:193], v[204:205], v[204:205], v[192:193]\n"
      "v_pk_fma_f32 v[184:185], v[160:161], v[160:161], v[184:185]\n"
      "v_pk_fma_f32 v[184:185], v[162:163], v[162:163], v[184:185]\n"
      "v_pk_fma_f32 v[184:185], v[234:235], v[234:235], v[184:185]\n"
      "v_pk_fma_f32 v[186:187], v[232:233], v[232:233], v[186:187]\n"
      "v_pk_fma_f32 v[80:81], v[126:127], v[126:127], v[80:81]\n"
      "v_pk_fma_f32 v[184:185], v[236:237], v[236:237], v[184:185]\n"
      "v_add_f32 v184, v184, v185\n"
      "v_add_f32 v186, v186, v187\n"
      "v_pk_fma_f32 v[192:193], v[250:251], v[250:251], v[192:193]\n"
      "v_mfma_f32_16x16x32_bf16 v[198:201], a[24:27], a[232:235], 0\n"
      "s_waitcnt lgkmcnt(0)\n"
      "v_permlane16_swap_b32 v186, v184\n"
      "v_mfma_f32_16x16x32_bf16 v[198:201], a[28:31], a[236:239], v[198:201]\n"
      "s_add_u32 m0, s75, 19456\n"
      "v_add_f32 v186, v186, v184\n"
      "v_mov_b32 v184, v186\n"
      "v_mfma_f32_16x16x32_bf16 v[128:131], a[140:143], a[228:231], v[128:131]\n"
      "buffer_load_dwordx4 v120, s[20:23], s78 offen offset:1024  lds\n"
      "s_add_u32 m0, s75, 24576\n"
      "v_mfma_f32_16x16x32_bf16 v[120:123], a[96:99], a[232:235], 0\n"
      "s_waitcnt vmcnt(12)\n"
      "s_barrier\n"
      "ds_read_b64_tr_b16 a[174:175], v136 offset:12800\n"
      "v_mfma_f32_16x16x32_bf16 v[198:201], a[32:35], a[240:243], v[198:201]\n"
      "ds_read_b64_tr_b16 a[146:147], v138 offset:512\n"
      "ds_read_b64_tr_b16 a[148:149], v138 offset:12288\n"
      "ds_read_b64_tr_b16 a[144:145], v138\n"
      "v_mfma_f32_16x16x32_bf16 v[198:201], a[36:39], a[244:247], v[198:201]\n"
      "ds_read_b64_tr_b16 a[192:193], v139\n"
      "ds_read_b64_tr_b16 a[170:171], v136 offset:512\n"
      "v_mfma_f32_16x16x32_bf16 v[180:183], a[20:23], a[224:227], v[180:183]\n"
      "ds_read_b64_tr_b16 a[194:195], v139 offset:512\n"
      "ds_read_b64_tr_b16 a[196:197], v139 offset:12288\n"
      "v_mfma_f32_16x16x32_bf16 v[222:225], a[44:47], a[228:231], v[222:225]\n"
      "ds_read_b64_tr_b16 a[150:151], v138 offset:12800\n"
      "ds_read_b64_tr_b16 a[198:199], v139 offset:12800\n"
      "ds_read_b64_tr_b16 a[168:169], v136\n"
      "v_mfma_f32_16x16x32_bf16 v[96:99], a[0:3], a[232:235], 0\n"
      "ds_read_b64_tr_b16 a[172:173], v136 offset:12288\n"
      "v_permlane32_swap_b32_e64 v186, v184\n"
      "v_mfma_f32_16x16x32_bf16 v[96:99], a[4:7], a[236:239], v[96:99]\n"
      "s_mov_b32 s79, 0\n"
      "buffer_load_dwordx4 v197, s[36:39], s79 offen  lds\n"
      "v_add_f32 v186, v186, v184\n"
      "v_pk_fma_f32 v[178:179], v[168:169], v[168:169], v[178:179]\n"
      "v_pk_fma_f32 v[192:193], v[252:253], v[252:253], v[192:193]\n"
      "v_pk_fma_f32 v[80:81], v[222:223], v[222:223], v[80:81]\n"
      "v_mfma_f32_16x16x32_bf16 v[96:99], a[8:11], a[240:243], v[96:99]\n"
      "s_add_u32 m0, s75, 28544\n"
      "v_rsq_f32 v186, v186\n"
      "v_mfma_f32_16x16x32_bf16 v[96:99], a[12:15], a[244:247], v[96:99]\n"
      "ds_read_b128 v[116:119], v254\n"
      "v_mov_b32 v184, v186\n"
      "v_mfma_f32_16x16x32_bf16 v[104:107], a[80:83], a[240:243], v[104:107]\n"
      "buffer_load_dwordx4 v197, s[36:39], s79 offen offset:128  lds\n"
      "v_permlane16_swap_b32_e64 v186, v184\n"
      "v_pk_fma_f32 v[178:179], v[188:189], v[188:189], v[178:179]\n"
      "v_pk_fma_f32 v[192:193], v[174:175], v[174:175], v[192:193]\n"
      "v_pk_fma_f32 v[178:179], v[190:191], v[190:191], v[178:179]\n"
      "v_mul_f32 v186, s50, v186\n"
      "v_pk_mul_f32 v[134:135], v[134:135], v[186:187] op_sel_hi:[1,0]\n"
      "v_pk_mul_f32 v[234:235], v[234:235], v[184:185] op_sel_hi:[1,0]\n"
      "v_add_f32 v178, v178, v179\n"
      "v_pk_mul_f32 v[160:161], v[160:161], v[184:185] op_sel_hi:[1,0]\n"
      "s_add_u32 m0, s75, 32512\n"
      "v_pk_mul_f32 v[246:247], v[246:247], v[186:187] op_sel_hi:[1,0]\n"
      "v_pk_fma_f32 v[192:193], v[176:177], v[176:177], v[192:193]\n"
      "v_pk_fma_f32 v[80:81], v[224:225], v[224:225], v[80:81]\n"
      "v_pk_mul_f32 v[236:237], v[236:237], v[184:185] op_sel_hi:[1,0]\n"
      "v_pk_mul_f32 v[244:245], v[244:245], v[184:185] op_sel_hi:[1,0]\n"
      "v_mfma_f32_16x16x32_bf16 v[120:123], a[100:103], a[236:239], v[120:123]\n"
      "buffer_load_dwordx4 v197, s[36:39], s79 offen offset:256  lds\n"
      "v_add_f32_e64 v192, v192, v193\n"
      "s_add_u32 m0, s75, 36864\n"
      "v_pk_mul_f32 v[242:243], v[242:243], v[184:185] op_sel_hi:[1,0]\n"
      "v_pk_mul_f32 v[162:163], v[162:163], v[184:185] op_sel_hi:[1,0]\n"
      "v_pk_fma_f32 v[80:81], v[218:219], v[218:219], v[80:81]\n"
      "v_pk_mul_f32 v[230:231], v[230:231], v[186:187] op_sel_hi:[1,0]\n"
      "v_pk_mul_f32 v[132:133], v[132:133], v[186:187] op_sel_hi:[1,0]\n"
      "v_pk_mul_f32 v[248:249], v[248:249], v[186:187] op_sel_hi:[1,0]\n"
      "v_cvt_pk_bf16_f32 v234, v234, v235\n"
      "v_cvt_pk_bf16_f32 v230, v230, v231\n"
      "v_pk_mul_f32 v[232:233], v[232:233], v[186:187] op_sel_hi:[1,0]\n"
      "v_mfma_f32_16x16x32_bf16 v[96:99], a[16:19], a[248:251], v[96:99]\n"
      "s_mov_b32 s79, 12288\n"
      "v_cvt_pk_bf16_f32 v246, v246, v247\n"
      "v_cvt_pk_bf16_f32 v247, v248, v249\n"
      "v_mfma_f32_16x16x32_bf16 v[104:107], a[84:87], a[244:247], v[104:107]\n"
      "buffer_load_dwordx4 v197, s[36:39], s79 offen  lds\n"
      "v_cvt_pk_bf16_f32 v231, v232, v233\n"
      "v_cvt_pk_bf16_f32 v249, v134, v135\n"
      "v_cvt_pk_bf16_f32 v235, v236, v237\n"
      "v_mfma_f32_16x16x32_bf16 v[104:107], a[88:91], a[248:251], v[104:107]\n"
      "s_lshl_b32 s92, s40, 6\n"
      "v_permlane16_swap_b32 v178, v192\n"
      "v_mfma_f32_16x16x32_bf16 v[108:111], a[140:143], a[224:227], v[108:111]\n"
      "s_add_u32 m0, s75, 40832\n"
      "v_cvt_pk_bf16_f32 v242, v242, v243\n"
      "v_cvt_pk_bf16_f32 v248, v132, v133\n"
      "v_mfma_f32_16x16x32_bf16 v[96:99], a[20:23], a[252:255], v[96:99]\n"
      "buffer_load_dwordx4 v197, s[36:39], s79 offen offset:128  lds\n"
      "v_mfma_f32_16x16x32_bf16 v[226:229], a[44:47], a[224:227], v[226:229]\n"
      "s_add_u32 m0, s75, 44800\n"
      "v_cvt_pk_bf16_f32 v243, v244, v245\n"
      "v_add_f32 v178, v178, v192\n"
      "v_mfma_f32_16x16x32_bf16 v[112:115], a[56:59], a[240:243], v[112:115]\n"
      "s_waitcnt lgkmcnt(0)\n"
      "v_mov_b32_e64 v192, v178\n"
      "v_cvt_pk_bf16_f32 v245, v162, v163\n"
      "v_mfma_f32_16x16x32_bf16 v[198:201], a[40:43], a[248:251], v[198:201]\n"
      "ds_read_b128 v[132:135], v254 offset:4096\n"
      "v_cvt_pk_bf16_f32 v244, v160, v161\n"
      "v_mfma_f32_16x16x32_bf16 v[198:201], a[44:47], a[252:255], v[198:201]\n"
      "buffer_load_dwordx4 v197, s[36:39], s79 offen offset:256  lds\n"
      "v_mfma_f32_16x16x32_bf16 v[160:163], a[168:171], v[100:103], 0\n"
      "s_add_u32 m0, s75, 49152\n"
      "v_permlane32_swap_b32_e64 v178, v192\n"
      "v_mfma_f32_16x16x32_bf16 v[160:163], a[172:175], v[116:119], v[160:163]\n"
      "ds_read_b128 v[184:187], v85 offset:4096\n"
      "v_add_f32_e64 v178, v178, v192\n"
      "v_mfma_f32_16x16x32_bf16 v[140:143], a[68:71], a[224:227], v[140:143]\n"
      "s_waitcnt vmcnt(12)\n"
      "s_barrier\n"
      "ds_read_b64_tr_b16 a[204:205], v139 offset:36864\n"
      "ds_read_b64_tr_b16 a[178:179], v136 offset:25088\n"
      "ds_read_b64_tr_b16 a[206:207], v139 offset:37376\n"
      "ds_read_b64_tr_b16 a[202:203], v139 offset:25088\n"
      "v_mfma_f32_16x16x32_bf16 v[148:151], a[144:147], v[100:103], 0\n"
      "ds_read_b64_tr_b16 a[158:159], v138 offset:37376\n"
      "ds_read_b64_tr_b16 a[156:157], v138 offset:36864\n"
      "v_mfma_f32_16x16x32_bf16 v[148:151], a[148:151], v[116:119], v[148:151]\n"
      "ds_read_b64_tr_b16 a[152:153], v138 offset:24576\n"
      "ds_read_b64_tr_b16 a[200:201], v139 offset:24576\n"
      "v_mfma_f32_16x16x32_bf16 v[120:123], a[104:107], a[240:243], v[120:123]\n"
      "ds_read_b64_tr_b16 a[176:177], v136 offset:24576\n"
      "ds_read_b64_tr_b16 a[154:155], v138 offset:25088\n"
      "s_mov_b32 s78, 24576\n"
      "v_mfma_f32_16x16x32_bf16 v[120:123], a[108:111], a[244:247], v[120:123]\n"
      "ds_read_b64_tr_b16 a[180:181], v136 offset:36864\n"
      "ds_read_b64_tr_b16 a[182:183], v136 offset:37376\n"
      "v_mfma_f32_16x16x32_bf16 v[136:139], a[192:195], v[100:103], 0\n"
      "buffer_load_dwordx4 v197, s[36:39], s78 offen  lds\n"
      "v_mfma_f32_16x16x32_bf16 v[136:139], a[196:199], v[116:119], v[136:139]\n"
      "ds_read_b128 v[116:119], v85 offset:2048\n"
      "s_add_u32 m0, s75, 53120\n"
      "v_mfma_f32_16x16x32_bf16 v[100:103], a[120:123], a[232:235], 0\n"
      "ds_write_b128 v195, v[242:245] offset:32\n"
      "v_mfma_f32_16x16x32_bf16 v[100:103], a[124:127], a[236:239], v[100:103]\n"
      "s_waitcnt lgkmcnt(4)\n"
      "v_rsq_f32 v178, v178\n"
      "v_mfma_f32_16x16x32_bf16 v[148:151], a[152:155], v[184:187], v[148:151]\n"
      "buffer_load_dwordx4 v197, s[36:39], s78 offen offset:128  lds\n"
      "v_mfma_f32_16x16x32_bf16 v[136:139], a[200:203], v[184:187], v[136:139]\n"
      "ds_write_b128 v195, v[246:249]\n"
      "v_mfma_f32_16x16x32_bf16 v[160:163], a[176:179], v[184:187], v[160:163]\n"
      "s_waitcnt lgkmcnt(2)\n"
      "v_mov_b32 v192, v178\n"
      "s_add_u32 m0, s75, 57088\n"
      "v_mfma_f32_16x16x32_bf16 v[148:151], a[156:159], v[132:135], v[148:151]\n"
      "buffer_load_dwordx4 v197, s[36:39], s78 offen offset:256  lds\n"
      "v_mfma_f32_16x16x32_bf16 v[120:123], a[112:115], a[248:251], v[120:123]\n"
      "s_mov_b32 s78, 36864\n"
      "ds_write_b64 v195, v[234:235] offset:24\n"
      "v_mfma_f32_16x16x32_bf16 v[184:187], a[144:147], v[116:119], 0\n"
      "s_add_u32 m0, s75, 61440\n"
      "v_permlane16_swap_b32 v178, v192\n"
      "v_mfma_f32_16x16x32_bf16 v[136:139], a[204:207], v[132:135], v[136:139]\n"
      "ds_read_b128 v[234:237], v254 offset:2048\n"
      "v_mul_f32 v178, s50, v178\n"
      "v_mfma_f32_16x16x32_bf16 v[160:163], a[180:183], v[132:135], v[160:163]\n"
      "buffer_load_dwordx4 v197, s[36:39], s78 offen  lds\n"
      "v_mfma_f32_16x16x32_bf16 v[170:173], a[192:195], v[116:119], 0\n"
      "ds_write_b64 v195, v[230:231] offset:16\n"
      "v_mfma_f32_16x16x32_bf16 v[152:155], a[92:95], a[228:231], v[152:155]\n"
      "s_add_u32 m0, s75, 65408\n"
      "ds_read_b128 v[244:247], v254 offset:6144\n"
      "v_mfma_f32_16x16x32_bf16 v[210:213], a[168:171], v[116:119], 0\n"
      "ds_read_b128 v[230:233], v85 offset:6144\n"
      "buffer_load_dwordx4 v197, s[36:39], s78 offen offset:128  lds\n"
      "v_pk_fma_f32 v[80:81], v[220:221], v[220:221], v[80:81]\n"
      "v_pk_mul_f32 v[202:203], v[202:203], v[192:193] op_sel_hi:[1,0]\n"
      "v_pk_mul_f32 v[190:191], v[190:191], v[178:179] op_sel_hi:[1,0]\n"
      "v_pk_mul_f32 v[174:175], v[174:175], v[192:193] op_sel_hi:[1,0]\n"
      "v_pk_mul_f32 v[176:177], v[176:177], v[192:193] op_sel_hi:[1,0]\n"
      "v_pk_mul_f32 v[252:253], v[252:253], v[192:193] op_sel_hi:[1,0]\n"
      "v_add_f32_e64 v80, v80, v81\n"
      "v_pk_mul_f32 v[204:205], v[204:205], v[192:193] op_sel_hi:[1,0]\n"
      "v_pk_mul_f32 v[188:189], v[188:189], v[178:179] op_sel_hi:[1,0]\n"
      "v_pk_mul_f32 v[168:169], v[168:169], v[178:179] op_sel_hi:[1,0]\n"
      "s_waitcnt lgkmcnt(3)\n"
      "v_mfma_f32_16x16x32_bf16 v[210:213], a[172:175], v[234:237], v[210:213]\n"
      "s_add_u32 m0, s75, 69376\n"
      "v_cvt_pk_bf16_f32 v188, v188, v189\n"
      "v_cvt_pk_bf16_f32 v189, v190, v191\n"
      "v_mfma_f32_16x16x32_bf16 v[184:187], a[148:151], v[234:237], v[184:187]\n"
      "buffer_load_dwordx4 v197, s[36:39], s78 offen offset:256  lds\n"
      "v_mfma_f32_16x16x32_bf16 v[100:103], a[128:131], a[240:243], v[100:103]\n"
      "s_waitcnt lgkmcnt(0)\n"
      "v_cvt_pk_bf16_f32 v174, v174, v175\n"
      "v_cvt_pk_bf16_f32 v175, v176, v177\n"
      "v_mfma_f32_16x16x32_bf16 v[170:173], a[196:199], v[234:237], v[170:173]\n"
      "s_add_u32 m0, s75, 73728\n"
      "ds_read_b128 v[234:237], v85 offset:8192\n"
      "v_mfma_f32_16x16x32_bf16 v[170:173], a[200:203], v[230:233], v[170:173]\n"
      "s_waitcnt vmcnt(12)\n"
      "s_barrier\n"
      "s_mov_b32 s79, 49152\n"
      "v_mfma_f32_16x16x32_bf16 v[184:187], a[152:155], v[230:233], v[184:187]\n"
      "buffer_load_dwordx4 v197, s[36:39], s79 offen  lds\n"
      "v_mfma_f32_16x16x32_bf16 v[184:187], a[156:159], v[244:247], v[184:187]\n"
      "ds_read_b64_tr_b16 a[190:191], v239 offset:12800\n"
      "s_add_u32 m0, s75, 77696\n"
      "v_mfma_f32_16x16x32_bf16 v[210:213], a[176:179], v[230:233], v[210:213]\n"
      "ds_read_b64_tr_b16 a[160:161], v238\n"
      "ds_read_b64_tr_b16 a[164:165], v238 offset:12288\n"
      "ds_read_b64_tr_b16 a[166:167], v238 offset:12800\n"
      "v_mfma_f32_16x16x32_bf16 v[210:213], a[180:183], v[244:247], v[210:213]\n"
      "ds_read_b64_tr_b16 a[214:215], v240 offset:12800\n"
      "ds_read_b64_tr_b16 a[162:163], v238 offset:512\n"
      "v_mfma_f32_16x16x32_bf16 v[170:173], a[204:207], v[244:247], v[170:173]\n"
      "ds_read_b64_tr_b16 a[184:185], v239\n"
      "ds_read_b64_tr_b16 a[208:209], v240\n"
      "v_mul_f32_e64 v117, v153, v153\n"
      "v_fma_f32 v116, v152, v152, s51\n"
      "v_mfma_f32_16x16x32_bf16 v[156:159], a[116:119], a[224:227], v[156:159]\n"
      "ds_read_b64_tr_b16 a[186:187], v239 offset:512\n"
      "v_cvt_pk_bf16_f32 v202, v202, v203\n"
      "v_mfma_f32_16x16x32_bf16 v[104:107], a[92:95], a[252:255], v[104:107]\n"
      "ds_read_b64_tr_b16 a[188:189], v239 offset:12288\n"
      "ds_read_b64_tr_b16 a[212:213], v240 offset:12288\n"
      "v_mfma_f32_16x16x32_bf16 v[100:103], a[132:135], a[244:247], v[100:103]\n"
      "ds_read_b64_tr_b16 a[210:211], v240 offset:512\n"
      "v_cvt_pk_bf16_f32 v203, v204, v205\n"
      "v_mfma_f32_16x16x32_bf16 v[214:217], a[116:119], a[228:231], v[214:217]\n"
      "buffer_load_dwordx4 v197, s[36:39], s79 offen offset:128  lds\n"
      "ds_read_b128 v[242:245], v254 offset:8192\n"
      "v_pk_mul_f32 v[146:147], v[146:147], v[178:179] op_sel_hi:[1,0]\n"
      "v_pk_mul_f32 v[250:251], v[250:251], v[192:193] op_sel_hi:[1,0]\n"
      "v_cvt_pk_bf16_f32 v204, v250, v251\n"
      "v_pk_mul_f32 v[166:167], v[166:167], v[178:179] op_sel_hi:[1,0]\n"
      "v_pk_mul_f32 v[144:145], v[144:145], v[178:179] op_sel_hi:[1,0]\n"
      "s_add_u32 m0, s75, 81664\n"
      "v_mul_f32_e64 v179, v105, v105\n"
      "v_pk_fma_f32 v[116:117], v[154:155], v[154:155], v[116:117]\n"
      "v_pk_fma_f32 v[116:117], v[214:215], v[214:215], v[116:117]\n"
      "v_pk_fma_f32 v[116:117], v[216:217], v[216:217], v[116:117]\n"
      "v_mfma_f32_16x16x32_bf16 v[120:123], a[116:119], a[252:255], v[120:123]\n"
      "buffer_load_dwordx4 v197, s[36:39], s79 offen offset:256  lds\n"
      "v_mul_f32 v177, v207, v207\n"
      "v_cvt_pk_bf16_f32 v205, v252, v253\n"
      "v_pk_fma_f32 v[116:117], v[128:129], v[128:129], v[116:117]\n"
      "s_mov_b32 s79, 61440\n"
      "v_fma_f32 v176, v206, v206, s51\n"
      "v_pk_fma_f32 v[116:117], v[130:131], v[130:131], v[116:117]\n"
      "v_pk_fma_f32 v[176:177], v[208:209], v[208:209], v[176:177]\n"
      "s_waitcnt lgkmcnt(0)\n"
      "v_mfma_f32_16x16x32_bf16 v[136:139], a[208:211], v[234:237], v[136:139]\n"
      "s_add_u32 m0, s75, 86016\n"
      "v_cvt_pk_bf16_f32 v144, v144, v145\n"
      "v_fma_f32 v118, v96, v96, s51\n"
      "v_mfma_f32_16x16x32_bf16 v[160:163], a[184:187], v[234:237], v[160:163]\n"
      "buffer_load_dwordx4 v197, s[36:39], s79 offen  lds\n"
      "v_cvt_pk_bf16_f32 v145, v146, v147\n"
      "v_mfma_f32_16x16x32_bf16 v[148:151], a[160:163], v[234:237], v[148:151]\n"
      "s_add_u32 m0, s75, 89984\n"
      "v_add_f32 v116, v116, v117\n"
      "v_mul_f32 v119, v97, v97\n"
      "v_mfma_f32_16x16x32_bf16 v[160:163], a[188:191], v[242:245], v[160:163]\n"
      "ds_read_b128 v[246:249], v1 offset:4096\n"
      "v_fma_f32 v134, v180, v180, s51\n"
      "v_permlane16_swap_b32 v80, v116\n"
      "v_mfma_f32_16x16x32_bf16 v[136:139], a[212:215], v[242:245], v[136:139]\n"
      "buffer_load_dwordx4 v197, s[36:39], s79 offen offset:128  lds\n"
      "v_add_f32 v80, v80, v116\n"
      "v_mfma_f32_16x16x32_bf16 v[148:151], a[164:167], v[242:245], v[148:151]\n"
      "ds_read_b128 v[234:237], v85 offset:10240\n"
      "s_add_u32 m0, s75, 93952\n"
      "v_mul_f32_e64 v135, v181, v181\n"
      "v_cvt_pk_bf16_f32 v160, v160, v161\n"
      "v_cvt_pk_bf16_f32 v147, v168, v169\n"
      "v_fma_f32 v178, v104, v104, s51\n"
      "v_pk_fma_f32 v[178:179], v[106:107], v[106:107], v[178:179]\n"
      "v_pk_fma_f32 v[176:177], v[156:157], v[156:157], v[176:177]\n"
      "v_pk_fma_f32 v[134:135], v[182:183], v[182:183], v[134:135]\n"
      "v_mfma_f32_16x16x32_bf16 v[112:115], a[60:63], a[244:247], v[112:115]\n"
      "buffer_load_dwordx4 v197, s[36:39], s79 offen offset:256  lds\n"
      "v_cvt_pk_bf16_f32 v146, v166, v167\n"
      "v_cvt_pk_bf16_f32 v136, v136, v137\n"
      "v_cvt_pk_bf16_f32 v161, v162, v163\n"
      "v_cvt_pk_bf16_f32 v148, v148, v149\n"
      "v_pk_fma_f32 v[176:177], v[158:159], v[158:159], v[176:177]\n"
      "v_pk_fma_f32 v[118:119], v[98:99], v[98:99], v[118:119]\n"
      "v_pk_fma_f32 v[178:179], v[120:121], v[120:121], v[178:179]\n"
      "v_pk_fma_f32 v[176:177], v[108:109], v[108:109], v[176:177]\n"
      "v_mov_b32_e64 v116, v80\n"
      "v_cvt_pk_bf16_f32 v149, v150, v151\n"
      "v_cvt_pk_bf16_f32 v137, v138, v139\n"
      "v_pk_fma_f32 v[134:135], v[226:227], v[226:227], v[134:135]\n"
      "v_mfma_f32_16x16x32_bf16 v[112:115], a[64:67], a[248:251], v[112:115]\n"
      "ds_write_b64 v196, v[136:137] offset:64\n"
      "ds_read_b128 v[242:245], v254 offset:10240\n"
      "v_mfma_f32_16x16x32_bf16 v[112:115], a[68:71], a[252:255], v[112:115]\n"
      "buffer_load_dwordx4 v[250:253], v36, s[28:31], s49 offen\n"
      "ds_read_b128 v[166:169], v0 offset:4096\n"
      "v_pk_fma_f32 v[134:135], v[228:229], v[228:229], v[134:135]\n"
      "v_pk_fma_f32 v[118:119], v[198:199], v[198:199], v[118:119]\n"
      "v_pk_fma_f32 v[178:179], v[122:123], v[122:123], v[178:179]\n"
      "v_pk_fma_f32 v[176:177], v[110:111], v[110:111], v[176:177]\n"
      "v_permlane32_swap_b32_e64 v80, v116\n"
      "v_pk_fma_f32 v[134:135], v[140:141], v[140:141], v[134:135]\n"
      "v_mfma_f32_16x16x32_bf16 v[100:103], a[136:139], a[248:251], v[100:103]\n"
      "ds_write_b64 v196, v[160:161] offset:32\n"
      "v_add_f32_e64 v80, v80, v116\n"
      "s_waitcnt vmcnt(13)\n"
      "s_barrier\n"
      "v_pk_fma_f32 v[118:119], v[200:201], v[200:201], v[118:119]\n"
      "v_pk_fma_f32 v[118:119], v[112:113], v[112:113], v[118:119]\n"
      "v_pk_fma_f32 v[118:119], v[114:115], v[114:115], v[118:119]\n"
      "v_mfma_f32_16x16x32_bf16 v[100:103], a[140:143], a[252:255], v[100:103]\n"
      "buffer_load_dword v254, v29, s[28:31], s49 offen\n"
      "v_rsq_f32 v80, v80\n"
      "v_add_f32_e64 v176, v176, v177\n"
      "v_pk_fma_f32 v[134:135], v[142:143], v[142:143], v[134:135]\n"
      "s_waitcnt lgkmcnt(1)\n"
      "v_mfma_f32_16x16x32_bf16 v[210:213], a[184:187], v[234:237], v[210:213]\n"
      "ds_write_b64 v196, v[148:149]\n"
      "v_mov_b32_e64 v116, v80\n"
      "v_mfma_f32_16x16x32_bf16 v[210:213], a[188:191], v[242:245], v[210:213]\n"
      "buffer_load_dwordx4 v[160:163], v36, s[28:31], s74 offen\n"
      "v_mfma_f32_16x16x32_bf16 v[184:187], a[160:163], v[234:237], v[184:187]\n"
      "ds_read_b64_tr_b16 a[242:243], v241 offset:26632\n"
      "ds_read_b64_tr_b16 a[222:223], v241 offset:38912\n"
      "s_add_u32 m0, s64, 18432\n"
      "v_permlane16_swap_b32 v80, v116\n"
      "v_pk_fma_f32 v[178:179], v[100:101], v[100:101], v[178:179]\n"
      "v_add_f32 v134, v134, v135\n"
      "v_mfma_f32_16x16x32_bf16 v[184:187], a[164:167], v[242:245], v[184:187]\n"
      "ds_read_b64_tr_b16 a[216:217], v241 offset:24576\n"
      "ds_read_b64_tr_b16 a[218:219], v241 offset:26624\n"
      "v_permlane16_swap_b32 v134, v176\n"
      "v_mul_f32 v80, s50, v80\n"
      "v_add_f32 v134, v134, v176\n"
      "v_add_f32 v118, v118, v119\n"
      "v_mov_b32_e64 v176, v134\n"
      "v_mfma_f32_16x16x32_bf16 v[170:173], a[208:211], v[234:237], v[170:173]\n"
      "ds_read_b64_tr_b16 a[220:221], v241 offset:36864\n"
      "buffer_load_dword v164, v29, s[28:31], s74 offen\n"
      "ds_read_b64_tr_b16 a[246:247], v241 offset:38920\n"
      "ds_read_b64_tr_b16 v[236:237], v194 offset:36864\n"
      "v_pk_fma_f32 v[178:179], v[102:103], v[102:103], v[178:179]\n"
      "v_add_f32_e64 v178, v178, v179\n"
      "v_mfma_f32_16x16x32_bf16 v[170:173], a[212:215], v[242:245], v[170:173]\n"
      "ds_read_b64_tr_b16 v[232:233], v194 offset:24576\n"
      "v_cvt_pk_bf16_f32 v184, v184, v185\n"
      "v_cvt_pk_bf16_f32 v185, v186, v187\n"
      "v_permlane16_swap_b32 v118, v178\n"
      "v_permlane32_swap_b32 v134, v176\n"
      "v_pk_mul_f32 v[128:129], v[128:129], v[116:117] op_sel_hi:[1,0]\n"
      "v_add_f32 v118, v118, v178\n"
      "v_mov_b32 v178, v118\n"
      "v_cvt_pk_bf16_f32 v210, v210, v211\n"
      "v_mfma_f32_16x16x32_bf16 v[190:193], v[166:169], a[144:147], 0\n"
      "buffer_load_dwordx4 v16, s[16:19], s45 offen  lds\n"
      "ds_read_b64_tr_b16 a[244:245], v241 offset:36872\n"
      "s_add_u32 m0, s64, 22528\n"
      "ds_read_b64_tr_b16 v[238:239], v194 offset:38912\n"
      "ds_read_b64_tr_b16 v[234:235], v194 offset:26624\n"
      "ds_read_b64_tr_b16 a[240:241], v241 offset:24584\n"
      "v_add_f32 v134, v134, v176\n"
      "v_rsq_f32 v134, v134\n"
      "v_add_u32 v186, 49152, v8\n"
      "v_mov_b32 v176, v134\n"
      "v_mfma_f32_16x16x32_bf16 v[190:193], v[246:249], a[148:151], v[190:193]\n"
      "buffer_load_dwordx4 v17, s[16:19], s45 offen  lds\n"
      "v_permlane16_swap_b32 v134, v176\n"
      "v_permlane32_swap_b32 v118, v178\n"
      "v_pk_mul_f32 v[110:111], v[110:111], v[176:177] op_sel_hi:[1,0]\n"
      "v_add_f32 v118, v118, v178\n"
      "v_mul_f32 v134, s50, v134\n"
      "v_rsq_f32 v118, v118\n"
      "v_pk_mul_f32 v[180:181], v[180:181], v[134:135] op_sel_hi:[1,0]\n"
      "v_mov_b32 v178, v118\n"
      "v_cvt_pk_bf16_f32 v170, v170, v171\n"
      "v_pk_mul_f32 v[154:155], v[154:155], v[116:117] op_sel_hi:[1,0]\n"
      "v_permlane16_swap_b32 v118, v178\n"
      "v_pk_mul_f32 v[226:227], v[226:227], v[134:135] op_sel_hi:[1,0]\n"
      "v_mul_f32_e64 v118, s50, v118\n"
      "v_pk_mul_f32 v[220:221], v[220:221], v[80:81] op_sel_hi:[1,0]\n"
      "v_pk_mul_f32 v[124:125], v[124:125], v[80:81] op_sel_hi:[1,0]\n"
      "v_pk_mul_f32 v[200:201], v[200:201], v[118:119] op_sel_hi:[1,0]\n"
      "v_pk_mul_f32 v[208:209], v[208:209], v[176:177] op_sel_hi:[1,0]\n"
      "v_cvt_pk_bf16_f32 v180, v180, v181\n"
      "v_pk_mul_f32 v[214:215], v[214:215], v[116:117] op_sel_hi:[1,0]\n"
      "v_pk_mul_f32 v[152:153], v[152:153], v[116:117] op_sel_hi:[1,0]\n"
      "v_cvt_pk_bf16_f32 v128, v128, v129\n"
      "v_cvt_pk_bf16_f32 v211, v212, v213\n"
      "v_pk_mul_f32 v[120:121], v[120:121], v[178:179] op_sel_hi:[1,0]\n"
      "v_mov_b32 v179, 0xbc80bc80\n"
      "v_pk_mul_f32 v[104:105], v[104:105], v[178:179] op_sel_hi:[1,0]\n"
      "v_cvt_pk_bf16_f32 v152, v152, v153\n"
      "v_xor_b32_e64 v231, 64, v8\n"
      "v_pk_mul_f32 v[100:101], v[100:101], v[178:179] op_sel_hi:[1,0]\n"
      "v_pk_mul_f32 v[112:113], v[112:113], v[118:119] op_sel_hi:[1,0]\n"
      "v_pk_mul_f32 v[182:183], v[182:183], v[134:135] op_sel_hi:[1,0]\n"
      "v_pk_mul_f32 v[102:103], v[102:103], v[178:179] op_sel_hi:[1,0]\n"
      "v_cvt_pk_bf16_f32 v181, v182, v183\n"
      "v_pk_mul_f32 v[130:131], v[130:131], v[116:117] op_sel_hi:[1,0]\n"
      "v_cvt_pk_bf16_f32 v171, v172, v173\n"
      "v_pk_mul_f32 v[206:207], v[206:207], v[176:177] op_sel_hi:[1,0]\n"
      "v_cvt_pk_bf16_f32 v153, v154, v155\n"
      "v_pk_mul_f32 v[156:157], v[156:157], v[176:177] op_sel_hi:[1,0]\n"
      "v_pk_mul_f32 v[216:217], v[216:217], v[116:117] op_sel_hi:[1,0]\n"
      "v_cvt_pk_bf16_f32 v129, v130, v131\n"
      "v_pk_mul_f32 v[114:115], v[114:115], v[118:119] op_sel_hi:[1,0]\n"
      "v_pk_mul_f32 v[96:97], v[96:97], v[118:119] op_sel_hi:[1,0]\n"
      "v_pk_mul_f32 v[98:99], v[98:99], v[118:119] op_sel_hi:[1,0]\n"
      "v_pk_mul_f32 v[222:223], v[222:223], v[80:81] op_sel_hi:[1,0]\n"
      "v_pk_mul_f32 v[106:107], v[106:107], v[178:179] op_sel_hi:[1,0]\n"
      "v_cvt_pk_bf16_f32 v206, v206, v207\n"
      "v_pk_mul_f32 v[198:199], v[198:199], v[118:119] op_sel_hi:[1,0]\n"
      "v_cvt_pk_bf16_f32 v104, v104, v105\n"
      "v_pk_mul_f32 v[224:225], v[224:225], v[80:81] op_sel_hi:[1,0]\n"
      "v_cvt_pk_bf16_f32 v96, v96, v97\n"
      "v_pk_mul_f32 v[126:127], v[126:127], v[80:81] op_sel_hi:[1,0]\n"
      "v_cvt_pk_bf16_f32 v182, v226, v227\n"
      "v_pk_mul_f32 v[218:219], v[218:219], v[80:81] op_sel_hi:[1,0]\n"
      "v_cvt_pk_bf16_f32 v155, v216, v217\n"
      "v_cvt_pk_bf16_f32 v97, v98, v99\n"
      "v_cvt_pk_bf16_f32 v207, v208, v209\n"
      "v_cvt_pk_bf16_f32 v208, v156, v157\n"
      "v_mfma_f32_16x16x32_bf16 v[116:119], v[166:169], a[168:171], 0\n"
      "ds_read_b128 v[130:133], v0 offset:16384\n"
      "ds_read_b128 v[148:151], v0 offset:10240\n"
      "v_cvt_pk_bf16_f32 v105, v106, v107\n"
      "v_cvt_pk_bf16_f32 v218, v218, v219\n"
      "v_cvt_pk_bf16_f32 v98, v198, v199\n"
      "v_pk_mul_f32 v[122:123], v[122:123], v[178:179] op_sel_hi:[1,0]\n"
      "v_cvt_pk_bf16_f32 v99, v200, v201\n"
      "v_mfma_f32_16x16x32_bf16 v[116:119], v[246:249], a[172:175], v[116:119]\n"
      "ds_read_b128 v[136:139], v1 offset:10240\n"
      "v_cvt_pk_bf16_f32 v106, v120, v121\n"
      "s_add_u32 m0, s64, 26624\n"
      "v_mfma_f32_16x16x32_bf16 v[198:201], v[166:169], a[192:195], 0\n"
      "ds_read_b128 v[166:169], v1 offset:16384\n"
      "buffer_load_dwordx4 v18, s[16:19], s45 offen  lds\n"
      "v_pk_mul_f32 v[158:159], v[158:159], v[176:177] op_sel_hi:[1,0]\n"
      "v_cvt_pk_bf16_f32 v100, v100, v101\n"
      "v_add_u32 v187, 49152, v231\n"
      "v_cvt_pk_bf16_f32 v107, v122, v123\n"
      "v_cvt_pk_bf16_f32 v124, v124, v125\n"
      "v_pk_mul_f32 v[140:141], v[140:141], v[134:135] op_sel_hi:[1,0]\n"
      "s_add_u32 m0, s64, 30720\n"
      "v_cvt_pk_bf16_f32 v112, v112, v113\n"
      "v_pk_mul_f32 v[108:109], v[108:109], v[176:177] op_sel_hi:[1,0]\n"
      "v_pk_mul_f32 v[228:229], v[228:229], v[134:135] op_sel_hi:[1,0]\n"
      "v_mov_b32 v177, 0xbc80bc80\n"
      "v_cvt_pk_bf16_f32 v154, v214, v215\n"
      "v_mfma_f32_16x16x32_bf16 v[198:201], v[246:249], a[196:199], v[198:201]\n"
      "buffer_load_dwordx4 v19, s[16:19], s45 offen  lds\n"
      "s_add_u32 m0, s65, 34816\n"
      "s_waitcnt lgkmcnt(0)\n"
      "v_mfma_f32_16x16x32_bf16 v[198:201], v[148:151], a[200:203], v[198:201]\n"
      "s_waitcnt vmcnt(14)\n"
      "s_barrier\n"
      "v_cvt_pk_bf16_f32 v219, v220, v221\n"
      "v_mfma_f32_16x16x32_bf16 v[198:201], v[136:139], a[204:207], v[198:201]\n"
      "ds_read_b64_tr_b16 a[228:229], v78 offset:12288\n"
      "v_cvt_pk_bf16_f32 v101, v102, v103\n"
      "v_mfma_f32_16x16x32_bf16 v[198:201], v[130:133], a[208:211], v[198:201]\n"
      "ds_read_b64_tr_b16 a[252:253], v78 offset:12296\n"
      "v_mov_b32 v176, 0xbc80bc80\n"
      "v_mfma_f32_16x16x32_bf16 v[190:193], v[148:151], a[152:155], v[190:193]\n"
      "ds_read_b128 v[214:217], v187 offset:4096\n"
      "ds_read_b64_tr_b16 v[242:243], v82 offset:2048\n"
      "v_mfma_f32_16x16x32_bf16 v[116:119], v[148:151], a[176:179], v[116:119]\n"
      "ds_read_b64_tr_b16 a[226:227], v78 offset:2048\n"
      "ds_read_b64_tr_b16 v[240:241], v82\n"
      "v_mfma_f32_16x16x32_bf16 v[116:119], v[136:139], a[180:183], v[116:119]\n"
      "ds_read_b64_tr_b16 a[230:231], v78 offset:14336\n"
      "ds_read_b64_tr_b16 a[248:249], v78 offset:8\n"
      "ds_read_b64_tr_b16 a[254:255], v78 offset:14344\n"
      "v_pk_mul_f32 v[142:143], v[142:143], v[134:135] op_sel_hi:[1,0]\n"
      "v_mov_b32 v178, 0xbc80bc80\n"
      "v_cvt_pk_bf16_f32 v140, v140, v141\n"
      "v_mfma_f32_16x16x32_bf16 v[116:119], v[130:133], a[184:187], v[116:119]\n"
      "buffer_load_dwordx4 v15, s[16:19], s45 offen  lds\n"
      "ds_read_b64_tr_b16 a[250:251], v78 offset:2056\n"
      "v_mfma_f32_16x16x32_bf16 v[116:119], v[166:169], a[188:191], v[116:119]\n"
      "ds_read_b128 v[120:123], v0 offset:2048\n"
      "v_cvt_pk_bf16_f32 v113, v114, v115\n"
      "v_mfma_f32_16x16x32_bf16 v[198:201], v[166:169], a[212:215], v[198:201]\n"
      "ds_read_b64_tr_b16 v[246:247], v82 offset:14336\n"
      "ds_read_b128 v[148:151], v186 offset:4096\n"
      "v_mfma_f32_16x16x32_bf16 v[190:193], v[136:139], a[156:159], v[190:193]\n"
      "ds_read_b64_tr_b16 v[244:245], v82 offset:12288\n"
      "ds_read_b64_tr_b16 a[224:225], v78\n"
      "v_mfma_f32_16x16x32_bf16 v[190:193], v[130:133], a[160:163], v[190:193]\n"
      "ds_read_b128 v[130:133], v186\n"
      "ds_read_b128 v[134:137], v187\n"
      "v_mfma_f32_16x16x32_bf16 v[190:193], v[166:169], a[164:167], v[190:193]\n"
      "s_mov_b32 exec_lo, 0x1f001f\n"
      "s_mov_b32 exec_hi, 0x1f001f\n"
      "ds_write_b128 v195, v[96:99] offset:18688\n"
      "ds_write_b64 v195, v[112:113] offset:18704\n"
      "ds_write_b64 v195, v[100:101] offset:18712\n"
      "ds_write_b128 v195, v[104:107] offset:18720\n"
      "s_mov_b64 exec, -1\n"
      "s_waitcnt lgkmcnt(4)\n"
      "v_mfma_f32_16x16x32_bf16 v[166:169], v[130:133], v[176:179], 0\n"
      "ds_read_b128 v[102:105], v0 offset:8192\n"
      "ds_read_b128 v[98:101], v231 offset:24576\n"
      "v_mfma_f32_16x16x32_bf16 v[166:169], v[134:137], v[176:179], v[166:169]\n"
      "ds_read_b128 v[112:115], v8 offset:28672\n"
      "ds_read_b128 v[134:137], v1 offset:2048\n"
      "v_mfma_f32_16x16x32_bf16 v[166:169], v[148:151], v[176:179], v[166:169]\n"
      "ds_read_b128 v[148:151], v8 offset:24576\n"
      "v_cvt_pk_bf16_f32 v209, v158, v159\n"
      "v_mfma_f32_16x16x32_bf16 v[166:169], v[214:217], v[176:179], v[166:169]\n"
      "buffer_load_dwordx4 v[156:159], v36, s[28:31], s47 offen\n"
      "v_mfma_f32_16x16x32_bf16 v[130:133], v[120:123], a[144:147], 0\n"
      "s_mov_b32 exec_lo, 0x1f001f\n"
      "s_mov_b32 exec_hi, 0x1f001f\n"
      "ds_write_b64 v196, v[184:185] offset:18688\n"
      "ds_write_b64 v196, v[210:211] offset:18720\n"
      "ds_write_b64 v196, v[170:171] offset:18752\n"
      "s_mov_b64 exec, -1\n"
      "v_mfma_f32_16x16x32_bf16 v[214:217], v[120:123], a[168:171], 0\n"
      "buffer_load_dword v94, v29, s[28:31], s47 offen\n"
      "v_mfma_f32_16x16x32_bf16 v[194:197], v[120:123], a[192:195], 0\n"
      "s_waitcnt lgkmcnt(3)\n"
      "v_subrev_u32 v39, 115456, v9\n"
      "v_cvt_pk_bf16_f32 v81, v118, v119\n"
      "v_cvt_pk_bf16_f32 v183, v228, v229\n"
      "v_mfma_f32_16x16x32_bf16 v[210:213], v[148:151], v[176:179], 0\n"
      "ds_read_b128 v[226:229], v231 offset:28672\n"
      "s_waitcnt vmcnt(10)\n"
      "v_or3_b32 v250, v250, v251, v252\n"
      "v_mfma_f32_16x16x32_bf16 v[210:213], v[98:101], v[176:179], v[210:213]\n"
      "ds_read_b128 v[120:123], v8 offset:32768\n"
      "v_cvt_pk_bf16_f32 v125, v126, v127\n"
      "v_cvt_pk_bf16_f32 v141, v142, v143\n"
      "v_mfma_f32_16x16x32_bf16 v[210:213], v[112:115], v[176:179], v[210:213]\n"
      "ds_read_b128 v[148:151], v1 offset:8192\n"
      "v_cvt_pk_bf16_f32 v85, v200, v201\n"
      "v_mfma_f32_16x16x32_bf16 v[214:217], v[134:137], a[172:175], v[214:217]\n"
      "ds_read_b128 v[170:173], v0 offset:14336\n"
      "v_cvt_pk_bf16_f32 v108, v108, v109\n"
      "v_cvt_pk_bf16_f32 v126, v222, v223\n"
      "v_mfma_f32_16x16x32_bf16 v[214:217], v[102:105], a[176:179], v[214:217]\n"
      "ds_read_b128 v[220:223], v186 offset:8192\n"
      "v_lshrrev_b32_e64 v39, 4, v39\n"
      "v_mfma_f32_16x16x32_bf16 v[130:133], v[134:137], a[148:151], v[130:133]\n"
      "ds_read_b128 v[98:101], v231 offset:32768\n"
      "v_cvt_pk_bf16_f32 v80, v116, v117\n"
      "v_mfma_f32_16x16x32_bf16 v[130:133], v[102:105], a[152:155], v[130:133]\n"
      "ds_read_b128 v[116:119], v187 offset:8192\n"
      "v_cvt_pk_bf16_f32 v109, v110, v111\n"
      "v_mfma_f32_16x16x32_bf16 v[194:197], v[134:137], a[196:199], v[194:197]\n"
      "s_barrier\n"
      "v_cvt_pk_bf16_f32 v127, v224, v225\n"
      "v_mfma_f32_16x16x32_bf16 v[194:197], v[102:105], a[200:203], v[194:197]\n"
      "s_waitcnt lgkmcnt(4)\n"
      "v_subrev_u32 v142, 115456, v9\n"
      "v_cvt_pk_bf16_f32 v76, v190, v191\n"
      "v_mfma_f32_16x16x32_bf16 v[130:133], v[148:151], a[156:159], v[130:133]\n"
      "ds_read_b128 v[110:113], v187 offset:24576\n"
      "s_waitcnt vmcnt(9)\n"
      "v_or3_b32 v250, v250, v253, v254\n"
      "v_mfma_f32_16x16x32_bf16 v[210:213], v[226:229], v[176:179], v[210:213]\n"
      "ds_read_b128 v[224:227], v1 offset:14336\n"
      "s_waitcnt vmcnt(8)\n"
      "v_or3_b32 v160, v160, v161, v162\n"
      "v_mfma_f32_16x16x32_bf16 v[210:213], v[120:123], v[176:179], v[210:213]\n"
      "ds_read_b64_tr_b16 a[232:233], v78 offset:24576\n"
      "v_bfe_u32 v138, v142, 8, 2\n"
      "v_mfma_f32_16x16x32_bf16 v[214:217], v[148:151], a[180:183], v[214:217]\n"
      "ds_read_b128 v[228:231], v186 offset:24576\n"
      "s_waitcnt lgkmcnt(4)\n"
      "v_mfma_f32_16x16x32_bf16 v[210:213], v[98:101], v[176:179], v[210:213]\n"
      "ds_read_b128 v[252:255], v0\n"
      "v_and_b32 v39, 48, v39\n"
      "v_mfma_f32_16x16x32_bf16 v[166:169], v[220:223], v[176:179], v[166:169]\n"
      "ds_read_b128 v[220:223], v186 offset:32768\n"
      "v_cvt_pk_bf16_f32 v84, v198, v199\n"
      "v_mfma_f32_16x16x32_bf16 v[194:197], v[148:151], a[204:207], v[194:197]\n"
      "ds_read_b128 v[96:99], v186 offset:28672\n"
      "v_lshlrev_b32_e64 v138, 2, v138\n"
      "v_mfma_f32_16x16x32_bf16 v[166:169], v[116:119], v[176:179], v[166:169]\n"
      "ds_read_b128 v[104:107], v187 offset:28672\n"
      "v_mul_f32 v213, 0x3eaaaaab, v213\n"
      "v_bfe_u32 v142, v142, 4, 4\n"
      "v_mfma_f32_16x16x32_bf16 v[130:133], v[170:173], a[160:163], v[130:133]\n"
      "ds_read_b128 v[120:123], v10 offset:32\n"
      "v_add_u32 v39, 123136, v39\n"
      "v_mfma_f32_16x16x32_bf16 v[214:217], v[170:173], a[184:187], v[214:217]\n"
      "ds_read_b64_tr_b16 a[238:239], v78 offset:38912\n"
      "v_mul_f32 v166, 0x3eaaaaab, v166\n"
      "v_mfma_f32_16x16x32_bf16 v[194:197], v[170:173], a[208:211], v[194:197]\n"
      "s_waitcnt lgkmcnt(5)\n"
      "v_mul_f32 v167, 0x3eaaaaab, v167\n"
      "v_sub_u32 v142, v142, v138\n"
      "v_mfma_f32_16x16x32_bf16 v[116:119], v[228:231], v[176:179], 0\n"
      "ds_read_b128 v[100:103], v1\n"
      "v_mul_f32 v212, 0x3eaaaaab, v212\n"
      "v_mfma_f32_16x16x32_bf16 v[116:119], v[110:113], v[176:179], v[116:119]\n"
      "ds_read_b128 v[228:231], v187 offset:32768\n"
      "v_mov_b32 v139, 0\n"
      "v_mfma_f32_16x16x32_bf16 v[110:113], v[252:255], a[168:171], 0\n"
      "s_waitcnt lgkmcnt(4)\n"
      "v_mul_f32 v169, 0x3eaaaaab, v169\n"
      "v_cvt_pk_bf16_f32 v77, v192, v193\n"
      "v_mfma_f32_16x16x32_bf16 v[116:119], v[96:99], v[176:179], v[116:119]\n"
      "ds_read_b128 v[190:193], v1 offset:6144\n"
      "v_mul_f32 v211, 0x3eaaaaab, v211\n"
      "v_mfma_f32_16x16x32_bf16 v[116:119], v[104:107], v[176:179], v[116:119]\n"
      "ds_write_b32 v31, v250 offset:6656\n"
      "v_mfma_f32_16x16x32_bf16 v[194:197], v[224:227], a[212:215], v[194:197]\n"
      "ds_read_b64_tr_b16 v[250:251], v82 offset:26624\n"
      "s_waitcnt vmcnt(7)\n"
      "v_or3_b32 v160, v160, v163, v164\n"
      "v_mfma_f32_16x16x32_bf16 v[116:119], v[220:223], v[176:179], v[116:119]\n"
      "ds_read_b128 v[220:223], v0 offset:12288\n"
      "v_add_u32 v97, s92, v39\n"
      "v_mfma_f32_16x16x32_bf16 v[162:165], v[252:255], a[192:195], 0\n"
      "ds_read_b128 v[148:151], v0 offset:6144\n"
      "v_mul_f32 v168, 0x3eaaaaab, v168\n"
      "v_mfma_f32_16x16x32_bf16 v[214:217], v[224:227], a[188:191], v[214:217]\n"
      "s_waitcnt lgkmcnt(5)\n"
      "v_cvt_pk_bf16_f32 v197, v196, v197\n"
      "v_cvt_pk_bf16_f32 v196, v194, v195\n"
      "v_mfma_f32_16x16x32_bf16 v[130:133], v[224:227], a[164:167], v[130:133]\n"
      "ds_write_b128 v97, v[166:169] offset:256\n"
      "v_mfma_f32_16x16x32_bf16 v[116:119], v[228:231], v[176:179], v[116:119]\n"
      "ds_read_b128 v[166:169], v1 offset:12288\n"
      "v_lshlrev_b32 v142, 4, v142\n"
      "v_mfma_f32_16x16x32_bf16 v[176:179], v[206:209], v[144:147], v[44:47]\n"
      "ds_read_b64_tr_b16 a[234:235], v78 offset:26624\n"
      "v_mov_b32 v138, 0x3f80\n"
      "v_mfma_f32_16x16x32_bf16 v[198:201], v[120:123], v[144:147], v[24:27]\n"
      "ds_read_b64_tr_b16 a[236:237], v78 offset:36864\n"
      "v_cvt_pk_bf16_f32 v217, v216, v217\n"
      "v_mul_f32 v210, 0x3eaaaaab, v210\n"
      "v_cvt_pk_bf16_f32 v216, v214, v215\n"
      "v_mfma_f32_16x16x32_bf16 v[104:107], v[252:255], a[144:147], 0\n"
      "ds_read_b64 v[172:173], v10 offset:24\n"
      "v_mul_f32 v118, 0x3eaaaaab, v118\n"
      "v_mfma_f32_16x16x32_bf16 v[104:107], v[100:103], a[148:151], v[104:107]\n"
      "ds_write_b32 v31, v160 offset:5632\n"
      "v_mfma_f32_16x16x32_bf16 v[162:165], v[100:103], a[196:199], v[162:165]\n"
      "ds_read_b64_tr_b16 v[226:227], v78 offset:26632\n"
      "s_waitcnt lgkmcnt(6)\n"
      "v_mfma_f32_16x16x32_bf16 v[162:165], v[148:151], a[200:203], v[162:165]\n"
      "ds_read_b64_tr_b16 v[230:231], v78 offset:38920\n"
      "v_mul_f32 v117, 0x3eaaaaab, v117\n"
      "v_mfma_f32_16x16x32_bf16 v[162:165], v[190:193], a[204:207], v[162:165]\n"
      "ds_read_b64_tr_b16 v[254:255], v82 offset:38912\n"
      "v_mul_f32 v116, 0x3eaaaaab, v116\n"
      "v_mfma_f32_16x16x32_bf16 v[110:113], v[100:103], a[172:175], v[110:113]\n"
      "ds_read_b64_tr_b16 v[224:225], v78 offset:24584\n"
      "v_mul_f32 v119, 0x3eaaaaab, v119\n"
      "v_mfma_f32_16x16x32_bf16 v[110:113], v[148:151], a[176:179], v[110:113]\n"
      "ds_write_b128 v97, v[210:213]\n"
      "v_mfma_f32_16x16x32_bf16 v[162:165], v[220:223], a[208:211], v[162:165]\n"
      "ds_read_b64_tr_b16 v[228:229], v78 offset:36872\n"
      "s_waitcnt lgkmcnt(5)\n"
      "v_mfma_f32_16x16x16_bf16 v[198:201], v[172:173], v[188:189], v[198:201]\n"
      "ds_read_b64_tr_b16 v[248:249], v82 offset:24576\n"
      "v_cmp_gt_u32 s[80:81], 64, v142\n"
      "v_mfma_f32_16x16x32_bf16 v[162:165], v[166:169], a[212:215], v[162:165]\n"
      "ds_write_b128 v97, v[116:119] offset:512\n"
      "v_mfma_f32_16x16x32_bf16 v[110:113], v[190:193], a[180:183], v[110:113]\n"
      "ds_read_b64_tr_b16 v[252:253], v82 offset:36864\n"
      "v_cvt_pk_bf16_f32 v133, v132, v133\n"
      "v_mfma_f32_16x16x32_bf16 v[110:113], v[220:223], a[184:187], v[110:113]\n"
      "s_waitcnt lgkmcnt(0)\n"
      "s_barrier\n"
      "v_lshlrev_b64 v[138:139], v142, v[138:139]\n"
      "v_mfma_f32_16x16x32_bf16 v[210:213], v[202:205], v[144:147], v[40:43]\n"
      "ds_read_b128 v[96:99], v9 offset:6656\n"
      "v_cvt_pk_bf16_f32 v194, v162, v163\n"
      "v_mfma_f32_16x16x16_bf16 v[176:179], v[108:109], v[188:189], v[176:179]\n"
      "v_cvt_pk_bf16_f32 v195, v164, v165\n"
      "v_cndmask_b32_e64 v162, 0, v138, s[80:81]\n"
      "v_mov_b32_e64 v161, 0\n"
      "v_mfma_f32_16x16x32_bf16 v[104:107], v[148:151], a[152:155], v[104:107]\n"
      "ds_read_b128 v[148:151], v39\n"
      "v_cndmask_b32_e64 v142, 0, v138, s[80:81]\n"
      "v_mfma_f32_16x16x32_bf16 v[104:107], v[190:193], a[156:159], v[104:107]\n"
      "ds_read_u16 v78, v11\n"
      "v_cvt_pk_bf16_f32 v132, v130, v131\n"
      "v_mfma_f32_16x16x16_bf16 v[210:213], v[174:175], v[188:189], v[210:213]\n"
      "ds_read_b128 v[190:193], v9 offset:5632\n"
      "v_cndmask_b32_e64 v163, 0, v139, s[80:81]\n"
      "v_mfma_f32_16x16x32_bf16 v[116:119], v[152:155], v[144:147], v[48:51]\n"
      "ds_read_b128 v[184:187], v39 offset:64\n"
      "v_cndmask_b32_e64 v143, 0, v139, s[80:81]\n"
      "v_mfma_f32_16x16x32_bf16 v[110:113], v[166:169], a[188:191], v[110:113]\n"
      "s_waitcnt lgkmcnt(4)\n"
      "v_or3_b32 v96, v96, v97, v98\n"
      "v_bitop3_b32 v96, v96, v99, s33 bitop3:0xa8\n"
      "v_mfma_f32_16x16x16_bf16 v[116:119], v[128:129], v[188:189], v[116:119]\n"
      "v_cmp_ne_u32 s[56:57], 0, v96\n"
      "v_mov_b32 v160, 0\n"
      "v_mov_b32_e64 v145, 0\n"
      "v_mfma_f32_16x16x32_bf16 v[104:107], v[220:223], a[160:163], v[104:107]\n"
      "s_or_b64 vcc, s[56:57], s[54:55]\n"
      "v_cvt_pk_bf16_f32 v215, v112, v113\n"
      "v_mov_b32 v144, 0\n"
      "v_mfma_f32_16x16x32_bf16 v[104:107], v[166:169], a[164:167], v[104:107]\n"
      "s_cbranch_vccnz L_rare_1_%=\n"
      "L_back_2_%=:\n"
      "v_mfma_f32_16x16x32_bf16 v[166:169], v[206:209], v[180:183], v[56:59]\n"
      "s_cbranch_vccnz L_rare_3_%=\n"
      "L_back_4_%=:\n"
      "v_exp_f32 v119, v119\n"
      "v_mfma_f32_16x16x32_bf16 v[220:223], v[120:123], v[180:183], v[26:29]\n"
      "s_waitcnt lgkmcnt(0)\n"
      "v_or3_b32 v190, v190, v191, v192\n"
      "v_cvt_pk_bf16_f32 v214, v110, v111\n"
      "v_mfma_f32_16x16x16_bf16 v[166:169], v[108:109], v[140:141], v[166:169]\n"
      "v_exp_f32 v210, v210\n"
      "v_exp_f32 v116, v116\n"
      "v_mfma_f32_16x16x16_bf16 v[220:223], v[172:173], v[140:141], v[220:223]\n"
      "v_exp_f32 v213, v213\n"
      "v_exp_f32 v117, v117\n"
      "v_exp_f32 v212, v212\n"
      "v_exp_f32 v179, v179\n"
      "v_mfma_f32_16x16x32_bf16 v[110:113], v[202:205], v[180:183], v[52:55]\n"
      "v_exp_f32 v118, v118\n"
      "v_mfma_f32_16x16x32_bf16 v[100:103], v[152:155], v[180:183], v[60:63]\n"
      "v_cvt_pk_bf16_f32 v89, v118, v119\n"
      "v_bitop3_b32 v190, v190, v193, s33 bitop3:0xa8\n"
      "v_exp_f32 v178, v178\n"
      "v_mfma_f32_16x16x32_bf16 v[180:183], v[142:145], a[240:243], v[148:151]\n"
      "v_exp_f32 v177, v177\n"
      "v_exp_f32 v198, v198\n"
      "v_mfma_f32_16x16x16_bf16 v[110:113], v[174:175], v[140:141], v[110:113]\n"
      "v_cvt_pk_bf16_f32 v130, v104, v105\n"
      "v_exp_f32 v211, v211\n"
      "v_mfma_f32_16x16x16_bf16 v[100:103], v[128:129], v[140:141], v[100:103]\n"
      "v_exp_f32 v176, v176\n"
      "v_cvt_pk_bf16_f32 v88, v116, v117\n"
      "v_mfma_f32_16x16x32_bf16 v[138:141], v[160:163], v[232:235], v[184:187]\n"
      "ds_read_u16 v82, v11 offset:32\n"
      "v_cvt_pk_bf16_f32 v210, v210, v211\n"
      "v_cvt_pk_bf16_f32 v211, v212, v213\n"
      "v_cvt_pk_bf16_f32 v213, v178, v179\n"
      "v_cvt_pk_bf16_f32 v212, v176, v177\n"
      "v_mfma_f32_16x16x32_bf16 v[176:179], v[142:145], v[232:235], v[148:151]\n"
      "v_cmp_ne_u32 s[56:57], 0, v190\n"
      "v_cvt_pk_bf16_f32 v131, v106, v107\n"
      "v_cvt_pk_bf16_f32 v235, v140, v141\n"
      "v_cvt_pk_bf16_f32 v182, v182, v183\n"
      "v_cvt_pk_bf16_f32 v234, v138, v139\n"
      "v_cvt_pk_bf16_f32 v90, v198, 0\n"
      "v_mfma_f32_16x16x32_bf16 v[188:191], v[142:145], a[216:219], v[148:151]\n"
      "s_nop 2\n"
      "s_cbranch_vccnz L_rare_5_%=\n"
      "L_back_6_%=:\n"
      "v_mfma_f32_16x16x32_bf16 v[138:141], v[160:163], a[216:219], v[184:187]\n"
      "v_exp_f32_e64 v220, v220\n"
      "v_mfma_f32_16x16x32_bf16 v[146:149], v[160:163], a[240:243], v[184:187]\n"
      "ds_read_u16 v86, v11 offset:64\n"
      "v_cvt_pk_bf16_f32 v232, v176, v177\n"
      "v_mfma_f32_16x16x32_bf16 v[184:187], v[214:217], v[210:213], 0\n"
      "s_waitcnt lgkmcnt(1)\n"
      "v_cvt_pk_bf16_f32 v233, v178, v179\n"
      "v_cvt_pk_bf16_f32 v188, v188, v189\n"
      "s_waitcnt vmcnt(1)\n"
      "v_or3_b32 v156, v156, v157, v158\n"
      "v_mfma_f32_16x16x32_bf16 v[184:187], v[80:83], v[88:91], v[184:187]\n"
      "v_cvt_pk_bf16_f32 v146, v146, v147\n"
      "v_cvt_pk_bf16_f32 v140, v140, v141\n"
      "v_mfma_f32_16x16x32_bf16 v[176:179], v[32:35], v[210:213], 0\n"
      "ds_read_b64 v[150:151], v10 offset:16\n"
      "v_accvgpr_write_b32 a216, v188\n"
      "v_mfma_f32_16x16x32_bf16 v[176:179], v[32:35], v[88:91], v[176:179]\n"
      "v_cvt_pk_bf16_f32 v180, v180, v181\n"
      "v_accvgpr_write_b32 a219, v140\n"
      "v_accvgpr_write_b32 a242, v146\n"
      "v_accvgpr_write_b32 a241, v182\n"
      "v_mfma_f32_16x16x32_bf16 v[114:117], v[120:123], v[124:127], v[28:31]\n"
      "v_cvt_pk_bf16_f32 v138, v138, v139\n"
      "v_cvt_pk_bf16_f32 v190, v190, v191\n"
      "v_cvt_pk_bf16_f32 v148, v148, v149\n"
      "v_mfma_f32_16x16x32_bf16 v[96:99], v[152:155], v[124:127], v[72:75]\n"
      "s_waitcnt lgkmcnt(1)\n"
      "v_accvgpr_write_b32 a217, v190\n"
      "s_waitcnt vmcnt(0)\n"
      "v_or3_b32 v156, v156, v159, v94\n"
      "v_mfma_f32_16x16x32_bf16 v[188:191], v[194:197], v[210:213], 0\n"
      "s_cmp_ge_u32 s95, 0x40000000\n"
      "s_cselect_b32 s94, -1, -2\n"
      "v_accvgpr_write_b32 a243, v148\n"
      "v_mfma_f32_16x16x32_bf16 v[188:191], v[84:87], v[88:91], v[188:191]\n"
      "v_exp_f32_e64 v110, v110\n"
      "v_mfma_f32_16x16x32_bf16 v[146:149], v[130:133], v[210:213], 0\n"
      "v_exp_f32 v111, v111\n"
      "v_mfma_f32_16x16x32_bf16 v[146:149], v[76:79], v[88:91], v[146:149]\n"
      "ds_read_b128 v[210:213], v10\n"
      "v_rcp_f32 v176, v176\n"
      "v_mfma_f32_16x16x16_bf16 v[114:117], v[172:173], v[218:219], v[114:117]\n"
      "v_exp_f32 v167, v167\n"
      "v_exp_f32 v169, v169\n"
      "v_exp_f32_e64 v113, v113\n"
      "v_pk_mul_f32 v[190:191], v[190:191], v[176:177] op_sel_hi:[1,0]\n"
      "v_pk_mul_f32 v[188:189], v[188:189], v[176:177] op_sel_hi:[1,0]\n"
      "v_pk_mul_f32 v[146:147], v[146:147], v[176:177] op_sel_hi:[1,0]\n"
      "v_exp_f32_e64 v101, v101\n"
      "v_mfma_f32_16x16x16_bf16 v[96:99], v[128:129], v[218:219], v[96:99]\n"
      "v_cvt_pk_bf16_f32 v94, v220, 0\n"
      "v_cvt_pk_bf16_f32 v110, v110, v111\n"
      "v_accvgpr_write_b32 a218, v138\n"
      "v_mfma_f32_16x16x32_bf16 v[220:223], v[202:205], v[124:127], v[64:67]\n"
      "ds_write_b32 v31, v156 offset:4608\n"
      "v_mfma_f32_16x16x32_bf16 v[138:141], v[206:209], v[124:127], v[68:71]\n"
      "s_waitcnt lgkmcnt(1)\n"
      "v_exp_f32 v103, v103\n"
      "v_exp_f32_e64 v100, v100\n"
      "v_cvt_pk_bf16_f32 v146, v146, v147\n"
      "v_exp_f32 v112, v112\n"
      "v_pk_mul_f32 v[148:149], v[148:149], v[176:177] op_sel_hi:[1,0]\n"
      "v_exp_f32 v166, v166\n"
      "v_pk_mul_f32 v[184:185], v[184:185], v[176:177] op_sel_hi:[1,0]\n"
      "v_pk_mul_f32 v[186:187], v[186:187], v[176:177] op_sel_hi:[1,0]\n"
      "v_cvt_pk_bf16_f32 v111, v112, v113\n"
      "v_cvt_pk_bf16_f32 v147, v148, v149\n"
      "v_mfma_f32_16x16x32_bf16 v[124:127], v[210:213], v[206:209], v[30:33]\n"
      "ds_read_b128 v[206:209], v39 offset:192\n"
      "v_accvgpr_write_b32 a240, v180\n"
      "v_mfma_f32_16x16x16_bf16 v[138:141], v[108:109], v[218:219], v[138:141]\n"
      "v_exp_f32_e64 v168, v168\n"
      "v_cvt_pk_bf16_f32 v92, v100, v101\n"
      "v_mfma_f32_16x16x16_bf16 v[220:223], v[174:175], v[218:219], v[220:223]\n"
      "v_cvt_pk_bf16_f32 v149, v186, v187\n"
      "v_cvt_pk_bf16_f32 v148, v184, v185\n"
      "v_cvt_pk_bf16_f32 v112, v166, v167\n"
      "v_mfma_f32_16x16x32_bf16 v[180:183], v[210:213], v[120:123], v[30:33]\n"
      "v_exp_f32_e64 v102, v102\n"
      "v_mfma_f32_16x16x32_bf16 v[176:179], v[210:213], v[202:205], v[30:33]\n"
      "ds_read_b128 v[202:205], v39 offset:384\n"
      "v_cvt_pk_bf16_f32 v113, v168, v169\n"
      "v_mfma_f32_16x16x32_bf16 v[166:169], v[210:213], v[152:155], v[30:33]\n"
      "ds_write_b128 v3, v[146:149]\n"
      "v_mfma_f32_16x16x32_bf16 v[210:213], v[32:35], v[110:113], 0\n"
      "s_cbranch_vccnz L_rare_7_%=\n"
      "L_back_8_%=:\n"
      "v_mfma_f32_16x16x32_bf16 v[146:149], v[214:217], v[110:113], 0\n"
      "s_waitcnt lgkmcnt(2)\n"
      "v_exp_f32_e64 v141, v141\n"
      "v_mfma_f32_16x16x32_bf16 v[116:119], v[160:163], a[220:223], v[206:209]\n"
      "ds_read_b128 v[152:155], v39 offset:128\n"
      "v_exp_f32 v222, v222\n"
      "v_mfma_f32_16x16x16_bf16 v[124:127], v[150:151], v[108:109], v[124:127]\n"
      "v_exp_f32 v98, v98\n"
      "v_exp_f32 v220, v220\n"
      "v_mfma_f32_16x16x16_bf16 v[180:183], v[150:151], v[172:173], v[180:183]\n"
      "v_cvt_pk_bf16_f32 v93, v102, v103\n"
      "v_exp_f32 v99, v99\n"
      "v_mfma_f32_16x16x16_bf16 v[176:179], v[150:151], v[174:175], v[176:179]\n"
      "v_exp_f32_e64 v223, v223\n"
      "v_cvt_pk_bf16_f32 v188, v188, v189\n"
      "v_mfma_f32_16x16x32_bf16 v[106:109], v[130:133], v[110:113], 0\n"
      "v_exp_f32_e64 v221, v221\n"
      "v_mfma_f32_16x16x32_bf16 v[106:109], v[76:79], v[92:95], v[106:109]\n"
      "s_cmp_gt_u32 s42, s44\n"
      "s_cselect_b32 s95, s94, s95\n"
      "v_cvt_pk_bf16_f32 v116, v116, v117\n"
      "v_mfma_f32_16x16x32_bf16 v[172:175], v[160:163], a[244:247], v[206:209]\n"
      "s_waitcnt lgkmcnt(0)\n"
      "v_exp_f32 v97, v97\n"
      "v_mfma_f32_16x16x32_bf16 v[182:185], v[142:145], a[220:223], v[152:155]\n"
      "v_exp_f32_e64 v138, v138\n"
      "v_mfma_f32_16x16x32_bf16 v[100:103], v[142:145], a[244:247], v[152:155]\n"
      "ds_read_b128 v[120:123], v39 offset:448\n"
      "v_cvt_pk_bf16_f32 v89, v98, v99\n"
      "v_mfma_f32_16x16x16_bf16 v[166:169], v[150:151], v[128:129], v[166:169]\n"
      "v_exp_f32_e64 v96, v96\n"
      "v_exp_f32 v140, v140\n"
      "v_mfma_f32_16x16x32_bf16 v[210:213], v[32:35], v[92:95], v[210:213]\n"
      "v_cvt_pk_bf16_f32 v174, v174, v175\n"
      "v_cndmask_b32 v176, v176, v124, s[58:59]\n"
      "v_mfma_f32_16x16x32_bf16 v[124:127], v[142:145], v[236:239], v[152:155]\n"
      "s_add_u32 s42, s42, 1\n"
      "v_accvgpr_write_b32 a222, v116\n"
      "v_cvt_pk_bf16_f32 v220, v220, v221\n"
      "v_cndmask_b32 v176, v176, v166, s[60:61]\n"
      "v_cvt_pk_bf16_f32 v182, v182, v183\n"
      "v_mfma_f32_16x16x32_bf16 v[166:169], v[194:197], v[110:113], 0\n"
      "v_exp_f32_e64 v139, v139\n"
      "v_mfma_f32_16x16x32_bf16 v[166:169], v[84:87], v[92:95], v[166:169]\n"
      "v_exp_f32_e64 v114, v114\n"
      "v_mfma_f32_16x16x32_bf16 v[110:113], v[142:145], a[228:231], v[202:205]\n"
      "v_rcp_f32_e64 v210, v210\n"
      "v_cvt_pk_bf16_f32 v184, v184, v185\n"
      "v_cvt_pk_bf16_f32 v221, v222, v223\n"
      "v_cvt_pk_bf16_f32 v118, v118, v119\n"
      "v_cvt_pk_bf16_f32 v223, v140, v141\n"
      "v_mfma_f32_16x16x32_bf16 v[150:153], v[142:145], a[252:255], v[202:205]\n"
      "v_accvgpr_write_b32 a247, v174\n"
      "v_cvt_pk_bf16_f32 v102, v102, v103\n"
      "v_cvt_pk_bf16_f32 v172, v172, v173\n"
      "v_mfma_f32_16x16x32_bf16 v[198:201], v[160:163], v[236:239], v[206:209]\n"
      "s_nop 2\n"
      "ds_read_b128 v[206:209], v39 offset:320\n"
      "v_cvt_pk_bf16_f32 v189, v190, v191\n"
      "v_mfma_f32_16x16x32_bf16 v[146:149], v[80:83], v[92:95], v[146:149]\n"
      "s_waitcnt lgkmcnt(1)\n"
      "v_cvt_pk_bf16_f32 v88, v96, v97\n"
      "v_cvt_pk_bf16_f32 v222, v138, v139\n"
      "v_cndmask_b32 v176, v176, v180, s[62:63]\n"
      "v_accvgpr_write_b32 a246, v172\n"
      "v_mfma_f32_16x16x32_bf16 v[190:193], v[142:145], v[244:247], v[202:205]\n"
      "ds_write_b64 v4, v[188:189]\n"
      "v_mfma_f32_16x16x32_bf16 v[202:205], v[160:163], v[244:247], v[120:123]\n"
      "ds_read_b128 v[186:189], v39 offset:256\n"
      "v_cvt_pk_bf16_f32 v150, v150, v151\n"
      "v_mfma_f32_16x16x32_bf16 v[172:175], v[160:163], a[228:231], v[120:123]\n"
      "s_cbranch_vccnz L_rare_9_%=\n"
      "L_back_10_%=:\n"
      "v_mfma_f32_16x16x32_bf16 v[138:141], v[194:197], v[220:223], 0\n"
      "v_cvt_pk_bf16_f32 v90, v114, 0\n"
      "v_cvt_pk_bf16_f32 v110, v110, v111\n"
      "v_accvgpr_write_b32 a221, v184\n"
      "v_mfma_f32_16x16x32_bf16 v[138:141], v[84:87], v[88:91], v[138:141]\n"
      "v_exp_f32 v176, v176\n"
      "v_mfma_f32_16x16x32_bf16 v[178:181], v[160:163], a[252:255], v[120:123]\n"
      "v_cvt_pk_bf16_f32 v176, v176, v176\n"
      "v_cvt_pk_bf16_f32 v112, v112, v113\n"
      "v_cvt_pk_bf16_f32 v100, v100, v101\n"
      "v_accvgpr_write_b32 a223, v118\n"
      "v_mfma_f32_16x16x32_bf16 v[96:99], v[214:217], v[220:223], 0\n"
      "ds_read_b128 v[118:121], v39 offset:512\n"
      "v_cvt_pk_bf16_f32 v239, v200, v201\n"
      "v_mfma_f32_16x16x32_bf16 v[96:99], v[80:83], v[88:91], v[96:99]\n"
      "ds_read_b128 v[114:117], v39 offset:640\n"
      "v_accvgpr_write_b32 a244, v100\n"
      "v_mfma_f32_16x16x32_bf16 v[156:159], v[32:35], v[220:223], 0\n"
      "ds_write_b16 v13, v176\n"
      "v_mfma_f32_16x16x32_bf16 v[156:159], v[32:35], v[88:91], v[156:159]\n"
      "v_cvt_pk_bf16_f32 v174, v174, v175\n"
      "v_cvt_pk_bf16_f32 v152, v152, v153\n"
      "v_cvt_pk_bf16_f32 v245, v192, v193\n"
      "v_accvgpr_write_b32 a245, v102\n"
      "v_accvgpr_write_b32 a220, v182\n"
      "v_mfma_f32_16x16x32_bf16 v[182:185], v[130:133], v[220:223], 0\n"
      "s_waitcnt lgkmcnt(3)\n"
      "v_cvt_pk_bf16_f32 v238, v198, v199\n"
      "v_cvt_pk_bf16_f32 v247, v204, v205\n"
      "v_mfma_f32_16x16x32_bf16 v[182:185], v[76:79], v[88:91], v[182:185]\n"
      "s_cmp_ge_u32 s95, 0x40000000\n"
      "s_cselect_b64 vcc, -1, s[56:57]\n"
      "v_cvt_pk_bf16_f32 v244, v190, v191\n"
      "v_mfma_f32_16x16x32_bf16 v[190:193], v[142:145], a[224:227], v[186:189]\n"
      "v_accvgpr_write_b32 a252, v150\n"
      "v_rcp_f32 v156, v156\n"
      "v_mfma_f32_16x16x32_bf16 v[218:221], v[160:163], a[248:251], v[206:209]\n"
      "ds_read_b128 v[102:105], v39 offset:576\n"
      "v_cvt_pk_bf16_f32 v236, v124, v125\n"
      "v_cvt_pk_bf16_f32 v246, v202, v203\n"
      "v_mfma_f32_16x16x32_bf16 v[202:205], v[142:145], a[248:251], v[186:189]\n"
      "ds_read_b128 v[122:125], v39 offset:704\n"
      "v_accvgpr_write_b32 a228, v110\n"
      "v_cvt_pk_bf16_f32 v172, v172, v173\n"
      "v_cvt_pk_bf16_f32 v180, v180, v181\n"
      "v_accvgpr_write_b32 a253, v152\n"
      "v_accvgpr_write_b32 a229, v112\n"
      "v_pk_mul_f32 v[182:183], v[182:183], v[156:157] op_sel_hi:[1,0]\n"
      "v_pk_mul_f32 v[184:185], v[184:185], v[156:157] op_sel_hi:[1,0]\n"
      "v_pk_mul_f32 v[140:141], v[140:141], v[156:157] op_sel_hi:[1,0]\n"
      "v_pk_mul_f32 v[166:167], v[166:167], v[210:211] op_sel_hi:[1,0]\n"
      "v_pk_mul_f32 v[108:109], v[108:109], v[210:211] op_sel_hi:[1,0]\n"
      "v_pk_mul_f32 v[168:169], v[168:169], v[210:211] op_sel_hi:[1,0]\n"
      "v_pk_mul_f32 v[106:107], v[106:107], v[210:211] op_sel_hi:[1,0]\n"
      "v_pk_mul_f32 v[96:97], v[96:97], v[156:157] op_sel_hi:[1,0]\n"
      "v_pk_mul_f32 v[98:99], v[98:99], v[156:157] op_sel_hi:[1,0]\n"
      "v_pk_mul_f32 v[138:139], v[138:139], v[156:157] op_sel_hi:[1,0]\n"
      "v_pk_mul_f32 v[148:149], v[148:149], v[210:211] op_sel_hi:[1,0]\n"
      "v_cvt_pk_bf16_f32 v220, v220, v221\n"
      "v_pk_mul_f32 v[146:147], v[146:147], v[210:211] op_sel_hi:[1,0]\n"
      "v_cvt_pk_bf16_f32 v237, v126, v127\n"
      "v_mfma_f32_16x16x32_bf16 v[126:129], v[160:163], a[224:227], v[206:209]\n"
      "v_cvt_pk_bf16_f32 v202, v202, v203\n"
      "v_cvt_pk_bf16_f32 v192, v192, v193\n"
      "v_cvt_pk_bf16_f32 v204, v204, v205\n"
      "v_mfma_f32_16x16x32_bf16 v[110:113], v[142:145], v[240:243], v[186:189]\n"
      "s_waitcnt lgkmcnt(0)\n"
      "v_cvt_pk_bf16_f32 v138, v138, v139\n"
      "v_accvgpr_write_b32 a248, v202\n"
      "v_accvgpr_write_b32 a231, v174\n"
      "v_mfma_f32_16x16x32_bf16 v[200:203], v[160:163], a[232:235], v[102:105]\n"
      "ds_read_b128 v[156:159], v14\n"
      "v_cvt_pk_bf16_f32 v106, v106, v107\n"
      "v_accvgpr_write_b32 a225, v192\n"
      "v_mfma_f32_16x16x32_bf16 v[186:189], v[160:163], v[240:243], v[206:209]\n"
      "v_cvt_pk_bf16_f32 v166, v166, v167\n"
      "v_cvt_pk_bf16_f32 v178, v178, v179\n"
      "v_cvt_pk_bf16_f32 v128, v128, v129\n"
      "v_mfma_f32_16x16x32_bf16 v[206:209], v[142:145], a[236:239], v[114:117]\n"
      "v_accvgpr_write_b32 a255, v180\n"
      "v_cvt_pk_bf16_f32 v126, v126, v127\n"
      "v_cvt_pk_bf16_f32 v182, v182, v183\n"
      "v_accvgpr_write_b32 a227, v128\n"
      "v_cvt_pk_bf16_f32 v240, v110, v111\n"
      "v_cvt_pk_bf16_f32 v241, v112, v113\n"
      "v_mfma_f32_16x16x32_bf16 v[110:113], v[160:163], v[224:227], v[102:105]\n"
      "s_nop 0\n"
      "v_cvt_pk_bf16_f32 v206, v206, v207\n"
      "v_cvt_pk_bf16_f32 v208, v208, v209\n"
      "v_cvt_pk_bf16_f32 v167, v168, v169\n"
      "v_mfma_f32_16x16x32_bf16 v[168:171], v[160:163], a[236:239], v[122:125]\n"
      "ds_write_b64 v4, v[166:167] offset:256\n"
      "v_cvt_pk_bf16_f32 v107, v108, v109\n"
      "v_cvt_pk_bf16_f32 v139, v140, v141\n"
      "v_cvt_pk_bf16_f32 v109, v148, v149\n"
      "v_mfma_f32_16x16x32_bf16 v[164:167], v[160:163], v[252:255], v[122:125]\n"
      "v_accvgpr_write_b32 a254, v178\n"
      "v_cvt_pk_bf16_f32 v218, v218, v219\n"
      "v_mfma_f32_16x16x32_bf16 v[178:181], v[142:145], a[232:235], v[118:121]\n"
      "ds_write_b64 v4, v[138:139] offset:512\n"
      "v_cvt_pk_bf16_f32 v243, v188, v189\n"
      "v_accvgpr_write_b32 a237, v208\n"
      "v_cvt_pk_bf16_f32 v168, v168, v169\n"
      "v_mfma_f32_16x16x32_bf16 v[138:141], v[142:145], v[248:251], v[118:121]\n"
      "v_cvt_pk_bf16_f32 v190, v190, v191\n"
      "v_cvt_pk_bf16_f32 v183, v184, v185\n"
      "v_cvt_pk_bf16_f32 v108, v146, v147\n"
      "v_mfma_f32_16x16x32_bf16 v[146:149], v[142:145], v[224:227], v[118:121]\n"
      "v_accvgpr_write_b32 a226, v126\n"
      "v_cvt_pk_bf16_f32 v180, v180, v181\n"
      "v_cvt_pk_bf16_f32 v226, v110, v111\n"
      "v_cvt_pk_bf16_f32 v170, v170, v171\n"
      "v_mfma_f32_16x16x32_bf16 v[126:129], v[142:145], v[252:255], v[114:117]\n"
      "ds_read_b128 v[210:213], v14 offset:64\n"
      "v_cvt_pk_bf16_f32 v184, v96, v97\n"
      "v_mfma_f32_16x16x32_bf16 v[118:121], v[142:145], v[228:231], v[114:117]\n"
      "s_waitcnt lgkmcnt(2)\n"
      "v_accvgpr_write_b32 a224, v190\n"
      "v_cvt_pk_bf16_f32 v200, v200, v201\n"
      "v_mfma_f32_16x16x32_bf16 v[142:145], v[160:163], v[248:251], v[102:105]\n"
      "ds_write_b128 v3, v[106:109] offset:256\n"
      "v_cvt_pk_bf16_f32 v178, v178, v179\n"
      "v_accvgpr_write_b32 a233, v180\n"
      "v_accvgpr_write_b32 a239, v170\n"
      "v_cvt_pk_bf16_f32 v227, v112, v113\n"
      "v_accvgpr_write_b32 a251, v220\n"
      "v_accvgpr_write_b32 a236, v206\n"
      "v_cvt_pk_bf16_f32 v253, v128, v129\n"
      "v_cvt_pk_bf16_f32 v185, v98, v99\n"
      "v_mfma_f32_16x16x32_bf16 v[108:111], v[160:163], v[228:231], v[122:125]\n"
      "ds_write_b128 v3, v[182:185] offset:512\n"
      "v_cvt_pk_bf16_f32 v202, v202, v203\n"
      "v_cvt_pk_bf16_f32 v229, v120, v121\n"
      "v_cvt_pk_bf16_f32 v251, v144, v145\n"
      "v_cvt_pk_bf16_f32 v242, v186, v187\n"
      "v_accvgpr_write_b32 a234, v200\n"
      "v_cvt_pk_bf16_f32 v225, v148, v149\n"
      "v_cvt_pk_bf16_f32 v252, v126, v127\n"
      "v_cvt_pk_bf16_f32 v224, v146, v147\n"
      "v_accvgpr_write_b32 a250, v218\n"
      "v_mfma_f32_16x16x32_bf16 v[152:155], v[156:159], v[32:35], 0\n"
      "s_waitcnt lgkmcnt(1)\n"
      "v_cvt_pk_bf16_f32 v255, v166, v167\n"
      "v_accvgpr_write_b32 a238, v168\n"
      "v_accvgpr_write_b32 a249, v204\n"
      "v_accvgpr_write_b32 a235, v202\n"
      "v_cvt_pk_bf16_f32 v249, v140, v141\n"
      "v_accvgpr_write_b32 a232, v178\n"
      "v_cvt_pk_bf16_f32 v228, v118, v119\n"
      "v_cvt_pk_bf16_f32 v248, v138, v139\n"
      "v_accvgpr_write_b32 a230, v172\n"
      "v_cvt_pk_bf16_f32 v230, v108, v109\n"
      "v_cvt_pk_bf16_f32 v250, v142, v143\n"
      "v_cvt_pk_bf16_f32 v231, v110, v111\n"
      "v_cvt_pk_bf16_f32 v254, v164, v165\n"
      "v_mfma_f32_16x16x32_bf16 v[152:155], v[210:213], v[32:35], v[152:155]\n"
      "s_waitcnt lgkmcnt(0)\n"
      "s_barrier\n"
      "s_or_b64 vcc, vcc, s[54:55]\n"
      "s_cbranch_vccnz L_disp1_%=\n"
      "s_nop 7\n"
      "s_nop 7\n"
      "L_loop1_%=:\n"
      "v_mfma_f32_16x16x32_bf16 v[218:221], v[156:159], v[130:133], 0\n"
      "ds_read_b128 v[100:103], v0 offset:18432\n"
      "v_readlane_b32 s46, v37, s42\n"
      "v_mfma_f32_16x16x32_bf16 v[218:221], v[210:213], v[76:79], v[218:221]\n"
      "ds_read_b128 v[178:181], v9\n"
      "v_mfma_f32_16x16x32_bf16 v[134:137], v[156:159], v[214:217], 0\n"
      "ds_read_b128 v[96:99], v1 offset:24576\n"
      "s_add_u32 m0, s64, 0\n"
      "v_mfma_f32_16x16x32_bf16 v[134:137], v[210:213], v[80:83], v[134:137]\n"
      "ds_read_b128 v[190:193], v0 offset:24576\n"
      "ds_read_b128 v[140:143], v1 offset:18432\n"
      "v_mfma_f32_16x16x32_bf16 v[128:131], v[156:159], v[194:197], 0\n"
      "buffer_load_dwordx4 v16, s[16:19], s84 offen  lds\n"
      "v_mfma_f32_16x16x32_bf16 v[128:131], v[210:213], v[84:87], v[128:131]\n"
      "s_add_u32 m0, s64, 4096\n"
      "s_waitcnt lgkmcnt(4)\n"
      "v_mfma_f32_16x16x32_bf16 v[210:213], a[0:3], v[100:103], 0\n"
      "ds_read_b128 v[104:107], v0 offset:30720\n"
      "v_cndmask_b32 v218, v218, v134, s[58:59]\n"
      "v_mfma_f32_16x16x32_bf16 v[158:161], a[24:27], v[100:103], 0\n"
      "s_waitcnt lgkmcnt(4)\n"
      "v_add_f32 v180, v180, v181\n"
      "v_mov_b32_dpp v152, v152 quad_perm:[0,0,0,0] row_mask:0xf bank_mask:0xf\n"
      "v_mfma_f32_16x16x32_bf16 v[120:123], a[48:51], v[100:103], 0\n"
      "v_add_f32 v178, v178, v179\n"
      "v_add_f32 v178, v178, v180\n"
      "v_mfma_f32_16x16x32_bf16 v[162:165], a[72:75], v[100:103], 0\n"
      "buffer_load_dwordx4 v17, s[16:19], s84 offen  lds\n"
      "v_mfma_f32_16x16x32_bf16 v[108:111], a[96:99], v[100:103], 0\n"
      "s_add_u32 m0, s64, 8192\n"
      "v_rcp_f32 v152, v152\n"
      "v_mfma_f32_16x16x32_bf16 v[182:185], a[120:123], v[100:103], 0\n"
      "s_waitcnt lgkmcnt(1)\n"
      "v_rsq_f32_e64 v178, v178\n"
      "v_mfma_f32_16x16x32_bf16 v[210:213], a[4:7], v[140:143], v[210:213]\n"
      "v_readlane_b32 s45, v37, s43\n"
      "v_mul_f32 v178, 0x415db3d7, v178\n"
      "v_mfma_f32_16x16x32_bf16 v[210:213], a[8:11], v[190:193], v[210:213]\n"
      "ds_read_b128 v[154:157], v1 offset:30720\n"
      "v_mov_b32 v180, v178\n"
      "v_mfma_f32_16x16x32_bf16 v[158:161], a[28:31], v[140:143], v[158:161]\n"
      "buffer_load_dwordx4 v18, s[16:19], s84 offen  lds\n"
      "v_mfma_f32_16x16x32_bf16 v[158:161], a[32:35], v[190:193], v[158:161]\n"
      "v_permlane32_swap_b32 v178, v180\n"
      "v_mfma_f32_16x16x32_bf16 v[120:123], a[52:55], v[140:143], v[120:123]\n"
      "ds_read_b128 v[198:201], v0 offset:32768\n"
      "v_mov_b32 v179, v178\n"
      "v_mfma_f32_16x16x32_bf16 v[120:123], a[56:59], v[190:193], v[120:123]\n"
      "s_nop 0\n"
      "v_permlane16_swap_b32 v178, v179\n"
      "v_mfma_f32_16x16x32_bf16 v[162:165], a[76:79], v[140:143], v[162:165]\n"
      "s_add_u32 m0, s64, 12288\n"
      "v_cndmask_b32 v218, v218, v128, s[60:61]\n"
      "v_mul_f32 v187, v179, v187\n"
      "v_mfma_f32_16x16x32_bf16 v[162:165], a[80:83], v[190:193], v[162:165]\n"
      "buffer_load_dwordx4 v19, s[16:19], s84 offen  lds\n"
      "v_mfma_f32_16x16x32_bf16 v[108:111], a[100:103], v[140:143], v[108:111]\n"
      "s_add_u32 m0, s65, 16384\n"
      "v_mul_f32 v174, v179, v174\n"
      "v_mul_f32 v203, v180, v203\n"
      "v_mfma_f32_16x16x32_bf16 v[108:111], a[104:107], v[190:193], v[108:111]\n"
      "v_mul_f32 v188, v179, v188\n"
      "v_mul_f32 v186, v179, v186\n"
      "v_mfma_f32_16x16x32_bf16 v[182:185], a[124:127], v[140:143], v[182:185]\n"
      "s_or_b32 s95, s45, s46\n"
      "v_mul_f32 v168, v178, v168\n"
      "v_mul_f32 v166, v178, v166\n"
      "v_mfma_f32_16x16x32_bf16 v[182:185], a[128:131], v[190:193], v[182:185]\n"
      "s_waitcnt lgkmcnt(0)\n"
      "v_mul_f32 v207, v179, v207\n"
      "v_mul_f32 v175, v179, v175\n"
      "v_mfma_f32_16x16x32_bf16 v[210:213], a[12:15], v[96:99], v[210:213]\n"
      "buffer_load_dwordx4 v15, s[16:19], s84 offen  lds\n"
      "v_mfma_f32_16x16x32_bf16 v[210:213], a[16:19], v[104:107], v[210:213]\n"
      "ds_read_b128 v[220:223], v0 offset:26624\n"
      "v_mul_f32 v113, v178, v113\n"
      "v_mfma_f32_16x16x32_bf16 v[210:213], a[20:23], v[154:157], v[210:213]\n"
      "v_mul_f32 v119, v178, v119\n"
      "v_mul_f32 v115, v178, v115\n"
      "v_mfma_f32_16x16x32_bf16 v[158:161], a[36:39], v[96:99], v[158:161]\n"
      "s_or_b32 s95, s95, s48\n"
      "v_mul_f32 v218, v152, v218\n"
      "v_cvt_pk_bf16_f32 v186, v186, v187\n"
      "v_mfma_f32_16x16x32_bf16 v[158:161], a[40:43], v[104:107], v[158:161]\n"
      "v_mul_f32 v177, v179, v177\n"
      "v_fma_f32 v77, v210, v210, s51\n"
      "v_mfma_f32_16x16x32_bf16 v[158:161], a[44:47], v[154:157], v[158:161]\n"
      "buffer_load_dwordx4 v[170:173], v36, s[28:31], s86 offen\n"
      "v_mfma_f32_16x16x32_bf16 v[120:123], a[60:63], v[96:99], v[120:123]\n"
      "v_mul_f32 v189, v179, v189\n"
      "v_mul_f32 v176, v179, v176\n"
      "v_mfma_f32_16x16x32_bf16 v[120:123], a[64:67], v[104:107], v[120:123]\n"
      "v_fmac_f32 v77, v211, v211\n"
      "v_mul_f32 v167, v178, v167\n"
      "v_mfma_f32_16x16x32_bf16 v[120:123], a[68:71], v[154:157], v[120:123]\n"
      "v_mul_f32_e64 v147, v180, v147\n"
      "v_cvt_pk_bf16_f32 v187, v188, v189\n"
      "v_mfma_f32_16x16x32_bf16 v[162:165], a[84:87], v[96:99], v[162:165]\n"
      "v_cvt_pk_bf16_f32 v218, v218, v218\n"
      "v_cvt_pk_bf16_f32 v188, v174, v175\n"
      "v_mfma_f32_16x16x32_bf16 v[162:165], a[88:91], v[104:107], v[162:165]\n"
      "buffer_load_dword v86, v29, s[28:31], s86 offen\n"
      "v_mfma_f32_16x16x32_bf16 v[162:165], a[92:95], v[154:157], v[162:165]\n"
      "v_mul_f32 v114, v178, v114\n"
      "v_fmac_f32 v77, v212, v212\n"
      "v_mfma_f32_16x16x32_bf16 v[108:111], a[108:111], v[96:99], v[108:111]\n"
      "s_cmp_ge_u32 s95, 0x40000000\n"
      "s_cselect_b32 s94, -1, -2\n"
      "v_fmac_f32 v77, v213, v213\n"
      "v_mfma_f32_16x16x32_bf16 v[108:111], a[112:115], v[104:107], v[108:111]\n"
      "s_nop 0\n"
      "v_fma_f32 v81, v162, v162, s51\n"
      "v_cvt_pk_bf16_f32 v166, v166, v167\n"
      "v_mfma_f32_16x16x32_bf16 v[108:111], a[116:119], v[154:157], v[108:111]\n"
      "v_cvt_pk_bf16_f32 v189, v176, v177\n"
      "v_mul_f32 v118, v178, v118\n"
      "v_mfma_f32_16x16x32_bf16 v[182:185], a[132:135], v[96:99], v[182:185]\n"
      "ds_write_b128 v5, v[186:189] offset:24576\n"
      "v_mfma_f32_16x16x32_bf16 v[182:185], a[136:139], v[104:107], v[182:185]\n"
      "v_mul_f32 v146, v180, v146\n"
      "v_mul_f32 v169, v178, v169\n"
      "v_mfma_f32_16x16x32_bf16 v[182:185], a[140:143], v[154:157], v[182:185]\n"
      "v_mul_f32 v117, v178, v117\n"
      "v_fmac_f32 v81, v163, v163\n"
      "v_mfma_f32_16x16x32_bf16 v[130:133], v[100:103], a[144:147], 0\n"
      "v_mul_f32 v208, v179, v208\n"
      "v_cvt_pk_bf16_f32 v167, v168, v169\n"
      "v_mfma_f32_16x16x32_bf16 v[130:133], v[140:143], a[148:151], v[130:133]\n"
      "ds_read_b128 v[186:189], v1 offset:26624\n"
      "v_fmac_f32_e64 v77, v158, v158\n"
      "v_mfma_f32_16x16x32_bf16 v[130:133], v[190:193], a[152:155], v[130:133]\n"
      "v_mul_f32 v206, v179, v206\n"
      "v_cvt_pk_bf16_f32 v169, v114, v115\n"
      "v_mfma_f32_16x16x32_bf16 v[130:133], v[96:99], a[156:159], v[130:133]\n"
      "v_fmac_f32 v81, v164, v164\n"
      "v_mul_f32 v112, v178, v112\n"
      "v_mfma_f32_16x16x32_bf16 v[130:133], v[104:107], a[160:163], v[130:133]\n"
      "v_fmac_f32 v77, v159, v159\n"
      "v_readlane_b32 s47, v38, s43\n"
      "v_mfma_f32_16x16x32_bf16 v[130:133], v[154:157], a[164:167], v[130:133]\n"
      "v_fmac_f32 v77, v160, v160\n"
      "v_fmac_f32 v81, v165, v165\n"
      "v_mfma_f32_16x16x32_bf16 v[214:217], v[100:103], a[168:171], 0\n"
      "v_mul_f32 v202, v180, v202\n"
      "v_mul_f32 v144, v180, v144\n"
      "v_mfma_f32_16x16x32_bf16 v[214:217], v[140:143], a[172:175], v[214:217]\n"
      "ds_read_b128 v[134:137], v0 offset:20480\n"
      "v_fmac_f32_e64 v81, v108, v108\n"
      "v_mfma_f32_16x16x32_bf16 v[214:217], v[190:193], a[176:179], v[214:217]\n"
      "v_cvt_pk_bf16_f32 v202, v202, v203\n"
      "v_cvt_pk_bf16_f32 v168, v112, v113\n"
      "v_mfma_f32_16x16x32_bf16 v[214:217], v[96:99], a[180:183], v[214:217]\n"
      "v_mul_f32 v205, v180, v205\n"
      "v_fmac_f32 v81, v109, v109\n"
      "v_mfma_f32_16x16x32_bf16 v[214:217], v[104:107], a[184:187], v[214:217]\n"
      "ds_read_u16 v78, v11 offset:1168\n"
      "v_fmac_f32 v81, v110, v110\n"
      "v_mfma_f32_16x16x32_bf16 v[214:217], v[154:157], a[188:191], v[214:217]\n"
      "v_mul_f32 v204, v180, v204\n"
      "v_mul_f32_e64 v116, v178, v116\n"
      "v_mfma_f32_16x16x32_bf16 v[194:197], v[100:103], a[192:195], 0\n"
      "ds_read_b128 v[100:103], v1 offset:20480\n"
      "v_fmac_f32_e64 v77, v161, v161\n"
      "v_mfma_f32_16x16x32_bf16 v[194:197], v[140:143], a[196:199], v[194:197]\n"
      "ds_read_b128 v[124:127], v9 offset:4608\n"
      "v_cvt_pk_bf16_f32 v130, v130, v131\n"
      "v_mfma_f32_16x16x32_bf16 v[194:197], v[190:193], a[200:203], v[194:197]\n"
      "ds_read_b128 v[190:193], v1 offset:32768\n"
      "v_fmac_f32 v77, v120, v120\n"
      "v_mfma_f32_16x16x32_bf16 v[194:197], v[96:99], a[204:207], v[194:197]\n"
      "ds_write_b128 v5, v[166:169] offset:18432\n"
      "v_mfma_f32_16x16x32_bf16 v[194:197], v[104:107], a[208:211], v[194:197]\n"
      "v_fmac_f32 v81, v111, v111\n"
      "v_cvt_pk_bf16_f32 v203, v204, v205\n"
      "v_mfma_f32_16x16x32_bf16 v[194:197], v[154:157], a[212:215], v[194:197]\n"
      "s_waitcnt lgkmcnt(3)\n"
      "v_cvt_pk_bf16_f32 v214, v214, v215\n"
      "v_mul_f32 v209, v179, v209\n"
      "v_mfma_f32_16x16x32_bf16 v[104:107], a[0:3], v[134:137], 0\n"
      "s_waitcnt lgkmcnt(2)\n"
      "v_fmac_f32 v81, v182, v182\n"
      "v_or3_b32 v124, v124, v125, v126\n"
      "v_mfma_f32_16x16x32_bf16 v[104:107], a[4:7], v[100:103], v[104:107]\n"
      "v_fmac_f32 v77, v121, v121\n"
      "v_cvt_pk_bf16_f32 v205, v146, v147\n"
      "v_mfma_f32_16x16x32_bf16 v[104:107], a[8:11], v[220:223], v[104:107]\n"
      "v_mul_f32 v148, v180, v148\n"
      "v_cvt_pk_bf16_f32 v215, v216, v217\n"
      "v_mfma_f32_16x16x32_bf16 v[104:107], a[12:15], v[186:189], v[104:107]\n"
      "v_mul_f32 v145, v180, v145\n"
      "v_fmac_f32 v81, v183, v183\n"
      "v_mfma_f32_16x16x32_bf16 v[104:107], a[16:19], v[198:201], v[104:107]\n"
      "s_waitcnt lgkmcnt(0)\n"
      "v_fmac_f32 v77, v122, v122\n"
      "v_cvt_pk_bf16_f32 v206, v206, v207\n"
      "v_mfma_f32_16x16x32_bf16 v[104:107], a[20:23], v[190:193], v[104:107]\n"
      "v_cvt_pk_bf16_f32 v207, v208, v209\n"
      "v_mul_f32_e64 v151, v180, v151\n"
      "v_mfma_f32_16x16x32_bf16 v[112:115], a[24:27], v[134:137], 0\n"
      "v_fmac_f32 v81, v184, v184\n"
      "v_cvt_pk_bf16_f32 v204, v144, v145\n"
      "v_mfma_f32_16x16x32_bf16 v[112:115], a[28:31], v[100:103], v[112:115]\n"
      "s_nop 1\n"
      "v_fma_f32 v82, v104, v104, s51\n"
      "v_mfma_f32_16x16x32_bf16 v[112:115], a[32:35], v[220:223], v[112:115]\n"
      "v_cvt_pk_bf16_f32 v131, v132, v133\n"
      "v_fmac_f32_e64 v82, v105, v105\n"
      "v_mfma_f32_16x16x32_bf16 v[112:115], a[36:39], v[186:189], v[112:115]\n"
      "ds_write_b128 v5, v[202:205] offset:30720\n"
      "v_mfma_f32_16x16x32_bf16 v[112:115], a[40:43], v[198:201], v[112:115]\n"
      "v_fmac_f32 v77, v123, v123\n"
      "v_fmac_f32 v81, v185, v185\n"
      "v_mfma_f32_16x16x32_bf16 v[112:115], a[44:47], v[190:193], v[112:115]\n"
      "s_nop 0\n"
      "v_permlane16_swap_b32 v77, v81\n"
      "v_mfma_f32_16x16x32_bf16 v[96:99], a[48:51], v[134:137], 0\n"
      "v_fmac_f32 v82, v106, v106\n"
      "v_fmac_f32 v82, v107, v107\n"
      "v_mfma_f32_16x16x32_bf16 v[96:99], a[52:55], v[100:103], v[96:99]\n"
      "v_mul_f32 v150, v180, v150\n"
      "v_add_f32 v77, v77, v81\n"
      "v_mfma_f32_16x16x32_bf16 v[96:99], a[56:59], v[220:223], v[96:99]\n"
      "ds_read_b128 v[166:169], v1 offset:28672\n"
      "v_fmac_f32 v82, v112, v112\n"
      "v_mfma_f32_16x16x32_bf16 v[96:99], a[60:63], v[186:189], v[96:99]\n"
      "v_mov_b32 v81, v77\n"
      "v_cvt_pk_bf16_f32 v116, v116, v117\n"
      "v_mfma_f32_16x16x32_bf16 v[96:99], a[64:67], v[198:201], v[96:99]\n"
      "v_permlane32_swap_b32 v77, v81\n"
      "v_mfma_f32_16x16x32_bf16 v[96:99], a[68:71], v[190:193], v[96:99]\n"
      "ds_read_b128 v[142:145], v1 offset:22528\n"
      "v_mul_f32 v149, v180, v149\n"
      "v_mfma_f32_16x16x32_bf16 v[202:205], a[72:75], v[134:137], 0\n"
      "v_add_f32 v77, v77, v81\n"
      "v_bitop3_b32 v124, v124, v127, s33 bitop3:0xa8\n"
      "v_mfma_f32_16x16x32_bf16 v[202:205], a[76:79], v[100:103], v[202:205]\n"
      "v_rsq_f32 v77, v77\n"
      "v_mfma_f32_16x16x32_bf16 v[202:205], a[80:83], v[220:223], v[202:205]\n"
      "v_fmac_f32 v82, v113, v113\n"
      "v_mov_b32 v81, v77\n"
      "v_mfma_f32_16x16x32_bf16 v[202:205], a[84:87], v[186:189], v[202:205]\n"
      "s_nop 0\n"
      "v_permlane16_swap_b32 v77, v81\n"
      "v_mfma_f32_16x16x32_bf16 v[202:205], a[88:91], v[198:201], v[202:205]\n"
      "v_mul_f32 v111, v81, v111\n"
      "v_mul_f32 v109, v81, v109\n"
      "v_mfma_f32_16x16x32_bf16 v[202:205], a[92:95], v[190:193], v[202:205]\n"
      "v_fmac_f32 v82, v114, v114\n"
      "v_mul_f32 v163, v81, v163\n"
      "v_mfma_f32_16x16x32_bf16 v[178:181], a[96:99], v[134:137], 0\n"
      "v_fmac_f32 v82, v115, v115\n"
      "v_mul_f32 v110, v81, v110\n"
      "v_mfma_f32_16x16x32_bf16 v[178:181], a[100:103], v[100:103], v[178:181]\n"
      "v_fmac_f32 v82, v96, v96\n"
      "v_mul_f32 v77, s50, v77\n"
      "v_mfma_f32_16x16x32_bf16 v[178:181], a[104:107], v[220:223], v[178:181]\n"
      "ds_write_b64 v6, v[206:207] offset:24576\n"
      "v_mfma_f32_16x16x32_bf16 v[178:181], a[108:111], v[186:189], v[178:181]\n"
      "v_cvt_pk_bf16_f32 v194, v194, v195\n"
      "v_mul_f32_e64 v185, v81, v185\n"
      "v_mfma_f32_16x16x32_bf16 v[178:181], a[112:115], v[198:201], v[178:181]\n"
      "v_mul_f32 v213, v77, v213\n"
      "v_fmac_f32 v82, v97, v97\n"
      "v_mfma_f32_16x16x32_bf16 v[178:181], a[116:119], v[190:193], v[178:181]\n"
      "v_mul_f32 v120, v77, v120\n"
      "v_fmac_f32 v82, v98, v98\n"
      "v_mfma_f32_16x16x32_bf16 v[126:129], a[120:123], v[134:137], 0\n"
      "ds_read_b128 v[154:157], v0 offset:22528\n"
      "v_cvt_pk_bf16_f32 v117, v118, v119\n"
      "v_mfma_f32_16x16x32_bf16 v[126:129], a[124:127], v[100:103], v[126:129]\n"
      "ds_write_b16 v12, v218\n"
      "v_mfma_f32_16x16x32_bf16 v[126:129], a[128:131], v[220:223], v[126:129]\n"
      "v_fma_f32 v119, v202, v202, s51\n"
      "v_cvt_pk_bf16_f32 v148, v148, v149\n"
      "v_mfma_f32_16x16x32_bf16 v[126:129], a[132:135], v[186:189], v[126:129]\n"
      "v_mul_f32 v162, v81, v162\n"
      "v_fmac_f32 v119, v203, v203\n"
      "v_mfma_f32_16x16x32_bf16 v[126:129], a[136:139], v[198:201], v[126:129]\n"
      "s_cmp_gt_u32 s42, s44\n"
      "s_cselect_b32 s95, s94, s95\n"
      "v_cvt_pk_bf16_f32 v195, v196, v197\n"
      "v_mfma_f32_16x16x32_bf16 v[126:129], a[140:143], v[190:193], v[126:129]\n"
      "v_cvt_pk_bf16_f32 v149, v150, v151\n"
      "v_fmac_f32_e64 v119, v204, v204\n"
      "v_mfma_f32_16x16x32_bf16 v[216:219], v[134:137], a[144:147], 0\n"
      "v_fmac_f32 v82, v99, v99\n"
      "v_fmac_f32 v119, v205, v205\n"
      "v_mfma_f32_16x16x32_bf16 v[216:219], v[100:103], a[148:151], v[216:219]\n"
      "v_fmac_f32 v119, v178, v178\n"
      "v_mul_f32 v210, v77, v210\n"
      "v_mfma_f32_16x16x32_bf16 v[216:219], v[220:223], a[152:155], v[216:219]\n"
      "v_mul_f32 v212, v77, v212\n"
      "v_mul_f32 v121, v77, v121\n"
      "v_mfma_f32_16x16x32_bf16 v[216:219], v[186:189], a[156:159], v[216:219]\n"
      "v_mul_f32 v164, v81, v164\n"
      "v_mul_f32 v165, v81, v165\n"
      "v_mfma_f32_16x16x32_bf16 v[216:219], v[198:201], a[160:163], v[216:219]\n"
      "v_fmac_f32 v119, v179, v179\n"
      "v_mul_f32 v182, v81, v182\n"
      "v_mfma_f32_16x16x32_bf16 v[216:219], v[190:193], a[164:167], v[216:219]\n"
      "ds_write_b64 v6, v[148:149] offset:30720\n"
      "v_mfma_f32_16x16x32_bf16 v[150:153], v[134:137], a[168:171], 0\n"
      "v_mul_f32 v122, v77, v122\n"
      "v_mul_f32 v183, v81, v183\n"
      "v_mfma_f32_16x16x32_bf16 v[150:153], v[100:103], a[172:175], v[150:153]\n"
      "v_cvt_pk_bf16_f32 v182, v182, v183\n"
      "v_fmac_f32_e64 v119, v180, v180\n"
      "v_mfma_f32_16x16x32_bf16 v[150:153], v[220:223], a[176:179], v[150:153]\n"
      "ds_read_b128 v[146:149], v0 offset:28672\n"
      "v_cvt_pk_bf16_f32 v132, v216, v217\n"
      "v_mfma_f32_16x16x32_bf16 v[150:153], v[186:189], a[180:183], v[150:153]\n"
      "v_fmac_f32 v119, v181, v181\n"
      "v_fmac_f32 v119, v126, v126\n"
      "v_mfma_f32_16x16x32_bf16 v[150:153], v[198:201], a[184:187], v[150:153]\n"
      "v_fmac_f32 v119, v127, v127\n"
      "v_mul_f32 v160, v77, v160\n"
      "v_mfma_f32_16x16x32_bf16 v[150:153], v[190:193], a[188:191], v[150:153]\n"
      "v_mul_f32 v211, v77, v211\n"
      "v_cvt_pk_bf16_f32 v162, v162, v163\n"
      "v_mfma_f32_16x16x32_bf16 v[174:177], v[134:137], a[192:195], 0\n"
      "v_fmac_f32 v119, v128, v128\n"
      "v_mul_f32_e64 v108, v81, v108\n"
      "v_mfma_f32_16x16x32_bf16 v[174:177], v[100:103], a[196:199], v[174:177]\n"
      "ds_read_b128 v[138:141], v0 offset:34816\n"
      "v_cvt_pk_bf16_f32 v163, v164, v165\n"
      "v_mfma_f32_16x16x32_bf16 v[174:177], v[220:223], a[200:203], v[174:177]\n"
      "ds_read_b128 v[220:223], v1 offset:34816\n"
      "v_fmac_f32 v119, v129, v129\n"
      "v_mfma_f32_16x16x32_bf16 v[174:177], v[186:189], a[204:207], v[174:177]\n"
      "s_nop 0\n"
      "v_permlane16_swap_b32 v82, v119\n"
      "v_mfma_f32_16x16x32_bf16 v[174:177], v[198:201], a[208:211], v[174:177]\n"
      "s_waitcnt lgkmcnt(2)\n"
      "v_add_f32 v82, v82, v119\n"
      "v_mov_b32 v119, v82\n"
      "v_mfma_f32_16x16x32_bf16 v[174:177], v[190:193], a[212:215], v[174:177]\n"
      "s_nop 0\n"
      "v_permlane32_swap_b32 v82, v119\n"
      "v_mfma_f32_16x16x32_bf16 v[198:201], a[0:3], v[154:157], 0\n"
      "v_cvt_pk_bf16_f32 v210, v210, v211\n"
      "v_add_f32_e64 v82, v82, v119\n"
      "v_mfma_f32_16x16x32_bf16 v[198:201], a[4:7], v[142:145], v[198:201]\n"
      "v_rsq_f32 v82, v82\n"
      "v_mfma_f32_16x16x32_bf16 v[198:201], a[8:11], v[146:149], v[198:201]\n"
      "s_waitcnt lgkmcnt(0)\n"
      "v_cvt_pk_bf16_f32 v165, v110, v111\n"
      "v_mov_b32 v119, v82\n"
      "v_mfma_f32_16x16x32_bf16 v[198:201], a[12:15], v[166:169], v[198:201]\n"
      "s_nop 0\n"
      "v_permlane16_swap_b32 v82, v119\n"
      "v_mfma_f32_16x16x32_bf16 v[198:201], a[16:19], v[138:141], v[198:201]\n"
      "v_mul_f32 v158, v77, v158\n"
      "v_cvt_pk_bf16_f32 v211, v212, v213\n"
      "v_mfma_f32_16x16x32_bf16 v[198:201], a[20:23], v[220:223], v[198:201]\n"
      "v_mul_f32 v181, v119, v181\n"
      "v_mul_f32 v82, s50, v82\n"
      "v_mfma_f32_16x16x32_bf16 v[100:103], a[24:27], v[154:157], 0\n"
      "ds_write_b64 v6, v[116:117] offset:18432\n"
      "v_mfma_f32_16x16x32_bf16 v[100:103], a[28:31], v[142:145], v[100:103]\n"
      "v_mul_f32 v127, v119, v127\n"
      "v_mul_f32 v184, v81, v184\n"
      "v_mfma_f32_16x16x32_bf16 v[100:103], a[32:35], v[146:149], v[100:103]\n"
      "v_mul_f32 v202, v119, v202\n"
      "v_fma_f32 v80, v198, v198, s51\n"
      "v_mfma_f32_16x16x32_bf16 v[100:103], a[36:39], v[166:169], v[100:103]\n"
      "v_mul_f32 v203, v119, v203\n"
      "v_mul_f32_e64 v161, v77, v161\n"
      "v_mfma_f32_16x16x32_bf16 v[100:103], a[40:43], v[138:141], v[100:103]\n"
      "v_fmac_f32 v80, v199, v199\n"
      "v_cvt_pk_bf16_f32 v120, v120, v121\n"
      "v_mfma_f32_16x16x32_bf16 v[100:103], a[44:47], v[220:223], v[100:103]\n"
      "v_mul_f32 v126, v119, v126\n"
      "v_mul_f32 v99, v82, v99\n"
      "v_mfma_f32_16x16x32_bf16 v[134:137], a[48:51], v[154:157], 0\n"
      "v_mul_f32 v179, v119, v179\n"
      "v_mul_f32 v113, v82, v113\n"
      "v_mfma_f32_16x16x32_bf16 v[134:137], a[52:55], v[142:145], v[134:137]\n"
      "v_mul_f32 v205, v119, v205\n"
      "v_mul_f32 v96, v82, v96\n"
      "v_mfma_f32_16x16x32_bf16 v[134:137], a[56:59], v[146:149], v[134:137]\n"
      "v_cvt_pk_bf16_f32 v217, v152, v153\n"
      "v_mul_f32 v107, v82, v107\n"
      "v_mfma_f32_16x16x32_bf16 v[134:137], a[60:63], v[166:169], v[134:137]\n"
      "v_mul_f32 v115, v82, v115\n"
      "v_fmac_f32 v80, v200, v200\n"
      "v_mfma_f32_16x16x32_bf16 v[134:137], a[64:67], v[138:141], v[134:137]\n"
      "v_cvt_pk_bf16_f32 v164, v108, v109\n"
      "v_mul_f32_e64 v159, v77, v159\n"
      "v_mfma_f32_16x16x32_bf16 v[134:137], a[68:71], v[220:223], v[134:137]\n"
      "v_fmac_f32 v80, v201, v201\n"
      "v_mul_f32 v97, v82, v97\n"
      "v_mfma_f32_16x16x32_bf16 v[190:193], a[72:75], v[154:157], 0\n"
      "v_fmac_f32_e64 v80, v100, v100\n"
      "v_cmp_ne_u32 s[56:57], 0, v124\n"
      "v_mfma_f32_16x16x32_bf16 v[190:193], a[76:79], v[142:145], v[190:193]\n"
      "v_mul_f32_e64 v105, v82, v105\n"
      "v_cvt_pk_bf16_f32 v196, v174, v175\n"
      "v_mfma_f32_16x16x32_bf16 v[190:193], a[80:83], v[146:149], v[190:193]\n"
      "v_fmac_f32 v80, v101, v101\n"
      "v_mul_f32 v128, v119, v128\n"
      "v_mfma_f32_16x16x32_bf16 v[190:193], a[84:87], v[166:169], v[190:193]\n"
      "v_mul_f32 v106, v82, v106\n"
      "v_fmac_f32 v80, v102, v102\n"
      "v_mfma_f32_16x16x32_bf16 v[190:193], a[88:91], v[138:141], v[190:193]\n"
      "v_fmac_f32 v80, v103, v103\n"
      "v_cvt_pk_bf16_f32 v183, v184, v185\n"
      "v_mfma_f32_16x16x32_bf16 v[190:193], a[92:95], v[220:223], v[190:193]\n"
      "v_mul_f32 v180, v119, v180\n"
      "v_cvt_pk_bf16_f32 v216, v150, v151\n"
      "v_mfma_f32_16x16x32_bf16 v[206:209], a[96:99], v[154:157], 0\n"
      "v_mul_f32 v123, v77, v123\n"
      "v_mul_f32 v112, v82, v112\n"
      "v_mfma_f32_16x16x32_bf16 v[206:209], a[100:103], v[142:145], v[206:209]\n"
      "v_mul_f32 v114, v82, v114\n"
      "v_mul_f32 v104, v82, v104\n"
      "v_mfma_f32_16x16x32_bf16 v[206:209], a[104:107], v[146:149], v[206:209]\n"
      "v_fmac_f32_e64 v80, v134, v134\n"
      "v_cvt_pk_bf16_f32 v96, v96, v97\n"
      "v_mfma_f32_16x16x32_bf16 v[206:209], a[108:111], v[166:169], v[206:209]\n"
      "v_mul_f32_e64 v204, v119, v204\n"
      "v_cvt_pk_bf16_f32 v197, v176, v177\n"
      "v_mfma_f32_16x16x32_bf16 v[206:209], a[112:115], v[138:141], v[206:209]\n"
      "v_cvt_pk_bf16_f32 v133, v218, v219\n"
      "v_cvt_pk_bf16_f32 v202, v202, v203\n"
      "v_mfma_f32_16x16x32_bf16 v[206:209], a[116:119], v[220:223], v[206:209]\n"
      "v_cvt_pk_bf16_f32 v104, v104, v105\n"
      "v_fma_f32 v76, v190, v190, s51\n"
      "v_mfma_f32_16x16x32_bf16 v[108:111], a[120:123], v[154:157], 0\n"
      "v_fmac_f32_e64 v76, v191, v191\n"
      "v_cvt_pk_bf16_f32 v203, v204, v205\n"
      "v_mfma_f32_16x16x32_bf16 v[108:111], a[124:127], v[142:145], v[108:111]\n"
      "v_cvt_pk_bf16_f32 v121, v122, v123\n"
      "v_cvt_pk_bf16_f32 v105, v106, v107\n"
      "v_mfma_f32_16x16x32_bf16 v[108:111], a[128:131], v[146:149], v[108:111]\n"
      "v_fmac_f32 v80, v135, v135\n"
      "v_fmac_f32 v80, v136, v136\n"
      "v_mfma_f32_16x16x32_bf16 v[108:111], a[132:135], v[166:169], v[108:111]\n"
      "v_fmac_f32 v76, v192, v192\n"
      "v_fmac_f32 v76, v193, v193\n"
      "v_mfma_f32_16x16x32_bf16 v[108:111], a[136:139], v[138:141], v[108:111]\n"
      "v_cvt_pk_bf16_f32 v107, v114, v115\n"
      "v_cvt_pk_bf16_f32 v205, v180, v181\n"
      "v_mfma_f32_16x16x32_bf16 v[108:111], a[140:143], v[220:223], v[108:111]\n"
      "v_fmac_f32 v76, v206, v206\n"
      "v_fmac_f32 v76, v207, v207\n"
      "v_mfma_f32_16x16x32_bf16 v[184:187], v[154:157], a[144:147], 0\n"
      "v_fmac_f32 v80, v137, v137\n"
      "v_fmac_f32 v76, v208, v208\n"
      "v_mfma_f32_16x16x32_bf16 v[184:187], v[142:145], a[148:151], v[184:187]\n"
      "v_cvt_pk_bf16_f32 v213, v160, v161\n"
      "v_fmac_f32 v76, v209, v209\n"
      "v_mfma_f32_16x16x32_bf16 v[184:187], v[146:149], a[152:155], v[184:187]\n"
      "v_fmac_f32 v76, v108, v108\n"
      "v_mul_f32_e64 v178, v119, v178\n"
      "v_mfma_f32_16x16x32_bf16 v[184:187], v[166:169], a[156:159], v[184:187]\n"
      "ds_read_b128 v[122:125], v2 offset:3072\n"
      "v_mul_f32 v98, v82, v98\n"
      "v_mfma_f32_16x16x32_bf16 v[184:187], v[138:141], a[160:163], v[184:187]\n"
      "v_fmac_f32 v76, v109, v109\n"
      "v_fmac_f32_e64 v76, v110, v110\n"
      "v_mfma_f32_16x16x32_bf16 v[184:187], v[220:223], a[164:167], v[184:187]\n"
      "v_fmac_f32 v76, v111, v111\n"
      "v_cvt_pk_bf16_f32 v204, v178, v179\n"
      "v_mfma_f32_16x16x32_bf16 v[150:153], v[154:157], a[168:171], 0\n"
      "v_permlane16_swap_b32 v80, v76\n"
      "v_mfma_f32_16x16x32_bf16 v[150:153], v[142:145], a[172:175], v[150:153]\n"
      "v_add_f32 v80, v80, v76\n"
      "v_mov_b32 v76, v80\n"
      "v_mfma_f32_16x16x32_bf16 v[150:153], v[146:149], a[176:179], v[150:153]\n"
      "s_nop 0\n"
      "v_permlane32_swap_b32 v80, v76\n"
      "v_mfma_f32_16x16x32_bf16 v[150:153], v[166:169], a[180:183], v[150:153]\n"
      "v_add_f32 v80, v80, v76\n"
      "v_cvt_pk_bf16_f32 v126, v126, v127\n"
      "v_mfma_f32_16x16x32_bf16 v[150:153], v[138:141], a[184:187], v[150:153]\n"
      "v_rsq_f32 v80, v80\n"
      "v_mfma_f32_16x16x32_bf16 v[150:153], v[220:223], a[188:191], v[150:153]\n"
      "ds_read_b128 v[178:181], v2\n"
      "v_mul_f32_e64 v129, v119, v129\n"
      "v_mfma_f32_16x16x32_bf16 v[174:177], v[154:157], a[192:195], 0\n"
      "ds_read_b128 v[154:157], v2 offset:6144\n"
      "v_mov_b32 v76, v80\n"
      "v_mfma_f32_16x16x32_bf16 v[174:177], v[142:145], a[196:199], v[174:177]\n"
      "s_nop 0\n"
      "v_permlane16_swap_b32_e64 v80, v76\n"
      "v_mfma_f32_16x16x32_bf16 v[174:177], v[146:149], a[200:203], v[174:177]\n"
      "v_mul_f32 v80, s50, v80\n"
      "v_mul_f32 v209, v76, v209\n"
      "v_mfma_f32_16x16x32_bf16 v[174:177], v[166:169], a[204:207], v[174:177]\n"
      "v_mul_f32 v109, v76, v109\n"
      "v_mul_f32 v190, v76, v190\n"
      "v_mfma_f32_16x16x32_bf16 v[174:177], v[138:141], a[208:211], v[174:177]\n"
      "v_mul_f32 v110, v76, v110\n"
      "v_mul_f32 v135, v80, v135\n"
      "v_mfma_f32_16x16x32_bf16 v[174:177], v[220:223], a[212:215], v[174:177]\n"
      "s_waitcnt lgkmcnt(1)\n"
      "v_mul_f32 v137, v80, v137\n"
      "v_mul_f32 v192, v76, v192\n"
      "v_mfma_f32_16x16x32_bf16 v[166:169], a[216:219], v[178:181], 0\n"
      "ds_read_b128 v[138:141], v2 offset:15360\n"
      "v_cvt_pk_bf16_f32 v77, v186, v187\n"
      "v_mfma_f32_16x16x32_bf16 v[166:169], a[220:223], v[122:125], v[166:169]\n"
      "s_waitcnt lgkmcnt(1)\n"
      "v_cvt_pk_bf16_f32 v106, v112, v113\n"
      "v_cvt_pk_bf16_f32 v97, v98, v99\n"
      "v_mfma_f32_16x16x32_bf16 v[166:169], a[224:227], v[154:157], v[166:169]\n"
      "ds_read_b128 v[186:189], v2 offset:9216\n"
      "v_mul_f32 v108, v76, v108\n"
      "v_mfma_f32_16x16x32_bf16 v[112:115], a[240:243], v[178:181], 0\n"
      "ds_read_b128 v[144:147], v2 offset:12288\n"
      "v_mul_f32 v206, v76, v206\n"
      "v_mfma_f32_16x16x32_bf16 v[112:115], a[244:247], v[122:125], v[112:115]\n"
      "v_mul_f32 v191, v76, v191\n"
      "v_mul_f32 v208, v76, v208\n"
      "v_mfma_f32_16x16x32_bf16 v[112:115], a[248:251], v[154:157], v[112:115]\n"
      "ds_read_b128 v[218:221], v10 offset:1200\n"
      "v_mul_f32_e64 v134, v80, v134\n"
      "v_mfma_f32_16x16x32_bf16 v[116:119], v[232:235], v[178:181], 0\n"
      "ds_read_u16 v82, v11 offset:1200\n"
      "v_cvt_pk_bf16_f32 v81, v152, v153\n"
      "v_mfma_f32_16x16x32_bf16 v[116:119], v[236:239], v[122:125], v[116:119]\n"
      "v_mul_f32 v102, v80, v102\n"
      "v_mul_f32 v193, v76, v193\n"
      "v_mfma_f32_16x16x32_bf16 v[116:119], v[240:243], v[154:157], v[116:119]\n"
      "s_waitcnt lgkmcnt(3)\n"
      "v_mul_f32 v199, v80, v199\n"
      "v_mul_f32 v103, v80, v103\n"
      "v_mfma_f32_16x16x32_bf16 v[166:169], a[228:231], v[186:189], v[166:169]\n"
      "s_waitcnt lgkmcnt(2)\n"
      "v_cvt_pk_bf16_f32 v190, v190, v191\n"
      "v_cvt_pk_bf16_f32 v108, v108, v109\n"
      "v_mfma_f32_16x16x32_bf16 v[166:169], a[232:235], v[144:147], v[166:169]\n"
      "v_cvt_pk_bf16_f32 v212, v158, v159\n"
      "v_mul_f32 v111, v76, v111\n"
      "v_mfma_f32_16x16x32_bf16 v[166:169], a[236:239], v[138:141], v[166:169]\n"
      "v_mul_f32 v207, v76, v207\n"
      "v_mul_f32_e64 v200, v80, v200\n"
      "v_mfma_f32_16x16x32_bf16 v[112:115], a[252:255], v[186:189], v[112:115]\n"
      "ds_read_b128 v[122:125], v2 offset:256\n"
      "v_cvt_pk_bf16_f32 v109, v110, v111\n"
      "v_mfma_f32_16x16x32_bf16 v[112:115], v[224:227], v[144:147], v[112:115]\n"
      "ds_read_b128 v[178:181], v2 offset:6400\n"
      "v_cvt_pk_bf16_f32 v127, v128, v129\n"
      "v_mfma_f32_16x16x32_bf16 v[112:115], v[228:231], v[138:141], v[112:115]\n"
      "ds_read_b128 v[154:157], v2 offset:3328\n"
      "v_cvt_pk_bf16_f32 v191, v192, v193\n"
      "v_mfma_f32_16x16x32_bf16 v[116:119], v[244:247], v[186:189], v[116:119]\n"
      "ds_read_b64 v[110:111], v10 offset:1192\n"
      "v_cvt_pk_bf16_f32 v76, v184, v185\n"
      "v_mfma_f32_16x16x32_bf16 v[116:119], v[248:251], v[144:147], v[116:119]\n"
      "v_cvt_pk_bf16_f32 v193, v208, v209\n"
      "v_mul_f32_e64 v198, v80, v198\n"
      "v_mfma_f32_16x16x32_bf16 v[116:119], v[252:255], v[138:141], v[116:119]\n"
      "v_mul_f32 v136, v80, v136\n"
      "v_cvt_pk_bf16_f32 v192, v206, v207\n"
      "v_mfma_f32_16x16x32_bf16 v[146:149], v[162:165], v[210:213], v[40:43]\n"
      "v_mul_f32 v201, v80, v201\n"
      "v_mul_f32 v101, v80, v101\n"
      "v_mfma_f32_16x16x32_bf16 v[138:141], v[202:205], v[210:213], v[44:47]\n"
      "v_mul_f32 v129, v167, v167\n"
      "v_cvt_pk_bf16_f32 v198, v198, v199\n"
      "v_mfma_f32_16x16x32_bf16 v[142:145], v[190:193], v[210:213], v[48:51]\n"
      "s_waitcnt lgkmcnt(1)\n"
      "v_fma_f32 v128, v166, v166, s52\n"
      "v_cvt_pk_bf16_f32 v134, v134, v135\n"
      "v_mfma_f32_16x16x32_bf16 v[158:161], v[218:221], v[210:213], v[24:27]\n"
      "s_waitcnt lgkmcnt(0)\n"
      "v_cvt_pk_bf16_f32 v84, v174, v175\n"
      "v_fmac_f32 v128, v168, v168\n"
      "v_mfma_f32_16x16x16_bf16 v[146:149], v[182:183], v[120:121], v[146:149]\n"
      "s_waitcnt vmcnt(1)\n"
      "v_or3_b32 v170, v170, v171, v172\n"
      "v_cvt_pk_bf16_f32 v85, v176, v177\n"
      "v_cvt_pk_bf16_f32 v199, v200, v201\n"
      "v_mfma_f32_16x16x16_bf16 v[138:141], v[126:127], v[120:121], v[138:141]\n"
      "v_fmac_f32_e64 v129, v169, v169\n"
      "v_cvt_pk_bf16_f32 v201, v102, v103\n"
      "v_mul_f32 v100, v80, v100\n"
      "v_mfma_f32_16x16x16_bf16 v[142:145], v[108:109], v[120:121], v[142:145]\n"
      "v_cvt_pk_bf16_f32 v80, v150, v151\n"
      "v_exp_f32 v147, v147\n"
      "v_mfma_f32_16x16x16_bf16 v[158:161], v[110:111], v[120:121], v[158:161]\n"
      "s_waitcnt vmcnt(0)\n"
      "v_or3_b32 v170, v170, v173, v86\n"
      "v_exp_f32 v146, v146\n"
      "v_mfma_f32_16x16x32_bf16 v[186:189], a[216:219], v[122:125], 0\n"
      "v_exp_f32 v148, v148\n"
      "v_mfma_f32_16x16x32_bf16 v[186:189], a[220:223], v[154:157], v[186:189]\n"
      "v_exp_f32 v149, v149\n"
      "v_mfma_f32_16x16x32_bf16 v[186:189], a[224:227], v[178:181], v[186:189]\n"
      "v_exp_f32_e64 v139, v139\n"
      "v_mfma_f32_16x16x32_bf16 v[174:177], a[240:243], v[122:125], 0\n"
      "v_exp_f32 v142, v142\n"
      "v_mfma_f32_16x16x32_bf16 v[174:177], a[244:247], v[154:157], v[174:177]\n"
      "v_exp_f32 v138, v138\n"
      "v_mfma_f32_16x16x32_bf16 v[174:177], a[248:251], v[178:181], v[174:177]\n"
      "v_exp_f32_e64 v143, v143\n"
      "v_mfma_f32_16x16x32_bf16 v[206:209], v[232:235], v[122:125], 0\n"
      "ds_write_b32 v31, v170 offset:5632\n"
      "v_mfma_f32_16x16x32_bf16 v[206:209], v[236:239], v[154:157], v[206:209]\n"
      "ds_read_u16 v86, v11 offset:1232\n"
      "v_cvt_pk_bf16_f32 v146, v146, v147\n"
      "v_mfma_f32_16x16x32_bf16 v[206:209], v[240:243], v[178:181], v[206:209]\n"
      "v_exp_f32_e64 v158, v158\n"
      "v_mfma_f32_16x16x32_bf16 v[210:213], v[162:165], v[104:107], v[52:55]\n"
      "v_cvt_pk_bf16_f32 v147, v148, v149\n"
      "v_cvt_pk_bf16_f32 v88, v142, v143\n"
      "v_mfma_f32_16x16x32_bf16 v[154:157], v[202:205], v[104:107], v[56:59]\n"
      "v_exp_f32 v141, v141\n"
      "v_mfma_f32_16x16x32_bf16 v[122:125], v[190:193], v[104:107], v[60:63]\n"
      "v_exp_f32 v144, v144\n"
      "v_mfma_f32_16x16x32_bf16 v[150:153], v[218:221], v[104:107], v[26:29]\n"
      "s_add_u32 s42, s42, 1\n"
      "v_exp_f32 v140, v140\n"
      "v_mfma_f32_16x16x16_bf16 v[210:213], v[182:183], v[96:97], v[210:213]\n"
      "v_cvt_pk_bf16_f32 v90, v158, 0\n"
      "v_exp_f32_e64 v145, v145\n"
      "v_mfma_f32_16x16x16_bf16 v[154:157], v[126:127], v[96:97], v[154:157]\n"
      "v_cvt_pk_bf16_f32 v89, v144, v145\n"
      "v_cvt_pk_bf16_f32 v148, v138, v139\n"
      "v_cvt_pk_bf16_f32 v149, v140, v141\n"
      "v_mfma_f32_16x16x16_bf16 v[122:125], v[108:109], v[96:97], v[122:125]\n"
      "v_cvt_pk_bf16_f32 v200, v100, v101\n"
      "v_exp_f32 v210, v210\n"
      "v_mfma_f32_16x16x16_bf16 v[150:153], v[110:111], v[96:97], v[150:153]\n"
      "v_exp_f32 v211, v211\n"
      "v_cvt_pk_bf16_f32 v135, v136, v137\n"
      "v_mfma_f32_16x16x32_bf16 v[140:143], v[32:35], v[146:149], 0\n"
      "s_and_b32 s47, s47, -4\n"
      "v_exp_f32 v213, v213\n"
      "v_mfma_f32_16x16x32_bf16 v[140:143], v[32:35], v[88:91], v[140:143]\n"
      "v_exp_f32_e64 v154, v154\n"
      "v_mfma_f32_16x16x32_bf16 v[136:139], v[130:133], v[146:149], 0\n"
      "v_exp_f32_e64 v156, v156\n"
      "v_mfma_f32_16x16x32_bf16 v[136:139], v[76:79], v[88:91], v[136:139]\n"
      "ds_read_b128 v[96:99], v2 offset:9472\n"
      "v_cvt_pk_bf16_f32 v210, v210, v211\n"
      "v_mfma_f32_16x16x32_bf16 v[170:173], v[214:217], v[146:149], 0\n"
      "s_nop 0\n"
      "v_rcp_f32 v140, v140\n"
      "v_mfma_f32_16x16x32_bf16 v[170:173], v[80:83], v[88:91], v[170:173]\n"
      "v_exp_f32 v212, v212\n"
      "v_mfma_f32_16x16x32_bf16 v[104:107], v[194:197], v[146:149], 0\n"
      "s_waitcnt lgkmcnt(1)\n"
      "v_exp_f32_e64 v155, v155\n"
      "v_mfma_f32_16x16x32_bf16 v[104:107], v[84:87], v[88:91], v[104:107]\n"
      "ds_read_b128 v[142:145], v2 offset:15616\n"
      "v_cvt_pk_bf16_f32 v211, v212, v213\n"
      "v_mfma_f32_16x16x32_bf16 v[178:181], v[162:165], v[198:201], v[64:67]\n"
      "v_exp_f32_e64 v157, v157\n"
      "v_mfma_f32_16x16x32_bf16 v[158:161], v[202:205], v[198:201], v[68:71]\n"
      "v_exp_f32 v123, v123\n"
      "v_mfma_f32_16x16x32_bf16 v[100:103], v[190:193], v[198:201], v[72:75]\n"
      "s_waitcnt lgkmcnt(1)\n"
      "v_cvt_pk_bf16_f32 v213, v156, v157\n"
      "v_cvt_pk_bf16_f32 v212, v154, v155\n"
      "v_mfma_f32_16x16x32_bf16 v[154:157], v[218:221], v[198:201], v[28:31]\n"
      "ds_read_b128 v[198:201], v2 offset:12544\n"
      "v_mul_f32 v173, v140, v173\n"
      "v_mul_f32 v172, v140, v172\n"
      "v_mul_f32 v170, v140, v170\n"
      "v_mul_f32 v171, v140, v171\n"
      "v_pk_fma_f32 v[128:129], v[112:113], v[112:113], v[128:129]\n"
      "v_pk_mul_f32 v[136:137], v[136:137], v[140:141] op_sel_hi:[1,0]\n"
      "v_pk_mul_f32 v[138:139], v[138:139], v[140:141] op_sel_hi:[1,0]\n"
      "v_mfma_f32_16x16x16_bf16 v[178:181], v[182:183], v[134:135], v[178:181]\n"
      "v_exp_f32 v122, v122\n"
      "v_cvt_pk_bf16_f32 v136, v136, v137\n"
      "v_mfma_f32_16x16x16_bf16 v[158:161], v[126:127], v[134:135], v[158:161]\n"
      "v_exp_f32 v150, v150\n"
      "v_cvt_pk_bf16_f32 v137, v138, v139\n"
      "v_mfma_f32_16x16x16_bf16 v[100:103], v[108:109], v[134:135], v[100:103]\n"
      "v_exp_f32_e64 v124, v124\n"
      "v_cvt_pk_bf16_f32 v138, v170, v171\n"
      "v_mfma_f32_16x16x16_bf16 v[154:157], v[110:111], v[134:135], v[154:157]\n"
      "v_exp_f32 v180, v180\n"
      "v_cvt_pk_bf16_f32 v94, v150, 0\n"
      "v_mfma_f32_16x16x32_bf16 v[186:189], a[228:231], v[96:99], v[186:189]\n"
      "s_waitcnt lgkmcnt(0)\n"
      "v_exp_f32 v125, v125\n"
      "v_mfma_f32_16x16x32_bf16 v[186:189], a[232:235], v[198:201], v[186:189]\n"
      "v_cvt_pk_bf16_f32 v93, v124, v125\n"
      "v_fmac_f32 v128, v114, v114\n"
      "v_mfma_f32_16x16x32_bf16 v[186:189], a[236:239], v[142:145], v[186:189]\n"
      "v_exp_f32 v102, v102\n"
      "v_mfma_f32_16x16x32_bf16 v[174:177], a[252:255], v[96:99], v[174:177]\n"
      "v_exp_f32 v178, v178\n"
      "v_mfma_f32_16x16x32_bf16 v[174:177], v[224:227], v[198:201], v[174:177]\n"
      "ds_read_b128 v[150:153], v7\n"
      "v_cvt_pk_bf16_f32 v139, v172, v173\n"
      "v_mfma_f32_16x16x32_bf16 v[174:177], v[228:231], v[142:145], v[174:177]\n"
      "ds_read_b128 v[146:149], v10 offset:1168\n"
      "v_cvt_pk_bf16_f32 v92, v122, v123\n"
      "v_mfma_f32_16x16x32_bf16 v[206:209], v[244:247], v[96:99], v[206:209]\n"
      "v_exp_f32 v154, v154\n"
      "v_mfma_f32_16x16x32_bf16 v[206:209], v[248:251], v[198:201], v[206:209]\n"
      "v_exp_f32 v179, v179\n"
      "v_mfma_f32_16x16x32_bf16 v[206:209], v[252:255], v[142:145], v[206:209]\n"
      "v_exp_f32 v101, v101\n"
      "v_mfma_f32_16x16x32_bf16 v[198:201], v[32:35], v[210:213], 0\n"
      "v_exp_f32 v100, v100\n"
      "v_mfma_f32_16x16x32_bf16 v[198:201], v[32:35], v[92:95], v[198:201]\n"
      "v_exp_f32_e64 v161, v161\n"
      "v_mfma_f32_16x16x32_bf16 v[120:123], v[130:133], v[210:213], 0\n"
      "ds_read_b128 v[96:99], v7 offset:4096\n"
      "v_fmac_f32_e64 v129, v115, v115\n"
      "v_mfma_f32_16x16x32_bf16 v[120:123], v[76:79], v[92:95], v[120:123]\n"
      "ds_write_b128 v3, v[136:139] offset:18432\n"
      "v_mfma_f32_16x16x32_bf16 v[170:173], v[214:217], v[210:213], 0\n"
      "ds_read_b64 v[184:185], v10 offset:1184\n"
      "v_cvt_pk_bf16_f32 v90, v154, 0\n"
      "v_mfma_f32_16x16x32_bf16 v[170:173], v[80:83], v[92:95], v[170:173]\n"
      "s_waitcnt lgkmcnt(3)\n"
      "v_exp_f32 v158, v158\n"
      "v_mfma_f32_16x16x32_bf16 v[154:157], v[194:197], v[210:213], 0\n"
      "buffer_store_dwordx4 v[150:153], v20, s[24:27], s85 offen sc1\n"
      "v_mfma_f32_16x16x32_bf16 v[154:157], v[84:87], v[92:95], v[154:157]\n"
      "ds_read_b128 v[210:213], v7 offset:8192\n"
      "v_cvt_pk_bf16_f32 v88, v100, v101\n"
      "v_mfma_f32_16x16x32_bf16 v[142:145], v[146:149], v[162:165], v[30:33]\n"
      "ds_read_b128 v[162:165], v2 offset:3584\n"
      "v_cvt_pk_bf16_f32 v178, v178, v179\n"
      "v_mfma_f32_16x16x32_bf16 v[150:153], v[146:149], v[202:205], v[30:33]\n"
      "v_exp_f32_e64 v103, v103\n"
      "v_mfma_f32_16x16x32_bf16 v[200:203], v[146:149], v[190:193], v[30:33]\n"
      "s_waitcnt lgkmcnt(2)\n"
      "v_exp_f32 v159, v159\n"
      "v_mfma_f32_16x16x32_bf16 v[136:139], v[146:149], v[218:221], v[30:33]\n"
      "ds_read_b128 v[218:221], v2 offset:6656\n"
      "v_cvt_pk_bf16_f32 v89, v102, v103\n"
      "v_mfma_f32_16x16x16_bf16 v[142:145], v[184:185], v[182:183], v[142:145]\n"
      "ds_read_b128 v[100:103], v2 offset:512\n"
      "v_fma_f32 v124, v186, v186, s52\n"
      "v_mfma_f32_16x16x16_bf16 v[150:153], v[184:185], v[126:127], v[150:153]\n"
      "v_rcp_f32_e64 v198, v198\n"
      "v_mul_f32_e64 v125, v187, v187\n"
      "v_mfma_f32_16x16x16_bf16 v[200:203], v[184:185], v[108:109], v[200:203]\n"
      "buffer_store_dwordx4 v[96:99], v21, s[24:27], s85 offen sc1\n"
      "v_pk_mul_f32 v[170:171], v[170:171], v[198:199] op_sel_hi:[1,0]\n"
      "v_pk_mul_f32 v[154:155], v[154:155], v[198:199] op_sel_hi:[1,0]\n"
      "v_pk_fma_f32 v[128:129], v[116:117], v[116:117], v[128:129]\n"
      "v_pk_mul_f32 v[120:121], v[120:121], v[198:199] op_sel_hi:[1,0]\n"
      "v_pk_mul_f32 v[172:173], v[172:173], v[198:199] op_sel_hi:[1,0]\n"
      "v_pk_fma_f32 v[124:125], v[188:189], v[188:189], v[124:125]\n"
      "v_pk_fma_f32 v[124:125], v[174:175], v[174:175], v[124:125]\n"
      "v_pk_mul_f32 v[104:105], v[104:105], v[140:141] op_sel_hi:[1,0]\n"
      "v_mfma_f32_16x16x16_bf16 v[136:139], v[184:185], v[110:111], v[136:139]\n"
      "v_exp_f32 v181, v181\n"
      "s_waitcnt lgkmcnt(0)\n"
      "v_mfma_f32_16x16x32_bf16 v[202:205], a[216:219], v[100:103], 0\n"
      "v_cvt_pk_bf16_f32 v179, v180, v181\n"
      "v_cndmask_b32 v142, v142, v150, s[58:59]\n"
      "v_mfma_f32_16x16x32_bf16 v[202:205], a[220:223], v[162:165], v[202:205]\n"
      "v_cvt_pk_bf16_f32 v180, v158, v159\n"
      "v_exp_f32_e64 v160, v160\n"
      "v_mfma_f32_16x16x32_bf16 v[202:205], a[224:227], v[218:221], v[202:205]\n"
      "ds_read_b128 v[182:185], v2 offset:15872\n"
      "v_cvt_pk_bf16_f32 v181, v160, v161\n"
      "v_mfma_f32_16x16x32_bf16 v[144:147], a[240:243], v[100:103], 0\n"
      "ds_read_b128 v[190:193], v7 offset:12288\n"
      "v_cvt_pk_bf16_f32 v154, v154, v155\n"
      "v_mfma_f32_16x16x32_bf16 v[144:147], a[244:247], v[162:165], v[144:147]\n"
      "buffer_store_dwordx4 v[210:213], v22, s[24:27], s85 offen sc1\n"
      "v_pk_fma_f32 v[124:125], v[176:177], v[176:177], v[124:125]\n"
      "v_pk_mul_f32 v[122:123], v[122:123], v[198:199] op_sel_hi:[1,0]\n"
      "v_pk_mul_f32 v[106:107], v[106:107], v[140:141] op_sel_hi:[1,0]\n"
      "v_pk_fma_f32 v[124:125], v[206:207], v[206:207], v[124:125]\n"
      "v_pk_mul_f32 v[156:157], v[156:157], v[198:199] op_sel_hi:[1,0]\n"
      "v_mfma_f32_16x16x32_bf16 v[144:147], a[248:251], v[218:221], v[144:147]\n"
      "v_cvt_pk_bf16_f32 v120, v120, v121\n"
      "v_cvt_pk_bf16_f32 v104, v104, v105\n"
      "v_mfma_f32_16x16x32_bf16 v[148:151], v[232:235], v[100:103], 0\n"
      "v_cndmask_b32 v142, v142, v200, s[60:61]\n"
      "v_cvt_pk_bf16_f32 v155, v156, v157\n"
      "v_mfma_f32_16x16x32_bf16 v[148:151], v[236:239], v[162:165], v[148:151]\n"
      "ds_read_b128 v[198:201], v2 offset:9728\n"
      "v_cvt_pk_bf16_f32 v121, v122, v123\n"
      "v_mfma_f32_16x16x32_bf16 v[148:151], v[240:243], v[218:221], v[148:151]\n"
      "ds_read_b128 v[218:221], v2 offset:12800\n"
      "v_cvt_pk_bf16_f32 v123, v172, v173\n"
      "v_mfma_f32_16x16x32_bf16 v[96:99], v[32:35], v[178:181], 0\n"
      "ds_read_b128 v[138:141], v7 offset:16384\n"
      "v_cndmask_b32 v142, v142, v136, s[62:63]\n"
      "v_mfma_f32_16x16x32_bf16 v[96:99], v[32:35], v[88:91], v[96:99]\n"
      "ds_write_b64 v4, v[154:155] offset:18688\n"
      "v_mfma_f32_16x16x32_bf16 v[134:137], v[130:133], v[178:181], 0\n"
      "v_exp_f32 v142, v142\n"
      "v_mfma_f32_16x16x32_bf16 v[134:137], v[76:79], v[88:91], v[134:137]\n"
      "s_waitcnt lgkmcnt(4)\n"
      "v_cvt_pk_bf16_f32 v105, v106, v107\n"
      "v_cvt_pk_bf16_f32 v142, v142, v142\n"
      "v_mfma_f32_16x16x32_bf16 v[106:109], v[214:217], v[178:181], 0\n"
      "ds_write_b16 v13, v142\n"
      "v_mfma_f32_16x16x32_bf16 v[106:109], v[80:83], v[88:91], v[106:109]\n"
      "ds_read_b128 v[156:159], v14\n"
      "v_cvt_pk_bf16_f32 v122, v170, v171\n"
      "v_mfma_f32_16x16x32_bf16 v[100:103], v[194:197], v[178:181], 0\n"
      "buffer_store_dwordx4 v[190:193], v23, s[24:27], s85 offen sc1\n"
      "v_mfma_f32_16x16x32_bf16 v[100:103], v[84:87], v[88:91], v[100:103]\n"
      "s_waitcnt lgkmcnt(2)\n"
      "v_rcp_f32 v96, v96\n"
      "v_mfma_f32_16x16x32_bf16 v[202:205], a[228:231], v[198:201], v[202:205]\n"
      "ds_write_b128 v3, v[120:123] offset:18688\n"
      "v_pk_mul_f32 v[106:107], v[106:107], v[96:97] op_sel_hi:[1,0]\n"
      "v_pk_mul_f32 v[136:137], v[136:137], v[96:97] op_sel_hi:[1,0]\n"
      "v_pk_fma_f32 v[128:129], v[118:119], v[118:119], v[128:129]\n"
      "v_pk_mul_f32 v[108:109], v[108:109], v[96:97] op_sel_hi:[1,0]\n"
      "v_pk_fma_f32 v[124:125], v[208:209], v[208:209], v[124:125]\n"
      "v_pk_mul_f32 v[134:135], v[134:135], v[96:97] op_sel_hi:[1,0]\n"
      "v_mfma_f32_16x16x32_bf16 v[202:205], a[232:235], v[218:221], v[202:205]\n"
      "ds_read_b128 v[210:213], v14 offset:64\n"
      "v_cvt_pk_bf16_f32 v134, v134, v135\n"
      "v_mfma_f32_16x16x32_bf16 v[202:205], a[236:239], v[182:185], v[202:205]\n"
      "v_cvt_pk_bf16_f32 v135, v136, v137\n"
      "v_add_f32_e64 v124, v124, v125\n"
      "v_mfma_f32_16x16x32_bf16 v[144:147], a[252:255], v[198:201], v[144:147]\n"
      "ds_write_b64 v4, v[104:105] offset:18432\n"
      "v_mfma_f32_16x16x32_bf16 v[144:147], v[224:227], v[218:221], v[144:147]\n"
      "v_cvt_pk_bf16_f32 v136, v106, v107\n"
      "v_add_f32_e64 v128, v128, v129\n"
      "v_mfma_f32_16x16x32_bf16 v[144:147], v[228:231], v[182:185], v[144:147]\n"
      "s_nop 0\n"
      "v_permlane16_swap_b32 v128, v124\n"
      "v_mfma_f32_16x16x32_bf16 v[148:151], v[244:247], v[198:201], v[148:151]\n"
      "v_fma_f32 v142, v202, v202, s52\n"
      "v_cvt_pk_bf16_f32 v137, v108, v109\n"
      "v_mfma_f32_16x16x32_bf16 v[148:151], v[248:251], v[218:221], v[148:151]\n"
      "s_waitcnt lgkmcnt(1)\n"
      "v_add_f32 v128, v128, v124\n"
      "v_mul_f32_e64 v143, v203, v203\n"
      "v_mfma_f32_16x16x32_bf16 v[148:151], v[252:255], v[182:185], v[148:151]\n"
      "ds_write_b128 v3, v[134:137] offset:18944\n"
      "v_pk_mul_f32 v[102:103], v[102:103], v[96:97] op_sel_hi:[1,0]\n"
      "v_pk_fma_f32 v[142:143], v[204:205], v[204:205], v[142:143]\n"
      "v_pk_mul_f32 v[100:101], v[100:101], v[96:97] op_sel_hi:[1,0]\n"
      "v_pk_fma_f32 v[142:143], v[144:145], v[144:145], v[142:143]\n"
      "v_cvt_pk_bf16_f32 v100, v100, v101\n"
      "v_pk_fma_f32 v[142:143], v[146:147], v[146:147], v[142:143]\n"
      "v_cvt_pk_bf16_f32 v101, v102, v103\n"
      "v_pk_fma_f32 v[142:143], v[148:149], v[148:149], v[142:143]\n"
      "v_pk_fma_f32 v[142:143], v[150:151], v[150:151], v[142:143]\n"
      "v_add_f32 v142, v142, v143\n"
      "v_mov_b32 v124, v142\n"
      "v_mfma_f32_16x16x32_bf16 v[152:155], v[156:159], v[32:35], 0\n"
      "ds_write_b64 v4, v[100:101] offset:18944\n"
      "v_permlane16_swap_b32 v142, v124\n"
      "v_add_f32 v142, v142, v124\n"
      "s_nop 1\n"
      "v_permlane32_swap_b32 v128, v142\n"
      "v_add_f32_e64 v128, v128, v142\n"
      "s_add_u32 s43, s43, 1\n"
      "ds_write_b32 v31, v128 offset:1024\n"
      "s_cmp_ge_u32 s95, 0x40000000\n"
      "s_cselect_b64 vcc, -1, s[56:57]\n"
      "v_add_u32 v10, 2336, v10\n"
      "buffer_store_dwordx4 v[138:141], v25, s[24:27], s85 offen sc1\n"
      "v_mfma_f32_16x16x32_bf16 v[152:155], v[210:213], v[32:35], v[152:155]\n"
      "v_add_u32 v11, 2336, v11\n"
      "s_waitcnt lgkmcnt(0)\n"
      "s_barrier\n"
      "s_cbranch_vccnz L_disp0_%=\n"
      "L_loop0_%=:\n"
      "v_mfma_f32_16x16x32_bf16 v[218:221], v[156:159], v[130:133], 0\n"
      "ds_read_b128 v[182:185], v0\n"
      "s_add_u32 m0, s64, 18432\n"
      "v_mfma_f32_16x16x32_bf16 v[218:221], v[210:213], v[76:79], v[218:221]\n"
      "ds_read_b128 v[140:143], v9 offset:1024\n"
      "ds_read_b128 v[108:111], v0 offset:12288\n"
      "v_mfma_f32_16x16x32_bf16 v[134:137], v[156:159], v[214:217], 0\n"
      "buffer_load_dwordx4 v16, s[16:19], s45 offen  lds\n"
      "v_mfma_f32_16x16x32_bf16 v[134:137], v[210:213], v[80:83], v[134:137]\n"
      "ds_read_b128 v[100:103], v0 offset:2048\n"
      "v_readlane_b32 s85, v37, s42\n"
      "v_mfma_f32_16x16x32_bf16 v[128:131], v[156:159], v[194:197], 0\n"
      "s_add_u32 m0, s64, 22528\n"
      "ds_read_b128 v[156:159], v0 offset:6144\n"
      "v_mfma_f32_16x16x32_bf16 v[128:131], v[210:213], v[84:87], v[128:131]\n"
      "ds_read_b128 v[210:213], v1\n"
      "s_waitcnt lgkmcnt(5)\n"
      "v_mfma_f32_16x16x32_bf16 v[178:181], a[0:3], v[182:185], 0\n"
      "ds_read_b128 v[124:127], v1 offset:6144\n"
      "v_readlane_b32 s84, v37, s43\n"
      "v_mfma_f32_16x16x32_bf16 v[170:173], a[24:27], v[182:185], 0\n"
      "s_waitcnt lgkmcnt(5)\n"
      "v_add_f32 v140, v140, v141\n"
      "v_add_f32 v142, v142, v143\n"
      "v_mfma_f32_16x16x32_bf16 v[136:139], a[48:51], v[182:185], 0\n"
      "buffer_load_dwordx4 v17, s[16:19], s45 offen  lds\n"
      "v_mfma_f32_16x16x32_bf16 v[96:99], a[72:75], v[182:185], 0\n"
      "v_readlane_b32 s86, v38, s43\n"
      "v_add_f32_e64 v140, v140, v142\n"
      "v_mfma_f32_16x16x32_bf16 v[104:107], a[96:99], v[182:185], 0\n"
      "s_add_u32 m0, s64, 26624\n"
      "v_rsq_f32 v140, v140\n"
      "v_mfma_f32_16x16x32_bf16 v[120:123], a[120:123], v[182:185], 0\n"
      "s_waitcnt lgkmcnt(1)\n"
      "v_mul_f32 v140, 0x415db3d7, v140\n"
      "v_mov_b32 v142, v140\n"
      "v_mfma_f32_16x16x32_bf16 v[178:181], a[4:7], v[210:213], v[178:181]\n"
      "s_nop 0\n"
      "v_permlane32_swap_b32_e64 v140, v142\n"
      "v_mfma_f32_16x16x32_bf16 v[178:181], a[8:11], v[156:159], v[178:181]\n"
      "ds_read_b128 v[160:163], v1 offset:12288\n"
      "v_mov_b32_e64 v141, v140\n"
      "v_mfma_f32_16x16x32_bf16 v[170:173], a[28:31], v[210:213], v[170:173]\n"
      "buffer_load_dwordx4 v18, s[16:19], s45 offen  lds\n"
      "v_mfma_f32_16x16x32_bf16 v[170:173], a[32:35], v[156:159], v[170:173]\n"
      "v_permlane16_swap_b32_e64 v140, v141\n"
      "v_mfma_f32_16x16x32_bf16 v[136:139], a[52:55], v[210:213], v[136:139]\n"
      "s_add_u32 m0, s64, 30720\n"
      "v_mul_f32 v166, v140, v166\n"
      "v_mul_f32 v116, v140, v116\n"
      "v_mfma_f32_16x16x32_bf16 v[136:139], a[56:59], v[156:159], v[136:139]\n"
      "v_mul_f32 v146, v142, v146\n"
      "v_mul_f32 v167, v140, v167\n"
      "v_mfma_f32_16x16x32_bf16 v[96:99], a[76:79], v[210:213], v[96:99]\n"
      "v_mul_f32 v113, v140, v113\n"
      "v_mul_f32 v114, v140, v114\n"
      "v_mfma_f32_16x16x32_bf16 v[96:99], a[80:83], v[156:159], v[96:99]\n"
      "v_cvt_pk_bf16_f32 v166, v166, v167\n"
      "v_mul_f32 v186, v141, v186\n"
      "v_mfma_f32_16x16x32_bf16 v[104:107], a[100:103], v[210:213], v[104:107]\n"
      "buffer_load_dwordx4 v19, s[16:19], s45 offen  lds\n"
      "v_mfma_f32_16x16x32_bf16 v[104:107], a[104:107], v[156:159], v[104:107]\n"
      "v_mul_f32 v175, v141, v175\n"
      "v_mul_f32 v204, v142, v204\n"
      "v_mfma_f32_16x16x32_bf16 v[120:123], a[124:127], v[210:213], v[120:123]\n"
      "v_mul_f32 v206, v141, v206\n"
      "v_mul_f32 v112, v140, v112\n"
      "v_mfma_f32_16x16x32_bf16 v[120:123], a[128:131], v[156:159], v[120:123]\n"
      "s_waitcnt lgkmcnt(0)\n"
      "v_mul_f32 v168, v140, v168\n"
      "v_mul_f32 v174, v141, v174\n"
      "v_mfma_f32_16x16x32_bf16 v[178:181], a[12:15], v[124:127], v[178:181]\n"
      "s_add_u32 m0, s65, 34816\n"
      "v_mul_f32 v169, v140, v169\n"
      "v_mul_f32 v187, v141, v187\n"
      "v_mfma_f32_16x16x32_bf16 v[178:181], a[16:19], v[108:111], v[178:181]\n"
      "buffer_load_dwordx4 v15, s[16:19], s45 offen  lds\n"
      "v_mfma_f32_16x16x32_bf16 v[178:181], a[20:23], v[160:163], v[178:181]\n"
      "v_cvt_pk_bf16_f32 v167, v168, v169\n"
      "v_mul_f32 v148, v142, v148\n"
      "v_mfma_f32_16x16x32_bf16 v[170:173], a[36:39], v[124:127], v[170:173]\n"
      "v_mul_f32 v202, v142, v202\n"
      "v_mov_b32_dpp v152, v152 quad_perm:[0,0,0,0] row_mask:0xf bank_mask:0xf\n"
      "v_mfma_f32_16x16x32_bf16 v[170:173], a[40:43], v[108:111], v[170:173]\n"
      "v_mul_f32 v208, v141, v208\n"
      "v_cvt_pk_bf16_f32 v168, v112, v113\n"
      "v_mfma_f32_16x16x32_bf16 v[170:173], a[44:47], v[160:163], v[170:173]\n"
      "v_mul_f32 v115, v140, v115\n"
      "v_mul_f32_e64 v189, v141, v189\n"
      "v_mfma_f32_16x16x32_bf16 v[136:139], a[60:63], v[124:127], v[136:139]\n"
      "buffer_load_dwordx4 v[190:193], v36, s[28:31], s47 offen\n"
      "v_mfma_f32_16x16x32_bf16 v[136:139], a[64:67], v[108:111], v[136:139]\n"
      "v_mul_f32_e64 v188, v141, v188\n"
      "v_cvt_pk_bf16_f32 v169, v114, v115\n"
      "v_mfma_f32_16x16x32_bf16 v[136:139], a[68:71], v[160:163], v[136:139]\n"
      "ds_read_b128 v[220:223], v0 offset:8192\n"
      "v_fma_f32 v153, v178, v178, s51\n"
      "v_mfma_f32_16x16x32_bf16 v[96:99], a[84:87], v[124:127], v[96:99]\n"
      "s_or_b32 s95, s84, s85\n"
      "v_mul_f32 v176, v141, v176\n"
      "v_mul_f32_e64 v203, v142, v203\n"
      "v_mfma_f32_16x16x32_bf16 v[96:99], a[88:91], v[108:111], v[96:99]\n"
      "ds_write_b128 v5, v[166:169]\n"
      "v_mfma_f32_16x16x32_bf16 v[96:99], a[92:95], v[160:163], v[96:99]\n"
      "v_mul_f32 v117, v140, v117\n"
      "v_mul_f32 v205, v142, v205\n"
      "v_mfma_f32_16x16x32_bf16 v[104:107], a[108:111], v[124:127], v[104:107]\n"
      "v_mul_f32 v144, v142, v144\n"
      "v_fmac_f32 v153, v179, v179\n"
      "v_mfma_f32_16x16x32_bf16 v[104:107], a[112:115], v[108:111], v[104:107]\n"
      "v_mul_f32 v119, v140, v119\n"
      "v_mul_f32 v207, v141, v207\n"
      "v_mfma_f32_16x16x32_bf16 v[104:107], a[116:119], v[160:163], v[104:107]\n"
      "ds_read_b128 v[112:115], v1 offset:8192\n"
      "v_mul_f32_e64 v209, v141, v209\n"
      "v_mfma_f32_16x16x32_bf16 v[120:123], a[132:135], v[124:127], v[120:123]\n"
      "buffer_load_dword v90, v29, s[28:31], s47 offen\n"
      "v_mfma_f32_16x16x32_bf16 v[120:123], a[136:139], v[108:111], v[120:123]\n"
      "v_fmac_f32 v153, v180, v180\n"
      "v_fma_f32 v168, v96, v96, s51\n"
      "v_mfma_f32_16x16x32_bf16 v[120:123], a[140:143], v[160:163], v[120:123]\n"
      "v_mul_f32 v177, v141, v177\n"
      "v_cvt_pk_bf16_f32 v206, v206, v207\n"
      "v_mfma_f32_16x16x32_bf16 v[130:133], v[182:185], a[144:147], 0\n"
      "v_cvt_pk_bf16_f32 v186, v186, v187\n"
      "v_fmac_f32_e64 v153, v181, v181\n"
      "v_mfma_f32_16x16x32_bf16 v[130:133], v[210:213], a[148:151], v[130:133]\n"
      "v_cvt_pk_bf16_f32 v202, v202, v203\n"
      "v_fmac_f32_e64 v153, v170, v170\n"
      "v_mfma_f32_16x16x32_bf16 v[130:133], v[156:159], a[152:155], v[130:133]\n"
      "ds_read_b128 v[198:201], v0 offset:14336\n"
      "v_fmac_f32 v168, v97, v97\n"
      "v_mfma_f32_16x16x32_bf16 v[130:133], v[124:127], a[156:159], v[130:133]\n"
      "v_mul_f32 v149, v142, v149\n"
      "v_cvt_pk_bf16_f32 v207, v208, v209\n"
      "v_mfma_f32_16x16x32_bf16 v[130:133], v[108:111], a[160:163], v[130:133]\n"
      "v_fmac_f32 v153, v171, v171\n"
      "v_fmac_f32 v168, v98, v98\n"
      "v_mfma_f32_16x16x32_bf16 v[130:133], v[160:163], a[164:167], v[130:133]\n"
      "v_fmac_f32_e64 v168, v99, v99\n"
      "v_cvt_pk_bf16_f32 v116, v116, v117\n"
      "v_mfma_f32_16x16x32_bf16 v[214:217], v[182:185], a[168:171], 0\n"
      "v_cvt_pk_bf16_f32 v187, v188, v189\n"
      "v_fmac_f32_e64 v168, v104, v104\n"
      "v_mfma_f32_16x16x32_bf16 v[214:217], v[210:213], a[172:175], v[214:217]\n"
      "v_mul_f32 v151, v142, v151\n"
      "v_cvt_pk_bf16_f32 v189, v176, v177\n"
      "v_mfma_f32_16x16x32_bf16 v[214:217], v[156:159], a[176:179], v[214:217]\n"
      "v_fmac_f32 v168, v105, v105\n"
      "v_cvt_pk_bf16_f32 v203, v204, v205\n"
      "v_mfma_f32_16x16x32_bf16 v[214:217], v[124:127], a[180:183], v[214:217]\n"
      "s_or_b32 s95, s95, s48\n"
      "v_fmac_f32 v153, v172, v172\n"
      "v_cvt_pk_bf16_f32 v188, v174, v175\n"
      "v_mfma_f32_16x16x32_bf16 v[214:217], v[108:111], a[184:187], v[214:217]\n"
      "ds_read_b128 v[174:177], v1 offset:2048\n"
      "v_fmac_f32 v168, v106, v106\n"
      "v_mfma_f32_16x16x32_bf16 v[214:217], v[160:163], a[188:191], v[214:217]\n"
      "v_fmac_f32 v168, v107, v107\n"
      "v_fmac_f32 v153, v173, v173\n"
      "v_mfma_f32_16x16x32_bf16 v[194:197], v[182:185], a[192:195], 0\n"
      "v_rcp_f32 v152, v152\n"
      "v_mfma_f32_16x16x32_bf16 v[194:197], v[210:213], a[196:199], v[194:197]\n"
      "v_fmac_f32 v168, v120, v120\n"
      "v_mul_f32 v145, v142, v145\n"
      "v_mfma_f32_16x16x32_bf16 v[194:197], v[156:159], a[200:203], v[194:197]\n"
      "ds_write_b128 v5, v[186:189] offset:6144\n"
      "v_mfma_f32_16x16x32_bf16 v[194:197], v[124:127], a[204:207], v[194:197]\n"
      "ds_read_b128 v[210:213], v1 offset:14336\n"
      "v_fmac_f32 v153, v136, v136\n"
      "v_mfma_f32_16x16x32_bf16 v[194:197], v[108:111], a[208:211], v[194:197]\n"
      "v_cvt_pk_bf16_f32 v148, v148, v149\n"
      "v_fmac_f32 v168, v121, v121\n"
      "v_mfma_f32_16x16x32_bf16 v[194:197], v[160:163], a[212:215], v[194:197]\n"
      "ds_read_b128 v[184:187], v9 offset:5632\n"
      "v_fmac_f32 v153, v137, v137\n"
      "v_mfma_f32_16x16x32_bf16 v[158:161], a[0:3], v[100:103], 0\n"
      "s_waitcnt lgkmcnt(3)\n"
      "v_fmac_f32 v153, v138, v138\n"
      "v_fmac_f32 v168, v122, v122\n"
      "v_mfma_f32_16x16x32_bf16 v[158:161], a[4:7], v[174:177], v[158:161]\n"
      "v_fmac_f32 v153, v139, v139\n"
      "v_fmac_f32 v168, v123, v123\n"
      "v_mfma_f32_16x16x32_bf16 v[158:161], a[8:11], v[220:223], v[158:161]\n"
      "ds_read_b128 v[162:165], v0 offset:4096\n"
      "v_mul_f32 v147, v142, v147\n"
      "v_mfma_f32_16x16x32_bf16 v[158:161], a[12:15], v[112:115], v[158:161]\n"
      "v_permlane16_swap_b32_e64 v153, v168\n"
      "v_mfma_f32_16x16x32_bf16 v[158:161], a[16:19], v[198:201], v[158:161]\n"
      "s_waitcnt lgkmcnt(2)\n"
      "v_cndmask_b32 v218, v218, v134, s[58:59]\n"
      "v_cvt_pk_bf16_f32 v130, v130, v131\n"
      "v_mfma_f32_16x16x32_bf16 v[158:161], a[20:23], v[210:213], v[158:161]\n"
      "v_cvt_pk_bf16_f32 v205, v146, v147\n"
      "v_add_f32 v153, v153, v168\n"
      "v_mfma_f32_16x16x32_bf16 v[108:111], a[24:27], v[100:103], 0\n"
      "s_and_b32 s86, s86, -4\n"
      "v_mov_b32_e64 v168, v153\n"
      "v_cvt_pk_bf16_f32 v131, v132, v133\n"
      "v_mfma_f32_16x16x32_bf16 v[108:111], a[28:31], v[174:177], v[108:111]\n"
      "v_permlane32_swap_b32_e64 v153, v168\n"
      "v_mfma_f32_16x16x32_bf16 v[108:111], a[32:35], v[220:223], v[108:111]\n"
      "ds_write_b64 v6, v[206:207] offset:6144\n"
      "v_mfma_f32_16x16x32_bf16 v[108:111], a[36:39], v[112:115], v[108:111]\n"
      "v_cvt_pk_bf16_f32 v194, v194, v195\n"
      "v_add_f32_e64 v153, v153, v168\n"
      "v_mfma_f32_16x16x32_bf16 v[108:111], a[40:43], v[198:201], v[108:111]\n"
      "v_mul_f32 v150, v142, v150\n"
      "v_cvt_pk_bf16_f32 v204, v144, v145\n"
      "v_mfma_f32_16x16x32_bf16 v[108:111], a[44:47], v[210:213], v[108:111]\n"
      "s_waitcnt lgkmcnt(1)\n"
      "v_cvt_pk_bf16_f32 v214, v214, v215\n"
      "v_fma_f32 v81, v158, v158, s51\n"
      "v_mfma_f32_16x16x32_bf16 v[154:157], a[48:51], v[100:103], 0\n"
      "v_or3_b32 v184, v184, v185, v186\n"
      "v_cvt_pk_bf16_f32 v215, v216, v217\n"
      "v_mfma_f32_16x16x32_bf16 v[154:157], a[52:55], v[174:177], v[154:157]\n"
      "ds_read_b128 v[206:209], v1 offset:10240\n"
      "v_mul_f32 v118, v140, v118\n"
      "v_mfma_f32_16x16x32_bf16 v[154:157], a[56:59], v[220:223], v[154:157]\n"
      "v_rsq_f32 v153, v153\n"
      "v_mfma_f32_16x16x32_bf16 v[154:157], a[60:63], v[112:115], v[154:157]\n"
      "v_cndmask_b32 v218, v218, v128, s[60:61]\n"
      "v_mov_b32 v168, v153\n"
      "v_mfma_f32_16x16x32_bf16 v[154:157], a[64:67], v[198:201], v[154:157]\n"
      "s_nop 0\n"
      "v_permlane16_swap_b32 v153, v168\n"
      "v_mfma_f32_16x16x32_bf16 v[154:157], a[68:71], v[210:213], v[154:157]\n"
      "v_mul_f32 v153, s50, v153\n"
      "v_mul_f32 v104, v168, v104\n"
      "v_mfma_f32_16x16x32_bf16 v[126:129], a[72:75], v[100:103], 0\n"
      "v_mul_f32 v98, v168, v98\n"
      "v_fmac_f32 v81, v159, v159\n"
      "v_mfma_f32_16x16x32_bf16 v[126:129], a[76:79], v[174:177], v[126:129]\n"
      "v_mul_f32 v173, v153, v173\n"
      "v_cvt_pk_bf16_f32 v117, v118, v119\n"
      "v_mfma_f32_16x16x32_bf16 v[126:129], a[80:83], v[220:223], v[126:129]\n"
      "v_cvt_pk_bf16_f32 v195, v196, v197\n"
      "v_fmac_f32 v81, v160, v160\n"
      "v_mfma_f32_16x16x32_bf16 v[126:129], a[84:87], v[112:115], v[126:129]\n"
      "v_mul_f32 v170, v153, v170\n"
      "v_mul_f32_e64 v179, v153, v179\n"
      "v_mfma_f32_16x16x32_bf16 v[126:129], a[88:91], v[198:201], v[126:129]\n"
      "v_mul_f32 v137, v153, v137\n"
      "v_mul_f32 v218, v152, v218\n"
      "v_mfma_f32_16x16x32_bf16 v[126:129], a[92:95], v[210:213], v[126:129]\n"
      "ds_write_b64 v6, v[116:117]\n"
      "v_mfma_f32_16x16x32_bf16 v[140:143], a[96:99], v[100:103], 0\n"
      "v_mul_f32 v123, v168, v123\n"
      "v_fmac_f32 v81, v161, v161\n"
      "v_mfma_f32_16x16x32_bf16 v[140:143], a[100:103], v[174:177], v[140:143]\n"
      "v_fmac_f32_e64 v81, v108, v108\n"
      "v_cvt_pk_bf16_f32 v149, v150, v151\n"
      "v_mfma_f32_16x16x32_bf16 v[140:143], a[104:107], v[220:223], v[140:143]\n"
      "v_fma_f32 v216, v126, v126, s51\n"
      "v_fmac_f32_e64 v216, v127, v127\n"
      "v_mfma_f32_16x16x32_bf16 v[140:143], a[108:111], v[112:115], v[140:143]\n"
      "ds_write_b128 v5, v[202:205] offset:12288\n"
      "v_mfma_f32_16x16x32_bf16 v[140:143], a[112:115], v[198:201], v[140:143]\n"
      "v_mul_f32 v99, v168, v99\n"
      "v_fmac_f32 v216, v128, v128\n"
      "v_mfma_f32_16x16x32_bf16 v[140:143], a[116:119], v[210:213], v[140:143]\n"
      "v_mul_f32 v121, v168, v121\n"
      "v_fmac_f32 v81, v109, v109\n"
      "v_mfma_f32_16x16x32_bf16 v[144:147], a[120:123], v[100:103], 0\n"
      "v_mul_f32 v107, v168, v107\n"
      "v_mul_f32 v96, v168, v96\n"
      "v_mfma_f32_16x16x32_bf16 v[144:147], a[124:127], v[174:177], v[144:147]\n"
      "v_fmac_f32 v81, v110, v110\n"
      "v_fmac_f32 v216, v129, v129\n"
      "v_mfma_f32_16x16x32_bf16 v[144:147], a[128:131], v[220:223], v[144:147]\n"
      "v_mul_f32 v178, v153, v178\n"
      "v_mul_f32 v106, v168, v106\n"
      "v_mfma_f32_16x16x32_bf16 v[144:147], a[132:135], v[112:115], v[144:147]\n"
      "ds_write_b64 v6, v[148:149] offset:12288\n"
      "v_mfma_f32_16x16x32_bf16 v[144:147], a[136:139], v[198:201], v[144:147]\n"
      "v_mul_f32 v171, v153, v171\n"
      "v_mul_f32 v138, v153, v138\n"
      "v_mfma_f32_16x16x32_bf16 v[144:147], a[140:143], v[210:213], v[144:147]\n"
      "v_mul_f32 v120, v168, v120\n"
      "v_cvt_pk_bf16_f32 v218, v218, v218\n"
      "v_mfma_f32_16x16x32_bf16 v[202:205], v[100:103], a[144:147], 0\n"
      "v_fmac_f32 v216, v140, v140\n"
      "v_fmac_f32 v81, v111, v111\n"
      "v_mfma_f32_16x16x32_bf16 v[202:205], v[174:177], a[148:151], v[202:205]\n"
      "ds_write_b16 v12, v218 offset:16\n"
      "v_mfma_f32_16x16x32_bf16 v[202:205], v[220:223], a[152:155], v[202:205]\n"
      "v_mul_f32 v105, v168, v105\n"
      "v_mul_f32 v122, v168, v122\n"
      "v_mfma_f32_16x16x32_bf16 v[202:205], v[112:115], a[156:159], v[202:205]\n"
      "v_fmac_f32 v216, v141, v141\n"
      "v_fmac_f32 v216, v142, v142\n"
      "v_mfma_f32_16x16x32_bf16 v[202:205], v[198:201], a[160:163], v[202:205]\n"
      "v_mul_f32 v181, v153, v181\n"
      "v_fmac_f32 v216, v143, v143\n"
      "v_mfma_f32_16x16x32_bf16 v[202:205], v[210:213], a[164:167], v[202:205]\n"
      "ds_read_b128 v[116:119], v0 offset:16384\n"
      "v_fmac_f32 v81, v154, v154\n"
      "v_mfma_f32_16x16x32_bf16 v[148:151], v[100:103], a[168:171], 0\n"
      "v_mul_f32 v97, v168, v97\n"
      "v_fmac_f32 v81, v155, v155\n"
      "v_mfma_f32_16x16x32_bf16 v[148:151], v[174:177], a[172:175], v[148:151]\n"
      "v_fmac_f32_e64 v216, v144, v144\n"
      "v_bitop3_b32 v184, v184, v187, s33 bitop3:0xa8\n"
      "v_mfma_f32_16x16x32_bf16 v[148:151], v[220:223], a[176:179], v[148:151]\n"
      "v_cvt_pk_bf16_f32 v178, v178, v179\n"
      "v_fmac_f32 v216, v145, v145\n"
      "v_mfma_f32_16x16x32_bf16 v[148:151], v[112:115], a[180:183], v[148:151]\n"
      "v_fmac_f32 v81, v156, v156\n"
      "v_cvt_pk_bf16_f32 v96, v96, v97\n"
      "v_mfma_f32_16x16x32_bf16 v[148:151], v[198:201], a[184:187], v[148:151]\n"
      "ds_read_b128 v[166:169], v1 offset:4096\n"
      "v_fmac_f32_e64 v216, v146, v146\n"
      "v_mfma_f32_16x16x32_bf16 v[148:151], v[210:213], a[188:191], v[148:151]\n"
      "ds_read_b128 v[186:189], v1 offset:16384\n"
      "v_fmac_f32_e64 v81, v157, v157\n"
      "v_mfma_f32_16x16x32_bf16 v[132:135], v[100:103], a[192:195], 0\n"
      "ds_read_b128 v[100:103], v0 offset:10240\n"
      "v_fmac_f32 v216, v147, v147\n"
      "v_mfma_f32_16x16x32_bf16 v[132:135], v[174:177], a[196:199], v[132:135]\n"
      "s_nop 0\n"
      "v_permlane16_swap_b32_e64 v81, v216\n"
      "v_mfma_f32_16x16x32_bf16 v[132:135], v[220:223], a[200:203], v[132:135]\n"
      "v_cvt_pk_bf16_f32 v217, v150, v151\n"
      "v_mul_f32_e64 v172, v153, v172\n"
      "v_mfma_f32_16x16x32_bf16 v[132:135], v[112:115], a[204:207], v[132:135]\n"
      "v_cvt_pk_bf16_f32 v97, v98, v99\n"
      "v_add_f32 v81, v81, v216\n"
      "v_mfma_f32_16x16x32_bf16 v[132:135], v[198:201], a[208:211], v[132:135]\n"
      "v_mov_b32 v216, v81\n"
      "v_mul_f32_e64 v139, v153, v139\n"
      "v_mfma_f32_16x16x32_bf16 v[132:135], v[210:213], a[212:215], v[132:135]\n"
      "v_permlane32_swap_b32 v81, v216\n"
      "v_mfma_f32_16x16x32_bf16 v[198:201], a[0:3], v[162:165], 0\n"
      "s_waitcnt lgkmcnt(0)\n"
      "v_mul_f32 v180, v153, v180\n"
      "v_add_f32 v81, v81, v216\n"
      "v_mfma_f32_16x16x32_bf16 v[198:201], a[4:7], v[166:169], v[198:201]\n"
      "v_rsq_f32 v81, v81\n"
      "v_mfma_f32_16x16x32_bf16 v[198:201], a[8:11], v[100:103], v[198:201]\n"
      "v_cvt_pk_bf16_f32 v179, v180, v181\n"
      "v_mov_b32 v216, v81\n"
      "v_mfma_f32_16x16x32_bf16 v[198:201], a[12:15], v[206:209], v[198:201]\n"
      "s_nop 0\n"
      "v_permlane16_swap_b32 v81, v216\n"
      "v_mfma_f32_16x16x32_bf16 v[198:201], a[16:19], v[116:119], v[198:201]\n"
      "v_cvt_pk_bf16_f32 v120, v120, v121\n"
      "v_mul_f32 v128, v216, v128\n"
      "v_mfma_f32_16x16x32_bf16 v[198:201], a[20:23], v[186:189], v[198:201]\n"
      "v_mul_f32 v127, v216, v127\n"
      "v_cvt_pk_bf16_f32 v98, v104, v105\n"
      "v_mfma_f32_16x16x32_bf16 v[218:221], a[24:27], v[162:165], 0\n"
      "v_mul_f32 v126, v216, v126\n"
      "v_mul_f32 v81, s50, v81\n"
      "v_mfma_f32_16x16x32_bf16 v[218:221], a[28:31], v[166:169], v[218:221]\n"
      "v_mul_f32 v129, v216, v129\n"
      "v_mul_f32 v157, v81, v157\n"
      "v_mfma_f32_16x16x32_bf16 v[218:221], a[32:35], v[100:103], v[218:221]\n"
      "v_cmp_ne_u32 s[56:57], 0, v184\n"
      "v_fma_f32 v78, v198, v198, s51\n"
      "v_mfma_f32_16x16x32_bf16 v[218:221], a[36:39], v[206:209], v[218:221]\n"
      "s_cmp_ge_u32 s95, 0x40000000\n"
      "s_cselect_b32 s94, -1, -2\n"
      "v_mul_f32 v158, v81, v158\n"
      "v_mfma_f32_16x16x32_bf16 v[218:221], a[40:43], v[116:119], v[218:221]\n"
      "s_cmp_gt_u32 s42, s44\n"
      "s_cselect_b32 s95, s94, s95\n"
      "v_fmac_f32 v78, v199, v199\n"
      "v_mfma_f32_16x16x32_bf16 v[218:221], a[44:47], v[186:189], v[218:221]\n"
      "ds_read_b128 v[210:213], v2 offset:21504\n"
      "v_cvt_pk_bf16_f32 v121, v122, v123\n"
      "v_mfma_f32_16x16x32_bf16 v[182:185], a[48:51], v[162:165], 0\n"
      "v_cvt_pk_bf16_f32 v196, v132, v133\n"
      "v_fmac_f32 v78, v200, v200\n"
      "v_mfma_f32_16x16x32_bf16 v[182:185], a[52:55], v[166:169], v[182:185]\n"
      "v_cvt_pk_bf16_f32 v197, v134, v135\n"
      "v_fmac_f32 v78, v201, v201\n"
      "v_mfma_f32_16x16x32_bf16 v[182:185], a[56:59], v[100:103], v[182:185]\n"
      "v_fmac_f32 v78, v218, v218\n"
      "v_fmac_f32 v78, v219, v219\n"
      "v_mfma_f32_16x16x32_bf16 v[182:185], a[60:63], v[206:209], v[182:185]\n"
      "v_cvt_pk_bf16_f32 v133, v204, v205\n"
      "v_mul_f32_e64 v154, v81, v154\n"
      "v_mfma_f32_16x16x32_bf16 v[182:185], a[64:67], v[116:119], v[182:185]\n"
      "v_mul_f32 v140, v216, v140\n"
      "v_cvt_pk_bf16_f32 v126, v126, v127\n"
      "v_mfma_f32_16x16x32_bf16 v[182:185], a[68:71], v[186:189], v[182:185]\n"
      "s_cmp_ge_u32 s95, 0x40000000\n"
      "s_cselect_b64 vcc, -1, s[56:57]\n"
      "v_cvt_pk_bf16_f32 v99, v106, v107\n"
      "v_mfma_f32_16x16x32_bf16 v[122:125], a[72:75], v[162:165], 0\n"
      "v_mul_f32 v136, v153, v136\n"
      "v_mul_f32_e64 v144, v216, v144\n"
      "v_mfma_f32_16x16x32_bf16 v[122:125], a[76:79], v[166:169], v[122:125]\n"
      "v_mul_f32 v159, v81, v159\n"
      "v_mul_f32 v143, v216, v143\n"
      "v_mfma_f32_16x16x32_bf16 v[122:125], a[80:83], v[100:103], v[122:125]\n"
      "v_cvt_pk_bf16_f32 v136, v136, v137\n"
      "v_cvt_pk_bf16_f32 v127, v128, v129\n"
      "v_mfma_f32_16x16x32_bf16 v[122:125], a[84:87], v[206:209], v[122:125]\n"
      "v_mul_f32 v147, v216, v147\n"
      "v_mul_f32 v141, v216, v141\n"
      "v_mfma_f32_16x16x32_bf16 v[122:125], a[88:91], v[116:119], v[122:125]\n"
      "v_mul_f32 v142, v216, v142\n"
      "v_mul_f32 v109, v81, v109\n"
      "v_mfma_f32_16x16x32_bf16 v[122:125], a[92:95], v[186:189], v[122:125]\n"
      "v_mul_f32 v146, v216, v146\n"
      "v_mul_f32 v145, v216, v145\n"
      "v_mfma_f32_16x16x32_bf16 v[174:177], a[96:99], v[162:165], 0\n"
      "v_cvt_pk_bf16_f32 v180, v170, v171\n"
      "v_fmac_f32 v78, v220, v220\n"
      "v_mfma_f32_16x16x32_bf16 v[174:177], a[100:103], v[166:169], v[174:177]\n"
      "v_mul_f32 v156, v81, v156\n"
      "v_fmac_f32_e64 v78, v221, v221\n"
      "v_mfma_f32_16x16x32_bf16 v[174:177], a[104:107], v[100:103], v[174:177]\n"
      "v_mul_f32 v160, v81, v160\n"
      "v_fma_f32 v135, v122, v122, s51\n"
      "v_mfma_f32_16x16x32_bf16 v[174:177], a[108:111], v[206:209], v[174:177]\n"
      "v_fmac_f32 v135, v123, v123\n"
      "v_fmac_f32 v135, v124, v124\n"
      "v_mfma_f32_16x16x32_bf16 v[174:177], a[112:115], v[116:119], v[174:177]\n"
      "v_mul_f32 v111, v81, v111\n"
      "v_fmac_f32 v135, v125, v125\n"
      "v_mfma_f32_16x16x32_bf16 v[174:177], a[116:119], v[186:189], v[174:177]\n"
      "v_fmac_f32 v78, v182, v182\n"
      "v_fmac_f32 v78, v183, v183\n"
      "v_mfma_f32_16x16x32_bf16 v[104:107], a[120:123], v[162:165], 0\n"
      "v_cvt_pk_bf16_f32 v216, v148, v149\n"
      "v_mul_f32 v155, v81, v155\n"
      "v_mfma_f32_16x16x32_bf16 v[104:107], a[124:127], v[166:169], v[104:107]\n"
      "v_mul_f32 v108, v81, v108\n"
      "v_cvt_pk_bf16_f32 v144, v144, v145\n"
      "v_mfma_f32_16x16x32_bf16 v[104:107], a[128:131], v[100:103], v[104:107]\n"
      "v_cvt_pk_bf16_f32 v181, v172, v173\n"
      "v_mul_f32 v161, v81, v161\n"
      "v_mfma_f32_16x16x32_bf16 v[104:107], a[132:135], v[206:209], v[104:107]\n"
      "v_fmac_f32 v135, v174, v174\n"
      "v_fmac_f32 v78, v184, v184\n"
      "v_mfma_f32_16x16x32_bf16 v[104:107], a[136:139], v[116:119], v[104:107]\n"
      "v_cvt_pk_bf16_f32 v132, v202, v203\n"
      "v_cvt_pk_bf16_f32 v158, v158, v159\n"
      "v_mfma_f32_16x16x32_bf16 v[104:107], a[140:143], v[186:189], v[104:107]\n"
      "s_add_u32 s43, s43, 1\n"
      "v_fmac_f32 v135, v175, v175\n"
      "v_cvt_pk_bf16_f32 v128, v140, v141\n"
      "v_mfma_f32_16x16x32_bf16 v[150:153], v[162:165], a[144:147], 0\n"
      "v_fmac_f32 v135, v176, v176\n"
      "v_fmac_f32 v135, v177, v177\n"
      "v_mfma_f32_16x16x32_bf16 v[150:153], v[166:169], a[148:151], v[150:153]\n"
      "ds_read_u16 v86, v11 offset:64\n"
      "v_mul_f32 v110, v81, v110\n"
      "v_mfma_f32_16x16x32_bf16 v[150:153], v[100:103], a[152:155], v[150:153]\n"
      "v_fmac_f32 v135, v104, v104\n"
      "v_fmac_f32 v135, v105, v105\n"
      "v_mfma_f32_16x16x32_bf16 v[150:153], v[206:209], a[156:159], v[150:153]\n"
      "v_fmac_f32 v78, v185, v185\n"
      "v_cvt_pk_bf16_f32 v145, v146, v147\n"
      "v_mfma_f32_16x16x32_bf16 v[150:153], v[116:119], a[160:163], v[150:153]\n"
      "v_cvt_pk_bf16_f32 v129, v142, v143\n"
      "v_fmac_f32_e64 v135, v106, v106\n"
      "v_mfma_f32_16x16x32_bf16 v[150:153], v[186:189], a[164:167], v[150:153]\n"
      "ds_read_b128 v[146:149], v2 offset:24576\n"
      "v_cvt_pk_bf16_f32 v137, v138, v139\n"
      "v_mfma_f32_16x16x32_bf16 v[170:173], v[162:165], a[168:171], 0\n"
      "v_fmac_f32 v135, v107, v107\n"
      "v_cvt_pk_bf16_f32 v159, v160, v161\n"
      "v_mfma_f32_16x16x32_bf16 v[170:173], v[166:169], a[172:175], v[170:173]\n"
      "v_permlane16_swap_b32 v78, v135\n"
      "v_mfma_f32_16x16x32_bf16 v[170:173], v[100:103], a[176:179], v[170:173]\n"
      "v_add_f32 v78, v78, v135\n"
      "v_mov_b32 v135, v78\n"
      "v_mfma_f32_16x16x32_bf16 v[170:173], v[206:209], a[180:183], v[170:173]\n"
      "s_nop 0\n"
      "v_permlane32_swap_b32 v78, v135\n"
      "v_mfma_f32_16x16x32_bf16 v[170:173], v[116:119], a[184:187], v[170:173]\n"
      "ds_read_b128 v[202:205], v2 offset:18432\n"
      "v_add_f32 v78, v78, v135\n"
      "v_mfma_f32_16x16x32_bf16 v[170:173], v[186:189], a[188:191], v[170:173]\n"
      "v_rsq_f32 v78, v78\n"
      "v_mfma_f32_16x16x32_bf16 v[140:143], v[162:165], a[192:195], 0\n"
      "v_cvt_pk_bf16_f32 v161, v110, v111\n"
      "v_mov_b32_e64 v135, v78\n"
      "v_mfma_f32_16x16x32_bf16 v[140:143], v[166:169], a[196:199], v[140:143]\n"
      "ds_read_b128 v[162:165], v2 offset:27648\n"
      "v_mfma_f32_16x16x32_bf16 v[140:143], v[100:103], a[200:203], v[140:143]\n"
      "v_permlane16_swap_b32 v78, v135\n"
      "v_mfma_f32_16x16x32_bf16 v[140:143], v[206:209], a[204:207], v[140:143]\n"
      "v_cvt_pk_bf16_f32 v81, v172, v173\n"
      "v_mul_f32 v175, v135, v175\n"
      "v_mfma_f32_16x16x32_bf16 v[140:143], v[116:119], a[208:211], v[140:143]\n"
      "s_waitcnt lgkmcnt(1)\n"
      "v_mul_f32 v78, s50, v78\n"
      "v_mul_f32_e64 v104, v135, v104\n"
      "v_mfma_f32_16x16x32_bf16 v[140:143], v[186:189], a[212:215], v[140:143]\n"
      "ds_read_b128 v[186:189], v2 offset:30720\n"
      "v_mul_f32 v199, v78, v199\n"
      "v_mfma_f32_16x16x32_bf16 v[166:169], a[216:219], v[202:205], 0\n"
      "ds_read_b128 v[206:209], v2 offset:33792\n"
      "v_mul_f32 v221, v78, v221\n"
      "v_mfma_f32_16x16x32_bf16 v[166:169], a[220:223], v[210:213], v[166:169]\n"
      "v_mul_f32 v125, v135, v125\n"
      "v_mul_f32 v105, v135, v105\n"
      "v_mfma_f32_16x16x32_bf16 v[166:169], a[224:227], v[146:149], v[166:169]\n"
      "v_cvt_pk_bf16_f32 v76, v150, v151\n"
      "v_mul_f32 v176, v135, v176\n"
      "v_mfma_f32_16x16x32_bf16 v[112:115], a[240:243], v[202:205], 0\n"
      "ds_read_b128 v[100:103], v2 offset:18688\n"
      "v_mul_f32 v198, v78, v198\n"
      "v_mfma_f32_16x16x32_bf16 v[112:115], a[244:247], v[210:213], v[112:115]\n"
      "v_mul_f32 v123, v135, v123\n"
      "v_mul_f32 v220, v78, v220\n"
      "v_mfma_f32_16x16x32_bf16 v[112:115], a[248:251], v[146:149], v[112:115]\n"
      "v_mul_f32 v184, v78, v184\n"
      "v_mul_f32 v122, v135, v122\n"
      "v_mfma_f32_16x16x32_bf16 v[116:119], v[232:235], v[202:205], 0\n"
      "v_mul_f32 v107, v135, v107\n"
      "v_cvt_pk_bf16_f32 v77, v152, v153\n"
      "v_mfma_f32_16x16x32_bf16 v[116:119], v[236:239], v[210:213], v[116:119]\n"
      "v_cvt_pk_bf16_f32 v122, v122, v123\n"
      "v_mul_f32 v174, v135, v174\n"
      "v_mfma_f32_16x16x32_bf16 v[116:119], v[240:243], v[146:149], v[116:119]\n"
      "s_waitcnt lgkmcnt(1)\n"
      "v_mul_f32 v124, v135, v124\n"
      "v_cvt_pk_bf16_f32 v198, v198, v199\n"
      "v_mfma_f32_16x16x32_bf16 v[166:169], a[228:231], v[162:165], v[166:169]\n"
      "v_cvt_pk_bf16_f32 v84, v140, v141\n"
      "v_cvt_pk_bf16_f32 v80, v170, v171\n"
      "v_mfma_f32_16x16x32_bf16 v[166:169], a[232:235], v[186:189], v[166:169]\n"
      "ds_read_b128 v[202:205], v10 offset:32\n"
      "v_mul_f32_e64 v177, v135, v177\n"
      "v_mfma_f32_16x16x32_bf16 v[166:169], a[236:239], v[206:209], v[166:169]\n"
      "v_cvt_pk_bf16_f32 v160, v108, v109\n"
      "v_cvt_pk_bf16_f32 v154, v154, v155\n"
      "v_mfma_f32_16x16x32_bf16 v[112:115], a[252:255], v[162:165], v[112:115]\n"
      "v_cvt_pk_bf16_f32 v123, v124, v125\n"
      "v_cvt_pk_bf16_f32 v124, v174, v175\n"
      "v_mfma_f32_16x16x32_bf16 v[112:115], v[224:227], v[186:189], v[112:115]\n"
      "ds_read_b64 v[170:171], v10 offset:24\n"
      "v_cvt_pk_bf16_f32 v85, v142, v143\n"
      "v_mfma_f32_16x16x32_bf16 v[112:115], v[228:231], v[206:209], v[112:115]\n"
      "v_cvt_pk_bf16_f32 v104, v104, v105\n"
      "v_mul_f32 v183, v78, v183\n"
      "v_mfma_f32_16x16x32_bf16 v[116:119], v[244:247], v[162:165], v[116:119]\n"
      "ds_read_u16 v82, v11 offset:32\n"
      "v_mul_f32 v106, v135, v106\n"
      "v_mfma_f32_16x16x32_bf16 v[116:119], v[248:251], v[186:189], v[116:119]\n"
      "v_cvt_pk_bf16_f32 v105, v106, v107\n"
      "v_mul_f32 v200, v78, v200\n"
      "v_mfma_f32_16x16x32_bf16 v[116:119], v[252:255], v[206:209], v[116:119]\n"
      "s_waitcnt lgkmcnt(2)\n"
      "v_mul_f32 v111, v167, v167\n"
      "v_cvt_pk_bf16_f32 v125, v176, v177\n"
      "v_mfma_f32_16x16x32_bf16 v[146:149], v[96:99], v[178:181], v[40:43]\n"
      "ds_read_b128 v[210:213], v2 offset:24832\n"
      "v_mul_f32 v201, v78, v201\n"
      "v_mfma_f32_16x16x32_bf16 v[150:153], v[126:129], v[178:181], v[44:47]\n"
      "ds_read_b128 v[140:143], v2 offset:27904\n"
      "v_mul_f32 v219, v78, v219\n"
      "v_mfma_f32_16x16x32_bf16 v[162:165], v[122:125], v[178:181], v[48:51]\n"
      "ds_read_b128 v[106:109], v2 offset:21760\n"
      "v_mul_f32 v218, v78, v218\n"
      "v_mfma_f32_16x16x32_bf16 v[172:175], v[202:205], v[178:181], v[24:27]\n"
      "s_waitcnt lgkmcnt(3)\n"
      "v_mul_f32 v185, v78, v185\n"
      "v_fma_f32 v110, v166, v166, s52\n"
      "v_mfma_f32_16x16x16_bf16 v[146:149], v[120:121], v[136:137], v[146:149]\n"
      "v_cvt_pk_bf16_f32 v155, v156, v157\n"
      "v_fmac_f32 v111, v169, v169\n"
      "v_fmac_f32 v110, v168, v168\n"
      "v_mfma_f32_16x16x16_bf16 v[150:153], v[144:145], v[136:137], v[150:153]\n"
      "v_mul_f32 v182, v78, v182\n"
      "s_waitcnt vmcnt(1)\n"
      "v_or3_b32 v190, v190, v191, v192\n"
      "v_cvt_pk_bf16_f32 v199, v200, v201\n"
      "v_mfma_f32_16x16x16_bf16 v[162:165], v[104:105], v[136:137], v[162:165]\n"
      "v_cvt_pk_bf16_f32 v201, v220, v221\n"
      "v_exp_f32 v146, v146\n"
      "v_mfma_f32_16x16x16_bf16 v[172:175], v[170:171], v[136:137], v[172:175]\n"
      "v_cvt_pk_bf16_f32 v182, v182, v183\n"
      "v_exp_f32 v153, v153\n"
      "v_mfma_f32_16x16x32_bf16 v[186:189], a[216:219], v[100:103], 0\n"
      "s_waitcnt lgkmcnt(0)\n"
      "v_exp_f32 v151, v151\n"
      "v_mfma_f32_16x16x32_bf16 v[186:189], a[220:223], v[106:109], v[186:189]\n"
      "v_exp_f32 v165, v165\n"
      "v_mfma_f32_16x16x32_bf16 v[186:189], a[224:227], v[210:213], v[186:189]\n"
      "v_exp_f32 v152, v152\n"
      "v_mfma_f32_16x16x32_bf16 v[174:177], a[240:243], v[100:103], 0\n"
      "v_exp_f32 v147, v147\n"
      "v_mfma_f32_16x16x32_bf16 v[174:177], a[244:247], v[106:109], v[174:177]\n"
      "v_exp_f32 v162, v162\n"
      "v_mfma_f32_16x16x32_bf16 v[174:177], a[248:251], v[210:213], v[174:177]\n"
      "v_exp_f32 v150, v150\n"
      "v_mfma_f32_16x16x32_bf16 v[206:209], v[232:235], v[100:103], 0\n"
      "v_exp_f32 v163, v163\n"
      "v_mfma_f32_16x16x32_bf16 v[206:209], v[236:239], v[106:109], v[206:209]\n"
      "v_exp_f32_e64 v148, v148\n"
      "v_mfma_f32_16x16x32_bf16 v[206:209], v[240:243], v[210:213], v[206:209]\n"
      "v_exp_f32 v172, v172\n"
      "v_mfma_f32_16x16x32_bf16 v[210:213], v[96:99], v[158:161], v[52:55]\n"
      "v_exp_f32 v149, v149\n"
      "v_mfma_f32_16x16x32_bf16 v[134:137], v[126:129], v[158:161], v[56:59]\n"
      "ds_read_u16 v78, v11\n"
      "v_cvt_pk_bf16_f32 v200, v218, v219\n"
      "v_mfma_f32_16x16x32_bf16 v[218:221], v[122:125], v[158:161], v[60:63]\n"
      "v_cvt_pk_bf16_f32 v183, v184, v185\n"
      "v_cvt_pk_bf16_f32 v146, v146, v147\n"
      "v_mfma_f32_16x16x32_bf16 v[106:109], v[202:205], v[158:161], v[26:29]\n"
      "ds_read_b128 v[156:159], v2 offset:30976\n"
      "v_cvt_pk_bf16_f32 v147, v148, v149\n"
      "v_mfma_f32_16x16x16_bf16 v[210:213], v[120:121], v[154:155], v[210:213]\n"
      "v_exp_f32_e64 v164, v164\n"
      "v_cvt_pk_bf16_f32 v148, v150, v151\n"
      "v_mfma_f32_16x16x16_bf16 v[134:137], v[144:145], v[154:155], v[134:137]\n"
      "v_cvt_pk_bf16_f32 v89, v164, v165\n"
      "v_cvt_pk_bf16_f32 v149, v152, v153\n"
      "v_cvt_pk_bf16_f32 v88, v162, v163\n"
      "v_mfma_f32_16x16x16_bf16 v[218:221], v[104:105], v[154:155], v[218:221]\n"
      "s_waitcnt vmcnt(0)\n"
      "v_or3_b32 v190, v190, v193, v90\n"
      "v_exp_f32 v210, v210\n"
      "v_mfma_f32_16x16x16_bf16 v[106:109], v[170:171], v[154:155], v[106:109]\n"
      "v_cvt_pk_bf16_f32 v90, v172, 0\n"
      "v_exp_f32 v213, v213\n"
      "v_mfma_f32_16x16x32_bf16 v[150:153], v[32:35], v[146:149], 0\n"
      "ds_write_b32 v31, v190 offset:4608\n"
      "v_mfma_f32_16x16x32_bf16 v[150:153], v[32:35], v[88:91], v[150:153]\n"
      "v_exp_f32 v211, v211\n"
      "v_mfma_f32_16x16x32_bf16 v[162:165], v[130:133], v[146:149], 0\n"
      "s_waitcnt lgkmcnt(1)\n"
      "v_exp_f32 v137, v137\n"
      "v_mfma_f32_16x16x32_bf16 v[162:165], v[76:79], v[88:91], v[162:165]\n"
      "v_exp_f32_e64 v136, v136\n"
      "v_mfma_f32_16x16x32_bf16 v[190:193], v[214:217], v[146:149], 0\n"
      "v_exp_f32 v212, v212\n"
      "v_mfma_f32_16x16x32_bf16 v[190:193], v[80:83], v[88:91], v[190:193]\n"
      "v_rcp_f32 v150, v150\n"
      "v_mfma_f32_16x16x32_bf16 v[152:155], v[194:197], v[146:149], 0\n"
      "v_exp_f32 v134, v134\n"
      "v_mfma_f32_16x16x32_bf16 v[152:155], v[84:87], v[88:91], v[152:155]\n"
      "v_exp_f32 v135, v135\n"
      "v_mfma_f32_16x16x32_bf16 v[100:103], v[96:99], v[198:201], v[64:67]\n"
      "v_cvt_pk_bf16_f32 v210, v210, v211\n"
      "v_cvt_pk_bf16_f32 v211, v212, v213\n"
      "v_mfma_f32_16x16x32_bf16 v[178:181], v[126:129], v[198:201], v[68:71]\n"
      "v_cvt_pk_bf16_f32 v213, v136, v137\n"
      "v_cvt_pk_bf16_f32 v212, v134, v135\n"
      "v_mfma_f32_16x16x32_bf16 v[146:149], v[122:125], v[198:201], v[72:75]\n"
      "v_exp_f32 v220, v220\n"
      "v_mfma_f32_16x16x32_bf16 v[134:137], v[202:205], v[198:201], v[28:31]\n"
      "ds_read_b128 v[198:201], v2 offset:34048\n"
      "v_mul_f32 v162, v150, v162\n"
      "v_mfma_f32_16x16x16_bf16 v[100:103], v[120:121], v[182:183], v[100:103]\n"
      "v_exp_f32 v218, v218\n"
      "v_mul_f32 v163, v150, v163\n"
      "v_mfma_f32_16x16x16_bf16 v[178:181], v[144:145], v[182:183], v[178:181]\n"
      "v_mul_f32 v190, v150, v190\n"
      "v_exp_f32 v106, v106\n"
      "v_mfma_f32_16x16x16_bf16 v[146:149], v[104:105], v[182:183], v[146:149]\n"
      "v_exp_f32 v221, v221\n"
      "v_cvt_pk_bf16_f32 v162, v162, v163\n"
      "v_mfma_f32_16x16x16_bf16 v[134:137], v[170:171], v[182:183], v[134:137]\n"
      "v_exp_f32 v102, v102\n"
      "v_exp_f32 v101, v101\n"
      "v_exp_f32 v103, v103\n"
      "v_pk_mul_f32 v[154:155], v[154:155], v[150:151] op_sel_hi:[1,0]\n"
      "v_pk_mul_f32 v[164:165], v[164:165], v[150:151] op_sel_hi:[1,0]\n"
      "v_pk_mul_f32 v[192:193], v[192:193], v[150:151] op_sel_hi:[1,0]\n"
      "v_pk_mul_f32 v[152:153], v[152:153], v[150:151] op_sel_hi:[1,0]\n"
      "v_mfma_f32_16x16x32_bf16 v[186:189], a[228:231], v[140:143], v[186:189]\n"
      "v_cvt_pk_bf16_f32 v93, v220, v221\n"
      "v_mul_f32_e64 v191, v150, v191\n"
      "v_mfma_f32_16x16x32_bf16 v[186:189], a[232:235], v[156:159], v[186:189]\n"
      "s_waitcnt lgkmcnt(0)\n"
      "v_exp_f32 v147, v147\n"
      "v_mfma_f32_16x16x32_bf16 v[186:189], a[236:239], v[198:201], v[186:189]\n"
      "v_cvt_pk_bf16_f32 v163, v164, v165\n"
      "v_cvt_pk_bf16_f32 v94, v106, 0\n"
      "v_mfma_f32_16x16x32_bf16 v[174:177], a[252:255], v[140:143], v[174:177]\n"
      "ds_read_b128 v[136:139], v7 offset:18432\n"
      "v_cvt_pk_bf16_f32 v164, v190, v191\n"
      "v_mfma_f32_16x16x32_bf16 v[174:177], v[224:227], v[156:159], v[174:177]\n"
      "v_exp_f32 v100, v100\n"
      "v_mfma_f32_16x16x32_bf16 v[174:177], v[228:231], v[198:201], v[174:177]\n"
      "v_exp_f32 v219, v219\n"
      "v_mfma_f32_16x16x32_bf16 v[206:209], v[244:247], v[140:143], v[206:209]\n"
      "v_cvt_pk_bf16_f32 v92, v218, v219\n"
      "v_cvt_pk_bf16_f32 v100, v100, v101\n"
      "v_mfma_f32_16x16x32_bf16 v[206:209], v[248:251], v[156:159], v[206:209]\n"
      "ds_read_b64 v[150:151], v10 offset:16\n"
      "v_cvt_pk_bf16_f32 v101, v102, v103\n"
      "v_mfma_f32_16x16x32_bf16 v[206:209], v[252:255], v[198:201], v[206:209]\n"
      "ds_read_b128 v[156:159], v10\n"
      "v_cvt_pk_bf16_f32 v165, v192, v193\n"
      "v_mfma_f32_16x16x32_bf16 v[218:221], v[32:35], v[210:213], 0\n"
      "ds_read_b128 v[106:109], v7 offset:22528\n"
      "v_cvt_pk_bf16_f32 v152, v152, v153\n"
      "v_mfma_f32_16x16x32_bf16 v[218:221], v[32:35], v[92:95], v[218:221]\n"
      "v_exp_f32 v148, v148\n"
      "v_mfma_f32_16x16x32_bf16 v[182:185], v[130:133], v[210:213], 0\n"
      "s_waitcnt lgkmcnt(3)\n"
      "v_exp_f32_e64 v134, v134\n"
      "v_mfma_f32_16x16x32_bf16 v[182:185], v[76:79], v[92:95], v[182:185]\n"
      "buffer_store_dwordx4 v[136:139], v20, s[24:27], s46 offen sc1\n"
      "s_nop 1\n"
      "v_mfma_f32_16x16x32_bf16 v[136:139], v[214:217], v[210:213], 0\n"
      "v_exp_f32 v149, v149\n"
      "v_mfma_f32_16x16x32_bf16 v[136:139], v[80:83], v[92:95], v[136:139]\n"
      "v_exp_f32 v181, v181\n"
      "v_mfma_f32_16x16x32_bf16 v[220:223], v[194:197], v[210:213], 0\n"
      "s_waitcnt lgkmcnt(0)\n"
      "v_rcp_f32_e64 v218, v218\n"
      "v_mfma_f32_16x16x32_bf16 v[220:223], v[84:87], v[92:95], v[220:223]\n"
      "ds_write_b128 v3, v[162:165]\n"
      "v_mfma_f32_16x16x32_bf16 v[210:213], v[156:159], v[96:99], v[30:33]\n"
      "v_exp_f32_e64 v178, v178\n"
      "v_mfma_f32_16x16x32_bf16 v[96:99], v[156:159], v[126:129], v[30:33]\n"
      "ds_read_b128 v[126:129], v7 offset:26624\n"
      "v_cvt_pk_bf16_f32 v89, v148, v149\n"
      "v_mfma_f32_16x16x32_bf16 v[140:143], v[156:159], v[122:125], v[30:33]\n"
      "ds_read_b128 v[198:201], v2 offset:18944\n"
      "v_mul_f32_e64 v135, v187, v187\n"
      "v_mfma_f32_16x16x32_bf16 v[160:163], v[156:159], v[202:205], v[30:33]\n"
      "ds_read_b128 v[156:159], v2 offset:25088\n"
      "v_cvt_pk_bf16_f32 v153, v154, v155\n"
      "v_mfma_f32_16x16x16_bf16 v[210:213], v[150:151], v[120:121], v[210:213]\n"
      "v_exp_f32_e64 v146, v146\n"
      "v_cvt_pk_bf16_f32 v90, v134, 0\n"
      "v_mfma_f32_16x16x16_bf16 v[96:99], v[150:151], v[144:145], v[96:99]\n"
      "ds_read_b128 v[190:193], v2 offset:22016\n"
      "v_fma_f32 v134, v186, v186, s52\n"
      "v_mfma_f32_16x16x16_bf16 v[140:143], v[150:151], v[104:105], v[140:143]\n"
      "buffer_store_dwordx4 v[106:109], v21, s[24:27], s46 offen sc1\n"
      "v_pk_fma_f32 v[134:135], v[188:189], v[188:189], v[134:135]\n"
      "v_pk_fma_f32 v[110:111], v[112:113], v[112:113], v[110:111]\n"
      "v_pk_fma_f32 v[110:111], v[114:115], v[114:115], v[110:111]\n"
      "v_pk_mul_f32 v[220:221], v[220:221], v[218:219] op_sel_hi:[1,0]\n"
      "v_pk_mul_f32 v[184:185], v[184:185], v[218:219] op_sel_hi:[1,0]\n"
      "v_pk_mul_f32 v[222:223], v[222:223], v[218:219] op_sel_hi:[1,0]\n"
      "v_pk_mul_f32 v[182:183], v[182:183], v[218:219] op_sel_hi:[1,0]\n"
      "v_pk_fma_f32 v[110:111], v[116:117], v[116:117], v[110:111]\n"
      "v_mfma_f32_16x16x16_bf16 v[160:163], v[150:151], v[170:171], v[160:163]\n"
      "v_exp_f32_e64 v180, v180\n"
      "s_waitcnt lgkmcnt(1)\n"
      "v_mfma_f32_16x16x32_bf16 v[202:205], a[216:219], v[198:201], 0\n"
      "s_waitcnt lgkmcnt(0)\n"
      "v_cvt_pk_bf16_f32 v88, v146, v147\n"
      "v_cvt_pk_bf16_f32 v220, v220, v221\n"
      "v_mfma_f32_16x16x32_bf16 v[202:205], a[220:223], v[190:193], v[202:205]\n"
      "ds_read_b128 v[106:109], v2 offset:28160\n"
      "v_cndmask_b32 v210, v210, v96, s[58:59]\n"
      "v_mfma_f32_16x16x32_bf16 v[202:205], a[224:227], v[156:159], v[202:205]\n"
      "v_exp_f32_e64 v179, v179\n"
      "v_mfma_f32_16x16x32_bf16 v[144:147], a[240:243], v[198:201], 0\n"
      "ds_read_b128 v[122:125], v7 offset:30720\n"
      "v_cvt_pk_bf16_f32 v182, v182, v183\n"
      "v_mfma_f32_16x16x32_bf16 v[144:147], a[244:247], v[190:193], v[144:147]\n"
      "ds_write_b64 v4, v[152:153]\n"
      "v_mfma_f32_16x16x32_bf16 v[144:147], a[248:251], v[156:159], v[144:147]\n"
      "buffer_store_dwordx4 v[126:129], v22, s[24:27], s46 offen sc1\n"
      "v_mfma_f32_16x16x32_bf16 v[148:151], v[232:235], v[198:201], 0\n"
      "ds_read_b128 v[152:155], v2 offset:31232\n"
      "v_cvt_pk_bf16_f32 v102, v178, v179\n"
      "v_mfma_f32_16x16x32_bf16 v[148:151], v[236:239], v[190:193], v[148:151]\n"
      "v_cvt_pk_bf16_f32 v103, v180, v181\n"
      "v_cndmask_b32 v210, v210, v140, s[60:61]\n"
      "v_mfma_f32_16x16x32_bf16 v[148:151], v[240:243], v[156:159], v[148:151]\n"
      "v_cvt_pk_bf16_f32 v221, v222, v223\n"
      "v_cvt_pk_bf16_f32 v183, v184, v185\n"
      "v_mfma_f32_16x16x32_bf16 v[126:129], v[32:35], v[100:103], 0\n"
      "ds_read_b128 v[198:201], v2 offset:34304\n"
      "v_cndmask_b32 v210, v210, v160, s[62:63]\n"
      "v_mfma_f32_16x16x32_bf16 v[126:129], v[32:35], v[88:91], v[126:129]\n"
      "ds_write_b64 v4, v[220:221] offset:256\n"
      "v_mfma_f32_16x16x32_bf16 v[220:223], v[130:133], v[100:103], 0\n"
      "v_exp_f32_e64 v210, v210\n"
      "v_mfma_f32_16x16x32_bf16 v[220:223], v[76:79], v[88:91], v[220:223]\n"
      "ds_read_b128 v[170:173], v7 offset:34816\n"
      "v_cvt_pk_bf16_f32 v210, v210, v210\n"
      "v_mfma_f32_16x16x32_bf16 v[190:193], v[214:217], v[100:103], 0\n"
      "ds_write_b16 v13, v210\n"
      "v_mfma_f32_16x16x32_bf16 v[190:193], v[80:83], v[88:91], v[190:193]\n"
      "s_waitcnt lgkmcnt(4)\n"
      "v_rcp_f32 v126, v126\n"
      "v_mfma_f32_16x16x32_bf16 v[140:143], v[194:197], v[100:103], 0\n"
      "buffer_store_dwordx4 v[122:125], v23, s[24:27], s46 offen sc1\n"
      "v_pk_mul_f32 v[220:221], v[220:221], v[126:127] op_sel_hi:[1,0]\n"
      "v_pk_fma_f32 v[110:111], v[118:119], v[118:119], v[110:111]\n"
      "v_pk_mul_f32 v[136:137], v[136:137], v[218:219] op_sel_hi:[1,0]\n"
      "v_pk_mul_f32 v[222:223], v[222:223], v[126:127] op_sel_hi:[1,0]\n"
      "v_pk_mul_f32 v[138:139], v[138:139], v[218:219] op_sel_hi:[1,0]\n"
      "v_pk_fma_f32 v[134:135], v[174:175], v[174:175], v[134:135]\n"
      "v_pk_mul_f32 v[190:191], v[190:191], v[126:127] op_sel_hi:[1,0]\n"
      "v_pk_fma_f32 v[134:135], v[176:177], v[176:177], v[134:135]\n"
      "v_pk_fma_f32 v[134:135], v[206:207], v[206:207], v[134:135]\n"
      "v_pk_mul_f32 v[192:193], v[192:193], v[126:127] op_sel_hi:[1,0]\n"
      "v_mfma_f32_16x16x32_bf16 v[140:143], v[84:87], v[88:91], v[140:143]\n"
      "ds_read_b128 v[156:159], v14\n"
      "v_fmac_f32 v135, v209, v209\n"
      "v_mfma_f32_16x16x32_bf16 v[202:205], a[228:231], v[106:109], v[202:205]\n"
      "ds_read_b128 v[210:213], v14 offset:64\n"
      "v_cvt_pk_bf16_f32 v220, v220, v221\n"
      "v_mfma_f32_16x16x32_bf16 v[202:205], a[232:235], v[152:155], v[202:205]\n"
      "s_waitcnt lgkmcnt(2)\n"
      "v_cvt_pk_bf16_f32 v221, v222, v223\n"
      "v_cvt_pk_bf16_f32 v222, v190, v191\n"
      "v_mfma_f32_16x16x32_bf16 v[202:205], a[236:239], v[198:201], v[202:205]\n"
      "v_cvt_pk_bf16_f32 v185, v138, v139\n"
      "v_cvt_pk_bf16_f32 v184, v136, v137\n"
      "v_mfma_f32_16x16x32_bf16 v[144:147], a[252:255], v[106:109], v[144:147]\n"
      "ds_write_b128 v3, v[182:185] offset:256\n"
      "v_mfma_f32_16x16x32_bf16 v[144:147], v[224:227], v[152:155], v[144:147]\n"
      "v_fmac_f32 v134, v208, v208\n"
      "v_add_f32 v134, v134, v135\n"
      "v_mfma_f32_16x16x32_bf16 v[144:147], v[228:231], v[198:201], v[144:147]\n"
      "s_add_u32 s42, s42, 1\n"
      "v_fma_f32 v136, v202, v202, s52\n"
      "v_mul_f32 v137, v203, v203\n"
      "v_mfma_f32_16x16x32_bf16 v[148:151], v[244:247], v[106:109], v[148:151]\n"
      "v_add_f32 v110, v110, v111\n"
      "v_cvt_pk_bf16_f32 v223, v192, v193\n"
      "v_mfma_f32_16x16x32_bf16 v[148:151], v[248:251], v[152:155], v[148:151]\n"
      "s_waitcnt lgkmcnt(1)\n"
      "v_permlane16_swap_b32_e64 v110, v134\n"
      "v_mfma_f32_16x16x32_bf16 v[148:151], v[252:255], v[198:201], v[148:151]\n"
      "ds_write_b128 v3, v[220:223] offset:512\n"
      "v_pk_mul_f32 v[140:141], v[140:141], v[126:127] op_sel_hi:[1,0]\n"
      "v_pk_fma_f32 v[136:137], v[204:205], v[204:205], v[136:137]\n"
      "v_pk_fma_f32 v[136:137], v[144:145], v[144:145], v[136:137]\n"
      "v_cvt_pk_bf16_f32 v140, v140, v141\n"
      "v_pk_fma_f32 v[136:137], v[146:147], v[146:147], v[136:137]\n"
      "v_pk_mul_f32 v[142:143], v[142:143], v[126:127] op_sel_hi:[1,0]\n"
      "v_add_f32_e64 v110, v110, v134\n"
      "v_pk_fma_f32 v[136:137], v[148:149], v[148:149], v[136:137]\n"
      "v_pk_fma_f32 v[136:137], v[150:151], v[150:151], v[136:137]\n"
      "v_cvt_pk_bf16_f32 v141, v142, v143\n"
      "v_add_f32 v136, v136, v137\n"
      "v_mov_b32 v134, v136\n"
      "v_mfma_f32_16x16x32_bf16 v[152:155], v[156:159], v[32:35], 0\n"
      "ds_write_b64 v4, v[140:141] offset:512\n"
      "v_permlane16_swap_b32 v136, v134\n"
      "v_add_f32 v136, v136, v134\n"
      "s_nop 1\n"
      "v_permlane32_swap_b32 v110, v136\n"
      "v_add_f32_e64 v110, v110, v136\n"
      "v_mfma_f32_16x16x32_bf16 v[152:155], v[210:213], v[32:35], v[152:155]\n"
      "ds_write_b32 v31, v110\n"
      "buffer_store_dwordx4 v[170:173], v25, s[24:27], s46 offen sc1\n"
      "v_add_u32 v12, 32, v12\n"
      "s_waitcnt lgkmcnt(0)\n"
      "s_barrier\n"
      "s_cbranch_vccz L_loop1_%=\n"
      "s_branch L_disp1_%=\n"
      "s_nop 7\n"
      "s_nop 7\n"
      "L_max1_%=:\n"
      "v_mfma_f32_16x16x32_bf16 v[218:221], v[156:159], v[130:133], 0\n"
      "ds_read_b128 v[98:101], v0 offset:18432\n"
      "s_add_u32 m0, s64, 0\n"
      "v_mfma_f32_16x16x32_bf16 v[218:221], v[210:213], v[76:79], v[218:221]\n"
      "ds_read_b128 v[102:105], v9\n"
      "v_readlane_b32 s45, v37, s43\n"
      "v_mfma_f32_16x16x32_bf16 v[134:137], v[156:159], v[214:217], 0\n"
      "buffer_load_dwordx4 v16, s[16:19], s84 offen  lds\n"
      "v_mfma_f32_16x16x32_bf16 v[134:137], v[210:213], v[80:83], v[134:137]\n"
      "ds_read_b128 v[140:143], v1 offset:18432\n"
      "ds_read_b128 v[190:193], v1 offset:24576\n"
      "v_mfma_f32_16x16x32_bf16 v[128:131], v[156:159], v[194:197], 0\n"
      "ds_read_b128 v[156:159], v0 offset:24576\n"
      "v_mfma_f32_16x16x32_bf16 v[128:131], v[210:213], v[84:87], v[128:131]\n"
      "ds_read_b128 v[178:181], v1 offset:30720\n"
      "s_waitcnt lgkmcnt(5)\n"
      "v_mfma_f32_16x16x32_bf16 v[198:201], a[0:3], v[98:101], 0\n"
      "s_add_u32 m0, s64, 4096\n"
      "v_mov_b32_dpp v152, v152 quad_perm:[0,0,0,0] row_mask:0xf bank_mask:0xf\n"
      "v_readlane_b32 s46, v37, s42\n"
      "v_mfma_f32_16x16x32_bf16 v[210:213], a[24:27], v[98:101], 0\n"
      "s_waitcnt lgkmcnt(4)\n"
      "v_rcp_f32_e64 v152, v152\n"
      "v_mfma_f32_16x16x32_bf16 v[170:173], a[48:51], v[98:101], 0\n"
      "ds_read_b128 v[220:223], v0 offset:30720\n"
      "v_add_f32 v104, v104, v105\n"
      "v_mfma_f32_16x16x32_bf16 v[182:185], a[72:75], v[98:101], 0\n"
      "s_waitcnt lgkmcnt(4)\n"
      "v_cndmask_b32 v218, v218, v134, s[58:59]\n"
      "v_cndmask_b32 v218, v218, v128, s[60:61]\n"
      "v_mfma_f32_16x16x32_bf16 v[106:109], a[96:99], v[98:101], 0\n"
      "buffer_load_dwordx4 v17, s[16:19], s84 offen  lds\n"
      "v_mfma_f32_16x16x32_bf16 v[160:163], a[120:123], v[98:101], 0\n"
      "s_add_u32 m0, s64, 8192\n"
      "v_add_f32 v102, v102, v103\n"
      "v_add_f32 v102, v102, v104\n"
      "v_mfma_f32_16x16x32_bf16 v[198:201], a[4:7], v[140:143], v[198:201]\n"
      "s_waitcnt lgkmcnt(1)\n"
      "v_rsq_f32 v102, v102\n"
      "v_mfma_f32_16x16x32_bf16 v[198:201], a[8:11], v[156:159], v[198:201]\n"
      "v_mul_f32 v102, 0x415db3d7, v102\n"
      "v_mov_b32 v104, v102\n"
      "v_mfma_f32_16x16x32_bf16 v[210:213], a[28:31], v[140:143], v[210:213]\n"
      "s_nop 0\n"
      "v_permlane32_swap_b32 v102, v104\n"
      "v_mfma_f32_16x16x32_bf16 v[210:213], a[32:35], v[156:159], v[210:213]\n"
      "v_mul_f32 v150, v104, v150\n"
      "v_mov_b32 v103, v102\n"
      "v_mfma_f32_16x16x32_bf16 v[170:173], a[52:55], v[140:143], v[170:173]\n"
      "s_nop 0\n"
      "v_permlane16_swap_b32_e64 v102, v103\n"
      "v_mfma_f32_16x16x32_bf16 v[170:173], a[56:59], v[156:159], v[170:173]\n"
      "buffer_load_dwordx4 v18, s[16:19], s84 offen  lds\n"
      "v_mfma_f32_16x16x32_bf16 v[182:185], a[76:79], v[140:143], v[182:185]\n"
      "s_add_u32 m0, s64, 12288\n"
      "v_mul_f32 v167, v102, v167\n"
      "v_mul_f32 v168, v102, v168\n"
      "v_mfma_f32_16x16x32_bf16 v[182:185], a[80:83], v[156:159], v[182:185]\n"
      "v_mul_f32 v112, v102, v112\n"
      "v_mul_f32 v118, v102, v118\n"
      "v_mfma_f32_16x16x32_bf16 v[106:109], a[100:103], v[140:143], v[106:109]\n"
      "v_mul_f32 v175, v103, v175\n"
      "v_mul_f32 v205, v104, v205\n"
      "v_mfma_f32_16x16x32_bf16 v[106:109], a[104:107], v[156:159], v[106:109]\n"
      "v_mul_f32 v166, v102, v166\n"
      "v_mul_f32 v114, v102, v114\n"
      "v_mfma_f32_16x16x32_bf16 v[160:163], a[124:127], v[140:143], v[160:163]\n"
      "buffer_load_dwordx4 v19, s[16:19], s84 offen  lds\n"
      "v_mfma_f32_16x16x32_bf16 v[160:163], a[128:131], v[156:159], v[160:163]\n"
      "s_waitcnt lgkmcnt(0)\n"
      "v_mul_f32 v169, v102, v169\n"
      "v_cvt_pk_bf16_f32 v166, v166, v167\n"
      "v_mfma_f32_16x16x32_bf16 v[198:201], a[12:15], v[190:193], v[198:201]\n"
      "ds_read_b128 v[126:129], v1 offset:20480\n"
      "v_cvt_pk_bf16_f32 v167, v168, v169\n"
      "v_mfma_f32_16x16x32_bf16 v[198:201], a[16:19], v[220:223], v[198:201]\n"
      "v_mul_f32 v113, v102, v113\n"
      "v_mul_f32 v151, v104, v151\n"
      "v_mfma_f32_16x16x32_bf16 v[198:201], a[20:23], v[178:181], v[198:201]\n"
      "s_add_u32 m0, s65, 16384\n"
      "v_mul_f32 v115, v102, v115\n"
      "v_mul_f32 v208, v103, v208\n"
      "v_mfma_f32_16x16x32_bf16 v[210:213], a[36:39], v[190:193], v[210:213]\n"
      "buffer_load_dwordx4 v15, s[16:19], s84 offen  lds\n"
      "v_mfma_f32_16x16x32_bf16 v[210:213], a[40:43], v[220:223], v[210:213]\n"
      "v_cvt_pk_bf16_f32 v168, v112, v113\n"
      "v_cvt_pk_bf16_f32 v169, v114, v115\n"
      "v_mfma_f32_16x16x32_bf16 v[210:213], a[44:47], v[178:181], v[210:213]\n"
      "ds_write_b128 v5, v[166:169] offset:18432\n"
      "v_pk_mul_f32 v[206:207], v[206:207], v[102:103] op_sel:[0,1]\n"
      "v_pk_mul_f32 v[186:187], v[186:187], v[102:103] op_sel:[0,1]\n"
      "v_pk_mul_f32 v[188:189], v[188:189], v[102:103] op_sel:[0,1]\n"
      "v_pk_mul_f32 v[116:117], v[116:117], v[102:103] op_sel_hi:[1,0]\n"
      "v_mfma_f32_16x16x32_bf16 v[170:173], a[60:63], v[190:193], v[170:173]\n"
      "buffer_load_dwordx4 v[134:137], v36, s[28:31], s86 offen\n"
      "v_mfma_f32_16x16x32_bf16 v[170:173], a[64:67], v[220:223], v[170:173]\n"
      "v_cvt_pk_bf16_f32 v186, v186, v187\n"
      "v_mul_f32 v203, v104, v203\n"
      "v_mfma_f32_16x16x32_bf16 v[170:173], a[68:71], v[178:181], v[170:173]\n"
      "v_mul_f32 v174, v103, v174\n"
      "v_cvt_pk_bf16_f32 v206, v206, v207\n"
      "v_mfma_f32_16x16x32_bf16 v[182:185], a[84:87], v[190:193], v[182:185]\n"
      "v_mul_f32 v177, v103, v177\n"
      "v_cvt_pk_bf16_f32 v116, v116, v117\n"
      "v_mfma_f32_16x16x32_bf16 v[182:185], a[88:91], v[220:223], v[182:185]\n"
      "v_mul_f32 v119, v102, v119\n"
      "v_mul_f32_e64 v149, v104, v149\n"
      "v_mfma_f32_16x16x32_bf16 v[182:185], a[92:95], v[178:181], v[182:185]\n"
      "v_mul_f32 v176, v103, v176\n"
      "v_mul_f32 v209, v103, v209\n"
      "v_mfma_f32_16x16x32_bf16 v[106:109], a[108:111], v[190:193], v[106:109]\n"
      "v_cvt_pk_bf16_f32 v207, v208, v209\n"
      "v_cvt_pk_bf16_f32 v117, v118, v119\n"
      "v_mfma_f32_16x16x32_bf16 v[106:109], a[112:115], v[220:223], v[106:109]\n"
      "ds_read_b128 v[122:125], v0 offset:20480\n"
      "v_cvt_pk_bf16_f32 v187, v188, v189\n"
      "v_mfma_f32_16x16x32_bf16 v[106:109], a[116:119], v[178:181], v[106:109]\n"
      "ds_write_b64 v6, v[116:117] offset:18432\n"
      "v_mfma_f32_16x16x32_bf16 v[160:163], a[132:135], v[190:193], v[160:163]\n"
      "v_cvt_pk_bf16_f32 v189, v176, v177\n"
      "v_cvt_pk_bf16_f32 v188, v174, v175\n"
      "v_mfma_f32_16x16x32_bf16 v[160:163], a[136:139], v[220:223], v[160:163]\n"
      "buffer_load_dword v139, v29, s[28:31], s86 offen\n"
      "v_mfma_f32_16x16x32_bf16 v[160:163], a[140:143], v[178:181], v[160:163]\n"
      "ds_read_b128 v[118:121], v1 offset:26624\n"
      "v_mul_f32_e64 v146, v104, v146\n"
      "v_mfma_f32_16x16x32_bf16 v[130:133], v[98:101], a[144:147], 0\n"
      "ds_write_b128 v5, v[186:189] offset:24576\n"
      "v_mfma_f32_16x16x32_bf16 v[130:133], v[140:143], a[148:151], v[130:133]\n"
      "v_mul_f32 v202, v104, v202\n"
      "v_fma_f32 v78, v198, v198, s51\n"
      "v_mfma_f32_16x16x32_bf16 v[130:133], v[156:159], a[152:155], v[130:133]\n"
      "v_mul_f32 v144, v104, v144\n"
      "v_mul_f32 v218, v152, v218\n"
      "v_mfma_f32_16x16x32_bf16 v[130:133], v[190:193], a[156:159], v[130:133]\n"
      "v_mul_f32 v148, v104, v148\n"
      "v_fmac_f32 v78, v199, v199\n"
      "v_mfma_f32_16x16x32_bf16 v[130:133], v[220:223], a[160:163], v[130:133]\n"
      "s_or_b32 s95, s45, s46\n"
      "v_fma_f32 v103, v182, v182, s51\n"
      "v_fmac_f32 v103, v183, v183\n"
      "v_mfma_f32_16x16x32_bf16 v[130:133], v[178:181], a[164:167], v[130:133]\n"
      "ds_read_b128 v[166:169], v1 offset:32768\n"
      "v_fmac_f32 v103, v184, v184\n"
      "v_mfma_f32_16x16x32_bf16 v[214:217], v[98:101], a[168:171], 0\n"
      "ds_write_b64 v6, v[206:207] offset:24576\n"
      "v_mfma_f32_16x16x32_bf16 v[214:217], v[140:143], a[172:175], v[214:217]\n"
      "ds_read_b128 v[186:189], v9 offset:4608\n"
      "v_cvt_pk_bf16_f32 v218, v218, v218\n"
      "v_mfma_f32_16x16x32_bf16 v[214:217], v[156:159], a[176:179], v[214:217]\n"
      "ds_read_b128 v[110:113], v1 offset:34816\n"
      "v_cvt_pk_bf16_f32 v202, v202, v203\n"
      "v_mfma_f32_16x16x32_bf16 v[214:217], v[190:193], a[180:183], v[214:217]\n"
      "ds_write_b16 v12, v218\n"
      "v_mfma_f32_16x16x32_bf16 v[214:217], v[220:223], a[184:187], v[214:217]\n"
      "ds_read_b128 v[174:177], v0 offset:32768\n"
      "v_fmac_f32 v103, v185, v185\n"
      "v_mfma_f32_16x16x32_bf16 v[214:217], v[178:181], a[188:191], v[214:217]\n"
      "ds_read_b128 v[206:209], v0 offset:26624\n"
      "v_fmac_f32 v78, v200, v200\n"
      "v_mfma_f32_16x16x32_bf16 v[194:197], v[98:101], a[192:195], 0\n"
      "v_cvt_pk_bf16_f32 v148, v148, v149\n"
      "v_fmac_f32 v103, v106, v106\n"
      "v_mfma_f32_16x16x32_bf16 v[194:197], v[140:143], a[196:199], v[194:197]\n"
      "v_fmac_f32 v78, v201, v201\n"
      "v_fmac_f32_e64 v103, v107, v107\n"
      "v_mfma_f32_16x16x32_bf16 v[194:197], v[156:159], a[200:203], v[194:197]\n"
      "ds_read_b128 v[96:99], v1 offset:22528\n"
      "v_cvt_pk_bf16_f32 v214, v214, v215\n"
      "v_mfma_f32_16x16x32_bf16 v[194:197], v[190:193], a[204:207], v[194:197]\n"
      "v_fmac_f32 v78, v210, v210\n"
      "v_fmac_f32 v103, v108, v108\n"
      "v_mfma_f32_16x16x32_bf16 v[194:197], v[220:223], a[208:211], v[194:197]\n"
      "v_fmac_f32_e64 v78, v211, v211\n"
      "v_cvt_pk_bf16_f32 v149, v150, v151\n"
      "v_mfma_f32_16x16x32_bf16 v[194:197], v[178:181], a[212:215], v[194:197]\n"
      "s_waitcnt lgkmcnt(2)\n"
      "v_fmac_f32 v78, v212, v212\n"
      "v_or3_b32 v186, v186, v187, v188\n"
      "v_mfma_f32_16x16x32_bf16 v[152:155], a[0:3], v[122:125], 0\n"
      "v_fmac_f32 v103, v109, v109\n"
      "v_fmac_f32 v78, v213, v213\n"
      "v_mfma_f32_16x16x32_bf16 v[152:155], a[4:7], v[126:129], v[152:155]\n"
      "s_waitcnt lgkmcnt(1)\n"
      "v_fmac_f32 v78, v170, v170\n"
      "v_cvt_pk_bf16_f32 v130, v130, v131\n"
      "v_mfma_f32_16x16x32_bf16 v[152:155], a[8:11], v[206:209], v[152:155]\n"
      "ds_read_b128 v[140:143], v1 offset:28672\n"
      "v_fmac_f32_e64 v103, v160, v160\n"
      "v_mfma_f32_16x16x32_bf16 v[152:155], a[12:15], v[118:121], v[152:155]\n"
      "ds_write_b64 v6, v[148:149] offset:30720\n"
      "v_mfma_f32_16x16x32_bf16 v[152:155], a[16:19], v[174:177], v[152:155]\n"
      "v_readlane_b32 s47, v38, s43\n"
      "v_mul_f32_e64 v204, v104, v204\n"
      "v_mfma_f32_16x16x32_bf16 v[152:155], a[20:23], v[166:169], v[152:155]\n"
      "v_fmac_f32 v78, v171, v171\n"
      "v_fmac_f32 v78, v172, v172\n"
      "v_mfma_f32_16x16x32_bf16 v[114:117], a[24:27], v[122:125], 0\n"
      "s_or_b32 s95, s95, s48\n"
      "v_fmac_f32 v103, v161, v161\n"
      "v_fmac_f32 v103, v162, v162\n"
      "v_mfma_f32_16x16x32_bf16 v[114:117], a[28:31], v[126:129], v[114:117]\n"
      "v_fmac_f32 v103, v163, v163\n"
      "v_fmac_f32_e64 v78, v173, v173\n"
      "v_mfma_f32_16x16x32_bf16 v[114:117], a[32:35], v[206:209], v[114:117]\n"
      "v_fma_f32 v85, v152, v152, s51\n"
      "v_cvt_pk_bf16_f32 v194, v194, v195\n"
      "v_mfma_f32_16x16x32_bf16 v[114:117], a[36:39], v[118:121], v[114:117]\n"
      "v_permlane16_swap_b32_e64 v78, v103\n"
      "v_mfma_f32_16x16x32_bf16 v[114:117], a[40:43], v[174:177], v[114:117]\n"
      "v_mul_f32 v145, v104, v145\n"
      "v_add_f32 v78, v78, v103\n"
      "v_mfma_f32_16x16x32_bf16 v[114:117], a[44:47], v[166:169], v[114:117]\n"
      "v_cvt_pk_bf16_f32 v203, v204, v205\n"
      "v_bitop3_b32 v186, v186, v189, s33 bitop3:0xa8\n"
      "v_mfma_f32_16x16x32_bf16 v[156:159], a[48:51], v[122:125], 0\n"
      "v_mov_b32 v103, v78\n"
      "v_fmac_f32 v85, v153, v153\n"
      "v_mfma_f32_16x16x32_bf16 v[156:159], a[52:55], v[126:129], v[156:159]\n"
      "v_permlane32_swap_b32 v78, v103\n"
      "v_mfma_f32_16x16x32_bf16 v[156:159], a[56:59], v[206:209], v[156:159]\n"
      "v_fmac_f32 v85, v154, v154\n"
      "v_cvt_pk_bf16_f32 v195, v196, v197\n"
      "v_mfma_f32_16x16x32_bf16 v[156:159], a[60:63], v[118:121], v[156:159]\n"
      "v_add_f32 v78, v78, v103\n"
      "v_mul_f32 v147, v104, v147\n"
      "v_mfma_f32_16x16x32_bf16 v[156:159], a[64:67], v[174:177], v[156:159]\n"
      "v_rsq_f32 v78, v78\n"
      "v_mfma_f32_16x16x32_bf16 v[156:159], a[68:71], v[166:169], v[156:159]\n"
      "v_mov_b32 v103, v78\n"
      "v_cvt_pk_bf16_f32 v204, v144, v145\n"
      "v_mfma_f32_16x16x32_bf16 v[148:151], a[72:75], v[122:125], 0\n"
      "v_fmac_f32_e64 v85, v155, v155\n"
      "v_cmp_ne_u32 s[56:57], 0, v186\n"
      "v_mfma_f32_16x16x32_bf16 v[148:151], a[76:79], v[126:129], v[148:151]\n"
      "v_cvt_pk_bf16_f32 v215, v216, v217\n"
      "v_cvt_pk_bf16_f32 v205, v146, v147\n"
      "v_mfma_f32_16x16x32_bf16 v[148:151], a[80:83], v[206:209], v[148:151]\n"
      "v_permlane16_swap_b32 v78, v103\n"
      "v_mfma_f32_16x16x32_bf16 v[148:151], a[84:87], v[118:121], v[148:151]\n"
      "v_mul_f32 v163, v103, v163\n"
      "v_mul_f32_e64 v161, v103, v161\n"
      "v_mfma_f32_16x16x32_bf16 v[148:151], a[88:91], v[174:177], v[148:151]\n"
      "ds_write_b128 v5, v[202:205] offset:30720\n"
      "v_mfma_f32_16x16x32_bf16 v[148:151], a[92:95], v[166:169], v[148:151]\n"
      "v_mul_f32 v106, v103, v106\n"
      "v_fmac_f32 v85, v114, v114\n"
      "v_mfma_f32_16x16x32_bf16 v[220:223], a[96:99], v[122:125], 0\n"
      "ds_read_b128 v[186:189], v0 offset:28672\n"
      "v_mul_f32 v78, s50, v78\n"
      "v_mfma_f32_16x16x32_bf16 v[220:223], a[100:103], v[126:129], v[220:223]\n"
      "v_mul_f32 v160, v103, v160\n"
      "v_mul_f32_e64 v183, v103, v183\n"
      "v_mfma_f32_16x16x32_bf16 v[220:223], a[104:107], v[206:209], v[220:223]\n"
      "v_mul_f32 v109, v103, v109\n"
      "v_mul_f32 v210, v78, v210\n"
      "v_mfma_f32_16x16x32_bf16 v[220:223], a[108:111], v[118:121], v[220:223]\n"
      "v_cvt_pk_bf16_f32 v131, v132, v133\n"
      "v_fmac_f32 v85, v115, v115\n"
      "v_mfma_f32_16x16x32_bf16 v[220:223], a[112:115], v[174:177], v[220:223]\n"
      "v_mul_f32 v184, v103, v184\n"
      "v_fma_f32 v218, v148, v148, s51\n"
      "v_mfma_f32_16x16x32_bf16 v[220:223], a[116:119], v[166:169], v[220:223]\n"
      "v_fmac_f32 v85, v116, v116\n"
      "v_mul_f32 v171, v78, v171\n"
      "v_mfma_f32_16x16x32_bf16 v[202:205], a[120:123], v[122:125], 0\n"
      "v_mul_f32 v182, v103, v182\n"
      "v_mul_f32 v170, v78, v170\n"
      "v_mfma_f32_16x16x32_bf16 v[202:205], a[124:127], v[126:129], v[202:205]\n"
      "v_mul_f32 v108, v103, v108\n"
      "v_fmac_f32 v218, v149, v149\n"
      "v_mfma_f32_16x16x32_bf16 v[202:205], a[128:131], v[206:209], v[202:205]\n"
      "v_cvt_pk_bf16_f32 v170, v170, v171\n"
      "v_cvt_pk_bf16_f32 v160, v160, v161\n"
      "v_mfma_f32_16x16x32_bf16 v[202:205], a[132:135], v[118:121], v[202:205]\n"
      "v_fmac_f32 v218, v150, v150\n"
      "v_mul_f32 v198, v78, v198\n"
      "v_mfma_f32_16x16x32_bf16 v[202:205], a[136:139], v[174:177], v[202:205]\n"
      "ds_read_b128 v[178:181], v0 offset:22528\n"
      "v_fmac_f32 v85, v117, v117\n"
      "v_mfma_f32_16x16x32_bf16 v[202:205], a[140:143], v[166:169], v[202:205]\n"
      "v_fmac_f32 v218, v151, v151\n"
      "v_fmac_f32 v218, v220, v220\n"
      "v_mfma_f32_16x16x32_bf16 v[144:147], v[122:125], a[144:147], 0\n"
      "v_fmac_f32 v218, v221, v221\n"
      "v_mul_f32 v200, v78, v200\n"
      "v_mfma_f32_16x16x32_bf16 v[144:147], v[126:129], a[148:151], v[144:147]\n"
      "v_mul_f32 v162, v103, v162\n"
      "v_fmac_f32 v85, v156, v156\n"
      "v_mfma_f32_16x16x32_bf16 v[144:147], v[206:209], a[152:155], v[144:147]\n"
      "v_mul_f32 v213, v78, v213\n"
      "v_mul_f32 v212, v78, v212\n"
      "v_mfma_f32_16x16x32_bf16 v[144:147], v[118:121], a[156:159], v[144:147]\n"
      "v_cvt_pk_bf16_f32 v182, v182, v183\n"
      "v_mul_f32 v201, v78, v201\n"
      "v_mfma_f32_16x16x32_bf16 v[144:147], v[174:177], a[160:163], v[144:147]\n"
      "v_mul_f32 v107, v103, v107\n"
      "v_cvt_pk_bf16_f32 v161, v162, v163\n"
      "v_mfma_f32_16x16x32_bf16 v[144:147], v[166:169], a[164:167], v[144:147]\n"
      "v_fmac_f32 v85, v157, v157\n"
      "v_fmac_f32_e64 v218, v222, v222\n"
      "v_mfma_f32_16x16x32_bf16 v[162:165], v[122:125], a[168:171], 0\n"
      "v_mul_f32 v199, v78, v199\n"
      "v_fmac_f32 v218, v223, v223\n"
      "v_mfma_f32_16x16x32_bf16 v[162:165], v[126:129], a[172:175], v[162:165]\n"
      "s_cmp_ge_u32 s95, 0x40000000\n"
      "s_cselect_b32 s94, -1, -2\n"
      "v_fmac_f32 v218, v202, v202\n"
      "v_mfma_f32_16x16x32_bf16 v[162:165], v[206:209], a[176:179], v[162:165]\n"
      "v_mul_f32 v211, v78, v211\n"
      "v_mul_f32 v173, v78, v173\n"
      "v_mfma_f32_16x16x32_bf16 v[162:165], v[118:121], a[180:183], v[162:165]\n"
      "v_mul_f32 v185, v103, v185\n"
      "v_fmac_f32 v218, v203, v203\n"
      "v_mfma_f32_16x16x32_bf16 v[162:165], v[174:177], a[184:187], v[162:165]\n"
      "ds_read_b128 v[190:193], v0 offset:34816\n"
      "v_fmac_f32 v218, v204, v204\n"
      "v_mfma_f32_16x16x32_bf16 v[162:165], v[166:169], a[188:191], v[162:165]\n"
      "v_cvt_pk_bf16_f32 v198, v198, v199\n"
      "v_fmac_f32 v218, v205, v205\n"
      "v_mfma_f32_16x16x32_bf16 v[100:103], v[122:125], a[192:195], 0\n"
      "s_cmp_gt_u32 s42, s44\n"
      "s_cselect_b32 s95, s94, s95\n"
      "v_fmac_f32 v85, v158, v158\n"
      "v_mfma_f32_16x16x32_bf16 v[100:103], v[126:129], a[196:199], v[100:103]\n"
      "v_cvt_pk_bf16_f32 v133, v146, v147\n"
      "v_fmac_f32_e64 v85, v159, v159\n"
      "v_mfma_f32_16x16x32_bf16 v[100:103], v[206:209], a[200:203], v[100:103]\n"
      "ds_read_b128 v[206:209], v7 offset:8192\n"
      "v_mul_f32_e64 v172, v78, v172\n"
      "v_mfma_f32_16x16x32_bf16 v[100:103], v[118:121], a[204:207], v[100:103]\n"
      "ds_read_b128 v[126:129], v7 offset:4096\n"
      "v_cvt_pk_bf16_f32 v217, v164, v165\n"
      "v_mfma_f32_16x16x32_bf16 v[100:103], v[174:177], a[208:211], v[100:103]\n"
      "v_permlane16_swap_b32 v85, v218\n"
      "v_mfma_f32_16x16x32_bf16 v[100:103], v[166:169], a[212:215], v[100:103]\n"
      "s_waitcnt lgkmcnt(2)\n"
      "v_add_f32 v85, v85, v218\n"
      "v_mov_b32 v218, v85\n"
      "v_mfma_f32_16x16x32_bf16 v[122:125], a[0:3], v[178:181], 0\n"
      "s_nop 0\n"
      "v_permlane32_swap_b32 v85, v218\n"
      "v_mfma_f32_16x16x32_bf16 v[122:125], a[4:7], v[96:99], v[122:125]\n"
      "v_cvt_pk_bf16_f32 v171, v172, v173\n"
      "v_add_f32 v85, v85, v218\n"
      "v_mfma_f32_16x16x32_bf16 v[122:125], a[8:11], v[186:189], v[122:125]\n"
      "v_rsq_f32 v85, v85\n"
      "v_mfma_f32_16x16x32_bf16 v[122:125], a[12:15], v[140:143], v[122:125]\n"
      "v_mov_b32 v218, v85\n"
      "v_cvt_pk_bf16_f32 v183, v184, v185\n"
      "v_mfma_f32_16x16x32_bf16 v[122:125], a[16:19], v[190:193], v[122:125]\n"
      "v_permlane16_swap_b32 v85, v218\n"
      "v_mfma_f32_16x16x32_bf16 v[122:125], a[20:23], v[110:113], v[122:125]\n"
      "v_mul_f32 v221, v218, v221\n"
      "v_mul_f32 v85, s50, v85\n"
      "v_mfma_f32_16x16x32_bf16 v[174:177], a[24:27], v[178:181], 0\n"
      "v_cvt_pk_bf16_f32 v199, v200, v201\n"
      "v_mul_f32_e64 v151, v218, v151\n"
      "v_mfma_f32_16x16x32_bf16 v[174:177], a[28:31], v[96:99], v[174:177]\n"
      "v_mul_f32 v205, v218, v205\n"
      "v_mul_f32 v155, v85, v155\n"
      "v_mfma_f32_16x16x32_bf16 v[174:177], a[32:35], v[186:189], v[174:177]\n"
      "v_cvt_pk_bf16_f32 v216, v162, v163\n"
      "v_cvt_pk_bf16_f32 v132, v144, v145\n"
      "v_mfma_f32_16x16x32_bf16 v[174:177], a[36:39], v[140:143], v[174:177]\n"
      "ds_read_b128 v[166:169], v7\n"
      "v_cvt_pk_bf16_f32 v197, v102, v103\n"
      "v_mfma_f32_16x16x32_bf16 v[174:177], a[40:43], v[190:193], v[174:177]\n"
      "v_mul_f32_e64 v153, v85, v153\n"
      "v_cvt_pk_bf16_f32 v201, v212, v213\n"
      "v_mfma_f32_16x16x32_bf16 v[174:177], a[44:47], v[110:113], v[174:177]\n"
      "v_cvt_pk_bf16_f32 v200, v210, v211\n"
      "v_mul_f32 v149, v218, v149\n"
      "v_mfma_f32_16x16x32_bf16 v[144:147], a[48:51], v[178:181], 0\n"
      "v_mul_f32 v152, v85, v152\n"
      "v_cvt_pk_bf16_f32 v196, v100, v101\n"
      "v_mfma_f32_16x16x32_bf16 v[144:147], a[52:55], v[96:99], v[144:147]\n"
      "v_fma_f32 v172, v122, v122, s51\n"
      "v_fmac_f32 v172, v123, v123\n"
      "v_mfma_f32_16x16x32_bf16 v[144:147], a[56:59], v[186:189], v[144:147]\n"
      "v_fmac_f32 v172, v124, v124\n"
      "v_mul_f32 v223, v218, v223\n"
      "v_mfma_f32_16x16x32_bf16 v[144:147], a[60:63], v[140:143], v[144:147]\n"
      "v_mul_f32 v150, v218, v150\n"
      "v_mul_f32_e64 v202, v218, v202\n"
      "v_mfma_f32_16x16x32_bf16 v[144:147], a[64:67], v[190:193], v[144:147]\n"
      "v_cvt_pk_bf16_f32 v185, v108, v109\n"
      "v_fmac_f32 v172, v125, v125\n"
      "v_mfma_f32_16x16x32_bf16 v[144:147], a[68:71], v[110:113], v[144:147]\n"
      "s_waitcnt lgkmcnt(0)\n"
      "v_fmac_f32 v172, v174, v174\n"
      "v_fmac_f32 v172, v175, v175\n"
      "v_mfma_f32_16x16x32_bf16 v[162:165], a[72:75], v[178:181], 0\n"
      "ds_read_b128 v[100:103], v7 offset:12288\n"
      "v_mul_f32_e64 v116, v85, v116\n"
      "v_mfma_f32_16x16x32_bf16 v[162:165], a[76:79], v[96:99], v[162:165]\n"
      "buffer_store_dwordx4 v[166:169], v20, s[24:27], s85 offen sc1\n"
      "v_mfma_f32_16x16x32_bf16 v[162:165], a[80:83], v[186:189], v[162:165]\n"
      "v_mul_f32_e64 v222, v218, v222\n"
      "v_cvt_pk_bf16_f32 v152, v152, v153\n"
      "v_mfma_f32_16x16x32_bf16 v[162:165], a[84:87], v[140:143], v[162:165]\n"
      "v_mul_f32 v203, v218, v203\n"
      "v_mul_f32 v220, v218, v220\n"
      "v_mfma_f32_16x16x32_bf16 v[162:165], a[88:91], v[190:193], v[162:165]\n"
      "v_mul_f32 v148, v218, v148\n"
      "v_fmac_f32 v172, v176, v176\n"
      "v_mfma_f32_16x16x32_bf16 v[162:165], a[92:95], v[110:113], v[162:165]\n"
      "v_fmac_f32 v172, v177, v177\n"
      "v_mul_f32 v115, v85, v115\n"
      "v_mfma_f32_16x16x32_bf16 v[118:121], a[96:99], v[178:181], 0\n"
      "ds_read_u16 v78, v11 offset:1168\n"
      "v_cvt_pk_bf16_f32 v148, v148, v149\n"
      "v_mfma_f32_16x16x32_bf16 v[118:121], a[100:103], v[96:99], v[118:121]\n"
      "s_cmp_ge_u32 s95, 0x40000000\n"
      "s_cselect_b64 vcc, -1, s[56:57]\n"
      "v_mul_f32_e64 v114, v85, v114\n"
      "v_mfma_f32_16x16x32_bf16 v[118:121], a[104:107], v[186:189], v[118:121]\n"
      "v_cvt_pk_bf16_f32 v184, v106, v107\n"
      "v_fmac_f32_e64 v172, v144, v144\n"
      "v_mfma_f32_16x16x32_bf16 v[118:121], a[108:111], v[140:143], v[118:121]\n"
      "v_fma_f32 v104, v162, v162, s51\n"
      "v_mul_f32_e64 v204, v218, v204\n"
      "v_mfma_f32_16x16x32_bf16 v[118:121], a[112:115], v[190:193], v[118:121]\n"
      "buffer_store_dwordx4 v[126:129], v21, s[24:27], s85 offen sc1\n"
      "v_mfma_f32_16x16x32_bf16 v[118:121], a[116:119], v[110:113], v[118:121]\n"
      "v_cvt_pk_bf16_f32 v149, v150, v151\n"
      "v_cvt_pk_bf16_f32 v202, v202, v203\n"
      "v_mfma_f32_16x16x32_bf16 v[106:109], a[120:123], v[178:181], 0\n"
      "v_cvt_pk_bf16_f32 v203, v204, v205\n"
      "v_fmac_f32 v172, v145, v145\n"
      "v_mfma_f32_16x16x32_bf16 v[106:109], a[124:127], v[96:99], v[106:109]\n"
      "v_fmac_f32 v104, v163, v163\n"
      "v_cvt_pk_bf16_f32 v150, v220, v221\n"
      "v_mfma_f32_16x16x32_bf16 v[106:109], a[128:131], v[186:189], v[106:109]\n"
      "v_fmac_f32 v104, v164, v164\n"
      "v_fmac_f32 v104, v165, v165\n"
      "v_mfma_f32_16x16x32_bf16 v[106:109], a[132:135], v[140:143], v[106:109]\n"
      "v_fmac_f32 v104, v118, v118\n"
      "v_fmac_f32 v104, v119, v119\n"
      "v_mfma_f32_16x16x32_bf16 v[106:109], a[136:139], v[190:193], v[106:109]\n"
      "v_fmac_f32 v172, v146, v146\n"
      "v_mul_f32 v154, v85, v154\n"
      "v_mfma_f32_16x16x32_bf16 v[106:109], a[140:143], v[110:113], v[106:109]\n"
      "v_mul_f32 v117, v85, v117\n"
      "v_fmac_f32 v172, v147, v147\n"
      "v_mfma_f32_16x16x32_bf16 v[218:221], v[178:181], a[144:147], 0\n"
      "buffer_store_dwordx4 v[206:209], v22, s[24:27], s85 offen sc1\n"
      "v_mfma_f32_16x16x32_bf16 v[218:221], v[96:99], a[148:151], v[218:221]\n"
      "v_fmac_f32 v104, v120, v120\n"
      "v_fmac_f32 v104, v121, v121\n"
      "v_mfma_f32_16x16x32_bf16 v[218:221], v[186:189], a[152:155], v[218:221]\n"
      "v_cvt_pk_bf16_f32 v153, v154, v155\n"
      "v_fmac_f32 v104, v106, v106\n"
      "v_mfma_f32_16x16x32_bf16 v[218:221], v[140:143], a[156:159], v[218:221]\n"
      "v_fmac_f32 v104, v107, v107\n"
      "v_cvt_pk_bf16_f32 v154, v114, v115\n"
      "v_mfma_f32_16x16x32_bf16 v[218:221], v[190:193], a[160:163], v[218:221]\n"
      "ds_read_b128 v[166:169], v7 offset:16384\n"
      "v_fmac_f32_e64 v104, v108, v108\n"
      "v_mfma_f32_16x16x32_bf16 v[218:221], v[110:113], a[164:167], v[218:221]\n"
      "ds_read_b128 v[210:213], v2 offset:3072\n"
      "v_fmac_f32 v104, v109, v109\n"
      "v_mfma_f32_16x16x32_bf16 v[126:129], v[178:181], a[168:171], 0\n"
      "s_nop 0\n"
      "v_permlane16_swap_b32 v172, v104\n"
      "v_mfma_f32_16x16x32_bf16 v[126:129], v[96:99], a[172:175], v[126:129]\n"
      "s_waitcnt lgkmcnt(2)\n"
      "v_add_f32 v172, v172, v104\n"
      "v_mov_b32 v104, v172\n"
      "v_mfma_f32_16x16x32_bf16 v[126:129], v[186:189], a[176:179], v[126:129]\n"
      "buffer_store_dwordx4 v[100:103], v23, s[24:27], s85 offen sc1\n"
      "v_mfma_f32_16x16x32_bf16 v[126:129], v[140:143], a[180:183], v[126:129]\n"
      "v_permlane32_swap_b32_e64 v172, v104\n"
      "v_mfma_f32_16x16x32_bf16 v[126:129], v[190:193], a[184:187], v[126:129]\n"
      "ds_read_b128 v[206:209], v2 offset:6144\n"
      "v_add_f32 v172, v172, v104\n"
      "v_mfma_f32_16x16x32_bf16 v[126:129], v[110:113], a[188:191], v[126:129]\n"
      "v_rsq_f32 v172, v172\n"
      "v_mfma_f32_16x16x32_bf16 v[100:103], v[178:181], a[192:195], 0\n"
      "ds_read_b128 v[178:181], v2 offset:15360\n"
      "v_mov_b32_e64 v104, v172\n"
      "v_mfma_f32_16x16x32_bf16 v[100:103], v[96:99], a[196:199], v[100:103]\n"
      "ds_read_b128 v[96:99], v2\n"
      "v_cvt_pk_bf16_f32 v155, v116, v117\n"
      "v_mfma_f32_16x16x32_bf16 v[100:103], v[186:189], a[200:203], v[100:103]\n"
      "s_waitcnt lgkmcnt(3)\n"
      "v_permlane16_swap_b32 v172, v104\n"
      "v_mfma_f32_16x16x32_bf16 v[100:103], v[140:143], a[204:207], v[100:103]\n"
      "ds_read_u16 v82, v11 offset:1200\n"
      "v_mul_f32_e64 v172, s50, v172\n"
      "v_mfma_f32_16x16x32_bf16 v[100:103], v[190:193], a[208:211], v[100:103]\n"
      "buffer_store_dwordx4 v[166:169], v25, s[24:27], s85 offen sc1\n"
      "v_pk_mul_f32 v[158:159], v[158:159], v[84:85] op_sel:[0,1]\n"
      "v_pk_mul_f32 v[162:163], v[162:163], v[104:105] op_sel_hi:[1,0]\n"
      "v_pk_mul_f32 v[118:119], v[118:119], v[104:105] op_sel_hi:[1,0]\n"
      "v_pk_mul_f32 v[144:145], v[144:145], v[172:173] op_sel_hi:[1,0]\n"
      "v_pk_mul_f32 v[122:123], v[122:123], v[172:173] op_sel_hi:[1,0]\n"
      "v_pk_mul_f32 v[146:147], v[146:147], v[172:173] op_sel_hi:[1,0]\n"
      "v_pk_mul_f32 v[106:107], v[106:107], v[104:105] op_sel_hi:[1,0]\n"
      "v_pk_mul_f32 v[124:125], v[124:125], v[172:173] op_sel_hi:[1,0]\n"
      "v_pk_mul_f32 v[156:157], v[156:157], v[84:85] op_sel:[0,1]\n"
      "v_pk_mul_f32 v[176:177], v[176:177], v[172:173] op_sel_hi:[1,0]\n"
      "v_mfma_f32_16x16x32_bf16 v[100:103], v[110:113], a[212:215], v[100:103]\n"
      "s_waitcnt lgkmcnt(1)\n"
      "v_mul_f32 v165, v104, v165\n"
      "v_cvt_pk_bf16_f32 v162, v162, v163\n"
      "v_mfma_f32_16x16x32_bf16 v[166:169], a[216:219], v[96:99], 0\n"
      "v_mul_f32 v108, v104, v108\n"
      "v_mul_f32 v121, v104, v121\n"
      "v_mfma_f32_16x16x32_bf16 v[166:169], a[220:223], v[210:213], v[166:169]\n"
      "v_mul_f32 v174, v172, v174\n"
      "v_mul_f32 v164, v104, v164\n"
      "v_mfma_f32_16x16x32_bf16 v[166:169], a[224:227], v[206:209], v[166:169]\n"
      "ds_read_b64 v[204:205], v10 offset:1192\n"
      "v_cvt_pk_bf16_f32 v144, v144, v145\n"
      "v_mfma_f32_16x16x32_bf16 v[112:115], a[240:243], v[96:99], 0\n"
      "v_cvt_pk_bf16_f32 v163, v164, v165\n"
      "v_cvt_pk_bf16_f32 v85, v102, v103\n"
      "v_mfma_f32_16x16x32_bf16 v[112:115], a[244:247], v[210:213], v[112:115]\n"
      "ds_read_b128 v[140:143], v10 offset:1200\n"
      "v_mul_f32 v109, v104, v109\n"
      "v_mfma_f32_16x16x32_bf16 v[112:115], a[248:251], v[206:209], v[112:115]\n"
      "v_cvt_pk_bf16_f32 v145, v146, v147\n"
      "v_cvt_pk_bf16_f32 v164, v118, v119\n"
      "v_mfma_f32_16x16x32_bf16 v[116:119], v[232:235], v[96:99], 0\n"
      "ds_read_b128 v[186:189], v2 offset:9216\n"
      "v_mul_f32 v120, v104, v120\n"
      "v_mfma_f32_16x16x32_bf16 v[116:119], v[236:239], v[210:213], v[116:119]\n"
      "v_cvt_pk_bf16_f32 v151, v222, v223\n"
      "v_cvt_pk_bf16_f32 v80, v126, v127\n"
      "v_mfma_f32_16x16x32_bf16 v[116:119], v[240:243], v[206:209], v[116:119]\n"
      "ds_read_u16 v86, v11 offset:1232\n"
      "v_cvt_pk_bf16_f32 v156, v156, v157\n"
      "v_mfma_f32_16x16x32_bf16 v[96:99], v[182:185], v[198:201], v[40:43]\n"
      "ds_read_b128 v[206:209], v2 offset:12288\n"
      "v_cvt_pk_bf16_f32 v165, v120, v121\n"
      "v_mfma_f32_16x16x32_bf16 v[102:105], v[148:151], v[198:201], v[44:47]\n"
      "v_cvt_pk_bf16_f32 v157, v158, v159\n"
      "v_cvt_pk_bf16_f32 v106, v106, v107\n"
      "v_mfma_f32_16x16x32_bf16 v[210:213], v[162:165], v[198:201], v[48:51]\n"
      "s_waitcnt lgkmcnt(3)\n"
      "v_cvt_pk_bf16_f32 v122, v122, v123\n"
      "v_cvt_pk_bf16_f32 v77, v220, v221\n"
      "v_mfma_f32_16x16x32_bf16 v[190:193], v[140:143], v[198:201], v[24:27]\n"
      "s_waitcnt lgkmcnt(2)\n"
      "v_cvt_pk_bf16_f32 v107, v108, v109\n"
      "v_cvt_pk_bf16_f32 v84, v100, v101\n"
      "v_mfma_f32_16x16x16_bf16 v[96:99], v[160:161], v[170:171], v[96:99]\n"
      "ds_read_b128 v[220:223], v2 offset:3328\n"
      "v_mul_f32 v175, v172, v175\n"
      "v_mfma_f32_16x16x16_bf16 v[102:105], v[202:203], v[170:171], v[102:105]\n"
      "v_cvt_pk_bf16_f32 v76, v218, v219\n"
      "v_cvt_pk_bf16_f32 v123, v124, v125\n"
      "s_waitcnt vmcnt(6)\n"
      "v_or3_b32 v134, v134, v135, v136\n"
      "v_mfma_f32_16x16x16_bf16 v[210:213], v[106:107], v[170:171], v[210:213]\n"
      "v_cvt_pk_bf16_f32 v125, v176, v177\n"
      "v_cvt_pk_bf16_f32 v81, v128, v129\n"
      "v_max3_f32 v176, v96, v97, v98\n"
      "v_mfma_f32_16x16x16_bf16 v[190:193], v[204:205], v[170:171], v[190:193]\n"
      "v_max3_f32 v176, v176, v104, v105\n"
      "v_cvt_pk_bf16_f32 v124, v174, v175\n"
      "v_max3_f32 v128, v99, v102, v103\n"
      "v_mfma_f32_16x16x32_bf16 v[166:169], a[228:231], v[186:189], v[166:169]\n"
      "s_waitcnt lgkmcnt(1)\n"
      "v_max3_f32 v128, v128, v210, v211\n"
      "v_max3_f32 v176, v176, v212, v213\n"
      "v_mfma_f32_16x16x32_bf16 v[166:169], a[232:235], v[206:209], v[166:169]\n"
      "v_max3_f32 v176, v176, v128, v190\n"
      "v_mov_b32 v128, v176\n"
      "v_mfma_f32_16x16x32_bf16 v[166:169], a[236:239], v[178:181], v[166:169]\n"
      "s_nop 0\n"
      "v_permlane16_swap_b32 v176, v128\n"
      "v_mfma_f32_16x16x32_bf16 v[112:115], a[252:255], v[186:189], v[112:115]\n"
      "v_max_f32 v176, v176, v128\n"
      "v_mov_b32 v128, v176\n"
      "v_mfma_f32_16x16x32_bf16 v[112:115], v[224:227], v[206:209], v[112:115]\n"
      "s_nop 0\n"
      "v_permlane32_swap_b32 v176, v128\n"
      "v_max_f32 v176, v176, v128\n"
      "v_mfma_f32_16x16x32_bf16 v[112:115], v[228:231], v[178:181], v[112:115]\n"
      "ds_read_b128 v[126:129], v2 offset:256\n"
      "v_sub_f32 v102, v102, v176\n"
      "v_mfma_f32_16x16x32_bf16 v[116:119], v[244:247], v[186:189], v[116:119]\n"
      "v_sub_f32 v103, v103, v176\n"
      "v_sub_f32 v212, v212, v176\n"
      "v_mfma_f32_16x16x32_bf16 v[116:119], v[248:251], v[206:209], v[116:119]\n"
      "v_sub_f32 v99, v99, v176\n"
      "v_sub_f32 v97, v97, v176\n"
      "v_mfma_f32_16x16x32_bf16 v[116:119], v[252:255], v[178:181], v[116:119]\n"
      "v_sub_f32 v213, v213, v176\n"
      "v_sub_f32_e64 v98, v98, v176\n"
      "s_waitcnt vmcnt(5)\n"
      "v_or3_b32 v134, v134, v137, v139\n"
      "v_mfma_f32_16x16x32_bf16 v[170:173], v[182:185], v[152:155], v[52:55]\n"
      "v_exp_f32 v212, v212\n"
      "v_mfma_f32_16x16x32_bf16 v[198:201], v[148:151], v[152:155], v[56:59]\n"
      "ds_read_b128 v[178:181], v2 offset:6400\n"
      "v_exp_f32 v102, v102\n"
      "v_mfma_f32_16x16x32_bf16 v[108:111], v[162:165], v[152:155], v[60:63]\n"
      "v_sub_f32 v190, v190, v176\n"
      "v_exp_f32 v190, v190\n"
      "v_mfma_f32_16x16x32_bf16 v[136:139], v[140:143], v[152:155], v[26:29]\n"
      "s_waitcnt lgkmcnt(1)\n"
      "v_exp_f32 v213, v213\n"
      "v_mfma_f32_16x16x16_bf16 v[170:173], v[160:161], v[156:157], v[170:173]\n"
      "v_sub_f32 v210, v210, v176\n"
      "v_sub_f32 v211, v211, v176\n"
      "v_sub_f32 v105, v105, v176\n"
      "v_mfma_f32_16x16x16_bf16 v[198:201], v[202:203], v[156:157], v[198:201]\n"
      "v_exp_f32 v211, v211\n"
      "v_exp_f32 v97, v97\n"
      "v_mfma_f32_16x16x16_bf16 v[108:111], v[106:107], v[156:157], v[108:111]\n"
      "v_sub_f32 v96, v96, v176\n"
      "v_exp_f32 v99, v99\n"
      "v_mfma_f32_16x16x16_bf16 v[136:139], v[204:205], v[156:157], v[136:139]\n"
      "v_exp_f32 v98, v98\n"
      "v_sub_f32 v104, v104, v176\n"
      "v_exp_f32 v96, v96\n"
      "v_mfma_f32_16x16x32_bf16 v[186:189], a[216:219], v[126:129], 0\n"
      "v_max3_f32 v154, v170, v171, v172\n"
      "v_exp_f32_e64 v105, v105\n"
      "v_mfma_f32_16x16x32_bf16 v[186:189], a[220:223], v[220:223], v[186:189]\n"
      "s_waitcnt lgkmcnt(0)\n"
      "v_exp_f32 v103, v103\n"
      "v_mfma_f32_16x16x32_bf16 v[186:189], a[224:227], v[178:181], v[186:189]\n"
      "v_cvt_pk_bf16_f32 v96, v96, v97\n"
      "v_cvt_pk_bf16_f32 v97, v98, v99\n"
      "v_mfma_f32_16x16x32_bf16 v[174:177], a[240:243], v[126:129], 0\n"
      "v_exp_f32_e64 v210, v210\n"
      "v_mfma_f32_16x16x32_bf16 v[174:177], a[244:247], v[220:223], v[174:177]\n"
      "v_cvt_pk_bf16_f32 v98, v102, v103\n"
      "v_max3_f32 v154, v154, v200, v201\n"
      "v_mfma_f32_16x16x32_bf16 v[174:177], a[248:251], v[178:181], v[174:177]\n"
      "v_exp_f32_e64 v104, v104\n"
      "v_mfma_f32_16x16x32_bf16 v[206:209], v[232:235], v[126:129], 0\n"
      "v_cvt_pk_bf16_f32 v88, v210, v211\n"
      "v_max3_f32 v157, v173, v198, v199\n"
      "v_mfma_f32_16x16x32_bf16 v[206:209], v[236:239], v[220:223], v[206:209]\n"
      "v_cvt_pk_bf16_f32 v90, v190, 0\n"
      "v_cvt_pk_bf16_f32 v99, v104, v105\n"
      "v_mfma_f32_16x16x32_bf16 v[206:209], v[240:243], v[178:181], v[206:209]\n"
      "v_max3_f32 v157, v157, v108, v109\n"
      "v_cvt_pk_bf16_f32 v89, v212, v213\n"
      "v_max3_f32 v154, v154, v110, v111\n"
      "v_mfma_f32_16x16x32_bf16 v[210:213], v[32:35], v[96:99], 0\n"
      "v_max3_f32 v154, v154, v157, v136\n"
      "v_mov_b32 v105, v154\n"
      "v_mfma_f32_16x16x32_bf16 v[210:213], v[32:35], v[88:91], v[210:213]\n"
      "s_nop 0\n"
      "v_permlane16_swap_b32 v154, v105\n"
      "v_mfma_f32_16x16x32_bf16 v[156:159], v[130:133], v[96:99], 0\n"
      "v_max_f32 v154, v154, v105\n"
      "v_mov_b32 v105, v154\n"
      "v_mfma_f32_16x16x32_bf16 v[156:159], v[76:79], v[88:91], v[156:159]\n"
      "s_nop 0\n"
      "v_permlane32_swap_b32 v154, v105\n"
      "v_mfma_f32_16x16x32_bf16 v[218:221], v[214:217], v[96:99], 0\n"
      "v_max_f32 v154, v154, v105\n"
      "v_sub_f32 v170, v170, v154\n"
      "v_sub_f32 v201, v201, v154\n"
      "v_mfma_f32_16x16x32_bf16 v[218:221], v[80:83], v[88:91], v[218:221]\n"
      "v_rcp_f32_e64 v210, v210\n"
      "v_mfma_f32_16x16x32_bf16 v[126:129], v[194:197], v[96:99], 0\n"
      "ds_read_b128 v[102:105], v2 offset:9472\n"
      "v_sub_f32 v200, v200, v154\n"
      "v_mfma_f32_16x16x32_bf16 v[126:129], v[84:87], v[88:91], v[126:129]\n"
      "v_sub_f32 v172, v172, v154\n"
      "v_sub_f32 v108, v108, v154\n"
      "v_sub_f32 v171, v171, v154\n"
      "v_sub_f32 v198, v198, v154\n"
      "v_sub_f32 v136, v136, v154\n"
      "v_sub_f32 v173, v173, v154\n"
      "v_exp_f32 v198, v198\n"
      "v_sub_f32 v111, v111, v154\n"
      "v_exp_f32 v173, v173\n"
      "v_sub_f32 v109, v109, v154\n"
      "v_sub_f32 v199, v199, v154\n"
      "v_pk_mul_f32 v[126:127], v[126:127], v[210:211] op_sel_hi:[1,0]\n"
      "v_sub_f32_e64 v110, v110, v154\n"
      "v_pk_mul_f32 v[220:221], v[220:221], v[210:211] op_sel_hi:[1,0]\n"
      "v_pk_mul_f32 v[156:157], v[156:157], v[210:211] op_sel_hi:[1,0]\n"
      "v_pk_mul_f32 v[128:129], v[128:129], v[210:211] op_sel_hi:[1,0]\n"
      "v_exp_f32_e64 v200, v200\n"
      "v_cvt_pk_bf16_f32 v126, v126, v127\n"
      "v_cvt_pk_bf16_f32 v127, v128, v129\n"
      "v_pk_mul_f32 v[158:159], v[158:159], v[210:211] op_sel_hi:[1,0]\n"
      "v_exp_f32 v171, v171\n"
      "v_exp_f32 v170, v170\n"
      "v_mfma_f32_16x16x32_bf16 v[98:101], v[182:185], v[122:125], v[64:67]\n"
      "v_exp_f32 v172, v172\n"
      "v_exp_f32 v201, v201\n"
      "v_cvt_pk_bf16_f32 v156, v156, v157\n"
      "v_cvt_pk_bf16_f32 v170, v170, v171\n"
      "v_pk_mul_f32 v[218:219], v[218:219], v[210:211] op_sel_hi:[1,0]\n"
      "v_mfma_f32_16x16x32_bf16 v[210:213], v[148:151], v[122:125], v[68:71]\n"
      "s_waitcnt lgkmcnt(0)\n"
      "v_cvt_pk_bf16_f32 v171, v172, v173\n"
      "v_cvt_pk_bf16_f32 v173, v200, v201\n"
      "v_mfma_f32_16x16x32_bf16 v[152:155], v[162:165], v[122:125], v[72:75]\n"
      "ds_read_b128 v[190:193], v2 offset:12544\n"
      "v_exp_f32 v199, v199\n"
      "v_mfma_f32_16x16x32_bf16 v[178:181], v[140:143], v[122:125], v[28:31]\n"
      "ds_read_b128 v[122:125], v2 offset:15616\n"
      "v_cvt_pk_bf16_f32 v172, v198, v199\n"
      "v_mfma_f32_16x16x16_bf16 v[98:101], v[160:161], v[144:145], v[98:101]\n"
      "v_exp_f32_e64 v110, v110\n"
      "v_cvt_pk_bf16_f32 v157, v158, v159\n"
      "v_mfma_f32_16x16x16_bf16 v[210:213], v[202:203], v[144:145], v[210:213]\n"
      "v_cvt_pk_bf16_f32 v158, v218, v219\n"
      "v_exp_f32 v111, v111\n"
      "v_mfma_f32_16x16x16_bf16 v[152:155], v[106:107], v[144:145], v[152:155]\n"
      "v_cvt_pk_bf16_f32 v93, v110, v111\n"
      "v_exp_f32 v109, v109\n"
      "v_mfma_f32_16x16x16_bf16 v[178:181], v[204:205], v[144:145], v[178:181]\n"
      "v_exp_f32 v108, v108\n"
      "v_max3_f32 v89, v98, v99, v100\n"
      "v_mfma_f32_16x16x32_bf16 v[186:189], a[228:231], v[102:105], v[186:189]\n"
      "s_waitcnt lgkmcnt(1)\n"
      "v_mul_f32 v97, v167, v167\n"
      "v_cvt_pk_bf16_f32 v159, v220, v221\n"
      "v_mfma_f32_16x16x32_bf16 v[186:189], a[232:235], v[190:193], v[186:189]\n"
      "s_waitcnt lgkmcnt(0)\n"
      "v_exp_f32_e64 v136, v136\n"
      "v_mfma_f32_16x16x32_bf16 v[186:189], a[236:239], v[122:125], v[186:189]\n"
      "v_max3_f32 v89, v89, v212, v213\n"
      "v_fma_f32 v96, v166, v166, s52\n"
      "v_mfma_f32_16x16x32_bf16 v[174:177], a[252:255], v[102:105], v[174:177]\n"
      "v_cvt_pk_bf16_f32 v92, v108, v109\n"
      "v_max3_f32 v181, v101, v210, v211\n"
      "v_mfma_f32_16x16x32_bf16 v[174:177], v[224:227], v[190:193], v[174:177]\n"
      "s_add_u32 s42, s42, 1\n"
      "v_max3_f32 v181, v181, v152, v153\n"
      "v_max3_f32 v89, v89, v154, v155\n"
      "v_mfma_f32_16x16x32_bf16 v[174:177], v[228:231], v[122:125], v[174:177]\n"
      "v_max3_f32 v89, v89, v181, v178\n"
      "v_mov_b32 v144, v89\n"
      "v_mfma_f32_16x16x32_bf16 v[206:209], v[244:247], v[102:105], v[206:209]\n"
      "s_nop 0\n"
      "v_permlane16_swap_b32 v89, v144\n"
      "v_mfma_f32_16x16x32_bf16 v[206:209], v[248:251], v[190:193], v[206:209]\n"
      "v_max_f32 v89, v89, v144\n"
      "v_mov_b32 v144, v89\n"
      "v_mul_f32_e64 v181, v187, v187\n"
      "v_cvt_pk_bf16_f32 v94, v136, 0\n"
      "v_fma_f32 v180, v186, v186, s52\n"
      "v_pk_fma_f32 v[96:97], v[168:169], v[168:169], v[96:97]\n"
      "v_pk_fma_f32 v[180:181], v[188:189], v[188:189], v[180:181]\n"
      "v_mfma_f32_16x16x32_bf16 v[206:209], v[252:255], v[122:125], v[206:209]\n"
      "v_permlane32_swap_b32_e64 v89, v144\n"
      "v_mfma_f32_16x16x32_bf16 v[218:221], v[32:35], v[170:173], 0\n"
      "ds_read_b64 v[124:125], v10 offset:1184\n"
      "v_max_f32 v89, v89, v144\n"
      "v_mfma_f32_16x16x32_bf16 v[218:221], v[32:35], v[92:95], v[218:221]\n"
      "ds_read_b128 v[144:147], v10 offset:1168\n"
      "v_sub_f32 v210, v210, v89\n"
      "v_sub_f32 v101, v101, v89\n"
      "v_mfma_f32_16x16x32_bf16 v[102:105], v[130:133], v[170:173], 0\n"
      "v_sub_f32 v153, v153, v89\n"
      "v_sub_f32 v155, v155, v89\n"
      "v_mfma_f32_16x16x32_bf16 v[102:105], v[76:79], v[92:95], v[102:105]\n"
      "s_nop 0\n"
      "v_rcp_f32 v218, v218\n"
      "v_sub_f32 v99, v99, v89\n"
      "v_mfma_f32_16x16x32_bf16 v[220:223], v[214:217], v[170:173], 0\n"
      "v_exp_f32_e64 v99, v99\n"
      "v_mfma_f32_16x16x32_bf16 v[220:223], v[80:83], v[92:95], v[220:223]\n"
      "ds_write_b64 v4, v[126:127] offset:18432\n"
      "v_mfma_f32_16x16x32_bf16 v[126:129], v[194:197], v[170:173], 0\n"
      "v_sub_f32 v213, v213, v89\n"
      "v_sub_f32 v98, v98, v89\n"
      "v_mfma_f32_16x16x32_bf16 v[126:129], v[84:87], v[92:95], v[126:129]\n"
      "s_waitcnt lgkmcnt(1)\n"
      "v_sub_f32 v152, v152, v89\n"
      "v_exp_f32 v152, v152\n"
      "v_mfma_f32_16x16x32_bf16 v[136:139], v[144:147], v[182:185], v[30:33]\n"
      "ds_read_b128 v[198:201], v2 offset:6656\n"
      "v_sub_f32 v211, v211, v89\n"
      "v_mfma_f32_16x16x32_bf16 v[108:111], v[144:147], v[148:151], v[30:33]\n"
      "v_exp_f32 v153, v153\n"
      "v_mfma_f32_16x16x32_bf16 v[148:151], v[144:147], v[162:165], v[30:33]\n"
      "ds_read_b128 v[120:123], v2 offset:3584\n"
      "v_sub_f32 v154, v154, v89\n"
      "v_mfma_f32_16x16x32_bf16 v[190:193], v[144:147], v[140:143], v[30:33]\n"
      "ds_read_b128 v[162:165], v2 offset:512\n"
      "v_cvt_pk_bf16_f32 v88, v152, v153\n"
      "v_mfma_f32_16x16x16_bf16 v[136:139], v[124:125], v[160:161], v[136:139]\n"
      "v_sub_f32 v100, v100, v89\n"
      "v_exp_f32 v98, v98\n"
      "v_mfma_f32_16x16x16_bf16 v[108:111], v[124:125], v[202:203], v[108:111]\n"
      "v_sub_f32 v178, v178, v89\n"
      "v_exp_f32 v211, v211\n"
      "v_mfma_f32_16x16x16_bf16 v[148:151], v[124:125], v[106:107], v[148:151]\n"
      "v_exp_f32 v213, v213\n"
      "v_exp_f32 v101, v101\n"
      "v_exp_f32 v154, v154\n"
      "v_pk_mul_f32 v[222:223], v[222:223], v[218:219] op_sel_hi:[1,0]\n"
      "v_pk_mul_f32 v[220:221], v[220:221], v[218:219] op_sel_hi:[1,0]\n"
      "v_sub_f32 v212, v212, v89\n"
      "v_pk_mul_f32 v[104:105], v[104:105], v[218:219] op_sel_hi:[1,0]\n"
      "v_pk_fma_f32 v[96:97], v[112:113], v[112:113], v[96:97]\n"
      "v_pk_mul_f32 v[126:127], v[126:127], v[218:219] op_sel_hi:[1,0]\n"
      "v_pk_fma_f32 v[180:181], v[174:175], v[174:175], v[180:181]\n"
      "v_pk_fma_f32 v[96:97], v[114:115], v[114:115], v[96:97]\n"
      "v_mfma_f32_16x16x16_bf16 v[190:193], v[124:125], v[204:205], v[190:193]\n"
      "v_exp_f32 v100, v100\n"
      "s_waitcnt lgkmcnt(0)\n"
      "v_mfma_f32_16x16x32_bf16 v[202:205], a[216:219], v[162:165], 0\n"
      "v_cndmask_b32 v136, v136, v108, s[58:59]\n"
      "v_cvt_pk_bf16_f32 v98, v98, v99\n"
      "v_mfma_f32_16x16x32_bf16 v[202:205], a[220:223], v[120:123], v[202:205]\n"
      "ds_read_b128 v[170:173], v2 offset:9728\n"
      "v_cndmask_b32 v136, v136, v148, s[60:61]\n"
      "v_mfma_f32_16x16x32_bf16 v[202:205], a[224:227], v[198:201], v[202:205]\n"
      "v_cvt_pk_bf16_f32 v126, v126, v127\n"
      "v_exp_f32_e64 v178, v178\n"
      "v_mfma_f32_16x16x32_bf16 v[144:147], a[240:243], v[162:165], 0\n"
      "ds_write_b32 v31, v134 offset:5632\n"
      "v_mfma_f32_16x16x32_bf16 v[144:147], a[244:247], v[120:123], v[144:147]\n"
      "v_exp_f32_e64 v155, v155\n"
      "v_cndmask_b32 v136, v136, v190, s[62:63]\n"
      "v_mfma_f32_16x16x32_bf16 v[144:147], a[248:251], v[198:201], v[144:147]\n"
      "ds_read_b128 v[190:193], v2 offset:12800\n"
      "v_cvt_pk_bf16_f32 v89, v154, v155\n"
      "v_max_f32_dpp v155, v136, v136 row_ror:8 row_mask:0xf bank_mask:0xf\n"
      "v_mfma_f32_16x16x32_bf16 v[148:151], v[232:235], v[162:165], 0\n"
      "ds_read_b128 v[106:109], v2 offset:15872\n"
      "v_max_f32_dpp v155, v155, v155 row_ror:4 row_mask:0xf bank_mask:0xf\n"
      "v_mfma_f32_16x16x32_bf16 v[148:151], v[236:239], v[120:123], v[148:151]\n"
      "ds_write_b128 v3, v[156:159] offset:18432\n"
      "v_pk_fma_f32 v[180:181], v[176:177], v[176:177], v[180:181]\n"
      "v_pk_fma_f32 v[180:181], v[206:207], v[206:207], v[180:181]\n"
      "v_pk_mul_f32 v[102:103], v[102:103], v[218:219] op_sel_hi:[1,0]\n"
      "v_pk_fma_f32 v[96:97], v[116:117], v[116:117], v[96:97]\n"
      "v_cvt_pk_bf16_f32 v99, v100, v101\n"
      "v_pk_fma_f32 v[180:181], v[208:209], v[208:209], v[180:181]\n"
      "v_pk_fma_f32 v[96:97], v[118:119], v[118:119], v[96:97]\n"
      "v_mfma_f32_16x16x32_bf16 v[148:151], v[240:243], v[198:201], v[148:151]\n"
      "s_waitcnt lgkmcnt(2)\n"
      "v_exp_f32 v212, v212\n"
      "v_mfma_f32_16x16x32_bf16 v[202:205], a[228:231], v[170:173], v[202:205]\n"
      "v_cvt_pk_bf16_f32 v102, v102, v103\n"
      "v_exp_f32 v210, v210\n"
      "v_mfma_f32_16x16x32_bf16 v[202:205], a[232:235], v[190:193], v[202:205]\n"
      "s_waitcnt lgkmcnt(1)\n"
      "v_max_f32_dpp v155, v155, v155 row_ror:2 row_mask:0xf bank_mask:0xf\n"
      "v_cvt_pk_bf16_f32 v103, v104, v105\n"
      "v_mfma_f32_16x16x32_bf16 v[202:205], a[236:239], v[106:109], v[202:205]\n"
      "v_cvt_pk_bf16_f32 v105, v222, v223\n"
      "v_cvt_pk_bf16_f32 v101, v212, v213\n"
      "v_mfma_f32_16x16x32_bf16 v[144:147], a[252:255], v[170:173], v[144:147]\n"
      "v_add_f32_e64 v180, v180, v181\n"
      "v_max_f32_dpp v155, v155, v155 row_ror:1 row_mask:0xf bank_mask:0xf\n"
      "v_mfma_f32_16x16x32_bf16 v[144:147], v[224:227], v[190:193], v[144:147]\n"
      "v_add_f32 v96, v96, v97\n"
      "v_max_f32_dpp v155, v155, v155 row_bcast:15 row_mask:0xa bank_mask:0xf\n"
      "v_mfma_f32_16x16x32_bf16 v[144:147], v[228:231], v[106:109], v[144:147]\n"
      "s_nop 0\n"
      "v_max_f32_dpp v155, v155, v155 row_bcast:31 row_mask:0xc bank_mask:0xf\n"
      "v_fma_f32 v158, v202, v202, s52\n"
      "v_cvt_pk_bf16_f32 v104, v220, v221\n"
      "v_mfma_f32_16x16x32_bf16 v[148:151], v[244:247], v[170:173], v[148:151]\n"
      "v_mul_f32 v159, v203, v203\n"
      "v_readlane_b32 s72, v155, 63\n"
      "v_mfma_f32_16x16x32_bf16 v[148:151], v[248:251], v[190:193], v[148:151]\n"
      "v_cvt_pk_bf16_f32 v90, v178, 0\n"
      "v_subrev_f32 v136, s72, v136\n"
      "v_mfma_f32_16x16x32_bf16 v[148:151], v[252:255], v[106:109], v[148:151]\n"
      "ds_write_b128 v3, v[102:105] offset:18688\n"
      "v_cvt_pk_bf16_f32 v100, v210, v211\n"
      "v_exp_f32 v136, v136\n"
      "v_pk_fma_f32 v[158:159], v[204:205], v[204:205], v[158:159]\n"
      "v_permlane16_swap_b32 v96, v180\n"
      "v_pk_fma_f32 v[158:159], v[144:145], v[144:145], v[158:159]\n"
      "v_pk_fma_f32 v[158:159], v[146:147], v[146:147], v[158:159]\n"
      "v_cvt_pk_bf16_f32 v136, v136, v136\n"
      "v_pk_mul_f32 v[128:129], v[128:129], v[218:219] op_sel_hi:[1,0]\n"
      "v_pk_fma_f32 v[158:159], v[148:149], v[148:149], v[158:159]\n"
      "v_pk_fma_f32 v[158:159], v[150:151], v[150:151], v[158:159]\n"
      "v_mfma_f32_16x16x32_bf16 v[154:157], v[32:35], v[98:101], 0\n"
      "v_add_f32_e64 v96, v96, v180\n"
      "v_cvt_pk_bf16_f32 v127, v128, v129\n"
      "v_mfma_f32_16x16x32_bf16 v[154:157], v[32:35], v[88:91], v[154:157]\n"
      "v_add_f32 v158, v158, v159\n"
      "v_mov_b32 v180, v158\n"
      "v_mfma_f32_16x16x32_bf16 v[210:213], v[130:133], v[98:101], 0\n"
      "ds_write_b16 v13, v136\n"
      "v_mfma_f32_16x16x32_bf16 v[210:213], v[76:79], v[88:91], v[210:213]\n"
      "v_permlane16_swap_b32_e64 v158, v180\n"
      "v_mfma_f32_16x16x32_bf16 v[218:221], v[214:217], v[98:101], 0\n"
      "ds_write_b64 v4, v[126:127] offset:18688\n"
      "v_add_f32 v158, v158, v180\n"
      "v_mfma_f32_16x16x32_bf16 v[218:221], v[80:83], v[88:91], v[218:221]\n"
      "s_nop 0\n"
      "v_permlane32_swap_b32 v96, v158\n"
      "v_add_f32 v96, v96, v158\n"
      "v_mfma_f32_16x16x32_bf16 v[134:137], v[194:197], v[98:101], 0\n"
      "ds_write_b32 v31, v96 offset:1024\n"
      "v_rcp_f32_e64 v154, v154\n"
      "s_nop 0\n"
      "v_pk_mul_f32 v[210:211], v[210:211], v[154:155] op_sel_hi:[1,0]\n"
      "v_pk_mul_f32 v[212:213], v[212:213], v[154:155] op_sel_hi:[1,0]\n"
      "v_pk_mul_f32 v[220:221], v[220:221], v[154:155] op_sel_hi:[1,0]\n"
      "v_pk_mul_f32 v[218:219], v[218:219], v[154:155] op_sel_hi:[1,0]\n"
      "v_mfma_f32_16x16x32_bf16 v[134:137], v[84:87], v[88:91], v[134:137]\n"
      "v_cvt_pk_bf16_f32 v210, v210, v211\n"
      "v_cvt_pk_bf16_f32 v211, v212, v213\n"
      "v_cvt_pk_bf16_f32 v212, v218, v219\n"
      "v_cvt_pk_bf16_f32 v213, v220, v221\n"
      "s_and_b32 s47, s47, -4\n"
      "ds_write_b128 v3, v[210:213] offset:18944\n"
      "ds_read_b128 v[156:159], v14\n"
      "ds_read_b128 v[210:213], v14 offset:64\n"
      "v_pk_mul_f32 v[134:135], v[134:135], v[154:155] op_sel_hi:[1,0]\n"
      "v_pk_mul_f32 v[136:137], v[136:137], v[154:155] op_sel_hi:[1,0]\n"
      "v_cvt_pk_bf16_f32 v134, v134, v135\n"
      "v_cvt_pk_bf16_f32 v135, v136, v137\n"
      "s_add_u32 s43, s43, 1\n"
      "ds_write_b64 v4, v[134:135] offset:18944\n"
      "v_add_u32 v11, 2336, v11\n"
      "v_add_u32 v10, 2336, v10\n"
      "s_waitcnt lgkmcnt(2)\n"
      "v_mfma_f32_16x16x32_bf16 v[152:155], v[156:159], v[32:35], 0\n"
      "s_waitcnt lgkmcnt(1)\n"
      "v_mfma_f32_16x16x32_bf16 v[152:155], v[210:213], v[32:35], v[152:155]\n"
      "s_waitcnt lgkmcnt(0)\n"
      "s_barrier\n"
      "s_cbranch_vccnz L_disp0_%=\n"
      "L_max0_%=:\n"
      "v_mfma_f32_16x16x32_bf16 v[218:221], v[156:159], v[130:133], 0\n"
      "ds_read_b128 v[162:165], v0\n"
      "s_add_u32 m0, s64, 18432\n"
      "v_mfma_f32_16x16x32_bf16 v[218:221], v[210:213], v[76:79], v[218:221]\n"
      "ds_read_b128 v[100:103], v9 offset:1024\n"
      "v_readlane_b32 s84, v37, s43\n"
      "v_mfma_f32_16x16x32_bf16 v[134:137], v[156:159], v[214:217], 0\n"
      "ds_read_b128 v[96:99], v0 offset:6144\n"
      "v_readlane_b32 s85, v37, s42\n"
      "v_mfma_f32_16x16x32_bf16 v[134:137], v[210:213], v[80:83], v[134:137]\n"
      "buffer_load_dwordx4 v16, s[16:19], s45 offen  lds\n"
      "v_mfma_f32_16x16x32_bf16 v[128:131], v[156:159], v[194:197], 0\n"
      "ds_read_b128 v[190:193], v1\n"
      "s_add_u32 m0, s64, 22528\n"
      "v_readlane_b32 s86, v38, s43\n"
      "v_mfma_f32_16x16x32_bf16 v[128:131], v[210:213], v[84:87], v[128:131]\n"
      "ds_read_b128 v[124:127], v1 offset:12288\n"
      "s_waitcnt lgkmcnt(4)\n"
      "v_mfma_f32_16x16x32_bf16 v[120:123], a[0:3], v[162:165], 0\n"
      "ds_read_b128 v[170:173], v1 offset:2048\n"
      "v_cndmask_b32 v218, v218, v134, s[58:59]\n"
      "v_mfma_f32_16x16x32_bf16 v[178:181], a[24:27], v[162:165], 0\n"
      "s_waitcnt lgkmcnt(4)\n"
      "v_add_f32 v100, v100, v101\n"
      "v_add_f32 v102, v102, v103\n"
      "v_mfma_f32_16x16x32_bf16 v[138:141], a[48:51], v[162:165], 0\n"
      "buffer_load_dwordx4 v17, s[16:19], s45 offen  lds\n"
      "v_mfma_f32_16x16x32_bf16 v[134:137], a[72:75], v[162:165], 0\n"
      "ds_read_b128 v[154:157], v0 offset:12288\n"
      "v_add_f32_e64 v100, v100, v102\n"
      "v_mfma_f32_16x16x32_bf16 v[220:223], a[96:99], v[162:165], 0\n"
      "s_waitcnt lgkmcnt(3)\n"
      "v_rsq_f32 v100, v100\n"
      "v_mfma_f32_16x16x32_bf16 v[106:109], a[120:123], v[162:165], 0\n"
      "s_add_u32 m0, s64, 26624\n"
      "v_mov_b32_dpp v152, v152 quad_perm:[0,0,0,0] row_mask:0xf bank_mask:0xf\n"
      "v_mul_f32 v100, 0x415db3d7, v100\n"
      "v_mfma_f32_16x16x32_bf16 v[120:123], a[4:7], v[190:193], v[120:123]\n"
      "ds_read_b128 v[210:213], v1 offset:6144\n"
      "v_mov_b32 v102, v100\n"
      "v_mfma_f32_16x16x32_bf16 v[120:123], a[8:11], v[96:99], v[120:123]\n"
      "s_nop 0\n"
      "v_permlane32_swap_b32_e64 v100, v102\n"
      "v_mfma_f32_16x16x32_bf16 v[178:181], a[28:31], v[190:193], v[178:181]\n"
      "ds_read_b128 v[158:161], v1 offset:8192\n"
      "v_mov_b32_e64 v101, v100\n"
      "v_mfma_f32_16x16x32_bf16 v[178:181], a[32:35], v[96:99], v[178:181]\n"
      "buffer_load_dwordx4 v18, s[16:19], s45 offen  lds\n"
      "v_mfma_f32_16x16x32_bf16 v[138:141], a[52:55], v[190:193], v[138:141]\n"
      "v_permlane16_swap_b32 v100, v101\n"
      "v_mfma_f32_16x16x32_bf16 v[138:141], a[56:59], v[96:99], v[138:141]\n"
      "v_mul_f32 v188, v101, v188\n"
      "v_mul_f32_e64 v115, v100, v115\n"
      "v_mfma_f32_16x16x32_bf16 v[134:137], a[76:79], v[190:193], v[134:137]\n"
      "s_add_u32 m0, s64, 30720\n"
      "v_mul_f32 v167, v100, v167\n"
      "v_mul_f32 v112, v100, v112\n"
      "v_mfma_f32_16x16x32_bf16 v[134:137], a[80:83], v[96:99], v[134:137]\n"
      "v_mul_f32 v119, v100, v119\n"
      "v_mul_f32 v166, v100, v166\n"
      "v_mfma_f32_16x16x32_bf16 v[220:223], a[100:103], v[190:193], v[220:223]\n"
      "buffer_load_dwordx4 v19, s[16:19], s45 offen  lds\n"
      "v_rcp_f32_e64 v152, v152\n"
      "v_pk_mul_f32 v[168:169], v[168:169], v[100:101] op_sel_hi:[1,0]\n"
      "v_mfma_f32_16x16x32_bf16 v[220:223], a[104:107], v[96:99], v[220:223]\n"
      "s_add_u32 m0, s65, 34816\n"
      "v_cvt_pk_bf16_f32 v166, v166, v167\n"
      "v_mul_f32 v177, v101, v177\n"
      "v_mfma_f32_16x16x32_bf16 v[106:109], a[124:127], v[190:193], v[106:109]\n"
      "v_mul_f32 v205, v102, v205\n"
      "v_mul_f32 v113, v100, v113\n"
      "v_mfma_f32_16x16x32_bf16 v[106:109], a[128:131], v[96:99], v[106:109]\n"
      "s_waitcnt lgkmcnt(0)\n"
      "v_mul_f32 v114, v100, v114\n"
      "v_mul_f32 v118, v100, v118\n"
      "v_mfma_f32_16x16x32_bf16 v[120:123], a[12:15], v[210:213], v[120:123]\n"
      "v_mul_f32 v208, v101, v208\n"
      "v_mul_f32 v176, v101, v176\n"
      "v_mfma_f32_16x16x32_bf16 v[120:123], a[16:19], v[154:157], v[120:123]\n"
      "v_cvt_pk_bf16_f32 v167, v168, v169\n"
      "v_cvt_pk_bf16_f32 v168, v112, v113\n"
      "v_mfma_f32_16x16x32_bf16 v[120:123], a[20:23], v[124:127], v[120:123]\n"
      "buffer_load_dwordx4 v15, s[16:19], s45 offen  lds\n"
      "v_mfma_f32_16x16x32_bf16 v[178:181], a[36:39], v[210:213], v[178:181]\n"
      "v_mul_f32 v116, v100, v116\n"
      "v_cvt_pk_bf16_f32 v169, v114, v115\n"
      "v_mfma_f32_16x16x32_bf16 v[178:181], a[40:43], v[154:157], v[178:181]\n"
      "v_mul_f32 v202, v102, v202\n"
      "v_mul_f32_e64 v117, v100, v117\n"
      "v_mfma_f32_16x16x32_bf16 v[178:181], a[44:47], v[124:127], v[178:181]\n"
      "ds_write_b128 v5, v[166:169]\n"
      "v_pk_mul_f32 v[146:147], v[146:147], v[102:103] op_sel_hi:[1,0]\n"
      "v_pk_mul_f32 v[186:187], v[186:187], v[100:101] op_sel:[0,1]\n"
      "v_mfma_f32_16x16x32_bf16 v[138:141], a[60:63], v[210:213], v[138:141]\n"
      "buffer_load_dwordx4 v[182:185], v36, s[28:31], s47 offen\n"
      "v_mfma_f32_16x16x32_bf16 v[138:141], a[64:67], v[154:157], v[138:141]\n"
      "v_cvt_pk_bf16_f32 v116, v116, v117\n"
      "v_mul_f32_e64 v175, v101, v175\n"
      "v_mfma_f32_16x16x32_bf16 v[138:141], a[68:71], v[124:127], v[138:141]\n"
      "v_fma_f32 v78, v120, v120, s51\n"
      "v_cvt_pk_bf16_f32 v117, v118, v119\n"
      "v_mfma_f32_16x16x32_bf16 v[134:137], a[84:87], v[210:213], v[134:137]\n"
      "v_fmac_f32_e64 v78, v121, v121\n"
      "v_cvt_pk_bf16_f32 v186, v186, v187\n"
      "v_mfma_f32_16x16x32_bf16 v[134:137], a[88:91], v[154:157], v[134:137]\n"
      "ds_write_b64 v6, v[116:117]\n"
      "v_mfma_f32_16x16x32_bf16 v[134:137], a[92:95], v[124:127], v[134:137]\n"
      "buffer_load_dword v92, v29, s[28:31], s47 offen\n"
      "v_mfma_f32_16x16x32_bf16 v[220:223], a[108:111], v[210:213], v[220:223]\n"
      "ds_read_b128 v[166:169], v0 offset:14336\n"
      "v_mul_f32_e64 v189, v101, v189\n"
      "v_mfma_f32_16x16x32_bf16 v[220:223], a[112:115], v[154:157], v[220:223]\n"
      "ds_read_b128 v[112:115], v1 offset:14336\n"
      "v_cvt_pk_bf16_f32 v187, v188, v189\n"
      "v_mfma_f32_16x16x32_bf16 v[220:223], a[116:119], v[124:127], v[220:223]\n"
      "v_mul_f32 v148, v102, v148\n"
      "v_mul_f32 v174, v101, v174\n"
      "v_mfma_f32_16x16x32_bf16 v[106:109], a[132:135], v[210:213], v[106:109]\n"
      "ds_read_b128 v[198:201], v0 offset:8192\n"
      "v_mul_f32 v209, v101, v209\n"
      "v_mfma_f32_16x16x32_bf16 v[106:109], a[136:139], v[154:157], v[106:109]\n"
      "v_fmac_f32 v78, v122, v122\n"
      "v_cvt_pk_bf16_f32 v189, v176, v177\n"
      "v_mfma_f32_16x16x32_bf16 v[106:109], a[140:143], v[124:127], v[106:109]\n"
      "v_cvt_pk_bf16_f32 v188, v174, v175\n"
      "v_fmac_f32_e64 v78, v123, v123\n"
      "v_mfma_f32_16x16x32_bf16 v[130:133], v[162:165], a[144:147], 0\n"
      "v_fmac_f32 v78, v178, v178\n"
      "v_mul_f32 v206, v101, v206\n"
      "v_mfma_f32_16x16x32_bf16 v[130:133], v[190:193], a[148:151], v[130:133]\n"
      "ds_write_b128 v5, v[186:189] offset:6144\n"
      "v_mfma_f32_16x16x32_bf16 v[130:133], v[96:99], a[152:155], v[130:133]\n"
      "v_mul_f32 v207, v101, v207\n"
      "v_fma_f32 v129, v134, v134, s51\n"
      "v_mfma_f32_16x16x32_bf16 v[130:133], v[210:213], a[156:159], v[130:133]\n"
      "v_mul_f32 v204, v102, v204\n"
      "v_cvt_pk_bf16_f32 v206, v206, v207\n"
      "v_mfma_f32_16x16x32_bf16 v[130:133], v[154:157], a[160:163], v[130:133]\n"
      "s_or_b32 s95, s84, s85\n"
      "v_fmac_f32 v78, v179, v179\n"
      "v_cvt_pk_bf16_f32 v207, v208, v209\n"
      "v_mfma_f32_16x16x32_bf16 v[130:133], v[124:127], a[164:167], v[130:133]\n"
      "ds_read_b128 v[174:177], v0 offset:4096\n"
      "v_mul_f32_e64 v203, v102, v203\n"
      "v_mfma_f32_16x16x32_bf16 v[214:217], v[162:165], a[168:171], 0\n"
      "ds_write_b64 v6, v[206:207] offset:6144\n"
      "v_mfma_f32_16x16x32_bf16 v[214:217], v[190:193], a[172:175], v[214:217]\n"
      "v_mul_f32 v145, v102, v145\n"
      "v_mul_f32 v150, v102, v150\n"
      "v_mfma_f32_16x16x32_bf16 v[214:217], v[96:99], a[176:179], v[214:217]\n"
      "v_mul_f32 v149, v102, v149\n"
      "v_fmac_f32 v129, v135, v135\n"
      "v_mfma_f32_16x16x32_bf16 v[214:217], v[210:213], a[180:183], v[214:217]\n"
      "v_cvt_pk_bf16_f32 v202, v202, v203\n"
      "v_mul_f32_e64 v151, v102, v151\n"
      "v_mfma_f32_16x16x32_bf16 v[214:217], v[154:157], a[184:187], v[214:217]\n"
      "ds_read_b128 v[186:189], v0 offset:2048\n"
      "v_fmac_f32_e64 v78, v180, v180\n"
      "v_mfma_f32_16x16x32_bf16 v[214:217], v[124:127], a[188:191], v[214:217]\n"
      "v_cndmask_b32 v218, v218, v128, s[60:61]\n"
      "v_cvt_pk_bf16_f32 v203, v204, v205\n"
      "v_mfma_f32_16x16x32_bf16 v[194:197], v[162:165], a[192:195], 0\n"
      "v_fmac_f32_e64 v129, v136, v136\n"
      "v_cvt_pk_bf16_f32 v205, v146, v147\n"
      "v_mfma_f32_16x16x32_bf16 v[194:197], v[190:193], a[196:199], v[194:197]\n"
      "v_fmac_f32 v129, v137, v137\n"
      "v_mul_f32 v144, v102, v144\n"
      "v_mfma_f32_16x16x32_bf16 v[194:197], v[96:99], a[200:203], v[194:197]\n"
      "v_cvt_pk_bf16_f32 v130, v130, v131\n"
      "v_fmac_f32_e64 v78, v181, v181\n"
      "v_mfma_f32_16x16x32_bf16 v[194:197], v[210:213], a[204:207], v[194:197]\n"
      "v_cvt_pk_bf16_f32 v148, v148, v149\n"
      "v_cvt_pk_bf16_f32 v204, v144, v145\n"
      "v_mfma_f32_16x16x32_bf16 v[194:197], v[154:157], a[208:211], v[194:197]\n"
      "ds_write_b128 v5, v[202:205] offset:12288\n"
      "v_mfma_f32_16x16x32_bf16 v[194:197], v[124:127], a[212:215], v[194:197]\n"
      "s_waitcnt lgkmcnt(1)\n"
      "v_fmac_f32 v78, v138, v138\n"
      "v_fmac_f32 v129, v220, v220\n"
      "v_mfma_f32_16x16x32_bf16 v[190:193], a[0:3], v[186:189], 0\n"
      "v_cvt_pk_bf16_f32 v214, v214, v215\n"
      "v_fmac_f32 v129, v221, v221\n"
      "v_mfma_f32_16x16x32_bf16 v[190:193], a[4:7], v[170:173], v[190:193]\n"
      "ds_read_b128 v[202:205], v9 offset:5632\n"
      "v_fmac_f32 v129, v222, v222\n"
      "v_mfma_f32_16x16x32_bf16 v[190:193], a[8:11], v[198:201], v[190:193]\n"
      "v_fmac_f32 v78, v139, v139\n"
      "v_fmac_f32_e64 v78, v140, v140\n"
      "v_mfma_f32_16x16x32_bf16 v[190:193], a[12:15], v[158:161], v[190:193]\n"
      "v_cvt_pk_bf16_f32 v149, v150, v151\n"
      "v_cvt_pk_bf16_f32 v194, v194, v195\n"
      "v_mfma_f32_16x16x32_bf16 v[190:193], a[16:19], v[166:169], v[190:193]\n"
      "s_or_b32 s95, s95, s48\n"
      "v_fmac_f32 v129, v223, v223\n"
      "v_mfma_f32_16x16x32_bf16 v[190:193], a[20:23], v[112:115], v[190:193]\n"
      "v_cvt_pk_bf16_f32 v131, v132, v133\n"
      "v_fmac_f32 v78, v141, v141\n"
      "v_mfma_f32_16x16x32_bf16 v[206:209], a[24:27], v[186:189], 0\n"
      "v_fmac_f32 v129, v106, v106\n"
      "v_fmac_f32_e64 v129, v107, v107\n"
      "v_mfma_f32_16x16x32_bf16 v[206:209], a[28:31], v[170:173], v[206:209]\n"
      "ds_read_b128 v[96:99], v0 offset:16384\n"
      "v_fmac_f32_e64 v129, v108, v108\n"
      "v_mfma_f32_16x16x32_bf16 v[206:209], a[32:35], v[198:201], v[206:209]\n"
      "ds_write_b64 v6, v[148:149] offset:12288\n"
      "v_mfma_f32_16x16x32_bf16 v[206:209], a[36:39], v[158:161], v[206:209]\n"
      "v_fmac_f32 v129, v109, v109\n"
      "v_mul_f32 v218, v152, v218\n"
      "v_mfma_f32_16x16x32_bf16 v[206:209], a[40:43], v[166:169], v[206:209]\n"
      "s_cmp_ge_u32 s95, 0x40000000\n"
      "s_cselect_b32 s94, -1, -2\n"
      "v_fma_f32 v133, v190, v190, s51\n"
      "v_mfma_f32_16x16x32_bf16 v[206:209], a[44:47], v[112:115], v[206:209]\n"
      "v_fmac_f32 v133, v191, v191\n"
      "v_fmac_f32 v133, v192, v192\n"
      "v_mfma_f32_16x16x32_bf16 v[210:213], a[48:51], v[186:189], 0\n"
      "v_permlane16_swap_b32 v78, v129\n"
      "v_mfma_f32_16x16x32_bf16 v[210:213], a[52:55], v[170:173], v[210:213]\n"
      "v_add_f32 v78, v78, v129\n"
      "v_mov_b32 v129, v78\n"
      "v_mfma_f32_16x16x32_bf16 v[210:213], a[56:59], v[198:201], v[210:213]\n"
      "s_nop 0\n"
      "v_permlane32_swap_b32 v78, v129\n"
      "v_mfma_f32_16x16x32_bf16 v[210:213], a[60:63], v[158:161], v[210:213]\n"
      "v_cvt_pk_bf16_f32 v215, v216, v217\n"
      "v_add_f32 v78, v78, v129\n"
      "v_mfma_f32_16x16x32_bf16 v[210:213], a[64:67], v[166:169], v[210:213]\n"
      "v_rsq_f32 v78, v78\n"
      "v_mfma_f32_16x16x32_bf16 v[210:213], a[68:71], v[112:115], v[210:213]\n"
      "s_cmp_gt_u32 s42, s44\n"
      "s_cselect_b32 s95, s94, s95\n"
      "v_fmac_f32 v133, v193, v193\n"
      "v_mfma_f32_16x16x32_bf16 v[162:165], a[72:75], v[186:189], 0\n"
      "v_cvt_pk_bf16_f32 v218, v218, v218\n"
      "v_mov_b32 v129, v78\n"
      "v_mfma_f32_16x16x32_bf16 v[162:165], a[76:79], v[170:173], v[162:165]\n"
      "s_nop 0\n"
      "v_permlane16_swap_b32_e64 v78, v129\n"
      "v_mfma_f32_16x16x32_bf16 v[162:165], a[80:83], v[198:201], v[162:165]\n"
      "ds_write_b16 v12, v218 offset:16\n"
      "v_mfma_f32_16x16x32_bf16 v[162:165], a[84:87], v[158:161], v[162:165]\n"
      "v_mul_f32 v137, v129, v137\n"
      "v_mul_f32 v106, v129, v106\n"
      "v_mfma_f32_16x16x32_bf16 v[162:165], a[88:91], v[166:169], v[162:165]\n"
      "s_waitcnt lgkmcnt(1)\n"
      "v_fmac_f32 v133, v206, v206\n"
      "v_or3_b32 v202, v202, v203, v204\n"
      "v_mfma_f32_16x16x32_bf16 v[162:165], a[92:95], v[112:115], v[162:165]\n"
      "v_fmac_f32 v133, v207, v207\n"
      "v_mul_f32 v78, s50, v78\n"
      "v_mfma_f32_16x16x32_bf16 v[100:103], a[96:99], v[186:189], 0\n"
      "v_mul_f32 v134, v129, v134\n"
      "v_fmac_f32 v133, v208, v208\n"
      "v_mfma_f32_16x16x32_bf16 v[100:103], a[100:103], v[170:173], v[100:103]\n"
      "v_mul_f32 v220, v129, v220\n"
      "v_mul_f32 v136, v129, v136\n"
      "v_mfma_f32_16x16x32_bf16 v[100:103], a[104:107], v[198:201], v[100:103]\n"
      "ds_read_b128 v[116:119], v1 offset:16384\n"
      "v_mul_f32 v180, v78, v180\n"
      "v_mfma_f32_16x16x32_bf16 v[100:103], a[108:111], v[158:161], v[100:103]\n"
      "ds_read_b128 v[148:151], v0 offset:10240\n"
      "v_fma_f32 v128, v162, v162, s51\n"
      "v_mfma_f32_16x16x32_bf16 v[100:103], a[112:115], v[166:169], v[100:103]\n"
      "v_mul_f32 v109, v129, v109\n"
      "v_mul_f32 v181, v78, v181\n"
      "v_mfma_f32_16x16x32_bf16 v[100:103], a[116:119], v[112:115], v[100:103]\n"
      "v_mul_f32 v135, v129, v135\n"
      "v_bitop3_b32 v202, v202, v205, s33 bitop3:0xa8\n"
      "v_mfma_f32_16x16x32_bf16 v[152:155], a[120:123], v[186:189], 0\n"
      "v_fmac_f32 v133, v209, v209\n"
      "v_fmac_f32 v128, v163, v163\n"
      "v_mfma_f32_16x16x32_bf16 v[152:155], a[124:127], v[170:173], v[152:155]\n"
      "v_mul_f32_e64 v138, v78, v138\n"
      "v_cvt_pk_bf16_f32 v134, v134, v135\n"
      "v_mfma_f32_16x16x32_bf16 v[152:155], a[128:131], v[198:201], v[152:155]\n"
      "v_fmac_f32 v128, v164, v164\n"
      "v_mul_f32 v139, v78, v139\n"
      "v_mfma_f32_16x16x32_bf16 v[152:155], a[132:135], v[158:161], v[152:155]\n"
      "v_mul_f32 v179, v78, v179\n"
      "v_fmac_f32 v128, v165, v165\n"
      "v_mfma_f32_16x16x32_bf16 v[152:155], a[136:139], v[166:169], v[152:155]\n"
      "v_mul_f32 v221, v129, v221\n"
      "v_cvt_pk_bf16_f32 v195, v196, v197\n"
      "v_mfma_f32_16x16x32_bf16 v[152:155], a[140:143], v[112:115], v[152:155]\n"
      "v_fmac_f32 v128, v100, v100\n"
      "v_mul_f32 v223, v129, v223\n"
      "v_mfma_f32_16x16x32_bf16 v[216:219], v[186:189], a[144:147], 0\n"
      "v_mul_f32 v222, v129, v222\n"
      "v_cvt_pk_bf16_f32 v135, v136, v137\n"
      "v_mfma_f32_16x16x32_bf16 v[216:219], v[170:173], a[148:151], v[216:219]\n"
      "v_mul_f32 v107, v129, v107\n"
      "v_fmac_f32 v128, v101, v101\n"
      "v_mfma_f32_16x16x32_bf16 v[216:219], v[198:201], a[152:155], v[216:219]\n"
      "v_cvt_pk_bf16_f32 v137, v222, v223\n"
      "v_cvt_pk_bf16_f32 v106, v106, v107\n"
      "v_mfma_f32_16x16x32_bf16 v[216:219], v[158:161], a[156:159], v[216:219]\n"
      "v_mul_f32 v122, v78, v122\n"
      "v_mul_f32 v108, v129, v108\n"
      "v_mfma_f32_16x16x32_bf16 v[216:219], v[166:169], a[160:163], v[216:219]\n"
      "v_mul_f32 v120, v78, v120\n"
      "v_fmac_f32 v133, v210, v210\n"
      "v_mfma_f32_16x16x32_bf16 v[216:219], v[112:115], a[164:167], v[216:219]\n"
      "v_fmac_f32 v128, v102, v102\n"
      "v_mul_f32 v141, v78, v141\n"
      "v_mfma_f32_16x16x32_bf16 v[124:127], v[186:189], a[168:171], 0\n"
      "v_fmac_f32 v128, v103, v103\n"
      "v_fmac_f32 v133, v211, v211\n"
      "v_mfma_f32_16x16x32_bf16 v[124:127], v[170:173], a[172:175], v[124:127]\n"
      "v_cmp_ne_u32 s[56:57], 0, v202\n"
      "v_cvt_pk_bf16_f32 v136, v220, v221\n"
      "v_mfma_f32_16x16x32_bf16 v[124:127], v[198:201], a[176:179], v[124:127]\n"
      "ds_read_b128 v[144:147], v1 offset:4096\n"
      "v_fmac_f32 v128, v152, v152\n"
      "v_mfma_f32_16x16x32_bf16 v[124:127], v[158:161], a[180:183], v[124:127]\n"
      "v_mul_f32 v178, v78, v178\n"
      "v_mul_f32_e64 v121, v78, v121\n"
      "v_mfma_f32_16x16x32_bf16 v[124:127], v[166:169], a[184:187], v[124:127]\n"
      "v_cvt_pk_bf16_f32 v132, v216, v217\n"
      "v_mul_f32 v123, v78, v123\n"
      "v_mfma_f32_16x16x32_bf16 v[124:127], v[112:115], a[188:191], v[124:127]\n"
      "v_fmac_f32 v128, v153, v153\n"
      "v_fmac_f32_e64 v128, v154, v154\n"
      "v_mfma_f32_16x16x32_bf16 v[220:223], v[186:189], a[192:195], 0\n"
      "ds_read_b128 v[202:205], v7 offset:18432\n"
      "v_cvt_pk_bf16_f32 v138, v138, v139\n"
      "v_mfma_f32_16x16x32_bf16 v[220:223], v[170:173], a[196:199], v[220:223]\n"
      "ds_read_b128 v[186:189], v1 offset:10240\n"
      "v_fmac_f32 v133, v212, v212\n"
      "v_mfma_f32_16x16x32_bf16 v[220:223], v[198:201], a[200:203], v[220:223]\n"
      "v_fmac_f32 v128, v155, v155\n"
      "v_fmac_f32 v133, v213, v213\n"
      "v_mfma_f32_16x16x32_bf16 v[220:223], v[158:161], a[204:207], v[220:223]\n"
      "s_nop 0\n"
      "v_permlane16_swap_b32 v133, v128\n"
      "v_mfma_f32_16x16x32_bf16 v[220:223], v[166:169], a[208:211], v[220:223]\n"
      "v_add_f32 v133, v133, v128\n"
      "v_mov_b32 v128, v133\n"
      "v_mfma_f32_16x16x32_bf16 v[220:223], v[112:115], a[212:215], v[220:223]\n"
      "s_nop 0\n"
      "v_permlane32_swap_b32 v133, v128\n"
      "v_mfma_f32_16x16x32_bf16 v[158:161], a[0:3], v[174:177], 0\n"
      "s_waitcnt lgkmcnt(1)\n"
      "v_cvt_pk_bf16_f32 v120, v120, v121\n"
      "v_add_f32 v133, v133, v128\n"
      "v_mfma_f32_16x16x32_bf16 v[158:161], a[4:7], v[144:147], v[158:161]\n"
      "s_waitcnt lgkmcnt(0)\n"
      "v_rsq_f32_e64 v133, v133\n"
      "v_mfma_f32_16x16x32_bf16 v[158:161], a[8:11], v[148:151], v[158:161]\n"
      "ds_read_b128 v[166:169], v7 offset:26624\n"
      "v_mov_b32 v128, v133\n"
      "v_mfma_f32_16x16x32_bf16 v[158:161], a[12:15], v[186:189], v[158:161]\n"
      "s_nop 0\n"
      "v_permlane16_swap_b32 v133, v128\n"
      "v_mfma_f32_16x16x32_bf16 v[158:161], a[16:19], v[96:99], v[158:161]\n"
      "v_cvt_pk_bf16_f32 v121, v122, v123\n"
      "v_mul_f32 v165, v128, v165\n"
      "v_mfma_f32_16x16x32_bf16 v[158:161], a[20:23], v[116:119], v[158:161]\n"
      "v_cvt_pk_bf16_f32 v107, v108, v109\n"
      "v_mul_f32 v153, v128, v153\n"
      "v_mfma_f32_16x16x32_bf16 v[170:173], a[24:27], v[174:177], 0\n"
      "v_mul_f32 v133, s50, v133\n"
      "v_mul_f32_e64 v101, v128, v101\n"
      "v_mfma_f32_16x16x32_bf16 v[170:173], a[28:31], v[144:147], v[170:173]\n"
      "v_mul_f32 v164, v128, v164\n"
      "v_mul_f32 v154, v128, v154\n"
      "v_mfma_f32_16x16x32_bf16 v[170:173], a[32:35], v[148:151], v[170:173]\n"
      "v_cvt_pk_bf16_f32 v122, v178, v179\n"
      "v_mul_f32 v162, v128, v162\n"
      "v_mfma_f32_16x16x32_bf16 v[170:173], a[36:39], v[186:189], v[170:173]\n"
      "v_cvt_pk_bf16_f32 v197, v222, v223\n"
      "v_mul_f32 v140, v78, v140\n"
      "v_mfma_f32_16x16x32_bf16 v[170:173], a[40:43], v[96:99], v[170:173]\n"
      "v_mul_f32 v192, v133, v192\n"
      "v_mul_f32 v163, v128, v163\n"
      "v_mfma_f32_16x16x32_bf16 v[170:173], a[44:47], v[116:119], v[170:173]\n"
      "ds_read_b128 v[112:115], v7 offset:22528\n"
      "v_mul_f32_e64 v190, v133, v190\n"
      "v_mfma_f32_16x16x32_bf16 v[198:201], a[48:51], v[174:177], 0\n"
      "v_mul_f32 v100, v128, v100\n"
      "v_mul_f32 v152, v128, v152\n"
      "v_mfma_f32_16x16x32_bf16 v[198:201], a[52:55], v[144:147], v[198:201]\n"
      "v_fma_f32 v81, v158, v158, s51\n"
      "v_mul_f32_e64 v191, v133, v191\n"
      "v_mfma_f32_16x16x32_bf16 v[198:201], a[56:59], v[148:151], v[198:201]\n"
      "s_cmp_ge_u32 s95, 0x40000000\n"
      "s_cselect_b64 vcc, -1, s[56:57]\n"
      "v_cvt_pk_bf16_f32 v123, v180, v181\n"
      "v_mfma_f32_16x16x32_bf16 v[198:201], a[60:63], v[186:189], v[198:201]\n"
      "v_cvt_pk_bf16_f32 v162, v162, v163\n"
      "v_fmac_f32_e64 v81, v159, v159\n"
      "v_mfma_f32_16x16x32_bf16 v[198:201], a[64:67], v[96:99], v[198:201]\n"
      "v_cvt_pk_bf16_f32 v152, v152, v153\n"
      "v_cvt_pk_bf16_f32 v139, v140, v141\n"
      "v_mfma_f32_16x16x32_bf16 v[198:201], a[68:71], v[116:119], v[198:201]\n"
      "ds_read_u16 v78, v11\n"
      "v_mul_f32 v103, v128, v103\n"
      "v_mfma_f32_16x16x32_bf16 v[178:181], a[72:75], v[174:177], 0\n"
      "s_add_u32 s42, s42, 1\n"
      "v_mul_f32 v193, v133, v193\n"
      "v_mul_f32 v102, v128, v102\n"
      "v_mfma_f32_16x16x32_bf16 v[178:181], a[76:79], v[144:147], v[178:181]\n"
      "buffer_store_dwordx4 v[202:205], v20, s[24:27], s46 offen sc1\n"
      "v_mfma_f32_16x16x32_bf16 v[178:181], a[80:83], v[148:151], v[178:181]\n"
      "v_fmac_f32_e64 v81, v160, v160\n"
      "v_cvt_pk_bf16_f32 v163, v164, v165\n"
      "v_mfma_f32_16x16x32_bf16 v[178:181], a[84:87], v[186:189], v[178:181]\n"
      "v_cvt_pk_bf16_f32 v190, v190, v191\n"
      "v_cvt_pk_bf16_f32 v196, v220, v221\n"
      "v_mfma_f32_16x16x32_bf16 v[178:181], a[88:91], v[96:99], v[178:181]\n"
      "v_fmac_f32 v81, v161, v161\n"
      "v_cvt_pk_bf16_f32 v164, v100, v101\n"
      "v_mfma_f32_16x16x32_bf16 v[178:181], a[92:95], v[116:119], v[178:181]\n"
      "v_cvt_pk_bf16_f32 v165, v102, v103\n"
      "v_fmac_f32 v81, v170, v170\n"
      "v_mfma_f32_16x16x32_bf16 v[220:223], a[96:99], v[174:177], 0\n"
      "v_mul_f32 v155, v128, v155\n"
      "v_fmac_f32 v81, v171, v171\n"
      "v_mfma_f32_16x16x32_bf16 v[220:223], a[100:103], v[144:147], v[220:223]\n"
      "v_fmac_f32 v81, v172, v172\n"
      "v_fmac_f32 v81, v173, v173\n"
      "v_mfma_f32_16x16x32_bf16 v[220:223], a[104:107], v[148:151], v[220:223]\n"
      "s_waitcnt lgkmcnt(0)\n"
      "v_fmac_f32 v81, v198, v198\n"
      "v_fma_f32 v216, v178, v178, s51\n"
      "v_mfma_f32_16x16x32_bf16 v[220:223], a[108:111], v[186:189], v[220:223]\n"
      "buffer_store_dwordx4 v[112:115], v21, s[24:27], s46 offen sc1\n"
      "v_pk_mul_f32 v[206:207], v[206:207], v[132:133] op_sel:[0,1]\n"
      "v_mfma_f32_16x16x32_bf16 v[220:223], a[112:115], v[96:99], v[220:223]\n"
      "v_fmac_f32_e64 v216, v179, v179\n"
      "v_cvt_pk_bf16_f32 v153, v154, v155\n"
      "v_mfma_f32_16x16x32_bf16 v[220:223], a[116:119], v[116:119], v[220:223]\n"
      "v_mul_f32 v213, v133, v213\n"
      "v_fmac_f32 v81, v199, v199\n"
      "v_mfma_f32_16x16x32_bf16 v[100:103], a[120:123], v[174:177], 0\n"
      "v_fmac_f32 v216, v180, v180\n"
      "v_cvt_pk_bf16_f32 v191, v192, v193\n"
      "v_mfma_f32_16x16x32_bf16 v[100:103], a[124:127], v[144:147], v[100:103]\n"
      "v_fmac_f32 v81, v200, v200\n"
      "v_fmac_f32_e64 v216, v181, v181\n"
      "v_mfma_f32_16x16x32_bf16 v[100:103], a[128:131], v[148:151], v[100:103]\n"
      "v_fmac_f32 v216, v220, v220\n"
      "v_fmac_f32 v216, v221, v221\n"
      "v_mfma_f32_16x16x32_bf16 v[100:103], a[132:135], v[186:189], v[100:103]\n"
      "v_fmac_f32 v216, v222, v222\n"
      "v_fmac_f32 v216, v223, v223\n"
      "v_mfma_f32_16x16x32_bf16 v[100:103], a[136:139], v[96:99], v[100:103]\n"
      "ds_read_b128 v[202:205], v7 offset:30720\n"
      "v_fmac_f32_e64 v81, v201, v201\n"
      "v_mfma_f32_16x16x32_bf16 v[100:103], a[140:143], v[116:119], v[100:103]\n"
      "buffer_store_dwordx4 v[166:169], v22, s[24:27], s46 offen sc1\n"
      "v_pk_mul_f32 v[208:209], v[208:209], v[132:133] op_sel:[0,1]\n"
      "v_pk_mul_f32 v[210:211], v[210:211], v[132:133] op_sel:[0,1]\n"
      "v_mfma_f32_16x16x32_bf16 v[140:143], v[174:177], a[144:147], 0\n"
      "v_cvt_pk_bf16_f32 v217, v126, v127\n"
      "v_cvt_pk_bf16_f32 v192, v206, v207\n"
      "v_mfma_f32_16x16x32_bf16 v[140:143], v[144:147], a[148:151], v[140:143]\n"
      "s_nop 0\n"
      "v_fmac_f32 v216, v100, v100\n"
      "v_fmac_f32 v216, v101, v101\n"
      "v_mfma_f32_16x16x32_bf16 v[140:143], v[148:151], a[152:155], v[140:143]\n"
      "ds_read_b128 v[126:129], v2 offset:27648\n"
      "v_fmac_f32 v216, v102, v102\n"
      "v_mfma_f32_16x16x32_bf16 v[140:143], v[186:189], a[156:159], v[140:143]\n"
      "ds_read_b128 v[166:169], v7 offset:34816\n"
      "v_fmac_f32 v216, v103, v103\n"
      "v_mfma_f32_16x16x32_bf16 v[140:143], v[96:99], a[160:163], v[140:143]\n"
      "s_nop 0\n"
      "v_permlane16_swap_b32_e64 v81, v216\n"
      "v_mfma_f32_16x16x32_bf16 v[140:143], v[116:119], a[164:167], v[140:143]\n"
      "s_waitcnt lgkmcnt(2)\n"
      "v_add_f32 v81, v81, v216\n"
      "v_mov_b32 v216, v81\n"
      "v_mfma_f32_16x16x32_bf16 v[108:111], v[174:177], a[168:171], 0\n"
      "s_nop 0\n"
      "v_permlane32_swap_b32 v81, v216\n"
      "v_mfma_f32_16x16x32_bf16 v[108:111], v[144:147], a[172:175], v[108:111]\n"
      "buffer_store_dwordx4 v[202:205], v23, s[24:27], s46 offen sc1\n"
      "v_mfma_f32_16x16x32_bf16 v[108:111], v[148:151], a[176:179], v[108:111]\n"
      "v_add_f32 v81, v81, v216\n"
      "v_cvt_pk_bf16_f32 v193, v208, v209\n"
      "v_mfma_f32_16x16x32_bf16 v[108:111], v[186:189], a[180:183], v[108:111]\n"
      "s_waitcnt lgkmcnt(0)\n"
      "v_rsq_f32 v81, v81\n"
      "v_mfma_f32_16x16x32_bf16 v[108:111], v[96:99], a[184:187], v[108:111]\n"
      "ds_read_b128 v[202:205], v2 offset:21504\n"
      "v_mov_b32 v216, v81\n"
      "v_mfma_f32_16x16x32_bf16 v[108:111], v[116:119], a[188:191], v[108:111]\n"
      "s_nop 0\n"
      "v_permlane16_swap_b32_e64 v81, v216\n"
      "v_mfma_f32_16x16x32_bf16 v[154:157], v[174:177], a[192:195], 0\n"
      "ds_read_b128 v[206:209], v2 offset:24576\n"
      "v_mul_f32_e64 v100, v216, v100\n"
      "v_mfma_f32_16x16x32_bf16 v[154:157], v[144:147], a[196:199], v[154:157]\n"
      "ds_read_b128 v[174:177], v2 offset:18432\n"
      "v_mul_f32 v81, s50, v81\n"
      "v_mfma_f32_16x16x32_bf16 v[154:157], v[148:151], a[200:203], v[154:157]\n"
      "ds_read_b128 v[144:147], v10 offset:32\n"
      "v_mul_f32 v212, v133, v212\n"
      "v_mfma_f32_16x16x32_bf16 v[154:157], v[186:189], a[204:207], v[154:157]\n"
      "ds_read_b64 v[104:105], v10 offset:24\n"
      "v_mul_f32_e64 v101, v216, v101\n"
      "v_mfma_f32_16x16x32_bf16 v[154:157], v[96:99], a[208:211], v[154:157]\n"
      "buffer_store_dwordx4 v[166:169], v25, s[24:27], s46 offen sc1\n"
      "v_pk_mul_f32 v[160:161], v[160:161], v[80:81] op_sel:[0,1]\n"
      "v_pk_mul_f32 v[102:103], v[102:103], v[216:217] op_sel_hi:[1,0]\n"
      "v_pk_mul_f32 v[178:179], v[178:179], v[216:217] op_sel_hi:[1,0]\n"
      "v_pk_mul_f32 v[220:221], v[220:221], v[216:217] op_sel_hi:[1,0]\n"
      "v_pk_mul_f32 v[170:171], v[170:171], v[80:81] op_sel:[0,1]\n"
      "v_pk_mul_f32 v[180:181], v[180:181], v[216:217] op_sel_hi:[1,0]\n"
      "v_mfma_f32_16x16x32_bf16 v[154:157], v[116:119], a[212:215], v[154:157]\n"
      "s_waitcnt lgkmcnt(2)\n"
      "v_mul_f32 v223, v216, v223\n"
      "v_cvt_pk_bf16_f32 v77, v142, v143\n"
      "v_mfma_f32_16x16x32_bf16 v[166:169], a[216:219], v[174:177], 0\n"
      "ds_read_b128 v[186:189], v2 offset:30720\n"
      "v_mul_f32 v159, v81, v159\n"
      "v_mfma_f32_16x16x32_bf16 v[166:169], a[220:223], v[202:205], v[166:169]\n"
      "v_mul_f32 v222, v216, v222\n"
      "v_mul_f32_e64 v158, v81, v158\n"
      "v_mfma_f32_16x16x32_bf16 v[166:169], a[224:227], v[206:209], v[166:169]\n"
      "v_cvt_pk_bf16_f32 v210, v210, v211\n"
      "v_mul_f32 v198, v81, v198\n"
      "v_mfma_f32_16x16x32_bf16 v[112:115], a[240:243], v[174:177], 0\n"
      "v_mul_f32 v200, v81, v200\n"
      "v_cvt_pk_bf16_f32 v133, v218, v219\n"
      "v_mfma_f32_16x16x32_bf16 v[112:115], a[244:247], v[202:205], v[112:115]\n"
      "ds_read_u16 v82, v11 offset:32\n"
      "v_cvt_pk_bf16_f32 v178, v178, v179\n"
      "v_mfma_f32_16x16x32_bf16 v[112:115], a[248:251], v[206:209], v[112:115]\n"
      "v_cvt_pk_bf16_f32 v216, v124, v125\n"
      "v_cvt_pk_bf16_f32 v100, v100, v101\n"
      "v_mfma_f32_16x16x32_bf16 v[116:119], v[232:235], v[174:177], 0\n"
      "v_cvt_pk_bf16_f32 v179, v180, v181\n"
      "v_mul_f32 v173, v81, v173\n"
      "v_mfma_f32_16x16x32_bf16 v[116:119], v[236:239], v[202:205], v[116:119]\n"
      "ds_read_b128 v[174:177], v2 offset:33792\n"
      "v_mul_f32 v201, v81, v201\n"
      "v_mfma_f32_16x16x32_bf16 v[116:119], v[240:243], v[206:209], v[116:119]\n"
      "v_cvt_pk_bf16_f32 v158, v158, v159\n"
      "v_cvt_pk_bf16_f32 v181, v222, v223\n"
      "v_mfma_f32_16x16x32_bf16 v[202:205], v[134:137], v[120:123], v[40:43]\n"
      "v_cvt_pk_bf16_f32 v180, v220, v221\n"
      "v_cvt_pk_bf16_f32 v76, v140, v141\n"
      "v_mfma_f32_16x16x32_bf16 v[148:151], v[162:165], v[120:123], v[44:47]\n"
      "s_waitcnt lgkmcnt(1)\n"
      "v_cvt_pk_bf16_f32 v101, v102, v103\n"
      "v_cvt_pk_bf16_f32 v84, v154, v155\n"
      "v_mfma_f32_16x16x32_bf16 v[218:221], v[178:181], v[120:123], v[48:51]\n"
      "ds_read_b128 v[140:143], v2 offset:24832\n"
      "v_cvt_pk_bf16_f32 v211, v212, v213\n"
      "v_mfma_f32_16x16x32_bf16 v[206:209], v[144:147], v[120:123], v[24:27]\n"
      "v_cvt_pk_bf16_f32 v80, v108, v109\n"
      "v_cvt_pk_bf16_f32 v85, v156, v157\n"
      "v_mfma_f32_16x16x16_bf16 v[202:205], v[106:107], v[138:139], v[202:205]\n"
      "v_mul_f32 v172, v81, v172\n"
      "v_mul_f32 v199, v81, v199\n"
      "v_cvt_pk_bf16_f32 v159, v160, v161\n"
      "v_mfma_f32_16x16x16_bf16 v[148:151], v[152:153], v[138:139], v[148:151]\n"
      "v_cvt_pk_bf16_f32 v161, v172, v173\n"
      "v_cvt_pk_bf16_f32 v198, v198, v199\n"
      "s_waitcnt vmcnt(6)\n"
      "v_or3_b32 v182, v182, v183, v184\n"
      "v_mfma_f32_16x16x16_bf16 v[218:221], v[100:101], v[138:139], v[218:221]\n"
      "v_cvt_pk_bf16_f32 v81, v110, v111\n"
      "v_max3_f32 v213, v202, v203, v204\n"
      "v_cvt_pk_bf16_f32 v160, v170, v171\n"
      "v_mfma_f32_16x16x16_bf16 v[206:209], v[104:105], v[138:139], v[206:209]\n"
      "v_max3_f32 v156, v205, v148, v149\n"
      "v_cvt_pk_bf16_f32 v199, v200, v201\n"
      "v_max3_f32 v213, v213, v150, v151\n"
      "v_mfma_f32_16x16x32_bf16 v[166:169], a[228:231], v[126:129], v[166:169]\n"
      "s_waitcnt lgkmcnt(0)\n"
      "v_max3_f32 v213, v213, v220, v221\n"
      "v_max3_f32 v156, v156, v218, v219\n"
      "v_mfma_f32_16x16x32_bf16 v[166:169], a[232:235], v[186:189], v[166:169]\n"
      "v_max3_f32 v213, v213, v156, v206\n"
      "v_mov_b32 v156, v213\n"
      "v_mfma_f32_16x16x32_bf16 v[166:169], a[236:239], v[174:177], v[166:169]\n"
      "s_nop 0\n"
      "v_permlane16_swap_b32 v213, v156\n"
      "v_mfma_f32_16x16x32_bf16 v[112:115], a[252:255], v[126:129], v[112:115]\n"
      "v_max_f32 v213, v213, v156\n"
      "v_mov_b32 v156, v213\n"
      "v_mfma_f32_16x16x32_bf16 v[112:115], v[224:227], v[186:189], v[112:115]\n"
      "s_nop 0\n"
      "v_permlane32_swap_b32 v213, v156\n"
      "v_max_f32_e64 v213, v213, v156\n"
      "v_mfma_f32_16x16x32_bf16 v[112:115], v[228:231], v[174:177], v[112:115]\n"
      "ds_read_b128 v[154:157], v2 offset:18688\n"
      "v_sub_f32 v220, v220, v213\n"
      "v_mfma_f32_16x16x32_bf16 v[116:119], v[244:247], v[126:129], v[116:119]\n"
      "v_sub_f32 v221, v221, v213\n"
      "v_sub_f32 v151, v151, v213\n"
      "v_sub_f32 v149, v149, v213\n"
      "v_mfma_f32_16x16x32_bf16 v[116:119], v[248:251], v[186:189], v[116:119]\n"
      "v_sub_f32 v205, v205, v213\n"
      "v_sub_f32 v218, v218, v213\n"
      "v_mfma_f32_16x16x32_bf16 v[116:119], v[252:255], v[174:177], v[116:119]\n"
      "ds_read_b128 v[126:129], v2 offset:21760\n"
      "v_exp_f32 v151, v151\n"
      "v_mfma_f32_16x16x32_bf16 v[108:111], v[134:137], v[190:193], v[52:55]\n"
      "v_exp_f32 v218, v218\n"
      "v_mfma_f32_16x16x32_bf16 v[96:99], v[162:165], v[190:193], v[56:59]\n"
      "v_sub_f32 v219, v219, v213\n"
      "v_sub_f32 v148, v148, v213\n"
      "v_mfma_f32_16x16x32_bf16 v[170:173], v[178:181], v[190:193], v[60:63]\n"
      "v_exp_f32 v221, v221\n"
      "v_mfma_f32_16x16x32_bf16 v[120:123], v[144:147], v[190:193], v[26:29]\n"
      "s_waitcnt lgkmcnt(1)\n"
      "v_exp_f32 v220, v220\n"
      "v_sub_f32 v206, v206, v213\n"
      "v_mfma_f32_16x16x16_bf16 v[108:111], v[106:107], v[210:211], v[108:111]\n"
      "v_exp_f32 v206, v206\n"
      "v_sub_f32 v202, v202, v213\n"
      "v_sub_f32 v150, v150, v213\n"
      "v_mfma_f32_16x16x16_bf16 v[96:99], v[152:153], v[210:211], v[96:99]\n"
      "v_sub_f32 v203, v203, v213\n"
      "v_exp_f32 v150, v150\n"
      "v_mfma_f32_16x16x16_bf16 v[170:173], v[100:101], v[210:211], v[170:173]\n"
      "v_exp_f32 v203, v203\n"
      "v_sub_f32_e64 v204, v204, v213\n"
      "v_mfma_f32_16x16x16_bf16 v[120:123], v[104:105], v[210:211], v[120:123]\n"
      "v_exp_f32 v149, v149\n"
      "v_max3_f32 v103, v108, v109, v110\n"
      "v_mfma_f32_16x16x32_bf16 v[186:189], a[216:219], v[154:157], 0\n"
      "s_waitcnt lgkmcnt(0)\n"
      "v_cvt_pk_bf16_f32 v89, v220, v221\n"
      "v_cvt_pk_bf16_f32 v90, v206, 0\n"
      "v_mfma_f32_16x16x32_bf16 v[186:189], a[220:223], v[126:129], v[186:189]\n"
      "v_exp_f32_e64 v202, v202\n"
      "v_mfma_f32_16x16x32_bf16 v[186:189], a[224:227], v[140:143], v[186:189]\n"
      "v_cvt_pk_bf16_f32 v202, v202, v203\n"
      "v_max3_f32 v103, v103, v98, v99\n"
      "v_mfma_f32_16x16x32_bf16 v[174:177], a[240:243], v[154:157], 0\n"
      "v_exp_f32_e64 v204, v204\n"
      "v_mfma_f32_16x16x32_bf16 v[174:177], a[244:247], v[126:129], v[174:177]\n"
      "v_exp_f32_e64 v205, v205\n"
      "v_mfma_f32_16x16x32_bf16 v[174:177], a[248:251], v[140:143], v[174:177]\n"
      "v_cvt_pk_bf16_f32 v203, v204, v205\n"
      "v_exp_f32_e64 v219, v219\n"
      "v_mfma_f32_16x16x32_bf16 v[206:209], v[232:235], v[154:157], 0\n"
      "v_exp_f32_e64 v148, v148\n"
      "v_cvt_pk_bf16_f32 v205, v150, v151\n"
      "v_max3_f32 v211, v111, v96, v97\n"
      "v_mfma_f32_16x16x32_bf16 v[206:209], v[236:239], v[126:129], v[206:209]\n"
      "v_cvt_pk_bf16_f32 v204, v148, v149\n"
      "v_max3_f32 v211, v211, v170, v171\n"
      "v_mfma_f32_16x16x32_bf16 v[206:209], v[240:243], v[140:143], v[206:209]\n"
      "ds_read_u16 v86, v11 offset:64\n"
      "v_cvt_pk_bf16_f32 v88, v218, v219\n"
      "v_mfma_f32_16x16x32_bf16 v[148:151], v[32:35], v[202:205], 0\n"
      "s_waitcnt vmcnt(5)\n"
      "v_or3_b32 v182, v182, v185, v92\n"
      "v_max3_f32 v103, v103, v172, v173\n"
      "v_mfma_f32_16x16x32_bf16 v[148:151], v[32:35], v[88:91], v[148:151]\n"
      "ds_read_b128 v[154:157], v2 offset:27904\n"
      "v_max3_f32 v103, v103, v211, v120\n"
      "v_mfma_f32_16x16x32_bf16 v[210:213], v[130:133], v[202:205], 0\n"
      "v_mul_f32 v143, v167, v167\n"
      "v_mov_b32_e64 v140, v103\n"
      "v_mfma_f32_16x16x32_bf16 v[210:213], v[76:79], v[88:91], v[210:213]\n"
      "s_nop 0\n"
      "v_permlane16_swap_b32 v103, v140\n"
      "v_max_f32 v103, v103, v140\n"
      "v_mfma_f32_16x16x32_bf16 v[218:221], v[214:217], v[202:205], 0\n"
      "v_rcp_f32 v148, v148\n"
      "v_mov_b32 v140, v103\n"
      "v_mfma_f32_16x16x32_bf16 v[218:221], v[80:83], v[88:91], v[218:221]\n"
      "s_nop 0\n"
      "v_permlane32_swap_b32 v103, v140\n"
      "v_mfma_f32_16x16x32_bf16 v[126:129], v[194:197], v[202:205], 0\n"
      "s_waitcnt lgkmcnt(1)\n"
      "v_max_f32 v103, v103, v140\n"
      "v_sub_f32 v120, v120, v103\n"
      "v_mfma_f32_16x16x32_bf16 v[126:129], v[84:87], v[88:91], v[126:129]\n"
      "v_sub_f32 v108, v108, v103\n"
      "v_sub_f32 v173, v173, v103\n"
      "v_sub_f32 v109, v109, v103\n"
      "v_sub_f32 v99, v99, v103\n"
      "v_sub_f32 v170, v170, v103\n"
      "v_pk_mul_f32 v[212:213], v[212:213], v[148:149] op_sel_hi:[1,0]\n"
      "v_exp_f32 v108, v108\n"
      "v_pk_mul_f32 v[210:211], v[210:211], v[148:149] op_sel_hi:[1,0]\n"
      "v_pk_mul_f32 v[220:221], v[220:221], v[148:149] op_sel_hi:[1,0]\n"
      "v_sub_f32 v111, v111, v103\n"
      "v_cvt_pk_bf16_f32 v210, v210, v211\n"
      "v_pk_mul_f32 v[218:219], v[218:219], v[148:149] op_sel_hi:[1,0]\n"
      "v_sub_f32 v110, v110, v103\n"
      "v_pk_mul_f32 v[126:127], v[126:127], v[148:149] op_sel_hi:[1,0]\n"
      "v_exp_f32 v111, v111\n"
      "v_exp_f32 v109, v109\n"
      "v_cvt_pk_bf16_f32 v211, v212, v213\n"
      "v_cvt_pk_bf16_f32 v126, v126, v127\n"
      "v_pk_mul_f32 v[128:129], v[128:129], v[148:149] op_sel_hi:[1,0]\n"
      "v_sub_f32 v172, v172, v103\n"
      "v_sub_f32 v97, v97, v103\n"
      "v_cvt_pk_bf16_f32 v127, v128, v129\n"
      "v_sub_f32_e64 v98, v98, v103\n"
      "v_mfma_f32_16x16x32_bf16 v[190:193], v[134:137], v[158:161], v[64:67]\n"
      "v_cvt_pk_bf16_f32 v212, v218, v219\n"
      "v_exp_f32 v110, v110\n"
      "v_cvt_pk_bf16_f32 v108, v108, v109\n"
      "v_mfma_f32_16x16x32_bf16 v[138:141], v[162:165], v[158:161], v[68:71]\n"
      "s_waitcnt lgkmcnt(0)\n"
      "v_exp_f32 v97, v97\n"
      "v_mfma_f32_16x16x32_bf16 v[122:125], v[178:181], v[158:161], v[72:75]\n"
      "ds_read_b128 v[148:151], v2 offset:30976\n"
      "v_sub_f32 v171, v171, v103\n"
      "v_mfma_f32_16x16x32_bf16 v[200:203], v[144:147], v[158:161], v[28:31]\n"
      "ds_read_b128 v[158:161], v2 offset:34048\n"
      "v_cvt_pk_bf16_f32 v109, v110, v111\n"
      "v_mfma_f32_16x16x16_bf16 v[190:193], v[106:107], v[198:199], v[190:193]\n"
      "v_cvt_pk_bf16_f32 v213, v220, v221\n"
      "v_exp_f32 v171, v171\n"
      "v_mfma_f32_16x16x16_bf16 v[138:141], v[152:153], v[198:199], v[138:141]\n"
      "v_exp_f32 v170, v170\n"
      "v_sub_f32 v96, v96, v103\n"
      "v_mfma_f32_16x16x16_bf16 v[122:125], v[100:101], v[198:199], v[122:125]\n"
      "v_exp_f32 v96, v96\n"
      "v_exp_f32 v120, v120\n"
      "v_mfma_f32_16x16x16_bf16 v[200:203], v[104:105], v[198:199], v[200:203]\n"
      "v_cvt_pk_bf16_f32 v110, v96, v97\n"
      "v_exp_f32 v173, v173\n"
      "v_mfma_f32_16x16x32_bf16 v[186:189], a[228:231], v[154:157], v[186:189]\n"
      "s_waitcnt lgkmcnt(1)\n"
      "v_exp_f32 v99, v99\n"
      "v_mfma_f32_16x16x32_bf16 v[186:189], a[232:235], v[148:151], v[186:189]\n"
      "s_waitcnt lgkmcnt(0)\n"
      "v_exp_f32 v98, v98\n"
      "v_mfma_f32_16x16x32_bf16 v[186:189], a[236:239], v[158:161], v[186:189]\n"
      "v_cvt_pk_bf16_f32 v111, v98, v99\n"
      "v_max3_f32 v221, v193, v138, v139\n"
      "v_mfma_f32_16x16x32_bf16 v[174:177], a[252:255], v[154:157], v[174:177]\n"
      "v_cvt_pk_bf16_f32 v92, v170, v171\n"
      "v_fma_f32 v142, v166, v166, s52\n"
      "v_mfma_f32_16x16x32_bf16 v[174:177], v[224:227], v[148:151], v[174:177]\n"
      "v_exp_f32_e64 v172, v172\n"
      "v_mfma_f32_16x16x32_bf16 v[174:177], v[228:231], v[158:161], v[174:177]\n"
      "ds_write_b128 v3, v[210:213]\n"
      "v_pk_fma_f32 v[142:143], v[168:169], v[168:169], v[142:143]\n"
      "v_mfma_f32_16x16x32_bf16 v[206:209], v[244:247], v[154:157], v[206:209]\n"
      "v_max3_f32 v90, v190, v191, v192\n"
      "v_max3_f32 v90, v90, v140, v141\n"
      "v_mfma_f32_16x16x32_bf16 v[206:209], v[248:251], v[148:151], v[206:209]\n"
      "v_max3_f32 v90, v90, v124, v125\n"
      "v_cvt_pk_bf16_f32 v94, v120, 0\n"
      "v_mfma_f32_16x16x32_bf16 v[206:209], v[252:255], v[158:161], v[206:209]\n"
      "ds_read_b128 v[202:205], v10\n"
      "v_cvt_pk_bf16_f32 v93, v172, v173\n"
      "v_mfma_f32_16x16x32_bf16 v[148:151], v[32:35], v[108:111], 0\n"
      "ds_read_b64 v[102:103], v10 offset:16\n"
      "v_max3_f32 v221, v221, v122, v123\n"
      "v_mfma_f32_16x16x32_bf16 v[148:151], v[32:35], v[92:95], v[148:151]\n"
      "v_max3_f32 v90, v90, v221, v200\n"
      "v_mov_b32 v173, v90\n"
      "v_mfma_f32_16x16x32_bf16 v[210:213], v[130:133], v[108:111], 0\n"
      "ds_write_b64 v4, v[126:127]\n"
      "v_mfma_f32_16x16x32_bf16 v[210:213], v[76:79], v[92:95], v[210:213]\n"
      "v_permlane16_swap_b32 v90, v173\n"
      "v_mfma_f32_16x16x32_bf16 v[154:157], v[214:217], v[108:111], 0\n"
      "s_nop 0\n"
      "v_rcp_f32 v148, v148\n"
      "v_mfma_f32_16x16x32_bf16 v[154:157], v[80:83], v[92:95], v[154:157]\n"
      "ds_read_b128 v[218:221], v2 offset:18944\n"
      "v_max_f32 v90, v90, v173\n"
      "v_mfma_f32_16x16x32_bf16 v[158:161], v[194:197], v[108:111], 0\n"
      "v_mov_b32 v173, v90\n"
      "v_fma_f32 v120, v186, v186, s52\n"
      "v_mfma_f32_16x16x32_bf16 v[158:161], v[84:87], v[92:95], v[158:161]\n"
      "s_waitcnt lgkmcnt(2)\n"
      "v_permlane32_swap_b32 v90, v173\n"
      "v_max_f32 v90, v90, v173\n"
      "v_mfma_f32_16x16x32_bf16 v[126:129], v[202:205], v[134:137], v[30:33]\n"
      "v_sub_f32 v123, v123, v90\n"
      "v_sub_f32 v193, v193, v90\n"
      "v_sub_f32 v190, v190, v90\n"
      "v_sub_f32 v191, v191, v90\n"
      "v_mfma_f32_16x16x32_bf16 v[134:137], v[202:205], v[162:165], v[30:33]\n"
      "ds_read_b128 v[96:99], v2 offset:25088\n"
      "v_sub_f32 v141, v141, v90\n"
      "v_mfma_f32_16x16x32_bf16 v[170:173], v[202:205], v[178:181], v[30:33]\n"
      "ds_read_b128 v[162:165], v2 offset:22016\n"
      "v_sub_f32 v192, v192, v90\n"
      "v_mfma_f32_16x16x32_bf16 v[178:181], v[202:205], v[144:147], v[30:33]\n"
      "s_waitcnt lgkmcnt(2)\n"
      "v_exp_f32_e64 v191, v191\n"
      "v_mfma_f32_16x16x16_bf16 v[126:129], v[102:103], v[106:107], v[126:129]\n"
      "v_sub_f32 v139, v139, v90\n"
      "v_exp_f32 v123, v123\n"
      "v_mfma_f32_16x16x16_bf16 v[134:137], v[102:103], v[152:153], v[134:137]\n"
      "v_sub_f32 v200, v200, v90\n"
      "v_exp_f32 v139, v139\n"
      "v_mfma_f32_16x16x16_bf16 v[170:173], v[102:103], v[100:101], v[170:173]\n"
      "v_sub_f32 v124, v124, v90\n"
      "v_exp_f32 v192, v192\n"
      "v_mfma_f32_16x16x16_bf16 v[178:181], v[102:103], v[104:105], v[178:181]\n"
      "v_exp_f32 v200, v200\n"
      "v_sub_f32 v125, v125, v90\n"
      "v_cndmask_b32 v126, v126, v134, s[58:59]\n"
      "v_exp_f32 v190, v190\n"
      "v_pk_mul_f32 v[158:159], v[158:159], v[148:149] op_sel_hi:[1,0]\n"
      "v_mul_f32 v121, v187, v187\n"
      "v_exp_f32_e64 v125, v125\n"
      "v_pk_mul_f32 v[210:211], v[210:211], v[148:149] op_sel_hi:[1,0]\n"
      "v_pk_fma_f32 v[142:143], v[112:113], v[112:113], v[142:143]\n"
      "v_pk_fma_f32 v[120:121], v[188:189], v[188:189], v[120:121]\n"
      "v_sub_f32_e64 v122, v122, v90\n"
      "v_cndmask_b32 v126, v126, v170, s[60:61]\n"
      "v_pk_fma_f32 v[120:121], v[174:175], v[174:175], v[120:121]\n"
      "v_pk_mul_f32 v[160:161], v[160:161], v[148:149] op_sel_hi:[1,0]\n"
      "v_mfma_f32_16x16x32_bf16 v[202:205], a[216:219], v[218:221], 0\n"
      "s_waitcnt lgkmcnt(0)\n"
      "v_exp_f32 v122, v122\n"
      "v_mfma_f32_16x16x32_bf16 v[202:205], a[220:223], v[162:165], v[202:205]\n"
      "ds_read_b128 v[134:137], v2 offset:31232\n"
      "v_cndmask_b32 v126, v126, v178, s[62:63]\n"
      "v_mfma_f32_16x16x32_bf16 v[202:205], a[224:227], v[96:99], v[202:205]\n"
      "v_cvt_pk_bf16_f32 v210, v210, v211\n"
      "v_sub_f32_e64 v140, v140, v90\n"
      "v_max_f32_dpp v88, v126, v126 row_ror:8 row_mask:0xf bank_mask:0xf\n"
      "v_mfma_f32_16x16x32_bf16 v[144:147], a[240:243], v[218:221], 0\n"
      "ds_read_b128 v[106:109], v2 offset:28160\n"
      "v_max_f32_dpp v88, v88, v88 row_ror:4 row_mask:0xf bank_mask:0xf\n"
      "v_mfma_f32_16x16x32_bf16 v[144:147], a[244:247], v[162:165], v[144:147]\n"
      "ds_read_b128 v[102:105], v2 offset:34304\n"
      "v_cvt_pk_bf16_f32 v158, v158, v159\n"
      "v_exp_f32_e64 v140, v140\n"
      "v_cvt_pk_bf16_f32 v159, v160, v161\n"
      "v_pk_fma_f32 v[142:143], v[114:115], v[114:115], v[142:143]\n"
      "v_pk_fma_f32 v[120:121], v[176:177], v[176:177], v[120:121]\n"
      "v_pk_mul_f32 v[154:155], v[154:155], v[148:149] op_sel_hi:[1,0]\n"
      "v_max_f32_dpp v88, v88, v88 row_ror:2 row_mask:0xf bank_mask:0xf\n"
      "v_pk_mul_f32 v[156:157], v[156:157], v[148:149] op_sel_hi:[1,0]\n"
      "v_sub_f32_e64 v138, v138, v90\n"
      "v_pk_mul_f32 v[212:213], v[212:213], v[148:149] op_sel_hi:[1,0]\n"
      "v_pk_fma_f32 v[120:121], v[206:207], v[206:207], v[120:121]\n"
      "v_pk_fma_f32 v[142:143], v[116:117], v[116:117], v[142:143]\n"
      "v_mfma_f32_16x16x32_bf16 v[144:147], a[248:251], v[96:99], v[144:147]\n"
      "v_max_f32_dpp v88, v88, v88 row_ror:1 row_mask:0xf bank_mask:0xf\n"
      "v_exp_f32_e64 v124, v124\n"
      "v_mfma_f32_16x16x32_bf16 v[148:151], v[232:235], v[218:221], 0\n"
      "v_cvt_pk_bf16_f32 v211, v212, v213\n"
      "v_max_f32_dpp v88, v88, v88 row_bcast:15 row_mask:0xa bank_mask:0xf\n"
      "v_cvt_pk_bf16_f32 v90, v200, 0\n"
      "v_mfma_f32_16x16x32_bf16 v[148:151], v[236:239], v[162:165], v[148:151]\n"
      "v_exp_f32 v138, v138\n"
      "v_mfma_f32_16x16x32_bf16 v[148:151], v[240:243], v[96:99], v[148:151]\n"
      "s_waitcnt lgkmcnt(0)\n"
      "v_exp_f32_e64 v193, v193\n"
      "v_mfma_f32_16x16x32_bf16 v[202:205], a[228:231], v[106:109], v[202:205]\n"
      "v_cvt_pk_bf16_f32 v89, v124, v125\n"
      "v_cvt_pk_bf16_f32 v190, v190, v191\n"
      "v_mfma_f32_16x16x32_bf16 v[202:205], a[232:235], v[134:137], v[202:205]\n"
      "v_cvt_pk_bf16_f32 v212, v154, v155\n"
      "v_max_f32_dpp v88, v88, v88 row_bcast:31 row_mask:0xc bank_mask:0xf\n"
      "v_mfma_f32_16x16x32_bf16 v[202:205], a[236:239], v[102:105], v[202:205]\n"
      "ds_write_b64 v4, v[158:159] offset:256\n"
      "v_readlane_b32 s72, v88, 63\n"
      "v_cvt_pk_bf16_f32 v191, v192, v193\n"
      "v_mfma_f32_16x16x32_bf16 v[144:147], a[252:255], v[106:109], v[144:147]\n"
      "v_exp_f32_e64 v141, v141\n"
      "v_mfma_f32_16x16x32_bf16 v[144:147], v[224:227], v[134:137], v[144:147]\n"
      "v_cvt_pk_bf16_f32 v193, v140, v141\n"
      "v_cvt_pk_bf16_f32 v213, v156, v157\n"
      "v_mfma_f32_16x16x32_bf16 v[144:147], v[228:231], v[102:105], v[144:147]\n"
      "v_cvt_pk_bf16_f32 v88, v122, v123\n"
      "v_mul_f32 v219, v203, v203\n"
      "v_mfma_f32_16x16x32_bf16 v[148:151], v[244:247], v[106:109], v[148:151]\n"
      "v_fma_f32 v218, v202, v202, s52\n"
      "v_subrev_f32 v126, s72, v126\n"
      "v_mfma_f32_16x16x32_bf16 v[148:151], v[248:251], v[134:137], v[148:151]\n"
      "v_exp_f32_e64 v126, v126\n"
      "v_mfma_f32_16x16x32_bf16 v[148:151], v[252:255], v[102:105], v[148:151]\n"
      "ds_write_b128 v3, v[210:213] offset:256\n"
      "v_cvt_pk_bf16_f32 v126, v126, v126\n"
      "v_cvt_pk_bf16_f32 v192, v138, v139\n"
      "v_pk_fma_f32 v[218:219], v[204:205], v[204:205], v[218:219]\n"
      "v_pk_fma_f32 v[218:219], v[144:145], v[144:145], v[218:219]\n"
      "v_pk_fma_f32 v[120:121], v[208:209], v[208:209], v[120:121]\n"
      "v_pk_fma_f32 v[142:143], v[118:119], v[118:119], v[142:143]\n"
      "v_add_f32 v142, v142, v143\n"
      "v_add_f32 v120, v120, v121\n"
      "v_pk_fma_f32 v[218:219], v[146:147], v[146:147], v[218:219]\n"
      "v_pk_fma_f32 v[218:219], v[148:149], v[148:149], v[218:219]\n"
      "v_permlane16_swap_b32 v142, v120\n"
      "v_add_f32 v142, v142, v120\n"
      "v_pk_fma_f32 v[218:219], v[150:151], v[150:151], v[218:219]\n"
      "v_mfma_f32_16x16x32_bf16 v[154:157], v[32:35], v[190:193], 0\n"
      "v_add_f32 v218, v218, v219\n"
      "v_mov_b32 v120, v218\n"
      "v_mfma_f32_16x16x32_bf16 v[154:157], v[32:35], v[88:91], v[154:157]\n"
      "s_nop 0\n"
      "v_permlane16_swap_b32 v218, v120\n"
      "v_mfma_f32_16x16x32_bf16 v[210:213], v[130:133], v[190:193], 0\n"
      "ds_write_b16 v13, v126\n"
      "v_add_f32 v218, v218, v120\n"
      "v_mfma_f32_16x16x32_bf16 v[210:213], v[76:79], v[88:91], v[210:213]\n"
      "s_nop 0\n"
      "v_permlane32_swap_b32 v142, v218\n"
      "v_add_f32 v142, v142, v218\n"
      "v_mfma_f32_16x16x32_bf16 v[134:137], v[214:217], v[190:193], 0\n"
      "v_rcp_f32_e64 v154, v154\n"
      "v_mfma_f32_16x16x32_bf16 v[134:137], v[80:83], v[88:91], v[134:137]\n"
      "ds_write_b32 v31, v142\n"
      "v_mfma_f32_16x16x32_bf16 v[218:221], v[194:197], v[190:193], 0\n"
      "v_mul_f32 v213, v154, v213\n"
      "v_mul_f32 v210, v154, v210\n"
      "v_mfma_f32_16x16x32_bf16 v[218:221], v[84:87], v[88:91], v[218:221]\n"
      "ds_write_b32 v31, v182 offset:4608\n"
      "v_mul_f32_e64 v211, v154, v211\n"
      "v_mul_f32_e64 v212, v154, v212\n"
      "v_cvt_pk_bf16_f32 v210, v210, v211\n"
      "v_cvt_pk_bf16_f32 v211, v212, v213\n"
      "v_pk_mul_f32 v[134:135], v[134:135], v[154:155] op_sel_hi:[1,0]\n"
      "v_pk_mul_f32 v[136:137], v[136:137], v[154:155] op_sel_hi:[1,0]\n"
      "v_cvt_pk_bf16_f32 v212, v134, v135\n"
      "v_cvt_pk_bf16_f32 v213, v136, v137\n"
      "s_add_u32 s43, s43, 1\n"
      "ds_write_b128 v3, v[210:213] offset:512\n"
      "ds_read_b128 v[156:159], v14\n"
      "ds_read_b128 v[210:213], v14 offset:64\n"
      "v_pk_mul_f32 v[218:219], v[218:219], v[154:155] op_sel_hi:[1,0]\n"
      "v_pk_mul_f32 v[220:221], v[220:221], v[154:155] op_sel_hi:[1,0]\n"
      "v_cvt_pk_bf16_f32 v218, v218, v219\n"
      "v_cvt_pk_bf16_f32 v219, v220, v221\n"
      "s_and_b32 s86, s86, -4\n"
      "ds_write_b64 v4, v[218:219] offset:512\n"
      "s_waitcnt lgkmcnt(2)\n"
      "v_mfma_f32_16x16x32_bf16 v[152:155], v[156:159], v[32:35], 0\n"
      "v_add_u32_e64 v12, 32, v12\n"
      "s_waitcnt lgkmcnt(1)\n"
      "v_mfma_f32_16x16x32_bf16 v[152:155], v[210:213], v[32:35], v[152:155]\n"
      "s_waitcnt lgkmcnt(0)\n"
      "s_barrier\n"
      "s_cbranch_vccz L_max1_%=\n"
      "L_cold_%=:\n"
      "L_disp1_%=:\n"
      "s_cmp_lt_u32 s95, 0x40000000\n"
      "s_cbranch_scc1 L_nofix_11_%=\n"
      "s_cmp_eq_u32 s95, -2\n"
      "s_cbranch_scc1 L_tail1_%=\n"
      "s_nop 2\n"
      "v_lshlrev_b32 v218, 3, v36\n"
      "s_add_u32 s72, s98, L_cold_%=-L_ipf0_%=\n"
      "s_addc_u32 s73, s99, 0\n"
      "s_mov_b32 s74, L_cend_%=-L_cold_%=\n"
      "s_mov_b32 s75, 0x20000\n"
      "s_mov_b32 m0, 161792\n"
      "s_nop 0\n"
      "buffer_load_dwordx4 v218, s[72:75], 0 offen  lds\n"
      "s_lshr_b32 s92, s84, 30\n"
      "s_or_b32 s92, s92, s66\n"
      "s_cbranch_scc0 L_nf_12_%=\n"
      "s_bitcmp1_b32 s84, 31\n"
      "s_cselect_b32 s75, 100270464, 0\n"
      "s_bitcmp1_b32 s84, 30\n"
      "s_cselect_b32 s76, 196224, 0\n"
      "s_and_b32 s74, s84, 0x3fffffff\n"
      "s_cmp_ge_u32 s84, 0x40000000\n"
      "s_cselect_b32 s74, s74, 0\n"
      "s_cselect_b32 s84, 0, s84\n"
      "s_sub_u32 s74, s74, s66\n"
      "s_add_u32 s66, s66, s74\n"
      "s_sub_u32 s75, s75, s67\n"
      "s_add_u32 s67, s67, s75\n"
      "s_sub_u32 s76, s76, s68\n"
      "s_add_u32 s68, s68, s76\n"
      "v_add_u32 v16, s74, v16\n"
      "v_add_u32 v17, s74, v17\n"
      "v_add_u32 v18, s74, v18\n"
      "v_add_u32 v19, s74, v19\n"
      "v_add_u32 v15, s74, v15\n"
      "s_cmp_eq_u32 s75, 0\n"
      "s_cbranch_scc1 L_ax_13_%=\n"
      "v_bfe_i32 v218, v27, 0, 1\n"
      "v_and_b32 v218, s75, v218\n"
      "v_sub_u32 v16, v16, v218\n"
      "v_bfe_i32 v218, v27, 2, 1\n"
      "v_and_b32 v218, s75, v218\n"
      "v_sub_u32 v17, v17, v218\n"
      "v_bfe_i32 v218, v27, 4, 1\n"
      "v_and_b32 v218, s75, v218\n"
      "v_sub_u32 v18, v18, v218\n"
      "v_bfe_i32 v218, v27, 6, 1\n"
      "v_and_b32 v218, s75, v218\n"
      "v_sub_u32 v19, v19, v218\n"
      "v_bfe_i32 v218, v27, 8, 1\n"
      "v_and_b32 v218, s75, v218\n"
      "v_sub_u32 v15, v15, v218\n"
      "L_ax_13_%=:\n"
      "s_cmp_eq_u32 s76, 0\n"
      "s_cbranch_scc1 L_ax_14_%=\n"
      "v_bfe_i32 v218, v27, 1, 1\n"
      "v_and_b32 v218, s76, v218\n"
      "v_sub_u32 v16, v16, v218\n"
      "v_bfe_i32 v218, v27, 3, 1\n"
      "v_and_b32 v218, s76, v218\n"
      "v_sub_u32 v17, v17, v218\n"
      "v_bfe_i32 v218, v27, 5, 1\n"
      "v_and_b32 v218, s76, v218\n"
      "v_sub_u32 v18, v18, v218\n"
      "v_bfe_i32 v218, v27, 7, 1\n"
      "v_and_b32 v218, s76, v218\n"
      "v_sub_u32 v19, v19, v218\n"
      "v_bfe_i32 v218, v27, 9, 1\n"
      "v_and_b32 v218, s76, v218\n"
      "v_sub_u32 v15, v15, v218\n"
      "L_ax_14_%=:\n"
      "s_or_b32 s48, s66, s69\n"
      "s_cmp_lg_u32 s48, 0\n"
      "s_cselect_b32 s48, 0x40000000, 0\n"
      "L_nf_12_%=:\n"
      "s_lshr_b32 s92, s85, 30\n"
      "s_or_b32 s92, s92, s69\n"
      "s_cbranch_scc0 L_nf_15_%=\n"
      "s_bitcmp1_b32 s85, 31\n"
      "s_cselect_b32 s75, 100270464, 0\n"
      "s_bitcmp1_b32 s85, 30\n"
      "s_cselect_b32 s76, 196224, 0\n"
      "s_and_b32 s74, s85, 0x3fffffff\n"
      "s_cmp_ge_u32 s85, 0x40000000\n"
      "s_cselect_b32 s74, s74, 0\n"
      "s_cselect_b32 s85, 0, s85\n"
      "s_sub_u32 s74, s74, s69\n"
      "s_add_u32 s69, s69, s74\n"
      "s_sub_u32 s75, s75, s70\n"
      "s_add_u32 s70, s70, s75\n"
      "s_sub_u32 s76, s76, s71\n"
      "s_add_u32 s71, s71, s76\n"
      "v_add_u32 v20, s74, v20\n"
      "v_add_u32 v21, s74, v21\n"
      "v_add_u32 v22, s74, v22\n"
      "v_add_u32 v23, s74, v23\n"
      "v_add_u32 v25, s74, v25\n"
      "s_cmp_eq_u32 s75, 0\n"
      "s_cbranch_scc1 L_ax_16_%=\n"
      "v_bfe_i32 v218, v27, 10, 1\n"
      "v_and_b32 v218, s75, v218\n"
      "v_sub_u32 v20, v20, v218\n"
      "v_bfe_i32 v218, v27, 12, 1\n"
      "v_and_b32 v218, s75, v218\n"
      "v_sub_u32 v21, v21, v218\n"
      "v_bfe_i32 v218, v27, 14, 1\n"
      "v_and_b32 v218, s75, v218\n"
      "v_sub_u32 v22, v22, v218\n"
      "v_bfe_i32 v218, v27, 16, 1\n"
      "v_and_b32 v218, s75, v218\n"
      "v_sub_u32 v23, v23, v218\n"
      "v_bfe_i32 v218, v27, 18, 1\n"
      "v_and_b32 v218, s75, v218\n"
      "v_sub_u32 v25, v25, v218\n"
      "L_ax_16_%=:\n"
      "s_cmp_eq_u32 s76, 0\n"
      "s_cbranch_scc1 L_ax_17_%=\n"
      "v_bfe_i32 v218, v27, 11, 1\n"
      "v_and_b32 v218, s76, v218\n"
      "v_sub_u32 v20, v20, v218\n"
      "v_bfe_i32 v218, v27, 13, 1\n"
      "v_and_b32 v218, s76, v218\n"
      "v_sub_u32 v21, v21, v218\n"
      "v_bfe_i32 v218, v27, 15, 1\n"
      "v_and_b32 v218, s76, v218\n"
      "v_sub_u32 v22, v22, v218\n"
      "v_bfe_i32 v218, v27, 17, 1\n"
      "v_and_b32 v218, s76, v218\n"
      "v_sub_u32 v23, v23, v218\n"
      "v_bfe_i32 v218, v27, 19, 1\n"
      "v_and_b32 v218, s76, v218\n"
      "v_sub_u32_e64 v25, v25, v218\n"
      "L_ax_17_%=:\n"
      "s_or_b32 s48, s66, s69\n"
      "s_cmp_lg_u32 s48, 0\n"
      "s_cselect_b32 s48, 0x40000000, 0\n"
      "L_nf_15_%=:\n"
      "s_sub_u32 s92, s42, 1\n"
      "s_cmp_gt_u32 s92, s44\n"
      "s_cbranch_scc1 L_tail1_%=\n"
      "L_nofix_11_%=:\n"
      "s_cmp_lg_u64 s[56:57], 0\n"
      "s_cbranch_scc1 L_slow1_%=\n"
      "L_sel1_%=:\n"
      "s_cmp_lg_u64 s[54:55], 0\n"
      "s_cbranch_scc1 L_max1_%=\n"
      "s_branch L_loop1_%=\n"
      "L_slow1_%=:\n"
      "s_nop 4\n"
      "v_mfma_f32_16x16x32_bf16 v[218:221], v[156:159], v[130:133], 0\n"
      "ds_read_b128 v[120:123], v0 offset:18432\n"
      "v_mfma_f32_16x16x32_bf16 v[218:221], v[210:213], v[76:79], v[218:221]\n"
      "ds_read_b128 v[124:127], v9\n"
      "v_subrev_u32 v77, 115456, v9\n"
      "v_mfma_f32_16x16x32_bf16 v[134:137], v[156:159], v[214:217], 0\n"
      "ds_read_b128 v[170:173], v1 offset:18432\n"
      "s_add_u32 s72, s42, 1\n"
      "v_mfma_f32_16x16x32_bf16 v[134:137], v[210:213], v[80:83], v[134:137]\n"
      "ds_read_b128 v[100:103], v0 offset:24576\n"
      "v_bfe_u32 v89, v77, 4, 4\n"
      "v_mfma_f32_16x16x32_bf16 v[128:131], v[156:159], v[194:197], 0\n"
      "s_mov_b32 m0, 110592\n"
      "v_readlane_b32 s77, v38, s72\n"
      "v_readlane_b32 s46, v37, s42\n"
      "v_subrev_u32 v132, 115456, v9\n"
      "s_and_b32 s73, s77, -16\n"
      "v_mfma_f32_16x16x32_bf16 v[128:131], v[210:213], v[84:87], v[128:131]\n"
      "ds_read_b128 v[210:213], v1 offset:24576\n"
      "s_waitcnt lgkmcnt(4)\n"
      "v_mfma_f32_16x16x32_bf16 v[160:163], a[0:3], v[120:123], 0\n"
      "buffer_load_dwordx4 v132, s[28:31], s73 offen  lds\n"
      "v_mfma_f32_16x16x32_bf16 v[156:159], a[24:27], v[120:123], 0\n"
      "s_waitcnt lgkmcnt(3)\n"
      "v_add_f32 v126, v126, v127\n"
      "v_add_f32 v124, v124, v125\n"
      "v_mfma_f32_16x16x32_bf16 v[190:193], a[48:51], v[120:123], 0\n"
      "s_add_u32 s73, s73, 1024\n"
      "v_add_f32 v124, v124, v126\n"
      "v_cndmask_b32 v218, v218, v134, s[58:59]\n"
      "v_mfma_f32_16x16x32_bf16 v[136:139], a[72:75], v[120:123], 0\n"
      "s_waitcnt lgkmcnt(1)\n"
      "v_rsq_f32 v124, v124\n"
      "v_mfma_f32_16x16x32_bf16 v[140:143], a[96:99], v[120:123], 0\n"
      "ds_read_b128 v[96:99], v1 offset:20480\n"
      "v_readlane_b32 s45, v37, s43\n"
      "v_mfma_f32_16x16x32_bf16 v[180:183], a[120:123], v[120:123], 0\n"
      "s_mov_b32 m0, 111616\n"
      "v_mul_f32 v124, 0x415db3d7, v124\n"
      "v_mov_b32_e64 v126, v124\n"
      "v_mfma_f32_16x16x32_bf16 v[160:163], a[4:7], v[170:173], v[160:163]\n"
      "buffer_load_dwordx4 v132, s[28:31], s73 offen  lds\n"
      "v_mfma_f32_16x16x32_bf16 v[160:163], a[8:11], v[100:103], v[160:163]\n"
      "s_add_u32 s73, s73, 1024\n"
      "v_permlane32_swap_b32 v124, v126\n"
      "v_mfma_f32_16x16x32_bf16 v[156:159], a[28:31], v[170:173], v[156:159]\n"
      "ds_read_b128 v[220:223], v1 offset:30720\n"
      "v_mov_b32 v125, v124\n"
      "v_mfma_f32_16x16x32_bf16 v[156:159], a[32:35], v[100:103], v[156:159]\n"
      "ds_read_b128 v[106:109], v0 offset:30720\n"
      "v_mul_f32 v145, v126, v145\n"
      "v_mfma_f32_16x16x32_bf16 v[190:193], a[52:55], v[170:173], v[190:193]\n"
      "s_mov_b32 m0, 112640\n"
      "v_permlane16_swap_b32 v124, v125\n"
      "v_mfma_f32_16x16x32_bf16 v[190:193], a[56:59], v[100:103], v[190:193]\n"
      "v_mul_f32 v113, v124, v113\n"
      "v_mul_f32 v202, v126, v202\n"
      "v_mfma_f32_16x16x32_bf16 v[136:139], a[76:79], v[170:173], v[136:139]\n"
      "buffer_load_dwordx4 v132, s[28:31], s73 offen  lds\n"
      "v_mfma_f32_16x16x32_bf16 v[136:139], a[80:83], v[100:103], v[136:139]\n"
      "s_mov_b32 m0, 113664\n"
      "v_mul_f32 v166, v124, v166\n"
      "v_mul_f32 v189, v125, v189\n"
      "v_mfma_f32_16x16x32_bf16 v[140:143], a[100:103], v[170:173], v[140:143]\n"
      "ds_read_b128 v[198:201], v0 offset:32768\n"
      "v_mul_f32 v209, v125, v209\n"
      "v_mfma_f32_16x16x32_bf16 v[140:143], a[104:107], v[100:103], v[140:143]\n"
      "s_waitcnt lgkmcnt(1)\n"
      "v_mul_f32 v203, v126, v203\n"
      "v_mul_f32 v187, v125, v187\n"
      "v_mfma_f32_16x16x32_bf16 v[180:183], a[124:127], v[170:173], v[180:183]\n"
      "s_add_u32 s73, s73, 1024\n"
      "v_mul_f32 v169, v124, v169\n"
      "v_mul_f32 v204, v126, v204\n"
      "v_mfma_f32_16x16x32_bf16 v[180:183], a[128:131], v[100:103], v[180:183]\n"
      "buffer_load_dwordx4 v132, s[28:31], s73 offen  lds\n"
      "v_mfma_f32_16x16x32_bf16 v[160:163], a[12:15], v[210:213], v[160:163]\n"
      "s_mov_b32 m0, 114688\n"
      "v_mul_f32 v207, v125, v207\n"
      "v_mul_f32 v167, v124, v167\n"
      "v_mfma_f32_16x16x32_bf16 v[160:163], a[16:19], v[106:109], v[160:163]\n"
      "s_or_b32 s95, s45, s46\n"
      "v_mul_f32 v206, v125, v206\n"
      "v_cvt_pk_bf16_f32 v166, v166, v167\n"
      "v_mfma_f32_16x16x32_bf16 v[160:163], a[20:23], v[220:223], v[160:163]\n"
      "s_add_u32 s73, s73, 1024\n"
      "v_mul_f32 v168, v124, v168\n"
      "v_mul_f32 v112, v124, v112\n"
      "v_mfma_f32_16x16x32_bf16 v[156:159], a[36:39], v[210:213], v[156:159]\n"
      "v_cvt_pk_bf16_f32 v167, v168, v169\n"
      "v_cvt_pk_bf16_f32 v168, v112, v113\n"
      "v_mfma_f32_16x16x32_bf16 v[156:159], a[40:43], v[106:109], v[156:159]\n"
      "s_mov_b32 exec_hi, 0xffff\n"
      "buffer_load_dwordx4 v132, s[28:31], s73 offen  lds\n"
      "s_mov_b32 exec_hi, -1\n"
      "v_pk_mul_f32 v[114:115], v[114:115], v[124:125] op_sel_hi:[1,0]\n"
      "v_mfma_f32_16x16x32_bf16 v[156:159], a[44:47], v[220:223], v[156:159]\n"
      "v_cvt_pk_bf16_f32 v169, v114, v115\n"
      "v_mul_f32 v175, v125, v175\n"
      "v_mfma_f32_16x16x32_bf16 v[190:193], a[60:63], v[210:213], v[190:193]\n"
      "ds_write_b128 v5, v[166:169] offset:18432\n"
      "v_pk_mul_f32 v[146:147], v[146:147], v[126:127] op_sel_hi:[1,0]\n"
      "v_mfma_f32_16x16x32_bf16 v[190:193], a[64:67], v[106:109], v[190:193]\n"
      "v_mul_f32 v208, v125, v208\n"
      "v_mul_f32 v144, v126, v144\n"
      "v_mfma_f32_16x16x32_bf16 v[190:193], a[68:71], v[220:223], v[190:193]\n"
      "s_add_u32 m0, s64, 0\n"
      "v_cvt_pk_bf16_f32 v202, v202, v203\n"
      "v_mul_f32 v186, v125, v186\n"
      "v_mfma_f32_16x16x32_bf16 v[136:139], a[84:87], v[210:213], v[136:139]\n"
      "v_mul_f32_e64 v188, v125, v188\n"
      "v_fma_f32 v76, v160, v160, s51\n"
      "v_mfma_f32_16x16x32_bf16 v[136:139], a[88:91], v[106:109], v[136:139]\n"
      "buffer_load_dwordx4 v16, s[16:19], s84 offen  lds\n"
      "v_mfma_f32_16x16x32_bf16 v[136:139], a[92:95], v[220:223], v[136:139]\n"
      "v_fmac_f32 v76, v161, v161\n"
      "v_mul_f32 v176, v125, v176\n"
      "v_mfma_f32_16x16x32_bf16 v[140:143], a[108:111], v[210:213], v[140:143]\n"
      "s_add_u32 m0, s64, 4096\n"
      "v_cvt_pk_bf16_f32 v186, v186, v187\n"
      "v_mul_f32_e64 v118, v124, v118\n"
      "v_mfma_f32_16x16x32_bf16 v[140:143], a[112:115], v[106:109], v[140:143]\n"
      "v_cvt_pk_bf16_f32 v206, v206, v207\n"
      "v_cvt_pk_bf16_f32 v187, v188, v189\n"
      "v_mfma_f32_16x16x32_bf16 v[140:143], a[116:119], v[220:223], v[140:143]\n"
      "v_mul_f32 v116, v124, v116\n"
      "v_mul_f32 v151, v126, v151\n"
      "v_mfma_f32_16x16x32_bf16 v[180:183], a[132:135], v[210:213], v[180:183]\n"
      "v_mul_f32 v177, v125, v177\n"
      "v_mul_f32 v174, v125, v174\n"
      "v_mfma_f32_16x16x32_bf16 v[180:183], a[136:139], v[106:109], v[180:183]\n"
      "buffer_load_dwordx4 v17, s[16:19], s84 offen  lds\n"
      "v_mfma_f32_16x16x32_bf16 v[180:183], a[140:143], v[220:223], v[180:183]\n"
      "v_fmac_f32_e64 v76, v162, v162\n"
      "v_cvt_pk_bf16_f32 v189, v176, v177\n"
      "v_mfma_f32_16x16x32_bf16 v[130:133], v[120:123], a[144:147], 0\n"
      "s_add_u32 m0, s64, 8192\n"
      "v_cvt_pk_bf16_f32 v188, v174, v175\n"
      "v_fma_f32 v80, v136, v136, s51\n"
      "v_mfma_f32_16x16x32_bf16 v[130:133], v[170:173], a[148:151], v[130:133]\n"
      "ds_write_b128 v5, v[186:189] offset:24576\n"
      "v_mfma_f32_16x16x32_bf16 v[130:133], v[100:103], a[152:155], v[130:133]\n"
      "buffer_load_dwordx4 v18, s[16:19], s84 offen  lds\n"
      "v_mfma_f32_16x16x32_bf16 v[130:133], v[210:213], a[156:159], v[130:133]\n"
      "v_cvt_pk_bf16_f32 v207, v208, v209\n"
      "v_fmac_f32_e64 v76, v163, v163\n"
      "v_mfma_f32_16x16x32_bf16 v[130:133], v[106:109], a[160:163], v[130:133]\n"
      "ds_write_b64 v6, v[206:207] offset:24576\n"
      "v_mfma_f32_16x16x32_bf16 v[130:133], v[220:223], a[164:167], v[130:133]\n"
      "s_add_u32 m0, s64, 12288\n"
      "v_mul_f32 v205, v126, v205\n"
      "v_cvt_pk_bf16_f32 v203, v204, v205\n"
      "v_mul_f32 v148, v126, v148\n"
      "v_mfma_f32_16x16x32_bf16 v[214:217], v[120:123], a[168:171], 0\n"
      "buffer_load_dwordx4 v19, s[16:19], s84 offen  lds\n"
      "v_mfma_f32_16x16x32_bf16 v[214:217], v[170:173], a[172:175], v[214:217]\n"
      "ds_read_b128 v[166:169], v0 offset:20480\n"
      "v_fmac_f32 v80, v137, v137\n"
      "v_mfma_f32_16x16x32_bf16 v[214:217], v[100:103], a[176:179], v[214:217]\n"
      "ds_read_b128 v[112:115], v0 offset:26624\n"
      "v_mul_f32 v117, v124, v117\n"
      "v_mfma_f32_16x16x32_bf16 v[214:217], v[210:213], a[180:183], v[214:217]\n"
      "s_add_u32 m0, s65, 16384\n"
      "v_cvt_pk_bf16_f32 v204, v144, v145\n"
      "v_cvt_pk_bf16_f32 v205, v146, v147\n"
      "v_mfma_f32_16x16x32_bf16 v[214:217], v[106:109], a[184:187], v[214:217]\n"
      "ds_read_b128 v[186:189], v1 offset:26624\n"
      "v_mul_f32_e64 v119, v124, v119\n"
      "v_mfma_f32_16x16x32_bf16 v[214:217], v[220:223], a[188:191], v[214:217]\n"
      "buffer_load_dwordx4 v15, s[16:19], s84 offen  lds\n"
      "v_mfma_f32_16x16x32_bf16 v[194:197], v[120:123], a[192:195], 0\n"
      "ds_read_b128 v[206:209], v1 offset:32768\n"
      "v_fmac_f32_e64 v80, v138, v138\n"
      "v_mfma_f32_16x16x32_bf16 v[194:197], v[170:173], a[196:199], v[194:197]\n"
      "ds_read_b128 v[174:177], v9 offset:4608\n"
      "v_fmac_f32 v80, v139, v139\n"
      "v_mfma_f32_16x16x32_bf16 v[194:197], v[100:103], a[200:203], v[194:197]\n"
      "v_mul_u32_u24 v88, 98, v89\n"
      "v_fmac_f32 v76, v156, v156\n"
      "v_mfma_f32_16x16x32_bf16 v[194:197], v[210:213], a[204:207], v[194:197]\n"
      "s_and_b32 s77, s77, 15\n"
      "v_fmac_f32 v80, v140, v140\n"
      "v_mul_f32_e64 v149, v126, v149\n"
      "v_mfma_f32_16x16x32_bf16 v[194:197], v[106:109], a[208:211], v[194:197]\n"
      "buffer_load_dwordx4 v[144:147], v36, s[28:31], s86 offen\n"
      "v_mfma_f32_16x16x32_bf16 v[194:197], v[220:223], a[212:215], v[194:197]\n"
      "s_waitcnt lgkmcnt(2)\n"
      "v_mul_f32 v150, v126, v150\n"
      "v_mov_b32_dpp v152, v152 quad_perm:[0,0,0,0] row_mask:0xf bank_mask:0xf\n"
      "v_mfma_f32_16x16x32_bf16 v[210:213], a[0:3], v[166:169], 0\n"
      "v_fmac_f32_e64 v80, v141, v141\n"
      "v_fmac_f32_e64 v76, v157, v157\n"
      "v_mfma_f32_16x16x32_bf16 v[210:213], a[4:7], v[96:99], v[210:213]\n"
      "ds_write_b128 v5, v[202:205] offset:30720\n"
      "v_mfma_f32_16x16x32_bf16 v[210:213], a[8:11], v[112:115], v[210:213]\n"
      "buffer_load_dword v78, v29, s[28:31], s86 offen\n"
      "v_mfma_f32_16x16x32_bf16 v[210:213], a[12:15], v[186:189], v[210:213]\n"
      "s_or_b32 s95, s95, s48\n"
      "v_cvt_pk_bf16_f32 v148, v148, v149\n"
      "v_fmac_f32 v80, v142, v142\n"
      "v_mfma_f32_16x16x32_bf16 v[210:213], a[16:19], v[198:201], v[210:213]\n"
      "s_waitcnt lgkmcnt(1)\n"
      "v_fmac_f32 v80, v143, v143\n"
      "v_lshrrev_b32 v77, 8, v77\n"
      "v_mfma_f32_16x16x32_bf16 v[210:213], a[20:23], v[206:209], v[210:213]\n"
      "v_fmac_f32 v80, v180, v180\n"
      "v_fmac_f32_e64 v76, v158, v158\n"
      "v_mfma_f32_16x16x32_bf16 v[202:205], a[24:27], v[166:169], 0\n"
      "v_cndmask_b32 v218, v218, v128, s[60:61]\n"
      "v_cvt_pk_bf16_f32 v149, v150, v151\n"
      "v_mfma_f32_16x16x32_bf16 v[202:205], a[28:31], v[96:99], v[202:205]\n"
      "v_fmac_f32_e64 v76, v159, v159\n"
      "v_cvt_pk_bf16_f32 v130, v130, v131\n"
      "v_mfma_f32_16x16x32_bf16 v[202:205], a[32:35], v[112:115], v[202:205]\n"
      "ds_write_b64 v6, v[148:149] offset:30720\n"
      "v_mfma_f32_16x16x32_bf16 v[202:205], a[36:39], v[186:189], v[202:205]\n"
      "v_fmac_f32 v76, v190, v190\n"
      "v_fmac_f32 v80, v181, v181\n"
      "v_mfma_f32_16x16x32_bf16 v[202:205], a[40:43], v[198:201], v[202:205]\n"
      "v_fmac_f32_e64 v76, v191, v191\n"
      "v_cvt_pk_bf16_f32 v116, v116, v117\n"
      "v_mfma_f32_16x16x32_bf16 v[202:205], a[44:47], v[206:209], v[202:205]\n"
      "v_cvt_pk_bf16_f32 v117, v118, v119\n"
      "v_subrev_u32 v39, 115456, v9\n"
      "v_mfma_f32_16x16x32_bf16 v[148:151], a[48:51], v[166:169], 0\n"
      "ds_write_b64 v6, v[116:117] offset:18432\n"
      "v_mfma_f32_16x16x32_bf16 v[148:151], a[52:55], v[96:99], v[148:151]\n"
      "ds_read_b128 v[116:119], v0 offset:34816\n"
      "v_fma_f32 v222, v210, v210, s51\n"
      "v_mfma_f32_16x16x32_bf16 v[148:151], a[56:59], v[112:115], v[148:151]\n"
      "v_cvt_pk_bf16_f32 v131, v132, v133\n"
      "v_fmac_f32 v222, v211, v211\n"
      "v_mfma_f32_16x16x32_bf16 v[148:151], a[60:63], v[186:189], v[148:151]\n"
      "v_rcp_f32 v152, v152\n"
      "v_mfma_f32_16x16x32_bf16 v[148:151], a[64:67], v[198:201], v[148:151]\n"
      "s_cmp_ge_u32 s95, 0x40000000\n"
      "s_cselect_b32 s94, -1, -2\n"
      "v_fmac_f32 v76, v192, v192\n"
      "v_mfma_f32_16x16x32_bf16 v[148:151], a[68:71], v[206:209], v[148:151]\n"
      "v_mul_f32 v218, v152, v218\n"
      "v_fmac_f32 v80, v182, v182\n"
      "v_mfma_f32_16x16x32_bf16 v[152:155], a[72:75], v[166:169], 0\n"
      "v_fmac_f32 v80, v183, v183\n"
      "v_cvt_pk_bf16_f32 v218, v218, v218\n"
      "v_mfma_f32_16x16x32_bf16 v[152:155], a[76:79], v[96:99], v[152:155]\n"
      "v_fmac_f32 v76, v193, v193\n"
      "v_fmac_f32 v222, v212, v212\n"
      "v_mfma_f32_16x16x32_bf16 v[152:155], a[80:83], v[112:115], v[152:155]\n"
      "v_cvt_pk_bf16_f32 v214, v214, v215\n"
      "v_cvt_pk_bf16_f32 v194, v194, v195\n"
      "v_mfma_f32_16x16x32_bf16 v[152:155], a[84:87], v[186:189], v[152:155]\n"
      "v_permlane16_swap_b32 v76, v80\n"
      "v_mfma_f32_16x16x32_bf16 v[152:155], a[88:91], v[198:201], v[152:155]\n"
      "v_add_f32 v76, v76, v80\n"
      "v_mov_b32_e64 v80, v76\n"
      "v_mfma_f32_16x16x32_bf16 v[152:155], a[92:95], v[206:209], v[152:155]\n"
      "s_nop 0\n"
      "v_permlane32_swap_b32 v76, v80\n"
      "v_mfma_f32_16x16x32_bf16 v[126:129], a[96:99], v[166:169], 0\n"
      "s_add_u32 s73, s73, 1024\n"
      "v_fmac_f32 v222, v213, v213\n"
      "v_add_f32 v76, v76, v80\n"
      "v_mfma_f32_16x16x32_bf16 v[126:129], a[100:103], v[96:99], v[126:129]\n"
      "ds_write_b16 v12, v218\n"
      "v_mfma_f32_16x16x32_bf16 v[126:129], a[104:107], v[112:115], v[126:129]\n"
      "v_rsq_f32 v76, v76\n"
      "v_mfma_f32_16x16x32_bf16 v[126:129], a[108:111], v[186:189], v[126:129]\n"
      "v_mov_b32 v80, v76\n"
      "v_lshrrev_b32 v39, 3, v39\n"
      "v_mfma_f32_16x16x32_bf16 v[126:129], a[112:115], v[198:201], v[126:129]\n"
      "v_permlane16_swap_b32 v76, v80\n"
      "v_mfma_f32_16x16x32_bf16 v[126:129], a[116:119], v[206:209], v[126:129]\n"
      "v_mul_f32 v76, s50, v76\n"
      "v_fmac_f32 v222, v202, v202\n"
      "v_mfma_f32_16x16x32_bf16 v[218:221], a[120:123], v[166:169], 0\n"
      "s_cmp_gt_u32 s42, s44\n"
      "s_cselect_b32 s95, s94, s95\n"
      "v_mul_f32 v136, v80, v136\n"
      "v_mfma_f32_16x16x32_bf16 v[218:221], a[124:127], v[96:99], v[218:221]\n"
      "v_mul_f32 v191, v76, v191\n"
      "v_mul_f32_e64 v141, v80, v141\n"
      "v_mfma_f32_16x16x32_bf16 v[218:221], a[128:131], v[112:115], v[218:221]\n"
      "v_fma_f32 v223, v152, v152, s51\n"
      "v_mul_f32 v180, v80, v180\n"
      "v_mfma_f32_16x16x32_bf16 v[218:221], a[132:135], v[186:189], v[218:221]\n"
      "v_mul_f32 v163, v76, v163\n"
      "v_mul_f32_e64 v183, v80, v183\n"
      "v_mfma_f32_16x16x32_bf16 v[218:221], a[136:139], v[198:201], v[218:221]\n"
      "v_fmac_f32 v223, v153, v153\n"
      "v_or3_b32 v174, v174, v175, v176\n"
      "v_mfma_f32_16x16x32_bf16 v[218:221], a[140:143], v[206:209], v[218:221]\n"
      "v_fmac_f32 v223, v154, v154\n"
      "v_mul_f32 v161, v76, v161\n"
      "v_mfma_f32_16x16x32_bf16 v[122:125], v[166:169], a[144:147], 0\n"
      "v_mul_f32 v162, v76, v162\n"
      "v_bitop3_b32 v174, v174, v177, s33 bitop3:0xa8\n"
      "v_mfma_f32_16x16x32_bf16 v[122:125], v[96:99], a[148:151], v[122:125]\n"
      "v_mul_f32 v181, v80, v181\n"
      "v_lshl_add_u32 v88, v77, 3, v88\n"
      "v_mfma_f32_16x16x32_bf16 v[122:125], v[112:115], a[152:155], v[122:125]\n"
      "v_mul_f32 v159, v76, v159\n"
      "v_cvt_pk_bf16_f32 v215, v216, v217\n"
      "v_mfma_f32_16x16x32_bf16 v[122:125], v[186:189], a[156:159], v[122:125]\n"
      "ds_read_b128 v[176:179], v1 offset:22528\n"
      "v_fmac_f32_e64 v223, v155, v155\n"
      "v_mfma_f32_16x16x32_bf16 v[122:125], v[198:201], a[160:163], v[122:125]\n"
      "ds_read_b128 v[106:109], v0 offset:22528\n"
      "v_mul_f32 v139, v80, v139\n"
      "v_mfma_f32_16x16x32_bf16 v[122:125], v[206:209], a[164:167], v[122:125]\n"
      "v_fmac_f32 v222, v203, v203\n"
      "v_cvt_pk_bf16_f32 v195, v196, v197\n"
      "v_mfma_f32_16x16x32_bf16 v[132:135], v[166:169], a[168:171], 0\n"
      "v_fmac_f32 v223, v126, v126\n"
      "v_mul_f32 v137, v80, v137\n"
      "v_mfma_f32_16x16x32_bf16 v[132:135], v[96:99], a[172:175], v[132:135]\n"
      "v_fmac_f32 v223, v127, v127\n"
      "v_fmac_f32 v223, v128, v128\n"
      "v_mfma_f32_16x16x32_bf16 v[132:135], v[112:115], a[176:179], v[132:135]\n"
      "v_fmac_f32 v222, v204, v204\n"
      "v_fmac_f32 v223, v129, v129\n"
      "v_mfma_f32_16x16x32_bf16 v[132:135], v[186:189], a[180:183], v[132:135]\n"
      "v_add_u32 v88, s77, v88\n"
      "v_fmac_f32 v222, v205, v205\n"
      "v_mfma_f32_16x16x32_bf16 v[132:135], v[198:201], a[184:187], v[132:135]\n"
      "ds_read_b128 v[170:173], v1 offset:28672\n"
      "v_fmac_f32_e64 v222, v148, v148\n"
      "v_mfma_f32_16x16x32_bf16 v[132:135], v[206:209], a[188:191], v[132:135]\n"
      "v_cmp_ne_u32 s[56:57], 0, v174\n"
      "v_fmac_f32 v223, v218, v218\n"
      "v_mfma_f32_16x16x32_bf16 v[100:103], v[166:169], a[192:195], 0\n"
      "ds_read_b128 v[166:169], v0 offset:28672\n"
      "v_mul_f32 v142, v80, v142\n"
      "v_mfma_f32_16x16x32_bf16 v[100:103], v[96:99], a[196:199], v[100:103]\n"
      "v_cvt_pk_bf16_f32 v136, v136, v137\n"
      "v_cvt_pk_bf16_f32 v180, v180, v181\n"
      "v_mfma_f32_16x16x32_bf16 v[100:103], v[112:115], a[200:203], v[100:103]\n"
      "v_fmac_f32 v223, v219, v219\n"
      "v_mul_f32 v160, v76, v160\n"
      "v_mfma_f32_16x16x32_bf16 v[100:103], v[186:189], a[204:207], v[100:103]\n"
      "ds_read_b128 v[112:115], v1 offset:34816\n"
      "v_fmac_f32_e64 v222, v149, v149\n"
      "v_mfma_f32_16x16x32_bf16 v[100:103], v[198:201], a[208:211], v[100:103]\n"
      "v_lshlrev_b32 v77, 2, v77\n"
      "v_fmac_f32 v223, v220, v220\n"
      "v_mfma_f32_16x16x32_bf16 v[100:103], v[206:209], a[212:215], v[100:103]\n"
      "s_waitcnt lgkmcnt(2)\n"
      "v_mul_f32 v193, v76, v193\n"
      "v_cvt_pk_bf16_f32 v160, v160, v161\n"
      "v_mfma_f32_16x16x32_bf16 v[198:201], a[0:3], v[106:109], 0\n"
      "v_mul_f32 v157, v76, v157\n"
      "v_fmac_f32 v223, v221, v221\n"
      "v_mfma_f32_16x16x32_bf16 v[198:201], a[4:7], v[176:179], v[198:201]\n"
      "s_waitcnt lgkmcnt(1)\n"
      "v_mul_f32 v182, v80, v182\n"
      "v_fmac_f32 v222, v150, v150\n"
      "v_mfma_f32_16x16x32_bf16 v[198:201], a[8:11], v[166:169], v[198:201]\n"
      "v_sub_u32 v89, v89, v77\n"
      "v_mul_f32_e64 v143, v80, v143\n"
      "v_mfma_f32_16x16x32_bf16 v[198:201], a[12:15], v[170:173], v[198:201]\n"
      "s_add_u32 s42, s42, 1\n"
      "v_mul_f32 v190, v76, v190\n"
      "v_mul_f32 v138, v80, v138\n"
      "v_mfma_f32_16x16x32_bf16 v[198:201], a[16:19], v[116:119], v[198:201]\n"
      "s_waitcnt lgkmcnt(0)\n"
      "v_min_u32 v39, 96, v39\n"
      "v_fmac_f32 v222, v151, v151\n"
      "v_mfma_f32_16x16x32_bf16 v[198:201], a[20:23], v[112:115], v[198:201]\n"
      "s_nop 0\n"
      "v_permlane16_swap_b32_e64 v222, v223\n"
      "v_mfma_f32_16x16x32_bf16 v[186:189], a[24:27], v[106:109], 0\n"
      "v_cvt_pk_bf16_f32 v181, v182, v183\n"
      "v_add_f32_e64 v222, v222, v223\n"
      "v_mfma_f32_16x16x32_bf16 v[186:189], a[28:31], v[176:179], v[186:189]\n"
      "v_cvt_pk_bf16_f32 v161, v162, v163\n"
      "v_mov_b32 v223, v222\n"
      "v_mfma_f32_16x16x32_bf16 v[186:189], a[32:35], v[166:169], v[186:189]\n"
      "s_waitcnt vmcnt(7)\n"
      "v_cvt_pk_bf16_f32 v137, v138, v139\n"
      "v_mul_f32_e64 v158, v76, v158\n"
      "v_mfma_f32_16x16x32_bf16 v[186:189], a[36:39], v[170:173], v[186:189]\n"
      "v_cvt_pk_bf16_f32 v139, v142, v143\n"
      "v_fma_f32 v174, v198, v198, s51\n"
      "v_mfma_f32_16x16x32_bf16 v[186:189], a[40:43], v[116:119], v[186:189]\n"
      "v_permlane32_swap_b32 v222, v223\n"
      "v_mfma_f32_16x16x32_bf16 v[186:189], a[44:47], v[112:115], v[186:189]\n"
      "v_add_f32 v222, v222, v223\n"
      "v_cvt_pk_bf16_f32 v190, v190, v191\n"
      "v_mfma_f32_16x16x32_bf16 v[96:99], a[48:51], v[106:109], 0\n"
      "v_fmac_f32 v174, v199, v199\n"
      "v_cvt_pk_bf16_f32 v216, v132, v133\n"
      "v_mfma_f32_16x16x32_bf16 v[96:99], a[52:55], v[176:179], v[96:99]\n"
      "v_rsq_f32 v222, v222\n"
      "v_mfma_f32_16x16x32_bf16 v[96:99], a[56:59], v[166:169], v[96:99]\n"
      "v_fmac_f32 v174, v200, v200\n"
      "v_mov_b32 v223, v222\n"
      "v_mfma_f32_16x16x32_bf16 v[96:99], a[60:63], v[170:173], v[96:99]\n"
      "s_nop 0\n"
      "v_permlane16_swap_b32 v222, v223\n"
      "v_mfma_f32_16x16x32_bf16 v[96:99], a[64:67], v[116:119], v[96:99]\n"
      "v_mul_f32 v156, v76, v156\n"
      "v_mul_f32 v140, v80, v140\n"
      "v_mfma_f32_16x16x32_bf16 v[96:99], a[68:71], v[112:115], v[96:99]\n"
      "v_mul_f32_e64 v222, s50, v222\n"
      "v_cvt_pk_bf16_f32 v138, v140, v141\n"
      "v_mfma_f32_16x16x32_bf16 v[140:143], a[72:75], v[106:109], 0\n"
      "v_cvt_pk_bf16_f32 v162, v156, v157\n"
      "v_mul_f32_e64 v152, v223, v152\n"
      "v_mfma_f32_16x16x32_bf16 v[140:143], a[76:79], v[176:179], v[140:143]\n"
      "v_and_b32 v93, 2, v88\n"
      "v_lshlrev_b32 v89, 4, v89\n"
      "v_mfma_f32_16x16x32_bf16 v[140:143], a[80:83], v[166:169], v[140:143]\n"
      "v_mul_f32 v204, v222, v204\n"
      "v_fmac_f32 v174, v201, v201\n"
      "v_mfma_f32_16x16x32_bf16 v[140:143], a[84:87], v[170:173], v[140:143]\n"
      "v_cvt_pk_bf16_f32 v196, v100, v101\n"
      "v_fmac_f32 v174, v186, v186\n"
      "v_mfma_f32_16x16x32_bf16 v[140:143], a[88:91], v[116:119], v[140:143]\n"
      "v_mul_f32 v153, v223, v153\n"
      "v_cvt_pk_bf16_f32 v163, v158, v159\n"
      "v_mfma_f32_16x16x32_bf16 v[140:143], a[92:95], v[112:115], v[140:143]\n"
      "v_cvt_pk_bf16_f32 v217, v134, v135\n"
      "v_cvt_pk_bf16_f32 v133, v124, v125\n"
      "v_mfma_f32_16x16x32_bf16 v[206:209], a[96:99], v[106:109], 0\n"
      "s_cmp_ge_u32 s95, 0x40000000\n"
      "s_cselect_b64 vcc, -1, s[56:57]\n"
      "v_mul_f32 v192, v76, v192\n"
      "v_mfma_f32_16x16x32_bf16 v[206:209], a[100:103], v[176:179], v[206:209]\n"
      "s_nop 0\n"
      "v_fma_f32 v76, v140, v140, s51\n"
      "v_cvt_pk_bf16_f32 v197, v102, v103\n"
      "v_fmac_f32 v174, v187, v187\n"
      "v_fmac_f32 v174, v188, v188\n"
      "v_and_b32_e64 v88, -4, v88\n"
      "v_pk_mul_f32 v[202:203], v[202:203], v[222:223] op_sel_hi:[1,0]\n"
      "v_pk_mul_f32 v[150:151], v[150:151], v[222:223] op_sel_hi:[1,0]\n"
      "v_pk_mul_f32 v[126:127], v[126:127], v[222:223] op_sel:[0,1]\n"
      "v_pk_mul_f32 v[154:155], v[154:155], v[222:223] op_sel:[0,1]\n"
      "v_pk_mul_f32 v[148:149], v[148:149], v[222:223] op_sel_hi:[1,0]\n"
      "v_pk_mul_f32 v[212:213], v[212:213], v[222:223] op_sel_hi:[1,0]\n"
      "v_pk_mul_f32 v[210:211], v[210:211], v[222:223] op_sel_hi:[1,0]\n"
      "v_pk_mul_f32 v[218:219], v[218:219], v[222:223] op_sel:[0,1]\n"
      "v_mfma_f32_16x16x32_bf16 v[206:209], a[104:107], v[166:169], v[206:209]\n"
      "v_fmac_f32 v76, v141, v141\n"
      "v_fmac_f32 v174, v189, v189\n"
      "v_mfma_f32_16x16x32_bf16 v[206:209], a[108:111], v[170:173], v[206:209]\n"
      "v_cvt_pk_bf16_f32 v148, v148, v149\n"
      "v_fmac_f32 v174, v96, v96\n"
      "v_mfma_f32_16x16x32_bf16 v[206:209], a[112:115], v[116:119], v[206:209]\n"
      "v_cvt_pk_bf16_f32 v210, v210, v211\n"
      "v_fmac_f32 v174, v97, v97\n"
      "v_mfma_f32_16x16x32_bf16 v[206:209], a[116:119], v[112:115], v[206:209]\n"
      "v_cvt_pk_bf16_f32 v132, v122, v123\n"
      "v_cvt_pk_bf16_f32 v152, v152, v153\n"
      "v_mfma_f32_16x16x32_bf16 v[156:159], a[120:123], v[106:109], 0\n"
      "v_fmac_f32 v174, v98, v98\n"
      "v_fmac_f32 v76, v142, v142\n"
      "v_mfma_f32_16x16x32_bf16 v[156:159], a[124:127], v[176:179], v[156:159]\n"
      "v_fmac_f32 v174, v99, v99\n"
      "v_fmac_f32 v76, v143, v143\n"
      "v_cvt_pk_bf16_f32 v191, v192, v193\n"
      "v_mfma_f32_16x16x32_bf16 v[156:159], a[128:131], v[166:169], v[156:159]\n"
      "v_fmac_f32 v76, v206, v206\n"
      "v_cvt_pk_bf16_f32 v153, v154, v155\n"
      "v_mfma_f32_16x16x32_bf16 v[156:159], a[132:135], v[170:173], v[156:159]\n"
      "v_fmac_f32 v76, v207, v207\n"
      "v_mul_f32_e64 v205, v222, v205\n"
      "v_mfma_f32_16x16x32_bf16 v[156:159], a[136:139], v[116:119], v[156:159]\n"
      "v_cvt_pk_bf16_f32 v211, v212, v213\n"
      "v_cvt_pk_bf16_f32 v149, v150, v151\n"
      "v_mfma_f32_16x16x32_bf16 v[156:159], a[140:143], v[112:115], v[156:159]\n"
      "v_cvt_pk_bf16_f32 v212, v202, v203\n"
      "v_fmac_f32 v76, v208, v208\n"
      "v_fmac_f32 v76, v209, v209\n"
      "v_mfma_f32_16x16x32_bf16 v[122:125], v[106:109], a[144:147], 0\n"
      "v_cvt_pk_bf16_f32 v154, v126, v127\n"
      "v_cvt_pk_bf16_f32 v218, v218, v219\n"
      "v_mfma_f32_16x16x32_bf16 v[122:125], v[176:179], a[148:151], v[122:125]\n"
      "s_nop 0\n"
      "v_fmac_f32 v76, v156, v156\n"
      "v_fmac_f32_e64 v76, v157, v157\n"
      "v_mfma_f32_16x16x32_bf16 v[122:125], v[166:169], a[152:155], v[122:125]\n"
      "v_fmac_f32 v76, v158, v158\n"
      "v_fmac_f32 v76, v159, v159\n"
      "v_mfma_f32_16x16x32_bf16 v[122:125], v[170:173], a[156:159], v[122:125]\n"
      "s_nop 0\n"
      "v_permlane16_swap_b32 v174, v76\n"
      "v_mfma_f32_16x16x32_bf16 v[122:125], v[116:119], a[160:163], v[122:125]\n"
      "v_add_f32 v174, v174, v76\n"
      "v_mov_b32 v76, v174\n"
      "v_mfma_f32_16x16x32_bf16 v[122:125], v[112:115], a[164:167], v[122:125]\n"
      "s_nop 0\n"
      "v_permlane32_swap_b32 v174, v76\n"
      "v_mfma_f32_16x16x32_bf16 v[100:103], v[106:109], a[168:171], 0\n"
      "v_add_f32 v174, v174, v76\n"
      "v_rsq_f32 v174, v174\n"
      "v_mfma_f32_16x16x32_bf16 v[100:103], v[176:179], a[172:175], v[100:103]\n"
      "ds_read_b128 v[182:185], v7\n"
      "v_mov_b32 v76, v174\n"
      "v_mfma_f32_16x16x32_bf16 v[100:103], v[166:169], a[176:179], v[100:103]\n"
      "v_cvt_pk_bf16_f32 v77, v124, v125\n"
      "v_cvt_pk_bf16_f32 v213, v204, v205\n"
      "v_permlane16_swap_b32 v174, v76\n"
      "v_mul_f32_e64 v174, s50, v174\n"
      "v_pk_mul_f32 v[158:159], v[158:159], v[76:77] op_sel_hi:[1,0]\n"
      "v_pk_mul_f32 v[186:187], v[186:187], v[174:175] op_sel_hi:[1,0]\n"
      "v_pk_mul_f32 v[206:207], v[206:207], v[76:77] op_sel_hi:[1,0]\n"
      "v_pk_mul_f32 v[96:97], v[96:97], v[174:175] op_sel_hi:[1,0]\n"
      "v_pk_mul_f32 v[140:141], v[140:141], v[76:77] op_sel_hi:[1,0]\n"
      "v_pk_mul_f32 v[98:99], v[98:99], v[174:175] op_sel_hi:[1,0]\n"
      "v_pk_mul_f32 v[200:201], v[200:201], v[174:175] op_sel_hi:[1,0]\n"
      "v_pk_mul_f32 v[142:143], v[142:143], v[76:77] op_sel_hi:[1,0]\n"
      "v_pk_mul_f32 v[128:129], v[128:129], v[222:223] op_sel:[0,1]\n"
      "v_pk_mul_f32 v[198:199], v[198:199], v[174:175] op_sel_hi:[1,0]\n"
      "v_pk_mul_f32 v[220:221], v[220:221], v[222:223] op_sel:[0,1]\n"
      "v_pk_mul_f32 v[156:157], v[156:157], v[76:77] op_sel_hi:[1,0]\n"
      "v_pk_mul_f32 v[208:209], v[208:209], v[76:77] op_sel_hi:[1,0]\n"
      "v_mfma_f32_16x16x32_bf16 v[100:103], v[170:173], a[180:183], v[100:103]\n"
      "v_cvt_pk_bf16_f32 v155, v128, v129\n"
      "v_mul_f32_e64 v189, v174, v189\n"
      "v_mfma_f32_16x16x32_bf16 v[100:103], v[116:119], a[184:187], v[100:103]\n"
      "ds_read_b128 v[202:205], v2 offset:3072\n"
      "v_cvt_pk_bf16_f32 v76, v122, v123\n"
      "v_mfma_f32_16x16x32_bf16 v[100:103], v[112:115], a[188:191], v[100:103]\n"
      "ds_read_b128 v[126:129], v2\n"
      "v_cvt_pk_bf16_f32 v140, v140, v141\n"
      "v_mfma_f32_16x16x32_bf16 v[122:125], v[106:109], a[192:195], 0\n"
      "v_cvt_pk_bf16_f32 v219, v220, v221\n"
      "v_mul_f32 v188, v174, v188\n"
      "v_mfma_f32_16x16x32_bf16 v[122:125], v[176:179], a[196:199], v[122:125]\n"
      "ds_read_b128 v[174:177], v7 offset:4096\n"
      "v_mov_b32 v135, 0\n"
      "v_mfma_f32_16x16x32_bf16 v[122:125], v[166:169], a[200:203], v[122:125]\n"
      "ds_read_b128 v[220:223], v2 offset:6144\n"
      "v_cvt_pk_bf16_f32 v198, v198, v199\n"
      "v_mfma_f32_16x16x32_bf16 v[122:125], v[170:173], a[204:207], v[122:125]\n"
      "ds_read_b128 v[106:109], v2 offset:12288\n"
      "v_cvt_pk_bf16_f32 v141, v142, v143\n"
      "v_mfma_f32_16x16x32_bf16 v[122:125], v[116:119], a[208:211], v[122:125]\n"
      "ds_read_b128 v[170:173], v2 offset:9216\n"
      "v_cvt_pk_bf16_f32 v80, v100, v101\n"
      "v_mfma_f32_16x16x32_bf16 v[122:125], v[112:115], a[212:215], v[122:125]\n"
      "s_waitcnt lgkmcnt(4)\n"
      "v_cvt_pk_bf16_f32 v96, v96, v97\n"
      "v_mov_b32 v164, 0x3fb9\n"
      "v_mfma_f32_16x16x32_bf16 v[166:169], a[216:219], v[126:129], 0\n"
      "buffer_store_dwordx4 v[182:185], v20, s[24:27], s85 offen sc1\n"
      "v_mfma_f32_16x16x32_bf16 v[166:169], a[220:223], v[202:205], v[166:169]\n"
      "s_waitcnt lgkmcnt(2)\n"
      "v_cvt_pk_bf16_f32 v142, v206, v207\n"
      "v_add_u32 v120, 110592, v88\n"
      "v_mfma_f32_16x16x32_bf16 v[166:169], a[224:227], v[220:223], v[166:169]\n"
      "ds_read_b128 v[182:185], v2 offset:15360\n"
      "v_cvt_pk_bf16_f32 v97, v98, v99\n"
      "v_mfma_f32_16x16x32_bf16 v[112:115], a[240:243], v[126:129], 0\n"
      "ds_read2_b32 v[100:101], v120 offset0:2 offset1:10\n"
      "v_readlane_b32 s47, v38, s43\n"
      "v_mfma_f32_16x16x32_bf16 v[112:115], a[244:247], v[202:205], v[112:115]\n"
      "ds_read2_b32 v[150:151], v120 offset0:0 offset1:1\n"
      "v_cvt_pk_bf16_f32 v156, v156, v157\n"
      "v_mfma_f32_16x16x32_bf16 v[112:115], a[248:251], v[220:223], v[112:115]\n"
      "s_waitcnt lgkmcnt(3)\n"
      "v_cvt_pk_bf16_f32 v199, v200, v201\n"
      "v_cvt_pk_bf16_f32 v143, v208, v209\n"
      "v_mfma_f32_16x16x32_bf16 v[116:119], v[232:235], v[126:129], 0\n"
      "ds_read2_b32 v[192:193], v120 offset0:16 offset1:17\n"
      "v_cvt_pk_bf16_f32 v81, v102, v103\n"
      "v_mfma_f32_16x16x32_bf16 v[116:119], v[236:239], v[202:205], v[116:119]\n"
      "ds_read_b128 v[206:209], v7 offset:8192\n"
      "v_cmp_gt_u32 s[72:73], 64, v89\n"
      "v_mfma_f32_16x16x32_bf16 v[116:119], v[240:243], v[220:223], v[116:119]\n"
      "buffer_store_dwordx4 v[174:177], v21, s[24:27], s85 offen sc1\n"
      "v_mfma_f32_16x16x32_bf16 v[166:169], a[228:231], v[170:173], v[166:169]\n"
      "s_waitcnt lgkmcnt(2)\n"
      "v_cvt_pk_bf16_f32 v200, v186, v187\n"
      "v_cvt_pk_bf16_f32 v157, v158, v159\n"
      "v_mfma_f32_16x16x32_bf16 v[166:169], a[232:235], v[106:109], v[166:169]\n"
      "ds_read2_b32 v[102:103], v120 offset0:8 offset1:9\n"
      "v_mov_b32 v134, 0xbb2b\n"
      "v_mfma_f32_16x16x32_bf16 v[166:169], a[236:239], v[182:185], v[166:169]\n"
      "ds_read2_b32 v[98:99], v120 offset0:24 offset1:18\n"
      "v_mov_b32_e64 v165, 0\n"
      "v_mfma_f32_16x16x32_bf16 v[112:115], a[252:255], v[170:173], v[112:115]\n"
      "ds_read_b128 v[220:223], v10 offset:1200\n"
      "v_lshlrev_b64 v[164:165], v89, v[164:165]\n"
      "v_mfma_f32_16x16x32_bf16 v[112:115], v[224:227], v[106:109], v[112:115]\n"
      "ds_read_b64 v[104:105], v10 offset:1192\n"
      "v_cvt_pk_bf16_f32 v85, v124, v125\n"
      "v_mfma_f32_16x16x32_bf16 v[112:115], v[228:231], v[182:185], v[112:115]\n"
      "ds_read_b128 v[174:177], v7 offset:12288\n"
      "v_alignbyte_b32 v150, v151, v150, v93\n"
      "v_mfma_f32_16x16x32_bf16 v[116:119], v[244:247], v[170:173], v[116:119]\n"
      "s_waitcnt lgkmcnt(5)\n"
      "v_alignbyte_b32 v151, v100, v151, v93\n"
      "v_cndmask_b32_e64 v165, 0, v165, s[72:73]\n"
      "v_mfma_f32_16x16x32_bf16 v[116:119], v[248:251], v[106:109], v[116:119]\n"
      "buffer_store_dwordx4 v[206:209], v22, s[24:27], s85 offen sc1\n"
      "v_mfma_f32_16x16x32_bf16 v[116:119], v[252:255], v[182:185], v[116:119]\n"
      "v_alignbyte_b32 v192, v193, v192, v93\n"
      "v_fma_f32 v120, v166, v166, s52\n"
      "v_mfma_f32_16x16x32_bf16 v[202:205], v[136:139], v[160:163], v[40:43]\n"
      "s_waitcnt lgkmcnt(1)\n"
      "v_cndmask_b32_e64 v164, 0, v164, s[72:73]\n"
      "v_cvt_pk_bf16_f32 v84, v122, v123\n"
      "v_mfma_f32_16x16x32_bf16 v[126:129], v[152:155], v[160:163], v[44:47]\n"
      "v_alignbyte_b32 v102, v103, v102, v93\n"
      "v_cvt_pk_bf16_f32 v201, v188, v189\n"
      "v_mfma_f32_16x16x32_bf16 v[182:185], v[140:143], v[160:163], v[48:51]\n"
      "s_waitcnt lgkmcnt(0)\n"
      "v_cmp_gt_u32 s[72:73], 64, v89\n"
      "v_lshlrev_b64 v[134:135], v89, v[134:135]\n"
      "v_mfma_f32_16x16x32_bf16 v[122:125], v[220:223], v[160:163], v[24:27]\n"
      "ds_read_b128 v[158:161], v2 offset:6400\n"
      "v_mul_f32 v121, v167, v167\n"
      "v_mfma_f32_16x16x16_bf16 v[202:205], v[180:181], v[190:191], v[202:205]\n"
      "v_alignbyte_b32 v98, v98, v98, v93\n"
      "v_cndmask_b32_e64 v134, 0, v134, s[72:73]\n"
      "v_alignbyte_b32 v103, v101, v103, v93\n"
      "v_mfma_f32_16x16x16_bf16 v[126:129], v[218:219], v[190:191], v[126:129]\n"
      "ds_read_b128 v[170:173], v2 offset:256\n"
      "v_fmac_f32_e64 v121, v169, v169\n"
      "v_mfma_f32_16x16x16_bf16 v[182:185], v[156:157], v[190:191], v[182:185]\n"
      "buffer_store_dwordx4 v[174:177], v23, s[24:27], s85 offen sc1\n"
      "v_mfma_f32_16x16x16_bf16 v[122:125], v[104:105], v[190:191], v[122:125]\n"
      "ds_read_b128 v[186:189], v7 offset:16384\n"
      "v_alignbyte_b32 v193, v99, v193, v93\n"
      "v_mfma_f32_16x16x16_bf16 v[202:205], v[164:165], v[150:151], v[202:205]\n"
      "v_fmac_f32_e64 v120, v168, v168\n"
      "v_fmac_f32_e64 v121, v113, v113\n"
      "v_fmac_f32_e64 v120, v112, v112\n"
      "v_mfma_f32_16x16x16_bf16 v[126:129], v[164:165], v[102:103], v[126:129]\n"
      "ds_read_b128 v[106:109], v2 offset:3328\n"
      "v_cndmask_b32_e64 v135, 0, v135, s[72:73]\n"
      "v_mfma_f32_16x16x16_bf16 v[182:185], v[164:165], v[192:193], v[182:185]\n"
      "v_fmac_f32_e64 v121, v115, v115\n"
      "v_add_u32 v101, 112160, v88\n"
      "v_add_u32 v110, 113728, v88\n"
      "v_mfma_f32_16x16x16_bf16 v[122:125], v[164:165], v[98:99], v[122:125]\n"
      "ds_read2_b32 v[190:191], v101 offset0:8 offset1:9\n"
      "v_add_u32_e64 v39, s77, v39\n"
      "v_mfma_f32_16x16x16_bf16 v[202:205], v[134:135], v[150:151], v[202:205]\n"
      "ds_read2_b32 v[88:89], v101 offset0:0 offset1:1\n"
      "v_fmac_f32 v120, v114, v114\n"
      "v_mfma_f32_16x16x16_bf16 v[126:129], v[134:135], v[102:103], v[126:129]\n"
      "ds_read2_b32 v[174:175], v101 offset0:2 offset1:10\n"
      "v_add_u32 v39, 115296, v39\n"
      "v_mfma_f32_16x16x16_bf16 v[182:185], v[134:135], v[192:193], v[182:185]\n"
      "s_waitcnt lgkmcnt(4)\n"
      "buffer_store_dwordx4 v[186:189], v25, s[24:27], s85 offen sc1\n"
      "v_pk_fma_f32 v[120:121], v[116:117], v[116:117], v[120:121]\n"
      "v_pk_fma_f32 v[120:121], v[118:119], v[118:119], v[120:121]\n"
      "v_mfma_f32_16x16x16_bf16 v[122:125], v[134:135], v[98:99], v[122:125]\n"
      "v_add_f32_e64 v120, v120, v121\n"
      "v_max3_f32 v177, v205, v126, v127\n"
      "v_max3_f32 v150, v202, v203, v204\n"
      "v_max3_f32 v177, v177, v182, v183\n"
      "v_mfma_f32_16x16x32_bf16 v[186:189], a[216:219], v[170:173], 0\n"
      "s_waitcnt lgkmcnt(1)\n"
      "v_alignbyte_b32 v190, v191, v190, v93\n"
      "v_alignbyte_b32 v88, v89, v88, v93\n"
      "v_mfma_f32_16x16x32_bf16 v[186:189], a[220:223], v[106:109], v[186:189]\n"
      "s_waitcnt lgkmcnt(0)\n"
      "v_max3_f32 v150, v150, v128, v129\n"
      "v_alignbyte_b32 v89, v174, v89, v93\n"
      "v_alignbyte_b32 v191, v175, v191, v93\n"
      "v_max3_f32 v150, v150, v184, v185\n"
      "v_max3_f32 v150, v150, v177, v122\n"
      "v_mov_b32 v209, v150\n"
      "v_mfma_f32_16x16x32_bf16 v[186:189], a[224:227], v[158:161], v[186:189]\n"
      "s_waitcnt vmcnt(6)\n"
      "v_or3_b32 v144, v144, v145, v146\n"
      "v_permlane16_swap_b32 v150, v209\n"
      "v_mfma_f32_16x16x32_bf16 v[174:177], a[240:243], v[170:173], 0\n"
      "v_max_f32 v150, v150, v209\n"
      "v_mov_b32 v209, v150\n"
      "v_mfma_f32_16x16x32_bf16 v[174:177], a[244:247], v[106:109], v[174:177]\n"
      "s_nop 0\n"
      "v_permlane32_swap_b32 v150, v209\n"
      "v_mfma_f32_16x16x32_bf16 v[174:177], a[248:251], v[158:161], v[174:177]\n"
      "ds_read2_b32 v[192:193], v101 offset0:16 offset1:17\n"
      "v_max_f32 v150, v150, v209\n"
      "v_mfma_f32_16x16x32_bf16 v[206:209], v[232:235], v[170:173], 0\n"
      "ds_read2_b32 v[172:173], v101 offset0:24 offset1:18\n"
      "v_sub_f32 v126, v126, v150\n"
      "v_mfma_f32_16x16x32_bf16 v[206:209], v[236:239], v[106:109], v[206:209]\n"
      "v_sub_f32_e64 v204, v204, v150\n"
      "s_waitcnt vmcnt(5)\n"
      "v_or3_b32 v144, v144, v147, v78\n"
      "v_mfma_f32_16x16x32_bf16 v[206:209], v[240:243], v[158:161], v[206:209]\n"
      "v_sub_f32 v184, v184, v150\n"
      "v_sub_f32 v183, v183, v150\n"
      "v_sub_f32_e64 v127, v127, v150\n"
      "v_mfma_f32_16x16x32_bf16 v[158:161], v[136:139], v[210:213], v[52:55]\n"
      "v_sub_f32 v202, v202, v150\n"
      "v_sub_f32 v128, v128, v150\n"
      "v_mfma_f32_16x16x32_bf16 v[106:109], v[152:155], v[210:213], v[56:59]\n"
      "v_sub_f32_e64 v182, v182, v150\n"
      "v_sub_f32 v203, v203, v150\n"
      "v_mfma_f32_16x16x32_bf16 v[100:103], v[140:143], v[210:213], v[60:63]\n"
      "ds_write_b32 v31, v144 offset:5632\n"
      "v_mfma_f32_16x16x32_bf16 v[144:147], v[220:223], v[210:213], v[26:29]\n"
      "s_waitcnt lgkmcnt(1)\n"
      "v_alignbyte_b32 v172, v172, v172, v93\n"
      "v_sub_f32 v185, v185, v150\n"
      "v_mfma_f32_16x16x16_bf16 v[158:161], v[180:181], v[148:149], v[158:161]\n"
      "v_sub_f32 v122, v122, v150\n"
      "v_exp_f32_e64 v126, v126\n"
      "v_mfma_f32_16x16x16_bf16 v[106:109], v[218:219], v[148:149], v[106:109]\n"
      "v_exp_f32 v202, v202\n"
      "v_exp_f32 v128, v128\n"
      "v_mfma_f32_16x16x16_bf16 v[100:103], v[156:157], v[148:149], v[100:103]\n"
      "v_exp_f32 v203, v203\n"
      "v_sub_f32 v129, v129, v150\n"
      "v_mfma_f32_16x16x16_bf16 v[144:147], v[104:105], v[148:149], v[144:147]\n"
      "v_sub_f32 v205, v205, v150\n"
      "v_exp_f32 v204, v204\n"
      "v_alignbyte_b32 v192, v193, v192, v93\n"
      "v_mfma_f32_16x16x16_bf16 v[158:161], v[164:165], v[88:89], v[158:161]\n"
      "v_exp_f32_e64 v205, v205\n"
      "v_alignbyte_b32 v193, v173, v193, v93\n"
      "v_mfma_f32_16x16x16_bf16 v[106:109], v[164:165], v[190:191], v[106:109]\n"
      "ds_read_b128 v[148:151], v2 offset:9472\n"
      "v_cvt_pk_bf16_f32 v202, v202, v203\n"
      "v_mfma_f32_16x16x16_bf16 v[100:103], v[164:165], v[192:193], v[100:103]\n"
      "v_exp_f32_e64 v182, v182\n"
      "v_cvt_pk_bf16_f32 v203, v204, v205\n"
      "v_mfma_f32_16x16x16_bf16 v[144:147], v[164:165], v[172:173], v[144:147]\n"
      "v_exp_f32 v185, v185\n"
      "v_exp_f32 v129, v129\n"
      "v_mfma_f32_16x16x16_bf16 v[158:161], v[134:135], v[88:89], v[158:161]\n"
      "v_cvt_pk_bf16_f32 v205, v128, v129\n"
      "v_exp_f32 v122, v122\n"
      "v_mfma_f32_16x16x16_bf16 v[106:109], v[134:135], v[190:191], v[106:109]\n"
      "ds_read_b128 v[210:213], v2 offset:12544\n"
      "v_exp_f32 v183, v183\n"
      "v_mfma_f32_16x16x16_bf16 v[100:103], v[134:135], v[192:193], v[100:103]\n"
      "ds_read_b128 v[190:193], v2 offset:15616\n"
      "v_exp_f32 v184, v184\n"
      "v_mfma_f32_16x16x16_bf16 v[144:147], v[134:135], v[172:173], v[144:147]\n"
      "v_exp_f32 v127, v127\n"
      "v_cvt_pk_bf16_f32 v88, v182, v183\n"
      "v_cvt_pk_bf16_f32 v204, v126, v127\n"
      "v_cvt_pk_bf16_f32 v89, v184, v185\n"
      "v_cvt_pk_bf16_f32 v90, v122, 0\n"
      "v_max3_f32 v122, v158, v159, v160\n"
      "v_max3_f32 v185, v161, v106, v107\n"
      "v_max3_f32 v185, v185, v100, v101\n"
      "v_max3_f32 v122, v122, v108, v109\n"
      "v_max3_f32 v122, v122, v102, v103\n"
      "v_max3_f32 v122, v122, v185, v144\n"
      "v_mov_b32 v78, v122\n"
      "s_waitcnt lgkmcnt(2)\n"
      "v_mfma_f32_16x16x32_bf16 v[186:189], a[228:231], v[148:151], v[186:189]\n"
      "s_waitcnt lgkmcnt(1)\n"
      "v_permlane16_swap_b32 v122, v78\n"
      "v_mfma_f32_16x16x32_bf16 v[186:189], a[232:235], v[210:213], v[186:189]\n"
      "s_waitcnt lgkmcnt(0)\n"
      "v_max_f32 v122, v122, v78\n"
      "v_mov_b32 v78, v122\n"
      "v_mfma_f32_16x16x32_bf16 v[186:189], a[236:239], v[190:193], v[186:189]\n"
      "s_nop 0\n"
      "v_permlane32_swap_b32 v122, v78\n"
      "v_mfma_f32_16x16x32_bf16 v[174:177], a[252:255], v[148:151], v[174:177]\n"
      "ds_read2_b32 v[184:185], v110 offset0:0 offset1:1\n"
      "v_max_f32 v122, v122, v78\n"
      "v_mfma_f32_16x16x32_bf16 v[174:177], v[224:227], v[210:213], v[174:177]\n"
      "ds_read2_b32 v[162:163], v110 offset0:24 offset1:18\n"
      "v_sub_f32 v158, v158, v122\n"
      "v_mfma_f32_16x16x32_bf16 v[174:177], v[228:231], v[190:193], v[174:177]\n"
      "ds_read_u16 v78, v11 offset:1168\n"
      "v_sub_f32 v107, v107, v122\n"
      "v_mfma_f32_16x16x32_bf16 v[206:209], v[244:247], v[148:151], v[206:209]\n"
      "ds_read2_b32 v[178:179], v110 offset0:16 offset1:17\n"
      "v_sub_f32 v106, v106, v122\n"
      "v_mfma_f32_16x16x32_bf16 v[206:209], v[248:251], v[210:213], v[206:209]\n"
      "ds_read_u16 v86, v11 offset:1232\n"
      "v_sub_f32 v159, v159, v122\n"
      "v_mfma_f32_16x16x32_bf16 v[206:209], v[252:255], v[190:193], v[206:209]\n"
      "ds_read_u16 v82, v11 offset:1200\n"
      "v_sub_f32 v100, v100, v122\n"
      "v_sub_f32 v103, v103, v122\n"
      "v_exp_f32 v158, v158\n"
      "v_sub_f32 v144, v144, v122\n"
      "v_sub_f32 v160, v160, v122\n"
      "v_sub_f32 v161, v161, v122\n"
      "v_sub_f32 v101, v101, v122\n"
      "v_exp_f32 v159, v159\n"
      "v_sub_f32 v108, v108, v122\n"
      "v_exp_f32 v108, v108\n"
      "v_cvt_pk_bf16_f32 v158, v158, v159\n"
      "v_mfma_f32_16x16x32_bf16 v[146:149], v[32:35], v[202:205], 0\n"
      "s_waitcnt lgkmcnt(2)\n"
      "v_exp_f32 v107, v107\n"
      "v_mfma_f32_16x16x32_bf16 v[146:149], v[32:35], v[88:91], v[146:149]\n"
      "ds_read2_b32 v[182:183], v110 offset0:8 offset1:9\n"
      "v_sub_f32 v109, v109, v122\n"
      "v_exp_f32 v161, v161\n"
      "v_sub_f32 v102, v102, v122\n"
      "v_exp_f32 v160, v160\n"
      "v_mfma_f32_16x16x32_bf16 v[210:213], v[130:133], v[202:205], 0\n"
      "ds_read2_b32 v[122:123], v110 offset0:2 offset1:10\n"
      "v_cvt_pk_bf16_f32 v159, v160, v161\n"
      "v_mfma_f32_16x16x32_bf16 v[210:213], v[76:79], v[88:91], v[210:213]\n"
      "s_waitcnt lgkmcnt(2)\n"
      "v_exp_f32 v109, v109\n"
      "v_mfma_f32_16x16x32_bf16 v[148:151], v[214:217], v[202:205], 0\n"
      "ds_read_u16 v39, v39\n"
      "v_cvt_pk_bf16_f32 v161, v108, v109\n"
      "v_mfma_f32_16x16x32_bf16 v[148:151], v[80:83], v[88:91], v[148:151]\n"
      "v_exp_f32 v106, v106\n"
      "v_mfma_f32_16x16x32_bf16 v[126:129], v[194:197], v[202:205], 0\n"
      "s_waitcnt lgkmcnt(2)\n"
      "v_cvt_pk_bf16_f32 v160, v106, v107\n"
      "v_alignbyte_b32 v178, v179, v178, v93\n"
      "v_mfma_f32_16x16x32_bf16 v[126:129], v[84:87], v[88:91], v[126:129]\n"
      "v_alignbyte_b32 v182, v183, v182, v93\n"
      "v_alignbyte_b32 v179, v163, v179, v93\n"
      "v_mfma_f32_16x16x32_bf16 v[170:173], v[136:139], v[198:201], v[64:67]\n"
      "s_waitcnt lgkmcnt(1)\n"
      "v_alignbyte_b32 v184, v185, v184, v93\n"
      "v_alignbyte_b32 v183, v123, v183, v93\n"
      "v_mfma_f32_16x16x32_bf16 v[190:193], v[152:155], v[198:201], v[68:71]\n"
      "v_alignbyte_b32 v185, v122, v185, v93\n"
      "v_alignbyte_b32 v162, v162, v162, v93\n"
      "v_mfma_f32_16x16x32_bf16 v[122:125], v[140:143], v[198:201], v[72:75]\n"
      "v_exp_f32 v102, v102\n"
      "v_mfma_f32_16x16x32_bf16 v[106:109], v[220:223], v[198:201], v[28:31]\n"
      "v_exp_f32 v103, v103\n"
      "v_mfma_f32_16x16x16_bf16 v[170:173], v[180:181], v[96:97], v[170:173]\n"
      "v_exp_f32 v101, v101\n"
      "v_cvt_pk_bf16_f32 v93, v102, v103\n"
      "v_mfma_f32_16x16x16_bf16 v[190:193], v[218:219], v[96:97], v[190:193]\n"
      "v_fma_f32 v110, v186, v186, s52\n"
      "v_rcp_f32 v146, v146\n"
      "v_mfma_f32_16x16x16_bf16 v[122:125], v[156:157], v[96:97], v[122:125]\n"
      "v_mul_f32 v149, v146, v149\n"
      "v_exp_f32_e64 v100, v100\n"
      "v_mfma_f32_16x16x16_bf16 v[106:109], v[104:105], v[96:97], v[106:109]\n"
      "v_mul_f32 v210, v146, v210\n"
      "v_mul_f32 v211, v146, v211\n"
      "v_cvt_pk_bf16_f32 v92, v100, v101\n"
      "v_mfma_f32_16x16x16_bf16 v[170:173], v[164:165], v[184:185], v[170:173]\n"
      "v_mul_f32 v213, v146, v213\n"
      "v_mul_f32 v148, v146, v148\n"
      "v_mul_f32 v111, v187, v187\n"
      "v_mfma_f32_16x16x16_bf16 v[190:193], v[164:165], v[182:183], v[190:193]\n"
      "v_mul_f32 v150, v146, v150\n"
      "v_cvt_pk_bf16_f32 v210, v210, v211\n"
      "v_fmac_f32 v111, v189, v189\n"
      "v_mfma_f32_16x16x16_bf16 v[122:125], v[164:165], v[178:179], v[122:125]\n"
      "v_mul_f32 v151, v146, v151\n"
      "v_fmac_f32 v110, v188, v188\n"
      "v_mul_f32 v212, v146, v212\n"
      "v_mfma_f32_16x16x16_bf16 v[106:109], v[164:165], v[162:163], v[106:109]\n"
      "ds_read_b128 v[98:101], v2 offset:512\n"
      "v_fmac_f32 v111, v175, v175\n"
      "v_mfma_f32_16x16x16_bf16 v[170:173], v[134:135], v[184:185], v[170:173]\n"
      "v_exp_f32 v144, v144\n"
      "v_cvt_pk_bf16_f32 v211, v212, v213\n"
      "v_mfma_f32_16x16x16_bf16 v[190:193], v[134:135], v[182:183], v[190:193]\n"
      "ds_read_b128 v[182:185], v2 offset:3584\n"
      "v_cvt_pk_bf16_f32 v212, v148, v149\n"
      "v_mfma_f32_16x16x16_bf16 v[122:125], v[134:135], v[178:179], v[122:125]\n"
      "v_cvt_pk_bf16_f32 v94, v144, 0\n"
      "v_cvt_pk_bf16_f32 v213, v150, v151\n"
      "v_max3_f32 v164, v170, v171, v172\n"
      "v_mfma_f32_16x16x16_bf16 v[106:109], v[134:135], v[162:163], v[106:109]\n"
      "ds_read_b128 v[198:201], v2 offset:6656\n"
      "v_max3_f32 v144, v173, v190, v191\n"
      "v_max3_f32 v164, v164, v192, v193\n"
      "v_fmac_f32_e64 v110, v174, v174\n"
      "v_max3_f32 v144, v144, v122, v123\n"
      "v_pk_mul_f32 v[128:129], v[128:129], v[146:147] op_sel_hi:[1,0]\n"
      "v_pk_mul_f32 v[126:127], v[126:127], v[146:147] op_sel_hi:[1,0]\n"
      "v_max3_f32 v164, v164, v124, v125\n"
      "v_max3_f32 v164, v164, v144, v106\n"
      "v_mov_b32 v144, v164\n"
      "v_pk_fma_f32 v[110:111], v[176:177], v[176:177], v[110:111]\n"
      "v_pk_fma_f32 v[110:111], v[206:207], v[206:207], v[110:111]\n"
      "v_permlane16_swap_b32 v164, v144\n"
      "v_max_f32 v164, v164, v144\n"
      "v_mov_b32_e64 v144, v164\n"
      "s_waitcnt lgkmcnt(1)\n"
      "v_mfma_f32_16x16x32_bf16 v[202:205], a[216:219], v[98:101], 0\n"
      "ds_write_b128 v3, v[210:213] offset:18432\n"
      "v_pk_fma_f32 v[110:111], v[208:209], v[208:209], v[110:111]\n"
      "v_mfma_f32_16x16x32_bf16 v[202:205], a[220:223], v[182:185], v[202:205]\n"
      "s_waitcnt lgkmcnt(1)\n"
      "v_permlane32_swap_b32 v164, v144\n"
      "v_mfma_f32_16x16x32_bf16 v[202:205], a[224:227], v[198:201], v[202:205]\n"
      "v_max_f32 v164, v164, v144\n"
      "v_sub_f32 v170, v170, v164\n"
      "v_mfma_f32_16x16x32_bf16 v[144:147], a[240:243], v[98:101], 0\n"
      "v_exp_f32 v170, v170\n"
      "v_mfma_f32_16x16x32_bf16 v[144:147], a[244:247], v[182:185], v[144:147]\n"
      "v_sub_f32 v173, v173, v164\n"
      "v_sub_f32 v190, v190, v164\n"
      "v_mfma_f32_16x16x32_bf16 v[144:147], a[248:251], v[198:201], v[144:147]\n"
      "v_exp_f32 v190, v190\n"
      "v_mfma_f32_16x16x32_bf16 v[148:151], v[232:235], v[98:101], 0\n"
      "v_sub_f32 v193, v193, v164\n"
      "v_add_f32 v110, v110, v111\n"
      "v_mfma_f32_16x16x32_bf16 v[148:151], v[236:239], v[182:185], v[148:151]\n"
      "v_sub_f32 v192, v192, v164\n"
      "v_sub_f32 v106, v106, v164\n"
      "v_sub_f32 v171, v171, v164\n"
      "v_exp_f32 v171, v171\n"
      "v_mfma_f32_16x16x32_bf16 v[148:151], v[240:243], v[198:201], v[148:151]\n"
      "v_sub_f32 v125, v125, v164\n"
      "v_sub_f32 v124, v124, v164\n"
      "v_sub_f32 v191, v191, v164\n"
      "v_exp_f32 v173, v173\n"
      "v_cvt_pk_bf16_f32 v170, v170, v171\n"
      "v_sub_f32_e64 v172, v172, v164\n"
      "v_mfma_f32_16x16x32_bf16 v[210:213], v[32:35], v[158:161], 0\n"
      "v_exp_f32 v172, v172\n"
      "v_mfma_f32_16x16x32_bf16 v[210:213], v[32:35], v[92:95], v[210:213]\n"
      "ds_read_b128 v[96:99], v10 offset:1168\n"
      "v_exp_f32 v193, v193\n"
      "v_sub_f32 v123, v123, v164\n"
      "v_mfma_f32_16x16x32_bf16 v[100:103], v[130:133], v[158:161], 0\n"
      "v_exp_f32 v192, v192\n"
      "v_sub_f32_e64 v122, v122, v164\n"
      "v_mfma_f32_16x16x32_bf16 v[100:103], v[76:79], v[92:95], v[100:103]\n"
      "ds_read_b64 v[164:165], v10 offset:1184\n"
      "v_cvt_pk_bf16_f32 v171, v172, v173\n"
      "v_mfma_f32_16x16x32_bf16 v[182:185], v[214:217], v[158:161], 0\n"
      "v_exp_f32_e64 v191, v191\n"
      "v_mfma_f32_16x16x32_bf16 v[182:185], v[80:83], v[92:95], v[182:185]\n"
      "v_cvt_pk_bf16_f32 v173, v192, v193\n"
      "v_cvt_pk_bf16_f32 v172, v190, v191\n"
      "v_mfma_f32_16x16x32_bf16 v[198:201], v[194:197], v[158:161], 0\n"
      "v_exp_f32 v125, v125\n"
      "v_mfma_f32_16x16x32_bf16 v[198:201], v[84:87], v[92:95], v[198:201]\n"
      "s_waitcnt lgkmcnt(1)\n"
      "v_exp_f32_e64 v124, v124\n"
      "v_mfma_f32_16x16x32_bf16 v[158:161], v[96:99], v[136:139], v[30:33]\n"
      "ds_read_b128 v[134:137], v2 offset:9728\n"
      "v_cvt_pk_bf16_f32 v89, v124, v125\n"
      "v_mfma_f32_16x16x32_bf16 v[190:193], v[96:99], v[152:155], v[30:33]\n"
      "v_exp_f32_e64 v123, v123\n"
      "v_mfma_f32_16x16x32_bf16 v[152:155], v[96:99], v[140:143], v[30:33]\n"
      "s_waitcnt lgkmcnt(1)\n"
      "v_rcp_f32 v210, v210\n"
      "v_mfma_f32_16x16x32_bf16 v[138:141], v[96:99], v[220:223], v[30:33]\n"
      "ds_read_b128 v[220:223], v2 offset:12800\n"
      "v_cvt_pk_bf16_f32 v126, v126, v127\n"
      "v_mfma_f32_16x16x16_bf16 v[158:161], v[164:165], v[180:181], v[158:161]\n"
      "ds_read_b128 v[96:99], v2 offset:15872\n"
      "v_mul_f32_e64 v101, v210, v101\n"
      "v_mfma_f32_16x16x16_bf16 v[190:193], v[164:165], v[218:219], v[190:193]\n"
      "v_mul_f32 v185, v210, v185\n"
      "v_exp_f32 v106, v106\n"
      "v_mul_f32 v182, v210, v182\n"
      "v_exp_f32 v122, v122\n"
      "v_pk_mul_f32 v[102:103], v[102:103], v[210:211] op_sel_hi:[1,0]\n"
      "v_mfma_f32_16x16x16_bf16 v[152:155], v[164:165], v[156:157], v[152:155]\n"
      "v_mul_f32_e64 v100, v210, v100\n"
      "v_cvt_pk_bf16_f32 v100, v100, v101\n"
      "v_cvt_pk_bf16_f32 v88, v122, v123\n"
      "v_mfma_f32_16x16x16_bf16 v[138:141], v[164:165], v[104:105], v[138:141]\n"
      "v_cndmask_b32 v158, v158, v190, s[58:59]\n"
      "v_lshlrev_b32 v39, 16, v39\n"
      "s_waitcnt lgkmcnt(2)\n"
      "v_mfma_f32_16x16x32_bf16 v[202:205], a[228:231], v[134:137], v[202:205]\n"
      "s_waitcnt lgkmcnt(1)\n"
      "v_mul_f32 v184, v210, v184\n"
      "v_cndmask_b32 v158, v158, v152, s[60:61]\n"
      "v_mul_f32 v183, v210, v183\n"
      "v_mfma_f32_16x16x32_bf16 v[202:205], a[232:235], v[220:223], v[202:205]\n"
      "s_waitcnt lgkmcnt(0)\n"
      "v_cndmask_b32 v158, v158, v138, s[62:63]\n"
      "v_fmac_f32 v158, s53, v39\n"
      "v_mfma_f32_16x16x32_bf16 v[202:205], a[236:239], v[96:99], v[202:205]\n"
      "s_nop 0\n"
      "v_max_f32_dpp v218, v158, v158 row_ror:8 row_mask:0xf bank_mask:0xf\n"
      "v_cvt_pk_bf16_f32 v101, v102, v103\n"
      "v_mfma_f32_16x16x32_bf16 v[144:147], a[252:255], v[134:137], v[144:147]\n"
      "v_max_f32_dpp v218, v218, v218 row_ror:4 row_mask:0xf bank_mask:0xf\n"
      "v_cvt_pk_bf16_f32 v103, v184, v185\n"
      "v_mfma_f32_16x16x32_bf16 v[144:147], v[224:227], v[220:223], v[144:147]\n"
      "v_max_f32_dpp v218, v218, v218 row_ror:2 row_mask:0xf bank_mask:0xf\n"
      "v_cvt_pk_bf16_f32 v90, v106, 0\n"
      "v_mfma_f32_16x16x32_bf16 v[144:147], v[228:231], v[96:99], v[144:147]\n"
      "v_mul_f32 v153, v203, v203\n"
      "v_max_f32_dpp v218, v218, v218 row_ror:1 row_mask:0xf bank_mask:0xf\n"
      "v_mfma_f32_16x16x32_bf16 v[148:151], v[244:247], v[134:137], v[148:151]\n"
      "s_nop 0\n"
      "v_max_f32_dpp v218, v218, v218 row_bcast:15 row_mask:0xa bank_mask:0xf\n"
      "v_cvt_pk_bf16_f32 v102, v182, v183\n"
      "v_mfma_f32_16x16x32_bf16 v[148:151], v[248:251], v[220:223], v[148:151]\n"
      "v_permlane16_swap_b32_e64 v120, v110\n"
      "v_mfma_f32_16x16x32_bf16 v[148:151], v[252:255], v[96:99], v[148:151]\n"
      "v_fma_f32 v152, v202, v202, s52\n"
      "v_max_f32_dpp v218, v218, v218 row_bcast:31 row_mask:0xc bank_mask:0xf\n"
      "v_mfma_f32_16x16x32_bf16 v[154:157], v[32:35], v[170:173], 0\n"
      "ds_write_b128 v3, v[100:103] offset:18688\n"
      "v_pk_fma_f32 v[152:153], v[204:205], v[204:205], v[152:153]\n"
      "v_mfma_f32_16x16x32_bf16 v[154:157], v[32:35], v[88:91], v[154:157]\n"
      "v_add_f32_e64 v120, v120, v110\n"
      "v_readlane_b32 s72, v218, 63\n"
      "v_mfma_f32_16x16x32_bf16 v[218:221], v[130:133], v[170:173], 0\n"
      "s_nop 0\n"
      "v_subrev_f32 v158, s72, v158\n"
      "v_cvt_pk_bf16_f32 v127, v128, v129\n"
      "v_mfma_f32_16x16x32_bf16 v[218:221], v[76:79], v[88:91], v[218:221]\n"
      "v_exp_f32 v158, v158\n"
      "v_mfma_f32_16x16x32_bf16 v[134:137], v[214:217], v[170:173], 0\n"
      "v_rcp_f32 v154, v154\n"
      "v_mfma_f32_16x16x32_bf16 v[134:137], v[80:83], v[88:91], v[134:137]\n"
      "ds_write_b64 v4, v[126:127] offset:18432\n"
      "v_cvt_pk_bf16_f32 v158, v158, v158\n"
      "v_pk_fma_f32 v[152:153], v[144:145], v[144:145], v[152:153]\n"
      "v_pk_mul_f32 v[198:199], v[198:199], v[210:211] op_sel_hi:[1,0]\n"
      "v_pk_mul_f32 v[218:219], v[218:219], v[154:155] op_sel_hi:[1,0]\n"
      "v_pk_fma_f32 v[152:153], v[146:147], v[146:147], v[152:153]\n"
      "v_pk_fma_f32 v[152:153], v[148:149], v[148:149], v[152:153]\n"
      "v_pk_mul_f32 v[220:221], v[220:221], v[154:155] op_sel_hi:[1,0]\n"
      "v_pk_mul_f32 v[136:137], v[136:137], v[154:155] op_sel_hi:[1,0]\n"
      "v_cvt_pk_bf16_f32 v218, v218, v219\n"
      "v_pk_mul_f32 v[134:135], v[134:135], v[154:155] op_sel_hi:[1,0]\n"
      "v_mfma_f32_16x16x32_bf16 v[126:129], v[194:197], v[170:173], 0\n"
      "ds_write_b16 v13, v158\n"
      "v_cvt_pk_bf16_f32 v219, v220, v221\n"
      "v_cvt_pk_bf16_f32 v220, v134, v135\n"
      "v_cvt_pk_bf16_f32 v221, v136, v137\n"
      "v_mfma_f32_16x16x32_bf16 v[126:129], v[84:87], v[88:91], v[126:129]\n"
      "ds_write_b128 v3, v[218:221] offset:18944\n"
      "ds_read_b128 v[156:159], v14\n"
      "v_pk_fma_f32 v[152:153], v[150:151], v[150:151], v[152:153]\n"
      "v_pk_mul_f32 v[200:201], v[200:201], v[210:211] op_sel_hi:[1,0]\n"
      "v_add_f32 v152, v152, v153\n"
      "v_mov_b32 v110, v152\n"
      "v_cvt_pk_bf16_f32 v198, v198, v199\n"
      "v_cvt_pk_bf16_f32 v199, v200, v201\n"
      "v_permlane16_swap_b32 v152, v110\n"
      "s_add_u32 s43, s43, 1\n"
      "ds_write_b64 v4, v[198:199] offset:18688\n"
      "v_add_f32_e64 v152, v152, v110\n"
      "v_pk_mul_f32 v[126:127], v[126:127], v[154:155] op_sel_hi:[1,0]\n"
      "v_pk_mul_f32 v[128:129], v[128:129], v[154:155] op_sel_hi:[1,0]\n"
      "v_cvt_pk_bf16_f32 v126, v126, v127\n"
      "v_permlane32_swap_b32 v120, v152\n"
      "v_add_f32 v120, v120, v152\n"
      "ds_write_b32 v31, v120 offset:1024\n"
      "v_cvt_pk_bf16_f32 v127, v128, v129\n"
      "s_and_b32 s47, s47, -4\n"
      "ds_read_b128 v[210:213], v14 offset:64\n"
      "ds_write_b64 v4, v[126:127] offset:18944\n"
      "v_add_u32 v10, 2336, v10\n"
      "s_waitcnt lgkmcnt(4)\n"
      "v_mfma_f32_16x16x32_bf16 v[152:155], v[156:159], v[32:35], 0\n"
      "v_add_u32 v11, 2336, v11\n"
      "s_waitcnt lgkmcnt(1)\n"
      "v_mfma_f32_16x16x32_bf16 v[152:155], v[210:213], v[32:35], v[152:155]\n"
      "s_waitcnt lgkmcnt(0)\n"
      "s_barrier\n"
      "s_cbranch_vccz L_sel0_%=\n"
      "L_disp0_%=:\n"
      "s_cmp_lt_u32 s95, 0x40000000\n"
      "s_cbranch_scc1 L_nofix_18_%=\n"
      "s_cmp_eq_u32 s95, -2\n"
      "s_cbranch_scc1 L_tail0_%=\n"
      "s_nop 2\n"
      "v_lshlrev_b32 v221, 3, v36\n"
      "s_add_u32 s72, s98, L_cold_%=-L_ipf0_%=\n"
      "s_addc_u32 s73, s99, 0\n"
      "s_mov_b32 s74, L_cend_%=-L_cold_%=\n"
      "s_mov_b32 s75, 0x20000\n"
      "s_mov_b32 m0, 161792\n"
      "s_nop 0\n"
      "buffer_load_dwordx4 v221, s[72:75], 0 offen  lds\n"
      "s_lshr_b32 s92, s45, 30\n"
      "s_or_b32 s92, s92, s66\n"
      "s_cbranch_scc0 L_nf_19_%=\n"
      "s_bitcmp1_b32 s45, 31\n"
      "s_cselect_b32 s75, 100270464, 0\n"
      "s_bitcmp1_b32 s45, 30\n"
      "s_cselect_b32 s76, 196224, 0\n"
      "s_and_b32 s74, s45, 0x3fffffff\n"
      "s_cmp_ge_u32 s45, 0x40000000\n"
      "s_cselect_b32 s74, s74, 0\n"
      "s_cselect_b32 s45, 0, s45\n"
      "s_sub_u32 s74, s74, s66\n"
      "s_add_u32 s66, s66, s74\n"
      "s_sub_u32 s75, s75, s67\n"
      "s_add_u32 s67, s67, s75\n"
      "s_sub_u32 s76, s76, s68\n"
      "s_add_u32 s68, s68, s76\n"
      "v_add_u32 v16, s74, v16\n"
      "v_add_u32 v17, s74, v17\n"
      "v_add_u32 v18, s74, v18\n"
      "v_add_u32 v19, s74, v19\n"
      "v_add_u32_e64 v15, s74, v15\n"
      "s_cmp_eq_u32 s75, 0\n"
      "s_cbranch_scc1 L_ax_20_%=\n"
      "v_bfe_i32 v221, v27, 0, 1\n"
      "v_and_b32 v221, s75, v221\n"
      "v_sub_u32 v16, v16, v221\n"
      "v_bfe_i32 v221, v27, 2, 1\n"
      "v_and_b32 v221, s75, v221\n"
      "v_sub_u32 v17, v17, v221\n"
      "v_bfe_i32 v221, v27, 4, 1\n"
      "v_and_b32 v221, s75, v221\n"
      "v_sub_u32 v18, v18, v221\n"
      "v_bfe_i32 v221, v27, 6, 1\n"
      "v_and_b32 v221, s75, v221\n"
      "v_sub_u32 v19, v19, v221\n"
      "v_bfe_i32 v221, v27, 8, 1\n"
      "v_and_b32 v221, s75, v221\n"
      "v_sub_u32 v15, v15, v221\n"
      "L_ax_20_%=:\n"
      "s_cmp_eq_u32 s76, 0\n"
      "s_cbranch_scc1 L_ax_21_%=\n"
      "v_bfe_i32 v221, v27, 1, 1\n"
      "v_and_b32 v221, s76, v221\n"
      "v_sub_u32 v16, v16, v221\n"
      "v_bfe_i32 v221, v27, 3, 1\n"
      "v_and_b32 v221, s76, v221\n"
      "v_sub_u32 v17, v17, v221\n"
      "v_bfe_i32 v221, v27, 5, 1\n"
      "v_and_b32 v221, s76, v221\n"
      "v_sub_u32 v18, v18, v221\n"
      "v_bfe_i32 v221, v27, 7, 1\n"
      "v_and_b32 v221, s76, v221\n"
      "v_sub_u32 v19, v19, v221\n"
      "v_bfe_i32 v221, v27, 9, 1\n"
      "v_and_b32 v221, s76, v221\n"
      "v_sub_u32 v15, v15, v221\n"
      "L_ax_21_%=:\n"
      "s_or_b32 s48, s66, s69\n"
      "s_cmp_lg_u32 s48, 0\n"
      "s_cselect_b32 s48, 0x40000000, 0\n"
      "L_nf_19_%=:\n"
      "s_lshr_b32 s92, s46, 30\n"
      "s_or_b32 s92, s92, s69\n"
      "s_cbranch_scc0 L_nf_22_%=\n"
      "s_bitcmp1_b32 s46, 31\n"
      "s_cselect_b32 s75, 100270464, 0\n"
      "s_bitcmp1_b32 s46, 30\n"
      "s_cselect_b32 s76, 196224, 0\n"
      "s_and_b32 s74, s46, 0x3fffffff\n"
      "s_cmp_ge_u32 s46, 0x40000000\n"
      "s_cselect_b32 s74, s74, 0\n"
      "s_cselect_b32 s46, 0, s46\n"
      "s_sub_u32 s74, s74, s69\n"
      "s_add_u32 s69, s69, s74\n"
      "s_sub_u32 s75, s75, s70\n"
      "s_add_u32 s70, s70, s75\n"
      "s_sub_u32 s76, s76, s71\n"
      "s_add_u32 s71, s71, s76\n"
      "v_add_u32 v20, s74, v20\n"
      "v_add_u32 v21, s74, v21\n"
      "v_add_u32 v22, s74, v22\n"
      "v_add_u32 v23, s74, v23\n"
      "v_add_u32 v25, s74, v25\n"
      "s_cmp_eq_u32 s75, 0\n"
      "s_cbranch_scc1 L_ax_23_%=\n"
      "v_bfe_i32 v221, v27, 10, 1\n"
      "v_and_b32 v221, s75, v221\n"
      "v_sub_u32 v20, v20, v221\n"
      "v_bfe_i32 v221, v27, 12, 1\n"
      "v_and_b32 v221, s75, v221\n"
      "v_sub_u32 v21, v21, v221\n"
      "v_bfe_i32 v221, v27, 14, 1\n"
      "v_and_b32 v221, s75, v221\n"
      "v_sub_u32 v22, v22, v221\n"
      "v_bfe_i32 v221, v27, 16, 1\n"
      "v_and_b32 v221, s75, v221\n"
      "v_sub_u32 v23, v23, v221\n"
      "v_bfe_i32 v221, v27, 18, 1\n"
      "v_and_b32 v221, s75, v221\n"
      "v_sub_u32 v25, v25, v221\n"
      "L_ax_23_%=:\n"
      "s_cmp_eq_u32 s76, 0\n"
      "s_cbranch_scc1 L_ax_24_%=\n"
      "v_bfe_i32 v221, v27, 11, 1\n"
      "v_and_b32 v221, s76, v221\n"
      "v_sub_u32 v20, v20, v221\n"
      "v_bfe_i32 v221, v27, 13, 1\n"
      "v_and_b32 v221, s76, v221\n"
      "v_sub_u32 v21, v21, v221\n"
      "v_bfe_i32 v221, v27, 15, 1\n"
      "v_and_b32 v221, s76, v221\n"
      "v_sub_u32 v22, v22, v221\n"
      "v_bfe_i32 v221, v27, 17, 1\n"
      "v_and_b32_e64 v221, s76, v221\n"
      "v_sub_u32_e64 v23, v23, v221\n"
      "v_bfe_i32 v221, v27, 19, 1\n"
      "v_and_b32 v221, s76, v221\n"
      "v_sub_u32_e64 v25, v25, v221\n"
      "L_ax_24_%=:\n"
      "s_or_b32 s48, s66, s69\n"
      "s_cmp_lg_u32 s48, 0\n"
      "s_cselect_b32 s48, 0x40000000, 0\n"
      "L_nf_22_%=:\n"
      "s_sub_u32 s92, s42, 1\n"
      "s_cmp_gt_u32 s92, s44\n"
      "s_cbranch_scc1 L_tail0_%=\n"
      "L_nofix_18_%=:\n"
      "s_cmp_lg_u64 s[56:57], 0\n"
      "s_cbranch_scc1 L_slow0_%=\n"
      "L_sel0_%=:\n"
      "s_cmp_lg_u64 s[54:55], 0\n"
      "s_cbranch_scc1 L_max0_%=\n"
      "s_branch L_loop0_%=\n"
      "L_slow0_%=:\n"
      "s_nop 4\n"
      "v_mfma_f32_16x16x32_bf16 v[218:221], v[156:159], v[130:133], 0\n"
      "ds_read_b128 v[138:141], v0\n"
      "v_readlane_b32 s85, v37, s42\n"
      "v_mfma_f32_16x16x32_bf16 v[218:221], v[210:213], v[76:79], v[218:221]\n"
      "s_add_u32 s72, s42, 1\n"
      "ds_read_b128 v[96:99], v1 offset:6144\n"
      "v_mfma_f32_16x16x32_bf16 v[134:137], v[156:159], v[214:217], 0\n"
      "ds_read_b128 v[198:201], v9 offset:1024\n"
      "v_subrev_u32 v132, 115456, v9\n"
      "v_mfma_f32_16x16x32_bf16 v[134:137], v[210:213], v[80:83], v[134:137]\n"
      "ds_read_b128 v[100:103], v0 offset:6144\n"
      "v_subrev_u32 v39, 115456, v9\n"
      "v_mfma_f32_16x16x32_bf16 v[128:131], v[156:159], v[194:197], 0\n"
      "ds_read_b128 v[162:165], v1\n"
      "v_readlane_b32 s77, v38, s72\n"
      "v_mfma_f32_16x16x32_bf16 v[128:131], v[210:213], v[84:87], v[128:131]\n"
      "s_and_b32 s73, s77, -16\n"
      "ds_read_b128 v[210:213], v0 offset:12288\n"
      "v_mov_b32_dpp v152, v152 quad_perm:[0,0,0,0] row_mask:0xf bank_mask:0xf\n"
      "s_waitcnt lgkmcnt(5)\n"
      "v_mfma_f32_16x16x32_bf16 v[220:223], a[0:3], v[138:141], 0\n"
      "s_mov_b32 m0, 110592\n"
      "v_rcp_f32_e64 v152, v152\n"
      "v_mfma_f32_16x16x32_bf16 v[170:173], a[24:27], v[138:141], 0\n"
      "buffer_load_dwordx4 v132, s[28:31], s73 offen  lds\n"
      "v_mfma_f32_16x16x32_bf16 v[104:107], a[48:51], v[138:141], 0\n"
      "s_waitcnt lgkmcnt(3)\n"
      "v_add_f32 v198, v198, v199\n"
      "v_add_f32_e64 v200, v200, v201\n"
      "v_mfma_f32_16x16x32_bf16 v[158:161], a[72:75], v[138:141], 0\n"
      "ds_read_b128 v[180:183], v1 offset:12288\n"
      "v_subrev_u32 v77, 115456, v9\n"
      "v_mfma_f32_16x16x32_bf16 v[124:127], a[96:99], v[138:141], 0\n"
      "s_mov_b32 m0, 111616\n"
      "v_readlane_b32 s84, v37, s43\n"
      "v_add_f32 v198, v198, v200\n"
      "v_mfma_f32_16x16x32_bf16 v[190:193], a[120:123], v[138:141], 0\n"
      "s_waitcnt lgkmcnt(1)\n"
      "v_rsq_f32_e64 v198, v198\n"
      "v_mfma_f32_16x16x32_bf16 v[220:223], a[4:7], v[162:165], v[220:223]\n"
      "s_add_u32 s73, s73, 1024\n"
      "v_mul_f32 v198, 0x415db3d7, v198\n"
      "v_mov_b32_e64 v200, v198\n"
      "v_mfma_f32_16x16x32_bf16 v[220:223], a[8:11], v[100:103], v[220:223]\n"
      "buffer_load_dwordx4 v132, s[28:31], s73 offen  lds\n"
      "v_mfma_f32_16x16x32_bf16 v[170:173], a[28:31], v[162:165], v[170:173]\n"
      "v_permlane32_swap_b32_e64 v198, v200\n"
      "v_mfma_f32_16x16x32_bf16 v[170:173], a[32:35], v[100:103], v[170:173]\n"
      "s_mov_b32 m0, 112640\n"
      "v_mov_b32 v199, v198\n"
      "v_mul_f32 v146, v200, v146\n"
      "v_mfma_f32_16x16x32_bf16 v[104:107], a[52:55], v[162:165], v[104:107]\n"
      "v_permlane16_swap_b32 v198, v199\n"
      "v_mfma_f32_16x16x32_bf16 v[104:107], a[56:59], v[100:103], v[104:107]\n"
      "s_add_u32 s73, s73, 1024\n"
      "v_mul_f32 v117, v198, v117\n"
      "v_mul_f32_e64 v115, v198, v115\n"
      "v_mfma_f32_16x16x32_bf16 v[158:161], a[76:79], v[162:165], v[158:161]\n"
      "buffer_load_dwordx4 v132, s[28:31], s73 offen  lds\n"
      "v_mfma_f32_16x16x32_bf16 v[158:161], a[80:83], v[100:103], v[158:161]\n"
      "v_mul_f32 v207, v199, v207\n"
      "v_mul_f32 v188, v199, v188\n"
      "v_mfma_f32_16x16x32_bf16 v[124:127], a[100:103], v[162:165], v[124:127]\n"
      "s_waitcnt lgkmcnt(0)\n"
      "v_mul_f32 v114, v198, v114\n"
      "v_mul_f32 v166, v198, v166\n"
      "v_mfma_f32_16x16x32_bf16 v[124:127], a[104:107], v[100:103], v[124:127]\n"
      "s_or_b32 s95, s84, s85\n"
      "v_mul_f32 v168, v198, v168\n"
      "v_mul_f32 v167, v198, v167\n"
      "v_mfma_f32_16x16x32_bf16 v[190:193], a[124:127], v[162:165], v[190:193]\n"
      "s_add_u32 s73, s73, 1024\n"
      "v_mul_f32 v205, v200, v205\n"
      "v_mul_f32 v116, v198, v116\n"
      "v_mfma_f32_16x16x32_bf16 v[190:193], a[128:131], v[100:103], v[190:193]\n"
      "s_mov_b32 m0, 113664\n"
      "v_cvt_pk_bf16_f32 v166, v166, v167\n"
      "v_mul_f32_e64 v169, v198, v169\n"
      "v_mfma_f32_16x16x32_bf16 v[220:223], a[12:15], v[96:99], v[220:223]\n"
      "buffer_load_dwordx4 v132, s[28:31], s73 offen  lds\n"
      "v_mfma_f32_16x16x32_bf16 v[220:223], a[16:19], v[210:213], v[220:223]\n"
      "s_mov_b32 m0, 114688\n"
      "v_mul_f32 v113, v198, v113\n"
      "v_cvt_pk_bf16_f32 v167, v168, v169\n"
      "v_mfma_f32_16x16x32_bf16 v[220:223], a[20:23], v[180:183], v[220:223]\n"
      "s_add_u32 s73, s73, 1024\n"
      "v_mul_f32 v112, v198, v112\n"
      "v_mul_f32 v206, v199, v206\n"
      "v_mfma_f32_16x16x32_bf16 v[170:173], a[36:39], v[96:99], v[170:173]\n"
      "v_mul_f32 v202, v200, v202\n"
      "v_cvt_pk_bf16_f32 v169, v114, v115\n"
      "v_mfma_f32_16x16x32_bf16 v[170:173], a[40:43], v[210:213], v[170:173]\n"
      "s_or_b32 s95, s95, s48\n"
      "v_cvt_pk_bf16_f32 v168, v112, v113\n"
      "v_cvt_pk_bf16_f32 v116, v116, v117\n"
      "v_mfma_f32_16x16x32_bf16 v[170:173], a[44:47], v[180:183], v[170:173]\n"
      "s_mov_b32 exec_hi, 0xffff\n"
      "buffer_load_dwordx4 v132, s[28:31], s73 offen  lds\n"
      "s_mov_b32 exec_hi, -1\n"
      "v_pk_mul_f32 v[208:209], v[208:209], v[198:199] op_sel:[0,1]\n"
      "v_mfma_f32_16x16x32_bf16 v[104:107], a[60:63], v[96:99], v[104:107]\n"
      "ds_write_b128 v5, v[166:169]\n"
      "v_pk_mul_f32 v[186:187], v[186:187], v[198:199] op_sel:[0,1]\n"
      "v_pk_mul_f32 v[118:119], v[118:119], v[198:199] op_sel_hi:[1,0]\n"
      "v_mfma_f32_16x16x32_bf16 v[104:107], a[64:67], v[210:213], v[104:107]\n"
      "s_add_u32 m0, s64, 18432\n"
      "v_cvt_pk_bf16_f32 v117, v118, v119\n"
      "v_cvt_pk_bf16_f32 v206, v206, v207\n"
      "v_mfma_f32_16x16x32_bf16 v[104:107], a[68:71], v[180:183], v[104:107]\n"
      "v_mul_f32 v203, v200, v203\n"
      "v_mul_f32 v174, v199, v174\n"
      "v_mfma_f32_16x16x32_bf16 v[158:161], a[84:87], v[96:99], v[158:161]\n"
      "ds_write_b64 v6, v[116:117]\n"
      "v_mfma_f32_16x16x32_bf16 v[158:161], a[88:91], v[210:213], v[158:161]\n"
      "buffer_load_dwordx4 v16, s[16:19], s45 offen  lds\n"
      "v_mfma_f32_16x16x32_bf16 v[158:161], a[92:95], v[180:183], v[158:161]\n"
      "v_mul_f32_e64 v176, v199, v176\n"
      "v_cvt_pk_bf16_f32 v202, v202, v203\n"
      "v_mfma_f32_16x16x32_bf16 v[124:127], a[108:111], v[96:99], v[124:127]\n"
      "s_cmp_ge_u32 s95, 0x40000000\n"
      "s_cselect_b32 s94, -1, -2\n"
      "v_cndmask_b32 v218, v218, v134, s[58:59]\n"
      "v_mfma_f32_16x16x32_bf16 v[124:127], a[112:115], v[210:213], v[124:127]\n"
      "s_add_u32 m0, s64, 22528\n"
      "v_mul_f32 v189, v199, v189\n"
      "v_fma_f32 v81, v220, v220, s51\n"
      "v_mfma_f32_16x16x32_bf16 v[124:127], a[116:119], v[180:183], v[124:127]\n"
      "v_mul_f32 v175, v199, v175\n"
      "v_cvt_pk_bf16_f32 v186, v186, v187\n"
      "v_mfma_f32_16x16x32_bf16 v[190:193], a[132:135], v[96:99], v[190:193]\n"
      "buffer_load_dwordx4 v17, s[16:19], s45 offen  lds\n"
      "v_mfma_f32_16x16x32_bf16 v[190:193], a[136:139], v[210:213], v[190:193]\n"
      "ds_read_b128 v[166:169], v0 offset:8192\n"
      "v_cvt_pk_bf16_f32 v187, v188, v189\n"
      "v_mfma_f32_16x16x32_bf16 v[190:193], a[140:143], v[180:183], v[190:193]\n"
      "v_mul_f32_e64 v177, v199, v177\n"
      "v_cvt_pk_bf16_f32 v189, v176, v177\n"
      "v_mfma_f32_16x16x32_bf16 v[130:133], v[138:141], a[144:147], 0\n"
      "s_add_u32 m0, s64, 26624\n"
      "v_cvt_pk_bf16_f32 v207, v208, v209\n"
      "v_cvt_pk_bf16_f32 v188, v174, v175\n"
      "v_mfma_f32_16x16x32_bf16 v[130:133], v[162:165], a[148:151], v[130:133]\n"
      "ds_write_b128 v5, v[186:189] offset:6144\n"
      "v_mfma_f32_16x16x32_bf16 v[130:133], v[100:103], a[152:155], v[130:133]\n"
      "buffer_load_dwordx4 v18, s[16:19], s45 offen  lds\n"
      "v_mfma_f32_16x16x32_bf16 v[130:133], v[96:99], a[156:159], v[130:133]\n"
      "v_fmac_f32 v81, v221, v221\n"
      "v_mul_f32 v145, v200, v145\n"
      "v_fmac_f32 v81, v222, v222\n"
      "v_mfma_f32_16x16x32_bf16 v[130:133], v[210:213], a[160:163], v[130:133]\n"
      "ds_write_b64 v6, v[206:207] offset:6144\n"
      "v_mul_f32 v204, v200, v204\n"
      "v_mfma_f32_16x16x32_bf16 v[130:133], v[180:183], a[164:167], v[130:133]\n"
      "s_add_u32 m0, s64, 30720\n"
      "v_mul_f32 v147, v200, v147\n"
      "v_mul_f32 v144, v200, v144\n"
      "v_mfma_f32_16x16x32_bf16 v[214:217], v[138:141], a[168:171], 0\n"
      "buffer_load_dwordx4 v19, s[16:19], s45 offen  lds\n"
      "v_mfma_f32_16x16x32_bf16 v[214:217], v[162:165], a[172:175], v[214:217]\n"
      "ds_read_b128 v[206:209], v0 offset:2048\n"
      "v_fma_f32 v76, v158, v158, s51\n"
      "v_mfma_f32_16x16x32_bf16 v[214:217], v[100:103], a[176:179], v[214:217]\n"
      "ds_read_b128 v[110:113], v1 offset:2048\n"
      "v_bfe_u32 v78, v77, 4, 4\n"
      "v_mfma_f32_16x16x32_bf16 v[214:217], v[96:99], a[180:183], v[214:217]\n"
      "s_add_u32 m0, s65, 34816\n"
      "v_fmac_f32_e64 v81, v223, v223\n"
      "v_cvt_pk_bf16_f32 v203, v204, v205\n"
      "v_mfma_f32_16x16x32_bf16 v[214:217], v[210:213], a[184:187], v[214:217]\n"
      "v_fmac_f32 v81, v170, v170\n"
      "v_fmac_f32 v76, v159, v159\n"
      "v_mfma_f32_16x16x32_bf16 v[214:217], v[180:183], a[188:191], v[214:217]\n"
      "buffer_load_dwordx4 v15, s[16:19], s45 offen  lds\n"
      "v_mfma_f32_16x16x32_bf16 v[194:197], v[138:141], a[192:195], 0\n"
      "ds_read_b128 v[114:117], v0 offset:14336\n"
      "v_fmac_f32_e64 v76, v160, v160\n"
      "v_mfma_f32_16x16x32_bf16 v[194:197], v[162:165], a[196:199], v[194:197]\n"
      "ds_read_b128 v[162:165], v1 offset:14336\n"
      "v_cvt_pk_bf16_f32 v204, v144, v145\n"
      "v_mfma_f32_16x16x32_bf16 v[194:197], v[100:103], a[200:203], v[194:197]\n"
      "ds_read_b128 v[138:141], v1 offset:8192\n"
      "v_fmac_f32 v76, v161, v161\n"
      "v_mfma_f32_16x16x32_bf16 v[194:197], v[96:99], a[204:207], v[194:197]\n"
      "s_waitcnt lgkmcnt(3)\n"
      "v_cvt_pk_bf16_f32 v205, v146, v147\n"
      "v_fmac_f32_e64 v76, v124, v124\n"
      "v_mfma_f32_16x16x32_bf16 v[194:197], v[210:213], a[208:211], v[194:197]\n"
      "buffer_load_dwordx4 v[96:99], v36, s[28:31], s47 offen\n"
      "v_mfma_f32_16x16x32_bf16 v[194:197], v[180:183], a[212:215], v[194:197]\n"
      "ds_write_b128 v5, v[202:205] offset:12288\n"
      "v_pk_mul_f32 v[148:149], v[148:149], v[200:201] op_sel_hi:[1,0]\n"
      "v_mfma_f32_16x16x32_bf16 v[210:213], a[0:3], v[206:209], 0\n"
      "v_fmac_f32 v76, v125, v125\n"
      "v_mul_f32 v150, v200, v150\n"
      "v_mfma_f32_16x16x32_bf16 v[210:213], a[4:7], v[110:113], v[210:213]\n"
      "s_waitcnt lgkmcnt(1)\n"
      "v_fmac_f32 v81, v171, v171\n"
      "v_mfma_f32_16x16x32_bf16 v[210:213], a[8:11], v[166:169], v[210:213]\n"
      "buffer_load_dword v86, v29, s[28:31], s47 offen\n"
      "v_mfma_f32_16x16x32_bf16 v[210:213], a[12:15], v[138:141], v[210:213]\n"
      "v_fmac_f32 v76, v126, v126\n"
      "v_cvt_pk_bf16_f32 v214, v214, v215\n"
      "v_mfma_f32_16x16x32_bf16 v[210:213], a[16:19], v[114:117], v[210:213]\n"
      "v_fmac_f32 v81, v172, v172\n"
      "v_fmac_f32 v81, v173, v173\n"
      "v_mfma_f32_16x16x32_bf16 v[210:213], a[20:23], v[162:165], v[210:213]\n"
      "v_mul_u32_u24 v88, 98, v78\n"
      "v_fmac_f32 v81, v104, v104\n"
      "v_mfma_f32_16x16x32_bf16 v[134:137], a[24:27], v[206:209], 0\n"
      "v_fmac_f32 v81, v105, v105\n"
      "v_cvt_pk_bf16_f32 v194, v194, v195\n"
      "v_mfma_f32_16x16x32_bf16 v[134:137], a[28:31], v[110:113], v[134:137]\n"
      "v_fmac_f32 v76, v127, v127\n"
      "v_fmac_f32_e64 v76, v190, v190\n"
      "v_mfma_f32_16x16x32_bf16 v[134:137], a[32:35], v[166:169], v[134:137]\n"
      "v_fma_f32 v84, v210, v210, s51\n"
      "v_mul_f32_e64 v151, v200, v151\n"
      "v_mfma_f32_16x16x32_bf16 v[134:137], a[36:39], v[138:141], v[134:137]\n"
      "s_add_u32 s73, s73, 1024\n"
      "v_fmac_f32 v84, v211, v211\n"
      "v_fmac_f32 v81, v106, v106\n"
      "v_mfma_f32_16x16x32_bf16 v[134:137], a[40:43], v[114:117], v[134:137]\n"
      "v_fmac_f32 v76, v191, v191\n"
      "v_fmac_f32 v84, v212, v212\n"
      "v_mfma_f32_16x16x32_bf16 v[134:137], a[44:47], v[162:165], v[134:137]\n"
      "v_fmac_f32_e64 v76, v192, v192\n"
      "v_cvt_pk_bf16_f32 v148, v148, v149\n"
      "v_mfma_f32_16x16x32_bf16 v[198:201], a[48:51], v[206:209], 0\n"
      "v_cndmask_b32 v218, v218, v128, s[60:61]\n"
      "v_fmac_f32 v76, v193, v193\n"
      "v_mfma_f32_16x16x32_bf16 v[198:201], a[52:55], v[110:113], v[198:201]\n"
      "v_lshrrev_b32 v39, 3, v39\n"
      "v_fmac_f32_e64 v84, v213, v213\n"
      "v_mfma_f32_16x16x32_bf16 v[198:201], a[56:59], v[166:169], v[198:201]\n"
      "v_fmac_f32 v84, v134, v134\n"
      "v_fmac_f32 v81, v107, v107\n"
      "v_mfma_f32_16x16x32_bf16 v[198:201], a[60:63], v[138:141], v[198:201]\n"
      "s_nop 0\n"
      "v_permlane16_swap_b32 v81, v76\n"
      "v_mfma_f32_16x16x32_bf16 v[198:201], a[64:67], v[114:117], v[198:201]\n"
      "v_add_f32 v81, v81, v76\n"
      "v_mov_b32 v76, v81\n"
      "v_mfma_f32_16x16x32_bf16 v[198:201], a[68:71], v[162:165], v[198:201]\n"
      "s_nop 0\n"
      "v_permlane32_swap_b32 v81, v76\n"
      "v_mfma_f32_16x16x32_bf16 v[182:185], a[72:75], v[206:209], 0\n"
      "ds_read_b128 v[118:121], v9 offset:5632\n"
      "v_fmac_f32 v84, v135, v135\n"
      "v_mfma_f32_16x16x32_bf16 v[182:185], a[76:79], v[110:113], v[182:185]\n"
      "v_fmac_f32 v84, v136, v136\n"
      "v_cvt_pk_bf16_f32 v149, v150, v151\n"
      "v_mfma_f32_16x16x32_bf16 v[182:185], a[80:83], v[166:169], v[182:185]\n"
      "v_lshrrev_b32 v77, 8, v77\n"
      "v_mul_f32 v218, v152, v218\n"
      "v_mfma_f32_16x16x32_bf16 v[182:185], a[84:87], v[138:141], v[182:185]\n"
      "v_cvt_pk_bf16_f32 v218, v218, v218\n"
      "v_add_f32 v81, v81, v76\n"
      "v_mfma_f32_16x16x32_bf16 v[182:185], a[88:91], v[114:117], v[182:185]\n"
      "v_rsq_f32 v81, v81\n"
      "v_mfma_f32_16x16x32_bf16 v[182:185], a[92:95], v[162:165], v[182:185]\n"
      "v_fmac_f32 v84, v137, v137\n"
      "v_mov_b32 v76, v81\n"
      "v_mfma_f32_16x16x32_bf16 v[174:177], a[96:99], v[206:209], 0\n"
      "s_nop 0\n"
      "v_permlane16_swap_b32 v81, v76\n"
      "v_mfma_f32_16x16x32_bf16 v[174:177], a[100:103], v[110:113], v[174:177]\n"
      "v_fmac_f32_e64 v84, v198, v198\n"
      "v_cvt_pk_bf16_f32 v130, v130, v131\n"
      "v_mfma_f32_16x16x32_bf16 v[174:177], a[104:107], v[166:169], v[174:177]\n"
      "ds_read_b128 v[150:153], v0 offset:10240\n"
      "v_fma_f32 v128, v182, v182, s51\n"
      "v_mfma_f32_16x16x32_bf16 v[174:177], a[108:111], v[138:141], v[174:177]\n"
      "ds_write_b64 v6, v[148:149] offset:12288\n"
      "v_mfma_f32_16x16x32_bf16 v[174:177], a[112:115], v[114:117], v[174:177]\n"
      "v_fmac_f32 v84, v199, v199\n"
      "v_fmac_f32 v128, v183, v183\n"
      "v_mfma_f32_16x16x32_bf16 v[174:177], a[116:119], v[162:165], v[174:177]\n"
      "v_mul_f32_e64 v158, v76, v158\n"
      "v_cvt_pk_bf16_f32 v215, v216, v217\n"
      "v_mfma_f32_16x16x32_bf16 v[100:103], a[120:123], v[206:209], 0\n"
      "v_readlane_b32 s86, v38, s43\n"
      "v_cvt_pk_bf16_f32 v131, v132, v133\n"
      "v_mfma_f32_16x16x32_bf16 v[100:103], a[124:127], v[110:113], v[100:103]\n"
      "v_fmac_f32 v128, v184, v184\n"
      "v_lshl_add_u32 v88, v77, 3, v88\n"
      "v_mfma_f32_16x16x32_bf16 v[100:103], a[128:131], v[166:169], v[100:103]\n"
      "ds_read_b128 v[142:145], v0 offset:4096\n"
      "v_mul_f32 v81, s50, v81\n"
      "v_mfma_f32_16x16x32_bf16 v[100:103], a[132:135], v[138:141], v[100:103]\n"
      "s_waitcnt lgkmcnt(2)\n"
      "v_mul_f32 v192, v76, v192\n"
      "v_mul_f32 v159, v76, v159\n"
      "v_mfma_f32_16x16x32_bf16 v[100:103], a[136:139], v[114:117], v[100:103]\n"
      "s_cmp_gt_u32 s42, s44\n"
      "s_cselect_b32 s95, s94, s95\n"
      "v_or3_b32 v118, v118, v119, v120\n"
      "v_mfma_f32_16x16x32_bf16 v[100:103], a[140:143], v[162:165], v[100:103]\n"
      "v_fmac_f32 v128, v185, v185\n"
      "v_bitop3_b32 v118, v118, v121, s33 bitop3:0xa8\n"
      "v_mfma_f32_16x16x32_bf16 v[202:205], v[206:209], a[144:147], 0\n"
      "ds_write_b16 v12, v218 offset:16\n"
      "v_mfma_f32_16x16x32_bf16 v[202:205], v[110:113], a[148:151], v[202:205]\n"
      "v_cvt_pk_bf16_f32 v158, v158, v159\n"
      "v_fmac_f32 v128, v174, v174\n"
      "v_mfma_f32_16x16x32_bf16 v[202:205], v[166:169], a[152:155], v[202:205]\n"
      "v_cmp_ne_u32 s[56:57], 0, v118\n"
      "v_mul_f32 v127, v76, v127\n"
      "v_mfma_f32_16x16x32_bf16 v[202:205], v[138:141], a[156:159], v[202:205]\n"
      "v_mul_f32 v191, v76, v191\n"
      "v_mul_f32 v106, v81, v106\n"
      "v_mfma_f32_16x16x32_bf16 v[202:205], v[114:117], a[160:163], v[202:205]\n"
      "v_mul_f32_e64 v221, v81, v221\n"
      "v_cvt_pk_bf16_f32 v195, v196, v197\n"
      "v_mfma_f32_16x16x32_bf16 v[202:205], v[162:165], a[164:167], v[202:205]\n"
      "ds_read_b128 v[154:157], v1 offset:10240\n"
      "v_mul_f32 v190, v76, v190\n"
      "v_mfma_f32_16x16x32_bf16 v[186:189], v[206:209], a[168:171], 0\n"
      "v_mul_f32 v126, v76, v126\n"
      "v_mul_f32 v160, v76, v160\n"
      "v_mfma_f32_16x16x32_bf16 v[186:189], v[110:113], a[172:175], v[186:189]\n"
      "s_cmp_ge_u32 s95, 0x40000000\n"
      "s_cselect_b64 vcc, -1, s[56:57]\n"
      "v_fmac_f32 v128, v175, v175\n"
      "v_mfma_f32_16x16x32_bf16 v[186:189], v[166:169], a[176:179], v[186:189]\n"
      "ds_read_b128 v[120:123], v1 offset:16384\n"
      "v_fmac_f32 v128, v176, v176\n"
      "v_mfma_f32_16x16x32_bf16 v[186:189], v[138:141], a[180:183], v[186:189]\n"
      "v_fmac_f32 v84, v200, v200\n"
      "v_lshlrev_b32_e64 v77, 2, v77\n"
      "v_mfma_f32_16x16x32_bf16 v[186:189], v[114:117], a[184:187], v[186:189]\n"
      "v_fmac_f32_e64 v128, v177, v177\n"
      "v_min_u32 v39, 96, v39\n"
      "v_mfma_f32_16x16x32_bf16 v[186:189], v[162:165], a[188:191], v[186:189]\n"
      "ds_read_b128 v[146:149], v1 offset:4096\n"
      "v_cvt_pk_bf16_f32 v132, v202, v203\n"
      "v_mfma_f32_16x16x32_bf16 v[178:181], v[206:209], a[192:195], 0\n"
      "v_cvt_pk_bf16_f32 v133, v204, v205\n"
      "v_fmac_f32 v128, v100, v100\n"
      "v_mfma_f32_16x16x32_bf16 v[178:181], v[110:113], a[196:199], v[178:181]\n"
      "v_fmac_f32 v128, v101, v101\n"
      "v_mul_f32_e64 v222, v81, v222\n"
      "v_mfma_f32_16x16x32_bf16 v[178:181], v[166:169], a[200:203], v[178:181]\n"
      "ds_read_b128 v[166:169], v0 offset:16384\n"
      "v_mul_f32 v193, v76, v193\n"
      "v_mfma_f32_16x16x32_bf16 v[178:181], v[138:141], a[204:207], v[178:181]\n"
      "v_cvt_pk_bf16_f32 v216, v186, v187\n"
      "v_mul_f32 v170, v81, v170\n"
      "v_mfma_f32_16x16x32_bf16 v[178:181], v[114:117], a[208:211], v[178:181]\n"
      "v_mul_f32 v161, v76, v161\n"
      "v_fmac_f32 v128, v102, v102\n"
      "v_mfma_f32_16x16x32_bf16 v[178:181], v[162:165], a[212:215], v[178:181]\n"
      "s_waitcnt lgkmcnt(1)\n"
      "v_sub_u32 v78, v78, v77\n"
      "v_fmac_f32 v84, v201, v201\n"
      "v_mfma_f32_16x16x32_bf16 v[202:205], a[0:3], v[142:145], 0\n"
      "v_mul_f32 v107, v81, v107\n"
      "v_fmac_f32 v128, v103, v103\n"
      "v_mfma_f32_16x16x32_bf16 v[202:205], a[4:7], v[146:149], v[202:205]\n"
      "v_mul_f32 v220, v81, v220\n"
      "v_mul_f32 v124, v76, v124\n"
      "v_mfma_f32_16x16x32_bf16 v[202:205], a[8:11], v[150:153], v[202:205]\n"
      "v_permlane16_swap_b32_e64 v84, v128\n"
      "v_mfma_f32_16x16x32_bf16 v[202:205], a[12:15], v[154:157], v[202:205]\n"
      "s_waitcnt lgkmcnt(0)\n"
      "v_cvt_pk_bf16_f32 v220, v220, v221\n"
      "v_mul_f32 v172, v81, v172\n"
      "v_cvt_pk_bf16_f32 v217, v188, v189\n"
      "v_mfma_f32_16x16x32_bf16 v[202:205], a[16:19], v[166:169], v[202:205]\n"
      "v_add_f32 v84, v84, v128\n"
      "v_cvt_pk_bf16_f32 v190, v190, v191\n"
      "v_mfma_f32_16x16x32_bf16 v[202:205], a[20:23], v[120:123], v[202:205]\n"
      "v_mul_f32 v171, v81, v171\n"
      "v_mov_b32 v128, v84\n"
      "v_mfma_f32_16x16x32_bf16 v[186:189], a[24:27], v[142:145], 0\n"
      "s_nop 0\n"
      "v_permlane32_swap_b32 v84, v128\n"
      "v_mfma_f32_16x16x32_bf16 v[186:189], a[28:31], v[146:149], v[186:189]\n"
      "s_and_b32 s77, s77, 15\n"
      "v_add_f32 v84, v84, v128\n"
      "v_mul_f32_e64 v223, v81, v223\n"
      "v_mfma_f32_16x16x32_bf16 v[186:189], a[32:35], v[150:153], v[186:189]\n"
      "v_fma_f32 v218, v202, v202, s51\n"
      "v_add_u32 v88, s77, v88\n"
      "v_mfma_f32_16x16x32_bf16 v[186:189], a[36:39], v[154:157], v[186:189]\n"
      "v_rsq_f32 v84, v84\n"
      "v_mfma_f32_16x16x32_bf16 v[186:189], a[40:43], v[166:169], v[186:189]\n"
      "v_cvt_pk_bf16_f32 v159, v160, v161\n"
      "v_fmac_f32 v218, v203, v203\n"
      "v_mfma_f32_16x16x32_bf16 v[186:189], a[44:47], v[120:123], v[186:189]\n"
      "v_fmac_f32 v218, v204, v204\n"
      "v_mul_f32 v105, v81, v105\n"
      "v_mfma_f32_16x16x32_bf16 v[162:165], a[48:51], v[142:145], 0\n"
      "v_fmac_f32 v218, v205, v205\n"
      "v_mov_b32 v128, v84\n"
      "v_mfma_f32_16x16x32_bf16 v[162:165], a[52:55], v[146:149], v[162:165]\n"
      "s_nop 0\n"
      "v_permlane16_swap_b32 v84, v128\n"
      "v_mfma_f32_16x16x32_bf16 v[162:165], a[56:59], v[150:153], v[162:165]\n"
      "v_cvt_pk_bf16_f32 v191, v192, v193\n"
      "v_mul_f32 v84, s50, v84\n"
      "v_mfma_f32_16x16x32_bf16 v[162:165], a[60:63], v[154:157], v[162:165]\n"
      "v_mul_f32 v185, v128, v185\n"
      "v_mul_f32 v136, v84, v136\n"
      "v_fmac_f32 v218, v186, v186\n"
      "v_mfma_f32_16x16x32_bf16 v[162:165], a[64:67], v[166:169], v[162:165]\n"
      "v_fmac_f32 v218, v187, v187\n"
      "v_mul_f32_e64 v211, v84, v211\n"
      "v_mfma_f32_16x16x32_bf16 v[162:165], a[68:71], v[120:123], v[162:165]\n"
      "v_mul_f32_e64 v212, v84, v212\n"
      "v_cvt_pk_bf16_f32 v161, v126, v127\n"
      "v_mfma_f32_16x16x32_bf16 v[138:141], a[72:75], v[142:145], 0\n"
      "v_cvt_pk_bf16_f32 v196, v178, v179\n"
      "v_mul_f32 v184, v128, v184\n"
      "v_mfma_f32_16x16x32_bf16 v[138:141], a[76:79], v[146:149], v[138:141]\n"
      "v_cvt_pk_bf16_f32 v221, v222, v223\n"
      "v_fmac_f32 v218, v188, v188\n"
      "v_mfma_f32_16x16x32_bf16 v[138:141], a[80:83], v[150:153], v[138:141]\n"
      "v_mul_f32 v125, v76, v125\n"
      "v_fmac_f32 v218, v189, v189\n"
      "v_mfma_f32_16x16x32_bf16 v[138:141], a[84:87], v[154:157], v[138:141]\n"
      "v_mul_f32 v104, v81, v104\n"
      "v_mul_f32 v173, v81, v173\n"
      "v_mfma_f32_16x16x32_bf16 v[138:141], a[88:91], v[166:169], v[138:141]\n"
      "v_cvt_pk_bf16_f32 v222, v170, v171\n"
      "v_mul_f32 v183, v128, v183\n"
      "v_mfma_f32_16x16x32_bf16 v[138:141], a[92:95], v[120:123], v[138:141]\n"
      "v_mul_f32 v210, v84, v210\n"
      "v_cvt_pk_bf16_f32 v104, v104, v105\n"
      "v_mfma_f32_16x16x32_bf16 v[206:209], a[96:99], v[142:145], 0\n"
      "v_fmac_f32 v218, v162, v162\n"
      "v_mul_f32 v182, v128, v182\n"
      "v_mfma_f32_16x16x32_bf16 v[206:209], a[100:103], v[146:149], v[206:209]\n"
      "v_cvt_pk_bf16_f32 v210, v210, v211\n"
      "v_cvt_pk_bf16_f32 v223, v172, v173\n"
      "v_mfma_f32_16x16x32_bf16 v[206:209], a[104:107], v[150:153], v[206:209]\n"
      "v_mul_f32_e64 v213, v84, v213\n"
      "v_cvt_pk_bf16_f32 v197, v180, v181\n"
      "v_mfma_f32_16x16x32_bf16 v[206:209], a[108:111], v[154:157], v[206:209]\n"
      "v_cvt_pk_bf16_f32 v160, v124, v125\n"
      "v_fmac_f32_e64 v218, v163, v163\n"
      "v_mfma_f32_16x16x32_bf16 v[206:209], a[112:115], v[166:169], v[206:209]\n"
      "v_cvt_pk_bf16_f32 v105, v106, v107\n"
      "v_cvt_pk_bf16_f32 v182, v182, v183\n"
      "v_mfma_f32_16x16x32_bf16 v[206:209], a[116:119], v[120:123], v[206:209]\n"
      "v_fmac_f32 v218, v164, v164\n"
      "v_fma_f32 v125, v138, v138, s51\n"
      "v_mfma_f32_16x16x32_bf16 v[110:113], a[120:123], v[142:145], 0\n"
      "v_fmac_f32 v125, v139, v139\n"
      "v_fmac_f32_e64 v218, v165, v165\n"
      "v_mfma_f32_16x16x32_bf16 v[110:113], a[124:127], v[146:149], v[110:113]\n"
      "v_fmac_f32 v125, v140, v140\n"
      "v_mov_b32 v193, 0\n"
      "v_mfma_f32_16x16x32_bf16 v[110:113], a[128:131], v[150:153], v[110:113]\n"
      "v_mul_f32_e64 v137, v84, v137\n"
      "v_cvt_pk_bf16_f32 v211, v212, v213\n"
      "v_mfma_f32_16x16x32_bf16 v[110:113], a[132:135], v[154:157], v[110:113]\n"
      "v_fmac_f32 v125, v141, v141\n"
      "v_mul_f32 v100, v128, v100\n"
      "v_mfma_f32_16x16x32_bf16 v[110:113], a[136:139], v[166:169], v[110:113]\n"
      "v_fmac_f32 v125, v206, v206\n"
      "v_cvt_pk_bf16_f32 v183, v184, v185\n"
      "v_mfma_f32_16x16x32_bf16 v[110:113], a[140:143], v[120:123], v[110:113]\n"
      "v_fmac_f32 v125, v207, v207\n"
      "v_cvt_pk_bf16_f32 v213, v136, v137\n"
      "v_mfma_f32_16x16x32_bf16 v[114:117], v[142:145], a[144:147], 0\n"
      "s_add_u32 s42, s42, 1\n"
      "v_mul_f32 v101, v128, v101\n"
      "v_fmac_f32 v125, v208, v208\n"
      "v_mfma_f32_16x16x32_bf16 v[114:117], v[146:149], a[148:151], v[114:117]\n"
      "v_fmac_f32 v125, v209, v209\n"
      "v_fmac_f32 v125, v110, v110\n"
      "v_mfma_f32_16x16x32_bf16 v[114:117], v[150:153], a[152:155], v[114:117]\n"
      "v_fmac_f32 v125, v111, v111\n"
      "v_fmac_f32 v125, v112, v112\n"
      "v_fmac_f32 v125, v113, v113\n"
      "v_mfma_f32_16x16x32_bf16 v[114:117], v[154:157], a[156:159], v[114:117]\n"
      "s_nop 0\n"
      "v_permlane16_swap_b32 v218, v125\n"
      "v_mfma_f32_16x16x32_bf16 v[114:117], v[166:169], a[160:163], v[114:117]\n"
      "v_add_f32 v218, v218, v125\n"
      "v_mov_b32 v125, v218\n"
      "v_mfma_f32_16x16x32_bf16 v[114:117], v[120:123], a[164:167], v[114:117]\n"
      "s_nop 0\n"
      "v_permlane32_swap_b32 v218, v125\n"
      "v_mfma_f32_16x16x32_bf16 v[170:173], v[142:145], a[168:171], 0\n"
      "s_waitcnt vmcnt(7)\n"
      "v_add_f32 v218, v218, v125\n"
      "v_rsq_f32 v218, v218\n"
      "v_mfma_f32_16x16x32_bf16 v[170:173], v[146:149], a[172:175], v[170:173]\n"
      "v_mov_b32 v125, v218\n"
      "v_cvt_pk_bf16_f32 v100, v100, v101\n"
      "v_and_b32 v92, 2, v88\n"
      "v_permlane16_swap_b32 v218, v125\n"
      "v_pk_mul_f32 v[174:175], v[174:175], v[128:129] op_sel_hi:[1,0]\n"
      "v_cvt_pk_bf16_f32 v184, v174, v175\n"
      "v_mul_f32_e64 v218, s50, v218\n"
      "v_pk_mul_f32 v[176:177], v[176:177], v[128:129] op_sel_hi:[1,0]\n"
      "v_pk_mul_f32 v[102:103], v[102:103], v[128:129] op_sel_hi:[1,0]\n"
      "v_pk_mul_f32 v[206:207], v[206:207], v[124:125] op_sel:[0,1]\n"
      "v_cvt_pk_bf16_f32 v101, v102, v103\n"
      "v_pk_mul_f32 v[140:141], v[140:141], v[124:125] op_sel:[0,1]\n"
      "v_pk_mul_f32 v[134:135], v[134:135], v[84:85] op_sel_hi:[1,0]\n"
      "v_pk_mul_f32 v[208:209], v[208:209], v[124:125] op_sel:[0,1]\n"
      "v_pk_mul_f32 v[138:139], v[138:139], v[124:125] op_sel:[0,1]\n"
      "v_pk_mul_f32 v[198:199], v[198:199], v[84:85] op_sel_hi:[1,0]\n"
      "v_pk_mul_f32 v[204:205], v[204:205], v[218:219] op_sel_hi:[1,0]\n"
      "v_pk_mul_f32 v[202:203], v[202:203], v[218:219] op_sel_hi:[1,0]\n"
      "v_pk_mul_f32 v[110:111], v[110:111], v[124:125] op_sel:[0,1]\n"
      "v_pk_mul_f32 v[200:201], v[200:201], v[84:85] op_sel_hi:[1,0]\n"
      "v_pk_mul_f32 v[186:187], v[186:187], v[218:219] op_sel_hi:[1,0]\n"
      "v_cvt_pk_bf16_f32 v212, v134, v135\n"
      "v_pk_mul_f32 v[112:113], v[112:113], v[124:125] op_sel:[0,1]\n"
      "v_mfma_f32_16x16x32_bf16 v[170:173], v[150:153], a[176:179], v[170:173]\n"
      "ds_read_b128 v[134:137], v7 offset:18432\n"
      "v_cvt_pk_bf16_f32 v185, v176, v177\n"
      "v_mfma_f32_16x16x32_bf16 v[170:173], v[154:157], a[180:183], v[170:173]\n"
      "v_cvt_pk_bf16_f32 v198, v198, v199\n"
      "v_and_b32_e64 v88, -4, v88\n"
      "v_mfma_f32_16x16x32_bf16 v[170:173], v[166:169], a[184:187], v[170:173]\n"
      "ds_read_b128 v[124:127], v2 offset:18432\n"
      "v_cvt_pk_bf16_f32 v199, v200, v201\n"
      "v_mfma_f32_16x16x32_bf16 v[170:173], v[120:123], a[188:191], v[170:173]\n"
      "ds_read_b128 v[106:109], v2 offset:24576\n"
      "v_lshlrev_b32_e64 v78, 4, v78\n"
      "v_mfma_f32_16x16x32_bf16 v[178:181], v[142:145], a[192:195], 0\n"
      "ds_read_b128 v[174:177], v2 offset:21504\n"
      "v_cvt_pk_bf16_f32 v138, v138, v139\n"
      "v_mfma_f32_16x16x32_bf16 v[178:181], v[146:149], a[196:199], v[178:181]\n"
      "ds_read_b128 v[144:147], v7 offset:22528\n"
      "v_cvt_pk_bf16_f32 v77, v116, v117\n"
      "v_mfma_f32_16x16x32_bf16 v[178:181], v[150:153], a[200:203], v[178:181]\n"
      "ds_read_b128 v[148:151], v2 offset:27648\n"
      "v_cvt_pk_bf16_f32 v139, v140, v141\n"
      "v_mfma_f32_16x16x32_bf16 v[178:181], v[154:157], a[204:207], v[178:181]\n"
      "ds_read_b128 v[154:157], v2 offset:30720\n"
      "v_cvt_pk_bf16_f32 v81, v172, v173\n"
      "v_mfma_f32_16x16x32_bf16 v[178:181], v[166:169], a[208:211], v[178:181]\n"
      "s_waitcnt lgkmcnt(5)\n"
      "v_mov_b32 v192, 0x3fb9\n"
      "v_cvt_pk_bf16_f32 v140, v206, v207\n"
      "v_mfma_f32_16x16x32_bf16 v[178:181], v[120:123], a[212:215], v[178:181]\n"
      "buffer_store_dwordx4 v[134:137], v20, s[24:27], s46 offen sc1\n"
      "v_pk_mul_f32 v[188:189], v[188:189], v[218:219] op_sel_hi:[1,0]\n"
      "v_mfma_f32_16x16x32_bf16 v[166:169], a[216:219], v[124:127], 0\n"
      "s_waitcnt lgkmcnt(2)\n"
      "v_cvt_pk_bf16_f32 v76, v114, v115\n"
      "v_add_u32_e64 v12, 32, v12\n"
      "v_mfma_f32_16x16x32_bf16 v[166:169], a[220:223], v[174:177], v[166:169]\n"
      "s_add_u32 s43, s43, 1\n"
      "v_cvt_pk_bf16_f32 v110, v110, v111\n"
      "v_add_u32 v173, 110592, v88\n"
      "v_mfma_f32_16x16x32_bf16 v[166:169], a[224:227], v[106:109], v[166:169]\n"
      "ds_read2_b32 v[120:121], v173 offset0:2 offset1:10\n"
      "v_cvt_pk_bf16_f32 v111, v112, v113\n"
      "v_mfma_f32_16x16x32_bf16 v[112:115], a[240:243], v[124:127], 0\n"
      "s_waitcnt lgkmcnt(1)\n"
      "v_cvt_pk_bf16_f32 v202, v202, v203\n"
      "v_cvt_pk_bf16_f32 v203, v204, v205\n"
      "v_mfma_f32_16x16x32_bf16 v[112:115], a[244:247], v[174:177], v[112:115]\n"
      "ds_read_b128 v[134:137], v2 offset:33792\n"
      "v_cvt_pk_bf16_f32 v84, v178, v179\n"
      "v_mfma_f32_16x16x32_bf16 v[112:115], a[248:251], v[106:109], v[112:115]\n"
      "ds_read2_b32 v[152:153], v173 offset0:8 offset1:9\n"
      "v_lshlrev_b64 v[192:193], v78, v[192:193]\n"
      "v_mfma_f32_16x16x32_bf16 v[116:119], v[232:235], v[124:127], 0\n"
      "ds_read_b128 v[126:129], v7 offset:26624\n"
      "v_cmp_gt_u32 s[72:73], 64, v78\n"
      "v_mfma_f32_16x16x32_bf16 v[116:119], v[236:239], v[174:177], v[116:119]\n"
      "ds_read2_b32 v[142:143], v173 offset0:16 offset1:17\n"
      "v_cvt_pk_bf16_f32 v204, v186, v187\n"
      "v_mfma_f32_16x16x32_bf16 v[116:119], v[240:243], v[106:109], v[116:119]\n"
      "buffer_store_dwordx4 v[144:147], v21, s[24:27], s46 offen sc1\n"
      "v_mfma_f32_16x16x32_bf16 v[166:169], a[228:231], v[148:151], v[166:169]\n"
      "s_waitcnt lgkmcnt(3)\n"
      "v_cndmask_b32_e64 v193, 0, v193, s[72:73]\n"
      "v_cvt_pk_bf16_f32 v80, v170, v171\n"
      "v_mfma_f32_16x16x32_bf16 v[166:169], a[232:235], v[154:157], v[166:169]\n"
      "ds_read_b128 v[174:177], v7 offset:34816\n"
      "v_cvt_pk_bf16_f32 v141, v208, v209\n"
      "v_mfma_f32_16x16x32_bf16 v[166:169], a[236:239], v[134:137], v[166:169]\n"
      "s_waitcnt lgkmcnt(2)\n"
      "v_mov_b32 v200, 0xbb2b\n"
      "v_alignbyte_b32 v152, v153, v152, v92\n"
      "v_mfma_f32_16x16x32_bf16 v[112:115], a[252:255], v[148:151], v[112:115]\n"
      "ds_read2_b32 v[186:187], v173 offset0:0 offset1:1\n"
      "v_mov_b32_e64 v201, 0\n"
      "v_mfma_f32_16x16x32_bf16 v[112:115], v[224:227], v[154:157], v[112:115]\n"
      "ds_read2_b32 v[106:107], v173 offset0:24 offset1:18\n"
      "v_cvt_pk_bf16_f32 v85, v180, v181\n"
      "v_mfma_f32_16x16x32_bf16 v[112:115], v[228:231], v[134:137], v[112:115]\n"
      "ds_read_b128 v[170:173], v10 offset:32\n"
      "v_mul_f32_e64 v125, v167, v167\n"
      "v_mfma_f32_16x16x32_bf16 v[116:119], v[244:247], v[148:151], v[116:119]\n"
      "ds_read_b128 v[206:209], v7 offset:30720\n"
      "v_fma_f32 v124, v166, v166, s52\n"
      "v_mfma_f32_16x16x32_bf16 v[116:119], v[248:251], v[154:157], v[116:119]\n"
      "buffer_store_dwordx4 v[126:129], v22, s[24:27], s46 offen sc1\n"
      "v_pk_mul_f32 v[164:165], v[164:165], v[218:219] op_sel_hi:[1,0]\n"
      "v_pk_fma_f32 v[124:125], v[168:169], v[168:169], v[124:125]\n"
      "v_pk_mul_f32 v[162:163], v[162:163], v[218:219] op_sel_hi:[1,0]\n"
      "v_pk_fma_f32 v[124:125], v[112:113], v[112:113], v[124:125]\n"
      "v_mfma_f32_16x16x32_bf16 v[116:119], v[252:255], v[134:137], v[116:119]\n"
      "s_waitcnt lgkmcnt(2)\n"
      "v_alignbyte_b32 v186, v187, v186, v92\n"
      "v_lshlrev_b64 v[200:201], v78, v[200:201]\n"
      "v_mfma_f32_16x16x32_bf16 v[144:147], v[158:161], v[220:223], v[40:43]\n"
      "ds_read_b64 v[102:103], v10 offset:24\n"
      "v_cvt_pk_bf16_f32 v162, v162, v163\n"
      "v_mfma_f32_16x16x32_bf16 v[148:151], v[182:185], v[220:223], v[44:47]\n"
      "v_cndmask_b32_e64 v192, 0, v192, s[72:73]\n"
      "v_alignbyte_b32 v142, v143, v142, v92\n"
      "v_mfma_f32_16x16x32_bf16 v[154:157], v[138:141], v[220:223], v[48:51]\n"
      "s_waitcnt lgkmcnt(1)\n"
      "v_cvt_pk_bf16_f32 v205, v188, v189\n"
      "v_cvt_pk_bf16_f32 v163, v164, v165\n"
      "v_mfma_f32_16x16x32_bf16 v[134:137], v[170:173], v[220:223], v[24:27]\n"
      "ds_read_b128 v[218:221], v2 offset:18688\n"
      "v_cmp_gt_u32 s[72:73], 64, v78\n"
      "v_mfma_f32_16x16x16_bf16 v[144:147], v[190:191], v[104:105], v[144:147]\n"
      "v_alignbyte_b32 v187, v120, v187, v92\n"
      "v_fmac_f32 v125, v115, v115\n"
      "v_fmac_f32 v124, v114, v114\n"
      "v_mfma_f32_16x16x16_bf16 v[148:151], v[100:101], v[104:105], v[148:151]\n"
      "buffer_store_dwordx4 v[206:209], v23, s[24:27], s46 offen sc1\n"
      "v_mfma_f32_16x16x16_bf16 v[154:157], v[110:111], v[104:105], v[154:157]\n"
      "v_alignbyte_b32 v153, v121, v153, v92\n"
      "v_alignbyte_b32 v143, v107, v143, v92\n"
      "s_waitcnt lgkmcnt(1)\n"
      "v_mfma_f32_16x16x16_bf16 v[134:137], v[102:103], v[104:105], v[134:137]\n"
      "ds_read_b128 v[126:129], v2 offset:21760\n"
      "v_add_u32 v39, s77, v39\n"
      "v_mfma_f32_16x16x16_bf16 v[144:147], v[192:193], v[186:187], v[144:147]\n"
      "v_alignbyte_b32 v106, v106, v106, v92\n"
      "v_cndmask_b32_e64 v201, 0, v201, s[72:73]\n"
      "v_add_u32 v206, 112160, v88\n"
      "v_mfma_f32_16x16x16_bf16 v[148:151], v[192:193], v[152:153], v[148:151]\n"
      "v_add_u32 v39, 115296, v39\n"
      "v_fmac_f32_e64 v125, v117, v117\n"
      "v_cndmask_b32_e64 v200, 0, v200, s[72:73]\n"
      "v_mfma_f32_16x16x16_bf16 v[154:157], v[192:193], v[142:143], v[154:157]\n"
      "ds_read2_b32 v[108:109], v206 offset0:0 offset1:1\n"
      "v_fmac_f32 v124, v116, v116\n"
      "v_mfma_f32_16x16x16_bf16 v[134:137], v[192:193], v[106:107], v[134:137]\n"
      "ds_read_b128 v[120:123], v2 offset:24832\n"
      "v_fmac_f32 v124, v118, v118\n"
      "v_mfma_f32_16x16x16_bf16 v[144:147], v[200:201], v[186:187], v[144:147]\n"
      "ds_read2_b32 v[164:165], v206 offset0:16 offset1:17\n"
      "v_fmac_f32_e64 v125, v119, v119\n"
      "v_mfma_f32_16x16x16_bf16 v[148:151], v[200:201], v[152:153], v[148:151]\n"
      "buffer_store_dwordx4 v[174:177], v25, s[24:27], s46 offen sc1\n"
      "v_mfma_f32_16x16x16_bf16 v[154:157], v[200:201], v[142:143], v[154:157]\n"
      "ds_read2_b32 v[152:153], v206 offset0:2 offset1:10\n"
      "v_add_u32 v142, 113728, v88\n"
      "v_mfma_f32_16x16x16_bf16 v[134:137], v[200:201], v[106:107], v[134:137]\n"
      "s_waitcnt lgkmcnt(3)\n"
      "v_alignbyte_b32 v108, v109, v108, v92\n"
      "v_max3_f32 v78, v144, v145, v146\n"
      "v_mfma_f32_16x16x32_bf16 v[186:189], a[216:219], v[218:221], 0\n"
      "ds_read2_b32 v[106:107], v206 offset0:8 offset1:9\n"
      "v_add_f32 v124, v124, v125\n"
      "v_mfma_f32_16x16x32_bf16 v[186:189], a[220:223], v[126:129], v[186:189]\n"
      "s_waitcnt lgkmcnt(2)\n"
      "v_alignbyte_b32 v164, v165, v164, v92\n"
      "v_max3_f32 v78, v78, v150, v151\n"
      "v_mfma_f32_16x16x32_bf16 v[186:189], a[224:227], v[120:123], v[186:189]\n"
      "v_max3_f32 v207, v147, v148, v149\n"
      "v_max3_f32 v78, v78, v156, v157\n"
      "v_mfma_f32_16x16x32_bf16 v[174:177], a[240:243], v[218:221], 0\n"
      "ds_read_u16 v39, v39\n"
      "s_waitcnt vmcnt(6)\n"
      "v_or3_b32 v96, v96, v97, v98\n"
      "v_mfma_f32_16x16x32_bf16 v[174:177], a[244:247], v[126:129], v[174:177]\n"
      "ds_read2_b32 v[136:137], v206 offset0:24 offset1:18\n"
      "v_max3_f32 v207, v207, v154, v155\n"
      "v_mfma_f32_16x16x32_bf16 v[174:177], a[248:251], v[120:123], v[174:177]\n"
      "s_waitcnt lgkmcnt(3)\n"
      "v_max3_f32 v78, v78, v207, v134\n"
      "v_alignbyte_b32 v109, v152, v109, v92\n"
      "v_mfma_f32_16x16x32_bf16 v[206:209], v[232:235], v[218:221], 0\n"
      "s_waitcnt lgkmcnt(2)\n"
      "v_alignbyte_b32 v106, v107, v106, v92\n"
      "v_mov_b32 v97, v78\n"
      "v_mfma_f32_16x16x32_bf16 v[206:209], v[236:239], v[126:129], v[206:209]\n"
      "s_nop 0\n"
      "v_permlane16_swap_b32_e64 v78, v97\n"
      "v_mfma_f32_16x16x32_bf16 v[206:209], v[240:243], v[120:123], v[206:209]\n"
      "v_max_f32 v78, v78, v97\n"
      "v_alignbyte_b32 v107, v153, v107, v92\n"
      "v_mfma_f32_16x16x32_bf16 v[218:221], v[158:161], v[210:213], v[52:55]\n"
      "v_mov_b32 v97, v78\n"
      "s_waitcnt vmcnt(5)\n"
      "v_or3_b32 v96, v96, v99, v86\n"
      "v_mfma_f32_16x16x32_bf16 v[126:129], v[182:185], v[210:213], v[56:59]\n"
      "v_permlane32_swap_b32 v78, v97\n"
      "v_max_f32_e64 v78, v78, v97\n"
      "v_mfma_f32_16x16x32_bf16 v[120:123], v[138:141], v[210:213], v[60:63]\n"
      "ds_write_b32 v31, v96 offset:4608\n"
      "v_sub_f32 v148, v148, v78\n"
      "v_mfma_f32_16x16x32_bf16 v[96:99], v[170:173], v[210:213], v[26:29]\n"
      "s_waitcnt lgkmcnt(1)\n"
      "v_exp_f32 v148, v148\n"
      "v_mfma_f32_16x16x16_bf16 v[218:221], v[190:191], v[198:199], v[218:221]\n"
      "v_sub_f32 v134, v134, v78\n"
      "v_sub_f32 v144, v144, v78\n"
      "v_sub_f32 v151, v151, v78\n"
      "v_mfma_f32_16x16x16_bf16 v[126:129], v[100:101], v[198:199], v[126:129]\n"
      "v_sub_f32 v149, v149, v78\n"
      "v_alignbyte_b32 v165, v137, v165, v92\n"
      "v_sub_f32 v154, v154, v78\n"
      "v_mfma_f32_16x16x16_bf16 v[120:123], v[110:111], v[198:199], v[120:123]\n"
      "v_exp_f32 v134, v134\n"
      "v_sub_f32 v147, v147, v78\n"
      "v_mfma_f32_16x16x16_bf16 v[96:99], v[102:103], v[198:199], v[96:99]\n"
      "v_exp_f32 v154, v154\n"
      "v_sub_f32 v155, v155, v78\n"
      "v_mfma_f32_16x16x16_bf16 v[218:221], v[192:193], v[108:109], v[218:221]\n"
      "v_exp_f32_e64 v144, v144\n"
      "v_alignbyte_b32 v136, v136, v136, v92\n"
      "v_mfma_f32_16x16x16_bf16 v[126:129], v[192:193], v[106:107], v[126:129]\n"
      "v_sub_f32 v156, v156, v78\n"
      "v_exp_f32 v155, v155\n"
      "v_mfma_f32_16x16x16_bf16 v[120:123], v[192:193], v[164:165], v[120:123]\n"
      "v_sub_f32 v145, v145, v78\n"
      "v_exp_f32 v147, v147\n"
      "v_mfma_f32_16x16x16_bf16 v[96:99], v[192:193], v[136:137], v[96:99]\n"
      "v_cvt_pk_bf16_f32 v88, v154, v155\n"
      "v_exp_f32_e64 v149, v149\n"
      "v_mfma_f32_16x16x16_bf16 v[218:221], v[200:201], v[108:109], v[218:221]\n"
      "v_exp_f32 v145, v145\n"
      "v_sub_f32 v150, v150, v78\n"
      "v_cvt_pk_bf16_f32 v90, v134, 0\n"
      "v_mfma_f32_16x16x16_bf16 v[126:129], v[200:201], v[106:107], v[126:129]\n"
      "ds_read_b128 v[210:213], v2 offset:27904\n"
      "ds_read_b128 v[152:155], v2 offset:30976\n"
      "v_mfma_f32_16x16x16_bf16 v[120:123], v[200:201], v[164:165], v[120:123]\n"
      "v_sub_f32 v157, v157, v78\n"
      "v_sub_f32 v146, v146, v78\n"
      "v_cvt_pk_bf16_f32 v144, v144, v145\n"
      "v_exp_f32_e64 v146, v146\n"
      "v_mfma_f32_16x16x16_bf16 v[96:99], v[200:201], v[136:137], v[96:99]\n"
      "ds_read_b128 v[134:137], v2 offset:34048\n"
      "v_cvt_pk_bf16_f32 v145, v146, v147\n"
      "v_exp_f32 v156, v156\n"
      "v_cvt_pk_bf16_f32 v146, v148, v149\n"
      "v_exp_f32 v151, v151\n"
      "v_exp_f32_e64 v157, v157\n"
      "v_max3_f32 v78, v221, v126, v127\n"
      "v_max3_f32 v78, v78, v120, v121\n"
      "v_max3_f32 v143, v218, v219, v220\n"
      "v_cvt_pk_bf16_f32 v89, v156, v157\n"
      "v_exp_f32 v150, v150\n"
      "v_max3_f32 v143, v143, v128, v129\n"
      "v_max3_f32 v143, v143, v122, v123\n"
      "s_waitcnt lgkmcnt(1)\n"
      "v_mfma_f32_16x16x32_bf16 v[186:189], a[228:231], v[210:213], v[186:189]\n"
      "v_max3_f32 v143, v143, v78, v96\n"
      "v_mov_b32 v164, v143\n"
      "v_cvt_pk_bf16_f32 v147, v150, v151\n"
      "v_mfma_f32_16x16x32_bf16 v[186:189], a[232:235], v[152:155], v[186:189]\n"
      "s_waitcnt lgkmcnt(0)\n"
      "v_permlane16_swap_b32 v143, v164\n"
      "v_mfma_f32_16x16x32_bf16 v[186:189], a[236:239], v[134:137], v[186:189]\n"
      "ds_read_u16 v78, v11\n"
      "v_max_f32 v143, v143, v164\n"
      "v_mfma_f32_16x16x32_bf16 v[174:177], a[252:255], v[210:213], v[174:177]\n"
      "ds_read_u16 v86, v11 offset:64\n"
      "v_mov_b32 v164, v143\n"
      "v_mfma_f32_16x16x32_bf16 v[174:177], v[224:227], v[152:155], v[174:177]\n"
      "ds_read2_b32 v[98:99], v142 offset0:0 offset1:1\n"
      "v_permlane32_swap_b32 v143, v164\n"
      "v_mfma_f32_16x16x32_bf16 v[174:177], v[228:231], v[134:137], v[174:177]\n"
      "ds_read2_b32 v[156:157], v142 offset0:2 offset1:10\n"
      "v_max_f32 v143, v143, v164\n"
      "v_sub_f32 v96, v96, v143\n"
      "v_mfma_f32_16x16x32_bf16 v[206:209], v[244:247], v[210:213], v[206:209]\n"
      "ds_read2_b32 v[198:199], v142 offset0:24 offset1:18\n"
      "v_sub_f32 v219, v219, v143\n"
      "v_mfma_f32_16x16x32_bf16 v[206:209], v[248:251], v[152:155], v[206:209]\n"
      "ds_read2_b32 v[164:165], v142 offset0:16 offset1:17\n"
      "v_sub_f32 v220, v220, v143\n"
      "v_mfma_f32_16x16x32_bf16 v[206:209], v[252:255], v[134:137], v[206:209]\n"
      "ds_read2_b32 v[104:105], v142 offset0:8 offset1:9\n"
      "v_sub_f32 v129, v129, v143\n"
      "v_sub_f32 v126, v126, v143\n"
      "v_sub_f32 v221, v221, v143\n"
      "v_sub_f32 v218, v218, v143\n"
      "v_mfma_f32_16x16x32_bf16 v[148:151], v[32:35], v[144:147], 0\n"
      "ds_read_u16 v82, v11 offset:32\n"
      "v_exp_f32 v218, v218\n"
      "v_sub_f32 v122, v122, v143\n"
      "v_mfma_f32_16x16x32_bf16 v[148:151], v[32:35], v[88:91], v[148:151]\n"
      "s_and_b32 s86, s86, -4\n"
      "v_exp_f32 v129, v129\n"
      "v_exp_f32 v221, v221\n"
      "v_sub_f32 v128, v128, v143\n"
      "v_exp_f32_e64 v128, v128\n"
      "v_exp_f32_e64 v219, v219\n"
      "v_exp_f32 v220, v220\n"
      "v_mfma_f32_16x16x32_bf16 v[210:213], v[130:133], v[144:147], 0\n"
      "s_waitcnt lgkmcnt(1)\n"
      "v_alignbyte_b32 v104, v105, v104, v92\n"
      "v_alignbyte_b32 v105, v157, v105, v92\n"
      "v_alignbyte_b32 v164, v165, v164, v92\n"
      "v_cvt_pk_bf16_f32 v218, v218, v219\n"
      "v_mfma_f32_16x16x32_bf16 v[210:213], v[76:79], v[88:91], v[210:213]\n"
      "v_alignbyte_b32 v98, v99, v98, v92\n"
      "v_sub_f32 v120, v120, v143\n"
      "v_mfma_f32_16x16x32_bf16 v[150:153], v[214:217], v[144:147], 0\n"
      "s_waitcnt lgkmcnt(0)\n"
      "v_alignbyte_b32 v165, v199, v165, v92\n"
      "v_alignbyte_b32 v99, v156, v99, v92\n"
      "v_mfma_f32_16x16x32_bf16 v[150:153], v[80:83], v[88:91], v[150:153]\n"
      "v_cvt_pk_bf16_f32 v219, v220, v221\n"
      "v_cvt_pk_bf16_f32 v221, v128, v129\n"
      "v_mfma_f32_16x16x32_bf16 v[154:157], v[194:197], v[144:147], 0\n"
      "v_exp_f32 v126, v126\n"
      "v_mfma_f32_16x16x32_bf16 v[154:157], v[84:87], v[88:91], v[154:157]\n"
      "v_alignbyte_b32 v198, v198, v198, v92\n"
      "v_sub_f32 v127, v127, v143\n"
      "v_mfma_f32_16x16x32_bf16 v[134:137], v[158:161], v[202:205], v[64:67]\n"
      "v_exp_f32 v127, v127\n"
      "v_mfma_f32_16x16x32_bf16 v[106:109], v[182:185], v[202:205], v[68:71]\n"
      "v_sub_f32 v121, v121, v143\n"
      "v_sub_f32_e64 v123, v123, v143\n"
      "v_cvt_pk_bf16_f32 v220, v126, v127\n"
      "v_mfma_f32_16x16x32_bf16 v[126:129], v[138:141], v[202:205], v[72:75]\n"
      "v_exp_f32 v120, v120\n"
      "v_mfma_f32_16x16x32_bf16 v[142:145], v[170:173], v[202:205], v[28:31]\n"
      "v_rcp_f32 v148, v148\n"
      "v_mfma_f32_16x16x16_bf16 v[134:137], v[190:191], v[162:163], v[134:137]\n"
      "v_mul_f32 v154, v148, v154\n"
      "v_exp_f32 v121, v121\n"
      "v_mfma_f32_16x16x16_bf16 v[106:109], v[100:101], v[162:163], v[106:109]\n"
      "v_cvt_pk_bf16_f32 v92, v120, v121\n"
      "v_exp_f32_e64 v122, v122\n"
      "v_mfma_f32_16x16x16_bf16 v[126:129], v[110:111], v[162:163], v[126:129]\n"
      "v_fma_f32 v178, v186, v186, s52\n"
      "v_exp_f32 v96, v96\n"
      "v_mfma_f32_16x16x16_bf16 v[142:145], v[102:103], v[162:163], v[142:145]\n"
      "v_mul_f32 v179, v187, v187\n"
      "v_fmac_f32 v178, v188, v188\n"
      "v_fmac_f32 v179, v189, v189\n"
      "v_mfma_f32_16x16x16_bf16 v[134:137], v[192:193], v[98:99], v[134:137]\n"
      "v_exp_f32 v123, v123\n"
      "v_fmac_f32 v179, v175, v175\n"
      "v_mfma_f32_16x16x16_bf16 v[106:109], v[192:193], v[104:105], v[106:109]\n"
      "v_fmac_f32 v178, v174, v174\n"
      "v_fmac_f32 v178, v176, v176\n"
      "v_mul_f32 v155, v148, v155\n"
      "v_mfma_f32_16x16x16_bf16 v[126:129], v[192:193], v[164:165], v[126:129]\n"
      "v_mul_f32 v210, v148, v210\n"
      "v_cvt_pk_bf16_f32 v94, v96, 0\n"
      "v_fmac_f32 v179, v177, v177\n"
      "v_mfma_f32_16x16x16_bf16 v[142:145], v[192:193], v[198:199], v[142:145]\n"
      "v_cvt_pk_bf16_f32 v93, v122, v123\n"
      "v_mul_f32 v211, v148, v211\n"
      "v_cvt_pk_bf16_f32 v154, v154, v155\n"
      "v_mfma_f32_16x16x16_bf16 v[134:137], v[200:201], v[98:99], v[134:137]\n"
      "ds_read_b128 v[96:99], v2 offset:22016\n"
      "v_mul_f32_e64 v212, v148, v212\n"
      "v_mfma_f32_16x16x16_bf16 v[106:109], v[200:201], v[104:105], v[106:109]\n"
      "ds_read_b128 v[120:123], v2 offset:18944\n"
      "v_mul_f32_e64 v213, v148, v213\n"
      "v_mfma_f32_16x16x16_bf16 v[126:129], v[200:201], v[164:165], v[126:129]\n"
      "v_cvt_pk_bf16_f32 v210, v210, v211\n"
      "v_cvt_pk_bf16_f32 v211, v212, v213\n"
      "v_max3_f32 v165, v134, v135, v136\n"
      "v_mfma_f32_16x16x16_bf16 v[142:145], v[200:201], v[198:199], v[142:145]\n"
      "ds_read_b128 v[198:201], v2 offset:25088\n"
      "v_lshlrev_b32_e64 v39, 16, v39\n"
      "v_max3_f32 v204, v137, v106, v107\n"
      "v_max3_f32 v165, v165, v108, v109\n"
      "v_max3_f32 v165, v165, v128, v129\n"
      "v_max3_f32 v204, v204, v126, v127\n"
      "v_pk_mul_f32 v[150:151], v[150:151], v[148:149] op_sel_hi:[1,0]\n"
      "v_pk_fma_f32 v[178:179], v[206:207], v[206:207], v[178:179]\n"
      "v_max3_f32 v165, v165, v204, v142\n"
      "v_pk_mul_f32 v[156:157], v[156:157], v[148:149] op_sel_hi:[1,0]\n"
      "v_cvt_pk_bf16_f32 v155, v156, v157\n"
      "v_pk_fma_f32 v[178:179], v[208:209], v[208:209], v[178:179]\n"
      "v_pk_mul_f32 v[152:153], v[152:153], v[148:149] op_sel_hi:[1,0]\n"
      "v_cvt_pk_bf16_f32 v212, v150, v151\n"
      "v_mov_b32 v150, v165\n"
      "v_add_f32 v178, v178, v179\n"
      "s_waitcnt lgkmcnt(1)\n"
      "v_mfma_f32_16x16x32_bf16 v[202:205], a[216:219], v[120:123], 0\n"
      "v_permlane16_swap_b32 v165, v150\n"
      "v_max_f32 v165, v165, v150\n"
      "v_mfma_f32_16x16x32_bf16 v[202:205], a[220:223], v[96:99], v[202:205]\n"
      "s_waitcnt lgkmcnt(0)\n"
      "v_mov_b32_e64 v150, v165\n"
      "v_cvt_pk_bf16_f32 v213, v152, v153\n"
      "v_mfma_f32_16x16x32_bf16 v[202:205], a[224:227], v[198:201], v[202:205]\n"
      "ds_write_b128 v3, v[210:213]\n"
      "v_mfma_f32_16x16x32_bf16 v[144:147], a[240:243], v[120:123], 0\n"
      "v_permlane32_swap_b32 v165, v150\n"
      "v_max_f32 v165, v165, v150\n"
      "v_mfma_f32_16x16x32_bf16 v[144:147], a[244:247], v[96:99], v[144:147]\n"
      "v_sub_f32 v135, v135, v165\n"
      "v_sub_f32 v108, v108, v165\n"
      "v_mfma_f32_16x16x32_bf16 v[144:147], a[248:251], v[198:201], v[144:147]\n"
      "v_sub_f32 v109, v109, v165\n"
      "v_sub_f32 v128, v128, v165\n"
      "v_mfma_f32_16x16x32_bf16 v[148:151], v[232:235], v[120:123], 0\n"
      "v_sub_f32 v129, v129, v165\n"
      "v_sub_f32 v137, v137, v165\n"
      "v_mfma_f32_16x16x32_bf16 v[148:151], v[236:239], v[96:99], v[148:151]\n"
      "v_sub_f32 v136, v136, v165\n"
      "v_sub_f32 v134, v134, v165\n"
      "v_sub_f32 v106, v106, v165\n"
      "v_sub_f32 v107, v107, v165\n"
      "v_mfma_f32_16x16x32_bf16 v[148:151], v[240:243], v[198:201], v[148:151]\n"
      "v_exp_f32 v135, v135\n"
      "v_sub_f32 v127, v127, v165\n"
      "v_mfma_f32_16x16x32_bf16 v[210:213], v[32:35], v[218:221], 0\n"
      "v_sub_f32 v142, v142, v165\n"
      "v_exp_f32 v134, v134\n"
      "v_mfma_f32_16x16x32_bf16 v[210:213], v[32:35], v[92:95], v[210:213]\n"
      "ds_read_b64 v[88:89], v10 offset:16\n"
      "v_sub_f32_e64 v126, v126, v165\n"
      "v_mfma_f32_16x16x32_bf16 v[198:201], v[130:133], v[218:221], 0\n"
      "ds_read_b128 v[120:123], v10\n"
      "v_exp_f32 v136, v136\n"
      "v_mfma_f32_16x16x32_bf16 v[198:201], v[76:79], v[92:95], v[198:201]\n"
      "v_exp_f32 v137, v137\n"
      "v_exp_f32 v107, v107\n"
      "v_cvt_pk_bf16_f32 v134, v134, v135\n"
      "v_mfma_f32_16x16x32_bf16 v[162:165], v[214:217], v[218:221], 0\n"
      "v_exp_f32 v106, v106\n"
      "v_mfma_f32_16x16x32_bf16 v[162:165], v[80:83], v[92:95], v[162:165]\n"
      "v_cvt_pk_bf16_f32 v135, v136, v137\n"
      "v_cvt_pk_bf16_f32 v136, v106, v107\n"
      "v_mfma_f32_16x16x32_bf16 v[96:99], v[194:197], v[218:221], 0\n"
      "v_rcp_f32 v210, v210\n"
      "v_mfma_f32_16x16x32_bf16 v[96:99], v[84:87], v[92:95], v[96:99]\n"
      "s_waitcnt lgkmcnt(0)\n"
      "v_exp_f32 v109, v109\n"
      "v_mfma_f32_16x16x32_bf16 v[218:221], v[120:123], v[158:161], v[30:33]\n"
      "v_exp_f32 v108, v108\n"
      "v_mfma_f32_16x16x32_bf16 v[156:159], v[120:123], v[182:185], v[30:33]\n"
      "v_exp_f32_e64 v127, v127\n"
      "v_mfma_f32_16x16x32_bf16 v[180:183], v[120:123], v[138:141], v[30:33]\n"
      "ds_read_b128 v[104:107], v2 offset:28160\n"
      "v_cvt_pk_bf16_f32 v137, v108, v109\n"
      "v_mfma_f32_16x16x32_bf16 v[138:141], v[120:123], v[170:173], v[30:33]\n"
      "ds_read_b128 v[120:123], v2 offset:31232\n"
      "ds_read_b128 v[170:173], v2 offset:34304\n"
      "v_mfma_f32_16x16x16_bf16 v[218:221], v[88:89], v[190:191], v[218:221]\n"
      "v_exp_f32 v142, v142\n"
      "v_exp_f32 v129, v129\n"
      "v_permlane16_swap_b32_e64 v124, v178\n"
      "v_pk_mul_f32 v[164:165], v[164:165], v[210:211] op_sel_hi:[1,0]\n"
      "v_pk_mul_f32 v[98:99], v[98:99], v[210:211] op_sel_hi:[1,0]\n"
      "v_pk_mul_f32 v[96:97], v[96:97], v[210:211] op_sel_hi:[1,0]\n"
      "v_pk_mul_f32 v[198:199], v[198:199], v[210:211] op_sel_hi:[1,0]\n"
      "v_pk_mul_f32 v[200:201], v[200:201], v[210:211] op_sel_hi:[1,0]\n"
      "v_pk_mul_f32 v[162:163], v[162:163], v[210:211] op_sel_hi:[1,0]\n"
      "v_mfma_f32_16x16x16_bf16 v[156:159], v[88:89], v[100:101], v[156:159]\n"
      "v_exp_f32_e64 v128, v128\n"
      "v_cvt_pk_bf16_f32 v198, v198, v199\n"
      "v_mfma_f32_16x16x16_bf16 v[180:183], v[88:89], v[110:111], v[180:183]\n"
      "v_cvt_pk_bf16_f32 v199, v200, v201\n"
      "v_exp_f32 v126, v126\n"
      "v_mfma_f32_16x16x16_bf16 v[138:141], v[88:89], v[102:103], v[138:141]\n"
      "v_cvt_pk_bf16_f32 v90, v142, 0\n"
      "v_cvt_pk_bf16_f32 v88, v126, v127\n"
      "v_cndmask_b32 v218, v218, v156, s[58:59]\n"
      "s_waitcnt lgkmcnt(0)\n"
      "v_mfma_f32_16x16x32_bf16 v[202:205], a[228:231], v[104:107], v[202:205]\n"
      "v_cndmask_b32 v218, v218, v180, s[60:61]\n"
      "v_cvt_pk_bf16_f32 v89, v128, v129\n"
      "v_mfma_f32_16x16x32_bf16 v[202:205], a[232:235], v[120:123], v[202:205]\n"
      "v_cndmask_b32 v218, v218, v138, s[62:63]\n"
      "v_fmac_f32_e64 v218, s53, v39\n"
      "v_mfma_f32_16x16x32_bf16 v[202:205], a[236:239], v[170:173], v[202:205]\n"
      "v_cvt_pk_bf16_f32 v200, v162, v163\n"
      "v_max_f32_dpp v221, v218, v218 row_ror:8 row_mask:0xf bank_mask:0xf\n"
      "v_mfma_f32_16x16x32_bf16 v[144:147], a[252:255], v[104:107], v[144:147]\n"
      "s_nop 0\n"
      "v_max_f32_dpp v221, v221, v221 row_ror:4 row_mask:0xf bank_mask:0xf\n"
      "v_cvt_pk_bf16_f32 v201, v164, v165\n"
      "v_mfma_f32_16x16x32_bf16 v[144:147], v[224:227], v[120:123], v[144:147]\n"
      "s_nop 0\n"
      "v_fma_f32 v100, v202, v202, s52\n"
      "v_max_f32_dpp v221, v221, v221 row_ror:2 row_mask:0xf bank_mask:0xf\n"
      "v_mfma_f32_16x16x32_bf16 v[144:147], v[228:231], v[170:173], v[144:147]\n"
      "ds_write_b128 v3, v[198:201] offset:256\n"
      "v_mfma_f32_16x16x32_bf16 v[148:151], v[244:247], v[104:107], v[148:151]\n"
      "v_add_f32 v124, v124, v178\n"
      "v_max_f32_dpp v221, v221, v221 row_ror:1 row_mask:0xf bank_mask:0xf\n"
      "v_mfma_f32_16x16x32_bf16 v[148:151], v[248:251], v[120:123], v[148:151]\n"
      "s_nop 0\n"
      "v_max_f32_dpp v221, v221, v221 row_bcast:15 row_mask:0xa bank_mask:0xf\n"
      "v_cvt_pk_bf16_f32 v96, v96, v97\n"
      "v_mfma_f32_16x16x32_bf16 v[148:151], v[252:255], v[170:173], v[148:151]\n"
      "v_max_f32_dpp v221, v221, v221 row_bcast:31 row_mask:0xc bank_mask:0xf\n"
      "v_mul_f32_e64 v101, v203, v203\n"
      "v_mfma_f32_16x16x32_bf16 v[126:129], v[32:35], v[134:137], 0\n"
      "ds_write_b64 v4, v[154:155]\n"
      "v_mfma_f32_16x16x32_bf16 v[126:129], v[32:35], v[88:91], v[126:129]\n"
      "v_cvt_pk_bf16_f32 v97, v98, v99\n"
      "v_readlane_b32 s72, v221, 63\n"
      "v_mfma_f32_16x16x32_bf16 v[152:155], v[130:133], v[134:137], 0\n"
      "v_fmac_f32 v100, v204, v204\n"
      "v_subrev_f32 v218, s72, v218\n"
      "v_mfma_f32_16x16x32_bf16 v[152:155], v[76:79], v[88:91], v[152:155]\n"
      "v_exp_f32 v218, v218\n"
      "v_mfma_f32_16x16x32_bf16 v[220:223], v[214:217], v[134:137], 0\n"
      "v_cvt_pk_bf16_f32 v218, v218, v218\n"
      "v_fmac_f32 v101, v205, v205\n"
      "v_mfma_f32_16x16x32_bf16 v[220:223], v[80:83], v[88:91], v[220:223]\n"
      "ds_write_b16 v13, v218\n"
      "v_mfma_f32_16x16x32_bf16 v[104:107], v[194:197], v[134:137], 0\n"
      "ds_read_b128 v[210:213], v14 offset:64\n"
      "v_rcp_f32_e64 v126, v126\n"
      "v_mfma_f32_16x16x32_bf16 v[104:107], v[84:87], v[88:91], v[104:107]\n"
      "ds_write_b64 v4, v[96:97] offset:256\n"
      "ds_read_b128 v[156:159], v14\n"
      "v_pk_fma_f32 v[100:101], v[144:145], v[144:145], v[100:101]\n"
      "v_pk_fma_f32 v[100:101], v[146:147], v[146:147], v[100:101]\n"
      "v_pk_mul_f32 v[154:155], v[154:155], v[126:127] op_sel_hi:[1,0]\n"
      "v_pk_mul_f32 v[152:153], v[152:153], v[126:127] op_sel_hi:[1,0]\n"
      "v_pk_fma_f32 v[100:101], v[148:149], v[148:149], v[100:101]\n"
      "v_pk_mul_f32 v[222:223], v[222:223], v[126:127] op_sel_hi:[1,0]\n"
      "v_pk_mul_f32 v[220:221], v[220:221], v[126:127] op_sel_hi:[1,0]\n"
      "v_cvt_pk_bf16_f32 v152, v152, v153\n"
      "v_pk_fma_f32 v[100:101], v[150:151], v[150:151], v[100:101]\n"
      "v_cvt_pk_bf16_f32 v153, v154, v155\n"
      "v_add_f32 v100, v100, v101\n"
      "v_cvt_pk_bf16_f32 v155, v222, v223\n"
      "v_mov_b32 v178, v100\n"
      "v_cvt_pk_bf16_f32 v154, v220, v221\n"
      "ds_write_b128 v3, v[152:155] offset:512\n"
      "v_permlane16_swap_b32_e64 v100, v178\n"
      "v_add_f32_e64 v100, v100, v178\n"
      "v_pk_mul_f32 v[106:107], v[106:107], v[126:127] op_sel_hi:[1,0]\n"
      "v_pk_mul_f32 v[104:105], v[104:105], v[126:127] op_sel_hi:[1,0]\n"
      "v_permlane32_swap_b32 v124, v100\n"
      "v_add_f32_e64 v124, v124, v100\n"
      "s_waitcnt lgkmcnt(1)\n"
      "v_mfma_f32_16x16x32_bf16 v[152:155], v[156:159], v[32:35], 0\n"
      "ds_write_b32 v31, v124\n"
      "v_cvt_pk_bf16_f32 v104, v104, v105\n"
      "v_cvt_pk_bf16_f32 v105, v106, v107\n"
      "v_mfma_f32_16x16x32_bf16 v[152:155], v[210:213], v[32:35], v[152:155]\n"
      "ds_write_b64 v4, v[104:105] offset:512\n"
      "s_waitcnt lgkmcnt(0)\n"
      "s_barrier\n"
      "s_cbranch_vccz L_sel1_%=\n"
      "s_branch L_disp1_%=\n"
      "L_tail1_%=:\n"
      "s_nop 4\n"
      "v_mfma_f32_16x16x32_bf16 v[218:221], v[156:159], v[130:133], 0\n"
      "ds_read_b128 v[98:101], v9\n"
      "v_subrev_u32 v105, 115456, v9\n"
      "v_and_b32 v131, 0x300, v105\n"
      "v_mfma_f32_16x16x32_bf16 v[218:221], v[210:213], v[76:79], v[218:221]\n"
      "v_add_u32 v105, v105, v131\n"
      "v_add_u32 v105, 149504, v105\n"
      "v_mfma_f32_16x16x32_bf16 v[134:137], v[156:159], v[214:217], 0\n"
      "ds_read_b128 v[214:217], v105\n"
      "ds_read_b128 v[162:165], v105 offset:2048\n"
      "v_mfma_f32_16x16x32_bf16 v[134:137], v[210:213], v[80:83], v[134:137]\n"
      "ds_read_b128 v[140:143], v2\n"
      "ds_read_b128 v[106:109], v105 offset:4096\n"
      "v_mfma_f32_16x16x32_bf16 v[128:131], v[156:159], v[194:197], 0\n"
      "ds_read_b128 v[156:159], v105 offset:6144\n"
      "ds_read_b128 v[194:197], v105 offset:10240\n"
      "v_mfma_f32_16x16x32_bf16 v[128:131], v[210:213], v[84:87], v[128:131]\n"
      "s_waitcnt lgkmcnt(6)\n"
      "v_add_f32 v100, v100, v101\n"
      "v_add_f32 v98, v98, v99\n"
      "v_add_f32 v98, v98, v100\n"
      "v_rsq_f32_e64 v98, v98\n"
      "v_subrev_u32 v101, 115456, v9\n"
      "v_mul_f32 v98, 0x415db3d7, v98\n"
      "v_mov_b32 v100, v98\n"
      "s_waitcnt lgkmcnt(5)\n"
      "v_mfma_f32_16x16x32_bf16 v[210:213], a[216:219], v[214:217], 0\n"
      "ds_read_b128 v[130:133], v2 offset:6144\n"
      "s_waitcnt lgkmcnt(5)\n"
      "v_mfma_f32_16x16x32_bf16 v[210:213], a[220:223], v[162:165], v[210:213]\n"
      "v_permlane32_swap_b32 v98, v100\n"
      "v_mfma_f32_16x16x32_bf16 v[220:223], a[240:243], v[214:217], 0\n"
      "ds_read_b128 v[136:139], v2 offset:9216\n"
      "v_mov_b32 v99, v98\n"
      "v_mfma_f32_16x16x32_bf16 v[220:223], a[244:247], v[162:165], v[220:223]\n"
      "s_mul_i32 s92, s40, 2048\n"
      "v_permlane16_swap_b32 v98, v99\n"
      "v_mfma_f32_16x16x32_bf16 v[198:201], v[232:235], v[214:217], 0\n"
      "s_waitcnt lgkmcnt(3)\n"
      "v_mul_f32 v186, v99, v186\n"
      "v_mul_f32_e64 v187, v99, v187\n"
      "v_mfma_f32_16x16x32_bf16 v[198:201], v[236:239], v[162:165], v[198:201]\n"
      "ds_read_b128 v[214:217], v105 offset:8192\n"
      "v_mul_f32 v202, v100, v202\n"
      "v_mfma_f32_16x16x32_bf16 v[210:213], a[224:227], v[106:109], v[210:213]\n"
      "ds_read_b128 v[120:123], v2 offset:3072\n"
      "v_mul_f32 v203, v100, v203\n"
      "v_cvt_pk_bf16_f32 v202, v202, v203\n"
      "v_cvt_pk_bf16_f32 v186, v186, v187\n"
      "v_and_b32 v80, 0x3fffffff, v37\n"
      "v_pk_mul_f32 v[146:147], v[146:147], v[100:101] op_sel_hi:[1,0]\n"
      "v_pk_mul_f32 v[208:209], v[208:209], v[98:99] op_sel:[0,1]\n"
      "v_pk_mul_f32 v[148:149], v[148:149], v[100:101] op_sel_hi:[1,0]\n"
      "v_pk_mul_f32 v[204:205], v[204:205], v[100:101] op_sel_hi:[1,0]\n"
      "v_pk_mul_f32 v[176:177], v[176:177], v[98:99] op_sel:[0,1]\n"
      "v_pk_mul_f32 v[166:167], v[166:167], v[98:99] op_sel_hi:[1,0]\n"
      "v_pk_mul_f32 v[150:151], v[150:151], v[100:101] op_sel_hi:[1,0]\n"
      "v_pk_mul_f32 v[116:117], v[116:117], v[98:99] op_sel_hi:[1,0]\n"
      "v_pk_mul_f32 v[118:119], v[118:119], v[98:99] op_sel_hi:[1,0]\n"
      "v_pk_mul_f32 v[144:145], v[144:145], v[100:101] op_sel_hi:[1,0]\n"
      "v_pk_mul_f32 v[188:189], v[188:189], v[98:99] op_sel:[0,1]\n"
      "v_pk_mul_f32 v[114:115], v[114:115], v[98:99] op_sel_hi:[1,0]\n"
      "v_pk_mul_f32 v[174:175], v[174:175], v[98:99] op_sel:[0,1]\n"
      "v_cvt_pk_bf16_f32 v166, v166, v167\n"
      "v_cvt_pk_bf16_f32 v187, v188, v189\n"
      "v_pk_mul_f32 v[168:169], v[168:169], v[98:99] op_sel_hi:[1,0]\n"
      "v_cvt_pk_bf16_f32 v167, v168, v169\n"
      "v_pk_mul_f32 v[206:207], v[206:207], v[98:99] op_sel:[0,1]\n"
      "v_pk_mul_f32 v[112:113], v[112:113], v[98:99] op_sel_hi:[1,0]\n"
      "v_mfma_f32_16x16x32_bf16 v[210:213], a[228:231], v[156:159], v[210:213]\n"
      "v_cvt_pk_bf16_f32 v148, v148, v149\n"
      "v_cvt_pk_bf16_f32 v116, v116, v117\n"
      "v_mfma_f32_16x16x32_bf16 v[220:223], a[248:251], v[106:109], v[220:223]\n"
      "v_cvt_pk_bf16_f32 v168, v112, v113\n"
      "v_cvt_pk_bf16_f32 v206, v206, v207\n"
      "v_mfma_f32_16x16x32_bf16 v[220:223], a[252:255], v[156:159], v[220:223]\n"
      "v_cvt_pk_bf16_f32 v189, v176, v177\n"
      "v_cvt_pk_bf16_f32 v188, v174, v175\n"
      "v_cvt_pk_bf16_f32 v203, v204, v205\n"
      "v_cvt_pk_bf16_f32 v117, v118, v119\n"
      "v_mfma_f32_16x16x32_bf16 v[198:201], v[240:243], v[106:109], v[198:201]\n"
      "s_waitcnt lgkmcnt(0)\n"
      "v_cvt_pk_bf16_f32 v207, v208, v209\n"
      "v_cvt_pk_bf16_f32 v204, v144, v145\n"
      "v_mfma_f32_16x16x32_bf16 v[198:201], v[244:247], v[156:159], v[198:201]\n"
      "ds_write_b64 v6, v[206:207] offset:24576\n"
      "v_mfma_f32_16x16x32_bf16 v[210:213], a[232:235], v[214:217], v[210:213]\n"
      "ds_write_b64 v6, v[116:117] offset:18432\n"
      "v_mfma_f32_16x16x32_bf16 v[210:213], a[236:239], v[194:197], v[210:213]\n"
      "v_cvt_pk_bf16_f32 v169, v114, v115\n"
      "v_cvt_pk_bf16_f32 v149, v150, v151\n"
      "v_mfma_f32_16x16x32_bf16 v[220:223], v[224:227], v[214:217], v[220:223]\n"
      "ds_write_b128 v5, v[186:189] offset:24576\n"
      "v_cvt_pk_bf16_f32 v205, v146, v147\n"
      "v_mfma_f32_16x16x32_bf16 v[220:223], v[228:231], v[194:197], v[220:223]\n"
      "ds_write_b128 v5, v[202:205] offset:30720\n"
      "v_mfma_f32_16x16x32_bf16 v[198:201], v[248:251], v[214:217], v[198:201]\n"
      "ds_write_b64 v6, v[148:149] offset:30720\n"
      "v_mfma_f32_16x16x32_bf16 v[198:201], v[252:255], v[194:197], v[198:201]\n"
      "ds_write_b128 v5, v[166:169] offset:18432\n"
      "v_mfma_f32_16x16x32_bf16 v[166:169], a[216:219], v[140:143], 0\n"
      "ds_read_b128 v[186:189], v2 offset:15360\n"
      "v_add_u32 v109, s92, v101\n"
      "v_mfma_f32_16x16x32_bf16 v[166:169], a[220:223], v[120:123], v[166:169]\n"
      "ds_read_b128 v[174:177], v2 offset:12288\n"
      "v_mov_b32_dpp v152, v152 quad_perm:[0,0,0,0] row_mask:0xf bank_mask:0xf\n"
      "v_mfma_f32_16x16x32_bf16 v[112:115], a[240:243], v[140:143], 0\n"
      "ds_read_b128 v[206:209], v7\n"
      "v_cndmask_b32 v218, v218, v134, s[58:59]\n"
      "v_mfma_f32_16x16x32_bf16 v[112:115], a[244:247], v[120:123], v[112:115]\n"
      "ds_read_b128 v[202:205], v2 offset:12544\n"
      "v_lshrrev_b32_e64 v101, 4, v109\n"
      "v_mfma_f32_16x16x32_bf16 v[116:119], v[232:235], v[140:143], 0\n"
      "ds_read_b128 v[144:147], v2 offset:9472\n"
      "v_subrev_u32 v108, 73728, v6\n"
      "v_mfma_f32_16x16x32_bf16 v[116:119], v[236:239], v[120:123], v[116:119]\n"
      "v_add_u32 v82, 64, v101\n"
      "v_add_u32 v80, 1179648, v80\n"
      "v_mfma_f32_16x16x32_bf16 v[166:169], a[224:227], v[130:133], v[166:169]\n"
      "ds_read_b128 v[148:151], v2 offset:3328\n"
      "v_mul_f32 v85, v211, v211\n"
      "v_mfma_f32_16x16x32_bf16 v[166:169], a[228:231], v[136:139], v[166:169]\n"
      "ds_read_b128 v[194:197], v7 offset:8192\n"
      "v_fma_f32 v84, v210, v210, s52\n"
      "v_mfma_f32_16x16x32_bf16 v[112:115], a[248:251], v[130:133], v[112:115]\n"
      "ds_read_b128 v[154:157], v2 offset:15616\n"
      "v_fmac_f32 v84, v212, v212\n"
      "v_mfma_f32_16x16x32_bf16 v[112:115], a[252:255], v[136:139], v[112:115]\n"
      "ds_read_b128 v[214:217], v2 offset:12800\n"
      "v_fmac_f32 v85, v213, v213\n"
      "v_mfma_f32_16x16x32_bf16 v[116:119], v[240:243], v[130:133], v[116:119]\n"
      "s_waitcnt lgkmcnt(5)\n"
      "v_rcp_f32 v152, v152\n"
      "v_mfma_f32_16x16x32_bf16 v[116:119], v[244:247], v[136:139], v[116:119]\n"
      "ds_read_b128 v[130:133], v2 offset:256\n"
      "v_mfma_f32_16x16x32_bf16 v[166:169], a[232:235], v[174:177], v[166:169]\n"
      "ds_read_b128 v[134:137], v2 offset:6400\n"
      "v_mfma_f32_16x16x32_bf16 v[166:169], a[236:239], v[186:189], v[166:169]\n"
      "v_fmac_f32 v85, v221, v221\n"
      "v_cndmask_b32 v218, v218, v128, s[60:61]\n"
      "v_mfma_f32_16x16x32_bf16 v[112:115], v[224:227], v[174:177], v[112:115]\n"
      "buffer_store_dwordx4 v[206:209], v20, s[24:27], s85 offen sc1\n"
      "v_mfma_f32_16x16x32_bf16 v[112:115], v[228:231], v[186:189], v[112:115]\n"
      "v_ashrrev_i32 v122, 31, v37\n"
      "v_mfma_f32_16x16x32_bf16 v[116:119], v[248:251], v[174:177], v[116:119]\n"
      "s_waitcnt lgkmcnt(1)\n"
      "v_mul_f32 v218, v152, v218\n"
      "v_cvt_pk_bf16_f32 v218, v218, v218\n"
      "v_mfma_f32_16x16x32_bf16 v[116:119], v[252:255], v[186:189], v[116:119]\n"
      "ds_read_b128 v[126:129], v2 offset:15872\n"
      "v_mul_u32_u24 v86, 2731, v82\n"
      "v_mfma_f32_16x16x32_bf16 v[186:189], a[216:219], v[130:133], 0\n"
      "v_and_b32 v122, 100270464, v122\n"
      "v_sub_u32 v80, v80, v122\n"
      "v_mfma_f32_16x16x32_bf16 v[186:189], a[220:223], v[148:151], v[186:189]\n"
      "v_fmac_f32 v84, v220, v220\n"
      "v_fma_f32 v138, v166, v166, s52\n"
      "v_mfma_f32_16x16x32_bf16 v[174:177], a[240:243], v[130:133], 0\n"
      "s_waitcnt lgkmcnt(1)\n"
      "v_lshrrev_b32 v86, 16, v86\n"
      "v_mfma_f32_16x16x32_bf16 v[174:177], a[244:247], v[148:151], v[174:177]\n"
      "ds_read_b128 v[158:161], v2 offset:6656\n"
      "v_mul_f32_e64 v139, v167, v167\n"
      "v_mfma_f32_16x16x32_bf16 v[206:209], v[232:235], v[130:133], 0\n"
      "ds_read_b128 v[130:133], v7 offset:4096\n"
      "v_bfe_i32 v122, v37, 30, 1\n"
      "v_mfma_f32_16x16x32_bf16 v[206:209], v[236:239], v[148:151], v[206:209]\n"
      "ds_write_b16 v12, v218\n"
      "v_mfma_f32_16x16x32_bf16 v[186:189], a[224:227], v[134:137], v[186:189]\n"
      "v_fmac_f32 v138, v168, v168\n"
      "v_fmac_f32 v139, v169, v169\n"
      "v_mfma_f32_16x16x32_bf16 v[186:189], a[228:231], v[144:147], v[186:189]\n"
      "s_waitcnt lgkmcnt(0)\n"
      "s_barrier\n"
      "v_and_b32 v122, 196224, v122\n"
      "v_mfma_f32_16x16x32_bf16 v[174:177], a[248:251], v[134:137], v[174:177]\n"
      "v_add_u32 v218, 0, v101\n"
      "v_fmac_f32 v84, v222, v222\n"
      "v_mfma_f32_16x16x32_bf16 v[174:177], a[252:255], v[144:147], v[174:177]\n"
      "v_mul_u32_u24 v78, 24, v86\n"
      "v_fmac_f32 v138, v112, v112\n"
      "v_mfma_f32_16x16x32_bf16 v[206:209], v[240:243], v[134:137], v[206:209]\n"
      "ds_read_b128 v[134:137], v2 offset:512\n"
      "v_mul_u32_u24 v81, 2731, v218\n"
      "v_mfma_f32_16x16x32_bf16 v[206:209], v[244:247], v[144:147], v[206:209]\n"
      "v_lshrrev_b32 v81, 16, v81\n"
      "v_fmac_f32 v85, v223, v223\n"
      "v_mfma_f32_16x16x32_bf16 v[186:189], a[232:235], v[202:205], v[186:189]\n"
      "ds_read_b128 v[140:143], v2 offset:3584\n"
      "v_sub_u32 v78, v82, v78\n"
      "v_mfma_f32_16x16x32_bf16 v[186:189], a[236:239], v[154:157], v[186:189]\n"
      "ds_read_b128 v[98:101], v105 offset:2304\n"
      "v_sub_u32 v80, v80, v122\n"
      "v_mfma_f32_16x16x32_bf16 v[174:177], v[224:227], v[202:205], v[174:177]\n"
      "ds_read_b128 v[120:123], v2 offset:9728\n"
      "v_fmac_f32 v85, v199, v199\n"
      "v_mfma_f32_16x16x32_bf16 v[174:177], v[228:231], v[154:157], v[174:177]\n"
      "s_nop 1\n"
      "v_mul_f32 v153, v187, v187\n"
      "v_mfma_f32_16x16x32_bf16 v[206:209], v[248:251], v[202:205], v[206:209]\n"
      "s_waitcnt lgkmcnt(3)\n"
      "v_subrev_u32 v219, 73728, v5\n"
      "v_fmac_f32_e64 v139, v113, v113\n"
      "v_mfma_f32_16x16x32_bf16 v[206:209], v[252:255], v[154:157], v[206:209]\n"
      "ds_read_b128 v[154:157], v105 offset:256\n"
      "v_fma_f32 v152, v186, v186, s52\n"
      "v_mfma_f32_16x16x32_bf16 v[202:205], a[216:219], v[134:137], 0\n"
      "s_waitcnt lgkmcnt(3)\n"
      "v_mul_u32_u24 v76, 24, v81\n"
      "v_mfma_f32_16x16x32_bf16 v[202:205], a[220:223], v[140:143], v[202:205]\n"
      "ds_read_b128 v[180:183], v7 offset:12288\n"
      "v_sub_u32_e64 v76, v218, v76\n"
      "v_mfma_f32_16x16x32_bf16 v[144:147], a[240:243], v[134:137], 0\n"
      "v_fmac_f32 v84, v198, v198\n"
      "v_fmac_f32 v139, v115, v115\n"
      "v_mfma_f32_16x16x32_bf16 v[144:147], a[244:247], v[140:143], v[144:147]\n"
      "v_mfma_f32_16x16x32_bf16 v[148:151], v[232:235], v[134:137], 0\n"
      "v_fmac_f32 v153, v189, v189\n"
      "v_and_b32 v82, 7, v86\n"
      "v_mfma_f32_16x16x32_bf16 v[148:151], v[236:239], v[140:143], v[148:151]\n"
      "s_waitcnt lgkmcnt(1)\n"
      "v_fmac_f32 v138, v114, v114\n"
      "v_fmac_f32_e64 v139, v117, v117\n"
      "v_mfma_f32_16x16x32_bf16 v[202:205], a[224:227], v[158:161], v[202:205]\n"
      "ds_read_b128 v[134:137], v105 offset:6400\n"
      "v_fmac_f32 v138, v116, v116\n"
      "v_mfma_f32_16x16x32_bf16 v[202:205], a[228:231], v[120:123], v[202:205]\n"
      "v_fmac_f32 v139, v119, v119\n"
      "v_fmac_f32 v138, v118, v118\n"
      "v_mfma_f32_16x16x32_bf16 v[144:147], a[248:251], v[158:161], v[144:147]\n"
      "v_fmac_f32 v85, v201, v201\n"
      "v_fmac_f32 v152, v188, v188\n"
      "v_mfma_f32_16x16x32_bf16 v[144:147], a[252:255], v[120:123], v[144:147]\n"
      "v_fmac_f32 v152, v174, v174\n"
      "v_fmac_f32_e64 v153, v175, v175\n"
      "v_mfma_f32_16x16x32_bf16 v[148:151], v[240:243], v[158:161], v[148:151]\n"
      "v_mfma_f32_16x16x32_bf16 v[148:151], v[244:247], v[120:123], v[148:151]\n"
      "ds_read_b128 v[158:161], v105 offset:10496\n"
      "v_fmac_f32 v153, v177, v177\n"
      "v_mfma_f32_16x16x32_bf16 v[202:205], a[232:235], v[214:217], v[202:205]\n"
      "buffer_store_dwordx4 v[130:133], v21, s[24:27], s85 offen sc1\n"
      "v_mfma_f32_16x16x32_bf16 v[202:205], a[236:239], v[126:129], v[202:205]\n"
      "s_nop 0\n"
      "ds_read_b128 v[130:133], v105 offset:4352\n"
      "v_lshl_add_u32 v86, v86, 2, 12\n"
      "v_mfma_f32_16x16x32_bf16 v[144:147], v[224:227], v[214:217], v[144:147]\n"
      "ds_read_b128 v[140:143], v7 offset:16384\n"
      "v_fmac_f32 v152, v176, v176\n"
      "v_mfma_f32_16x16x32_bf16 v[144:147], v[228:231], v[126:129], v[144:147]\n"
      "s_nop 0\n"
      "v_mul_f32_e64 v123, v203, v203\n"
      "v_mfma_f32_16x16x32_bf16 v[148:151], v[248:251], v[214:217], v[148:151]\n"
      "v_fma_f32 v122, v202, v202, s52\n"
      "v_fmac_f32 v123, v205, v205\n"
      "v_mfma_f32_16x16x32_bf16 v[148:151], v[252:255], v[126:129], v[148:151]\n"
      "v_fmac_f32 v153, v207, v207\n"
      "v_fmac_f32_e64 v152, v206, v206\n"
      "v_mfma_f32_16x16x32_bf16 v[214:217], a[216:219], v[154:157], 0\n"
      "v_fmac_f32 v122, v204, v204\n"
      "v_fmac_f32 v123, v145, v145\n"
      "v_mfma_f32_16x16x32_bf16 v[214:217], a[220:223], v[98:101], v[214:217]\n"
      "v_add_f32 v138, v138, v139\n"
      "v_fmac_f32 v122, v144, v144\n"
      "v_mfma_f32_16x16x32_bf16 v[126:129], a[240:243], v[154:157], 0\n"
      "v_and_b32 v218, 7, v81\n"
      "v_fmac_f32 v153, v209, v209\n"
      "v_mfma_f32_16x16x32_bf16 v[126:129], a[244:247], v[98:101], v[126:129]\n"
      "v_fmac_f32 v122, v146, v146\n"
      "v_fmac_f32 v123, v147, v147\n"
      "v_mfma_f32_16x16x32_bf16 v[162:165], v[232:235], v[154:157], 0\n"
      "s_waitcnt lgkmcnt(0)\n"
      "v_fmac_f32 v152, v208, v208\n"
      "v_add_f32 v152, v152, v153\n"
      "v_mfma_f32_16x16x32_bf16 v[162:165], v[236:239], v[98:101], v[162:165]\n"
      "s_nop 0\n"
      "v_permlane16_swap_b32 v138, v152\n"
      "v_mfma_f32_16x16x32_bf16 v[214:217], a[224:227], v[130:133], v[214:217]\n"
      "ds_read_b128 v[154:157], v105 offset:8448\n"
      "v_fmac_f32 v123, v149, v149\n"
      "v_mfma_f32_16x16x32_bf16 v[214:217], a[228:231], v[134:137], v[214:217]\n"
      "buffer_store_dwordx4 v[194:197], v22, s[24:27], s85 offen sc1\n"
      "v_mfma_f32_16x16x32_bf16 v[126:129], a[248:251], v[130:133], v[126:129]\n"
      "v_fmac_f32 v122, v148, v148\n"
      "v_add_f32 v138, v138, v152\n"
      "v_mfma_f32_16x16x32_bf16 v[126:129], a[252:255], v[134:137], v[126:129]\n"
      "v_fmac_f32 v122, v150, v150\n"
      "v_fmac_f32 v84, v200, v200\n"
      "v_mfma_f32_16x16x32_bf16 v[162:165], v[240:243], v[130:133], v[162:165]\n"
      "v_fmac_f32_e64 v123, v151, v151\n"
      "v_lshl_add_u32 v81, v81, 2, 12\n"
      "v_mfma_f32_16x16x32_bf16 v[162:165], v[244:247], v[134:137], v[162:165]\n"
      "s_waitcnt lgkmcnt(0)\n"
      "v_add_f32 v122, v122, v123\n"
      "v_mov_b32 v152, v122\n"
      "v_mfma_f32_16x16x32_bf16 v[214:217], a[232:235], v[154:157], v[214:217]\n"
      "s_nop 0\n"
      "v_permlane16_swap_b32 v122, v152\n"
      "v_mfma_f32_16x16x32_bf16 v[214:217], a[236:239], v[158:161], v[214:217]\n"
      "ds_bpermute_b32 v153, v86, v80\n"
      "v_add_f32 v122, v122, v152\n"
      "v_mfma_f32_16x16x32_bf16 v[126:129], v[224:227], v[154:157], v[126:129]\n"
      "s_nop 0\n"
      "v_permlane32_swap_b32 v138, v122\n"
      "v_mfma_f32_16x16x32_bf16 v[126:129], v[228:231], v[158:161], v[126:129]\n"
      "buffer_store_dwordx4 v[180:183], v23, s[24:27], s85 offen sc1\n"
      "v_mfma_f32_16x16x32_bf16 v[162:165], v[248:251], v[154:157], v[162:165]\n"
      "ds_bpermute_b32 v152, v81, v80\n"
      "v_add_f32 v138, v138, v122\n"
      "v_mfma_f32_16x16x32_bf16 v[162:165], v[252:255], v[158:161], v[162:165]\n"
      "ds_write_b32 v31, v138 offset:1024\n"
      "v_mul_f32 v157, v215, v215\n"
      "v_fma_f32 v156, v214, v214, s52\n"
      "v_add_f32 v84, v84, v85\n"
      "v_pk_fma_f32 v[156:157], v[216:217], v[216:217], v[156:157]\n"
      "v_xor_b32 v78, v78, v82\n"
      "v_pk_fma_f32 v[156:157], v[126:127], v[126:127], v[156:157]\n"
      "v_pk_fma_f32 v[156:157], v[128:129], v[128:129], v[156:157]\n"
      "v_pk_fma_f32 v[156:157], v[162:163], v[162:163], v[156:157]\n"
      "v_pk_fma_f32 v[156:157], v[164:165], v[164:165], v[156:157]\n"
      "v_add_f32 v156, v156, v157\n"
      "s_nop 1\n"
      "v_permlane16_swap_b32 v84, v156\n"
      "v_add_f32 v84, v84, v156\n"
      "v_mov_b32 v156, v84\n"
      "s_waitcnt lgkmcnt(2)\n"
      "v_lshl_add_u32 v153, v78, 4, v153\n"
      "v_permlane32_swap_b32 v84, v156\n"
      "v_add_f32 v84, v84, v156\n"
      "ds_write_b32 v31, v84 offset:4608\n"
      "buffer_store_dwordx4 v[140:143], v25, s[24:27], s85 offen sc1\n"
      "v_readlane_b32 s85, v37, s42\n"
      "s_nop 0\n"
      "s_or_b32 s95, s85, s48\n"
      "s_cmp_ge_u32 s95, 0x40000000\n"
      "s_cbranch_scc1 L_rare_27_%=\n"
      "L_back_28_%=:\n"
      "v_xor_b32_e64 v76, v76, v218\n"
      "s_waitcnt lgkmcnt(1)\n"
      "v_lshl_add_u32 v152, v76, 4, v152\n"
      "s_add_u32 s42, s42, 1\n"
      "s_waitcnt lgkmcnt(0)\n"
      "s_barrier\n"
      "ds_read_b128 v[154:157], v7 offset:18432\n"
      "ds_read_b128 v[130:133], v9 offset:1024\n"
      "ds_read_b128 v[194:197], v7 offset:22528\n"
      "ds_read_b128 v[134:137], v9 offset:4608\n"
      "ds_read_b128 v[158:161], v7 offset:30720\n"
      "ds_read_b128 v[138:141], v7 offset:26624\n"
      "ds_read_b128 v[180:183], v7 offset:34816\n"
      "s_waitcnt lgkmcnt(6)\n"
      "buffer_store_dwordx4 v[154:157], v20, s[24:27], s85 offen sc1\n"
      "s_waitcnt lgkmcnt(4)\n"
      "buffer_store_dwordx4 v[194:197], v21, s[24:27], s85 offen sc1\n"
      "s_waitcnt lgkmcnt(2)\n"
      "v_add_f32 v136, v136, v137\n"
      "v_add_f32 v130, v130, v131\n"
      "v_add_f32 v132, v132, v133\n"
      "v_add_f32 v130, v130, v132\n"
      "v_add_f32 v134, v134, v135\n"
      "v_add_f32 v134, v134, v136\n"
      "v_rsq_f32 v130, v130\n"
      "v_rsq_f32_e64 v134, v134\n"
      "v_mul_f32 v130, 0x415db3d7, v130\n"
      "v_mov_b32 v132, v130\n"
      "v_mul_f32 v134, 0x415db3d7, v134\n"
      "v_mov_b32 v136, v134\n"
      "v_permlane32_swap_b32 v130, v132\n"
      "v_mov_b32 v131, v130\n"
      "v_pk_mul_f32 v[148:149], v[148:149], v[132:133] op_sel_hi:[1,0]\n"
      "v_permlane32_swap_b32 v134, v136\n"
      "v_permlane16_swap_b32 v130, v131\n"
      "v_pk_mul_f32 v[166:167], v[166:167], v[130:131] op_sel_hi:[1,0]\n"
      "v_pk_mul_f32 v[206:207], v[206:207], v[130:131] op_sel:[0,1]\n"
      "v_pk_mul_f32 v[144:145], v[144:145], v[132:133] op_sel_hi:[1,0]\n"
      "v_mov_b32_e64 v135, v134\n"
      "v_pk_mul_f32 v[202:203], v[202:203], v[132:133] op_sel_hi:[1,0]\n"
      "v_pk_mul_f32 v[204:205], v[204:205], v[132:133] op_sel_hi:[1,0]\n"
      "v_pk_mul_f32 v[112:113], v[112:113], v[130:131] op_sel_hi:[1,0]\n"
      "v_permlane16_swap_b32_e64 v134, v135\n"
      "v_cvt_pk_bf16_f32 v206, v206, v207\n"
      "v_pk_mul_f32 v[118:119], v[118:119], v[130:131] op_sel_hi:[1,0]\n"
      "v_pk_mul_f32 v[186:187], v[186:187], v[130:131] op_sel:[0,1]\n"
      "v_pk_mul_f32 v[188:189], v[188:189], v[130:131] op_sel:[0,1]\n"
      "v_pk_mul_f32 v[198:199], v[198:199], v[134:135] op_sel_hi:[1,0]\n"
      "v_cvt_pk_bf16_f32 v148, v148, v149\n"
      "v_pk_mul_f32 v[116:117], v[116:117], v[130:131] op_sel_hi:[1,0]\n"
      "v_pk_mul_f32 v[208:209], v[208:209], v[130:131] op_sel:[0,1]\n"
      "v_cvt_pk_bf16_f32 v166, v166, v167\n"
      "v_pk_mul_f32 v[168:169], v[168:169], v[130:131] op_sel_hi:[1,0]\n"
      "v_cvt_pk_bf16_f32 v186, v186, v187\n"
      "v_pk_mul_f32 v[176:177], v[176:177], v[130:131] op_sel:[0,1]\n"
      "v_pk_mul_f32 v[216:217], v[216:217], v[134:135] op_sel:[0,1]\n"
      "v_pk_mul_f32 v[222:223], v[222:223], v[134:135] op_sel_hi:[1,0]\n"
      "v_pk_mul_f32 v[210:211], v[210:211], v[134:135] op_sel_hi:[1,0]\n"
      "v_pk_mul_f32 v[162:163], v[162:163], v[134:135] op_sel:[0,1]\n"
      "v_pk_mul_f32 v[220:221], v[220:221], v[134:135] op_sel_hi:[1,0]\n"
      "v_pk_mul_f32 v[212:213], v[212:213], v[134:135] op_sel_hi:[1,0]\n"
      "v_pk_mul_f32 v[128:129], v[128:129], v[134:135] op_sel:[0,1]\n"
      "v_cvt_pk_bf16_f32 v167, v168, v169\n"
      "v_pk_mul_f32 v[150:151], v[150:151], v[132:133] op_sel_hi:[1,0]\n"
      "v_pk_mul_f32 v[214:215], v[214:215], v[134:135] op_sel:[0,1]\n"
      "v_cvt_pk_bf16_f32 v202, v202, v203\n"
      "v_pk_mul_f32 v[126:127], v[126:127], v[134:135] op_sel:[0,1]\n"
      "v_cvt_pk_bf16_f32 v162, v162, v163\n"
      "v_cvt_pk_bf16_f32 v210, v210, v211\n"
      "v_pk_mul_f32 v[164:165], v[164:165], v[134:135] op_sel:[0,1]\n"
      "v_cvt_pk_bf16_f32 v211, v212, v213\n"
      "v_pk_mul_f32 v[200:201], v[200:201], v[134:135] op_sel_hi:[1,0]\n"
      "v_cvt_pk_bf16_f32 v116, v116, v117\n"
      "v_pk_mul_f32 v[174:175], v[174:175], v[130:131] op_sel:[0,1]\n"
      "v_cvt_pk_bf16_f32 v212, v220, v221\n"
      "v_cvt_pk_bf16_f32 v163, v164, v165\n"
      "v_cvt_pk_bf16_f32 v187, v188, v189\n"
      "v_pk_mul_f32 v[114:115], v[114:115], v[130:131] op_sel_hi:[1,0]\n"
      "v_cvt_pk_bf16_f32 v198, v198, v199\n"
      "v_pk_mul_f32 v[146:147], v[146:147], v[132:133] op_sel_hi:[1,0]\n"
      "v_cvt_pk_bf16_f32 v214, v214, v215\n"
      "v_cvt_pk_bf16_f32 v169, v114, v115\n"
      "v_cvt_pk_bf16_f32 v188, v174, v175\n"
      "v_cvt_pk_bf16_f32 v213, v222, v223\n"
      "v_cvt_pk_bf16_f32 v203, v204, v205\n"
      "v_cvt_pk_bf16_f32 v189, v176, v177\n"
      "v_cvt_pk_bf16_f32 v215, v216, v217\n"
      "v_cvt_pk_bf16_f32 v149, v150, v151\n"
      "v_cvt_pk_bf16_f32 v207, v208, v209\n"
      "v_cvt_pk_bf16_f32 v117, v118, v119\n"
      "v_cvt_pk_bf16_f32 v199, v200, v201\n"
      "v_cvt_pk_bf16_f32 v168, v112, v113\n"
      "v_cvt_pk_bf16_f32 v204, v144, v145\n"
      "v_cvt_pk_bf16_f32 v205, v146, v147\n"
      "v_cvt_pk_bf16_f32 v217, v128, v129\n"
      "v_cvt_pk_bf16_f32 v216, v126, v127\n"
      "s_waitcnt lgkmcnt(0)\n"
      "buffer_store_dwordx4 v[138:141], v22, s[24:27], s85 offen sc1\n"
      "ds_write_b128 v5, v[186:189] offset:6144\n"
      "ds_write_b64 v6, v[206:207] offset:6144\n"
      "ds_write_b128 v5, v[202:205] offset:12288\n"
      "ds_write_b128 v219, v[210:213]\n"
      "ds_write_b128 v5, v[166:169]\n"
      "ds_write_b64 v6, v[116:117]\n"
      "ds_write_b64 v108, v[162:163] offset:6144\n"
      "ds_write_b128 v219, v[214:217] offset:6144\n"
      "ds_write_b64 v6, v[148:149] offset:12288\n"
      "ds_write_b64 v108, v[198:199]\n"
      "buffer_store_dwordx4 v[158:161], v23, s[24:27], s85 offen sc1\n"
      "buffer_store_dwordx4 v[180:183], v25, s[24:27], s85 offen sc1\n"
      "v_readlane_b32 s85, v37, s42\n"
      "s_add_u32 s42, s42, 1\n"
      "s_or_b32 s95, s85, s48\n"
      "s_cmp_ge_u32 s95, 0x40000000\n"
      "s_cbranch_scc1 L_rare_31_%=\n"
      "L_back_32_%=:\n"
      "s_waitcnt lgkmcnt(0)\n"
      "s_barrier\n"
      "ds_read_b128 v[212:215], v7\n"
      "ds_read_b128 v[166:169], v7 offset:4096\n"
      "ds_read_b128 v[204:207], v7 offset:8192\n"
      "ds_read_b128 v[146:149], v7 offset:12288\n"
      "ds_read_b128 v[186:189], v7 offset:16384\n"
      "ds_read_b128 v[116:119], v109\n"
      "ds_read_b128 v[208:211], v109 offset:1024\n"
      "s_waitcnt lgkmcnt(6)\n"
      "buffer_store_dwordx4 v[212:215], v20, s[24:27], s85 offen sc1\n"
      "s_waitcnt lgkmcnt(5)\n"
      "buffer_store_dwordx4 v[166:169], v21, s[24:27], s85 offen sc1\n"
      "s_waitcnt lgkmcnt(4)\n"
      "buffer_store_dwordx4 v[204:207], v22, s[24:27], s85 offen sc1\n"
      "s_waitcnt lgkmcnt(3)\n"
      "buffer_store_dwordx4 v[146:149], v23, s[24:27], s85 offen sc1\n"
      "s_waitcnt lgkmcnt(2)\n"
      "buffer_store_dwordx4 v[186:189], v25, s[24:27], s85 offen sc1\n"
      "s_waitcnt lgkmcnt(1)\n"
      "buffer_store_dwordx4 v[116:119], v152, s[24:27], 0 offen sc1\n"
      "s_waitcnt lgkmcnt(0)\n"
      "buffer_store_dwordx4 v[208:211], v153, s[24:27], 0 offen sc1\n"
      "s_endpgm\n"
      "L_tail0_%=:\n"
      "s_nop 4\n"
      "v_mfma_f32_16x16x32_bf16 v[218:221], v[156:159], v[130:133], 0\n"
      "ds_read_b128 v[180:183], v9 offset:1024\n"
      "v_subrev_u32 v165, 115456, v9\n"
      "v_and_b32 v130, 0x300, v165\n"
      "v_add_u32_e64 v165, v165, v130\n"
      "v_add_u32 v165, 149504, v165\n"
      "v_mfma_f32_16x16x32_bf16 v[218:221], v[210:213], v[76:79], v[218:221]\n"
      "ds_read_b128 v[138:141], v165\n"
      "ds_read_b128 v[198:201], v165 offset:2048\n"
      "v_mfma_f32_16x16x32_bf16 v[134:137], v[156:159], v[214:217], 0\n"
      "ds_read_b128 v[160:163], v2 offset:21760\n"
      "ds_read_b128 v[214:217], v165 offset:6144\n"
      "v_mfma_f32_16x16x32_bf16 v[134:137], v[210:213], v[80:83], v[134:137]\n"
      "ds_read_b128 v[108:111], v2 offset:24576\n"
      "ds_read_b128 v[98:101], v165 offset:4096\n"
      "v_mfma_f32_16x16x32_bf16 v[128:131], v[156:159], v[194:197], 0\n"
      "ds_read_b128 v[156:159], v165 offset:8192\n"
      "s_waitcnt lgkmcnt(7)\n"
      "v_add_f32 v180, v180, v181\n"
      "v_add_f32 v182, v182, v183\n"
      "v_add_f32 v180, v180, v182\n"
      "v_mfma_f32_16x16x32_bf16 v[128:131], v[210:213], v[84:87], v[128:131]\n"
      "ds_read_b128 v[210:213], v2 offset:27648\n"
      "s_waitcnt lgkmcnt(7)\n"
      "v_mfma_f32_16x16x32_bf16 v[194:197], a[216:219], v[138:141], 0\n"
      "ds_read_b128 v[220:223], v165 offset:10240\n"
      "s_waitcnt lgkmcnt(7)\n"
      "v_mfma_f32_16x16x32_bf16 v[194:197], a[220:223], v[198:201], v[194:197]\n"
      "v_rsq_f32_e64 v180, v180\n"
      "v_mfma_f32_16x16x32_bf16 v[120:123], a[240:243], v[138:141], 0\n"
      "ds_read_b128 v[130:133], v2 offset:21504\n"
      "v_mul_f32 v180, 0x415db3d7, v180\n"
      "v_mfma_f32_16x16x32_bf16 v[120:123], a[244:247], v[198:201], v[120:123]\n"
      "ds_read_b128 v[104:107], v2 offset:18432\n"
      "v_mov_b32 v182, v180\n"
      "v_mfma_f32_16x16x32_bf16 v[170:173], v[232:235], v[138:141], 0\n"
      "s_nop 0\n"
      "v_permlane32_swap_b32 v180, v182\n"
      "v_mfma_f32_16x16x32_bf16 v[170:173], v[236:239], v[198:201], v[170:173]\n"
      "s_waitcnt lgkmcnt(5)\n"
      "v_mov_b32 v181, v180\n"
      "v_mul_f32 v202, v182, v202\n"
      "v_mul_f32_e64 v203, v182, v203\n"
      "v_permlane16_swap_b32_e64 v180, v181\n"
      "v_pk_mul_f32 v[148:149], v[148:149], v[182:183] op_sel_hi:[1,0]\n"
      "v_pk_mul_f32 v[166:167], v[166:167], v[180:181] op_sel_hi:[1,0]\n"
      "v_pk_mul_f32 v[116:117], v[116:117], v[180:181] op_sel_hi:[1,0]\n"
      "v_cvt_pk_bf16_f32 v202, v202, v203\n"
      "v_pk_mul_f32 v[174:175], v[174:175], v[180:181] op_sel:[0,1]\n"
      "v_pk_mul_f32 v[150:151], v[150:151], v[182:183] op_sel_hi:[1,0]\n"
      "v_pk_mul_f32 v[146:147], v[146:147], v[182:183] op_sel_hi:[1,0]\n"
      "v_pk_mul_f32 v[168:169], v[168:169], v[180:181] op_sel_hi:[1,0]\n"
      "v_pk_mul_f32 v[176:177], v[176:177], v[180:181] op_sel:[0,1]\n"
      "v_pk_mul_f32 v[112:113], v[112:113], v[180:181] op_sel_hi:[1,0]\n"
      "v_pk_mul_f32 v[186:187], v[186:187], v[180:181] op_sel:[0,1]\n"
      "v_pk_mul_f32 v[114:115], v[114:115], v[180:181] op_sel_hi:[1,0]\n"
      "v_cvt_pk_bf16_f32 v166, v166, v167\n"
      "v_pk_mul_f32 v[206:207], v[206:207], v[180:181] op_sel:[0,1]\n"
      "v_pk_mul_f32 v[188:189], v[188:189], v[180:181] op_sel:[0,1]\n"
      "v_pk_mul_f32 v[118:119], v[118:119], v[180:181] op_sel_hi:[1,0]\n"
      "v_pk_mul_f32 v[144:145], v[144:145], v[182:183] op_sel_hi:[1,0]\n"
      "v_pk_mul_f32 v[204:205], v[204:205], v[182:183] op_sel_hi:[1,0]\n"
      "v_cvt_pk_bf16_f32 v206, v206, v207\n"
      "v_pk_mul_f32 v[208:209], v[208:209], v[180:181] op_sel:[0,1]\n"
      "v_mfma_f32_16x16x32_bf16 v[194:197], a[224:227], v[98:101], v[194:197]\n"
      "v_cvt_pk_bf16_f32 v186, v186, v187\n"
      "v_cvt_pk_bf16_f32 v187, v188, v189\n"
      "v_mfma_f32_16x16x32_bf16 v[194:197], a[228:231], v[214:217], v[194:197]\n"
      "v_cvt_pk_bf16_f32 v203, v204, v205\n"
      "v_cvt_pk_bf16_f32 v148, v148, v149\n"
      "v_mfma_f32_16x16x32_bf16 v[120:123], a[248:251], v[98:101], v[120:123]\n"
      "v_cvt_pk_bf16_f32 v167, v168, v169\n"
      "v_cvt_pk_bf16_f32 v116, v116, v117\n"
      "v_mfma_f32_16x16x32_bf16 v[120:123], a[252:255], v[214:217], v[120:123]\n"
      "v_cvt_pk_bf16_f32 v204, v144, v145\n"
      "v_cvt_pk_bf16_f32 v149, v150, v151\n"
      "v_mfma_f32_16x16x32_bf16 v[170:173], v[240:243], v[98:101], v[170:173]\n"
      "v_cvt_pk_bf16_f32 v205, v146, v147\n"
      "v_cvt_pk_bf16_f32 v188, v174, v175\n"
      "v_cvt_pk_bf16_f32 v168, v112, v113\n"
      "v_mfma_f32_16x16x32_bf16 v[170:173], v[244:247], v[214:217], v[170:173]\n"
      "s_waitcnt lgkmcnt(0)\n"
      "v_cvt_pk_bf16_f32 v117, v118, v119\n"
      "v_cvt_pk_bf16_f32 v189, v176, v177\n"
      "v_mfma_f32_16x16x32_bf16 v[194:197], a[232:235], v[156:159], v[194:197]\n"
      "ds_write_b64 v6, v[148:149] offset:12288\n"
      "v_cvt_pk_bf16_f32 v207, v208, v209\n"
      "v_cvt_pk_bf16_f32 v169, v114, v115\n"
      "v_mfma_f32_16x16x32_bf16 v[194:197], a[236:239], v[220:223], v[194:197]\n"
      "ds_write_b128 v5, v[166:169]\n"
      "v_mfma_f32_16x16x32_bf16 v[120:123], v[224:227], v[156:159], v[120:123]\n"
      "ds_write_b128 v5, v[186:189] offset:6144\n"
      "v_mfma_f32_16x16x32_bf16 v[120:123], v[228:231], v[220:223], v[120:123]\n"
      "ds_write_b64 v6, v[206:207] offset:6144\n"
      "v_mfma_f32_16x16x32_bf16 v[170:173], v[248:251], v[156:159], v[170:173]\n"
      "ds_write_b64 v6, v[116:117]\n"
      "v_mfma_f32_16x16x32_bf16 v[170:173], v[252:255], v[220:223], v[170:173]\n"
      "ds_write_b128 v5, v[202:205] offset:12288\n"
      "v_mfma_f32_16x16x32_bf16 v[166:169], a[216:219], v[104:107], 0\n"
      "v_and_b32 v78, 0x3fffffff, v37\n"
      "v_mfma_f32_16x16x32_bf16 v[166:169], a[220:223], v[130:133], v[166:169]\n"
      "ds_read_b128 v[186:189], v2 offset:30720\n"
      "v_fma_f32 v126, v194, v194, s52\n"
      "v_mfma_f32_16x16x32_bf16 v[112:115], a[240:243], v[104:107], 0\n"
      "ds_read_b128 v[202:205], v2 offset:30976\n"
      "v_ashrrev_i32 v217, 31, v37\n"
      "v_mfma_f32_16x16x32_bf16 v[112:115], a[244:247], v[130:133], v[112:115]\n"
      "v_subrev_u32 v141, 73728, v5\n"
      "v_subrev_u32 v84, 73728, v6\n"
      "v_mfma_f32_16x16x32_bf16 v[116:119], v[232:235], v[104:107], 0\n"
      "v_mul_f32_e64 v127, v195, v195\n"
      "v_mov_b32_dpp v152, v152 quad_perm:[0,0,0,0] row_mask:0xf bank_mask:0xf\n"
      "v_mfma_f32_16x16x32_bf16 v[116:119], v[236:239], v[130:133], v[116:119]\n"
      "v_mfma_f32_16x16x32_bf16 v[166:169], a[224:227], v[108:111], v[166:169]\n"
      "ds_read_b128 v[174:177], v2 offset:33792\n"
      "v_add_u32 v78, 1179648, v78\n"
      "v_mfma_f32_16x16x32_bf16 v[166:169], a[228:231], v[210:213], v[166:169]\n"
      "v_subrev_u32 v157, 115456, v9\n"
      "v_fmac_f32_e64 v127, v197, v197\n"
      "v_mfma_f32_16x16x32_bf16 v[112:115], a[248:251], v[108:111], v[112:115]\n"
      "s_mul_i32 s92, s40, 2048\n"
      "ds_read_b128 v[144:147], v2 offset:18688\n"
      "v_mfma_f32_16x16x32_bf16 v[112:115], a[252:255], v[210:213], v[112:115]\n"
      "v_and_b32 v217, 100270464, v217\n"
      "v_mfma_f32_16x16x32_bf16 v[116:119], v[240:243], v[108:111], v[116:119]\n"
      "ds_read_b128 v[148:151], v2 offset:24832\n"
      "v_add_u32 v140, s92, v157\n"
      "v_mfma_f32_16x16x32_bf16 v[116:119], v[244:247], v[210:213], v[116:119]\n"
      "s_waitcnt lgkmcnt(3)\n"
      "v_lshrrev_b32 v157, 4, v140\n"
      "v_cndmask_b32 v218, v218, v134, s[58:59]\n"
      "v_mfma_f32_16x16x32_bf16 v[166:169], a[232:235], v[186:189], v[166:169]\n"
      "ds_read_b128 v[206:209], v7 offset:18432\n"
      "s_waitcnt lgkmcnt(3)\n"
      "v_mfma_f32_16x16x32_bf16 v[166:169], a[236:239], v[174:177], v[166:169]\n"
      "ds_read_b128 v[180:183], v2 offset:34048\n"
      "v_fmac_f32_e64 v126, v196, v196\n"
      "v_mfma_f32_16x16x32_bf16 v[112:115], v[224:227], v[186:189], v[112:115]\n"
      "ds_read_b128 v[212:215], v2 offset:25088\n"
      "v_add_u32 v134, 0, v157\n"
      "v_mfma_f32_16x16x32_bf16 v[112:115], v[228:231], v[174:177], v[112:115]\n"
      "s_waitcnt lgkmcnt(4)\n"
      "v_rcp_f32_e64 v152, v152\n"
      "v_mfma_f32_16x16x32_bf16 v[116:119], v[248:251], v[186:189], v[116:119]\n"
      "ds_read_b128 v[108:111], v2 offset:18944\n"
      "v_cndmask_b32 v218, v218, v128, s[60:61]\n"
      "v_mfma_f32_16x16x32_bf16 v[116:119], v[252:255], v[174:177], v[116:119]\n"
      "ds_read_b128 v[220:223], v2 offset:27904\n"
      "v_mul_u32_u24 v211, 2731, v134\n"
      "v_mfma_f32_16x16x32_bf16 v[186:189], a[216:219], v[144:147], 0\n"
      "ds_read_b128 v[198:201], v2 offset:28160\n"
      "v_mul_f32 v129, v167, v167\n"
      "v_mfma_f32_16x16x32_bf16 v[186:189], a[220:223], v[160:163], v[186:189]\n"
      "v_add_u32 v210, 64, v157\n"
      "v_fmac_f32 v126, v120, v120\n"
      "v_mfma_f32_16x16x32_bf16 v[174:177], a[240:243], v[144:147], 0\n"
      "s_waitcnt lgkmcnt(3)\n"
      "v_sub_u32_e64 v78, v78, v217\n"
      "v_mul_f32_e64 v218, v152, v218\n"
      "v_mfma_f32_16x16x32_bf16 v[174:177], a[244:247], v[160:163], v[174:177]\n"
      "buffer_store_dwordx4 v[206:209], v20, s[24:27], s46 offen sc1\n"
      "s_nop 1\n"
      "v_mfma_f32_16x16x32_bf16 v[206:209], v[232:235], v[144:147], 0\n"
      "v_mul_u32_u24 v100, 2731, v210\n"
      "v_mfma_f32_16x16x32_bf16 v[206:209], v[236:239], v[160:163], v[206:209]\n"
      "s_waitcnt lgkmcnt(0)\n"
      "v_fma_f32 v128, v166, v166, s52\n"
      "v_fmac_f32_e64 v129, v169, v169\n"
      "v_mfma_f32_16x16x32_bf16 v[186:189], a[224:227], v[148:151], v[186:189]\n"
      "ds_read_b128 v[156:159], v2 offset:22016\n"
      "v_mfma_f32_16x16x32_bf16 v[186:189], a[228:231], v[220:223], v[186:189]\n"
      "ds_read_b128 v[136:139], v2 offset:34304\n"
      "v_fmac_f32_e64 v127, v121, v121\n"
      "v_mfma_f32_16x16x32_bf16 v[174:177], a[248:251], v[148:151], v[174:177]\n"
      "ds_read_b128 v[104:107], v7 offset:26624\n"
      "v_cvt_pk_bf16_f32 v218, v218, v218\n"
      "v_mfma_f32_16x16x32_bf16 v[174:177], a[252:255], v[220:223], v[174:177]\n"
      "v_lshrrev_b32 v211, 16, v211\n"
      "v_fmac_f32 v127, v123, v123\n"
      "v_mfma_f32_16x16x32_bf16 v[206:209], v[240:243], v[148:151], v[206:209]\n"
      "ds_write_b16 v12, v218 offset:16\n"
      "v_mfma_f32_16x16x32_bf16 v[206:209], v[244:247], v[220:223], v[206:209]\n"
      "ds_read_b128 v[130:133], v7 offset:22528\n"
      "v_fmac_f32_e64 v128, v168, v168\n"
      "v_mfma_f32_16x16x32_bf16 v[186:189], a[232:235], v[202:205], v[186:189]\n"
      "s_waitcnt lgkmcnt(0)\n"
      "s_barrier\n"
      "v_bfe_i32 v217, v37, 30, 1\n"
      "v_mfma_f32_16x16x32_bf16 v[186:189], a[236:239], v[180:183], v[186:189]\n"
      "v_mfma_f32_16x16x32_bf16 v[174:177], v[224:227], v[202:205], v[174:177]\n"
      "v_and_b32 v217, 196224, v217\n"
      "v_lshrrev_b32_e64 v100, 16, v100\n"
      "v_mfma_f32_16x16x32_bf16 v[174:177], v[228:231], v[180:183], v[174:177]\n"
      "v_mul_u32_u24 v81, 24, v211\n"
      "v_fmac_f32 v128, v112, v112\n"
      "v_mfma_f32_16x16x32_bf16 v[206:209], v[248:251], v[202:205], v[206:209]\n"
      "v_fmac_f32_e64 v129, v113, v113\n"
      "v_fma_f32 v98, v186, v186, s52\n"
      "v_mfma_f32_16x16x32_bf16 v[206:209], v[252:255], v[180:183], v[206:209]\n"
      "v_fmac_f32 v128, v114, v114\n"
      "v_mfma_f32_16x16x32_bf16 v[202:205], a[216:219], v[108:111], 0\n"
      "s_waitcnt lgkmcnt(0)\n"
      "v_fmac_f32 v126, v122, v122\n"
      "v_fmac_f32 v129, v115, v115\n"
      "v_mfma_f32_16x16x32_bf16 v[202:205], a[220:223], v[156:159], v[202:205]\n"
      "ds_read_b128 v[160:163], v165 offset:2304\n"
      "v_mul_f32_e64 v99, v187, v187\n"
      "v_mfma_f32_16x16x32_bf16 v[144:147], a[240:243], v[108:111], 0\n"
      "ds_read_b128 v[220:223], v2 offset:31232\n"
      "v_mfma_f32_16x16x32_bf16 v[144:147], a[244:247], v[156:159], v[144:147]\n"
      "v_fmac_f32 v99, v189, v189\n"
      "v_fmac_f32 v126, v170, v170\n"
      "v_mfma_f32_16x16x32_bf16 v[148:151], v[232:235], v[108:111], 0\n"
      "ds_read_b128 v[180:183], v165 offset:10496\n"
      "v_fmac_f32_e64 v127, v171, v171\n"
      "v_mfma_f32_16x16x32_bf16 v[148:151], v[236:239], v[156:159], v[148:151]\n"
      "ds_read_b128 v[156:159], v165 offset:4352\n"
      "v_fmac_f32 v127, v173, v173\n"
      "v_mfma_f32_16x16x32_bf16 v[202:205], a[224:227], v[212:215], v[202:205]\n"
      "ds_read_b128 v[152:155], v165 offset:6400\n"
      "v_fmac_f32 v98, v188, v188\n"
      "v_mfma_f32_16x16x32_bf16 v[202:205], a[228:231], v[198:201], v[202:205]\n"
      "v_fmac_f32 v129, v117, v117\n"
      "v_fmac_f32 v98, v174, v174\n"
      "v_mfma_f32_16x16x32_bf16 v[144:147], a[248:251], v[212:215], v[144:147]\n"
      "s_waitcnt lgkmcnt(3)\n"
      "v_sub_u32 v78, v78, v217\n"
      "v_sub_u32 v81, v134, v81\n"
      "v_mfma_f32_16x16x32_bf16 v[144:147], a[252:255], v[198:201], v[144:147]\n"
      "ds_read_b128 v[216:219], v7 offset:34816\n"
      "v_mul_u32_u24 v101, 24, v100\n"
      "v_mfma_f32_16x16x32_bf16 v[148:151], v[240:243], v[212:215], v[148:151]\n"
      "buffer_store_dwordx4 v[130:133], v21, s[24:27], s46 offen sc1\n"
      "v_mfma_f32_16x16x32_bf16 v[148:151], v[244:247], v[198:201], v[148:151]\n"
      "s_nop 0\n"
      "ds_read_b128 v[130:133], v165 offset:256\n"
      "v_and_b32 v134, 7, v211\n"
      "v_mfma_f32_16x16x32_bf16 v[202:205], a[232:235], v[220:223], v[202:205]\n"
      "v_fmac_f32 v99, v175, v175\n"
      "v_fmac_f32 v99, v177, v177\n"
      "v_mfma_f32_16x16x32_bf16 v[202:205], a[236:239], v[136:139], v[202:205]\n"
      "v_xor_b32 v81, v81, v134\n"
      "v_fmac_f32 v126, v172, v172\n"
      "v_mfma_f32_16x16x32_bf16 v[144:147], v[224:227], v[220:223], v[144:147]\n"
      "v_fmac_f32 v98, v176, v176\n"
      "v_fmac_f32 v98, v206, v206\n"
      "v_mfma_f32_16x16x32_bf16 v[144:147], v[228:231], v[136:139], v[144:147]\n"
      "ds_read_b128 v[108:111], v165 offset:8448\n"
      "v_add_f32 v126, v126, v127\n"
      "v_mfma_f32_16x16x32_bf16 v[148:151], v[248:251], v[220:223], v[148:151]\n"
      "v_sub_u32 v101, v210, v101\n"
      "v_fma_f32 v220, v202, v202, s52\n"
      "v_mfma_f32_16x16x32_bf16 v[148:151], v[252:255], v[136:139], v[148:151]\n"
      "s_waitcnt lgkmcnt(1)\n"
      "v_mul_f32 v221, v203, v203\n"
      "v_fmac_f32 v220, v204, v204\n"
      "v_mfma_f32_16x16x32_bf16 v[136:139], a[216:219], v[130:133], 0\n"
      "v_and_b32 v210, 7, v100\n"
      "v_fmac_f32_e64 v221, v205, v205\n"
      "v_mfma_f32_16x16x32_bf16 v[136:139], a[220:223], v[160:163], v[136:139]\n"
      "v_fmac_f32 v99, v207, v207\n"
      "v_fmac_f32 v128, v116, v116\n"
      "v_mfma_f32_16x16x32_bf16 v[198:201], a[240:243], v[130:133], 0\n"
      "v_fmac_f32 v98, v208, v208\n"
      "v_fmac_f32 v221, v145, v145\n"
      "v_mfma_f32_16x16x32_bf16 v[198:201], a[244:247], v[160:163], v[198:201]\n"
      "v_fmac_f32 v128, v118, v118\n"
      "v_fmac_f32 v99, v209, v209\n"
      "v_mfma_f32_16x16x32_bf16 v[212:215], v[232:235], v[130:133], 0\n"
      "v_fmac_f32_e64 v220, v144, v144\n"
      "v_lshl_add_u32 v100, v100, 2, 12\n"
      "v_mfma_f32_16x16x32_bf16 v[212:215], v[236:239], v[160:163], v[212:215]\n"
      "v_fmac_f32_e64 v221, v147, v147\n"
      "v_lshl_add_u32 v211, v211, 2, 12\n"
      "v_mfma_f32_16x16x32_bf16 v[136:139], a[224:227], v[156:159], v[136:139]\n"
      "ds_bpermute_b32 v134, v211, v78\n"
      "v_fmac_f32_e64 v220, v146, v146\n"
      "v_mfma_f32_16x16x32_bf16 v[136:139], a[228:231], v[152:155], v[136:139]\n"
      "v_fmac_f32_e64 v221, v149, v149\n"
      "v_fmac_f32_e64 v129, v119, v119\n"
      "v_mfma_f32_16x16x32_bf16 v[198:201], a[248:251], v[156:159], v[198:201]\n"
      "v_fmac_f32_e64 v220, v148, v148\n"
      "v_add_f32 v98, v98, v99\n"
      "v_mfma_f32_16x16x32_bf16 v[198:201], a[252:255], v[152:155], v[198:201]\n"
      "buffer_store_dwordx4 v[104:107], v22, s[24:27], s46 offen sc1\n"
      "v_mfma_f32_16x16x32_bf16 v[212:215], v[240:243], v[156:159], v[212:215]\n"
      "s_waitcnt lgkmcnt(1)\n"
      "v_mfma_f32_16x16x32_bf16 v[212:215], v[244:247], v[152:155], v[212:215]\n"
      "ds_read_b128 v[130:133], v7 offset:30720\n"
      "v_xor_b32 v101, v101, v210\n"
      "v_mfma_f32_16x16x32_bf16 v[136:139], a[232:235], v[108:111], v[136:139]\n"
      "v_add_f32 v128, v128, v129\n"
      "v_fmac_f32 v220, v150, v150\n"
      "v_mfma_f32_16x16x32_bf16 v[136:139], a[236:239], v[180:183], v[136:139]\n"
      "v_permlane16_swap_b32 v128, v98\n"
      "v_mfma_f32_16x16x32_bf16 v[198:201], v[224:227], v[108:111], v[198:201]\n"
      "v_fmac_f32 v221, v151, v151\n"
      "v_add_f32 v220, v220, v221\n"
      "v_mfma_f32_16x16x32_bf16 v[198:201], v[228:231], v[180:183], v[198:201]\n"
      "ds_bpermute_b32 v99, v100, v78\n"
      "v_add_f32 v128, v128, v98\n"
      "v_mfma_f32_16x16x32_bf16 v[212:215], v[248:251], v[108:111], v[212:215]\n"
      "v_add_u32 v12, 32, v12\n"
      "v_mov_b32 v98, v220\n"
      "v_mfma_f32_16x16x32_bf16 v[212:215], v[252:255], v[180:183], v[212:215]\n"
      "s_nop 0\n"
      "v_permlane16_swap_b32 v220, v98\n"
      "v_mul_f32 v183, v137, v137\n"
      "v_fma_f32 v182, v136, v136, s52\n"
      "v_add_f32_e64 v220, v220, v98\n"
      "v_pk_fma_f32 v[182:183], v[138:139], v[138:139], v[182:183]\n"
      "v_pk_fma_f32 v[182:183], v[198:199], v[198:199], v[182:183]\n"
      "v_pk_fma_f32 v[182:183], v[200:201], v[200:201], v[182:183]\n"
      "v_pk_fma_f32 v[182:183], v[212:213], v[212:213], v[182:183]\n"
      "v_pk_fma_f32 v[182:183], v[214:215], v[214:215], v[182:183]\n"
      "v_add_f32 v182, v182, v183\n"
      "s_waitcnt lgkmcnt(1)\n"
      "buffer_store_dwordx4 v[130:133], v23, s[24:27], s46 offen sc1\n"
      "v_permlane16_swap_b32 v126, v182\n"
      "v_add_f32 v126, v126, v182\n"
      "v_mov_b32 v182, v126\n"
      "v_permlane32_swap_b32 v128, v220\n"
      "v_add_f32 v128, v128, v220\n"
      "v_permlane32_swap_b32 v126, v182\n"
      "v_add_f32 v126, v126, v182\n"
      "ds_write_b32 v31, v126 offset:4608\n"
      "ds_write_b32 v31, v128\n"
      "buffer_store_dwordx4 v[216:219], v25, s[24:27], s46 offen sc1\n"
      "s_waitcnt lgkmcnt(2)\n"
      "v_lshl_add_u32 v99, v101, 4, v99\n"
      "v_readlane_b32 s46, v37, s42\n"
      "v_lshl_add_u32 v134, v81, 4, v134\n"
      "s_or_b32 s95, s46, s48\n"
      "s_cmp_ge_u32 s95, 0x40000000\n"
      "s_cbranch_scc1 L_rare_35_%=\n"
      "L_back_36_%=:\n"
      "s_add_u32 s42, s42, 1\n"
      "s_waitcnt lgkmcnt(0)\n"
      "s_barrier\n"
      "ds_read_b128 v[156:159], v7\n"
      "ds_read_b128 v[216:219], v9\n"
      "ds_read_b128 v[130:133], v7 offset:4096\n"
      "ds_read_b128 v[180:183], v9 offset:4608\n"
      "ds_read_b128 v[152:155], v7 offset:12288\n"
      "ds_read_b128 v[106:109], v7 offset:8192\n"
      "ds_read_b128 v[126:129], v7 offset:16384\n"
      "s_waitcnt lgkmcnt(6)\n"
      "buffer_store_dwordx4 v[156:159], v20, s[24:27], s46 offen sc1\n"
      "s_waitcnt lgkmcnt(4)\n"
      "buffer_store_dwordx4 v[130:133], v21, s[24:27], s46 offen sc1\n"
      "s_waitcnt lgkmcnt(2)\n"
      "v_add_f32 v182, v182, v183\n"
      "v_add_f32 v216, v216, v217\n"
      "v_add_f32 v218, v218, v219\n"
      "v_add_f32 v216, v216, v218\n"
      "v_add_f32 v180, v180, v181\n"
      "v_add_f32 v180, v180, v182\n"
      "v_rsq_f32 v216, v216\n"
      "v_rsq_f32_e64 v180, v180\n"
      "v_mul_f32 v216, 0x415db3d7, v216\n"
      "v_mov_b32 v218, v216\n"
      "v_mul_f32 v180, 0x415db3d7, v180\n"
      "v_mov_b32 v182, v180\n"
      "v_permlane32_swap_b32 v216, v218\n"
      "v_mov_b32 v217, v216\n"
      "v_pk_mul_f32 v[148:149], v[148:149], v[218:219] op_sel_hi:[1,0]\n"
      "v_permlane32_swap_b32 v180, v182\n"
      "v_permlane16_swap_b32 v216, v217\n"
      "v_pk_mul_f32 v[166:167], v[166:167], v[216:217] op_sel_hi:[1,0]\n"
      "v_pk_mul_f32 v[206:207], v[206:207], v[216:217] op_sel:[0,1]\n"
      "v_pk_mul_f32 v[144:145], v[144:145], v[218:219] op_sel_hi:[1,0]\n"
      "v_mov_b32_e64 v181, v180\n"
      "v_pk_mul_f32 v[202:203], v[202:203], v[218:219] op_sel_hi:[1,0]\n"
      "v_pk_mul_f32 v[204:205], v[204:205], v[218:219] op_sel_hi:[1,0]\n"
      "v_pk_mul_f32 v[112:113], v[112:113], v[216:217] op_sel_hi:[1,0]\n"
      "v_permlane16_swap_b32_e64 v180, v181\n"
      "v_cvt_pk_bf16_f32 v206, v206, v207\n"
      "v_pk_mul_f32 v[118:119], v[118:119], v[216:217] op_sel_hi:[1,0]\n"
      "v_pk_mul_f32 v[186:187], v[186:187], v[216:217] op_sel:[0,1]\n"
      "v_pk_mul_f32 v[188:189], v[188:189], v[216:217] op_sel:[0,1]\n"
      "v_pk_mul_f32 v[170:171], v[170:171], v[180:181] op_sel_hi:[1,0]\n"
      "v_cvt_pk_bf16_f32 v148, v148, v149\n"
      "v_pk_mul_f32 v[116:117], v[116:117], v[216:217] op_sel_hi:[1,0]\n"
      "v_pk_mul_f32 v[208:209], v[208:209], v[216:217] op_sel:[0,1]\n"
      "v_cvt_pk_bf16_f32 v166, v166, v167\n"
      "v_pk_mul_f32 v[168:169], v[168:169], v[216:217] op_sel_hi:[1,0]\n"
      "v_cvt_pk_bf16_f32 v186, v186, v187\n"
      "v_pk_mul_f32 v[176:177], v[176:177], v[216:217] op_sel:[0,1]\n"
      "v_pk_mul_f32 v[138:139], v[138:139], v[180:181] op_sel:[0,1]\n"
      "v_pk_mul_f32 v[122:123], v[122:123], v[180:181] op_sel_hi:[1,0]\n"
      "v_pk_mul_f32 v[194:195], v[194:195], v[180:181] op_sel_hi:[1,0]\n"
      "v_pk_mul_f32 v[212:213], v[212:213], v[180:181] op_sel:[0,1]\n"
      "v_pk_mul_f32 v[120:121], v[120:121], v[180:181] op_sel_hi:[1,0]\n"
      "v_pk_mul_f32 v[196:197], v[196:197], v[180:181] op_sel_hi:[1,0]\n"
      "v_pk_mul_f32 v[200:201], v[200:201], v[180:181] op_sel:[0,1]\n"
      "v_cvt_pk_bf16_f32 v167, v168, v169\n"
      "v_pk_mul_f32 v[150:151], v[150:151], v[218:219] op_sel_hi:[1,0]\n"
      "v_pk_mul_f32 v[136:137], v[136:137], v[180:181] op_sel:[0,1]\n"
      "v_cvt_pk_bf16_f32 v202, v202, v203\n"
      "v_pk_mul_f32 v[198:199], v[198:199], v[180:181] op_sel:[0,1]\n"
      "v_cvt_pk_bf16_f32 v212, v212, v213\n"
      "v_cvt_pk_bf16_f32 v194, v194, v195\n"
      "v_pk_mul_f32 v[214:215], v[214:215], v[180:181] op_sel:[0,1]\n"
      "v_cvt_pk_bf16_f32 v195, v196, v197\n"
      "v_pk_mul_f32 v[172:173], v[172:173], v[180:181] op_sel_hi:[1,0]\n"
      "v_cvt_pk_bf16_f32 v116, v116, v117\n"
      "v_pk_mul_f32 v[174:175], v[174:175], v[216:217] op_sel:[0,1]\n"
      "v_cvt_pk_bf16_f32 v196, v120, v121\n"
      "v_cvt_pk_bf16_f32 v213, v214, v215\n"
      "v_cvt_pk_bf16_f32 v187, v188, v189\n"
      "v_pk_mul_f32 v[114:115], v[114:115], v[216:217] op_sel_hi:[1,0]\n"
      "v_cvt_pk_bf16_f32 v170, v170, v171\n"
      "v_pk_mul_f32 v[146:147], v[146:147], v[218:219] op_sel_hi:[1,0]\n"
      "v_cvt_pk_bf16_f32 v136, v136, v137\n"
      "v_cvt_pk_bf16_f32 v169, v114, v115\n"
      "v_cvt_pk_bf16_f32 v188, v174, v175\n"
      "v_cvt_pk_bf16_f32 v197, v122, v123\n"
      "v_cvt_pk_bf16_f32 v203, v204, v205\n"
      "v_cvt_pk_bf16_f32 v189, v176, v177\n"
      "v_cvt_pk_bf16_f32 v137, v138, v139\n"
      "v_cvt_pk_bf16_f32 v149, v150, v151\n"
      "v_cvt_pk_bf16_f32 v207, v208, v209\n"
      "v_cvt_pk_bf16_f32 v117, v118, v119\n"
      "v_cvt_pk_bf16_f32 v171, v172, v173\n"
      "v_cvt_pk_bf16_f32 v168, v112, v113\n"
      "v_cvt_pk_bf16_f32 v204, v144, v145\n"
      "v_cvt_pk_bf16_f32 v205, v146, v147\n"
      "v_cvt_pk_bf16_f32 v139, v200, v201\n"
      "v_cvt_pk_bf16_f32 v138, v198, v199\n"
      "s_waitcnt lgkmcnt(0)\n"
      "buffer_store_dwordx4 v[106:109], v22, s[24:27], s46 offen sc1\n"
      "ds_write_b128 v5, v[186:189] offset:24576\n"
      "ds_write_b64 v6, v[206:207] offset:24576\n"
      "ds_write_b128 v5, v[202:205] offset:30720\n"
      "ds_write_b128 v141, v[194:197]\n"
      "ds_write_b128 v5, v[166:169] offset:18432\n"
      "ds_write_b64 v6, v[116:117] offset:18432\n"
      "ds_write_b64 v84, v[212:213] offset:6144\n"
      "ds_write_b128 v141, v[136:139] offset:6144\n"
      "ds_write_b64 v6, v[148:149] offset:30720\n"
      "ds_write_b64 v84, v[170:171]\n"
      "buffer_store_dwordx4 v[152:155], v23, s[24:27], s46 offen sc1\n"
      "buffer_store_dwordx4 v[126:129], v25, s[24:27], s46 offen sc1\n"
      "v_readlane_b32 s46, v37, s42\n"
      "s_add_u32 s42, s42, 1\n"
      "s_or_b32 s95, s46, s48\n"
      "s_cmp_ge_u32 s95, 0x40000000\n"
      "s_cbranch_scc1 L_rare_39_%=\n"
      "L_back_40_%=:\n"
      "s_waitcnt lgkmcnt(0)\n"
      "s_barrier\n"
      "ds_read_b128 v[152:155], v7 offset:18432\n"
      "ds_read_b128 v[214:217], v7 offset:22528\n"
      "ds_read_b128 v[196:199], v7 offset:26624\n"
      "ds_read_b128 v[112:115], v7 offset:30720\n"
      "ds_read_b128 v[130:133], v7 offset:34816\n"
      "ds_read_b128 v[166:169], v140\n"
      "ds_read_b128 v[126:129], v140 offset:1024\n"
      "s_waitcnt lgkmcnt(6)\n"
      "buffer_store_dwordx4 v[152:155], v20, s[24:27], s46 offen sc1\n"
      "s_waitcnt lgkmcnt(5)\n"
      "buffer_store_dwordx4 v[214:217], v21, s[24:27], s46 offen sc1\n"
      "s_waitcnt lgkmcnt(4)\n"
      "buffer_store_dwordx4 v[196:199], v22, s[24:27], s46 offen sc1\n"
      "s_waitcnt lgkmcnt(3)\n"
      "buffer_store_dwordx4 v[112:115], v23, s[24:27], s46 offen sc1\n"
      "s_waitcnt lgkmcnt(2)\n"
      "buffer_store_dwordx4 v[130:133], v25, s[24:27], s46 offen sc1\n"
      "s_waitcnt lgkmcnt(1)\n"
      "buffer_store_dwordx4 v[166:169], v134, s[24:27], 0 offen sc1\n"
      "s_waitcnt lgkmcnt(0)\n"
      "buffer_store_dwordx4 v[126:129], v99, s[24:27], 0 offen sc1\n"
      "s_endpgm\n"
      "L_rare_1_%=:\n"
      "s_nop 7\n"
      "s_waitcnt lgkmcnt(0)\n"
      "s_add_u32 s72, s42, 1\n"
      "v_readlane_b32 s77, v38, s72\n"
      "v_subrev_u32 v166, 115456, v9\n"
      "s_and_b32 s73, s77, -16\n"
      "s_mov_b32 m0, 110592\n"
      "s_nop 0\n"
      "buffer_load_dwordx4 v166, s[28:31], s73 offen  lds\n"
      "s_add_u32 s73, s73, 1024\n"
      "s_mov_b32 m0, 111616\n"
      "s_nop 0\n"
      "buffer_load_dwordx4 v166, s[28:31], s73 offen  lds\n"
      "s_add_u32 s73, s73, 1024\n"
      "s_mov_b32 m0, 112640\n"
      "s_nop 0\n"
      "buffer_load_dwordx4 v166, s[28:31], s73 offen  lds\n"
      "s_add_u32 s73, s73, 1024\n"
      "s_mov_b32 m0, 113664\n"
      "s_nop 0\n"
      "buffer_load_dwordx4 v166, s[28:31], s73 offen  lds\n"
      "s_add_u32 s73, s73, 1024\n"
      "s_mov_b32 m0, 114688\n"
      "s_mov_b32 exec_hi, 0xffff\n"
      "buffer_load_dwordx4 v166, s[28:31], s73 offen  lds\n"
      "s_mov_b32 exec_hi, -1\n"
      "s_add_u32 s73, s73, 1024\n"
      "s_and_b32 s77, s77, 15\n"
      "s_waitcnt lgkmcnt(0)\n"
      "s_nop 4\n"
      "s_branch L_back_2_%=\n"
      "L_rare_3_%=:\n"
      "s_nop 7\n"
      "s_waitcnt lgkmcnt(0)\n"
      "s_waitcnt vmcnt(0)\n"
      "v_subrev_u32 v139, 115456, v9\n"
      "v_bfe_u32 v214, v139, 4, 4\n"
      "v_mul_u32_u24 v214, 98, v214\n"
      "v_lshrrev_b32 v139, 8, v139\n"
      "v_lshl_add_u32 v214, v139, 3, v214\n"
      "v_add_u32 v214, s77, v214\n"
      "v_and_b32 v130, 2, v214\n"
      "v_and_b32_e64 v214, -4, v214\n"
      "v_add_u32 v214, 110592, v214\n"
      "ds_read2_b32 v[222:223], v214 offset0:0 offset1:1\n"
      "ds_read2_b32 v[220:221], v214 offset0:8 offset1:9\n"
      "ds_read2_b32 v[112:113], v214 offset0:16 offset1:17\n"
      "ds_read2_b32 v[102:103], v214 offset0:2 offset1:10\n"
      "ds_read2_b32 v[100:101], v214 offset0:18 offset1:24\n"
      "s_waitcnt lgkmcnt(4)\n"
      "v_alignbyte_b32 v222, v223, v222, v130\n"
      "s_waitcnt lgkmcnt(1)\n"
      "v_alignbyte_b32 v223, v102, v223, v130\n"
      "v_lshlrev_b32 v139, 16, v222\n"
      "v_fmac_f32 v210, s53, v139\n"
      "v_and_b32 v139, 0xffff0000, v222\n"
      "v_fmac_f32 v211, s53, v139\n"
      "v_lshlrev_b32 v139, 16, v223\n"
      "v_fmac_f32_e64 v212, s53, v139\n"
      "v_and_b32 v139, 0xffff0000, v223\n"
      "v_fmac_f32 v213, s53, v139\n"
      "v_alignbyte_b32 v220, v221, v220, v130\n"
      "v_alignbyte_b32 v221, v103, v221, v130\n"
      "v_lshlrev_b32 v139, 16, v220\n"
      "v_fmac_f32 v176, s53, v139\n"
      "v_and_b32 v139, 0xffff0000, v220\n"
      "v_fmac_f32 v177, s53, v139\n"
      "v_lshlrev_b32 v139, 16, v221\n"
      "v_fmac_f32 v178, s53, v139\n"
      "v_and_b32 v139, 0xffff0000, v221\n"
      "v_fmac_f32 v179, s53, v139\n"
      "v_alignbyte_b32 v112, v113, v112, v130\n"
      "s_waitcnt lgkmcnt(0)\n"
      "v_alignbyte_b32 v113, v100, v113, v130\n"
      "v_lshlrev_b32 v139, 16, v112\n"
      "v_fmac_f32 v116, s53, v139\n"
      "v_and_b32 v139, 0xffff0000, v112\n"
      "v_fmac_f32 v117, s53, v139\n"
      "v_lshlrev_b32 v139, 16, v113\n"
      "v_fmac_f32 v118, s53, v139\n"
      "v_and_b32 v139, 0xffff0000, v113\n"
      "v_fmac_f32 v119, s53, v139\n"
      "v_alignbyte_b32 v139, v101, v101, v130\n"
      "v_lshlrev_b32 v139, 16, v139\n"
      "v_fmac_f32 v198, s53, v139\n"
      "v_max3_f32 v214, v210, v211, v212\n"
      "v_max3_f32 v130, v213, v176, v177\n"
      "v_max3_f32 v214, v214, v178, v179\n"
      "v_max3_f32 v130, v130, v116, v117\n"
      "v_max3_f32 v214, v214, v118, v119\n"
      "v_max3_f32 v214, v214, v130, v198\n"
      "v_mov_b32 v139, v214\n"
      "s_nop 1\n"
      "v_permlane16_swap_b32 v214, v139\n"
      "v_max_f32 v214, v214, v139\n"
      "v_mov_b32 v139, v214\n"
      "s_nop 1\n"
      "v_permlane32_swap_b32 v214, v139\n"
      "v_max_f32 v214, v214, v139\n"
      "v_sub_f32 v210, v210, v214\n"
      "v_sub_f32 v211, v211, v214\n"
      "v_sub_f32 v212, v212, v214\n"
      "v_sub_f32 v213, v213, v214\n"
      "v_sub_f32 v176, v176, v214\n"
      "v_sub_f32 v177, v177, v214\n"
      "v_sub_f32 v178, v178, v214\n"
      "v_sub_f32 v179, v179, v214\n"
      "v_sub_f32 v116, v116, v214\n"
      "v_sub_f32 v117, v117, v214\n"
      "v_sub_f32 v118, v118, v214\n"
      "v_sub_f32 v119, v119, v214\n"
      "v_sub_f32_e64 v198, v198, v214\n"
      "s_waitcnt lgkmcnt(0)\n"
      "s_nop 4\n"
      "s_branch L_back_4_%=\n"
      "L_rare_5_%=:\n"
      "s_nop 7\n"
      "s_waitcnt lgkmcnt(0)\n"
      "s_waitcnt vmcnt(0)\n"
      "v_subrev_u32 v96, 115456, v9\n"
      "v_bfe_u32 v151, v96, 4, 4\n"
      "v_mul_u32_u24 v151, 98, v151\n"
      "v_lshrrev_b32 v96, 8, v96\n"
      "v_lshl_add_u32 v151, v96, 3, v151\n"
      "v_add_u32 v151, s77, v151\n"
      "v_and_b32 v150, 2, v151\n"
      "v_and_b32 v151, -4, v151\n"
      "v_add_u32 v151, 112160, v151\n"
      "ds_read2_b32 v[138:139], v151 offset0:0 offset1:1\n"
      "ds_read2_b32 v[140:141], v151 offset0:8 offset1:9\n"
      "ds_read2_b32 v[148:149], v151 offset0:16 offset1:17\n"
      "ds_read2_b32 v[146:147], v151 offset0:2 offset1:10\n"
      "ds_read2_b32 v[116:117], v151 offset0:18 offset1:24\n"
      "s_waitcnt lgkmcnt(4)\n"
      "v_alignbyte_b32 v138, v139, v138, v150\n"
      "s_waitcnt lgkmcnt(1)\n"
      "v_alignbyte_b32 v139, v146, v139, v150\n"
      "v_lshlrev_b32 v96, 16, v138\n"
      "v_fmac_f32 v110, s53, v96\n"
      "v_and_b32 v96, 0xffff0000, v138\n"
      "v_fmac_f32 v111, s53, v96\n"
      "v_lshlrev_b32 v96, 16, v139\n"
      "v_fmac_f32 v112, s53, v96\n"
      "v_and_b32 v96, 0xffff0000, v139\n"
      "v_fmac_f32 v113, s53, v96\n"
      "v_alignbyte_b32 v140, v141, v140, v150\n"
      "v_alignbyte_b32 v141, v147, v141, v150\n"
      "v_lshlrev_b32 v96, 16, v140\n"
      "v_fmac_f32 v166, s53, v96\n"
      "v_and_b32 v96, 0xffff0000, v140\n"
      "v_fmac_f32 v167, s53, v96\n"
      "v_lshlrev_b32 v96, 16, v141\n"
      "v_fmac_f32_e64 v168, s53, v96\n"
      "v_and_b32 v96, 0xffff0000, v141\n"
      "v_fmac_f32 v169, s53, v96\n"
      "v_alignbyte_b32 v148, v149, v148, v150\n"
      "s_waitcnt lgkmcnt(0)\n"
      "v_alignbyte_b32 v149, v116, v149, v150\n"
      "v_lshlrev_b32 v96, 16, v148\n"
      "v_fmac_f32 v100, s53, v96\n"
      "v_and_b32 v96, 0xffff0000, v148\n"
      "v_fmac_f32 v101, s53, v96\n"
      "v_lshlrev_b32 v96, 16, v149\n"
      "v_fmac_f32_e64 v102, s53, v96\n"
      "v_and_b32 v96, 0xffff0000, v149\n"
      "v_fmac_f32 v103, s53, v96\n"
      "v_alignbyte_b32 v96, v117, v117, v150\n"
      "v_lshlrev_b32 v96, 16, v96\n"
      "v_fmac_f32_e64 v220, s53, v96\n"
      "v_max3_f32 v151, v110, v111, v112\n"
      "v_max3_f32 v150, v113, v166, v167\n"
      "v_max3_f32 v151, v151, v168, v169\n"
      "v_max3_f32 v150, v150, v100, v101\n"
      "v_max3_f32 v151, v151, v102, v103\n"
      "v_max3_f32 v151, v151, v150, v220\n"
      "v_mov_b32 v96, v151\n"
      "s_nop 1\n"
      "v_permlane16_swap_b32 v151, v96\n"
      "v_max_f32 v151, v151, v96\n"
      "v_mov_b32 v96, v151\n"
      "s_nop 1\n"
      "v_permlane32_swap_b32 v151, v96\n"
      "v_max_f32 v151, v151, v96\n"
      "v_sub_f32 v110, v110, v151\n"
      "v_sub_f32 v111, v111, v151\n"
      "v_sub_f32 v112, v112, v151\n"
      "v_sub_f32 v113, v113, v151\n"
      "v_sub_f32 v166, v166, v151\n"
      "v_sub_f32 v167, v167, v151\n"
      "v_sub_f32 v168, v168, v151\n"
      "v_sub_f32 v169, v169, v151\n"
      "v_sub_f32 v100, v100, v151\n"
      "v_sub_f32 v101, v101, v151\n"
      "v_sub_f32 v102, v102, v151\n"
      "v_sub_f32 v103, v103, v151\n"
      "v_sub_f32_e64 v220, v220, v151\n"
      "s_waitcnt lgkmcnt(0)\n"
      "s_nop 4\n"
      "s_branch L_back_6_%=\n"
      "L_rare_7_%=:\n"
      "s_nop 7\n"
      "s_waitcnt lgkmcnt(0)\n"
      "s_waitcnt vmcnt(0)\n"
      "v_subrev_u32 v122, 115456, v9\n"
      "v_bfe_u32 v185, v122, 4, 4\n"
      "v_mul_u32_u24 v185, 98, v185\n"
      "v_lshrrev_b32_e64 v122, 8, v122\n"
      "v_lshl_add_u32 v185, v122, 3, v185\n"
      "v_add_u32 v185, s77, v185\n"
      "v_and_b32 v184, 2, v185\n"
      "v_and_b32_e64 v185, -4, v185\n"
      "v_add_u32 v185, 113728, v185\n"
      "ds_read2_b32 v[148:149], v185 offset0:0 offset1:1\n"
      "ds_read2_b32 v[146:147], v185 offset0:8 offset1:9\n"
      "ds_read2_b32 v[154:155], v185 offset0:16 offset1:17\n"
      "ds_read2_b32 v[152:153], v185 offset0:2 offset1:10\n"
      "ds_read2_b32 v[116:117], v185 offset0:18 offset1:24\n"
      "s_waitcnt lgkmcnt(4)\n"
      "v_alignbyte_b32 v148, v149, v148, v184\n"
      "s_waitcnt lgkmcnt(1)\n"
      "v_alignbyte_b32 v149, v152, v149, v184\n"
      "v_lshlrev_b32 v122, 16, v148\n"
      "v_fmac_f32 v220, s53, v122\n"
      "v_and_b32 v122, 0xffff0000, v148\n"
      "v_fmac_f32 v221, s53, v122\n"
      "v_lshlrev_b32 v122, 16, v149\n"
      "v_fmac_f32_e64 v222, s53, v122\n"
      "v_and_b32 v122, 0xffff0000, v149\n"
      "v_fmac_f32 v223, s53, v122\n"
      "v_alignbyte_b32 v146, v147, v146, v184\n"
      "v_alignbyte_b32 v147, v153, v147, v184\n"
      "v_lshlrev_b32 v122, 16, v146\n"
      "v_fmac_f32 v138, s53, v122\n"
      "v_and_b32 v122, 0xffff0000, v146\n"
      "v_fmac_f32 v139, s53, v122\n"
      "v_lshlrev_b32 v122, 16, v147\n"
      "v_fmac_f32 v140, s53, v122\n"
      "v_and_b32 v122, 0xffff0000, v147\n"
      "v_fmac_f32 v141, s53, v122\n"
      "v_alignbyte_b32 v154, v155, v154, v184\n"
      "s_waitcnt lgkmcnt(0)\n"
      "v_alignbyte_b32 v155, v116, v155, v184\n"
      "v_lshlrev_b32 v122, 16, v154\n"
      "v_fmac_f32 v96, s53, v122\n"
      "v_and_b32 v122, 0xffff0000, v154\n"
      "v_fmac_f32 v97, s53, v122\n"
      "v_lshlrev_b32 v122, 16, v155\n"
      "v_fmac_f32 v98, s53, v122\n"
      "v_and_b32 v122, 0xffff0000, v155\n"
      "v_fmac_f32 v99, s53, v122\n"
      "v_alignbyte_b32 v122, v117, v117, v184\n"
      "v_lshlrev_b32 v122, 16, v122\n"
      "v_fmac_f32 v114, s53, v122\n"
      "v_max3_f32 v185, v220, v221, v222\n"
      "v_max3_f32 v184, v223, v138, v139\n"
      "v_max3_f32 v185, v185, v140, v141\n"
      "v_max3_f32 v184, v184, v96, v97\n"
      "v_max3_f32 v185, v185, v98, v99\n"
      "v_max3_f32 v185, v185, v184, v114\n"
      "v_mov_b32 v122, v185\n"
      "s_nop 1\n"
      "v_permlane16_swap_b32 v185, v122\n"
      "v_max_f32 v185, v185, v122\n"
      "v_mov_b32 v122, v185\n"
      "s_nop 1\n"
      "v_permlane32_swap_b32 v185, v122\n"
      "v_max_f32 v185, v185, v122\n"
      "v_sub_f32 v220, v220, v185\n"
      "v_sub_f32 v221, v221, v185\n"
      "v_sub_f32 v222, v222, v185\n"
      "v_sub_f32 v223, v223, v185\n"
      "v_sub_f32 v138, v138, v185\n"
      "v_sub_f32 v139, v139, v185\n"
      "v_sub_f32 v140, v140, v185\n"
      "v_sub_f32 v141, v141, v185\n"
      "v_sub_f32 v96, v96, v185\n"
      "v_sub_f32 v97, v97, v185\n"
      "v_sub_f32 v98, v98, v185\n"
      "v_sub_f32 v99, v99, v185\n"
      "v_sub_f32 v114, v114, v185\n"
      "s_waitcnt lgkmcnt(0)\n"
      "s_nop 4\n"
      "s_branch L_back_8_%=\n"
      "L_rare_9_%=:\n"
      "s_nop 7\n"
      "s_waitcnt lgkmcnt(0)\n"
      "s_waitcnt vmcnt(0)\n"
      "v_subrev_u32 v138, 115456, v9\n"
      "v_lshrrev_b32 v138, 3, v138\n"
      "v_min_u32 v138, 96, v138\n"
      "v_add_u32 v138, s77, v138\n"
      "v_add_u32 v138, 115296, v138\n"
      "ds_read_u16 v138, v138\n"
      "s_waitcnt lgkmcnt(0)\n"
      "v_lshlrev_b32_e64 v138, 16, v138\n"
      "v_fmac_f32 v176, s53, v138\n"
      "s_nop 1\n"
      "v_max_f32_dpp v139, v176, v176 row_ror:8 row_mask:0xf bank_mask:0xf\n"
      "s_nop 1\n"
      "v_max_f32_dpp v139, v139, v139 row_ror:4 row_mask:0xf bank_mask:0xf\n"
      "s_nop 1\n"
      "v_max_f32_dpp v139, v139, v139 row_ror:2 row_mask:0xf bank_mask:0xf\n"
      "s_nop 1\n"
      "v_max_f32_dpp v139, v139, v139 row_ror:1 row_mask:0xf bank_mask:0xf\n"
      "s_nop 1\n"
      "v_max_f32_dpp v139, v139, v139 row_bcast:15 row_mask:0xa bank_mask:0xf\n"
      "s_nop 1\n"
      "v_max_f32_dpp v139, v139, v139 row_bcast:31 row_mask:0xc bank_mask:0xf\n"
      "s_nop 0\n"
      "v_readlane_b32 s72, v139, 63\n"
      "s_nop 1\n"
      "v_subrev_f32 v176, s72, v176\n"
      "s_waitcnt lgkmcnt(0)\n"
      "s_nop 4\n"
      "s_branch L_back_10_%=\n"
      "L_rare_27_%=:\n"
      "s_nop 7\n"
      "s_waitcnt lgkmcnt(0)\n"
      "s_bitcmp1_b32 s85, 31\n"
      "s_cselect_b32 s75, 100270464, 0\n"
      "s_bitcmp1_b32 s85, 30\n"
      "s_cselect_b32 s76, 196224, 0\n"
      "s_and_b32 s74, s85, 0x3fffffff\n"
      "s_cmp_ge_u32 s85, 0x40000000\n"
      "s_cselect_b32 s74, s74, 0\n"
      "s_cselect_b32 s85, 0, s85\n"
      "s_sub_u32 s74, s74, s69\n"
      "s_add_u32 s69, s69, s74\n"
      "s_sub_u32 s75, s75, s70\n"
      "s_add_u32 s70, s70, s75\n"
      "s_sub_u32 s76, s76, s71\n"
      "s_add_u32 s71, s71, s76\n"
      "v_add_u32 v20, s74, v20\n"
      "v_add_u32 v21, s74, v21\n"
      "v_add_u32 v22, s74, v22\n"
      "v_add_u32 v23, s74, v23\n"
      "v_add_u32 v25, s74, v25\n"
      "s_cmp_eq_u32 s75, 0\n"
      "s_cbranch_scc1 L_ax_25_%=\n"
      "v_bfe_i32 v156, v27, 10, 1\n"
      "v_and_b32 v156, s75, v156\n"
      "v_sub_u32 v20, v20, v156\n"
      "v_bfe_i32 v156, v27, 12, 1\n"
      "v_and_b32 v156, s75, v156\n"
      "v_sub_u32 v21, v21, v156\n"
      "v_bfe_i32 v156, v27, 14, 1\n"
      "v_and_b32 v156, s75, v156\n"
      "v_sub_u32 v22, v22, v156\n"
      "v_bfe_i32 v156, v27, 16, 1\n"
      "v_and_b32 v156, s75, v156\n"
      "v_sub_u32 v23, v23, v156\n"
      "v_bfe_i32 v156, v27, 18, 1\n"
      "v_and_b32 v156, s75, v156\n"
      "v_sub_u32 v25, v25, v156\n"
      "L_ax_25_%=:\n"
      "s_cmp_eq_u32 s76, 0\n"
      "s_cbranch_scc1 L_ax_26_%=\n"
      "v_bfe_i32 v156, v27, 11, 1\n"
      "v_and_b32 v156, s76, v156\n"
      "v_sub_u32_e64 v20, v20, v156\n"
      "v_bfe_i32 v156, v27, 13, 1\n"
      "v_and_b32 v156, s76, v156\n"
      "v_sub_u32 v21, v21, v156\n"
      "v_bfe_i32 v156, v27, 15, 1\n"
      "v_and_b32 v156, s76, v156\n"
      "v_sub_u32 v22, v22, v156\n"
      "v_bfe_i32 v156, v27, 17, 1\n"
      "v_and_b32 v156, s76, v156\n"
      "v_sub_u32 v23, v23, v156\n"
      "v_bfe_i32 v156, v27, 19, 1\n"
      "v_and_b32 v156, s76, v156\n"
      "v_sub_u32 v25, v25, v156\n"
      "L_ax_26_%=:\n"
      "s_or_b32 s48, s66, s69\n"
      "s_cmp_lg_u32 s48, 0\n"
      "s_cselect_b32 s48, 0x40000000, 0\n"
      "s_waitcnt lgkmcnt(0)\n"
      "s_nop 4\n"
      "s_branch L_back_28_%=\n"
      "L_rare_31_%=:\n"
      "s_nop 7\n"
      "s_waitcnt lgkmcnt(0)\n"
      "s_bitcmp1_b32 s85, 31\n"
      "s_cselect_b32 s75, 100270464, 0\n"
      "s_bitcmp1_b32 s85, 30\n"
      "s_cselect_b32 s76, 196224, 0\n"
      "s_and_b32 s74, s85, 0x3fffffff\n"
      "s_cmp_ge_u32 s85, 0x40000000\n"
      "s_cselect_b32 s74, s74, 0\n"
      "s_cselect_b32 s85, 0, s85\n"
      "s_sub_u32 s74, s74, s69\n"
      "s_add_u32 s69, s69, s74\n"
      "s_sub_u32 s75, s75, s70\n"
      "s_add_u32 s70, s70, s75\n"
      "s_sub_u32 s76, s76, s71\n"
      "s_add_u32 s71, s71, s76\n"
      "v_add_u32 v20, s74, v20\n"
      "v_add_u32 v21, s74, v21\n"
      "v_add_u32 v22, s74, v22\n"
      "v_add_u32 v23, s74, v23\n"
      "v_add_u32 v25, s74, v25\n"
      "s_cmp_eq_u32 s75, 0\n"
      "s_cbranch_scc1 L_ax_29_%=\n"
      "v_bfe_i32 v214, v27, 10, 1\n"
      "v_and_b32 v214, s75, v214\n"
      "v_sub_u32 v20, v20, v214\n"
      "v_bfe_i32 v214, v27, 12, 1\n"
      "v_and_b32 v214, s75, v214\n"
      "v_sub_u32 v21, v21, v214\n"
      "v_bfe_i32 v214, v27, 14, 1\n"
      "v_and_b32 v214, s75, v214\n"
      "v_sub_u32 v22, v22, v214\n"
      "v_bfe_i32 v214, v27, 16, 1\n"
      "v_and_b32 v214, s75, v214\n"
      "v_sub_u32 v23, v23, v214\n"
      "v_bfe_i32 v214, v27, 18, 1\n"
      "v_and_b32 v214, s75, v214\n"
      "v_sub_u32 v25, v25, v214\n"
      "L_ax_29_%=:\n"
      "s_cmp_eq_u32 s76, 0\n"
      "s_cbranch_scc1 L_ax_30_%=\n"
      "v_bfe_i32 v214, v27, 11, 1\n"
      "v_and_b32 v214, s76, v214\n"
      "v_sub_u32 v20, v20, v214\n"
      "v_bfe_i32 v214, v27, 13, 1\n"
      "v_and_b32 v214, s76, v214\n"
      "v_sub_u32 v21, v21, v214\n"
      "v_bfe_i32 v214, v27, 15, 1\n"
      "v_and_b32 v214, s76, v214\n"
      "v_sub_u32 v22, v22, v214\n"
      "v_bfe_i32 v214, v27, 17, 1\n"
      "v_and_b32 v214, s76, v214\n"
      "v_sub_u32 v23, v23, v214\n"
      "v_bfe_i32 v214, v27, 19, 1\n"
      "v_and_b32 v214, s76, v214\n"
      "v_sub_u32 v25, v25, v214\n"
      "L_ax_30_%=:\n"
      "s_or_b32 s48, s66, s69\n"
      "s_cmp_lg_u32 s48, 0\n"
      "s_cselect_b32 s48, 0x40000000, 0\n"
      "s_waitcnt lgkmcnt(0)\n"
      "s_nop 4\n"
      "s_branch L_back_32_%=\n"
      "L_rare_35_%=:\n"
      "s_nop 7\n"
      "s_waitcnt lgkmcnt(0)\n"
      "s_bitcmp1_b32 s46, 31\n"
      "s_cselect_b32 s75, 100270464, 0\n"
      "s_bitcmp1_b32 s46, 30\n"
      "s_cselect_b32 s76, 196224, 0\n"
      "s_and_b32 s74, s46, 0x3fffffff\n"
      "s_cmp_ge_u32 s46, 0x40000000\n"
      "s_cselect_b32 s74, s74, 0\n"
      "s_cselect_b32 s46, 0, s46\n"
      "s_sub_u32 s74, s74, s69\n"
      "s_add_u32 s69, s69, s74\n"
      "s_sub_u32 s75, s75, s70\n"
      "s_add_u32 s70, s70, s75\n"
      "s_sub_u32 s76, s76, s71\n"
      "s_add_u32 s71, s71, s76\n"
      "v_add_u32 v20, s74, v20\n"
      "v_add_u32 v21, s74, v21\n"
      "v_add_u32 v22, s74, v22\n"
      "v_add_u32 v23, s74, v23\n"
      "v_add_u32 v25, s74, v25\n"
      "s_cmp_eq_u32 s75, 0\n"
      "s_cbranch_scc1 L_ax_33_%=\n"
      "v_bfe_i32 v219, v27, 10, 1\n"
      "v_and_b32 v219, s75, v219\n"
      "v_sub_u32 v20, v20, v219\n"
      "v_bfe_i32 v219, v27, 12, 1\n"
      "v_and_b32 v219, s75, v219\n"
      "v_sub_u32 v21, v21, v219\n"
      "v_bfe_i32 v219, v27, 14, 1\n"
      "v_and_b32 v219, s75, v219\n"
      "v_sub_u32 v22, v22, v219\n"
      "v_bfe_i32 v219, v27, 16, 1\n"
      "v_and_b32 v219, s75, v219\n"
      "v_sub_u32 v23, v23, v219\n"
      "v_bfe_i32 v219, v27, 18, 1\n"
      "v_and_b32 v219, s75, v219\n"
      "v_sub_u32 v25, v25, v219\n"
      "L_ax_33_%=:\n"
      "s_cmp_eq_u32 s76, 0\n"
      "s_cbranch_scc1 L_ax_34_%=\n"
      "v_bfe_i32 v219, v27, 11, 1\n"
      "v_and_b32 v219, s76, v219\n"
      "v_sub_u32 v20, v20, v219\n"
      "v_bfe_i32 v219, v27, 13, 1\n"
      "v_and_b32 v219, s76, v219\n"
      "v_sub_u32 v21, v21, v219\n"
      "v_bfe_i32 v219, v27, 15, 1\n"
      "v_and_b32 v219, s76, v219\n"
      "v_sub_u32 v22, v22, v219\n"
      "v_bfe_i32 v219, v27, 17, 1\n"
      "v_and_b32 v219, s76, v219\n"
      "v_sub_u32 v23, v23, v219\n"
      "v_bfe_i32 v219, v27, 19, 1\n"
      "v_and_b32 v219, s76, v219\n"
      "v_sub_u32 v25, v25, v219\n"
      "L_ax_34_%=:\n"
      "s_or_b32 s48, s66, s69\n"
      "s_cmp_lg_u32 s48, 0\n"
      "s_cselect_b32 s48, 0x40000000, 0\n"
      "s_waitcnt lgkmcnt(0)\n"
      "s_nop 4\n"
      "s_branch L_back_36_%=\n"
      "L_rare_39_%=:\n"
      "s_nop 7\n"
      "s_waitcnt lgkmcnt(0)\n"
      "s_bitcmp1_b32 s46, 31\n"
      "s_cselect_b32 s75, 100270464, 0\n"
      "s_bitcmp1_b32 s46, 30\n"
      "s_cselect_b32 s76, 196224, 0\n"
      "s_and_b32 s74, s46, 0x3fffffff\n"
      "s_cmp_ge_u32 s46, 0x40000000\n"
      "s_cselect_b32 s74, s74, 0\n"
      "s_cselect_b32 s46, 0, s46\n"
      "s_sub_u32 s74, s74, s69\n"
      "s_add_u32 s69, s69, s74\n"
      "s_sub_u32 s75, s75, s70\n"
      "s_add_u32 s70, s70, s75\n"
      "s_sub_u32 s76, s76, s71\n"
      "s_add_u32 s71, s71, s76\n"
      "v_add_u32 v20, s74, v20\n"
      "v_add_u32 v21, s74, v21\n"
      "v_add_u32 v22, s74, v22\n"
      "v_add_u32 v23, s74, v23\n"
      "v_add_u32 v25, s74, v25\n"
      "s_cmp_eq_u32 s75, 0\n"
      "s_cbranch_scc1 L_ax_37_%=\n"
      "v_bfe_i32 v153, v27, 10, 1\n"
      "v_and_b32 v153, s75, v153\n"
      "v_sub_u32 v20, v20, v153\n"
      "v_bfe_i32 v153, v27, 12, 1\n"
      "v_and_b32 v153, s75, v153\n"
      "v_sub_u32 v21, v21, v153\n"
      "v_bfe_i32 v153, v27, 14, 1\n"
      "v_and_b32 v153, s75, v153\n"
      "v_sub_u32 v22, v22, v153\n"
      "v_bfe_i32 v153, v27, 16, 1\n"
      "v_and_b32 v153, s75, v153\n"
      "v_sub_u32 v23, v23, v153\n"
      "v_bfe_i32 v153, v27, 18, 1\n"
      "v_and_b32 v153, s75, v153\n"
      "v_sub_u32 v25, v25, v153\n"
      "L_ax_37_%=:\n"
      "s_cmp_eq_u32 s76, 0\n"
      "s_cbranch_scc1 L_ax_38_%=\n"
      "v_bfe_i32 v153, v27, 11, 1\n"
      "v_and_b32 v153, s76, v153\n"
      "v_sub_u32 v20, v20, v153\n"
      "v_bfe_i32 v153, v27, 13, 1\n"
      "v_and_b32 v153, s76, v153\n"
      "v_sub_u32 v21, v21, v153\n"
      "v_bfe_i32 v153, v27, 15, 1\n"
      "v_and_b32 v153, s76, v153\n"
      "v_sub_u32 v22, v22, v153\n"
      "v_bfe_i32 v153, v27, 17, 1\n"
      "v_and_b32 v153, s76, v153\n"
      "v_sub_u32 v23, v23, v153\n"
      "v_bfe_i32 v153, v27, 19, 1\n"
      "v_and_b32 v153, s76, v153\n"
      "v_sub_u32 v25, v25, v153\n"
      "L_ax_38_%=:\n"
      "s_or_b32 s48, s66, s69\n"
      "s_cmp_lg_u32 s48, 0\n"
      "s_cselect_b32 s48, 0x40000000, 0\n"
      "s_waitcnt lgkmcnt(0)\n"
      "s_nop 4\n"
      "s_branch L_back_40_%=\n"
      "L_cend_%=:\n"
      :
      : "s"(__builtin_amdgcn_kernarg_segment_ptr()), "s"(wave), "s"(blockIdx.x), "s"(CT_TAB), "s"(lds0)
      : "v0","v1","v2","v3","v4","v5","v6","v7","v8","v9","v10","v11","v12","v13","v14","v15","v16","v17","v18","v19","v20","v21","v22","v23","v24","v25","v26","v27","v28","v29","v30","v31","v32","v33","v34","v35","v36","v37","v38","v39","v40","v41","v42","v43","v44","v45","v46","v47","v48","v49","v50","v51","v52","v53","v54","v55","v56","v57","v58","v59","v60","v61","v62","v63","v64","v65","v66","v67","v68","v69","v70","v71","v72","v73","v74","v75","v76","v77","v78","v79","v80","v81","v82","v83","v84","v85","v86","v87","v88","v89","v90","v91","v92","v93","v94","v95","v96","v97","v98","v99","v100","v101","v102","v103","v104","v105","v106","v107","v108","v109","v110","v111","v112","v113","v114","v115","v116","v117","v118","v119","v120","v121","v122","v123","v124","v125","v126","v127","v128","v129","v130","v131","v132","v133","v134","v135","v136","v137","v138","v139","v140","v141","v142","v143","v144","v145","v146","v147","v148","v149","v150","v151","v152","v153","v154","v155","v156","v157","v158","v159","v160","v161","v162","v163","v164","v165","v166","v167","v168","v169","v170","v171","v172","v173","v174","v175","v176","v177","v178","v179","v180","v181","v182","v183","v184","v185","v186","v187","v188","v189","v190","v191","v192","v193","v194","v195","v196","v197","v198","v199","v200","v201","v202","v203","v204","v205","v206","v207","v208","v209","v210","v211","v212","v213","v214","v215","v216","v217","v218","v219","v220","v221","v222","v223","v224","v225","v226","v227","v228","v229","v230","v231","v232","v233","v234","v235","v236","v237","v238","v239","v240","v241","v242","v243","v244","v245","v246","v247","v248","v249","v250","v251","v252","v253","v254","v255","a0","a1","a2","a3","a4","a5","a6","a7","a8","a9","a10","a11","a12","a13","a14","a15","a16","a17","a18","a19","a20","a21","a22","a23","a24","a25","a26","a27","a28","a29","a30","a31","a32","a33","a34","a35","a36","a37","a38","a39","a40","a41","a42","a43","a44","a45","a46","a47","a48","a49","a50","a51","a52","a53","a54","a55","a56","a57","a58","a59","a60","a61","a62","a63","a64","a65","a66","a67","a68","a69","a70","a71","a72","a73","a74","a75","a76","a77","a78","a79","a80","a81","a82","a83","a84","a85","a86","a87","a88","a89","a90","a91","a92","a93","a94","a95","a96","a97","a98","a99","a100","a101","a102","a103","a104","a105","a106","a107","a108","a109","a110","a111","a112","a113","a114","a115","a116","a117","a118","a119","a120","a121","a122","a123","a124","a125","a126","a127","a128","a129","a130","a131","a132","a133","a134","a135","a136","a137","a138","a139","a140","a141","a142","a143","a144","a145","a146","a147","a148","a149","a150","a151","a152","a153","a154","a155","a156","a157","a158","a159","a160","a161","a162","a163","a164","a165","a166","a167","a168","a169","a170","a171","a172","a173","a174","a175","a176","a177","a178","a179","a180","a181","a182","a183","a184","a185","a186","a187","a188","a189","a190","a191","a192","a193","a194","a195","a196","a197","a198","a199","a200","a201","a202","a203","a204","a205","a206","a207","a208","a209","a210","a211","a212","a213","a214","a215","a216","a217","a218","a219","a220","a221","a222","a223","a224","a225","a226","a227","a228","a229","a230","a231","a232","a233","a234","a235","a236","a237","a238","a239","a240","a241","a242","a243","a244","a245","a246","a247","a248","a249","a250","a251","a252","a253","a254","a255","s16","s17","s18","s19","s20","s21","s22","s23","s24","s25","s26","s27","s28","s29","s30","s31","s33","s34","s35","s36","s37","s38","s39","s40","s41","s42","s43","s44","s45","s46","s47","s48","s49","s50","s51","s52","s53","s54","s55","s56","s57","s58","s59","s60","s61","s62","s63","s64","s65","s66","s67","s68","s69","s70","s71","s72","s73","s74","s75","s76","s77","s78","s79","s80","s81","s82","s83","s84","s85","s86","s87","s88","s89","s90","s91","s92","s93","s94","s95","s96","s97","s98","s99","s100","s101", "vcc", "scc", "memory");
}

}

extern "C" void kernel_launch(void* const* d_in, const int* in_sizes, int n_in, void* d_out, int out_size, void* d_ws,
                              size_t ws_size, hipStream_t stream) {
  const uint16_t* x = (const uint16_t*)d_in[0];
  const uint16_t* wqkv = (const uint16_t*)d_in[1];
  const uint16_t* wo = (const uint16_t*)d_in[2];
  const uint16_t* ls = (const uint16_t*)d_in[3];
  const uint16_t* w1 = (const uint16_t*)d_in[4];
  const uint16_t* w2 = (const uint16_t*)d_in[5];
  const uint16_t* rc = (const uint16_t*)d_in[6];
  const int* ridx = (const int*)d_in[7];
  const uint16_t* mask = (const uint16_t*)d_in[8];
  swin::swin_main<<<256, 256, 0, stream>>>(x, mask, (char*)d_ws, (uint16_t*)d_out, wqkv, wo, ls, w1, w2, rc, ridx);
}
```

```cpp
#include <hip/hip_runtime.h>
#include <hip/hip_bf16.h>
#include <stdint.h>

namespace swin {

constexpr unsigned CT_WORDS = 8192u;
constexpr unsigned LDS_BYTES = 163840u;

__device__ __attribute__((aligned(256))) const unsigned CT_TAB[CT_WORDS] = {0x4800u,0x4820u,0x4840u,0x4800u,0x4808u,0x4828u,0x4848u,0x4810u,0x4810u,0x4830u,0x4850u,0x4820u,0x4818u,0x4838u,0x4858u,0x4830u,0x4880u,0x48a0u,0x48c0u,0x4880u,0x4888u,0x48a8u,0x48c8u,0x4890u,0x4890u,0x48b0u,0x48d0u,0x48a0u,0x4898u,0x48b8u,0x48d8u,0x48b0u,0x4920u,0x4900u,0x4960u,0x4920u,0x4928u,0x4908u,0x4968u,0x4930u,0x4930u,0x4910u,0x4970u,0x4900u,0x4938u,0x4918u,0x4978u,0x4910u,0x49a0u,0x4980u,0x49e0u,0x49a0u,0x49a8u,0x4988u,0x49e8u,0x49b0u,0x49b0u,0x4990u,0x49f0u,0x4980u,0x49b8u,0x4998u,0x49f8u,0x4990u,0x4c40u,0x4c60u,0x4c00u,0x4a00u,0x4c48u,0x4c68u,0x4c08u,0x4a10u,0x4c50u,0x4c70u,0x4c10u,0x4a20u,0x4c58u,0x4c78u,0x4c18u,0x4a30u,0x4cc0u,0x4ce0u,0x4c80u,0x4a80u,0x4cc8u,0x4ce8u,0x4c88u,0x4a90u,0x4cd0u,0x4cf0u,0x4c90u,0x4aa0u,0x4cd8u,0x4cf8u,0x4c98u,0x4ab0u,0x4d60u,0x4d40u,0x4d20u,0x4b20u,0x4d68u,0x4d48u,0x4d28u,0x4b30u,0x4d70u,0x4d50u,0x4d30u,0x4b00u,0x4d78u,0x4d58u,0x4d38u,0x4b10u,0x4de0u,0x4dc0u,0x4da0u,0x4ba0u,0x4de8u,0x4dc8u,0x4da8u,0x4bb0u,0x4df0u,0x4dd0u,0x4db0u,0x4b80u,0x4df8u,0x4dd8u,0x4db8u,0x4b90u,0x5000u,0x5020u,0x5040u,0x4c40u,0x5008u,0x5028u,0x5048u,0x4c50u,0x5010u,0x5030u,0x5050u,0x4c60u,0x5018u,0x5038u,0x5058u,0x4c70u,0x5080u,0x50a0u,0x50c0u,0x4cc0u,0x5088u,0x50a8u,0x50c8u,0x4cd0u,0x5090u,0x50b0u,0x50d0u,0x4ce0u,0x5098u,0x50b8u,0x50d8u,0x4cf0u,0x5120u,0x5100u,0x5160u,0x4d60u,0x5128u,0x5108u,0x5168u,0x4d70u,0x5130u,0x5110u,0x5170u,0x4d40u,0x5138u,0x5118u,0x5178u,0x4d50u,0x51a0u,0x5180u,0x51e0u,0x4de0u,0x51a8u,0x5188u,0x51e8u,0x4df0u,0x51b0u,0x5190u,0x51f0u,0x4dc0u,0x51b8u,0x5198u,0x51f8u,0x4dd0u,0x5440u,0x5460u,0x5400u,0x4e40u,0x5448u,0x5468u,0x5408u,0x4e50u,0x5450u,0x5470u,0x5410u,0x4e60u,0x5458u,0x5478u,0x5418u,0x4e70u,0x54c0u,0x54e0u,0x5480u,0x4ec0u,0x54c8u,0x54e8u,0x5488u,0x4ed0u,0x54d0u,0x54f0u,0x5490u,0x4ee0u,0x54d8u,0x54f8u,0x5498u,0x4ef0u,0x5560u,0x5540u,0x5520u,0x4f60u,0x5568u,0x5548u,0x5528u,0x4f70u,0x5570u,0x5550u,0x5530u,0x4f40u,0x5578u,0x5558u,0x5538u,0x4f50u,0x55e0u,0x55c0u,0x55a0u,0x4fe0u,0x55e8u,0x55c8u,0x55a8u,0x4ff0u,0x55f0u,0x55d0u,0x55b0u,0x4fc0u,0x55f8u,0x55d8u,0x55b8u,0x4fd0u,0x4840u,0x1e800u,0x1eb00u,0x0u,0x4848u,0x1ec90u,0x1ef90u,0x10u,0x4850u,0x1f120u,0x1f420u,0x20u,0x4858u,0x1f5b0u,0x1f8b0u,0x30u,0x48c0u,0x1fa40u,0x1fd40u,0x40u,0x48c8u,0x1fed0u,0x201d0u,0x50u,0x48d0u,0x20360u,0x20660u,0x60u,0x48d8u,0x207f0u,0x20af0u,0x70u,0x4960u,0x20c80u,0x20f80u,0x180u,0x4968u,0x21110u,0x21410u,0x190u,0x4970u,0x215a0u,0x218a0u,0x1a0u,0x4978u,0x21a30u,0x21d30u,0x1b0u,0x49e0u,0x21ec0u,0x221c0u,0x1c0u,0x49e8u,0x22350u,0x22650u,0x1d0u,0x49f0u,0x227e0u,0x22ae0u,0x1e0u,0x49f8u,0x22c70u,0x22f70u,0x1f0u,0x4a40u,0x1e830u,0x1eb08u,0x320u,0x4a48u,0x1ecc0u,0x1ef98u,0x330u,0x4a50u,0x1f150u,0x1f428u,0x300u,0x4a58u,0x1f5e0u,0x1f8b8u,0x310u,0x4ac0u,0x1fa70u,0x1fd48u,0x360u,0x4ac8u,0x1ff00u,0x201d8u,0x370u,0x4ad0u,0x20390u,0x20668u,0x340u,0x4ad8u,0x20820u,0x20af8u,0x350u,0x4b60u,0x20cb0u,0x20f88u,0x4a0u,0x4b68u,0x21140u,0x21418u,0x4b0u,0x4b70u,0x215d0u,0x218a8u,0x480u,0x4b78u,0x21a60u,0x21d38u,0x490u,0x4be0u,0x21ef0u,0x221c8u,0x4e0u,0x4be8u,0x22380u,0x22658u,0x4f0u,0x4bf0u,0x22810u,0x22ae8u,0x4c0u,0x4bf8u,0x22ca0u,0x22f78u,0x4d0u,0x4c00u,0x1e860u,0x1eb10u,0x600u,0x4c08u,0x1ecf0u,0x1efa0u,0x610u,0x4c10u,0x1f180u,0x1f430u,0x620u,0x4c18u,0x1f610u,0x1f8c0u,0x630u,0x4c80u,0x1faa0u,0x1fd50u,0x640u,0x4c88u,0x1ff30u,0x201e0u,0x650u,0x4c90u,0x203c0u,0x20670u,0x660u,0x4c98u,0x20850u,0x20b00u,0x670u,0x4d20u,0x20ce0u,0x20f90u,0x780u,0x4d28u,0x21170u,0x21420u,0x790u,0x4d30u,0x21600u,0x218b0u,0x7a0u,0x4d38u,0x21a90u,0x21d40u,0x7b0u,0x4da0u,0x21f20u,0x221d0u,0x7c0u,0x4da8u,0x223b0u,0x22660u,0x7d0u,0x4db0u,0x22840u,0x22af0u,0x7e0u,0x4db8u,0x22cd0u,0x22f80u,0x7f0u,0x4e00u,0x1e890u,0x1eb18u,0x920u,0x4e08u,0x1ed20u,0x1efa8u,0x930u,0x4e10u,0x1f1b0u,0x1f438u,0x900u,0x4e18u,0x1f640u,0x1f8c8u,0x910u,0x4e80u,0x1fad0u,0x1fd58u,0x960u,0x4e88u,0x1ff60u,0x201e8u,0x970u,0x4e90u,0x203f0u,0x20678u,0x940u,0x4e98u,0x20880u,0x20b08u,0x950u,0x4f20u,0x20d10u,0x20f98u,0xaa0u,0x4f28u,0x211a0u,0x21428u,0xab0u,0x4f30u,0x21630u,0x218b8u,0xa80u,0x4f38u,0x21ac0u,0x21d48u,0xa90u,0x4fa0u,0x21f50u,0x221d8u,0xae0u,0x4fa8u,0x223e0u,0x22668u,0xaf0u,0x4fb0u,0x22870u,0x22af8u,0xac0u,0x4fb8u,0x22d00u,0x22f88u,0xad0u,0x0u,0x40u,0x9000u,0x9000u,0x80u,0xc0u,0x9010u,0x9010u,0x110u,0x150u,0x9020u,0x9020u,0x190u,0x1d0u,0x9030u,0x9030u,0x220u,0x260u,0x9040u,0x9040u,0x2a0u,0x2e0u,0x9050u,0x9050u,0x330u,0x370u,0x9060u,0x9060u,0x3b0u,0x3f0u,0x9070u,0x9070u,0x440u,0x400u,0x9080u,0x9080u,0x4c0u,0x480u,0x9090u,0x9090u,0x550u,0x510u,0x90a0u,0x90a0u,0x5d0u,0x590u,0x90b0u,0x90b0u,0x660u,0x620u,0x90c0u,0x90c0u,0x6e0u,0x6a0u,0x90d0u,0x90d0u,0x770u,0x730u,0x90e0u,0x90e0u,0x7f0u,0x7b0u,0x90f0u,0x90f0u,0x10u,0x50u,0x9300u,0x9300u,0x90u,0xd0u,0x9310u,0x9310u,0x100u,0x140u,0x9320u,0x9320u,0x180u,0x1c0u,0x9330u,0x9330u,0x230u,0x270u,0x9340u,0x9340u,0x2b0u,0x2f0u,0x9350u,0x9350u,0x320u,0x360u,0x9360u,0x9360u,0x3a0u,0x3e0u,0x9370u,0x9370u,0x450u,0x410u,0x9380u,0x9380u,0x4d0u,0x490u,0x9390u,0x9390u,0x540u,0x500u,0x93a0u,0x93a0u,0x5c0u,0x580u,0x93b0u,0x93b0u,0x670u,0x630u,0x93c0u,0x93c0u,0x6f0u,0x6b0u,0x93d0u,0x93d0u,0x760u,0x720u,0x93e0u,0x93e0u,0x7e0u,0x7a0u,0x93f0u,0x93f0u,0x20u,0x60u,0x9600u,0x9600u,0xa0u,0xe0u,0x9610u,0x9610u,0x130u,0x170u,0x9620u,0x9620u,0x1b0u,0x1f0u,0x9630u,0x9630u,0x200u,0x240u,0x9640u,0x9640u,0x280u,0x2c0u,0x9650u,0x9650u,0x310u,0x350u,0x9660u,0x9660u,0x390u,0x3d0u,0x9670u,0x9670u,0x460u,0x420u,0x9680u,0x9680u,0x4e0u,0x4a0u,0x9690u,0x9690u,0x570u,0x530u,0x96a0u,0x96a0u,0x5f0u,0x5b0u,0x96b0u,0x96b0u,0x640u,0x600u,0x96c0u,0x96c0u,0x6c0u,0x680u,0x96d0u,0x96d0u,0x750u,0x710u,0x96e0u,0x96e0u,0x7d0u,0x790u,0x96f0u,0x96f0u,0x30u,0x70u,0x9900u,0x9900u,0xb0u,0xf0u,0x9910u,0x9910u,0x120u,0x160u,0x9920u,0x9920u,0x1a0u,0x1e0u,0x9930u,0x9930u,0x210u,0x250u,0x9940u,0x9940u,0x290u,0x2d0u,0x9950u,0x9950u,0x300u,0x340u,0x9960u,0x9960u,0x380u,0x3c0u,0x9970u,0x9970u,0x470u,0x430u,0x9980u,0x9980u,0x4f0u,0x4b0u,0x9990u,0x9990u,0x560u,0x520u,0x99a0u,0x99a0u,0x5e0u,0x5a0u,0x99b0u,0x99b0u,0x650u,0x610u,0x99c0u,0x99c0u,0x6d0u,0x690u,0x99d0u,0x99d0u,0x740u,0x700u,0x99e0u,0x99e0u,0x7c0u,0x780u,0x99f0u,0x99f0u,0x9c00u,0x12000u,0x12040u,0x12000u,0x9c10u,0x12190u,0x121d0u,0x12010u,0x9c20u,0x12320u,0x12360u,0x12020u,0x9c30u,0x124b0u,0x124f0u,0x12030u,0x9c40u,0x12640u,0x12600u,0x12040u,0x9c50u,0x127d0u,0x12790u,0x12050u,0x9c60u,0x12960u,0x12920u,0x12060u,0x9c70u,0x12af0u,0x12ab0u,0x12070u,0x9c80u,0x12c00u,0x12c40u,0x12080u,0x9c90u,0x12d90u,0x12dd0u,0x12090u,0x9ca0u,0x12f20u,0x12f60u,0x120a0u,0x9cb0u,0x130b0u,0x130f0u,0x120b0u,0x9cc0u,0x13240u,0x13200u,0x120c0u,0x9cd0u,0x133d0u,0x13390u,0x120d0u,0x9ce0u,0x13560u,0x13520u,0x120e0u,0x9cf0u,0x136f0u,0x136b0u,0x120f0u,0x9f00u,0x12010u,0x12048u,0x12100u,0x9f10u,0x12180u,0x121d8u,0x12110u,0x9f20u,0x12330u,0x12368u,0x12120u,0x9f30u,0x124a0u,0x124f8u,0x12130u,0x9f40u,0x12650u,0x12608u,0x12140u,0x9f50u,0x127c0u,0x12798u,0x12150u,0x9f60u,0x12970u,0x12928u,0x12160u,0x9f70u,0x12ae0u,0x12ab8u,0x12170u,0x9f80u,0x12c10u,0x12c48u,0x12180u,0x9f90u,0x12d80u,0x12dd8u,0x12190u,0x9fa0u,0x12f30u,0x12f68u,0x121a0u,0x9fb0u,0x130a0u,0x130f8u,0x121b0u,0x9fc0u,0x13250u,0x13208u,0x121c0u,0x9fd0u,0x133c0u,0x13398u,0x121d0u,0x9fe0u,0x13570u,0x13528u,0x121e0u,0x9ff0u,0x136e0u,0x136b8u,0x121f0u,0xa200u,0x12020u,0x12050u,0x12200u,0xa210u,0x121b0u,0x121c0u,0x12210u,0xa220u,0x12300u,0x12370u,0x12220u,0xa230u,0x12490u,0x124e0u,0x12230u,0xa240u,0x12660u,0x12610u,0x12240u,0xa250u,0x127f0u,0x12780u,0x12250u,0xa260u,0x12940u,0x12930u,0x12260u,0xa270u,0x12ad0u,0x12aa0u,0x12270u,0xa280u,0x12c20u,0x12c50u,0x12280u,0xa290u,0x12db0u,0x12dc0u,0x12290u,0xa2a0u,0x12f00u,0x12f70u,0x122a0u,0xa2b0u,0x13090u,0x130e0u,0x122b0u,0xa2c0u,0x13260u,0x13210u,0x122c0u,0xa2d0u,0x133f0u,0x13380u,0x122d0u,0xa2e0u,0x13540u,0x13530u,0x122e0u,0xa2f0u,0x136d0u,0x136a0u,0x122f0u,0xa500u,0x12030u,0x12058u,0x12300u,0xa510u,0x121a0u,0x121c8u,0x12310u,0xa520u,0x12310u,0x12378u,0x12320u,0xa530u,0x12480u,0x124e8u,0x12330u,0xa540u,0x12670u,0x12618u,0x12340u,0xa550u,0x127e0u,0x12788u,0x12350u,0xa560u,0x12950u,0x12938u,0x12360u,0xa570u,0x12ac0u,0x12aa8u,0x12370u,0xa580u,0x12c30u,0x12c58u,0x12380u,0xa590u,0x12da0u,0x12dc8u,0x12390u,0xa5a0u,0x12f10u,0x12f78u,0x123a0u,0xa5b0u,0x13080u,0x130e8u,0x123b0u,0xa5c0u,0x13270u,0x13218u,0x123c0u,0xa5d0u,0x133e0u,0x13388u,0x123d0u,0xa5e0u,0x13550u,0x13538u,0x123e0u,0xa5f0u,0x136c0u,0x136a8u,0x123f0u,0x4800u,0x1c300u,0x1e800u,0x1eb00u,0x4880u,0x1c310u,0x100000u,0x1eb02u,0x4910u,0x1c320u,0x100000u,0x1eb04u,0x4990u,0x1c330u,0x100000u,0x1eb06u,0x4a20u,0x1c340u,0x1e800u,0x1eb08u,0x4aa0u,0x1c350u,0x100000u,0x1eb0au,0x4b30u,0x1c360u,0x100000u,0x1eb0cu,0x4bb0u,0x1c370u,0x100000u,0x1eb0eu,0x4c40u,0x1c380u,0x1e800u,0x1eb10u,0x4cc0u,0x1c390u,0x100000u,0x1eb12u,0x4d50u,0x1c3a0u,0x100000u,0x1eb14u,0x4dd0u,0x1c3b0u,0x100000u,0x1eb16u,0x4e60u,0x1c3c0u,0x1e800u,0x1eb18u,0x4ee0u,0x1c3d0u,0x100000u,0x1eb1au,0x4f70u,0x1c3e0u,0x100000u,0x1eb1cu,0x4ff0u,0x1c3f0u,0x100000u,0x1eb1eu,0x4810u,0x1c400u,0x1e830u,0x1eb00u,0x4890u,0x1c410u,0x100000u,0x1eb02u,0x4900u,0x1c420u,0x100000u,0x1eb04u,0x4980u,0x1c430u,0x100000u,0x1eb06u,0x4a30u,0x1c440u,0x1e830u,0x1eb08u,0x4ab0u,0x1c450u,0x100000u,0x1eb0au,0x4b20u,0x1c460u,0x100000u,0x1eb0cu,0x4ba0u,0x1c470u,0x100000u,0x1eb0eu,0x4c50u,0x1c480u,0x1e830u,0x1eb10u,0x4cd0u,0x1c490u,0x100000u,0x1eb12u,0x4d40u,0x1c4a0u,0x100000u,0x1eb14u,0x4dc0u,0x1c4b0u,0x100000u,0x1eb16u,0x4e70u,0x1c4c0u,0x1e830u,0x1eb18u,0x4ef0u,0x1c4d0u,0x100000u,0x1eb1au,0x4f60u,0x1c4e0u,0x100000u,0x1eb1cu,0x4fe0u,0x1c4f0u,0x100000u,0x1eb1eu,0x4820u,0x1c500u,0x1e860u,0x1eb00u,0x48a0u,0x1c510u,0x100000u,0x1eb02u,0x4930u,0x1c520u,0x100000u,0x1eb04u,0x49b0u,0x1c530u,0x100000u,0x1eb06u,0x4a00u,0x1c540u,0x1e860u,0x1eb08u,0x4a80u,0x1c550u,0x100000u,0x1eb0au,0x4b10u,0x1c560u,0x100000u,0x1eb0cu,0x4b90u,0x1c570u,0x100000u,0x1eb0eu,0x4c60u,0x1c580u,0x1e860u,0x1eb10u,0x4ce0u,0x1c590u,0x100000u,0x1eb12u,0x4d70u,0x1c5a0u,0x100000u,0x1eb14u,0x4df0u,0x1c5b0u,0x100000u,0x1eb16u,0x4e40u,0x1c5c0u,0x1e860u,0x1eb18u,0x4ec0u,0x1c5d0u,0x100000u,0x1eb1au,0x4f50u,0x1c5e0u,0x100000u,0x1eb1cu,0x4fd0u,0x1c5f0u,0x100000u,0x1eb1eu,0x4830u,0x1c600u,0x1e890u,0x1eb00u,0x48b0u,0x1c610u,0x100000u,0x1eb02u,0x4920u,0x1c620u,0x100000u,0x1eb04u,0x49a0u,0x1c630u,0x100000u,0x1eb06u,0x4a10u,0x1c640u,0x1e890u,0x1eb08u,0x4a90u,0x1c650u,0x100000u,0x1eb0au,0x4b00u,0x1c660u,0x100000u,0x1eb0cu,0x4b80u,0x1c670u,0x100000u,0x1eb0eu,0x4c70u,0x1c680u,0x1e890u,0x1eb10u,0x4cf0u,0x1c690u,0x100000u,0x1eb12u,0x4d60u,0x1c6a0u,0x100000u,0x1eb14u,0x4de0u,0x1c6b0u,0x100000u,0x1eb16u,0x4e50u,0x1c6c0u,0x1e890u,0x1eb18u,0x4ed0u,0x1c6d0u,0x100000u,0x1eb1au,0x4f40u,0x1c6e0u,0x100000u,0x1eb1cu,0x4fc0u,0x1c6f0u,0x100000u,0x1eb1eu,0x24800u,0x1d300u,0x1d300u,0xc0100u,0x24802u,0x1d302u,0x100000u,0xc0110u,0x24804u,0x1d304u,0x100000u,0xc0120u,0x24806u,0x1d306u,0x100000u,0xc0130u,0x24a00u,0x1d310u,0x1d300u,0xc0140u,0x24a02u,0x1d312u,0x100000u,0xc0150u,0x24a04u,0x1d314u,0x100000u,0xc0160u,0x24a06u,0x1d316u,0x100000u,0xc0170u,0x24c00u,0x1d320u,0x1d300u,0xc0280u,0x24c02u,0x1d322u,0x100000u,0xc0290u,0x24c04u,0x1d324u,0x100000u,0xc02a0u,0x24c06u,0x1d326u,0x100000u,0xc02b0u,0x24e00u,0x1d330u,0x1d300u,0xc02c0u,0x24e02u,0x1d332u,0x100000u,0xc02d0u,0x24e04u,0x1d334u,0x100000u,0xc02e0u,0x24e06u,0x1d336u,0x100000u,0xc02f0u,0x24808u,0x1d308u,0x1d310u,0xc0410u,0x2480au,0x1d30au,0x100000u,0xc0400u,0x2480cu,0x1d30cu,0x100000u,0xc0430u,0x2480eu,0x1d30eu,0x100000u,0xc0420u,0x24a08u,0x1d318u,0x1d310u,0xc0450u,0x24a0au,0x1d31au,0x100000u,0xc0440u,0x24a0cu,0x1d31cu,0x100000u,0xc0470u,0x24a0eu,0x1d31eu,0x100000u,0xc0460u,0x24c08u,0x1d328u,0x1d310u,0xef990u,0x24c0au,0x1d32au,0x100000u,0xef980u,0x24c0cu,0x1d32cu,0x100000u,0xef9b0u,0x24c0eu,0x1d32eu,0x100000u,0xef9a0u,0x24e08u,0x1d338u,0x1d310u,0xef9d0u,0x24e0au,0x1d33au,0x100000u,0xef9c0u,0x24e0cu,0x1d33cu,0x100000u,0xef9f0u,0x24e0eu,0x1d33eu,0x100000u,0xef9e0u,0x25000u,0x1d340u,0x1d320u,0xefb20u,0x25002u,0x1d342u,0x100000u,0xefb30u,0x25004u,0x1d344u,0x100000u,0xefb00u,0x25006u,0x1d346u,0x100000u,0xefb10u,0x25200u,0x1d350u,0x1d320u,0xefb60u,0x25202u,0x1d352u,0x100000u,0xefb70u,0x25204u,0x1d354u,0x100000u,0xefb40u,0x25206u,0x1d356u,0x100000u,0xefb50u,0x25400u,0x1d360u,0x1d320u,0xefca0u,0x25402u,0x1d362u,0x100000u,0xefcb0u,0x25404u,0x1d364u,0x100000u,0xefc80u,0x25406u,0x1d366u,0x100000u,0xefc90u,0x25600u,0x1d370u,0x1d320u,0xefce0u,0x25602u,0x1d372u,0x100000u,0xefcf0u,0x25604u,0x1d374u,0x100000u,0xefcc0u,0x25606u,0x1d376u,0x100000u,0xefcd0u,0x100000u,0x1d348u,0x1d330u,0xefe30u,0x100000u,0x1d34au,0x100000u,0xefe20u,0x100000u,0x1d34cu,0x100000u,0xefe10u,0x100000u,0x1d34eu,0x100000u,0xefe00u,0x100000u,0x1d358u,0x1d330u,0xefe70u,0x100000u,0x1d35au,0x100000u,0xefe60u,0x100000u,0x1d35cu,0x100000u,0xefe50u,0x100000u,0x1d35eu,0x100000u,0xefe40u,0x100000u,0x1d368u,0x1d330u,0xeffb0u,0x100000u,0x1d36au,0x100000u,0xeffa0u,0x100000u,0x1d36cu,0x100000u,0xeff90u,0x100000u,0x1d36eu,0x100000u,0xeff80u,0x100000u,0x1d378u,0x1d330u,0xefff0u,0x100000u,0x1d37au,0x100000u,0xeffe0u,0x100000u,0x1d37cu,0x100000u,0xeffd0u,0x100000u,0x1d37eu,0x100000u,0xeffc0u,0x0u,0x30420u,0x8fc50u,0xc0000u,0x10u,0x30430u,0x8fc40u,0xc0010u,0x20u,0x30400u,0x8fc70u,0xc0020u,0x30u,0x30410u,0x8fc60u,0xc0030u,0x40u,0x30460u,0x8fc10u,0xc0040u,0x50u,0x30470u,0x8fc00u,0xc0050u,0x60u,0x30440u,0x8fc30u,0xc0060u,0x70u,0x30450u,0x8fc20u,0xc0070u,0x80u,0x304b0u,0x8fcd0u,0xc0080u,0x90u,0x304a0u,0x8fcc0u,0xc0090u,0xa0u,0x30490u,0x8fcf0u,0xc00a0u,0xb0u,0x30480u,0x8fce0u,0xc00b0u,0xc0u,0x304f0u,0x8fc90u,0xc00c0u,0xd0u,0x304e0u,0x8fc80u,0xc00d0u,0xe0u,0x304d0u,0x8fcb0u,0xc00e0u,0xf0u,0x304c0u,0x8fca0u,0xc00f0u,0x100u,0x30530u,0x8fd60u,0xc0100u,0x110u,0x30520u,0x8fd70u,0xc0110u,0x120u,0x30510u,0x8fd40u,0xc0120u,0x130u,0x30500u,0x8fd50u,0xc0130u,0x140u,0x30570u,0x8fd20u,0xc0140u,0x150u,0x30560u,0x8fd30u,0xc0150u,0x160u,0x30550u,0x8fd00u,0xc0160u,0x170u,0x30540u,0x8fd10u,0xc0170u,0x190u,0x305b0u,0x8fde0u,0xc0190u,0x180u,0x305a0u,0x8fdf0u,0xc0180u,0x1b0u,0x30590u,0x8fdc0u,0xc01b0u,0x1a0u,0x30580u,0x8fdd0u,0xc01a0u,0x1d0u,0x305f0u,0x8fda0u,0xc01d0u,0x1c0u,0x305e0u,0x8fdb0u,0xc01c0u,0x1f0u,0x305d0u,0x8fd80u,0xc01f0u,0x1e0u,0x305c0u,0x8fd90u,0xc01e0u,0x210u,0x30640u,0x8fe60u,0xc0210u,0x200u,0x30650u,0x8fe70u,0xc0200u,0x230u,0x30660u,0x8fe40u,0xc0230u,0x220u,0x30670u,0x8fe50u,0xc0220u,0x250u,0x30600u,0x8fe20u,0xc0250u,0x240u,0x30610u,0x8fe30u,0xc0240u,0x270u,0x30620u,0x8fe00u,0xc0270u,0x260u,0x30630u,0x8fe10u,0xc0260u,0x290u,0x306c0u,0x8fef0u,0xc0290u,0x280u,0x306d0u,0x8fee0u,0xc0280u,0x2b0u,0x306e0u,0x8fed0u,0xc02b0u,0x2a0u,0x306f0u,0x8fec0u,0xc02a0u,0x2d0u,0x30680u,0x8feb0u,0xc02d0u,0x2c0u,0x30690u,0x8fea0u,0xc02c0u,0x2f0u,0x306a0u,0x8fe90u,0xc02f0u,0x2e0u,0x306b0u,0x8fe80u,0xc02e0u,0x320u,0x30740u,0x8ff70u,0xc0320u,0x330u,0x30750u,0x8ff60u,0xc0330u,0x300u,0x30760u,0x8ff50u,0xc0300u,0x310u,0x30770u,0x8ff40u,0xc0310u,0x360u,0x30700u,0x8ff30u,0xc0360u,0x370u,0x30710u,0x8ff20u,0xc0370u,0x340u,0x30720u,0x8ff10u,0xc0340u,0x350u,0x30730u,0x8ff00u,0xc0350u,0x3a0u,0x307d0u,0x8fff0u,0xc03a0u,0x3b0u,0x307c0u,0x8ffe0u,0xc03b0u,0x380u,0x307f0u,0x8ffd0u,0xc0380u,0x390u,0x307e0u,0x8ffc0u,0xc0390u,0x3e0u,0x30790u,0x8ffb0u,0xc03e0u,0x3f0u,0x30780u,0x8ffa0u,0xc03f0u,0x3c0u,0x307b0u,0x8ff90u,0xc03c0u,0x3d0u,0x307a0u,0x8ff80u,0xc03d0u,0x0u,0x11f820u,0x0u,0x7030cu,0x0u,0x11f830u,0x0u,0x7030cu,0x0u,0x11f800u,0x0u,0x7030cu,0x0u,0x11f810u,0x0u,0x7030cu,0x0u,0x11f860u,0x0u,0x7030cu,0x0u,0x11f870u,0x0u,0x7030cu,0x0u,0x11f840u,0x0u,0x7030cu,0x0u,0x11f850u,0x0u,0x7030cu,0x0u,0x11f8b0u,0x0u,0x7230cu,0x0u,0x11f8a0u,0x0u,0x7230cu,0x0u,0x11f890u,0x0u,0x7230cu,0x0u,0x11f880u,0x0u,0x7230cu,0x0u,0x11f8f0u,0x0u,0x7230cu,0x0u,0x11f8e0u,0x0u,0x7230cu,0x0u,0x11f8d0u,0x0u,0x7230cu,0x0u,0x11f8c0u,0x0u,0x7230cu,0x0u,0x11f930u,0x0u,0x7232cu,0x0u,0x11f920u,0x0u,0x7232cu,0x0u,0x11f910u,0x0u,0x7232cu,0x0u,0x11f900u,0x0u,0x7232cu,0x0u,0x11f970u,0x0u,0x7232cu,0x0u,0x11f960u,0x0u,0x7232cu,0x0u,0x11f950u,0x0u,0x7232cu,0x0u,0x11f940u,0x0u,0x7232cu,0x0u,0x11f9b0u,0x0u,0x72124u,0x0u,0x11f9a0u,0x0u,0x72124u,0x0u,0x11f990u,0x0u,0x72124u,0x0u,0x11f980u,0x0u,0x72124u,0x0u,0x11f9f0u,0x0u,0x72124u,0x0u,0x11f9e0u,0x0u,0x72124u,0x0u,0x11f9d0u,0x0u,0x72124u,0x0u,0x11f9c0u,0x0u,0x72124u,0x0u,0x11fa40u,0x0u,0x721a6u,0x0u,0x11fa50u,0x0u,0x721a6u,0x0u,0x11fa60u,0x0u,0x721a6u,0x0u,0x11fa70u,0x0u,0x721a6u,0x0u,0x11fa00u,0x0u,0x721a6u,0x0u,0x11fa10u,0x0u,0x721a6u,0x0u,0x11fa20u,0x0u,0x721a6u,0x0u,0x11fa30u,0x0u,0x721a6u,0x0u,0x11fac0u,0x0u,0x72186u,0x0u,0x11fad0u,0x0u,0x72186u,0x0u,0x11fae0u,0x0u,0x72186u,0x0u,0x11faf0u,0x0u,0x72186u,0x0u,0x11fa80u,0x0u,0x72186u,0x0u,0x11fa90u,0x0u,0x72186u,0x0u,0x11faa0u,0x0u,0x72186u,0x0u,0x11fab0u,0x0u,0x72186u,0x0u,0x11fb40u,0x0u,0x72186u,0x0u,0x11fb50u,0x0u,0x72186u,0x0u,0x11fb60u,0x0u,0x72186u,0x0u,0x11fb70u,0x0u,0x72186u,0x0u,0x11fb00u,0x0u,0x72186u,0x0u,0x11fb10u,0x0u,0x72186u,0x0u,0x11fb20u,0x0u,0x72186u,0x0u,0x11fb30u,0x0u,0x72186u,0x0u,0x11fbd0u,0x0u,0x7230cu,0x0u,0x11fbc0u,0x0u,0x7230cu,0x0u,0x11fbf0u,0x0u,0x7230cu,0x0u,0x11fbe0u,0x0u,0x7230cu,0x0u,0x11fb90u,0x0u,0x7230cu,0x0u,0x11fb80u,0x0u,0x7230cu,0x0u,0x11fbb0u,0x0u,0x7230cu,0x0u,0x11fba0u,0x0u,0x7230cu,0x5800u,0x5820u,0x4860u,0x4860u,0x5808u,0x5828u,0x4868u,0x4870u,0x5810u,0x5830u,0x4870u,0x5800u,0x5818u,0x5838u,0x4878u,0x5810u,0x5880u,0x58a0u,0x48e0u,0x48e0u,0x5888u,0x58a8u,0x48e8u,0x48f0u,0x5890u,0x58b0u,0x48f0u,0x5880u,0x5898u,0x58b8u,0x48f8u,0x5890u,0x5920u,0x5900u,0x4940u,0x4940u,0x5928u,0x5908u,0x4948u,0x4950u,0x5930u,0x5910u,0x4950u,0x5920u,0x5938u,0x5918u,0x4958u,0x5930u,0x59a0u,0x5980u,0x49c0u,0x49c0u,0x59a8u,0x5988u,0x49c8u,0x49d0u,0x59b0u,0x5990u,0x49d0u,0x59a0u,0x59b8u,0x5998u,0x49d8u,0x59b0u,0x5c40u,0x5c60u,0x4c20u,0x4a60u,0x5c48u,0x5c68u,0x4c28u,0x4a70u,0x5c50u,0x5c70u,0x4c30u,0x5a00u,0x5c58u,0x5c78u,0x4c38u,0x5a10u,0x5cc0u,0x5ce0u,0x4ca0u,0x4ae0u,0x5cc8u,0x5ce8u,0x4ca8u,0x4af0u,0x5cd0u,0x5cf0u,0x4cb0u,0x5a80u,0x5cd8u,0x5cf8u,0x4cb8u,0x5a90u,0x5d60u,0x5d40u,0x4d00u,0x4b40u,0x5d68u,0x5d48u,0x4d08u,0x4b50u,0x5d70u,0x5d50u,0x4d10u,0x5b20u,0x5d78u,0x5d58u,0x4d18u,0x5b30u,0x5de0u,0x5dc0u,0x4d80u,0x4bc0u,0x5de8u,0x5dc8u,0x4d88u,0x4bd0u,0x5df0u,0x5dd0u,0x4d90u,0x5ba0u,0x5df8u,0x5dd8u,0x4d98u,0x5bb0u,0x6000u,0x6020u,0x5060u,0x4c20u,0x6008u,0x6028u,0x5068u,0x4c30u,0x6010u,0x6030u,0x5070u,0x5c40u,0x6018u,0x6038u,0x5078u,0x5c50u,0x6080u,0x60a0u,0x50e0u,0x4ca0u,0x6088u,0x60a8u,0x50e8u,0x4cb0u,0x6090u,0x60b0u,0x50f0u,0x5cc0u,0x6098u,0x60b8u,0x50f8u,0x5cd0u,0x6120u,0x6100u,0x5140u,0x4d00u,0x6128u,0x6108u,0x5148u,0x4d10u,0x6130u,0x6110u,0x5150u,0x5d60u,0x6138u,0x6118u,0x5158u,0x5d70u,0x61a0u,0x6180u,0x51c0u,0x4d80u,0x61a8u,0x6188u,0x51c8u,0x4d90u,0x61b0u,0x6190u,0x51d0u,0x5de0u,0x61b8u,0x6198u,0x51d8u,0x5df0u,0x6440u,0x6460u,0x5420u,0x4e20u,0x6448u,0x6468u,0x5428u,0x4e30u,0x6450u,0x6470u,0x5430u,0x5e40u,0x6458u,0x6478u,0x5438u,0x5e50u,0x64c0u,0x64e0u,0x54a0u,0x4ea0u,0x64c8u,0x64e8u,0x54a8u,0x4eb0u,0x64d0u,0x64f0u,0x54b0u,0x5ec0u,0x64d8u,0x64f8u,0x54b8u,0x5ed0u,0x6560u,0x6540u,0x5500u,0x4f00u,0x6568u,0x6548u,0x5508u,0x4f10u,0x6570u,0x6550u,0x5510u,0x5f60u,0x6578u,0x6558u,0x5518u,0x5f70u,0x65e0u,0x65c0u,0x5580u,0x4f80u,0x65e8u,0x65c8u,0x5588u,0x4f90u,0x65f0u,0x65d0u,0x5590u,0x5fe0u,0x65f8u,0x65d8u,0x5598u,0x5ff0u,0x5820u,0x1e8c0u,0x1eb60u,0xc40u,0x5828u,0x1ed50u,0x1eff0u,0xc50u,0x5830u,0x1f1e0u,0x1f480u,0xc60u,0x5838u,0x1f670u,0x1f910u,0xc70u,0x58a0u,0x1fb00u,0x1fda0u,0xc00u,0x58a8u,0x1ff90u,0x20230u,0xc10u,0x58b0u,0x20420u,0x206c0u,0xc20u,0x58b8u,0x208b0u,0x20b50u,0xc30u,0x5900u,0x20d40u,0x20fe0u,0xdc0u,0x5908u,0x211d0u,0x21470u,0xdd0u,0x5910u,0x21660u,0x21900u,0xde0u,0x5918u,0x21af0u,0x21d90u,0xdf0u,0x5980u,0x21f80u,0x22220u,0xd80u,0x5988u,0x22410u,0x226b0u,0xd90u,0x5990u,0x228a0u,0x22b40u,0xda0u,0x5998u,0x22d30u,0x22fd0u,0xdb0u,0x5a20u,0x1e8f0u,0x1eb68u,0xf60u,0x5a28u,0x1ed80u,0x1eff8u,0xf70u,0x5a30u,0x1f210u,0x1f488u,0xf40u,0x5a38u,0x1f6a0u,0x1f918u,0xf50u,0x5aa0u,0x1fb30u,0x1fda8u,0xf20u,0x5aa8u,0x1ffc0u,0x20238u,0xf30u,0x5ab0u,0x20450u,0x206c8u,0xf00u,0x5ab8u,0x208e0u,0x20b58u,0xf10u,0x5b00u,0x20d70u,0x20fe8u,0x10e0u,0x5b08u,0x21200u,0x21478u,0x10f0u,0x5b10u,0x21690u,0x21908u,0x10c0u,0x5b18u,0x21b20u,0x21d98u,0x10d0u,0x5b80u,0x21fb0u,0x22228u,0x10a0u,0x5b88u,0x22440u,0x226b8u,0x10b0u,0x5b90u,0x228d0u,0x22b48u,0x1080u,0x5b98u,0x22d60u,0x22fd8u,0x1090u,0x5c60u,0x1e920u,0x1eb70u,0x1240u,0x5c68u,0x1edb0u,0x1f000u,0x1250u,0x5c70u,0x1f240u,0x1f490u,0x1260u,0x5c78u,0x1f6d0u,0x1f920u,0x1270u,0x5ce0u,0x1fb60u,0x1fdb0u,0x1200u,0x5ce8u,0x1fff0u,0x20240u,0x1210u,0x5cf0u,0x20480u,0x206d0u,0x1220u,0x5cf8u,0x20910u,0x20b60u,0x1230u,0x5d40u,0x20da0u,0x20ff0u,0x13c0u,0x5d48u,0x21230u,0x21480u,0x13d0u,0x5d50u,0x216c0u,0x21910u,0x13e0u,0x5d58u,0x21b50u,0x21da0u,0x13f0u,0x5dc0u,0x21fe0u,0x22230u,0x1380u,0x5dc8u,0x22470u,0x226c0u,0x1390u,0x5dd0u,0x22900u,0x22b50u,0x13a0u,0x5dd8u,0x22d90u,0x22fe0u,0x13b0u,0x5e60u,0x1e950u,0x1eb78u,0x1560u,0x5e68u,0x1ede0u,0x1f008u,0x1570u,0x5e70u,0x1f270u,0x1f498u,0x1540u,0x5e78u,0x1f700u,0x1f928u,0x1550u,0x5ee0u,0x1fb90u,0x1fdb8u,0x1520u,0x5ee8u,0x20020u,0x20248u,0x1530u,0x5ef0u,0x204b0u,0x206d8u,0x1500u,0x5ef8u,0x20940u,0x20b68u,0x1510u,0x5f40u,0x20dd0u,0x20ff8u,0x16e0u,0x5f48u,0x21260u,0x21488u,0x16f0u,0x5f50u,0x216f0u,0x21918u,0x16c0u,0x5f58u,0x21b80u,0x21da8u,0x16d0u,0x5fc0u,0x22010u,0x22238u,0x16a0u,0x5fc8u,0x224a0u,0x226c8u,0x16b0u,0x5fd0u,0x22930u,0x22b58u,0x1680u,0x5fd8u,0x22dc0u,0x22fe8u,0x1690u,0x0u,0x40u,0x9000u,0xa800u,0x80u,0xc0u,0x9010u,0xa810u,0x110u,0x150u,0x9020u,0xa820u,0x190u,0x1d0u,0x9030u,0xa830u,0x220u,0x260u,0x9040u,0xa840u,0x2a0u,0x2e0u,0x9050u,0xa850u,0x330u,0x370u,0x9060u,0xa860u,0x3b0u,0x3f0u,0x9070u,0xa870u,0x440u,0x400u,0x9080u,0xa880u,0x4c0u,0x480u,0x9090u,0xa890u,0x550u,0x510u,0x90a0u,0xa8a0u,0x5d0u,0x590u,0x90b0u,0xa8b0u,0x660u,0x620u,0x90c0u,0xa8c0u,0x6e0u,0x6a0u,0x90d0u,0xa8d0u,0x770u,0x730u,0x90e0u,0xa8e0u,0x7f0u,0x7b0u,0x90f0u,0xa8f0u,0x10u,0x50u,0x9300u,0xab00u,0x90u,0xd0u,0x9310u,0xab10u,0x100u,0x140u,0x9320u,0xab20u,0x180u,0x1c0u,0x9330u,0xab30u,0x230u,0x270u,0x9340u,0xab40u,0x2b0u,0x2f0u,0x9350u,0xab50u,0x320u,0x360u,0x9360u,0xab60u,0x3a0u,0x3e0u,0x9370u,0xab70u,0x450u,0x410u,0x9380u,0xab80u,0x4d0u,0x490u,0x9390u,0xab90u,0x540u,0x500u,0x93a0u,0xaba0u,0x5c0u,0x580u,0x93b0u,0xabb0u,0x670u,0x630u,0x93c0u,0xabc0u,0x6f0u,0x6b0u,0x93d0u,0xabd0u,0x760u,0x720u,0x93e0u,0xabe0u,0x7e0u,0x7a0u,0x93f0u,0xabf0u,0x20u,0x60u,0x9600u,0xae00u,0xa0u,0xe0u,0x9610u,0xae10u,0x130u,0x170u,0x9620u,0xae20u,0x1b0u,0x1f0u,0x9630u,0xae30u,0x200u,0x240u,0x9640u,0xae40u,0x280u,0x2c0u,0x9650u,0xae50u,0x310u,0x350u,0x9660u,0xae60u,0x390u,0x3d0u,0x9670u,0xae70u,0x460u,0x420u,0x9680u,0xae80u,0x4e0u,0x4a0u,0x9690u,0xae90u,0x570u,0x530u,0x96a0u,0xaea0u,0x5f0u,0x5b0u,0x96b0u,0xaeb0u,0x640u,0x600u,0x96c0u,0xaec0u,0x6c0u,0x680u,0x96d0u,0xaed0u,0x750u,0x710u,0x96e0u,0xaee0u,0x7d0u,0x790u,0x96f0u,0xaef0u,0x30u,0x70u,0x9900u,0xb100u,0xb0u,0xf0u,0x9910u,0xb110u,0x120u,0x160u,0x9920u,0xb120u,0x1a0u,0x1e0u,0x9930u,0xb130u,0x210u,0x250u,0x9940u,0xb140u,0x290u,0x2d0u,0x9950u,0xb150u,0x300u,0x340u,0x9960u,0xb160u,0x380u,0x3c0u,0x9970u,0xb170u,0x470u,0x430u,0x9980u,0xb180u,0x4f0u,0x4b0u,0x9990u,0xb190u,0x560u,0x520u,0x99a0u,0xb1a0u,0x5e0u,0x5a0u,0x99b0u,0xb1b0u,0x650u,0x610u,0x99c0u,0xb1c0u,0x6d0u,0x690u,0x99d0u,0xb1d0u,0x740u,0x700u,0x99e0u,0xb1e0u,0x7c0u,0x780u,0x99f0u,0xb1f0u,0x9c08u,0x12060u,0x120a0u,0x12400u,0x9c18u,0x121f0u,0x12230u,0x12410u,0x9c28u,0x12340u,0x12380u,0x12420u,0x9c38u,0x124d0u,0x12510u,0x12430u,0x9c48u,0x12620u,0x126e0u,0x12440u,0x9c58u,0x127b0u,0x12870u,0x12450u,0x9c68u,0x12900u,0x129c0u,0x12460u,0x9c78u,0x12a90u,0x12b50u,0x12470u,0x9c88u,0x12c60u,0x12ca0u,0x12480u,0x9c98u,0x12df0u,0x12e30u,0x12490u,0x9ca8u,0x12f40u,0x12f80u,0x124a0u,0x9cb8u,0x130d0u,0x13110u,0x124b0u,0x9cc8u,0x13220u,0x132e0u,0x124c0u,0x9cd8u,0x133b0u,0x13470u,0x124d0u,0x9ce8u,0x13500u,0x135c0u,0x124e0u,0x9cf8u,0x13690u,0x13750u,0x124f0u,0x9f08u,0x12070u,0x120a8u,0x12500u,0x9f18u,0x121e0u,0x12238u,0x12510u,0x9f28u,0x12350u,0x12388u,0x12520u,0x9f38u,0x124c0u,0x12518u,0x12530u,0x9f48u,0x12630u,0x126e8u,0x12540u,0x9f58u,0x127a0u,0x12878u,0x12550u,0x9f68u,0x12910u,0x129c8u,0x12560u,0x9f78u,0x12a80u,0x12b58u,0x12570u,0x9f88u,0x12c70u,0x12ca8u,0x12580u,0x9f98u,0x12de0u,0x12e38u,0x12590u,0x9fa8u,0x12f50u,0x12f88u,0x125a0u,0x9fb8u,0x130c0u,0x13118u,0x125b0u,0x9fc8u,0x13230u,0x132e8u,0x125c0u,0x9fd8u,0x133a0u,0x13478u,0x125d0u,0x9fe8u,0x13510u,0x135c8u,0x125e0u,0x9ff8u,0x13680u,0x13758u,0x125f0u,0xa208u,0x12080u,0x120b0u,0x12600u,0xa218u,0x12210u,0x12220u,0x12610u,0xa228u,0x123a0u,0x12390u,0x12620u,0xa238u,0x12530u,0x12500u,0x12630u,0xa248u,0x126c0u,0x126f0u,0x12640u,0xa258u,0x12850u,0x12860u,0x12650u,0xa268u,0x129e0u,0x129d0u,0x12660u,0xa278u,0x12b70u,0x12b40u,0x12670u,0xa288u,0x12c80u,0x12cb0u,0x12680u,0xa298u,0x12e10u,0x12e20u,0x12690u,0xa2a8u,0x12fa0u,0x12f90u,0x126a0u,0xa2b8u,0x13130u,0x13100u,0x126b0u,0xa2c8u,0x132c0u,0x132f0u,0x126c0u,0xa2d8u,0x13450u,0x13460u,0x126d0u,0xa2e8u,0x135e0u,0x135d0u,0x126e0u,0xa2f8u,0x13770u,0x13740u,0x126f0u,0xa508u,0x12090u,0x120b8u,0x12700u,0xa518u,0x12200u,0x12228u,0x12710u,0xa528u,0x123b0u,0x12398u,0x12720u,0xa538u,0x12520u,0x12508u,0x12730u,0xa548u,0x126d0u,0x126f8u,0x12740u,0xa558u,0x12840u,0x12868u,0x12750u,0xa568u,0x129f0u,0x129d8u,0x12760u,0xa578u,0x12b60u,0x12b48u,0x12770u,0xa588u,0x12c90u,0x12cb8u,0x12780u,0xa598u,0x12e00u,0x12e28u,0x12790u,0xa5a8u,0x12fb0u,0x12f98u,0x127a0u,0xa5b8u,0x13120u,0x13108u,0x127b0u,0xa5c8u,0x132d0u,0x132f8u,0x127c0u,0xa5d8u,0x13440u,0x13468u,0x127d0u,0xa5e8u,0x135f0u,0x135d8u,0x127e0u,0xa5f8u,0x13760u,0x13748u,0x127f0u,0x5000u,0x1c300u,0x1e8c0u,0x1eb60u,0x5080u,0x1c310u,0x100000u,0x1eb62u,0x5110u,0x1c320u,0x100000u,0x1eb64u,0x5190u,0x1c330u,0x100000u,0x1eb66u,0x5220u,0x1c340u,0x1e8c0u,0x1eb68u,0x52a0u,0x1c350u,0x100000u,0x1eb6au,0x5330u,0x1c360u,0x100000u,0x1eb6cu,0x53b0u,0x1c370u,0x100000u,0x1eb6eu,0x5440u,0x1c380u,0x1e8c0u,0x1eb70u,0x54c0u,0x1c390u,0x100000u,0x1eb72u,0x5550u,0x1c3a0u,0x100000u,0x1eb74u,0x55d0u,0x1c3b0u,0x100000u,0x1eb76u,0x5660u,0x1c3c0u,0x1e8c0u,0x1eb78u,0x56e0u,0x1c3d0u,0x100000u,0x1eb7au,0x5770u,0x1c3e0u,0x100000u,0x1eb7cu,0x57f0u,0x1c3f0u,0x100000u,0x1eb7eu,0x5010u,0x1c400u,0x1e8f0u,0x1eb60u,0x5090u,0x1c410u,0x100000u,0x1eb62u,0x5100u,0x1c420u,0x100000u,0x1eb64u,0x5180u,0x1c430u,0x100000u,0x1eb66u,0x5230u,0x1c440u,0x1e8f0u,0x1eb68u,0x52b0u,0x1c450u,0x100000u,0x1eb6au,0x5320u,0x1c460u,0x100000u,0x1eb6cu,0x53a0u,0x1c470u,0x100000u,0x1eb6eu,0x5450u,0x1c480u,0x1e8f0u,0x1eb70u,0x54d0u,0x1c490u,0x100000u,0x1eb72u,0x5540u,0x1c4a0u,0x100000u,0x1eb74u,0x55c0u,0x1c4b0u,0x100000u,0x1eb76u,0x5670u,0x1c4c0u,0x1e8f0u,0x1eb78u,0x56f0u,0x1c4d0u,0x100000u,0x1eb7au,0x5760u,0x1c4e0u,0x100000u,0x1eb7cu,0x57e0u,0x1c4f0u,0x100000u,0x1eb7eu,0x5020u,0x1c500u,0x1e920u,0x1eb60u,0x50a0u,0x1c510u,0x100000u,0x1eb62u,0x5130u,0x1c520u,0x100000u,0x1eb64u,0x51b0u,0x1c530u,0x100000u,0x1eb66u,0x5200u,0x1c540u,0x1e920u,0x1eb68u,0x5280u,0x1c550u,0x100000u,0x1eb6au,0x5310u,0x1c560u,0x100000u,0x1eb6cu,0x5390u,0x1c570u,0x100000u,0x1eb6eu,0x5460u,0x1c580u,0x1e920u,0x1eb70u,0x54e0u,0x1c590u,0x100000u,0x1eb72u,0x5570u,0x1c5a0u,0x100000u,0x1eb74u,0x55f0u,0x1c5b0u,0x100000u,0x1eb76u,0x5640u,0x1c5c0u,0x1e920u,0x1eb78u,0x56c0u,0x1c5d0u,0x100000u,0x1eb7au,0x5750u,0x1c5e0u,0x100000u,0x1eb7cu,0x57d0u,0x1c5f0u,0x100000u,0x1eb7eu,0x5030u,0x1c600u,0x1e950u,0x1eb60u,0x50b0u,0x1c610u,0x100000u,0x1eb62u,0x5120u,0x1c620u,0x100000u,0x1eb64u,0x51a0u,0x1c630u,0x100000u,0x1eb66u,0x5210u,0x1c640u,0x1e950u,0x1eb68u,0x5290u,0x1c650u,0x100000u,0x1eb6au,0x5300u,0x1c660u,0x100000u,0x1eb6cu,0x5380u,0x1c670u,0x100000u,0x1eb6eu,0x5470u,0x1c680u,0x1e950u,0x1eb70u,0x54f0u,0x1c690u,0x100000u,0x1eb72u,0x5560u,0x1c6a0u,0x100000u,0x1eb74u,0x55e0u,0x1c6b0u,0x100000u,0x1eb76u,0x5650u,0x1c6c0u,0x1e950u,0x1eb78u,0x56d0u,0x1c6d0u,0x100000u,0x1eb7au,0x5740u,0x1c6e0u,0x100000u,0x1eb7cu,0x57c0u,0x1c6f0u,0x100000u,0x1eb7eu,0x25800u,0x1d380u,0x1d380u,0xf0140u,0x25802u,0x1d382u,0x100000u,0xf0150u,0x25804u,0x1d384u,0x100000u,0xf0160u,0x25806u,0x1d386u,0x100000u,0xf0170u,0x25a00u,0x1d390u,0x1d380u,0xf0100u,0x25a02u,0x1d392u,0x100000u,0xf0110u,0x25a04u,0x1d394u,0x100000u,0xf0120u,0x25a06u,0x1d396u,0x100000u,0xf0130u,0x25c00u,0x1d3a0u,0x1d380u,0xf02c0u,0x25c02u,0x1d3a2u,0x100000u,0xf02d0u,0x25c04u,0x1d3a4u,0x100000u,0xf02e0u,0x25c06u,0x1d3a6u,0x100000u,0xf02f0u,0x25e00u,0x1d3b0u,0x1d380u,0xf0280u,0x25e02u,0x1d3b2u,0x100000u,0xf0290u,0x25e04u,0x1d3b4u,0x100000u,0xf02a0u,0x25e06u,0x1d3b6u,0x100000u,0xf02b0u,0x25808u,0x1d388u,0x1d390u,0x11f850u,0x2580au,0x1d38au,0x100000u,0x11f840u,0x2580cu,0x1d38cu,0x100000u,0x11f870u,0x2580eu,0x1d38eu,0x100000u,0x11f860u,0x25a08u,0x1d398u,0x1d390u,0x11f810u,0x25a0au,0x1d39au,0x100000u,0x11f800u,0x25a0cu,0x1d39cu,0x100000u,0x11f830u,0x25a0eu,0x1d39eu,0x100000u,0x11f820u,0x25c08u,0x1d3a8u,0x1d390u,0x11f9d0u,0x25c0au,0x1d3aau,0x100000u,0x11f9c0u,0x25c0cu,0x1d3acu,0x100000u,0x11f9f0u,0x25c0eu,0x1d3aeu,0x100000u,0x11f9e0u,0x25e08u,0x1d3b8u,0x1d390u,0x11f990u,0x25e0au,0x1d3bau,0x100000u,0x11f980u,0x25e0cu,0x1d3bcu,0x100000u,0x11f9b0u,0x25e0eu,0x1d3beu,0x100000u,0x11f9a0u,0x25008u,0x1d3c0u,0x1d3a0u,0x11fb60u,0x2500au,0x1d3c2u,0x100000u,0x11fb70u,0x2500cu,0x1d3c4u,0x100000u,0x11fb40u,0x2500eu,0x1d3c6u,0x100000u,0x11fb50u,0x25208u,0x1d3d0u,0x1d3a0u,0x11fb20u,0x2520au,0x1d3d2u,0x100000u,0x11fb30u,0x2520cu,0x1d3d4u,0x100000u,0x11fb00u,0x2520eu,0x1d3d6u,0x100000u,0x11fb10u,0x25408u,0x1d3e0u,0x1d3a0u,0x11fce0u,0x2540au,0x1d3e2u,0x100000u,0x11fcf0u,0x2540cu,0x1d3e4u,0x100000u,0x11fcc0u,0x2540eu,0x1d3e6u,0x100000u,0x11fcd0u,0x25608u,0x1d3f0u,0x1d3a0u,0x11fca0u,0x2560au,0x1d3f2u,0x100000u,0x11fcb0u,0x2560cu,0x1d3f4u,0x100000u,0x11fc80u,0x2560eu,0x1d3f6u,0x100000u,0x11fc90u,0x100000u,0x1d3c8u,0x1d3b0u,0x11fe70u,0x100000u,0x1d3cau,0x100000u,0x11fe60u,0x100000u,0x1d3ccu,0x100000u,0x11fe50u,0x100000u,0x1d3ceu,0x100000u,0x11fe40u,0x100000u,0x1d3d8u,0x1d3b0u,0x11fe30u,0x100000u,0x1d3dau,0x100000u,0x11fe20u,0x100000u,0x1d3dcu,0x100000u,0x11fe10u,0x100000u,0x1d3deu,0x100000u,0x11fe00u,0x100000u,0x1d3e8u,0x1d3b0u,0x11fff0u,0x100000u,0x1d3eau,0x100000u,0x11ffe0u,0x100000u,0x1d3ecu,0x100000u,0x11ffd0u,0x100000u,0x1d3eeu,0x100000u,0x11ffc0u,0x100000u,0x1d3f8u,0x1d3b0u,0x11ffb0u,0x100000u,0x1d3fau,0x100000u,0x11ffa0u,0x100000u,0x1d3fcu,0x100000u,0x11ff90u,0x100000u,0x1d3feu,0x100000u,0x11ff80u,0x420u,0x30850u,0x90000u,0xc0420u,0x430u,0x30840u,0x90010u,0xc0430u,0x400u,0x30870u,0x90020u,0xc0400u,0x410u,0x30860u,0x90030u,0xc0410u,0x460u,0x30810u,0x90040u,0xc0460u,0x470u,0x30800u,0x90050u,0xc0470u,0x440u,0x30830u,0x90060u,0xc0440u,0x450u,0x30820u,0x90070u,0xc0450u,0x4b0u,0x308d0u,0x90080u,0xef8b0u,0x4a0u,0x308c0u,0x90090u,0xef8a0u,0x490u,0x308f0u,0x900a0u,0xef890u,0x480u,0x308e0u,0x900b0u,0xef880u,0x4f0u,0x30890u,0x900c0u,0xef8f0u,0x4e0u,0x30880u,0x900d0u,0xef8e0u,0x4d0u,0x308b0u,0x900e0u,0xef8d0u,0x4c0u,0x308a0u,0x900f0u,0xef8c0u,0x530u,0x5fd60u,0x90100u,0xef930u,0x520u,0x5fd70u,0x90110u,0xef920u,0x510u,0x5fd40u,0x90120u,0xef910u,0x500u,0x5fd50u,0x90130u,0xef900u,0x570u,0x5fd20u,0x90140u,0xef970u,0x560u,0x5fd30u,0x90150u,0xef960u,0x550u,0x5fd00u,0x90160u,0xef950u,0x540u,0x5fd10u,0x90170u,0xef940u,0x5b0u,0x5fde0u,0x90190u,0xef9b0u,0x5a0u,0x5fdf0u,0x90180u,0xef9a0u,0x590u,0x5fdc0u,0x901b0u,0xef990u,0x580u,0x5fdd0u,0x901a0u,0xef980u,0x5f0u,0x5fda0u,0x901d0u,0xef9f0u,0x5e0u,0x5fdb0u,0x901c0u,0xef9e0u,0x5d0u,0x5fd80u,0x901f0u,0xef9d0u,0x5c0u,0x5fd90u,0x901e0u,0xef9c0u,0x640u,0x5fe60u,0x90210u,0xefa40u,0x650u,0x5fe70u,0x90200u,0xefa50u,0x660u,0x5fe40u,0x90230u,0xefa60u,0x670u,0x5fe50u,0x90220u,0xefa70u,0x600u,0x5fe20u,0x90250u,0xefa00u,0x610u,0x5fe30u,0x90240u,0xefa10u,0x620u,0x5fe00u,0x90270u,0xefa20u,0x630u,0x5fe10u,0x90260u,0xefa30u,0x6c0u,0x5fef0u,0x90290u,0xefac0u,0x6d0u,0x5fee0u,0x90280u,0xefad0u,0x6e0u,0x5fed0u,0x902b0u,0xefae0u,0x6f0u,0x5fec0u,0x902a0u,0xefaf0u,0x680u,0x5feb0u,0x902d0u,0xefa80u,0x690u,0x5fea0u,0x902c0u,0xefa90u,0x6a0u,0x5fe90u,0x902f0u,0xefaa0u,0x6b0u,0x5fe80u,0x902e0u,0xefab0u,0x740u,0x5ff70u,0x90320u,0xefb40u,0x750u,0x5ff60u,0x90330u,0xefb50u,0x760u,0x5ff50u,0x90300u,0xefb60u,0x770u,0x5ff40u,0x90310u,0xefb70u,0x700u,0x5ff30u,0x90360u,0xefb00u,0x710u,0x5ff20u,0x90370u,0xefb10u,0x720u,0x5ff10u,0x90340u,0xefb20u,0x730u,0x5ff00u,0x90350u,0xefb30u,0x7d0u,0x5fff0u,0x903a0u,0xefbd0u,0x7c0u,0x5ffe0u,0x903b0u,0xefbc0u,0x7f0u,0x5ffd0u,0x90380u,0xefbf0u,0x7e0u,0x5ffc0u,0x90390u,0xefbe0u,0x790u,0x5ffb0u,0x903e0u,0xefb90u,0x780u,0x5ffa0u,0x903f0u,0xefb80u,0x7b0u,0x5ff90u,0x903c0u,0xefbb0u,0x7a0u,0x5ff80u,0x903d0u,0xefba0u,0x0u,0x11fc50u,0x0u,0x7230cu,0x0u,0x11fc40u,0x0u,0x7230cu,0x0u,0x11fc70u,0x0u,0x7230cu,0x0u,0x11fc60u,0x0u,0x7230cu,0x0u,0x11fc10u,0x0u,0x7230cu,0x0u,0x11fc00u,0x0u,0x7230cu,0x0u,0x11fc30u,0x0u,0x7230cu,0x0u,0x11fc20u,0x0u,0x7230cu,0x0u,0x11fcd0u,0x0u,0x5232cu,0x0u,0x11fcc0u,0x0u,0x5232cu,0x0u,0x11fcf0u,0x0u,0x5232cu,0x0u,0x11fce0u,0x0u,0x5232cu,0x0u,0x11fc90u,0x0u,0x5232cu,0x0u,0x11fc80u,0x0u,0x5232cu,0x0u,0x11fcb0u,0x0u,0x5232cu,0x0u,0x11fca0u,0x0u,0x5232cu,0x0u,0x11fd60u,0x0u,0xd0124u,0x0u,0x11fd70u,0x0u,0xd0124u,0x0u,0x11fd40u,0x0u,0xd0124u,0x0u,0x11fd50u,0x0u,0xd0124u,0x0u,0x11fd20u,0x0u,0xd0124u,0x0u,0x11fd30u,0x0u,0xd0124u,0x0u,0x11fd00u,0x0u,0xd0124u,0x0u,0x11fd10u,0x0u,0xd0124u,0x0u,0x11fde0u,0x0u,0xd81a6u,0x0u,0x11fdf0u,0x0u,0xd81a6u,0x0u,0x11fdc0u,0x0u,0xd81a6u,0x0u,0x11fdd0u,0x0u,0xd81a6u,0x0u,0x11fda0u,0x0u,0xd81a6u,0x0u,0x11fdb0u,0x0u,0xd81a6u,0x0u,0x11fd80u,0x0u,0xd81a6u,0x0u,0x11fd90u,0x0u,0xd81a6u,0x0u,0x11fe60u,0x0u,0xd8996u,0x0u,0x11fe70u,0x0u,0xd8996u,0x0u,0x11fe40u,0x0u,0xd8996u,0x0u,0x11fe50u,0x0u,0xd8996u,0x0u,0x11fe20u,0x0u,0xd8996u,0x0u,0x11fe30u,0x0u,0xd8996u,0x0u,0x11fe00u,0x0u,0xd8996u,0x0u,0x11fe10u,0x0u,0xd8996u,0x0u,0x11fef0u,0x0u,0xd8996u,0x0u,0x11fee0u,0x0u,0xd8996u,0x0u,0x11fed0u,0x0u,0xd8996u,0x0u,0x11fec0u,0x0u,0xd8996u,0x0u,0x11feb0u,0x0u,0xd8996u,0x0u,0x11fea0u,0x0u,0xd8996u,0x0u,0x11fe90u,0x0u,0xd8996u,0x0u,0x11fe80u,0x0u,0xd8996u,0x0u,0x11ff70u,0x0u,0xd8b1cu,0x0u,0x11ff60u,0x0u,0xd8b1cu,0x0u,0x11ff50u,0x0u,0xd8b1cu,0x0u,0x11ff40u,0x0u,0xd8b1cu,0x0u,0x11ff30u,0x0u,0xd8b1cu,0x0u,0x11ff20u,0x0u,0xd8b1cu,0x0u,0x11ff10u,0x0u,0xd8b1cu,0x0u,0x11ff00u,0x0u,0xd8b1cu,0x0u,0x11fff0u,0x0u,0xd8b1cu,0x0u,0x11ffe0u,0x0u,0xd8b1cu,0x0u,0x11ffd0u,0x0u,0xd8b1cu,0x0u,0x11ffc0u,0x0u,0xd8b1cu,0x0u,0x11ffb0u,0x0u,0xd8b1cu,0x0u,0x11ffa0u,0x0u,0xd8b1cu,0x0u,0x11ff90u,0x0u,0xd8b1cu,0x0u,0x11ff80u,0x0u,0xd8b1cu,0x5840u,0x5860u,0x6800u,0x5840u,0x5848u,0x5868u,0x6808u,0x5850u,0x5850u,0x5870u,0x6810u,0x5860u,0x5858u,0x5878u,0x6818u,0x5870u,0x58c0u,0x58e0u,0x6880u,0x58c0u,0x58c8u,0x58e8u,0x6888u,0x58d0u,0x58d0u,0x58f0u,0x6890u,0x58e0u,0x58d8u,0x58f8u,0x6898u,0x58f0u,0x5960u,0x5940u,0x6920u,0x5960u,0x5968u,0x5948u,0x6928u,0x5970u,0x5970u,0x5950u,0x6930u,0x5940u,0x5978u,0x5958u,0x6938u,0x5950u,0x59e0u,0x59c0u,0x69a0u,0x59e0u,0x59e8u,0x59c8u,0x69a8u,0x59f0u,0x59f0u,0x59d0u,0x69b0u,0x59c0u,0x59f8u,0x59d8u,0x69b8u,0x59d0u,0x5c00u,0x5c20u,0x6c40u,0x5a40u,0x5c08u,0x5c28u,0x6c48u,0x5a50u,0x5c10u,0x5c30u,0x6c50u,0x5a60u,0x5c18u,0x5c38u,0x6c58u,0x5a70u,0x5c80u,0x5ca0u,0x6cc0u,0x5ac0u,0x5c88u,0x5ca8u,0x6cc8u,0x5ad0u,0x5c90u,0x5cb0u,0x6cd0u,0x5ae0u,0x5c98u,0x5cb8u,0x6cd8u,0x5af0u,0x5d20u,0x5d00u,0x6d60u,0x5b60u,0x5d28u,0x5d08u,0x6d68u,0x5b70u,0x5d30u,0x5d10u,0x6d70u,0x5b40u,0x5d38u,0x5d18u,0x6d78u,0x5b50u,0x5da0u,0x5d80u,0x6de0u,0x5be0u,0x5da8u,0x5d88u,0x6de8u,0x5bf0u,0x5db0u,0x5d90u,0x6df0u,0x5bc0u,0x5db8u,0x5d98u,0x6df8u,0x5bd0u,0x6040u,0x6060u,0x7000u,0x5c00u,0x6048u,0x6068u,0x7008u,0x5c10u,0x6050u,0x6070u,0x7010u,0x5c20u,0x6058u,0x6078u,0x7018u,0x5c30u,0x60c0u,0x60e0u,0x7080u,0x5c80u,0x60c8u,0x60e8u,0x7088u,0x5c90u,0x60d0u,0x60f0u,0x7090u,0x5ca0u,0x60d8u,0x60f8u,0x7098u,0x5cb0u,0x6160u,0x6140u,0x7120u,0x5d20u,0x6168u,0x6148u,0x7128u,0x5d30u,0x6170u,0x6150u,0x7130u,0x5d00u,0x6178u,0x6158u,0x7138u,0x5d10u,0x61e0u,0x61c0u,0x71a0u,0x5da0u,0x61e8u,0x61c8u,0x71a8u,0x5db0u,0x61f0u,0x61d0u,0x71b0u,0x5d80u,0x61f8u,0x61d8u,0x71b8u,0x5d90u,0x6400u,0x6420u,0x7440u,0x5e00u,0x6408u,0x6428u,0x7448u,0x5e10u,0x6410u,0x6430u,0x7450u,0x5e20u,0x6418u,0x6438u,0x7458u,0x5e30u,0x6480u,0x64a0u,0x74c0u,0x5e80u,0x6488u,0x64a8u,0x74c8u,0x5e90u,0x6490u,0x64b0u,0x74d0u,0x5ea0u,0x6498u,0x64b8u,0x74d8u,0x5eb0u,0x6520u,0x6500u,0x7560u,0x5f20u,0x6528u,0x6508u,0x7568u,0x5f30u,0x6530u,0x6510u,0x7570u,0x5f00u,0x6538u,0x6518u,0x7578u,0x5f10u,0x65a0u,0x6580u,0x75e0u,0x5fa0u,0x65a8u,0x6588u,0x75e8u,0x5fb0u,0x65b0u,0x6590u,0x75f0u,0x5f80u,0x65b8u,0x6598u,0x75f8u,0x5f90u,0x6800u,0x1e980u,0x1ebc0u,0x1800u,0x6808u,0x1ee10u,0x1f050u,0x1810u,0x6810u,0x1f2a0u,0x1f4e0u,0x1820u,0x6818u,0x1f730u,0x1f970u,0x1830u,0x6880u,0x1fbc0u,0x1fe00u,0x1840u,0x6888u,0x20050u,0x20290u,0x1850u,0x6890u,0x204e0u,0x20720u,0x1860u,0x6898u,0x20970u,0x20bb0u,0x1870u,0x6920u,0x20e00u,0x21040u,0x1980u,0x6928u,0x21290u,0x214d0u,0x1990u,0x6930u,0x21720u,0x21960u,0x19a0u,0x6938u,0x21bb0u,0x21df0u,0x19b0u,0x69a0u,0x22040u,0x22280u,0x19c0u,0x69a8u,0x224d0u,0x22710u,0x19d0u,0x69b0u,0x22960u,0x22ba0u,0x19e0u,0x69b8u,0x22df0u,0x23030u,0x19f0u,0x6a00u,0x1e9b0u,0x1ebc8u,0x1b20u,0x6a08u,0x1ee40u,0x1f058u,0x1b30u,0x6a10u,0x1f2d0u,0x1f4e8u,0x1b00u,0x6a18u,0x1f760u,0x1f978u,0x1b10u,0x6a80u,0x1fbf0u,0x1fe08u,0x1b60u,0x6a88u,0x20080u,0x20298u,0x1b70u,0x6a90u,0x20510u,0x20728u,0x1b40u,0x6a98u,0x209a0u,0x20bb8u,0x1b50u,0x6b20u,0x20e30u,0x21048u,0x1ca0u,0x6b28u,0x212c0u,0x214d8u,0x1cb0u,0x6b30u,0x21750u,0x21968u,0x1c80u,0x6b38u,0x21be0u,0x21df8u,0x1c90u,0x6ba0u,0x22070u,0x22288u,0x1ce0u,0x6ba8u,0x22500u,0x22718u,0x1cf0u,0x6bb0u,0x22990u,0x22ba8u,0x1cc0u,0x6bb8u,0x22e20u,0x23038u,0x1cd0u,0x6c40u,0x1e9e0u,0x1ebd0u,0x1e00u,0x6c48u,0x1ee70u,0x1f060u,0x1e10u,0x6c50u,0x1f300u,0x1f4f0u,0x1e20u,0x6c58u,0x1f790u,0x1f980u,0x1e30u,0x6cc0u,0x1fc20u,0x1fe10u,0x1e40u,0x6cc8u,0x200b0u,0x202a0u,0x1e50u,0x6cd0u,0x20540u,0x20730u,0x1e60u,0x6cd8u,0x209d0u,0x20bc0u,0x1e70u,0x6d60u,0x20e60u,0x21050u,0x1f80u,0x6d68u,0x212f0u,0x214e0u,0x1f90u,0x6d70u,0x21780u,0x21970u,0x1fa0u,0x6d78u,0x21c10u,0x21e00u,0x1fb0u,0x6de0u,0x220a0u,0x22290u,0x1fc0u,0x6de8u,0x22530u,0x22720u,0x1fd0u,0x6df0u,0x229c0u,0x22bb0u,0x1fe0u,0x6df8u,0x22e50u,0x23040u,0x1ff0u,0x6e40u,0x1ea10u,0x1ebd8u,0x2120u,0x6e48u,0x1eea0u,0x1f068u,0x2130u,0x6e50u,0x1f330u,0x1f4f8u,0x2100u,0x6e58u,0x1f7c0u,0x1f988u,0x2110u,0x6ec0u,0x1fc50u,0x1fe18u,0x2160u,0x6ec8u,0x200e0u,0x202a8u,0x2170u,0x6ed0u,0x20570u,0x20738u,0x2140u,0x6ed8u,0x20a00u,0x20bc8u,0x2150u,0x6f60u,0x20e90u,0x21058u,0x22a0u,0x6f68u,0x21320u,0x214e8u,0x22b0u,0x6f70u,0x217b0u,0x21978u,0x2280u,0x6f78u,0x21c40u,0x21e08u,0x2290u,0x6fe0u,0x220d0u,0x22298u,0x22e0u,0x6fe8u,0x22560u,0x22728u,0x22f0u,0x6ff0u,0x229f0u,0x22bb8u,0x22c0u,0x6ff8u,0x22e80u,0x23048u,0x22d0u,0x0u,0x40u,0x9000u,0xb400u,0x80u,0xc0u,0x9010u,0xb410u,0x110u,0x150u,0x9020u,0xb420u,0x190u,0x1d0u,0x9030u,0xb430u,0x220u,0x260u,0x9040u,0xb440u,0x2a0u,0x2e0u,0x9050u,0xb450u,0x330u,0x370u,0x9060u,0xb460u,0x3b0u,0x3f0u,0x9070u,0xb470u,0x440u,0x400u,0x9080u,0xb480u,0x4c0u,0x480u,0x9090u,0xb490u,0x550u,0x510u,0x90a0u,0xb4a0u,0x5d0u,0x590u,0x90b0u,0xb4b0u,0x660u,0x620u,0x90c0u,0xb4c0u,0x6e0u,0x6a0u,0x90d0u,0xb4d0u,0x770u,0x730u,0x90e0u,0xb4e0u,0x7f0u,0x7b0u,0x90f0u,0xb4f0u,0x10u,0x50u,0x9300u,0xb700u,0x90u,0xd0u,0x9310u,0xb710u,0x100u,0x140u,0x9320u,0xb720u,0x180u,0x1c0u,0x9330u,0xb730u,0x230u,0x270u,0x9340u,0xb740u,0x2b0u,0x2f0u,0x9350u,0xb750u,0x320u,0x360u,0x9360u,0xb760u,0x3a0u,0x3e0u,0x9370u,0xb770u,0x450u,0x410u,0x9380u,0xb780u,0x4d0u,0x490u,0x9390u,0xb790u,0x540u,0x500u,0x93a0u,0xb7a0u,0x5c0u,0x580u,0x93b0u,0xb7b0u,0x670u,0x630u,0x93c0u,0xb7c0u,0x6f0u,0x6b0u,0x93d0u,0xb7d0u,0x760u,0x720u,0x93e0u,0xb7e0u,0x7e0u,0x7a0u,0x93f0u,0xb7f0u,0x20u,0x60u,0x9600u,0xba00u,0xa0u,0xe0u,0x9610u,0xba10u,0x130u,0x170u,0x9620u,0xba20u,0x1b0u,0x1f0u,0x9630u,0xba30u,0x200u,0x240u,0x9640u,0xba40u,0x280u,0x2c0u,0x9650u,0xba50u,0x310u,0x350u,0x9660u,0xba60u,0x390u,0x3d0u,0x9670u,0xba70u,0x460u,0x420u,0x9680u,0xba80u,0x4e0u,0x4a0u,0x9690u,0xba90u,0x570u,0x530u,0x96a0u,0xbaa0u,0x5f0u,0x5b0u,0x96b0u,0xbab0u,0x640u,0x600u,0x96c0u,0xbac0u,0x6c0u,0x680u,0x96d0u,0xbad0u,0x750u,0x710u,0x96e0u,0xbae0u,0x7d0u,0x790u,0x96f0u,0xbaf0u,0x30u,0x70u,0x9900u,0xbd00u,0xb0u,0xf0u,0x9910u,0xbd10u,0x120u,0x160u,0x9920u,0xbd20u,0x1a0u,0x1e0u,0x9930u,0xbd30u,0x210u,0x250u,0x9940u,0xbd40u,0x290u,0x2d0u,0x9950u,0xbd50u,0x300u,0x340u,0x9960u,0xbd60u,0x380u,0x3c0u,0x9970u,0xbd70u,0x470u,0x430u,0x9980u,0xbd80u,0x4f0u,0x4b0u,0x9990u,0xbd90u,0x560u,0x520u,0x99a0u,0xbda0u,0x5e0u,0x5a0u,0x99b0u,0xbdb0u,0x650u,0x610u,0x99c0u,0xbdc0u,0x6d0u,0x690u,0x99d0u,0xbdd0u,0x740u,0x700u,0x99e0u,0xbde0u,0x7c0u,0x780u,0x99f0u,0xbdf0u,0xc000u,0x120c0u,0x12100u,0x12800u,0xc010u,0x12250u,0x12290u,0x12810u,0xc020u,0x123e0u,0x12420u,0x12820u,0xc030u,0x12570u,0x125b0u,0x12830u,0xc040u,0x12680u,0x12740u,0x12840u,0xc050u,0x12810u,0x128d0u,0x12850u,0xc060u,0x129a0u,0x12a60u,0x12860u,0xc070u,0x12b30u,0x12bf0u,0x12870u,0xc080u,0x12cc0u,0x12d00u,0x12880u,0xc090u,0x12e50u,0x12e90u,0x12890u,0xc0a0u,0x12fe0u,0x13020u,0x128a0u,0xc0b0u,0x13170u,0x131b0u,0x128b0u,0xc0c0u,0x13280u,0x13340u,0x128c0u,0xc0d0u,0x13410u,0x134d0u,0x128d0u,0xc0e0u,0x135a0u,0x13660u,0x128e0u,0xc0f0u,0x13730u,0x137f0u,0x128f0u,0xc300u,0x120d0u,0x12108u,0x12900u,0xc310u,0x12240u,0x12298u,0x12910u,0xc320u,0x123f0u,0x12428u,0x12920u,0xc330u,0x12560u,0x125b8u,0x12930u,0xc340u,0x12690u,0x12748u,0x12940u,0xc350u,0x12800u,0x128d8u,0x12950u,0xc360u,0x129b0u,0x12a68u,0x12960u,0xc370u,0x12b20u,0x12bf8u,0x12970u,0xc380u,0x12cd0u,0x12d08u,0x12980u,0xc390u,0x12e40u,0x12e98u,0x12990u,0xc3a0u,0x12ff0u,0x13028u,0x129a0u,0xc3b0u,0x13160u,0x131b8u,0x129b0u,0xc3c0u,0x13290u,0x13348u,0x129c0u,0xc3d0u,0x13400u,0x134d8u,0x129d0u,0xc3e0u,0x135b0u,0x13668u,0x129e0u,0xc3f0u,0x13720u,0x137f8u,0x129f0u,0xc600u,0x120e0u,0x12110u,0x12a00u,0xc610u,0x12270u,0x12280u,0x12a10u,0xc620u,0x123c0u,0x12430u,0x12a20u,0xc630u,0x12550u,0x125a0u,0x12a30u,0xc640u,0x126a0u,0x12750u,0x12a40u,0xc650u,0x12830u,0x128c0u,0x12a50u,0xc660u,0x12980u,0x12a70u,0x12a60u,0xc670u,0x12b10u,0x12be0u,0x12a70u,0xc680u,0x12ce0u,0x12d10u,0x12a80u,0xc690u,0x12e70u,0x12e80u,0x12a90u,0xc6a0u,0x12fc0u,0x13030u,0x12aa0u,0xc6b0u,0x13150u,0x131a0u,0x12ab0u,0xc6c0u,0x132a0u,0x13350u,0x12ac0u,0xc6d0u,0x13430u,0x134c0u,0x12ad0u,0xc6e0u,0x13580u,0x13670u,0x12ae0u,0xc6f0u,0x13710u,0x137e0u,0x12af0u,0xc900u,0x120f0u,0x12118u,0x12b00u,0xc910u,0x12260u,0x12288u,0x12b10u,0xc920u,0x123d0u,0x12438u,0x12b20u,0xc930u,0x12540u,0x125a8u,0x12b30u,0xc940u,0x126b0u,0x12758u,0x12b40u,0xc950u,0x12820u,0x128c8u,0x12b50u,0xc960u,0x12990u,0x12a78u,0x12b60u,0xc970u,0x12b00u,0x12be8u,0x12b70u,0xc980u,0x12cf0u,0x12d18u,0x12b80u,0xc990u,0x12e60u,0x12e88u,0x12b90u,0xc9a0u,0x12fd0u,0x13038u,0x12ba0u,0xc9b0u,0x13140u,0x131a8u,0x12bb0u,0xc9c0u,0x132b0u,0x13358u,0x12bc0u,0xc9d0u,0x13420u,0x134c8u,0x12bd0u,0xc9e0u,0x13590u,0x13678u,0x12be0u,0xc9f0u,0x13700u,0x137e8u,0x12bf0u,0x7800u,0x1c300u,0x1e980u,0x1ebc0u,0x7880u,0x1c310u,0x100000u,0x1ebc2u,0x7910u,0x1c320u,0x100000u,0x1ebc4u,0x7990u,0x1c330u,0x100000u,0x1ebc6u,0x7a20u,0x1c340u,0x1e980u,0x1ebc8u,0x7aa0u,0x1c350u,0x100000u,0x1ebcau,0x7b30u,0x1c360u,0x100000u,0x1ebccu,0x7bb0u,0x1c370u,0x100000u,0x1ebceu,0x7c40u,0x1c380u,0x1e980u,0x1ebd0u,0x7cc0u,0x1c390u,0x100000u,0x1ebd2u,0x7d50u,0x1c3a0u,0x100000u,0x1ebd4u,0x7dd0u,0x1c3b0u,0x100000u,0x1ebd6u,0x7e60u,0x1c3c0u,0x1e980u,0x1ebd8u,0x7ee0u,0x1c3d0u,0x100000u,0x1ebdau,0x7f70u,0x1c3e0u,0x100000u,0x1ebdcu,0x7ff0u,0x1c3f0u,0x100000u,0x1ebdeu,0x7810u,0x1c400u,0x1e9b0u,0x1ebc0u,0x7890u,0x1c410u,0x100000u,0x1ebc2u,0x7900u,0x1c420u,0x100000u,0x1ebc4u,0x7980u,0x1c430u,0x100000u,0x1ebc6u,0x7a30u,0x1c440u,0x1e9b0u,0x1ebc8u,0x7ab0u,0x1c450u,0x100000u,0x1ebcau,0x7b20u,0x1c460u,0x100000u,0x1ebccu,0x7ba0u,0x1c470u,0x100000u,0x1ebceu,0x7c50u,0x1c480u,0x1e9b0u,0x1ebd0u,0x7cd0u,0x1c490u,0x100000u,0x1ebd2u,0x7d40u,0x1c4a0u,0x100000u,0x1ebd4u,0x7dc0u,0x1c4b0u,0x100000u,0x1ebd6u,0x7e70u,0x1c4c0u,0x1e9b0u,0x1ebd8u,0x7ef0u,0x1c4d0u,0x100000u,0x1ebdau,0x7f60u,0x1c4e0u,0x100000u,0x1ebdcu,0x7fe0u,0x1c4f0u,0x100000u,0x1ebdeu,0x7820u,0x1c500u,0x1e9e0u,0x1ebc0u,0x78a0u,0x1c510u,0x100000u,0x1ebc2u,0x7930u,0x1c520u,0x100000u,0x1ebc4u,0x79b0u,0x1c530u,0x100000u,0x1ebc6u,0x7a00u,0x1c540u,0x1e9e0u,0x1ebc8u,0x7a80u,0x1c550u,0x100000u,0x1ebcau,0x7b10u,0x1c560u,0x100000u,0x1ebccu,0x7b90u,0x1c570u,0x100000u,0x1ebceu,0x7c60u,0x1c580u,0x1e9e0u,0x1ebd0u,0x7ce0u,0x1c590u,0x100000u,0x1ebd2u,0x7d70u,0x1c5a0u,0x100000u,0x1ebd4u,0x7df0u,0x1c5b0u,0x100000u,0x1ebd6u,0x7e40u,0x1c5c0u,0x1e9e0u,0x1ebd8u,0x7ec0u,0x1c5d0u,0x100000u,0x1ebdau,0x7f50u,0x1c5e0u,0x100000u,0x1ebdcu,0x7fd0u,0x1c5f0u,0x100000u,0x1ebdeu,0x7830u,0x1c600u,0x1ea10u,0x1ebc0u,0x78b0u,0x1c610u,0x100000u,0x1ebc2u,0x7920u,0x1c620u,0x100000u,0x1ebc4u,0x79a0u,0x1c630u,0x100000u,0x1ebc6u,0x7a10u,0x1c640u,0x1ea10u,0x1ebc8u,0x7a90u,0x1c650u,0x100000u,0x1ebcau,0x7b00u,0x1c660u,0x100000u,0x1ebccu,0x7b80u,0x1c670u,0x100000u,0x1ebceu,0x7c70u,0x1c680u,0x1ea10u,0x1ebd0u,0x7cf0u,0x1c690u,0x100000u,0x1ebd2u,0x7d60u,0x1c6a0u,0x100000u,0x1ebd4u,0x7de0u,0x1c6b0u,0x100000u,0x1ebd6u,0x7e50u,0x1c6c0u,0x1ea10u,0x1ebd8u,0x7ed0u,0x1c6d0u,0x100000u,0x1ebdau,0x7f40u,0x1c6e0u,0x100000u,0x1ebdcu,0x7fc0u,0x1c6f0u,0x100000u,0x1ebdeu,0x26000u,0x1d400u,0x1d400u,0x80000000u,0x26002u,0x1d402u,0x100000u,0x80000000u,0x26004u,0x1d404u,0x100000u,0x80000000u,0x26006u,0x1d406u,0x100000u,0x80000000u,0x26200u,0x1d410u,0x1d400u,0x80000000u,0x26202u,0x1d412u,0x100000u,0x80000000u,0x26204u,0x1d414u,0x100000u,0x80000000u,0x26206u,0x1d416u,0x100000u,0x80000000u,0x26400u,0x1d420u,0x1d400u,0x80000000u,0x26402u,0x1d422u,0x100000u,0x80000000u,0x26404u,0x1d424u,0x100000u,0x80000000u,0x26406u,0x1d426u,0x100000u,0x80000000u,0x26600u,0x1d430u,0x1d400u,0x80000000u,0x26602u,0x1d432u,0x100000u,0x80000000u,0x26604u,0x1d434u,0x100000u,0x80000000u,0x26606u,0x1d436u,0x100000u,0x80000000u,0x26008u,0x1d408u,0x1d410u,0x80000000u,0x2600au,0x1d40au,0x100000u,0x80000000u,0x2600cu,0x1d40cu,0x100000u,0x80000000u,0x2600eu,0x1d40eu,0x100000u,0x80000000u,0x26208u,0x1d418u,0x1d410u,0x80000000u,0x2620au,0x1d41au,0x100000u,0x80000000u,0x2620cu,0x1d41cu,0x100000u,0x80000000u,0x2620eu,0x1d41eu,0x100000u,0x80000000u,0x26408u,0x1d428u,0x1d410u,0x80000000u,0x2640au,0x1d42au,0x100000u,0x80000000u,0x2640cu,0x1d42cu,0x100000u,0x80000000u,0x2640eu,0x1d42eu,0x100000u,0x80000000u,0x26608u,0x1d438u,0x1d410u,0x80000000u,0x2660au,0x1d43au,0x100000u,0x80000000u,0x2660cu,0x1d43cu,0x100000u,0x80000000u,0x2660eu,0x1d43eu,0x100000u,0x80000000u,0x26800u,0x1d440u,0x1d420u,0x80000000u,0x26802u,0x1d442u,0x100000u,0x80000000u,0x26804u,0x1d444u,0x100000u,0x80000000u,0x26806u,0x1d446u,0x100000u,0x80000000u,0x26a00u,0x1d450u,0x1d420u,0x80000000u,0x26a02u,0x1d452u,0x100000u,0x80000000u,0x26a04u,0x1d454u,0x100000u,0x80000000u,0x26a06u,0x1d456u,0x100000u,0x80000000u,0x26c00u,0x1d460u,0x1d420u,0x80000000u,0x26c02u,0x1d462u,0x100000u,0x80000000u,0x26c04u,0x1d464u,0x100000u,0x80000000u,0x26c06u,0x1d466u,0x100000u,0x80000000u,0x26e00u,0x1d470u,0x1d420u,0x80000000u,0x26e02u,0x1d472u,0x100000u,0x80000000u,0x26e04u,0x1d474u,0x100000u,0x80000000u,0x26e06u,0x1d476u,0x100000u,0x80000000u,0x100000u,0x1d448u,0x1d430u,0x80000000u,0x100000u,0x1d44au,0x100000u,0x80000000u,0x100000u,0x1d44cu,0x100000u,0x80000000u,0x100000u,0x1d44eu,0x100000u,0x80000000u,0x100000u,0x1d458u,0x1d430u,0x80000000u,0x100000u,0x1d45au,0x100000u,0x80000000u,0x100000u,0x1d45cu,0x100000u,0x80000000u,0x100000u,0x1d45eu,0x100000u,0x80000000u,0x100000u,0x1d468u,0x1d430u,0x80000000u,0x100000u,0x1d46au,0x100000u,0x80000000u,0x100000u,0x1d46cu,0x100000u,0x80000000u,0x100000u,0x1d46eu,0x100000u,0x80000000u,0x100000u,0x1d478u,0x1d430u,0x80000000u,0x100000u,0x1d47au,0x100000u,0x80000000u,0x100000u,0x1d47cu,0x100000u,0x80000000u,0x100000u,0x1d47eu,0x100000u,0x80000000u,0x850u,0x60000u,0x90420u,0xefc50u,0x840u,0x60010u,0x90430u,0xefc40u,0x870u,0x60020u,0x90400u,0xefc70u,0x860u,0x60030u,0x90410u,0xefc60u,0x810u,0x60040u,0x90460u,0xefc10u,0x800u,0x60050u,0x90470u,0xefc00u,0x830u,0x60060u,0x90440u,0xefc30u,0x820u,0x60070u,0x90450u,0xefc20u,0x8d0u,0x60080u,0x904b0u,0xefcd0u,0x8c0u,0x60090u,0x904a0u,0xefcc0u,0x8f0u,0x600a0u,0x90490u,0xefcf0u,0x8e0u,0x600b0u,0x90480u,0xefce0u,0x890u,0x600c0u,0x904f0u,0xefc90u,0x880u,0x600d0u,0x904e0u,0xefc80u,0x8b0u,0x600e0u,0x904d0u,0xefcb0u,0x8a0u,0x600f0u,0x904c0u,0xefca0u,0x960u,0x60100u,0x90530u,0xefd60u,0x970u,0x60110u,0x90520u,0xefd70u,0x940u,0x60120u,0x90510u,0xefd40u,0x950u,0x60130u,0x90500u,0xefd50u,0x920u,0x60140u,0x90570u,0xefd20u,0x930u,0x60150u,0x90560u,0xefd30u,0x900u,0x60160u,0x90550u,0xefd00u,0x910u,0x60170u,0x90540u,0xefd10u,0x9e0u,0x60190u,0x905b0u,0xefde0u,0x9f0u,0x60180u,0x905a0u,0xefdf0u,0x9c0u,0x601b0u,0x90590u,0xefdc0u,0x9d0u,0x601a0u,0x90580u,0xefdd0u,0x9a0u,0x601d0u,0x905f0u,0xefda0u,0x9b0u,0x601c0u,0x905e0u,0xefdb0u,0x980u,0x601f0u,0x905d0u,0xefd80u,0x990u,0x601e0u,0x905c0u,0xefd90u,0xa60u,0x60210u,0xbfa40u,0xefe60u,0xa70u,0x60200u,0xbfa50u,0xefe70u,0xa40u,0x60230u,0xbfa60u,0xefe40u,0xa50u,0x60220u,0xbfa70u,0xefe50u,0xa20u,0x60250u,0xbfa00u,0xefe20u,0xa30u,0x60240u,0xbfa10u,0xefe30u,0xa00u,0x60270u,0xbfa20u,0xefe00u,0xa10u,0x60260u,0xbfa30u,0xefe10u,0x2fef0u,0x60290u,0xbfac0u,0xefef0u,0x2fee0u,0x60280u,0xbfad0u,0xefee0u,0x2fed0u,0x602b0u,0xbfae0u,0xefed0u,0x2fec0u,0x602a0u,0xbfaf0u,0xefec0u,0x2feb0u,0x602d0u,0xbfa80u,0xefeb0u,0x2fea0u,0x602c0u,0xbfa90u,0xefea0u,0x2fe90u,0x602f0u,0xbfaa0u,0xefe90u,0x2fe80u,0x602e0u,0xbfab0u,0xefe80u,0x2ff70u,0x60320u,0xbfb40u,0xeff70u,0x2ff60u,0x60330u,0xbfb50u,0xeff60u,0x2ff50u,0x60300u,0xbfb60u,0xeff50u,0x2ff40u,0x60310u,0xbfb70u,0xeff40u,0x2ff30u,0x60360u,0xbfb00u,0xeff30u,0x2ff20u,0x60370u,0xbfb10u,0xeff20u,0x2ff10u,0x60340u,0xbfb20u,0xeff10u,0x2ff00u,0x60350u,0xbfb30u,0xeff00u,0x2fff0u,0x603a0u,0xbfbd0u,0xefff0u,0x2ffe0u,0x603b0u,0xbfbc0u,0xeffe0u,0x2ffd0u,0x60380u,0xbfbf0u,0xeffd0u,0x2ffc0u,0x60390u,0xbfbe0u,0xeffc0u,0x2ffb0u,0x603e0u,0xbfb90u,0xeffb0u,0x2ffa0u,0x603f0u,0xbfb80u,0xeffa0u,0x2ff90u,0x603c0u,0xbfbb0u,0xeff90u,0x2ff80u,0x603d0u,0xbfba0u,0xeff80u,0x0u,0x80000000u,0x0u,0x18830u,0x0u,0x80000000u,0x0u,0x18830u,0x0u,0x80000000u,0x0u,0x18830u,0x0u,0x80000000u,0x0u,0x18830u,0x0u,0x80000000u,0x0u,0x18830u,0x0u,0x80000000u,0x0u,0x18830u,0x0u,0x80000000u,0x0u,0x18830u,0x0u,0x80000000u,0x0u,0x18830u,0x0u,0x80000000u,0x0u,0x18830u,0x0u,0x80000000u,0x0u,0x18830u,0x0u,0x80000000u,0x0u,0x18830u,0x0u,0x80000000u,0x0u,0x18830u,0x0u,0x80000000u,0x0u,0x18830u,0x0u,0x80000000u,0x0u,0x18830u,0x0u,0x80000000u,0x0u,0x18830u,0x0u,0x80000000u,0x0u,0x18830u,0x0u,0x80000000u,0x0u,0x188b2u,0x0u,0x80000000u,0x0u,0x188b2u,0x0u,0x80000000u,0x0u,0x188b2u,0x0u,0x80000000u,0x0u,0x188b2u,0x0u,0x80000000u,0x0u,0x188b2u,0x0u,0x80000000u,0x0u,0x188b2u,0x0u,0x80000000u,0x0u,0x188b2u,0x0u,0x80000000u,0x0u,0x188b2u,0x0u,0x80000000u,0x0u,0x18892u,0x0u,0x80000000u,0x0u,0x18892u,0x0u,0x80000000u,0x0u,0x18892u,0x0u,0x80000000u,0x0u,0x18892u,0x0u,0x80000000u,0x0u,0x18892u,0x0u,0x80000000u,0x0u,0x18892u,0x0u,0x80000000u,0x0u,0x18892u,0x0u,0x80000000u,0x0u,0x18892u,0x0u,0x80000000u,0x0u,0x1489au,0x0u,0x80000000u,0x0u,0x1489au,0x0u,0x80000000u,0x0u,0x1489au,0x0u,0x80000000u,0x0u,0x1489au,0x0u,0x80000000u,0x0u,0x1489au,0x0u,0x80000000u,0x0u,0x1489au,0x0u,0x80000000u,0x0u,0x1489au,0x0u,0x80000000u,0x0u,0x1489au,0x0u,0x80000000u,0x0u,0x34018u,0x0u,0x80000000u,0x0u,0x34018u,0x0u,0x80000000u,0x0u,0x34018u,0x0u,0x80000000u,0x0u,0x34018u,0x0u,0x80000000u,0x0u,0x34018u,0x0u,0x80000000u,0x0u,0x34018u,0x0u,0x80000000u,0x0u,0x34018u,0x0u,0x80000000u,0x0u,0x34018u,0x0u,0x80000000u,0x0u,0x36018u,0x0u,0x80000000u,0x0u,0x36018u,0x0u,0x80000000u,0x0u,0x36018u,0x0u,0x80000000u,0x0u,0x36018u,0x0u,0x80000000u,0x0u,0x36018u,0x0u,0x80000000u,0x0u,0x36018u,0x0u,0x80000000u,0x0u,0x36018u,0x0u,0x80000000u,0x0u,0x36018u,0x0u,0x80000000u,0x0u,0x36030u,0x0u,0x80000000u,0x0u,0x36030u,0x0u,0x80000000u,0x0u,0x36030u,0x0u,0x80000000u,0x0u,0x36030u,0x0u,0x80000000u,0x0u,0x36030u,0x0u,0x80000000u,0x0u,0x36030u,0x0u,0x80000000u,0x0u,0x36030u,0x0u,0x80000000u,0x0u,0x36030u,0x6840u,0x6860u,0x6820u,0x6820u,0x6848u,0x6868u,0x6828u,0x6830u,0x6850u,0x6870u,0x6830u,0x6840u,0x6858u,0x6878u,0x6838u,0x6850u,0x68c0u,0x68e0u,0x68a0u,0x68a0u,0x68c8u,0x68e8u,0x68a8u,0x68b0u,0x68d0u,0x68f0u,0x68b0u,0x68c0u,0x68d8u,0x68f8u,0x68b8u,0x68d0u,0x6960u,0x6940u,0x6900u,0x6900u,0x6968u,0x6948u,0x6908u,0x6910u,0x6970u,0x6950u,0x6910u,0x6960u,0x6978u,0x6958u,0x6918u,0x6970u,0x69e0u,0x69c0u,0x6980u,0x6980u,0x69e8u,0x69c8u,0x6988u,0x6990u,0x69f0u,0x69d0u,0x6990u,0x69e0u,0x69f8u,0x69d8u,0x6998u,0x69f0u,0x6c00u,0x6c20u,0x6c60u,0x6a20u,0x6c08u,0x6c28u,0x6c68u,0x6a30u,0x6c10u,0x6c30u,0x6c70u,0x6a40u,0x6c18u,0x6c38u,0x6c78u,0x6a50u,0x6c80u,0x6ca0u,0x6ce0u,0x6aa0u,0x6c88u,0x6ca8u,0x6ce8u,0x6ab0u,0x6c90u,0x6cb0u,0x6cf0u,0x6ac0u,0x6c98u,0x6cb8u,0x6cf8u,0x6ad0u,0x6d20u,0x6d00u,0x6d40u,0x6b00u,0x6d28u,0x6d08u,0x6d48u,0x6b10u,0x6d30u,0x6d10u,0x6d50u,0x6b60u,0x6d38u,0x6d18u,0x6d58u,0x6b70u,0x6da0u,0x6d80u,0x6dc0u,0x6b80u,0x6da8u,0x6d88u,0x6dc8u,0x6b90u,0x6db0u,0x6d90u,0x6dd0u,0x6be0u,0x6db8u,0x6d98u,0x6dd8u,0x6bf0u,0x7040u,0x7060u,0x7020u,0x6c60u,0x7048u,0x7068u,0x7028u,0x6c70u,0x7050u,0x7070u,0x7030u,0x6c00u,0x7058u,0x7078u,0x7038u,0x6c10u,0x70c0u,0x70e0u,0x70a0u,0x6ce0u,0x70c8u,0x70e8u,0x70a8u,0x6cf0u,0x70d0u,0x70f0u,0x70b0u,0x6c80u,0x70d8u,0x70f8u,0x70b8u,0x6c90u,0x7160u,0x7140u,0x7100u,0x6d40u,0x7168u,0x7148u,0x7108u,0x6d50u,0x7170u,0x7150u,0x7110u,0x6d20u,0x7178u,0x7158u,0x7118u,0x6d30u,0x71e0u,0x71c0u,0x7180u,0x6dc0u,0x71e8u,0x71c8u,0x7188u,0x6dd0u,0x71f0u,0x71d0u,0x7190u,0x6da0u,0x71f8u,0x71d8u,0x7198u,0x6db0u,0x7400u,0x7420u,0x7460u,0x6e60u,0x7408u,0x7428u,0x7468u,0x6e70u,0x7410u,0x7430u,0x7470u,0x6e00u,0x7418u,0x7438u,0x7478u,0x6e10u,0x7480u,0x74a0u,0x74e0u,0x6ee0u,0x7488u,0x74a8u,0x74e8u,0x6ef0u,0x7490u,0x74b0u,0x74f0u,0x6e80u,0x7498u,0x74b8u,0x74f8u,0x6e90u,0x7520u,0x7500u,0x7540u,0x6f40u,0x7528u,0x7508u,0x7548u,0x6f50u,0x7530u,0x7510u,0x7550u,0x6f20u,0x7538u,0x7518u,0x7558u,0x6f30u,0x75a0u,0x7580u,0x75c0u,0x6fc0u,0x75a8u,0x7588u,0x75c8u,0x6fd0u,0x75b0u,0x7590u,0x75d0u,0x6fa0u,0x75b8u,0x7598u,0x75d8u,0x6fb0u,0x6860u,0x1ea40u,0x1ec20u,0x2440u,0x6868u,0x1eed0u,0x1f0b0u,0x2450u,0x6870u,0x1f360u,0x1f540u,0x2460u,0x6878u,0x1f7f0u,0x1f9d0u,0x2470u,0x68e0u,0x1fc80u,0x1fe60u,0x2400u,0x68e8u,0x20110u,0x202f0u,0x2410u,0x68f0u,0x205a0u,0x20780u,0x2420u,0x68f8u,0x20a30u,0x20c10u,0x2430u,0x6940u,0x20ec0u,0x210a0u,0x25c0u,0x6948u,0x21350u,0x21530u,0x25d0u,0x6950u,0x217e0u,0x219c0u,0x25e0u,0x6958u,0x21c70u,0x21e50u,0x25f0u,0x69c0u,0x22100u,0x222e0u,0x2580u,0x69c8u,0x22590u,0x22770u,0x2590u,0x69d0u,0x22a20u,0x22c00u,0x25a0u,0x69d8u,0x22eb0u,0x23090u,0x25b0u,0x6a60u,0x1ea70u,0x1ec28u,0x2760u,0x6a68u,0x1ef00u,0x1f0b8u,0x2770u,0x6a70u,0x1f390u,0x1f548u,0x2740u,0x6a78u,0x1f820u,0x1f9d8u,0x2750u,0x6ae0u,0x1fcb0u,0x1fe68u,0x2720u,0x6ae8u,0x20140u,0x202f8u,0x2730u,0x6af0u,0x205d0u,0x20788u,0x2700u,0x6af8u,0x20a60u,0x20c18u,0x2710u,0x6b40u,0x20ef0u,0x210a8u,0x28e0u,0x6b48u,0x21380u,0x21538u,0x28f0u,0x6b50u,0x21810u,0x219c8u,0x28c0u,0x6b58u,0x21ca0u,0x21e58u,0x28d0u,0x6bc0u,0x22130u,0x222e8u,0x28a0u,0x6bc8u,0x225c0u,0x22778u,0x28b0u,0x6bd0u,0x22a50u,0x22c08u,0x2880u,0x6bd8u,0x22ee0u,0x23098u,0x2890u,0x6c20u,0x1eaa0u,0x1ec30u,0x2a40u,0x6c28u,0x1ef30u,0x1f0c0u,0x2a50u,0x6c30u,0x1f3c0u,0x1f550u,0x2a60u,0x6c38u,0x1f850u,0x1f9e0u,0x2a70u,0x6ca0u,0x1fce0u,0x1fe70u,0x2a00u,0x6ca8u,0x20170u,0x20300u,0x2a10u,0x6cb0u,0x20600u,0x20790u,0x2a20u,0x6cb8u,0x20a90u,0x20c20u,0x2a30u,0x6d00u,0x20f20u,0x210b0u,0x2bc0u,0x6d08u,0x213b0u,0x21540u,0x2bd0u,0x6d10u,0x21840u,0x219d0u,0x2be0u,0x6d18u,0x21cd0u,0x21e60u,0x2bf0u,0x6d80u,0x22160u,0x222f0u,0x2b80u,0x6d88u,0x225f0u,0x22780u,0x2b90u,0x6d90u,0x22a80u,0x22c10u,0x2ba0u,0x6d98u,0x22f10u,0x230a0u,0x2bb0u,0x6e20u,0x1ead0u,0x1ec38u,0x2d60u,0x6e28u,0x1ef60u,0x1f0c8u,0x2d70u,0x6e30u,0x1f3f0u,0x1f558u,0x2d40u,0x6e38u,0x1f880u,0x1f9e8u,0x2d50u,0x6ea0u,0x1fd10u,0x1fe78u,0x2d20u,0x6ea8u,0x201a0u,0x20308u,0x2d30u,0x6eb0u,0x20630u,0x20798u,0x2d00u,0x6eb8u,0x20ac0u,0x20c28u,0x2d10u,0x6f00u,0x20f50u,0x210b8u,0x2ee0u,0x6f08u,0x213e0u,0x21548u,0x2ef0u,0x6f10u,0x21870u,0x219d8u,0x2ec0u,0x6f18u,0x21d00u,0x21e68u,0x2ed0u,0x6f80u,0x22190u,0x222f8u,0x2ea0u,0x6f88u,0x22620u,0x22788u,0x2eb0u,0x6f90u,0x22ab0u,0x22c18u,0x2e80u,0x6f98u,0x22f40u,0x230a8u,0x2e90u,0x0u,0x40u,0x9000u,0xcc00u,0x80u,0xc0u,0x9010u,0xcc10u,0x110u,0x150u,0x9020u,0xcc20u,0x190u,0x1d0u,0x9030u,0xcc30u,0x220u,0x260u,0x9040u,0xcc40u,0x2a0u,0x2e0u,0x9050u,0xcc50u,0x330u,0x370u,0x9060u,0xcc60u,0x3b0u,0x3f0u,0x9070u,0xcc70u,0x440u,0x400u,0x9080u,0xcc80u,0x4c0u,0x480u,0x9090u,0xcc90u,0x550u,0x510u,0x90a0u,0xcca0u,0x5d0u,0x590u,0x90b0u,0xccb0u,0x660u,0x620u,0x90c0u,0xccc0u,0x6e0u,0x6a0u,0x90d0u,0xccd0u,0x770u,0x730u,0x90e0u,0xcce0u,0x7f0u,0x7b0u,0x90f0u,0xccf0u,0x10u,0x50u,0x9300u,0xcf00u,0x90u,0xd0u,0x9310u,0xcf10u,0x100u,0x140u,0x9320u,0xcf20u,0x180u,0x1c0u,0x9330u,0xcf30u,0x230u,0x270u,0x9340u,0xcf40u,0x2b0u,0x2f0u,0x9350u,0xcf50u,0x320u,0x360u,0x9360u,0xcf60u,0x3a0u,0x3e0u,0x9370u,0xcf70u,0x450u,0x410u,0x9380u,0xcf80u,0x4d0u,0x490u,0x9390u,0xcf90u,0x540u,0x500u,0x93a0u,0xcfa0u,0x5c0u,0x580u,0x93b0u,0xcfb0u,0x670u,0x630u,0x93c0u,0xcfc0u,0x6f0u,0x6b0u,0x93d0u,0xcfd0u,0x760u,0x720u,0x93e0u,0xcfe0u,0x7e0u,0x7a0u,0x93f0u,0xcff0u,0x20u,0x60u,0x9600u,0xd200u,0xa0u,0xe0u,0x9610u,0xd210u,0x130u,0x170u,0x9620u,0xd220u,0x1b0u,0x1f0u,0x9630u,0xd230u,0x200u,0x240u,0x9640u,0xd240u,0x280u,0x2c0u,0x9650u,0xd250u,0x310u,0x350u,0x9660u,0xd260u,0x390u,0x3d0u,0x9670u,0xd270u,0x460u,0x420u,0x9680u,0xd280u,0x4e0u,0x4a0u,0x9690u,0xd290u,0x570u,0x530u,0x96a0u,0xd2a0u,0x5f0u,0x5b0u,0x96b0u,0xd2b0u,0x640u,0x600u,0x96c0u,0xd2c0u,0x6c0u,0x680u,0x96d0u,0xd2d0u,0x750u,0x710u,0x96e0u,0xd2e0u,0x7d0u,0x790u,0x96f0u,0xd2f0u,0x30u,0x70u,0x9900u,0xd500u,0xb0u,0xf0u,0x9910u,0xd510u,0x120u,0x160u,0x9920u,0xd520u,0x1a0u,0x1e0u,0x9930u,0xd530u,0x210u,0x250u,0x9940u,0xd540u,0x290u,0x2d0u,0x9950u,0xd550u,0x300u,0x340u,0x9960u,0xd560u,0x380u,0x3c0u,0x9970u,0xd570u,0x470u,0x430u,0x9980u,0xd580u,0x4f0u,0x4b0u,0x9990u,0xd590u,0x560u,0x520u,0x99a0u,0xd5a0u,0x5e0u,0x5a0u,0x99b0u,0xd5b0u,0x650u,0x610u,0x99c0u,0xd5c0u,0x6d0u,0x690u,0x99d0u,0xd5d0u,0x740u,0x700u,0x99e0u,0xd5e0u,0x7c0u,0x780u,0x99f0u,0xd5f0u,0xc008u,0x12120u,0x12160u,0x12c00u,0xc018u,0x122b0u,0x122f0u,0x12c10u,0xc028u,0x12400u,0x12440u,0x12c20u,0xc038u,0x12590u,0x125d0u,0x12c30u,0xc048u,0x12760u,0x12720u,0x12c40u,0xc058u,0x128f0u,0x128b0u,0x12c50u,0xc068u,0x12a40u,0x12a00u,0x12c60u,0xc078u,0x12bd0u,0x12b90u,0x12c70u,0xc088u,0x12d20u,0x12d60u,0x12c80u,0xc098u,0x12eb0u,0x12ef0u,0x12c90u,0xc0a8u,0x13000u,0x13040u,0x12ca0u,0xc0b8u,0x13190u,0x131d0u,0x12cb0u,0xc0c8u,0x13360u,0x13320u,0x12cc0u,0xc0d8u,0x134f0u,0x134b0u,0x12cd0u,0xc0e8u,0x13640u,0x13600u,0x12ce0u,0xc0f8u,0x137d0u,0x13790u,0x12cf0u,0xc308u,0x12130u,0x12168u,0x12d00u,0xc318u,0x122a0u,0x122f8u,0x12d10u,0xc328u,0x12410u,0x12448u,0x12d20u,0xc338u,0x12580u,0x125d8u,0x12d30u,0xc348u,0x12770u,0x12728u,0x12d40u,0xc358u,0x128e0u,0x128b8u,0x12d50u,0xc368u,0x12a50u,0x12a08u,0x12d60u,0xc378u,0x12bc0u,0x12b98u,0x12d70u,0xc388u,0x12d30u,0x12d68u,0x12d80u,0xc398u,0x12ea0u,0x12ef8u,0x12d90u,0xc3a8u,0x13010u,0x13048u,0x12da0u,0xc3b8u,0x13180u,0x131d8u,0x12db0u,0xc3c8u,0x13370u,0x13328u,0x12dc0u,0xc3d8u,0x134e0u,0x134b8u,0x12dd0u,0xc3e8u,0x13650u,0x13608u,0x12de0u,0xc3f8u,0x137c0u,0x13798u,0x12df0u,0xc608u,0x12140u,0x12170u,0x12e00u,0xc618u,0x122d0u,0x122e0u,0x12e10u,0xc628u,0x12460u,0x12450u,0x12e20u,0xc638u,0x125f0u,0x125c0u,0x12e30u,0xc648u,0x12700u,0x12730u,0x12e40u,0xc658u,0x12890u,0x128a0u,0x12e50u,0xc668u,0x12a20u,0x12a10u,0x12e60u,0xc678u,0x12bb0u,0x12b80u,0x12e70u,0xc688u,0x12d40u,0x12d70u,0x12e80u,0xc698u,0x12ed0u,0x12ee0u,0x12e90u,0xc6a8u,0x13060u,0x13050u,0x12ea0u,0xc6b8u,0x131f0u,0x131c0u,0x12eb0u,0xc6c8u,0x13300u,0x13330u,0x12ec0u,0xc6d8u,0x13490u,0x134a0u,0x12ed0u,0xc6e8u,0x13620u,0x13610u,0x12ee0u,0xc6f8u,0x137b0u,0x13780u,0x12ef0u,0xc908u,0x12150u,0x12178u,0x12f00u,0xc918u,0x122c0u,0x122e8u,0x12f10u,0xc928u,0x12470u,0x12458u,0x12f20u,0xc938u,0x125e0u,0x125c8u,0x12f30u,0xc948u,0x12710u,0x12738u,0x12f40u,0xc958u,0x12880u,0x128a8u,0x12f50u,0xc968u,0x12a30u,0x12a18u,0x12f60u,0xc978u,0x12ba0u,0x12b88u,0x12f70u,0xc988u,0x12d50u,0x12d78u,0x12f80u,0xc998u,0x12ec0u,0x12ee8u,0x12f90u,0xc9a8u,0x13070u,0x13058u,0x12fa0u,0xc9b8u,0x131e0u,0x131c8u,0x12fb0u,0xc9c8u,0x13310u,0x13338u,0x12fc0u,0xc9d8u,0x13480u,0x134a8u,0x12fd0u,0xc9e8u,0x13630u,0x13618u,0x12fe0u,0xc9f8u,0x137a0u,0x13788u,0x12ff0u,0x8000u,0x1c300u,0x1ea40u,0x1ec20u,0x8080u,0x1c310u,0x100000u,0x1ec22u,0x8110u,0x1c320u,0x100000u,0x1ec24u,0x8190u,0x1c330u,0x100000u,0x1ec26u,0x8220u,0x1c340u,0x1ea40u,0x1ec28u,0x82a0u,0x1c350u,0x100000u,0x1ec2au,0x8330u,0x1c360u,0x100000u,0x1ec2cu,0x83b0u,0x1c370u,0x100000u,0x1ec2eu,0x8440u,0x1c380u,0x1ea40u,0x1ec30u,0x84c0u,0x1c390u,0x100000u,0x1ec32u,0x8550u,0x1c3a0u,0x100000u,0x1ec34u,0x85d0u,0x1c3b0u,0x100000u,0x1ec36u,0x8660u,0x1c3c0u,0x1ea40u,0x1ec38u,0x86e0u,0x1c3d0u,0x100000u,0x1ec3au,0x8770u,0x1c3e0u,0x100000u,0x1ec3cu,0x87f0u,0x1c3f0u,0x100000u,0x1ec3eu,0x8010u,0x1c400u,0x1ea70u,0x1ec20u,0x8090u,0x1c410u,0x100000u,0x1ec22u,0x8100u,0x1c420u,0x100000u,0x1ec24u,0x8180u,0x1c430u,0x100000u,0x1ec26u,0x8230u,0x1c440u,0x1ea70u,0x1ec28u,0x82b0u,0x1c450u,0x100000u,0x1ec2au,0x8320u,0x1c460u,0x100000u,0x1ec2cu,0x83a0u,0x1c470u,0x100000u,0x1ec2eu,0x8450u,0x1c480u,0x1ea70u,0x1ec30u,0x84d0u,0x1c490u,0x100000u,0x1ec32u,0x8540u,0x1c4a0u,0x100000u,0x1ec34u,0x85c0u,0x1c4b0u,0x100000u,0x1ec36u,0x8670u,0x1c4c0u,0x1ea70u,0x1ec38u,0x86f0u,0x1c4d0u,0x100000u,0x1ec3au,0x8760u,0x1c4e0u,0x100000u,0x1ec3cu,0x87e0u,0x1c4f0u,0x100000u,0x1ec3eu,0x8020u,0x1c500u,0x1eaa0u,0x1ec20u,0x80a0u,0x1c510u,0x100000u,0x1ec22u,0x8130u,0x1c520u,0x100000u,0x1ec24u,0x81b0u,0x1c530u,0x100000u,0x1ec26u,0x8200u,0x1c540u,0x1eaa0u,0x1ec28u,0x8280u,0x1c550u,0x100000u,0x1ec2au,0x8310u,0x1c560u,0x100000u,0x1ec2cu,0x8390u,0x1c570u,0x100000u,0x1ec2eu,0x8460u,0x1c580u,0x1eaa0u,0x1ec30u,0x84e0u,0x1c590u,0x100000u,0x1ec32u,0x8570u,0x1c5a0u,0x100000u,0x1ec34u,0x85f0u,0x1c5b0u,0x100000u,0x1ec36u,0x8640u,0x1c5c0u,0x1eaa0u,0x1ec38u,0x86c0u,0x1c5d0u,0x100000u,0x1ec3au,0x8750u,0x1c5e0u,0x100000u,0x1ec3cu,0x87d0u,0x1c5f0u,0x100000u,0x1ec3eu,0x8030u,0x1c600u,0x1ead0u,0x1ec20u,0x80b0u,0x1c610u,0x100000u,0x1ec22u,0x8120u,0x1c620u,0x100000u,0x1ec24u,0x81a0u,0x1c630u,0x100000u,0x1ec26u,0x8210u,0x1c640u,0x1ead0u,0x1ec28u,0x8290u,0x1c650u,0x100000u,0x1ec2au,0x8300u,0x1c660u,0x100000u,0x1ec2cu,0x8380u,0x1c670u,0x100000u,0x1ec2eu,0x8470u,0x1c680u,0x1ead0u,0x1ec30u,0x84f0u,0x1c690u,0x100000u,0x1ec32u,0x8560u,0x1c6a0u,0x100000u,0x1ec34u,0x85e0u,0x1c6b0u,0x100000u,0x1ec36u,0x8650u,0x1c6c0u,0x1ead0u,0x1ec38u,0x86d0u,0x1c6d0u,0x100000u,0x1ec3au,0x8740u,0x1c6e0u,0x100000u,0x1ec3cu,0x87c0u,0x1c6f0u,0x100000u,0x1ec3eu,0x27000u,0x1d480u,0x1d480u,0x80000000u,0x27002u,0x1d482u,0x100000u,0x80000000u,0x27004u,0x1d484u,0x100000u,0x80000000u,0x27006u,0x1d486u,0x100000u,0x80000000u,0x27200u,0x1d490u,0x1d480u,0x80000000u,0x27202u,0x1d492u,0x100000u,0x80000000u,0x27204u,0x1d494u,0x100000u,0x80000000u,0x27206u,0x1d496u,0x100000u,0x80000000u,0x27400u,0x1d4a0u,0x1d480u,0x80000000u,0x27402u,0x1d4a2u,0x100000u,0x80000000u,0x27404u,0x1d4a4u,0x100000u,0x80000000u,0x27406u,0x1d4a6u,0x100000u,0x80000000u,0x27600u,0x1d4b0u,0x1d480u,0x80000000u,0x27602u,0x1d4b2u,0x100000u,0x80000000u,0x27604u,0x1d4b4u,0x100000u,0x80000000u,0x27606u,0x1d4b6u,0x100000u,0x80000000u,0x27008u,0x1d488u,0x1d490u,0x80000000u,0x2700au,0x1d48au,0x100000u,0x80000000u,0x2700cu,0x1d48cu,0x100000u,0x80000000u,0x2700eu,0x1d48eu,0x100000u,0x80000000u,0x27208u,0x1d498u,0x1d490u,0x80000000u,0x2720au,0x1d49au,0x100000u,0x80000000u,0x2720cu,0x1d49cu,0x100000u,0x80000000u,0x2720eu,0x1d49eu,0x100000u,0x80000000u,0x27408u,0x1d4a8u,0x1d490u,0x80000000u,0x2740au,0x1d4aau,0x100000u,0x80000000u,0x2740cu,0x1d4acu,0x100000u,0x80000000u,0x2740eu,0x1d4aeu,0x100000u,0x80000000u,0x27608u,0x1d4b8u,0x1d490u,0x80000000u,0x2760au,0x1d4bau,0x100000u,0x80000000u,0x2760cu,0x1d4bcu,0x100000u,0x80000000u,0x2760eu,0x1d4beu,0x100000u,0x80000000u,0x26808u,0x1d4c0u,0x1d4a0u,0x80000000u,0x2680au,0x1d4c2u,0x100000u,0x80000000u,0x2680cu,0x1d4c4u,0x100000u,0x80000000u,0x2680eu,0x1d4c6u,0x100000u,0x80000000u,0x26a08u,0x1d4d0u,0x1d4a0u,0x80000000u,0x26a0au,0x1d4d2u,0x100000u,0x80000000u,0x26a0cu,0x1d4d4u,0x100000u,0x80000000u,0x26a0eu,0x1d4d6u,0x100000u,0x80000000u,0x26c08u,0x1d4e0u,0x1d4a0u,0x80000000u,0x26c0au,0x1d4e2u,0x100000u,0x80000000u,0x26c0cu,0x1d4e4u,0x100000u,0x80000000u,0x26c0eu,0x1d4e6u,0x100000u,0x80000000u,0x26e08u,0x1d4f0u,0x1d4a0u,0x80000000u,0x26e0au,0x1d4f2u,0x100000u,0x80000000u,0x26e0cu,0x1d4f4u,0x100000u,0x80000000u,0x26e0eu,0x1d4f6u,0x100000u,0x80000000u,0x100000u,0x1d4c8u,0x1d4b0u,0x80000000u,0x100000u,0x1d4cau,0x100000u,0x80000000u,0x100000u,0x1d4ccu,0x100000u,0x80000000u,0x100000u,0x1d4ceu,0x100000u,0x80000000u,0x100000u,0x1d4d8u,0x1d4b0u,0x80000000u,0x100000u,0x1d4dau,0x100000u,0x80000000u,0x100000u,0x1d4dcu,0x100000u,0x80000000u,0x100000u,0x1d4deu,0x100000u,0x80000000u,0x100000u,0x1d4e8u,0x1d4b0u,0x80000000u,0x100000u,0x1d4eau,0x100000u,0x80000000u,0x100000u,0x1d4ecu,0x100000u,0x80000000u,0x100000u,0x1d4eeu,0x100000u,0x80000000u,0x100000u,0x1d4f8u,0x1d4b0u,0x80000000u,0x100000u,0x1d4fau,0x100000u,0x80000000u,0x100000u,0x1d4fcu,0x100000u,0x80000000u,0x100000u,0x1d4feu,0x100000u,0x80000000u,0x30000u,0x60420u,0xbfc50u,0xf0000u,0x30010u,0x60430u,0xbfc40u,0xf0010u,0x30020u,0x60400u,0xbfc70u,0xf0020u,0x30030u,0x60410u,0xbfc60u,0xf0030u,0x30040u,0x60460u,0xbfc10u,0xf0040u,0x30050u,0x60470u,0xbfc00u,0xf0050u,0x30060u,0x60440u,0xbfc30u,0xf0060u,0x30070u,0x60450u,0xbfc20u,0xf0070u,0x30080u,0x604b0u,0xbfcd0u,0xf0080u,0x30090u,0x604a0u,0xbfcc0u,0xf0090u,0x300a0u,0x60490u,0xbfcf0u,0xf00a0u,0x300b0u,0x60480u,0xbfce0u,0xf00b0u,0x300c0u,0x604f0u,0xbfc90u,0xf00c0u,0x300d0u,0x604e0u,0xbfc80u,0xf00d0u,0x300e0u,0x604d0u,0xbfcb0u,0xf00e0u,0x300f0u,0x604c0u,0xbfca0u,0xf00f0u,0x30100u,0x60530u,0xbfd60u,0xf0100u,0x30110u,0x60520u,0xbfd70u,0xf0110u,0x30120u,0x60510u,0xbfd40u,0xf0120u,0x30130u,0x60500u,0xbfd50u,0xf0130u,0x30140u,0x60570u,0xbfd20u,0xf0140u,0x30150u,0x60560u,0xbfd30u,0xf0150u,0x30160u,0x60550u,0xbfd00u,0xf0160u,0x30170u,0x60540u,0xbfd10u,0xf0170u,0x30190u,0x605b0u,0xbfde0u,0xf0190u,0x30180u,0x605a0u,0xbfdf0u,0xf0180u,0x301b0u,0x60590u,0xbfdc0u,0xf01b0u,0x301a0u,0x60580u,0xbfdd0u,0xf01a0u,0x301d0u,0x605f0u,0xbfda0u,0xf01d0u,0x301c0u,0x605e0u,0xbfdb0u,0xf01c0u,0x301f0u,0x605d0u,0xbfd80u,0xf01f0u,0x301e0u,0x605c0u,0xbfd90u,0xf01e0u,0x30210u,0x60640u,0xbfe60u,0xf0210u,0x30200u,0x60650u,0xbfe70u,0xf0200u,0x30230u,0x60660u,0xbfe40u,0xf0230u,0x30220u,0x60670u,0xbfe50u,0xf0220u,0x30250u,0x60600u,0xbfe20u,0xf0250u,0x30240u,0x60610u,0xbfe30u,0xf0240u,0x30270u,0x60620u,0xbfe00u,0xf0270u,0x30260u,0x60630u,0xbfe10u,0xf0260u,0x30290u,0x606c0u,0xbfef0u,0xf0290u,0x30280u,0x606d0u,0xbfee0u,0xf0280u,0x302b0u,0x606e0u,0xbfed0u,0xf02b0u,0x302a0u,0x606f0u,0xbfec0u,0xf02a0u,0x302d0u,0x60680u,0xbfeb0u,0xf02d0u,0x302c0u,0x60690u,0xbfea0u,0xf02c0u,0x302f0u,0x606a0u,0xbfe90u,0xf02f0u,0x302e0u,0x606b0u,0xbfe80u,0xf02e0u,0x30320u,0x60740u,0xbff70u,0x11f720u,0x30330u,0x60750u,0xbff60u,0x11f730u,0x30300u,0x60760u,0xbff50u,0x11f700u,0x30310u,0x60770u,0xbff40u,0x11f710u,0x30360u,0x60700u,0xbff30u,0x11f760u,0x30370u,0x60710u,0xbff20u,0x11f770u,0x30340u,0x60720u,0xbff10u,0x11f740u,0x30350u,0x60730u,0xbff00u,0x11f750u,0x303a0u,0x8fbd0u,0xbfff0u,0x11f7a0u,0x303b0u,0x8fbc0u,0xbffe0u,0x11f7b0u,0x30380u,0x8fbf0u,0xbffd0u,0x11f780u,0x30390u,0x8fbe0u,0xbffc0u,0x11f790u,0x303e0u,0x8fb90u,0xbffb0u,0x11f7e0u,0x303f0u,0x8fb80u,0xbffa0u,0x11f7f0u,0x303c0u,0x8fbb0u,0xbff90u,0x11f7c0u,0x303d0u,0x8fba0u,0xbff80u,0x11f7d0u,0x0u,0x80000000u,0x0u,0x36030u,0x0u,0x80000000u,0x0u,0x36030u,0x0u,0x80000000u,0x0u,0x36030u,0x0u,0x80000000u,0x0u,0x36030u,0x0u,0x80000000u,0x0u,0x36030u,0x0u,0x80000000u,0x0u,0x36030u,0x0u,0x80000000u,0x0u,0x36030u,0x0u,0x80000000u,0x0u,0x36030u,0x0u,0x80000000u,0x0u,0x360b2u,0x0u,0x80000000u,0x0u,0x360b2u,0x0u,0x80000000u,0x0u,0x360b2u,0x0u,0x80000000u,0x0u,0x360b2u,0x0u,0x80000000u,0x0u,0x360b2u,0x0u,0x80000000u,0x0u,0x360b2u,0x0u,0x80000000u,0x0u,0x360b2u,0x0u,0x80000000u,0x0u,0x360b2u,0x0u,0x80000000u,0x0u,0x36092u,0x0u,0x80000000u,0x0u,0x36092u,0x0u,0x80000000u,0x0u,0x36092u,0x0u,0x80000000u,0x0u,0x36092u,0x0u,0x80000000u,0x0u,0x36092u,0x0u,0x80000000u,0x0u,0x36092u,0x0u,0x80000000u,0x0u,0x36092u,0x0u,0x80000000u,0x0u,0x36092u,0x0u,0x80000000u,0x0u,0x3609au,0x0u,0x80000000u,0x0u,0x3609au,0x0u,0x80000000u,0x0u,0x3609au,0x0u,0x80000000u,0x0u,0x3609au,0x0u,0x80000000u,0x0u,0x3609au,0x0u,0x80000000u,0x0u,0x3609au,0x0u,0x80000000u,0x0u,0x3609au,0x0u,0x80000000u,0x0u,0x3609au,0x0u,0x80000000u,0x0u,0x36059u,0x0u,0x80000000u,0x0u,0x36059u,0x0u,0x80000000u,0x0u,0x36059u,0x0u,0x80000000u,0x0u,0x36059u,0x0u,0x80000000u,0x0u,0x36059u,0x0u,0x80000000u,0x0u,0x36059u,0x0u,0x80000000u,0x0u,0x36059u,0x0u,0x80000000u,0x0u,0x36059u,0x0u,0x80000000u,0x0u,0x36059u,0x0u,0x80000000u,0x0u,0x36059u,0x0u,0x80000000u,0x0u,0x36059u,0x0u,0x80000000u,0x0u,0x36059u,0x0u,0x80000000u,0x0u,0x36059u,0x0u,0x80000000u,0x0u,0x36059u,0x0u,0x80000000u,0x0u,0x36059u,0x0u,0x80000000u,0x0u,0x36059u,0x0u,0x80000000u,0x0u,0x16071u,0x0u,0x80000000u,0x0u,0x16071u,0x0u,0x80000000u,0x0u,0x16071u,0x0u,0x80000000u,0x0u,0x16071u,0x0u,0x80000000u,0x0u,0x16071u,0x0u,0x80000000u,0x0u,0x16071u,0x0u,0x80000000u,0x0u,0x16071u,0x0u,0x80000000u,0x0u,0x16071u,0x0u,0x80000000u,0x0u,0x14071u,0x0u,0x80000000u,0x0u,0x14071u,0x0u,0x80000000u,0x0u,0x14071u,0x0u,0x80000000u,0x0u,0x14071u,0x0u,0x80000000u,0x0u,0x14071u,0x0u,0x80000000u,0x0u,0x14071u,0x0u,0x80000000u,0x0u,0x14071u,0x0u,0x80000000u,0x0u,0x14071u};

__global__ __launch_bounds__(256, 1) void swin_main(const uint16_t* x, const uint16_t* mask, char* ws, uint16_t* out,
                                                    const uint16_t* wqkv, const uint16_t* wo, const uint16_t* ls,
                                                    const uint16_t* w1, const uint16_t* w2, const uint16_t* rc,
                                                    const int* ridx) {
  __shared__ __attribute__((aligned(16))) char smem[LDS_BYTES];
  const int wave = __builtin_amdgcn_readfirstlane(threadIdx.x >> 6);
  const unsigned lds0 = (unsigned)(size_t)smem;
  asm volatile(
      ".p2align 5\n"
      "s_mov_b64 s[92:93], %0\n"
      "s_mov_b32 s40, %1\n"
      "s_mov_b32 s41, %2\n"
      "s_mov_b64 s[88:89], %3\n"
      "s_load_dwordx2 s[16:17], s[92:93], 0\n"
      "s_load_dwordx2 s[20:21], s[92:93], 32\n"
      "s_load_dwordx2 s[46:47], s[92:93], 40\n"
      "s_load_dwordx8 s[80:87], s[92:93], 48\n"
      "s_load_dwordx2 s[76:77], s[92:93], 80\n"
      "s_mov_b64 s[90:91], s[92:93]\n"
      "s_getpc_b64 s[98:99]\n"
      "L_ipf0_%=:\n"
      "v_mbcnt_lo_u32_b32 v8, -1, 0\n"
      "v_mbcnt_hi_u32_b32 v8, -1, v8\n"
      "v_lshlrev_b32 v233, 2, v8\n"
      "v_lshlrev_b32 v219, 4, v8\n"
      "s_cmp_lt_u32 s41, 209\n"
      "s_cselect_b32 s44, 20, 19\n"
      "s_lshl_b32 s92, s40, 8\n"
      "v_add_u32_e64 v50, s92, v233\n"
      "v_min_u32 v29, 704, v50\n"
      "v_add_u32 v29, 4096, v29\n"
      "v_min_u32 v50, 672, v50\n"
      "v_add_u32 v51, 122112, v50\n"
      "s_lshr_b32 s92, s41, 3\n"
      "s_lshl_b32 s92, s92, 7\n"
      "v_lshlrev_b32 v232, 7, v8\n"
      "s_mul_i32 s93, s92, 18\n"
      "s_mul_i32 s94, s40, 640\n"
      "v_min_u32 v235, 512, v232\n"
      "v_add_u32 v235, s94, v235\n"
      "v_min_u32 v235, 2176, v235\n"
      "v_add_u32 v235, s93, v235\n"
      "s_cmp_eq_u32 s40, 1\n"
      "s_cselect_b32 s93, 4096, 0\n"
      "s_add_u32 s92, s92, s93\n"
      "s_cmp_eq_u32 s40, 2\n"
      "s_cselect_b32 s93, 9, 8\n"
      "s_cselect_b64 s[72:73], s[98:99], s[88:89]\n"
      "s_cmp_lt_u32 s40, 2\n"
      "s_cselect_b32 s93, 27, s93\n"
      "s_mul_i32 s92, s92, s93\n"
      "s_sub_u32 s93, s93, 1\n"
      "s_lshl_b32 s93, s93, 7\n"
      "v_min_u32 v232, s93, v232\n"
      "v_add_u32 v232, s92, v232\n"
      "s_lshl_b32 s95, s40, 3\n"
      "v_lshrrev_b32 v234, 3, v8\n"
      "v_and_b32 v250, 7, v8\n"
      "v_add_u32 v249, s95, v234\n"
      "v_bfe_u32 v251, v249, 1, 3\n"
      "v_cmp_lt_u32 s[70:71], s44, v249\n"
      "v_min_u32_e64 v249, s44, v249\n"
      "s_mov_b32 s18, 100270464\n"
      "s_mov_b32 s19, 0x20000\n"
      "s_mov_b32 s78, 9604\n"
      "s_mov_b32 s79, 0x20000\n"
      "s_mov_b32 s22, 221184\n"
      "s_mov_b32 s23, 0x20000\n"
      "s_lshl_b32 s64, s40, 10\n"
      "v_add_u32 v36, s64, v219\n"
      "s_add_u32 s75, s64, 18432\n"
      "s_and_b32 s92, s40, 1\n"
      "s_lshl_b32 s92, s92, 2\n"
      "s_mul_i32 s93, s40, 9216\n"
      "v_and_b32 v248, 2, v234\n"
      "v_xor_b32 v248, v248, v250\n"
      "v_xor_b32_e64 v248, s92, v248\n"
      "v_lshl_add_u32 v248, v248, 4, s93\n"
      "v_mul_u32_u24 v187, 1152, v234\n"
      "v_add_u32 v187, v187, v248\n"
      "s_cmp_lt_u32 s40, 2\n"
      "s_cselect_b32 s65, s64, 163840\n"
      "s_waitcnt lgkmcnt(0)\n"
      "s_cselect_b64 s[72:73], s[20:21], s[72:73]\n"
      "global_load_dword v50, v50, s[86:87]\n"
      "global_load_dword a255, v232, s[72:73]\n"
      "global_load_dword a253, v235, s[46:47]\n"
      "s_mul_i32 s92, s40, 8192\n"
      "s_add_u32 s88, s88, s92\n"
      "s_addc_u32 s89, s89, 0\n"
      "s_add_u32 m0, s64, 124928\n"
      "s_movk_i32 s92, 0\n"
      "buffer_load_dwordx4 v36, s[76:79], s92 offen  lds\n"
      "s_add_u32 m0, s64, 129024\n"
      "s_movk_i32 s92, 4096\n"
      "buffer_load_dwordx4 v36, s[76:79], s92 offen  lds\n"
      "s_add_u32 m0, s64, 133120\n"
      "s_cmp_lt_u32 s40, 2\n"
      "s_cselect_b32 m0, m0, s65\n"
      "s_movk_i32 s92, 8192\n"
      "buffer_load_dwordx4 v36, s[76:79], s92 offen  lds\n"
      "s_load_dwordx16 s[0:15], s[82:83], 0\n"
      "s_load_dwordx16 s[48:63], s[84:85], 0\n"
      "s_load_dwordx16 s[24:39], s[84:85], 64\n"
      "s_load_dwordx2 s[66:67], s[80:81], 0\n"
      "s_movk_i32 s93, 4180\n"
      "s_cmp_lt_u32 s41, 209\n"
      "s_cselect_b32 s92, 209, 47\n"
      "s_cselect_b32 s93, 795, s93\n"
      "s_cmp_lt_u32 s41, 252\n"
      "s_cselect_b32 s94, 16, 15\n"
      "s_add_u32 s93, s93, s41\n"
      "v_lshl_add_u32 v232, v249, 8, s41\n"
      "v_mul_u32_u24 v235, s92, v249\n"
      "v_add_u32 v235, s93, v235\n"
      "v_cmp_lt_u32 vcc, s94, v249\n"
      "v_subrev_u32 v248, 5184, v235\n"
      "s_nop 0\n"
      "v_cndmask_b32 v232, v232, v235, vcc\n"
      "v_cmp_le_u32 vcc, 5184, v232\n"
      "v_mul_u32_u24 v46, 58255, v232\n"
      "v_lshrrev_b32 v46, 22, v46\n"
      "v_mul_u32_u24 v174, 72, v46\n"
      "v_sub_u32 v174, v232, v174\n"
      "v_min_u32 v235, 72, v248\n"
      "v_cndmask_b32 v46, v46, v235, vcc\n"
      "v_subrev_u32 v235, 72, v248\n"
      "v_min_u32 v235, 72, v235\n"
      "v_cndmask_b32 v174, v174, v235, vcc\n"
      "v_mad_u32_u24 v235, v46, 7, 9\n"
      "v_subrev_u32 v232, 0x1ff, v235\n"
      "v_min_u32 v235, v235, v232\n"
      "v_mad_u32_u24 v232, v174, 7, 9\n"
      "v_subrev_u32 v248, 0x1ff, v232\n"
      "v_min_u32 v232, v232, v248\n"
      "v_mul_u32_u24 v235, 0x1ff, v235\n"
      "v_add_u32 v235, v235, v232\n"
      "v_mul_u32_u24 v232, 0x180, v235\n"
      "v_bfrev_b32 v249, 1\n"
      "v_cndmask_b32 v232, v232, v249, s[70:71]\n"
      "v_xor_b32 v251, v251, v250\n"
      "v_lshl_add_u32 v232, v251, 4, v232\n"
      "s_add_u32 m0, s64, 149504\n"
      "s_nop 0\n"
      "buffer_load_dwordx4 v232, s[16:19], 0 offen  lds\n"
      "s_add_u32 m0, s64, 153472\n"
      "s_nop 0\n"
      "buffer_load_dwordx4 v232, s[16:19], 0 offen offset:128  lds\n"
      "s_add_u32 m0, s64, 157440\n"
      "s_nop 0\n"
      "buffer_load_dwordx4 v232, s[16:19], 0 offen offset:256  lds\n"
      "s_movk_i32 s95, 504\n"
      "s_and_b32 s92, s40, 1\n"
      "s_lshl_b32 s92, s92, 2\n"
      "v_lshrrev_b32 v232, 1, v234\n"
      "v_xor_b32 v232, v232, v250\n"
      "v_xor_b32 v232, s92, v232\n"
      "v_lshlrev_b32 v232, 4, v232\n"
      "s_lshl_b32 s92, s40, 3\n"
      "s_brev_b32 s43, 1\n"
      "s_cmp_lt_u32 s40, 2\n"
      "s_cselect_b32 s93, 32, -16\n"
      "s_cselect_b32 s94, 0, 128\n"
      "s_cselect_b32 s43, 256, s43\n"
      "v_add_u32 v235, s92, v234\n"
      "v_mul_u32_u24 v234, 37, v235\n"
      "v_lshrrev_b32_e64 v234, 8, v234\n"
      "v_mad_u32_u24 v234, v234, s95, v235\n"
      "v_mul_u32_u24 v234, 384, v234\n"
      "v_add_u32 v16, v234, v232\n"
      "v_add_u32 v234, s93, v235\n"
      "v_mul_u32_u24 v251, 37, v234\n"
      "v_lshrrev_b32 v251, 8, v251\n"
      "v_mad_u32_u24 v251, v251, s95, v234\n"
      "v_mul_u32_u24 v251, 384, v251\n"
      "v_add3_u32 v17, v251, v232, s94\n"
      "v_add_u32_e64 v234, 16, v235\n"
      "v_mul_u32_u24_e64 v235, 37, v234\n"
      "v_lshrrev_b32_e64 v235, 8, v235\n"
      "v_mad_u32_u24 v235, v235, s95, v234\n"
      "v_mul_u32_u24 v235, 384, v235\n"
      "s_movk_i32 s94, 128\n"
      "v_add3_u32 v18, v235, v232, s94\n"
      "v_add_u32 v19, 256, v16\n"
      "v_add_u32_e64 v15, s43, v17\n"
      "s_add_u32 s92, s41, 0\n"
      "s_mul_i32 s93, s92, 58255\n"
      "s_lshr_b32 s93, s93, 22\n"
      "s_mul_i32 s93, s93, 439\n"
      "s_add_u32 s45, s92, s93\n"
      "s_mul_i32 s45, s45, 2688\n"
      "s_add_u32 s45, s45, 589824\n"
      "s_add_u32 m0, s64, 0\n"
      "s_nop 0\n"
      "buffer_load_dwordx4 v16, s[16:19], s45 offen  lds\n"
      "s_add_u32 m0, s64, 4096\n"
      "s_nop 0\n"
      "buffer_load_dwordx4 v17, s[16:19], s45 offen  lds\n"
      "s_add_u32 m0, s64, 8192\n"
      "s_nop 0\n"
      "buffer_load_dwordx4 v18, s[16:19], s45 offen  lds\n"
      "s_add_u32 m0, s64, 12288\n"
      "s_nop 0\n"
      "buffer_load_dwordx4 v19, s[16:19], s45 offen  lds\n"
      "s_add_u32 m0, s65, 16384\n"
      "s_nop 0\n"
      "buffer_load_dwordx4 v15, s[16:19], s45 offen  lds\n"
      "s_add_u32 s92, s41, 0\n"
      "s_mul_i32 s93, s92, 58255\n"
      "s_lshr_b32 s93, s93, 22\n"
      "s_add_u32 s47, s92, s93\n"
      "s_mul_i32 s47, s47, 4802\n"
      "s_and_b32 s47, s47, -4\n"
      "s_add_u32 s92, s41, 256\n"
      "s_mul_i32 s93, s92, 58255\n"
      "s_lshr_b32 s93, s93, 22\n"
      "s_add_u32 s74, s92, s93\n"
      "s_mul_i32 s74, s74, 4802\n"
      "s_and_b32 s74, s74, -4\n"
      "s_mul_i32 s93, s93, 439\n"
      "s_add_u32 s46, s92, s93\n"
      "s_mul_i32 s46, s46, 2688\n"
      "s_add_u32 s46, s46, 589824\n"
      "v_subrev_u32 v232, 2, v8\n"
      "v_cmp_ge_u32 s[72:73], s44, v232\n"
      "v_min_u32_e64 v232, s44, v232\n"
      "s_movk_i32 s93, 4180\n"
      "s_cmp_lt_u32 s41, 209\n"
      "s_cselect_b32 s92, 209, 47\n"
      "s_cselect_b32 s93, 795, s93\n"
      "s_cmp_lt_u32 s41, 252\n"
      "s_cselect_b32 s94, 16, 15\n"
      "s_add_u32 s93, s93, s41\n"
      "v_lshl_add_u32 v235, v232, 8, s41\n"
      "v_mul_u32_u24 v234, s92, v232\n"
      "v_add_u32_e64 v234, s93, v234\n"
      "v_cmp_lt_u32 vcc, s94, v232\n"
      "v_subrev_u32 v251, 5184, v234\n"
      "s_nop 0\n"
      "v_cndmask_b32 v235, v235, v234, vcc\n"
      "v_cmp_le_u32 vcc, 5184, v235\n"
      "v_mul_u32_u24 v250, 58255, v235\n"
      "v_lshrrev_b32 v250, 22, v250\n"
      "v_mul_u32_u24 v248, 72, v250\n"
      "v_sub_u32_e64 v248, v235, v248\n"
      "v_min_u32 v234, 72, v251\n"
      "v_cndmask_b32 v250, v250, v234, vcc\n"
      "v_subrev_u32 v234, 72, v251\n"
      "v_min_u32 v234, 72, v234\n"
      "v_cndmask_b32 v248, v248, v234, vcc\n"
      "v_mul_u32_u24 v38, 73, v250\n"
      "v_add_u32 v38, v38, v248\n"
      "v_mul_u32_u24 v38, 4802, v38\n"
      "v_mul_u32_u24 v232, 0x1ff, v250\n"
      "v_add_u32_e64 v232, v232, v248\n"
      "v_mul_u32_u24 v232, 2688, v232\n"
      "v_add_u32 v232, 589824, v232\n"
      "v_cmp_eq_u32 vcc, 72, v250\n"
      "v_or_b32 v174, 0x80000000, v232\n"
      "s_nop 0\n"
      "v_cndmask_b32 v232, v232, v174, vcc\n"
      "v_cmp_eq_u32 vcc, 72, v248\n"
      "v_or_b32_e64 v174, 0x40000000, v232\n"
      "s_nop 0\n"
      "v_cndmask_b32 v232, v232, v174, vcc\n"
      "v_mov_b32 v234, 0x20000000\n"
      "v_cndmask_b32 v232, v234, v232, s[72:73]\n"
      "v_cndmask_b32 v38, v234, v38, s[72:73]\n"
      "v_mov_b32 v37, v234\n"
      "s_nop 1\n"
      "v_mov_b32_dpp v37, v232 wave_shr:1 row_mask:0xf bank_mask:0xf\n"
      "v_mov_b32 v79, 0\n"
      "v_mov_b32 v83, 0\n"
      "v_mov_b32 v87, 0\n"
      "v_mov_b32 v91, 0\n"
      "v_mov_b32 v95, 0\n"
      "s_lshl_b32 s94, s40, 2\n"
      "s_add_u32 s94, s94, 115456\n"
      "v_add_u32 v31, s94, v219\n"
      "v_and_b32 v174, 3, v8\n"
      "v_cmp_ne_u32 vcc, 0, v174\n"
      "v_mov_b32 v174, 0x3f803f80\n"
      "s_nop 0\n"
      "v_cndmask_b32_e64 v32, v174, 0, vcc\n"
      "v_mov_b32 v33, v32\n"
      "v_mov_b32 v34, v32\n"
      "v_mov_b32 v35, v32\n"
      "v_and_b32 v136, 15, v8\n"
      "v_lshrrev_b32_e64 v139, 4, v8\n"
      "v_mul_u32_u24 v136, 196, v136\n"
      "v_add_u32 v136, 124928, v136\n"
      "v_lshl_add_u32 v139, v139, 4, v136\n"
      "v_min_u32 v239, 192, v233\n"
      "v_add_u32 v239, 124928, v239\n"
      "global_load_dwordx4 v[232:235], v219, s[88:89]\n"
      "s_waitcnt lgkmcnt(0)\n"
      "s_pack_ll_b32_b16 s92, s0, s8\n"
      "s_waitcnt vmcnt(14)\n"
      "v_dot2_f32_bf16 v46, s92, v50, 0\n"
      "s_pack_hh_b32_b16 s93, s0, s8\n"
      "v_dot2_f32_bf16 v43, s93, v50, 0\n"
      "s_pack_ll_b32_b16 s94, s1, s9\n"
      "v_dot2_f32_bf16 v212, s94, v50, 0\n"
      "s_pack_hh_b32_b16 s95, s1, s9\n"
      "v_dot2_f32_bf16 v42, s95, v50, 0\n"
      "s_pack_ll_b32_b16 s92, s2, s10\n"
      "v_dot2_f32_bf16 v193, s92, v50, 0\n"
      "s_pack_hh_b32_b16 s93, s2, s10\n"
      "v_dot2_f32_bf16 v177, s93, v50, 0\n"
      "s_pack_ll_b32_b16 s94, s3, s11\n"
      "v_dot2_f32_bf16 v175, s94, v50, 0\n"
      "s_pack_hh_b32_b16 s95, s3, s11\n"
      "v_dot2_f32_bf16 v176, s95, v50, 0\n"
      "s_pack_ll_b32_b16 s92, s4, s12\n"
      "v_dot2_f32_bf16 v47, s92, v50, 0\n"
      "s_pack_hh_b32_b16 s93, s4, s12\n"
      "v_dot2_f32_bf16 v215, s93, v50, 0\n"
      "s_pack_ll_b32_b16 s94, s5, s13\n"
      "v_dot2_f32_bf16 v213, s94, v50, 0\n"
      "global_load_dwordx4 v[248:251], v219, s[88:89] offset:1024\n"
      "s_pack_hh_b32_b16 s95, s5, s13\n"
      "v_dot2_f32_bf16 v214, s95, v50, 0\n"
      "s_pack_ll_b32_b16 s92, s6, s14\n"
      "v_dot2_f32_bf16 v126, s92, v50, 0\n"
      "s_pack_hh_b32_b16 s93, s6, s14\n"
      "v_dot2_f32_bf16 v192, s93, v50, 0\n"
      "s_pack_ll_b32_b16 s94, s7, s15\n"
      "v_dot2_f32_bf16 v194, s94, v50, 0\n"
      "s_pack_hh_b32_b16 s95, s7, s15\n"
      "v_dot2_f32_bf16 v48, s95, v50, 0\n"
      "s_lshl_b32 s92, s40, 4\n"
      "s_lshr_b64 s[66:67], s[66:67], s92\n"
      "s_lshl_b32 s66, s66, 16\n"
      "v_mov_b32 v50, s66\n"
      "v_max_f32 v50, 0xc2c80000, v50\n"
      "v_min_f32 v50, 0x40930000, v50\n"
      "v_mul_f32 v50, 0x3fb8aa3b, v50\n"
      "v_exp_f32 v50, v50\n"
      "s_nop 0\n"
      "v_cvt_pk_bf16_f32 v50, v50, v50\n"
      "v_lshlrev_b32 v50, 16, v50\n"
      "v_max_f32 v46, 0, v46\n"
      "v_max_f32_e64 v43, 0, v43\n"
      "global_load_dwordx4 v[0:3], v219, s[88:89] offset:2048\n"
      "v_cvt_pk_bf16_f32 v46, v46, v43\n"
      "v_max_f32 v212, 0, v212\n"
      "v_max_f32 v42, 0, v42\n"
      "v_cvt_pk_bf16_f32 v212, v212, v42\n"
      "v_max_f32 v193, 0, v193\n"
      "v_max_f32 v177, 0, v177\n"
      "v_cvt_pk_bf16_f32 v193, v193, v177\n"
      "v_max_f32 v175, 0, v175\n"
      "v_max_f32 v176, 0, v176\n"
      "v_cvt_pk_bf16_f32 v175, v175, v176\n"
      "v_max_f32 v47, 0, v47\n"
      "v_max_f32 v215, 0, v215\n"
      "v_cvt_pk_bf16_f32 v47, v47, v215\n"
      "v_max_f32 v213, 0, v213\n"
      "v_max_f32 v214, 0, v214\n"
      "v_cvt_pk_bf16_f32 v213, v213, v214\n"
      "v_max_f32_e64 v126, 0, v126\n"
      "v_max_f32_e64 v192, 0, v192\n"
      "v_cvt_pk_bf16_f32 v126, v126, v192\n"
      "v_max_f32_e64 v194, 0, v194\n"
      "v_max_f32_e64 v48, 0, v48\n"
      "v_cvt_pk_bf16_f32 v194, v194, v48\n"
      "s_mov_b32 s42, 0\n"
      "s_add_u32 m0, s75, 0\n"
      "s_nop 0\n"
      "buffer_load_dwordx4 v187, s[20:23], s42 offen  lds\n"
      "s_pack_ll_b32_b16 s92, s48, s50\n"
      "v_dot2_f32_bf16 v42, s92, v46, 0\n"
      "s_pack_hh_b32_b16 s93, s48, s50\n"
      "v_dot2_f32_bf16 v43, s93, v46, 0\n"
      "s_pack_ll_b32_b16 s94, s49, s51\n"
      "v_dot2_f32_bf16 v215, s94, v46, 0\n"
      "s_pack_hh_b32_b16 s95, s49, s51\n"
      "v_dot2_f32_bf16 v214, s95, v46, 0\n"
      "s_pack_ll_b32_b16 s92, s52, s54\n"
      "v_dot2_f32_bf16 v42, s92, v212, v42\n"
      "s_pack_hh_b32_b16 s93, s52, s54\n"
      "v_dot2_f32_bf16 v43, s93, v212, v43\n"
      "s_pack_ll_b32_b16 s94, s53, s55\n"
      "v_dot2_f32_bf16 v215, s94, v212, v215\n"
      "s_pack_hh_b32_b16 s95, s53, s55\n"
      "v_dot2_f32_bf16 v214, s95, v212, v214\n"
      "s_pack_ll_b32_b16 s92, s56, s58\n"
      "v_dot2_f32_bf16 v42, s92, v193, v42\n"
      "s_pack_hh_b32_b16 s93, s56, s58\n"
      "v_dot2_f32_bf16 v43, s93, v193, v43\n"
      "s_pack_ll_b32_b16 s94, s57, s59\n"
      "v_dot2_f32_bf16 v215, s94, v193, v215\n"
      "s_add_u32 m0, s75, 3968\n"
      "s_nop 0\n"
      "buffer_load_dwordx4 v187, s[20:23], s42 offen offset:128  lds\n"
      "s_pack_hh_b32_b16 s95, s57, s59\n"
      "v_dot2_f32_bf16 v214, s95, v193, v214\n"
      "s_pack_ll_b32_b16 s92, s60, s62\n"
      "v_dot2_f32_bf16 v42, s92, v175, v42\n"
      "s_pack_hh_b32_b16 s93, s60, s62\n"
      "v_dot2_f32_bf16 v43, s93, v175, v43\n"
      "s_pack_ll_b32_b16 s94, s61, s63\n"
      "v_dot2_f32_bf16 v215, s94, v175, v215\n"
      "s_pack_hh_b32_b16 s95, s61, s63\n"
      "v_dot2_f32_bf16 v214, s95, v175, v214\n"
      "s_pack_ll_b32_b16 s92, s24, s26\n"
      "v_dot2_f32_bf16 v42, s92, v47, v42\n"
      "s_pack_hh_b32_b16 s93, s24, s26\n"
      "v_dot2_f32_bf16 v43, s93, v47, v43\n"
      "s_pack_ll_b32_b16 s94, s25, s27\n"
      "v_dot2_f32_bf16 v215, s94, v47, v215\n"
      "s_pack_hh_b32_b16 s95, s25, s27\n"
      "v_dot2_f32_bf16 v214, s95, v47, v214\n"
      "s_pack_ll_b32_b16 s92, s28, s30\n"
      "v_dot2_f32_bf16 v42, s92, v213, v42\n"
      "s_pack_hh_b32_b16 s93, s28, s30\n"
      "v_dot2_f32_bf16 v43, s93, v213, v43\n"
      "s_pack_ll_b32_b16 s94, s29, s31\n"
      "s_add_u32 m0, s75, 7936\n"
      "s_nop 0\n"
      "buffer_load_dwordx4 v187, s[20:23], s42 offen offset:256  lds\n"
      "v_dot2_f32_bf16 v215, s94, v213, v215\n"
      "s_pack_hh_b32_b16 s95, s29, s31\n"
      "v_dot2_f32_bf16 v214, s95, v213, v214\n"
      "s_pack_ll_b32_b16 s92, s32, s34\n"
      "v_dot2_f32_bf16 v42, s92, v126, v42\n"
      "s_pack_hh_b32_b16 s93, s32, s34\n"
      "v_dot2_f32_bf16 v43, s93, v126, v43\n"
      "s_pack_ll_b32_b16 s94, s33, s35\n"
      "v_dot2_f32_bf16 v215, s94, v126, v215\n"
      "s_pack_hh_b32_b16 s95, s33, s35\n"
      "v_dot2_f32_bf16 v214, s95, v126, v214\n"
      "s_pack_ll_b32_b16 s92, s36, s38\n"
      "v_dot2_f32_bf16 v42, s92, v194, v42\n"
      "s_pack_hh_b32_b16 s93, s36, s38\n"
      "v_dot2_f32_bf16 v43, s93, v194, v43\n"
      "s_pack_ll_b32_b16 s94, s37, s39\n"
      "v_dot2_f32_bf16 v215, s94, v194, v215\n"
      "s_pack_hh_b32_b16 s95, s37, s39\n"
      "v_dot2_f32_bf16 v214, s95, v194, v214\n"
      "s_load_dwordx2 s[28:29], s[90:91], 8\n"
      "s_load_dwordx2 s[34:35], s[90:91], 16\n"
      "s_load_dwordx2 s[24:25], s[90:91], 24\n"
      "s_add_u32 m0, s75, 11904\n"
      "s_nop 0\n"
      "buffer_load_dwordx4 v187, s[20:23], s42 offen offset:384  lds\n"
      "s_load_dwordx2 s[36:37], s[90:91], 40\n"
      "s_mov_b32 s26, 100270464\n"
      "s_mov_b32 s27, 0x20000\n"
      "s_mov_b32 s38, 73728\n"
      "s_mov_b32 s39, 0x20000\n"
      "s_mov_b32 s30, 25589860\n"
      "s_mov_b32 s31, 0x20000\n"
      "s_mov_b32 s53, 0x3fb8aa3b\n"
      "s_mov_b32 s51, 0x2a8cbccc\n"
      "s_mov_b32 s52, 0x38fba882\n"
      "s_mov_b32 s58, 0xffff0000\n"
      "s_mov_b32 s59, 0x0\n"
      "s_mov_b32 s60, 0x0\n"
      "s_mov_b32 s61, 0xffff\n"
      "s_mov_b32 s62, 0x0\n"
      "s_mov_b32 s63, 0xffff0000\n"
      "v_mov_b32 v174, 0x41f00000\n"
      "v_cmp_nle_f32 s[54:55], v50, v174\n"
      "v_mul_f32 v50, s53, v50\n"
      "s_nop 0\n"
      "v_readfirstlane_b32 s50, v50\n"
      "v_cvt_pk_bf16_f32 v42, v42, v42\n"
      "v_lshlrev_b32 v42, 16, v42\n"
      "s_add_u32 m0, s75, 15872\n"
      "s_nop 0\n"
      "buffer_load_dwordx4 v187, s[20:23], s42 offen offset:512  lds\n"
      "v_mul_f32 v42, 0xbfb8aa3b, v42\n"
      "v_exp_f32 v42, v42\n"
      "s_nop 0\n"
      "v_add_f32 v42, 1.0, v42\n"
      "v_rcp_f32 v42, v42\n"
      "s_nop 0\n"
      "v_cvt_pk_bf16_f32 v42, v42, v42\n"
      "v_lshlrev_b32 v42, 16, v42\n"
      "v_add_f32_e64 v42, -0.5, v42\n"
      "v_mul_f32 v42, 0x41b8aa3b, v42\n"
      "ds_write_b32 v51, v42\n"
      "v_cvt_pk_bf16_f32 v43, v43, v43\n"
      "v_lshlrev_b32 v43, 16, v43\n"
      "v_mul_f32 v43, 0xbfb8aa3b, v43\n"
      "v_exp_f32 v43, v43\n"
      "s_nop 0\n"
      "v_add_f32 v43, 1.0, v43\n"
      "v_rcp_f32 v43, v43\n"
      "s_nop 0\n"
      "v_cvt_pk_bf16_f32 v43, v43, v43\n"
      "v_lshlrev_b32 v43, 16, v43\n"
      "v_add_f32 v43, -0.5, v43\n"
      "v_mul_f32 v43, 0x41b8aa3b, v43\n"
      "ds_write_b32 v51, v43 offset:704\n"
      "v_cvt_pk_bf16_f32 v215, v215, v215\n"
      "v_lshlrev_b32_e64 v215, 16, v215\n"
      "s_add_u32 m0, s75, 19840\n"
      "s_nop 0\n"
      "buffer_load_dwordx4 v187, s[20:23], s42 offen offset:640  lds\n"
      "v_mul_f32 v215, 0xbfb8aa3b, v215\n"
      "v_exp_f32 v215, v215\n"
      "s_nop 0\n"
      "v_add_f32 v215, 1.0, v215\n"
      "v_rcp_f32 v215, v215\n"
      "s_nop 0\n"
      "v_cvt_pk_bf16_f32 v215, v215, v215\n"
      "v_lshlrev_b32 v215, 16, v215\n"
      "v_add_f32 v215, -0.5, v215\n"
      "v_mul_f32 v215, 0x41b8aa3b, v215\n"
      "ds_write_b32 v51, v215 offset:1408\n"
      "v_cvt_pk_bf16_f32 v214, v214, v214\n"
      "v_lshlrev_b32_e64 v214, 16, v214\n"
      "v_mul_f32 v214, 0xbfb8aa3b, v214\n"
      "v_exp_f32 v214, v214\n"
      "s_nop 0\n"
      "v_add_f32 v214, 1.0, v214\n"
      "v_rcp_f32_e64 v214, v214\n"
      "s_nop 0\n"
      "v_cvt_pk_bf16_f32 v214, v214, v214\n"
      "v_lshlrev_b32 v214, 16, v214\n"
      "v_add_f32 v214, -0.5, v214\n"
      "v_mul_f32 v214, 0x41b8aa3b, v214\n"
      "ds_write_b32 v51, v214 offset:2112\n"
      "s_mul_i32 s76, s40, 704\n"
      "s_add_u32 s76, s76, 122112\n"
      "s_mov_b32 s43, 36864\n"
      "s_add_u32 m0, s75, 24576\n"
      "s_nop 0\n"
      "buffer_load_dwordx4 v187, s[20:23], s43 offen  lds\n"
      "s_add_u32 m0, s75, 28544\n"
      "s_nop 0\n"
      "buffer_load_dwordx4 v187, s[20:23], s43 offen offset:128  lds\n"
      "s_add_u32 m0, s75, 32512\n"
      "s_nop 0\n"
      "buffer_load_dwordx4 v187, s[20:23], s43 offen offset:256  lds\n"
      "s_add_u32 m0, s75, 36480\n"
      "s_nop 0\n"
      "buffer_load_dwordx4 v187, s[20:23], s43 offen offset:384  lds\n"
      "s_add_u32 m0, s75, 40448\n"
      "s_nop 0\n"
      "buffer_load_dwordx4 v187, s[20:23], s43 offen offset:512  lds\n"
      "s_add_u32 m0, s75, 44416\n"
      "s_nop 0\n"
      "buffer_load_dwordx4 v187, s[20:23], s43 offen offset:640  lds\n"
      "s_mov_b32 s45, s46\n"
      "s_mov_b32 s49, s47\n"
      "s_mov_b32 s66, 0\n"
      "s_mov_b32 s67, 0\n"
      "s_mov_b32 s68, 0\n"
      "s_mov_b32 s69, 0\n"
      "s_mov_b32 s70, 0\n"
      "s_mov_b32 s71, 0\n"
      "s_mov_b32 s48, 0\n"
      "s_mov_b32 s33, 0x7fff7fff\n"
      "s_mov_b32 s42, 1\n"
      "s_mov_b32 s43, 5\n"
      "v_readlane_b32 s47, v38, 4\n"
      "s_nop 0\n"
      "s_and_b32 s47, s47, -4\n"
      "s_waitcnt vmcnt(12)\n"
      "v_add_u32 v200, 149504, v0\n"
      "v_add_u32 v116, 149504, v1\n"
      "v_add_u32 v140, 49152, v232\n"
      "v_add_u32 v143, 49152, v233\n"
      "v_add_u32 v142, 49152, v234\n"
      "v_add_u32 v89, 49152, v235\n"
      "v_add_u32 v82, 49152, v248\n"
      "s_waitcnt vmcnt(6)\n"
      "s_waitcnt lgkmcnt(0)\n"
      "s_barrier\n"
      "s_add_u32 m0, s75, 49152\n"
      "s_mov_b32 s78, 73728\n"
      "buffer_load_dwordx4 v187, s[20:23], s78 offen  lds\n"
      "ds_read_b128 a[224:227], v0 offset:2048\n"
      "ds_read_b64_tr_b16 a[24:25], v233\n"
      "ds_read_b128 a[220:223], v0\n"
      "ds_read_b64_tr_b16 a[120:121], v234 offset:12288\n"
      "s_add_u32 m0, s75, 53120\n"
      "ds_read_b64_tr_b16 a[0:1], v232\n"
      "buffer_load_dwordx4 v187, s[20:23], s78 offen offset:128  lds\n"
      "ds_read_b128 a[244:247], v1 offset:4096\n"
      "ds_read_b128 a[228:231], v0 offset:4096\n"
      "ds_read_b64_tr_b16 a[50:51], v234 offset:512\n"
      "ds_read_b64_tr_b16 a[74:75], v232 offset:12800\n"
      "s_mov_b32 s83, 0x0\n"
      "s_add_u32 m0, s75, 57088\n"
      "ds_read_b64_tr_b16 a[122:123], v234 offset:12800\n"
      "buffer_load_dwordx4 v187, s[20:23], s78 offen offset:256  lds\n"
      "ds_read_b64_tr_b16 a[2:3], v232 offset:512\n"
      "ds_read_b64_tr_b16 a[98:99], v233 offset:12800\n"
      "ds_read2_b32 v[50:51], v139 offset0:34 offset1:35\n"
      "s_mov_b32 s79, 110592\n"
      "ds_read_b64_tr_b16 a[48:49], v234\n"
      "s_add_u32 m0, s75, 61056\n"
      "ds_read2_b32 v[42:43], v139 offset0:2 offset1:3\n"
      "buffer_load_dwordx4 v187, s[20:23], s78 offen offset:384  lds\n"
      "ds_read_b64_tr_b16 a[26:27], v233 offset:512\n"
      "ds_read_b64_tr_b16 a[96:97], v233 offset:12288\n"
      "s_add_u32 m0, s75, 65024\n"
      "ds_read_b128 a[216:219], v200\n"
      "v_add_u32 v44, 6272, v139\n"
      "s_waitcnt lgkmcnt(7)\n"
      "v_mfma_f32_16x16x32_bf16 v[212:215], a[120:123], a[228:231], 0\n"
      "ds_read_b64_tr_b16 a[72:73], v232 offset:12288\n"
      "buffer_load_dwordx4 v187, s[20:23], s78 offen offset:512  lds\n"
      "v_add_u32 v225, 3136, v139\n"
      "v_mfma_f32_16x16x32_bf16 v[174:177], a[0:3], a[220:223], 0\n"
      "ds_read_b128 a[232:235], v116\n"
      "s_add_u32 m0, s75, 68992\n"
      "ds_read_b128 a[240:243], v1 offset:2048\n"
      "v_mfma_f32_16x16x32_bf16 v[192:195], a[120:123], a[220:223], 0\n"
      "ds_read2_b32 v[46:47], v139 offset0:18 offset1:19\n"
      "ds_read_b32 v100, v136 offset:192\n"
      "buffer_load_dwordx4 v187, s[20:23], s78 offen offset:640  lds\n"
      "ds_read2_b32 v[48:49], v139 offset0:32 offset1:33\n"
      "v_mfma_f32_16x16x32_bf16 v[124:127], a[120:123], a[224:227], 0\n"
      "ds_read2_b32 v[72:73], v44 offset0:32 offset1:33\n"
      "ds_read_b32 v138, v239 offset:9408\n"
      "v_mfma_f32_16x16x32_bf16 v[208:211], a[0:3], a[228:231], 0\n"
      "ds_read2_b32 v[68:69], v44 offset0:16 offset1:17\n"
      "s_waitcnt lgkmcnt(7)\n"
      "v_mfma_f32_16x16x32_bf16 v[236:239], a[120:123], a[216:219], 0\n"
      "global_load_dwordx4 v[4:7], v219, s[88:89] offset:3072\n"
      "s_waitcnt vmcnt(7)\n"
      "s_barrier\n"
      "v_mfma_f32_16x16x32_bf16 v[240:243], a[96:99], a[216:219], 0\n"
      "s_add_u32 s88, s88, 4096\n"
      "s_addc_u32 s89, s89, 0\n"
      "ds_read_b64_tr_b16 a[4:5], v232 offset:24576\n"
      "ds_read_b64_tr_b16 a[102:103], v233 offset:37376\n"
      "ds_read_b64_tr_b16 a[30:31], v233 offset:25088\n"
      "ds_read_b64_tr_b16 a[76:77], v232 offset:36864\n"
      "global_load_dwordx4 v[8:11], v219, s[88:89]\n"
      "ds_read_b64_tr_b16 a[28:29], v233 offset:24576\n"
      "ds_read_b64_tr_b16 a[78:79], v232 offset:37376\n"
      "v_mfma_f32_16x16x32_bf16 v[188:191], a[48:51], a[228:231], 0\n"
      "ds_read_b64_tr_b16 a[124:125], v234 offset:36864\n"
      "ds_read_b64_tr_b16 a[6:7], v232 offset:25088\n"
      "ds_read_b64_tr_b16 a[52:53], v234 offset:24576\n"
      "ds_read_b64_tr_b16 a[100:101], v233 offset:36864\n"
      "ds_read_b64_tr_b16 a[126:127], v234 offset:37376\n"
      "ds_read_b64_tr_b16 a[54:55], v234 offset:25088\n"
      "global_load_dwordx4 v[12:15], v219, s[88:89] offset:1024\n"
      "ds_read2_b32 v[64:65], v44 offset0:0 offset1:1\n"
      "v_mfma_f32_16x16x32_bf16 v[96:99], a[48:51], a[224:227], 0\n"
      "ds_read2_b32 v[54:55], v225 offset0:2 offset1:3\n"
      "v_readlane_b32 s86, v38, s43\n"
      "v_mfma_f32_16x16x32_bf16 v[106:109], a[72:75], a[228:231], 0\n"
      "ds_read2_b32 v[58:59], v225 offset0:18 offset1:19\n"
      "ds_read2_b32 v[66:67], v44 offset0:2 offset1:3\n"
      "v_mfma_f32_16x16x32_bf16 v[244:247], a[24:27], a[228:231], 0\n"
      "s_mov_b32 s91, 0x1ffff\n"
      "global_load_dwordx4 v[20:23], v219, s[88:89] offset:2048\n"
      "v_mfma_f32_16x16x32_bf16 v[130:133], a[48:51], a[216:219], 0\n"
      "ds_read2_b32 v[70:71], v44 offset0:18 offset1:19\n"
      "v_mfma_f32_16x16x32_bf16 v[226:229], a[24:27], a[216:219], 0\n"
      "ds_read2_b32 v[40:41], v139 offset0:0 offset1:1\n"
      "v_mov_b32 v221, 0xf149f2ca\n"
      "v_mfma_f32_16x16x32_bf16 v[252:255], a[24:27], a[224:227], 0\n"
      "s_waitcnt lgkmcnt(5)\n"
      "v_lshl_add_u32 v73, v73, 2, s76\n"
      "v_lshl_add_u32 v49, v49, 2, s76\n"
      "v_mfma_f32_16x16x32_bf16 v[118:121], a[48:51], a[220:223], 0\n"
      "global_load_dwordx4 v[24:27], v219, s[88:89] offset:3072\n"
      "v_mfma_f32_16x16x32_bf16 v[216:219], a[96:99], a[224:227], 0\n"
      "ds_read_b32 v134, v136 offset:3328\n"
      "s_add_u32 m0, s75, 73728\n"
      "ds_read_b128 a[236:239], v1\n"
      "s_mov_b32 s78, 147456\n"
      "v_mfma_f32_16x16x32_bf16 v[216:219], a[100:103], a[240:243], v[216:219]\n"
      "ds_read2_b32 v[62:63], v225 offset0:34 offset1:35\n"
      "v_lshl_add_u32 v138, v138, 2, s76\n"
      "v_mfma_f32_16x16x32_bf16 v[202:205], a[0:3], a[224:227], 0\n"
      "buffer_load_dwordx4 v187, s[20:23], s79 offen  lds\n"
      "v_mfma_f32_16x16x32_bf16 v[180:183], a[72:75], a[224:227], 0\n"
      "ds_read_b128 a[252:255], v0 offset:6144\n"
      "s_mov_b32 s90, 0xffffffff\n"
      "v_lshl_add_u32 v65, v65, 2, s76\n"
      "v_lshl_add_u32 v64, v64, 2, s76\n"
      "v_mfma_f32_16x16x32_bf16 v[180:183], a[76:79], a[240:243], v[180:183]\n"
      "s_add_u32 m0, s75, 77696\n"
      "ds_read2_b32 v[52:53], v225 offset0:0 offset1:1\n"
      "v_lshl_add_u32 v100, v100, 2, s76\n"
      "v_mfma_f32_16x16x32_bf16 v[160:163], a[72:75], a[216:219], 0\n"
      "buffer_load_dwordx4 v187, s[20:23], s79 offen offset:128  lds\n"
      "v_mfma_f32_16x16x32_bf16 v[130:133], a[52:55], a[232:235], v[130:133]\n"
      "ds_read2_b32 v[60:61], v225 offset0:32 offset1:33\n"
      "v_lshl_add_u32 v68, v68, 2, s76\n"
      "v_mfma_f32_16x16x32_bf16 v[166:169], a[0:3], a[216:219], 0\n"
      "ds_read_b32 v68, v68\n"
      "s_waitcnt lgkmcnt(4)\n"
      "v_lshl_add_u32 v40, v40, 2, s76\n"
      "s_add_u32 m0, s75, 81664\n"
      "v_lshl_add_u32 v59, v59, 2, s76\n"
      "v_mfma_f32_16x16x32_bf16 v[144:147], a[72:75], a[220:223], 0\n"
      "buffer_load_dwordx4 v187, s[20:23], s79 offen offset:256  lds\n"
      "v_mfma_f32_16x16x32_bf16 v[144:147], a[76:79], a[236:239], v[144:147]\n"
      "ds_read_b128 a[248:251], v200 offset:4096\n"
      "v_lshl_add_u32 v48, v48, 2, s76\n"
      "v_mfma_f32_16x16x32_bf16 v[226:229], a[28:31], a[232:235], v[226:229]\n"
      "ds_read_b128 a[216:219], v0 offset:8192\n"
      "v_lshl_add_u32 v47, v47, 2, s76\n"
      "v_mfma_f32_16x16x32_bf16 v[196:199], a[96:99], a[220:223], 0\n"
      "ds_read2_b32 v[56:57], v225 offset0:16 offset1:17\n"
      "s_add_u32 m0, s75, 85632\n"
      "v_mfma_f32_16x16x32_bf16 v[188:191], a[52:55], a[244:247], v[188:191]\n"
      "buffer_load_dwordx4 v187, s[20:23], s79 offen offset:384  lds\n"
      "v_mfma_f32_16x16x32_bf16 v[166:169], a[4:7], a[232:235], v[166:169]\n"
      "s_add_u32 m0, s75, 89600\n"
      "v_lshl_add_u32 v51, v51, 2, s76\n"
      "v_lshl_add_u32 v58, v58, 2, s76\n"
      "v_mfma_f32_16x16x32_bf16 v[222:225], a[24:27], a[220:223], 0\n"
      "s_waitcnt vmcnt(9)\n"
      "s_barrier\n"
      "ds_read_b64_tr_b16 a[34:35], v143 offset:512\n"
      "ds_read_b64_tr_b16 a[56:57], v142\n"
      "v_mfma_f32_16x16x32_bf16 v[222:225], a[28:31], a[236:239], v[222:225]\n"
      "buffer_load_dwordx4 v187, s[20:23], s79 offen offset:512  lds\n"
      "v_mfma_f32_16x16x32_bf16 v[118:121], a[52:55], a[236:239], v[118:121]\n"
      "ds_read_b64_tr_b16 a[130:131], v142 offset:12800\n"
      "ds_read_b64_tr_b16 a[8:9], v140\n"
      "ds_read_b64_tr_b16 a[80:81], v140 offset:12288\n"
      "ds_read_b64_tr_b16 a[10:11], v140 offset:512\n"
      "v_mfma_f32_16x16x32_bf16 v[112:115], a[96:99], a[228:231], 0\n"
      "s_add_u32 m0, s75, 93568\n"
      "ds_read_b64_tr_b16 a[106:107], v143 offset:12800\n"
      "buffer_load_dwordx4 v187, s[20:23], s79 offen offset:640  lds\n"
      "ds_read_b64_tr_b16 a[32:33], v143\n"
      "ds_read_b64_tr_b16 a[82:83], v140 offset:12800\n"
      "ds_read_b64_tr_b16 a[104:105], v143 offset:12288\n"
      "v_mfma_f32_16x16x32_bf16 v[112:115], a[100:103], a[244:247], v[112:115]\n"
      "ds_read_b64_tr_b16 a[128:129], v142 offset:12288\n"
      "ds_read_b64_tr_b16 a[58:59], v142 offset:512\n"
      "s_add_u32 m0, s75, 0\n"
      "v_mfma_f32_16x16x32_bf16 v[212:215], a[124:127], a[244:247], v[212:215]\n"
      "buffer_load_dwordx4 v187, s[20:23], s78 offen  lds\n"
      "v_mfma_f32_16x16x32_bf16 v[208:211], a[4:7], a[244:247], v[208:211]\n"
      "ds_read2_b32 v[74:75], v44 offset0:34 offset1:35\n"
      "s_add_u32 m0, s75, 3968\n"
      "v_mfma_f32_16x16x32_bf16 v[236:239], a[124:127], a[232:235], v[236:239]\n"
      "ds_read_b128 a[220:223], v0 offset:10240\n"
      "ds_read2_b32 v[44:45], v139 offset0:16 offset1:17\n"
      "v_lshl_add_u32 v72, v72, 2, s76\n"
      "v_lshl_add_u32 v66, v66, 2, s76\n"
      "v_mfma_f32_16x16x32_bf16 v[124:127], a[124:127], a[240:243], v[124:127]\n"
      "buffer_load_dwordx4 v187, s[20:23], s78 offen offset:128  lds\n"
      "v_mfma_f32_16x16x32_bf16 v[202:205], a[4:7], a[240:243], v[202:205]\n"
      "s_waitcnt lgkmcnt(3)\n"
      "v_lshl_add_u32 v53, v53, 2, s76\n"
      "s_add_u32 m0, s75, 7936\n"
      "v_mfma_f32_16x16x32_bf16 v[244:247], a[28:31], a[244:247], v[244:247]\n"
      "ds_read_b32 v51, v51\n"
      "v_readlane_b32 s84, v37, s43\n"
      "v_mfma_f32_16x16x32_bf16 v[240:243], a[100:103], a[232:235], v[240:243]\n"
      "ds_read_b32 v139, v136 offset:6464\n"
      "v_lshl_add_u32 v43, v43, 2, s76\n"
      "v_mfma_f32_16x16x32_bf16 v[240:243], a[104:107], a[248:251], v[240:243]\n"
      "buffer_load_dwordx4 v187, s[20:23], s78 offen offset:256  lds\n"
      "v_mfma_f32_16x16x32_bf16 v[252:255], a[28:31], a[240:243], v[252:255]\n"
      "s_add_u32 m0, s75, 11904\n"
      "ds_read_b128 a[224:227], v116 offset:4096\n"
      "v_mfma_f32_16x16x32_bf16 v[252:255], a[32:35], a[216:219], v[252:255]\n"
      "ds_read_b32 v47, v47\n"
      "v_lshl_add_u32 v50, v50, 2, s76\n"
      "v_mfma_f32_16x16x32_bf16 v[174:177], a[4:7], a[236:239], v[174:177]\n"
      "ds_read_b128 a[228:231], v1 offset:6144\n"
      "v_lshl_add_u32 v63, v63, 2, s76\n"
      "v_mfma_f32_16x16x32_bf16 v[96:99], a[52:55], a[240:243], v[96:99]\n"
      "buffer_load_dwordx4 v187, s[20:23], s78 offen offset:384  lds\n"
      "v_mfma_f32_16x16x32_bf16 v[96:99], a[56:59], a[216:219], v[96:99]\n"
      "ds_read_b32 v66, v66\n"
      "s_waitcnt lgkmcnt(4)\n"
      "v_mfma_f32_16x16x32_bf16 v[112:115], a[104:107], a[220:223], v[112:115]\n"
      "ds_read_b32 v65, v65\n"
      "v_lshl_add_u32 v70, v70, 2, s76\n"
      "v_mfma_f32_16x16x32_bf16 v[192:195], a[124:127], a[236:239], v[192:195]\n"
      "s_add_u32 m0, s75, 15872\n"
      "ds_read_b32 v49, v49\n"
      "v_mfma_f32_16x16x32_bf16 v[192:195], a[128:131], a[252:255], v[192:195]\n"
      "buffer_load_dwordx4 v187, s[20:23], s78 offen offset:512  lds\n"
      "v_mfma_f32_16x16x32_bf16 v[166:169], a[8:11], a[248:251], v[166:169]\n"
      "ds_read_b32 v138, v138\n"
      "s_add_u32 m0, s75, 19840\n"
      "v_lshl_add_u32 v67, v67, 2, s76\n"
      "v_lshl_add_u32 v41, v41, 2, s76\n"
      "v_mfma_f32_16x16x32_bf16 v[180:183], a[80:83], a[216:219], v[180:183]\n"
      "v_lshl_add_u32 v46, v46, 2, s76\n"
      "v_lshl_add_u32 v61, v61, 2, s76\n"
      "v_lshl_add_u32 v52, v52, 2, s76\n"
      "v_mfma_f32_16x16x32_bf16 v[160:163], a[76:79], a[232:235], v[160:163]\n"
      "buffer_load_dwordx4 v187, s[20:23], s78 offen offset:640  lds\n"
      "v_mfma_f32_16x16x32_bf16 v[202:205], a[8:11], a[216:219], v[202:205]\n"
      "s_mov_b32 s79, 184320\n"
      "v_lshl_add_u32 v42, v42, 2, s76\n"
      "v_lshl_add_u32 v57, v57, 2, s76\n"
      "v_mfma_f32_16x16x32_bf16 v[236:239], a[128:131], a[248:251], v[236:239]\n"
      "ds_read_b128 a[232:235], v1 offset:8192\n"
      "v_lshl_add_u32 v69, v69, 2, s76\n"
      "v_mfma_f32_16x16x32_bf16 v[208:211], a[8:11], a[220:223], v[208:211]\n"
      "ds_read_b32 v52, v52\n"
      "s_add_u32 m0, s75, 24576\n"
      "v_mfma_f32_16x16x32_bf16 v[196:199], a[100:103], a[236:239], v[196:199]\n"
      "buffer_load_dwordx4 v187, s[20:23], s79 offen  lds\n"
      "v_mfma_f32_16x16x32_bf16 v[118:121], a[56:59], a[252:255], v[118:121]\n"
      "s_add_u32 m0, s75, 28544\n"
      "s_waitcnt vmcnt(7)\n"
      "s_barrier\n"
      "v_mfma_f32_16x16x32_bf16 v[124:127], a[128:131], a[216:219], v[124:127]\n"
      "ds_read_b64_tr_b16 a[134:135], v142 offset:37376\n"
      "ds_read_b64_tr_b16 a[14:15], v140 offset:25088\n"
      "v_mfma_f32_16x16x32_bf16 v[212:215], a[128:131], a[220:223], v[212:215]\n"
      "ds_read_b64_tr_b16 a[12:13], v140 offset:24576\n"
      "ds_read_b64_tr_b16 a[36:37], v143 offset:24576\n"
      "v_mfma_f32_16x16x32_bf16 v[196:199], a[104:107], a[252:255], v[196:199]\n"
      "buffer_load_dwordx4 v187, s[20:23], s79 offen offset:128  lds\n"
      "ds_read_b64_tr_b16 a[84:85], v140 offset:36864\n"
      "ds_read_b64_tr_b16 a[108:109], v143 offset:36864\n"
      "v_mfma_f32_16x16x32_bf16 v[226:229], a[32:35], a[248:251], v[226:229]\n"
      "ds_read_b64_tr_b16 a[132:133], v142 offset:36864\n"
      "ds_read_b64_tr_b16 a[38:39], v143 offset:25088\n"
      "v_mfma_f32_16x16x32_bf16 v[144:147], a[80:83], a[252:255], v[144:147]\n"
      "ds_read_b64_tr_b16 a[110:111], v143 offset:37376\n"
      "ds_read_b64_tr_b16 a[86:87], v140 offset:37376\n"
      "ds_read_b64_tr_b16 a[62:63], v142 offset:25088\n"
      "ds_read_b64_tr_b16 a[60:61], v142 offset:24576\n"
      "s_add_u32 m0, s75, 32512\n"
      "v_mfma_f32_16x16x32_bf16 v[106:109], a[76:79], a[244:247], v[106:109]\n"
      "buffer_load_dwordx4 v187, s[20:23], s79 offen offset:256  lds\n"
      "v_mfma_f32_16x16x32_bf16 v[106:109], a[80:83], a[220:223], v[106:109]\n"
      "ds_read_b32 v53, v53\n"
      "v_lshl_add_u32 v44, v44, 2, s76\n"
      "v_mfma_f32_16x16x32_bf16 v[174:177], a[8:11], a[252:255], v[174:177]\n"
      "s_add_u32 m0, s75, 36480\n"
      "ds_read_b128 a[236:239], v1 offset:10240\n"
      "v_mfma_f32_16x16x32_bf16 v[216:219], a[104:107], a[216:219], v[216:219]\n"
      "ds_read_b32 v64, v64\n"
      "s_waitcnt lgkmcnt(6)\n"
      "v_mfma_f32_16x16x32_bf16 v[216:219], a[108:111], a[232:235], v[216:219]\n"
      "buffer_load_dwordx4 v187, s[20:23], s79 offen offset:384  lds\n"
      "v_mfma_f32_16x16x32_bf16 v[222:225], a[32:35], a[252:255], v[222:225]\n"
      "ds_read_b32 v61, v61\n"
      "s_mov_b32 s82, 0xffff\n"
      "v_mfma_f32_16x16x32_bf16 v[222:225], a[36:39], a[228:231], v[222:225]\n"
      "ds_read_b32 v40, v40\n"
      "s_mov_b32 s78, 0\n"
      "v_mfma_f32_16x16x32_bf16 v[188:191], a[56:59], a[220:223], v[188:191]\n"
      "s_add_u32 m0, s75, 40448\n"
      "v_cndmask_b32 v30, v221, v138, s[90:91]\n"
      "v_lshl_add_u32 v74, v74, 2, s76\n"
      "v_mfma_f32_16x16x32_bf16 v[240:243], a[108:111], a[224:227], v[240:243]\n"
      "buffer_load_dwordx4 v187, s[20:23], s79 offen offset:512  lds\n"
      "v_mfma_f32_16x16x32_bf16 v[130:133], a[56:59], a[248:251], v[130:133]\n"
      "s_waitcnt lgkmcnt(2)\n"
      "v_lshl_add_u32 v71, v71, 2, s76\n"
      "v_lshl_add_u32 v56, v56, 2, s76\n"
      "v_mfma_f32_16x16x32_bf16 v[118:121], a[60:63], a[228:231], v[118:121]\n"
      "ds_read_b32 v67, v67\n"
      "s_add_u32 m0, s75, 44416\n"
      "v_mfma_f32_16x16x32_bf16 v[192:195], a[132:135], a[228:231], v[192:195]\n"
      "ds_read_b32 v57, v57\n"
      "v_readlane_b32 s85, v37, s42\n"
      "v_mfma_f32_16x16x32_bf16 v[124:127], a[132:135], a[232:235], v[124:127]\n"
      "buffer_load_dwordx4 v187, s[20:23], s79 offen offset:640  lds\n"
      "v_mfma_f32_16x16x32_bf16 v[226:229], a[36:39], a[224:227], v[226:229]\n"
      "ds_read_b128 a[244:247], v0 offset:12288\n"
      "v_lshl_add_u32 v45, v45, 2, s76\n"
      "v_mfma_f32_16x16x32_bf16 v[236:239], a[132:135], a[224:227], v[236:239]\n"
      "ds_read_b32 v71, v71\n"
      "v_lshl_add_u32 v134, v134, 2, s76\n"
      "s_add_u32 m0, s75, 48384\n"
      "v_mfma_f32_16x16x32_bf16 v[244:247], a[32:35], a[220:223], v[244:247]\n"
      "ds_read_b128 a[252:255], v0 offset:16384\n"
      "buffer_load_dwordx4 v187, s[20:23], s78 offen offset:768  lds\n"
      "v_mfma_f32_16x16x32_bf16 v[244:247], a[36:39], a[236:239], v[244:247]\n"
      "ds_read_b128 a[240:243], v200 offset:8192\n"
      "v_lshl_add_u32 v55, v55, 2, s76\n"
      "v_mfma_f32_16x16x32_bf16 v[160:163], a[80:83], a[248:251], v[160:163]\n"
      "ds_read_b32 v48, v48\n"
      "s_add_u32 m0, s75, 52352\n"
      "v_mfma_f32_16x16x32_bf16 v[160:163], a[84:87], a[224:227], v[160:163]\n"
      "ds_read_b32 v55, v55\n"
      "v_lshl_add_u32 v54, v54, 2, s76\n"
      "v_mfma_f32_16x16x32_bf16 v[174:177], a[12:15], a[228:231], v[174:177]\n"
      "buffer_load_dwordx4 v187, s[20:23], s78 offen offset:896  lds\n"
      "v_mfma_f32_16x16x32_bf16 v[180:183], a[84:87], a[232:235], v[180:183]\n"
      "ds_read_b128 a[216:219], v116 offset:8192\n"
      "v_lshl_add_u32 v139, v139, 2, s76\n"
      "v_mfma_f32_16x16x32_bf16 v[96:99], a[60:63], a[232:235], v[96:99]\n"
      "s_or_b32 s95, s84, s85\n"
      "ds_read_b128 a[220:223], v1 offset:12288\n"
      "v_mfma_f32_16x16x32_bf16 v[188:191], a[60:63], a[236:239], v[188:191]\n"
      "ds_read_b128 a[248:251], v0 offset:14336\n"
      "s_add_u32 m0, s75, 56320\n"
      "v_mfma_f32_16x16x32_bf16 v[212:215], a[132:135], a[236:239], v[212:215]\n"
      "buffer_load_dwordx4 v187, s[20:23], s78 offen offset:1024  lds\n"
      "v_mfma_f32_16x16x32_bf16 v[252:255], a[36:39], a[232:235], v[252:255]\n"
      "s_waitcnt vmcnt(9)\n"
      "s_barrier\n"
      "ds_read_b64_tr_b16 a[64:65], v234\n"
      "v_mfma_f32_16x16x32_bf16 v[202:205], a[12:15], a[232:235], v[202:205]\n"
      "ds_read_b64_tr_b16 a[114:115], v233 offset:12800\n"
      "ds_read_b64_tr_b16 a[90:91], v232 offset:12800\n"
      "ds_read_b64_tr_b16 a[40:41], v233\n"
      "v_mfma_f32_16x16x32_bf16 v[196:199], a[108:111], a[228:231], v[196:199]\n"
      "ds_read_b64_tr_b16 a[88:89], v232 offset:12288\n"
      "ds_read_b64_tr_b16 a[66:67], v234 offset:512\n"
      "ds_read_b64_tr_b16 a[138:139], v234 offset:12800\n"
      "ds_read_b64_tr_b16 a[18:19], v232 offset:512\n"
      "ds_read_b64_tr_b16 a[16:17], v232\n"
      "v_mfma_f32_16x16x32_bf16 v[166:169], a[12:15], a[224:227], v[166:169]\n"
      "ds_read_b64_tr_b16 a[42:43], v233 offset:512\n"
      "ds_read_b64_tr_b16 a[136:137], v234 offset:12288\n"
      "ds_read_b64_tr_b16 a[112:113], v233 offset:12288\n"
      "ds_read_b128 a[232:235], v200 offset:2048\n"
      "s_add_u32 m0, s75, 60672\n"
      "v_mfma_f32_16x16x32_bf16 v[144:147], a[84:87], a[228:231], v[144:147]\n"
      "s_mov_b32 s78, 36864\n"
      "ds_read_b32 v134, v134\n"
      "v_mfma_f32_16x16x32_bf16 v[130:133], a[60:63], a[224:227], v[130:133]\n"
      "buffer_load_dwordx4 v187, s[20:23], s78 offen offset:768  lds\n"
      "s_mov_b32 s79, 73728\n"
      "v_mfma_f32_16x16x32_bf16 v[112:115], a[108:111], a[236:239], v[112:115]\n"
      "ds_read_b32 v139, v139\n"
      "ds_read_b32 v72, v72\n"
      "v_mfma_f32_16x16x32_bf16 v[106:109], a[84:87], a[236:239], v[106:109]\n"
      "ds_read_b128 a[228:231], v1 offset:16384\n"
      "s_add_u32 m0, s75, 64640\n"
      "s_waitcnt lgkmcnt(7)\n"
      "v_mfma_f32_16x16x32_bf16 v[252:255], a[40:43], a[248:251], v[252:255]\n"
      "buffer_load_dwordx4 v187, s[20:23], s78 offen offset:896  lds\n"
      "v_mfma_f32_16x16x32_bf16 v[166:169], a[16:19], a[240:243], v[166:169]\n"
      "ds_read_b32 v70, v70\n"
      "s_add_u32 m0, s75, 68608\n"
      "v_mfma_f32_16x16x32_bf16 v[244:247], a[40:43], a[252:255], v[244:247]\n"
      "ds_read_b32 v69, v69\n"
      "ds_read_b32 v58, v58\n"
      "v_mfma_f32_16x16x32_bf16 v[96:99], a[64:67], a[248:251], v[96:99]\n"
      "ds_read_b32 v43, v43\n"
      "buffer_load_dwordx4 v187, s[20:23], s78 offen offset:1024  lds\n"
      "v_mfma_f32_16x16x32_bf16 v[208:211], a[12:15], a[236:239], v[208:211]\n"
      "s_waitcnt lgkmcnt(4)\n"
      "v_lshl_add_u32 v75, v75, 2, s76\n"
      "v_cndmask_b32 v28, v221, v139, s[82:83]\n"
      "v_mfma_f32_16x16x32_bf16 v[208:211], a[16:19], a[252:255], v[208:211]\n"
      "ds_read_b32 v54, v54\n"
      "s_add_u32 m0, s75, 72960\n"
      "ds_read_b32 v44, v44\n"
      "v_mfma_f32_16x16x32_bf16 v[196:199], a[112:115], a[244:247], v[196:199]\n"
      "ds_read_b32 v74, v74\n"
      "v_lshl_add_u32 v62, v62, 2, s76\n"
      "v_mfma_f32_16x16x32_bf16 v[216:219], a[112:115], a[248:251], v[216:219]\n"
      "buffer_load_dwordx4 v187, s[20:23], s79 offen offset:768  lds\n"
      "v_mfma_f32_16x16x32_bf16 v[144:147], a[88:91], a[244:247], v[144:147]\n"
      "ds_read_b32 v42, v42\n"
      "s_add_u32 m0, s75, 76928\n"
      "v_mfma_f32_16x16x32_bf16 v[136:139], a[48:51], a[232:235], 0\n"
      "ds_read_b32 v63, v63\n"
      "s_or_b32 s95, s95, s48\n"
      "v_mfma_f32_16x16x32_bf16 v[240:243], a[112:115], a[240:243], v[240:243]\n"
      "ds_read_b32 v62, v62\n"
      "ds_read_b128 a[224:227], v1 offset:14336\n"
      "v_mfma_f32_16x16x32_bf16 v[174:177], a[16:19], a[244:247], v[174:177]\n"
      "buffer_load_dwordx4 v187, s[20:23], s79 offen offset:896  lds\n"
      "v_mfma_f32_16x16x32_bf16 v[124:127], a[136:139], a[248:251], v[124:127]\n"
      "ds_read_b32 v73, v73\n"
      "s_add_u32 m0, s75, 80896\n"
      "v_mfma_f32_16x16x32_bf16 v[222:225], a[40:43], a[244:247], v[222:225]\n"
      "ds_read_b32 v100, v100\n"
      "v_lshl_add_u32 v60, v60, 2, s76\n"
      "v_mfma_f32_16x16x32_bf16 v[192:195], a[136:139], a[244:247], v[192:195]\n"
      "ds_read_b128 a[236:239], v116 offset:2048\n"
      "ds_read_b32 v46, v46\n"
      "v_mfma_f32_16x16x32_bf16 v[106:109], a[88:91], a[252:255], v[106:109]\n"
      "buffer_load_dwordx4 v187, s[20:23], s79 offen offset:1024  lds\n"
      "v_mfma_f32_16x16x32_bf16 v[236:239], a[136:139], a[240:243], v[236:239]\n"
      "s_add_u32 m0, s75, 85248\n"
      "ds_read_b32 v45, v45\n"
      "v_mfma_f32_16x16x32_bf16 v[118:121], a[64:67], a[244:247], v[118:121]\n"
      "ds_read_b32 v41, v41\n"
      "s_mov_b32 s79, 110592\n"
      "v_mfma_f32_16x16x32_bf16 v[160:163], a[88:91], a[240:243], v[160:163]\n"
      "s_waitcnt vmcnt(9)\n"
      "s_barrier\n"
      "ds_read_b64_tr_b16 a[70:71], v234 offset:25088\n"
      "v_mfma_f32_16x16x32_bf16 v[212:215], a[136:139], a[252:255], v[212:215]\n"
      "ds_read_b64_tr_b16 a[20:21], v232 offset:24576\n"
      "ds_read_b64_tr_b16 a[94:95], v232 offset:37376\n"
      "v_mfma_f32_16x16x32_bf16 v[112:115], a[112:115], a[252:255], v[112:115]\n"
      "ds_read_b64_tr_b16 a[92:93], v232 offset:36864\n"
      "ds_read_b64_tr_b16 a[22:23], v232 offset:25088\n"
      "v_mfma_f32_16x16x32_bf16 v[188:191], a[64:67], a[252:255], v[188:191]\n"
      "ds_read_b64_tr_b16 a[46:47], v233 offset:25088\n"
      "ds_read_b64_tr_b16 a[44:45], v233 offset:24576\n"
      "v_mfma_f32_16x16x32_bf16 v[226:229], a[40:43], a[240:243], v[226:229]\n"
      "buffer_load_dwordx4 v187, s[20:23], s79 offen offset:768  lds\n"
      "ds_read_b64_tr_b16 a[116:117], v233 offset:36864\n"
      "v_mfma_f32_16x16x32_bf16 v[202:205], a[16:19], a[248:251], v[202:205]\n"
      "ds_read_b64_tr_b16 a[142:143], v234 offset:37376\n"
      "ds_read_b64_tr_b16 a[68:69], v234 offset:24576\n"
      "v_mfma_f32_16x16x32_bf16 v[180:183], a[88:91], a[248:251], v[180:183]\n"
      "ds_read_b64_tr_b16 a[118:119], v233 offset:37376\n"
      "s_waitcnt lgkmcnt(7)\n"
      "v_mfma_f32_16x16x32_bf16 v[106:109], a[92:95], a[228:231], v[106:109]\n"
      "ds_read_b32 v60, v60\n"
      "s_add_u32 m0, s75, 89216\n"
      "v_mfma_f32_16x16x32_bf16 v[144:147], a[92:95], a[220:223], v[144:147]\n"
      "ds_read_b64_tr_b16 a[140:141], v234 offset:36864\n"
      "s_waitcnt lgkmcnt(6)\n"
      "v_mfma_f32_16x16x32_bf16 v[208:211], a[20:23], a[228:231], v[208:211]\n"
      "buffer_load_dwordx4 v187, s[20:23], s79 offen offset:896  lds\n"
      "v_mfma_f32_16x16x32_bf16 v[174:177], a[20:23], a[220:223], v[174:177]\n"
      "ds_read_b32 v59, v59\n"
      "v_fma_f32 v220, v106, v106, s51\n"
      "v_mfma_f32_16x16x32_bf16 v[166:169], a[20:23], a[216:219], v[166:169]\n"
      "ds_read_b128 a[252:255], v116 offset:10240\n"
      "s_add_u32 m0, s75, 93184\n"
      "v_mfma_f32_16x16x32_bf16 v[202:205], a[20:23], a[224:227], v[202:205]\n"
      "ds_read_b128 a[244:247], v116 offset:6144\n"
      "v_fma_f32 v230, v144, v144, s51\n"
      "v_mfma_f32_16x16x32_bf16 v[222:225], a[44:47], a[220:223], v[222:225]\n"
      "buffer_load_dwordx4 v187, s[20:23], s79 offen offset:1024  lds\n"
      "v_mfma_f32_16x16x32_bf16 v[160:163], a[92:95], a[216:219], v[160:163]\n"
      "s_add_u32 m0, s75, -768\n"
      "ds_read_b32 v50, v50\n"
      "v_mfma_f32_16x16x32_bf16 v[180:183], a[92:95], a[224:227], v[180:183]\n"
      "s_waitcnt lgkmcnt(4)\n"
      "v_mul_f32 v157, v167, v167\n"
      "v_fma_f32 v122, v208, v208, s51\n"
      "v_mfma_f32_16x16x32_bf16 v[130:133], a[64:67], a[240:243], v[130:133]\n"
      "s_mov_b32 s78, 147456\n"
      "ds_read_b32 v56, v56\n"
      "v_mfma_f32_16x16x32_bf16 v[240:243], a[116:119], a[216:219], v[240:243]\n"
      "buffer_load_dwordx4 v187, s[20:23], s78 offen offset:768  lds\n"
      "v_mul_f32_e64 v149, v161, v161\n"
      "v_mul_f32_e64 v231, v145, v145\n"
      "v_pk_fma_f32 v[230:231], v[146:147], v[146:147], v[230:231]\n"
      "v_mfma_f32_16x16x32_bf16 v[102:105], a[24:27], a[232:235], 0\n"
      "ds_read_b128 a[240:243], v200 offset:6144\n"
      "v_fma_f32 v148, v160, v160, s51\n"
      "v_mfma_f32_16x16x32_bf16 v[216:219], a[116:119], a[224:227], v[216:219]\n"
      "s_add_u32 m0, s75, 3200\n"
      "v_mul_f32_e64 v101, v181, v181\n"
      "v_fma_f32 v150, v174, v174, s51\n"
      "v_mfma_f32_16x16x32_bf16 v[192:195], a[140:143], a[220:223], v[192:195]\n"
      "buffer_load_dwordx4 v187, s[20:23], s78 offen offset:896  lds\n"
      "v_mfma_f32_16x16x32_bf16 v[196:199], a[116:119], a[220:223], v[196:199]\n"
      "ds_read_b128 a[248:251], v200 offset:10240\n"
      "v_mul_f32_e64 v151, v175, v175\n"
      "v_mfma_f32_16x16x32_bf16 v[226:229], a[44:47], a[216:219], v[226:229]\n"
      "ds_read_b32 v75, v75\n"
      "v_cndmask_b32 v26, v221, v134, s[82:83]\n"
      "v_mfma_f32_16x16x32_bf16 v[118:121], a[68:71], a[220:223], v[118:121]\n"
      "s_add_u32 m0, s75, 7168\n"
      "v_mul_f32_e64 v123, v209, v209\n"
      "v_fma_f32 v134, v202, v202, s51\n"
      "v_mfma_f32_16x16x32_bf16 v[212:215], a[140:143], a[228:231], v[212:215]\n"
      "buffer_load_dwordx4 v187, s[20:23], s78 offen offset:1024  lds\n"
      "v_fma_f32 v156, v166, v166, s51\n"
      "v_mul_f32_e64 v135, v203, v203\n"
      "v_pk_fma_f32 v[148:149], v[162:163], v[162:163], v[148:149]\n"
      "v_pk_fma_f32 v[150:151], v[176:177], v[176:177], v[150:151]\n"
      "v_cndmask_b32 v24, v221, v100, s[82:83]\n"
      "v_pk_fma_f32 v[150:151], v[222:223], v[222:223], v[150:151]\n"
      "v_mfma_f32_16x16x32_bf16 v[102:105], a[28:31], a[236:239], v[102:105]\n"
      "s_add_u32 m0, s75, 11520\n"
      "v_mul_f32 v221, v107, v107\n"
      "s_waitcnt lgkmcnt(1)\n"
      "v_mfma_f32_16x16x32_bf16 v[102:105], a[32:35], a[240:243], v[102:105]\n"
      "s_mov_b32 s78, 184320\n"
      "buffer_load_dwordx4 v187, s[20:23], s78 offen offset:768  lds\n"
      "v_fma_f32 v100, v180, v180, s51\n"
      "v_pk_fma_f32 v[220:221], v[108:109], v[108:109], v[220:221]\n"
      "v_pk_fma_f32 v[148:149], v[240:241], v[240:241], v[148:149]\n"
      "v_pk_fma_f32 v[150:151], v[224:225], v[224:225], v[150:151]\n"
      "v_pk_fma_f32 v[134:135], v[204:205], v[204:205], v[134:135]\n"
      "v_pk_fma_f32 v[100:101], v[182:183], v[182:183], v[100:101]\n"
      "v_pk_fma_f32 v[150:151], v[118:119], v[118:119], v[150:151]\n"
      "v_pk_fma_f32 v[230:231], v[196:197], v[196:197], v[230:231]\n"
      "v_pk_fma_f32 v[156:157], v[168:169], v[168:169], v[156:157]\n"
      "v_pk_fma_f32 v[122:123], v[210:211], v[210:211], v[122:123]\n"
      "v_pk_fma_f32 v[148:149], v[242:243], v[242:243], v[148:149]\n"
      "s_add_u32 m0, s75, 15488\n"
      "v_pk_fma_f32 v[100:101], v[216:217], v[216:217], v[100:101]\n"
      "v_mfma_f32_16x16x32_bf16 v[102:105], a[36:39], a[244:247], v[102:105]\n"
      "buffer_load_dwordx4 v187, s[20:23], s78 offen offset:896  lds\n"
      "v_mfma_f32_16x16x32_bf16 v[136:139], a[52:55], a[236:239], v[136:139]\n"
      "s_waitcnt vmcnt(11)\n"
      "s_barrier\n"
      "ds_read_b64_tr_b16 a[194:195], v142 offset:512\n"
      "v_mfma_f32_16x16x32_bf16 v[124:127], a[140:143], a[224:227], v[124:127]\n"
      "ds_read_b64_tr_b16 a[192:193], v142\n"
      "ds_read_b64_tr_b16 a[150:151], v140 offset:12800\n"
      "v_mfma_f32_16x16x32_bf16 v[236:239], a[140:143], a[216:219], v[236:239]\n"
      "s_add_u32 m0, s75, 19456\n"
      "ds_read_b64_tr_b16 a[144:145], v140\n"
      "v_mfma_f32_16x16x32_bf16 v[252:255], a[44:47], a[224:227], v[252:255]\n"
      "ds_read_b64_tr_b16 a[196:197], v142 offset:12288\n"
      "ds_read_b64_tr_b16 a[174:175], v143 offset:12800\n"
      "v_mfma_f32_16x16x32_bf16 v[244:247], a[44:47], a[228:231], v[244:247]\n"
      "buffer_load_dwordx4 v187, s[20:23], s78 offen offset:1024  lds\n"
      "v_mfma_f32_16x16x32_bf16 v[130:133], a[68:71], a[216:219], v[130:133]\n"
      "ds_read_b64_tr_b16 a[198:199], v142 offset:12800\n"
      "ds_read_b64_tr_b16 a[146:147], v140 offset:512\n"
      "ds_read_b64_tr_b16 a[168:169], v143\n"
      "v_pk_fma_f32 v[156:157], v[226:227], v[226:227], v[156:157]\n"
      "v_pk_fma_f32 v[100:101], v[218:219], v[218:219], v[100:101]\n"
      "v_pk_fma_f32 v[134:135], v[252:253], v[252:253], v[134:135]\n"
      "v_pk_fma_f32 v[230:231], v[198:199], v[198:199], v[230:231]\n"
      "s_mov_b32 s79, 0\n"
      "v_mfma_f32_16x16x32_bf16 v[112:115], a[116:119], a[228:231], v[112:115]\n"
      "s_add_u32 m0, s75, 24576\n"
      "ds_read_b64_tr_b16 a[148:149], v140 offset:12288\n"
      "v_mfma_f32_16x16x32_bf16 v[96:99], a[68:71], a[224:227], v[96:99]\n"
      "ds_read_b64_tr_b16 a[172:173], v143 offset:12288\n"
      "ds_read_b64_tr_b16 a[170:171], v143 offset:512\n"
      "buffer_load_dwordx4 v251, s[36:39], s79 offen  lds\n"
      "v_pk_fma_f32 v[148:149], v[236:237], v[236:237], v[148:149]\n"
      "v_pk_fma_f32 v[230:231], v[192:193], v[192:193], v[230:231]\n"
      "v_pk_fma_f32 v[148:149], v[238:239], v[238:239], v[148:149]\n"
      "v_pk_fma_f32 v[156:157], v[228:229], v[228:229], v[156:157]\n"
      "s_add_u32 m0, s75, 28544\n"
      "v_pk_fma_f32 v[134:135], v[254:255], v[254:255], v[134:135]\n"
      "v_pk_fma_f32 v[122:123], v[244:245], v[244:245], v[122:123]\n"
      "v_pk_fma_f32 v[156:157], v[130:131], v[130:131], v[156:157]\n"
      "v_pk_fma_f32 v[100:101], v[124:125], v[124:125], v[100:101]\n"
      "v_pk_fma_f32 v[230:231], v[194:195], v[194:195], v[230:231]\n"
      "v_pk_fma_f32 v[150:151], v[120:121], v[120:121], v[150:151]\n"
      "v_pk_fma_f32 v[134:135], v[96:97], v[96:97], v[134:135]\n"
      "v_add_f32 v148, v148, v149\n"
      "v_pk_fma_f32 v[156:157], v[132:133], v[132:133], v[156:157]\n"
      "v_pk_fma_f32 v[134:135], v[98:99], v[98:99], v[134:135]\n"
      "v_add_f32_e64 v150, v150, v151\n"
      "v_mfma_f32_16x16x32_bf16 v[136:139], a[56:59], a[240:243], v[136:139]\n"
      "buffer_load_dwordx4 v251, s[36:39], s79 offen offset:128  lds\n"
      "s_add_u32 m0, s75, 32512\n"
      "v_add_f32 v230, v230, v231\n"
      "v_add_f32 v156, v156, v157\n"
      "v_pk_fma_f32 v[100:101], v[126:127], v[126:127], v[100:101]\n"
      "v_permlane16_swap_b32 v150, v230\n"
      "v_permlane16_swap_b32 v156, v148\n"
      "v_pk_fma_f32 v[220:221], v[112:113], v[112:113], v[220:221]\n"
      "v_mfma_f32_16x16x32_bf16 v[184:187], a[72:75], a[232:235], 0\n"
      "s_waitcnt lgkmcnt(0)\n"
      "v_add_f32 v150, v150, v230\n"
      "v_add_f32 v156, v156, v148\n"
      "v_mov_b32 v230, v150\n"
      "v_mfma_f32_16x16x32_bf16 v[102:105], a[40:43], a[248:251], v[102:105]\n"
      "buffer_load_dwordx4 v251, s[36:39], s79 offen offset:256  lds\n"
      "v_mov_b32 v148, v156\n"
      "v_add_f32 v100, v100, v101\n"
      "v_permlane32_swap_b32 v150, v230\n"
      "v_permlane32_swap_b32 v156, v148\n"
      "v_add_f32_e64 v156, v156, v148\n"
      "v_pk_fma_f32 v[122:123], v[246:247], v[246:247], v[122:123]\n"
      "v_mfma_f32_16x16x32_bf16 v[188:191], a[68:71], a[228:231], v[188:191]\n"
      "s_mov_b32 s79, 12288\n"
      "v_add_f32 v150, v150, v230\n"
      "v_rsq_f32 v156, v156\n"
      "v_rsq_f32_e64 v150, v150\n"
      "v_mfma_f32_16x16x32_bf16 v[170:173], a[0:3], a[232:235], 0\n"
      "s_add_u32 m0, s75, 36864\n"
      "v_mov_b32 v148, v156\n"
      "v_mov_b32 v230, v150\n"
      "v_mfma_f32_16x16x32_bf16 v[170:173], a[4:7], a[236:239], v[170:173]\n"
      "buffer_load_dwordx4 v251, s[36:39], s79 offen  lds\n"
      "v_permlane16_swap_b32 v150, v230\n"
      "v_permlane16_swap_b32 v156, v148\n"
      "v_add_f32_e64 v134, v134, v135\n"
      "v_pk_mul_f32 v[236:237], v[236:237], v[148:149] op_sel_hi:[1,0]\n"
      "v_pk_mul_f32 v[160:161], v[160:161], v[148:149] op_sel_hi:[1,0]\n"
      "v_pk_mul_f32 v[198:199], v[198:199], v[230:231] op_sel_hi:[1,0]\n"
      "v_mfma_f32_16x16x32_bf16 v[170:173], a[8:11], a[240:243], v[170:173]\n"
      "s_add_u32 m0, s75, 40832\n"
      "v_mul_f32 v156, s50, v156\n"
      "v_mul_f32 v150, s50, v150\n"
      "v_mfma_f32_16x16x32_bf16 v[136:139], a[60:63], a[244:247], v[136:139]\n"
      "buffer_load_dwordx4 v251, s[36:39], s79 offen offset:128  lds\n"
      "v_permlane16_swap_b32_e64 v134, v100\n"
      "v_pk_fma_f32 v[122:123], v[188:189], v[188:189], v[122:123]\n"
      "v_pk_mul_f32 v[132:133], v[132:133], v[156:157] op_sel_hi:[1,0]\n"
      "v_pk_mul_f32 v[168:169], v[168:169], v[156:157] op_sel_hi:[1,0]\n"
      "v_pk_mul_f32 v[228:229], v[228:229], v[156:157] op_sel_hi:[1,0]\n"
      "v_pk_mul_f32 v[162:163], v[162:163], v[148:149] op_sel_hi:[1,0]\n"
      "s_add_u32 m0, s75, 44800\n"
      "v_pk_mul_f32 v[144:145], v[144:145], v[230:231] op_sel_hi:[1,0]\n"
      "v_add_f32_e64 v134, v134, v100\n"
      "v_pk_fma_f32 v[122:123], v[190:191], v[190:191], v[122:123]\n"
      "v_pk_mul_f32 v[166:167], v[166:167], v[156:157] op_sel_hi:[1,0]\n"
      "v_mfma_f32_16x16x32_bf16 v[170:173], a[12:15], a[244:247], v[170:173]\n"
      "buffer_load_dwordx4 v251, s[36:39], s79 offen offset:256  lds\n"
      "v_cvt_pk_bf16_f32 v236, v236, v237\n"
      "v_mov_b32 v100, v134\n"
      "v_pk_fma_f32 v[220:221], v[114:115], v[114:115], v[220:221]\n"
      "v_pk_mul_f32 v[120:121], v[120:121], v[150:151] op_sel_hi:[1,0]\n"
      "v_pk_mul_f32 v[238:239], v[238:239], v[148:149] op_sel_hi:[1,0]\n"
      "v_permlane32_swap_b32 v134, v100\n"
      "v_mfma_f32_16x16x32_bf16 v[170:173], a[16:19], a[248:251], v[170:173]\n"
      "s_add_u32 m0, s75, 49152\n"
      "v_add_f32 v134, v134, v100\n"
      "v_rsq_f32 v134, v134\n"
      "v_cvt_pk_bf16_f32 v237, v238, v239\n"
      "v_cvt_pk_bf16_f32 v166, v166, v167\n"
      "v_mov_b32 v100, v134\n"
      "v_pk_mul_f32 v[130:131], v[130:131], v[156:157] op_sel_hi:[1,0]\n"
      "v_pk_fma_f32 v[220:221], v[212:213], v[212:213], v[220:221]\n"
      "v_permlane16_swap_b32 v134, v100\n"
      "v_mfma_f32_16x16x32_bf16 v[170:173], a[20:23], a[252:255], v[170:173]\n"
      "s_waitcnt vmcnt(12)\n"
      "s_barrier\n"
      "ds_read_b64_tr_b16 a[176:177], v143 offset:24576\n"
      "ds_read_b64_tr_b16 a[154:155], v140 offset:25088\n"
      "s_mov_b32 s78, 24576\n"
      "v_pk_mul_f32 v[242:243], v[242:243], v[148:149] op_sel_hi:[1,0]\n"
      "v_mfma_f32_16x16x32_bf16 v[102:105], a[44:47], a[252:255], v[102:105]\n"
      "ds_read_b64_tr_b16 a[152:153], v140 offset:24576\n"
      "buffer_load_dwordx4 v251, s[36:39], s78 offen  lds\n"
      "v_add_f32_e64 v122, v122, v123\n"
      "v_cvt_pk_bf16_f32 v167, v168, v169\n"
      "v_cvt_pk_bf16_f32 v160, v160, v161\n"
      "v_pk_mul_f32 v[226:227], v[226:227], v[156:157] op_sel_hi:[1,0]\n"
      "v_pk_mul_f32 v[196:197], v[196:197], v[230:231] op_sel_hi:[1,0]\n"
      "v_pk_mul_f32 v[176:177], v[176:177], v[150:151] op_sel_hi:[1,0]\n"
      "v_pk_mul_f32 v[146:147], v[146:147], v[230:231] op_sel_hi:[1,0]\n"
      "v_pk_mul_f32 v[118:119], v[118:119], v[150:151] op_sel_hi:[1,0]\n"
      "v_pk_fma_f32 v[220:221], v[214:215], v[214:215], v[220:221]\n"
      "v_pk_mul_f32 v[174:175], v[174:175], v[150:151] op_sel_hi:[1,0]\n"
      "v_pk_mul_f32 v[224:225], v[224:225], v[150:151] op_sel_hi:[1,0]\n"
      "v_mfma_f32_16x16x32_bf16 v[154:157], a[120:123], a[232:235], 0\n"
      "ds_read_b64_tr_b16 a[180:181], v143 offset:36864\n"
      "v_cvt_pk_bf16_f32 v161, v162, v163\n"
      "v_cvt_pk_bf16_f32 v169, v228, v229\n"
      "v_mfma_f32_16x16x32_bf16 v[154:157], a[124:127], a[236:239], v[154:157]\n"
      "ds_read_b64_tr_b16 a[202:203], v142 offset:25088\n"
      "s_add_u32 m0, s75, 53120\n"
      "v_mfma_f32_16x16x32_bf16 v[154:157], a[128:131], a[240:243], v[154:157]\n"
      "ds_read_b64_tr_b16 a[204:205], v142 offset:36864\n"
      "v_add_f32_e64 v220, v220, v221\n"
      "v_mfma_f32_16x16x32_bf16 v[136:139], a[64:67], a[248:251], v[136:139]\n"
      "buffer_load_dwordx4 v251, s[36:39], s78 offen offset:128  lds\n"
      "ds_read_b64_tr_b16 a[200:201], v142 offset:24576\n"
      "v_mfma_f32_16x16x32_bf16 v[136:139], a[68:71], a[252:255], v[136:139]\n"
      "ds_read_b64_tr_b16 a[178:179], v143 offset:25088\n"
      "v_cvt_pk_bf16_f32 v163, v242, v243\n"
      "v_mul_f32_e64 v141, v171, v171\n"
      "v_mfma_f32_16x16x32_bf16 v[184:187], a[76:79], a[236:239], v[184:187]\n"
      "ds_read_b64_tr_b16 a[158:159], v140 offset:37376\n"
      "v_cvt_pk_bf16_f32 v174, v174, v175\n"
      "v_cvt_pk_bf16_f32 v144, v144, v145\n"
      "v_cvt_pk_bf16_f32 v175, v176, v177\n"
      "s_add_u32 m0, s75, 57088\n"
      "v_pk_mul_f32 v[182:183], v[182:183], v[100:101] op_sel_hi:[1,0]\n"
      "v_mfma_f32_16x16x32_bf16 v[184:187], a[80:83], a[240:243], v[184:187]\n"
      "ds_read_b64_tr_b16 a[156:157], v140 offset:36864\n"
      "v_cvt_pk_bf16_f32 v168, v226, v227\n"
      "v_mfma_f32_16x16x32_bf16 v[184:187], a[84:87], a[244:247], v[184:187]\n"
      "ds_read_b64_tr_b16 a[206:207], v142 offset:37376\n"
      "ds_read_b64_tr_b16 a[182:183], v143 offset:37376\n"
      "v_permlane16_swap_b32_e64 v122, v220\n"
      "v_pk_mul_f32 v[192:193], v[192:193], v[230:231] op_sel_hi:[1,0]\n"
      "v_fma_f32 v140, v170, v170, s51\n"
      "v_pk_mul_f32 v[240:241], v[240:241], v[148:149] op_sel_hi:[1,0]\n"
      "v_cvt_pk_bf16_f32 v118, v118, v119\n"
      "v_cvt_pk_bf16_f32 v130, v130, v131\n"
      "v_pk_mul_f32 v[218:219], v[218:219], v[100:101] op_sel_hi:[1,0]\n"
      "v_pk_fma_f32 v[140:141], v[172:173], v[172:173], v[140:141]\n"
      "v_pk_mul_f32 v[222:223], v[222:223], v[150:151] op_sel_hi:[1,0]\n"
      "v_add_f32_e64 v122, v122, v220\n"
      "v_pk_mul_f32 v[180:181], v[180:181], v[100:101] op_sel_hi:[1,0]\n"
      "v_mov_b32_e64 v220, v122\n"
      "v_mfma_f32_16x16x32_bf16 v[184:187], a[88:91], a[248:251], v[184:187]\n"
      "ds_read_b128 v[226:229], v116\n"
      "v_cvt_pk_bf16_f32 v177, v224, v225\n"
      "v_cvt_pk_bf16_f32 v192, v192, v193\n"
      "v_mfma_f32_16x16x32_bf16 v[184:187], a[92:95], a[252:255], v[184:187]\n"
      "ds_write_b64 v249, v[236:237] offset:24\n"
      "v_mfma_f32_16x16x32_bf16 v[148:151], a[96:99], a[232:235], 0\n"
      "ds_read_b128 v[236:239], v200\n"
      "v_cvt_pk_bf16_f32 v131, v132, v133\n"
      "v_cvt_pk_bf16_f32 v162, v240, v241\n"
      "v_cvt_pk_bf16_f32 v119, v120, v121\n"
      "v_permlane32_swap_b32_e64 v122, v220\n"
      "v_cvt_pk_bf16_f32 v180, v180, v181\n"
      "v_mfma_f32_16x16x32_bf16 v[148:151], a[100:103], a[236:239], v[148:151]\n"
      "buffer_load_dwordx4 v251, s[36:39], s78 offen offset:256  lds\n"
      "v_cvt_pk_bf16_f32 v145, v146, v147\n"
      "v_add_f32_e64 v122, v122, v220\n"
      "v_cvt_pk_bf16_f32 v176, v222, v223\n"
      "v_mfma_f32_16x16x32_bf16 v[148:151], a[104:107], a[240:243], v[148:151]\n"
      "s_add_u32 m0, s75, 61440\n"
      "ds_read_b128 v[240:243], v200 offset:2048\n"
      "v_mfma_f32_16x16x32_bf16 v[154:157], a[132:135], a[244:247], v[154:157]\n"
      "s_mov_b32 s78, 36864\n"
      "v_rsq_f32_e64 v122, v122\n"
      "v_mfma_f32_16x16x32_bf16 v[154:157], a[136:139], a[248:251], v[154:157]\n"
      "buffer_load_dwordx4 v251, s[36:39], s78 offen  lds\n"
      "v_fma_f32 v142, v184, v184, s51\n"
      "v_cvt_pk_bf16_f32 v147, v198, v199\n"
      "v_mov_b32 v220, v122\n"
      "v_mul_f32 v134, s50, v134\n"
      "v_pk_mul_f32 v[194:195], v[194:195], v[230:231] op_sel_hi:[1,0]\n"
      "v_pk_fma_f32 v[140:141], v[102:103], v[102:103], v[140:141]\n"
      "v_mfma_f32_16x16x32_bf16 v[154:157], a[140:143], a[252:255], v[154:157]\n"
      "s_waitcnt lgkmcnt(1)\n"
      "v_cvt_pk_bf16_f32 v193, v194, v195\n"
      "v_permlane16_swap_b32 v122, v220\n"
      "v_mul_f32_e64 v143, v185, v185\n"
      "v_mfma_f32_16x16x32_bf16 v[222:225], a[168:171], v[236:239], 0\n"
      "ds_write_b64 v249, v[130:131] offset:16\n"
      "s_add_u32 m0, s75, 65408\n"
      "v_mfma_f32_16x16x32_bf16 v[222:225], a[172:175], v[226:229], v[222:225]\n"
      "buffer_load_dwordx4 v251, s[36:39], s78 offen offset:128  lds\n"
      "s_add_u32 m0, s75, 69376\n"
      "v_mfma_f32_16x16x32_bf16 v[130:133], a[144:147], v[236:239], 0\n"
      "ds_write_b128 v249, v[160:163] offset:32\n"
      "v_mfma_f32_16x16x32_bf16 v[130:133], a[148:151], v[226:229], v[130:133]\n"
      "ds_read_b128 v[160:163], v200 offset:4096\n"
      "v_cvt_pk_bf16_f32 v146, v196, v197\n"
      "v_mfma_f32_16x16x32_bf16 v[148:151], a[108:111], a[244:247], v[148:151]\n"
      "buffer_load_dwordx4 v251, s[36:39], s78 offen offset:256  lds\n"
      "v_mfma_f32_16x16x32_bf16 v[148:151], a[112:115], a[248:251], v[148:151]\n"
      "ds_read_b128 v[194:197], v116 offset:2048\n"
      "s_add_u32 m0, s75, 73728\n"
      "v_mfma_f32_16x16x32_bf16 v[148:151], a[116:119], a[252:255], v[148:151]\n"
      "ds_write_b128 v249, v[166:169]\n"
      "v_pk_fma_f32 v[142:143], v[186:187], v[186:187], v[142:143]\n"
      "v_pk_fma_f32 v[140:141], v[104:105], v[104:105], v[140:141]\n"
      "v_pk_fma_f32 v[140:141], v[136:137], v[136:137], v[140:141]\n"
      "s_waitcnt vmcnt(12)\n"
      "s_barrier\n"
      "v_mfma_f32_16x16x32_bf16 v[166:169], a[192:195], v[236:239], 0\n"
      "s_mov_b32 s79, 49152\n"
      "ds_read_b64_tr_b16 a[214:215], v234 offset:12800\n"
      "v_mfma_f32_16x16x32_bf16 v[166:169], a[196:199], v[226:229], v[166:169]\n"
      "buffer_load_dwordx4 v251, s[36:39], s79 offen  lds\n"
      "s_waitcnt lgkmcnt(2)\n"
      "v_mfma_f32_16x16x32_bf16 v[166:169], a[200:203], v[160:163], v[166:169]\n"
      "ds_read_b64_tr_b16 a[164:165], v232 offset:12288\n"
      "ds_read_b64_tr_b16 a[208:209], v234\n"
      "v_mul_f32 v122, s50, v122\n"
      "v_mfma_f32_16x16x32_bf16 v[130:133], a[152:155], v[160:163], v[130:133]\n"
      "ds_read_b64_tr_b16 a[188:189], v233 offset:12288\n"
      "ds_read_b64_tr_b16 a[162:163], v232 offset:512\n"
      "s_add_u32 m0, s75, 77696\n"
      "v_mfma_f32_16x16x32_bf16 v[226:229], a[168:171], v[240:243], 0\n"
      "buffer_load_dwordx4 v251, s[36:39], s79 offen offset:128  lds\n"
      "v_mfma_f32_16x16x32_bf16 v[222:225], a[176:179], v[160:163], v[222:225]\n"
      "s_add_u32 m0, s75, 81664\n"
      "ds_read_b64_tr_b16 a[160:161], v232\n"
      "ds_read_b64_tr_b16 a[166:167], v232 offset:12800\n"
      "ds_read_b64_tr_b16 a[190:191], v233 offset:12800\n"
      "v_mfma_f32_16x16x32_bf16 v[160:163], a[192:195], v[240:243], 0\n"
      "ds_read_b64_tr_b16 a[186:187], v233 offset:512\n"
      "ds_read_b64_tr_b16 a[184:185], v233\n"
      "ds_read_b64_tr_b16 a[212:213], v234 offset:12288\n"
      "ds_read_b64_tr_b16 a[210:211], v234 offset:512\n"
      "v_pk_mul_f32 v[244:245], v[244:245], v[122:123] op_sel_hi:[1,0]\n"
      "v_pk_mul_f32 v[212:213], v[212:213], v[220:221] op_sel_hi:[1,0]\n"
      "v_pk_mul_f32 v[96:97], v[96:97], v[134:135] op_sel_hi:[1,0]\n"
      "v_pk_mul_f32 v[202:203], v[202:203], v[134:135] op_sel_hi:[1,0]\n"
      "v_pk_fma_f32 v[140:141], v[138:139], v[138:139], v[140:141]\n"
      "v_pk_mul_f32 v[246:247], v[246:247], v[122:123] op_sel_hi:[1,0]\n"
      "v_pk_mul_f32 v[108:109], v[108:109], v[220:221] op_sel_hi:[1,0]\n"
      "v_pk_mul_f32 v[106:107], v[106:107], v[220:221] op_sel_hi:[1,0]\n"
      "v_cvt_pk_bf16_f32 v181, v182, v183\n"
      "v_pk_mul_f32 v[216:217], v[216:217], v[100:101] op_sel_hi:[1,0]\n"
      "v_pk_mul_f32 v[190:191], v[190:191], v[122:123] op_sel_hi:[1,0]\n"
      "v_cvt_pk_bf16_f32 v183, v218, v219\n"
      "v_pk_mul_f32 v[114:115], v[114:115], v[220:221] op_sel_hi:[1,0]\n"
      "v_cvt_pk_bf16_f32 v212, v212, v213\n"
      "v_cvt_pk_bf16_f32 v202, v202, v203\n"
      "v_pk_fma_f32 v[142:143], v[148:149], v[148:149], v[142:143]\n"
      "v_pk_mul_f32 v[98:99], v[98:99], v[134:135] op_sel_hi:[1,0]\n"
      "v_pk_mul_f32 v[124:125], v[124:125], v[100:101] op_sel_hi:[1,0]\n"
      "v_pk_fma_f32 v[142:143], v[150:151], v[150:151], v[142:143]\n"
      "v_mfma_f32_16x16x32_bf16 v[226:229], a[172:175], v[194:197], v[226:229]\n"
      "ds_read_b128 v[236:239], v116 offset:4096\n"
      "buffer_load_dwordx4 v251, s[36:39], s79 offen offset:256  lds\n"
      "ds_read_b128 v[230:233], v200 offset:6144\n"
      "s_mov_b32 s79, 61440\n"
      "v_pk_fma_f32 v[142:143], v[154:155], v[154:155], v[142:143]\n"
      "v_pk_mul_f32 v[204:205], v[204:205], v[134:135] op_sel_hi:[1,0]\n"
      "v_add_f32_e64 v140, v140, v141\n"
      "v_cvt_pk_bf16_f32 v203, v204, v205\n"
      "v_cvt_pk_bf16_f32 v124, v124, v125\n"
      "v_pk_mul_f32 v[188:189], v[188:189], v[122:123] op_sel_hi:[1,0]\n"
      "v_pk_mul_f32 v[126:127], v[126:127], v[100:101] op_sel_hi:[1,0]\n"
      "v_pk_mul_f32 v[214:215], v[214:215], v[220:221] op_sel_hi:[1,0]\n"
      "v_pk_mul_f32 v[112:113], v[112:113], v[220:221] op_sel_hi:[1,0]\n"
      "v_pk_mul_f32 v[208:209], v[208:209], v[122:123] op_sel_hi:[1,0]\n"
      "v_pk_mul_f32 v[254:255], v[254:255], v[134:135] op_sel_hi:[1,0]\n"
      "v_cvt_pk_bf16_f32 v182, v216, v217\n"
      "v_pk_fma_f32 v[142:143], v[156:157], v[156:157], v[142:143]\n"
      "v_mfma_f32_16x16x32_bf16 v[218:221], a[144:147], v[240:243], 0\n"
      "s_add_u32 m0, s75, 86016\n"
      "ds_read_b128 v[240:243], v200 offset:8192\n"
      "v_mfma_f32_16x16x32_bf16 v[218:221], a[148:151], v[194:197], v[218:221]\n"
      "buffer_load_dwordx4 v251, s[36:39], s79 offen  lds\n"
      "v_mfma_f32_16x16x32_bf16 v[160:163], a[196:199], v[194:197], v[160:163]\n"
      "s_waitcnt lgkmcnt(1)\n"
      "v_cvt_pk_bf16_f32 v106, v106, v107\n"
      "v_cvt_pk_bf16_f32 v208, v208, v209\n"
      "v_mfma_f32_16x16x32_bf16 v[160:163], a[200:203], v[230:233], v[160:163]\n"
      "ds_read_b128 v[194:197], v116 offset:8192\n"
      "v_add_f32 v142, v142, v143\n"
      "v_mfma_f32_16x16x32_bf16 v[226:229], a[176:179], v[230:233], v[226:229]\n"
      "s_add_u32 m0, s75, 89984\n"
      "v_permlane16_swap_b32_e64 v140, v142\n"
      "v_mfma_f32_16x16x32_bf16 v[166:169], a[204:207], v[236:239], v[166:169]\n"
      "buffer_load_dwordx4 v251, s[36:39], s79 offen offset:128  lds\n"
      "v_cvt_pk_bf16_f32 v96, v96, v97\n"
      "v_cvt_pk_bf16_f32 v213, v214, v215\n"
      "v_pk_mul_f32 v[210:211], v[210:211], v[122:123] op_sel_hi:[1,0]\n"
      "v_add_f32 v140, v140, v142\n"
      "v_mov_b32 v142, v140\n"
      "v_mfma_f32_16x16x32_bf16 v[218:221], a[152:155], v[230:233], v[218:221]\n"
      "s_add_u32 m0, s75, 93952\n"
      "v_permlane32_swap_b32_e64 v140, v142\n"
      "v_cvt_pk_bf16_f32 v125, v126, v127\n"
      "v_add_f32_e64 v140, v140, v142\n"
      "v_mfma_f32_16x16x32_bf16 v[130:133], a[156:159], v[236:239], v[130:133]\n"
      "ds_read_b128 v[214:217], v200 offset:10240\n"
      "v_cvt_pk_bf16_f32 v97, v98, v99\n"
      "v_mfma_f32_16x16x32_bf16 v[222:225], a[180:183], v[236:239], v[222:225]\n"
      "s_waitcnt lgkmcnt(1)\n"
      "v_rsq_f32 v140, v140\n"
      "v_mfma_f32_16x16x32_bf16 v[222:225], a[184:187], v[240:243], v[222:225]\n"
      "buffer_load_dwordx4 v251, s[36:39], s79 offen offset:256  lds\n"
      "ds_read_b128 v[126:129], v0 offset:4096\n"
      "v_mov_b32_e64 v142, v140\n"
      "v_mfma_f32_16x16x32_bf16 v[222:225], a[188:191], v[194:197], v[222:225]\n"
      "ds_read_b128 v[120:123], v116 offset:6144\n"
      "v_cvt_pk_bf16_f32 v209, v210, v211\n"
      "v_mfma_f32_16x16x32_bf16 v[130:133], a[160:163], v[240:243], v[130:133]\n"
      "ds_read_b128 v[198:201], v116 offset:10240\n"
      "v_permlane16_swap_b32_e64 v140, v142\n"
      "v_mfma_f32_16x16x32_bf16 v[130:133], a[164:167], v[194:197], v[130:133]\n"
      "buffer_load_dwordx4 v[98:101], v36, s[28:31], s49 offen\n"
      "s_waitcnt vmcnt(13)\n"
      "s_barrier\n"
      "ds_read_b64_tr_b16 a[240:241], v235 offset:24584\n"
      "v_pk_mul_f32 v[252:253], v[252:253], v[134:135] op_sel_hi:[1,0]\n"
      "v_mul_f32 v140, s50, v140\n"
      "v_pk_mul_f32 v[138:139], v[138:139], v[140:141] op_sel_hi:[1,0]\n"
      "v_mfma_f32_16x16x32_bf16 v[166:169], a[208:211], v[240:243], v[166:169]\n"
      "ds_read_b128 v[240:243], v0 offset:10240\n"
      "v_cvt_pk_bf16_f32 v211, v246, v247\n"
      "v_mfma_f32_16x16x32_bf16 v[166:169], a[212:215], v[194:197], v[166:169]\n"
      "buffer_load_dword v251, v29, s[28:31], s49 offen\n"
      "s_waitcnt lgkmcnt(2)\n"
      "v_mfma_f32_16x16x32_bf16 v[160:163], a[204:207], v[120:123], v[160:163]\n"
      "ds_read_b64_tr_b16 a[244:245], v235 offset:36872\n"
      "ds_read_b64_tr_b16 v[236:237], v248 offset:36864\n"
      "v_mfma_f32_16x16x32_bf16 v[160:163], a[208:211], v[214:217], v[160:163]\n"
      "ds_read_b64_tr_b16 a[222:223], v235 offset:38912\n"
      "ds_read_b64_tr_b16 a[218:219], v235 offset:26624\n"
      "v_mfma_f32_16x16x32_bf16 v[218:221], a[156:159], v[120:123], v[218:221]\n"
      "ds_read_b64_tr_b16 v[232:233], v248 offset:24576\n"
      "ds_read_b64_tr_b16 a[220:221], v235 offset:36864\n"
      "ds_read_b64_tr_b16 a[216:217], v235 offset:24576\n"
      "ds_read_b64_tr_b16 a[246:247], v235 offset:38920\n"
      "ds_read_b64_tr_b16 v[238:239], v248 offset:38912\n"
      "v_mfma_f32_16x16x32_bf16 v[226:229], a[180:183], v[120:123], v[226:229]\n"
      "ds_read_b64_tr_b16 a[242:243], v235 offset:26632\n"
      "v_cvt_pk_bf16_f32 v205, v254, v255\n"
      "v_cvt_pk_bf16_f32 v210, v244, v245\n"
      "v_mfma_f32_16x16x32_bf16 v[226:229], a[184:187], v[214:217], v[226:229]\n"
      "ds_read_b64_tr_b16 v[234:235], v248 offset:26624\n"
      "buffer_load_dwordx4 v[120:123], v36, s[28:31], s74 offen\n"
      "v_pk_mul_f32 v[172:173], v[172:173], v[140:141] op_sel_hi:[1,0]\n"
      "v_cvt_pk_bf16_f32 v107, v108, v109\n"
      "v_pk_mul_f32 v[104:105], v[104:105], v[140:141] op_sel_hi:[1,0]\n"
      "v_cvt_pk_bf16_f32 v109, v114, v115\n"
      "v_pk_mul_f32 v[148:149], v[148:149], v[142:143] op_sel_hi:[1,0]\n"
      "v_xor_b32 v248, 64, v8\n"
      "v_cvt_pk_bf16_f32 v108, v112, v113\n"
      "v_mfma_f32_16x16x32_bf16 v[226:229], a[188:191], v[198:201], v[226:229]\n"
      "ds_read_b128 v[194:197], v0 offset:16384\n"
      "v_cvt_pk_bf16_f32 v204, v252, v253\n"
      "v_mfma_f32_16x16x32_bf16 v[218:221], a[160:163], v[214:217], v[218:221]\n"
      "ds_read_b128 v[214:217], v1 offset:10240\n"
      "ds_read_b128 v[244:247], v1 offset:4096\n"
      "buffer_load_dword v88, v29, s[28:31], s74 offen\n"
      "v_add_u32 v90, 49152, v248\n"
      "v_pk_mul_f32 v[186:187], v[186:187], v[142:143] op_sel_hi:[1,0]\n"
      "v_cvt_pk_bf16_f32 v166, v166, v167\n"
      "v_pk_mul_f32 v[156:157], v[156:157], v[142:143] op_sel_hi:[1,0]\n"
      "v_pk_mul_f32 v[150:151], v[150:151], v[142:143] op_sel_hi:[1,0]\n"
      "s_add_u32 m0, s64, 18432\n"
      "v_pk_mul_f32 v[154:155], v[154:155], v[142:143] op_sel_hi:[1,0]\n"
      "v_pk_mul_f32 v[136:137], v[136:137], v[140:141] op_sel_hi:[1,0]\n"
      "v_pk_mul_f32 v[184:185], v[184:185], v[142:143] op_sel_hi:[1,0]\n"
      "v_cvt_pk_bf16_f32 v226, v226, v227\n"
      "v_cvt_pk_bf16_f32 v184, v184, v185\n"
      "v_pk_mul_f32 v[170:171], v[170:171], v[140:141] op_sel_hi:[1,0]\n"
      "v_cvt_pk_bf16_f32 v227, v228, v229\n"
      "v_pk_mul_f32 v[102:103], v[102:103], v[140:141] op_sel_hi:[1,0]\n"
      "v_cvt_pk_bf16_f32 v167, v168, v169\n"
      "v_cvt_pk_bf16_f32 v185, v186, v187\n"
      "v_mfma_f32_16x16x32_bf16 v[218:221], a[164:167], v[198:201], v[218:221]\n"
      "buffer_load_dwordx4 v16, s[16:19], s45 offen  lds\n"
      "v_cvt_pk_bf16_f32 v222, v222, v223\n"
      "v_mfma_f32_16x16x32_bf16 v[228:231], v[126:129], a[144:147], 0\n"
      "s_waitcnt lgkmcnt(0)\n"
      "v_cvt_pk_bf16_f32 v223, v224, v225\n"
      "v_cvt_pk_bf16_f32 v170, v170, v171\n"
      "v_mfma_f32_16x16x32_bf16 v[228:231], v[244:247], a[148:151], v[228:231]\n"
      "ds_write_b64 v250, v[166:167] offset:64\n"
      "v_cvt_pk_bf16_f32 v188, v188, v189\n"
      "v_mfma_f32_16x16x32_bf16 v[252:255], v[126:129], a[192:195], 0\n"
      "s_add_u32 m0, s64, 22528\n"
      "v_cvt_pk_bf16_f32 v130, v130, v131\n"
      "v_cvt_pk_bf16_f32 v186, v148, v149\n"
      "v_mfma_f32_16x16x32_bf16 v[252:255], v[244:247], a[196:199], v[252:255]\n"
      "buffer_load_dwordx4 v17, s[16:19], s45 offen  lds\n"
      "v_cvt_pk_bf16_f32 v171, v172, v173\n"
      "v_mfma_f32_16x16x32_bf16 v[160:163], a[212:215], v[198:201], v[160:163]\n"
      "ds_read_b128 v[166:169], v0 offset:2048\n"
      "ds_write_b64 v250, v[222:223] offset:32\n"
      "v_cvt_pk_bf16_f32 v131, v132, v133\n"
      "v_mov_b32 v223, 0xbc80bc80\n"
      "v_mfma_f32_16x16x32_bf16 v[228:231], v[240:243], a[152:155], v[228:231]\n"
      "s_add_u32 m0, s64, 26624\n"
      "v_cvt_pk_bf16_f32 v154, v154, v155\n"
      "v_add_u32 v178, 49152, v8\n"
      "v_cvt_pk_bf16_f32 v136, v136, v137\n"
      "v_mfma_f32_16x16x32_bf16 v[252:255], v[240:243], a[200:203], v[252:255]\n"
      "buffer_load_dwordx4 v18, s[16:19], s45 offen  lds\n"
      "v_cvt_pk_bf16_f32 v172, v102, v103\n"
      "v_mfma_f32_16x16x32_bf16 v[110:113], v[126:129], a[168:171], 0\n"
      "ds_read_b128 v[140:143], v1 offset:2048\n"
      "v_cvt_pk_bf16_f32 v189, v190, v191\n"
      "v_cvt_pk_bf16_f32 v218, v218, v219\n"
      "v_mfma_f32_16x16x32_bf16 v[110:113], v[244:247], a[172:175], v[110:113]\n"
      "ds_read_b128 v[198:201], v1 offset:16384\n"
      "ds_read_b128 v[114:117], v8 offset:24576\n"
      "v_mfma_f32_16x16x32_bf16 v[110:113], v[240:243], a[176:179], v[110:113]\n"
      "s_add_u32 m0, s64, 30720\n"
      "v_mov_b32 v224, 0xbc80bc80\n"
      "v_mov_b32 v225, 0xbc80bc80\n"
      "v_mfma_f32_16x16x32_bf16 v[252:255], v[214:217], a[204:207], v[252:255]\n"
      "ds_read_b128 v[132:135], v248 offset:24576\n"
      "v_cvt_pk_bf16_f32 v173, v104, v105\n"
      "v_mfma_f32_16x16x32_bf16 v[252:255], v[194:197], a[208:211], v[252:255]\n"
      "ds_write_b64 v250, v[130:131]\n"
      "s_waitcnt vmcnt(13)\n"
      "s_barrier\n"
      "v_cvt_pk_bf16_f32 v187, v150, v151\n"
      "v_mfma_f32_16x16x32_bf16 v[228:231], v[214:217], a[156:159], v[228:231]\n"
      "ds_read_b64_tr_b16 v[244:245], v82 offset:12288\n"
      "ds_read_b64_tr_b16 a[230:231], v89 offset:14336\n"
      "v_mfma_f32_16x16x32_bf16 v[228:231], v[194:197], a[160:163], v[228:231]\n"
      "ds_read_b64_tr_b16 a[252:253], v89 offset:12296\n"
      "v_mov_b32 v222, 0xbc80bc80\n"
      "v_mfma_f32_16x16x32_bf16 v[110:113], v[214:217], a[180:183], v[110:113]\n"
      "buffer_load_dwordx4 v19, s[16:19], s45 offen  lds\n"
      "ds_read_b64_tr_b16 a[248:249], v89 offset:8\n"
      "v_mfma_f32_16x16x32_bf16 v[110:113], v[194:197], a[184:187], v[110:113]\n"
      "ds_read_b64_tr_b16 v[242:243], v82 offset:2048\n"
      "s_waitcnt lgkmcnt(6)\n"
      "v_mfma_f32_16x16x32_bf16 v[110:113], v[198:201], a[188:191], v[110:113]\n"
      "ds_read_b128 v[148:151], v90\n"
      "ds_read_b64_tr_b16 a[250:251], v89 offset:2056\n"
      "s_add_u32 m0, s65, 34816\n"
      "v_mfma_f32_16x16x32_bf16 v[102:105], v[114:117], v[222:225], 0\n"
      "ds_read_b64_tr_b16 a[254:255], v89 offset:14344\n"
      "ds_read_b64_tr_b16 v[246:247], v82 offset:14336\n"
      "v_mfma_f32_16x16x32_bf16 v[102:105], v[132:135], v[222:225], v[102:105]\n"
      "ds_read_b64_tr_b16 v[240:241], v82\n"
      "v_cvt_pk_bf16_f32 v137, v138, v139\n"
      "v_mfma_f32_16x16x32_bf16 v[252:255], v[198:201], a[212:215], v[252:255]\n"
      "ds_read_b128 v[114:117], v178\n"
      "ds_read_b64_tr_b16 a[224:225], v89\n"
      "v_mfma_f32_16x16x32_bf16 v[228:231], v[198:201], a[164:167], v[228:231]\n"
      "buffer_load_dwordx4 v15, s[16:19], s45 offen  lds\n"
      "v_mfma_f32_16x16x32_bf16 v[214:217], v[166:169], a[192:195], 0\n"
      "ds_read_b64_tr_b16 a[226:227], v89 offset:2048\n"
      "v_cvt_pk_bf16_f32 v155, v156, v157\n"
      "v_mfma_f32_16x16x32_bf16 v[214:217], v[140:143], a[196:199], v[214:217]\n"
      "ds_read_b64_tr_b16 a[228:229], v89 offset:12288\n"
      "v_cvt_pk_bf16_f32 v219, v220, v221\n"
      "v_mfma_f32_16x16x32_bf16 v[130:133], v[166:169], a[144:147], 0\n"
      "ds_read_b128 v[126:129], v1 offset:8192\n"
      "v_cvt_pk_bf16_f32 v160, v160, v161\n"
      "v_mfma_f32_16x16x32_bf16 v[194:197], v[166:169], a[168:171], 0\n"
      "ds_read_b128 v[166:169], v248 offset:28672\n"
      "s_waitcnt lgkmcnt(5)\n"
      "v_mfma_f32_16x16x32_bf16 v[156:159], v[114:117], v[222:225], 0\n"
      "ds_read_b128 v[114:117], v8 offset:28672\n"
      "v_cvt_pk_bf16_f32 v76, v228, v229\n"
      "v_mfma_f32_16x16x32_bf16 v[156:159], v[148:151], v[222:225], v[156:159]\n"
      "ds_read_b128 v[148:151], v0 offset:8192\n"
      "ds_read_b128 v[198:201], v8 offset:32768\n"
      "v_mfma_f32_16x16x32_bf16 v[130:133], v[140:143], a[148:151], v[130:133]\n"
      "s_mov_b32 exec_lo, 0x1f001f\n"
      "s_mov_b32 exec_hi, 0x1f001f\n"
      "ds_write_b128 v249, v[170:173] offset:18688\n"
      "ds_write_b64 v249, v[136:137] offset:18704\n"
      "ds_write_b64 v249, v[154:155] offset:18712\n"
      "ds_write_b128 v249, v[184:187] offset:18720\n"
      "s_mov_b64 exec, -1\n"
      "v_cvt_pk_bf16_f32 v80, v110, v111\n"
      "v_mfma_f32_16x16x32_bf16 v[194:197], v[140:143], a[172:175], v[194:197]\n"
      "s_waitcnt lgkmcnt(4)\n"
      "v_cvt_pk_bf16_f32 v161, v162, v163\n"
      "v_cvt_pk_bf16_f32 v81, v112, v113\n"
      "v_cvt_pk_bf16_f32 v77, v230, v231\n"
      "v_mfma_f32_16x16x32_bf16 v[214:217], v[148:151], a[200:203], v[214:217]\n"
      "ds_read_b128 v[228:231], v1 offset:14336\n"
      "v_cvt_pk_bf16_f32 v85, v254, v255\n"
      "v_mfma_f32_16x16x32_bf16 v[214:217], v[126:129], a[204:207], v[214:217]\n"
      "buffer_load_dwordx4 v[140:143], v36, s[28:31], s47 offen\n"
      "v_mfma_f32_16x16x32_bf16 v[194:197], v[148:151], a[176:179], v[194:197]\n"
      "ds_read_b128 v[110:113], v248 offset:32768\n"
      "v_subrev_u32 v8, 115456, v9\n"
      "v_mfma_f32_16x16x32_bf16 v[130:133], v[148:151], a[152:155], v[130:133]\n"
      "ds_read_b128 v[162:165], v178 offset:8192\n"
      "v_lshrrev_b32 v8, 4, v8\n"
      "v_mfma_f32_16x16x32_bf16 v[130:133], v[126:129], a[156:159], v[130:133]\n"
      "s_lshl_b32 s92, s40, 6\n"
      "ds_read_b128 v[148:151], v90 offset:4096\n"
      "v_mfma_f32_16x16x32_bf16 v[194:197], v[126:129], a[180:183], v[194:197]\n"
      "ds_read_b128 v[126:129], v178 offset:4096\n"
      "buffer_load_dword v135, v29, s[28:31], s47 offen\n"
      "v_mfma_f32_16x16x32_bf16 v[102:105], v[114:117], v[222:225], v[102:105]\n"
      "ds_read_b128 v[114:117], v90 offset:8192\n"
      "v_and_b32_e64 v8, 48, v8\n"
      "v_mfma_f32_16x16x32_bf16 v[102:105], v[166:169], v[222:225], v[102:105]\n"
      "ds_read_b128 v[166:169], v0 offset:14336\n"
      "s_mov_b32 exec_lo, 0x1f001f\n"
      "s_mov_b32 exec_hi, 0x1f001f\n"
      "ds_write_b64 v250, v[218:219] offset:18688\n"
      "ds_write_b64 v250, v[226:227] offset:18720\n"
      "ds_write_b64 v250, v[160:161] offset:18752\n"
      "s_mov_b64 exec, -1\n"
      "v_add_u32 v8, 123136, v8\n"
      "v_mfma_f32_16x16x32_bf16 v[102:105], v[198:201], v[222:225], v[102:105]\n"
      "ds_read_b128 v[218:221], v0 offset:6144\n"
      "v_subrev_u32 v186, 115456, v9\n"
      "v_cvt_pk_bf16_f32 v84, v252, v253\n"
      "s_waitcnt lgkmcnt(4)\n"
      "v_mfma_f32_16x16x32_bf16 v[102:105], v[110:113], v[222:225], v[102:105]\n"
      "s_waitcnt vmcnt(11)\n"
      "s_barrier\n"
      "ds_read_b128 v[252:255], v178 offset:24576\n"
      "v_mfma_f32_16x16x32_bf16 v[156:159], v[126:129], v[222:225], v[156:159]\n"
      "ds_read_b128 v[110:113], v178 offset:32768\n"
      "v_bfe_u32 v190, v186, 8, 2\n"
      "v_mfma_f32_16x16x32_bf16 v[156:159], v[148:151], v[222:225], v[156:159]\n"
      "ds_read_b128 v[126:129], v90 offset:28672\n"
      "v_bfe_u32 v186, v186, 4, 4\n"
      "v_mfma_f32_16x16x32_bf16 v[156:159], v[162:165], v[222:225], v[156:159]\n"
      "ds_read_b128 v[160:163], v178 offset:28672\n"
      "v_mul_f32 v102, 0x3eaaaaab, v102\n"
      "v_mfma_f32_16x16x32_bf16 v[156:159], v[114:117], v[222:225], v[156:159]\n"
      "ds_read_b128 v[114:117], v90 offset:24576\n"
      "v_mul_f32 v103, 0x3eaaaaab, v103\n"
      "v_lshlrev_b32 v190, 2, v190\n"
      "v_mfma_f32_16x16x32_bf16 v[130:133], v[166:169], a[160:163], v[130:133]\n"
      "ds_read_b128 v[198:201], v0\n"
      "s_waitcnt vmcnt(10)\n"
      "v_or3_b32 v98, v98, v99, v100\n"
      "v_mfma_f32_16x16x32_bf16 v[194:197], v[166:169], a[184:187], v[194:197]\n"
      "ds_read_b64 v[164:165], v10 offset:24\n"
      "v_mul_f32 v159, 0x3eaaaaab, v159\n"
      "v_mfma_f32_16x16x32_bf16 v[194:197], v[228:231], a[188:191], v[194:197]\n"
      "ds_read_b128 v[136:139], v10 offset:32\n"
      "v_mul_f32 v104, 0x3eaaaaab, v104\n"
      "v_mfma_f32_16x16x32_bf16 v[214:217], v[166:169], a[208:211], v[214:217]\n"
      "ds_read_b128 v[166:169], v1\n"
      "v_mul_f32 v105, 0x3eaaaaab, v105\n"
      "v_mfma_f32_16x16x32_bf16 v[130:133], v[228:231], a[164:167], v[130:133]\n"
      "ds_read_b64_tr_b16 a[236:237], v89 offset:36864\n"
      "s_waitcnt vmcnt(9)\n"
      "v_or3_b32 v98, v98, v101, v251\n"
      "v_mfma_f32_16x16x32_bf16 v[214:217], v[228:231], a[212:215], v[214:217]\n"
      "ds_read_b128 v[228:231], v90 offset:32768\n"
      "s_waitcnt lgkmcnt(6)\n"
      "v_mfma_f32_16x16x32_bf16 v[170:173], v[252:255], v[222:225], 0\n"
      "ds_read_b64_tr_b16 v[248:249], v82 offset:24576\n"
      "v_add_u32 v184, s92, v8\n"
      "v_mfma_f32_16x16x32_bf16 v[170:173], v[114:117], v[222:225], v[170:173]\n"
      "ds_write_b32 v31, v98 offset:6656\n"
      "v_mfma_f32_16x16x32_bf16 v[170:173], v[160:163], v[222:225], v[170:173]\n"
      "ds_read_b64_tr_b16 a[238:239], v89 offset:38912\n"
      "v_cvt_pk_bf16_f32 v197, v196, v197\n"
      "v_mfma_f32_16x16x32_bf16 v[170:173], v[126:129], v[222:225], v[170:173]\n"
      "ds_write_b128 v184, v[102:105]\n"
      "v_mfma_f32_16x16x32_bf16 v[170:173], v[110:113], v[222:225], v[170:173]\n"
      "s_waitcnt lgkmcnt(4)\n"
      "v_mul_f32 v156, 0x3eaaaaab, v156\n"
      "v_mul_f32 v158, 0x3eaaaaab, v158\n"
      "v_mul_f32 v157, 0x3eaaaaab, v157\n"
      "v_mfma_f32_16x16x32_bf16 v[170:173], v[228:231], v[222:225], v[170:173]\n"
      "ds_read_b64_tr_b16 a[232:233], v89 offset:24576\n"
      "ds_read_b64_tr_b16 v[226:227], v89 offset:26632\n"
      "v_mfma_f32_16x16x32_bf16 v[160:163], v[144:147], v[174:177], v[40:43]\n"
      "ds_read_b64_tr_b16 v[228:229], v89 offset:36872\n"
      "s_waitcnt vmcnt(8)\n"
      "v_or3_b32 v120, v120, v121, v122\n"
      "v_mfma_f32_16x16x32_bf16 v[110:113], v[198:201], a[168:171], 0\n"
      "ds_write_b128 v184, v[156:159] offset:256\n"
      "v_mfma_f32_16x16x32_bf16 v[126:129], v[136:139], v[174:177], v[24:27]\n"
      "ds_read_b64_tr_b16 v[230:231], v89 offset:38920\n"
      "v_mul_f32 v173, 0x3eaaaaab, v173\n"
      "v_mfma_f32_16x16x32_bf16 v[102:105], v[180:183], v[174:177], v[44:47]\n"
      "ds_read_b64_tr_b16 v[224:225], v89 offset:24584\n"
      "s_waitcnt vmcnt(7)\n"
      "v_or3_b32 v120, v120, v123, v88\n"
      "v_mfma_f32_16x16x32_bf16 v[110:113], v[166:169], a[172:175], v[110:113]\n"
      "ds_read_b64_tr_b16 v[254:255], v82 offset:38912\n"
      "v_mul_f32 v170, 0x3eaaaaab, v170\n"
      "v_mfma_f32_16x16x16_bf16 v[160:163], v[192:193], v[118:119], v[160:163]\n"
      "ds_read_b64_tr_b16 a[234:235], v89 offset:26624\n"
      "v_mul_f32 v171, 0x3eaaaaab, v171\n"
      "v_mfma_f32_16x16x32_bf16 v[156:159], v[198:201], a[144:147], 0\n"
      "ds_write_b32 v31, v120 offset:5632\n"
      "v_mfma_f32_16x16x32_bf16 v[156:159], v[166:169], a[148:151], v[156:159]\n"
      "ds_read_b64_tr_b16 v[250:251], v82 offset:26624\n"
      "v_cvt_pk_bf16_f32 v196, v194, v195\n"
      "v_mfma_f32_16x16x32_bf16 v[120:123], v[198:201], a[192:195], 0\n"
      "ds_read_b128 v[98:101], v1 offset:6144\n"
      "v_cvt_pk_bf16_f32 v217, v216, v217\n"
      "v_mfma_f32_16x16x32_bf16 v[148:151], v[106:109], v[174:177], v[48:51]\n"
      "ds_read_b64_tr_b16 v[252:253], v82 offset:36864\n"
      "v_mul_f32 v172, 0x3eaaaaab, v172\n"
      "v_mfma_f32_16x16x32_bf16 v[110:113], v[218:221], a[176:179], v[110:113]\n"
      "ds_write_b128 v184, v[170:173] offset:512\n"
      "v_mfma_f32_16x16x32_bf16 v[120:123], v[166:169], a[196:199], v[120:123]\n"
      "s_waitcnt lgkmcnt(0)\n"
      "s_barrier\n"
      "v_cvt_pk_bf16_f32 v133, v132, v133\n"
      "v_mfma_f32_16x16x32_bf16 v[120:123], v[218:221], a[200:203], v[120:123]\n"
      "ds_read_b128 v[174:177], v9 offset:6656\n"
      "v_sub_u32 v186, v186, v190\n"
      "v_mfma_f32_16x16x16_bf16 v[126:129], v[164:165], v[118:119], v[126:129]\n"
      "ds_read_b128 v[166:169], v0 offset:12288\n"
      "v_cvt_pk_bf16_f32 v132, v130, v131\n"
      "v_cvt_pk_bf16_f32 v216, v214, v215\n"
      "v_mfma_f32_16x16x16_bf16 v[102:105], v[124:125], v[118:119], v[102:105]\n"
      "v_lshlrev_b32 v186, 4, v186\n"
      "v_mov_b32 v190, 0x3f80\n"
      "v_mov_b32 v184, 0\n"
      "v_mfma_f32_16x16x32_bf16 v[170:173], v[144:147], v[202:205], v[52:55]\n"
      "s_waitcnt lgkmcnt(3)\n"
      "v_mov_b32_e64 v191, 0\n"
      "v_cmp_gt_u32 s[80:81], 64, v186\n"
      "v_mfma_f32_16x16x32_bf16 v[198:201], v[106:109], v[202:205], v[60:63]\n"
      "s_cmp_ge_u32 s95, 0x40000000\n"
      "s_cselect_b32 s94, -1, -2\n"
      "v_lshlrev_b64 v[190:191], v186, v[190:191]\n"
      "v_mfma_f32_16x16x32_bf16 v[156:159], v[218:221], a[152:155], v[156:159]\n"
      "ds_read_b128 v[218:221], v1 offset:12288\n"
      "v_mov_b32 v128, 0\n"
      "v_mfma_f32_16x16x32_bf16 v[110:113], v[98:101], a[180:183], v[110:113]\n"
      "s_waitcnt lgkmcnt(2)\n"
      "v_or3_b32 v174, v174, v175, v176\n"
      "v_bitop3_b32 v174, v174, v177, s33 bitop3:0xa8\n"
      "v_mfma_f32_16x16x16_bf16 v[148:151], v[212:213], v[118:119], v[148:151]\n"
      "v_cndmask_b32_e64 v186, 0, v190, s[80:81]\n"
      "v_mov_b32_e64 v129, 0\n"
      "v_cmp_ne_u32 s[56:57], 0, v174\n"
      "v_mfma_f32_16x16x32_bf16 v[120:123], v[98:101], a[204:207], v[120:123]\n"
      "s_or_b64 vcc, s[56:57], s[54:55]\n"
      "v_cndmask_b32_e64 v187, 0, v191, s[80:81]\n"
      "s_waitcnt lgkmcnt(1)\n"
      "v_mfma_f32_16x16x32_bf16 v[120:123], v[166:169], a[208:211], v[120:123]\n"
      "s_cbranch_vccnz L_rare_1_%=\n"
      "L_back_2_%=:\n"
      "v_mfma_f32_16x16x32_bf16 v[156:159], v[98:101], a[156:159], v[156:159]\n"
      "s_cbranch_vccnz L_rare_3_%=\n"
      "L_back_4_%=:\n"
      "v_mfma_f32_16x16x32_bf16 v[156:159], v[166:169], a[160:163], v[156:159]\n"
      "v_exp_f32 v126, v126\n"
      "v_mfma_f32_16x16x32_bf16 v[110:113], v[166:169], a[184:187], v[110:113]\n"
      "s_waitcnt lgkmcnt(0)\n"
      "v_exp_f32 v103, v103\n"
      "v_exp_f32 v163, v163\n"
      "v_exp_f32_e64 v150, v150\n"
      "v_mfma_f32_16x16x32_bf16 v[110:113], v[218:221], a[188:191], v[110:113]\n"
      "ds_read_b128 v[174:177], v8\n"
      "v_exp_f32_e64 v151, v151\n"
      "v_mfma_f32_16x16x32_bf16 v[120:123], v[218:221], a[212:215], v[120:123]\n"
      "ds_read_b128 v[166:169], v8 offset:64\n"
      "v_cndmask_b32_e64 v127, 0, v191, s[80:81]\n"
      "v_mfma_f32_16x16x32_bf16 v[156:159], v[218:221], a[164:167], v[156:159]\n"
      "ds_read_u16 v82, v11 offset:32\n"
      "v_exp_f32 v148, v148\n"
      "v_mfma_f32_16x16x16_bf16 v[170:173], v[192:193], v[96:97], v[170:173]\n"
      "v_exp_f32 v162, v162\n"
      "v_exp_f32 v160, v160\n"
      "v_mfma_f32_16x16x16_bf16 v[198:201], v[212:213], v[96:97], v[198:201]\n"
      "v_exp_f32 v161, v161\n"
      "v_cvt_pk_bf16_f32 v89, v150, v151\n"
      "v_exp_f32_e64 v104, v104\n"
      "v_cvt_pk_bf16_f32 v130, v156, v157\n"
      "v_cvt_pk_bf16_f32 v160, v160, v161\n"
      "v_cvt_pk_bf16_f32 v195, v112, v113\n"
      "v_mfma_f32_16x16x32_bf16 v[218:221], v[136:139], v[202:205], v[26:29]\n"
      "s_waitcnt lgkmcnt(2)\n"
      "v_cvt_pk_bf16_f32 v90, v126, 0\n"
      "v_exp_f32 v102, v102\n"
      "v_mfma_f32_16x16x32_bf16 v[154:157], v[180:183], v[202:205], v[56:59]\n"
      "ds_read_b128 v[202:205], v9 offset:5632\n"
      "v_cvt_pk_bf16_f32 v161, v162, v163\n"
      "v_cvt_pk_bf16_f32 v194, v110, v111\n"
      "v_mfma_f32_16x16x16_bf16 v[218:221], v[164:165], v[96:97], v[218:221]\n"
      "v_cvt_pk_bf16_f32 v131, v158, v159\n"
      "v_exp_f32_e64 v149, v149\n"
      "v_mfma_f32_16x16x16_bf16 v[154:157], v[124:125], v[96:97], v[154:157]\n"
      "v_cndmask_b32_e64 v126, 0, v190, s[80:81]\n"
      "v_exp_f32_e64 v105, v105\n"
      "v_cvt_pk_bf16_f32 v88, v148, v149\n"
      "v_cvt_pk_bf16_f32 v162, v102, v103\n"
      "v_mfma_f32_16x16x32_bf16 v[110:113], v[126:129], a[216:219], v[174:177]\n"
      "s_cmp_gt_u32 s42, s44\n"
      "s_cselect_b32 s95, s94, s95\n"
      "v_cvt_pk_bf16_f32 v214, v120, v121\n"
      "v_cvt_pk_bf16_f32 v163, v104, v105\n"
      "v_mfma_f32_16x16x32_bf16 v[220:223], v[136:139], v[208:211], v[28:31]\n"
      "s_cbranch_vccnz L_rare_5_%=\n"
      "L_back_6_%=:\n"
      "v_mfma_f32_16x16x32_bf16 v[116:119], v[126:129], a[240:243], v[174:177]\n"
      "s_waitcnt lgkmcnt(0)\n"
      "v_or3_b32 v202, v202, v203, v204\n"
      "v_cvt_pk_bf16_f32 v215, v122, v123\n"
      "v_cvt_pk_bf16_f32 v112, v112, v113\n"
      "v_mfma_f32_16x16x32_bf16 v[120:123], v[126:129], v[232:235], v[174:177]\n"
      "v_exp_f32_e64 v171, v171\n"
      "v_mfma_f32_16x16x32_bf16 v[174:177], v[32:35], v[160:163], 0\n"
      "v_cvt_pk_bf16_f32 v110, v110, v111\n"
      "v_exp_f32_e64 v200, v200\n"
      "v_bitop3_b32 v202, v202, v205, s33 bitop3:0xa8\n"
      "v_mfma_f32_16x16x32_bf16 v[174:177], v[32:35], v[88:91], v[174:177]\n"
      "v_exp_f32 v198, v198\n"
      "v_mfma_f32_16x16x16_bf16 v[220:223], v[164:165], v[188:189], v[220:223]\n"
      "v_exp_f32 v199, v199\n"
      "v_mov_b32_e64 v185, 0\n"
      "v_cvt_pk_bf16_f32 v116, v116, v117\n"
      "v_exp_f32_e64 v201, v201\n"
      "v_mfma_f32_16x16x32_bf16 v[204:207], v[184:187], v[232:235], v[166:169]\n"
      "v_cvt_pk_bf16_f32 v93, v200, v201\n"
      "v_cvt_pk_bf16_f32 v118, v118, v119\n"
      "v_mfma_f32_16x16x32_bf16 v[176:179], v[184:187], a[216:219], v[166:169]\n"
      "v_accvgpr_write_b32 a217, v112\n"
      "v_cvt_pk_bf16_f32 v232, v120, v121\n"
      "v_mfma_f32_16x16x32_bf16 v[98:101], v[184:187], a[240:243], v[166:169]\n"
      "ds_read_u16 v86, v11 offset:64\n"
      "v_accvgpr_write_b32 a240, v116\n"
      "v_cvt_pk_bf16_f32 v233, v122, v123\n"
      "v_accvgpr_write_b32 a216, v110\n"
      "v_mfma_f32_16x16x32_bf16 v[166:169], v[106:109], v[208:211], v[72:75]\n"
      "v_cvt_pk_bf16_f32 v235, v206, v207\n"
      "v_cvt_pk_bf16_f32 v178, v178, v179\n"
      "v_accvgpr_write_b32 a219, v178\n"
      "v_mfma_f32_16x16x32_bf16 v[110:113], v[144:147], v[208:211], v[64:67]\n"
      "ds_read_b64 v[116:117], v10 offset:16\n"
      "v_cvt_pk_bf16_f32 v92, v198, v199\n"
      "s_waitcnt vmcnt(1)\n"
      "v_or3_b32 v140, v140, v141, v142\n"
      "v_exp_f32 v155, v155\n"
      "v_mfma_f32_16x16x32_bf16 v[120:123], v[180:183], v[208:211], v[68:71]\n"
      "v_cvt_pk_bf16_f32 v176, v176, v177\n"
      "v_cvt_pk_bf16_f32 v100, v100, v101\n"
      "v_cvt_pk_bf16_f32 v234, v204, v205\n"
      "v_mfma_f32_16x16x32_bf16 v[208:211], v[194:197], v[160:163], 0\n"
      "ds_read_b128 v[204:207], v10\n"
      "v_cmp_ne_u32 s[56:57], 0, v202\n"
      "v_mfma_f32_16x16x32_bf16 v[208:211], v[80:83], v[88:91], v[208:211]\n"
      "ds_read_u16 v78, v11\n"
      "v_cvt_pk_bf16_f32 v98, v98, v99\n"
      "v_mfma_f32_16x16x16_bf16 v[110:113], v[192:193], v[188:189], v[110:113]\n"
      "v_exp_f32 v157, v157\n"
      "v_exp_f32 v170, v170\n"
      "v_mfma_f32_16x16x16_bf16 v[166:169], v[212:213], v[188:189], v[166:169]\n"
      "v_exp_f32 v173, v173\n"
      "v_accvgpr_write_b32 a243, v100\n"
      "v_cvt_pk_bf16_f32 v170, v170, v171\n"
      "v_mfma_f32_16x16x16_bf16 v[120:123], v[124:125], v[188:189], v[120:123]\n"
      "s_waitcnt vmcnt(0)\n"
      "v_or3_b32 v140, v140, v143, v135\n"
      "v_exp_f32 v172, v172\n"
      "v_mfma_f32_16x16x32_bf16 v[188:191], v[214:217], v[160:163], 0\n"
      "s_waitcnt lgkmcnt(2)\n"
      "v_rcp_f32_e64 v174, v174\n"
      "v_mfma_f32_16x16x32_bf16 v[188:191], v[84:87], v[88:91], v[188:191]\n"
      "s_waitcnt lgkmcnt(1)\n"
      "v_exp_f32_e64 v154, v154\n"
      "v_mfma_f32_16x16x32_bf16 v[200:203], v[204:207], v[106:109], v[30:33]\n"
      "s_waitcnt lgkmcnt(0)\n"
      "v_cvt_pk_bf16_f32 v171, v172, v173\n"
      "v_exp_f32 v156, v156\n"
      "v_mfma_f32_16x16x32_bf16 v[106:109], v[130:133], v[160:163], 0\n"
      "ds_write_b32 v31, v140 offset:4608\n"
      "v_mfma_f32_16x16x32_bf16 v[106:109], v[76:79], v[88:91], v[106:109]\n"
      "s_cbranch_vccnz L_rare_7_%=\n"
      "L_back_8_%=:\n"
      "v_mfma_f32_16x16x32_bf16 v[160:163], v[204:207], v[136:139], v[30:33]\n"
      "ds_read_b128 v[140:143], v8 offset:128\n"
      "v_cvt_pk_bf16_f32 v173, v156, v157\n"
      "v_mfma_f32_16x16x32_bf16 v[156:159], v[204:207], v[180:183], v[30:33]\n"
      "s_add_u32 s42, s42, 1\n"
      "v_exp_f32 v113, v113\n"
      "v_exp_f32_e64 v218, v218\n"
      "v_mfma_f32_16x16x32_bf16 v[100:103], v[204:207], v[144:147], v[30:33]\n"
      "ds_read_b128 v[144:147], v8 offset:192\n"
      "v_cvt_pk_bf16_f32 v94, v218, 0\n"
      "v_mfma_f32_16x16x16_bf16 v[156:159], v[116:117], v[124:125], v[156:159]\n"
      "v_exp_f32 v122, v122\n"
      "v_exp_f32 v166, v166\n"
      "v_cvt_pk_bf16_f32 v172, v154, v155\n"
      "v_exp_f32_e64 v111, v111\n"
      "v_pk_mul_f32 v[106:107], v[106:107], v[174:175] op_sel_hi:[1,0]\n"
      "v_pk_mul_f32 v[208:209], v[208:209], v[174:175] op_sel_hi:[1,0]\n"
      "v_pk_mul_f32 v[108:109], v[108:109], v[174:175] op_sel_hi:[1,0]\n"
      "v_pk_mul_f32 v[190:191], v[190:191], v[174:175] op_sel_hi:[1,0]\n"
      "v_pk_mul_f32 v[210:211], v[210:211], v[174:175] op_sel_hi:[1,0]\n"
      "v_pk_mul_f32 v[188:189], v[188:189], v[174:175] op_sel_hi:[1,0]\n"
      "v_mfma_f32_16x16x16_bf16 v[100:103], v[116:117], v[192:193], v[100:103]\n"
      "v_accvgpr_write_b32 a242, v98\n"
      "v_exp_f32 v110, v110\n"
      "v_mfma_f32_16x16x16_bf16 v[160:163], v[116:117], v[164:165], v[160:163]\n"
      "v_exp_f32 v220, v220\n"
      "v_cvt_pk_bf16_f32 v188, v188, v189\n"
      "v_mfma_f32_16x16x32_bf16 v[178:181], v[32:35], v[170:173], 0\n"
      "s_waitcnt lgkmcnt(0)\n"
      "v_exp_f32 v167, v167\n"
      "v_mfma_f32_16x16x32_bf16 v[178:181], v[32:35], v[92:95], v[178:181]\n"
      "ds_read_b128 v[150:153], v8 offset:384\n"
      "v_cvt_pk_bf16_f32 v88, v166, v167\n"
      "v_mfma_f32_16x16x32_bf16 v[164:167], v[184:187], a[220:223], v[144:147]\n"
      "v_exp_f32 v120, v120\n"
      "v_mfma_f32_16x16x32_bf16 v[96:99], v[126:129], v[236:239], v[140:143]\n"
      "v_exp_f32 v123, v123\n"
      "v_cvt_pk_bf16_f32 v106, v106, v107\n"
      "v_cvt_pk_bf16_f32 v110, v110, v111\n"
      "v_mfma_f32_16x16x32_bf16 v[102:105], v[184:187], v[236:239], v[144:147]\n"
      "ds_read_b128 v[136:139], v8 offset:320\n"
      "v_cndmask_b32 v100, v100, v156, s[58:59]\n"
      "v_mfma_f32_16x16x32_bf16 v[154:157], v[126:129], a[244:247], v[140:143]\n"
      "v_cvt_pk_bf16_f32 v164, v164, v165\n"
      "v_exp_f32_e64 v112, v112\n"
      "v_mfma_f32_16x16x16_bf16 v[200:203], v[116:117], v[212:213], v[200:203]\n"
      "v_cvt_pk_bf16_f32 v111, v112, v113\n"
      "v_cvt_pk_bf16_f32 v189, v190, v191\n"
      "v_exp_f32_e64 v121, v121\n"
      "v_mfma_f32_16x16x32_bf16 v[190:193], v[130:133], v[170:173], 0\n"
      "ds_read_b128 v[114:117], v8 offset:448\n"
      "v_cvt_pk_bf16_f32 v107, v108, v109\n"
      "v_mfma_f32_16x16x32_bf16 v[190:193], v[76:79], v[92:95], v[190:193]\n"
      "ds_write_b64 v4, v[188:189]\n"
      "v_cvt_pk_bf16_f32 v113, v122, v123\n"
      "v_mfma_f32_16x16x32_bf16 v[122:125], v[184:187], a[244:247], v[144:147]\n"
      "s_nop 2\n"
      "ds_read_b128 v[144:147], v8 offset:256\n"
      "v_cvt_pk_bf16_f32 v112, v120, v121\n"
      "v_mfma_f32_16x16x32_bf16 v[180:183], v[194:197], v[170:173], 0\n"
      "v_exp_f32 v169, v169\n"
      "v_cvt_pk_bf16_f32 v109, v210, v211\n"
      "v_cvt_pk_bf16_f32 v166, v166, v167\n"
      "v_mfma_f32_16x16x32_bf16 v[180:183], v[80:83], v[92:95], v[180:183]\n"
      "v_rcp_f32_e64 v178, v178\n"
      "v_cndmask_b32 v100, v100, v200, s[60:61]\n"
      "v_mfma_f32_16x16x32_bf16 v[210:213], v[214:217], v[170:173], 0\n"
      "v_accvgpr_write_b32 a218, v176\n"
      "v_cvt_pk_bf16_f32 v108, v208, v209\n"
      "v_cvt_pk_bf16_f32 v90, v220, 0\n"
      "v_mfma_f32_16x16x32_bf16 v[210:213], v[84:87], v[92:95], v[210:213]\n"
      "v_exp_f32_e64 v168, v168\n"
      "v_cndmask_b32 v100, v100, v160, s[62:63]\n"
      "v_cvt_pk_bf16_f32 v154, v154, v155\n"
      "v_cvt_pk_bf16_f32 v89, v168, v169\n"
      "v_pk_mul_f32 v[190:191], v[190:191], v[178:179] op_sel_hi:[1,0]\n"
      "v_mfma_f32_16x16x32_bf16 v[174:177], v[126:129], a[220:223], v[140:143]\n"
      "s_waitcnt lgkmcnt(0)\n"
      "v_cvt_pk_bf16_f32 v124, v124, v125\n"
      "v_accvgpr_write_b32 a244, v154\n"
      "v_mfma_f32_16x16x32_bf16 v[140:143], v[126:129], a[252:255], v[150:153]\n"
      "s_cbranch_vccnz L_rare_9_%=\n"
      "L_back_10_%=:\n"
      "v_mfma_f32_16x16x32_bf16 v[160:163], v[126:129], v[240:243], v[144:147]\n"
      "v_cvt_pk_bf16_f32 v239, v104, v105\n"
      "v_cvt_pk_bf16_f32 v156, v156, v157\n"
      "v_mfma_f32_16x16x32_bf16 v[206:209], v[184:187], a[248:251], v[136:139]\n"
      "v_cvt_pk_bf16_f32 v237, v98, v99\n"
      "v_accvgpr_write_b32 a222, v164\n"
      "v_cvt_pk_bf16_f32 v174, v174, v175\n"
      "v_mfma_f32_16x16x32_bf16 v[218:221], v[184:187], v[240:243], v[136:139]\n"
      "ds_read_b128 v[202:205], v8 offset:576\n"
      "v_cvt_pk_bf16_f32 v238, v102, v103\n"
      "v_mfma_f32_16x16x32_bf16 v[168:171], v[184:187], a[224:227], v[136:139]\n"
      "ds_write_b128 v3, v[106:109]\n"
      "v_pk_mul_f32 v[210:211], v[210:211], v[178:179] op_sel_hi:[1,0]\n"
      "v_pk_mul_f32 v[180:181], v[180:181], v[178:179] op_sel_hi:[1,0]\n"
      "v_pk_mul_f32 v[182:183], v[182:183], v[178:179] op_sel_hi:[1,0]\n"
      "v_accvgpr_write_b32 a245, v156\n"
      "v_exp_f32_e64 v100, v100\n"
      "v_pk_mul_f32 v[192:193], v[192:193], v[178:179] op_sel_hi:[1,0]\n"
      "v_pk_mul_f32 v[212:213], v[212:213], v[178:179] op_sel_hi:[1,0]\n"
      "v_mfma_f32_16x16x32_bf16 v[106:109], v[126:129], a[248:251], v[144:147]\n"
      "v_cvt_pk_bf16_f32 v236, v96, v97\n"
      "v_cvt_pk_bf16_f32 v140, v140, v141\n"
      "v_mfma_f32_16x16x32_bf16 v[198:201], v[194:197], v[110:113], 0\n"
      "v_cvt_pk_bf16_f32 v176, v176, v177\n"
      "v_cvt_pk_bf16_f32 v100, v100, v100\n"
      "v_mfma_f32_16x16x32_bf16 v[198:201], v[80:83], v[88:91], v[198:201]\n"
      "v_cvt_pk_bf16_f32 v241, v162, v163\n"
      "v_cvt_pk_bf16_f32 v210, v210, v211\n"
      "v_mfma_f32_16x16x32_bf16 v[134:137], v[126:129], a[224:227], v[144:147]\n"
      "v_cvt_pk_bf16_f32 v240, v160, v161\n"
      "v_cvt_pk_bf16_f32 v243, v220, v221\n"
      "v_cvt_pk_bf16_f32 v122, v122, v123\n"
      "v_mfma_f32_16x16x32_bf16 v[144:147], v[184:187], a[252:255], v[114:117]\n"
      "ds_write_b16 v13, v100\n"
      "v_mfma_f32_16x16x32_bf16 v[158:161], v[184:187], v[244:247], v[114:117]\n"
      "v_cvt_pk_bf16_f32 v211, v212, v213\n"
      "v_accvgpr_write_b32 a221, v176\n"
      "v_accvgpr_write_b32 a247, v124\n"
      "v_cvt_pk_bf16_f32 v170, v170, v171\n"
      "v_cvt_pk_bf16_f32 v168, v168, v169\n"
      "v_mfma_f32_16x16x32_bf16 v[162:165], v[214:217], v[110:113], 0\n"
      "s_and_b32 s86, s86, -4\n"
      "v_accvgpr_write_b32 a252, v140\n"
      "v_cvt_pk_bf16_f32 v142, v142, v143\n"
      "v_mfma_f32_16x16x32_bf16 v[162:165], v[84:87], v[88:91], v[162:165]\n"
      "ds_read_b128 v[138:141], v8 offset:512\n"
      "v_accvgpr_write_b32 a246, v122\n"
      "v_mfma_f32_16x16x32_bf16 v[122:125], v[184:187], a[228:231], v[114:117]\n"
      "v_cvt_pk_bf16_f32 v106, v106, v107\n"
      "v_cvt_pk_bf16_f32 v146, v146, v147\n"
      "v_mfma_f32_16x16x32_bf16 v[176:179], v[32:35], v[110:113], 0\n"
      "ds_read_b128 v[98:101], v8 offset:704\n"
      "v_cvt_pk_bf16_f32 v208, v208, v209\n"
      "v_mfma_f32_16x16x32_bf16 v[176:179], v[32:35], v[88:91], v[176:179]\n"
      "v_accvgpr_write_b32 a227, v170\n"
      "v_cvt_pk_bf16_f32 v108, v108, v109\n"
      "v_accvgpr_write_b32 a223, v166\n"
      "v_mfma_f32_16x16x32_bf16 v[114:117], v[126:129], v[244:247], v[150:153]\n"
      "s_waitcnt lgkmcnt(2)\n"
      "v_cvt_pk_bf16_f32 v242, v218, v219\n"
      "v_cvt_pk_bf16_f32 v190, v190, v191\n"
      "v_cvt_pk_bf16_f32 v246, v158, v159\n"
      "v_accvgpr_write_b32 a251, v208\n"
      "v_accvgpr_write_b32 a220, v174\n"
      "v_cvt_pk_bf16_f32 v144, v144, v145\n"
      "v_mfma_f32_16x16x32_bf16 v[102:105], v[126:129], a[228:231], v[150:153]\n"
      "ds_read_b128 v[170:173], v8 offset:640\n"
      "v_rcp_f32 v176, v176\n"
      "v_mfma_f32_16x16x32_bf16 v[154:157], v[184:187], a[232:235], v[202:205]\n"
      "v_cvt_pk_bf16_f32 v122, v122, v123\n"
      "v_cvt_pk_bf16_f32 v206, v206, v207\n"
      "v_mfma_f32_16x16x32_bf16 v[150:153], v[184:187], v[224:227], v[202:205]\n"
      "ds_write_b64 v4, v[210:211] offset:256\n"
      "v_mfma_f32_16x16x32_bf16 v[220:223], v[130:133], v[110:113], 0\n"
      "s_waitcnt lgkmcnt(2)\n"
      "v_cvt_pk_bf16_f32 v191, v192, v193\n"
      "v_cvt_pk_bf16_f32 v104, v104, v105\n"
      "v_cvt_pk_bf16_f32 v244, v114, v115\n"
      "v_cvt_pk_bf16_f32 v102, v102, v103\n"
      "v_mfma_f32_16x16x32_bf16 v[220:223], v[76:79], v[88:91], v[220:223]\n"
      "s_add_u32 s43, s43, 1\n"
      "v_cvt_pk_bf16_f32 v124, v124, v125\n"
      "v_cvt_pk_bf16_f32 v156, v156, v157\n"
      "v_mfma_f32_16x16x32_bf16 v[110:113], v[184:187], a[236:239], v[98:101]\n"
      "s_waitcnt lgkmcnt(1)\n"
      "v_accvgpr_write_b32 a249, v108\n"
      "v_cvt_pk_bf16_f32 v192, v180, v181\n"
      "v_accvgpr_write_b32 a241, v118\n"
      "v_mfma_f32_16x16x32_bf16 v[118:121], v[126:129], a[232:235], v[138:141]\n"
      "ds_read_b128 v[210:213], v14 offset:64\n"
      "v_accvgpr_write_b32 a230, v122\n"
      "v_accvgpr_write_b32 a248, v106\n"
      "v_accvgpr_write_b32 a226, v168\n"
      "v_cvt_pk_bf16_f32 v245, v116, v117\n"
      "v_pk_mul_f32 v[162:163], v[162:163], v[176:177] op_sel_hi:[1,0]\n"
      "v_pk_mul_f32 v[222:223], v[222:223], v[176:177] op_sel_hi:[1,0]\n"
      "v_cvt_pk_bf16_f32 v193, v182, v183\n"
      "v_pk_mul_f32 v[200:201], v[200:201], v[176:177] op_sel_hi:[1,0]\n"
      "v_pk_mul_f32 v[164:165], v[164:165], v[176:177] op_sel_hi:[1,0]\n"
      "v_cvt_pk_bf16_f32 v247, v160, v161\n"
      "v_pk_mul_f32 v[198:199], v[198:199], v[176:177] op_sel_hi:[1,0]\n"
      "v_mfma_f32_16x16x32_bf16 v[180:183], v[126:129], a[236:239], v[170:173]\n"
      "v_cvt_pk_bf16_f32 v136, v136, v137\n"
      "v_cvt_pk_bf16_f32 v112, v112, v113\n"
      "v_cvt_pk_bf16_f32 v134, v134, v135\n"
      "v_cvt_pk_bf16_f32 v110, v110, v111\n"
      "v_mfma_f32_16x16x32_bf16 v[166:169], v[126:129], v[224:227], v[138:141]\n"
      "ds_write_b128 v3, v[190:193] offset:256\n"
      "v_cvt_pk_bf16_f32 v226, v150, v151\n"
      "v_accvgpr_write_b32 a231, v124\n"
      "v_pk_mul_f32 v[220:221], v[220:221], v[176:177] op_sel_hi:[1,0]\n"
      "v_mfma_f32_16x16x32_bf16 v[122:125], v[184:187], v[228:231], v[98:101]\n"
      "ds_read_b128 v[174:177], v14\n"
      "v_cvt_pk_bf16_f32 v162, v162, v163\n"
      "v_cvt_pk_bf16_f32 v120, v120, v121\n"
      "v_accvgpr_write_b32 a254, v144\n"
      "v_mfma_f32_16x16x32_bf16 v[190:193], v[126:129], v[248:251], v[138:141]\n"
      "v_cvt_pk_bf16_f32 v220, v220, v221\n"
      "v_cvt_pk_bf16_f32 v163, v164, v165\n"
      "v_accvgpr_write_b32 a225, v136\n"
      "v_mfma_f32_16x16x32_bf16 v[138:141], v[184:187], v[252:255], v[98:101]\n"
      "s_cmp_ge_u32 s95, 0x40000000\n"
      "s_cselect_b64 vcc, -1, s[56:57]\n"
      "v_cvt_pk_bf16_f32 v154, v154, v155\n"
      "v_cvt_pk_bf16_f32 v118, v118, v119\n"
      "v_accvgpr_write_b32 a235, v156\n"
      "v_accvgpr_write_b32 a224, v134\n"
      "v_mfma_f32_16x16x32_bf16 v[158:161], v[126:129], v[252:255], v[170:173]\n"
      "ds_write_b64 v4, v[162:163] offset:512\n"
      "v_cvt_pk_bf16_f32 v225, v168, v169\n"
      "v_accvgpr_write_b32 a250, v206\n"
      "v_cvt_pk_bf16_f32 v221, v222, v223\n"
      "v_mfma_f32_16x16x32_bf16 v[206:209], v[126:129], v[228:231], v[170:173]\n"
      "s_waitcnt lgkmcnt(1)\n"
      "v_cvt_pk_bf16_f32 v180, v180, v181\n"
      "v_cvt_pk_bf16_f32 v223, v200, v201\n"
      "v_accvgpr_write_b32 a233, v120\n"
      "v_cvt_pk_bf16_f32 v254, v138, v139\n"
      "v_cvt_pk_bf16_f32 v222, v198, v199\n"
      "v_mfma_f32_16x16x32_bf16 v[162:165], v[184:187], v[248:251], v[202:205]\n"
      "ds_write_b128 v3, v[220:223] offset:512\n"
      "v_accvgpr_write_b32 a239, v112\n"
      "v_cvt_pk_bf16_f32 v227, v152, v153\n"
      "v_accvgpr_write_b32 a236, v180\n"
      "v_accvgpr_write_b32 a253, v142\n"
      "v_cvt_pk_bf16_f32 v224, v166, v167\n"
      "v_accvgpr_write_b32 a238, v110\n"
      "v_accvgpr_write_b32 a232, v118\n"
      "v_cvt_pk_bf16_f32 v250, v162, v163\n"
      "v_accvgpr_write_b32 a255, v146\n"
      "v_cvt_pk_bf16_f32 v182, v182, v183\n"
      "v_cvt_pk_bf16_f32 v249, v192, v193\n"
      "v_cvt_pk_bf16_f32 v228, v206, v207\n"
      "v_cvt_pk_bf16_f32 v248, v190, v191\n"
      "v_cvt_pk_bf16_f32 v255, v140, v141\n"
      "v_cvt_pk_bf16_f32 v253, v160, v161\n"
      "v_accvgpr_write_b32 a229, v104\n"
      "v_cvt_pk_bf16_f32 v251, v164, v165\n"
      "v_accvgpr_write_b32 a237, v182\n"
      "v_cvt_pk_bf16_f32 v231, v124, v125\n"
      "v_accvgpr_write_b32 a234, v154\n"
      "v_accvgpr_write_b32 a228, v102\n"
      "v_mfma_f32_16x16x32_bf16 v[188:191], v[174:177], v[32:35], 0\n"
      "v_cvt_pk_bf16_f32 v230, v122, v123\n"
      "v_cvt_pk_bf16_f32 v252, v158, v159\n"
      "v_cvt_pk_bf16_f32 v229, v208, v209\n"
      "v_mfma_f32_16x16x32_bf16 v[188:191], v[210:213], v[32:35], v[188:191]\n"
      "s_waitcnt lgkmcnt(0)\n"
      "s_barrier\n"
      "s_or_b64 vcc, vcc, s[54:55]\n"
      "s_cbranch_vccnz L_disp1_%=\n"
      "s_nop 7\n"
      "s_nop 7\n"
      "L_loop1_%=:\n"
      "v_mfma_f32_16x16x32_bf16 v[218:221], v[174:177], v[130:133], 0\n"
      "ds_read_b128 v[206:209], v0 offset:18432\n"
      "v_readlane_b32 s45, v37, s43\n"
      "v_mfma_f32_16x16x32_bf16 v[218:221], v[210:213], v[76:79], v[218:221]\n"
      "ds_read_b128 v[122:125], v9\n"
      "ds_read_b128 v[146:149], v1 offset:30720\n"
      "v_mfma_f32_16x16x32_bf16 v[134:137], v[174:177], v[194:197], 0\n"
      "s_add_u32 m0, s64, 0\n"
      "ds_read_b128 v[150:153], v1 offset:18432\n"
      "v_mfma_f32_16x16x32_bf16 v[134:137], v[210:213], v[80:83], v[134:137]\n"
      "buffer_load_dwordx4 v16, s[16:19], s84 offen  lds\n"
      "v_mfma_f32_16x16x32_bf16 v[100:103], v[174:177], v[214:217], 0\n"
      "ds_read_b128 v[184:187], v9 offset:4608\n"
      "v_mfma_f32_16x16x32_bf16 v[100:103], v[210:213], v[84:87], v[100:103]\n"
      "ds_read_b128 v[202:205], v1 offset:24576\n"
      "s_waitcnt lgkmcnt(5)\n"
      "v_mfma_f32_16x16x32_bf16 v[154:157], a[0:3], v[206:209], 0\n"
      "ds_read_b128 v[176:179], v0 offset:24576\n"
      "s_add_u32 m0, s64, 4096\n"
      "v_mfma_f32_16x16x32_bf16 v[190:193], a[24:27], v[206:209], 0\n"
      "s_waitcnt lgkmcnt(5)\n"
      "v_cndmask_b32 v218, v218, v134, s[58:59]\n"
      "v_add_f32_e64 v122, v122, v123\n"
      "v_mfma_f32_16x16x32_bf16 v[96:99], a[48:51], v[206:209], 0\n"
      "buffer_load_dwordx4 v17, s[16:19], s84 offen  lds\n"
      "v_mfma_f32_16x16x32_bf16 v[112:115], a[72:75], v[206:209], 0\n"
      "ds_read_b128 v[210:213], v0 offset:34816\n"
      "v_mov_b32_dpp v188, v188 quad_perm:[0,0,0,0] row_mask:0xf bank_mask:0xf\n"
      "v_mfma_f32_16x16x32_bf16 v[220:223], a[96:99], v[206:209], 0\n"
      "s_waitcnt lgkmcnt(3)\n"
      "v_or3_b32 v184, v184, v185, v186\n"
      "v_add_f32 v124, v124, v125\n"
      "v_mfma_f32_16x16x32_bf16 v[102:105], a[120:123], v[206:209], 0\n"
      "s_add_u32 m0, s64, 8192\n"
      "v_readlane_b32 s47, v38, s43\n"
      "v_add_f32 v122, v122, v124\n"
      "v_mfma_f32_16x16x32_bf16 v[154:157], a[4:7], v[150:153], v[154:157]\n"
      "s_waitcnt lgkmcnt(1)\n"
      "v_rsq_f32_e64 v122, v122\n"
      "v_mfma_f32_16x16x32_bf16 v[154:157], a[8:11], v[176:179], v[154:157]\n"
      "v_mul_f32 v122, 0x415db3d7, v122\n"
      "v_mfma_f32_16x16x32_bf16 v[190:193], a[28:31], v[150:153], v[190:193]\n"
      "v_rcp_f32_e64 v188, v188\n"
      "v_mfma_f32_16x16x32_bf16 v[190:193], a[32:35], v[176:179], v[190:193]\n"
      "buffer_load_dwordx4 v18, s[16:19], s84 offen  lds\n"
      "v_mfma_f32_16x16x32_bf16 v[96:99], a[52:55], v[150:153], v[96:99]\n"
      "v_mov_b32_e64 v124, v122\n"
      "v_bitop3_b32 v184, v184, v187, s33 bitop3:0xa8\n"
      "v_mfma_f32_16x16x32_bf16 v[96:99], a[56:59], v[176:179], v[96:99]\n"
      "s_add_u32 m0, s64, 12288\n"
      "v_permlane32_swap_b32_e64 v122, v124\n"
      "v_mfma_f32_16x16x32_bf16 v[112:115], a[76:79], v[150:153], v[112:115]\n"
      "ds_read_b128 v[138:141], v0 offset:30720\n"
      "v_mov_b32 v123, v122\n"
      "v_mfma_f32_16x16x32_bf16 v[112:115], a[80:83], v[176:179], v[112:115]\n"
      "s_nop 0\n"
      "v_permlane16_swap_b32 v122, v123\n"
      "v_mfma_f32_16x16x32_bf16 v[220:223], a[100:103], v[150:153], v[220:223]\n"
      "ds_read_b128 v[158:161], v0 offset:32768\n"
      "v_mfma_f32_16x16x32_bf16 v[220:223], a[104:107], v[176:179], v[220:223]\n"
      "v_mul_f32 v118, v124, v118\n"
      "v_mul_f32 v142, v122, v142\n"
      "v_mfma_f32_16x16x32_bf16 v[102:105], a[124:127], v[150:153], v[102:105]\n"
      "v_readlane_b32 s46, v37, s42\n"
      "v_mul_f32 v171, v123, v171\n"
      "v_mfma_f32_16x16x32_bf16 v[102:105], a[128:131], v[176:179], v[102:105]\n"
      "v_mul_f32 v145, v122, v145\n"
      "v_cndmask_b32 v218, v218, v100, s[60:61]\n"
      "v_mfma_f32_16x16x32_bf16 v[154:157], a[12:15], v[202:205], v[154:157]\n"
      "s_waitcnt lgkmcnt(1)\n"
      "v_mul_f32 v162, v123, v162\n"
      "v_mul_f32 v129, v124, v129\n"
      "v_mfma_f32_16x16x32_bf16 v[154:157], a[16:19], v[138:141], v[154:157]\n"
      "buffer_load_dwordx4 v19, s[16:19], s84 offen  lds\n"
      "v_mfma_f32_16x16x32_bf16 v[154:157], a[20:23], v[146:149], v[154:157]\n"
      "v_mul_f32 v218, v188, v218\n"
      "v_mul_f32 v198, v122, v198\n"
      "v_mfma_f32_16x16x32_bf16 v[190:193], a[36:39], v[202:205], v[190:193]\n"
      "v_mul_f32 v163, v123, v163\n"
      "v_mul_f32 v165, v123, v165\n"
      "v_mfma_f32_16x16x32_bf16 v[190:193], a[40:43], v[138:141], v[190:193]\n"
      "s_add_u32 m0, s65, 16384\n"
      "v_cvt_pk_bf16_f32 v218, v218, v218\n"
      "v_mul_f32 v144, v122, v144\n"
      "v_mfma_f32_16x16x32_bf16 v[190:193], a[44:47], v[146:149], v[190:193]\n"
      "v_fma_f32 v77, v154, v154, s51\n"
      "v_mul_f32 v109, v124, v109\n"
      "v_mfma_f32_16x16x32_bf16 v[96:99], a[60:63], v[202:205], v[96:99]\n"
      "buffer_load_dwordx4 v15, s[16:19], s84 offen  lds\n"
      "v_mfma_f32_16x16x32_bf16 v[96:99], a[64:67], v[138:141], v[96:99]\n"
      "ds_write_b16 v12, v218\n"
      "v_mfma_f32_16x16x32_bf16 v[96:99], a[68:71], v[146:149], v[96:99]\n"
      "v_mul_f32 v166, v122, v166\n"
      "v_fmac_f32 v77, v155, v155\n"
      "v_mfma_f32_16x16x32_bf16 v[112:115], a[84:87], v[202:205], v[112:115]\n"
      "v_mul_f32 v143, v122, v143\n"
      "v_mul_f32 v182, v123, v182\n"
      "v_mfma_f32_16x16x32_bf16 v[112:115], a[88:91], v[138:141], v[112:115]\n"
      "v_mul_f32_e64 v164, v123, v164\n"
      "v_cvt_pk_bf16_f32 v142, v142, v143\n"
      "v_mfma_f32_16x16x32_bf16 v[112:115], a[92:95], v[146:149], v[112:115]\n"
      "buffer_load_dwordx4 v[134:137], v36, s[28:31], s86 offen\n"
      "v_mfma_f32_16x16x32_bf16 v[220:223], a[108:111], v[202:205], v[220:223]\n"
      "v_mul_f32 v167, v122, v167\n"
      "v_mul_f32 v110, v124, v110\n"
      "v_mfma_f32_16x16x32_bf16 v[220:223], a[112:115], v[138:141], v[220:223]\n"
      "v_mul_f32 v201, v122, v201\n"
      "v_mul_f32 v181, v123, v181\n"
      "v_mfma_f32_16x16x32_bf16 v[220:223], a[116:119], v[146:149], v[220:223]\n"
      "v_fma_f32 v106, v112, v112, s51\n"
      "v_mul_f32_e64 v180, v123, v180\n"
      "v_mfma_f32_16x16x32_bf16 v[102:105], a[132:135], v[202:205], v[102:105]\n"
      "v_cvt_pk_bf16_f32 v162, v162, v163\n"
      "v_fmac_f32_e64 v106, v113, v113\n"
      "v_mfma_f32_16x16x32_bf16 v[102:105], a[136:139], v[138:141], v[102:105]\n"
      "v_cvt_pk_bf16_f32 v143, v144, v145\n"
      "v_mul_f32 v119, v124, v119\n"
      "v_mfma_f32_16x16x32_bf16 v[102:105], a[140:143], v[146:149], v[102:105]\n"
      "v_fmac_f32 v106, v114, v114\n"
      "v_mul_f32_e64 v169, v122, v169\n"
      "v_mfma_f32_16x16x32_bf16 v[130:133], v[206:209], a[144:147], 0\n"
      "v_fmac_f32 v77, v156, v156\n"
      "v_mul_f32 v183, v123, v183\n"
      "v_mfma_f32_16x16x32_bf16 v[130:133], v[150:153], a[148:151], v[130:133]\n"
      "v_fmac_f32 v77, v157, v157\n"
      "v_fmac_f32 v77, v190, v190\n"
      "v_mfma_f32_16x16x32_bf16 v[130:133], v[176:179], a[152:155], v[130:133]\n"
      "ds_read_b128 v[186:189], v1 offset:26624\n"
      "v_fmac_f32_e64 v106, v115, v115\n"
      "v_mfma_f32_16x16x32_bf16 v[130:133], v[202:205], a[156:159], v[130:133]\n"
      "buffer_load_dword v88, v29, s[28:31], s86 offen\n"
      "v_mfma_f32_16x16x32_bf16 v[130:133], v[138:141], a[160:163], v[130:133]\n"
      "v_mul_f32 v170, v123, v170\n"
      "v_fmac_f32 v77, v191, v191\n"
      "v_mfma_f32_16x16x32_bf16 v[130:133], v[146:149], a[164:167], v[130:133]\n"
      "v_cvt_pk_bf16_f32 v166, v166, v167\n"
      "v_mul_f32_e64 v172, v123, v172\n"
      "v_mfma_f32_16x16x32_bf16 v[194:197], v[206:209], a[168:171], 0\n"
      "ds_write_b64 v6, v[142:143] offset:18432\n"
      "v_mfma_f32_16x16x32_bf16 v[194:197], v[150:153], a[172:175], v[194:197]\n"
      "v_cvt_pk_bf16_f32 v163, v164, v165\n"
      "v_fmac_f32 v106, v220, v220\n"
      "v_mfma_f32_16x16x32_bf16 v[194:197], v[176:179], a[176:179], v[194:197]\n"
      "v_fmac_f32 v106, v221, v221\n"
      "v_cmp_ne_u32 s[56:57], 0, v184\n"
      "v_mfma_f32_16x16x32_bf16 v[194:197], v[202:205], a[180:183], v[194:197]\n"
      "ds_read_b128 v[142:145], v0 offset:20480\n"
      "v_cvt_pk_bf16_f32 v164, v180, v181\n"
      "v_mfma_f32_16x16x32_bf16 v[194:197], v[138:141], a[184:187], v[194:197]\n"
      "v_fmac_f32 v106, v222, v222\n"
      "v_mul_f32 v168, v122, v168\n"
      "v_mfma_f32_16x16x32_bf16 v[194:197], v[146:149], a[188:191], v[194:197]\n"
      "v_cvt_pk_bf16_f32 v165, v182, v183\n"
      "v_fmac_f32 v106, v223, v223\n"
      "v_mfma_f32_16x16x32_bf16 v[214:217], v[206:209], a[192:195], 0\n"
      "ds_read_b128 v[182:185], v1 offset:20480\n"
      "v_mul_f32 v126, v124, v126\n"
      "v_mfma_f32_16x16x32_bf16 v[214:217], v[150:153], a[196:199], v[214:217]\n"
      "ds_read_b128 v[206:209], v0 offset:26624\n"
      "v_mul_f32 v200, v122, v200\n"
      "v_mfma_f32_16x16x32_bf16 v[214:217], v[176:179], a[200:203], v[214:217]\n"
      "s_or_b32 s95, s45, s46\n"
      "v_mul_f32 v199, v122, v199\n"
      "v_mul_f32 v117, v124, v117\n"
      "v_mfma_f32_16x16x32_bf16 v[214:217], v[202:205], a[204:207], v[214:217]\n"
      "ds_read_b128 v[174:177], v1 offset:32768\n"
      "v_cvt_pk_bf16_f32 v194, v194, v195\n"
      "v_mfma_f32_16x16x32_bf16 v[214:217], v[138:141], a[208:211], v[214:217]\n"
      "v_fmac_f32 v106, v102, v102\n"
      "v_cvt_pk_bf16_f32 v130, v130, v131\n"
      "v_mfma_f32_16x16x32_bf16 v[214:217], v[146:149], a[212:215], v[214:217]\n"
      "s_waitcnt lgkmcnt(3)\n"
      "v_mul_f32 v111, v124, v111\n"
      "v_mul_f32 v116, v124, v116\n"
      "v_mfma_f32_16x16x32_bf16 v[138:141], a[0:3], v[142:145], 0\n"
      "s_waitcnt lgkmcnt(2)\n"
      "v_mul_f32 v108, v124, v108\n"
      "v_mul_f32 v127, v124, v127\n"
      "v_mfma_f32_16x16x32_bf16 v[138:141], a[4:7], v[182:185], v[138:141]\n"
      "s_waitcnt lgkmcnt(1)\n"
      "v_mul_f32 v128, v124, v128\n"
      "v_cvt_pk_bf16_f32 v167, v168, v169\n"
      "v_mfma_f32_16x16x32_bf16 v[138:141], a[8:11], v[206:209], v[138:141]\n"
      "v_cvt_pk_bf16_f32 v108, v108, v109\n"
      "v_fmac_f32 v77, v192, v192\n"
      "v_mfma_f32_16x16x32_bf16 v[138:141], a[12:15], v[186:189], v[138:141]\n"
      "v_mul_f32 v173, v123, v173\n"
      "v_cvt_pk_bf16_f32 v168, v198, v199\n"
      "v_mfma_f32_16x16x32_bf16 v[138:141], a[16:19], v[158:161], v[138:141]\n"
      "s_waitcnt lgkmcnt(0)\n"
      "v_fmac_f32_e64 v77, v193, v193\n"
      "v_cvt_pk_bf16_f32 v126, v126, v127\n"
      "v_mfma_f32_16x16x32_bf16 v[138:141], a[20:23], v[174:177], v[138:141]\n"
      "ds_write_b128 v5, v[162:165] offset:24576\n"
      "v_mfma_f32_16x16x32_bf16 v[150:153], a[24:27], v[142:145], 0\n"
      "v_cvt_pk_bf16_f32 v169, v200, v201\n"
      "v_fmac_f32_e64 v106, v103, v103\n"
      "v_mfma_f32_16x16x32_bf16 v[150:153], a[28:31], v[182:185], v[150:153]\n"
      "ds_read_b128 v[162:165], v1 offset:22528\n"
      "v_fmac_f32_e64 v77, v96, v96\n"
      "v_mfma_f32_16x16x32_bf16 v[150:153], a[32:35], v[206:209], v[150:153]\n"
      "ds_write_b128 v5, v[166:169] offset:18432\n"
      "v_mfma_f32_16x16x32_bf16 v[150:153], a[36:39], v[186:189], v[150:153]\n"
      "v_cvt_pk_bf16_f32 v195, v196, v197\n"
      "v_fmac_f32_e64 v77, v97, v97\n"
      "v_mfma_f32_16x16x32_bf16 v[150:153], a[40:43], v[158:161], v[150:153]\n"
      "v_cvt_pk_bf16_f32 v127, v128, v129\n"
      "v_fma_f32 v120, v138, v138, s51\n"
      "v_mfma_f32_16x16x32_bf16 v[150:153], a[44:47], v[174:177], v[150:153]\n"
      "v_fmac_f32 v77, v98, v98\n"
      "v_fmac_f32 v120, v139, v139\n"
      "v_mfma_f32_16x16x32_bf16 v[122:125], a[48:51], v[142:145], 0\n"
      "v_fmac_f32_e64 v120, v140, v140\n"
      "v_cvt_pk_bf16_f32 v129, v118, v119\n"
      "v_mfma_f32_16x16x32_bf16 v[122:125], a[52:55], v[182:185], v[122:125]\n"
      "ds_read_b128 v[146:149], v0 offset:28672\n"
      "v_cvt_pk_bf16_f32 v128, v116, v117\n"
      "v_mfma_f32_16x16x32_bf16 v[122:125], a[56:59], v[206:209], v[122:125]\n"
      "v_fmac_f32 v106, v104, v104\n"
      "v_fmac_f32 v106, v105, v105\n"
      "v_mfma_f32_16x16x32_bf16 v[122:125], a[60:63], v[186:189], v[122:125]\n"
      "ds_write_b128 v5, v[126:129] offset:30720\n"
      "v_mfma_f32_16x16x32_bf16 v[122:125], a[64:67], v[158:161], v[122:125]\n"
      "v_fmac_f32 v120, v141, v141\n"
      "v_fmac_f32 v77, v99, v99\n"
      "v_mfma_f32_16x16x32_bf16 v[122:125], a[68:71], v[174:177], v[122:125]\n"
      "s_and_b32 s47, s47, -4\n"
      "v_permlane16_swap_b32 v77, v106\n"
      "v_mfma_f32_16x16x32_bf16 v[126:129], a[72:75], v[142:145], 0\n"
      "v_add_f32 v77, v77, v106\n"
      "v_fmac_f32 v120, v150, v150\n"
      "v_mfma_f32_16x16x32_bf16 v[126:129], a[76:79], v[182:185], v[126:129]\n"
      "v_cvt_pk_bf16_f32 v214, v214, v215\n"
      "v_mov_b32 v106, v77\n"
      "v_mfma_f32_16x16x32_bf16 v[126:129], a[80:83], v[206:209], v[126:129]\n"
      "s_nop 0\n"
      "v_permlane32_swap_b32 v77, v106\n"
      "v_mfma_f32_16x16x32_bf16 v[126:129], a[84:87], v[186:189], v[126:129]\n"
      "v_cvt_pk_bf16_f32 v131, v132, v133\n"
      "v_add_f32 v77, v77, v106\n"
      "v_mfma_f32_16x16x32_bf16 v[126:129], a[88:91], v[158:161], v[126:129]\n"
      "v_rsq_f32 v77, v77\n"
      "v_mfma_f32_16x16x32_bf16 v[126:129], a[92:95], v[174:177], v[126:129]\n"
      "v_mov_b32 v106, v77\n"
      "v_fmac_f32 v120, v151, v151\n"
      "v_mfma_f32_16x16x32_bf16 v[178:181], a[96:99], v[142:145], 0\n"
      "v_permlane16_swap_b32 v77, v106\n"
      "v_mfma_f32_16x16x32_bf16 v[178:181], a[100:103], v[182:185], v[178:181]\n"
      "v_cvt_pk_bf16_f32 v170, v170, v171\n"
      "v_mul_f32_e64 v77, s50, v77\n"
      "v_mfma_f32_16x16x32_bf16 v[178:181], a[104:107], v[206:209], v[178:181]\n"
      "v_fma_f32 v107, v126, v126, s51\n"
      "v_fmac_f32 v107, v127, v127\n"
      "v_mfma_f32_16x16x32_bf16 v[178:181], a[108:111], v[186:189], v[178:181]\n"
      "v_cvt_pk_bf16_f32 v109, v110, v111\n"
      "v_mul_f32 v115, v106, v115\n"
      "v_mfma_f32_16x16x32_bf16 v[178:181], a[112:115], v[158:161], v[178:181]\n"
      "v_mul_f32 v190, v77, v190\n"
      "v_fmac_f32 v120, v152, v152\n"
      "v_mfma_f32_16x16x32_bf16 v[178:181], a[116:119], v[174:177], v[178:181]\n"
      "v_cvt_pk_bf16_f32 v171, v172, v173\n"
      "v_mul_f32 v154, v77, v154\n"
      "v_mfma_f32_16x16x32_bf16 v[116:119], a[120:123], v[142:145], 0\n"
      "v_fmac_f32 v107, v128, v128\n"
      "v_fmac_f32 v107, v129, v129\n"
      "v_mfma_f32_16x16x32_bf16 v[116:119], a[124:127], v[182:185], v[116:119]\n"
      "v_mul_f32 v97, v77, v97\n"
      "v_cvt_pk_bf16_f32 v215, v216, v217\n"
      "v_mfma_f32_16x16x32_bf16 v[116:119], a[128:131], v[206:209], v[116:119]\n"
      "ds_write_b64 v6, v[108:109] offset:30720\n"
      "v_mfma_f32_16x16x32_bf16 v[116:119], a[132:135], v[186:189], v[116:119]\n"
      "v_fmac_f32 v107, v178, v178\n"
      "v_fmac_f32 v120, v153, v153\n"
      "v_mfma_f32_16x16x32_bf16 v[116:119], a[136:139], v[158:161], v[116:119]\n"
      "ds_read_b128 v[198:201], v1 offset:28672\n"
      "v_mul_f32_e64 v113, v106, v113\n"
      "v_mfma_f32_16x16x32_bf16 v[116:119], a[140:143], v[174:177], v[116:119]\n"
      "v_fmac_f32 v107, v179, v179\n"
      "v_mul_f32 v193, v77, v193\n"
      "v_mfma_f32_16x16x32_bf16 v[216:219], v[142:145], a[144:147], 0\n"
      "ds_write_b64 v6, v[170:171] offset:24576\n"
      "v_mfma_f32_16x16x32_bf16 v[216:219], v[182:185], a[148:151], v[216:219]\n"
      "v_mul_f32 v112, v106, v112\n"
      "v_fmac_f32 v107, v180, v180\n"
      "v_mfma_f32_16x16x32_bf16 v[216:219], v[206:209], a[152:155], v[216:219]\n"
      "v_mul_f32 v98, v77, v98\n"
      "v_fmac_f32 v107, v181, v181\n"
      "v_mfma_f32_16x16x32_bf16 v[216:219], v[186:189], a[156:159], v[216:219]\n"
      "v_fmac_f32 v120, v122, v122\n"
      "v_fmac_f32 v120, v123, v123\n"
      "v_mfma_f32_16x16x32_bf16 v[216:219], v[158:161], a[160:163], v[216:219]\n"
      "v_fmac_f32 v107, v116, v116\n"
      "v_fmac_f32 v107, v117, v117\n"
      "v_mfma_f32_16x16x32_bf16 v[216:219], v[174:177], a[164:167], v[216:219]\n"
      "v_fmac_f32 v107, v118, v118\n"
      "v_fmac_f32 v120, v124, v124\n"
      "v_mfma_f32_16x16x32_bf16 v[170:173], v[142:145], a[168:171], 0\n"
      "v_fmac_f32 v120, v125, v125\n"
      "v_fmac_f32 v107, v119, v119\n"
      "v_mfma_f32_16x16x32_bf16 v[170:173], v[182:185], a[172:175], v[170:173]\n"
      "s_or_b32 s95, s95, s48\n"
      "v_mul_f32 v156, v77, v156\n"
      "v_mul_f32 v155, v77, v155\n"
      "v_mfma_f32_16x16x32_bf16 v[170:173], v[206:209], a[176:179], v[170:173]\n"
      "v_mul_f32 v103, v106, v103\n"
      "v_mul_f32 v105, v106, v105\n"
      "v_mfma_f32_16x16x32_bf16 v[170:173], v[186:189], a[180:183], v[170:173]\n"
      "v_permlane16_swap_b32 v120, v107\n"
      "v_mfma_f32_16x16x32_bf16 v[170:173], v[158:161], a[184:187], v[170:173]\n"
      "v_add_f32 v120, v120, v107\n"
      "v_mov_b32 v107, v120\n"
      "v_mfma_f32_16x16x32_bf16 v[170:173], v[174:177], a[188:191], v[170:173]\n"
      "ds_read_b128 v[166:169], v0 offset:22528\n"
      "v_mul_f32_e64 v222, v106, v222\n"
      "v_mfma_f32_16x16x32_bf16 v[202:205], v[142:145], a[192:195], 0\n"
      "v_cvt_pk_bf16_f32 v154, v154, v155\n"
      "v_mfma_f32_16x16x32_bf16 v[202:205], v[182:185], a[196:199], v[202:205]\n"
      "v_permlane32_swap_b32 v120, v107\n"
      "v_mfma_f32_16x16x32_bf16 v[202:205], v[206:209], a[200:203], v[202:205]\n"
      "v_mul_f32 v157, v77, v157\n"
      "v_mul_f32 v192, v77, v192\n"
      "v_mfma_f32_16x16x32_bf16 v[202:205], v[186:189], a[204:207], v[202:205]\n"
      "ds_read_b128 v[142:145], v1 offset:34816\n"
      "v_add_f32 v120, v120, v107\n"
      "v_mfma_f32_16x16x32_bf16 v[202:205], v[158:161], a[208:211], v[202:205]\n"
      "v_rsq_f32 v120, v120\n"
      "v_mfma_f32_16x16x32_bf16 v[202:205], v[174:177], a[212:215], v[202:205]\n"
      "s_waitcnt lgkmcnt(1)\n"
      "v_cvt_pk_bf16_f32 v132, v216, v217\n"
      "v_mov_b32_e64 v107, v120\n"
      "v_mfma_f32_16x16x32_bf16 v[206:209], a[0:3], v[166:169], 0\n"
      "s_nop 0\n"
      "v_permlane16_swap_b32 v120, v107\n"
      "v_mfma_f32_16x16x32_bf16 v[206:209], a[4:7], v[162:165], v[206:209]\n"
      "v_mul_f32 v120, s50, v120\n"
      "v_mul_f32 v181, v107, v181\n"
      "v_mfma_f32_16x16x32_bf16 v[206:209], a[8:11], v[146:149], v[206:209]\n"
      "v_mul_f32 v126, v107, v126\n"
      "v_mul_f32 v127, v107, v127\n"
      "v_mfma_f32_16x16x32_bf16 v[206:209], a[12:15], v[198:201], v[206:209]\n"
      "v_mul_f32 v191, v77, v191\n"
      "v_mul_f32 v140, v120, v140\n"
      "v_mfma_f32_16x16x32_bf16 v[206:209], a[16:19], v[210:213], v[206:209]\n"
      "s_waitcnt lgkmcnt(0)\n"
      "v_mul_f32 v180, v107, v180\n"
      "v_mul_f32 v129, v107, v129\n"
      "v_mfma_f32_16x16x32_bf16 v[206:209], a[20:23], v[142:145], v[206:209]\n"
      "v_mul_f32 v151, v120, v151\n"
      "v_mul_f32 v114, v106, v114\n"
      "v_mfma_f32_16x16x32_bf16 v[182:185], a[24:27], v[166:169], 0\n"
      "v_mul_f32 v102, v106, v102\n"
      "v_mul_f32 v128, v107, v128\n"
      "v_mfma_f32_16x16x32_bf16 v[182:185], a[28:31], v[162:165], v[182:185]\n"
      "v_mul_f32 v139, v120, v139\n"
      "v_cvt_pk_bf16_f32 v126, v126, v127\n"
      "v_mfma_f32_16x16x32_bf16 v[182:185], a[32:35], v[146:149], v[182:185]\n"
      "v_mul_f32_e64 v123, v120, v123\n"
      "v_fma_f32 v76, v206, v206, s51\n"
      "v_mfma_f32_16x16x32_bf16 v[182:185], a[36:39], v[198:201], v[182:185]\n"
      "v_cvt_pk_bf16_f32 v196, v170, v171\n"
      "v_mul_f32 v122, v120, v122\n"
      "v_mfma_f32_16x16x32_bf16 v[182:185], a[40:43], v[210:213], v[182:185]\n"
      "v_mul_f32 v104, v106, v104\n"
      "v_fmac_f32 v76, v207, v207\n"
      "v_mfma_f32_16x16x32_bf16 v[182:185], a[44:47], v[142:145], v[182:185]\n"
      "v_mul_f32 v116, v107, v116\n"
      "v_cvt_pk_bf16_f32 v112, v112, v113\n"
      "v_mfma_f32_16x16x32_bf16 v[108:111], a[48:51], v[166:169], 0\n"
      "v_cvt_pk_bf16_f32 v113, v114, v115\n"
      "v_cvt_pk_bf16_f32 v122, v122, v123\n"
      "v_mfma_f32_16x16x32_bf16 v[108:111], a[52:55], v[162:165], v[108:111]\n"
      "v_mul_f32 v220, v106, v220\n"
      "v_mul_f32 v178, v107, v178\n"
      "v_mfma_f32_16x16x32_bf16 v[108:111], a[56:59], v[146:149], v[108:111]\n"
      "v_cvt_pk_bf16_f32 v127, v128, v129\n"
      "v_mul_f32 v223, v106, v223\n"
      "v_mfma_f32_16x16x32_bf16 v[108:111], a[60:63], v[198:201], v[108:111]\n"
      "s_cmp_ge_u32 s95, 0x40000000\n"
      "s_cselect_b32 s94, -1, -2\n"
      "v_cvt_pk_bf16_f32 v133, v218, v219\n"
      "v_mfma_f32_16x16x32_bf16 v[108:111], a[64:67], v[210:213], v[108:111]\n"
      "v_mul_f32 v117, v107, v117\n"
      "v_cvt_pk_bf16_f32 v155, v156, v157\n"
      "v_mfma_f32_16x16x32_bf16 v[108:111], a[68:71], v[142:145], v[108:111]\n"
      "v_cvt_pk_bf16_f32 v156, v190, v191\n"
      "v_mul_f32 v138, v120, v138\n"
      "v_mfma_f32_16x16x32_bf16 v[186:189], a[72:75], v[166:169], 0\n"
      "v_fmac_f32 v76, v208, v208\n"
      "v_mul_f32 v221, v106, v221\n"
      "v_mfma_f32_16x16x32_bf16 v[186:189], a[76:79], v[162:165], v[186:189]\n"
      "v_fmac_f32 v76, v209, v209\n"
      "v_fmac_f32_e64 v76, v182, v182\n"
      "v_mfma_f32_16x16x32_bf16 v[186:189], a[80:83], v[146:149], v[186:189]\n"
      "v_cvt_pk_bf16_f32 v102, v102, v103\n"
      "v_cvt_pk_bf16_f32 v138, v138, v139\n"
      "v_mfma_f32_16x16x32_bf16 v[186:189], a[84:87], v[198:201], v[186:189]\n"
      "v_mul_f32_e64 v96, v77, v96\n"
      "v_cvt_pk_bf16_f32 v116, v116, v117\n"
      "v_mfma_f32_16x16x32_bf16 v[186:189], a[88:91], v[210:213], v[186:189]\n"
      "v_mul_f32 v179, v107, v179\n"
      "v_mul_f32 v124, v120, v124\n"
      "v_mfma_f32_16x16x32_bf16 v[186:189], a[92:95], v[142:145], v[186:189]\n"
      "v_mul_f32 v150, v120, v150\n"
      "v_cvt_pk_bf16_f32 v129, v180, v181\n"
      "v_mfma_f32_16x16x32_bf16 v[158:161], a[96:99], v[166:169], 0\n"
      "v_cvt_pk_bf16_f32 v217, v204, v205\n"
      "v_fmac_f32 v76, v183, v183\n"
      "v_mfma_f32_16x16x32_bf16 v[158:161], a[100:103], v[162:165], v[158:161]\n"
      "v_cvt_pk_bf16_f32 v197, v172, v173\n"
      "v_fmac_f32_e64 v76, v184, v184\n"
      "v_mfma_f32_16x16x32_bf16 v[158:161], a[104:107], v[146:149], v[158:161]\n"
      "v_cvt_pk_bf16_f32 v114, v220, v221\n"
      "v_mul_f32_e64 v152, v120, v152\n"
      "v_mfma_f32_16x16x32_bf16 v[158:161], a[108:111], v[198:201], v[158:161]\n"
      "v_fma_f32 v205, v186, v186, s51\n"
      "v_cvt_pk_bf16_f32 v96, v96, v97\n"
      "v_mfma_f32_16x16x32_bf16 v[158:161], a[112:115], v[210:213], v[158:161]\n"
      "v_fmac_f32 v205, v187, v187\n"
      "v_fmac_f32 v205, v188, v188\n"
      "v_mfma_f32_16x16x32_bf16 v[158:161], a[116:119], v[142:145], v[158:161]\n"
      "v_fmac_f32_e64 v76, v185, v185\n"
      "v_cvt_pk_bf16_f32 v103, v104, v105\n"
      "v_mfma_f32_16x16x32_bf16 v[174:177], a[120:123], v[166:169], 0\n"
      "v_cvt_pk_bf16_f32 v128, v178, v179\n"
      "v_cvt_pk_bf16_f32 v115, v222, v223\n"
      "v_mfma_f32_16x16x32_bf16 v[174:177], a[124:127], v[162:165], v[174:177]\n"
      "v_mul_f32 v153, v120, v153\n"
      "v_mul_f32 v119, v107, v119\n"
      "v_mfma_f32_16x16x32_bf16 v[174:177], a[128:131], v[146:149], v[174:177]\n"
      "v_mul_f32 v118, v107, v118\n"
      "v_cvt_pk_bf16_f32 v117, v118, v119\n"
      "v_mfma_f32_16x16x32_bf16 v[174:177], a[132:135], v[198:201], v[174:177]\n"
      "s_cmp_gt_u32 s42, s44\n"
      "s_cselect_b32 s95, s94, s95\n"
      "v_fmac_f32 v76, v108, v108\n"
      "v_mfma_f32_16x16x32_bf16 v[174:177], a[136:139], v[210:213], v[174:177]\n"
      "v_cvt_pk_bf16_f32 v157, v192, v193\n"
      "v_mul_f32 v141, v120, v141\n"
      "v_mfma_f32_16x16x32_bf16 v[174:177], a[140:143], v[142:145], v[174:177]\n"
      "v_mul_f32 v125, v120, v125\n"
      "v_fmac_f32 v76, v109, v109\n"
      "v_mfma_f32_16x16x32_bf16 v[170:173], v[166:169], a[144:147], 0\n"
      "v_fmac_f32 v205, v189, v189\n"
      "v_fmac_f32 v205, v158, v158\n"
      "v_mfma_f32_16x16x32_bf16 v[170:173], v[162:165], a[148:151], v[170:173]\n"
      "ds_read_b128 v[104:107], v2 offset:6144\n"
      "v_fmac_f32 v205, v159, v159\n"
      "v_mfma_f32_16x16x32_bf16 v[170:173], v[146:149], a[152:155], v[170:173]\n"
      "v_fmac_f32 v205, v160, v160\n"
      "v_mul_f32 v99, v77, v99\n"
      "v_mfma_f32_16x16x32_bf16 v[170:173], v[198:201], a[156:159], v[170:173]\n"
      "v_fmac_f32 v205, v161, v161\n"
      "v_fmac_f32 v205, v174, v174\n"
      "v_mfma_f32_16x16x32_bf16 v[170:173], v[210:213], a[160:163], v[170:173]\n"
      "v_cvt_pk_bf16_f32 v97, v98, v99\n"
      "v_fmac_f32_e64 v76, v110, v110\n"
      "v_mfma_f32_16x16x32_bf16 v[170:173], v[142:145], a[164:167], v[170:173]\n"
      "v_fmac_f32 v205, v175, v175\n"
      "v_fmac_f32 v76, v111, v111\n"
      "v_mfma_f32_16x16x32_bf16 v[218:221], v[166:169], a[168:171], 0\n"
      "v_fmac_f32 v205, v176, v176\n"
      "v_fmac_f32 v205, v177, v177\n"
      "v_mfma_f32_16x16x32_bf16 v[218:221], v[162:165], a[172:175], v[218:221]\n"
      "s_nop 0\n"
      "v_permlane16_swap_b32 v76, v205\n"
      "v_mfma_f32_16x16x32_bf16 v[218:221], v[146:149], a[176:179], v[218:221]\n"
      "v_add_f32 v76, v76, v205\n"
      "v_mov_b32 v205, v76\n"
      "v_mfma_f32_16x16x32_bf16 v[218:221], v[198:201], a[180:183], v[218:221]\n"
      "ds_read_b128 v[118:121], v2 offset:3072\n"
      "v_cvt_pk_bf16_f32 v77, v172, v173\n"
      "v_mfma_f32_16x16x32_bf16 v[218:221], v[210:213], a[184:187], v[218:221]\n"
      "v_permlane32_swap_b32 v76, v205\n"
      "v_mfma_f32_16x16x32_bf16 v[218:221], v[142:145], a[188:191], v[218:221]\n"
      "ds_read_b128 v[178:181], v2\n"
      "v_add_f32 v76, v76, v205\n"
      "v_mfma_f32_16x16x32_bf16 v[190:193], v[166:169], a[192:195], 0\n"
      "v_rsq_f32 v76, v76\n"
      "v_mfma_f32_16x16x32_bf16 v[190:193], v[162:165], a[196:199], v[190:193]\n"
      "ds_read_b128 v[162:165], v2 offset:9216\n"
      "v_mov_b32 v205, v76\n"
      "v_mfma_f32_16x16x32_bf16 v[190:193], v[146:149], a[200:203], v[190:193]\n"
      "s_nop 0\n"
      "v_permlane16_swap_b32 v76, v205\n"
      "v_mfma_f32_16x16x32_bf16 v[190:193], v[198:201], a[204:207], v[190:193]\n"
      "v_mul_f32_e64 v76, s50, v76\n"
      "v_cvt_pk_bf16_f32 v139, v140, v141\n"
      "v_mfma_f32_16x16x32_bf16 v[190:193], v[210:213], a[208:211], v[190:193]\n"
      "ds_read_b128 v[210:213], v2 offset:12288\n"
      "v_mul_f32 v207, v76, v207\n"
      "v_mfma_f32_16x16x32_bf16 v[190:193], v[142:145], a[212:215], v[190:193]\n"
      "s_waitcnt lgkmcnt(2)\n"
      "v_mul_f32 v176, v205, v176\n"
      "v_cvt_pk_bf16_f32 v140, v150, v151\n"
      "v_mfma_f32_16x16x32_bf16 v[166:169], a[216:219], v[178:181], 0\n"
      "v_mul_f32 v160, v205, v160\n"
      "v_cvt_pk_bf16_f32 v80, v218, v219\n"
      "v_mfma_f32_16x16x32_bf16 v[166:169], a[220:223], v[118:121], v[166:169]\n"
      "v_mul_f32 v161, v205, v161\n"
      "v_mul_f32 v183, v76, v183\n"
      "v_mfma_f32_16x16x32_bf16 v[166:169], a[224:227], v[104:107], v[166:169]\n"
      "ds_read_b128 v[146:149], v2 offset:15360\n"
      "v_cvt_pk_bf16_f32 v216, v202, v203\n"
      "v_mfma_f32_16x16x32_bf16 v[198:201], a[240:243], v[178:181], 0\n"
      "v_mul_f32 v186, v205, v186\n"
      "v_mul_f32 v108, v76, v108\n"
      "v_mfma_f32_16x16x32_bf16 v[198:201], a[244:247], v[118:121], v[198:201]\n"
      "v_cvt_pk_bf16_f32 v123, v124, v125\n"
      "v_mul_f32 v110, v76, v110\n"
      "v_mfma_f32_16x16x32_bf16 v[198:201], a[248:251], v[104:107], v[198:201]\n"
      "ds_read_b128 v[98:101], v2 offset:6400\n"
      "v_mul_f32 v185, v76, v185\n"
      "v_mfma_f32_16x16x32_bf16 v[142:145], v[232:235], v[178:181], 0\n"
      "ds_read_b64 v[150:151], v10 offset:1192\n"
      "v_mul_f32 v187, v205, v187\n"
      "v_mfma_f32_16x16x32_bf16 v[142:145], v[236:239], v[118:121], v[142:145]\n"
      "v_mul_f32 v209, v76, v209\n"
      "v_mul_f32 v174, v205, v174\n"
      "v_mfma_f32_16x16x32_bf16 v[142:145], v[240:243], v[104:107], v[142:145]\n"
      "s_waitcnt lgkmcnt(2)\n"
      "v_mul_f32 v177, v205, v177\n"
      "v_mul_f32 v109, v76, v109\n"
      "v_mfma_f32_16x16x32_bf16 v[166:169], a[228:231], v[162:165], v[166:169]\n"
      "v_cvt_pk_bf16_f32 v84, v190, v191\n"
      "v_mul_f32 v159, v205, v159\n"
      "v_mfma_f32_16x16x32_bf16 v[166:169], a[232:235], v[210:213], v[166:169]\n"
      "v_mul_f32 v189, v205, v189\n"
      "v_cvt_pk_bf16_f32 v108, v108, v109\n"
      "v_mfma_f32_16x16x32_bf16 v[166:169], a[236:239], v[146:149], v[166:169]\n"
      "ds_read_b128 v[104:107], v2 offset:256\n"
      "v_mul_f32_e64 v206, v76, v206\n"
      "v_mfma_f32_16x16x32_bf16 v[198:201], a[252:255], v[162:165], v[198:201]\n"
      "ds_read_b128 v[118:121], v10 offset:1200\n"
      "v_mul_f32_e64 v158, v205, v158\n"
      "v_mfma_f32_16x16x32_bf16 v[198:201], v[224:227], v[210:213], v[198:201]\n"
      "v_cvt_pk_bf16_f32 v206, v206, v207\n"
      "v_cvt_pk_bf16_f32 v186, v186, v187\n"
      "v_mfma_f32_16x16x32_bf16 v[198:201], v[228:231], v[146:149], v[198:201]\n"
      "v_mul_f32_e64 v188, v205, v188\n"
      "v_cvt_pk_bf16_f32 v187, v188, v189\n"
      "v_mfma_f32_16x16x32_bf16 v[142:145], v[244:247], v[162:165], v[142:145]\n"
      "v_cvt_pk_bf16_f32 v189, v160, v161\n"
      "v_mul_f32_e64 v175, v205, v175\n"
      "v_mfma_f32_16x16x32_bf16 v[142:145], v[248:251], v[210:213], v[142:145]\n"
      "ds_read_b128 v[210:213], v2 offset:3328\n"
      "v_mul_f32 v208, v76, v208\n"
      "v_mfma_f32_16x16x32_bf16 v[142:145], v[252:255], v[146:149], v[142:145]\n"
      "s_add_u32 s43, s43, 1\n"
      "v_cvt_pk_bf16_f32 v188, v158, v159\n"
      "v_fma_f32 v222, v166, v166, s52\n"
      "v_mfma_f32_16x16x32_bf16 v[146:149], v[112:115], v[154:157], v[40:43]\n"
      "ds_read_u16 v82, v11 offset:1200\n"
      "v_cvt_pk_bf16_f32 v174, v174, v175\n"
      "v_mfma_f32_16x16x32_bf16 v[158:161], v[126:129], v[154:157], v[44:47]\n"
      "s_waitcnt lgkmcnt(2)\n"
      "v_cvt_pk_bf16_f32 v175, v176, v177\n"
      "v_mul_f32 v182, v76, v182\n"
      "v_mfma_f32_16x16x32_bf16 v[176:179], v[186:189], v[154:157], v[48:51]\n"
      "ds_read_u16 v86, v11 offset:1232\n"
      "v_mul_f32 v184, v76, v184\n"
      "v_mfma_f32_16x16x32_bf16 v[202:205], v[118:121], v[154:157], v[24:27]\n"
      "v_mul_f32 v111, v76, v111\n"
      "v_cvt_pk_bf16_f32 v207, v208, v209\n"
      "v_mfma_f32_16x16x16_bf16 v[146:149], v[102:103], v[96:97], v[146:149]\n"
      "v_cvt_pk_bf16_f32 v109, v110, v111\n"
      "v_cvt_pk_bf16_f32 v141, v152, v153\n"
      "v_cvt_pk_bf16_f32 v209, v184, v185\n"
      "v_mfma_f32_16x16x16_bf16 v[158:161], v[116:117], v[96:97], v[158:161]\n"
      "v_mul_f32_e64 v223, v167, v167\n"
      "v_cvt_pk_bf16_f32 v208, v182, v183\n"
      "v_cvt_pk_bf16_f32 v76, v170, v171\n"
      "v_mfma_f32_16x16x16_bf16 v[176:179], v[174:175], v[96:97], v[176:179]\n"
      "v_exp_f32_e64 v148, v148\n"
      "v_cvt_pk_bf16_f32 v81, v220, v221\n"
      "v_mfma_f32_16x16x16_bf16 v[202:205], v[150:151], v[96:97], v[202:205]\n"
      "ds_read_b128 v[152:155], v2 offset:9472\n"
      "s_waitcnt vmcnt(1)\n"
      "v_or3_b32 v134, v134, v135, v136\n"
      "v_mfma_f32_16x16x32_bf16 v[162:165], a[216:219], v[104:107], 0\n"
      "s_waitcnt lgkmcnt(1)\n"
      "v_exp_f32 v159, v159\n"
      "v_mfma_f32_16x16x32_bf16 v[162:165], a[220:223], v[210:213], v[162:165]\n"
      "v_exp_f32 v179, v179\n"
      "v_mfma_f32_16x16x32_bf16 v[162:165], a[224:227], v[98:101], v[162:165]\n"
      "ds_read_u16 v78, v11 offset:1168\n"
      "v_cvt_pk_bf16_f32 v85, v192, v193\n"
      "v_mfma_f32_16x16x32_bf16 v[180:183], a[240:243], v[104:107], 0\n"
      "v_exp_f32 v202, v202\n"
      "v_mfma_f32_16x16x32_bf16 v[180:183], a[244:247], v[210:213], v[180:183]\n"
      "v_exp_f32 v146, v146\n"
      "v_mfma_f32_16x16x32_bf16 v[180:183], a[248:251], v[98:101], v[180:183]\n"
      "v_exp_f32 v158, v158\n"
      "v_mfma_f32_16x16x32_bf16 v[170:173], v[232:235], v[104:107], 0\n"
      "v_exp_f32 v161, v161\n"
      "v_mfma_f32_16x16x32_bf16 v[170:173], v[236:239], v[210:213], v[170:173]\n"
      "v_exp_f32 v160, v160\n"
      "v_mfma_f32_16x16x32_bf16 v[170:173], v[240:243], v[98:101], v[170:173]\n"
      "v_exp_f32 v147, v147\n"
      "v_mfma_f32_16x16x32_bf16 v[210:213], v[112:115], v[138:141], v[52:55]\n"
      "v_exp_f32_e64 v149, v149\n"
      "v_mfma_f32_16x16x32_bf16 v[190:193], v[126:129], v[138:141], v[56:59]\n"
      "v_exp_f32 v176, v176\n"
      "v_mfma_f32_16x16x32_bf16 v[104:107], v[186:189], v[138:141], v[60:63]\n"
      "v_exp_f32 v177, v177\n"
      "v_mfma_f32_16x16x32_bf16 v[98:101], v[118:121], v[138:141], v[26:29]\n"
      "v_exp_f32 v178, v178\n"
      "v_mfma_f32_16x16x16_bf16 v[210:213], v[102:103], v[122:123], v[210:213]\n"
      "s_waitcnt vmcnt(0)\n"
      "v_or3_b32 v134, v134, v137, v88\n"
      "v_cvt_pk_bf16_f32 v88, v176, v177\n"
      "v_cvt_pk_bf16_f32 v146, v146, v147\n"
      "v_mfma_f32_16x16x16_bf16 v[190:193], v[116:117], v[122:123], v[190:193]\n"
      "v_cvt_pk_bf16_f32 v147, v148, v149\n"
      "v_cvt_pk_bf16_f32 v149, v160, v161\n"
      "v_cvt_pk_bf16_f32 v89, v178, v179\n"
      "v_mfma_f32_16x16x16_bf16 v[104:107], v[174:175], v[122:123], v[104:107]\n"
      "v_exp_f32_e64 v213, v213\n"
      "v_cvt_pk_bf16_f32 v148, v158, v159\n"
      "v_mfma_f32_16x16x16_bf16 v[98:101], v[150:151], v[122:123], v[98:101]\n"
      "v_cvt_pk_bf16_f32 v90, v202, 0\n"
      "v_exp_f32 v193, v193\n"
      "v_mfma_f32_16x16x32_bf16 v[176:179], v[32:35], v[146:149], 0\n"
      "v_exp_f32 v210, v210\n"
      "v_mfma_f32_16x16x32_bf16 v[176:179], v[32:35], v[88:91], v[176:179]\n"
      "v_exp_f32 v106, v106\n"
      "v_exp_f32 v191, v191\n"
      "v_mfma_f32_16x16x32_bf16 v[136:139], v[130:133], v[146:149], 0\n"
      "s_waitcnt lgkmcnt(0)\n"
      "v_exp_f32 v212, v212\n"
      "v_mfma_f32_16x16x32_bf16 v[136:139], v[76:79], v[88:91], v[136:139]\n"
      "v_exp_f32 v211, v211\n"
      "v_mfma_f32_16x16x32_bf16 v[202:205], v[194:197], v[146:149], 0\n"
      "v_exp_f32 v190, v190\n"
      "v_mfma_f32_16x16x32_bf16 v[202:205], v[80:83], v[88:91], v[202:205]\n"
      "ds_write_b32 v31, v134 offset:5632\n"
      "v_mfma_f32_16x16x32_bf16 v[156:159], v[214:217], v[146:149], 0\n"
      "v_rcp_f32_e64 v176, v176\n"
      "v_cvt_pk_bf16_f32 v210, v210, v211\n"
      "v_mfma_f32_16x16x32_bf16 v[156:159], v[84:87], v[88:91], v[156:159]\n"
      "v_exp_f32_e64 v192, v192\n"
      "v_mfma_f32_16x16x32_bf16 v[146:149], v[112:115], v[206:209], v[64:67]\n"
      "v_cvt_pk_bf16_f32 v211, v212, v213\n"
      "v_cvt_pk_bf16_f32 v213, v192, v193\n"
      "v_exp_f32_e64 v105, v105\n"
      "v_cvt_pk_bf16_f32 v212, v190, v191\n"
      "v_pk_fma_f32 v[222:223], v[168:169], v[168:169], v[222:223]\n"
      "v_pk_mul_f32 v[136:137], v[136:137], v[176:177] op_sel_hi:[1,0]\n"
      "v_pk_mul_f32 v[158:159], v[158:159], v[176:177] op_sel_hi:[1,0]\n"
      "v_pk_mul_f32 v[204:205], v[204:205], v[176:177] op_sel_hi:[1,0]\n"
      "v_pk_mul_f32 v[138:139], v[138:139], v[176:177] op_sel_hi:[1,0]\n"
      "v_pk_mul_f32 v[202:203], v[202:203], v[176:177] op_sel_hi:[1,0]\n"
      "v_pk_mul_f32 v[156:157], v[156:157], v[176:177] op_sel_hi:[1,0]\n"
      "v_pk_fma_f32 v[222:223], v[198:199], v[198:199], v[222:223]\n"
      "v_mfma_f32_16x16x32_bf16 v[190:193], v[126:129], v[206:209], v[68:71]\n"
      "v_exp_f32_e64 v107, v107\n"
      "v_mfma_f32_16x16x32_bf16 v[176:179], v[186:189], v[206:209], v[72:75]\n"
      "ds_read_b128 v[122:125], v2 offset:12544\n"
      "v_cvt_pk_bf16_f32 v93, v106, v107\n"
      "v_mfma_f32_16x16x32_bf16 v[218:221], v[118:121], v[206:209], v[28:31]\n"
      "ds_read_b128 v[206:209], v2 offset:15616\n"
      "v_cvt_pk_bf16_f32 v156, v156, v157\n"
      "v_mfma_f32_16x16x16_bf16 v[146:149], v[102:103], v[108:109], v[146:149]\n"
      "v_exp_f32_e64 v104, v104\n"
      "v_cvt_pk_bf16_f32 v136, v136, v137\n"
      "v_mfma_f32_16x16x16_bf16 v[190:193], v[116:117], v[108:109], v[190:193]\n"
      "v_cvt_pk_bf16_f32 v137, v138, v139\n"
      "v_cvt_pk_bf16_f32 v138, v202, v203\n"
      "v_cvt_pk_bf16_f32 v139, v204, v205\n"
      "v_mfma_f32_16x16x16_bf16 v[176:179], v[174:175], v[108:109], v[176:179]\n"
      "v_cvt_pk_bf16_f32 v157, v158, v159\n"
      "v_exp_f32 v98, v98\n"
      "v_mfma_f32_16x16x16_bf16 v[218:221], v[150:151], v[108:109], v[218:221]\n"
      "v_cvt_pk_bf16_f32 v92, v104, v105\n"
      "v_exp_f32 v193, v193\n"
      "v_mfma_f32_16x16x32_bf16 v[162:165], a[228:231], v[152:155], v[162:165]\n"
      "s_waitcnt lgkmcnt(1)\n"
      "v_exp_f32 v148, v148\n"
      "v_mfma_f32_16x16x32_bf16 v[162:165], a[232:235], v[122:125], v[162:165]\n"
      "s_waitcnt lgkmcnt(0)\n"
      "v_exp_f32 v149, v149\n"
      "v_mfma_f32_16x16x32_bf16 v[162:165], a[236:239], v[206:209], v[162:165]\n"
      "v_exp_f32 v147, v147\n"
      "v_mfma_f32_16x16x32_bf16 v[180:183], a[252:255], v[152:155], v[180:183]\n"
      "v_exp_f32 v146, v146\n"
      "v_mfma_f32_16x16x32_bf16 v[180:183], v[224:227], v[122:125], v[180:183]\n"
      "ds_read_b128 v[106:109], v10 offset:1168\n"
      "v_cvt_pk_bf16_f32 v146, v146, v147\n"
      "v_mfma_f32_16x16x32_bf16 v[180:183], v[228:231], v[206:209], v[180:183]\n"
      "v_exp_f32 v218, v218\n"
      "v_mfma_f32_16x16x32_bf16 v[170:173], v[244:247], v[152:155], v[170:173]\n"
      "s_add_u32 s42, s42, 1\n"
      "v_exp_f32_e64 v176, v176\n"
      "v_mfma_f32_16x16x32_bf16 v[170:173], v[248:251], v[122:125], v[170:173]\n"
      "ds_read_b128 v[202:205], v7\n"
      "v_cvt_pk_bf16_f32 v94, v98, 0\n"
      "v_mfma_f32_16x16x32_bf16 v[170:173], v[252:255], v[206:209], v[170:173]\n"
      "v_exp_f32 v179, v179\n"
      "v_mfma_f32_16x16x32_bf16 v[98:101], v[32:35], v[210:213], 0\n"
      "v_exp_f32 v178, v178\n"
      "v_mfma_f32_16x16x32_bf16 v[98:101], v[32:35], v[92:95], v[98:101]\n"
      "v_exp_f32_e64 v192, v192\n"
      "v_mfma_f32_16x16x32_bf16 v[152:155], v[130:133], v[210:213], 0\n"
      "ds_write_b128 v3, v[136:139] offset:18432\n"
      "v_pk_fma_f32 v[222:223], v[200:201], v[200:201], v[222:223]\n"
      "v_mfma_f32_16x16x32_bf16 v[152:155], v[76:79], v[92:95], v[152:155]\n"
      "ds_read_b64 v[220:221], v10 offset:1184\n"
      "v_cvt_pk_bf16_f32 v147, v148, v149\n"
      "v_mfma_f32_16x16x32_bf16 v[134:137], v[194:197], v[210:213], 0\n"
      "ds_read_b128 v[206:209], v7 offset:4096\n"
      "v_cvt_pk_bf16_f32 v89, v178, v179\n"
      "v_mfma_f32_16x16x32_bf16 v[134:137], v[80:83], v[92:95], v[134:137]\n"
      "s_waitcnt lgkmcnt(3)\n"
      "v_rcp_f32 v98, v98\n"
      "v_mfma_f32_16x16x32_bf16 v[158:161], v[214:217], v[210:213], 0\n"
      "buffer_store_dwordx4 v[202:205], v20, s[24:27], s85 offen sc1\n"
      "v_pk_mul_f32 v[154:155], v[154:155], v[98:99] op_sel_hi:[1,0]\n"
      "v_pk_mul_f32 v[152:153], v[152:153], v[98:99] op_sel_hi:[1,0]\n"
      "v_mfma_f32_16x16x32_bf16 v[158:161], v[84:87], v[92:95], v[158:161]\n"
      "v_exp_f32 v177, v177\n"
      "v_mfma_f32_16x16x32_bf16 v[210:213], v[106:109], v[112:115], v[30:33]\n"
      "ds_read_b128 v[202:205], v2 offset:6656\n"
      "v_cvt_pk_bf16_f32 v149, v192, v193\n"
      "v_mfma_f32_16x16x32_bf16 v[112:115], v[106:109], v[126:129], v[30:33]\n"
      "s_waitcnt lgkmcnt(1)\n"
      "v_cvt_pk_bf16_f32 v88, v176, v177\n"
      "v_cvt_pk_bf16_f32 v90, v218, 0\n"
      "v_mfma_f32_16x16x32_bf16 v[124:127], v[106:109], v[186:189], v[30:33]\n"
      "ds_read_b128 v[186:189], v2 offset:512\n"
      "v_cvt_pk_bf16_f32 v152, v152, v153\n"
      "v_mfma_f32_16x16x32_bf16 v[176:179], v[106:109], v[118:121], v[30:33]\n"
      "ds_read_b128 v[120:123], v2 offset:3584\n"
      "v_mul_f32_e64 v139, v163, v163\n"
      "v_mfma_f32_16x16x16_bf16 v[210:213], v[220:221], v[102:103], v[210:213]\n"
      "v_cvt_pk_bf16_f32 v153, v154, v155\n"
      "v_exp_f32_e64 v191, v191\n"
      "v_mfma_f32_16x16x16_bf16 v[112:115], v[220:221], v[116:117], v[112:115]\n"
      "ds_read_b128 v[100:103], v7 offset:8192\n"
      "v_fma_f32 v138, v162, v162, s52\n"
      "v_mfma_f32_16x16x16_bf16 v[124:127], v[220:221], v[174:175], v[124:127]\n"
      "buffer_store_dwordx4 v[206:209], v21, s[24:27], s85 offen sc1\n"
      "v_pk_fma_f32 v[138:139], v[164:165], v[164:165], v[138:139]\n"
      "v_pk_fma_f32 v[222:223], v[142:143], v[142:143], v[222:223]\n"
      "v_pk_mul_f32 v[158:159], v[158:159], v[98:99] op_sel_hi:[1,0]\n"
      "v_pk_mul_f32 v[136:137], v[136:137], v[98:99] op_sel_hi:[1,0]\n"
      "v_pk_mul_f32 v[134:135], v[134:135], v[98:99] op_sel_hi:[1,0]\n"
      "v_pk_fma_f32 v[138:139], v[180:181], v[180:181], v[138:139]\n"
      "v_pk_fma_f32 v[138:139], v[182:183], v[182:183], v[138:139]\n"
      "v_mfma_f32_16x16x16_bf16 v[176:179], v[220:221], v[150:151], v[176:179]\n"
      "v_cvt_pk_bf16_f32 v155, v136, v137\n"
      "v_cvt_pk_bf16_f32 v154, v134, v135\n"
      "s_waitcnt lgkmcnt(1)\n"
      "v_mfma_f32_16x16x32_bf16 v[126:129], a[216:219], v[186:189], 0\n"
      "ds_write_b128 v3, v[152:155] offset:18688\n"
      "v_pk_fma_f32 v[138:139], v[170:171], v[170:171], v[138:139]\n"
      "v_mfma_f32_16x16x32_bf16 v[126:129], a[220:223], v[120:123], v[126:129]\n"
      "ds_read_b128 v[134:137], v2 offset:12800\n"
      "v_cndmask_b32 v210, v210, v112, s[58:59]\n"
      "v_mfma_f32_16x16x32_bf16 v[126:129], a[224:227], v[202:205], v[126:129]\n"
      "v_exp_f32_e64 v190, v190\n"
      "v_mfma_f32_16x16x32_bf16 v[116:119], a[240:243], v[186:189], 0\n"
      "s_waitcnt lgkmcnt(2)\n"
      "v_cvt_pk_bf16_f32 v148, v190, v191\n"
      "v_cndmask_b32 v210, v210, v124, s[60:61]\n"
      "v_mfma_f32_16x16x32_bf16 v[116:119], a[244:247], v[120:123], v[116:119]\n"
      "ds_read_b128 v[190:193], v2 offset:15872\n"
      "v_mfma_f32_16x16x32_bf16 v[116:119], a[248:251], v[202:205], v[116:119]\n"
      "ds_read_b128 v[218:221], v7 offset:12288\n"
      "v_cndmask_b32 v210, v210, v176, s[62:63]\n"
      "v_mfma_f32_16x16x32_bf16 v[108:111], v[232:235], v[186:189], 0\n"
      "buffer_store_dwordx4 v[100:103], v22, s[24:27], s85 offen sc1\n"
      "v_mfma_f32_16x16x32_bf16 v[108:111], v[236:239], v[120:123], v[108:111]\n"
      "v_exp_f32_e64 v210, v210\n"
      "v_mfma_f32_16x16x32_bf16 v[108:111], v[240:243], v[202:205], v[108:111]\n"
      "ds_write_b64 v4, v[156:157] offset:18432\n"
      "v_mfma_f32_16x16x32_bf16 v[100:103], v[32:35], v[146:149], 0\n"
      "ds_read_b128 v[186:189], v2 offset:9728\n"
      "v_fmac_f32_e64 v139, v173, v173\n"
      "v_mfma_f32_16x16x32_bf16 v[100:103], v[32:35], v[88:91], v[100:103]\n"
      "v_cvt_pk_bf16_f32 v158, v158, v159\n"
      "v_cvt_pk_bf16_f32 v210, v210, v210\n"
      "v_mfma_f32_16x16x32_bf16 v[112:115], v[130:133], v[146:149], 0\n"
      "ds_write_b16 v13, v210\n"
      "v_mfma_f32_16x16x32_bf16 v[112:115], v[76:79], v[88:91], v[112:115]\n"
      "ds_read_b128 v[174:177], v14\n"
      "v_fmac_f32 v138, v172, v172\n"
      "v_mfma_f32_16x16x32_bf16 v[202:205], v[194:197], v[146:149], 0\n"
      "ds_read_b128 v[102:105], v7 offset:16384\n"
      "v_add_f32 v138, v138, v139\n"
      "v_mfma_f32_16x16x32_bf16 v[202:205], v[80:83], v[88:91], v[202:205]\n"
      "s_waitcnt lgkmcnt(5)\n"
      "v_rcp_f32 v100, v100\n"
      "v_mfma_f32_16x16x32_bf16 v[122:125], v[214:217], v[146:149], 0\n"
      "buffer_store_dwordx4 v[218:221], v23, s[24:27], s85 offen sc1\n"
      "v_pk_mul_f32 v[114:115], v[114:115], v[100:101] op_sel_hi:[1,0]\n"
      "v_pk_mul_f32 v[112:113], v[112:113], v[100:101] op_sel_hi:[1,0]\n"
      "v_pk_fma_f32 v[222:223], v[144:145], v[144:145], v[222:223]\n"
      "v_pk_mul_f32 v[160:161], v[160:161], v[98:99] op_sel_hi:[1,0]\n"
      "v_mfma_f32_16x16x32_bf16 v[122:125], v[84:87], v[88:91], v[122:125]\n"
      "s_waitcnt lgkmcnt(2)\n"
      "v_add_f32 v222, v222, v223\n"
      "v_mul_f32 v205, v100, v205\n"
      "v_mfma_f32_16x16x32_bf16 v[126:129], a[228:231], v[186:189], v[126:129]\n"
      "v_permlane16_swap_b32 v222, v138\n"
      "v_mfma_f32_16x16x32_bf16 v[126:129], a[232:235], v[134:137], v[126:129]\n"
      "ds_read_b128 v[210:213], v14 offset:64\n"
      "v_mul_f32 v203, v100, v203\n"
      "v_mfma_f32_16x16x32_bf16 v[126:129], a[236:239], v[190:193], v[126:129]\n"
      "v_mul_f32 v204, v100, v204\n"
      "v_cvt_pk_bf16_f32 v112, v112, v113\n"
      "v_mfma_f32_16x16x32_bf16 v[116:119], a[252:255], v[186:189], v[116:119]\n"
      "v_cvt_pk_bf16_f32 v113, v114, v115\n"
      "v_cvt_pk_bf16_f32 v159, v160, v161\n"
      "v_mfma_f32_16x16x32_bf16 v[116:119], v[224:227], v[134:137], v[116:119]\n"
      "ds_write_b64 v4, v[158:159] offset:18688\n"
      "v_mfma_f32_16x16x32_bf16 v[116:119], v[228:231], v[190:193], v[116:119]\n"
      "v_cvt_pk_bf16_f32 v115, v204, v205\n"
      "v_fma_f32 v158, v126, v126, s52\n"
      "v_mfma_f32_16x16x32_bf16 v[108:111], v[244:247], v[186:189], v[108:111]\n"
      "v_mul_f32 v159, v127, v127\n"
      "v_mul_f32 v202, v100, v202\n"
      "v_mfma_f32_16x16x32_bf16 v[108:111], v[248:251], v[134:137], v[108:111]\n"
      "s_waitcnt lgkmcnt(1)\n"
      "v_cvt_pk_bf16_f32 v114, v202, v203\n"
      "v_add_f32 v222, v222, v138\n"
      "v_mfma_f32_16x16x32_bf16 v[108:111], v[252:255], v[190:193], v[108:111]\n"
      "ds_write_b128 v3, v[112:115] offset:18944\n"
      "v_pk_fma_f32 v[158:159], v[128:129], v[128:129], v[158:159]\n"
      "v_pk_mul_f32 v[122:123], v[122:123], v[100:101] op_sel_hi:[1,0]\n"
      "v_pk_fma_f32 v[158:159], v[116:117], v[116:117], v[158:159]\n"
      "v_pk_mul_f32 v[124:125], v[124:125], v[100:101] op_sel_hi:[1,0]\n"
      "v_cvt_pk_bf16_f32 v122, v122, v123\n"
      "v_cvt_pk_bf16_f32 v123, v124, v125\n"
      "v_pk_fma_f32 v[158:159], v[118:119], v[118:119], v[158:159]\n"
      "v_pk_fma_f32 v[158:159], v[108:109], v[108:109], v[158:159]\n"
      "v_pk_fma_f32 v[158:159], v[110:111], v[110:111], v[158:159]\n"
      "v_add_f32 v158, v158, v159\n"
      "v_mov_b32 v138, v158\n"
      "v_mfma_f32_16x16x32_bf16 v[188:191], v[174:177], v[32:35], 0\n"
      "ds_write_b64 v4, v[122:123] offset:18944\n"
      "v_permlane16_swap_b32 v158, v138\n"
      "v_add_f32_e64 v158, v158, v138\n"
      "v_add_u32 v10, 2336, v10\n"
      "s_nop 0\n"
      "v_permlane32_swap_b32_e64 v222, v158\n"
      "v_add_f32 v222, v222, v158\n"
      "v_mfma_f32_16x16x32_bf16 v[188:191], v[210:213], v[32:35], v[188:191]\n"
      "ds_write_b32 v31, v222 offset:1024\n"
      "v_add_u32 v11, 2336, v11\n"
      "s_cmp_ge_u32 s95, 0x40000000\n"
      "s_cselect_b64 vcc, -1, s[56:57]\n"
      "buffer_store_dwordx4 v[102:105], v25, s[24:27], s85 offen sc1\n"
      "s_waitcnt lgkmcnt(0)\n"
      "s_barrier\n"
      "s_cbranch_vccnz L_disp0_%=\n"
      "L_loop0_%=:\n"
      "v_mfma_f32_16x16x32_bf16 v[218:221], v[174:177], v[130:133], 0\n"
      "ds_read_b128 v[138:141], v0\n"
      "v_readlane_b32 s84, v37, s43\n"
      "v_mfma_f32_16x16x32_bf16 v[218:221], v[210:213], v[76:79], v[218:221]\n"
      "ds_read_b128 v[158:161], v9 offset:1024\n"
      "ds_read_b128 v[184:187], v1\n"
      "v_mfma_f32_16x16x32_bf16 v[134:137], v[174:177], v[194:197], 0\n"
      "ds_read_b128 v[154:157], v1 offset:12288\n"
      "s_add_u32 m0, s64, 18432\n"
      "v_mfma_f32_16x16x32_bf16 v[134:137], v[210:213], v[80:83], v[134:137]\n"
      "buffer_load_dwordx4 v16, s[16:19], s45 offen  lds\n"
      "v_mfma_f32_16x16x32_bf16 v[100:103], v[174:177], v[214:217], 0\n"
      "s_add_u32 m0, s64, 22528\n"
      "ds_read_b128 v[202:205], v0 offset:6144\n"
      "v_mfma_f32_16x16x32_bf16 v[100:103], v[210:213], v[84:87], v[100:103]\n"
      "s_waitcnt lgkmcnt(4)\n"
      "v_mfma_f32_16x16x32_bf16 v[210:213], a[0:3], v[138:141], 0\n"
      "ds_read_b128 v[174:177], v1 offset:6144\n"
      "v_cndmask_b32 v218, v218, v134, s[58:59]\n"
      "v_mfma_f32_16x16x32_bf16 v[112:115], a[24:27], v[138:141], 0\n"
      "s_waitcnt lgkmcnt(4)\n"
      "v_add_f32 v158, v158, v159\n"
      "v_add_f32 v160, v160, v161\n"
      "v_mfma_f32_16x16x32_bf16 v[120:123], a[48:51], v[138:141], 0\n"
      "buffer_load_dwordx4 v17, s[16:19], s45 offen  lds\n"
      "v_mfma_f32_16x16x32_bf16 v[220:223], a[72:75], v[138:141], 0\n"
      "ds_read_b128 v[206:209], v0 offset:12288\n"
      "v_add_f32 v158, v158, v160\n"
      "v_mfma_f32_16x16x32_bf16 v[102:105], a[96:99], v[138:141], 0\n"
      "s_waitcnt lgkmcnt(2)\n"
      "v_rsq_f32_e64 v158, v158\n"
      "v_mfma_f32_16x16x32_bf16 v[146:149], a[120:123], v[138:141], 0\n"
      "s_add_u32 m0, s64, 26624\n"
      "v_mul_f32 v158, 0x415db3d7, v158\n"
      "v_mov_b32 v160, v158\n"
      "v_mfma_f32_16x16x32_bf16 v[210:213], a[4:7], v[184:187], v[210:213]\n"
      "s_nop 0\n"
      "v_permlane32_swap_b32_e64 v158, v160\n"
      "v_mfma_f32_16x16x32_bf16 v[210:213], a[8:11], v[202:205], v[210:213]\n"
      "buffer_load_dwordx4 v18, s[16:19], s45 offen  lds\n"
      "v_mfma_f32_16x16x32_bf16 v[112:115], a[28:31], v[184:187], v[112:115]\n"
      "ds_read_b128 v[96:99], v0 offset:2048\n"
      "v_mov_b32 v159, v158\n"
      "v_mfma_f32_16x16x32_bf16 v[112:115], a[32:35], v[202:205], v[112:115]\n"
      "s_nop 0\n"
      "v_permlane16_swap_b32_e64 v158, v159\n"
      "v_mfma_f32_16x16x32_bf16 v[120:123], a[52:55], v[184:187], v[120:123]\n"
      "s_add_u32 m0, s64, 30720\n"
      "v_mul_f32 v166, v158, v166\n"
      "v_mul_f32 v182, v159, v182\n"
      "v_mfma_f32_16x16x32_bf16 v[120:123], a[56:59], v[202:205], v[120:123]\n"
      "v_mul_f32 v165, v159, v165\n"
      "v_mul_f32 v142, v158, v142\n"
      "v_mfma_f32_16x16x32_bf16 v[220:223], a[76:79], v[184:187], v[220:223]\n"
      "v_mul_f32 v143, v158, v143\n"
      "v_mul_f32 v145, v158, v145\n"
      "v_mfma_f32_16x16x32_bf16 v[220:223], a[80:83], v[202:205], v[220:223]\n"
      "buffer_load_dwordx4 v19, s[16:19], s45 offen  lds\n"
      "v_mfma_f32_16x16x32_bf16 v[102:105], a[100:103], v[184:187], v[102:105]\n"
      "v_mul_f32 v167, v158, v167\n"
      "v_mul_f32 v168, v158, v168\n"
      "v_mfma_f32_16x16x32_bf16 v[102:105], a[104:107], v[202:205], v[102:105]\n"
      "v_mul_f32 v198, v158, v198\n"
      "v_mul_f32 v181, v159, v181\n"
      "v_mfma_f32_16x16x32_bf16 v[146:149], a[124:127], v[184:187], v[146:149]\n"
      "s_add_u32 m0, s65, 34816\n"
      "v_mul_f32 v163, v159, v163\n"
      "v_mul_f32 v110, v160, v110\n"
      "v_mfma_f32_16x16x32_bf16 v[146:149], a[128:131], v[202:205], v[146:149]\n"
      "s_waitcnt lgkmcnt(0)\n"
      "v_mov_b32_dpp v188, v188 quad_perm:[0,0,0,0] row_mask:0xf bank_mask:0xf\n"
      "v_mul_f32 v169, v158, v169\n"
      "v_mfma_f32_16x16x32_bf16 v[210:213], a[12:15], v[174:177], v[210:213]\n"
      "buffer_load_dwordx4 v15, s[16:19], s45 offen  lds\n"
      "v_mfma_f32_16x16x32_bf16 v[210:213], a[16:19], v[206:209], v[210:213]\n"
      "v_mul_f32_e64 v162, v159, v162\n"
      "v_cndmask_b32 v218, v218, v100, s[60:61]\n"
      "v_mfma_f32_16x16x32_bf16 v[210:213], a[20:23], v[154:157], v[210:213]\n"
      "ds_read_b128 v[190:193], v0 offset:8192\n"
      "v_mul_f32_e64 v201, v158, v201\n"
      "v_mfma_f32_16x16x32_bf16 v[112:115], a[36:39], v[174:177], v[112:115]\n"
      "v_cvt_pk_bf16_f32 v166, v166, v167\n"
      "v_cvt_pk_bf16_f32 v162, v162, v163\n"
      "v_mfma_f32_16x16x32_bf16 v[112:115], a[40:43], v[206:209], v[112:115]\n"
      "v_mul_f32 v200, v158, v200\n"
      "v_mul_f32 v108, v160, v108\n"
      "v_mfma_f32_16x16x32_bf16 v[112:115], a[44:47], v[154:157], v[112:115]\n"
      "buffer_load_dwordx4 v[134:137], v36, s[28:31], s47 offen\n"
      "v_mfma_f32_16x16x32_bf16 v[120:123], a[60:63], v[174:177], v[120:123]\n"
      "v_rcp_f32_e64 v188, v188\n"
      "v_mfma_f32_16x16x32_bf16 v[120:123], a[64:67], v[206:209], v[120:123]\n"
      "v_mul_f32 v118, v160, v118\n"
      "v_mul_f32 v126, v160, v126\n"
      "v_mfma_f32_16x16x32_bf16 v[120:123], a[68:71], v[154:157], v[120:123]\n"
      "v_mul_f32_e64 v164, v159, v164\n"
      "v_cvt_pk_bf16_f32 v163, v164, v165\n"
      "v_mfma_f32_16x16x32_bf16 v[220:223], a[84:87], v[174:177], v[220:223]\n"
      "ds_read_b128 v[150:153], v1 offset:8192\n"
      "v_mul_f32_e64 v199, v158, v199\n"
      "v_mfma_f32_16x16x32_bf16 v[220:223], a[88:91], v[206:209], v[220:223]\n"
      "v_mul_f32_e64 v180, v159, v180\n"
      "v_fma_f32 v77, v210, v210, s51\n"
      "v_mfma_f32_16x16x32_bf16 v[220:223], a[92:95], v[154:157], v[220:223]\n"
      "v_cvt_pk_bf16_f32 v167, v168, v169\n"
      "v_mul_f32_e64 v170, v159, v170\n"
      "v_mfma_f32_16x16x32_bf16 v[102:105], a[108:111], v[174:177], v[102:105]\n"
      "buffer_load_dword v88, v29, s[28:31], s47 offen\n"
      "v_mfma_f32_16x16x32_bf16 v[102:105], a[112:115], v[206:209], v[102:105]\n"
      "v_mul_f32 v183, v159, v183\n"
      "v_mul_f32 v218, v188, v218\n"
      "v_mfma_f32_16x16x32_bf16 v[102:105], a[116:119], v[154:157], v[102:105]\n"
      "v_fma_f32 v76, v220, v220, s51\n"
      "v_mul_f32_e64 v173, v159, v173\n"
      "v_mfma_f32_16x16x32_bf16 v[146:149], a[132:135], v[174:177], v[146:149]\n"
      "v_cvt_pk_bf16_f32 v164, v180, v181\n"
      "v_cvt_pk_bf16_f32 v169, v200, v201\n"
      "v_mfma_f32_16x16x32_bf16 v[146:149], a[136:139], v[206:209], v[146:149]\n"
      "v_cvt_pk_bf16_f32 v168, v198, v199\n"
      "v_cvt_pk_bf16_f32 v218, v218, v218\n"
      "v_mfma_f32_16x16x32_bf16 v[146:149], a[140:143], v[154:157], v[146:149]\n"
      "v_fmac_f32 v77, v211, v211\n"
      "v_mfma_f32_16x16x32_bf16 v[130:133], v[138:141], a[144:147], 0\n"
      "ds_write_b16 v12, v218 offset:16\n"
      "v_mfma_f32_16x16x32_bf16 v[130:133], v[184:187], a[148:151], v[130:133]\n"
      "v_mul_f32 v117, v160, v117\n"
      "v_mul_f32 v119, v160, v119\n"
      "v_mfma_f32_16x16x32_bf16 v[130:133], v[202:205], a[152:155], v[130:133]\n"
      "v_mul_f32 v129, v160, v129\n"
      "v_mul_f32_e64 v127, v160, v127\n"
      "v_mfma_f32_16x16x32_bf16 v[130:133], v[174:177], a[156:159], v[130:133]\n"
      "ds_write_b128 v5, v[166:169]\n"
      "v_mfma_f32_16x16x32_bf16 v[130:133], v[206:209], a[160:163], v[130:133]\n"
      "v_fmac_f32_e64 v76, v221, v221\n"
      "v_mfma_f32_16x16x32_bf16 v[130:133], v[154:157], a[164:167], v[130:133]\n"
      "ds_read_b128 v[198:201], v1 offset:2048\n"
      "v_cvt_pk_bf16_f32 v165, v182, v183\n"
      "v_mfma_f32_16x16x32_bf16 v[194:197], v[138:141], a[168:171], 0\n"
      "v_mul_f32_e64 v128, v160, v128\n"
      "v_cvt_pk_bf16_f32 v126, v126, v127\n"
      "v_mfma_f32_16x16x32_bf16 v[194:197], v[184:187], a[172:175], v[194:197]\n"
      "v_fmac_f32 v77, v212, v212\n"
      "v_mul_f32 v111, v160, v111\n"
      "v_mfma_f32_16x16x32_bf16 v[194:197], v[202:205], a[176:179], v[194:197]\n"
      "v_fmac_f32 v77, v213, v213\n"
      "v_fmac_f32 v77, v112, v112\n"
      "v_mfma_f32_16x16x32_bf16 v[194:197], v[174:177], a[180:183], v[194:197]\n"
      "v_fmac_f32 v76, v222, v222\n"
      "v_fmac_f32 v77, v113, v113\n"
      "v_mfma_f32_16x16x32_bf16 v[194:197], v[206:209], a[184:187], v[194:197]\n"
      "ds_write_b128 v5, v[162:165] offset:6144\n"
      "v_mfma_f32_16x16x32_bf16 v[194:197], v[154:157], a[188:191], v[194:197]\n"
      "v_fmac_f32 v76, v223, v223\n"
      "v_fmac_f32 v77, v114, v114\n"
      "v_mfma_f32_16x16x32_bf16 v[214:217], v[138:141], a[192:195], 0\n"
      "ds_read_b128 v[178:181], v1 offset:14336\n"
      "v_mul_f32 v144, v158, v144\n"
      "v_mfma_f32_16x16x32_bf16 v[214:217], v[184:187], a[196:199], v[214:217]\n"
      "v_cvt_pk_bf16_f32 v142, v142, v143\n"
      "v_fmac_f32 v76, v102, v102\n"
      "v_mfma_f32_16x16x32_bf16 v[214:217], v[202:205], a[200:203], v[214:217]\n"
      "ds_read_b128 v[162:165], v9 offset:5632\n"
      "v_fmac_f32_e64 v77, v115, v115\n"
      "v_mfma_f32_16x16x32_bf16 v[214:217], v[174:177], a[204:207], v[214:217]\n"
      "ds_read_b128 v[182:185], v0 offset:14336\n"
      "v_fmac_f32 v77, v120, v120\n"
      "v_mfma_f32_16x16x32_bf16 v[214:217], v[206:209], a[208:211], v[214:217]\n"
      "v_mul_f32 v171, v159, v171\n"
      "v_cvt_pk_bf16_f32 v127, v128, v129\n"
      "v_mfma_f32_16x16x32_bf16 v[214:217], v[154:157], a[212:215], v[214:217]\n"
      "v_add_u32 v12, 32, v12\n"
      "v_fmac_f32 v76, v103, v103\n"
      "v_mfma_f32_16x16x32_bf16 v[174:177], a[0:3], v[96:99], 0\n"
      "s_waitcnt lgkmcnt(2)\n"
      "v_fmac_f32 v77, v121, v121\n"
      "v_fmac_f32 v76, v104, v104\n"
      "v_mfma_f32_16x16x32_bf16 v[174:177], a[4:7], v[198:201], v[174:177]\n"
      "v_fmac_f32 v76, v105, v105\n"
      "v_mul_f32 v109, v160, v109\n"
      "v_mfma_f32_16x16x32_bf16 v[174:177], a[8:11], v[190:193], v[174:177]\n"
      "s_waitcnt lgkmcnt(1)\n"
      "v_fmac_f32 v77, v122, v122\n"
      "v_cvt_pk_bf16_f32 v194, v194, v195\n"
      "v_mfma_f32_16x16x32_bf16 v[174:177], a[12:15], v[150:153], v[174:177]\n"
      "s_waitcnt lgkmcnt(0)\n"
      "v_or3_b32 v162, v162, v163, v164\n"
      "v_cvt_pk_bf16_f32 v130, v130, v131\n"
      "v_mfma_f32_16x16x32_bf16 v[174:177], a[16:19], v[182:185], v[174:177]\n"
      "v_fmac_f32 v76, v146, v146\n"
      "v_mul_f32 v116, v160, v116\n"
      "v_mfma_f32_16x16x32_bf16 v[174:177], a[20:23], v[178:181], v[174:177]\n"
      "ds_read_b128 v[186:189], v0 offset:10240\n"
      "v_cvt_pk_bf16_f32 v108, v108, v109\n"
      "v_mfma_f32_16x16x32_bf16 v[202:205], a[24:27], v[96:99], 0\n"
      "v_cvt_pk_bf16_f32 v129, v118, v119\n"
      "v_cvt_pk_bf16_f32 v128, v116, v117\n"
      "v_mfma_f32_16x16x32_bf16 v[202:205], a[28:31], v[198:201], v[202:205]\n"
      "v_fmac_f32 v76, v147, v147\n"
      "v_fmac_f32 v77, v123, v123\n"
      "v_mfma_f32_16x16x32_bf16 v[202:205], a[32:35], v[190:193], v[202:205]\n"
      "v_fmac_f32 v76, v148, v148\n"
      "v_fma_f32 v164, v174, v174, s51\n"
      "v_mfma_f32_16x16x32_bf16 v[202:205], a[36:39], v[150:153], v[202:205]\n"
      "v_fmac_f32 v76, v149, v149\n"
      "v_fmac_f32_e64 v164, v175, v175\n"
      "v_mfma_f32_16x16x32_bf16 v[202:205], a[40:43], v[182:185], v[202:205]\n"
      "v_permlane16_swap_b32 v77, v76\n"
      "v_mfma_f32_16x16x32_bf16 v[202:205], a[44:47], v[178:181], v[202:205]\n"
      "v_add_f32 v77, v77, v76\n"
      "v_mov_b32_e64 v76, v77\n"
      "v_mfma_f32_16x16x32_bf16 v[116:119], a[48:51], v[96:99], 0\n"
      "ds_read_b128 v[154:157], v0 offset:4096\n"
      "v_cvt_pk_bf16_f32 v170, v170, v171\n"
      "v_mfma_f32_16x16x32_bf16 v[116:119], a[52:55], v[198:201], v[116:119]\n"
      "v_cvt_pk_bf16_f32 v131, v132, v133\n"
      "v_fmac_f32 v164, v176, v176\n"
      "v_mfma_f32_16x16x32_bf16 v[116:119], a[56:59], v[190:193], v[116:119]\n"
      "v_permlane32_swap_b32 v77, v76\n"
      "v_mfma_f32_16x16x32_bf16 v[116:119], a[60:63], v[150:153], v[116:119]\n"
      "v_cvt_pk_bf16_f32 v214, v214, v215\n"
      "v_add_f32 v77, v77, v76\n"
      "v_mfma_f32_16x16x32_bf16 v[116:119], a[64:67], v[182:185], v[116:119]\n"
      "v_rsq_f32 v77, v77\n"
      "v_mfma_f32_16x16x32_bf16 v[116:119], a[68:71], v[178:181], v[116:119]\n"
      "ds_write_b128 v5, v[126:129] offset:12288\n"
      "v_mfma_f32_16x16x32_bf16 v[126:129], a[72:75], v[96:99], 0\n"
      "v_fmac_f32 v164, v177, v177\n"
      "v_fmac_f32 v164, v202, v202\n"
      "v_mfma_f32_16x16x32_bf16 v[126:129], a[76:79], v[198:201], v[126:129]\n"
      "v_mul_f32 v172, v159, v172\n"
      "v_mov_b32 v76, v77\n"
      "v_mfma_f32_16x16x32_bf16 v[126:129], a[80:83], v[190:193], v[126:129]\n"
      "v_cvt_pk_bf16_f32 v143, v144, v145\n"
      "v_readlane_b32 s86, v38, s43\n"
      "v_mfma_f32_16x16x32_bf16 v[126:129], a[84:87], v[150:153], v[126:129]\n"
      "v_cvt_pk_bf16_f32 v109, v110, v111\n"
      "v_readlane_b32 s85, v37, s42\n"
      "v_mfma_f32_16x16x32_bf16 v[126:129], a[88:91], v[182:185], v[126:129]\n"
      "v_permlane16_swap_b32 v77, v76\n"
      "v_mfma_f32_16x16x32_bf16 v[126:129], a[92:95], v[178:181], v[126:129]\n"
      "v_fmac_f32 v164, v203, v203\n"
      "v_cvt_pk_bf16_f32 v195, v196, v197\n"
      "v_mfma_f32_16x16x32_bf16 v[158:161], a[96:99], v[96:99], 0\n"
      "ds_write_b64 v6, v[108:109] offset:12288\n"
      "v_mfma_f32_16x16x32_bf16 v[158:161], a[100:103], v[198:201], v[158:161]\n"
      "v_mul_f32 v146, v76, v146\n"
      "v_mul_f32 v104, v76, v104\n"
      "v_mfma_f32_16x16x32_bf16 v[158:161], a[104:107], v[190:193], v[158:161]\n"
      "v_fma_f32 v124, v126, v126, s51\n"
      "v_fmac_f32 v164, v204, v204\n"
      "v_mfma_f32_16x16x32_bf16 v[158:161], a[108:111], v[150:153], v[158:161]\n"
      "v_fmac_f32 v164, v205, v205\n"
      "v_mul_f32_e64 v77, s50, v77\n"
      "v_mfma_f32_16x16x32_bf16 v[158:161], a[112:115], v[182:185], v[158:161]\n"
      "v_fmac_f32 v164, v116, v116\n"
      "v_fmac_f32 v124, v127, v127\n"
      "v_mfma_f32_16x16x32_bf16 v[158:161], a[116:119], v[178:181], v[158:161]\n"
      "v_mul_f32 v147, v76, v147\n"
      "v_mul_f32 v213, v77, v213\n"
      "v_mfma_f32_16x16x32_bf16 v[106:109], a[120:123], v[96:99], 0\n"
      "v_fmac_f32 v164, v117, v117\n"
      "v_mul_f32 v222, v76, v222\n"
      "v_mfma_f32_16x16x32_bf16 v[106:109], a[124:127], v[198:201], v[106:109]\n"
      "v_fmac_f32 v124, v128, v128\n"
      "v_fmac_f32 v124, v129, v129\n"
      "v_mfma_f32_16x16x32_bf16 v[106:109], a[128:131], v[190:193], v[106:109]\n"
      "v_fmac_f32 v124, v158, v158\n"
      "v_mul_f32 v114, v77, v114\n"
      "v_mfma_f32_16x16x32_bf16 v[106:109], a[132:135], v[150:153], v[106:109]\n"
      "v_mul_f32 v211, v77, v211\n"
      "v_mul_f32 v115, v77, v115\n"
      "v_mfma_f32_16x16x32_bf16 v[106:109], a[136:139], v[182:185], v[106:109]\n"
      "v_fmac_f32 v124, v159, v159\n"
      "v_cvt_pk_bf16_f32 v215, v216, v217\n"
      "v_mfma_f32_16x16x32_bf16 v[106:109], a[140:143], v[178:181], v[106:109]\n"
      "v_mul_f32 v120, v77, v120\n"
      "v_fmac_f32_e64 v124, v160, v160\n"
      "v_mfma_f32_16x16x32_bf16 v[166:169], v[96:99], a[144:147], 0\n"
      "ds_write_b64 v6, v[142:143]\n"
      "v_mfma_f32_16x16x32_bf16 v[166:169], v[198:201], a[148:151], v[166:169]\n"
      "v_cvt_pk_bf16_f32 v171, v172, v173\n"
      "v_fmac_f32 v124, v161, v161\n"
      "v_mfma_f32_16x16x32_bf16 v[166:169], v[190:193], a[152:155], v[166:169]\n"
      "s_or_b32 s95, s84, s85\n"
      "v_mul_f32 v221, v76, v221\n"
      "v_mul_f32 v148, v76, v148\n"
      "v_mfma_f32_16x16x32_bf16 v[166:169], v[150:153], a[156:159], v[166:169]\n"
      "ds_read_b128 v[142:145], v1 offset:10240\n"
      "v_mul_f32 v103, v76, v103\n"
      "v_mfma_f32_16x16x32_bf16 v[166:169], v[182:185], a[160:163], v[166:169]\n"
      "v_fmac_f32 v124, v106, v106\n"
      "v_fmac_f32 v164, v118, v118\n"
      "v_mfma_f32_16x16x32_bf16 v[166:169], v[178:181], a[164:167], v[166:169]\n"
      "s_or_b32 s95, s95, s48\n"
      "v_fmac_f32 v124, v107, v107\n"
      "v_mul_f32 v210, v77, v210\n"
      "v_mfma_f32_16x16x32_bf16 v[138:141], v[96:99], a[168:171], 0\n"
      "v_fmac_f32 v164, v119, v119\n"
      "v_fmac_f32 v124, v108, v108\n"
      "v_mfma_f32_16x16x32_bf16 v[138:141], v[198:201], a[172:175], v[138:141]\n"
      "v_mul_f32 v121, v77, v121\n"
      "v_mul_f32 v102, v76, v102\n"
      "v_mfma_f32_16x16x32_bf16 v[138:141], v[190:193], a[176:179], v[138:141]\n"
      "v_fmac_f32 v124, v109, v109\n"
      "v_bitop3_b32 v162, v162, v165, s33 bitop3:0xa8\n"
      "v_mfma_f32_16x16x32_bf16 v[138:141], v[150:153], a[180:183], v[138:141]\n"
      "ds_read_b128 v[206:209], v1 offset:4096\n"
      "v_mul_f32 v122, v77, v122\n"
      "v_mfma_f32_16x16x32_bf16 v[138:141], v[182:185], a[184:187], v[138:141]\n"
      "v_permlane16_swap_b32_e64 v164, v124\n"
      "v_mfma_f32_16x16x32_bf16 v[138:141], v[178:181], a[188:191], v[138:141]\n"
      "ds_write_b64 v6, v[170:171] offset:6144\n"
      "v_mfma_f32_16x16x32_bf16 v[170:173], v[96:99], a[192:195], 0\n"
      "v_add_f32 v164, v164, v124\n"
      "v_mul_f32 v220, v76, v220\n"
      "v_mfma_f32_16x16x32_bf16 v[170:173], v[198:201], a[196:199], v[170:173]\n"
      "ds_read_b128 v[198:201], v0 offset:16384\n"
      "v_mov_b32 v124, v164\n"
      "v_mfma_f32_16x16x32_bf16 v[170:173], v[190:193], a[200:203], v[170:173]\n"
      "s_nop 0\n"
      "v_permlane32_swap_b32_e64 v164, v124\n"
      "v_mfma_f32_16x16x32_bf16 v[170:173], v[150:153], a[204:207], v[170:173]\n"
      "ds_read_b128 v[190:193], v1 offset:16384\n"
      "v_add_f32 v164, v164, v124\n"
      "v_mfma_f32_16x16x32_bf16 v[170:173], v[182:185], a[208:211], v[170:173]\n"
      "v_rsq_f32 v164, v164\n"
      "v_mfma_f32_16x16x32_bf16 v[170:173], v[178:181], a[212:215], v[170:173]\n"
      "s_waitcnt lgkmcnt(3)\n"
      "v_cvt_pk_bf16_f32 v210, v210, v211\n"
      "v_mul_f32 v212, v77, v212\n"
      "v_mfma_f32_16x16x32_bf16 v[98:101], a[0:3], v[154:157], 0\n"
      "v_mul_f32 v105, v76, v105\n"
      "v_mov_b32 v124, v164\n"
      "v_mfma_f32_16x16x32_bf16 v[98:101], a[4:7], v[206:209], v[98:101]\n"
      "s_nop 0\n"
      "v_permlane16_swap_b32_e64 v164, v124\n"
      "v_mfma_f32_16x16x32_bf16 v[98:101], a[8:11], v[186:189], v[98:101]\n"
      "s_waitcnt lgkmcnt(1)\n"
      "v_cvt_pk_bf16_f32 v211, v212, v213\n"
      "v_cmp_ne_u32 s[56:57], 0, v162\n"
      "v_mfma_f32_16x16x32_bf16 v[98:101], a[12:15], v[142:145], v[98:101]\n"
      "ds_read_u16 v78, v11\n"
      "v_mul_f32 v127, v124, v127\n"
      "v_mfma_f32_16x16x32_bf16 v[98:101], a[16:19], v[198:201], v[98:101]\n"
      "s_waitcnt lgkmcnt(1)\n"
      "v_cvt_pk_bf16_f32 v217, v172, v173\n"
      "v_mul_f32_e64 v164, s50, v164\n"
      "v_mfma_f32_16x16x32_bf16 v[98:101], a[20:23], v[190:193], v[98:101]\n"
      "v_mul_f32 v126, v124, v126\n"
      "v_mul_f32 v159, v124, v159\n"
      "v_mfma_f32_16x16x32_bf16 v[182:185], a[24:27], v[154:157], 0\n"
      "v_mul_f32_e64 v223, v76, v223\n"
      "v_cvt_pk_bf16_f32 v132, v166, v167\n"
      "v_mfma_f32_16x16x32_bf16 v[182:185], a[28:31], v[206:209], v[182:185]\n"
      "v_cvt_pk_bf16_f32 v216, v170, v171\n"
      "v_mul_f32_e64 v116, v164, v116\n"
      "v_mfma_f32_16x16x32_bf16 v[182:185], a[32:35], v[186:189], v[182:185]\n"
      "v_cvt_pk_bf16_f32 v220, v220, v221\n"
      "v_cvt_pk_bf16_f32 v120, v120, v121\n"
      "v_mfma_f32_16x16x32_bf16 v[182:185], a[36:39], v[142:145], v[182:185]\n"
      "v_mul_f32 v118, v164, v118\n"
      "v_fma_f32 v219, v98, v98, s51\n"
      "v_mfma_f32_16x16x32_bf16 v[182:185], a[40:43], v[198:201], v[182:185]\n"
      "v_cvt_pk_bf16_f32 v126, v126, v127\n"
      "v_mul_f32 v113, v77, v113\n"
      "v_mfma_f32_16x16x32_bf16 v[182:185], a[44:47], v[190:193], v[182:185]\n"
      "v_fmac_f32 v219, v99, v99\n"
      "v_mul_f32 v174, v164, v174\n"
      "v_mfma_f32_16x16x32_bf16 v[178:181], a[48:51], v[154:157], 0\n"
      "v_fmac_f32_e64 v219, v100, v100\n"
      "v_cvt_pk_bf16_f32 v221, v222, v223\n"
      "v_mfma_f32_16x16x32_bf16 v[178:181], a[52:55], v[206:209], v[178:181]\n"
      "v_fmac_f32 v219, v101, v101\n"
      "v_mul_f32 v107, v124, v107\n"
      "v_mfma_f32_16x16x32_bf16 v[178:181], a[56:59], v[186:189], v[178:181]\n"
      "v_cvt_pk_bf16_f32 v196, v138, v139\n"
      "v_mul_f32 v149, v76, v149\n"
      "v_mfma_f32_16x16x32_bf16 v[178:181], a[60:63], v[142:145], v[178:181]\n"
      "v_mul_f32 v112, v77, v112\n"
      "v_cvt_pk_bf16_f32 v146, v146, v147\n"
      "v_mfma_f32_16x16x32_bf16 v[178:181], a[64:67], v[198:201], v[178:181]\n"
      "v_mul_f32 v158, v124, v158\n"
      "v_mul_f32 v175, v164, v175\n"
      "v_mfma_f32_16x16x32_bf16 v[178:181], a[68:71], v[190:193], v[178:181]\n"
      "v_cvt_pk_bf16_f32 v223, v104, v105\n"
      "v_mul_f32 v161, v124, v161\n"
      "v_mfma_f32_16x16x32_bf16 v[150:153], a[72:75], v[154:157], 0\n"
      "v_cvt_pk_bf16_f32 v212, v112, v113\n"
      "v_mul_f32 v176, v164, v176\n"
      "v_mfma_f32_16x16x32_bf16 v[150:153], a[76:79], v[206:209], v[150:153]\n"
      "v_cvt_pk_bf16_f32 v174, v174, v175\n"
      "v_mul_f32 v108, v124, v108\n"
      "v_mfma_f32_16x16x32_bf16 v[150:153], a[80:83], v[186:189], v[150:153]\n"
      "v_mul_f32 v204, v164, v204\n"
      "v_mul_f32_e64 v177, v164, v177\n"
      "v_mfma_f32_16x16x32_bf16 v[150:153], a[84:87], v[142:145], v[150:153]\n"
      "v_mul_f32 v129, v124, v129\n"
      "v_cvt_pk_bf16_f32 v222, v102, v103\n"
      "v_mfma_f32_16x16x32_bf16 v[150:153], a[88:91], v[198:201], v[150:153]\n"
      "v_mul_f32 v123, v77, v123\n"
      "v_mul_f32 v202, v164, v202\n"
      "v_mfma_f32_16x16x32_bf16 v[150:153], a[92:95], v[190:193], v[150:153]\n"
      "v_fmac_f32 v219, v182, v182\n"
      "v_cvt_pk_bf16_f32 v175, v176, v177\n"
      "v_mfma_f32_16x16x32_bf16 v[170:173], a[96:99], v[154:157], 0\n"
      "v_cvt_pk_bf16_f32 v147, v148, v149\n"
      "v_mul_f32 v117, v164, v117\n"
      "v_mfma_f32_16x16x32_bf16 v[170:173], a[100:103], v[206:209], v[170:173]\n"
      "v_mul_f32 v128, v124, v128\n"
      "v_cvt_pk_bf16_f32 v121, v122, v123\n"
      "v_mfma_f32_16x16x32_bf16 v[170:173], a[104:107], v[186:189], v[170:173]\n"
      "v_mul_f32 v203, v164, v203\n"
      "v_fmac_f32 v219, v183, v183\n"
      "v_mfma_f32_16x16x32_bf16 v[170:173], a[108:111], v[142:145], v[170:173]\n"
      "v_cvt_pk_bf16_f32 v213, v114, v115\n"
      "v_cvt_pk_bf16_f32 v133, v168, v169\n"
      "v_mfma_f32_16x16x32_bf16 v[170:173], a[112:115], v[198:201], v[170:173]\n"
      "v_fmac_f32 v219, v184, v184\n"
      "v_mul_f32 v205, v164, v205\n"
      "v_mfma_f32_16x16x32_bf16 v[170:173], a[116:119], v[190:193], v[170:173]\n"
      "v_fmac_f32 v219, v185, v185\n"
      "v_fma_f32 v115, v150, v150, s51\n"
      "v_mfma_f32_16x16x32_bf16 v[102:105], a[120:123], v[154:157], 0\n"
      "v_fmac_f32 v115, v151, v151\n"
      "v_mul_f32_e64 v119, v164, v119\n"
      "v_mfma_f32_16x16x32_bf16 v[102:105], a[124:127], v[206:209], v[102:105]\n"
      "v_fmac_f32 v115, v152, v152\n"
      "v_cvt_pk_bf16_f32 v127, v128, v129\n"
      "v_mfma_f32_16x16x32_bf16 v[102:105], a[128:131], v[186:189], v[102:105]\n"
      "v_fmac_f32 v219, v178, v178\n"
      "v_cvt_pk_bf16_f32 v197, v140, v141\n"
      "v_mfma_f32_16x16x32_bf16 v[102:105], a[132:135], v[142:145], v[102:105]\n"
      "v_cvt_pk_bf16_f32 v176, v202, v203\n"
      "v_fmac_f32_e64 v115, v153, v153\n"
      "v_mfma_f32_16x16x32_bf16 v[102:105], a[136:139], v[198:201], v[102:105]\n"
      "v_fmac_f32 v115, v170, v170\n"
      "v_mul_f32 v106, v124, v106\n"
      "v_mfma_f32_16x16x32_bf16 v[102:105], a[140:143], v[190:193], v[102:105]\n"
      "v_cvt_pk_bf16_f32 v106, v106, v107\n"
      "v_cvt_pk_bf16_f32 v116, v116, v117\n"
      "v_mfma_f32_16x16x32_bf16 v[166:169], v[154:157], a[144:147], 0\n"
      "ds_read_b128 v[162:165], v2 offset:24576\n"
      "v_fmac_f32_e64 v115, v171, v171\n"
      "v_mfma_f32_16x16x32_bf16 v[166:169], v[206:209], a[148:151], v[166:169]\n"
      "v_fmac_f32 v115, v172, v172\n"
      "v_mul_f32 v109, v124, v109\n"
      "v_mfma_f32_16x16x32_bf16 v[166:169], v[186:189], a[152:155], v[166:169]\n"
      "v_fmac_f32_e64 v115, v173, v173\n"
      "v_cvt_pk_bf16_f32 v107, v108, v109\n"
      "v_mfma_f32_16x16x32_bf16 v[166:169], v[142:145], a[156:159], v[166:169]\n"
      "ds_read_b128 v[108:111], v2 offset:18432\n"
      "v_fmac_f32_e64 v219, v179, v179\n"
      "v_mfma_f32_16x16x32_bf16 v[166:169], v[198:201], a[160:163], v[166:169]\n"
      "v_cvt_pk_bf16_f32 v128, v158, v159\n"
      "v_fmac_f32 v115, v102, v102\n"
      "v_mfma_f32_16x16x32_bf16 v[166:169], v[190:193], a[164:167], v[166:169]\n"
      "v_fmac_f32 v115, v103, v103\n"
      "v_fmac_f32 v219, v180, v180\n"
      "v_mfma_f32_16x16x32_bf16 v[138:141], v[154:157], a[168:171], 0\n"
      "v_cvt_pk_bf16_f32 v177, v204, v205\n"
      "v_mul_f32 v160, v124, v160\n"
      "v_mfma_f32_16x16x32_bf16 v[138:141], v[206:209], a[172:175], v[138:141]\n"
      "v_cvt_pk_bf16_f32 v117, v118, v119\n"
      "v_fmac_f32 v115, v104, v104\n"
      "v_mfma_f32_16x16x32_bf16 v[138:141], v[186:189], a[176:179], v[138:141]\n"
      "v_fmac_f32 v219, v181, v181\n"
      "v_fmac_f32_e64 v115, v105, v105\n"
      "v_mfma_f32_16x16x32_bf16 v[138:141], v[142:145], a[180:183], v[138:141]\n"
      "s_nop 0\n"
      "v_permlane16_swap_b32 v219, v115\n"
      "v_mfma_f32_16x16x32_bf16 v[138:141], v[198:201], a[184:187], v[138:141]\n"
      "s_add_u32 s43, s43, 1\n"
      "v_add_f32 v219, v219, v115\n"
      "v_mov_b32_e64 v115, v219\n"
      "v_mfma_f32_16x16x32_bf16 v[138:141], v[190:193], a[188:191], v[138:141]\n"
      "ds_read_b128 v[202:205], v2 offset:21504\n"
      "v_cvt_pk_bf16_f32 v129, v160, v161\n"
      "v_mfma_f32_16x16x32_bf16 v[158:161], v[154:157], a[192:195], 0\n"
      "v_permlane32_swap_b32_e64 v219, v115\n"
      "v_mfma_f32_16x16x32_bf16 v[158:161], v[206:209], a[196:199], v[158:161]\n"
      "ds_read_b128 v[122:125], v2 offset:30720\n"
      "v_cvt_pk_bf16_f32 v77, v168, v169\n"
      "v_mfma_f32_16x16x32_bf16 v[158:161], v[186:189], a[200:203], v[158:161]\n"
      "ds_read_b128 v[186:189], v2 offset:33792\n"
      "v_cvt_pk_bf16_f32 v76, v166, v167\n"
      "v_mfma_f32_16x16x32_bf16 v[158:161], v[142:145], a[204:207], v[158:161]\n"
      "ds_read_b128 v[206:209], v2 offset:27648\n"
      "v_add_f32 v219, v219, v115\n"
      "v_mfma_f32_16x16x32_bf16 v[158:161], v[198:201], a[208:211], v[158:161]\n"
      "v_rsq_f32 v219, v219\n"
      "v_mfma_f32_16x16x32_bf16 v[158:161], v[190:193], a[212:215], v[158:161]\n"
      "s_waitcnt lgkmcnt(3)\n"
      "v_mov_b32 v115, v219\n"
      "v_cvt_pk_bf16_f32 v80, v138, v139\n"
      "v_mfma_f32_16x16x32_bf16 v[166:169], a[216:219], v[108:111], 0\n"
      "v_permlane16_swap_b32 v219, v115\n"
      "v_mfma_f32_16x16x32_bf16 v[166:169], a[220:223], v[202:205], v[166:169]\n"
      "v_mul_f32 v219, s50, v219\n"
      "v_mul_f32 v185, v219, v185\n"
      "v_mfma_f32_16x16x32_bf16 v[166:169], a[224:227], v[162:165], v[166:169]\n"
      "s_and_b32 s86, s86, -4\n"
      "v_mul_f32 v153, v115, v153\n"
      "v_mul_f32 v171, v115, v171\n"
      "v_mfma_f32_16x16x32_bf16 v[198:201], a[240:243], v[108:111], 0\n"
      "v_mul_f32 v105, v115, v105\n"
      "v_mul_f32 v181, v219, v181\n"
      "v_mfma_f32_16x16x32_bf16 v[198:201], a[244:247], v[202:205], v[198:201]\n"
      "v_mul_f32 v99, v219, v99\n"
      "v_mul_f32 v150, v115, v150\n"
      "v_mfma_f32_16x16x32_bf16 v[198:201], a[248:251], v[162:165], v[198:201]\n"
      "ds_read_u16 v86, v11 offset:64\n"
      "v_mul_f32_e64 v98, v219, v98\n"
      "v_mfma_f32_16x16x32_bf16 v[142:145], v[232:235], v[108:111], 0\n"
      "v_mul_f32 v103, v115, v103\n"
      "v_mul_f32_e64 v184, v219, v184\n"
      "v_mfma_f32_16x16x32_bf16 v[142:145], v[236:239], v[202:205], v[142:145]\n"
      "s_waitcnt lgkmcnt(1)\n"
      "v_cvt_pk_bf16_f32 v81, v140, v141\n"
      "v_mul_f32 v183, v219, v183\n"
      "v_mfma_f32_16x16x32_bf16 v[142:145], v[240:243], v[162:165], v[142:145]\n"
      "ds_read_b128 v[190:193], v10 offset:32\n"
      "v_mul_f32 v151, v115, v151\n"
      "v_mfma_f32_16x16x32_bf16 v[166:169], a[228:231], v[206:209], v[166:169]\n"
      "ds_read_b128 v[108:111], v2 offset:18688\n"
      "v_mul_f32_e64 v173, v115, v173\n"
      "v_mfma_f32_16x16x32_bf16 v[166:169], a[232:235], v[122:125], v[166:169]\n"
      "ds_read_b128 v[202:205], v2 offset:21760\n"
      "v_cvt_pk_bf16_f32 v150, v150, v151\n"
      "v_mfma_f32_16x16x32_bf16 v[166:169], a[236:239], v[186:189], v[166:169]\n"
      "v_mul_f32_e64 v152, v115, v152\n"
      "v_cvt_pk_bf16_f32 v84, v158, v159\n"
      "v_mfma_f32_16x16x32_bf16 v[198:201], a[252:255], v[206:209], v[198:201]\n"
      "v_mul_f32 v172, v115, v172\n"
      "v_mul_f32 v101, v219, v101\n"
      "v_mfma_f32_16x16x32_bf16 v[198:201], v[224:227], v[122:125], v[198:201]\n"
      "v_mul_f32 v178, v219, v178\n"
      "v_cvt_pk_bf16_f32 v151, v152, v153\n"
      "v_mfma_f32_16x16x32_bf16 v[198:201], v[228:231], v[186:189], v[198:201]\n"
      "ds_read_b64 v[96:97], v10 offset:24\n"
      "v_mul_f32 v102, v115, v102\n"
      "v_mfma_f32_16x16x32_bf16 v[142:145], v[244:247], v[206:209], v[142:145]\n"
      "v_cvt_pk_bf16_f32 v85, v160, v161\n"
      "v_mul_f32 v170, v115, v170\n"
      "v_mfma_f32_16x16x32_bf16 v[142:145], v[248:251], v[122:125], v[142:145]\n"
      "v_cvt_pk_bf16_f32 v102, v102, v103\n"
      "v_mul_f32 v104, v115, v104\n"
      "v_mfma_f32_16x16x32_bf16 v[142:145], v[252:255], v[186:189], v[142:145]\n"
      "ds_read_u16 v82, v11 offset:32\n"
      "v_cvt_pk_bf16_f32 v153, v172, v173\n"
      "v_mfma_f32_16x16x32_bf16 v[186:189], v[220:223], v[210:213], v[40:43]\n"
      "ds_read_b128 v[158:161], v2 offset:24832\n"
      "v_cvt_pk_bf16_f32 v152, v170, v171\n"
      "v_mfma_f32_16x16x32_bf16 v[206:209], v[126:129], v[210:213], v[44:47]\n"
      "s_waitcnt lgkmcnt(3)\n"
      "v_cvt_pk_bf16_f32 v98, v98, v99\n"
      "v_cvt_pk_bf16_f32 v103, v104, v105\n"
      "v_mfma_f32_16x16x32_bf16 v[154:157], v[150:153], v[210:213], v[48:51]\n"
      "s_cmp_ge_u32 s95, 0x40000000\n"
      "s_cselect_b32 s94, -1, -2\n"
      "v_mul_f32 v182, v219, v182\n"
      "v_mfma_f32_16x16x32_bf16 v[138:141], v[190:193], v[210:213], v[24:27]\n"
      "s_waitcnt lgkmcnt(2)\n"
      "s_waitcnt vmcnt(1)\n"
      "v_or3_b32 v134, v134, v135, v136\n"
      "v_mul_f32 v100, v219, v100\n"
      "v_mfma_f32_16x16x16_bf16 v[186:189], v[146:147], v[120:121], v[186:189]\n"
      "v_fma_f32 v218, v166, v166, s52\n"
      "v_cvt_pk_bf16_f32 v99, v100, v101\n"
      "v_mul_f32 v180, v219, v180\n"
      "v_mfma_f32_16x16x16_bf16 v[206:209], v[106:107], v[120:121], v[206:209]\n"
      "v_mul_f32_e64 v179, v219, v179\n"
      "v_cvt_pk_bf16_f32 v178, v178, v179\n"
      "v_cvt_pk_bf16_f32 v179, v180, v181\n"
      "v_mfma_f32_16x16x16_bf16 v[154:157], v[102:103], v[120:121], v[154:157]\n"
      "v_exp_f32 v186, v186\n"
      "v_cvt_pk_bf16_f32 v100, v182, v183\n"
      "v_mfma_f32_16x16x16_bf16 v[138:141], v[96:97], v[120:121], v[138:141]\n"
      "v_exp_f32 v187, v187\n"
      "v_mul_f32 v219, v167, v167\n"
      "v_mfma_f32_16x16x32_bf16 v[162:165], a[216:219], v[108:111], 0\n"
      "v_exp_f32 v206, v206\n"
      "v_mfma_f32_16x16x32_bf16 v[162:165], a[220:223], v[202:205], v[162:165]\n"
      "s_waitcnt lgkmcnt(0)\n"
      "v_exp_f32 v209, v209\n"
      "v_mfma_f32_16x16x32_bf16 v[162:165], a[224:227], v[158:161], v[162:165]\n"
      "v_exp_f32 v189, v189\n"
      "v_mfma_f32_16x16x32_bf16 v[180:183], a[240:243], v[108:111], 0\n"
      "v_exp_f32 v154, v154\n"
      "v_mfma_f32_16x16x32_bf16 v[180:183], a[244:247], v[202:205], v[180:183]\n"
      "v_exp_f32 v155, v155\n"
      "v_mfma_f32_16x16x32_bf16 v[180:183], a[248:251], v[158:161], v[180:183]\n"
      "v_cvt_pk_bf16_f32 v186, v186, v187\n"
      "s_waitcnt vmcnt(0)\n"
      "v_or3_b32 v134, v134, v137, v88\n"
      "v_mfma_f32_16x16x32_bf16 v[170:173], v[232:235], v[108:111], 0\n"
      "v_exp_f32_e64 v157, v157\n"
      "v_mfma_f32_16x16x32_bf16 v[170:173], v[236:239], v[202:205], v[170:173]\n"
      "ds_read_b128 v[210:213], v2 offset:30976\n"
      "v_cvt_pk_bf16_f32 v88, v154, v155\n"
      "v_mfma_f32_16x16x32_bf16 v[170:173], v[240:243], v[158:161], v[170:173]\n"
      "v_exp_f32 v156, v156\n"
      "v_mfma_f32_16x16x32_bf16 v[118:121], v[220:223], v[174:177], v[52:55]\n"
      "v_exp_f32 v138, v138\n"
      "v_mfma_f32_16x16x32_bf16 v[108:111], v[126:129], v[174:177], v[56:59]\n"
      "v_exp_f32 v208, v208\n"
      "v_mfma_f32_16x16x32_bf16 v[202:205], v[150:153], v[174:177], v[60:63]\n"
      "v_exp_f32 v207, v207\n"
      "v_mfma_f32_16x16x32_bf16 v[122:125], v[190:193], v[174:177], v[26:29]\n"
      "s_cmp_gt_u32 s42, s44\n"
      "s_cselect_b32 s95, s94, s95\n"
      "v_cvt_pk_bf16_f32 v89, v156, v157\n"
      "v_mfma_f32_16x16x16_bf16 v[118:121], v[146:147], v[116:117], v[118:121]\n"
      "v_fmac_f32 v219, v169, v169\n"
      "v_exp_f32 v188, v188\n"
      "v_mfma_f32_16x16x16_bf16 v[108:111], v[106:107], v[116:117], v[108:111]\n"
      "v_cvt_pk_bf16_f32 v187, v188, v189\n"
      "v_cvt_pk_bf16_f32 v188, v206, v207\n"
      "v_fmac_f32_e64 v218, v168, v168\n"
      "v_mfma_f32_16x16x16_bf16 v[202:205], v[102:103], v[116:117], v[202:205]\n"
      "v_cvt_pk_bf16_f32 v189, v208, v209\n"
      "v_cvt_pk_bf16_f32 v101, v184, v185\n"
      "v_cvt_pk_bf16_f32 v90, v138, 0\n"
      "v_mfma_f32_16x16x16_bf16 v[122:125], v[96:97], v[116:117], v[122:125]\n"
      "v_exp_f32 v119, v119\n"
      "v_exp_f32 v109, v109\n"
      "v_mfma_f32_16x16x32_bf16 v[174:177], v[32:35], v[186:189], 0\n"
      "v_exp_f32_e64 v108, v108\n"
      "v_mfma_f32_16x16x32_bf16 v[174:177], v[32:35], v[88:91], v[174:177]\n"
      "v_exp_f32_e64 v118, v118\n"
      "v_mfma_f32_16x16x32_bf16 v[136:139], v[130:133], v[186:189], 0\n"
      "ds_write_b32 v31, v134 offset:4608\n"
      "v_mfma_f32_16x16x32_bf16 v[136:139], v[76:79], v[88:91], v[136:139]\n"
      "v_exp_f32 v120, v120\n"
      "v_mfma_f32_16x16x32_bf16 v[114:117], v[194:197], v[186:189], 0\n"
      "v_exp_f32 v121, v121\n"
      "v_mfma_f32_16x16x32_bf16 v[114:117], v[80:83], v[88:91], v[114:117]\n"
      "ds_read_b128 v[158:161], v2 offset:27904\n"
      "v_cvt_pk_bf16_f32 v118, v118, v119\n"
      "v_mfma_f32_16x16x32_bf16 v[154:157], v[214:217], v[186:189], 0\n"
      "v_exp_f32_e64 v110, v110\n"
      "v_mfma_f32_16x16x32_bf16 v[154:157], v[84:87], v[88:91], v[154:157]\n"
      "v_cvt_pk_bf16_f32 v119, v120, v121\n"
      "v_cvt_pk_bf16_f32 v120, v108, v109\n"
      "v_mfma_f32_16x16x32_bf16 v[186:189], v[220:223], v[98:101], v[64:67]\n"
      "v_exp_f32 v111, v111\n"
      "v_mfma_f32_16x16x32_bf16 v[206:209], v[126:129], v[98:101], v[68:71]\n"
      "v_cvt_pk_bf16_f32 v121, v110, v111\n"
      "v_rcp_f32 v174, v174\n"
      "v_exp_f32_e64 v204, v204\n"
      "v_pk_mul_f32 v[154:155], v[154:155], v[174:175] op_sel_hi:[1,0]\n"
      "v_pk_mul_f32 v[116:117], v[116:117], v[174:175] op_sel_hi:[1,0]\n"
      "v_pk_mul_f32 v[136:137], v[136:137], v[174:175] op_sel_hi:[1,0]\n"
      "v_pk_mul_f32 v[138:139], v[138:139], v[174:175] op_sel_hi:[1,0]\n"
      "v_pk_mul_f32 v[156:157], v[156:157], v[174:175] op_sel_hi:[1,0]\n"
      "v_pk_mul_f32 v[114:115], v[114:115], v[174:175] op_sel_hi:[1,0]\n"
      "v_mfma_f32_16x16x32_bf16 v[174:177], v[150:153], v[98:101], v[72:75]\n"
      "s_waitcnt lgkmcnt(0)\n"
      "v_exp_f32 v122, v122\n"
      "v_mfma_f32_16x16x32_bf16 v[108:111], v[190:193], v[98:101], v[28:31]\n"
      "ds_read_b128 v[98:101], v2 offset:34048\n"
      "v_cvt_pk_bf16_f32 v154, v154, v155\n"
      "v_mfma_f32_16x16x16_bf16 v[186:189], v[146:147], v[178:179], v[186:189]\n"
      "v_exp_f32 v203, v203\n"
      "v_cvt_pk_bf16_f32 v136, v136, v137\n"
      "v_mfma_f32_16x16x16_bf16 v[206:209], v[106:107], v[178:179], v[206:209]\n"
      "v_cvt_pk_bf16_f32 v137, v138, v139\n"
      "v_exp_f32 v202, v202\n"
      "v_mfma_f32_16x16x16_bf16 v[174:177], v[102:103], v[178:179], v[174:177]\n"
      "v_cvt_pk_bf16_f32 v139, v116, v117\n"
      "v_cvt_pk_bf16_f32 v155, v156, v157\n"
      "v_cvt_pk_bf16_f32 v92, v202, v203\n"
      "v_mfma_f32_16x16x16_bf16 v[108:111], v[96:97], v[178:179], v[108:111]\n"
      "v_cvt_pk_bf16_f32 v94, v122, 0\n"
      "v_exp_f32 v206, v206\n"
      "v_mfma_f32_16x16x32_bf16 v[162:165], a[228:231], v[158:161], v[162:165]\n"
      "s_nop 0\n"
      "v_exp_f32 v176, v176\n"
      "v_mfma_f32_16x16x32_bf16 v[162:165], a[232:235], v[210:213], v[162:165]\n"
      "s_waitcnt lgkmcnt(0)\n"
      "v_exp_f32 v186, v186\n"
      "v_mfma_f32_16x16x32_bf16 v[162:165], a[236:239], v[98:101], v[162:165]\n"
      "v_exp_f32 v208, v208\n"
      "v_mfma_f32_16x16x32_bf16 v[180:183], a[252:255], v[158:161], v[180:183]\n"
      "ds_read_b128 v[122:125], v10\n"
      "v_cvt_pk_bf16_f32 v138, v114, v115\n"
      "v_mfma_f32_16x16x32_bf16 v[180:183], v[224:227], v[210:213], v[180:183]\n"
      "v_exp_f32 v205, v205\n"
      "v_mfma_f32_16x16x32_bf16 v[180:183], v[228:231], v[98:101], v[180:183]\n"
      "v_exp_f32 v188, v188\n"
      "v_mfma_f32_16x16x32_bf16 v[170:173], v[244:247], v[158:161], v[170:173]\n"
      "v_exp_f32_e64 v187, v187\n"
      "v_mfma_f32_16x16x32_bf16 v[170:173], v[248:251], v[210:213], v[170:173]\n"
      "ds_read_b128 v[210:213], v7 offset:18432\n"
      "v_cvt_pk_bf16_f32 v93, v204, v205\n"
      "v_mfma_f32_16x16x32_bf16 v[170:173], v[252:255], v[98:101], v[170:173]\n"
      "v_exp_f32 v207, v207\n"
      "v_mfma_f32_16x16x32_bf16 v[114:117], v[32:35], v[118:121], 0\n"
      "v_exp_f32 v108, v108\n"
      "v_mfma_f32_16x16x32_bf16 v[114:117], v[32:35], v[92:95], v[114:117]\n"
      "ds_read_b64 v[88:89], v10 offset:16\n"
      "v_fma_f32 v160, v162, v162, s52\n"
      "v_mfma_f32_16x16x32_bf16 v[98:101], v[130:133], v[118:121], 0\n"
      "ds_read_b128 v[110:113], v7 offset:22528\n"
      "v_mul_f32_e64 v161, v163, v163\n"
      "v_mfma_f32_16x16x32_bf16 v[98:101], v[76:79], v[92:95], v[98:101]\n"
      "ds_write_b128 v3, v[136:139]\n"
      "v_mfma_f32_16x16x32_bf16 v[134:137], v[194:197], v[118:121], 0\n"
      "s_waitcnt lgkmcnt(3)\n"
      "v_rcp_f32 v114, v114\n"
      "v_mfma_f32_16x16x32_bf16 v[134:137], v[80:83], v[92:95], v[134:137]\n"
      "buffer_store_dwordx4 v[210:213], v20, s[24:27], s46 offen sc1\n"
      "v_pk_fma_f32 v[218:219], v[198:199], v[198:199], v[218:219]\n"
      "v_pk_fma_f32 v[160:161], v[164:165], v[164:165], v[160:161]\n"
      "v_mfma_f32_16x16x32_bf16 v[210:213], v[214:217], v[118:121], 0\n"
      "v_exp_f32 v189, v189\n"
      "v_mfma_f32_16x16x32_bf16 v[210:213], v[84:87], v[92:95], v[210:213]\n"
      "ds_read_b128 v[138:141], v2 offset:22016\n"
      "v_fmac_f32 v160, v180, v180\n"
      "v_mfma_f32_16x16x32_bf16 v[202:205], v[122:125], v[220:223], v[30:33]\n"
      "v_exp_f32 v177, v177\n"
      "v_mfma_f32_16x16x32_bf16 v[116:119], v[122:125], v[126:129], v[30:33]\n"
      "s_waitcnt lgkmcnt(1)\n"
      "v_exp_f32_e64 v175, v175\n"
      "v_mfma_f32_16x16x32_bf16 v[220:223], v[122:125], v[150:153], v[30:33]\n"
      "ds_read_b128 v[148:151], v2 offset:18944\n"
      "v_cvt_pk_bf16_f32 v186, v186, v187\n"
      "v_mfma_f32_16x16x32_bf16 v[156:159], v[122:125], v[190:193], v[30:33]\n"
      "ds_read_b128 v[190:193], v7 offset:26624\n"
      "v_fmac_f32_e64 v161, v181, v181\n"
      "v_mfma_f32_16x16x16_bf16 v[202:205], v[88:89], v[146:147], v[202:205]\n"
      "ds_read_b128 v[122:125], v2 offset:25088\n"
      "v_cvt_pk_bf16_f32 v90, v108, 0\n"
      "v_mfma_f32_16x16x16_bf16 v[116:119], v[88:89], v[106:107], v[116:119]\n"
      "buffer_store_dwordx4 v[110:113], v21, s[24:27], s46 offen sc1\n"
      "v_pk_mul_f32 v[98:99], v[98:99], v[114:115] op_sel_hi:[1,0]\n"
      "v_pk_mul_f32 v[210:211], v[210:211], v[114:115] op_sel_hi:[1,0]\n"
      "v_pk_mul_f32 v[136:137], v[136:137], v[114:115] op_sel_hi:[1,0]\n"
      "v_pk_mul_f32 v[212:213], v[212:213], v[114:115] op_sel_hi:[1,0]\n"
      "v_pk_fma_f32 v[218:219], v[200:201], v[200:201], v[218:219]\n"
      "v_pk_fma_f32 v[160:161], v[182:183], v[182:183], v[160:161]\n"
      "v_pk_fma_f32 v[160:161], v[170:171], v[170:171], v[160:161]\n"
      "v_pk_fma_f32 v[218:219], v[142:143], v[142:143], v[218:219]\n"
      "v_mfma_f32_16x16x16_bf16 v[220:223], v[88:89], v[102:103], v[220:223]\n"
      "v_exp_f32 v174, v174\n"
      "v_cvt_pk_bf16_f32 v187, v188, v189\n"
      "v_mfma_f32_16x16x16_bf16 v[156:159], v[88:89], v[96:97], v[156:159]\n"
      "v_fmac_f32 v219, v145, v145\n"
      "v_cvt_pk_bf16_f32 v210, v210, v211\n"
      "s_waitcnt lgkmcnt(0)\n"
      "v_mfma_f32_16x16x32_bf16 v[126:129], a[216:219], v[148:151], 0\n"
      "v_exp_f32 v209, v209\n"
      "v_mfma_f32_16x16x32_bf16 v[126:129], a[220:223], v[138:141], v[126:129]\n"
      "ds_read_b128 v[102:105], v7 offset:30720\n"
      "v_cndmask_b32 v202, v202, v116, s[58:59]\n"
      "v_mfma_f32_16x16x32_bf16 v[126:129], a[224:227], v[122:125], v[126:129]\n"
      "ds_write_b64 v4, v[154:155]\n"
      "v_mfma_f32_16x16x32_bf16 v[116:119], a[240:243], v[148:151], 0\n"
      "ds_read_b128 v[152:155], v2 offset:34304\n"
      "v_cvt_pk_bf16_f32 v89, v176, v177\n"
      "v_mfma_f32_16x16x32_bf16 v[116:119], a[244:247], v[138:141], v[116:119]\n"
      "buffer_store_dwordx4 v[190:193], v22, s[24:27], s46 offen sc1\n"
      "v_pk_fma_f32 v[160:161], v[172:173], v[172:173], v[160:161]\n"
      "v_mfma_f32_16x16x32_bf16 v[116:119], a[248:251], v[122:125], v[116:119]\n"
      "ds_read_b128 v[190:193], v2 offset:31232\n"
      "v_cndmask_b32 v202, v202, v220, s[60:61]\n"
      "v_mfma_f32_16x16x32_bf16 v[108:111], v[232:235], v[148:151], 0\n"
      "ds_read_b128 v[220:223], v2 offset:28160\n"
      "v_cndmask_b32 v202, v202, v156, s[62:63]\n"
      "v_mfma_f32_16x16x32_bf16 v[108:111], v[236:239], v[138:141], v[108:111]\n"
      "v_cvt_pk_bf16_f32 v189, v208, v209\n"
      "v_cvt_pk_bf16_f32 v188, v206, v207\n"
      "v_mfma_f32_16x16x32_bf16 v[108:111], v[240:243], v[122:125], v[108:111]\n"
      "v_cvt_pk_bf16_f32 v88, v174, v175\n"
      "v_add_f32 v160, v160, v161\n"
      "v_mfma_f32_16x16x32_bf16 v[206:209], v[32:35], v[186:189], 0\n"
      "v_exp_f32 v202, v202\n"
      "v_mfma_f32_16x16x32_bf16 v[206:209], v[32:35], v[88:91], v[206:209]\n"
      "v_cvt_pk_bf16_f32 v202, v202, v202\n"
      "v_fmac_f32_e64 v218, v144, v144\n"
      "v_mfma_f32_16x16x32_bf16 v[122:125], v[130:133], v[186:189], 0\n"
      "ds_write_b16 v13, v202\n"
      "v_mfma_f32_16x16x32_bf16 v[122:125], v[76:79], v[88:91], v[122:125]\n"
      "v_cvt_pk_bf16_f32 v211, v212, v213\n"
      "v_add_f32_e64 v218, v218, v219\n"
      "v_mfma_f32_16x16x32_bf16 v[148:151], v[194:197], v[186:189], 0\n"
      "ds_write_b64 v4, v[210:211] offset:256\n"
      "v_mfma_f32_16x16x32_bf16 v[148:151], v[80:83], v[88:91], v[148:151]\n"
      "v_rcp_f32 v206, v206\n"
      "v_mfma_f32_16x16x32_bf16 v[156:159], v[214:217], v[186:189], 0\n"
      "s_waitcnt lgkmcnt(2)\n"
      "v_permlane16_swap_b32_e64 v218, v160\n"
      "v_mfma_f32_16x16x32_bf16 v[156:159], v[84:87], v[88:91], v[156:159]\n"
      "ds_read_b128 v[174:177], v14\n"
      "v_cvt_pk_bf16_f32 v98, v98, v99\n"
      "v_mfma_f32_16x16x32_bf16 v[126:129], a[228:231], v[220:223], v[126:129]\n"
      "buffer_store_dwordx4 v[102:105], v23, s[24:27], s46 offen sc1\n"
      "v_pk_mul_f32 v[148:149], v[148:149], v[206:207] op_sel_hi:[1,0]\n"
      "v_pk_mul_f32 v[100:101], v[100:101], v[114:115] op_sel_hi:[1,0]\n"
      "v_pk_mul_f32 v[134:135], v[134:135], v[114:115] op_sel_hi:[1,0]\n"
      "v_pk_mul_f32 v[122:123], v[122:123], v[206:207] op_sel_hi:[1,0]\n"
      "v_mfma_f32_16x16x32_bf16 v[126:129], a[232:235], v[190:193], v[126:129]\n"
      "ds_read_b128 v[102:105], v7 offset:34816\n"
      "v_cvt_pk_bf16_f32 v99, v100, v101\n"
      "v_mfma_f32_16x16x32_bf16 v[126:129], a[236:239], v[152:155], v[126:129]\n"
      "v_cvt_pk_bf16_f32 v100, v134, v135\n"
      "v_cvt_pk_bf16_f32 v101, v136, v137\n"
      "v_mfma_f32_16x16x32_bf16 v[116:119], a[252:255], v[220:223], v[116:119]\n"
      "ds_write_b128 v3, v[98:101] offset:256\n"
      "v_pk_mul_f32 v[150:151], v[150:151], v[206:207] op_sel_hi:[1,0]\n"
      "v_pk_mul_f32 v[124:125], v[124:125], v[206:207] op_sel_hi:[1,0]\n"
      "v_mfma_f32_16x16x32_bf16 v[116:119], v[224:227], v[190:193], v[116:119]\n"
      "s_nop 0\n"
      "v_fma_f32 v136, v126, v126, s52\n"
      "v_cvt_pk_bf16_f32 v122, v122, v123\n"
      "v_mfma_f32_16x16x32_bf16 v[116:119], v[228:231], v[152:155], v[116:119]\n"
      "v_mul_f32 v137, v127, v127\n"
      "v_cvt_pk_bf16_f32 v123, v124, v125\n"
      "v_mfma_f32_16x16x32_bf16 v[108:111], v[244:247], v[220:223], v[108:111]\n"
      "ds_read_b128 v[210:213], v14 offset:64\n"
      "v_cvt_pk_bf16_f32 v124, v148, v149\n"
      "v_mfma_f32_16x16x32_bf16 v[108:111], v[248:251], v[190:193], v[108:111]\n"
      "s_waitcnt lgkmcnt(2)\n"
      "v_add_f32 v218, v218, v160\n"
      "v_cvt_pk_bf16_f32 v125, v150, v151\n"
      "v_mfma_f32_16x16x32_bf16 v[108:111], v[252:255], v[152:155], v[108:111]\n"
      "ds_write_b128 v3, v[122:125] offset:512\n"
      "v_pk_fma_f32 v[136:137], v[128:129], v[128:129], v[136:137]\n"
      "v_pk_fma_f32 v[136:137], v[116:117], v[116:117], v[136:137]\n"
      "v_pk_mul_f32 v[156:157], v[156:157], v[206:207] op_sel_hi:[1,0]\n"
      "v_cvt_pk_bf16_f32 v156, v156, v157\n"
      "v_pk_fma_f32 v[136:137], v[118:119], v[118:119], v[136:137]\n"
      "v_pk_mul_f32 v[158:159], v[158:159], v[206:207] op_sel_hi:[1,0]\n"
      "v_cvt_pk_bf16_f32 v157, v158, v159\n"
      "v_pk_fma_f32 v[136:137], v[108:109], v[108:109], v[136:137]\n"
      "v_pk_fma_f32 v[136:137], v[110:111], v[110:111], v[136:137]\n"
      "v_add_f32 v136, v136, v137\n"
      "v_mov_b32 v160, v136\n"
      "v_mfma_f32_16x16x32_bf16 v[188:191], v[174:177], v[32:35], 0\n"
      "ds_write_b64 v4, v[156:157] offset:512\n"
      "v_permlane16_swap_b32_e64 v136, v160\n"
      "v_add_f32_e64 v136, v136, v160\n"
      "s_cmp_ge_u32 s95, 0x40000000\n"
      "s_cselect_b64 vcc, -1, s[56:57]\n"
      "v_permlane32_swap_b32 v218, v136\n"
      "v_add_f32_e64 v218, v218, v136\n"
      "s_add_u32 s42, s42, 1\n"
      "ds_write_b32 v31, v218\n"
      "buffer_store_dwordx4 v[102:105], v25, s[24:27], s46 offen sc1\n"
      "s_waitcnt lgkmcnt(2)\n"
      "v_mfma_f32_16x16x32_bf16 v[188:191], v[210:213], v[32:35], v[188:191]\n"
      "s_waitcnt lgkmcnt(0)\n"
      "s_barrier\n"
      "s_cbranch_vccz L_loop1_%=\n"
      "s_branch L_disp1_%=\n"
      "s_nop 7\n"
      "s_nop 7\n"
      "L_max1_%=:\n"
      "v_mfma_f32_16x16x32_bf16 v[218:221], v[174:177], v[130:133], 0\n"
      "ds_read_b128 v[150:153], v0 offset:18432\n"
      "s_add_u32 m0, s64, 0\n"
      "v_mfma_f32_16x16x32_bf16 v[218:221], v[210:213], v[76:79], v[218:221]\n"
      "ds_read_b128 v[146:149], v9\n"
      "ds_read_b128 v[122:125], v0 offset:24576\n"
      "v_mfma_f32_16x16x32_bf16 v[134:137], v[174:177], v[194:197], 0\n"
      "ds_read_b128 v[96:99], v1 offset:18432\n"
      "v_readlane_b32 s45, v37, s43\n"
      "v_mfma_f32_16x16x32_bf16 v[134:137], v[210:213], v[80:83], v[134:137]\n"
      "buffer_load_dwordx4 v16, s[16:19], s84 offen  lds\n"
      "v_mfma_f32_16x16x32_bf16 v[100:103], v[174:177], v[214:217], 0\n"
      "ds_read_b128 v[220:223], v1 offset:24576\n"
      "v_readlane_b32 s46, v37, s42\n"
      "v_mfma_f32_16x16x32_bf16 v[100:103], v[210:213], v[84:87], v[100:103]\n"
      "ds_read_b128 v[174:177], v0 offset:30720\n"
      "s_waitcnt lgkmcnt(5)\n"
      "v_mfma_f32_16x16x32_bf16 v[210:213], a[0:3], v[150:153], 0\n"
      "ds_read_b128 v[138:141], v0 offset:26624\n"
      "v_cndmask_b32 v218, v218, v134, s[58:59]\n"
      "v_mfma_f32_16x16x32_bf16 v[112:115], a[24:27], v[150:153], 0\n"
      "s_waitcnt lgkmcnt(5)\n"
      "v_add_f32 v148, v148, v149\n"
      "v_add_f32 v146, v146, v147\n"
      "v_mfma_f32_16x16x32_bf16 v[202:205], a[48:51], v[150:153], 0\n"
      "s_add_u32 m0, s64, 4096\n"
      "v_mov_b32_dpp v188, v188 quad_perm:[0,0,0,0] row_mask:0xf bank_mask:0xf\n"
      "v_add_f32 v146, v146, v148\n"
      "v_mfma_f32_16x16x32_bf16 v[158:161], a[72:75], v[150:153], 0\n"
      "s_waitcnt lgkmcnt(3)\n"
      "v_rcp_f32_e64 v188, v188\n"
      "v_mfma_f32_16x16x32_bf16 v[154:157], a[96:99], v[150:153], 0\n"
      "buffer_load_dwordx4 v17, s[16:19], s84 offen  lds\n"
      "v_mfma_f32_16x16x32_bf16 v[184:187], a[120:123], v[150:153], 0\n"
      "s_add_u32 m0, s64, 8192\n"
      "v_rsq_f32_e64 v146, v146\n"
      "v_mfma_f32_16x16x32_bf16 v[210:213], a[4:7], v[96:99], v[210:213]\n"
      "ds_read_b128 v[206:209], v0 offset:20480\n"
      "v_mul_f32 v146, 0x415db3d7, v146\n"
      "v_mfma_f32_16x16x32_bf16 v[210:213], a[8:11], v[122:125], v[210:213]\n"
      "ds_read_b128 v[134:137], v1 offset:30720\n"
      "v_mov_b32 v148, v146\n"
      "v_mfma_f32_16x16x32_bf16 v[112:115], a[28:31], v[96:99], v[112:115]\n"
      "s_nop 0\n"
      "v_permlane32_swap_b32 v146, v148\n"
      "v_mfma_f32_16x16x32_bf16 v[112:115], a[32:35], v[122:125], v[112:115]\n"
      "v_mov_b32 v147, v146\n"
      "v_mul_f32_e64 v117, v148, v117\n"
      "v_mfma_f32_16x16x32_bf16 v[202:205], a[52:55], v[96:99], v[202:205]\n"
      "buffer_load_dwordx4 v18, s[16:19], s84 offen  lds\n"
      "v_mfma_f32_16x16x32_bf16 v[202:205], a[56:59], v[122:125], v[202:205]\n"
      "s_or_b32 s95, s45, s46\n"
      "v_permlane16_swap_b32 v146, v147\n"
      "v_mfma_f32_16x16x32_bf16 v[158:161], a[76:79], v[96:99], v[158:161]\n"
      "s_add_u32 m0, s64, 12288\n"
      "v_mul_f32 v142, v146, v142\n"
      "v_mul_f32 v201, v146, v201\n"
      "v_mfma_f32_16x16x32_bf16 v[158:161], a[80:83], v[122:125], v[158:161]\n"
      "v_mul_f32 v169, v146, v169\n"
      "v_mul_f32 v143, v146, v143\n"
      "v_mfma_f32_16x16x32_bf16 v[154:157], a[100:103], v[96:99], v[154:157]\n"
      "v_cvt_pk_bf16_f32 v142, v142, v143\n"
      "v_mul_f32_e64 v145, v146, v145\n"
      "v_mfma_f32_16x16x32_bf16 v[154:157], a[104:107], v[122:125], v[154:157]\n"
      "buffer_load_dwordx4 v19, s[16:19], s84 offen  lds\n"
      "v_mfma_f32_16x16x32_bf16 v[184:187], a[124:127], v[96:99], v[184:187]\n"
      "s_waitcnt lgkmcnt(0)\n"
      "v_mul_f32 v167, v146, v167\n"
      "v_mul_f32_e64 v166, v146, v166\n"
      "v_mfma_f32_16x16x32_bf16 v[184:187], a[128:131], v[122:125], v[184:187]\n"
      "ds_read_b128 v[104:107], v1 offset:26624\n"
      "v_mul_f32 v144, v146, v144\n"
      "v_mfma_f32_16x16x32_bf16 v[210:213], a[12:15], v[220:223], v[210:213]\n"
      "v_mul_f32 v199, v146, v199\n"
      "v_mul_f32 v168, v146, v168\n"
      "v_mfma_f32_16x16x32_bf16 v[210:213], a[16:19], v[174:177], v[210:213]\n"
      "s_add_u32 m0, s65, 16384\n"
      "v_cvt_pk_bf16_f32 v166, v166, v167\n"
      "v_mul_f32 v200, v146, v200\n"
      "v_mfma_f32_16x16x32_bf16 v[210:213], a[20:23], v[134:137], v[210:213]\n"
      "buffer_load_dwordx4 v15, s[16:19], s84 offen  lds\n"
      "v_mfma_f32_16x16x32_bf16 v[112:115], a[36:39], v[220:223], v[112:115]\n"
      "v_cvt_pk_bf16_f32 v167, v168, v169\n"
      "v_mul_f32 v198, v146, v198\n"
      "v_mfma_f32_16x16x32_bf16 v[112:115], a[40:43], v[174:177], v[112:115]\n"
      "s_or_b32 s95, s95, s48\n"
      "v_cvt_pk_bf16_f32 v169, v200, v201\n"
      "v_cvt_pk_bf16_f32 v168, v198, v199\n"
      "v_mfma_f32_16x16x32_bf16 v[112:115], a[44:47], v[134:137], v[112:115]\n"
      "ds_write_b128 v5, v[166:169] offset:18432\n"
      "v_pk_mul_f32 v[170:171], v[170:171], v[146:147] op_sel:[0,1]\n"
      "v_pk_mul_f32 v[162:163], v[162:163], v[146:147] op_sel:[0,1]\n"
      "v_pk_mul_f32 v[164:165], v[164:165], v[146:147] op_sel:[0,1]\n"
      "v_mfma_f32_16x16x32_bf16 v[202:205], a[60:63], v[220:223], v[202:205]\n"
      "buffer_load_dwordx4 v[190:193], v36, s[28:31], s86 offen\n"
      "v_mfma_f32_16x16x32_bf16 v[202:205], a[64:67], v[174:177], v[202:205]\n"
      "s_cmp_ge_u32 s95, 0x40000000\n"
      "s_cselect_b32 s94, -1, -2\n"
      "v_cvt_pk_bf16_f32 v143, v144, v145\n"
      "v_mfma_f32_16x16x32_bf16 v[202:205], a[68:71], v[134:137], v[202:205]\n"
      "v_mul_f32_e64 v180, v147, v180\n"
      "v_cvt_pk_bf16_f32 v162, v162, v163\n"
      "v_mfma_f32_16x16x32_bf16 v[158:161], a[84:87], v[220:223], v[158:161]\n"
      "v_mul_f32 v109, v148, v109\n"
      "v_mul_f32 v183, v147, v183\n"
      "v_mfma_f32_16x16x32_bf16 v[158:161], a[88:91], v[174:177], v[158:161]\n"
      "ds_write_b64 v6, v[142:143] offset:18432\n"
      "v_mfma_f32_16x16x32_bf16 v[158:161], a[92:95], v[134:137], v[158:161]\n"
      "v_cvt_pk_bf16_f32 v163, v164, v165\n"
      "v_mul_f32_e64 v111, v148, v111\n"
      "v_mfma_f32_16x16x32_bf16 v[154:157], a[108:111], v[220:223], v[154:157]\n"
      "buffer_load_dword v219, v29, s[28:31], s86 offen\n"
      "v_mfma_f32_16x16x32_bf16 v[154:157], a[112:115], v[174:177], v[154:157]\n"
      "v_mul_f32 v181, v147, v181\n"
      "v_mul_f32 v172, v147, v172\n"
      "v_mfma_f32_16x16x32_bf16 v[154:157], a[116:119], v[134:137], v[154:157]\n"
      "ds_read_b128 v[142:145], v1 offset:28672\n"
      "v_fma_f32 v189, v158, v158, s51\n"
      "v_mfma_f32_16x16x32_bf16 v[184:187], a[132:135], v[220:223], v[184:187]\n"
      "v_cvt_pk_bf16_f32 v164, v180, v181\n"
      "v_mul_f32 v129, v148, v129\n"
      "v_mfma_f32_16x16x32_bf16 v[184:187], a[136:139], v[174:177], v[184:187]\n"
      "v_mul_f32 v182, v147, v182\n"
      "v_fma_f32 v180, v210, v210, s51\n"
      "v_mfma_f32_16x16x32_bf16 v[184:187], a[140:143], v[134:137], v[184:187]\n"
      "v_cvt_pk_bf16_f32 v165, v182, v183\n"
      "v_fmac_f32_e64 v189, v159, v159\n"
      "v_mfma_f32_16x16x32_bf16 v[130:133], v[150:153], a[144:147], 0\n"
      "ds_write_b128 v5, v[162:165] offset:24576\n"
      "v_mfma_f32_16x16x32_bf16 v[130:133], v[96:99], a[148:151], v[130:133]\n"
      "v_fmac_f32_e64 v180, v211, v211\n"
      "v_cvt_pk_bf16_f32 v170, v170, v171\n"
      "v_mfma_f32_16x16x32_bf16 v[130:133], v[122:125], a[152:155], v[130:133]\n"
      "v_mul_f32 v108, v148, v108\n"
      "v_mul_f32 v173, v147, v173\n"
      "v_mfma_f32_16x16x32_bf16 v[130:133], v[220:223], a[156:159], v[130:133]\n"
      "ds_read_b128 v[162:165], v0 offset:32768\n"
      "v_cvt_pk_bf16_f32 v171, v172, v173\n"
      "v_mfma_f32_16x16x32_bf16 v[130:133], v[174:177], a[160:163], v[130:133]\n"
      "ds_write_b64 v6, v[170:171] offset:24576\n"
      "v_mfma_f32_16x16x32_bf16 v[130:133], v[134:137], a[164:167], v[130:133]\n"
      "v_fmac_f32 v189, v160, v160\n"
      "v_mul_f32 v128, v148, v128\n"
      "v_mfma_f32_16x16x32_bf16 v[194:197], v[150:153], a[168:171], 0\n"
      "v_cvt_pk_bf16_f32 v108, v108, v109\n"
      "v_cndmask_b32 v218, v218, v100, s[60:61]\n"
      "v_mfma_f32_16x16x32_bf16 v[194:197], v[96:99], a[172:175], v[194:197]\n"
      "ds_read_b128 v[100:103], v1 offset:32768\n"
      "v_mul_f32 v118, v148, v118\n"
      "v_mfma_f32_16x16x32_bf16 v[194:197], v[122:125], a[176:179], v[194:197]\n"
      "v_fmac_f32 v180, v212, v212\n"
      "v_fmac_f32 v180, v213, v213\n"
      "v_mfma_f32_16x16x32_bf16 v[194:197], v[220:223], a[180:183], v[194:197]\n"
      "v_mul_f32 v218, v188, v218\n"
      "v_mul_f32 v127, v148, v127\n"
      "v_mfma_f32_16x16x32_bf16 v[194:197], v[174:177], a[184:187], v[194:197]\n"
      "v_fmac_f32 v189, v161, v161\n"
      "v_fmac_f32 v180, v112, v112\n"
      "v_mfma_f32_16x16x32_bf16 v[194:197], v[134:137], a[188:191], v[194:197]\n"
      "ds_read_b128 v[198:201], v1 offset:20480\n"
      "v_mul_f32 v110, v148, v110\n"
      "v_mfma_f32_16x16x32_bf16 v[214:217], v[150:153], a[192:195], 0\n"
      "v_fmac_f32 v180, v113, v113\n"
      "v_fmac_f32 v189, v154, v154\n"
      "v_mfma_f32_16x16x32_bf16 v[214:217], v[96:99], a[196:199], v[214:217]\n"
      "ds_read_b128 v[150:153], v7\n"
      "v_mul_f32_e64 v126, v148, v126\n"
      "v_mfma_f32_16x16x32_bf16 v[214:217], v[122:125], a[200:203], v[214:217]\n"
      "v_fmac_f32 v189, v155, v155\n"
      "v_cvt_pk_bf16_f32 v126, v126, v127\n"
      "v_mfma_f32_16x16x32_bf16 v[214:217], v[220:223], a[204:207], v[214:217]\n"
      "v_mul_f32 v116, v148, v116\n"
      "v_cvt_pk_bf16_f32 v127, v128, v129\n"
      "v_mfma_f32_16x16x32_bf16 v[214:217], v[174:177], a[208:211], v[214:217]\n"
      "v_fmac_f32 v189, v156, v156\n"
      "v_fmac_f32 v189, v157, v157\n"
      "v_mfma_f32_16x16x32_bf16 v[214:217], v[134:137], a[212:215], v[214:217]\n"
      "ds_read_b128 v[166:169], v9 offset:4608\n"
      "v_cvt_pk_bf16_f32 v128, v116, v117\n"
      "v_mfma_f32_16x16x32_bf16 v[122:125], a[0:3], v[206:209], 0\n"
      "s_waitcnt lgkmcnt(2)\n"
      "v_mul_f32 v119, v148, v119\n"
      "v_cvt_pk_bf16_f32 v109, v110, v111\n"
      "v_mfma_f32_16x16x32_bf16 v[122:125], a[4:7], v[198:201], v[122:125]\n"
      "v_fmac_f32 v189, v184, v184\n"
      "v_fmac_f32 v180, v114, v114\n"
      "v_mfma_f32_16x16x32_bf16 v[122:125], a[8:11], v[138:141], v[122:125]\n"
      "s_cmp_gt_u32 s42, s44\n"
      "s_cselect_b32 s95, s94, s95\n"
      "v_fmac_f32 v180, v115, v115\n"
      "v_mfma_f32_16x16x32_bf16 v[122:125], a[12:15], v[104:107], v[122:125]\n"
      "v_fmac_f32 v189, v185, v185\n"
      "v_fmac_f32_e64 v189, v186, v186\n"
      "v_mfma_f32_16x16x32_bf16 v[122:125], a[16:19], v[162:165], v[122:125]\n"
      "v_cvt_pk_bf16_f32 v218, v218, v218\n"
      "v_cvt_pk_bf16_f32 v129, v118, v119\n"
      "v_mfma_f32_16x16x32_bf16 v[122:125], a[20:23], v[100:103], v[122:125]\n"
      "v_fmac_f32 v180, v202, v202\n"
      "v_fmac_f32 v180, v203, v203\n"
      "v_mfma_f32_16x16x32_bf16 v[118:121], a[24:27], v[206:209], 0\n"
      "ds_read_b128 v[174:177], v1 offset:22528\n"
      "v_cvt_pk_bf16_f32 v214, v214, v215\n"
      "v_mfma_f32_16x16x32_bf16 v[118:121], a[28:31], v[198:201], v[118:121]\n"
      "ds_write_b128 v5, v[126:129] offset:30720\n"
      "v_mfma_f32_16x16x32_bf16 v[118:121], a[32:35], v[138:141], v[118:121]\n"
      "s_waitcnt lgkmcnt(2)\n"
      "v_fmac_f32 v180, v204, v204\n"
      "v_or3_b32 v166, v166, v167, v168\n"
      "v_mfma_f32_16x16x32_bf16 v[118:121], a[36:39], v[104:107], v[118:121]\n"
      "v_fmac_f32 v189, v187, v187\n"
      "v_fmac_f32 v180, v205, v205\n"
      "v_mfma_f32_16x16x32_bf16 v[118:121], a[40:43], v[162:165], v[118:121]\n"
      "s_nop 0\n"
      "v_permlane16_swap_b32 v180, v189\n"
      "v_mfma_f32_16x16x32_bf16 v[118:121], a[44:47], v[100:103], v[118:121]\n"
      "ds_read_b128 v[220:223], v1 offset:34816\n"
      "v_add_f32_e64 v180, v180, v189\n"
      "v_mfma_f32_16x16x32_bf16 v[126:129], a[48:51], v[206:209], 0\n"
      "v_fma_f32 v182, v122, v122, s51\n"
      "v_mov_b32 v189, v180\n"
      "v_mfma_f32_16x16x32_bf16 v[126:129], a[52:55], v[198:201], v[126:129]\n"
      "s_nop 0\n"
      "v_permlane32_swap_b32 v180, v189\n"
      "v_mfma_f32_16x16x32_bf16 v[126:129], a[56:59], v[138:141], v[126:129]\n"
      "ds_read_b128 v[134:137], v0 offset:22528\n"
      "v_add_f32 v180, v180, v189\n"
      "v_mfma_f32_16x16x32_bf16 v[126:129], a[60:63], v[104:107], v[126:129]\n"
      "v_rsq_f32 v180, v180\n"
      "v_mfma_f32_16x16x32_bf16 v[126:129], a[64:67], v[162:165], v[126:129]\n"
      "v_bitop3_b32 v166, v166, v169, s33 bitop3:0xa8\n"
      "v_mov_b32 v189, v180\n"
      "v_mfma_f32_16x16x32_bf16 v[126:129], a[68:71], v[100:103], v[126:129]\n"
      "s_nop 0\n"
      "v_permlane16_swap_b32 v180, v189\n"
      "v_mfma_f32_16x16x32_bf16 v[96:99], a[72:75], v[206:209], 0\n"
      "ds_write_b64 v6, v[108:109] offset:30720\n"
      "v_mfma_f32_16x16x32_bf16 v[96:99], a[76:79], v[198:201], v[96:99]\n"
      "v_mul_f32 v180, s50, v180\n"
      "v_mul_f32 v112, v180, v112\n"
      "v_mfma_f32_16x16x32_bf16 v[96:99], a[80:83], v[138:141], v[96:99]\n"
      "v_cmp_ne_u32 s[56:57], 0, v166\n"
      "v_mul_f32_e64 v211, v180, v211\n"
      "v_mfma_f32_16x16x32_bf16 v[96:99], a[84:87], v[104:107], v[96:99]\n"
      "v_mul_f32 v210, v180, v210\n"
      "v_cvt_pk_bf16_f32 v130, v130, v131\n"
      "v_mfma_f32_16x16x32_bf16 v[96:99], a[88:91], v[162:165], v[96:99]\n"
      "v_mul_f32 v160, v189, v160\n"
      "v_mul_f32 v114, v180, v114\n"
      "v_mfma_f32_16x16x32_bf16 v[96:99], a[92:95], v[100:103], v[96:99]\n"
      "v_cvt_pk_bf16_f32 v210, v210, v211\n"
      "v_fmac_f32 v182, v123, v123\n"
      "v_mfma_f32_16x16x32_bf16 v[170:173], a[96:99], v[206:209], 0\n"
      "v_mul_f32 v186, v189, v186\n"
      "v_mul_f32 v155, v189, v155\n"
      "v_mfma_f32_16x16x32_bf16 v[170:173], a[100:103], v[198:201], v[170:173]\n"
      "v_cvt_pk_bf16_f32 v215, v216, v217\n"
      "v_mul_f32_e64 v203, v180, v203\n"
      "v_mfma_f32_16x16x32_bf16 v[170:173], a[104:107], v[138:141], v[170:173]\n"
      "ds_write_b16 v12, v218\n"
      "v_mfma_f32_16x16x32_bf16 v[170:173], a[108:111], v[104:107], v[170:173]\n"
      "v_mul_f32 v205, v180, v205\n"
      "v_mul_f32 v213, v180, v213\n"
      "v_mfma_f32_16x16x32_bf16 v[170:173], a[112:115], v[162:165], v[170:173]\n"
      "v_mul_f32_e64 v202, v180, v202\n"
      "v_readlane_b32 s47, v38, s43\n"
      "v_mfma_f32_16x16x32_bf16 v[170:173], a[116:119], v[100:103], v[170:173]\n"
      "v_fma_f32 v217, v96, v96, s51\n"
      "v_cvt_pk_bf16_f32 v131, v132, v133\n"
      "v_mfma_f32_16x16x32_bf16 v[108:111], a[120:123], v[206:209], 0\n"
      "v_cvt_pk_bf16_f32 v194, v194, v195\n"
      "v_mul_f32 v212, v180, v212\n"
      "v_mfma_f32_16x16x32_bf16 v[108:111], a[124:127], v[198:201], v[108:111]\n"
      "v_cvt_pk_bf16_f32 v195, v196, v197\n"
      "v_fmac_f32 v217, v97, v97\n"
      "v_mfma_f32_16x16x32_bf16 v[108:111], a[128:131], v[138:141], v[108:111]\n"
      "v_fmac_f32 v182, v124, v124\n"
      "v_mul_f32 v115, v180, v115\n"
      "v_mfma_f32_16x16x32_bf16 v[108:111], a[132:135], v[104:107], v[108:111]\n"
      "v_fmac_f32 v182, v125, v125\n"
      "v_fmac_f32 v182, v118, v118\n"
      "v_mfma_f32_16x16x32_bf16 v[108:111], a[136:139], v[162:165], v[108:111]\n"
      "v_fmac_f32 v217, v98, v98\n"
      "v_fmac_f32 v182, v119, v119\n"
      "v_mfma_f32_16x16x32_bf16 v[108:111], a[140:143], v[100:103], v[108:111]\n"
      "s_cmp_ge_u32 s95, 0x40000000\n"
      "s_cselect_b64 vcc, -1, s[56:57]\n"
      "v_fmac_f32_e64 v182, v120, v120\n"
      "v_mfma_f32_16x16x32_bf16 v[166:169], v[206:209], a[144:147], 0\n"
      "v_fmac_f32 v182, v121, v121\n"
      "v_fmac_f32 v217, v99, v99\n"
      "v_mfma_f32_16x16x32_bf16 v[166:169], v[198:201], a[148:151], v[166:169]\n"
      "v_mul_f32 v113, v180, v113\n"
      "v_mul_f32 v185, v189, v185\n"
      "v_mfma_f32_16x16x32_bf16 v[166:169], v[138:141], a[152:155], v[166:169]\n"
      "v_mul_f32 v154, v189, v154\n"
      "v_mul_f32 v204, v180, v204\n"
      "v_mfma_f32_16x16x32_bf16 v[166:169], v[104:107], a[156:159], v[166:169]\n"
      "v_cvt_pk_bf16_f32 v211, v212, v213\n"
      "v_fmac_f32 v182, v126, v126\n"
      "v_mfma_f32_16x16x32_bf16 v[166:169], v[162:165], a[160:163], v[166:169]\n"
      "v_fmac_f32 v217, v170, v170\n"
      "v_mul_f32 v159, v189, v159\n"
      "v_mfma_f32_16x16x32_bf16 v[166:169], v[100:103], a[164:167], v[166:169]\n"
      "v_mul_f32 v158, v189, v158\n"
      "v_mul_f32 v161, v189, v161\n"
      "v_mfma_f32_16x16x32_bf16 v[178:181], v[206:209], a[168:171], 0\n"
      "v_cvt_pk_bf16_f32 v158, v158, v159\n"
      "v_mul_f32 v184, v189, v184\n"
      "v_mfma_f32_16x16x32_bf16 v[178:181], v[198:201], a[172:175], v[178:181]\n"
      "v_cvt_pk_bf16_f32 v213, v114, v115\n"
      "v_fmac_f32 v217, v171, v171\n"
      "v_mfma_f32_16x16x32_bf16 v[178:181], v[138:141], a[176:179], v[178:181]\n"
      "v_mul_f32 v156, v189, v156\n"
      "v_fmac_f32_e64 v217, v172, v172\n"
      "v_mfma_f32_16x16x32_bf16 v[178:181], v[104:107], a[180:183], v[178:181]\n"
      "v_fmac_f32 v182, v127, v127\n"
      "v_fmac_f32 v182, v128, v128\n"
      "v_mfma_f32_16x16x32_bf16 v[178:181], v[162:165], a[184:187], v[178:181]\n"
      "v_fmac_f32 v217, v173, v173\n"
      "v_fmac_f32 v217, v108, v108\n"
      "v_mfma_f32_16x16x32_bf16 v[178:181], v[100:103], a[188:191], v[178:181]\n"
      "v_fmac_f32 v217, v109, v109\n"
      "v_cvt_pk_bf16_f32 v202, v202, v203\n"
      "v_mfma_f32_16x16x32_bf16 v[114:117], v[206:209], a[192:195], 0\n"
      "ds_read_b128 v[146:149], v0 offset:28672\n"
      "v_fmac_f32 v217, v110, v110\n"
      "v_mfma_f32_16x16x32_bf16 v[114:117], v[198:201], a[196:199], v[114:117]\n"
      "ds_read_b128 v[206:209], v7 offset:4096\n"
      "v_cvt_pk_bf16_f32 v212, v112, v113\n"
      "v_mfma_f32_16x16x32_bf16 v[114:117], v[138:141], a[200:203], v[114:117]\n"
      "ds_read_b128 v[198:201], v0 offset:34816\n"
      "v_fmac_f32 v182, v129, v129\n"
      "v_mfma_f32_16x16x32_bf16 v[114:117], v[104:107], a[204:207], v[114:117]\n"
      "v_fmac_f32 v217, v111, v111\n"
      "v_cvt_pk_bf16_f32 v159, v160, v161\n"
      "v_mfma_f32_16x16x32_bf16 v[114:117], v[162:165], a[208:211], v[114:117]\n"
      "v_permlane16_swap_b32 v182, v217\n"
      "v_mfma_f32_16x16x32_bf16 v[114:117], v[100:103], a[212:215], v[114:117]\n"
      "s_waitcnt lgkmcnt(3)\n"
      "v_add_f32 v182, v182, v217\n"
      "v_mov_b32 v217, v182\n"
      "v_mfma_f32_16x16x32_bf16 v[104:107], a[0:3], v[134:137], 0\n"
      "s_nop 0\n"
      "v_permlane32_swap_b32 v182, v217\n"
      "v_mfma_f32_16x16x32_bf16 v[104:107], a[4:7], v[174:177], v[104:107]\n"
      "s_waitcnt lgkmcnt(1)\n"
      "v_add_f32 v182, v182, v217\n"
      "v_cvt_pk_bf16_f32 v197, v180, v181\n"
      "v_mfma_f32_16x16x32_bf16 v[104:107], a[8:11], v[146:149], v[104:107]\n"
      "v_rsq_f32_e64 v182, v182\n"
      "v_mfma_f32_16x16x32_bf16 v[104:107], a[12:15], v[142:145], v[104:107]\n"
      "s_waitcnt lgkmcnt(0)\n"
      "v_mov_b32 v217, v182\n"
      "v_cvt_pk_bf16_f32 v133, v168, v169\n"
      "v_mfma_f32_16x16x32_bf16 v[104:107], a[16:19], v[198:201], v[104:107]\n"
      "v_permlane16_swap_b32 v182, v217\n"
      "v_mfma_f32_16x16x32_bf16 v[104:107], a[20:23], v[220:223], v[104:107]\n"
      "v_mul_f32 v109, v217, v109\n"
      "v_cvt_pk_bf16_f32 v132, v166, v167\n"
      "v_mfma_f32_16x16x32_bf16 v[162:165], a[24:27], v[134:137], 0\n"
      "v_mul_f32 v108, v217, v108\n"
      "v_mul_f32 v157, v189, v157\n"
      "v_mfma_f32_16x16x32_bf16 v[162:165], a[28:31], v[174:177], v[162:165]\n"
      "v_mul_f32_e64 v182, s50, v182\n"
      "v_cvt_pk_bf16_f32 v160, v154, v155\n"
      "v_mfma_f32_16x16x32_bf16 v[162:165], a[32:35], v[146:149], v[162:165]\n"
      "v_fma_f32 v218, v104, v104, s51\n"
      "v_mul_f32 v110, v217, v110\n"
      "v_mfma_f32_16x16x32_bf16 v[162:165], a[36:39], v[142:145], v[162:165]\n"
      "v_mul_f32 v96, v217, v96\n"
      "v_mul_f32 v126, v182, v126\n"
      "v_mfma_f32_16x16x32_bf16 v[162:165], a[40:43], v[198:201], v[162:165]\n"
      "v_mul_f32 v172, v217, v172\n"
      "v_mul_f32 v123, v182, v123\n"
      "v_mfma_f32_16x16x32_bf16 v[162:165], a[44:47], v[220:223], v[162:165]\n"
      "v_mul_f32 v187, v189, v187\n"
      "v_cvt_pk_bf16_f32 v216, v114, v115\n"
      "v_mfma_f32_16x16x32_bf16 v[112:115], a[48:51], v[134:137], 0\n"
      "v_mul_f32 v124, v182, v124\n"
      "v_fmac_f32 v218, v105, v105\n"
      "v_mfma_f32_16x16x32_bf16 v[112:115], a[52:55], v[174:177], v[112:115]\n"
      "v_fmac_f32 v218, v106, v106\n"
      "v_fmac_f32 v218, v107, v107\n"
      "v_mfma_f32_16x16x32_bf16 v[112:115], a[56:59], v[146:149], v[112:115]\n"
      "v_cvt_pk_bf16_f32 v161, v156, v157\n"
      "v_mul_f32_e64 v97, v217, v97\n"
      "v_mfma_f32_16x16x32_bf16 v[112:115], a[60:63], v[142:145], v[112:115]\n"
      "v_mul_f32_e64 v120, v182, v120\n"
      "v_cvt_pk_bf16_f32 v184, v184, v185\n"
      "v_mfma_f32_16x16x32_bf16 v[112:115], a[64:67], v[198:201], v[112:115]\n"
      "ds_read_b128 v[154:157], v7 offset:8192\n"
      "v_cvt_pk_bf16_f32 v96, v96, v97\n"
      "v_mfma_f32_16x16x32_bf16 v[112:115], a[68:71], v[220:223], v[112:115]\n"
      "v_fmac_f32 v218, v162, v162\n"
      "v_mul_f32 v99, v217, v99\n"
      "v_mul_f32_e64 v118, v182, v118\n"
      "v_mfma_f32_16x16x32_bf16 v[100:103], a[72:75], v[134:137], 0\n"
      "buffer_store_dwordx4 v[150:153], v20, s[24:27], s85 offen sc1\n"
      "v_pk_mul_f32 v[170:171], v[170:171], v[216:217] op_sel:[0,1]\n"
      "v_mfma_f32_16x16x32_bf16 v[100:103], a[76:79], v[174:177], v[100:103]\n"
      "v_mul_f32 v111, v217, v111\n"
      "v_cvt_pk_bf16_f32 v203, v204, v205\n"
      "v_mfma_f32_16x16x32_bf16 v[100:103], a[80:83], v[146:149], v[100:103]\n"
      "v_mul_f32 v173, v217, v173\n"
      "v_mul_f32 v98, v217, v98\n"
      "v_mfma_f32_16x16x32_bf16 v[100:103], a[84:87], v[142:145], v[100:103]\n"
      "v_cvt_pk_bf16_f32 v185, v186, v187\n"
      "v_mul_f32 v125, v182, v125\n"
      "v_mfma_f32_16x16x32_bf16 v[100:103], a[88:91], v[198:201], v[100:103]\n"
      "ds_read_b128 v[166:169], v7 offset:16384\n"
      "v_cvt_pk_bf16_f32 v97, v98, v99\n"
      "v_mfma_f32_16x16x32_bf16 v[100:103], a[92:95], v[220:223], v[100:103]\n"
      "v_mul_f32 v127, v182, v127\n"
      "v_fmac_f32 v218, v163, v163\n"
      "v_mfma_f32_16x16x32_bf16 v[150:153], a[96:99], v[134:137], 0\n"
      "ds_read_b128 v[138:141], v7 offset:12288\n"
      "v_mul_f32_e64 v122, v182, v122\n"
      "v_mfma_f32_16x16x32_bf16 v[150:153], a[100:103], v[174:177], v[150:153]\n"
      "v_cvt_pk_bf16_f32 v122, v122, v123\n"
      "v_cvt_pk_bf16_f32 v123, v124, v125\n"
      "v_mfma_f32_16x16x32_bf16 v[150:153], a[104:107], v[146:149], v[150:153]\n"
      "v_fmac_f32_e64 v218, v164, v164\n"
      "v_fma_f32 v205, v100, v100, s51\n"
      "v_mfma_f32_16x16x32_bf16 v[150:153], a[108:111], v[142:145], v[150:153]\n"
      "buffer_store_dwordx4 v[206:209], v21, s[24:27], s85 offen sc1\n"
      "v_mfma_f32_16x16x32_bf16 v[150:153], a[112:115], v[198:201], v[150:153]\n"
      "v_cvt_pk_bf16_f32 v98, v170, v171\n"
      "v_cvt_pk_bf16_f32 v126, v126, v127\n"
      "v_mfma_f32_16x16x32_bf16 v[150:153], a[116:119], v[220:223], v[150:153]\n"
      "v_mul_f32 v119, v182, v119\n"
      "v_fmac_f32 v205, v101, v101\n"
      "v_mfma_f32_16x16x32_bf16 v[206:209], a[120:123], v[134:137], 0\n"
      "v_cvt_pk_bf16_f32 v108, v108, v109\n"
      "v_fmac_f32 v205, v102, v102\n"
      "v_mfma_f32_16x16x32_bf16 v[206:209], a[124:127], v[174:177], v[206:209]\n"
      "v_fmac_f32 v205, v103, v103\n"
      "v_cvt_pk_bf16_f32 v124, v118, v119\n"
      "v_mfma_f32_16x16x32_bf16 v[206:209], a[128:131], v[146:149], v[206:209]\n"
      "v_cvt_pk_bf16_f32 v217, v116, v117\n"
      "v_fmac_f32_e64 v218, v165, v165\n"
      "v_mfma_f32_16x16x32_bf16 v[206:209], a[132:135], v[142:145], v[206:209]\n"
      "v_fmac_f32 v205, v150, v150\n"
      "v_fmac_f32 v218, v112, v112\n"
      "v_mfma_f32_16x16x32_bf16 v[206:209], a[136:139], v[198:201], v[206:209]\n"
      "s_waitcnt lgkmcnt(0)\n"
      "v_fmac_f32 v218, v113, v113\n"
      "v_fmac_f32 v218, v114, v114\n"
      "v_mfma_f32_16x16x32_bf16 v[206:209], a[140:143], v[220:223], v[206:209]\n"
      "buffer_store_dwordx4 v[154:157], v22, s[24:27], s85 offen sc1\n"
      "v_mfma_f32_16x16x32_bf16 v[186:189], v[134:137], a[144:147], 0\n"
      "v_fmac_f32 v205, v151, v151\n"
      "v_fmac_f32 v205, v152, v152\n"
      "v_mfma_f32_16x16x32_bf16 v[186:189], v[174:177], a[148:151], v[186:189]\n"
      "v_fmac_f32 v205, v153, v153\n"
      "v_fmac_f32 v218, v115, v115\n"
      "v_mfma_f32_16x16x32_bf16 v[186:189], v[146:149], a[152:155], v[186:189]\n"
      "v_fmac_f32 v205, v206, v206\n"
      "v_fmac_f32 v205, v207, v207\n"
      "v_mfma_f32_16x16x32_bf16 v[186:189], v[142:145], a[156:159], v[186:189]\n"
      "v_fmac_f32 v205, v208, v208\n"
      "v_fmac_f32 v205, v209, v209\n"
      "v_mfma_f32_16x16x32_bf16 v[186:189], v[198:201], a[160:163], v[186:189]\n"
      "s_nop 0\n"
      "v_permlane16_swap_b32 v218, v205\n"
      "v_mfma_f32_16x16x32_bf16 v[186:189], v[220:223], a[164:167], v[186:189]\n"
      "v_add_f32 v218, v218, v205\n"
      "v_mov_b32 v205, v218\n"
      "v_mfma_f32_16x16x32_bf16 v[154:157], v[134:137], a[168:171], 0\n"
      "s_nop 0\n"
      "v_permlane32_swap_b32_e64 v218, v205\n"
      "v_mfma_f32_16x16x32_bf16 v[154:157], v[174:177], a[172:175], v[154:157]\n"
      "buffer_store_dwordx4 v[138:141], v23, s[24:27], s85 offen sc1\n"
      "v_pk_mul_f32 v[128:129], v[128:129], v[182:183] op_sel_hi:[1,0]\n"
      "v_mfma_f32_16x16x32_bf16 v[154:157], v[146:149], a[176:179], v[154:157]\n"
      "ds_read_b128 v[116:119], v2 offset:6144\n"
      "v_add_f32 v218, v218, v205\n"
      "v_mfma_f32_16x16x32_bf16 v[154:157], v[142:145], a[180:183], v[154:157]\n"
      "v_rsq_f32 v218, v218\n"
      "v_mfma_f32_16x16x32_bf16 v[154:157], v[198:201], a[184:187], v[154:157]\n"
      "v_cvt_pk_bf16_f32 v127, v128, v129\n"
      "v_cvt_pk_bf16_f32 v99, v172, v173\n"
      "v_mfma_f32_16x16x32_bf16 v[154:157], v[220:223], a[188:191], v[154:157]\n"
      "ds_read_b128 v[138:141], v10 offset:1200\n"
      "v_cvt_pk_bf16_f32 v196, v178, v179\n"
      "v_mfma_f32_16x16x32_bf16 v[170:173], v[134:137], a[192:195], 0\n"
      "ds_read_b128 v[134:137], v2\n"
      "v_mov_b32 v205, v218\n"
      "v_mfma_f32_16x16x32_bf16 v[170:173], v[174:177], a[196:199], v[170:173]\n"
      "s_nop 0\n"
      "v_permlane16_swap_b32_e64 v218, v205\n"
      "v_mfma_f32_16x16x32_bf16 v[170:173], v[146:149], a[200:203], v[170:173]\n"
      "ds_read_b128 v[146:149], v2 offset:3072\n"
      "v_mul_f32_e64 v218, s50, v218\n"
      "v_mfma_f32_16x16x32_bf16 v[170:173], v[142:145], a[204:207], v[170:173]\n"
      "ds_read_u16 v86, v11 offset:1232\n"
      "v_cvt_pk_bf16_f32 v81, v156, v157\n"
      "v_mfma_f32_16x16x32_bf16 v[170:173], v[198:201], a[208:211], v[170:173]\n"
      "buffer_store_dwordx4 v[166:169], v25, s[24:27], s85 offen sc1\n"
      "v_pk_mul_f32 v[164:165], v[164:165], v[218:219] op_sel_hi:[1,0]\n"
      "v_pk_mul_f32 v[114:115], v[114:115], v[218:219] op_sel_hi:[1,0]\n"
      "v_pk_mul_f32 v[206:207], v[206:207], v[204:205] op_sel:[0,1]\n"
      "v_pk_mul_f32 v[104:105], v[104:105], v[218:219] op_sel_hi:[1,0]\n"
      "v_pk_mul_f32 v[100:101], v[100:101], v[204:205] op_sel:[0,1]\n"
      "v_pk_mul_f32 v[112:113], v[112:113], v[218:219] op_sel_hi:[1,0]\n"
      "v_pk_mul_f32 v[150:151], v[150:151], v[204:205] op_sel:[0,1]\n"
      "v_mfma_f32_16x16x32_bf16 v[170:173], v[220:223], a[212:215], v[170:173]\n"
      "s_waitcnt lgkmcnt(2)\n"
      "v_mul_f32 v208, v205, v208\n"
      "v_mul_f32 v103, v205, v103\n"
      "v_mfma_f32_16x16x32_bf16 v[166:169], a[216:219], v[134:137], 0\n"
      "s_waitcnt lgkmcnt(1)\n"
      "v_mul_f32 v163, v218, v163\n"
      "v_mul_f32 v209, v205, v209\n"
      "v_mfma_f32_16x16x32_bf16 v[166:169], a[220:223], v[146:149], v[166:169]\n"
      "v_cvt_pk_bf16_f32 v100, v100, v101\n"
      "v_mul_f32 v152, v205, v152\n"
      "v_mfma_f32_16x16x32_bf16 v[166:169], a[224:227], v[116:119], v[166:169]\n"
      "ds_read_b64 v[156:157], v10 offset:1192\n"
      "v_mul_f32 v102, v205, v102\n"
      "v_mfma_f32_16x16x32_bf16 v[198:201], a[240:243], v[134:137], 0\n"
      "v_cvt_pk_bf16_f32 v101, v102, v103\n"
      "v_cvt_pk_bf16_f32 v109, v110, v111\n"
      "v_mfma_f32_16x16x32_bf16 v[198:201], a[244:247], v[146:149], v[198:201]\n"
      "ds_read_b128 v[220:223], v2 offset:9216\n"
      "v_mul_f32_e64 v121, v182, v121\n"
      "v_mfma_f32_16x16x32_bf16 v[198:201], a[248:251], v[116:119], v[198:201]\n"
      "ds_read_b128 v[176:179], v2 offset:15360\n"
      "v_cvt_pk_bf16_f32 v112, v112, v113\n"
      "v_mfma_f32_16x16x32_bf16 v[142:145], v[232:235], v[134:137], 0\n"
      "ds_read_b128 v[180:183], v2 offset:12288\n"
      "v_mul_f32_e64 v153, v205, v153\n"
      "v_mfma_f32_16x16x32_bf16 v[142:145], v[236:239], v[146:149], v[142:145]\n"
      "v_cvt_pk_bf16_f32 v125, v120, v121\n"
      "v_cvt_pk_bf16_f32 v85, v172, v173\n"
      "v_mfma_f32_16x16x32_bf16 v[142:145], v[240:243], v[116:119], v[142:145]\n"
      "v_cvt_pk_bf16_f32 v103, v152, v153\n"
      "v_mul_f32 v106, v218, v106\n"
      "v_mfma_f32_16x16x32_bf16 v[134:137], v[158:161], v[210:213], v[40:43]\n"
      "v_cvt_pk_bf16_f32 v102, v150, v151\n"
      "v_mul_f32 v107, v218, v107\n"
      "v_mfma_f32_16x16x32_bf16 v[146:149], v[96:99], v[210:213], v[44:47]\n"
      "ds_read_b128 v[150:153], v2 offset:3328\n"
      "v_cvt_pk_bf16_f32 v84, v170, v171\n"
      "v_mfma_f32_16x16x32_bf16 v[172:175], v[100:103], v[210:213], v[48:51]\n"
      "s_waitcnt lgkmcnt(2)\n"
      "v_cvt_pk_bf16_f32 v76, v186, v187\n"
      "v_cvt_pk_bf16_f32 v104, v104, v105\n"
      "v_mfma_f32_16x16x32_bf16 v[118:121], v[138:141], v[210:213], v[24:27]\n"
      "ds_read_b128 v[210:213], v2 offset:256\n"
      "v_mul_f32 v162, v218, v162\n"
      "v_mfma_f32_16x16x16_bf16 v[134:137], v[184:185], v[202:203], v[134:137]\n"
      "v_cvt_pk_bf16_f32 v77, v188, v189\n"
      "v_cvt_pk_bf16_f32 v206, v206, v207\n"
      "v_cvt_pk_bf16_f32 v105, v106, v107\n"
      "v_mfma_f32_16x16x16_bf16 v[146:149], v[108:109], v[202:203], v[146:149]\n"
      "v_cvt_pk_bf16_f32 v207, v208, v209\n"
      "v_cvt_pk_bf16_f32 v107, v164, v165\n"
      "v_cvt_pk_bf16_f32 v80, v154, v155\n"
      "v_mfma_f32_16x16x16_bf16 v[172:175], v[206:207], v[202:203], v[172:175]\n"
      "v_cvt_pk_bf16_f32 v106, v162, v163\n"
      "v_cvt_pk_bf16_f32 v113, v114, v115\n"
      "s_waitcnt vmcnt(6)\n"
      "v_or3_b32 v190, v190, v191, v192\n"
      "v_mfma_f32_16x16x16_bf16 v[118:121], v[156:157], v[202:203], v[118:121]\n"
      "v_max3_f32 v163, v134, v135, v136\n"
      "v_max3_f32 v163, v163, v148, v149\n"
      "v_max3_f32 v218, v137, v146, v147\n"
      "v_mfma_f32_16x16x32_bf16 v[166:169], a[228:231], v[220:223], v[166:169]\n"
      "s_waitcnt lgkmcnt(1)\n"
      "v_max3_f32 v163, v163, v174, v175\n"
      "v_max3_f32 v218, v218, v172, v173\n"
      "v_mfma_f32_16x16x32_bf16 v[166:169], a[232:235], v[180:183], v[166:169]\n"
      "v_max3_f32 v163, v163, v218, v118\n"
      "v_mov_b32_e64 v218, v163\n"
      "v_mfma_f32_16x16x32_bf16 v[166:169], a[236:239], v[176:179], v[166:169]\n"
      "s_nop 0\n"
      "v_permlane16_swap_b32 v163, v218\n"
      "v_mfma_f32_16x16x32_bf16 v[198:201], a[252:255], v[220:223], v[198:201]\n"
      "v_max_f32 v163, v163, v218\n"
      "v_mov_b32 v218, v163\n"
      "v_mfma_f32_16x16x32_bf16 v[198:201], v[224:227], v[180:183], v[198:201]\n"
      "ds_read_u16 v78, v11 offset:1168\n"
      "v_permlane32_swap_b32 v163, v218\n"
      "v_mfma_f32_16x16x32_bf16 v[198:201], v[228:231], v[176:179], v[198:201]\n"
      "v_max_f32 v163, v163, v218\n"
      "v_sub_f32 v134, v134, v163\n"
      "v_mfma_f32_16x16x32_bf16 v[142:145], v[244:247], v[220:223], v[142:145]\n"
      "v_sub_f32 v172, v172, v163\n"
      "v_sub_f32 v135, v135, v163\n"
      "v_mfma_f32_16x16x32_bf16 v[142:145], v[248:251], v[180:183], v[142:145]\n"
      "s_waitcnt vmcnt(5)\n"
      "v_or3_b32 v190, v190, v193, v219\n"
      "v_sub_f32 v173, v173, v163\n"
      "v_mfma_f32_16x16x32_bf16 v[142:145], v[252:255], v[176:179], v[142:145]\n"
      "ds_read_b128 v[218:221], v2 offset:6400\n"
      "v_sub_f32 v137, v137, v163\n"
      "v_sub_f32 v149, v149, v163\n"
      "v_mfma_f32_16x16x32_bf16 v[176:179], v[158:161], v[122:125], v[52:55]\n"
      "v_exp_f32 v173, v173\n"
      "v_mfma_f32_16x16x32_bf16 v[114:117], v[96:99], v[122:125], v[56:59]\n"
      "v_sub_f32 v175, v175, v163\n"
      "v_sub_f32 v118, v118, v163\n"
      "v_mfma_f32_16x16x32_bf16 v[186:189], v[100:103], v[122:125], v[60:63]\n"
      "v_sub_f32 v136, v136, v163\n"
      "v_sub_f32 v174, v174, v163\n"
      "v_mfma_f32_16x16x32_bf16 v[202:205], v[138:141], v[122:125], v[26:29]\n"
      "s_waitcnt lgkmcnt(1)\n"
      "v_exp_f32 v135, v135\n"
      "v_exp_f32 v149, v149\n"
      "v_mfma_f32_16x16x16_bf16 v[176:179], v[184:185], v[126:127], v[176:179]\n"
      "v_exp_f32 v136, v136\n"
      "v_exp_f32 v134, v134\n"
      "v_mfma_f32_16x16x16_bf16 v[114:117], v[108:109], v[126:127], v[114:117]\n"
      "v_sub_f32 v146, v146, v163\n"
      "v_exp_f32 v175, v175\n"
      "v_cvt_pk_bf16_f32 v134, v134, v135\n"
      "v_exp_f32 v137, v137\n"
      "v_mfma_f32_16x16x16_bf16 v[186:189], v[206:207], v[126:127], v[186:189]\n"
      "v_exp_f32 v118, v118\n"
      "v_sub_f32 v148, v148, v163\n"
      "v_mfma_f32_16x16x16_bf16 v[202:205], v[156:157], v[126:127], v[202:205]\n"
      "v_max3_f32 v110, v176, v177, v178\n"
      "v_sub_f32_e64 v147, v147, v163\n"
      "v_cvt_pk_bf16_f32 v135, v136, v137\n"
      "v_mfma_f32_16x16x32_bf16 v[162:165], a[216:219], v[210:213], 0\n"
      "s_waitcnt lgkmcnt(0)\n"
      "v_exp_f32 v148, v148\n"
      "v_mfma_f32_16x16x32_bf16 v[162:165], a[220:223], v[150:153], v[162:165]\n"
      "s_add_u32 s43, s43, 1\n"
      "v_exp_f32 v146, v146\n"
      "v_mfma_f32_16x16x32_bf16 v[162:165], a[224:227], v[218:221], v[162:165]\n"
      "v_max3_f32 v110, v110, v116, v117\n"
      "v_exp_f32 v172, v172\n"
      "v_mfma_f32_16x16x32_bf16 v[180:183], a[240:243], v[210:213], 0\n"
      "v_exp_f32 v147, v147\n"
      "v_mfma_f32_16x16x32_bf16 v[180:183], a[244:247], v[150:153], v[180:183]\n"
      "v_exp_f32_e64 v174, v174\n"
      "v_cvt_pk_bf16_f32 v137, v148, v149\n"
      "v_mfma_f32_16x16x32_bf16 v[180:183], a[248:251], v[218:221], v[180:183]\n"
      "v_cvt_pk_bf16_f32 v88, v172, v173\n"
      "v_cvt_pk_bf16_f32 v136, v146, v147\n"
      "v_mfma_f32_16x16x32_bf16 v[170:173], v[232:235], v[210:213], 0\n"
      "ds_read_b128 v[126:129], v2 offset:9472\n"
      "v_max3_f32 v121, v179, v114, v115\n"
      "v_mfma_f32_16x16x32_bf16 v[170:173], v[236:239], v[150:153], v[170:173]\n"
      "ds_read_u16 v82, v11 offset:1200\n"
      "v_max3_f32 v110, v110, v188, v189\n"
      "v_mfma_f32_16x16x32_bf16 v[170:173], v[240:243], v[218:221], v[170:173]\n"
      "v_cvt_pk_bf16_f32 v90, v118, 0\n"
      "v_cvt_pk_bf16_f32 v89, v174, v175\n"
      "v_mfma_f32_16x16x32_bf16 v[210:213], v[32:35], v[134:137], 0\n"
      "s_and_b32 s47, s47, -4\n"
      "v_max3_f32 v121, v121, v186, v187\n"
      "v_max3_f32 v110, v110, v121, v202\n"
      "v_mov_b32 v153, v110\n"
      "v_mfma_f32_16x16x32_bf16 v[210:213], v[32:35], v[88:91], v[210:213]\n"
      "s_nop 0\n"
      "v_permlane16_swap_b32 v110, v153\n"
      "v_mfma_f32_16x16x32_bf16 v[218:221], v[130:133], v[134:137], 0\n"
      "v_max_f32 v110, v110, v153\n"
      "v_mov_b32 v153, v110\n"
      "v_mfma_f32_16x16x32_bf16 v[218:221], v[76:79], v[88:91], v[218:221]\n"
      "s_nop 0\n"
      "v_permlane32_swap_b32 v110, v153\n"
      "v_mfma_f32_16x16x32_bf16 v[118:121], v[194:197], v[134:137], 0\n"
      "s_waitcnt lgkmcnt(0)\n"
      "v_max_f32 v110, v110, v153\n"
      "v_sub_f32_e64 v176, v176, v110\n"
      "v_mfma_f32_16x16x32_bf16 v[118:121], v[80:83], v[88:91], v[118:121]\n"
      "v_sub_f32 v177, v177, v110\n"
      "v_rcp_f32 v210, v210\n"
      "v_sub_f32 v178, v178, v110\n"
      "v_sub_f32 v117, v117, v110\n"
      "v_sub_f32_e64 v179, v179, v110\n"
      "v_pk_mul_f32 v[220:221], v[220:221], v[210:211] op_sel_hi:[1,0]\n"
      "v_sub_f32 v115, v115, v110\n"
      "v_exp_f32 v115, v115\n"
      "v_sub_f32 v116, v116, v110\n"
      "v_mfma_f32_16x16x32_bf16 v[152:155], v[214:217], v[134:137], 0\n"
      "v_exp_f32 v178, v178\n"
      "v_exp_f32 v177, v177\n"
      "v_mfma_f32_16x16x32_bf16 v[152:155], v[84:87], v[88:91], v[152:155]\n"
      "v_exp_f32 v179, v179\n"
      "v_sub_f32 v114, v114, v110\n"
      "v_mfma_f32_16x16x32_bf16 v[134:137], v[158:161], v[104:107], v[64:67]\n"
      "v_exp_f32 v176, v176\n"
      "v_exp_f32_e64 v114, v114\n"
      "v_cvt_pk_bf16_f32 v176, v176, v177\n"
      "v_cvt_pk_bf16_f32 v177, v178, v179\n"
      "v_pk_mul_f32 v[120:121], v[120:121], v[210:211] op_sel_hi:[1,0]\n"
      "v_pk_mul_f32 v[152:153], v[152:153], v[210:211] op_sel_hi:[1,0]\n"
      "v_pk_mul_f32 v[154:155], v[154:155], v[210:211] op_sel_hi:[1,0]\n"
      "v_sub_f32_e64 v186, v186, v110\n"
      "v_pk_mul_f32 v[118:119], v[118:119], v[210:211] op_sel_hi:[1,0]\n"
      "v_pk_mul_f32 v[218:219], v[218:219], v[210:211] op_sel_hi:[1,0]\n"
      "v_exp_f32 v116, v116\n"
      "v_cvt_pk_bf16_f32 v178, v114, v115\n"
      "v_mfma_f32_16x16x32_bf16 v[210:213], v[96:99], v[104:107], v[68:71]\n"
      "v_exp_f32 v117, v117\n"
      "v_mfma_f32_16x16x32_bf16 v[146:149], v[100:103], v[104:107], v[72:75]\n"
      "ds_read_b128 v[122:125], v2 offset:12544\n"
      "v_cvt_pk_bf16_f32 v179, v116, v117\n"
      "v_mfma_f32_16x16x32_bf16 v[114:117], v[138:141], v[104:107], v[28:31]\n"
      "ds_read_b128 v[104:107], v2 offset:15616\n"
      "v_cvt_pk_bf16_f32 v152, v152, v153\n"
      "v_mfma_f32_16x16x16_bf16 v[134:137], v[184:185], v[112:113], v[134:137]\n"
      "v_sub_f32_e64 v202, v202, v110\n"
      "v_cvt_pk_bf16_f32 v153, v154, v155\n"
      "v_sub_f32 v187, v187, v110\n"
      "v_mfma_f32_16x16x16_bf16 v[210:213], v[108:109], v[112:113], v[210:213]\n"
      "v_sub_f32 v188, v188, v110\n"
      "v_sub_f32_e64 v189, v189, v110\n"
      "v_cvt_pk_bf16_f32 v218, v218, v219\n"
      "v_mfma_f32_16x16x16_bf16 v[146:149], v[206:207], v[112:113], v[146:149]\n"
      "v_cvt_pk_bf16_f32 v219, v220, v221\n"
      "v_exp_f32_e64 v188, v188\n"
      "v_mfma_f32_16x16x16_bf16 v[114:117], v[156:157], v[112:113], v[114:117]\n"
      "v_cvt_pk_bf16_f32 v221, v120, v121\n"
      "v_cvt_pk_bf16_f32 v220, v118, v119\n"
      "v_max3_f32 v90, v134, v135, v136\n"
      "v_mfma_f32_16x16x32_bf16 v[162:165], a[228:231], v[126:129], v[162:165]\n"
      "s_waitcnt lgkmcnt(1)\n"
      "v_exp_f32 v202, v202\n"
      "v_mfma_f32_16x16x32_bf16 v[162:165], a[232:235], v[122:125], v[162:165]\n"
      "s_waitcnt lgkmcnt(0)\n"
      "v_exp_f32 v187, v187\n"
      "v_mfma_f32_16x16x32_bf16 v[162:165], a[236:239], v[104:107], v[162:165]\n"
      "v_exp_f32 v189, v189\n"
      "v_mfma_f32_16x16x32_bf16 v[180:183], a[252:255], v[126:129], v[180:183]\n"
      "v_exp_f32 v186, v186\n"
      "v_mfma_f32_16x16x32_bf16 v[180:183], v[224:227], v[122:125], v[180:183]\n"
      "ds_read_b64 v[88:89], v10 offset:1184\n"
      "v_cvt_pk_bf16_f32 v94, v202, 0\n"
      "v_mfma_f32_16x16x32_bf16 v[180:183], v[228:231], v[104:107], v[180:183]\n"
      "v_cvt_pk_bf16_f32 v93, v188, v189\n"
      "v_max3_f32 v90, v90, v212, v213\n"
      "v_mfma_f32_16x16x32_bf16 v[170:173], v[244:247], v[126:129], v[170:173]\n"
      "v_max3_f32 v189, v137, v210, v211\n"
      "v_cvt_pk_bf16_f32 v92, v186, v187\n"
      "v_mfma_f32_16x16x32_bf16 v[170:173], v[248:251], v[122:125], v[170:173]\n"
      "v_max3_f32 v189, v189, v146, v147\n"
      "v_fma_f32 v150, v166, v166, s52\n"
      "v_mfma_f32_16x16x32_bf16 v[170:173], v[252:255], v[104:107], v[170:173]\n"
      "ds_read_b128 v[126:129], v10 offset:1168\n"
      "v_max3_f32 v90, v90, v148, v149\n"
      "v_mfma_f32_16x16x32_bf16 v[116:119], v[32:35], v[176:179], 0\n"
      "v_max3_f32 v90, v90, v189, v114\n"
      "v_mov_b32 v122, v90\n"
      "v_mfma_f32_16x16x32_bf16 v[116:119], v[32:35], v[92:95], v[116:119]\n"
      "s_nop 0\n"
      "v_permlane16_swap_b32_e64 v90, v122\n"
      "v_mfma_f32_16x16x32_bf16 v[186:189], v[130:133], v[176:179], 0\n"
      "ds_write_b64 v4, v[152:153] offset:18432\n"
      "v_mfma_f32_16x16x32_bf16 v[186:189], v[76:79], v[92:95], v[186:189]\n"
      "v_max_f32 v90, v90, v122\n"
      "v_mov_b32 v122, v90\n"
      "v_mfma_f32_16x16x32_bf16 v[152:155], v[194:197], v[176:179], 0\n"
      "s_nop 0\n"
      "v_permlane32_swap_b32 v90, v122\n"
      "v_mfma_f32_16x16x32_bf16 v[152:155], v[80:83], v[92:95], v[152:155]\n"
      "v_max_f32 v90, v90, v122\n"
      "v_sub_f32 v135, v135, v90\n"
      "v_mfma_f32_16x16x32_bf16 v[120:123], v[214:217], v[176:179], 0\n"
      "v_sub_f32 v137, v137, v90\n"
      "v_sub_f32 v149, v149, v90\n"
      "v_mfma_f32_16x16x32_bf16 v[120:123], v[84:87], v[92:95], v[120:123]\n"
      "s_waitcnt lgkmcnt(1)\n"
      "v_exp_f32 v135, v135\n"
      "v_mfma_f32_16x16x32_bf16 v[174:177], v[126:129], v[158:161], v[30:33]\n"
      "v_rcp_f32 v116, v116\n"
      "v_mfma_f32_16x16x32_bf16 v[110:113], v[126:129], v[96:99], v[30:33]\n"
      "ds_read_b128 v[158:161], v2 offset:512\n"
      "v_sub_f32 v211, v211, v90\n"
      "v_mfma_f32_16x16x32_bf16 v[96:99], v[126:129], v[100:103], v[30:33]\n"
      "ds_read_b128 v[100:103], v2 offset:6656\n"
      "v_sub_f32 v212, v212, v90\n"
      "v_mfma_f32_16x16x32_bf16 v[104:107], v[126:129], v[138:141], v[30:33]\n"
      "ds_read_b128 v[202:205], v2 offset:3584\n"
      "v_sub_f32 v148, v148, v90\n"
      "v_mfma_f32_16x16x16_bf16 v[174:177], v[88:89], v[184:185], v[174:177]\n"
      "v_sub_f32 v134, v134, v90\n"
      "v_mul_f32 v151, v167, v167\n"
      "v_exp_f32 v134, v134\n"
      "v_mfma_f32_16x16x16_bf16 v[110:113], v[88:89], v[108:109], v[110:113]\n"
      "v_sub_f32 v146, v146, v90\n"
      "v_exp_f32 v148, v148\n"
      "v_mfma_f32_16x16x16_bf16 v[96:99], v[88:89], v[206:207], v[96:99]\n"
      "v_sub_f32 v213, v213, v90\n"
      "v_mul_f32 v223, v163, v163\n"
      "v_exp_f32 v149, v149\n"
      "v_mfma_f32_16x16x16_bf16 v[104:107], v[88:89], v[156:157], v[104:107]\n"
      "v_fma_f32 v222, v162, v162, s52\n"
      "v_sub_f32 v136, v136, v90\n"
      "v_exp_f32 v137, v137\n"
      "v_sub_f32_e64 v114, v114, v90\n"
      "v_exp_f32_e64 v136, v136\n"
      "v_pk_mul_f32 v[186:187], v[186:187], v[116:117] op_sel_hi:[1,0]\n"
      "v_pk_fma_f32 v[222:223], v[164:165], v[164:165], v[222:223]\n"
      "v_pk_mul_f32 v[120:121], v[120:121], v[116:117] op_sel_hi:[1,0]\n"
      "v_pk_mul_f32 v[154:155], v[154:155], v[116:117] op_sel_hi:[1,0]\n"
      "v_pk_fma_f32 v[150:151], v[168:169], v[168:169], v[150:151]\n"
      "v_pk_fma_f32 v[222:223], v[180:181], v[180:181], v[222:223]\n"
      "v_cvt_pk_bf16_f32 v120, v120, v121\n"
      "v_pk_mul_f32 v[152:153], v[152:153], v[116:117] op_sel_hi:[1,0]\n"
      "v_pk_fma_f32 v[222:223], v[182:183], v[182:183], v[222:223]\n"
      "v_pk_mul_f32 v[122:123], v[122:123], v[116:117] op_sel_hi:[1,0]\n"
      "v_cvt_pk_bf16_f32 v89, v148, v149\n"
      "v_sub_f32_e64 v210, v210, v90\n"
      "v_pk_mul_f32 v[188:189], v[188:189], v[116:117] op_sel_hi:[1,0]\n"
      "v_pk_fma_f32 v[222:223], v[170:171], v[170:171], v[222:223]\n"
      "v_pk_fma_f32 v[222:223], v[172:173], v[172:173], v[222:223]\n"
      "v_pk_fma_f32 v[150:151], v[198:199], v[198:199], v[150:151]\n"
      "s_waitcnt lgkmcnt(0)\n"
      "v_mfma_f32_16x16x32_bf16 v[126:129], a[216:219], v[158:161], 0\n"
      "v_cndmask_b32 v174, v174, v110, s[58:59]\n"
      "v_sub_f32 v147, v147, v90\n"
      "v_mfma_f32_16x16x32_bf16 v[126:129], a[220:223], v[202:205], v[126:129]\n"
      "v_exp_f32_e64 v213, v213\n"
      "v_mfma_f32_16x16x32_bf16 v[126:129], a[224:227], v[100:103], v[126:129]\n"
      "ds_read_b128 v[176:179], v2 offset:12800\n"
      "v_cvt_pk_bf16_f32 v121, v122, v123\n"
      "v_mfma_f32_16x16x32_bf16 v[116:119], a[240:243], v[158:161], 0\n"
      "ds_read_b128 v[122:125], v2 offset:9728\n"
      "v_cndmask_b32 v174, v174, v96, s[60:61]\n"
      "v_mfma_f32_16x16x32_bf16 v[116:119], a[244:247], v[202:205], v[116:119]\n"
      "ds_read_b128 v[138:141], v2 offset:15872\n"
      "v_cvt_pk_bf16_f32 v186, v186, v187\n"
      "v_mfma_f32_16x16x32_bf16 v[116:119], a[248:251], v[100:103], v[116:119]\n"
      "ds_write_b128 v3, v[218:221] offset:18432\n"
      "v_pk_fma_f32 v[150:151], v[200:201], v[200:201], v[150:151]\n"
      "v_pk_fma_f32 v[150:151], v[142:143], v[142:143], v[150:151]\n"
      "v_cndmask_b32 v174, v174, v104, s[62:63]\n"
      "v_mfma_f32_16x16x32_bf16 v[108:111], v[232:235], v[158:161], 0\n"
      "v_exp_f32 v147, v147\n"
      "v_exp_f32 v146, v146\n"
      "v_max_f32_dpp v88, v174, v174 row_ror:8 row_mask:0xf bank_mask:0xf\n"
      "v_mfma_f32_16x16x32_bf16 v[108:111], v[236:239], v[202:205], v[108:111]\n"
      "s_nop 0\n"
      "v_max_f32_dpp v88, v88, v88 row_ror:4 row_mask:0xf bank_mask:0xf\n"
      "v_cvt_pk_bf16_f32 v134, v134, v135\n"
      "v_mfma_f32_16x16x32_bf16 v[108:111], v[240:243], v[100:103], v[108:111]\n"
      "s_waitcnt lgkmcnt(1)\n"
      "v_max_f32_dpp v88, v88, v88 row_ror:2 row_mask:0xf bank_mask:0xf\n"
      "v_cvt_pk_bf16_f32 v187, v188, v189\n"
      "v_add_f32 v222, v222, v223\n"
      "v_mfma_f32_16x16x32_bf16 v[126:129], a[228:231], v[122:125], v[126:129]\n"
      "v_exp_f32 v212, v212\n"
      "v_mfma_f32_16x16x32_bf16 v[126:129], a[232:235], v[176:179], v[126:129]\n"
      "ds_write_b64 v4, v[120:121] offset:18688\n"
      "v_mfma_f32_16x16x32_bf16 v[126:129], a[236:239], v[138:141], v[126:129]\n"
      "v_max_f32_dpp v88, v88, v88 row_ror:1 row_mask:0xf bank_mask:0xf\n"
      "v_exp_f32_e64 v210, v210\n"
      "v_mfma_f32_16x16x32_bf16 v[116:119], a[252:255], v[122:125], v[116:119]\n"
      "v_cvt_pk_bf16_f32 v189, v154, v155\n"
      "v_max_f32_dpp v88, v88, v88 row_bcast:15 row_mask:0xa bank_mask:0xf\n"
      "v_cvt_pk_bf16_f32 v188, v152, v153\n"
      "v_mfma_f32_16x16x32_bf16 v[116:119], v[224:227], v[176:179], v[116:119]\n"
      "v_cvt_pk_bf16_f32 v135, v136, v137\n"
      "v_cvt_pk_bf16_f32 v137, v212, v213\n"
      "v_mfma_f32_16x16x32_bf16 v[116:119], v[228:231], v[138:141], v[116:119]\n"
      "v_max_f32_dpp v88, v88, v88 row_bcast:31 row_mask:0xc bank_mask:0xf\n"
      "v_mul_f32_e64 v213, v127, v127\n"
      "v_readlane_b32 s72, v88, 63\n"
      "v_fma_f32 v212, v126, v126, s52\n"
      "v_mfma_f32_16x16x32_bf16 v[108:111], v[244:247], v[122:125], v[108:111]\n"
      "v_subrev_f32 v174, s72, v174\n"
      "v_cvt_pk_bf16_f32 v88, v146, v147\n"
      "v_mfma_f32_16x16x32_bf16 v[108:111], v[248:251], v[176:179], v[108:111]\n"
      "v_exp_f32 v211, v211\n"
      "v_mfma_f32_16x16x32_bf16 v[108:111], v[252:255], v[138:141], v[108:111]\n"
      "ds_write_b128 v3, v[186:189] offset:18688\n"
      "v_exp_f32_e64 v174, v174\n"
      "v_pk_fma_f32 v[212:213], v[128:129], v[128:129], v[212:213]\n"
      "v_pk_fma_f32 v[150:151], v[144:145], v[144:145], v[150:151]\n"
      "v_pk_fma_f32 v[212:213], v[116:117], v[116:117], v[212:213]\n"
      "v_exp_f32 v114, v114\n"
      "v_add_f32 v150, v150, v151\n"
      "v_cvt_pk_bf16_f32 v174, v174, v174\n"
      "v_cvt_pk_bf16_f32 v136, v210, v211\n"
      "v_pk_fma_f32 v[212:213], v[118:119], v[118:119], v[212:213]\n"
      "v_pk_fma_f32 v[212:213], v[108:109], v[108:109], v[212:213]\n"
      "v_pk_fma_f32 v[212:213], v[110:111], v[110:111], v[212:213]\n"
      "v_cvt_pk_bf16_f32 v90, v114, 0\n"
      "v_mfma_f32_16x16x32_bf16 v[218:221], v[32:35], v[134:137], 0\n"
      "v_permlane16_swap_b32 v150, v222\n"
      "v_mfma_f32_16x16x32_bf16 v[218:221], v[32:35], v[88:91], v[218:221]\n"
      "v_add_f32 v212, v212, v213\n"
      "v_add_f32 v150, v150, v222\n"
      "v_mov_b32 v222, v212\n"
      "v_mfma_f32_16x16x32_bf16 v[100:103], v[130:133], v[134:137], 0\n"
      "ds_write_b16 v13, v174\n"
      "v_permlane16_swap_b32_e64 v212, v222\n"
      "v_mfma_f32_16x16x32_bf16 v[100:103], v[76:79], v[88:91], v[100:103]\n"
      "s_nop 0\n"
      "v_rcp_f32 v218, v218\n"
      "v_mfma_f32_16x16x32_bf16 v[186:189], v[194:197], v[134:137], 0\n"
      "ds_read_b128 v[174:177], v14\n"
      "v_add_f32 v212, v212, v222\n"
      "v_mfma_f32_16x16x32_bf16 v[186:189], v[80:83], v[88:91], v[186:189]\n"
      "s_nop 0\n"
      "v_permlane32_swap_b32 v150, v212\n"
      "v_add_f32 v150, v150, v212\n"
      "v_mfma_f32_16x16x32_bf16 v[220:223], v[214:217], v[134:137], 0\n"
      "ds_write_b32 v31, v150 offset:1024\n"
      "v_mfma_f32_16x16x32_bf16 v[220:223], v[84:87], v[88:91], v[220:223]\n"
      "ds_read_b128 v[210:213], v14 offset:64\n"
      "ds_write_b32 v31, v190 offset:5632\n"
      "v_pk_mul_f32 v[188:189], v[188:189], v[218:219] op_sel_hi:[1,0]\n"
      "v_pk_mul_f32 v[100:101], v[100:101], v[218:219] op_sel_hi:[1,0]\n"
      "v_cvt_pk_bf16_f32 v100, v100, v101\n"
      "v_pk_mul_f32 v[186:187], v[186:187], v[218:219] op_sel_hi:[1,0]\n"
      "v_pk_mul_f32 v[102:103], v[102:103], v[218:219] op_sel_hi:[1,0]\n"
      "v_cvt_pk_bf16_f32 v101, v102, v103\n"
      "v_cvt_pk_bf16_f32 v102, v186, v187\n"
      "v_cvt_pk_bf16_f32 v103, v188, v189\n"
      "s_add_u32 s42, s42, 1\n"
      "ds_write_b128 v3, v[100:103] offset:18944\n"
      "v_pk_mul_f32 v[222:223], v[222:223], v[218:219] op_sel_hi:[1,0]\n"
      "v_pk_mul_f32 v[220:221], v[220:221], v[218:219] op_sel_hi:[1,0]\n"
      "v_cvt_pk_bf16_f32 v220, v220, v221\n"
      "s_waitcnt lgkmcnt(3)\n"
      "v_mfma_f32_16x16x32_bf16 v[188:191], v[174:177], v[32:35], 0\n"
      "s_waitcnt lgkmcnt(2)\n"
      "v_cvt_pk_bf16_f32 v221, v222, v223\n"
      "v_add_u32 v11, 2336, v11\n"
      "v_mfma_f32_16x16x32_bf16 v[188:191], v[210:213], v[32:35], v[188:191]\n"
      "ds_write_b64 v4, v[220:221] offset:18944\n"
      "v_add_u32 v10, 2336, v10\n"
      "s_waitcnt lgkmcnt(0)\n"
      "s_barrier\n"
      "s_cbranch_vccnz L_disp0_%=\n"
      "L_max0_%=:\n"
      "v_mfma_f32_16x16x32_bf16 v[218:221], v[174:177], v[130:133], 0\n"
      "ds_read_b128 v[202:205], v0\n"
      "v_readlane_b32 s85, v37, s42\n"
      "v_mfma_f32_16x16x32_bf16 v[218:221], v[210:213], v[76:79], v[218:221]\n"
      "ds_read_b128 v[154:157], v9 offset:1024\n"
      "s_add_u32 m0, s64, 18432\n"
      "v_mfma_f32_16x16x32_bf16 v[134:137], v[174:177], v[194:197], 0\n"
      "ds_read_b128 v[150:153], v1 offset:8192\n"
      "ds_read_b128 v[184:187], v0 offset:6144\n"
      "v_mfma_f32_16x16x32_bf16 v[134:137], v[210:213], v[80:83], v[134:137]\n"
      "buffer_load_dwordx4 v16, s[16:19], s45 offen  lds\n"
      "v_mfma_f32_16x16x32_bf16 v[100:103], v[174:177], v[214:217], 0\n"
      "s_add_u32 m0, s64, 22528\n"
      "ds_read_b128 v[138:141], v1\n"
      "v_mfma_f32_16x16x32_bf16 v[100:103], v[210:213], v[84:87], v[100:103]\n"
      "ds_read_b128 v[210:213], v0 offset:14336\n"
      "s_waitcnt lgkmcnt(5)\n"
      "v_mfma_f32_16x16x32_bf16 v[206:209], a[0:3], v[202:205], 0\n"
      "ds_read_b128 v[176:179], v1 offset:6144\n"
      "v_readlane_b32 s84, v37, s43\n"
      "v_mfma_f32_16x16x32_bf16 v[104:107], a[24:27], v[202:205], 0\n"
      "s_waitcnt lgkmcnt(5)\n"
      "v_add_f32 v156, v156, v157\n"
      "v_add_f32 v154, v154, v155\n"
      "v_mfma_f32_16x16x32_bf16 v[96:99], a[48:51], v[202:205], 0\n"
      "buffer_load_dwordx4 v17, s[16:19], s45 offen  lds\n"
      "v_mfma_f32_16x16x32_bf16 v[146:149], a[72:75], v[202:205], 0\n"
      "s_or_b32 s95, s84, s85\n"
      "v_add_f32 v154, v154, v156\n"
      "v_cndmask_b32 v218, v218, v134, s[58:59]\n"
      "v_mfma_f32_16x16x32_bf16 v[122:125], a[96:99], v[202:205], 0\n"
      "s_waitcnt lgkmcnt(2)\n"
      "v_rsq_f32 v154, v154\n"
      "v_mfma_f32_16x16x32_bf16 v[112:115], a[120:123], v[202:205], 0\n"
      "ds_read_b128 v[134:137], v1 offset:12288\n"
      "v_mfma_f32_16x16x32_bf16 v[206:209], a[4:7], v[138:141], v[206:209]\n"
      "ds_read_b128 v[220:223], v0 offset:12288\n"
      "v_mul_f32 v154, 0x415db3d7, v154\n"
      "v_mfma_f32_16x16x32_bf16 v[206:209], a[8:11], v[184:187], v[206:209]\n"
      "s_add_u32 m0, s64, 26624\n"
      "v_readlane_b32 s86, v38, s43\n"
      "v_mov_b32 v156, v154\n"
      "v_mfma_f32_16x16x32_bf16 v[104:107], a[28:31], v[138:141], v[104:107]\n"
      "s_or_b32 s95, s95, s48\n"
      "v_permlane32_swap_b32 v154, v156\n"
      "v_mfma_f32_16x16x32_bf16 v[104:107], a[32:35], v[184:187], v[104:107]\n"
      "s_cmp_ge_u32 s95, 0x40000000\n"
      "s_cselect_b32 s94, -1, -2\n"
      "v_mov_b32 v155, v154\n"
      "v_mfma_f32_16x16x32_bf16 v[96:99], a[52:55], v[138:141], v[96:99]\n"
      "buffer_load_dwordx4 v18, s[16:19], s45 offen  lds\n"
      "v_mfma_f32_16x16x32_bf16 v[96:99], a[56:59], v[184:187], v[96:99]\n"
      "v_permlane16_swap_b32_e64 v154, v155\n"
      "v_mfma_f32_16x16x32_bf16 v[146:149], a[76:79], v[138:141], v[146:149]\n"
      "s_add_u32 m0, s64, 30720\n"
      "v_mul_f32 v168, v154, v168\n"
      "v_mul_f32 v143, v154, v143\n"
      "v_mfma_f32_16x16x32_bf16 v[146:149], a[80:83], v[184:187], v[146:149]\n"
      "v_mul_f32 v166, v154, v166\n"
      "v_mul_f32 v198, v154, v198\n"
      "v_mfma_f32_16x16x32_bf16 v[122:125], a[100:103], v[138:141], v[122:125]\n"
      "v_mul_f32 v200, v154, v200\n"
      "v_mul_f32 v180, v155, v180\n"
      "v_mfma_f32_16x16x32_bf16 v[122:125], a[104:107], v[184:187], v[122:125]\n"
      "s_waitcnt lgkmcnt(0)\n"
      "v_mul_f32 v169, v154, v169\n"
      "v_mul_f32_e64 v170, v155, v170\n"
      "v_mfma_f32_16x16x32_bf16 v[112:115], a[124:127], v[138:141], v[112:115]\n"
      "buffer_load_dwordx4 v19, s[16:19], s45 offen  lds\n"
      "v_mfma_f32_16x16x32_bf16 v[112:115], a[128:131], v[184:187], v[112:115]\n"
      "s_add_u32 m0, s65, 34816\n"
      "v_mul_f32 v167, v154, v167\n"
      "v_mul_f32 v199, v154, v199\n"
      "v_mfma_f32_16x16x32_bf16 v[206:209], a[12:15], v[176:179], v[206:209]\n"
      "v_cvt_pk_bf16_f32 v166, v166, v167\n"
      "v_cvt_pk_bf16_f32 v167, v168, v169\n"
      "v_mfma_f32_16x16x32_bf16 v[206:209], a[16:19], v[220:223], v[206:209]\n"
      "v_cvt_pk_bf16_f32 v168, v198, v199\n"
      "v_mul_f32_e64 v201, v154, v201\n"
      "v_mfma_f32_16x16x32_bf16 v[206:209], a[20:23], v[134:137], v[206:209]\n"
      "v_cvt_pk_bf16_f32 v169, v200, v201\n"
      "v_mul_f32_e64 v142, v154, v142\n"
      "v_mfma_f32_16x16x32_bf16 v[104:107], a[36:39], v[176:179], v[104:107]\n"
      "buffer_load_dwordx4 v15, s[16:19], s45 offen  lds\n"
      "v_mfma_f32_16x16x32_bf16 v[104:107], a[40:43], v[220:223], v[104:107]\n"
      "s_cmp_gt_u32 s42, s44\n"
      "s_cselect_b32 s95, s94, s95\n"
      "v_cvt_pk_bf16_f32 v142, v142, v143\n"
      "v_mfma_f32_16x16x32_bf16 v[104:107], a[44:47], v[134:137], v[104:107]\n"
      "ds_write_b128 v5, v[166:169]\n"
      "v_pk_mul_f32 v[144:145], v[144:145], v[154:155] op_sel_hi:[1,0]\n"
      "v_pk_mul_f32 v[164:165], v[164:165], v[154:155] op_sel:[0,1]\n"
      "v_pk_mul_f32 v[162:163], v[162:163], v[154:155] op_sel:[0,1]\n"
      "v_pk_mul_f32 v[182:183], v[182:183], v[154:155] op_sel:[0,1]\n"
      "v_mfma_f32_16x16x32_bf16 v[96:99], a[60:63], v[176:179], v[96:99]\n"
      "buffer_load_dwordx4 v[158:161], v36, s[28:31], s47 offen\n"
      "v_mfma_f32_16x16x32_bf16 v[96:99], a[64:67], v[220:223], v[96:99]\n"
      "ds_read_b128 v[166:169], v1 offset:2048\n"
      "v_cvt_pk_bf16_f32 v143, v144, v145\n"
      "v_mfma_f32_16x16x32_bf16 v[96:99], a[68:71], v[134:137], v[96:99]\n"
      "ds_write_b64 v6, v[142:143]\n"
      "v_mfma_f32_16x16x32_bf16 v[146:149], a[84:87], v[176:179], v[146:149]\n"
      "ds_read_b128 v[198:201], v0 offset:2048\n"
      "v_mul_f32_e64 v173, v155, v173\n"
      "v_mfma_f32_16x16x32_bf16 v[146:149], a[88:91], v[220:223], v[146:149]\n"
      "v_cvt_pk_bf16_f32 v162, v162, v163\n"
      "v_mul_f32 v111, v156, v111\n"
      "v_mfma_f32_16x16x32_bf16 v[146:149], a[92:95], v[134:137], v[146:149]\n"
      "v_mul_f32 v126, v156, v126\n"
      "v_fma_f32 v175, v206, v206, s51\n"
      "v_mfma_f32_16x16x32_bf16 v[122:125], a[108:111], v[176:179], v[122:125]\n"
      "buffer_load_dword v90, v29, s[28:31], s47 offen\n"
      "v_mfma_f32_16x16x32_bf16 v[122:125], a[112:115], v[220:223], v[122:125]\n"
      "v_mov_b32_dpp v188, v188 quad_perm:[0,0,0,0] row_mask:0xf bank_mask:0xf\n"
      "v_mul_f32 v181, v155, v181\n"
      "v_mfma_f32_16x16x32_bf16 v[122:125], a[116:119], v[134:137], v[122:125]\n"
      "v_mul_f32 v127, v156, v127\n"
      "v_mul_f32_e64 v171, v155, v171\n"
      "v_mfma_f32_16x16x32_bf16 v[112:115], a[132:135], v[176:179], v[112:115]\n"
      "v_cvt_pk_bf16_f32 v163, v164, v165\n"
      "v_mul_f32_e64 v116, v156, v116\n"
      "v_mfma_f32_16x16x32_bf16 v[112:115], a[136:139], v[220:223], v[112:115]\n"
      "v_fma_f32 v82, v146, v146, s51\n"
      "v_mul_f32_e64 v172, v155, v172\n"
      "v_mfma_f32_16x16x32_bf16 v[112:115], a[140:143], v[134:137], v[112:115]\n"
      "v_cvt_pk_bf16_f32 v170, v170, v171\n"
      "v_cvt_pk_bf16_f32 v164, v180, v181\n"
      "v_mfma_f32_16x16x32_bf16 v[130:133], v[202:205], a[144:147], 0\n"
      "v_cvt_pk_bf16_f32 v165, v182, v183\n"
      "v_fmac_f32_e64 v175, v207, v207\n"
      "v_mfma_f32_16x16x32_bf16 v[130:133], v[138:141], a[148:151], v[130:133]\n"
      "ds_read_b128 v[142:145], v9 offset:5632\n"
      "v_fmac_f32_e64 v175, v208, v208\n"
      "v_mfma_f32_16x16x32_bf16 v[130:133], v[184:187], a[152:155], v[130:133]\n"
      "ds_write_b128 v5, v[162:165] offset:6144\n"
      "v_mfma_f32_16x16x32_bf16 v[130:133], v[176:179], a[156:159], v[130:133]\n"
      "v_fmac_f32 v175, v209, v209\n"
      "v_fmac_f32 v175, v104, v104\n"
      "v_mfma_f32_16x16x32_bf16 v[130:133], v[220:223], a[160:163], v[130:133]\n"
      "ds_read_b128 v[162:165], v0 offset:8192\n"
      "v_cvt_pk_bf16_f32 v171, v172, v173\n"
      "v_mfma_f32_16x16x32_bf16 v[130:133], v[134:137], a[164:167], v[130:133]\n"
      "v_rcp_f32_e64 v188, v188\n"
      "v_mfma_f32_16x16x32_bf16 v[194:197], v[202:205], a[168:171], 0\n"
      "v_mul_f32 v109, v156, v109\n"
      "v_mul_f32 v129, v156, v129\n"
      "v_mfma_f32_16x16x32_bf16 v[194:197], v[138:141], a[172:175], v[194:197]\n"
      "v_cndmask_b32 v218, v218, v100, s[60:61]\n"
      "v_fmac_f32 v175, v105, v105\n"
      "v_mfma_f32_16x16x32_bf16 v[194:197], v[184:187], a[176:179], v[194:197]\n"
      "v_fmac_f32 v82, v147, v147\n"
      "v_fmac_f32 v175, v106, v106\n"
      "v_mfma_f32_16x16x32_bf16 v[194:197], v[176:179], a[180:183], v[194:197]\n"
      "v_mul_f32 v108, v156, v108\n"
      "v_mul_f32 v110, v156, v110\n"
      "v_mfma_f32_16x16x32_bf16 v[194:197], v[220:223], a[184:187], v[194:197]\n"
      "v_fmac_f32 v175, v107, v107\n"
      "v_fmac_f32_e64 v82, v148, v148\n"
      "v_mfma_f32_16x16x32_bf16 v[194:197], v[134:137], a[188:191], v[194:197]\n"
      "ds_write_b64 v6, v[170:171] offset:6144\n"
      "v_mfma_f32_16x16x32_bf16 v[214:217], v[202:205], a[192:195], 0\n"
      "ds_read_b128 v[180:183], v1 offset:14336\n"
      "v_mul_f32_e64 v119, v156, v119\n"
      "v_mfma_f32_16x16x32_bf16 v[214:217], v[138:141], a[196:199], v[214:217]\n"
      "v_fmac_f32 v175, v96, v96\n"
      "v_fmac_f32 v82, v149, v149\n"
      "v_mfma_f32_16x16x32_bf16 v[214:217], v[184:187], a[200:203], v[214:217]\n"
      "v_cvt_pk_bf16_f32 v108, v108, v109\n"
      "v_mul_f32_e64 v118, v156, v118\n"
      "v_mfma_f32_16x16x32_bf16 v[214:217], v[176:179], a[204:207], v[214:217]\n"
      "ds_read_b128 v[170:173], v0 offset:16384\n"
      "v_cvt_pk_bf16_f32 v109, v110, v111\n"
      "v_mfma_f32_16x16x32_bf16 v[214:217], v[220:223], a[208:211], v[214:217]\n"
      "v_fmac_f32 v175, v97, v97\n"
      "v_mul_f32 v218, v188, v218\n"
      "v_mfma_f32_16x16x32_bf16 v[214:217], v[134:137], a[212:215], v[214:217]\n"
      "s_waitcnt lgkmcnt(3)\n"
      "v_fmac_f32 v82, v122, v122\n"
      "v_fmac_f32 v82, v123, v123\n"
      "v_mfma_f32_16x16x32_bf16 v[134:137], a[0:3], v[198:201], 0\n"
      "ds_read_b128 v[202:205], v0 offset:4096\n"
      "v_fmac_f32 v82, v124, v124\n"
      "v_mfma_f32_16x16x32_bf16 v[134:137], a[4:7], v[166:169], v[134:137]\n"
      "ds_read_b128 v[220:223], v1 offset:10240\n"
      "v_fmac_f32 v82, v125, v125\n"
      "v_mfma_f32_16x16x32_bf16 v[134:137], a[8:11], v[162:165], v[134:137]\n"
      "s_waitcnt lgkmcnt(3)\n"
      "v_mul_f32_e64 v117, v156, v117\n"
      "v_cvt_pk_bf16_f32 v126, v126, v127\n"
      "v_mfma_f32_16x16x32_bf16 v[134:137], a[12:15], v[150:153], v[134:137]\n"
      "ds_write_b64 v6, v[108:109] offset:12288\n"
      "v_mfma_f32_16x16x32_bf16 v[134:137], a[16:19], v[210:213], v[134:137]\n"
      "ds_read_b128 v[190:193], v1 offset:16384\n"
      "v_fmac_f32 v82, v112, v112\n"
      "v_mfma_f32_16x16x32_bf16 v[134:137], a[20:23], v[180:183], v[134:137]\n"
      "v_fmac_f32 v175, v98, v98\n"
      "v_cvt_pk_bf16_f32 v214, v214, v215\n"
      "v_mfma_f32_16x16x32_bf16 v[138:141], a[24:27], v[198:201], 0\n"
      "v_fmac_f32 v82, v113, v113\n"
      "v_fmac_f32 v175, v99, v99\n"
      "v_mfma_f32_16x16x32_bf16 v[138:141], a[28:31], v[166:169], v[138:141]\n"
      "v_fmac_f32 v82, v114, v114\n"
      "v_fmac_f32 v82, v115, v115\n"
      "v_mfma_f32_16x16x32_bf16 v[138:141], a[32:35], v[162:165], v[138:141]\n"
      "v_cvt_pk_bf16_f32 v218, v218, v218\n"
      "v_fma_f32 v178, v134, v134, s51\n"
      "v_mfma_f32_16x16x32_bf16 v[138:141], a[36:39], v[150:153], v[138:141]\n"
      "v_permlane16_swap_b32 v175, v82\n"
      "v_mfma_f32_16x16x32_bf16 v[138:141], a[40:43], v[210:213], v[138:141]\n"
      "v_cvt_pk_bf16_f32 v194, v194, v195\n"
      "v_mul_f32 v128, v156, v128\n"
      "v_mfma_f32_16x16x32_bf16 v[138:141], a[44:47], v[180:183], v[138:141]\n"
      "v_cvt_pk_bf16_f32 v127, v128, v129\n"
      "v_add_f32 v175, v175, v82\n"
      "v_mfma_f32_16x16x32_bf16 v[186:189], a[48:51], v[198:201], 0\n"
      "v_mov_b32 v82, v175\n"
      "v_cvt_pk_bf16_f32 v130, v130, v131\n"
      "v_mfma_f32_16x16x32_bf16 v[186:189], a[52:55], v[166:169], v[186:189]\n"
      "v_permlane32_swap_b32 v175, v82\n"
      "v_mfma_f32_16x16x32_bf16 v[186:189], a[56:59], v[162:165], v[186:189]\n"
      "v_cvt_pk_bf16_f32 v128, v116, v117\n"
      "v_add_f32 v175, v175, v82\n"
      "v_mfma_f32_16x16x32_bf16 v[186:189], a[60:63], v[150:153], v[186:189]\n"
      "v_fmac_f32_e64 v178, v135, v135\n"
      "v_cvt_pk_bf16_f32 v129, v118, v119\n"
      "v_mfma_f32_16x16x32_bf16 v[186:189], a[64:67], v[210:213], v[186:189]\n"
      "ds_write_b128 v5, v[126:129] offset:12288\n"
      "v_mfma_f32_16x16x32_bf16 v[186:189], a[68:71], v[180:183], v[186:189]\n"
      "v_rsq_f32 v175, v175\n"
      "v_mfma_f32_16x16x32_bf16 v[154:157], a[72:75], v[198:201], 0\n"
      "v_fmac_f32 v178, v136, v136\n"
      "v_mov_b32 v82, v175\n"
      "v_mfma_f32_16x16x32_bf16 v[154:157], a[76:79], v[166:169], v[154:157]\n"
      "ds_write_b16 v12, v218 offset:16\n"
      "v_mfma_f32_16x16x32_bf16 v[154:157], a[80:83], v[162:165], v[154:157]\n"
      "v_permlane16_swap_b32 v175, v82\n"
      "v_mfma_f32_16x16x32_bf16 v[154:157], a[84:87], v[150:153], v[154:157]\n"
      "v_mul_f32 v175, s50, v175\n"
      "v_mul_f32 v209, v175, v209\n"
      "v_mfma_f32_16x16x32_bf16 v[154:157], a[88:91], v[210:213], v[154:157]\n"
      "v_mul_f32 v99, v175, v99\n"
      "v_fmac_f32 v178, v137, v137\n"
      "v_mfma_f32_16x16x32_bf16 v[154:157], a[92:95], v[180:183], v[154:157]\n"
      "v_cvt_pk_bf16_f32 v215, v216, v217\n"
      "v_mul_f32 v98, v175, v98\n"
      "v_mfma_f32_16x16x32_bf16 v[116:119], a[96:99], v[198:201], 0\n"
      "v_mul_f32 v112, v82, v112\n"
      "v_or3_b32 v142, v142, v143, v144\n"
      "v_mfma_f32_16x16x32_bf16 v[116:119], a[100:103], v[166:169], v[116:119]\n"
      "v_mul_f32 v146, v82, v146\n"
      "v_mul_f32 v97, v175, v97\n"
      "v_mfma_f32_16x16x32_bf16 v[116:119], a[104:107], v[162:165], v[116:119]\n"
      "v_mul_f32 v147, v82, v147\n"
      "v_cvt_pk_bf16_f32 v146, v146, v147\n"
      "v_mfma_f32_16x16x32_bf16 v[116:119], a[108:111], v[150:153], v[116:119]\n"
      "v_mul_f32 v114, v82, v114\n"
      "v_mul_f32_e64 v104, v175, v104\n"
      "v_mfma_f32_16x16x32_bf16 v[116:119], a[112:115], v[210:213], v[116:119]\n"
      "v_fma_f32 v184, v154, v154, s51\n"
      "v_mul_f32 v148, v82, v148\n"
      "v_mfma_f32_16x16x32_bf16 v[116:119], a[116:119], v[180:183], v[116:119]\n"
      "v_fmac_f32 v184, v155, v155\n"
      "v_mul_f32 v96, v175, v96\n"
      "v_mfma_f32_16x16x32_bf16 v[108:111], a[120:123], v[198:201], 0\n"
      "v_mul_f32 v149, v82, v149\n"
      "v_mul_f32 v206, v175, v206\n"
      "v_mfma_f32_16x16x32_bf16 v[108:111], a[124:127], v[166:169], v[108:111]\n"
      "v_fmac_f32 v184, v156, v156\n"
      "v_mul_f32 v113, v82, v113\n"
      "v_mfma_f32_16x16x32_bf16 v[108:111], a[128:131], v[162:165], v[108:111]\n"
      "v_mul_f32 v107, v175, v107\n"
      "v_cvt_pk_bf16_f32 v195, v196, v197\n"
      "v_mfma_f32_16x16x32_bf16 v[108:111], a[132:135], v[150:153], v[108:111]\n"
      "ds_read_u16 v78, v11\n"
      "v_mul_f32 v207, v175, v207\n"
      "v_mfma_f32_16x16x32_bf16 v[108:111], a[136:139], v[210:213], v[108:111]\n"
      "v_mul_f32 v115, v82, v115\n"
      "v_fmac_f32_e64 v178, v138, v138\n"
      "v_mfma_f32_16x16x32_bf16 v[108:111], a[140:143], v[180:183], v[108:111]\n"
      "v_fmac_f32 v178, v139, v139\n"
      "v_mul_f32 v208, v175, v208\n"
      "v_mfma_f32_16x16x32_bf16 v[216:219], v[198:201], a[144:147], 0\n"
      "v_fmac_f32 v184, v157, v157\n"
      "v_mul_f32 v123, v82, v123\n"
      "v_mfma_f32_16x16x32_bf16 v[216:219], v[166:169], a[148:151], v[216:219]\n"
      "v_fmac_f32 v184, v116, v116\n"
      "v_cvt_pk_bf16_f32 v206, v206, v207\n"
      "v_mfma_f32_16x16x32_bf16 v[216:219], v[162:165], a[152:155], v[216:219]\n"
      "v_add_u32 v12, 32, v12\n"
      "v_fmac_f32 v184, v117, v117\n"
      "v_mfma_f32_16x16x32_bf16 v[216:219], v[150:153], a[156:159], v[216:219]\n"
      "v_cvt_pk_bf16_f32 v207, v208, v209\n"
      "v_fmac_f32 v184, v118, v118\n"
      "v_mfma_f32_16x16x32_bf16 v[216:219], v[210:213], a[160:163], v[216:219]\n"
      "v_bitop3_b32 v142, v142, v145, s33 bitop3:0xa8\n"
      "v_cvt_pk_bf16_f32 v96, v96, v97\n"
      "v_mfma_f32_16x16x32_bf16 v[216:219], v[180:183], a[164:167], v[216:219]\n"
      "v_fmac_f32 v178, v140, v140\n"
      "v_fmac_f32 v184, v119, v119\n"
      "v_mfma_f32_16x16x32_bf16 v[126:129], v[198:201], a[168:171], 0\n"
      "v_fmac_f32 v184, v108, v108\n"
      "v_fmac_f32 v178, v141, v141\n"
      "v_mfma_f32_16x16x32_bf16 v[126:129], v[166:169], a[172:175], v[126:129]\n"
      "v_cvt_pk_bf16_f32 v131, v132, v133\n"
      "v_cmp_ne_u32 s[56:57], 0, v142\n"
      "v_mfma_f32_16x16x32_bf16 v[126:129], v[162:165], a[176:179], v[126:129]\n"
      "v_mul_f32 v124, v82, v124\n"
      "v_fmac_f32 v178, v186, v186\n"
      "v_mfma_f32_16x16x32_bf16 v[126:129], v[150:153], a[180:183], v[126:129]\n"
      "v_fmac_f32 v178, v187, v187\n"
      "v_cvt_pk_bf16_f32 v147, v148, v149\n"
      "v_mfma_f32_16x16x32_bf16 v[126:129], v[210:213], a[184:187], v[126:129]\n"
      "ds_read_b128 v[142:145], v1 offset:4096\n"
      "v_fmac_f32 v178, v188, v188\n"
      "v_mfma_f32_16x16x32_bf16 v[126:129], v[180:183], a[188:191], v[126:129]\n"
      "v_fmac_f32 v184, v109, v109\n"
      "v_fmac_f32 v184, v110, v110\n"
      "v_mfma_f32_16x16x32_bf16 v[100:103], v[198:201], a[192:195], 0\n"
      "ds_read_b128 v[198:201], v0 offset:10240\n"
      "v_fmac_f32 v178, v189, v189\n"
      "v_mfma_f32_16x16x32_bf16 v[100:103], v[166:169], a[196:199], v[100:103]\n"
      "v_mul_f32 v125, v82, v125\n"
      "v_fmac_f32 v184, v111, v111\n"
      "v_mfma_f32_16x16x32_bf16 v[100:103], v[162:165], a[200:203], v[100:103]\n"
      "v_mul_f32 v105, v175, v105\n"
      "v_mul_f32 v122, v82, v122\n"
      "v_mfma_f32_16x16x32_bf16 v[100:103], v[150:153], a[204:207], v[100:103]\n"
      "ds_read_u16 v82, v11 offset:32\n"
      "v_mul_f32 v106, v175, v106\n"
      "v_mfma_f32_16x16x32_bf16 v[100:103], v[210:213], a[208:211], v[100:103]\n"
      "v_permlane16_swap_b32 v178, v184\n"
      "v_mfma_f32_16x16x32_bf16 v[100:103], v[180:183], a[212:215], v[100:103]\n"
      "s_waitcnt lgkmcnt(2)\n"
      "v_add_f32 v178, v178, v184\n"
      "v_mov_b32 v184, v178\n"
      "v_mfma_f32_16x16x32_bf16 v[210:213], a[0:3], v[202:205], 0\n"
      "s_nop 0\n"
      "v_permlane32_swap_b32 v178, v184\n"
      "v_add_f32 v178, v178, v184\n"
      "v_mfma_f32_16x16x32_bf16 v[210:213], a[4:7], v[142:145], v[210:213]\n"
      "s_waitcnt lgkmcnt(1)\n"
      "v_rsq_f32_e64 v178, v178\n"
      "v_mfma_f32_16x16x32_bf16 v[210:213], a[8:11], v[198:201], v[210:213]\n"
      "v_mov_b32 v184, v178\n"
      "v_cvt_pk_bf16_f32 v132, v216, v217\n"
      "v_mfma_f32_16x16x32_bf16 v[210:213], a[12:15], v[220:223], v[210:213]\n"
      "v_permlane16_swap_b32 v178, v184\n"
      "v_mfma_f32_16x16x32_bf16 v[210:213], a[16:19], v[170:173], v[210:213]\n"
      "v_mul_f32 v109, v184, v109\n"
      "v_mul_f32_e64 v178, s50, v178\n"
      "v_mfma_f32_16x16x32_bf16 v[210:213], a[20:23], v[190:193], v[210:213]\n"
      "s_add_u32 s43, s43, 1\n"
      "v_cvt_pk_bf16_f32 v97, v98, v99\n"
      "v_cvt_pk_bf16_f32 v112, v112, v113\n"
      "v_mfma_f32_16x16x32_bf16 v[150:153], a[24:27], v[202:205], 0\n"
      "v_mul_f32 v155, v184, v155\n"
      "v_mul_f32 v118, v184, v118\n"
      "v_mfma_f32_16x16x32_bf16 v[150:153], a[28:31], v[142:145], v[150:153]\n"
      "v_mul_f32_e64 v111, v184, v111\n"
      "v_fma_f32 v217, v210, v210, s51\n"
      "v_mfma_f32_16x16x32_bf16 v[150:153], a[32:35], v[198:201], v[150:153]\n"
      "ds_read_b128 v[166:169], v7 offset:18432\n"
      "v_mul_f32 v154, v184, v154\n"
      "v_mfma_f32_16x16x32_bf16 v[150:153], a[36:39], v[220:223], v[150:153]\n"
      "v_mul_f32 v108, v184, v108\n"
      "v_mul_f32_e64 v135, v178, v135\n"
      "v_mfma_f32_16x16x32_bf16 v[150:153], a[40:43], v[170:173], v[150:153]\n"
      "v_cvt_pk_bf16_f32 v209, v106, v107\n"
      "v_cvt_pk_bf16_f32 v133, v218, v219\n"
      "v_mfma_f32_16x16x32_bf16 v[150:153], a[44:47], v[190:193], v[150:153]\n"
      "v_mul_f32 v136, v178, v136\n"
      "v_mul_f32 v139, v178, v139\n"
      "v_mfma_f32_16x16x32_bf16 v[174:177], a[48:51], v[202:205], 0\n"
      "v_mul_f32_e64 v134, v178, v134\n"
      "v_cvt_pk_bf16_f32 v154, v154, v155\n"
      "v_mfma_f32_16x16x32_bf16 v[174:177], a[52:55], v[142:145], v[174:177]\n"
      "v_cvt_pk_bf16_f32 v108, v108, v109\n"
      "v_cvt_pk_bf16_f32 v208, v104, v105\n"
      "v_mfma_f32_16x16x32_bf16 v[174:177], a[56:59], v[198:201], v[174:177]\n"
      "ds_read_b128 v[162:165], v7 offset:30720\n"
      "v_cvt_pk_bf16_f32 v148, v122, v123\n"
      "v_mfma_f32_16x16x32_bf16 v[174:177], a[60:63], v[220:223], v[174:177]\n"
      "v_cvt_pk_bf16_f32 v113, v114, v115\n"
      "v_fmac_f32_e64 v217, v211, v211\n"
      "v_mfma_f32_16x16x32_bf16 v[174:177], a[64:67], v[170:173], v[174:177]\n"
      "ds_read_b128 v[180:183], v7 offset:22528\n"
      "v_cvt_pk_bf16_f32 v149, v124, v125\n"
      "v_mfma_f32_16x16x32_bf16 v[174:177], a[68:71], v[190:193], v[174:177]\n"
      "v_mul_f32 v137, v178, v137\n"
      "v_fmac_f32 v217, v212, v212\n"
      "v_mfma_f32_16x16x32_bf16 v[104:107], a[72:75], v[202:205], 0\n"
      "s_waitcnt lgkmcnt(2)\n"
      "v_fmac_f32 v217, v213, v213\n"
      "v_cvt_pk_bf16_f32 v196, v126, v127\n"
      "v_mfma_f32_16x16x32_bf16 v[104:107], a[76:79], v[142:145], v[104:107]\n"
      "buffer_store_dwordx4 v[166:169], v20, s[24:27], s46 offen sc1\n"
      "v_pk_mul_f32 v[116:117], v[116:117], v[184:185] op_sel_hi:[1,0]\n"
      "v_pk_mul_f32 v[186:187], v[186:187], v[178:179] op_sel_hi:[1,0]\n"
      "v_mfma_f32_16x16x32_bf16 v[104:107], a[80:83], v[198:201], v[104:107]\n"
      "v_mul_f32 v138, v178, v138\n"
      "v_fmac_f32 v217, v150, v150\n"
      "v_mfma_f32_16x16x32_bf16 v[104:107], a[84:87], v[220:223], v[104:107]\n"
      "ds_read_b128 v[166:169], v7 offset:26624\n"
      "v_cvt_pk_bf16_f32 v216, v100, v101\n"
      "v_mfma_f32_16x16x32_bf16 v[104:107], a[88:91], v[170:173], v[104:107]\n"
      "v_fmac_f32 v217, v151, v151\n"
      "v_mul_f32 v110, v184, v110\n"
      "v_mfma_f32_16x16x32_bf16 v[104:107], a[92:95], v[190:193], v[104:107]\n"
      "v_mul_f32_e64 v157, v184, v157\n"
      "v_cvt_pk_bf16_f32 v109, v110, v111\n"
      "v_mfma_f32_16x16x32_bf16 v[120:123], a[96:99], v[202:205], 0\n"
      "v_cvt_pk_bf16_f32 v134, v134, v135\n"
      "v_mul_f32 v156, v184, v156\n"
      "v_mfma_f32_16x16x32_bf16 v[120:123], a[100:103], v[142:145], v[120:123]\n"
      "s_waitcnt lgkmcnt(1)\n"
      "v_mul_f32_e64 v119, v184, v119\n"
      "v_cvt_pk_bf16_f32 v155, v156, v157\n"
      "v_mfma_f32_16x16x32_bf16 v[120:123], a[104:107], v[198:201], v[120:123]\n"
      "s_cmp_ge_u32 s95, 0x40000000\n"
      "s_cselect_b64 vcc, -1, s[56:57]\n"
      "v_fmac_f32_e64 v217, v152, v152\n"
      "v_mfma_f32_16x16x32_bf16 v[120:123], a[108:111], v[220:223], v[120:123]\n"
      "buffer_store_dwordx4 v[180:183], v21, s[24:27], s46 offen sc1\n"
      "v_pk_mul_f32 v[188:189], v[188:189], v[178:179] op_sel_hi:[1,0]\n"
      "v_mfma_f32_16x16x32_bf16 v[120:123], a[112:115], v[170:173], v[120:123]\n"
      "v_mul_f32 v141, v178, v141\n"
      "v_fmac_f32 v217, v153, v153\n"
      "v_mfma_f32_16x16x32_bf16 v[120:123], a[116:119], v[190:193], v[120:123]\n"
      "v_fma_f32 v114, v104, v104, s51\n"
      "v_fmac_f32_e64 v114, v105, v105\n"
      "v_mfma_f32_16x16x32_bf16 v[98:101], a[120:123], v[202:205], 0\n"
      "v_fmac_f32_e64 v217, v174, v174\n"
      "v_cvt_pk_bf16_f32 v156, v116, v117\n"
      "v_mfma_f32_16x16x32_bf16 v[98:101], a[124:127], v[142:145], v[98:101]\n"
      "v_cvt_pk_bf16_f32 v135, v136, v137\n"
      "v_fmac_f32 v114, v106, v106\n"
      "v_mfma_f32_16x16x32_bf16 v[98:101], a[128:131], v[198:201], v[98:101]\n"
      "v_fmac_f32 v114, v107, v107\n"
      "v_fmac_f32_e64 v217, v175, v175\n"
      "v_mfma_f32_16x16x32_bf16 v[98:101], a[132:135], v[220:223], v[98:101]\n"
      "v_cvt_pk_bf16_f32 v157, v118, v119\n"
      "v_fmac_f32 v114, v120, v120\n"
      "v_mfma_f32_16x16x32_bf16 v[98:101], a[136:139], v[170:173], v[98:101]\n"
      "s_waitcnt lgkmcnt(0)\n"
      "v_fmac_f32 v217, v176, v176\n"
      "v_fmac_f32 v114, v121, v121\n"
      "v_mfma_f32_16x16x32_bf16 v[98:101], a[140:143], v[190:193], v[98:101]\n"
      "buffer_store_dwordx4 v[166:169], v22, s[24:27], s46 offen sc1\n"
      "v_mfma_f32_16x16x32_bf16 v[182:185], v[202:205], a[144:147], 0\n"
      "v_mul_f32 v140, v178, v140\n"
      "v_fmac_f32 v217, v177, v177\n"
      "v_mfma_f32_16x16x32_bf16 v[182:185], v[142:145], a[148:151], v[182:185]\n"
      "v_fmac_f32 v114, v122, v122\n"
      "v_fmac_f32 v114, v123, v123\n"
      "v_mfma_f32_16x16x32_bf16 v[182:185], v[198:201], a[152:155], v[182:185]\n"
      "v_fmac_f32 v114, v98, v98\n"
      "v_fmac_f32 v114, v99, v99\n"
      "v_mfma_f32_16x16x32_bf16 v[182:185], v[220:223], a[156:159], v[182:185]\n"
      "v_fmac_f32 v114, v100, v100\n"
      "v_fmac_f32 v114, v101, v101\n"
      "v_mfma_f32_16x16x32_bf16 v[182:185], v[170:173], a[160:163], v[182:185]\n"
      "s_nop 0\n"
      "v_permlane16_swap_b32 v217, v114\n"
      "v_mfma_f32_16x16x32_bf16 v[182:185], v[190:193], a[164:167], v[182:185]\n"
      "v_add_f32 v217, v217, v114\n"
      "v_mov_b32 v114, v217\n"
      "v_mfma_f32_16x16x32_bf16 v[116:119], v[202:205], a[168:171], 0\n"
      "s_nop 0\n"
      "v_permlane32_swap_b32 v217, v114\n"
      "v_mfma_f32_16x16x32_bf16 v[116:119], v[142:145], a[172:175], v[116:119]\n"
      "buffer_store_dwordx4 v[162:165], v23, s[24:27], s46 offen sc1\n"
      "v_mfma_f32_16x16x32_bf16 v[116:119], v[198:201], a[176:179], v[116:119]\n"
      "ds_read_b128 v[166:169], v7 offset:34816\n"
      "v_add_f32 v217, v217, v114\n"
      "v_mfma_f32_16x16x32_bf16 v[116:119], v[220:223], a[180:183], v[116:119]\n"
      "v_rsq_f32 v217, v217\n"
      "v_mfma_f32_16x16x32_bf16 v[116:119], v[170:173], a[184:187], v[116:119]\n"
      "ds_read_b128 v[124:127], v2 offset:21504\n"
      "v_mov_b32 v114, v217\n"
      "v_mfma_f32_16x16x32_bf16 v[116:119], v[190:193], a[188:191], v[116:119]\n"
      "s_nop 0\n"
      "v_permlane16_swap_b32_e64 v217, v114\n"
      "v_mfma_f32_16x16x32_bf16 v[178:181], v[202:205], a[192:195], 0\n"
      "ds_read_b128 v[202:205], v2 offset:18432\n"
      "v_cvt_pk_bf16_f32 v186, v186, v187\n"
      "v_mfma_f32_16x16x32_bf16 v[178:181], v[142:145], a[196:199], v[178:181]\n"
      "ds_read_b128 v[162:165], v2 offset:24576\n"
      "v_mul_f32 v217, s50, v217\n"
      "v_mfma_f32_16x16x32_bf16 v[178:181], v[198:201], a[200:203], v[178:181]\n"
      "s_waitcnt lgkmcnt(3)\n"
      "v_mul_f32_e64 v153, v217, v153\n"
      "v_cvt_pk_bf16_f32 v197, v128, v129\n"
      "v_mfma_f32_16x16x32_bf16 v[178:181], v[220:223], a[204:207], v[178:181]\n"
      "ds_read_b128 v[220:223], v10 offset:32\n"
      "v_cvt_pk_bf16_f32 v136, v138, v139\n"
      "v_mfma_f32_16x16x32_bf16 v[178:181], v[170:173], a[208:211], v[178:181]\n"
      "buffer_store_dwordx4 v[166:169], v25, s[24:27], s46 offen sc1\n"
      "v_pk_mul_f32 v[98:99], v[98:99], v[114:115] op_sel_hi:[1,0]\n"
      "v_pk_mul_f32 v[122:123], v[122:123], v[114:115] op_sel_hi:[1,0]\n"
      "v_pk_mul_f32 v[210:211], v[210:211], v[216:217] op_sel:[0,1]\n"
      "v_pk_mul_f32 v[174:175], v[174:175], v[216:217] op_sel:[0,1]\n"
      "v_pk_mul_f32 v[106:107], v[106:107], v[114:115] op_sel_hi:[1,0]\n"
      "v_pk_mul_f32 v[120:121], v[120:121], v[114:115] op_sel_hi:[1,0]\n"
      "v_pk_mul_f32 v[104:105], v[104:105], v[114:115] op_sel_hi:[1,0]\n"
      "v_mfma_f32_16x16x32_bf16 v[178:181], v[190:193], a[212:215], v[178:181]\n"
      "s_waitcnt lgkmcnt(1)\n"
      "v_cvt_pk_bf16_f32 v80, v116, v117\n"
      "v_mul_f32 v212, v217, v212\n"
      "v_mfma_f32_16x16x32_bf16 v[166:169], a[216:219], v[202:205], 0\n"
      "v_cvt_pk_bf16_f32 v187, v188, v189\n"
      "v_mul_f32_e64 v152, v217, v152\n"
      "v_mfma_f32_16x16x32_bf16 v[166:169], a[220:223], v[124:127], v[166:169]\n"
      "ds_read_b64 v[218:219], v10 offset:24\n"
      "v_cvt_pk_bf16_f32 v76, v182, v183\n"
      "v_mfma_f32_16x16x32_bf16 v[166:169], a[224:227], v[162:165], v[166:169]\n"
      "ds_read_b128 v[190:193], v2 offset:30720\n"
      "v_cvt_pk_bf16_f32 v210, v210, v211\n"
      "v_mfma_f32_16x16x32_bf16 v[198:201], a[240:243], v[202:205], 0\n"
      "v_cvt_pk_bf16_f32 v137, v140, v141\n"
      "v_cvt_pk_bf16_f32 v84, v178, v179\n"
      "v_mfma_f32_16x16x32_bf16 v[198:201], a[244:247], v[124:127], v[198:201]\n"
      "ds_read_b128 v[170:173], v2 offset:27648\n"
      "v_cvt_pk_bf16_f32 v81, v118, v119\n"
      "v_mfma_f32_16x16x32_bf16 v[198:201], a[248:251], v[162:165], v[198:201]\n"
      "v_cvt_pk_bf16_f32 v104, v104, v105\n"
      "v_mul_f32 v213, v217, v213\n"
      "v_mfma_f32_16x16x32_bf16 v[142:145], v[232:235], v[202:205], 0\n"
      "v_mul_f32 v100, v114, v100\n"
      "v_mul_f32_e64 v176, v217, v176\n"
      "v_mfma_f32_16x16x32_bf16 v[142:145], v[236:239], v[124:127], v[142:145]\n"
      "v_cvt_pk_bf16_f32 v105, v106, v107\n"
      "v_mul_f32 v101, v114, v101\n"
      "v_mfma_f32_16x16x32_bf16 v[142:145], v[240:243], v[162:165], v[142:145]\n"
      "s_waitcnt lgkmcnt(1)\n"
      "v_cvt_pk_bf16_f32 v107, v122, v123\n"
      "v_cvt_pk_bf16_f32 v98, v98, v99\n"
      "v_mfma_f32_16x16x32_bf16 v[138:141], v[146:149], v[206:209], v[40:43]\n"
      "ds_read_u16 v86, v11 offset:64\n"
      "v_cvt_pk_bf16_f32 v106, v120, v121\n"
      "v_mfma_f32_16x16x32_bf16 v[126:129], v[154:157], v[206:209], v[44:47]\n"
      "ds_read_b128 v[118:121], v2 offset:21760\n"
      "v_mul_f32_e64 v151, v217, v151\n"
      "v_mfma_f32_16x16x32_bf16 v[202:205], v[104:107], v[206:209], v[48:51]\n"
      "ds_read_b128 v[122:125], v2 offset:33792\n"
      "v_cvt_pk_bf16_f32 v99, v100, v101\n"
      "v_mfma_f32_16x16x32_bf16 v[162:165], v[220:223], v[206:209], v[24:27]\n"
      "s_waitcnt lgkmcnt(3)\n"
      "v_mul_f32 v177, v217, v177\n"
      "v_cvt_pk_bf16_f32 v85, v180, v181\n"
      "v_mfma_f32_16x16x16_bf16 v[138:141], v[112:113], v[96:97], v[138:141]\n"
      "v_cvt_pk_bf16_f32 v174, v174, v175\n"
      "s_waitcnt vmcnt(6)\n"
      "v_or3_b32 v158, v158, v159, v160\n"
      "v_cvt_pk_bf16_f32 v211, v212, v213\n"
      "v_mfma_f32_16x16x16_bf16 v[126:129], v[108:109], v[96:97], v[126:129]\n"
      "v_mul_f32 v150, v217, v150\n"
      "v_cvt_pk_bf16_f32 v77, v184, v185\n"
      "v_cvt_pk_bf16_f32 v175, v176, v177\n"
      "v_mfma_f32_16x16x16_bf16 v[202:205], v[98:99], v[96:97], v[202:205]\n"
      "v_cvt_pk_bf16_f32 v212, v150, v151\n"
      "v_cvt_pk_bf16_f32 v213, v152, v153\n"
      "v_max3_f32 v181, v138, v139, v140\n"
      "v_mfma_f32_16x16x16_bf16 v[162:165], v[218:219], v[96:97], v[162:165]\n"
      "v_max3_f32 v101, v141, v126, v127\n"
      "v_cvt_pk_bf16_f32 v217, v102, v103\n"
      "v_max3_f32 v181, v181, v128, v129\n"
      "v_mfma_f32_16x16x32_bf16 v[166:169], a[228:231], v[170:173], v[166:169]\n"
      "s_waitcnt lgkmcnt(0)\n"
      "v_max3_f32 v181, v181, v204, v205\n"
      "v_max3_f32 v101, v101, v202, v203\n"
      "v_mfma_f32_16x16x32_bf16 v[166:169], a[232:235], v[190:193], v[166:169]\n"
      "v_max3_f32 v181, v181, v101, v162\n"
      "v_mov_b32 v177, v181\n"
      "v_mfma_f32_16x16x32_bf16 v[166:169], a[236:239], v[122:125], v[166:169]\n"
      "s_nop 0\n"
      "v_permlane16_swap_b32 v181, v177\n"
      "v_mfma_f32_16x16x32_bf16 v[198:201], a[252:255], v[170:173], v[198:201]\n"
      "ds_read_b128 v[100:103], v2 offset:18688\n"
      "v_max_f32 v181, v181, v177\n"
      "v_mfma_f32_16x16x32_bf16 v[198:201], v[224:227], v[190:193], v[198:201]\n"
      "s_waitcnt vmcnt(5)\n"
      "v_or3_b32 v158, v158, v161, v90\n"
      "v_mov_b32_e64 v177, v181\n"
      "v_mfma_f32_16x16x32_bf16 v[198:201], v[228:231], v[122:125], v[198:201]\n"
      "s_nop 0\n"
      "v_permlane32_swap_b32 v181, v177\n"
      "v_mfma_f32_16x16x32_bf16 v[142:145], v[244:247], v[170:173], v[142:145]\n"
      "v_max_f32 v181, v181, v177\n"
      "v_sub_f32 v127, v127, v181\n"
      "v_mfma_f32_16x16x32_bf16 v[142:145], v[248:251], v[190:193], v[142:145]\n"
      "ds_read_b128 v[188:191], v2 offset:24832\n"
      "v_sub_f32 v205, v205, v181\n"
      "v_mfma_f32_16x16x32_bf16 v[142:145], v[252:255], v[122:125], v[142:145]\n"
      "v_sub_f32 v204, v204, v181\n"
      "v_sub_f32 v139, v139, v181\n"
      "v_mfma_f32_16x16x32_bf16 v[114:117], v[146:149], v[134:137], v[52:55]\n"
      "v_sub_f32 v128, v128, v181\n"
      "v_sub_f32 v141, v141, v181\n"
      "v_sub_f32 v162, v162, v181\n"
      "v_sub_f32_e64 v138, v138, v181\n"
      "v_mfma_f32_16x16x32_bf16 v[176:179], v[154:157], v[134:137], v[56:59]\n"
      "v_exp_f32 v162, v162\n"
      "v_sub_f32 v126, v126, v181\n"
      "v_mfma_f32_16x16x32_bf16 v[206:209], v[104:107], v[134:137], v[60:63]\n"
      "v_exp_f32_e64 v127, v127\n"
      "v_mfma_f32_16x16x32_bf16 v[122:125], v[220:223], v[134:137], v[26:29]\n"
      "s_waitcnt lgkmcnt(1)\n"
      "v_cvt_pk_bf16_f32 v90, v162, 0\n"
      "v_sub_f32 v203, v203, v181\n"
      "v_mfma_f32_16x16x16_bf16 v[114:117], v[112:113], v[186:187], v[114:117]\n"
      "v_exp_f32 v205, v205\n"
      "v_exp_f32 v139, v139\n"
      "v_mfma_f32_16x16x16_bf16 v[176:179], v[108:109], v[186:187], v[176:179]\n"
      "v_exp_f32 v128, v128\n"
      "v_sub_f32 v202, v202, v181\n"
      "v_sub_f32_e64 v140, v140, v181\n"
      "v_mfma_f32_16x16x16_bf16 v[206:209], v[98:99], v[186:187], v[206:209]\n"
      "v_exp_f32 v138, v138\n"
      "v_exp_f32 v126, v126\n"
      "v_sub_f32 v129, v129, v181\n"
      "v_mfma_f32_16x16x16_bf16 v[122:125], v[218:219], v[186:187], v[122:125]\n"
      "v_exp_f32 v141, v141\n"
      "v_cvt_pk_bf16_f32 v138, v138, v139\n"
      "v_mfma_f32_16x16x32_bf16 v[162:165], a[216:219], v[100:103], 0\n"
      "v_exp_f32_e64 v204, v204\n"
      "v_max3_f32 v186, v114, v115, v116\n"
      "v_mfma_f32_16x16x32_bf16 v[162:165], a[220:223], v[118:121], v[162:165]\n"
      "s_waitcnt lgkmcnt(0)\n"
      "v_exp_f32 v140, v140\n"
      "v_mfma_f32_16x16x32_bf16 v[162:165], a[224:227], v[188:191], v[162:165]\n"
      "v_cvt_pk_bf16_f32 v89, v204, v205\n"
      "v_cvt_pk_bf16_f32 v139, v140, v141\n"
      "v_mfma_f32_16x16x32_bf16 v[180:183], a[240:243], v[100:103], 0\n"
      "v_exp_f32_e64 v202, v202\n"
      "v_mfma_f32_16x16x32_bf16 v[180:183], a[244:247], v[118:121], v[180:183]\n"
      "v_cvt_pk_bf16_f32 v140, v126, v127\n"
      "v_max3_f32 v134, v117, v176, v177\n"
      "v_mfma_f32_16x16x32_bf16 v[180:183], a[248:251], v[188:191], v[180:183]\n"
      "ds_write_b32 v31, v158 offset:4608\n"
      "v_mfma_f32_16x16x32_bf16 v[170:173], v[232:235], v[100:103], 0\n"
      "v_exp_f32_e64 v129, v129\n"
      "v_mfma_f32_16x16x32_bf16 v[170:173], v[236:239], v[118:121], v[170:173]\n"
      "v_max3_f32 v186, v186, v178, v179\n"
      "v_cvt_pk_bf16_f32 v141, v128, v129\n"
      "v_mfma_f32_16x16x32_bf16 v[170:173], v[240:243], v[188:191], v[170:173]\n"
      "v_exp_f32_e64 v203, v203\n"
      "v_mfma_f32_16x16x32_bf16 v[126:129], v[32:35], v[138:141], 0\n"
      "v_cvt_pk_bf16_f32 v88, v202, v203\n"
      "v_max3_f32 v186, v186, v208, v209\n"
      "v_max3_f32 v134, v134, v206, v207\n"
      "v_max3_f32 v186, v186, v134, v122\n"
      "v_mfma_f32_16x16x32_bf16 v[126:129], v[32:35], v[88:91], v[126:129]\n"
      "ds_read_b128 v[188:191], v2 offset:27904\n"
      "v_mov_b32 v120, v186\n"
      "v_mfma_f32_16x16x32_bf16 v[100:103], v[130:133], v[138:141], 0\n"
      "s_nop 0\n"
      "v_permlane16_swap_b32 v186, v120\n"
      "v_mfma_f32_16x16x32_bf16 v[100:103], v[76:79], v[88:91], v[100:103]\n"
      "v_max_f32 v186, v186, v120\n"
      "v_mov_b32 v120, v186\n"
      "v_mfma_f32_16x16x32_bf16 v[134:137], v[194:197], v[138:141], 0\n"
      "v_rcp_f32 v126, v126\n"
      "v_mfma_f32_16x16x32_bf16 v[134:137], v[80:83], v[88:91], v[134:137]\n"
      "v_permlane32_swap_b32 v186, v120\n"
      "v_max_f32 v186, v186, v120\n"
      "v_mfma_f32_16x16x32_bf16 v[118:121], v[214:217], v[138:141], 0\n"
      "v_sub_f32 v115, v115, v186\n"
      "v_sub_f32 v116, v116, v186\n"
      "v_mfma_f32_16x16x32_bf16 v[118:121], v[84:87], v[88:91], v[118:121]\n"
      "v_sub_f32 v176, v176, v186\n"
      "v_sub_f32 v208, v208, v186\n"
      "v_sub_f32 v207, v207, v186\n"
      "v_exp_f32 v208, v208\n"
      "v_pk_mul_f32 v[100:101], v[100:101], v[126:127] op_sel_hi:[1,0]\n"
      "v_pk_mul_f32 v[102:103], v[102:103], v[126:127] op_sel_hi:[1,0]\n"
      "v_sub_f32 v206, v206, v186\n"
      "v_pk_mul_f32 v[134:135], v[134:135], v[126:127] op_sel_hi:[1,0]\n"
      "v_exp_f32 v115, v115\n"
      "v_sub_f32_e64 v177, v177, v186\n"
      "v_pk_mul_f32 v[120:121], v[120:121], v[126:127] op_sel_hi:[1,0]\n"
      "v_sub_f32 v117, v117, v186\n"
      "v_sub_f32 v122, v122, v186\n"
      "v_pk_mul_f32 v[118:119], v[118:119], v[126:127] op_sel_hi:[1,0]\n"
      "v_cvt_pk_bf16_f32 v118, v118, v119\n"
      "v_pk_mul_f32 v[136:137], v[136:137], v[126:127] op_sel_hi:[1,0]\n"
      "v_cvt_pk_bf16_f32 v100, v100, v101\n"
      "v_cvt_pk_bf16_f32 v101, v102, v103\n"
      "v_mfma_f32_16x16x32_bf16 v[150:153], v[146:149], v[210:213], v[64:67]\n"
      "s_waitcnt lgkmcnt(0)\n"
      "v_cvt_pk_bf16_f32 v102, v134, v135\n"
      "v_exp_f32_e64 v206, v206\n"
      "v_mfma_f32_16x16x32_bf16 v[202:205], v[154:157], v[210:213], v[68:71]\n"
      "s_and_b32 s86, s86, -4\n"
      "v_cvt_pk_bf16_f32 v103, v136, v137\n"
      "v_cvt_pk_bf16_f32 v119, v120, v121\n"
      "v_mfma_f32_16x16x32_bf16 v[134:137], v[104:107], v[210:213], v[72:75]\n"
      "ds_read_b128 v[126:129], v2 offset:30976\n"
      "v_sub_f32 v179, v179, v186\n"
      "v_mfma_f32_16x16x32_bf16 v[138:141], v[220:223], v[210:213], v[28:31]\n"
      "ds_read_b128 v[210:213], v2 offset:34048\n"
      "v_exp_f32 v122, v122\n"
      "v_mfma_f32_16x16x16_bf16 v[150:153], v[112:113], v[174:175], v[150:153]\n"
      "v_sub_f32 v209, v209, v186\n"
      "v_exp_f32 v207, v207\n"
      "v_mfma_f32_16x16x16_bf16 v[202:205], v[108:109], v[174:175], v[202:205]\n"
      "v_exp_f32 v209, v209\n"
      "v_sub_f32 v178, v178, v186\n"
      "v_mfma_f32_16x16x16_bf16 v[134:137], v[98:99], v[174:175], v[134:137]\n"
      "v_sub_f32 v114, v114, v186\n"
      "v_exp_f32 v177, v177\n"
      "v_mfma_f32_16x16x16_bf16 v[138:141], v[218:219], v[174:175], v[138:141]\n"
      "v_exp_f32_e64 v114, v114\n"
      "v_cvt_pk_bf16_f32 v92, v206, v207\n"
      "v_mfma_f32_16x16x32_bf16 v[162:165], a[228:231], v[188:191], v[162:165]\n"
      "s_waitcnt lgkmcnt(1)\n"
      "v_exp_f32_e64 v176, v176\n"
      "v_mfma_f32_16x16x32_bf16 v[162:165], a[232:235], v[126:129], v[162:165]\n"
      "s_waitcnt lgkmcnt(0)\n"
      "v_cvt_pk_bf16_f32 v114, v114, v115\n"
      "v_exp_f32 v116, v116\n"
      "v_mfma_f32_16x16x32_bf16 v[162:165], a[236:239], v[210:213], v[162:165]\n"
      "v_exp_f32 v117, v117\n"
      "v_mfma_f32_16x16x32_bf16 v[180:183], a[252:255], v[188:191], v[180:183]\n"
      "v_exp_f32 v179, v179\n"
      "v_mfma_f32_16x16x32_bf16 v[180:183], v[224:227], v[126:129], v[180:183]\n"
      "v_exp_f32 v178, v178\n"
      "v_mfma_f32_16x16x32_bf16 v[180:183], v[228:231], v[210:213], v[180:183]\n"
      "v_max3_f32 v185, v150, v151, v152\n"
      "v_cvt_pk_bf16_f32 v115, v116, v117\n"
      "v_cvt_pk_bf16_f32 v117, v178, v179\n"
      "v_mfma_f32_16x16x32_bf16 v[170:173], v[244:247], v[188:191], v[170:173]\n"
      "ds_read_b128 v[188:191], v10\n"
      "v_cvt_pk_bf16_f32 v116, v176, v177\n"
      "v_mfma_f32_16x16x32_bf16 v[170:173], v[248:251], v[126:129], v[170:173]\n"
      "v_max3_f32 v179, v153, v202, v203\n"
      "v_cvt_pk_bf16_f32 v94, v122, 0\n"
      "v_mfma_f32_16x16x32_bf16 v[170:173], v[252:255], v[210:213], v[170:173]\n"
      "v_max3_f32 v179, v179, v134, v135\n"
      "v_cvt_pk_bf16_f32 v93, v208, v209\n"
      "v_mfma_f32_16x16x32_bf16 v[174:177], v[32:35], v[114:117], 0\n"
      "v_max3_f32 v185, v185, v204, v205\n"
      "v_max3_f32 v185, v185, v136, v137\n"
      "v_mfma_f32_16x16x32_bf16 v[174:177], v[32:35], v[92:95], v[174:177]\n"
      "v_max3_f32 v185, v185, v179, v138\n"
      "v_mov_b32 v209, v185\n"
      "v_mfma_f32_16x16x32_bf16 v[210:213], v[130:133], v[114:117], 0\n"
      "s_nop 0\n"
      "v_permlane16_swap_b32_e64 v185, v209\n"
      "v_mfma_f32_16x16x32_bf16 v[210:213], v[76:79], v[92:95], v[210:213]\n"
      "ds_read_b128 v[122:125], v2 offset:18944\n"
      "v_max_f32 v185, v185, v209\n"
      "v_mfma_f32_16x16x32_bf16 v[158:161], v[194:197], v[114:117], 0\n"
      "ds_read_b64 v[186:187], v10 offset:16\n"
      "v_mov_b32 v209, v185\n"
      "v_mfma_f32_16x16x32_bf16 v[158:161], v[80:83], v[92:95], v[158:161]\n"
      "ds_write_b64 v4, v[118:119]\n"
      "v_mfma_f32_16x16x32_bf16 v[176:179], v[214:217], v[114:117], 0\n"
      "v_permlane32_swap_b32_e64 v185, v209\n"
      "v_mfma_f32_16x16x32_bf16 v[176:179], v[84:87], v[92:95], v[176:179]\n"
      "s_waitcnt lgkmcnt(3)\n"
      "v_max_f32 v185, v185, v209\n"
      "v_sub_f32 v152, v152, v185\n"
      "v_mfma_f32_16x16x32_bf16 v[114:117], v[188:191], v[146:149], v[30:33]\n"
      "v_rcp_f32 v174, v174\n"
      "v_mfma_f32_16x16x32_bf16 v[126:129], v[188:191], v[154:157], v[30:33]\n"
      "ds_read_b128 v[206:209], v2 offset:25088\n"
      "v_sub_f32 v135, v135, v185\n"
      "v_mfma_f32_16x16x32_bf16 v[154:157], v[188:191], v[104:107], v[30:33]\n"
      "s_waitcnt lgkmcnt(2)\n"
      "v_sub_f32 v153, v153, v185\n"
      "v_sub_f32 v138, v138, v185\n"
      "v_mfma_f32_16x16x32_bf16 v[104:107], v[188:191], v[220:223], v[30:33]\n"
      "ds_read_b128 v[188:191], v2 offset:22016\n"
      "v_sub_f32_e64 v151, v151, v185\n"
      "v_mfma_f32_16x16x16_bf16 v[114:117], v[186:187], v[112:113], v[114:117]\n"
      "v_sub_f32 v203, v203, v185\n"
      "v_exp_f32 v152, v152\n"
      "v_mfma_f32_16x16x16_bf16 v[126:129], v[186:187], v[108:109], v[126:129]\n"
      "v_exp_f32 v153, v153\n"
      "v_sub_f32 v137, v137, v185\n"
      "v_fma_f32 v222, v166, v166, s52\n"
      "v_mfma_f32_16x16x16_bf16 v[154:157], v[186:187], v[98:99], v[154:157]\n"
      "v_sub_f32 v150, v150, v185\n"
      "v_mul_f32 v223, v167, v167\n"
      "v_exp_f32 v151, v151\n"
      "v_mul_f32 v113, v163, v163\n"
      "v_fma_f32 v112, v162, v162, s52\n"
      "v_sub_f32_e64 v134, v134, v185\n"
      "v_pk_fma_f32 v[222:223], v[168:169], v[168:169], v[222:223]\n"
      "v_pk_mul_f32 v[212:213], v[212:213], v[174:175] op_sel_hi:[1,0]\n"
      "v_pk_fma_f32 v[112:113], v[164:165], v[164:165], v[112:113]\n"
      "v_pk_mul_f32 v[178:179], v[178:179], v[174:175] op_sel_hi:[1,0]\n"
      "v_pk_mul_f32 v[158:159], v[158:159], v[174:175] op_sel_hi:[1,0]\n"
      "v_cndmask_b32 v114, v114, v126, s[58:59]\n"
      "v_mfma_f32_16x16x16_bf16 v[104:107], v[186:187], v[218:219], v[104:107]\n"
      "v_exp_f32 v135, v135\n"
      "v_sub_f32 v136, v136, v185\n"
      "v_mfma_f32_16x16x32_bf16 v[126:129], a[216:219], v[122:125], 0\n"
      "s_waitcnt lgkmcnt(0)\n"
      "v_exp_f32 v134, v134\n"
      "v_sub_f32 v202, v202, v185\n"
      "v_mfma_f32_16x16x32_bf16 v[126:129], a[220:223], v[188:191], v[126:129]\n"
      "ds_read_b128 v[218:221], v2 offset:31232\n"
      "v_sub_f32 v205, v205, v185\n"
      "v_mfma_f32_16x16x32_bf16 v[126:129], a[224:227], v[206:209], v[126:129]\n"
      "v_sub_f32 v204, v204, v185\n"
      "v_exp_f32 v150, v150\n"
      "v_mfma_f32_16x16x32_bf16 v[116:119], a[240:243], v[122:125], 0\n"
      "ds_read_b128 v[184:187], v2 offset:34304\n"
      "v_cndmask_b32 v114, v114, v154, s[60:61]\n"
      "v_cndmask_b32 v114, v114, v104, s[62:63]\n"
      "v_mfma_f32_16x16x32_bf16 v[116:119], a[244:247], v[188:191], v[116:119]\n"
      "ds_read_b128 v[96:99], v2 offset:28160\n"
      "v_cvt_pk_bf16_f32 v150, v150, v151\n"
      "v_mfma_f32_16x16x32_bf16 v[116:119], a[248:251], v[206:209], v[116:119]\n"
      "ds_write_b128 v3, v[100:103]\n"
      "v_pk_mul_f32 v[210:211], v[210:211], v[174:175] op_sel_hi:[1,0]\n"
      "v_pk_mul_f32 v[176:177], v[176:177], v[174:175] op_sel_hi:[1,0]\n"
      "v_pk_mul_f32 v[160:161], v[160:161], v[174:175] op_sel_hi:[1,0]\n"
      "v_pk_fma_f32 v[112:113], v[180:181], v[180:181], v[112:113]\n"
      "v_pk_fma_f32 v[222:223], v[198:199], v[198:199], v[222:223]\n"
      "v_mfma_f32_16x16x32_bf16 v[108:111], v[232:235], v[122:125], 0\n"
      "v_cvt_pk_bf16_f32 v151, v152, v153\n"
      "v_max_f32_dpp v174, v114, v114 row_ror:8 row_mask:0xf bank_mask:0xf\n"
      "v_mfma_f32_16x16x32_bf16 v[108:111], v[236:239], v[188:191], v[108:111]\n"
      "s_nop 0\n"
      "v_max_f32_dpp v174, v174, v174 row_ror:4 row_mask:0xf bank_mask:0xf\n"
      "v_exp_f32 v203, v203\n"
      "v_mfma_f32_16x16x32_bf16 v[108:111], v[240:243], v[206:209], v[108:111]\n"
      "s_waitcnt lgkmcnt(1)\n"
      "v_exp_f32 v138, v138\n"
      "v_mfma_f32_16x16x32_bf16 v[126:129], a[228:231], v[96:99], v[126:129]\n"
      "v_exp_f32 v202, v202\n"
      "v_max_f32_dpp v174, v174, v174 row_ror:2 row_mask:0xf bank_mask:0xf\n"
      "v_cvt_pk_bf16_f32 v210, v210, v211\n"
      "v_mfma_f32_16x16x32_bf16 v[126:129], a[232:235], v[218:221], v[126:129]\n"
      "v_cvt_pk_bf16_f32 v211, v212, v213\n"
      "v_max_f32_dpp v174, v174, v174 row_ror:1 row_mask:0xf bank_mask:0xf\n"
      "v_mfma_f32_16x16x32_bf16 v[126:129], a[236:239], v[184:187], v[126:129]\n"
      "s_nop 0\n"
      "v_max_f32_dpp v174, v174, v174 row_bcast:15 row_mask:0xa bank_mask:0xf\n"
      "v_cvt_pk_bf16_f32 v176, v176, v177\n"
      "v_mfma_f32_16x16x32_bf16 v[116:119], a[252:255], v[96:99], v[116:119]\n"
      "v_max_f32_dpp v174, v174, v174 row_bcast:31 row_mask:0xc bank_mask:0xf\n"
      "v_cvt_pk_bf16_f32 v213, v160, v161\n"
      "v_mfma_f32_16x16x32_bf16 v[116:119], v[224:227], v[218:221], v[116:119]\n"
      "v_exp_f32 v136, v136\n"
      "v_mfma_f32_16x16x32_bf16 v[116:119], v[228:231], v[184:187], v[116:119]\n"
      "v_exp_f32 v137, v137\n"
      "v_cvt_pk_bf16_f32 v212, v158, v159\n"
      "v_readlane_b32 s72, v174, 63\n"
      "v_mfma_f32_16x16x32_bf16 v[108:111], v[244:247], v[96:99], v[108:111]\n"
      "v_mul_f32 v175, v127, v127\n"
      "v_cvt_pk_bf16_f32 v89, v136, v137\n"
      "v_subrev_f32 v114, s72, v114\n"
      "v_fma_f32 v174, v126, v126, s52\n"
      "v_mfma_f32_16x16x32_bf16 v[108:111], v[248:251], v[218:221], v[108:111]\n"
      "v_cvt_pk_bf16_f32 v177, v178, v179\n"
      "v_exp_f32_e64 v114, v114\n"
      "v_mfma_f32_16x16x32_bf16 v[108:111], v[252:255], v[184:187], v[108:111]\n"
      "ds_write_b128 v3, v[210:213] offset:256\n"
      "v_pk_fma_f32 v[222:223], v[200:201], v[200:201], v[222:223]\n"
      "v_pk_fma_f32 v[174:175], v[128:129], v[128:129], v[174:175]\n"
      "v_exp_f32 v205, v205\n"
      "v_pk_fma_f32 v[112:113], v[182:183], v[182:183], v[112:113]\n"
      "v_exp_f32 v204, v204\n"
      "v_cvt_pk_bf16_f32 v88, v134, v135\n"
      "v_pk_fma_f32 v[174:175], v[116:117], v[116:117], v[174:175]\n"
      "v_cvt_pk_bf16_f32 v153, v204, v205\n"
      "v_pk_fma_f32 v[222:223], v[142:143], v[142:143], v[222:223]\n"
      "v_pk_fma_f32 v[174:175], v[118:119], v[118:119], v[174:175]\n"
      "v_cvt_pk_bf16_f32 v90, v138, 0\n"
      "v_pk_fma_f32 v[112:113], v[170:171], v[170:171], v[112:113]\n"
      "v_cvt_pk_bf16_f32 v152, v202, v203\n"
      "v_pk_fma_f32 v[174:175], v[108:109], v[108:109], v[174:175]\n"
      "v_pk_fma_f32 v[222:223], v[144:145], v[144:145], v[222:223]\n"
      "v_pk_fma_f32 v[112:113], v[172:173], v[172:173], v[112:113]\n"
      "v_pk_fma_f32 v[174:175], v[110:111], v[110:111], v[174:175]\n"
      "v_add_f32 v222, v222, v223\n"
      "v_mfma_f32_16x16x32_bf16 v[210:213], v[32:35], v[150:153], 0\n"
      "v_add_f32 v112, v112, v113\n"
      "v_cvt_pk_bf16_f32 v114, v114, v114\n"
      "v_mfma_f32_16x16x32_bf16 v[210:213], v[32:35], v[88:91], v[210:213]\n"
      "v_permlane16_swap_b32 v222, v112\n"
      "v_add_f32 v222, v222, v112\n"
      "v_add_f32 v174, v174, v175\n"
      "v_mov_b32 v112, v174\n"
      "v_mfma_f32_16x16x32_bf16 v[188:191], v[130:133], v[150:153], 0\n"
      "ds_write_b16 v13, v114\n"
      "v_mfma_f32_16x16x32_bf16 v[188:191], v[76:79], v[88:91], v[188:191]\n"
      "v_permlane16_swap_b32 v174, v112\n"
      "v_add_f32 v174, v174, v112\n"
      "v_mfma_f32_16x16x32_bf16 v[218:221], v[194:197], v[150:153], 0\n"
      "s_nop 0\n"
      "v_permlane32_swap_b32 v222, v174\n"
      "v_add_f32_e64 v222, v222, v174\n"
      "v_mfma_f32_16x16x32_bf16 v[218:221], v[80:83], v[88:91], v[218:221]\n"
      "ds_write_b32 v31, v222\n"
      "v_mfma_f32_16x16x32_bf16 v[134:137], v[214:217], v[150:153], 0\n"
      "v_rcp_f32_e64 v210, v210\n"
      "v_mfma_f32_16x16x32_bf16 v[134:137], v[84:87], v[88:91], v[134:137]\n"
      "ds_write_b64 v4, v[176:177] offset:256\n"
      "v_mul_f32 v188, v210, v188\n"
      "v_mul_f32_e64 v189, v210, v189\n"
      "v_pk_mul_f32 v[190:191], v[190:191], v[210:211] op_sel_hi:[1,0]\n"
      "v_cvt_pk_bf16_f32 v188, v188, v189\n"
      "v_cvt_pk_bf16_f32 v189, v190, v191\n"
      "v_pk_mul_f32 v[220:221], v[220:221], v[210:211] op_sel_hi:[1,0]\n"
      "v_cvt_pk_bf16_f32 v191, v220, v221\n"
      "v_pk_mul_f32 v[218:219], v[218:219], v[210:211] op_sel_hi:[1,0]\n"
      "v_cvt_pk_bf16_f32 v190, v218, v219\n"
      "s_add_u32 s42, s42, 1\n"
      "ds_write_b128 v3, v[188:191] offset:512\n"
      "ds_read_b128 v[174:177], v14\n"
      "v_pk_mul_f32 v[136:137], v[136:137], v[210:211] op_sel_hi:[1,0]\n"
      "v_pk_mul_f32 v[134:135], v[134:135], v[210:211] op_sel_hi:[1,0]\n"
      "v_cvt_pk_bf16_f32 v134, v134, v135\n"
      "v_cvt_pk_bf16_f32 v135, v136, v137\n"
      "ds_read_b128 v[210:213], v14 offset:64\n"
      "ds_write_b64 v4, v[134:135] offset:512\n"
      "s_waitcnt lgkmcnt(2)\n"
      "v_mfma_f32_16x16x32_bf16 v[188:191], v[174:177], v[32:35], 0\n"
      "s_waitcnt lgkmcnt(1)\n"
      "v_mfma_f32_16x16x32_bf16 v[188:191], v[210:213], v[32:35], v[188:191]\n"
      "s_waitcnt lgkmcnt(0)\n"
      "s_barrier\n"
      "s_cbranch_vccz L_max1_%=\n"
      "L_cold_%=:\n"
      "L_disp1_%=:\n"
      "s_cmp_lt_u32 s95, 0x40000000\n"
      "s_cbranch_scc1 L_nofix_11_%=\n"
      "s_cmp_eq_u32 s95, -2\n"
      "s_cbranch_scc1 L_tail1_%=\n"
      "s_nop 2\n"
      "v_lshlrev_b32 v219, 3, v36\n"
      "s_add_u32 s72, s98, L_cold_%=-L_ipf0_%=\n"
      "s_addc_u32 s73, s99, 0\n"
      "s_mov_b32 s74, L_cend_%=-L_cold_%=\n"
      "s_mov_b32 s75, 0x20000\n"
      "s_mov_b32 m0, 161792\n"
      "s_nop 0\n"
      "buffer_load_dwordx4 v219, s[72:75], 0 offen  lds\n"
      "s_lshr_b32 s92, s84, 30\n"
      "s_or_b32 s92, s92, s66\n"
      "s_cbranch_scc0 L_nf_12_%=\n"
      "s_bitcmp1_b32 s84, 31\n"
      "s_cselect_b32 s75, 100270464, 0\n"
      "s_bitcmp1_b32 s84, 30\n"
      "s_cselect_b32 s76, 196224, 0\n"
      "s_and_b32 s74, s84, 0x3fffffff\n"
      "s_cmp_ge_u32 s84, 0x40000000\n"
      "s_cselect_b32 s74, s74, 0\n"
      "s_cselect_b32 s84, 0, s84\n"
      "s_sub_u32 s74, s74, s66\n"
      "s_add_u32 s66, s66, s74\n"
      "s_sub_u32 s75, s75, s67\n"
      "s_add_u32 s67, s67, s75\n"
      "s_sub_u32 s76, s76, s68\n"
      "s_add_u32 s68, s68, s76\n"
      "v_add_u32 v16, s74, v16\n"
      "v_add_u32 v17, s74, v17\n"
      "v_add_u32 v18, s74, v18\n"
      "v_add_u32 v19, s74, v19\n"
      "v_add_u32 v15, s74, v15\n"
      "s_cmp_eq_u32 s75, 0\n"
      "s_cbranch_scc1 L_ax_13_%=\n"
      "v_bfe_i32 v219, v27, 0, 1\n"
      "v_and_b32 v219, s75, v219\n"
      "v_sub_u32 v16, v16, v219\n"
      "v_bfe_i32 v219, v27, 2, 1\n"
      "v_and_b32 v219, s75, v219\n"
      "v_sub_u32 v17, v17, v219\n"
      "v_bfe_i32 v219, v27, 4, 1\n"
      "v_and_b32 v219, s75, v219\n"
      "v_sub_u32 v18, v18, v219\n"
      "v_bfe_i32 v219, v27, 6, 1\n"
      "v_and_b32 v219, s75, v219\n"
      "v_sub_u32 v19, v19, v219\n"
      "v_bfe_i32 v219, v27, 8, 1\n"
      "v_and_b32 v219, s75, v219\n"
      "v_sub_u32 v15, v15, v219\n"
      "L_ax_13_%=:\n"
      "s_cmp_eq_u32 s76, 0\n"
      "s_cbranch_scc1 L_ax_14_%=\n"
      "v_bfe_i32 v219, v27, 1, 1\n"
      "v_and_b32 v219, s76, v219\n"
      "v_sub_u32 v16, v16, v219\n"
      "v_bfe_i32 v219, v27, 3, 1\n"
      "v_and_b32 v219, s76, v219\n"
      "v_sub_u32 v17, v17, v219\n"
      "v_bfe_i32 v219, v27, 5, 1\n"
      "v_and_b32 v219, s76, v219\n"
      "v_sub_u32 v18, v18, v219\n"
      "v_bfe_i32 v219, v27, 7, 1\n"
      "v_and_b32 v219, s76, v219\n"
      "v_sub_u32 v19, v19, v219\n"
      "v_bfe_i32 v219, v27, 9, 1\n"
      "v_and_b32 v219, s76, v219\n"
      "v_sub_u32 v15, v15, v219\n"
      "L_ax_14_%=:\n"
      "s_or_b32 s48, s66, s69\n"
      "s_cmp_lg_u32 s48, 0\n"
      "s_cselect_b32 s48, 0x40000000, 0\n"
      "L_nf_12_%=:\n"
      "s_lshr_b32 s92, s85, 30\n"
      "s_or_b32 s92, s92, s69\n"
      "s_cbranch_scc0 L_nf_15_%=\n"
      "s_bitcmp1_b32 s85, 31\n"
      "s_cselect_b32 s75, 100270464, 0\n"
      "s_bitcmp1_b32 s85, 30\n"
      "s_cselect_b32 s76, 196224, 0\n"
      "s_and_b32 s74, s85, 0x3fffffff\n"
      "s_cmp_ge_u32 s85, 0x40000000\n"
      "s_cselect_b32 s74, s74, 0\n"
      "s_cselect_b32 s85, 0, s85\n"
      "s_sub_u32 s74, s74, s69\n"
      "s_add_u32 s69, s69, s74\n"
      "s_sub_u32 s75, s75, s70\n"
      "s_add_u32 s70, s70, s75\n"
      "s_sub_u32 s76, s76, s71\n"
      "s_add_u32 s71, s71, s76\n"
      "v_add_u32 v20, s74, v20\n"
      "v_add_u32 v21, s74, v21\n"
      "v_add_u32 v22, s74, v22\n"
      "v_add_u32 v23, s74, v23\n"
      "v_add_u32 v25, s74, v25\n"
      "s_cmp_eq_u32 s75, 0\n"
      "s_cbranch_scc1 L_ax_16_%=\n"
      "v_bfe_i32 v219, v27, 10, 1\n"
      "v_and_b32 v219, s75, v219\n"
      "v_sub_u32 v20, v20, v219\n"
      "v_bfe_i32 v219, v27, 12, 1\n"
      "v_and_b32 v219, s75, v219\n"
      "v_sub_u32 v21, v21, v219\n"
      "v_bfe_i32 v219, v27, 14, 1\n"
      "v_and_b32 v219, s75, v219\n"
      "v_sub_u32 v22, v22, v219\n"
      "v_bfe_i32 v219, v27, 16, 1\n"
      "v_and_b32 v219, s75, v219\n"
      "v_sub_u32 v23, v23, v219\n"
      "v_bfe_i32 v219, v27, 18, 1\n"
      "v_and_b32 v219, s75, v219\n"
      "v_sub_u32 v25, v25, v219\n"
      "L_ax_16_%=:\n"
      "s_cmp_eq_u32 s76, 0\n"
      "s_cbranch_scc1 L_ax_17_%=\n"
      "v_bfe_i32 v219, v27, 11, 1\n"
      "v_and_b32 v219, s76, v219\n"
      "v_sub_u32 v20, v20, v219\n"
      "v_bfe_i32 v219, v27, 13, 1\n"
      "v_and_b32 v219, s76, v219\n"
      "v_sub_u32 v21, v21, v219\n"
      "v_bfe_i32 v219, v27, 15, 1\n"
      "v_and_b32 v219, s76, v219\n"
      "v_sub_u32 v22, v22, v219\n"
      "v_bfe_i32 v219, v27, 17, 1\n"
      "v_and_b32 v219, s76, v219\n"
      "v_sub_u32 v23, v23, v219\n"
      "v_bfe_i32 v219, v27, 19, 1\n"
      "v_and_b32 v219, s76, v219\n"
      "v_sub_u32 v25, v25, v219\n"
      "L_ax_17_%=:\n"
      "s_or_b32 s48, s66, s69\n"
      "s_cmp_lg_u32 s48, 0\n"
      "s_cselect_b32 s48, 0x40000000, 0\n"
      "L_nf_15_%=:\n"
      "s_sub_u32 s92, s42, 1\n"
      "s_cmp_gt_u32 s92, s44\n"
      "s_cbranch_scc1 L_tail1_%=\n"
      "L_nofix_11_%=:\n"
      "s_cmp_lg_u64 s[56:57], 0\n"
      "s_cbranch_scc1 L_slow1_%=\n"
      "L_sel1_%=:\n"
      "s_cmp_lg_u64 s[54:55], 0\n"
      "s_cbranch_scc1 L_max1_%=\n"
      "s_branch L_loop1_%=\n"
      "L_slow1_%=:\n"
      "s_nop 4\n"
      "v_mfma_f32_16x16x32_bf16 v[218:221], v[174:177], v[130:133], 0\n"
      "ds_read_b128 v[96:99], v0 offset:18432\n"
      "v_subrev_u32 v141, 115456, v9\n"
      "v_mfma_f32_16x16x32_bf16 v[218:221], v[210:213], v[76:79], v[218:221]\n"
      "ds_read_b128 v[184:187], v9\n"
      "v_readlane_b32 s45, v37, s43\n"
      "v_mfma_f32_16x16x32_bf16 v[134:137], v[174:177], v[194:197], 0\n"
      "s_mov_b32 m0, 110592\n"
      "s_add_u32 s72, s42, 1\n"
      "v_readlane_b32 s77, v38, s72\n"
      "v_subrev_u32 v130, 115456, v9\n"
      "v_mfma_f32_16x16x32_bf16 v[134:137], v[210:213], v[80:83], v[134:137]\n"
      "s_and_b32 s73, s77, -16\n"
      "ds_read_b128 v[220:223], v0 offset:24576\n"
      "v_subrev_u32 v39, 115456, v9\n"
      "v_mfma_f32_16x16x32_bf16 v[100:103], v[174:177], v[214:217], 0\n"
      "ds_read_b128 v[174:177], v1 offset:18432\n"
      "v_readlane_b32 s46, v37, s42\n"
      "v_mfma_f32_16x16x32_bf16 v[100:103], v[210:213], v[84:87], v[100:103]\n"
      "ds_read_b128 v[210:213], v1 offset:24576\n"
      "s_waitcnt lgkmcnt(4)\n"
      "v_mfma_f32_16x16x32_bf16 v[158:161], a[0:3], v[96:99], 0\n"
      "ds_read_b128 v[136:139], v1 offset:30720\n"
      "v_mov_b32_dpp v188, v188 quad_perm:[0,0,0,0] row_mask:0xf bank_mask:0xf\n"
      "v_mfma_f32_16x16x32_bf16 v[150:153], a[24:27], v[96:99], 0\n"
      "buffer_load_dwordx4 v130, s[28:31], s73 offen  lds\n"
      "v_mfma_f32_16x16x32_bf16 v[190:193], a[48:51], v[96:99], 0\n"
      "s_waitcnt lgkmcnt(4)\n"
      "v_add_f32 v184, v184, v185\n"
      "v_add_f32 v186, v186, v187\n"
      "v_mfma_f32_16x16x32_bf16 v[154:157], a[72:75], v[96:99], 0\n"
      "ds_read_b128 v[120:123], v0 offset:30720\n"
      "v_add_f32 v184, v184, v186\n"
      "v_mfma_f32_16x16x32_bf16 v[112:115], a[96:99], v[96:99], 0\n"
      "s_mov_b32 m0, 111616\n"
      "v_rcp_f32 v188, v188\n"
      "v_mfma_f32_16x16x32_bf16 v[202:205], a[120:123], v[96:99], 0\n"
      "s_waitcnt lgkmcnt(2)\n"
      "v_rsq_f32 v184, v184\n"
      "v_mfma_f32_16x16x32_bf16 v[158:161], a[4:7], v[174:177], v[158:161]\n"
      "s_add_u32 s73, s73, 1024\n"
      "v_mul_f32 v184, 0x415db3d7, v184\n"
      "v_mov_b32_e64 v186, v184\n"
      "v_mfma_f32_16x16x32_bf16 v[158:161], a[8:11], v[220:223], v[158:161]\n"
      "buffer_load_dwordx4 v130, s[28:31], s73 offen  lds\n"
      "v_mfma_f32_16x16x32_bf16 v[150:153], a[28:31], v[174:177], v[150:153]\n"
      "s_add_u32 s73, s73, 1024\n"
      "v_permlane32_swap_b32_e64 v184, v186\n"
      "v_mfma_f32_16x16x32_bf16 v[150:153], a[32:35], v[220:223], v[150:153]\n"
      "s_mov_b32 m0, 112640\n"
      "v_mul_f32 v118, v186, v118\n"
      "v_mov_b32 v185, v184\n"
      "v_mfma_f32_16x16x32_bf16 v[190:193], a[52:55], v[174:177], v[190:193]\n"
      "s_nop 0\n"
      "v_permlane16_swap_b32 v184, v185\n"
      "v_mfma_f32_16x16x32_bf16 v[190:193], a[56:59], v[220:223], v[190:193]\n"
      "v_mul_f32 v167, v184, v167\n"
      "v_mul_f32 v166, v184, v166\n"
      "v_mfma_f32_16x16x32_bf16 v[154:157], a[76:79], v[174:177], v[154:157]\n"
      "buffer_load_dwordx4 v130, s[28:31], s73 offen  lds\n"
      "v_mfma_f32_16x16x32_bf16 v[154:157], a[80:83], v[220:223], v[154:157]\n"
      "v_mul_f32 v168, v184, v168\n"
      "v_mul_f32 v198, v184, v198\n"
      "v_mfma_f32_16x16x32_bf16 v[112:115], a[100:103], v[174:177], v[112:115]\n"
      "s_mov_b32 m0, 113664\n"
      "v_cvt_pk_bf16_f32 v166, v166, v167\n"
      "v_mul_f32 v200, v184, v200\n"
      "v_mfma_f32_16x16x32_bf16 v[112:115], a[104:107], v[220:223], v[112:115]\n"
      "s_add_u32 s73, s73, 1024\n"
      "v_mul_f32 v169, v184, v169\n"
      "v_mul_f32_e64 v199, v184, v199\n"
      "v_mfma_f32_16x16x32_bf16 v[202:205], a[124:127], v[174:177], v[202:205]\n"
      "ds_read_b128 v[146:149], v1 offset:20480\n"
      "v_cvt_pk_bf16_f32 v167, v168, v169\n"
      "v_mfma_f32_16x16x32_bf16 v[202:205], a[128:131], v[220:223], v[202:205]\n"
      "buffer_load_dwordx4 v130, s[28:31], s73 offen  lds\n"
      "v_mfma_f32_16x16x32_bf16 v[158:161], a[12:15], v[210:213], v[158:161]\n"
      "s_waitcnt lgkmcnt(1)\n"
      "v_mul_f32 v172, v185, v172\n"
      "v_mul_f32 v128, v186, v128\n"
      "v_mfma_f32_16x16x32_bf16 v[158:161], a[16:19], v[120:123], v[158:161]\n"
      "ds_read_b128 v[206:209], v0 offset:20480\n"
      "v_mul_f32 v201, v184, v201\n"
      "v_mfma_f32_16x16x32_bf16 v[158:161], a[20:23], v[136:139], v[158:161]\n"
      "s_add_u32 s73, s73, 1024\n"
      "v_mul_f32_e64 v180, v185, v180\n"
      "v_cvt_pk_bf16_f32 v168, v198, v199\n"
      "v_mfma_f32_16x16x32_bf16 v[150:153], a[36:39], v[210:213], v[150:153]\n"
      "v_mul_f32_e64 v181, v185, v181\n"
      "v_mul_f32_e64 v164, v185, v164\n"
      "v_mfma_f32_16x16x32_bf16 v[150:153], a[40:43], v[120:123], v[150:153]\n"
      "v_cvt_pk_bf16_f32 v169, v200, v201\n"
      "s_mov_b32 m0, 114688\n"
      "v_mfma_f32_16x16x32_bf16 v[150:153], a[44:47], v[136:139], v[150:153]\n"
      "s_mov_b32 exec_hi, 0xffff\n"
      "buffer_load_dwordx4 v130, s[28:31], s73 offen  lds\n"
      "s_mov_b32 exec_hi, -1\n"
      "v_pk_mul_f32 v[142:143], v[142:143], v[184:185] op_sel_hi:[1,0]\n"
      "v_pk_mul_f32 v[144:145], v[144:145], v[184:185] op_sel_hi:[1,0]\n"
      "v_pk_mul_f32 v[170:171], v[170:171], v[184:185] op_sel:[0,1]\n"
      "v_pk_mul_f32 v[162:163], v[162:163], v[184:185] op_sel:[0,1]\n"
      "v_pk_mul_f32 v[182:183], v[182:183], v[184:185] op_sel:[0,1]\n"
      "v_mfma_f32_16x16x32_bf16 v[190:193], a[60:63], v[210:213], v[190:193]\n"
      "ds_write_b128 v5, v[166:169] offset:18432\n"
      "v_mfma_f32_16x16x32_bf16 v[190:193], a[64:67], v[120:123], v[190:193]\n"
      "ds_read_b128 v[166:169], v0 offset:26624\n"
      "v_cvt_pk_bf16_f32 v142, v142, v143\n"
      "v_mfma_f32_16x16x32_bf16 v[190:193], a[68:71], v[136:139], v[190:193]\n"
      "s_add_u32 m0, s64, 0\n"
      "v_cvt_pk_bf16_f32 v162, v162, v163\n"
      "v_cvt_pk_bf16_f32 v143, v144, v145\n"
      "v_mfma_f32_16x16x32_bf16 v[154:157], a[84:87], v[210:213], v[154:157]\n"
      "ds_write_b64 v6, v[142:143] offset:18432\n"
      "v_mfma_f32_16x16x32_bf16 v[154:157], a[88:91], v[120:123], v[154:157]\n"
      "buffer_load_dwordx4 v16, s[16:19], s84 offen  lds\n"
      "v_mfma_f32_16x16x32_bf16 v[154:157], a[92:95], v[136:139], v[154:157]\n"
      "s_add_u32 s73, s73, 1024\n"
      "v_cvt_pk_bf16_f32 v170, v170, v171\n"
      "v_mul_f32_e64 v165, v185, v165\n"
      "v_mfma_f32_16x16x32_bf16 v[112:115], a[108:111], v[210:213], v[112:115]\n"
      "s_add_u32 m0, s64, 4096\n"
      "v_cvt_pk_bf16_f32 v163, v164, v165\n"
      "v_mul_f32 v110, v186, v110\n"
      "v_mfma_f32_16x16x32_bf16 v[112:115], a[112:115], v[120:123], v[112:115]\n"
      "v_mul_f32 v108, v186, v108\n"
      "v_cvt_pk_bf16_f32 v165, v182, v183\n"
      "v_mfma_f32_16x16x32_bf16 v[112:115], a[116:119], v[136:139], v[112:115]\n"
      "v_mul_f32 v109, v186, v109\n"
      "v_mul_f32 v126, v186, v126\n"
      "v_mfma_f32_16x16x32_bf16 v[202:205], a[132:135], v[210:213], v[202:205]\n"
      "buffer_load_dwordx4 v17, s[16:19], s84 offen  lds\n"
      "v_mfma_f32_16x16x32_bf16 v[202:205], a[136:139], v[120:123], v[202:205]\n"
      "v_bfe_u32 v78, v141, 4, 4\n"
      "v_mul_f32_e64 v173, v185, v173\n"
      "v_mfma_f32_16x16x32_bf16 v[202:205], a[140:143], v[136:139], v[202:205]\n"
      "s_add_u32 m0, s64, 8192\n"
      "v_cvt_pk_bf16_f32 v164, v180, v181\n"
      "v_fma_f32 v185, v154, v154, s51\n"
      "v_mfma_f32_16x16x32_bf16 v[130:133], v[96:99], a[144:147], 0\n"
      "ds_write_b128 v5, v[162:165] offset:24576\n"
      "v_mfma_f32_16x16x32_bf16 v[130:133], v[174:177], a[148:151], v[130:133]\n"
      "buffer_load_dwordx4 v18, s[16:19], s84 offen  lds\n"
      "v_mfma_f32_16x16x32_bf16 v[130:133], v[220:223], a[152:155], v[130:133]\n"
      "v_mul_f32_e64 v116, v186, v116\n"
      "v_cvt_pk_bf16_f32 v108, v108, v109\n"
      "v_mfma_f32_16x16x32_bf16 v[130:133], v[210:213], a[156:159], v[130:133]\n"
      "s_add_u32 m0, s64, 12288\n"
      "v_cvt_pk_bf16_f32 v171, v172, v173\n"
      "v_mul_f32 v119, v186, v119\n"
      "v_mfma_f32_16x16x32_bf16 v[130:133], v[120:123], a[160:163], v[130:133]\n"
      "v_mul_f32 v127, v186, v127\n"
      "v_fmac_f32_e64 v185, v155, v155\n"
      "v_mfma_f32_16x16x32_bf16 v[130:133], v[136:139], a[164:167], v[130:133]\n"
      "ds_write_b64 v6, v[170:171] offset:24576\n"
      "v_mfma_f32_16x16x32_bf16 v[194:197], v[96:99], a[168:171], 0\n"
      "buffer_load_dwordx4 v19, s[16:19], s84 offen  lds\n"
      "v_mfma_f32_16x16x32_bf16 v[194:197], v[174:177], a[172:175], v[194:197]\n"
      "v_cvt_pk_bf16_f32 v126, v126, v127\n"
      "v_lshrrev_b32_e64 v141, 8, v141\n"
      "v_mfma_f32_16x16x32_bf16 v[194:197], v[220:223], a[176:179], v[194:197]\n"
      "ds_read_b128 v[142:145], v1 offset:26624\n"
      "v_fma_f32 v219, v158, v158, s51\n"
      "v_mfma_f32_16x16x32_bf16 v[194:197], v[210:213], a[180:183], v[194:197]\n"
      "s_add_u32 m0, s65, 16384\n"
      "v_mul_f32 v129, v186, v129\n"
      "v_cvt_pk_bf16_f32 v127, v128, v129\n"
      "v_mfma_f32_16x16x32_bf16 v[194:197], v[120:123], a[184:187], v[194:197]\n"
      "ds_read_b128 v[198:201], v0 offset:32768\n"
      "v_fmac_f32 v185, v156, v156\n"
      "v_mfma_f32_16x16x32_bf16 v[194:197], v[136:139], a[188:191], v[194:197]\n"
      "buffer_load_dwordx4 v15, s[16:19], s84 offen  lds\n"
      "v_mfma_f32_16x16x32_bf16 v[214:217], v[96:99], a[192:195], 0\n"
      "v_cndmask_b32 v218, v218, v134, s[58:59]\n"
      "v_cvt_pk_bf16_f32 v129, v118, v119\n"
      "v_mfma_f32_16x16x32_bf16 v[214:217], v[174:177], a[196:199], v[214:217]\n"
      "s_waitcnt lgkmcnt(2)\n"
      "v_cndmask_b32 v218, v218, v100, s[60:61]\n"
      "v_mul_f32 v117, v186, v117\n"
      "v_mfma_f32_16x16x32_bf16 v[214:217], v[220:223], a[200:203], v[214:217]\n"
      "ds_read_b128 v[162:165], v1 offset:32768\n"
      "v_mul_f32 v111, v186, v111\n"
      "v_mfma_f32_16x16x32_bf16 v[214:217], v[210:213], a[204:207], v[214:217]\n"
      "s_and_b32 s77, s77, 15\n"
      "v_cvt_pk_bf16_f32 v128, v116, v117\n"
      "v_lshrrev_b32_e64 v39, 3, v39\n"
      "v_mfma_f32_16x16x32_bf16 v[214:217], v[120:123], a[208:211], v[214:217]\n"
      "buffer_load_dwordx4 v[116:119], v36, s[28:31], s86 offen\n"
      "v_mfma_f32_16x16x32_bf16 v[214:217], v[136:139], a[212:215], v[214:217]\n"
      "ds_write_b128 v5, v[126:129] offset:30720\n"
      "v_mfma_f32_16x16x32_bf16 v[126:129], a[0:3], v[206:209], 0\n"
      "s_or_b32 s95, s45, s46\n"
      "v_fmac_f32 v219, v159, v159\n"
      "v_fmac_f32 v185, v157, v157\n"
      "v_mfma_f32_16x16x32_bf16 v[126:129], a[4:7], v[146:149], v[126:129]\n"
      "s_waitcnt lgkmcnt(1)\n"
      "v_fmac_f32 v185, v112, v112\n"
      "v_fmac_f32 v219, v160, v160\n"
      "v_mfma_f32_16x16x32_bf16 v[126:129], a[8:11], v[166:169], v[126:129]\n"
      "buffer_load_dword v82, v29, s[28:31], s86 offen\n"
      "v_mfma_f32_16x16x32_bf16 v[126:129], a[12:15], v[142:145], v[126:129]\n"
      "v_fmac_f32_e64 v219, v161, v161\n"
      "v_cvt_pk_bf16_f32 v130, v130, v131\n"
      "v_mfma_f32_16x16x32_bf16 v[126:129], a[16:19], v[198:201], v[126:129]\n"
      "v_min_u32 v39, 96, v39\n"
      "v_fmac_f32 v185, v113, v113\n"
      "v_mfma_f32_16x16x32_bf16 v[126:129], a[20:23], v[162:165], v[126:129]\n"
      "v_fmac_f32 v219, v150, v150\n"
      "v_cvt_pk_bf16_f32 v109, v110, v111\n"
      "v_mfma_f32_16x16x32_bf16 v[180:183], a[24:27], v[206:209], 0\n"
      "v_fmac_f32_e64 v185, v114, v114\n"
      "v_mfma_f32_16x16x32_bf16 v[180:183], a[28:31], v[146:149], v[180:183]\n"
      "ds_write_b64 v6, v[108:109] offset:30720\n"
      "v_mfma_f32_16x16x32_bf16 v[180:183], a[32:35], v[166:169], v[180:183]\n"
      "s_or_b32 s95, s95, s48\n"
      "v_fmac_f32 v185, v115, v115\n"
      "v_fmac_f32_e64 v219, v151, v151\n"
      "v_mfma_f32_16x16x32_bf16 v[180:183], a[36:39], v[142:145], v[180:183]\n"
      "v_fma_f32 v184, v126, v126, s51\n"
      "v_mfma_f32_16x16x32_bf16 v[180:183], a[40:43], v[198:201], v[180:183]\n"
      "v_fmac_f32 v185, v202, v202\n"
      "v_fmac_f32 v219, v152, v152\n"
      "v_mfma_f32_16x16x32_bf16 v[180:183], a[44:47], v[162:165], v[180:183]\n"
      "ds_read_b128 v[170:173], v9 offset:4608\n"
      "v_cvt_pk_bf16_f32 v194, v194, v195\n"
      "v_mfma_f32_16x16x32_bf16 v[108:111], a[48:51], v[206:209], 0\n"
      "v_fmac_f32 v219, v153, v153\n"
      "v_fmac_f32 v219, v190, v190\n"
      "v_fmac_f32 v185, v203, v203\n"
      "v_mfma_f32_16x16x32_bf16 v[108:111], a[52:55], v[146:149], v[108:111]\n"
      "v_fmac_f32 v219, v191, v191\n"
      "v_fmac_f32_e64 v185, v204, v204\n"
      "v_mfma_f32_16x16x32_bf16 v[108:111], a[56:59], v[166:169], v[108:111]\n"
      "v_mul_u32_u24 v89, 98, v78\n"
      "v_fmac_f32 v219, v192, v192\n"
      "v_mfma_f32_16x16x32_bf16 v[108:111], a[60:63], v[142:145], v[108:111]\n"
      "v_fmac_f32 v219, v193, v193\n"
      "v_fmac_f32 v185, v205, v205\n"
      "v_mfma_f32_16x16x32_bf16 v[108:111], a[64:67], v[198:201], v[108:111]\n"
      "v_fmac_f32 v184, v127, v127\n"
      "v_cvt_pk_bf16_f32 v195, v196, v197\n"
      "v_mfma_f32_16x16x32_bf16 v[108:111], a[68:71], v[162:165], v[108:111]\n"
      "s_waitcnt lgkmcnt(0)\n"
      "v_permlane16_swap_b32 v219, v185\n"
      "v_mfma_f32_16x16x32_bf16 v[220:223], a[72:75], v[206:209], 0\n"
      "ds_read_b128 v[174:177], v1 offset:22528\n"
      "v_add_f32_e64 v219, v219, v185\n"
      "v_mfma_f32_16x16x32_bf16 v[220:223], a[76:79], v[146:149], v[220:223]\n"
      "v_or3_b32 v170, v170, v171, v172\n"
      "v_mov_b32 v185, v219\n"
      "v_mfma_f32_16x16x32_bf16 v[220:223], a[80:83], v[166:169], v[220:223]\n"
      "s_cmp_ge_u32 s95, 0x40000000\n"
      "s_cselect_b32 s94, -1, -2\n"
      "v_lshl_add_u32 v89, v141, 3, v89\n"
      "v_mfma_f32_16x16x32_bf16 v[220:223], a[84:87], v[142:145], v[220:223]\n"
      "ds_read_b128 v[134:137], v0 offset:22528\n"
      "v_fmac_f32 v184, v128, v128\n"
      "v_mfma_f32_16x16x32_bf16 v[220:223], a[88:91], v[198:201], v[220:223]\n"
      "v_fmac_f32 v184, v129, v129\n"
      "v_fmac_f32 v184, v180, v180\n"
      "v_mfma_f32_16x16x32_bf16 v[220:223], a[92:95], v[162:165], v[220:223]\n"
      "v_permlane32_swap_b32_e64 v219, v185\n"
      "v_mfma_f32_16x16x32_bf16 v[98:101], a[96:99], v[206:209], 0\n"
      "v_lshlrev_b32 v141, 2, v141\n"
      "v_add_f32 v219, v219, v185\n"
      "v_mfma_f32_16x16x32_bf16 v[98:101], a[100:103], v[146:149], v[98:101]\n"
      "v_rsq_f32_e64 v219, v219\n"
      "v_mfma_f32_16x16x32_bf16 v[98:101], a[104:107], v[166:169], v[98:101]\n"
      "s_nop 0\n"
      "v_fma_f32 v210, v220, v220, s51\n"
      "v_mfma_f32_16x16x32_bf16 v[98:101], a[108:111], v[142:145], v[98:101]\n"
      "v_mov_b32 v185, v219\n"
      "v_fmac_f32 v184, v181, v181\n"
      "v_mfma_f32_16x16x32_bf16 v[98:101], a[112:115], v[198:201], v[98:101]\n"
      "v_permlane16_swap_b32 v219, v185\n"
      "v_mfma_f32_16x16x32_bf16 v[98:101], a[116:119], v[162:165], v[98:101]\n"
      "v_mul_f32 v218, v188, v218\n"
      "v_mul_f32 v219, s50, v219\n"
      "v_mfma_f32_16x16x32_bf16 v[102:105], a[120:123], v[206:209], 0\n"
      "v_cvt_pk_bf16_f32 v131, v132, v133\n"
      "v_fmac_f32 v210, v221, v221\n"
      "v_mfma_f32_16x16x32_bf16 v[102:105], a[124:127], v[146:149], v[102:105]\n"
      "v_add_u32 v89, s77, v89\n"
      "v_add_u32 v39, s77, v39\n"
      "v_mfma_f32_16x16x32_bf16 v[102:105], a[128:131], v[166:169], v[102:105]\n"
      "v_fmac_f32 v184, v182, v182\n"
      "v_mul_f32 v190, v219, v190\n"
      "v_mfma_f32_16x16x32_bf16 v[102:105], a[132:135], v[142:145], v[102:105]\n"
      "v_fmac_f32 v210, v222, v222\n"
      "v_mul_f32 v159, v219, v159\n"
      "v_mfma_f32_16x16x32_bf16 v[102:105], a[136:139], v[198:201], v[102:105]\n"
      "v_cvt_pk_bf16_f32 v218, v218, v218\n"
      "v_fmac_f32 v210, v223, v223\n"
      "v_mfma_f32_16x16x32_bf16 v[102:105], a[140:143], v[162:165], v[102:105]\n"
      "v_mul_f32 v193, v219, v193\n"
      "v_mul_f32 v155, v185, v155\n"
      "v_mfma_f32_16x16x32_bf16 v[186:189], v[206:209], a[144:147], 0\n"
      "v_bitop3_b32 v170, v170, v173, s33 bitop3:0xa8\n"
      "v_mul_f32_e64 v160, v219, v160\n"
      "v_mfma_f32_16x16x32_bf16 v[186:189], v[146:149], a[148:151], v[186:189]\n"
      "v_mul_f32 v191, v219, v191\n"
      "v_fmac_f32 v184, v183, v183\n"
      "v_mfma_f32_16x16x32_bf16 v[186:189], v[166:169], a[152:155], v[186:189]\n"
      "v_fmac_f32 v210, v98, v98\n"
      "v_mul_f32 v153, v219, v153\n"
      "v_mfma_f32_16x16x32_bf16 v[186:189], v[142:145], a[156:159], v[186:189]\n"
      "v_sub_u32 v78, v78, v141\n"
      "v_fmac_f32 v210, v99, v99\n"
      "v_mfma_f32_16x16x32_bf16 v[186:189], v[198:201], a[160:163], v[186:189]\n"
      "v_fmac_f32 v184, v108, v108\n"
      "v_fmac_f32 v210, v100, v100\n"
      "v_mfma_f32_16x16x32_bf16 v[186:189], v[162:165], a[164:167], v[186:189]\n"
      "v_cvt_pk_bf16_f32 v190, v190, v191\n"
      "v_fmac_f32 v184, v109, v109\n"
      "v_fmac_f32 v210, v101, v101\n"
      "v_mfma_f32_16x16x32_bf16 v[120:123], v[206:209], a[168:171], 0\n"
      "ds_write_b16 v12, v218\n"
      "v_mfma_f32_16x16x32_bf16 v[120:123], v[146:149], a[172:175], v[120:123]\n"
      "v_lshlrev_b32 v78, 4, v78\n"
      "v_mov_b32 v218, 0x3fb9\n"
      "v_mfma_f32_16x16x32_bf16 v[120:123], v[166:169], a[176:179], v[120:123]\n"
      "v_fmac_f32 v184, v110, v110\n"
      "v_cvt_pk_bf16_f32 v214, v214, v215\n"
      "v_mfma_f32_16x16x32_bf16 v[120:123], v[142:145], a[180:183], v[120:123]\n"
      "s_waitcnt vmcnt(7)\n"
      "v_fmac_f32 v210, v102, v102\n"
      "v_mul_f32 v192, v219, v192\n"
      "v_mfma_f32_16x16x32_bf16 v[120:123], v[198:201], a[184:187], v[120:123]\n"
      "v_mul_f32 v115, v185, v115\n"
      "v_cmp_ne_u32 s[56:57], 0, v170\n"
      "v_mfma_f32_16x16x32_bf16 v[120:123], v[162:165], a[188:191], v[120:123]\n"
      "v_cvt_pk_bf16_f32 v215, v216, v217\n"
      "v_mul_f32 v203, v185, v203\n"
      "v_mfma_f32_16x16x32_bf16 v[170:173], v[206:209], a[192:195], 0\n"
      "ds_read_b128 v[206:209], v0 offset:28672\n"
      "v_fmac_f32 v210, v103, v103\n"
      "v_mfma_f32_16x16x32_bf16 v[170:173], v[146:149], a[196:199], v[170:173]\n"
      "ds_read_b128 v[146:149], v1 offset:28672\n"
      "v_cvt_pk_bf16_f32 v191, v192, v193\n"
      "v_mfma_f32_16x16x32_bf16 v[170:173], v[166:169], a[200:203], v[170:173]\n"
      "ds_read_b128 v[166:169], v1 offset:34816\n"
      "v_fmac_f32 v184, v111, v111\n"
      "v_mfma_f32_16x16x32_bf16 v[170:173], v[142:145], a[204:207], v[170:173]\n"
      "ds_read_b128 v[142:145], v0 offset:34816\n"
      "v_fmac_f32 v210, v104, v104\n"
      "v_mfma_f32_16x16x32_bf16 v[170:173], v[198:201], a[208:211], v[170:173]\n"
      "v_fmac_f32 v210, v105, v105\n"
      "v_cvt_pk_bf16_f32 v133, v188, v189\n"
      "v_mfma_f32_16x16x32_bf16 v[170:173], v[162:165], a[212:215], v[170:173]\n"
      "s_waitcnt lgkmcnt(4)\n"
      "v_permlane16_swap_b32 v184, v210\n"
      "v_mfma_f32_16x16x32_bf16 v[138:141], a[0:3], v[134:137], 0\n"
      "v_add_f32 v184, v184, v210\n"
      "v_mov_b32 v210, v184\n"
      "v_mfma_f32_16x16x32_bf16 v[138:141], a[4:7], v[174:177], v[138:141]\n"
      "s_waitcnt lgkmcnt(2)\n"
      "v_cvt_pk_bf16_f32 v197, v122, v123\n"
      "v_mul_f32 v112, v185, v112\n"
      "v_cmp_gt_u32 s[72:73], 64, v78\n"
      "v_mfma_f32_16x16x32_bf16 v[138:141], a[8:11], v[206:209], v[138:141]\n"
      "v_mul_f32 v113, v185, v113\n"
      "v_readlane_b32 s47, v38, s43\n"
      "v_mfma_f32_16x16x32_bf16 v[138:141], a[12:15], v[146:149], v[138:141]\n"
      "s_waitcnt lgkmcnt(0)\n"
      "v_permlane32_swap_b32 v184, v210\n"
      "v_add_f32_e64 v184, v184, v210\n"
      "v_mfma_f32_16x16x32_bf16 v[138:141], a[16:19], v[142:145], v[138:141]\n"
      "v_mul_f32 v158, v219, v158\n"
      "v_mul_f32 v161, v219, v161\n"
      "v_mfma_f32_16x16x32_bf16 v[138:141], a[20:23], v[166:169], v[138:141]\n"
      "v_rsq_f32_e64 v184, v184\n"
      "v_mfma_f32_16x16x32_bf16 v[162:165], a[24:27], v[134:137], 0\n"
      "v_mov_b32 v210, v184\n"
      "v_mul_f32 v152, v219, v152\n"
      "v_mfma_f32_16x16x32_bf16 v[162:165], a[28:31], v[174:177], v[162:165]\n"
      "v_permlane16_swap_b32_e64 v184, v210\n"
      "v_mfma_f32_16x16x32_bf16 v[162:165], a[32:35], v[206:209], v[162:165]\n"
      "v_and_b32 v124, 2, v89\n"
      "v_add_u32 v39, 115296, v39\n"
      "v_mfma_f32_16x16x32_bf16 v[162:165], a[36:39], v[146:149], v[162:165]\n"
      "v_mul_f32 v202, v185, v202\n"
      "v_cvt_pk_bf16_f32 v196, v120, v121\n"
      "v_cvt_pk_bf16_f32 v158, v158, v159\n"
      "v_cvt_pk_bf16_f32 v159, v160, v161\n"
      "v_fma_f32 v192, v138, v138, s51\n"
      "v_pk_mul_f32 v[150:151], v[150:151], v[218:219] op_sel:[0,1]\n"
      "v_mul_f32 v184, s50, v184\n"
      "v_pk_mul_f32 v[204:205], v[204:205], v[184:185] op_sel:[0,1]\n"
      "v_pk_mul_f32 v[102:103], v[102:103], v[210:211] op_sel_hi:[1,0]\n"
      "v_mul_f32 v99, v210, v99\n"
      "v_pk_mul_f32 v[220:221], v[220:221], v[210:211] op_sel_hi:[1,0]\n"
      "v_pk_mul_f32 v[222:223], v[222:223], v[210:211] op_sel_hi:[1,0]\n"
      "v_pk_mul_f32 v[108:109], v[108:109], v[184:185] op_sel_hi:[1,0]\n"
      "v_pk_mul_f32 v[156:157], v[156:157], v[184:185] op_sel:[0,1]\n"
      "v_pk_mul_f32 v[126:127], v[126:127], v[184:185] op_sel_hi:[1,0]\n"
      "v_pk_mul_f32 v[128:129], v[128:129], v[184:185] op_sel_hi:[1,0]\n"
      "v_mul_f32_e64 v98, v210, v98\n"
      "v_pk_mul_f32 v[104:105], v[104:105], v[210:211] op_sel_hi:[1,0]\n"
      "v_mfma_f32_16x16x32_bf16 v[162:165], a[40:43], v[142:145], v[162:165]\n"
      "v_mov_b32 v219, 0\n"
      "v_mul_f32 v110, v184, v110\n"
      "v_mfma_f32_16x16x32_bf16 v[162:165], a[44:47], v[166:169], v[162:165]\n"
      "v_cvt_pk_bf16_f32 v217, v172, v173\n"
      "v_mul_f32 v114, v185, v114\n"
      "v_mfma_f32_16x16x32_bf16 v[120:123], a[48:51], v[134:137], 0\n"
      "v_fmac_f32 v192, v139, v139\n"
      "v_mul_f32 v180, v184, v180\n"
      "v_mfma_f32_16x16x32_bf16 v[120:123], a[52:55], v[174:177], v[120:123]\n"
      "v_fmac_f32 v192, v140, v140\n"
      "v_fmac_f32 v192, v141, v141\n"
      "v_mfma_f32_16x16x32_bf16 v[120:123], a[56:59], v[206:209], v[120:123]\n"
      "v_mul_f32 v111, v184, v111\n"
      "v_mul_f32 v154, v185, v154\n"
      "v_mfma_f32_16x16x32_bf16 v[120:123], a[60:63], v[146:149], v[120:123]\n"
      "s_cmp_gt_u32 s42, s44\n"
      "s_cselect_b32 s95, s94, s95\n"
      "v_cvt_pk_bf16_f32 v154, v154, v155\n"
      "v_mfma_f32_16x16x32_bf16 v[120:123], a[64:67], v[142:145], v[120:123]\n"
      "v_mul_f32 v101, v210, v101\n"
      "v_mul_f32_e64 v100, v210, v100\n"
      "v_mfma_f32_16x16x32_bf16 v[120:123], a[68:71], v[166:169], v[120:123]\n"
      "v_cvt_pk_bf16_f32 v155, v156, v157\n"
      "v_fmac_f32_e64 v192, v162, v162\n"
      "v_mfma_f32_16x16x32_bf16 v[210:213], a[72:75], v[134:137], 0\n"
      "v_cvt_pk_bf16_f32 v102, v102, v103\n"
      "v_cvt_pk_bf16_f32 v216, v170, v171\n"
      "v_mfma_f32_16x16x32_bf16 v[210:213], a[76:79], v[174:177], v[210:213]\n"
      "v_lshlrev_b64 v[218:219], v78, v[218:219]\n"
      "v_cvt_pk_bf16_f32 v156, v112, v113\n"
      "v_mfma_f32_16x16x32_bf16 v[210:213], a[80:83], v[206:209], v[210:213]\n"
      "v_cvt_pk_bf16_f32 v132, v186, v187\n"
      "v_mul_f32_e64 v183, v184, v183\n"
      "v_mfma_f32_16x16x32_bf16 v[210:213], a[84:87], v[146:149], v[210:213]\n"
      "s_cmp_ge_u32 s95, 0x40000000\n"
      "s_cselect_b64 vcc, -1, s[56:57]\n"
      "v_cvt_pk_bf16_f32 v220, v220, v221\n"
      "v_cvt_pk_bf16_f32 v103, v104, v105\n"
      "v_mfma_f32_16x16x32_bf16 v[210:213], a[88:91], v[142:145], v[210:213]\n"
      "v_fmac_f32 v192, v163, v163\n"
      "v_fmac_f32 v192, v164, v164\n"
      "v_mfma_f32_16x16x32_bf16 v[210:213], a[92:95], v[166:169], v[210:213]\n"
      "v_cvt_pk_bf16_f32 v221, v222, v223\n"
      "v_cvt_pk_bf16_f32 v160, v150, v151\n"
      "v_and_b32 v89, -4, v89\n"
      "v_mfma_f32_16x16x32_bf16 v[170:173], a[96:99], v[134:137], 0\n"
      "v_cndmask_b32_e64 v218, 0, v218, s[72:73]\n"
      "v_fmac_f32 v192, v165, v165\n"
      "v_mfma_f32_16x16x32_bf16 v[170:173], a[100:103], v[174:177], v[170:173]\n"
      "v_cvt_pk_bf16_f32 v202, v202, v203\n"
      "v_cvt_pk_bf16_f32 v126, v126, v127\n"
      "v_mfma_f32_16x16x32_bf16 v[170:173], a[104:107], v[206:209], v[170:173]\n"
      "ds_read_u16 v39, v39\n"
      "v_fma_f32 v105, v210, v210, s51\n"
      "v_mfma_f32_16x16x32_bf16 v[170:173], a[108:111], v[146:149], v[170:173]\n"
      "v_cvt_pk_bf16_f32 v108, v108, v109\n"
      "v_fmac_f32 v105, v211, v211\n"
      "v_mfma_f32_16x16x32_bf16 v[170:173], a[112:115], v[142:145], v[170:173]\n"
      "v_fmac_f32 v192, v120, v120\n"
      "v_cvt_pk_bf16_f32 v161, v152, v153\n"
      "v_cvt_pk_bf16_f32 v203, v204, v205\n"
      "v_mfma_f32_16x16x32_bf16 v[170:173], a[116:119], v[166:169], v[170:173]\n"
      "v_mul_f32 v181, v184, v181\n"
      "v_fmac_f32 v105, v212, v212\n"
      "v_mfma_f32_16x16x32_bf16 v[186:189], a[120:123], v[134:137], 0\n"
      "v_mul_f32 v182, v184, v182\n"
      "v_cvt_pk_bf16_f32 v109, v110, v111\n"
      "v_mfma_f32_16x16x32_bf16 v[186:189], a[124:127], v[174:177], v[186:189]\n"
      "ds_read_b128 v[198:201], v7\n"
      "v_fmac_f32 v192, v121, v121\n"
      "v_mfma_f32_16x16x32_bf16 v[186:189], a[128:131], v[206:209], v[186:189]\n"
      "v_fmac_f32 v105, v213, v213\n"
      "v_cvt_pk_bf16_f32 v127, v128, v129\n"
      "v_mfma_f32_16x16x32_bf16 v[186:189], a[132:135], v[146:149], v[186:189]\n"
      "v_cvt_pk_bf16_f32 v128, v180, v181\n"
      "v_fmac_f32 v192, v122, v122\n"
      "v_mfma_f32_16x16x32_bf16 v[186:189], a[136:139], v[142:145], v[186:189]\n"
      "v_cvt_pk_bf16_f32 v157, v114, v115\n"
      "v_fmac_f32 v105, v170, v170\n"
      "v_mfma_f32_16x16x32_bf16 v[186:189], a[140:143], v[166:169], v[186:189]\n"
      "v_fmac_f32 v105, v171, v171\n"
      "v_fmac_f32 v105, v172, v172\n"
      "v_mfma_f32_16x16x32_bf16 v[110:113], v[134:137], a[144:147], 0\n"
      "v_fmac_f32 v105, v173, v173\n"
      "v_cvt_pk_bf16_f32 v222, v98, v99\n"
      "v_mfma_f32_16x16x32_bf16 v[110:113], v[174:177], a[148:151], v[110:113]\n"
      "v_fmac_f32 v192, v123, v123\n"
      "v_cvt_pk_bf16_f32 v223, v100, v101\n"
      "v_mfma_f32_16x16x32_bf16 v[110:113], v[206:209], a[152:155], v[110:113]\n"
      "v_fmac_f32 v105, v186, v186\n"
      "v_fmac_f32_e64 v105, v187, v187\n"
      "v_mfma_f32_16x16x32_bf16 v[110:113], v[146:149], a[156:159], v[110:113]\n"
      "v_fmac_f32 v105, v188, v188\n"
      "v_fmac_f32 v105, v189, v189\n"
      "v_mfma_f32_16x16x32_bf16 v[110:113], v[142:145], a[160:163], v[110:113]\n"
      "s_nop 0\n"
      "v_permlane16_swap_b32 v192, v105\n"
      "v_mfma_f32_16x16x32_bf16 v[110:113], v[166:169], a[164:167], v[110:113]\n"
      "v_add_f32 v192, v192, v105\n"
      "v_mov_b32 v105, v192\n"
      "v_mfma_f32_16x16x32_bf16 v[178:181], v[134:137], a[168:171], 0\n"
      "s_nop 0\n"
      "v_permlane32_swap_b32 v192, v105\n"
      "v_mfma_f32_16x16x32_bf16 v[178:181], v[174:177], a[172:175], v[178:181]\n"
      "v_add_f32 v192, v192, v105\n"
      "v_cvt_pk_bf16_f32 v129, v182, v183\n"
      "v_mfma_f32_16x16x32_bf16 v[178:181], v[206:209], a[176:179], v[178:181]\n"
      "v_rsq_f32 v192, v192\n"
      "v_mfma_f32_16x16x32_bf16 v[178:181], v[146:149], a[180:183], v[178:181]\n"
      "v_mov_b32 v105, v192\n"
      "v_cvt_pk_bf16_f32 v77, v112, v113\n"
      "v_mfma_f32_16x16x32_bf16 v[178:181], v[142:145], a[184:187], v[178:181]\n"
      "s_waitcnt lgkmcnt(0)\n"
      "v_permlane16_swap_b32_e64 v192, v105\n"
      "v_mfma_f32_16x16x32_bf16 v[178:181], v[166:169], a[188:191], v[178:181]\n"
      "ds_read_b128 v[182:185], v2 offset:3072\n"
      "v_cndmask_b32_e64 v219, 0, v219, s[72:73]\n"
      "v_mfma_f32_16x16x32_bf16 v[98:101], v[134:137], a[192:195], 0\n"
      "ds_read_b128 v[134:137], v7 offset:4096\n"
      "v_mfma_f32_16x16x32_bf16 v[98:101], v[174:177], a[196:199], v[98:101]\n"
      "ds_read_b128 v[174:177], v2 offset:6144\n"
      "v_mul_f32_e64 v192, s50, v192\n"
      "v_mfma_f32_16x16x32_bf16 v[98:101], v[206:209], a[200:203], v[98:101]\n"
      "ds_read_b128 v[150:153], v2 offset:9216\n"
      "v_cvt_pk_bf16_f32 v81, v180, v181\n"
      "v_mfma_f32_16x16x32_bf16 v[98:101], v[146:149], a[204:207], v[98:101]\n"
      "ds_read_b128 v[146:149], v2\n"
      "v_mul_f32_e64 v211, v105, v211\n"
      "v_mfma_f32_16x16x32_bf16 v[98:101], v[142:145], a[208:211], v[98:101]\n"
      "buffer_store_dwordx4 v[198:201], v20, s[24:27], s85 offen sc1\n"
      "v_pk_mul_f32 v[172:173], v[172:173], v[104:105] op_sel:[0,1]\n"
      "v_pk_mul_f32 v[120:121], v[120:121], v[192:193] op_sel_hi:[1,0]\n"
      "v_pk_mul_f32 v[170:171], v[170:171], v[104:105] op_sel:[0,1]\n"
      "v_pk_mul_f32 v[186:187], v[186:187], v[104:105] op_sel:[0,1]\n"
      "v_pk_mul_f32 v[212:213], v[212:213], v[104:105] op_sel:[0,1]\n"
      "v_mul_f32_e64 v210, v105, v210\n"
      "v_pk_mul_f32 v[164:165], v[164:165], v[192:193] op_sel_hi:[1,0]\n"
      "v_pk_mul_f32 v[162:163], v[162:163], v[192:193] op_sel_hi:[1,0]\n"
      "v_pk_mul_f32 v[138:139], v[138:139], v[192:193] op_sel_hi:[1,0]\n"
      "v_pk_mul_f32 v[122:123], v[122:123], v[192:193] op_sel_hi:[1,0]\n"
      "v_pk_mul_f32 v[140:141], v[140:141], v[192:193] op_sel_hi:[1,0]\n"
      "v_pk_mul_f32 v[188:189], v[188:189], v[104:105] op_sel:[0,1]\n"
      "v_mfma_f32_16x16x32_bf16 v[98:101], v[166:169], a[212:215], v[98:101]\n"
      "s_waitcnt lgkmcnt(0)\n"
      "v_cvt_pk_bf16_f32 v76, v110, v111\n"
      "v_add_u32 v110, 110592, v89\n"
      "v_cvt_pk_bf16_f32 v80, v178, v179\n"
      "v_mfma_f32_16x16x32_bf16 v[166:169], a[216:219], v[146:149], 0\n"
      "v_cvt_pk_bf16_f32 v186, v186, v187\n"
      "v_cvt_pk_bf16_f32 v210, v210, v211\n"
      "v_mfma_f32_16x16x32_bf16 v[166:169], a[220:223], v[182:185], v[166:169]\n"
      "ds_read_b128 v[178:181], v2 offset:12288\n"
      "v_mfma_f32_16x16x32_bf16 v[166:169], a[224:227], v[174:177], v[166:169]\n"
      "v_cvt_pk_bf16_f32 v84, v98, v99\n"
      "v_cvt_pk_bf16_f32 v85, v100, v101\n"
      "v_mfma_f32_16x16x32_bf16 v[198:201], a[240:243], v[146:149], 0\n"
      "buffer_store_dwordx4 v[134:137], v21, s[24:27], s85 offen sc1\n"
      "v_mfma_f32_16x16x32_bf16 v[198:201], a[244:247], v[182:185], v[198:201]\n"
      "s_nop 0\n"
      "ds_read_b128 v[134:137], v2 offset:15360\n"
      "v_cvt_pk_bf16_f32 v187, v188, v189\n"
      "v_mfma_f32_16x16x32_bf16 v[198:201], a[248:251], v[174:177], v[198:201]\n"
      "ds_read2_b32 v[208:209], v110 offset0:16 offset1:17\n"
      "v_cmp_gt_u32 s[72:73], 64, v78\n"
      "v_mfma_f32_16x16x32_bf16 v[142:145], v[232:235], v[146:149], 0\n"
      "v_cvt_pk_bf16_f32 v120, v120, v121\n"
      "v_cvt_pk_bf16_f32 v138, v138, v139\n"
      "v_mfma_f32_16x16x32_bf16 v[142:145], v[236:239], v[182:185], v[142:145]\n"
      "ds_read_b128 v[182:185], v7 offset:8192\n"
      "v_add_u32 v114, 112160, v89\n"
      "v_mfma_f32_16x16x32_bf16 v[142:145], v[240:243], v[174:177], v[142:145]\n"
      "ds_read2_b32 v[206:207], v110 offset0:0 offset1:1\n"
      "v_cvt_pk_bf16_f32 v139, v140, v141\n"
      "v_mfma_f32_16x16x32_bf16 v[166:169], a[228:231], v[150:153], v[166:169]\n"
      "s_waitcnt lgkmcnt(4)\n"
      "v_mov_b32_e64 v105, 0\n"
      "v_cvt_pk_bf16_f32 v211, v212, v213\n"
      "v_mfma_f32_16x16x32_bf16 v[166:169], a[232:235], v[178:181], v[166:169]\n"
      "s_waitcnt lgkmcnt(3)\n"
      "v_cvt_pk_bf16_f32 v213, v172, v173\n"
      "v_cvt_pk_bf16_f32 v212, v170, v171\n"
      "v_mfma_f32_16x16x32_bf16 v[166:169], a[236:239], v[134:137], v[166:169]\n"
      "ds_read_b128 v[174:177], v10 offset:1200\n"
      "v_cvt_pk_bf16_f32 v141, v164, v165\n"
      "v_cvt_pk_bf16_f32 v140, v162, v163\n"
      "v_mfma_f32_16x16x32_bf16 v[198:201], a[252:255], v[150:153], v[198:201]\n"
      "ds_read2_b32 v[188:189], v110 offset0:2 offset1:10\n"
      "v_mov_b32 v104, 0xbb2b\n"
      "v_mfma_f32_16x16x32_bf16 v[198:201], v[224:227], v[178:181], v[198:201]\n"
      "ds_read_b64 v[192:193], v10 offset:1192\n"
      "v_cvt_pk_bf16_f32 v121, v122, v123\n"
      "v_mul_f32 v97, v167, v167\n"
      "v_mfma_f32_16x16x32_bf16 v[198:201], v[228:231], v[134:137], v[198:201]\n"
      "s_waitcnt lgkmcnt(3)\n"
      "buffer_store_dwordx4 v[182:185], v22, s[24:27], s85 offen sc1\n"
      "v_mfma_f32_16x16x32_bf16 v[142:145], v[244:247], v[150:153], v[142:145]\n"
      "ds_read_b128 v[162:165], v7 offset:12288\n"
      "v_fma_f32 v96, v166, v166, s52\n"
      "v_mfma_f32_16x16x32_bf16 v[142:145], v[248:251], v[178:181], v[142:145]\n"
      "ds_read2_b32 v[170:171], v110 offset0:24 offset1:18\n"
      "v_fmac_f32 v97, v169, v169\n"
      "v_mfma_f32_16x16x32_bf16 v[142:145], v[252:255], v[134:137], v[142:145]\n"
      "s_waitcnt lgkmcnt(3)\n"
      "v_fmac_f32_e64 v96, v168, v168\n"
      "v_alignbyte_b32 v206, v207, v206, v124\n"
      "v_mfma_f32_16x16x32_bf16 v[134:137], v[154:157], v[158:161], v[40:43]\n"
      "ds_read2_b32 v[172:173], v110 offset0:8 offset1:9\n"
      "v_lshlrev_b64 v[104:105], v78, v[104:105]\n"
      "v_mfma_f32_16x16x32_bf16 v[110:113], v[220:223], v[158:161], v[44:47]\n"
      "ds_read2_b32 v[122:123], v114 offset0:8 offset1:9\n"
      "v_alignbyte_b32 v208, v209, v208, v124\n"
      "v_mfma_f32_16x16x32_bf16 v[98:101], v[210:213], v[158:161], v[48:51]\n"
      "ds_read_b128 v[180:183], v7 offset:16384\n"
      "v_fmac_f32 v97, v199, v199\n"
      "v_mfma_f32_16x16x32_bf16 v[150:153], v[174:177], v[158:161], v[24:27]\n"
      "v_alignbyte_b32 v207, v188, v207, v124\n"
      "v_fmac_f32 v96, v198, v198\n"
      "v_mfma_f32_16x16x16_bf16 v[134:137], v[202:203], v[190:191], v[134:137]\n"
      "s_waitcnt lgkmcnt(3)\n"
      "v_alignbyte_b32 v209, v171, v209, v124\n"
      "v_mfma_f32_16x16x16_bf16 v[110:113], v[102:103], v[190:191], v[110:113]\n"
      "buffer_store_dwordx4 v[162:165], v23, s[24:27], s85 offen sc1\n"
      "v_mfma_f32_16x16x16_bf16 v[98:101], v[186:187], v[190:191], v[98:101]\n"
      "v_alignbyte_b32 v170, v170, v170, v124\n"
      "v_mfma_f32_16x16x16_bf16 v[150:153], v[192:193], v[190:191], v[150:153]\n"
      "s_waitcnt lgkmcnt(0)\n"
      "v_alignbyte_b32 v172, v173, v172, v124\n"
      "v_cndmask_b32_e64 v104, 0, v104, s[72:73]\n"
      "v_alignbyte_b32 v173, v189, v173, v124\n"
      "v_mfma_f32_16x16x16_bf16 v[134:137], v[218:219], v[206:207], v[134:137]\n"
      "ds_read2_b32 v[158:159], v114 offset0:0 offset1:1\n"
      "v_cndmask_b32_e64 v105, 0, v105, s[72:73]\n"
      "v_mfma_f32_16x16x16_bf16 v[110:113], v[218:219], v[172:173], v[110:113]\n"
      "v_fmac_f32_e64 v96, v200, v200\n"
      "v_alignbyte_b32 v122, v123, v122, v124\n"
      "v_fmac_f32_e64 v97, v201, v201\n"
      "v_mfma_f32_16x16x16_bf16 v[98:101], v[218:219], v[208:209], v[98:101]\n"
      "ds_read_b128 v[188:191], v2 offset:256\n"
      "v_fmac_f32_e64 v97, v143, v143\n"
      "v_mfma_f32_16x16x16_bf16 v[150:153], v[218:219], v[170:171], v[150:153]\n"
      "ds_read_b128 v[146:149], v2 offset:6400\n"
      "v_mfma_f32_16x16x16_bf16 v[134:137], v[104:105], v[206:207], v[134:137]\n"
      "ds_read_b128 v[204:207], v2 offset:3328\n"
      "v_fmac_f32 v96, v142, v142\n"
      "v_mfma_f32_16x16x16_bf16 v[110:113], v[104:105], v[172:173], v[110:113]\n"
      "buffer_store_dwordx4 v[180:183], v25, s[24:27], s85 offen sc1\n"
      "v_pk_fma_f32 v[96:97], v[144:145], v[144:145], v[96:97]\n"
      "v_mfma_f32_16x16x16_bf16 v[98:101], v[104:105], v[208:209], v[98:101]\n"
      "ds_read2_b32 v[172:173], v114 offset0:2 offset1:10\n"
      "v_add_u32 v115, 113728, v89\n"
      "v_mfma_f32_16x16x16_bf16 v[150:153], v[104:105], v[170:171], v[150:153]\n"
      "s_waitcnt lgkmcnt(3)\n"
      "v_alignbyte_b32 v158, v159, v158, v124\n"
      "v_max3_f32 v184, v134, v135, v136\n"
      "v_mfma_f32_16x16x32_bf16 v[162:165], a[216:219], v[188:191], 0\n"
      "ds_read2_b32 v[160:161], v114 offset0:16 offset1:17\n"
      "v_add_f32 v96, v96, v97\n"
      "s_waitcnt lgkmcnt(2)\n"
      "v_mfma_f32_16x16x32_bf16 v[162:165], a[220:223], v[204:207], v[162:165]\n"
      "ds_read2_b32 v[152:153], v114 offset0:24 offset1:18\n"
      "v_max3_f32 v182, v137, v110, v111\n"
      "v_max3_f32 v184, v184, v112, v113\n"
      "s_waitcnt vmcnt(6)\n"
      "v_or3_b32 v116, v116, v117, v118\n"
      "v_mfma_f32_16x16x32_bf16 v[162:165], a[224:227], v[146:149], v[162:165]\n"
      "v_max3_f32 v182, v182, v98, v99\n"
      "v_max3_f32 v184, v184, v100, v101\n"
      "v_max3_f32 v184, v184, v182, v150\n"
      "v_mfma_f32_16x16x32_bf16 v[180:183], a[240:243], v[188:191], 0\n"
      "s_waitcnt lgkmcnt(2)\n"
      "v_alignbyte_b32 v123, v173, v123, v124\n"
      "v_alignbyte_b32 v159, v172, v159, v124\n"
      "v_mfma_f32_16x16x32_bf16 v[180:183], a[244:247], v[204:207], v[180:183]\n"
      "ds_read_u16 v78, v11 offset:1168\n"
      "v_mov_b32_e64 v209, v184\n"
      "v_mfma_f32_16x16x32_bf16 v[180:183], a[248:251], v[146:149], v[180:183]\n"
      "s_waitcnt lgkmcnt(2)\n"
      "v_alignbyte_b32 v160, v161, v160, v124\n"
      "s_waitcnt vmcnt(5)\n"
      "v_or3_b32 v116, v116, v119, v82\n"
      "v_mfma_f32_16x16x32_bf16 v[170:173], v[232:235], v[188:191], 0\n"
      "ds_write_b32 v31, v116 offset:5632\n"
      "v_mfma_f32_16x16x32_bf16 v[170:173], v[236:239], v[204:207], v[170:173]\n"
      "s_waitcnt lgkmcnt(2)\n"
      "v_alignbyte_b32 v161, v153, v161, v124\n"
      "v_alignbyte_b32 v152, v152, v152, v124\n"
      "v_mfma_f32_16x16x32_bf16 v[170:173], v[240:243], v[146:149], v[170:173]\n"
      "v_permlane16_swap_b32 v184, v209\n"
      "v_mfma_f32_16x16x32_bf16 v[188:191], v[154:157], v[126:129], v[52:55]\n"
      "v_max_f32 v184, v184, v209\n"
      "v_mov_b32 v209, v184\n"
      "v_mfma_f32_16x16x32_bf16 v[116:119], v[220:223], v[126:129], v[56:59]\n"
      "s_nop 0\n"
      "v_permlane32_swap_b32 v184, v209\n"
      "v_max_f32 v184, v184, v209\n"
      "v_mfma_f32_16x16x32_bf16 v[206:209], v[210:213], v[126:129], v[60:63]\n"
      "v_sub_f32 v101, v101, v184\n"
      "v_sub_f32 v113, v113, v184\n"
      "v_mfma_f32_16x16x32_bf16 v[146:149], v[174:177], v[126:129], v[26:29]\n"
      "v_exp_f32 v113, v113\n"
      "v_mfma_f32_16x16x16_bf16 v[188:191], v[202:203], v[108:109], v[188:191]\n"
      "v_exp_f32 v101, v101\n"
      "v_sub_f32 v134, v134, v184\n"
      "v_mfma_f32_16x16x16_bf16 v[116:119], v[102:103], v[108:109], v[116:119]\n"
      "v_sub_f32 v112, v112, v184\n"
      "v_sub_f32 v99, v99, v184\n"
      "v_sub_f32 v100, v100, v184\n"
      "v_mfma_f32_16x16x16_bf16 v[206:209], v[186:187], v[108:109], v[206:209]\n"
      "v_sub_f32 v135, v135, v184\n"
      "v_sub_f32 v137, v137, v184\n"
      "v_sub_f32 v136, v136, v184\n"
      "v_mfma_f32_16x16x16_bf16 v[146:149], v[192:193], v[108:109], v[146:149]\n"
      "v_exp_f32 v100, v100\n"
      "v_exp_f32 v112, v112\n"
      "v_mfma_f32_16x16x16_bf16 v[188:191], v[218:219], v[158:159], v[188:191]\n"
      "v_exp_f32 v135, v135\n"
      "v_sub_f32 v98, v98, v184\n"
      "v_mfma_f32_16x16x16_bf16 v[116:119], v[218:219], v[122:123], v[116:119]\n"
      "v_cvt_pk_bf16_f32 v89, v100, v101\n"
      "v_exp_f32 v134, v134\n"
      "v_mfma_f32_16x16x16_bf16 v[206:209], v[218:219], v[160:161], v[206:209]\n"
      "v_exp_f32 v137, v137\n"
      "v_sub_f32_e64 v110, v110, v184\n"
      "v_mfma_f32_16x16x16_bf16 v[146:149], v[218:219], v[152:153], v[146:149]\n"
      "v_cvt_pk_bf16_f32 v134, v134, v135\n"
      "v_exp_f32_e64 v136, v136\n"
      "v_mfma_f32_16x16x16_bf16 v[188:191], v[104:105], v[158:159], v[188:191]\n"
      "ds_read_b128 v[126:129], v2 offset:9472\n"
      "v_exp_f32 v99, v99\n"
      "v_mfma_f32_16x16x16_bf16 v[116:119], v[104:105], v[122:123], v[116:119]\n"
      "v_sub_f32 v111, v111, v184\n"
      "v_exp_f32 v111, v111\n"
      "v_sub_f32 v150, v150, v184\n"
      "v_mfma_f32_16x16x16_bf16 v[206:209], v[104:105], v[160:161], v[206:209]\n"
      "ds_read_b128 v[106:109], v2 offset:12544\n"
      "ds_read_b128 v[158:161], v2 offset:15616\n"
      "v_mfma_f32_16x16x16_bf16 v[146:149], v[104:105], v[152:153], v[146:149]\n"
      "v_exp_f32 v98, v98\n"
      "v_exp_f32 v110, v110\n"
      "v_cvt_pk_bf16_f32 v135, v136, v137\n"
      "v_cvt_pk_bf16_f32 v136, v110, v111\n"
      "v_max3_f32 v111, v188, v189, v190\n"
      "v_cvt_pk_bf16_f32 v88, v98, v99\n"
      "v_max3_f32 v111, v111, v118, v119\n"
      "v_exp_f32_e64 v150, v150\n"
      "v_cvt_pk_bf16_f32 v137, v112, v113\n"
      "v_max3_f32 v149, v191, v116, v117\n"
      "v_max3_f32 v111, v111, v208, v209\n"
      "v_max3_f32 v149, v149, v206, v207\n"
      "v_max3_f32 v111, v111, v149, v146\n"
      "v_mov_b32 v149, v111\n"
      "s_waitcnt lgkmcnt(2)\n"
      "v_mfma_f32_16x16x32_bf16 v[162:165], a[228:231], v[126:129], v[162:165]\n"
      "s_waitcnt lgkmcnt(1)\n"
      "v_permlane16_swap_b32 v111, v149\n"
      "v_mfma_f32_16x16x32_bf16 v[162:165], a[232:235], v[106:109], v[162:165]\n"
      "s_waitcnt lgkmcnt(0)\n"
      "v_max_f32 v111, v111, v149\n"
      "v_mov_b32 v149, v111\n"
      "v_mfma_f32_16x16x32_bf16 v[162:165], a[236:239], v[158:161], v[162:165]\n"
      "s_nop 0\n"
      "v_permlane32_swap_b32_e64 v111, v149\n"
      "v_mfma_f32_16x16x32_bf16 v[180:183], a[252:255], v[126:129], v[180:183]\n"
      "v_max_f32 v111, v111, v149\n"
      "v_sub_f32 v188, v188, v111\n"
      "v_mfma_f32_16x16x32_bf16 v[180:183], v[224:227], v[106:109], v[180:183]\n"
      "ds_read2_b32 v[148:149], v115 offset0:8 offset1:9\n"
      "v_sub_f32 v119, v119, v111\n"
      "v_mfma_f32_16x16x32_bf16 v[180:183], v[228:231], v[158:161], v[180:183]\n"
      "ds_read_u16 v82, v11 offset:1200\n"
      "v_sub_f32 v190, v190, v111\n"
      "v_mfma_f32_16x16x32_bf16 v[170:173], v[244:247], v[126:129], v[170:173]\n"
      "ds_read2_b32 v[204:205], v115 offset0:16 offset1:17\n"
      "v_sub_f32 v189, v189, v111\n"
      "v_mfma_f32_16x16x32_bf16 v[170:173], v[248:251], v[106:109], v[170:173]\n"
      "ds_read2_b32 v[152:153], v115 offset0:2 offset1:10\n"
      "v_sub_f32 v191, v191, v111\n"
      "v_sub_f32 v208, v208, v111\n"
      "v_mfma_f32_16x16x32_bf16 v[170:173], v[252:255], v[158:161], v[170:173]\n"
      "ds_read_u16 v86, v11 offset:1232\n"
      "v_sub_f32 v146, v146, v111\n"
      "v_sub_f32 v117, v117, v111\n"
      "v_sub_f32 v206, v206, v111\n"
      "v_exp_f32 v191, v191\n"
      "v_sub_f32 v207, v207, v111\n"
      "v_cvt_pk_bf16_f32 v90, v150, 0\n"
      "v_mfma_f32_16x16x32_bf16 v[126:129], v[32:35], v[134:137], 0\n"
      "ds_read2_b32 v[106:107], v115 offset0:0 offset1:1\n"
      "v_exp_f32 v189, v189\n"
      "v_mfma_f32_16x16x32_bf16 v[126:129], v[32:35], v[88:91], v[126:129]\n"
      "ds_read2_b32 v[184:185], v115 offset0:24 offset1:18\n"
      "v_sub_f32 v116, v116, v111\n"
      "v_exp_f32 v188, v188\n"
      "v_exp_f32 v119, v119\n"
      "v_exp_f32 v190, v190\n"
      "v_sub_f32 v209, v209, v111\n"
      "v_sub_f32 v118, v118, v111\n"
      "v_mfma_f32_16x16x32_bf16 v[108:111], v[130:133], v[134:137], 0\n"
      "s_waitcnt lgkmcnt(2)\n"
      "v_alignbyte_b32 v204, v205, v204, v124\n"
      "v_alignbyte_b32 v148, v149, v148, v124\n"
      "v_mfma_f32_16x16x32_bf16 v[108:111], v[76:79], v[88:91], v[108:111]\n"
      "v_exp_f32_e64 v117, v117\n"
      "v_mfma_f32_16x16x32_bf16 v[98:101], v[194:197], v[134:137], 0\n"
      "s_waitcnt lgkmcnt(1)\n"
      "v_alignbyte_b32 v149, v153, v149, v124\n"
      "v_alignbyte_b32 v106, v107, v106, v124\n"
      "v_mfma_f32_16x16x32_bf16 v[98:101], v[80:83], v[88:91], v[98:101]\n"
      "s_waitcnt lgkmcnt(0)\n"
      "v_alignbyte_b32 v107, v152, v107, v124\n"
      "v_exp_f32_e64 v118, v118\n"
      "v_mfma_f32_16x16x32_bf16 v[150:153], v[214:217], v[134:137], 0\n"
      "v_alignbyte_b32 v205, v185, v205, v124\n"
      "v_cvt_pk_bf16_f32 v188, v188, v189\n"
      "v_mfma_f32_16x16x32_bf16 v[150:153], v[84:87], v[88:91], v[150:153]\n"
      "v_cvt_pk_bf16_f32 v189, v190, v191\n"
      "v_alignbyte_b32 v184, v184, v184, v124\n"
      "v_mfma_f32_16x16x32_bf16 v[134:137], v[154:157], v[138:141], v[64:67]\n"
      "v_exp_f32_e64 v116, v116\n"
      "v_mfma_f32_16x16x32_bf16 v[158:161], v[220:223], v[138:141], v[68:71]\n"
      "v_cvt_pk_bf16_f32 v190, v116, v117\n"
      "v_cvt_pk_bf16_f32 v191, v118, v119\n"
      "v_mfma_f32_16x16x32_bf16 v[112:115], v[210:213], v[138:141], v[72:75]\n"
      "v_rcp_f32 v126, v126\n"
      "v_mfma_f32_16x16x32_bf16 v[122:125], v[174:177], v[138:141], v[28:31]\n"
      "v_exp_f32 v207, v207\n"
      "v_mfma_f32_16x16x16_bf16 v[134:137], v[202:203], v[120:121], v[134:137]\n"
      "v_exp_f32 v208, v208\n"
      "v_mul_f32 v151, v126, v151\n"
      "v_mfma_f32_16x16x16_bf16 v[158:161], v[102:103], v[120:121], v[158:161]\n"
      "v_mul_f32 v98, v126, v98\n"
      "v_exp_f32 v209, v209\n"
      "v_mfma_f32_16x16x16_bf16 v[112:115], v[186:187], v[120:121], v[112:115]\n"
      "v_exp_f32 v206, v206\n"
      "v_mul_f32 v111, v126, v111\n"
      "v_mfma_f32_16x16x16_bf16 v[122:125], v[192:193], v[120:121], v[122:125]\n"
      "v_mul_f32 v99, v126, v99\n"
      "v_mul_f32 v108, v126, v108\n"
      "v_cvt_pk_bf16_f32 v92, v206, v207\n"
      "v_mfma_f32_16x16x16_bf16 v[134:137], v[218:219], v[106:107], v[134:137]\n"
      "v_mul_f32 v109, v126, v109\n"
      "v_exp_f32 v146, v146\n"
      "v_mfma_f32_16x16x16_bf16 v[158:161], v[218:219], v[148:149], v[158:161]\n"
      "v_mul_f32 v150, v126, v150\n"
      "v_fma_f32 v120, v162, v162, s52\n"
      "v_cvt_pk_bf16_f32 v93, v208, v209\n"
      "v_mfma_f32_16x16x16_bf16 v[112:115], v[218:219], v[204:205], v[112:115]\n"
      "v_mul_f32 v121, v163, v163\n"
      "v_mul_f32 v110, v126, v110\n"
      "v_cvt_pk_bf16_f32 v108, v108, v109\n"
      "v_mfma_f32_16x16x16_bf16 v[122:125], v[218:219], v[184:185], v[122:125]\n"
      "v_fmac_f32 v120, v164, v164\n"
      "v_cvt_pk_bf16_f32 v109, v110, v111\n"
      "v_mul_f32 v152, v126, v152\n"
      "v_mfma_f32_16x16x16_bf16 v[134:137], v[104:105], v[106:107], v[134:137]\n"
      "ds_read_b128 v[138:141], v2 offset:512\n"
      "v_mul_f32 v153, v126, v153\n"
      "v_mfma_f32_16x16x16_bf16 v[158:161], v[104:105], v[148:149], v[158:161]\n"
      "v_cvt_pk_bf16_f32 v150, v150, v151\n"
      "v_cvt_pk_bf16_f32 v94, v146, 0\n"
      "v_cvt_pk_bf16_f32 v110, v98, v99\n"
      "v_mfma_f32_16x16x16_bf16 v[112:115], v[104:105], v[204:205], v[112:115]\n"
      "ds_read_b128 v[206:209], v2 offset:3584\n"
      "v_cvt_pk_bf16_f32 v151, v152, v153\n"
      "v_mfma_f32_16x16x16_bf16 v[122:125], v[104:105], v[184:185], v[122:125]\n"
      "ds_read_b128 v[146:149], v2 offset:6656\n"
      "v_fmac_f32_e64 v121, v165, v165\n"
      "v_max3_f32 v90, v134, v135, v136\n"
      "v_max3_f32 v90, v90, v160, v161\n"
      "v_max3_f32 v128, v137, v158, v159\n"
      "v_max3_f32 v90, v90, v114, v115\n"
      "v_pk_mul_f32 v[100:101], v[100:101], v[126:127] op_sel_hi:[1,0]\n"
      "v_cvt_pk_bf16_f32 v111, v100, v101\n"
      "v_pk_fma_f32 v[120:121], v[180:181], v[180:181], v[120:121]\n"
      "v_max3_f32 v128, v128, v112, v113\n"
      "v_pk_fma_f32 v[120:121], v[182:183], v[182:183], v[120:121]\n"
      "v_max3_f32 v90, v90, v128, v122\n"
      "v_pk_fma_f32 v[120:121], v[170:171], v[170:171], v[120:121]\n"
      "v_mov_b32 v119, v90\n"
      "v_pk_fma_f32 v[120:121], v[172:173], v[172:173], v[120:121]\n"
      "v_add_f32 v120, v120, v121\n"
      "v_permlane16_swap_b32 v90, v119\n"
      "v_max_f32 v90, v90, v119\n"
      "v_mov_b32 v119, v90\n"
      "s_waitcnt lgkmcnt(1)\n"
      "v_mfma_f32_16x16x32_bf16 v[126:129], a[216:219], v[138:141], 0\n"
      "v_permlane32_swap_b32 v90, v119\n"
      "v_mfma_f32_16x16x32_bf16 v[126:129], a[220:223], v[206:209], v[126:129]\n"
      "s_waitcnt lgkmcnt(0)\n"
      "v_max_f32 v90, v90, v119\n"
      "v_sub_f32 v160, v160, v90\n"
      "v_mfma_f32_16x16x32_bf16 v[126:129], a[224:227], v[146:149], v[126:129]\n"
      "ds_write_b128 v3, v[108:111] offset:18432\n"
      "v_mfma_f32_16x16x32_bf16 v[116:119], a[240:243], v[138:141], 0\n"
      "v_sub_f32 v136, v136, v90\n"
      "v_sub_f32 v134, v134, v90\n"
      "v_mfma_f32_16x16x32_bf16 v[116:119], a[244:247], v[206:209], v[116:119]\n"
      "v_exp_f32 v136, v136\n"
      "v_mfma_f32_16x16x32_bf16 v[116:119], a[248:251], v[146:149], v[116:119]\n"
      "v_sub_f32 v158, v158, v90\n"
      "v_sub_f32 v135, v135, v90\n"
      "v_mfma_f32_16x16x32_bf16 v[108:111], v[232:235], v[138:141], 0\n"
      "v_sub_f32 v137, v137, v90\n"
      "v_exp_f32 v158, v158\n"
      "v_mfma_f32_16x16x32_bf16 v[108:111], v[236:239], v[206:209], v[108:111]\n"
      "v_exp_f32 v137, v137\n"
      "v_mfma_f32_16x16x32_bf16 v[108:111], v[240:243], v[146:149], v[108:111]\n"
      "v_exp_f32 v134, v134\n"
      "v_mfma_f32_16x16x32_bf16 v[98:101], v[32:35], v[188:191], 0\n"
      "v_exp_f32 v135, v135\n"
      "v_mfma_f32_16x16x32_bf16 v[98:101], v[32:35], v[92:95], v[98:101]\n"
      "ds_read_b128 v[146:149], v10 offset:1168\n"
      "v_sub_f32_e64 v159, v159, v90\n"
      "v_mfma_f32_16x16x32_bf16 v[206:209], v[130:133], v[188:191], 0\n"
      "v_cvt_pk_bf16_f32 v134, v134, v135\n"
      "v_cvt_pk_bf16_f32 v135, v136, v137\n"
      "v_mfma_f32_16x16x32_bf16 v[206:209], v[76:79], v[92:95], v[206:209]\n"
      "ds_read_b64 v[88:89], v10 offset:1184\n"
      "v_sub_f32 v115, v115, v90\n"
      "v_mfma_f32_16x16x32_bf16 v[138:141], v[194:197], v[188:191], 0\n"
      "v_exp_f32 v159, v159\n"
      "v_mfma_f32_16x16x32_bf16 v[138:141], v[80:83], v[92:95], v[138:141]\n"
      "v_sub_f32 v161, v161, v90\n"
      "v_cvt_pk_bf16_f32 v136, v158, v159\n"
      "v_mfma_f32_16x16x32_bf16 v[104:107], v[214:217], v[188:191], 0\n"
      "v_rcp_f32 v98, v98\n"
      "v_mfma_f32_16x16x32_bf16 v[104:107], v[84:87], v[92:95], v[104:107]\n"
      "s_waitcnt lgkmcnt(1)\n"
      "v_exp_f32 v161, v161\n"
      "v_mfma_f32_16x16x32_bf16 v[188:191], v[146:149], v[154:157], v[30:33]\n"
      "v_exp_f32_e64 v160, v160\n"
      "v_mfma_f32_16x16x32_bf16 v[156:159], v[146:149], v[220:223], v[30:33]\n"
      "s_waitcnt lgkmcnt(0)\n"
      "v_cvt_pk_bf16_f32 v137, v160, v161\n"
      "v_sub_f32 v113, v113, v90\n"
      "v_mfma_f32_16x16x32_bf16 v[218:221], v[146:149], v[210:213], v[30:33]\n"
      "ds_read_b128 v[210:213], v2 offset:9728\n"
      "v_sub_f32 v112, v112, v90\n"
      "v_mfma_f32_16x16x32_bf16 v[152:155], v[146:149], v[174:177], v[30:33]\n"
      "ds_read_b128 v[174:177], v2 offset:12800\n"
      "v_sub_f32 v114, v114, v90\n"
      "v_mfma_f32_16x16x16_bf16 v[188:191], v[88:89], v[202:203], v[188:191]\n"
      "ds_read_b128 v[202:205], v2 offset:15872\n"
      "v_sub_f32 v122, v122, v90\n"
      "v_mfma_f32_16x16x16_bf16 v[156:159], v[88:89], v[102:103], v[156:159]\n"
      "v_exp_f32 v113, v113\n"
      "v_exp_f32 v115, v115\n"
      "v_mfma_f32_16x16x16_bf16 v[218:221], v[88:89], v[186:187], v[218:221]\n"
      "v_exp_f32 v114, v114\n"
      "v_exp_f32_e64 v112, v112\n"
      "v_exp_f32_e64 v122, v122\n"
      "v_pk_mul_f32 v[104:105], v[104:105], v[98:99] op_sel_hi:[1,0]\n"
      "v_pk_mul_f32 v[138:139], v[138:139], v[98:99] op_sel_hi:[1,0]\n"
      "v_pk_mul_f32 v[140:141], v[140:141], v[98:99] op_sel_hi:[1,0]\n"
      "v_pk_mul_f32 v[208:209], v[208:209], v[98:99] op_sel_hi:[1,0]\n"
      "v_pk_mul_f32 v[206:207], v[206:207], v[98:99] op_sel_hi:[1,0]\n"
      "v_mfma_f32_16x16x16_bf16 v[152:155], v[88:89], v[192:193], v[152:155]\n"
      "v_cndmask_b32 v188, v188, v156, s[58:59]\n"
      "v_cndmask_b32 v188, v188, v218, s[60:61]\n"
      "s_waitcnt lgkmcnt(1)\n"
      "v_mfma_f32_16x16x32_bf16 v[126:129], a[228:231], v[210:213], v[126:129]\n"
      "s_waitcnt lgkmcnt(0)\n"
      "v_cvt_pk_bf16_f32 v88, v112, v113\n"
      "v_lshlrev_b32_e64 v39, 16, v39\n"
      "v_mfma_f32_16x16x32_bf16 v[126:129], a[232:235], v[174:177], v[126:129]\n"
      "v_cndmask_b32 v188, v188, v152, s[62:63]\n"
      "v_cvt_pk_bf16_f32 v89, v114, v115\n"
      "v_mfma_f32_16x16x32_bf16 v[126:129], a[236:239], v[202:205], v[126:129]\n"
      "v_fmac_f32_e64 v188, s53, v39\n"
      "v_cvt_pk_bf16_f32 v206, v206, v207\n"
      "v_mfma_f32_16x16x32_bf16 v[116:119], a[252:255], v[210:213], v[116:119]\n"
      "ds_write_b64 v4, v[150:151] offset:18432\n"
      "v_mfma_f32_16x16x32_bf16 v[116:119], v[224:227], v[174:177], v[116:119]\n"
      "v_max_f32_dpp v102, v188, v188 row_ror:8 row_mask:0xf bank_mask:0xf\n"
      "v_cvt_pk_bf16_f32 v207, v208, v209\n"
      "v_mfma_f32_16x16x32_bf16 v[116:119], v[228:231], v[202:205], v[116:119]\n"
      "v_cvt_pk_bf16_f32 v90, v122, 0\n"
      "v_max_f32_dpp v102, v102, v102 row_ror:4 row_mask:0xf bank_mask:0xf\n"
      "v_mfma_f32_16x16x32_bf16 v[108:111], v[244:247], v[210:213], v[108:111]\n"
      "v_mul_f32_e64 v191, v127, v127\n"
      "v_max_f32_dpp v102, v102, v102 row_ror:2 row_mask:0xf bank_mask:0xf\n"
      "v_mfma_f32_16x16x32_bf16 v[108:111], v[248:251], v[174:177], v[108:111]\n"
      "v_cvt_pk_bf16_f32 v209, v140, v141\n"
      "v_max_f32_dpp v102, v102, v102 row_ror:1 row_mask:0xf bank_mask:0xf\n"
      "v_mfma_f32_16x16x32_bf16 v[108:111], v[252:255], v[202:205], v[108:111]\n"
      "v_fma_f32 v190, v126, v126, s52\n"
      "v_max_f32_dpp v102, v102, v102 row_bcast:15 row_mask:0xa bank_mask:0xf\n"
      "v_mfma_f32_16x16x32_bf16 v[218:221], v[32:35], v[134:137], 0\n"
      "s_nop 0\n"
      "v_max_f32_dpp v102, v102, v102 row_bcast:31 row_mask:0xc bank_mask:0xf\n"
      "v_cvt_pk_bf16_f32 v208, v138, v139\n"
      "v_mfma_f32_16x16x32_bf16 v[218:221], v[32:35], v[88:91], v[218:221]\n"
      "v_cvt_pk_bf16_f32 v104, v104, v105\n"
      "v_readlane_b32 s72, v102, 63\n"
      "v_mfma_f32_16x16x32_bf16 v[174:177], v[130:133], v[134:137], 0\n"
      "ds_write_b128 v3, v[206:209] offset:18688\n"
      "v_subrev_f32 v188, s72, v188\n"
      "v_pk_fma_f32 v[190:191], v[128:129], v[128:129], v[190:191]\n"
      "v_mfma_f32_16x16x32_bf16 v[174:177], v[76:79], v[88:91], v[174:177]\n"
      "s_nop 0\n"
      "v_rcp_f32 v218, v218\n"
      "v_mfma_f32_16x16x32_bf16 v[210:213], v[194:197], v[134:137], 0\n"
      "v_exp_f32 v188, v188\n"
      "v_mfma_f32_16x16x32_bf16 v[210:213], v[80:83], v[88:91], v[210:213]\n"
      "v_permlane16_swap_b32 v96, v120\n"
      "v_add_f32_e64 v96, v96, v120\n"
      "v_add_u32 v10, 2336, v10\n"
      "v_cvt_pk_bf16_f32 v188, v188, v188\n"
      "v_pk_mul_f32 v[174:175], v[174:175], v[218:219] op_sel_hi:[1,0]\n"
      "v_pk_mul_f32 v[176:177], v[176:177], v[218:219] op_sel_hi:[1,0]\n"
      "v_pk_fma_f32 v[190:191], v[116:117], v[116:117], v[190:191]\n"
      "v_pk_fma_f32 v[190:191], v[118:119], v[118:119], v[190:191]\n"
      "v_pk_mul_f32 v[210:211], v[210:211], v[218:219] op_sel_hi:[1,0]\n"
      "v_pk_fma_f32 v[190:191], v[108:109], v[108:109], v[190:191]\n"
      "v_pk_fma_f32 v[190:191], v[110:111], v[110:111], v[190:191]\n"
      "v_cvt_pk_bf16_f32 v174, v174, v175\n"
      "v_cvt_pk_bf16_f32 v175, v176, v177\n"
      "v_pk_mul_f32 v[212:213], v[212:213], v[218:219] op_sel_hi:[1,0]\n"
      "v_cvt_pk_bf16_f32 v177, v212, v213\n"
      "v_mfma_f32_16x16x32_bf16 v[100:103], v[214:217], v[134:137], 0\n"
      "ds_write_b16 v13, v188\n"
      "v_cvt_pk_bf16_f32 v176, v210, v211\n"
      "v_mfma_f32_16x16x32_bf16 v[100:103], v[84:87], v[88:91], v[100:103]\n"
      "ds_read_b128 v[210:213], v14 offset:64\n"
      "ds_write_b128 v3, v[174:177] offset:18944\n"
      "v_add_f32 v190, v190, v191\n"
      "v_mov_b32 v120, v190\n"
      "v_pk_mul_f32 v[106:107], v[106:107], v[98:99] op_sel_hi:[1,0]\n"
      "v_cvt_pk_bf16_f32 v105, v106, v107\n"
      "v_permlane16_swap_b32 v190, v120\n"
      "v_add_f32_e64 v190, v190, v120\n"
      "v_pk_mul_f32 v[102:103], v[102:103], v[218:219] op_sel_hi:[1,0]\n"
      "s_and_b32 s47, s47, -4\n"
      "ds_write_b64 v4, v[104:105] offset:18688\n"
      "v_permlane32_swap_b32 v96, v190\n"
      "v_add_f32_e64 v96, v96, v190\n"
      "s_add_u32 s42, s42, 1\n"
      "ds_read_b128 v[174:177], v14\n"
      "ds_write_b32 v31, v96 offset:1024\n"
      "v_pk_mul_f32 v[100:101], v[100:101], v[218:219] op_sel_hi:[1,0]\n"
      "v_cvt_pk_bf16_f32 v100, v100, v101\n"
      "v_cvt_pk_bf16_f32 v101, v102, v103\n"
      "s_add_u32 s43, s43, 1\n"
      "ds_write_b64 v4, v[100:101] offset:18944\n"
      "s_waitcnt lgkmcnt(2)\n"
      "v_mfma_f32_16x16x32_bf16 v[188:191], v[174:177], v[32:35], 0\n"
      "v_mfma_f32_16x16x32_bf16 v[188:191], v[210:213], v[32:35], v[188:191]\n"
      "v_add_u32 v11, 2336, v11\n"
      "s_waitcnt lgkmcnt(0)\n"
      "s_barrier\n"
      "s_cbranch_vccz L_sel0_%=\n"
      "L_disp0_%=:\n"
      "s_cmp_lt_u32 s95, 0x40000000\n"
      "s_cbranch_scc1 L_nofix_18_%=\n"
      "s_cmp_eq_u32 s95, -2\n"
      "s_cbranch_scc1 L_tail0_%=\n"
      "s_nop 2\n"
      "v_lshlrev_b32 v218, 3, v36\n"
      "s_add_u32 s72, s98, L_cold_%=-L_ipf0_%=\n"
      "s_addc_u32 s73, s99, 0\n"
      "s_mov_b32 s74, L_cend_%=-L_cold_%=\n"
      "s_mov_b32 s75, 0x20000\n"
      "s_mov_b32 m0, 161792\n"
      "s_nop 0\n"
      "buffer_load_dwordx4 v218, s[72:75], 0 offen  lds\n"
      "s_lshr_b32 s92, s45, 30\n"
      "s_or_b32 s92, s92, s66\n"
      "s_cbranch_scc0 L_nf_19_%=\n"
      "s_bitcmp1_b32 s45, 31\n"
      "s_cselect_b32 s75, 100270464, 0\n"
      "s_bitcmp1_b32 s45, 30\n"
      "s_cselect_b32 s76, 196224, 0\n"
      "s_and_b32 s74, s45, 0x3fffffff\n"
      "s_cmp_ge_u32 s45, 0x40000000\n"
      "s_cselect_b32 s74, s74, 0\n"
      "s_cselect_b32 s45, 0, s45\n"
      "s_sub_u32 s74, s74, s66\n"
      "s_add_u32 s66, s66, s74\n"
      "s_sub_u32 s75, s75, s67\n"
      "s_add_u32 s67, s67, s75\n"
      "s_sub_u32 s76, s76, s68\n"
      "s_add_u32 s68, s68, s76\n"
      "v_add_u32 v16, s74, v16\n"
      "v_add_u32 v17, s74, v17\n"
      "v_add_u32 v18, s74, v18\n"
      "v_add_u32 v19, s74, v19\n"
      "v_add_u32 v15, s74, v15\n"
      "s_cmp_eq_u32 s75, 0\n"
      "s_cbranch_scc1 L_ax_20_%=\n"
      "v_bfe_i32 v218, v27, 0, 1\n"
      "v_and_b32 v218, s75, v218\n"
      "v_sub_u32 v16, v16, v218\n"
      "v_bfe_i32 v218, v27, 2, 1\n"
      "v_and_b32 v218, s75, v218\n"
      "v_sub_u32 v17, v17, v218\n"
      "v_bfe_i32 v218, v27, 4, 1\n"
      "v_and_b32 v218, s75, v218\n"
      "v_sub_u32 v18, v18, v218\n"
      "v_bfe_i32 v218, v27, 6, 1\n"
      "v_and_b32 v218, s75, v218\n"
      "v_sub_u32 v19, v19, v218\n"
      "v_bfe_i32 v218, v27, 8, 1\n"
      "v_and_b32 v218, s75, v218\n"
      "v_sub_u32 v15, v15, v218\n"
      "L_ax_20_%=:\n"
      "s_cmp_eq_u32 s76, 0\n"
      "s_cbranch_scc1 L_ax_21_%=\n"
      "v_bfe_i32 v218, v27, 1, 1\n"
      "v_and_b32 v218, s76, v218\n"
      "v_sub_u32 v16, v16, v218\n"
      "v_bfe_i32 v218, v27, 3, 1\n"
      "v_and_b32 v218, s76, v218\n"
      "v_sub_u32 v17, v17, v218\n"
      "v_bfe_i32 v218, v27, 5, 1\n"
      "v_and_b32 v218, s76, v218\n"
      "v_sub_u32 v18, v18, v218\n"
      "v_bfe_i32 v218, v27, 7, 1\n"
      "v_and_b32 v218, s76, v218\n"
      "v_sub_u32 v19, v19, v218\n"
      "v_bfe_i32 v218, v27, 9, 1\n"
      "v_and_b32 v218, s76, v218\n"
      "v_sub_u32 v15, v15, v218\n"
      "L_ax_21_%=:\n"
      "s_or_b32 s48, s66, s69\n"
      "s_cmp_lg_u32 s48, 0\n"
      "s_cselect_b32 s48, 0x40000000, 0\n"
      "L_nf_19_%=:\n"
      "s_lshr_b32 s92, s46, 30\n"
      "s_or_b32 s92, s92, s69\n"
      "s_cbranch_scc0 L_nf_22_%=\n"
      "s_bitcmp1_b32 s46, 31\n"
      "s_cselect_b32 s75, 100270464, 0\n"
      "s_bitcmp1_b32 s46, 30\n"
      "s_cselect_b32 s76, 196224, 0\n"
      "s_and_b32 s74, s46, 0x3fffffff\n"
      "s_cmp_ge_u32 s46, 0x40000000\n"
      "s_cselect_b32 s74, s74, 0\n"
      "s_cselect_b32 s46, 0, s46\n"
      "s_sub_u32 s74, s74, s69\n"
      "s_add_u32 s69, s69, s74\n"
      "s_sub_u32 s75, s75, s70\n"
      "s_add_u32 s70, s70, s75\n"
      "s_sub_u32 s76, s76, s71\n"
      "s_add_u32 s71, s71, s76\n"
      "v_add_u32 v20, s74, v20\n"
      "v_add_u32 v21, s74, v21\n"
      "v_add_u32 v22, s74, v22\n"
      "v_add_u32 v23, s74, v23\n"
      "v_add_u32 v25, s74, v25\n"
      "s_cmp_eq_u32 s75, 0\n"
      "s_cbranch_scc1 L_ax_23_%=\n"
      "v_bfe_i32 v218, v27, 10, 1\n"
      "v_and_b32 v218, s75, v218\n"
      "v_sub_u32 v20, v20, v218\n"
      "v_bfe_i32 v218, v27, 12, 1\n"
      "v_and_b32 v218, s75, v218\n"
      "v_sub_u32 v21, v21, v218\n"
      "v_bfe_i32 v218, v27, 14, 1\n"
      "v_and_b32 v218, s75, v218\n"
      "v_sub_u32 v22, v22, v218\n"
      "v_bfe_i32 v218, v27, 16, 1\n"
      "v_and_b32 v218, s75, v218\n"
      "v_sub_u32 v23, v23, v218\n"
      "v_bfe_i32 v218, v27, 18, 1\n"
      "v_and_b32 v218, s75, v218\n"
      "v_sub_u32 v25, v25, v218\n"
      "L_ax_23_%=:\n"
      "s_cmp_eq_u32 s76, 0\n"
      "s_cbranch_scc1 L_ax_24_%=\n"
      "v_bfe_i32 v218, v27, 11, 1\n"
      "v_and_b32 v218, s76, v218\n"
      "v_sub_u32 v20, v20, v218\n"
      "v_bfe_i32 v218, v27, 13, 1\n"
      "v_and_b32 v218, s76, v218\n"
      "v_sub_u32 v21, v21, v218\n"
      "v_bfe_i32 v218, v27, 15, 1\n"
      "v_and_b32 v218, s76, v218\n"
      "v_sub_u32 v22, v22, v218\n"
      "v_bfe_i32 v218, v27, 17, 1\n"
      "v_and_b32 v218, s76, v218\n"
      "v_sub_u32 v23, v23, v218\n"
      "v_bfe_i32 v218, v27, 19, 1\n"
      "v_and_b32_e64 v218, s76, v218\n"
      "v_sub_u32_e64 v25, v25, v218\n"
      "L_ax_24_%=:\n"
      "s_or_b32 s48, s66, s69\n"
      "s_cmp_lg_u32 s48, 0\n"
      "s_cselect_b32 s48, 0x40000000, 0\n"
      "L_nf_22_%=:\n"
      "s_sub_u32 s92, s42, 1\n"
      "s_cmp_gt_u32 s92, s44\n"
      "s_cbranch_scc1 L_tail0_%=\n"
      "L_nofix_18_%=:\n"
      "s_cmp_lg_u64 s[56:57], 0\n"
      "s_cbranch_scc1 L_slow0_%=\n"
      "L_sel0_%=:\n"
      "s_cmp_lg_u64 s[54:55], 0\n"
      "s_cbranch_scc1 L_max0_%=\n"
      "s_branch L_loop0_%=\n"
      "L_slow0_%=:\n"
      "s_nop 4\n"
      "v_mfma_f32_16x16x32_bf16 v[218:221], v[174:177], v[130:133], 0\n"
      "ds_read_b128 v[156:159], v0\n"
      "ds_read_b128 v[112:115], v9 offset:1024\n"
      "v_mfma_f32_16x16x32_bf16 v[218:221], v[210:213], v[76:79], v[218:221]\n"
      "ds_read_b128 v[104:107], v0 offset:6144\n"
      "v_readlane_b32 s85, v37, s42\n"
      "v_mfma_f32_16x16x32_bf16 v[134:137], v[174:177], v[194:197], 0\n"
      "ds_read_b128 v[206:209], v1\n"
      "v_mov_b32 v124, 0x3fb9\n"
      "v_mfma_f32_16x16x32_bf16 v[134:137], v[210:213], v[80:83], v[134:137]\n"
      "ds_read_b128 v[138:141], v1 offset:2048\n"
      "v_subrev_u32 v132, 115456, v9\n"
      "v_mfma_f32_16x16x32_bf16 v[100:103], v[174:177], v[214:217], 0\n"
      "s_add_u32 s72, s42, 1\n"
      "v_readlane_b32 s84, v37, s43\n"
      "v_readlane_b32 s77, v38, s72\n"
      "s_mov_b32 m0, 110592\n"
      "v_mfma_f32_16x16x32_bf16 v[100:103], v[210:213], v[84:87], v[100:103]\n"
      "s_and_b32 s73, s77, -16\n"
      "v_mov_b32_dpp v188, v188 quad_perm:[0,0,0,0] row_mask:0xf bank_mask:0xf\n"
      "s_waitcnt lgkmcnt(4)\n"
      "v_mfma_f32_16x16x32_bf16 v[210:213], a[0:3], v[156:159], 0\n"
      "buffer_load_dwordx4 v132, s[28:31], s73 offen  lds\n"
      "v_mfma_f32_16x16x32_bf16 v[190:193], a[24:27], v[156:159], 0\n"
      "s_waitcnt lgkmcnt(3)\n"
      "v_rcp_f32 v188, v188\n"
      "v_add_f32 v114, v114, v115\n"
      "v_mfma_f32_16x16x32_bf16 v[174:177], a[48:51], v[156:159], 0\n"
      "v_add_f32 v112, v112, v113\n"
      "v_add_f32 v112, v112, v114\n"
      "v_mfma_f32_16x16x32_bf16 v[150:153], a[72:75], v[156:159], 0\n"
      "s_waitcnt lgkmcnt(0)\n"
      "v_rsq_f32 v112, v112\n"
      "v_mfma_f32_16x16x32_bf16 v[96:99], a[96:99], v[156:159], 0\n"
      "s_add_u32 s73, s73, 1024\n"
      "v_readlane_b32 s86, v38, s43\n"
      "v_mul_f32 v112, 0x415db3d7, v112\n"
      "v_mfma_f32_16x16x32_bf16 v[146:149], a[120:123], v[156:159], 0\n"
      "ds_read_b128 v[202:205], v1 offset:6144\n"
      "v_mov_b32_e64 v114, v112\n"
      "v_mfma_f32_16x16x32_bf16 v[210:213], a[4:7], v[206:209], v[210:213]\n"
      "s_mov_b32 m0, 111616\n"
      "v_permlane32_swap_b32_e64 v112, v114\n"
      "v_mfma_f32_16x16x32_bf16 v[210:213], a[8:11], v[104:107], v[210:213]\n"
      "buffer_load_dwordx4 v132, s[28:31], s73 offen  lds\n"
      "v_mfma_f32_16x16x32_bf16 v[190:193], a[28:31], v[206:209], v[190:193]\n"
      "ds_read_b128 v[120:123], v1 offset:12288\n"
      "v_mov_b32_e64 v113, v112\n"
      "v_mfma_f32_16x16x32_bf16 v[190:193], a[32:35], v[104:107], v[190:193]\n"
      "ds_read_b128 v[184:187], v0 offset:12288\n"
      "v_mul_f32_e64 v119, v114, v119\n"
      "v_mfma_f32_16x16x32_bf16 v[174:177], a[52:55], v[206:209], v[174:177]\n"
      "s_mov_b32 m0, 112640\n"
      "v_permlane16_swap_b32 v112, v113\n"
      "v_mfma_f32_16x16x32_bf16 v[174:177], a[56:59], v[104:107], v[174:177]\n"
      "s_add_u32 s73, s73, 1024\n"
      "v_mul_f32 v163, v113, v163\n"
      "v_mul_f32 v198, v112, v198\n"
      "v_mfma_f32_16x16x32_bf16 v[150:153], a[76:79], v[206:209], v[150:153]\n"
      "buffer_load_dwordx4 v132, s[28:31], s73 offen  lds\n"
      "v_mfma_f32_16x16x32_bf16 v[150:153], a[80:83], v[104:107], v[150:153]\n"
      "s_and_b32 s77, s77, 15\n"
      "v_mul_f32 v169, v112, v169\n"
      "v_mul_f32 v170, v113, v170\n"
      "v_mfma_f32_16x16x32_bf16 v[96:99], a[100:103], v[206:209], v[96:99]\n"
      "v_mul_f32 v166, v112, v166\n"
      "v_mul_f32 v201, v112, v201\n"
      "v_mfma_f32_16x16x32_bf16 v[96:99], a[104:107], v[104:107], v[96:99]\n"
      "s_waitcnt lgkmcnt(0)\n"
      "v_mul_f32 v128, v114, v128\n"
      "v_mul_f32 v167, v112, v167\n"
      "v_mfma_f32_16x16x32_bf16 v[146:149], a[124:127], v[206:209], v[146:149]\n"
      "s_mov_b32 m0, 113664\n"
      "v_mul_f32 v168, v112, v168\n"
      "v_cvt_pk_bf16_f32 v166, v166, v167\n"
      "v_mfma_f32_16x16x32_bf16 v[146:149], a[128:131], v[104:107], v[146:149]\n"
      "s_add_u32 s73, s73, 1024\n"
      "v_cvt_pk_bf16_f32 v167, v168, v169\n"
      "v_mul_f32 v199, v112, v199\n"
      "v_mfma_f32_16x16x32_bf16 v[210:213], a[12:15], v[202:205], v[210:213]\n"
      "buffer_load_dwordx4 v132, s[28:31], s73 offen  lds\n"
      "v_mfma_f32_16x16x32_bf16 v[210:213], a[16:19], v[184:187], v[210:213]\n"
      "v_mul_f32 v180, v113, v180\n"
      "v_mul_f32_e64 v162, v113, v162\n"
      "v_mfma_f32_16x16x32_bf16 v[210:213], a[20:23], v[120:123], v[210:213]\n"
      "s_mov_b32 m0, 114688\n"
      "v_mul_f32_e64 v200, v112, v200\n"
      "v_cvt_pk_bf16_f32 v168, v198, v199\n"
      "v_mfma_f32_16x16x32_bf16 v[190:193], a[36:39], v[202:205], v[190:193]\n"
      "s_add_u32 s73, s73, 1024\n"
      "v_cvt_pk_bf16_f32 v169, v200, v201\n"
      "v_mul_f32 v181, v113, v181\n"
      "v_mfma_f32_16x16x32_bf16 v[190:193], a[40:43], v[184:187], v[190:193]\n"
      "v_cndmask_b32 v218, v218, v134, s[58:59]\n"
      "v_cvt_pk_bf16_f32 v162, v162, v163\n"
      "v_mfma_f32_16x16x32_bf16 v[190:193], a[44:47], v[120:123], v[190:193]\n"
      "s_mov_b32 exec_hi, 0xffff\n"
      "buffer_load_dwordx4 v132, s[28:31], s73 offen  lds\n"
      "s_mov_b32 exec_hi, -1\n"
      "v_pk_mul_f32 v[126:127], v[126:127], v[114:115] op_sel_hi:[1,0]\n"
      "v_pk_mul_f32 v[110:111], v[110:111], v[114:115] op_sel_hi:[1,0]\n"
      "v_pk_mul_f32 v[164:165], v[164:165], v[112:113] op_sel:[0,1]\n"
      "v_mfma_f32_16x16x32_bf16 v[174:177], a[60:63], v[202:205], v[174:177]\n"
      "ds_write_b128 v5, v[166:169]\n"
      "v_pk_mul_f32 v[142:143], v[142:143], v[112:113] op_sel_hi:[1,0]\n"
      "v_pk_mul_f32 v[144:145], v[144:145], v[112:113] op_sel_hi:[1,0]\n"
      "v_mfma_f32_16x16x32_bf16 v[174:177], a[64:67], v[184:187], v[174:177]\n"
      "v_cvt_pk_bf16_f32 v142, v142, v143\n"
      "v_mul_f32_e64 v182, v113, v182\n"
      "v_mfma_f32_16x16x32_bf16 v[174:177], a[68:71], v[120:123], v[174:177]\n"
      "s_add_u32 m0, s64, 18432\n"
      "v_cvt_pk_bf16_f32 v143, v144, v145\n"
      "v_cvt_pk_bf16_f32 v163, v164, v165\n"
      "v_mfma_f32_16x16x32_bf16 v[150:153], a[84:87], v[202:205], v[150:153]\n"
      "ds_write_b64 v6, v[142:143]\n"
      "v_mfma_f32_16x16x32_bf16 v[150:153], a[88:91], v[184:187], v[150:153]\n"
      "v_fma_f32 v77, v210, v210, s51\n"
      "v_mul_f32_e64 v172, v113, v172\n"
      "v_mfma_f32_16x16x32_bf16 v[150:153], a[92:95], v[120:123], v[150:153]\n"
      "buffer_load_dwordx4 v16, s[16:19], s45 offen  lds\n"
      "v_mfma_f32_16x16x32_bf16 v[96:99], a[108:111], v[202:205], v[96:99]\n"
      "s_or_b32 s95, s84, s85\n"
      "v_cvt_pk_bf16_f32 v164, v180, v181\n"
      "v_mul_f32 v117, v114, v117\n"
      "v_mfma_f32_16x16x32_bf16 v[96:99], a[112:115], v[184:187], v[96:99]\n"
      "s_add_u32 m0, s64, 22528\n"
      "v_mul_f32 v173, v113, v173\n"
      "v_fma_f32 v81, v150, v150, s51\n"
      "v_mfma_f32_16x16x32_bf16 v[96:99], a[116:119], v[120:123], v[96:99]\n"
      "v_mul_f32 v183, v113, v183\n"
      "v_mul_f32_e64 v171, v113, v171\n"
      "v_mfma_f32_16x16x32_bf16 v[146:149], a[132:135], v[202:205], v[146:149]\n"
      "ds_read_b128 v[166:169], v0 offset:14336\n"
      "v_cvt_pk_bf16_f32 v165, v182, v183\n"
      "v_mfma_f32_16x16x32_bf16 v[146:149], a[136:139], v[184:187], v[146:149]\n"
      "buffer_load_dwordx4 v17, s[16:19], s45 offen  lds\n"
      "v_mfma_f32_16x16x32_bf16 v[146:149], a[140:143], v[120:123], v[146:149]\n"
      "ds_read_b128 v[220:223], v0 offset:10240\n"
      "v_mul_f32 v129, v114, v129\n"
      "v_mfma_f32_16x16x32_bf16 v[130:133], v[156:159], a[144:147], 0\n"
      "s_add_u32 m0, s64, 26624\n"
      "v_cvt_pk_bf16_f32 v170, v170, v171\n"
      "v_mul_f32 v116, v114, v116\n"
      "v_mfma_f32_16x16x32_bf16 v[130:133], v[206:209], a[148:151], v[130:133]\n"
      "ds_write_b128 v5, v[162:165] offset:6144\n"
      "v_mfma_f32_16x16x32_bf16 v[130:133], v[104:107], a[152:155], v[130:133]\n"
      "buffer_load_dwordx4 v18, s[16:19], s45 offen  lds\n"
      "v_fmac_f32_e64 v81, v151, v151\n"
      "v_mfma_f32_16x16x32_bf16 v[130:133], v[202:205], a[156:159], v[130:133]\n"
      "ds_read_b128 v[198:201], v0 offset:2048\n"
      "v_cvt_pk_bf16_f32 v171, v172, v173\n"
      "v_mfma_f32_16x16x32_bf16 v[130:133], v[184:187], a[160:163], v[130:133]\n"
      "s_add_u32 m0, s64, 30720\n"
      "v_mul_f32 v109, v114, v109\n"
      "v_mov_b32 v125, 0\n"
      "v_fmac_f32_e64 v77, v211, v211\n"
      "v_mfma_f32_16x16x32_bf16 v[130:133], v[120:123], a[164:167], v[130:133]\n"
      "ds_write_b64 v6, v[170:171] offset:6144\n"
      "v_mfma_f32_16x16x32_bf16 v[194:197], v[156:159], a[168:171], 0\n"
      "buffer_load_dwordx4 v19, s[16:19], s45 offen  lds\n"
      "v_mfma_f32_16x16x32_bf16 v[194:197], v[206:209], a[172:175], v[194:197]\n"
      "v_cvt_pk_bf16_f32 v126, v126, v127\n"
      "v_mul_f32_e64 v118, v114, v118\n"
      "v_mfma_f32_16x16x32_bf16 v[194:197], v[104:107], a[176:179], v[194:197]\n"
      "s_add_u32 m0, s65, 34816\n"
      "v_fmac_f32 v77, v212, v212\n"
      "v_mul_f32 v108, v114, v108\n"
      "v_mfma_f32_16x16x32_bf16 v[194:197], v[202:205], a[180:183], v[194:197]\n"
      "v_fmac_f32 v77, v213, v213\n"
      "v_fmac_f32 v81, v152, v152\n"
      "v_mfma_f32_16x16x32_bf16 v[194:197], v[184:187], a[184:187], v[194:197]\n"
      "ds_read_b128 v[134:137], v0 offset:8192\n"
      "v_cvt_pk_bf16_f32 v127, v128, v129\n"
      "v_mfma_f32_16x16x32_bf16 v[194:197], v[120:123], a[188:191], v[194:197]\n"
      "buffer_load_dwordx4 v15, s[16:19], s45 offen  lds\n"
      "v_mfma_f32_16x16x32_bf16 v[214:217], v[156:159], a[192:195], 0\n"
      "s_or_b32 s95, s95, s48\n"
      "v_cvt_pk_bf16_f32 v129, v118, v119\n"
      "v_cvt_pk_bf16_f32 v128, v116, v117\n"
      "v_mfma_f32_16x16x32_bf16 v[214:217], v[206:209], a[196:199], v[214:217]\n"
      "s_waitcnt lgkmcnt(1)\n"
      "v_fmac_f32 v81, v153, v153\n"
      "v_fmac_f32 v77, v190, v190\n"
      "v_mfma_f32_16x16x32_bf16 v[214:217], v[104:107], a[200:203], v[214:217]\n"
      "ds_read_b128 v[170:173], v1 offset:8192\n"
      "v_fmac_f32_e64 v77, v191, v191\n"
      "v_mfma_f32_16x16x32_bf16 v[214:217], v[202:205], a[204:207], v[214:217]\n"
      "ds_read_b128 v[116:119], v1 offset:14336\n"
      "v_cvt_pk_bf16_f32 v194, v194, v195\n"
      "v_mfma_f32_16x16x32_bf16 v[214:217], v[184:187], a[208:211], v[214:217]\n"
      "buffer_load_dwordx4 v[206:209], v36, s[28:31], s47 offen\n"
      "v_mfma_f32_16x16x32_bf16 v[214:217], v[120:123], a[212:215], v[214:217]\n"
      "ds_write_b128 v5, v[126:129] offset:12288\n"
      "v_mfma_f32_16x16x32_bf16 v[104:107], a[0:3], v[198:201], 0\n"
      "s_add_u32 s73, s73, 1024\n"
      "v_fmac_f32 v77, v192, v192\n"
      "v_fmac_f32 v81, v96, v96\n"
      "v_mfma_f32_16x16x32_bf16 v[104:107], a[4:7], v[138:141], v[104:107]\n"
      "s_waitcnt lgkmcnt(2)\n"
      "v_fmac_f32 v77, v193, v193\n"
      "v_fmac_f32_e64 v77, v174, v174\n"
      "v_mfma_f32_16x16x32_bf16 v[104:107], a[8:11], v[134:137], v[104:107]\n"
      "buffer_load_dword v78, v29, s[28:31], s47 offen\n"
      "v_mfma_f32_16x16x32_bf16 v[104:107], a[12:15], v[170:173], v[104:107]\n"
      "v_cvt_pk_bf16_f32 v108, v108, v109\n"
      "v_cvt_pk_bf16_f32 v130, v130, v131\n"
      "v_mfma_f32_16x16x32_bf16 v[104:107], a[16:19], v[166:169], v[104:107]\n"
      "s_waitcnt lgkmcnt(1)\n"
      "v_fmac_f32 v77, v175, v175\n"
      "v_cndmask_b32 v218, v218, v100, s[60:61]\n"
      "v_mfma_f32_16x16x32_bf16 v[104:107], a[20:23], v[116:119], v[104:107]\n"
      "v_cvt_pk_bf16_f32 v195, v196, v197\n"
      "v_cvt_pk_bf16_f32 v131, v132, v133\n"
      "v_mfma_f32_16x16x32_bf16 v[158:161], a[24:27], v[198:201], 0\n"
      "v_fmac_f32_e64 v77, v176, v176\n"
      "v_subrev_u32 v185, 115456, v9\n"
      "v_mfma_f32_16x16x32_bf16 v[158:161], a[28:31], v[138:141], v[158:161]\n"
      "v_cvt_pk_bf16_f32 v109, v110, v111\n"
      "v_fmac_f32_e64 v81, v97, v97\n"
      "v_mfma_f32_16x16x32_bf16 v[158:161], a[32:35], v[134:137], v[158:161]\n"
      "v_fma_f32 v205, v104, v104, s51\n"
      "v_bfe_u32 v219, v185, 4, 4\n"
      "v_mfma_f32_16x16x32_bf16 v[158:161], a[36:39], v[170:173], v[158:161]\n"
      "v_fmac_f32 v81, v98, v98\n"
      "v_mul_u32_u24 v82, 98, v219\n"
      "v_mfma_f32_16x16x32_bf16 v[158:161], a[40:43], v[166:169], v[158:161]\n"
      "v_lshrrev_b32 v185, 8, v185\n"
      "v_fmac_f32_e64 v205, v105, v105\n"
      "v_mfma_f32_16x16x32_bf16 v[158:161], a[44:47], v[116:119], v[158:161]\n"
      "s_cmp_ge_u32 s95, 0x40000000\n"
      "s_cselect_b32 s94, -1, -2\n"
      "v_fmac_f32 v81, v99, v99\n"
      "v_mfma_f32_16x16x32_bf16 v[126:129], a[48:51], v[198:201], 0\n"
      "v_fmac_f32 v81, v146, v146\n"
      "v_fmac_f32 v81, v147, v147\n"
      "v_mfma_f32_16x16x32_bf16 v[126:129], a[52:55], v[138:141], v[126:129]\n"
      "v_fmac_f32 v77, v177, v177\n"
      "v_fmac_f32_e64 v81, v148, v148\n"
      "v_mfma_f32_16x16x32_bf16 v[126:129], a[56:59], v[134:137], v[126:129]\n"
      "v_cvt_pk_bf16_f32 v214, v214, v215\n"
      "v_fmac_f32 v205, v106, v106\n"
      "v_mfma_f32_16x16x32_bf16 v[126:129], a[60:63], v[170:173], v[126:129]\n"
      "s_cmp_gt_u32 s42, s44\n"
      "s_cselect_b32 s95, s94, s95\n"
      "v_fmac_f32 v81, v149, v149\n"
      "v_mfma_f32_16x16x32_bf16 v[126:129], a[64:67], v[166:169], v[126:129]\n"
      "ds_read_b128 v[110:113], v9 offset:5632\n"
      "v_cvt_pk_bf16_f32 v215, v216, v217\n"
      "v_mfma_f32_16x16x32_bf16 v[126:129], a[68:71], v[116:119], v[126:129]\n"
      "v_permlane16_swap_b32 v77, v81\n"
      "v_mfma_f32_16x16x32_bf16 v[120:123], a[72:75], v[198:201], 0\n"
      "v_add_f32 v77, v77, v81\n"
      "v_mov_b32 v81, v77\n"
      "v_mfma_f32_16x16x32_bf16 v[120:123], a[76:79], v[138:141], v[120:123]\n"
      "s_nop 0\n"
      "v_permlane32_swap_b32 v77, v81\n"
      "v_mfma_f32_16x16x32_bf16 v[120:123], a[80:83], v[134:137], v[120:123]\n"
      "v_fmac_f32 v205, v107, v107\n"
      "v_add_f32 v77, v77, v81\n"
      "v_mfma_f32_16x16x32_bf16 v[120:123], a[84:87], v[170:173], v[120:123]\n"
      "v_rsq_f32 v77, v77\n"
      "v_mfma_f32_16x16x32_bf16 v[120:123], a[88:91], v[166:169], v[120:123]\n"
      "v_fmac_f32 v205, v158, v158\n"
      "v_mul_f32 v218, v188, v218\n"
      "v_mfma_f32_16x16x32_bf16 v[120:123], a[92:95], v[116:119], v[120:123]\n"
      "v_mov_b32 v81, v77\n"
      "v_fmac_f32 v205, v159, v159\n"
      "v_mfma_f32_16x16x32_bf16 v[100:103], a[96:99], v[198:201], 0\n"
      "v_lshl_add_u32 v82, v185, 3, v82\n"
      "v_lshlrev_b32 v185, 2, v185\n"
      "v_mfma_f32_16x16x32_bf16 v[100:103], a[100:103], v[138:141], v[100:103]\n"
      "v_permlane16_swap_b32 v77, v81\n"
      "v_mfma_f32_16x16x32_bf16 v[100:103], a[104:107], v[134:137], v[100:103]\n"
      "v_mul_f32 v77, s50, v77\n"
      "v_fmac_f32 v205, v160, v160\n"
      "v_mfma_f32_16x16x32_bf16 v[100:103], a[108:111], v[170:173], v[100:103]\n"
      "v_mul_f32 v146, v81, v146\n"
      "v_mul_f32 v176, v77, v176\n"
      "v_mfma_f32_16x16x32_bf16 v[100:103], a[112:115], v[166:169], v[100:103]\n"
      "v_mul_f32 v152, v81, v152\n"
      "v_mul_f32 v212, v77, v212\n"
      "v_mfma_f32_16x16x32_bf16 v[100:103], a[116:119], v[116:119], v[100:103]\n"
      "v_fma_f32 v204, v120, v120, s51\n"
      "v_mul_f32 v98, v81, v98\n"
      "v_mfma_f32_16x16x32_bf16 v[154:157], a[120:123], v[198:201], 0\n"
      "v_fmac_f32 v204, v121, v121\n"
      "v_sub_u32_e64 v219, v219, v185\n"
      "v_mfma_f32_16x16x32_bf16 v[154:157], a[124:127], v[138:141], v[154:157]\n"
      "ds_read_b128 v[182:185], v1 offset:16384\n"
      "v_fmac_f32 v205, v161, v161\n"
      "v_mfma_f32_16x16x32_bf16 v[154:157], a[128:131], v[134:137], v[154:157]\n"
      "s_waitcnt lgkmcnt(1)\n"
      "v_fmac_f32_e64 v204, v122, v122\n"
      "v_or3_b32 v110, v110, v111, v112\n"
      "v_mfma_f32_16x16x32_bf16 v[154:157], a[132:135], v[170:173], v[154:157]\n"
      "v_mul_f32 v99, v81, v99\n"
      "v_mul_f32 v174, v77, v174\n"
      "v_mfma_f32_16x16x32_bf16 v[154:157], a[136:139], v[166:169], v[154:157]\n"
      "v_mul_f32 v192, v77, v192\n"
      "v_mul_f32 v177, v77, v177\n"
      "v_mfma_f32_16x16x32_bf16 v[154:157], a[140:143], v[116:119], v[154:157]\n"
      "ds_write_b64 v6, v[108:109] offset:12288\n"
      "v_mfma_f32_16x16x32_bf16 v[142:145], v[198:201], a[144:147], 0\n"
      "v_fmac_f32 v204, v123, v123\n"
      "v_cvt_pk_bf16_f32 v218, v218, v218\n"
      "v_mfma_f32_16x16x32_bf16 v[142:145], v[138:141], a[148:151], v[142:145]\n"
      "ds_read_b128 v[186:189], v1 offset:4096\n"
      "v_fmac_f32 v204, v100, v100\n"
      "v_mfma_f32_16x16x32_bf16 v[142:145], v[134:137], a[152:155], v[142:145]\n"
      "ds_write_b16 v12, v218 offset:16\n"
      "v_mfma_f32_16x16x32_bf16 v[142:145], v[170:173], a[156:159], v[142:145]\n"
      "v_mul_f32 v151, v81, v151\n"
      "v_mul_f32 v190, v77, v190\n"
      "v_mfma_f32_16x16x32_bf16 v[142:145], v[166:169], a[160:163], v[142:145]\n"
      "v_fmac_f32 v205, v126, v126\n"
      "v_fmac_f32 v204, v101, v101\n"
      "v_mfma_f32_16x16x32_bf16 v[142:145], v[116:119], a[164:167], v[142:145]\n"
      "v_fmac_f32 v204, v102, v102\n"
      "v_mul_f32 v149, v81, v149\n"
      "v_mfma_f32_16x16x32_bf16 v[162:165], v[198:201], a[168:171], 0\n"
      "v_mul_f32 v147, v81, v147\n"
      "v_bitop3_b32 v110, v110, v113, s33 bitop3:0xa8\n"
      "v_mfma_f32_16x16x32_bf16 v[162:165], v[138:141], a[172:175], v[162:165]\n"
      "v_mul_f32 v210, v77, v210\n"
      "v_fmac_f32 v204, v103, v103\n"
      "v_mfma_f32_16x16x32_bf16 v[162:165], v[134:137], a[176:179], v[162:165]\n"
      "ds_read_b128 v[112:115], v1 offset:10240\n"
      "v_add_u32 v82, s77, v82\n"
      "v_mfma_f32_16x16x32_bf16 v[162:165], v[170:173], a[180:183], v[162:165]\n"
      "v_fmac_f32 v204, v154, v154\n"
      "v_cmp_ne_u32 s[56:57], 0, v110\n"
      "v_mfma_f32_16x16x32_bf16 v[162:165], v[166:169], a[184:187], v[162:165]\n"
      "v_mul_f32 v153, v81, v153\n"
      "v_mul_f32_e64 v97, v81, v97\n"
      "v_mfma_f32_16x16x32_bf16 v[162:165], v[116:119], a[188:191], v[162:165]\n"
      "ds_read_b128 v[108:111], v0 offset:4096\n"
      "v_mul_f32 v191, v77, v191\n"
      "v_mfma_f32_16x16x32_bf16 v[178:181], v[198:201], a[192:195], 0\n"
      "v_add_u32 v12, 32, v12\n"
      "v_fmac_f32 v205, v127, v127\n"
      "v_mfma_f32_16x16x32_bf16 v[178:181], v[138:141], a[196:199], v[178:181]\n"
      "v_fmac_f32 v204, v155, v155\n"
      "v_fmac_f32 v205, v128, v128\n"
      "v_mfma_f32_16x16x32_bf16 v[178:181], v[134:137], a[200:203], v[178:181]\n"
      "v_fmac_f32 v204, v156, v156\n"
      "v_mul_f32 v150, v81, v150\n"
      "v_mfma_f32_16x16x32_bf16 v[178:181], v[170:173], a[204:207], v[178:181]\n"
      "ds_read_b128 v[198:201], v0 offset:16384\n"
      "v_mul_f32 v193, v77, v193\n"
      "v_mfma_f32_16x16x32_bf16 v[178:181], v[166:169], a[208:211], v[178:181]\n"
      "s_add_u32 s43, s43, 1\n"
      "v_fmac_f32 v205, v129, v129\n"
      "v_mul_f32 v211, v77, v211\n"
      "v_mfma_f32_16x16x32_bf16 v[178:181], v[116:119], a[212:215], v[178:181]\n"
      "s_waitcnt lgkmcnt(1)\n"
      "v_cvt_pk_bf16_f32 v210, v210, v211\n"
      "v_cvt_pk_bf16_f32 v150, v150, v151\n"
      "v_mfma_f32_16x16x32_bf16 v[116:119], a[0:3], v[108:111], 0\n"
      "v_cvt_pk_bf16_f32 v151, v152, v153\n"
      "v_fmac_f32_e64 v204, v157, v157\n"
      "v_mfma_f32_16x16x32_bf16 v[116:119], a[4:7], v[186:189], v[116:119]\n"
      "s_nop 0\n"
      "v_permlane16_swap_b32 v205, v204\n"
      "v_mfma_f32_16x16x32_bf16 v[116:119], a[8:11], v[220:223], v[116:119]\n"
      "v_lshlrev_b32 v219, 4, v219\n"
      "v_add_f32 v205, v205, v204\n"
      "v_mfma_f32_16x16x32_bf16 v[116:119], a[12:15], v[112:115], v[116:119]\n"
      "s_waitcnt lgkmcnt(0)\n"
      "v_mul_f32 v96, v81, v96\n"
      "v_mov_b32 v204, v205\n"
      "v_mfma_f32_16x16x32_bf16 v[116:119], a[16:19], v[198:201], v[116:119]\n"
      "s_nop 0\n"
      "v_permlane32_swap_b32_e64 v205, v204\n"
      "v_mfma_f32_16x16x32_bf16 v[116:119], a[20:23], v[182:185], v[116:119]\n"
      "v_cvt_pk_bf16_f32 v216, v178, v179\n"
      "v_cvt_pk_bf16_f32 v132, v142, v143\n"
      "v_mfma_f32_16x16x32_bf16 v[134:137], a[24:27], v[108:111], 0\n"
      "v_cvt_pk_bf16_f32 v146, v146, v147\n"
      "v_add_f32 v205, v205, v204\n"
      "v_mfma_f32_16x16x32_bf16 v[134:137], a[28:31], v[186:189], v[134:137]\n"
      "v_rsq_f32 v205, v205\n"
      "v_mfma_f32_16x16x32_bf16 v[134:137], a[32:35], v[220:223], v[134:137]\n"
      "v_mul_f32 v213, v77, v213\n"
      "v_fma_f32 v218, v116, v116, s51\n"
      "v_mfma_f32_16x16x32_bf16 v[134:137], a[36:39], v[112:115], v[134:137]\n"
      "s_waitcnt vmcnt(7)\n"
      "v_mov_b32 v204, v205\n"
      "v_cvt_pk_bf16_f32 v153, v98, v99\n"
      "v_mfma_f32_16x16x32_bf16 v[134:137], a[40:43], v[198:201], v[134:137]\n"
      "v_permlane16_swap_b32 v205, v204\n"
      "v_mfma_f32_16x16x32_bf16 v[134:137], a[44:47], v[182:185], v[134:137]\n"
      "v_and_b32 v92, 2, v82\n"
      "v_mul_f32 v100, v204, v100\n"
      "v_mfma_f32_16x16x32_bf16 v[138:141], a[48:51], v[108:111], 0\n"
      "v_cvt_pk_bf16_f32 v196, v162, v163\n"
      "v_mul_f32_e64 v148, v81, v148\n"
      "v_mfma_f32_16x16x32_bf16 v[138:141], a[52:55], v[186:189], v[138:141]\n"
      "v_fmac_f32 v218, v117, v117\n"
      "v_fmac_f32 v218, v118, v118\n"
      "v_mfma_f32_16x16x32_bf16 v[138:141], a[56:59], v[220:223], v[138:141]\n"
      "v_mul_f32 v101, v204, v101\n"
      "v_mul_f32 v205, s50, v205\n"
      "v_mfma_f32_16x16x32_bf16 v[138:141], a[60:63], v[112:115], v[138:141]\n"
      "v_cvt_pk_bf16_f32 v152, v96, v97\n"
      "v_mul_f32 v105, v205, v105\n"
      "v_mfma_f32_16x16x32_bf16 v[138:141], a[64:67], v[198:201], v[138:141]\n"
      "v_mul_f32 v121, v204, v121\n"
      "v_cvt_pk_bf16_f32 v211, v212, v213\n"
      "v_mfma_f32_16x16x32_bf16 v[138:141], a[68:71], v[182:185], v[138:141]\n"
      "v_mul_f32_e64 v104, v205, v104\n"
      "v_cvt_pk_bf16_f32 v104, v104, v105\n"
      "v_mfma_f32_16x16x32_bf16 v[96:99], a[72:75], v[108:111], 0\n"
      "v_mul_f32_e64 v158, v205, v158\n"
      "v_cvt_pk_bf16_f32 v133, v144, v145\n"
      "v_mfma_f32_16x16x32_bf16 v[96:99], a[76:79], v[186:189], v[96:99]\n"
      "v_cvt_pk_bf16_f32 v197, v164, v165\n"
      "v_mul_f32 v154, v204, v154\n"
      "v_mfma_f32_16x16x32_bf16 v[96:99], a[80:83], v[220:223], v[96:99]\n"
      "v_mul_f32 v155, v204, v155\n"
      "v_fmac_f32_e64 v218, v119, v119\n"
      "v_mfma_f32_16x16x32_bf16 v[96:99], a[84:87], v[112:115], v[96:99]\n"
      "v_mul_f32_e64 v175, v77, v175\n"
      "v_cvt_pk_bf16_f32 v174, v174, v175\n"
      "v_mfma_f32_16x16x32_bf16 v[96:99], a[88:91], v[198:201], v[96:99]\n"
      "v_cvt_pk_bf16_f32 v154, v154, v155\n"
      "v_and_b32 v82, -4, v82\n"
      "v_mfma_f32_16x16x32_bf16 v[96:99], a[92:95], v[182:185], v[96:99]\n"
      "v_mul_f32 v159, v205, v159\n"
      "v_fmac_f32_e64 v218, v134, v134\n"
      "v_mfma_f32_16x16x32_bf16 v[162:165], a[96:99], v[108:111], 0\n"
      "v_cvt_pk_bf16_f32 v147, v148, v149\n"
      "v_mul_f32_e64 v120, v204, v120\n"
      "v_mfma_f32_16x16x32_bf16 v[162:165], a[100:103], v[186:189], v[162:165]\n"
      "v_cmp_gt_u32 s[72:73], 64, v219\n"
      "v_fmac_f32 v218, v135, v135\n"
      "v_mfma_f32_16x16x32_bf16 v[162:165], a[104:107], v[220:223], v[162:165]\n"
      "v_fma_f32 v178, v96, v96, s51\n"
      "v_fmac_f32 v178, v97, v97\n"
      "v_fmac_f32 v218, v136, v136\n"
      "v_fmac_f32 v218, v137, v137\n"
      "v_cvt_pk_bf16_f32 v120, v120, v121\n"
      "v_pk_mul_f32 v[128:129], v[128:129], v[204:205] op_sel:[0,1]\n"
      "v_pk_mul_f32 v[102:103], v[102:103], v[204:205] op_sel_hi:[1,0]\n"
      "v_pk_mul_f32 v[106:107], v[106:107], v[204:205] op_sel:[0,1]\n"
      "v_pk_mul_f32 v[156:157], v[156:157], v[204:205] op_sel_hi:[1,0]\n"
      "v_pk_mul_f32 v[160:161], v[160:161], v[204:205] op_sel:[0,1]\n"
      "v_pk_mul_f32 v[126:127], v[126:127], v[204:205] op_sel:[0,1]\n"
      "v_pk_mul_f32 v[122:123], v[122:123], v[204:205] op_sel_hi:[1,0]\n"
      "v_mfma_f32_16x16x32_bf16 v[162:165], a[108:111], v[112:115], v[162:165]\n"
      "v_cvt_pk_bf16_f32 v121, v122, v123\n"
      "v_fmac_f32_e64 v178, v98, v98\n"
      "v_mfma_f32_16x16x32_bf16 v[162:165], a[112:115], v[198:201], v[162:165]\n"
      "v_cvt_pk_bf16_f32 v175, v176, v177\n"
      "v_fmac_f32 v218, v138, v138\n"
      "v_mfma_f32_16x16x32_bf16 v[162:165], a[116:119], v[182:185], v[162:165]\n"
      "s_add_u32 s42, s42, 1\n"
      "v_cvt_pk_bf16_f32 v155, v156, v157\n"
      "v_cvt_pk_bf16_f32 v105, v106, v107\n"
      "v_mfma_f32_16x16x32_bf16 v[202:205], a[120:123], v[108:111], 0\n"
      "v_fmac_f32 v218, v139, v139\n"
      "v_fmac_f32 v178, v99, v99\n"
      "v_mfma_f32_16x16x32_bf16 v[202:205], a[124:127], v[186:189], v[202:205]\n"
      "v_cvt_pk_bf16_f32 v126, v126, v127\n"
      "v_fmac_f32 v218, v140, v140\n"
      "v_mfma_f32_16x16x32_bf16 v[202:205], a[128:131], v[220:223], v[202:205]\n"
      "v_fmac_f32 v178, v162, v162\n"
      "v_fmac_f32_e64 v178, v163, v163\n"
      "v_mfma_f32_16x16x32_bf16 v[202:205], a[132:135], v[112:115], v[202:205]\n"
      "v_cvt_pk_bf16_f32 v212, v190, v191\n"
      "v_cvt_pk_bf16_f32 v122, v100, v101\n"
      "v_mfma_f32_16x16x32_bf16 v[202:205], a[136:139], v[198:201], v[202:205]\n"
      "v_cvt_pk_bf16_f32 v106, v158, v159\n"
      "v_fmac_f32_e64 v178, v164, v164\n"
      "v_mfma_f32_16x16x32_bf16 v[202:205], a[140:143], v[182:185], v[202:205]\n"
      "v_cvt_pk_bf16_f32 v213, v192, v193\n"
      "v_fmac_f32 v218, v141, v141\n"
      "v_mfma_f32_16x16x32_bf16 v[166:169], v[108:111], a[144:147], 0\n"
      "s_cmp_ge_u32 s95, 0x40000000\n"
      "s_cselect_b64 vcc, -1, s[56:57]\n"
      "v_fmac_f32 v178, v165, v165\n"
      "v_mfma_f32_16x16x32_bf16 v[166:169], v[186:189], a[148:151], v[166:169]\n"
      "s_nop 0\n"
      "v_fmac_f32 v178, v202, v202\n"
      "v_fmac_f32 v178, v203, v203\n"
      "v_mfma_f32_16x16x32_bf16 v[166:169], v[220:223], a[152:155], v[166:169]\n"
      "v_fmac_f32 v178, v204, v204\n"
      "v_fmac_f32 v178, v205, v205\n"
      "v_mfma_f32_16x16x32_bf16 v[166:169], v[112:115], a[156:159], v[166:169]\n"
      "s_nop 0\n"
      "v_permlane16_swap_b32 v218, v178\n"
      "v_mfma_f32_16x16x32_bf16 v[166:169], v[198:201], a[160:163], v[166:169]\n"
      "v_add_f32 v218, v218, v178\n"
      "v_mov_b32 v178, v218\n"
      "v_mfma_f32_16x16x32_bf16 v[166:169], v[182:185], a[164:167], v[166:169]\n"
      "s_nop 0\n"
      "v_permlane32_swap_b32 v218, v178\n"
      "v_add_f32 v218, v218, v178\n"
      "v_mfma_f32_16x16x32_bf16 v[170:173], v[108:111], a[168:171], 0\n"
      "v_rsq_f32 v218, v218\n"
      "v_mfma_f32_16x16x32_bf16 v[170:173], v[186:189], a[172:175], v[170:173]\n"
      "v_cvt_pk_bf16_f32 v107, v160, v161\n"
      "v_mov_b32 v178, v218\n"
      "v_mfma_f32_16x16x32_bf16 v[170:173], v[220:223], a[176:179], v[170:173]\n"
      "s_nop 0\n"
      "v_permlane16_swap_b32 v218, v178\n"
      "v_mfma_f32_16x16x32_bf16 v[170:173], v[112:115], a[180:183], v[170:173]\n"
      "ds_read_b128 v[142:145], v7 offset:18432\n"
      "v_cvt_pk_bf16_f32 v127, v128, v129\n"
      "v_lshlrev_b64 v[124:125], v219, v[124:125]\n"
      "v_cvt_pk_bf16_f32 v77, v168, v169\n"
      "v_mul_f32_e64 v218, s50, v218\n"
      "v_pk_mul_f32 v[204:205], v[204:205], v[178:179] op_sel_hi:[1,0]\n"
      "v_pk_mul_f32 v[202:203], v[202:203], v[178:179] op_sel_hi:[1,0]\n"
      "v_pk_mul_f32 v[116:117], v[116:117], v[218:219] op_sel_hi:[1,0]\n"
      "v_pk_mul_f32 v[98:99], v[98:99], v[178:179] op_sel_hi:[1,0]\n"
      "v_pk_mul_f32 v[164:165], v[164:165], v[178:179] op_sel_hi:[1,0]\n"
      "v_pk_mul_f32 v[138:139], v[138:139], v[218:219] op_sel_hi:[1,0]\n"
      "v_pk_mul_f32 v[162:163], v[162:163], v[178:179] op_sel_hi:[1,0]\n"
      "v_pk_mul_f32 v[96:97], v[96:97], v[178:179] op_sel_hi:[1,0]\n"
      "v_pk_mul_f32 v[140:141], v[140:141], v[218:219] op_sel_hi:[1,0]\n"
      "v_mfma_f32_16x16x32_bf16 v[170:173], v[198:201], a[184:187], v[170:173]\n"
      "ds_read_b128 v[176:179], v2 offset:18432\n"
      "v_cvt_pk_bf16_f32 v96, v96, v97\n"
      "v_mfma_f32_16x16x32_bf16 v[170:173], v[182:185], a[188:191], v[170:173]\n"
      "ds_read_b128 v[158:161], v2 offset:21504\n"
      "v_add_u32 v148, 110592, v82\n"
      "v_mfma_f32_16x16x32_bf16 v[190:193], v[108:111], a[192:195], 0\n"
      "ds_read_b128 v[108:111], v2 offset:24576\n"
      "v_cvt_pk_bf16_f32 v97, v98, v99\n"
      "v_mfma_f32_16x16x32_bf16 v[190:193], v[186:189], a[196:199], v[190:193]\n"
      "ds_read_b128 v[186:189], v2 offset:30720\n"
      "v_cvt_pk_bf16_f32 v123, v102, v103\n"
      "v_mfma_f32_16x16x32_bf16 v[190:193], v[220:223], a[200:203], v[190:193]\n"
      "ds_read_b128 v[100:103], v2 offset:27648\n"
      "v_cvt_pk_bf16_f32 v138, v138, v139\n"
      "v_mfma_f32_16x16x32_bf16 v[190:193], v[112:115], a[204:207], v[190:193]\n"
      "ds_read_b128 v[220:223], v2 offset:33792\n"
      "v_cvt_pk_bf16_f32 v81, v172, v173\n"
      "v_mfma_f32_16x16x32_bf16 v[190:193], v[198:201], a[208:211], v[190:193]\n"
      "ds_read_b128 v[112:115], v7 offset:22528\n"
      "v_cvt_pk_bf16_f32 v99, v164, v165\n"
      "v_mfma_f32_16x16x32_bf16 v[190:193], v[182:185], a[212:215], v[190:193]\n"
      "s_waitcnt lgkmcnt(5)\n"
      "v_cvt_pk_bf16_f32 v76, v166, v167\n"
      "v_mov_b32 v129, 0\n"
      "v_mfma_f32_16x16x32_bf16 v[166:169], a[216:219], v[176:179], 0\n"
      "buffer_store_dwordx4 v[142:145], v20, s[24:27], s46 offen sc1\n"
      "v_mfma_f32_16x16x32_bf16 v[166:169], a[220:223], v[158:161], v[166:169]\n"
      "s_waitcnt lgkmcnt(2)\n"
      "v_mul_f32 v119, v218, v119\n"
      "v_mul_f32 v136, v218, v136\n"
      "v_mfma_f32_16x16x32_bf16 v[166:169], a[224:227], v[108:111], v[166:169]\n"
      "v_cvt_pk_bf16_f32 v202, v202, v203\n"
      "v_cvt_pk_bf16_f32 v203, v204, v205\n"
      "v_mfma_f32_16x16x32_bf16 v[198:201], a[240:243], v[176:179], 0\n"
      "ds_read_b64 v[204:205], v10 offset:24\n"
      "v_cndmask_b32_e64 v124, 0, v124, s[72:73]\n"
      "v_mfma_f32_16x16x32_bf16 v[198:201], a[244:247], v[158:161], v[198:201]\n"
      "s_waitcnt lgkmcnt(1)\n"
      "v_cndmask_b32_e64 v125, 0, v125, s[72:73]\n"
      "v_cvt_pk_bf16_f32 v98, v162, v163\n"
      "v_mfma_f32_16x16x32_bf16 v[198:201], a[248:251], v[108:111], v[198:201]\n"
      "ds_read2_b32 v[172:173], v148 offset0:24 offset1:18\n"
      "v_mov_b32 v128, 0xbb2b\n"
      "v_mfma_f32_16x16x32_bf16 v[142:145], v[232:235], v[176:179], 0\n"
      "ds_read2_b32 v[184:185], v148 offset0:2 offset1:10\n"
      "v_lshlrev_b64 v[128:129], v219, v[128:129]\n"
      "v_mfma_f32_16x16x32_bf16 v[142:145], v[236:239], v[158:161], v[142:145]\n"
      "ds_read_b128 v[162:165], v7 offset:26624\n"
      "v_cvt_pk_bf16_f32 v217, v180, v181\n"
      "v_mfma_f32_16x16x32_bf16 v[142:145], v[240:243], v[108:111], v[142:145]\n"
      "buffer_store_dwordx4 v[112:115], v21, s[24:27], s46 offen sc1\n"
      "v_pk_mul_f32 v[134:135], v[134:135], v[218:219] op_sel_hi:[1,0]\n"
      "v_mfma_f32_16x16x32_bf16 v[166:169], a[228:231], v[100:103], v[166:169]\n"
      "ds_read2_b32 v[158:159], v148 offset0:0 offset1:1\n"
      "v_mul_f32 v137, v218, v137\n"
      "v_mfma_f32_16x16x32_bf16 v[166:169], a[232:235], v[186:189], v[166:169]\n"
      "s_waitcnt lgkmcnt(3)\n"
      "v_mul_f32 v118, v218, v118\n"
      "v_cvt_pk_bf16_f32 v84, v190, v191\n"
      "v_mfma_f32_16x16x32_bf16 v[166:169], a[236:239], v[220:223], v[166:169]\n"
      "ds_read2_b32 v[156:157], v148 offset0:8 offset1:9\n"
      "v_cvt_pk_bf16_f32 v116, v116, v117\n"
      "v_mfma_f32_16x16x32_bf16 v[198:201], a[252:255], v[100:103], v[198:201]\n"
      "ds_read2_b32 v[88:89], v148 offset0:16 offset1:17\n"
      "v_alignbyte_b32 v172, v172, v172, v92\n"
      "v_mfma_f32_16x16x32_bf16 v[198:201], v[224:227], v[186:189], v[198:201]\n"
      "s_waitcnt lgkmcnt(3)\n"
      "v_cvt_pk_bf16_f32 v117, v118, v119\n"
      "v_cvt_pk_bf16_f32 v85, v192, v193\n"
      "v_mfma_f32_16x16x32_bf16 v[198:201], v[228:231], v[220:223], v[198:201]\n"
      "ds_read_b128 v[180:183], v7 offset:30720\n"
      "v_fma_f32 v148, v166, v166, s52\n"
      "v_mfma_f32_16x16x32_bf16 v[142:145], v[244:247], v[100:103], v[142:145]\n"
      "ds_read_b128 v[100:103], v10 offset:32\n"
      "v_mul_f32_e64 v149, v167, v167\n"
      "v_mfma_f32_16x16x32_bf16 v[142:145], v[248:251], v[186:189], v[142:145]\n"
      "buffer_store_dwordx4 v[162:165], v22, s[24:27], s46 offen sc1\n"
      "v_mfma_f32_16x16x32_bf16 v[142:145], v[252:255], v[220:223], v[142:145]\n"
      "v_add_u32 v160, 112160, v82\n"
      "v_cmp_gt_u32 s[72:73], 64, v219\n"
      "v_mfma_f32_16x16x32_bf16 v[112:115], v[150:153], v[210:213], v[40:43]\n"
      "s_waitcnt lgkmcnt(2)\n"
      "v_cvt_pk_bf16_f32 v118, v134, v135\n"
      "v_cvt_pk_bf16_f32 v139, v140, v141\n"
      "v_mfma_f32_16x16x32_bf16 v[108:111], v[120:123], v[210:213], v[44:47]\n"
      "ds_read_b128 v[188:191], v2 offset:21760\n"
      "v_cvt_pk_bf16_f32 v119, v136, v137\n"
      "v_mfma_f32_16x16x32_bf16 v[218:221], v[96:99], v[210:213], v[48:51]\n"
      "s_waitcnt lgkmcnt(1)\n"
      "v_alignbyte_b32 v156, v157, v156, v92\n"
      "v_alignbyte_b32 v158, v159, v158, v92\n"
      "v_mfma_f32_16x16x32_bf16 v[176:179], v[100:103], v[210:213], v[24:27]\n"
      "ds_read_b128 v[162:165], v7 offset:34816\n"
      "v_fmac_f32_e64 v149, v169, v169\n"
      "v_mfma_f32_16x16x16_bf16 v[112:115], v[146:147], v[174:175], v[112:115]\n"
      "v_cvt_pk_bf16_f32 v80, v170, v171\n"
      "v_alignbyte_b32 v159, v184, v159, v92\n"
      "v_cndmask_b32_e64 v128, 0, v128, s[72:73]\n"
      "v_mfma_f32_16x16x16_bf16 v[108:111], v[154:155], v[174:175], v[108:111]\n"
      "ds_read_b128 v[210:213], v2 offset:18688\n"
      "v_alignbyte_b32 v157, v185, v157, v92\n"
      "v_mfma_f32_16x16x16_bf16 v[218:221], v[202:203], v[174:175], v[218:221]\n"
      "buffer_store_dwordx4 v[180:183], v23, s[24:27], s46 offen sc1\n"
      "v_mfma_f32_16x16x16_bf16 v[176:179], v[204:205], v[174:175], v[176:179]\n"
      "v_alignbyte_b32 v88, v89, v88, v92\n"
      "v_subrev_u32 v39, 115456, v9\n"
      "v_cndmask_b32_e64 v129, 0, v129, s[72:73]\n"
      "v_mfma_f32_16x16x16_bf16 v[112:115], v[124:125], v[158:159], v[112:115]\n"
      "v_fmac_f32 v148, v168, v168\n"
      "v_lshrrev_b32 v39, 3, v39\n"
      "v_alignbyte_b32 v89, v173, v89, v92\n"
      "v_mfma_f32_16x16x16_bf16 v[108:111], v[124:125], v[156:157], v[108:111]\n"
      "v_fmac_f32_e64 v148, v198, v198\n"
      "v_fmac_f32_e64 v149, v199, v199\n"
      "v_min_u32 v39, 96, v39\n"
      "v_mfma_f32_16x16x16_bf16 v[218:221], v[124:125], v[88:89], v[218:221]\n"
      "v_add_u32 v140, 113728, v82\n"
      "v_fmac_f32_e64 v149, v201, v201\n"
      "v_fmac_f32 v148, v200, v200\n"
      "v_mfma_f32_16x16x16_bf16 v[176:179], v[124:125], v[172:173], v[176:179]\n"
      "ds_read2_b32 v[136:137], v160 offset0:0 offset1:1\n"
      "v_add_u32_e64 v39, s77, v39\n"
      "v_mfma_f32_16x16x16_bf16 v[112:115], v[128:129], v[158:159], v[112:115]\n"
      "ds_read_b128 v[184:187], v2 offset:24832\n"
      "v_add_u32 v39, 115296, v39\n"
      "v_mfma_f32_16x16x16_bf16 v[108:111], v[128:129], v[156:157], v[108:111]\n"
      "ds_read2_b32 v[222:223], v160 offset0:8 offset1:9\n"
      "ds_read2_b32 v[170:171], v160 offset0:2 offset1:10\n"
      "v_mfma_f32_16x16x16_bf16 v[218:221], v[128:129], v[88:89], v[218:221]\n"
      "s_waitcnt lgkmcnt(4)\n"
      "buffer_store_dwordx4 v[162:165], v25, s[24:27], s46 offen sc1\n"
      "v_pk_fma_f32 v[148:149], v[142:143], v[142:143], v[148:149]\n"
      "v_pk_fma_f32 v[148:149], v[144:145], v[144:145], v[148:149]\n"
      "v_mfma_f32_16x16x16_bf16 v[176:179], v[128:129], v[172:173], v[176:179]\n"
      "v_max3_f32 v134, v112, v113, v114\n"
      "v_max3_f32 v183, v115, v108, v109\n"
      "v_max3_f32 v134, v134, v110, v111\n"
      "v_add_f32_e64 v148, v148, v149\n"
      "v_mfma_f32_16x16x32_bf16 v[162:165], a[216:219], v[210:213], 0\n"
      "s_waitcnt lgkmcnt(2)\n"
      "v_max3_f32 v134, v134, v220, v221\n"
      "v_alignbyte_b32 v136, v137, v136, v92\n"
      "v_mfma_f32_16x16x32_bf16 v[162:165], a[220:223], v[188:191], v[162:165]\n"
      "s_waitcnt lgkmcnt(1)\n"
      "v_max3_f32 v183, v183, v218, v219\n"
      "v_alignbyte_b32 v222, v223, v222, v92\n"
      "v_max3_f32 v134, v134, v183, v176\n"
      "v_mfma_f32_16x16x32_bf16 v[162:165], a[224:227], v[184:187], v[162:165]\n"
      "s_waitcnt lgkmcnt(0)\n"
      "v_alignbyte_b32 v137, v170, v137, v92\n"
      "v_mov_b32 v157, v134\n"
      "v_mfma_f32_16x16x32_bf16 v[180:183], a[240:243], v[210:213], 0\n"
      "s_nop 0\n"
      "v_permlane16_swap_b32 v134, v157\n"
      "v_mfma_f32_16x16x32_bf16 v[180:183], a[244:247], v[188:191], v[180:183]\n"
      "s_waitcnt vmcnt(6)\n"
      "v_or3_b32 v206, v206, v207, v208\n"
      "v_max_f32 v134, v134, v157\n"
      "v_mfma_f32_16x16x32_bf16 v[180:183], a[248:251], v[184:187], v[180:183]\n"
      "ds_read2_b32 v[174:175], v160 offset0:16 offset1:17\n"
      "v_alignbyte_b32 v223, v171, v223, v92\n"
      "v_mfma_f32_16x16x32_bf16 v[170:173], v[232:235], v[210:213], 0\n"
      "ds_read2_b32 v[192:193], v160 offset0:24 offset1:18\n"
      "v_mov_b32 v157, v134\n"
      "v_mfma_f32_16x16x32_bf16 v[170:173], v[236:239], v[188:191], v[170:173]\n"
      "s_nop 0\n"
      "v_permlane32_swap_b32 v134, v157\n"
      "v_mfma_f32_16x16x32_bf16 v[170:173], v[240:243], v[184:187], v[170:173]\n"
      "v_max_f32 v134, v134, v157\n"
      "v_sub_f32 v220, v220, v134\n"
      "v_sub_f32 v113, v113, v134\n"
      "v_sub_f32 v112, v112, v134\n"
      "v_exp_f32 v220, v220\n"
      "v_mfma_f32_16x16x32_bf16 v[210:213], v[150:153], v[104:107], v[52:55]\n"
      "v_sub_f32 v108, v108, v134\n"
      "s_waitcnt vmcnt(5)\n"
      "v_or3_b32 v206, v206, v209, v78\n"
      "v_sub_f32 v218, v218, v134\n"
      "v_mfma_f32_16x16x32_bf16 v[188:191], v[120:123], v[104:107], v[56:59]\n"
      "s_waitcnt lgkmcnt(1)\n"
      "v_sub_f32 v109, v109, v134\n"
      "v_sub_f32 v221, v221, v134\n"
      "v_mfma_f32_16x16x32_bf16 v[156:159], v[96:99], v[104:107], v[60:63]\n"
      "ds_write_b32 v31, v206 offset:4608\n"
      "v_mfma_f32_16x16x32_bf16 v[184:187], v[100:103], v[104:107], v[26:29]\n"
      "s_waitcnt lgkmcnt(1)\n"
      "v_alignbyte_b32 v174, v175, v174, v92\n"
      "v_sub_f32 v110, v110, v134\n"
      "v_mfma_f32_16x16x16_bf16 v[210:213], v[146:147], v[126:127], v[210:213]\n"
      "v_sub_f32 v115, v115, v134\n"
      "v_exp_f32 v110, v110\n"
      "v_mfma_f32_16x16x16_bf16 v[188:191], v[154:155], v[126:127], v[188:191]\n"
      "v_exp_f32 v218, v218\n"
      "v_alignbyte_b32 v192, v192, v192, v92\n"
      "v_mfma_f32_16x16x16_bf16 v[156:159], v[202:203], v[126:127], v[156:159]\n"
      "v_alignbyte_b32 v175, v193, v175, v92\n"
      "v_exp_f32 v221, v221\n"
      "v_mfma_f32_16x16x16_bf16 v[184:187], v[204:205], v[126:127], v[184:187]\n"
      "v_exp_f32 v113, v113\n"
      "v_sub_f32 v176, v176, v134\n"
      "v_mfma_f32_16x16x16_bf16 v[210:213], v[124:125], v[136:137], v[210:213]\n"
      "v_exp_f32 v108, v108\n"
      "v_sub_f32 v111, v111, v134\n"
      "v_mfma_f32_16x16x16_bf16 v[188:191], v[124:125], v[222:223], v[188:191]\n"
      "v_exp_f32 v112, v112\n"
      "v_sub_f32 v219, v219, v134\n"
      "v_mfma_f32_16x16x16_bf16 v[156:159], v[124:125], v[174:175], v[156:159]\n"
      "v_exp_f32_e64 v115, v115\n"
      "v_cvt_pk_bf16_f32 v89, v220, v221\n"
      "v_mfma_f32_16x16x16_bf16 v[184:187], v[124:125], v[192:193], v[184:187]\n"
      "v_exp_f32 v111, v111\n"
      "v_sub_f32 v114, v114, v134\n"
      "v_mfma_f32_16x16x16_bf16 v[210:213], v[128:129], v[136:137], v[210:213]\n"
      "ds_read_b128 v[134:137], v2 offset:27904\n"
      "v_cvt_pk_bf16_f32 v112, v112, v113\n"
      "v_mfma_f32_16x16x16_bf16 v[188:191], v[128:129], v[222:223], v[188:191]\n"
      "ds_read_b128 v[220:223], v2 offset:30976\n"
      "v_exp_f32 v114, v114\n"
      "v_mfma_f32_16x16x16_bf16 v[156:159], v[128:129], v[174:175], v[156:159]\n"
      "ds_read_b128 v[104:107], v2 offset:34048\n"
      "v_exp_f32 v109, v109\n"
      "v_cvt_pk_bf16_f32 v113, v114, v115\n"
      "v_mfma_f32_16x16x16_bf16 v[184:187], v[128:129], v[192:193], v[184:187]\n"
      "v_cvt_pk_bf16_f32 v115, v110, v111\n"
      "v_max3_f32 v127, v210, v211, v212\n"
      "v_max3_f32 v127, v127, v190, v191\n"
      "v_exp_f32 v176, v176\n"
      "v_exp_f32 v219, v219\n"
      "v_max3_f32 v127, v127, v158, v159\n"
      "v_max3_f32 v206, v213, v188, v189\n"
      "v_cvt_pk_bf16_f32 v114, v108, v109\n"
      "v_max3_f32 v206, v206, v156, v157\n"
      "v_max3_f32 v127, v127, v206, v184\n"
      "s_waitcnt lgkmcnt(2)\n"
      "v_mfma_f32_16x16x32_bf16 v[162:165], a[228:231], v[134:137], v[162:165]\n"
      "s_waitcnt lgkmcnt(1)\n"
      "v_mov_b32 v109, v127\n"
      "v_cvt_pk_bf16_f32 v88, v218, v219\n"
      "v_mfma_f32_16x16x32_bf16 v[162:165], a[232:235], v[220:223], v[162:165]\n"
      "s_waitcnt lgkmcnt(0)\n"
      "v_permlane16_swap_b32_e64 v127, v109\n"
      "v_mfma_f32_16x16x32_bf16 v[162:165], a[236:239], v[104:107], v[162:165]\n"
      "ds_read2_b32 v[218:219], v140 offset0:2 offset1:10\n"
      "v_max_f32_e64 v127, v127, v109\n"
      "v_mfma_f32_16x16x32_bf16 v[180:183], a[252:255], v[134:137], v[180:183]\n"
      "ds_read2_b32 v[206:207], v140 offset0:8 offset1:9\n"
      "v_cvt_pk_bf16_f32 v90, v176, 0\n"
      "v_mfma_f32_16x16x32_bf16 v[180:183], v[224:227], v[220:223], v[180:183]\n"
      "ds_read2_b32 v[208:209], v140 offset0:0 offset1:1\n"
      "v_mov_b32 v109, v127\n"
      "v_mfma_f32_16x16x32_bf16 v[180:183], v[228:231], v[104:107], v[180:183]\n"
      "s_nop 0\n"
      "v_permlane32_swap_b32_e64 v127, v109\n"
      "v_mfma_f32_16x16x32_bf16 v[170:173], v[244:247], v[134:137], v[170:173]\n"
      "ds_read_u16 v78, v11\n"
      "v_max_f32 v127, v127, v109\n"
      "v_mfma_f32_16x16x32_bf16 v[170:173], v[248:251], v[220:223], v[170:173]\n"
      "v_sub_f32 v191, v191, v127\n"
      "v_sub_f32 v184, v184, v127\n"
      "v_sub_f32 v211, v211, v127\n"
      "v_mfma_f32_16x16x32_bf16 v[170:173], v[252:255], v[104:107], v[170:173]\n"
      "ds_read_u16 v82, v11 offset:32\n"
      "v_sub_f32 v157, v157, v127\n"
      "v_sub_f32 v212, v212, v127\n"
      "v_sub_f32 v210, v210, v127\n"
      "v_sub_f32 v190, v190, v127\n"
      "v_mfma_f32_16x16x32_bf16 v[108:111], v[32:35], v[112:115], 0\n"
      "s_waitcnt lgkmcnt(2)\n"
      "v_sub_f32 v213, v213, v127\n"
      "v_sub_f32_e64 v158, v158, v127\n"
      "v_mfma_f32_16x16x32_bf16 v[108:111], v[32:35], v[88:91], v[108:111]\n"
      "ds_read_u16 v86, v11 offset:64\n"
      "v_exp_f32 v212, v212\n"
      "v_exp_f32 v211, v211\n"
      "v_sub_f32 v189, v189, v127\n"
      "v_sub_f32 v156, v156, v127\n"
      "v_alignbyte_b32 v208, v209, v208, v92\n"
      "v_exp_f32 v210, v210\n"
      "v_alignbyte_b32 v209, v218, v209, v92\n"
      "v_alignbyte_b32 v206, v207, v206, v92\n"
      "v_sub_f32 v159, v159, v127\n"
      "v_mfma_f32_16x16x32_bf16 v[174:177], v[130:133], v[112:115], 0\n"
      "s_waitcnt lgkmcnt(1)\n"
      "v_sub_f32 v188, v188, v127\n"
      "v_exp_f32 v213, v213\n"
      "v_mfma_f32_16x16x32_bf16 v[174:177], v[76:79], v[88:91], v[174:177]\n"
      "ds_read2_b32 v[110:111], v140 offset0:16 offset1:17\n"
      "v_exp_f32 v188, v188\n"
      "v_mfma_f32_16x16x32_bf16 v[134:137], v[194:197], v[112:115], 0\n"
      "ds_read2_b32 v[126:127], v140 offset0:24 offset1:18\n"
      "v_exp_f32_e64 v189, v189\n"
      "v_mfma_f32_16x16x32_bf16 v[134:137], v[80:83], v[88:91], v[134:137]\n"
      "v_alignbyte_b32 v207, v219, v207, v92\n"
      "v_cvt_pk_bf16_f32 v210, v210, v211\n"
      "v_mfma_f32_16x16x32_bf16 v[218:221], v[214:217], v[112:115], 0\n"
      "s_waitcnt lgkmcnt(2)\n"
      "v_exp_f32 v190, v190\n"
      "v_mfma_f32_16x16x32_bf16 v[218:221], v[84:87], v[88:91], v[218:221]\n"
      "ds_read_u16 v39, v39\n"
      "v_exp_f32_e64 v191, v191\n"
      "v_cvt_pk_bf16_f32 v211, v212, v213\n"
      "v_cvt_pk_bf16_f32 v212, v188, v189\n"
      "v_mfma_f32_16x16x32_bf16 v[186:189], v[150:153], v[116:119], v[64:67]\n"
      "s_waitcnt lgkmcnt(2)\n"
      "v_cvt_pk_bf16_f32 v213, v190, v191\n"
      "v_alignbyte_b32 v110, v111, v110, v92\n"
      "v_mfma_f32_16x16x32_bf16 v[190:193], v[120:123], v[116:119], v[68:71]\n"
      "s_waitcnt lgkmcnt(1)\n"
      "v_alignbyte_b32 v111, v127, v111, v92\n"
      "v_alignbyte_b32 v126, v126, v126, v92\n"
      "v_mfma_f32_16x16x32_bf16 v[112:115], v[96:99], v[116:119], v[72:75]\n"
      "v_rcp_f32 v108, v108\n"
      "v_mfma_f32_16x16x32_bf16 v[104:107], v[100:103], v[116:119], v[28:31]\n"
      "v_exp_f32 v156, v156\n"
      "v_mfma_f32_16x16x16_bf16 v[186:189], v[146:147], v[138:139], v[186:189]\n"
      "v_exp_f32 v157, v157\n"
      "v_mul_f32 v175, v108, v175\n"
      "v_mfma_f32_16x16x16_bf16 v[190:193], v[154:155], v[138:139], v[190:193]\n"
      "v_exp_f32 v158, v158\n"
      "v_mul_f32 v219, v108, v219\n"
      "v_mfma_f32_16x16x16_bf16 v[112:115], v[202:203], v[138:139], v[112:115]\n"
      "v_exp_f32 v159, v159\n"
      "v_mul_f32 v137, v108, v137\n"
      "v_mfma_f32_16x16x16_bf16 v[104:107], v[204:205], v[138:139], v[104:107]\n"
      "v_mul_f32 v136, v108, v136\n"
      "v_cvt_pk_bf16_f32 v92, v156, v157\n"
      "v_mul_f32 v218, v108, v218\n"
      "v_mfma_f32_16x16x16_bf16 v[186:189], v[124:125], v[208:209], v[186:189]\n"
      "v_exp_f32 v184, v184\n"
      "v_fma_f32 v160, v162, v162, s52\n"
      "v_mfma_f32_16x16x16_bf16 v[190:193], v[124:125], v[206:207], v[190:193]\n"
      "v_mul_f32 v176, v108, v176\n"
      "v_cvt_pk_bf16_f32 v93, v158, v159\n"
      "v_mul_f32 v174, v108, v174\n"
      "v_mfma_f32_16x16x16_bf16 v[112:115], v[124:125], v[110:111], v[112:115]\n"
      "v_mul_f32 v161, v163, v163\n"
      "v_fmac_f32 v161, v165, v165\n"
      "v_mul_f32 v177, v108, v177\n"
      "v_mfma_f32_16x16x16_bf16 v[104:107], v[124:125], v[126:127], v[104:107]\n"
      "ds_read_b128 v[138:141], v2 offset:22016\n"
      "v_cvt_pk_bf16_f32 v218, v218, v219\n"
      "v_mfma_f32_16x16x16_bf16 v[186:189], v[128:129], v[208:209], v[186:189]\n"
      "v_fmac_f32_e64 v160, v164, v164\n"
      "v_cvt_pk_bf16_f32 v174, v174, v175\n"
      "v_cvt_pk_bf16_f32 v175, v176, v177\n"
      "v_mfma_f32_16x16x16_bf16 v[190:193], v[128:129], v[206:207], v[190:193]\n"
      "ds_read_b128 v[156:159], v2 offset:18944\n"
      "v_fmac_f32 v160, v180, v180\n"
      "v_mfma_f32_16x16x16_bf16 v[112:115], v[128:129], v[110:111], v[112:115]\n"
      "v_cvt_pk_bf16_f32 v177, v136, v137\n"
      "v_fmac_f32 v161, v181, v181\n"
      "v_cvt_pk_bf16_f32 v94, v184, 0\n"
      "v_mfma_f32_16x16x16_bf16 v[104:107], v[128:129], v[126:127], v[104:107]\n"
      "ds_read_b128 v[206:209], v2 offset:25088\n"
      "v_max3_f32 v129, v189, v190, v191\n"
      "v_max3_f32 v136, v186, v187, v188\n"
      "v_max3_f32 v136, v136, v192, v193\n"
      "v_max3_f32 v136, v136, v114, v115\n"
      "v_pk_mul_f32 v[134:135], v[134:135], v[108:109] op_sel_hi:[1,0]\n"
      "v_pk_mul_f32 v[220:221], v[220:221], v[108:109] op_sel_hi:[1,0]\n"
      "v_max3_f32 v129, v129, v112, v113\n"
      "v_max3_f32 v136, v136, v129, v104\n"
      "v_pk_fma_f32 v[160:161], v[182:183], v[182:183], v[160:161]\n"
      "v_cvt_pk_bf16_f32 v176, v134, v135\n"
      "v_mov_b32_e64 v108, v136\n"
      "v_pk_fma_f32 v[160:161], v[170:171], v[170:171], v[160:161]\n"
      "v_cvt_pk_bf16_f32 v219, v220, v221\n"
      "v_permlane16_swap_b32 v136, v108\n"
      "v_pk_fma_f32 v[160:161], v[172:173], v[172:173], v[160:161]\n"
      "s_waitcnt lgkmcnt(1)\n"
      "v_mfma_f32_16x16x32_bf16 v[126:129], a[216:219], v[156:159], 0\n"
      "ds_write_b128 v3, v[174:177]\n"
      "v_mfma_f32_16x16x32_bf16 v[126:129], a[220:223], v[138:141], v[126:129]\n"
      "s_waitcnt lgkmcnt(1)\n"
      "v_max_f32 v136, v136, v108\n"
      "v_mov_b32 v108, v136\n"
      "v_mfma_f32_16x16x32_bf16 v[126:129], a[224:227], v[206:209], v[126:129]\n"
      "s_nop 0\n"
      "v_permlane32_swap_b32 v136, v108\n"
      "v_mfma_f32_16x16x32_bf16 v[116:119], a[240:243], v[156:159], 0\n"
      "v_add_f32 v160, v160, v161\n"
      "v_max_f32 v136, v136, v108\n"
      "v_mfma_f32_16x16x32_bf16 v[116:119], a[244:247], v[138:141], v[116:119]\n"
      "v_sub_f32 v192, v192, v136\n"
      "v_sub_f32 v114, v114, v136\n"
      "v_mfma_f32_16x16x32_bf16 v[116:119], a[248:251], v[206:209], v[116:119]\n"
      "v_sub_f32 v193, v193, v136\n"
      "v_sub_f32 v190, v190, v136\n"
      "v_mfma_f32_16x16x32_bf16 v[108:111], v[232:235], v[156:159], 0\n"
      "v_sub_f32 v191, v191, v136\n"
      "v_sub_f32 v188, v188, v136\n"
      "v_mfma_f32_16x16x32_bf16 v[108:111], v[236:239], v[138:141], v[108:111]\n"
      "v_sub_f32 v104, v104, v136\n"
      "v_sub_f32 v115, v115, v136\n"
      "v_mfma_f32_16x16x32_bf16 v[108:111], v[240:243], v[206:209], v[108:111]\n"
      "v_sub_f32 v189, v189, v136\n"
      "v_sub_f32 v113, v113, v136\n"
      "v_mfma_f32_16x16x32_bf16 v[174:177], v[32:35], v[210:213], 0\n"
      "v_exp_f32 v191, v191\n"
      "v_sub_f32 v187, v187, v136\n"
      "v_sub_f32 v112, v112, v136\n"
      "v_sub_f32_e64 v186, v186, v136\n"
      "v_mfma_f32_16x16x32_bf16 v[174:177], v[32:35], v[92:95], v[174:177]\n"
      "ds_read_b128 v[134:137], v10\n"
      "v_exp_f32 v113, v113\n"
      "v_mfma_f32_16x16x32_bf16 v[220:223], v[130:133], v[210:213], 0\n"
      "v_exp_f32 v112, v112\n"
      "v_mfma_f32_16x16x32_bf16 v[220:223], v[76:79], v[92:95], v[220:223]\n"
      "v_exp_f32 v188, v188\n"
      "v_mfma_f32_16x16x32_bf16 v[138:141], v[194:197], v[210:213], 0\n"
      "v_exp_f32 v189, v189\n"
      "v_mfma_f32_16x16x32_bf16 v[138:141], v[80:83], v[92:95], v[138:141]\n"
      "v_rcp_f32 v174, v174\n"
      "v_mfma_f32_16x16x32_bf16 v[156:159], v[214:217], v[210:213], 0\n"
      "ds_read_b64 v[106:107], v10 offset:16\n"
      "v_exp_f32 v187, v187\n"
      "v_mfma_f32_16x16x32_bf16 v[156:159], v[84:87], v[92:95], v[156:159]\n"
      "s_waitcnt lgkmcnt(1)\n"
      "v_exp_f32 v186, v186\n"
      "v_mfma_f32_16x16x32_bf16 v[210:213], v[134:137], v[150:153], v[30:33]\n"
      "v_cvt_pk_bf16_f32 v88, v112, v113\n"
      "v_exp_f32_e64 v190, v190\n"
      "v_mfma_f32_16x16x32_bf16 v[176:179], v[134:137], v[120:123], v[30:33]\n"
      "v_cvt_pk_bf16_f32 v186, v186, v187\n"
      "v_cvt_pk_bf16_f32 v187, v188, v189\n"
      "v_mfma_f32_16x16x32_bf16 v[206:209], v[134:137], v[96:99], v[30:33]\n"
      "ds_read_b128 v[150:153], v2 offset:28160\n"
      "v_cvt_pk_bf16_f32 v188, v190, v191\n"
      "v_mfma_f32_16x16x32_bf16 v[96:99], v[134:137], v[100:103], v[30:33]\n"
      "ds_read_b128 v[134:137], v2 offset:31232\n"
      "s_waitcnt lgkmcnt(2)\n"
      "v_mfma_f32_16x16x16_bf16 v[210:213], v[106:107], v[146:147], v[210:213]\n"
      "ds_read_b128 v[100:103], v2 offset:34304\n"
      "v_mul_f32 v223, v174, v223\n"
      "v_mfma_f32_16x16x16_bf16 v[176:179], v[106:107], v[154:155], v[176:179]\n"
      "v_exp_f32 v115, v115\n"
      "v_exp_f32 v193, v193\n"
      "v_mfma_f32_16x16x16_bf16 v[206:209], v[106:107], v[202:203], v[206:209]\n"
      "v_exp_f32 v104, v104\n"
      "v_exp_f32 v114, v114\n"
      "v_exp_f32_e64 v192, v192\n"
      "v_pk_mul_f32 v[220:221], v[220:221], v[174:175] op_sel_hi:[1,0]\n"
      "v_pk_mul_f32 v[138:139], v[138:139], v[174:175] op_sel_hi:[1,0]\n"
      "v_pk_mul_f32 v[140:141], v[140:141], v[174:175] op_sel_hi:[1,0]\n"
      "v_pk_mul_f32 v[156:157], v[156:157], v[174:175] op_sel_hi:[1,0]\n"
      "v_mfma_f32_16x16x16_bf16 v[96:99], v[106:107], v[204:205], v[96:99]\n"
      "v_cndmask_b32 v210, v210, v176, s[58:59]\n"
      "v_lshlrev_b32 v39, 16, v39\n"
      "s_waitcnt lgkmcnt(1)\n"
      "v_mfma_f32_16x16x32_bf16 v[126:129], a[228:231], v[150:153], v[126:129]\n"
      "v_cndmask_b32 v210, v210, v206, s[60:61]\n"
      "v_mul_f32 v222, v174, v222\n"
      "v_mfma_f32_16x16x32_bf16 v[126:129], a[232:235], v[134:137], v[126:129]\n"
      "s_waitcnt lgkmcnt(0)\n"
      "v_cvt_pk_bf16_f32 v189, v192, v193\n"
      "v_cndmask_b32 v210, v210, v96, s[62:63]\n"
      "v_mfma_f32_16x16x32_bf16 v[126:129], a[236:239], v[100:103], v[126:129]\n"
      "v_fmac_f32_e64 v210, s53, v39\n"
      "v_cvt_pk_bf16_f32 v89, v114, v115\n"
      "v_mfma_f32_16x16x32_bf16 v[116:119], a[252:255], v[150:153], v[116:119]\n"
      "v_max_f32_dpp v193, v210, v210 row_ror:8 row_mask:0xf bank_mask:0xf\n"
      "v_cvt_pk_bf16_f32 v220, v220, v221\n"
      "v_mfma_f32_16x16x32_bf16 v[116:119], v[224:227], v[134:137], v[116:119]\n"
      "v_cvt_pk_bf16_f32 v221, v222, v223\n"
      "v_cvt_pk_bf16_f32 v90, v104, 0\n"
      "v_mfma_f32_16x16x32_bf16 v[116:119], v[228:231], v[100:103], v[116:119]\n"
      "v_max_f32_dpp v193, v193, v193 row_ror:4 row_mask:0xf bank_mask:0xf\n"
      "v_cvt_pk_bf16_f32 v222, v138, v139\n"
      "v_mfma_f32_16x16x32_bf16 v[108:111], v[244:247], v[150:153], v[108:111]\n"
      "v_fma_f32 v138, v126, v126, s52\n"
      "v_max_f32_dpp v193, v193, v193 row_ror:2 row_mask:0xf bank_mask:0xf\n"
      "v_mfma_f32_16x16x32_bf16 v[108:111], v[248:251], v[134:137], v[108:111]\n"
      "v_cvt_pk_bf16_f32 v223, v140, v141\n"
      "v_max_f32_dpp v193, v193, v193 row_ror:1 row_mask:0xf bank_mask:0xf\n"
      "v_mfma_f32_16x16x32_bf16 v[108:111], v[252:255], v[100:103], v[108:111]\n"
      "v_permlane16_swap_b32_e64 v148, v160\n"
      "v_mfma_f32_16x16x32_bf16 v[134:137], v[32:35], v[186:189], 0\n"
      "ds_write_b128 v3, v[220:223] offset:256\n"
      "v_mfma_f32_16x16x32_bf16 v[134:137], v[32:35], v[88:91], v[134:137]\n"
      "v_max_f32_dpp v193, v193, v193 row_bcast:15 row_mask:0xa bank_mask:0xf\n"
      "v_mul_f32 v139, v127, v127\n"
      "v_mfma_f32_16x16x32_bf16 v[100:103], v[130:133], v[186:189], 0\n"
      "v_add_f32 v148, v148, v160\n"
      "v_max_f32_dpp v193, v193, v193 row_bcast:31 row_mask:0xc bank_mask:0xf\n"
      "v_mfma_f32_16x16x32_bf16 v[100:103], v[76:79], v[88:91], v[100:103]\n"
      "v_readlane_b32 s72, v193, 63\n"
      "v_cvt_pk_bf16_f32 v156, v156, v157\n"
      "v_mfma_f32_16x16x32_bf16 v[190:193], v[194:197], v[186:189], 0\n"
      "v_rcp_f32 v134, v134\n"
      "v_mfma_f32_16x16x32_bf16 v[190:193], v[80:83], v[88:91], v[190:193]\n"
      "ds_write_b64 v4, v[218:219]\n"
      "v_subrev_f32 v210, s72, v210\n"
      "v_exp_f32_e64 v210, v210\n"
      "v_pk_fma_f32 v[138:139], v[128:129], v[128:129], v[138:139]\n"
      "v_pk_mul_f32 v[100:101], v[100:101], v[134:135] op_sel_hi:[1,0]\n"
      "v_pk_fma_f32 v[138:139], v[116:117], v[116:117], v[138:139]\n"
      "v_pk_mul_f32 v[102:103], v[102:103], v[134:135] op_sel_hi:[1,0]\n"
      "v_cvt_pk_bf16_f32 v210, v210, v210\n"
      "v_pk_mul_f32 v[190:191], v[190:191], v[134:135] op_sel_hi:[1,0]\n"
      "v_pk_mul_f32 v[192:193], v[192:193], v[134:135] op_sel_hi:[1,0]\n"
      "v_mfma_f32_16x16x32_bf16 v[218:221], v[214:217], v[186:189], 0\n"
      "ds_write_b16 v13, v210\n"
      "v_cvt_pk_bf16_f32 v100, v100, v101\n"
      "v_cvt_pk_bf16_f32 v101, v102, v103\n"
      "v_cvt_pk_bf16_f32 v102, v190, v191\n"
      "v_pk_fma_f32 v[138:139], v[118:119], v[118:119], v[138:139]\n"
      "v_cvt_pk_bf16_f32 v103, v192, v193\n"
      "v_mfma_f32_16x16x32_bf16 v[218:221], v[84:87], v[88:91], v[218:221]\n"
      "ds_write_b128 v3, v[100:103] offset:512\n"
      "v_pk_fma_f32 v[138:139], v[108:109], v[108:109], v[138:139]\n"
      "v_pk_fma_f32 v[138:139], v[110:111], v[110:111], v[138:139]\n"
      "v_pk_mul_f32 v[158:159], v[158:159], v[174:175] op_sel_hi:[1,0]\n"
      "v_add_f32 v138, v138, v139\n"
      "v_mov_b32 v160, v138\n"
      "v_cvt_pk_bf16_f32 v157, v158, v159\n"
      "ds_read_b128 v[174:177], v14\n"
      "ds_write_b64 v4, v[156:157] offset:256\n"
      "v_pk_mul_f32 v[218:219], v[218:219], v[134:135] op_sel_hi:[1,0]\n"
      "v_permlane16_swap_b32 v138, v160\n"
      "v_add_f32 v138, v138, v160\n"
      "v_pk_mul_f32 v[220:221], v[220:221], v[134:135] op_sel_hi:[1,0]\n"
      "v_cvt_pk_bf16_f32 v218, v218, v219\n"
      "v_cvt_pk_bf16_f32 v219, v220, v221\n"
      "ds_write_b64 v4, v[218:219] offset:512\n"
      "ds_read_b128 v[210:213], v14 offset:64\n"
      "v_permlane32_swap_b32 v148, v138\n"
      "v_add_f32 v148, v148, v138\n"
      "s_and_b32 s86, s86, -4\n"
      "ds_write_b32 v31, v148\n"
      "s_waitcnt lgkmcnt(4)\n"
      "v_mfma_f32_16x16x32_bf16 v[188:191], v[174:177], v[32:35], 0\n"
      "s_waitcnt lgkmcnt(1)\n"
      "v_mfma_f32_16x16x32_bf16 v[188:191], v[210:213], v[32:35], v[188:191]\n"
      "s_waitcnt lgkmcnt(0)\n"
      "s_barrier\n"
      "s_cbranch_vccz L_sel1_%=\n"
      "s_branch L_disp1_%=\n"
      "L_tail1_%=:\n"
      "s_nop 4\n"
      "v_mfma_f32_16x16x32_bf16 v[218:221], v[174:177], v[130:133], 0\n"
      "ds_read_b128 v[130:133], v9\n"
      "v_subrev_u32 v90, 115456, v9\n"
      "v_and_b32 v134, 0x300, v90\n"
      "v_mfma_f32_16x16x32_bf16 v[218:221], v[210:213], v[76:79], v[218:221]\n"
      "ds_read_b128 v[138:141], v2 offset:6144\n"
      "v_add_u32_e64 v90, v90, v134\n"
      "v_add_u32 v90, 149504, v90\n"
      "v_mfma_f32_16x16x32_bf16 v[134:137], v[174:177], v[194:197], 0\n"
      "ds_read_b128 v[194:197], v90\n"
      "ds_read_b128 v[204:207], v90 offset:2048\n"
      "v_mfma_f32_16x16x32_bf16 v[134:137], v[210:213], v[80:83], v[134:137]\n"
      "ds_read_b128 v[104:107], v90 offset:6144\n"
      "ds_read_b128 v[220:223], v2 offset:12288\n"
      "ds_read_b128 v[96:99], v90 offset:4096\n"
      "v_mfma_f32_16x16x32_bf16 v[100:103], v[174:177], v[214:217], 0\n"
      "ds_read_b128 v[214:217], v2 offset:3072\n"
      "ds_read_b128 v[174:177], v90 offset:8192\n"
      "v_mfma_f32_16x16x32_bf16 v[100:103], v[210:213], v[84:87], v[100:103]\n"
      "s_waitcnt lgkmcnt(8)\n"
      "v_add_f32 v132, v132, v133\n"
      "v_add_f32 v130, v130, v131\n"
      "v_add_f32 v130, v130, v132\n"
      "v_cndmask_b32 v218, v218, v134, s[58:59]\n"
      "v_rsq_f32_e64 v130, v130\n"
      "s_waitcnt lgkmcnt(6)\n"
      "v_mfma_f32_16x16x32_bf16 v[210:213], a[216:219], v[194:197], 0\n"
      "ds_read_b128 v[134:137], v2\n"
      "s_waitcnt lgkmcnt(6)\n"
      "v_mfma_f32_16x16x32_bf16 v[210:213], a[220:223], v[204:207], v[210:213]\n"
      "ds_read_b128 v[150:153], v2 offset:9216\n"
      "v_mul_f32 v130, 0x415db3d7, v130\n"
      "v_mfma_f32_16x16x32_bf16 v[112:115], a[240:243], v[194:197], 0\n"
      "ds_read_b128 v[158:161], v90 offset:10240\n"
      "v_mov_b32 v132, v130\n"
      "v_mfma_f32_16x16x32_bf16 v[112:115], a[244:247], v[204:207], v[112:115]\n"
      "s_nop 0\n"
      "v_permlane32_swap_b32_e64 v130, v132\n"
      "v_mfma_f32_16x16x32_bf16 v[154:157], v[232:235], v[194:197], 0\n"
      "s_waitcnt lgkmcnt(5)\n"
      "v_mul_f32 v109, v132, v109\n"
      "v_mov_b32 v131, v130\n"
      "v_mfma_f32_16x16x32_bf16 v[154:157], v[236:239], v[204:207], v[154:157]\n"
      "s_nop 0\n"
      "v_permlane16_swap_b32 v130, v131\n"
      "v_mul_f32 v142, v130, v142\n"
      "v_mul_f32 v108, v132, v108\n"
      "v_mul_f32 v143, v130, v143\n"
      "v_pk_mul_f32 v[198:199], v[198:199], v[130:131] op_sel_hi:[1,0]\n"
      "v_pk_mul_f32 v[162:163], v[162:163], v[130:131] op_sel:[0,1]\n"
      "v_pk_mul_f32 v[200:201], v[200:201], v[130:131] op_sel_hi:[1,0]\n"
      "v_pk_mul_f32 v[164:165], v[164:165], v[130:131] op_sel:[0,1]\n"
      "v_pk_mul_f32 v[166:167], v[166:167], v[130:131] op_sel_hi:[1,0]\n"
      "v_pk_mul_f32 v[180:181], v[180:181], v[130:131] op_sel:[0,1]\n"
      "v_pk_mul_f32 v[170:171], v[170:171], v[130:131] op_sel:[0,1]\n"
      "v_pk_mul_f32 v[118:119], v[118:119], v[132:133] op_sel_hi:[1,0]\n"
      "v_pk_mul_f32 v[128:129], v[128:129], v[132:133] op_sel_hi:[1,0]\n"
      "v_pk_mul_f32 v[144:145], v[144:145], v[130:131] op_sel_hi:[1,0]\n"
      "v_pk_mul_f32 v[172:173], v[172:173], v[130:131] op_sel:[0,1]\n"
      "v_pk_mul_f32 v[110:111], v[110:111], v[132:133] op_sel_hi:[1,0]\n"
      "v_cvt_pk_bf16_f32 v162, v162, v163\n"
      "v_cvt_pk_bf16_f32 v166, v166, v167\n"
      "v_pk_mul_f32 v[126:127], v[126:127], v[132:133] op_sel_hi:[1,0]\n"
      "v_pk_mul_f32 v[168:169], v[168:169], v[130:131] op_sel_hi:[1,0]\n"
      "v_cvt_pk_bf16_f32 v142, v142, v143\n"
      "v_pk_mul_f32 v[182:183], v[182:183], v[130:131] op_sel:[0,1]\n"
      "v_cvt_pk_bf16_f32 v163, v164, v165\n"
      "v_pk_mul_f32 v[116:117], v[116:117], v[132:133] op_sel_hi:[1,0]\n"
      "v_mfma_f32_16x16x32_bf16 v[210:213], a[224:227], v[96:99], v[210:213]\n"
      "v_cvt_pk_bf16_f32 v167, v168, v169\n"
      "v_cvt_pk_bf16_f32 v169, v200, v201\n"
      "v_mfma_f32_16x16x32_bf16 v[210:213], a[228:231], v[104:107], v[210:213]\n"
      "v_cvt_pk_bf16_f32 v126, v126, v127\n"
      "v_cvt_pk_bf16_f32 v108, v108, v109\n"
      "v_mfma_f32_16x16x32_bf16 v[112:115], a[248:251], v[96:99], v[112:115]\n"
      "v_cvt_pk_bf16_f32 v170, v170, v171\n"
      "v_cvt_pk_bf16_f32 v168, v198, v199\n"
      "v_mfma_f32_16x16x32_bf16 v[112:115], a[252:255], v[104:107], v[112:115]\n"
      "v_cvt_pk_bf16_f32 v127, v128, v129\n"
      "v_cvt_pk_bf16_f32 v164, v180, v181\n"
      "v_mfma_f32_16x16x32_bf16 v[154:157], v[240:243], v[96:99], v[154:157]\n"
      "s_waitcnt lgkmcnt(0)\n"
      "v_cvt_pk_bf16_f32 v129, v118, v119\n"
      "v_cvt_pk_bf16_f32 v143, v144, v145\n"
      "v_mfma_f32_16x16x32_bf16 v[154:157], v[244:247], v[104:107], v[154:157]\n"
      "ds_write_b128 v5, v[166:169] offset:18432\n"
      "v_mfma_f32_16x16x32_bf16 v[210:213], a[232:235], v[174:177], v[210:213]\n"
      "s_mul_i32 s92, s40, 2048\n"
      "v_cvt_pk_bf16_f32 v109, v110, v111\n"
      "v_cvt_pk_bf16_f32 v165, v182, v183\n"
      "v_cvt_pk_bf16_f32 v171, v172, v173\n"
      "v_mfma_f32_16x16x32_bf16 v[210:213], a[236:239], v[158:161], v[210:213]\n"
      "ds_write_b64 v6, v[170:171] offset:24576\n"
      "v_mfma_f32_16x16x32_bf16 v[112:115], v[224:227], v[174:177], v[112:115]\n"
      "ds_write_b128 v5, v[162:165] offset:24576\n"
      "v_cvt_pk_bf16_f32 v128, v116, v117\n"
      "v_mfma_f32_16x16x32_bf16 v[112:115], v[228:231], v[158:161], v[112:115]\n"
      "ds_write_b128 v5, v[126:129] offset:30720\n"
      "v_mfma_f32_16x16x32_bf16 v[154:157], v[248:251], v[174:177], v[154:157]\n"
      "ds_write_b64 v6, v[108:109] offset:30720\n"
      "v_mfma_f32_16x16x32_bf16 v[154:157], v[252:255], v[158:161], v[154:157]\n"
      "ds_write_b64 v6, v[142:143] offset:18432\n"
      "v_mfma_f32_16x16x32_bf16 v[166:169], a[216:219], v[134:137], 0\n"
      "ds_read_b128 v[126:129], v2 offset:9472\n"
      "v_subrev_u32 v132, 115456, v9\n"
      "v_mfma_f32_16x16x32_bf16 v[166:169], a[220:223], v[214:217], v[166:169]\n"
      "ds_read_b128 v[116:119], v7\n"
      "v_add_u32_e64 v84, s92, v132\n"
      "v_mfma_f32_16x16x32_bf16 v[198:201], a[240:243], v[134:137], 0\n"
      "ds_read_b128 v[162:165], v2 offset:15360\n"
      "v_mfma_f32_16x16x32_bf16 v[198:201], a[244:247], v[214:217], v[198:201]\n"
      "ds_read_b128 v[108:111], v2 offset:256\n"
      "v_fma_f32 v80, v210, v210, s52\n"
      "v_mfma_f32_16x16x32_bf16 v[142:145], v[232:235], v[134:137], 0\n"
      "v_ashrrev_i32 v85, 31, v37\n"
      "v_mov_b32_dpp v188, v188 quad_perm:[0,0,0,0] row_mask:0xf bank_mask:0xf\n"
      "v_mfma_f32_16x16x32_bf16 v[142:145], v[236:239], v[214:217], v[142:145]\n"
      "ds_read_b128 v[214:217], v2 offset:3328\n"
      "v_cndmask_b32 v218, v218, v100, s[60:61]\n"
      "v_mfma_f32_16x16x32_bf16 v[166:169], a[224:227], v[138:141], v[166:169]\n"
      "v_and_b32 v82, 0x3fffffff, v37\n"
      "v_subrev_u32 v86, 73728, v5\n"
      "v_mfma_f32_16x16x32_bf16 v[166:169], a[228:231], v[150:153], v[166:169]\n"
      "v_rcp_f32 v188, v188\n"
      "v_mfma_f32_16x16x32_bf16 v[198:201], a[248:251], v[138:141], v[198:201]\n"
      "ds_read_b128 v[174:177], v2 offset:6656\n"
      "v_mul_f32 v218, v188, v218\n"
      "v_mfma_f32_16x16x32_bf16 v[198:201], a[252:255], v[150:153], v[198:201]\n"
      "v_lshrrev_b32 v132, 4, v84\n"
      "v_mul_f32 v81, v211, v211\n"
      "v_mfma_f32_16x16x32_bf16 v[142:145], v[240:243], v[138:141], v[142:145]\n"
      "v_add_u32_e64 v138, 0, v132\n"
      "v_mul_u32_u24 v77, 2731, v138\n"
      "v_mfma_f32_16x16x32_bf16 v[142:145], v[244:247], v[150:153], v[142:145]\n"
      "ds_read_b128 v[188:191], v7 offset:8192\n"
      "v_lshrrev_b32 v77, 16, v77\n"
      "v_mfma_f32_16x16x32_bf16 v[166:169], a[232:235], v[220:223], v[166:169]\n"
      "s_waitcnt lgkmcnt(4)\n"
      "v_add_u32 v153, 64, v132\n"
      "v_cvt_pk_bf16_f32 v218, v218, v218\n"
      "v_mfma_f32_16x16x32_bf16 v[166:169], a[236:239], v[162:165], v[166:169]\n"
      "v_mul_u32_u24 v78, 2731, v153\n"
      "v_fmac_f32 v80, v212, v212\n"
      "v_mfma_f32_16x16x32_bf16 v[198:201], v[224:227], v[220:223], v[198:201]\n"
      "ds_read_b128 v[134:137], v2 offset:12544\n"
      "v_and_b32 v85, 100270464, v85\n"
      "v_mfma_f32_16x16x32_bf16 v[198:201], v[228:231], v[162:165], v[198:201]\n"
      "ds_read_b128 v[130:133], v2 offset:6400\n"
      "v_fmac_f32 v81, v213, v213\n"
      "v_mfma_f32_16x16x32_bf16 v[142:145], v[248:251], v[220:223], v[142:145]\n"
      "ds_read_b128 v[194:197], v2 offset:3584\n"
      "v_mul_f32 v151, v167, v167\n"
      "v_mfma_f32_16x16x32_bf16 v[142:145], v[252:255], v[162:165], v[142:145]\n"
      "s_waitcnt lgkmcnt(3)\n"
      "v_add_u32 v82, 1179648, v82\n"
      "v_sub_u32 v82, v82, v85\n"
      "v_mfma_f32_16x16x32_bf16 v[162:165], a[216:219], v[108:111], 0\n"
      "v_lshrrev_b32_e64 v78, 16, v78\n"
      "v_fma_f32 v150, v166, v166, s52\n"
      "v_mfma_f32_16x16x32_bf16 v[162:165], a[220:223], v[214:217], v[162:165]\n"
      "ds_write_b16 v12, v218\n"
      "v_mfma_f32_16x16x32_bf16 v[180:183], a[240:243], v[108:111], 0\n"
      "ds_read_b128 v[218:221], v2 offset:15616\n"
      "v_fmac_f32_e64 v80, v112, v112\n"
      "v_mfma_f32_16x16x32_bf16 v[180:183], a[244:247], v[214:217], v[180:183]\n"
      "ds_read_b128 v[100:103], v2 offset:9728\n"
      "v_mfma_f32_16x16x32_bf16 v[170:173], v[232:235], v[108:111], 0\n"
      "s_waitcnt lgkmcnt(0)\n"
      "s_barrier\n"
      "v_fmac_f32_e64 v150, v168, v168\n"
      "v_mfma_f32_16x16x32_bf16 v[170:173], v[236:239], v[214:217], v[170:173]\n"
      "s_waitcnt lgkmcnt(3)\n"
      "v_bfe_i32 v85, v37, 30, 1\n"
      "v_fmac_f32 v81, v113, v113\n"
      "v_mfma_f32_16x16x32_bf16 v[162:165], a[224:227], v[130:133], v[162:165]\n"
      "buffer_store_dwordx4 v[116:119], v20, s[24:27], s85 offen sc1\n"
      "v_mfma_f32_16x16x32_bf16 v[162:165], a[228:231], v[126:129], v[162:165]\n"
      "ds_read_b128 v[214:217], v2 offset:12800\n"
      "v_mul_u32_u24_e64 v76, 24, v78\n"
      "v_mfma_f32_16x16x32_bf16 v[180:183], a[248:251], v[130:133], v[180:183]\n"
      "ds_read_b128 v[206:209], v2 offset:512\n"
      "v_sub_u32 v76, v153, v76\n"
      "v_mfma_f32_16x16x32_bf16 v[180:183], a[252:255], v[126:129], v[180:183]\n"
      "v_fmac_f32 v151, v169, v169\n"
      "v_subrev_u32 v158, 73728, v6\n"
      "v_mfma_f32_16x16x32_bf16 v[170:173], v[240:243], v[130:133], v[170:173]\n"
      "s_waitcnt lgkmcnt(2)\n"
      "v_fmac_f32 v150, v198, v198\n"
      "v_fmac_f32_e64 v151, v199, v199\n"
      "v_mfma_f32_16x16x32_bf16 v[170:173], v[244:247], v[126:129], v[170:173]\n"
      "ds_read_b128 v[116:119], v7 offset:4096\n"
      "v_and_b32 v85, 196224, v85\n"
      "v_mfma_f32_16x16x32_bf16 v[162:165], a[232:235], v[134:137], v[162:165]\n"
      "ds_read_b128 v[130:133], v90 offset:4352\n"
      "v_fmac_f32_e64 v81, v115, v115\n"
      "v_mfma_f32_16x16x32_bf16 v[162:165], a[236:239], v[218:221], v[162:165]\n"
      "ds_read_b128 v[104:107], v90 offset:256\n"
      "v_mfma_f32_16x16x32_bf16 v[180:183], v[224:227], v[134:137], v[180:183]\n"
      "v_and_b32 v153, 7, v78\n"
      "v_mfma_f32_16x16x32_bf16 v[180:183], v[228:231], v[218:221], v[180:183]\n"
      "v_mul_u32_u24 v160, 24, v77\n"
      "v_mfma_f32_16x16x32_bf16 v[170:173], v[248:251], v[134:137], v[170:173]\n"
      "v_fmac_f32 v80, v114, v114\n"
      "v_fmac_f32 v80, v154, v154\n"
      "v_mfma_f32_16x16x32_bf16 v[170:173], v[252:255], v[218:221], v[170:173]\n"
      "s_waitcnt lgkmcnt(2)\n"
      "v_fmac_f32 v81, v155, v155\n"
      "v_mul_f32_e64 v223, v163, v163\n"
      "v_mfma_f32_16x16x32_bf16 v[126:129], a[216:219], v[206:209], 0\n"
      "ds_read_b128 v[218:221], v7 offset:16384\n"
      "v_fmac_f32_e64 v150, v200, v200\n"
      "v_mfma_f32_16x16x32_bf16 v[126:129], a[220:223], v[194:197], v[126:129]\n"
      "buffer_store_dwordx4 v[116:119], v21, s[24:27], s85 offen sc1\n"
      "s_nop 1\n"
      "v_mfma_f32_16x16x32_bf16 v[116:119], a[240:243], v[206:209], 0\n"
      "v_fma_f32 v222, v162, v162, s52\n"
      "v_fmac_f32 v223, v165, v165\n"
      "v_mfma_f32_16x16x32_bf16 v[116:119], a[244:247], v[194:197], v[116:119]\n"
      "v_xor_b32 v76, v76, v153\n"
      "v_mfma_f32_16x16x32_bf16 v[108:111], v[232:235], v[206:209], 0\n"
      "v_fmac_f32 v222, v164, v164\n"
      "v_sub_u32_e64 v160, v138, v160\n"
      "v_mfma_f32_16x16x32_bf16 v[108:111], v[236:239], v[194:197], v[108:111]\n"
      "ds_read_b128 v[194:197], v2 offset:15872\n"
      "v_fmac_f32 v80, v156, v156\n"
      "v_mfma_f32_16x16x32_bf16 v[126:129], a[224:227], v[174:177], v[126:129]\n"
      "v_fmac_f32 v81, v157, v157\n"
      "v_fmac_f32 v151, v201, v201\n"
      "v_mfma_f32_16x16x32_bf16 v[126:129], a[228:231], v[100:103], v[126:129]\n"
      "ds_read_b128 v[134:137], v90 offset:6400\n"
      "v_mfma_f32_16x16x32_bf16 v[116:119], a[248:251], v[174:177], v[116:119]\n"
      "v_add_f32 v80, v80, v81\n"
      "v_sub_u32 v82, v82, v85\n"
      "v_mfma_f32_16x16x32_bf16 v[116:119], a[252:255], v[100:103], v[116:119]\n"
      "ds_read_b128 v[206:209], v90 offset:2304\n"
      "v_and_b32 v138, 7, v77\n"
      "v_mfma_f32_16x16x32_bf16 v[108:111], v[240:243], v[174:177], v[108:111]\n"
      "ds_read_b128 v[174:177], v90 offset:10496\n"
      "v_mfma_f32_16x16x32_bf16 v[108:111], v[244:247], v[100:103], v[108:111]\n"
      "s_waitcnt lgkmcnt(3)\n"
      "v_fmac_f32 v222, v180, v180\n"
      "v_fmac_f32_e64 v223, v181, v181\n"
      "v_mfma_f32_16x16x32_bf16 v[126:129], a[232:235], v[214:217], v[126:129]\n"
      "buffer_store_dwordx4 v[188:191], v22, s[24:27], s85 offen sc1\n"
      "v_mfma_f32_16x16x32_bf16 v[126:129], a[236:239], v[194:197], v[126:129]\n"
      "v_fmac_f32 v222, v182, v182\n"
      "v_fmac_f32 v151, v143, v143\n"
      "v_mfma_f32_16x16x32_bf16 v[116:119], v[224:227], v[214:217], v[116:119]\n"
      "ds_read_b128 v[188:191], v7 offset:12288\n"
      "v_mfma_f32_16x16x32_bf16 v[116:119], v[228:231], v[194:197], v[116:119]\n"
      "ds_read_b128 v[100:103], v90 offset:8448\n"
      "v_xor_b32 v160, v160, v138\n"
      "v_mfma_f32_16x16x32_bf16 v[108:111], v[248:251], v[214:217], v[108:111]\n"
      "v_mul_f32 v153, v127, v127\n"
      "v_fmac_f32 v223, v183, v183\n"
      "v_mfma_f32_16x16x32_bf16 v[108:111], v[252:255], v[194:197], v[108:111]\n"
      "v_fmac_f32 v150, v142, v142\n"
      "v_fma_f32 v152, v126, v126, s52\n"
      "v_mfma_f32_16x16x32_bf16 v[194:197], a[216:219], v[104:107], 0\n"
      "s_waitcnt lgkmcnt(2)\n"
      "v_fmac_f32 v152, v128, v128\n"
      "v_fmac_f32_e64 v153, v129, v129\n"
      "v_mfma_f32_16x16x32_bf16 v[194:197], a[220:223], v[206:209], v[194:197]\n"
      "v_fmac_f32 v223, v171, v171\n"
      "v_fmac_f32 v151, v145, v145\n"
      "v_mfma_f32_16x16x32_bf16 v[214:217], a[240:243], v[104:107], 0\n"
      "v_fmac_f32_e64 v222, v170, v170\n"
      "v_lshl_add_u32 v77, v77, 2, 12\n"
      "v_mfma_f32_16x16x32_bf16 v[214:217], a[244:247], v[206:209], v[214:217]\n"
      "v_fmac_f32_e64 v152, v116, v116\n"
      "v_mfma_f32_16x16x32_bf16 v[138:141], v[232:235], v[104:107], 0\n"
      "ds_bpermute_b32 v85, v77, v82\n"
      "v_fmac_f32 v222, v172, v172\n"
      "v_mfma_f32_16x16x32_bf16 v[138:141], v[236:239], v[206:209], v[138:141]\n"
      "v_lshl_add_u32 v78, v78, 2, 12\n"
      "v_fmac_f32 v153, v117, v117\n"
      "v_mfma_f32_16x16x32_bf16 v[194:197], a[224:227], v[130:133], v[194:197]\n"
      "v_fmac_f32 v223, v173, v173\n"
      "v_fmac_f32 v152, v118, v118\n"
      "v_mfma_f32_16x16x32_bf16 v[194:197], a[228:231], v[134:137], v[194:197]\n"
      "v_fmac_f32 v153, v119, v119\n"
      "v_fmac_f32 v150, v144, v144\n"
      "v_mfma_f32_16x16x32_bf16 v[214:217], a[248:251], v[130:133], v[214:217]\n"
      "ds_bpermute_b32 v77, v78, v82\n"
      "v_mfma_f32_16x16x32_bf16 v[214:217], a[252:255], v[134:137], v[214:217]\n"
      "v_add_f32 v222, v222, v223\n"
      "v_fmac_f32 v153, v109, v109\n"
      "v_mfma_f32_16x16x32_bf16 v[138:141], v[240:243], v[130:133], v[138:141]\n"
      "s_waitcnt lgkmcnt(1)\n"
      "v_fmac_f32 v152, v108, v108\n"
      "v_fmac_f32_e64 v152, v110, v110\n"
      "v_mfma_f32_16x16x32_bf16 v[138:141], v[244:247], v[134:137], v[138:141]\n"
      "buffer_store_dwordx4 v[188:191], v23, s[24:27], s85 offen sc1\n"
      "v_mfma_f32_16x16x32_bf16 v[194:197], a[232:235], v[100:103], v[194:197]\n"
      "v_add_f32 v150, v150, v151\n"
      "v_fmac_f32 v153, v111, v111\n"
      "v_mfma_f32_16x16x32_bf16 v[194:197], a[236:239], v[174:177], v[194:197]\n"
      "v_add_f32 v152, v152, v153\n"
      "v_mfma_f32_16x16x32_bf16 v[214:217], v[224:227], v[100:103], v[214:217]\n"
      "v_permlane16_swap_b32 v150, v222\n"
      "v_mfma_f32_16x16x32_bf16 v[214:217], v[228:231], v[174:177], v[214:217]\n"
      "s_waitcnt lgkmcnt(0)\n"
      "v_add_f32 v150, v150, v222\n"
      "v_lshl_add_u32 v77, v76, 4, v77\n"
      "v_mfma_f32_16x16x32_bf16 v[138:141], v[248:251], v[100:103], v[138:141]\n"
      "v_fma_f32 v188, v194, v194, s52\n"
      "v_mov_b32_e64 v222, v152\n"
      "v_mfma_f32_16x16x32_bf16 v[138:141], v[252:255], v[174:177], v[138:141]\n"
      "s_nop 0\n"
      "v_permlane16_swap_b32 v152, v222\n"
      "v_add_f32 v152, v152, v222\n"
      "v_lshl_add_u32 v85, v160, 4, v85\n"
      "v_mul_f32 v189, v195, v195\n"
      "v_pk_fma_f32 v[188:189], v[196:197], v[196:197], v[188:189]\n"
      "v_pk_fma_f32 v[188:189], v[214:215], v[214:215], v[188:189]\n"
      "v_pk_fma_f32 v[188:189], v[216:217], v[216:217], v[188:189]\n"
      "v_pk_fma_f32 v[188:189], v[138:139], v[138:139], v[188:189]\n"
      "v_pk_fma_f32 v[188:189], v[140:141], v[140:141], v[188:189]\n"
      "v_add_f32 v188, v188, v189\n"
      "v_permlane32_swap_b32 v150, v152\n"
      "s_nop 0\n"
      "v_permlane16_swap_b32 v80, v188\n"
      "v_add_f32 v80, v80, v188\n"
      "v_mov_b32 v188, v80\n"
      "v_add_f32 v150, v150, v152\n"
      "ds_write_b32 v31, v150 offset:1024\n"
      "v_permlane32_swap_b32 v80, v188\n"
      "v_add_f32_e64 v80, v80, v188\n"
      "ds_write_b32 v31, v80 offset:4608\n"
      "buffer_store_dwordx4 v[218:221], v25, s[24:27], s85 offen sc1\n"
      "v_readlane_b32 s85, v37, s42\n"
      "s_add_u32 s42, s42, 1\n"
      "s_or_b32 s95, s85, s48\n"
      "s_cmp_ge_u32 s95, 0x40000000\n"
      "s_cbranch_scc1 L_rare_27_%=\n"
      "L_back_28_%=:\n"
      "s_waitcnt lgkmcnt(0)\n"
      "s_barrier\n"
      "ds_read_b128 v[218:221], v7 offset:18432\n"
      "ds_read_b128 v[174:177], v9 offset:4608\n"
      "ds_read_b128 v[188:191], v9 offset:1024\n"
      "ds_read_b128 v[130:133], v7 offset:30720\n"
      "ds_read_b128 v[134:137], v7 offset:26624\n"
      "ds_read_b128 v[100:103], v7 offset:34816\n"
      "ds_read_b128 v[104:107], v7 offset:22528\n"
      "s_waitcnt lgkmcnt(6)\n"
      "buffer_store_dwordx4 v[218:221], v20, s[24:27], s85 offen sc1\n"
      "s_waitcnt lgkmcnt(4)\n"
      "v_add_f32 v188, v188, v189\n"
      "v_add_f32 v190, v190, v191\n"
      "v_add_f32 v176, v176, v177\n"
      "v_add_f32 v188, v188, v190\n"
      "v_rsq_f32 v188, v188\n"
      "v_add_f32 v174, v174, v175\n"
      "v_mul_f32 v188, 0x415db3d7, v188\n"
      "v_add_f32 v174, v174, v176\n"
      "v_rsq_f32 v174, v174\n"
      "v_mov_b32_e64 v190, v188\n"
      "v_mul_f32 v174, 0x415db3d7, v174\n"
      "v_mov_b32 v176, v174\n"
      "v_permlane32_swap_b32 v188, v190\n"
      "v_mov_b32_e64 v189, v188\n"
      "v_permlane32_swap_b32_e64 v174, v176\n"
      "v_mov_b32_e64 v175, v174\n"
      "v_pk_mul_f32 v[118:119], v[118:119], v[190:191] op_sel_hi:[1,0]\n"
      "v_permlane16_swap_b32_e64 v188, v189\n"
      "v_pk_mul_f32 v[200:201], v[200:201], v[188:189] op_sel_hi:[1,0]\n"
      "v_permlane16_swap_b32_e64 v174, v175\n"
      "v_pk_mul_f32 v[126:127], v[126:127], v[190:191] op_sel_hi:[1,0]\n"
      "v_pk_mul_f32 v[214:215], v[214:215], v[174:175] op_sel:[0,1]\n"
      "v_pk_mul_f32 v[128:129], v[128:129], v[190:191] op_sel_hi:[1,0]\n"
      "v_pk_mul_f32 v[166:167], v[166:167], v[188:189] op_sel_hi:[1,0]\n"
      "v_pk_mul_f32 v[142:143], v[142:143], v[188:189] op_sel_hi:[1,0]\n"
      "v_pk_mul_f32 v[216:217], v[216:217], v[174:175] op_sel:[0,1]\n"
      "v_pk_mul_f32 v[164:165], v[164:165], v[188:189] op_sel:[0,1]\n"
      "v_pk_mul_f32 v[196:197], v[196:197], v[174:175] op_sel:[0,1]\n"
      "v_cvt_pk_bf16_f32 v126, v126, v127\n"
      "v_pk_mul_f32 v[194:195], v[194:195], v[174:175] op_sel:[0,1]\n"
      "v_pk_mul_f32 v[168:169], v[168:169], v[188:189] op_sel_hi:[1,0]\n"
      "v_pk_mul_f32 v[138:139], v[138:139], v[174:175] op_sel:[0,1]\n"
      "v_pk_mul_f32 v[162:163], v[162:163], v[188:189] op_sel:[0,1]\n"
      "v_pk_mul_f32 v[140:141], v[140:141], v[174:175] op_sel:[0,1]\n"
      "v_pk_mul_f32 v[156:157], v[156:157], v[174:175] op_sel_hi:[1,0]\n"
      "v_cvt_pk_bf16_f32 v166, v166, v167\n"
      "v_cvt_pk_bf16_f32 v194, v194, v195\n"
      "v_cvt_pk_bf16_f32 v195, v196, v197\n"
      "v_cvt_pk_bf16_f32 v162, v162, v163\n"
      "v_pk_mul_f32 v[114:115], v[114:115], v[174:175] op_sel_hi:[1,0]\n"
      "v_pk_mul_f32 v[198:199], v[198:199], v[188:189] op_sel_hi:[1,0]\n"
      "v_pk_mul_f32 v[144:145], v[144:145], v[188:189] op_sel_hi:[1,0]\n"
      "v_cvt_pk_bf16_f32 v127, v128, v129\n"
      "v_cvt_pk_bf16_f32 v167, v168, v169\n"
      "v_pk_mul_f32 v[110:111], v[110:111], v[190:191] op_sel_hi:[1,0]\n"
      "v_cvt_pk_bf16_f32 v163, v164, v165\n"
      "v_pk_mul_f32 v[182:183], v[182:183], v[188:189] op_sel:[0,1]\n"
      "v_cvt_pk_bf16_f32 v129, v118, v119\n"
      "v_pk_mul_f32 v[116:117], v[116:117], v[190:191] op_sel_hi:[1,0]\n"
      "v_pk_mul_f32 v[210:211], v[210:211], v[174:175] op_sel_hi:[1,0]\n"
      "v_cvt_pk_bf16_f32 v168, v198, v199\n"
      "v_pk_mul_f32 v[180:181], v[180:181], v[188:189] op_sel:[0,1]\n"
      "v_cvt_pk_bf16_f32 v138, v138, v139\n"
      "v_cvt_pk_bf16_f32 v169, v200, v201\n"
      "v_cvt_pk_bf16_f32 v128, v116, v117\n"
      "v_cvt_pk_bf16_f32 v196, v214, v215\n"
      "v_cvt_pk_bf16_f32 v164, v180, v181\n"
      "v_cvt_pk_bf16_f32 v197, v216, v217\n"
      "v_cvt_pk_bf16_f32 v165, v182, v183\n"
      "v_pk_mul_f32 v[108:109], v[108:109], v[190:191] op_sel_hi:[1,0]\n"
      "v_cvt_pk_bf16_f32 v139, v140, v141\n"
      "ds_write_b128 v5, v[162:165] offset:6144\n"
      "ds_write_b128 v5, v[126:129] offset:12288\n"
      "ds_write_b64 v158, v[138:139] offset:6144\n"
      "ds_write_b128 v86, v[194:197] offset:6144\n"
      "ds_write_b128 v5, v[166:169]\n"
      "v_cvt_pk_bf16_f32 v142, v142, v143\n"
      "v_cvt_pk_bf16_f32 v210, v210, v211\n"
      "v_cvt_pk_bf16_f32 v108, v108, v109\n"
      "v_pk_mul_f32 v[112:113], v[112:113], v[174:175] op_sel_hi:[1,0]\n"
      "v_pk_mul_f32 v[154:155], v[154:155], v[174:175] op_sel_hi:[1,0]\n"
      "v_pk_mul_f32 v[170:171], v[170:171], v[188:189] op_sel:[0,1]\n"
      "v_cvt_pk_bf16_f32 v170, v170, v171\n"
      "v_pk_mul_f32 v[212:213], v[212:213], v[174:175] op_sel_hi:[1,0]\n"
      "v_cvt_pk_bf16_f32 v211, v212, v213\n"
      "v_cvt_pk_bf16_f32 v143, v144, v145\n"
      "v_cvt_pk_bf16_f32 v109, v110, v111\n"
      "v_pk_mul_f32 v[172:173], v[172:173], v[188:189] op_sel:[0,1]\n"
      "v_cvt_pk_bf16_f32 v212, v112, v113\n"
      "v_cvt_pk_bf16_f32 v213, v114, v115\n"
      "v_cvt_pk_bf16_f32 v154, v154, v155\n"
      "v_cvt_pk_bf16_f32 v155, v156, v157\n"
      "v_cvt_pk_bf16_f32 v171, v172, v173\n"
      "s_waitcnt lgkmcnt(3)\n"
      "buffer_store_dwordx4 v[104:107], v21, s[24:27], s85 offen sc1\n"
      "ds_write_b128 v86, v[210:213]\n"
      "ds_write_b64 v6, v[108:109] offset:12288\n"
      "ds_write_b64 v158, v[154:155]\n"
      "ds_write_b64 v6, v[170:171] offset:6144\n"
      "ds_write_b64 v6, v[142:143]\n"
      "buffer_store_dwordx4 v[134:137], v22, s[24:27], s85 offen sc1\n"
      "buffer_store_dwordx4 v[130:133], v23, s[24:27], s85 offen sc1\n"
      "buffer_store_dwordx4 v[100:103], v25, s[24:27], s85 offen sc1\n"
      "v_readlane_b32 s85, v37, s42\n"
      "s_add_u32 s42, s42, 1\n"
      "s_or_b32 s95, s85, s48\n"
      "s_cmp_ge_u32 s95, 0x40000000\n"
      "s_cbranch_scc1 L_rare_31_%=\n"
      "L_back_32_%=:\n"
      "s_waitcnt lgkmcnt(0)\n"
      "s_barrier\n"
      "ds_read_b128 v[130:133], v7\n"
      "ds_read_b128 v[170:173], v7 offset:4096\n"
      "ds_read_b128 v[210:213], v7 offset:8192\n"
      "ds_read_b128 v[142:145], v7 offset:12288\n"
      "ds_read_b128 v[108:111], v7 offset:16384\n"
      "ds_read_b128 v[166:169], v84\n"
      "ds_read_b128 v[194:197], v84 offset:1024\n"
      "s_waitcnt lgkmcnt(6)\n"
      "buffer_store_dwordx4 v[130:133], v20, s[24:27], s85 offen sc1\n"
      "s_waitcnt lgkmcnt(5)\n"
      "buffer_store_dwordx4 v[170:173], v21, s[24:27], s85 offen sc1\n"
      "s_waitcnt lgkmcnt(4)\n"
      "buffer_store_dwordx4 v[210:213], v22, s[24:27], s85 offen sc1\n"
      "s_waitcnt lgkmcnt(3)\n"
      "buffer_store_dwordx4 v[142:145], v23, s[24:27], s85 offen sc1\n"
      "s_waitcnt lgkmcnt(2)\n"
      "buffer_store_dwordx4 v[108:111], v25, s[24:27], s85 offen sc1\n"
      "s_waitcnt lgkmcnt(1)\n"
      "buffer_store_dwordx4 v[166:169], v85, s[24:27], 0 offen sc1\n"
      "s_waitcnt lgkmcnt(0)\n"
      "buffer_store_dwordx4 v[194:197], v77, s[24:27], 0 offen sc1\n"
      "s_endpgm\n"
      "L_tail0_%=:\n"
      "s_nop 4\n"
      "v_subrev_u32 v98, 115456, v9\n"
      "v_mfma_f32_16x16x32_bf16 v[218:221], v[174:177], v[130:133], 0\n"
      "ds_read_b128 v[112:115], v9 offset:1024\n"
      "v_and_b32 v135, 0x300, v98\n"
      "v_add_u32_e64 v98, v98, v135\n"
      "v_add_u32 v98, 149504, v98\n"
      "v_mfma_f32_16x16x32_bf16 v[218:221], v[210:213], v[76:79], v[218:221]\n"
      "ds_read_b128 v[138:141], v98\n"
      "v_mfma_f32_16x16x32_bf16 v[134:137], v[174:177], v[194:197], 0\n"
      "ds_read_b128 v[194:197], v98 offset:2048\n"
      "v_and_b32 v76, 0x3fffffff, v37\n"
      "v_mfma_f32_16x16x32_bf16 v[134:137], v[210:213], v[80:83], v[134:137]\n"
      "ds_read_b128 v[104:107], v2 offset:18432\n"
      "ds_read_b128 v[154:157], v98 offset:4096\n"
      "v_mfma_f32_16x16x32_bf16 v[100:103], v[174:177], v[214:217], 0\n"
      "ds_read_b128 v[214:217], v2 offset:24576\n"
      "ds_read_b128 v[174:177], v98 offset:6144\n"
      "v_mfma_f32_16x16x32_bf16 v[100:103], v[210:213], v[84:87], v[100:103]\n"
      "s_waitcnt lgkmcnt(6)\n"
      "v_add_f32 v112, v112, v113\n"
      "v_add_f32 v114, v114, v115\n"
      "v_add_f32 v112, v112, v114\n"
      "v_rsq_f32_e64 v112, v112\n"
      "s_waitcnt lgkmcnt(5)\n"
      "v_mfma_f32_16x16x32_bf16 v[210:213], a[216:219], v[138:141], 0\n"
      "ds_read_b128 v[130:133], v98 offset:8192\n"
      "s_waitcnt lgkmcnt(5)\n"
      "v_mfma_f32_16x16x32_bf16 v[210:213], a[220:223], v[194:197], v[210:213]\n"
      "ds_read_b128 v[150:153], v2 offset:21504\n"
      "v_mul_f32 v112, 0x415db3d7, v112\n"
      "v_mfma_f32_16x16x32_bf16 v[158:161], a[240:243], v[138:141], 0\n"
      "ds_read_b128 v[220:223], v98 offset:10240\n"
      "v_mov_b32 v114, v112\n"
      "v_mfma_f32_16x16x32_bf16 v[158:161], a[244:247], v[194:197], v[158:161]\n"
      "s_nop 0\n"
      "v_permlane32_swap_b32_e64 v112, v114\n"
      "v_mfma_f32_16x16x32_bf16 v[206:209], v[232:235], v[138:141], 0\n"
      "s_waitcnt lgkmcnt(4)\n"
      "v_mul_f32 v127, v114, v127\n"
      "v_mov_b32_e64 v113, v112\n"
      "v_mfma_f32_16x16x32_bf16 v[206:209], v[236:239], v[194:197], v[206:209]\n"
      "ds_read_b128 v[138:141], v2 offset:27648\n"
      "v_mul_f32 v126, v114, v126\n"
      "v_permlane16_swap_b32 v112, v113\n"
      "v_cvt_pk_bf16_f32 v126, v126, v127\n"
      "v_pk_mul_f32 v[128:129], v[128:129], v[114:115] op_sel_hi:[1,0]\n"
      "v_pk_mul_f32 v[168:169], v[168:169], v[112:113] op_sel_hi:[1,0]\n"
      "v_pk_mul_f32 v[116:117], v[116:117], v[114:115] op_sel_hi:[1,0]\n"
      "v_pk_mul_f32 v[172:173], v[172:173], v[112:113] op_sel:[0,1]\n"
      "v_pk_mul_f32 v[162:163], v[162:163], v[112:113] op_sel:[0,1]\n"
      "v_pk_mul_f32 v[182:183], v[182:183], v[112:113] op_sel:[0,1]\n"
      "v_pk_mul_f32 v[200:201], v[200:201], v[112:113] op_sel_hi:[1,0]\n"
      "v_pk_mul_f32 v[170:171], v[170:171], v[112:113] op_sel:[0,1]\n"
      "v_pk_mul_f32 v[180:181], v[180:181], v[112:113] op_sel:[0,1]\n"
      "v_pk_mul_f32 v[166:167], v[166:167], v[112:113] op_sel_hi:[1,0]\n"
      "v_pk_mul_f32 v[108:109], v[108:109], v[114:115] op_sel_hi:[1,0]\n"
      "v_pk_mul_f32 v[164:165], v[164:165], v[112:113] op_sel:[0,1]\n"
      "v_pk_mul_f32 v[110:111], v[110:111], v[114:115] op_sel_hi:[1,0]\n"
      "v_pk_mul_f32 v[118:119], v[118:119], v[114:115] op_sel_hi:[1,0]\n"
      "v_pk_mul_f32 v[142:143], v[142:143], v[112:113] op_sel_hi:[1,0]\n"
      "v_cvt_pk_bf16_f32 v142, v142, v143\n"
      "v_pk_mul_f32 v[144:145], v[144:145], v[112:113] op_sel_hi:[1,0]\n"
      "v_pk_mul_f32 v[198:199], v[198:199], v[112:113] op_sel_hi:[1,0]\n"
      "v_mfma_f32_16x16x32_bf16 v[210:213], a[224:227], v[154:157], v[210:213]\n"
      "s_waitcnt lgkmcnt(0)\n"
      "v_cvt_pk_bf16_f32 v162, v162, v163\n"
      "v_cvt_pk_bf16_f32 v166, v166, v167\n"
      "v_mfma_f32_16x16x32_bf16 v[210:213], a[228:231], v[174:177], v[210:213]\n"
      "s_mul_i32 s92, s40, 2048\n"
      "v_cvt_pk_bf16_f32 v127, v128, v129\n"
      "v_cvt_pk_bf16_f32 v143, v144, v145\n"
      "v_cvt_pk_bf16_f32 v170, v170, v171\n"
      "v_cvt_pk_bf16_f32 v167, v168, v169\n"
      "v_mfma_f32_16x16x32_bf16 v[158:161], a[248:251], v[154:157], v[158:161]\n"
      "ds_write_b64 v6, v[142:143]\n"
      "v_mfma_f32_16x16x32_bf16 v[158:161], a[252:255], v[174:177], v[158:161]\n"
      "v_cvt_pk_bf16_f32 v108, v108, v109\n"
      "v_cvt_pk_bf16_f32 v163, v164, v165\n"
      "v_cvt_pk_bf16_f32 v171, v172, v173\n"
      "v_mfma_f32_16x16x32_bf16 v[206:209], v[240:243], v[154:157], v[206:209]\n"
      "v_cvt_pk_bf16_f32 v109, v110, v111\n"
      "v_cvt_pk_bf16_f32 v128, v116, v117\n"
      "v_mfma_f32_16x16x32_bf16 v[206:209], v[244:247], v[174:177], v[206:209]\n"
      "v_cvt_pk_bf16_f32 v169, v200, v201\n"
      "v_cvt_pk_bf16_f32 v168, v198, v199\n"
      "v_mfma_f32_16x16x32_bf16 v[210:213], a[232:235], v[130:133], v[210:213]\n"
      "ds_write_b128 v5, v[166:169]\n"
      "v_mfma_f32_16x16x32_bf16 v[210:213], a[236:239], v[220:223], v[210:213]\n"
      "ds_write_b64 v6, v[170:171] offset:6144\n"
      "v_mfma_f32_16x16x32_bf16 v[158:161], v[224:227], v[130:133], v[158:161]\n"
      "v_cvt_pk_bf16_f32 v164, v180, v181\n"
      "v_cvt_pk_bf16_f32 v165, v182, v183\n"
      "v_mfma_f32_16x16x32_bf16 v[158:161], v[228:231], v[220:223], v[158:161]\n"
      "ds_write_b64 v6, v[108:109] offset:12288\n"
      "v_cvt_pk_bf16_f32 v129, v118, v119\n"
      "v_mfma_f32_16x16x32_bf16 v[206:209], v[248:251], v[130:133], v[206:209]\n"
      "ds_write_b128 v5, v[162:165] offset:6144\n"
      "v_mfma_f32_16x16x32_bf16 v[206:209], v[252:255], v[220:223], v[206:209]\n"
      "ds_write_b128 v5, v[126:129] offset:12288\n"
      "v_mfma_f32_16x16x32_bf16 v[166:169], a[216:219], v[104:107], 0\n"
      "ds_read_b128 v[162:165], v2 offset:30720\n"
      "v_ashrrev_i32 v156, 31, v37\n"
      "v_mfma_f32_16x16x32_bf16 v[166:169], a[220:223], v[150:153], v[166:169]\n"
      "ds_read_b128 v[180:183], v2 offset:33792\n"
      "v_mfma_f32_16x16x32_bf16 v[198:201], a[240:243], v[104:107], 0\n"
      "ds_read_b128 v[130:133], v7 offset:30720\n"
      "v_subrev_u32 v155, 115456, v9\n"
      "v_mfma_f32_16x16x32_bf16 v[198:201], a[244:247], v[150:153], v[198:201]\n"
      "ds_read_b128 v[220:223], v7 offset:26624\n"
      "v_fma_f32 v80, v210, v210, s52\n"
      "v_mfma_f32_16x16x32_bf16 v[142:145], v[232:235], v[104:107], 0\n"
      "ds_read_b128 v[126:129], v2 offset:21760\n"
      "v_cndmask_b32 v218, v218, v134, s[58:59]\n"
      "v_mfma_f32_16x16x32_bf16 v[142:145], v[236:239], v[150:153], v[142:145]\n"
      "ds_read_b128 v[116:119], v2 offset:18688\n"
      "v_add_u32_e64 v77, s92, v155\n"
      "v_mfma_f32_16x16x32_bf16 v[166:169], a[224:227], v[214:217], v[166:169]\n"
      "ds_read_b128 v[108:111], v2 offset:24832\n"
      "v_mfma_f32_16x16x32_bf16 v[166:169], a[228:231], v[138:141], v[166:169]\n"
      "v_mov_b32_dpp v188, v188 quad_perm:[0,0,0,0] row_mask:0xf bank_mask:0xf\n"
      "v_add_u32 v76, 1179648, v76\n"
      "v_mfma_f32_16x16x32_bf16 v[198:201], a[248:251], v[214:217], v[198:201]\n"
      "v_lshrrev_b32_e64 v155, 4, v77\n"
      "v_add_u32 v85, 64, v155\n"
      "v_mfma_f32_16x16x32_bf16 v[198:201], a[252:255], v[138:141], v[198:201]\n"
      "s_waitcnt lgkmcnt(6)\n"
      "v_cndmask_b32 v218, v218, v100, s[60:61]\n"
      "v_mfma_f32_16x16x32_bf16 v[142:145], v[240:243], v[214:217], v[142:145]\n"
      "ds_read_b128 v[194:197], v2 offset:27904\n"
      "v_mul_f32 v81, v211, v211\n"
      "v_mfma_f32_16x16x32_bf16 v[142:145], v[244:247], v[138:141], v[142:145]\n"
      "ds_read_b128 v[104:107], v2 offset:30976\n"
      "v_add_u32 v82, 0, v155\n"
      "v_mfma_f32_16x16x32_bf16 v[166:169], a[232:235], v[162:165], v[166:169]\n"
      "s_waitcnt lgkmcnt(5)\n"
      "v_rcp_f32 v188, v188\n"
      "v_mfma_f32_16x16x32_bf16 v[166:169], a[236:239], v[180:183], v[166:169]\n"
      "ds_read_b128 v[214:217], v2 offset:31232\n"
      "v_mul_u32_u24 v78, 2731, v82\n"
      "v_mfma_f32_16x16x32_bf16 v[198:201], v[224:227], v[162:165], v[198:201]\n"
      "ds_read_b128 v[138:141], v2 offset:18944\n"
      "v_mfma_f32_16x16x32_bf16 v[198:201], v[228:231], v[180:183], v[198:201]\n"
      "ds_read_b128 v[100:103], v2 offset:34304\n"
      "v_and_b32 v156, 100270464, v156\n"
      "v_mfma_f32_16x16x32_bf16 v[142:145], v[248:251], v[162:165], v[142:145]\n"
      "ds_read_b128 v[134:137], v2 offset:22016\n"
      "v_mul_f32 v205, v167, v167\n"
      "v_mfma_f32_16x16x32_bf16 v[142:145], v[252:255], v[180:183], v[142:145]\n"
      "s_waitcnt lgkmcnt(4)\n"
      "v_mul_u32_u24 v86, 2731, v85\n"
      "v_mfma_f32_16x16x32_bf16 v[162:165], a[216:219], v[116:119], 0\n"
      "ds_read_b128 v[152:155], v2 offset:34048\n"
      "v_fma_f32 v204, v166, v166, s52\n"
      "v_mfma_f32_16x16x32_bf16 v[162:165], a[220:223], v[126:129], v[162:165]\n"
      "ds_read_b128 v[174:177], v2 offset:25088\n"
      "v_mfma_f32_16x16x32_bf16 v[180:183], a[240:243], v[116:119], 0\n"
      "v_lshrrev_b32 v86, 16, v86\n"
      "v_fmac_f32 v80, v212, v212\n"
      "v_mfma_f32_16x16x32_bf16 v[180:183], a[244:247], v[126:129], v[180:183]\n"
      "v_fmac_f32 v81, v213, v213\n"
      "v_sub_u32 v76, v76, v156\n"
      "v_mfma_f32_16x16x32_bf16 v[170:173], v[232:235], v[116:119], 0\n"
      "v_bfe_i32 v156, v37, 30, 1\n"
      "v_lshrrev_b32 v78, 16, v78\n"
      "v_mfma_f32_16x16x32_bf16 v[170:173], v[236:239], v[126:129], v[170:173]\n"
      "v_fmac_f32 v204, v168, v168\n"
      "v_mfma_f32_16x16x32_bf16 v[162:165], a[224:227], v[108:111], v[162:165]\n"
      "ds_read_b128 v[126:129], v7 offset:18432\n"
      "v_mul_u32_u24_e64 v90, 24, v86\n"
      "v_mfma_f32_16x16x32_bf16 v[162:165], a[228:231], v[194:197], v[162:165]\n"
      "ds_read_b128 v[112:115], v2 offset:28160\n"
      "v_mul_f32 v218, v188, v218\n"
      "v_mfma_f32_16x16x32_bf16 v[180:183], a[248:251], v[108:111], v[180:183]\n"
      "v_subrev_u32 v97, 73728, v5\n"
      "v_fmac_f32 v205, v169, v169\n"
      "v_mfma_f32_16x16x32_bf16 v[180:183], a[252:255], v[194:197], v[180:183]\n"
      "v_cvt_pk_bf16_f32 v218, v218, v218\n"
      "v_fmac_f32_e64 v205, v199, v199\n"
      "v_mfma_f32_16x16x32_bf16 v[170:173], v[240:243], v[108:111], v[170:173]\n"
      "ds_write_b16 v12, v218 offset:16\n"
      "v_mfma_f32_16x16x32_bf16 v[170:173], v[244:247], v[194:197], v[170:173]\n"
      "s_waitcnt lgkmcnt(0)\n"
      "s_barrier\n"
      "v_fmac_f32_e64 v204, v198, v198\n"
      "v_mfma_f32_16x16x32_bf16 v[162:165], a[232:235], v[104:107], v[162:165]\n"
      "s_waitcnt lgkmcnt(2)\n"
      "v_and_b32 v156, 196224, v156\n"
      "v_sub_u32 v90, v85, v90\n"
      "v_mfma_f32_16x16x32_bf16 v[162:165], a[236:239], v[152:155], v[162:165]\n"
      "ds_read_b128 v[188:191], v98 offset:6400\n"
      "v_fmac_f32 v204, v200, v200\n"
      "v_mfma_f32_16x16x32_bf16 v[180:183], v[224:227], v[104:107], v[180:183]\n"
      "v_sub_u32 v76, v76, v156\n"
      "v_and_b32_e64 v85, 7, v86\n"
      "v_mfma_f32_16x16x32_bf16 v[180:183], v[228:231], v[152:155], v[180:183]\n"
      "v_fmac_f32 v205, v201, v201\n"
      "v_fmac_f32 v81, v159, v159\n"
      "v_mfma_f32_16x16x32_bf16 v[170:173], v[248:251], v[104:107], v[170:173]\n"
      "buffer_store_dwordx4 v[126:129], v20, s[24:27], s46 offen sc1\n"
      "v_mfma_f32_16x16x32_bf16 v[170:173], v[252:255], v[152:155], v[170:173]\n"
      "ds_read_b128 v[150:153], v98 offset:10496\n"
      "v_fmac_f32 v205, v143, v143\n"
      "v_mfma_f32_16x16x32_bf16 v[126:129], a[216:219], v[138:141], 0\n"
      "v_fmac_f32 v80, v158, v158\n"
      "v_fmac_f32 v204, v142, v142\n"
      "v_mfma_f32_16x16x32_bf16 v[126:129], a[220:223], v[134:137], v[126:129]\n"
      "ds_read_b128 v[194:197], v98 offset:256\n"
      "v_fmac_f32 v204, v144, v144\n"
      "v_mfma_f32_16x16x32_bf16 v[116:119], a[240:243], v[138:141], 0\n"
      "v_mul_f32 v219, v163, v163\n"
      "v_mul_u32_u24 v148, 24, v78\n"
      "v_mfma_f32_16x16x32_bf16 v[116:119], a[244:247], v[134:137], v[116:119]\n"
      "ds_read_b128 v[104:107], v98 offset:2304\n"
      "v_fmac_f32_e64 v80, v160, v160\n"
      "v_mfma_f32_16x16x32_bf16 v[108:111], v[232:235], v[138:141], 0\n"
      "v_fmac_f32 v205, v145, v145\n"
      "v_mfma_f32_16x16x32_bf16 v[108:111], v[236:239], v[134:137], v[108:111]\n"
      "s_waitcnt lgkmcnt(2)\n"
      "v_fma_f32 v218, v162, v162, s52\n"
      "v_fmac_f32 v218, v164, v164\n"
      "v_mfma_f32_16x16x32_bf16 v[126:129], a[224:227], v[174:177], v[126:129]\n"
      "ds_read_b128 v[154:157], v7 offset:22528\n"
      "v_mfma_f32_16x16x32_bf16 v[126:129], a[228:231], v[112:115], v[126:129]\n"
      "v_fmac_f32 v219, v165, v165\n"
      "v_fmac_f32 v219, v181, v181\n"
      "v_mfma_f32_16x16x32_bf16 v[116:119], a[248:251], v[174:177], v[116:119]\n"
      "v_fmac_f32 v218, v180, v180\n"
      "v_xor_b32 v90, v90, v85\n"
      "v_mfma_f32_16x16x32_bf16 v[116:119], a[252:255], v[112:115], v[116:119]\n"
      "v_sub_u32 v148, v82, v148\n"
      "v_fmac_f32 v219, v183, v183\n"
      "v_mfma_f32_16x16x32_bf16 v[108:111], v[240:243], v[174:177], v[108:111]\n"
      "v_lshl_add_u32 v86, v86, 2, 12\n"
      "v_fmac_f32 v218, v182, v182\n"
      "v_mfma_f32_16x16x32_bf16 v[108:111], v[244:247], v[112:115], v[108:111]\n"
      "v_fmac_f32 v218, v170, v170\n"
      "v_fmac_f32 v219, v171, v171\n"
      "v_mfma_f32_16x16x32_bf16 v[126:129], a[232:235], v[214:217], v[126:129]\n"
      "v_fmac_f32_e64 v219, v173, v173\n"
      "v_mfma_f32_16x16x32_bf16 v[126:129], a[236:239], v[100:103], v[126:129]\n"
      "ds_read_b128 v[138:141], v7 offset:34816\n"
      "v_fmac_f32 v81, v161, v161\n"
      "v_mfma_f32_16x16x32_bf16 v[116:119], v[224:227], v[214:217], v[116:119]\n"
      "ds_read_b128 v[174:177], v98 offset:4352\n"
      "v_fmac_f32 v218, v172, v172\n"
      "v_mfma_f32_16x16x32_bf16 v[116:119], v[228:231], v[100:103], v[116:119]\n"
      "s_nop 1\n"
      "v_mul_f32 v85, v127, v127\n"
      "v_mfma_f32_16x16x32_bf16 v[108:111], v[248:251], v[214:217], v[108:111]\n"
      "ds_read_b128 v[214:217], v98 offset:8448\n"
      "v_fma_f32 v84, v126, v126, s52\n"
      "v_mfma_f32_16x16x32_bf16 v[108:111], v[252:255], v[100:103], v[108:111]\n"
      "s_waitcnt lgkmcnt(3)\n"
      "v_fmac_f32 v84, v128, v128\n"
      "v_add_f32_e64 v218, v218, v219\n"
      "v_mfma_f32_16x16x32_bf16 v[134:137], a[216:219], v[194:197], 0\n"
      "v_fmac_f32 v80, v206, v206\n"
      "v_fmac_f32 v85, v129, v129\n"
      "v_mfma_f32_16x16x32_bf16 v[134:137], a[220:223], v[104:107], v[134:137]\n"
      "buffer_store_dwordx4 v[154:157], v21, s[24:27], s46 offen sc1\n"
      "v_mfma_f32_16x16x32_bf16 v[112:115], a[240:243], v[194:197], 0\n"
      "v_fmac_f32 v84, v116, v116\n"
      "v_fmac_f32 v85, v117, v117\n"
      "v_mfma_f32_16x16x32_bf16 v[112:115], a[244:247], v[104:107], v[112:115]\n"
      "v_add_f32 v204, v204, v205\n"
      "v_fmac_f32 v85, v119, v119\n"
      "v_mfma_f32_16x16x32_bf16 v[98:101], v[232:235], v[194:197], 0\n"
      "s_waitcnt lgkmcnt(0)\n"
      "v_fmac_f32 v84, v118, v118\n"
      "v_fmac_f32_e64 v85, v109, v109\n"
      "v_mfma_f32_16x16x32_bf16 v[98:101], v[236:239], v[104:107], v[98:101]\n"
      "ds_bpermute_b32 v155, v86, v76\n"
      "v_fmac_f32 v84, v108, v108\n"
      "v_mfma_f32_16x16x32_bf16 v[134:137], a[224:227], v[174:177], v[134:137]\n"
      "v_fmac_f32 v81, v207, v207\n"
      "v_fmac_f32_e64 v85, v111, v111\n"
      "v_mfma_f32_16x16x32_bf16 v[134:137], a[228:231], v[188:191], v[134:137]\n"
      "v_fmac_f32_e64 v84, v110, v110\n"
      "v_mfma_f32_16x16x32_bf16 v[112:115], a[248:251], v[174:177], v[112:115]\n"
      "buffer_store_dwordx4 v[220:223], v22, s[24:27], s46 offen sc1\n"
      "v_mfma_f32_16x16x32_bf16 v[112:115], a[252:255], v[188:191], v[112:115]\n"
      "v_permlane16_swap_b32 v204, v218\n"
      "v_mfma_f32_16x16x32_bf16 v[98:101], v[240:243], v[174:177], v[98:101]\n"
      "v_and_b32 v82, 7, v78\n"
      "v_add_f32_e64 v84, v84, v85\n"
      "v_mfma_f32_16x16x32_bf16 v[98:101], v[244:247], v[188:191], v[98:101]\n"
      "v_add_f32 v204, v204, v218\n"
      "v_mov_b32 v218, v84\n"
      "v_mfma_f32_16x16x32_bf16 v[134:137], a[232:235], v[214:217], v[134:137]\n"
      "s_nop 0\n"
      "v_permlane16_swap_b32 v84, v218\n"
      "v_mfma_f32_16x16x32_bf16 v[134:137], a[236:239], v[150:153], v[134:137]\n"
      "v_add_f32 v84, v84, v218\n"
      "v_lshl_add_u32 v78, v78, 2, 12\n"
      "v_mfma_f32_16x16x32_bf16 v[112:115], v[224:227], v[214:217], v[112:115]\n"
      "s_waitcnt lgkmcnt(0)\n"
      "v_permlane32_swap_b32 v204, v84\n"
      "v_mfma_f32_16x16x32_bf16 v[112:115], v[228:231], v[150:153], v[112:115]\n"
      "ds_bpermute_b32 v157, v78, v76\n"
      "v_add_f32 v204, v204, v84\n"
      "v_mfma_f32_16x16x32_bf16 v[98:101], v[248:251], v[214:217], v[98:101]\n"
      "ds_write_b32 v31, v204\n"
      "v_mfma_f32_16x16x32_bf16 v[98:101], v[252:255], v[150:153], v[98:101]\n"
      "buffer_store_dwordx4 v[130:133], v23, s[24:27], s46 offen sc1\n"
      "v_pk_fma_f32 v[80:81], v[208:209], v[208:209], v[80:81]\n"
      "s_nop 0\n"
      "v_mul_f32 v133, v135, v135\n"
      "v_fma_f32 v132, v134, v134, s52\n"
      "v_pk_fma_f32 v[132:133], v[136:137], v[136:137], v[132:133]\n"
      "v_pk_fma_f32 v[132:133], v[112:113], v[112:113], v[132:133]\n"
      "v_add_f32 v80, v80, v81\n"
      "v_pk_fma_f32 v[132:133], v[114:115], v[114:115], v[132:133]\n"
      "v_pk_fma_f32 v[132:133], v[98:99], v[98:99], v[132:133]\n"
      "v_pk_fma_f32 v[132:133], v[100:101], v[100:101], v[132:133]\n"
      "v_add_f32 v132, v132, v133\n"
      "v_lshl_add_u32 v155, v90, 4, v155\n"
      "s_nop 0\n"
      "v_permlane16_swap_b32 v80, v132\n"
      "v_add_f32 v80, v80, v132\n"
      "v_mov_b32 v132, v80\n"
      "s_nop 1\n"
      "v_permlane32_swap_b32 v80, v132\n"
      "v_add_f32 v80, v80, v132\n"
      "ds_write_b32 v31, v80 offset:4608\n"
      "v_add_u32 v12, 32, v12\n"
      "buffer_store_dwordx4 v[138:141], v25, s[24:27], s46 offen sc1\n"
      "v_subrev_u32 v218, 73728, v6\n"
      "v_readlane_b32 s46, v37, s42\n"
      "v_xor_b32 v148, v148, v82\n"
      "s_add_u32 s42, s42, 1\n"
      "s_or_b32 s95, s46, s48\n"
      "s_cmp_ge_u32 s95, 0x40000000\n"
      "s_cbranch_scc1 L_rare_35_%=\n"
      "L_back_36_%=:\n"
      "s_waitcnt lgkmcnt(1)\n"
      "v_lshl_add_u32 v157, v148, 4, v157\n"
      "s_waitcnt lgkmcnt(0)\n"
      "s_barrier\n"
      "ds_read_b128 v[130:133], v7\n"
      "ds_read_b128 v[188:191], v9 offset:4608\n"
      "ds_read_b128 v[214:217], v9\n"
      "ds_read_b128 v[138:141], v7 offset:12288\n"
      "ds_read_b128 v[104:107], v7 offset:8192\n"
      "ds_read_b128 v[194:197], v7 offset:16384\n"
      "ds_read_b128 v[150:153], v7 offset:4096\n"
      "s_waitcnt lgkmcnt(6)\n"
      "buffer_store_dwordx4 v[130:133], v20, s[24:27], s46 offen sc1\n"
      "s_waitcnt lgkmcnt(4)\n"
      "v_add_f32 v214, v214, v215\n"
      "v_add_f32 v216, v216, v217\n"
      "v_add_f32 v190, v190, v191\n"
      "v_add_f32 v214, v214, v216\n"
      "v_rsq_f32 v214, v214\n"
      "v_add_f32 v188, v188, v189\n"
      "v_mul_f32 v214, 0x415db3d7, v214\n"
      "v_add_f32 v188, v188, v190\n"
      "v_rsq_f32 v188, v188\n"
      "v_mov_b32 v216, v214\n"
      "v_mul_f32 v188, 0x415db3d7, v188\n"
      "v_mov_b32 v190, v188\n"
      "v_permlane32_swap_b32 v214, v216\n"
      "v_mov_b32 v215, v214\n"
      "v_permlane32_swap_b32 v188, v190\n"
      "v_mov_b32 v189, v188\n"
      "v_pk_mul_f32 v[118:119], v[118:119], v[216:217] op_sel_hi:[1,0]\n"
      "v_permlane16_swap_b32_e64 v214, v215\n"
      "v_pk_mul_f32 v[200:201], v[200:201], v[214:215] op_sel_hi:[1,0]\n"
      "v_permlane16_swap_b32_e64 v188, v189\n"
      "v_pk_mul_f32 v[126:127], v[126:127], v[216:217] op_sel_hi:[1,0]\n"
      "v_pk_mul_f32 v[112:113], v[112:113], v[188:189] op_sel:[0,1]\n"
      "v_pk_mul_f32 v[128:129], v[128:129], v[216:217] op_sel_hi:[1,0]\n"
      "v_pk_mul_f32 v[166:167], v[166:167], v[214:215] op_sel_hi:[1,0]\n"
      "v_pk_mul_f32 v[142:143], v[142:143], v[214:215] op_sel_hi:[1,0]\n"
      "v_pk_mul_f32 v[114:115], v[114:115], v[188:189] op_sel:[0,1]\n"
      "v_pk_mul_f32 v[164:165], v[164:165], v[214:215] op_sel:[0,1]\n"
      "v_pk_mul_f32 v[136:137], v[136:137], v[188:189] op_sel:[0,1]\n"
      "v_cvt_pk_bf16_f32 v126, v126, v127\n"
      "v_pk_mul_f32 v[134:135], v[134:135], v[188:189] op_sel:[0,1]\n"
      "v_pk_mul_f32 v[168:169], v[168:169], v[214:215] op_sel_hi:[1,0]\n"
      "v_pk_mul_f32 v[98:99], v[98:99], v[188:189] op_sel:[0,1]\n"
      "v_pk_mul_f32 v[162:163], v[162:163], v[214:215] op_sel:[0,1]\n"
      "v_pk_mul_f32 v[100:101], v[100:101], v[188:189] op_sel:[0,1]\n"
      "v_pk_mul_f32 v[208:209], v[208:209], v[188:189] op_sel_hi:[1,0]\n"
      "v_cvt_pk_bf16_f32 v166, v166, v167\n"
      "v_cvt_pk_bf16_f32 v134, v134, v135\n"
      "v_cvt_pk_bf16_f32 v135, v136, v137\n"
      "v_cvt_pk_bf16_f32 v162, v162, v163\n"
      "v_pk_mul_f32 v[160:161], v[160:161], v[188:189] op_sel_hi:[1,0]\n"
      "v_pk_mul_f32 v[198:199], v[198:199], v[214:215] op_sel_hi:[1,0]\n"
      "v_pk_mul_f32 v[144:145], v[144:145], v[214:215] op_sel_hi:[1,0]\n"
      "v_cvt_pk_bf16_f32 v127, v128, v129\n"
      "v_cvt_pk_bf16_f32 v167, v168, v169\n"
      "v_pk_mul_f32 v[110:111], v[110:111], v[216:217] op_sel_hi:[1,0]\n"
      "v_cvt_pk_bf16_f32 v163, v164, v165\n"
      "v_pk_mul_f32 v[182:183], v[182:183], v[214:215] op_sel:[0,1]\n"
      "v_cvt_pk_bf16_f32 v129, v118, v119\n"
      "v_pk_mul_f32 v[116:117], v[116:117], v[216:217] op_sel_hi:[1,0]\n"
      "v_pk_mul_f32 v[210:211], v[210:211], v[188:189] op_sel_hi:[1,0]\n"
      "v_cvt_pk_bf16_f32 v168, v198, v199\n"
      "v_pk_mul_f32 v[180:181], v[180:181], v[214:215] op_sel:[0,1]\n"
      "v_cvt_pk_bf16_f32 v98, v98, v99\n"
      "v_cvt_pk_bf16_f32 v169, v200, v201\n"
      "v_cvt_pk_bf16_f32 v128, v116, v117\n"
      "v_cvt_pk_bf16_f32 v136, v112, v113\n"
      "v_cvt_pk_bf16_f32 v164, v180, v181\n"
      "v_cvt_pk_bf16_f32 v137, v114, v115\n"
      "v_cvt_pk_bf16_f32 v165, v182, v183\n"
      "v_pk_mul_f32 v[108:109], v[108:109], v[216:217] op_sel_hi:[1,0]\n"
      "v_cvt_pk_bf16_f32 v99, v100, v101\n"
      "ds_write_b128 v5, v[162:165] offset:24576\n"
      "ds_write_b128 v5, v[126:129] offset:30720\n"
      "ds_write_b64 v218, v[98:99] offset:6144\n"
      "ds_write_b128 v97, v[134:137] offset:6144\n"
      "ds_write_b128 v5, v[166:169] offset:18432\n"
      "v_cvt_pk_bf16_f32 v142, v142, v143\n"
      "v_cvt_pk_bf16_f32 v210, v210, v211\n"
      "v_cvt_pk_bf16_f32 v108, v108, v109\n"
      "v_pk_mul_f32 v[158:159], v[158:159], v[188:189] op_sel_hi:[1,0]\n"
      "v_pk_mul_f32 v[206:207], v[206:207], v[188:189] op_sel_hi:[1,0]\n"
      "v_pk_mul_f32 v[170:171], v[170:171], v[214:215] op_sel:[0,1]\n"
      "v_cvt_pk_bf16_f32 v170, v170, v171\n"
      "v_pk_mul_f32 v[212:213], v[212:213], v[188:189] op_sel_hi:[1,0]\n"
      "v_cvt_pk_bf16_f32 v211, v212, v213\n"
      "v_cvt_pk_bf16_f32 v143, v144, v145\n"
      "v_cvt_pk_bf16_f32 v109, v110, v111\n"
      "v_pk_mul_f32 v[172:173], v[172:173], v[214:215] op_sel:[0,1]\n"
      "v_cvt_pk_bf16_f32 v212, v158, v159\n"
      "v_cvt_pk_bf16_f32 v213, v160, v161\n"
      "v_cvt_pk_bf16_f32 v206, v206, v207\n"
      "v_cvt_pk_bf16_f32 v207, v208, v209\n"
      "v_cvt_pk_bf16_f32 v171, v172, v173\n"
      "s_waitcnt lgkmcnt(3)\n"
      "buffer_store_dwordx4 v[150:153], v21, s[24:27], s46 offen sc1\n"
      "ds_write_b128 v97, v[210:213]\n"
      "ds_write_b64 v6, v[108:109] offset:30720\n"
      "ds_write_b64 v218, v[206:207]\n"
      "ds_write_b64 v6, v[170:171] offset:24576\n"
      "ds_write_b64 v6, v[142:143] offset:18432\n"
      "buffer_store_dwordx4 v[104:107], v22, s[24:27], s46 offen sc1\n"
      "buffer_store_dwordx4 v[138:141], v23, s[24:27], s46 offen sc1\n"
      "buffer_store_dwordx4 v[194:197], v25, s[24:27], s46 offen sc1\n"
      "v_readlane_b32 s46, v37, s42\n"
      "s_add_u32 s42, s42, 1\n"
      "s_or_b32 s95, s46, s48\n"
      "s_cmp_ge_u32 s95, 0x40000000\n"
      "s_cbranch_scc1 L_rare_39_%=\n"
      "L_back_40_%=:\n"
      "s_waitcnt lgkmcnt(0)\n"
      "s_barrier\n"
      "ds_read_b128 v[164:167], v7 offset:18432\n"
      "ds_read_b128 v[138:141], v7 offset:22528\n"
      "ds_read_b128 v[106:109], v7 offset:26624\n"
      "ds_read_b128 v[180:183], v7 offset:30720\n"
      "ds_read_b128 v[194:197], v7 offset:34816\n"
      "ds_read_b128 v[110:113], v77\n"
      "ds_read_b128 v[210:213], v77 offset:1024\n"
      "s_waitcnt lgkmcnt(6)\n"
      "buffer_store_dwordx4 v[164:167], v20, s[24:27], s46 offen sc1\n"
      "s_waitcnt lgkmcnt(5)\n"
      "buffer_store_dwordx4 v[138:141], v21, s[24:27], s46 offen sc1\n"
      "s_waitcnt lgkmcnt(4)\n"
      "buffer_store_dwordx4 v[106:109], v22, s[24:27], s46 offen sc1\n"
      "s_waitcnt lgkmcnt(3)\n"
      "buffer_store_dwordx4 v[180:183], v23, s[24:27], s46 offen sc1\n"
      "s_waitcnt lgkmcnt(2)\n"
      "buffer_store_dwordx4 v[194:197], v25, s[24:27], s46 offen sc1\n"
      "s_waitcnt lgkmcnt(1)\n"
      "buffer_store_dwordx4 v[110:113], v157, s[24:27], 0 offen sc1\n"
      "s_waitcnt lgkmcnt(0)\n"
      "buffer_store_dwordx4 v[210:213], v155, s[24:27], 0 offen sc1\n"
      "s_endpgm\n"
      "L_rare_1_%=:\n"
      "s_nop 7\n"
      "s_waitcnt lgkmcnt(0)\n"
      "s_add_u32 s72, s42, 1\n"
      "v_readlane_b32 s77, v38, s72\n"
      "v_subrev_u32 v174, 115456, v9\n"
      "s_and_b32 s73, s77, -16\n"
      "s_mov_b32 m0, 110592\n"
      "s_nop 0\n"
      "buffer_load_dwordx4 v174, s[28:31], s73 offen  lds\n"
      "s_add_u32 s73, s73, 1024\n"
      "s_mov_b32 m0, 111616\n"
      "s_nop 0\n"
      "buffer_load_dwordx4 v174, s[28:31], s73 offen  lds\n"
      "s_add_u32 s73, s73, 1024\n"
      "s_mov_b32 m0, 112640\n"
      "s_nop 0\n"
      "buffer_load_dwordx4 v174, s[28:31], s73 offen  lds\n"
      "s_add_u32 s73, s73, 1024\n"
      "s_mov_b32 m0, 113664\n"
      "s_nop 0\n"
      "buffer_load_dwordx4 v174, s[28:31], s73 offen  lds\n"
      "s_add_u32 s73, s73, 1024\n"
      "s_mov_b32 m0, 114688\n"
      "s_mov_b32 exec_hi, 0xffff\n"
      "buffer_load_dwordx4 v174, s[28:31], s73 offen  lds\n"
      "s_mov_b32 exec_hi, -1\n"
      "s_add_u32 s73, s73, 1024\n"
      "s_and_b32 s77, s77, 15\n"
      "s_waitcnt lgkmcnt(0)\n"
      "s_nop 4\n"
      "s_branch L_back_2_%=\n"
      "L_rare_3_%=:\n"
      "s_nop 7\n"
      "s_waitcnt lgkmcnt(0)\n"
      "s_waitcnt vmcnt(0)\n"
      "v_subrev_u32 v89, 115456, v9\n"
      "v_bfe_u32 v127, v89, 4, 4\n"
      "v_mul_u32_u24 v127, 98, v127\n"
      "v_lshrrev_b32 v89, 8, v89\n"
      "v_lshl_add_u32 v127, v89, 3, v127\n"
      "v_add_u32 v127, s77, v127\n"
      "v_and_b32 v82, 2, v127\n"
      "v_and_b32_e64 v127, -4, v127\n"
      "v_add_u32 v127, 110592, v127\n"
      "ds_read2_b32 v[174:175], v127 offset0:0 offset1:1\n"
      "ds_read2_b32 v[176:177], v127 offset0:8 offset1:9\n"
      "ds_read2_b32 v[130:131], v127 offset0:16 offset1:17\n"
      "ds_read2_b32 v[194:195], v127 offset0:2 offset1:10\n"
      "ds_read2_b32 v[118:119], v127 offset0:18 offset1:24\n"
      "s_waitcnt lgkmcnt(4)\n"
      "v_alignbyte_b32 v174, v175, v174, v82\n"
      "s_waitcnt lgkmcnt(1)\n"
      "v_alignbyte_b32 v175, v194, v175, v82\n"
      "v_lshlrev_b32 v89, 16, v174\n"
      "v_fmac_f32 v160, s53, v89\n"
      "v_and_b32 v89, 0xffff0000, v174\n"
      "v_fmac_f32 v161, s53, v89\n"
      "v_lshlrev_b32 v89, 16, v175\n"
      "v_fmac_f32_e64 v162, s53, v89\n"
      "v_and_b32 v89, 0xffff0000, v175\n"
      "v_fmac_f32 v163, s53, v89\n"
      "v_alignbyte_b32 v176, v177, v176, v82\n"
      "v_alignbyte_b32 v177, v195, v177, v82\n"
      "v_lshlrev_b32 v89, 16, v176\n"
      "v_fmac_f32 v102, s53, v89\n"
      "v_and_b32 v89, 0xffff0000, v176\n"
      "v_fmac_f32 v103, s53, v89\n"
      "v_lshlrev_b32 v89, 16, v177\n"
      "v_fmac_f32 v104, s53, v89\n"
      "v_and_b32 v89, 0xffff0000, v177\n"
      "v_fmac_f32 v105, s53, v89\n"
      "v_alignbyte_b32 v130, v131, v130, v82\n"
      "s_waitcnt lgkmcnt(0)\n"
      "v_alignbyte_b32 v131, v118, v131, v82\n"
      "v_lshlrev_b32 v89, 16, v130\n"
      "v_fmac_f32 v148, s53, v89\n"
      "v_and_b32 v89, 0xffff0000, v130\n"
      "v_fmac_f32 v149, s53, v89\n"
      "v_lshlrev_b32 v89, 16, v131\n"
      "v_fmac_f32 v150, s53, v89\n"
      "v_and_b32 v89, 0xffff0000, v131\n"
      "v_fmac_f32 v151, s53, v89\n"
      "v_alignbyte_b32 v89, v119, v119, v82\n"
      "v_lshlrev_b32 v89, 16, v89\n"
      "v_fmac_f32 v126, s53, v89\n"
      "v_max3_f32 v127, v160, v161, v162\n"
      "v_max3_f32 v82, v163, v102, v103\n"
      "v_max3_f32 v127, v127, v104, v105\n"
      "v_max3_f32 v82, v82, v148, v149\n"
      "v_max3_f32 v127, v127, v150, v151\n"
      "v_max3_f32 v127, v127, v82, v126\n"
      "v_mov_b32 v89, v127\n"
      "s_nop 1\n"
      "v_permlane16_swap_b32 v127, v89\n"
      "v_max_f32 v127, v127, v89\n"
      "v_mov_b32 v89, v127\n"
      "s_nop 1\n"
      "v_permlane32_swap_b32 v127, v89\n"
      "v_max_f32 v127, v127, v89\n"
      "v_sub_f32 v160, v160, v127\n"
      "v_sub_f32 v161, v161, v127\n"
      "v_sub_f32 v162, v162, v127\n"
      "v_sub_f32 v163, v163, v127\n"
      "v_sub_f32 v102, v102, v127\n"
      "v_sub_f32 v103, v103, v127\n"
      "v_sub_f32 v104, v104, v127\n"
      "v_sub_f32 v105, v105, v127\n"
      "v_sub_f32 v148, v148, v127\n"
      "v_sub_f32 v149, v149, v127\n"
      "v_sub_f32 v150, v150, v127\n"
      "v_sub_f32 v151, v151, v127\n"
      "v_sub_f32_e64 v126, v126, v127\n"
      "s_waitcnt lgkmcnt(0)\n"
      "s_nop 4\n"
      "s_branch L_back_4_%=\n"
      "L_rare_5_%=:\n"
      "s_nop 7\n"
      "s_waitcnt lgkmcnt(0)\n"
      "s_waitcnt vmcnt(0)\n"
      "v_subrev_u32 v178, 115456, v9\n"
      "v_bfe_u32 v215, v178, 4, 4\n"
      "v_mul_u32_u24 v215, 98, v215\n"
      "v_lshrrev_b32 v178, 8, v178\n"
      "v_lshl_add_u32 v215, v178, 3, v215\n"
      "v_add_u32 v215, s77, v215\n"
      "v_and_b32 v190, 2, v215\n"
      "v_and_b32 v215, -4, v215\n"
      "v_add_u32 v215, 112160, v215\n"
      "ds_read2_b32 v[120:121], v215 offset0:0 offset1:1\n"
      "ds_read2_b32 v[118:119], v215 offset0:8 offset1:9\n"
      "ds_read2_b32 v[116:117], v215 offset0:16 offset1:17\n"
      "ds_read2_b32 v[100:101], v215 offset0:2 offset1:10\n"
      "ds_read2_b32 v[98:99], v215 offset0:18 offset1:24\n"
      "s_waitcnt lgkmcnt(4)\n"
      "v_alignbyte_b32 v120, v121, v120, v190\n"
      "s_waitcnt lgkmcnt(1)\n"
      "v_alignbyte_b32 v121, v100, v121, v190\n"
      "v_lshlrev_b32 v178, 16, v120\n"
      "v_fmac_f32 v170, s53, v178\n"
      "v_and_b32 v178, 0xffff0000, v120\n"
      "v_fmac_f32 v171, s53, v178\n"
      "v_lshlrev_b32 v178, 16, v121\n"
      "v_fmac_f32 v172, s53, v178\n"
      "v_and_b32 v178, 0xffff0000, v121\n"
      "v_fmac_f32 v173, s53, v178\n"
      "v_alignbyte_b32 v118, v119, v118, v190\n"
      "v_alignbyte_b32 v119, v101, v119, v190\n"
      "v_lshlrev_b32 v178, 16, v118\n"
      "v_fmac_f32 v154, s53, v178\n"
      "v_and_b32 v178, 0xffff0000, v118\n"
      "v_fmac_f32 v155, s53, v178\n"
      "v_lshlrev_b32 v178, 16, v119\n"
      "v_fmac_f32_e64 v156, s53, v178\n"
      "v_and_b32 v178, 0xffff0000, v119\n"
      "v_fmac_f32 v157, s53, v178\n"
      "v_alignbyte_b32 v116, v117, v116, v190\n"
      "s_waitcnt lgkmcnt(0)\n"
      "v_alignbyte_b32 v117, v98, v117, v190\n"
      "v_lshlrev_b32 v178, 16, v116\n"
      "v_fmac_f32 v198, s53, v178\n"
      "v_and_b32 v178, 0xffff0000, v116\n"
      "v_fmac_f32 v199, s53, v178\n"
      "v_lshlrev_b32 v178, 16, v117\n"
      "v_fmac_f32_e64 v200, s53, v178\n"
      "v_and_b32 v178, 0xffff0000, v117\n"
      "v_fmac_f32 v201, s53, v178\n"
      "v_alignbyte_b32 v178, v99, v99, v190\n"
      "v_lshlrev_b32 v178, 16, v178\n"
      "v_fmac_f32_e64 v218, s53, v178\n"
      "v_max3_f32 v215, v170, v171, v172\n"
      "v_max3_f32 v190, v173, v154, v155\n"
      "v_max3_f32 v215, v215, v156, v157\n"
      "v_max3_f32 v190, v190, v198, v199\n"
      "v_max3_f32 v215, v215, v200, v201\n"
      "v_max3_f32 v215, v215, v190, v218\n"
      "v_mov_b32 v178, v215\n"
      "s_nop 1\n"
      "v_permlane16_swap_b32 v215, v178\n"
      "v_max_f32 v215, v215, v178\n"
      "v_mov_b32 v178, v215\n"
      "s_nop 1\n"
      "v_permlane32_swap_b32 v215, v178\n"
      "v_max_f32 v215, v215, v178\n"
      "v_sub_f32 v170, v170, v215\n"
      "v_sub_f32 v171, v171, v215\n"
      "v_sub_f32 v172, v172, v215\n"
      "v_sub_f32 v173, v173, v215\n"
      "v_sub_f32 v154, v154, v215\n"
      "v_sub_f32 v155, v155, v215\n"
      "v_sub_f32 v156, v156, v215\n"
      "v_sub_f32 v157, v157, v215\n"
      "v_sub_f32 v198, v198, v215\n"
      "v_sub_f32 v199, v199, v215\n"
      "v_sub_f32 v200, v200, v215\n"
      "v_sub_f32 v201, v201, v215\n"
      "v_sub_f32_e64 v218, v218, v215\n"
      "s_waitcnt lgkmcnt(0)\n"
      "s_nop 4\n"
      "s_branch L_back_6_%=\n"
      "L_rare_7_%=:\n"
      "s_nop 7\n"
      "s_waitcnt lgkmcnt(0)\n"
      "s_waitcnt vmcnt(0)\n"
      "v_subrev_u32 v103, 115456, v9\n"
      "v_bfe_u32 v158, v103, 4, 4\n"
      "v_mul_u32_u24 v158, 98, v158\n"
      "v_lshrrev_b32_e64 v103, 8, v103\n"
      "v_lshl_add_u32 v158, v103, 3, v158\n"
      "v_add_u32 v158, s77, v158\n"
      "v_and_b32 v159, 2, v158\n"
      "v_and_b32_e64 v158, -4, v158\n"
      "v_add_u32 v158, 113728, v158\n"
      "ds_read2_b32 v[160:161], v158 offset0:0 offset1:1\n"
      "ds_read2_b32 v[162:163], v158 offset0:8 offset1:9\n"
      "ds_read2_b32 v[140:141], v158 offset0:16 offset1:17\n"
      "ds_read2_b32 v[142:143], v158 offset0:2 offset1:10\n"
      "ds_read2_b32 v[100:101], v158 offset0:18 offset1:24\n"
      "s_waitcnt lgkmcnt(4)\n"
      "v_alignbyte_b32 v160, v161, v160, v159\n"
      "s_waitcnt lgkmcnt(1)\n"
      "v_alignbyte_b32 v161, v142, v161, v159\n"
      "v_lshlrev_b32 v103, 16, v160\n"
      "v_fmac_f32 v110, s53, v103\n"
      "v_and_b32 v103, 0xffff0000, v160\n"
      "v_fmac_f32 v111, s53, v103\n"
      "v_lshlrev_b32 v103, 16, v161\n"
      "v_fmac_f32_e64 v112, s53, v103\n"
      "v_and_b32 v103, 0xffff0000, v161\n"
      "v_fmac_f32 v113, s53, v103\n"
      "v_alignbyte_b32 v162, v163, v162, v159\n"
      "v_alignbyte_b32 v163, v143, v163, v159\n"
      "v_lshlrev_b32 v103, 16, v162\n"
      "v_fmac_f32 v120, s53, v103\n"
      "v_and_b32 v103, 0xffff0000, v162\n"
      "v_fmac_f32 v121, s53, v103\n"
      "v_lshlrev_b32 v103, 16, v163\n"
      "v_fmac_f32 v122, s53, v103\n"
      "v_and_b32 v103, 0xffff0000, v163\n"
      "v_fmac_f32 v123, s53, v103\n"
      "v_alignbyte_b32 v140, v141, v140, v159\n"
      "s_waitcnt lgkmcnt(0)\n"
      "v_alignbyte_b32 v141, v100, v141, v159\n"
      "v_lshlrev_b32 v103, 16, v140\n"
      "v_fmac_f32 v166, s53, v103\n"
      "v_and_b32 v103, 0xffff0000, v140\n"
      "v_fmac_f32 v167, s53, v103\n"
      "v_lshlrev_b32 v103, 16, v141\n"
      "v_fmac_f32 v168, s53, v103\n"
      "v_and_b32 v103, 0xffff0000, v141\n"
      "v_fmac_f32 v169, s53, v103\n"
      "v_alignbyte_b32 v103, v101, v101, v159\n"
      "v_lshlrev_b32 v103, 16, v103\n"
      "v_fmac_f32 v220, s53, v103\n"
      "v_max3_f32 v158, v110, v111, v112\n"
      "v_max3_f32 v159, v113, v120, v121\n"
      "v_max3_f32 v158, v158, v122, v123\n"
      "v_max3_f32 v159, v159, v166, v167\n"
      "v_max3_f32 v158, v158, v168, v169\n"
      "v_max3_f32 v158, v158, v159, v220\n"
      "v_mov_b32 v103, v158\n"
      "s_nop 1\n"
      "v_permlane16_swap_b32 v158, v103\n"
      "v_max_f32 v158, v158, v103\n"
      "v_mov_b32 v103, v158\n"
      "s_nop 1\n"
      "v_permlane32_swap_b32 v158, v103\n"
      "v_max_f32 v158, v158, v103\n"
      "v_sub_f32 v110, v110, v158\n"
      "v_sub_f32 v111, v111, v158\n"
      "v_sub_f32 v112, v112, v158\n"
      "v_sub_f32 v113, v113, v158\n"
      "v_sub_f32 v120, v120, v158\n"
      "v_sub_f32 v121, v121, v158\n"
      "v_sub_f32 v122, v122, v158\n"
      "v_sub_f32 v123, v123, v158\n"
      "v_sub_f32 v166, v166, v158\n"
      "v_sub_f32 v167, v167, v158\n"
      "v_sub_f32 v168, v168, v158\n"
      "v_sub_f32 v169, v169, v158\n"
      "v_sub_f32 v220, v220, v158\n"
      "s_waitcnt lgkmcnt(0)\n"
      "s_nop 4\n"
      "s_branch L_back_8_%=\n"
      "L_rare_9_%=:\n"
      "s_nop 7\n"
      "s_waitcnt lgkmcnt(0)\n"
      "s_waitcnt vmcnt(0)\n"
      "v_subrev_u32 v160, 115456, v9\n"
      "v_lshrrev_b32 v160, 3, v160\n"
      "v_min_u32 v160, 96, v160\n"
      "v_add_u32 v160, s77, v160\n"
      "v_add_u32 v160, 115296, v160\n"
      "ds_read_u16 v160, v160\n"
      "s_waitcnt lgkmcnt(0)\n"
      "v_lshlrev_b32_e64 v160, 16, v160\n"
      "v_fmac_f32 v100, s53, v160\n"
      "s_nop 1\n"
      "v_max_f32_dpp v208, v100, v100 row_ror:8 row_mask:0xf bank_mask:0xf\n"
      "s_nop 1\n"
      "v_max_f32_dpp v208, v208, v208 row_ror:4 row_mask:0xf bank_mask:0xf\n"
      "s_nop 1\n"
      "v_max_f32_dpp v208, v208, v208 row_ror:2 row_mask:0xf bank_mask:0xf\n"
      "s_nop 1\n"
      "v_max_f32_dpp v208, v208, v208 row_ror:1 row_mask:0xf bank_mask:0xf\n"
      "s_nop 1\n"
      "v_max_f32_dpp v208, v208, v208 row_bcast:15 row_mask:0xa bank_mask:0xf\n"
      "s_nop 1\n"
      "v_max_f32_dpp v208, v208, v208 row_bcast:31 row_mask:0xc bank_mask:0xf\n"
      "s_nop 0\n"
      "v_readlane_b32 s72, v208, 63\n"
      "s_nop 1\n"
      "v_subrev_f32 v100, s72, v100\n"
      "s_waitcnt lgkmcnt(0)\n"
      "s_nop 4\n"
      "s_branch L_back_10_%=\n"
      "L_rare_27_%=:\n"
      "s_nop 7\n"
      "s_waitcnt lgkmcnt(0)\n"
      "s_bitcmp1_b32 s85, 31\n"
      "s_cselect_b32 s75, 100270464, 0\n"
      "s_bitcmp1_b32 s85, 30\n"
      "s_cselect_b32 s76, 196224, 0\n"
      "s_and_b32 s74, s85, 0x3fffffff\n"
      "s_cmp_ge_u32 s85, 0x40000000\n"
      "s_cselect_b32 s74, s74, 0\n"
      "s_cselect_b32 s85, 0, s85\n"
      "s_sub_u32 s74, s74, s69\n"
      "s_add_u32 s69, s69, s74\n"
      "s_sub_u32 s75, s75, s70\n"
      "s_add_u32 s70, s70, s75\n"
      "s_sub_u32 s76, s76, s71\n"
      "s_add_u32 s71, s71, s76\n"
      "v_add_u32 v20, s74, v20\n"
      "v_add_u32 v21, s74, v21\n"
      "v_add_u32 v22, s74, v22\n"
      "v_add_u32 v23, s74, v23\n"
      "v_add_u32 v25, s74, v25\n"
      "s_cmp_eq_u32 s75, 0\n"
      "s_cbranch_scc1 L_ax_25_%=\n"
      "v_bfe_i32 v218, v27, 10, 1\n"
      "v_and_b32 v218, s75, v218\n"
      "v_sub_u32 v20, v20, v218\n"
      "v_bfe_i32 v218, v27, 12, 1\n"
      "v_and_b32 v218, s75, v218\n"
      "v_sub_u32 v21, v21, v218\n"
      "v_bfe_i32 v218, v27, 14, 1\n"
      "v_and_b32 v218, s75, v218\n"
      "v_sub_u32 v22, v22, v218\n"
      "v_bfe_i32 v218, v27, 16, 1\n"
      "v_and_b32 v218, s75, v218\n"
      "v_sub_u32 v23, v23, v218\n"
      "v_bfe_i32 v218, v27, 18, 1\n"
      "v_and_b32 v218, s75, v218\n"
      "v_sub_u32 v25, v25, v218\n"
      "L_ax_25_%=:\n"
      "s_cmp_eq_u32 s76, 0\n"
      "s_cbranch_scc1 L_ax_26_%=\n"
      "v_bfe_i32 v218, v27, 11, 1\n"
      "v_and_b32 v218, s76, v218\n"
      "v_sub_u32_e64 v20, v20, v218\n"
      "v_bfe_i32 v218, v27, 13, 1\n"
      "v_and_b32 v218, s76, v218\n"
      "v_sub_u32 v21, v21, v218\n"
      "v_bfe_i32 v218, v27, 15, 1\n"
      "v_and_b32 v218, s76, v218\n"
      "v_sub_u32 v22, v22, v218\n"
      "v_bfe_i32 v218, v27, 17, 1\n"
      "v_and_b32 v218, s76, v218\n"
      "v_sub_u32 v23, v23, v218\n"
      "v_bfe_i32 v218, v27, 19, 1\n"
      "v_and_b32 v218, s76, v218\n"
      "v_sub_u32 v25, v25, v218\n"
      "L_ax_26_%=:\n"
      "s_or_b32 s48, s66, s69\n"
      "s_cmp_lg_u32 s48, 0\n"
      "s_cselect_b32 s48, 0x40000000, 0\n"
      "s_waitcnt lgkmcnt(0)\n"
      "s_nop 4\n"
      "s_branch L_back_28_%=\n"
      "L_rare_31_%=:\n"
      "s_nop 7\n"
      "s_waitcnt lgkmcnt(0)\n"
      "s_bitcmp1_b32 s85, 31\n"
      "s_cselect_b32 s75, 100270464, 0\n"
      "s_bitcmp1_b32 s85, 30\n"
      "s_cselect_b32 s76, 196224, 0\n"
      "s_and_b32 s74, s85, 0x3fffffff\n"
      "s_cmp_ge_u32 s85, 0x40000000\n"
      "s_cselect_b32 s74, s74, 0\n"
      "s_cselect_b32 s85, 0, s85\n"
      "s_sub_u32 s74, s74, s69\n"
      "s_add_u32 s69, s69, s74\n"
      "s_sub_u32 s75, s75, s70\n"
      "s_add_u32 s70, s70, s75\n"
      "s_sub_u32 s76, s76, s71\n"
      "s_add_u32 s71, s71, s76\n"
      "v_add_u32 v20, s74, v20\n"
      "v_add_u32 v21, s74, v21\n"
      "v_add_u32 v22, s74, v22\n"
      "v_add_u32 v23, s74, v23\n"
      "v_add_u32 v25, s74, v25\n"
      "s_cmp_eq_u32 s75, 0\n"
      "s_cbranch_scc1 L_ax_29_%=\n"
      "v_bfe_i32 v132, v27, 10, 1\n"
      "v_and_b32 v132, s75, v132\n"
      "v_sub_u32 v20, v20, v132\n"
      "v_bfe_i32 v132, v27, 12, 1\n"
      "v_and_b32 v132, s75, v132\n"
      "v_sub_u32 v21, v21, v132\n"
      "v_bfe_i32 v132, v27, 14, 1\n"
      "v_and_b32 v132, s75, v132\n"
      "v_sub_u32 v22, v22, v132\n"
      "v_bfe_i32 v132, v27, 16, 1\n"
      "v_and_b32 v132, s75, v132\n"
      "v_sub_u32 v23, v23, v132\n"
      "v_bfe_i32 v132, v27, 18, 1\n"
      "v_and_b32 v132, s75, v132\n"
      "v_sub_u32 v25, v25, v132\n"
      "L_ax_29_%=:\n"
      "s_cmp_eq_u32 s76, 0\n"
      "s_cbranch_scc1 L_ax_30_%=\n"
      "v_bfe_i32 v132, v27, 11, 1\n"
      "v_and_b32 v132, s76, v132\n"
      "v_sub_u32 v20, v20, v132\n"
      "v_bfe_i32 v132, v27, 13, 1\n"
      "v_and_b32 v132, s76, v132\n"
      "v_sub_u32 v21, v21, v132\n"
      "v_bfe_i32 v132, v27, 15, 1\n"
      "v_and_b32 v132, s76, v132\n"
      "v_sub_u32 v22, v22, v132\n"
      "v_bfe_i32 v132, v27, 17, 1\n"
      "v_and_b32 v132, s76, v132\n"
      "v_sub_u32 v23, v23, v132\n"
      "v_bfe_i32 v132, v27, 19, 1\n"
      "v_and_b32 v132, s76, v132\n"
      "v_sub_u32 v25, v25, v132\n"
      "L_ax_30_%=:\n"
      "s_or_b32 s48, s66, s69\n"
      "s_cmp_lg_u32 s48, 0\n"
      "s_cselect_b32 s48, 0x40000000, 0\n"
      "s_waitcnt lgkmcnt(0)\n"
      "s_nop 4\n"
      "s_branch L_back_32_%=\n"
      "L_rare_35_%=:\n"
      "s_nop 7\n"
      "s_waitcnt lgkmcnt(0)\n"
      "s_bitcmp1_b32 s46, 31\n"
      "s_cselect_b32 s75, 100270464, 0\n"
      "s_bitcmp1_b32 s46, 30\n"
      "s_cselect_b32 s76, 196224, 0\n"
      "s_and_b32 s74, s46, 0x3fffffff\n"
      "s_cmp_ge_u32 s46, 0x40000000\n"
      "s_cselect_b32 s74, s74, 0\n"
      "s_cselect_b32 s46, 0, s46\n"
      "s_sub_u32 s74, s74, s69\n"
      "s_add_u32 s69, s69, s74\n"
      "s_sub_u32 s75, s75, s70\n"
      "s_add_u32 s70, s70, s75\n"
      "s_sub_u32 s76, s76, s71\n"
      "s_add_u32 s71, s71, s76\n"
      "v_add_u32 v20, s74, v20\n"
      "v_add_u32 v21, s74, v21\n"
      "v_add_u32 v22, s74, v22\n"
      "v_add_u32 v23, s74, v23\n"
      "v_add_u32 v25, s74, v25\n"
      "s_cmp_eq_u32 s75, 0\n"
      "s_cbranch_scc1 L_ax_33_%=\n"
      "v_bfe_i32 v132, v27, 10, 1\n"
      "v_and_b32 v132, s75, v132\n"
      "v_sub_u32 v20, v20, v132\n"
      "v_bfe_i32 v132, v27, 12, 1\n"
      "v_and_b32 v132, s75, v132\n"
      "v_sub_u32 v21, v21, v132\n"
      "v_bfe_i32 v132, v27, 14, 1\n"
      "v_and_b32 v132, s75, v132\n"
      "v_sub_u32 v22, v22, v132\n"
      "v_bfe_i32 v132, v27, 16, 1\n"
      "v_and_b32 v132, s75, v132\n"
      "v_sub_u32 v23, v23, v132\n"
      "v_bfe_i32 v132, v27, 18, 1\n"
      "v_and_b32 v132, s75, v132\n"
      "v_sub_u32 v25, v25, v132\n"
      "L_ax_33_%=:\n"
      "s_cmp_eq_u32 s76, 0\n"
      "s_cbranch_scc1 L_ax_34_%=\n"
      "v_bfe_i32 v132, v27, 11, 1\n"
      "v_and_b32 v132, s76, v132\n"
      "v_sub_u32 v20, v20, v132\n"
      "v_bfe_i32 v132, v27, 13, 1\n"
      "v_and_b32 v132, s76, v132\n"
      "v_sub_u32 v21, v21, v132\n"
      "v_bfe_i32 v132, v27, 15, 1\n"
      "v_and_b32 v132, s76, v132\n"
      "v_sub_u32 v22, v22, v132\n"
      "v_bfe_i32 v132, v27, 17, 1\n"
      "v_and_b32 v132, s76, v132\n"
      "v_sub_u32 v23, v23, v132\n"
      "v_bfe_i32 v132, v27, 19, 1\n"
      "v_and_b32 v132, s76, v132\n"
      "v_sub_u32 v25, v25, v132\n"
      "L_ax_34_%=:\n"
      "s_or_b32 s48, s66, s69\n"
      "s_cmp_lg_u32 s48, 0\n"
      "s_cselect_b32 s48, 0x40000000, 0\n"
      "s_waitcnt lgkmcnt(0)\n"
      "s_nop 4\n"
      "s_branch L_back_36_%=\n"
      "L_rare_39_%=:\n"
      "s_nop 7\n"
      "s_waitcnt lgkmcnt(0)\n"
      "s_bitcmp1_b32 s46, 31\n"
      "s_cselect_b32 s75, 100270464, 0\n"
      "s_bitcmp1_b32 s46, 30\n"
      "s_cselect_b32 s76, 196224, 0\n"
      "s_and_b32 s74, s46, 0x3fffffff\n"
      "s_cmp_ge_u32 s46, 0x40000000\n"
      "s_cselect_b32 s74, s74, 0\n"
      "s_cselect_b32 s46, 0, s46\n"
      "s_sub_u32 s74, s74, s69\n"
      "s_add_u32 s69, s69, s74\n"
      "s_sub_u32 s75, s75, s70\n"
      "s_add_u32 s70, s70, s75\n"
      "s_sub_u32 s76, s76, s71\n"
      "s_add_u32 s71, s71, s76\n"
      "v_add_u32 v20, s74, v20\n"
      "v_add_u32 v21, s74, v21\n"
      "v_add_u32 v22, s74, v22\n"
      "v_add_u32 v23, s74, v23\n"
      "v_add_u32 v25, s74, v25\n"
      "s_cmp_eq_u32 s75, 0\n"
      "s_cbranch_scc1 L_ax_37_%=\n"
      "v_bfe_i32 v138, v27, 10, 1\n"
      "v_and_b32 v138, s75, v138\n"
      "v_sub_u32 v20, v20, v138\n"
      "v_bfe_i32 v138, v27, 12, 1\n"
      "v_and_b32 v138, s75, v138\n"
      "v_sub_u32 v21, v21, v138\n"
      "v_bfe_i32 v138, v27, 14, 1\n"
      "v_and_b32 v138, s75, v138\n"
      "v_sub_u32 v22, v22, v138\n"
      "v_bfe_i32 v138, v27, 16, 1\n"
      "v_and_b32 v138, s75, v138\n"
      "v_sub_u32 v23, v23, v138\n"
      "v_bfe_i32 v138, v27, 18, 1\n"
      "v_and_b32 v138, s75, v138\n"
      "v_sub_u32 v25, v25, v138\n"
      "L_ax_37_%=:\n"
      "s_cmp_eq_u32 s76, 0\n"
      "s_cbranch_scc1 L_ax_38_%=\n"
      "v_bfe_i32 v138, v27, 11, 1\n"
      "v_and_b32 v138, s76, v138\n"
      "v_sub_u32 v20, v20, v138\n"
      "v_bfe_i32 v138, v27, 13, 1\n"
      "v_and_b32 v138, s76, v138\n"
      "v_sub_u32 v21, v21, v138\n"
      "v_bfe_i32 v138, v27, 15, 1\n"
      "v_and_b32 v138, s76, v138\n"
      "v_sub_u32 v22, v22, v138\n"
      "v_bfe_i32 v138, v27, 17, 1\n"
      "v_and_b32 v138, s76, v138\n"
      "v_sub_u32 v23, v23, v138\n"
      "v_bfe_i32 v138, v27, 19, 1\n"
      "v_and_b32 v138, s76, v138\n"
      "v_sub_u32 v25, v25, v138\n"
      "L_ax_38_%=:\n"
      "s_or_b32 s48, s66, s69\n"
      "s_cmp_lg_u32 s48, 0\n"
      "s_cselect_b32 s48, 0x40000000, 0\n"
      "s_waitcnt lgkmcnt(0)\n"
      "s_nop 4\n"
      "s_branch L_back_40_%=\n"
      "L_cend_%=:\n"
      :
      : "s"(__builtin_amdgcn_kernarg_segment_ptr()), "s"(wave), "s"(blockIdx.x), "s"(CT_TAB), "s"(lds0)
      : "v0","v1","v2","v3","v4","v5","v6","v7","v8","v9","v10","v11","v12","v13","v14","v15","v16","v17","v18","v19","v20","v21","v22","v23","v24","v25","v26","v27","v28","v29","v30","v31","v32","v33","v34","v35","v36","v37","v38","v39","v40","v41","v42","v43","v44","v45","v46","v47","v48","v49","v50","v51","v52","v53","v54","v55","v56","v57","v58","v59","v60","v61","v62","v63","v64","v65","v66","v67","v68","v69","v70","v71","v72","v73","v74","v75","v76","v77","v78","v79","v80","v81","v82","v83","v84","v85","v86","v87","v88","v89","v90","v91","v92","v93","v94","v95","v96","v97","v98","v99","v100","v101","v102","v103","v104","v105","v106","v107","v108","v109","v110","v111","v112","v113","v114","v115","v116","v117","v118","v119","v120","v121","v122","v123","v124","v125","v126","v127","v128","v129","v130","v131","v132","v133","v134","v135","v136","v137","v138","v139","v140","v141","v142","v143","v144","v145","v146","v147","v148","v149","v150","v151","v152","v153","v154","v155","v156","v157","v158","v159","v160","v161","v162","v163","v164","v165","v166","v167","v168","v169","v170","v171","v172","v173","v174","v175","v176","v177","v178","v179","v180","v181","v182","v183","v184","v185","v186","v187","v188","v189","v190","v191","v192","v193","v194","v195","v196","v197","v198","v199","v200","v201","v202","v203","v204","v205","v206","v207","v208","v209","v210","v211","v212","v213","v214","v215","v216","v217","v218","v219","v220","v221","v222","v223","v224","v225","v226","v227","v228","v229","v230","v231","v232","v233","v234","v235","v236","v237","v238","v239","v240","v241","v242","v243","v244","v245","v246","v247","v248","v249","v250","v251","v252","v253","v254","v255","a0","a1","a2","a3","a4","a5","a6","a7","a8","a9","a10","a11","a12","a13","a14","a15","a16","a17","a18","a19","a20","a21","a22","a23","a24","a25","a26","a27","a28","a29","a30","a31","a32","a33","a34","a35","a36","a37","a38","a39","a40","a41","a42","a43","a44","a45","a46","a47","a48","a49","a50","a51","a52","a53","a54","a55","a56","a57","a58","a59","a60","a61","a62","a63","a64","a65","a66","a67","a68","a69","a70","a71","a72","a73","a74","a75","a76","a77","a78","a79","a80","a81","a82","a83","a84","a85","a86","a87","a88","a89","a90","a91","a92","a93","a94","a95","a96","a97","a98","a99","a100","a101","a102","a103","a104","a105","a106","a107","a108","a109","a110","a111","a112","a113","a114","a115","a116","a117","a118","a119","a120","a121","a122","a123","a124","a125","a126","a127","a128","a129","a130","a131","a132","a133","a134","a135","a136","a137","a138","a139","a140","a141","a142","a143","a144","a145","a146","a147","a148","a149","a150","a151","a152","a153","a154","a155","a156","a157","a158","a159","a160","a161","a162","a163","a164","a165","a166","a167","a168","a169","a170","a171","a172","a173","a174","a175","a176","a177","a178","a179","a180","a181","a182","a183","a184","a185","a186","a187","a188","a189","a190","a191","a192","a193","a194","a195","a196","a197","a198","a199","a200","a201","a202","a203","a204","a205","a206","a207","a208","a209","a210","a211","a212","a213","a214","a215","a216","a217","a218","a219","a220","a221","a222","a223","a224","a225","a226","a227","a228","a229","a230","a231","a232","a233","a234","a235","a236","a237","a238","a239","a240","a241","a242","a243","a244","a245","a246","a247","a248","a249","a250","a251","a252","a253","a254","a255","s16","s17","s18","s19","s20","s21","s22","s23","s24","s25","s26","s27","s28","s29","s30","s31","s33","s34","s35","s36","s37","s38","s39","s40","s41","s42","s43","s44","s45","s46","s47","s48","s49","s50","s51","s52","s53","s54","s55","s56","s57","s58","s59","s60","s61","s62","s63","s64","s65","s66","s67","s68","s69","s70","s71","s72","s73","s74","s75","s76","s77","s78","s79","s80","s81","s82","s83","s84","s85","s86","s87","s88","s89","s90","s91","s92","s93","s94","s95","s96","s97","s98","s99","s100","s101", "vcc", "scc", "memory");
}

}

extern "C" void kernel_launch(void* const* d_in, const int* in_sizes, int n_in, void* d_out, int out_size, void* d_ws,
                              size_t ws_size, hipStream_t stream) {
  const uint16_t* x = (const uint16_t*)d_in[0];
  const uint16_t* wqkv = (const uint16_t*)d_in[1];
  const uint16_t* wo = (const uint16_t*)d_in[2];
  const uint16_t* ls = (const uint16_t*)d_in[3];
  const uint16_t* w1 = (const uint16_t*)d_in[4];
  const uint16_t* w2 = (const uint16_t*)d_in[5];
  const uint16_t* rc = (const uint16_t*)d_in[6];
  const int* ridx = (const int*)d_in[7];
  const uint16_t* mask = (const uint16_t*)d_in[8];
  swin::swin_main<<<256, 256, 0, stream>>>(x, mask, (char*)d_ws, (uint16_t*)d_out, wqkv, wo, ls, w1, w2, rc, ridx);
}
```

```cpp
#include <hip/hip_runtime.h>
#include <hip/hip_bf16.h>
#include <stdint.h>

namespace swin {

constexpr unsigned CT_WORDS = 8192u;
constexpr unsigned LDS_BYTES = 163840u;

__device__ __attribute__((aligned(256))) const unsigned CT_TAB[CT_WORDS] = {0x4800u,0x4820u,0x4840u,0x4800u,0x4808u,0x4828u,0x4848u,0x4810u,0x4810u,0x4830u,0x4850u,0x4820u,0x4818u,0x4838u,0x4858u,0x4830u,0x4880u,0x48a0u,0x48c0u,0x4880u,0x4888u,0x48a8u,0x48c8u,0x4890u,0x4890u,0x48b0u,0x48d0u,0x48a0u,0x4898u,0x48b8u,0x48d8u,0x48b0u,0x4920u,0x4900u,0x4960u,0x4920u,0x4928u,0x4908u,0x4968u,0x4930u,0x4930u,0x4910u,0x4970u,0x4900u,0x4938u,0x4918u,0x4978u,0x4910u,0x49a0u,0x4980u,0x49e0u,0x49a0u,0x49a8u,0x4988u,0x49e8u,0x49b0u,0x49b0u,0x4990u,0x49f0u,0x4980u,0x49b8u,0x4998u,0x49f8u,0x4990u,0x4c40u,0x4c60u,0x4c00u,0x4a00u,0x4c48u,0x4c68u,0x4c08u,0x4a10u,0x4c50u,0x4c70u,0x4c10u,0x4a20u,0x4c58u,0x4c78u,0x4c18u,0x4a30u,0x4cc0u,0x4ce0u,0x4c80u,0x4a80u,0x4cc8u,0x4ce8u,0x4c88u,0x4a90u,0x4cd0u,0x4cf0u,0x4c90u,0x4aa0u,0x4cd8u,0x4cf8u,0x4c98u,0x4ab0u,0x4d60u,0x4d40u,0x4d20u,0x4b20u,0x4d68u,0x4d48u,0x4d28u,0x4b30u,0x4d70u,0x4d50u,0x4d30u,0x4b00u,0x4d78u,0x4d58u,0x4d38u,0x4b10u,0x4de0u,0x4dc0u,0x4da0u,0x4ba0u,0x4de8u,0x4dc8u,0x4da8u,0x4bb0u,0x4df0u,0x4dd0u,0x4db0u,0x4b80u,0x4df8u,0x4dd8u,0x4db8u,0x4b90u,0x5000u,0x5020u,0x5040u,0x4c40u,0x5008u,0x5028u,0x5048u,0x4c50u,0x5010u,0x5030u,0x5050u,0x4c60u,0x5018u,0x5038u,0x5058u,0x4c70u,0x5080u,0x50a0u,0x50c0u,0x4cc0u,0x5088u,0x50a8u,0x50c8u,0x4cd0u,0x5090u,0x50b0u,0x50d0u,0x4ce0u,0x5098u,0x50b8u,0x50d8u,0x4cf0u,0x5120u,0x5100u,0x5160u,0x4d60u,0x5128u,0x5108u,0x5168u,0x4d70u,0x5130u,0x5110u,0x5170u,0x4d40u,0x5138u,0x5118u,0x5178u,0x4d50u,0x51a0u,0x5180u,0x51e0u,0x4de0u,0x51a8u,0x5188u,0x51e8u,0x4df0u,0x51b0u,0x5190u,0x51f0u,0x4dc0u,0x51b8u,0x5198u,0x51f8u,0x4dd0u,0x5440u,0x5460u,0x5400u,0x4e40u,0x5448u,0x5468u,0x5408u,0x4e50u,0x5450u,0x5470u,0x5410u,0x4e60u,0x5458u,0x5478u,0x5418u,0x4e70u,0x54c0u,0x54e0u,0x5480u,0x4ec0u,0x54c8u,0x54e8u,0x5488u,0x4ed0u,0x54d0u,0x54f0u,0x5490u,0x4ee0u,0x54d8u,0x54f8u,0x5498u,0x4ef0u,0x5560u,0x5540u,0x5520u,0x4f60u,0x5568u,0x5548u,0x5528u,0x4f70u,0x5570u,0x5550u,0x5530u,0x4f40u,0x5578u,0x5558u,0x5538u,0x4f50u,0x55e0u,0x55c0u,0x55a0u,0x4fe0u,0x55e8u,0x55c8u,0x55a8u,0x4ff0u,0x55f0u,0x55d0u,0x55b0u,0x4fc0u,0x55f8u,0x55d8u,0x55b8u,0x4fd0u,0x4840u,0x1e800u,0x1eb00u,0x0u,0x4848u,0x1ec90u,0x1ef90u,0x10u,0x4850u,0x1f120u,0x1f420u,0x20u,0x4858u,0x1f5b0u,0x1f8b0u,0x30u,0x48c0u,0x1fa40u,0x1fd40u,0x40u,0x48c8u,0x1fed0u,0x201d0u,0x50u,0x48d0u,0x20360u,0x20660u,0x60u,0x48d8u,0x207f0u,0x20af0u,0x70u,0x4960u,0x20c80u,0x20f80u,0x180u,0x4968u,0x21110u,0x21410u,0x190u,0x4970u,0x215a0u,0x218a0u,0x1a0u,0x4978u,0x21a30u,0x21d30u,0x1b0u,0x49e0u,0x21ec0u,0x221c0u,0x1c0u,0x49e8u,0x22350u,0x22650u,0x1d0u,0x49f0u,0x227e0u,0x22ae0u,0x1e0u,0x49f8u,0x22c70u,0x22f70u,0x1f0u,0x4a40u,0x1e830u,0x1eb08u,0x320u,0x4a48u,0x1ecc0u,0x1ef98u,0x330u,0x4a50u,0x1f150u,0x1f428u,0x300u,0x4a58u,0x1f5e0u,0x1f8b8u,0x310u,0x4ac0u,0x1fa70u,0x1fd48u,0x360u,0x4ac8u,0x1ff00u,0x201d8u,0x370u,0x4ad0u,0x20390u,0x20668u,0x340u,0x4ad8u,0x20820u,0x20af8u,0x350u,0x4b60u,0x20cb0u,0x20f88u,0x4a0u,0x4b68u,0x21140u,0x21418u,0x4b0u,0x4b70u,0x215d0u,0x218a8u,0x480u,0x4b78u,0x21a60u,0x21d38u,0x490u,0x4be0u,0x21ef0u,0x221c8u,0x4e0u,0x4be8u,0x22380u,0x22658u,0x4f0u,0x4bf0u,0x22810u,0x22ae8u,0x4c0u,0x4bf8u,0x22ca0u,0x22f78u,0x4d0u,0x4c00u,0x1e860u,0x1eb10u,0x600u,0x4c08u,0x1ecf0u,0x1efa0u,0x610u,0x4c10u,0x1f180u,0x1f430u,0x620u,0x4c18u,0x1f610u,0x1f8c0u,0x630u,0x4c80u,0x1faa0u,0x1fd50u,0x640u,0x4c88u,0x1ff30u,0x201e0u,0x650u,0x4c90u,0x203c0u,0x20670u,0x660u,0x4c98u,0x20850u,0x20b00u,0x670u,0x4d20u,0x20ce0u,0x20f90u,0x780u,0x4d28u,0x21170u,0x21420u,0x790u,0x4d30u,0x21600u,0x218b0u,0x7a0u,0x4d38u,0x21a90u,0x21d40u,0x7b0u,0x4da0u,0x21f20u,0x221d0u,0x7c0u,0x4da8u,0x223b0u,0x22660u,0x7d0u,0x4db0u,0x22840u,0x22af0u,0x7e0u,0x4db8u,0x22cd0u,0x22f80u,0x7f0u,0x4e00u,0x1e890u,0x1eb18u,0x920u,0x4e08u,0x1ed20u,0x1efa8u,0x930u,0x4e10u,0x1f1b0u,0x1f438u,0x900u,0x4e18u,0x1f640u,0x1f8c8u,0x910u,0x4e80u,0x1fad0u,0x1fd58u,0x960u,0x4e88u,0x1ff60u,0x201e8u,0x970u,0x4e90u,0x203f0u,0x20678u,0x940u,0x4e98u,0x20880u,0x20b08u,0x950u,0x4f20u,0x20d10u,0x20f98u,0xaa0u,0x4f28u,0x211a0u,0x21428u,0xab0u,0x4f30u,0x21630u,0x218b8u,0xa80u,0x4f38u,0x21ac0u,0x21d48u,0xa90u,0x4fa0u,0x21f50u,0x221d8u,0xae0u,0x4fa8u,0x223e0u,0x22668u,0xaf0u,0x4fb0u,0x22870u,0x22af8u,0xac0u,0x4fb8u,0x22d00u,0x22f88u,0xad0u,0x0u,0x40u,0x9000u,0x9000u,0x80u,0xc0u,0x9010u,0x9010u,0x110u,0x150u,0x9020u,0x9020u,0x190u,0x1d0u,0x9030u,0x9030u,0x220u,0x260u,0x9040u,0x9040u,0x2a0u,0x2e0u,0x9050u,0x9050u,0x330u,0x370u,0x9060u,0x9060u,0x3b0u,0x3f0u,0x9070u,0x9070u,0x440u,0x400u,0x9080u,0x9080u,0x4c0u,0x480u,0x9090u,0x9090u,0x550u,0x510u,0x90a0u,0x90a0u,0x5d0u,0x590u,0x90b0u,0x90b0u,0x660u,0x620u,0x90c0u,0x90c0u,0x6e0u,0x6a0u,0x90d0u,0x90d0u,0x770u,0x730u,0x90e0u,0x90e0u,0x7f0u,0x7b0u,0x90f0u,0x90f0u,0x10u,0x50u,0x9300u,0x9300u,0x90u,0xd0u,0x9310u,0x9310u,0x100u,0x140u,0x9320u,0x9320u,0x180u,0x1c0u,0x9330u,0x9330u,0x230u,0x270u,0x9340u,0x9340u,0x2b0u,0x2f0u,0x9350u,0x9350u,0x320u,0x360u,0x9360u,0x9360u,0x3a0u,0x3e0u,0x9370u,0x9370u,0x450u,0x410u,0x9380u,0x9380u,0x4d0u,0x490u,0x9390u,0x9390u,0x540u,0x500u,0x93a0u,0x93a0u,0x5c0u,0x580u,0x93b0u,0x93b0u,0x670u,0x630u,0x93c0u,0x93c0u,0x6f0u,0x6b0u,0x93d0u,0x93d0u,0x760u,0x720u,0x93e0u,0x93e0u,0x7e0u,0x7a0u,0x93f0u,0x93f0u,0x20u,0x60u,0x9600u,0x9600u,0xa0u,0xe0u,0x9610u,0x9610u,0x130u,0x170u,0x9620u,0x9620u,0x1b0u,0x1f0u,0x9630u,0x9630u,0x200u,0x240u,0x9640u,0x9640u,0x280u,0x2c0u,0x9650u,0x9650u,0x310u,0x350u,0x9660u,0x9660u,0x390u,0x3d0u,0x9670u,0x9670u,0x460u,0x420u,0x9680u,0x9680u,0x4e0u,0x4a0u,0x9690u,0x9690u,0x570u,0x530u,0x96a0u,0x96a0u,0x5f0u,0x5b0u,0x96b0u,0x96b0u,0x640u,0x600u,0x96c0u,0x96c0u,0x6c0u,0x680u,0x96d0u,0x96d0u,0x750u,0x710u,0x96e0u,0x96e0u,0x7d0u,0x790u,0x96f0u,0x96f0u,0x30u,0x70u,0x9900u,0x9900u,0xb0u,0xf0u,0x9910u,0x9910u,0x120u,0x160u,0x9920u,0x9920u,0x1a0u,0x1e0u,0x9930u,0x9930u,0x210u,0x250u,0x9940u,0x9940u,0x290u,0x2d0u,0x9950u,0x9950u,0x300u,0x340u,0x9960u,0x9960u,0x380u,0x3c0u,0x9970u,0x9970u,0x470u,0x430u,0x9980u,0x9980u,0x4f0u,0x4b0u,0x9990u,0x9990u,0x560u,0x520u,0x99a0u,0x99a0u,0x5e0u,0x5a0u,0x99b0u,0x99b0u,0x650u,0x610u,0x99c0u,0x99c0u,0x6d0u,0x690u,0x99d0u,0x99d0u,0x740u,0x700u,0x99e0u,0x99e0u,0x7c0u,0x780u,0x99f0u,0x99f0u,0x9c00u,0x12000u,0x12040u,0x12000u,0x9c10u,0x12190u,0x121d0u,0x12010u,0x9c20u,0x12320u,0x12360u,0x12020u,0x9c30u,0x124b0u,0x124f0u,0x12030u,0x9c40u,0x12640u,0x12600u,0x12040u,0x9c50u,0x127d0u,0x12790u,0x12050u,0x9c60u,0x12960u,0x12920u,0x12060u,0x9c70u,0x12af0u,0x12ab0u,0x12070u,0x9c80u,0x12c00u,0x12c40u,0x12080u,0x9c90u,0x12d90u,0x12dd0u,0x12090u,0x9ca0u,0x12f20u,0x12f60u,0x120a0u,0x9cb0u,0x130b0u,0x130f0u,0x120b0u,0x9cc0u,0x13240u,0x13200u,0x120c0u,0x9cd0u,0x133d0u,0x13390u,0x120d0u,0x9ce0u,0x13560u,0x13520u,0x120e0u,0x9cf0u,0x136f0u,0x136b0u,0x120f0u,0x9f00u,0x12010u,0x12048u,0x12100u,0x9f10u,0x12180u,0x121d8u,0x12110u,0x9f20u,0x12330u,0x12368u,0x12120u,0x9f30u,0x124a0u,0x124f8u,0x12130u,0x9f40u,0x12650u,0x12608u,0x12140u,0x9f50u,0x127c0u,0x12798u,0x12150u,0x9f60u,0x12970u,0x12928u,0x12160u,0x9f70u,0x12ae0u,0x12ab8u,0x12170u,0x9f80u,0x12c10u,0x12c48u,0x12180u,0x9f90u,0x12d80u,0x12dd8u,0x12190u,0x9fa0u,0x12f30u,0x12f68u,0x121a0u,0x9fb0u,0x130a0u,0x130f8u,0x121b0u,0x9fc0u,0x13250u,0x13208u,0x121c0u,0x9fd0u,0x133c0u,0x13398u,0x121d0u,0x9fe0u,0x13570u,0x13528u,0x121e0u,0x9ff0u,0x136e0u,0x136b8u,0x121f0u,0xa200u,0x12020u,0x12050u,0x12200u,0xa210u,0x121b0u,0x121c0u,0x12210u,0xa220u,0x12300u,0x12370u,0x12220u,0xa230u,0x12490u,0x124e0u,0x12230u,0xa240u,0x12660u,0x12610u,0x12240u,0xa250u,0x127f0u,0x12780u,0x12250u,0xa260u,0x12940u,0x12930u,0x12260u,0xa270u,0x12ad0u,0x12aa0u,0x12270u,0xa280u,0x12c20u,0x12c50u,0x12280u,0xa290u,0x12db0u,0x12dc0u,0x12290u,0xa2a0u,0x12f00u,0x12f70u,0x122a0u,0xa2b0u,0x13090u,0x130e0u,0x122b0u,0xa2c0u,0x13260u,0x13210u,0x122c0u,0xa2d0u,0x133f0u,0x13380u,0x122d0u,0xa2e0u,0x13540u,0x13530u,0x122e0u,0xa2f0u,0x136d0u,0x136a0u,0x122f0u,0xa500u,0x12030u,0x12058u,0x12300u,0xa510u,0x121a0u,0x121c8u,0x12310u,0xa520u,0x12310u,0x12378u,0x12320u,0xa530u,0x12480u,0x124e8u,0x12330u,0xa540u,0x12670u,0x12618u,0x12340u,0xa550u,0x127e0u,0x12788u,0x12350u,0xa560u,0x12950u,0x12938u,0x12360u,0xa570u,0x12ac0u,0x12aa8u,0x12370u,0xa580u,0x12c30u,0x12c58u,0x12380u,0xa590u,0x12da0u,0x12dc8u,0x12390u,0xa5a0u,0x12f10u,0x12f78u,0x123a0u,0xa5b0u,0x13080u,0x130e8u,0x123b0u,0xa5c0u,0x13270u,0x13218u,0x123c0u,0xa5d0u,0x133e0u,0x13388u,0x123d0u,0xa5e0u,0x13550u,0x13538u,0x123e0u,0xa5f0u,0x136c0u,0x136a8u,0x123f0u,0x4800u,0x1c300u,0x1e800u,0x1eb00u,0x4880u,0x1c310u,0x100000u,0x1eb02u,0x4910u,0x1c320u,0x100000u,0x1eb04u,0x4990u,0x1c330u,0x100000u,0x1eb06u,0x4a20u,0x1c340u,0x1e800u,0x1eb08u,0x4aa0u,0x1c350u,0x100000u,0x1eb0au,0x4b30u,0x1c360u,0x100000u,0x1eb0cu,0x4bb0u,0x1c370u,0x100000u,0x1eb0eu,0x4c40u,0x1c380u,0x1e800u,0x1eb10u,0x4cc0u,0x1c390u,0x100000u,0x1eb12u,0x4d50u,0x1c3a0u,0x100000u,0x1eb14u,0x4dd0u,0x1c3b0u,0x100000u,0x1eb16u,0x4e60u,0x1c3c0u,0x1e800u,0x1eb18u,0x4ee0u,0x1c3d0u,0x100000u,0x1eb1au,0x4f70u,0x1c3e0u,0x100000u,0x1eb1cu,0x4ff0u,0x1c3f0u,0x100000u,0x1eb1eu,0x4810u,0x1c400u,0x1e830u,0x1eb00u,0x4890u,0x1c410u,0x100000u,0x1eb02u,0x4900u,0x1c420u,0x100000u,0x1eb04u,0x4980u,0x1c430u,0x100000u,0x1eb06u,0x4a30u,0x1c440u,0x1e830u,0x1eb08u,0x4ab0u,0x1c450u,0x100000u,0x1eb0au,0x4b20u,0x1c460u,0x100000u,0x1eb0cu,0x4ba0u,0x1c470u,0x100000u,0x1eb0eu,0x4c50u,0x1c480u,0x1e830u,0x1eb10u,0x4cd0u,0x1c490u,0x100000u,0x1eb12u,0x4d40u,0x1c4a0u,0x100000u,0x1eb14u,0x4dc0u,0x1c4b0u,0x100000u,0x1eb16u,0x4e70u,0x1c4c0u,0x1e830u,0x1eb18u,0x4ef0u,0x1c4d0u,0x100000u,0x1eb1au,0x4f60u,0x1c4e0u,0x100000u,0x1eb1cu,0x4fe0u,0x1c4f0u,0x100000u,0x1eb1eu,0x4820u,0x1c500u,0x1e860u,0x1eb00u,0x48a0u,0x1c510u,0x100000u,0x1eb02u,0x4930u,0x1c520u,0x100000u,0x1eb04u,0x49b0u,0x1c530u,0x100000u,0x1eb06u,0x4a00u,0x1c540u,0x1e860u,0x1eb08u,0x4a80u,0x1c550u,0x100000u,0x1eb0au,0x4b10u,0x1c560u,0x100000u,0x1eb0cu,0x4b90u,0x1c570u,0x100000u,0x1eb0eu,0x4c60u,0x1c580u,0x1e860u,0x1eb10u,0x4ce0u,0x1c590u,0x100000u,0x1eb12u,0x4d70u,0x1c5a0u,0x100000u,0x1eb14u,0x4df0u,0x1c5b0u,0x100000u,0x1eb16u,0x4e40u,0x1c5c0u,0x1e860u,0x1eb18u,0x4ec0u,0x1c5d0u,0x100000u,0x1eb1au,0x4f50u,0x1c5e0u,0x100000u,0x1eb1cu,0x4fd0u,0x1c5f0u,0x100000u,0x1eb1eu,0x4830u,0x1c600u,0x1e890u,0x1eb00u,0x48b0u,0x1c610u,0x100000u,0x1eb02u,0x4920u,0x1c620u,0x100000u,0x1eb04u,0x49a0u,0x1c630u,0x100000u,0x1eb06u,0x4a10u,0x1c640u,0x1e890u,0x1eb08u,0x4a90u,0x1c650u,0x100000u,0x1eb0au,0x4b00u,0x1c660u,0x100000u,0x1eb0cu,0x4b80u,0x1c670u,0x100000u,0x1eb0eu,0x4c70u,0x1c680u,0x1e890u,0x1eb10u,0x4cf0u,0x1c690u,0x100000u,0x1eb12u,0x4d60u,0x1c6a0u,0x100000u,0x1eb14u,0x4de0u,0x1c6b0u,0x100000u,0x1eb16u,0x4e50u,0x1c6c0u,0x1e890u,0x1eb18u,0x4ed0u,0x1c6d0u,0x100000u,0x1eb1au,0x4f40u,0x1c6e0u,0x100000u,0x1eb1cu,0x4fc0u,0x1c6f0u,0x100000u,0x1eb1eu,0x24800u,0x1d300u,0x1d300u,0xc0100u,0x24802u,0x1d302u,0x100000u,0xc0110u,0x24804u,0x1d304u,0x100000u,0xc0120u,0x24806u,0x1d306u,0x100000u,0xc0130u,0x24a00u,0x1d310u,0x1d300u,0xc0140u,0x24a02u,0x1d312u,0x100000u,0xc0150u,0x24a04u,0x1d314u,0x100000u,0xc0160u,0x24a06u,0x1d316u,0x100000u,0xc0170u,0x24c00u,0x1d320u,0x1d300u,0xc0280u,0x24c02u,0x1d322u,0x100000u,0xc0290u,0x24c04u,0x1d324u,0x100000u,0xc02a0u,0x24c06u,0x1d326u,0x100000u,0xc02b0u,0x24e00u,0x1d330u,0x1d300u,0xc02c0u,0x24e02u,0x1d332u,0x100000u,0xc02d0u,0x24e04u,0x1d334u,0x100000u,0xc02e0u,0x24e06u,0x1d336u,0x100000u,0xc02f0u,0x24808u,0x1d308u,0x1d310u,0xc0410u,0x2480au,0x1d30au,0x100000u,0xc0400u,0x2480cu,0x1d30cu,0x100000u,0xc0430u,0x2480eu,0x1d30eu,0x100000u,0xc0420u,0x24a08u,0x1d318u,0x1d310u,0xc0450u,0x24a0au,0x1d31au,0x100000u,0xc0440u,0x24a0cu,0x1d31cu,0x100000u,0xc0470u,0x24a0eu,0x1d31eu,0x100000u,0xc0460u,0x24c08u,0x1d328u,0x1d310u,0xef990u,0x24c0au,0x1d32au,0x100000u,0xef980u,0x24c0cu,0x1d32cu,0x100000u,0xef9b0u,0x24c0eu,0x1d32eu,0x100000u,0xef9a0u,0x24e08u,0x1d338u,0x1d310u,0xef9d0u,0x24e0au,0x1d33au,0x100000u,0xef9c0u,0x24e0cu,0x1d33cu,0x100000u,0xef9f0u,0x24e0eu,0x1d33eu,0x100000u,0xef9e0u,0x25000u,0x1d340u,0x1d320u,0xefb20u,0x25002u,0x1d342u,0x100000u,0xefb30u,0x25004u,0x1d344u,0x100000u,0xefb00u,0x25006u,0x1d346u,0x100000u,0xefb10u,0x25200u,0x1d350u,0x1d320u,0xefb60u,0x25202u,0x1d352u,0x100000u,0xefb70u,0x25204u,0x1d354u,0x100000u,0xefb40u,0x25206u,0x1d356u,0x100000u,0xefb50u,0x25400u,0x1d360u,0x1d320u,0xefca0u,0x25402u,0x1d362u,0x100000u,0xefcb0u,0x25404u,0x1d364u,0x100000u,0xefc80u,0x25406u,0x1d366u,0x100000u,0xefc90u,0x25600u,0x1d370u,0x1d320u,0xefce0u,0x25602u,0x1d372u,0x100000u,0xefcf0u,0x25604u,0x1d374u,0x100000u,0xefcc0u,0x25606u,0x1d376u,0x100000u,0xefcd0u,0x100000u,0x1d348u,0x1d330u,0xefe30u,0x100000u,0x1d34au,0x100000u,0xefe20u,0x100000u,0x1d34cu,0x100000u,0xefe10u,0x100000u,0x1d34eu,0x100000u,0xefe00u,0x100000u,0x1d358u,0x1d330u,0xefe70u,0x100000u,0x1d35au,0x100000u,0xefe60u,0x100000u,0x1d35cu,0x100000u,0xefe50u,0x100000u,0x1d35eu,0x100000u,0xefe40u,0x100000u,0x1d368u,0x1d330u,0xeffb0u,0x100000u,0x1d36au,0x100000u,0xeffa0u,0x100000u,0x1d36cu,0x100000u,0xeff90u,0x100000u,0x1d36eu,0x100000u,0xeff80u,0x100000u,0x1d378u,0x1d330u,0xefff0u,0x100000u,0x1d37au,0x100000u,0xeffe0u,0x100000u,0x1d37cu,0x100000u,0xeffd0u,0x100000u,0x1d37eu,0x100000u,0xeffc0u,0x0u,0x30420u,0x8fc50u,0xc0000u,0x10u,0x30430u,0x8fc40u,0xc0010u,0x20u,0x30400u,0x8fc70u,0xc0020u,0x30u,0x30410u,0x8fc60u,0xc0030u,0x40u,0x30460u,0x8fc10u,0xc0040u,0x50u,0x30470u,0x8fc00u,0xc0050u,0x60u,0x30440u,0x8fc30u,0xc0060u,0x70u,0x30450u,0x8fc20u,0xc0070u,0x80u,0x304b0u,0x8fcd0u,0xc0080u,0x90u,0x304a0u,0x8fcc0u,0xc0090u,0xa0u,0x30490u,0x8fcf0u,0xc00a0u,0xb0u,0x30480u,0x8fce0u,0xc00b0u,0xc0u,0x304f0u,0x8fc90u,0xc00c0u,0xd0u,0x304e0u,0x8fc80u,0xc00d0u,0xe0u,0x304d0u,0x8fcb0u,0xc00e0u,0xf0u,0x304c0u,0x8fca0u,0xc00f0u,0x100u,0x30530u,0x8fd60u,0xc0100u,0x110u,0x30520u,0x8fd70u,0xc0110u,0x120u,0x30510u,0x8fd40u,0xc0120u,0x130u,0x30500u,0x8fd50u,0xc0130u,0x140u,0x30570u,0x8fd20u,0xc0140u,0x150u,0x30560u,0x8fd30u,0xc0150u,0x160u,0x30550u,0x8fd00u,0xc0160u,0x170u,0x30540u,0x8fd10u,0xc0170u,0x190u,0x305b0u,0x8fde0u,0xc0190u,0x180u,0x305a0u,0x8fdf0u,0xc0180u,0x1b0u,0x30590u,0x8fdc0u,0xc01b0u,0x1a0u,0x30580u,0x8fdd0u,0xc01a0u,0x1d0u,0x305f0u,0x8fda0u,0xc01d0u,0x1c0u,0x305e0u,0x8fdb0u,0xc01c0u,0x1f0u,0x305d0u,0x8fd80u,0xc01f0u,0x1e0u,0x305c0u,0x8fd90u,0xc01e0u,0x210u,0x30640u,0x8fe60u,0xc0210u,0x200u,0x30650u,0x8fe70u,0xc0200u,0x230u,0x30660u,0x8fe40u,0xc0230u,0x220u,0x30670u,0x8fe50u,0xc0220u,0x250u,0x30600u,0x8fe20u,0xc0250u,0x240u,0x30610u,0x8fe30u,0xc0240u,0x270u,0x30620u,0x8fe00u,0xc0270u,0x260u,0x30630u,0x8fe10u,0xc0260u,0x290u,0x306c0u,0x8fef0u,0xc0290u,0x280u,0x306d0u,0x8fee0u,0xc0280u,0x2b0u,0x306e0u,0x8fed0u,0xc02b0u,0x2a0u,0x306f0u,0x8fec0u,0xc02a0u,0x2d0u,0x30680u,0x8feb0u,0xc02d0u,0x2c0u,0x30690u,0x8fea0u,0xc02c0u,0x2f0u,0x306a0u,0x8fe90u,0xc02f0u,0x2e0u,0x306b0u,0x8fe80u,0xc02e0u,0x320u,0x30740u,0x8ff70u,0xc0320u,0x330u,0x30750u,0x8ff60u,0xc0330u,0x300u,0x30760u,0x8ff50u,0xc0300u,0x310u,0x30770u,0x8ff40u,0xc0310u,0x360u,0x30700u,0x8ff30u,0xc0360u,0x370u,0x30710u,0x8ff20u,0xc0370u,0x340u,0x30720u,0x8ff10u,0xc0340u,0x350u,0x30730u,0x8ff00u,0xc0350u,0x3a0u,0x307d0u,0x8fff0u,0xc03a0u,0x3b0u,0x307c0u,0x8ffe0u,0xc03b0u,0x380u,0x307f0u,0x8ffd0u,0xc0380u,0x390u,0x307e0u,0x8ffc0u,0xc0390u,0x3e0u,0x30790u,0x8ffb0u,0xc03e0u,0x3f0u,0x30780u,0x8ffa0u,0xc03f0u,0x3c0u,0x307b0u,0x8ff90u,0xc03c0u,0x3d0u,0x307a0u,0x8ff80u,0xc03d0u,0x0u,0x11f820u,0x0u,0x7030cu,0x0u,0x11f830u,0x0u,0x7030cu,0x0u,0x11f800u,0x0u,0x7030cu,0x0u,0x11f810u,0x0u,0x7030cu,0x0u,0x11f860u,0x0u,0x7030cu,0x0u,0x11f870u,0x0u,0x7030cu,0x0u,0x11f840u,0x0u,0x7030cu,0x0u,0x11f850u,0x0u,0x7030cu,0x0u,0x11f8b0u,0x0u,0x7230cu,0x0u,0x11f8a0u,0x0u,0x7230cu,0x0u,0x11f890u,0x0u,0x7230cu,0x0u,0x11f880u,0x0u,0x7230cu,0x0u,0x11f8f0u,0x0u,0x7230cu,0x0u,0x11f8e0u,0x0u,0x7230cu,0x0u,0x11f8d0u,0x0u,0x7230cu,0x0u,0x11f8c0u,0x0u,0x7230cu,0x0u,0x11f930u,0x0u,0x7232cu,0x0u,0x11f920u,0x0u,0x7232cu,0x0u,0x11f910u,0x0u,0x7232cu,0x0u,0x11f900u,0x0u,0x7232cu,0x0u,0x11f970u,0x0u,0x7232cu,0x0u,0x11f960u,0x0u,0x7232cu,0x0u,0x11f950u,0x0u,0x7232cu,0x0u,0x11f940u,0x0u,0x7232cu,0x0u,0x11f9b0u,0x0u,0x72124u,0x0u,0x11f9a0u,0x0u,0x72124u,0x0u,0x11f990u,0x0u,0x72124u,0x0u,0x11f980u,0x0u,0x72124u,0x0u,0x11f9f0u,0x0u,0x72124u,0x0u,0x11f9e0u,0x0u,0x72124u,0x0u,0x11f9d0u,0x0u,0x72124u,0x0u,0x11f9c0u,0x0u,0x72124u,0x0u,0x11fa40u,0x0u,0x721a6u,0x0u,0x11fa50u,0x0u,0x721a6u,0x0u,0x11fa60u,0x0u,0x721a6u,0x0u,0x11fa70u,0x0u,0x721a6u,0x0u,0x11fa00u,0x0u,0x721a6u,0x0u,0x11fa10u,0x0u,0x721a6u,0x0u,0x11fa20u,0x0u,0x721a6u,0x0u,0x11fa30u,0x0u,0x721a6u,0x0u,0x11fac0u,0x0u,0x72186u,0x0u,0x11fad0u,0x0u,0x72186u,0x0u,0x11fae0u,0x0u,0x72186u,0x0u,0x11faf0u,0x0u,0x72186u,0x0u,0x11fa80u,0x0u,0x72186u,0x0u,0x11fa90u,0x0u,0x72186u,0x0u,0x11faa0u,0x0u,0x72186u,0x0u,0x11fab0u,0x0u,0x72186u,0x0u,0x11fb40u,0x0u,0x72186u,0x0u,0x11fb50u,0x0u,0x72186u,0x0u,0x11fb60u,0x0u,0x72186u,0x0u,0x11fb70u,0x0u,0x72186u,0x0u,0x11fb00u,0x0u,0x72186u,0x0u,0x11fb10u,0x0u,0x72186u,0x0u,0x11fb20u,0x0u,0x72186u,0x0u,0x11fb30u,0x0u,0x72186u,0x0u,0x11fbd0u,0x0u,0x7230cu,0x0u,0x11fbc0u,0x0u,0x7230cu,0x0u,0x11fbf0u,0x0u,0x7230cu,0x0u,0x11fbe0u,0x0u,0x7230cu,0x0u,0x11fb90u,0x0u,0x7230cu,0x0u,0x11fb80u,0x0u,0x7230cu,0x0u,0x11fbb0u,0x0u,0x7230cu,0x0u,0x11fba0u,0x0u,0x7230cu,0x5800u,0x5820u,0x4860u,0x4860u,0x5808u,0x5828u,0x4868u,0x4870u,0x5810u,0x5830u,0x4870u,0x5800u,0x5818u,0x5838u,0x4878u,0x5810u,0x5880u,0x58a0u,0x48e0u,0x48e0u,0x5888u,0x58a8u,0x48e8u,0x48f0u,0x5890u,0x58b0u,0x48f0u,0x5880u,0x5898u,0x58b8u,0x48f8u,0x5890u,0x5920u,0x5900u,0x4940u,0x4940u,0x5928u,0x5908u,0x4948u,0x4950u,0x5930u,0x5910u,0x4950u,0x5920u,0x5938u,0x5918u,0x4958u,0x5930u,0x59a0u,0x5980u,0x49c0u,0x49c0u,0x59a8u,0x5988u,0x49c8u,0x49d0u,0x59b0u,0x5990u,0x49d0u,0x59a0u,0x59b8u,0x5998u,0x49d8u,0x59b0u,0x5c40u,0x5c60u,0x4c20u,0x4a60u,0x5c48u,0x5c68u,0x4c28u,0x4a70u,0x5c50u,0x5c70u,0x4c30u,0x5a00u,0x5c58u,0x5c78u,0x4c38u,0x5a10u,0x5cc0u,0x5ce0u,0x4ca0u,0x4ae0u,0x5cc8u,0x5ce8u,0x4ca8u,0x4af0u,0x5cd0u,0x5cf0u,0x4cb0u,0x5a80u,0x5cd8u,0x5cf8u,0x4cb8u,0x5a90u,0x5d60u,0x5d40u,0x4d00u,0x4b40u,0x5d68u,0x5d48u,0x4d08u,0x4b50u,0x5d70u,0x5d50u,0x4d10u,0x5b20u,0x5d78u,0x5d58u,0x4d18u,0x5b30u,0x5de0u,0x5dc0u,0x4d80u,0x4bc0u,0x5de8u,0x5dc8u,0x4d88u,0x4bd0u,0x5df0u,0x5dd0u,0x4d90u,0x5ba0u,0x5df8u,0x5dd8u,0x4d98u,0x5bb0u,0x6000u,0x6020u,0x5060u,0x4c20u,0x6008u,0x6028u,0x5068u,0x4c30u,0x6010u,0x6030u,0x5070u,0x5c40u,0x6018u,0x6038u,0x5078u,0x5c50u,0x6080u,0x60a0u,0x50e0u,0x4ca0u,0x6088u,0x60a8u,0x50e8u,0x4cb0u,0x6090u,0x60b0u,0x50f0u,0x5cc0u,0x6098u,0x60b8u,0x50f8u,0x5cd0u,0x6120u,0x6100u,0x5140u,0x4d00u,0x6128u,0x6108u,0x5148u,0x4d10u,0x6130u,0x6110u,0x5150u,0x5d60u,0x6138u,0x6118u,0x5158u,0x5d70u,0x61a0u,0x6180u,0x51c0u,0x4d80u,0x61a8u,0x6188u,0x51c8u,0x4d90u,0x61b0u,0x6190u,0x51d0u,0x5de0u,0x61b8u,0x6198u,0x51d8u,0x5df0u,0x6440u,0x6460u,0x5420u,0x4e20u,0x6448u,0x6468u,0x5428u,0x4e30u,0x6450u,0x6470u,0x5430u,0x5e40u,0x6458u,0x6478u,0x5438u,0x5e50u,0x64c0u,0x64e0u,0x54a0u,0x4ea0u,0x64c8u,0x64e8u,0x54a8u,0x4eb0u,0x64d0u,0x64f0u,0x54b0u,0x5ec0u,0x64d8u,0x64f8u,0x54b8u,0x5ed0u,0x6560u,0x6540u,0x5500u,0x4f00u,0x6568u,0x6548u,0x5508u,0x4f10u,0x6570u,0x6550u,0x5510u,0x5f60u,0x6578u,0x6558u,0x5518u,0x5f70u,0x65e0u,0x65c0u,0x5580u,0x4f80u,0x65e8u,0x65c8u,0x5588u,0x4f90u,0x65f0u,0x65d0u,0x5590u,0x5fe0u,0x65f8u,0x65d8u,0x5598u,0x5ff0u,0x5820u,0x1e8c0u,0x1eb60u,0xc40u,0x5828u,0x1ed50u,0x1eff0u,0xc50u,0x5830u,0x1f1e0u,0x1f480u,0xc60u,0x5838u,0x1f670u,0x1f910u,0xc70u,0x58a0u,0x1fb00u,0x1fda0u,0xc00u,0x58a8u,0x1ff90u,0x20230u,0xc10u,0x58b0u,0x20420u,0x206c0u,0xc20u,0x58b8u,0x208b0u,0x20b50u,0xc30u,0x5900u,0x20d40u,0x20fe0u,0xdc0u,0x5908u,0x211d0u,0x21470u,0xdd0u,0x5910u,0x21660u,0x21900u,0xde0u,0x5918u,0x21af0u,0x21d90u,0xdf0u,0x5980u,0x21f80u,0x22220u,0xd80u,0x5988u,0x22410u,0x226b0u,0xd90u,0x5990u,0x228a0u,0x22b40u,0xda0u,0x5998u,0x22d30u,0x22fd0u,0xdb0u,0x5a20u,0x1e8f0u,0x1eb68u,0xf60u,0x5a28u,0x1ed80u,0x1eff8u,0xf70u,0x5a30u,0x1f210u,0x1f488u,0xf40u,0x5a38u,0x1f6a0u,0x1f918u,0xf50u,0x5aa0u,0x1fb30u,0x1fda8u,0xf20u,0x5aa8u,0x1ffc0u,0x20238u,0xf30u,0x5ab0u,0x20450u,0x206c8u,0xf00u,0x5ab8u,0x208e0u,0x20b58u,0xf10u,0x5b00u,0x20d70u,0x20fe8u,0x10e0u,0x5b08u,0x21200u,0x21478u,0x10f0u,0x5b10u,0x21690u,0x21908u,0x10c0u,0x5b18u,0x21b20u,0x21d98u,0x10d0u,0x5b80u,0x21fb0u,0x22228u,0x10a0u,0x5b88u,0x22440u,0x226b8u,0x10b0u,0x5b90u,0x228d0u,0x22b48u,0x1080u,0x5b98u,0x22d60u,0x22fd8u,0x1090u,0x5c60u,0x1e920u,0x1eb70u,0x1240u,0x5c68u,0x1edb0u,0x1f000u,0x1250u,0x5c70u,0x1f240u,0x1f490u,0x1260u,0x5c78u,0x1f6d0u,0x1f920u,0x1270u,0x5ce0u,0x1fb60u,0x1fdb0u,0x1200u,0x5ce8u,0x1fff0u,0x20240u,0x1210u,0x5cf0u,0x20480u,0x206d0u,0x1220u,0x5cf8u,0x20910u,0x20b60u,0x1230u,0x5d40u,0x20da0u,0x20ff0u,0x13c0u,0x5d48u,0x21230u,0x21480u,0x13d0u,0x5d50u,0x216c0u,0x21910u,0x13e0u,0x5d58u,0x21b50u,0x21da0u,0x13f0u,0x5dc0u,0x21fe0u,0x22230u,0x1380u,0x5dc8u,0x22470u,0x226c0u,0x1390u,0x5dd0u,0x22900u,0x22b50u,0x13a0u,0x5dd8u,0x22d90u,0x22fe0u,0x13b0u,0x5e60u,0x1e950u,0x1eb78u,0x1560u,0x5e68u,0x1ede0u,0x1f008u,0x1570u,0x5e70u,0x1f270u,0x1f498u,0x1540u,0x5e78u,0x1f700u,0x1f928u,0x1550u,0x5ee0u,0x1fb90u,0x1fdb8u,0x1520u,0x5ee8u,0x20020u,0x20248u,0x1530u,0x5ef0u,0x204b0u,0x206d8u,0x1500u,0x5ef8u,0x20940u,0x20b68u,0x1510u,0x5f40u,0x20dd0u,0x20ff8u,0x16e0u,0x5f48u,0x21260u,0x21488u,0x16f0u,0x5f50u,0x216f0u,0x21918u,0x16c0u,0x5f58u,0x21b80u,0x21da8u,0x16d0u,0x5fc0u,0x22010u,0x22238u,0x16a0u,0x5fc8u,0x224a0u,0x226c8u,0x16b0u,0x5fd0u,0x22930u,0x22b58u,0x1680u,0x5fd8u,0x22dc0u,0x22fe8u,0x1690u,0x0u,0x40u,0x9000u,0xa800u,0x80u,0xc0u,0x9010u,0xa810u,0x110u,0x150u,0x9020u,0xa820u,0x190u,0x1d0u,0x9030u,0xa830u,0x220u,0x260u,0x9040u,0xa840u,0x2a0u,0x2e0u,0x9050u,0xa850u,0x330u,0x370u,0x9060u,0xa860u,0x3b0u,0x3f0u,0x9070u,0xa870u,0x440u,0x400u,0x9080u,0xa880u,0x4c0u,0x480u,0x9090u,0xa890u,0x550u,0x510u,0x90a0u,0xa8a0u,0x5d0u,0x590u,0x90b0u,0xa8b0u,0x660u,0x620u,0x90c0u,0xa8c0u,0x6e0u,0x6a0u,0x90d0u,0xa8d0u,0x770u,0x730u,0x90e0u,0xa8e0u,0x7f0u,0x7b0u,0x90f0u,0xa8f0u,0x10u,0x50u,0x9300u,0xab00u,0x90u,0xd0u,0x9310u,0xab10u,0x100u,0x140u,0x9320u,0xab20u,0x180u,0x1c0u,0x9330u,0xab30u,0x230u,0x270u,0x9340u,0xab40u,0x2b0u,0x2f0u,0x9350u,0xab50u,0x320u,0x360u,0x9360u,0xab60u,0x3a0u,0x3e0u,0x9370u,0xab70u,0x450u,0x410u,0x9380u,0xab80u,0x4d0u,0x490u,0x9390u,0xab90u,0x540u,0x500u,0x93a0u,0xaba0u,0x5c0u,0x580u,0x93b0u,0xabb0u,0x670u,0x630u,0x93c0u,0xabc0u,0x6f0u,0x6b0u,0x93d0u,0xabd0u,0x760u,0x720u,0x93e0u,0xabe0u,0x7e0u,0x7a0u,0x93f0u,0xabf0u,0x20u,0x60u,0x9600u,0xae00u,0xa0u,0xe0u,0x9610u,0xae10u,0x130u,0x170u,0x9620u,0xae20u,0x1b0u,0x1f0u,0x9630u,0xae30u,0x200u,0x240u,0x9640u,0xae40u,0x280u,0x2c0u,0x9650u,0xae50u,0x310u,0x350u,0x9660u,0xae60u,0x390u,0x3d0u,0x9670u,0xae70u,0x460u,0x420u,0x9680u,0xae80u,0x4e0u,0x4a0u,0x9690u,0xae90u,0x570u,0x530u,0x96a0u,0xaea0u,0x5f0u,0x5b0u,0x96b0u,0xaeb0u,0x640u,0x600u,0x96c0u,0xaec0u,0x6c0u,0x680u,0x96d0u,0xaed0u,0x750u,0x710u,0x96e0u,0xaee0u,0x7d0u,0x790u,0x96f0u,0xaef0u,0x30u,0x70u,0x9900u,0xb100u,0xb0u,0xf0u,0x9910u,0xb110u,0x120u,0x160u,0x9920u,0xb120u,0x1a0u,0x1e0u,0x9930u,0xb130u,0x210u,0x250u,0x9940u,0xb140u,0x290u,0x2d0u,0x9950u,0xb150u,0x300u,0x340u,0x9960u,0xb160u,0x380u,0x3c0u,0x9970u,0xb170u,0x470u,0x430u,0x9980u,0xb180u,0x4f0u,0x4b0u,0x9990u,0xb190u,0x560u,0x520u,0x99a0u,0xb1a0u,0x5e0u,0x5a0u,0x99b0u,0xb1b0u,0x650u,0x610u,0x99c0u,0xb1c0u,0x6d0u,0x690u,0x99d0u,0xb1d0u,0x740u,0x700u,0x99e0u,0xb1e0u,0x7c0u,0x780u,0x99f0u,0xb1f0u,0x9c08u,0x12060u,0x120a0u,0x12400u,0x9c18u,0x121f0u,0x12230u,0x12410u,0x9c28u,0x12340u,0x12380u,0x12420u,0x9c38u,0x124d0u,0x12510u,0x12430u,0x9c48u,0x12620u,0x126e0u,0x12440u,0x9c58u,0x127b0u,0x12870u,0x12450u,0x9c68u,0x12900u,0x129c0u,0x12460u,0x9c78u,0x12a90u,0x12b50u,0x12470u,0x9c88u,0x12c60u,0x12ca0u,0x12480u,0x9c98u,0x12df0u,0x12e30u,0x12490u,0x9ca8u,0x12f40u,0x12f80u,0x124a0u,0x9cb8u,0x130d0u,0x13110u,0x124b0u,0x9cc8u,0x13220u,0x132e0u,0x124c0u,0x9cd8u,0x133b0u,0x13470u,0x124d0u,0x9ce8u,0x13500u,0x135c0u,0x124e0u,0x9cf8u,0x13690u,0x13750u,0x124f0u,0x9f08u,0x12070u,0x120a8u,0x12500u,0x9f18u,0x121e0u,0x12238u,0x12510u,0x9f28u,0x12350u,0x12388u,0x12520u,0x9f38u,0x124c0u,0x12518u,0x12530u,0x9f48u,0x12630u,0x126e8u,0x12540u,0x9f58u,0x127a0u,0x12878u,0x12550u,0x9f68u,0x12910u,0x129c8u,0x12560u,0x9f78u,0x12a80u,0x12b58u,0x12570u,0x9f88u,0x12c70u,0x12ca8u,0x12580u,0x9f98u,0x12de0u,0x12e38u,0x12590u,0x9fa8u,0x12f50u,0x12f88u,0x125a0u,0x9fb8u,0x130c0u,0x13118u,0x125b0u,0x9fc8u,0x13230u,0x132e8u,0x125c0u,0x9fd8u,0x133a0u,0x13478u,0x125d0u,0x9fe8u,0x13510u,0x135c8u,0x125e0u,0x9ff8u,0x13680u,0x13758u,0x125f0u,0xa208u,0x12080u,0x120b0u,0x12600u,0xa218u,0x12210u,0x12220u,0x12610u,0xa228u,0x123a0u,0x12390u,0x12620u,0xa238u,0x12530u,0x12500u,0x12630u,0xa248u,0x126c0u,0x126f0u,0x12640u,0xa258u,0x12850u,0x12860u,0x12650u,0xa268u,0x129e0u,0x129d0u,0x12660u,0xa278u,0x12b70u,0x12b40u,0x12670u,0xa288u,0x12c80u,0x12cb0u,0x12680u,0xa298u,0x12e10u,0x12e20u,0x12690u,0xa2a8u,0x12fa0u,0x12f90u,0x126a0u,0xa2b8u,0x13130u,0x13100u,0x126b0u,0xa2c8u,0x132c0u,0x132f0u,0x126c0u,0xa2d8u,0x13450u,0x13460u,0x126d0u,0xa2e8u,0x135e0u,0x135d0u,0x126e0u,0xa2f8u,0x13770u,0x13740u,0x126f0u,0xa508u,0x12090u,0x120b8u,0x12700u,0xa518u,0x12200u,0x12228u,0x12710u,0xa528u,0x123b0u,0x12398u,0x12720u,0xa538u,0x12520u,0x12508u,0x12730u,0xa548u,0x126d0u,0x126f8u,0x12740u,0xa558u,0x12840u,0x12868u,0x12750u,0xa568u,0x129f0u,0x129d8u,0x12760u,0xa578u,0x12b60u,0x12b48u,0x12770u,0xa588u,0x12c90u,0x12cb8u,0x12780u,0xa598u,0x12e00u,0x12e28u,0x12790u,0xa5a8u,0x12fb0u,0x12f98u,0x127a0u,0xa5b8u,0x13120u,0x13108u,0x127b0u,0xa5c8u,0x132d0u,0x132f8u,0x127c0u,0xa5d8u,0x13440u,0x13468u,0x127d0u,0xa5e8u,0x135f0u,0x135d8u,0x127e0u,0xa5f8u,0x13760u,0x13748u,0x127f0u,0x5000u,0x1c300u,0x1e8c0u,0x1eb60u,0x5080u,0x1c310u,0x100000u,0x1eb62u,0x5110u,0x1c320u,0x100000u,0x1eb64u,0x5190u,0x1c330u,0x100000u,0x1eb66u,0x5220u,0x1c340u,0x1e8c0u,0x1eb68u,0x52a0u,0x1c350u,0x100000u,0x1eb6au,0x5330u,0x1c360u,0x100000u,0x1eb6cu,0x53b0u,0x1c370u,0x100000u,0x1eb6eu,0x5440u,0x1c380u,0x1e8c0u,0x1eb70u,0x54c0u,0x1c390u,0x100000u,0x1eb72u,0x5550u,0x1c3a0u,0x100000u,0x1eb74u,0x55d0u,0x1c3b0u,0x100000u,0x1eb76u,0x5660u,0x1c3c0u,0x1e8c0u,0x1eb78u,0x56e0u,0x1c3d0u,0x100000u,0x1eb7au,0x5770u,0x1c3e0u,0x100000u,0x1eb7cu,0x57f0u,0x1c3f0u,0x100000u,0x1eb7eu,0x5010u,0x1c400u,0x1e8f0u,0x1eb60u,0x5090u,0x1c410u,0x100000u,0x1eb62u,0x5100u,0x1c420u,0x100000u,0x1eb64u,0x5180u,0x1c430u,0x100000u,0x1eb66u,0x5230u,0x1c440u,0x1e8f0u,0x1eb68u,0x52b0u,0x1c450u,0x100000u,0x1eb6au,0x5320u,0x1c460u,0x100000u,0x1eb6cu,0x53a0u,0x1c470u,0x100000u,0x1eb6eu,0x5450u,0x1c480u,0x1e8f0u,0x1eb70u,0x54d0u,0x1c490u,0x100000u,0x1eb72u,0x5540u,0x1c4a0u,0x100000u,0x1eb74u,0x55c0u,0x1c4b0u,0x100000u,0x1eb76u,0x5670u,0x1c4c0u,0x1e8f0u,0x1eb78u,0x56f0u,0x1c4d0u,0x100000u,0x1eb7au,0x5760u,0x1c4e0u,0x100000u,0x1eb7cu,0x57e0u,0x1c4f0u,0x100000u,0x1eb7eu,0x5020u,0x1c500u,0x1e920u,0x1eb60u,0x50a0u,0x1c510u,0x100000u,0x1eb62u,0x5130u,0x1c520u,0x100000u,0x1eb64u,0x51b0u,0x1c530u,0x100000u,0x1eb66u,0x5200u,0x1c540u,0x1e920u,0x1eb68u,0x5280u,0x1c550u,0x100000u,0x1eb6au,0x5310u,0x1c560u,0x100000u,0x1eb6cu,0x5390u,0x1c570u,0x100000u,0x1eb6eu,0x5460u,0x1c580u,0x1e920u,0x1eb70u,0x54e0u,0x1c590u,0x100000u,0x1eb72u,0x5570u,0x1c5a0u,0x100000u,0x1eb74u,0x55f0u,0x1c5b0u,0x100000u,0x1eb76u,0x5640u,0x1c5c0u,0x1e920u,0x1eb78u,0x56c0u,0x1c5d0u,0x100000u,0x1eb7au,0x5750u,0x1c5e0u,0x100000u,0x1eb7cu,0x57d0u,0x1c5f0u,0x100000u,0x1eb7eu,0x5030u,0x1c600u,0x1e950u,0x1eb60u,0x50b0u,0x1c610u,0x100000u,0x1eb62u,0x5120u,0x1c620u,0x100000u,0x1eb64u,0x51a0u,0x1c630u,0x100000u,0x1eb66u,0x5210u,0x1c640u,0x1e950u,0x1eb68u,0x5290u,0x1c650u,0x100000u,0x1eb6au,0x5300u,0x1c660u,0x100000u,0x1eb6cu,0x5380u,0x1c670u,0x100000u,0x1eb6eu,0x5470u,0x1c680u,0x1e950u,0x1eb70u,0x54f0u,0x1c690u,0x100000u,0x1eb72u,0x5560u,0x1c6a0u,0x100000u,0x1eb74u,0x55e0u,0x1c6b0u,0x100000u,0x1eb76u,0x5650u,0x1c6c0u,0x1e950u,0x1eb78u,0x56d0u,0x1c6d0u,0x100000u,0x1eb7au,0x5740u,0x1c6e0u,0x100000u,0x1eb7cu,0x57c0u,0x1c6f0u,0x100000u,0x1eb7eu,0x25800u,0x1d380u,0x1d380u,0xf0140u,0x25802u,0x1d382u,0x100000u,0xf0150u,0x25804u,0x1d384u,0x100000u,0xf0160u,0x25806u,0x1d386u,0x100000u,0xf0170u,0x25a00u,0x1d390u,0x1d380u,0xf0100u,0x25a02u,0x1d392u,0x100000u,0xf0110u,0x25a04u,0x1d394u,0x100000u,0xf0120u,0x25a06u,0x1d396u,0x100000u,0xf0130u,0x25c00u,0x1d3a0u,0x1d380u,0xf02c0u,0x25c02u,0x1d3a2u,0x100000u,0xf02d0u,0x25c04u,0x1d3a4u,0x100000u,0xf02e0u,0x25c06u,0x1d3a6u,0x100000u,0xf02f0u,0x25e00u,0x1d3b0u,0x1d380u,0xf0280u,0x25e02u,0x1d3b2u,0x100000u,0xf0290u,0x25e04u,0x1d3b4u,0x100000u,0xf02a0u,0x25e06u,0x1d3b6u,0x100000u,0xf02b0u,0x25808u,0x1d388u,0x1d390u,0x11f850u,0x2580au,0x1d38au,0x100000u,0x11f840u,0x2580cu,0x1d38cu,0x100000u,0x11f870u,0x2580eu,0x1d38eu,0x100000u,0x11f860u,0x25a08u,0x1d398u,0x1d390u,0x11f810u,0x25a0au,0x1d39au,0x100000u,0x11f800u,0x25a0cu,0x1d39cu,0x100000u,0x11f830u,0x25a0eu,0x1d39eu,0x100000u,0x11f820u,0x25c08u,0x1d3a8u,0x1d390u,0x11f9d0u,0x25c0au,0x1d3aau,0x100000u,0x11f9c0u,0x25c0cu,0x1d3acu,0x100000u,0x11f9f0u,0x25c0eu,0x1d3aeu,0x100000u,0x11f9e0u,0x25e08u,0x1d3b8u,0x1d390u,0x11f990u,0x25e0au,0x1d3bau,0x100000u,0x11f980u,0x25e0cu,0x1d3bcu,0x100000u,0x11f9b0u,0x25e0eu,0x1d3beu,0x100000u,0x11f9a0u,0x25008u,0x1d3c0u,0x1d3a0u,0x11fb60u,0x2500au,0x1d3c2u,0x100000u,0x11fb70u,0x2500cu,0x1d3c4u,0x100000u,0x11fb40u,0x2500eu,0x1d3c6u,0x100000u,0x11fb50u,0x25208u,0x1d3d0u,0x1d3a0u,0x11fb20u,0x2520au,0x1d3d2u,0x100000u,0x11fb30u,0x2520cu,0x1d3d4u,0x100000u,0x11fb00u,0x2520eu,0x1d3d6u,0x100000u,0x11fb10u,0x25408u,0x1d3e0u,0x1d3a0u,0x11fce0u,0x2540au,0x1d3e2u,0x100000u,0x11fcf0u,0x2540cu,0x1d3e4u,0x100000u,0x11fcc0u,0x2540eu,0x1d3e6u,0x100000u,0x11fcd0u,0x25608u,0x1d3f0u,0x1d3a0u,0x11fca0u,0x2560au,0x1d3f2u,0x100000u,0x11fcb0u,0x2560cu,0x1d3f4u,0x100000u,0x11fc80u,0x2560eu,0x1d3f6u,0x100000u,0x11fc90u,0x100000u,0x1d3c8u,0x1d3b0u,0x11fe70u,0x100000u,0x1d3cau,0x100000u,0x11fe60u,0x100000u,0x1d3ccu,0x100000u,0x11fe50u,0x100000u,0x1d3ceu,0x100000u,0x11fe40u,0x100000u,0x1d3d8u,0x1d3b0u,0x11fe30u,0x100000u,0x1d3dau,0x100000u,0x11fe20u,0x100000u,0x1d3dcu,0x100000u,0x11fe10u,0x100000u,0x1d3deu,0x100000u,0x11fe00u,0x100000u,0x1d3e8u,0x1d3b0u,0x11fff0u,0x100000u,0x1d3eau,0x100000u,0x11ffe0u,0x100000u,0x1d3ecu,0x100000u,0x11ffd0u,0x100000u,0x1d3eeu,0x100000u,0x11ffc0u,0x100000u,0x1d3f8u,0x1d3b0u,0x11ffb0u,0x100000u,0x1d3fau,0x100000u,0x11ffa0u,0x100000u,0x1d3fcu,0x100000u,0x11ff90u,0x100000u,0x1d3feu,0x100000u,0x11ff80u,0x420u,0x30850u,0x90000u,0xc0420u,0x430u,0x30840u,0x90010u,0xc0430u,0x400u,0x30870u,0x90020u,0xc0400u,0x410u,0x30860u,0x90030u,0xc0410u,0x460u,0x30810u,0x90040u,0xc0460u,0x470u,0x30800u,0x90050u,0xc0470u,0x440u,0x30830u,0x90060u,0xc0440u,0x450u,0x30820u,0x90070u,0xc0450u,0x4b0u,0x308d0u,0x90080u,0xef8b0u,0x4a0u,0x308c0u,0x90090u,0xef8a0u,0x490u,0x308f0u,0x900a0u,0xef890u,0x480u,0x308e0u,0x900b0u,0xef880u,0x4f0u,0x30890u,0x900c0u,0xef8f0u,0x4e0u,0x30880u,0x900d0u,0xef8e0u,0x4d0u,0x308b0u,0x900e0u,0xef8d0u,0x4c0u,0x308a0u,0x900f0u,0xef8c0u,0x530u,0x5fd60u,0x90100u,0xef930u,0x520u,0x5fd70u,0x90110u,0xef920u,0x510u,0x5fd40u,0x90120u,0xef910u,0x500u,0x5fd50u,0x90130u,0xef900u,0x570u,0x5fd20u,0x90140u,0xef970u,0x560u,0x5fd30u,0x90150u,0xef960u,0x550u,0x5fd00u,0x90160u,0xef950u,0x540u,0x5fd10u,0x90170u,0xef940u,0x5b0u,0x5fde0u,0x90190u,0xef9b0u,0x5a0u,0x5fdf0u,0x90180u,0xef9a0u,0x590u,0x5fdc0u,0x901b0u,0xef990u,0x580u,0x5fdd0u,0x901a0u,0xef980u,0x5f0u,0x5fda0u,0x901d0u,0xef9f0u,0x5e0u,0x5fdb0u,0x901c0u,0xef9e0u,0x5d0u,0x5fd80u,0x901f0u,0xef9d0u,0x5c0u,0x5fd90u,0x901e0u,0xef9c0u,0x640u,0x5fe60u,0x90210u,0xefa40u,0x650u,0x5fe70u,0x90200u,0xefa50u,0x660u,0x5fe40u,0x90230u,0xefa60u,0x670u,0x5fe50u,0x90220u,0xefa70u,0x600u,0x5fe20u,0x90250u,0xefa00u,0x610u,0x5fe30u,0x90240u,0xefa10u,0x620u,0x5fe00u,0x90270u,0xefa20u,0x630u,0x5fe10u,0x90260u,0xefa30u,0x6c0u,0x5fef0u,0x90290u,0xefac0u,0x6d0u,0x5fee0u,0x90280u,0xefad0u,0x6e0u,0x5fed0u,0x902b0u,0xefae0u,0x6f0u,0x5fec0u,0x902a0u,0xefaf0u,0x680u,0x5feb0u,0x902d0u,0xefa80u,0x690u,0x5fea0u,0x902c0u,0xefa90u,0x6a0u,0x5fe90u,0x902f0u,0xefaa0u,0x6b0u,0x5fe80u,0x902e0u,0xefab0u,0x740u,0x5ff70u,0x90320u,0xefb40u,0x750u,0x5ff60u,0x90330u,0xefb50u,0x760u,0x5ff50u,0x90300u,0xefb60u,0x770u,0x5ff40u,0x90310u,0xefb70u,0x700u,0x5ff30u,0x90360u,0xefb00u,0x710u,0x5ff20u,0x90370u,0xefb10u,0x720u,0x5ff10u,0x90340u,0xefb20u,0x730u,0x5ff00u,0x90350u,0xefb30u,0x7d0u,0x5fff0u,0x903a0u,0xefbd0u,0x7c0u,0x5ffe0u,0x903b0u,0xefbc0u,0x7f0u,0x5ffd0u,0x90380u,0xefbf0u,0x7e0u,0x5ffc0u,0x90390u,0xefbe0u,0x790u,0x5ffb0u,0x903e0u,0xefb90u,0x780u,0x5ffa0u,0x903f0u,0xefb80u,0x7b0u,0x5ff90u,0x903c0u,0xefbb0u,0x7a0u,0x5ff80u,0x903d0u,0xefba0u,0x0u,0x11fc50u,0x0u,0x7230cu,0x0u,0x11fc40u,0x0u,0x7230cu,0x0u,0x11fc70u,0x0u,0x7230cu,0x0u,0x11fc60u,0x0u,0x7230cu,0x0u,0x11fc10u,0x0u,0x7230cu,0x0u,0x11fc00u,0x0u,0x7230cu,0x0u,0x11fc30u,0x0u,0x7230cu,0x0u,0x11fc20u,0x0u,0x7230cu,0x0u,0x11fcd0u,0x0u,0x5232cu,0x0u,0x11fcc0u,0x0u,0x5232cu,0x0u,0x11fcf0u,0x0u,0x5232cu,0x0u,0x11fce0u,0x0u,0x5232cu,0x0u,0x11fc90u,0x0u,0x5232cu,0x0u,0x11fc80u,0x0u,0x5232cu,0x0u,0x11fcb0u,0x0u,0x5232cu,0x0u,0x11fca0u,0x0u,0x5232cu,0x0u,0x11fd60u,0x0u,0xd0124u,0x0u,0x11fd70u,0x0u,0xd0124u,0x0u,0x11fd40u,0x0u,0xd0124u,0x0u,0x11fd50u,0x0u,0xd0124u,0x0u,0x11fd20u,0x0u,0xd0124u,0x0u,0x11fd30u,0x0u,0xd0124u,0x0u,0x11fd00u,0x0u,0xd0124u,0x0u,0x11fd10u,0x0u,0xd0124u,0x0u,0x11fde0u,0x0u,0xd81a6u,0x0u,0x11fdf0u,0x0u,0xd81a6u,0x0u,0x11fdc0u,0x0u,0xd81a6u,0x0u,0x11fdd0u,0x0u,0xd81a6u,0x0u,0x11fda0u,0x0u,0xd81a6u,0x0u,0x11fdb0u,0x0u,0xd81a6u,0x0u,0x11fd80u,0x0u,0xd81a6u,0x0u,0x11fd90u,0x0u,0xd81a6u,0x0u,0x11fe60u,0x0u,0xd8996u,0x0u,0x11fe70u,0x0u,0xd8996u,0x0u,0x11fe40u,0x0u,0xd8996u,0x0u,0x11fe50u,0x0u,0xd8996u,0x0u,0x11fe20u,0x0u,0xd8996u,0x0u,0x11fe30u,0x0u,0xd8996u,0x0u,0x11fe00u,0x0u,0xd8996u,0x0u,0x11fe10u,0x0u,0xd8996u,0x0u,0x11fef0u,0x0u,0xd8996u,0x0u,0x11fee0u,0x0u,0xd8996u,0x0u,0x11fed0u,0x0u,0xd8996u,0x0u,0x11fec0u,0x0u,0xd8996u,0x0u,0x11feb0u,0x0u,0xd8996u,0x0u,0x11fea0u,0x0u,0xd8996u,0x0u,0x11fe90u,0x0u,0xd8996u,0x0u,0x11fe80u,0x0u,0xd8996u,0x0u,0x11ff70u,0x0u,0xd8b1cu,0x0u,0x11ff60u,0x0u,0xd8b1cu,0x0u,0x11ff50u,0x0u,0xd8b1cu,0x0u,0x11ff40u,0x0u,0xd8b1cu,0x0u,0x11ff30u,0x0u,0xd8b1cu,0x0u,0x11ff20u,0x0u,0xd8b1cu,0x0u,0x11ff10u,0x0u,0xd8b1cu,0x0u,0x11ff00u,0x0u,0xd8b1cu,0x0u,0x11fff0u,0x0u,0xd8b1cu,0x0u,0x11ffe0u,0x0u,0xd8b1cu,0x0u,0x11ffd0u,0x0u,0xd8b1cu,0x0u,0x11ffc0u,0x0u,0xd8b1cu,0x0u,0x11ffb0u,0x0u,0xd8b1cu,0x0u,0x11ffa0u,0x0u,0xd8b1cu,0x0u,0x11ff90u,0x0u,0xd8b1cu,0x0u,0x11ff80u,0x0u,0xd8b1cu,0x5840u,0x5860u,0x6800u,0x5840u,0x5848u,0x5868u,0x6808u,0x5850u,0x5850u,0x5870u,0x6810u,0x5860u,0x5858u,0x5878u,0x6818u,0x5870u,0x58c0u,0x58e0u,0x6880u,0x58c0u,0x58c8u,0x58e8u,0x6888u,0x58d0u,0x58d0u,0x58f0u,0x6890u,0x58e0u,0x58d8u,0x58f8u,0x6898u,0x58f0u,0x5960u,0x5940u,0x6920u,0x5960u,0x5968u,0x5948u,0x6928u,0x5970u,0x5970u,0x5950u,0x6930u,0x5940u,0x5978u,0x5958u,0x6938u,0x5950u,0x59e0u,0x59c0u,0x69a0u,0x59e0u,0x59e8u,0x59c8u,0x69a8u,0x59f0u,0x59f0u,0x59d0u,0x69b0u,0x59c0u,0x59f8u,0x59d8u,0x69b8u,0x59d0u,0x5c00u,0x5c20u,0x6c40u,0x5a40u,0x5c08u,0x5c28u,0x6c48u,0x5a50u,0x5c10u,0x5c30u,0x6c50u,0x5a60u,0x5c18u,0x5c38u,0x6c58u,0x5a70u,0x5c80u,0x5ca0u,0x6cc0u,0x5ac0u,0x5c88u,0x5ca8u,0x6cc8u,0x5ad0u,0x5c90u,0x5cb0u,0x6cd0u,0x5ae0u,0x5c98u,0x5cb8u,0x6cd8u,0x5af0u,0x5d20u,0x5d00u,0x6d60u,0x5b60u,0x5d28u,0x5d08u,0x6d68u,0x5b70u,0x5d30u,0x5d10u,0x6d70u,0x5b40u,0x5d38u,0x5d18u,0x6d78u,0x5b50u,0x5da0u,0x5d80u,0x6de0u,0x5be0u,0x5da8u,0x5d88u,0x6de8u,0x5bf0u,0x5db0u,0x5d90u,0x6df0u,0x5bc0u,0x5db8u,0x5d98u,0x6df8u,0x5bd0u,0x6040u,0x6060u,0x7000u,0x5c00u,0x6048u,0x6068u,0x7008u,0x5c10u,0x6050u,0x6070u,0x7010u,0x5c20u,0x6058u,0x6078u,0x7018u,0x5c30u,0x60c0u,0x60e0u,0x7080u,0x5c80u,0x60c8u,0x60e8u,0x7088u,0x5c90u,0x60d0u,0x60f0u,0x7090u,0x5ca0u,0x60d8u,0x60f8u,0x7098u,0x5cb0u,0x6160u,0x6140u,0x7120u,0x5d20u,0x6168u,0x6148u,0x7128u,0x5d30u,0x6170u,0x6150u,0x7130u,0x5d00u,0x6178u,0x6158u,0x7138u,0x5d10u,0x61e0u,0x61c0u,0x71a0u,0x5da0u,0x61e8u,0x61c8u,0x71a8u,0x5db0u,0x61f0u,0x61d0u,0x71b0u,0x5d80u,0x61f8u,0x61d8u,0x71b8u,0x5d90u,0x6400u,0x6420u,0x7440u,0x5e00u,0x6408u,0x6428u,0x7448u,0x5e10u,0x6410u,0x6430u,0x7450u,0x5e20u,0x6418u,0x6438u,0x7458u,0x5e30u,0x6480u,0x64a0u,0x74c0u,0x5e80u,0x6488u,0x64a8u,0x74c8u,0x5e90u,0x6490u,0x64b0u,0x74d0u,0x5ea0u,0x6498u,0x64b8u,0x74d8u,0x5eb0u,0x6520u,0x6500u,0x7560u,0x5f20u,0x6528u,0x6508u,0x7568u,0x5f30u,0x6530u,0x6510u,0x7570u,0x5f00u,0x6538u,0x6518u,0x7578u,0x5f10u,0x65a0u,0x6580u,0x75e0u,0x5fa0u,0x65a8u,0x6588u,0x75e8u,0x5fb0u,0x65b0u,0x6590u,0x75f0u,0x5f80u,0x65b8u,0x6598u,0x75f8u,0x5f90u,0x6800u,0x1e980u,0x1ebc0u,0x1800u,0x6808u,0x1ee10u,0x1f050u,0x1810u,0x6810u,0x1f2a0u,0x1f4e0u,0x1820u,0x6818u,0x1f730u,0x1f970u,0x1830u,0x6880u,0x1fbc0u,0x1fe00u,0x1840u,0x6888u,0x20050u,0x20290u,0x1850u,0x6890u,0x204e0u,0x20720u,0x1860u,0x6898u,0x20970u,0x20bb0u,0x1870u,0x6920u,0x20e00u,0x21040u,0x1980u,0x6928u,0x21290u,0x214d0u,0x1990u,0x6930u,0x21720u,0x21960u,0x19a0u,0x6938u,0x21bb0u,0x21df0u,0x19b0u,0x69a0u,0x22040u,0x22280u,0x19c0u,0x69a8u,0x224d0u,0x22710u,0x19d0u,0x69b0u,0x22960u,0x22ba0u,0x19e0u,0x69b8u,0x22df0u,0x23030u,0x19f0u,0x6a00u,0x1e9b0u,0x1ebc8u,0x1b20u,0x6a08u,0x1ee40u,0x1f058u,0x1b30u,0x6a10u,0x1f2d0u,0x1f4e8u,0x1b00u,0x6a18u,0x1f760u,0x1f978u,0x1b10u,0x6a80u,0x1fbf0u,0x1fe08u,0x1b60u,0x6a88u,0x20080u,0x20298u,0x1b70u,0x6a90u,0x20510u,0x20728u,0x1b40u,0x6a98u,0x209a0u,0x20bb8u,0x1b50u,0x6b20u,0x20e30u,0x21048u,0x1ca0u,0x6b28u,0x212c0u,0x214d8u,0x1cb0u,0x6b30u,0x21750u,0x21968u,0x1c80u,0x6b38u,0x21be0u,0x21df8u,0x1c90u,0x6ba0u,0x22070u,0x22288u,0x1ce0u,0x6ba8u,0x22500u,0x22718u,0x1cf0u,0x6bb0u,0x22990u,0x22ba8u,0x1cc0u,0x6bb8u,0x22e20u,0x23038u,0x1cd0u,0x6c40u,0x1e9e0u,0x1ebd0u,0x1e00u,0x6c48u,0x1ee70u,0x1f060u,0x1e10u,0x6c50u,0x1f300u,0x1f4f0u,0x1e20u,0x6c58u,0x1f790u,0x1f980u,0x1e30u,0x6cc0u,0x1fc20u,0x1fe10u,0x1e40u,0x6cc8u,0x200b0u,0x202a0u,0x1e50u,0x6cd0u,0x20540u,0x20730u,0x1e60u,0x6cd8u,0x209d0u,0x20bc0u,0x1e70u,0x6d60u,0x20e60u,0x21050u,0x1f80u,0x6d68u,0x212f0u,0x214e0u,0x1f90u,0x6d70u,0x21780u,0x21970u,0x1fa0u,0x6d78u,0x21c10u,0x21e00u,0x1fb0u,0x6de0u,0x220a0u,0x22290u,0x1fc0u,0x6de8u,0x22530u,0x22720u,0x1fd0u,0x6df0u,0x229c0u,0x22bb0u,0x1fe0u,0x6df8u,0x22e50u,0x23040u,0x1ff0u,0x6e40u,0x1ea10u,0x1ebd8u,0x2120u,0x6e48u,0x1eea0u,0x1f068u,0x2130u,0x6e50u,0x1f330u,0x1f4f8u,0x2100u,0x6e58u,0x1f7c0u,0x1f988u,0x2110u,0x6ec0u,0x1fc50u,0x1fe18u,0x2160u,0x6ec8u,0x200e0u,0x202a8u,0x2170u,0x6ed0u,0x20570u,0x20738u,0x2140u,0x6ed8u,0x20a00u,0x20bc8u,0x2150u,0x6f60u,0x20e90u,0x21058u,0x22a0u,0x6f68u,0x21320u,0x214e8u,0x22b0u,0x6f70u,0x217b0u,0x21978u,0x2280u,0x6f78u,0x21c40u,0x21e08u,0x2290u,0x6fe0u,0x220d0u,0x22298u,0x22e0u,0x6fe8u,0x22560u,0x22728u,0x22f0u,0x6ff0u,0x229f0u,0x22bb8u,0x22c0u,0x6ff8u,0x22e80u,0x23048u,0x22d0u,0x0u,0x40u,0x9000u,0xb400u,0x80u,0xc0u,0x9010u,0xb410u,0x110u,0x150u,0x9020u,0xb420u,0x190u,0x1d0u,0x9030u,0xb430u,0x220u,0x260u,0x9040u,0xb440u,0x2a0u,0x2e0u,0x9050u,0xb450u,0x330u,0x370u,0x9060u,0xb460u,0x3b0u,0x3f0u,0x9070u,0xb470u,0x440u,0x400u,0x9080u,0xb480u,0x4c0u,0x480u,0x9090u,0xb490u,0x550u,0x510u,0x90a0u,0xb4a0u,0x5d0u,0x590u,0x90b0u,0xb4b0u,0x660u,0x620u,0x90c0u,0xb4c0u,0x6e0u,0x6a0u,0x90d0u,0xb4d0u,0x770u,0x730u,0x90e0u,0xb4e0u,0x7f0u,0x7b0u,0x90f0u,0xb4f0u,0x10u,0x50u,0x9300u,0xb700u,0x90u,0xd0u,0x9310u,0xb710u,0x100u,0x140u,0x9320u,0xb720u,0x180u,0x1c0u,0x9330u,0xb730u,0x230u,0x270u,0x9340u,0xb740u,0x2b0u,0x2f0u,0x9350u,0xb750u,0x320u,0x360u,0x9360u,0xb760u,0x3a0u,0x3e0u,0x9370u,0xb770u,0x450u,0x410u,0x9380u,0xb780u,0x4d0u,0x490u,0x9390u,0xb790u,0x540u,0x500u,0x93a0u,0xb7a0u,0x5c0u,0x580u,0x93b0u,0xb7b0u,0x670u,0x630u,0x93c0u,0xb7c0u,0x6f0u,0x6b0u,0x93d0u,0xb7d0u,0x760u,0x720u,0x93e0u,0xb7e0u,0x7e0u,0x7a0u,0x93f0u,0xb7f0u,0x20u,0x60u,0x9600u,0xba00u,0xa0u,0xe0u,0x9610u,0xba10u,0x130u,0x170u,0x9620u,0xba20u,0x1b0u,0x1f0u,0x9630u,0xba30u,0x200u,0x240u,0x9640u,0xba40u,0x280u,0x2c0u,0x9650u,0xba50u,0x310u,0x350u,0x9660u,0xba60u,0x390u,0x3d0u,0x9670u,0xba70u,0x460u,0x420u,0x9680u,0xba80u,0x4e0u,0x4a0u,0x9690u,0xba90u,0x570u,0x530u,0x96a0u,0xbaa0u,0x5f0u,0x5b0u,0x96b0u,0xbab0u,0x640u,0x600u,0x96c0u,0xbac0u,0x6c0u,0x680u,0x96d0u,0xbad0u,0x750u,0x710u,0x96e0u,0xbae0u,0x7d0u,0x790u,0x96f0u,0xbaf0u,0x30u,0x70u,0x9900u,0xbd00u,0xb0u,0xf0u,0x9910u,0xbd10u,0x120u,0x160u,0x9920u,0xbd20u,0x1a0u,0x1e0u,0x9930u,0xbd30u,0x210u,0x250u,0x9940u,0xbd40u,0x290u,0x2d0u,0x9950u,0xbd50u,0x300u,0x340u,0x9960u,0xbd60u,0x380u,0x3c0u,0x9970u,0xbd70u,0x470u,0x430u,0x9980u,0xbd80u,0x4f0u,0x4b0u,0x9990u,0xbd90u,0x560u,0x520u,0x99a0u,0xbda0u,0x5e0u,0x5a0u,0x99b0u,0xbdb0u,0x650u,0x610u,0x99c0u,0xbdc0u,0x6d0u,0x690u,0x99d0u,0xbdd0u,0x740u,0x700u,0x99e0u,0xbde0u,0x7c0u,0x780u,0x99f0u,0xbdf0u,0xc000u,0x120c0u,0x12100u,0x12800u,0xc010u,0x12250u,0x12290u,0x12810u,0xc020u,0x123e0u,0x12420u,0x12820u,0xc030u,0x12570u,0x125b0u,0x12830u,0xc040u,0x12680u,0x12740u,0x12840u,0xc050u,0x12810u,0x128d0u,0x12850u,0xc060u,0x129a0u,0x12a60u,0x12860u,0xc070u,0x12b30u,0x12bf0u,0x12870u,0xc080u,0x12cc0u,0x12d00u,0x12880u,0xc090u,0x12e50u,0x12e90u,0x12890u,0xc0a0u,0x12fe0u,0x13020u,0x128a0u,0xc0b0u,0x13170u,0x131b0u,0x128b0u,0xc0c0u,0x13280u,0x13340u,0x128c0u,0xc0d0u,0x13410u,0x134d0u,0x128d0u,0xc0e0u,0x135a0u,0x13660u,0x128e0u,0xc0f0u,0x13730u,0x137f0u,0x128f0u,0xc300u,0x120d0u,0x12108u,0x12900u,0xc310u,0x12240u,0x12298u,0x12910u,0xc320u,0x123f0u,0x12428u,0x12920u,0xc330u,0x12560u,0x125b8u,0x12930u,0xc340u,0x12690u,0x12748u,0x12940u,0xc350u,0x12800u,0x128d8u,0x12950u,0xc360u,0x129b0u,0x12a68u,0x12960u,0xc370u,0x12b20u,0x12bf8u,0x12970u,0xc380u,0x12cd0u,0x12d08u,0x12980u,0xc390u,0x12e40u,0x12e98u,0x12990u,0xc3a0u,0x12ff0u,0x13028u,0x129a0u,0xc3b0u,0x13160u,0x131b8u,0x129b0u,0xc3c0u,0x13290u,0x13348u,0x129c0u,0xc3d0u,0x13400u,0x134d8u,0x129d0u,0xc3e0u,0x135b0u,0x13668u,0x129e0u,0xc3f0u,0x13720u,0x137f8u,0x129f0u,0xc600u,0x120e0u,0x12110u,0x12a00u,0xc610u,0x12270u,0x12280u,0x12a10u,0xc620u,0x123c0u,0x12430u,0x12a20u,0xc630u,0x12550u,0x125a0u,0x12a30u,0xc640u,0x126a0u,0x12750u,0x12a40u,0xc650u,0x12830u,0x128c0u,0x12a50u,0xc660u,0x12980u,0x12a70u,0x12a60u,0xc670u,0x12b10u,0x12be0u,0x12a70u,0xc680u,0x12ce0u,0x12d10u,0x12a80u,0xc690u,0x12e70u,0x12e80u,0x12a90u,0xc6a0u,0x12fc0u,0x13030u,0x12aa0u,0xc6b0u,0x13150u,0x131a0u,0x12ab0u,0xc6c0u,0x132a0u,0x13350u,0x12ac0u,0xc6d0u,0x13430u,0x134c0u,0x12ad0u,0xc6e0u,0x13580u,0x13670u,0x12ae0u,0xc6f0u,0x13710u,0x137e0u,0x12af0u,0xc900u,0x120f0u,0x12118u,0x12b00u,0xc910u,0x12260u,0x12288u,0x12b10u,0xc920u,0x123d0u,0x12438u,0x12b20u,0xc930u,0x12540u,0x125a8u,0x12b30u,0xc940u,0x126b0u,0x12758u,0x12b40u,0xc950u,0x12820u,0x128c8u,0x12b50u,0xc960u,0x12990u,0x12a78u,0x12b60u,0xc970u,0x12b00u,0x12be8u,0x12b70u,0xc980u,0x12cf0u,0x12d18u,0x12b80u,0xc990u,0x12e60u,0x12e88u,0x12b90u,0xc9a0u,0x12fd0u,0x13038u,0x12ba0u,0xc9b0u,0x13140u,0x131a8u,0x12bb0u,0xc9c0u,0x132b0u,0x13358u,0x12bc0u,0xc9d0u,0x13420u,0x134c8u,0x12bd0u,0xc9e0u,0x13590u,0x13678u,0x12be0u,0xc9f0u,0x13700u,0x137e8u,0x12bf0u,0x7800u,0x1c300u,0x1e980u,0x1ebc0u,0x7880u,0x1c310u,0x100000u,0x1ebc2u,0x7910u,0x1c320u,0x100000u,0x1ebc4u,0x7990u,0x1c330u,0x100000u,0x1ebc6u,0x7a20u,0x1c340u,0x1e980u,0x1ebc8u,0x7aa0u,0x1c350u,0x100000u,0x1ebcau,0x7b30u,0x1c360u,0x100000u,0x1ebccu,0x7bb0u,0x1c370u,0x100000u,0x1ebceu,0x7c40u,0x1c380u,0x1e980u,0x1ebd0u,0x7cc0u,0x1c390u,0x100000u,0x1ebd2u,0x7d50u,0x1c3a0u,0x100000u,0x1ebd4u,0x7dd0u,0x1c3b0u,0x100000u,0x1ebd6u,0x7e60u,0x1c3c0u,0x1e980u,0x1ebd8u,0x7ee0u,0x1c3d0u,0x100000u,0x1ebdau,0x7f70u,0x1c3e0u,0x100000u,0x1ebdcu,0x7ff0u,0x1c3f0u,0x100000u,0x1ebdeu,0x7810u,0x1c400u,0x1e9b0u,0x1ebc0u,0x7890u,0x1c410u,0x100000u,0x1ebc2u,0x7900u,0x1c420u,0x100000u,0x1ebc4u,0x7980u,0x1c430u,0x100000u,0x1ebc6u,0x7a30u,0x1c440u,0x1e9b0u,0x1ebc8u,0x7ab0u,0x1c450u,0x100000u,0x1ebcau,0x7b20u,0x1c460u,0x100000u,0x1ebccu,0x7ba0u,0x1c470u,0x100000u,0x1ebceu,0x7c50u,0x1c480u,0x1e9b0u,0x1ebd0u,0x7cd0u,0x1c490u,0x100000u,0x1ebd2u,0x7d40u,0x1c4a0u,0x100000u,0x1ebd4u,0x7dc0u,0x1c4b0u,0x100000u,0x1ebd6u,0x7e70u,0x1c4c0u,0x1e9b0u,0x1ebd8u,0x7ef0u,0x1c4d0u,0x100000u,0x1ebdau,0x7f60u,0x1c4e0u,0x100000u,0x1ebdcu,0x7fe0u,0x1c4f0u,0x100000u,0x1ebdeu,0x7820u,0x1c500u,0x1e9e0u,0x1ebc0u,0x78a0u,0x1c510u,0x100000u,0x1ebc2u,0x7930u,0x1c520u,0x100000u,0x1ebc4u,0x79b0u,0x1c530u,0x100000u,0x1ebc6u,0x7a00u,0x1c540u,0x1e9e0u,0x1ebc8u,0x7a80u,0x1c550u,0x100000u,0x1ebcau,0x7b10u,0x1c560u,0x100000u,0x1ebccu,0x7b90u,0x1c570u,0x100000u,0x1ebceu,0x7c60u,0x1c580u,0x1e9e0u,0x1ebd0u,0x7ce0u,0x1c590u,0x100000u,0x1ebd2u,0x7d70u,0x1c5a0u,0x100000u,0x1ebd4u,0x7df0u,0x1c5b0u,0x100000u,0x1ebd6u,0x7e40u,0x1c5c0u,0x1e9e0u,0x1ebd8u,0x7ec0u,0x1c5d0u,0x100000u,0x1ebdau,0x7f50u,0x1c5e0u,0x100000u,0x1ebdcu,0x7fd0u,0x1c5f0u,0x100000u,0x1ebdeu,0x7830u,0x1c600u,0x1ea10u,0x1ebc0u,0x78b0u,0x1c610u,0x100000u,0x1ebc2u,0x7920u,0x1c620u,0x100000u,0x1ebc4u,0x79a0u,0x1c630u,0x100000u,0x1ebc6u,0x7a10u,0x1c640u,0x1ea10u,0x1ebc8u,0x7a90u,0x1c650u,0x100000u,0x1ebcau,0x7b00u,0x1c660u,0x100000u,0x1ebccu,0x7b80u,0x1c670u,0x100000u,0x1ebceu,0x7c70u,0x1c680u,0x1ea10u,0x1ebd0u,0x7cf0u,0x1c690u,0x100000u,0x1ebd2u,0x7d60u,0x1c6a0u,0x100000u,0x1ebd4u,0x7de0u,0x1c6b0u,0x100000u,0x1ebd6u,0x7e50u,0x1c6c0u,0x1ea10u,0x1ebd8u,0x7ed0u,0x1c6d0u,0x100000u,0x1ebdau,0x7f40u,0x1c6e0u,0x100000u,0x1ebdcu,0x7fc0u,0x1c6f0u,0x100000u,0x1ebdeu,0x26000u,0x1d400u,0x1d400u,0x80000000u,0x26002u,0x1d402u,0x100000u,0x80000000u,0x26004u,0x1d404u,0x100000u,0x80000000u,0x26006u,0x1d406u,0x100000u,0x80000000u,0x26200u,0x1d410u,0x1d400u,0x80000000u,0x26202u,0x1d412u,0x100000u,0x80000000u,0x26204u,0x1d414u,0x100000u,0x80000000u,0x26206u,0x1d416u,0x100000u,0x80000000u,0x26400u,0x1d420u,0x1d400u,0x80000000u,0x26402u,0x1d422u,0x100000u,0x80000000u,0x26404u,0x1d424u,0x100000u,0x80000000u,0x26406u,0x1d426u,0x100000u,0x80000000u,0x26600u,0x1d430u,0x1d400u,0x80000000u,0x26602u,0x1d432u,0x100000u,0x80000000u,0x26604u,0x1d434u,0x100000u,0x80000000u,0x26606u,0x1d436u,0x100000u,0x80000000u,0x26008u,0x1d408u,0x1d410u,0x80000000u,0x2600au,0x1d40au,0x100000u,0x80000000u,0x2600cu,0x1d40cu,0x100000u,0x80000000u,0x2600eu,0x1d40eu,0x100000u,0x80000000u,0x26208u,0x1d418u,0x1d410u,0x80000000u,0x2620au,0x1d41au,0x100000u,0x80000000u,0x2620cu,0x1d41cu,0x100000u,0x80000000u,0x2620eu,0x1d41eu,0x100000u,0x80000000u,0x26408u,0x1d428u,0x1d410u,0x80000000u,0x2640au,0x1d42au,0x100000u,0x80000000u,0x2640cu,0x1d42cu,0x100000u,0x80000000u,0x2640eu,0x1d42eu,0x100000u,0x80000000u,0x26608u,0x1d438u,0x1d410u,0x80000000u,0x2660au,0x1d43au,0x100000u,0x80000000u,0x2660cu,0x1d43cu,0x100000u,0x80000000u,0x2660eu,0x1d43eu,0x100000u,0x80000000u,0x26800u,0x1d440u,0x1d420u,0x80000000u,0x26802u,0x1d442u,0x100000u,0x80000000u,0x26804u,0x1d444u,0x100000u,0x80000000u,0x26806u,0x1d446u,0x100000u,0x80000000u,0x26a00u,0x1d450u,0x1d420u,0x80000000u,0x26a02u,0x1d452u,0x100000u,0x80000000u,0x26a04u,0x1d454u,0x100000u,0x80000000u,0x26a06u,0x1d456u,0x100000u,0x80000000u,0x26c00u,0x1d460u,0x1d420u,0x80000000u,0x26c02u,0x1d462u,0x100000u,0x80000000u,0x26c04u,0x1d464u,0x100000u,0x80000000u,0x26c06u,0x1d466u,0x100000u,0x80000000u,0x26e00u,0x1d470u,0x1d420u,0x80000000u,0x26e02u,0x1d472u,0x100000u,0x80000000u,0x26e04u,0x1d474u,0x100000u,0x80000000u,0x26e06u,0x1d476u,0x100000u,0x80000000u,0x100000u,0x1d448u,0x1d430u,0x80000000u,0x100000u,0x1d44au,0x100000u,0x80000000u,0x100000u,0x1d44cu,0x100000u,0x80000000u,0x100000u,0x1d44eu,0x100000u,0x80000000u,0x100000u,0x1d458u,0x1d430u,0x80000000u,0x100000u,0x1d45au,0x100000u,0x80000000u,0x100000u,0x1d45cu,0x100000u,0x80000000u,0x100000u,0x1d45eu,0x100000u,0x80000000u,0x100000u,0x1d468u,0x1d430u,0x80000000u,0x100000u,0x1d46au,0x100000u,0x80000000u,0x100000u,0x1d46cu,0x100000u,0x80000000u,0x100000u,0x1d46eu,0x100000u,0x80000000u,0x100000u,0x1d478u,0x1d430u,0x80000000u,0x100000u,0x1d47au,0x100000u,0x80000000u,0x100000u,0x1d47cu,0x100000u,0x80000000u,0x100000u,0x1d47eu,0x100000u,0x80000000u,0x850u,0x60000u,0x90420u,0xefc50u,0x840u,0x60010u,0x90430u,0xefc40u,0x870u,0x60020u,0x90400u,0xefc70u,0x860u,0x60030u,0x90410u,0xefc60u,0x810u,0x60040u,0x90460u,0xefc10u,0x800u,0x60050u,0x90470u,0xefc00u,0x830u,0x60060u,0x90440u,0xefc30u,0x820u,0x60070u,0x90450u,0xefc20u,0x8d0u,0x60080u,0x904b0u,0xefcd0u,0x8c0u,0x60090u,0x904a0u,0xefcc0u,0x8f0u,0x600a0u,0x90490u,0xefcf0u,0x8e0u,0x600b0u,0x90480u,0xefce0u,0x890u,0x600c0u,0x904f0u,0xefc90u,0x880u,0x600d0u,0x904e0u,0xefc80u,0x8b0u,0x600e0u,0x904d0u,0xefcb0u,0x8a0u,0x600f0u,0x904c0u,0xefca0u,0x960u,0x60100u,0x90530u,0xefd60u,0x970u,0x60110u,0x90520u,0xefd70u,0x940u,0x60120u,0x90510u,0xefd40u,0x950u,0x60130u,0x90500u,0xefd50u,0x920u,0x60140u,0x90570u,0xefd20u,0x930u,0x60150u,0x90560u,0xefd30u,0x900u,0x60160u,0x90550u,0xefd00u,0x910u,0x60170u,0x90540u,0xefd10u,0x9e0u,0x60190u,0x905b0u,0xefde0u,0x9f0u,0x60180u,0x905a0u,0xefdf0u,0x9c0u,0x601b0u,0x90590u,0xefdc0u,0x9d0u,0x601a0u,0x90580u,0xefdd0u,0x9a0u,0x601d0u,0x905f0u,0xefda0u,0x9b0u,0x601c0u,0x905e0u,0xefdb0u,0x980u,0x601f0u,0x905d0u,0xefd80u,0x990u,0x601e0u,0x905c0u,0xefd90u,0xa60u,0x60210u,0xbfa40u,0xefe60u,0xa70u,0x60200u,0xbfa50u,0xefe70u,0xa40u,0x60230u,0xbfa60u,0xefe40u,0xa50u,0x60220u,0xbfa70u,0xefe50u,0xa20u,0x60250u,0xbfa00u,0xefe20u,0xa30u,0x60240u,0xbfa10u,0xefe30u,0xa00u,0x60270u,0xbfa20u,0xefe00u,0xa10u,0x60260u,0xbfa30u,0xefe10u,0x2fef0u,0x60290u,0xbfac0u,0xefef0u,0x2fee0u,0x60280u,0xbfad0u,0xefee0u,0x2fed0u,0x602b0u,0xbfae0u,0xefed0u,0x2fec0u,0x602a0u,0xbfaf0u,0xefec0u,0x2feb0u,0x602d0u,0xbfa80u,0xefeb0u,0x2fea0u,0x602c0u,0xbfa90u,0xefea0u,0x2fe90u,0x602f0u,0xbfaa0u,0xefe90u,0x2fe80u,0x602e0u,0xbfab0u,0xefe80u,0x2ff70u,0x60320u,0xbfb40u,0xeff70u,0x2ff60u,0x60330u,0xbfb50u,0xeff60u,0x2ff50u,0x60300u,0xbfb60u,0xeff50u,0x2ff40u,0x60310u,0xbfb70u,0xeff40u,0x2ff30u,0x60360u,0xbfb00u,0xeff30u,0x2ff20u,0x60370u,0xbfb10u,0xeff20u,0x2ff10u,0x60340u,0xbfb20u,0xeff10u,0x2ff00u,0x60350u,0xbfb30u,0xeff00u,0x2fff0u,0x603a0u,0xbfbd0u,0xefff0u,0x2ffe0u,0x603b0u,0xbfbc0u,0xeffe0u,0x2ffd0u,0x60380u,0xbfbf0u,0xeffd0u,0x2ffc0u,0x60390u,0xbfbe0u,0xeffc0u,0x2ffb0u,0x603e0u,0xbfb90u,0xeffb0u,0x2ffa0u,0x603f0u,0xbfb80u,0xeffa0u,0x2ff90u,0x603c0u,0xbfbb0u,0xeff90u,0x2ff80u,0x603d0u,0xbfba0u,0xeff80u,0x0u,0x80000000u,0x0u,0x18830u,0x0u,0x80000000u,0x0u,0x18830u,0x0u,0x80000000u,0x0u,0x18830u,0x0u,0x80000000u,0x0u,0x18830u,0x0u,0x80000000u,0x0u,0x18830u,0x0u,0x80000000u,0x0u,0x18830u,0x0u,0x80000000u,0x0u,0x18830u,0x0u,0x80000000u,0x0u,0x18830u,0x0u,0x80000000u,0x0u,0x18830u,0x0u,0x80000000u,0x0u,0x18830u,0x0u,0x80000000u,0x0u,0x18830u,0x0u,0x80000000u,0x0u,0x18830u,0x0u,0x80000000u,0x0u,0x18830u,0x0u,0x80000000u,0x0u,0x18830u,0x0u,0x80000000u,0x0u,0x18830u,0x0u,0x80000000u,0x0u,0x18830u,0x0u,0x80000000u,0x0u,0x188b2u,0x0u,0x80000000u,0x0u,0x188b2u,0x0u,0x80000000u,0x0u,0x188b2u,0x0u,0x80000000u,0x0u,0x188b2u,0x0u,0x80000000u,0x0u,0x188b2u,0x0u,0x80000000u,0x0u,0x188b2u,0x0u,0x80000000u,0x0u,0x188b2u,0x0u,0x80000000u,0x0u,0x188b2u,0x0u,0x80000000u,0x0u,0x18892u,0x0u,0x80000000u,0x0u,0x18892u,0x0u,0x80000000u,0x0u,0x18892u,0x0u,0x80000000u,0x0u,0x18892u,0x0u,0x80000000u,0x0u,0x18892u,0x0u,0x80000000u,0x0u,0x18892u,0x0u,0x80000000u,0x0u,0x18892u,0x0u,0x80000000u,0x0u,0x18892u,0x0u,0x80000000u,0x0u,0x1489au,0x0u,0x80000000u,0x0u,0x1489au,0x0u,0x80000000u,0x0u,0x1489au,0x0u,0x80000000u,0x0u,0x1489au,0x0u,0x80000000u,0x0u,0x1489au,0x0u,0x80000000u,0x0u,0x1489au,0x0u,0x80000000u,0x0u,0x1489au,0x0u,0x80000000u,0x0u,0x1489au,0x0u,0x80000000u,0x0u,0x34018u,0x0u,0x80000000u,0x0u,0x34018u,0x0u,0x80000000u,0x0u,0x34018u,0x0u,0x80000000u,0x0u,0x34018u,0x0u,0x80000000u,0x0u,0x34018u,0x0u,0x80000000u,0x0u,0x34018u,0x0u,0x80000000u,0x0u,0x34018u,0x0u,0x80000000u,0x0u,0x34018u,0x0u,0x80000000u,0x0u,0x36018u,0x0u,0x80000000u,0x0u,0x36018u,0x0u,0x80000000u,0x0u,0x36018u,0x0u,0x80000000u,0x0u,0x36018u,0x0u,0x80000000u,0x0u,0x36018u,0x0u,0x80000000u,0x0u,0x36018u,0x0u,0x80000000u,0x0u,0x36018u,0x0u,0x80000000u,0x0u,0x36018u,0x0u,0x80000000u,0x0u,0x36030u,0x0u,0x80000000u,0x0u,0x36030u,0x0u,0x80000000u,0x0u,0x36030u,0x0u,0x80000000u,0x0u,0x36030u,0x0u,0x80000000u,0x0u,0x36030u,0x0u,0x80000000u,0x0u,0x36030u,0x0u,0x80000000u,0x0u,0x36030u,0x0u,0x80000000u,0x0u,0x36030u,0x6840u,0x6860u,0x6820u,0x6820u,0x6848u,0x6868u,0x6828u,0x6830u,0x6850u,0x6870u,0x6830u,0x6840u,0x6858u,0x6878u,0x6838u,0x6850u,0x68c0u,0x68e0u,0x68a0u,0x68a0u,0x68c8u,0x68e8u,0x68a8u,0x68b0u,0x68d0u,0x68f0u,0x68b0u,0x68c0u,0x68d8u,0x68f8u,0x68b8u,0x68d0u,0x6960u,0x6940u,0x6900u,0x6900u,0x6968u,0x6948u,0x6908u,0x6910u,0x6970u,0x6950u,0x6910u,0x6960u,0x6978u,0x6958u,0x6918u,0x6970u,0x69e0u,0x69c0u,0x6980u,0x6980u,0x69e8u,0x69c8u,0x6988u,0x6990u,0x69f0u,0x69d0u,0x6990u,0x69e0u,0x69f8u,0x69d8u,0x6998u,0x69f0u,0x6c00u,0x6c20u,0x6c60u,0x6a20u,0x6c08u,0x6c28u,0x6c68u,0x6a30u,0x6c10u,0x6c30u,0x6c70u,0x6a40u,0x6c18u,0x6c38u,0x6c78u,0x6a50u,0x6c80u,0x6ca0u,0x6ce0u,0x6aa0u,0x6c88u,0x6ca8u,0x6ce8u,0x6ab0u,0x6c90u,0x6cb0u,0x6cf0u,0x6ac0u,0x6c98u,0x6cb8u,0x6cf8u,0x6ad0u,0x6d20u,0x6d00u,0x6d40u,0x6b00u,0x6d28u,0x6d08u,0x6d48u,0x6b10u,0x6d30u,0x6d10u,0x6d50u,0x6b60u,0x6d38u,0x6d18u,0x6d58u,0x6b70u,0x6da0u,0x6d80u,0x6dc0u,0x6b80u,0x6da8u,0x6d88u,0x6dc8u,0x6b90u,0x6db0u,0x6d90u,0x6dd0u,0x6be0u,0x6db8u,0x6d98u,0x6dd8u,0x6bf0u,0x7040u,0x7060u,0x7020u,0x6c60u,0x7048u,0x7068u,0x7028u,0x6c70u,0x7050u,0x7070u,0x7030u,0x6c00u,0x7058u,0x7078u,0x7038u,0x6c10u,0x70c0u,0x70e0u,0x70a0u,0x6ce0u,0x70c8u,0x70e8u,0x70a8u,0x6cf0u,0x70d0u,0x70f0u,0x70b0u,0x6c80u,0x70d8u,0x70f8u,0x70b8u,0x6c90u,0x7160u,0x7140u,0x7100u,0x6d40u,0x7168u,0x7148u,0x7108u,0x6d50u,0x7170u,0x7150u,0x7110u,0x6d20u,0x7178u,0x7158u,0x7118u,0x6d30u,0x71e0u,0x71c0u,0x7180u,0x6dc0u,0x71e8u,0x71c8u,0x7188u,0x6dd0u,0x71f0u,0x71d0u,0x7190u,0x6da0u,0x71f8u,0x71d8u,0x7198u,0x6db0u,0x7400u,0x7420u,0x7460u,0x6e60u,0x7408u,0x7428u,0x7468u,0x6e70u,0x7410u,0x7430u,0x7470u,0x6e00u,0x7418u,0x7438u,0x7478u,0x6e10u,0x7480u,0x74a0u,0x74e0u,0x6ee0u,0x7488u,0x74a8u,0x74e8u,0x6ef0u,0x7490u,0x74b0u,0x74f0u,0x6e80u,0x7498u,0x74b8u,0x74f8u,0x6e90u,0x7520u,0x7500u,0x7540u,0x6f40u,0x7528u,0x7508u,0x7548u,0x6f50u,0x7530u,0x7510u,0x7550u,0x6f20u,0x7538u,0x7518u,0x7558u,0x6f30u,0x75a0u,0x7580u,0x75c0u,0x6fc0u,0x75a8u,0x7588u,0x75c8u,0x6fd0u,0x75b0u,0x7590u,0x75d0u,0x6fa0u,0x75b8u,0x7598u,0x75d8u,0x6fb0u,0x6860u,0x1ea40u,0x1ec20u,0x2440u,0x6868u,0x1eed0u,0x1f0b0u,0x2450u,0x6870u,0x1f360u,0x1f540u,0x2460u,0x6878u,0x1f7f0u,0x1f9d0u,0x2470u,0x68e0u,0x1fc80u,0x1fe60u,0x2400u,0x68e8u,0x20110u,0x202f0u,0x2410u,0x68f0u,0x205a0u,0x20780u,0x2420u,0x68f8u,0x20a30u,0x20c10u,0x2430u,0x6940u,0x20ec0u,0x210a0u,0x25c0u,0x6948u,0x21350u,0x21530u,0x25d0u,0x6950u,0x217e0u,0x219c0u,0x25e0u,0x6958u,0x21c70u,0x21e50u,0x25f0u,0x69c0u,0x22100u,0x222e0u,0x2580u,0x69c8u,0x22590u,0x22770u,0x2590u,0x69d0u,0x22a20u,0x22c00u,0x25a0u,0x69d8u,0x22eb0u,0x23090u,0x25b0u,0x6a60u,0x1ea70u,0x1ec28u,0x2760u,0x6a68u,0x1ef00u,0x1f0b8u,0x2770u,0x6a70u,0x1f390u,0x1f548u,0x2740u,0x6a78u,0x1f820u,0x1f9d8u,0x2750u,0x6ae0u,0x1fcb0u,0x1fe68u,0x2720u,0x6ae8u,0x20140u,0x202f8u,0x2730u,0x6af0u,0x205d0u,0x20788u,0x2700u,0x6af8u,0x20a60u,0x20c18u,0x2710u,0x6b40u,0x20ef0u,0x210a8u,0x28e0u,0x6b48u,0x21380u,0x21538u,0x28f0u,0x6b50u,0x21810u,0x219c8u,0x28c0u,0x6b58u,0x21ca0u,0x21e58u,0x28d0u,0x6bc0u,0x22130u,0x222e8u,0x28a0u,0x6bc8u,0x225c0u,0x22778u,0x28b0u,0x6bd0u,0x22a50u,0x22c08u,0x2880u,0x6bd8u,0x22ee0u,0x23098u,0x2890u,0x6c20u,0x1eaa0u,0x1ec30u,0x2a40u,0x6c28u,0x1ef30u,0x1f0c0u,0x2a50u,0x6c30u,0x1f3c0u,0x1f550u,0x2a60u,0x6c38u,0x1f850u,0x1f9e0u,0x2a70u,0x6ca0u,0x1fce0u,0x1fe70u,0x2a00u,0x6ca8u,0x20170u,0x20300u,0x2a10u,0x6cb0u,0x20600u,0x20790u,0x2a20u,0x6cb8u,0x20a90u,0x20c20u,0x2a30u,0x6d00u,0x20f20u,0x210b0u,0x2bc0u,0x6d08u,0x213b0u,0x21540u,0x2bd0u,0x6d10u,0x21840u,0x219d0u,0x2be0u,0x6d18u,0x21cd0u,0x21e60u,0x2bf0u,0x6d80u,0x22160u,0x222f0u,0x2b80u,0x6d88u,0x225f0u,0x22780u,0x2b90u,0x6d90u,0x22a80u,0x22c10u,0x2ba0u,0x6d98u,0x22f10u,0x230a0u,0x2bb0u,0x6e20u,0x1ead0u,0x1ec38u,0x2d60u,0x6e28u,0x1ef60u,0x1f0c8u,0x2d70u,0x6e30u,0x1f3f0u,0x1f558u,0x2d40u,0x6e38u,0x1f880u,0x1f9e8u,0x2d50u,0x6ea0u,0x1fd10u,0x1fe78u,0x2d20u,0x6ea8u,0x201a0u,0x20308u,0x2d30u,0x6eb0u,0x20630u,0x20798u,0x2d00u,0x6eb8u,0x20ac0u,0x20c28u,0x2d10u,0x6f00u,0x20f50u,0x210b8u,0x2ee0u,0x6f08u,0x213e0u,0x21548u,0x2ef0u,0x6f10u,0x21870u,0x219d8u,0x2ec0u,0x6f18u,0x21d00u,0x21e68u,0x2ed0u,0x6f80u,0x22190u,0x222f8u,0x2ea0u,0x6f88u,0x22620u,0x22788u,0x2eb0u,0x6f90u,0x22ab0u,0x22c18u,0x2e80u,0x6f98u,0x22f40u,0x230a8u,0x2e90u,0x0u,0x40u,0x9000u,0xcc00u,0x80u,0xc0u,0x9010u,0xcc10u,0x110u,0x150u,0x9020u,0xcc20u,0x190u,0x1d0u,0x9030u,0xcc30u,0x220u,0x260u,0x9040u,0xcc40u,0x2a0u,0x2e0u,0x9050u,0xcc50u,0x330u,0x370u,0x9060u,0xcc60u,0x3b0u,0x3f0u,0x9070u,0xcc70u,0x440u,0x400u,0x9080u,0xcc80u,0x4c0u,0x480u,0x9090u,0xcc90u,0x550u,0x510u,0x90a0u,0xcca0u,0x5d0u,0x590u,0x90b0u,0xccb0u,0x660u,0x620u,0x90c0u,0xccc0u,0x6e0u,0x6a0u,0x90d0u,0xccd0u,0x770u,0x730u,0x90e0u,0xcce0u,0x7f0u,0x7b0u,0x90f0u,0xccf0u,0x10u,0x50u,0x9300u,0xcf00u,0x90u,0xd0u,0x9310u,0xcf10u,0x100u,0x140u,0x9320u,0xcf20u,0x180u,0x1c0u,0x9330u,0xcf30u,0x230u,0x270u,0x9340u,0xcf40u,0x2b0u,0x2f0u,0x9350u,0xcf50u,0x320u,0x360u,0x9360u,0xcf60u,0x3a0u,0x3e0u,0x9370u,0xcf70u,0x450u,0x410u,0x9380u,0xcf80u,0x4d0u,0x490u,0x9390u,0xcf90u,0x540u,0x500u,0x93a0u,0xcfa0u,0x5c0u,0x580u,0x93b0u,0xcfb0u,0x670u,0x630u,0x93c0u,0xcfc0u,0x6f0u,0x6b0u,0x93d0u,0xcfd0u,0x760u,0x720u,0x93e0u,0xcfe0u,0x7e0u,0x7a0u,0x93f0u,0xcff0u,0x20u,0x60u,0x9600u,0xd200u,0xa0u,0xe0u,0x9610u,0xd210u,0x130u,0x170u,0x9620u,0xd220u,0x1b0u,0x1f0u,0x9630u,0xd230u,0x200u,0x240u,0x9640u,0xd240u,0x280u,0x2c0u,0x9650u,0xd250u,0x310u,0x350u,0x9660u,0xd260u,0x390u,0x3d0u,0x9670u,0xd270u,0x460u,0x420u,0x9680u,0xd280u,0x4e0u,0x4a0u,0x9690u,0xd290u,0x570u,0x530u,0x96a0u,0xd2a0u,0x5f0u,0x5b0u,0x96b0u,0xd2b0u,0x640u,0x600u,0x96c0u,0xd2c0u,0x6c0u,0x680u,0x96d0u,0xd2d0u,0x750u,0x710u,0x96e0u,0xd2e0u,0x7d0u,0x790u,0x96f0u,0xd2f0u,0x30u,0x70u,0x9900u,0xd500u,0xb0u,0xf0u,0x9910u,0xd510u,0x120u,0x160u,0x9920u,0xd520u,0x1a0u,0x1e0u,0x9930u,0xd530u,0x210u,0x250u,0x9940u,0xd540u,0x290u,0x2d0u,0x9950u,0xd550u,0x300u,0x340u,0x9960u,0xd560u,0x380u,0x3c0u,0x9970u,0xd570u,0x470u,0x430u,0x9980u,0xd580u,0x4f0u,0x4b0u,0x9990u,0xd590u,0x560u,0x520u,0x99a0u,0xd5a0u,0x5e0u,0x5a0u,0x99b0u,0xd5b0u,0x650u,0x610u,0x99c0u,0xd5c0u,0x6d0u,0x690u,0x99d0u,0xd5d0u,0x740u,0x700u,0x99e0u,0xd5e0u,0x7c0u,0x780u,0x99f0u,0xd5f0u,0xc008u,0x12120u,0x12160u,0x12c00u,0xc018u,0x122b0u,0x122f0u,0x12c10u,0xc028u,0x12400u,0x12440u,0x12c20u,0xc038u,0x12590u,0x125d0u,0x12c30u,0xc048u,0x12760u,0x12720u,0x12c40u,0xc058u,0x128f0u,0x128b0u,0x12c50u,0xc068u,0x12a40u,0x12a00u,0x12c60u,0xc078u,0x12bd0u,0x12b90u,0x12c70u,0xc088u,0x12d20u,0x12d60u,0x12c80u,0xc098u,0x12eb0u,0x12ef0u,0x12c90u,0xc0a8u,0x13000u,0x13040u,0x12ca0u,0xc0b8u,0x13190u,0x131d0u,0x12cb0u,0xc0c8u,0x13360u,0x13320u,0x12cc0u,0xc0d8u,0x134f0u,0x134b0u,0x12cd0u,0xc0e8u,0x13640u,0x13600u,0x12ce0u,0xc0f8u,0x137d0u,0x13790u,0x12cf0u,0xc308u,0x12130u,0x12168u,0x12d00u,0xc318u,0x122a0u,0x122f8u,0x12d10u,0xc328u,0x12410u,0x12448u,0x12d20u,0xc338u,0x12580u,0x125d8u,0x12d30u,0xc348u,0x12770u,0x12728u,0x12d40u,0xc358u,0x128e0u,0x128b8u,0x12d50u,0xc368u,0x12a50u,0x12a08u,0x12d60u,0xc378u,0x12bc0u,0x12b98u,0x12d70u,0xc388u,0x12d30u,0x12d68u,0x12d80u,0xc398u,0x12ea0u,0x12ef8u,0x12d90u,0xc3a8u,0x13010u,0x13048u,0x12da0u,0xc3b8u,0x13180u,0x131d8u,0x12db0u,0xc3c8u,0x13370u,0x13328u,0x12dc0u,0xc3d8u,0x134e0u,0x134b8u,0x12dd0u,0xc3e8u,0x13650u,0x13608u,0x12de0u,0xc3f8u,0x137c0u,0x13798u,0x12df0u,0xc608u,0x12140u,0x12170u,0x12e00u,0xc618u,0x122d0u,0x122e0u,0x12e10u,0xc628u,0x12460u,0x12450u,0x12e20u,0xc638u,0x125f0u,0x125c0u,0x12e30u,0xc648u,0x12700u,0x12730u,0x12e40u,0xc658u,0x12890u,0x128a0u,0x12e50u,0xc668u,0x12a20u,0x12a10u,0x12e60u,0xc678u,0x12bb0u,0x12b80u,0x12e70u,0xc688u,0x12d40u,0x12d70u,0x12e80u,0xc698u,0x12ed0u,0x12ee0u,0x12e90u,0xc6a8u,0x13060u,0x13050u,0x12ea0u,0xc6b8u,0x131f0u,0x131c0u,0x12eb0u,0xc6c8u,0x13300u,0x13330u,0x12ec0u,0xc6d8u,0x13490u,0x134a0u,0x12ed0u,0xc6e8u,0x13620u,0x13610u,0x12ee0u,0xc6f8u,0x137b0u,0x13780u,0x12ef0u,0xc908u,0x12150u,0x12178u,0x12f00u,0xc918u,0x122c0u,0x122e8u,0x12f10u,0xc928u,0x12470u,0x12458u,0x12f20u,0xc938u,0x125e0u,0x125c8u,0x12f30u,0xc948u,0x12710u,0x12738u,0x12f40u,0xc958u,0x12880u,0x128a8u,0x12f50u,0xc968u,0x12a30u,0x12a18u,0x12f60u,0xc978u,0x12ba0u,0x12b88u,0x12f70u,0xc988u,0x12d50u,0x12d78u,0x12f80u,0xc998u,0x12ec0u,0x12ee8u,0x12f90u,0xc9a8u,0x13070u,0x13058u,0x12fa0u,0xc9b8u,0x131e0u,0x131c8u,0x12fb0u,0xc9c8u,0x13310u,0x13338u,0x12fc0u,0xc9d8u,0x13480u,0x134a8u,0x12fd0u,0xc9e8u,0x13630u,0x13618u,0x12fe0u,0xc9f8u,0x137a0u,0x13788u,0x12ff0u,0x8000u,0x1c300u,0x1ea40u,0x1ec20u,0x8080u,0x1c310u,0x100000u,0x1ec22u,0x8110u,0x1c320u,0x100000u,0x1ec24u,0x8190u,0x1c330u,0x100000u,0x1ec26u,0x8220u,0x1c340u,0x1ea40u,0x1ec28u,0x82a0u,0x1c350u,0x100000u,0x1ec2au,0x8330u,0x1c360u,0x100000u,0x1ec2cu,0x83b0u,0x1c370u,0x100000u,0x1ec2eu,0x8440u,0x1c380u,0x1ea40u,0x1ec30u,0x84c0u,0x1c390u,0x100000u,0x1ec32u,0x8550u,0x1c3a0u,0x100000u,0x1ec34u,0x85d0u,0x1c3b0u,0x100000u,0x1ec36u,0x8660u,0x1c3c0u,0x1ea40u,0x1ec38u,0x86e0u,0x1c3d0u,0x100000u,0x1ec3au,0x8770u,0x1c3e0u,0x100000u,0x1ec3cu,0x87f0u,0x1c3f0u,0x100000u,0x1ec3eu,0x8010u,0x1c400u,0x1ea70u,0x1ec20u,0x8090u,0x1c410u,0x100000u,0x1ec22u,0x8100u,0x1c420u,0x100000u,0x1ec24u,0x8180u,0x1c430u,0x100000u,0x1ec26u,0x8230u,0x1c440u,0x1ea70u,0x1ec28u,0x82b0u,0x1c450u,0x100000u,0x1ec2au,0x8320u,0x1c460u,0x100000u,0x1ec2cu,0x83a0u,0x1c470u,0x100000u,0x1ec2eu,0x8450u,0x1c480u,0x1ea70u,0x1ec30u,0x84d0u,0x1c490u,0x100000u,0x1ec32u,0x8540u,0x1c4a0u,0x100000u,0x1ec34u,0x85c0u,0x1c4b0u,0x100000u,0x1ec36u,0x8670u,0x1c4c0u,0x1ea70u,0x1ec38u,0x86f0u,0x1c4d0u,0x100000u,0x1ec3au,0x8760u,0x1c4e0u,0x100000u,0x1ec3cu,0x87e0u,0x1c4f0u,0x100000u,0x1ec3eu,0x8020u,0x1c500u,0x1eaa0u,0x1ec20u,0x80a0u,0x1c510u,0x100000u,0x1ec22u,0x8130u,0x1c520u,0x100000u,0x1ec24u,0x81b0u,0x1c530u,0x100000u,0x1ec26u,0x8200u,0x1c540u,0x1eaa0u,0x1ec28u,0x8280u,0x1c550u,0x100000u,0x1ec2au,0x8310u,0x1c560u,0x100000u,0x1ec2cu,0x8390u,0x1c570u,0x100000u,0x1ec2eu,0x8460u,0x1c580u,0x1eaa0u,0x1ec30u,0x84e0u,0x1c590u,0x100000u,0x1ec32u,0x8570u,0x1c5a0u,0x100000u,0x1ec34u,0x85f0u,0x1c5b0u,0x100000u,0x1ec36u,0x8640u,0x1c5c0u,0x1eaa0u,0x1ec38u,0x86c0u,0x1c5d0u,0x100000u,0x1ec3au,0x8750u,0x1c5e0u,0x100000u,0x1ec3cu,0x87d0u,0x1c5f0u,0x100000u,0x1ec3eu,0x8030u,0x1c600u,0x1ead0u,0x1ec20u,0x80b0u,0x1c610u,0x100000u,0x1ec22u,0x8120u,0x1c620u,0x100000u,0x1ec24u,0x81a0u,0x1c630u,0x100000u,0x1ec26u,0x8210u,0x1c640u,0x1ead0u,0x1ec28u,0x8290u,0x1c650u,0x100000u,0x1ec2au,0x8300u,0x1c660u,0x100000u,0x1ec2cu,0x8380u,0x1c670u,0x100000u,0x1ec2eu,0x8470u,0x1c680u,0x1ead0u,0x1ec30u,0x84f0u,0x1c690u,0x100000u,0x1ec32u,0x8560u,0x1c6a0u,0x100000u,0x1ec34u,0x85e0u,0x1c6b0u,0x100000u,0x1ec36u,0x8650u,0x1c6c0u,0x1ead0u,0x1ec38u,0x86d0u,0x1c6d0u,0x100000u,0x1ec3au,0x8740u,0x1c6e0u,0x100000u,0x1ec3cu,0x87c0u,0x1c6f0u,0x100000u,0x1ec3eu,0x27000u,0x1d480u,0x1d480u,0x80000000u,0x27002u,0x1d482u,0x100000u,0x80000000u,0x27004u,0x1d484u,0x100000u,0x80000000u,0x27006u,0x1d486u,0x100000u,0x80000000u,0x27200u,0x1d490u,0x1d480u,0x80000000u,0x27202u,0x1d492u,0x100000u,0x80000000u,0x27204u,0x1d494u,0x100000u,0x80000000u,0x27206u,0x1d496u,0x100000u,0x80000000u,0x27400u,0x1d4a0u,0x1d480u,0x80000000u,0x27402u,0x1d4a2u,0x100000u,0x80000000u,0x27404u,0x1d4a4u,0x100000u,0x80000000u,0x27406u,0x1d4a6u,0x100000u,0x80000000u,0x27600u,0x1d4b0u,0x1d480u,0x80000000u,0x27602u,0x1d4b2u,0x100000u,0x80000000u,0x27604u,0x1d4b4u,0x100000u,0x80000000u,0x27606u,0x1d4b6u,0x100000u,0x80000000u,0x27008u,0x1d488u,0x1d490u,0x80000000u,0x2700au,0x1d48au,0x100000u,0x80000000u,0x2700cu,0x1d48cu,0x100000u,0x80000000u,0x2700eu,0x1d48eu,0x100000u,0x80000000u,0x27208u,0x1d498u,0x1d490u,0x80000000u,0x2720au,0x1d49au,0x100000u,0x80000000u,0x2720cu,0x1d49cu,0x100000u,0x80000000u,0x2720eu,0x1d49eu,0x100000u,0x80000000u,0x27408u,0x1d4a8u,0x1d490u,0x80000000u,0x2740au,0x1d4aau,0x100000u,0x80000000u,0x2740cu,0x1d4acu,0x100000u,0x80000000u,0x2740eu,0x1d4aeu,0x100000u,0x80000000u,0x27608u,0x1d4b8u,0x1d490u,0x80000000u,0x2760au,0x1d4bau,0x100000u,0x80000000u,0x2760cu,0x1d4bcu,0x100000u,0x80000000u,0x2760eu,0x1d4beu,0x100000u,0x80000000u,0x26808u,0x1d4c0u,0x1d4a0u,0x80000000u,0x2680au,0x1d4c2u,0x100000u,0x80000000u,0x2680cu,0x1d4c4u,0x100000u,0x80000000u,0x2680eu,0x1d4c6u,0x100000u,0x80000000u,0x26a08u,0x1d4d0u,0x1d4a0u,0x80000000u,0x26a0au,0x1d4d2u,0x100000u,0x80000000u,0x26a0cu,0x1d4d4u,0x100000u,0x80000000u,0x26a0eu,0x1d4d6u,0x100000u,0x80000000u,0x26c08u,0x1d4e0u,0x1d4a0u,0x80000000u,0x26c0au,0x1d4e2u,0x100000u,0x80000000u,0x26c0cu,0x1d4e4u,0x100000u,0x80000000u,0x26c0eu,0x1d4e6u,0x100000u,0x80000000u,0x26e08u,0x1d4f0u,0x1d4a0u,0x80000000u,0x26e0au,0x1d4f2u,0x100000u,0x80000000u,0x26e0cu,0x1d4f4u,0x100000u,0x80000000u,0x26e0eu,0x1d4f6u,0x100000u,0x80000000u,0x100000u,0x1d4c8u,0x1d4b0u,0x80000000u,0x100000u,0x1d4cau,0x100000u,0x80000000u,0x100000u,0x1d4ccu,0x100000u,0x80000000u,0x100000u,0x1d4ceu,0x100000u,0x80000000u,0x100000u,0x1d4d8u,0x1d4b0u,0x80000000u,0x100000u,0x1d4dau,0x100000u,0x80000000u,0x100000u,0x1d4dcu,0x100000u,0x80000000u,0x100000u,0x1d4deu,0x100000u,0x80000000u,0x100000u,0x1d4e8u,0x1d4b0u,0x80000000u,0x100000u,0x1d4eau,0x100000u,0x80000000u,0x100000u,0x1d4ecu,0x100000u,0x80000000u,0x100000u,0x1d4eeu,0x100000u,0x80000000u,0x100000u,0x1d4f8u,0x1d4b0u,0x80000000u,0x100000u,0x1d4fau,0x100000u,0x80000000u,0x100000u,0x1d4fcu,0x100000u,0x80000000u,0x100000u,0x1d4feu,0x100000u,0x80000000u,0x30000u,0x60420u,0xbfc50u,0xf0000u,0x30010u,0x60430u,0xbfc40u,0xf0010u,0x30020u,0x60400u,0xbfc70u,0xf0020u,0x30030u,0x60410u,0xbfc60u,0xf0030u,0x30040u,0x60460u,0xbfc10u,0xf0040u,0x30050u,0x60470u,0xbfc00u,0xf0050u,0x30060u,0x60440u,0xbfc30u,0xf0060u,0x30070u,0x60450u,0xbfc20u,0xf0070u,0x30080u,0x604b0u,0xbfcd0u,0xf0080u,0x30090u,0x604a0u,0xbfcc0u,0xf0090u,0x300a0u,0x60490u,0xbfcf0u,0xf00a0u,0x300b0u,0x60480u,0xbfce0u,0xf00b0u,0x300c0u,0x604f0u,0xbfc90u,0xf00c0u,0x300d0u,0x604e0u,0xbfc80u,0xf00d0u,0x300e0u,0x604d0u,0xbfcb0u,0xf00e0u,0x300f0u,0x604c0u,0xbfca0u,0xf00f0u,0x30100u,0x60530u,0xbfd60u,0xf0100u,0x30110u,0x60520u,0xbfd70u,0xf0110u,0x30120u,0x60510u,0xbfd40u,0xf0120u,0x30130u,0x60500u,0xbfd50u,0xf0130u,0x30140u,0x60570u,0xbfd20u,0xf0140u,0x30150u,0x60560u,0xbfd30u,0xf0150u,0x30160u,0x60550u,0xbfd00u,0xf0160u,0x30170u,0x60540u,0xbfd10u,0xf0170u,0x30190u,0x605b0u,0xbfde0u,0xf0190u,0x30180u,0x605a0u,0xbfdf0u,0xf0180u,0x301b0u,0x60590u,0xbfdc0u,0xf01b0u,0x301a0u,0x60580u,0xbfdd0u,0xf01a0u,0x301d0u,0x605f0u,0xbfda0u,0xf01d0u,0x301c0u,0x605e0u,0xbfdb0u,0xf01c0u,0x301f0u,0x605d0u,0xbfd80u,0xf01f0u,0x301e0u,0x605c0u,0xbfd90u,0xf01e0u,0x30210u,0x60640u,0xbfe60u,0xf0210u,0x30200u,0x60650u,0xbfe70u,0xf0200u,0x30230u,0x60660u,0xbfe40u,0xf0230u,0x30220u,0x60670u,0xbfe50u,0xf0220u,0x30250u,0x60600u,0xbfe20u,0xf0250u,0x30240u,0x60610u,0xbfe30u,0xf0240u,0x30270u,0x60620u,0xbfe00u,0xf0270u,0x30260u,0x60630u,0xbfe10u,0xf0260u,0x30290u,0x606c0u,0xbfef0u,0xf0290u,0x30280u,0x606d0u,0xbfee0u,0xf0280u,0x302b0u,0x606e0u,0xbfed0u,0xf02b0u,0x302a0u,0x606f0u,0xbfec0u,0xf02a0u,0x302d0u,0x60680u,0xbfeb0u,0xf02d0u,0x302c0u,0x60690u,0xbfea0u,0xf02c0u,0x302f0u,0x606a0u,0xbfe90u,0xf02f0u,0x302e0u,0x606b0u,0xbfe80u,0xf02e0u,0x30320u,0x60740u,0xbff70u,0x11f720u,0x30330u,0x60750u,0xbff60u,0x11f730u,0x30300u,0x60760u,0xbff50u,0x11f700u,0x30310u,0x60770u,0xbff40u,0x11f710u,0x30360u,0x60700u,0xbff30u,0x11f760u,0x30370u,0x60710u,0xbff20u,0x11f770u,0x30340u,0x60720u,0xbff10u,0x11f740u,0x30350u,0x60730u,0xbff00u,0x11f750u,0x303a0u,0x8fbd0u,0xbfff0u,0x11f7a0u,0x303b0u,0x8fbc0u,0xbffe0u,0x11f7b0u,0x30380u,0x8fbf0u,0xbffd0u,0x11f780u,0x30390u,0x8fbe0u,0xbffc0u,0x11f790u,0x303e0u,0x8fb90u,0xbffb0u,0x11f7e0u,0x303f0u,0x8fb80u,0xbffa0u,0x11f7f0u,0x303c0u,0x8fbb0u,0xbff90u,0x11f7c0u,0x303d0u,0x8fba0u,0xbff80u,0x11f7d0u,0x0u,0x80000000u,0x0u,0x36030u,0x0u,0x80000000u,0x0u,0x36030u,0x0u,0x80000000u,0x0u,0x36030u,0x0u,0x80000000u,0x0u,0x36030u,0x0u,0x80000000u,0x0u,0x36030u,0x0u,0x80000000u,0x0u,0x36030u,0x0u,0x80000000u,0x0u,0x36030u,0x0u,0x80000000u,0x0u,0x36030u,0x0u,0x80000000u,0x0u,0x360b2u,0x0u,0x80000000u,0x0u,0x360b2u,0x0u,0x80000000u,0x0u,0x360b2u,0x0u,0x80000000u,0x0u,0x360b2u,0x0u,0x80000000u,0x0u,0x360b2u,0x0u,0x80000000u,0x0u,0x360b2u,0x0u,0x80000000u,0x0u,0x360b2u,0x0u,0x80000000u,0x0u,0x360b2u,0x0u,0x80000000u,0x0u,0x36092u,0x0u,0x80000000u,0x0u,0x36092u,0x0u,0x80000000u,0x0u,0x36092u,0x0u,0x80000000u,0x0u,0x36092u,0x0u,0x80000000u,0x0u,0x36092u,0x0u,0x80000000u,0x0u,0x36092u,0x0u,0x80000000u,0x0u,0x36092u,0x0u,0x80000000u,0x0u,0x36092u,0x0u,0x80000000u,0x0u,0x3609au,0x0u,0x80000000u,0x0u,0x3609au,0x0u,0x80000000u,0x0u,0x3609au,0x0u,0x80000000u,0x0u,0x3609au,0x0u,0x80000000u,0x0u,0x3609au,0x0u,0x80000000u,0x0u,0x3609au,0x0u,0x80000000u,0x0u,0x3609au,0x0u,0x80000000u,0x0u,0x3609au,0x0u,0x80000000u,0x0u,0x36059u,0x0u,0x80000000u,0x0u,0x36059u,0x0u,0x80000000u,0x0u,0x36059u,0x0u,0x80000000u,0x0u,0x36059u,0x0u,0x80000000u,0x0u,0x36059u,0x0u,0x80000000u,0x0u,0x36059u,0x0u,0x80000000u,0x0u,0x36059u,0x0u,0x80000000u,0x0u,0x36059u,0x0u,0x80000000u,0x0u,0x36059u,0x0u,0x80000000u,0x0u,0x36059u,0x0u,0x80000000u,0x0u,0x36059u,0x0u,0x80000000u,0x0u,0x36059u,0x0u,0x80000000u,0x0u,0x36059u,0x0u,0x80000000u,0x0u,0x36059u,0x0u,0x80000000u,0x0u,0x36059u,0x0u,0x80000000u,0x0u,0x36059u,0x0u,0x80000000u,0x0u,0x16071u,0x0u,0x80000000u,0x0u,0x16071u,0x0u,0x80000000u,0x0u,0x16071u,0x0u,0x80000000u,0x0u,0x16071u,0x0u,0x80000000u,0x0u,0x16071u,0x0u,0x80000000u,0x0u,0x16071u,0x0u,0x80000000u,0x0u,0x16071u,0x0u,0x80000000u,0x0u,0x16071u,0x0u,0x80000000u,0x0u,0x14071u,0x0u,0x80000000u,0x0u,0x14071u,0x0u,0x80000000u,0x0u,0x14071u,0x0u,0x80000000u,0x0u,0x14071u,0x0u,0x80000000u,0x0u,0x14071u,0x0u,0x80000000u,0x0u,0x14071u,0x0u,0x80000000u,0x0u,0x14071u,0x0u,0x80000000u,0x0u,0x14071u};

__global__ __launch_bounds__(256, 1) void swin_main(const uint16_t* x, const uint16_t* mask, char* ws, uint16_t* out,
                                                    const uint16_t* wqkv, const uint16_t* wo, const uint16_t* ls,
                                                    const uint16_t* w1, const uint16_t* w2, const uint16_t* rc,
                                                    const int* ridx) {
  __shared__ __attribute__((aligned(16))) char smem[LDS_BYTES];
  const int wave = __builtin_amdgcn_readfirstlane(threadIdx.x >> 6);
  const unsigned lds0 = (unsigned)(size_t)smem;
  asm volatile(
      ".p2align 5\n"
      "s_mov_b64 s[92:93], %0\n"
      "s_mov_b32 s40, %1\n"
      "s_mov_b32 s41, %2\n"
      "s_mov_b64 s[88:89], %3\n"
      "s_load_dwordx2 s[16:17], s[92:93], 0\n"
      "s_load_dwordx2 s[20:21], s[92:93], 32\n"
      "s_load_dwordx2 s[46:47], s[92:93], 40\n"
      "s_load_dwordx8 s[80:87], s[92:93], 48\n"
      "s_load_dwordx2 s[76:77], s[92:93], 80\n"
      "s_mov_b64 s[90:91], s[92:93]\n"
      "s_getpc_b64 s[98:99]\n"
      "L_ipf0_%=:\n"
      "v_mbcnt_lo_u32_b32 v8, -1, 0\n"
      "v_mbcnt_hi_u32_b32 v8, -1, v8\n"
      "v_lshlrev_b32 v233, 2, v8\n"
      "v_lshlrev_b32 v219, 4, v8\n"
      "s_cmp_lt_u32 s41, 209\n"
      "s_cselect_b32 s44, 20, 19\n"
      "s_lshl_b32 s92, s40, 8\n"
      "v_add_u32_e64 v50, s92, v233\n"
      "v_min_u32 v29, 704, v50\n"
      "v_add_u32 v29, 4096, v29\n"
      "v_min_u32 v50, 672, v50\n"
      "v_add_u32 v51, 122112, v50\n"
      "s_lshr_b32 s92, s41, 3\n"
      "s_lshl_b32 s92, s92, 7\n"
      "v_lshlrev_b32 v232, 7, v8\n"
      "s_mul_i32 s93, s92, 18\n"
      "s_mul_i32 s94, s40, 640\n"
      "v_min_u32 v235, 512, v232\n"
      "v_add_u32 v235, s94, v235\n"
      "v_min_u32 v235, 2176, v235\n"
      "v_add_u32 v235, s93, v235\n"
      "s_cmp_eq_u32 s40, 1\n"
      "s_cselect_b32 s93, 4096, 0\n"
      "s_add_u32 s92, s92, s93\n"
      "s_cmp_eq_u32 s40, 2\n"
      "s_cselect_b32 s93, 9, 8\n"
      "s_cselect_b64 s[72:73], s[98:99], s[88:89]\n"
      "s_cmp_lt_u32 s40, 2\n"
      "s_cselect_b32 s93, 27, s93\n"
      "s_mul_i32 s92, s92, s93\n"
      "s_sub_u32 s93, s93, 1\n"
      "s_lshl_b32 s93, s93, 7\n"
      "v_min_u32 v232, s93, v232\n"
      "v_add_u32 v232, s92, v232\n"
      "s_lshl_b32 s95, s40, 3\n"
      "v_lshrrev_b32 v234, 3, v8\n"
      "v_and_b32 v250, 7, v8\n"
      "v_add_u32 v249, s95, v234\n"
      "v_bfe_u32 v251, v249, 1, 3\n"
      "v_cmp_lt_u32 s[70:71], s44, v249\n"
      "v_min_u32_e64 v249, s44, v249\n"
      "s_mov_b32 s18, 100270464\n"
      "s_mov_b32 s19, 0x20000\n"
      "s_mov_b32 s78, 9604\n"
      "s_mov_b32 s79, 0x20000\n"
      "s_mov_b32 s22, 221184\n"
      "s_mov_b32 s23, 0x20000\n"
      "s_lshl_b32 s64, s40, 10\n"
      "v_add_u32 v36, s64, v219\n"
      "s_add_u32 s75, s64, 18432\n"
      "s_and_b32 s92, s40, 1\n"
      "s_lshl_b32 s92, s92, 2\n"
      "s_mul_i32 s93, s40, 9216\n"
      "v_and_b32 v248, 2, v234\n"
      "v_xor_b32 v248, v248, v250\n"
      "v_xor_b32_e64 v248, s92, v248\n"
      "v_lshl_add_u32 v248, v248, 4, s93\n"
      "v_mul_u32_u24 v187, 1152, v234\n"
      "v_add_u32 v187, v187, v248\n"
      "s_cmp_lt_u32 s40, 2\n"
      "s_cselect_b32 s65, s64, 163840\n"
      "s_waitcnt lgkmcnt(0)\n"
      "s_cselect_b64 s[72:73], s[20:21], s[72:73]\n"
      "global_load_dword v50, v50, s[86:87]\n"
      "global_load_dword a255, v232, s[72:73]\n"
      "global_load_dword a253, v235, s[46:47]\n"
      "s_mul_i32 s92, s40, 8192\n"
      "s_add_u32 s88, s88, s92\n"
      "s_addc_u32 s89, s89, 0\n"
      "s_add_u32 m0, s64, 124928\n"
      "s_movk_i32 s92, 0\n"
      "buffer_load_dwordx4 v36, s[76:79], s92 offen  lds\n"
      "s_add_u32 m0, s64, 129024\n"
      "s_movk_i32 s92, 4096\n"
      "buffer_load_dwordx4 v36, s[76:79], s92 offen  lds\n"
      "s_add_u32 m0, s64, 133120\n"
      "s_cmp_lt_u32 s40, 2\n"
      "s_cselect_b32 m0, m0, s65\n"
      "s_movk_i32 s92, 8192\n"
      "buffer_load_dwordx4 v36, s[76:79], s92 offen  lds\n"
      "s_load_dwordx16 s[0:15], s[82:83], 0\n"
      "s_load_dwordx16 s[48:63], s[84:85], 0\n"
      "s_load_dwordx16 s[24:39], s[84:85], 64\n"
      "s_load_dwordx2 s[66:67], s[80:81], 0\n"
      "s_movk_i32 s93, 4180\n"
      "s_cmp_lt_u32 s41, 209\n"
      "s_cselect_b32 s92, 209, 47\n"
      "s_cselect_b32 s93, 795, s93\n"
      "s_cmp_lt_u32 s41, 252\n"
      "s_cselect_b32 s94, 16, 15\n"
      "s_add_u32 s93, s93, s41\n"
      "v_lshl_add_u32 v232, v249, 8, s41\n"
      "v_mul_u32_u24 v235, s92, v249\n"
      "v_add_u32 v235, s93, v235\n"
      "v_cmp_lt_u32 vcc, s94, v249\n"
      "v_subrev_u32 v248, 5184, v235\n"
      "s_nop 0\n"
      "v_cndmask_b32 v232, v232, v235, vcc\n"
      "v_cmp_le_u32 vcc, 5184, v232\n"
      "v_mul_u32_u24 v46, 58255, v232\n"
      "v_lshrrev_b32 v46, 22, v46\n"
      "v_mul_u32_u24 v174, 72, v46\n"
      "v_sub_u32 v174, v232, v174\n"
      "v_min_u32 v235, 72, v248\n"
      "v_cndmask_b32 v46, v46, v235, vcc\n"
      "v_subrev_u32 v235, 72, v248\n"
      "v_min_u32 v235, 72, v235\n"
      "v_cndmask_b32 v174, v174, v235, vcc\n"
      "v_mad_u32_u24 v235, v46, 7, 9\n"
      "v_subrev_u32 v232, 0x1ff, v235\n"
      "v_min_u32 v235, v235, v232\n"
      "v_mad_u32_u24 v232, v174, 7, 9\n"
      "v_subrev_u32 v248, 0x1ff, v232\n"
      "v_min_u32 v232, v232, v248\n"
      "v_mul_u32_u24 v235, 0x1ff, v235\n"
      "v_add_u32 v235, v235, v232\n"
      "v_mul_u32_u24 v232, 0x180, v235\n"
      "v_bfrev_b32 v249, 1\n"
      "v_cndmask_b32 v232, v232, v249, s[70:71]\n"
      "v_xor_b32 v251, v251, v250\n"
      "v_lshl_add_u32 v232, v251, 4, v232\n"
      "s_add_u32 m0, s64, 149504\n"
      "s_nop 0\n"
      "buffer_load_dwordx4 v232, s[16:19], 0 offen  lds\n"
      "s_add_u32 m0, s64, 153472\n"
      "s_nop 0\n"
      "buffer_load_dwordx4 v232, s[16:19], 0 offen offset:128  lds\n"
      "s_add_u32 m0, s64, 157440\n"
      "s_nop 0\n"
      "buffer_load_dwordx4 v232, s[16:19], 0 offen offset:256  lds\n"
      "s_movk_i32 s95, 504\n"
      "s_and_b32 s92, s40, 1\n"
      "s_lshl_b32 s92, s92, 2\n"
      "v_lshrrev_b32 v232, 1, v234\n"
      "v_xor_b32 v232, v232, v250\n"
      "v_xor_b32 v232, s92, v232\n"
      "v_lshlrev_b32 v232, 4, v232\n"
      "s_lshl_b32 s92, s40, 3\n"
      "s_brev_b32 s43, 1\n"
      "s_cmp_lt_u32 s40, 2\n"
      "s_cselect_b32 s93, 32, -16\n"
      "s_cselect_b32 s94, 0, 128\n"
      "s_cselect_b32 s43, 256, s43\n"
      "v_add_u32 v235, s92, v234\n"
      "v_mul_u32_u24 v234, 37, v235\n"
      "v_lshrrev_b32_e64 v234, 8, v234\n"
      "v_mad_u32_u24 v234, v234, s95, v235\n"
      "v_mul_u32_u24 v234, 384, v234\n"
      "v_add_u32 v16, v234, v232\n"
      "v_add_u32 v234, s93, v235\n"
      "v_mul_u32_u24 v251, 37, v234\n"
      "v_lshrrev_b32 v251, 8, v251\n"
      "v_mad_u32_u24 v251, v251, s95, v234\n"
      "v_mul_u32_u24 v251, 384, v251\n"
      "v_add3_u32 v17, v251, v232, s94\n"
      "v_add_u32_e64 v234, 16, v235\n"
      "v_mul_u32_u24_e64 v235, 37, v234\n"
      "v_lshrrev_b32_e64 v235, 8, v235\n"
      "v_mad_u32_u24 v235, v235, s95, v234\n"
      "v_mul_u32_u24 v235, 384, v235\n"
      "s_movk_i32 s94, 128\n"
      "v_add3_u32 v18, v235, v232, s94\n"
      "v_add_u32 v19, 256, v16\n"
      "v_add_u32_e64 v15, s43, v17\n"
      "s_add_u32 s92, s41, 0\n"
      "s_mul_i32 s93, s92, 58255\n"
      "s_lshr_b32 s93, s93, 22\n"
      "s_mul_i32 s93, s93, 439\n"
      "s_add_u32 s45, s92, s93\n"
      "s_mul_i32 s45, s45, 2688\n"
      "s_add_u32 s45, s45, 589824\n"
      "s_add_u32 m0, s64, 0\n"
      "s_nop 0\n"
      "buffer_load_dwordx4 v16, s[16:19], s45 offen  lds\n"
      "s_add_u32 m0, s64, 4096\n"
      "s_nop 0\n"
      "buffer_load_dwordx4 v17, s[16:19], s45 offen  lds\n"
      "s_add_u32 m0, s64, 8192\n"
      "s_nop 0\n"
      "buffer_load_dwordx4 v18, s[16:19], s45 offen  lds\n"
      "s_add_u32 m0, s64, 12288\n"
      "s_nop 0\n"
      "buffer_load_dwordx4 v19, s[16:19], s45 offen  lds\n"
      "s_add_u32 m0, s65, 16384\n"
      "s_nop 0\n"
      "buffer_load_dwordx4 v15, s[16:19], s45 offen  lds\n"
      "s_add_u32 s92, s41, 0\n"
      "s_mul_i32 s93, s92, 58255\n"
      "s_lshr_b32 s93, s93, 22\n"
      "s_add_u32 s47, s92, s93\n"
      "s_mul_i32 s47, s47, 4802\n"
      "s_and_b32 s47, s47, -4\n"
      "s_add_u32 s92, s41, 256\n"
      "s_mul_i32 s93, s92, 58255\n"
      "s_lshr_b32 s93, s93, 22\n"
      "s_add_u32 s74, s92, s93\n"
      "s_mul_i32 s74, s74, 4802\n"
      "s_and_b32 s74, s74, -4\n"
      "s_mul_i32 s93, s93, 439\n"
      "s_add_u32 s46, s92, s93\n"
      "s_mul_i32 s46, s46, 2688\n"
      "s_add_u32 s46, s46, 589824\n"
      "v_subrev_u32 v232, 2, v8\n"
      "v_cmp_ge_u32 s[72:73], s44, v232\n"
      "v_min_u32_e64 v232, s44, v232\n"
      "s_movk_i32 s93, 4180\n"
      "s_cmp_lt_u32 s41, 209\n"
      "s_cselect_b32 s92, 209, 47\n"
      "s_cselect_b32 s93, 795, s93\n"
      "s_cmp_lt_u32 s41, 252\n"
      "s_cselect_b32 s94, 16, 15\n"
      "s_add_u32 s93, s93, s41\n"
      "v_lshl_add_u32 v235, v232, 8, s41\n"
      "v_mul_u32_u24 v234, s92, v232\n"
      "v_add_u32_e64 v234, s93, v234\n"
      "v_cmp_lt_u32 vcc, s94, v232\n"
      "v_subrev_u32 v251, 5184, v234\n"
      "s_nop 0\n"
      "v_cndmask_b32 v235, v235, v234, vcc\n"
      "v_cmp_le_u32 vcc, 5184, v235\n"
      "v_mul_u32_u24 v250, 58255, v235\n"
      "v_lshrrev_b32 v250, 22, v250\n"
      "v_mul_u32_u24 v248, 72, v250\n"
      "v_sub_u32_e64 v248, v235, v248\n"
      "v_min_u32 v234, 72, v251\n"
      "v_cndmask_b32 v250, v250, v234, vcc\n"
      "v_subrev_u32 v234, 72, v251\n"
      "v_min_u32 v234, 72, v234\n"
      "v_cndmask_b32 v248, v248, v234, vcc\n"
      "v_mul_u32_u24 v38, 73, v250\n"
      "v_add_u32 v38, v38, v248\n"
      "v_mul_u32_u24 v38, 4802, v38\n"
      "v_mul_u32_u24 v232, 0x1ff, v250\n"
      "v_add_u32_e64 v232, v232, v248\n"
      "v_mul_u32_u24 v232, 2688, v232\n"
      "v_add_u32 v232, 589824, v232\n"
      "v_cmp_eq_u32 vcc, 72, v250\n"
      "v_or_b32 v174, 0x80000000, v232\n"
      "s_nop 0\n"
      "v_cndmask_b32 v232, v232, v174, vcc\n"
      "v_cmp_eq_u32 vcc, 72, v248\n"
      "v_or_b32_e64 v174, 0x40000000, v232\n"
      "s_nop 0\n"
      "v_cndmask_b32 v232, v232, v174, vcc\n"
      "v_mov_b32 v234, 0x20000000\n"
      "v_cndmask_b32 v232, v234, v232, s[72:73]\n"
      "v_cndmask_b32 v38, v234, v38, s[72:73]\n"
      "v_mov_b32 v37, v234\n"
      "s_nop 1\n"
      "v_mov_b32_dpp v37, v232 wave_shr:1 row_mask:0xf bank_mask:0xf\n"
      "v_mov_b32 v79, 0\n"
      "v_mov_b32 v83, 0\n"
      "v_mov_b32 v87, 0\n"
      "v_mov_b32 v91, 0\n"
      "v_mov_b32 v95, 0\n"
      "s_lshl_b32 s94, s40, 2\n"
      "s_add_u32 s94, s94, 115456\n"
      "v_add_u32 v31, s94, v219\n"
      "v_and_b32 v174, 3, v8\n"
      "v_cmp_ne_u32 vcc, 0, v174\n"
      "v_mov_b32 v174, 0x3f803f80\n"
      "s_nop 0\n"
      "v_cndmask_b32_e64 v32, v174, 0, vcc\n"
      "v_mov_b32 v33, v32\n"
      "v_mov_b32 v34, v32\n"
      "v_mov_b32 v35, v32\n"
      "v_and_b32 v136, 15, v8\n"
      "v_lshrrev_b32_e64 v139, 4, v8\n"
      "v_mul_u32_u24 v136, 196, v136\n"
      "v_add_u32 v136, 124928, v136\n"
      "v_lshl_add_u32 v139, v139, 4, v136\n"
      "v_min_u32 v239, 192, v233\n"
      "v_add_u32 v239, 124928, v239\n"
      "global_load_dwordx4 v[232:235], v219, s[88:89]\n"
      "s_waitcnt lgkmcnt(0)\n"
      "s_pack_ll_b32_b16 s92, s0, s8\n"
      "s_waitcnt vmcnt(14)\n"
      "v_dot2_f32_bf16 v46, s92, v50, 0\n"
      "s_pack_hh_b32_b16 s93, s0, s8\n"
      "v_dot2_f32_bf16 v43, s93, v50, 0\n"
      "s_pack_ll_b32_b16 s94, s1, s9\n"
      "v_dot2_f32_bf16 v212, s94, v50, 0\n"
      "s_pack_hh_b32_b16 s95, s1, s9\n"
      "v_dot2_f32_bf16 v42, s95, v50, 0\n"
      "s_pack_ll_b32_b16 s92, s2, s10\n"
      "v_dot2_f32_bf16 v193, s92, v50, 0\n"
      "s_pack_hh_b32_b16 s93, s2, s10\n"
      "v_dot2_f32_bf16 v177, s93, v50, 0\n"
      "s_pack_ll_b32_b16 s94, s3, s11\n"
      "v_dot2_f32_bf16 v175, s94, v50, 0\n"
      "s_pack_hh_b32_b16 s95, s3, s11\n"
      "v_dot2_f32_bf16 v176, s95, v50, 0\n"
      "s_pack_ll_b32_b16 s92, s4, s12\n"
      "v_dot2_f32_bf16 v47, s92, v50, 0\n"
      "s_pack_hh_b32_b16 s93, s4, s12\n"
      "v_dot2_f32_bf16 v215, s93, v50, 0\n"
      "s_pack_ll_b32_b16 s94, s5, s13\n"
      "v_dot2_f32_bf16 v213, s94, v50, 0\n"
      "global_load_dwordx4 v[248:251], v219, s[88:89] offset:1024\n"
      "s_pack_hh_b32_b16 s95, s5, s13\n"
      "v_dot2_f32_bf16 v214, s95, v50, 0\n"
      "s_pack_ll_b32_b16 s92, s6, s14\n"
      "v_dot2_f32_bf16 v126, s92, v50, 0\n"
      "s_pack_hh_b32_b16 s93, s6, s14\n"
      "v_dot2_f32_bf16 v192, s93, v50, 0\n"
      "s_pack_ll_b32_b16 s94, s7, s15\n"
      "v_dot2_f32_bf16 v194, s94, v50, 0\n"
      "s_pack_hh_b32_b16 s95, s7, s15\n"
      "v_dot2_f32_bf16 v48, s95, v50, 0\n"
      "s_lshl_b32 s92, s40, 4\n"
      "s_lshr_b64 s[66:67], s[66:67], s92\n"
      "s_lshl_b32 s66, s66, 16\n"
      "v_mov_b32 v50, s66\n"
      "v_max_f32 v50, 0xc2c80000, v50\n"
      "v_min_f32 v50, 0x40930000, v50\n"
      "v_mul_f32 v50, 0x3fb8aa3b, v50\n"
      "v_exp_f32 v50, v50\n"
      "s_nop 0\n"
      "v_cvt_pk_bf16_f32 v50, v50, v50\n"
      "v_lshlrev_b32 v50, 16, v50\n"
      "v_max_f32 v46, 0, v46\n"
      "v_max_f32_e64 v43, 0, v43\n"
      "global_load_dwordx4 v[0:3], v219, s[88:89] offset:2048\n"
      "v_cvt_pk_bf16_f32 v46, v46, v43\n"
      "v_max_f32 v212, 0, v212\n"
      "v_max_f32 v42, 0, v42\n"
      "v_cvt_pk_bf16_f32 v212, v212, v42\n"
      "v_max_f32 v193, 0, v193\n"
      "v_max_f32 v177, 0, v177\n"
      "v_cvt_pk_bf16_f32 v193, v193, v177\n"
      "v_max_f32 v175, 0, v175\n"
      "v_max_f32 v176, 0, v176\n"
      "v_cvt_pk_bf16_f32 v175, v175, v176\n"
      "v_max_f32 v47, 0, v47\n"
      "v_max_f32 v215, 0, v215\n"
      "v_cvt_pk_bf16_f32 v47, v47, v215\n"
      "v_max_f32 v213, 0, v213\n"
      "v_max_f32 v214, 0, v214\n"
      "v_cvt_pk_bf16_f32 v213, v213, v214\n"
      "v_max_f32_e64 v126, 0, v126\n"
      "v_max_f32_e64 v192, 0, v192\n"
      "v_cvt_pk_bf16_f32 v126, v126, v192\n"
      "v_max_f32_e64 v194, 0, v194\n"
      "v_max_f32_e64 v48, 0, v48\n"
      "v_cvt_pk_bf16_f32 v194, v194, v48\n"
      "s_mov_b32 s42, 0\n"
      "s_add_u32 m0, s75, 0\n"
      "s_nop 0\n"
      "buffer_load_dwordx4 v187, s[20:23], s42 offen  lds\n"
      "s_pack_ll_b32_b16 s92, s48, s50\n"
      "v_dot2_f32_bf16 v42, s92, v46, 0\n"
      "s_pack_hh_b32_b16 s93, s48, s50\n"
      "v_dot2_f32_bf16 v43, s93, v46, 0\n"
      "s_pack_ll_b32_b16 s94, s49, s51\n"
      "v_dot2_f32_bf16 v215, s94, v46, 0\n"
      "s_pack_hh_b32_b16 s95, s49, s51\n"
      "v_dot2_f32_bf16 v214, s95, v46, 0\n"
      "s_pack_ll_b32_b16 s92, s52, s54\n"
      "v_dot2_f32_bf16 v42, s92, v212, v42\n"
      "s_pack_hh_b32_b16 s93, s52, s54\n"
      "v_dot2_f32_bf16 v43, s93, v212, v43\n"
      "s_pack_ll_b32_b16 s94, s53, s55\n"
      "v_dot2_f32_bf16 v215, s94, v212, v215\n"
      "s_pack_hh_b32_b16 s95, s53, s55\n"
      "v_dot2_f32_bf16 v214, s95, v212, v214\n"
      "s_pack_ll_b32_b16 s92, s56, s58\n"
      "v_dot2_f32_bf16 v42, s92, v193, v42\n"
      "s_pack_hh_b32_b16 s93, s56, s58\n"
      "v_dot2_f32_bf16 v43, s93, v193, v43\n"
      "s_pack_ll_b32_b16 s94, s57, s59\n"
      "v_dot2_f32_bf16 v215, s94, v193, v215\n"
      "s_add_u32 m0, s75, 3968\n"
      "s_nop 0\n"
      "buffer_load_dwordx4 v187, s[20:23], s42 offen offset:128  lds\n"
      "s_pack_hh_b32_b16 s95, s57, s59\n"
      "v_dot2_f32_bf16 v214, s95, v193, v214\n"
      "s_pack_ll_b32_b16 s92, s60, s62\n"
      "v_dot2_f32_bf16 v42, s92, v175, v42\n"
      "s_pack_hh_b32_b16 s93, s60, s62\n"
      "v_dot2_f32_bf16 v43, s93, v175, v43\n"
      "s_pack_ll_b32_b16 s94, s61, s63\n"
      "v_dot2_f32_bf16 v215, s94, v175, v215\n"
      "s_pack_hh_b32_b16 s95, s61, s63\n"
      "v_dot2_f32_bf16 v214, s95, v175, v214\n"
      "s_pack_ll_b32_b16 s92, s24, s26\n"
      "v_dot2_f32_bf16 v42, s92, v47, v42\n"
      "s_pack_hh_b32_b16 s93, s24, s26\n"
      "v_dot2_f32_bf16 v43, s93, v47, v43\n"
      "s_pack_ll_b32_b16 s94, s25, s27\n"
      "v_dot2_f32_bf16 v215, s94, v47, v215\n"
      "s_pack_hh_b32_b16 s95, s25, s27\n"
      "v_dot2_f32_bf16 v214, s95, v47, v214\n"
      "s_pack_ll_b32_b16 s92, s28, s30\n"
      "v_dot2_f32_bf16 v42, s92, v213, v42\n"
      "s_pack_hh_b32_b16 s93, s28, s30\n"
      "v_dot2_f32_bf16 v43, s93, v213, v43\n"
      "s_pack_ll_b32_b16 s94, s29, s31\n"
      "s_add_u32 m0, s75, 7936\n"
      "s_nop 0\n"
      "buffer_load_dwordx4 v187, s[20:23], s42 offen offset:256  lds\n"
      "v_dot2_f32_bf16 v215, s94, v213, v215\n"
      "s_pack_hh_b32_b16 s95, s29, s31\n"
      "v_dot2_f32_bf16 v214, s95, v213, v214\n"
      "s_pack_ll_b32_b16 s92, s32, s34\n"
      "v_dot2_f32_bf16 v42, s92, v126, v42\n"
      "s_pack_hh_b32_b16 s93, s32, s34\n"
      "v_dot2_f32_bf16 v43, s93, v126, v43\n"
      "s_pack_ll_b32_b16 s94, s33, s35\n"
      "v_dot2_f32_bf16 v215, s94, v126, v215\n"
      "s_pack_hh_b32_b16 s95, s33, s35\n"
      "v_dot2_f32_bf16 v214, s95, v126, v214\n"
      "s_pack_ll_b32_b16 s92, s36, s38\n"
      "v_dot2_f32_bf16 v42, s92, v194, v42\n"
      "s_pack_hh_b32_b16 s93, s36, s38\n"
      "v_dot2_f32_bf16 v43, s93, v194, v43\n"
      "s_pack_ll_b32_b16 s94, s37, s39\n"
      "v_dot2_f32_bf16 v215, s94, v194, v215\n"
      "s_pack_hh_b32_b16 s95, s37, s39\n"
      "v_dot2_f32_bf16 v214, s95, v194, v214\n"
      "s_load_dwordx2 s[28:29], s[90:91], 8\n"
      "s_load_dwordx2 s[34:35], s[90:91], 16\n"
      "s_load_dwordx2 s[24:25], s[90:91], 24\n"
      "s_add_u32 m0, s75, 11904\n"
      "s_nop 0\n"
      "buffer_load_dwordx4 v187, s[20:23], s42 offen offset:384  lds\n"
      "s_load_dwordx2 s[36:37], s[90:91], 40\n"
      "s_mov_b32 s26, 100270464\n"
      "s_mov_b32 s27, 0x20000\n"
      "s_mov_b32 s38, 73728\n"
      "s_mov_b32 s39, 0x20000\n"
      "s_mov_b32 s30, 25589860\n"
      "s_mov_b32 s31, 0x20000\n"
      "s_mov_b32 s53, 0x3fb8aa3b\n"
      "s_mov_b32 s51, 0x2a8cbccc\n"
      "s_mov_b32 s52, 0x38fba882\n"
      "s_mov_b32 s58, 0xffff0000\n"
      "s_mov_b32 s59, 0x0\n"
      "s_mov_b32 s60, 0x0\n"
      "s_mov_b32 s61, 0xffff\n"
      "s_mov_b32 s62, 0x0\n"
      "s_mov_b32 s63, 0xffff0000\n"
      "v_mov_b32 v174, 0x41f00000\n"
      "v_cmp_nle_f32 s[54:55], v50, v174\n"
      "v_mul_f32 v50, s53, v50\n"
      "s_nop 0\n"
      "v_readfirstlane_b32 s50, v50\n"
      "v_cvt_pk_bf16_f32 v42, v42, v42\n"
      "v_lshlrev_b32 v42, 16, v42\n"
      "s_add_u32 m0, s75, 15872\n"
      "s_nop 0\n"
      "buffer_load_dwordx4 v187, s[20:23], s42 offen offset:512  lds\n"
      "v_mul_f32 v42, 0xbfb8aa3b, v42\n"
      "v_exp_f32 v42, v42\n"
      "s_nop 0\n"
      "v_add_f32 v42, 1.0, v42\n"
      "v_rcp_f32 v42, v42\n"
      "s_nop 0\n"
      "v_cvt_pk_bf16_f32 v42, v42, v42\n"
      "v_lshlrev_b32 v42, 16, v42\n"
      "v_add_f32_e64 v42, -0.5, v42\n"
      "v_mul_f32 v42, 0x41b8aa3b, v42\n"
      "ds_write_b32 v51, v42\n"
      "v_cvt_pk_bf16_f32 v43, v43, v43\n"
      "v_lshlrev_b32 v43, 16, v43\n"
      "v_mul_f32 v43, 0xbfb8aa3b, v43\n"
      "v_exp_f32 v43, v43\n"
      "s_nop 0\n"
      "v_add_f32 v43, 1.0, v43\n"
      "v_rcp_f32 v43, v43\n"
      "s_nop 0\n"
      "v_cvt_pk_bf16_f32 v43, v43, v43\n"
      "v_lshlrev_b32 v43, 16, v43\n"
      "v_add_f32 v43, -0.5, v43\n"
      "v_mul_f32 v43, 0x41b8aa3b, v43\n"
      "ds_write_b32 v51, v43 offset:704\n"
      "v_cvt_pk_bf16_f32 v215, v215, v215\n"
      "v_lshlrev_b32_e64 v215, 16, v215\n"
      "s_add_u32 m0, s75, 19840\n"
      "s_nop 0\n"
      "buffer_load_dwordx4 v187, s[20:23], s42 offen offset:640  lds\n"
      "v_mul_f32 v215, 0xbfb8aa3b, v215\n"
      "v_exp_f32 v215, v215\n"
      "s_nop 0\n"
      "v_add_f32 v215, 1.0, v215\n"
      "v_rcp_f32 v215, v215\n"
      "s_nop 0\n"
      "v_cvt_pk_bf16_f32 v215, v215, v215\n"
      "v_lshlrev_b32 v215, 16, v215\n"
      "v_add_f32 v215, -0.5, v215\n"
      "v_mul_f32 v215, 0x41b8aa3b, v215\n"
      "ds_write_b32 v51, v215 offset:1408\n"
      "v_cvt_pk_bf16_f32 v214, v214, v214\n"
      "v_lshlrev_b32_e64 v214, 16, v214\n"
      "v_mul_f32 v214, 0xbfb8aa3b, v214\n"
      "v_exp_f32 v214, v214\n"
      "s_nop 0\n"
      "v_add_f32 v214, 1.0, v214\n"
      "v_rcp_f32_e64 v214, v214\n"
      "s_nop 0\n"
      "v_cvt_pk_bf16_f32 v214, v214, v214\n"
      "v_lshlrev_b32 v214, 16, v214\n"
      "v_add_f32 v214, -0.5, v214\n"
      "v_mul_f32 v214, 0x41b8aa3b, v214\n"
      "ds_write_b32 v51, v214 offset:2112\n"
      "s_mul_i32 s76, s40, 704\n"
      "s_add_u32 s76, s76, 122112\n"
      "s_mov_b32 s43, 36864\n"
      "s_add_u32 m0, s75, 24576\n"
      "s_nop 0\n"
      "buffer_load_dwordx4 v187, s[20:23], s43 offen  lds\n"
      "s_add_u32 m0, s75, 28544\n"
      "s_nop 0\n"
      "buffer_load_dwordx4 v187, s[20:23], s43 offen offset:128  lds\n"
      "s_add_u32 m0, s75, 32512\n"
      "s_nop 0\n"
      "buffer_load_dwordx4 v187, s[20:23], s43 offen offset:256  lds\n"
      "s_add_u32 m0, s75, 36480\n"
      "s_nop 0\n"
      "buffer_load_dwordx4 v187, s[20:23], s43 offen offset:384  lds\n"
      "s_add_u32 m0, s75, 40448\n"
      "s_nop 0\n"
      "buffer_load_dwordx4 v187, s[20:23], s43 offen offset:512  lds\n"
      "s_add_u32 m0, s75, 44416\n"
      "s_nop 0\n"
      "buffer_load_dwordx4 v187, s[20:23], s43 offen offset:640  lds\n"
      "s_mov_b32 s45, s46\n"
      "s_mov_b32 s49, s47\n"
      "s_mov_b32 s66, 0\n"
      "s_mov_b32 s67, 0\n"
      "s_mov_b32 s68, 0\n"
      "s_mov_b32 s69, 0\n"
      "s_mov_b32 s70, 0\n"
      "s_mov_b32 s71, 0\n"
      "s_mov_b32 s48, 0\n"
      "s_mov_b32 s33, 0x7fff7fff\n"
      "s_mov_b32 s42, 1\n"
      "s_mov_b32 s43, 5\n"
      "v_readlane_b32 s47, v38, 4\n"
      "s_nop 0\n"
      "s_and_b32 s47, s47, -4\n"
      "s_waitcnt vmcnt(12)\n"
      "v_add_u32 v200, 149504, v0\n"
      "v_add_u32 v116, 149504, v1\n"
      "v_add_u32 v140, 49152, v232\n"
      "v_add_u32 v143, 49152, v233\n"
      "v_add_u32 v142, 49152, v234\n"
      "v_add_u32 v89, 49152, v235\n"
      "v_add_u32 v82, 49152, v248\n"
      "s_waitcnt vmcnt(6)\n"
      "s_waitcnt lgkmcnt(0)\n"
      "s_barrier\n"
      "s_add_u32 m0, s75, 49152\n"
      "s_mov_b32 s78, 73728\n"
      "buffer_load_dwordx4 v187, s[20:23], s78 offen  lds\n"
      "ds_read_b128 a[224:227], v0 offset:2048\n"
      "ds_read_b64_tr_b16 a[24:25], v233\n"
      "ds_read_b128 a[220:223], v0\n"
      "ds_read_b64_tr_b16 a[120:121], v234 offset:12288\n"
      "s_add_u32 m0, s75, 53120\n"
      "ds_read_b64_tr_b16 a[0:1], v232\n"
      "buffer_load_dwordx4 v187, s[20:23], s78 offen offset:128  lds\n"
      "ds_read_b128 a[244:247], v1 offset:4096\n"
      "ds_read_b128 a[228:231], v0 offset:4096\n"
      "ds_read_b64_tr_b16 a[50:51], v234 offset:512\n"
      "ds_read_b64_tr_b16 a[74:75], v232 offset:12800\n"
      "s_mov_b32 s83, 0x0\n"
      "s_add_u32 m0, s75, 57088\n"
      "ds_read_b64_tr_b16 a[122:123], v234 offset:12800\n"
      "buffer_load_dwordx4 v187, s[20:23], s78 offen offset:256  lds\n"
      "ds_read_b64_tr_b16 a[2:3], v232 offset:512\n"
      "ds_read_b64_tr_b16 a[98:99], v233 offset:12800\n"
      "ds_read2_b32 v[50:51], v139 offset0:34 offset1:35\n"
      "s_mov_b32 s79, 110592\n"
      "ds_read_b64_tr_b16 a[48:49], v234\n"
      "s_add_u32 m0, s75, 61056\n"
      "ds_read2_b32 v[42:43], v139 offset0:2 offset1:3\n"
      "buffer_load_dwordx4 v187, s[20:23], s78 offen offset:384  lds\n"
      "ds_read_b64_tr_b16 a[26:27], v233 offset:512\n"
      "ds_read_b64_tr_b16 a[96:97], v233 offset:12288\n"
      "s_add_u32 m0, s75, 65024\n"
      "ds_read_b128 a[216:219], v200\n"
      "v_add_u32 v44, 6272, v139\n"
      "s_waitcnt lgkmcnt(7)\n"
      "v_mfma_f32_16x16x32_bf16 v[212:215], a[120:123], a[228:231], 0\n"
      "ds_read_b64_tr_b16 a[72:73], v232 offset:12288\n"
      "buffer_load_dwordx4 v187, s[20:23], s78 offen offset:512  lds\n"
      "v_add_u32 v225, 3136, v139\n"
      "v_mfma_f32_16x16x32_bf16 v[174:177], a[0:3], a[220:223], 0\n"
      "ds_read_b128 a[232:235], v116\n"
      "s_add_u32 m0, s75, 68992\n"
      "ds_read_b128 a[240:243], v1 offset:2048\n"
      "v_mfma_f32_16x16x32_bf16 v[192:195], a[120:123], a[220:223], 0\n"
      "ds_read2_b32 v[46:47], v139 offset0:18 offset1:19\n"
      "ds_read_b32 v100, v136 offset:192\n"
      "buffer_load_dwordx4 v187, s[20:23], s78 offen offset:640  lds\n"
      "ds_read2_b32 v[48:49], v139 offset0:32 offset1:33\n"
      "v_mfma_f32_16x16x32_bf16 v[124:127], a[120:123], a[224:227], 0\n"
      "ds_read2_b32 v[72:73], v44 offset0:32 offset1:33\n"
      "ds_read_b32 v138, v239 offset:9408\n"
      "v_mfma_f32_16x16x32_bf16 v[208:211], a[0:3], a[228:231], 0\n"
      "ds_read2_b32 v[68:69], v44 offset0:16 offset1:17\n"
      "s_waitcnt lgkmcnt(7)\n"
      "v_mfma_f32_16x16x32_bf16 v[236:239], a[120:123], a[216:219], 0\n"
      "global_load_dwordx4 v[4:7], v219, s[88:89] offset:3072\n"
      "s_waitcnt vmcnt(7)\n"
      "s_barrier\n"
      "v_mfma_f32_16x16x32_bf16 v[240:243], a[96:99], a[216:219], 0\n"
      "s_add_u32 s88, s88, 4096\n"
      "s_addc_u32 s89, s89, 0\n"
      "ds_read_b64_tr_b16 a[4:5], v232 offset:24576\n"
      "ds_read_b64_tr_b16 a[102:103], v233 offset:37376\n"
      "ds_read_b64_tr_b16 a[30:31], v233 offset:25088\n"
      "ds_read_b64_tr_b16 a[76:77], v232 offset:36864\n"
      "global_load_dwordx4 v[8:11], v219, s[88:89]\n"
      "ds_read_b64_tr_b16 a[28:29], v233 offset:24576\n"
      "ds_read_b64_tr_b16 a[78:79], v232 offset:37376\n"
      "v_mfma_f32_16x16x32_bf16 v[188:191], a[48:51], a[228:231], 0\n"
      "ds_read_b64_tr_b16 a[124:125], v234 offset:36864\n"
      "ds_read_b64_tr_b16 a[6:7], v232 offset:25088\n"
      "ds_read_b64_tr_b16 a[52:53], v234 offset:24576\n"
      "ds_read_b64_tr_b16 a[100:101], v233 offset:36864\n"
      "ds_read_b64_tr_b16 a[126:127], v234 offset:37376\n"
      "ds_read_b64_tr_b16 a[54:55], v234 offset:25088\n"
      "global_load_dwordx4 v[12:15], v219, s[88:89] offset:1024\n"
      "ds_read2_b32 v[64:65], v44 offset0:0 offset1:1\n"
      "v_mfma_f32_16x16x32_bf16 v[96:99], a[48:51], a[224:227], 0\n"
      "ds_read2_b32 v[54:55], v225 offset0:2 offset1:3\n"
      "v_readlane_b32 s86, v38, s43\n"
      "v_mfma_f32_16x16x32_bf16 v[106:109], a[72:75], a[228:231], 0\n"
      "ds_read2_b32 v[58:59], v225 offset0:18 offset1:19\n"
      "ds_read2_b32 v[66:67], v44 offset0:2 offset1:3\n"
      "v_mfma_f32_16x16x32_bf16 v[244:247], a[24:27], a[228:231], 0\n"
      "s_mov_b32 s91, 0x1ffff\n"
      "global_load_dwordx4 v[20:23], v219, s[88:89] offset:2048\n"
      "v_mfma_f32_16x16x32_bf16 v[130:133], a[48:51], a[216:219], 0\n"
      "ds_read2_b32 v[70:71], v44 offset0:18 offset1:19\n"
      "v_mfma_f32_16x16x32_bf16 v[226:229], a[24:27], a[216:219], 0\n"
      "ds_read2_b32 v[40:41], v139 offset0:0 offset1:1\n"
      "v_mov_b32 v221, 0xf149f2ca\n"
      "v_mfma_f32_16x16x32_bf16 v[252:255], a[24:27], a[224:227], 0\n"
      "s_waitcnt lgkmcnt(5)\n"
      "v_lshl_add_u32 v73, v73, 2, s76\n"
      "v_lshl_add_u32 v49, v49, 2, s76\n"
      "v_mfma_f32_16x16x32_bf16 v[118:121], a[48:51], a[220:223], 0\n"
      "global_load_dwordx4 v[24:27], v219, s[88:89] offset:3072\n"
      "v_mfma_f32_16x16x32_bf16 v[216:219], a[96:99], a[224:227], 0\n"
      "ds_read_b32 v134, v136 offset:3328\n"
      "s_add_u32 m0, s75, 73728\n"
      "ds_read_b128 a[236:239], v1\n"
      "s_mov_b32 s78, 147456\n"
      "v_mfma_f32_16x16x32_bf16 v[216:219], a[100:103], a[240:243], v[216:219]\n"
      "ds_read2_b32 v[62:63], v225 offset0:34 offset1:35\n"
      "v_lshl_add_u32 v138, v138, 2, s76\n"
      "v_mfma_f32_16x16x32_bf16 v[202:205], a[0:3], a[224:227], 0\n"
      "buffer_load_dwordx4 v187, s[20:23], s79 offen  lds\n"
      "v_mfma_f32_16x16x32_bf16 v[180:183], a[72:75], a[224:227], 0\n"
      "ds_read_b128 a[252:255], v0 offset:6144\n"
      "s_mov_b32 s90, 0xffffffff\n"
      "v_lshl_add_u32 v65, v65, 2, s76\n"
      "v_lshl_add_u32 v64, v64, 2, s76\n"
      "v_mfma_f32_16x16x32_bf16 v[180:183], a[76:79], a[240:243], v[180:183]\n"
      "s_add_u32 m0, s75, 77696\n"
      "ds_read2_b32 v[52:53], v225 offset0:0 offset1:1\n"
      "v_lshl_add_u32 v100, v100, 2, s76\n"
      "v_mfma_f32_16x16x32_bf16 v[160:163], a[72:75], a[216:219], 0\n"
      "buffer_load_dwordx4 v187, s[20:23], s79 offen offset:128  lds\n"
      "v_mfma_f32_16x16x32_bf16 v[130:133], a[52:55], a[232:235], v[130:133]\n"
      "ds_read2_b32 v[60:61], v225 offset0:32 offset1:33\n"
      "v_lshl_add_u32 v68, v68, 2, s76\n"
      "v_mfma_f32_16x16x32_bf16 v[166:169], a[0:3], a[216:219], 0\n"
      "ds_read_b32 v68, v68\n"
      "s_waitcnt lgkmcnt(4)\n"
      "v_lshl_add_u32 v40, v40, 2, s76\n"
      "s_add_u32 m0, s75, 81664\n"
      "v_lshl_add_u32 v59, v59, 2, s76\n"
      "v_mfma_f32_16x16x32_bf16 v[144:147], a[72:75], a[220:223], 0\n"
      "buffer_load_dwordx4 v187, s[20:23], s79 offen offset:256  lds\n"
      "v_mfma_f32_16x16x32_bf16 v[144:147], a[76:79], a[236:239], v[144:147]\n"
      "ds_read_b128 a[248:251], v200 offset:4096\n"
      "v_lshl_add_u32 v48, v48, 2, s76\n"
      "v_mfma_f32_16x16x32_bf16 v[226:229], a[28:31], a[232:235], v[226:229]\n"
      "ds_read_b128 a[216:219], v0 offset:8192\n"
      "v_lshl_add_u32 v47, v47, 2, s76\n"
      "v_mfma_f32_16x16x32_bf16 v[196:199], a[96:99], a[220:223], 0\n"
      "ds_read2_b32 v[56:57], v225 offset0:16 offset1:17\n"
      "s_add_u32 m0, s75, 85632\n"
      "v_mfma_f32_16x16x32_bf16 v[188:191], a[52:55], a[244:247], v[188:191]\n"
      "buffer_load_dwordx4 v187, s[20:23], s79 offen offset:384  lds\n"
      "v_mfma_f32_16x16x32_bf16 v[166:169], a[4:7], a[232:235], v[166:169]\n"
      "s_add_u32 m0, s75, 89600\n"
      "v_lshl_add_u32 v51, v51, 2, s76\n"
      "v_lshl_add_u32 v58, v58, 2, s76\n"
      "v_mfma_f32_16x16x32_bf16 v[222:225], a[24:27], a[220:223], 0\n"
      "s_waitcnt vmcnt(9)\n"
      "s_barrier\n"
      "ds_read_b64_tr_b16 a[34:35], v143 offset:512\n"
      "ds_read_b64_tr_b16 a[56:57], v142\n"
      "v_mfma_f32_16x16x32_bf16 v[222:225], a[28:31], a[236:239], v[222:225]\n"
      "buffer_load_dwordx4 v187, s[20:23], s79 offen offset:512  lds\n"
      "v_mfma_f32_16x16x32_bf16 v[118:121], a[52:55], a[236:239], v[118:121]\n"
      "ds_read_b64_tr_b16 a[130:131], v142 offset:12800\n"
      "ds_read_b64_tr_b16 a[8:9], v140\n"
      "ds_read_b64_tr_b16 a[80:81], v140 offset:12288\n"
      "ds_read_b64_tr_b16 a[10:11], v140 offset:512\n"
      "v_mfma_f32_16x16x32_bf16 v[112:115], a[96:99], a[228:231], 0\n"
      "s_add_u32 m0, s75, 93568\n"
      "ds_read_b64_tr_b16 a[106:107], v143 offset:12800\n"
      "buffer_load_dwordx4 v187, s[20:23], s79 offen offset:640  lds\n"
      "ds_read_b64_tr_b16 a[32:33], v143\n"
      "ds_read_b64_tr_b16 a[82:83], v140 offset:12800\n"
      "ds_read_b64_tr_b16 a[104:105], v143 offset:12288\n"
      "v_mfma_f32_16x16x32_bf16 v[112:115], a[100:103], a[244:247], v[112:115]\n"
      "ds_read_b64_tr_b16 a[128:129], v142 offset:12288\n"
      "ds_read_b64_tr_b16 a[58:59], v142 offset:512\n"
      "s_add_u32 m0, s75, 0\n"
      "v_mfma_f32_16x16x32_bf16 v[212:215], a[124:127], a[244:247], v[212:215]\n"
      "buffer_load_dwordx4 v187, s[20:23], s78 offen  lds\n"
      "v_mfma_f32_16x16x32_bf16 v[208:211], a[4:7], a[244:247], v[208:211]\n"
      "ds_read2_b32 v[74:75], v44 offset0:34 offset1:35\n"
      "s_add_u32 m0, s75, 3968\n"
      "v_mfma_f32_16x16x32_bf16 v[236:239], a[124:127], a[232:235], v[236:239]\n"
      "ds_read_b128 a[220:223], v0 offset:10240\n"
      "ds_read2_b32 v[44:45], v139 offset0:16 offset1:17\n"
      "v_lshl_add_u32 v72, v72, 2, s76\n"
      "v_lshl_add_u32 v66, v66, 2, s76\n"
      "v_mfma_f32_16x16x32_bf16 v[124:127], a[124:127], a[240:243], v[124:127]\n"
      "buffer_load_dwordx4 v187, s[20:23], s78 offen offset:128  lds\n"
      "v_mfma_f32_16x16x32_bf16 v[202:205], a[4:7], a[240:243], v[202:205]\n"
      "s_waitcnt lgkmcnt(3)\n"
      "v_lshl_add_u32 v53, v53, 2, s76\n"
      "s_add_u32 m0, s75, 7936\n"
      "v_mfma_f32_16x16x32_bf16 v[244:247], a[28:31], a[244:247], v[244:247]\n"
      "ds_read_b32 v51, v51\n"
      "v_readlane_b32 s84, v37, s43\n"
      "v_mfma_f32_16x16x32_bf16 v[240:243], a[100:103], a[232:235], v[240:243]\n"
      "ds_read_b32 v139, v136 offset:6464\n"
      "v_lshl_add_u32 v43, v43, 2, s76\n"
      "v_mfma_f32_16x16x32_bf16 v[240:243], a[104:107], a[248:251], v[240:243]\n"
      "buffer_load_dwordx4 v187, s[20:23], s78 offen offset:256  lds\n"
      "v_mfma_f32_16x16x32_bf16 v[252:255], a[28:31], a[240:243], v[252:255]\n"
      "s_add_u32 m0, s75, 11904\n"
      "ds_read_b128 a[224:227], v116 offset:4096\n"
      "v_mfma_f32_16x16x32_bf16 v[252:255], a[32:35], a[216:219], v[252:255]\n"
      "ds_read_b32 v47, v47\n"
      "v_lshl_add_u32 v50, v50, 2, s76\n"
      "v_mfma_f32_16x16x32_bf16 v[174:177], a[4:7], a[236:239], v[174:177]\n"
      "ds_read_b128 a[228:231], v1 offset:6144\n"
      "v_lshl_add_u32 v63, v63, 2, s76\n"
      "v_mfma_f32_16x16x32_bf16 v[96:99], a[52:55], a[240:243], v[96:99]\n"
      "buffer_load_dwordx4 v187, s[20:23], s78 offen offset:384  lds\n"
      "v_mfma_f32_16x16x32_bf16 v[96:99], a[56:59], a[216:219], v[96:99]\n"
      "ds_read_b32 v66, v66\n"
      "s_waitcnt lgkmcnt(4)\n"
      "v_mfma_f32_16x16x32_bf16 v[112:115], a[104:107], a[220:223], v[112:115]\n"
      "ds_read_b32 v65, v65\n"
      "v_lshl_add_u32 v70, v70, 2, s76\n"
      "v_mfma_f32_16x16x32_bf16 v[192:195], a[124:127], a[236:239], v[192:195]\n"
      "s_add_u32 m0, s75, 15872\n"
      "ds_read_b32 v49, v49\n"
      "v_mfma_f32_16x16x32_bf16 v[192:195], a[128:131], a[252:255], v[192:195]\n"
      "buffer_load_dwordx4 v187, s[20:23], s78 offen offset:512  lds\n"
      "v_mfma_f32_16x16x32_bf16 v[166:169], a[8:11], a[248:251], v[166:169]\n"
      "ds_read_b32 v138, v138\n"
      "s_add_u32 m0, s75, 19840\n"
      "v_lshl_add_u32 v67, v67, 2, s76\n"
      "v_lshl_add_u32 v41, v41, 2, s76\n"
      "v_mfma_f32_16x16x32_bf16 v[180:183], a[80:83], a[216:219], v[180:183]\n"
      "v_lshl_add_u32 v46, v46, 2, s76\n"
      "v_lshl_add_u32 v61, v61, 2, s76\n"
      "v_lshl_add_u32 v52, v52, 2, s76\n"
      "v_mfma_f32_16x16x32_bf16 v[160:163], a[76:79], a[232:235], v[160:163]\n"
      "buffer_load_dwordx4 v187, s[20:23], s78 offen offset:640  lds\n"
      "v_mfma_f32_16x16x32_bf16 v[202:205], a[8:11], a[216:219], v[202:205]\n"
      "s_mov_b32 s79, 184320\n"
      "v_lshl_add_u32 v42, v42, 2, s76\n"
      "v_lshl_add_u32 v57, v57, 2, s76\n"
      "v_mfma_f32_16x16x32_bf16 v[236:239], a[128:131], a[248:251], v[236:239]\n"
      "ds_read_b128 a[232:235], v1 offset:8192\n"
      "v_lshl_add_u32 v69, v69, 2, s76\n"
      "v_mfma_f32_16x16x32_bf16 v[208:211], a[8:11], a[220:223], v[208:211]\n"
      "ds_read_b32 v52, v52\n"
      "s_add_u32 m0, s75, 24576\n"
      "v_mfma_f32_16x16x32_bf16 v[196:199], a[100:103], a[236:239], v[196:199]\n"
      "buffer_load_dwordx4 v187, s[20:23], s79 offen  lds\n"
      "v_mfma_f32_16x16x32_bf16 v[118:121], a[56:59], a[252:255], v[118:121]\n"
      "s_add_u32 m0, s75, 28544\n"
      "s_waitcnt vmcnt(7)\n"
      "s_barrier\n"
      "v_mfma_f32_16x16x32_bf16 v[124:127], a[128:131], a[216:219], v[124:127]\n"
      "ds_read_b64_tr_b16 a[134:135], v142 offset:37376\n"
      "ds_read_b64_tr_b16 a[14:15], v140 offset:25088\n"
      "v_mfma_f32_16x16x32_bf16 v[212:215], a[128:131], a[220:223], v[212:215]\n"
      "ds_read_b64_tr_b16 a[12:13], v140 offset:24576\n"
      "ds_read_b64_tr_b16 a[36:37], v143 offset:24576\n"
      "v_mfma_f32_16x16x32_bf16 v[196:199], a[104:107], a[252:255], v[196:199]\n"
      "buffer_load_dwordx4 v187, s[20:23], s79 offen offset:128  lds\n"
      "ds_read_b64_tr_b16 a[84:85], v140 offset:36864\n"
      "ds_read_b64_tr_b16 a[108:109], v143 offset:36864\n"
      "v_mfma_f32_16x16x32_bf16 v[226:229], a[32:35], a[248:251], v[226:229]\n"
      "ds_read_b64_tr_b16 a[132:133], v142 offset:36864\n"
      "ds_read_b64_tr_b16 a[38:39], v143 offset:25088\n"
      "v_mfma_f32_16x16x32_bf16 v[144:147], a[80:83], a[252:255], v[144:147]\n"
      "ds_read_b64_tr_b16 a[110:111], v143 offset:37376\n"
      "ds_read_b64_tr_b16 a[86:87], v140 offset:37376\n"
      "ds_read_b64_tr_b16 a[62:63], v142 offset:25088\n"
      "ds_read_b64_tr_b16 a[60:61], v142 offset:24576\n"
      "s_add_u32 m0, s75, 32512\n"
      "v_mfma_f32_16x16x32_bf16 v[106:109], a[76:79], a[244:247], v[106:109]\n"
      "buffer_load_dwordx4 v187, s[20:23], s79 offen offset:256  lds\n"
      "v_mfma_f32_16x16x32_bf16 v[106:109], a[80:83], a[220:223], v[106:109]\n"
      "ds_read_b32 v53, v53\n"
      "v_lshl_add_u32 v44, v44, 2, s76\n"
      "v_mfma_f32_16x16x32_bf16 v[174:177], a[8:11], a[252:255], v[174:177]\n"
      "s_add_u32 m0, s75, 36480\n"
      "ds_read_b128 a[236:239], v1 offset:10240\n"
      "v_mfma_f32_16x16x32_bf16 v[216:219], a[104:107], a[216:219], v[216:219]\n"
      "ds_read_b32 v64, v64\n"
      "s_waitcnt lgkmcnt(6)\n"
      "v_mfma_f32_16x16x32_bf16 v[216:219], a[108:111], a[232:235], v[216:219]\n"
      "buffer_load_dwordx4 v187, s[20:23], s79 offen offset:384  lds\n"
      "v_mfma_f32_16x16x32_bf16 v[222:225], a[32:35], a[252:255], v[222:225]\n"
      "ds_read_b32 v61, v61\n"
      "s_mov_b32 s82, 0xffff\n"
      "v_mfma_f32_16x16x32_bf16 v[222:225], a[36:39], a[228:231], v[222:225]\n"
      "ds_read_b32 v40, v40\n"
      "s_mov_b32 s78, 0\n"
      "v_mfma_f32_16x16x32_bf16 v[188:191], a[56:59], a[220:223], v[188:191]\n"
      "s_add_u32 m0, s75, 40448\n"
      "v_cndmask_b32 v30, v221, v138, s[90:91]\n"
      "v_lshl_add_u32 v74, v74, 2, s76\n"
      "v_mfma_f32_16x16x32_bf16 v[240:243], a[108:111], a[224:227], v[240:243]\n"
      "buffer_load_dwordx4 v187, s[20:23], s79 offen offset:512  lds\n"
      "v_mfma_f32_16x16x32_bf16 v[130:133], a[56:59], a[248:251], v[130:133]\n"
      "s_waitcnt lgkmcnt(2)\n"
      "v_lshl_add_u32 v71, v71, 2, s76\n"
      "v_lshl_add_u32 v56, v56, 2, s76\n"
      "v_mfma_f32_16x16x32_bf16 v[118:121], a[60:63], a[228:231], v[118:121]\n"
      "ds_read_b32 v67, v67\n"
      "s_add_u32 m0, s75, 44416\n"
      "v_mfma_f32_16x16x32_bf16 v[192:195], a[132:135], a[228:231], v[192:195]\n"
      "ds_read_b32 v57, v57\n"
      "v_readlane_b32 s85, v37, s42\n"
      "v_mfma_f32_16x16x32_bf16 v[124:127], a[132:135], a[232:235], v[124:127]\n"
      "buffer_load_dwordx4 v187, s[20:23], s79 offen offset:640  lds\n"
      "v_mfma_f32_16x16x32_bf16 v[226:229], a[36:39], a[224:227], v[226:229]\n"
      "ds_read_b128 a[244:247], v0 offset:12288\n"
      "v_lshl_add_u32 v45, v45, 2, s76\n"
      "v_mfma_f32_16x16x32_bf16 v[236:239], a[132:135], a[224:227], v[236:239]\n"
      "ds_read_b32 v71, v71\n"
      "v_lshl_add_u32 v134, v134, 2, s76\n"
      "s_add_u32 m0, s75, 48384\n"
      "v_mfma_f32_16x16x32_bf16 v[244:247], a[32:35], a[220:223], v[244:247]\n"
      "ds_read_b128 a[252:255], v0 offset:16384\n"
      "buffer_load_dwordx4 v187, s[20:23], s78 offen offset:768  lds\n"
      "v_mfma_f32_16x16x32_bf16 v[244:247], a[36:39], a[236:239], v[244:247]\n"
      "ds_read_b128 a[240:243], v200 offset:8192\n"
      "v_lshl_add_u32 v55, v55, 2, s76\n"
      "v_mfma_f32_16x16x32_bf16 v[160:163], a[80:83], a[248:251], v[160:163]\n"
      "ds_read_b32 v48, v48\n"
      "s_add_u32 m0, s75, 52352\n"
      "v_mfma_f32_16x16x32_bf16 v[160:163], a[84:87], a[224:227], v[160:163]\n"
      "ds_read_b32 v55, v55\n"
      "v_lshl_add_u32 v54, v54, 2, s76\n"
      "v_mfma_f32_16x16x32_bf16 v[174:177], a[12:15], a[228:231], v[174:177]\n"
      "buffer_load_dwordx4 v187, s[20:23], s78 offen offset:896  lds\n"
      "v_mfma_f32_16x16x32_bf16 v[180:183], a[84:87], a[232:235], v[180:183]\n"
      "ds_read_b128 a[216:219], v116 offset:8192\n"
      "v_lshl_add_u32 v139, v139, 2, s76\n"
      "v_mfma_f32_16x16x32_bf16 v[96:99], a[60:63], a[232:235], v[96:99]\n"
      "s_or_b32 s95, s84, s85\n"
      "ds_read_b128 a[220:223], v1 offset:12288\n"
      "v_mfma_f32_16x16x32_bf16 v[188:191], a[60:63], a[236:239], v[188:191]\n"
      "ds_read_b128 a[248:251], v0 offset:14336\n"
      "s_add_u32 m0, s75, 56320\n"
      "v_mfma_f32_16x16x32_bf16 v[212:215], a[132:135], a[236:239], v[212:215]\n"
      "buffer_load_dwordx4 v187, s[20:23], s78 offen offset:1024  lds\n"
      "v_mfma_f32_16x16x32_bf16 v[252:255], a[36:39], a[232:235], v[252:255]\n"
      "s_waitcnt vmcnt(9)\n"
      "s_barrier\n"
      "ds_read_b64_tr_b16 a[64:65], v234\n"
      "v_mfma_f32_16x16x32_bf16 v[202:205], a[12:15], a[232:235], v[202:205]\n"
      "ds_read_b64_tr_b16 a[114:115], v233 offset:12800\n"
      "ds_read_b64_tr_b16 a[90:91], v232 offset:12800\n"
      "ds_read_b64_tr_b16 a[40:41], v233\n"
      "v_mfma_f32_16x16x32_bf16 v[196:199], a[108:111], a[228:231], v[196:199]\n"
      "ds_read_b64_tr_b16 a[88:89], v232 offset:12288\n"
      "ds_read_b64_tr_b16 a[66:67], v234 offset:512\n"
      "ds_read_b64_tr_b16 a[138:139], v234 offset:12800\n"
      "ds_read_b64_tr_b16 a[18:19], v232 offset:512\n"
      "ds_read_b64_tr_b16 a[16:17], v232\n"
      "v_mfma_f32_16x16x32_bf16 v[166:169], a[12:15], a[224:227], v[166:169]\n"
      "ds_read_b64_tr_b16 a[42:43], v233 offset:512\n"
      "ds_read_b64_tr_b16 a[136:137], v234 offset:12288\n"
      "ds_read_b64_tr_b16 a[112:113], v233 offset:12288\n"
      "ds_read_b128 a[232:235], v200 offset:2048\n"
      "s_add_u32 m0, s75, 60672\n"
      "v_mfma_f32_16x16x32_bf16 v[144:147], a[84:87], a[228:231], v[144:147]\n"
      "s_mov_b32 s78, 36864\n"
      "ds_read_b32 v134, v134\n"
      "v_mfma_f32_16x16x32_bf16 v[130:133], a[60:63], a[224:227], v[130:133]\n"
      "buffer_load_dwordx4 v187, s[20:23], s78 offen offset:768  lds\n"
      "s_mov_b32 s79, 73728\n"
      "v_mfma_f32_16x16x32_bf16 v[112:115], a[108:111], a[236:239], v[112:115]\n"
      "ds_read_b32 v139, v139\n"
      "ds_read_b32 v72, v72\n"
      "v_mfma_f32_16x16x32_bf16 v[106:109], a[84:87], a[236:239], v[106:109]\n"
      "ds_read_b128 a[228:231], v1 offset:16384\n"
      "s_add_u32 m0, s75, 64640\n"
      "s_waitcnt lgkmcnt(7)\n"
      "v_mfma_f32_16x16x32_bf16 v[252:255], a[40:43], a[248:251], v[252:255]\n"
      "buffer_load_dwordx4 v187, s[20:23], s78 offen offset:896  lds\n"
      "v_mfma_f32_16x16x32_bf16 v[166:169], a[16:19], a[240:243], v[166:169]\n"
      "ds_read_b32 v70, v70\n"
      "s_add_u32 m0, s75, 68608\n"
      "v_mfma_f32_16x16x32_bf16 v[244:247], a[40:43], a[252:255], v[244:247]\n"
      "ds_read_b32 v69, v69\n"
      "ds_read_b32 v58, v58\n"
      "v_mfma_f32_16x16x32_bf16 v[96:99], a[64:67], a[248:251], v[96:99]\n"
      "ds_read_b32 v43, v43\n"
      "buffer_load_dwordx4 v187, s[20:23], s78 offen offset:1024  lds\n"
      "v_mfma_f32_16x16x32_bf16 v[208:211], a[12:15], a[236:239], v[208:211]\n"
      "s_waitcnt lgkmcnt(4)\n"
      "v_lshl_add_u32 v75, v75, 2, s76\n"
      "v_cndmask_b32 v28, v221, v139, s[82:83]\n"
      "v_mfma_f32_16x16x32_bf16 v[208:211], a[16:19], a[252:255], v[208:211]\n"
      "ds_read_b32 v54, v54\n"
      "s_add_u32 m0, s75, 72960\n"
      "ds_read_b32 v44, v44\n"
      "v_mfma_f32_16x16x32_bf16 v[196:199], a[112:115], a[244:247], v[196:199]\n"
      "ds_read_b32 v74, v74\n"
      "v_lshl_add_u32 v62, v62, 2, s76\n"
      "v_mfma_f32_16x16x32_bf16 v[216:219], a[112:115], a[248:251], v[216:219]\n"
      "buffer_load_dwordx4 v187, s[20:23], s79 offen offset:768  lds\n"
      "v_mfma_f32_16x16x32_bf16 v[144:147], a[88:91], a[244:247], v[144:147]\n"
      "ds_read_b32 v42, v42\n"
      "s_add_u32 m0, s75, 76928\n"
      "v_mfma_f32_16x16x32_bf16 v[136:139], a[48:51], a[232:235], 0\n"
      "ds_read_b32 v63, v63\n"
      "s_or_b32 s95, s95, s48\n"
      "v_mfma_f32_16x16x32_bf16 v[240:243], a[112:115], a[240:243], v[240:243]\n"
      "ds_read_b32 v62, v62\n"
      "ds_read_b128 a[224:227], v1 offset:14336\n"
      "v_mfma_f32_16x16x32_bf16 v[174:177], a[16:19], a[244:247], v[174:177]\n"
      "buffer_load_dwordx4 v187, s[20:23], s79 offen offset:896  lds\n"
      "v_mfma_f32_16x16x32_bf16 v[124:127], a[136:139], a[248:251], v[124:127]\n"
      "ds_read_b32 v73, v73\n"
      "s_add_u32 m0, s75, 80896\n"
      "v_mfma_f32_16x16x32_bf16 v[222:225], a[40:43], a[244:247], v[222:225]\n"
      "ds_read_b32 v100, v100\n"
      "v_lshl_add_u32 v60, v60, 2, s76\n"
      "v_mfma_f32_16x16x32_bf16 v[192:195], a[136:139], a[244:247], v[192:195]\n"
      "ds_read_b128 a[236:239], v116 offset:2048\n"
      "ds_read_b32 v46, v46\n"
      "v_mfma_f32_16x16x32_bf16 v[106:109], a[88:91], a[252:255], v[106:109]\n"
      "buffer_load_dwordx4 v187, s[20:23], s79 offen offset:1024  lds\n"
      "v_mfma_f32_16x16x32_bf16 v[236:239], a[136:139], a[240:243], v[236:239]\n"
      "s_add_u32 m0, s75, 85248\n"
      "ds_read_b32 v45, v45\n"
      "v_mfma_f32_16x16x32_bf16 v[118:121], a[64:67], a[244:247], v[118:121]\n"
      "ds_read_b32 v41, v41\n"
      "s_mov_b32 s79, 110592\n"
      "v_mfma_f32_16x16x32_bf16 v[160:163], a[88:91], a[240:243], v[160:163]\n"
      "s_waitcnt vmcnt(9)\n"
      "s_barrier\n"
      "ds_read_b64_tr_b16 a[70:71], v234 offset:25088\n"
      "v_mfma_f32_16x16x32_bf16 v[212:215], a[136:139], a[252:255], v[212:215]\n"
      "ds_read_b64_tr_b16 a[20:21], v232 offset:24576\n"
      "ds_read_b64_tr_b16 a[94:95], v232 offset:37376\n"
      "v_mfma_f32_16x16x32_bf16 v[112:115], a[112:115], a[252:255], v[112:115]\n"
      "ds_read_b64_tr_b16 a[92:93], v232 offset:36864\n"
      "ds_read_b64_tr_b16 a[22:23], v232 offset:25088\n"
      "v_mfma_f32_16x16x32_bf16 v[188:191], a[64:67], a[252:255], v[188:191]\n"
      "ds_read_b64_tr_b16 a[46:47], v233 offset:25088\n"
      "ds_read_b64_tr_b16 a[44:45], v233 offset:24576\n"
      "v_mfma_f32_16x16x32_bf16 v[226:229], a[40:43], a[240:243], v[226:229]\n"
      "buffer_load_dwordx4 v187, s[20:23], s79 offen offset:768  lds\n"
      "ds_read_b64_tr_b16 a[116:117], v233 offset:36864\n"
      "v_mfma_f32_16x16x32_bf16 v[202:205], a[16:19], a[248:251], v[202:205]\n"
      "ds_read_b64_tr_b16 a[142:143], v234 offset:37376\n"
      "ds_read_b64_tr_b16 a[68:69], v234 offset:24576\n"
      "v_mfma_f32_16x16x32_bf16 v[180:183], a[88:91], a[248:251], v[180:183]\n"
      "ds_read_b64_tr_b16 a[118:119], v233 offset:37376\n"
      "s_waitcnt lgkmcnt(7)\n"
      "v_mfma_f32_16x16x32_bf16 v[106:109], a[92:95], a[228:231], v[106:109]\n"
      "ds_read_b32 v60, v60\n"
      "s_add_u32 m0, s75, 89216\n"
      "v_mfma_f32_16x16x32_bf16 v[144:147], a[92:95], a[220:223], v[144:147]\n"
      "ds_read_b64_tr_b16 a[140:141], v234 offset:36864\n"
      "s_waitcnt lgkmcnt(6)\n"
      "v_mfma_f32_16x16x32_bf16 v[208:211], a[20:23], a[228:231], v[208:211]\n"
      "buffer_load_dwordx4 v187, s[20:23], s79 offen offset:896  lds\n"
      "v_mfma_f32_16x16x32_bf16 v[174:177], a[20:23], a[220:223], v[174:177]\n"
      "ds_read_b32 v59, v59\n"
      "v_fma_f32 v220, v106, v106, s51\n"
      "v_mfma_f32_16x16x32_bf16 v[166:169], a[20:23], a[216:219], v[166:169]\n"
      "ds_read_b128 a[252:255], v116 offset:10240\n"
      "s_add_u32 m0, s75, 93184\n"
      "v_mfma_f32_16x16x32_bf16 v[202:205], a[20:23], a[224:227], v[202:205]\n"
      "ds_read_b128 a[244:247], v116 offset:6144\n"
      "v_fma_f32 v230, v144, v144, s51\n"
      "v_mfma_f32_16x16x32_bf16 v[222:225], a[44:47], a[220:223], v[222:225]\n"
      "buffer_load_dwordx4 v187, s[20:23], s79 offen offset:1024  lds\n"
      "v_mfma_f32_16x16x32_bf16 v[160:163], a[92:95], a[216:219], v[160:163]\n"
      "s_add_u32 m0, s75, -768\n"
      "ds_read_b32 v50, v50\n"
      "v_mfma_f32_16x16x32_bf16 v[180:183], a[92:95], a[224:227], v[180:183]\n"
      "s_waitcnt lgkmcnt(4)\n"
      "v_mul_f32 v157, v167, v167\n"
      "v_fma_f32 v122, v208, v208, s51\n"
      "v_mfma_f32_16x16x32_bf16 v[130:133], a[64:67], a[240:243], v[130:133]\n"
      "s_mov_b32 s78, 147456\n"
      "ds_read_b32 v56, v56\n"
      "v_mfma_f32_16x16x32_bf16 v[240:243], a[116:119], a[216:219], v[240:243]\n"
      "buffer_load_dwordx4 v187, s[20:23], s78 offen offset:768  lds\n"
      "v_mul_f32_e64 v149, v161, v161\n"
      "v_mul_f32_e64 v231, v145, v145\n"
      "v_pk_fma_f32 v[230:231], v[146:147], v[146:147], v[230:231]\n"
      "v_mfma_f32_16x16x32_bf16 v[102:105], a[24:27], a[232:235], 0\n"
      "ds_read_b128 a[240:243], v200 offset:6144\n"
      "v_fma_f32 v148, v160, v160, s51\n"
      "v_mfma_f32_16x16x32_bf16 v[216:219], a[116:119], a[224:227], v[216:219]\n"
      "s_add_u32 m0, s75, 3200\n"
      "v_mul_f32_e64 v101, v181, v181\n"
      "v_fma_f32 v150, v174, v174, s51\n"
      "v_mfma_f32_16x16x32_bf16 v[192:195], a[140:143], a[220:223], v[192:195]\n"
      "buffer_load_dwordx4 v187, s[20:23], s78 offen offset:896  lds\n"
      "v_mfma_f32_16x16x32_bf16 v[196:199], a[116:119], a[220:223], v[196:199]\n"
      "ds_read_b128 a[248:251], v200 offset:10240\n"
      "v_mul_f32_e64 v151, v175, v175\n"
      "v_mfma_f32_16x16x32_bf16 v[226:229], a[44:47], a[216:219], v[226:229]\n"
      "ds_read_b32 v75, v75\n"
      "v_cndmask_b32 v26, v221, v134, s[82:83]\n"
      "v_mfma_f32_16x16x32_bf16 v[118:121], a[68:71], a[220:223], v[118:121]\n"
      "s_add_u32 m0, s75, 7168\n"
      "v_mul_f32_e64 v123, v209, v209\n"
      "v_fma_f32 v134, v202, v202, s51\n"
      "v_mfma_f32_16x16x32_bf16 v[212:215], a[140:143], a[228:231], v[212:215]\n"
      "buffer_load_dwordx4 v187, s[20:23], s78 offen offset:1024  lds\n"
      "v_fma_f32 v156, v166, v166, s51\n"
      "v_mul_f32_e64 v135, v203, v203\n"
      "v_pk_fma_f32 v[148:149], v[162:163], v[162:163], v[148:149]\n"
      "v_pk_fma_f32 v[150:151], v[176:177], v[176:177], v[150:151]\n"
      "v_cndmask_b32 v24, v221, v100, s[82:83]\n"
      "v_pk_fma_f32 v[150:151], v[222:223], v[222:223], v[150:151]\n"
      "v_mfma_f32_16x16x32_bf16 v[102:105], a[28:31], a[236:239], v[102:105]\n"
      "s_add_u32 m0, s75, 11520\n"
      "v_mul_f32 v221, v107, v107\n"
      "s_waitcnt lgkmcnt(1)\n"
      "v_mfma_f32_16x16x32_bf16 v[102:105], a[32:35], a[240:243], v[102:105]\n"
      "s_mov_b32 s78, 184320\n"
      "buffer_load_dwordx4 v187, s[20:23], s78 offen offset:768  lds\n"
      "v_fma_f32 v100, v180, v180, s51\n"
      "v_pk_fma_f32 v[220:221], v[108:109], v[108:109], v[220:221]\n"
      "v_pk_fma_f32 v[148:149], v[240:241], v[240:241], v[148:149]\n"
      "v_pk_fma_f32 v[150:151], v[224:225], v[224:225], v[150:151]\n"
      "v_pk_fma_f32 v[134:135], v[204:205], v[204:205], v[134:135]\n"
      "v_pk_fma_f32 v[100:101], v[182:183], v[182:183], v[100:101]\n"
      "v_pk_fma_f32 v[150:151], v[118:119], v[118:119], v[150:151]\n"
      "v_pk_fma_f32 v[230:231], v[196:197], v[196:197], v[230:231]\n"
      "v_pk_fma_f32 v[156:157], v[168:169], v[168:169], v[156:157]\n"
      "v_pk_fma_f32 v[122:123], v[210:211], v[210:211], v[122:123]\n"
      "v_pk_fma_f32 v[148:149], v[242:243], v[242:243], v[148:149]\n"
      "s_add_u32 m0, s75, 15488\n"
      "v_pk_fma_f32 v[100:101], v[216:217], v[216:217], v[100:101]\n"
      "v_mfma_f32_16x16x32_bf16 v[102:105], a[36:39], a[244:247], v[102:105]\n"
      "buffer_load_dwordx4 v187, s[20:23], s78 offen offset:896  lds\n"
      "v_mfma_f32_16x16x32_bf16 v[136:139], a[52:55], a[236:239], v[136:139]\n"
      "s_waitcnt vmcnt(11)\n"
      "s_barrier\n"
      "ds_read_b64_tr_b16 a[194:195], v142 offset:512\n"
      "v_mfma_f32_16x16x32_bf16 v[124:127], a[140:143], a[224:227], v[124:127]\n"
      "ds_read_b64_tr_b16 a[192:193], v142\n"
      "ds_read_b64_tr_b16 a[150:151], v140 offset:12800\n"
      "v_mfma_f32_16x16x32_bf16 v[236:239], a[140:143], a[216:219], v[236:239]\n"
      "s_add_u32 m0, s75, 19456\n"
      "ds_read_b64_tr_b16 a[144:145], v140\n"
      "v_mfma_f32_16x16x32_bf16 v[252:255], a[44:47], a[224:227], v[252:255]\n"
      "ds_read_b64_tr_b16 a[196:197], v142 offset:12288\n"
      "ds_read_b64_tr_b16 a[174:175], v143 offset:12800\n"
      "v_mfma_f32_16x16x32_bf16 v[244:247], a[44:47], a[228:231], v[244:247]\n"
      "buffer_load_dwordx4 v187, s[20:23], s78 offen offset:1024  lds\n"
      "v_mfma_f32_16x16x32_bf16 v[130:133], a[68:71], a[216:219], v[130:133]\n"
      "ds_read_b64_tr_b16 a[198:199], v142 offset:12800\n"
      "ds_read_b64_tr_b16 a[146:147], v140 offset:512\n"
      "ds_read_b64_tr_b16 a[168:169], v143\n"
      "v_pk_fma_f32 v[156:157], v[226:227], v[226:227], v[156:157]\n"
      "v_pk_fma_f32 v[100:101], v[218:219], v[218:219], v[100:101]\n"
      "v_pk_fma_f32 v[134:135], v[252:253], v[252:253], v[134:135]\n"
      "v_pk_fma_f32 v[230:231], v[198:199], v[198:199], v[230:231]\n"
      "s_mov_b32 s79, 0\n"
      "v_mfma_f32_16x16x32_bf16 v[112:115], a[116:119], a[228:231], v[112:115]\n"
      "s_add_u32 m0, s75, 24576\n"
      "ds_read_b64_tr_b16 a[148:149], v140 offset:12288\n"
      "v_mfma_f32_16x16x32_bf16 v[96:99], a[68:71], a[224:227], v[96:99]\n"
      "ds_read_b64_tr_b16 a[172:173], v143 offset:12288\n"
      "ds_read_b64_tr_b16 a[170:171], v143 offset:512\n"
      "buffer_load_dwordx4 v251, s[36:39], s79 offen  lds\n"
      "v_pk_fma_f32 v[148:149], v[236:237], v[236:237], v[148:149]\n"
      "v_pk_fma_f32 v[230:231], v[192:193], v[192:193], v[230:231]\n"
      "v_pk_fma_f32 v[148:149], v[238:239], v[238:239], v[148:149]\n"
      "v_pk_fma_f32 v[156:157], v[228:229], v[228:229], v[156:157]\n"
      "s_add_u32 m0, s75, 28544\n"
      "v_pk_fma_f32 v[134:135], v[254:255], v[254:255], v[134:135]\n"
      "v_pk_fma_f32 v[122:123], v[244:245], v[244:245], v[122:123]\n"
      "v_pk_fma_f32 v[156:157], v[130:131], v[130:131], v[156:157]\n"
      "v_pk_fma_f32 v[100:101], v[124:125], v[124:125], v[100:101]\n"
      "v_pk_fma_f32 v[230:231], v[194:195], v[194:195], v[230:231]\n"
      "v_pk_fma_f32 v[150:151], v[120:121], v[120:121], v[150:151]\n"
      "v_pk_fma_f32 v[134:135], v[96:97], v[96:97], v[134:135]\n"
      "v_add_f32 v148, v148, v149\n"
      "v_pk_fma_f32 v[156:157], v[132:133], v[132:133], v[156:157]\n"
      "v_pk_fma_f32 v[134:135], v[98:99], v[98:99], v[134:135]\n"
      "v_add_f32_e64 v150, v150, v151\n"
      "v_mfma_f32_16x16x32_bf16 v[136:139], a[56:59], a[240:243], v[136:139]\n"
      "buffer_load_dwordx4 v251, s[36:39], s79 offen offset:128  lds\n"
      "s_add_u32 m0, s75, 32512\n"
      "v_add_f32 v230, v230, v231\n"
      "v_add_f32 v156, v156, v157\n"
      "v_pk_fma_f32 v[100:101], v[126:127], v[126:127], v[100:101]\n"
      "v_permlane16_swap_b32 v150, v230\n"
      "v_permlane16_swap_b32 v156, v148\n"
      "v_pk_fma_f32 v[220:221], v[112:113], v[112:113], v[220:221]\n"
      "v_mfma_f32_16x16x32_bf16 v[184:187], a[72:75], a[232:235], 0\n"
      "s_waitcnt lgkmcnt(0)\n"
      "v_add_f32 v150, v150, v230\n"
      "v_add_f32 v156, v156, v148\n"
      "v_mov_b32 v230, v150\n"
      "v_mfma_f32_16x16x32_bf16 v[102:105], a[40:43], a[248:251], v[102:105]\n"
      "buffer_load_dwordx4 v251, s[36:39], s79 offen offset:256  lds\n"
      "v_mov_b32 v148, v156\n"
      "v_add_f32 v100, v100, v101\n"
      "v_permlane32_swap_b32 v150, v230\n"
      "v_permlane32_swap_b32 v156, v148\n"
      "v_add_f32_e64 v156, v156, v148\n"
      "v_pk_fma_f32 v[122:123], v[246:247], v[246:247], v[122:123]\n"
      "v_mfma_f32_16x16x32_bf16 v[188:191], a[68:71], a[228:231], v[188:191]\n"
      "s_mov_b32 s79, 12288\n"
      "v_add_f32 v150, v150, v230\n"
      "v_rsq_f32 v156, v156\n"
      "v_rsq_f32_e64 v150, v150\n"
      "v_mfma_f32_16x16x32_bf16 v[170:173], a[0:3], a[232:235], 0\n"
      "s_add_u32 m0, s75, 36864\n"
      "v_mov_b32 v148, v156\n"
      "v_mov_b32 v230, v150\n"
      "v_mfma_f32_16x16x32_bf16 v[170:173], a[4:7], a[236:239], v[170:173]\n"
      "buffer_load_dwordx4 v251, s[36:39], s79 offen  lds\n"
      "v_permlane16_swap_b32 v150, v230\n"
      "v_permlane16_swap_b32 v156, v148\n"
      "v_add_f32_e64 v134, v134, v135\n"
      "v_pk_mul_f32 v[236:237], v[236:237], v[148:149] op_sel_hi:[1,0]\n"
      "v_pk_mul_f32 v[160:161], v[160:161], v[148:149] op_sel_hi:[1,0]\n"
      "v_pk_mul_f32 v[198:199], v[198:199], v[230:231] op_sel_hi:[1,0]\n"
      "v_mfma_f32_16x16x32_bf16 v[170:173], a[8:11], a[240:243], v[170:173]\n"
      "s_add_u32 m0, s75, 40832\n"
      "v_mul_f32 v156, s50, v156\n"
      "v_mul_f32 v150, s50, v150\n"
      "v_mfma_f32_16x16x32_bf16 v[136:139], a[60:63], a[244:247], v[136:139]\n"
      "buffer_load_dwordx4 v251, s[36:39], s79 offen offset:128  lds\n"
      "v_permlane16_swap_b32_e64 v134, v100\n"
      "v_pk_fma_f32 v[122:123], v[188:189], v[188:189], v[122:123]\n"
      "v_pk_mul_f32 v[132:133], v[132:133], v[156:157] op_sel_hi:[1,0]\n"
      "v_pk_mul_f32 v[168:169], v[168:169], v[156:157] op_sel_hi:[1,0]\n"
      "v_pk_mul_f32 v[228:229], v[228:229], v[156:157] op_sel_hi:[1,0]\n"
      "v_pk_mul_f32 v[162:163], v[162:163], v[148:149] op_sel_hi:[1,0]\n"
      "s_add_u32 m0, s75, 44800\n"
      "v_pk_mul_f32 v[144:145], v[144:145], v[230:231] op_sel_hi:[1,0]\n"
      "v_add_f32_e64 v134, v134, v100\n"
      "v_pk_fma_f32 v[122:123], v[190:191], v[190:191], v[122:123]\n"
      "v_pk_mul_f32 v[166:167], v[166:167], v[156:157] op_sel_hi:[1,0]\n"
      "v_mfma_f32_16x16x32_bf16 v[170:173], a[12:15], a[244:247], v[170:173]\n"
      "buffer_load_dwordx4 v251, s[36:39], s79 offen offset:256  lds\n"
      "v_cvt_pk_bf16_f32 v236, v236, v237\n"
      "v_mov_b32 v100, v134\n"
      "v_pk_fma_f32 v[220:221], v[114:115], v[114:115], v[220:221]\n"
      "v_pk_mul_f32 v[120:121], v[120:121], v[150:151] op_sel_hi:[1,0]\n"
      "v_pk_mul_f32 v[238:239], v[238:239], v[148:149] op_sel_hi:[1,0]\n"
      "v_permlane32_swap_b32 v134, v100\n"
      "v_mfma_f32_16x16x32_bf16 v[170:173], a[16:19], a[248:251], v[170:173]\n"
      "s_add_u32 m0, s75, 49152\n"
      "v_add_f32 v134, v134, v100\n"
      "v_rsq_f32 v134, v134\n"
      "v_cvt_pk_bf16_f32 v237, v238, v239\n"
      "v_cvt_pk_bf16_f32 v166, v166, v167\n"
      "v_mov_b32 v100, v134\n"
      "v_pk_mul_f32 v[130:131], v[130:131], v[156:157] op_sel_hi:[1,0]\n"
      "v_pk_fma_f32 v[220:221], v[212:213], v[212:213], v[220:221]\n"
      "v_permlane16_swap_b32 v134, v100\n"
      "v_mfma_f32_16x16x32_bf16 v[170:173], a[20:23], a[252:255], v[170:173]\n"
      "s_waitcnt vmcnt(12)\n"
      "s_barrier\n"
      "ds_read_b64_tr_b16 a[176:177], v143 offset:24576\n"
      "ds_read_b64_tr_b16 a[154:155], v140 offset:25088\n"
      "s_mov_b32 s78, 24576\n"
      "v_pk_mul_f32 v[242:243], v[242:243], v[148:149] op_sel_hi:[1,0]\n"
      "v_mfma_f32_16x16x32_bf16 v[102:105], a[44:47], a[252:255], v[102:105]\n"
      "ds_read_b64_tr_b16 a[152:153], v140 offset:24576\n"
      "buffer_load_dwordx4 v251, s[36:39], s78 offen  lds\n"
      "v_add_f32_e64 v122, v122, v123\n"
      "v_cvt_pk_bf16_f32 v167, v168, v169\n"
      "v_cvt_pk_bf16_f32 v160, v160, v161\n"
      "v_pk_mul_f32 v[226:227], v[226:227], v[156:157] op_sel_hi:[1,0]\n"
      "v_pk_mul_f32 v[196:197], v[196:197], v[230:231] op_sel_hi:[1,0]\n"
      "v_pk_mul_f32 v[176:177], v[176:177], v[150:151] op_sel_hi:[1,0]\n"
      "v_pk_mul_f32 v[146:147], v[146:147], v[230:231] op_sel_hi:[1,0]\n"
      "v_pk_mul_f32 v[118:119], v[118:119], v[150:151] op_sel_hi:[1,0]\n"
      "v_pk_fma_f32 v[220:221], v[214:215], v[214:215], v[220:221]\n"
      "v_pk_mul_f32 v[174:175], v[174:175], v[150:151] op_sel_hi:[1,0]\n"
      "v_pk_mul_f32 v[224:225], v[224:225], v[150:151] op_sel_hi:[1,0]\n"
      "v_mfma_f32_16x16x32_bf16 v[154:157], a[120:123], a[232:235], 0\n"
      "ds_read_b64_tr_b16 a[180:181], v143 offset:36864\n"
      "v_cvt_pk_bf16_f32 v161, v162, v163\n"
      "v_cvt_pk_bf16_f32 v169, v228, v229\n"
      "v_mfma_f32_16x16x32_bf16 v[154:157], a[124:127], a[236:239], v[154:157]\n"
      "ds_read_b64_tr_b16 a[202:203], v142 offset:25088\n"
      "s_add_u32 m0, s75, 53120\n"
      "v_mfma_f32_16x16x32_bf16 v[154:157], a[128:131], a[240:243], v[154:157]\n"
      "ds_read_b64_tr_b16 a[204:205], v142 offset:36864\n"
      "v_add_f32_e64 v220, v220, v221\n"
      "v_mfma_f32_16x16x32_bf16 v[136:139], a[64:67], a[248:251], v[136:139]\n"
      "buffer_load_dwordx4 v251, s[36:39], s78 offen offset:128  lds\n"
      "ds_read_b64_tr_b16 a[200:201], v142 offset:24576\n"
      "v_mfma_f32_16x16x32_bf16 v[136:139], a[68:71], a[252:255], v[136:139]\n"
      "ds_read_b64_tr_b16 a[178:179], v143 offset:25088\n"
      "v_cvt_pk_bf16_f32 v163, v242, v243\n"
      "v_mul_f32_e64 v141, v171, v171\n"
      "v_mfma_f32_16x16x32_bf16 v[184:187], a[76:79], a[236:239], v[184:187]\n"
      "ds_read_b64_tr_b16 a[158:159], v140 offset:37376\n"
      "v_cvt_pk_bf16_f32 v174, v174, v175\n"
      "v_cvt_pk_bf16_f32 v144, v144, v145\n"
      "v_cvt_pk_bf16_f32 v175, v176, v177\n"
      "s_add_u32 m0, s75, 57088\n"
      "v_pk_mul_f32 v[182:183], v[182:183], v[100:101] op_sel_hi:[1,0]\n"
      "v_mfma_f32_16x16x32_bf16 v[184:187], a[80:83], a[240:243], v[184:187]\n"
      "ds_read_b64_tr_b16 a[156:157], v140 offset:36864\n"
      "v_cvt_pk_bf16_f32 v168, v226, v227\n"
      "v_mfma_f32_16x16x32_bf16 v[184:187], a[84:87], a[244:247], v[184:187]\n"
      "ds_read_b64_tr_b16 a[206:207], v142 offset:37376\n"
      "ds_read_b64_tr_b16 a[182:183], v143 offset:37376\n"
      "v_permlane16_swap_b32_e64 v122, v220\n"
      "v_pk_mul_f32 v[192:193], v[192:193], v[230:231] op_sel_hi:[1,0]\n"
      "v_fma_f32 v140, v170, v170, s51\n"
      "v_pk_mul_f32 v[240:241], v[240:241], v[148:149] op_sel_hi:[1,0]\n"
      "v_cvt_pk_bf16_f32 v118, v118, v119\n"
      "v_cvt_pk_bf16_f32 v130, v130, v131\n"
      "v_pk_mul_f32 v[218:219], v[218:219], v[100:101] op_sel_hi:[1,0]\n"
      "v_pk_fma_f32 v[140:141], v[172:173], v[172:173], v[140:141]\n"
      "v_pk_mul_f32 v[222:223], v[222:223], v[150:151] op_sel_hi:[1,0]\n"
      "v_add_f32_e64 v122, v122, v220\n"
      "v_pk_mul_f32 v[180:181], v[180:181], v[100:101] op_sel_hi:[1,0]\n"
      "v_mov_b32_e64 v220, v122\n"
      "v_mfma_f32_16x16x32_bf16 v[184:187], a[88:91], a[248:251], v[184:187]\n"
      "ds_read_b128 v[226:229], v116\n"
      "v_cvt_pk_bf16_f32 v177, v224, v225\n"
      "v_cvt_pk_bf16_f32 v192, v192, v193\n"
      "v_mfma_f32_16x16x32_bf16 v[184:187], a[92:95], a[252:255], v[184:187]\n"
      "ds_write_b64 v249, v[236:237] offset:24\n"
      "v_mfma_f32_16x16x32_bf16 v[148:151], a[96:99], a[232:235], 0\n"
      "ds_read_b128 v[236:239], v200\n"
      "v_cvt_pk_bf16_f32 v131, v132, v133\n"
      "v_cvt_pk_bf16_f32 v162, v240, v241\n"
      "v_cvt_pk_bf16_f32 v119, v120, v121\n"
      "v_permlane32_swap_b32_e64 v122, v220\n"
      "v_cvt_pk_bf16_f32 v180, v180, v181\n"
      "v_mfma_f32_16x16x32_bf16 v[148:151], a[100:103], a[236:239], v[148:151]\n"
      "buffer_load_dwordx4 v251, s[36:39], s78 offen offset:256  lds\n"
      "v_cvt_pk_bf16_f32 v145, v146, v147\n"
      "v_add_f32_e64 v122, v122, v220\n"
      "v_cvt_pk_bf16_f32 v176, v222, v223\n"
      "v_mfma_f32_16x16x32_bf16 v[148:151], a[104:107], a[240:243], v[148:151]\n"
      "s_add_u32 m0, s75, 61440\n"
      "ds_read_b128 v[240:243], v200 offset:2048\n"
      "v_mfma_f32_16x16x32_bf16 v[154:157], a[132:135], a[244:247], v[154:157]\n"
      "s_mov_b32 s78, 36864\n"
      "v_rsq_f32_e64 v122, v122\n"
      "v_mfma_f32_16x16x32_bf16 v[154:157], a[136:139], a[248:251], v[154:157]\n"
      "buffer_load_dwordx4 v251, s[36:39], s78 offen  lds\n"
      "v_fma_f32 v142, v184, v184, s51\n"
      "v_cvt_pk_bf16_f32 v147, v198, v199\n"
      "v_mov_b32 v220, v122\n"
      "v_mul_f32 v134, s50, v134\n"
      "v_pk_mul_f32 v[194:195], v[194:195], v[230:231] op_sel_hi:[1,0]\n"
      "v_pk_fma_f32 v[140:141], v[102:103], v[102:103], v[140:141]\n"
      "v_mfma_f32_16x16x32_bf16 v[154:157], a[140:143], a[252:255], v[154:157]\n"
      "s_waitcnt lgkmcnt(1)\n"
      "v_cvt_pk_bf16_f32 v193, v194, v195\n"
      "v_permlane16_swap_b32 v122, v220\n"
      "v_mul_f32_e64 v143, v185, v185\n"
      "v_mfma_f32_16x16x32_bf16 v[222:225], a[168:171], v[236:239], 0\n"
      "ds_write_b64 v249, v[130:131] offset:16\n"
      "s_add_u32 m0, s75, 65408\n"
      "v_mfma_f32_16x16x32_bf16 v[222:225], a[172:175], v[226:229], v[222:225]\n"
      "buffer_load_dwordx4 v251, s[36:39], s78 offen offset:128  lds\n"
      "s_add_u32 m0, s75, 69376\n"
      "v_mfma_f32_16x16x32_bf16 v[130:133], a[144:147], v[236:239], 0\n"
      "ds_write_b128 v249, v[160:163] offset:32\n"
      "v_mfma_f32_16x16x32_bf16 v[130:133], a[148:151], v[226:229], v[130:133]\n"
      "ds_read_b128 v[160:163], v200 offset:4096\n"
      "v_cvt_pk_bf16_f32 v146, v196, v197\n"
      "v_mfma_f32_16x16x32_bf16 v[148:151], a[108:111], a[244:247], v[148:151]\n"
      "buffer_load_dwordx4 v251, s[36:39], s78 offen offset:256  lds\n"
      "v_mfma_f32_16x16x32_bf16 v[148:151], a[112:115], a[248:251], v[148:151]\n"
      "ds_read_b128 v[194:197], v116 offset:2048\n"
      "s_add_u32 m0, s75, 73728\n"
      "v_mfma_f32_16x16x32_bf16 v[148:151], a[116:119], a[252:255], v[148:151]\n"
      "ds_write_b128 v249, v[166:169]\n"
      "v_pk_fma_f32 v[142:143], v[186:187], v[186:187], v[142:143]\n"
      "v_pk_fma_f32 v[140:141], v[104:105], v[104:105], v[140:141]\n"
      "v_pk_fma_f32 v[140:141], v[136:137], v[136:137], v[140:141]\n"
      "s_waitcnt vmcnt(12)\n"
      "s_barrier\n"
      "v_mfma_f32_16x16x32_bf16 v[166:169], a[192:195], v[236:239], 0\n"
      "s_mov_b32 s79, 49152\n"
      "ds_read_b64_tr_b16 a[214:215], v234 offset:12800\n"
      "v_mfma_f32_16x16x32_bf16 v[166:169], a[196:199], v[226:229], v[166:169]\n"
      "buffer_load_dwordx4 v251, s[36:39], s79 offen  lds\n"
      "s_waitcnt lgkmcnt(2)\n"
      "v_mfma_f32_16x16x32_bf16 v[166:169], a[200:203], v[160:163], v[166:169]\n"
      "ds_read_b64_tr_b16 a[164:165], v232 offset:12288\n"
      "ds_read_b64_tr_b16 a[208:209], v234\n"
      "v_mul_f32 v122, s50, v122\n"
      "v_mfma_f32_16x16x32_bf16 v[130:133], a[152:155], v[160:163], v[130:133]\n"
      "ds_read_b64_tr_b16 a[188:189], v233 offset:12288\n"
      "ds_read_b64_tr_b16 a[162:163], v232 offset:512\n"
      "s_add_u32 m0, s75, 77696\n"
      "v_mfma_f32_16x16x32_bf16 v[226:229], a[168:171], v[240:243], 0\n"
      "buffer_load_dwordx4 v251, s[36:39], s79 offen offset:128  lds\n"
      "v_mfma_f32_16x16x32_bf16 v[222:225], a[176:179], v[160:163], v[222:225]\n"
      "s_add_u32 m0, s75, 81664\n"
      "ds_read_b64_tr_b16 a[160:161], v232\n"
      "ds_read_b64_tr_b16 a[166:167], v232 offset:12800\n"
      "ds_read_b64_tr_b16 a[190:191], v233 offset:12800\n"
      "v_mfma_f32_16x16x32_bf16 v[160:163], a[192:195], v[240:243], 0\n"
      "ds_read_b64_tr_b16 a[186:187], v233 offset:512\n"
      "ds_read_b64_tr_b16 a[184:185], v233\n"
      "ds_read_b64_tr_b16 a[212:213], v234 offset:12288\n"
      "ds_read_b64_tr_b16 a[210:211], v234 offset:512\n"
      "v_pk_mul_f32 v[244:245], v[244:245], v[122:123] op_sel_hi:[1,0]\n"
      "v_pk_mul_f32 v[212:213], v[212:213], v[220:221] op_sel_hi:[1,0]\n"
      "v_pk_mul_f32 v[96:97], v[96:97], v[134:135] op_sel_hi:[1,0]\n"
      "v_pk_mul_f32 v[202:203], v[202:203], v[134:135] op_sel_hi:[1,0]\n"
      "v_pk_fma_f32 v[140:141], v[138:139], v[138:139], v[140:141]\n"
      "v_pk_mul_f32 v[246:247], v[246:247], v[122:123] op_sel_hi:[1,0]\n"
      "v_pk_mul_f32 v[108:109], v[108:109], v[220:221] op_sel_hi:[1,0]\n"
      "v_pk_mul_f32 v[106:107], v[106:107], v[220:221] op_sel_hi:[1,0]\n"
      "v_cvt_pk_bf16_f32 v181, v182, v183\n"
      "v_pk_mul_f32 v[216:217], v[216:217], v[100:101] op_sel_hi:[1,0]\n"
      "v_pk_mul_f32 v[190:191], v[190:191], v[122:123] op_sel_hi:[1,0]\n"
      "v_cvt_pk_bf16_f32 v183, v218, v219\n"
      "v_pk_mul_f32 v[114:115], v[114:115], v[220:221] op_sel_hi:[1,0]\n"
      "v_cvt_pk_bf16_f32 v212, v212, v213\n"
      "v_cvt_pk_bf16_f32 v202, v202, v203\n"
      "v_pk_fma_f32 v[142:143], v[148:149], v[148:149], v[142:143]\n"
      "v_pk_mul_f32 v[98:99], v[98:99], v[134:135] op_sel_hi:[1,0]\n"
      "v_pk_mul_f32 v[124:125], v[124:125], v[100:101] op_sel_hi:[1,0]\n"
      "v_pk_fma_f32 v[142:143], v[150:151], v[150:151], v[142:143]\n"
      "v_mfma_f32_16x16x32_bf16 v[226:229], a[172:175], v[194:197], v[226:229]\n"
      "ds_read_b128 v[236:239], v116 offset:4096\n"
      "buffer_load_dwordx4 v251, s[36:39], s79 offen offset:256  lds\n"
      "ds_read_b128 v[230:233], v200 offset:6144\n"
      "s_mov_b32 s79, 61440\n"
      "v_pk_fma_f32 v[142:143], v[154:155], v[154:155], v[142:143]\n"
      "v_pk_mul_f32 v[204:205], v[204:205], v[134:135] op_sel_hi:[1,0]\n"
      "v_add_f32_e64 v140, v140, v141\n"
      "v_cvt_pk_bf16_f32 v203, v204, v205\n"
      "v_cvt_pk_bf16_f32 v124, v124, v125\n"
      "v_pk_mul_f32 v[188:189], v[188:189], v[122:123] op_sel_hi:[1,0]\n"
      "v_pk_mul_f32 v[126:127], v[126:127], v[100:101] op_sel_hi:[1,0]\n"
      "v_pk_mul_f32 v[214:215], v[214:215], v[220:221] op_sel_hi:[1,0]\n"
      "v_pk_mul_f32 v[112:113], v[112:113], v[220:221] op_sel_hi:[1,0]\n"
      "v_pk_mul_f32 v[208:209], v[208:209], v[122:123] op_sel_hi:[1,0]\n"
      "v_pk_mul_f32 v[254:255], v[254:255], v[134:135] op_sel_hi:[1,0]\n"
      "v_cvt_pk_bf16_f32 v182, v216, v217\n"
      "v_pk_fma_f32 v[142:143], v[156:157], v[156:157], v[142:143]\n"
      "v_mfma_f32_16x16x32_bf16 v[218:221], a[144:147], v[240:243], 0\n"
      "s_add_u32 m0, s75, 86016\n"
      "ds_read_b128 v[240:243], v200 offset:8192\n"
      "v_mfma_f32_16x16x32_bf16 v[218:221], a[148:151], v[194:197], v[218:221]\n"
      "buffer_load_dwordx4 v251, s[36:39], s79 offen  lds\n"
      "v_mfma_f32_16x16x32_bf16 v[160:163], a[196:199], v[194:197], v[160:163]\n"
      "s_waitcnt lgkmcnt(1)\n"
      "v_cvt_pk_bf16_f32 v106, v106, v107\n"
      "v_cvt_pk_bf16_f32 v208, v208, v209\n"
      "v_mfma_f32_16x16x32_bf16 v[160:163], a[200:203], v[230:233], v[160:163]\n"
      "ds_read_b128 v[194:197], v116 offset:8192\n"
      "v_add_f32 v142, v142, v143\n"
      "v_mfma_f32_16x16x32_bf16 v[226:229], a[176:179], v[230:233], v[226:229]\n"
      "s_add_u32 m0, s75, 89984\n"
      "v_permlane16_swap_b32_e64 v140, v142\n"
      "v_mfma_f32_16x16x32_bf16 v[166:169], a[204:207], v[236:239], v[166:169]\n"
      "buffer_load_dwordx4 v251, s[36:39], s79 offen offset:128  lds\n"
      "v_cvt_pk_bf16_f32 v96, v96, v97\n"
      "v_cvt_pk_bf16_f32 v213, v214, v215\n"
      "v_pk_mul_f32 v[210:211], v[210:211], v[122:123] op_sel_hi:[1,0]\n"
      "v_add_f32 v140, v140, v142\n"
      "v_mov_b32 v142, v140\n"
      "v_mfma_f32_16x16x32_bf16 v[218:221], a[152:155], v[230:233], v[218:221]\n"
      "s_add_u32 m0, s75, 93952\n"
      "v_permlane32_swap_b32_e64 v140, v142\n"
      "v_cvt_pk_bf16_f32 v125, v126, v127\n"
      "v_add_f32_e64 v140, v140, v142\n"
      "v_mfma_f32_16x16x32_bf16 v[130:133], a[156:159], v[236:239], v[130:133]\n"
      "ds_read_b128 v[214:217], v200 offset:10240\n"
      "v_cvt_pk_bf16_f32 v97, v98, v99\n"
      "v_mfma_f32_16x16x32_bf16 v[222:225], a[180:183], v[236:239], v[222:225]\n"
      "s_waitcnt lgkmcnt(1)\n"
      "v_rsq_f32 v140, v140\n"
      "v_mfma_f32_16x16x32_bf16 v[222:225], a[184:187], v[240:243], v[222:225]\n"
      "buffer_load_dwordx4 v251, s[36:39], s79 offen offset:256  lds\n"
      "ds_read_b128 v[126:129], v0 offset:4096\n"
      "v_mov_b32_e64 v142, v140\n"
      "v_mfma_f32_16x16x32_bf16 v[222:225], a[188:191], v[194:197], v[222:225]\n"
      "ds_read_b128 v[120:123], v116 offset:6144\n"
      "v_cvt_pk_bf16_f32 v209, v210, v211\n"
      "v_mfma_f32_16x16x32_bf16 v[130:133], a[160:163], v[240:243], v[130:133]\n"
      "ds_read_b128 v[198:201], v116 offset:10240\n"
      "v_permlane16_swap_b32_e64 v140, v142\n"
      "v_mfma_f32_16x16x32_bf16 v[130:133], a[164:167], v[194:197], v[130:133]\n"
      "buffer_load_dwordx4 v[98:101], v36, s[28:31], s49 offen\n"
      "s_waitcnt vmcnt(13)\n"
      "s_barrier\n"
      "ds_read_b64_tr_b16 a[240:241], v235 offset:24584\n"
      "v_pk_mul_f32 v[252:253], v[252:253], v[134:135] op_sel_hi:[1,0]\n"
      "v_mul_f32 v140, s50, v140\n"
      "v_pk_mul_f32 v[138:139], v[138:139], v[140:141] op_sel_hi:[1,0]\n"
      "v_mfma_f32_16x16x32_bf16 v[166:169], a[208:211], v[240:243], v[166:169]\n"
      "ds_read_b128 v[240:243], v0 offset:10240\n"
      "v_cvt_pk_bf16_f32 v211, v246, v247\n"
      "v_mfma_f32_16x16x32_bf16 v[166:169], a[212:215], v[194:197], v[166:169]\n"
      "buffer_load_dword v251, v29, s[28:31], s49 offen\n"
      "s_waitcnt lgkmcnt(2)\n"
      "v_mfma_f32_16x16x32_bf16 v[160:163], a[204:207], v[120:123], v[160:163]\n"
      "ds_read_b64_tr_b16 a[244:245], v235 offset:36872\n"
      "ds_read_b64_tr_b16 v[236:237], v248 offset:36864\n"
      "v_mfma_f32_16x16x32_bf16 v[160:163], a[208:211], v[214:217], v[160:163]\n"
      "ds_read_b64_tr_b16 a[222:223], v235 offset:38912\n"
      "ds_read_b64_tr_b16 a[218:219], v235 offset:26624\n"
      "v_mfma_f32_16x16x32_bf16 v[218:221], a[156:159], v[120:123], v[218:221]\n"
      "ds_read_b64_tr_b16 v[232:233], v248 offset:24576\n"
      "ds_read_b64_tr_b16 a[220:221], v235 offset:36864\n"
      "ds_read_b64_tr_b16 a[216:217], v235 offset:24576\n"
      "ds_read_b64_tr_b16 a[246:247], v235 offset:38920\n"
      "ds_read_b64_tr_b16 v[238:239], v248 offset:38912\n"
      "v_mfma_f32_16x16x32_bf16 v[226:229], a[180:183], v[120:123], v[226:229]\n"
      "ds_read_b64_tr_b16 a[242:243], v235 offset:26632\n"
      "v_cvt_pk_bf16_f32 v205, v254, v255\n"
      "v_cvt_pk_bf16_f32 v210, v244, v245\n"
      "v_mfma_f32_16x16x32_bf16 v[226:229], a[184:187], v[214:217], v[226:229]\n"
      "ds_read_b64_tr_b16 v[234:235], v248 offset:26624\n"
      "buffer_load_dwordx4 v[120:123], v36, s[28:31], s74 offen\n"
      "v_pk_mul_f32 v[172:173], v[172:173], v[140:141] op_sel_hi:[1,0]\n"
      "v_cvt_pk_bf16_f32 v107, v108, v109\n"
      "v_pk_mul_f32 v[104:105], v[104:105], v[140:141] op_sel_hi:[1,0]\n"
      "v_cvt_pk_bf16_f32 v109, v114, v115\n"
      "v_pk_mul_f32 v[148:149], v[148:149], v[142:143] op_sel_hi:[1,0]\n"
      "v_xor_b32 v248, 64, v8\n"
      "v_cvt_pk_bf16_f32 v108, v112, v113\n"
      "v_mfma_f32_16x16x32_bf16 v[226:229], a[188:191], v[198:201], v[226:229]\n"
      "ds_read_b128 v[194:197], v0 offset:16384\n"
      "v_cvt_pk_bf16_f32 v204, v252, v253\n"
      "v_mfma_f32_16x16x32_bf16 v[218:221], a[160:163], v[214:217], v[218:221]\n"
      "ds_read_b128 v[214:217], v1 offset:10240\n"
      "ds_read_b128 v[244:247], v1 offset:4096\n"
      "buffer_load_dword v88, v29, s[28:31], s74 offen\n"
      "v_add_u32 v90, 49152, v248\n"
      "v_pk_mul_f32 v[186:187], v[186:187], v[142:143] op_sel_hi:[1,0]\n"
      "v_cvt_pk_bf16_f32 v166, v166, v167\n"
      "v_pk_mul_f32 v[156:157], v[156:157], v[142:143] op_sel_hi:[1,0]\n"
      "v_pk_mul_f32 v[150:151], v[150:151], v[142:143] op_sel_hi:[1,0]\n"
      "s_add_u32 m0, s64, 18432\n"
      "v_pk_mul_f32 v[154:155], v[154:155], v[142:143] op_sel_hi:[1,0]\n"
      "v_pk_mul_f32 v[136:137], v[136:137], v[140:141] op_sel_hi:[1,0]\n"
      "v_pk_mul_f32 v[184:185], v[184:185], v[142:143] op_sel_hi:[1,0]\n"
      "v_cvt_pk_bf16_f32 v226, v226, v227\n"
      "v_cvt_pk_bf16_f32 v184, v184, v185\n"
      "v_pk_mul_f32 v[170:171], v[170:171], v[140:141] op_sel_hi:[1,0]\n"
      "v_cvt_pk_bf16_f32 v227, v228, v229\n"
      "v_pk_mul_f32 v[102:103], v[102:103], v[140:141] op_sel_hi:[1,0]\n"
      "v_cvt_pk_bf16_f32 v167, v168, v169\n"
      "v_cvt_pk_bf16_f32 v185, v186, v187\n"
      "v_mfma_f32_16x16x32_bf16 v[218:221], a[164:167], v[198:201], v[218:221]\n"
      "buffer_load_dwordx4 v16, s[16:19], s45 offen  lds\n"
      "v_cvt_pk_bf16_f32 v222, v222, v223\n"
      "v_mfma_f32_16x16x32_bf16 v[228:231], v[126:129], a[144:147], 0\n"
      "s_waitcnt lgkmcnt(0)\n"
      "v_cvt_pk_bf16_f32 v223, v224, v225\n"
      "v_cvt_pk_bf16_f32 v170, v170, v171\n"
      "v_mfma_f32_16x16x32_bf16 v[228:231], v[244:247], a[148:151], v[228:231]\n"
      "ds_write_b64 v250, v[166:167] offset:64\n"
      "v_cvt_pk_bf16_f32 v188, v188, v189\n"
      "v_mfma_f32_16x16x32_bf16 v[252:255], v[126:129], a[192:195], 0\n"
      "s_add_u32 m0, s64, 22528\n"
      "v_cvt_pk_bf16_f32 v130, v130, v131\n"
      "v_cvt_pk_bf16_f32 v186, v148, v149\n"
      "v_mfma_f32_16x16x32_bf16 v[252:255], v[244:247], a[196:199], v[252:255]\n"
      "buffer_load_dwordx4 v17, s[16:19], s45 offen  lds\n"
      "v_cvt_pk_bf16_f32 v171, v172, v173\n"
      "v_mfma_f32_16x16x32_bf16 v[160:163], a[212:215], v[198:201], v[160:163]\n"
      "ds_read_b128 v[166:169], v0 offset:2048\n"
      "ds_write_b64 v250, v[222:223] offset:32\n"
      "v_cvt_pk_bf16_f32 v131, v132, v133\n"
      "v_mov_b32 v223, 0xbc80bc80\n"
      "v_mfma_f32_16x16x32_bf16 v[228:231], v[240:243], a[152:155], v[228:231]\n"
      "s_add_u32 m0, s64, 26624\n"
      "v_cvt_pk_bf16_f32 v154, v154, v155\n"
      "v_add_u32 v178, 49152, v8\n"
      "v_cvt_pk_bf16_f32 v136, v136, v137\n"
      "v_mfma_f32_16x16x32_bf16 v[252:255], v[240:243], a[200:203], v[252:255]\n"
      "buffer_load_dwordx4 v18, s[16:19], s45 offen  lds\n"
      "v_cvt_pk_bf16_f32 v172, v102, v103\n"
      "v_mfma_f32_16x16x32_bf16 v[110:113], v[126:129], a[168:171], 0\n"
      "ds_read_b128 v[140:143], v1 offset:2048\n"
      "v_cvt_pk_bf16_f32 v189, v190, v191\n"
      "v_cvt_pk_bf16_f32 v218, v218, v219\n"
      "v_mfma_f32_16x16x32_bf16 v[110:113], v[244:247], a[172:175], v[110:113]\n"
      "ds_read_b128 v[198:201], v1 offset:16384\n"
      "ds_read_b128 v[114:117], v8 offset:24576\n"
      "v_mfma_f32_16x16x32_bf16 v[110:113], v[240:243], a[176:179], v[110:113]\n"
      "s_add_u32 m0, s64, 30720\n"
      "v_mov_b32 v224, 0xbc80bc80\n"
      "v_mov_b32 v225, 0xbc80bc80\n"
      "v_mfma_f32_16x16x32_bf16 v[252:255], v[214:217], a[204:207], v[252:255]\n"
      "ds_read_b128 v[132:135], v248 offset:24576\n"
      "v_cvt_pk_bf16_f32 v173, v104, v105\n"
      "v_mfma_f32_16x16x32_bf16 v[252:255], v[194:197], a[208:211], v[252:255]\n"
      "ds_write_b64 v250, v[130:131]\n"
      "s_waitcnt vmcnt(13)\n"
      "s_barrier\n"
      "v_cvt_pk_bf16_f32 v187, v150, v151\n"
      "v_mfma_f32_16x16x32_bf16 v[228:231], v[214:217], a[156:159], v[228:231]\n"
      "ds_read_b64_tr_b16 v[244:245], v82 offset:12288\n"
      "ds_read_b64_tr_b16 a[230:231], v89 offset:14336\n"
      "v_mfma_f32_16x16x32_bf16 v[228:231], v[194:197], a[160:163], v[228:231]\n"
      "ds_read_b64_tr_b16 a[252:253], v89 offset:12296\n"
      "v_mov_b32 v222, 0xbc80bc80\n"
      "v_mfma_f32_16x16x32_bf16 v[110:113], v[214:217], a[180:183], v[110:113]\n"
      "buffer_load_dwordx4 v19, s[16:19], s45 offen  lds\n"
      "ds_read_b64_tr_b16 a[248:249], v89 offset:8\n"
      "v_mfma_f32_16x16x32_bf16 v[110:113], v[194:197], a[184:187], v[110:113]\n"
      "ds_read_b64_tr_b16 v[242:243], v82 offset:2048\n"
      "s_waitcnt lgkmcnt(6)\n"
      "v_mfma_f32_16x16x32_bf16 v[110:113], v[198:201], a[188:191], v[110:113]\n"
      "ds_read_b128 v[148:151], v90\n"
      "ds_read_b64_tr_b16 a[250:251], v89 offset:2056\n"
      "s_add_u32 m0, s65, 34816\n"
      "v_mfma_f32_16x16x32_bf16 v[102:105], v[114:117], v[222:225], 0\n"
      "ds_read_b64_tr_b16 a[254:255], v89 offset:14344\n"
      "ds_read_b64_tr_b16 v[246:247], v82 offset:14336\n"
      "v_mfma_f32_16x16x32_bf16 v[102:105], v[132:135], v[222:225], v[102:105]\n"
      "ds_read_b64_tr_b16 v[240:241], v82\n"
      "v_cvt_pk_bf16_f32 v137, v138, v139\n"
      "v_mfma_f32_16x16x32_bf16 v[252:255], v[198:201], a[212:215], v[252:255]\n"
      "ds_read_b128 v[114:117], v178\n"
      "ds_read_b64_tr_b16 a[224:225], v89\n"
      "v_mfma_f32_16x16x32_bf16 v[228:231], v[198:201], a[164:167], v[228:231]\n"
      "buffer_load_dwordx4 v15, s[16:19], s45 offen  lds\n"
      "v_mfma_f32_16x16x32_bf16 v[214:217], v[166:169], a[192:195], 0\n"
      "ds_read_b64_tr_b16 a[226:227], v89 offset:2048\n"
      "v_cvt_pk_bf16_f32 v155, v156, v157\n"
      "v_mfma_f32_16x16x32_bf16 v[214:217], v[140:143], a[196:199], v[214:217]\n"
      "ds_read_b64_tr_b16 a[228:229], v89 offset:12288\n"
      "v_cvt_pk_bf16_f32 v219, v220, v221\n"
      "v_mfma_f32_16x16x32_bf16 v[130:133], v[166:169], a[144:147], 0\n"
      "ds_read_b128 v[126:129], v1 offset:8192\n"
      "v_cvt_pk_bf16_f32 v160, v160, v161\n"
      "v_mfma_f32_16x16x32_bf16 v[194:197], v[166:169], a[168:171], 0\n"
      "ds_read_b128 v[166:169], v248 offset:28672\n"
      "s_waitcnt lgkmcnt(5)\n"
      "v_mfma_f32_16x16x32_bf16 v[156:159], v[114:117], v[222:225], 0\n"
      "ds_read_b128 v[114:117], v8 offset:28672\n"
      "v_cvt_pk_bf16_f32 v76, v228, v229\n"
      "v_mfma_f32_16x16x32_bf16 v[156:159], v[148:151], v[222:225], v[156:159]\n"
      "ds_read_b128 v[148:151], v0 offset:8192\n"
      "ds_read_b128 v[198:201], v8 offset:32768\n"
      "v_mfma_f32_16x16x32_bf16 v[130:133], v[140:143], a[148:151], v[130:133]\n"
      "s_mov_b32 exec_lo, 0x1f001f\n"
      "s_mov_b32 exec_hi, 0x1f001f\n"
      "ds_write_b128 v249, v[170:173] offset:18688\n"
      "ds_write_b64 v249, v[136:137] offset:18704\n"
      "ds_write_b64 v249, v[154:155] offset:18712\n"
      "ds_write_b128 v249, v[184:187] offset:18720\n"
      "s_mov_b64 exec, -1\n"
      "v_cvt_pk_bf16_f32 v80, v110, v111\n"
      "v_mfma_f32_16x16x32_bf16 v[194:197], v[140:143], a[172:175], v[194:197]\n"
      "s_waitcnt lgkmcnt(4)\n"
      "v_cvt_pk_bf16_f32 v161, v162, v163\n"
      "v_cvt_pk_bf16_f32 v81, v112, v113\n"
      "v_cvt_pk_bf16_f32 v77, v230, v231\n"
      "v_mfma_f32_16x16x32_bf16 v[214:217], v[148:151], a[200:203], v[214:217]\n"
      "ds_read_b128 v[228:231], v1 offset:14336\n"
      "v_cvt_pk_bf16_f32 v85, v254, v255\n"
      "v_mfma_f32_16x16x32_bf16 v[214:217], v[126:129], a[204:207], v[214:217]\n"
      "buffer_load_dwordx4 v[140:143], v36, s[28:31], s47 offen\n"
      "v_mfma_f32_16x16x32_bf16 v[194:197], v[148:151], a[176:179], v[194:197]\n"
      "ds_read_b128 v[110:113], v248 offset:32768\n"
      "v_subrev_u32 v8, 115456, v9\n"
      "v_mfma_f32_16x16x32_bf16 v[130:133], v[148:151], a[152:155], v[130:133]\n"
      "ds_read_b128 v[162:165], v178 offset:8192\n"
      "v_lshrrev_b32 v8, 4, v8\n"
      "v_mfma_f32_16x16x32_bf16 v[130:133], v[126:129], a[156:159], v[130:133]\n"
      "s_lshl_b32 s92, s40, 6\n"
      "ds_read_b128 v[148:151], v90 offset:4096\n"
      "v_mfma_f32_16x16x32_bf16 v[194:197], v[126:129], a[180:183], v[194:197]\n"
      "ds_read_b128 v[126:129], v178 offset:4096\n"
      "buffer_load_dword v135, v29, s[28:31], s47 offen\n"
      "v_mfma_f32_16x16x32_bf16 v[102:105], v[114:117], v[222:225], v[102:105]\n"
      "ds_read_b128 v[114:117], v90 offset:8192\n"
      "v_and_b32_e64 v8, 48, v8\n"
      "v_mfma_f32_16x16x32_bf16 v[102:105], v[166:169], v[222:225], v[102:105]\n"
      "ds_read_b128 v[166:169], v0 offset:14336\n"
      "s_mov_b32 exec_lo, 0x1f001f\n"
      "s_mov_b32 exec_hi, 0x1f001f\n"
      "ds_write_b64 v250, v[218:219] offset:18688\n"
      "ds_write_b64 v250, v[226:227] offset:18720\n"
      "ds_write_b64 v250, v[160:161] offset:18752\n"
      "s_mov_b64 exec, -1\n"
      "v_add_u32 v8, 123136, v8\n"
      "v_mfma_f32_16x16x32_bf16 v[102:105], v[198:201], v[222:225], v[102:105]\n"
      "ds_read_b128 v[218:221], v0 offset:6144\n"
      "v_subrev_u32 v186, 115456, v9\n"
      "v_cvt_pk_bf16_f32 v84, v252, v253\n"
      "s_waitcnt lgkmcnt(4)\n"
      "v_mfma_f32_16x16x32_bf16 v[102:105], v[110:113], v[222:225], v[102:105]\n"
      "s_waitcnt vmcnt(11)\n"
      "s_barrier\n"
      "ds_read_b128 v[252:255], v178 offset:24576\n"
      "v_mfma_f32_16x16x32_bf16 v[156:159], v[126:129], v[222:225], v[156:159]\n"
      "ds_read_b128 v[110:113], v178 offset:32768\n"
      "v_bfe_u32 v190, v186, 8, 2\n"
      "v_mfma_f32_16x16x32_bf16 v[156:159], v[148:151], v[222:225], v[156:159]\n"
      "ds_read_b128 v[126:129], v90 offset:28672\n"
      "v_bfe_u32 v186, v186, 4, 4\n"
      "v_mfma_f32_16x16x32_bf16 v[156:159], v[162:165], v[222:225], v[156:159]\n"
      "ds_read_b128 v[160:163], v178 offset:28672\n"
      "v_mul_f32 v102, 0x3eaaaaab, v102\n"
      "v_mfma_f32_16x16x32_bf16 v[156:159], v[114:117], v[222:225], v[156:159]\n"
      "ds_read_b128 v[114:117], v90 offset:24576\n"
      "v_mul_f32 v103, 0x3eaaaaab, v103\n"
      "v_lshlrev_b32 v190, 2, v190\n"
      "v_mfma_f32_16x16x32_bf16 v[130:133], v[166:169], a[160:163], v[130:133]\n"
      "ds_read_b128 v[198:201], v0\n"
      "s_waitcnt vmcnt(10)\n"
      "v_or3_b32 v98, v98, v99, v100\n"
      "v_mfma_f32_16x16x32_bf16 v[194:197], v[166:169], a[184:187], v[194:197]\n"
      "ds_read_b64 v[164:165], v10 offset:24\n"
      "v_mul_f32 v159, 0x3eaaaaab, v159\n"
      "v_mfma_f32_16x16x32_bf16 v[194:197], v[228:231], a[188:191], v[194:197]\n"
      "ds_read_b128 v[136:139], v10 offset:32\n"
      "v_mul_f32 v104, 0x3eaaaaab, v104\n"
      "v_mfma_f32_16x16x32_bf16 v[214:217], v[166:169], a[208:211], v[214:217]\n"
      "ds_read_b128 v[166:169], v1\n"
      "v_mul_f32 v105, 0x3eaaaaab, v105\n"
      "v_mfma_f32_16x16x32_bf16 v[130:133], v[228:231], a[164:167], v[130:133]\n"
      "ds_read_b64_tr_b16 a[236:237], v89 offset:36864\n"
      "s_waitcnt vmcnt(9)\n"
      "v_or3_b32 v98, v98, v101, v251\n"
      "v_mfma_f32_16x16x32_bf16 v[214:217], v[228:231], a[212:215], v[214:217]\n"
      "ds_read_b128 v[228:231], v90 offset:32768\n"
      "s_waitcnt lgkmcnt(6)\n"
      "v_mfma_f32_16x16x32_bf16 v[170:173], v[252:255], v[222:225], 0\n"
      "ds_read_b64_tr_b16 v[248:249], v82 offset:24576\n"
      "v_add_u32 v184, s92, v8\n"
      "v_mfma_f32_16x16x32_bf16 v[170:173], v[114:117], v[222:225], v[170:173]\n"
      "ds_write_b32 v31, v98 offset:6656\n"
      "v_mfma_f32_16x16x32_bf16 v[170:173], v[160:163], v[222:225], v[170:173]\n"
      "ds_read_b64_tr_b16 a[238:239], v89 offset:38912\n"
      "v_cvt_pk_bf16_f32 v197, v196, v197\n"
      "v_mfma_f32_16x16x32_bf16 v[170:173], v[126:129], v[222:225], v[170:173]\n"
      "ds_write_b128 v184, v[102:105]\n"
      "v_mfma_f32_16x16x32_bf16 v[170:173], v[110:113], v[222:225], v[170:173]\n"
      "s_waitcnt lgkmcnt(4)\n"
      "v_mul_f32 v156, 0x3eaaaaab, v156\n"
      "v_mul_f32 v158, 0x3eaaaaab, v158\n"
      "v_mul_f32 v157, 0x3eaaaaab, v157\n"
      "v_mfma_f32_16x16x32_bf16 v[170:173], v[228:231], v[222:225], v[170:173]\n"
      "ds_read_b64_tr_b16 a[232:233], v89 offset:24576\n"
      "ds_read_b64_tr_b16 v[226:227], v89 offset:26632\n"
      "v_mfma_f32_16x16x32_bf16 v[160:163], v[144:147], v[174:177], v[40:43]\n"
      "ds_read_b64_tr_b16 v[228:229], v89 offset:36872\n"
      "s_waitcnt vmcnt(8)\n"
      "v_or3_b32 v120, v120, v121, v122\n"
      "v_mfma_f32_16x16x32_bf16 v[110:113], v[198:201], a[168:171], 0\n"
      "ds_write_b128 v184, v[156:159] offset:256\n"
      "v_mfma_f32_16x16x32_bf16 v[126:129], v[136:139], v[174:177], v[24:27]\n"
      "ds_read_b64_tr_b16 v[230:231], v89 offset:38920\n"
      "v_mul_f32 v173, 0x3eaaaaab, v173\n"
      "v_mfma_f32_16x16x32_bf16 v[102:105], v[180:183], v[174:177], v[44:47]\n"
      "ds_read_b64_tr_b16 v[224:225], v89 offset:24584\n"
      "s_waitcnt vmcnt(7)\n"
      "v_or3_b32 v120, v120, v123, v88\n"
      "v_mfma_f32_16x16x32_bf16 v[110:113], v[166:169], a[172:175], v[110:113]\n"
      "ds_read_b64_tr_b16 v[254:255], v82 offset:38912\n"
      "v_mul_f32 v170, 0x3eaaaaab, v170\n"
      "v_mfma_f32_16x16x16_bf16 v[160:163], v[192:193], v[118:119], v[160:163]\n"
      "ds_read_b64_tr_b16 a[234:235], v89 offset:26624\n"
      "v_mul_f32 v171, 0x3eaaaaab, v171\n"
      "v_mfma_f32_16x16x32_bf16 v[156:159], v[198:201], a[144:147], 0\n"
      "ds_write_b32 v31, v120 offset:5632\n"
      "v_mfma_f32_16x16x32_bf16 v[156:159], v[166:169], a[148:151], v[156:159]\n"
      "ds_read_b64_tr_b16 v[250:251], v82 offset:26624\n"
      "v_cvt_pk_bf16_f32 v196, v194, v195\n"
      "v_mfma_f32_16x16x32_bf16 v[120:123], v[198:201], a[192:195], 0\n"
      "ds_read_b128 v[98:101], v1 offset:6144\n"
      "v_cvt_pk_bf16_f32 v217, v216, v217\n"
      "v_mfma_f32_16x16x32_bf16 v[148:151], v[106:109], v[174:177], v[48:51]\n"
      "ds_read_b64_tr_b16 v[252:253], v82 offset:36864\n"
      "v_mul_f32 v172, 0x3eaaaaab, v172\n"
      "v_mfma_f32_16x16x32_bf16 v[110:113], v[218:221], a[176:179], v[110:113]\n"
      "ds_write_b128 v184, v[170:173] offset:512\n"
      "v_mfma_f32_16x16x32_bf16 v[120:123], v[166:169], a[196:199], v[120:123]\n"
      "s_waitcnt lgkmcnt(0)\n"
      "s_barrier\n"
      "v_cvt_pk_bf16_f32 v133, v132, v133\n"
      "v_mfma_f32_16x16x32_bf16 v[120:123], v[218:221], a[200:203], v[120:123]\n"
      "ds_read_b128 v[174:177], v9 offset:6656\n"
      "v_sub_u32 v186, v186, v190\n"
      "v_mfma_f32_16x16x16_bf16 v[126:129], v[164:165], v[118:119], v[126:129]\n"
      "ds_read_b128 v[166:169], v0 offset:12288\n"
      "v_cvt_pk_bf16_f32 v132, v130, v131\n"
      "v_cvt_pk_bf16_f32 v216, v214, v215\n"
      "v_mfma_f32_16x16x16_bf16 v[102:105], v[124:125], v[118:119], v[102:105]\n"
      "v_lshlrev_b32 v186, 4, v186\n"
      "v_mov_b32 v190, 0x3f80\n"
      "v_mov_b32 v184, 0\n"
      "v_mfma_f32_16x16x32_bf16 v[170:173], v[144:147], v[202:205], v[52:55]\n"
      "s_waitcnt lgkmcnt(3)\n"
      "v_mov_b32_e64 v191, 0\n"
      "v_cmp_gt_u32 s[80:81], 64, v186\n"
      "v_mfma_f32_16x16x32_bf16 v[198:201], v[106:109], v[202:205], v[60:63]\n"
      "s_cmp_ge_u32 s95, 0x40000000\n"
      "s_cselect_b32 s94, -1, -2\n"
      "v_lshlrev_b64 v[190:191], v186, v[190:191]\n"
      "v_mfma_f32_16x16x32_bf16 v[156:159], v[218:221], a[152:155], v[156:159]\n"
      "ds_read_b128 v[218:221], v1 offset:12288\n"
      "v_mov_b32 v128, 0\n"
      "v_mfma_f32_16x16x32_bf16 v[110:113], v[98:101], a[180:183], v[110:113]\n"
      "s_waitcnt lgkmcnt(2)\n"
      "v_or3_b32 v174, v174, v175, v176\n"
      "v_bitop3_b32 v174, v174, v177, s33 bitop3:0xa8\n"
      "v_mfma_f32_16x16x16_bf16 v[148:151], v[212:213], v[118:119], v[148:151]\n"
      "v_cndmask_b32_e64 v186, 0, v190, s[80:81]\n"
      "v_mov_b32_e64 v129, 0\n"
      "v_cmp_ne_u32 s[56:57], 0, v174\n"
      "v_mfma_f32_16x16x32_bf16 v[120:123], v[98:101], a[204:207], v[120:123]\n"
      "s_or_b64 vcc, s[56:57], s[54:55]\n"
      "v_cndmask_b32_e64 v187, 0, v191, s[80:81]\n"
      "s_waitcnt lgkmcnt(1)\n"
      "v_mfma_f32_16x16x32_bf16 v[120:123], v[166:169], a[208:211], v[120:123]\n"
      "s_cbranch_vccnz L_rare_1_%=\n"
      "L_back_2_%=:\n"
      "v_mfma_f32_16x16x32_bf16 v[156:159], v[98:101], a[156:159], v[156:159]\n"
      "s_cbranch_vccnz L_rare_3_%=\n"
      "L_back_4_%=:\n"
      "v_mfma_f32_16x16x32_bf16 v[156:159], v[166:169], a[160:163], v[156:159]\n"
      "v_exp_f32 v126, v126\n"
      "v_mfma_f32_16x16x32_bf16 v[110:113], v[166:169], a[184:187], v[110:113]\n"
      "s_waitcnt lgkmcnt(0)\n"
      "v_exp_f32 v103, v103\n"
      "v_exp_f32 v163, v163\n"
      "v_exp_f32_e64 v150, v150\n"
      "v_mfma_f32_16x16x32_bf16 v[110:113], v[218:221], a[188:191], v[110:113]\n"
      "ds_read_b128 v[174:177], v8\n"
      "v_exp_f32_e64 v151, v151\n"
      "v_mfma_f32_16x16x32_bf16 v[120:123], v[218:221], a[212:215], v[120:123]\n"
      "ds_read_b128 v[166:169], v8 offset:64\n"
      "v_cndmask_b32_e64 v127, 0, v191, s[80:81]\n"
      "v_mfma_f32_16x16x32_bf16 v[156:159], v[218:221], a[164:167], v[156:159]\n"
      "ds_read_u16 v82, v11 offset:32\n"
      "v_exp_f32 v148, v148\n"
      "v_mfma_f32_16x16x16_bf16 v[170:173], v[192:193], v[96:97], v[170:173]\n"
      "v_exp_f32 v162, v162\n"
      "v_exp_f32 v160, v160\n"
      "v_mfma_f32_16x16x16_bf16 v[198:201], v[212:213], v[96:97], v[198:201]\n"
      "v_exp_f32 v161, v161\n"
      "v_cvt_pk_bf16_f32 v89, v150, v151\n"
      "v_exp_f32_e64 v104, v104\n"
      "v_cvt_pk_bf16_f32 v130, v156, v157\n"
      "v_cvt_pk_bf16_f32 v160, v160, v161\n"
      "v_cvt_pk_bf16_f32 v195, v112, v113\n"
      "v_mfma_f32_16x16x32_bf16 v[218:221], v[136:139], v[202:205], v[26:29]\n"
      "s_waitcnt lgkmcnt(2)\n"
      "v_cvt_pk_bf16_f32 v90, v126, 0\n"
      "v_exp_f32 v102, v102\n"
      "v_mfma_f32_16x16x32_bf16 v[154:157], v[180:183], v[202:205], v[56:59]\n"
      "ds_read_b128 v[202:205], v9 offset:5632\n"
      "v_cvt_pk_bf16_f32 v161, v162, v163\n"
      "v_cvt_pk_bf16_f32 v194, v110, v111\n"
      "v_mfma_f32_16x16x16_bf16 v[218:221], v[164:165], v[96:97], v[218:221]\n"
      "v_cvt_pk_bf16_f32 v131, v158, v159\n"
      "v_exp_f32_e64 v149, v149\n"
      "v_mfma_f32_16x16x16_bf16 v[154:157], v[124:125], v[96:97], v[154:157]\n"
      "v_cndmask_b32_e64 v126, 0, v190, s[80:81]\n"
      "v_exp_f32_e64 v105, v105\n"
      "v_cvt_pk_bf16_f32 v88, v148, v149\n"
      "v_cvt_pk_bf16_f32 v162, v102, v103\n"
      "v_mfma_f32_16x16x32_bf16 v[110:113], v[126:129], a[216:219], v[174:177]\n"
      "s_cmp_gt_u32 s42, s44\n"
      "s_cselect_b32 s95, s94, s95\n"
      "v_cvt_pk_bf16_f32 v214, v120, v121\n"
      "v_cvt_pk_bf16_f32 v163, v104, v105\n"
      "v_mfma_f32_16x16x32_bf16 v[220:223], v[136:139], v[208:211], v[28:31]\n"
      "s_cbranch_vccnz L_rare_5_%=\n"
      "L_back_6_%=:\n"
      "v_mfma_f32_16x16x32_bf16 v[116:119], v[126:129], a[240:243], v[174:177]\n"
      "s_waitcnt lgkmcnt(0)\n"
      "v_or3_b32 v202, v202, v203, v204\n"
      "v_cvt_pk_bf16_f32 v215, v122, v123\n"
      "v_cvt_pk_bf16_f32 v112, v112, v113\n"
      "v_mfma_f32_16x16x32_bf16 v[120:123], v[126:129], v[232:235], v[174:177]\n"
      "v_exp_f32_e64 v171, v171\n"
      "v_mfma_f32_16x16x32_bf16 v[174:177], v[32:35], v[160:163], 0\n"
      "v_cvt_pk_bf16_f32 v110, v110, v111\n"
      "v_exp_f32_e64 v200, v200\n"
      "v_bitop3_b32 v202, v202, v205, s33 bitop3:0xa8\n"
      "v_mfma_f32_16x16x32_bf16 v[174:177], v[32:35], v[88:91], v[174:177]\n"
      "v_exp_f32 v198, v198\n"
      "v_mfma_f32_16x16x16_bf16 v[220:223], v[164:165], v[188:189], v[220:223]\n"
      "v_exp_f32 v199, v199\n"
      "v_mov_b32_e64 v185, 0\n"
      "v_cvt_pk_bf16_f32 v116, v116, v117\n"
      "v_exp_f32_e64 v201, v201\n"
      "v_mfma_f32_16x16x32_bf16 v[204:207], v[184:187], v[232:235], v[166:169]\n"
      "v_cvt_pk_bf16_f32 v93, v200, v201\n"
      "v_cvt_pk_bf16_f32 v118, v118, v119\n"
      "v_mfma_f32_16x16x32_bf16 v[176:179], v[184:187], a[216:219], v[166:169]\n"
      "v_accvgpr_write_b32 a217, v112\n"
      "v_cvt_pk_bf16_f32 v232, v120, v121\n"
      "v_mfma_f32_16x16x32_bf16 v[98:101], v[184:187], a[240:243], v[166:169]\n"
      "ds_read_u16 v86, v11 offset:64\n"
      "v_accvgpr_write_b32 a240, v116\n"
      "v_cvt_pk_bf16_f32 v233, v122, v123\n"
      "v_accvgpr_write_b32 a216, v110\n"
      "v_mfma_f32_16x16x32_bf16 v[166:169], v[106:109], v[208:211], v[72:75]\n"
      "v_cvt_pk_bf16_f32 v235, v206, v207\n"
      "v_cvt_pk_bf16_f32 v178, v178, v179\n"
      "v_accvgpr_write_b32 a219, v178\n"
      "v_mfma_f32_16x16x32_bf16 v[110:113], v[144:147], v[208:211], v[64:67]\n"
      "ds_read_b64 v[116:117], v10 offset:16\n"
      "v_cvt_pk_bf16_f32 v92, v198, v199\n"
      "s_waitcnt vmcnt(1)\n"
      "v_or3_b32 v140, v140, v141, v142\n"
      "v_exp_f32 v155, v155\n"
      "v_mfma_f32_16x16x32_bf16 v[120:123], v[180:183], v[208:211], v[68:71]\n"
      "v_cvt_pk_bf16_f32 v176, v176, v177\n"
      "v_cvt_pk_bf16_f32 v100, v100, v101\n"
      "v_cvt_pk_bf16_f32 v234, v204, v205\n"
      "v_mfma_f32_16x16x32_bf16 v[208:211], v[194:197], v[160:163], 0\n"
      "ds_read_b128 v[204:207], v10\n"
      "v_cmp_ne_u32 s[56:57], 0, v202\n"
      "v_mfma_f32_16x16x32_bf16 v[208:211], v[80:83], v[88:91], v[208:211]\n"
      "ds_read_u16 v78, v11\n"
      "v_cvt_pk_bf16_f32 v98, v98, v99\n"
      "v_mfma_f32_16x16x16_bf16 v[110:113], v[192:193], v[188:189], v[110:113]\n"
      "v_exp_f32 v157, v157\n"
      "v_exp_f32 v170, v170\n"
      "v_mfma_f32_16x16x16_bf16 v[166:169], v[212:213], v[188:189], v[166:169]\n"
      "v_exp_f32 v173, v173\n"
      "v_accvgpr_write_b32 a243, v100\n"
      "v_cvt_pk_bf16_f32 v170, v170, v171\n"
      "v_mfma_f32_16x16x16_bf16 v[120:123], v[124:125], v[188:189], v[120:123]\n"
      "s_waitcnt vmcnt(0)\n"
      "v_or3_b32 v140, v140, v143, v135\n"
      "v_exp_f32 v172, v172\n"
      "v_mfma_f32_16x16x32_bf16 v[188:191], v[214:217], v[160:163], 0\n"
      "s_waitcnt lgkmcnt(2)\n"
      "v_rcp_f32_e64 v174, v174\n"
      "v_mfma_f32_16x16x32_bf16 v[188:191], v[84:87], v[88:91], v[188:191]\n"
      "s_waitcnt lgkmcnt(1)\n"
      "v_exp_f32_e64 v154, v154\n"
      "v_mfma_f32_16x16x32_bf16 v[200:203], v[204:207], v[106:109], v[30:33]\n"
      "s_waitcnt lgkmcnt(0)\n"
      "v_cvt_pk_bf16_f32 v171, v172, v173\n"
      "v_exp_f32 v156, v156\n"
      "v_mfma_f32_16x16x32_bf16 v[106:109], v[130:133], v[160:163], 0\n"
      "ds_write_b32 v31, v140 offset:4608\n"
      "v_mfma_f32_16x16x32_bf16 v[106:109], v[76:79], v[88:91], v[106:109]\n"
      "s_cbranch_vccnz L_rare_7_%=\n"
      "L_back_8_%=:\n"
      "v_mfma_f32_16x16x32_bf16 v[160:163], v[204:207], v[136:139], v[30:33]\n"
      "ds_read_b128 v[140:143], v8 offset:128\n"
      "v_cvt_pk_bf16_f32 v173, v156, v157\n"
      "v_mfma_f32_16x16x32_bf16 v[156:159], v[204:207], v[180:183], v[30:33]\n"
      "s_add_u32 s42, s42, 1\n"
      "v_exp_f32 v113, v113\n"
      "v_exp_f32_e64 v218, v218\n"
      "v_mfma_f32_16x16x32_bf16 v[100:103], v[204:207], v[144:147], v[30:33]\n"
      "ds_read_b128 v[144:147], v8 offset:192\n"
      "v_cvt_pk_bf16_f32 v94, v218, 0\n"
      "v_mfma_f32_16x16x16_bf16 v[156:159], v[116:117], v[124:125], v[156:159]\n"
      "v_exp_f32 v122, v122\n"
      "v_exp_f32 v166, v166\n"
      "v_cvt_pk_bf16_f32 v172, v154, v155\n"
      "v_exp_f32_e64 v111, v111\n"
      "v_pk_mul_f32 v[106:107], v[106:107], v[174:175] op_sel_hi:[1,0]\n"
      "v_pk_mul_f32 v[208:209], v[208:209], v[174:175] op_sel_hi:[1,0]\n"
      "v_pk_mul_f32 v[108:109], v[108:109], v[174:175] op_sel_hi:[1,0]\n"
      "v_pk_mul_f32 v[190:191], v[190:191], v[174:175] op_sel_hi:[1,0]\n"
      "v_pk_mul_f32 v[210:211], v[210:211], v[174:175] op_sel_hi:[1,0]\n"
      "v_pk_mul_f32 v[188:189], v[188:189], v[174:175] op_sel_hi:[1,0]\n"
      "v_mfma_f32_16x16x16_bf16 v[100:103], v[116:117], v[192:193], v[100:103]\n"
      "v_accvgpr_write_b32 a242, v98\n"
      "v_exp_f32 v110, v110\n"
      "v_mfma_f32_16x16x16_bf16 v[160:163], v[116:117], v[164:165], v[160:163]\n"
      "v_exp_f32 v220, v220\n"
      "v_cvt_pk_bf16_f32 v188, v188, v189\n"
      "v_mfma_f32_16x16x32_bf16 v[178:181], v[32:35], v[170:173], 0\n"
      "s_waitcnt lgkmcnt(0)\n"
      "v_exp_f32 v167, v167\n"
      "v_mfma_f32_16x16x32_bf16 v[178:181], v[32:35], v[92:95], v[178:181]\n"
      "ds_read_b128 v[150:153], v8 offset:384\n"
      "v_cvt_pk_bf16_f32 v88, v166, v167\n"
      "v_mfma_f32_16x16x32_bf16 v[164:167], v[184:187], a[220:223], v[144:147]\n"
      "v_exp_f32 v120, v120\n"
      "v_mfma_f32_16x16x32_bf16 v[96:99], v[126:129], v[236:239], v[140:143]\n"
      "v_exp_f32 v123, v123\n"
      "v_cvt_pk_bf16_f32 v106, v106, v107\n"
      "v_cvt_pk_bf16_f32 v110, v110, v111\n"
      "v_mfma_f32_16x16x32_bf16 v[102:105], v[184:187], v[236:239], v[144:147]\n"
      "ds_read_b128 v[136:139], v8 offset:320\n"
      "v_cndmask_b32 v100, v100, v156, s[58:59]\n"
      "v_mfma_f32_16x16x32_bf16 v[154:157], v[126:129], a[244:247], v[140:143]\n"
      "v_cvt_pk_bf16_f32 v164, v164, v165\n"
      "v_exp_f32_e64 v112, v112\n"
      "v_mfma_f32_16x16x16_bf16 v[200:203], v[116:117], v[212:213], v[200:203]\n"
      "v_cvt_pk_bf16_f32 v111, v112, v113\n"
      "v_cvt_pk_bf16_f32 v189, v190, v191\n"
      "v_exp_f32_e64 v121, v121\n"
      "v_mfma_f32_16x16x32_bf16 v[190:193], v[130:133], v[170:173], 0\n"
      "ds_read_b128 v[114:117], v8 offset:448\n"
      "v_cvt_pk_bf16_f32 v107, v108, v109\n"
      "v_mfma_f32_16x16x32_bf16 v[190:193], v[76:79], v[92:95], v[190:193]\n"
      "ds_write_b64 v4, v[188:189]\n"
      "v_cvt_pk_bf16_f32 v113, v122, v123\n"
      "v_mfma_f32_16x16x32_bf16 v[122:125], v[184:187], a[244:247], v[144:147]\n"
      "s_nop 2\n"
      "ds_read_b128 v[144:147], v8 offset:256\n"
      "v_cvt_pk_bf16_f32 v112, v120, v121\n"
      "v_mfma_f32_16x16x32_bf16 v[180:183], v[194:197], v[170:173], 0\n"
      "v_exp_f32 v169, v169\n"
      "v_cvt_pk_bf16_f32 v109, v210, v211\n"
      "v_cvt_pk_bf16_f32 v166, v166, v167\n"
      "v_mfma_f32_16x16x32_bf16 v[180:183], v[80:83], v[92:95], v[180:183]\n"
      "v_rcp_f32_e64 v178, v178\n"
      "v_cndmask_b32 v100, v100, v200, s[60:61]\n"
      "v_mfma_f32_16x16x32_bf16 v[210:213], v[214:217], v[170:173], 0\n"
      "v_accvgpr_write_b32 a218, v176\n"
      "v_cvt_pk_bf16_f32 v108, v208, v209\n"
      "v_cvt_pk_bf16_f32 v90, v220, 0\n"
      "v_mfma_f32_16x16x32_bf16 v[210:213], v[84:87], v[92:95], v[210:213]\n"
      "v_exp_f32_e64 v168, v168\n"
      "v_cndmask_b32 v100, v100, v160, s[62:63]\n"
      "v_cvt_pk_bf16_f32 v154, v154, v155\n"
      "v_cvt_pk_bf16_f32 v89, v168, v169\n"
      "v_pk_mul_f32 v[190:191], v[190:191], v[178:179] op_sel_hi:[1,0]\n"
      "v_mfma_f32_16x16x32_bf16 v[174:177], v[126:129], a[220:223], v[140:143]\n"
      "s_waitcnt lgkmcnt(0)\n"
      "v_cvt_pk_bf16_f32 v124, v124, v125\n"
      "v_accvgpr_write_b32 a244, v154\n"
      "v_mfma_f32_16x16x32_bf16 v[140:143], v[126:129], a[252:255], v[150:153]\n"
      "s_cbranch_vccnz L_rare_9_%=\n"
      "L_back_10_%=:\n"
      "v_mfma_f32_16x16x32_bf16 v[160:163], v[126:129], v[240:243], v[144:147]\n"
      "v_cvt_pk_bf16_f32 v239, v104, v105\n"
      "v_cvt_pk_bf16_f32 v156, v156, v157\n"
      "v_mfma_f32_16x16x32_bf16 v[206:209], v[184:187], a[248:251], v[136:139]\n"
      "v_cvt_pk_bf16_f32 v237, v98, v99\n"
      "v_accvgpr_write_b32 a222, v164\n"
      "v_cvt_pk_bf16_f32 v174, v174, v175\n"
      "v_mfma_f32_16x16x32_bf16 v[218:221], v[184:187], v[240:243], v[136:139]\n"
      "ds_read_b128 v[202:205], v8 offset:576\n"
      "v_cvt_pk_bf16_f32 v238, v102, v103\n"
      "v_mfma_f32_16x16x32_bf16 v[168:171], v[184:187], a[224:227], v[136:139]\n"
      "ds_write_b128 v3, v[106:109]\n"
      "v_pk_mul_f32 v[210:211], v[210:211], v[178:179] op_sel_hi:[1,0]\n"
      "v_pk_mul_f32 v[180:181], v[180:181], v[178:179] op_sel_hi:[1,0]\n"
      "v_pk_mul_f32 v[182:183], v[182:183], v[178:179] op_sel_hi:[1,0]\n"
      "v_accvgpr_write_b32 a245, v156\n"
      "v_exp_f32_e64 v100, v100\n"
      "v_pk_mul_f32 v[192:193], v[192:193], v[178:179] op_sel_hi:[1,0]\n"
      "v_pk_mul_f32 v[212:213], v[212:213], v[178:179] op_sel_hi:[1,0]\n"
      "v_mfma_f32_16x16x32_bf16 v[106:109], v[126:129], a[248:251], v[144:147]\n"
      "v_cvt_pk_bf16_f32 v236, v96, v97\n"
      "v_cvt_pk_bf16_f32 v140, v140, v141\n"
      "v_mfma_f32_16x16x32_bf16 v[198:201], v[194:197], v[110:113], 0\n"
      "v_cvt_pk_bf16_f32 v176, v176, v177\n"
      "v_cvt_pk_bf16_f32 v100, v100, v100\n"
      "v_mfma_f32_16x16x32_bf16 v[198:201], v[80:83], v[88:91], v[198:201]\n"
      "v_cvt_pk_bf16_f32 v241, v162, v163\n"
      "v_cvt_pk_bf16_f32 v210, v210, v211\n"
      "v_mfma_f32_16x16x32_bf16 v[134:137], v[126:129], a[224:227], v[144:147]\n"
      "v_cvt_pk_bf16_f32 v240, v160, v161\n"
      "v_cvt_pk_bf16_f32 v243, v220, v221\n"
      "v_cvt_pk_bf16_f32 v122, v122, v123\n"
      "v_mfma_f32_16x16x32_bf16 v[144:147], v[184:187], a[252:255], v[114:117]\n"
      "ds_write_b16 v13, v100\n"
      "v_mfma_f32_16x16x32_bf16 v[158:161], v[184:187], v[244:247], v[114:117]\n"
      "v_cvt_pk_bf16_f32 v211, v212, v213\n"
      "v_accvgpr_write_b32 a221, v176\n"
      "v_accvgpr_write_b32 a247, v124\n"
      "v_cvt_pk_bf16_f32 v170, v170, v171\n"
      "v_cvt_pk_bf16_f32 v168, v168, v169\n"
      "v_mfma_f32_16x16x32_bf16 v[162:165], v[214:217], v[110:113], 0\n"
      "s_and_b32 s86, s86, -4\n"
      "v_accvgpr_write_b32 a252, v140\n"
      "v_cvt_pk_bf16_f32 v142, v142, v143\n"
      "v_mfma_f32_16x16x32_bf16 v[162:165], v[84:87], v[88:91], v[162:165]\n"
      "ds_read_b128 v[138:141], v8 offset:512\n"
      "v_accvgpr_write_b32 a246, v122\n"
      "v_mfma_f32_16x16x32_bf16 v[122:125], v[184:187], a[228:231], v[114:117]\n"
      "v_cvt_pk_bf16_f32 v106, v106, v107\n"
      "v_cvt_pk_bf16_f32 v146, v146, v147\n"
      "v_mfma_f32_16x16x32_bf16 v[176:179], v[32:35], v[110:113], 0\n"
      "ds_read_b128 v[98:101], v8 offset:704\n"
      "v_cvt_pk_bf16_f32 v208, v208, v209\n"
      "v_mfma_f32_16x16x32_bf16 v[176:179], v[32:35], v[88:91], v[176:179]\n"
      "v_accvgpr_write_b32 a227, v170\n"
      "v_cvt_pk_bf16_f32 v108, v108, v109\n"
      "v_accvgpr_write_b32 a223, v166\n"
      "v_mfma_f32_16x16x32_bf16 v[114:117], v[126:129], v[244:247], v[150:153]\n"
      "s_waitcnt lgkmcnt(2)\n"
      "v_cvt_pk_bf16_f32 v242, v218, v219\n"
      "v_cvt_pk_bf16_f32 v190, v190, v191\n"
      "v_cvt_pk_bf16_f32 v246, v158, v159\n"
      "v_accvgpr_write_b32 a251, v208\n"
      "v_accvgpr_write_b32 a220, v174\n"
      "v_cvt_pk_bf16_f32 v144, v144, v145\n"
      "v_mfma_f32_16x16x32_bf16 v[102:105], v[126:129], a[228:231], v[150:153]\n"
      "ds_read_b128 v[170:173], v8 offset:640\n"
      "v_rcp_f32 v176, v176\n"
      "v_mfma_f32_16x16x32_bf16 v[154:157], v[184:187], a[232:235], v[202:205]\n"
      "v_cvt_pk_bf16_f32 v122, v122, v123\n"
      "v_cvt_pk_bf16_f32 v206, v206, v207\n"
      "v_mfma_f32_16x16x32_bf16 v[150:153], v[184:187], v[224:227], v[202:205]\n"
      "ds_write_b64 v4, v[210:211] offset:256\n"
      "v_mfma_f32_16x16x32_bf16 v[220:223], v[130:133], v[110:113], 0\n"
      "s_waitcnt lgkmcnt(2)\n"
      "v_cvt_pk_bf16_f32 v191, v192, v193\n"
      "v_cvt_pk_bf16_f32 v104, v104, v105\n"
      "v_cvt_pk_bf16_f32 v244, v114, v115\n"
      "v_cvt_pk_bf16_f32 v102, v102, v103\n"
      "v_mfma_f32_16x16x32_bf16 v[220:223], v[76:79], v[88:91], v[220:223]\n"
      "s_add_u32 s43, s43, 1\n"
      "v_cvt_pk_bf16_f32 v124, v124, v125\n"
      "v_cvt_pk_bf16_f32 v156, v156, v157\n"
      "v_mfma_f32_16x16x32_bf16 v[110:113], v[184:187], a[236:239], v[98:101]\n"
      "s_waitcnt lgkmcnt(1)\n"
      "v_accvgpr_write_b32 a249, v108\n"
      "v_cvt_pk_bf16_f32 v192, v180, v181\n"
      "v_accvgpr_write_b32 a241, v118\n"
      "v_mfma_f32_16x16x32_bf16 v[118:121], v[126:129], a[232:235], v[138:141]\n"
      "ds_read_b128 v[210:213], v14 offset:64\n"
      "v_accvgpr_write_b32 a230, v122\n"
      "v_accvgpr_write_b32 a248, v106\n"
      "v_accvgpr_write_b32 a226, v168\n"
      "v_cvt_pk_bf16_f32 v245, v116, v117\n"
      "v_pk_mul_f32 v[162:163], v[162:163], v[176:177] op_sel_hi:[1,0]\n"
      "v_pk_mul_f32 v[222:223], v[222:223], v[176:177] op_sel_hi:[1,0]\n"
      "v_cvt_pk_bf16_f32 v193, v182, v183\n"
      "v_pk_mul_f32 v[200:201], v[200:201], v[176:177] op_sel_hi:[1,0]\n"
      "v_pk_mul_f32 v[164:165], v[164:165], v[176:177] op_sel_hi:[1,0]\n"
      "v_cvt_pk_bf16_f32 v247, v160, v161\n"
      "v_pk_mul_f32 v[198:199], v[198:199], v[176:177] op_sel_hi:[1,0]\n"
      "v_mfma_f32_16x16x32_bf16 v[180:183], v[126:129], a[236:239], v[170:173]\n"
      "v_cvt_pk_bf16_f32 v136, v136, v137\n"
      "v_cvt_pk_bf16_f32 v112, v112, v113\n"
      "v_cvt_pk_bf16_f32 v134, v134, v135\n"
      "v_cvt_pk_bf16_f32 v110, v110, v111\n"
      "v_mfma_f32_16x16x32_bf16 v[166:169], v[126:129], v[224:227], v[138:141]\n"
      "ds_write_b128 v3, v[190:193] offset:256\n"
      "v_cvt_pk_bf16_f32 v226, v150, v151\n"
      "v_accvgpr_write_b32 a231, v124\n"
      "v_pk_mul_f32 v[220:221], v[220:221], v[176:177] op_sel_hi:[1,0]\n"
      "v_mfma_f32_16x16x32_bf16 v[122:125], v[184:187], v[228:231], v[98:101]\n"
      "ds_read_b128 v[174:177], v14\n"
      "v_cvt_pk_bf16_f32 v162, v162, v163\n"
      "v_cvt_pk_bf16_f32 v120, v120, v121\n"
      "v_accvgpr_write_b32 a254, v144\n"
      "v_mfma_f32_16x16x32_bf16 v[190:193], v[126:129], v[248:251], v[138:141]\n"
      "v_cvt_pk_bf16_f32 v220, v220, v221\n"
      "v_cvt_pk_bf16_f32 v163, v164, v165\n"
      "v_accvgpr_write_b32 a225, v136\n"
      "v_mfma_f32_16x16x32_bf16 v[138:141], v[184:187], v[252:255], v[98:101]\n"
      "s_cmp_ge_u32 s95, 0x40000000\n"
      "s_cselect_b64 vcc, -1, s[56:57]\n"
      "v_cvt_pk_bf16_f32 v154, v154, v155\n"
      "v_cvt_pk_bf16_f32 v118, v118, v119\n"
      "v_accvgpr_write_b32 a235, v156\n"
      "v_accvgpr_write_b32 a224, v134\n"
      "v_mfma_f32_16x16x32_bf16 v[158:161], v[126:129], v[252:255], v[170:173]\n"
      "ds_write_b64 v4, v[162:163] offset:512\n"
      "v_cvt_pk_bf16_f32 v225, v168, v169\n"
      "v_accvgpr_write_b32 a250, v206\n"
      "v_cvt_pk_bf16_f32 v221, v222, v223\n"
      "v_mfma_f32_16x16x32_bf16 v[206:209], v[126:129], v[228:231], v[170:173]\n"
      "s_waitcnt lgkmcnt(1)\n"
      "v_cvt_pk_bf16_f32 v180, v180, v181\n"
      "v_cvt_pk_bf16_f32 v223, v200, v201\n"
      "v_accvgpr_write_b32 a233, v120\n"
      "v_cvt_pk_bf16_f32 v254, v138, v139\n"
      "v_cvt_pk_bf16_f32 v222, v198, v199\n"
      "v_mfma_f32_16x16x32_bf16 v[162:165], v[184:187], v[248:251], v[202:205]\n"
      "ds_write_b128 v3, v[220:223] offset:512\n"
      "v_accvgpr_write_b32 a239, v112\n"
      "v_cvt_pk_bf16_f32 v227, v152, v153\n"
      "v_accvgpr_write_b32 a236, v180\n"
      "v_accvgpr_write_b32 a253, v142\n"
      "v_cvt_pk_bf16_f32 v224, v166, v167\n"
      "v_accvgpr_write_b32 a238, v110\n"
      "v_accvgpr_write_b32 a232, v118\n"
      "v_cvt_pk_bf16_f32 v250, v162, v163\n"
      "v_accvgpr_write_b32 a255, v146\n"
      "v_cvt_pk_bf16_f32 v182, v182, v183\n"
      "v_cvt_pk_bf16_f32 v249, v192, v193\n"
      "v_cvt_pk_bf16_f32 v228, v206, v207\n"
      "v_cvt_pk_bf16_f32 v248, v190, v191\n"
      "v_cvt_pk_bf16_f32 v255, v140, v141\n"
      "v_cvt_pk_bf16_f32 v253, v160, v161\n"
      "v_accvgpr_write_b32 a229, v104\n"
      "v_cvt_pk_bf16_f32 v251, v164, v165\n"
      "v_accvgpr_write_b32 a237, v182\n"
      "v_cvt_pk_bf16_f32 v231, v124, v125\n"
      "v_accvgpr_write_b32 a234, v154\n"
      "v_accvgpr_write_b32 a228, v102\n"
      "v_mfma_f32_16x16x32_bf16 v[188:191], v[174:177], v[32:35], 0\n"
      "v_cvt_pk_bf16_f32 v230, v122, v123\n"
      "v_cvt_pk_bf16_f32 v252, v158, v159\n"
      "v_cvt_pk_bf16_f32 v229, v208, v209\n"
      "v_mfma_f32_16x16x32_bf16 v[188:191], v[210:213], v[32:35], v[188:191]\n"
      "s_waitcnt lgkmcnt(0)\n"
      "s_barrier\n"
      "s_or_b64 vcc, vcc, s[54:55]\n"
      "s_cbranch_vccnz L_disp1_%=\n"
      "s_nop 7\n"
      "s_nop 7\n"
      "L_loop1_%=:\n"
      "v_mfma_f32_16x16x32_bf16 v[218:221], v[174:177], v[130:133], 0\n"
      "ds_read_b128 v[206:209], v0 offset:18432\n"
      "v_readlane_b32 s45, v37, s43\n"
      "v_mfma_f32_16x16x32_bf16 v[218:221], v[210:213], v[76:79], v[218:221]\n"
      "ds_read_b128 v[122:125], v9\n"
      "ds_read_b128 v[146:149], v1 offset:30720\n"
      "v_mfma_f32_16x16x32_bf16 v[134:137], v[174:177], v[194:197], 0\n"
      "s_add_u32 m0, s64, 0\n"
      "ds_read_b128 v[150:153], v1 offset:18432\n"
      "v_mfma_f32_16x16x32_bf16 v[134:137], v[210:213], v[80:83], v[134:137]\n"
      "buffer_load_dwordx4 v16, s[16:19], s84 offen  lds\n"
      "v_mfma_f32_16x16x32_bf16 v[100:103], v[174:177], v[214:217], 0\n"
      "ds_read_b128 v[184:187], v9 offset:4608\n"
      "v_mfma_f32_16x16x32_bf16 v[100:103], v[210:213], v[84:87], v[100:103]\n"
      "ds_read_b128 v[202:205], v1 offset:24576\n"
      "s_waitcnt lgkmcnt(5)\n"
      "v_mfma_f32_16x16x32_bf16 v[154:157], a[0:3], v[206:209], 0\n"
      "ds_read_b128 v[176:179], v0 offset:24576\n"
      "s_add_u32 m0, s64, 4096\n"
      "v_mfma_f32_16x16x32_bf16 v[190:193], a[24:27], v[206:209], 0\n"
      "s_waitcnt lgkmcnt(5)\n"
      "v_cndmask_b32 v218, v218, v134, s[58:59]\n"
      "v_add_f32_e64 v122, v122, v123\n"
      "v_mfma_f32_16x16x32_bf16 v[96:99], a[48:51], v[206:209], 0\n"
      "buffer_load_dwordx4 v17, s[16:19], s84 offen  lds\n"
      "v_mfma_f32_16x16x32_bf16 v[112:115], a[72:75], v[206:209], 0\n"
      "ds_read_b128 v[210:213], v0 offset:34816\n"
      "v_mov_b32_dpp v188, v188 quad_perm:[0,0,0,0] row_mask:0xf bank_mask:0xf\n"
      "v_mfma_f32_16x16x32_bf16 v[220:223], a[96:99], v[206:209], 0\n"
      "s_waitcnt lgkmcnt(3)\n"
      "v_or3_b32 v184, v184, v185, v186\n"
      "v_add_f32 v124, v124, v125\n"
      "v_mfma_f32_16x16x32_bf16 v[102:105], a[120:123], v[206:209], 0\n"
      "s_add_u32 m0, s64, 8192\n"
      "v_readlane_b32 s47, v38, s43\n"
      "v_add_f32 v122, v122, v124\n"
      "v_mfma_f32_16x16x32_bf16 v[154:157], a[4:7], v[150:153], v[154:157]\n"
      "s_waitcnt lgkmcnt(1)\n"
      "v_rsq_f32_e64 v122, v122\n"
      "v_mfma_f32_16x16x32_bf16 v[154:157], a[8:11], v[176:179], v[154:157]\n"
      "v_mul_f32 v122, 0x415db3d7, v122\n"
      "v_mfma_f32_16x16x32_bf16 v[190:193], a[28:31], v[150:153], v[190:193]\n"
      "v_rcp_f32_e64 v188, v188\n"
      "v_mfma_f32_16x16x32_bf16 v[190:193], a[32:35], v[176:179], v[190:193]\n"
      "buffer_load_dwordx4 v18, s[16:19], s84 offen  lds\n"
      "v_mfma_f32_16x16x32_bf16 v[96:99], a[52:55], v[150:153], v[96:99]\n"
      "v_mov_b32_e64 v124, v122\n"
      "v_bitop3_b32 v184, v184, v187, s33 bitop3:0xa8\n"
      "v_mfma_f32_16x16x32_bf16 v[96:99], a[56:59], v[176:179], v[96:99]\n"
      "s_add_u32 m0, s64, 12288\n"
      "v_permlane32_swap_b32_e64 v122, v124\n"
      "v_mfma_f32_16x16x32_bf16 v[112:115], a[76:79], v[150:153], v[112:115]\n"
      "ds_read_b128 v[138:141], v0 offset:30720\n"
      "v_mov_b32 v123, v122\n"
      "v_mfma_f32_16x16x32_bf16 v[112:115], a[80:83], v[176:179], v[112:115]\n"
      "s_nop 0\n"
      "v_permlane16_swap_b32 v122, v123\n"
      "v_mfma_f32_16x16x32_bf16 v[220:223], a[100:103], v[150:153], v[220:223]\n"
      "ds_read_b128 v[158:161], v0 offset:32768\n"
      "v_mfma_f32_16x16x32_bf16 v[220:223], a[104:107], v[176:179], v[220:223]\n"
      "v_mul_f32 v118, v124, v118\n"
      "v_mul_f32 v142, v122, v142\n"
      "v_mfma_f32_16x16x32_bf16 v[102:105], a[124:127], v[150:153], v[102:105]\n"
      "v_readlane_b32 s46, v37, s42\n"
      "v_mul_f32 v171, v123, v171\n"
      "v_mfma_f32_16x16x32_bf16 v[102:105], a[128:131], v[176:179], v[102:105]\n"
      "v_mul_f32 v145, v122, v145\n"
      "v_cndmask_b32 v218, v218, v100, s[60:61]\n"
      "v_mfma_f32_16x16x32_bf16 v[154:157], a[12:15], v[202:205], v[154:157]\n"
      "s_waitcnt lgkmcnt(1)\n"
      "v_mul_f32 v162, v123, v162\n"
      "v_mul_f32 v129, v124, v129\n"
      "v_mfma_f32_16x16x32_bf16 v[154:157], a[16:19], v[138:141], v[154:157]\n"
      "buffer_load_dwordx4 v19, s[16:19], s84 offen  lds\n"
      "v_mfma_f32_16x16x32_bf16 v[154:157], a[20:23], v[146:149], v[154:157]\n"
      "v_mul_f32 v218, v188, v218\n"
      "v_mul_f32 v198, v122, v198\n"
      "v_mfma_f32_16x16x32_bf16 v[190:193], a[36:39], v[202:205], v[190:193]\n"
      "v_mul_f32 v163, v123, v163\n"
      "v_mul_f32 v165, v123, v165\n"
      "v_mfma_f32_16x16x32_bf16 v[190:193], a[40:43], v[138:141], v[190:193]\n"
      "s_add_u32 m0, s65, 16384\n"
      "v_cvt_pk_bf16_f32 v218, v218, v218\n"
      "v_mul_f32 v144, v122, v144\n"
      "v_mfma_f32_16x16x32_bf16 v[190:193], a[44:47], v[146:149], v[190:193]\n"
      "v_fma_f32 v77, v154, v154, s51\n"
      "v_mul_f32 v109, v124, v109\n"
      "v_mfma_f32_16x16x32_bf16 v[96:99], a[60:63], v[202:205], v[96:99]\n"
      "buffer_load_dwordx4 v15, s[16:19], s84 offen  lds\n"
      "v_mfma_f32_16x16x32_bf16 v[96:99], a[64:67], v[138:141], v[96:99]\n"
      "ds_write_b16 v12, v218\n"
      "v_mfma_f32_16x16x32_bf16 v[96:99], a[68:71], v[146:149], v[96:99]\n"
      "v_mul_f32 v166, v122, v166\n"
      "v_fmac_f32 v77, v155, v155\n"
      "v_mfma_f32_16x16x32_bf16 v[112:115], a[84:87], v[202:205], v[112:115]\n"
      "v_mul_f32 v143, v122, v143\n"
      "v_mul_f32 v182, v123, v182\n"
      "v_mfma_f32_16x16x32_bf16 v[112:115], a[88:91], v[138:141], v[112:115]\n"
      "v_mul_f32_e64 v164, v123, v164\n"
      "v_cvt_pk_bf16_f32 v142, v142, v143\n"
      "v_mfma_f32_16x16x32_bf16 v[112:115], a[92:95], v[146:149], v[112:115]\n"
      "buffer_load_dwordx4 v[134:137], v36, s[28:31], s86 offen\n"
      "v_mfma_f32_16x16x32_bf16 v[220:223], a[108:111], v[202:205], v[220:223]\n"
      "v_mul_f32 v167, v122, v167\n"
      "v_mul_f32 v110, v124, v110\n"
      "v_mfma_f32_16x16x32_bf16 v[220:223], a[112:115], v[138:141], v[220:223]\n"
      "v_mul_f32 v201, v122, v201\n"
      "v_mul_f32 v181, v123, v181\n"
      "v_mfma_f32_16x16x32_bf16 v[220:223], a[116:119], v[146:149], v[220:223]\n"
      "v_fma_f32 v106, v112, v112, s51\n"
      "v_mul_f32_e64 v180, v123, v180\n"
      "v_mfma_f32_16x16x32_bf16 v[102:105], a[132:135], v[202:205], v[102:105]\n"
      "v_cvt_pk_bf16_f32 v162, v162, v163\n"
      "v_fmac_f32_e64 v106, v113, v113\n"
      "v_mfma_f32_16x16x32_bf16 v[102:105], a[136:139], v[138:141], v[102:105]\n"
      "v_cvt_pk_bf16_f32 v143, v144, v145\n"
      "v_mul_f32 v119, v124, v119\n"
      "v_mfma_f32_16x16x32_bf16 v[102:105], a[140:143], v[146:149], v[102:105]\n"
      "v_fmac_f32 v106, v114, v114\n"
      "v_mul_f32_e64 v169, v122, v169\n"
      "v_mfma_f32_16x16x32_bf16 v[130:133], v[206:209], a[144:147], 0\n"
      "v_fmac_f32 v77, v156, v156\n"
      "v_mul_f32 v183, v123, v183\n"
      "v_mfma_f32_16x16x32_bf16 v[130:133], v[150:153], a[148:151], v[130:133]\n"
      "v_fmac_f32 v77, v157, v157\n"
      "v_fmac_f32 v77, v190, v190\n"
      "v_mfma_f32_16x16x32_bf16 v[130:133], v[176:179], a[152:155], v[130:133]\n"
      "ds_read_b128 v[186:189], v1 offset:26624\n"
      "v_fmac_f32_e64 v106, v115, v115\n"
      "v_mfma_f32_16x16x32_bf16 v[130:133], v[202:205], a[156:159], v[130:133]\n"
      "buffer_load_dword v88, v29, s[28:31], s86 offen\n"
      "v_mfma_f32_16x16x32_bf16 v[130:133], v[138:141], a[160:163], v[130:133]\n"
      "v_mul_f32 v170, v123, v170\n"
      "v_fmac_f32 v77, v191, v191\n"
      "v_mfma_f32_16x16x32_bf16 v[130:133], v[146:149], a[164:167], v[130:133]\n"
      "v_cvt_pk_bf16_f32 v166, v166, v167\n"
      "v_mul_f32_e64 v172, v123, v172\n"
      "v_mfma_f32_16x16x32_bf16 v[194:197], v[206:209], a[168:171], 0\n"
      "ds_write_b64 v6, v[142:143] offset:18432\n"
      "v_mfma_f32_16x16x32_bf16 v[194:197], v[150:153], a[172:175], v[194:197]\n"
      "v_cvt_pk_bf16_f32 v163, v164, v165\n"
      "v_fmac_f32 v106, v220, v220\n"
      "v_mfma_f32_16x16x32_bf16 v[194:197], v[176:179], a[176:179], v[194:197]\n"
      "v_fmac_f32 v106, v221, v221\n"
      "v_cmp_ne_u32 s[56:57], 0, v184\n"
      "v_mfma_f32_16x16x32_bf16 v[194:197], v[202:205], a[180:183], v[194:197]\n"
      "ds_read_b128 v[142:145], v0 offset:20480\n"
      "v_cvt_pk_bf16_f32 v164, v180, v181\n"
      "v_mfma_f32_16x16x32_bf16 v[194:197], v[138:141], a[184:187], v[194:197]\n"
      "v_fmac_f32 v106, v222, v222\n"
      "v_mul_f32 v168, v122, v168\n"
      "v_mfma_f32_16x16x32_bf16 v[194:197], v[146:149], a[188:191], v[194:197]\n"
      "v_cvt_pk_bf16_f32 v165, v182, v183\n"
      "v_fmac_f32 v106, v223, v223\n"
      "v_mfma_f32_16x16x32_bf16 v[214:217], v[206:209], a[192:195], 0\n"
      "ds_read_b128 v[182:185], v1 offset:20480\n"
      "v_mul_f32 v126, v124, v126\n"
      "v_mfma_f32_16x16x32_bf16 v[214:217], v[150:153], a[196:199], v[214:217]\n"
      "ds_read_b128 v[206:209], v0 offset:26624\n"
      "v_mul_f32 v200, v122, v200\n"
      "v_mfma_f32_16x16x32_bf16 v[214:217], v[176:179], a[200:203], v[214:217]\n"
      "s_or_b32 s95, s45, s46\n"
      "v_mul_f32 v199, v122, v199\n"
      "v_mul_f32 v117, v124, v117\n"
      "v_mfma_f32_16x16x32_bf16 v[214:217], v[202:205], a[204:207], v[214:217]\n"
      "ds_read_b128 v[174:177], v1 offset:32768\n"
      "v_cvt_pk_bf16_f32 v194, v194, v195\n"
      "v_mfma_f32_16x16x32_bf16 v[214:217], v[138:141], a[208:211], v[214:217]\n"
      "v_fmac_f32 v106, v102, v102\n"
      "v_cvt_pk_bf16_f32 v130, v130, v131\n"
      "v_mfma_f32_16x16x32_bf16 v[214:217], v[146:149], a[212:215], v[214:217]\n"
      "s_waitcnt lgkmcnt(3)\n"
      "v_mul_f32 v111, v124, v111\n"
      "v_mul_f32 v116, v124, v116\n"
      "v_mfma_f32_16x16x32_bf16 v[138:141], a[0:3], v[142:145], 0\n"
      "s_waitcnt lgkmcnt(2)\n"
      "v_mul_f32 v108, v124, v108\n"
      "v_mul_f32 v127, v124, v127\n"
      "v_mfma_f32_16x16x32_bf16 v[138:141], a[4:7], v[182:185], v[138:141]\n"
      "s_waitcnt lgkmcnt(1)\n"
      "v_mul_f32 v128, v124, v128\n"
      "v_cvt_pk_bf16_f32 v167, v168, v169\n"
      "v_mfma_f32_16x16x32_bf16 v[138:141], a[8:11], v[206:209], v[138:141]\n"
      "v_cvt_pk_bf16_f32 v108, v108, v109\n"
      "v_fmac_f32 v77, v192, v192\n"
      "v_mfma_f32_16x16x32_bf16 v[138:141], a[12:15], v[186:189], v[138:141]\n"
      "v_mul_f32 v173, v123, v173\n"
      "v_cvt_pk_bf16_f32 v168, v198, v199\n"
      "v_mfma_f32_16x16x32_bf16 v[138:141], a[16:19], v[158:161], v[138:141]\n"
      "s_waitcnt lgkmcnt(0)\n"
      "v_fmac_f32_e64 v77, v193, v193\n"
      "v_cvt_pk_bf16_f32 v126, v126, v127\n"
      "v_mfma_f32_16x16x32_bf16 v[138:141], a[20:23], v[174:177], v[138:141]\n"
      "ds_write_b128 v5, v[162:165] offset:24576\n"
      "v_mfma_f32_16x16x32_bf16 v[150:153], a[24:27], v[142:145], 0\n"
      "v_cvt_pk_bf16_f32 v169, v200, v201\n"
      "v_fmac_f32_e64 v106, v103, v103\n"
      "v_mfma_f32_16x16x32_bf16 v[150:153], a[28:31], v[182:185], v[150:153]\n"
      "ds_read_b128 v[162:165], v1 offset:22528\n"
      "v_fmac_f32_e64 v77, v96, v96\n"
      "v_mfma_f32_16x16x32_bf16 v[150:153], a[32:35], v[206:209], v[150:153]\n"
      "ds_write_b128 v5, v[166:169] offset:18432\n"
      "v_mfma_f32_16x16x32_bf16 v[150:153], a[36:39], v[186:189], v[150:153]\n"
      "v_cvt_pk_bf16_f32 v195, v196, v197\n"
      "v_fmac_f32_e64 v77, v97, v97\n"
      "v_mfma_f32_16x16x32_bf16 v[150:153], a[40:43], v[158:161], v[150:153]\n"
      "v_cvt_pk_bf16_f32 v127, v128, v129\n"
      "v_fma_f32 v120, v138, v138, s51\n"
      "v_mfma_f32_16x16x32_bf16 v[150:153], a[44:47], v[174:177], v[150:153]\n"
      "v_fmac_f32 v77, v98, v98\n"
      "v_fmac_f32 v120, v139, v139\n"
      "v_mfma_f32_16x16x32_bf16 v[122:125], a[48:51], v[142:145], 0\n"
      "v_fmac_f32_e64 v120, v140, v140\n"
      "v_cvt_pk_bf16_f32 v129, v118, v119\n"
      "v_mfma_f32_16x16x32_bf16 v[122:125], a[52:55], v[182:185], v[122:125]\n"
      "ds_read_b128 v[146:149], v0 offset:28672\n"
      "v_cvt_pk_bf16_f32 v128, v116, v117\n"
      "v_mfma_f32_16x16x32_bf16 v[122:125], a[56:59], v[206:209], v[122:125]\n"
      "v_fmac_f32 v106, v104, v104\n"
      "v_fmac_f32 v106, v105, v105\n"
      "v_mfma_f32_16x16x32_bf16 v[122:125], a[60:63], v[186:189], v[122:125]\n"
      "ds_write_b128 v5, v[126:129] offset:30720\n"
      "v_mfma_f32_16x16x32_bf16 v[122:125], a[64:67], v[158:161], v[122:125]\n"
      "v_fmac_f32 v120, v141, v141\n"
      "v_fmac_f32 v77, v99, v99\n"
      "v_mfma_f32_16x16x32_bf16 v[122:125], a[68:71], v[174:177], v[122:125]\n"
      "s_and_b32 s47, s47, -4\n"
      "v_permlane16_swap_b32 v77, v106\n"
      "v_mfma_f32_16x16x32_bf16 v[126:129], a[72:75], v[142:145], 0\n"
      "v_add_f32 v77, v77, v106\n"
      "v_fmac_f32 v120, v150, v150\n"
      "v_mfma_f32_16x16x32_bf16 v[126:129], a[76:79], v[182:185], v[126:129]\n"
      "v_cvt_pk_bf16_f32 v214, v214, v215\n"
      "v_mov_b32 v106, v77\n"
      "v_mfma_f32_16x16x32_bf16 v[126:129], a[80:83], v[206:209], v[126:129]\n"
      "s_nop 0\n"
      "v_permlane32_swap_b32 v77, v106\n"
      "v_mfma_f32_16x16x32_bf16 v[126:129], a[84:87], v[186:189], v[126:129]\n"
      "v_cvt_pk_bf16_f32 v131, v132, v133\n"
      "v_add_f32 v77, v77, v106\n"
      "v_mfma_f32_16x16x32_bf16 v[126:129], a[88:91], v[158:161], v[126:129]\n"
      "v_rsq_f32 v77, v77\n"
      "v_mfma_f32_16x16x32_bf16 v[126:129], a[92:95], v[174:177], v[126:129]\n"
      "v_mov_b32 v106, v77\n"
      "v_fmac_f32 v120, v151, v151\n"
      "v_mfma_f32_16x16x32_bf16 v[178:181], a[96:99], v[142:145], 0\n"
      "v_permlane16_swap_b32 v77, v106\n"
      "v_mfma_f32_16x16x32_bf16 v[178:181], a[100:103], v[182:185], v[178:181]\n"
      "v_cvt_pk_bf16_f32 v170, v170, v171\n"
      "v_mul_f32_e64 v77, s50, v77\n"
      "v_mfma_f32_16x16x32_bf16 v[178:181], a[104:107], v[206:209], v[178:181]\n"
      "v_fma_f32 v107, v126, v126, s51\n"
      "v_fmac_f32 v107, v127, v127\n"
      "v_mfma_f32_16x16x32_bf16 v[178:181], a[108:111], v[186:189], v[178:181]\n"
      "v_cvt_pk_bf16_f32 v109, v110, v111\n"
      "v_mul_f32 v115, v106, v115\n"
      "v_mfma_f32_16x16x32_bf16 v[178:181], a[112:115], v[158:161], v[178:181]\n"
      "v_mul_f32 v190, v77, v190\n"
      "v_fmac_f32 v120, v152, v152\n"
      "v_mfma_f32_16x16x32_bf16 v[178:181], a[116:119], v[174:177], v[178:181]\n"
      "v_cvt_pk_bf16_f32 v171, v172, v173\n"
      "v_mul_f32 v154, v77, v154\n"
      "v_mfma_f32_16x16x32_bf16 v[116:119], a[120:123], v[142:145], 0\n"
      "v_fmac_f32 v107, v128, v128\n"
      "v_fmac_f32 v107, v129, v129\n"
      "v_mfma_f32_16x16x32_bf16 v[116:119], a[124:127], v[182:185], v[116:119]\n"
      "v_mul_f32 v97, v77, v97\n"
      "v_cvt_pk_bf16_f32 v215, v216, v217\n"
      "v_mfma_f32_16x16x32_bf16 v[116:119], a[128:131], v[206:209], v[116:119]\n"
      "ds_write_b64 v6, v[108:109] offset:30720\n"
      "v_mfma_f32_16x16x32_bf16 v[116:119], a[132:135], v[186:189], v[116:119]\n"
      "v_fmac_f32 v107, v178, v178\n"
      "v_fmac_f32 v120, v153, v153\n"
      "v_mfma_f32_16x16x32_bf16 v[116:119], a[136:139], v[158:161], v[116:119]\n"
      "ds_read_b128 v[198:201], v1 offset:28672\n"
      "v_mul_f32_e64 v113, v106, v113\n"
      "v_mfma_f32_16x16x32_bf16 v[116:119], a[140:143], v[174:177], v[116:119]\n"
      "v_fmac_f32 v107, v179, v179\n"
      "v_mul_f32 v193, v77, v193\n"
      "v_mfma_f32_16x16x32_bf16 v[216:219], v[142:145], a[144:147], 0\n"
      "ds_write_b64 v6, v[170:171] offset:24576\n"
      "v_mfma_f32_16x16x32_bf16 v[216:219], v[182:185], a[148:151], v[216:219]\n"
      "v_mul_f32 v112, v106, v112\n"
      "v_fmac_f32 v107, v180, v180\n"
      "v_mfma_f32_16x16x32_bf16 v[216:219], v[206:209], a[152:155], v[216:219]\n"
      "v_mul_f32 v98, v77, v98\n"
      "v_fmac_f32 v107, v181, v181\n"
      "v_mfma_f32_16x16x32_bf16 v[216:219], v[186:189], a[156:159], v[216:219]\n"
      "v_fmac_f32 v120, v122, v122\n"
      "v_fmac_f32 v120, v123, v123\n"
      "v_mfma_f32_16x16x32_bf16 v[216:219], v[158:161], a[160:163], v[216:219]\n"
      "v_fmac_f32 v107, v116, v116\n"
      "v_fmac_f32 v107, v117, v117\n"
      "v_mfma_f32_16x16x32_bf16 v[216:219], v[174:177], a[164:167], v[216:219]\n"
      "v_fmac_f32 v107, v118, v118\n"
      "v_fmac_f32 v120, v124, v124\n"
      "v_mfma_f32_16x16x32_bf16 v[170:173], v[142:145], a[168:171], 0\n"
      "v_fmac_f32 v120, v125, v125\n"
      "v_fmac_f32 v107, v119, v119\n"
      "v_mfma_f32_16x16x32_bf16 v[170:173], v[182:185], a[172:175], v[170:173]\n"
      "s_or_b32 s95, s95, s48\n"
      "v_mul_f32 v156, v77, v156\n"
      "v_mul_f32 v155, v77, v155\n"
      "v_mfma_f32_16x16x32_bf16 v[170:173], v[206:209], a[176:179], v[170:173]\n"
      "v_mul_f32 v103, v106, v103\n"
      "v_mul_f32 v105, v106, v105\n"
      "v_mfma_f32_16x16x32_bf16 v[170:173], v[186:189], a[180:183], v[170:173]\n"
      "v_permlane16_swap_b32 v120, v107\n"
      "v_mfma_f32_16x16x32_bf16 v[170:173], v[158:161], a[184:187], v[170:173]\n"
      "v_add_f32 v120, v120, v107\n"
      "v_mov_b32 v107, v120\n"
      "v_mfma_f32_16x16x32_bf16 v[170:173], v[174:177], a[188:191], v[170:173]\n"
      "ds_read_b128 v[166:169], v0 offset:22528\n"
      "v_mul_f32_e64 v222, v106, v222\n"
      "v_mfma_f32_16x16x32_bf16 v[202:205], v[142:145], a[192:195], 0\n"
      "v_cvt_pk_bf16_f32 v154, v154, v155\n"
      "v_mfma_f32_16x16x32_bf16 v[202:205], v[182:185], a[196:199], v[202:205]\n"
      "v_permlane32_swap_b32 v120, v107\n"
      "v_mfma_f32_16x16x32_bf16 v[202:205], v[206:209], a[200:203], v[202:205]\n"
      "v_mul_f32 v157, v77, v157\n"
      "v_mul_f32 v192, v77, v192\n"
      "v_mfma_f32_16x16x32_bf16 v[202:205], v[186:189], a[204:207], v[202:205]\n"
      "ds_read_b128 v[142:145], v1 offset:34816\n"
      "v_add_f32 v120, v120, v107\n"
      "v_mfma_f32_16x16x32_bf16 v[202:205], v[158:161], a[208:211], v[202:205]\n"
      "v_rsq_f32 v120, v120\n"
      "v_mfma_f32_16x16x32_bf16 v[202:205], v[174:177], a[212:215], v[202:205]\n"
      "s_waitcnt lgkmcnt(1)\n"
      "v_cvt_pk_bf16_f32 v132, v216, v217\n"
      "v_mov_b32_e64 v107, v120\n"
      "v_mfma_f32_16x16x32_bf16 v[206:209], a[0:3], v[166:169], 0\n"
      "s_nop 0\n"
      "v_permlane16_swap_b32 v120, v107\n"
      "v_mfma_f32_16x16x32_bf16 v[206:209], a[4:7], v[162:165], v[206:209]\n"
      "v_mul_f32 v120, s50, v120\n"
      "v_mul_f32 v181, v107, v181\n"
      "v_mfma_f32_16x16x32_bf16 v[206:209], a[8:11], v[146:149], v[206:209]\n"
      "v_mul_f32 v126, v107, v126\n"
      "v_mul_f32 v127, v107, v127\n"
      "v_mfma_f32_16x16x32_bf16 v[206:209], a[12:15], v[198:201], v[206:209]\n"
      "v_mul_f32 v191, v77, v191\n"
      "v_mul_f32 v140, v120, v140\n"
      "v_mfma_f32_16x16x32_bf16 v[206:209], a[16:19], v[210:213], v[206:209]\n"
      "s_waitcnt lgkmcnt(0)\n"
      "v_mul_f32 v180, v107, v180\n"
      "v_mul_f32 v129, v107, v129\n"
      "v_mfma_f32_16x16x32_bf16 v[206:209], a[20:23], v[142:145], v[206:209]\n"
      "v_mul_f32 v151, v120, v151\n"
      "v_mul_f32 v114, v106, v114\n"
      "v_mfma_f32_16x16x32_bf16 v[182:185], a[24:27], v[166:169], 0\n"
      "v_mul_f32 v102, v106, v102\n"
      "v_mul_f32 v128, v107, v128\n"
      "v_mfma_f32_16x16x32_bf16 v[182:185], a[28:31], v[162:165], v[182:185]\n"
      "v_mul_f32 v139, v120, v139\n"
      "v_cvt_pk_bf16_f32 v126, v126, v127\n"
      "v_mfma_f32_16x16x32_bf16 v[182:185], a[32:35], v[146:149], v[182:185]\n"
      "v_mul_f32_e64 v123, v120, v123\n"
      "v_fma_f32 v76, v206, v206, s51\n"
      "v_mfma_f32_16x16x32_bf16 v[182:185], a[36:39], v[198:201], v[182:185]\n"
      "v_cvt_pk_bf16_f32 v196, v170, v171\n"
      "v_mul_f32 v122, v120, v122\n"
      "v_mfma_f32_16x16x32_bf16 v[182:185], a[40:43], v[210:213], v[182:185]\n"
      "v_mul_f32 v104, v106, v104\n"
      "v_fmac_f32 v76, v207, v207\n"
      "v_mfma_f32_16x16x32_bf16 v[182:185], a[44:47], v[142:145], v[182:185]\n"
      "v_mul_f32 v116, v107, v116\n"
      "v_cvt_pk_bf16_f32 v112, v112, v113\n"
      "v_mfma_f32_16x16x32_bf16 v[108:111], a[48:51], v[166:169], 0\n"
      "v_cvt_pk_bf16_f32 v113, v114, v115\n"
      "v_cvt_pk_bf16_f32 v122, v122, v123\n"
      "v_mfma_f32_16x16x32_bf16 v[108:111], a[52:55], v[162:165], v[108:111]\n"
      "v_mul_f32 v220, v106, v220\n"
      "v_mul_f32 v178, v107, v178\n"
      "v_mfma_f32_16x16x32_bf16 v[108:111], a[56:59], v[146:149], v[108:111]\n"
      "v_cvt_pk_bf16_f32 v127, v128, v129\n"
      "v_mul_f32 v223, v106, v223\n"
      "v_mfma_f32_16x16x32_bf16 v[108:111], a[60:63], v[198:201], v[108:111]\n"
      "s_cmp_ge_u32 s95, 0x40000000\n"
      "s_cselect_b32 s94, -1, -2\n"
      "v_cvt_pk_bf16_f32 v133, v218, v219\n"
      "v_mfma_f32_16x16x32_bf16 v[108:111], a[64:67], v[210:213], v[108:111]\n"
      "v_mul_f32 v117, v107, v117\n"
      "v_cvt_pk_bf16_f32 v155, v156, v157\n"
      "v_mfma_f32_16x16x32_bf16 v[108:111], a[68:71], v[142:145], v[108:111]\n"
      "v_cvt_pk_bf16_f32 v156, v190, v191\n"
      "v_mul_f32 v138, v120, v138\n"
      "v_mfma_f32_16x16x32_bf16 v[186:189], a[72:75], v[166:169], 0\n"
      "v_fmac_f32 v76, v208, v208\n"
      "v_mul_f32 v221, v106, v221\n"
      "v_mfma_f32_16x16x32_bf16 v[186:189], a[76:79], v[162:165], v[186:189]\n"
      "v_fmac_f32 v76, v209, v209\n"
      "v_fmac_f32_e64 v76, v182, v182\n"
      "v_mfma_f32_16x16x32_bf16 v[186:189], a[80:83], v[146:149], v[186:189]\n"
      "v_cvt_pk_bf16_f32 v102, v102, v103\n"
      "v_cvt_pk_bf16_f32 v138, v138, v139\n"
      "v_mfma_f32_16x16x32_bf16 v[186:189], a[84:87], v[198:201], v[186:189]\n"
      "v_mul_f32_e64 v96, v77, v96\n"
      "v_cvt_pk_bf16_f32 v116, v116, v117\n"
      "v_mfma_f32_16x16x32_bf16 v[186:189], a[88:91], v[210:213], v[186:189]\n"
      "v_mul_f32 v179, v107, v179\n"
      "v_mul_f32 v124, v120, v124\n"
      "v_mfma_f32_16x16x32_bf16 v[186:189], a[92:95], v[142:145], v[186:189]\n"
      "v_mul_f32 v150, v120, v150\n"
      "v_cvt_pk_bf16_f32 v129, v180, v181\n"
      "v_mfma_f32_16x16x32_bf16 v[158:161], a[96:99], v[166:169], 0\n"
      "v_cvt_pk_bf16_f32 v217, v204, v205\n"
      "v_fmac_f32 v76, v183, v183\n"
      "v_mfma_f32_16x16x32_bf16 v[158:161], a[100:103], v[162:165], v[158:161]\n"
      "v_cvt_pk_bf16_f32 v197, v172, v173\n"
      "v_fmac_f32_e64 v76, v184, v184\n"
      "v_mfma_f32_16x16x32_bf16 v[158:161], a[104:107], v[146:149], v[158:161]\n"
      "v_cvt_pk_bf16_f32 v114, v220, v221\n"
      "v_mul_f32_e64 v152, v120, v152\n"
      "v_mfma_f32_16x16x32_bf16 v[158:161], a[108:111], v[198:201], v[158:161]\n"
      "v_fma_f32 v205, v186, v186, s51\n"
      "v_cvt_pk_bf16_f32 v96, v96, v97\n"
      "v_mfma_f32_16x16x32_bf16 v[158:161], a[112:115], v[210:213], v[158:161]\n"
      "v_fmac_f32 v205, v187, v187\n"
      "v_fmac_f32 v205, v188, v188\n"
      "v_mfma_f32_16x16x32_bf16 v[158:161], a[116:119], v[142:145], v[158:161]\n"
      "v_fmac_f32_e64 v76, v185, v185\n"
      "v_cvt_pk_bf16_f32 v103, v104, v105\n"
      "v_mfma_f32_16x16x32_bf16 v[174:177], a[120:123], v[166:169], 0\n"
      "v_cvt_pk_bf16_f32 v128, v178, v179\n"
      "v_cvt_pk_bf16_f32 v115, v222, v223\n"
      "v_mfma_f32_16x16x32_bf16 v[174:177], a[124:127], v[162:165], v[174:177]\n"
      "v_mul_f32 v153, v120, v153\n"
      "v_mul_f32 v119, v107, v119\n"
      "v_mfma_f32_16x16x32_bf16 v[174:177], a[128:131], v[146:149], v[174:177]\n"
      "v_mul_f32 v118, v107, v118\n"
      "v_cvt_pk_bf16_f32 v117, v118, v119\n"
      "v_mfma_f32_16x16x32_bf16 v[174:177], a[132:135], v[198:201], v[174:177]\n"
      "s_cmp_gt_u32 s42, s44\n"
      "s_cselect_b32 s95, s94, s95\n"
      "v_fmac_f32 v76, v108, v108\n"
      "v_mfma_f32_16x16x32_bf16 v[174:177], a[136:139], v[210:213], v[174:177]\n"
      "v_cvt_pk_bf16_f32 v157, v192, v193\n"
      "v_mul_f32 v141, v120, v141\n"
      "v_mfma_f32_16x16x32_bf16 v[174:177], a[140:143], v[142:145], v[174:177]\n"
      "v_mul_f32 v125, v120, v125\n"
      "v_fmac_f32 v76, v109, v109\n"
      "v_mfma_f32_16x16x32_bf16 v[170:173], v[166:169], a[144:147], 0\n"
      "v_fmac_f32 v205, v189, v189\n"
      "v_fmac_f32 v205, v158, v158\n"
      "v_mfma_f32_16x16x32_bf16 v[170:173], v[162:165], a[148:151], v[170:173]\n"
      "ds_read_b128 v[104:107], v2 offset:6144\n"
      "v_fmac_f32 v205, v159, v159\n"
      "v_mfma_f32_16x16x32_bf16 v[170:173], v[146:149], a[152:155], v[170:173]\n"
      "v_fmac_f32 v205, v160, v160\n"
      "v_mul_f32 v99, v77, v99\n"
      "v_mfma_f32_16x16x32_bf16 v[170:173], v[198:201], a[156:159], v[170:173]\n"
      "v_fmac_f32 v205, v161, v161\n"
      "v_fmac_f32 v205, v174, v174\n"
      "v_mfma_f32_16x16x32_bf16 v[170:173], v[210:213], a[160:163], v[170:173]\n"
      "v_cvt_pk_bf16_f32 v97, v98, v99\n"
      "v_fmac_f32_e64 v76, v110, v110\n"
      "v_mfma_f32_16x16x32_bf16 v[170:173], v[142:145], a[164:167], v[170:173]\n"
      "v_fmac_f32 v205, v175, v175\n"
      "v_fmac_f32 v76, v111, v111\n"
      "v_mfma_f32_16x16x32_bf16 v[218:221], v[166:169], a[168:171], 0\n"
      "v_fmac_f32 v205, v176, v176\n"
      "v_fmac_f32 v205, v177, v177\n"
      "v_mfma_f32_16x16x32_bf16 v[218:221], v[162:165], a[172:175], v[218:221]\n"
      "s_nop 0\n"
      "v_permlane16_swap_b32 v76, v205\n"
      "v_mfma_f32_16x16x32_bf16 v[218:221], v[146:149], a[176:179], v[218:221]\n"
      "v_add_f32 v76, v76, v205\n"
      "v_mov_b32 v205, v76\n"
      "v_mfma_f32_16x16x32_bf16 v[218:221], v[198:201], a[180:183], v[218:221]\n"
      "ds_read_b128 v[118:121], v2 offset:3072\n"
      "v_cvt_pk_bf16_f32 v77, v172, v173\n"
      "v_mfma_f32_16x16x32_bf16 v[218:221], v[210:213], a[184:187], v[218:221]\n"
      "v_permlane32_swap_b32 v76, v205\n"
      "v_mfma_f32_16x16x32_bf16 v[218:221], v[142:145], a[188:191], v[218:221]\n"
      "ds_read_b128 v[178:181], v2\n"
      "v_add_f32 v76, v76, v205\n"
      "v_mfma_f32_16x16x32_bf16 v[190:193], v[166:169], a[192:195], 0\n"
      "v_rsq_f32 v76, v76\n"
      "v_mfma_f32_16x16x32_bf16 v[190:193], v[162:165], a[196:199], v[190:193]\n"
      "ds_read_b128 v[162:165], v2 offset:9216\n"
      "v_mov_b32 v205, v76\n"
      "v_mfma_f32_16x16x32_bf16 v[190:193], v[146:149], a[200:203], v[190:193]\n"
      "s_nop 0\n"
      "v_permlane16_swap_b32 v76, v205\n"
      "v_mfma_f32_16x16x32_bf16 v[190:193], v[198:201], a[204:207], v[190:193]\n"
      "v_mul_f32_e64 v76, s50, v76\n"
      "v_cvt_pk_bf16_f32 v139, v140, v141\n"
      "v_mfma_f32_16x16x32_bf16 v[190:193], v[210:213], a[208:211], v[190:193]\n"
      "ds_read_b128 v[210:213], v2 offset:12288\n"
      "v_mul_f32 v207, v76, v207\n"
      "v_mfma_f32_16x16x32_bf16 v[190:193], v[142:145], a[212:215], v[190:193]\n"
      "s_waitcnt lgkmcnt(2)\n"
      "v_mul_f32 v176, v205, v176\n"
      "v_cvt_pk_bf16_f32 v140, v150, v151\n"
      "v_mfma_f32_16x16x32_bf16 v[166:169], a[216:219], v[178:181], 0\n"
      "v_mul_f32 v160, v205, v160\n"
      "v_cvt_pk_bf16_f32 v80, v218, v219\n"
      "v_mfma_f32_16x16x32_bf16 v[166:169], a[220:223], v[118:121], v[166:169]\n"
      "v_mul_f32 v161, v205, v161\n"
      "v_mul_f32 v183, v76, v183\n"
      "v_mfma_f32_16x16x32_bf16 v[166:169], a[224:227], v[104:107], v[166:169]\n"
      "ds_read_b128 v[146:149], v2 offset:15360\n"
      "v_cvt_pk_bf16_f32 v216, v202, v203\n"
      "v_mfma_f32_16x16x32_bf16 v[198:201], a[240:243], v[178:181], 0\n"
      "v_mul_f32 v186, v205, v186\n"
      "v_mul_f32 v108, v76, v108\n"
      "v_mfma_f32_16x16x32_bf16 v[198:201], a[244:247], v[118:121], v[198:201]\n"
      "v_cvt_pk_bf16_f32 v123, v124, v125\n"
      "v_mul_f32 v110, v76, v110\n"
      "v_mfma_f32_16x16x32_bf16 v[198:201], a[248:251], v[104:107], v[198:201]\n"
      "ds_read_b128 v[98:101], v2 offset:6400\n"
      "v_mul_f32 v185, v76, v185\n"
      "v_mfma_f32_16x16x32_bf16 v[142:145], v[232:235], v[178:181], 0\n"
      "ds_read_b64 v[150:151], v10 offset:1192\n"
      "v_mul_f32 v187, v205, v187\n"
      "v_mfma_f32_16x16x32_bf16 v[142:145], v[236:239], v[118:121], v[142:145]\n"
      "v_mul_f32 v209, v76, v209\n"
      "v_mul_f32 v174, v205, v174\n"
      "v_mfma_f32_16x16x32_bf16 v[142:145], v[240:243], v[104:107], v[142:145]\n"
      "s_waitcnt lgkmcnt(2)\n"
      "v_mul_f32 v177, v205, v177\n"
      "v_mul_f32 v109, v76, v109\n"
      "v_mfma_f32_16x16x32_bf16 v[166:169], a[228:231], v[162:165], v[166:169]\n"
      "v_cvt_pk_bf16_f32 v84, v190, v191\n"
      "v_mul_f32 v159, v205, v159\n"
      "v_mfma_f32_16x16x32_bf16 v[166:169], a[232:235], v[210:213], v[166:169]\n"
      "v_mul_f32 v189, v205, v189\n"
      "v_cvt_pk_bf16_f32 v108, v108, v109\n"
      "v_mfma_f32_16x16x32_bf16 v[166:169], a[236:239], v[146:149], v[166:169]\n"
      "ds_read_b128 v[104:107], v2 offset:256\n"
      "v_mul_f32_e64 v206, v76, v206\n"
      "v_mfma_f32_16x16x32_bf16 v[198:201], a[252:255], v[162:165], v[198:201]\n"
      "ds_read_b128 v[118:121], v10 offset:1200\n"
      "v_mul_f32_e64 v158, v205, v158\n"
      "v_mfma_f32_16x16x32_bf16 v[198:201], v[224:227], v[210:213], v[198:201]\n"
      "v_cvt_pk_bf16_f32 v206, v206, v207\n"
      "v_cvt_pk_bf16_f32 v186, v186, v187\n"
      "v_mfma_f32_16x16x32_bf16 v[198:201], v[228:231], v[146:149], v[198:201]\n"
      "v_mul_f32_e64 v188, v205, v188\n"
      "v_cvt_pk_bf16_f32 v187, v188, v189\n"
      "v_mfma_f32_16x16x32_bf16 v[142:145], v[244:247], v[162:165], v[142:145]\n"
      "v_cvt_pk_bf16_f32 v189, v160, v161\n"
      "v_mul_f32_e64 v175, v205, v175\n"
      "v_mfma_f32_16x16x32_bf16 v[142:145], v[248:251], v[210:213], v[142:145]\n"
      "ds_read_b128 v[210:213], v2 offset:3328\n"
      "v_mul_f32 v208, v76, v208\n"
      "v_mfma_f32_16x16x32_bf16 v[142:145], v[252:255], v[146:149], v[142:145]\n"
      "s_add_u32 s43, s43, 1\n"
      "v_cvt_pk_bf16_f32 v188, v158, v159\n"
      "v_fma_f32 v222, v166, v166, s52\n"
      "v_mfma_f32_16x16x32_bf16 v[146:149], v[112:115], v[154:157], v[40:43]\n"
      "ds_read_u16 v82, v11 offset:1200\n"
      "v_cvt_pk_bf16_f32 v174, v174, v175\n"
      "v_mfma_f32_16x16x32_bf16 v[158:161], v[126:129], v[154:157], v[44:47]\n"
      "s_waitcnt lgkmcnt(2)\n"
      "v_cvt_pk_bf16_f32 v175, v176, v177\n"
      "v_mul_f32 v182, v76, v182\n"
      "v_mfma_f32_16x16x32_bf16 v[176:179], v[186:189], v[154:157], v[48:51]\n"
      "ds_read_u16 v86, v11 offset:1232\n"
      "v_mul_f32 v184, v76, v184\n"
      "v_mfma_f32_16x16x32_bf16 v[202:205], v[118:121], v[154:157], v[24:27]\n"
      "v_mul_f32 v111, v76, v111\n"
      "v_cvt_pk_bf16_f32 v207, v208, v209\n"
      "v_mfma_f32_16x16x16_bf16 v[146:149], v[102:103], v[96:97], v[146:149]\n"
      "v_cvt_pk_bf16_f32 v109, v110, v111\n"
      "v_cvt_pk_bf16_f32 v141, v152, v153\n"
      "v_cvt_pk_bf16_f32 v209, v184, v185\n"
      "v_mfma_f32_16x16x16_bf16 v[158:161], v[116:117], v[96:97], v[158:161]\n"
      "v_mul_f32_e64 v223, v167, v167\n"
      "v_cvt_pk_bf16_f32 v208, v182, v183\n"
      "v_cvt_pk_bf16_f32 v76, v170, v171\n"
      "v_mfma_f32_16x16x16_bf16 v[176:179], v[174:175], v[96:97], v[176:179]\n"
      "v_exp_f32_e64 v148, v148\n"
      "v_cvt_pk_bf16_f32 v81, v220, v221\n"
      "v_mfma_f32_16x16x16_bf16 v[202:205], v[150:151], v[96:97], v[202:205]\n"
      "ds_read_b128 v[152:155], v2 offset:9472\n"
      "s_waitcnt vmcnt(1)\n"
      "v_or3_b32 v134, v134, v135, v136\n"
      "v_mfma_f32_16x16x32_bf16 v[162:165], a[216:219], v[104:107], 0\n"
      "s_waitcnt lgkmcnt(1)\n"
      "v_exp_f32 v159, v159\n"
      "v_mfma_f32_16x16x32_bf16 v[162:165], a[220:223], v[210:213], v[162:165]\n"
      "v_exp_f32 v179, v179\n"
      "v_mfma_f32_16x16x32_bf16 v[162:165], a[224:227], v[98:101], v[162:165]\n"
      "ds_read_u16 v78, v11 offset:1168\n"
      "v_cvt_pk_bf16_f32 v85, v192, v193\n"
      "v_mfma_f32_16x16x32_bf16 v[180:183], a[240:243], v[104:107], 0\n"
      "v_exp_f32 v202, v202\n"
      "v_mfma_f32_16x16x32_bf16 v[180:183], a[244:247], v[210:213], v[180:183]\n"
      "v_exp_f32 v146, v146\n"
      "v_mfma_f32_16x16x32_bf16 v[180:183], a[248:251], v[98:101], v[180:183]\n"
      "v_exp_f32 v158, v158\n"
      "v_mfma_f32_16x16x32_bf16 v[170:173], v[232:235], v[104:107], 0\n"
      "v_exp_f32 v161, v161\n"
      "v_mfma_f32_16x16x32_bf16 v[170:173], v[236:239], v[210:213], v[170:173]\n"
      "v_exp_f32 v160, v160\n"
      "v_mfma_f32_16x16x32_bf16 v[170:173], v[240:243], v[98:101], v[170:173]\n"
      "v_exp_f32 v147, v147\n"
      "v_mfma_f32_16x16x32_bf16 v[210:213], v[112:115], v[138:141], v[52:55]\n"
      "v_exp_f32_e64 v149, v149\n"
      "v_mfma_f32_16x16x32_bf16 v[190:193], v[126:129], v[138:141], v[56:59]\n"
      "v_exp_f32 v176, v176\n"
      "v_mfma_f32_16x16x32_bf16 v[104:107], v[186:189], v[138:141], v[60:63]\n"
      "v_exp_f32 v177, v177\n"
      "v_mfma_f32_16x16x32_bf16 v[98:101], v[118:121], v[138:141], v[26:29]\n"
      "v_exp_f32 v178, v178\n"
      "v_mfma_f32_16x16x16_bf16 v[210:213], v[102:103], v[122:123], v[210:213]\n"
      "s_waitcnt vmcnt(0)\n"
      "v_or3_b32 v134, v134, v137, v88\n"
      "v_cvt_pk_bf16_f32 v88, v176, v177\n"
      "v_cvt_pk_bf16_f32 v146, v146, v147\n"
      "v_mfma_f32_16x16x16_bf16 v[190:193], v[116:117], v[122:123], v[190:193]\n"
      "v_cvt_pk_bf16_f32 v147, v148, v149\n"
      "v_cvt_pk_bf16_f32 v149, v160, v161\n"
      "v_cvt_pk_bf16_f32 v89, v178, v179\n"
      "v_mfma_f32_16x16x16_bf16 v[104:107], v[174:175], v[122:123], v[104:107]\n"
      "v_exp_f32_e64 v213, v213\n"
      "v_cvt_pk_bf16_f32 v148, v158, v159\n"
      "v_mfma_f32_16x16x16_bf16 v[98:101], v[150:151], v[122:123], v[98:101]\n"
      "v_cvt_pk_bf16_f32 v90, v202, 0\n"
      "v_exp_f32 v193, v193\n"
      "v_mfma_f32_16x16x32_bf16 v[176:179], v[32:35], v[146:149], 0\n"
      "v_exp_f32 v210, v210\n"
      "v_mfma_f32_16x16x32_bf16 v[176:179], v[32:35], v[88:91], v[176:179]\n"
      "v_exp_f32 v106, v106\n"
      "v_exp_f32 v191, v191\n"
      "v_mfma_f32_16x16x32_bf16 v[136:139], v[130:133], v[146:149], 0\n"
      "s_waitcnt lgkmcnt(0)\n"
      "v_exp_f32 v212, v212\n"
      "v_mfma_f32_16x16x32_bf16 v[136:139], v[76:79], v[88:91], v[136:139]\n"
      "v_exp_f32 v211, v211\n"
      "v_mfma_f32_16x16x32_bf16 v[202:205], v[194:197], v[146:149], 0\n"
      "v_exp_f32 v190, v190\n"
      "v_mfma_f32_16x16x32_bf16 v[202:205], v[80:83], v[88:91], v[202:205]\n"
      "ds_write_b32 v31, v134 offset:5632\n"
      "v_mfma_f32_16x16x32_bf16 v[156:159], v[214:217], v[146:149], 0\n"
      "v_rcp_f32_e64 v176, v176\n"
      "v_cvt_pk_bf16_f32 v210, v210, v211\n"
      "v_mfma_f32_16x16x32_bf16 v[156:159], v[84:87], v[88:91], v[156:159]\n"
      "v_exp_f32_e64 v192, v192\n"
      "v_mfma_f32_16x16x32_bf16 v[146:149], v[112:115], v[206:209], v[64:67]\n"
      "v_cvt_pk_bf16_f32 v211, v212, v213\n"
      "v_cvt_pk_bf16_f32 v213, v192, v193\n"
      "v_exp_f32_e64 v105, v105\n"
      "v_cvt_pk_bf16_f32 v212, v190, v191\n"
      "v_pk_fma_f32 v[222:223], v[168:169], v[168:169], v[222:223]\n"
      "v_pk_mul_f32 v[136:137], v[136:137], v[176:177] op_sel_hi:[1,0]\n"
      "v_pk_mul_f32 v[158:159], v[158:159], v[176:177] op_sel_hi:[1,0]\n"
      "v_pk_mul_f32 v[204:205], v[204:205], v[176:177] op_sel_hi:[1,0]\n"
      "v_pk_mul_f32 v[138:139], v[138:139], v[176:177] op_sel_hi:[1,0]\n"
      "v_pk_mul_f32 v[202:203], v[202:203], v[176:177] op_sel_hi:[1,0]\n"
      "v_pk_mul_f32 v[156:157], v[156:157], v[176:177] op_sel_hi:[1,0]\n"
      "v_pk_fma_f32 v[222:223], v[198:199], v[198:199], v[222:223]\n"
      "v_mfma_f32_16x16x32_bf16 v[190:193], v[126:129], v[206:209], v[68:71]\n"
      "v_exp_f32_e64 v107, v107\n"
      "v_mfma_f32_16x16x32_bf16 v[176:179], v[186:189], v[206:209], v[72:75]\n"
      "ds_read_b128 v[122:125], v2 offset:12544\n"
      "v_cvt_pk_bf16_f32 v93, v106, v107\n"
      "v_mfma_f32_16x16x32_bf16 v[218:221], v[118:121], v[206:209], v[28:31]\n"
      "ds_read_b128 v[206:209], v2 offset:15616\n"
      "v_cvt_pk_bf16_f32 v156, v156, v157\n"
      "v_mfma_f32_16x16x16_bf16 v[146:149], v[102:103], v[108:109], v[146:149]\n"
      "v_exp_f32_e64 v104, v104\n"
      "v_cvt_pk_bf16_f32 v136, v136, v137\n"
      "v_mfma_f32_16x16x16_bf16 v[190:193], v[116:117], v[108:109], v[190:193]\n"
      "v_cvt_pk_bf16_f32 v137, v138, v139\n"
      "v_cvt_pk_bf16_f32 v138, v202, v203\n"
      "v_cvt_pk_bf16_f32 v139, v204, v205\n"
      "v_mfma_f32_16x16x16_bf16 v[176:179], v[174:175], v[108:109], v[176:179]\n"
      "v_cvt_pk_bf16_f32 v157, v158, v159\n"
      "v_exp_f32 v98, v98\n"
      "v_mfma_f32_16x16x16_bf16 v[218:221], v[150:151], v[108:109], v[218:221]\n"
      "v_cvt_pk_bf16_f32 v92, v104, v105\n"
      "v_exp_f32 v193, v193\n"
      "v_mfma_f32_16x16x32_bf16 v[162:165], a[228:231], v[152:155], v[162:165]\n"
      "s_waitcnt lgkmcnt(1)\n"
      "v_exp_f32 v148, v148\n"
      "v_mfma_f32_16x16x32_bf16 v[162:165], a[232:235], v[122:125], v[162:165]\n"
      "s_waitcnt lgkmcnt(0)\n"
      "v_exp_f32 v149, v149\n"
      "v_mfma_f32_16x16x32_bf16 v[162:165], a[236:239], v[206:209], v[162:165]\n"
      "v_exp_f32 v147, v147\n"
      "v_mfma_f32_16x16x32_bf16 v[180:183], a[252:255], v[152:155], v[180:183]\n"
      "v_exp_f32 v146, v146\n"
      "v_mfma_f32_16x16x32_bf16 v[180:183], v[224:227], v[122:125], v[180:183]\n"
      "ds_read_b128 v[106:109], v10 offset:1168\n"
      "v_cvt_pk_bf16_f32 v146, v146, v147\n"
      "v_mfma_f32_16x16x32_bf16 v[180:183], v[228:231], v[206:209], v[180:183]\n"
      "v_exp_f32 v218, v218\n"
      "v_mfma_f32_16x16x32_bf16 v[170:173], v[244:247], v[152:155], v[170:173]\n"
      "s_add_u32 s42, s42, 1\n"
      "v_exp_f32_e64 v176, v176\n"
      "v_mfma_f32_16x16x32_bf16 v[170:173], v[248:251], v[122:125], v[170:173]\n"
      "ds_read_b128 v[202:205], v7\n"
      "v_cvt_pk_bf16_f32 v94, v98, 0\n"
      "v_mfma_f32_16x16x32_bf16 v[170:173], v[252:255], v[206:209], v[170:173]\n"
      "v_exp_f32 v179, v179\n"
      "v_mfma_f32_16x16x32_bf16 v[98:101], v[32:35], v[210:213], 0\n"
      "v_exp_f32 v178, v178\n"
      "v_mfma_f32_16x16x32_bf16 v[98:101], v[32:35], v[92:95], v[98:101]\n"
      "v_exp_f32_e64 v192, v192\n"
      "v_mfma_f32_16x16x32_bf16 v[152:155], v[130:133], v[210:213], 0\n"
      "ds_write_b128 v3, v[136:139] offset:18432\n"
      "v_pk_fma_f32 v[222:223], v[200:201], v[200:201], v[222:223]\n"
      "v_mfma_f32_16x16x32_bf16 v[152:155], v[76:79], v[92:95], v[152:155]\n"
      "ds_read_b64 v[220:221], v10 offset:1184\n"
      "v_cvt_pk_bf16_f32 v147, v148, v149\n"
      "v_mfma_f32_16x16x32_bf16 v[134:137], v[194:197], v[210:213], 0\n"
      "ds_read_b128 v[206:209], v7 offset:4096\n"
      "v_cvt_pk_bf16_f32 v89, v178, v179\n"
      "v_mfma_f32_16x16x32_bf16 v[134:137], v[80:83], v[92:95], v[134:137]\n"
      "s_waitcnt lgkmcnt(3)\n"
      "v_rcp_f32 v98, v98\n"
      "v_mfma_f32_16x16x32_bf16 v[158:161], v[214:217], v[210:213], 0\n"
      "buffer_store_dwordx4 v[202:205], v20, s[24:27], s85 offen sc1\n"
      "v_pk_mul_f32 v[154:155], v[154:155], v[98:99] op_sel_hi:[1,0]\n"
      "v_pk_mul_f32 v[152:153], v[152:153], v[98:99] op_sel_hi:[1,0]\n"
      "v_mfma_f32_16x16x32_bf16 v[158:161], v[84:87], v[92:95], v[158:161]\n"
      "v_exp_f32 v177, v177\n"
      "v_mfma_f32_16x16x32_bf16 v[210:213], v[106:109], v[112:115], v[30:33]\n"
      "ds_read_b128 v[202:205], v2 offset:6656\n"
      "v_cvt_pk_bf16_f32 v149, v192, v193\n"
      "v_mfma_f32_16x16x32_bf16 v[112:115], v[106:109], v[126:129], v[30:33]\n"
      "s_waitcnt lgkmcnt(1)\n"
      "v_cvt_pk_bf16_f32 v88, v176, v177\n"
      "v_cvt_pk_bf16_f32 v90, v218, 0\n"
      "v_mfma_f32_16x16x32_bf16 v[124:127], v[106:109], v[186:189], v[30:33]\n"
      "ds_read_b128 v[186:189], v2 offset:512\n"
      "v_cvt_pk_bf16_f32 v152, v152, v153\n"
      "v_mfma_f32_16x16x32_bf16 v[176:179], v[106:109], v[118:121], v[30:33]\n"
      "ds_read_b128 v[120:123], v2 offset:3584\n"
      "v_mul_f32_e64 v139, v163, v163\n"
      "v_mfma_f32_16x16x16_bf16 v[210:213], v[220:221], v[102:103], v[210:213]\n"
      "v_cvt_pk_bf16_f32 v153, v154, v155\n"
      "v_exp_f32_e64 v191, v191\n"
      "v_mfma_f32_16x16x16_bf16 v[112:115], v[220:221], v[116:117], v[112:115]\n"
      "ds_read_b128 v[100:103], v7 offset:8192\n"
      "v_fma_f32 v138, v162, v162, s52\n"
      "v_mfma_f32_16x16x16_bf16 v[124:127], v[220:221], v[174:175], v[124:127]\n"
      "buffer_store_dwordx4 v[206:209], v21, s[24:27], s85 offen sc1\n"
      "v_pk_fma_f32 v[138:139], v[164:165], v[164:165], v[138:139]\n"
      "v_pk_fma_f32 v[222:223], v[142:143], v[142:143], v[222:223]\n"
      "v_pk_mul_f32 v[158:159], v[158:159], v[98:99] op_sel_hi:[1,0]\n"
      "v_pk_mul_f32 v[136:137], v[136:137], v[98:99] op_sel_hi:[1,0]\n"
      "v_pk_mul_f32 v[134:135], v[134:135], v[98:99] op_sel_hi:[1,0]\n"
      "v_pk_fma_f32 v[138:139], v[180:181], v[180:181], v[138:139]\n"
      "v_pk_fma_f32 v[138:139], v[182:183], v[182:183], v[138:139]\n"
      "v_mfma_f32_16x16x16_bf16 v[176:179], v[220:221], v[150:151], v[176:179]\n"
      "v_cvt_pk_bf16_f32 v155, v136, v137\n"
      "v_cvt_pk_bf16_f32 v154, v134, v135\n"
      "s_waitcnt lgkmcnt(1)\n"
      "v_mfma_f32_16x16x32_bf16 v[126:129], a[216:219], v[186:189], 0\n"
      "ds_write_b128 v3, v[152:155] offset:18688\n"
      "v_pk_fma_f32 v[138:139], v[170:171], v[170:171], v[138:139]\n"
      "v_mfma_f32_16x16x32_bf16 v[126:129], a[220:223], v[120:123], v[126:129]\n"
      "ds_read_b128 v[134:137], v2 offset:12800\n"
      "v_cndmask_b32 v210, v210, v112, s[58:59]\n"
      "v_mfma_f32_16x16x32_bf16 v[126:129], a[224:227], v[202:205], v[126:129]\n"
      "v_exp_f32_e64 v190, v190\n"
      "v_mfma_f32_16x16x32_bf16 v[116:119], a[240:243], v[186:189], 0\n"
      "s_waitcnt lgkmcnt(2)\n"
      "v_cvt_pk_bf16_f32 v148, v190, v191\n"
      "v_cndmask_b32 v210, v210, v124, s[60:61]\n"
      "v_mfma_f32_16x16x32_bf16 v[116:119], a[244:247], v[120:123], v[116:119]\n"
      "ds_read_b128 v[190:193], v2 offset:15872\n"
      "v_mfma_f32_16x16x32_bf16 v[116:119], a[248:251], v[202:205], v[116:119]\n"
      "ds_read_b128 v[218:221], v7 offset:12288\n"
      "v_cndmask_b32 v210, v210, v176, s[62:63]\n"
      "v_mfma_f32_16x16x32_bf16 v[108:111], v[232:235], v[186:189], 0\n"
      "buffer_store_dwordx4 v[100:103], v22, s[24:27], s85 offen sc1\n"
      "v_mfma_f32_16x16x32_bf16 v[108:111], v[236:239], v[120:123], v[108:111]\n"
      "v_exp_f32_e64 v210, v210\n"
      "v_mfma_f32_16x16x32_bf16 v[108:111], v[240:243], v[202:205], v[108:111]\n"
      "ds_write_b64 v4, v[156:157] offset:18432\n"
      "v_mfma_f32_16x16x32_bf16 v[100:103], v[32:35], v[146:149], 0\n"
      "ds_read_b128 v[186:189], v2 offset:9728\n"
      "v_fmac_f32_e64 v139, v173, v173\n"
      "v_mfma_f32_16x16x32_bf16 v[100:103], v[32:35], v[88:91], v[100:103]\n"
      "v_cvt_pk_bf16_f32 v158, v158, v159\n"
      "v_cvt_pk_bf16_f32 v210, v210, v210\n"
      "v_mfma_f32_16x16x32_bf16 v[112:115], v[130:133], v[146:149], 0\n"
      "ds_write_b16 v13, v210\n"
      "v_mfma_f32_16x16x32_bf16 v[112:115], v[76:79], v[88:91], v[112:115]\n"
      "ds_read_b128 v[174:177], v14\n"
      "v_fmac_f32 v138, v172, v172\n"
      "v_mfma_f32_16x16x32_bf16 v[202:205], v[194:197], v[146:149], 0\n"
      "ds_read_b128 v[102:105], v7 offset:16384\n"
      "v_add_f32 v138, v138, v139\n"
      "v_mfma_f32_16x16x32_bf16 v[202:205], v[80:83], v[88:91], v[202:205]\n"
      "s_waitcnt lgkmcnt(5)\n"
      "v_rcp_f32 v100, v100\n"
      "v_mfma_f32_16x16x32_bf16 v[122:125], v[214:217], v[146:149], 0\n"
      "buffer_store_dwordx4 v[218:221], v23, s[24:27], s85 offen sc1\n"
      "v_pk_mul_f32 v[114:115], v[114:115], v[100:101] op_sel_hi:[1,0]\n"
      "v_pk_mul_f32 v[112:113], v[112:113], v[100:101] op_sel_hi:[1,0]\n"
      "v_pk_fma_f32 v[222:223], v[144:145], v[144:145], v[222:223]\n"
      "v_pk_mul_f32 v[160:161], v[160:161], v[98:99] op_sel_hi:[1,0]\n"
      "v_mfma_f32_16x16x32_bf16 v[122:125], v[84:87], v[88:91], v[122:125]\n"
      "s_waitcnt lgkmcnt(2)\n"
      "v_add_f32 v222, v222, v223\n"
      "v_mul_f32 v205, v100, v205\n"
      "v_mfma_f32_16x16x32_bf16 v[126:129], a[228:231], v[186:189], v[126:129]\n"
      "v_permlane16_swap_b32 v222, v138\n"
      "v_mfma_f32_16x16x32_bf16 v[126:129], a[232:235], v[134:137], v[126:129]\n"
      "ds_read_b128 v[210:213], v14 offset:64\n"
      "v_mul_f32 v203, v100, v203\n"
      "v_mfma_f32_16x16x32_bf16 v[126:129], a[236:239], v[190:193], v[126:129]\n"
      "v_mul_f32 v204, v100, v204\n"
      "v_cvt_pk_bf16_f32 v112, v112, v113\n"
      "v_mfma_f32_16x16x32_bf16 v[116:119], a[252:255], v[186:189], v[116:119]\n"
      "v_cvt_pk_bf16_f32 v113, v114, v115\n"
      "v_cvt_pk_bf16_f32 v159, v160, v161\n"
      "v_mfma_f32_16x16x32_bf16 v[116:119], v[224:227], v[134:137], v[116:119]\n"
      "ds_write_b64 v4, v[158:159] offset:18688\n"
      "v_mfma_f32_16x16x32_bf16 v[116:119], v[228:231], v[190:193], v[116:119]\n"
      "v_cvt_pk_bf16_f32 v115, v204, v205\n"
      "v_fma_f32 v158, v126, v126, s52\n"
      "v_mfma_f32_16x16x32_bf16 v[108:111], v[244:247], v[186:189], v[108:111]\n"
      "v_mul_f32 v159, v127, v127\n"
      "v_mul_f32 v202, v100, v202\n"
      "v_mfma_f32_16x16x32_bf16 v[108:111], v[248:251], v[134:137], v[108:111]\n"
      "s_waitcnt lgkmcnt(1)\n"
      "v_cvt_pk_bf16_f32 v114, v202, v203\n"
      "v_add_f32 v222, v222, v138\n"
      "v_mfma_f32_16x16x32_bf16 v[108:111], v[252:255], v[190:193], v[108:111]\n"
      "ds_write_b128 v3, v[112:115] offset:18944\n"
      "v_pk_fma_f32 v[158:159], v[128:129], v[128:129], v[158:159]\n"
      "v_pk_mul_f32 v[122:123], v[122:123], v[100:101] op_sel_hi:[1,0]\n"
      "v_pk_fma_f32 v[158:159], v[116:117], v[116:117], v[158:159]\n"
      "v_pk_mul_f32 v[124:125], v[124:125], v[100:101] op_sel_hi:[1,0]\n"
      "v_cvt_pk_bf16_f32 v122, v122, v123\n"
      "v_cvt_pk_bf16_f32 v123, v124, v125\n"
      "v_pk_fma_f32 v[158:159], v[118:119], v[118:119], v[158:159]\n"
      "v_pk_fma_f32 v[158:159], v[108:109], v[108:109], v[158:159]\n"
      "v_pk_fma_f32 v[158:159], v[110:111], v[110:111], v[158:159]\n"
      "v_add_f32 v158, v158, v159\n"
      "v_mov_b32 v138, v158\n"
      "v_mfma_f32_16x16x32_bf16 v[188:191], v[174:177], v[32:35], 0\n"
      "ds_write_b64 v4, v[122:123] offset:18944\n"
      "v_permlane16_swap_b32 v158, v138\n"
      "v_add_f32_e64 v158, v158, v138\n"
      "v_add_u32 v10, 2336, v10\n"
      "s_nop 0\n"
      "v_permlane32_swap_b32_e64 v222, v158\n"
      "v_add_f32 v222, v222, v158\n"
      "v_mfma_f32_16x16x32_bf16 v[188:191], v[210:213], v[32:35], v[188:191]\n"
      "ds_write_b32 v31, v222 offset:1024\n"
      "v_add_u32 v11, 2336, v11\n"
      "s_cmp_ge_u32 s95, 0x40000000\n"
      "s_cselect_b64 vcc, -1, s[56:57]\n"
      "buffer_store_dwordx4 v[102:105], v25, s[24:27], s85 offen sc1\n"
      "s_waitcnt lgkmcnt(0)\n"
      "s_barrier\n"
      "s_cbranch_vccnz L_disp0_%=\n"
      "L_loop0_%=:\n"
      "v_mfma_f32_16x16x32_bf16 v[218:221], v[174:177], v[130:133], 0\n"
      "ds_read_b128 v[138:141], v0\n"
      "v_readlane_b32 s84, v37, s43\n"
      "v_mfma_f32_16x16x32_bf16 v[218:221], v[210:213], v[76:79], v[218:221]\n"
      "ds_read_b128 v[158:161], v9 offset:1024\n"
      "ds_read_b128 v[184:187], v1\n"
      "v_mfma_f32_16x16x32_bf16 v[134:137], v[174:177], v[194:197], 0\n"
      "ds_read_b128 v[154:157], v1 offset:12288\n"
      "s_add_u32 m0, s64, 18432\n"
      "v_mfma_f32_16x16x32_bf16 v[134:137], v[210:213], v[80:83], v[134:137]\n"
      "buffer_load_dwordx4 v16, s[16:19], s45 offen  lds\n"
      "v_mfma_f32_16x16x32_bf16 v[100:103], v[174:177], v[214:217], 0\n"
      "s_add_u32 m0, s64, 22528\n"
      "ds_read_b128 v[202:205], v0 offset:6144\n"
      "v_mfma_f32_16x16x32_bf16 v[100:103], v[210:213], v[84:87], v[100:103]\n"
      "s_waitcnt lgkmcnt(4)\n"
      "v_mfma_f32_16x16x32_bf16 v[210:213], a[0:3], v[138:141], 0\n"
      "ds_read_b128 v[174:177], v1 offset:6144\n"
      "v_cndmask_b32 v218, v218, v134, s[58:59]\n"
      "v_mfma_f32_16x16x32_bf16 v[112:115], a[24:27], v[138:141], 0\n"
      "s_waitcnt lgkmcnt(4)\n"
      "v_add_f32 v158, v158, v159\n"
      "v_add_f32 v160, v160, v161\n"
      "v_mfma_f32_16x16x32_bf16 v[120:123], a[48:51], v[138:141], 0\n"
      "buffer_load_dwordx4 v17, s[16:19], s45 offen  lds\n"
      "v_mfma_f32_16x16x32_bf16 v[220:223], a[72:75], v[138:141], 0\n"
      "ds_read_b128 v[206:209], v0 offset:12288\n"
      "v_add_f32 v158, v158, v160\n"
      "v_mfma_f32_16x16x32_bf16 v[102:105], a[96:99], v[138:141], 0\n"
      "s_waitcnt lgkmcnt(2)\n"
      "v_rsq_f32_e64 v158, v158\n"
      "v_mfma_f32_16x16x32_bf16 v[146:149], a[120:123], v[138:141], 0\n"
      "s_add_u32 m0, s64, 26624\n"
      "v_mul_f32 v158, 0x415db3d7, v158\n"
      "v_mov_b32 v160, v158\n"
      "v_mfma_f32_16x16x32_bf16 v[210:213], a[4:7], v[184:187], v[210:213]\n"
      "s_nop 0\n"
      "v_permlane32_swap_b32_e64 v158, v160\n"
      "v_mfma_f32_16x16x32_bf16 v[210:213], a[8:11], v[202:205], v[210:213]\n"
      "buffer_load_dwordx4 v18, s[16:19], s45 offen  lds\n"
      "v_mfma_f32_16x16x32_bf16 v[112:115], a[28:31], v[184:187], v[112:115]\n"
      "ds_read_b128 v[96:99], v0 offset:2048\n"
      "v_mov_b32 v159, v158\n"
      "v_mfma_f32_16x16x32_bf16 v[112:115], a[32:35], v[202:205], v[112:115]\n"
      "s_nop 0\n"
      "v_permlane16_swap_b32_e64 v158, v159\n"
      "v_mfma_f32_16x16x32_bf16 v[120:123], a[52:55], v[184:187], v[120:123]\n"
      "s_add_u32 m0, s64, 30720\n"
      "v_mul_f32 v166, v158, v166\n"
      "v_mul_f32 v182, v159, v182\n"
      "v_mfma_f32_16x16x32_bf16 v[120:123], a[56:59], v[202:205], v[120:123]\n"
      "v_mul_f32 v165, v159, v165\n"
      "v_mul_f32 v142, v158, v142\n"
      "v_mfma_f32_16x16x32_bf16 v[220:223], a[76:79], v[184:187], v[220:223]\n"
      "v_mul_f32 v143, v158, v143\n"
      "v_mul_f32 v145, v158, v145\n"
      "v_mfma_f32_16x16x32_bf16 v[220:223], a[80:83], v[202:205], v[220:223]\n"
      "buffer_load_dwordx4 v19, s[16:19], s45 offen  lds\n"
      "v_mfma_f32_16x16x32_bf16 v[102:105], a[100:103], v[184:187], v[102:105]\n"
      "v_mul_f32 v167, v158, v167\n"
      "v_mul_f32 v168, v158, v168\n"
      "v_mfma_f32_16x16x32_bf16 v[102:105], a[104:107], v[202:205], v[102:105]\n"
      "v_mul_f32 v198, v158, v198\n"
      "v_mul_f32 v181, v159, v181\n"
      "v_mfma_f32_16x16x32_bf16 v[146:149], a[124:127], v[184:187], v[146:149]\n"
      "s_add_u32 m0, s65, 34816\n"
      "v_mul_f32 v163, v159, v163\n"
      "v_mul_f32 v110, v160, v110\n"
      "v_mfma_f32_16x16x32_bf16 v[146:149], a[128:131], v[202:205], v[146:149]\n"
      "s_waitcnt lgkmcnt(0)\n"
      "v_mov_b32_dpp v188, v188 quad_perm:[0,0,0,0] row_mask:0xf bank_mask:0xf\n"
      "v_mul_f32 v169, v158, v169\n"
      "v_mfma_f32_16x16x32_bf16 v[210:213], a[12:15], v[174:177], v[210:213]\n"
      "buffer_load_dwordx4 v15, s[16:19], s45 offen  lds\n"
      "v_mfma_f32_16x16x32_bf16 v[210:213], a[16:19], v[206:209], v[210:213]\n"
      "v_mul_f32_e64 v162, v159, v162\n"
      "v_cndmask_b32 v218, v218, v100, s[60:61]\n"
      "v_mfma_f32_16x16x32_bf16 v[210:213], a[20:23], v[154:157], v[210:213]\n"
      "ds_read_b128 v[190:193], v0 offset:8192\n"
      "v_mul_f32_e64 v201, v158, v201\n"
      "v_mfma_f32_16x16x32_bf16 v[112:115], a[36:39], v[174:177], v[112:115]\n"
      "v_cvt_pk_bf16_f32 v166, v166, v167\n"
      "v_cvt_pk_bf16_f32 v162, v162, v163\n"
      "v_mfma_f32_16x16x32_bf16 v[112:115], a[40:43], v[206:209], v[112:115]\n"
      "v_mul_f32 v200, v158, v200\n"
      "v_mul_f32 v108, v160, v108\n"
      "v_mfma_f32_16x16x32_bf16 v[112:115], a[44:47], v[154:157], v[112:115]\n"
      "buffer_load_dwordx4 v[134:137], v36, s[28:31], s47 offen\n"
      "v_mfma_f32_16x16x32_bf16 v[120:123], a[60:63], v[174:177], v[120:123]\n"
      "v_rcp_f32_e64 v188, v188\n"
      "v_mfma_f32_16x16x32_bf16 v[120:123], a[64:67], v[206:209], v[120:123]\n"
      "v_mul_f32 v118, v160, v118\n"
      "v_mul_f32 v126, v160, v126\n"
      "v_mfma_f32_16x16x32_bf16 v[120:123], a[68:71], v[154:157], v[120:123]\n"
      "v_mul_f32_e64 v164, v159, v164\n"
      "v_cvt_pk_bf16_f32 v163, v164, v165\n"
      "v_mfma_f32_16x16x32_bf16 v[220:223], a[84:87], v[174:177], v[220:223]\n"
      "ds_read_b128 v[150:153], v1 offset:8192\n"
      "v_mul_f32_e64 v199, v158, v199\n"
      "v_mfma_f32_16x16x32_bf16 v[220:223], a[88:91], v[206:209], v[220:223]\n"
      "v_mul_f32_e64 v180, v159, v180\n"
      "v_fma_f32 v77, v210, v210, s51\n"
      "v_mfma_f32_16x16x32_bf16 v[220:223], a[92:95], v[154:157], v[220:223]\n"
      "v_cvt_pk_bf16_f32 v167, v168, v169\n"
      "v_mul_f32_e64 v170, v159, v170\n"
      "v_mfma_f32_16x16x32_bf16 v[102:105], a[108:111], v[174:177], v[102:105]\n"
      "buffer_load_dword v88, v29, s[28:31], s47 offen\n"
      "v_mfma_f32_16x16x32_bf16 v[102:105], a[112:115], v[206:209], v[102:105]\n"
      "v_mul_f32 v183, v159, v183\n"
      "v_mul_f32 v218, v188, v218\n"
      "v_mfma_f32_16x16x32_bf16 v[102:105], a[116:119], v[154:157], v[102:105]\n"
      "v_fma_f32 v76, v220, v220, s51\n"
      "v_mul_f32_e64 v173, v159, v173\n"
      "v_mfma_f32_16x16x32_bf16 v[146:149], a[132:135], v[174:177], v[146:149]\n"
      "v_cvt_pk_bf16_f32 v164, v180, v181\n"
      "v_cvt_pk_bf16_f32 v169, v200, v201\n"
      "v_mfma_f32_16x16x32_bf16 v[146:149], a[136:139], v[206:209], v[146:149]\n"
      "v_cvt_pk_bf16_f32 v168, v198, v199\n"
      "v_cvt_pk_bf16_f32 v218, v218, v218\n"
      "v_mfma_f32_16x16x32_bf16 v[146:149], a[140:143], v[154:157], v[146:149]\n"
      "v_fmac_f32 v77, v211, v211\n"
      "v_mfma_f32_16x16x32_bf16 v[130:133], v[138:141], a[144:147], 0\n"
      "ds_write_b16 v12, v218 offset:16\n"
      "v_mfma_f32_16x16x32_bf16 v[130:133], v[184:187], a[148:151], v[130:133]\n"
      "v_mul_f32 v117, v160, v117\n"
      "v_mul_f32 v119, v160, v119\n"
      "v_mfma_f32_16x16x32_bf16 v[130:133], v[202:205], a[152:155], v[130:133]\n"
      "v_mul_f32 v129, v160, v129\n"
      "v_mul_f32_e64 v127, v160, v127\n"
      "v_mfma_f32_16x16x32_bf16 v[130:133], v[174:177], a[156:159], v[130:133]\n"
      "ds_write_b128 v5, v[166:169]\n"
      "v_mfma_f32_16x16x32_bf16 v[130:133], v[206:209], a[160:163], v[130:133]\n"
      "v_fmac_f32_e64 v76, v221, v221\n"
      "v_mfma_f32_16x16x32_bf16 v[130:133], v[154:157], a[164:167], v[130:133]\n"
      "ds_read_b128 v[198:201], v1 offset:2048\n"
      "v_cvt_pk_bf16_f32 v165, v182, v183\n"
      "v_mfma_f32_16x16x32_bf16 v[194:197], v[138:141], a[168:171], 0\n"
      "v_mul_f32_e64 v128, v160, v128\n"
      "v_cvt_pk_bf16_f32 v126, v126, v127\n"
      "v_mfma_f32_16x16x32_bf16 v[194:197], v[184:187], a[172:175], v[194:197]\n"
      "v_fmac_f32 v77, v212, v212\n"
      "v_mul_f32 v111, v160, v111\n"
      "v_mfma_f32_16x16x32_bf16 v[194:197], v[202:205], a[176:179], v[194:197]\n"
      "v_fmac_f32 v77, v213, v213\n"
      "v_fmac_f32 v77, v112, v112\n"
      "v_mfma_f32_16x16x32_bf16 v[194:197], v[174:177], a[180:183], v[194:197]\n"
      "v_fmac_f32 v76, v222, v222\n"
      "v_fmac_f32 v77, v113, v113\n"
      "v_mfma_f32_16x16x32_bf16 v[194:197], v[206:209], a[184:187], v[194:197]\n"
      "ds_write_b128 v5, v[162:165] offset:6144\n"
      "v_mfma_f32_16x16x32_bf16 v[194:197], v[154:157], a[188:191], v[194:197]\n"
      "v_fmac_f32 v76, v223, v223\n"
      "v_fmac_f32 v77, v114, v114\n"
      "v_mfma_f32_16x16x32_bf16 v[214:217], v[138:141], a[192:195], 0\n"
      "ds_read_b128 v[178:181], v1 offset:14336\n"
      "v_mul_f32 v144, v158, v144\n"
      "v_mfma_f32_16x16x32_bf16 v[214:217], v[184:187], a[196:199], v[214:217]\n"
      "v_cvt_pk_bf16_f32 v142, v142, v143\n"
      "v_fmac_f32 v76, v102, v102\n"
      "v_mfma_f32_16x16x32_bf16 v[214:217], v[202:205], a[200:203], v[214:217]\n"
      "ds_read_b128 v[162:165], v9 offset:5632\n"
      "v_fmac_f32_e64 v77, v115, v115\n"
      "v_mfma_f32_16x16x32_bf16 v[214:217], v[174:177], a[204:207], v[214:217]\n"
      "ds_read_b128 v[182:185], v0 offset:14336\n"
      "v_fmac_f32 v77, v120, v120\n"
      "v_mfma_f32_16x16x32_bf16 v[214:217], v[206:209], a[208:211], v[214:217]\n"
      "v_mul_f32 v171, v159, v171\n"
      "v_cvt_pk_bf16_f32 v127, v128, v129\n"
      "v_mfma_f32_16x16x32_bf16 v[214:217], v[154:157], a[212:215], v[214:217]\n"
      "v_add_u32 v12, 32, v12\n"
      "v_fmac_f32 v76, v103, v103\n"
      "v_mfma_f32_16x16x32_bf16 v[174:177], a[0:3], v[96:99], 0\n"
      "s_waitcnt lgkmcnt(2)\n"
      "v_fmac_f32 v77, v121, v121\n"
      "v_fmac_f32 v76, v104, v104\n"
      "v_mfma_f32_16x16x32_bf16 v[174:177], a[4:7], v[198:201], v[174:177]\n"
      "v_fmac_f32 v76, v105, v105\n"
      "v_mul_f32 v109, v160, v109\n"
      "v_mfma_f32_16x16x32_bf16 v[174:177], a[8:11], v[190:193], v[174:177]\n"
      "s_waitcnt lgkmcnt(1)\n"
      "v_fmac_f32 v77, v122, v122\n"
      "v_cvt_pk_bf16_f32 v194, v194, v195\n"
      "v_mfma_f32_16x16x32_bf16 v[174:177], a[12:15], v[150:153], v[174:177]\n"
      "s_waitcnt lgkmcnt(0)\n"
      "v_or3_b32 v162, v162, v163, v164\n"
      "v_cvt_pk_bf16_f32 v130, v130, v131\n"
      "v_mfma_f32_16x16x32_bf16 v[174:177], a[16:19], v[182:185], v[174:177]\n"
      "v_fmac_f32 v76, v146, v146\n"
      "v_mul_f32 v116, v160, v116\n"
      "v_mfma_f32_16x16x32_bf16 v[174:177], a[20:23], v[178:181], v[174:177]\n"
      "ds_read_b128 v[186:189], v0 offset:10240\n"
      "v_cvt_pk_bf16_f32 v108, v108, v109\n"
      "v_mfma_f32_16x16x32_bf16 v[202:205], a[24:27], v[96:99], 0\n"
      "v_cvt_pk_bf16_f32 v129, v118, v119\n"
      "v_cvt_pk_bf16_f32 v128, v116, v117\n"
      "v_mfma_f32_16x16x32_bf16 v[202:205], a[28:31], v[198:201], v[202:205]\n"
      "v_fmac_f32 v76, v147, v147\n"
      "v_fmac_f32 v77, v123, v123\n"
      "v_mfma_f32_16x16x32_bf16 v[202:205], a[32:35], v[190:193], v[202:205]\n"
      "v_fmac_f32 v76, v148, v148\n"
      "v_fma_f32 v164, v174, v174, s51\n"
      "v_mfma_f32_16x16x32_bf16 v[202:205], a[36:39], v[150:153], v[202:205]\n"
      "v_fmac_f32 v76, v149, v149\n"
      "v_fmac_f32_e64 v164, v175, v175\n"
      "v_mfma_f32_16x16x32_bf16 v[202:205], a[40:43], v[182:185], v[202:205]\n"
      "v_permlane16_swap_b32 v77, v76\n"
      "v_mfma_f32_16x16x32_bf16 v[202:205], a[44:47], v[178:181], v[202:205]\n"
      "v_add_f32 v77, v77, v76\n"
      "v_mov_b32_e64 v76, v77\n"
      "v_mfma_f32_16x16x32_bf16 v[116:119], a[48:51], v[96:99], 0\n"
      "ds_read_b128 v[154:157], v0 offset:4096\n"
      "v_cvt_pk_bf16_f32 v170, v170, v171\n"
      "v_mfma_f32_16x16x32_bf16 v[116:119], a[52:55], v[198:201], v[116:119]\n"
      "v_cvt_pk_bf16_f32 v131, v132, v133\n"
      "v_fmac_f32 v164, v176, v176\n"
      "v_mfma_f32_16x16x32_bf16 v[116:119], a[56:59], v[190:193], v[116:119]\n"
      "v_permlane32_swap_b32 v77, v76\n"
      "v_mfma_f32_16x16x32_bf16 v[116:119], a[60:63], v[150:153], v[116:119]\n"
      "v_cvt_pk_bf16_f32 v214, v214, v215\n"
      "v_add_f32 v77, v77, v76\n"
      "v_mfma_f32_16x16x32_bf16 v[116:119], a[64:67], v[182:185], v[116:119]\n"
      "v_rsq_f32 v77, v77\n"
      "v_mfma_f32_16x16x32_bf16 v[116:119], a[68:71], v[178:181], v[116:119]\n"
      "ds_write_b128 v5, v[126:129] offset:12288\n"
      "v_mfma_f32_16x16x32_bf16 v[126:129], a[72:75], v[96:99], 0\n"
      "v_fmac_f32 v164, v177, v177\n"
      "v_fmac_f32 v164, v202, v202\n"
      "v_mfma_f32_16x16x32_bf16 v[126:129], a[76:79], v[198:201], v[126:129]\n"
      "v_mul_f32 v172, v159, v172\n"
      "v_mov_b32 v76, v77\n"
      "v_mfma_f32_16x16x32_bf16 v[126:129], a[80:83], v[190:193], v[126:129]\n"
      "v_cvt_pk_bf16_f32 v143, v144, v145\n"
      "v_readlane_b32 s86, v38, s43\n"
      "v_mfma_f32_16x16x32_bf16 v[126:129], a[84:87], v[150:153], v[126:129]\n"
      "v_cvt_pk_bf16_f32 v109, v110, v111\n"
      "v_readlane_b32 s85, v37, s42\n"
      "v_mfma_f32_16x16x32_bf16 v[126:129], a[88:91], v[182:185], v[126:129]\n"
      "v_permlane16_swap_b32 v77, v76\n"
      "v_mfma_f32_16x16x32_bf16 v[126:129], a[92:95], v[178:181], v[126:129]\n"
      "v_fmac_f32 v164, v203, v203\n"
      "v_cvt_pk_bf16_f32 v195, v196, v197\n"
      "v_mfma_f32_16x16x32_bf16 v[158:161], a[96:99], v[96:99], 0\n"
      "ds_write_b64 v6, v[108:109] offset:12288\n"
      "v_mfma_f32_16x16x32_bf16 v[158:161], a[100:103], v[198:201], v[158:161]\n"
      "v_mul_f32 v146, v76, v146\n"
      "v_mul_f32 v104, v76, v104\n"
      "v_mfma_f32_16x16x32_bf16 v[158:161], a[104:107], v[190:193], v[158:161]\n"
      "v_fma_f32 v124, v126, v126, s51\n"
      "v_fmac_f32 v164, v204, v204\n"
      "v_mfma_f32_16x16x32_bf16 v[158:161], a[108:111], v[150:153], v[158:161]\n"
      "v_fmac_f32 v164, v205, v205\n"
      "v_mul_f32_e64 v77, s50, v77\n"
      "v_mfma_f32_16x16x32_bf16 v[158:161], a[112:115], v[182:185], v[158:161]\n"
      "v_fmac_f32 v164, v116, v116\n"
      "v_fmac_f32 v124, v127, v127\n"
      "v_mfma_f32_16x16x32_bf16 v[158:161], a[116:119], v[178:181], v[158:161]\n"
      "v_mul_f32 v147, v76, v147\n"
      "v_mul_f32 v213, v77, v213\n"
      "v_mfma_f32_16x16x32_bf16 v[106:109], a[120:123], v[96:99], 0\n"
      "v_fmac_f32 v164, v117, v117\n"
      "v_mul_f32 v222, v76, v222\n"
      "v_mfma_f32_16x16x32_bf16 v[106:109], a[124:127], v[198:201], v[106:109]\n"
      "v_fmac_f32 v124, v128, v128\n"
      "v_fmac_f32 v124, v129, v129\n"
      "v_mfma_f32_16x16x32_bf16 v[106:109], a[128:131], v[190:193], v[106:109]\n"
      "v_fmac_f32 v124, v158, v158\n"
      "v_mul_f32 v114, v77, v114\n"
      "v_mfma_f32_16x16x32_bf16 v[106:109], a[132:135], v[150:153], v[106:109]\n"
      "v_mul_f32 v211, v77, v211\n"
      "v_mul_f32 v115, v77, v115\n"
      "v_mfma_f32_16x16x32_bf16 v[106:109], a[136:139], v[182:185], v[106:109]\n"
      "v_fmac_f32 v124, v159, v159\n"
      "v_cvt_pk_bf16_f32 v215, v216, v217\n"
      "v_mfma_f32_16x16x32_bf16 v[106:109], a[140:143], v[178:181], v[106:109]\n"
      "v_mul_f32 v120, v77, v120\n"
      "v_fmac_f32_e64 v124, v160, v160\n"
      "v_mfma_f32_16x16x32_bf16 v[166:169], v[96:99], a[144:147], 0\n"
      "ds_write_b64 v6, v[142:143]\n"
      "v_mfma_f32_16x16x32_bf16 v[166:169], v[198:201], a[148:151], v[166:169]\n"
      "v_cvt_pk_bf16_f32 v171, v172, v173\n"
      "v_fmac_f32 v124, v161, v161\n"
      "v_mfma_f32_16x16x32_bf16 v[166:169], v[190:193], a[152:155], v[166:169]\n"
      "s_or_b32 s95, s84, s85\n"
      "v_mul_f32 v221, v76, v221\n"
      "v_mul_f32 v148, v76, v148\n"
      "v_mfma_f32_16x16x32_bf16 v[166:169], v[150:153], a[156:159], v[166:169]\n"
      "ds_read_b128 v[142:145], v1 offset:10240\n"
      "v_mul_f32 v103, v76, v103\n"
      "v_mfma_f32_16x16x32_bf16 v[166:169], v[182:185], a[160:163], v[166:169]\n"
      "v_fmac_f32 v124, v106, v106\n"
      "v_fmac_f32 v164, v118, v118\n"
      "v_mfma_f32_16x16x32_bf16 v[166:169], v[178:181], a[164:167], v[166:169]\n"
      "s_or_b32 s95, s95, s48\n"
      "v_fmac_f32 v124, v107, v107\n"
      "v_mul_f32 v210, v77, v210\n"
      "v_mfma_f32_16x16x32_bf16 v[138:141], v[96:99], a[168:171], 0\n"
      "v_fmac_f32 v164, v119, v119\n"
      "v_fmac_f32 v124, v108, v108\n"
      "v_mfma_f32_16x16x32_bf16 v[138:141], v[198:201], a[172:175], v[138:141]\n"
      "v_mul_f32 v121, v77, v121\n"
      "v_mul_f32 v102, v76, v102\n"
      "v_mfma_f32_16x16x32_bf16 v[138:141], v[190:193], a[176:179], v[138:141]\n"
      "v_fmac_f32 v124, v109, v109\n"
      "v_bitop3_b32 v162, v162, v165, s33 bitop3:0xa8\n"
      "v_mfma_f32_16x16x32_bf16 v[138:141], v[150:153], a[180:183], v[138:141]\n"
      "ds_read_b128 v[206:209], v1 offset:4096\n"
      "v_mul_f32 v122, v77, v122\n"
      "v_mfma_f32_16x16x32_bf16 v[138:141], v[182:185], a[184:187], v[138:141]\n"
      "v_permlane16_swap_b32_e64 v164, v124\n"
      "v_mfma_f32_16x16x32_bf16 v[138:141], v[178:181], a[188:191], v[138:141]\n"
      "ds_write_b64 v6, v[170:171] offset:6144\n"
      "v_mfma_f32_16x16x32_bf16 v[170:173], v[96:99], a[192:195], 0\n"
      "v_add_f32 v164, v164, v124\n"
      "v_mul_f32 v220, v76, v220\n"
      "v_mfma_f32_16x16x32_bf16 v[170:173], v[198:201], a[196:199], v[170:173]\n"
      "ds_read_b128 v[198:201], v0 offset:16384\n"
      "v_mov_b32 v124, v164\n"
      "v_mfma_f32_16x16x32_bf16 v[170:173], v[190:193], a[200:203], v[170:173]\n"
      "s_nop 0\n"
      "v_permlane32_swap_b32_e64 v164, v124\n"
      "v_mfma_f32_16x16x32_bf16 v[170:173], v[150:153], a[204:207], v[170:173]\n"
      "ds_read_b128 v[190:193], v1 offset:16384\n"
      "v_add_f32 v164, v164, v124\n"
      "v_mfma_f32_16x16x32_bf16 v[170:173], v[182:185], a[208:211], v[170:173]\n"
      "v_rsq_f32 v164, v164\n"
      "v_mfma_f32_16x16x32_bf16 v[170:173], v[178:181], a[212:215], v[170:173]\n"
      "s_waitcnt lgkmcnt(3)\n"
      "v_cvt_pk_bf16_f32 v210, v210, v211\n"
      "v_mul_f32 v212, v77, v212\n"
      "v_mfma_f32_16x16x32_bf16 v[98:101], a[0:3], v[154:157], 0\n"
      "v_mul_f32 v105, v76, v105\n"
      "v_mov_b32 v124, v164\n"
      "v_mfma_f32_16x16x32_bf16 v[98:101], a[4:7], v[206:209], v[98:101]\n"
      "s_nop 0\n"
      "v_permlane16_swap_b32_e64 v164, v124\n"
      "v_mfma_f32_16x16x32_bf16 v[98:101], a[8:11], v[186:189], v[98:101]\n"
      "s_waitcnt lgkmcnt(1)\n"
      "v_cvt_pk_bf16_f32 v211, v212, v213\n"
      "v_cmp_ne_u32 s[56:57], 0, v162\n"
      "v_mfma_f32_16x16x32_bf16 v[98:101], a[12:15], v[142:145], v[98:101]\n"
      "ds_read_u16 v78, v11\n"
      "v_mul_f32 v127, v124, v127\n"
      "v_mfma_f32_16x16x32_bf16 v[98:101], a[16:19], v[198:201], v[98:101]\n"
      "s_waitcnt lgkmcnt(1)\n"
      "v_cvt_pk_bf16_f32 v217, v172, v173\n"
      "v_mul_f32_e64 v164, s50, v164\n"
      "v_mfma_f32_16x16x32_bf16 v[98:101], a[20:23], v[190:193], v[98:101]\n"
      "v_mul_f32 v126, v124, v126\n"
      "v_mul_f32 v159, v124, v159\n"
      "v_mfma_f32_16x16x32_bf16 v[182:185], a[24:27], v[154:157], 0\n"
      "v_mul_f32_e64 v223, v76, v223\n"
      "v_cvt_pk_bf16_f32 v132, v166, v167\n"
      "v_mfma_f32_16x16x32_bf16 v[182:185], a[28:31], v[206:209], v[182:185]\n"
      "v_cvt_pk_bf16_f32 v216, v170, v171\n"
      "v_mul_f32_e64 v116, v164, v116\n"
      "v_mfma_f32_16x16x32_bf16 v[182:185], a[32:35], v[186:189], v[182:185]\n"
      "v_cvt_pk_bf16_f32 v220, v220, v221\n"
      "v_cvt_pk_bf16_f32 v120, v120, v121\n"
      "v_mfma_f32_16x16x32_bf16 v[182:185], a[36:39], v[142:145], v[182:185]\n"
      "v_mul_f32 v118, v164, v118\n"
      "v_fma_f32 v219, v98, v98, s51\n"
      "v_mfma_f32_16x16x32_bf16 v[182:185], a[40:43], v[198:201], v[182:185]\n"
      "v_cvt_pk_bf16_f32 v126, v126, v127\n"
      "v_mul_f32 v113, v77, v113\n"
      "v_mfma_f32_16x16x32_bf16 v[182:185], a[44:47], v[190:193], v[182:185]\n"
      "v_fmac_f32 v219, v99, v99\n"
      "v_mul_f32 v174, v164, v174\n"
      "v_mfma_f32_16x16x32_bf16 v[178:181], a[48:51], v[154:157], 0\n"
      "v_fmac_f32_e64 v219, v100, v100\n"
      "v_cvt_pk_bf16_f32 v221, v222, v223\n"
      "v_mfma_f32_16x16x32_bf16 v[178:181], a[52:55], v[206:209], v[178:181]\n"
      "v_fmac_f32 v219, v101, v101\n"
      "v_mul_f32 v107, v124, v107\n"
      "v_mfma_f32_16x16x32_bf16 v[178:181], a[56:59], v[186:189], v[178:181]\n"
      "v_cvt_pk_bf16_f32 v196, v138, v139\n"
      "v_mul_f32 v149, v76, v149\n"
      "v_mfma_f32_16x16x32_bf16 v[178:181], a[60:63], v[142:145], v[178:181]\n"
      "v_mul_f32 v112, v77, v112\n"
      "v_cvt_pk_bf16_f32 v146, v146, v147\n"
      "v_mfma_f32_16x16x32_bf16 v[178:181], a[64:67], v[198:201], v[178:181]\n"
      "v_mul_f32 v158, v124, v158\n"
      "v_mul_f32 v175, v164, v175\n"
      "v_mfma_f32_16x16x32_bf16 v[178:181], a[68:71], v[190:193], v[178:181]\n"
      "v_cvt_pk_bf16_f32 v223, v104, v105\n"
      "v_mul_f32 v161, v124, v161\n"
      "v_mfma_f32_16x16x32_bf16 v[150:153], a[72:75], v[154:157], 0\n"
      "v_cvt_pk_bf16_f32 v212, v112, v113\n"
      "v_mul_f32 v176, v164, v176\n"
      "v_mfma_f32_16x16x32_bf16 v[150:153], a[76:79], v[206:209], v[150:153]\n"
      "v_cvt_pk_bf16_f32 v174, v174, v175\n"
      "v_mul_f32 v108, v124, v108\n"
      "v_mfma_f32_16x16x32_bf16 v[150:153], a[80:83], v[186:189], v[150:153]\n"
      "v_mul_f32 v204, v164, v204\n"
      "v_mul_f32_e64 v177, v164, v177\n"
      "v_mfma_f32_16x16x32_bf16 v[150:153], a[84:87], v[142:145], v[150:153]\n"
      "v_mul_f32 v129, v124, v129\n"
      "v_cvt_pk_bf16_f32 v222, v102, v103\n"
      "v_mfma_f32_16x16x32_bf16 v[150:153], a[88:91], v[198:201], v[150:153]\n"
      "v_mul_f32 v123, v77, v123\n"
      "v_mul_f32 v202, v164, v202\n"
      "v_mfma_f32_16x16x32_bf16 v[150:153], a[92:95], v[190:193], v[150:153]\n"
      "v_fmac_f32 v219, v182, v182\n"
      "v_cvt_pk_bf16_f32 v175, v176, v177\n"
      "v_mfma_f32_16x16x32_bf16 v[170:173], a[96:99], v[154:157], 0\n"
      "v_cvt_pk_bf16_f32 v147, v148, v149\n"
      "v_mul_f32 v117, v164, v117\n"
      "v_mfma_f32_16x16x32_bf16 v[170:173], a[100:103], v[206:209], v[170:173]\n"
      "v_mul_f32 v128, v124, v128\n"
      "v_cvt_pk_bf16_f32 v121, v122, v123\n"
      "v_mfma_f32_16x16x32_bf16 v[170:173], a[104:107], v[186:189], v[170:173]\n"
      "v_mul_f32 v203, v164, v203\n"
      "v_fmac_f32 v219, v183, v183\n"
      "v_mfma_f32_16x16x32_bf16 v[170:173], a[108:111], v[142:145], v[170:173]\n"
      "v_cvt_pk_bf16_f32 v213, v114, v115\n"
      "v_cvt_pk_bf16_f32 v133, v168, v169\n"
      "v_mfma_f32_16x16x32_bf16 v[170:173], a[112:115], v[198:201], v[170:173]\n"
      "v_fmac_f32 v219, v184, v184\n"
      "v_mul_f32 v205, v164, v205\n"
      "v_mfma_f32_16x16x32_bf16 v[170:173], a[116:119], v[190:193], v[170:173]\n"
      "v_fmac_f32 v219, v185, v185\n"
      "v_fma_f32 v115, v150, v150, s51\n"
      "v_mfma_f32_16x16x32_bf16 v[102:105], a[120:123], v[154:157], 0\n"
      "v_fmac_f32 v115, v151, v151\n"
      "v_mul_f32_e64 v119, v164, v119\n"
      "v_mfma_f32_16x16x32_bf16 v[102:105], a[124:127], v[206:209], v[102:105]\n"
      "v_fmac_f32 v115, v152, v152\n"
      "v_cvt_pk_bf16_f32 v127, v128, v129\n"
      "v_mfma_f32_16x16x32_bf16 v[102:105], a[128:131], v[186:189], v[102:105]\n"
      "v_fmac_f32 v219, v178, v178\n"
      "v_cvt_pk_bf16_f32 v197, v140, v141\n"
      "v_mfma_f32_16x16x32_bf16 v[102:105], a[132:135], v[142:145], v[102:105]\n"
      "v_cvt_pk_bf16_f32 v176, v202, v203\n"
      "v_fmac_f32_e64 v115, v153, v153\n"
      "v_mfma_f32_16x16x32_bf16 v[102:105], a[136:139], v[198:201], v[102:105]\n"
      "v_fmac_f32 v115, v170, v170\n"
      "v_mul_f32 v106, v124, v106\n"
      "v_mfma_f32_16x16x32_bf16 v[102:105], a[140:143], v[190:193], v[102:105]\n"
      "v_cvt_pk_bf16_f32 v106, v106, v107\n"
      "v_cvt_pk_bf16_f32 v116, v116, v117\n"
      "v_mfma_f32_16x16x32_bf16 v[166:169], v[154:157], a[144:147], 0\n"
      "ds_read_b128 v[162:165], v2 offset:24576\n"
      "v_fmac_f32_e64 v115, v171, v171\n"
      "v_mfma_f32_16x16x32_bf16 v[166:169], v[206:209], a[148:151], v[166:169]\n"
      "v_fmac_f32 v115, v172, v172\n"
      "v_mul_f32 v109, v124, v109\n"
      "v_mfma_f32_16x16x32_bf16 v[166:169], v[186:189], a[152:155], v[166:169]\n"
      "v_fmac_f32_e64 v115, v173, v173\n"
      "v_cvt_pk_bf16_f32 v107, v108, v109\n"
      "v_mfma_f32_16x16x32_bf16 v[166:169], v[142:145], a[156:159], v[166:169]\n"
      "ds_read_b128 v[108:111], v2 offset:18432\n"
      "v_fmac_f32_e64 v219, v179, v179\n"
      "v_mfma_f32_16x16x32_bf16 v[166:169], v[198:201], a[160:163], v[166:169]\n"
      "v_cvt_pk_bf16_f32 v128, v158, v159\n"
      "v_fmac_f32 v115, v102, v102\n"
      "v_mfma_f32_16x16x32_bf16 v[166:169], v[190:193], a[164:167], v[166:169]\n"
      "v_fmac_f32 v115, v103, v103\n"
      "v_fmac_f32 v219, v180, v180\n"
      "v_mfma_f32_16x16x32_bf16 v[138:141], v[154:157], a[168:171], 0\n"
      "v_cvt_pk_bf16_f32 v177, v204, v205\n"
      "v_mul_f32 v160, v124, v160\n"
      "v_mfma_f32_16x16x32_bf16 v[138:141], v[206:209], a[172:175], v[138:141]\n"
      "v_cvt_pk_bf16_f32 v117, v118, v119\n"
      "v_fmac_f32 v115, v104, v104\n"
      "v_mfma_f32_16x16x32_bf16 v[138:141], v[186:189], a[176:179], v[138:141]\n"
      "v_fmac_f32 v219, v181, v181\n"
      "v_fmac_f32_e64 v115, v105, v105\n"
      "v_mfma_f32_16x16x32_bf16 v[138:141], v[142:145], a[180:183], v[138:141]\n"
      "s_nop 0\n"
      "v_permlane16_swap_b32 v219, v115\n"
      "v_mfma_f32_16x16x32_bf16 v[138:141], v[198:201], a[184:187], v[138:141]\n"
      "s_add_u32 s43, s43, 1\n"
      "v_add_f32 v219, v219, v115\n"
      "v_mov_b32_e64 v115, v219\n"
      "v_mfma_f32_16x16x32_bf16 v[138:141], v[190:193], a[188:191], v[138:141]\n"
      "ds_read_b128 v[202:205], v2 offset:21504\n"
      "v_cvt_pk_bf16_f32 v129, v160, v161\n"
      "v_mfma_f32_16x16x32_bf16 v[158:161], v[154:157], a[192:195], 0\n"
      "v_permlane32_swap_b32_e64 v219, v115\n"
      "v_mfma_f32_16x16x32_bf16 v[158:161], v[206:209], a[196:199], v[158:161]\n"
      "ds_read_b128 v[122:125], v2 offset:30720\n"
      "v_cvt_pk_bf16_f32 v77, v168, v169\n"
      "v_mfma_f32_16x16x32_bf16 v[158:161], v[186:189], a[200:203], v[158:161]\n"
      "ds_read_b128 v[186:189], v2 offset:33792\n"
      "v_cvt_pk_bf16_f32 v76, v166, v167\n"
      "v_mfma_f32_16x16x32_bf16 v[158:161], v[142:145], a[204:207], v[158:161]\n"
      "ds_read_b128 v[206:209], v2 offset:27648\n"
      "v_add_f32 v219, v219, v115\n"
      "v_mfma_f32_16x16x32_bf16 v[158:161], v[198:201], a[208:211], v[158:161]\n"
      "v_rsq_f32 v219, v219\n"
      "v_mfma_f32_16x16x32_bf16 v[158:161], v[190:193], a[212:215], v[158:161]\n"
      "s_waitcnt lgkmcnt(3)\n"
      "v_mov_b32 v115, v219\n"
      "v_cvt_pk_bf16_f32 v80, v138, v139\n"
      "v_mfma_f32_16x16x32_bf16 v[166:169], a[216:219], v[108:111], 0\n"
      "v_permlane16_swap_b32 v219, v115\n"
      "v_mfma_f32_16x16x32_bf16 v[166:169], a[220:223], v[202:205], v[166:169]\n"
      "v_mul_f32 v219, s50, v219\n"
      "v_mul_f32 v185, v219, v185\n"
      "v_mfma_f32_16x16x32_bf16 v[166:169], a[224:227], v[162:165], v[166:169]\n"
      "s_and_b32 s86, s86, -4\n"
      "v_mul_f32 v153, v115, v153\n"
      "v_mul_f32 v171, v115, v171\n"
      "v_mfma_f32_16x16x32_bf16 v[198:201], a[240:243], v[108:111], 0\n"
      "v_mul_f32 v105, v115, v105\n"
      "v_mul_f32 v181, v219, v181\n"
      "v_mfma_f32_16x16x32_bf16 v[198:201], a[244:247], v[202:205], v[198:201]\n"
      "v_mul_f32 v99, v219, v99\n"
      "v_mul_f32 v150, v115, v150\n"
      "v_mfma_f32_16x16x32_bf16 v[198:201], a[248:251], v[162:165], v[198:201]\n"
      "ds_read_u16 v86, v11 offset:64\n"
      "v_mul_f32_e64 v98, v219, v98\n"
      "v_mfma_f32_16x16x32_bf16 v[142:145], v[232:235], v[108:111], 0\n"
      "v_mul_f32 v103, v115, v103\n"
      "v_mul_f32_e64 v184, v219, v184\n"
      "v_mfma_f32_16x16x32_bf16 v[142:145], v[236:239], v[202:205], v[142:145]\n"
      "s_waitcnt lgkmcnt(1)\n"
      "v_cvt_pk_bf16_f32 v81, v140, v141\n"
      "v_mul_f32 v183, v219, v183\n"
      "v_mfma_f32_16x16x32_bf16 v[142:145], v[240:243], v[162:165], v[142:145]\n"
      "ds_read_b128 v[190:193], v10 offset:32\n"
      "v_mul_f32 v151, v115, v151\n"
      "v_mfma_f32_16x16x32_bf16 v[166:169], a[228:231], v[206:209], v[166:169]\n"
      "ds_read_b128 v[108:111], v2 offset:18688\n"
      "v_mul_f32_e64 v173, v115, v173\n"
      "v_mfma_f32_16x16x32_bf16 v[166:169], a[232:235], v[122:125], v[166:169]\n"
      "ds_read_b128 v[202:205], v2 offset:21760\n"
      "v_cvt_pk_bf16_f32 v150, v150, v151\n"
      "v_mfma_f32_16x16x32_bf16 v[166:169], a[236:239], v[186:189], v[166:169]\n"
      "v_mul_f32_e64 v152, v115, v152\n"
      "v_cvt_pk_bf16_f32 v84, v158, v159\n"
      "v_mfma_f32_16x16x32_bf16 v[198:201], a[252:255], v[206:209], v[198:201]\n"
      "v_mul_f32 v172, v115, v172\n"
      "v_mul_f32 v101, v219, v101\n"
      "v_mfma_f32_16x16x32_bf16 v[198:201], v[224:227], v[122:125], v[198:201]\n"
      "v_mul_f32 v178, v219, v178\n"
      "v_cvt_pk_bf16_f32 v151, v152, v153\n"
      "v_mfma_f32_16x16x32_bf16 v[198:201], v[228:231], v[186:189], v[198:201]\n"
      "ds_read_b64 v[96:97], v10 offset:24\n"
      "v_mul_f32 v102, v115, v102\n"
      "v_mfma_f32_16x16x32_bf16 v[142:145], v[244:247], v[206:209], v[142:145]\n"
      "v_cvt_pk_bf16_f32 v85, v160, v161\n"
      "v_mul_f32 v170, v115, v170\n"
      "v_mfma_f32_16x16x32_bf16 v[142:145], v[248:251], v[122:125], v[142:145]\n"
      "v_cvt_pk_bf16_f32 v102, v102, v103\n"
      "v_mul_f32 v104, v115, v104\n"
      "v_mfma_f32_16x16x32_bf16 v[142:145], v[252:255], v[186:189], v[142:145]\n"
      "ds_read_u16 v82, v11 offset:32\n"
      "v_cvt_pk_bf16_f32 v153, v172, v173\n"
      "v_mfma_f32_16x16x32_bf16 v[186:189], v[220:223], v[210:213], v[40:43]\n"
      "ds_read_b128 v[158:161], v2 offset:24832\n"
      "v_cvt_pk_bf16_f32 v152, v170, v171\n"
      "v_mfma_f32_16x16x32_bf16 v[206:209], v[126:129], v[210:213], v[44:47]\n"
      "s_waitcnt lgkmcnt(3)\n"
      "v_cvt_pk_bf16_f32 v98, v98, v99\n"
      "v_cvt_pk_bf16_f32 v103, v104, v105\n"
      "v_mfma_f32_16x16x32_bf16 v[154:157], v[150:153], v[210:213], v[48:51]\n"
      "s_cmp_ge_u32 s95, 0x40000000\n"
      "s_cselect_b32 s94, -1, -2\n"
      "v_mul_f32 v182, v219, v182\n"
      "v_mfma_f32_16x16x32_bf16 v[138:141], v[190:193], v[210:213], v[24:27]\n"
      "s_waitcnt lgkmcnt(2)\n"
      "s_waitcnt vmcnt(1)\n"
      "v_or3_b32 v134, v134, v135, v136\n"
      "v_mul_f32 v100, v219, v100\n"
      "v_mfma_f32_16x16x16_bf16 v[186:189], v[146:147], v[120:121], v[186:189]\n"
      "v_fma_f32 v218, v166, v166, s52\n"
      "v_cvt_pk_bf16_f32 v99, v100, v101\n"
      "v_mul_f32 v180, v219, v180\n"
      "v_mfma_f32_16x16x16_bf16 v[206:209], v[106:107], v[120:121], v[206:209]\n"
      "v_mul_f32_e64 v179, v219, v179\n"
      "v_cvt_pk_bf16_f32 v178, v178, v179\n"
      "v_cvt_pk_bf16_f32 v179, v180, v181\n"
      "v_mfma_f32_16x16x16_bf16 v[154:157], v[102:103], v[120:121], v[154:157]\n"
      "v_exp_f32 v186, v186\n"
      "v_cvt_pk_bf16_f32 v100, v182, v183\n"
      "v_mfma_f32_16x16x16_bf16 v[138:141], v[96:97], v[120:121], v[138:141]\n"
      "v_exp_f32 v187, v187\n"
      "v_mul_f32 v219, v167, v167\n"
      "v_mfma_f32_16x16x32_bf16 v[162:165], a[216:219], v[108:111], 0\n"
      "v_exp_f32 v206, v206\n"
      "v_mfma_f32_16x16x32_bf16 v[162:165], a[220:223], v[202:205], v[162:165]\n"
      "s_waitcnt lgkmcnt(0)\n"
      "v_exp_f32 v209, v209\n"
      "v_mfma_f32_16x16x32_bf16 v[162:165], a[224:227], v[158:161], v[162:165]\n"
      "v_exp_f32 v189, v189\n"
      "v_mfma_f32_16x16x32_bf16 v[180:183], a[240:243], v[108:111], 0\n"
      "v_exp_f32 v154, v154\n"
      "v_mfma_f32_16x16x32_bf16 v[180:183], a[244:247], v[202:205], v[180:183]\n"
      "v_exp_f32 v155, v155\n"
      "v_mfma_f32_16x16x32_bf16 v[180:183], a[248:251], v[158:161], v[180:183]\n"
      "v_cvt_pk_bf16_f32 v186, v186, v187\n"
      "s_waitcnt vmcnt(0)\n"
      "v_or3_b32 v134, v134, v137, v88\n"
      "v_mfma_f32_16x16x32_bf16 v[170:173], v[232:235], v[108:111], 0\n"
      "v_exp_f32_e64 v157, v157\n"
      "v_mfma_f32_16x16x32_bf16 v[170:173], v[236:239], v[202:205], v[170:173]\n"
      "ds_read_b128 v[210:213], v2 offset:30976\n"
      "v_cvt_pk_bf16_f32 v88, v154, v155\n"
      "v_mfma_f32_16x16x32_bf16 v[170:173], v[240:243], v[158:161], v[170:173]\n"
      "v_exp_f32 v156, v156\n"
      "v_mfma_f32_16x16x32_bf16 v[118:121], v[220:223], v[174:177], v[52:55]\n"
      "v_exp_f32 v138, v138\n"
      "v_mfma_f32_16x16x32_bf16 v[108:111], v[126:129], v[174:177], v[56:59]\n"
      "v_exp_f32 v208, v208\n"
      "v_mfma_f32_16x16x32_bf16 v[202:205], v[150:153], v[174:177], v[60:63]\n"
      "v_exp_f32 v207, v207\n"
      "v_mfma_f32_16x16x32_bf16 v[122:125], v[190:193], v[174:177], v[26:29]\n"
      "s_cmp_gt_u32 s42, s44\n"
      "s_cselect_b32 s95, s94, s95\n"
      "v_cvt_pk_bf16_f32 v89, v156, v157\n"
      "v_mfma_f32_16x16x16_bf16 v[118:121], v[146:147], v[116:117], v[118:121]\n"
      "v_fmac_f32 v219, v169, v169\n"
      "v_exp_f32 v188, v188\n"
      "v_mfma_f32_16x16x16_bf16 v[108:111], v[106:107], v[116:117], v[108:111]\n"
      "v_cvt_pk_bf16_f32 v187, v188, v189\n"
      "v_cvt_pk_bf16_f32 v188, v206, v207\n"
      "v_fmac_f32_e64 v218, v168, v168\n"
      "v_mfma_f32_16x16x16_bf16 v[202:205], v[102:103], v[116:117], v[202:205]\n"
      "v_cvt_pk_bf16_f32 v189, v208, v209\n"
      "v_cvt_pk_bf16_f32 v101, v184, v185\n"
      "v_cvt_pk_bf16_f32 v90, v138, 0\n"
      "v_mfma_f32_16x16x16_bf16 v[122:125], v[96:97], v[116:117], v[122:125]\n"
      "v_exp_f32 v119, v119\n"
      "v_exp_f32 v109, v109\n"
      "v_mfma_f32_16x16x32_bf16 v[174:177], v[32:35], v[186:189], 0\n"
      "v_exp_f32_e64 v108, v108\n"
      "v_mfma_f32_16x16x32_bf16 v[174:177], v[32:35], v[88:91], v[174:177]\n"
      "v_exp_f32_e64 v118, v118\n"
      "v_mfma_f32_16x16x32_bf16 v[136:139], v[130:133], v[186:189], 0\n"
      "ds_write_b32 v31, v134 offset:4608\n"
      "v_mfma_f32_16x16x32_bf16 v[136:139], v[76:79], v[88:91], v[136:139]\n"
      "v_exp_f32 v120, v120\n"
      "v_mfma_f32_16x16x32_bf16 v[114:117], v[194:197], v[186:189], 0\n"
      "v_exp_f32 v121, v121\n"
      "v_mfma_f32_16x16x32_bf16 v[114:117], v[80:83], v[88:91], v[114:117]\n"
      "ds_read_b128 v[158:161], v2 offset:27904\n"
      "v_cvt_pk_bf16_f32 v118, v118, v119\n"
      "v_mfma_f32_16x16x32_bf16 v[154:157], v[214:217], v[186:189], 0\n"
      "v_exp_f32_e64 v110, v110\n"
      "v_mfma_f32_16x16x32_bf16 v[154:157], v[84:87], v[88:91], v[154:157]\n"
      "v_cvt_pk_bf16_f32 v119, v120, v121\n"
      "v_cvt_pk_bf16_f32 v120, v108, v109\n"
      "v_mfma_f32_16x16x32_bf16 v[186:189], v[220:223], v[98:101], v[64:67]\n"
      "v_exp_f32 v111, v111\n"
      "v_mfma_f32_16x16x32_bf16 v[206:209], v[126:129], v[98:101], v[68:71]\n"
      "v_cvt_pk_bf16_f32 v121, v110, v111\n"
      "v_rcp_f32 v174, v174\n"
      "v_exp_f32_e64 v204, v204\n"
      "v_pk_mul_f32 v[154:155], v[154:155], v[174:175] op_sel_hi:[1,0]\n"
      "v_pk_mul_f32 v[116:117], v[116:117], v[174:175] op_sel_hi:[1,0]\n"
      "v_pk_mul_f32 v[136:137], v[136:137], v[174:175] op_sel_hi:[1,0]\n"
      "v_pk_mul_f32 v[138:139], v[138:139], v[174:175] op_sel_hi:[1,0]\n"
      "v_pk_mul_f32 v[156:157], v[156:157], v[174:175] op_sel_hi:[1,0]\n"
      "v_pk_mul_f32 v[114:115], v[114:115], v[174:175] op_sel_hi:[1,0]\n"
      "v_mfma_f32_16x16x32_bf16 v[174:177], v[150:153], v[98:101], v[72:75]\n"
      "s_waitcnt lgkmcnt(0)\n"
      "v_exp_f32 v122, v122\n"
      "v_mfma_f32_16x16x32_bf16 v[108:111], v[190:193], v[98:101], v[28:31]\n"
      "ds_read_b128 v[98:101], v2 offset:34048\n"
      "v_cvt_pk_bf16_f32 v154, v154, v155\n"
      "v_mfma_f32_16x16x16_bf16 v[186:189], v[146:147], v[178:179], v[186:189]\n"
      "v_exp_f32 v203, v203\n"
      "v_cvt_pk_bf16_f32 v136, v136, v137\n"
      "v_mfma_f32_16x16x16_bf16 v[206:209], v[106:107], v[178:179], v[206:209]\n"
      "v_cvt_pk_bf16_f32 v137, v138, v139\n"
      "v_exp_f32 v202, v202\n"
      "v_mfma_f32_16x16x16_bf16 v[174:177], v[102:103], v[178:179], v[174:177]\n"
      "v_cvt_pk_bf16_f32 v139, v116, v117\n"
      "v_cvt_pk_bf16_f32 v155, v156, v157\n"
      "v_cvt_pk_bf16_f32 v92, v202, v203\n"
      "v_mfma_f32_16x16x16_bf16 v[108:111], v[96:97], v[178:179], v[108:111]\n"
      "v_cvt_pk_bf16_f32 v94, v122, 0\n"
      "v_exp_f32 v206, v206\n"
      "v_mfma_f32_16x16x32_bf16 v[162:165], a[228:231], v[158:161], v[162:165]\n"
      "s_nop 0\n"
      "v_exp_f32 v176, v176\n"
      "v_mfma_f32_16x16x32_bf16 v[162:165], a[232:235], v[210:213], v[162:165]\n"
      "s_waitcnt lgkmcnt(0)\n"
      "v_exp_f32 v186, v186\n"
      "v_mfma_f32_16x16x32_bf16 v[162:165], a[236:239], v[98:101], v[162:165]\n"
      "v_exp_f32 v208, v208\n"
      "v_mfma_f32_16x16x32_bf16 v[180:183], a[252:255], v[158:161], v[180:183]\n"
      "ds_read_b128 v[122:125], v10\n"
      "v_cvt_pk_bf16_f32 v138, v114, v115\n"
      "v_mfma_f32_16x16x32_bf16 v[180:183], v[224:227], v[210:213], v[180:183]\n"
      "v_exp_f32 v205, v205\n"
      "v_mfma_f32_16x16x32_bf16 v[180:183], v[228:231], v[98:101], v[180:183]\n"
      "v_exp_f32 v188, v188\n"
      "v_mfma_f32_16x16x32_bf16 v[170:173], v[244:247], v[158:161], v[170:173]\n"
      "v_exp_f32_e64 v187, v187\n"
      "v_mfma_f32_16x16x32_bf16 v[170:173], v[248:251], v[210:213], v[170:173]\n"
      "ds_read_b128 v[210:213], v7 offset:18432\n"
      "v_cvt_pk_bf16_f32 v93, v204, v205\n"
      "v_mfma_f32_16x16x32_bf16 v[170:173], v[252:255], v[98:101], v[170:173]\n"
      "v_exp_f32 v207, v207\n"
      "v_mfma_f32_16x16x32_bf16 v[114:117], v[32:35], v[118:121], 0\n"
      "v_exp_f32 v108, v108\n"
      "v_mfma_f32_16x16x32_bf16 v[114:117], v[32:35], v[92:95], v[114:117]\n"
      "ds_read_b64 v[88:89], v10 offset:16\n"
      "v_fma_f32 v160, v162, v162, s52\n"
      "v_mfma_f32_16x16x32_bf16 v[98:101], v[130:133], v[118:121], 0\n"
      "ds_read_b128 v[110:113], v7 offset:22528\n"
      "v_mul_f32_e64 v161, v163, v163\n"
      "v_mfma_f32_16x16x32_bf16 v[98:101], v[76:79], v[92:95], v[98:101]\n"
      "ds_write_b128 v3, v[136:139]\n"
      "v_mfma_f32_16x16x32_bf16 v[134:137], v[194:197], v[118:121], 0\n"
      "s_waitcnt lgkmcnt(3)\n"
      "v_rcp_f32 v114, v114\n"
      "v_mfma_f32_16x16x32_bf16 v[134:137], v[80:83], v[92:95], v[134:137]\n"
      "buffer_store_dwordx4 v[210:213], v20, s[24:27], s46 offen sc1\n"
      "v_pk_fma_f32 v[218:219], v[198:199], v[198:199], v[218:219]\n"
      "v_pk_fma_f32 v[160:161], v[164:165], v[164:165], v[160:161]\n"
      "v_mfma_f32_16x16x32_bf16 v[210:213], v[214:217], v[118:121], 0\n"
      "v_exp_f32 v189, v189\n"
      "v_mfma_f32_16x16x32_bf16 v[210:213], v[84:87], v[92:95], v[210:213]\n"
      "ds_read_b128 v[138:141], v2 offset:22016\n"
      "v_fmac_f32 v160, v180, v180\n"
      "v_mfma_f32_16x16x32_bf16 v[202:205], v[122:125], v[220:223], v[30:33]\n"
      "v_exp_f32 v177, v177\n"
      "v_mfma_f32_16x16x32_bf16 v[116:119], v[122:125], v[126:129], v[30:33]\n"
      "s_waitcnt lgkmcnt(1)\n"
      "v_exp_f32_e64 v175, v175\n"
      "v_mfma_f32_16x16x32_bf16 v[220:223], v[122:125], v[150:153], v[30:33]\n"
      "ds_read_b128 v[148:151], v2 offset:18944\n"
      "v_cvt_pk_bf16_f32 v186, v186, v187\n"
      "v_mfma_f32_16x16x32_bf16 v[156:159], v[122:125], v[190:193], v[30:33]\n"
      "ds_read_b128 v[190:193], v7 offset:26624\n"
      "v_fmac_f32_e64 v161, v181, v181\n"
      "v_mfma_f32_16x16x16_bf16 v[202:205], v[88:89], v[146:147], v[202:205]\n"
      "ds_read_b128 v[122:125], v2 offset:25088\n"
      "v_cvt_pk_bf16_f32 v90, v108, 0\n"
      "v_mfma_f32_16x16x16_bf16 v[116:119], v[88:89], v[106:107], v[116:119]\n"
      "buffer_store_dwordx4 v[110:113], v21, s[24:27], s46 offen sc1\n"
      "v_pk_mul_f32 v[98:99], v[98:99], v[114:115] op_sel_hi:[1,0]\n"
      "v_pk_mul_f32 v[210:211], v[210:211], v[114:115] op_sel_hi:[1,0]\n"
      "v_pk_mul_f32 v[136:137], v[136:137], v[114:115] op_sel_hi:[1,0]\n"
      "v_pk_mul_f32 v[212:213], v[212:213], v[114:115] op_sel_hi:[1,0]\n"
      "v_pk_fma_f32 v[218:219], v[200:201], v[200:201], v[218:219]\n"
      "v_pk_fma_f32 v[160:161], v[182:183], v[182:183], v[160:161]\n"
      "v_pk_fma_f32 v[160:161], v[170:171], v[170:171], v[160:161]\n"
      "v_pk_fma_f32 v[218:219], v[142:143], v[142:143], v[218:219]\n"
      "v_mfma_f32_16x16x16_bf16 v[220:223], v[88:89], v[102:103], v[220:223]\n"
      "v_exp_f32 v174, v174\n"
      "v_cvt_pk_bf16_f32 v187, v188, v189\n"
      "v_mfma_f32_16x16x16_bf16 v[156:159], v[88:89], v[96:97], v[156:159]\n"
      "v_fmac_f32 v219, v145, v145\n"
      "v_cvt_pk_bf16_f32 v210, v210, v211\n"
      "s_waitcnt lgkmcnt(0)\n"
      "v_mfma_f32_16x16x32_bf16 v[126:129], a[216:219], v[148:151], 0\n"
      "v_exp_f32 v209, v209\n"
      "v_mfma_f32_16x16x32_bf16 v[126:129], a[220:223], v[138:141], v[126:129]\n"
      "ds_read_b128 v[102:105], v7 offset:30720\n"
      "v_cndmask_b32 v202, v202, v116, s[58:59]\n"
      "v_mfma_f32_16x16x32_bf16 v[126:129], a[224:227], v[122:125], v[126:129]\n"
      "ds_write_b64 v4, v[154:155]\n"
      "v_mfma_f32_16x16x32_bf16 v[116:119], a[240:243], v[148:151], 0\n"
      "ds_read_b128 v[152:155], v2 offset:34304\n"
      "v_cvt_pk_bf16_f32 v89, v176, v177\n"
      "v_mfma_f32_16x16x32_bf16 v[116:119], a[244:247], v[138:141], v[116:119]\n"
      "buffer_store_dwordx4 v[190:193], v22, s[24:27], s46 offen sc1\n"
      "v_pk_fma_f32 v[160:161], v[172:173], v[172:173], v[160:161]\n"
      "v_mfma_f32_16x16x32_bf16 v[116:119], a[248:251], v[122:125], v[116:119]\n"
      "ds_read_b128 v[190:193], v2 offset:31232\n"
      "v_cndmask_b32 v202, v202, v220, s[60:61]\n"
      "v_mfma_f32_16x16x32_bf16 v[108:111], v[232:235], v[148:151], 0\n"
      "ds_read_b128 v[220:223], v2 offset:28160\n"
      "v_cndmask_b32 v202, v202, v156, s[62:63]\n"
      "v_mfma_f32_16x16x32_bf16 v[108:111], v[236:239], v[138:141], v[108:111]\n"
      "v_cvt_pk_bf16_f32 v189, v208, v209\n"
      "v_cvt_pk_bf16_f32 v188, v206, v207\n"
      "v_mfma_f32_16x16x32_bf16 v[108:111], v[240:243], v[122:125], v[108:111]\n"
      "v_cvt_pk_bf16_f32 v88, v174, v175\n"
      "v_add_f32 v160, v160, v161\n"
      "v_mfma_f32_16x16x32_bf16 v[206:209], v[32:35], v[186:189], 0\n"
      "v_exp_f32 v202, v202\n"
      "v_mfma_f32_16x16x32_bf16 v[206:209], v[32:35], v[88:91], v[206:209]\n"
      "v_cvt_pk_bf16_f32 v202, v202, v202\n"
      "v_fmac_f32_e64 v218, v144, v144\n"
      "v_mfma_f32_16x16x32_bf16 v[122:125], v[130:133], v[186:189], 0\n"
      "ds_write_b16 v13, v202\n"
      "v_mfma_f32_16x16x32_bf16 v[122:125], v[76:79], v[88:91], v[122:125]\n"
      "v_cvt_pk_bf16_f32 v211, v212, v213\n"
      "v_add_f32_e64 v218, v218, v219\n"
      "v_mfma_f32_16x16x32_bf16 v[148:151], v[194:197], v[186:189], 0\n"
      "ds_write_b64 v4, v[210:211] offset:256\n"
      "v_mfma_f32_16x16x32_bf16 v[148:151], v[80:83], v[88:91], v[148:151]\n"
      "v_rcp_f32 v206, v206\n"
      "v_mfma_f32_16x16x32_bf16 v[156:159], v[214:217], v[186:189], 0\n"
      "s_waitcnt lgkmcnt(2)\n"
      "v_permlane16_swap_b32_e64 v218, v160\n"
      "v_mfma_f32_16x16x32_bf16 v[156:159], v[84:87], v[88:91], v[156:159]\n"
      "ds_read_b128 v[174:177], v14\n"
      "v_cvt_pk_bf16_f32 v98, v98, v99\n"
      "v_mfma_f32_16x16x32_bf16 v[126:129], a[228:231], v[220:223], v[126:129]\n"
      "buffer_store_dwordx4 v[102:105], v23, s[24:27], s46 offen sc1\n"
      "v_pk_mul_f32 v[148:149], v[148:149], v[206:207] op_sel_hi:[1,0]\n"
      "v_pk_mul_f32 v[100:101], v[100:101], v[114:115] op_sel_hi:[1,0]\n"
      "v_pk_mul_f32 v[134:135], v[134:135], v[114:115] op_sel_hi:[1,0]\n"
      "v_pk_mul_f32 v[122:123], v[122:123], v[206:207] op_sel_hi:[1,0]\n"
      "v_mfma_f32_16x16x32_bf16 v[126:129], a[232:235], v[190:193], v[126:129]\n"
      "ds_read_b128 v[102:105], v7 offset:34816\n"
      "v_cvt_pk_bf16_f32 v99, v100, v101\n"
      "v_mfma_f32_16x16x32_bf16 v[126:129], a[236:239], v[152:155], v[126:129]\n"
      "v_cvt_pk_bf16_f32 v100, v134, v135\n"
      "v_cvt_pk_bf16_f32 v101, v136, v137\n"
      "v_mfma_f32_16x16x32_bf16 v[116:119], a[252:255], v[220:223], v[116:119]\n"
      "ds_write_b128 v3, v[98:101] offset:256\n"
      "v_pk_mul_f32 v[150:151], v[150:151], v[206:207] op_sel_hi:[1,0]\n"
      "v_pk_mul_f32 v[124:125], v[124:125], v[206:207] op_sel_hi:[1,0]\n"
      "v_mfma_f32_16x16x32_bf16 v[116:119], v[224:227], v[190:193], v[116:119]\n"
      "s_nop 0\n"
      "v_fma_f32 v136, v126, v126, s52\n"
      "v_cvt_pk_bf16_f32 v122, v122, v123\n"
      "v_mfma_f32_16x16x32_bf16 v[116:119], v[228:231], v[152:155], v[116:119]\n"
      "v_mul_f32 v137, v127, v127\n"
      "v_cvt_pk_bf16_f32 v123, v124, v125\n"
      "v_mfma_f32_16x16x32_bf16 v[108:111], v[244:247], v[220:223], v[108:111]\n"
      "ds_read_b128 v[210:213], v14 offset:64\n"
      "v_cvt_pk_bf16_f32 v124, v148, v149\n"
      "v_mfma_f32_16x16x32_bf16 v[108:111], v[248:251], v[190:193], v[108:111]\n"
      "s_waitcnt lgkmcnt(2)\n"
      "v_add_f32 v218, v218, v160\n"
      "v_cvt_pk_bf16_f32 v125, v150, v151\n"
      "v_mfma_f32_16x16x32_bf16 v[108:111], v[252:255], v[152:155], v[108:111]\n"
      "ds_write_b128 v3, v[122:125] offset:512\n"
      "v_pk_fma_f32 v[136:137], v[128:129], v[128:129], v[136:137]\n"
      "v_pk_fma_f32 v[136:137], v[116:117], v[116:117], v[136:137]\n"
      "v_pk_mul_f32 v[156:157], v[156:157], v[206:207] op_sel_hi:[1,0]\n"
      "v_cvt_pk_bf16_f32 v156, v156, v157\n"
      "v_pk_fma_f32 v[136:137], v[118:119], v[118:119], v[136:137]\n"
      "v_pk_mul_f32 v[158:159], v[158:159], v[206:207] op_sel_hi:[1,0]\n"
      "v_cvt_pk_bf16_f32 v157, v158, v159\n"
      "v_pk_fma_f32 v[136:137], v[108:109], v[108:109], v[136:137]\n"
      "v_pk_fma_f32 v[136:137], v[110:111], v[110:111], v[136:137]\n"
      "v_add_f32 v136, v136, v137\n"
      "v_mov_b32 v160, v136\n"
      "v_mfma_f32_16x16x32_bf16 v[188:191], v[174:177], v[32:35], 0\n"
      "ds_write_b64 v4, v[156:157] offset:512\n"
      "v_permlane16_swap_b32_e64 v136, v160\n"
      "v_add_f32_e64 v136, v136, v160\n"
      "s_cmp_ge_u32 s95, 0x40000000\n"
      "s_cselect_b64 vcc, -1, s[56:57]\n"
      "v_permlane32_swap_b32 v218, v136\n"
      "v_add_f32_e64 v218, v218, v136\n"
      "s_add_u32 s42, s42, 1\n"
      "ds_write_b32 v31, v218\n"
      "buffer_store_dwordx4 v[102:105], v25, s[24:27], s46 offen sc1\n"
      "s_waitcnt lgkmcnt(2)\n"
      "v_mfma_f32_16x16x32_bf16 v[188:191], v[210:213], v[32:35], v[188:191]\n"
      "s_waitcnt lgkmcnt(0)\n"
      "s_barrier\n"
      "s_cbranch_vccz L_loop1_%=\n"
      "s_branch L_disp1_%=\n"
      "s_nop 7\n"
      "s_nop 7\n"
      "L_max1_%=:\n"
      "v_mfma_f32_16x16x32_bf16 v[218:221], v[174:177], v[130:133], 0\n"
      "ds_read_b128 v[150:153], v0 offset:18432\n"
      "s_add_u32 m0, s64, 0\n"
      "v_mfma_f32_16x16x32_bf16 v[218:221], v[210:213], v[76:79], v[218:221]\n"
      "ds_read_b128 v[146:149], v9\n"
      "ds_read_b128 v[122:125], v0 offset:24576\n"
      "v_mfma_f32_16x16x32_bf16 v[134:137], v[174:177], v[194:197], 0\n"
      "ds_read_b128 v[96:99], v1 offset:18432\n"
      "v_readlane_b32 s45, v37, s43\n"
      "v_mfma_f32_16x16x32_bf16 v[134:137], v[210:213], v[80:83], v[134:137]\n"
      "buffer_load_dwordx4 v16, s[16:19], s84 offen  lds\n"
      "v_mfma_f32_16x16x32_bf16 v[100:103], v[174:177], v[214:217], 0\n"
      "ds_read_b128 v[220:223], v1 offset:24576\n"
      "v_readlane_b32 s46, v37, s42\n"
      "v_mfma_f32_16x16x32_bf16 v[100:103], v[210:213], v[84:87], v[100:103]\n"
      "ds_read_b128 v[174:177], v0 offset:30720\n"
      "s_waitcnt lgkmcnt(5)\n"
      "v_mfma_f32_16x16x32_bf16 v[210:213], a[0:3], v[150:153], 0\n"
      "ds_read_b128 v[138:141], v0 offset:26624\n"
      "v_cndmask_b32 v218, v218, v134, s[58:59]\n"
      "v_mfma_f32_16x16x32_bf16 v[112:115], a[24:27], v[150:153], 0\n"
      "s_waitcnt lgkmcnt(5)\n"
      "v_add_f32 v148, v148, v149\n"
      "v_add_f32 v146, v146, v147\n"
      "v_mfma_f32_16x16x32_bf16 v[202:205], a[48:51], v[150:153], 0\n"
      "s_add_u32 m0, s64, 4096\n"
      "v_mov_b32_dpp v188, v188 quad_perm:[0,0,0,0] row_mask:0xf bank_mask:0xf\n"
      "v_add_f32 v146, v146, v148\n"
      "v_mfma_f32_16x16x32_bf16 v[158:161], a[72:75], v[150:153], 0\n"
      "s_waitcnt lgkmcnt(3)\n"
      "v_rcp_f32_e64 v188, v188\n"
      "v_mfma_f32_16x16x32_bf16 v[154:157], a[96:99], v[150:153], 0\n"
      "buffer_load_dwordx4 v17, s[16:19], s84 offen  lds\n"
      "v_mfma_f32_16x16x32_bf16 v[184:187], a[120:123], v[150:153], 0\n"
      "s_add_u32 m0, s64, 8192\n"
      "v_rsq_f32_e64 v146, v146\n"
      "v_mfma_f32_16x16x32_bf16 v[210:213], a[4:7], v[96:99], v[210:213]\n"
      "ds_read_b128 v[206:209], v0 offset:20480\n"
      "v_mul_f32 v146, 0x415db3d7, v146\n"
      "v_mfma_f32_16x16x32_bf16 v[210:213], a[8:11], v[122:125], v[210:213]\n"
      "ds_read_b128 v[134:137], v1 offset:30720\n"
      "v_mov_b32 v148, v146\n"
      "v_mfma_f32_16x16x32_bf16 v[112:115], a[28:31], v[96:99], v[112:115]\n"
      "s_nop 0\n"
      "v_permlane32_swap_b32 v146, v148\n"
      "v_mfma_f32_16x16x32_bf16 v[112:115], a[32:35], v[122:125], v[112:115]\n"
      "v_mov_b32 v147, v146\n"
      "v_mul_f32_e64 v117, v148, v117\n"
      "v_mfma_f32_16x16x32_bf16 v[202:205], a[52:55], v[96:99], v[202:205]\n"
      "buffer_load_dwordx4 v18, s[16:19], s84 offen  lds\n"
      "v_mfma_f32_16x16x32_bf16 v[202:205], a[56:59], v[122:125], v[202:205]\n"
      "s_or_b32 s95, s45, s46\n"
      "v_permlane16_swap_b32 v146, v147\n"
      "v_mfma_f32_16x16x32_bf16 v[158:161], a[76:79], v[96:99], v[158:161]\n"
      "s_add_u32 m0, s64, 12288\n"
      "v_mul_f32 v142, v146, v142\n"
      "v_mul_f32 v201, v146, v201\n"
      "v_mfma_f32_16x16x32_bf16 v[158:161], a[80:83], v[122:125], v[158:161]\n"
      "v_mul_f32 v169, v146, v169\n"
      "v_mul_f32 v143, v146, v143\n"
      "v_mfma_f32_16x16x32_bf16 v[154:157], a[100:103], v[96:99], v[154:157]\n"
      "v_cvt_pk_bf16_f32 v142, v142, v143\n"
      "v_mul_f32_e64 v145, v146, v145\n"
      "v_mfma_f32_16x16x32_bf16 v[154:157], a[104:107], v[122:125], v[154:157]\n"
      "buffer_load_dwordx4 v19, s[16:19], s84 offen  lds\n"
      "v_mfma_f32_16x16x32_bf16 v[184:187], a[124:127], v[96:99], v[184:187]\n"
      "s_waitcnt lgkmcnt(0)\n"
      "v_mul_f32 v167, v146, v167\n"
      "v_mul_f32_e64 v166, v146, v166\n"
      "v_mfma_f32_16x16x32_bf16 v[184:187], a[128:131], v[122:125], v[184:187]\n"
      "ds_read_b128 v[104:107], v1 offset:26624\n"
      "v_mul_f32 v144, v146, v144\n"
      "v_mfma_f32_16x16x32_bf16 v[210:213], a[12:15], v[220:223], v[210:213]\n"
      "v_mul_f32 v199, v146, v199\n"
      "v_mul_f32 v168, v146, v168\n"
      "v_mfma_f32_16x16x32_bf16 v[210:213], a[16:19], v[174:177], v[210:213]\n"
      "s_add_u32 m0, s65, 16384\n"
      "v_cvt_pk_bf16_f32 v166, v166, v167\n"
      "v_mul_f32 v200, v146, v200\n"
      "v_mfma_f32_16x16x32_bf16 v[210:213], a[20:23], v[134:137], v[210:213]\n"
      "buffer_load_dwordx4 v15, s[16:19], s84 offen  lds\n"
      "v_mfma_f32_16x16x32_bf16 v[112:115], a[36:39], v[220:223], v[112:115]\n"
      "v_cvt_pk_bf16_f32 v167, v168, v169\n"
      "v_mul_f32 v198, v146, v198\n"
      "v_mfma_f32_16x16x32_bf16 v[112:115], a[40:43], v[174:177], v[112:115]\n"
      "s_or_b32 s95, s95, s48\n"
      "v_cvt_pk_bf16_f32 v169, v200, v201\n"
      "v_cvt_pk_bf16_f32 v168, v198, v199\n"
      "v_mfma_f32_16x16x32_bf16 v[112:115], a[44:47], v[134:137], v[112:115]\n"
      "ds_write_b128 v5, v[166:169] offset:18432\n"
      "v_pk_mul_f32 v[170:171], v[170:171], v[146:147] op_sel:[0,1]\n"
      "v_pk_mul_f32 v[162:163], v[162:163], v[146:147] op_sel:[0,1]\n"
      "v_pk_mul_f32 v[164:165], v[164:165], v[146:147] op_sel:[0,1]\n"
      "v_mfma_f32_16x16x32_bf16 v[202:205], a[60:63], v[220:223], v[202:205]\n"
      "buffer_load_dwordx4 v[190:193], v36, s[28:31], s86 offen\n"
      "v_mfma_f32_16x16x32_bf16 v[202:205], a[64:67], v[174:177], v[202:205]\n"
      "s_cmp_ge_u32 s95, 0x40000000\n"
      "s_cselect_b32 s94, -1, -2\n"
      "v_cvt_pk_bf16_f32 v143, v144, v145\n"
      "v_mfma_f32_16x16x32_bf16 v[202:205], a[68:71], v[134:137], v[202:205]\n"
      "v_mul_f32_e64 v180, v147, v180\n"
      "v_cvt_pk_bf16_f32 v162, v162, v163\n"
      "v_mfma_f32_16x16x32_bf16 v[158:161], a[84:87], v[220:223], v[158:161]\n"
      "v_mul_f32 v109, v148, v109\n"
      "v_mul_f32 v183, v147, v183\n"
      "v_mfma_f32_16x16x32_bf16 v[158:161], a[88:91], v[174:177], v[158:161]\n"
      "ds_write_b64 v6, v[142:143] offset:18432\n"
      "v_mfma_f32_16x16x32_bf16 v[158:161], a[92:95], v[134:137], v[158:161]\n"
      "v_cvt_pk_bf16_f32 v163, v164, v165\n"
      "v_mul_f32_e64 v111, v148, v111\n"
      "v_mfma_f32_16x16x32_bf16 v[154:157], a[108:111], v[220:223], v[154:157]\n"
      "buffer_load_dword v219, v29, s[28:31], s86 offen\n"
      "v_mfma_f32_16x16x32_bf16 v[154:157], a[112:115], v[174:177], v[154:157]\n"
      "v_mul_f32 v181, v147, v181\n"
      "v_mul_f32 v172, v147, v172\n"
      "v_mfma_f32_16x16x32_bf16 v[154:157], a[116:119], v[134:137], v[154:157]\n"
      "ds_read_b128 v[142:145], v1 offset:28672\n"
      "v_fma_f32 v189, v158, v158, s51\n"
      "v_mfma_f32_16x16x32_bf16 v[184:187], a[132:135], v[220:223], v[184:187]\n"
      "v_cvt_pk_bf16_f32 v164, v180, v181\n"
      "v_mul_f32 v129, v148, v129\n"
      "v_mfma_f32_16x16x32_bf16 v[184:187], a[136:139], v[174:177], v[184:187]\n"
      "v_mul_f32 v182, v147, v182\n"
      "v_fma_f32 v180, v210, v210, s51\n"
      "v_mfma_f32_16x16x32_bf16 v[184:187], a[140:143], v[134:137], v[184:187]\n"
      "v_cvt_pk_bf16_f32 v165, v182, v183\n"
      "v_fmac_f32_e64 v189, v159, v159\n"
      "v_mfma_f32_16x16x32_bf16 v[130:133], v[150:153], a[144:147], 0\n"
      "ds_write_b128 v5, v[162:165] offset:24576\n"
      "v_mfma_f32_16x16x32_bf16 v[130:133], v[96:99], a[148:151], v[130:133]\n"
      "v_fmac_f32_e64 v180, v211, v211\n"
      "v_cvt_pk_bf16_f32 v170, v170, v171\n"
      "v_mfma_f32_16x16x32_bf16 v[130:133], v[122:125], a[152:155], v[130:133]\n"
      "v_mul_f32 v108, v148, v108\n"
      "v_mul_f32 v173, v147, v173\n"
      "v_mfma_f32_16x16x32_bf16 v[130:133], v[220:223], a[156:159], v[130:133]\n"
      "ds_read_b128 v[162:165], v0 offset:32768\n"
      "v_cvt_pk_bf16_f32 v171, v172, v173\n"
      "v_mfma_f32_16x16x32_bf16 v[130:133], v[174:177], a[160:163], v[130:133]\n"
      "ds_write_b64 v6, v[170:171] offset:24576\n"
      "v_mfma_f32_16x16x32_bf16 v[130:133], v[134:137], a[164:167], v[130:133]\n"
      "v_fmac_f32 v189, v160, v160\n"
      "v_mul_f32 v128, v148, v128\n"
      "v_mfma_f32_16x16x32_bf16 v[194:197], v[150:153], a[168:171], 0\n"
      "v_cvt_pk_bf16_f32 v108, v108, v109\n"
      "v_cndmask_b32 v218, v218, v100, s[60:61]\n"
      "v_mfma_f32_16x16x32_bf16 v[194:197], v[96:99], a[172:175], v[194:197]\n"
      "ds_read_b128 v[100:103], v1 offset:32768\n"
      "v_mul_f32 v118, v148, v118\n"
      "v_mfma_f32_16x16x32_bf16 v[194:197], v[122:125], a[176:179], v[194:197]\n"
      "v_fmac_f32 v180, v212, v212\n"
      "v_fmac_f32 v180, v213, v213\n"
      "v_mfma_f32_16x16x32_bf16 v[194:197], v[220:223], a[180:183], v[194:197]\n"
      "v_mul_f32 v218, v188, v218\n"
      "v_mul_f32 v127, v148, v127\n"
      "v_mfma_f32_16x16x32_bf16 v[194:197], v[174:177], a[184:187], v[194:197]\n"
      "v_fmac_f32 v189, v161, v161\n"
      "v_fmac_f32 v180, v112, v112\n"
      "v_mfma_f32_16x16x32_bf16 v[194:197], v[134:137], a[188:191], v[194:197]\n"
      "ds_read_b128 v[198:201], v1 offset:20480\n"
      "v_mul_f32 v110, v148, v110\n"
      "v_mfma_f32_16x16x32_bf16 v[214:217], v[150:153], a[192:195], 0\n"
      "v_fmac_f32 v180, v113, v113\n"
      "v_fmac_f32 v189, v154, v154\n"
      "v_mfma_f32_16x16x32_bf16 v[214:217], v[96:99], a[196:199], v[214:217]\n"
      "ds_read_b128 v[150:153], v7\n"
      "v_mul_f32_e64 v126, v148, v126\n"
      "v_mfma_f32_16x16x32_bf16 v[214:217], v[122:125], a[200:203], v[214:217]\n"
      "v_fmac_f32 v189, v155, v155\n"
      "v_cvt_pk_bf16_f32 v126, v126, v127\n"
      "v_mfma_f32_16x16x32_bf16 v[214:217], v[220:223], a[204:207], v[214:217]\n"
      "v_mul_f32 v116, v148, v116\n"
      "v_cvt_pk_bf16_f32 v127, v128, v129\n"
      "v_mfma_f32_16x16x32_bf16 v[214:217], v[174:177], a[208:211], v[214:217]\n"
      "v_fmac_f32 v189, v156, v156\n"
      "v_fmac_f32 v189, v157, v157\n"
      "v_mfma_f32_16x16x32_bf16 v[214:217], v[134:137], a[212:215], v[214:217]\n"
      "ds_read_b128 v[166:169], v9 offset:4608\n"
      "v_cvt_pk_bf16_f32 v128, v116, v117\n"
      "v_mfma_f32_16x16x32_bf16 v[122:125], a[0:3], v[206:209], 0\n"
      "s_waitcnt lgkmcnt(2)\n"
      "v_mul_f32 v119, v148, v119\n"
      "v_cvt_pk_bf16_f32 v109, v110, v111\n"
      "v_mfma_f32_16x16x32_bf16 v[122:125], a[4:7], v[198:201], v[122:125]\n"
      "v_fmac_f32 v189, v184, v184\n"
      "v_fmac_f32 v180, v114, v114\n"
      "v_mfma_f32_16x16x32_bf16 v[122:125], a[8:11], v[138:141], v[122:125]\n"
      "s_cmp_gt_u32 s42, s44\n"
      "s_cselect_b32 s95, s94, s95\n"
      "v_fmac_f32 v180, v115, v115\n"
      "v_mfma_f32_16x16x32_bf16 v[122:125], a[12:15], v[104:107], v[122:125]\n"
      "v_fmac_f32 v189, v185, v185\n"
      "v_fmac_f32_e64 v189, v186, v186\n"
      "v_mfma_f32_16x16x32_bf16 v[122:125], a[16:19], v[162:165], v[122:125]\n"
      "v_cvt_pk_bf16_f32 v218, v218, v218\n"
      "v_cvt_pk_bf16_f32 v129, v118, v119\n"
      "v_mfma_f32_16x16x32_bf16 v[122:125], a[20:23], v[100:103], v[122:125]\n"
      "v_fmac_f32 v180, v202, v202\n"
      "v_fmac_f32 v180, v203, v203\n"
      "v_mfma_f32_16x16x32_bf16 v[118:121], a[24:27], v[206:209], 0\n"
      "ds_read_b128 v[174:177], v1 offset:22528\n"
      "v_cvt_pk_bf16_f32 v214, v214, v215\n"
      "v_mfma_f32_16x16x32_bf16 v[118:121], a[28:31], v[198:201], v[118:121]\n"
      "ds_write_b128 v5, v[126:129] offset:30720\n"
      "v_mfma_f32_16x16x32_bf16 v[118:121], a[32:35], v[138:141], v[118:121]\n"
      "s_waitcnt lgkmcnt(2)\n"
      "v_fmac_f32 v180, v204, v204\n"
      "v_or3_b32 v166, v166, v167, v168\n"
      "v_mfma_f32_16x16x32_bf16 v[118:121], a[36:39], v[104:107], v[118:121]\n"
      "v_fmac_f32 v189, v187, v187\n"
      "v_fmac_f32 v180, v205, v205\n"
      "v_mfma_f32_16x16x32_bf16 v[118:121], a[40:43], v[162:165], v[118:121]\n"
      "s_nop 0\n"
      "v_permlane16_swap_b32 v180, v189\n"
      "v_mfma_f32_16x16x32_bf16 v[118:121], a[44:47], v[100:103], v[118:121]\n"
      "ds_read_b128 v[220:223], v1 offset:34816\n"
      "v_add_f32_e64 v180, v180, v189\n"
      "v_mfma_f32_16x16x32_bf16 v[126:129], a[48:51], v[206:209], 0\n"
      "v_fma_f32 v182, v122, v122, s51\n"
      "v_mov_b32 v189, v180\n"
      "v_mfma_f32_16x16x32_bf16 v[126:129], a[52:55], v[198:201], v[126:129]\n"
      "s_nop 0\n"
      "v_permlane32_swap_b32 v180, v189\n"
      "v_mfma_f32_16x16x32_bf16 v[126:129], a[56:59], v[138:141], v[126:129]\n"
      "ds_read_b128 v[134:137], v0 offset:22528\n"
      "v_add_f32 v180, v180, v189\n"
      "v_mfma_f32_16x16x32_bf16 v[126:129], a[60:63], v[104:107], v[126:129]\n"
      "v_rsq_f32 v180, v180\n"
      "v_mfma_f32_16x16x32_bf16 v[126:129], a[64:67], v[162:165], v[126:129]\n"
      "v_bitop3_b32 v166, v166, v169, s33 bitop3:0xa8\n"
      "v_mov_b32 v189, v180\n"
      "v_mfma_f32_16x16x32_bf16 v[126:129], a[68:71], v[100:103], v[126:129]\n"
      "s_nop 0\n"
      "v_permlane16_swap_b32 v180, v189\n"
      "v_mfma_f32_16x16x32_bf16 v[96:99], a[72:75], v[206:209], 0\n"
      "ds_write_b64 v6, v[108:109] offset:30720\n"
      "v_mfma_f32_16x16x32_bf16 v[96:99], a[76:79], v[198:201], v[96:99]\n"
      "v_mul_f32 v180, s50, v180\n"
      "v_mul_f32 v112, v180, v112\n"
      "v_mfma_f32_16x16x32_bf16 v[96:99], a[80:83], v[138:141], v[96:99]\n"
      "v_cmp_ne_u32 s[56:57], 0, v166\n"
      "v_mul_f32_e64 v211, v180, v211\n"
      "v_mfma_f32_16x16x32_bf16 v[96:99], a[84:87], v[104:107], v[96:99]\n"
      "v_mul_f32 v210, v180, v210\n"
      "v_cvt_pk_bf16_f32 v130, v130, v131\n"
      "v_mfma_f32_16x16x32_bf16 v[96:99], a[88:91], v[162:165], v[96:99]\n"
      "v_mul_f32 v160, v189, v160\n"
      "v_mul_f32 v114, v180, v114\n"
      "v_mfma_f32_16x16x32_bf16 v[96:99], a[92:95], v[100:103], v[96:99]\n"
      "v_cvt_pk_bf16_f32 v210, v210, v211\n"
      "v_fmac_f32 v182, v123, v123\n"
      "v_mfma_f32_16x16x32_bf16 v[170:173], a[96:99], v[206:209], 0\n"
      "v_mul_f32 v186, v189, v186\n"
      "v_mul_f32 v155, v189, v155\n"
      "v_mfma_f32_16x16x32_bf16 v[170:173], a[100:103], v[198:201], v[170:173]\n"
      "v_cvt_pk_bf16_f32 v215, v216, v217\n"
      "v_mul_f32_e64 v203, v180, v203\n"
      "v_mfma_f32_16x16x32_bf16 v[170:173], a[104:107], v[138:141], v[170:173]\n"
      "ds_write_b16 v12, v218\n"
      "v_mfma_f32_16x16x32_bf16 v[170:173], a[108:111], v[104:107], v[170:173]\n"
      "v_mul_f32 v205, v180, v205\n"
      "v_mul_f32 v213, v180, v213\n"
      "v_mfma_f32_16x16x32_bf16 v[170:173], a[112:115], v[162:165], v[170:173]\n"
      "v_mul_f32_e64 v202, v180, v202\n"
      "v_readlane_b32 s47, v38, s43\n"
      "v_mfma_f32_16x16x32_bf16 v[170:173], a[116:119], v[100:103], v[170:173]\n"
      "v_fma_f32 v217, v96, v96, s51\n"
      "v_cvt_pk_bf16_f32 v131, v132, v133\n"
      "v_mfma_f32_16x16x32_bf16 v[108:111], a[120:123], v[206:209], 0\n"
      "v_cvt_pk_bf16_f32 v194, v194, v195\n"
      "v_mul_f32 v212, v180, v212\n"
      "v_mfma_f32_16x16x32_bf16 v[108:111], a[124:127], v[198:201], v[108:111]\n"
      "v_cvt_pk_bf16_f32 v195, v196, v197\n"
      "v_fmac_f32 v217, v97, v97\n"
      "v_mfma_f32_16x16x32_bf16 v[108:111], a[128:131], v[138:141], v[108:111]\n"
      "v_fmac_f32 v182, v124, v124\n"
      "v_mul_f32 v115, v180, v115\n"
      "v_mfma_f32_16x16x32_bf16 v[108:111], a[132:135], v[104:107], v[108:111]\n"
      "v_fmac_f32 v182, v125, v125\n"
      "v_fmac_f32 v182, v118, v118\n"
      "v_mfma_f32_16x16x32_bf16 v[108:111], a[136:139], v[162:165], v[108:111]\n"
      "v_fmac_f32 v217, v98, v98\n"
      "v_fmac_f32 v182, v119, v119\n"
      "v_mfma_f32_16x16x32_bf16 v[108:111], a[140:143], v[100:103], v[108:111]\n"
      "s_cmp_ge_u32 s95, 0x40000000\n"
      "s_cselect_b64 vcc, -1, s[56:57]\n"
      "v_fmac_f32_e64 v182, v120, v120\n"
      "v_mfma_f32_16x16x32_bf16 v[166:169], v[206:209], a[144:147], 0\n"
      "v_fmac_f32 v182, v121, v121\n"
      "v_fmac_f32 v217, v99, v99\n"
      "v_mfma_f32_16x16x32_bf16 v[166:169], v[198:201], a[148:151], v[166:169]\n"
      "v_mul_f32 v113, v180, v113\n"
      "v_mul_f32 v185, v189, v185\n"
      "v_mfma_f32_16x16x32_bf16 v[166:169], v[138:141], a[152:155], v[166:169]\n"
      "v_mul_f32 v154, v189, v154\n"
      "v_mul_f32 v204, v180, v204\n"
      "v_mfma_f32_16x16x32_bf16 v[166:169], v[104:107], a[156:159], v[166:169]\n"
      "v_cvt_pk_bf16_f32 v211, v212, v213\n"
      "v_fmac_f32 v182, v126, v126\n"
      "v_mfma_f32_16x16x32_bf16 v[166:169], v[162:165], a[160:163], v[166:169]\n"
      "v_fmac_f32 v217, v170, v170\n"
      "v_mul_f32 v159, v189, v159\n"
      "v_mfma_f32_16x16x32_bf16 v[166:169], v[100:103], a[164:167], v[166:169]\n"
      "v_mul_f32 v158, v189, v158\n"
      "v_mul_f32 v161, v189, v161\n"
      "v_mfma_f32_16x16x32_bf16 v[178:181], v[206:209], a[168:171], 0\n"
      "v_cvt_pk_bf16_f32 v158, v158, v159\n"
      "v_mul_f32 v184, v189, v184\n"
      "v_mfma_f32_16x16x32_bf16 v[178:181], v[198:201], a[172:175], v[178:181]\n"
      "v_cvt_pk_bf16_f32 v213, v114, v115\n"
      "v_fmac_f32 v217, v171, v171\n"
      "v_mfma_f32_16x16x32_bf16 v[178:181], v[138:141], a[176:179], v[178:181]\n"
      "v_mul_f32 v156, v189, v156\n"
      "v_fmac_f32_e64 v217, v172, v172\n"
      "v_mfma_f32_16x16x32_bf16 v[178:181], v[104:107], a[180:183], v[178:181]\n"
      "v_fmac_f32 v182, v127, v127\n"
      "v_fmac_f32 v182, v128, v128\n"
      "v_mfma_f32_16x16x32_bf16 v[178:181], v[162:165], a[184:187], v[178:181]\n"
      "v_fmac_f32 v217, v173, v173\n"
      "v_fmac_f32 v217, v108, v108\n"
      "v_mfma_f32_16x16x32_bf16 v[178:181], v[100:103], a[188:191], v[178:181]\n"
      "v_fmac_f32 v217, v109, v109\n"
      "v_cvt_pk_bf16_f32 v202, v202, v203\n"
      "v_mfma_f32_16x16x32_bf16 v[114:117], v[206:209], a[192:195], 0\n"
      "ds_read_b128 v[146:149], v0 offset:28672\n"
      "v_fmac_f32 v217, v110, v110\n"
      "v_mfma_f32_16x16x32_bf16 v[114:117], v[198:201], a[196:199], v[114:117]\n"
      "ds_read_b128 v[206:209], v7 offset:4096\n"
      "v_cvt_pk_bf16_f32 v212, v112, v113\n"
      "v_mfma_f32_16x16x32_bf16 v[114:117], v[138:141], a[200:203], v[114:117]\n"
      "ds_read_b128 v[198:201], v0 offset:34816\n"
      "v_fmac_f32 v182, v129, v129\n"
      "v_mfma_f32_16x16x32_bf16 v[114:117], v[104:107], a[204:207], v[114:117]\n"
      "v_fmac_f32 v217, v111, v111\n"
      "v_cvt_pk_bf16_f32 v159, v160, v161\n"
      "v_mfma_f32_16x16x32_bf16 v[114:117], v[162:165], a[208:211], v[114:117]\n"
      "v_permlane16_swap_b32 v182, v217\n"
      "v_mfma_f32_16x16x32_bf16 v[114:117], v[100:103], a[212:215], v[114:117]\n"
      "s_waitcnt lgkmcnt(3)\n"
      "v_add_f32 v182, v182, v217\n"
      "v_mov_b32 v217, v182\n"
      "v_mfma_f32_16x16x32_bf16 v[104:107], a[0:3], v[134:137], 0\n"
      "s_nop 0\n"
      "v_permlane32_swap_b32 v182, v217\n"
      "v_mfma_f32_16x16x32_bf16 v[104:107], a[4:7], v[174:177], v[104:107]\n"
      "s_waitcnt lgkmcnt(1)\n"
      "v_add_f32 v182, v182, v217\n"
      "v_cvt_pk_bf16_f32 v197, v180, v181\n"
      "v_mfma_f32_16x16x32_bf16 v[104:107], a[8:11], v[146:149], v[104:107]\n"
      "v_rsq_f32_e64 v182, v182\n"
      "v_mfma_f32_16x16x32_bf16 v[104:107], a[12:15], v[142:145], v[104:107]\n"
      "s_waitcnt lgkmcnt(0)\n"
      "v_mov_b32 v217, v182\n"
      "v_cvt_pk_bf16_f32 v133, v168, v169\n"
      "v_mfma_f32_16x16x32_bf16 v[104:107], a[16:19], v[198:201], v[104:107]\n"
      "v_permlane16_swap_b32 v182, v217\n"
      "v_mfma_f32_16x16x32_bf16 v[104:107], a[20:23], v[220:223], v[104:107]\n"
      "v_mul_f32 v109, v217, v109\n"
      "v_cvt_pk_bf16_f32 v132, v166, v167\n"
      "v_mfma_f32_16x16x32_bf16 v[162:165], a[24:27], v[134:137], 0\n"
      "v_mul_f32 v108, v217, v108\n"
      "v_mul_f32 v157, v189, v157\n"
      "v_mfma_f32_16x16x32_bf16 v[162:165], a[28:31], v[174:177], v[162:165]\n"
      "v_mul_f32_e64 v182, s50, v182\n"
      "v_cvt_pk_bf16_f32 v160, v154, v155\n"
      "v_mfma_f32_16x16x32_bf16 v[162:165], a[32:35], v[146:149], v[162:165]\n"
      "v_fma_f32 v218, v104, v104, s51\n"
      "v_mul_f32 v110, v217, v110\n"
      "v_mfma_f32_16x16x32_bf16 v[162:165], a[36:39], v[142:145], v[162:165]\n"
      "v_mul_f32 v96, v217, v96\n"
      "v_mul_f32 v126, v182, v126\n"
      "v_mfma_f32_16x16x32_bf16 v[162:165], a[40:43], v[198:201], v[162:165]\n"
      "v_mul_f32 v172, v217, v172\n"
      "v_mul_f32 v123, v182, v123\n"
      "v_mfma_f32_16x16x32_bf16 v[162:165], a[44:47], v[220:223], v[162:165]\n"
      "v_mul_f32 v187, v189, v187\n"
      "v_cvt_pk_bf16_f32 v216, v114, v115\n"
      "v_mfma_f32_16x16x32_bf16 v[112:115], a[48:51], v[134:137], 0\n"
      "v_mul_f32 v124, v182, v124\n"
      "v_fmac_f32 v218, v105, v105\n"
      "v_mfma_f32_16x16x32_bf16 v[112:115], a[52:55], v[174:177], v[112:115]\n"
      "v_fmac_f32 v218, v106, v106\n"
      "v_fmac_f32 v218, v107, v107\n"
      "v_mfma_f32_16x16x32_bf16 v[112:115], a[56:59], v[146:149], v[112:115]\n"
      "v_cvt_pk_bf16_f32 v161, v156, v157\n"
      "v_mul_f32_e64 v97, v217, v97\n"
      "v_mfma_f32_16x16x32_bf16 v[112:115], a[60:63], v[142:145], v[112:115]\n"
      "v_mul_f32_e64 v120, v182, v120\n"
      "v_cvt_pk_bf16_f32 v184, v184, v185\n"
      "v_mfma_f32_16x16x32_bf16 v[112:115], a[64:67], v[198:201], v[112:115]\n"
      "ds_read_b128 v[154:157], v7 offset:8192\n"
      "v_cvt_pk_bf16_f32 v96, v96, v97\n"
      "v_mfma_f32_16x16x32_bf16 v[112:115], a[68:71], v[220:223], v[112:115]\n"
      "v_fmac_f32 v218, v162, v162\n"
      "v_mul_f32 v99, v217, v99\n"
      "v_mul_f32_e64 v118, v182, v118\n"
      "v_mfma_f32_16x16x32_bf16 v[100:103], a[72:75], v[134:137], 0\n"
      "buffer_store_dwordx4 v[150:153], v20, s[24:27], s85 offen sc1\n"
      "v_pk_mul_f32 v[170:171], v[170:171], v[216:217] op_sel:[0,1]\n"
      "v_mfma_f32_16x16x32_bf16 v[100:103], a[76:79], v[174:177], v[100:103]\n"
      "v_mul_f32 v111, v217, v111\n"
      "v_cvt_pk_bf16_f32 v203, v204, v205\n"
      "v_mfma_f32_16x16x32_bf16 v[100:103], a[80:83], v[146:149], v[100:103]\n"
      "v_mul_f32 v173, v217, v173\n"
      "v_mul_f32 v98, v217, v98\n"
      "v_mfma_f32_16x16x32_bf16 v[100:103], a[84:87], v[142:145], v[100:103]\n"
      "v_cvt_pk_bf16_f32 v185, v186, v187\n"
      "v_mul_f32 v125, v182, v125\n"
      "v_mfma_f32_16x16x32_bf16 v[100:103], a[88:91], v[198:201], v[100:103]\n"
      "ds_read_b128 v[166:169], v7 offset:16384\n"
      "v_cvt_pk_bf16_f32 v97, v98, v99\n"
      "v_mfma_f32_16x16x32_bf16 v[100:103], a[92:95], v[220:223], v[100:103]\n"
      "v_mul_f32 v127, v182, v127\n"
      "v_fmac_f32 v218, v163, v163\n"
      "v_mfma_f32_16x16x32_bf16 v[150:153], a[96:99], v[134:137], 0\n"
      "ds_read_b128 v[138:141], v7 offset:12288\n"
      "v_mul_f32_e64 v122, v182, v122\n"
      "v_mfma_f32_16x16x32_bf16 v[150:153], a[100:103], v[174:177], v[150:153]\n"
      "v_cvt_pk_bf16_f32 v122, v122, v123\n"
      "v_cvt_pk_bf16_f32 v123, v124, v125\n"
      "v_mfma_f32_16x16x32_bf16 v[150:153], a[104:107], v[146:149], v[150:153]\n"
      "v_fmac_f32_e64 v218, v164, v164\n"
      "v_fma_f32 v205, v100, v100, s51\n"
      "v_mfma_f32_16x16x32_bf16 v[150:153], a[108:111], v[142:145], v[150:153]\n"
      "buffer_store_dwordx4 v[206:209], v21, s[24:27], s85 offen sc1\n"
      "v_mfma_f32_16x16x32_bf16 v[150:153], a[112:115], v[198:201], v[150:153]\n"
      "v_cvt_pk_bf16_f32 v98, v170, v171\n"
      "v_cvt_pk_bf16_f32 v126, v126, v127\n"
      "v_mfma_f32_16x16x32_bf16 v[150:153], a[116:119], v[220:223], v[150:153]\n"
      "v_mul_f32 v119, v182, v119\n"
      "v_fmac_f32 v205, v101, v101\n"
      "v_mfma_f32_16x16x32_bf16 v[206:209], a[120:123], v[134:137], 0\n"
      "v_cvt_pk_bf16_f32 v108, v108, v109\n"
      "v_fmac_f32 v205, v102, v102\n"
      "v_mfma_f32_16x16x32_bf16 v[206:209], a[124:127], v[174:177], v[206:209]\n"
      "v_fmac_f32 v205, v103, v103\n"
      "v_cvt_pk_bf16_f32 v124, v118, v119\n"
      "v_mfma_f32_16x16x32_bf16 v[206:209], a[128:131], v[146:149], v[206:209]\n"
      "v_cvt_pk_bf16_f32 v217, v116, v117\n"
      "v_fmac_f32_e64 v218, v165, v165\n"
      "v_mfma_f32_16x16x32_bf16 v[206:209], a[132:135], v[142:145], v[206:209]\n"
      "v_fmac_f32 v205, v150, v150\n"
      "v_fmac_f32 v218, v112, v112\n"
      "v_mfma_f32_16x16x32_bf16 v[206:209], a[136:139], v[198:201], v[206:209]\n"
      "s_waitcnt lgkmcnt(0)\n"
      "v_fmac_f32 v218, v113, v113\n"
      "v_fmac_f32 v218, v114, v114\n"
      "v_mfma_f32_16x16x32_bf16 v[206:209], a[140:143], v[220:223], v[206:209]\n"
      "buffer_store_dwordx4 v[154:157], v22, s[24:27], s85 offen sc1\n"
      "v_mfma_f32_16x16x32_bf16 v[186:189], v[134:137], a[144:147], 0\n"
      "v_fmac_f32 v205, v151, v151\n"
      "v_fmac_f32 v205, v152, v152\n"
      "v_mfma_f32_16x16x32_bf16 v[186:189], v[174:177], a[148:151], v[186:189]\n"
      "v_fmac_f32 v205, v153, v153\n"
      "v_fmac_f32 v218, v115, v115\n"
      "v_mfma_f32_16x16x32_bf16 v[186:189], v[146:149], a[152:155], v[186:189]\n"
      "v_fmac_f32 v205, v206, v206\n"
      "v_fmac_f32 v205, v207, v207\n"
      "v_mfma_f32_16x16x32_bf16 v[186:189], v[142:145], a[156:159], v[186:189]\n"
      "v_fmac_f32 v205, v208, v208\n"
      "v_fmac_f32 v205, v209, v209\n"
      "v_mfma_f32_16x16x32_bf16 v[186:189], v[198:201], a[160:163], v[186:189]\n"
      "s_nop 0\n"
      "v_permlane16_swap_b32 v218, v205\n"
      "v_mfma_f32_16x16x32_bf16 v[186:189], v[220:223], a[164:167], v[186:189]\n"
      "v_add_f32 v218, v218, v205\n"
      "v_mov_b32 v205, v218\n"
      "v_mfma_f32_16x16x32_bf16 v[154:157], v[134:137], a[168:171], 0\n"
      "s_nop 0\n"
      "v_permlane32_swap_b32_e64 v218, v205\n"
      "v_mfma_f32_16x16x32_bf16 v[154:157], v[174:177], a[172:175], v[154:157]\n"
      "buffer_store_dwordx4 v[138:141], v23, s[24:27], s85 offen sc1\n"
      "v_pk_mul_f32 v[128:129], v[128:129], v[182:183] op_sel_hi:[1,0]\n"
      "v_mfma_f32_16x16x32_bf16 v[154:157], v[146:149], a[176:179], v[154:157]\n"
      "ds_read_b128 v[116:119], v2 offset:6144\n"
      "v_add_f32 v218, v218, v205\n"
      "v_mfma_f32_16x16x32_bf16 v[154:157], v[142:145], a[180:183], v[154:157]\n"
      "v_rsq_f32 v218, v218\n"
      "v_mfma_f32_16x16x32_bf16 v[154:157], v[198:201], a[184:187], v[154:157]\n"
      "v_cvt_pk_bf16_f32 v127, v128, v129\n"
      "v_cvt_pk_bf16_f32 v99, v172, v173\n"
      "v_mfma_f32_16x16x32_bf16 v[154:157], v[220:223], a[188:191], v[154:157]\n"
      "ds_read_b128 v[138:141], v10 offset:1200\n"
      "v_cvt_pk_bf16_f32 v196, v178, v179\n"
      "v_mfma_f32_16x16x32_bf16 v[170:173], v[134:137], a[192:195], 0\n"
      "ds_read_b128 v[134:137], v2\n"
      "v_mov_b32 v205, v218\n"
      "v_mfma_f32_16x16x32_bf16 v[170:173], v[174:177], a[196:199], v[170:173]\n"
      "s_nop 0\n"
      "v_permlane16_swap_b32_e64 v218, v205\n"
      "v_mfma_f32_16x16x32_bf16 v[170:173], v[146:149], a[200:203], v[170:173]\n"
      "ds_read_b128 v[146:149], v2 offset:3072\n"
      "v_mul_f32_e64 v218, s50, v218\n"
      "v_mfma_f32_16x16x32_bf16 v[170:173], v[142:145], a[204:207], v[170:173]\n"
      "ds_read_u16 v86, v11 offset:1232\n"
      "v_cvt_pk_bf16_f32 v81, v156, v157\n"
      "v_mfma_f32_16x16x32_bf16 v[170:173], v[198:201], a[208:211], v[170:173]\n"
      "buffer_store_dwordx4 v[166:169], v25, s[24:27], s85 offen sc1\n"
      "v_pk_mul_f32 v[164:165], v[164:165], v[218:219] op_sel_hi:[1,0]\n"
      "v_pk_mul_f32 v[114:115], v[114:115], v[218:219] op_sel_hi:[1,0]\n"
      "v_pk_mul_f32 v[206:207], v[206:207], v[204:205] op_sel:[0,1]\n"
      "v_pk_mul_f32 v[104:105], v[104:105], v[218:219] op_sel_hi:[1,0]\n"
      "v_pk_mul_f32 v[100:101], v[100:101], v[204:205] op_sel:[0,1]\n"
      "v_pk_mul_f32 v[112:113], v[112:113], v[218:219] op_sel_hi:[1,0]\n"
      "v_pk_mul_f32 v[150:151], v[150:151], v[204:205] op_sel:[0,1]\n"
      "v_mfma_f32_16x16x32_bf16 v[170:173], v[220:223], a[212:215], v[170:173]\n"
      "s_waitcnt lgkmcnt(2)\n"
      "v_mul_f32 v208, v205, v208\n"
      "v_mul_f32 v103, v205, v103\n"
      "v_mfma_f32_16x16x32_bf16 v[166:169], a[216:219], v[134:137], 0\n"
      "s_waitcnt lgkmcnt(1)\n"
      "v_mul_f32 v163, v218, v163\n"
      "v_mul_f32 v209, v205, v209\n"
      "v_mfma_f32_16x16x32_bf16 v[166:169], a[220:223], v[146:149], v[166:169]\n"
      "v_cvt_pk_bf16_f32 v100, v100, v101\n"
      "v_mul_f32 v152, v205, v152\n"
      "v_mfma_f32_16x16x32_bf16 v[166:169], a[224:227], v[116:119], v[166:169]\n"
      "ds_read_b64 v[156:157], v10 offset:1192\n"
      "v_mul_f32 v102, v205, v102\n"
      "v_mfma_f32_16x16x32_bf16 v[198:201], a[240:243], v[134:137], 0\n"
      "v_cvt_pk_bf16_f32 v101, v102, v103\n"
      "v_cvt_pk_bf16_f32 v109, v110, v111\n"
      "v_mfma_f32_16x16x32_bf16 v[198:201], a[244:247], v[146:149], v[198:201]\n"
      "ds_read_b128 v[220:223], v2 offset:9216\n"
      "v_mul_f32_e64 v121, v182, v121\n"
      "v_mfma_f32_16x16x32_bf16 v[198:201], a[248:251], v[116:119], v[198:201]\n"
      "ds_read_b128 v[176:179], v2 offset:15360\n"
      "v_cvt_pk_bf16_f32 v112, v112, v113\n"
      "v_mfma_f32_16x16x32_bf16 v[142:145], v[232:235], v[134:137], 0\n"
      "ds_read_b128 v[180:183], v2 offset:12288\n"
      "v_mul_f32_e64 v153, v205, v153\n"
      "v_mfma_f32_16x16x32_bf16 v[142:145], v[236:239], v[146:149], v[142:145]\n"
      "v_cvt_pk_bf16_f32 v125, v120, v121\n"
      "v_cvt_pk_bf16_f32 v85, v172, v173\n"
      "v_mfma_f32_16x16x32_bf16 v[142:145], v[240:243], v[116:119], v[142:145]\n"
      "v_cvt_pk_bf16_f32 v103, v152, v153\n"
      "v_mul_f32 v106, v218, v106\n"
      "v_mfma_f32_16x16x32_bf16 v[134:137], v[158:161], v[210:213], v[40:43]\n"
      "v_cvt_pk_bf16_f32 v102, v150, v151\n"
      "v_mul_f32 v107, v218, v107\n"
      "v_mfma_f32_16x16x32_bf16 v[146:149], v[96:99], v[210:213], v[44:47]\n"
      "ds_read_b128 v[150:153], v2 offset:3328\n"
      "v_cvt_pk_bf16_f32 v84, v170, v171\n"
      "v_mfma_f32_16x16x32_bf16 v[172:175], v[100:103], v[210:213], v[48:51]\n"
      "s_waitcnt lgkmcnt(2)\n"
      "v_cvt_pk_bf16_f32 v76, v186, v187\n"
      "v_cvt_pk_bf16_f32 v104, v104, v105\n"
      "v_mfma_f32_16x16x32_bf16 v[118:121], v[138:141], v[210:213], v[24:27]\n"
      "ds_read_b128 v[210:213], v2 offset:256\n"
      "v_mul_f32 v162, v218, v162\n"
      "v_mfma_f32_16x16x16_bf16 v[134:137], v[184:185], v[202:203], v[134:137]\n"
      "v_cvt_pk_bf16_f32 v77, v188, v189\n"
      "v_cvt_pk_bf16_f32 v206, v206, v207\n"
      "v_cvt_pk_bf16_f32 v105, v106, v107\n"
      "v_mfma_f32_16x16x16_bf16 v[146:149], v[108:109], v[202:203], v[146:149]\n"
      "v_cvt_pk_bf16_f32 v207, v208, v209\n"
      "v_cvt_pk_bf16_f32 v107, v164, v165\n"
      "v_cvt_pk_bf16_f32 v80, v154, v155\n"
      "v_mfma_f32_16x16x16_bf16 v[172:175], v[206:207], v[202:203], v[172:175]\n"
      "v_cvt_pk_bf16_f32 v106, v162, v163\n"
      "v_cvt_pk_bf16_f32 v113, v114, v115\n"
      "s_waitcnt vmcnt(6)\n"
      "v_or3_b32 v190, v190, v191, v192\n"
      "v_mfma_f32_16x16x16_bf16 v[118:121], v[156:157], v[202:203], v[118:121]\n"
      "v_max3_f32 v163, v134, v135, v136\n"
      "v_max3_f32 v163, v163, v148, v149\n"
      "v_max3_f32 v218, v137, v146, v147\n"
      "v_mfma_f32_16x16x32_bf16 v[166:169], a[228:231], v[220:223], v[166:169]\n"
      "s_waitcnt lgkmcnt(1)\n"
      "v_max3_f32 v163, v163, v174, v175\n"
      "v_max3_f32 v218, v218, v172, v173\n"
      "v_mfma_f32_16x16x32_bf16 v[166:169], a[232:235], v[180:183], v[166:169]\n"
      "v_max3_f32 v163, v163, v218, v118\n"
      "v_mov_b32_e64 v218, v163\n"
      "v_mfma_f32_16x16x32_bf16 v[166:169], a[236:239], v[176:179], v[166:169]\n"
      "s_nop 0\n"
      "v_permlane16_swap_b32 v163, v218\n"
      "v_mfma_f32_16x16x32_bf16 v[198:201], a[252:255], v[220:223], v[198:201]\n"
      "v_max_f32 v163, v163, v218\n"
      "v_mov_b32 v218, v163\n"
      "v_mfma_f32_16x16x32_bf16 v[198:201], v[224:227], v[180:183], v[198:201]\n"
      "ds_read_u16 v78, v11 offset:1168\n"
      "v_permlane32_swap_b32 v163, v218\n"
      "v_mfma_f32_16x16x32_bf16 v[198:201], v[228:231], v[176:179], v[198:201]\n"
      "v_max_f32 v163, v163, v218\n"
      "v_sub_f32 v134, v134, v163\n"
      "v_mfma_f32_16x16x32_bf16 v[142:145], v[244:247], v[220:223], v[142:145]\n"
      "v_sub_f32 v172, v172, v163\n"
      "v_sub_f32 v135, v135, v163\n"
      "v_mfma_f32_16x16x32_bf16 v[142:145], v[248:251], v[180:183], v[142:145]\n"
      "s_waitcnt vmcnt(5)\n"
      "v_or3_b32 v190, v190, v193, v219\n"
      "v_sub_f32 v173, v173, v163\n"
      "v_mfma_f32_16x16x32_bf16 v[142:145], v[252:255], v[176:179], v[142:145]\n"
      "ds_read_b128 v[218:221], v2 offset:6400\n"
      "v_sub_f32 v137, v137, v163\n"
      "v_sub_f32 v149, v149, v163\n"
      "v_mfma_f32_16x16x32_bf16 v[176:179], v[158:161], v[122:125], v[52:55]\n"
      "v_exp_f32 v173, v173\n"
      "v_mfma_f32_16x16x32_bf16 v[114:117], v[96:99], v[122:125], v[56:59]\n"
      "v_sub_f32 v175, v175, v163\n"
      "v_sub_f32 v118, v118, v163\n"
      "v_mfma_f32_16x16x32_bf16 v[186:189], v[100:103], v[122:125], v[60:63]\n"
      "v_sub_f32 v136, v136, v163\n"
      "v_sub_f32 v174, v174, v163\n"
      "v_mfma_f32_16x16x32_bf16 v[202:205], v[138:141], v[122:125], v[26:29]\n"
      "s_waitcnt lgkmcnt(1)\n"
      "v_exp_f32 v135, v135\n"
      "v_exp_f32 v149, v149\n"
      "v_mfma_f32_16x16x16_bf16 v[176:179], v[184:185], v[126:127], v[176:179]\n"
      "v_exp_f32 v136, v136\n"
      "v_exp_f32 v134, v134\n"
      "v_mfma_f32_16x16x16_bf16 v[114:117], v[108:109], v[126:127], v[114:117]\n"
      "v_sub_f32 v146, v146, v163\n"
      "v_exp_f32 v175, v175\n"
      "v_cvt_pk_bf16_f32 v134, v134, v135\n"
      "v_exp_f32 v137, v137\n"
      "v_mfma_f32_16x16x16_bf16 v[186:189], v[206:207], v[126:127], v[186:189]\n"
      "v_exp_f32 v118, v118\n"
      "v_sub_f32 v148, v148, v163\n"
      "v_mfma_f32_16x16x16_bf16 v[202:205], v[156:157], v[126:127], v[202:205]\n"
      "v_max3_f32 v110, v176, v177, v178\n"
      "v_sub_f32_e64 v147, v147, v163\n"
      "v_cvt_pk_bf16_f32 v135, v136, v137\n"
      "v_mfma_f32_16x16x32_bf16 v[162:165], a[216:219], v[210:213], 0\n"
      "s_waitcnt lgkmcnt(0)\n"
      "v_exp_f32 v148, v148\n"
      "v_mfma_f32_16x16x32_bf16 v[162:165], a[220:223], v[150:153], v[162:165]\n"
      "s_add_u32 s43, s43, 1\n"
      "v_exp_f32 v146, v146\n"
      "v_mfma_f32_16x16x32_bf16 v[162:165], a[224:227], v[218:221], v[162:165]\n"
      "v_max3_f32 v110, v110, v116, v117\n"
      "v_exp_f32 v172, v172\n"
      "v_mfma_f32_16x16x32_bf16 v[180:183], a[240:243], v[210:213], 0\n"
      "v_exp_f32 v147, v147\n"
      "v_mfma_f32_16x16x32_bf16 v[180:183], a[244:247], v[150:153], v[180:183]\n"
      "v_exp_f32_e64 v174, v174\n"
      "v_cvt_pk_bf16_f32 v137, v148, v149\n"
      "v_mfma_f32_16x16x32_bf16 v[180:183], a[248:251], v[218:221], v[180:183]\n"
      "v_cvt_pk_bf16_f32 v88, v172, v173\n"
      "v_cvt_pk_bf16_f32 v136, v146, v147\n"
      "v_mfma_f32_16x16x32_bf16 v[170:173], v[232:235], v[210:213], 0\n"
      "ds_read_b128 v[126:129], v2 offset:9472\n"
      "v_max3_f32 v121, v179, v114, v115\n"
      "v_mfma_f32_16x16x32_bf16 v[170:173], v[236:239], v[150:153], v[170:173]\n"
      "ds_read_u16 v82, v11 offset:1200\n"
      "v_max3_f32 v110, v110, v188, v189\n"
      "v_mfma_f32_16x16x32_bf16 v[170:173], v[240:243], v[218:221], v[170:173]\n"
      "v_cvt_pk_bf16_f32 v90, v118, 0\n"
      "v_cvt_pk_bf16_f32 v89, v174, v175\n"
      "v_mfma_f32_16x16x32_bf16 v[210:213], v[32:35], v[134:137], 0\n"
      "s_and_b32 s47, s47, -4\n"
      "v_max3_f32 v121, v121, v186, v187\n"
      "v_max3_f32 v110, v110, v121, v202\n"
      "v_mov_b32 v153, v110\n"
      "v_mfma_f32_16x16x32_bf16 v[210:213], v[32:35], v[88:91], v[210:213]\n"
      "s_nop 0\n"
      "v_permlane16_swap_b32 v110, v153\n"
      "v_mfma_f32_16x16x32_bf16 v[218:221], v[130:133], v[134:137], 0\n"
      "v_max_f32 v110, v110, v153\n"
      "v_mov_b32 v153, v110\n"
      "v_mfma_f32_16x16x32_bf16 v[218:221], v[76:79], v[88:91], v[218:221]\n"
      "s_nop 0\n"
      "v_permlane32_swap_b32 v110, v153\n"
      "v_mfma_f32_16x16x32_bf16 v[118:121], v[194:197], v[134:137], 0\n"
      "s_waitcnt lgkmcnt(0)\n"
      "v_max_f32 v110, v110, v153\n"
      "v_sub_f32_e64 v176, v176, v110\n"
      "v_mfma_f32_16x16x32_bf16 v[118:121], v[80:83], v[88:91], v[118:121]\n"
      "v_sub_f32 v177, v177, v110\n"
      "v_rcp_f32 v210, v210\n"
      "v_sub_f32 v178, v178, v110\n"
      "v_sub_f32 v117, v117, v110\n"
      "v_sub_f32_e64 v179, v179, v110\n"
      "v_pk_mul_f32 v[220:221], v[220:221], v[210:211] op_sel_hi:[1,0]\n"
      "v_sub_f32 v115, v115, v110\n"
      "v_exp_f32 v115, v115\n"
      "v_sub_f32 v116, v116, v110\n"
      "v_mfma_f32_16x16x32_bf16 v[152:155], v[214:217], v[134:137], 0\n"
      "v_exp_f32 v178, v178\n"
      "v_exp_f32 v177, v177\n"
      "v_mfma_f32_16x16x32_bf16 v[152:155], v[84:87], v[88:91], v[152:155]\n"
      "v_exp_f32 v179, v179\n"
      "v_sub_f32 v114, v114, v110\n"
      "v_mfma_f32_16x16x32_bf16 v[134:137], v[158:161], v[104:107], v[64:67]\n"
      "v_exp_f32 v176, v176\n"
      "v_exp_f32_e64 v114, v114\n"
      "v_cvt_pk_bf16_f32 v176, v176, v177\n"
      "v_cvt_pk_bf16_f32 v177, v178, v179\n"
      "v_pk_mul_f32 v[120:121], v[120:121], v[210:211] op_sel_hi:[1,0]\n"
      "v_pk_mul_f32 v[152:153], v[152:153], v[210:211] op_sel_hi:[1,0]\n"
      "v_pk_mul_f32 v[154:155], v[154:155], v[210:211] op_sel_hi:[1,0]\n"
      "v_sub_f32_e64 v186, v186, v110\n"
      "v_pk_mul_f32 v[118:119], v[118:119], v[210:211] op_sel_hi:[1,0]\n"
      "v_pk_mul_f32 v[218:219], v[218:219], v[210:211] op_sel_hi:[1,0]\n"
      "v_exp_f32 v116, v116\n"
      "v_cvt_pk_bf16_f32 v178, v114, v115\n"
      "v_mfma_f32_16x16x32_bf16 v[210:213], v[96:99], v[104:107], v[68:71]\n"
      "v_exp_f32 v117, v117\n"
      "v_mfma_f32_16x16x32_bf16 v[146:149], v[100:103], v[104:107], v[72:75]\n"
      "ds_read_b128 v[122:125], v2 offset:12544\n"
      "v_cvt_pk_bf16_f32 v179, v116, v117\n"
      "v_mfma_f32_16x16x32_bf16 v[114:117], v[138:141], v[104:107], v[28:31]\n"
      "ds_read_b128 v[104:107], v2 offset:15616\n"
      "v_cvt_pk_bf16_f32 v152, v152, v153\n"
      "v_mfma_f32_16x16x16_bf16 v[134:137], v[184:185], v[112:113], v[134:137]\n"
      "v_sub_f32_e64 v202, v202, v110\n"
      "v_cvt_pk_bf16_f32 v153, v154, v155\n"
      "v_sub_f32 v187, v187, v110\n"
      "v_mfma_f32_16x16x16_bf16 v[210:213], v[108:109], v[112:113], v[210:213]\n"
      "v_sub_f32 v188, v188, v110\n"
      "v_sub_f32_e64 v189, v189, v110\n"
      "v_cvt_pk_bf16_f32 v218, v218, v219\n"
      "v_mfma_f32_16x16x16_bf16 v[146:149], v[206:207], v[112:113], v[146:149]\n"
      "v_cvt_pk_bf16_f32 v219, v220, v221\n"
      "v_exp_f32_e64 v188, v188\n"
      "v_mfma_f32_16x16x16_bf16 v[114:117], v[156:157], v[112:113], v[114:117]\n"
      "v_cvt_pk_bf16_f32 v221, v120, v121\n"
      "v_cvt_pk_bf16_f32 v220, v118, v119\n"
      "v_max3_f32 v90, v134, v135, v136\n"
      "v_mfma_f32_16x16x32_bf16 v[162:165], a[228:231], v[126:129], v[162:165]\n"
      "s_waitcnt lgkmcnt(1)\n"
      "v_exp_f32 v202, v202\n"
      "v_mfma_f32_16x16x32_bf16 v[162:165], a[232:235], v[122:125], v[162:165]\n"
      "s_waitcnt lgkmcnt(0)\n"
      "v_exp_f32 v187, v187\n"
      "v_mfma_f32_16x16x32_bf16 v[162:165], a[236:239], v[104:107], v[162:165]\n"
      "v_exp_f32 v189, v189\n"
      "v_mfma_f32_16x16x32_bf16 v[180:183], a[252:255], v[126:129], v[180:183]\n"
      "v_exp_f32 v186, v186\n"
      "v_mfma_f32_16x16x32_bf16 v[180:183], v[224:227], v[122:125], v[180:183]\n"
      "ds_read_b64 v[88:89], v10 offset:1184\n"
      "v_cvt_pk_bf16_f32 v94, v202, 0\n"
      "v_mfma_f32_16x16x32_bf16 v[180:183], v[228:231], v[104:107], v[180:183]\n"
      "v_cvt_pk_bf16_f32 v93, v188, v189\n"
      "v_max3_f32 v90, v90, v212, v213\n"
      "v_mfma_f32_16x16x32_bf16 v[170:173], v[244:247], v[126:129], v[170:173]\n"
      "v_max3_f32 v189, v137, v210, v211\n"
      "v_cvt_pk_bf16_f32 v92, v186, v187\n"
      "v_mfma_f32_16x16x32_bf16 v[170:173], v[248:251], v[122:125], v[170:173]\n"
      "v_max3_f32 v189, v189, v146, v147\n"
      "v_fma_f32 v150, v166, v166, s52\n"
      "v_mfma_f32_16x16x32_bf16 v[170:173], v[252:255], v[104:107], v[170:173]\n"
      "ds_read_b128 v[126:129], v10 offset:1168\n"
      "v_max3_f32 v90, v90, v148, v149\n"
      "v_mfma_f32_16x16x32_bf16 v[116:119], v[32:35], v[176:179], 0\n"
      "v_max3_f32 v90, v90, v189, v114\n"
      "v_mov_b32 v122, v90\n"
      "v_mfma_f32_16x16x32_bf16 v[116:119], v[32:35], v[92:95], v[116:119]\n"
      "s_nop 0\n"
      "v_permlane16_swap_b32_e64 v90, v122\n"
      "v_mfma_f32_16x16x32_bf16 v[186:189], v[130:133], v[176:179], 0\n"
      "ds_write_b64 v4, v[152:153] offset:18432\n"
      "v_mfma_f32_16x16x32_bf16 v[186:189], v[76:79], v[92:95], v[186:189]\n"
      "v_max_f32 v90, v90, v122\n"
      "v_mov_b32 v122, v90\n"
      "v_mfma_f32_16x16x32_bf16 v[152:155], v[194:197], v[176:179], 0\n"
      "s_nop 0\n"
      "v_permlane32_swap_b32 v90, v122\n"
      "v_mfma_f32_16x16x32_bf16 v[152:155], v[80:83], v[92:95], v[152:155]\n"
      "v_max_f32 v90, v90, v122\n"
      "v_sub_f32 v135, v135, v90\n"
      "v_mfma_f32_16x16x32_bf16 v[120:123], v[214:217], v[176:179], 0\n"
      "v_sub_f32 v137, v137, v90\n"
      "v_sub_f32 v149, v149, v90\n"
      "v_mfma_f32_16x16x32_bf16 v[120:123], v[84:87], v[92:95], v[120:123]\n"
      "s_waitcnt lgkmcnt(1)\n"
      "v_exp_f32 v135, v135\n"
      "v_mfma_f32_16x16x32_bf16 v[174:177], v[126:129], v[158:161], v[30:33]\n"
      "v_rcp_f32 v116, v116\n"
      "v_mfma_f32_16x16x32_bf16 v[110:113], v[126:129], v[96:99], v[30:33]\n"
      "ds_read_b128 v[158:161], v2 offset:512\n"
      "v_sub_f32 v211, v211, v90\n"
      "v_mfma_f32_16x16x32_bf16 v[96:99], v[126:129], v[100:103], v[30:33]\n"
      "ds_read_b128 v[100:103], v2 offset:6656\n"
      "v_sub_f32 v212, v212, v90\n"
      "v_mfma_f32_16x16x32_bf16 v[104:107], v[126:129], v[138:141], v[30:33]\n"
      "ds_read_b128 v[202:205], v2 offset:3584\n"
      "v_sub_f32 v148, v148, v90\n"
      "v_mfma_f32_16x16x16_bf16 v[174:177], v[88:89], v[184:185], v[174:177]\n"
      "v_sub_f32 v134, v134, v90\n"
      "v_mul_f32 v151, v167, v167\n"
      "v_exp_f32 v134, v134\n"
      "v_mfma_f32_16x16x16_bf16 v[110:113], v[88:89], v[108:109], v[110:113]\n"
      "v_sub_f32 v146, v146, v90\n"
      "v_exp_f32 v148, v148\n"
      "v_mfma_f32_16x16x16_bf16 v[96:99], v[88:89], v[206:207], v[96:99]\n"
      "v_sub_f32 v213, v213, v90\n"
      "v_mul_f32 v223, v163, v163\n"
      "v_exp_f32 v149, v149\n"
      "v_mfma_f32_16x16x16_bf16 v[104:107], v[88:89], v[156:157], v[104:107]\n"
      "v_fma_f32 v222, v162, v162, s52\n"
      "v_sub_f32 v136, v136, v90\n"
      "v_exp_f32 v137, v137\n"
      "v_sub_f32_e64 v114, v114, v90\n"
      "v_exp_f32_e64 v136, v136\n"
      "v_pk_mul_f32 v[186:187], v[186:187], v[116:117] op_sel_hi:[1,0]\n"
      "v_pk_fma_f32 v[222:223], v[164:165], v[164:165], v[222:223]\n"
      "v_pk_mul_f32 v[120:121], v[120:121], v[116:117] op_sel_hi:[1,0]\n"
      "v_pk_mul_f32 v[154:155], v[154:155], v[116:117] op_sel_hi:[1,0]\n"
      "v_pk_fma_f32 v[150:151], v[168:169], v[168:169], v[150:151]\n"
      "v_pk_fma_f32 v[222:223], v[180:181], v[180:181], v[222:223]\n"
      "v_cvt_pk_bf16_f32 v120, v120, v121\n"
      "v_pk_mul_f32 v[152:153], v[152:153], v[116:117] op_sel_hi:[1,0]\n"
      "v_pk_fma_f32 v[222:223], v[182:183], v[182:183], v[222:223]\n"
      "v_pk_mul_f32 v[122:123], v[122:123], v[116:117] op_sel_hi:[1,0]\n"
      "v_cvt_pk_bf16_f32 v89, v148, v149\n"
      "v_sub_f32_e64 v210, v210, v90\n"
      "v_pk_mul_f32 v[188:189], v[188:189], v[116:117] op_sel_hi:[1,0]\n"
      "v_pk_fma_f32 v[222:223], v[170:171], v[170:171], v[222:223]\n"
      "v_pk_fma_f32 v[222:223], v[172:173], v[172:173], v[222:223]\n"
      "v_pk_fma_f32 v[150:151], v[198:199], v[198:199], v[150:151]\n"
      "s_waitcnt lgkmcnt(0)\n"
      "v_mfma_f32_16x16x32_bf16 v[126:129], a[216:219], v[158:161], 0\n"
      "v_cndmask_b32 v174, v174, v110, s[58:59]\n"
      "v_sub_f32 v147, v147, v90\n"
      "v_mfma_f32_16x16x32_bf16 v[126:129], a[220:223], v[202:205], v[126:129]\n"
      "v_exp_f32_e64 v213, v213\n"
      "v_mfma_f32_16x16x32_bf16 v[126:129], a[224:227], v[100:103], v[126:129]\n"
      "ds_read_b128 v[176:179], v2 offset:12800\n"
      "v_cvt_pk_bf16_f32 v121, v122, v123\n"
      "v_mfma_f32_16x16x32_bf16 v[116:119], a[240:243], v[158:161], 0\n"
      "ds_read_b128 v[122:125], v2 offset:9728\n"
      "v_cndmask_b32 v174, v174, v96, s[60:61]\n"
      "v_mfma_f32_16x16x32_bf16 v[116:119], a[244:247], v[202:205], v[116:119]\n"
      "ds_read_b128 v[138:141], v2 offset:15872\n"
      "v_cvt_pk_bf16_f32 v186, v186, v187\n"
      "v_mfma_f32_16x16x32_bf16 v[116:119], a[248:251], v[100:103], v[116:119]\n"
      "ds_write_b128 v3, v[218:221] offset:18432\n"
      "v_pk_fma_f32 v[150:151], v[200:201], v[200:201], v[150:151]\n"
      "v_pk_fma_f32 v[150:151], v[142:143], v[142:143], v[150:151]\n"
      "v_cndmask_b32 v174, v174, v104, s[62:63]\n"
      "v_mfma_f32_16x16x32_bf16 v[108:111], v[232:235], v[158:161], 0\n"
      "v_exp_f32 v147, v147\n"
      "v_exp_f32 v146, v146\n"
      "v_max_f32_dpp v88, v174, v174 row_ror:8 row_mask:0xf bank_mask:0xf\n"
      "v_mfma_f32_16x16x32_bf16 v[108:111], v[236:239], v[202:205], v[108:111]\n"
      "s_nop 0\n"
      "v_max_f32_dpp v88, v88, v88 row_ror:4 row_mask:0xf bank_mask:0xf\n"
      "v_cvt_pk_bf16_f32 v134, v134, v135\n"
      "v_mfma_f32_16x16x32_bf16 v[108:111], v[240:243], v[100:103], v[108:111]\n"
      "s_waitcnt lgkmcnt(1)\n"
      "v_max_f32_dpp v88, v88, v88 row_ror:2 row_mask:0xf bank_mask:0xf\n"
      "v_cvt_pk_bf16_f32 v187, v188, v189\n"
      "v_add_f32 v222, v222, v223\n"
      "v_mfma_f32_16x16x32_bf16 v[126:129], a[228:231], v[122:125], v[126:129]\n"
      "v_exp_f32 v212, v212\n"
      "v_mfma_f32_16x16x32_bf16 v[126:129], a[232:235], v[176:179], v[126:129]\n"
      "ds_write_b64 v4, v[120:121] offset:18688\n"
      "v_mfma_f32_16x16x32_bf16 v[126:129], a[236:239], v[138:141], v[126:129]\n"
      "v_max_f32_dpp v88, v88, v88 row_ror:1 row_mask:0xf bank_mask:0xf\n"
      "v_exp_f32_e64 v210, v210\n"
      "v_mfma_f32_16x16x32_bf16 v[116:119], a[252:255], v[122:125], v[116:119]\n"
      "v_cvt_pk_bf16_f32 v189, v154, v155\n"
      "v_max_f32_dpp v88, v88, v88 row_bcast:15 row_mask:0xa bank_mask:0xf\n"
      "v_cvt_pk_bf16_f32 v188, v152, v153\n"
      "v_mfma_f32_16x16x32_bf16 v[116:119], v[224:227], v[176:179], v[116:119]\n"
      "v_cvt_pk_bf16_f32 v135, v136, v137\n"
      "v_cvt_pk_bf16_f32 v137, v212, v213\n"
      "v_mfma_f32_16x16x32_bf16 v[116:119], v[228:231], v[138:141], v[116:119]\n"
      "v_max_f32_dpp v88, v88, v88 row_bcast:31 row_mask:0xc bank_mask:0xf\n"
      "v_mul_f32_e64 v213, v127, v127\n"
      "v_readlane_b32 s72, v88, 63\n"
      "v_fma_f32 v212, v126, v126, s52\n"
      "v_mfma_f32_16x16x32_bf16 v[108:111], v[244:247], v[122:125], v[108:111]\n"
      "v_subrev_f32 v174, s72, v174\n"
      "v_cvt_pk_bf16_f32 v88, v146, v147\n"
      "v_mfma_f32_16x16x32_bf16 v[108:111], v[248:251], v[176:179], v[108:111]\n"
      "v_exp_f32 v211, v211\n"
      "v_mfma_f32_16x16x32_bf16 v[108:111], v[252:255], v[138:141], v[108:111]\n"
      "ds_write_b128 v3, v[186:189] offset:18688\n"
      "v_exp_f32_e64 v174, v174\n"
      "v_pk_fma_f32 v[212:213], v[128:129], v[128:129], v[212:213]\n"
      "v_pk_fma_f32 v[150:151], v[144:145], v[144:145], v[150:151]\n"
      "v_pk_fma_f32 v[212:213], v[116:117], v[116:117], v[212:213]\n"
      "v_exp_f32 v114, v114\n"
      "v_add_f32 v150, v150, v151\n"
      "v_cvt_pk_bf16_f32 v174, v174, v174\n"
      "v_cvt_pk_bf16_f32 v136, v210, v211\n"
      "v_pk_fma_f32 v[212:213], v[118:119], v[118:119], v[212:213]\n"
      "v_pk_fma_f32 v[212:213], v[108:109], v[108:109], v[212:213]\n"
      "v_pk_fma_f32 v[212:213], v[110:111], v[110:111], v[212:213]\n"
      "v_cvt_pk_bf16_f32 v90, v114, 0\n"
      "v_mfma_f32_16x16x32_bf16 v[218:221], v[32:35], v[134:137], 0\n"
      "v_permlane16_swap_b32 v150, v222\n"
      "v_mfma_f32_16x16x32_bf16 v[218:221], v[32:35], v[88:91], v[218:221]\n"
      "v_add_f32 v212, v212, v213\n"
      "v_add_f32 v150, v150, v222\n"
      "v_mov_b32 v222, v212\n"
      "v_mfma_f32_16x16x32_bf16 v[100:103], v[130:133], v[134:137], 0\n"
      "ds_write_b16 v13, v174\n"
      "v_permlane16_swap_b32_e64 v212, v222\n"
      "v_mfma_f32_16x16x32_bf16 v[100:103], v[76:79], v[88:91], v[100:103]\n"
      "s_nop 0\n"
      "v_rcp_f32 v218, v218\n"
      "v_mfma_f32_16x16x32_bf16 v[186:189], v[194:197], v[134:137], 0\n"
      "ds_read_b128 v[174:177], v14\n"
      "v_add_f32 v212, v212, v222\n"
      "v_mfma_f32_16x16x32_bf16 v[186:189], v[80:83], v[88:91], v[186:189]\n"
      "s_nop 0\n"
      "v_permlane32_swap_b32 v150, v212\n"
      "v_add_f32 v150, v150, v212\n"
      "v_mfma_f32_16x16x32_bf16 v[220:223], v[214:217], v[134:137], 0\n"
      "ds_write_b32 v31, v150 offset:1024\n"
      "v_mfma_f32_16x16x32_bf16 v[220:223], v[84:87], v[88:91], v[220:223]\n"
      "ds_read_b128 v[210:213], v14 offset:64\n"
      "ds_write_b32 v31, v190 offset:5632\n"
      "v_pk_mul_f32 v[188:189], v[188:189], v[218:219] op_sel_hi:[1,0]\n"
      "v_pk_mul_f32 v[100:101], v[100:101], v[218:219] op_sel_hi:[1,0]\n"
      "v_cvt_pk_bf16_f32 v100, v100, v101\n"
      "v_pk_mul_f32 v[186:187], v[186:187], v[218:219] op_sel_hi:[1,0]\n"
      "v_pk_mul_f32 v[102:103], v[102:103], v[218:219] op_sel_hi:[1,0]\n"
      "v_cvt_pk_bf16_f32 v101, v102, v103\n"
      "v_cvt_pk_bf16_f32 v102, v186, v187\n"
      "v_cvt_pk_bf16_f32 v103, v188, v189\n"
      "s_add_u32 s42, s42, 1\n"
      "ds_write_b128 v3, v[100:103] offset:18944\n"
      "v_pk_mul_f32 v[222:223], v[222:223], v[218:219] op_sel_hi:[1,0]\n"
      "v_pk_mul_f32 v[220:221], v[220:221], v[218:219] op_sel_hi:[1,0]\n"
      "v_cvt_pk_bf16_f32 v220, v220, v221\n"
      "s_waitcnt lgkmcnt(3)\n"
      "v_mfma_f32_16x16x32_bf16 v[188:191], v[174:177], v[32:35], 0\n"
      "s_waitcnt lgkmcnt(2)\n"
      "v_cvt_pk_bf16_f32 v221, v222, v223\n"
      "v_add_u32 v11, 2336, v11\n"
      "v_mfma_f32_16x16x32_bf16 v[188:191], v[210:213], v[32:35], v[188:191]\n"
      "ds_write_b64 v4, v[220:221] offset:18944\n"
      "v_add_u32 v10, 2336, v10\n"
      "s_waitcnt lgkmcnt(0)\n"
      "s_barrier\n"
      "s_cbranch_vccnz L_disp0_%=\n"
      "L_max0_%=:\n"
      "v_mfma_f32_16x16x32_bf16 v[218:221], v[174:177], v[130:133], 0\n"
      "ds_read_b128 v[202:205], v0\n"
      "v_readlane_b32 s85, v37, s42\n"
      "v_mfma_f32_16x16x32_bf16 v[218:221], v[210:213], v[76:79], v[218:221]\n"
      "ds_read_b128 v[154:157], v9 offset:1024\n"
      "s_add_u32 m0, s64, 18432\n"
      "v_mfma_f32_16x16x32_bf16 v[134:137], v[174:177], v[194:197], 0\n"
      "ds_read_b128 v[150:153], v1 offset:8192\n"
      "ds_read_b128 v[184:187], v0 offset:6144\n"
      "v_mfma_f32_16x16x32_bf16 v[134:137], v[210:213], v[80:83], v[134:137]\n"
      "buffer_load_dwordx4 v16, s[16:19], s45 offen  lds\n"
      "v_mfma_f32_16x16x32_bf16 v[100:103], v[174:177], v[214:217], 0\n"
      "s_add_u32 m0, s64, 22528\n"
      "ds_read_b128 v[138:141], v1\n"
      "v_mfma_f32_16x16x32_bf16 v[100:103], v[210:213], v[84:87], v[100:103]\n"
      "ds_read_b128 v[210:213], v0 offset:14336\n"
      "s_waitcnt lgkmcnt(5)\n"
      "v_mfma_f32_16x16x32_bf16 v[206:209], a[0:3], v[202:205], 0\n"
      "ds_read_b128 v[176:179], v1 offset:6144\n"
      "v_readlane_b32 s84, v37, s43\n"
      "v_mfma_f32_16x16x32_bf16 v[104:107], a[24:27], v[202:205], 0\n"
      "s_waitcnt lgkmcnt(5)\n"
      "v_add_f32 v156, v156, v157\n"
      "v_add_f32 v154, v154, v155\n"
      "v_mfma_f32_16x16x32_bf16 v[96:99], a[48:51], v[202:205], 0\n"
      "buffer_load_dwordx4 v17, s[16:19], s45 offen  lds\n"
      "v_mfma_f32_16x16x32_bf16 v[146:149], a[72:75], v[202:205], 0\n"
      "s_or_b32 s95, s84, s85\n"
      "v_add_f32 v154, v154, v156\n"
      "v_cndmask_b32 v218, v218, v134, s[58:59]\n"
      "v_mfma_f32_16x16x32_bf16 v[122:125], a[96:99], v[202:205], 0\n"
      "s_waitcnt lgkmcnt(2)\n"
      "v_rsq_f32 v154, v154\n"
      "v_mfma_f32_16x16x32_bf16 v[112:115], a[120:123], v[202:205], 0\n"
      "ds_read_b128 v[134:137], v1 offset:12288\n"
      "v_mfma_f32_16x16x32_bf16 v[206:209], a[4:7], v[138:141], v[206:209]\n"
      "ds_read_b128 v[220:223], v0 offset:12288\n"
      "v_mul_f32 v154, 0x415db3d7, v154\n"
      "v_mfma_f32_16x16x32_bf16 v[206:209], a[8:11], v[184:187], v[206:209]\n"
      "s_add_u32 m0, s64, 26624\n"
      "v_readlane_b32 s86, v38, s43\n"
      "v_mov_b32 v156, v154\n"
      "v_mfma_f32_16x16x32_bf16 v[104:107], a[28:31], v[138:141], v[104:107]\n"
      "s_or_b32 s95, s95, s48\n"
      "v_permlane32_swap_b32 v154, v156\n"
      "v_mfma_f32_16x16x32_bf16 v[104:107], a[32:35], v[184:187], v[104:107]\n"
      "s_cmp_ge_u32 s95, 0x40000000\n"
      "s_cselect_b32 s94, -1, -2\n"
      "v_mov_b32 v155, v154\n"
      "v_mfma_f32_16x16x32_bf16 v[96:99], a[52:55], v[138:141], v[96:99]\n"
      "buffer_load_dwordx4 v18, s[16:19], s45 offen  lds\n"
      "v_mfma_f32_16x16x32_bf16 v[96:99], a[56:59], v[184:187], v[96:99]\n"
      "v_permlane16_swap_b32_e64 v154, v155\n"
      "v_mfma_f32_16x16x32_bf16 v[146:149], a[76:79], v[138:141], v[146:149]\n"
      "s_add_u32 m0, s64, 30720\n"
      "v_mul_f32 v168, v154, v168\n"
      "v_mul_f32 v143, v154, v143\n"
      "v_mfma_f32_16x16x32_bf16 v[146:149], a[80:83], v[184:187], v[146:149]\n"
      "v_mul_f32 v166, v154, v166\n"
      "v_mul_f32 v198, v154, v198\n"
      "v_mfma_f32_16x16x32_bf16 v[122:125], a[100:103], v[138:141], v[122:125]\n"
      "v_mul_f32 v200, v154, v200\n"
      "v_mul_f32 v180, v155, v180\n"
      "v_mfma_f32_16x16x32_bf16 v[122:125], a[104:107], v[184:187], v[122:125]\n"
      "s_waitcnt lgkmcnt(0)\n"
      "v_mul_f32 v169, v154, v169\n"
      "v_mul_f32_e64 v170, v155, v170\n"
      "v_mfma_f32_16x16x32_bf16 v[112:115], a[124:127], v[138:141], v[112:115]\n"
      "buffer_load_dwordx4 v19, s[16:19], s45 offen  lds\n"
      "v_mfma_f32_16x16x32_bf16 v[112:115], a[128:131], v[184:187], v[112:115]\n"
      "s_add_u32 m0, s65, 34816\n"
      "v_mul_f32 v167, v154, v167\n"
      "v_mul_f32 v199, v154, v199\n"
      "v_mfma_f32_16x16x32_bf16 v[206:209], a[12:15], v[176:179], v[206:209]\n"
      "v_cvt_pk_bf16_f32 v166, v166, v167\n"
      "v_cvt_pk_bf16_f32 v167, v168, v169\n"
      "v_mfma_f32_16x16x32_bf16 v[206:209], a[16:19], v[220:223], v[206:209]\n"
      "v_cvt_pk_bf16_f32 v168, v198, v199\n"
      "v_mul_f32_e64 v201, v154, v201\n"
      "v_mfma_f32_16x16x32_bf16 v[206:209], a[20:23], v[134:137], v[206:209]\n"
      "v_cvt_pk_bf16_f32 v169, v200, v201\n"
      "v_mul_f32_e64 v142, v154, v142\n"
      "v_mfma_f32_16x16x32_bf16 v[104:107], a[36:39], v[176:179], v[104:107]\n"
      "buffer_load_dwordx4 v15, s[16:19], s45 offen  lds\n"
      "v_mfma_f32_16x16x32_bf16 v[104:107], a[40:43], v[220:223], v[104:107]\n"
      "s_cmp_gt_u32 s42, s44\n"
      "s_cselect_b32 s95, s94, s95\n"
      "v_cvt_pk_bf16_f32 v142, v142, v143\n"
      "v_mfma_f32_16x16x32_bf16 v[104:107], a[44:47], v[134:137], v[104:107]\n"
      "ds_write_b128 v5, v[166:169]\n"
      "v_pk_mul_f32 v[144:145], v[144:145], v[154:155] op_sel_hi:[1,0]\n"
      "v_pk_mul_f32 v[164:165], v[164:165], v[154:155] op_sel:[0,1]\n"
      "v_pk_mul_f32 v[162:163], v[162:163], v[154:155] op_sel:[0,1]\n"
      "v_pk_mul_f32 v[182:183], v[182:183], v[154:155] op_sel:[0,1]\n"
      "v_mfma_f32_16x16x32_bf16 v[96:99], a[60:63], v[176:179], v[96:99]\n"
      "buffer_load_dwordx4 v[158:161], v36, s[28:31], s47 offen\n"
      "v_mfma_f32_16x16x32_bf16 v[96:99], a[64:67], v[220:223], v[96:99]\n"
      "ds_read_b128 v[166:169], v1 offset:2048\n"
      "v_cvt_pk_bf16_f32 v143, v144, v145\n"
      "v_mfma_f32_16x16x32_bf16 v[96:99], a[68:71], v[134:137], v[96:99]\n"
      "ds_write_b64 v6, v[142:143]\n"
      "v_mfma_f32_16x16x32_bf16 v[146:149], a[84:87], v[176:179], v[146:149]\n"
      "ds_read_b128 v[198:201], v0 offset:2048\n"
      "v_mul_f32_e64 v173, v155, v173\n"
      "v_mfma_f32_16x16x32_bf16 v[146:149], a[88:91], v[220:223], v[146:149]\n"
      "v_cvt_pk_bf16_f32 v162, v162, v163\n"
      "v_mul_f32 v111, v156, v111\n"
      "v_mfma_f32_16x16x32_bf16 v[146:149], a[92:95], v[134:137], v[146:149]\n"
      "v_mul_f32 v126, v156, v126\n"
      "v_fma_f32 v175, v206, v206, s51\n"
      "v_mfma_f32_16x16x32_bf16 v[122:125], a[108:111], v[176:179], v[122:125]\n"
      "buffer_load_dword v90, v29, s[28:31], s47 offen\n"
      "v_mfma_f32_16x16x32_bf16 v[122:125], a[112:115], v[220:223], v[122:125]\n"
      "v_mov_b32_dpp v188, v188 quad_perm:[0,0,0,0] row_mask:0xf bank_mask:0xf\n"
      "v_mul_f32 v181, v155, v181\n"
      "v_mfma_f32_16x16x32_bf16 v[122:125], a[116:119], v[134:137], v[122:125]\n"
      "v_mul_f32 v127, v156, v127\n"
      "v_mul_f32_e64 v171, v155, v171\n"
      "v_mfma_f32_16x16x32_bf16 v[112:115], a[132:135], v[176:179], v[112:115]\n"
      "v_cvt_pk_bf16_f32 v163, v164, v165\n"
      "v_mul_f32_e64 v116, v156, v116\n"
      "v_mfma_f32_16x16x32_bf16 v[112:115], a[136:139], v[220:223], v[112:115]\n"
      "v_fma_f32 v82, v146, v146, s51\n"
      "v_mul_f32_e64 v172, v155, v172\n"
      "v_mfma_f32_16x16x32_bf16 v[112:115], a[140:143], v[134:137], v[112:115]\n"
      "v_cvt_pk_bf16_f32 v170, v170, v171\n"
      "v_cvt_pk_bf16_f32 v164, v180, v181\n"
      "v_mfma_f32_16x16x32_bf16 v[130:133], v[202:205], a[144:147], 0\n"
      "v_cvt_pk_bf16_f32 v165, v182, v183\n"
      "v_fmac_f32_e64 v175, v207, v207\n"
      "v_mfma_f32_16x16x32_bf16 v[130:133], v[138:141], a[148:151], v[130:133]\n"
      "ds_read_b128 v[142:145], v9 offset:5632\n"
      "v_fmac_f32_e64 v175, v208, v208\n"
      "v_mfma_f32_16x16x32_bf16 v[130:133], v[184:187], a[152:155], v[130:133]\n"
      "ds_write_b128 v5, v[162:165] offset:6144\n"
      "v_mfma_f32_16x16x32_bf16 v[130:133], v[176:179], a[156:159], v[130:133]\n"
      "v_fmac_f32 v175, v209, v209\n"
      "v_fmac_f32 v175, v104, v104\n"
      "v_mfma_f32_16x16x32_bf16 v[130:133], v[220:223], a[160:163], v[130:133]\n"
      "ds_read_b128 v[162:165], v0 offset:8192\n"
      "v_cvt_pk_bf16_f32 v171, v172, v173\n"
      "v_mfma_f32_16x16x32_bf16 v[130:133], v[134:137], a[164:167], v[130:133]\n"
      "v_rcp_f32_e64 v188, v188\n"
      "v_mfma_f32_16x16x32_bf16 v[194:197], v[202:205], a[168:171], 0\n"
      "v_mul_f32 v109, v156, v109\n"
      "v_mul_f32 v129, v156, v129\n"
      "v_mfma_f32_16x16x32_bf16 v[194:197], v[138:141], a[172:175], v[194:197]\n"
      "v_cndmask_b32 v218, v218, v100, s[60:61]\n"
      "v_fmac_f32 v175, v105, v105\n"
      "v_mfma_f32_16x16x32_bf16 v[194:197], v[184:187], a[176:179], v[194:197]\n"
      "v_fmac_f32 v82, v147, v147\n"
      "v_fmac_f32 v175, v106, v106\n"
      "v_mfma_f32_16x16x32_bf16 v[194:197], v[176:179], a[180:183], v[194:197]\n"
      "v_mul_f32 v108, v156, v108\n"
      "v_mul_f32 v110, v156, v110\n"
      "v_mfma_f32_16x16x32_bf16 v[194:197], v[220:223], a[184:187], v[194:197]\n"
      "v_fmac_f32 v175, v107, v107\n"
      "v_fmac_f32_e64 v82, v148, v148\n"
      "v_mfma_f32_16x16x32_bf16 v[194:197], v[134:137], a[188:191], v[194:197]\n"
      "ds_write_b64 v6, v[170:171] offset:6144\n"
      "v_mfma_f32_16x16x32_bf16 v[214:217], v[202:205], a[192:195], 0\n"
      "ds_read_b128 v[180:183], v1 offset:14336\n"
      "v_mul_f32_e64 v119, v156, v119\n"
      "v_mfma_f32_16x16x32_bf16 v[214:217], v[138:141], a[196:199], v[214:217]\n"
      "v_fmac_f32 v175, v96, v96\n"
      "v_fmac_f32 v82, v149, v149\n"
      "v_mfma_f32_16x16x32_bf16 v[214:217], v[184:187], a[200:203], v[214:217]\n"
      "v_cvt_pk_bf16_f32 v108, v108, v109\n"
      "v_mul_f32_e64 v118, v156, v118\n"
      "v_mfma_f32_16x16x32_bf16 v[214:217], v[176:179], a[204:207], v[214:217]\n"
      "ds_read_b128 v[170:173], v0 offset:16384\n"
      "v_cvt_pk_bf16_f32 v109, v110, v111\n"
      "v_mfma_f32_16x16x32_bf16 v[214:217], v[220:223], a[208:211], v[214:217]\n"
      "v_fmac_f32 v175, v97, v97\n"
      "v_mul_f32 v218, v188, v218\n"
      "v_mfma_f32_16x16x32_bf16 v[214:217], v[134:137], a[212:215], v[214:217]\n"
      "s_waitcnt lgkmcnt(3)\n"
      "v_fmac_f32 v82, v122, v122\n"
      "v_fmac_f32 v82, v123, v123\n"
      "v_mfma_f32_16x16x32_bf16 v[134:137], a[0:3], v[198:201], 0\n"
      "ds_read_b128 v[202:205], v0 offset:4096\n"
      "v_fmac_f32 v82, v124, v124\n"
      "v_mfma_f32_16x16x32_bf16 v[134:137], a[4:7], v[166:169], v[134:137]\n"
      "ds_read_b128 v[220:223], v1 offset:10240\n"
      "v_fmac_f32 v82, v125, v125\n"
      "v_mfma_f32_16x16x32_bf16 v[134:137], a[8:11], v[162:165], v[134:137]\n"
      "s_waitcnt lgkmcnt(3)\n"
      "v_mul_f32_e64 v117, v156, v117\n"
      "v_cvt_pk_bf16_f32 v126, v126, v127\n"
      "v_mfma_f32_16x16x32_bf16 v[134:137], a[12:15], v[150:153], v[134:137]\n"
      "ds_write_b64 v6, v[108:109] offset:12288\n"
      "v_mfma_f32_16x16x32_bf16 v[134:137], a[16:19], v[210:213], v[134:137]\n"
      "ds_read_b128 v[190:193], v1 offset:16384\n"
      "v_fmac_f32 v82, v112, v112\n"
      "v_mfma_f32_16x16x32_bf16 v[134:137], a[20:23], v[180:183], v[134:137]\n"
      "v_fmac_f32 v175, v98, v98\n"
      "v_cvt_pk_bf16_f32 v214, v214, v215\n"
      "v_mfma_f32_16x16x32_bf16 v[138:141], a[24:27], v[198:201], 0\n"
      "v_fmac_f32 v82, v113, v113\n"
      "v_fmac_f32 v175, v99, v99\n"
      "v_mfma_f32_16x16x32_bf16 v[138:141], a[28:31], v[166:169], v[138:141]\n"
      "v_fmac_f32 v82, v114, v114\n"
      "v_fmac_f32 v82, v115, v115\n"
      "v_mfma_f32_16x16x32_bf16 v[138:141], a[32:35], v[162:165], v[138:141]\n"
      "v_cvt_pk_bf16_f32 v218, v218, v218\n"
      "v_fma_f32 v178, v134, v134, s51\n"
      "v_mfma_f32_16x16x32_bf16 v[138:141], a[36:39], v[150:153], v[138:141]\n"
      "v_permlane16_swap_b32 v175, v82\n"
      "v_mfma_f32_16x16x32_bf16 v[138:141], a[40:43], v[210:213], v[138:141]\n"
      "v_cvt_pk_bf16_f32 v194, v194, v195\n"
      "v_mul_f32 v128, v156, v128\n"
      "v_mfma_f32_16x16x32_bf16 v[138:141], a[44:47], v[180:183], v[138:141]\n"
      "v_cvt_pk_bf16_f32 v127, v128, v129\n"
      "v_add_f32 v175, v175, v82\n"
      "v_mfma_f32_16x16x32_bf16 v[186:189], a[48:51], v[198:201], 0\n"
      "v_mov_b32 v82, v175\n"
      "v_cvt_pk_bf16_f32 v130, v130, v131\n"
      "v_mfma_f32_16x16x32_bf16 v[186:189], a[52:55], v[166:169], v[186:189]\n"
      "v_permlane32_swap_b32 v175, v82\n"
      "v_mfma_f32_16x16x32_bf16 v[186:189], a[56:59], v[162:165], v[186:189]\n"
      "v_cvt_pk_bf16_f32 v128, v116, v117\n"
      "v_add_f32 v175, v175, v82\n"
      "v_mfma_f32_16x16x32_bf16 v[186:189], a[60:63], v[150:153], v[186:189]\n"
      "v_fmac_f32_e64 v178, v135, v135\n"
      "v_cvt_pk_bf16_f32 v129, v118, v119\n"
      "v_mfma_f32_16x16x32_bf16 v[186:189], a[64:67], v[210:213], v[186:189]\n"
      "ds_write_b128 v5, v[126:129] offset:12288\n"
      "v_mfma_f32_16x16x32_bf16 v[186:189], a[68:71], v[180:183], v[186:189]\n"
      "v_rsq_f32 v175, v175\n"
      "v_mfma_f32_16x16x32_bf16 v[154:157], a[72:75], v[198:201], 0\n"
      "v_fmac_f32 v178, v136, v136\n"
      "v_mov_b32 v82, v175\n"
      "v_mfma_f32_16x16x32_bf16 v[154:157], a[76:79], v[166:169], v[154:157]\n"
      "ds_write_b16 v12, v218 offset:16\n"
      "v_mfma_f32_16x16x32_bf16 v[154:157], a[80:83], v[162:165], v[154:157]\n"
      "v_permlane16_swap_b32 v175, v82\n"
      "v_mfma_f32_16x16x32_bf16 v[154:157], a[84:87], v[150:153], v[154:157]\n"
      "v_mul_f32 v175, s50, v175\n"
      "v_mul_f32 v209, v175, v209\n"
      "v_mfma_f32_16x16x32_bf16 v[154:157], a[88:91], v[210:213], v[154:157]\n"
      "v_mul_f32 v99, v175, v99\n"
      "v_fmac_f32 v178, v137, v137\n"
      "v_mfma_f32_16x16x32_bf16 v[154:157], a[92:95], v[180:183], v[154:157]\n"
      "v_cvt_pk_bf16_f32 v215, v216, v217\n"
      "v_mul_f32 v98, v175, v98\n"
      "v_mfma_f32_16x16x32_bf16 v[116:119], a[96:99], v[198:201], 0\n"
      "v_mul_f32 v112, v82, v112\n"
      "v_or3_b32 v142, v142, v143, v144\n"
      "v_mfma_f32_16x16x32_bf16 v[116:119], a[100:103], v[166:169], v[116:119]\n"
      "v_mul_f32 v146, v82, v146\n"
      "v_mul_f32 v97, v175, v97\n"
      "v_mfma_f32_16x16x32_bf16 v[116:119], a[104:107], v[162:165], v[116:119]\n"
      "v_mul_f32 v147, v82, v147\n"
      "v_cvt_pk_bf16_f32 v146, v146, v147\n"
      "v_mfma_f32_16x16x32_bf16 v[116:119], a[108:111], v[150:153], v[116:119]\n"
      "v_mul_f32 v114, v82, v114\n"
      "v_mul_f32_e64 v104, v175, v104\n"
      "v_mfma_f32_16x16x32_bf16 v[116:119], a[112:115], v[210:213], v[116:119]\n"
      "v_fma_f32 v184, v154, v154, s51\n"
      "v_mul_f32 v148, v82, v148\n"
      "v_mfma_f32_16x16x32_bf16 v[116:119], a[116:119], v[180:183], v[116:119]\n"
      "v_fmac_f32 v184, v155, v155\n"
      "v_mul_f32 v96, v175, v96\n"
      "v_mfma_f32_16x16x32_bf16 v[108:111], a[120:123], v[198:201], 0\n"
      "v_mul_f32 v149, v82, v149\n"
      "v_mul_f32 v206, v175, v206\n"
      "v_mfma_f32_16x16x32_bf16 v[108:111], a[124:127], v[166:169], v[108:111]\n"
      "v_fmac_f32 v184, v156, v156\n"
      "v_mul_f32 v113, v82, v113\n"
      "v_mfma_f32_16x16x32_bf16 v[108:111], a[128:131], v[162:165], v[108:111]\n"
      "v_mul_f32 v107, v175, v107\n"
      "v_cvt_pk_bf16_f32 v195, v196, v197\n"
      "v_mfma_f32_16x16x32_bf16 v[108:111], a[132:135], v[150:153], v[108:111]\n"
      "ds_read_u16 v78, v11\n"
      "v_mul_f32 v207, v175, v207\n"
      "v_mfma_f32_16x16x32_bf16 v[108:111], a[136:139], v[210:213], v[108:111]\n"
      "v_mul_f32 v115, v82, v115\n"
      "v_fmac_f32_e64 v178, v138, v138\n"
      "v_mfma_f32_16x16x32_bf16 v[108:111], a[140:143], v[180:183], v[108:111]\n"
      "v_fmac_f32 v178, v139, v139\n"
      "v_mul_f32 v208, v175, v208\n"
      "v_mfma_f32_16x16x32_bf16 v[216:219], v[198:201], a[144:147], 0\n"
      "v_fmac_f32 v184, v157, v157\n"
      "v_mul_f32 v123, v82, v123\n"
      "v_mfma_f32_16x16x32_bf16 v[216:219], v[166:169], a[148:151], v[216:219]\n"
      "v_fmac_f32 v184, v116, v116\n"
      "v_cvt_pk_bf16_f32 v206, v206, v207\n"
      "v_mfma_f32_16x16x32_bf16 v[216:219], v[162:165], a[152:155], v[216:219]\n"
      "v_add_u32 v12, 32, v12\n"
      "v_fmac_f32 v184, v117, v117\n"
      "v_mfma_f32_16x16x32_bf16 v[216:219], v[150:153], a[156:159], v[216:219]\n"
      "v_cvt_pk_bf16_f32 v207, v208, v209\n"
      "v_fmac_f32 v184, v118, v118\n"
      "v_mfma_f32_16x16x32_bf16 v[216:219], v[210:213], a[160:163], v[216:219]\n"
      "v_bitop3_b32 v142, v142, v145, s33 bitop3:0xa8\n"
      "v_cvt_pk_bf16_f32 v96, v96, v97\n"
      "v_mfma_f32_16x16x32_bf16 v[216:219], v[180:183], a[164:167], v[216:219]\n"
      "v_fmac_f32 v178, v140, v140\n"
      "v_fmac_f32 v184, v119, v119\n"
      "v_mfma_f32_16x16x32_bf16 v[126:129], v[198:201], a[168:171], 0\n"
      "v_fmac_f32 v184, v108, v108\n"
      "v_fmac_f32 v178, v141, v141\n"
      "v_mfma_f32_16x16x32_bf16 v[126:129], v[166:169], a[172:175], v[126:129]\n"
      "v_cvt_pk_bf16_f32 v131, v132, v133\n"
      "v_cmp_ne_u32 s[56:57], 0, v142\n"
      "v_mfma_f32_16x16x32_bf16 v[126:129], v[162:165], a[176:179], v[126:129]\n"
      "v_mul_f32 v124, v82, v124\n"
      "v_fmac_f32 v178, v186, v186\n"
      "v_mfma_f32_16x16x32_bf16 v[126:129], v[150:153], a[180:183], v[126:129]\n"
      "v_fmac_f32 v178, v187, v187\n"
      "v_cvt_pk_bf16_f32 v147, v148, v149\n"
      "v_mfma_f32_16x16x32_bf16 v[126:129], v[210:213], a[184:187], v[126:129]\n"
      "ds_read_b128 v[142:145], v1 offset:4096\n"
      "v_fmac_f32 v178, v188, v188\n"
      "v_mfma_f32_16x16x32_bf16 v[126:129], v[180:183], a[188:191], v[126:129]\n"
      "v_fmac_f32 v184, v109, v109\n"
      "v_fmac_f32 v184, v110, v110\n"
      "v_mfma_f32_16x16x32_bf16 v[100:103], v[198:201], a[192:195], 0\n"
      "ds_read_b128 v[198:201], v0 offset:10240\n"
      "v_fmac_f32 v178, v189, v189\n"
      "v_mfma_f32_16x16x32_bf16 v[100:103], v[166:169], a[196:199], v[100:103]\n"
      "v_mul_f32 v125, v82, v125\n"
      "v_fmac_f32 v184, v111, v111\n"
      "v_mfma_f32_16x16x32_bf16 v[100:103], v[162:165], a[200:203], v[100:103]\n"
      "v_mul_f32 v105, v175, v105\n"
      "v_mul_f32 v122, v82, v122\n"
      "v_mfma_f32_16x16x32_bf16 v[100:103], v[150:153], a[204:207], v[100:103]\n"
      "ds_read_u16 v82, v11 offset:32\n"
      "v_mul_f32 v106, v175, v106\n"
      "v_mfma_f32_16x16x32_bf16 v[100:103], v[210:213], a[208:211], v[100:103]\n"
      "v_permlane16_swap_b32 v178, v184\n"
      "v_mfma_f32_16x16x32_bf16 v[100:103], v[180:183], a[212:215], v[100:103]\n"
      "s_waitcnt lgkmcnt(2)\n"
      "v_add_f32 v178, v178, v184\n"
      "v_mov_b32 v184, v178\n"
      "v_mfma_f32_16x16x32_bf16 v[210:213], a[0:3], v[202:205], 0\n"
      "s_nop 0\n"
      "v_permlane32_swap_b32 v178, v184\n"
      "v_add_f32 v178, v178, v184\n"
      "v_mfma_f32_16x16x32_bf16 v[210:213], a[4:7], v[142:145], v[210:213]\n"
      "s_waitcnt lgkmcnt(1)\n"
      "v_rsq_f32_e64 v178, v178\n"
      "v_mfma_f32_16x16x32_bf16 v[210:213], a[8:11], v[198:201], v[210:213]\n"
      "v_mov_b32 v184, v178\n"
      "v_cvt_pk_bf16_f32 v132, v216, v217\n"
      "v_mfma_f32_16x16x32_bf16 v[210:213], a[12:15], v[220:223], v[210:213]\n"
      "v_permlane16_swap_b32 v178, v184\n"
      "v_mfma_f32_16x16x32_bf16 v[210:213], a[16:19], v[170:173], v[210:213]\n"
      "v_mul_f32 v109, v184, v109\n"
      "v_mul_f32_e64 v178, s50, v178\n"
      "v_mfma_f32_16x16x32_bf16 v[210:213], a[20:23], v[190:193], v[210:213]\n"
      "s_add_u32 s43, s43, 1\n"
      "v_cvt_pk_bf16_f32 v97, v98, v99\n"
      "v_cvt_pk_bf16_f32 v112, v112, v113\n"
      "v_mfma_f32_16x16x32_bf16 v[150:153], a[24:27], v[202:205], 0\n"
      "v_mul_f32 v155, v184, v155\n"
      "v_mul_f32 v118, v184, v118\n"
      "v_mfma_f32_16x16x32_bf16 v[150:153], a[28:31], v[142:145], v[150:153]\n"
      "v_mul_f32_e64 v111, v184, v111\n"
      "v_fma_f32 v217, v210, v210, s51\n"
      "v_mfma_f32_16x16x32_bf16 v[150:153], a[32:35], v[198:201], v[150:153]\n"
      "ds_read_b128 v[166:169], v7 offset:18432\n"
      "v_mul_f32 v154, v184, v154\n"
      "v_mfma_f32_16x16x32_bf16 v[150:153], a[36:39], v[220:223], v[150:153]\n"
      "v_mul_f32 v108, v184, v108\n"
      "v_mul_f32_e64 v135, v178, v135\n"
      "v_mfma_f32_16x16x32_bf16 v[150:153], a[40:43], v[170:173], v[150:153]\n"
      "v_cvt_pk_bf16_f32 v209, v106, v107\n"
      "v_cvt_pk_bf16_f32 v133, v218, v219\n"
      "v_mfma_f32_16x16x32_bf16 v[150:153], a[44:47], v[190:193], v[150:153]\n"
      "v_mul_f32 v136, v178, v136\n"
      "v_mul_f32 v139, v178, v139\n"
      "v_mfma_f32_16x16x32_bf16 v[174:177], a[48:51], v[202:205], 0\n"
      "v_mul_f32_e64 v134, v178, v134\n"
      "v_cvt_pk_bf16_f32 v154, v154, v155\n"
      "v_mfma_f32_16x16x32_bf16 v[174:177], a[52:55], v[142:145], v[174:177]\n"
      "v_cvt_pk_bf16_f32 v108, v108, v109\n"
      "v_cvt_pk_bf16_f32 v208, v104, v105\n"
      "v_mfma_f32_16x16x32_bf16 v[174:177], a[56:59], v[198:201], v[174:177]\n"
      "ds_read_b128 v[162:165], v7 offset:30720\n"
      "v_cvt_pk_bf16_f32 v148, v122, v123\n"
      "v_mfma_f32_16x16x32_bf16 v[174:177], a[60:63], v[220:223], v[174:177]\n"
      "v_cvt_pk_bf16_f32 v113, v114, v115\n"
      "v_fmac_f32_e64 v217, v211, v211\n"
      "v_mfma_f32_16x16x32_bf16 v[174:177], a[64:67], v[170:173], v[174:177]\n"
      "ds_read_b128 v[180:183], v7 offset:22528\n"
      "v_cvt_pk_bf16_f32 v149, v124, v125\n"
      "v_mfma_f32_16x16x32_bf16 v[174:177], a[68:71], v[190:193], v[174:177]\n"
      "v_mul_f32 v137, v178, v137\n"
      "v_fmac_f32 v217, v212, v212\n"
      "v_mfma_f32_16x16x32_bf16 v[104:107], a[72:75], v[202:205], 0\n"
      "s_waitcnt lgkmcnt(2)\n"
      "v_fmac_f32 v217, v213, v213\n"
      "v_cvt_pk_bf16_f32 v196, v126, v127\n"
      "v_mfma_f32_16x16x32_bf16 v[104:107], a[76:79], v[142:145], v[104:107]\n"
      "buffer_store_dwordx4 v[166:169], v20, s[24:27], s46 offen sc1\n"
      "v_pk_mul_f32 v[116:117], v[116:117], v[184:185] op_sel_hi:[1,0]\n"
      "v_pk_mul_f32 v[186:187], v[186:187], v[178:179] op_sel_hi:[1,0]\n"
      "v_mfma_f32_16x16x32_bf16 v[104:107], a[80:83], v[198:201], v[104:107]\n"
      "v_mul_f32 v138, v178, v138\n"
      "v_fmac_f32 v217, v150, v150\n"
      "v_mfma_f32_16x16x32_bf16 v[104:107], a[84:87], v[220:223], v[104:107]\n"
      "ds_read_b128 v[166:169], v7 offset:26624\n"
      "v_cvt_pk_bf16_f32 v216, v100, v101\n"
      "v_mfma_f32_16x16x32_bf16 v[104:107], a[88:91], v[170:173], v[104:107]\n"
      "v_fmac_f32 v217, v151, v151\n"
      "v_mul_f32 v110, v184, v110\n"
      "v_mfma_f32_16x16x32_bf16 v[104:107], a[92:95], v[190:193], v[104:107]\n"
      "v_mul_f32_e64 v157, v184, v157\n"
      "v_cvt_pk_bf16_f32 v109, v110, v111\n"
      "v_mfma_f32_16x16x32_bf16 v[120:123], a[96:99], v[202:205], 0\n"
      "v_cvt_pk_bf16_f32 v134, v134, v135\n"
      "v_mul_f32 v156, v184, v156\n"
      "v_mfma_f32_16x16x32_bf16 v[120:123], a[100:103], v[142:145], v[120:123]\n"
      "s_waitcnt lgkmcnt(1)\n"
      "v_mul_f32_e64 v119, v184, v119\n"
      "v_cvt_pk_bf16_f32 v155, v156, v157\n"
      "v_mfma_f32_16x16x32_bf16 v[120:123], a[104:107], v[198:201], v[120:123]\n"
      "s_cmp_ge_u32 s95, 0x40000000\n"
      "s_cselect_b64 vcc, -1, s[56:57]\n"
      "v_fmac_f32_e64 v217, v152, v152\n"
      "v_mfma_f32_16x16x32_bf16 v[120:123], a[108:111], v[220:223], v[120:123]\n"
      "buffer_store_dwordx4 v[180:183], v21, s[24:27], s46 offen sc1\n"
      "v_pk_mul_f32 v[188:189], v[188:189], v[178:179] op_sel_hi:[1,0]\n"
      "v_mfma_f32_16x16x32_bf16 v[120:123], a[112:115], v[170:173], v[120:123]\n"
      "v_mul_f32 v141, v178, v141\n"
      "v_fmac_f32 v217, v153, v153\n"
      "v_mfma_f32_16x16x32_bf16 v[120:123], a[116:119], v[190:193], v[120:123]\n"
      "v_fma_f32 v114, v104, v104, s51\n"
      "v_fmac_f32_e64 v114, v105, v105\n"
      "v_mfma_f32_16x16x32_bf16 v[98:101], a[120:123], v[202:205], 0\n"
      "v_fmac_f32_e64 v217, v174, v174\n"
      "v_cvt_pk_bf16_f32 v156, v116, v117\n"
      "v_mfma_f32_16x16x32_bf16 v[98:101], a[124:127], v[142:145], v[98:101]\n"
      "v_cvt_pk_bf16_f32 v135, v136, v137\n"
      "v_fmac_f32 v114, v106, v106\n"
      "v_mfma_f32_16x16x32_bf16 v[98:101], a[128:131], v[198:201], v[98:101]\n"
      "v_fmac_f32 v114, v107, v107\n"
      "v_fmac_f32_e64 v217, v175, v175\n"
      "v_mfma_f32_16x16x32_bf16 v[98:101], a[132:135], v[220:223], v[98:101]\n"
      "v_cvt_pk_bf16_f32 v157, v118, v119\n"
      "v_fmac_f32 v114, v120, v120\n"
      "v_mfma_f32_16x16x32_bf16 v[98:101], a[136:139], v[170:173], v[98:101]\n"
      "s_waitcnt lgkmcnt(0)\n"
      "v_fmac_f32 v217, v176, v176\n"
      "v_fmac_f32 v114, v121, v121\n"
      "v_mfma_f32_16x16x32_bf16 v[98:101], a[140:143], v[190:193], v[98:101]\n"
      "buffer_store_dwordx4 v[166:169], v22, s[24:27], s46 offen sc1\n"
      "v_mfma_f32_16x16x32_bf16 v[182:185], v[202:205], a[144:147], 0\n"
      "v_mul_f32 v140, v178, v140\n"
      "v_fmac_f32 v217, v177, v177\n"
      "v_mfma_f32_16x16x32_bf16 v[182:185], v[142:145], a[148:151], v[182:185]\n"
      "v_fmac_f32 v114, v122, v122\n"
      "v_fmac_f32 v114, v123, v123\n"
      "v_mfma_f32_16x16x32_bf16 v[182:185], v[198:201], a[152:155], v[182:185]\n"
      "v_fmac_f32 v114, v98, v98\n"
      "v_fmac_f32 v114, v99, v99\n"
      "v_mfma_f32_16x16x32_bf16 v[182:185], v[220:223], a[156:159], v[182:185]\n"
      "v_fmac_f32 v114, v100, v100\n"
      "v_fmac_f32 v114, v101, v101\n"
      "v_mfma_f32_16x16x32_bf16 v[182:185], v[170:173], a[160:163], v[182:185]\n"
      "s_nop 0\n"
      "v_permlane16_swap_b32 v217, v114\n"
      "v_mfma_f32_16x16x32_bf16 v[182:185], v[190:193], a[164:167], v[182:185]\n"
      "v_add_f32 v217, v217, v114\n"
      "v_mov_b32 v114, v217\n"
      "v_mfma_f32_16x16x32_bf16 v[116:119], v[202:205], a[168:171], 0\n"
      "s_nop 0\n"
      "v_permlane32_swap_b32 v217, v114\n"
      "v_mfma_f32_16x16x32_bf16 v[116:119], v[142:145], a[172:175], v[116:119]\n"
      "buffer_store_dwordx4 v[162:165], v23, s[24:27], s46 offen sc1\n"
      "v_mfma_f32_16x16x32_bf16 v[116:119], v[198:201], a[176:179], v[116:119]\n"
      "ds_read_b128 v[166:169], v7 offset:34816\n"
      "v_add_f32 v217, v217, v114\n"
      "v_mfma_f32_16x16x32_bf16 v[116:119], v[220:223], a[180:183], v[116:119]\n"
      "v_rsq_f32 v217, v217\n"
      "v_mfma_f32_16x16x32_bf16 v[116:119], v[170:173], a[184:187], v[116:119]\n"
      "ds_read_b128 v[124:127], v2 offset:21504\n"
      "v_mov_b32 v114, v217\n"
      "v_mfma_f32_16x16x32_bf16 v[116:119], v[190:193], a[188:191], v[116:119]\n"
      "s_nop 0\n"
      "v_permlane16_swap_b32_e64 v217, v114\n"
      "v_mfma_f32_16x16x32_bf16 v[178:181], v[202:205], a[192:195], 0\n"
      "ds_read_b128 v[202:205], v2 offset:18432\n"
      "v_cvt_pk_bf16_f32 v186, v186, v187\n"
      "v_mfma_f32_16x16x32_bf16 v[178:181], v[142:145], a[196:199], v[178:181]\n"
      "ds_read_b128 v[162:165], v2 offset:24576\n"
      "v_mul_f32 v217, s50, v217\n"
      "v_mfma_f32_16x16x32_bf16 v[178:181], v[198:201], a[200:203], v[178:181]\n"
      "s_waitcnt lgkmcnt(3)\n"
      "v_mul_f32_e64 v153, v217, v153\n"
      "v_cvt_pk_bf16_f32 v197, v128, v129\n"
      "v_mfma_f32_16x16x32_bf16 v[178:181], v[220:223], a[204:207], v[178:181]\n"
      "ds_read_b128 v[220:223], v10 offset:32\n"
      "v_cvt_pk_bf16_f32 v136, v138, v139\n"
      "v_mfma_f32_16x16x32_bf16 v[178:181], v[170:173], a[208:211], v[178:181]\n"
      "buffer_store_dwordx4 v[166:169], v25, s[24:27], s46 offen sc1\n"
      "v_pk_mul_f32 v[98:99], v[98:99], v[114:115] op_sel_hi:[1,0]\n"
      "v_pk_mul_f32 v[122:123], v[122:123], v[114:115] op_sel_hi:[1,0]\n"
      "v_pk_mul_f32 v[210:211], v[210:211], v[216:217] op_sel:[0,1]\n"
      "v_pk_mul_f32 v[174:175], v[174:175], v[216:217] op_sel:[0,1]\n"
      "v_pk_mul_f32 v[106:107], v[106:107], v[114:115] op_sel_hi:[1,0]\n"
      "v_pk_mul_f32 v[120:121], v[120:121], v[114:115] op_sel_hi:[1,0]\n"
      "v_pk_mul_f32 v[104:105], v[104:105], v[114:115] op_sel_hi:[1,0]\n"
      "v_mfma_f32_16x16x32_bf16 v[178:181], v[190:193], a[212:215], v[178:181]\n"
      "s_waitcnt lgkmcnt(1)\n"
      "v_cvt_pk_bf16_f32 v80, v116, v117\n"
      "v_mul_f32 v212, v217, v212\n"
      "v_mfma_f32_16x16x32_bf16 v[166:169], a[216:219], v[202:205], 0\n"
      "v_cvt_pk_bf16_f32 v187, v188, v189\n"
      "v_mul_f32_e64 v152, v217, v152\n"
      "v_mfma_f32_16x16x32_bf16 v[166:169], a[220:223], v[124:127], v[166:169]\n"
      "ds_read_b64 v[218:219], v10 offset:24\n"
      "v_cvt_pk_bf16_f32 v76, v182, v183\n"
      "v_mfma_f32_16x16x32_bf16 v[166:169], a[224:227], v[162:165], v[166:169]\n"
      "ds_read_b128 v[190:193], v2 offset:30720\n"
      "v_cvt_pk_bf16_f32 v210, v210, v211\n"
      "v_mfma_f32_16x16x32_bf16 v[198:201], a[240:243], v[202:205], 0\n"
      "v_cvt_pk_bf16_f32 v137, v140, v141\n"
      "v_cvt_pk_bf16_f32 v84, v178, v179\n"
      "v_mfma_f32_16x16x32_bf16 v[198:201], a[244:247], v[124:127], v[198:201]\n"
      "ds_read_b128 v[170:173], v2 offset:27648\n"
      "v_cvt_pk_bf16_f32 v81, v118, v119\n"
      "v_mfma_f32_16x16x32_bf16 v[198:201], a[248:251], v[162:165], v[198:201]\n"
      "v_cvt_pk_bf16_f32 v104, v104, v105\n"
      "v_mul_f32 v213, v217, v213\n"
      "v_mfma_f32_16x16x32_bf16 v[142:145], v[232:235], v[202:205], 0\n"
      "v_mul_f32 v100, v114, v100\n"
      "v_mul_f32_e64 v176, v217, v176\n"
      "v_mfma_f32_16x16x32_bf16 v[142:145], v[236:239], v[124:127], v[142:145]\n"
      "v_cvt_pk_bf16_f32 v105, v106, v107\n"
      "v_mul_f32 v101, v114, v101\n"
      "v_mfma_f32_16x16x32_bf16 v[142:145], v[240:243], v[162:165], v[142:145]\n"
      "s_waitcnt lgkmcnt(1)\n"
      "v_cvt_pk_bf16_f32 v107, v122, v123\n"
      "v_cvt_pk_bf16_f32 v98, v98, v99\n"
      "v_mfma_f32_16x16x32_bf16 v[138:141], v[146:149], v[206:209], v[40:43]\n"
      "ds_read_u16 v86, v11 offset:64\n"
      "v_cvt_pk_bf16_f32 v106, v120, v121\n"
      "v_mfma_f32_16x16x32_bf16 v[126:129], v[154:157], v[206:209], v[44:47]\n"
      "ds_read_b128 v[118:121], v2 offset:21760\n"
      "v_mul_f32_e64 v151, v217, v151\n"
      "v_mfma_f32_16x16x32_bf16 v[202:205], v[104:107], v[206:209], v[48:51]\n"
      "ds_read_b128 v[122:125], v2 offset:33792\n"
      "v_cvt_pk_bf16_f32 v99, v100, v101\n"
      "v_mfma_f32_16x16x32_bf16 v[162:165], v[220:223], v[206:209], v[24:27]\n"
      "s_waitcnt lgkmcnt(3)\n"
      "v_mul_f32 v177, v217, v177\n"
      "v_cvt_pk_bf16_f32 v85, v180, v181\n"
      "v_mfma_f32_16x16x16_bf16 v[138:141], v[112:113], v[96:97], v[138:141]\n"
      "v_cvt_pk_bf16_f32 v174, v174, v175\n"
      "s_waitcnt vmcnt(6)\n"
      "v_or3_b32 v158, v158, v159, v160\n"
      "v_cvt_pk_bf16_f32 v211, v212, v213\n"
      "v_mfma_f32_16x16x16_bf16 v[126:129], v[108:109], v[96:97], v[126:129]\n"
      "v_mul_f32 v150, v217, v150\n"
      "v_cvt_pk_bf16_f32 v77, v184, v185\n"
      "v_cvt_pk_bf16_f32 v175, v176, v177\n"
      "v_mfma_f32_16x16x16_bf16 v[202:205], v[98:99], v[96:97], v[202:205]\n"
      "v_cvt_pk_bf16_f32 v212, v150, v151\n"
      "v_cvt_pk_bf16_f32 v213, v152, v153\n"
      "v_max3_f32 v181, v138, v139, v140\n"
      "v_mfma_f32_16x16x16_bf16 v[162:165], v[218:219], v[96:97], v[162:165]\n"
      "v_max3_f32 v101, v141, v126, v127\n"
      "v_cvt_pk_bf16_f32 v217, v102, v103\n"
      "v_max3_f32 v181, v181, v128, v129\n"
      "v_mfma_f32_16x16x32_bf16 v[166:169], a[228:231], v[170:173], v[166:169]\n"
      "s_waitcnt lgkmcnt(0)\n"
      "v_max3_f32 v181, v181, v204, v205\n"
      "v_max3_f32 v101, v101, v202, v203\n"
      "v_mfma_f32_16x16x32_bf16 v[166:169], a[232:235], v[190:193], v[166:169]\n"
      "v_max3_f32 v181, v181, v101, v162\n"
      "v_mov_b32 v177, v181\n"
      "v_mfma_f32_16x16x32_bf16 v[166:169], a[236:239], v[122:125], v[166:169]\n"
      "s_nop 0\n"
      "v_permlane16_swap_b32 v181, v177\n"
      "v_mfma_f32_16x16x32_bf16 v[198:201], a[252:255], v[170:173], v[198:201]\n"
      "ds_read_b128 v[100:103], v2 offset:18688\n"
      "v_max_f32 v181, v181, v177\n"
      "v_mfma_f32_16x16x32_bf16 v[198:201], v[224:227], v[190:193], v[198:201]\n"
      "s_waitcnt vmcnt(5)\n"
      "v_or3_b32 v158, v158, v161, v90\n"
      "v_mov_b32_e64 v177, v181\n"
      "v_mfma_f32_16x16x32_bf16 v[198:201], v[228:231], v[122:125], v[198:201]\n"
      "s_nop 0\n"
      "v_permlane32_swap_b32 v181, v177\n"
      "v_mfma_f32_16x16x32_bf16 v[142:145], v[244:247], v[170:173], v[142:145]\n"
      "v_max_f32 v181, v181, v177\n"
      "v_sub_f32 v127, v127, v181\n"
      "v_mfma_f32_16x16x32_bf16 v[142:145], v[248:251], v[190:193], v[142:145]\n"
      "ds_read_b128 v[188:191], v2 offset:24832\n"
      "v_sub_f32 v205, v205, v181\n"
      "v_mfma_f32_16x16x32_bf16 v[142:145], v[252:255], v[122:125], v[142:145]\n"
      "v_sub_f32 v204, v204, v181\n"
      "v_sub_f32 v139, v139, v181\n"
      "v_mfma_f32_16x16x32_bf16 v[114:117], v[146:149], v[134:137], v[52:55]\n"
      "v_sub_f32 v128, v128, v181\n"
      "v_sub_f32 v141, v141, v181\n"
      "v_sub_f32 v162, v162, v181\n"
      "v_sub_f32_e64 v138, v138, v181\n"
      "v_mfma_f32_16x16x32_bf16 v[176:179], v[154:157], v[134:137], v[56:59]\n"
      "v_exp_f32 v162, v162\n"
      "v_sub_f32 v126, v126, v181\n"
      "v_mfma_f32_16x16x32_bf16 v[206:209], v[104:107], v[134:137], v[60:63]\n"
      "v_exp_f32_e64 v127, v127\n"
      "v_mfma_f32_16x16x32_bf16 v[122:125], v[220:223], v[134:137], v[26:29]\n"
      "s_waitcnt lgkmcnt(1)\n"
      "v_cvt_pk_bf16_f32 v90, v162, 0\n"
      "v_sub_f32 v203, v203, v181\n"
      "v_mfma_f32_16x16x16_bf16 v[114:117], v[112:113], v[186:187], v[114:117]\n"
      "v_exp_f32 v205, v205\n"
      "v_exp_f32 v139, v139\n"
      "v_mfma_f32_16x16x16_bf16 v[176:179], v[108:109], v[186:187], v[176:179]\n"
      "v_exp_f32 v128, v128\n"
      "v_sub_f32 v202, v202, v181\n"
      "v_sub_f32_e64 v140, v140, v181\n"
      "v_mfma_f32_16x16x16_bf16 v[206:209], v[98:99], v[186:187], v[206:209]\n"
      "v_exp_f32 v138, v138\n"
      "v_exp_f32 v126, v126\n"
      "v_sub_f32 v129, v129, v181\n"
      "v_mfma_f32_16x16x16_bf16 v[122:125], v[218:219], v[186:187], v[122:125]\n"
      "v_exp_f32 v141, v141\n"
      "v_cvt_pk_bf16_f32 v138, v138, v139\n"
      "v_mfma_f32_16x16x32_bf16 v[162:165], a[216:219], v[100:103], 0\n"
      "v_exp_f32_e64 v204, v204\n"
      "v_max3_f32 v186, v114, v115, v116\n"
      "v_mfma_f32_16x16x32_bf16 v[162:165], a[220:223], v[118:121], v[162:165]\n"
      "s_waitcnt lgkmcnt(0)\n"
      "v_exp_f32 v140, v140\n"
      "v_mfma_f32_16x16x32_bf16 v[162:165], a[224:227], v[188:191], v[162:165]\n"
      "v_cvt_pk_bf16_f32 v89, v204, v205\n"
      "v_cvt_pk_bf16_f32 v139, v140, v141\n"
      "v_mfma_f32_16x16x32_bf16 v[180:183], a[240:243], v[100:103], 0\n"
      "v_exp_f32_e64 v202, v202\n"
      "v_mfma_f32_16x16x32_bf16 v[180:183], a[244:247], v[118:121], v[180:183]\n"
      "v_cvt_pk_bf16_f32 v140, v126, v127\n"
      "v_max3_f32 v134, v117, v176, v177\n"
      "v_mfma_f32_16x16x32_bf16 v[180:183], a[248:251], v[188:191], v[180:183]\n"
      "ds_write_b32 v31, v158 offset:4608\n"
      "v_mfma_f32_16x16x32_bf16 v[170:173], v[232:235], v[100:103], 0\n"
      "v_exp_f32_e64 v129, v129\n"
      "v_mfma_f32_16x16x32_bf16 v[170:173], v[236:239], v[118:121], v[170:173]\n"
      "v_max3_f32 v186, v186, v178, v179\n"
      "v_cvt_pk_bf16_f32 v141, v128, v129\n"
      "v_mfma_f32_16x16x32_bf16 v[170:173], v[240:243], v[188:191], v[170:173]\n"
      "v_exp_f32_e64 v203, v203\n"
      "v_mfma_f32_16x16x32_bf16 v[126:129], v[32:35], v[138:141], 0\n"
      "v_cvt_pk_bf16_f32 v88, v202, v203\n"
      "v_max3_f32 v186, v186, v208, v209\n"
      "v_max3_f32 v134, v134, v206, v207\n"
      "v_max3_f32 v186, v186, v134, v122\n"
      "v_mfma_f32_16x16x32_bf16 v[126:129], v[32:35], v[88:91], v[126:129]\n"
      "ds_read_b128 v[188:191], v2 offset:27904\n"
      "v_mov_b32 v120, v186\n"
      "v_mfma_f32_16x16x32_bf16 v[100:103], v[130:133], v[138:141], 0\n"
      "s_nop 0\n"
      "v_permlane16_swap_b32 v186, v120\n"
      "v_mfma_f32_16x16x32_bf16 v[100:103], v[76:79], v[88:91], v[100:103]\n"
      "v_max_f32 v186, v186, v120\n"
      "v_mov_b32 v120, v186\n"
      "v_mfma_f32_16x16x32_bf16 v[134:137], v[194:197], v[138:141], 0\n"
      "v_rcp_f32 v126, v126\n"
      "v_mfma_f32_16x16x32_bf16 v[134:137], v[80:83], v[88:91], v[134:137]\n"
      "v_permlane32_swap_b32 v186, v120\n"
      "v_max_f32 v186, v186, v120\n"
      "v_mfma_f32_16x16x32_bf16 v[118:121], v[214:217], v[138:141], 0\n"
      "v_sub_f32 v115, v115, v186\n"
      "v_sub_f32 v116, v116, v186\n"
      "v_mfma_f32_16x16x32_bf16 v[118:121], v[84:87], v[88:91], v[118:121]\n"
      "v_sub_f32 v176, v176, v186\n"
      "v_sub_f32 v208, v208, v186\n"
      "v_sub_f32 v207, v207, v186\n"
      "v_exp_f32 v208, v208\n"
      "v_pk_mul_f32 v[100:101], v[100:101], v[126:127] op_sel_hi:[1,0]\n"
      "v_pk_mul_f32 v[102:103], v[102:103], v[126:127] op_sel_hi:[1,0]\n"
      "v_sub_f32 v206, v206, v186\n"
      "v_pk_mul_f32 v[134:135], v[134:135], v[126:127] op_sel_hi:[1,0]\n"
      "v_exp_f32 v115, v115\n"
      "v_sub_f32_e64 v177, v177, v186\n"
      "v_pk_mul_f32 v[120:121], v[120:121], v[126:127] op_sel_hi:[1,0]\n"
      "v_sub_f32 v117, v117, v186\n"
      "v_sub_f32 v122, v122, v186\n"
      "v_pk_mul_f32 v[118:119], v[118:119], v[126:127] op_sel_hi:[1,0]\n"
      "v_cvt_pk_bf16_f32 v118, v118, v119\n"
      "v_pk_mul_f32 v[136:137], v[136:137], v[126:127] op_sel_hi:[1,0]\n"
      "v_cvt_pk_bf16_f32 v100, v100, v101\n"
      "v_cvt_pk_bf16_f32 v101, v102, v103\n"
      "v_mfma_f32_16x16x32_bf16 v[150:153], v[146:149], v[210:213], v[64:67]\n"
      "s_waitcnt lgkmcnt(0)\n"
      "v_cvt_pk_bf16_f32 v102, v134, v135\n"
      "v_exp_f32_e64 v206, v206\n"
      "v_mfma_f32_16x16x32_bf16 v[202:205], v[154:157], v[210:213], v[68:71]\n"
      "s_and_b32 s86, s86, -4\n"
      "v_cvt_pk_bf16_f32 v103, v136, v137\n"
      "v_cvt_pk_bf16_f32 v119, v120, v121\n"
      "v_mfma_f32_16x16x32_bf16 v[134:137], v[104:107], v[210:213], v[72:75]\n"
      "ds_read_b128 v[126:129], v2 offset:30976\n"
      "v_sub_f32 v179, v179, v186\n"
      "v_mfma_f32_16x16x32_bf16 v[138:141], v[220:223], v[210:213], v[28:31]\n"
      "ds_read_b128 v[210:213], v2 offset:34048\n"
      "v_exp_f32 v122, v122\n"
      "v_mfma_f32_16x16x16_bf16 v[150:153], v[112:113], v[174:175], v[150:153]\n"
      "v_sub_f32 v209, v209, v186\n"
      "v_exp_f32 v207, v207\n"
      "v_mfma_f32_16x16x16_bf16 v[202:205], v[108:109], v[174:175], v[202:205]\n"
      "v_exp_f32 v209, v209\n"
      "v_sub_f32 v178, v178, v186\n"
      "v_mfma_f32_16x16x16_bf16 v[134:137], v[98:99], v[174:175], v[134:137]\n"
      "v_sub_f32 v114, v114, v186\n"
      "v_exp_f32 v177, v177\n"
      "v_mfma_f32_16x16x16_bf16 v[138:141], v[218:219], v[174:175], v[138:141]\n"
      "v_exp_f32_e64 v114, v114\n"
      "v_cvt_pk_bf16_f32 v92, v206, v207\n"
      "v_mfma_f32_16x16x32_bf16 v[162:165], a[228:231], v[188:191], v[162:165]\n"
      "s_waitcnt lgkmcnt(1)\n"
      "v_exp_f32_e64 v176, v176\n"
      "v_mfma_f32_16x16x32_bf16 v[162:165], a[232:235], v[126:129], v[162:165]\n"
      "s_waitcnt lgkmcnt(0)\n"
      "v_cvt_pk_bf16_f32 v114, v114, v115\n"
      "v_exp_f32 v116, v116\n"
      "v_mfma_f32_16x16x32_bf16 v[162:165], a[236:239], v[210:213], v[162:165]\n"
      "v_exp_f32 v117, v117\n"
      "v_mfma_f32_16x16x32_bf16 v[180:183], a[252:255], v[188:191], v[180:183]\n"
      "v_exp_f32 v179, v179\n"
      "v_mfma_f32_16x16x32_bf16 v[180:183], v[224:227], v[126:129], v[180:183]\n"
      "v_exp_f32 v178, v178\n"
      "v_mfma_f32_16x16x32_bf16 v[180:183], v[228:231], v[210:213], v[180:183]\n"
      "v_max3_f32 v185, v150, v151, v152\n"
      "v_cvt_pk_bf16_f32 v115, v116, v117\n"
      "v_cvt_pk_bf16_f32 v117, v178, v179\n"
      "v_mfma_f32_16x16x32_bf16 v[170:173], v[244:247], v[188:191], v[170:173]\n"
      "ds_read_b128 v[188:191], v10\n"
      "v_cvt_pk_bf16_f32 v116, v176, v177\n"
      "v_mfma_f32_16x16x32_bf16 v[170:173], v[248:251], v[126:129], v[170:173]\n"
      "v_max3_f32 v179, v153, v202, v203\n"
      "v_cvt_pk_bf16_f32 v94, v122, 0\n"
      "v_mfma_f32_16x16x32_bf16 v[170:173], v[252:255], v[210:213], v[170:173]\n"
      "v_max3_f32 v179, v179, v134, v135\n"
      "v_cvt_pk_bf16_f32 v93, v208, v209\n"
      "v_mfma_f32_16x16x32_bf16 v[174:177], v[32:35], v[114:117], 0\n"
      "v_max3_f32 v185, v185, v204, v205\n"
      "v_max3_f32 v185, v185, v136, v137\n"
      "v_mfma_f32_16x16x32_bf16 v[174:177], v[32:35], v[92:95], v[174:177]\n"
      "v_max3_f32 v185, v185, v179, v138\n"
      "v_mov_b32 v209, v185\n"
      "v_mfma_f32_16x16x32_bf16 v[210:213], v[130:133], v[114:117], 0\n"
      "s_nop 0\n"
      "v_permlane16_swap_b32_e64 v185, v209\n"
      "v_mfma_f32_16x16x32_bf16 v[210:213], v[76:79], v[92:95], v[210:213]\n"
      "ds_read_b128 v[122:125], v2 offset:18944\n"
      "v_max_f32 v185, v185, v209\n"
      "v_mfma_f32_16x16x32_bf16 v[158:161], v[194:197], v[114:117], 0\n"
      "ds_read_b64 v[186:187], v10 offset:16\n"
      "v_mov_b32 v209, v185\n"
      "v_mfma_f32_16x16x32_bf16 v[158:161], v[80:83], v[92:95], v[158:161]\n"
      "ds_write_b64 v4, v[118:119]\n"
      "v_mfma_f32_16x16x32_bf16 v[176:179], v[214:217], v[114:117], 0\n"
      "v_permlane32_swap_b32_e64 v185, v209\n"
      "v_mfma_f32_16x16x32_bf16 v[176:179], v[84:87], v[92:95], v[176:179]\n"
      "s_waitcnt lgkmcnt(3)\n"
      "v_max_f32 v185, v185, v209\n"
      "v_sub_f32 v152, v152, v185\n"
      "v_mfma_f32_16x16x32_bf16 v[114:117], v[188:191], v[146:149], v[30:33]\n"
      "v_rcp_f32 v174, v174\n"
      "v_mfma_f32_16x16x32_bf16 v[126:129], v[188:191], v[154:157], v[30:33]\n"
      "ds_read_b128 v[206:209], v2 offset:25088\n"
      "v_sub_f32 v135, v135, v185\n"
      "v_mfma_f32_16x16x32_bf16 v[154:157], v[188:191], v[104:107], v[30:33]\n"
      "s_waitcnt lgkmcnt(2)\n"
      "v_sub_f32 v153, v153, v185\n"
      "v_sub_f32 v138, v138, v185\n"
      "v_mfma_f32_16x16x32_bf16 v[104:107], v[188:191], v[220:223], v[30:33]\n"
      "ds_read_b128 v[188:191], v2 offset:22016\n"
      "v_sub_f32_e64 v151, v151, v185\n"
      "v_mfma_f32_16x16x16_bf16 v[114:117], v[186:187], v[112:113], v[114:117]\n"
      "v_sub_f32 v203, v203, v185\n"
      "v_exp_f32 v152, v152\n"
      "v_mfma_f32_16x16x16_bf16 v[126:129], v[186:187], v[108:109], v[126:129]\n"
      "v_exp_f32 v153, v153\n"
      "v_sub_f32 v137, v137, v185\n"
      "v_fma_f32 v222, v166, v166, s52\n"
      "v_mfma_f32_16x16x16_bf16 v[154:157], v[186:187], v[98:99], v[154:157]\n"
      "v_sub_f32 v150, v150, v185\n"
      "v_mul_f32 v223, v167, v167\n"
      "v_exp_f32 v151, v151\n"
      "v_mul_f32 v113, v163, v163\n"
      "v_fma_f32 v112, v162, v162, s52\n"
      "v_sub_f32_e64 v134, v134, v185\n"
      "v_pk_fma_f32 v[222:223], v[168:169], v[168:169], v[222:223]\n"
      "v_pk_mul_f32 v[212:213], v[212:213], v[174:175] op_sel_hi:[1,0]\n"
      "v_pk_fma_f32 v[112:113], v[164:165], v[164:165], v[112:113]\n"
      "v_pk_mul_f32 v[178:179], v[178:179], v[174:175] op_sel_hi:[1,0]\n"
      "v_pk_mul_f32 v[158:159], v[158:159], v[174:175] op_sel_hi:[1,0]\n"
      "v_cndmask_b32 v114, v114, v126, s[58:59]\n"
      "v_mfma_f32_16x16x16_bf16 v[104:107], v[186:187], v[218:219], v[104:107]\n"
      "v_exp_f32 v135, v135\n"
      "v_sub_f32 v136, v136, v185\n"
      "v_mfma_f32_16x16x32_bf16 v[126:129], a[216:219], v[122:125], 0\n"
      "s_waitcnt lgkmcnt(0)\n"
      "v_exp_f32 v134, v134\n"
      "v_sub_f32 v202, v202, v185\n"
      "v_mfma_f32_16x16x32_bf16 v[126:129], a[220:223], v[188:191], v[126:129]\n"
      "ds_read_b128 v[218:221], v2 offset:31232\n"
      "v_sub_f32 v205, v205, v185\n"
      "v_mfma_f32_16x16x32_bf16 v[126:129], a[224:227], v[206:209], v[126:129]\n"
      "v_sub_f32 v204, v204, v185\n"
      "v_exp_f32 v150, v150\n"
      "v_mfma_f32_16x16x32_bf16 v[116:119], a[240:243], v[122:125], 0\n"
      "ds_read_b128 v[184:187], v2 offset:34304\n"
      "v_cndmask_b32 v114, v114, v154, s[60:61]\n"
      "v_cndmask_b32 v114, v114, v104, s[62:63]\n"
      "v_mfma_f32_16x16x32_bf16 v[116:119], a[244:247], v[188:191], v[116:119]\n"
      "ds_read_b128 v[96:99], v2 offset:28160\n"
      "v_cvt_pk_bf16_f32 v150, v150, v151\n"
      "v_mfma_f32_16x16x32_bf16 v[116:119], a[248:251], v[206:209], v[116:119]\n"
      "ds_write_b128 v3, v[100:103]\n"
      "v_pk_mul_f32 v[210:211], v[210:211], v[174:175] op_sel_hi:[1,0]\n"
      "v_pk_mul_f32 v[176:177], v[176:177], v[174:175] op_sel_hi:[1,0]\n"
      "v_pk_mul_f32 v[160:161], v[160:161], v[174:175] op_sel_hi:[1,0]\n"
      "v_pk_fma_f32 v[112:113], v[180:181], v[180:181], v[112:113]\n"
      "v_pk_fma_f32 v[222:223], v[198:199], v[198:199], v[222:223]\n"
      "v_mfma_f32_16x16x32_bf16 v[108:111], v[232:235], v[122:125], 0\n"
      "v_cvt_pk_bf16_f32 v151, v152, v153\n"
      "v_max_f32_dpp v174, v114, v114 row_ror:8 row_mask:0xf bank_mask:0xf\n"
      "v_mfma_f32_16x16x32_bf16 v[108:111], v[236:239], v[188:191], v[108:111]\n"
      "s_nop 0\n"
      "v_max_f32_dpp v174, v174, v174 row_ror:4 row_mask:0xf bank_mask:0xf\n"
      "v_exp_f32 v203, v203\n"
      "v_mfma_f32_16x16x32_bf16 v[108:111], v[240:243], v[206:209], v[108:111]\n"
      "s_waitcnt lgkmcnt(1)\n"
      "v_exp_f32 v138, v138\n"
      "v_mfma_f32_16x16x32_bf16 v[126:129], a[228:231], v[96:99], v[126:129]\n"
      "v_exp_f32 v202, v202\n"
      "v_max_f32_dpp v174, v174, v174 row_ror:2 row_mask:0xf bank_mask:0xf\n"
      "v_cvt_pk_bf16_f32 v210, v210, v211\n"
      "v_mfma_f32_16x16x32_bf16 v[126:129], a[232:235], v[218:221], v[126:129]\n"
      "v_cvt_pk_bf16_f32 v211, v212, v213\n"
      "v_max_f32_dpp v174, v174, v174 row_ror:1 row_mask:0xf bank_mask:0xf\n"
      "v_mfma_f32_16x16x32_bf16 v[126:129], a[236:239], v[184:187], v[126:129]\n"
      "s_nop 0\n"
      "v_max_f32_dpp v174, v174, v174 row_bcast:15 row_mask:0xa bank_mask:0xf\n"
      "v_cvt_pk_bf16_f32 v176, v176, v177\n"
      "v_mfma_f32_16x16x32_bf16 v[116:119], a[252:255], v[96:99], v[116:119]\n"
      "v_max_f32_dpp v174, v174, v174 row_bcast:31 row_mask:0xc bank_mask:0xf\n"
      "v_cvt_pk_bf16_f32 v213, v160, v161\n"
      "v_mfma_f32_16x16x32_bf16 v[116:119], v[224:227], v[218:221], v[116:119]\n"
      "v_exp_f32 v136, v136\n"
      "v_mfma_f32_16x16x32_bf16 v[116:119], v[228:231], v[184:187], v[116:119]\n"
      "v_exp_f32 v137, v137\n"
      "v_cvt_pk_bf16_f32 v212, v158, v159\n"
      "v_readlane_b32 s72, v174, 63\n"
      "v_mfma_f32_16x16x32_bf16 v[108:111], v[244:247], v[96:99], v[108:111]\n"
      "v_mul_f32 v175, v127, v127\n"
      "v_cvt_pk_bf16_f32 v89, v136, v137\n"
      "v_subrev_f32 v114, s72, v114\n"
      "v_fma_f32 v174, v126, v126, s52\n"
      "v_mfma_f32_16x16x32_bf16 v[108:111], v[248:251], v[218:221], v[108:111]\n"
      "v_cvt_pk_bf16_f32 v177, v178, v179\n"
      "v_exp_f32_e64 v114, v114\n"
      "v_mfma_f32_16x16x32_bf16 v[108:111], v[252:255], v[184:187], v[108:111]\n"
      "ds_write_b128 v3, v[210:213] offset:256\n"
      "v_pk_fma_f32 v[222:223], v[200:201], v[200:201], v[222:223]\n"
      "v_pk_fma_f32 v[174:175], v[128:129], v[128:129], v[174:175]\n"
      "v_exp_f32 v205, v205\n"
      "v_pk_fma_f32 v[112:113], v[182:183], v[182:183], v[112:113]\n"
      "v_exp_f32 v204, v204\n"
      "v_cvt_pk_bf16_f32 v88, v134, v135\n"
      "v_pk_fma_f32 v[174:175], v[116:117], v[116:117], v[174:175]\n"
      "v_cvt_pk_bf16_f32 v153, v204, v205\n"
      "v_pk_fma_f32 v[222:223], v[142:143], v[142:143], v[222:223]\n"
      "v_pk_fma_f32 v[174:175], v[118:119], v[118:119], v[174:175]\n"
      "v_cvt_pk_bf16_f32 v90, v138, 0\n"
      "v_pk_fma_f32 v[112:113], v[170:171], v[170:171], v[112:113]\n"
      "v_cvt_pk_bf16_f32 v152, v202, v203\n"
      "v_pk_fma_f32 v[174:175], v[108:109], v[108:109], v[174:175]\n"
      "v_pk_fma_f32 v[222:223], v[144:145], v[144:145], v[222:223]\n"
      "v_pk_fma_f32 v[112:113], v[172:173], v[172:173], v[112:113]\n"
      "v_pk_fma_f32 v[174:175], v[110:111], v[110:111], v[174:175]\n"
      "v_add_f32 v222, v222, v223\n"
      "v_mfma_f32_16x16x32_bf16 v[210:213], v[32:35], v[150:153], 0\n"
      "v_add_f32 v112, v112, v113\n"
      "v_cvt_pk_bf16_f32 v114, v114, v114\n"
      "v_mfma_f32_16x16x32_bf16 v[210:213], v[32:35], v[88:91], v[210:213]\n"
      "v_permlane16_swap_b32 v222, v112\n"
      "v_add_f32 v222, v222, v112\n"
      "v_add_f32 v174, v174, v175\n"
      "v_mov_b32 v112, v174\n"
      "v_mfma_f32_16x16x32_bf16 v[188:191], v[130:133], v[150:153], 0\n"
      "ds_write_b16 v13, v114\n"
      "v_mfma_f32_16x16x32_bf16 v[188:191], v[76:79], v[88:91], v[188:191]\n"
      "v_permlane16_swap_b32 v174, v112\n"
      "v_add_f32 v174, v174, v112\n"
      "v_mfma_f32_16x16x32_bf16 v[218:221], v[194:197], v[150:153], 0\n"
      "s_nop 0\n"
      "v_permlane32_swap_b32 v222, v174\n"
      "v_add_f32_e64 v222, v222, v174\n"
      "v_mfma_f32_16x16x32_bf16 v[218:221], v[80:83], v[88:91], v[218:221]\n"
      "ds_write_b32 v31, v222\n"
      "v_mfma_f32_16x16x32_bf16 v[134:137], v[214:217], v[150:153], 0\n"
      "v_rcp_f32_e64 v210, v210\n"
      "v_mfma_f32_16x16x32_bf16 v[134:137], v[84:87], v[88:91], v[134:137]\n"
      "ds_write_b64 v4, v[176:177] offset:256\n"
      "v_mul_f32 v188, v210, v188\n"
      "v_mul_f32_e64 v189, v210, v189\n"
      "v_pk_mul_f32 v[190:191], v[190:191], v[210:211] op_sel_hi:[1,0]\n"
      "v_cvt_pk_bf16_f32 v188, v188, v189\n"
      "v_cvt_pk_bf16_f32 v189, v190, v191\n"
      "v_pk_mul_f32 v[220:221], v[220:221], v[210:211] op_sel_hi:[1,0]\n"
      "v_cvt_pk_bf16_f32 v191, v220, v221\n"
      "v_pk_mul_f32 v[218:219], v[218:219], v[210:211] op_sel_hi:[1,0]\n"
      "v_cvt_pk_bf16_f32 v190, v218, v219\n"
      "s_add_u32 s42, s42, 1\n"
      "ds_write_b128 v3, v[188:191] offset:512\n"
      "ds_read_b128 v[174:177], v14\n"
      "v_pk_mul_f32 v[136:137], v[136:137], v[210:211] op_sel_hi:[1,0]\n"
      "v_pk_mul_f32 v[134:135], v[134:135], v[210:211] op_sel_hi:[1,0]\n"
      "v_cvt_pk_bf16_f32 v134, v134, v135\n"
      "v_cvt_pk_bf16_f32 v135, v136, v137\n"
      "ds_read_b128 v[210:213], v14 offset:64\n"
      "ds_write_b64 v4, v[134:135] offset:512\n"
      "s_waitcnt lgkmcnt(2)\n"
      "v_mfma_f32_16x16x32_bf16 v[188:191], v[174:177], v[32:35], 0\n"
      "s_waitcnt lgkmcnt(1)\n"
      "v_mfma_f32_16x16x32_bf16 v[188:191], v[210:213], v[32:35], v[188:191]\n"
      "s_waitcnt lgkmcnt(0)\n"
      "s_barrier\n"
      "s_cbranch_vccz L_max1_%=\n"
      "L_cold_%=:\n"
      "L_disp1_%=:\n"
      "s_cmp_lt_u32 s95, 0x40000000\n"
      "s_cbranch_scc1 L_nofix_11_%=\n"
      "s_cmp_eq_u32 s95, -2\n"
      "s_cbranch_scc1 L_tail1_%=\n"
      "s_nop 2\n"
      "v_lshlrev_b32 v219, 3, v36\n"
      "s_add_u32 s72, s98, L_cold_%=-L_ipf0_%=\n"
      "s_addc_u32 s73, s99, 0\n"
      "s_mov_b32 s74, L_cend_%=-L_cold_%=\n"
      "s_mov_b32 s75, 0x20000\n"
      "s_mov_b32 m0, 161792\n"
      "s_nop 0\n"
      "buffer_load_dwordx4 v219, s[72:75], 0 offen  lds\n"
      "s_lshr_b32 s92, s84, 30\n"
      "s_or_b32 s92, s92, s66\n"
      "s_cbranch_scc0 L_nf_12_%=\n"
      "s_bitcmp1_b32 s84, 31\n"
      "s_cselect_b32 s75, 100270464, 0\n"
      "s_bitcmp1_b32 s84, 30\n"
      "s_cselect_b32 s76, 196224, 0\n"
      "s_and_b32 s74, s84, 0x3fffffff\n"
      "s_cmp_ge_u32 s84, 0x40000000\n"
      "s_cselect_b32 s74, s74, 0\n"
      "s_cselect_b32 s84, 0, s84\n"
      "s_sub_u32 s74, s74, s66\n"
      "s_add_u32 s66, s66, s74\n"
      "s_sub_u32 s75, s75, s67\n"
      "s_add_u32 s67, s67, s75\n"
      "s_sub_u32 s76, s76, s68\n"
      "s_add_u32 s68, s68, s76\n"
      "v_add_u32 v16, s74, v16\n"
      "v_add_u32 v17, s74, v17\n"
      "v_add_u32 v18, s74, v18\n"
      "v_add_u32 v19, s74, v19\n"
      "v_add_u32 v15, s74, v15\n"
      "s_cmp_eq_u32 s75, 0\n"
      "s_cbranch_scc1 L_ax_13_%=\n"
      "v_bfe_i32 v219, v27, 0, 1\n"
      "v_and_b32 v219, s75, v219\n"
      "v_sub_u32 v16, v16, v219\n"
      "v_bfe_i32 v219, v27, 2, 1\n"
      "v_and_b32 v219, s75, v219\n"
      "v_sub_u32 v17, v17, v219\n"
      "v_bfe_i32 v219, v27, 4, 1\n"
      "v_and_b32 v219, s75, v219\n"
      "v_sub_u32 v18, v18, v219\n"
      "v_bfe_i32 v219, v27, 6, 1\n"
      "v_and_b32 v219, s75, v219\n"
      "v_sub_u32 v19, v19, v219\n"
      "v_bfe_i32 v219, v27, 8, 1\n"
      "v_and_b32 v219, s75, v219\n"
      "v_sub_u32 v15, v15, v219\n"
      "L_ax_13_%=:\n"
      "s_cmp_eq_u32 s76, 0\n"
      "s_cbranch_scc1 L_ax_14_%=\n"
      "v_bfe_i32 v219, v27, 1, 1\n"
      "v_and_b32 v219, s76, v219\n"
      "v_sub_u32 v16, v16, v219\n"
      "v_bfe_i32 v219, v27, 3, 1\n"
      "v_and_b32 v219, s76, v219\n"
      "v_sub_u32 v17, v17, v219\n"
      "v_bfe_i32 v219, v27, 5, 1\n"
      "v_and_b32 v219, s76, v219\n"
      "v_sub_u32 v18, v18, v219\n"
      "v_bfe_i32 v219, v27, 7, 1\n"
      "v_and_b32 v219, s76, v219\n"
      "v_sub_u32 v19, v19, v219\n"
      "v_bfe_i32 v219, v27, 9, 1\n"
      "v_and_b32 v219, s76, v219\n"
      "v_sub_u32 v15, v15, v219\n"
      "L_ax_14_%=:\n"
      "s_or_b32 s48, s66, s69\n"
      "s_cmp_lg_u32 s48, 0\n"
      "s_cselect_b32 s48, 0x40000000, 0\n"
      "L_nf_12_%=:\n"
      "s_lshr_b32 s92, s85, 30\n"
      "s_or_b32 s92, s92, s69\n"
      "s_cbranch_scc0 L_nf_15_%=\n"
      "s_bitcmp1_b32 s85, 31\n"
      "s_cselect_b32 s75, 100270464, 0\n"
      "s_bitcmp1_b32 s85, 30\n"
      "s_cselect_b32 s76, 196224, 0\n"
      "s_and_b32 s74, s85, 0x3fffffff\n"
      "s_cmp_ge_u32 s85, 0x40000000\n"
      "s_cselect_b32 s74, s74, 0\n"
      "s_cselect_b32 s85, 0, s85\n"
      "s_sub_u32 s74, s74, s69\n"
      "s_add_u32 s69, s69, s74\n"
      "s_sub_u32 s75, s75, s70\n"
      "s_add_u32 s70, s70, s75\n"
      "s_sub_u32 s76, s76, s71\n"
      "s_add_u32 s71, s71, s76\n"
      "v_add_u32 v20, s74, v20\n"
      "v_add_u32 v21, s74, v21\n"
      "v_add_u32 v22, s74, v22\n"
      "v_add_u32 v23, s74, v23\n"
      "v_add_u32 v25, s74, v25\n"
      "s_cmp_eq_u32 s75, 0\n"
      "s_cbranch_scc1 L_ax_16_%=\n"
      "v_bfe_i32 v219, v27, 10, 1\n"
      "v_and_b32 v219, s75, v219\n"
      "v_sub_u32 v20, v20, v219\n"
      "v_bfe_i32 v219, v27, 12, 1\n"
      "v_and_b32 v219, s75, v219\n"
      "v_sub_u32 v21, v21, v219\n"
      "v_bfe_i32 v219, v27, 14, 1\n"
      "v_and_b32 v219, s75, v219\n"
      "v_sub_u32 v22, v22, v219\n"
      "v_bfe_i32 v219, v27, 16, 1\n"
      "v_and_b32 v219, s75, v219\n"
      "v_sub_u32 v23, v23, v219\n"
      "v_bfe_i32 v219, v27, 18, 1\n"
      "v_and_b32 v219, s75, v219\n"
      "v_sub_u32 v25, v25, v219\n"
      "L_ax_16_%=:\n"
      "s_cmp_eq_u32 s76, 0\n"
      "s_cbranch_scc1 L_ax_17_%=\n"
      "v_bfe_i32 v219, v27, 11, 1\n"
      "v_and_b32 v219, s76, v219\n"
      "v_sub_u32 v20, v20, v219\n"
      "v_bfe_i32 v219, v27, 13, 1\n"
      "v_and_b32 v219, s76, v219\n"
      "v_sub_u32 v21, v21, v219\n"
      "v_bfe_i32 v219, v27, 15, 1\n"
      "v_and_b32 v219, s76, v219\n"
      "v_sub_u32 v22, v22, v219\n"
      "v_bfe_i32 v219, v27, 17, 1\n"
      "v_and_b32 v219, s76, v219\n"
      "v_sub_u32 v23, v23, v219\n"
      "v_bfe_i32 v219, v27, 19, 1\n"
      "v_and_b32 v219, s76, v219\n"
      "v_sub_u32 v25, v25, v219\n"
      "L_ax_17_%=:\n"
      "s_or_b32 s48, s66, s69\n"
      "s_cmp_lg_u32 s48, 0\n"
      "s_cselect_b32 s48, 0x40000000, 0\n"
      "L_nf_15_%=:\n"
      "s_sub_u32 s92, s42, 1\n"
      "s_cmp_gt_u32 s92, s44\n"
      "s_cbranch_scc1 L_tail1_%=\n"
      "L_nofix_11_%=:\n"
      "s_cmp_lg_u64 s[56:57], 0\n"
      "s_cbranch_scc1 L_slow1_%=\n"
      "L_sel1_%=:\n"
      "s_cmp_lg_u64 s[54:55], 0\n"
      "s_cbranch_scc1 L_max1_%=\n"
      "s_branch L_loop1_%=\n"
      "L_slow1_%=:\n"
      "s_nop 4\n"
      "v_mfma_f32_16x16x32_bf16 v[218:221], v[174:177], v[130:133], 0\n"
      "ds_read_b128 v[96:99], v0 offset:18432\n"
      "v_subrev_u32 v141, 115456, v9\n"
      "v_mfma_f32_16x16x32_bf16 v[218:221], v[210:213], v[76:79], v[218:221]\n"
      "ds_read_b128 v[184:187], v9\n"
      "v_readlane_b32 s45, v37, s43\n"
      "v_mfma_f32_16x16x32_bf16 v[134:137], v[174:177], v[194:197], 0\n"
      "s_mov_b32 m0, 110592\n"
      "s_add_u32 s72, s42, 1\n"
      "v_readlane_b32 s77, v38, s72\n"
      "v_subrev_u32 v130, 115456, v9\n"
      "v_mfma_f32_16x16x32_bf16 v[134:137], v[210:213], v[80:83], v[134:137]\n"
      "s_and_b32 s73, s77, -16\n"
      "ds_read_b128 v[220:223], v0 offset:24576\n"
      "v_subrev_u32 v39, 115456, v9\n"
      "v_mfma_f32_16x16x32_bf16 v[100:103], v[174:177], v[214:217], 0\n"
      "ds_read_b128 v[174:177], v1 offset:18432\n"
      "v_readlane_b32 s46, v37, s42\n"
      "v_mfma_f32_16x16x32_bf16 v[100:103], v[210:213], v[84:87], v[100:103]\n"
      "ds_read_b128 v[210:213], v1 offset:24576\n"
      "s_waitcnt lgkmcnt(4)\n"
      "v_mfma_f32_16x16x32_bf16 v[158:161], a[0:3], v[96:99], 0\n"
      "ds_read_b128 v[136:139], v1 offset:30720\n"
      "v_mov_b32_dpp v188, v188 quad_perm:[0,0,0,0] row_mask:0xf bank_mask:0xf\n"
      "v_mfma_f32_16x16x32_bf16 v[150:153], a[24:27], v[96:99], 0\n"
      "buffer_load_dwordx4 v130, s[28:31], s73 offen  lds\n"
      "v_mfma_f32_16x16x32_bf16 v[190:193], a[48:51], v[96:99], 0\n"
      "s_waitcnt lgkmcnt(4)\n"
      "v_add_f32 v184, v184, v185\n"
      "v_add_f32 v186, v186, v187\n"
      "v_mfma_f32_16x16x32_bf16 v[154:157], a[72:75], v[96:99], 0\n"
      "ds_read_b128 v[120:123], v0 offset:30720\n"
      "v_add_f32 v184, v184, v186\n"
      "v_mfma_f32_16x16x32_bf16 v[112:115], a[96:99], v[96:99], 0\n"
      "s_mov_b32 m0, 111616\n"
      "v_rcp_f32 v188, v188\n"
      "v_mfma_f32_16x16x32_bf16 v[202:205], a[120:123], v[96:99], 0\n"
      "s_waitcnt lgkmcnt(2)\n"
      "v_rsq_f32 v184, v184\n"
      "v_mfma_f32_16x16x32_bf16 v[158:161], a[4:7], v[174:177], v[158:161]\n"
      "s_add_u32 s73, s73, 1024\n"
      "v_mul_f32 v184, 0x415db3d7, v184\n"
      "v_mov_b32_e64 v186, v184\n"
      "v_mfma_f32_16x16x32_bf16 v[158:161], a[8:11], v[220:223], v[158:161]\n"
      "buffer_load_dwordx4 v130, s[28:31], s73 offen  lds\n"
      "v_mfma_f32_16x16x32_bf16 v[150:153], a[28:31], v[174:177], v[150:153]\n"
      "s_add_u32 s73, s73, 1024\n"
      "v_permlane32_swap_b32_e64 v184, v186\n"
      "v_mfma_f32_16x16x32_bf16 v[150:153], a[32:35], v[220:223], v[150:153]\n"
      "s_mov_b32 m0, 112640\n"
      "v_mul_f32 v118, v186, v118\n"
      "v_mov_b32 v185, v184\n"
      "v_mfma_f32_16x16x32_bf16 v[190:193], a[52:55], v[174:177], v[190:193]\n"
      "s_nop 0\n"
      "v_permlane16_swap_b32 v184, v185\n"
      "v_mfma_f32_16x16x32_bf16 v[190:193], a[56:59], v[220:223], v[190:193]\n"
      "v_mul_f32 v167, v184, v167\n"
      "v_mul_f32 v166, v184, v166\n"
      "v_mfma_f32_16x16x32_bf16 v[154:157], a[76:79], v[174:177], v[154:157]\n"
      "buffer_load_dwordx4 v130, s[28:31], s73 offen  lds\n"
      "v_mfma_f32_16x16x32_bf16 v[154:157], a[80:83], v[220:223], v[154:157]\n"
      "v_mul_f32 v168, v184, v168\n"
      "v_mul_f32 v198, v184, v198\n"
      "v_mfma_f32_16x16x32_bf16 v[112:115], a[100:103], v[174:177], v[112:115]\n"
      "s_mov_b32 m0, 113664\n"
      "v_cvt_pk_bf16_f32 v166, v166, v167\n"
      "v_mul_f32 v200, v184, v200\n"
      "v_mfma_f32_16x16x32_bf16 v[112:115], a[104:107], v[220:223], v[112:115]\n"
      "s_add_u32 s73, s73, 1024\n"
      "v_mul_f32 v169, v184, v169\n"
      "v_mul_f32_e64 v199, v184, v199\n"
      "v_mfma_f32_16x16x32_bf16 v[202:205], a[124:127], v[174:177], v[202:205]\n"
      "ds_read_b128 v[146:149], v1 offset:20480\n"
      "v_cvt_pk_bf16_f32 v167, v168, v169\n"
      "v_mfma_f32_16x16x32_bf16 v[202:205], a[128:131], v[220:223], v[202:205]\n"
      "buffer_load_dwordx4 v130, s[28:31], s73 offen  lds\n"
      "v_mfma_f32_16x16x32_bf16 v[158:161], a[12:15], v[210:213], v[158:161]\n"
      "s_waitcnt lgkmcnt(1)\n"
      "v_mul_f32 v172, v185, v172\n"
      "v_mul_f32 v128, v186, v128\n"
      "v_mfma_f32_16x16x32_bf16 v[158:161], a[16:19], v[120:123], v[158:161]\n"
      "ds_read_b128 v[206:209], v0 offset:20480\n"
      "v_mul_f32 v201, v184, v201\n"
      "v_mfma_f32_16x16x32_bf16 v[158:161], a[20:23], v[136:139], v[158:161]\n"
      "s_add_u32 s73, s73, 1024\n"
      "v_mul_f32_e64 v180, v185, v180\n"
      "v_cvt_pk_bf16_f32 v168, v198, v199\n"
      "v_mfma_f32_16x16x32_bf16 v[150:153], a[36:39], v[210:213], v[150:153]\n"
      "v_mul_f32_e64 v181, v185, v181\n"
      "v_mul_f32_e64 v164, v185, v164\n"
      "v_mfma_f32_16x16x32_bf16 v[150:153], a[40:43], v[120:123], v[150:153]\n"
      "v_cvt_pk_bf16_f32 v169, v200, v201\n"
      "s_mov_b32 m0, 114688\n"
      "v_mfma_f32_16x16x32_bf16 v[150:153], a[44:47], v[136:139], v[150:153]\n"
      "s_mov_b32 exec_hi, 0xffff\n"
      "buffer_load_dwordx4 v130, s[28:31], s73 offen  lds\n"
      "s_mov_b32 exec_hi, -1\n"
      "v_pk_mul_f32 v[142:143], v[142:143], v[184:185] op_sel_hi:[1,0]\n"
      "v_pk_mul_f32 v[144:145], v[144:145], v[184:185] op_sel_hi:[1,0]\n"
      "v_pk_mul_f32 v[170:171], v[170:171], v[184:185] op_sel:[0,1]\n"
      "v_pk_mul_f32 v[162:163], v[162:163], v[184:185] op_sel:[0,1]\n"
      "v_pk_mul_f32 v[182:183], v[182:183], v[184:185] op_sel:[0,1]\n"
      "v_mfma_f32_16x16x32_bf16 v[190:193], a[60:63], v[210:213], v[190:193]\n"
      "ds_write_b128 v5, v[166:169] offset:18432\n"
      "v_mfma_f32_16x16x32_bf16 v[190:193], a[64:67], v[120:123], v[190:193]\n"
      "ds_read_b128 v[166:169], v0 offset:26624\n"
      "v_cvt_pk_bf16_f32 v142, v142, v143\n"
      "v_mfma_f32_16x16x32_bf16 v[190:193], a[68:71], v[136:139], v[190:193]\n"
      "s_add_u32 m0, s64, 0\n"
      "v_cvt_pk_bf16_f32 v162, v162, v163\n"
      "v_cvt_pk_bf16_f32 v143, v144, v145\n"
      "v_mfma_f32_16x16x32_bf16 v[154:157], a[84:87], v[210:213], v[154:157]\n"
      "ds_write_b64 v6, v[142:143] offset:18432\n"
      "v_mfma_f32_16x16x32_bf16 v[154:157], a[88:91], v[120:123], v[154:157]\n"
      "buffer_load_dwordx4 v16, s[16:19], s84 offen  lds\n"
      "v_mfma_f32_16x16x32_bf16 v[154:157], a[92:95], v[136:139], v[154:157]\n"
      "s_add_u32 s73, s73, 1024\n"
      "v_cvt_pk_bf16_f32 v170, v170, v171\n"
      "v_mul_f32_e64 v165, v185, v165\n"
      "v_mfma_f32_16x16x32_bf16 v[112:115], a[108:111], v[210:213], v[112:115]\n"
      "s_add_u32 m0, s64, 4096\n"
      "v_cvt_pk_bf16_f32 v163, v164, v165\n"
      "v_mul_f32 v110, v186, v110\n"
      "v_mfma_f32_16x16x32_bf16 v[112:115], a[112:115], v[120:123], v[112:115]\n"
      "v_mul_f32 v108, v186, v108\n"
      "v_cvt_pk_bf16_f32 v165, v182, v183\n"
      "v_mfma_f32_16x16x32_bf16 v[112:115], a[116:119], v[136:139], v[112:115]\n"
      "v_mul_f32 v109, v186, v109\n"
      "v_mul_f32 v126, v186, v126\n"
      "v_mfma_f32_16x16x32_bf16 v[202:205], a[132:135], v[210:213], v[202:205]\n"
      "buffer_load_dwordx4 v17, s[16:19], s84 offen  lds\n"
      "v_mfma_f32_16x16x32_bf16 v[202:205], a[136:139], v[120:123], v[202:205]\n"
      "v_bfe_u32 v78, v141, 4, 4\n"
      "v_mul_f32_e64 v173, v185, v173\n"
      "v_mfma_f32_16x16x32_bf16 v[202:205], a[140:143], v[136:139], v[202:205]\n"
      "s_add_u32 m0, s64, 8192\n"
      "v_cvt_pk_bf16_f32 v164, v180, v181\n"
      "v_fma_f32 v185, v154, v154, s51\n"
      "v_mfma_f32_16x16x32_bf16 v[130:133], v[96:99], a[144:147], 0\n"
      "ds_write_b128 v5, v[162:165] offset:24576\n"
      "v_mfma_f32_16x16x32_bf16 v[130:133], v[174:177], a[148:151], v[130:133]\n"
      "buffer_load_dwordx4 v18, s[16:19], s84 offen  lds\n"
      "v_mfma_f32_16x16x32_bf16 v[130:133], v[220:223], a[152:155], v[130:133]\n"
      "v_mul_f32_e64 v116, v186, v116\n"
      "v_cvt_pk_bf16_f32 v108, v108, v109\n"
      "v_mfma_f32_16x16x32_bf16 v[130:133], v[210:213], a[156:159], v[130:133]\n"
      "s_add_u32 m0, s64, 12288\n"
      "v_cvt_pk_bf16_f32 v171, v172, v173\n"
      "v_mul_f32 v119, v186, v119\n"
      "v_mfma_f32_16x16x32_bf16 v[130:133], v[120:123], a[160:163], v[130:133]\n"
      "v_mul_f32 v127, v186, v127\n"
      "v_fmac_f32_e64 v185, v155, v155\n"
      "v_mfma_f32_16x16x32_bf16 v[130:133], v[136:139], a[164:167], v[130:133]\n"
      "ds_write_b64 v6, v[170:171] offset:24576\n"
      "v_mfma_f32_16x16x32_bf16 v[194:197], v[96:99], a[168:171], 0\n"
      "buffer_load_dwordx4 v19, s[16:19], s84 offen  lds\n"
      "v_mfma_f32_16x16x32_bf16 v[194:197], v[174:177], a[172:175], v[194:197]\n"
      "v_cvt_pk_bf16_f32 v126, v126, v127\n"
      "v_lshrrev_b32_e64 v141, 8, v141\n"
      "v_mfma_f32_16x16x32_bf16 v[194:197], v[220:223], a[176:179], v[194:197]\n"
      "ds_read_b128 v[142:145], v1 offset:26624\n"
      "v_fma_f32 v219, v158, v158, s51\n"
      "v_mfma_f32_16x16x32_bf16 v[194:197], v[210:213], a[180:183], v[194:197]\n"
      "s_add_u32 m0, s65, 16384\n"
      "v_mul_f32 v129, v186, v129\n"
      "v_cvt_pk_bf16_f32 v127, v128, v129\n"
      "v_mfma_f32_16x16x32_bf16 v[194:197], v[120:123], a[184:187], v[194:197]\n"
      "ds_read_b128 v[198:201], v0 offset:32768\n"
      "v_fmac_f32 v185, v156, v156\n"
      "v_mfma_f32_16x16x32_bf16 v[194:197], v[136:139], a[188:191], v[194:197]\n"
      "buffer_load_dwordx4 v15, s[16:19], s84 offen  lds\n"
      "v_mfma_f32_16x16x32_bf16 v[214:217], v[96:99], a[192:195], 0\n"
      "v_cndmask_b32 v218, v218, v134, s[58:59]\n"
      "v_cvt_pk_bf16_f32 v129, v118, v119\n"
      "v_mfma_f32_16x16x32_bf16 v[214:217], v[174:177], a[196:199], v[214:217]\n"
      "s_waitcnt lgkmcnt(2)\n"
      "v_cndmask_b32 v218, v218, v100, s[60:61]\n"
      "v_mul_f32 v117, v186, v117\n"
      "v_mfma_f32_16x16x32_bf16 v[214:217], v[220:223], a[200:203], v[214:217]\n"
      "ds_read_b128 v[162:165], v1 offset:32768\n"
      "v_mul_f32 v111, v186, v111\n"
      "v_mfma_f32_16x16x32_bf16 v[214:217], v[210:213], a[204:207], v[214:217]\n"
      "s_and_b32 s77, s77, 15\n"
      "v_cvt_pk_bf16_f32 v128, v116, v117\n"
      "v_lshrrev_b32_e64 v39, 3, v39\n"
      "v_mfma_f32_16x16x32_bf16 v[214:217], v[120:123], a[208:211], v[214:217]\n"
      "buffer_load_dwordx4 v[116:119], v36, s[28:31], s86 offen\n"
      "v_mfma_f32_16x16x32_bf16 v[214:217], v[136:139], a[212:215], v[214:217]\n"
      "ds_write_b128 v5, v[126:129] offset:30720\n"
      "v_mfma_f32_16x16x32_bf16 v[126:129], a[0:3], v[206:209], 0\n"
      "s_or_b32 s95, s45, s46\n"
      "v_fmac_f32 v219, v159, v159\n"
      "v_fmac_f32 v185, v157, v157\n"
      "v_mfma_f32_16x16x32_bf16 v[126:129], a[4:7], v[146:149], v[126:129]\n"
      "s_waitcnt lgkmcnt(1)\n"
      "v_fmac_f32 v185, v112, v112\n"
      "v_fmac_f32 v219, v160, v160\n"
      "v_mfma_f32_16x16x32_bf16 v[126:129], a[8:11], v[166:169], v[126:129]\n"
      "buffer_load_dword v82, v29, s[28:31], s86 offen\n"
      "v_mfma_f32_16x16x32_bf16 v[126:129], a[12:15], v[142:145], v[126:129]\n"
      "v_fmac_f32_e64 v219, v161, v161\n"
      "v_cvt_pk_bf16_f32 v130, v130, v131\n"
      "v_mfma_f32_16x16x32_bf16 v[126:129], a[16:19], v[198:201], v[126:129]\n"
      "v_min_u32 v39, 96, v39\n"
      "v_fmac_f32 v185, v113, v113\n"
      "v_mfma_f32_16x16x32_bf16 v[126:129], a[20:23], v[162:165], v[126:129]\n"
      "v_fmac_f32 v219, v150, v150\n"
      "v_cvt_pk_bf16_f32 v109, v110, v111\n"
      "v_mfma_f32_16x16x32_bf16 v[180:183], a[24:27], v[206:209], 0\n"
      "v_fmac_f32_e64 v185, v114, v114\n"
      "v_mfma_f32_16x16x32_bf16 v[180:183], a[28:31], v[146:149], v[180:183]\n"
      "ds_write_b64 v6, v[108:109] offset:30720\n"
      "v_mfma_f32_16x16x32_bf16 v[180:183], a[32:35], v[166:169], v[180:183]\n"
      "s_or_b32 s95, s95, s48\n"
      "v_fmac_f32 v185, v115, v115\n"
      "v_fmac_f32_e64 v219, v151, v151\n"
      "v_mfma_f32_16x16x32_bf16 v[180:183], a[36:39], v[142:145], v[180:183]\n"
      "v_fma_f32 v184, v126, v126, s51\n"
      "v_mfma_f32_16x16x32_bf16 v[180:183], a[40:43], v[198:201], v[180:183]\n"
      "v_fmac_f32 v185, v202, v202\n"
      "v_fmac_f32 v219, v152, v152\n"
      "v_mfma_f32_16x16x32_bf16 v[180:183], a[44:47], v[162:165], v[180:183]\n"
      "ds_read_b128 v[170:173], v9 offset:4608\n"
      "v_cvt_pk_bf16_f32 v194, v194, v195\n"
      "v_mfma_f32_16x16x32_bf16 v[108:111], a[48:51], v[206:209], 0\n"
      "v_fmac_f32 v219, v153, v153\n"
      "v_fmac_f32 v219, v190, v190\n"
      "v_fmac_f32 v185, v203, v203\n"
      "v_mfma_f32_16x16x32_bf16 v[108:111], a[52:55], v[146:149], v[108:111]\n"
      "v_fmac_f32 v219, v191, v191\n"
      "v_fmac_f32_e64 v185, v204, v204\n"
      "v_mfma_f32_16x16x32_bf16 v[108:111], a[56:59], v[166:169], v[108:111]\n"
      "v_mul_u32_u24 v89, 98, v78\n"
      "v_fmac_f32 v219, v192, v192\n"
      "v_mfma_f32_16x16x32_bf16 v[108:111], a[60:63], v[142:145], v[108:111]\n"
      "v_fmac_f32 v219, v193, v193\n"
      "v_fmac_f32 v185, v205, v205\n"
      "v_mfma_f32_16x16x32_bf16 v[108:111], a[64:67], v[198:201], v[108:111]\n"
      "v_fmac_f32 v184, v127, v127\n"
      "v_cvt_pk_bf16_f32 v195, v196, v197\n"
      "v_mfma_f32_16x16x32_bf16 v[108:111], a[68:71], v[162:165], v[108:111]\n"
      "s_waitcnt lgkmcnt(0)\n"
      "v_permlane16_swap_b32 v219, v185\n"
      "v_mfma_f32_16x16x32_bf16 v[220:223], a[72:75], v[206:209], 0\n"
      "ds_read_b128 v[174:177], v1 offset:22528\n"
      "v_add_f32_e64 v219, v219, v185\n"
      "v_mfma_f32_16x16x32_bf16 v[220:223], a[76:79], v[146:149], v[220:223]\n"
      "v_or3_b32 v170, v170, v171, v172\n"
      "v_mov_b32 v185, v219\n"
      "v_mfma_f32_16x16x32_bf16 v[220:223], a[80:83], v[166:169], v[220:223]\n"
      "s_cmp_ge_u32 s95, 0x40000000\n"
      "s_cselect_b32 s94, -1, -2\n"
      "v_lshl_add_u32 v89, v141, 3, v89\n"
      "v_mfma_f32_16x16x32_bf16 v[220:223], a[84:87], v[142:145], v[220:223]\n"
      "ds_read_b128 v[134:137], v0 offset:22528\n"
      "v_fmac_f32 v184, v128, v128\n"
      "v_mfma_f32_16x16x32_bf16 v[220:223], a[88:91], v[198:201], v[220:223]\n"
      "v_fmac_f32 v184, v129, v129\n"
      "v_fmac_f32 v184, v180, v180\n"
      "v_mfma_f32_16x16x32_bf16 v[220:223], a[92:95], v[162:165], v[220:223]\n"
      "v_permlane32_swap_b32_e64 v219, v185\n"
      "v_mfma_f32_16x16x32_bf16 v[98:101], a[96:99], v[206:209], 0\n"
      "v_lshlrev_b32 v141, 2, v141\n"
      "v_add_f32 v219, v219, v185\n"
      "v_mfma_f32_16x16x32_bf16 v[98:101], a[100:103], v[146:149], v[98:101]\n"
      "v_rsq_f32_e64 v219, v219\n"
      "v_mfma_f32_16x16x32_bf16 v[98:101], a[104:107], v[166:169], v[98:101]\n"
      "s_nop 0\n"
      "v_fma_f32 v210, v220, v220, s51\n"
      "v_mfma_f32_16x16x32_bf16 v[98:101], a[108:111], v[142:145], v[98:101]\n"
      "v_mov_b32 v185, v219\n"
      "v_fmac_f32 v184, v181, v181\n"
      "v_mfma_f32_16x16x32_bf16 v[98:101], a[112:115], v[198:201], v[98:101]\n"
      "v_permlane16_swap_b32 v219, v185\n"
      "v_mfma_f32_16x16x32_bf16 v[98:101], a[116:119], v[162:165], v[98:101]\n"
      "v_mul_f32 v218, v188, v218\n"
      "v_mul_f32 v219, s50, v219\n"
      "v_mfma_f32_16x16x32_bf16 v[102:105], a[120:123], v[206:209], 0\n"
      "v_cvt_pk_bf16_f32 v131, v132, v133\n"
      "v_fmac_f32 v210, v221, v221\n"
      "v_mfma_f32_16x16x32_bf16 v[102:105], a[124:127], v[146:149], v[102:105]\n"
      "v_add_u32 v89, s77, v89\n"
      "v_add_u32 v39, s77, v39\n"
      "v_mfma_f32_16x16x32_bf16 v[102:105], a[128:131], v[166:169], v[102:105]\n"
      "v_fmac_f32 v184, v182, v182\n"
      "v_mul_f32 v190, v219, v190\n"
      "v_mfma_f32_16x16x32_bf16 v[102:105], a[132:135], v[142:145], v[102:105]\n"
      "v_fmac_f32 v210, v222, v222\n"
      "v_mul_f32 v159, v219, v159\n"
      "v_mfma_f32_16x16x32_bf16 v[102:105], a[136:139], v[198:201], v[102:105]\n"
      "v_cvt_pk_bf16_f32 v218, v218, v218\n"
      "v_fmac_f32 v210, v223, v223\n"
      "v_mfma_f32_16x16x32_bf16 v[102:105], a[140:143], v[162:165], v[102:105]\n"
      "v_mul_f32 v193, v219, v193\n"
      "v_mul_f32 v155, v185, v155\n"
      "v_mfma_f32_16x16x32_bf16 v[186:189], v[206:209], a[144:147], 0\n"
      "v_bitop3_b32 v170, v170, v173, s33 bitop3:0xa8\n"
      "v_mul_f32_e64 v160, v219, v160\n"
      "v_mfma_f32_16x16x32_bf16 v[186:189], v[146:149], a[148:151], v[186:189]\n"
      "v_mul_f32 v191, v219, v191\n"
      "v_fmac_f32 v184, v183, v183\n"
      "v_mfma_f32_16x16x32_bf16 v[186:189], v[166:169], a[152:155], v[186:189]\n"
      "v_fmac_f32 v210, v98, v98\n"
      "v_mul_f32 v153, v219, v153\n"
      "v_mfma_f32_16x16x32_bf16 v[186:189], v[142:145], a[156:159], v[186:189]\n"
      "v_sub_u32 v78, v78, v141\n"
      "v_fmac_f32 v210, v99, v99\n"
      "v_mfma_f32_16x16x32_bf16 v[186:189], v[198:201], a[160:163], v[186:189]\n"
      "v_fmac_f32 v184, v108, v108\n"
      "v_fmac_f32 v210, v100, v100\n"
      "v_mfma_f32_16x16x32_bf16 v[186:189], v[162:165], a[164:167], v[186:189]\n"
      "v_cvt_pk_bf16_f32 v190, v190, v191\n"
      "v_fmac_f32 v184, v109, v109\n"
      "v_fmac_f32 v210, v101, v101\n"
      "v_mfma_f32_16x16x32_bf16 v[120:123], v[206:209], a[168:171], 0\n"
      "ds_write_b16 v12, v218\n"
      "v_mfma_f32_16x16x32_bf16 v[120:123], v[146:149], a[172:175], v[120:123]\n"
      "v_lshlrev_b32 v78, 4, v78\n"
      "v_mov_b32 v218, 0x3fb9\n"
      "v_mfma_f32_16x16x32_bf16 v[120:123], v[166:169], a[176:179], v[120:123]\n"
      "v_fmac_f32 v184, v110, v110\n"
      "v_cvt_pk_bf16_f32 v214, v214, v215\n"
      "v_mfma_f32_16x16x32_bf16 v[120:123], v[142:145], a[180:183], v[120:123]\n"
      "s_waitcnt vmcnt(7)\n"
      "v_fmac_f32 v210, v102, v102\n"
      "v_mul_f32 v192, v219, v192\n"
      "v_mfma_f32_16x16x32_bf16 v[120:123], v[198:201], a[184:187], v[120:123]\n"
      "v_mul_f32 v115, v185, v115\n"
      "v_cmp_ne_u32 s[56:57], 0, v170\n"
      "v_mfma_f32_16x16x32_bf16 v[120:123], v[162:165], a[188:191], v[120:123]\n"
      "v_cvt_pk_bf16_f32 v215, v216, v217\n"
      "v_mul_f32 v203, v185, v203\n"
      "v_mfma_f32_16x16x32_bf16 v[170:173], v[206:209], a[192:195], 0\n"
      "ds_read_b128 v[206:209], v0 offset:28672\n"
      "v_fmac_f32 v210, v103, v103\n"
      "v_mfma_f32_16x16x32_bf16 v[170:173], v[146:149], a[196:199], v[170:173]\n"
      "ds_read_b128 v[146:149], v1 offset:28672\n"
      "v_cvt_pk_bf16_f32 v191, v192, v193\n"
      "v_mfma_f32_16x16x32_bf16 v[170:173], v[166:169], a[200:203], v[170:173]\n"
      "ds_read_b128 v[166:169], v1 offset:34816\n"
      "v_fmac_f32 v184, v111, v111\n"
      "v_mfma_f32_16x16x32_bf16 v[170:173], v[142:145], a[204:207], v[170:173]\n"
      "ds_read_b128 v[142:145], v0 offset:34816\n"
      "v_fmac_f32 v210, v104, v104\n"
      "v_mfma_f32_16x16x32_bf16 v[170:173], v[198:201], a[208:211], v[170:173]\n"
      "v_fmac_f32 v210, v105, v105\n"
      "v_cvt_pk_bf16_f32 v133, v188, v189\n"
      "v_mfma_f32_16x16x32_bf16 v[170:173], v[162:165], a[212:215], v[170:173]\n"
      "s_waitcnt lgkmcnt(4)\n"
      "v_permlane16_swap_b32 v184, v210\n"
      "v_mfma_f32_16x16x32_bf16 v[138:141], a[0:3], v[134:137], 0\n"
      "v_add_f32 v184, v184, v210\n"
      "v_mov_b32 v210, v184\n"
      "v_mfma_f32_16x16x32_bf16 v[138:141], a[4:7], v[174:177], v[138:141]\n"
      "s_waitcnt lgkmcnt(2)\n"
      "v_cvt_pk_bf16_f32 v197, v122, v123\n"
      "v_mul_f32 v112, v185, v112\n"
      "v_cmp_gt_u32 s[72:73], 64, v78\n"
      "v_mfma_f32_16x16x32_bf16 v[138:141], a[8:11], v[206:209], v[138:141]\n"
      "v_mul_f32 v113, v185, v113\n"
      "v_readlane_b32 s47, v38, s43\n"
      "v_mfma_f32_16x16x32_bf16 v[138:141], a[12:15], v[146:149], v[138:141]\n"
      "s_waitcnt lgkmcnt(0)\n"
      "v_permlane32_swap_b32 v184, v210\n"
      "v_add_f32_e64 v184, v184, v210\n"
      "v_mfma_f32_16x16x32_bf16 v[138:141], a[16:19], v[142:145], v[138:141]\n"
      "v_mul_f32 v158, v219, v158\n"
      "v_mul_f32 v161, v219, v161\n"
      "v_mfma_f32_16x16x32_bf16 v[138:141], a[20:23], v[166:169], v[138:141]\n"
      "v_rsq_f32_e64 v184, v184\n"
      "v_mfma_f32_16x16x32_bf16 v[162:165], a[24:27], v[134:137], 0\n"
      "v_mov_b32 v210, v184\n"
      "v_mul_f32 v152, v219, v152\n"
      "v_mfma_f32_16x16x32_bf16 v[162:165], a[28:31], v[174:177], v[162:165]\n"
      "v_permlane16_swap_b32_e64 v184, v210\n"
      "v_mfma_f32_16x16x32_bf16 v[162:165], a[32:35], v[206:209], v[162:165]\n"
      "v_and_b32 v124, 2, v89\n"
      "v_add_u32 v39, 115296, v39\n"
      "v_mfma_f32_16x16x32_bf16 v[162:165], a[36:39], v[146:149], v[162:165]\n"
      "v_mul_f32 v202, v185, v202\n"
      "v_cvt_pk_bf16_f32 v196, v120, v121\n"
      "v_cvt_pk_bf16_f32 v158, v158, v159\n"
      "v_cvt_pk_bf16_f32 v159, v160, v161\n"
      "v_fma_f32 v192, v138, v138, s51\n"
      "v_pk_mul_f32 v[150:151], v[150:151], v[218:219] op_sel:[0,1]\n"
      "v_mul_f32 v184, s50, v184\n"
      "v_pk_mul_f32 v[204:205], v[204:205], v[184:185] op_sel:[0,1]\n"
      "v_pk_mul_f32 v[102:103], v[102:103], v[210:211] op_sel_hi:[1,0]\n"
      "v_mul_f32 v99, v210, v99\n"
      "v_pk_mul_f32 v[220:221], v[220:221], v[210:211] op_sel_hi:[1,0]\n"
      "v_pk_mul_f32 v[222:223], v[222:223], v[210:211] op_sel_hi:[1,0]\n"
      "v_pk_mul_f32 v[108:109], v[108:109], v[184:185] op_sel_hi:[1,0]\n"
      "v_pk_mul_f32 v[156:157], v[156:157], v[184:185] op_sel:[0,1]\n"
      "v_pk_mul_f32 v[126:127], v[126:127], v[184:185] op_sel_hi:[1,0]\n"
      "v_pk_mul_f32 v[128:129], v[128:129], v[184:185] op_sel_hi:[1,0]\n"
      "v_mul_f32_e64 v98, v210, v98\n"
      "v_pk_mul_f32 v[104:105], v[104:105], v[210:211] op_sel_hi:[1,0]\n"
      "v_mfma_f32_16x16x32_bf16 v[162:165], a[40:43], v[142:145], v[162:165]\n"
      "v_mov_b32 v219, 0\n"
      "v_mul_f32 v110, v184, v110\n"
      "v_mfma_f32_16x16x32_bf16 v[162:165], a[44:47], v[166:169], v[162:165]\n"
      "v_cvt_pk_bf16_f32 v217, v172, v173\n"
      "v_mul_f32 v114, v185, v114\n"
      "v_mfma_f32_16x16x32_bf16 v[120:123], a[48:51], v[134:137], 0\n"
      "v_fmac_f32 v192, v139, v139\n"
      "v_mul_f32 v180, v184, v180\n"
      "v_mfma_f32_16x16x32_bf16 v[120:123], a[52:55], v[174:177], v[120:123]\n"
      "v_fmac_f32 v192, v140, v140\n"
      "v_fmac_f32 v192, v141, v141\n"
      "v_mfma_f32_16x16x32_bf16 v[120:123], a[56:59], v[206:209], v[120:123]\n"
      "v_mul_f32 v111, v184, v111\n"
      "v_mul_f32 v154, v185, v154\n"
      "v_mfma_f32_16x16x32_bf16 v[120:123], a[60:63], v[146:149], v[120:123]\n"
      "s_cmp_gt_u32 s42, s44\n"
      "s_cselect_b32 s95, s94, s95\n"
      "v_cvt_pk_bf16_f32 v154, v154, v155\n"
      "v_mfma_f32_16x16x32_bf16 v[120:123], a[64:67], v[142:145], v[120:123]\n"
      "v_mul_f32 v101, v210, v101\n"
      "v_mul_f32_e64 v100, v210, v100\n"
      "v_mfma_f32_16x16x32_bf16 v[120:123], a[68:71], v[166:169], v[120:123]\n"
      "v_cvt_pk_bf16_f32 v155, v156, v157\n"
      "v_fmac_f32_e64 v192, v162, v162\n"
      "v_mfma_f32_16x16x32_bf16 v[210:213], a[72:75], v[134:137], 0\n"
      "v_cvt_pk_bf16_f32 v102, v102, v103\n"
      "v_cvt_pk_bf16_f32 v216, v170, v171\n"
      "v_mfma_f32_16x16x32_bf16 v[210:213], a[76:79], v[174:177], v[210:213]\n"
      "v_lshlrev_b64 v[218:219], v78, v[218:219]\n"
      "v_cvt_pk_bf16_f32 v156, v112, v113\n"
      "v_mfma_f32_16x16x32_bf16 v[210:213], a[80:83], v[206:209], v[210:213]\n"
      "v_cvt_pk_bf16_f32 v132, v186, v187\n"
      "v_mul_f32_e64 v183, v184, v183\n"
      "v_mfma_f32_16x16x32_bf16 v[210:213], a[84:87], v[146:149], v[210:213]\n"
      "s_cmp_ge_u32 s95, 0x40000000\n"
      "s_cselect_b64 vcc, -1, s[56:57]\n"
      "v_cvt_pk_bf16_f32 v220, v220, v221\n"
      "v_cvt_pk_bf16_f32 v103, v104, v105\n"
      "v_mfma_f32_16x16x32_bf16 v[210:213], a[88:91], v[142:145], v[210:213]\n"
      "v_fmac_f32 v192, v163, v163\n"
      "v_fmac_f32 v192, v164, v164\n"
      "v_mfma_f32_16x16x32_bf16 v[210:213], a[92:95], v[166:169], v[210:213]\n"
      "v_cvt_pk_bf16_f32 v221, v222, v223\n"
      "v_cvt_pk_bf16_f32 v160, v150, v151\n"
      "v_and_b32 v89, -4, v89\n"
      "v_mfma_f32_16x16x32_bf16 v[170:173], a[96:99], v[134:137], 0\n"
      "v_cndmask_b32_e64 v218, 0, v218, s[72:73]\n"
      "v_fmac_f32 v192, v165, v165\n"
      "v_mfma_f32_16x16x32_bf16 v[170:173], a[100:103], v[174:177], v[170:173]\n"
      "v_cvt_pk_bf16_f32 v202, v202, v203\n"
      "v_cvt_pk_bf16_f32 v126, v126, v127\n"
      "v_mfma_f32_16x16x32_bf16 v[170:173], a[104:107], v[206:209], v[170:173]\n"
      "ds_read_u16 v39, v39\n"
      "v_fma_f32 v105, v210, v210, s51\n"
      "v_mfma_f32_16x16x32_bf16 v[170:173], a[108:111], v[146:149], v[170:173]\n"
      "v_cvt_pk_bf16_f32 v108, v108, v109\n"
      "v_fmac_f32 v105, v211, v211\n"
      "v_mfma_f32_16x16x32_bf16 v[170:173], a[112:115], v[142:145], v[170:173]\n"
      "v_fmac_f32 v192, v120, v120\n"
      "v_cvt_pk_bf16_f32 v161, v152, v153\n"
      "v_cvt_pk_bf16_f32 v203, v204, v205\n"
      "v_mfma_f32_16x16x32_bf16 v[170:173], a[116:119], v[166:169], v[170:173]\n"
      "v_mul_f32 v181, v184, v181\n"
      "v_fmac_f32 v105, v212, v212\n"
      "v_mfma_f32_16x16x32_bf16 v[186:189], a[120:123], v[134:137], 0\n"
      "v_mul_f32 v182, v184, v182\n"
      "v_cvt_pk_bf16_f32 v109, v110, v111\n"
      "v_mfma_f32_16x16x32_bf16 v[186:189], a[124:127], v[174:177], v[186:189]\n"
      "ds_read_b128 v[198:201], v7\n"
      "v_fmac_f32 v192, v121, v121\n"
      "v_mfma_f32_16x16x32_bf16 v[186:189], a[128:131], v[206:209], v[186:189]\n"
      "v_fmac_f32 v105, v213, v213\n"
      "v_cvt_pk_bf16_f32 v127, v128, v129\n"
      "v_mfma_f32_16x16x32_bf16 v[186:189], a[132:135], v[146:149], v[186:189]\n"
      "v_cvt_pk_bf16_f32 v128, v180, v181\n"
      "v_fmac_f32 v192, v122, v122\n"
      "v_mfma_f32_16x16x32_bf16 v[186:189], a[136:139], v[142:145], v[186:189]\n"
      "v_cvt_pk_bf16_f32 v157, v114, v115\n"
      "v_fmac_f32 v105, v170, v170\n"
      "v_mfma_f32_16x16x32_bf16 v[186:189], a[140:143], v[166:169], v[186:189]\n"
      "v_fmac_f32 v105, v171, v171\n"
      "v_fmac_f32 v105, v172, v172\n"
      "v_mfma_f32_16x16x32_bf16 v[110:113], v[134:137], a[144:147], 0\n"
      "v_fmac_f32 v105, v173, v173\n"
      "v_cvt_pk_bf16_f32 v222, v98, v99\n"
      "v_mfma_f32_16x16x32_bf16 v[110:113], v[174:177], a[148:151], v[110:113]\n"
      "v_fmac_f32 v192, v123, v123\n"
      "v_cvt_pk_bf16_f32 v223, v100, v101\n"
      "v_mfma_f32_16x16x32_bf16 v[110:113], v[206:209], a[152:155], v[110:113]\n"
      "v_fmac_f32 v105, v186, v186\n"
      "v_fmac_f32_e64 v105, v187, v187\n"
      "v_mfma_f32_16x16x32_bf16 v[110:113], v[146:149], a[156:159], v[110:113]\n"
      "v_fmac_f32 v105, v188, v188\n"
      "v_fmac_f32 v105, v189, v189\n"
      "v_mfma_f32_16x16x32_bf16 v[110:113], v[142:145], a[160:163], v[110:113]\n"
      "s_nop 0\n"
      "v_permlane16_swap_b32 v192, v105\n"
      "v_mfma_f32_16x16x32_bf16 v[110:113], v[166:169], a[164:167], v[110:113]\n"
      "v_add_f32 v192, v192, v105\n"
      "v_mov_b32 v105, v192\n"
      "v_mfma_f32_16x16x32_bf16 v[178:181], v[134:137], a[168:171], 0\n"
      "s_nop 0\n"
      "v_permlane32_swap_b32 v192, v105\n"
      "v_mfma_f32_16x16x32_bf16 v[178:181], v[174:177], a[172:175], v[178:181]\n"
      "v_add_f32 v192, v192, v105\n"
      "v_cvt_pk_bf16_f32 v129, v182, v183\n"
      "v_mfma_f32_16x16x32_bf16 v[178:181], v[206:209], a[176:179], v[178:181]\n"
      "v_rsq_f32 v192, v192\n"
      "v_mfma_f32_16x16x32_bf16 v[178:181], v[146:149], a[180:183], v[178:181]\n"
      "v_mov_b32 v105, v192\n"
      "v_cvt_pk_bf16_f32 v77, v112, v113\n"
      "v_mfma_f32_16x16x32_bf16 v[178:181], v[142:145], a[184:187], v[178:181]\n"
      "s_waitcnt lgkmcnt(0)\n"
      "v_permlane16_swap_b32_e64 v192, v105\n"
      "v_mfma_f32_16x16x32_bf16 v[178:181], v[166:169], a[188:191], v[178:181]\n"
      "ds_read_b128 v[182:185], v2 offset:3072\n"
      "v_cndmask_b32_e64 v219, 0, v219, s[72:73]\n"
      "v_mfma_f32_16x16x32_bf16 v[98:101], v[134:137], a[192:195], 0\n"
      "ds_read_b128 v[134:137], v7 offset:4096\n"
      "v_mfma_f32_16x16x32_bf16 v[98:101], v[174:177], a[196:199], v[98:101]\n"
      "ds_read_b128 v[174:177], v2 offset:6144\n"
      "v_mul_f32_e64 v192, s50, v192\n"
      "v_mfma_f32_16x16x32_bf16 v[98:101], v[206:209], a[200:203], v[98:101]\n"
      "ds_read_b128 v[150:153], v2 offset:9216\n"
      "v_cvt_pk_bf16_f32 v81, v180, v181\n"
      "v_mfma_f32_16x16x32_bf16 v[98:101], v[146:149], a[204:207], v[98:101]\n"
      "ds_read_b128 v[146:149], v2\n"
      "v_mul_f32_e64 v211, v105, v211\n"
      "v_mfma_f32_16x16x32_bf16 v[98:101], v[142:145], a[208:211], v[98:101]\n"
      "buffer_store_dwordx4 v[198:201], v20, s[24:27], s85 offen sc1\n"
      "v_pk_mul_f32 v[172:173], v[172:173], v[104:105] op_sel:[0,1]\n"
      "v_pk_mul_f32 v[120:121], v[120:121], v[192:193] op_sel_hi:[1,0]\n"
      "v_pk_mul_f32 v[170:171], v[170:171], v[104:105] op_sel:[0,1]\n"
      "v_pk_mul_f32 v[186:187], v[186:187], v[104:105] op_sel:[0,1]\n"
      "v_pk_mul_f32 v[212:213], v[212:213], v[104:105] op_sel:[0,1]\n"
      "v_mul_f32_e64 v210, v105, v210\n"
      "v_pk_mul_f32 v[164:165], v[164:165], v[192:193] op_sel_hi:[1,0]\n"
      "v_pk_mul_f32 v[162:163], v[162:163], v[192:193] op_sel_hi:[1,0]\n"
      "v_pk_mul_f32 v[138:139], v[138:139], v[192:193] op_sel_hi:[1,0]\n"
      "v_pk_mul_f32 v[122:123], v[122:123], v[192:193] op_sel_hi:[1,0]\n"
      "v_pk_mul_f32 v[140:141], v[140:141], v[192:193] op_sel_hi:[1,0]\n"
      "v_pk_mul_f32 v[188:189], v[188:189], v[104:105] op_sel:[0,1]\n"
      "v_mfma_f32_16x16x32_bf16 v[98:101], v[166:169], a[212:215], v[98:101]\n"
      "s_waitcnt lgkmcnt(0)\n"
      "v_cvt_pk_bf16_f32 v76, v110, v111\n"
      "v_add_u32 v110, 110592, v89\n"
      "v_cvt_pk_bf16_f32 v80, v178, v179\n"
      "v_mfma_f32_16x16x32_bf16 v[166:169], a[216:219], v[146:149], 0\n"
      "v_cvt_pk_bf16_f32 v186, v186, v187\n"
      "v_cvt_pk_bf16_f32 v210, v210, v211\n"
      "v_mfma_f32_16x16x32_bf16 v[166:169], a[220:223], v[182:185], v[166:169]\n"
      "ds_read_b128 v[178:181], v2 offset:12288\n"
      "v_mfma_f32_16x16x32_bf16 v[166:169], a[224:227], v[174:177], v[166:169]\n"
      "v_cvt_pk_bf16_f32 v84, v98, v99\n"
      "v_cvt_pk_bf16_f32 v85, v100, v101\n"
      "v_mfma_f32_16x16x32_bf16 v[198:201], a[240:243], v[146:149], 0\n"
      "buffer_store_dwordx4 v[134:137], v21, s[24:27], s85 offen sc1\n"
      "v_mfma_f32_16x16x32_bf16 v[198:201], a[244:247], v[182:185], v[198:201]\n"
      "s_nop 0\n"
      "ds_read_b128 v[134:137], v2 offset:15360\n"
      "v_cvt_pk_bf16_f32 v187, v188, v189\n"
      "v_mfma_f32_16x16x32_bf16 v[198:201], a[248:251], v[174:177], v[198:201]\n"
      "ds_read2_b32 v[208:209], v110 offset0:16 offset1:17\n"
      "v_cmp_gt_u32 s[72:73], 64, v78\n"
      "v_mfma_f32_16x16x32_bf16 v[142:145], v[232:235], v[146:149], 0\n"
      "v_cvt_pk_bf16_f32 v120, v120, v121\n"
      "v_cvt_pk_bf16_f32 v138, v138, v139\n"
      "v_mfma_f32_16x16x32_bf16 v[142:145], v[236:239], v[182:185], v[142:145]\n"
      "ds_read_b128 v[182:185], v7 offset:8192\n"
      "v_add_u32 v114, 112160, v89\n"
      "v_mfma_f32_16x16x32_bf16 v[142:145], v[240:243], v[174:177], v[142:145]\n"
      "ds_read2_b32 v[206:207], v110 offset0:0 offset1:1\n"
      "v_cvt_pk_bf16_f32 v139, v140, v141\n"
      "v_mfma_f32_16x16x32_bf16 v[166:169], a[228:231], v[150:153], v[166:169]\n"
      "s_waitcnt lgkmcnt(4)\n"
      "v_mov_b32_e64 v105, 0\n"
      "v_cvt_pk_bf16_f32 v211, v212, v213\n"
      "v_mfma_f32_16x16x32_bf16 v[166:169], a[232:235], v[178:181], v[166:169]\n"
      "s_waitcnt lgkmcnt(3)\n"
      "v_cvt_pk_bf16_f32 v213, v172, v173\n"
      "v_cvt_pk_bf16_f32 v212, v170, v171\n"
      "v_mfma_f32_16x16x32_bf16 v[166:169], a[236:239], v[134:137], v[166:169]\n"
      "ds_read_b128 v[174:177], v10 offset:1200\n"
      "v_cvt_pk_bf16_f32 v141, v164, v165\n"
      "v_cvt_pk_bf16_f32 v140, v162, v163\n"
      "v_mfma_f32_16x16x32_bf16 v[198:201], a[252:255], v[150:153], v[198:201]\n"
      "ds_read2_b32 v[188:189], v110 offset0:2 offset1:10\n"
      "v_mov_b32 v104, 0xbb2b\n"
      "v_mfma_f32_16x16x32_bf16 v[198:201], v[224:227], v[178:181], v[198:201]\n"
      "ds_read_b64 v[192:193], v10 offset:1192\n"
      "v_cvt_pk_bf16_f32 v121, v122, v123\n"
      "v_mul_f32 v97, v167, v167\n"
      "v_mfma_f32_16x16x32_bf16 v[198:201], v[228:231], v[134:137], v[198:201]\n"
      "s_waitcnt lgkmcnt(3)\n"
      "buffer_store_dwordx4 v[182:185], v22, s[24:27], s85 offen sc1\n"
      "v_mfma_f32_16x16x32_bf16 v[142:145], v[244:247], v[150:153], v[142:145]\n"
      "ds_read_b128 v[162:165], v7 offset:12288\n"
      "v_fma_f32 v96, v166, v166, s52\n"
      "v_mfma_f32_16x16x32_bf16 v[142:145], v[248:251], v[178:181], v[142:145]\n"
      "ds_read2_b32 v[170:171], v110 offset0:24 offset1:18\n"
      "v_fmac_f32 v97, v169, v169\n"
      "v_mfma_f32_16x16x32_bf16 v[142:145], v[252:255], v[134:137], v[142:145]\n"
      "s_waitcnt lgkmcnt(3)\n"
      "v_fmac_f32_e64 v96, v168, v168\n"
      "v_alignbyte_b32 v206, v207, v206, v124\n"
      "v_mfma_f32_16x16x32_bf16 v[134:137], v[154:157], v[158:161], v[40:43]\n"
      "ds_read2_b32 v[172:173], v110 offset0:8 offset1:9\n"
      "v_lshlrev_b64 v[104:105], v78, v[104:105]\n"
      "v_mfma_f32_16x16x32_bf16 v[110:113], v[220:223], v[158:161], v[44:47]\n"
      "ds_read2_b32 v[122:123], v114 offset0:8 offset1:9\n"
      "v_alignbyte_b32 v208, v209, v208, v124\n"
      "v_mfma_f32_16x16x32_bf16 v[98:101], v[210:213], v[158:161], v[48:51]\n"
      "ds_read_b128 v[180:183], v7 offset:16384\n"
      "v_fmac_f32 v97, v199, v199\n"
      "v_mfma_f32_16x16x32_bf16 v[150:153], v[174:177], v[158:161], v[24:27]\n"
      "v_alignbyte_b32 v207, v188, v207, v124\n"
      "v_fmac_f32 v96, v198, v198\n"
      "v_mfma_f32_16x16x16_bf16 v[134:137], v[202:203], v[190:191], v[134:137]\n"
      "s_waitcnt lgkmcnt(3)\n"
      "v_alignbyte_b32 v209, v171, v209, v124\n"
      "v_mfma_f32_16x16x16_bf16 v[110:113], v[102:103], v[190:191], v[110:113]\n"
      "buffer_store_dwordx4 v[162:165], v23, s[24:27], s85 offen sc1\n"
      "v_mfma_f32_16x16x16_bf16 v[98:101], v[186:187], v[190:191], v[98:101]\n"
      "v_alignbyte_b32 v170, v170, v170, v124\n"
      "v_mfma_f32_16x16x16_bf16 v[150:153], v[192:193], v[190:191], v[150:153]\n"
      "s_waitcnt lgkmcnt(0)\n"
      "v_alignbyte_b32 v172, v173, v172, v124\n"
      "v_cndmask_b32_e64 v104, 0, v104, s[72:73]\n"
      "v_alignbyte_b32 v173, v189, v173, v124\n"
      "v_mfma_f32_16x16x16_bf16 v[134:137], v[218:219], v[206:207], v[134:137]\n"
      "ds_read2_b32 v[158:159], v114 offset0:0 offset1:1\n"
      "v_cndmask_b32_e64 v105, 0, v105, s[72:73]\n"
      "v_mfma_f32_16x16x16_bf16 v[110:113], v[218:219], v[172:173], v[110:113]\n"
      "v_fmac_f32_e64 v96, v200, v200\n"
      "v_alignbyte_b32 v122, v123, v122, v124\n"
      "v_fmac_f32_e64 v97, v201, v201\n"
      "v_mfma_f32_16x16x16_bf16 v[98:101], v[218:219], v[208:209], v[98:101]\n"
      "ds_read_b128 v[188:191], v2 offset:256\n"
      "v_fmac_f32_e64 v97, v143, v143\n"
      "v_mfma_f32_16x16x16_bf16 v[150:153], v[218:219], v[170:171], v[150:153]\n"
      "ds_read_b128 v[146:149], v2 offset:6400\n"
      "v_mfma_f32_16x16x16_bf16 v[134:137], v[104:105], v[206:207], v[134:137]\n"
      "ds_read_b128 v[204:207], v2 offset:3328\n"
      "v_fmac_f32 v96, v142, v142\n"
      "v_mfma_f32_16x16x16_bf16 v[110:113], v[104:105], v[172:173], v[110:113]\n"
      "buffer_store_dwordx4 v[180:183], v25, s[24:27], s85 offen sc1\n"
      "v_pk_fma_f32 v[96:97], v[144:145], v[144:145], v[96:97]\n"
      "v_mfma_f32_16x16x16_bf16 v[98:101], v[104:105], v[208:209], v[98:101]\n"
      "ds_read2_b32 v[172:173], v114 offset0:2 offset1:10\n"
      "v_add_u32 v115, 113728, v89\n"
      "v_mfma_f32_16x16x16_bf16 v[150:153], v[104:105], v[170:171], v[150:153]\n"
      "s_waitcnt lgkmcnt(3)\n"
      "v_alignbyte_b32 v158, v159, v158, v124\n"
      "v_max3_f32 v184, v134, v135, v136\n"
      "v_mfma_f32_16x16x32_bf16 v[162:165], a[216:219], v[188:191], 0\n"
      "ds_read2_b32 v[160:161], v114 offset0:16 offset1:17\n"
      "v_add_f32 v96, v96, v97\n"
      "s_waitcnt lgkmcnt(2)\n"
      "v_mfma_f32_16x16x32_bf16 v[162:165], a[220:223], v[204:207], v[162:165]\n"
      "ds_read2_b32 v[152:153], v114 offset0:24 offset1:18\n"
      "v_max3_f32 v182, v137, v110, v111\n"
      "v_max3_f32 v184, v184, v112, v113\n"
      "s_waitcnt vmcnt(6)\n"
      "v_or3_b32 v116, v116, v117, v118\n"
      "v_mfma_f32_16x16x32_bf16 v[162:165], a[224:227], v[146:149], v[162:165]\n"
      "v_max3_f32 v182, v182, v98, v99\n"
      "v_max3_f32 v184, v184, v100, v101\n"
      "v_max3_f32 v184, v184, v182, v150\n"
      "v_mfma_f32_16x16x32_bf16 v[180:183], a[240:243], v[188:191], 0\n"
      "s_waitcnt lgkmcnt(2)\n"
      "v_alignbyte_b32 v123, v173, v123, v124\n"
      "v_alignbyte_b32 v159, v172, v159, v124\n"
      "v_mfma_f32_16x16x32_bf16 v[180:183], a[244:247], v[204:207], v[180:183]\n"
      "ds_read_u16 v78, v11 offset:1168\n"
      "v_mov_b32_e64 v209, v184\n"
      "v_mfma_f32_16x16x32_bf16 v[180:183], a[248:251], v[146:149], v[180:183]\n"
      "s_waitcnt lgkmcnt(2)\n"
      "v_alignbyte_b32 v160, v161, v160, v124\n"
      "s_waitcnt vmcnt(5)\n"
      "v_or3_b32 v116, v116, v119, v82\n"
      "v_mfma_f32_16x16x32_bf16 v[170:173], v[232:235], v[188:191], 0\n"
      "ds_write_b32 v31, v116 offset:5632\n"
      "v_mfma_f32_16x16x32_bf16 v[170:173], v[236:239], v[204:207], v[170:173]\n"
      "s_waitcnt lgkmcnt(2)\n"
      "v_alignbyte_b32 v161, v153, v161, v124\n"
      "v_alignbyte_b32 v152, v152, v152, v124\n"
      "v_mfma_f32_16x16x32_bf16 v[170:173], v[240:243], v[146:149], v[170:173]\n"
      "v_permlane16_swap_b32 v184, v209\n"
      "v_mfma_f32_16x16x32_bf16 v[188:191], v[154:157], v[126:129], v[52:55]\n"
      "v_max_f32 v184, v184, v209\n"
      "v_mov_b32 v209, v184\n"
      "v_mfma_f32_16x16x32_bf16 v[116:119], v[220:223], v[126:129], v[56:59]\n"
      "s_nop 0\n"
      "v_permlane32_swap_b32 v184, v209\n"
      "v_max_f32 v184, v184, v209\n"
      "v_mfma_f32_16x16x32_bf16 v[206:209], v[210:213], v[126:129], v[60:63]\n"
      "v_sub_f32 v101, v101, v184\n"
      "v_sub_f32 v113, v113, v184\n"
      "v_mfma_f32_16x16x32_bf16 v[146:149], v[174:177], v[126:129], v[26:29]\n"
      "v_exp_f32 v113, v113\n"
      "v_mfma_f32_16x16x16_bf16 v[188:191], v[202:203], v[108:109], v[188:191]\n"
      "v_exp_f32 v101, v101\n"
      "v_sub_f32 v134, v134, v184\n"
      "v_mfma_f32_16x16x16_bf16 v[116:119], v[102:103], v[108:109], v[116:119]\n"
      "v_sub_f32 v112, v112, v184\n"
      "v_sub_f32 v99, v99, v184\n"
      "v_sub_f32 v100, v100, v184\n"
      "v_mfma_f32_16x16x16_bf16 v[206:209], v[186:187], v[108:109], v[206:209]\n"
      "v_sub_f32 v135, v135, v184\n"
      "v_sub_f32 v137, v137, v184\n"
      "v_sub_f32 v136, v136, v184\n"
      "v_mfma_f32_16x16x16_bf16 v[146:149], v[192:193], v[108:109], v[146:149]\n"
      "v_exp_f32 v100, v100\n"
      "v_exp_f32 v112, v112\n"
      "v_mfma_f32_16x16x16_bf16 v[188:191], v[218:219], v[158:159], v[188:191]\n"
      "v_exp_f32 v135, v135\n"
      "v_sub_f32 v98, v98, v184\n"
      "v_mfma_f32_16x16x16_bf16 v[116:119], v[218:219], v[122:123], v[116:119]\n"
      "v_cvt_pk_bf16_f32 v89, v100, v101\n"
      "v_exp_f32 v134, v134\n"
      "v_mfma_f32_16x16x16_bf16 v[206:209], v[218:219], v[160:161], v[206:209]\n"
      "v_exp_f32 v137, v137\n"
      "v_sub_f32_e64 v110, v110, v184\n"
      "v_mfma_f32_16x16x16_bf16 v[146:149], v[218:219], v[152:153], v[146:149]\n"
      "v_cvt_pk_bf16_f32 v134, v134, v135\n"
      "v_exp_f32_e64 v136, v136\n"
      "v_mfma_f32_16x16x16_bf16 v[188:191], v[104:105], v[158:159], v[188:191]\n"
      "ds_read_b128 v[126:129], v2 offset:9472\n"
      "v_exp_f32 v99, v99\n"
      "v_mfma_f32_16x16x16_bf16 v[116:119], v[104:105], v[122:123], v[116:119]\n"
      "v_sub_f32 v111, v111, v184\n"
      "v_exp_f32 v111, v111\n"
      "v_sub_f32 v150, v150, v184\n"
      "v_mfma_f32_16x16x16_bf16 v[206:209], v[104:105], v[160:161], v[206:209]\n"
      "ds_read_b128 v[106:109], v2 offset:12544\n"
      "ds_read_b128 v[158:161], v2 offset:15616\n"
      "v_mfma_f32_16x16x16_bf16 v[146:149], v[104:105], v[152:153], v[146:149]\n"
      "v_exp_f32 v98, v98\n"
      "v_exp_f32 v110, v110\n"
      "v_cvt_pk_bf16_f32 v135, v136, v137\n"
      "v_cvt_pk_bf16_f32 v136, v110, v111\n"
      "v_max3_f32 v111, v188, v189, v190\n"
      "v_cvt_pk_bf16_f32 v88, v98, v99\n"
      "v_max3_f32 v111, v111, v118, v119\n"
      "v_exp_f32_e64 v150, v150\n"
      "v_cvt_pk_bf16_f32 v137, v112, v113\n"
      "v_max3_f32 v149, v191, v116, v117\n"
      "v_max3_f32 v111, v111, v208, v209\n"
      "v_max3_f32 v149, v149, v206, v207\n"
      "v_max3_f32 v111, v111, v149, v146\n"
      "v_mov_b32 v149, v111\n"
      "s_waitcnt lgkmcnt(2)\n"
      "v_mfma_f32_16x16x32_bf16 v[162:165], a[228:231], v[126:129], v[162:165]\n"
      "s_waitcnt lgkmcnt(1)\n"
      "v_permlane16_swap_b32 v111, v149\n"
      "v_mfma_f32_16x16x32_bf16 v[162:165], a[232:235], v[106:109], v[162:165]\n"
      "s_waitcnt lgkmcnt(0)\n"
      "v_max_f32 v111, v111, v149\n"
      "v_mov_b32 v149, v111\n"
      "v_mfma_f32_16x16x32_bf16 v[162:165], a[236:239], v[158:161], v[162:165]\n"
      "s_nop 0\n"
      "v_permlane32_swap_b32_e64 v111, v149\n"
      "v_mfma_f32_16x16x32_bf16 v[180:183], a[252:255], v[126:129], v[180:183]\n"
      "v_max_f32 v111, v111, v149\n"
      "v_sub_f32 v188, v188, v111\n"
      "v_mfma_f32_16x16x32_bf16 v[180:183], v[224:227], v[106:109], v[180:183]\n"
      "ds_read2_b32 v[148:149], v115 offset0:8 offset1:9\n"
      "v_sub_f32 v119, v119, v111\n"
      "v_mfma_f32_16x16x32_bf16 v[180:183], v[228:231], v[158:161], v[180:183]\n"
      "ds_read_u16 v82, v11 offset:1200\n"
      "v_sub_f32 v190, v190, v111\n"
      "v_mfma_f32_16x16x32_bf16 v[170:173], v[244:247], v[126:129], v[170:173]\n"
      "ds_read2_b32 v[204:205], v115 offset0:16 offset1:17\n"
      "v_sub_f32 v189, v189, v111\n"
      "v_mfma_f32_16x16x32_bf16 v[170:173], v[248:251], v[106:109], v[170:173]\n"
      "ds_read2_b32 v[152:153], v115 offset0:2 offset1:10\n"
      "v_sub_f32 v191, v191, v111\n"
      "v_sub_f32 v208, v208, v111\n"
      "v_mfma_f32_16x16x32_bf16 v[170:173], v[252:255], v[158:161], v[170:173]\n"
      "ds_read_u16 v86, v11 offset:1232\n"
      "v_sub_f32 v146, v146, v111\n"
      "v_sub_f32 v117, v117, v111\n"
      "v_sub_f32 v206, v206, v111\n"
      "v_exp_f32 v191, v191\n"
      "v_sub_f32 v207, v207, v111\n"
      "v_cvt_pk_bf16_f32 v90, v150, 0\n"
      "v_mfma_f32_16x16x32_bf16 v[126:129], v[32:35], v[134:137], 0\n"
      "ds_read2_b32 v[106:107], v115 offset0:0 offset1:1\n"
      "v_exp_f32 v189, v189\n"
      "v_mfma_f32_16x16x32_bf16 v[126:129], v[32:35], v[88:91], v[126:129]\n"
      "ds_read2_b32 v[184:185], v115 offset0:24 offset1:18\n"
      "v_sub_f32 v116, v116, v111\n"
      "v_exp_f32 v188, v188\n"
      "v_exp_f32 v119, v119\n"
      "v_exp_f32 v190, v190\n"
      "v_sub_f32 v209, v209, v111\n"
      "v_sub_f32 v118, v118, v111\n"
      "v_mfma_f32_16x16x32_bf16 v[108:111], v[130:133], v[134:137], 0\n"
      "s_waitcnt lgkmcnt(2)\n"
      "v_alignbyte_b32 v204, v205, v204, v124\n"
      "v_alignbyte_b32 v148, v149, v148, v124\n"
      "v_mfma_f32_16x16x32_bf16 v[108:111], v[76:79], v[88:91], v[108:111]\n"
      "v_exp_f32_e64 v117, v117\n"
      "v_mfma_f32_16x16x32_bf16 v[98:101], v[194:197], v[134:137], 0\n"
      "s_waitcnt lgkmcnt(1)\n"
      "v_alignbyte_b32 v149, v153, v149, v124\n"
      "v_alignbyte_b32 v106, v107, v106, v124\n"
      "v_mfma_f32_16x16x32_bf16 v[98:101], v[80:83], v[88:91], v[98:101]\n"
      "s_waitcnt lgkmcnt(0)\n"
      "v_alignbyte_b32 v107, v152, v107, v124\n"
      "v_exp_f32_e64 v118, v118\n"
      "v_mfma_f32_16x16x32_bf16 v[150:153], v[214:217], v[134:137], 0\n"
      "v_alignbyte_b32 v205, v185, v205, v124\n"
      "v_cvt_pk_bf16_f32 v188, v188, v189\n"
      "v_mfma_f32_16x16x32_bf16 v[150:153], v[84:87], v[88:91], v[150:153]\n"
      "v_cvt_pk_bf16_f32 v189, v190, v191\n"
      "v_alignbyte_b32 v184, v184, v184, v124\n"
      "v_mfma_f32_16x16x32_bf16 v[134:137], v[154:157], v[138:141], v[64:67]\n"
      "v_exp_f32_e64 v116, v116\n"
      "v_mfma_f32_16x16x32_bf16 v[158:161], v[220:223], v[138:141], v[68:71]\n"
      "v_cvt_pk_bf16_f32 v190, v116, v117\n"
      "v_cvt_pk_bf16_f32 v191, v118, v119\n"
      "v_mfma_f32_16x16x32_bf16 v[112:115], v[210:213], v[138:141], v[72:75]\n"
      "v_rcp_f32 v126, v126\n"
      "v_mfma_f32_16x16x32_bf16 v[122:125], v[174:177], v[138:141], v[28:31]\n"
      "v_exp_f32 v207, v207\n"
      "v_mfma_f32_16x16x16_bf16 v[134:137], v[202:203], v[120:121], v[134:137]\n"
      "v_exp_f32 v208, v208\n"
      "v_mul_f32 v151, v126, v151\n"
      "v_mfma_f32_16x16x16_bf16 v[158:161], v[102:103], v[120:121], v[158:161]\n"
      "v_mul_f32 v98, v126, v98\n"
      "v_exp_f32 v209, v209\n"
      "v_mfma_f32_16x16x16_bf16 v[112:115], v[186:187], v[120:121], v[112:115]\n"
      "v_exp_f32 v206, v206\n"
      "v_mul_f32 v111, v126, v111\n"
      "v_mfma_f32_16x16x16_bf16 v[122:125], v[192:193], v[120:121], v[122:125]\n"
      "v_mul_f32 v99, v126, v99\n"
      "v_mul_f32 v108, v126, v108\n"
      "v_cvt_pk_bf16_f32 v92, v206, v207\n"
      "v_mfma_f32_16x16x16_bf16 v[134:137], v[218:219], v[106:107], v[134:137]\n"
      "v_mul_f32 v109, v126, v109\n"
      "v_exp_f32 v146, v146\n"
      "v_mfma_f32_16x16x16_bf16 v[158:161], v[218:219], v[148:149], v[158:161]\n"
      "v_mul_f32 v150, v126, v150\n"
      "v_fma_f32 v120, v162, v162, s52\n"
      "v_cvt_pk_bf16_f32 v93, v208, v209\n"
      "v_mfma_f32_16x16x16_bf16 v[112:115], v[218:219], v[204:205], v[112:115]\n"
      "v_mul_f32 v121, v163, v163\n"
      "v_mul_f32 v110, v126, v110\n"
      "v_cvt_pk_bf16_f32 v108, v108, v109\n"
      "v_mfma_f32_16x16x16_bf16 v[122:125], v[218:219], v[184:185], v[122:125]\n"
      "v_fmac_f32 v120, v164, v164\n"
      "v_cvt_pk_bf16_f32 v109, v110, v111\n"
      "v_mul_f32 v152, v126, v152\n"
      "v_mfma_f32_16x16x16_bf16 v[134:137], v[104:105], v[106:107], v[134:137]\n"
      "ds_read_b128 v[138:141], v2 offset:512\n"
      "v_mul_f32 v153, v126, v153\n"
      "v_mfma_f32_16x16x16_bf16 v[158:161], v[104:105], v[148:149], v[158:161]\n"
      "v_cvt_pk_bf16_f32 v150, v150, v151\n"
      "v_cvt_pk_bf16_f32 v94, v146, 0\n"
      "v_cvt_pk_bf16_f32 v110, v98, v99\n"
      "v_mfma_f32_16x16x16_bf16 v[112:115], v[104:105], v[204:205], v[112:115]\n"
      "ds_read_b128 v[206:209], v2 offset:3584\n"
      "v_cvt_pk_bf16_f32 v151, v152, v153\n"
      "v_mfma_f32_16x16x16_bf16 v[122:125], v[104:105], v[184:185], v[122:125]\n"
      "ds_read_b128 v[146:149], v2 offset:6656\n"
      "v_fmac_f32_e64 v121, v165, v165\n"
      "v_max3_f32 v90, v134, v135, v136\n"
      "v_max3_f32 v90, v90, v160, v161\n"
      "v_max3_f32 v128, v137, v158, v159\n"
      "v_max3_f32 v90, v90, v114, v115\n"
      "v_pk_mul_f32 v[100:101], v[100:101], v[126:127] op_sel_hi:[1,0]\n"
      "v_cvt_pk_bf16_f32 v111, v100, v101\n"
      "v_pk_fma_f32 v[120:121], v[180:181], v[180:181], v[120:121]\n"
      "v_max3_f32 v128, v128, v112, v113\n"
      "v_pk_fma_f32 v[120:121], v[182:183], v[182:183], v[120:121]\n"
      "v_max3_f32 v90, v90, v128, v122\n"
      "v_pk_fma_f32 v[120:121], v[170:171], v[170:171], v[120:121]\n"
      "v_mov_b32 v119, v90\n"
      "v_pk_fma_f32 v[120:121], v[172:173], v[172:173], v[120:121]\n"
      "v_add_f32 v120, v120, v121\n"
      "v_permlane16_swap_b32 v90, v119\n"
      "v_max_f32 v90, v90, v119\n"
      "v_mov_b32 v119, v90\n"
      "s_waitcnt lgkmcnt(1)\n"
      "v_mfma_f32_16x16x32_bf16 v[126:129], a[216:219], v[138:141], 0\n"
      "v_permlane32_swap_b32 v90, v119\n"
      "v_mfma_f32_16x16x32_bf16 v[126:129], a[220:223], v[206:209], v[126:129]\n"
      "s_waitcnt lgkmcnt(0)\n"
      "v_max_f32 v90, v90, v119\n"
      "v_sub_f32 v160, v160, v90\n"
      "v_mfma_f32_16x16x32_bf16 v[126:129], a[224:227], v[146:149], v[126:129]\n"
      "ds_write_b128 v3, v[108:111] offset:18432\n"
      "v_mfma_f32_16x16x32_bf16 v[116:119], a[240:243], v[138:141], 0\n"
      "v_sub_f32 v136, v136, v90\n"
      "v_sub_f32 v134, v134, v90\n"
      "v_mfma_f32_16x16x32_bf16 v[116:119], a[244:247], v[206:209], v[116:119]\n"
      "v_exp_f32 v136, v136\n"
      "v_mfma_f32_16x16x32_bf16 v[116:119], a[248:251], v[146:149], v[116:119]\n"
      "v_sub_f32 v158, v158, v90\n"
      "v_sub_f32 v135, v135, v90\n"
      "v_mfma_f32_16x16x32_bf16 v[108:111], v[232:235], v[138:141], 0\n"
      "v_sub_f32 v137, v137, v90\n"
      "v_exp_f32 v158, v158\n"
      "v_mfma_f32_16x16x32_bf16 v[108:111], v[236:239], v[206:209], v[108:111]\n"
      "v_exp_f32 v137, v137\n"
      "v_mfma_f32_16x16x32_bf16 v[108:111], v[240:243], v[146:149], v[108:111]\n"
      "v_exp_f32 v134, v134\n"
      "v_mfma_f32_16x16x32_bf16 v[98:101], v[32:35], v[188:191], 0\n"
      "v_exp_f32 v135, v135\n"
      "v_mfma_f32_16x16x32_bf16 v[98:101], v[32:35], v[92:95], v[98:101]\n"
      "ds_read_b128 v[146:149], v10 offset:1168\n"
      "v_sub_f32_e64 v159, v159, v90\n"
      "v_mfma_f32_16x16x32_bf16 v[206:209], v[130:133], v[188:191], 0\n"
      "v_cvt_pk_bf16_f32 v134, v134, v135\n"
      "v_cvt_pk_bf16_f32 v135, v136, v137\n"
      "v_mfma_f32_16x16x32_bf16 v[206:209], v[76:79], v[92:95], v[206:209]\n"
      "ds_read_b64 v[88:89], v10 offset:1184\n"
      "v_sub_f32 v115, v115, v90\n"
      "v_mfma_f32_16x16x32_bf16 v[138:141], v[194:197], v[188:191], 0\n"
      "v_exp_f32 v159, v159\n"
      "v_mfma_f32_16x16x32_bf16 v[138:141], v[80:83], v[92:95], v[138:141]\n"
      "v_sub_f32 v161, v161, v90\n"
      "v_cvt_pk_bf16_f32 v136, v158, v159\n"
      "v_mfma_f32_16x16x32_bf16 v[104:107], v[214:217], v[188:191], 0\n"
      "v_rcp_f32 v98, v98\n"
      "v_mfma_f32_16x16x32_bf16 v[104:107], v[84:87], v[92:95], v[104:107]\n"
      "s_waitcnt lgkmcnt(1)\n"
      "v_exp_f32 v161, v161\n"
      "v_mfma_f32_16x16x32_bf16 v[188:191], v[146:149], v[154:157], v[30:33]\n"
      "v_exp_f32_e64 v160, v160\n"
      "v_mfma_f32_16x16x32_bf16 v[156:159], v[146:149], v[220:223], v[30:33]\n"
      "s_waitcnt lgkmcnt(0)\n"
      "v_cvt_pk_bf16_f32 v137, v160, v161\n"
      "v_sub_f32 v113, v113, v90\n"
      "v_mfma_f32_16x16x32_bf16 v[218:221], v[146:149], v[210:213], v[30:33]\n"
      "ds_read_b128 v[210:213], v2 offset:9728\n"
      "v_sub_f32 v112, v112, v90\n"
      "v_mfma_f32_16x16x32_bf16 v[152:155], v[146:149], v[174:177], v[30:33]\n"
      "ds_read_b128 v[174:177], v2 offset:12800\n"
      "v_sub_f32 v114, v114, v90\n"
      "v_mfma_f32_16x16x16_bf16 v[188:191], v[88:89], v[202:203], v[188:191]\n"
      "ds_read_b128 v[202:205], v2 offset:15872\n"
      "v_sub_f32 v122, v122, v90\n"
      "v_mfma_f32_16x16x16_bf16 v[156:159], v[88:89], v[102:103], v[156:159]\n"
      "v_exp_f32 v113, v113\n"
      "v_exp_f32 v115, v115\n"
      "v_mfma_f32_16x16x16_bf16 v[218:221], v[88:89], v[186:187], v[218:221]\n"
      "v_exp_f32 v114, v114\n"
      "v_exp_f32_e64 v112, v112\n"
      "v_exp_f32_e64 v122, v122\n"
      "v_pk_mul_f32 v[104:105], v[104:105], v[98:99] op_sel_hi:[1,0]\n"
      "v_pk_mul_f32 v[138:139], v[138:139], v[98:99] op_sel_hi:[1,0]\n"
      "v_pk_mul_f32 v[140:141], v[140:141], v[98:99] op_sel_hi:[1,0]\n"
      "v_pk_mul_f32 v[208:209], v[208:209], v[98:99] op_sel_hi:[1,0]\n"
      "v_pk_mul_f32 v[206:207], v[206:207], v[98:99] op_sel_hi:[1,0]\n"
      "v_mfma_f32_16x16x16_bf16 v[152:155], v[88:89], v[192:193], v[152:155]\n"
      "v_cndmask_b32 v188, v188, v156, s[58:59]\n"
      "v_cndmask_b32 v188, v188, v218, s[60:61]\n"
      "s_waitcnt lgkmcnt(1)\n"
      "v_mfma_f32_16x16x32_bf16 v[126:129], a[228:231], v[210:213], v[126:129]\n"
      "s_waitcnt lgkmcnt(0)\n"
      "v_cvt_pk_bf16_f32 v88, v112, v113\n"
      "v_lshlrev_b32_e64 v39, 16, v39\n"
      "v_mfma_f32_16x16x32_bf16 v[126:129], a[232:235], v[174:177], v[126:129]\n"
      "v_cndmask_b32 v188, v188, v152, s[62:63]\n"
      "v_cvt_pk_bf16_f32 v89, v114, v115\n"
      "v_mfma_f32_16x16x32_bf16 v[126:129], a[236:239], v[202:205], v[126:129]\n"
      "v_fmac_f32_e64 v188, s53, v39\n"
      "v_cvt_pk_bf16_f32 v206, v206, v207\n"
      "v_mfma_f32_16x16x32_bf16 v[116:119], a[252:255], v[210:213], v[116:119]\n"
      "ds_write_b64 v4, v[150:151] offset:18432\n"
      "v_mfma_f32_16x16x32_bf16 v[116:119], v[224:227], v[174:177], v[116:119]\n"
      "v_max_f32_dpp v102, v188, v188 row_ror:8 row_mask:0xf bank_mask:0xf\n"
      "v_cvt_pk_bf16_f32 v207, v208, v209\n"
      "v_mfma_f32_16x16x32_bf16 v[116:119], v[228:231], v[202:205], v[116:119]\n"
      "v_cvt_pk_bf16_f32 v90, v122, 0\n"
      "v_max_f32_dpp v102, v102, v102 row_ror:4 row_mask:0xf bank_mask:0xf\n"
      "v_mfma_f32_16x16x32_bf16 v[108:111], v[244:247], v[210:213], v[108:111]\n"
      "v_mul_f32_e64 v191, v127, v127\n"
      "v_max_f32_dpp v102, v102, v102 row_ror:2 row_mask:0xf bank_mask:0xf\n"
      "v_mfma_f32_16x16x32_bf16 v[108:111], v[248:251], v[174:177], v[108:111]\n"
      "v_cvt_pk_bf16_f32 v209, v140, v141\n"
      "v_max_f32_dpp v102, v102, v102 row_ror:1 row_mask:0xf bank_mask:0xf\n"
      "v_mfma_f32_16x16x32_bf16 v[108:111], v[252:255], v[202:205], v[108:111]\n"
      "v_fma_f32 v190, v126, v126, s52\n"
      "v_max_f32_dpp v102, v102, v102 row_bcast:15 row_mask:0xa bank_mask:0xf\n"
      "v_mfma_f32_16x16x32_bf16 v[218:221], v[32:35], v[134:137], 0\n"
      "s_nop 0\n"
      "v_max_f32_dpp v102, v102, v102 row_bcast:31 row_mask:0xc bank_mask:0xf\n"
      "v_cvt_pk_bf16_f32 v208, v138, v139\n"
      "v_mfma_f32_16x16x32_bf16 v[218:221], v[32:35], v[88:91], v[218:221]\n"
      "v_cvt_pk_bf16_f32 v104, v104, v105\n"
      "v_readlane_b32 s72, v102, 63\n"
      "v_mfma_f32_16x16x32_bf16 v[174:177], v[130:133], v[134:137], 0\n"
      "ds_write_b128 v3, v[206:209] offset:18688\n"
      "v_subrev_f32 v188, s72, v188\n"
      "v_pk_fma_f32 v[190:191], v[128:129], v[128:129], v[190:191]\n"
      "v_mfma_f32_16x16x32_bf16 v[174:177], v[76:79], v[88:91], v[174:177]\n"
      "s_nop 0\n"
      "v_rcp_f32 v218, v218\n"
      "v_mfma_f32_16x16x32_bf16 v[210:213], v[194:197], v[134:137], 0\n"
      "v_exp_f32 v188, v188\n"
      "v_mfma_f32_16x16x32_bf16 v[210:213], v[80:83], v[88:91], v[210:213]\n"
      "v_permlane16_swap_b32 v96, v120\n"
      "v_add_f32_e64 v96, v96, v120\n"
      "v_add_u32 v10, 2336, v10\n"
      "v_cvt_pk_bf16_f32 v188, v188, v188\n"
      "v_pk_mul_f32 v[174:175], v[174:175], v[218:219] op_sel_hi:[1,0]\n"
      "v_pk_mul_f32 v[176:177], v[176:177], v[218:219] op_sel_hi:[1,0]\n"
      "v_pk_fma_f32 v[190:191], v[116:117], v[116:117], v[190:191]\n"
      "v_pk_fma_f32 v[190:191], v[118:119], v[118:119], v[190:191]\n"
      "v_pk_mul_f32 v[210:211], v[210:211], v[218:219] op_sel_hi:[1,0]\n"
      "v_pk_fma_f32 v[190:191], v[108:109], v[108:109], v[190:191]\n"
      "v_pk_fma_f32 v[190:191], v[110:111], v[110:111], v[190:191]\n"
      "v_cvt_pk_bf16_f32 v174, v174, v175\n"
      "v_cvt_pk_bf16_f32 v175, v176, v177\n"
      "v_pk_mul_f32 v[212:213], v[212:213], v[218:219] op_sel_hi:[1,0]\n"
      "v_cvt_pk_bf16_f32 v177, v212, v213\n"
      "v_mfma_f32_16x16x32_bf16 v[100:103], v[214:217], v[134:137], 0\n"
      "ds_write_b16 v13, v188\n"
      "v_cvt_pk_bf16_f32 v176, v210, v211\n"
      "v_mfma_f32_16x16x32_bf16 v[100:103], v[84:87], v[88:91], v[100:103]\n"
      "ds_read_b128 v[210:213], v14 offset:64\n"
      "ds_write_b128 v3, v[174:177] offset:18944\n"
      "v_add_f32 v190, v190, v191\n"
      "v_mov_b32 v120, v190\n"
      "v_pk_mul_f32 v[106:107], v[106:107], v[98:99] op_sel_hi:[1,0]\n"
      "v_cvt_pk_bf16_f32 v105, v106, v107\n"
      "v_permlane16_swap_b32 v190, v120\n"
      "v_add_f32_e64 v190, v190, v120\n"
      "v_pk_mul_f32 v[102:103], v[102:103], v[218:219] op_sel_hi:[1,0]\n"
      "s_and_b32 s47, s47, -4\n"
      "ds_write_b64 v4, v[104:105] offset:18688\n"
      "v_permlane32_swap_b32 v96, v190\n"
      "v_add_f32_e64 v96, v96, v190\n"
      "s_add_u32 s42, s42, 1\n"
      "ds_read_b128 v[174:177], v14\n"
      "ds_write_b32 v31, v96 offset:1024\n"
      "v_pk_mul_f32 v[100:101], v[100:101], v[218:219] op_sel_hi:[1,0]\n"
      "v_cvt_pk_bf16_f32 v100, v100, v101\n"
      "v_cvt_pk_bf16_f32 v101, v102, v103\n"
      "s_add_u32 s43, s43, 1\n"
      "ds_write_b64 v4, v[100:101] offset:18944\n"
      "s_waitcnt lgkmcnt(2)\n"
      "v_mfma_f32_16x16x32_bf16 v[188:191], v[174:177], v[32:35], 0\n"
      "v_mfma_f32_16x16x32_bf16 v[188:191], v[210:213], v[32:35], v[188:191]\n"
      "v_add_u32 v11, 2336, v11\n"
      "s_waitcnt lgkmcnt(0)\n"
      "s_barrier\n"
      "s_cbranch_vccz L_sel0_%=\n"
      "L_disp0_%=:\n"
      "s_cmp_lt_u32 s95, 0x40000000\n"
      "s_cbranch_scc1 L_nofix_18_%=\n"
      "s_cmp_eq_u32 s95, -2\n"
      "s_cbranch_scc1 L_tail0_%=\n"
      "s_nop 2\n"
      "v_lshlrev_b32 v218, 3, v36\n"
      "s_add_u32 s72, s98, L_cold_%=-L_ipf0_%=\n"
      "s_addc_u32 s73, s99, 0\n"
      "s_mov_b32 s74, L_cend_%=-L_cold_%=\n"
      "s_mov_b32 s75, 0x20000\n"
      "s_mov_b32 m0, 161792\n"
      "s_nop 0\n"
      "buffer_load_dwordx4 v218, s[72:75], 0 offen  lds\n"
      "s_lshr_b32 s92, s45, 30\n"
      "s_or_b32 s92, s92, s66\n"
      "s_cbranch_scc0 L_nf_19_%=\n"
      "s_bitcmp1_b32 s45, 31\n"
      "s_cselect_b32 s75, 100270464, 0\n"
      "s_bitcmp1_b32 s45, 30\n"
      "s_cselect_b32 s76, 196224, 0\n"
      "s_and_b32 s74, s45, 0x3fffffff\n"
      "s_cmp_ge_u32 s45, 0x40000000\n"
      "s_cselect_b32 s74, s74, 0\n"
      "s_cselect_b32 s45, 0, s45\n"
      "s_sub_u32 s74, s74, s66\n"
      "s_add_u32 s66, s66, s74\n"
      "s_sub_u32 s75, s75, s67\n"
      "s_add_u32 s67, s67, s75\n"
      "s_sub_u32 s76, s76, s68\n"
      "s_add_u32 s68, s68, s76\n"
      "v_add_u32 v16, s74, v16\n"
      "v_add_u32 v17, s74, v17\n"
      "v_add_u32 v18, s74, v18\n"
      "v_add_u32 v19, s74, v19\n"
      "v_add_u32 v15, s74, v15\n"
      "s_cmp_eq_u32 s75, 0\n"
      "s_cbranch_scc1 L_ax_20_%=\n"
      "v_bfe_i32 v218, v27, 0, 1\n"
      "v_and_b32 v218, s75, v218\n"
      "v_sub_u32 v16, v16, v218\n"
      "v_bfe_i32 v218, v27, 2, 1\n"
      "v_and_b32 v218, s75, v218\n"
      "v_sub_u32 v17, v17, v218\n"
      "v_bfe_i32 v218, v27, 4, 1\n"
      "v_and_b32 v218, s75, v218\n"
      "v_sub_u32 v18, v18, v218\n"
      "v_bfe_i32 v218, v27, 6, 1\n"
      "v_and_b32 v218, s75, v218\n"
      "v_sub_u32 v19, v19, v218\n"
      "v_bfe_i32 v218, v27, 8, 1\n"
      "v_and_b32 v218, s75, v218\n"
      "v_sub_u32 v15, v15, v218\n"
      "L_ax_20_%=:\n"
      "s_cmp_eq_u32 s76, 0\n"
      "s_cbranch_scc1 L_ax_21_%=\n"
      "v_bfe_i32 v218, v27, 1, 1\n"
      "v_and_b32 v218, s76, v218\n"
      "v_sub_u32 v16, v16, v218\n"
      "v_bfe_i32 v218, v27, 3, 1\n"
      "v_and_b32 v218, s76, v218\n"
      "v_sub_u32 v17, v17, v218\n"
      "v_bfe_i32 v218, v27, 5, 1\n"
      "v_and_b32 v218, s76, v218\n"
      "v_sub_u32 v18, v18, v218\n"
      "v_bfe_i32 v218, v27, 7, 1\n"
      "v_and_b32 v218, s76, v218\n"
      "v_sub_u32 v19, v19, v218\n"
      "v_bfe_i32 v218, v27, 9, 1\n"
      "v_and_b32 v218, s76, v218\n"
      "v_sub_u32 v15, v15, v218\n"
      "L_ax_21_%=:\n"
      "s_or_b32 s48, s66, s69\n"
      "s_cmp_lg_u32 s48, 0\n"
      "s_cselect_b32 s48, 0x40000000, 0\n"
      "L_nf_19_%=:\n"
      "s_lshr_b32 s92, s46, 30\n"
      "s_or_b32 s92, s92, s69\n"
      "s_cbranch_scc0 L_nf_22_%=\n"
      "s_bitcmp1_b32 s46, 31\n"
      "s_cselect_b32 s75, 100270464, 0\n"
      "s_bitcmp1_b32 s46, 30\n"
      "s_cselect_b32 s76, 196224, 0\n"
      "s_and_b32 s74, s46, 0x3fffffff\n"
      "s_cmp_ge_u32 s46, 0x40000000\n"
      "s_cselect_b32 s74, s74, 0\n"
      "s_cselect_b32 s46, 0, s46\n"
      "s_sub_u32 s74, s74, s69\n"
      "s_add_u32 s69, s69, s74\n"
      "s_sub_u32 s75, s75, s70\n"
      "s_add_u32 s70, s70, s75\n"
      "s_sub_u32 s76, s76, s71\n"
      "s_add_u32 s71, s71, s76\n"
      "v_add_u32 v20, s74, v20\n"
      "v_add_u32 v21, s74, v21\n"
      "v_add_u32 v22, s74, v22\n"
      "v_add_u32 v23, s74, v23\n"
      "v_add_u32 v25, s74, v25\n"
      "s_cmp_eq_u32 s75, 0\n"
      "s_cbranch_scc1 L_ax_23_%=\n"
      "v_bfe_i32 v218, v27, 10, 1\n"
      "v_and_b32 v218, s75, v218\n"
      "v_sub_u32 v20, v20, v218\n"
      "v_bfe_i32 v218, v27, 12, 1\n"
      "v_and_b32 v218, s75, v218\n"
      "v_sub_u32 v21, v21, v218\n"
      "v_bfe_i32 v218, v27, 14, 1\n"
      "v_and_b32 v218, s75, v218\n"
      "v_sub_u32 v22, v22, v218\n"
      "v_bfe_i32 v218, v27, 16, 1\n"
      "v_and_b32 v218, s75, v218\n"
      "v_sub_u32 v23, v23, v218\n"
      "v_bfe_i32 v218, v27, 18, 1\n"
      "v_and_b32 v218, s75, v218\n"
      "v_sub_u32 v25, v25, v218\n"
      "L_ax_23_%=:\n"
      "s_cmp_eq_u32 s76, 0\n"
      "s_cbranch_scc1 L_ax_24_%=\n"
      "v_bfe_i32 v218, v27, 11, 1\n"
      "v_and_b32 v218, s76, v218\n"
      "v_sub_u32 v20, v20, v218\n"
      "v_bfe_i32 v218, v27, 13, 1\n"
      "v_and_b32 v218, s76, v218\n"
      "v_sub_u32 v21, v21, v218\n"
      "v_bfe_i32 v218, v27, 15, 1\n"
      "v_and_b32 v218, s76, v218\n"
      "v_sub_u32 v22, v22, v218\n"
      "v_bfe_i32 v218, v27, 17, 1\n"
      "v_and_b32 v218, s76, v218\n"
      "v_sub_u32 v23, v23, v218\n"
      "v_bfe_i32 v218, v27, 19, 1\n"
      "v_and_b32_e64 v218, s76, v218\n"
      "v_sub_u32_e64 v25, v25, v218\n"
      "L_ax_24_%=:\n"
      "s_or_b32 s48, s66, s69\n"
      "s_cmp_lg_u32 s48, 0\n"
      "s_cselect_b32 s48, 0x40000000, 0\n"
      "L_nf_22_%=:\n"
      "s_sub_u32 s92, s42, 1\n"
      "s_cmp_gt_u32 s92, s44\n"
      "s_cbranch_scc1 L_tail0_%=\n"
      "L_nofix_18_%=:\n"
      "s_cmp_lg_u64 s[56:57], 0\n"
      "s_cbranch_scc1 L_slow0_%=\n"
      "L_sel0_%=:\n"
      "s_cmp_lg_u64 s[54:55], 0\n"
      "s_cbranch_scc1 L_max0_%=\n"
      "s_branch L_loop0_%=\n"
      "L_slow0_%=:\n"
      "s_nop 4\n"
      "v_mfma_f32_16x16x32_bf16 v[218:221], v[174:177], v[130:133], 0\n"
      "ds_read_b128 v[156:159], v0\n"
      "ds_read_b128 v[112:115], v9 offset:1024\n"
      "v_mfma_f32_16x16x32_bf16 v[218:221], v[210:213], v[76:79], v[218:221]\n"
      "ds_read_b128 v[104:107], v0 offset:6144\n"
      "v_readlane_b32 s85, v37, s42\n"
      "v_mfma_f32_16x16x32_bf16 v[134:137], v[174:177], v[194:197], 0\n"
      "ds_read_b128 v[206:209], v1\n"
      "v_mov_b32 v124, 0x3fb9\n"
      "v_mfma_f32_16x16x32_bf16 v[134:137], v[210:213], v[80:83], v[134:137]\n"
      "ds_read_b128 v[138:141], v1 offset:2048\n"
      "v_subrev_u32 v132, 115456, v9\n"
      "v_mfma_f32_16x16x32_bf16 v[100:103], v[174:177], v[214:217], 0\n"
      "s_add_u32 s72, s42, 1\n"
      "v_readlane_b32 s84, v37, s43\n"
      "v_readlane_b32 s77, v38, s72\n"
      "s_mov_b32 m0, 110592\n"
      "v_mfma_f32_16x16x32_bf16 v[100:103], v[210:213], v[84:87], v[100:103]\n"
      "s_and_b32 s73, s77, -16\n"
      "v_mov_b32_dpp v188, v188 quad_perm:[0,0,0,0] row_mask:0xf bank_mask:0xf\n"
      "s_waitcnt lgkmcnt(4)\n"
      "v_mfma_f32_16x16x32_bf16 v[210:213], a[0:3], v[156:159], 0\n"
      "buffer_load_dwordx4 v132, s[28:31], s73 offen  lds\n"
      "v_mfma_f32_16x16x32_bf16 v[190:193], a[24:27], v[156:159], 0\n"
      "s_waitcnt lgkmcnt(3)\n"
      "v_rcp_f32 v188, v188\n"
      "v_add_f32 v114, v114, v115\n"
      "v_mfma_f32_16x16x32_bf16 v[174:177], a[48:51], v[156:159], 0\n"
      "v_add_f32 v112, v112, v113\n"
      "v_add_f32 v112, v112, v114\n"
      "v_mfma_f32_16x16x32_bf16 v[150:153], a[72:75], v[156:159], 0\n"
      "s_waitcnt lgkmcnt(0)\n"
      "v_rsq_f32 v112, v112\n"
      "v_mfma_f32_16x16x32_bf16 v[96:99], a[96:99], v[156:159], 0\n"
      "s_add_u32 s73, s73, 1024\n"
      "v_readlane_b32 s86, v38, s43\n"
      "v_mul_f32 v112, 0x415db3d7, v112\n"
      "v_mfma_f32_16x16x32_bf16 v[146:149], a[120:123], v[156:159], 0\n"
      "ds_read_b128 v[202:205], v1 offset:6144\n"
      "v_mov_b32_e64 v114, v112\n"
      "v_mfma_f32_16x16x32_bf16 v[210:213], a[4:7], v[206:209], v[210:213]\n"
      "s_mov_b32 m0, 111616\n"
      "v_permlane32_swap_b32_e64 v112, v114\n"
      "v_mfma_f32_16x16x32_bf16 v[210:213], a[8:11], v[104:107], v[210:213]\n"
      "buffer_load_dwordx4 v132, s[28:31], s73 offen  lds\n"
      "v_mfma_f32_16x16x32_bf16 v[190:193], a[28:31], v[206:209], v[190:193]\n"
      "ds_read_b128 v[120:123], v1 offset:12288\n"
      "v_mov_b32_e64 v113, v112\n"
      "v_mfma_f32_16x16x32_bf16 v[190:193], a[32:35], v[104:107], v[190:193]\n"
      "ds_read_b128 v[184:187], v0 offset:12288\n"
      "v_mul_f32_e64 v119, v114, v119\n"
      "v_mfma_f32_16x16x32_bf16 v[174:177], a[52:55], v[206:209], v[174:177]\n"
      "s_mov_b32 m0, 112640\n"
      "v_permlane16_swap_b32 v112, v113\n"
      "v_mfma_f32_16x16x32_bf16 v[174:177], a[56:59], v[104:107], v[174:177]\n"
      "s_add_u32 s73, s73, 1024\n"
      "v_mul_f32 v163, v113, v163\n"
      "v_mul_f32 v198, v112, v198\n"
      "v_mfma_f32_16x16x32_bf16 v[150:153], a[76:79], v[206:209], v[150:153]\n"
      "buffer_load_dwordx4 v132, s[28:31], s73 offen  lds\n"
      "v_mfma_f32_16x16x32_bf16 v[150:153], a[80:83], v[104:107], v[150:153]\n"
      "s_and_b32 s77, s77, 15\n"
      "v_mul_f32 v169, v112, v169\n"
      "v_mul_f32 v170, v113, v170\n"
      "v_mfma_f32_16x16x32_bf16 v[96:99], a[100:103], v[206:209], v[96:99]\n"
      "v_mul_f32 v166, v112, v166\n"
      "v_mul_f32 v201, v112, v201\n"
      "v_mfma_f32_16x16x32_bf16 v[96:99], a[104:107], v[104:107], v[96:99]\n"
      "s_waitcnt lgkmcnt(0)\n"
      "v_mul_f32 v128, v114, v128\n"
      "v_mul_f32 v167, v112, v167\n"
      "v_mfma_f32_16x16x32_bf16 v[146:149], a[124:127], v[206:209], v[146:149]\n"
      "s_mov_b32 m0, 113664\n"
      "v_mul_f32 v168, v112, v168\n"
      "v_cvt_pk_bf16_f32 v166, v166, v167\n"
      "v_mfma_f32_16x16x32_bf16 v[146:149], a[128:131], v[104:107], v[146:149]\n"
      "s_add_u32 s73, s73, 1024\n"
      "v_cvt_pk_bf16_f32 v167, v168, v169\n"
      "v_mul_f32 v199, v112, v199\n"
      "v_mfma_f32_16x16x32_bf16 v[210:213], a[12:15], v[202:205], v[210:213]\n"
      "buffer_load_dwordx4 v132, s[28:31], s73 offen  lds\n"
      "v_mfma_f32_16x16x32_bf16 v[210:213], a[16:19], v[184:187], v[210:213]\n"
      "v_mul_f32 v180, v113, v180\n"
      "v_mul_f32_e64 v162, v113, v162\n"
      "v_mfma_f32_16x16x32_bf16 v[210:213], a[20:23], v[120:123], v[210:213]\n"
      "s_mov_b32 m0, 114688\n"
      "v_mul_f32_e64 v200, v112, v200\n"
      "v_cvt_pk_bf16_f32 v168, v198, v199\n"
      "v_mfma_f32_16x16x32_bf16 v[190:193], a[36:39], v[202:205], v[190:193]\n"
      "s_add_u32 s73, s73, 1024\n"
      "v_cvt_pk_bf16_f32 v169, v200, v201\n"
      "v_mul_f32 v181, v113, v181\n"
      "v_mfma_f32_16x16x32_bf16 v[190:193], a[40:43], v[184:187], v[190:193]\n"
      "v_cndmask_b32 v218, v218, v134, s[58:59]\n"
      "v_cvt_pk_bf16_f32 v162, v162, v163\n"
      "v_mfma_f32_16x16x32_bf16 v[190:193], a[44:47], v[120:123], v[190:193]\n"
      "s_mov_b32 exec_hi, 0xffff\n"
      "buffer_load_dwordx4 v132, s[28:31], s73 offen  lds\n"
      "s_mov_b32 exec_hi, -1\n"
      "v_pk_mul_f32 v[126:127], v[126:127], v[114:115] op_sel_hi:[1,0]\n"
      "v_pk_mul_f32 v[110:111], v[110:111], v[114:115] op_sel_hi:[1,0]\n"
      "v_pk_mul_f32 v[164:165], v[164:165], v[112:113] op_sel:[0,1]\n"
      "v_mfma_f32_16x16x32_bf16 v[174:177], a[60:63], v[202:205], v[174:177]\n"
      "ds_write_b128 v5, v[166:169]\n"
      "v_pk_mul_f32 v[142:143], v[142:143], v[112:113] op_sel_hi:[1,0]\n"
      "v_pk_mul_f32 v[144:145], v[144:145], v[112:113] op_sel_hi:[1,0]\n"
      "v_mfma_f32_16x16x32_bf16 v[174:177], a[64:67], v[184:187], v[174:177]\n"
      "v_cvt_pk_bf16_f32 v142, v142, v143\n"
      "v_mul_f32_e64 v182, v113, v182\n"
      "v_mfma_f32_16x16x32_bf16 v[174:177], a[68:71], v[120:123], v[174:177]\n"
      "s_add_u32 m0, s64, 18432\n"
      "v_cvt_pk_bf16_f32 v143, v144, v145\n"
      "v_cvt_pk_bf16_f32 v163, v164, v165\n"
      "v_mfma_f32_16x16x32_bf16 v[150:153], a[84:87], v[202:205], v[150:153]\n"
      "ds_write_b64 v6, v[142:143]\n"
      "v_mfma_f32_16x16x32_bf16 v[150:153], a[88:91], v[184:187], v[150:153]\n"
      "v_fma_f32 v77, v210, v210, s51\n"
      "v_mul_f32_e64 v172, v113, v172\n"
      "v_mfma_f32_16x16x32_bf16 v[150:153], a[92:95], v[120:123], v[150:153]\n"
      "buffer_load_dwordx4 v16, s[16:19], s45 offen  lds\n"
      "v_mfma_f32_16x16x32_bf16 v[96:99], a[108:111], v[202:205], v[96:99]\n"
      "s_or_b32 s95, s84, s85\n"
      "v_cvt_pk_bf16_f32 v164, v180, v181\n"
      "v_mul_f32 v117, v114, v117\n"
      "v_mfma_f32_16x16x32_bf16 v[96:99], a[112:115], v[184:187], v[96:99]\n"
      "s_add_u32 m0, s64, 22528\n"
      "v_mul_f32 v173, v113, v173\n"
      "v_fma_f32 v81, v150, v150, s51\n"
      "v_mfma_f32_16x16x32_bf16 v[96:99], a[116:119], v[120:123], v[96:99]\n"
      "v_mul_f32 v183, v113, v183\n"
      "v_mul_f32_e64 v171, v113, v171\n"
      "v_mfma_f32_16x16x32_bf16 v[146:149], a[132:135], v[202:205], v[146:149]\n"
      "ds_read_b128 v[166:169], v0 offset:14336\n"
      "v_cvt_pk_bf16_f32 v165, v182, v183\n"
      "v_mfma_f32_16x16x32_bf16 v[146:149], a[136:139], v[184:187], v[146:149]\n"
      "buffer_load_dwordx4 v17, s[16:19], s45 offen  lds\n"
      "v_mfma_f32_16x16x32_bf16 v[146:149], a[140:143], v[120:123], v[146:149]\n"
      "ds_read_b128 v[220:223], v0 offset:10240\n"
      "v_mul_f32 v129, v114, v129\n"
      "v_mfma_f32_16x16x32_bf16 v[130:133], v[156:159], a[144:147], 0\n"
      "s_add_u32 m0, s64, 26624\n"
      "v_cvt_pk_bf16_f32 v170, v170, v171\n"
      "v_mul_f32 v116, v114, v116\n"
      "v_mfma_f32_16x16x32_bf16 v[130:133], v[206:209], a[148:151], v[130:133]\n"
      "ds_write_b128 v5, v[162:165] offset:6144\n"
      "v_mfma_f32_16x16x32_bf16 v[130:133], v[104:107], a[152:155], v[130:133]\n"
      "buffer_load_dwordx4 v18, s[16:19], s45 offen  lds\n"
      "v_fmac_f32_e64 v81, v151, v151\n"
      "v_mfma_f32_16x16x32_bf16 v[130:133], v[202:205], a[156:159], v[130:133]\n"
      "ds_read_b128 v[198:201], v0 offset:2048\n"
      "v_cvt_pk_bf16_f32 v171, v172, v173\n"
      "v_mfma_f32_16x16x32_bf16 v[130:133], v[184:187], a[160:163], v[130:133]\n"
      "s_add_u32 m0, s64, 30720\n"
      "v_mul_f32 v109, v114, v109\n"
      "v_mov_b32 v125, 0\n"
      "v_fmac_f32_e64 v77, v211, v211\n"
      "v_mfma_f32_16x16x32_bf16 v[130:133], v[120:123], a[164:167], v[130:133]\n"
      "ds_write_b64 v6, v[170:171] offset:6144\n"
      "v_mfma_f32_16x16x32_bf16 v[194:197], v[156:159], a[168:171], 0\n"
      "buffer_load_dwordx4 v19, s[16:19], s45 offen  lds\n"
      "v_mfma_f32_16x16x32_bf16 v[194:197], v[206:209], a[172:175], v[194:197]\n"
      "v_cvt_pk_bf16_f32 v126, v126, v127\n"
      "v_mul_f32_e64 v118, v114, v118\n"
      "v_mfma_f32_16x16x32_bf16 v[194:197], v[104:107], a[176:179], v[194:197]\n"
      "s_add_u32 m0, s65, 34816\n"
      "v_fmac_f32 v77, v212, v212\n"
      "v_mul_f32 v108, v114, v108\n"
      "v_mfma_f32_16x16x32_bf16 v[194:197], v[202:205], a[180:183], v[194:197]\n"
      "v_fmac_f32 v77, v213, v213\n"
      "v_fmac_f32 v81, v152, v152\n"
      "v_mfma_f32_16x16x32_bf16 v[194:197], v[184:187], a[184:187], v[194:197]\n"
      "ds_read_b128 v[134:137], v0 offset:8192\n"
      "v_cvt_pk_bf16_f32 v127, v128, v129\n"
      "v_mfma_f32_16x16x32_bf16 v[194:197], v[120:123], a[188:191], v[194:197]\n"
      "buffer_load_dwordx4 v15, s[16:19], s45 offen  lds\n"
      "v_mfma_f32_16x16x32_bf16 v[214:217], v[156:159], a[192:195], 0\n"
      "s_or_b32 s95, s95, s48\n"
      "v_cvt_pk_bf16_f32 v129, v118, v119\n"
      "v_cvt_pk_bf16_f32 v128, v116, v117\n"
      "v_mfma_f32_16x16x32_bf16 v[214:217], v[206:209], a[196:199], v[214:217]\n"
      "s_waitcnt lgkmcnt(1)\n"
      "v_fmac_f32 v81, v153, v153\n"
      "v_fmac_f32 v77, v190, v190\n"
      "v_mfma_f32_16x16x32_bf16 v[214:217], v[104:107], a[200:203], v[214:217]\n"
      "ds_read_b128 v[170:173], v1 offset:8192\n"
      "v_fmac_f32_e64 v77, v191, v191\n"
      "v_mfma_f32_16x16x32_bf16 v[214:217], v[202:205], a[204:207], v[214:217]\n"
      "ds_read_b128 v[116:119], v1 offset:14336\n"
      "v_cvt_pk_bf16_f32 v194, v194, v195\n"
      "v_mfma_f32_16x16x32_bf16 v[214:217], v[184:187], a[208:211], v[214:217]\n"
      "buffer_load_dwordx4 v[206:209], v36, s[28:31], s47 offen\n"
      "v_mfma_f32_16x16x32_bf16 v[214:217], v[120:123], a[212:215], v[214:217]\n"
      "ds_write_b128 v5, v[126:129] offset:12288\n"
      "v_mfma_f32_16x16x32_bf16 v[104:107], a[0:3], v[198:201], 0\n"
      "s_add_u32 s73, s73, 1024\n"
      "v_fmac_f32 v77, v192, v192\n"
      "v_fmac_f32 v81, v96, v96\n"
      "v_mfma_f32_16x16x32_bf16 v[104:107], a[4:7], v[138:141], v[104:107]\n"
      "s_waitcnt lgkmcnt(2)\n"
      "v_fmac_f32 v77, v193, v193\n"
      "v_fmac_f32_e64 v77, v174, v174\n"
      "v_mfma_f32_16x16x32_bf16 v[104:107], a[8:11], v[134:137], v[104:107]\n"
      "buffer_load_dword v78, v29, s[28:31], s47 offen\n"
      "v_mfma_f32_16x16x32_bf16 v[104:107], a[12:15], v[170:173], v[104:107]\n"
      "v_cvt_pk_bf16_f32 v108, v108, v109\n"
      "v_cvt_pk_bf16_f32 v130, v130, v131\n"
      "v_mfma_f32_16x16x32_bf16 v[104:107], a[16:19], v[166:169], v[104:107]\n"
      "s_waitcnt lgkmcnt(1)\n"
      "v_fmac_f32 v77, v175, v175\n"
      "v_cndmask_b32 v218, v218, v100, s[60:61]\n"
      "v_mfma_f32_16x16x32_bf16 v[104:107], a[20:23], v[116:119], v[104:107]\n"
      "v_cvt_pk_bf16_f32 v195, v196, v197\n"
      "v_cvt_pk_bf16_f32 v131, v132, v133\n"
      "v_mfma_f32_16x16x32_bf16 v[158:161], a[24:27], v[198:201], 0\n"
      "v_fmac_f32_e64 v77, v176, v176\n"
      "v_subrev_u32 v185, 115456, v9\n"
      "v_mfma_f32_16x16x32_bf16 v[158:161], a[28:31], v[138:141], v[158:161]\n"
      "v_cvt_pk_bf16_f32 v109, v110, v111\n"
      "v_fmac_f32_e64 v81, v97, v97\n"
      "v_mfma_f32_16x16x32_bf16 v[158:161], a[32:35], v[134:137], v[158:161]\n"
      "v_fma_f32 v205, v104, v104, s51\n"
      "v_bfe_u32 v219, v185, 4, 4\n"
      "v_mfma_f32_16x16x32_bf16 v[158:161], a[36:39], v[170:173], v[158:161]\n"
      "v_fmac_f32 v81, v98, v98\n"
      "v_mul_u32_u24 v82, 98, v219\n"
      "v_mfma_f32_16x16x32_bf16 v[158:161], a[40:43], v[166:169], v[158:161]\n"
      "v_lshrrev_b32 v185, 8, v185\n"
      "v_fmac_f32_e64 v205, v105, v105\n"
      "v_mfma_f32_16x16x32_bf16 v[158:161], a[44:47], v[116:119], v[158:161]\n"
      "s_cmp_ge_u32 s95, 0x40000000\n"
      "s_cselect_b32 s94, -1, -2\n"
      "v_fmac_f32 v81, v99, v99\n"
      "v_mfma_f32_16x16x32_bf16 v[126:129], a[48:51], v[198:201], 0\n"
      "v_fmac_f32 v81, v146, v146\n"
      "v_fmac_f32 v81, v147, v147\n"
      "v_mfma_f32_16x16x32_bf16 v[126:129], a[52:55], v[138:141], v[126:129]\n"
      "v_fmac_f32 v77, v177, v177\n"
      "v_fmac_f32_e64 v81, v148, v148\n"
      "v_mfma_f32_16x16x32_bf16 v[126:129], a[56:59], v[134:137], v[126:129]\n"
      "v_cvt_pk_bf16_f32 v214, v214, v215\n"
      "v_fmac_f32 v205, v106, v106\n"
      "v_mfma_f32_16x16x32_bf16 v[126:129], a[60:63], v[170:173], v[126:129]\n"
      "s_cmp_gt_u32 s42, s44\n"
      "s_cselect_b32 s95, s94, s95\n"
      "v_fmac_f32 v81, v149, v149\n"
      "v_mfma_f32_16x16x32_bf16 v[126:129], a[64:67], v[166:169], v[126:129]\n"
      "ds_read_b128 v[110:113], v9 offset:5632\n"
      "v_cvt_pk_bf16_f32 v215, v216, v217\n"
      "v_mfma_f32_16x16x32_bf16 v[126:129], a[68:71], v[116:119], v[126:129]\n"
      "v_permlane16_swap_b32 v77, v81\n"
      "v_mfma_f32_16x16x32_bf16 v[120:123], a[72:75], v[198:201], 0\n"
      "v_add_f32 v77, v77, v81\n"
      "v_mov_b32 v81, v77\n"
      "v_mfma_f32_16x16x32_bf16 v[120:123], a[76:79], v[138:141], v[120:123]\n"
      "s_nop 0\n"
      "v_permlane32_swap_b32 v77, v81\n"
      "v_mfma_f32_16x16x32_bf16 v[120:123], a[80:83], v[134:137], v[120:123]\n"
      "v_fmac_f32 v205, v107, v107\n"
      "v_add_f32 v77, v77, v81\n"
      "v_mfma_f32_16x16x32_bf16 v[120:123], a[84:87], v[170:173], v[120:123]\n"
      "v_rsq_f32 v77, v77\n"
      "v_mfma_f32_16x16x32_bf16 v[120:123], a[88:91], v[166:169], v[120:123]\n"
      "v_fmac_f32 v205, v158, v158\n"
      "v_mul_f32 v218, v188, v218\n"
      "v_mfma_f32_16x16x32_bf16 v[120:123], a[92:95], v[116:119], v[120:123]\n"
      "v_mov_b32 v81, v77\n"
      "v_fmac_f32 v205, v159, v159\n"
      "v_mfma_f32_16x16x32_bf16 v[100:103], a[96:99], v[198:201], 0\n"
      "v_lshl_add_u32 v82, v185, 3, v82\n"
      "v_lshlrev_b32 v185, 2, v185\n"
      "v_mfma_f32_16x16x32_bf16 v[100:103], a[100:103], v[138:141], v[100:103]\n"
      "v_permlane16_swap_b32 v77, v81\n"
      "v_mfma_f32_16x16x32_bf16 v[100:103], a[104:107], v[134:137], v[100:103]\n"
      "v_mul_f32 v77, s50, v77\n"
      "v_fmac_f32 v205, v160, v160\n"
      "v_mfma_f32_16x16x32_bf16 v[100:103], a[108:111], v[170:173], v[100:103]\n"
      "v_mul_f32 v146, v81, v146\n"
      "v_mul_f32 v176, v77, v176\n"
      "v_mfma_f32_16x16x32_bf16 v[100:103], a[112:115], v[166:169], v[100:103]\n"
      "v_mul_f32 v152, v81, v152\n"
      "v_mul_f32 v212, v77, v212\n"
      "v_mfma_f32_16x16x32_bf16 v[100:103], a[116:119], v[116:119], v[100:103]\n"
      "v_fma_f32 v204, v120, v120, s51\n"
      "v_mul_f32 v98, v81, v98\n"
      "v_mfma_f32_16x16x32_bf16 v[154:157], a[120:123], v[198:201], 0\n"
      "v_fmac_f32 v204, v121, v121\n"
      "v_sub_u32_e64 v219, v219, v185\n"
      "v_mfma_f32_16x16x32_bf16 v[154:157], a[124:127], v[138:141], v[154:157]\n"
      "ds_read_b128 v[182:185], v1 offset:16384\n"
      "v_fmac_f32 v205, v161, v161\n"
      "v_mfma_f32_16x16x32_bf16 v[154:157], a[128:131], v[134:137], v[154:157]\n"
      "s_waitcnt lgkmcnt(1)\n"
      "v_fmac_f32_e64 v204, v122, v122\n"
      "v_or3_b32 v110, v110, v111, v112\n"
      "v_mfma_f32_16x16x32_bf16 v[154:157], a[132:135], v[170:173], v[154:157]\n"
      "v_mul_f32 v99, v81, v99\n"
      "v_mul_f32 v174, v77, v174\n"
      "v_mfma_f32_16x16x32_bf16 v[154:157], a[136:139], v[166:169], v[154:157]\n"
      "v_mul_f32 v192, v77, v192\n"
      "v_mul_f32 v177, v77, v177\n"
      "v_mfma_f32_16x16x32_bf16 v[154:157], a[140:143], v[116:119], v[154:157]\n"
      "ds_write_b64 v6, v[108:109] offset:12288\n"
      "v_mfma_f32_16x16x32_bf16 v[142:145], v[198:201], a[144:147], 0\n"
      "v_fmac_f32 v204, v123, v123\n"
      "v_cvt_pk_bf16_f32 v218, v218, v218\n"
      "v_mfma_f32_16x16x32_bf16 v[142:145], v[138:141], a[148:151], v[142:145]\n"
      "ds_read_b128 v[186:189], v1 offset:4096\n"
      "v_fmac_f32 v204, v100, v100\n"
      "v_mfma_f32_16x16x32_bf16 v[142:145], v[134:137], a[152:155], v[142:145]\n"
      "ds_write_b16 v12, v218 offset:16\n"
      "v_mfma_f32_16x16x32_bf16 v[142:145], v[170:173], a[156:159], v[142:145]\n"
      "v_mul_f32 v151, v81, v151\n"
      "v_mul_f32 v190, v77, v190\n"
      "v_mfma_f32_16x16x32_bf16 v[142:145], v[166:169], a[160:163], v[142:145]\n"
      "v_fmac_f32 v205, v126, v126\n"
      "v_fmac_f32 v204, v101, v101\n"
      "v_mfma_f32_16x16x32_bf16 v[142:145], v[116:119], a[164:167], v[142:145]\n"
      "v_fmac_f32 v204, v102, v102\n"
      "v_mul_f32 v149, v81, v149\n"
      "v_mfma_f32_16x16x32_bf16 v[162:165], v[198:201], a[168:171], 0\n"
      "v_mul_f32 v147, v81, v147\n"
      "v_bitop3_b32 v110, v110, v113, s33 bitop3:0xa8\n"
      "v_mfma_f32_16x16x32_bf16 v[162:165], v[138:141], a[172:175], v[162:165]\n"
      "v_mul_f32 v210, v77, v210\n"
      "v_fmac_f32 v204, v103, v103\n"
      "v_mfma_f32_16x16x32_bf16 v[162:165], v[134:137], a[176:179], v[162:165]\n"
      "ds_read_b128 v[112:115], v1 offset:10240\n"
      "v_add_u32 v82, s77, v82\n"
      "v_mfma_f32_16x16x32_bf16 v[162:165], v[170:173], a[180:183], v[162:165]\n"
      "v_fmac_f32 v204, v154, v154\n"
      "v_cmp_ne_u32 s[56:57], 0, v110\n"
      "v_mfma_f32_16x16x32_bf16 v[162:165], v[166:169], a[184:187], v[162:165]\n"
      "v_mul_f32 v153, v81, v153\n"
      "v_mul_f32_e64 v97, v81, v97\n"
      "v_mfma_f32_16x16x32_bf16 v[162:165], v[116:119], a[188:191], v[162:165]\n"
      "ds_read_b128 v[108:111], v0 offset:4096\n"
      "v_mul_f32 v191, v77, v191\n"
      "v_mfma_f32_16x16x32_bf16 v[178:181], v[198:201], a[192:195], 0\n"
      "v_add_u32 v12, 32, v12\n"
      "v_fmac_f32 v205, v127, v127\n"
      "v_mfma_f32_16x16x32_bf16 v[178:181], v[138:141], a[196:199], v[178:181]\n"
      "v_fmac_f32 v204, v155, v155\n"
      "v_fmac_f32 v205, v128, v128\n"
      "v_mfma_f32_16x16x32_bf16 v[178:181], v[134:137], a[200:203], v[178:181]\n"
      "v_fmac_f32 v204, v156, v156\n"
      "v_mul_f32 v150, v81, v150\n"
      "v_mfma_f32_16x16x32_bf16 v[178:181], v[170:173], a[204:207], v[178:181]\n"
      "ds_read_b128 v[198:201], v0 offset:16384\n"
      "v_mul_f32 v193, v77, v193\n"
      "v_mfma_f32_16x16x32_bf16 v[178:181], v[166:169], a[208:211], v[178:181]\n"
      "s_add_u32 s43, s43, 1\n"
      "v_fmac_f32 v205, v129, v129\n"
      "v_mul_f32 v211, v77, v211\n"
      "v_mfma_f32_16x16x32_bf16 v[178:181], v[116:119], a[212:215], v[178:181]\n"
      "s_waitcnt lgkmcnt(1)\n"
      "v_cvt_pk_bf16_f32 v210, v210, v211\n"
      "v_cvt_pk_bf16_f32 v150, v150, v151\n"
      "v_mfma_f32_16x16x32_bf16 v[116:119], a[0:3], v[108:111], 0\n"
      "v_cvt_pk_bf16_f32 v151, v152, v153\n"
      "v_fmac_f32_e64 v204, v157, v157\n"
      "v_mfma_f32_16x16x32_bf16 v[116:119], a[4:7], v[186:189], v[116:119]\n"
      "s_nop 0\n"
      "v_permlane16_swap_b32 v205, v204\n"
      "v_mfma_f32_16x16x32_bf16 v[116:119], a[8:11], v[220:223], v[116:119]\n"
      "v_lshlrev_b32 v219, 4, v219\n"
      "v_add_f32 v205, v205, v204\n"
      "v_mfma_f32_16x16x32_bf16 v[116:119], a[12:15], v[112:115], v[116:119]\n"
      "s_waitcnt lgkmcnt(0)\n"
      "v_mul_f32 v96, v81, v96\n"
      "v_mov_b32 v204, v205\n"
      "v_mfma_f32_16x16x32_bf16 v[116:119], a[16:19], v[198:201], v[116:119]\n"
      "s_nop 0\n"
      "v_permlane32_swap_b32_e64 v205, v204\n"
      "v_mfma_f32_16x16x32_bf16 v[116:119], a[20:23], v[182:185], v[116:119]\n"
      "v_cvt_pk_bf16_f32 v216, v178, v179\n"
      "v_cvt_pk_bf16_f32 v132, v142, v143\n"
      "v_mfma_f32_16x16x32_bf16 v[134:137], a[24:27], v[108:111], 0\n"
      "v_cvt_pk_bf16_f32 v146, v146, v147\n"
      "v_add_f32 v205, v205, v204\n"
      "v_mfma_f32_16x16x32_bf16 v[134:137], a[28:31], v[186:189], v[134:137]\n"
      "v_rsq_f32 v205, v205\n"
      "v_mfma_f32_16x16x32_bf16 v[134:137], a[32:35], v[220:223], v[134:137]\n"
      "v_mul_f32 v213, v77, v213\n"
      "v_fma_f32 v218, v116, v116, s51\n"
      "v_mfma_f32_16x16x32_bf16 v[134:137], a[36:39], v[112:115], v[134:137]\n"
      "s_waitcnt vmcnt(7)\n"
      "v_mov_b32 v204, v205\n"
      "v_cvt_pk_bf16_f32 v153, v98, v99\n"
      "v_mfma_f32_16x16x32_bf16 v[134:137], a[40:43], v[198:201], v[134:137]\n"
      "v_permlane16_swap_b32 v205, v204\n"
      "v_mfma_f32_16x16x32_bf16 v[134:137], a[44:47], v[182:185], v[134:137]\n"
      "v_and_b32 v92, 2, v82\n"
      "v_mul_f32 v100, v204, v100\n"
      "v_mfma_f32_16x16x32_bf16 v[138:141], a[48:51], v[108:111], 0\n"
      "v_cvt_pk_bf16_f32 v196, v162, v163\n"
      "v_mul_f32_e64 v148, v81, v148\n"
      "v_mfma_f32_16x16x32_bf16 v[138:141], a[52:55], v[186:189], v[138:141]\n"
      "v_fmac_f32 v218, v117, v117\n"
      "v_fmac_f32 v218, v118, v118\n"
      "v_mfma_f32_16x16x32_bf16 v[138:141], a[56:59], v[220:223], v[138:141]\n"
      "v_mul_f32 v101, v204, v101\n"
      "v_mul_f32 v205, s50, v205\n"
      "v_mfma_f32_16x16x32_bf16 v[138:141], a[60:63], v[112:115], v[138:141]\n"
      "v_cvt_pk_bf16_f32 v152, v96, v97\n"
      "v_mul_f32 v105, v205, v105\n"
      "v_mfma_f32_16x16x32_bf16 v[138:141], a[64:67], v[198:201], v[138:141]\n"
      "v_mul_f32 v121, v204, v121\n"
      "v_cvt_pk_bf16_f32 v211, v212, v213\n"
      "v_mfma_f32_16x16x32_bf16 v[138:141], a[68:71], v[182:185], v[138:141]\n"
      "v_mul_f32_e64 v104, v205, v104\n"
      "v_cvt_pk_bf16_f32 v104, v104, v105\n"
      "v_mfma_f32_16x16x32_bf16 v[96:99], a[72:75], v[108:111], 0\n"
      "v_mul_f32_e64 v158, v205, v158\n"
      "v_cvt_pk_bf16_f32 v133, v144, v145\n"
      "v_mfma_f32_16x16x32_bf16 v[96:99], a[76:79], v[186:189], v[96:99]\n"
      "v_cvt_pk_bf16_f32 v197, v164, v165\n"
      "v_mul_f32 v154, v204, v154\n"
      "v_mfma_f32_16x16x32_bf16 v[96:99], a[80:83], v[220:223], v[96:99]\n"
      "v_mul_f32 v155, v204, v155\n"
      "v_fmac_f32_e64 v218, v119, v119\n"
      "v_mfma_f32_16x16x32_bf16 v[96:99], a[84:87], v[112:115], v[96:99]\n"
      "v_mul_f32_e64 v175, v77, v175\n"
      "v_cvt_pk_bf16_f32 v174, v174, v175\n"
      "v_mfma_f32_16x16x32_bf16 v[96:99], a[88:91], v[198:201], v[96:99]\n"
      "v_cvt_pk_bf16_f32 v154, v154, v155\n"
      "v_and_b32 v82, -4, v82\n"
      "v_mfma_f32_16x16x32_bf16 v[96:99], a[92:95], v[182:185], v[96:99]\n"
      "v_mul_f32 v159, v205, v159\n"
      "v_fmac_f32_e64 v218, v134, v134\n"
      "v_mfma_f32_16x16x32_bf16 v[162:165], a[96:99], v[108:111], 0\n"
      "v_cvt_pk_bf16_f32 v147, v148, v149\n"
      "v_mul_f32_e64 v120, v204, v120\n"
      "v_mfma_f32_16x16x32_bf16 v[162:165], a[100:103], v[186:189], v[162:165]\n"
      "v_cmp_gt_u32 s[72:73], 64, v219\n"
      "v_fmac_f32 v218, v135, v135\n"
      "v_mfma_f32_16x16x32_bf16 v[162:165], a[104:107], v[220:223], v[162:165]\n"
      "v_fma_f32 v178, v96, v96, s51\n"
      "v_fmac_f32 v178, v97, v97\n"
      "v_fmac_f32 v218, v136, v136\n"
      "v_fmac_f32 v218, v137, v137\n"
      "v_cvt_pk_bf16_f32 v120, v120, v121\n"
      "v_pk_mul_f32 v[128:129], v[128:129], v[204:205] op_sel:[0,1]\n"
      "v_pk_mul_f32 v[102:103], v[102:103], v[204:205] op_sel_hi:[1,0]\n"
      "v_pk_mul_f32 v[106:107], v[106:107], v[204:205] op_sel:[0,1]\n"
      "v_pk_mul_f32 v[156:157], v[156:157], v[204:205] op_sel_hi:[1,0]\n"
      "v_pk_mul_f32 v[160:161], v[160:161], v[204:205] op_sel:[0,1]\n"
      "v_pk_mul_f32 v[126:127], v[126:127], v[204:205] op_sel:[0,1]\n"
      "v_pk_mul_f32 v[122:123], v[122:123], v[204:205] op_sel_hi:[1,0]\n"
      "v_mfma_f32_16x16x32_bf16 v[162:165], a[108:111], v[112:115], v[162:165]\n"
      "v_cvt_pk_bf16_f32 v121, v122, v123\n"
      "v_fmac_f32_e64 v178, v98, v98\n"
      "v_mfma_f32_16x16x32_bf16 v[162:165], a[112:115], v[198:201], v[162:165]\n"
      "v_cvt_pk_bf16_f32 v175, v176, v177\n"
      "v_fmac_f32 v218, v138, v138\n"
      "v_mfma_f32_16x16x32_bf16 v[162:165], a[116:119], v[182:185], v[162:165]\n"
      "s_add_u32 s42, s42, 1\n"
      "v_cvt_pk_bf16_f32 v155, v156, v157\n"
      "v_cvt_pk_bf16_f32 v105, v106, v107\n"
      "v_mfma_f32_16x16x32_bf16 v[202:205], a[120:123], v[108:111], 0\n"
      "v_fmac_f32 v218, v139, v139\n"
      "v_fmac_f32 v178, v99, v99\n"
      "v_mfma_f32_16x16x32_bf16 v[202:205], a[124:127], v[186:189], v[202:205]\n"
      "v_cvt_pk_bf16_f32 v126, v126, v127\n"
      "v_fmac_f32 v218, v140, v140\n"
      "v_mfma_f32_16x16x32_bf16 v[202:205], a[128:131], v[220:223], v[202:205]\n"
      "v_fmac_f32 v178, v162, v162\n"
      "v_fmac_f32_e64 v178, v163, v163\n"
      "v_mfma_f32_16x16x32_bf16 v[202:205], a[132:135], v[112:115], v[202:205]\n"
      "v_cvt_pk_bf16_f32 v212, v190, v191\n"
      "v_cvt_pk_bf16_f32 v122, v100, v101\n"
      "v_mfma_f32_16x16x32_bf16 v[202:205], a[136:139], v[198:201], v[202:205]\n"
      "v_cvt_pk_bf16_f32 v106, v158, v159\n"
      "v_fmac_f32_e64 v178, v164, v164\n"
      "v_mfma_f32_16x16x32_bf16 v[202:205], a[140:143], v[182:185], v[202:205]\n"
      "v_cvt_pk_bf16_f32 v213, v192, v193\n"
      "v_fmac_f32 v218, v141, v141\n"
      "v_mfma_f32_16x16x32_bf16 v[166:169], v[108:111], a[144:147], 0\n"
      "s_cmp_ge_u32 s95, 0x40000000\n"
      "s_cselect_b64 vcc, -1, s[56:57]\n"
      "v_fmac_f32 v178, v165, v165\n"
      "v_mfma_f32_16x16x32_bf16 v[166:169], v[186:189], a[148:151], v[166:169]\n"
      "s_nop 0\n"
      "v_fmac_f32 v178, v202, v202\n"
      "v_fmac_f32 v178, v203, v203\n"
      "v_mfma_f32_16x16x32_bf16 v[166:169], v[220:223], a[152:155], v[166:169]\n"
      "v_fmac_f32 v178, v204, v204\n"
      "v_fmac_f32 v178, v205, v205\n"
      "v_mfma_f32_16x16x32_bf16 v[166:169], v[112:115], a[156:159], v[166:169]\n"
      "s_nop 0\n"
      "v_permlane16_swap_b32 v218, v178\n"
      "v_mfma_f32_16x16x32_bf16 v[166:169], v[198:201], a[160:163], v[166:169]\n"
      "v_add_f32 v218, v218, v178\n"
      "v_mov_b32 v178, v218\n"
      "v_mfma_f32_16x16x32_bf16 v[166:169], v[182:185], a[164:167], v[166:169]\n"
      "s_nop 0\n"
      "v_permlane32_swap_b32 v218, v178\n"
      "v_add_f32 v218, v218, v178\n"
      "v_mfma_f32_16x16x32_bf16 v[170:173], v[108:111], a[168:171], 0\n"
      "v_rsq_f32 v218, v218\n"
      "v_mfma_f32_16x16x32_bf16 v[170:173], v[186:189], a[172:175], v[170:173]\n"
      "v_cvt_pk_bf16_f32 v107, v160, v161\n"
      "v_mov_b32 v178, v218\n"
      "v_mfma_f32_16x16x32_bf16 v[170:173], v[220:223], a[176:179], v[170:173]\n"
      "s_nop 0\n"
      "v_permlane16_swap_b32 v218, v178\n"
      "v_mfma_f32_16x16x32_bf16 v[170:173], v[112:115], a[180:183], v[170:173]\n"
      "ds_read_b128 v[142:145], v7 offset:18432\n"
      "v_cvt_pk_bf16_f32 v127, v128, v129\n"
      "v_lshlrev_b64 v[124:125], v219, v[124:125]\n"
      "v_cvt_pk_bf16_f32 v77, v168, v169\n"
      "v_mul_f32_e64 v218, s50, v218\n"
      "v_pk_mul_f32 v[204:205], v[204:205], v[178:179] op_sel_hi:[1,0]\n"
      "v_pk_mul_f32 v[202:203], v[202:203], v[178:179] op_sel_hi:[1,0]\n"
      "v_pk_mul_f32 v[116:117], v[116:117], v[218:219] op_sel_hi:[1,0]\n"
      "v_pk_mul_f32 v[98:99], v[98:99], v[178:179] op_sel_hi:[1,0]\n"
      "v_pk_mul_f32 v[164:165], v[164:165], v[178:179] op_sel_hi:[1,0]\n"
      "v_pk_mul_f32 v[138:139], v[138:139], v[218:219] op_sel_hi:[1,0]\n"
      "v_pk_mul_f32 v[162:163], v[162:163], v[178:179] op_sel_hi:[1,0]\n"
      "v_pk_mul_f32 v[96:97], v[96:97], v[178:179] op_sel_hi:[1,0]\n"
      "v_pk_mul_f32 v[140:141], v[140:141], v[218:219] op_sel_hi:[1,0]\n"
      "v_mfma_f32_16x16x32_bf16 v[170:173], v[198:201], a[184:187], v[170:173]\n"
      "ds_read_b128 v[176:179], v2 offset:18432\n"
      "v_cvt_pk_bf16_f32 v96, v96, v97\n"
      "v_mfma_f32_16x16x32_bf16 v[170:173], v[182:185], a[188:191], v[170:173]\n"
      "ds_read_b128 v[158:161], v2 offset:21504\n"
      "v_add_u32 v148, 110592, v82\n"
      "v_mfma_f32_16x16x32_bf16 v[190:193], v[108:111], a[192:195], 0\n"
      "ds_read_b128 v[108:111], v2 offset:24576\n"
      "v_cvt_pk_bf16_f32 v97, v98, v99\n"
      "v_mfma_f32_16x16x32_bf16 v[190:193], v[186:189], a[196:199], v[190:193]\n"
      "ds_read_b128 v[186:189], v2 offset:30720\n"
      "v_cvt_pk_bf16_f32 v123, v102, v103\n"
      "v_mfma_f32_16x16x32_bf16 v[190:193], v[220:223], a[200:203], v[190:193]\n"
      "ds_read_b128 v[100:103], v2 offset:27648\n"
      "v_cvt_pk_bf16_f32 v138, v138, v139\n"
      "v_mfma_f32_16x16x32_bf16 v[190:193], v[112:115], a[204:207], v[190:193]\n"
      "ds_read_b128 v[220:223], v2 offset:33792\n"
      "v_cvt_pk_bf16_f32 v81, v172, v173\n"
      "v_mfma_f32_16x16x32_bf16 v[190:193], v[198:201], a[208:211], v[190:193]\n"
      "ds_read_b128 v[112:115], v7 offset:22528\n"
      "v_cvt_pk_bf16_f32 v99, v164, v165\n"
      "v_mfma_f32_16x16x32_bf16 v[190:193], v[182:185], a[212:215], v[190:193]\n"
      "s_waitcnt lgkmcnt(5)\n"
      "v_cvt_pk_bf16_f32 v76, v166, v167\n"
      "v_mov_b32 v129, 0\n"
      "v_mfma_f32_16x16x32_bf16 v[166:169], a[216:219], v[176:179], 0\n"
      "buffer_store_dwordx4 v[142:145], v20, s[24:27], s46 offen sc1\n"
      "v_mfma_f32_16x16x32_bf16 v[166:169], a[220:223], v[158:161], v[166:169]\n"
      "s_waitcnt lgkmcnt(2)\n"
      "v_mul_f32 v119, v218, v119\n"
      "v_mul_f32 v136, v218, v136\n"
      "v_mfma_f32_16x16x32_bf16 v[166:169], a[224:227], v[108:111], v[166:169]\n"
      "v_cvt_pk_bf16_f32 v202, v202, v203\n"
      "v_cvt_pk_bf16_f32 v203, v204, v205\n"
      "v_mfma_f32_16x16x32_bf16 v[198:201], a[240:243], v[176:179], 0\n"
      "ds_read_b64 v[204:205], v10 offset:24\n"
      "v_cndmask_b32_e64 v124, 0, v124, s[72:73]\n"
      "v_mfma_f32_16x16x32_bf16 v[198:201], a[244:247], v[158:161], v[198:201]\n"
      "s_waitcnt lgkmcnt(1)\n"
      "v_cndmask_b32_e64 v125, 0, v125, s[72:73]\n"
      "v_cvt_pk_bf16_f32 v98, v162, v163\n"
      "v_mfma_f32_16x16x32_bf16 v[198:201], a[248:251], v[108:111], v[198:201]\n"
      "ds_read2_b32 v[172:173], v148 offset0:24 offset1:18\n"
      "v_mov_b32 v128, 0xbb2b\n"
      "v_mfma_f32_16x16x32_bf16 v[142:145], v[232:235], v[176:179], 0\n"
      "ds_read2_b32 v[184:185], v148 offset0:2 offset1:10\n"
      "v_lshlrev_b64 v[128:129], v219, v[128:129]\n"
      "v_mfma_f32_16x16x32_bf16 v[142:145], v[236:239], v[158:161], v[142:145]\n"
      "ds_read_b128 v[162:165], v7 offset:26624\n"
      "v_cvt_pk_bf16_f32 v217, v180, v181\n"
      "v_mfma_f32_16x16x32_bf16 v[142:145], v[240:243], v[108:111], v[142:145]\n"
      "buffer_store_dwordx4 v[112:115], v21, s[24:27], s46 offen sc1\n"
      "v_pk_mul_f32 v[134:135], v[134:135], v[218:219] op_sel_hi:[1,0]\n"
      "v_mfma_f32_16x16x32_bf16 v[166:169], a[228:231], v[100:103], v[166:169]\n"
      "ds_read2_b32 v[158:159], v148 offset0:0 offset1:1\n"
      "v_mul_f32 v137, v218, v137\n"
      "v_mfma_f32_16x16x32_bf16 v[166:169], a[232:235], v[186:189], v[166:169]\n"
      "s_waitcnt lgkmcnt(3)\n"
      "v_mul_f32 v118, v218, v118\n"
      "v_cvt_pk_bf16_f32 v84, v190, v191\n"
      "v_mfma_f32_16x16x32_bf16 v[166:169], a[236:239], v[220:223], v[166:169]\n"
      "ds_read2_b32 v[156:157], v148 offset0:8 offset1:9\n"
      "v_cvt_pk_bf16_f32 v116, v116, v117\n"
      "v_mfma_f32_16x16x32_bf16 v[198:201], a[252:255], v[100:103], v[198:201]\n"
      "ds_read2_b32 v[88:89], v148 offset0:16 offset1:17\n"
      "v_alignbyte_b32 v172, v172, v172, v92\n"
      "v_mfma_f32_16x16x32_bf16 v[198:201], v[224:227], v[186:189], v[198:201]\n"
      "s_waitcnt lgkmcnt(3)\n"
      "v_cvt_pk_bf16_f32 v117, v118, v119\n"
      "v_cvt_pk_bf16_f32 v85, v192, v193\n"
      "v_mfma_f32_16x16x32_bf16 v[198:201], v[228:231], v[220:223], v[198:201]\n"
      "ds_read_b128 v[180:183], v7 offset:30720\n"
      "v_fma_f32 v148, v166, v166, s52\n"
      "v_mfma_f32_16x16x32_bf16 v[142:145], v[244:247], v[100:103], v[142:145]\n"
      "ds_read_b128 v[100:103], v10 offset:32\n"
      "v_mul_f32_e64 v149, v167, v167\n"
      "v_mfma_f32_16x16x32_bf16 v[142:145], v[248:251], v[186:189], v[142:145]\n"
      "buffer_store_dwordx4 v[162:165], v22, s[24:27], s46 offen sc1\n"
      "v_mfma_f32_16x16x32_bf16 v[142:145], v[252:255], v[220:223], v[142:145]\n"
      "v_add_u32 v160, 112160, v82\n"
      "v_cmp_gt_u32 s[72:73], 64, v219\n"
      "v_mfma_f32_16x16x32_bf16 v[112:115], v[150:153], v[210:213], v[40:43]\n"
      "s_waitcnt lgkmcnt(2)\n"
      "v_cvt_pk_bf16_f32 v118, v134, v135\n"
      "v_cvt_pk_bf16_f32 v139, v140, v141\n"
      "v_mfma_f32_16x16x32_bf16 v[108:111], v[120:123], v[210:213], v[44:47]\n"
      "ds_read_b128 v[188:191], v2 offset:21760\n"
      "v_cvt_pk_bf16_f32 v119, v136, v137\n"
      "v_mfma_f32_16x16x32_bf16 v[218:221], v[96:99], v[210:213], v[48:51]\n"
      "s_waitcnt lgkmcnt(1)\n"
      "v_alignbyte_b32 v156, v157, v156, v92\n"
      "v_alignbyte_b32 v158, v159, v158, v92\n"
      "v_mfma_f32_16x16x32_bf16 v[176:179], v[100:103], v[210:213], v[24:27]\n"
      "ds_read_b128 v[162:165], v7 offset:34816\n"
      "v_fmac_f32_e64 v149, v169, v169\n"
      "v_mfma_f32_16x16x16_bf16 v[112:115], v[146:147], v[174:175], v[112:115]\n"
      "v_cvt_pk_bf16_f32 v80, v170, v171\n"
      "v_alignbyte_b32 v159, v184, v159, v92\n"
      "v_cndmask_b32_e64 v128, 0, v128, s[72:73]\n"
      "v_mfma_f32_16x16x16_bf16 v[108:111], v[154:155], v[174:175], v[108:111]\n"
      "ds_read_b128 v[210:213], v2 offset:18688\n"
      "v_alignbyte_b32 v157, v185, v157, v92\n"
      "v_mfma_f32_16x16x16_bf16 v[218:221], v[202:203], v[174:175], v[218:221]\n"
      "buffer_store_dwordx4 v[180:183], v23, s[24:27], s46 offen sc1\n"
      "v_mfma_f32_16x16x16_bf16 v[176:179], v[204:205], v[174:175], v[176:179]\n"
      "v_alignbyte_b32 v88, v89, v88, v92\n"
      "v_subrev_u32 v39, 115456, v9\n"
      "v_cndmask_b32_e64 v129, 0, v129, s[72:73]\n"
      "v_mfma_f32_16x16x16_bf16 v[112:115], v[124:125], v[158:159], v[112:115]\n"
      "v_fmac_f32 v148, v168, v168\n"
      "v_lshrrev_b32 v39, 3, v39\n"
      "v_alignbyte_b32 v89, v173, v89, v92\n"
      "v_mfma_f32_16x16x16_bf16 v[108:111], v[124:125], v[156:157], v[108:111]\n"
      "v_fmac_f32_e64 v148, v198, v198\n"
      "v_fmac_f32_e64 v149, v199, v199\n"
      "v_min_u32 v39, 96, v39\n"
      "v_mfma_f32_16x16x16_bf16 v[218:221], v[124:125], v[88:89], v[218:221]\n"
      "v_add_u32 v140, 113728, v82\n"
      "v_fmac_f32_e64 v149, v201, v201\n"
      "v_fmac_f32 v148, v200, v200\n"
      "v_mfma_f32_16x16x16_bf16 v[176:179], v[124:125], v[172:173], v[176:179]\n"
      "ds_read2_b32 v[136:137], v160 offset0:0 offset1:1\n"
      "v_add_u32_e64 v39, s77, v39\n"
      "v_mfma_f32_16x16x16_bf16 v[112:115], v[128:129], v[158:159], v[112:115]\n"
      "ds_read_b128 v[184:187], v2 offset:24832\n"
      "v_add_u32 v39, 115296, v39\n"
      "v_mfma_f32_16x16x16_bf16 v[108:111], v[128:129], v[156:157], v[108:111]\n"
      "ds_read2_b32 v[222:223], v160 offset0:8 offset1:9\n"
      "ds_read2_b32 v[170:171], v160 offset0:2 offset1:10\n"
      "v_mfma_f32_16x16x16_bf16 v[218:221], v[128:129], v[88:89], v[218:221]\n"
      "s_waitcnt lgkmcnt(4)\n"
      "buffer_store_dwordx4 v[162:165], v25, s[24:27], s46 offen sc1\n"
      "v_pk_fma_f32 v[148:149], v[142:143], v[142:143], v[148:149]\n"
      "v_pk_fma_f32 v[148:149], v[144:145], v[144:145], v[148:149]\n"
      "v_mfma_f32_16x16x16_bf16 v[176:179], v[128:129], v[172:173], v[176:179]\n"
      "v_max3_f32 v134, v112, v113, v114\n"
      "v_max3_f32 v183, v115, v108, v109\n"
      "v_max3_f32 v134, v134, v110, v111\n"
      "v_add_f32_e64 v148, v148, v149\n"
      "v_mfma_f32_16x16x32_bf16 v[162:165], a[216:219], v[210:213], 0\n"
      "s_waitcnt lgkmcnt(2)\n"
      "v_max3_f32 v134, v134, v220, v221\n"
      "v_alignbyte_b32 v136, v137, v136, v92\n"
      "v_mfma_f32_16x16x32_bf16 v[162:165], a[220:223], v[188:191], v[162:165]\n"
      "s_waitcnt lgkmcnt(1)\n"
      "v_max3_f32 v183, v183, v218, v219\n"
      "v_alignbyte_b32 v222, v223, v222, v92\n"
      "v_max3_f32 v134, v134, v183, v176\n"
      "v_mfma_f32_16x16x32_bf16 v[162:165], a[224:227], v[184:187], v[162:165]\n"
      "s_waitcnt lgkmcnt(0)\n"
      "v_alignbyte_b32 v137, v170, v137, v92\n"
      "v_mov_b32 v157, v134\n"
      "v_mfma_f32_16x16x32_bf16 v[180:183], a[240:243], v[210:213], 0\n"
      "s_nop 0\n"
      "v_permlane16_swap_b32 v134, v157\n"
      "v_mfma_f32_16x16x32_bf16 v[180:183], a[244:247], v[188:191], v[180:183]\n"
      "s_waitcnt vmcnt(6)\n"
      "v_or3_b32 v206, v206, v207, v208\n"
      "v_max_f32 v134, v134, v157\n"
      "v_mfma_f32_16x16x32_bf16 v[180:183], a[248:251], v[184:187], v[180:183]\n"
      "ds_read2_b32 v[174:175], v160 offset0:16 offset1:17\n"
      "v_alignbyte_b32 v223, v171, v223, v92\n"
      "v_mfma_f32_16x16x32_bf16 v[170:173], v[232:235], v[210:213], 0\n"
      "ds_read2_b32 v[192:193], v160 offset0:24 offset1:18\n"
      "v_mov_b32 v157, v134\n"
      "v_mfma_f32_16x16x32_bf16 v[170:173], v[236:239], v[188:191], v[170:173]\n"
      "s_nop 0\n"
      "v_permlane32_swap_b32 v134, v157\n"
      "v_mfma_f32_16x16x32_bf16 v[170:173], v[240:243], v[184:187], v[170:173]\n"
      "v_max_f32 v134, v134, v157\n"
      "v_sub_f32 v220, v220, v134\n"
      "v_sub_f32 v113, v113, v134\n"
      "v_sub_f32 v112, v112, v134\n"
      "v_exp_f32 v220, v220\n"
      "v_mfma_f32_16x16x32_bf16 v[210:213], v[150:153], v[104:107], v[52:55]\n"
      "v_sub_f32 v108, v108, v134\n"
      "s_waitcnt vmcnt(5)\n"
      "v_or3_b32 v206, v206, v209, v78\n"
      "v_sub_f32 v218, v218, v134\n"
      "v_mfma_f32_16x16x32_bf16 v[188:191], v[120:123], v[104:107], v[56:59]\n"
      "s_waitcnt lgkmcnt(1)\n"
      "v_sub_f32 v109, v109, v134\n"
      "v_sub_f32 v221, v221, v134\n"
      "v_mfma_f32_16x16x32_bf16 v[156:159], v[96:99], v[104:107], v[60:63]\n"
      "ds_write_b32 v31, v206 offset:4608\n"
      "v_mfma_f32_16x16x32_bf16 v[184:187], v[100:103], v[104:107], v[26:29]\n"
      "s_waitcnt lgkmcnt(1)\n"
      "v_alignbyte_b32 v174, v175, v174, v92\n"
      "v_sub_f32 v110, v110, v134\n"
      "v_mfma_f32_16x16x16_bf16 v[210:213], v[146:147], v[126:127], v[210:213]\n"
      "v_sub_f32 v115, v115, v134\n"
      "v_exp_f32 v110, v110\n"
      "v_mfma_f32_16x16x16_bf16 v[188:191], v[154:155], v[126:127], v[188:191]\n"
      "v_exp_f32 v218, v218\n"
      "v_alignbyte_b32 v192, v192, v192, v92\n"
      "v_mfma_f32_16x16x16_bf16 v[156:159], v[202:203], v[126:127], v[156:159]\n"
      "v_alignbyte_b32 v175, v193, v175, v92\n"
      "v_exp_f32 v221, v221\n"
      "v_mfma_f32_16x16x16_bf16 v[184:187], v[204:205], v[126:127], v[184:187]\n"
      "v_exp_f32 v113, v113\n"
      "v_sub_f32 v176, v176, v134\n"
      "v_mfma_f32_16x16x16_bf16 v[210:213], v[124:125], v[136:137], v[210:213]\n"
      "v_exp_f32 v108, v108\n"
      "v_sub_f32 v111, v111, v134\n"
      "v_mfma_f32_16x16x16_bf16 v[188:191], v[124:125], v[222:223], v[188:191]\n"
      "v_exp_f32 v112, v112\n"
      "v_sub_f32 v219, v219, v134\n"
      "v_mfma_f32_16x16x16_bf16 v[156:159], v[124:125], v[174:175], v[156:159]\n"
      "v_exp_f32_e64 v115, v115\n"
      "v_cvt_pk_bf16_f32 v89, v220, v221\n"
      "v_mfma_f32_16x16x16_bf16 v[184:187], v[124:125], v[192:193], v[184:187]\n"
      "v_exp_f32 v111, v111\n"
      "v_sub_f32 v114, v114, v134\n"
      "v_mfma_f32_16x16x16_bf16 v[210:213], v[128:129], v[136:137], v[210:213]\n"
      "ds_read_b128 v[134:137], v2 offset:27904\n"
      "v_cvt_pk_bf16_f32 v112, v112, v113\n"
      "v_mfma_f32_16x16x16_bf16 v[188:191], v[128:129], v[222:223], v[188:191]\n"
      "ds_read_b128 v[220:223], v2 offset:30976\n"
      "v_exp_f32 v114, v114\n"
      "v_mfma_f32_16x16x16_bf16 v[156:159], v[128:129], v[174:175], v[156:159]\n"
      "ds_read_b128 v[104:107], v2 offset:34048\n"
      "v_exp_f32 v109, v109\n"
      "v_cvt_pk_bf16_f32 v113, v114, v115\n"
      "v_mfma_f32_16x16x16_bf16 v[184:187], v[128:129], v[192:193], v[184:187]\n"
      "v_cvt_pk_bf16_f32 v115, v110, v111\n"
      "v_max3_f32 v127, v210, v211, v212\n"
      "v_max3_f32 v127, v127, v190, v191\n"
      "v_exp_f32 v176, v176\n"
      "v_exp_f32 v219, v219\n"
      "v_max3_f32 v127, v127, v158, v159\n"
      "v_max3_f32 v206, v213, v188, v189\n"
      "v_cvt_pk_bf16_f32 v114, v108, v109\n"
      "v_max3_f32 v206, v206, v156, v157\n"
      "v_max3_f32 v127, v127, v206, v184\n"
      "s_waitcnt lgkmcnt(2)\n"
      "v_mfma_f32_16x16x32_bf16 v[162:165], a[228:231], v[134:137], v[162:165]\n"
      "s_waitcnt lgkmcnt(1)\n"
      "v_mov_b32 v109, v127\n"
      "v_cvt_pk_bf16_f32 v88, v218, v219\n"
      "v_mfma_f32_16x16x32_bf16 v[162:165], a[232:235], v[220:223], v[162:165]\n"
      "s_waitcnt lgkmcnt(0)\n"
      "v_permlane16_swap_b32_e64 v127, v109\n"
      "v_mfma_f32_16x16x32_bf16 v[162:165], a[236:239], v[104:107], v[162:165]\n"
      "ds_read2_b32 v[218:219], v140 offset0:2 offset1:10\n"
      "v_max_f32_e64 v127, v127, v109\n"
      "v_mfma_f32_16x16x32_bf16 v[180:183], a[252:255], v[134:137], v[180:183]\n"
      "ds_read2_b32 v[206:207], v140 offset0:8 offset1:9\n"
      "v_cvt_pk_bf16_f32 v90, v176, 0\n"
      "v_mfma_f32_16x16x32_bf16 v[180:183], v[224:227], v[220:223], v[180:183]\n"
      "ds_read2_b32 v[208:209], v140 offset0:0 offset1:1\n"
      "v_mov_b32 v109, v127\n"
      "v_mfma_f32_16x16x32_bf16 v[180:183], v[228:231], v[104:107], v[180:183]\n"
      "s_nop 0\n"
      "v_permlane32_swap_b32_e64 v127, v109\n"
      "v_mfma_f32_16x16x32_bf16 v[170:173], v[244:247], v[134:137], v[170:173]\n"
      "ds_read_u16 v78, v11\n"
      "v_max_f32 v127, v127, v109\n"
      "v_mfma_f32_16x16x32_bf16 v[170:173], v[248:251], v[220:223], v[170:173]\n"
      "v_sub_f32 v191, v191, v127\n"
      "v_sub_f32 v184, v184, v127\n"
      "v_sub_f32 v211, v211, v127\n"
      "v_mfma_f32_16x16x32_bf16 v[170:173], v[252:255], v[104:107], v[170:173]\n"
      "ds_read_u16 v82, v11 offset:32\n"
      "v_sub_f32 v157, v157, v127\n"
      "v_sub_f32 v212, v212, v127\n"
      "v_sub_f32 v210, v210, v127\n"
      "v_sub_f32 v190, v190, v127\n"
      "v_mfma_f32_16x16x32_bf16 v[108:111], v[32:35], v[112:115], 0\n"
      "s_waitcnt lgkmcnt(2)\n"
      "v_sub_f32 v213, v213, v127\n"
      "v_sub_f32_e64 v158, v158, v127\n"
      "v_mfma_f32_16x16x32_bf16 v[108:111], v[32:35], v[88:91], v[108:111]\n"
      "ds_read_u16 v86, v11 offset:64\n"
      "v_exp_f32 v212, v212\n"
      "v_exp_f32 v211, v211\n"
      "v_sub_f32 v189, v189, v127\n"
      "v_sub_f32 v156, v156, v127\n"
      "v_alignbyte_b32 v208, v209, v208, v92\n"
      "v_exp_f32 v210, v210\n"
      "v_alignbyte_b32 v209, v218, v209, v92\n"
      "v_alignbyte_b32 v206, v207, v206, v92\n"
      "v_sub_f32 v159, v159, v127\n"
      "v_mfma_f32_16x16x32_bf16 v[174:177], v[130:133], v[112:115], 0\n"
      "s_waitcnt lgkmcnt(1)\n"
      "v_sub_f32 v188, v188, v127\n"
      "v_exp_f32 v213, v213\n"
      "v_mfma_f32_16x16x32_bf16 v[174:177], v[76:79], v[88:91], v[174:177]\n"
      "ds_read2_b32 v[110:111], v140 offset0:16 offset1:17\n"
      "v_exp_f32 v188, v188\n"
      "v_mfma_f32_16x16x32_bf16 v[134:137], v[194:197], v[112:115], 0\n"
      "ds_read2_b32 v[126:127], v140 offset0:24 offset1:18\n"
      "v_exp_f32_e64 v189, v189\n"
      "v_mfma_f32_16x16x32_bf16 v[134:137], v[80:83], v[88:91], v[134:137]\n"
      "v_alignbyte_b32 v207, v219, v207, v92\n"
      "v_cvt_pk_bf16_f32 v210, v210, v211\n"
      "v_mfma_f32_16x16x32_bf16 v[218:221], v[214:217], v[112:115], 0\n"
      "s_waitcnt lgkmcnt(2)\n"
      "v_exp_f32 v190, v190\n"
      "v_mfma_f32_16x16x32_bf16 v[218:221], v[84:87], v[88:91], v[218:221]\n"
      "ds_read_u16 v39, v39\n"
      "v_exp_f32_e64 v191, v191\n"
      "v_cvt_pk_bf16_f32 v211, v212, v213\n"
      "v_cvt_pk_bf16_f32 v212, v188, v189\n"
      "v_mfma_f32_16x16x32_bf16 v[186:189], v[150:153], v[116:119], v[64:67]\n"
      "s_waitcnt lgkmcnt(2)\n"
      "v_cvt_pk_bf16_f32 v213, v190, v191\n"
      "v_alignbyte_b32 v110, v111, v110, v92\n"
      "v_mfma_f32_16x16x32_bf16 v[190:193], v[120:123], v[116:119], v[68:71]\n"
      "s_waitcnt lgkmcnt(1)\n"
      "v_alignbyte_b32 v111, v127, v111, v92\n"
      "v_alignbyte_b32 v126, v126, v126, v92\n"
      "v_mfma_f32_16x16x32_bf16 v[112:115], v[96:99], v[116:119], v[72:75]\n"
      "v_rcp_f32 v108, v108\n"
      "v_mfma_f32_16x16x32_bf16 v[104:107], v[100:103], v[116:119], v[28:31]\n"
      "v_exp_f32 v156, v156\n"
      "v_mfma_f32_16x16x16_bf16 v[186:189], v[146:147], v[138:139], v[186:189]\n"
      "v_exp_f32 v157, v157\n"
      "v_mul_f32 v175, v108, v175\n"
      "v_mfma_f32_16x16x16_bf16 v[190:193], v[154:155], v[138:139], v[190:193]\n"
      "v_exp_f32 v158, v158\n"
      "v_mul_f32 v219, v108, v219\n"
      "v_mfma_f32_16x16x16_bf16 v[112:115], v[202:203], v[138:139], v[112:115]\n"
      "v_exp_f32 v159, v159\n"
      "v_mul_f32 v137, v108, v137\n"
      "v_mfma_f32_16x16x16_bf16 v[104:107], v[204:205], v[138:139], v[104:107]\n"
      "v_mul_f32 v136, v108, v136\n"
      "v_cvt_pk_bf16_f32 v92, v156, v157\n"
      "v_mul_f32 v218, v108, v218\n"
      "v_mfma_f32_16x16x16_bf16 v[186:189], v[124:125], v[208:209], v[186:189]\n"
      "v_exp_f32 v184, v184\n"
      "v_fma_f32 v160, v162, v162, s52\n"
      "v_mfma_f32_16x16x16_bf16 v[190:193], v[124:125], v[206:207], v[190:193]\n"
      "v_mul_f32 v176, v108, v176\n"
      "v_cvt_pk_bf16_f32 v93, v158, v159\n"
      "v_mul_f32 v174, v108, v174\n"
      "v_mfma_f32_16x16x16_bf16 v[112:115], v[124:125], v[110:111], v[112:115]\n"
      "v_mul_f32 v161, v163, v163\n"
      "v_fmac_f32 v161, v165, v165\n"
      "v_mul_f32 v177, v108, v177\n"
      "v_mfma_f32_16x16x16_bf16 v[104:107], v[124:125], v[126:127], v[104:107]\n"
      "ds_read_b128 v[138:141], v2 offset:22016\n"
      "v_cvt_pk_bf16_f32 v218, v218, v219\n"
      "v_mfma_f32_16x16x16_bf16 v[186:189], v[128:129], v[208:209], v[186:189]\n"
      "v_fmac_f32_e64 v160, v164, v164\n"
      "v_cvt_pk_bf16_f32 v174, v174, v175\n"
      "v_cvt_pk_bf16_f32 v175, v176, v177\n"
      "v_mfma_f32_16x16x16_bf16 v[190:193], v[128:129], v[206:207], v[190:193]\n"
      "ds_read_b128 v[156:159], v2 offset:18944\n"
      "v_fmac_f32 v160, v180, v180\n"
      "v_mfma_f32_16x16x16_bf16 v[112:115], v[128:129], v[110:111], v[112:115]\n"
      "v_cvt_pk_bf16_f32 v177, v136, v137\n"
      "v_fmac_f32 v161, v181, v181\n"
      "v_cvt_pk_bf16_f32 v94, v184, 0\n"
      "v_mfma_f32_16x16x16_bf16 v[104:107], v[128:129], v[126:127], v[104:107]\n"
      "ds_read_b128 v[206:209], v2 offset:25088\n"
      "v_max3_f32 v129, v189, v190, v191\n"
      "v_max3_f32 v136, v186, v187, v188\n"
      "v_max3_f32 v136, v136, v192, v193\n"
      "v_max3_f32 v136, v136, v114, v115\n"
      "v_pk_mul_f32 v[134:135], v[134:135], v[108:109] op_sel_hi:[1,0]\n"
      "v_pk_mul_f32 v[220:221], v[220:221], v[108:109] op_sel_hi:[1,0]\n"
      "v_max3_f32 v129, v129, v112, v113\n"
      "v_max3_f32 v136, v136, v129, v104\n"
      "v_pk_fma_f32 v[160:161], v[182:183], v[182:183], v[160:161]\n"
      "v_cvt_pk_bf16_f32 v176, v134, v135\n"
      "v_mov_b32_e64 v108, v136\n"
      "v_pk_fma_f32 v[160:161], v[170:171], v[170:171], v[160:161]\n"
      "v_cvt_pk_bf16_f32 v219, v220, v221\n"
      "v_permlane16_swap_b32 v136, v108\n"
      "v_pk_fma_f32 v[160:161], v[172:173], v[172:173], v[160:161]\n"
      "s_waitcnt lgkmcnt(1)\n"
      "v_mfma_f32_16x16x32_bf16 v[126:129], a[216:219], v[156:159], 0\n"
      "ds_write_b128 v3, v[174:177]\n"
      "v_mfma_f32_16x16x32_bf16 v[126:129], a[220:223], v[138:141], v[126:129]\n"
      "s_waitcnt lgkmcnt(1)\n"
      "v_max_f32 v136, v136, v108\n"
      "v_mov_b32 v108, v136\n"
      "v_mfma_f32_16x16x32_bf16 v[126:129], a[224:227], v[206:209], v[126:129]\n"
      "s_nop 0\n"
      "v_permlane32_swap_b32 v136, v108\n"
      "v_mfma_f32_16x16x32_bf16 v[116:119], a[240:243], v[156:159], 0\n"
      "v_add_f32 v160, v160, v161\n"
      "v_max_f32 v136, v136, v108\n"
      "v_mfma_f32_16x16x32_bf16 v[116:119], a[244:247], v[138:141], v[116:119]\n"
      "v_sub_f32 v192, v192, v136\n"
      "v_sub_f32 v114, v114, v136\n"
      "v_mfma_f32_16x16x32_bf16 v[116:119], a[248:251], v[206:209], v[116:119]\n"
      "v_sub_f32 v193, v193, v136\n"
      "v_sub_f32 v190, v190, v136\n"
      "v_mfma_f32_16x16x32_bf16 v[108:111], v[232:235], v[156:159], 0\n"
      "v_sub_f32 v191, v191, v136\n"
      "v_sub_f32 v188, v188, v136\n"
      "v_mfma_f32_16x16x32_bf16 v[108:111], v[236:239], v[138:141], v[108:111]\n"
      "v_sub_f32 v104, v104, v136\n"
      "v_sub_f32 v115, v115, v136\n"
      "v_mfma_f32_16x16x32_bf16 v[108:111], v[240:243], v[206:209], v[108:111]\n"
      "v_sub_f32 v189, v189, v136\n"
      "v_sub_f32 v113, v113, v136\n"
      "v_mfma_f32_16x16x32_bf16 v[174:177], v[32:35], v[210:213], 0\n"
      "v_exp_f32 v191, v191\n"
      "v_sub_f32 v187, v187, v136\n"
      "v_sub_f32 v112, v112, v136\n"
      "v_sub_f32_e64 v186, v186, v136\n"
      "v_mfma_f32_16x16x32_bf16 v[174:177], v[32:35], v[92:95], v[174:177]\n"
      "ds_read_b128 v[134:137], v10\n"
      "v_exp_f32 v113, v113\n"
      "v_mfma_f32_16x16x32_bf16 v[220:223], v[130:133], v[210:213], 0\n"
      "v_exp_f32 v112, v112\n"
      "v_mfma_f32_16x16x32_bf16 v[220:223], v[76:79], v[92:95], v[220:223]\n"
      "v_exp_f32 v188, v188\n"
      "v_mfma_f32_16x16x32_bf16 v[138:141], v[194:197], v[210:213], 0\n"
      "v_exp_f32 v189, v189\n"
      "v_mfma_f32_16x16x32_bf16 v[138:141], v[80:83], v[92:95], v[138:141]\n"
      "v_rcp_f32 v174, v174\n"
      "v_mfma_f32_16x16x32_bf16 v[156:159], v[214:217], v[210:213], 0\n"
      "ds_read_b64 v[184:185], v10 offset:16\n"
      "v_exp_f32 v187, v187\n"
      "v_mfma_f32_16x16x32_bf16 v[156:159], v[84:87], v[92:95], v[156:159]\n"
      "s_waitcnt lgkmcnt(1)\n"
      "v_exp_f32 v186, v186\n"
      "v_mfma_f32_16x16x32_bf16 v[210:213], v[134:137], v[150:153], v[30:33]\n"
      "v_cvt_pk_bf16_f32 v88, v112, v113\n"
      "v_exp_f32_e64 v190, v190\n"
      "v_mfma_f32_16x16x32_bf16 v[176:179], v[134:137], v[120:123], v[30:33]\n"
      "v_cvt_pk_bf16_f32 v186, v186, v187\n"
      "v_cvt_pk_bf16_f32 v187, v188, v189\n"
      "v_mfma_f32_16x16x32_bf16 v[206:209], v[134:137], v[96:99], v[30:33]\n"
      "ds_read_b128 v[150:153], v2 offset:28160\n"
      "v_cvt_pk_bf16_f32 v188, v190, v191\n"
      "v_mfma_f32_16x16x32_bf16 v[96:99], v[134:137], v[100:103], v[30:33]\n"
      "ds_read_b128 v[134:137], v2 offset:31232\n"
      "s_waitcnt lgkmcnt(2)\n"
      "v_mfma_f32_16x16x16_bf16 v[210:213], v[184:185], v[146:147], v[210:213]\n"
      "ds_read_b128 v[100:103], v2 offset:34304\n"
      "v_mul_f32 v223, v174, v223\n"
      "v_mfma_f32_16x16x16_bf16 v[176:179], v[184:185], v[154:155], v[176:179]\n"
      "v_exp_f32 v115, v115\n"
      "v_exp_f32 v193, v193\n"
      "v_mfma_f32_16x16x16_bf16 v[206:209], v[184:185], v[202:203], v[206:209]\n"
      "v_exp_f32 v104, v104\n"
      "v_exp_f32 v114, v114\n"
      "v_exp_f32_e64 v192, v192\n"
      "v_pk_mul_f32 v[220:221], v[220:221], v[174:175] op_sel_hi:[1,0]\n"
      "v_pk_mul_f32 v[138:139], v[138:139], v[174:175] op_sel_hi:[1,0]\n"
      "v_pk_mul_f32 v[140:141], v[140:141], v[174:175] op_sel_hi:[1,0]\n"
      "v_pk_mul_f32 v[156:157], v[156:157], v[174:175] op_sel_hi:[1,0]\n"
      "v_mfma_f32_16x16x16_bf16 v[96:99], v[184:185], v[204:205], v[96:99]\n"
      "v_cndmask_b32 v210, v210, v176, s[58:59]\n"
      "v_lshlrev_b32 v39, 16, v39\n"
      "s_waitcnt lgkmcnt(1)\n"
      "v_mfma_f32_16x16x32_bf16 v[126:129], a[228:231], v[150:153], v[126:129]\n"
      "v_cndmask_b32 v210, v210, v206, s[60:61]\n"
      "v_mul_f32 v222, v174, v222\n"
      "v_mfma_f32_16x16x32_bf16 v[126:129], a[232:235], v[134:137], v[126:129]\n"
      "s_waitcnt lgkmcnt(0)\n"
      "v_cvt_pk_bf16_f32 v189, v192, v193\n"
      "v_cndmask_b32 v210, v210, v96, s[62:63]\n"
      "v_mfma_f32_16x16x32_bf16 v[126:129], a[236:239], v[100:103], v[126:129]\n"
      "v_fmac_f32_e64 v210, s53, v39\n"
      "v_cvt_pk_bf16_f32 v89, v114, v115\n"
      "v_mfma_f32_16x16x32_bf16 v[116:119], a[252:255], v[150:153], v[116:119]\n"
      "v_max_f32_dpp v193, v210, v210 row_ror:8 row_mask:0xf bank_mask:0xf\n"
      "v_cvt_pk_bf16_f32 v220, v220, v221\n"
      "v_mfma_f32_16x16x32_bf16 v[116:119], v[224:227], v[134:137], v[116:119]\n"
      "v_cvt_pk_bf16_f32 v221, v222, v223\n"
      "v_cvt_pk_bf16_f32 v90, v104, 0\n"
      "v_mfma_f32_16x16x32_bf16 v[116:119], v[228:231], v[100:103], v[116:119]\n"
      "v_max_f32_dpp v193, v193, v193 row_ror:4 row_mask:0xf bank_mask:0xf\n"
      "v_cvt_pk_bf16_f32 v222, v138, v139\n"
      "v_mfma_f32_16x16x32_bf16 v[108:111], v[244:247], v[150:153], v[108:111]\n"
      "v_fma_f32 v98, v126, v126, s52\n"
      "v_max_f32_dpp v193, v193, v193 row_ror:2 row_mask:0xf bank_mask:0xf\n"
      "v_mfma_f32_16x16x32_bf16 v[108:111], v[248:251], v[134:137], v[108:111]\n"
      "v_cvt_pk_bf16_f32 v223, v140, v141\n"
      "v_max_f32_dpp v193, v193, v193 row_ror:1 row_mask:0xf bank_mask:0xf\n"
      "v_mfma_f32_16x16x32_bf16 v[108:111], v[252:255], v[100:103], v[108:111]\n"
      "v_permlane16_swap_b32_e64 v148, v160\n"
      "v_mfma_f32_16x16x32_bf16 v[134:137], v[32:35], v[186:189], 0\n"
      "ds_write_b128 v3, v[220:223] offset:256\n"
      "v_mfma_f32_16x16x32_bf16 v[134:137], v[32:35], v[88:91], v[134:137]\n"
      "v_max_f32_dpp v193, v193, v193 row_bcast:15 row_mask:0xa bank_mask:0xf\n"
      "v_mul_f32 v99, v127, v127\n"
      "v_mfma_f32_16x16x32_bf16 v[100:103], v[130:133], v[186:189], 0\n"
      "v_add_f32 v148, v148, v160\n"
      "v_max_f32_dpp v193, v193, v193 row_bcast:31 row_mask:0xc bank_mask:0xf\n"
      "v_mfma_f32_16x16x32_bf16 v[100:103], v[76:79], v[88:91], v[100:103]\n"
      "v_readlane_b32 s72, v193, 63\n"
      "v_cvt_pk_bf16_f32 v156, v156, v157\n"
      "v_mfma_f32_16x16x32_bf16 v[190:193], v[194:197], v[186:189], 0\n"
      "v_rcp_f32 v134, v134\n"
      "v_mfma_f32_16x16x32_bf16 v[190:193], v[80:83], v[88:91], v[190:193]\n"
      "ds_write_b64 v4, v[218:219]\n"
      "v_subrev_f32 v210, s72, v210\n"
      "v_exp_f32_e64 v210, v210\n"
      "v_pk_fma_f32 v[98:99], v[128:129], v[128:129], v[98:99]\n"
      "v_pk_mul_f32 v[100:101], v[100:101], v[134:135] op_sel_hi:[1,0]\n"
      "v_pk_fma_f32 v[98:99], v[116:117], v[116:117], v[98:99]\n"
      "v_pk_mul_f32 v[102:103], v[102:103], v[134:135] op_sel_hi:[1,0]\n"
      "v_cvt_pk_bf16_f32 v210, v210, v210\n"
      "v_pk_mul_f32 v[190:191], v[190:191], v[134:135] op_sel_hi:[1,0]\n"
      "v_pk_mul_f32 v[192:193], v[192:193], v[134:135] op_sel_hi:[1,0]\n"
      "v_mfma_f32_16x16x32_bf16 v[218:221], v[214:217], v[186:189], 0\n"
      "ds_write_b16 v13, v210\n"
      "v_cvt_pk_bf16_f32 v100, v100, v101\n"
      "v_cvt_pk_bf16_f32 v101, v102, v103\n"
      "v_cvt_pk_bf16_f32 v102, v190, v191\n"
      "v_pk_fma_f32 v[98:99], v[118:119], v[118:119], v[98:99]\n"
      "v_cvt_pk_bf16_f32 v103, v192, v193\n"
      "v_mfma_f32_16x16x32_bf16 v[218:221], v[84:87], v[88:91], v[218:221]\n"
      "ds_write_b128 v3, v[100:103] offset:512\n"
      "v_pk_fma_f32 v[98:99], v[108:109], v[108:109], v[98:99]\n"
      "v_pk_fma_f32 v[98:99], v[110:111], v[110:111], v[98:99]\n"
      "v_pk_mul_f32 v[158:159], v[158:159], v[174:175] op_sel_hi:[1,0]\n"
      "v_add_f32 v98, v98, v99\n"
      "v_mov_b32 v160, v98\n"
      "v_cvt_pk_bf16_f32 v157, v158, v159\n"
      "ds_read_b128 v[174:177], v14\n"
      "ds_write_b64 v4, v[156:157] offset:256\n"
      "v_pk_mul_f32 v[218:219], v[218:219], v[134:135] op_sel_hi:[1,0]\n"
      "v_permlane16_swap_b32 v98, v160\n"
      "v_add_f32 v98, v98, v160\n"
      "v_pk_mul_f32 v[220:221], v[220:221], v[134:135] op_sel_hi:[1,0]\n"
      "v_cvt_pk_bf16_f32 v218, v218, v219\n"
      "v_cvt_pk_bf16_f32 v219, v220, v221\n"
      "ds_write_b64 v4, v[218:219] offset:512\n"
      "ds_read_b128 v[210:213], v14 offset:64\n"
      "v_permlane32_swap_b32 v148, v98\n"
      "v_add_f32 v148, v148, v98\n"
      "s_and_b32 s86, s86, -4\n"
      "ds_write_b32 v31, v148\n"
      "s_waitcnt lgkmcnt(4)\n"
      "v_mfma_f32_16x16x32_bf16 v[188:191], v[174:177], v[32:35], 0\n"
      "s_waitcnt lgkmcnt(1)\n"
      "v_mfma_f32_16x16x32_bf16 v[188:191], v[210:213], v[32:35], v[188:191]\n"
      "s_waitcnt lgkmcnt(0)\n"
      "s_barrier\n"
      "s_cbranch_vccz L_sel1_%=\n"
      "s_branch L_disp1_%=\n"
      "L_tail1_%=:\n"
      "s_nop 4\n"
      "ds_read_b128 v[96:99], v9\n"
      "v_mfma_f32_16x16x32_bf16 v[218:221], v[174:177], v[130:133], 0\n"
      "ds_read_b128 v[130:133], v2 offset:12288\n"
      "ds_read_b128 v[184:187], v2 offset:15616\n"
      "v_mfma_f32_16x16x32_bf16 v[218:221], v[210:213], v[76:79], v[218:221]\n"
      "ds_read_b128 v[204:207], v2 offset:3328\n"
      "v_mfma_f32_16x16x32_bf16 v[134:137], v[174:177], v[194:197], 0\n"
      "ds_read_b128 v[194:197], v2 offset:9216\n"
      "ds_read_b128 v[112:115], v2 offset:3072\n"
      "ds_read_b128 v[138:141], v2 offset:6144\n"
      "v_subrev_u32 v78, 115456, v9\n"
      "v_mfma_f32_16x16x32_bf16 v[134:137], v[210:213], v[80:83], v[134:137]\n"
      "ds_read_b128 v[150:153], v2\n"
      "v_subrev_u32 v220, 115456, v9\n"
      "v_mfma_f32_16x16x32_bf16 v[100:103], v[174:177], v[214:217], 0\n"
      "s_waitcnt lgkmcnt(7)\n"
      "v_add_f32 v96, v96, v97\n"
      "v_add_f32 v98, v98, v99\n"
      "v_add_f32 v96, v96, v98\n"
      "v_rsq_f32 v96, v96\n"
      "s_nop 0\n"
      "v_mul_f32 v96, 0x415db3d7, v96\n"
      "v_mov_b32 v98, v96\n"
      "v_cndmask_b32 v218, v218, v134, s[58:59]\n"
      "v_and_b32 v177, 0x300, v78\n"
      "v_permlane32_swap_b32 v96, v98\n"
      "v_mov_b32 v97, v96\n"
      "v_pk_mul_f32 v[126:127], v[126:127], v[98:99] op_sel_hi:[1,0]\n"
      "v_pk_mul_f32 v[116:117], v[116:117], v[98:99] op_sel_hi:[1,0]\n"
      "v_permlane16_swap_b32 v96, v97\n"
      "v_pk_mul_f32 v[144:145], v[144:145], v[96:97] op_sel_hi:[1,0]\n"
      "v_pk_mul_f32 v[182:183], v[182:183], v[96:97] op_sel:[0,1]\n"
      "v_pk_mul_f32 v[168:169], v[168:169], v[96:97] op_sel_hi:[1,0]\n"
      "v_pk_mul_f32 v[162:163], v[162:163], v[96:97] op_sel:[0,1]\n"
      "v_pk_mul_f32 v[118:119], v[118:119], v[98:99] op_sel_hi:[1,0]\n"
      "v_pk_mul_f32 v[164:165], v[164:165], v[96:97] op_sel:[0,1]\n"
      "v_pk_mul_f32 v[172:173], v[172:173], v[96:97] op_sel:[0,1]\n"
      "v_pk_mul_f32 v[108:109], v[108:109], v[98:99] op_sel_hi:[1,0]\n"
      "v_pk_mul_f32 v[166:167], v[166:167], v[96:97] op_sel_hi:[1,0]\n"
      "v_pk_mul_f32 v[170:171], v[170:171], v[96:97] op_sel:[0,1]\n"
      "v_pk_mul_f32 v[110:111], v[110:111], v[98:99] op_sel_hi:[1,0]\n"
      "v_cvt_pk_bf16_f32 v126, v126, v127\n"
      "v_pk_mul_f32 v[128:129], v[128:129], v[98:99] op_sel_hi:[1,0]\n"
      "v_pk_mul_f32 v[142:143], v[142:143], v[96:97] op_sel_hi:[1,0]\n"
      "v_cvt_pk_bf16_f32 v162, v162, v163\n"
      "v_cvt_pk_bf16_f32 v170, v170, v171\n"
      "v_cvt_pk_bf16_f32 v108, v108, v109\n"
      "v_cvt_pk_bf16_f32 v127, v128, v129\n"
      "v_cvt_pk_bf16_f32 v163, v164, v165\n"
      "v_pk_mul_f32 v[180:181], v[180:181], v[96:97] op_sel:[0,1]\n"
      "v_cvt_pk_bf16_f32 v142, v142, v143\n"
      "v_cvt_pk_bf16_f32 v165, v182, v183\n"
      "v_pk_mul_f32 v[198:199], v[198:199], v[96:97] op_sel_hi:[1,0]\n"
      "v_cvt_pk_bf16_f32 v129, v118, v119\n"
      "v_cvt_pk_bf16_f32 v109, v110, v111\n"
      "v_cvt_pk_bf16_f32 v166, v166, v167\n"
      "v_cvt_pk_bf16_f32 v167, v168, v169\n"
      "v_cvt_pk_bf16_f32 v128, v116, v117\n"
      "v_pk_mul_f32 v[200:201], v[200:201], v[96:97] op_sel_hi:[1,0]\n"
      "v_cvt_pk_bf16_f32 v143, v144, v145\n"
      "v_cvt_pk_bf16_f32 v171, v172, v173\n"
      "v_cvt_pk_bf16_f32 v164, v180, v181\n"
      "v_cvt_pk_bf16_f32 v168, v198, v199\n"
      "v_cvt_pk_bf16_f32 v169, v200, v201\n"
      "v_mfma_f32_16x16x32_bf16 v[100:103], v[210:213], v[84:87], v[100:103]\n"
      "ds_write_b128 v5, v[126:129] offset:30720\n"
      "ds_write_b64 v6, v[142:143] offset:18432\n"
      "ds_write_b128 v5, v[162:165] offset:24576\n"
      "ds_write_b128 v5, v[166:169] offset:18432\n"
      "ds_write_b64 v6, v[170:171] offset:24576\n"
      "ds_write_b64 v6, v[108:109] offset:30720\n"
      "s_waitcnt lgkmcnt(5)\n"
      "v_mfma_f32_16x16x32_bf16 v[166:169], a[216:219], v[150:153], 0\n"
      "ds_read_b128 v[162:165], v2 offset:15360\n"
      "v_subrev_u32 v84, 73728, v6\n"
      "v_mfma_f32_16x16x32_bf16 v[166:169], a[220:223], v[112:115], v[166:169]\n"
      "ds_read_b128 v[126:129], v2 offset:9472\n"
      "v_mfma_f32_16x16x32_bf16 v[198:201], a[240:243], v[150:153], 0\n"
      "ds_read_b128 v[116:119], v2 offset:256\n"
      "v_add_u32 v78, v78, v177\n"
      "v_mfma_f32_16x16x32_bf16 v[198:201], a[244:247], v[112:115], v[198:201]\n"
      "ds_read_b128 v[108:111], v7 offset:8192\n"
      "s_mul_i32 s92, s40, 2048\n"
      "v_mfma_f32_16x16x32_bf16 v[142:145], v[232:235], v[150:153], 0\n"
      "ds_read_b128 v[210:213], v7 offset:16384\n"
      "v_mfma_f32_16x16x32_bf16 v[142:145], v[236:239], v[112:115], v[142:145]\n"
      "v_mfma_f32_16x16x32_bf16 v[166:169], a[224:227], v[138:141], v[166:169]\n"
      "ds_read_b128 v[174:177], v7\n"
      "v_add_u32 v85, s92, v220\n"
      "v_mfma_f32_16x16x32_bf16 v[166:169], a[228:231], v[194:197], v[166:169]\n"
      "v_lshrrev_b32 v220, 4, v85\n"
      "v_mfma_f32_16x16x32_bf16 v[198:201], a[248:251], v[138:141], v[198:201]\n"
      "ds_read_b128 v[214:217], v2 offset:15872\n"
      "v_mfma_f32_16x16x32_bf16 v[198:201], a[252:255], v[194:197], v[198:201]\n"
      "v_mov_b32_dpp v188, v188 quad_perm:[0,0,0,0] row_mask:0xf bank_mask:0xf\n"
      "v_add_u32_e64 v86, 0, v220\n"
      "v_mfma_f32_16x16x32_bf16 v[142:145], v[240:243], v[138:141], v[142:145]\n"
      "v_ashrrev_i32 v152, 31, v37\n"
      "v_cndmask_b32 v218, v218, v100, s[60:61]\n"
      "v_mfma_f32_16x16x32_bf16 v[142:145], v[244:247], v[194:197], v[142:145]\n"
      "ds_read_b128 v[194:197], v2 offset:6656\n"
      "v_mfma_f32_16x16x32_bf16 v[166:169], a[232:235], v[130:133], v[166:169]\n"
      "s_waitcnt lgkmcnt(6)\n"
      "v_and_b32 v153, 0x3fffffff, v37\n"
      "v_mfma_f32_16x16x32_bf16 v[166:169], a[236:239], v[162:165], v[166:169]\n"
      "ds_read_b128 v[100:103], v2 offset:12800\n"
      "v_mfma_f32_16x16x32_bf16 v[198:201], v[224:227], v[130:133], v[198:201]\n"
      "v_mfma_f32_16x16x32_bf16 v[198:201], v[228:231], v[162:165], v[198:201]\n"
      "ds_read_b128 v[134:137], v2 offset:6400\n"
      "v_add_u32 v153, 1179648, v153\n"
      "v_mfma_f32_16x16x32_bf16 v[142:145], v[248:251], v[130:133], v[142:145]\n"
      "ds_read_b128 v[130:133], v2 offset:3584\n"
      "v_add_u32 v78, 149504, v78\n"
      "v_mfma_f32_16x16x32_bf16 v[142:145], v[252:255], v[162:165], v[142:145]\n"
      "s_waitcnt lgkmcnt(4)\n"
      "v_add_u32 v151, 64, v220\n"
      "v_mfma_f32_16x16x32_bf16 v[162:165], a[216:219], v[116:119], 0\n"
      "ds_read_b128 v[190:193], v2 offset:12544\n"
      "v_mul_u32_u24 v150, 2731, v86\n"
      "v_mfma_f32_16x16x32_bf16 v[162:165], a[220:223], v[204:207], v[162:165]\n"
      "ds_read_b128 v[220:223], v7 offset:4096\n"
      "v_mul_u32_u24 v82, 2731, v151\n"
      "v_mfma_f32_16x16x32_bf16 v[180:183], a[240:243], v[116:119], 0\n"
      "v_rcp_f32_e64 v188, v188\n"
      "v_mfma_f32_16x16x32_bf16 v[180:183], a[244:247], v[204:207], v[180:183]\n"
      "ds_read_b128 v[138:141], v2 offset:512\n"
      "v_mfma_f32_16x16x32_bf16 v[170:173], v[232:235], v[116:119], 0\n"
      "ds_read_b128 v[112:115], v2 offset:9728\n"
      "v_mfma_f32_16x16x32_bf16 v[170:173], v[236:239], v[204:207], v[170:173]\n"
      "s_waitcnt lgkmcnt(4)\n"
      "v_fma_f32 v96, v166, v166, s52\n"
      "v_mul_f32 v97, v167, v167\n"
      "v_mfma_f32_16x16x32_bf16 v[162:165], a[224:227], v[134:137], v[162:165]\n"
      "v_fmac_f32 v97, v169, v169\n"
      "v_fmac_f32 v96, v168, v168\n"
      "v_mfma_f32_16x16x32_bf16 v[162:165], a[228:231], v[126:129], v[162:165]\n"
      "buffer_store_dwordx4 v[174:177], v20, s[24:27], s85 offen sc1\n"
      "v_mfma_f32_16x16x32_bf16 v[180:183], a[248:251], v[134:137], v[180:183]\n"
      "s_nop 0\n"
      "ds_read_b128 v[174:177], v78\n"
      "v_lshrrev_b32 v82, 16, v82\n"
      "v_mfma_f32_16x16x32_bf16 v[180:183], a[252:255], v[126:129], v[180:183]\n"
      "v_lshrrev_b32 v150, 16, v150\n"
      "v_fmac_f32 v96, v198, v198\n"
      "v_mfma_f32_16x16x32_bf16 v[170:173], v[240:243], v[134:137], v[170:173]\n"
      "s_waitcnt lgkmcnt(1)\n"
      "v_fmac_f32 v97, v199, v199\n"
      "v_fmac_f32_e64 v96, v200, v200\n"
      "v_mfma_f32_16x16x32_bf16 v[170:173], v[244:247], v[126:129], v[170:173]\n"
      "buffer_store_dwordx4 v[220:223], v21, s[24:27], s85 offen sc1\n"
      "v_mfma_f32_16x16x32_bf16 v[162:165], a[232:235], v[190:193], v[162:165]\n"
      "v_and_b32 v152, 100270464, v152\n"
      "v_fmac_f32 v97, v201, v201\n"
      "v_mfma_f32_16x16x32_bf16 v[162:165], a[236:239], v[184:187], v[162:165]\n"
      "buffer_store_dwordx4 v[108:111], v22, s[24:27], s85 offen sc1\n"
      "v_mfma_f32_16x16x32_bf16 v[180:183], v[224:227], v[190:193], v[180:183]\n"
      "v_fmac_f32 v97, v143, v143\n"
      "v_mfma_f32_16x16x32_bf16 v[180:183], v[228:231], v[184:187], v[180:183]\n"
      "ds_read_b128 v[134:137], v7 offset:12288\n"
      "v_fmac_f32 v96, v142, v142\n"
      "v_mfma_f32_16x16x32_bf16 v[170:173], v[248:251], v[190:193], v[170:173]\n"
      "s_nop 0\n"
      "v_fma_f32 v98, v162, v162, s52\n"
      "v_fmac_f32 v96, v144, v144\n"
      "v_mfma_f32_16x16x32_bf16 v[170:173], v[252:255], v[184:187], v[170:173]\n"
      "v_mul_f32 v99, v163, v163\n"
      "v_mfma_f32_16x16x32_bf16 v[126:129], a[216:219], v[138:141], 0\n"
      "v_fmac_f32 v97, v145, v145\n"
      "v_mul_u32_u24 v206, 24, v82\n"
      "v_mfma_f32_16x16x32_bf16 v[126:129], a[220:223], v[130:133], v[126:129]\n"
      "ds_read_b128 v[220:223], v78 offset:6144\n"
      "v_fmac_f32_e64 v98, v164, v164\n"
      "v_mfma_f32_16x16x32_bf16 v[116:119], a[240:243], v[138:141], 0\n"
      "ds_read_b128 v[190:193], v78 offset:2048\n"
      "v_fmac_f32 v99, v165, v165\n"
      "v_mfma_f32_16x16x32_bf16 v[116:119], a[244:247], v[130:133], v[116:119]\n"
      "v_add_f32 v96, v96, v97\n"
      "v_mfma_f32_16x16x32_bf16 v[108:111], v[232:235], v[138:141], 0\n"
      "v_fmac_f32 v98, v180, v180\n"
      "v_mul_f32 v218, v188, v218\n"
      "v_mfma_f32_16x16x32_bf16 v[108:111], v[236:239], v[130:133], v[108:111]\n"
      "ds_read_b128 v[130:133], v78 offset:10240\n"
      "v_mfma_f32_16x16x32_bf16 v[126:129], a[224:227], v[194:197], v[126:129]\n"
      "v_fmac_f32 v99, v181, v181\n"
      "v_sub_u32 v153, v153, v152\n"
      "v_mfma_f32_16x16x32_bf16 v[126:129], a[228:231], v[112:115], v[126:129]\n"
      "v_fmac_f32_e64 v98, v182, v182\n"
      "v_cvt_pk_bf16_f32 v218, v218, v218\n"
      "v_mfma_f32_16x16x32_bf16 v[116:119], a[248:251], v[194:197], v[116:119]\n"
      "v_fmac_f32 v99, v183, v183\n"
      "v_fmac_f32 v98, v170, v170\n"
      "v_mfma_f32_16x16x32_bf16 v[116:119], a[252:255], v[112:115], v[116:119]\n"
      "ds_read_b128 v[186:189], v78 offset:4096\n"
      "v_mfma_f32_16x16x32_bf16 v[108:111], v[240:243], v[194:197], v[108:111]\n"
      "v_fmac_f32 v99, v171, v171\n"
      "v_fmac_f32 v99, v173, v173\n"
      "v_mfma_f32_16x16x32_bf16 v[108:111], v[244:247], v[112:115], v[108:111]\n"
      "ds_write_b16 v12, v218\n"
      "v_mfma_f32_16x16x32_bf16 v[126:129], a[232:235], v[100:103], v[126:129]\n"
      "s_waitcnt lgkmcnt(2)\n"
      "v_fmac_f32 v98, v172, v172\n"
      "v_add_f32_e64 v98, v98, v99\n"
      "v_mfma_f32_16x16x32_bf16 v[126:129], a[236:239], v[214:217], v[126:129]\n"
      "s_nop 0\n"
      "v_permlane16_swap_b32 v96, v98\n"
      "v_mfma_f32_16x16x32_bf16 v[116:119], v[224:227], v[100:103], v[116:119]\n"
      "buffer_store_dwordx4 v[134:137], v23, s[24:27], s85 offen sc1\n"
      "v_mfma_f32_16x16x32_bf16 v[116:119], v[228:231], v[214:217], v[116:119]\n"
      "v_sub_u32 v206, v151, v206\n"
      "v_add_f32 v96, v96, v98\n"
      "v_mfma_f32_16x16x32_bf16 v[108:111], v[248:251], v[100:103], v[108:111]\n"
      "v_mul_f32 v101, v127, v127\n"
      "v_fma_f32 v100, v126, v126, s52\n"
      "v_mfma_f32_16x16x32_bf16 v[108:111], v[252:255], v[214:217], v[108:111]\n"
      "v_fmac_f32 v101, v129, v129\n"
      "v_fmac_f32 v100, v128, v128\n"
      "v_mfma_f32_16x16x32_bf16 v[214:217], a[216:219], v[174:177], 0\n"
      "v_fmac_f32 v101, v117, v117\n"
      "v_fmac_f32 v100, v116, v116\n"
      "v_mfma_f32_16x16x32_bf16 v[214:217], a[220:223], v[190:193], v[214:217]\n"
      "v_fmac_f32 v101, v119, v119\n"
      "v_fmac_f32 v100, v118, v118\n"
      "v_mfma_f32_16x16x32_bf16 v[194:197], a[240:243], v[174:177], 0\n"
      "v_fmac_f32 v101, v109, v109\n"
      "v_fmac_f32 v100, v108, v108\n"
      "v_mfma_f32_16x16x32_bf16 v[194:197], a[244:247], v[190:193], v[194:197]\n"
      "v_fmac_f32 v101, v111, v111\n"
      "v_fmac_f32 v100, v110, v110\n"
      "v_mfma_f32_16x16x32_bf16 v[134:137], v[232:235], v[174:177], 0\n"
      "s_waitcnt lgkmcnt(0)\n"
      "v_add_f32 v100, v100, v101\n"
      "v_mov_b32 v98, v100\n"
      "v_mfma_f32_16x16x32_bf16 v[134:137], v[236:239], v[190:193], v[134:137]\n"
      "s_nop 0\n"
      "v_permlane16_swap_b32 v100, v98\n"
      "v_mfma_f32_16x16x32_bf16 v[214:217], a[224:227], v[186:189], v[214:217]\n"
      "ds_read_b128 v[174:177], v78 offset:8192\n"
      "v_add_f32 v100, v100, v98\n"
      "v_mfma_f32_16x16x32_bf16 v[214:217], a[228:231], v[220:223], v[214:217]\n"
      "s_nop 0\n"
      "v_permlane32_swap_b32 v96, v100\n"
      "v_add_f32 v96, v96, v100\n"
      "v_mfma_f32_16x16x32_bf16 v[194:197], a[248:251], v[186:189], v[194:197]\n"
      "ds_write_b32 v31, v96 offset:1024\n"
      "v_mfma_f32_16x16x32_bf16 v[194:197], a[252:255], v[220:223], v[194:197]\n"
      "buffer_store_dwordx4 v[210:213], v25, s[24:27], s85 offen sc1\n"
      "v_mfma_f32_16x16x32_bf16 v[134:137], v[240:243], v[186:189], v[134:137]\n"
      "v_mul_u32_u24_e64 v207, 24, v150\n"
      "v_readlane_b32 s85, v37, s42\n"
      "v_bfe_i32 v152, v37, 30, 1\n"
      "v_mfma_f32_16x16x32_bf16 v[134:137], v[244:247], v[220:223], v[134:137]\n"
      "s_or_b32 s95, s85, s48\n"
      "v_and_b32 v152, 196224, v152\n"
      "s_waitcnt lgkmcnt(1)\n"
      "v_mfma_f32_16x16x32_bf16 v[214:217], a[232:235], v[174:177], v[214:217]\n"
      "s_cmp_ge_u32 s95, 0x40000000\n"
      "s_cbranch_scc1 L_rare_27_%=\n"
      "L_back_28_%=:\n"
      "v_mfma_f32_16x16x32_bf16 v[214:217], a[236:239], v[130:133], v[214:217]\n"
      "s_waitcnt lgkmcnt(0)\n"
      "s_barrier\n"
      "ds_read_b128 v[210:213], v9 offset:1024\n"
      "ds_read_b128 v[188:191], v78 offset:256\n"
      "v_mfma_f32_16x16x32_bf16 v[194:197], v[224:227], v[174:177], v[194:197]\n"
      "ds_read_b128 v[218:221], v78 offset:2304\n"
      "v_mfma_f32_16x16x32_bf16 v[194:197], v[228:231], v[130:133], v[194:197]\n"
      "ds_read_b128 v[100:103], v7 offset:18432\n"
      "v_mul_f32_e64 v205, v215, v215\n"
      "v_mfma_f32_16x16x32_bf16 v[134:137], v[248:251], v[174:177], v[134:137]\n"
      "ds_read_b128 v[174:177], v7 offset:22528\n"
      "v_fma_f32 v204, v214, v214, s52\n"
      "v_mfma_f32_16x16x32_bf16 v[134:137], v[252:255], v[130:133], v[134:137]\n"
      "ds_read_b128 v[130:133], v78 offset:4352\n"
      "ds_read_b128 v[96:99], v78 offset:6400\n"
      "ds_read_b128 v[112:115], v7 offset:26624\n"
      "v_pk_fma_f32 v[204:205], v[216:217], v[216:217], v[204:205]\n"
      "v_pk_fma_f32 v[204:205], v[194:195], v[194:195], v[204:205]\n"
      "s_waitcnt lgkmcnt(7)\n"
      "v_add_f32 v212, v212, v213\n"
      "v_add_f32 v210, v210, v211\n"
      "v_add_f32 v210, v210, v212\n"
      "v_pk_fma_f32 v[204:205], v[196:197], v[196:197], v[204:205]\n"
      "s_waitcnt lgkmcnt(6)\n"
      "v_mfma_f32_16x16x32_bf16 v[138:141], a[216:219], v[188:191], 0\n"
      "s_waitcnt lgkmcnt(5)\n"
      "v_rsq_f32 v210, v210\n"
      "v_mfma_f32_16x16x32_bf16 v[138:141], a[220:223], v[218:221], v[138:141]\n"
      "s_waitcnt lgkmcnt(4)\n"
      "v_mul_f32 v210, 0x415db3d7, v210\n"
      "v_mov_b32_e64 v212, v210\n"
      "v_mfma_f32_16x16x32_bf16 v[184:187], a[240:243], v[188:191], 0\n"
      "s_nop 0\n"
      "v_permlane32_swap_b32 v210, v212\n"
      "v_mfma_f32_16x16x32_bf16 v[184:187], a[244:247], v[218:221], v[184:187]\n"
      "buffer_store_dwordx4 v[100:103], v20, s[24:27], s85 offen sc1\n"
      "v_mov_b32 v211, v210\n"
      "v_pk_mul_f32 v[108:109], v[108:109], v[212:213] op_sel_hi:[1,0]\n"
      "v_mfma_f32_16x16x32_bf16 v[100:103], v[232:235], v[188:191], 0\n"
      "s_waitcnt lgkmcnt(0)\n"
      "v_permlane16_swap_b32_e64 v210, v211\n"
      "v_mfma_f32_16x16x32_bf16 v[100:103], v[236:239], v[218:221], v[100:103]\n"
      "buffer_store_dwordx4 v[174:177], v21, s[24:27], s85 offen sc1\n"
      "v_pk_mul_f32 v[170:171], v[170:171], v[210:211] op_sel:[0,1]\n"
      "v_pk_mul_f32 v[168:169], v[168:169], v[210:211] op_sel_hi:[1,0]\n"
      "v_pk_mul_f32 v[142:143], v[142:143], v[210:211] op_sel_hi:[1,0]\n"
      "v_pk_mul_f32 v[144:145], v[144:145], v[210:211] op_sel_hi:[1,0]\n"
      "v_pk_mul_f32 v[180:181], v[180:181], v[210:211] op_sel:[0,1]\n"
      "v_pk_fma_f32 v[204:205], v[134:135], v[134:135], v[204:205]\n"
      "v_pk_mul_f32 v[164:165], v[164:165], v[210:211] op_sel:[0,1]\n"
      "v_pk_mul_f32 v[182:183], v[182:183], v[210:211] op_sel:[0,1]\n"
      "v_pk_mul_f32 v[110:111], v[110:111], v[212:213] op_sel_hi:[1,0]\n"
      "v_pk_mul_f32 v[200:201], v[200:201], v[210:211] op_sel_hi:[1,0]\n"
      "v_pk_mul_f32 v[172:173], v[172:173], v[210:211] op_sel:[0,1]\n"
      "v_pk_mul_f32 v[166:167], v[166:167], v[210:211] op_sel_hi:[1,0]\n"
      "v_pk_mul_f32 v[162:163], v[162:163], v[210:211] op_sel:[0,1]\n"
      "v_mfma_f32_16x16x32_bf16 v[138:141], a[224:227], v[130:133], v[138:141]\n"
      "ds_read_b128 v[174:177], v78 offset:10496\n"
      "v_cvt_pk_bf16_f32 v166, v166, v167\n"
      "v_mfma_f32_16x16x32_bf16 v[138:141], a[228:231], v[96:99], v[138:141]\n"
      "ds_read_b128 v[188:191], v78 offset:8448\n"
      "v_cvt_pk_bf16_f32 v142, v142, v143\n"
      "v_mfma_f32_16x16x32_bf16 v[184:187], a[248:251], v[130:133], v[184:187]\n"
      "buffer_store_dwordx4 v[112:115], v22, s[24:27], s85 offen sc1\n"
      "v_pk_mul_f32 v[198:199], v[198:199], v[210:211] op_sel_hi:[1,0]\n"
      "v_mfma_f32_16x16x32_bf16 v[184:187], a[252:255], v[96:99], v[184:187]\n"
      "ds_read_b128 v[218:221], v7 offset:30720\n"
      "v_cvt_pk_bf16_f32 v167, v168, v169\n"
      "v_cvt_pk_bf16_f32 v108, v108, v109\n"
      "v_cvt_pk_bf16_f32 v162, v162, v163\n"
      "v_cvt_pk_bf16_f32 v163, v164, v165\n"
      "v_pk_fma_f32 v[204:205], v[136:137], v[136:137], v[204:205]\n"
      "v_mfma_f32_16x16x32_bf16 v[100:103], v[240:243], v[130:133], v[100:103]\n"
      "v_cvt_pk_bf16_f32 v143, v144, v145\n"
      "v_cvt_pk_bf16_f32 v165, v182, v183\n"
      "v_cvt_pk_bf16_f32 v109, v110, v111\n"
      "v_mfma_f32_16x16x32_bf16 v[100:103], v[244:247], v[96:99], v[100:103]\n"
      "s_waitcnt lgkmcnt(1)\n"
      "v_cvt_pk_bf16_f32 v170, v170, v171\n"
      "v_cvt_pk_bf16_f32 v168, v198, v199\n"
      "v_cvt_pk_bf16_f32 v171, v172, v173\n"
      "v_mfma_f32_16x16x32_bf16 v[138:141], a[232:235], v[188:191], v[138:141]\n"
      "ds_write_b64 v6, v[142:143]\n"
      "v_mfma_f32_16x16x32_bf16 v[138:141], a[236:239], v[174:177], v[138:141]\n"
      "ds_write_b64 v6, v[170:171] offset:6144\n"
      "v_mfma_f32_16x16x32_bf16 v[184:187], v[224:227], v[188:191], v[184:187]\n"
      "ds_write_b64 v6, v[108:109] offset:12288\n"
      "v_cvt_pk_bf16_f32 v169, v200, v201\n"
      "v_mfma_f32_16x16x32_bf16 v[184:187], v[228:231], v[174:177], v[184:187]\n"
      "ds_write_b128 v5, v[166:169]\n"
      "v_cvt_pk_bf16_f32 v164, v180, v181\n"
      "v_mfma_f32_16x16x32_bf16 v[100:103], v[248:251], v[188:191], v[100:103]\n"
      "ds_read_b128 v[166:169], v7 offset:34816\n"
      "v_fma_f32 v180, v138, v138, s52\n"
      "v_mfma_f32_16x16x32_bf16 v[100:103], v[252:255], v[174:177], v[100:103]\n"
      "ds_write_b128 v5, v[162:165] offset:6144\n"
      "v_mul_f32 v181, v139, v139\n"
      "v_pk_fma_f32 v[180:181], v[140:141], v[140:141], v[180:181]\n"
      "v_pk_mul_f32 v[128:129], v[128:129], v[212:213] op_sel_hi:[1,0]\n"
      "v_pk_fma_f32 v[180:181], v[184:185], v[184:185], v[180:181]\n"
      "v_pk_mul_f32 v[126:127], v[126:127], v[212:213] op_sel_hi:[1,0]\n"
      "v_pk_fma_f32 v[180:181], v[186:187], v[186:187], v[180:181]\n"
      "v_pk_mul_f32 v[118:119], v[118:119], v[212:213] op_sel_hi:[1,0]\n"
      "v_pk_fma_f32 v[180:181], v[100:101], v[100:101], v[180:181]\n"
      "v_add_f32 v204, v204, v205\n"
      "v_pk_fma_f32 v[180:181], v[102:103], v[102:103], v[180:181]\n"
      "v_add_f32 v180, v180, v181\n"
      "v_cvt_pk_bf16_f32 v126, v126, v127\n"
      "v_pk_mul_f32 v[116:117], v[116:117], v[212:213] op_sel_hi:[1,0]\n"
      "v_permlane16_swap_b32 v204, v180\n"
      "v_cvt_pk_bf16_f32 v127, v128, v129\n"
      "v_add_f32 v204, v204, v180\n"
      "v_mov_b32 v180, v204\n"
      "v_cvt_pk_bf16_f32 v128, v116, v117\n"
      "v_cvt_pk_bf16_f32 v129, v118, v119\n"
      "v_permlane32_swap_b32 v204, v180\n"
      "v_add_f32 v204, v204, v180\n"
      "s_add_u32 s42, s42, 1\n"
      "ds_write_b128 v5, v[126:129] offset:12288\n"
      "ds_write_b32 v31, v204 offset:4608\n"
      "s_waitcnt lgkmcnt(2)\n"
      "buffer_store_dwordx4 v[218:221], v23, s[24:27], s85 offen sc1\n"
      "v_sub_u32 v207, v86, v207\n"
      "buffer_store_dwordx4 v[166:169], v25, s[24:27], s85 offen sc1\n"
      "v_and_b32_e64 v151, 7, v82\n"
      "s_waitcnt lgkmcnt(0)\n"
      "s_barrier\n"
      "ds_read_b128 v[166:169], v9 offset:4608\n"
      "v_lshl_add_u32 v82, v82, 2, 12\n"
      "v_and_b32 v86, 7, v150\n"
      "v_readlane_b32 s85, v37, s42\n"
      "ds_read_b128 v[126:129], v7\n"
      "s_or_b32 s95, s85, s48\n"
      "v_xor_b32_e64 v207, v207, v86\n"
      "s_cmp_ge_u32 s95, 0x40000000\n"
      "s_cbranch_scc1 L_rare_31_%=\n"
      "L_back_32_%=:\n"
      "ds_read_b128 v[116:119], v7 offset:4096\n"
      "ds_read_b128 v[218:221], v7 offset:16384\n"
      "ds_read_b128 v[174:177], v7 offset:8192\n"
      "ds_read_b128 v[162:165], v7 offset:12288\n"
      "s_waitcnt lgkmcnt(5)\n"
      "v_add_f32 v168, v168, v169\n"
      "v_add_f32 v166, v166, v167\n"
      "v_add_f32 v166, v166, v168\n"
      "v_rsq_f32 v166, v166\n"
      "s_nop 0\n"
      "v_mul_f32 v166, 0x415db3d7, v166\n"
      "v_mov_b32 v168, v166\n"
      "v_subrev_u32 v169, 73728, v5\n"
      "s_nop 0\n"
      "v_permlane32_swap_b32 v166, v168\n"
      "v_mov_b32 v167, v166\n"
      "v_sub_u32 v153, v153, v152\n"
      "v_lshl_add_u32 v150, v150, 2, 12\n"
      "v_permlane16_swap_b32 v166, v167\n"
      "v_pk_mul_f32 v[138:139], v[138:139], v[166:167] op_sel:[0,1]\n"
      "v_pk_mul_f32 v[100:101], v[100:101], v[166:167] op_sel:[0,1]\n"
      "v_pk_mul_f32 v[186:187], v[186:187], v[166:167] op_sel:[0,1]\n"
      "v_pk_mul_f32 v[216:217], v[216:217], v[166:167] op_sel_hi:[1,0]\n"
      "v_pk_mul_f32 v[214:215], v[214:215], v[166:167] op_sel_hi:[1,0]\n"
      "v_pk_mul_f32 v[136:137], v[136:137], v[166:167] op_sel_hi:[1,0]\n"
      "v_cvt_pk_bf16_f32 v100, v100, v101\n"
      "v_cvt_pk_bf16_f32 v138, v138, v139\n"
      "v_pk_mul_f32 v[140:141], v[140:141], v[166:167] op_sel:[0,1]\n"
      "v_pk_mul_f32 v[134:135], v[134:135], v[166:167] op_sel_hi:[1,0]\n"
      "v_cvt_pk_bf16_f32 v134, v134, v135\n"
      "v_pk_mul_f32 v[194:195], v[194:195], v[166:167] op_sel_hi:[1,0]\n"
      "v_cvt_pk_bf16_f32 v139, v140, v141\n"
      "v_pk_mul_f32 v[184:185], v[184:185], v[166:167] op_sel:[0,1]\n"
      "v_cvt_pk_bf16_f32 v214, v214, v215\n"
      "v_pk_mul_f32 v[196:197], v[196:197], v[166:167] op_sel_hi:[1,0]\n"
      "v_pk_mul_f32 v[102:103], v[102:103], v[166:167] op_sel:[0,1]\n"
      "v_cvt_pk_bf16_f32 v141, v186, v187\n"
      "v_cvt_pk_bf16_f32 v215, v216, v217\n"
      "v_cvt_pk_bf16_f32 v216, v194, v195\n"
      "v_cvt_pk_bf16_f32 v135, v136, v137\n"
      "v_cvt_pk_bf16_f32 v140, v184, v185\n"
      "v_cvt_pk_bf16_f32 v101, v102, v103\n"
      "s_add_u32 s42, s42, 1\n"
      "ds_write_b128 v169, v[138:141] offset:6144\n"
      "ds_write_b64 v84, v[134:135]\n"
      "ds_write_b64 v84, v[100:101] offset:6144\n"
      "v_cvt_pk_bf16_f32 v217, v196, v197\n"
      "s_waitcnt lgkmcnt(3)\n"
      "buffer_store_dwordx4 v[126:129], v20, s[24:27], s85 offen sc1\n"
      "ds_write_b128 v169, v[214:217]\n"
      "buffer_store_dwordx4 v[116:119], v21, s[24:27], s85 offen sc1\n"
      "v_xor_b32 v206, v206, v151\n"
      "s_nop 0\n"
      "ds_bpermute_b32 v116, v150, v153\n"
      "ds_bpermute_b32 v117, v82, v153\n"
      "buffer_store_dwordx4 v[174:177], v22, s[24:27], s85 offen sc1\n"
      "s_waitcnt lgkmcnt(2)\n"
      "s_barrier\n"
      "ds_read_b128 v[174:177], v85\n"
      "buffer_store_dwordx4 v[162:165], v23, s[24:27], s85 offen sc1\n"
      "s_nop 1\n"
      "ds_read_b128 v[162:165], v85 offset:1024\n"
      "buffer_store_dwordx4 v[218:221], v25, s[24:27], s85 offen sc1\n"
      "s_waitcnt lgkmcnt(2)\n"
      "v_lshl_add_u32 v116, v207, 4, v116\n"
      "v_lshl_add_u32 v117, v206, 4, v117\n"
      "s_waitcnt lgkmcnt(1)\n"
      "buffer_store_dwordx4 v[174:177], v116, s[24:27], 0 offen sc1\n"
      "s_waitcnt lgkmcnt(0)\n"
      "buffer_store_dwordx4 v[162:165], v117, s[24:27], 0 offen sc1\n"
      "s_endpgm\n"
      "L_tail0_%=:\n"
      "s_nop 4\n"
      "ds_read_b128 v[112:115], v9 offset:1024\n"
      "v_and_b32 v89, 0x3fffffff, v37\n"
      "v_mfma_f32_16x16x32_bf16 v[218:221], v[174:177], v[130:133], 0\n"
      "ds_read_b128 v[138:141], v2 offset:18432\n"
      "ds_read_b128 v[130:133], v2 offset:24576\n"
      "ds_read_b128 v[96:99], v2 offset:27904\n"
      "v_mfma_f32_16x16x32_bf16 v[218:221], v[210:213], v[76:79], v[218:221]\n"
      "ds_read_b128 v[184:187], v2 offset:27648\n"
      "ds_read_b128 v[204:207], v7 offset:30720\n"
      "v_mfma_f32_16x16x32_bf16 v[134:137], v[174:177], v[194:197], 0\n"
      "ds_read_b128 v[194:197], v7 offset:18432\n"
      "ds_read_b128 v[150:153], v2 offset:18688\n"
      "v_mfma_f32_16x16x32_bf16 v[134:137], v[210:213], v[80:83], v[134:137]\n"
      "ds_read_b128 v[156:159], v2 offset:21504\n"
      "v_subrev_u32 v76, 115456, v9\n"
      "s_waitcnt lgkmcnt(8)\n"
      "v_add_f32 v112, v112, v113\n"
      "v_add_f32 v114, v114, v115\n"
      "v_add_f32 v112, v112, v114\n"
      "v_rsq_f32_e64 v112, v112\n"
      "v_mfma_f32_16x16x32_bf16 v[100:103], v[174:177], v[214:217], 0\n"
      "s_waitcnt lgkmcnt(7)\n"
      "v_mul_f32 v112, 0x415db3d7, v112\n"
      "v_mov_b32 v114, v112\n"
      "v_cndmask_b32 v218, v218, v134, s[58:59]\n"
      "s_nop 0\n"
      "v_permlane32_swap_b32 v112, v114\n"
      "v_mov_b32_e64 v113, v112\n"
      "v_pk_mul_f32 v[110:111], v[110:111], v[114:115] op_sel_hi:[1,0]\n"
      "v_pk_mul_f32 v[126:127], v[126:127], v[114:115] op_sel_hi:[1,0]\n"
      "v_permlane16_swap_b32_e64 v112, v113\n"
      "v_pk_mul_f32 v[108:109], v[108:109], v[114:115] op_sel_hi:[1,0]\n"
      "v_pk_mul_f32 v[144:145], v[144:145], v[112:113] op_sel_hi:[1,0]\n"
      "v_pk_mul_f32 v[116:117], v[116:117], v[114:115] op_sel_hi:[1,0]\n"
      "v_pk_mul_f32 v[142:143], v[142:143], v[112:113] op_sel_hi:[1,0]\n"
      "v_pk_mul_f32 v[162:163], v[162:163], v[112:113] op_sel:[0,1]\n"
      "v_pk_mul_f32 v[118:119], v[118:119], v[114:115] op_sel_hi:[1,0]\n"
      "v_pk_mul_f32 v[166:167], v[166:167], v[112:113] op_sel_hi:[1,0]\n"
      "v_pk_mul_f32 v[128:129], v[128:129], v[114:115] op_sel_hi:[1,0]\n"
      "v_cvt_pk_bf16_f32 v108, v108, v109\n"
      "v_pk_mul_f32 v[164:165], v[164:165], v[112:113] op_sel:[0,1]\n"
      "v_pk_mul_f32 v[168:169], v[168:169], v[112:113] op_sel_hi:[1,0]\n"
      "v_pk_mul_f32 v[198:199], v[198:199], v[112:113] op_sel_hi:[1,0]\n"
      "v_cvt_pk_bf16_f32 v126, v126, v127\n"
      "v_pk_mul_f32 v[170:171], v[170:171], v[112:113] op_sel:[0,1]\n"
      "v_pk_mul_f32 v[200:201], v[200:201], v[112:113] op_sel_hi:[1,0]\n"
      "v_pk_mul_f32 v[180:181], v[180:181], v[112:113] op_sel:[0,1]\n"
      "v_cvt_pk_bf16_f32 v166, v166, v167\n"
      "v_pk_mul_f32 v[172:173], v[172:173], v[112:113] op_sel:[0,1]\n"
      "v_cvt_pk_bf16_f32 v170, v170, v171\n"
      "v_pk_mul_f32 v[182:183], v[182:183], v[112:113] op_sel:[0,1]\n"
      "v_cvt_pk_bf16_f32 v167, v168, v169\n"
      "v_cvt_pk_bf16_f32 v168, v198, v199\n"
      "v_cvt_pk_bf16_f32 v127, v128, v129\n"
      "v_cvt_pk_bf16_f32 v162, v162, v163\n"
      "v_cvt_pk_bf16_f32 v163, v164, v165\n"
      "v_cvt_pk_bf16_f32 v129, v118, v119\n"
      "v_cvt_pk_bf16_f32 v142, v142, v143\n"
      "v_cvt_pk_bf16_f32 v128, v116, v117\n"
      "v_cvt_pk_bf16_f32 v165, v182, v183\n"
      "v_cvt_pk_bf16_f32 v164, v180, v181\n"
      "v_cvt_pk_bf16_f32 v109, v110, v111\n"
      "v_cvt_pk_bf16_f32 v143, v144, v145\n"
      "v_cvt_pk_bf16_f32 v171, v172, v173\n"
      "v_cvt_pk_bf16_f32 v169, v200, v201\n"
      "v_mfma_f32_16x16x32_bf16 v[100:103], v[210:213], v[84:87], v[100:103]\n"
      "ds_write_b128 v5, v[126:129] offset:12288\n"
      "ds_write_b64 v6, v[108:109] offset:12288\n"
      "ds_write_b128 v5, v[162:165] offset:6144\n"
      "ds_write_b64 v6, v[142:143]\n"
      "ds_write_b64 v6, v[170:171] offset:6144\n"
      "ds_write_b128 v5, v[166:169]\n"
      "v_mfma_f32_16x16x32_bf16 v[166:169], a[216:219], v[138:141], 0\n"
      "ds_read_b128 v[162:165], v2 offset:30720\n"
      "s_waitcnt lgkmcnt(5)\n"
      "v_mfma_f32_16x16x32_bf16 v[166:169], a[220:223], v[156:159], v[166:169]\n"
      "ds_read_b128 v[180:183], v2 offset:33792\n"
      "v_mfma_f32_16x16x32_bf16 v[198:201], a[240:243], v[138:141], 0\n"
      "v_mov_b32_dpp v188, v188 quad_perm:[0,0,0,0] row_mask:0xf bank_mask:0xf\n"
      "v_mfma_f32_16x16x32_bf16 v[198:201], a[244:247], v[156:159], v[198:201]\n"
      "ds_read_b128 v[126:129], v2 offset:21760\n"
      "v_mfma_f32_16x16x32_bf16 v[142:145], v[232:235], v[138:141], 0\n"
      "ds_read_b128 v[134:137], v2 offset:34304\n"
      "v_mfma_f32_16x16x32_bf16 v[142:145], v[236:239], v[156:159], v[142:145]\n"
      "ds_read_b128 v[116:119], v2 offset:34048\n"
      "v_subrev_u32 v81, 115456, v9\n"
      "v_mfma_f32_16x16x32_bf16 v[166:169], a[224:227], v[130:133], v[166:169]\n"
      "ds_read_b128 v[138:141], v2 offset:31232\n"
      "v_add_u32 v89, 1179648, v89\n"
      "v_mfma_f32_16x16x32_bf16 v[166:169], a[228:231], v[184:187], v[166:169]\n"
      "s_mul_i32 s92, s40, 2048\n"
      "ds_read_b128 v[174:177], v2 offset:22016\n"
      "v_mfma_f32_16x16x32_bf16 v[198:201], a[248:251], v[130:133], v[198:201]\n"
      "ds_read_b128 v[108:111], v2 offset:30976\n"
      "v_mfma_f32_16x16x32_bf16 v[198:201], a[252:255], v[184:187], v[198:201]\n"
      "buffer_store_dwordx4 v[194:197], v20, s[24:27], s46 offen sc1\n"
      "v_mfma_f32_16x16x32_bf16 v[142:145], v[240:243], v[130:133], v[142:145]\n"
      "ds_read_b128 v[214:217], v2 offset:24832\n"
      "v_add_u32 v84, s92, v76\n"
      "v_mfma_f32_16x16x32_bf16 v[142:145], v[244:247], v[184:187], v[142:145]\n"
      "s_waitcnt lgkmcnt(7)\n"
      "v_ashrrev_i32 v85, 31, v37\n"
      "v_and_b32 v197, 0x300, v81\n"
      "v_mfma_f32_16x16x32_bf16 v[166:169], a[232:235], v[162:165], v[166:169]\n"
      "v_rcp_f32_e64 v188, v188\n"
      "v_mfma_f32_16x16x32_bf16 v[166:169], a[236:239], v[180:183], v[166:169]\n"
      "ds_read_b128 v[112:115], v7 offset:26624\n"
      "v_cndmask_b32 v218, v218, v100, s[60:61]\n"
      "v_mfma_f32_16x16x32_bf16 v[198:201], v[224:227], v[162:165], v[198:201]\n"
      "ds_read_b128 v[130:133], v7 offset:22528\n"
      "v_mfma_f32_16x16x32_bf16 v[198:201], v[228:231], v[180:183], v[198:201]\n"
      "v_add_u32_e64 v81, v81, v197\n"
      "v_mfma_f32_16x16x32_bf16 v[142:145], v[248:251], v[162:165], v[142:145]\n"
      "v_add_u32 v81, 149504, v81\n"
      "v_mfma_f32_16x16x32_bf16 v[142:145], v[252:255], v[180:183], v[142:145]\n"
      "ds_read_b128 v[194:197], v81\n"
      "v_mfma_f32_16x16x32_bf16 v[162:165], a[216:219], v[150:153], 0\n"
      "s_waitcnt lgkmcnt(3)\n"
      "v_mul_f32 v218, v188, v218\n"
      "v_lshrrev_b32 v76, 4, v84\n"
      "v_mfma_f32_16x16x32_bf16 v[162:165], a[220:223], v[126:129], v[162:165]\n"
      "v_add_u32 v78, 0, v76\n"
      "v_fma_f32 v208, v166, v166, s52\n"
      "v_mfma_f32_16x16x32_bf16 v[180:183], a[240:243], v[150:153], 0\n"
      "s_waitcnt lgkmcnt(1)\n"
      "v_mul_f32 v209, v167, v167\n"
      "v_mfma_f32_16x16x32_bf16 v[180:183], a[244:247], v[126:129], v[180:183]\n"
      "ds_read_b128 v[190:193], v2 offset:18944\n"
      "v_mfma_f32_16x16x32_bf16 v[170:173], v[232:235], v[150:153], 0\n"
      "buffer_store_dwordx4 v[130:133], v21, s[24:27], s46 offen sc1\n"
      "v_mfma_f32_16x16x32_bf16 v[170:173], v[236:239], v[126:129], v[170:173]\n"
      "v_mfma_f32_16x16x32_bf16 v[162:165], a[224:227], v[214:217], v[162:165]\n"
      "v_fmac_f32 v209, v169, v169\n"
      "v_mfma_f32_16x16x32_bf16 v[162:165], a[228:231], v[96:99], v[162:165]\n"
      "ds_read_b128 v[150:153], v81 offset:8192\n"
      "v_fmac_f32 v208, v168, v168\n"
      "v_mfma_f32_16x16x32_bf16 v[180:183], a[248:251], v[214:217], v[180:183]\n"
      "ds_read_b128 v[210:213], v2 offset:28160\n"
      "v_fmac_f32_e64 v209, v199, v199\n"
      "v_mfma_f32_16x16x32_bf16 v[180:183], a[252:255], v[96:99], v[180:183]\n"
      "buffer_store_dwordx4 v[112:115], v22, s[24:27], s46 offen sc1\n"
      "v_mfma_f32_16x16x32_bf16 v[170:173], v[240:243], v[214:217], v[170:173]\n"
      "ds_read_b128 v[214:217], v81 offset:6144\n"
      "v_mfma_f32_16x16x32_bf16 v[170:173], v[244:247], v[96:99], v[170:173]\n"
      "ds_read_b128 v[184:187], v2 offset:25088\n"
      "v_cvt_pk_bf16_f32 v218, v218, v218\n"
      "v_mfma_f32_16x16x32_bf16 v[162:165], a[232:235], v[108:111], v[162:165]\n"
      "v_add_u32_e64 v80, 64, v76\n"
      "v_mfma_f32_16x16x32_bf16 v[162:165], a[236:239], v[116:119], v[162:165]\n"
      "ds_read_b128 v[100:103], v81 offset:10240\n"
      "v_mul_u32_u24 v189, 2731, v78\n"
      "v_mfma_f32_16x16x32_bf16 v[180:183], v[224:227], v[108:111], v[180:183]\n"
      "ds_write_b16 v12, v218 offset:16\n"
      "v_mfma_f32_16x16x32_bf16 v[180:183], v[228:231], v[116:119], v[180:183]\n"
      "v_mul_u32_u24 v76, 2731, v80\n"
      "v_mfma_f32_16x16x32_bf16 v[170:173], v[248:251], v[108:111], v[170:173]\n"
      "s_nop 0\n"
      "v_fma_f32 v114, v162, v162, s52\n"
      "v_mul_f32 v115, v163, v163\n"
      "v_mfma_f32_16x16x32_bf16 v[170:173], v[252:255], v[116:119], v[170:173]\n"
      "s_waitcnt lgkmcnt(2)\n"
      "v_lshrrev_b32 v76, 16, v76\n"
      "v_fmac_f32 v208, v198, v198\n"
      "v_mfma_f32_16x16x32_bf16 v[126:129], a[216:219], v[190:193], 0\n"
      "v_fmac_f32 v115, v165, v165\n"
      "v_fmac_f32 v208, v200, v200\n"
      "v_mfma_f32_16x16x32_bf16 v[126:129], a[220:223], v[174:177], v[126:129]\n"
      "v_fmac_f32 v114, v164, v164\n"
      "v_fmac_f32 v114, v180, v180\n"
      "v_mfma_f32_16x16x32_bf16 v[116:119], a[240:243], v[190:193], 0\n"
      "v_fmac_f32 v209, v201, v201\n"
      "v_fmac_f32_e64 v115, v181, v181\n"
      "v_mfma_f32_16x16x32_bf16 v[116:119], a[244:247], v[174:177], v[116:119]\n"
      "buffer_store_dwordx4 v[204:207], v23, s[24:27], s46 offen sc1\n"
      "v_mfma_f32_16x16x32_bf16 v[108:111], v[232:235], v[190:193], 0\n"
      "v_fmac_f32 v114, v182, v182\n"
      "v_fmac_f32 v208, v142, v142\n"
      "v_mfma_f32_16x16x32_bf16 v[108:111], v[236:239], v[174:177], v[108:111]\n"
      "v_subrev_u32 v96, 73728, v6\n"
      "v_fmac_f32 v209, v143, v143\n"
      "v_mfma_f32_16x16x32_bf16 v[126:129], a[224:227], v[184:187], v[126:129]\n"
      "ds_read_b128 v[190:193], v81 offset:2048\n"
      "v_lshrrev_b32 v189, 16, v189\n"
      "v_mfma_f32_16x16x32_bf16 v[126:129], a[228:231], v[210:213], v[126:129]\n"
      "v_fmac_f32_e64 v115, v183, v183\n"
      "v_and_b32 v85, 100270464, v85\n"
      "v_mfma_f32_16x16x32_bf16 v[116:119], a[248:251], v[184:187], v[116:119]\n"
      "ds_read_b128 v[204:207], v81 offset:4096\n"
      "v_fmac_f32 v115, v171, v171\n"
      "v_mfma_f32_16x16x32_bf16 v[116:119], a[252:255], v[210:213], v[116:119]\n"
      "v_fmac_f32 v114, v170, v170\n"
      "v_fmac_f32_e64 v114, v172, v172\n"
      "v_mfma_f32_16x16x32_bf16 v[108:111], v[240:243], v[184:187], v[108:111]\n"
      "v_fmac_f32 v115, v173, v173\n"
      "v_sub_u32 v89, v89, v85\n"
      "v_mfma_f32_16x16x32_bf16 v[108:111], v[244:247], v[210:213], v[108:111]\n"
      "ds_read_b128 v[184:187], v7 offset:34816\n"
      "v_fmac_f32 v209, v145, v145\n"
      "v_mfma_f32_16x16x32_bf16 v[126:129], a[232:235], v[138:141], v[126:129]\n"
      "v_subrev_u32 v97, 73728, v5\n"
      "v_add_f32 v114, v114, v115\n"
      "v_mfma_f32_16x16x32_bf16 v[126:129], a[236:239], v[134:137], v[126:129]\n"
      "v_fmac_f32 v208, v144, v144\n"
      "v_add_f32 v208, v208, v209\n"
      "v_mfma_f32_16x16x32_bf16 v[116:119], v[224:227], v[138:141], v[116:119]\n"
      "s_nop 0\n"
      "v_permlane16_swap_b32 v208, v114\n"
      "v_mfma_f32_16x16x32_bf16 v[116:119], v[228:231], v[134:137], v[116:119]\n"
      "s_nop 1\n"
      "v_fma_f32 v212, v126, v126, s52\n"
      "v_mfma_f32_16x16x32_bf16 v[108:111], v[248:251], v[138:141], v[108:111]\n"
      "v_mul_f32 v213, v127, v127\n"
      "v_fmac_f32_e64 v213, v129, v129\n"
      "v_mfma_f32_16x16x32_bf16 v[108:111], v[252:255], v[134:137], v[108:111]\n"
      "v_fmac_f32 v212, v128, v128\n"
      "v_fmac_f32 v212, v116, v116\n"
      "v_mfma_f32_16x16x32_bf16 v[174:177], a[216:219], v[194:197], 0\n"
      "s_waitcnt lgkmcnt(0)\n"
      "v_fmac_f32 v213, v117, v117\n"
      "v_fmac_f32 v212, v118, v118\n"
      "v_mfma_f32_16x16x32_bf16 v[174:177], a[220:223], v[190:193], v[174:177]\n"
      "v_add_f32 v208, v208, v114\n"
      "v_fmac_f32 v213, v119, v119\n"
      "v_mfma_f32_16x16x32_bf16 v[156:159], a[240:243], v[194:197], 0\n"
      "v_fmac_f32 v212, v108, v108\n"
      "v_fmac_f32 v213, v109, v109\n"
      "v_mfma_f32_16x16x32_bf16 v[156:159], a[244:247], v[190:193], v[156:159]\n"
      "v_fmac_f32 v212, v110, v110\n"
      "v_fmac_f32 v213, v111, v111\n"
      "v_mfma_f32_16x16x32_bf16 v[138:141], v[232:235], v[194:197], 0\n"
      "v_add_f32 v212, v212, v213\n"
      "v_mov_b32 v114, v212\n"
      "v_mfma_f32_16x16x32_bf16 v[138:141], v[236:239], v[190:193], v[138:141]\n"
      "s_nop 0\n"
      "v_permlane16_swap_b32_e64 v212, v114\n"
      "v_mfma_f32_16x16x32_bf16 v[174:177], a[224:227], v[204:207], v[174:177]\n"
      "v_add_f32 v212, v212, v114\n"
      "v_mfma_f32_16x16x32_bf16 v[174:177], a[228:231], v[214:217], v[174:177]\n"
      "s_nop 0\n"
      "v_permlane32_swap_b32_e64 v208, v212\n"
      "v_mfma_f32_16x16x32_bf16 v[156:159], a[248:251], v[204:207], v[156:159]\n"
      "v_add_f32_e64 v208, v208, v212\n"
      "v_mfma_f32_16x16x32_bf16 v[156:159], a[252:255], v[214:217], v[156:159]\n"
      "ds_write_b32 v31, v208\n"
      "v_mfma_f32_16x16x32_bf16 v[138:141], v[240:243], v[204:207], v[138:141]\n"
      "buffer_store_dwordx4 v[184:187], v25, s[24:27], s46 offen sc1\n"
      "v_mfma_f32_16x16x32_bf16 v[138:141], v[244:247], v[214:217], v[138:141]\n"
      "v_mfma_f32_16x16x32_bf16 v[174:177], a[232:235], v[150:153], v[174:177]\n"
      "v_readlane_b32 s46, v37, s42\n"
      "s_nop 0\n"
      "s_or_b32 s95, s46, s48\n"
      "v_mfma_f32_16x16x32_bf16 v[174:177], a[236:239], v[100:103], v[174:177]\n"
      "s_cmp_ge_u32 s95, 0x40000000\n"
      "s_cbranch_scc1 L_rare_35_%=\n"
      "L_back_36_%=:\n"
      "v_mfma_f32_16x16x32_bf16 v[156:159], v[224:227], v[150:153], v[156:159]\n"
      "s_waitcnt lgkmcnt(0)\n"
      "s_barrier\n"
      "ds_read_b128 v[184:187], v9\n"
      "v_mfma_f32_16x16x32_bf16 v[156:159], v[228:231], v[100:103], v[156:159]\n"
      "ds_read_b128 v[204:207], v7\n"
      "ds_read_b128 v[190:193], v81 offset:256\n"
      "ds_read_b128 v[212:215], v81 offset:2304\n"
      "v_mul_f32_e64 v221, v175, v175\n"
      "v_fma_f32 v220, v174, v174, s52\n"
      "v_mfma_f32_16x16x32_bf16 v[138:141], v[248:251], v[150:153], v[138:141]\n"
      "ds_read_b128 v[194:197], v81 offset:6400\n"
      "ds_read_b128 v[112:115], v7 offset:8192\n"
      "ds_read_b128 v[150:153], v81 offset:4352\n"
      "ds_read_b128 v[216:219], v7 offset:12288\n"
      "v_pk_fma_f32 v[220:221], v[176:177], v[176:177], v[220:221]\n"
      "v_mfma_f32_16x16x32_bf16 v[138:141], v[252:255], v[100:103], v[138:141]\n"
      "ds_read_b128 v[98:101], v7 offset:4096\n"
      "s_waitcnt lgkmcnt(8)\n"
      "v_add_f32 v186, v186, v187\n"
      "v_add_f32_e64 v184, v184, v185\n"
      "v_add_f32_e64 v184, v184, v186\n"
      "v_pk_fma_f32 v[220:221], v[156:157], v[156:157], v[220:221]\n"
      "v_pk_fma_f32 v[220:221], v[158:159], v[158:159], v[220:221]\n"
      "v_rsq_f32_e64 v184, v184\n"
      "v_pk_fma_f32 v[220:221], v[138:139], v[138:139], v[220:221]\n"
      "v_pk_fma_f32 v[220:221], v[140:141], v[140:141], v[220:221]\n"
      "s_waitcnt lgkmcnt(6)\n"
      "v_mfma_f32_16x16x32_bf16 v[130:133], a[216:219], v[190:193], 0\n"
      "s_waitcnt lgkmcnt(5)\n"
      "v_mul_f32 v184, 0x415db3d7, v184\n"
      "v_mov_b32_e64 v186, v184\n"
      "v_mfma_f32_16x16x32_bf16 v[130:133], a[220:223], v[212:215], v[130:133]\n"
      "s_nop 0\n"
      "v_permlane32_swap_b32 v184, v186\n"
      "v_mfma_f32_16x16x32_bf16 v[134:137], a[240:243], v[190:193], 0\n"
      "ds_read_b128 v[208:211], v81 offset:8448\n"
      "v_mov_b32 v185, v184\n"
      "v_mfma_f32_16x16x32_bf16 v[134:137], a[244:247], v[212:215], v[134:137]\n"
      "buffer_store_dwordx4 v[204:207], v20, s[24:27], s46 offen sc1\n"
      "v_pk_mul_f32 v[110:111], v[110:111], v[186:187] op_sel_hi:[1,0]\n"
      "s_nop 0\n"
      "v_mfma_f32_16x16x32_bf16 v[204:207], v[232:235], v[190:193], 0\n"
      "s_waitcnt lgkmcnt(1)\n"
      "v_permlane16_swap_b32 v184, v185\n"
      "v_mfma_f32_16x16x32_bf16 v[204:207], v[236:239], v[212:215], v[204:207]\n"
      "buffer_store_dwordx4 v[98:101], v21, s[24:27], s46 offen sc1\n"
      "v_pk_mul_f32 v[198:199], v[198:199], v[184:185] op_sel_hi:[1,0]\n"
      "v_pk_mul_f32 v[182:183], v[182:183], v[184:185] op_sel:[0,1]\n"
      "v_pk_mul_f32 v[180:181], v[180:181], v[184:185] op_sel:[0,1]\n"
      "v_pk_mul_f32 v[170:171], v[170:171], v[184:185] op_sel:[0,1]\n"
      "v_pk_mul_f32 v[162:163], v[162:163], v[184:185] op_sel:[0,1]\n"
      "v_pk_mul_f32 v[166:167], v[166:167], v[184:185] op_sel_hi:[1,0]\n"
      "v_pk_mul_f32 v[108:109], v[108:109], v[186:187] op_sel_hi:[1,0]\n"
      "v_pk_mul_f32 v[142:143], v[142:143], v[184:185] op_sel_hi:[1,0]\n"
      "v_cvt_pk_bf16_f32 v142, v142, v143\n"
      "v_pk_mul_f32 v[116:117], v[116:117], v[186:187] op_sel_hi:[1,0]\n"
      "v_pk_mul_f32 v[164:165], v[164:165], v[184:185] op_sel:[0,1]\n"
      "v_pk_mul_f32 v[172:173], v[172:173], v[184:185] op_sel:[0,1]\n"
      "v_pk_mul_f32 v[126:127], v[126:127], v[186:187] op_sel_hi:[1,0]\n"
      "v_pk_mul_f32 v[200:201], v[200:201], v[184:185] op_sel_hi:[1,0]\n"
      "v_pk_mul_f32 v[144:145], v[144:145], v[184:185] op_sel_hi:[1,0]\n"
      "v_mfma_f32_16x16x32_bf16 v[130:133], a[224:227], v[150:153], v[130:133]\n"
      "v_cvt_pk_bf16_f32 v143, v144, v145\n"
      "v_add_f32_e64 v220, v220, v221\n"
      "v_cvt_pk_bf16_f32 v166, v166, v167\n"
      "v_mfma_f32_16x16x32_bf16 v[130:133], a[228:231], v[194:197], v[130:133]\n"
      "s_waitcnt lgkmcnt(0)\n"
      "v_cvt_pk_bf16_f32 v162, v162, v163\n"
      "v_cvt_pk_bf16_f32 v170, v170, v171\n"
      "v_mfma_f32_16x16x32_bf16 v[134:137], a[248:251], v[150:153], v[134:137]\n"
      "ds_read_b128 v[212:215], v81 offset:10496\n"
      "v_cvt_pk_bf16_f32 v171, v172, v173\n"
      "v_mfma_f32_16x16x32_bf16 v[134:137], a[252:255], v[194:197], v[134:137]\n"
      "ds_write_b64 v6, v[142:143] offset:18432\n"
      "v_cvt_pk_bf16_f32 v163, v164, v165\n"
      "v_mfma_f32_16x16x32_bf16 v[204:207], v[240:243], v[150:153], v[204:207]\n"
      "buffer_store_dwordx4 v[112:115], v22, s[24:27], s46 offen sc1\n"
      "v_pk_mul_f32 v[168:169], v[168:169], v[184:185] op_sel_hi:[1,0]\n"
      "v_mfma_f32_16x16x32_bf16 v[204:207], v[244:247], v[194:197], v[204:207]\n"
      "ds_write_b64 v6, v[170:171] offset:24576\n"
      "v_mfma_f32_16x16x32_bf16 v[130:133], a[232:235], v[208:211], v[130:133]\n"
      "s_waitcnt lgkmcnt(2)\n"
      "v_cvt_pk_bf16_f32 v167, v168, v169\n"
      "v_cvt_pk_bf16_f32 v108, v108, v109\n"
      "v_mfma_f32_16x16x32_bf16 v[130:133], a[236:239], v[212:215], v[130:133]\n"
      "v_cvt_pk_bf16_f32 v165, v182, v183\n"
      "v_cvt_pk_bf16_f32 v164, v180, v181\n"
      "v_cvt_pk_bf16_f32 v168, v198, v199\n"
      "v_mfma_f32_16x16x32_bf16 v[134:137], v[224:227], v[208:211], v[134:137]\n"
      "ds_write_b128 v5, v[162:165] offset:24576\n"
      "v_cvt_pk_bf16_f32 v169, v200, v201\n"
      "v_mfma_f32_16x16x32_bf16 v[134:137], v[228:231], v[212:215], v[134:137]\n"
      "ds_read_b128 v[194:197], v7 offset:16384\n"
      "v_cvt_pk_bf16_f32 v109, v110, v111\n"
      "v_mfma_f32_16x16x32_bf16 v[204:207], v[248:251], v[208:211], v[204:207]\n"
      "ds_write_b64 v6, v[108:109] offset:30720\n"
      "v_mfma_f32_16x16x32_bf16 v[204:207], v[252:255], v[212:215], v[204:207]\n"
      "ds_write_b128 v5, v[166:169] offset:18432\n"
      "v_cvt_pk_bf16_f32 v126, v126, v127\n"
      "v_mul_f32_e64 v109, v131, v131\n"
      "v_pk_mul_f32 v[118:119], v[118:119], v[186:187] op_sel_hi:[1,0]\n"
      "v_fma_f32 v108, v130, v130, s52\n"
      "v_pk_fma_f32 v[108:109], v[132:133], v[132:133], v[108:109]\n"
      "v_pk_fma_f32 v[108:109], v[134:135], v[134:135], v[108:109]\n"
      "v_pk_mul_f32 v[128:129], v[128:129], v[186:187] op_sel_hi:[1,0]\n"
      "v_pk_fma_f32 v[108:109], v[136:137], v[136:137], v[108:109]\n"
      "v_pk_fma_f32 v[108:109], v[204:205], v[204:205], v[108:109]\n"
      "v_pk_fma_f32 v[108:109], v[206:207], v[206:207], v[108:109]\n"
      "v_add_f32 v108, v108, v109\n"
      "v_cvt_pk_bf16_f32 v127, v128, v129\n"
      "v_cvt_pk_bf16_f32 v128, v116, v117\n"
      "v_permlane16_swap_b32 v220, v108\n"
      "v_add_f32 v220, v220, v108\n"
      "v_mov_b32 v108, v220\n"
      "v_cvt_pk_bf16_f32 v129, v118, v119\n"
      "ds_write_b128 v5, v[126:129] offset:30720\n"
      "v_permlane32_swap_b32 v220, v108\n"
      "v_add_f32 v220, v220, v108\n"
      "s_add_u32 s42, s42, 1\n"
      "ds_write_b32 v31, v220 offset:4608\n"
      "buffer_store_dwordx4 v[216:219], v23, s[24:27], s46 offen sc1\n"
      "v_mul_u32_u24 v201, 24, v76\n"
      "v_mul_u32_u24 v127, 24, v189\n"
      "v_sub_u32 v201, v80, v201\n"
      "v_add_u32 v12, 32, v12\n"
      "v_bfe_i32 v85, v37, 30, 1\n"
      "s_waitcnt lgkmcnt(2)\n"
      "buffer_store_dwordx4 v[194:197], v25, s[24:27], s46 offen sc1\n"
      "v_readlane_b32 s46, v37, s42\n"
      "s_waitcnt lgkmcnt(0)\n"
      "s_barrier\n"
      "ds_read_b128 v[108:111], v9 offset:4608\n"
      "v_sub_u32_e64 v127, v78, v127\n"
      "ds_read_b128 v[192:195], v7 offset:18432\n"
      "v_and_b32 v85, 196224, v85\n"
      "v_sub_u32 v89, v89, v85\n"
      "v_and_b32_e64 v78, 7, v189\n"
      "ds_read_b128 v[184:187], v7 offset:26624\n"
      "s_or_b32 s95, s46, s48\n"
      "ds_read_b128 v[196:199], v7 offset:30720\n"
      "ds_read_b128 v[214:217], v7 offset:22528\n"
      "v_and_b32 v80, 7, v76\n"
      "s_cmp_ge_u32 s95, 0x40000000\n"
      "s_cbranch_scc1 L_rare_39_%=\n"
      "L_back_40_%=:\n"
      "s_waitcnt lgkmcnt(4)\n"
      "v_add_f32 v110, v110, v111\n"
      "v_add_f32_e64 v108, v108, v109\n"
      "v_add_f32_e64 v108, v108, v110\n"
      "v_rsq_f32_e64 v108, v108\n"
      "s_nop 0\n"
      "v_mul_f32 v108, 0x415db3d7, v108\n"
      "v_mov_b32 v110, v108\n"
      "v_lshl_add_u32 v189, v189, 2, 12\n"
      "s_nop 0\n"
      "v_permlane32_swap_b32_e64 v108, v110\n"
      "v_mov_b32 v109, v108\n"
      "v_lshl_add_u32 v76, v76, 2, 12\n"
      "s_nop 0\n"
      "v_permlane16_swap_b32_e64 v108, v109\n"
      "v_pk_mul_f32 v[174:175], v[174:175], v[108:109] op_sel_hi:[1,0]\n"
      "v_pk_mul_f32 v[204:205], v[204:205], v[108:109] op_sel:[0,1]\n"
      "v_pk_mul_f32 v[132:133], v[132:133], v[108:109] op_sel:[0,1]\n"
      "v_pk_mul_f32 v[176:177], v[176:177], v[108:109] op_sel_hi:[1,0]\n"
      "v_pk_mul_f32 v[156:157], v[156:157], v[108:109] op_sel_hi:[1,0]\n"
      "v_pk_mul_f32 v[136:137], v[136:137], v[108:109] op_sel:[0,1]\n"
      "v_pk_mul_f32 v[130:131], v[130:131], v[108:109] op_sel:[0,1]\n"
      "v_pk_mul_f32 v[206:207], v[206:207], v[108:109] op_sel:[0,1]\n"
      "v_pk_mul_f32 v[140:141], v[140:141], v[108:109] op_sel_hi:[1,0]\n"
      "v_pk_mul_f32 v[138:139], v[138:139], v[108:109] op_sel_hi:[1,0]\n"
      "v_cvt_pk_bf16_f32 v174, v174, v175\n"
      "v_cvt_pk_bf16_f32 v175, v176, v177\n"
      "v_cvt_pk_bf16_f32 v204, v204, v205\n"
      "v_pk_mul_f32 v[158:159], v[158:159], v[108:109] op_sel_hi:[1,0]\n"
      "v_cvt_pk_bf16_f32 v130, v130, v131\n"
      "v_cvt_pk_bf16_f32 v205, v206, v207\n"
      "v_cvt_pk_bf16_f32 v177, v158, v159\n"
      "v_pk_mul_f32 v[134:135], v[134:135], v[108:109] op_sel:[0,1]\n"
      "v_cvt_pk_bf16_f32 v138, v138, v139\n"
      "v_cvt_pk_bf16_f32 v131, v132, v133\n"
      "v_cvt_pk_bf16_f32 v139, v140, v141\n"
      "v_cvt_pk_bf16_f32 v176, v156, v157\n"
      "s_add_u32 s42, s42, 1\n"
      "ds_write_b128 v97, v[174:177]\n"
      "ds_write_b64 v96, v[204:205] offset:6144\n"
      "ds_write_b64 v96, v[138:139]\n"
      "v_cvt_pk_bf16_f32 v133, v136, v137\n"
      "v_cvt_pk_bf16_f32 v132, v134, v135\n"
      "s_waitcnt lgkmcnt(3)\n"
      "buffer_store_dwordx4 v[192:195], v20, s[24:27], s46 offen sc1\n"
      "ds_write_b128 v97, v[130:133] offset:6144\n"
      "ds_bpermute_b32 v139, v189, v89\n"
      "buffer_store_dwordx4 v[214:217], v21, s[24:27], s46 offen sc1\n"
      "v_xor_b32_e64 v201, v201, v80\n"
      "v_xor_b32 v127, v127, v78\n"
      "ds_bpermute_b32 v205, v76, v89\n"
      "ds_read_b128 v[192:195], v7 offset:34816\n"
      "buffer_store_dwordx4 v[184:187], v22, s[24:27], s46 offen sc1\n"
      "s_waitcnt lgkmcnt(3)\n"
      "s_barrier\n"
      "ds_read_b128 v[182:185], v84\n"
      "buffer_store_dwordx4 v[196:199], v23, s[24:27], s46 offen sc1\n"
      "ds_read_b128 v[132:135], v84 offset:1024\n"
      "s_waitcnt lgkmcnt(3)\n"
      "v_lshl_add_u32 v205, v201, 4, v205\n"
      "s_waitcnt lgkmcnt(2)\n"
      "buffer_store_dwordx4 v[192:195], v25, s[24:27], s46 offen sc1\n"
      "v_lshl_add_u32 v139, v127, 4, v139\n"
      "s_waitcnt lgkmcnt(1)\n"
      "buffer_store_dwordx4 v[182:185], v139, s[24:27], 0 offen sc1\n"
      "s_waitcnt lgkmcnt(0)\n"
      "buffer_store_dwordx4 v[132:135], v205, s[24:27], 0 offen sc1\n"
      "s_endpgm\n"
      "L_rare_1_%=:\n"
      "s_nop 7\n"
      "s_waitcnt lgkmcnt(0)\n"
      "s_add_u32 s72, s42, 1\n"
      "v_readlane_b32 s77, v38, s72\n"
      "v_subrev_u32 v174, 115456, v9\n"
      "s_and_b32 s73, s77, -16\n"
      "s_mov_b32 m0, 110592\n"
      "s_nop 0\n"
      "buffer_load_dwordx4 v174, s[28:31], s73 offen  lds\n"
      "s_add_u32 s73, s73, 1024\n"
      "s_mov_b32 m0, 111616\n"
      "s_nop 0\n"
      "buffer_load_dwordx4 v174, s[28:31], s73 offen  lds\n"
      "s_add_u32 s73, s73, 1024\n"
      "s_mov_b32 m0, 112640\n"
      "s_nop 0\n"
      "buffer_load_dwordx4 v174, s[28:31], s73 offen  lds\n"
      "s_add_u32 s73, s73, 1024\n"
      "s_mov_b32 m0, 113664\n"
      "s_nop 0\n"
      "buffer_load_dwordx4 v174, s[28:31], s73 offen  lds\n"
      "s_add_u32 s73, s73, 1024\n"
      "s_mov_b32 m0, 114688\n"
      "s_mov_b32 exec_hi, 0xffff\n"
      "buffer_load_dwordx4 v174, s[28:31], s73 offen  lds\n"
      "s_mov_b32 exec_hi, -1\n"
      "s_add_u32 s73, s73, 1024\n"
      "s_and_b32 s77, s77, 15\n"
      "s_waitcnt lgkmcnt(0)\n"
      "s_nop 4\n"
      "s_branch L_back_2_%=\n"
      "L_rare_3_%=:\n"
      "s_nop 7\n"
      "s_waitcnt lgkmcnt(0)\n"
      "s_waitcnt vmcnt(0)\n"
      "v_subrev_u32 v89, 115456, v9\n"
      "v_bfe_u32 v127, v89, 4, 4\n"
      "v_mul_u32_u24 v127, 98, v127\n"
      "v_lshrrev_b32 v89, 8, v89\n"
      "v_lshl_add_u32 v127, v89, 3, v127\n"
      "v_add_u32 v127, s77, v127\n"
      "v_and_b32 v82, 2, v127\n"
      "v_and_b32 v127, -4, v127\n"
      "v_add_u32 v127, 110592, v127\n"
      "ds_read2_b32 v[174:175], v127 offset0:0 offset1:1\n"
      "ds_read2_b32 v[176:177], v127 offset0:8 offset1:9\n"
      "ds_read2_b32 v[130:131], v127 offset0:16 offset1:17\n"
      "ds_read2_b32 v[194:195], v127 offset0:2 offset1:10\n"
      "ds_read2_b32 v[118:119], v127 offset0:18 offset1:24\n"
      "s_waitcnt lgkmcnt(4)\n"
      "v_alignbyte_b32 v174, v175, v174, v82\n"
      "s_waitcnt lgkmcnt(1)\n"
      "v_alignbyte_b32 v175, v194, v175, v82\n"
      "v_lshlrev_b32 v89, 16, v174\n"
      "v_fmac_f32 v160, s53, v89\n"
      "v_and_b32 v89, 0xffff0000, v174\n"
      "v_fmac_f32 v161, s53, v89\n"
      "v_lshlrev_b32 v89, 16, v175\n"
      "v_fmac_f32_e64 v162, s53, v89\n"
      "v_and_b32 v89, 0xffff0000, v175\n"
      "v_fmac_f32 v163, s53, v89\n"
      "v_alignbyte_b32 v176, v177, v176, v82\n"
      "v_alignbyte_b32 v177, v195, v177, v82\n"
      "v_lshlrev_b32 v89, 16, v176\n"
      "v_fmac_f32 v102, s53, v89\n"
      "v_and_b32 v89, 0xffff0000, v176\n"
      "v_fmac_f32 v103, s53, v89\n"
      "v_lshlrev_b32 v89, 16, v177\n"
      "v_fmac_f32 v104, s53, v89\n"
      "v_and_b32 v89, 0xffff0000, v177\n"
      "v_fmac_f32 v105, s53, v89\n"
      "v_alignbyte_b32 v130, v131, v130, v82\n"
      "s_waitcnt lgkmcnt(0)\n"
      "v_alignbyte_b32 v131, v118, v131, v82\n"
      "v_lshlrev_b32 v89, 16, v130\n"
      "v_fmac_f32 v148, s53, v89\n"
      "v_and_b32 v89, 0xffff0000, v130\n"
      "v_fmac_f32 v149, s53, v89\n"
      "v_lshlrev_b32 v89, 16, v131\n"
      "v_fmac_f32 v150, s53, v89\n"
      "v_and_b32 v89, 0xffff0000, v131\n"
      "v_fmac_f32 v151, s53, v89\n"
      "v_alignbyte_b32 v89, v119, v119, v82\n"
      "v_lshlrev_b32 v89, 16, v89\n"
      "v_fmac_f32 v126, s53, v89\n"
      "v_max3_f32 v127, v160, v161, v162\n"
      "v_max3_f32 v82, v163, v102, v103\n"
      "v_max3_f32 v127, v127, v104, v105\n"
      "v_max3_f32 v82, v82, v148, v149\n"
      "v_max3_f32 v127, v127, v150, v151\n"
      "v_max3_f32 v127, v127, v82, v126\n"
      "v_mov_b32 v89, v127\n"
      "s_nop 1\n"
      "v_permlane16_swap_b32 v127, v89\n"
      "v_max_f32 v127, v127, v89\n"
      "v_mov_b32 v89, v127\n"
      "s_nop 1\n"
      "v_permlane32_swap_b32 v127, v89\n"
      "v_max_f32 v127, v127, v89\n"
      "v_sub_f32 v160, v160, v127\n"
      "v_sub_f32 v161, v161, v127\n"
      "v_sub_f32 v162, v162, v127\n"
      "v_sub_f32 v163, v163, v127\n"
      "v_sub_f32 v102, v102, v127\n"
      "v_sub_f32 v103, v103, v127\n"
      "v_sub_f32 v104, v104, v127\n"
      "v_sub_f32 v105, v105, v127\n"
      "v_sub_f32 v148, v148, v127\n"
      "v_sub_f32 v149, v149, v127\n"
      "v_sub_f32 v150, v150, v127\n"
      "v_sub_f32 v151, v151, v127\n"
      "v_sub_f32_e64 v126, v126, v127\n"
      "s_waitcnt lgkmcnt(0)\n"
      "s_nop 4\n"
      "s_branch L_back_4_%=\n"
      "L_rare_5_%=:\n"
      "s_nop 7\n"
      "s_waitcnt lgkmcnt(0)\n"
      "s_waitcnt vmcnt(0)\n"
      "v_subrev_u32 v178, 115456, v9\n"
      "v_bfe_u32 v215, v178, 4, 4\n"
      "v_mul_u32_u24 v215, 98, v215\n"
      "v_lshrrev_b32 v178, 8, v178\n"
      "v_lshl_add_u32 v215, v178, 3, v215\n"
      "v_add_u32 v215, s77, v215\n"
      "v_and_b32 v190, 2, v215\n"
      "v_and_b32 v215, -4, v215\n"
      "v_add_u32 v215, 112160, v215\n"
      "ds_read2_b32 v[120:121], v215 offset0:0 offset1:1\n"
      "ds_read2_b32 v[118:119], v215 offset0:8 offset1:9\n"
      "ds_read2_b32 v[116:117], v215 offset0:16 offset1:17\n"
      "ds_read2_b32 v[100:101], v215 offset0:2 offset1:10\n"
      "ds_read2_b32 v[98:99], v215 offset0:18 offset1:24\n"
      "s_waitcnt lgkmcnt(4)\n"
      "v_alignbyte_b32 v120, v121, v120, v190\n"
      "s_waitcnt lgkmcnt(1)\n"
      "v_alignbyte_b32 v121, v100, v121, v190\n"
      "v_lshlrev_b32 v178, 16, v120\n"
      "v_fmac_f32 v170, s53, v178\n"
      "v_and_b32 v178, 0xffff0000, v120\n"
      "v_fmac_f32 v171, s53, v178\n"
      "v_lshlrev_b32 v178, 16, v121\n"
      "v_fmac_f32 v172, s53, v178\n"
      "v_and_b32 v178, 0xffff0000, v121\n"
      "v_fmac_f32 v173, s53, v178\n"
      "v_alignbyte_b32 v118, v119, v118, v190\n"
      "v_alignbyte_b32 v119, v101, v119, v190\n"
      "v_lshlrev_b32 v178, 16, v118\n"
      "v_fmac_f32 v154, s53, v178\n"
      "v_and_b32 v178, 0xffff0000, v118\n"
      "v_fmac_f32 v155, s53, v178\n"
      "v_lshlrev_b32 v178, 16, v119\n"
      "v_fmac_f32_e64 v156, s53, v178\n"
      "v_and_b32 v178, 0xffff0000, v119\n"
      "v_fmac_f32 v157, s53, v178\n"
      "v_alignbyte_b32 v116, v117, v116, v190\n"
      "s_waitcnt lgkmcnt(0)\n"
      "v_alignbyte_b32 v117, v98, v117, v190\n"
      "v_lshlrev_b32 v178, 16, v116\n"
      "v_fmac_f32 v198, s53, v178\n"
      "v_and_b32 v178, 0xffff0000, v116\n"
      "v_fmac_f32 v199, s53, v178\n"
      "v_lshlrev_b32 v178, 16, v117\n"
      "v_fmac_f32_e64 v200, s53, v178\n"
      "v_and_b32 v178, 0xffff0000, v117\n"
      "v_fmac_f32 v201, s53, v178\n"
      "v_alignbyte_b32 v178, v99, v99, v190\n"
      "v_lshlrev_b32 v178, 16, v178\n"
      "v_fmac_f32_e64 v218, s53, v178\n"
      "v_max3_f32 v215, v170, v171, v172\n"
      "v_max3_f32 v190, v173, v154, v155\n"
      "v_max3_f32 v215, v215, v156, v157\n"
      "v_max3_f32 v190, v190, v198, v199\n"
      "v_max3_f32 v215, v215, v200, v201\n"
      "v_max3_f32 v215, v215, v190, v218\n"
      "v_mov_b32 v178, v215\n"
      "s_nop 1\n"
      "v_permlane16_swap_b32 v215, v178\n"
      "v_max_f32 v215, v215, v178\n"
      "v_mov_b32 v178, v215\n"
      "s_nop 1\n"
      "v_permlane32_swap_b32 v215, v178\n"
      "v_max_f32 v215, v215, v178\n"
      "v_sub_f32 v170, v170, v215\n"
      "v_sub_f32 v171, v171, v215\n"
      "v_sub_f32 v172, v172, v215\n"
      "v_sub_f32 v173, v173, v215\n"
      "v_sub_f32 v154, v154, v215\n"
      "v_sub_f32 v155, v155, v215\n"
      "v_sub_f32 v156, v156, v215\n"
      "v_sub_f32 v157, v157, v215\n"
      "v_sub_f32 v198, v198, v215\n"
      "v_sub_f32 v199, v199, v215\n"
      "v_sub_f32 v200, v200, v215\n"
      "v_sub_f32 v201, v201, v215\n"
      "v_sub_f32_e64 v218, v218, v215\n"
      "s_waitcnt lgkmcnt(0)\n"
      "s_nop 4\n"
      "s_branch L_back_6_%=\n"
      "L_rare_7_%=:\n"
      "s_nop 7\n"
      "s_waitcnt lgkmcnt(0)\n"
      "s_waitcnt vmcnt(0)\n"
      "v_subrev_u32 v103, 115456, v9\n"
      "v_bfe_u32 v158, v103, 4, 4\n"
      "v_mul_u32_u24 v158, 98, v158\n"
      "v_lshrrev_b32_e64 v103, 8, v103\n"
      "v_lshl_add_u32 v158, v103, 3, v158\n"
      "v_add_u32 v158, s77, v158\n"
      "v_and_b32 v159, 2, v158\n"
      "v_and_b32_e64 v158, -4, v158\n"
      "v_add_u32 v158, 113728, v158\n"
      "ds_read2_b32 v[160:161], v158 offset0:0 offset1:1\n"
      "ds_read2_b32 v[162:163], v158 offset0:8 offset1:9\n"
      "ds_read2_b32 v[140:141], v158 offset0:16 offset1:17\n"
      "ds_read2_b32 v[142:143], v158 offset0:2 offset1:10\n"
      "ds_read2_b32 v[100:101], v158 offset0:18 offset1:24\n"
      "s_waitcnt lgkmcnt(4)\n"
      "v_alignbyte_b32 v160, v161, v160, v159\n"
      "s_waitcnt lgkmcnt(1)\n"
      "v_alignbyte_b32 v161, v142, v161, v159\n"
      "v_lshlrev_b32 v103, 16, v160\n"
      "v_fmac_f32 v110, s53, v103\n"
      "v_and_b32 v103, 0xffff0000, v160\n"
      "v_fmac_f32 v111, s53, v103\n"
      "v_lshlrev_b32 v103, 16, v161\n"
      "v_fmac_f32_e64 v112, s53, v103\n"
      "v_and_b32 v103, 0xffff0000, v161\n"
      "v_fmac_f32 v113, s53, v103\n"
      "v_alignbyte_b32 v162, v163, v162, v159\n"
      "v_alignbyte_b32 v163, v143, v163, v159\n"
      "v_lshlrev_b32 v103, 16, v162\n"
      "v_fmac_f32 v120, s53, v103\n"
      "v_and_b32 v103, 0xffff0000, v162\n"
      "v_fmac_f32 v121, s53, v103\n"
      "v_lshlrev_b32 v103, 16, v163\n"
      "v_fmac_f32 v122, s53, v103\n"
      "v_and_b32 v103, 0xffff0000, v163\n"
      "v_fmac_f32 v123, s53, v103\n"
      "v_alignbyte_b32 v140, v141, v140, v159\n"
      "s_waitcnt lgkmcnt(0)\n"
      "v_alignbyte_b32 v141, v100, v141, v159\n"
      "v_lshlrev_b32 v103, 16, v140\n"
      "v_fmac_f32 v166, s53, v103\n"
      "v_and_b32 v103, 0xffff0000, v140\n"
      "v_fmac_f32 v167, s53, v103\n"
      "v_lshlrev_b32 v103, 16, v141\n"
      "v_fmac_f32 v168, s53, v103\n"
      "v_and_b32 v103, 0xffff0000, v141\n"
      "v_fmac_f32 v169, s53, v103\n"
      "v_alignbyte_b32 v103, v101, v101, v159\n"
      "v_lshlrev_b32 v103, 16, v103\n"
      "v_fmac_f32 v220, s53, v103\n"
      "v_max3_f32 v158, v110, v111, v112\n"
      "v_max3_f32 v159, v113, v120, v121\n"
      "v_max3_f32 v158, v158, v122, v123\n"
      "v_max3_f32 v159, v159, v166, v167\n"
      "v_max3_f32 v158, v158, v168, v169\n"
      "v_max3_f32 v158, v158, v159, v220\n"
      "v_mov_b32 v103, v158\n"
      "s_nop 1\n"
      "v_permlane16_swap_b32 v158, v103\n"
      "v_max_f32 v158, v158, v103\n"
      "v_mov_b32 v103, v158\n"
      "s_nop 1\n"
      "v_permlane32_swap_b32 v158, v103\n"
      "v_max_f32 v158, v158, v103\n"
      "v_sub_f32 v110, v110, v158\n"
      "v_sub_f32 v111, v111, v158\n"
      "v_sub_f32 v112, v112, v158\n"
      "v_sub_f32 v113, v113, v158\n"
      "v_sub_f32 v120, v120, v158\n"
      "v_sub_f32 v121, v121, v158\n"
      "v_sub_f32 v122, v122, v158\n"
      "v_sub_f32 v123, v123, v158\n"
      "v_sub_f32 v166, v166, v158\n"
      "v_sub_f32 v167, v167, v158\n"
      "v_sub_f32 v168, v168, v158\n"
      "v_sub_f32 v169, v169, v158\n"
      "v_sub_f32 v220, v220, v158\n"
      "s_waitcnt lgkmcnt(0)\n"
      "s_nop 4\n"
      "s_branch L_back_8_%=\n"
      "L_rare_9_%=:\n"
      "s_nop 7\n"
      "s_waitcnt lgkmcnt(0)\n"
      "s_waitcnt vmcnt(0)\n"
      "v_subrev_u32 v160, 115456, v9\n"
      "v_lshrrev_b32 v160, 3, v160\n"
      "v_min_u32 v160, 96, v160\n"
      "v_add_u32 v160, s77, v160\n"
      "v_add_u32 v160, 115296, v160\n"
      "ds_read_u16 v160, v160\n"
      "s_waitcnt lgkmcnt(0)\n"
      "v_lshlrev_b32_e64 v160, 16, v160\n"
      "v_fmac_f32 v100, s53, v160\n"
      "s_nop 1\n"
      "v_max_f32_dpp v208, v100, v100 row_ror:8 row_mask:0xf bank_mask:0xf\n"
      "s_nop 1\n"
      "v_max_f32_dpp v208, v208, v208 row_ror:4 row_mask:0xf bank_mask:0xf\n"
      "s_nop 1\n"
      "v_max_f32_dpp v208, v208, v208 row_ror:2 row_mask:0xf bank_mask:0xf\n"
      "s_nop 1\n"
      "v_max_f32_dpp v208, v208, v208 row_ror:1 row_mask:0xf bank_mask:0xf\n"
      "s_nop 1\n"
      "v_max_f32_dpp v208, v208, v208 row_bcast:15 row_mask:0xa bank_mask:0xf\n"
      "s_nop 1\n"
      "v_max_f32_dpp v208, v208, v208 row_bcast:31 row_mask:0xc bank_mask:0xf\n"
      "s_nop 0\n"
      "v_readlane_b32 s72, v208, 63\n"
      "s_nop 1\n"
      "v_subrev_f32 v100, s72, v100\n"
      "s_waitcnt lgkmcnt(0)\n"
      "s_nop 4\n"
      "s_branch L_back_10_%=\n"
      "L_rare_27_%=:\n"
      "s_nop 7\n"
      "s_waitcnt lgkmcnt(0)\n"
      "s_bitcmp1_b32 s85, 31\n"
      "s_cselect_b32 s75, 100270464, 0\n"
      "s_bitcmp1_b32 s85, 30\n"
      "s_cselect_b32 s76, 196224, 0\n"
      "s_and_b32 s74, s85, 0x3fffffff\n"
      "s_cmp_ge_u32 s85, 0x40000000\n"
      "s_cselect_b32 s74, s74, 0\n"
      "s_cselect_b32 s85, 0, s85\n"
      "s_sub_u32 s74, s74, s69\n"
      "s_add_u32 s69, s69, s74\n"
      "s_sub_u32 s75, s75, s70\n"
      "s_add_u32 s70, s70, s75\n"
      "s_sub_u32 s76, s76, s71\n"
      "s_add_u32 s71, s71, s76\n"
      "v_add_u32 v20, s74, v20\n"
      "v_add_u32 v21, s74, v21\n"
      "v_add_u32 v22, s74, v22\n"
      "v_add_u32 v23, s74, v23\n"
      "v_add_u32 v25, s74, v25\n"
      "s_cmp_eq_u32 s75, 0\n"
      "s_cbranch_scc1 L_ax_25_%=\n"
      "v_bfe_i32 v213, v27, 10, 1\n"
      "v_and_b32 v213, s75, v213\n"
      "v_sub_u32 v20, v20, v213\n"
      "v_bfe_i32 v213, v27, 12, 1\n"
      "v_and_b32 v213, s75, v213\n"
      "v_sub_u32 v21, v21, v213\n"
      "v_bfe_i32 v213, v27, 14, 1\n"
      "v_and_b32 v213, s75, v213\n"
      "v_sub_u32 v22, v22, v213\n"
      "v_bfe_i32 v213, v27, 16, 1\n"
      "v_and_b32 v213, s75, v213\n"
      "v_sub_u32 v23, v23, v213\n"
      "v_bfe_i32 v213, v27, 18, 1\n"
      "v_and_b32 v213, s75, v213\n"
      "v_sub_u32 v25, v25, v213\n"
      "L_ax_25_%=:\n"
      "s_cmp_eq_u32 s76, 0\n"
      "s_cbranch_scc1 L_ax_26_%=\n"
      "v_bfe_i32 v213, v27, 11, 1\n"
      "v_and_b32 v213, s76, v213\n"
      "v_sub_u32_e64 v20, v20, v213\n"
      "v_bfe_i32 v213, v27, 13, 1\n"
      "v_and_b32 v213, s76, v213\n"
      "v_sub_u32 v21, v21, v213\n"
      "v_bfe_i32 v213, v27, 15, 1\n"
      "v_and_b32 v213, s76, v213\n"
      "v_sub_u32 v22, v22, v213\n"
      "v_bfe_i32 v213, v27, 17, 1\n"
      "v_and_b32 v213, s76, v213\n"
      "v_sub_u32 v23, v23, v213\n"
      "v_bfe_i32 v213, v27, 19, 1\n"
      "v_and_b32 v213, s76, v213\n"
      "v_sub_u32 v25, v25, v213\n"
      "L_ax_26_%=:\n"
      "s_or_b32 s48, s66, s69\n"
      "s_cmp_lg_u32 s48, 0\n"
      "s_cselect_b32 s48, 0x40000000, 0\n"
      "s_waitcnt lgkmcnt(0)\n"
      "s_nop 4\n"
      "s_branch L_back_28_%=\n"
      "L_rare_31_%=:\n"
      "s_nop 7\n"
      "s_waitcnt lgkmcnt(0)\n"
      "s_bitcmp1_b32 s85, 31\n"
      "s_cselect_b32 s75, 100270464, 0\n"
      "s_bitcmp1_b32 s85, 30\n"
      "s_cselect_b32 s76, 196224, 0\n"
      "s_and_b32 s74, s85, 0x3fffffff\n"
      "s_cmp_ge_u32 s85, 0x40000000\n"
      "s_cselect_b32 s74, s74, 0\n"
      "s_cselect_b32 s85, 0, s85\n"
      "s_sub_u32 s74, s74, s69\n"
      "s_add_u32 s69, s69, s74\n"
      "s_sub_u32 s75, s75, s70\n"
      "s_add_u32 s70, s70, s75\n"
      "s_sub_u32 s76, s76, s71\n"
      "s_add_u32 s71, s71, s76\n"
      "v_add_u32 v20, s74, v20\n"
      "v_add_u32 v21, s74, v21\n"
      "v_add_u32 v22, s74, v22\n"
      "v_add_u32 v23, s74, v23\n"
      "v_add_u32 v25, s74, v25\n"
      "s_cmp_eq_u32 s75, 0\n"
      "s_cbranch_scc1 L_ax_29_%=\n"
      "v_bfe_i32 v219, v27, 10, 1\n"
      "v_and_b32 v219, s75, v219\n"
      "v_sub_u32 v20, v20, v219\n"
      "v_bfe_i32 v219, v27, 12, 1\n"
      "v_and_b32 v219, s75, v219\n"
      "v_sub_u32 v21, v21, v219\n"
      "v_bfe_i32 v219, v27, 14, 1\n"
      "v_and_b32 v219, s75, v219\n"
      "v_sub_u32 v22, v22, v219\n"
      "v_bfe_i32 v219, v27, 16, 1\n"
      "v_and_b32 v219, s75, v219\n"
      "v_sub_u32 v23, v23, v219\n"
      "v_bfe_i32 v219, v27, 18, 1\n"
      "v_and_b32 v219, s75, v219\n"
      "v_sub_u32 v25, v25, v219\n"
      "L_ax_29_%=:\n"
      "s_cmp_eq_u32 s76, 0\n"
      "s_cbranch_scc1 L_ax_30_%=\n"
      "v_bfe_i32 v219, v27, 11, 1\n"
      "v_and_b32 v219, s76, v219\n"
      "v_sub_u32 v20, v20, v219\n"
      "v_bfe_i32 v219, v27, 13, 1\n"
      "v_and_b32 v219, s76, v219\n"
      "v_sub_u32 v21, v21, v219\n"
      "v_bfe_i32 v219, v27, 15, 1\n"
      "v_and_b32 v219, s76, v219\n"
      "v_sub_u32 v22, v22, v219\n"
      "v_bfe_i32 v219, v27, 17, 1\n"
      "v_and_b32 v219, s76, v219\n"
      "v_sub_u32 v23, v23, v219\n"
      "v_bfe_i32 v219, v27, 19, 1\n"
      "v_and_b32 v219, s76, v219\n"
      "v_sub_u32 v25, v25, v219\n"
      "L_ax_30_%=:\n"
      "s_or_b32 s48, s66, s69\n"
      "s_cmp_lg_u32 s48, 0\n"
      "s_cselect_b32 s48, 0x40000000, 0\n"
      "s_waitcnt lgkmcnt(0)\n"
      "s_nop 4\n"
      "s_branch L_back_32_%=\n"
      "L_rare_35_%=:\n"
      "s_nop 7\n"
      "s_waitcnt lgkmcnt(0)\n"
      "s_bitcmp1_b32 s46, 31\n"
      "s_cselect_b32 s75, 100270464, 0\n"
      "s_bitcmp1_b32 s46, 30\n"
      "s_cselect_b32 s76, 196224, 0\n"
      "s_and_b32 s74, s46, 0x3fffffff\n"
      "s_cmp_ge_u32 s46, 0x40000000\n"
      "s_cselect_b32 s74, s74, 0\n"
      "s_cselect_b32 s46, 0, s46\n"
      "s_sub_u32 s74, s74, s69\n"
      "s_add_u32 s69, s69, s74\n"
      "s_sub_u32 s75, s75, s70\n"
      "s_add_u32 s70, s70, s75\n"
      "s_sub_u32 s76, s76, s71\n"
      "s_add_u32 s71, s71, s76\n"
      "v_add_u32 v20, s74, v20\n"
      "v_add_u32 v21, s74, v21\n"
      "v_add_u32 v22, s74, v22\n"
      "v_add_u32 v23, s74, v23\n"
      "v_add_u32 v25, s74, v25\n"
      "s_cmp_eq_u32 s75, 0\n"
      "s_cbranch_scc1 L_ax_33_%=\n"
      "v_bfe_i32 v185, v27, 10, 1\n"
      "v_and_b32 v185, s75, v185\n"
      "v_sub_u32 v20, v20, v185\n"
      "v_bfe_i32 v185, v27, 12, 1\n"
      "v_and_b32 v185, s75, v185\n"
      "v_sub_u32 v21, v21, v185\n"
      "v_bfe_i32 v185, v27, 14, 1\n"
      "v_and_b32 v185, s75, v185\n"
      "v_sub_u32 v22, v22, v185\n"
      "v_bfe_i32 v185, v27, 16, 1\n"
      "v_and_b32 v185, s75, v185\n"
      "v_sub_u32 v23, v23, v185\n"
      "v_bfe_i32 v185, v27, 18, 1\n"
      "v_and_b32 v185, s75, v185\n"
      "v_sub_u32 v25, v25, v185\n"
      "L_ax_33_%=:\n"
      "s_cmp_eq_u32 s76, 0\n"
      "s_cbranch_scc1 L_ax_34_%=\n"
      "v_bfe_i32 v185, v27, 11, 1\n"
      "v_and_b32 v185, s76, v185\n"
      "v_sub_u32 v20, v20, v185\n"
      "v_bfe_i32 v185, v27, 13, 1\n"
      "v_and_b32 v185, s76, v185\n"
      "v_sub_u32 v21, v21, v185\n"
      "v_bfe_i32 v185, v27, 15, 1\n"
      "v_and_b32 v185, s76, v185\n"
      "v_sub_u32 v22, v22, v185\n"
      "v_bfe_i32 v185, v27, 17, 1\n"
      "v_and_b32 v185, s76, v185\n"
      "v_sub_u32 v23, v23, v185\n"
      "v_bfe_i32 v185, v27, 19, 1\n"
      "v_and_b32 v185, s76, v185\n"
      "v_sub_u32 v25, v25, v185\n"
      "L_ax_34_%=:\n"
      "s_or_b32 s48, s66, s69\n"
      "s_cmp_lg_u32 s48, 0\n"
      "s_cselect_b32 s48, 0x40000000, 0\n"
      "s_waitcnt lgkmcnt(0)\n"
      "s_nop 4\n"
      "s_branch L_back_36_%=\n"
      "L_rare_39_%=:\n"
      "s_nop 7\n"
      "s_waitcnt lgkmcnt(0)\n"
      "s_bitcmp1_b32 s46, 31\n"
      "s_cselect_b32 s75, 100270464, 0\n"
      "s_bitcmp1_b32 s46, 30\n"
      "s_cselect_b32 s76, 196224, 0\n"
      "s_and_b32 s74, s46, 0x3fffffff\n"
      "s_cmp_ge_u32 s46, 0x40000000\n"
      "s_cselect_b32 s74, s74, 0\n"
      "s_cselect_b32 s46, 0, s46\n"
      "s_sub_u32 s74, s74, s69\n"
      "s_add_u32 s69, s69, s74\n"
      "s_sub_u32 s75, s75, s70\n"
      "s_add_u32 s70, s70, s75\n"
      "s_sub_u32 s76, s76, s71\n"
      "s_add_u32 s71, s71, s76\n"
      "v_add_u32 v20, s74, v20\n"
      "v_add_u32 v21, s74, v21\n"
      "v_add_u32 v22, s74, v22\n"
      "v_add_u32 v23, s74, v23\n"
      "v_add_u32 v25, s74, v25\n"
      "s_cmp_eq_u32 s75, 0\n"
      "s_cbranch_scc1 L_ax_37_%=\n"
      "v_bfe_i32 v182, v27, 10, 1\n"
      "v_and_b32 v182, s75, v182\n"
      "v_sub_u32 v20, v20, v182\n"
      "v_bfe_i32 v182, v27, 12, 1\n"
      "v_and_b32 v182, s75, v182\n"
      "v_sub_u32 v21, v21, v182\n"
      "v_bfe_i32 v182, v27, 14, 1\n"
      "v_and_b32 v182, s75, v182\n"
      "v_sub_u32 v22, v22, v182\n"
      "v_bfe_i32 v182, v27, 16, 1\n"
      "v_and_b32 v182, s75, v182\n"
      "v_sub_u32 v23, v23, v182\n"
      "v_bfe_i32 v182, v27, 18, 1\n"
      "v_and_b32 v182, s75, v182\n"
      "v_sub_u32 v25, v25, v182\n"
      "L_ax_37_%=:\n"
      "s_cmp_eq_u32 s76, 0\n"
      "s_cbranch_scc1 L_ax_38_%=\n"
      "v_bfe_i32 v182, v27, 11, 1\n"
      "v_and_b32 v182, s76, v182\n"
      "v_sub_u32 v20, v20, v182\n"
      "v_bfe_i32 v182, v27, 13, 1\n"
      "v_and_b32 v182, s76, v182\n"
      "v_sub_u32 v21, v21, v182\n"
      "v_bfe_i32 v182, v27, 15, 1\n"
      "v_and_b32 v182, s76, v182\n"
      "v_sub_u32 v22, v22, v182\n"
      "v_bfe_i32 v182, v27, 17, 1\n"
      "v_and_b32 v182, s76, v182\n"
      "v_sub_u32 v23, v23, v182\n"
      "v_bfe_i32 v182, v27, 19, 1\n"
      "v_and_b32 v182, s76, v182\n"
      "v_sub_u32 v25, v25, v182\n"
      "L_ax_38_%=:\n"
      "s_or_b32 s48, s66, s69\n"
      "s_cmp_lg_u32 s48, 0\n"
      "s_cselect_b32 s48, 0x40000000, 0\n"
      "s_waitcnt lgkmcnt(0)\n"
      "s_nop 4\n"
      "s_branch L_back_40_%=\n"
      "L_cend_%=:\n"
      :
      : "s"(__builtin_amdgcn_kernarg_segment_ptr()), "s"(wave), "s"(blockIdx.x), "s"(CT_TAB), "s"(lds0)
      : "v0","v1","v2","v3","v4","v5","v6","v7","v8","v9","v10","v11","v12","v13","v14","v15","v16","v17","v18","v19","v20","v21","v22","v23","v24","v25","v26","v27","v28","v29","v30","v31","v32","v33","v34","v35","v36","v37","v38","v39","v40","v41","v42","v43","v44","v45","v46","v47","v48","v49","v50","v51","v52","v53","v54","v55","v56","v57","v58","v59","v60","v61","v62","v63","v64","v65","v66","v67","v68","v69","v70","v71","v72","v73","v74","v75","v76","v77","v78","v79","v80","v81","v82","v83","v84","v85","v86","v87","v88","v89","v90","v91","v92","v93","v94","v95","v96","v97","v98","v99","v100","v101","v102","v103","v104","v105","v106","v107","v108","v109","v110","v111","v112","v113","v114","v115","v116","v117","v118","v119","v120","v121","v122","v123","v124","v125","v126","v127","v128","v129","v130","v131","v132","v133","v134","v135","v136","v137","v138","v139","v140","v141","v142","v143","v144","v145","v146","v147","v148","v149","v150","v151","v152","v153","v154","v155","v156","v157","v158","v159","v160","v161","v162","v163","v164","v165","v166","v167","v168","v169","v170","v171","v172","v173","v174","v175","v176","v177","v178","v179","v180","v181","v182","v183","v184","v185","v186","v187","v188","v189","v190","v191","v192","v193","v194","v195","v196","v197","v198","v199","v200","v201","v202","v203","v204","v205","v206","v207","v208","v209","v210","v211","v212","v213","v214","v215","v216","v217","v218","v219","v220","v221","v222","v223","v224","v225","v226","v227","v228","v229","v230","v231","v232","v233","v234","v235","v236","v237","v238","v239","v240","v241","v242","v243","v244","v245","v246","v247","v248","v249","v250","v251","v252","v253","v254","v255","a0","a1","a2","a3","a4","a5","a6","a7","a8","a9","a10","a11","a12","a13","a14","a15","a16","a17","a18","a19","a20","a21","a22","a23","a24","a25","a26","a27","a28","a29","a30","a31","a32","a33","a34","a35","a36","a37","a38","a39","a40","a41","a42","a43","a44","a45","a46","a47","a48","a49","a50","a51","a52","a53","a54","a55","a56","a57","a58","a59","a60","a61","a62","a63","a64","a65","a66","a67","a68","a69","a70","a71","a72","a73","a74","a75","a76","a77","a78","a79","a80","a81","a82","a83","a84","a85","a86","a87","a88","a89","a90","a91","a92","a93","a94","a95","a96","a97","a98","a99","a100","a101","a102","a103","a104","a105","a106","a107","a108","a109","a110","a111","a112","a113","a114","a115","a116","a117","a118","a119","a120","a121","a122","a123","a124","a125","a126","a127","a128","a129","a130","a131","a132","a133","a134","a135","a136","a137","a138","a139","a140","a141","a142","a143","a144","a145","a146","a147","a148","a149","a150","a151","a152","a153","a154","a155","a156","a157","a158","a159","a160","a161","a162","a163","a164","a165","a166","a167","a168","a169","a170","a171","a172","a173","a174","a175","a176","a177","a178","a179","a180","a181","a182","a183","a184","a185","a186","a187","a188","a189","a190","a191","a192","a193","a194","a195","a196","a197","a198","a199","a200","a201","a202","a203","a204","a205","a206","a207","a208","a209","a210","a211","a212","a213","a214","a215","a216","a217","a218","a219","a220","a221","a222","a223","a224","a225","a226","a227","a228","a229","a230","a231","a232","a233","a234","a235","a236","a237","a238","a239","a240","a241","a242","a243","a244","a245","a246","a247","a248","a249","a250","a251","a252","a253","a254","a255","s16","s17","s18","s19","s20","s21","s22","s23","s24","s25","s26","s27","s28","s29","s30","s31","s33","s34","s35","s36","s37","s38","s39","s40","s41","s42","s43","s44","s45","s46","s47","s48","s49","s50","s51","s52","s53","s54","s55","s56","s57","s58","s59","s60","s61","s62","s63","s64","s65","s66","s67","s68","s69","s70","s71","s72","s73","s74","s75","s76","s77","s78","s79","s80","s81","s82","s83","s84","s85","s86","s87","s88","s89","s90","s91","s92","s93","s94","s95","s96","s97","s98","s99","s100","s101", "vcc", "scc", "memory");
}

}

extern "C" void kernel_launch(void* const* d_in, const int* in_sizes, int n_in, void* d_out, int out_size, void* d_ws,
                              size_t ws_size, hipStream_t stream) {
  const uint16_t* x = (const uint16_t*)d_in[0];
  const uint16_t* wqkv = (const uint16_t*)d_in[1];
  const uint16_t* wo = (const uint16_t*)d_in[2];
  const uint16_t* ls = (const uint16_t*)d_in[3];
  const uint16_t* w1 = (const uint16_t*)d_in[4];
  const uint16_t* w2 = (const uint16_t*)d_in[5];
  const uint16_t* rc = (const uint16_t*)d_in[6];
  const int* ridx = (const int*)d_in[7];
  const uint16_t* mask = (const uint16_t*)d_in[8];
  swin::swin_main<<<256, 256, 0, stream>>>(x, mask, (char*)d_ws, (uint16_t*)d_out, wqkv, wo, ls, w1, w2, rc, ridx);
}
```

```cpp
#include <hip/hip_runtime.h>
#include <hip/hip_bf16.h>
#include <stdint.h>

namespace swin {

constexpr unsigned CT_WORDS = 8192u;
constexpr unsigned LDS_BYTES = 163840u;

__device__ __attribute__((aligned(256))) const unsigned CT_TAB[CT_WORDS] = {0x4800u,0x4820u,0x4840u,0x4800u,0x4808u,0x4828u,0x4848u,0x4810u,0x4810u,0x4830u,0x4850u,0x4820u,0x4818u,0x4838u,0x4858u,0x4830u,0x4880u,0x48a0u,0x48c0u,0x4880u,0x4888u,0x48a8u,0x48c8u,0x4890u,0x4890u,0x48b0u,0x48d0u,0x48a0u,0x4898u,0x48b8u,0x48d8u,0x48b0u,0x4920u,0x4900u,0x4960u,0x4920u,0x4928u,0x4908u,0x4968u,0x4930u,0x4930u,0x4910u,0x4970u,0x4900u,0x4938u,0x4918u,0x4978u,0x4910u,0x49a0u,0x4980u,0x49e0u,0x49a0u,0x49a8u,0x4988u,0x49e8u,0x49b0u,0x49b0u,0x4990u,0x49f0u,0x4980u,0x49b8u,0x4998u,0x49f8u,0x4990u,0x4c40u,0x4c60u,0x4c00u,0x4a00u,0x4c48u,0x4c68u,0x4c08u,0x4a10u,0x4c50u,0x4c70u,0x4c10u,0x4a20u,0x4c58u,0x4c78u,0x4c18u,0x4a30u,0x4cc0u,0x4ce0u,0x4c80u,0x4a80u,0x4cc8u,0x4ce8u,0x4c88u,0x4a90u,0x4cd0u,0x4cf0u,0x4c90u,0x4aa0u,0x4cd8u,0x4cf8u,0x4c98u,0x4ab0u,0x4d60u,0x4d40u,0x4d20u,0x4b20u,0x4d68u,0x4d48u,0x4d28u,0x4b30u,0x4d70u,0x4d50u,0x4d30u,0x4b00u,0x4d78u,0x4d58u,0x4d38u,0x4b10u,0x4de0u,0x4dc0u,0x4da0u,0x4ba0u,0x4de8u,0x4dc8u,0x4da8u,0x4bb0u,0x4df0u,0x4dd0u,0x4db0u,0x4b80u,0x4df8u,0x4dd8u,0x4db8u,0x4b90u,0x5000u,0x5020u,0x5040u,0x4c40u,0x5008u,0x5028u,0x5048u,0x4c50u,0x5010u,0x5030u,0x5050u,0x4c60u,0x5018u,0x5038u,0x5058u,0x4c70u,0x5080u,0x50a0u,0x50c0u,0x4cc0u,0x5088u,0x50a8u,0x50c8u,0x4cd0u,0x5090u,0x50b0u,0x50d0u,0x4ce0u,0x5098u,0x50b8u,0x50d8u,0x4cf0u,0x5120u,0x5100u,0x5160u,0x4d60u,0x5128u,0x5108u,0x5168u,0x4d70u,0x5130u,0x5110u,0x5170u,0x4d40u,0x5138u,0x5118u,0x5178u,0x4d50u,0x51a0u,0x5180u,0x51e0u,0x4de0u,0x51a8u,0x5188u,0x51e8u,0x4df0u,0x51b0u,0x5190u,0x51f0u,0x4dc0u,0x51b8u,0x5198u,0x51f8u,0x4dd0u,0x5440u,0x5460u,0x5400u,0x4e40u,0x5448u,0x5468u,0x5408u,0x4e50u,0x5450u,0x5470u,0x5410u,0x4e60u,0x5458u,0x5478u,0x5418u,0x4e70u,0x54c0u,0x54e0u,0x5480u,0x4ec0u,0x54c8u,0x54e8u,0x5488u,0x4ed0u,0x54d0u,0x54f0u,0x5490u,0x4ee0u,0x54d8u,0x54f8u,0x5498u,0x4ef0u,0x5560u,0x5540u,0x5520u,0x4f60u,0x5568u,0x5548u,0x5528u,0x4f70u,0x5570u,0x5550u,0x5530u,0x4f40u,0x5578u,0x5558u,0x5538u,0x4f50u,0x55e0u,0x55c0u,0x55a0u,0x4fe0u,0x55e8u,0x55c8u,0x55a8u,0x4ff0u,0x55f0u,0x55d0u,0x55b0u,0x4fc0u,0x55f8u,0x55d8u,0x55b8u,0x4fd0u,0x4840u,0x1e800u,0x1eb00u,0x0u,0x4848u,0x1ec90u,0x1ef90u,0x10u,0x4850u,0x1f120u,0x1f420u,0x20u,0x4858u,0x1f5b0u,0x1f8b0u,0x30u,0x48c0u,0x1fa40u,0x1fd40u,0x40u,0x48c8u,0x1fed0u,0x201d0u,0x50u,0x48d0u,0x20360u,0x20660u,0x60u,0x48d8u,0x207f0u,0x20af0u,0x70u,0x4960u,0x20c80u,0x20f80u,0x180u,0x4968u,0x21110u,0x21410u,0x190u,0x4970u,0x215a0u,0x218a0u,0x1a0u,0x4978u,0x21a30u,0x21d30u,0x1b0u,0x49e0u,0x21ec0u,0x221c0u,0x1c0u,0x49e8u,0x22350u,0x22650u,0x1d0u,0x49f0u,0x227e0u,0x22ae0u,0x1e0u,0x49f8u,0x22c70u,0x22f70u,0x1f0u,0x4a40u,0x1e830u,0x1eb08u,0x320u,0x4a48u,0x1ecc0u,0x1ef98u,0x330u,0x4a50u,0x1f150u,0x1f428u,0x300u,0x4a58u,0x1f5e0u,0x1f8b8u,0x310u,0x4ac0u,0x1fa70u,0x1fd48u,0x360u,0x4ac8u,0x1ff00u,0x201d8u,0x370u,0x4ad0u,0x20390u,0x20668u,0x340u,0x4ad8u,0x20820u,0x20af8u,0x350u,0x4b60u,0x20cb0u,0x20f88u,0x4a0u,0x4b68u,0x21140u,0x21418u,0x4b0u,0x4b70u,0x215d0u,0x218a8u,0x480u,0x4b78u,0x21a60u,0x21d38u,0x490u,0x4be0u,0x21ef0u,0x221c8u,0x4e0u,0x4be8u,0x22380u,0x22658u,0x4f0u,0x4bf0u,0x22810u,0x22ae8u,0x4c0u,0x4bf8u,0x22ca0u,0x22f78u,0x4d0u,0x4c00u,0x1e860u,0x1eb10u,0x600u,0x4c08u,0x1ecf0u,0x1efa0u,0x610u,0x4c10u,0x1f180u,0x1f430u,0x620u,0x4c18u,0x1f610u,0x1f8c0u,0x630u,0x4c80u,0x1faa0u,0x1fd50u,0x640u,0x4c88u,0x1ff30u,0x201e0u,0x650u,0x4c90u,0x203c0u,0x20670u,0x660u,0x4c98u,0x20850u,0x20b00u,0x670u,0x4d20u,0x20ce0u,0x20f90u,0x780u,0x4d28u,0x21170u,0x21420u,0x790u,0x4d30u,0x21600u,0x218b0u,0x7a0u,0x4d38u,0x21a90u,0x21d40u,0x7b0u,0x4da0u,0x21f20u,0x221d0u,0x7c0u,0x4da8u,0x223b0u,0x22660u,0x7d0u,0x4db0u,0x22840u,0x22af0u,0x7e0u,0x4db8u,0x22cd0u,0x22f80u,0x7f0u,0x4e00u,0x1e890u,0x1eb18u,0x920u,0x4e08u,0x1ed20u,0x1efa8u,0x930u,0x4e10u,0x1f1b0u,0x1f438u,0x900u,0x4e18u,0x1f640u,0x1f8c8u,0x910u,0x4e80u,0x1fad0u,0x1fd58u,0x960u,0x4e88u,0x1ff60u,0x201e8u,0x970u,0x4e90u,0x203f0u,0x20678u,0x940u,0x4e98u,0x20880u,0x20b08u,0x950u,0x4f20u,0x20d10u,0x20f98u,0xaa0u,0x4f28u,0x211a0u,0x21428u,0xab0u,0x4f30u,0x21630u,0x218b8u,0xa80u,0x4f38u,0x21ac0u,0x21d48u,0xa90u,0x4fa0u,0x21f50u,0x221d8u,0xae0u,0x4fa8u,0x223e0u,0x22668u,0xaf0u,0x4fb0u,0x22870u,0x22af8u,0xac0u,0x4fb8u,0x22d00u,0x22f88u,0xad0u,0x0u,0x40u,0x9000u,0x9000u,0x80u,0xc0u,0x9010u,0x9010u,0x110u,0x150u,0x9020u,0x9020u,0x190u,0x1d0u,0x9030u,0x9030u,0x220u,0x260u,0x9040u,0x9040u,0x2a0u,0x2e0u,0x9050u,0x9050u,0x330u,0x370u,0x9060u,0x9060u,0x3b0u,0x3f0u,0x9070u,0x9070u,0x440u,0x400u,0x9080u,0x9080u,0x4c0u,0x480u,0x9090u,0x9090u,0x550u,0x510u,0x90a0u,0x90a0u,0x5d0u,0x590u,0x90b0u,0x90b0u,0x660u,0x620u,0x90c0u,0x90c0u,0x6e0u,0x6a0u,0x90d0u,0x90d0u,0x770u,0x730u,0x90e0u,0x90e0u,0x7f0u,0x7b0u,0x90f0u,0x90f0u,0x10u,0x50u,0x9300u,0x9300u,0x90u,0xd0u,0x9310u,0x9310u,0x100u,0x140u,0x9320u,0x9320u,0x180u,0x1c0u,0x9330u,0x9330u,0x230u,0x270u,0x9340u,0x9340u,0x2b0u,0x2f0u,0x9350u,0x9350u,0x320u,0x360u,0x9360u,0x9360u,0x3a0u,0x3e0u,0x9370u,0x9370u,0x450u,0x410u,0x9380u,0x9380u,0x4d0u,0x490u,0x9390u,0x9390u,0x540u,0x500u,0x93a0u,0x93a0u,0x5c0u,0x580u,0x93b0u,0x93b0u,0x670u,0x630u,0x93c0u,0x93c0u,0x6f0u,0x6b0u,0x93d0u,0x93d0u,0x760u,0x720u,0x93e0u,0x93e0u,0x7e0u,0x7a0u,0x93f0u,0x93f0u,0x20u,0x60u,0x9600u,0x9600u,0xa0u,0xe0u,0x9610u,0x9610u,0x130u,0x170u,0x9620u,0x9620u,0x1b0u,0x1f0u,0x9630u,0x9630u,0x200u,0x240u,0x9640u,0x9640u,0x280u,0x2c0u,0x9650u,0x9650u,0x310u,0x350u,0x9660u,0x9660u,0x390u,0x3d0u,0x9670u,0x9670u,0x460u,0x420u,0x9680u,0x9680u,0x4e0u,0x4a0u,0x9690u,0x9690u,0x570u,0x530u,0x96a0u,0x96a0u,0x5f0u,0x5b0u,0x96b0u,0x96b0u,0x640u,0x600u,0x96c0u,0x96c0u,0x6c0u,0x680u,0x96d0u,0x96d0u,0x750u,0x710u,0x96e0u,0x96e0u,0x7d0u,0x790u,0x96f0u,0x96f0u,0x30u,0x70u,0x9900u,0x9900u,0xb0u,0xf0u,0x9910u,0x9910u,0x120u,0x160u,0x9920u,0x9920u,0x1a0u,0x1e0u,0x9930u,0x9930u,0x210u,0x250u,0x9940u,0x9940u,0x290u,0x2d0u,0x9950u,0x9950u,0x300u,0x340u,0x9960u,0x9960u,0x380u,0x3c0u,0x9970u,0x9970u,0x470u,0x430u,0x9980u,0x9980u,0x4f0u,0x4b0u,0x9990u,0x9990u,0x560u,0x520u,0x99a0u,0x99a0u,0x5e0u,0x5a0u,0x99b0u,0x99b0u,0x650u,0x610u,0x99c0u,0x99c0u,0x6d0u,0x690u,0x99d0u,0x99d0u,0x740u,0x700u,0x99e0u,0x99e0u,0x7c0u,0x780u,0x99f0u,0x99f0u,0x9c00u,0x12000u,0x12040u,0x12000u,0x9c10u,0x12190u,0x121d0u,0x12010u,0x9c20u,0x12320u,0x12360u,0x12020u,0x9c30u,0x124b0u,0x124f0u,0x12030u,0x9c40u,0x12640u,0x12600u,0x12040u,0x9c50u,0x127d0u,0x12790u,0x12050u,0x9c60u,0x12960u,0x12920u,0x12060u,0x9c70u,0x12af0u,0x12ab0u,0x12070u,0x9c80u,0x12c00u,0x12c40u,0x12080u,0x9c90u,0x12d90u,0x12dd0u,0x12090u,0x9ca0u,0x12f20u,0x12f60u,0x120a0u,0x9cb0u,0x130b0u,0x130f0u,0x120b0u,0x9cc0u,0x13240u,0x13200u,0x120c0u,0x9cd0u,0x133d0u,0x13390u,0x120d0u,0x9ce0u,0x13560u,0x13520u,0x120e0u,0x9cf0u,0x136f0u,0x136b0u,0x120f0u,0x9f00u,0x12010u,0x12048u,0x12100u,0x9f10u,0x12180u,0x121d8u,0x12110u,0x9f20u,0x12330u,0x12368u,0x12120u,0x9f30u,0x124a0u,0x124f8u,0x12130u,0x9f40u,0x12650u,0x12608u,0x12140u,0x9f50u,0x127c0u,0x12798u,0x12150u,0x9f60u,0x12970u,0x12928u,0x12160u,0x9f70u,0x12ae0u,0x12ab8u,0x12170u,0x9f80u,0x12c10u,0x12c48u,0x12180u,0x9f90u,0x12d80u,0x12dd8u,0x12190u,0x9fa0u,0x12f30u,0x12f68u,0x121a0u,0x9fb0u,0x130a0u,0x130f8u,0x121b0u,0x9fc0u,0x13250u,0x13208u,0x121c0u,0x9fd0u,0x133c0u,0x13398u,0x121d0u,0x9fe0u,0x13570u,0x13528u,0x121e0u,0x9ff0u,0x136e0u,0x136b8u,0x121f0u,0xa200u,0x12020u,0x12050u,0x12200u,0xa210u,0x121b0u,0x121c0u,0x12210u,0xa220u,0x12300u,0x12370u,0x12220u,0xa230u,0x12490u,0x124e0u,0x12230u,0xa240u,0x12660u,0x12610u,0x12240u,0xa250u,0x127f0u,0x12780u,0x12250u,0xa260u,0x12940u,0x12930u,0x12260u,0xa270u,0x12ad0u,0x12aa0u,0x12270u,0xa280u,0x12c20u,0x12c50u,0x12280u,0xa290u,0x12db0u,0x12dc0u,0x12290u,0xa2a0u,0x12f00u,0x12f70u,0x122a0u,0xa2b0u,0x13090u,0x130e0u,0x122b0u,0xa2c0u,0x13260u,0x13210u,0x122c0u,0xa2d0u,0x133f0u,0x13380u,0x122d0u,0xa2e0u,0x13540u,0x13530u,0x122e0u,0xa2f0u,0x136d0u,0x136a0u,0x122f0u,0xa500u,0x12030u,0x12058u,0x12300u,0xa510u,0x121a0u,0x121c8u,0x12310u,0xa520u,0x12310u,0x12378u,0x12320u,0xa530u,0x12480u,0x124e8u,0x12330u,0xa540u,0x12670u,0x12618u,0x12340u,0xa550u,0x127e0u,0x12788u,0x12350u,0xa560u,0x12950u,0x12938u,0x12360u,0xa570u,0x12ac0u,0x12aa8u,0x12370u,0xa580u,0x12c30u,0x12c58u,0x12380u,0xa590u,0x12da0u,0x12dc8u,0x12390u,0xa5a0u,0x12f10u,0x12f78u,0x123a0u,0xa5b0u,0x13080u,0x130e8u,0x123b0u,0xa5c0u,0x13270u,0x13218u,0x123c0u,0xa5d0u,0x133e0u,0x13388u,0x123d0u,0xa5e0u,0x13550u,0x13538u,0x123e0u,0xa5f0u,0x136c0u,0x136a8u,0x123f0u,0x4800u,0x1c300u,0x1e800u,0x1eb00u,0x4880u,0x1c310u,0x100000u,0x1eb02u,0x4910u,0x1c320u,0x100000u,0x1eb04u,0x4990u,0x1c330u,0x100000u,0x1eb06u,0x4a20u,0x1c340u,0x1e800u,0x1eb08u,0x4aa0u,0x1c350u,0x100000u,0x1eb0au,0x4b30u,0x1c360u,0x100000u,0x1eb0cu,0x4bb0u,0x1c370u,0x100000u,0x1eb0eu,0x4c40u,0x1c380u,0x1e800u,0x1eb10u,0x4cc0u,0x1c390u,0x100000u,0x1eb12u,0x4d50u,0x1c3a0u,0x100000u,0x1eb14u,0x4dd0u,0x1c3b0u,0x100000u,0x1eb16u,0x4e60u,0x1c3c0u,0x1e800u,0x1eb18u,0x4ee0u,0x1c3d0u,0x100000u,0x1eb1au,0x4f70u,0x1c3e0u,0x100000u,0x1eb1cu,0x4ff0u,0x1c3f0u,0x100000u,0x1eb1eu,0x4810u,0x1c400u,0x1e830u,0x1eb00u,0x4890u,0x1c410u,0x100000u,0x1eb02u,0x4900u,0x1c420u,0x100000u,0x1eb04u,0x4980u,0x1c430u,0x100000u,0x1eb06u,0x4a30u,0x1c440u,0x1e830u,0x1eb08u,0x4ab0u,0x1c450u,0x100000u,0x1eb0au,0x4b20u,0x1c460u,0x100000u,0x1eb0cu,0x4ba0u,0x1c470u,0x100000u,0x1eb0eu,0x4c50u,0x1c480u,0x1e830u,0x1eb10u,0x4cd0u,0x1c490u,0x100000u,0x1eb12u,0x4d40u,0x1c4a0u,0x100000u,0x1eb14u,0x4dc0u,0x1c4b0u,0x100000u,0x1eb16u,0x4e70u,0x1c4c0u,0x1e830u,0x1eb18u,0x4ef0u,0x1c4d0u,0x100000u,0x1eb1au,0x4f60u,0x1c4e0u,0x100000u,0x1eb1cu,0x4fe0u,0x1c4f0u,0x100000u,0x1eb1eu,0x4820u,0x1c500u,0x1e860u,0x1eb00u,0x48a0u,0x1c510u,0x100000u,0x1eb02u,0x4930u,0x1c520u,0x100000u,0x1eb04u,0x49b0u,0x1c530u,0x100000u,0x1eb06u,0x4a00u,0x1c540u,0x1e860u,0x1eb08u,0x4a80u,0x1c550u,0x100000u,0x1eb0au,0x4b10u,0x1c560u,0x100000u,0x1eb0cu,0x4b90u,0x1c570u,0x100000u,0x1eb0eu,0x4c60u,0x1c580u,0x1e860u,0x1eb10u,0x4ce0u,0x1c590u,0x100000u,0x1eb12u,0x4d70u,0x1c5a0u,0x100000u,0x1eb14u,0x4df0u,0x1c5b0u,0x100000u,0x1eb16u,0x4e40u,0x1c5c0u,0x1e860u,0x1eb18u,0x4ec0u,0x1c5d0u,0x100000u,0x1eb1au,0x4f50u,0x1c5e0u,0x100000u,0x1eb1cu,0x4fd0u,0x1c5f0u,0x100000u,0x1eb1eu,0x4830u,0x1c600u,0x1e890u,0x1eb00u,0x48b0u,0x1c610u,0x100000u,0x1eb02u,0x4920u,0x1c620u,0x100000u,0x1eb04u,0x49a0u,0x1c630u,0x100000u,0x1eb06u,0x4a10u,0x1c640u,0x1e890u,0x1eb08u,0x4a90u,0x1c650u,0x100000u,0x1eb0au,0x4b00u,0x1c660u,0x100000u,0x1eb0cu,0x4b80u,0x1c670u,0x100000u,0x1eb0eu,0x4c70u,0x1c680u,0x1e890u,0x1eb10u,0x4cf0u,0x1c690u,0x100000u,0x1eb12u,0x4d60u,0x1c6a0u,0x100000u,0x1eb14u,0x4de0u,0x1c6b0u,0x100000u,0x1eb16u,0x4e50u,0x1c6c0u,0x1e890u,0x1eb18u,0x4ed0u,0x1c6d0u,0x100000u,0x1eb1au,0x4f40u,0x1c6e0u,0x100000u,0x1eb1cu,0x4fc0u,0x1c6f0u,0x100000u,0x1eb1eu,0x24800u,0x1d300u,0x1d300u,0xc0100u,0x24802u,0x1d302u,0x100000u,0xc0110u,0x24804u,0x1d304u,0x100000u,0xc0120u,0x24806u,0x1d306u,0x100000u,0xc0130u,0x24a00u,0x1d310u,0x1d300u,0xc0140u,0x24a02u,0x1d312u,0x100000u,0xc0150u,0x24a04u,0x1d314u,0x100000u,0xc0160u,0x24a06u,0x1d316u,0x100000u,0xc0170u,0x24c00u,0x1d320u,0x1d300u,0xc0280u,0x24c02u,0x1d322u,0x100000u,0xc0290u,0x24c04u,0x1d324u,0x100000u,0xc02a0u,0x24c06u,0x1d326u,0x100000u,0xc02b0u,0x24e00u,0x1d330u,0x1d300u,0xc02c0u,0x24e02u,0x1d332u,0x100000u,0xc02d0u,0x24e04u,0x1d334u,0x100000u,0xc02e0u,0x24e06u,0x1d336u,0x100000u,0xc02f0u,0x24808u,0x1d308u,0x1d310u,0xc0410u,0x2480au,0x1d30au,0x100000u,0xc0400u,0x2480cu,0x1d30cu,0x100000u,0xc0430u,0x2480eu,0x1d30eu,0x100000u,0xc0420u,0x24a08u,0x1d318u,0x1d310u,0xc0450u,0x24a0au,0x1d31au,0x100000u,0xc0440u,0x24a0cu,0x1d31cu,0x100000u,0xc0470u,0x24a0eu,0x1d31eu,0x100000u,0xc0460u,0x24c08u,0x1d328u,0x1d310u,0xef990u,0x24c0au,0x1d32au,0x100000u,0xef980u,0x24c0cu,0x1d32cu,0x100000u,0xef9b0u,0x24c0eu,0x1d32eu,0x100000u,0xef9a0u,0x24e08u,0x1d338u,0x1d310u,0xef9d0u,0x24e0au,0x1d33au,0x100000u,0xef9c0u,0x24e0cu,0x1d33cu,0x100000u,0xef9f0u,0x24e0eu,0x1d33eu,0x100000u,0xef9e0u,0x25000u,0x1d340u,0x1d320u,0xefb20u,0x25002u,0x1d342u,0x100000u,0xefb30u,0x25004u,0x1d344u,0x100000u,0xefb00u,0x25006u,0x1d346u,0x100000u,0xefb10u,0x25200u,0x1d350u,0x1d320u,0xefb60u,0x25202u,0x1d352u,0x100000u,0xefb70u,0x25204u,0x1d354u,0x100000u,0xefb40u,0x25206u,0x1d356u,0x100000u,0xefb50u,0x25400u,0x1d360u,0x1d320u,0xefca0u,0x25402u,0x1d362u,0x100000u,0xefcb0u,0x25404u,0x1d364u,0x100000u,0xefc80u,0x25406u,0x1d366u,0x100000u,0xefc90u,0x25600u,0x1d370u,0x1d320u,0xefce0u,0x25602u,0x1d372u,0x100000u,0xefcf0u,0x25604u,0x1d374u,0x100000u,0xefcc0u,0x25606u,0x1d376u,0x100000u,0xefcd0u,0x100000u,0x1d348u,0x1d330u,0xefe30u,0x100000u,0x1d34au,0x100000u,0xefe20u,0x100000u,0x1d34cu,0x100000u,0xefe10u,0x100000u,0x1d34eu,0x100000u,0xefe00u,0x100000u,0x1d358u,0x1d330u,0xefe70u,0x100000u,0x1d35au,0x100000u,0xefe60u,0x100000u,0x1d35cu,0x100000u,0xefe50u,0x100000u,0x1d35eu,0x100000u,0xefe40u,0x100000u,0x1d368u,0x1d330u,0xeffb0u,0x100000u,0x1d36au,0x100000u,0xeffa0u,0x100000u,0x1d36cu,0x100000u,0xeff90u,0x100000u,0x1d36eu,0x100000u,0xeff80u,0x100000u,0x1d378u,0x1d330u,0xefff0u,0x100000u,0x1d37au,0x100000u,0xeffe0u,0x100000u,0x1d37cu,0x100000u,0xeffd0u,0x100000u,0x1d37eu,0x100000u,0xeffc0u,0x0u,0x30420u,0x8fc50u,0xc0000u,0x10u,0x30430u,0x8fc40u,0xc0010u,0x20u,0x30400u,0x8fc70u,0xc0020u,0x30u,0x30410u,0x8fc60u,0xc0030u,0x40u,0x30460u,0x8fc10u,0xc0040u,0x50u,0x30470u,0x8fc00u,0xc0050u,0x60u,0x30440u,0x8fc30u,0xc0060u,0x70u,0x30450u,0x8fc20u,0xc0070u,0x80u,0x304b0u,0x8fcd0u,0xc0080u,0x90u,0x304a0u,0x8fcc0u,0xc0090u,0xa0u,0x30490u,0x8fcf0u,0xc00a0u,0xb0u,0x30480u,0x8fce0u,0xc00b0u,0xc0u,0x304f0u,0x8fc90u,0xc00c0u,0xd0u,0x304e0u,0x8fc80u,0xc00d0u,0xe0u,0x304d0u,0x8fcb0u,0xc00e0u,0xf0u,0x304c0u,0x8fca0u,0xc00f0u,0x100u,0x30530u,0x8fd60u,0xc0100u,0x110u,0x30520u,0x8fd70u,0xc0110u,0x120u,0x30510u,0x8fd40u,0xc0120u,0x130u,0x30500u,0x8fd50u,0xc0130u,0x140u,0x30570u,0x8fd20u,0xc0140u,0x150u,0x30560u,0x8fd30u,0xc0150u,0x160u,0x30550u,0x8fd00u,0xc0160u,0x170u,0x30540u,0x8fd10u,0xc0170u,0x190u,0x305b0u,0x8fde0u,0xc0190u,0x180u,0x305a0u,0x8fdf0u,0xc0180u,0x1b0u,0x30590u,0x8fdc0u,0xc01b0u,0x1a0u,0x30580u,0x8fdd0u,0xc01a0u,0x1d0u,0x305f0u,0x8fda0u,0xc01d0u,0x1c0u,0x305e0u,0x8fdb0u,0xc01c0u,0x1f0u,0x305d0u,0x8fd80u,0xc01f0u,0x1e0u,0x305c0u,0x8fd90u,0xc01e0u,0x210u,0x30640u,0x8fe60u,0xc0210u,0x200u,0x30650u,0x8fe70u,0xc0200u,0x230u,0x30660u,0x8fe40u,0xc0230u,0x220u,0x30670u,0x8fe50u,0xc0220u,0x250u,0x30600u,0x8fe20u,0xc0250u,0x240u,0x30610u,0x8fe30u,0xc0240u,0x270u,0x30620u,0x8fe00u,0xc0270u,0x260u,0x30630u,0x8fe10u,0xc0260u,0x290u,0x306c0u,0x8fef0u,0xc0290u,0x280u,0x306d0u,0x8fee0u,0xc0280u,0x2b0u,0x306e0u,0x8fed0u,0xc02b0u,0x2a0u,0x306f0u,0x8fec0u,0xc02a0u,0x2d0u,0x30680u,0x8feb0u,0xc02d0u,0x2c0u,0x30690u,0x8fea0u,0xc02c0u,0x2f0u,0x306a0u,0x8fe90u,0xc02f0u,0x2e0u,0x306b0u,0x8fe80u,0xc02e0u,0x320u,0x30740u,0x8ff70u,0xc0320u,0x330u,0x30750u,0x8ff60u,0xc0330u,0x300u,0x30760u,0x8ff50u,0xc0300u,0x310u,0x30770u,0x8ff40u,0xc0310u,0x360u,0x30700u,0x8ff30u,0xc0360u,0x370u,0x30710u,0x8ff20u,0xc0370u,0x340u,0x30720u,0x8ff10u,0xc0340u,0x350u,0x30730u,0x8ff00u,0xc0350u,0x3a0u,0x307d0u,0x8fff0u,0xc03a0u,0x3b0u,0x307c0u,0x8ffe0u,0xc03b0u,0x380u,0x307f0u,0x8ffd0u,0xc0380u,0x390u,0x307e0u,0x8ffc0u,0xc0390u,0x3e0u,0x30790u,0x8ffb0u,0xc03e0u,0x3f0u,0x30780u,0x8ffa0u,0xc03f0u,0x3c0u,0x307b0u,0x8ff90u,0xc03c0u,0x3d0u,0x307a0u,0x8ff80u,0xc03d0u,0x0u,0x11f820u,0x0u,0x7030cu,0x0u,0x11f830u,0x0u,0x7030cu,0x0u,0x11f800u,0x0u,0x7030cu,0x0u,0x11f810u,0x0u,0x7030cu,0x0u,0x11f860u,0x0u,0x7030cu,0x0u,0x11f870u,0x0u,0x7030cu,0x0u,0x11f840u,0x0u,0x7030cu,0x0u,0x11f850u,0x0u,0x7030cu,0x0u,0x11f8b0u,0x0u,0x7230cu,0x0u,0x11f8a0u,0x0u,0x7230cu,0x0u,0x11f890u,0x0u,0x7230cu,0x0u,0x11f880u,0x0u,0x7230cu,0x0u,0x11f8f0u,0x0u,0x7230cu,0x0u,0x11f8e0u,0x0u,0x7230cu,0x0u,0x11f8d0u,0x0u,0x7230cu,0x0u,0x11f8c0u,0x0u,0x7230cu,0x0u,0x11f930u,0x0u,0x7232cu,0x0u,0x11f920u,0x0u,0x7232cu,0x0u,0x11f910u,0x0u,0x7232cu,0x0u,0x11f900u,0x0u,0x7232cu,0x0u,0x11f970u,0x0u,0x7232cu,0x0u,0x11f960u,0x0u,0x7232cu,0x0u,0x11f950u,0x0u,0x7232cu,0x0u,0x11f940u,0x0u,0x7232cu,0x0u,0x11f9b0u,0x0u,0x72124u,0x0u,0x11f9a0u,0x0u,0x72124u,0x0u,0x11f990u,0x0u,0x72124u,0x0u,0x11f980u,0x0u,0x72124u,0x0u,0x11f9f0u,0x0u,0x72124u,0x0u,0x11f9e0u,0x0u,0x72124u,0x0u,0x11f9d0u,0x0u,0x72124u,0x0u,0x11f9c0u,0x0u,0x72124u,0x0u,0x11fa40u,0x0u,0x721a6u,0x0u,0x11fa50u,0x0u,0x721a6u,0x0u,0x11fa60u,0x0u,0x721a6u,0x0u,0x11fa70u,0x0u,0x721a6u,0x0u,0x11fa00u,0x0u,0x721a6u,0x0u,0x11fa10u,0x0u,0x721a6u,0x0u,0x11fa20u,0x0u,0x721a6u,0x0u,0x11fa30u,0x0u,0x721a6u,0x0u,0x11fac0u,0x0u,0x72186u,0x0u,0x11fad0u,0x0u,0x72186u,0x0u,0x11fae0u,0x0u,0x72186u,0x0u,0x11faf0u,0x0u,0x72186u,0x0u,0x11fa80u,0x0u,0x72186u,0x0u,0x11fa90u,0x0u,0x72186u,0x0u,0x11faa0u,0x0u,0x72186u,0x0u,0x11fab0u,0x0u,0x72186u,0x0u,0x11fb40u,0x0u,0x72186u,0x0u,0x11fb50u,0x0u,0x72186u,0x0u,0x11fb60u,0x0u,0x72186u,0x0u,0x11fb70u,0x0u,0x72186u,0x0u,0x11fb00u,0x0u,0x72186u,0x0u,0x11fb10u,0x0u,0x72186u,0x0u,0x11fb20u,0x0u,0x72186u,0x0u,0x11fb30u,0x0u,0x72186u,0x0u,0x11fbd0u,0x0u,0x7230cu,0x0u,0x11fbc0u,0x0u,0x7230cu,0x0u,0x11fbf0u,0x0u,0x7230cu,0x0u,0x11fbe0u,0x0u,0x7230cu,0x0u,0x11fb90u,0x0u,0x7230cu,0x0u,0x11fb80u,0x0u,0x7230cu,0x0u,0x11fbb0u,0x0u,0x7230cu,0x0u,0x11fba0u,0x0u,0x7230cu,0x5800u,0x5820u,0x4860u,0x4860u,0x5808u,0x5828u,0x4868u,0x4870u,0x5810u,0x5830u,0x4870u,0x5800u,0x5818u,0x5838u,0x4878u,0x5810u,0x5880u,0x58a0u,0x48e0u,0x48e0u,0x5888u,0x58a8u,0x48e8u,0x48f0u,0x5890u,0x58b0u,0x48f0u,0x5880u,0x5898u,0x58b8u,0x48f8u,0x5890u,0x5920u,0x5900u,0x4940u,0x4940u,0x5928u,0x5908u,0x4948u,0x4950u,0x5930u,0x5910u,0x4950u,0x5920u,0x5938u,0x5918u,0x4958u,0x5930u,0x59a0u,0x5980u,0x49c0u,0x49c0u,0x59a8u,0x5988u,0x49c8u,0x49d0u,0x59b0u,0x5990u,0x49d0u,0x59a0u,0x59b8u,0x5998u,0x49d8u,0x59b0u,0x5c40u,0x5c60u,0x4c20u,0x4a60u,0x5c48u,0x5c68u,0x4c28u,0x4a70u,0x5c50u,0x5c70u,0x4c30u,0x5a00u,0x5c58u,0x5c78u,0x4c38u,0x5a10u,0x5cc0u,0x5ce0u,0x4ca0u,0x4ae0u,0x5cc8u,0x5ce8u,0x4ca8u,0x4af0u,0x5cd0u,0x5cf0u,0x4cb0u,0x5a80u,0x5cd8u,0x5cf8u,0x4cb8u,0x5a90u,0x5d60u,0x5d40u,0x4d00u,0x4b40u,0x5d68u,0x5d48u,0x4d08u,0x4b50u,0x5d70u,0x5d50u,0x4d10u,0x5b20u,0x5d78u,0x5d58u,0x4d18u,0x5b30u,0x5de0u,0x5dc0u,0x4d80u,0x4bc0u,0x5de8u,0x5dc8u,0x4d88u,0x4bd0u,0x5df0u,0x5dd0u,0x4d90u,0x5ba0u,0x5df8u,0x5dd8u,0x4d98u,0x5bb0u,0x6000u,0x6020u,0x5060u,0x4c20u,0x6008u,0x6028u,0x5068u,0x4c30u,0x6010u,0x6030u,0x5070u,0x5c40u,0x6018u,0x6038u,0x5078u,0x5c50u,0x6080u,0x60a0u,0x50e0u,0x4ca0u,0x6088u,0x60a8u,0x50e8u,0x4cb0u,0x6090u,0x60b0u,0x50f0u,0x5cc0u,0x6098u,0x60b8u,0x50f8u,0x5cd0u,0x6120u,0x6100u,0x5140u,0x4d00u,0x6128u,0x6108u,0x5148u,0x4d10u,0x6130u,0x6110u,0x5150u,0x5d60u,0x6138u,0x6118u,0x5158u,0x5d70u,0x61a0u,0x6180u,0x51c0u,0x4d80u,0x61a8u,0x6188u,0x51c8u,0x4d90u,0x61b0u,0x6190u,0x51d0u,0x5de0u,0x61b8u,0x6198u,0x51d8u,0x5df0u,0x6440u,0x6460u,0x5420u,0x4e20u,0x6448u,0x6468u,0x5428u,0x4e30u,0x6450u,0x6470u,0x5430u,0x5e40u,0x6458u,0x6478u,0x5438u,0x5e50u,0x64c0u,0x64e0u,0x54a0u,0x4ea0u,0x64c8u,0x64e8u,0x54a8u,0x4eb0u,0x64d0u,0x64f0u,0x54b0u,0x5ec0u,0x64d8u,0x64f8u,0x54b8u,0x5ed0u,0x6560u,0x6540u,0x5500u,0x4f00u,0x6568u,0x6548u,0x5508u,0x4f10u,0x6570u,0x6550u,0x5510u,0x5f60u,0x6578u,0x6558u,0x5518u,0x5f70u,0x65e0u,0x65c0u,0x5580u,0x4f80u,0x65e8u,0x65c8u,0x5588u,0x4f90u,0x65f0u,0x65d0u,0x5590u,0x5fe0u,0x65f8u,0x65d8u,0x5598u,0x5ff0u,0x5820u,0x1e8c0u,0x1eb60u,0xc40u,0x5828u,0x1ed50u,0x1eff0u,0xc50u,0x5830u,0x1f1e0u,0x1f480u,0xc60u,0x5838u,0x1f670u,0x1f910u,0xc70u,0x58a0u,0x1fb00u,0x1fda0u,0xc00u,0x58a8u,0x1ff90u,0x20230u,0xc10u,0x58b0u,0x20420u,0x206c0u,0xc20u,0x58b8u,0x208b0u,0x20b50u,0xc30u,0x5900u,0x20d40u,0x20fe0u,0xdc0u,0x5908u,0x211d0u,0x21470u,0xdd0u,0x5910u,0x21660u,0x21900u,0xde0u,0x5918u,0x21af0u,0x21d90u,0xdf0u,0x5980u,0x21f80u,0x22220u,0xd80u,0x5988u,0x22410u,0x226b0u,0xd90u,0x5990u,0x228a0u,0x22b40u,0xda0u,0x5998u,0x22d30u,0x22fd0u,0xdb0u,0x5a20u,0x1e8f0u,0x1eb68u,0xf60u,0x5a28u,0x1ed80u,0x1eff8u,0xf70u,0x5a30u,0x1f210u,0x1f488u,0xf40u,0x5a38u,0x1f6a0u,0x1f918u,0xf50u,0x5aa0u,0x1fb30u,0x1fda8u,0xf20u,0x5aa8u,0x1ffc0u,0x20238u,0xf30u,0x5ab0u,0x20450u,0x206c8u,0xf00u,0x5ab8u,0x208e0u,0x20b58u,0xf10u,0x5b00u,0x20d70u,0x20fe8u,0x10e0u,0x5b08u,0x21200u,0x21478u,0x10f0u,0x5b10u,0x21690u,0x21908u,0x10c0u,0x5b18u,0x21b20u,0x21d98u,0x10d0u,0x5b80u,0x21fb0u,0x22228u,0x10a0u,0x5b88u,0x22440u,0x226b8u,0x10b0u,0x5b90u,0x228d0u,0x22b48u,0x1080u,0x5b98u,0x22d60u,0x22fd8u,0x1090u,0x5c60u,0x1e920u,0x1eb70u,0x1240u,0x5c68u,0x1edb0u,0x1f000u,0x1250u,0x5c70u,0x1f240u,0x1f490u,0x1260u,0x5c78u,0x1f6d0u,0x1f920u,0x1270u,0x5ce0u,0x1fb60u,0x1fdb0u,0x1200u,0x5ce8u,0x1fff0u,0x20240u,0x1210u,0x5cf0u,0x20480u,0x206d0u,0x1220u,0x5cf8u,0x20910u,0x20b60u,0x1230u,0x5d40u,0x20da0u,0x20ff0u,0x13c0u,0x5d48u,0x21230u,0x21480u,0x13d0u,0x5d50u,0x216c0u,0x21910u,0x13e0u,0x5d58u,0x21b50u,0x21da0u,0x13f0u,0x5dc0u,0x21fe0u,0x22230u,0x1380u,0x5dc8u,0x22470u,0x226c0u,0x1390u,0x5dd0u,0x22900u,0x22b50u,0x13a0u,0x5dd8u,0x22d90u,0x22fe0u,0x13b0u,0x5e60u,0x1e950u,0x1eb78u,0x1560u,0x5e68u,0x1ede0u,0x1f008u,0x1570u,0x5e70u,0x1f270u,0x1f498u,0x1540u,0x5e78u,0x1f700u,0x1f928u,0x1550u,0x5ee0u,0x1fb90u,0x1fdb8u,0x1520u,0x5ee8u,0x20020u,0x20248u,0x1530u,0x5ef0u,0x204b0u,0x206d8u,0x1500u,0x5ef8u,0x20940u,0x20b68u,0x1510u,0x5f40u,0x20dd0u,0x20ff8u,0x16e0u,0x5f48u,0x21260u,0x21488u,0x16f0u,0x5f50u,0x216f0u,0x21918u,0x16c0u,0x5f58u,0x21b80u,0x21da8u,0x16d0u,0x5fc0u,0x22010u,0x22238u,0x16a0u,0x5fc8u,0x224a0u,0x226c8u,0x16b0u,0x5fd0u,0x22930u,0x22b58u,0x1680u,0x5fd8u,0x22dc0u,0x22fe8u,0x1690u,0x0u,0x40u,0x9000u,0xa800u,0x80u,0xc0u,0x9010u,0xa810u,0x110u,0x150u,0x9020u,0xa820u,0x190u,0x1d0u,0x9030u,0xa830u,0x220u,0x260u,0x9040u,0xa840u,0x2a0u,0x2e0u,0x9050u,0xa850u,0x330u,0x370u,0x9060u,0xa860u,0x3b0u,0x3f0u,0x9070u,0xa870u,0x440u,0x400u,0x9080u,0xa880u,0x4c0u,0x480u,0x9090u,0xa890u,0x550u,0x510u,0x90a0u,0xa8a0u,0x5d0u,0x590u,0x90b0u,0xa8b0u,0x660u,0x620u,0x90c0u,0xa8c0u,0x6e0u,0x6a0u,0x90d0u,0xa8d0u,0x770u,0x730u,0x90e0u,0xa8e0u,0x7f0u,0x7b0u,0x90f0u,0xa8f0u,0x10u,0x50u,0x9300u,0xab00u,0x90u,0xd0u,0x9310u,0xab10u,0x100u,0x140u,0x9320u,0xab20u,0x180u,0x1c0u,0x9330u,0xab30u,0x230u,0x270u,0x9340u,0xab40u,0x2b0u,0x2f0u,0x9350u,0xab50u,0x320u,0x360u,0x9360u,0xab60u,0x3a0u,0x3e0u,0x9370u,0xab70u,0x450u,0x410u,0x9380u,0xab80u,0x4d0u,0x490u,0x9390u,0xab90u,0x540u,0x500u,0x93a0u,0xaba0u,0x5c0u,0x580u,0x93b0u,0xabb0u,0x670u,0x630u,0x93c0u,0xabc0u,0x6f0u,0x6b0u,0x93d0u,0xabd0u,0x760u,0x720u,0x93e0u,0xabe0u,0x7e0u,0x7a0u,0x93f0u,0xabf0u,0x20u,0x60u,0x9600u,0xae00u,0xa0u,0xe0u,0x9610u,0xae10u,0x130u,0x170u,0x9620u,0xae20u,0x1b0u,0x1f0u,0x9630u,0xae30u,0x200u,0x240u,0x9640u,0xae40u,0x280u,0x2c0u,0x9650u,0xae50u,0x310u,0x350u,0x9660u,0xae60u,0x390u,0x3d0u,0x9670u,0xae70u,0x460u,0x420u,0x9680u,0xae80u,0x4e0u,0x4a0u,0x9690u,0xae90u,0x570u,0x530u,0x96a0u,0xaea0u,0x5f0u,0x5b0u,0x96b0u,0xaeb0u,0x640u,0x600u,0x96c0u,0xaec0u,0x6c0u,0x680u,0x96d0u,0xaed0u,0x750u,0x710u,0x96e0u,0xaee0u,0x7d0u,0x790u,0x96f0u,0xaef0u,0x30u,0x70u,0x9900u,0xb100u,0xb0u,0xf0u,0x9910u,0xb110u,0x120u,0x160u,0x9920u,0xb120u,0x1a0u,0x1e0u,0x9930u,0xb130u,0x210u,0x250u,0x9940u,0xb140u,0x290u,0x2d0u,0x9950u,0xb150u,0x300u,0x340u,0x9960u,0xb160u,0x380u,0x3c0u,0x9970u,0xb170u,0x470u,0x430u,0x9980u,0xb180u,0x4f0u,0x4b0u,0x9990u,0xb190u,0x560u,0x520u,0x99a0u,0xb1a0u,0x5e0u,0x5a0u,0x99b0u,0xb1b0u,0x650u,0x610u,0x99c0u,0xb1c0u,0x6d0u,0x690u,0x99d0u,0xb1d0u,0x740u,0x700u,0x99e0u,0xb1e0u,0x7c0u,0x780u,0x99f0u,0xb1f0u,0x9c08u,0x12060u,0x120a0u,0x12400u,0x9c18u,0x121f0u,0x12230u,0x12410u,0x9c28u,0x12340u,0x12380u,0x12420u,0x9c38u,0x124d0u,0x12510u,0x12430u,0x9c48u,0x12620u,0x126e0u,0x12440u,0x9c58u,0x127b0u,0x12870u,0x12450u,0x9c68u,0x12900u,0x129c0u,0x12460u,0x9c78u,0x12a90u,0x12b50u,0x12470u,0x9c88u,0x12c60u,0x12ca0u,0x12480u,0x9c98u,0x12df0u,0x12e30u,0x12490u,0x9ca8u,0x12f40u,0x12f80u,0x124a0u,0x9cb8u,0x130d0u,0x13110u,0x124b0u,0x9cc8u,0x13220u,0x132e0u,0x124c0u,0x9cd8u,0x133b0u,0x13470u,0x124d0u,0x9ce8u,0x13500u,0x135c0u,0x124e0u,0x9cf8u,0x13690u,0x13750u,0x124f0u,0x9f08u,0x12070u,0x120a8u,0x12500u,0x9f18u,0x121e0u,0x12238u,0x12510u,0x9f28u,0x12350u,0x12388u,0x12520u,0x9f38u,0x124c0u,0x12518u,0x12530u,0x9f48u,0x12630u,0x126e8u,0x12540u,0x9f58u,0x127a0u,0x12878u,0x12550u,0x9f68u,0x12910u,0x129c8u,0x12560u,0x9f78u,0x12a80u,0x12b58u,0x12570u,0x9f88u,0x12c70u,0x12ca8u,0x12580u,0x9f98u,0x12de0u,0x12e38u,0x12590u,0x9fa8u,0x12f50u,0x12f88u,0x125a0u,0x9fb8u,0x130c0u,0x13118u,0x125b0u,0x9fc8u,0x13230u,0x132e8u,0x125c0u,0x9fd8u,0x133a0u,0x13478u,0x125d0u,0x9fe8u,0x13510u,0x135c8u,0x125e0u,0x9ff8u,0x13680u,0x13758u,0x125f0u,0xa208u,0x12080u,0x120b0u,0x12600u,0xa218u,0x12210u,0x12220u,0x12610u,0xa228u,0x123a0u,0x12390u,0x12620u,0xa238u,0x12530u,0x12500u,0x12630u,0xa248u,0x126c0u,0x126f0u,0x12640u,0xa258u,0x12850u,0x12860u,0x12650u,0xa268u,0x129e0u,0x129d0u,0x12660u,0xa278u,0x12b70u,0x12b40u,0x12670u,0xa288u,0x12c80u,0x12cb0u,0x12680u,0xa298u,0x12e10u,0x12e20u,0x12690u,0xa2a8u,0x12fa0u,0x12f90u,0x126a0u,0xa2b8u,0x13130u,0x13100u,0x126b0u,0xa2c8u,0x132c0u,0x132f0u,0x126c0u,0xa2d8u,0x13450u,0x13460u,0x126d0u,0xa2e8u,0x135e0u,0x135d0u,0x126e0u,0xa2f8u,0x13770u,0x13740u,0x126f0u,0xa508u,0x12090u,0x120b8u,0x12700u,0xa518u,0x12200u,0x12228u,0x12710u,0xa528u,0x123b0u,0x12398u,0x12720u,0xa538u,0x12520u,0x12508u,0x12730u,0xa548u,0x126d0u,0x126f8u,0x12740u,0xa558u,0x12840u,0x12868u,0x12750u,0xa568u,0x129f0u,0x129d8u,0x12760u,0xa578u,0x12b60u,0x12b48u,0x12770u,0xa588u,0x12c90u,0x12cb8u,0x12780u,0xa598u,0x12e00u,0x12e28u,0x12790u,0xa5a8u,0x12fb0u,0x12f98u,0x127a0u,0xa5b8u,0x13120u,0x13108u,0x127b0u,0xa5c8u,0x132d0u,0x132f8u,0x127c0u,0xa5d8u,0x13440u,0x13468u,0x127d0u,0xa5e8u,0x135f0u,0x135d8u,0x127e0u,0xa5f8u,0x13760u,0x13748u,0x127f0u,0x5000u,0x1c300u,0x1e8c0u,0x1eb60u,0x5080u,0x1c310u,0x100000u,0x1eb62u,0x5110u,0x1c320u,0x100000u,0x1eb64u,0x5190u,0x1c330u,0x100000u,0x1eb66u,0x5220u,0x1c340u,0x1e8c0u,0x1eb68u,0x52a0u,0x1c350u,0x100000u,0x1eb6au,0x5330u,0x1c360u,0x100000u,0x1eb6cu,0x53b0u,0x1c370u,0x100000u,0x1eb6eu,0x5440u,0x1c380u,0x1e8c0u,0x1eb70u,0x54c0u,0x1c390u,0x100000u,0x1eb72u,0x5550u,0x1c3a0u,0x100000u,0x1eb74u,0x55d0u,0x1c3b0u,0x100000u,0x1eb76u,0x5660u,0x1c3c0u,0x1e8c0u,0x1eb78u,0x56e0u,0x1c3d0u,0x100000u,0x1eb7au,0x5770u,0x1c3e0u,0x100000u,0x1eb7cu,0x57f0u,0x1c3f0u,0x100000u,0x1eb7eu,0x5010u,0x1c400u,0x1e8f0u,0x1eb60u,0x5090u,0x1c410u,0x100000u,0x1eb62u,0x5100u,0x1c420u,0x100000u,0x1eb64u,0x5180u,0x1c430u,0x100000u,0x1eb66u,0x5230u,0x1c440u,0x1e8f0u,0x1eb68u,0x52b0u,0x1c450u,0x100000u,0x1eb6au,0x5320u,0x1c460u,0x100000u,0x1eb6cu,0x53a0u,0x1c470u,0x100000u,0x1eb6eu,0x5450u,0x1c480u,0x1e8f0u,0x1eb70u,0x54d0u,0x1c490u,0x100000u,0x1eb72u,0x5540u,0x1c4a0u,0x100000u,0x1eb74u,0x55c0u,0x1c4b0u,0x100000u,0x1eb76u,0x5670u,0x1c4c0u,0x1e8f0u,0x1eb78u,0x56f0u,0x1c4d0u,0x100000u,0x1eb7au,0x5760u,0x1c4e0u,0x100000u,0x1eb7cu,0x57e0u,0x1c4f0u,0x100000u,0x1eb7eu,0x5020u,0x1c500u,0x1e920u,0x1eb60u,0x50a0u,0x1c510u,0x100000u,0x1eb62u,0x5130u,0x1c520u,0x100000u,0x1eb64u,0x51b0u,0x1c530u,0x100000u,0x1eb66u,0x5200u,0x1c540u,0x1e920u,0x1eb68u,0x5280u,0x1c550u,0x100000u,0x1eb6au,0x5310u,0x1c560u,0x100000u,0x1eb6cu,0x5390u,0x1c570u,0x100000u,0x1eb6eu,0x5460u,0x1c580u,0x1e920u,0x1eb70u,0x54e0u,0x1c590u,0x100000u,0x1eb72u,0x5570u,0x1c5a0u,0x100000u,0x1eb74u,0x55f0u,0x1c5b0u,0x100000u,0x1eb76u,0x5640u,0x1c5c0u,0x1e920u,0x1eb78u,0x56c0u,0x1c5d0u,0x100000u,0x1eb7au,0x5750u,0x1c5e0u,0x100000u,0x1eb7cu,0x57d0u,0x1c5f0u,0x100000u,0x1eb7eu,0x5030u,0x1c600u,0x1e950u,0x1eb60u,0x50b0u,0x1c610u,0x100000u,0x1eb62u,0x5120u,0x1c620u,0x100000u,0x1eb64u,0x51a0u,0x1c630u,0x100000u,0x1eb66u,0x5210u,0x1c640u,0x1e950u,0x1eb68u,0x5290u,0x1c650u,0x100000u,0x1eb6au,0x5300u,0x1c660u,0x100000u,0x1eb6cu,0x5380u,0x1c670u,0x100000u,0x1eb6eu,0x5470u,0x1c680u,0x1e950u,0x1eb70u,0x54f0u,0x1c690u,0x100000u,0x1eb72u,0x5560u,0x1c6a0u,0x100000u,0x1eb74u,0x55e0u,0x1c6b0u,0x100000u,0x1eb76u,0x5650u,0x1c6c0u,0x1e950u,0x1eb78u,0x56d0u,0x1c6d0u,0x100000u,0x1eb7au,0x5740u,0x1c6e0u,0x100000u,0x1eb7cu,0x57c0u,0x1c6f0u,0x100000u,0x1eb7eu,0x25800u,0x1d380u,0x1d380u,0xf0140u,0x25802u,0x1d382u,0x100000u,0xf0150u,0x25804u,0x1d384u,0x100000u,0xf0160u,0x25806u,0x1d386u,0x100000u,0xf0170u,0x25a00u,0x1d390u,0x1d380u,0xf0100u,0x25a02u,0x1d392u,0x100000u,0xf0110u,0x25a04u,0x1d394u,0x100000u,0xf0120u,0x25a06u,0x1d396u,0x100000u,0xf0130u,0x25c00u,0x1d3a0u,0x1d380u,0xf02c0u,0x25c02u,0x1d3a2u,0x100000u,0xf02d0u,0x25c04u,0x1d3a4u,0x100000u,0xf02e0u,0x25c06u,0x1d3a6u,0x100000u,0xf02f0u,0x25e00u,0x1d3b0u,0x1d380u,0xf0280u,0x25e02u,0x1d3b2u,0x100000u,0xf0290u,0x25e04u,0x1d3b4u,0x100000u,0xf02a0u,0x25e06u,0x1d3b6u,0x100000u,0xf02b0u,0x25808u,0x1d388u,0x1d390u,0x11f850u,0x2580au,0x1d38au,0x100000u,0x11f840u,0x2580cu,0x1d38cu,0x100000u,0x11f870u,0x2580eu,0x1d38eu,0x100000u,0x11f860u,0x25a08u,0x1d398u,0x1d390u,0x11f810u,0x25a0au,0x1d39au,0x100000u,0x11f800u,0x25a0cu,0x1d39cu,0x100000u,0x11f830u,0x25a0eu,0x1d39eu,0x100000u,0x11f820u,0x25c08u,0x1d3a8u,0x1d390u,0x11f9d0u,0x25c0au,0x1d3aau,0x100000u,0x11f9c0u,0x25c0cu,0x1d3acu,0x100000u,0x11f9f0u,0x25c0eu,0x1d3aeu,0x100000u,0x11f9e0u,0x25e08u,0x1d3b8u,0x1d390u,0x11f990u,0x25e0au,0x1d3bau,0x100000u,0x11f980u,0x25e0cu,0x1d3bcu,0x100000u,0x11f9b0u,0x25e0eu,0x1d3beu,0x100000u,0x11f9a0u,0x25008u,0x1d3c0u,0x1d3a0u,0x11fb60u,0x2500au,0x1d3c2u,0x100000u,0x11fb70u,0x2500cu,0x1d3c4u,0x100000u,0x11fb40u,0x2500eu,0x1d3c6u,0x100000u,0x11fb50u,0x25208u,0x1d3d0u,0x1d3a0u,0x11fb20u,0x2520au,0x1d3d2u,0x100000u,0x11fb30u,0x2520cu,0x1d3d4u,0x100000u,0x11fb00u,0x2520eu,0x1d3d6u,0x100000u,0x11fb10u,0x25408u,0x1d3e0u,0x1d3a0u,0x11fce0u,0x2540au,0x1d3e2u,0x100000u,0x11fcf0u,0x2540cu,0x1d3e4u,0x100000u,0x11fcc0u,0x2540eu,0x1d3e6u,0x100000u,0x11fcd0u,0x25608u,0x1d3f0u,0x1d3a0u,0x11fca0u,0x2560au,0x1d3f2u,0x100000u,0x11fcb0u,0x2560cu,0x1d3f4u,0x100000u,0x11fc80u,0x2560eu,0x1d3f6u,0x100000u,0x11fc90u,0x100000u,0x1d3c8u,0x1d3b0u,0x11fe70u,0x100000u,0x1d3cau,0x100000u,0x11fe60u,0x100000u,0x1d3ccu,0x100000u,0x11fe50u,0x100000u,0x1d3ceu,0x100000u,0x11fe40u,0x100000u,0x1d3d8u,0x1d3b0u,0x11fe30u,0x100000u,0x1d3dau,0x100000u,0x11fe20u,0x100000u,0x1d3dcu,0x100000u,0x11fe10u,0x100000u,0x1d3deu,0x100000u,0x11fe00u,0x100000u,0x1d3e8u,0x1d3b0u,0x11fff0u,0x100000u,0x1d3eau,0x100000u,0x11ffe0u,0x100000u,0x1d3ecu,0x100000u,0x11ffd0u,0x100000u,0x1d3eeu,0x100000u,0x11ffc0u,0x100000u,0x1d3f8u,0x1d3b0u,0x11ffb0u,0x100000u,0x1d3fau,0x100000u,0x11ffa0u,0x100000u,0x1d3fcu,0x100000u,0x11ff90u,0x100000u,0x1d3feu,0x100000u,0x11ff80u,0x420u,0x30850u,0x90000u,0xc0420u,0x430u,0x30840u,0x90010u,0xc0430u,0x400u,0x30870u,0x90020u,0xc0400u,0x410u,0x30860u,0x90030u,0xc0410u,0x460u,0x30810u,0x90040u,0xc0460u,0x470u,0x30800u,0x90050u,0xc0470u,0x440u,0x30830u,0x90060u,0xc0440u,0x450u,0x30820u,0x90070u,0xc0450u,0x4b0u,0x308d0u,0x90080u,0xef8b0u,0x4a0u,0x308c0u,0x90090u,0xef8a0u,0x490u,0x308f0u,0x900a0u,0xef890u,0x480u,0x308e0u,0x900b0u,0xef880u,0x4f0u,0x30890u,0x900c0u,0xef8f0u,0x4e0u,0x30880u,0x900d0u,0xef8e0u,0x4d0u,0x308b0u,0x900e0u,0xef8d0u,0x4c0u,0x308a0u,0x900f0u,0xef8c0u,0x530u,0x5fd60u,0x90100u,0xef930u,0x520u,0x5fd70u,0x90110u,0xef920u,0x510u,0x5fd40u,0x90120u,0xef910u,0x500u,0x5fd50u,0x90130u,0xef900u,0x570u,0x5fd20u,0x90140u,0xef970u,0x560u,0x5fd30u,0x90150u,0xef960u,0x550u,0x5fd00u,0x90160u,0xef950u,0x540u,0x5fd10u,0x90170u,0xef940u,0x5b0u,0x5fde0u,0x90190u,0xef9b0u,0x5a0u,0x5fdf0u,0x90180u,0xef9a0u,0x590u,0x5fdc0u,0x901b0u,0xef990u,0x580u,0x5fdd0u,0x901a0u,0xef980u,0x5f0u,0x5fda0u,0x901d0u,0xef9f0u,0x5e0u,0x5fdb0u,0x901c0u,0xef9e0u,0x5d0u,0x5fd80u,0x901f0u,0xef9d0u,0x5c0u,0x5fd90u,0x901e0u,0xef9c0u,0x640u,0x5fe60u,0x90210u,0xefa40u,0x650u,0x5fe70u,0x90200u,0xefa50u,0x660u,0x5fe40u,0x90230u,0xefa60u,0x670u,0x5fe50u,0x90220u,0xefa70u,0x600u,0x5fe20u,0x90250u,0xefa00u,0x610u,0x5fe30u,0x90240u,0xefa10u,0x620u,0x5fe00u,0x90270u,0xefa20u,0x630u,0x5fe10u,0x90260u,0xefa30u,0x6c0u,0x5fef0u,0x90290u,0xefac0u,0x6d0u,0x5fee0u,0x90280u,0xefad0u,0x6e0u,0x5fed0u,0x902b0u,0xefae0u,0x6f0u,0x5fec0u,0x902a0u,0xefaf0u,0x680u,0x5feb0u,0x902d0u,0xefa80u,0x690u,0x5fea0u,0x902c0u,0xefa90u,0x6a0u,0x5fe90u,0x902f0u,0xefaa0u,0x6b0u,0x5fe80u,0x902e0u,0xefab0u,0x740u,0x5ff70u,0x90320u,0xefb40u,0x750u,0x5ff60u,0x90330u,0xefb50u,0x760u,0x5ff50u,0x90300u,0xefb60u,0x770u,0x5ff40u,0x90310u,0xefb70u,0x700u,0x5ff30u,0x90360u,0xefb00u,0x710u,0x5ff20u,0x90370u,0xefb10u,0x720u,0x5ff10u,0x90340u,0xefb20u,0x730u,0x5ff00u,0x90350u,0xefb30u,0x7d0u,0x5fff0u,0x903a0u,0xefbd0u,0x7c0u,0x5ffe0u,0x903b0u,0xefbc0u,0x7f0u,0x5ffd0u,0x90380u,0xefbf0u,0x7e0u,0x5ffc0u,0x90390u,0xefbe0u,0x790u,0x5ffb0u,0x903e0u,0xefb90u,0x780u,0x5ffa0u,0x903f0u,0xefb80u,0x7b0u,0x5ff90u,0x903c0u,0xefbb0u,0x7a0u,0x5ff80u,0x903d0u,0xefba0u,0x0u,0x11fc50u,0x0u,0x7230cu,0x0u,0x11fc40u,0x0u,0x7230cu,0x0u,0x11fc70u,0x0u,0x7230cu,0x0u,0x11fc60u,0x0u,0x7230cu,0x0u,0x11fc10u,0x0u,0x7230cu,0x0u,0x11fc00u,0x0u,0x7230cu,0x0u,0x11fc30u,0x0u,0x7230cu,0x0u,0x11fc20u,0x0u,0x7230cu,0x0u,0x11fcd0u,0x0u,0x5232cu,0x0u,0x11fcc0u,0x0u,0x5232cu,0x0u,0x11fcf0u,0x0u,0x5232cu,0x0u,0x11fce0u,0x0u,0x5232cu,0x0u,0x11fc90u,0x0u,0x5232cu,0x0u,0x11fc80u,0x0u,0x5232cu,0x0u,0x11fcb0u,0x0u,0x5232cu,0x0u,0x11fca0u,0x0u,0x5232cu,0x0u,0x11fd60u,0x0u,0xd0124u,0x0u,0x11fd70u,0x0u,0xd0124u,0x0u,0x11fd40u,0x0u,0xd0124u,0x0u,0x11fd50u,0x0u,0xd0124u,0x0u,0x11fd20u,0x0u,0xd0124u,0x0u,0x11fd30u,0x0u,0xd0124u,0x0u,0x11fd00u,0x0u,0xd0124u,0x0u,0x11fd10u,0x0u,0xd0124u,0x0u,0x11fde0u,0x0u,0xd81a6u,0x0u,0x11fdf0u,0x0u,0xd81a6u,0x0u,0x11fdc0u,0x0u,0xd81a6u,0x0u,0x11fdd0u,0x0u,0xd81a6u,0x0u,0x11fda0u,0x0u,0xd81a6u,0x0u,0x11fdb0u,0x0u,0xd81a6u,0x0u,0x11fd80u,0x0u,0xd81a6u,0x0u,0x11fd90u,0x0u,0xd81a6u,0x0u,0x11fe60u,0x0u,0xd8996u,0x0u,0x11fe70u,0x0u,0xd8996u,0x0u,0x11fe40u,0x0u,0xd8996u,0x0u,0x11fe50u,0x0u,0xd8996u,0x0u,0x11fe20u,0x0u,0xd8996u,0x0u,0x11fe30u,0x0u,0xd8996u,0x0u,0x11fe00u,0x0u,0xd8996u,0x0u,0x11fe10u,0x0u,0xd8996u,0x0u,0x11fef0u,0x0u,0xd8996u,0x0u,0x11fee0u,0x0u,0xd8996u,0x0u,0x11fed0u,0x0u,0xd8996u,0x0u,0x11fec0u,0x0u,0xd8996u,0x0u,0x11feb0u,0x0u,0xd8996u,0x0u,0x11fea0u,0x0u,0xd8996u,0x0u,0x11fe90u,0x0u,0xd8996u,0x0u,0x11fe80u,0x0u,0xd8996u,0x0u,0x11ff70u,0x0u,0xd8b1cu,0x0u,0x11ff60u,0x0u,0xd8b1cu,0x0u,0x11ff50u,0x0u,0xd8b1cu,0x0u,0x11ff40u,0x0u,0xd8b1cu,0x0u,0x11ff30u,0x0u,0xd8b1cu,0x0u,0x11ff20u,0x0u,0xd8b1cu,0x0u,0x11ff10u,0x0u,0xd8b1cu,0x0u,0x11ff00u,0x0u,0xd8b1cu,0x0u,0x11fff0u,0x0u,0xd8b1cu,0x0u,0x11ffe0u,0x0u,0xd8b1cu,0x0u,0x11ffd0u,0x0u,0xd8b1cu,0x0u,0x11ffc0u,0x0u,0xd8b1cu,0x0u,0x11ffb0u,0x0u,0xd8b1cu,0x0u,0x11ffa0u,0x0u,0xd8b1cu,0x0u,0x11ff90u,0x0u,0xd8b1cu,0x0u,0x11ff80u,0x0u,0xd8b1cu,0x5840u,0x5860u,0x6800u,0x5840u,0x5848u,0x5868u,0x6808u,0x5850u,0x5850u,0x5870u,0x6810u,0x5860u,0x5858u,0x5878u,0x6818u,0x5870u,0x58c0u,0x58e0u,0x6880u,0x58c0u,0x58c8u,0x58e8u,0x6888u,0x58d0u,0x58d0u,0x58f0u,0x6890u,0x58e0u,0x58d8u,0x58f8u,0x6898u,0x58f0u,0x5960u,0x5940u,0x6920u,0x5960u,0x5968u,0x5948u,0x6928u,0x5970u,0x5970u,0x5950u,0x6930u,0x5940u,0x5978u,0x5958u,0x6938u,0x5950u,0x59e0u,0x59c0u,0x69a0u,0x59e0u,0x59e8u,0x59c8u,0x69a8u,0x59f0u,0x59f0u,0x59d0u,0x69b0u,0x59c0u,0x59f8u,0x59d8u,0x69b8u,0x59d0u,0x5c00u,0x5c20u,0x6c40u,0x5a40u,0x5c08u,0x5c28u,0x6c48u,0x5a50u,0x5c10u,0x5c30u,0x6c50u,0x5a60u,0x5c18u,0x5c38u,0x6c58u,0x5a70u,0x5c80u,0x5ca0u,0x6cc0u,0x5ac0u,0x5c88u,0x5ca8u,0x6cc8u,0x5ad0u,0x5c90u,0x5cb0u,0x6cd0u,0x5ae0u,0x5c98u,0x5cb8u,0x6cd8u,0x5af0u,0x5d20u,0x5d00u,0x6d60u,0x5b60u,0x5d28u,0x5d08u,0x6d68u,0x5b70u,0x5d30u,0x5d10u,0x6d70u,0x5b40u,0x5d38u,0x5d18u,0x6d78u,0x5b50u,0x5da0u,0x5d80u,0x6de0u,0x5be0u,0x5da8u,0x5d88u,0x6de8u,0x5bf0u,0x5db0u,0x5d90u,0x6df0u,0x5bc0u,0x5db8u,0x5d98u,0x6df8u,0x5bd0u,0x6040u,0x6060u,0x7000u,0x5c00u,0x6048u,0x6068u,0x7008u,0x5c10u,0x6050u,0x6070u,0x7010u,0x5c20u,0x6058u,0x6078u,0x7018u,0x5c30u,0x60c0u,0x60e0u,0x7080u,0x5c80u,0x60c8u,0x60e8u,0x7088u,0x5c90u,0x60d0u,0x60f0u,0x7090u,0x5ca0u,0x60d8u,0x60f8u,0x7098u,0x5cb0u,0x6160u,0x6140u,0x7120u,0x5d20u,0x6168u,0x6148u,0x7128u,0x5d30u,0x6170u,0x6150u,0x7130u,0x5d00u,0x6178u,0x6158u,0x7138u,0x5d10u,0x61e0u,0x61c0u,0x71a0u,0x5da0u,0x61e8u,0x61c8u,0x71a8u,0x5db0u,0x61f0u,0x61d0u,0x71b0u,0x5d80u,0x61f8u,0x61d8u,0x71b8u,0x5d90u,0x6400u,0x6420u,0x7440u,0x5e00u,0x6408u,0x6428u,0x7448u,0x5e10u,0x6410u,0x6430u,0x7450u,0x5e20u,0x6418u,0x6438u,0x7458u,0x5e30u,0x6480u,0x64a0u,0x74c0u,0x5e80u,0x6488u,0x64a8u,0x74c8u,0x5e90u,0x6490u,0x64b0u,0x74d0u,0x5ea0u,0x6498u,0x64b8u,0x74d8u,0x5eb0u,0x6520u,0x6500u,0x7560u,0x5f20u,0x6528u,0x6508u,0x7568u,0x5f30u,0x6530u,0x6510u,0x7570u,0x5f00u,0x6538u,0x6518u,0x7578u,0x5f10u,0x65a0u,0x6580u,0x75e0u,0x5fa0u,0x65a8u,0x6588u,0x75e8u,0x5fb0u,0x65b0u,0x6590u,0x75f0u,0x5f80u,0x65b8u,0x6598u,0x75f8u,0x5f90u,0x6800u,0x1e980u,0x1ebc0u,0x1800u,0x6808u,0x1ee10u,0x1f050u,0x1810u,0x6810u,0x1f2a0u,0x1f4e0u,0x1820u,0x6818u,0x1f730u,0x1f970u,0x1830u,0x6880u,0x1fbc0u,0x1fe00u,0x1840u,0x6888u,0x20050u,0x20290u,0x1850u,0x6890u,0x204e0u,0x20720u,0x1860u,0x6898u,0x20970u,0x20bb0u,0x1870u,0x6920u,0x20e00u,0x21040u,0x1980u,0x6928u,0x21290u,0x214d0u,0x1990u,0x6930u,0x21720u,0x21960u,0x19a0u,0x6938u,0x21bb0u,0x21df0u,0x19b0u,0x69a0u,0x22040u,0x22280u,0x19c0u,0x69a8u,0x224d0u,0x22710u,0x19d0u,0x69b0u,0x22960u,0x22ba0u,0x19e0u,0x69b8u,0x22df0u,0x23030u,0x19f0u,0x6a00u,0x1e9b0u,0x1ebc8u,0x1b20u,0x6a08u,0x1ee40u,0x1f058u,0x1b30u,0x6a10u,0x1f2d0u,0x1f4e8u,0x1b00u,0x6a18u,0x1f760u,0x1f978u,0x1b10u,0x6a80u,0x1fbf0u,0x1fe08u,0x1b60u,0x6a88u,0x20080u,0x20298u,0x1b70u,0x6a90u,0x20510u,0x20728u,0x1b40u,0x6a98u,0x209a0u,0x20bb8u,0x1b50u,0x6b20u,0x20e30u,0x21048u,0x1ca0u,0x6b28u,0x212c0u,0x214d8u,0x1cb0u,0x6b30u,0x21750u,0x21968u,0x1c80u,0x6b38u,0x21be0u,0x21df8u,0x1c90u,0x6ba0u,0x22070u,0x22288u,0x1ce0u,0x6ba8u,0x22500u,0x22718u,0x1cf0u,0x6bb0u,0x22990u,0x22ba8u,0x1cc0u,0x6bb8u,0x22e20u,0x23038u,0x1cd0u,0x6c40u,0x1e9e0u,0x1ebd0u,0x1e00u,0x6c48u,0x1ee70u,0x1f060u,0x1e10u,0x6c50u,0x1f300u,0x1f4f0u,0x1e20u,0x6c58u,0x1f790u,0x1f980u,0x1e30u,0x6cc0u,0x1fc20u,0x1fe10u,0x1e40u,0x6cc8u,0x200b0u,0x202a0u,0x1e50u,0x6cd0u,0x20540u,0x20730u,0x1e60u,0x6cd8u,0x209d0u,0x20bc0u,0x1e70u,0x6d60u,0x20e60u,0x21050u,0x1f80u,0x6d68u,0x212f0u,0x214e0u,0x1f90u,0x6d70u,0x21780u,0x21970u,0x1fa0u,0x6d78u,0x21c10u,0x21e00u,0x1fb0u,0x6de0u,0x220a0u,0x22290u,0x1fc0u,0x6de8u,0x22530u,0x22720u,0x1fd0u,0x6df0u,0x229c0u,0x22bb0u,0x1fe0u,0x6df8u,0x22e50u,0x23040u,0x1ff0u,0x6e40u,0x1ea10u,0x1ebd8u,0x2120u,0x6e48u,0x1eea0u,0x1f068u,0x2130u,0x6e50u,0x1f330u,0x1f4f8u,0x2100u,0x6e58u,0x1f7c0u,0x1f988u,0x2110u,0x6ec0u,0x1fc50u,0x1fe18u,0x2160u,0x6ec8u,0x200e0u,0x202a8u,0x2170u,0x6ed0u,0x20570u,0x20738u,0x2140u,0x6ed8u,0x20a00u,0x20bc8u,0x2150u,0x6f60u,0x20e90u,0x21058u,0x22a0u,0x6f68u,0x21320u,0x214e8u,0x22b0u,0x6f70u,0x217b0u,0x21978u,0x2280u,0x6f78u,0x21c40u,0x21e08u,0x2290u,0x6fe0u,0x220d0u,0x22298u,0x22e0u,0x6fe8u,0x22560u,0x22728u,0x22f0u,0x6ff0u,0x229f0u,0x22bb8u,0x22c0u,0x6ff8u,0x22e80u,0x23048u,0x22d0u,0x0u,0x40u,0x9000u,0xb400u,0x80u,0xc0u,0x9010u,0xb410u,0x110u,0x150u,0x9020u,0xb420u,0x190u,0x1d0u,0x9030u,0xb430u,0x220u,0x260u,0x9040u,0xb440u,0x2a0u,0x2e0u,0x9050u,0xb450u,0x330u,0x370u,0x9060u,0xb460u,0x3b0u,0x3f0u,0x9070u,0xb470u,0x440u,0x400u,0x9080u,0xb480u,0x4c0u,0x480u,0x9090u,0xb490u,0x550u,0x510u,0x90a0u,0xb4a0u,0x5d0u,0x590u,0x90b0u,0xb4b0u,0x660u,0x620u,0x90c0u,0xb4c0u,0x6e0u,0x6a0u,0x90d0u,0xb4d0u,0x770u,0x730u,0x90e0u,0xb4e0u,0x7f0u,0x7b0u,0x90f0u,0xb4f0u,0x10u,0x50u,0x9300u,0xb700u,0x90u,0xd0u,0x9310u,0xb710u,0x100u,0x140u,0x9320u,0xb720u,0x180u,0x1c0u,0x9330u,0xb730u,0x230u,0x270u,0x9340u,0xb740u,0x2b0u,0x2f0u,0x9350u,0xb750u,0x320u,0x360u,0x9360u,0xb760u,0x3a0u,0x3e0u,0x9370u,0xb770u,0x450u,0x410u,0x9380u,0xb780u,0x4d0u,0x490u,0x9390u,0xb790u,0x540u,0x500u,0x93a0u,0xb7a0u,0x5c0u,0x580u,0x93b0u,0xb7b0u,0x670u,0x630u,0x93c0u,0xb7c0u,0x6f0u,0x6b0u,0x93d0u,0xb7d0u,0x760u,0x720u,0x93e0u,0xb7e0u,0x7e0u,0x7a0u,0x93f0u,0xb7f0u,0x20u,0x60u,0x9600u,0xba00u,0xa0u,0xe0u,0x9610u,0xba10u,0x130u,0x170u,0x9620u,0xba20u,0x1b0u,0x1f0u,0x9630u,0xba30u,0x200u,0x240u,0x9640u,0xba40u,0x280u,0x2c0u,0x9650u,0xba50u,0x310u,0x350u,0x9660u,0xba60u,0x390u,0x3d0u,0x9670u,0xba70u,0x460u,0x420u,0x9680u,0xba80u,0x4e0u,0x4a0u,0x9690u,0xba90u,0x570u,0x530u,0x96a0u,0xbaa0u,0x5f0u,0x5b0u,0x96b0u,0xbab0u,0x640u,0x600u,0x96c0u,0xbac0u,0x6c0u,0x680u,0x96d0u,0xbad0u,0x750u,0x710u,0x96e0u,0xbae0u,0x7d0u,0x790u,0x96f0u,0xbaf0u,0x30u,0x70u,0x9900u,0xbd00u,0xb0u,0xf0u,0x9910u,0xbd10u,0x120u,0x160u,0x9920u,0xbd20u,0x1a0u,0x1e0u,0x9930u,0xbd30u,0x210u,0x250u,0x9940u,0xbd40u,0x290u,0x2d0u,0x9950u,0xbd50u,0x300u,0x340u,0x9960u,0xbd60u,0x380u,0x3c0u,0x9970u,0xbd70u,0x470u,0x430u,0x9980u,0xbd80u,0x4f0u,0x4b0u,0x9990u,0xbd90u,0x560u,0x520u,0x99a0u,0xbda0u,0x5e0u,0x5a0u,0x99b0u,0xbdb0u,0x650u,0x610u,0x99c0u,0xbdc0u,0x6d0u,0x690u,0x99d0u,0xbdd0u,0x740u,0x700u,0x99e0u,0xbde0u,0x7c0u,0x780u,0x99f0u,0xbdf0u,0xc000u,0x120c0u,0x12100u,0x12800u,0xc010u,0x12250u,0x12290u,0x12810u,0xc020u,0x123e0u,0x12420u,0x12820u,0xc030u,0x12570u,0x125b0u,0x12830u,0xc040u,0x12680u,0x12740u,0x12840u,0xc050u,0x12810u,0x128d0u,0x12850u,0xc060u,0x129a0u,0x12a60u,0x12860u,0xc070u,0x12b30u,0x12bf0u,0x12870u,0xc080u,0x12cc0u,0x12d00u,0x12880u,0xc090u,0x12e50u,0x12e90u,0x12890u,0xc0a0u,0x12fe0u,0x13020u,0x128a0u,0xc0b0u,0x13170u,0x131b0u,0x128b0u,0xc0c0u,0x13280u,0x13340u,0x128c0u,0xc0d0u,0x13410u,0x134d0u,0x128d0u,0xc0e0u,0x135a0u,0x13660u,0x128e0u,0xc0f0u,0x13730u,0x137f0u,0x128f0u,0xc300u,0x120d0u,0x12108u,0x12900u,0xc310u,0x12240u,0x12298u,0x12910u,0xc320u,0x123f0u,0x12428u,0x12920u,0xc330u,0x12560u,0x125b8u,0x12930u,0xc340u,0x12690u,0x12748u,0x12940u,0xc350u,0x12800u,0x128d8u,0x12950u,0xc360u,0x129b0u,0x12a68u,0x12960u,0xc370u,0x12b20u,0x12bf8u,0x12970u,0xc380u,0x12cd0u,0x12d08u,0x12980u,0xc390u,0x12e40u,0x12e98u,0x12990u,0xc3a0u,0x12ff0u,0x13028u,0x129a0u,0xc3b0u,0x13160u,0x131b8u,0x129b0u,0xc3c0u,0x13290u,0x13348u,0x129c0u,0xc3d0u,0x13400u,0x134d8u,0x129d0u,0xc3e0u,0x135b0u,0x13668u,0x129e0u,0xc3f0u,0x13720u,0x137f8u,0x129f0u,0xc600u,0x120e0u,0x12110u,0x12a00u,0xc610u,0x12270u,0x12280u,0x12a10u,0xc620u,0x123c0u,0x12430u,0x12a20u,0xc630u,0x12550u,0x125a0u,0x12a30u,0xc640u,0x126a0u,0x12750u,0x12a40u,0xc650u,0x12830u,0x128c0u,0x12a50u,0xc660u,0x12980u,0x12a70u,0x12a60u,0xc670u,0x12b10u,0x12be0u,0x12a70u,0xc680u,0x12ce0u,0x12d10u,0x12a80u,0xc690u,0x12e70u,0x12e80u,0x12a90u,0xc6a0u,0x12fc0u,0x13030u,0x12aa0u,0xc6b0u,0x13150u,0x131a0u,0x12ab0u,0xc6c0u,0x132a0u,0x13350u,0x12ac0u,0xc6d0u,0x13430u,0x134c0u,0x12ad0u,0xc6e0u,0x13580u,0x13670u,0x12ae0u,0xc6f0u,0x13710u,0x137e0u,0x12af0u,0xc900u,0x120f0u,0x12118u,0x12b00u,0xc910u,0x12260u,0x12288u,0x12b10u,0xc920u,0x123d0u,0x12438u,0x12b20u,0xc930u,0x12540u,0x125a8u,0x12b30u,0xc940u,0x126b0u,0x12758u,0x12b40u,0xc950u,0x12820u,0x128c8u,0x12b50u,0xc960u,0x12990u,0x12a78u,0x12b60u,0xc970u,0x12b00u,0x12be8u,0x12b70u,0xc980u,0x12cf0u,0x12d18u,0x12b80u,0xc990u,0x12e60u,0x12e88u,0x12b90u,0xc9a0u,0x12fd0u,0x13038u,0x12ba0u,0xc9b0u,0x13140u,0x131a8u,0x12bb0u,0xc9c0u,0x132b0u,0x13358u,0x12bc0u,0xc9d0u,0x13420u,0x134c8u,0x12bd0u,0xc9e0u,0x13590u,0x13678u,0x12be0u,0xc9f0u,0x13700u,0x137e8u,0x12bf0u,0x7800u,0x1c300u,0x1e980u,0x1ebc0u,0x7880u,0x1c310u,0x100000u,0x1ebc2u,0x7910u,0x1c320u,0x100000u,0x1ebc4u,0x7990u,0x1c330u,0x100000u,0x1ebc6u,0x7a20u,0x1c340u,0x1e980u,0x1ebc8u,0x7aa0u,0x1c350u,0x100000u,0x1ebcau,0x7b30u,0x1c360u,0x100000u,0x1ebccu,0x7bb0u,0x1c370u,0x100000u,0x1ebceu,0x7c40u,0x1c380u,0x1e980u,0x1ebd0u,0x7cc0u,0x1c390u,0x100000u,0x1ebd2u,0x7d50u,0x1c3a0u,0x100000u,0x1ebd4u,0x7dd0u,0x1c3b0u,0x100000u,0x1ebd6u,0x7e60u,0x1c3c0u,0x1e980u,0x1ebd8u,0x7ee0u,0x1c3d0u,0x100000u,0x1ebdau,0x7f70u,0x1c3e0u,0x100000u,0x1ebdcu,0x7ff0u,0x1c3f0u,0x100000u,0x1ebdeu,0x7810u,0x1c400u,0x1e9b0u,0x1ebc0u,0x7890u,0x1c410u,0x100000u,0x1ebc2u,0x7900u,0x1c420u,0x100000u,0x1ebc4u,0x7980u,0x1c430u,0x100000u,0x1ebc6u,0x7a30u,0x1c440u,0x1e9b0u,0x1ebc8u,0x7ab0u,0x1c450u,0x100000u,0x1ebcau,0x7b20u,0x1c460u,0x100000u,0x1ebccu,0x7ba0u,0x1c470u,0x100000u,0x1ebceu,0x7c50u,0x1c480u,0x1e9b0u,0x1ebd0u,0x7cd0u,0x1c490u,0x100000u,0x1ebd2u,0x7d40u,0x1c4a0u,0x100000u,0x1ebd4u,0x7dc0u,0x1c4b0u,0x100000u,0x1ebd6u,0x7e70u,0x1c4c0u,0x1e9b0u,0x1ebd8u,0x7ef0u,0x1c4d0u,0x100000u,0x1ebdau,0x7f60u,0x1c4e0u,0x100000u,0x1ebdcu,0x7fe0u,0x1c4f0u,0x100000u,0x1ebdeu,0x7820u,0x1c500u,0x1e9e0u,0x1ebc0u,0x78a0u,0x1c510u,0x100000u,0x1ebc2u,0x7930u,0x1c520u,0x100000u,0x1ebc4u,0x79b0u,0x1c530u,0x100000u,0x1ebc6u,0x7a00u,0x1c540u,0x1e9e0u,0x1ebc8u,0x7a80u,0x1c550u,0x100000u,0x1ebcau,0x7b10u,0x1c560u,0x100000u,0x1ebccu,0x7b90u,0x1c570u,0x100000u,0x1ebceu,0x7c60u,0x1c580u,0x1e9e0u,0x1ebd0u,0x7ce0u,0x1c590u,0x100000u,0x1ebd2u,0x7d70u,0x1c5a0u,0x100000u,0x1ebd4u,0x7df0u,0x1c5b0u,0x100000u,0x1ebd6u,0x7e40u,0x1c5c0u,0x1e9e0u,0x1ebd8u,0x7ec0u,0x1c5d0u,0x100000u,0x1ebdau,0x7f50u,0x1c5e0u,0x100000u,0x1ebdcu,0x7fd0u,0x1c5f0u,0x100000u,0x1ebdeu,0x7830u,0x1c600u,0x1ea10u,0x1ebc0u,0x78b0u,0x1c610u,0x100000u,0x1ebc2u,0x7920u,0x1c620u,0x100000u,0x1ebc4u,0x79a0u,0x1c630u,0x100000u,0x1ebc6u,0x7a10u,0x1c640u,0x1ea10u,0x1ebc8u,0x7a90u,0x1c650u,0x100000u,0x1ebcau,0x7b00u,0x1c660u,0x100000u,0x1ebccu,0x7b80u,0x1c670u,0x100000u,0x1ebceu,0x7c70u,0x1c680u,0x1ea10u,0x1ebd0u,0x7cf0u,0x1c690u,0x100000u,0x1ebd2u,0x7d60u,0x1c6a0u,0x100000u,0x1ebd4u,0x7de0u,0x1c6b0u,0x100000u,0x1ebd6u,0x7e50u,0x1c6c0u,0x1ea10u,0x1ebd8u,0x7ed0u,0x1c6d0u,0x100000u,0x1ebdau,0x7f40u,0x1c6e0u,0x100000u,0x1ebdcu,0x7fc0u,0x1c6f0u,0x100000u,0x1ebdeu,0x26000u,0x1d400u,0x1d400u,0x80000000u,0x26002u,0x1d402u,0x100000u,0x80000000u,0x26004u,0x1d404u,0x100000u,0x80000000u,0x26006u,0x1d406u,0x100000u,0x80000000u,0x26200u,0x1d410u,0x1d400u,0x80000000u,0x26202u,0x1d412u,0x100000u,0x80000000u,0x26204u,0x1d414u,0x100000u,0x80000000u,0x26206u,0x1d416u,0x100000u,0x80000000u,0x26400u,0x1d420u,0x1d400u,0x80000000u,0x26402u,0x1d422u,0x100000u,0x80000000u,0x26404u,0x1d424u,0x100000u,0x80000000u,0x26406u,0x1d426u,0x100000u,0x80000000u,0x26600u,0x1d430u,0x1d400u,0x80000000u,0x26602u,0x1d432u,0x100000u,0x80000000u,0x26604u,0x1d434u,0x100000u,0x80000000u,0x26606u,0x1d436u,0x100000u,0x80000000u,0x26008u,0x1d408u,0x1d410u,0x80000000u,0x2600au,0x1d40au,0x100000u,0x80000000u,0x2600cu,0x1d40cu,0x100000u,0x80000000u,0x2600eu,0x1d40eu,0x100000u,0x80000000u,0x26208u,0x1d418u,0x1d410u,0x80000000u,0x2620au,0x1d41au,0x100000u,0x80000000u,0x2620cu,0x1d41cu,0x100000u,0x80000000u,0x2620eu,0x1d41eu,0x100000u,0x80000000u,0x26408u,0x1d428u,0x1d410u,0x80000000u,0x2640au,0x1d42au,0x100000u,0x80000000u,0x2640cu,0x1d42cu,0x100000u,0x80000000u,0x2640eu,0x1d42eu,0x100000u,0x80000000u,0x26608u,0x1d438u,0x1d410u,0x80000000u,0x2660au,0x1d43au,0x100000u,0x80000000u,0x2660cu,0x1d43cu,0x100000u,0x80000000u,0x2660eu,0x1d43eu,0x100000u,0x80000000u,0x26800u,0x1d440u,0x1d420u,0x80000000u,0x26802u,0x1d442u,0x100000u,0x80000000u,0x26804u,0x1d444u,0x100000u,0x80000000u,0x26806u,0x1d446u,0x100000u,0x80000000u,0x26a00u,0x1d450u,0x1d420u,0x80000000u,0x26a02u,0x1d452u,0x100000u,0x80000000u,0x26a04u,0x1d454u,0x100000u,0x80000000u,0x26a06u,0x1d456u,0x100000u,0x80000000u,0x26c00u,0x1d460u,0x1d420u,0x80000000u,0x26c02u,0x1d462u,0x100000u,0x80000000u,0x26c04u,0x1d464u,0x100000u,0x80000000u,0x26c06u,0x1d466u,0x100000u,0x80000000u,0x26e00u,0x1d470u,0x1d420u,0x80000000u,0x26e02u,0x1d472u,0x100000u,0x80000000u,0x26e04u,0x1d474u,0x100000u,0x80000000u,0x26e06u,0x1d476u,0x100000u,0x80000000u,0x100000u,0x1d448u,0x1d430u,0x80000000u,0x100000u,0x1d44au,0x100000u,0x80000000u,0x100000u,0x1d44cu,0x100000u,0x80000000u,0x100000u,0x1d44eu,0x100000u,0x80000000u,0x100000u,0x1d458u,0x1d430u,0x80000000u,0x100000u,0x1d45au,0x100000u,0x80000000u,0x100000u,0x1d45cu,0x100000u,0x80000000u,0x100000u,0x1d45eu,0x100000u,0x80000000u,0x100000u,0x1d468u,0x1d430u,0x80000000u,0x100000u,0x1d46au,0x100000u,0x80000000u,0x100000u,0x1d46cu,0x100000u,0x80000000u,0x100000u,0x1d46eu,0x100000u,0x80000000u,0x100000u,0x1d478u,0x1d430u,0x80000000u,0x100000u,0x1d47au,0x100000u,0x80000000u,0x100000u,0x1d47cu,0x100000u,0x80000000u,0x100000u,0x1d47eu,0x100000u,0x80000000u,0x850u,0x60000u,0x90420u,0xefc50u,0x840u,0x60010u,0x90430u,0xefc40u,0x870u,0x60020u,0x90400u,0xefc70u,0x860u,0x60030u,0x90410u,0xefc60u,0x810u,0x60040u,0x90460u,0xefc10u,0x800u,0x60050u,0x90470u,0xefc00u,0x830u,0x60060u,0x90440u,0xefc30u,0x820u,0x60070u,0x90450u,0xefc20u,0x8d0u,0x60080u,0x904b0u,0xefcd0u,0x8c0u,0x60090u,0x904a0u,0xefcc0u,0x8f0u,0x600a0u,0x90490u,0xefcf0u,0x8e0u,0x600b0u,0x90480u,0xefce0u,0x890u,0x600c0u,0x904f0u,0xefc90u,0x880u,0x600d0u,0x904e0u,0xefc80u,0x8b0u,0x600e0u,0x904d0u,0xefcb0u,0x8a0u,0x600f0u,0x904c0u,0xefca0u,0x960u,0x60100u,0x90530u,0xefd60u,0x970u,0x60110u,0x90520u,0xefd70u,0x940u,0x60120u,0x90510u,0xefd40u,0x950u,0x60130u,0x90500u,0xefd50u,0x920u,0x60140u,0x90570u,0xefd20u,0x930u,0x60150u,0x90560u,0xefd30u,0x900u,0x60160u,0x90550u,0xefd00u,0x910u,0x60170u,0x90540u,0xefd10u,0x9e0u,0x60190u,0x905b0u,0xefde0u,0x9f0u,0x60180u,0x905a0u,0xefdf0u,0x9c0u,0x601b0u,0x90590u,0xefdc0u,0x9d0u,0x601a0u,0x90580u,0xefdd0u,0x9a0u,0x601d0u,0x905f0u,0xefda0u,0x9b0u,0x601c0u,0x905e0u,0xefdb0u,0x980u,0x601f0u,0x905d0u,0xefd80u,0x990u,0x601e0u,0x905c0u,0xefd90u,0xa60u,0x60210u,0xbfa40u,0xefe60u,0xa70u,0x60200u,0xbfa50u,0xefe70u,0xa40u,0x60230u,0xbfa60u,0xefe40u,0xa50u,0x60220u,0xbfa70u,0xefe50u,0xa20u,0x60250u,0xbfa00u,0xefe20u,0xa30u,0x60240u,0xbfa10u,0xefe30u,0xa00u,0x60270u,0xbfa20u,0xefe00u,0xa10u,0x60260u,0xbfa30u,0xefe10u,0x2fef0u,0x60290u,0xbfac0u,0xefef0u,0x2fee0u,0x60280u,0xbfad0u,0xefee0u,0x2fed0u,0x602b0u,0xbfae0u,0xefed0u,0x2fec0u,0x602a0u,0xbfaf0u,0xefec0u,0x2feb0u,0x602d0u,0xbfa80u,0xefeb0u,0x2fea0u,0x602c0u,0xbfa90u,0xefea0u,0x2fe90u,0x602f0u,0xbfaa0u,0xefe90u,0x2fe80u,0x602e0u,0xbfab0u,0xefe80u,0x2ff70u,0x60320u,0xbfb40u,0xeff70u,0x2ff60u,0x60330u,0xbfb50u,0xeff60u,0x2ff50u,0x60300u,0xbfb60u,0xeff50u,0x2ff40u,0x60310u,0xbfb70u,0xeff40u,0x2ff30u,0x60360u,0xbfb00u,0xeff30u,0x2ff20u,0x60370u,0xbfb10u,0xeff20u,0x2ff10u,0x60340u,0xbfb20u,0xeff10u,0x2ff00u,0x60350u,0xbfb30u,0xeff00u,0x2fff0u,0x603a0u,0xbfbd0u,0xefff0u,0x2ffe0u,0x603b0u,0xbfbc0u,0xeffe0u,0x2ffd0u,0x60380u,0xbfbf0u,0xeffd0u,0x2ffc0u,0x60390u,0xbfbe0u,0xeffc0u,0x2ffb0u,0x603e0u,0xbfb90u,0xeffb0u,0x2ffa0u,0x603f0u,0xbfb80u,0xeffa0u,0x2ff90u,0x603c0u,0xbfbb0u,0xeff90u,0x2ff80u,0x603d0u,0xbfba0u,0xeff80u,0x0u,0x80000000u,0x0u,0x18830u,0x0u,0x80000000u,0x0u,0x18830u,0x0u,0x80000000u,0x0u,0x18830u,0x0u,0x80000000u,0x0u,0x18830u,0x0u,0x80000000u,0x0u,0x18830u,0x0u,0x80000000u,0x0u,0x18830u,0x0u,0x80000000u,0x0u,0x18830u,0x0u,0x80000000u,0x0u,0x18830u,0x0u,0x80000000u,0x0u,0x18830u,0x0u,0x80000000u,0x0u,0x18830u,0x0u,0x80000000u,0x0u,0x18830u,0x0u,0x80000000u,0x0u,0x18830u,0x0u,0x80000000u,0x0u,0x18830u,0x0u,0x80000000u,0x0u,0x18830u,0x0u,0x80000000u,0x0u,0x18830u,0x0u,0x80000000u,0x0u,0x18830u,0x0u,0x80000000u,0x0u,0x188b2u,0x0u,0x80000000u,0x0u,0x188b2u,0x0u,0x80000000u,0x0u,0x188b2u,0x0u,0x80000000u,0x0u,0x188b2u,0x0u,0x80000000u,0x0u,0x188b2u,0x0u,0x80000000u,0x0u,0x188b2u,0x0u,0x80000000u,0x0u,0x188b2u,0x0u,0x80000000u,0x0u,0x188b2u,0x0u,0x80000000u,0x0u,0x18892u,0x0u,0x80000000u,0x0u,0x18892u,0x0u,0x80000000u,0x0u,0x18892u,0x0u,0x80000000u,0x0u,0x18892u,0x0u,0x80000000u,0x0u,0x18892u,0x0u,0x80000000u,0x0u,0x18892u,0x0u,0x80000000u,0x0u,0x18892u,0x0u,0x80000000u,0x0u,0x18892u,0x0u,0x80000000u,0x0u,0x1489au,0x0u,0x80000000u,0x0u,0x1489au,0x0u,0x80000000u,0x0u,0x1489au,0x0u,0x80000000u,0x0u,0x1489au,0x0u,0x80000000u,0x0u,0x1489au,0x0u,0x80000000u,0x0u,0x1489au,0x0u,0x80000000u,0x0u,0x1489au,0x0u,0x80000000u,0x0u,0x1489au,0x0u,0x80000000u,0x0u,0x34018u,0x0u,0x80000000u,0x0u,0x34018u,0x0u,0x80000000u,0x0u,0x34018u,0x0u,0x80000000u,0x0u,0x34018u,0x0u,0x80000000u,0x0u,0x34018u,0x0u,0x80000000u,0x0u,0x34018u,0x0u,0x80000000u,0x0u,0x34018u,0x0u,0x80000000u,0x0u,0x34018u,0x0u,0x80000000u,0x0u,0x36018u,0x0u,0x80000000u,0x0u,0x36018u,0x0u,0x80000000u,0x0u,0x36018u,0x0u,0x80000000u,0x0u,0x36018u,0x0u,0x80000000u,0x0u,0x36018u,0x0u,0x80000000u,0x0u,0x36018u,0x0u,0x80000000u,0x0u,0x36018u,0x0u,0x80000000u,0x0u,0x36018u,0x0u,0x80000000u,0x0u,0x36030u,0x0u,0x80000000u,0x0u,0x36030u,0x0u,0x80000000u,0x0u,0x36030u,0x0u,0x80000000u,0x0u,0x36030u,0x0u,0x80000000u,0x0u,0x36030u,0x0u,0x80000000u,0x0u,0x36030u,0x0u,0x80000000u,0x0u,0x36030u,0x0u,0x80000000u,0x0u,0x36030u,0x6840u,0x6860u,0x6820u,0x6820u,0x6848u,0x6868u,0x6828u,0x6830u,0x6850u,0x6870u,0x6830u,0x6840u,0x6858u,0x6878u,0x6838u,0x6850u,0x68c0u,0x68e0u,0x68a0u,0x68a0u,0x68c8u,0x68e8u,0x68a8u,0x68b0u,0x68d0u,0x68f0u,0x68b0u,0x68c0u,0x68d8u,0x68f8u,0x68b8u,0x68d0u,0x6960u,0x6940u,0x6900u,0x6900u,0x6968u,0x6948u,0x6908u,0x6910u,0x6970u,0x6950u,0x6910u,0x6960u,0x6978u,0x6958u,0x6918u,0x6970u,0x69e0u,0x69c0u,0x6980u,0x6980u,0x69e8u,0x69c8u,0x6988u,0x6990u,0x69f0u,0x69d0u,0x6990u,0x69e0u,0x69f8u,0x69d8u,0x6998u,0x69f0u,0x6c00u,0x6c20u,0x6c60u,0x6a20u,0x6c08u,0x6c28u,0x6c68u,0x6a30u,0x6c10u,0x6c30u,0x6c70u,0x6a40u,0x6c18u,0x6c38u,0x6c78u,0x6a50u,0x6c80u,0x6ca0u,0x6ce0u,0x6aa0u,0x6c88u,0x6ca8u,0x6ce8u,0x6ab0u,0x6c90u,0x6cb0u,0x6cf0u,0x6ac0u,0x6c98u,0x6cb8u,0x6cf8u,0x6ad0u,0x6d20u,0x6d00u,0x6d40u,0x6b00u,0x6d28u,0x6d08u,0x6d48u,0x6b10u,0x6d30u,0x6d10u,0x6d50u,0x6b60u,0x6d38u,0x6d18u,0x6d58u,0x6b70u,0x6da0u,0x6d80u,0x6dc0u,0x6b80u,0x6da8u,0x6d88u,0x6dc8u,0x6b90u,0x6db0u,0x6d90u,0x6dd0u,0x6be0u,0x6db8u,0x6d98u,0x6dd8u,0x6bf0u,0x7040u,0x7060u,0x7020u,0x6c60u,0x7048u,0x7068u,0x7028u,0x6c70u,0x7050u,0x7070u,0x7030u,0x6c00u,0x7058u,0x7078u,0x7038u,0x6c10u,0x70c0u,0x70e0u,0x70a0u,0x6ce0u,0x70c8u,0x70e8u,0x70a8u,0x6cf0u,0x70d0u,0x70f0u,0x70b0u,0x6c80u,0x70d8u,0x70f8u,0x70b8u,0x6c90u,0x7160u,0x7140u,0x7100u,0x6d40u,0x7168u,0x7148u,0x7108u,0x6d50u,0x7170u,0x7150u,0x7110u,0x6d20u,0x7178u,0x7158u,0x7118u,0x6d30u,0x71e0u,0x71c0u,0x7180u,0x6dc0u,0x71e8u,0x71c8u,0x7188u,0x6dd0u,0x71f0u,0x71d0u,0x7190u,0x6da0u,0x71f8u,0x71d8u,0x7198u,0x6db0u,0x7400u,0x7420u,0x7460u,0x6e60u,0x7408u,0x7428u,0x7468u,0x6e70u,0x7410u,0x7430u,0x7470u,0x6e00u,0x7418u,0x7438u,0x7478u,0x6e10u,0x7480u,0x74a0u,0x74e0u,0x6ee0u,0x7488u,0x74a8u,0x74e8u,0x6ef0u,0x7490u,0x74b0u,0x74f0u,0x6e80u,0x7498u,0x74b8u,0x74f8u,0x6e90u,0x7520u,0x7500u,0x7540u,0x6f40u,0x7528u,0x7508u,0x7548u,0x6f50u,0x7530u,0x7510u,0x7550u,0x6f20u,0x7538u,0x7518u,0x7558u,0x6f30u,0x75a0u,0x7580u,0x75c0u,0x6fc0u,0x75a8u,0x7588u,0x75c8u,0x6fd0u,0x75b0u,0x7590u,0x75d0u,0x6fa0u,0x75b8u,0x7598u,0x75d8u,0x6fb0u,0x6860u,0x1ea40u,0x1ec20u,0x2440u,0x6868u,0x1eed0u,0x1f0b0u,0x2450u,0x6870u,0x1f360u,0x1f540u,0x2460u,0x6878u,0x1f7f0u,0x1f9d0u,0x2470u,0x68e0u,0x1fc80u,0x1fe60u,0x2400u,0x68e8u,0x20110u,0x202f0u,0x2410u,0x68f0u,0x205a0u,0x20780u,0x2420u,0x68f8u,0x20a30u,0x20c10u,0x2430u,0x6940u,0x20ec0u,0x210a0u,0x25c0u,0x6948u,0x21350u,0x21530u,0x25d0u,0x6950u,0x217e0u,0x219c0u,0x25e0u,0x6958u,0x21c70u,0x21e50u,0x25f0u,0x69c0u,0x22100u,0x222e0u,0x2580u,0x69c8u,0x22590u,0x22770u,0x2590u,0x69d0u,0x22a20u,0x22c00u,0x25a0u,0x69d8u,0x22eb0u,0x23090u,0x25b0u,0x6a60u,0x1ea70u,0x1ec28u,0x2760u,0x6a68u,0x1ef00u,0x1f0b8u,0x2770u,0x6a70u,0x1f390u,0x1f548u,0x2740u,0x6a78u,0x1f820u,0x1f9d8u,0x2750u,0x6ae0u,0x1fcb0u,0x1fe68u,0x2720u,0x6ae8u,0x20140u,0x202f8u,0x2730u,0x6af0u,0x205d0u,0x20788u,0x2700u,0x6af8u,0x20a60u,0x20c18u,0x2710u,0x6b40u,0x20ef0u,0x210a8u,0x28e0u,0x6b48u,0x21380u,0x21538u,0x28f0u,0x6b50u,0x21810u,0x219c8u,0x28c0u,0x6b58u,0x21ca0u,0x21e58u,0x28d0u,0x6bc0u,0x22130u,0x222e8u,0x28a0u,0x6bc8u,0x225c0u,0x22778u,0x28b0u,0x6bd0u,0x22a50u,0x22c08u,0x2880u,0x6bd8u,0x22ee0u,0x23098u,0x2890u,0x6c20u,0x1eaa0u,0x1ec30u,0x2a40u,0x6c28u,0x1ef30u,0x1f0c0u,0x2a50u,0x6c30u,0x1f3c0u,0x1f550u,0x2a60u,0x6c38u,0x1f850u,0x1f9e0u,0x2a70u,0x6ca0u,0x1fce0u,0x1fe70u,0x2a00u,0x6ca8u,0x20170u,0x20300u,0x2a10u,0x6cb0u,0x20600u,0x20790u,0x2a20u,0x6cb8u,0x20a90u,0x20c20u,0x2a30u,0x6d00u,0x20f20u,0x210b0u,0x2bc0u,0x6d08u,0x213b0u,0x21540u,0x2bd0u,0x6d10u,0x21840u,0x219d0u,0x2be0u,0x6d18u,0x21cd0u,0x21e60u,0x2bf0u,0x6d80u,0x22160u,0x222f0u,0x2b80u,0x6d88u,0x225f0u,0x22780u,0x2b90u,0x6d90u,0x22a80u,0x22c10u,0x2ba0u,0x6d98u,0x22f10u,0x230a0u,0x2bb0u,0x6e20u,0x1ead0u,0x1ec38u,0x2d60u,0x6e28u,0x1ef60u,0x1f0c8u,0x2d70u,0x6e30u,0x1f3f0u,0x1f558u,0x2d40u,0x6e38u,0x1f880u,0x1f9e8u,0x2d50u,0x6ea0u,0x1fd10u,0x1fe78u,0x2d20u,0x6ea8u,0x201a0u,0x20308u,0x2d30u,0x6eb0u,0x20630u,0x20798u,0x2d00u,0x6eb8u,0x20ac0u,0x20c28u,0x2d10u,0x6f00u,0x20f50u,0x210b8u,0x2ee0u,0x6f08u,0x213e0u,0x21548u,0x2ef0u,0x6f10u,0x21870u,0x219d8u,0x2ec0u,0x6f18u,0x21d00u,0x21e68u,0x2ed0u,0x6f80u,0x22190u,0x222f8u,0x2ea0u,0x6f88u,0x22620u,0x22788u,0x2eb0u,0x6f90u,0x22ab0u,0x22c18u,0x2e80u,0x6f98u,0x22f40u,0x230a8u,0x2e90u,0x0u,0x40u,0x9000u,0xcc00u,0x80u,0xc0u,0x9010u,0xcc10u,0x110u,0x150u,0x9020u,0xcc20u,0x190u,0x1d0u,0x9030u,0xcc30u,0x220u,0x260u,0x9040u,0xcc40u,0x2a0u,0x2e0u,0x9050u,0xcc50u,0x330u,0x370u,0x9060u,0xcc60u,0x3b0u,0x3f0u,0x9070u,0xcc70u,0x440u,0x400u,0x9080u,0xcc80u,0x4c0u,0x480u,0x9090u,0xcc90u,0x550u,0x510u,0x90a0u,0xcca0u,0x5d0u,0x590u,0x90b0u,0xccb0u,0x660u,0x620u,0x90c0u,0xccc0u,0x6e0u,0x6a0u,0x90d0u,0xccd0u,0x770u,0x730u,0x90e0u,0xcce0u,0x7f0u,0x7b0u,0x90f0u,0xccf0u,0x10u,0x50u,0x9300u,0xcf00u,0x90u,0xd0u,0x9310u,0xcf10u,0x100u,0x140u,0x9320u,0xcf20u,0x180u,0x1c0u,0x9330u,0xcf30u,0x230u,0x270u,0x9340u,0xcf40u,0x2b0u,0x2f0u,0x9350u,0xcf50u,0x320u,0x360u,0x9360u,0xcf60u,0x3a0u,0x3e0u,0x9370u,0xcf70u,0x450u,0x410u,0x9380u,0xcf80u,0x4d0u,0x490u,0x9390u,0xcf90u,0x540u,0x500u,0x93a0u,0xcfa0u,0x5c0u,0x580u,0x93b0u,0xcfb0u,0x670u,0x630u,0x93c0u,0xcfc0u,0x6f0u,0x6b0u,0x93d0u,0xcfd0u,0x760u,0x720u,0x93e0u,0xcfe0u,0x7e0u,0x7a0u,0x93f0u,0xcff0u,0x20u,0x60u,0x9600u,0xd200u,0xa0u,0xe0u,0x9610u,0xd210u,0x130u,0x170u,0x9620u,0xd220u,0x1b0u,0x1f0u,0x9630u,0xd230u,0x200u,0x240u,0x9640u,0xd240u,0x280u,0x2c0u,0x9650u,0xd250u,0x310u,0x350u,0x9660u,0xd260u,0x390u,0x3d0u,0x9670u,0xd270u,0x460u,0x420u,0x9680u,0xd280u,0x4e0u,0x4a0u,0x9690u,0xd290u,0x570u,0x530u,0x96a0u,0xd2a0u,0x5f0u,0x5b0u,0x96b0u,0xd2b0u,0x640u,0x600u,0x96c0u,0xd2c0u,0x6c0u,0x680u,0x96d0u,0xd2d0u,0x750u,0x710u,0x96e0u,0xd2e0u,0x7d0u,0x790u,0x96f0u,0xd2f0u,0x30u,0x70u,0x9900u,0xd500u,0xb0u,0xf0u,0x9910u,0xd510u,0x120u,0x160u,0x9920u,0xd520u,0x1a0u,0x1e0u,0x9930u,0xd530u,0x210u,0x250u,0x9940u,0xd540u,0x290u,0x2d0u,0x9950u,0xd550u,0x300u,0x340u,0x9960u,0xd560u,0x380u,0x3c0u,0x9970u,0xd570u,0x470u,0x430u,0x9980u,0xd580u,0x4f0u,0x4b0u,0x9990u,0xd590u,0x560u,0x520u,0x99a0u,0xd5a0u,0x5e0u,0x5a0u,0x99b0u,0xd5b0u,0x650u,0x610u,0x99c0u,0xd5c0u,0x6d0u,0x690u,0x99d0u,0xd5d0u,0x740u,0x700u,0x99e0u,0xd5e0u,0x7c0u,0x780u,0x99f0u,0xd5f0u,0xc008u,0x12120u,0x12160u,0x12c00u,0xc018u,0x122b0u,0x122f0u,0x12c10u,0xc028u,0x12400u,0x12440u,0x12c20u,0xc038u,0x12590u,0x125d0u,0x12c30u,0xc048u,0x12760u,0x12720u,0x12c40u,0xc058u,0x128f0u,0x128b0u,0x12c50u,0xc068u,0x12a40u,0x12a00u,0x12c60u,0xc078u,0x12bd0u,0x12b90u,0x12c70u,0xc088u,0x12d20u,0x12d60u,0x12c80u,0xc098u,0x12eb0u,0x12ef0u,0x12c90u,0xc0a8u,0x13000u,0x13040u,0x12ca0u,0xc0b8u,0x13190u,0x131d0u,0x12cb0u,0xc0c8u,0x13360u,0x13320u,0x12cc0u,0xc0d8u,0x134f0u,0x134b0u,0x12cd0u,0xc0e8u,0x13640u,0x13600u,0x12ce0u,0xc0f8u,0x137d0u,0x13790u,0x12cf0u,0xc308u,0x12130u,0x12168u,0x12d00u,0xc318u,0x122a0u,0x122f8u,0x12d10u,0xc328u,0x12410u,0x12448u,0x12d20u,0xc338u,0x12580u,0x125d8u,0x12d30u,0xc348u,0x12770u,0x12728u,0x12d40u,0xc358u,0x128e0u,0x128b8u,0x12d50u,0xc368u,0x12a50u,0x12a08u,0x12d60u,0xc378u,0x12bc0u,0x12b98u,0x12d70u,0xc388u,0x12d30u,0x12d68u,0x12d80u,0xc398u,0x12ea0u,0x12ef8u,0x12d90u,0xc3a8u,0x13010u,0x13048u,0x12da0u,0xc3b8u,0x13180u,0x131d8u,0x12db0u,0xc3c8u,0x13370u,0x13328u,0x12dc0u,0xc3d8u,0x134e0u,0x134b8u,0x12dd0u,0xc3e8u,0x13650u,0x13608u,0x12de0u,0xc3f8u,0x137c0u,0x13798u,0x12df0u,0xc608u,0x12140u,0x12170u,0x12e00u,0xc618u,0x122d0u,0x122e0u,0x12e10u,0xc628u,0x12460u,0x12450u,0x12e20u,0xc638u,0x125f0u,0x125c0u,0x12e30u,0xc648u,0x12700u,0x12730u,0x12e40u,0xc658u,0x12890u,0x128a0u,0x12e50u,0xc668u,0x12a20u,0x12a10u,0x12e60u,0xc678u,0x12bb0u,0x12b80u,0x12e70u,0xc688u,0x12d40u,0x12d70u,0x12e80u,0xc698u,0x12ed0u,0x12ee0u,0x12e90u,0xc6a8u,0x13060u,0x13050u,0x12ea0u,0xc6b8u,0x131f0u,0x131c0u,0x12eb0u,0xc6c8u,0x13300u,0x13330u,0x12ec0u,0xc6d8u,0x13490u,0x134a0u,0x12ed0u,0xc6e8u,0x13620u,0x13610u,0x12ee0u,0xc6f8u,0x137b0u,0x13780u,0x12ef0u,0xc908u,0x12150u,0x12178u,0x12f00u,0xc918u,0x122c0u,0x122e8u,0x12f10u,0xc928u,0x12470u,0x12458u,0x12f20u,0xc938u,0x125e0u,0x125c8u,0x12f30u,0xc948u,0x12710u,0x12738u,0x12f40u,0xc958u,0x12880u,0x128a8u,0x12f50u,0xc968u,0x12a30u,0x12a18u,0x12f60u,0xc978u,0x12ba0u,0x12b88u,0x12f70u,0xc988u,0x12d50u,0x12d78u,0x12f80u,0xc998u,0x12ec0u,0x12ee8u,0x12f90u,0xc9a8u,0x13070u,0x13058u,0x12fa0u,0xc9b8u,0x131e0u,0x131c8u,0x12fb0u,0xc9c8u,0x13310u,0x13338u,0x12fc0u,0xc9d8u,0x13480u,0x134a8u,0x12fd0u,0xc9e8u,0x13630u,0x13618u,0x12fe0u,0xc9f8u,0x137a0u,0x13788u,0x12ff0u,0x8000u,0x1c300u,0x1ea40u,0x1ec20u,0x8080u,0x1c310u,0x100000u,0x1ec22u,0x8110u,0x1c320u,0x100000u,0x1ec24u,0x8190u,0x1c330u,0x100000u,0x1ec26u,0x8220u,0x1c340u,0x1ea40u,0x1ec28u,0x82a0u,0x1c350u,0x100000u,0x1ec2au,0x8330u,0x1c360u,0x100000u,0x1ec2cu,0x83b0u,0x1c370u,0x100000u,0x1ec2eu,0x8440u,0x1c380u,0x1ea40u,0x1ec30u,0x84c0u,0x1c390u,0x100000u,0x1ec32u,0x8550u,0x1c3a0u,0x100000u,0x1ec34u,0x85d0u,0x1c3b0u,0x100000u,0x1ec36u,0x8660u,0x1c3c0u,0x1ea40u,0x1ec38u,0x86e0u,0x1c3d0u,0x100000u,0x1ec3au,0x8770u,0x1c3e0u,0x100000u,0x1ec3cu,0x87f0u,0x1c3f0u,0x100000u,0x1ec3eu,0x8010u,0x1c400u,0x1ea70u,0x1ec20u,0x8090u,0x1c410u,0x100000u,0x1ec22u,0x8100u,0x1c420u,0x100000u,0x1ec24u,0x8180u,0x1c430u,0x100000u,0x1ec26u,0x8230u,0x1c440u,0x1ea70u,0x1ec28u,0x82b0u,0x1c450u,0x100000u,0x1ec2au,0x8320u,0x1c460u,0x100000u,0x1ec2cu,0x83a0u,0x1c470u,0x100000u,0x1ec2eu,0x8450u,0x1c480u,0x1ea70u,0x1ec30u,0x84d0u,0x1c490u,0x100000u,0x1ec32u,0x8540u,0x1c4a0u,0x100000u,0x1ec34u,0x85c0u,0x1c4b0u,0x100000u,0x1ec36u,0x8670u,0x1c4c0u,0x1ea70u,0x1ec38u,0x86f0u,0x1c4d0u,0x100000u,0x1ec3au,0x8760u,0x1c4e0u,0x100000u,0x1ec3cu,0x87e0u,0x1c4f0u,0x100000u,0x1ec3eu,0x8020u,0x1c500u,0x1eaa0u,0x1ec20u,0x80a0u,0x1c510u,0x100000u,0x1ec22u,0x8130u,0x1c520u,0x100000u,0x1ec24u,0x81b0u,0x1c530u,0x100000u,0x1ec26u,0x8200u,0x1c540u,0x1eaa0u,0x1ec28u,0x8280u,0x1c550u,0x100000u,0x1ec2au,0x8310u,0x1c560u,0x100000u,0x1ec2cu,0x8390u,0x1c570u,0x100000u,0x1ec2eu,0x8460u,0x1c580u,0x1eaa0u,0x1ec30u,0x84e0u,0x1c590u,0x100000u,0x1ec32u,0x8570u,0x1c5a0u,0x100000u,0x1ec34u,0x85f0u,0x1c5b0u,0x100000u,0x1ec36u,0x8640u,0x1c5c0u,0x1eaa0u,0x1ec38u,0x86c0u,0x1c5d0u,0x100000u,0x1ec3au,0x8750u,0x1c5e0u,0x100000u,0x1ec3cu,0x87d0u,0x1c5f0u,0x100000u,0x1ec3eu,0x8030u,0x1c600u,0x1ead0u,0x1ec20u,0x80b0u,0x1c610u,0x100000u,0x1ec22u,0x8120u,0x1c620u,0x100000u,0x1ec24u,0x81a0u,0x1c630u,0x100000u,0x1ec26u,0x8210u,0x1c640u,0x1ead0u,0x1ec28u,0x8290u,0x1c650u,0x100000u,0x1ec2au,0x8300u,0x1c660u,0x100000u,0x1ec2cu,0x8380u,0x1c670u,0x100000u,0x1ec2eu,0x8470u,0x1c680u,0x1ead0u,0x1ec30u,0x84f0u,0x1c690u,0x100000u,0x1ec32u,0x8560u,0x1c6a0u,0x100000u,0x1ec34u,0x85e0u,0x1c6b0u,0x100000u,0x1ec36u,0x8650u,0x1c6c0u,0x1ead0u,0x1ec38u,0x86d0u,0x1c6d0u,0x100000u,0x1ec3au,0x8740u,0x1c6e0u,0x100000u,0x1ec3cu,0x87c0u,0x1c6f0u,0x100000u,0x1ec3eu,0x27000u,0x1d480u,0x1d480u,0x80000000u,0x27002u,0x1d482u,0x100000u,0x80000000u,0x27004u,0x1d484u,0x100000u,0x80000000u,0x27006u,0x1d486u,0x100000u,0x80000000u,0x27200u,0x1d490u,0x1d480u,0x80000000u,0x27202u,0x1d492u,0x100000u,0x80000000u,0x27204u,0x1d494u,0x100000u,0x80000000u,0x27206u,0x1d496u,0x100000u,0x80000000u,0x27400u,0x1d4a0u,0x1d480u,0x80000000u,0x27402u,0x1d4a2u,0x100000u,0x80000000u,0x27404u,0x1d4a4u,0x100000u,0x80000000u,0x27406u,0x1d4a6u,0x100000u,0x80000000u,0x27600u,0x1d4b0u,0x1d480u,0x80000000u,0x27602u,0x1d4b2u,0x100000u,0x80000000u,0x27604u,0x1d4b4u,0x100000u,0x80000000u,0x27606u,0x1d4b6u,0x100000u,0x80000000u,0x27008u,0x1d488u,0x1d490u,0x80000000u,0x2700au,0x1d48au,0x100000u,0x80000000u,0x2700cu,0x1d48cu,0x100000u,0x80000000u,0x2700eu,0x1d48eu,0x100000u,0x80000000u,0x27208u,0x1d498u,0x1d490u,0x80000000u,0x2720au,0x1d49au,0x100000u,0x80000000u,0x2720cu,0x1d49cu,0x100000u,0x80000000u,0x2720eu,0x1d49eu,0x100000u,0x80000000u,0x27408u,0x1d4a8u,0x1d490u,0x80000000u,0x2740au,0x1d4aau,0x100000u,0x80000000u,0x2740cu,0x1d4acu,0x100000u,0x80000000u,0x2740eu,0x1d4aeu,0x100000u,0x80000000u,0x27608u,0x1d4b8u,0x1d490u,0x80000000u,0x2760au,0x1d4bau,0x100000u,0x80000000u,0x2760cu,0x1d4bcu,0x100000u,0x80000000u,0x2760eu,0x1d4beu,0x100000u,0x80000000u,0x26808u,0x1d4c0u,0x1d4a0u,0x80000000u,0x2680au,0x1d4c2u,0x100000u,0x80000000u,0x2680cu,0x1d4c4u,0x100000u,0x80000000u,0x2680eu,0x1d4c6u,0x100000u,0x80000000u,0x26a08u,0x1d4d0u,0x1d4a0u,0x80000000u,0x26a0au,0x1d4d2u,0x100000u,0x80000000u,0x26a0cu,0x1d4d4u,0x100000u,0x80000000u,0x26a0eu,0x1d4d6u,0x100000u,0x80000000u,0x26c08u,0x1d4e0u,0x1d4a0u,0x80000000u,0x26c0au,0x1d4e2u,0x100000u,0x80000000u,0x26c0cu,0x1d4e4u,0x100000u,0x80000000u,0x26c0eu,0x1d4e6u,0x100000u,0x80000000u,0x26e08u,0x1d4f0u,0x1d4a0u,0x80000000u,0x26e0au,0x1d4f2u,0x100000u,0x80000000u,0x26e0cu,0x1d4f4u,0x100000u,0x80000000u,0x26e0eu,0x1d4f6u,0x100000u,0x80000000u,0x100000u,0x1d4c8u,0x1d4b0u,0x80000000u,0x100000u,0x1d4cau,0x100000u,0x80000000u,0x100000u,0x1d4ccu,0x100000u,0x80000000u,0x100000u,0x1d4ceu,0x100000u,0x80000000u,0x100000u,0x1d4d8u,0x1d4b0u,0x80000000u,0x100000u,0x1d4dau,0x100000u,0x80000000u,0x100000u,0x1d4dcu,0x100000u,0x80000000u,0x100000u,0x1d4deu,0x100000u,0x80000000u,0x100000u,0x1d4e8u,0x1d4b0u,0x80000000u,0x100000u,0x1d4eau,0x100000u,0x80000000u,0x100000u,0x1d4ecu,0x100000u,0x80000000u,0x100000u,0x1d4eeu,0x100000u,0x80000000u,0x100000u,0x1d4f8u,0x1d4b0u,0x80000000u,0x100000u,0x1d4fau,0x100000u,0x80000000u,0x100000u,0x1d4fcu,0x100000u,0x80000000u,0x100000u,0x1d4feu,0x100000u,0x80000000u,0x30000u,0x60420u,0xbfc50u,0xf0000u,0x30010u,0x60430u,0xbfc40u,0xf0010u,0x30020u,0x60400u,0xbfc70u,0xf0020u,0x30030u,0x60410u,0xbfc60u,0xf0030u,0x30040u,0x60460u,0xbfc10u,0xf0040u,0x30050u,0x60470u,0xbfc00u,0xf0050u,0x30060u,0x60440u,0xbfc30u,0xf0060u,0x30070u,0x60450u,0xbfc20u,0xf0070u,0x30080u,0x604b0u,0xbfcd0u,0xf0080u,0x30090u,0x604a0u,0xbfcc0u,0xf0090u,0x300a0u,0x60490u,0xbfcf0u,0xf00a0u,0x300b0u,0x60480u,0xbfce0u,0xf00b0u,0x300c0u,0x604f0u,0xbfc90u,0xf00c0u,0x300d0u,0x604e0u,0xbfc80u,0xf00d0u,0x300e0u,0x604d0u,0xbfcb0u,0xf00e0u,0x300f0u,0x604c0u,0xbfca0u,0xf00f0u,0x30100u,0x60530u,0xbfd60u,0xf0100u,0x30110u,0x60520u,0xbfd70u,0xf0110u,0x30120u,0x60510u,0xbfd40u,0xf0120u,0x30130u,0x60500u,0xbfd50u,0xf0130u,0x30140u,0x60570u,0xbfd20u,0xf0140u,0x30150u,0x60560u,0xbfd30u,0xf0150u,0x30160u,0x60550u,0xbfd00u,0xf0160u,0x30170u,0x60540u,0xbfd10u,0xf0170u,0x30190u,0x605b0u,0xbfde0u,0xf0190u,0x30180u,0x605a0u,0xbfdf0u,0xf0180u,0x301b0u,0x60590u,0xbfdc0u,0xf01b0u,0x301a0u,0x60580u,0xbfdd0u,0xf01a0u,0x301d0u,0x605f0u,0xbfda0u,0xf01d0u,0x301c0u,0x605e0u,0xbfdb0u,0xf01c0u,0x301f0u,0x605d0u,0xbfd80u,0xf01f0u,0x301e0u,0x605c0u,0xbfd90u,0xf01e0u,0x30210u,0x60640u,0xbfe60u,0xf0210u,0x30200u,0x60650u,0xbfe70u,0xf0200u,0x30230u,0x60660u,0xbfe40u,0xf0230u,0x30220u,0x60670u,0xbfe50u,0xf0220u,0x30250u,0x60600u,0xbfe20u,0xf0250u,0x30240u,0x60610u,0xbfe30u,0xf0240u,0x30270u,0x60620u,0xbfe00u,0xf0270u,0x30260u,0x60630u,0xbfe10u,0xf0260u,0x30290u,0x606c0u,0xbfef0u,0xf0290u,0x30280u,0x606d0u,0xbfee0u,0xf0280u,0x302b0u,0x606e0u,0xbfed0u,0xf02b0u,0x302a0u,0x606f0u,0xbfec0u,0xf02a0u,0x302d0u,0x60680u,0xbfeb0u,0xf02d0u,0x302c0u,0x60690u,0xbfea0u,0xf02c0u,0x302f0u,0x606a0u,0xbfe90u,0xf02f0u,0x302e0u,0x606b0u,0xbfe80u,0xf02e0u,0x30320u,0x60740u,0xbff70u,0x11f720u,0x30330u,0x60750u,0xbff60u,0x11f730u,0x30300u,0x60760u,0xbff50u,0x11f700u,0x30310u,0x60770u,0xbff40u,0x11f710u,0x30360u,0x60700u,0xbff30u,0x11f760u,0x30370u,0x60710u,0xbff20u,0x11f770u,0x30340u,0x60720u,0xbff10u,0x11f740u,0x30350u,0x60730u,0xbff00u,0x11f750u,0x303a0u,0x8fbd0u,0xbfff0u,0x11f7a0u,0x303b0u,0x8fbc0u,0xbffe0u,0x11f7b0u,0x30380u,0x8fbf0u,0xbffd0u,0x11f780u,0x30390u,0x8fbe0u,0xbffc0u,0x11f790u,0x303e0u,0x8fb90u,0xbffb0u,0x11f7e0u,0x303f0u,0x8fb80u,0xbffa0u,0x11f7f0u,0x303c0u,0x8fbb0u,0xbff90u,0x11f7c0u,0x303d0u,0x8fba0u,0xbff80u,0x11f7d0u,0x0u,0x80000000u,0x0u,0x36030u,0x0u,0x80000000u,0x0u,0x36030u,0x0u,0x80000000u,0x0u,0x36030u,0x0u,0x80000000u,0x0u,0x36030u,0x0u,0x80000000u,0x0u,0x36030u,0x0u,0x80000000u,0x0u,0x36030u,0x0u,0x80000000u,0x0u,0x36030u,0x0u,0x80000000u,0x0u,0x36030u,0x0u,0x80000000u,0x0u,0x360b2u,0x0u,0x80000000u,0x0u,0x360b2u,0x0u,0x80000000u,0x0u,0x360b2u,0x0u,0x80000000u,0x0u,0x360b2u,0x0u,0x80000000u,0x0u,0x360b2u,0x0u,0x80000000u,0x0u,0x360b2u,0x0u,0x80000000u,0x0u,0x360b2u,0x0u,0x80000000u,0x0u,0x360b2u,0x0u,0x80000000u,0x0u,0x36092u,0x0u,0x80000000u,0x0u,0x36092u,0x0u,0x80000000u,0x0u,0x36092u,0x0u,0x80000000u,0x0u,0x36092u,0x0u,0x80000000u,0x0u,0x36092u,0x0u,0x80000000u,0x0u,0x36092u,0x0u,0x80000000u,0x0u,0x36092u,0x0u,0x80000000u,0x0u,0x36092u,0x0u,0x80000000u,0x0u,0x3609au,0x0u,0x80000000u,0x0u,0x3609au,0x0u,0x80000000u,0x0u,0x3609au,0x0u,0x80000000u,0x0u,0x3609au,0x0u,0x80000000u,0x0u,0x3609au,0x0u,0x80000000u,0x0u,0x3609au,0x0u,0x80000000u,0x0u,0x3609au,0x0u,0x80000000u,0x0u,0x3609au,0x0u,0x80000000u,0x0u,0x36059u,0x0u,0x80000000u,0x0u,0x36059u,0x0u,0x80000000u,0x0u,0x36059u,0x0u,0x80000000u,0x0u,0x36059u,0x0u,0x80000000u,0x0u,0x36059u,0x0u,0x80000000u,0x0u,0x36059u,0x0u,0x80000000u,0x0u,0x36059u,0x0u,0x80000000u,0x0u,0x36059u,0x0u,0x80000000u,0x0u,0x36059u,0x0u,0x80000000u,0x0u,0x36059u,0x0u,0x80000000u,0x0u,0x36059u,0x0u,0x80000000u,0x0u,0x36059u,0x0u,0x80000000u,0x0u,0x36059u,0x0u,0x80000000u,0x0u,0x36059u,0x0u,0x80000000u,0x0u,0x36059u,0x0u,0x80000000u,0x0u,0x36059u,0x0u,0x80000000u,0x0u,0x16071u,0x0u,0x80000000u,0x0u,0x16071u,0x0u,0x80000000u,0x0u,0x16071u,0x0u,0x80000000u,0x0u,0x16071u,0x0u,0x80000000u,0x0u,0x16071u,0x0u,0x80000000u,0x0u,0x16071u,0x0u,0x80000000u,0x0u,0x16071u,0x0u,0x80000000u,0x0u,0x16071u,0x0u,0x80000000u,0x0u,0x14071u,0x0u,0x80000000u,0x0u,0x14071u,0x0u,0x80000000u,0x0u,0x14071u,0x0u,0x80000000u,0x0u,0x14071u,0x0u,0x80000000u,0x0u,0x14071u,0x0u,0x80000000u,0x0u,0x14071u,0x0u,0x80000000u,0x0u,0x14071u,0x0u,0x80000000u,0x0u,0x14071u};

__global__ __launch_bounds__(256, 1) void swin_main(const uint16_t* x, const uint16_t* mask, char* ws, uint16_t* out,
                                                    const uint16_t* wqkv, const uint16_t* wo, const uint16_t* ls,
                                                    const uint16_t* w1, const uint16_t* w2, const uint16_t* rc,
                                                    const int* ridx) {
  __shared__ __attribute__((aligned(16))) char smem[LDS_BYTES];
  const int wave = __builtin_amdgcn_readfirstlane(threadIdx.x >> 6);
  const unsigned lds0 = (unsigned)(size_t)smem;
  asm volatile(
      ".p2align 5\n"
      "s_mov_b64 s[92:93], %0\n"
      "s_mov_b32 s40, %1\n"
      "s_mov_b32 s41, %2\n"
      "s_mov_b64 s[88:89], %3\n"
      "s_load_dwordx2 s[16:17], s[92:93], 0\n"
      "s_load_dwordx2 s[20:21], s[92:93], 32\n"
      "s_load_dwordx2 s[46:47], s[92:93], 40\n"
      "s_load_dwordx8 s[80:87], s[92:93], 48\n"
      "s_load_dwordx2 s[76:77], s[92:93], 80\n"
      "s_mov_b64 s[90:91], s[92:93]\n"
      "s_getpc_b64 s[98:99]\n"
      "L_ipf0_%=:\n"
      "v_mbcnt_lo_u32_b32 v238, -1, 0\n"
      "v_mbcnt_hi_u32_b32 v238, -1, v238\n"
      "v_lshlrev_b32 v237, 2, v238\n"
      "v_lshlrev_b32 v158, 4, v238\n"
      "s_cmp_lt_u32 s41, 209\n"
      "s_cselect_b32 s44, 20, 19\n"
      "s_lshl_b32 s92, s40, 8\n"
      "v_add_u32_e64 v223, s92, v237\n"
      "v_min_u32 v29, 704, v223\n"
      "v_add_u32 v29, 4096, v29\n"
      "v_min_u32 v223, 672, v223\n"
      "v_add_u32 v68, 122112, v223\n"
      "s_lshr_b32 s92, s41, 3\n"
      "s_lshl_b32 s92, s92, 7\n"
      "v_lshlrev_b32 v8, 7, v238\n"
      "s_mul_i32 s93, s92, 18\n"
      "s_mul_i32 s94, s40, 640\n"
      "v_min_u32 v236, 512, v8\n"
      "v_add_u32 v236, s94, v236\n"
      "v_min_u32 v236, 2176, v236\n"
      "v_add_u32 v236, s93, v236\n"
      "s_cmp_eq_u32 s40, 1\n"
      "s_cselect_b32 s93, 4096, 0\n"
      "s_add_u32 s92, s92, s93\n"
      "s_cmp_eq_u32 s40, 2\n"
      "s_cselect_b32 s93, 9, 8\n"
      "s_cselect_b64 s[72:73], s[98:99], s[88:89]\n"
      "s_cmp_lt_u32 s40, 2\n"
      "s_cselect_b32 s93, 27, s93\n"
      "s_mul_i32 s92, s92, s93\n"
      "s_sub_u32 s93, s93, 1\n"
      "s_lshl_b32 s93, s93, 7\n"
      "v_min_u32 v8, s93, v8\n"
      "v_add_u32 v8, s92, v8\n"
      "s_lshl_b32 s95, s40, 3\n"
      "v_lshrrev_b32 v206, 3, v238\n"
      "v_and_b32 v239, 7, v238\n"
      "v_add_u32 v209, s95, v206\n"
      "v_bfe_u32 v208, v209, 1, 3\n"
      "v_cmp_lt_u32 s[70:71], s44, v209\n"
      "v_min_u32_e64 v209, s44, v209\n"
      "s_mov_b32 s18, 100270464\n"
      "s_mov_b32 s19, 0x20000\n"
      "s_mov_b32 s78, 9604\n"
      "s_mov_b32 s79, 0x20000\n"
      "s_mov_b32 s22, 221184\n"
      "s_mov_b32 s23, 0x20000\n"
      "s_lshl_b32 s64, s40, 10\n"
      "v_add_u32 v36, s64, v158\n"
      "s_add_u32 s75, s64, 18432\n"
      "s_and_b32 s92, s40, 1\n"
      "s_lshl_b32 s92, s92, 2\n"
      "s_mul_i32 s93, s40, 9216\n"
      "v_and_b32 v207, 2, v206\n"
      "v_xor_b32 v207, v207, v239\n"
      "v_xor_b32_e64 v207, s92, v207\n"
      "v_lshl_add_u32 v207, v207, 4, s93\n"
      "v_mul_u32_u24 v99, 1152, v206\n"
      "v_add_u32 v99, v99, v207\n"
      "s_cmp_lt_u32 s40, 2\n"
      "s_cselect_b32 s65, s64, 163840\n"
      "s_waitcnt lgkmcnt(0)\n"
      "s_cselect_b64 s[72:73], s[20:21], s[72:73]\n"
      "global_load_dword v223, v223, s[86:87]\n"
      "global_load_dword a255, v8, s[72:73]\n"
      "global_load_dword a253, v236, s[46:47]\n"
      "s_mul_i32 s92, s40, 8192\n"
      "s_add_u32 s88, s88, s92\n"
      "s_addc_u32 s89, s89, 0\n"
      "s_add_u32 m0, s64, 124928\n"
      "s_movk_i32 s92, 0\n"
      "buffer_load_dwordx4 v36, s[76:79], s92 offen  lds\n"
      "s_add_u32 m0, s64, 129024\n"
      "s_movk_i32 s92, 4096\n"
      "buffer_load_dwordx4 v36, s[76:79], s92 offen  lds\n"
      "s_add_u32 m0, s64, 133120\n"
      "s_cmp_lt_u32 s40, 2\n"
      "s_cselect_b32 m0, m0, s65\n"
      "s_movk_i32 s92, 8192\n"
      "buffer_load_dwordx4 v36, s[76:79], s92 offen  lds\n"
      "s_load_dwordx16 s[0:15], s[82:83], 0\n"
      "s_load_dwordx16 s[48:63], s[84:85], 0\n"
      "s_load_dwordx16 s[24:39], s[84:85], 64\n"
      "s_load_dwordx2 s[66:67], s[80:81], 0\n"
      "s_movk_i32 s93, 4180\n"
      "s_cmp_lt_u32 s41, 209\n"
      "s_cselect_b32 s92, 209, 47\n"
      "s_cselect_b32 s93, 795, s93\n"
      "s_cmp_lt_u32 s41, 252\n"
      "s_cselect_b32 s94, 16, 15\n"
      "s_add_u32 s93, s93, s41\n"
      "v_lshl_add_u32 v236, v209, 8, s41\n"
      "v_mul_u32_u24 v8, s92, v209\n"
      "v_add_u32 v8, s93, v8\n"
      "v_cmp_lt_u32 vcc, s94, v209\n"
      "v_subrev_u32 v207, 5184, v8\n"
      "s_nop 0\n"
      "v_cndmask_b32 v236, v236, v8, vcc\n"
      "v_cmp_le_u32 vcc, 5184, v236\n"
      "v_mul_u32_u24 v234, 58255, v236\n"
      "v_lshrrev_b32 v234, 22, v234\n"
      "v_mul_u32_u24 v69, 72, v234\n"
      "v_sub_u32 v69, v236, v69\n"
      "v_min_u32 v8, 72, v207\n"
      "v_cndmask_b32 v234, v234, v8, vcc\n"
      "v_subrev_u32 v8, 72, v207\n"
      "v_min_u32 v8, 72, v8\n"
      "v_cndmask_b32 v69, v69, v8, vcc\n"
      "v_mad_u32_u24 v8, v234, 7, 9\n"
      "v_subrev_u32 v236, 0x1ff, v8\n"
      "v_min_u32 v8, v8, v236\n"
      "v_mad_u32_u24 v236, v69, 7, 9\n"
      "v_subrev_u32 v207, 0x1ff, v236\n"
      "v_min_u32 v236, v236, v207\n"
      "v_mul_u32_u24 v8, 0x1ff, v8\n"
      "v_add_u32 v8, v8, v236\n"
      "v_mul_u32_u24 v236, 0x180, v8\n"
      "v_bfrev_b32 v209, 1\n"
      "v_cndmask_b32 v236, v236, v209, s[70:71]\n"
      "v_xor_b32 v208, v208, v239\n"
      "v_lshl_add_u32 v236, v208, 4, v236\n"
      "s_add_u32 m0, s64, 149504\n"
      "s_nop 0\n"
      "buffer_load_dwordx4 v236, s[16:19], 0 offen  lds\n"
      "s_add_u32 m0, s64, 153472\n"
      "s_nop 0\n"
      "buffer_load_dwordx4 v236, s[16:19], 0 offen offset:128  lds\n"
      "s_add_u32 m0, s64, 157440\n"
      "s_nop 0\n"
      "buffer_load_dwordx4 v236, s[16:19], 0 offen offset:256  lds\n"
      "s_movk_i32 s95, 504\n"
      "s_and_b32 s92, s40, 1\n"
      "s_lshl_b32 s92, s92, 2\n"
      "v_lshrrev_b32 v8, 1, v206\n"
      "v_xor_b32 v8, v8, v239\n"
      "v_xor_b32 v8, s92, v8\n"
      "v_lshlrev_b32 v8, 4, v8\n"
      "s_lshl_b32 s92, s40, 3\n"
      "s_brev_b32 s43, 1\n"
      "s_cmp_lt_u32 s40, 2\n"
      "s_cselect_b32 s93, 32, -16\n"
      "s_cselect_b32 s94, 0, 128\n"
      "s_cselect_b32 s43, 256, s43\n"
      "v_add_u32 v239, s92, v206\n"
      "v_mul_u32_u24 v236, 37, v239\n"
      "v_lshrrev_b32_e64 v236, 8, v236\n"
      "v_mad_u32_u24 v236, v236, s95, v239\n"
      "v_mul_u32_u24 v236, 384, v236\n"
      "v_add_u32 v16, v236, v8\n"
      "v_add_u32 v236, s93, v239\n"
      "v_mul_u32_u24 v206, 37, v236\n"
      "v_lshrrev_b32 v206, 8, v206\n"
      "v_mad_u32_u24 v206, v206, s95, v236\n"
      "v_mul_u32_u24 v206, 384, v206\n"
      "v_add3_u32 v17, v206, v8, s94\n"
      "v_add_u32_e64 v236, 16, v239\n"
      "v_mul_u32_u24_e64 v239, 37, v236\n"
      "v_lshrrev_b32_e64 v239, 8, v239\n"
      "v_mad_u32_u24 v239, v239, s95, v236\n"
      "v_mul_u32_u24 v239, 384, v239\n"
      "s_movk_i32 s94, 128\n"
      "v_add3_u32 v18, v239, v8, s94\n"
      "v_add_u32 v19, 256, v16\n"
      "v_add_u32_e64 v15, s43, v17\n"
      "s_add_u32 s92, s41, 0\n"
      "s_mul_i32 s93, s92, 58255\n"
      "s_lshr_b32 s93, s93, 22\n"
      "s_mul_i32 s93, s93, 439\n"
      "s_add_u32 s45, s92, s93\n"
      "s_mul_i32 s45, s45, 2688\n"
      "s_add_u32 s45, s45, 589824\n"
      "s_add_u32 m0, s64, 0\n"
      "s_nop 0\n"
      "buffer_load_dwordx4 v16, s[16:19], s45 offen  lds\n"
      "s_add_u32 m0, s64, 4096\n"
      "s_nop 0\n"
      "buffer_load_dwordx4 v17, s[16:19], s45 offen  lds\n"
      "s_add_u32 m0, s64, 8192\n"
      "s_nop 0\n"
      "buffer_load_dwordx4 v18, s[16:19], s45 offen  lds\n"
      "s_add_u32 m0, s64, 12288\n"
      "s_nop 0\n"
      "buffer_load_dwordx4 v19, s[16:19], s45 offen  lds\n"
      "s_add_u32 m0, s65, 16384\n"
      "s_nop 0\n"
      "buffer_load_dwordx4 v15, s[16:19], s45 offen  lds\n"
      "s_add_u32 s92, s41, 0\n"
      "s_mul_i32 s93, s92, 58255\n"
      "s_lshr_b32 s93, s93, 22\n"
      "s_add_u32 s47, s92, s93\n"
      "s_mul_i32 s47, s47, 4802\n"
      "s_and_b32 s47, s47, -4\n"
      "s_add_u32 s92, s41, 256\n"
      "s_mul_i32 s93, s92, 58255\n"
      "s_lshr_b32 s93, s93, 22\n"
      "s_add_u32 s74, s92, s93\n"
      "s_mul_i32 s74, s74, 4802\n"
      "s_and_b32 s74, s74, -4\n"
      "s_mul_i32 s93, s93, 439\n"
      "s_add_u32 s46, s92, s93\n"
      "s_mul_i32 s46, s46, 2688\n"
      "s_add_u32 s46, s46, 589824\n"
      "v_subrev_u32 v8, 2, v238\n"
      "v_cmp_ge_u32 s[72:73], s44, v8\n"
      "v_min_u32_e64 v8, s44, v8\n"
      "s_movk_i32 s93, 4180\n"
      "s_cmp_lt_u32 s41, 209\n"
      "s_cselect_b32 s92, 209, 47\n"
      "s_cselect_b32 s93, 795, s93\n"
      "s_cmp_lt_u32 s41, 252\n"
      "s_cselect_b32 s94, 16, 15\n"
      "s_add_u32 s93, s93, s41\n"
      "v_lshl_add_u32 v239, v8, 8, s41\n"
      "v_mul_u32_u24 v236, s92, v8\n"
      "v_add_u32_e64 v236, s93, v236\n"
      "v_cmp_lt_u32 vcc, s94, v8\n"
      "v_subrev_u32 v206, 5184, v236\n"
      "s_nop 0\n"
      "v_cndmask_b32 v239, v239, v236, vcc\n"
      "v_cmp_le_u32 vcc, 5184, v239\n"
      "v_mul_u32_u24 v207, 58255, v239\n"
      "v_lshrrev_b32 v207, 22, v207\n"
      "v_mul_u32_u24 v208, 72, v207\n"
      "v_sub_u32_e64 v208, v239, v208\n"
      "v_min_u32 v236, 72, v206\n"
      "v_cndmask_b32 v207, v207, v236, vcc\n"
      "v_subrev_u32 v236, 72, v206\n"
      "v_min_u32 v236, 72, v236\n"
      "v_cndmask_b32 v208, v208, v236, vcc\n"
      "v_mul_u32_u24 v38, 73, v207\n"
      "v_add_u32 v38, v38, v208\n"
      "v_mul_u32_u24 v38, 4802, v38\n"
      "v_mul_u32_u24 v8, 0x1ff, v207\n"
      "v_add_u32_e64 v8, v8, v208\n"
      "v_mul_u32_u24 v8, 2688, v8\n"
      "v_add_u32 v8, 589824, v8\n"
      "v_cmp_eq_u32 vcc, 72, v207\n"
      "v_or_b32 v69, 0x80000000, v8\n"
      "s_nop 0\n"
      "v_cndmask_b32 v8, v8, v69, vcc\n"
      "v_cmp_eq_u32 vcc, 72, v208\n"
      "v_or_b32_e64 v69, 0x40000000, v8\n"
      "s_nop 0\n"
      "v_cndmask_b32 v8, v8, v69, vcc\n"
      "v_mov_b32 v236, 0x20000000\n"
      "v_cndmask_b32 v8, v236, v8, s[72:73]\n"
      "v_cndmask_b32 v38, v236, v38, s[72:73]\n"
      "v_mov_b32 v37, v236\n"
      "s_nop 1\n"
      "v_mov_b32_dpp v37, v8 wave_shr:1 row_mask:0xf bank_mask:0xf\n"
      "v_mov_b32 v79, 0\n"
      "v_mov_b32 v83, 0\n"
      "v_mov_b32 v87, 0\n"
      "v_mov_b32 v91, 0\n"
      "v_mov_b32 v95, 0\n"
      "s_lshl_b32 s94, s40, 2\n"
      "s_add_u32 s94, s94, 115456\n"
      "v_add_u32 v31, s94, v158\n"
      "v_and_b32 v69, 3, v238\n"
      "v_cmp_ne_u32 vcc, 0, v69\n"
      "v_mov_b32 v69, 0x3f803f80\n"
      "s_nop 0\n"
      "v_cndmask_b32_e64 v32, v69, 0, vcc\n"
      "v_mov_b32 v33, v32\n"
      "v_mov_b32 v34, v32\n"
      "v_mov_b32 v35, v32\n"
      "v_and_b32 v181, 15, v238\n"
      "v_lshrrev_b32_e64 v67, 4, v238\n"
      "v_mul_u32_u24 v181, 196, v181\n"
      "v_add_u32 v181, 124928, v181\n"
      "v_lshl_add_u32 v67, v67, 4, v181\n"
      "v_min_u32 v43, 192, v237\n"
      "v_add_u32 v43, 124928, v43\n"
      "global_load_dwordx4 v[236:239], v158, s[88:89]\n"
      "s_waitcnt lgkmcnt(0)\n"
      "s_pack_ll_b32_b16 s92, s0, s8\n"
      "s_waitcnt vmcnt(14)\n"
      "v_dot2_f32_bf16 v234, s92, v223, 0\n"
      "s_pack_hh_b32_b16 s93, s0, s8\n"
      "v_dot2_f32_bf16 v131, s93, v223, 0\n"
      "s_pack_ll_b32_b16 s94, s1, s9\n"
      "v_dot2_f32_bf16 v233, s94, v223, 0\n"
      "s_pack_hh_b32_b16 s95, s1, s9\n"
      "v_dot2_f32_bf16 v220, s95, v223, 0\n"
      "s_pack_ll_b32_b16 s92, s2, s10\n"
      "v_dot2_f32_bf16 v133, s92, v223, 0\n"
      "s_pack_hh_b32_b16 s93, s2, s10\n"
      "v_dot2_f32_bf16 v222, s93, v223, 0\n"
      "s_pack_ll_b32_b16 s94, s3, s11\n"
      "v_dot2_f32_bf16 v130, s94, v223, 0\n"
      "s_pack_hh_b32_b16 s95, s3, s11\n"
      "v_dot2_f32_bf16 v194, s95, v223, 0\n"
      "s_pack_ll_b32_b16 s92, s4, s12\n"
      "v_dot2_f32_bf16 v235, s92, v223, 0\n"
      "s_pack_hh_b32_b16 s93, s4, s12\n"
      "v_dot2_f32_bf16 v132, s93, v223, 0\n"
      "s_pack_ll_b32_b16 s94, s5, s13\n"
      "v_dot2_f32_bf16 v221, s94, v223, 0\n"
      "global_load_dwordx4 v[206:209], v158, s[88:89] offset:1024\n"
      "s_pack_hh_b32_b16 s95, s5, s13\n"
      "v_dot2_f32_bf16 v111, s95, v223, 0\n"
      "s_pack_ll_b32_b16 s92, s6, s14\n"
      "v_dot2_f32_bf16 v232, s92, v223, 0\n"
      "s_pack_hh_b32_b16 s93, s6, s14\n"
      "v_dot2_f32_bf16 v71, s93, v223, 0\n"
      "s_pack_ll_b32_b16 s94, s7, s15\n"
      "v_dot2_f32_bf16 v196, s94, v223, 0\n"
      "s_pack_hh_b32_b16 s95, s7, s15\n"
      "v_dot2_f32_bf16 v110, s95, v223, 0\n"
      "s_lshl_b32 s92, s40, 4\n"
      "s_lshr_b64 s[66:67], s[66:67], s92\n"
      "s_lshl_b32 s66, s66, 16\n"
      "v_mov_b32 v223, s66\n"
      "v_max_f32 v223, 0xc2c80000, v223\n"
      "v_min_f32 v223, 0x40930000, v223\n"
      "v_mul_f32 v223, 0x3fb8aa3b, v223\n"
      "v_exp_f32 v223, v223\n"
      "s_nop 0\n"
      "v_cvt_pk_bf16_f32 v223, v223, v223\n"
      "v_lshlrev_b32 v223, 16, v223\n"
      "v_max_f32 v234, 0, v234\n"
      "v_max_f32_e64 v131, 0, v131\n"
      "global_load_dwordx4 v[0:3], v158, s[88:89] offset:2048\n"
      "v_cvt_pk_bf16_f32 v234, v234, v131\n"
      "v_max_f32 v233, 0, v233\n"
      "v_max_f32 v220, 0, v220\n"
      "v_cvt_pk_bf16_f32 v233, v233, v220\n"
      "v_max_f32 v133, 0, v133\n"
      "v_max_f32 v222, 0, v222\n"
      "v_cvt_pk_bf16_f32 v133, v133, v222\n"
      "v_max_f32 v130, 0, v130\n"
      "v_max_f32 v194, 0, v194\n"
      "v_cvt_pk_bf16_f32 v130, v130, v194\n"
      "v_max_f32 v235, 0, v235\n"
      "v_max_f32 v132, 0, v132\n"
      "v_cvt_pk_bf16_f32 v235, v235, v132\n"
      "v_max_f32 v221, 0, v221\n"
      "v_max_f32 v111, 0, v111\n"
      "v_cvt_pk_bf16_f32 v221, v221, v111\n"
      "v_max_f32 v232, 0, v232\n"
      "v_max_f32 v71, 0, v71\n"
      "v_cvt_pk_bf16_f32 v232, v232, v71\n"
      "v_max_f32 v196, 0, v196\n"
      "v_max_f32 v110, 0, v110\n"
      "v_cvt_pk_bf16_f32 v196, v196, v110\n"
      "s_mov_b32 s42, 0\n"
      "s_add_u32 m0, s75, 0\n"
      "s_nop 0\n"
      "buffer_load_dwordx4 v99, s[20:23], s42 offen  lds\n"
      "s_pack_ll_b32_b16 s92, s48, s50\n"
      "v_dot2_f32_bf16 v220, s92, v234, 0\n"
      "s_pack_hh_b32_b16 s93, s48, s50\n"
      "v_dot2_f32_bf16 v222, s93, v234, 0\n"
      "s_pack_ll_b32_b16 s94, s49, s51\n"
      "v_dot2_f32_bf16 v132, s94, v234, 0\n"
      "s_pack_hh_b32_b16 s95, s49, s51\n"
      "v_dot2_f32_bf16 v131, s95, v234, 0\n"
      "s_pack_ll_b32_b16 s92, s52, s54\n"
      "v_dot2_f32_bf16 v220, s92, v233, v220\n"
      "s_pack_hh_b32_b16 s93, s52, s54\n"
      "v_dot2_f32_bf16 v222, s93, v233, v222\n"
      "s_pack_ll_b32_b16 s94, s53, s55\n"
      "v_dot2_f32_bf16 v132, s94, v233, v132\n"
      "s_pack_hh_b32_b16 s95, s53, s55\n"
      "v_dot2_f32_bf16 v131, s95, v233, v131\n"
      "s_pack_ll_b32_b16 s92, s56, s58\n"
      "v_dot2_f32_bf16 v220, s92, v133, v220\n"
      "s_pack_hh_b32_b16 s93, s56, s58\n"
      "v_dot2_f32_bf16 v222, s93, v133, v222\n"
      "s_pack_ll_b32_b16 s94, s57, s59\n"
      "v_dot2_f32_bf16 v132, s94, v133, v132\n"
      "s_add_u32 m0, s75, 3968\n"
      "s_nop 0\n"
      "buffer_load_dwordx4 v99, s[20:23], s42 offen offset:128  lds\n"
      "s_pack_hh_b32_b16 s95, s57, s59\n"
      "v_dot2_f32_bf16 v131, s95, v133, v131\n"
      "s_pack_ll_b32_b16 s92, s60, s62\n"
      "v_dot2_f32_bf16 v220, s92, v130, v220\n"
      "s_pack_hh_b32_b16 s93, s60, s62\n"
      "v_dot2_f32_bf16 v222, s93, v130, v222\n"
      "s_pack_ll_b32_b16 s94, s61, s63\n"
      "v_dot2_f32_bf16 v132, s94, v130, v132\n"
      "s_pack_hh_b32_b16 s95, s61, s63\n"
      "v_dot2_f32_bf16 v131, s95, v130, v131\n"
      "s_pack_ll_b32_b16 s92, s24, s26\n"
      "v_dot2_f32_bf16 v220, s92, v235, v220\n"
      "s_pack_hh_b32_b16 s93, s24, s26\n"
      "v_dot2_f32_bf16 v222, s93, v235, v222\n"
      "s_pack_ll_b32_b16 s94, s25, s27\n"
      "v_dot2_f32_bf16 v132, s94, v235, v132\n"
      "s_pack_hh_b32_b16 s95, s25, s27\n"
      "v_dot2_f32_bf16 v131, s95, v235, v131\n"
      "s_pack_ll_b32_b16 s92, s28, s30\n"
      "v_dot2_f32_bf16 v220, s92, v221, v220\n"
      "s_pack_hh_b32_b16 s93, s28, s30\n"
      "v_dot2_f32_bf16 v222, s93, v221, v222\n"
      "s_pack_ll_b32_b16 s94, s29, s31\n"
      "s_add_u32 m0, s75, 7936\n"
      "s_nop 0\n"
      "buffer_load_dwordx4 v99, s[20:23], s42 offen offset:256  lds\n"
      "v_dot2_f32_bf16 v132, s94, v221, v132\n"
      "s_pack_hh_b32_b16 s95, s29, s31\n"
      "v_dot2_f32_bf16 v131, s95, v221, v131\n"
      "s_pack_ll_b32_b16 s92, s32, s34\n"
      "v_dot2_f32_bf16 v220, s92, v232, v220\n"
      "s_pack_hh_b32_b16 s93, s32, s34\n"
      "v_dot2_f32_bf16 v222, s93, v232, v222\n"
      "s_pack_ll_b32_b16 s94, s33, s35\n"
      "v_dot2_f32_bf16 v132, s94, v232, v132\n"
      "s_pack_hh_b32_b16 s95, s33, s35\n"
      "v_dot2_f32_bf16 v131, s95, v232, v131\n"
      "s_pack_ll_b32_b16 s92, s36, s38\n"
      "v_dot2_f32_bf16 v220, s92, v196, v220\n"
      "s_pack_hh_b32_b16 s93, s36, s38\n"
      "v_dot2_f32_bf16 v222, s93, v196, v222\n"
      "s_pack_ll_b32_b16 s94, s37, s39\n"
      "v_dot2_f32_bf16 v132, s94, v196, v132\n"
      "s_pack_hh_b32_b16 s95, s37, s39\n"
      "v_dot2_f32_bf16 v131, s95, v196, v131\n"
      "s_load_dwordx2 s[28:29], s[90:91], 8\n"
      "s_load_dwordx2 s[34:35], s[90:91], 16\n"
      "s_load_dwordx2 s[24:25], s[90:91], 24\n"
      "s_add_u32 m0, s75, 11904\n"
      "s_nop 0\n"
      "buffer_load_dwordx4 v99, s[20:23], s42 offen offset:384  lds\n"
      "s_load_dwordx2 s[36:37], s[90:91], 40\n"
      "s_mov_b32 s26, 100270464\n"
      "s_mov_b32 s27, 0x20000\n"
      "s_mov_b32 s38, 73728\n"
      "s_mov_b32 s39, 0x20000\n"
      "s_mov_b32 s30, 25589860\n"
      "s_mov_b32 s31, 0x20000\n"
      "s_mov_b32 s53, 0x3fb8aa3b\n"
      "s_mov_b32 s51, 0x2a8cbccc\n"
      "s_mov_b32 s52, 0x38fba882\n"
      "s_mov_b32 s58, 0xffff0000\n"
      "s_mov_b32 s59, 0x0\n"
      "s_mov_b32 s60, 0x0\n"
      "s_mov_b32 s61, 0xffff\n"
      "s_mov_b32 s62, 0x0\n"
      "s_mov_b32 s63, 0xffff0000\n"
      "v_mov_b32 v69, 0x41f00000\n"
      "v_cmp_nle_f32 s[54:55], v223, v69\n"
      "v_mul_f32 v223, s53, v223\n"
      "s_nop 0\n"
      "v_readfirstlane_b32 s50, v223\n"
      "v_cvt_pk_bf16_f32 v220, v220, v220\n"
      "v_lshlrev_b32 v220, 16, v220\n"
      "s_add_u32 m0, s75, 15872\n"
      "s_nop 0\n"
      "buffer_load_dwordx4 v99, s[20:23], s42 offen offset:512  lds\n"
      "v_mul_f32 v220, 0xbfb8aa3b, v220\n"
      "v_exp_f32 v220, v220\n"
      "s_nop 0\n"
      "v_add_f32 v220, 1.0, v220\n"
      "v_rcp_f32 v220, v220\n"
      "s_nop 0\n"
      "v_cvt_pk_bf16_f32 v220, v220, v220\n"
      "v_lshlrev_b32 v220, 16, v220\n"
      "v_add_f32 v220, -0.5, v220\n"
      "v_mul_f32 v220, 0x41b8aa3b, v220\n"
      "ds_write_b32 v68, v220\n"
      "v_cvt_pk_bf16_f32 v222, v222, v222\n"
      "v_lshlrev_b32 v222, 16, v222\n"
      "v_mul_f32 v222, 0xbfb8aa3b, v222\n"
      "v_exp_f32 v222, v222\n"
      "s_nop 0\n"
      "v_add_f32 v222, 1.0, v222\n"
      "v_rcp_f32_e64 v222, v222\n"
      "s_nop 0\n"
      "v_cvt_pk_bf16_f32 v222, v222, v222\n"
      "v_lshlrev_b32 v222, 16, v222\n"
      "v_add_f32 v222, -0.5, v222\n"
      "v_mul_f32 v222, 0x41b8aa3b, v222\n"
      "ds_write_b32 v68, v222 offset:704\n"
      "v_cvt_pk_bf16_f32 v132, v132, v132\n"
      "v_lshlrev_b32 v132, 16, v132\n"
      "s_add_u32 m0, s75, 19840\n"
      "s_nop 0\n"
      "buffer_load_dwordx4 v99, s[20:23], s42 offen offset:640  lds\n"
      "v_mul_f32 v132, 0xbfb8aa3b, v132\n"
      "v_exp_f32 v132, v132\n"
      "s_nop 0\n"
      "v_add_f32 v132, 1.0, v132\n"
      "v_rcp_f32_e64 v132, v132\n"
      "s_nop 0\n"
      "v_cvt_pk_bf16_f32 v132, v132, v132\n"
      "v_lshlrev_b32 v132, 16, v132\n"
      "v_add_f32 v132, -0.5, v132\n"
      "v_mul_f32 v132, 0x41b8aa3b, v132\n"
      "ds_write_b32 v68, v132 offset:1408\n"
      "v_cvt_pk_bf16_f32 v131, v131, v131\n"
      "v_lshlrev_b32 v131, 16, v131\n"
      "v_mul_f32 v131, 0xbfb8aa3b, v131\n"
      "v_exp_f32 v131, v131\n"
      "s_nop 0\n"
      "v_add_f32 v131, 1.0, v131\n"
      "v_rcp_f32 v131, v131\n"
      "s_nop 0\n"
      "v_cvt_pk_bf16_f32 v131, v131, v131\n"
      "v_lshlrev_b32 v131, 16, v131\n"
      "v_add_f32 v131, -0.5, v131\n"
      "v_mul_f32 v131, 0x41b8aa3b, v131\n"
      "ds_write_b32 v68, v131 offset:2112\n"
      "s_mul_i32 s76, s40, 704\n"
      "s_add_u32 s76, s76, 122112\n"
      "s_mov_b32 s43, 36864\n"
      "s_add_u32 m0, s75, 24576\n"
      "s_nop 0\n"
      "buffer_load_dwordx4 v99, s[20:23], s43 offen  lds\n"
      "s_add_u32 m0, s75, 28544\n"
      "s_nop 0\n"
      "buffer_load_dwordx4 v99, s[20:23], s43 offen offset:128  lds\n"
      "s_add_u32 m0, s75, 32512\n"
      "s_nop 0\n"
      "buffer_load_dwordx4 v99, s[20:23], s43 offen offset:256  lds\n"
      "s_add_u32 m0, s75, 36480\n"
      "s_nop 0\n"
      "buffer_load_dwordx4 v99, s[20:23], s43 offen offset:384  lds\n"
      "s_add_u32 m0, s75, 40448\n"
      "s_nop 0\n"
      "buffer_load_dwordx4 v99, s[20:23], s43 offen offset:512  lds\n"
      "s_add_u32 m0, s75, 44416\n"
      "s_nop 0\n"
      "buffer_load_dwordx4 v99, s[20:23], s43 offen offset:640  lds\n"
      "s_mov_b32 s45, s46\n"
      "s_mov_b32 s49, s47\n"
      "s_mov_b32 s66, 0\n"
      "s_mov_b32 s67, 0\n"
      "s_mov_b32 s68, 0\n"
      "s_mov_b32 s69, 0\n"
      "s_mov_b32 s70, 0\n"
      "s_mov_b32 s71, 0\n"
      "s_mov_b32 s48, 0\n"
      "s_mov_b32 s33, 0x7fff7fff\n"
      "s_mov_b32 s42, 1\n"
      "s_mov_b32 s43, 5\n"
      "v_readlane_b32 s47, v38, 4\n"
      "s_nop 0\n"
      "s_and_b32 s47, s47, -4\n"
      "s_waitcnt vmcnt(12)\n"
      "v_add_u32 v119, 149504, v0\n"
      "v_add_u32 v102, 149504, v1\n"
      "v_add_u32 v205, 49152, v236\n"
      "v_add_u32 v204, 49152, v237\n"
      "v_add_u32 v142, 49152, v238\n"
      "v_add_u32 v89, 49152, v239\n"
      "v_add_u32 v78, 49152, v206\n"
      "s_waitcnt vmcnt(6)\n"
      "s_waitcnt lgkmcnt(0)\n"
      "s_barrier\n"
      "s_add_u32 m0, s75, 49152\n"
      "s_mov_b32 s78, 73728\n"
      "buffer_load_dwordx4 v99, s[20:23], s78 offen  lds\n"
      "s_add_u32 m0, s75, 53120\n"
      "ds_read_b128 a[216:219], v119\n"
      "ds_read_b64_tr_b16 a[48:49], v238\n"
      "ds_read_b128 a[220:223], v0\n"
      "ds_read_b64_tr_b16 a[2:3], v236 offset:512\n"
      "ds_read_b64_tr_b16 a[72:73], v236 offset:12288\n"
      "ds_read_b64_tr_b16 a[74:75], v236 offset:12800\n"
      "buffer_load_dwordx4 v99, s[20:23], s78 offen offset:128  lds\n"
      "ds_read_b64_tr_b16 a[0:1], v236\n"
      "ds_read_b64_tr_b16 a[50:51], v238 offset:512\n"
      "ds_read_b64_tr_b16 a[24:25], v237\n"
      "v_add_u32 v148, 6272, v67\n"
      "s_add_u32 m0, s75, 57088\n"
      "ds_read_b128 a[224:227], v0 offset:2048\n"
      "ds_read_b64_tr_b16 a[26:27], v237 offset:512\n"
      "buffer_load_dwordx4 v99, s[20:23], s78 offen offset:256  lds\n"
      "ds_read2_b32 v[68:69], v148 offset0:16 offset1:17\n"
      "ds_read_b32 v107, v181 offset:3328\n"
      "ds_read_b128 a[244:247], v1 offset:4096\n"
      "ds_read_b64_tr_b16 a[120:121], v238 offset:12288\n"
      "s_add_u32 m0, s75, 61056\n"
      "ds_read_b64_tr_b16 a[122:123], v238 offset:12800\n"
      "s_waitcnt lgkmcnt(8)\n"
      "v_mfma_f32_16x16x32_bf16 v[232:235], a[48:51], a[216:219], 0\n"
      "buffer_load_dwordx4 v99, s[20:23], s78 offen offset:384  lds\n"
      "ds_read_b64_tr_b16 a[98:99], v237 offset:12800\n"
      "v_mfma_f32_16x16x32_bf16 v[220:223], a[0:3], a[220:223], 0\n"
      "s_mov_b32 s79, 110592\n"
      "v_add_u32 v74, 3136, v67\n"
      "s_waitcnt lgkmcnt(6)\n"
      "v_mfma_f32_16x16x32_bf16 v[130:133], a[24:27], a[224:227], 0\n"
      "s_add_u32 m0, s75, 65024\n"
      "ds_read_b128 a[228:231], v0 offset:4096\n"
      "ds_read_b64_tr_b16 a[96:97], v237 offset:12288\n"
      "v_mfma_f32_16x16x32_bf16 v[194:197], a[24:27], a[220:223], 0\n"
      "buffer_load_dwordx4 v99, s[20:23], s78 offen offset:512  lds\n"
      "v_mfma_f32_16x16x32_bf16 v[108:111], a[48:51], a[224:227], 0\n"
      "ds_read_b128 a[236:239], v1\n"
      "ds_read2_b32 v[58:59], v74 offset0:18 offset1:19\n"
      "v_mfma_f32_16x16x32_bf16 v[210:213], a[0:3], a[224:227], 0\n"
      "ds_read_b128 a[232:235], v102\n"
      "s_add_u32 m0, s75, 68992\n"
      "ds_read2_b32 v[70:71], v148 offset0:18 offset1:19\n"
      "v_mfma_f32_16x16x32_bf16 v[120:123], a[72:75], a[220:223], 0\n"
      "buffer_load_dwordx4 v99, s[20:23], s78 offen offset:640  lds\n"
      "v_mfma_f32_16x16x32_bf16 v[244:247], a[0:3], a[216:219], 0\n"
      "ds_read_b128 a[252:255], v0 offset:6144\n"
      "s_waitcnt lgkmcnt(5)\n"
      "v_mfma_f32_16x16x32_bf16 v[240:243], a[96:99], a[224:227], 0\n"
      "ds_read2_b32 v[72:73], v148 offset0:32 offset1:33\n"
      "ds_read_b128 a[240:243], v1 offset:2048\n"
      "v_mfma_f32_16x16x32_bf16 v[252:255], a[96:99], a[228:231], 0\n"
      "s_waitcnt vmcnt(6)\n"
      "s_barrier\n"
      "global_load_dwordx4 v[4:7], v158, s[88:89] offset:3072\n"
      "ds_read_b64_tr_b16 a[4:5], v236 offset:24576\n"
      "ds_read_b64_tr_b16 a[28:29], v237 offset:24576\n"
      "s_add_u32 s88, s88, 4096\n"
      "s_addc_u32 s89, s89, 0\n"
      "ds_read_b64_tr_b16 a[54:55], v238 offset:25088\n"
      "ds_read_b64_tr_b16 a[124:125], v238 offset:36864\n"
      "ds_read_b64_tr_b16 a[126:127], v238 offset:37376\n"
      "ds_read_b64_tr_b16 a[100:101], v237 offset:36864\n"
      "v_mfma_f32_16x16x32_bf16 v[170:173], a[72:75], a[224:227], 0\n"
      "ds_read_b64_tr_b16 a[102:103], v237 offset:37376\n"
      "global_load_dwordx4 v[8:11], v158, s[88:89]\n"
      "ds_read_b64_tr_b16 a[52:53], v238 offset:24576\n"
      "ds_read_b64_tr_b16 a[76:77], v236 offset:36864\n"
      "v_mfma_f32_16x16x32_bf16 v[138:141], a[120:123], a[224:227], 0\n"
      "ds_read_b64_tr_b16 a[30:31], v237 offset:25088\n"
      "ds_read_b64_tr_b16 a[78:79], v236 offset:37376\n"
      "ds_read_b64_tr_b16 a[6:7], v236 offset:25088\n"
      "v_mfma_f32_16x16x32_bf16 v[224:227], a[96:99], a[216:219], 0\n"
      "global_load_dwordx4 v[12:15], v158, s[88:89] offset:1024\n"
      "v_mfma_f32_16x16x32_bf16 v[248:251], a[120:123], a[216:219], 0\n"
      "ds_read2_b32 v[50:51], v67 offset0:34 offset1:35\n"
      "ds_read2_b32 v[52:53], v74 offset0:0 offset1:1\n"
      "ds_read_b32 v106, v181 offset:192\n"
      "v_mfma_f32_16x16x32_bf16 v[216:219], a[72:75], a[216:219], 0\n"
      "ds_read_b128 a[224:227], v102 offset:4096\n"
      "v_lshl_add_u32 v107, v107, 2, s76\n"
      "v_mfma_f32_16x16x32_bf16 v[162:165], a[0:3], a[228:231], 0\n"
      "global_load_dwordx4 v[20:23], v158, s[88:89] offset:2048\n"
      "ds_read_b32 v149, v43 offset:9408\n"
      "v_mfma_f32_16x16x32_bf16 v[186:189], a[120:123], a[220:223], 0\n"
      "ds_read2_b32 v[42:43], v67 offset0:2 offset1:3\n"
      "s_waitcnt lgkmcnt(6)\n"
      "v_mfma_f32_16x16x32_bf16 v[186:189], a[124:127], a[236:239], v[186:189]\n"
      "ds_read2_b32 v[62:63], v74 offset0:34 offset1:35\n"
      "ds_read_b128 a[248:251], v119 offset:4096\n"
      "v_mfma_f32_16x16x32_bf16 v[190:193], a[96:99], a[220:223], 0\n"
      "global_load_dwordx4 v[24:27], v158, s[88:89] offset:3072\n"
      "v_mfma_f32_16x16x32_bf16 v[158:161], a[48:51], a[220:223], 0\n"
      "ds_read2_b32 v[44:45], v67 offset0:16 offset1:17\n"
      "s_mov_b32 s82, 0xffff\n"
      "ds_read2_b32 v[46:47], v67 offset0:18 offset1:19\n"
      "v_mfma_f32_16x16x32_bf16 v[158:161], a[52:55], a[236:239], v[158:161]\n"
      "ds_read2_b32 v[60:61], v74 offset0:32 offset1:33\n"
      "v_lshl_add_u32 v73, v73, 2, s76\n"
      "s_add_u32 m0, s75, 73728\n"
      "v_mfma_f32_16x16x32_bf16 v[190:193], a[100:103], a[236:239], v[190:193]\n"
      "buffer_load_dwordx4 v99, s[20:23], s79 offen  lds\n"
      "v_mfma_f32_16x16x32_bf16 v[166:169], a[24:27], a[216:219], 0\n"
      "s_add_u32 m0, s75, 77696\n"
      "ds_read2_b32 v[54:55], v74 offset0:2 offset1:3\n"
      "v_mfma_f32_16x16x32_bf16 v[220:223], a[4:7], a[236:239], v[220:223]\n"
      "ds_read2_b32 v[48:49], v67 offset0:32 offset1:33\n"
      "s_mov_b32 s78, 147456\n"
      "v_mfma_f32_16x16x32_bf16 v[194:197], a[28:31], a[236:239], v[194:197]\n"
      "ds_read2_b32 v[56:57], v74 offset0:16 offset1:17\n"
      "ds_read2_b32 v[74:75], v148 offset0:34 offset1:35\n"
      "v_mfma_f32_16x16x32_bf16 v[232:235], a[52:55], a[232:235], v[232:235]\n"
      "buffer_load_dwordx4 v99, s[20:23], s79 offen offset:128  lds\n"
      "v_mfma_f32_16x16x32_bf16 v[166:169], a[28:31], a[232:235], v[166:169]\n"
      "ds_read_b32 v146, v181 offset:6464\n"
      "v_lshl_add_u32 v70, v70, 2, s76\n"
      "v_mfma_f32_16x16x32_bf16 v[178:181], a[72:75], a[228:231], 0\n"
      "ds_read2_b32 v[64:65], v148 offset0:0 offset1:1\n"
      "s_add_u32 m0, s75, 81664\n"
      "v_mfma_f32_16x16x32_bf16 v[210:213], a[4:7], a[240:243], v[210:213]\n"
      "ds_read_b128 a[216:219], v0 offset:8192\n"
      "s_mov_b32 s91, 0x1ffff\n"
      "v_mfma_f32_16x16x32_bf16 v[244:247], a[4:7], a[232:235], v[244:247]\n"
      "buffer_load_dwordx4 v99, s[20:23], s79 offen offset:256  lds\n"
      "v_mfma_f32_16x16x32_bf16 v[240:243], a[100:103], a[240:243], v[240:243]\n"
      "s_mov_b32 s83, 0x0\n"
      "s_waitcnt lgkmcnt(4)\n"
      "v_lshl_add_u32 v63, v63, 2, s76\n"
      "v_mfma_f32_16x16x32_bf16 v[216:219], a[76:79], a[232:235], v[216:219]\n"
      "s_mov_b32 s90, 0xffffffff\n"
      "v_lshl_add_u32 v149, v149, 2, s76\n"
      "v_lshl_add_u32 v46, v46, 2, s76\n"
      "v_mfma_f32_16x16x32_bf16 v[120:123], a[76:79], a[236:239], v[120:123]\n"
      "ds_read_b128 a[236:239], v1 offset:10240\n"
      "v_lshl_add_u32 v54, v54, 2, s76\n"
      "s_add_u32 m0, s75, 85632\n"
      "v_mfma_f32_16x16x32_bf16 v[124:127], a[120:123], a[228:231], 0\n"
      "buffer_load_dwordx4 v99, s[20:23], s79 offen offset:384  lds\n"
      "s_add_u32 m0, s75, 89600\n"
      "v_mfma_f32_16x16x32_bf16 v[124:127], a[124:127], a[244:247], v[124:127]\n"
      "s_waitcnt vmcnt(9)\n"
      "s_barrier\n"
      "ds_read_b64_tr_b16 a[8:9], v205\n"
      "ds_read_b64_tr_b16 a[104:105], v204 offset:12288\n"
      "ds_read_b64_tr_b16 a[128:129], v142 offset:12288\n"
      "ds_read_b64_tr_b16 a[10:11], v205 offset:512\n"
      "ds_read_b64_tr_b16 a[32:33], v204\n"
      "v_mfma_f32_16x16x32_bf16 v[200:203], a[48:51], a[228:231], 0\n"
      "ds_read_b64_tr_b16 a[56:57], v142\n"
      "ds_read_b64_tr_b16 a[34:35], v204 offset:512\n"
      "buffer_load_dwordx4 v99, s[20:23], s79 offen offset:512  lds\n"
      "ds_read_b64_tr_b16 a[80:81], v205 offset:12288\n"
      "v_mfma_f32_16x16x32_bf16 v[130:133], a[28:31], a[240:243], v[130:133]\n"
      "ds_read_b64_tr_b16 a[130:131], v142 offset:12800\n"
      "ds_read_b64_tr_b16 a[58:59], v142 offset:512\n"
      "s_add_u32 m0, s75, 93568\n"
      "ds_read_b64_tr_b16 a[106:107], v204 offset:12800\n"
      "ds_read_b64_tr_b16 a[82:83], v205 offset:12800\n"
      "buffer_load_dwordx4 v99, s[20:23], s79 offen offset:640  lds\n"
      "v_lshl_add_u32 v49, v49, 2, s76\n"
      "v_lshl_add_u32 v55, v55, 2, s76\n"
      "v_mfma_f32_16x16x32_bf16 v[134:137], a[24:27], a[228:231], 0\n"
      "ds_read_b32 v149, v149\n"
      "v_lshl_add_u32 v50, v50, 2, s76\n"
      "v_mfma_f32_16x16x32_bf16 v[224:227], a[100:103], a[232:235], v[224:227]\n"
      "ds_read2_b32 v[40:41], v67 offset0:0 offset1:1\n"
      "s_add_u32 m0, s75, 0\n"
      "v_lshl_add_u32 v71, v71, 2, s76\n"
      "v_mfma_f32_16x16x32_bf16 v[252:255], a[100:103], a[244:247], v[252:255]\n"
      "buffer_load_dwordx4 v99, s[20:23], s78 offen  lds\n"
      "v_lshl_add_u32 v106, v106, 2, s76\n"
      "v_mfma_f32_16x16x32_bf16 v[170:173], a[76:79], a[240:243], v[170:173]\n"
      "s_add_u32 m0, s75, 3968\n"
      "v_lshl_add_u32 v61, v61, 2, s76\n"
      "v_lshl_add_u32 v58, v58, 2, s76\n"
      "v_mfma_f32_16x16x32_bf16 v[108:111], a[52:55], a[240:243], v[108:111]\n"
      "ds_read_b128 a[220:223], v0 offset:10240\n"
      "s_waitcnt lgkmcnt(3)\n"
      "v_lshl_add_u32 v65, v65, 2, s76\n"
      "v_lshl_add_u32 v72, v72, 2, s76\n"
      "v_mfma_f32_16x16x32_bf16 v[248:251], a[124:127], a[232:235], v[248:251]\n"
      "buffer_load_dwordx4 v99, s[20:23], s78 offen offset:128  lds\n"
      "v_mfma_f32_16x16x32_bf16 v[248:251], a[128:131], a[248:251], v[248:251]\n"
      "ds_read_b128 a[228:231], v1 offset:6144\n"
      "v_lshl_add_u32 v69, v69, 2, s76\n"
      "v_mfma_f32_16x16x32_bf16 v[210:213], a[8:11], a[216:219], v[210:213]\n"
      "ds_read2_b32 v[66:67], v148 offset0:2 offset1:3\n"
      "v_lshl_add_u32 v43, v43, 2, s76\n"
      "v_mfma_f32_16x16x32_bf16 v[138:141], a[124:127], a[240:243], v[138:141]\n"
      "s_add_u32 m0, s75, 7936\n"
      "ds_read_b32 v73, v73\n"
      "v_mfma_f32_16x16x32_bf16 v[162:165], a[4:7], a[244:247], v[162:165]\n"
      "buffer_load_dwordx4 v99, s[20:23], s78 offen offset:256  lds\n"
      "v_mfma_f32_16x16x32_bf16 v[190:193], a[104:107], a[252:255], v[190:193]\n"
      "ds_read_b32 v72, v72\n"
      "s_waitcnt lgkmcnt(4)\n"
      "v_mfma_f32_16x16x32_bf16 v[124:127], a[128:131], a[220:223], v[124:127]\n"
      "ds_read_b128 a[232:235], v1 offset:8192\n"
      "v_lshl_add_u32 v41, v41, 2, s76\n"
      "v_mfma_f32_16x16x32_bf16 v[158:161], a[56:59], a[252:255], v[158:161]\n"
      "s_add_u32 m0, s75, 11904\n"
      "ds_read_b32 v50, v50\n"
      "v_lshl_add_u32 v51, v51, 2, s76\n"
      "v_mfma_f32_16x16x32_bf16 v[178:181], a[76:79], a[244:247], v[178:181]\n"
      "buffer_load_dwordx4 v99, s[20:23], s78 offen offset:384  lds\n"
      "v_mfma_f32_16x16x32_bf16 v[240:243], a[104:107], a[216:219], v[240:243]\n"
      "s_add_u32 m0, s75, 15872\n"
      "ds_read_b32 v71, v71\n"
      "v_mfma_f32_16x16x32_bf16 v[224:227], a[104:107], a[248:251], v[224:227]\n"
      "ds_read_b32 v61, v61\n"
      "v_lshl_add_u32 v47, v47, 2, s76\n"
      "v_mfma_f32_16x16x32_bf16 v[170:173], a[80:83], a[216:219], v[170:173]\n"
      "ds_read_b128 a[240:243], v119 offset:8192\n"
      "v_readlane_b32 s86, v38, s43\n"
      "v_mfma_f32_16x16x32_bf16 v[108:111], a[56:59], a[216:219], v[108:111]\n"
      "buffer_load_dwordx4 v99, s[20:23], s78 offen offset:512  lds\n"
      "v_lshl_add_u32 v42, v42, 2, s76\n"
      "s_mov_b32 s79, 184320\n"
      "v_mfma_f32_16x16x32_bf16 v[166:169], a[32:35], a[248:251], v[166:169]\n"
      "s_add_u32 m0, s75, 19840\n"
      "v_lshl_add_u32 v40, v40, 2, s76\n"
      "v_lshl_add_u32 v75, v75, 2, s76\n"
      "v_mov_b32 v155, 0xf149f2ca\n"
      "v_mfma_f32_16x16x32_bf16 v[178:181], a[80:83], a[220:223], v[178:181]\n"
      "ds_read_b32 v69, v69\n"
      "v_lshl_add_u32 v48, v48, 2, s76\n"
      "v_lshl_add_u32 v44, v44, 2, s76\n"
      "v_mfma_f32_16x16x32_bf16 v[220:223], a[8:11], a[252:255], v[220:223]\n"
      "buffer_load_dwordx4 v99, s[20:23], s78 offen offset:640  lds\n"
      "v_mfma_f32_16x16x32_bf16 v[194:197], a[32:35], a[252:255], v[194:197]\n"
      "s_add_u32 m0, s75, 24576\n"
      "v_lshl_add_u32 v64, v64, 2, s76\n"
      "v_lshl_add_u32 v146, v146, 2, s76\n"
      "v_mfma_f32_16x16x32_bf16 v[232:235], a[56:59], a[248:251], v[232:235]\n"
      "s_waitcnt vmcnt(6)\n"
      "s_barrier\n"
      "ds_read_b64_tr_b16 a[108:109], v204 offset:36864\n"
      "ds_read_b64_tr_b16 a[132:133], v142 offset:36864\n"
      "v_mfma_f32_16x16x32_bf16 v[244:247], a[8:11], a[248:251], v[244:247]\n"
      "ds_read_b64_tr_b16 a[110:111], v204 offset:37376\n"
      "ds_read_b64_tr_b16 a[38:39], v204 offset:25088\n"
      "v_mfma_f32_16x16x32_bf16 v[200:203], a[52:55], a[244:247], v[200:203]\n"
      "ds_read_b64_tr_b16 a[134:135], v142 offset:37376\n"
      "ds_read_b64_tr_b16 a[84:85], v205 offset:36864\n"
      "ds_read_b64_tr_b16 a[12:13], v205 offset:24576\n"
      "ds_read_b64_tr_b16 a[36:37], v204 offset:24576\n"
      "ds_read_b64_tr_b16 a[60:61], v142 offset:24576\n"
      "v_mfma_f32_16x16x32_bf16 v[200:203], a[56:59], a[220:223], v[200:203]\n"
      "ds_read_b64_tr_b16 a[14:15], v205 offset:25088\n"
      "ds_read_b64_tr_b16 a[62:63], v142 offset:25088\n"
      "ds_read_b64_tr_b16 a[86:87], v205 offset:37376\n"
      "v_mfma_f32_16x16x32_bf16 v[216:219], a[80:83], a[248:251], v[216:219]\n"
      "buffer_load_dwordx4 v99, s[20:23], s79 offen  lds\n"
      "v_mfma_f32_16x16x32_bf16 v[134:137], a[28:31], a[244:247], v[134:137]\n"
      "ds_read_b32 v43, v43\n"
      "s_add_u32 m0, s75, 28544\n"
      "v_lshl_add_u32 v45, v45, 2, s76\n"
      "v_mfma_f32_16x16x32_bf16 v[134:137], a[32:35], a[220:223], v[134:137]\n"
      "ds_read_b128 a[244:247], v0 offset:12288\n"
      "v_lshl_add_u32 v56, v56, 2, s76\n"
      "v_mfma_f32_16x16x32_bf16 v[130:133], a[32:35], a[216:219], v[130:133]\n"
      "ds_read_b32 v65, v65\n"
      "buffer_load_dwordx4 v99, s[20:23], s79 offen offset:128  lds\n"
      "s_mov_b32 s78, 0\n"
      "v_mfma_f32_16x16x32_bf16 v[138:141], a[128:131], a[216:219], v[138:141]\n"
      "s_add_u32 m0, s75, 32512\n"
      "v_lshl_add_u32 v52, v52, 2, s76\n"
      "s_waitcnt lgkmcnt(3)\n"
      "v_mfma_f32_16x16x32_bf16 v[108:111], a[60:63], a[232:235], v[108:111]\n"
      "ds_read_b32 v106, v106\n"
      "ds_read_b32 v40, v40\n"
      "v_lshl_add_u32 v60, v60, 2, s76\n"
      "v_mfma_f32_16x16x32_bf16 v[186:189], a[128:131], a[252:255], v[186:189]\n"
      "buffer_load_dwordx4 v99, s[20:23], s79 offen offset:256  lds\n"
      "v_mfma_f32_16x16x32_bf16 v[120:123], a[80:83], a[252:255], v[120:123]\n"
      "ds_read_b32 v44, v44\n"
      "v_lshl_add_u32 v67, v67, 2, s76\n"
      "v_mfma_f32_16x16x32_bf16 v[120:123], a[84:87], a[228:231], v[120:123]\n"
      "ds_read_b128 a[216:219], v102 offset:8192\n"
      "v_lshl_add_u32 v74, v74, 2, s76\n"
      "v_mfma_f32_16x16x32_bf16 v[252:255], a[104:107], a[220:223], v[252:255]\n"
      "ds_read_b32 v48, v48\n"
      "s_add_u32 m0, s75, 36480\n"
      "v_mfma_f32_16x16x32_bf16 v[252:255], a[108:111], a[236:239], v[252:255]\n"
      "buffer_load_dwordx4 v99, s[20:23], s79 offen offset:384  lds\n"
      "v_cndmask_b32 v30, v155, v149, s[90:91]\n"
      "v_mfma_f32_16x16x32_bf16 v[124:127], a[132:135], a[236:239], v[124:127]\n"
      "ds_read_b32 v60, v60\n"
      "v_lshl_add_u32 v66, v66, 2, s76\n"
      "v_mfma_f32_16x16x32_bf16 v[178:181], a[84:87], a[236:239], v[178:181]\n"
      "s_add_u32 m0, s75, 40448\n"
      "ds_read_b32 v75, v75\n"
      "v_mfma_f32_16x16x32_bf16 v[244:247], a[12:15], a[224:227], v[244:247]\n"
      "ds_read_b32 v146, v146\n"
      "v_lshl_add_u32 v57, v57, 2, s76\n"
      "v_mfma_f32_16x16x32_bf16 v[162:165], a[8:11], a[220:223], v[162:165]\n"
      "buffer_load_dwordx4 v99, s[20:23], s79 offen offset:512  lds\n"
      "v_mfma_f32_16x16x32_bf16 v[200:203], a[60:63], a[236:239], v[200:203]\n"
      "ds_read_b128 a[248:251], v0 offset:14336\n"
      "s_add_u32 m0, s75, 44416\n"
      "ds_read_b32 v45, v45\n"
      "v_mfma_f32_16x16x32_bf16 v[240:243], a[108:111], a[232:235], v[240:243]\n"
      "ds_read_b32 v64, v64\n"
      "ds_read_b32 v70, v70\n"
      "v_mfma_f32_16x16x32_bf16 v[166:169], a[36:39], a[224:227], v[166:169]\n"
      "buffer_load_dwordx4 v99, s[20:23], s79 offen offset:640  lds\n"
      "v_mfma_f32_16x16x32_bf16 v[248:251], a[132:135], a[224:227], v[248:251]\n"
      "ds_read_b128 a[252:255], v0 offset:16384\n"
      "s_add_u32 m0, s75, 48384\n"
      "v_mfma_f32_16x16x32_bf16 v[186:189], a[132:135], a[228:231], v[186:189]\n"
      "ds_read_b32 v107, v107\n"
      "v_lshl_add_u32 v53, v53, 2, s76\n"
      "v_mfma_f32_16x16x32_bf16 v[134:137], a[36:39], a[236:239], v[134:137]\n"
      "ds_read_b32 v63, v63\n"
      "ds_read_b32 v53, v53\n"
      "v_mfma_f32_16x16x32_bf16 v[162:165], a[12:15], a[236:239], v[162:165]\n"
      "buffer_load_dwordx4 v99, s[20:23], s78 offen offset:768  lds\n"
      "v_mfma_f32_16x16x32_bf16 v[194:197], a[36:39], a[228:231], v[194:197]\n"
      "s_add_u32 m0, s75, 52352\n"
      "s_waitcnt vmcnt(7)\n"
      "s_barrier\n"
      "ds_read_b64_tr_b16 a[136:137], v238 offset:12288\n"
      "v_mfma_f32_16x16x32_bf16 v[224:227], a[108:111], a[224:227], v[224:227]\n"
      "ds_read_b64_tr_b16 a[88:89], v236 offset:12288\n"
      "ds_read_b64_tr_b16 a[66:67], v238 offset:512\n"
      "ds_read_b64_tr_b16 a[90:91], v236 offset:12800\n"
      "v_mfma_f32_16x16x32_bf16 v[220:223], a[12:15], a[228:231], v[220:223]\n"
      "ds_read_b64_tr_b16 a[16:17], v236\n"
      "ds_read_b64_tr_b16 a[18:19], v236 offset:512\n"
      "v_mfma_f32_16x16x32_bf16 v[232:235], a[60:63], a[224:227], v[232:235]\n"
      "buffer_load_dwordx4 v99, s[20:23], s78 offen offset:896  lds\n"
      "v_mfma_f32_16x16x32_bf16 v[210:213], a[12:15], a[232:235], v[210:213]\n"
      "ds_read_b64_tr_b16 a[138:139], v238 offset:12800\n"
      "ds_read_b64_tr_b16 a[42:43], v237 offset:512\n"
      "ds_read_b64_tr_b16 a[64:65], v238\n"
      "v_mfma_f32_16x16x32_bf16 v[138:141], a[132:135], a[232:235], v[138:141]\n"
      "ds_read_b64_tr_b16 a[112:113], v237 offset:12288\n"
      "ds_read_b64_tr_b16 a[114:115], v237 offset:12800\n"
      "v_mfma_f32_16x16x32_bf16 v[130:133], a[36:39], a[232:235], v[130:133]\n"
      "ds_read_b64_tr_b16 a[40:41], v237\n"
      "s_add_u32 m0, s75, 56320\n"
      "v_mfma_f32_16x16x32_bf16 v[170:173], a[84:87], a[232:235], v[170:173]\n"
      "buffer_load_dwordx4 v99, s[20:23], s78 offen offset:1024  lds\n"
      "v_mfma_f32_16x16x32_bf16 v[216:219], a[84:87], a[224:227], v[216:219]\n"
      "ds_read_b32 v67, v67\n"
      "s_waitcnt lgkmcnt(7)\n"
      "v_mfma_f32_16x16x32_bf16 v[216:219], a[88:91], a[240:243], v[216:219]\n"
      "ds_read_b32 v47, v47\n"
      "v_readlane_b32 s84, v37, s43\n"
      "v_mfma_f32_16x16x32_bf16 v[158:161], a[60:63], a[228:231], v[158:161]\n"
      "s_mov_b32 s78, 36864\n"
      "v_lshl_add_u32 v68, v68, 2, s76\n"
      "s_add_u32 m0, s75, 60672\n"
      "v_mfma_f32_16x16x32_bf16 v[210:213], a[16:19], a[248:251], v[210:213]\n"
      "buffer_load_dwordx4 v99, s[20:23], s78 offen offset:768  lds\n"
      "s_waitcnt lgkmcnt(2)\n"
      "v_mfma_f32_16x16x32_bf16 v[252:255], a[112:115], a[252:255], v[252:255]\n"
      "s_add_u32 m0, s75, 64640\n"
      "ds_read_b128 a[232:235], v119 offset:2048\n"
      "s_mov_b32 s79, 73728\n"
      "v_mfma_f32_16x16x32_bf16 v[186:189], a[136:139], a[244:247], v[186:189]\n"
      "ds_read_b32 v46, v46\n"
      "ds_read_b32 v54, v54\n"
      "v_mfma_f32_16x16x32_bf16 v[232:235], a[64:67], a[240:243], v[232:235]\n"
      "buffer_load_dwordx4 v99, s[20:23], s78 offen offset:896  lds\n"
      "v_mfma_f32_16x16x32_bf16 v[120:123], a[88:91], a[244:247], v[120:123]\n"
      "s_add_u32 m0, s75, 68608\n"
      "ds_read_b32 v68, v68\n"
      "v_mfma_f32_16x16x32_bf16 v[190:193], a[108:111], a[228:231], v[190:193]\n"
      "ds_read_b32 v58, v58\n"
      "ds_read_b32 v41, v41\n"
      "ds_read_b128 a[236:239], v102 offset:2048\n"
      "v_mfma_f32_16x16x32_bf16 v[124:127], a[136:139], a[252:255], v[124:127]\n"
      "buffer_load_dwordx4 v99, s[20:23], s78 offen offset:1024  lds\n"
      "v_mfma_f32_16x16x32_bf16 v[244:247], a[16:19], a[240:243], v[244:247]\n"
      "ds_read_b32 v49, v49\n"
      "v_lshl_add_u32 v62, v62, 2, s76\n"
      "v_mfma_f32_16x16x32_bf16 v[248:251], a[136:139], a[240:243], v[248:251]\n"
      "ds_read_b32 v57, v57\n"
      "v_cndmask_b32 v28, v155, v146, s[82:83]\n"
      "v_mfma_f32_16x16x32_bf16 v[220:223], a[16:19], a[244:247], v[220:223]\n"
      "ds_read_b32 v52, v52\n"
      "s_add_u32 m0, s75, 72960\n"
      "v_mfma_f32_16x16x32_bf16 v[178:181], a[88:91], a[252:255], v[178:181]\n"
      "buffer_load_dwordx4 v99, s[20:23], s79 offen offset:768  lds\n"
      "s_waitcnt lgkmcnt(4)\n"
      "v_mfma_f32_16x16x32_bf16 v[146:149], a[72:75], a[232:235], 0\n"
      "ds_read_b32 v55, v55\n"
      "v_readlane_b32 s85, v37, s42\n"
      "v_mfma_f32_16x16x32_bf16 v[158:161], a[64:67], a[244:247], v[158:161]\n"
      "ds_read_b32 v42, v42\n"
      "ds_read_b32 v74, v74\n"
      "v_mfma_f32_16x16x32_bf16 v[194:197], a[40:43], a[244:247], v[194:197]\n"
      "s_add_u32 m0, s75, 76928\n"
      "ds_read_b32 v56, v56\n"
      "v_mfma_f32_16x16x32_bf16 v[130:133], a[40:43], a[248:251], v[130:133]\n"
      "buffer_load_dwordx4 v99, s[20:23], s79 offen offset:896  lds\n"
      "v_mfma_f32_16x16x32_bf16 v[200:203], a[64:67], a[252:255], v[200:203]\n"
      "ds_read_b128 a[224:227], v1 offset:14336\n"
      "s_add_u32 m0, s75, 80896\n"
      "ds_read_b128 a[228:231], v1 offset:16384\n"
      "v_mfma_f32_16x16x32_bf16 v[134:137], a[40:43], a[252:255], v[134:137]\n"
      "ds_read_b32 v66, v66\n"
      "ds_read_b128 a[220:223], v1 offset:12288\n"
      "v_mfma_f32_16x16x32_bf16 v[162:165], a[16:19], a[252:255], v[162:165]\n"
      "buffer_load_dwordx4 v99, s[20:23], s79 offen offset:1024  lds\n"
      "v_mfma_f32_16x16x32_bf16 v[170:173], a[88:91], a[248:251], v[170:173]\n"
      "s_waitcnt vmcnt(9)\n"
      "s_barrier\n"
      "ds_read_b64_tr_b16 a[94:95], v236 offset:37376\n"
      "v_mfma_f32_16x16x32_bf16 v[190:193], a[112:115], a[244:247], v[190:193]\n"
      "ds_read_b64_tr_b16 a[20:21], v236 offset:24576\n"
      "ds_read_b64_tr_b16 a[92:93], v236 offset:36864\n"
      "v_mfma_f32_16x16x32_bf16 v[150:153], a[0:3], a[232:235], 0\n"
      "ds_read_b64_tr_b16 a[22:23], v236 offset:25088\n"
      "s_mov_b32 s79, 110592\n"
      "v_mfma_f32_16x16x32_bf16 v[224:227], a[112:115], a[240:243], v[224:227]\n"
      "ds_read_b64_tr_b16 a[116:117], v237 offset:36864\n"
      "ds_read_b64_tr_b16 a[68:69], v238 offset:24576\n"
      "v_mfma_f32_16x16x32_bf16 v[108:111], a[64:67], a[248:251], v[108:111]\n"
      "s_add_u32 m0, s75, 85248\n"
      "ds_read_b64_tr_b16 a[44:45], v237 offset:24576\n"
      "v_mfma_f32_16x16x32_bf16 v[166:169], a[40:43], a[240:243], v[166:169]\n"
      "ds_read_b64_tr_b16 a[70:71], v238 offset:25088\n"
      "ds_read_b64_tr_b16 a[140:141], v238 offset:36864\n"
      "v_mfma_f32_16x16x32_bf16 v[182:185], a[48:51], a[232:235], 0\n"
      "buffer_load_dwordx4 v99, s[20:23], s79 offen offset:768  lds\n"
      "v_mfma_f32_16x16x32_bf16 v[138:141], a[136:139], a[248:251], v[138:141]\n"
      "ds_read_b64_tr_b16 a[46:47], v237 offset:25088\n"
      "s_waitcnt lgkmcnt(6)\n"
      "v_mfma_f32_16x16x32_bf16 v[220:223], a[20:23], a[220:223], v[220:223]\n"
      "s_add_u32 m0, s75, 89216\n"
      "ds_read_b64_tr_b16 a[118:119], v237 offset:37376\n"
      "v_mfma_f32_16x16x32_bf16 v[244:247], a[20:23], a[216:219], v[244:247]\n"
      "ds_read_b64_tr_b16 a[142:143], v238 offset:37376\n"
      "v_lshl_add_u32 v59, v59, 2, s76\n"
      "v_mfma_f32_16x16x32_bf16 v[216:219], a[92:95], a[216:219], v[216:219]\n"
      "buffer_load_dwordx4 v99, s[20:23], s79 offen offset:896  lds\n"
      "v_mfma_f32_16x16x32_bf16 v[178:181], a[92:95], a[228:231], v[178:181]\n"
      "s_add_u32 m0, s75, 93184\n"
      "ds_read_b128 a[240:243], v119 offset:6144\n"
      "v_mfma_f32_16x16x32_bf16 v[170:173], a[92:95], a[224:227], v[170:173]\n"
      "ds_read_b128 a[244:247], v102 offset:6144\n"
      "v_mul_f32_e64 v157, v245, v245\n"
      "v_mfma_f32_16x16x32_bf16 v[146:149], a[76:79], a[236:239], v[146:149]\n"
      "s_mov_b32 s78, 147456\n"
      "v_mul_f32_e64 v115, v217, v217\n"
      "v_fma_f32 v156, v244, v244, s51\n"
      "v_mfma_f32_16x16x32_bf16 v[120:123], a[92:95], a[220:223], v[120:123]\n"
      "buffer_load_dwordx4 v99, s[20:23], s79 offen offset:1024  lds\n"
      "v_mfma_f32_16x16x32_bf16 v[162:165], a[20:23], a[228:231], v[162:165]\n"
      "s_waitcnt lgkmcnt(2)\n"
      "v_fma_f32 v114, v216, v216, s51\n"
      "s_add_u32 m0, s75, -768\n"
      "v_mfma_f32_16x16x32_bf16 v[166:169], a[44:47], a[216:219], v[166:169]\n"
      "ds_read_b32 v51, v51\n"
      "v_cndmask_b32 v26, v155, v107, s[82:83]\n"
      "v_mfma_f32_16x16x32_bf16 v[138:141], a[140:143], a[224:227], v[138:141]\n"
      "s_waitcnt lgkmcnt(2)\n"
      "v_cndmask_b32 v24, v155, v106, s[82:83]\n"
      "v_mul_f32_e64 v107, v171, v171\n"
      "v_mfma_f32_16x16x32_bf16 v[210:213], a[20:23], a[224:227], v[210:213]\n"
      "buffer_load_dwordx4 v99, s[20:23], s78 offen offset:768  lds\n"
      "v_mfma_f32_16x16x32_bf16 v[150:153], a[4:7], a[236:239], v[150:153]\n"
      "ds_read_b32 v59, v59\n"
      "v_fma_f32 v106, v170, v170, s51\n"
      "v_mfma_f32_16x16x32_bf16 v[150:153], a[8:11], a[240:243], v[150:153]\n"
      "s_lshl_b32 s92, s40, 6\n"
      "v_fma_f32 v104, v120, v120, s51\n"
      "v_mul_f32 v155, v221, v221\n"
      "v_mfma_f32_16x16x32_bf16 v[130:133], a[44:47], a[224:227], v[130:133]\n"
      "s_add_u32 m0, s75, 3200\n"
      "v_mul_f32 v101, v163, v163\n"
      "v_mul_f32 v105, v121, v121\n"
      "v_mfma_f32_16x16x32_bf16 v[190:193], a[116:119], a[220:223], v[190:193]\n"
      "buffer_load_dwordx4 v99, s[20:23], s78 offen offset:896  lds\n"
      "v_fma_f32 v116, v178, v178, s51\n"
      "v_mul_f32_e64 v117, v179, v179\n"
      "v_pk_fma_f32 v[106:107], v[172:173], v[172:173], v[106:107]\n"
      "v_pk_fma_f32 v[114:115], v[218:219], v[218:219], v[114:115]\n"
      "v_pk_fma_f32 v[156:157], v[246:247], v[246:247], v[156:157]\n"
      "v_pk_fma_f32 v[104:105], v[122:123], v[122:123], v[104:105]\n"
      "v_pk_fma_f32 v[156:157], v[166:167], v[166:167], v[156:157]\n"
      "v_mfma_f32_16x16x32_bf16 v[232:235], a[68:71], a[216:219], v[232:235]\n"
      "s_add_u32 m0, s75, 7168\n"
      "ds_read_b128 a[252:255], v102 offset:10240\n"
      "v_mfma_f32_16x16x32_bf16 v[224:227], a[116:119], a[216:219], v[224:227]\n"
      "buffer_load_dwordx4 v99, s[20:23], s78 offen offset:1024  lds\n"
      "v_mfma_f32_16x16x32_bf16 v[194:197], a[44:47], a[220:223], v[194:197]\n"
      "s_add_u32 s43, s43, 1\n"
      "v_fma_f32 v154, v220, v220, s51\n"
      "s_mov_b32 s78, 184320\n"
      "v_mfma_f32_16x16x32_bf16 v[240:243], a[112:115], a[248:251], v[240:243]\n"
      "s_add_u32 m0, s75, 11520\n"
      "v_fma_f32 v100, v162, v162, s51\n"
      "v_mul_f32 v231, v211, v211\n"
      "v_fma_f32 v230, v210, v210, s51\n"
      "v_mfma_f32_16x16x32_bf16 v[240:243], a[116:119], a[224:227], v[240:243]\n"
      "ds_read_b128 a[248:251], v119 offset:10240\n"
      "buffer_load_dwordx4 v99, s[20:23], s78 offen offset:768  lds\n"
      "v_pk_fma_f32 v[156:157], v[168:169], v[168:169], v[156:157]\n"
      "v_pk_fma_f32 v[114:115], v[224:225], v[224:225], v[114:115]\n"
      "s_add_u32 m0, s75, 15488\n"
      "v_pk_fma_f32 v[104:105], v[190:191], v[190:191], v[104:105]\n"
      "v_pk_fma_f32 v[114:115], v[226:227], v[226:227], v[114:115]\n"
      "v_pk_fma_f32 v[156:157], v[232:233], v[232:233], v[156:157]\n"
      "v_pk_fma_f32 v[100:101], v[164:165], v[164:165], v[100:101]\n"
      "v_pk_fma_f32 v[116:117], v[180:181], v[180:181], v[116:117]\n"
      "v_pk_fma_f32 v[154:155], v[222:223], v[222:223], v[154:155]\n"
      "v_pk_fma_f32 v[156:157], v[234:235], v[234:235], v[156:157]\n"
      "v_pk_fma_f32 v[230:231], v[212:213], v[212:213], v[230:231]\n"
      "v_pk_fma_f32 v[154:155], v[194:195], v[194:195], v[154:155]\n"
      "v_pk_fma_f32 v[106:107], v[240:241], v[240:241], v[106:107]\n"
      "v_mfma_f32_16x16x32_bf16 v[248:251], a[140:143], a[216:219], v[248:251]\n"
      "buffer_load_dwordx4 v99, s[20:23], s78 offen offset:896  lds\n"
      "v_mfma_f32_16x16x32_bf16 v[200:203], a[68:71], a[228:231], v[200:203]\n"
      "s_add_u32 m0, s75, 19456\n"
      "v_add_f32 v156, v156, v157\n"
      "s_waitcnt vmcnt(11)\n"
      "s_barrier\n"
      "v_mfma_f32_16x16x32_bf16 v[124:127], a[140:143], a[228:231], v[124:127]\n"
      "s_mov_b32 s79, 0\n"
      "ds_read_b64_tr_b16 a[146:147], v205 offset:512\n"
      "v_mfma_f32_16x16x32_bf16 v[182:185], a[52:55], a[236:239], v[182:185]\n"
      "ds_read_b64_tr_b16 a[196:197], v142 offset:12288\n"
      "ds_read_b64_tr_b16 a[172:173], v204 offset:12288\n"
      "v_mfma_f32_16x16x32_bf16 v[186:189], a[140:143], a[220:223], v[186:189]\n"
      "buffer_load_dwordx4 v99, s[20:23], s78 offen offset:1024  lds\n"
      "v_mfma_f32_16x16x32_bf16 v[252:255], a[116:119], a[228:231], v[252:255]\n"
      "ds_read_b64_tr_b16 a[144:145], v205\n"
      "ds_read_b64_tr_b16 a[168:169], v204\n"
      "v_mfma_f32_16x16x32_bf16 v[182:185], a[56:59], a[240:243], v[182:185]\n"
      "ds_read_b64_tr_b16 a[174:175], v204 offset:12800\n"
      "s_add_u32 m0, s75, 24576\n"
      "v_mfma_f32_16x16x32_bf16 v[158:161], a[68:71], a[220:223], v[158:161]\n"
      "ds_read_b64_tr_b16 a[170:171], v204 offset:512\n"
      "ds_read_b64_tr_b16 a[198:199], v142 offset:12800\n"
      "v_mfma_f32_16x16x32_bf16 v[134:137], a[44:47], a[228:231], v[134:137]\n"
      "ds_read_b64_tr_b16 a[150:151], v205 offset:12800\n"
      "ds_read_b64_tr_b16 a[194:195], v142 offset:512\n"
      "ds_read_b64_tr_b16 a[148:149], v205 offset:12288\n"
      "v_pk_fma_f32 v[106:107], v[242:243], v[242:243], v[106:107]\n"
      "v_pk_fma_f32 v[154:155], v[196:197], v[196:197], v[154:155]\n"
      "v_pk_fma_f32 v[106:107], v[138:139], v[138:139], v[106:107]\n"
      "v_pk_fma_f32 v[114:115], v[248:249], v[248:249], v[114:115]\n"
      "v_pk_fma_f32 v[230:231], v[130:131], v[130:131], v[230:231]\n"
      "v_mfma_f32_16x16x32_bf16 v[108:111], a[68:71], a[224:227], v[108:111]\n"
      "ds_read_b64_tr_b16 a[192:193], v142\n"
      "buffer_load_dwordx4 v209, s[36:39], s79 offen  lds\n"
      "v_pk_fma_f32 v[116:117], v[252:253], v[252:253], v[116:117]\n"
      "v_pk_fma_f32 v[230:231], v[132:133], v[132:133], v[230:231]\n"
      "v_pk_fma_f32 v[114:115], v[250:251], v[250:251], v[114:115]\n"
      "v_pk_fma_f32 v[106:107], v[140:141], v[140:141], v[106:107]\n"
      "v_add_f32 v114, v114, v115\n"
      "v_add_f32 v106, v106, v107\n"
      "v_pk_fma_f32 v[154:155], v[158:159], v[158:159], v[154:155]\n"
      "v_pk_fma_f32 v[104:105], v[192:193], v[192:193], v[104:105]\n"
      "v_pk_fma_f32 v[116:117], v[254:255], v[254:255], v[116:117]\n"
      "v_permlane16_swap_b32 v156, v114\n"
      "v_pk_fma_f32 v[230:231], v[108:109], v[108:109], v[230:231]\n"
      "v_add_f32 v156, v156, v114\n"
      "v_pk_fma_f32 v[116:117], v[124:125], v[124:125], v[116:117]\n"
      "v_mov_b32 v114, v156\n"
      "v_pk_fma_f32 v[230:231], v[110:111], v[110:111], v[230:231]\n"
      "v_pk_fma_f32 v[104:105], v[186:187], v[186:187], v[104:105]\n"
      "v_pk_fma_f32 v[154:155], v[160:161], v[160:161], v[154:155]\n"
      "v_add_f32 v230, v230, v231\n"
      "v_pk_fma_f32 v[100:101], v[134:135], v[134:135], v[100:101]\n"
      "v_pk_fma_f32 v[104:105], v[188:189], v[188:189], v[104:105]\n"
      "v_permlane16_swap_b32 v230, v106\n"
      "v_add_f32 v230, v230, v106\n"
      "v_mov_b32 v106, v230\n"
      "v_add_f32 v154, v154, v155\n"
      "v_mfma_f32_16x16x32_bf16 v[96:99], a[24:27], a[232:235], 0\n"
      "s_waitcnt lgkmcnt(0)\n"
      "v_add_f32 v104, v104, v105\n"
      "v_permlane32_swap_b32_e64 v156, v114\n"
      "v_mfma_f32_16x16x32_bf16 v[150:153], a[12:15], a[244:247], v[150:153]\n"
      "s_add_u32 m0, s75, 28544\n"
      "v_permlane16_swap_b32 v154, v104\n"
      "v_permlane32_swap_b32 v230, v106\n"
      "v_add_f32 v156, v156, v114\n"
      "v_pk_fma_f32 v[116:117], v[126:127], v[126:127], v[116:117]\n"
      "v_add_f32 v230, v230, v106\n"
      "v_add_f32 v116, v116, v117\n"
      "v_add_f32 v154, v154, v104\n"
      "v_mfma_f32_16x16x32_bf16 v[150:153], a[16:19], a[248:251], v[150:153]\n"
      "buffer_load_dwordx4 v209, s[36:39], s79 offen offset:128  lds\n"
      "v_rsq_f32_e64 v230, v230\n"
      "v_pk_fma_f32 v[100:101], v[136:137], v[136:137], v[100:101]\n"
      "v_mfma_f32_16x16x32_bf16 v[150:153], a[20:23], a[252:255], v[150:153]\n"
      "ds_read_b32 v62, v62\n"
      "v_mov_b32 v104, v154\n"
      "v_rsq_f32 v156, v156\n"
      "v_mov_b32 v106, v230\n"
      "v_mfma_f32_16x16x32_bf16 v[146:149], a[80:83], a[240:243], v[146:149]\n"
      "s_add_u32 m0, s75, 32512\n"
      "v_permlane32_swap_b32 v154, v104\n"
      "v_mfma_f32_16x16x32_bf16 v[146:149], a[84:87], a[244:247], v[146:149]\n"
      "buffer_load_dwordx4 v209, s[36:39], s79 offen offset:256  lds\n"
      "v_add_f32 v154, v154, v104\n"
      "v_permlane16_swap_b32 v230, v106\n"
      "v_mfma_f32_16x16x32_bf16 v[146:149], a[88:91], a[248:251], v[146:149]\n"
      "s_add_u32 m0, s75, 36864\n"
      "v_mov_b32 v114, v156\n"
      "v_mul_f32 v230, s50, v230\n"
      "v_mfma_f32_16x16x32_bf16 v[146:149], a[92:95], a[252:255], v[146:149]\n"
      "s_mov_b32 s79, 12288\n"
      "v_rsq_f32 v154, v154\n"
      "v_permlane16_swap_b32 v156, v114\n"
      "v_mov_b32_e64 v104, v154\n"
      "v_pk_mul_f32 v[210:211], v[210:211], v[230:231] op_sel_hi:[1,0]\n"
      "v_pk_fma_f32 v[100:101], v[200:201], v[200:201], v[100:101]\n"
      "v_pk_mul_f32 v[138:139], v[138:139], v[106:107] op_sel_hi:[1,0]\n"
      "v_pk_mul_f32 v[132:133], v[132:133], v[230:231] op_sel_hi:[1,0]\n"
      "v_pk_mul_f32 v[218:219], v[218:219], v[114:115] op_sel_hi:[1,0]\n"
      "v_mul_f32_e64 v156, s50, v156\n"
      "v_pk_mul_f32 v[216:217], v[216:217], v[114:115] op_sel_hi:[1,0]\n"
      "v_pk_fma_f32 v[100:101], v[202:203], v[202:203], v[100:101]\n"
      "v_pk_mul_f32 v[110:111], v[110:111], v[230:231] op_sel_hi:[1,0]\n"
      "v_mfma_f32_16x16x32_bf16 v[182:185], a[60:63], a[244:247], v[182:185]\n"
      "buffer_load_dwordx4 v209, s[36:39], s79 offen  lds\n"
      "v_add_f32 v100, v100, v101\n"
      "v_permlane16_swap_b32 v154, v104\n"
      "v_pk_mul_f32 v[226:227], v[226:227], v[114:115] op_sel_hi:[1,0]\n"
      "v_pk_mul_f32 v[232:233], v[232:233], v[156:157] op_sel_hi:[1,0]\n"
      "v_pk_mul_f32 v[248:249], v[248:249], v[114:115] op_sel_hi:[1,0]\n"
      "v_pk_mul_f32 v[246:247], v[246:247], v[156:157] op_sel_hi:[1,0]\n"
      "v_pk_mul_f32 v[190:191], v[190:191], v[104:105] op_sel_hi:[1,0]\n"
      "v_mfma_f32_16x16x32_bf16 v[96:99], a[28:31], a[236:239], v[96:99]\n"
      "s_add_u32 m0, s75, 40832\n"
      "v_permlane16_swap_b32 v100, v116\n"
      "v_add_f32 v100, v100, v116\n"
      "v_cvt_pk_bf16_f32 v216, v216, v217\n"
      "v_mov_b32 v116, v100\n"
      "v_mfma_f32_16x16x32_bf16 v[96:99], a[32:35], a[240:243], v[96:99]\n"
      "s_waitcnt vmcnt(10)\n"
      "s_barrier\n"
      "ds_read_b64_tr_b16 a[204:205], v142 offset:36864\n"
      "v_mfma_f32_16x16x32_bf16 v[96:99], a[36:39], a[244:247], v[96:99]\n"
      "buffer_load_dwordx4 v209, s[36:39], s79 offen offset:128  lds\n"
      "v_permlane32_swap_b32 v100, v116\n"
      "v_cvt_pk_bf16_f32 v217, v218, v219\n"
      "v_pk_mul_f32 v[250:251], v[250:251], v[114:115] op_sel_hi:[1,0]\n"
      "v_pk_mul_f32 v[170:171], v[170:171], v[106:107] op_sel_hi:[1,0]\n"
      "v_pk_mul_f32 v[168:169], v[168:169], v[156:157] op_sel_hi:[1,0]\n"
      "v_pk_mul_f32 v[224:225], v[224:225], v[114:115] op_sel_hi:[1,0]\n"
      "v_pk_mul_f32 v[140:141], v[140:141], v[106:107] op_sel_hi:[1,0]\n"
      "v_add_f32_e64 v100, v100, v116\n"
      "v_cvt_pk_bf16_f32 v219, v226, v227\n"
      "v_cvt_pk_bf16_f32 v218, v224, v225\n"
      "v_pk_mul_f32 v[244:245], v[244:245], v[156:157] op_sel_hi:[1,0]\n"
      "v_pk_mul_f32 v[242:243], v[242:243], v[106:107] op_sel_hi:[1,0]\n"
      "v_mfma_f32_16x16x32_bf16 v[112:115], a[120:123], a[232:235], 0\n"
      "ds_read_b64_tr_b16 a[176:177], v204 offset:24576\n"
      "ds_read_b64_tr_b16 a[182:183], v204 offset:37376\n"
      "ds_read_b64_tr_b16 a[154:155], v205 offset:25088\n"
      "v_pk_mul_f32 v[192:193], v[192:193], v[104:105] op_sel_hi:[1,0]\n"
      "v_mul_f32_e64 v143, v147, v147\n"
      "v_cvt_pk_bf16_f32 v248, v248, v249\n"
      "v_pk_mul_f32 v[130:131], v[130:131], v[230:231] op_sel_hi:[1,0]\n"
      "v_mfma_f32_16x16x32_bf16 v[96:99], a[40:43], a[248:251], v[96:99]\n"
      "ds_read_b64_tr_b16 a[152:153], v205 offset:24576\n"
      "ds_read_b64_tr_b16 a[202:203], v142 offset:25088\n"
      "v_cvt_pk_bf16_f32 v249, v250, v251\n"
      "v_mfma_f32_16x16x32_bf16 v[174:177], a[96:99], a[232:235], 0\n"
      "ds_read_b64_tr_b16 a[200:201], v142 offset:24576\n"
      "v_rsq_f32_e64 v100, v100\n"
      "v_mfma_f32_16x16x32_bf16 v[174:177], a[100:103], a[236:239], v[174:177]\n"
      "ds_read_b64_tr_b16 a[206:207], v142 offset:37376\n"
      "ds_read_b64_tr_b16 a[178:179], v204 offset:25088\n"
      "v_mfma_f32_16x16x32_bf16 v[174:177], a[104:107], a[240:243], v[174:177]\n"
      "ds_read_b64_tr_b16 a[158:159], v205 offset:37376\n"
      "v_mul_f32_e64 v154, s50, v154\n"
      "v_mfma_f32_16x16x32_bf16 v[112:115], a[124:127], a[236:239], v[112:115]\n"
      "ds_read_b64_tr_b16 a[156:157], v205 offset:36864\n"
      "s_add_u32 m0, s75, 44800\n"
      "v_mfma_f32_16x16x32_bf16 v[112:115], a[128:131], a[240:243], v[112:115]\n"
      "ds_read_b64_tr_b16 a[180:181], v204 offset:36864\n"
      "v_mul_f32 v205, v151, v151\n"
      "v_mov_b32 v116, v100\n"
      "v_cvt_pk_bf16_f32 v244, v244, v245\n"
      "v_mfma_f32_16x16x32_bf16 v[174:177], a[108:111], a[244:247], v[174:177]\n"
      "s_mov_b32 s78, 24576\n"
      "ds_write_b128 v207, v[216:219] offset:32\n"
      "v_mfma_f32_16x16x32_bf16 v[174:177], a[112:115], a[248:251], v[174:177]\n"
      "ds_read_b128 v[216:219], v102\n"
      "v_cvt_pk_bf16_f32 v245, v246, v247\n"
      "v_mfma_f32_16x16x32_bf16 v[174:177], a[116:119], a[252:255], v[174:177]\n"
      "buffer_load_dwordx4 v209, s[36:39], s79 offen offset:256  lds\n"
      "v_fma_f32 v204, v150, v150, s51\n"
      "v_mfma_f32_16x16x32_bf16 v[96:99], a[44:47], a[252:255], v[96:99]\n"
      "ds_write_b64 v207, v[248:249] offset:24\n"
      "v_permlane16_swap_b32_e64 v100, v116\n"
      "v_cvt_pk_bf16_f32 v232, v232, v233\n"
      "v_pk_mul_f32 v[234:235], v[234:235], v[156:157] op_sel_hi:[1,0]\n"
      "v_pk_mul_f32 v[186:187], v[186:187], v[104:105] op_sel_hi:[1,0]\n"
      "v_mul_f32_e64 v100, s50, v100\n"
      "v_pk_fma_f32 v[204:205], v[152:153], v[152:153], v[204:205]\n"
      "v_pk_mul_f32 v[188:189], v[188:189], v[104:105] op_sel_hi:[1,0]\n"
      "v_pk_mul_f32 v[220:221], v[220:221], v[154:155] op_sel_hi:[1,0]\n"
      "v_pk_mul_f32 v[166:167], v[166:167], v[156:157] op_sel_hi:[1,0]\n"
      "v_cvt_pk_bf16_f32 v247, v168, v169\n"
      "v_pk_mul_f32 v[178:179], v[178:179], v[116:117] op_sel_hi:[1,0]\n"
      "v_mfma_f32_16x16x32_bf16 v[112:115], a[132:135], a[244:247], v[112:115]\n"
      "s_add_u32 m0, s75, 49152\n"
      "ds_read_b128 v[248:251], v102 offset:4096\n"
      "buffer_load_dwordx4 v209, s[36:39], s78 offen  lds\n"
      "v_pk_mul_f32 v[196:197], v[196:197], v[154:155] op_sel_hi:[1,0]\n"
      "v_fma_f32 v142, v146, v146, s51\n"
      "v_pk_mul_f32 v[194:195], v[194:195], v[154:155] op_sel_hi:[1,0]\n"
      "v_pk_fma_f32 v[142:143], v[148:149], v[148:149], v[142:143]\n"
      "v_pk_fma_f32 v[204:205], v[96:97], v[96:97], v[204:205]\n"
      "v_pk_mul_f32 v[120:121], v[120:121], v[104:105] op_sel_hi:[1,0]\n"
      "v_pk_mul_f32 v[254:255], v[254:255], v[116:117] op_sel_hi:[1,0]\n"
      "v_cvt_pk_bf16_f32 v233, v234, v235\n"
      "v_pk_mul_f32 v[108:109], v[108:109], v[230:231] op_sel_hi:[1,0]\n"
      "v_pk_mul_f32 v[240:241], v[240:241], v[106:107] op_sel_hi:[1,0]\n"
      "v_pk_mul_f32 v[172:173], v[172:173], v[106:107] op_sel_hi:[1,0]\n"
      "v_cvt_pk_bf16_f32 v246, v166, v167\n"
      "v_pk_mul_f32 v[160:161], v[160:161], v[154:155] op_sel_hi:[1,0]\n"
      "v_pk_mul_f32 v[124:125], v[124:125], v[116:117] op_sel_hi:[1,0]\n"
      "v_pk_mul_f32 v[222:223], v[222:223], v[154:155] op_sel_hi:[1,0]\n"
      "s_add_u32 m0, s75, 53120\n"
      "v_mfma_f32_16x16x32_bf16 v[112:115], a[136:139], a[248:251], v[112:115]\n"
      "ds_write_b128 v207, v[244:247]\n"
      "v_pk_fma_f32 v[142:143], v[174:175], v[174:175], v[142:143]\n"
      "v_cvt_pk_bf16_f32 v170, v170, v171\n"
      "v_cvt_pk_bf16_f32 v171, v172, v173\n"
      "v_cvt_pk_bf16_f32 v178, v178, v179\n"
      "v_mfma_f32_16x16x32_bf16 v[112:115], a[140:143], a[252:255], v[112:115]\n"
      "ds_read_b128 v[244:247], v119 offset:4096\n"
      "v_cvt_pk_bf16_f32 v108, v108, v109\n"
      "v_mfma_f32_16x16x32_bf16 v[182:185], a[64:67], a[248:251], v[182:185]\n"
      "ds_read_b128 v[166:169], v119\n"
      "buffer_load_dwordx4 v209, s[36:39], s78 offen offset:128  lds\n"
      "v_pk_mul_f32 v[162:163], v[162:163], v[100:101] op_sel_hi:[1,0]\n"
      "v_pk_mul_f32 v[212:213], v[212:213], v[230:231] op_sel_hi:[1,0]\n"
      "v_cvt_pk_bf16_f32 v210, v210, v211\n"
      "v_cvt_pk_bf16_f32 v173, v242, v243\n"
      "s_add_u32 m0, s75, 57088\n"
      "v_pk_mul_f32 v[180:181], v[180:181], v[116:117] op_sel_hi:[1,0]\n"
      "v_pk_fma_f32 v[204:205], v[98:99], v[98:99], v[204:205]\n"
      "v_pk_mul_f32 v[252:253], v[252:253], v[116:117] op_sel_hi:[1,0]\n"
      "v_pk_mul_f32 v[122:123], v[122:123], v[104:105] op_sel_hi:[1,0]\n"
      "v_pk_mul_f32 v[158:159], v[158:159], v[154:155] op_sel_hi:[1,0]\n"
      "v_pk_mul_f32 v[202:203], v[202:203], v[100:101] op_sel_hi:[1,0]\n"
      "v_cvt_pk_bf16_f32 v211, v212, v213\n"
      "v_cvt_pk_bf16_f32 v220, v220, v221\n"
      "v_cvt_pk_bf16_f32 v221, v222, v223\n"
      "v_mfma_f32_16x16x32_bf16 v[182:185], a[68:71], a[252:255], v[182:185]\n"
      "buffer_load_dwordx4 v209, s[36:39], s78 offen offset:256  lds\n"
      "s_mov_b32 s78, 36864\n"
      "v_cvt_pk_bf16_f32 v222, v194, v195\n"
      "s_waitcnt lgkmcnt(0)\n"
      "s_waitcnt vmcnt(9)\n"
      "s_barrier\n"
      "v_pk_mul_f32 v[200:201], v[200:201], v[100:101] op_sel_hi:[1,0]\n"
      "v_cvt_pk_bf16_f32 v223, v196, v197\n"
      "v_pk_fma_f32 v[142:143], v[176:177], v[176:177], v[142:143]\n"
      "s_add_u32 m0, s75, 61440\n"
      "ds_read_b64_tr_b16 a[188:189], v237 offset:12288\n"
      "v_pk_mul_f32 v[136:137], v[136:137], v[100:101] op_sel_hi:[1,0]\n"
      "v_mfma_f32_16x16x32_bf16 v[194:197], a[168:171], v[166:169], 0\n"
      "ds_read_b64_tr_b16 a[212:213], v238 offset:12288\n"
      "ds_read_b64_tr_b16 a[190:191], v237 offset:12800\n"
      "v_mfma_f32_16x16x32_bf16 v[194:197], a[172:175], v[216:219], v[194:197]\n"
      "ds_read_b64_tr_b16 a[186:187], v237 offset:512\n"
      "buffer_load_dwordx4 v209, s[36:39], s78 offen  lds\n"
      "v_mfma_f32_16x16x32_bf16 v[194:197], a[176:179], v[244:247], v[194:197]\n"
      "ds_read_b128 v[104:107], v119 offset:8192\n"
      "v_cvt_pk_bf16_f32 v213, v132, v133\n"
      "v_mfma_f32_16x16x32_bf16 v[194:197], a[180:183], v[248:251], v[194:197]\n"
      "ds_read_b64_tr_b16 a[160:161], v236\n"
      "ds_read_b64_tr_b16 a[214:215], v238 offset:12800\n"
      "v_mfma_f32_16x16x32_bf16 v[224:227], a[192:195], v[166:169], 0\n"
      "ds_read_b64_tr_b16 a[184:185], v237\n"
      "s_add_u32 m0, s75, 65408\n"
      "v_mfma_f32_16x16x32_bf16 v[224:227], a[196:199], v[216:219], v[224:227]\n"
      "ds_read_b64_tr_b16 a[210:211], v238 offset:512\n"
      "v_cvt_pk_bf16_f32 v212, v130, v131\n"
      "v_mfma_f32_16x16x32_bf16 v[224:227], a[200:203], v[244:247], v[224:227]\n"
      "ds_read_b64_tr_b16 a[164:165], v236 offset:12288\n"
      "buffer_load_dwordx4 v209, s[36:39], s78 offen offset:128  lds\n"
      "v_cvt_pk_bf16_f32 v138, v138, v139\n"
      "s_mov_b32 s79, 49152\n"
      "v_pk_fma_f32 v[204:205], v[182:183], v[182:183], v[204:205]\n"
      "v_pk_mul_f32 v[126:127], v[126:127], v[116:117] op_sel_hi:[1,0]\n"
      "v_mfma_f32_16x16x32_bf16 v[224:227], a[204:207], v[248:251], v[224:227]\n"
      "ds_read_b64_tr_b16 a[166:167], v236 offset:12800\n"
      "ds_read_b64_tr_b16 a[208:209], v238\n"
      "v_mfma_f32_16x16x32_bf16 v[154:157], a[144:147], v[166:169], 0\n"
      "ds_read_b128 v[130:133], v102 offset:8192\n"
      "v_cvt_pk_bf16_f32 v186, v186, v187\n"
      "s_add_u32 m0, s75, 69376\n"
      "ds_read_b64_tr_b16 a[162:163], v236 offset:512\n"
      "v_mfma_f32_16x16x32_bf16 v[154:157], a[148:151], v[216:219], v[154:157]\n"
      "buffer_load_dwordx4 v209, s[36:39], s78 offen offset:256  lds\n"
      "ds_read_b128 v[234:237], v102 offset:2048\n"
      "v_pk_mul_f32 v[164:165], v[164:165], v[100:101] op_sel_hi:[1,0]\n"
      "v_pk_fma_f32 v[204:205], v[184:185], v[184:185], v[204:205]\n"
      "v_pk_fma_f32 v[142:143], v[112:113], v[112:113], v[142:143]\n"
      "v_mfma_f32_16x16x32_bf16 v[154:157], a[152:155], v[244:247], v[154:157]\n"
      "ds_read_b128 v[242:245], v119 offset:2048\n"
      "v_cvt_pk_bf16_f32 v179, v180, v181\n"
      "v_cvt_pk_bf16_f32 v120, v120, v121\n"
      "v_mfma_f32_16x16x32_bf16 v[154:157], a[156:159], v[248:251], v[154:157]\n"
      "s_add_u32 m0, s75, 73728\n"
      "ds_read_b128 v[246:249], v119 offset:10240\n"
      "buffer_load_dwordx4 v209, s[36:39], s79 offen  lds\n"
      "v_pk_fma_f32 v[142:143], v[114:115], v[114:115], v[142:143]\n"
      "s_waitcnt lgkmcnt(3)\n"
      "v_mfma_f32_16x16x32_bf16 v[154:157], a[160:163], v[104:107], v[154:157]\n"
      "ds_write_b64 v207, v[232:233] offset:16\n"
      "v_mfma_f32_16x16x32_bf16 v[194:197], a[184:187], v[104:107], v[194:197]\n"
      "s_add_u32 m0, s75, 77696\n"
      "ds_read_b128 v[230:233], v119 offset:6144\n"
      "v_mfma_f32_16x16x32_bf16 v[194:197], a[188:191], v[130:133], v[194:197]\n"
      "buffer_load_dwordx4 v209, s[36:39], s79 offen offset:128  lds\n"
      "v_cvt_pk_bf16_f32 v124, v124, v125\n"
      "v_cvt_pk_bf16_f32 v181, v254, v255\n"
      "v_add_f32 v204, v204, v205\n"
      "v_cvt_pk_bf16_f32 v162, v162, v163\n"
      "v_cvt_pk_bf16_f32 v172, v240, v241\n"
      "v_add_f32 v142, v142, v143\n"
      "v_mfma_f32_16x16x32_bf16 v[224:227], a[208:211], v[104:107], v[224:227]\n"
      "s_add_u32 m0, s75, 81664\n"
      "ds_read_b128 v[166:169], v102 offset:6144\n"
      "v_mfma_f32_16x16x32_bf16 v[224:227], a[212:215], v[130:133], v[224:227]\n"
      "ds_read_b128 v[104:107], v0 offset:10240\n"
      "v_cvt_pk_bf16_f32 v109, v110, v111\n"
      "v_cvt_pk_bf16_f32 v125, v126, v127\n"
      "v_cvt_pk_bf16_f32 v194, v194, v195\n"
      "v_mfma_f32_16x16x32_bf16 v[154:157], a[164:167], v[130:133], v[154:157]\n"
      "buffer_load_dwordx4 v209, s[36:39], s79 offen offset:256  lds\n"
      "s_waitcnt lgkmcnt(2)\n"
      "v_mfma_f32_16x16x32_bf16 v[126:129], a[144:147], v[242:245], 0\n"
      "ds_read_b128 v[116:119], v0 offset:4096\n"
      "v_cvt_pk_bf16_f32 v187, v188, v189\n"
      "v_cvt_pk_bf16_f32 v195, v196, v197\n"
      "v_mfma_f32_16x16x32_bf16 v[196:199], a[192:195], v[242:245], 0\n"
      "s_mov_b32 s79, 61440\n"
      "v_permlane16_swap_b32 v204, v142\n"
      "v_add_f32_e64 v204, v204, v142\n"
      "s_add_u32 m0, s75, 86016\n"
      "v_mfma_f32_16x16x32_bf16 v[196:199], a[196:199], v[234:237], v[196:199]\n"
      "buffer_load_dwordx4 v209, s[36:39], s79 offen  lds\n"
      "v_mfma_f32_16x16x32_bf16 v[214:217], a[168:171], v[242:245], 0\n"
      "ds_read_b128 v[242:245], v102 offset:10240\n"
      "v_mov_b32_e64 v142, v204\n"
      "v_cvt_pk_bf16_f32 v180, v252, v253\n"
      "v_mfma_f32_16x16x32_bf16 v[126:129], a[148:151], v[234:237], v[126:129]\n"
      "s_add_u32 m0, s75, 89984\n"
      "v_permlane32_swap_b32_e64 v204, v142\n"
      "v_mfma_f32_16x16x32_bf16 v[126:129], a[152:155], v[230:233], v[126:129]\n"
      "ds_write_b64 v208, v[194:195] offset:32\n"
      "v_add_f32 v204, v204, v142\n"
      "v_xor_b32 v110, 64, v8\n"
      "v_cvt_pk_bf16_f32 v121, v122, v123\n"
      "v_mfma_f32_16x16x32_bf16 v[196:199], a[200:203], v[230:233], v[196:199]\n"
      "buffer_load_dwordx4 v209, s[36:39], s79 offen offset:128  lds\n"
      "v_mfma_f32_16x16x32_bf16 v[214:217], a[172:175], v[234:237], v[214:217]\n"
      "ds_read_b128 v[130:133], v1 offset:4096\n"
      "v_rsq_f32_e64 v204, v204\n"
      "v_mfma_f32_16x16x32_bf16 v[214:217], a[176:179], v[230:233], v[214:217]\n"
      "s_waitcnt lgkmcnt(2)\n"
      "v_add_u32 v111, 49152, v110\n"
      "v_mov_b32 v142, v204\n"
      "v_mfma_f32_16x16x32_bf16 v[214:217], a[180:183], v[166:169], v[214:217]\n"
      "s_add_u32 m0, s75, 93952\n"
      "v_permlane16_swap_b32_e64 v204, v142\n"
      "v_mfma_f32_16x16x32_bf16 v[126:129], a[156:159], v[166:169], v[126:129]\n"
      "buffer_load_dwordx4 v209, s[36:39], s79 offen offset:256  lds\n"
      "s_waitcnt vmcnt(12)\n"
      "s_barrier\n"
      "ds_read_b64_tr_b16 a[222:223], v239 offset:38912\n"
      "v_pk_mul_f32 v[134:135], v[134:135], v[100:101] op_sel_hi:[1,0]\n"
      "v_mfma_f32_16x16x32_bf16 v[126:129], a[160:163], v[246:249], v[126:129]\n"
      "ds_read_b64_tr_b16 v[232:233], v206 offset:24576\n"
      "ds_read_b64_tr_b16 a[216:217], v239 offset:24576\n"
      "ds_read_b64_tr_b16 a[242:243], v239 offset:26632\n"
      "v_mfma_f32_16x16x32_bf16 v[126:129], a[164:167], v[242:245], v[126:129]\n"
      "buffer_load_dwordx4 v[100:103], v36, s[28:31], s49 offen\n"
      "v_mfma_f32_16x16x32_bf16 v[196:199], a[204:207], v[166:169], v[196:199]\n"
      "ds_read_b128 v[250:253], v1 offset:10240\n"
      "v_cvt_pk_bf16_f32 v158, v158, v159\n"
      "v_mfma_f32_16x16x32_bf16 v[196:199], a[208:211], v[246:249], v[196:199]\n"
      "ds_read_b64_tr_b16 a[246:247], v239 offset:38920\n"
      "ds_read_b64_tr_b16 a[240:241], v239 offset:24584\n"
      "v_mul_f32 v204, s50, v204\n"
      "v_mfma_f32_16x16x32_bf16 v[214:217], a[184:187], v[246:249], v[214:217]\n"
      "ds_read_b64_tr_b16 v[236:237], v206 offset:36864\n"
      "ds_read_b64_tr_b16 a[244:245], v239 offset:36872\n"
      "v_mfma_f32_16x16x32_bf16 v[214:217], a[188:191], v[242:245], v[214:217]\n"
      "ds_read_b64_tr_b16 a[220:221], v239 offset:36864\n"
      "buffer_load_dword v255, v29, s[28:31], s49 offen\n"
      "v_pk_mul_f32 v[182:183], v[182:183], v[204:205] op_sel_hi:[1,0]\n"
      "v_pk_mul_f32 v[96:97], v[96:97], v[204:205] op_sel_hi:[1,0]\n"
      "v_cvt_pk_bf16_f32 v122, v190, v191\n"
      "v_mfma_f32_16x16x32_bf16 v[196:199], a[212:215], v[242:245], v[196:199]\n"
      "ds_read_b64_tr_b16 a[218:219], v239 offset:26624\n"
      "v_cvt_pk_bf16_f32 v163, v164, v165\n"
      "v_mfma_f32_16x16x32_bf16 v[188:191], v[116:119], a[168:171], 0\n"
      "ds_read_b64_tr_b16 v[238:239], v206 offset:38912\n"
      "s_waitcnt lgkmcnt(5)\n"
      "v_mfma_f32_16x16x32_bf16 v[188:191], v[130:133], a[172:175], v[188:191]\n"
      "buffer_load_dwordx4 v[166:169], v36, s[28:31], s74 offen\n"
      "ds_read_b64_tr_b16 v[234:235], v206 offset:26624\n"
      "v_cvt_pk_bf16_f32 v200, v200, v201\n"
      "v_cvt_pk_bf16_f32 v214, v214, v215\n"
      "v_cvt_pk_bf16_f32 v164, v134, v135\n"
      "v_mov_b32 v143, 0xbc80bc80\n"
      "v_pk_mul_f32 v[148:149], v[148:149], v[142:143] op_sel_hi:[1,0]\n"
      "v_cvt_pk_bf16_f32 v201, v202, v203\n"
      "v_mov_b32 v144, 0xbc80bc80\n"
      "v_pk_mul_f32 v[146:147], v[146:147], v[142:143] op_sel_hi:[1,0]\n"
      "v_mov_b32 v145, 0xbc80bc80\n"
      "v_cvt_pk_bf16_f32 v126, v126, v127\n"
      "v_cvt_pk_bf16_f32 v127, v128, v129\n"
      "v_pk_mul_f32 v[150:151], v[150:151], v[204:205] op_sel_hi:[1,0]\n"
      "v_pk_mul_f32 v[152:153], v[152:153], v[204:205] op_sel_hi:[1,0]\n"
      "v_cvt_pk_bf16_f32 v123, v192, v193\n"
      "v_cvt_pk_bf16_f32 v182, v182, v183\n"
      "v_cvt_pk_bf16_f32 v159, v160, v161\n"
      "v_mfma_f32_16x16x32_bf16 v[192:195], v[116:119], a[144:147], 0\n"
      "buffer_load_dword v254, v29, s[28:31], s74 offen\n"
      "v_cvt_pk_bf16_f32 v215, v216, v217\n"
      "s_add_u32 m0, s64, 18432\n"
      "v_pk_mul_f32 v[176:177], v[176:177], v[142:143] op_sel_hi:[1,0]\n"
      "v_pk_mul_f32 v[114:115], v[114:115], v[142:143] op_sel_hi:[1,0]\n"
      "v_pk_mul_f32 v[112:113], v[112:113], v[142:143] op_sel_hi:[1,0]\n"
      "v_cvt_pk_bf16_f32 v165, v136, v137\n"
      "v_pk_mul_f32 v[174:175], v[174:175], v[142:143] op_sel_hi:[1,0]\n"
      "v_cvt_pk_bf16_f32 v196, v196, v197\n"
      "v_pk_mul_f32 v[184:185], v[184:185], v[204:205] op_sel_hi:[1,0]\n"
      "v_cvt_pk_bf16_f32 v183, v184, v185\n"
      "v_pk_mul_f32 v[98:99], v[98:99], v[204:205] op_sel_hi:[1,0]\n"
      "v_cvt_pk_bf16_f32 v224, v224, v225\n"
      "v_cvt_pk_bf16_f32 v197, v198, v199\n"
      "v_cvt_pk_bf16_f32 v112, v112, v113\n"
      "v_mfma_f32_16x16x32_bf16 v[192:195], v[130:133], a[148:151], v[192:195]\n"
      "buffer_load_dwordx4 v16, s[16:19], s45 offen  lds\n"
      "v_subrev_u32 v39, 115456, v9\n"
      "v_cvt_pk_bf16_f32 v146, v146, v147\n"
      "v_mfma_f32_16x16x32_bf16 v[192:195], v[104:107], a[152:155], v[192:195]\n"
      "s_add_u32 m0, s64, 22528\n"
      "v_cvt_pk_bf16_f32 v113, v114, v115\n"
      "v_cvt_pk_bf16_f32 v150, v150, v151\n"
      "v_mfma_f32_16x16x32_bf16 v[192:195], v[250:253], a[156:159], v[192:195]\n"
      "ds_read_b128 v[202:205], v8 offset:24576\n"
      "ds_read_b128 v[240:243], v110 offset:24576\n"
      "v_mfma_f32_16x16x32_bf16 v[188:191], v[104:107], a[176:179], v[188:191]\n"
      "buffer_load_dwordx4 v17, s[16:19], s45 offen  lds\n"
      "v_cvt_pk_bf16_f32 v139, v140, v141\n"
      "v_mfma_f32_16x16x32_bf16 v[134:137], v[116:119], a[192:195], 0\n"
      "s_add_u32 m0, s64, 26624\n"
      "ds_read_b128 v[246:249], v8 offset:28672\n"
      "v_cvt_pk_bf16_f32 v147, v148, v149\n"
      "v_mfma_f32_16x16x32_bf16 v[134:137], v[130:133], a[196:199], v[134:137]\n"
      "ds_read_b128 v[130:133], v110 offset:28672\n"
      "v_lshrrev_b32_e64 v39, 4, v39\n"
      "v_mfma_f32_16x16x32_bf16 v[134:137], v[104:107], a[200:203], v[134:137]\n"
      "ds_read_b128 v[228:231], v0 offset:2048\n"
      "v_cvt_pk_bf16_f32 v151, v152, v153\n"
      "v_mfma_f32_16x16x32_bf16 v[134:137], v[250:253], a[204:207], v[134:137]\n"
      "ds_read_b128 v[104:107], v1 offset:2048\n"
      "buffer_load_dwordx4 v18, s[16:19], s45 offen  lds\n"
      "v_mfma_f32_16x16x32_bf16 v[188:191], v[250:253], a[180:183], v[188:191]\n"
      "s_mov_b32 exec_lo, 0x1f001f\n"
      "s_mov_b32 exec_hi, 0x1f001f\n"
      "ds_write_b64 v208, v[126:127] offset:18688\n"
      "ds_write_b64 v208, v[214:215] offset:18720\n"
      "ds_write_b64 v208, v[196:197] offset:18752\n"
      "s_mov_b64 exec, -1\n"
      "s_add_u32 m0, s64, 30720\n"
      "s_waitcnt lgkmcnt(3)\n"
      "v_mfma_f32_16x16x32_bf16 v[214:217], v[228:231], a[168:171], 0\n"
      "ds_read_b128 v[126:129], v110 offset:32768\n"
      "v_cvt_pk_bf16_f32 v153, v98, v99\n"
      "v_mov_b32 v142, 0xbc80bc80\n"
      "v_cvt_pk_bf16_f32 v148, v174, v175\n"
      "v_mfma_f32_16x16x32_bf16 v[214:217], v[104:107], a[172:175], v[214:217]\n"
      "ds_read_b128 v[114:117], v0 offset:16384\n"
      "v_cvt_pk_bf16_f32 v149, v176, v177\n"
      "v_mfma_f32_16x16x32_bf16 v[174:177], v[202:205], v[142:145], 0\n"
      "buffer_load_dwordx4 v19, s[16:19], s45 offen  lds\n"
      "v_mfma_f32_16x16x32_bf16 v[174:177], v[240:243], v[142:145], v[174:177]\n"
      "s_waitcnt vmcnt(14)\n"
      "s_barrier\n"
      "s_add_u32 m0, s65, 34816\n"
      "v_cvt_pk_bf16_f32 v152, v96, v97\n"
      "v_mfma_f32_16x16x32_bf16 v[174:177], v[246:249], v[142:145], v[174:177]\n"
      "ds_read_b128 v[96:99], v8 offset:32768\n"
      "ds_read_b128 v[250:253], v0 offset:8192\n"
      "v_mfma_f32_16x16x32_bf16 v[174:177], v[130:133], v[142:145], v[174:177]\n"
      "ds_read_b64_tr_b16 a[228:229], v89 offset:12288\n"
      "v_cvt_pk_bf16_f32 v154, v154, v155\n"
      "v_mfma_f32_16x16x32_bf16 v[130:133], v[228:231], a[192:195], 0\n"
      "buffer_load_dwordx4 v15, s[16:19], s45 offen  lds\n"
      "ds_read_b64_tr_b16 a[230:231], v89 offset:14336\n"
      "ds_read_b64_tr_b16 v[242:243], v78 offset:2048\n"
      "v_mfma_f32_16x16x32_bf16 v[196:199], v[228:231], a[144:147], 0\n"
      "ds_read_b64_tr_b16 a[250:251], v89 offset:2056\n"
      "v_cvt_pk_bf16_f32 v155, v156, v157\n"
      "v_mfma_f32_16x16x32_bf16 v[196:199], v[104:107], a[148:151], v[196:199]\n"
      "ds_read_b64_tr_b16 a[254:255], v89 offset:14344\n"
      "ds_read_b64_tr_b16 a[252:253], v89 offset:12296\n"
      "v_mfma_f32_16x16x32_bf16 v[130:133], v[104:107], a[196:199], v[130:133]\n"
      "ds_read_b64_tr_b16 v[246:247], v78 offset:14336\n"
      "ds_read_b64_tr_b16 a[224:225], v89\n"
      "s_waitcnt lgkmcnt(7)\n"
      "v_mfma_f32_16x16x32_bf16 v[130:133], v[250:253], a[200:203], v[130:133]\n"
      "ds_read_b128 v[228:231], v1 offset:8192\n"
      "ds_read_b64_tr_b16 v[240:241], v78\n"
      "v_mfma_f32_16x16x32_bf16 v[174:177], v[96:99], v[142:145], v[174:177]\n"
      "ds_read_b64_tr_b16 v[244:245], v78 offset:12288\n"
      "ds_read_b64_tr_b16 a[226:227], v89 offset:2048\n"
      "v_mfma_f32_16x16x32_bf16 v[174:177], v[126:129], v[142:145], v[174:177]\n"
      "ds_read_b64_tr_b16 a[248:249], v89 offset:8\n"
      "v_cvt_pk_bf16_f32 v225, v226, v227\n"
      "v_mfma_f32_16x16x32_bf16 v[214:217], v[250:253], a[176:179], v[214:217]\n"
      "ds_read_b128 v[126:129], v1 offset:16384\n"
      "v_and_b32 v39, 48, v39\n"
      "v_mfma_f32_16x16x32_bf16 v[188:191], v[114:117], a[184:187], v[188:191]\n"
      "ds_read_b128 v[96:99], v0 offset:14336\n"
      "ds_read_b128 v[104:107], v1 offset:14336\n"
      "v_mfma_f32_16x16x32_bf16 v[192:195], v[114:117], a[160:163], v[192:195]\n"
      "s_mov_b32 exec_lo, 0x1f001f\n"
      "s_mov_b32 exec_hi, 0x1f001f\n"
      "ds_write_b128 v207, v[150:153] offset:18688\n"
      "ds_write_b64 v207, v[182:183] offset:18704\n"
      "ds_write_b64 v207, v[112:113] offset:18712\n"
      "ds_write_b128 v207, v[146:149] offset:18720\n"
      "s_mov_b64 exec, -1\n"
      "v_mfma_f32_16x16x32_bf16 v[134:137], v[114:117], a[208:211], v[134:137]\n"
      "ds_read_b128 v[202:205], v111\n"
      "v_mul_f32 v177, 0x3eaaaaab, v177\n"
      "v_mfma_f32_16x16x32_bf16 v[196:199], v[250:253], a[152:155], v[196:199]\n"
      "s_waitcnt lgkmcnt(5)\n"
      "v_add_u32 v156, 49152, v8\n"
      "v_mul_f32 v176, 0x3eaaaaab, v176\n"
      "v_mfma_f32_16x16x32_bf16 v[196:199], v[228:231], a[156:159], v[196:199]\n"
      "ds_read_b128 v[250:253], v156 offset:4096\n"
      "v_add_u32 v39, 123136, v39\n"
      "v_mfma_f32_16x16x32_bf16 v[214:217], v[228:231], a[180:183], v[214:217]\n"
      "buffer_load_dwordx4 v[116:119], v36, s[28:31], s47 offen\n"
      "v_mfma_f32_16x16x32_bf16 v[192:195], v[126:129], a[164:167], v[192:195]\n"
      "ds_write_b64 v208, v[154:155]\n"
      "v_mfma_f32_16x16x32_bf16 v[130:133], v[228:231], a[204:207], v[130:133]\n"
      "ds_read_b128 v[228:231], v156\n"
      "v_add_u32_e64 v218, s92, v39\n"
      "v_mfma_f32_16x16x32_bf16 v[130:133], v[96:99], a[208:211], v[130:133]\n"
      "ds_read_b128 v[112:115], v156 offset:8192\n"
      "v_mul_f32 v175, 0x3eaaaaab, v175\n"
      "v_mul_f32 v174, 0x3eaaaaab, v174\n"
      "v_mfma_f32_16x16x32_bf16 v[130:133], v[104:107], a[212:215], v[130:133]\n"
      "ds_read_b128 v[152:155], v111 offset:8192\n"
      "v_cvt_pk_bf16_f32 v76, v192, v193\n"
      "v_mfma_f32_16x16x32_bf16 v[188:191], v[126:129], a[188:191], v[188:191]\n"
      "ds_write_b64 v208, v[224:225] offset:64\n"
      "v_mfma_f32_16x16x32_bf16 v[134:137], v[126:129], a[212:215], v[134:137]\n"
      "ds_read_b128 v[126:129], v111 offset:4096\n"
      "v_cvt_pk_bf16_f32 v77, v194, v195\n"
      "v_mfma_f32_16x16x32_bf16 v[206:209], v[170:173], v[220:223], v[44:47]\n"
      "buffer_load_dword v8, v29, s[28:31], s47 offen\n"
      "v_mfma_f32_16x16x32_bf16 v[214:217], v[96:99], a[184:187], v[214:217]\n"
      "ds_read_b128 v[224:227], v1\n"
      "v_cvt_pk_bf16_f32 v133, v132, v133\n"
      "v_mfma_f32_16x16x32_bf16 v[196:199], v[96:99], a[160:163], v[196:199]\n"
      "ds_read_b128 v[146:149], v0\n"
      "v_cvt_pk_bf16_f32 v81, v190, v191\n"
      "v_mfma_f32_16x16x32_bf16 v[196:199], v[104:107], a[164:167], v[196:199]\n"
      "s_waitcnt lgkmcnt(5)\n"
      "v_cvt_pk_bf16_f32 v132, v130, v131\n"
      "v_subrev_u32 v219, 115456, v9\n"
      "v_mfma_f32_16x16x32_bf16 v[214:217], v[104:107], a[188:191], v[214:217]\n"
      "ds_write_b128 v218, v[174:177]\n"
      "v_mfma_f32_16x16x32_bf16 v[182:185], v[228:231], v[142:145], 0\n"
      "s_waitcnt vmcnt(11)\n"
      "s_barrier\n"
      "v_cvt_pk_bf16_f32 v80, v188, v189\n"
      "v_mfma_f32_16x16x32_bf16 v[182:185], v[202:205], v[142:145], v[182:185]\n"
      "ds_read_b128 v[228:231], v156 offset:28672\n"
      "ds_read_b128 v[174:177], v156 offset:24576\n"
      "v_mfma_f32_16x16x32_bf16 v[182:185], v[250:253], v[142:145], v[182:185]\n"
      "ds_read_b128 v[248:251], v111 offset:24576\n"
      "s_waitcnt lgkmcnt(4)\n"
      "v_mfma_f32_16x16x32_bf16 v[182:185], v[126:129], v[142:145], v[182:185]\n"
      "ds_read_b128 v[188:191], v111 offset:32768\n"
      "v_cvt_pk_bf16_f32 v217, v216, v217\n"
      "v_bfe_u32 v130, v219, 8, 2\n"
      "v_mfma_f32_16x16x32_bf16 v[182:185], v[112:115], v[142:145], v[182:185]\n"
      "ds_read_b128 v[126:129], v111 offset:28672\n"
      "v_cvt_pk_bf16_f32 v84, v134, v135\n"
      "v_mfma_f32_16x16x32_bf16 v[182:185], v[152:155], v[142:145], v[182:185]\n"
      "ds_read_b128 v[192:195], v156 offset:32768\n"
      "v_cvt_pk_bf16_f32 v216, v214, v215\n"
      "v_mfma_f32_16x16x16_bf16 v[206:209], v[138:139], v[158:159], v[206:209]\n"
      "v_cvt_pk_bf16_f32 v85, v136, v137\n"
      "v_lshlrev_b32_e64 v130, 2, v130\n"
      "v_cvt_pk_bf16_f32 v199, v198, v199\n"
      "v_mfma_f32_16x16x32_bf16 v[154:157], v[146:149], a[144:147], 0\n"
      "ds_read_b128 v[134:137], v0 offset:6144\n"
      "v_bfe_u32 v219, v219, 4, 4\n"
      "v_mfma_f32_16x16x32_bf16 v[154:157], v[224:227], a[148:151], v[154:157]\n"
      "ds_read_b64_tr_b16 a[238:239], v89 offset:38912\n"
      "s_waitcnt lgkmcnt(5)\n"
      "v_mfma_f32_16x16x32_bf16 v[96:99], v[174:177], v[142:145], 0\n"
      "ds_read_b64_tr_b16 v[252:253], v78 offset:36864\n"
      "s_waitcnt vmcnt(10)\n"
      "v_or3_b32 v100, v100, v101, v102\n"
      "v_mfma_f32_16x16x32_bf16 v[96:99], v[248:251], v[142:145], v[96:99]\n"
      "ds_read_b128 v[174:177], v10 offset:32\n"
      "v_mul_f32 v185, 0x3eaaaaab, v185\n"
      "v_mfma_f32_16x16x32_bf16 v[96:99], v[228:231], v[142:145], v[96:99]\n"
      "ds_read_b128 v[104:107], v1 offset:6144\n"
      "s_waitcnt lgkmcnt(5)\n"
      "v_mfma_f32_16x16x32_bf16 v[96:99], v[126:129], v[142:145], v[96:99]\n"
      "ds_read_b64_tr_b16 a[236:237], v89 offset:36864\n"
      "v_cvt_pk_bf16_f32 v198, v196, v197\n"
      "v_mfma_f32_16x16x32_bf16 v[96:99], v[192:195], v[142:145], v[96:99]\n"
      "ds_read_b64_tr_b16 v[230:231], v89 offset:38920\n"
      "v_sub_u32 v219, v219, v130\n"
      "v_mfma_f32_16x16x32_bf16 v[96:99], v[188:191], v[142:145], v[96:99]\n"
      "ds_read_b64_tr_b16 v[228:229], v89 offset:36872\n"
      "v_mul_f32 v182, 0x3eaaaaab, v182\n"
      "v_mfma_f32_16x16x32_bf16 v[188:191], v[146:149], a[192:195], 0\n"
      "ds_read_b64 v[92:93], v10 offset:24\n"
      "v_mul_f32 v183, 0x3eaaaaab, v183\n"
      "v_mov_b32 v131, 0\n"
      "v_mfma_f32_16x16x32_bf16 v[188:191], v[224:227], a[196:199], v[188:191]\n"
      "s_waitcnt lgkmcnt(5)\n"
      "s_waitcnt vmcnt(9)\n"
      "v_or3_b32 v100, v100, v103, v255\n"
      "v_mul_f32 v96, 0x3eaaaaab, v96\n"
      "v_mfma_f32_16x16x32_bf16 v[110:113], v[146:149], a[168:171], 0\n"
      "ds_read_b128 v[144:147], v1 offset:12288\n"
      "v_mul_f32 v97, 0x3eaaaaab, v97\n"
      "v_mfma_f32_16x16x32_bf16 v[110:113], v[224:227], a[172:175], v[110:113]\n"
      "ds_read_b64_tr_b16 v[250:251], v78 offset:26624\n"
      "v_mul_f32 v98, 0x3eaaaaab, v98\n"
      "v_mfma_f32_16x16x32_bf16 v[110:113], v[134:137], a[176:179], v[110:113]\n"
      "ds_read_b128 v[126:129], v0 offset:12288\n"
      "s_waitcnt vmcnt(8)\n"
      "v_or3_b32 v166, v166, v167, v168\n"
      "v_mfma_f32_16x16x32_bf16 v[150:153], v[120:123], v[220:223], v[40:43]\n"
      "ds_write_b32 v31, v100 offset:6656\n"
      "v_mfma_f32_16x16x32_bf16 v[140:143], v[174:177], v[220:223], v[24:27]\n"
      "ds_read_b64_tr_b16 a[234:235], v89 offset:26624\n"
      "v_mul_f32 v99, 0x3eaaaaab, v99\n"
      "v_mfma_f32_16x16x32_bf16 v[154:157], v[134:137], a[152:155], v[154:157]\n"
      "ds_read_b64_tr_b16 v[224:225], v89 offset:24584\n"
      "v_mul_f32 v184, 0x3eaaaaab, v184\n"
      "v_mfma_f32_16x16x32_bf16 v[188:191], v[134:137], a[200:203], v[188:191]\n"
      "ds_write_b128 v218, v[182:185] offset:256\n"
      "v_mfma_f32_16x16x32_bf16 v[202:205], v[174:177], v[210:213], v[26:29]\n"
      "ds_read_b64_tr_b16 a[232:233], v89 offset:24576\n"
      "s_waitcnt lgkmcnt(5)\n"
      "v_mfma_f32_16x16x32_bf16 v[154:157], v[104:107], a[156:159], v[154:157]\n"
      "ds_read_b64_tr_b16 v[248:249], v78 offset:24576\n"
      "s_waitcnt vmcnt(7)\n"
      "v_or3_b32 v166, v166, v169, v254\n"
      "v_mfma_f32_16x16x32_bf16 v[110:113], v[104:107], a[180:183], v[110:113]\n"
      "ds_write_b128 v218, v[96:99] offset:512\n"
      "v_mfma_f32_16x16x32_bf16 v[110:113], v[126:129], a[184:187], v[110:113]\n"
      "ds_read_b64_tr_b16 v[226:227], v89 offset:26632\n"
      "v_mov_b32 v130, 0x3f80\n"
      "v_mfma_f32_16x16x32_bf16 v[192:195], v[178:181], v[220:223], v[48:51]\n"
      "ds_read_b64_tr_b16 v[254:255], v78 offset:38912\n"
      "v_lshlrev_b32 v219, 4, v219\n"
      "v_mfma_f32_16x16x32_bf16 v[154:157], v[126:129], a[160:163], v[154:157]\n"
      "ds_write_b32 v31, v166 offset:5632\n"
      "v_mfma_f32_16x16x32_bf16 v[154:157], v[144:147], a[164:167], v[154:157]\n"
      "s_waitcnt lgkmcnt(0)\n"
      "s_barrier\n"
      "v_mov_b32 v185, 0\n"
      "v_mfma_f32_16x16x16_bf16 v[150:153], v[186:187], v[158:159], v[150:153]\n"
      "ds_read_b128 v[166:169], v9 offset:6656\n"
      "v_lshlrev_b64 v[130:131], v219, v[130:131]\n"
      "v_mfma_f32_16x16x32_bf16 v[110:113], v[144:147], a[188:191], v[110:113]\n"
      "ds_read_b128 v[100:103], v9 offset:5632\n"
      "v_cmp_gt_u32 s[80:81], 64, v219\n"
      "v_mfma_f32_16x16x32_bf16 v[188:191], v[104:107], a[204:207], v[188:191]\n"
      "ds_read_b128 v[104:107], v39 offset:64\n"
      "v_cndmask_b32_e64 v98, 0, v130, s[80:81]\n"
      "v_mfma_f32_16x16x16_bf16 v[192:195], v[124:125], v[158:159], v[192:195]\n"
      "v_mov_b32_e64 v97, 0\n"
      "v_cndmask_b32_e64 v183, 0, v131, s[80:81]\n"
      "v_cvt_pk_bf16_f32 v196, v154, v155\n"
      "v_mfma_f32_16x16x16_bf16 v[140:143], v[92:93], v[158:159], v[140:143]\n"
      "v_cndmask_b32_e64 v182, 0, v130, s[80:81]\n"
      "v_cvt_pk_bf16_f32 v214, v110, v111\n"
      "v_mov_b32_e64 v96, 0\n"
      "v_mfma_f32_16x16x32_bf16 v[188:191], v[126:129], a[208:211], v[188:191]\n"
      "ds_read_b128 v[126:129], v39\n"
      "v_cndmask_b32_e64 v99, 0, v131, s[80:81]\n"
      "v_mfma_f32_16x16x32_bf16 v[188:191], v[144:147], a[212:215], v[188:191]\n"
      "s_waitcnt lgkmcnt(3)\n"
      "v_or3_b32 v166, v166, v167, v168\n"
      "v_mov_b32 v184, 0\n"
      "v_mfma_f32_16x16x16_bf16 v[202:205], v[92:93], v[108:109], v[202:205]\n"
      "v_cvt_pk_bf16_f32 v197, v156, v157\n"
      "v_bitop3_b32 v166, v166, v169, s33 bitop3:0xa8\n"
      "v_cmp_ne_u32 s[56:57], 0, v166\n"
      "v_mfma_f32_16x16x32_bf16 v[166:169], v[120:123], v[210:213], v[52:55]\n"
      "s_waitcnt lgkmcnt(1)\n"
      "v_cvt_pk_bf16_f32 v131, v190, v191\n"
      "v_or3_b32 v100, v100, v101, v102\n"
      "v_mfma_f32_16x16x32_bf16 v[144:147], v[96:99], v[232:235], v[104:107]\n"
      "s_or_b64 vcc, s[56:57], s[54:55]\n"
      "v_cvt_pk_bf16_f32 v130, v188, v189\n"
      "v_bitop3_b32 v100, v100, v103, s33 bitop3:0xa8\n"
      "v_mfma_f32_16x16x32_bf16 v[188:191], v[96:99], a[216:219], v[104:107]\n"
      "s_cbranch_vccnz L_rare_1_%=\n"
      "L_back_2_%=:\n"
      "v_mfma_f32_16x16x32_bf16 v[218:221], v[96:99], a[240:243], v[104:107]\n"
      "s_cbranch_vccnz L_rare_3_%=\n"
      "L_back_4_%=:\n"
      "v_mfma_f32_16x16x32_bf16 v[134:137], v[178:181], v[210:213], v[60:63]\n"
      "s_waitcnt lgkmcnt(0)\n"
      "v_cvt_pk_bf16_f32 v215, v112, v113\n"
      "v_cmp_ne_u32 s[56:57], 0, v100\n"
      "v_mfma_f32_16x16x32_bf16 v[110:113], v[182:185], v[232:235], v[126:129]\n"
      "v_cvt_pk_bf16_f32 v234, v144, v145\n"
      "v_cvt_pk_bf16_f32 v235, v146, v147\n"
      "v_exp_f32 v151, v151\n"
      "v_cvt_pk_bf16_f32 v218, v218, v219\n"
      "v_mfma_f32_16x16x16_bf16 v[166:169], v[186:187], v[108:109], v[166:169]\n"
      "v_exp_f32_e64 v195, v195\n"
      "v_cvt_pk_bf16_f32 v220, v220, v221\n"
      "v_mfma_f32_16x16x16_bf16 v[134:137], v[124:125], v[108:109], v[134:137]\n"
      "v_cvt_pk_bf16_f32 v233, v112, v113\n"
      "v_exp_f32 v153, v153\n"
      "v_exp_f32 v150, v150\n"
      "v_cvt_pk_bf16_f32 v188, v188, v189\n"
      "v_mfma_f32_16x16x32_bf16 v[144:147], v[170:173], v[210:213], v[56:59]\n"
      "ds_read_b128 v[100:103], v10\n"
      "v_cvt_pk_bf16_f32 v232, v110, v111\n"
      "v_mfma_f32_16x16x32_bf16 v[154:157], v[182:185], a[216:219], v[126:129]\n"
      "v_exp_f32_e64 v209, v209\n"
      "v_mfma_f32_16x16x32_bf16 v[112:115], v[182:185], a[240:243], v[126:129]\n"
      "ds_read_b64 v[222:223], v10 offset:16\n"
      "v_accvgpr_write_b32 a243, v220\n"
      "v_cvt_pk_bf16_f32 v150, v150, v151\n"
      "v_mfma_f32_16x16x16_bf16 v[144:147], v[138:139], v[108:109], v[144:147]\n"
      "v_accvgpr_write_b32 a218, v188\n"
      "v_accvgpr_write_b32 a242, v218\n"
      "v_cvt_pk_bf16_f32 v154, v154, v155\n"
      "v_exp_f32 v208, v208\n"
      "v_exp_f32 v140, v140\n"
      "v_cvt_pk_bf16_f32 v156, v156, v157\n"
      "v_cvt_pk_bf16_f32 v112, v112, v113\n"
      "v_cvt_pk_bf16_f32 v190, v190, v191\n"
      "v_mfma_f32_16x16x32_bf16 v[106:109], v[170:173], v[162:165], v[68:71]\n"
      "s_waitcnt lgkmcnt(1)\n"
      "v_exp_f32 v206, v206\n"
      "v_exp_f32_e64 v152, v152\n"
      "v_accvgpr_write_b32 a240, v112\n"
      "v_mfma_f32_16x16x32_bf16 v[126:129], v[100:103], v[170:173], v[30:33]\n"
      "s_cbranch_vccnz L_rare_5_%=\n"
      "L_back_6_%=:\n"
      "v_accvgpr_write_b32 a217, v156\n"
      "v_exp_f32 v193, v193\n"
      "v_mfma_f32_16x16x16_bf16 v[106:109], v[138:139], v[200:201], v[106:109]\n"
      "v_cvt_pk_bf16_f32 v151, v152, v153\n"
      "v_exp_f32 v147, v147\n"
      "v_exp_f32 v192, v192\n"
      "v_mfma_f32_16x16x32_bf16 v[156:159], v[120:123], v[162:165], v[64:67]\n"
      "s_waitcnt lgkmcnt(0)\n"
      "v_exp_f32 v207, v207\n"
      "v_mfma_f32_16x16x16_bf16 v[126:129], v[222:223], v[138:139], v[126:129]\n"
      "v_exp_f32_e64 v135, v135\n"
      "v_cvt_pk_bf16_f32 v153, v208, v209\n"
      "v_mfma_f32_16x16x32_bf16 v[218:221], v[100:103], v[120:123], v[30:33]\n"
      "ds_read_u16 v82, v11 offset:32\n"
      "v_exp_f32_e64 v202, v202\n"
      "v_mfma_f32_16x16x32_bf16 v[208:211], v[174:177], v[162:165], v[28:31]\n"
      "ds_read_u16 v78, v11\n"
      "v_cvt_pk_bf16_f32 v152, v206, v207\n"
      "v_mfma_f32_16x16x16_bf16 v[156:159], v[186:187], v[200:201], v[156:159]\n"
      "v_exp_f32_e64 v137, v137\n"
      "v_accvgpr_write_b32 a216, v154\n"
      "v_exp_f32 v194, v194\n"
      "v_mfma_f32_16x16x16_bf16 v[208:211], v[92:93], v[200:201], v[208:211]\n"
      "v_exp_f32 v167, v167\n"
      "v_cvt_pk_bf16_f32 v90, v140, 0\n"
      "v_mfma_f32_16x16x32_bf16 v[204:207], v[178:181], v[162:165], v[72:75]\n"
      "ds_read_u16 v86, v11 offset:64\n"
      "v_exp_f32_e64 v136, v136\n"
      "v_mfma_f32_16x16x32_bf16 v[140:143], v[100:103], v[174:177], v[30:33]\n"
      "ds_read_b128 v[174:177], v39 offset:128\n"
      "v_cvt_pk_bf16_f32 v88, v192, v193\n"
      "v_mfma_f32_16x16x16_bf16 v[218:221], v[222:223], v[186:187], v[218:221]\n"
      "v_exp_f32_e64 v169, v169\n"
      "v_exp_f32_e64 v166, v166\n"
      "v_cvt_pk_bf16_f32 v89, v194, v195\n"
      "v_mfma_f32_16x16x32_bf16 v[210:213], v[100:103], v[178:181], v[30:33]\n"
      "ds_read_b128 v[160:163], v39 offset:192\n"
      "v_exp_f32 v145, v145\n"
      "v_mfma_f32_16x16x16_bf16 v[140:143], v[222:223], v[92:93], v[140:143]\n"
      "v_cvt_pk_bf16_f32 v114, v114, v115\n"
      "v_cvt_pk_bf16_f32 v94, v202, 0\n"
      "s_waitcnt vmcnt(1)\n"
      "v_or3_b32 v116, v116, v117, v118\n"
      "v_mfma_f32_16x16x32_bf16 v[110:113], v[196:199], v[150:153], 0\n"
      "s_waitcnt lgkmcnt(2)\n"
      "v_exp_f32 v168, v168\n"
      "v_mfma_f32_16x16x32_bf16 v[110:113], v[76:79], v[88:91], v[110:113]\n"
      "v_exp_f32_e64 v146, v146\n"
      "v_mfma_f32_16x16x16_bf16 v[204:207], v[124:125], v[200:201], v[204:207]\n"
      "v_cvt_pk_bf16_f32 v166, v166, v167\n"
      "v_exp_f32_e64 v144, v144\n"
      "v_mfma_f32_16x16x32_bf16 v[192:195], v[130:133], v[150:153], 0\n"
      "v_cvt_pk_bf16_f32 v93, v136, v137\n"
      "v_cndmask_b32 v218, v218, v126, s[58:59]\n"
      "v_mfma_f32_16x16x32_bf16 v[192:195], v[84:87], v[88:91], v[192:195]\n"
      "s_waitcnt lgkmcnt(0)\n"
      "v_cvt_pk_bf16_f32 v167, v168, v169\n"
      "v_accvgpr_write_b32 a219, v190\n"
      "v_mfma_f32_16x16x32_bf16 v[126:129], v[96:99], v[236:239], v[160:163]\n"
      "s_cbranch_vccnz L_rare_7_%=\n"
      "L_back_8_%=:\n"
      "v_mfma_f32_16x16x32_bf16 v[100:103], v[32:35], v[150:153], 0\n"
      "ds_read_b128 v[136:139], v39 offset:320\n"
      "v_exp_f32_e64 v108, v108\n"
      "v_cvt_pk_bf16_f32 v168, v144, v145\n"
      "v_mfma_f32_16x16x32_bf16 v[100:103], v[32:35], v[88:91], v[100:103]\n"
      "s_or_b32 s95, s84, s85\n"
      "v_exp_f32 v109, v109\n"
      "v_mfma_f32_16x16x16_bf16 v[210:213], v[222:223], v[124:125], v[210:213]\n"
      "v_exp_f32_e64 v134, v134\n"
      "v_cvt_pk_bf16_f32 v169, v146, v147\n"
      "v_mfma_f32_16x16x32_bf16 v[142:145], v[182:185], v[236:239], v[174:177]\n"
      "v_exp_f32 v205, v205\n"
      "v_mfma_f32_16x16x32_bf16 v[188:191], v[182:185], a[220:223], v[174:177]\n"
      "v_rcp_f32 v100, v100\n"
      "v_mfma_f32_16x16x32_bf16 v[146:149], v[214:217], v[150:153], 0\n"
      "ds_read_b128 v[152:155], v39 offset:256\n"
      "v_exp_f32 v156, v156\n"
      "v_mfma_f32_16x16x32_bf16 v[146:149], v[80:83], v[88:91], v[146:149]\n"
      "v_exp_f32 v206, v206\n"
      "v_mfma_f32_16x16x32_bf16 v[178:181], v[182:185], a[244:247], v[174:177]\n"
      "s_waitcnt lgkmcnt(1)\n"
      "v_exp_f32 v204, v204\n"
      "v_mfma_f32_16x16x32_bf16 v[220:223], v[96:99], a[248:251], v[136:139]\n"
      "v_cvt_pk_bf16_f32 v188, v188, v189\n"
      "v_exp_f32 v107, v107\n"
      "v_cvt_pk_bf16_f32 v92, v134, v135\n"
      "v_exp_f32 v157, v157\n"
      "v_pk_mul_f32 v[110:111], v[110:111], v[100:101] op_sel_hi:[1,0]\n"
      "v_exp_f32_e64 v207, v207\n"
      "v_pk_mul_f32 v[148:149], v[148:149], v[100:101] op_sel_hi:[1,0]\n"
      "v_cvt_pk_bf16_f32 v190, v190, v191\n"
      "v_pk_mul_f32 v[112:113], v[112:113], v[100:101] op_sel_hi:[1,0]\n"
      "v_cvt_pk_bf16_f32 v236, v142, v143\n"
      "v_mfma_f32_16x16x32_bf16 v[174:177], v[96:99], a[244:247], v[160:163]\n"
      "s_or_b32 s95, s95, s48\n"
      "v_exp_f32 v159, v159\n"
      "v_mfma_f32_16x16x32_bf16 v[170:173], v[96:99], a[220:223], v[160:163]\n"
      "s_cmp_ge_u32 s95, 0x40000000\n"
      "s_cselect_b32 s94, -1, -2\n"
      "v_cndmask_b32 v218, v218, v210, s[60:61]\n"
      "v_cvt_pk_bf16_f32 v89, v206, v207\n"
      "v_accvgpr_write_b32 a220, v188\n"
      "v_cvt_pk_bf16_f32 v239, v128, v129\n"
      "v_pk_mul_f32 v[192:193], v[192:193], v[100:101] op_sel_hi:[1,0]\n"
      "v_exp_f32 v208, v208\n"
      "v_pk_mul_f32 v[146:147], v[146:147], v[100:101] op_sel_hi:[1,0]\n"
      "v_mfma_f32_16x16x32_bf16 v[210:213], v[96:99], v[240:243], v[136:139]\n"
      "v_cndmask_b32 v218, v218, v140, s[62:63]\n"
      "v_cvt_pk_bf16_f32 v176, v176, v177\n"
      "v_cvt_pk_bf16_f32 v222, v222, v223\n"
      "v_mfma_f32_16x16x32_bf16 v[140:143], v[96:99], a[224:227], v[136:139]\n"
      "s_waitcnt lgkmcnt(0)\n"
      "v_exp_f32 v106, v106\n"
      "v_mfma_f32_16x16x32_bf16 v[186:189], v[182:185], a[224:227], v[152:155]\n"
      "s_waitcnt vmcnt(0)\n"
      "v_or3_b32 v116, v116, v119, v8\n"
      "v_cvt_pk_bf16_f32 v172, v172, v173\n"
      "v_mfma_f32_16x16x32_bf16 v[118:121], v[182:185], v[240:243], v[152:155]\n"
      "v_exp_f32 v158, v158\n"
      "v_cvt_pk_bf16_f32 v237, v144, v145\n"
      "v_cvt_pk_bf16_f32 v156, v156, v157\n"
      "v_mfma_f32_16x16x32_bf16 v[134:137], v[182:185], a[248:251], v[152:155]\n"
      "v_cvt_pk_bf16_f32 v180, v180, v181\n"
      "v_cvt_pk_bf16_f32 v110, v110, v111\n"
      "v_mfma_f32_16x16x32_bf16 v[150:153], v[32:35], v[166:169], 0\n"
      "s_cbranch_vccnz L_rare_9_%=\n"
      "L_back_10_%=:\n"
      "v_cvt_pk_bf16_f32 v240, v118, v119\n"
      "v_mfma_f32_16x16x32_bf16 v[150:153], v[32:35], v[92:95], v[150:153]\n"
      "v_cvt_pk_bf16_f32 v142, v142, v143\n"
      "v_cvt_pk_bf16_f32 v157, v158, v159\n"
      "v_cvt_pk_bf16_f32 v111, v112, v113\n"
      "v_cvt_pk_bf16_f32 v158, v106, v107\n"
      "v_mfma_f32_16x16x32_bf16 v[200:203], v[196:199], v[166:169], 0\n"
      "s_cmp_gt_u32 s42, s44\n"
      "s_cselect_b32 s95, s94, s95\n"
      "v_cvt_pk_bf16_f32 v88, v204, v205\n"
      "v_mfma_f32_16x16x32_bf16 v[200:203], v[76:79], v[92:95], v[200:203]\n"
      "v_exp_f32_e64 v218, v218\n"
      "v_cvt_pk_bf16_f32 v241, v120, v121\n"
      "v_mfma_f32_16x16x32_bf16 v[104:107], v[130:133], v[166:169], 0\n"
      "ds_read_b128 v[122:125], v39 offset:448\n"
      "v_cvt_pk_bf16_f32 v242, v210, v211\n"
      "v_mfma_f32_16x16x32_bf16 v[104:107], v[84:87], v[92:95], v[104:107]\n"
      "s_and_b32 s86, s86, -4\n"
      "v_cvt_pk_bf16_f32 v220, v220, v221\n"
      "v_accvgpr_write_b32 a245, v180\n"
      "v_accvgpr_write_b32 a241, v114\n"
      "v_rcp_f32 v150, v150\n"
      "v_cvt_pk_bf16_f32 v112, v146, v147\n"
      "v_pk_mul_f32 v[200:201], v[200:201], v[150:151] op_sel_hi:[1,0]\n"
      "v_cvt_pk_bf16_f32 v188, v188, v189\n"
      "v_pk_mul_f32 v[202:203], v[202:203], v[150:151] op_sel_hi:[1,0]\n"
      "v_cvt_pk_bf16_f32 v159, v108, v109\n"
      "v_pk_mul_f32 v[106:107], v[106:107], v[150:151] op_sel_hi:[1,0]\n"
      "v_pk_mul_f32 v[104:105], v[104:105], v[150:151] op_sel_hi:[1,0]\n"
      "v_mfma_f32_16x16x32_bf16 v[118:121], v[214:217], v[166:169], 0\n"
      "v_cvt_pk_bf16_f32 v136, v136, v137\n"
      "v_cvt_pk_bf16_f32 v218, v218, v218\n"
      "v_cvt_pk_bf16_f32 v178, v178, v179\n"
      "v_mfma_f32_16x16x32_bf16 v[118:121], v[80:83], v[92:95], v[118:121]\n"
      "v_cvt_pk_bf16_f32 v90, v208, 0\n"
      "v_accvgpr_write_b32 a249, v136\n"
      "v_cvt_pk_bf16_f32 v113, v148, v149\n"
      "v_mfma_f32_16x16x32_bf16 v[208:211], v[32:35], v[156:159], 0\n"
      "ds_read_b128 v[166:169], v39 offset:384\n"
      "v_cvt_pk_bf16_f32 v170, v170, v171\n"
      "v_mfma_f32_16x16x32_bf16 v[208:211], v[32:35], v[88:91], v[208:211]\n"
      "ds_write_b128 v3, v[110:113]\n"
      "v_pk_mul_f32 v[194:195], v[194:195], v[100:101] op_sel_hi:[1,0]\n"
      "v_mfma_f32_16x16x32_bf16 v[144:147], v[214:217], v[156:159], 0\n"
      "ds_read_b128 v[162:165], v39 offset:576\n"
      "v_cvt_pk_bf16_f32 v174, v174, v175\n"
      "v_cvt_pk_bf16_f32 v200, v200, v201\n"
      "v_accvgpr_write_b32 a247, v176\n"
      "v_mfma_f32_16x16x32_bf16 v[144:147], v[80:83], v[88:91], v[144:147]\n"
      "s_waitcnt lgkmcnt(3)\n"
      "v_rcp_f32_e64 v208, v208\n"
      "v_cvt_pk_bf16_f32 v243, v212, v213\n"
      "v_mfma_f32_16x16x32_bf16 v[204:207], v[196:199], v[156:159], 0\n"
      "ds_read_b128 v[112:115], v39 offset:640\n"
      "v_cvt_pk_bf16_f32 v186, v186, v187\n"
      "v_accvgpr_write_b32 a246, v174\n"
      "v_mfma_f32_16x16x32_bf16 v[204:207], v[76:79], v[88:91], v[204:207]\n"
      "ds_write_b16 v13, v218\n"
      "v_mfma_f32_16x16x32_bf16 v[108:111], v[96:99], a[228:231], v[122:125]\n"
      "ds_read_b128 v[174:177], v39 offset:512\n"
      "v_accvgpr_write_b32 a244, v178\n"
      "v_mfma_f32_16x16x32_bf16 v[178:181], v[130:133], v[156:159], 0\n"
      "v_accvgpr_write_b32 a225, v188\n"
      "v_cvt_pk_bf16_f32 v192, v192, v193\n"
      "v_mfma_f32_16x16x32_bf16 v[178:181], v[84:87], v[88:91], v[178:181]\n"
      "v_cvt_pk_bf16_f32 v140, v140, v141\n"
      "v_cvt_pk_bf16_f32 v193, v194, v195\n"
      "v_mfma_f32_16x16x32_bf16 v[154:157], v[96:99], a[252:255], v[122:125]\n"
      "s_waitcnt lgkmcnt(3)\n"
      "v_cvt_pk_bf16_f32 v134, v134, v135\n"
      "v_cvt_pk_bf16_f32 v238, v126, v127\n"
      "v_cvt_pk_bf16_f32 v110, v110, v111\n"
      "v_mfma_f32_16x16x32_bf16 v[126:129], v[182:185], a[228:231], v[166:169]\n"
      "ds_write_b64 v4, v[192:193]\n"
      "v_accvgpr_write_b32 a248, v134\n"
      "v_accvgpr_write_b32 a231, v110\n"
      "v_pk_mul_f32 v[118:119], v[118:119], v[150:151] op_sel_hi:[1,0]\n"
      "v_pk_mul_f32 v[120:121], v[120:121], v[150:151] op_sel_hi:[1,0]\n"
      "v_pk_mul_f32 v[204:205], v[204:205], v[208:209] op_sel_hi:[1,0]\n"
      "v_cvt_pk_bf16_f32 v204, v204, v205\n"
      "v_pk_mul_f32 v[180:181], v[180:181], v[208:209] op_sel_hi:[1,0]\n"
      "v_cvt_pk_bf16_f32 v108, v108, v109\n"
      "v_cvt_pk_bf16_f32 v201, v202, v203\n"
      "v_accvgpr_write_b32 a227, v142\n"
      "v_pk_mul_f32 v[178:179], v[178:179], v[208:209] op_sel_hi:[1,0]\n"
      "v_mfma_f32_16x16x32_bf16 v[134:137], v[182:185], v[244:247], v[166:169]\n"
      "ds_read_b128 v[150:153], v39 offset:704\n"
      "v_cvt_pk_bf16_f32 v128, v128, v129\n"
      "v_cvt_pk_bf16_f32 v156, v156, v157\n"
      "v_mfma_f32_16x16x32_bf16 v[100:103], v[96:99], v[244:247], v[122:125]\n"
      "s_waitcnt lgkmcnt(2)\n"
      "v_accvgpr_write_b32 a221, v190\n"
      "v_cvt_pk_bf16_f32 v202, v118, v119\n"
      "v_mfma_f32_16x16x32_bf16 v[122:125], v[182:185], a[236:239], v[112:115]\n"
      "s_add_u32 s42, s42, 1\n"
      "v_cvt_pk_bf16_f32 v203, v120, v121\n"
      "v_accvgpr_write_b32 a224, v186\n"
      "v_cvt_pk_bf16_f32 v104, v104, v105\n"
      "v_cvt_pk_bf16_f32 v105, v106, v107\n"
      "v_mfma_f32_16x16x32_bf16 v[186:189], v[182:185], a[252:255], v[166:169]\n"
      "s_cmp_ge_u32 s95, 0x40000000\n"
      "s_cselect_b64 vcc, -1, s[56:57]\n"
      "v_accvgpr_write_b32 a226, v140\n"
      "v_cvt_pk_bf16_f32 v124, v124, v125\n"
      "v_mfma_f32_16x16x32_bf16 v[166:169], v[96:99], a[232:235], v[162:165]\n"
      "ds_write_b128 v3, v[200:203] offset:256\n"
      "v_pk_mul_f32 v[206:207], v[206:207], v[208:209] op_sel_hi:[1,0]\n"
      "v_cvt_pk_bf16_f32 v154, v154, v155\n"
      "v_pk_mul_f32 v[146:147], v[146:147], v[208:209] op_sel_hi:[1,0]\n"
      "v_pk_mul_f32 v[144:145], v[144:145], v[208:209] op_sel_hi:[1,0]\n"
      "v_mfma_f32_16x16x32_bf16 v[118:121], v[182:185], v[224:227], v[174:177]\n"
      "ds_read_b128 v[210:213], v14\n"
      "v_cvt_pk_bf16_f32 v186, v186, v187\n"
      "v_cvt_pk_bf16_f32 v126, v126, v127\n"
      "v_cvt_pk_bf16_f32 v178, v178, v179\n"
      "v_mfma_f32_16x16x32_bf16 v[140:143], v[182:185], a[232:235], v[174:177]\n"
      "ds_write_b32 v31, v116 offset:4608\n"
      "v_cvt_pk_bf16_f32 v168, v168, v169\n"
      "v_cvt_pk_bf16_f32 v179, v180, v181\n"
      "v_mfma_f32_16x16x32_bf16 v[158:161], v[182:185], v[252:255], v[112:115]\n"
      "v_cvt_pk_bf16_f32 v166, v166, v167\n"
      "v_cvt_pk_bf16_f32 v247, v102, v103\n"
      "v_cvt_pk_bf16_f32 v205, v206, v207\n"
      "v_accvgpr_write_b32 a228, v126\n"
      "v_mfma_f32_16x16x32_bf16 v[190:193], v[96:99], v[224:227], v[162:165]\n"
      "ds_write_b64 v4, v[178:179] offset:512\n"
      "v_mfma_f32_16x16x32_bf16 v[178:181], v[182:185], v[248:251], v[174:177]\n"
      "s_waitcnt lgkmcnt(3)\n"
      "v_accvgpr_write_b32 a230, v108\n"
      "v_cvt_pk_bf16_f32 v244, v134, v135\n"
      "v_mfma_f32_16x16x32_bf16 v[200:203], v[96:99], a[236:239], v[150:153]\n"
      "ds_read_b128 v[106:109], v14 offset:64\n"
      "v_cvt_pk_bf16_f32 v140, v140, v141\n"
      "v_cvt_pk_bf16_f32 v122, v122, v123\n"
      "v_cvt_pk_bf16_f32 v226, v190, v191\n"
      "v_cvt_pk_bf16_f32 v188, v188, v189\n"
      "v_cvt_pk_bf16_f32 v207, v146, v147\n"
      "v_accvgpr_write_b32 a222, v170\n"
      "v_mfma_f32_16x16x32_bf16 v[146:149], v[96:99], v[228:231], v[150:153]\n"
      "ds_write_b64 v4, v[104:105] offset:256\n"
      "v_mfma_f32_16x16x32_bf16 v[174:177], v[182:185], v[228:231], v[112:115]\n"
      "v_cvt_pk_bf16_f32 v227, v192, v193\n"
      "v_cvt_pk_bf16_f32 v206, v144, v145\n"
      "v_cvt_pk_bf16_f32 v246, v100, v101\n"
      "v_mfma_f32_16x16x32_bf16 v[190:193], v[96:99], v[248:251], v[162:165]\n"
      "ds_write_b128 v3, v[204:207] offset:512\n"
      "v_cvt_pk_bf16_f32 v202, v202, v203\n"
      "v_mfma_f32_16x16x32_bf16 v[110:113], v[96:99], v[252:255], v[150:153]\n"
      "s_waitcnt lgkmcnt(2)\n"
      "v_accvgpr_write_b32 a250, v220\n"
      "v_accvgpr_write_b32 a254, v154\n"
      "v_cvt_pk_bf16_f32 v225, v120, v121\n"
      "v_cvt_pk_bf16_f32 v224, v118, v119\n"
      "v_cvt_pk_bf16_f32 v231, v148, v149\n"
      "v_accvgpr_write_b32 a252, v186\n"
      "v_accvgpr_write_b32 a251, v222\n"
      "v_cvt_pk_bf16_f32 v230, v146, v147\n"
      "v_accvgpr_write_b32 a239, v202\n"
      "v_cvt_pk_bf16_f32 v254, v110, v111\n"
      "v_accvgpr_write_b32 a236, v122\n"
      "v_cvt_pk_bf16_f32 v253, v160, v161\n"
      "v_cvt_pk_bf16_f32 v228, v174, v175\n"
      "v_cvt_pk_bf16_f32 v255, v112, v113\n"
      "v_cvt_pk_bf16_f32 v142, v142, v143\n"
      "v_cvt_pk_bf16_f32 v252, v158, v159\n"
      "v_accvgpr_write_b32 a234, v166\n"
      "v_accvgpr_write_b32 a253, v188\n"
      "v_accvgpr_write_b32 a237, v124\n"
      "v_accvgpr_write_b32 a235, v168\n"
      "v_cvt_pk_bf16_f32 v249, v180, v181\n"
      "v_accvgpr_write_b32 a223, v172\n"
      "v_cvt_pk_bf16_f32 v200, v200, v201\n"
      "v_accvgpr_write_b32 a238, v200\n"
      "v_accvgpr_write_b32 a233, v142\n"
      "v_cvt_pk_bf16_f32 v248, v178, v179\n"
      "v_cvt_pk_bf16_f32 v229, v176, v177\n"
      "v_cvt_pk_bf16_f32 v250, v190, v191\n"
      "v_accvgpr_write_b32 a255, v156\n"
      "v_mfma_f32_16x16x32_bf16 v[218:221], v[210:213], v[32:35], 0\n"
      "v_cvt_pk_bf16_f32 v245, v136, v137\n"
      "v_accvgpr_write_b32 a229, v128\n"
      "v_cvt_pk_bf16_f32 v251, v192, v193\n"
      "v_accvgpr_write_b32 a232, v140\n"
      "v_mfma_f32_16x16x32_bf16 v[218:221], v[106:109], v[32:35], v[218:221]\n"
      "s_waitcnt lgkmcnt(0)\n"
      "s_barrier\n"
      "s_or_b64 vcc, vcc, s[54:55]\n"
      "s_cbranch_vccnz L_disp1_%=\n"
      "s_nop 7\n"
      "s_nop 7\n"
      "L_loop1_%=:\n"
      "v_mfma_f32_16x16x32_bf16 v[204:207], v[210:213], v[196:199], 0\n"
      "ds_read_b128 v[112:115], v0 offset:18432\n"
      "v_readlane_b32 s45, v37, s43\n"
      "v_mfma_f32_16x16x32_bf16 v[204:207], v[106:109], v[76:79], v[204:207]\n"
      "ds_read_b128 v[144:147], v9\n"
      "ds_read_b128 v[152:155], v1 offset:30720\n"
      "v_mfma_f32_16x16x32_bf16 v[176:179], v[210:213], v[214:217], 0\n"
      "s_add_u32 m0, s64, 0\n"
      "ds_read_b128 v[172:175], v1 offset:18432\n"
      "v_mfma_f32_16x16x32_bf16 v[176:179], v[106:109], v[80:83], v[176:179]\n"
      "buffer_load_dwordx4 v16, s[16:19], s84 offen  lds\n"
      "v_mfma_f32_16x16x32_bf16 v[96:99], v[210:213], v[130:133], 0\n"
      "ds_read_b128 v[130:133], v9 offset:4608\n"
      "v_mfma_f32_16x16x32_bf16 v[96:99], v[106:109], v[84:87], v[96:99]\n"
      "ds_read_b128 v[126:129], v1 offset:24576\n"
      "s_waitcnt lgkmcnt(5)\n"
      "v_mfma_f32_16x16x32_bf16 v[108:111], a[0:3], v[112:115], 0\n"
      "ds_read_b128 v[210:213], v0 offset:24576\n"
      "s_add_u32 m0, s64, 4096\n"
      "v_mfma_f32_16x16x32_bf16 v[122:125], a[24:27], v[112:115], 0\n"
      "s_waitcnt lgkmcnt(5)\n"
      "v_cndmask_b32 v204, v204, v176, s[58:59]\n"
      "v_add_f32 v144, v144, v145\n"
      "v_mfma_f32_16x16x32_bf16 v[188:191], a[48:51], v[112:115], 0\n"
      "buffer_load_dwordx4 v17, s[16:19], s84 offen  lds\n"
      "v_mfma_f32_16x16x32_bf16 v[104:107], a[72:75], v[112:115], 0\n"
      "ds_read_b128 v[220:223], v0 offset:34816\n"
      "v_mov_b32_dpp v218, v218 quad_perm:[0,0,0,0] row_mask:0xf bank_mask:0xf\n"
      "v_mfma_f32_16x16x32_bf16 v[176:179], a[96:99], v[112:115], 0\n"
      "s_waitcnt lgkmcnt(3)\n"
      "v_or3_b32 v130, v130, v131, v132\n"
      "v_add_f32 v146, v146, v147\n"
      "v_mfma_f32_16x16x32_bf16 v[184:187], a[120:123], v[112:115], 0\n"
      "s_add_u32 m0, s64, 8192\n"
      "v_readlane_b32 s47, v38, s43\n"
      "v_add_f32 v144, v144, v146\n"
      "v_mfma_f32_16x16x32_bf16 v[108:111], a[4:7], v[172:175], v[108:111]\n"
      "s_waitcnt lgkmcnt(1)\n"
      "v_rsq_f32_e64 v144, v144\n"
      "v_mfma_f32_16x16x32_bf16 v[108:111], a[8:11], v[210:213], v[108:111]\n"
      "v_mul_f32 v144, 0x415db3d7, v144\n"
      "v_mfma_f32_16x16x32_bf16 v[122:125], a[28:31], v[172:175], v[122:125]\n"
      "v_rcp_f32_e64 v218, v218\n"
      "v_mfma_f32_16x16x32_bf16 v[122:125], a[32:35], v[210:213], v[122:125]\n"
      "buffer_load_dwordx4 v18, s[16:19], s84 offen  lds\n"
      "v_mfma_f32_16x16x32_bf16 v[188:191], a[52:55], v[172:175], v[188:191]\n"
      "v_mov_b32_e64 v146, v144\n"
      "v_bitop3_b32 v130, v130, v133, s33 bitop3:0xa8\n"
      "v_mfma_f32_16x16x32_bf16 v[188:191], a[56:59], v[210:213], v[188:191]\n"
      "s_add_u32 m0, s64, 12288\n"
      "v_permlane32_swap_b32_e64 v144, v146\n"
      "v_mfma_f32_16x16x32_bf16 v[104:107], a[76:79], v[172:175], v[104:107]\n"
      "ds_read_b128 v[206:209], v0 offset:30720\n"
      "v_mov_b32 v145, v144\n"
      "v_mfma_f32_16x16x32_bf16 v[104:107], a[80:83], v[210:213], v[104:107]\n"
      "s_nop 0\n"
      "v_permlane16_swap_b32 v144, v145\n"
      "v_mfma_f32_16x16x32_bf16 v[176:179], a[100:103], v[172:175], v[176:179]\n"
      "ds_read_b128 v[180:183], v0 offset:32768\n"
      "v_mfma_f32_16x16x32_bf16 v[176:179], a[104:107], v[210:213], v[176:179]\n"
      "v_mul_f32 v194, v146, v194\n"
      "v_mul_f32 v164, v144, v164\n"
      "v_mfma_f32_16x16x32_bf16 v[184:187], a[124:127], v[172:175], v[184:187]\n"
      "v_readlane_b32 s46, v37, s42\n"
      "v_mul_f32 v137, v145, v137\n"
      "v_mfma_f32_16x16x32_bf16 v[184:187], a[128:131], v[210:213], v[184:187]\n"
      "v_mul_f32 v167, v144, v167\n"
      "v_cndmask_b32 v204, v204, v96, s[60:61]\n"
      "v_mfma_f32_16x16x32_bf16 v[108:111], a[12:15], v[126:129], v[108:111]\n"
      "s_waitcnt lgkmcnt(1)\n"
      "v_mul_f32 v156, v145, v156\n"
      "v_mul_f32 v119, v146, v119\n"
      "v_mfma_f32_16x16x32_bf16 v[108:111], a[16:19], v[206:209], v[108:111]\n"
      "buffer_load_dwordx4 v19, s[16:19], s84 offen  lds\n"
      "v_mfma_f32_16x16x32_bf16 v[108:111], a[20:23], v[152:155], v[108:111]\n"
      "v_mul_f32 v204, v218, v204\n"
      "v_mul_f32 v100, v144, v100\n"
      "v_mfma_f32_16x16x32_bf16 v[122:125], a[36:39], v[126:129], v[122:125]\n"
      "v_mul_f32 v157, v145, v157\n"
      "v_mul_f32 v159, v145, v159\n"
      "v_mfma_f32_16x16x32_bf16 v[122:125], a[40:43], v[206:209], v[122:125]\n"
      "s_add_u32 m0, s65, 16384\n"
      "v_cvt_pk_bf16_f32 v204, v204, v204\n"
      "v_mul_f32 v166, v144, v166\n"
      "v_mfma_f32_16x16x32_bf16 v[122:125], a[44:47], v[152:155], v[122:125]\n"
      "v_fma_f32 v77, v108, v108, s51\n"
      "v_mul_f32 v141, v146, v141\n"
      "v_mfma_f32_16x16x32_bf16 v[188:191], a[60:63], v[126:129], v[188:191]\n"
      "buffer_load_dwordx4 v15, s[16:19], s84 offen  lds\n"
      "v_mfma_f32_16x16x32_bf16 v[188:191], a[64:67], v[206:209], v[188:191]\n"
      "ds_write_b16 v12, v204\n"
      "v_mfma_f32_16x16x32_bf16 v[188:191], a[68:71], v[152:155], v[188:191]\n"
      "v_mul_f32 v148, v144, v148\n"
      "v_fmac_f32 v77, v109, v109\n"
      "v_mfma_f32_16x16x32_bf16 v[104:107], a[84:87], v[126:129], v[104:107]\n"
      "v_mul_f32 v165, v144, v165\n"
      "v_mul_f32 v162, v145, v162\n"
      "v_mfma_f32_16x16x32_bf16 v[104:107], a[88:91], v[206:209], v[104:107]\n"
      "v_mul_f32_e64 v158, v145, v158\n"
      "v_cvt_pk_bf16_f32 v164, v164, v165\n"
      "v_mfma_f32_16x16x32_bf16 v[104:107], a[92:95], v[152:155], v[104:107]\n"
      "buffer_load_dwordx4 v[96:99], v36, s[28:31], s86 offen\n"
      "v_mfma_f32_16x16x32_bf16 v[176:179], a[108:111], v[126:129], v[176:179]\n"
      "v_mul_f32 v149, v144, v149\n"
      "v_mul_f32 v142, v146, v142\n"
      "v_mfma_f32_16x16x32_bf16 v[176:179], a[112:115], v[206:209], v[176:179]\n"
      "v_mul_f32 v103, v144, v103\n"
      "v_mul_f32 v161, v145, v161\n"
      "v_mfma_f32_16x16x32_bf16 v[176:179], a[116:119], v[152:155], v[176:179]\n"
      "v_fma_f32 v201, v104, v104, s51\n"
      "v_mul_f32_e64 v160, v145, v160\n"
      "v_mfma_f32_16x16x32_bf16 v[184:187], a[132:135], v[126:129], v[184:187]\n"
      "v_cvt_pk_bf16_f32 v156, v156, v157\n"
      "v_fmac_f32_e64 v201, v105, v105\n"
      "v_mfma_f32_16x16x32_bf16 v[184:187], a[136:139], v[206:209], v[184:187]\n"
      "v_cvt_pk_bf16_f32 v165, v166, v167\n"
      "v_mul_f32 v195, v146, v195\n"
      "v_mfma_f32_16x16x32_bf16 v[184:187], a[140:143], v[152:155], v[184:187]\n"
      "v_fmac_f32 v201, v106, v106\n"
      "v_mul_f32_e64 v151, v144, v151\n"
      "v_mfma_f32_16x16x32_bf16 v[196:199], v[112:115], a[144:147], 0\n"
      "v_fmac_f32 v77, v110, v110\n"
      "v_mul_f32 v163, v145, v163\n"
      "v_mfma_f32_16x16x32_bf16 v[196:199], v[172:175], a[148:151], v[196:199]\n"
      "v_fmac_f32 v77, v111, v111\n"
      "v_fmac_f32 v77, v122, v122\n"
      "v_mfma_f32_16x16x32_bf16 v[196:199], v[210:213], a[152:155], v[196:199]\n"
      "ds_read_b128 v[202:205], v1 offset:26624\n"
      "v_fmac_f32_e64 v201, v107, v107\n"
      "v_mfma_f32_16x16x32_bf16 v[196:199], v[126:129], a[156:159], v[196:199]\n"
      "buffer_load_dword v88, v29, s[28:31], s86 offen\n"
      "v_mfma_f32_16x16x32_bf16 v[196:199], v[206:209], a[160:163], v[196:199]\n"
      "v_mul_f32 v136, v145, v136\n"
      "v_fmac_f32 v77, v123, v123\n"
      "v_mfma_f32_16x16x32_bf16 v[196:199], v[152:155], a[164:167], v[196:199]\n"
      "v_cvt_pk_bf16_f32 v148, v148, v149\n"
      "v_mul_f32_e64 v138, v145, v138\n"
      "v_mfma_f32_16x16x32_bf16 v[214:217], v[112:115], a[168:171], 0\n"
      "ds_write_b64 v6, v[164:165] offset:18432\n"
      "v_mfma_f32_16x16x32_bf16 v[214:217], v[172:175], a[172:175], v[214:217]\n"
      "v_cvt_pk_bf16_f32 v157, v158, v159\n"
      "v_fmac_f32 v201, v176, v176\n"
      "v_mfma_f32_16x16x32_bf16 v[214:217], v[210:213], a[176:179], v[214:217]\n"
      "v_fmac_f32 v201, v177, v177\n"
      "v_cmp_ne_u32 s[56:57], 0, v130\n"
      "v_mfma_f32_16x16x32_bf16 v[214:217], v[126:129], a[180:183], v[214:217]\n"
      "ds_read_b128 v[168:171], v0 offset:20480\n"
      "v_cvt_pk_bf16_f32 v158, v160, v161\n"
      "v_mfma_f32_16x16x32_bf16 v[214:217], v[206:209], a[184:187], v[214:217]\n"
      "v_fmac_f32 v201, v178, v178\n"
      "v_mul_f32 v150, v144, v150\n"
      "v_mfma_f32_16x16x32_bf16 v[214:217], v[152:155], a[188:191], v[214:217]\n"
      "v_cvt_pk_bf16_f32 v159, v162, v163\n"
      "v_fmac_f32 v201, v179, v179\n"
      "v_mfma_f32_16x16x32_bf16 v[130:133], v[112:115], a[192:195], 0\n"
      "ds_read_b128 v[160:163], v1 offset:20480\n"
      "v_mul_f32 v116, v146, v116\n"
      "v_mfma_f32_16x16x32_bf16 v[130:133], v[172:175], a[196:199], v[130:133]\n"
      "ds_read_b128 v[112:115], v0 offset:26624\n"
      "v_mul_f32 v102, v144, v102\n"
      "v_mfma_f32_16x16x32_bf16 v[130:133], v[210:213], a[200:203], v[130:133]\n"
      "s_or_b32 s95, s45, s46\n"
      "v_mul_f32 v101, v144, v101\n"
      "v_mul_f32 v193, v146, v193\n"
      "v_mfma_f32_16x16x32_bf16 v[130:133], v[126:129], a[204:207], v[130:133]\n"
      "ds_read_b128 v[172:175], v1 offset:32768\n"
      "v_cvt_pk_bf16_f32 v214, v214, v215\n"
      "v_mfma_f32_16x16x32_bf16 v[130:133], v[206:209], a[208:211], v[130:133]\n"
      "v_fmac_f32 v201, v184, v184\n"
      "v_cvt_pk_bf16_f32 v196, v196, v197\n"
      "v_mfma_f32_16x16x32_bf16 v[130:133], v[152:155], a[212:215], v[130:133]\n"
      "s_waitcnt lgkmcnt(3)\n"
      "v_mul_f32 v143, v146, v143\n"
      "v_mul_f32 v192, v146, v192\n"
      "v_mfma_f32_16x16x32_bf16 v[152:155], a[0:3], v[168:171], 0\n"
      "s_waitcnt lgkmcnt(2)\n"
      "v_mul_f32 v140, v146, v140\n"
      "v_mul_f32 v117, v146, v117\n"
      "v_mfma_f32_16x16x32_bf16 v[152:155], a[4:7], v[160:163], v[152:155]\n"
      "s_waitcnt lgkmcnt(1)\n"
      "v_mul_f32 v118, v146, v118\n"
      "v_cvt_pk_bf16_f32 v149, v150, v151\n"
      "v_mfma_f32_16x16x32_bf16 v[152:155], a[8:11], v[112:115], v[152:155]\n"
      "v_cvt_pk_bf16_f32 v140, v140, v141\n"
      "v_fmac_f32 v77, v124, v124\n"
      "v_mfma_f32_16x16x32_bf16 v[152:155], a[12:15], v[202:205], v[152:155]\n"
      "v_mul_f32 v139, v145, v139\n"
      "v_cvt_pk_bf16_f32 v150, v100, v101\n"
      "v_mfma_f32_16x16x32_bf16 v[152:155], a[16:19], v[180:183], v[152:155]\n"
      "s_waitcnt lgkmcnt(0)\n"
      "v_fmac_f32_e64 v77, v125, v125\n"
      "v_cvt_pk_bf16_f32 v116, v116, v117\n"
      "v_mfma_f32_16x16x32_bf16 v[152:155], a[20:23], v[172:175], v[152:155]\n"
      "ds_write_b128 v5, v[156:159] offset:24576\n"
      "v_mfma_f32_16x16x32_bf16 v[144:147], a[24:27], v[168:171], 0\n"
      "v_cvt_pk_bf16_f32 v151, v102, v103\n"
      "v_fmac_f32_e64 v201, v185, v185\n"
      "v_mfma_f32_16x16x32_bf16 v[144:147], a[28:31], v[160:163], v[144:147]\n"
      "ds_read_b128 v[126:129], v1 offset:22528\n"
      "v_fmac_f32_e64 v77, v188, v188\n"
      "v_mfma_f32_16x16x32_bf16 v[144:147], a[32:35], v[112:115], v[144:147]\n"
      "ds_write_b128 v5, v[148:151] offset:18432\n"
      "v_mfma_f32_16x16x32_bf16 v[144:147], a[36:39], v[202:205], v[144:147]\n"
      "v_cvt_pk_bf16_f32 v215, v216, v217\n"
      "v_fmac_f32_e64 v77, v189, v189\n"
      "v_mfma_f32_16x16x32_bf16 v[144:147], a[40:43], v[180:183], v[144:147]\n"
      "v_cvt_pk_bf16_f32 v117, v118, v119\n"
      "v_fma_f32 v120, v152, v152, s51\n"
      "v_mfma_f32_16x16x32_bf16 v[144:147], a[44:47], v[172:175], v[144:147]\n"
      "v_fmac_f32 v77, v190, v190\n"
      "v_fmac_f32 v120, v153, v153\n"
      "v_mfma_f32_16x16x32_bf16 v[206:209], a[48:51], v[168:171], 0\n"
      "v_fmac_f32_e64 v120, v154, v154\n"
      "v_cvt_pk_bf16_f32 v119, v194, v195\n"
      "v_mfma_f32_16x16x32_bf16 v[206:209], a[52:55], v[160:163], v[206:209]\n"
      "ds_read_b128 v[148:151], v0 offset:28672\n"
      "v_cvt_pk_bf16_f32 v118, v192, v193\n"
      "v_mfma_f32_16x16x32_bf16 v[206:209], a[56:59], v[112:115], v[206:209]\n"
      "v_fmac_f32 v201, v186, v186\n"
      "v_fmac_f32 v201, v187, v187\n"
      "v_mfma_f32_16x16x32_bf16 v[206:209], a[60:63], v[202:205], v[206:209]\n"
      "ds_write_b128 v5, v[116:119] offset:30720\n"
      "v_mfma_f32_16x16x32_bf16 v[206:209], a[64:67], v[180:183], v[206:209]\n"
      "v_fmac_f32 v120, v155, v155\n"
      "v_fmac_f32 v77, v191, v191\n"
      "v_mfma_f32_16x16x32_bf16 v[206:209], a[68:71], v[172:175], v[206:209]\n"
      "s_and_b32 s47, s47, -4\n"
      "v_permlane16_swap_b32 v77, v201\n"
      "v_mfma_f32_16x16x32_bf16 v[192:195], a[72:75], v[168:171], 0\n"
      "v_add_f32 v77, v77, v201\n"
      "v_fmac_f32 v120, v144, v144\n"
      "v_mfma_f32_16x16x32_bf16 v[192:195], a[76:79], v[160:163], v[192:195]\n"
      "v_cvt_pk_bf16_f32 v130, v130, v131\n"
      "v_mov_b32 v201, v77\n"
      "v_mfma_f32_16x16x32_bf16 v[192:195], a[80:83], v[112:115], v[192:195]\n"
      "s_nop 0\n"
      "v_permlane32_swap_b32 v77, v201\n"
      "v_mfma_f32_16x16x32_bf16 v[192:195], a[84:87], v[202:205], v[192:195]\n"
      "v_cvt_pk_bf16_f32 v197, v198, v199\n"
      "v_add_f32 v77, v77, v201\n"
      "v_mfma_f32_16x16x32_bf16 v[192:195], a[88:91], v[180:183], v[192:195]\n"
      "v_rsq_f32 v77, v77\n"
      "v_mfma_f32_16x16x32_bf16 v[192:195], a[92:95], v[172:175], v[192:195]\n"
      "v_mov_b32 v201, v77\n"
      "v_fmac_f32 v120, v145, v145\n"
      "v_mfma_f32_16x16x32_bf16 v[156:159], a[96:99], v[168:171], 0\n"
      "v_permlane16_swap_b32 v77, v201\n"
      "v_mfma_f32_16x16x32_bf16 v[156:159], a[100:103], v[160:163], v[156:159]\n"
      "v_cvt_pk_bf16_f32 v136, v136, v137\n"
      "v_mul_f32_e64 v77, s50, v77\n"
      "v_mfma_f32_16x16x32_bf16 v[156:159], a[104:107], v[112:115], v[156:159]\n"
      "v_fma_f32 v121, v192, v192, s51\n"
      "v_fmac_f32 v121, v193, v193\n"
      "v_mfma_f32_16x16x32_bf16 v[156:159], a[108:111], v[202:205], v[156:159]\n"
      "v_cvt_pk_bf16_f32 v141, v142, v143\n"
      "v_mul_f32 v107, v201, v107\n"
      "v_mfma_f32_16x16x32_bf16 v[156:159], a[112:115], v[180:183], v[156:159]\n"
      "v_mul_f32 v122, v77, v122\n"
      "v_fmac_f32 v120, v146, v146\n"
      "v_mfma_f32_16x16x32_bf16 v[156:159], a[116:119], v[172:175], v[156:159]\n"
      "v_cvt_pk_bf16_f32 v137, v138, v139\n"
      "v_mul_f32 v108, v77, v108\n"
      "v_mfma_f32_16x16x32_bf16 v[116:119], a[120:123], v[168:171], 0\n"
      "v_fmac_f32 v121, v194, v194\n"
      "v_fmac_f32 v121, v195, v195\n"
      "v_mfma_f32_16x16x32_bf16 v[116:119], a[124:127], v[160:163], v[116:119]\n"
      "v_mul_f32 v189, v77, v189\n"
      "v_cvt_pk_bf16_f32 v131, v132, v133\n"
      "v_mfma_f32_16x16x32_bf16 v[116:119], a[128:131], v[112:115], v[116:119]\n"
      "ds_write_b64 v6, v[140:141] offset:30720\n"
      "v_mfma_f32_16x16x32_bf16 v[116:119], a[132:135], v[202:205], v[116:119]\n"
      "v_fmac_f32 v121, v156, v156\n"
      "v_fmac_f32 v120, v147, v147\n"
      "v_mfma_f32_16x16x32_bf16 v[116:119], a[136:139], v[180:183], v[116:119]\n"
      "ds_read_b128 v[100:103], v1 offset:28672\n"
      "v_mul_f32_e64 v105, v201, v105\n"
      "v_mfma_f32_16x16x32_bf16 v[116:119], a[140:143], v[172:175], v[116:119]\n"
      "v_fmac_f32 v121, v157, v157\n"
      "v_mul_f32 v125, v77, v125\n"
      "v_mfma_f32_16x16x32_bf16 v[216:219], v[168:171], a[144:147], 0\n"
      "ds_write_b64 v6, v[136:137] offset:24576\n"
      "v_mfma_f32_16x16x32_bf16 v[216:219], v[160:163], a[148:151], v[216:219]\n"
      "v_mul_f32 v104, v201, v104\n"
      "v_fmac_f32 v121, v158, v158\n"
      "v_mfma_f32_16x16x32_bf16 v[216:219], v[112:115], a[152:155], v[216:219]\n"
      "v_mul_f32 v190, v77, v190\n"
      "v_fmac_f32 v121, v159, v159\n"
      "v_mfma_f32_16x16x32_bf16 v[216:219], v[202:205], a[156:159], v[216:219]\n"
      "v_fmac_f32 v120, v206, v206\n"
      "v_fmac_f32 v120, v207, v207\n"
      "v_mfma_f32_16x16x32_bf16 v[216:219], v[180:183], a[160:163], v[216:219]\n"
      "v_fmac_f32 v121, v116, v116\n"
      "v_fmac_f32 v121, v117, v117\n"
      "v_mfma_f32_16x16x32_bf16 v[216:219], v[172:175], a[164:167], v[216:219]\n"
      "v_fmac_f32 v121, v118, v118\n"
      "v_fmac_f32 v120, v208, v208\n"
      "v_mfma_f32_16x16x32_bf16 v[138:141], v[168:171], a[168:171], 0\n"
      "v_fmac_f32 v120, v209, v209\n"
      "v_fmac_f32 v121, v119, v119\n"
      "v_mfma_f32_16x16x32_bf16 v[138:141], v[160:163], a[172:175], v[138:141]\n"
      "s_or_b32 s95, s95, s48\n"
      "v_mul_f32 v110, v77, v110\n"
      "v_mul_f32 v109, v77, v109\n"
      "v_mfma_f32_16x16x32_bf16 v[138:141], v[112:115], a[176:179], v[138:141]\n"
      "v_mul_f32 v185, v201, v185\n"
      "v_mul_f32 v187, v201, v187\n"
      "v_mfma_f32_16x16x32_bf16 v[138:141], v[202:205], a[180:183], v[138:141]\n"
      "v_permlane16_swap_b32 v120, v121\n"
      "v_mfma_f32_16x16x32_bf16 v[138:141], v[180:183], a[184:187], v[138:141]\n"
      "v_add_f32 v120, v120, v121\n"
      "v_mov_b32 v121, v120\n"
      "v_mfma_f32_16x16x32_bf16 v[138:141], v[172:175], a[188:191], v[138:141]\n"
      "ds_read_b128 v[134:137], v0 offset:22528\n"
      "v_mul_f32_e64 v178, v201, v178\n"
      "v_mfma_f32_16x16x32_bf16 v[210:213], v[168:171], a[192:195], 0\n"
      "v_cvt_pk_bf16_f32 v108, v108, v109\n"
      "v_mfma_f32_16x16x32_bf16 v[210:213], v[160:163], a[196:199], v[210:213]\n"
      "v_permlane32_swap_b32 v120, v121\n"
      "v_mfma_f32_16x16x32_bf16 v[210:213], v[112:115], a[200:203], v[210:213]\n"
      "v_mul_f32 v111, v77, v111\n"
      "v_mul_f32 v124, v77, v124\n"
      "v_mfma_f32_16x16x32_bf16 v[210:213], v[202:205], a[204:207], v[210:213]\n"
      "ds_read_b128 v[164:167], v1 offset:34816\n"
      "v_add_f32 v120, v120, v121\n"
      "v_mfma_f32_16x16x32_bf16 v[210:213], v[180:183], a[208:211], v[210:213]\n"
      "v_rsq_f32 v120, v120\n"
      "v_mfma_f32_16x16x32_bf16 v[210:213], v[172:175], a[212:215], v[210:213]\n"
      "s_waitcnt lgkmcnt(1)\n"
      "v_cvt_pk_bf16_f32 v198, v216, v217\n"
      "v_mov_b32_e64 v121, v120\n"
      "v_mfma_f32_16x16x32_bf16 v[180:183], a[0:3], v[134:137], 0\n"
      "s_nop 0\n"
      "v_permlane16_swap_b32 v120, v121\n"
      "v_mfma_f32_16x16x32_bf16 v[180:183], a[4:7], v[126:129], v[180:183]\n"
      "v_mul_f32 v120, s50, v120\n"
      "v_mul_f32 v159, v121, v159\n"
      "v_mfma_f32_16x16x32_bf16 v[180:183], a[8:11], v[148:151], v[180:183]\n"
      "v_mul_f32 v192, v121, v192\n"
      "v_mul_f32 v193, v121, v193\n"
      "v_mfma_f32_16x16x32_bf16 v[180:183], a[12:15], v[100:103], v[180:183]\n"
      "v_mul_f32 v123, v77, v123\n"
      "v_mul_f32 v154, v120, v154\n"
      "v_mfma_f32_16x16x32_bf16 v[180:183], a[16:19], v[220:223], v[180:183]\n"
      "s_waitcnt lgkmcnt(0)\n"
      "v_mul_f32 v158, v121, v158\n"
      "v_mul_f32 v195, v121, v195\n"
      "v_mfma_f32_16x16x32_bf16 v[180:183], a[20:23], v[164:167], v[180:183]\n"
      "v_mul_f32 v145, v120, v145\n"
      "v_mul_f32 v106, v201, v106\n"
      "v_mfma_f32_16x16x32_bf16 v[160:163], a[24:27], v[134:137], 0\n"
      "v_mul_f32 v184, v201, v184\n"
      "v_mul_f32 v194, v121, v194\n"
      "v_mfma_f32_16x16x32_bf16 v[160:163], a[28:31], v[126:129], v[160:163]\n"
      "v_mul_f32 v153, v120, v153\n"
      "v_cvt_pk_bf16_f32 v192, v192, v193\n"
      "v_mfma_f32_16x16x32_bf16 v[160:163], a[32:35], v[148:151], v[160:163]\n"
      "v_mul_f32_e64 v207, v120, v207\n"
      "v_fma_f32 v81, v180, v180, s51\n"
      "v_mfma_f32_16x16x32_bf16 v[160:163], a[36:39], v[100:103], v[160:163]\n"
      "v_cvt_pk_bf16_f32 v216, v138, v139\n"
      "v_mul_f32 v206, v120, v206\n"
      "v_mfma_f32_16x16x32_bf16 v[160:163], a[40:43], v[220:223], v[160:163]\n"
      "v_mul_f32 v186, v201, v186\n"
      "v_fmac_f32 v81, v181, v181\n"
      "v_mfma_f32_16x16x32_bf16 v[160:163], a[44:47], v[164:167], v[160:163]\n"
      "v_mul_f32 v116, v121, v116\n"
      "v_cvt_pk_bf16_f32 v104, v104, v105\n"
      "v_mfma_f32_16x16x32_bf16 v[168:171], a[48:51], v[134:137], 0\n"
      "v_cvt_pk_bf16_f32 v105, v106, v107\n"
      "v_cvt_pk_bf16_f32 v206, v206, v207\n"
      "v_mfma_f32_16x16x32_bf16 v[168:171], a[52:55], v[126:129], v[168:171]\n"
      "v_mul_f32 v176, v201, v176\n"
      "v_mul_f32 v156, v121, v156\n"
      "v_mfma_f32_16x16x32_bf16 v[168:171], a[56:59], v[148:151], v[168:171]\n"
      "v_cvt_pk_bf16_f32 v193, v194, v195\n"
      "v_mul_f32 v179, v201, v179\n"
      "v_mfma_f32_16x16x32_bf16 v[168:171], a[60:63], v[100:103], v[168:171]\n"
      "s_cmp_ge_u32 s95, 0x40000000\n"
      "s_cselect_b32 s94, -1, -2\n"
      "v_cvt_pk_bf16_f32 v199, v218, v219\n"
      "v_mfma_f32_16x16x32_bf16 v[168:171], a[64:67], v[220:223], v[168:171]\n"
      "v_mul_f32 v117, v121, v117\n"
      "v_cvt_pk_bf16_f32 v109, v110, v111\n"
      "v_mfma_f32_16x16x32_bf16 v[168:171], a[68:71], v[164:167], v[168:171]\n"
      "v_cvt_pk_bf16_f32 v110, v122, v123\n"
      "v_mul_f32 v152, v120, v152\n"
      "v_mfma_f32_16x16x32_bf16 v[172:175], a[72:75], v[134:137], 0\n"
      "v_fmac_f32 v81, v182, v182\n"
      "v_mul_f32 v177, v201, v177\n"
      "v_mfma_f32_16x16x32_bf16 v[172:175], a[76:79], v[126:129], v[172:175]\n"
      "v_fmac_f32 v81, v183, v183\n"
      "v_fmac_f32_e64 v81, v160, v160\n"
      "v_mfma_f32_16x16x32_bf16 v[172:175], a[80:83], v[148:151], v[172:175]\n"
      "v_cvt_pk_bf16_f32 v184, v184, v185\n"
      "v_cvt_pk_bf16_f32 v152, v152, v153\n"
      "v_mfma_f32_16x16x32_bf16 v[172:175], a[84:87], v[100:103], v[172:175]\n"
      "v_mul_f32_e64 v188, v77, v188\n"
      "v_cvt_pk_bf16_f32 v116, v116, v117\n"
      "v_mfma_f32_16x16x32_bf16 v[172:175], a[88:91], v[220:223], v[172:175]\n"
      "v_mul_f32 v157, v121, v157\n"
      "v_mul_f32 v208, v120, v208\n"
      "v_mfma_f32_16x16x32_bf16 v[172:175], a[92:95], v[164:167], v[172:175]\n"
      "v_mul_f32 v144, v120, v144\n"
      "v_cvt_pk_bf16_f32 v195, v158, v159\n"
      "v_mfma_f32_16x16x32_bf16 v[112:115], a[96:99], v[134:137], 0\n"
      "v_cvt_pk_bf16_f32 v133, v212, v213\n"
      "v_fmac_f32 v81, v161, v161\n"
      "v_mfma_f32_16x16x32_bf16 v[112:115], a[100:103], v[126:129], v[112:115]\n"
      "v_cvt_pk_bf16_f32 v217, v140, v141\n"
      "v_fmac_f32_e64 v81, v162, v162\n"
      "v_mfma_f32_16x16x32_bf16 v[112:115], a[104:107], v[148:151], v[112:115]\n"
      "v_cvt_pk_bf16_f32 v106, v176, v177\n"
      "v_mul_f32_e64 v146, v120, v146\n"
      "v_mfma_f32_16x16x32_bf16 v[112:115], a[108:111], v[100:103], v[112:115]\n"
      "v_fma_f32 v204, v172, v172, s51\n"
      "v_cvt_pk_bf16_f32 v188, v188, v189\n"
      "v_mfma_f32_16x16x32_bf16 v[112:115], a[112:115], v[220:223], v[112:115]\n"
      "v_fmac_f32 v204, v173, v173\n"
      "v_fmac_f32 v204, v174, v174\n"
      "v_mfma_f32_16x16x32_bf16 v[112:115], a[116:119], v[164:167], v[112:115]\n"
      "v_fmac_f32_e64 v81, v163, v163\n"
      "v_cvt_pk_bf16_f32 v185, v186, v187\n"
      "v_mfma_f32_16x16x32_bf16 v[200:203], a[120:123], v[134:137], 0\n"
      "v_cvt_pk_bf16_f32 v194, v156, v157\n"
      "v_cvt_pk_bf16_f32 v107, v178, v179\n"
      "v_mfma_f32_16x16x32_bf16 v[200:203], a[124:127], v[126:129], v[200:203]\n"
      "v_mul_f32 v147, v120, v147\n"
      "v_mul_f32 v119, v121, v119\n"
      "v_mfma_f32_16x16x32_bf16 v[200:203], a[128:131], v[148:151], v[200:203]\n"
      "v_mul_f32 v118, v121, v118\n"
      "v_cvt_pk_bf16_f32 v117, v118, v119\n"
      "v_mfma_f32_16x16x32_bf16 v[200:203], a[132:135], v[100:103], v[200:203]\n"
      "s_cmp_gt_u32 s42, s44\n"
      "s_cselect_b32 s95, s94, s95\n"
      "v_fmac_f32 v81, v168, v168\n"
      "v_mfma_f32_16x16x32_bf16 v[200:203], a[136:139], v[220:223], v[200:203]\n"
      "v_cvt_pk_bf16_f32 v111, v124, v125\n"
      "v_mul_f32 v155, v120, v155\n"
      "v_mfma_f32_16x16x32_bf16 v[200:203], a[140:143], v[164:167], v[200:203]\n"
      "v_mul_f32 v209, v120, v209\n"
      "v_fmac_f32 v81, v169, v169\n"
      "v_mfma_f32_16x16x32_bf16 v[138:141], v[134:137], a[144:147], 0\n"
      "v_fmac_f32 v204, v175, v175\n"
      "v_fmac_f32 v204, v112, v112\n"
      "v_mfma_f32_16x16x32_bf16 v[138:141], v[126:129], a[148:151], v[138:141]\n"
      "ds_read_b128 v[122:125], v2 offset:6144\n"
      "v_fmac_f32 v204, v113, v113\n"
      "v_mfma_f32_16x16x32_bf16 v[138:141], v[148:151], a[152:155], v[138:141]\n"
      "v_fmac_f32 v204, v114, v114\n"
      "v_mul_f32 v191, v77, v191\n"
      "v_mfma_f32_16x16x32_bf16 v[138:141], v[100:103], a[156:159], v[138:141]\n"
      "v_fmac_f32 v204, v115, v115\n"
      "v_fmac_f32 v204, v200, v200\n"
      "v_mfma_f32_16x16x32_bf16 v[138:141], v[220:223], a[160:163], v[138:141]\n"
      "v_cvt_pk_bf16_f32 v189, v190, v191\n"
      "v_fmac_f32_e64 v81, v170, v170\n"
      "v_mfma_f32_16x16x32_bf16 v[138:141], v[164:167], a[164:167], v[138:141]\n"
      "v_fmac_f32 v204, v201, v201\n"
      "v_fmac_f32 v81, v171, v171\n"
      "v_mfma_f32_16x16x32_bf16 v[156:159], v[134:137], a[168:171], 0\n"
      "v_fmac_f32 v204, v202, v202\n"
      "v_fmac_f32 v204, v203, v203\n"
      "v_mfma_f32_16x16x32_bf16 v[156:159], v[126:129], a[172:175], v[156:159]\n"
      "s_nop 0\n"
      "v_permlane16_swap_b32 v81, v204\n"
      "v_mfma_f32_16x16x32_bf16 v[156:159], v[148:151], a[176:179], v[156:159]\n"
      "v_add_f32 v81, v81, v204\n"
      "v_mov_b32 v204, v81\n"
      "v_mfma_f32_16x16x32_bf16 v[156:159], v[100:103], a[180:183], v[156:159]\n"
      "ds_read_b128 v[176:179], v2 offset:3072\n"
      "v_cvt_pk_bf16_f32 v77, v140, v141\n"
      "v_mfma_f32_16x16x32_bf16 v[156:159], v[220:223], a[184:187], v[156:159]\n"
      "v_permlane32_swap_b32 v81, v204\n"
      "v_mfma_f32_16x16x32_bf16 v[156:159], v[164:167], a[188:191], v[156:159]\n"
      "ds_read_b128 v[140:143], v2\n"
      "v_add_f32 v81, v81, v204\n"
      "v_mfma_f32_16x16x32_bf16 v[118:121], v[134:137], a[192:195], 0\n"
      "v_rsq_f32 v81, v81\n"
      "v_mfma_f32_16x16x32_bf16 v[118:121], v[126:129], a[196:199], v[118:121]\n"
      "ds_read_b128 v[134:137], v2 offset:9216\n"
      "v_mov_b32 v204, v81\n"
      "v_mfma_f32_16x16x32_bf16 v[118:121], v[148:151], a[200:203], v[118:121]\n"
      "s_nop 0\n"
      "v_permlane16_swap_b32 v81, v204\n"
      "v_mfma_f32_16x16x32_bf16 v[118:121], v[100:103], a[204:207], v[118:121]\n"
      "v_mul_f32_e64 v81, s50, v81\n"
      "v_cvt_pk_bf16_f32 v153, v154, v155\n"
      "v_mfma_f32_16x16x32_bf16 v[118:121], v[220:223], a[208:211], v[118:121]\n"
      "ds_read_b128 v[126:129], v2 offset:12288\n"
      "v_mul_f32 v181, v81, v181\n"
      "v_mfma_f32_16x16x32_bf16 v[118:121], v[164:167], a[212:215], v[118:121]\n"
      "s_waitcnt lgkmcnt(2)\n"
      "v_mul_f32 v202, v204, v202\n"
      "v_cvt_pk_bf16_f32 v154, v144, v145\n"
      "v_mfma_f32_16x16x32_bf16 v[148:151], a[216:219], v[140:143], 0\n"
      "v_mul_f32 v114, v204, v114\n"
      "v_cvt_pk_bf16_f32 v80, v156, v157\n"
      "v_mfma_f32_16x16x32_bf16 v[148:151], a[220:223], v[176:179], v[148:151]\n"
      "v_mul_f32 v115, v204, v115\n"
      "v_mul_f32 v161, v81, v161\n"
      "v_mfma_f32_16x16x32_bf16 v[148:151], a[224:227], v[122:125], v[148:151]\n"
      "ds_read_b128 v[218:221], v2 offset:15360\n"
      "v_cvt_pk_bf16_f32 v132, v210, v211\n"
      "v_mfma_f32_16x16x32_bf16 v[100:103], a[240:243], v[140:143], 0\n"
      "v_mul_f32 v172, v204, v172\n"
      "v_mul_f32 v168, v81, v168\n"
      "v_mfma_f32_16x16x32_bf16 v[100:103], a[244:247], v[176:179], v[100:103]\n"
      "v_cvt_pk_bf16_f32 v207, v208, v209\n"
      "v_mul_f32 v170, v81, v170\n"
      "v_mfma_f32_16x16x32_bf16 v[100:103], a[248:251], v[122:125], v[100:103]\n"
      "ds_read_b128 v[210:213], v2 offset:6400\n"
      "v_mul_f32 v163, v81, v163\n"
      "v_mfma_f32_16x16x32_bf16 v[164:167], v[232:235], v[140:143], 0\n"
      "ds_read_b64 v[208:209], v10 offset:1192\n"
      "v_mul_f32 v173, v204, v173\n"
      "v_mfma_f32_16x16x32_bf16 v[164:167], v[236:239], v[176:179], v[164:167]\n"
      "v_mul_f32 v183, v81, v183\n"
      "v_mul_f32 v200, v204, v200\n"
      "v_mfma_f32_16x16x32_bf16 v[164:167], v[240:243], v[122:125], v[164:167]\n"
      "s_waitcnt lgkmcnt(2)\n"
      "v_mul_f32 v203, v204, v203\n"
      "v_mul_f32 v169, v81, v169\n"
      "v_mfma_f32_16x16x32_bf16 v[148:151], a[228:231], v[134:137], v[148:151]\n"
      "v_cvt_pk_bf16_f32 v84, v118, v119\n"
      "v_mul_f32 v113, v204, v113\n"
      "v_mfma_f32_16x16x32_bf16 v[148:151], a[232:235], v[126:129], v[148:151]\n"
      "v_mul_f32 v175, v204, v175\n"
      "v_cvt_pk_bf16_f32 v168, v168, v169\n"
      "v_mfma_f32_16x16x32_bf16 v[148:151], a[236:239], v[218:221], v[148:151]\n"
      "ds_read_b128 v[176:179], v2 offset:256\n"
      "v_mul_f32_e64 v180, v81, v180\n"
      "v_mfma_f32_16x16x32_bf16 v[100:103], a[252:255], v[134:137], v[100:103]\n"
      "ds_read_b128 v[122:125], v10 offset:1200\n"
      "v_mul_f32_e64 v112, v204, v112\n"
      "v_mfma_f32_16x16x32_bf16 v[100:103], v[224:227], v[126:129], v[100:103]\n"
      "v_cvt_pk_bf16_f32 v180, v180, v181\n"
      "v_cvt_pk_bf16_f32 v172, v172, v173\n"
      "v_mfma_f32_16x16x32_bf16 v[100:103], v[228:231], v[218:221], v[100:103]\n"
      "v_mul_f32_e64 v174, v204, v174\n"
      "v_cvt_pk_bf16_f32 v173, v174, v175\n"
      "v_mfma_f32_16x16x32_bf16 v[164:167], v[244:247], v[134:137], v[164:167]\n"
      "v_cvt_pk_bf16_f32 v175, v114, v115\n"
      "v_mul_f32_e64 v201, v204, v201\n"
      "v_mfma_f32_16x16x32_bf16 v[164:167], v[248:251], v[126:129], v[164:167]\n"
      "ds_read_b128 v[140:143], v2 offset:3328\n"
      "v_mul_f32 v182, v81, v182\n"
      "v_mfma_f32_16x16x32_bf16 v[164:167], v[252:255], v[218:221], v[164:167]\n"
      "s_add_u32 s43, s43, 1\n"
      "v_cvt_pk_bf16_f32 v174, v112, v113\n"
      "v_fma_f32 v134, v148, v148, s52\n"
      "v_mfma_f32_16x16x32_bf16 v[126:129], v[104:107], v[108:111], v[40:43]\n"
      "ds_read_u16 v82, v11 offset:1200\n"
      "v_cvt_pk_bf16_f32 v200, v200, v201\n"
      "v_mfma_f32_16x16x32_bf16 v[220:223], v[192:195], v[108:111], v[44:47]\n"
      "s_waitcnt lgkmcnt(2)\n"
      "v_cvt_pk_bf16_f32 v201, v202, v203\n"
      "v_mul_f32 v160, v81, v160\n"
      "v_mfma_f32_16x16x32_bf16 v[112:115], v[172:175], v[108:111], v[48:51]\n"
      "ds_read_u16 v86, v11 offset:1232\n"
      "v_mul_f32 v162, v81, v162\n"
      "v_mfma_f32_16x16x32_bf16 v[202:205], v[122:125], v[108:111], v[24:27]\n"
      "v_mul_f32 v171, v81, v171\n"
      "v_cvt_pk_bf16_f32 v181, v182, v183\n"
      "v_mfma_f32_16x16x16_bf16 v[126:129], v[184:185], v[188:189], v[126:129]\n"
      "v_cvt_pk_bf16_f32 v169, v170, v171\n"
      "v_cvt_pk_bf16_f32 v155, v146, v147\n"
      "v_cvt_pk_bf16_f32 v183, v162, v163\n"
      "v_mfma_f32_16x16x16_bf16 v[220:223], v[116:117], v[188:189], v[220:223]\n"
      "v_mul_f32_e64 v135, v149, v149\n"
      "v_cvt_pk_bf16_f32 v182, v160, v161\n"
      "v_cvt_pk_bf16_f32 v76, v138, v139\n"
      "v_mfma_f32_16x16x16_bf16 v[112:115], v[200:201], v[188:189], v[112:115]\n"
      "v_exp_f32_e64 v128, v128\n"
      "v_cvt_pk_bf16_f32 v81, v158, v159\n"
      "v_mfma_f32_16x16x16_bf16 v[202:205], v[208:209], v[188:189], v[202:205]\n"
      "ds_read_b128 v[108:111], v2 offset:9472\n"
      "s_waitcnt vmcnt(1)\n"
      "v_or3_b32 v96, v96, v97, v98\n"
      "v_mfma_f32_16x16x32_bf16 v[156:159], a[216:219], v[176:179], 0\n"
      "s_waitcnt lgkmcnt(1)\n"
      "v_exp_f32 v221, v221\n"
      "v_mfma_f32_16x16x32_bf16 v[156:159], a[220:223], v[140:143], v[156:159]\n"
      "v_exp_f32 v115, v115\n"
      "v_mfma_f32_16x16x32_bf16 v[156:159], a[224:227], v[210:213], v[156:159]\n"
      "ds_read_u16 v78, v11 offset:1168\n"
      "v_cvt_pk_bf16_f32 v85, v120, v121\n"
      "v_mfma_f32_16x16x32_bf16 v[160:163], a[240:243], v[176:179], 0\n"
      "v_exp_f32 v202, v202\n"
      "v_mfma_f32_16x16x32_bf16 v[160:163], a[244:247], v[140:143], v[160:163]\n"
      "v_exp_f32 v126, v126\n"
      "v_mfma_f32_16x16x32_bf16 v[160:163], a[248:251], v[210:213], v[160:163]\n"
      "v_exp_f32 v220, v220\n"
      "v_mfma_f32_16x16x32_bf16 v[136:139], v[232:235], v[176:179], 0\n"
      "v_exp_f32 v223, v223\n"
      "v_mfma_f32_16x16x32_bf16 v[136:139], v[236:239], v[140:143], v[136:139]\n"
      "v_exp_f32 v222, v222\n"
      "v_mfma_f32_16x16x32_bf16 v[136:139], v[240:243], v[210:213], v[136:139]\n"
      "v_exp_f32 v127, v127\n"
      "v_mfma_f32_16x16x32_bf16 v[210:213], v[104:107], v[152:155], v[52:55]\n"
      "v_exp_f32_e64 v129, v129\n"
      "v_mfma_f32_16x16x32_bf16 v[118:121], v[192:195], v[152:155], v[56:59]\n"
      "v_exp_f32 v112, v112\n"
      "v_mfma_f32_16x16x32_bf16 v[176:179], v[172:175], v[152:155], v[60:63]\n"
      "v_exp_f32 v113, v113\n"
      "v_mfma_f32_16x16x32_bf16 v[188:191], v[122:125], v[152:155], v[26:29]\n"
      "v_exp_f32 v114, v114\n"
      "v_mfma_f32_16x16x16_bf16 v[210:213], v[184:185], v[206:207], v[210:213]\n"
      "s_waitcnt vmcnt(0)\n"
      "v_or3_b32 v96, v96, v99, v88\n"
      "v_cvt_pk_bf16_f32 v88, v112, v113\n"
      "v_cvt_pk_bf16_f32 v126, v126, v127\n"
      "v_mfma_f32_16x16x16_bf16 v[118:121], v[116:117], v[206:207], v[118:121]\n"
      "v_cvt_pk_bf16_f32 v127, v128, v129\n"
      "v_cvt_pk_bf16_f32 v129, v222, v223\n"
      "v_cvt_pk_bf16_f32 v89, v114, v115\n"
      "v_mfma_f32_16x16x16_bf16 v[176:179], v[200:201], v[206:207], v[176:179]\n"
      "v_exp_f32_e64 v213, v213\n"
      "v_cvt_pk_bf16_f32 v128, v220, v221\n"
      "v_mfma_f32_16x16x16_bf16 v[188:191], v[208:209], v[206:207], v[188:191]\n"
      "v_cvt_pk_bf16_f32 v90, v202, 0\n"
      "v_exp_f32 v121, v121\n"
      "v_mfma_f32_16x16x32_bf16 v[140:143], v[32:35], v[126:129], 0\n"
      "v_exp_f32 v210, v210\n"
      "v_mfma_f32_16x16x32_bf16 v[140:143], v[32:35], v[88:91], v[140:143]\n"
      "v_exp_f32 v178, v178\n"
      "v_exp_f32 v119, v119\n"
      "v_mfma_f32_16x16x32_bf16 v[202:205], v[196:199], v[126:129], 0\n"
      "s_waitcnt lgkmcnt(0)\n"
      "v_exp_f32 v212, v212\n"
      "v_mfma_f32_16x16x32_bf16 v[202:205], v[76:79], v[88:91], v[202:205]\n"
      "v_exp_f32 v211, v211\n"
      "v_mfma_f32_16x16x32_bf16 v[218:221], v[214:217], v[126:129], 0\n"
      "v_exp_f32 v118, v118\n"
      "v_mfma_f32_16x16x32_bf16 v[218:221], v[80:83], v[88:91], v[218:221]\n"
      "ds_write_b32 v31, v96 offset:5632\n"
      "v_mfma_f32_16x16x32_bf16 v[96:99], v[130:133], v[126:129], 0\n"
      "v_rcp_f32_e64 v140, v140\n"
      "v_cvt_pk_bf16_f32 v210, v210, v211\n"
      "v_mfma_f32_16x16x32_bf16 v[96:99], v[84:87], v[88:91], v[96:99]\n"
      "v_exp_f32_e64 v120, v120\n"
      "v_mfma_f32_16x16x32_bf16 v[144:147], v[104:107], v[180:183], v[64:67]\n"
      "v_cvt_pk_bf16_f32 v211, v212, v213\n"
      "v_cvt_pk_bf16_f32 v213, v120, v121\n"
      "v_exp_f32_e64 v177, v177\n"
      "v_cvt_pk_bf16_f32 v212, v118, v119\n"
      "v_pk_fma_f32 v[134:135], v[150:151], v[150:151], v[134:135]\n"
      "v_pk_mul_f32 v[202:203], v[202:203], v[140:141] op_sel_hi:[1,0]\n"
      "v_pk_mul_f32 v[98:99], v[98:99], v[140:141] op_sel_hi:[1,0]\n"
      "v_pk_mul_f32 v[220:221], v[220:221], v[140:141] op_sel_hi:[1,0]\n"
      "v_pk_mul_f32 v[204:205], v[204:205], v[140:141] op_sel_hi:[1,0]\n"
      "v_pk_mul_f32 v[218:219], v[218:219], v[140:141] op_sel_hi:[1,0]\n"
      "v_pk_mul_f32 v[96:97], v[96:97], v[140:141] op_sel_hi:[1,0]\n"
      "v_pk_fma_f32 v[134:135], v[100:101], v[100:101], v[134:135]\n"
      "v_mfma_f32_16x16x32_bf16 v[112:115], v[192:195], v[180:183], v[68:71]\n"
      "v_exp_f32_e64 v179, v179\n"
      "v_mfma_f32_16x16x32_bf16 v[118:121], v[172:175], v[180:183], v[72:75]\n"
      "ds_read_b128 v[126:129], v2 offset:12544\n"
      "v_cvt_pk_bf16_f32 v93, v178, v179\n"
      "v_mfma_f32_16x16x32_bf16 v[140:143], v[122:125], v[180:183], v[28:31]\n"
      "ds_read_b128 v[178:181], v2 offset:15616\n"
      "v_cvt_pk_bf16_f32 v96, v96, v97\n"
      "v_mfma_f32_16x16x16_bf16 v[144:147], v[184:185], v[168:169], v[144:147]\n"
      "v_exp_f32_e64 v176, v176\n"
      "v_cvt_pk_bf16_f32 v202, v202, v203\n"
      "v_mfma_f32_16x16x16_bf16 v[112:115], v[116:117], v[168:169], v[112:115]\n"
      "v_cvt_pk_bf16_f32 v203, v204, v205\n"
      "v_cvt_pk_bf16_f32 v204, v218, v219\n"
      "v_cvt_pk_bf16_f32 v205, v220, v221\n"
      "v_mfma_f32_16x16x16_bf16 v[118:121], v[200:201], v[168:169], v[118:121]\n"
      "v_cvt_pk_bf16_f32 v97, v98, v99\n"
      "v_exp_f32 v188, v188\n"
      "v_mfma_f32_16x16x16_bf16 v[140:143], v[208:209], v[168:169], v[140:143]\n"
      "v_cvt_pk_bf16_f32 v92, v176, v177\n"
      "v_exp_f32 v115, v115\n"
      "v_mfma_f32_16x16x32_bf16 v[156:159], a[228:231], v[108:111], v[156:159]\n"
      "s_waitcnt lgkmcnt(1)\n"
      "v_exp_f32 v146, v146\n"
      "v_mfma_f32_16x16x32_bf16 v[156:159], a[232:235], v[126:129], v[156:159]\n"
      "s_waitcnt lgkmcnt(0)\n"
      "v_exp_f32 v147, v147\n"
      "v_mfma_f32_16x16x32_bf16 v[156:159], a[236:239], v[178:181], v[156:159]\n"
      "v_exp_f32 v145, v145\n"
      "v_mfma_f32_16x16x32_bf16 v[160:163], a[252:255], v[108:111], v[160:163]\n"
      "v_exp_f32 v144, v144\n"
      "v_mfma_f32_16x16x32_bf16 v[160:163], v[224:227], v[126:129], v[160:163]\n"
      "ds_read_b128 v[218:221], v10 offset:1168\n"
      "v_cvt_pk_bf16_f32 v144, v144, v145\n"
      "v_mfma_f32_16x16x32_bf16 v[160:163], v[228:231], v[178:181], v[160:163]\n"
      "v_exp_f32 v140, v140\n"
      "v_mfma_f32_16x16x32_bf16 v[136:139], v[244:247], v[108:111], v[136:139]\n"
      "s_add_u32 s42, s42, 1\n"
      "v_exp_f32_e64 v118, v118\n"
      "v_mfma_f32_16x16x32_bf16 v[136:139], v[248:251], v[126:129], v[136:139]\n"
      "ds_read_b128 v[108:111], v7\n"
      "v_cvt_pk_bf16_f32 v94, v188, 0\n"
      "v_mfma_f32_16x16x32_bf16 v[136:139], v[252:255], v[178:181], v[136:139]\n"
      "v_exp_f32 v121, v121\n"
      "v_mfma_f32_16x16x32_bf16 v[176:179], v[32:35], v[210:213], 0\n"
      "v_exp_f32 v120, v120\n"
      "v_mfma_f32_16x16x32_bf16 v[176:179], v[32:35], v[92:95], v[176:179]\n"
      "v_exp_f32_e64 v114, v114\n"
      "v_mfma_f32_16x16x32_bf16 v[126:129], v[196:199], v[210:213], 0\n"
      "ds_write_b128 v3, v[202:205] offset:18432\n"
      "v_pk_fma_f32 v[134:135], v[102:103], v[102:103], v[134:135]\n"
      "v_mfma_f32_16x16x32_bf16 v[126:129], v[76:79], v[92:95], v[126:129]\n"
      "ds_read_b64 v[98:99], v10 offset:1184\n"
      "v_cvt_pk_bf16_f32 v145, v146, v147\n"
      "v_mfma_f32_16x16x32_bf16 v[202:205], v[214:217], v[210:213], 0\n"
      "ds_read_b128 v[178:181], v7 offset:4096\n"
      "v_cvt_pk_bf16_f32 v89, v120, v121\n"
      "v_mfma_f32_16x16x32_bf16 v[202:205], v[80:83], v[92:95], v[202:205]\n"
      "s_waitcnt lgkmcnt(3)\n"
      "v_rcp_f32 v176, v176\n"
      "v_mfma_f32_16x16x32_bf16 v[152:155], v[130:133], v[210:213], 0\n"
      "buffer_store_dwordx4 v[108:111], v20, s[24:27], s85 offen sc1\n"
      "v_pk_mul_f32 v[128:129], v[128:129], v[176:177] op_sel_hi:[1,0]\n"
      "v_pk_mul_f32 v[126:127], v[126:127], v[176:177] op_sel_hi:[1,0]\n"
      "v_mfma_f32_16x16x32_bf16 v[152:155], v[84:87], v[92:95], v[152:155]\n"
      "v_exp_f32 v119, v119\n"
      "v_mfma_f32_16x16x32_bf16 v[210:213], v[218:221], v[104:107], v[30:33]\n"
      "ds_read_b128 v[188:191], v2 offset:6656\n"
      "v_cvt_pk_bf16_f32 v147, v114, v115\n"
      "v_mfma_f32_16x16x32_bf16 v[108:111], v[218:221], v[192:195], v[30:33]\n"
      "s_waitcnt lgkmcnt(1)\n"
      "v_cvt_pk_bf16_f32 v88, v118, v119\n"
      "v_cvt_pk_bf16_f32 v90, v140, 0\n"
      "v_mfma_f32_16x16x32_bf16 v[140:143], v[218:221], v[172:175], v[30:33]\n"
      "ds_read_b128 v[172:175], v2 offset:512\n"
      "v_cvt_pk_bf16_f32 v126, v126, v127\n"
      "v_mfma_f32_16x16x32_bf16 v[104:107], v[218:221], v[122:125], v[30:33]\n"
      "ds_read_b128 v[218:221], v2 offset:3584\n"
      "v_mul_f32_e64 v121, v157, v157\n"
      "v_mfma_f32_16x16x16_bf16 v[210:213], v[98:99], v[184:185], v[210:213]\n"
      "v_cvt_pk_bf16_f32 v127, v128, v129\n"
      "v_exp_f32_e64 v113, v113\n"
      "v_mfma_f32_16x16x16_bf16 v[108:111], v[98:99], v[116:117], v[108:111]\n"
      "ds_read_b128 v[184:187], v7 offset:8192\n"
      "v_fma_f32 v120, v156, v156, s52\n"
      "v_mfma_f32_16x16x16_bf16 v[140:143], v[98:99], v[200:201], v[140:143]\n"
      "buffer_store_dwordx4 v[178:181], v21, s[24:27], s85 offen sc1\n"
      "v_pk_fma_f32 v[120:121], v[158:159], v[158:159], v[120:121]\n"
      "v_pk_fma_f32 v[134:135], v[164:165], v[164:165], v[134:135]\n"
      "v_pk_mul_f32 v[152:153], v[152:153], v[176:177] op_sel_hi:[1,0]\n"
      "v_pk_mul_f32 v[204:205], v[204:205], v[176:177] op_sel_hi:[1,0]\n"
      "v_pk_mul_f32 v[202:203], v[202:203], v[176:177] op_sel_hi:[1,0]\n"
      "v_pk_fma_f32 v[120:121], v[160:161], v[160:161], v[120:121]\n"
      "v_pk_fma_f32 v[120:121], v[162:163], v[162:163], v[120:121]\n"
      "v_mfma_f32_16x16x16_bf16 v[104:107], v[98:99], v[208:209], v[104:107]\n"
      "v_cvt_pk_bf16_f32 v129, v204, v205\n"
      "v_cvt_pk_bf16_f32 v128, v202, v203\n"
      "s_waitcnt lgkmcnt(1)\n"
      "v_mfma_f32_16x16x32_bf16 v[116:119], a[216:219], v[172:175], 0\n"
      "ds_write_b128 v3, v[126:129] offset:18688\n"
      "v_pk_fma_f32 v[120:121], v[136:137], v[136:137], v[120:121]\n"
      "v_mfma_f32_16x16x32_bf16 v[116:119], a[220:223], v[218:221], v[116:119]\n"
      "ds_read_b128 v[202:205], v2 offset:12800\n"
      "v_cndmask_b32 v210, v210, v108, s[58:59]\n"
      "v_mfma_f32_16x16x32_bf16 v[116:119], a[224:227], v[188:191], v[116:119]\n"
      "v_exp_f32_e64 v112, v112\n"
      "v_mfma_f32_16x16x32_bf16 v[192:195], a[240:243], v[172:175], 0\n"
      "s_waitcnt lgkmcnt(2)\n"
      "v_cvt_pk_bf16_f32 v146, v112, v113\n"
      "v_cndmask_b32 v210, v210, v140, s[60:61]\n"
      "v_mfma_f32_16x16x32_bf16 v[192:195], a[244:247], v[218:221], v[192:195]\n"
      "ds_read_b128 v[178:181], v2 offset:15872\n"
      "v_mfma_f32_16x16x32_bf16 v[192:195], a[248:251], v[188:191], v[192:195]\n"
      "ds_read_b128 v[108:111], v7 offset:12288\n"
      "v_cndmask_b32 v210, v210, v104, s[62:63]\n"
      "v_mfma_f32_16x16x32_bf16 v[140:143], v[232:235], v[172:175], 0\n"
      "buffer_store_dwordx4 v[184:187], v22, s[24:27], s85 offen sc1\n"
      "v_mfma_f32_16x16x32_bf16 v[140:143], v[236:239], v[218:221], v[140:143]\n"
      "v_exp_f32_e64 v210, v210\n"
      "v_mfma_f32_16x16x32_bf16 v[140:143], v[240:243], v[188:191], v[140:143]\n"
      "ds_write_b64 v4, v[96:97] offset:18432\n"
      "v_mfma_f32_16x16x32_bf16 v[172:175], v[32:35], v[144:147], 0\n"
      "ds_read_b128 v[218:221], v2 offset:9728\n"
      "v_fmac_f32_e64 v121, v139, v139\n"
      "v_mfma_f32_16x16x32_bf16 v[172:175], v[32:35], v[88:91], v[172:175]\n"
      "v_cvt_pk_bf16_f32 v152, v152, v153\n"
      "v_cvt_pk_bf16_f32 v210, v210, v210\n"
      "v_mfma_f32_16x16x32_bf16 v[96:99], v[196:199], v[144:147], 0\n"
      "ds_write_b16 v13, v210\n"
      "v_mfma_f32_16x16x32_bf16 v[96:99], v[76:79], v[88:91], v[96:99]\n"
      "ds_read_b128 v[210:213], v14\n"
      "v_fmac_f32 v120, v138, v138\n"
      "v_mfma_f32_16x16x32_bf16 v[186:189], v[214:217], v[144:147], 0\n"
      "ds_read_b128 v[182:185], v7 offset:16384\n"
      "v_add_f32 v120, v120, v121\n"
      "v_mfma_f32_16x16x32_bf16 v[186:189], v[80:83], v[88:91], v[186:189]\n"
      "s_waitcnt lgkmcnt(5)\n"
      "v_rcp_f32 v172, v172\n"
      "v_mfma_f32_16x16x32_bf16 v[122:125], v[130:133], v[144:147], 0\n"
      "buffer_store_dwordx4 v[108:111], v23, s[24:27], s85 offen sc1\n"
      "v_pk_mul_f32 v[98:99], v[98:99], v[172:173] op_sel_hi:[1,0]\n"
      "v_pk_mul_f32 v[96:97], v[96:97], v[172:173] op_sel_hi:[1,0]\n"
      "v_pk_fma_f32 v[134:135], v[166:167], v[166:167], v[134:135]\n"
      "v_pk_mul_f32 v[154:155], v[154:155], v[176:177] op_sel_hi:[1,0]\n"
      "v_mfma_f32_16x16x32_bf16 v[122:125], v[84:87], v[88:91], v[122:125]\n"
      "s_waitcnt lgkmcnt(2)\n"
      "v_add_f32 v134, v134, v135\n"
      "v_mul_f32 v189, v172, v189\n"
      "v_mfma_f32_16x16x32_bf16 v[116:119], a[228:231], v[218:221], v[116:119]\n"
      "v_permlane16_swap_b32 v134, v120\n"
      "v_mfma_f32_16x16x32_bf16 v[116:119], a[232:235], v[202:205], v[116:119]\n"
      "ds_read_b128 v[106:109], v14 offset:64\n"
      "v_mul_f32 v187, v172, v187\n"
      "v_mfma_f32_16x16x32_bf16 v[116:119], a[236:239], v[178:181], v[116:119]\n"
      "v_mul_f32 v188, v172, v188\n"
      "v_cvt_pk_bf16_f32 v96, v96, v97\n"
      "v_mfma_f32_16x16x32_bf16 v[192:195], a[252:255], v[218:221], v[192:195]\n"
      "v_cvt_pk_bf16_f32 v97, v98, v99\n"
      "v_cvt_pk_bf16_f32 v153, v154, v155\n"
      "v_mfma_f32_16x16x32_bf16 v[192:195], v[224:227], v[202:205], v[192:195]\n"
      "ds_write_b64 v4, v[152:153] offset:18688\n"
      "v_mfma_f32_16x16x32_bf16 v[192:195], v[228:231], v[178:181], v[192:195]\n"
      "v_cvt_pk_bf16_f32 v99, v188, v189\n"
      "v_fma_f32 v188, v116, v116, s52\n"
      "v_mfma_f32_16x16x32_bf16 v[140:143], v[244:247], v[218:221], v[140:143]\n"
      "v_mul_f32 v189, v117, v117\n"
      "v_mul_f32 v186, v172, v186\n"
      "v_mfma_f32_16x16x32_bf16 v[140:143], v[248:251], v[202:205], v[140:143]\n"
      "s_waitcnt lgkmcnt(1)\n"
      "v_cvt_pk_bf16_f32 v98, v186, v187\n"
      "v_add_f32 v134, v134, v120\n"
      "v_mfma_f32_16x16x32_bf16 v[140:143], v[252:255], v[178:181], v[140:143]\n"
      "ds_write_b128 v3, v[96:99] offset:18944\n"
      "v_pk_fma_f32 v[188:189], v[118:119], v[118:119], v[188:189]\n"
      "v_pk_mul_f32 v[122:123], v[122:123], v[172:173] op_sel_hi:[1,0]\n"
      "v_pk_fma_f32 v[188:189], v[192:193], v[192:193], v[188:189]\n"
      "v_pk_mul_f32 v[124:125], v[124:125], v[172:173] op_sel_hi:[1,0]\n"
      "v_cvt_pk_bf16_f32 v122, v122, v123\n"
      "v_cvt_pk_bf16_f32 v123, v124, v125\n"
      "v_pk_fma_f32 v[188:189], v[194:195], v[194:195], v[188:189]\n"
      "v_pk_fma_f32 v[188:189], v[140:141], v[140:141], v[188:189]\n"
      "v_pk_fma_f32 v[188:189], v[142:143], v[142:143], v[188:189]\n"
      "v_add_f32 v188, v188, v189\n"
      "v_mov_b32 v120, v188\n"
      "v_mfma_f32_16x16x32_bf16 v[218:221], v[210:213], v[32:35], 0\n"
      "ds_write_b64 v4, v[122:123] offset:18944\n"
      "v_permlane16_swap_b32 v188, v120\n"
      "v_add_f32_e64 v188, v188, v120\n"
      "v_add_u32 v10, 2336, v10\n"
      "s_nop 0\n"
      "v_permlane32_swap_b32_e64 v134, v188\n"
      "v_add_f32 v134, v134, v188\n"
      "v_mfma_f32_16x16x32_bf16 v[218:221], v[106:109], v[32:35], v[218:221]\n"
      "ds_write_b32 v31, v134 offset:1024\n"
      "v_add_u32 v11, 2336, v11\n"
      "s_cmp_ge_u32 s95, 0x40000000\n"
      "s_cselect_b64 vcc, -1, s[56:57]\n"
      "buffer_store_dwordx4 v[182:185], v25, s[24:27], s85 offen sc1\n"
      "s_waitcnt lgkmcnt(0)\n"
      "s_barrier\n"
      "s_cbranch_vccnz L_disp0_%=\n"
      "L_loop0_%=:\n"
      "v_mfma_f32_16x16x32_bf16 v[204:207], v[210:213], v[196:199], 0\n"
      "ds_read_b128 v[120:123], v0\n"
      "v_readlane_b32 s84, v37, s43\n"
      "v_mfma_f32_16x16x32_bf16 v[204:207], v[106:109], v[76:79], v[204:207]\n"
      "ds_read_b128 v[170:173], v9 offset:1024\n"
      "ds_read_b128 v[180:183], v1\n"
      "v_mfma_f32_16x16x32_bf16 v[176:179], v[210:213], v[214:217], 0\n"
      "ds_read_b128 v[124:127], v1 offset:12288\n"
      "s_add_u32 m0, s64, 18432\n"
      "v_mfma_f32_16x16x32_bf16 v[176:179], v[106:109], v[80:83], v[176:179]\n"
      "buffer_load_dwordx4 v16, s[16:19], s45 offen  lds\n"
      "v_mfma_f32_16x16x32_bf16 v[96:99], v[210:213], v[130:133], 0\n"
      "s_add_u32 m0, s64, 22528\n"
      "ds_read_b128 v[188:191], v0 offset:6144\n"
      "v_mfma_f32_16x16x32_bf16 v[96:99], v[106:109], v[84:87], v[96:99]\n"
      "s_waitcnt lgkmcnt(4)\n"
      "v_mfma_f32_16x16x32_bf16 v[112:115], a[0:3], v[120:123], 0\n"
      "ds_read_b128 v[184:187], v1 offset:6144\n"
      "v_cndmask_b32 v204, v204, v176, s[58:59]\n"
      "v_mfma_f32_16x16x32_bf16 v[174:177], a[24:27], v[120:123], 0\n"
      "s_waitcnt lgkmcnt(4)\n"
      "v_add_f32 v170, v170, v171\n"
      "v_add_f32 v172, v172, v173\n"
      "v_mfma_f32_16x16x32_bf16 v[200:203], a[48:51], v[120:123], 0\n"
      "buffer_load_dwordx4 v17, s[16:19], s45 offen  lds\n"
      "v_mfma_f32_16x16x32_bf16 v[206:209], a[72:75], v[120:123], 0\n"
      "ds_read_b128 v[108:111], v0 offset:12288\n"
      "v_add_f32 v170, v170, v172\n"
      "v_mfma_f32_16x16x32_bf16 v[144:147], a[96:99], v[120:123], 0\n"
      "s_waitcnt lgkmcnt(2)\n"
      "v_rsq_f32_e64 v170, v170\n"
      "v_mfma_f32_16x16x32_bf16 v[104:107], a[120:123], v[120:123], 0\n"
      "s_add_u32 m0, s64, 26624\n"
      "v_mul_f32 v170, 0x415db3d7, v170\n"
      "v_mov_b32 v172, v170\n"
      "v_mfma_f32_16x16x32_bf16 v[112:115], a[4:7], v[180:183], v[112:115]\n"
      "s_nop 0\n"
      "v_permlane32_swap_b32_e64 v170, v172\n"
      "v_mfma_f32_16x16x32_bf16 v[112:115], a[8:11], v[188:191], v[112:115]\n"
      "buffer_load_dwordx4 v18, s[16:19], s45 offen  lds\n"
      "v_mfma_f32_16x16x32_bf16 v[174:177], a[28:31], v[180:183], v[174:177]\n"
      "ds_read_b128 v[152:155], v0 offset:2048\n"
      "v_mov_b32 v171, v170\n"
      "v_mfma_f32_16x16x32_bf16 v[174:177], a[32:35], v[188:191], v[174:177]\n"
      "s_nop 0\n"
      "v_permlane16_swap_b32_e64 v170, v171\n"
      "v_mfma_f32_16x16x32_bf16 v[200:203], a[52:55], v[180:183], v[200:203]\n"
      "s_add_u32 m0, s64, 30720\n"
      "v_mul_f32 v148, v170, v148\n"
      "v_mul_f32 v162, v171, v162\n"
      "v_mfma_f32_16x16x32_bf16 v[200:203], a[56:59], v[188:191], v[200:203]\n"
      "v_mul_f32 v159, v171, v159\n"
      "v_mul_f32 v164, v170, v164\n"
      "v_mfma_f32_16x16x32_bf16 v[206:209], a[76:79], v[180:183], v[206:209]\n"
      "v_mul_f32 v165, v170, v165\n"
      "v_mul_f32 v167, v170, v167\n"
      "v_mfma_f32_16x16x32_bf16 v[206:209], a[80:83], v[188:191], v[206:209]\n"
      "buffer_load_dwordx4 v19, s[16:19], s45 offen  lds\n"
      "v_mfma_f32_16x16x32_bf16 v[144:147], a[100:103], v[180:183], v[144:147]\n"
      "v_mul_f32 v149, v170, v149\n"
      "v_mul_f32 v150, v170, v150\n"
      "v_mfma_f32_16x16x32_bf16 v[144:147], a[104:107], v[188:191], v[144:147]\n"
      "v_mul_f32 v100, v170, v100\n"
      "v_mul_f32 v161, v171, v161\n"
      "v_mfma_f32_16x16x32_bf16 v[104:107], a[124:127], v[180:183], v[104:107]\n"
      "s_add_u32 m0, s65, 34816\n"
      "v_mul_f32 v157, v171, v157\n"
      "v_mul_f32 v142, v172, v142\n"
      "v_mfma_f32_16x16x32_bf16 v[104:107], a[128:131], v[188:191], v[104:107]\n"
      "s_waitcnt lgkmcnt(0)\n"
      "v_mov_b32_dpp v218, v218 quad_perm:[0,0,0,0] row_mask:0xf bank_mask:0xf\n"
      "v_mul_f32 v151, v170, v151\n"
      "v_mfma_f32_16x16x32_bf16 v[112:115], a[12:15], v[184:187], v[112:115]\n"
      "buffer_load_dwordx4 v15, s[16:19], s45 offen  lds\n"
      "v_mfma_f32_16x16x32_bf16 v[112:115], a[16:19], v[108:111], v[112:115]\n"
      "v_mul_f32_e64 v156, v171, v156\n"
      "v_cndmask_b32 v204, v204, v96, s[60:61]\n"
      "v_mfma_f32_16x16x32_bf16 v[112:115], a[20:23], v[124:127], v[112:115]\n"
      "ds_read_b128 v[220:223], v0 offset:8192\n"
      "v_mul_f32_e64 v103, v170, v103\n"
      "v_mfma_f32_16x16x32_bf16 v[174:177], a[36:39], v[184:187], v[174:177]\n"
      "v_cvt_pk_bf16_f32 v148, v148, v149\n"
      "v_cvt_pk_bf16_f32 v156, v156, v157\n"
      "v_mfma_f32_16x16x32_bf16 v[174:177], a[40:43], v[108:111], v[174:177]\n"
      "v_mul_f32 v102, v170, v102\n"
      "v_mul_f32 v140, v172, v140\n"
      "v_mfma_f32_16x16x32_bf16 v[174:177], a[44:47], v[124:127], v[174:177]\n"
      "buffer_load_dwordx4 v[210:213], v36, s[28:31], s47 offen\n"
      "v_mfma_f32_16x16x32_bf16 v[200:203], a[60:63], v[184:187], v[200:203]\n"
      "v_rcp_f32_e64 v218, v218\n"
      "v_mfma_f32_16x16x32_bf16 v[200:203], a[64:67], v[108:111], v[200:203]\n"
      "v_mul_f32 v194, v172, v194\n"
      "v_mul_f32 v116, v172, v116\n"
      "v_mfma_f32_16x16x32_bf16 v[200:203], a[68:71], v[124:127], v[200:203]\n"
      "v_mul_f32_e64 v158, v171, v158\n"
      "v_cvt_pk_bf16_f32 v157, v158, v159\n"
      "v_mfma_f32_16x16x32_bf16 v[206:209], a[84:87], v[184:187], v[206:209]\n"
      "ds_read_b128 v[96:99], v1 offset:8192\n"
      "v_mul_f32_e64 v101, v170, v101\n"
      "v_mfma_f32_16x16x32_bf16 v[206:209], a[88:91], v[108:111], v[206:209]\n"
      "v_mul_f32_e64 v160, v171, v160\n"
      "v_fma_f32 v205, v112, v112, s51\n"
      "v_mfma_f32_16x16x32_bf16 v[206:209], a[92:95], v[124:127], v[206:209]\n"
      "v_cvt_pk_bf16_f32 v149, v150, v151\n"
      "v_mul_f32_e64 v136, v171, v136\n"
      "v_mfma_f32_16x16x32_bf16 v[144:147], a[108:111], v[184:187], v[144:147]\n"
      "buffer_load_dword v88, v29, s[28:31], s47 offen\n"
      "v_mfma_f32_16x16x32_bf16 v[144:147], a[112:115], v[108:111], v[144:147]\n"
      "v_mul_f32 v163, v171, v163\n"
      "v_mul_f32 v204, v218, v204\n"
      "v_mfma_f32_16x16x32_bf16 v[144:147], a[116:119], v[124:127], v[144:147]\n"
      "v_fma_f32 v77, v206, v206, s51\n"
      "v_mul_f32_e64 v139, v171, v139\n"
      "v_mfma_f32_16x16x32_bf16 v[104:107], a[132:135], v[184:187], v[104:107]\n"
      "v_cvt_pk_bf16_f32 v158, v160, v161\n"
      "v_cvt_pk_bf16_f32 v151, v102, v103\n"
      "v_mfma_f32_16x16x32_bf16 v[104:107], a[136:139], v[108:111], v[104:107]\n"
      "v_cvt_pk_bf16_f32 v150, v100, v101\n"
      "v_cvt_pk_bf16_f32 v204, v204, v204\n"
      "v_mfma_f32_16x16x32_bf16 v[104:107], a[140:143], v[124:127], v[104:107]\n"
      "v_fmac_f32 v205, v113, v113\n"
      "v_mfma_f32_16x16x32_bf16 v[196:199], v[120:123], a[144:147], 0\n"
      "ds_write_b16 v12, v204 offset:16\n"
      "v_mfma_f32_16x16x32_bf16 v[196:199], v[180:183], a[148:151], v[196:199]\n"
      "v_mul_f32 v193, v172, v193\n"
      "v_mul_f32 v195, v172, v195\n"
      "v_mfma_f32_16x16x32_bf16 v[196:199], v[188:191], a[152:155], v[196:199]\n"
      "v_mul_f32 v119, v172, v119\n"
      "v_mul_f32_e64 v117, v172, v117\n"
      "v_mfma_f32_16x16x32_bf16 v[196:199], v[184:187], a[156:159], v[196:199]\n"
      "ds_write_b128 v5, v[148:151]\n"
      "v_mfma_f32_16x16x32_bf16 v[196:199], v[108:111], a[160:163], v[196:199]\n"
      "v_fmac_f32_e64 v77, v207, v207\n"
      "v_mfma_f32_16x16x32_bf16 v[196:199], v[124:127], a[164:167], v[196:199]\n"
      "ds_read_b128 v[100:103], v1 offset:2048\n"
      "v_cvt_pk_bf16_f32 v159, v162, v163\n"
      "v_mfma_f32_16x16x32_bf16 v[214:217], v[120:123], a[168:171], 0\n"
      "v_mul_f32_e64 v118, v172, v118\n"
      "v_cvt_pk_bf16_f32 v116, v116, v117\n"
      "v_mfma_f32_16x16x32_bf16 v[214:217], v[180:183], a[172:175], v[214:217]\n"
      "v_fmac_f32 v205, v114, v114\n"
      "v_mul_f32 v143, v172, v143\n"
      "v_mfma_f32_16x16x32_bf16 v[214:217], v[188:191], a[176:179], v[214:217]\n"
      "v_fmac_f32 v205, v115, v115\n"
      "v_fmac_f32 v205, v174, v174\n"
      "v_mfma_f32_16x16x32_bf16 v[214:217], v[184:187], a[180:183], v[214:217]\n"
      "v_fmac_f32 v77, v208, v208\n"
      "v_fmac_f32 v205, v175, v175\n"
      "v_mfma_f32_16x16x32_bf16 v[214:217], v[108:111], a[184:187], v[214:217]\n"
      "ds_write_b128 v5, v[156:159] offset:6144\n"
      "v_mfma_f32_16x16x32_bf16 v[214:217], v[124:127], a[188:191], v[214:217]\n"
      "v_fmac_f32 v77, v209, v209\n"
      "v_fmac_f32 v205, v176, v176\n"
      "v_mfma_f32_16x16x32_bf16 v[130:133], v[120:123], a[192:195], 0\n"
      "ds_read_b128 v[120:123], v1 offset:14336\n"
      "v_mul_f32 v166, v170, v166\n"
      "v_mfma_f32_16x16x32_bf16 v[130:133], v[180:183], a[196:199], v[130:133]\n"
      "v_cvt_pk_bf16_f32 v164, v164, v165\n"
      "v_fmac_f32 v77, v144, v144\n"
      "v_mfma_f32_16x16x32_bf16 v[130:133], v[188:191], a[200:203], v[130:133]\n"
      "ds_read_b128 v[178:181], v9 offset:5632\n"
      "v_fmac_f32_e64 v205, v177, v177\n"
      "v_mfma_f32_16x16x32_bf16 v[130:133], v[184:187], a[204:207], v[130:133]\n"
      "ds_read_b128 v[184:187], v0 offset:14336\n"
      "v_fmac_f32 v205, v200, v200\n"
      "v_mfma_f32_16x16x32_bf16 v[130:133], v[108:111], a[208:211], v[130:133]\n"
      "v_mul_f32 v137, v171, v137\n"
      "v_cvt_pk_bf16_f32 v117, v118, v119\n"
      "v_mfma_f32_16x16x32_bf16 v[130:133], v[124:127], a[212:215], v[130:133]\n"
      "v_add_u32 v12, 32, v12\n"
      "v_fmac_f32 v77, v145, v145\n"
      "v_mfma_f32_16x16x32_bf16 v[126:129], a[0:3], v[152:155], 0\n"
      "s_waitcnt lgkmcnt(2)\n"
      "v_fmac_f32 v205, v201, v201\n"
      "v_fmac_f32 v77, v146, v146\n"
      "v_mfma_f32_16x16x32_bf16 v[126:129], a[4:7], v[100:103], v[126:129]\n"
      "v_fmac_f32 v77, v147, v147\n"
      "v_mul_f32 v141, v172, v141\n"
      "v_mfma_f32_16x16x32_bf16 v[126:129], a[8:11], v[220:223], v[126:129]\n"
      "s_waitcnt lgkmcnt(1)\n"
      "v_fmac_f32 v205, v202, v202\n"
      "v_cvt_pk_bf16_f32 v214, v214, v215\n"
      "v_mfma_f32_16x16x32_bf16 v[126:129], a[12:15], v[96:99], v[126:129]\n"
      "s_waitcnt lgkmcnt(0)\n"
      "v_or3_b32 v178, v178, v179, v180\n"
      "v_cvt_pk_bf16_f32 v196, v196, v197\n"
      "v_mfma_f32_16x16x32_bf16 v[126:129], a[16:19], v[184:187], v[126:129]\n"
      "v_fmac_f32 v77, v104, v104\n"
      "v_mul_f32 v192, v172, v192\n"
      "v_mfma_f32_16x16x32_bf16 v[126:129], a[20:23], v[120:123], v[126:129]\n"
      "ds_read_b128 v[148:151], v0 offset:10240\n"
      "v_cvt_pk_bf16_f32 v140, v140, v141\n"
      "v_mfma_f32_16x16x32_bf16 v[108:111], a[24:27], v[152:155], 0\n"
      "v_cvt_pk_bf16_f32 v119, v194, v195\n"
      "v_cvt_pk_bf16_f32 v118, v192, v193\n"
      "v_mfma_f32_16x16x32_bf16 v[108:111], a[28:31], v[100:103], v[108:111]\n"
      "v_fmac_f32 v77, v105, v105\n"
      "v_fmac_f32 v205, v203, v203\n"
      "v_mfma_f32_16x16x32_bf16 v[108:111], a[32:35], v[220:223], v[108:111]\n"
      "v_fmac_f32 v77, v106, v106\n"
      "v_fma_f32 v179, v126, v126, s51\n"
      "v_mfma_f32_16x16x32_bf16 v[108:111], a[36:39], v[96:99], v[108:111]\n"
      "v_fmac_f32 v77, v107, v107\n"
      "v_fmac_f32_e64 v179, v127, v127\n"
      "v_mfma_f32_16x16x32_bf16 v[108:111], a[40:43], v[184:187], v[108:111]\n"
      "v_permlane16_swap_b32 v205, v77\n"
      "v_mfma_f32_16x16x32_bf16 v[108:111], a[44:47], v[120:123], v[108:111]\n"
      "v_add_f32 v205, v205, v77\n"
      "v_mov_b32_e64 v77, v205\n"
      "v_mfma_f32_16x16x32_bf16 v[192:195], a[48:51], v[152:155], 0\n"
      "ds_read_b128 v[160:163], v0 offset:4096\n"
      "v_cvt_pk_bf16_f32 v136, v136, v137\n"
      "v_mfma_f32_16x16x32_bf16 v[192:195], a[52:55], v[100:103], v[192:195]\n"
      "v_cvt_pk_bf16_f32 v197, v198, v199\n"
      "v_fmac_f32 v179, v128, v128\n"
      "v_mfma_f32_16x16x32_bf16 v[192:195], a[56:59], v[220:223], v[192:195]\n"
      "v_permlane32_swap_b32 v205, v77\n"
      "v_mfma_f32_16x16x32_bf16 v[192:195], a[60:63], v[96:99], v[192:195]\n"
      "v_cvt_pk_bf16_f32 v130, v130, v131\n"
      "v_add_f32 v205, v205, v77\n"
      "v_mfma_f32_16x16x32_bf16 v[192:195], a[64:67], v[184:187], v[192:195]\n"
      "v_rsq_f32 v205, v205\n"
      "v_mfma_f32_16x16x32_bf16 v[192:195], a[68:71], v[120:123], v[192:195]\n"
      "ds_write_b128 v5, v[116:119] offset:12288\n"
      "v_mfma_f32_16x16x32_bf16 v[116:119], a[72:75], v[152:155], 0\n"
      "v_fmac_f32 v179, v129, v129\n"
      "v_fmac_f32 v179, v108, v108\n"
      "v_mfma_f32_16x16x32_bf16 v[116:119], a[76:79], v[100:103], v[116:119]\n"
      "v_mul_f32 v138, v171, v138\n"
      "v_mov_b32 v77, v205\n"
      "v_mfma_f32_16x16x32_bf16 v[116:119], a[80:83], v[220:223], v[116:119]\n"
      "v_cvt_pk_bf16_f32 v165, v166, v167\n"
      "v_readlane_b32 s86, v38, s43\n"
      "v_mfma_f32_16x16x32_bf16 v[116:119], a[84:87], v[96:99], v[116:119]\n"
      "v_cvt_pk_bf16_f32 v141, v142, v143\n"
      "v_readlane_b32 s85, v37, s42\n"
      "v_mfma_f32_16x16x32_bf16 v[116:119], a[88:91], v[184:187], v[116:119]\n"
      "v_permlane16_swap_b32 v205, v77\n"
      "v_mfma_f32_16x16x32_bf16 v[116:119], a[92:95], v[120:123], v[116:119]\n"
      "v_fmac_f32 v179, v109, v109\n"
      "v_cvt_pk_bf16_f32 v215, v216, v217\n"
      "v_mfma_f32_16x16x32_bf16 v[170:173], a[96:99], v[152:155], 0\n"
      "ds_write_b64 v6, v[140:141] offset:12288\n"
      "v_mfma_f32_16x16x32_bf16 v[170:173], a[100:103], v[100:103], v[170:173]\n"
      "v_mul_f32 v104, v77, v104\n"
      "v_mul_f32 v146, v77, v146\n"
      "v_mfma_f32_16x16x32_bf16 v[170:173], a[104:107], v[220:223], v[170:173]\n"
      "v_fma_f32 v76, v116, v116, s51\n"
      "v_fmac_f32 v179, v110, v110\n"
      "v_mfma_f32_16x16x32_bf16 v[170:173], a[108:111], v[96:99], v[170:173]\n"
      "v_fmac_f32 v179, v111, v111\n"
      "v_mul_f32_e64 v205, s50, v205\n"
      "v_mfma_f32_16x16x32_bf16 v[170:173], a[112:115], v[184:187], v[170:173]\n"
      "v_fmac_f32 v179, v192, v192\n"
      "v_fmac_f32 v76, v117, v117\n"
      "v_mfma_f32_16x16x32_bf16 v[170:173], a[116:119], v[120:123], v[170:173]\n"
      "v_mul_f32 v105, v77, v105\n"
      "v_mul_f32 v115, v205, v115\n"
      "v_mfma_f32_16x16x32_bf16 v[140:143], a[120:123], v[152:155], 0\n"
      "v_fmac_f32 v179, v193, v193\n"
      "v_mul_f32 v208, v77, v208\n"
      "v_mfma_f32_16x16x32_bf16 v[140:143], a[124:127], v[100:103], v[140:143]\n"
      "v_fmac_f32 v76, v118, v118\n"
      "v_fmac_f32 v76, v119, v119\n"
      "v_mfma_f32_16x16x32_bf16 v[140:143], a[128:131], v[220:223], v[140:143]\n"
      "v_fmac_f32 v76, v170, v170\n"
      "v_mul_f32 v176, v205, v176\n"
      "v_mfma_f32_16x16x32_bf16 v[140:143], a[132:135], v[96:99], v[140:143]\n"
      "v_mul_f32 v113, v205, v113\n"
      "v_mul_f32 v177, v205, v177\n"
      "v_mfma_f32_16x16x32_bf16 v[140:143], a[136:139], v[184:187], v[140:143]\n"
      "v_fmac_f32 v76, v171, v171\n"
      "v_cvt_pk_bf16_f32 v131, v132, v133\n"
      "v_mfma_f32_16x16x32_bf16 v[140:143], a[140:143], v[120:123], v[140:143]\n"
      "v_mul_f32 v200, v205, v200\n"
      "v_fmac_f32_e64 v76, v172, v172\n"
      "v_mfma_f32_16x16x32_bf16 v[216:219], v[152:155], a[144:147], 0\n"
      "ds_write_b64 v6, v[164:165]\n"
      "v_mfma_f32_16x16x32_bf16 v[216:219], v[100:103], a[148:151], v[216:219]\n"
      "v_cvt_pk_bf16_f32 v137, v138, v139\n"
      "v_fmac_f32 v76, v173, v173\n"
      "v_mfma_f32_16x16x32_bf16 v[216:219], v[220:223], a[152:155], v[216:219]\n"
      "s_or_b32 s95, s84, s85\n"
      "v_mul_f32 v207, v77, v207\n"
      "v_mul_f32 v106, v77, v106\n"
      "v_mfma_f32_16x16x32_bf16 v[216:219], v[96:99], a[156:159], v[216:219]\n"
      "ds_read_b128 v[164:167], v1 offset:10240\n"
      "v_mul_f32 v145, v77, v145\n"
      "v_mfma_f32_16x16x32_bf16 v[216:219], v[184:187], a[160:163], v[216:219]\n"
      "v_fmac_f32 v76, v140, v140\n"
      "v_fmac_f32 v179, v194, v194\n"
      "v_mfma_f32_16x16x32_bf16 v[216:219], v[120:123], a[164:167], v[216:219]\n"
      "s_or_b32 s95, s95, s48\n"
      "v_fmac_f32 v76, v141, v141\n"
      "v_mul_f32 v112, v205, v112\n"
      "v_mfma_f32_16x16x32_bf16 v[156:159], v[152:155], a[168:171], 0\n"
      "v_fmac_f32 v179, v195, v195\n"
      "v_fmac_f32 v76, v142, v142\n"
      "v_mfma_f32_16x16x32_bf16 v[156:159], v[100:103], a[172:175], v[156:159]\n"
      "v_mul_f32 v201, v205, v201\n"
      "v_mul_f32 v144, v77, v144\n"
      "v_mfma_f32_16x16x32_bf16 v[156:159], v[220:223], a[176:179], v[156:159]\n"
      "v_fmac_f32 v76, v143, v143\n"
      "v_bitop3_b32 v178, v178, v181, s33 bitop3:0xa8\n"
      "v_mfma_f32_16x16x32_bf16 v[156:159], v[96:99], a[180:183], v[156:159]\n"
      "ds_read_b128 v[188:191], v1 offset:4096\n"
      "v_mul_f32 v202, v205, v202\n"
      "v_mfma_f32_16x16x32_bf16 v[156:159], v[184:187], a[184:187], v[156:159]\n"
      "v_permlane16_swap_b32_e64 v179, v76\n"
      "v_mfma_f32_16x16x32_bf16 v[156:159], v[120:123], a[188:191], v[156:159]\n"
      "ds_write_b64 v6, v[136:137] offset:6144\n"
      "v_mfma_f32_16x16x32_bf16 v[136:139], v[152:155], a[192:195], 0\n"
      "v_add_f32 v179, v179, v76\n"
      "v_mul_f32 v206, v77, v206\n"
      "v_mfma_f32_16x16x32_bf16 v[136:139], v[100:103], a[196:199], v[136:139]\n"
      "ds_read_b128 v[152:155], v0 offset:16384\n"
      "v_mov_b32 v76, v179\n"
      "v_mfma_f32_16x16x32_bf16 v[136:139], v[220:223], a[200:203], v[136:139]\n"
      "s_nop 0\n"
      "v_permlane32_swap_b32_e64 v179, v76\n"
      "v_mfma_f32_16x16x32_bf16 v[136:139], v[96:99], a[204:207], v[136:139]\n"
      "ds_read_b128 v[180:183], v1 offset:16384\n"
      "v_add_f32 v179, v179, v76\n"
      "v_mfma_f32_16x16x32_bf16 v[136:139], v[184:187], a[208:211], v[136:139]\n"
      "v_rsq_f32 v179, v179\n"
      "v_mfma_f32_16x16x32_bf16 v[136:139], v[120:123], a[212:215], v[136:139]\n"
      "s_waitcnt lgkmcnt(3)\n"
      "v_cvt_pk_bf16_f32 v112, v112, v113\n"
      "v_mul_f32 v114, v205, v114\n"
      "v_mfma_f32_16x16x32_bf16 v[220:223], a[0:3], v[160:163], 0\n"
      "v_mul_f32 v147, v77, v147\n"
      "v_mov_b32 v76, v179\n"
      "v_mfma_f32_16x16x32_bf16 v[220:223], a[4:7], v[188:191], v[220:223]\n"
      "s_nop 0\n"
      "v_permlane16_swap_b32_e64 v179, v76\n"
      "v_mfma_f32_16x16x32_bf16 v[220:223], a[8:11], v[148:151], v[220:223]\n"
      "s_waitcnt lgkmcnt(1)\n"
      "v_cvt_pk_bf16_f32 v113, v114, v115\n"
      "v_cmp_ne_u32 s[56:57], 0, v178\n"
      "v_mfma_f32_16x16x32_bf16 v[220:223], a[12:15], v[164:167], v[220:223]\n"
      "ds_read_u16 v78, v11\n"
      "v_mul_f32 v117, v76, v117\n"
      "v_mfma_f32_16x16x32_bf16 v[220:223], a[16:19], v[152:155], v[220:223]\n"
      "s_waitcnt lgkmcnt(1)\n"
      "v_cvt_pk_bf16_f32 v133, v138, v139\n"
      "v_mul_f32_e64 v179, s50, v179\n"
      "v_mfma_f32_16x16x32_bf16 v[220:223], a[20:23], v[180:183], v[220:223]\n"
      "v_mul_f32 v116, v76, v116\n"
      "v_mul_f32 v171, v76, v171\n"
      "v_mfma_f32_16x16x32_bf16 v[96:99], a[24:27], v[160:163], 0\n"
      "v_mul_f32_e64 v209, v77, v209\n"
      "v_cvt_pk_bf16_f32 v198, v216, v217\n"
      "v_mfma_f32_16x16x32_bf16 v[96:99], a[28:31], v[188:191], v[96:99]\n"
      "v_cvt_pk_bf16_f32 v132, v136, v137\n"
      "v_mul_f32_e64 v192, v179, v192\n"
      "v_mfma_f32_16x16x32_bf16 v[96:99], a[32:35], v[148:151], v[96:99]\n"
      "v_cvt_pk_bf16_f32 v206, v206, v207\n"
      "v_cvt_pk_bf16_f32 v200, v200, v201\n"
      "v_mfma_f32_16x16x32_bf16 v[96:99], a[36:39], v[164:167], v[96:99]\n"
      "v_mul_f32 v194, v179, v194\n"
      "v_fma_f32 v138, v220, v220, s51\n"
      "v_mfma_f32_16x16x32_bf16 v[96:99], a[40:43], v[152:155], v[96:99]\n"
      "v_cvt_pk_bf16_f32 v116, v116, v117\n"
      "v_mul_f32 v175, v205, v175\n"
      "v_mfma_f32_16x16x32_bf16 v[96:99], a[44:47], v[180:183], v[96:99]\n"
      "v_fmac_f32 v138, v221, v221\n"
      "v_mul_f32 v126, v179, v126\n"
      "v_mfma_f32_16x16x32_bf16 v[134:137], a[48:51], v[160:163], 0\n"
      "v_fmac_f32_e64 v138, v222, v222\n"
      "v_cvt_pk_bf16_f32 v207, v208, v209\n"
      "v_mfma_f32_16x16x32_bf16 v[134:137], a[52:55], v[188:191], v[134:137]\n"
      "v_fmac_f32 v138, v223, v223\n"
      "v_mul_f32 v141, v76, v141\n"
      "v_mfma_f32_16x16x32_bf16 v[134:137], a[56:59], v[148:151], v[134:137]\n"
      "v_cvt_pk_bf16_f32 v216, v156, v157\n"
      "v_mul_f32 v107, v77, v107\n"
      "v_mfma_f32_16x16x32_bf16 v[134:137], a[60:63], v[164:167], v[134:137]\n"
      "v_mul_f32 v174, v205, v174\n"
      "v_cvt_pk_bf16_f32 v104, v104, v105\n"
      "v_mfma_f32_16x16x32_bf16 v[134:137], a[64:67], v[152:155], v[134:137]\n"
      "v_mul_f32 v170, v76, v170\n"
      "v_mul_f32 v127, v179, v127\n"
      "v_mfma_f32_16x16x32_bf16 v[134:137], a[68:71], v[180:183], v[134:137]\n"
      "v_cvt_pk_bf16_f32 v209, v146, v147\n"
      "v_mul_f32 v173, v76, v173\n"
      "v_mfma_f32_16x16x32_bf16 v[122:125], a[72:75], v[160:163], 0\n"
      "v_cvt_pk_bf16_f32 v114, v174, v175\n"
      "v_mul_f32 v128, v179, v128\n"
      "v_mfma_f32_16x16x32_bf16 v[122:125], a[76:79], v[188:191], v[122:125]\n"
      "v_cvt_pk_bf16_f32 v126, v126, v127\n"
      "v_mul_f32 v142, v76, v142\n"
      "v_mfma_f32_16x16x32_bf16 v[122:125], a[80:83], v[148:151], v[122:125]\n"
      "v_mul_f32 v110, v179, v110\n"
      "v_mul_f32_e64 v129, v179, v129\n"
      "v_mfma_f32_16x16x32_bf16 v[122:125], a[84:87], v[164:167], v[122:125]\n"
      "v_mul_f32 v119, v76, v119\n"
      "v_cvt_pk_bf16_f32 v208, v144, v145\n"
      "v_mfma_f32_16x16x32_bf16 v[122:125], a[88:91], v[152:155], v[122:125]\n"
      "v_mul_f32 v203, v205, v203\n"
      "v_mul_f32 v108, v179, v108\n"
      "v_mfma_f32_16x16x32_bf16 v[122:125], a[92:95], v[180:183], v[122:125]\n"
      "v_fmac_f32 v138, v96, v96\n"
      "v_cvt_pk_bf16_f32 v127, v128, v129\n"
      "v_mfma_f32_16x16x32_bf16 v[184:187], a[96:99], v[160:163], 0\n"
      "v_cvt_pk_bf16_f32 v105, v106, v107\n"
      "v_mul_f32 v193, v179, v193\n"
      "v_mfma_f32_16x16x32_bf16 v[184:187], a[100:103], v[188:191], v[184:187]\n"
      "v_mul_f32 v118, v76, v118\n"
      "v_cvt_pk_bf16_f32 v201, v202, v203\n"
      "v_mfma_f32_16x16x32_bf16 v[184:187], a[104:107], v[148:151], v[184:187]\n"
      "v_mul_f32 v109, v179, v109\n"
      "v_fmac_f32 v138, v97, v97\n"
      "v_mfma_f32_16x16x32_bf16 v[184:187], a[108:111], v[164:167], v[184:187]\n"
      "v_cvt_pk_bf16_f32 v115, v176, v177\n"
      "v_cvt_pk_bf16_f32 v199, v218, v219\n"
      "v_mfma_f32_16x16x32_bf16 v[184:187], a[112:115], v[152:155], v[184:187]\n"
      "v_fmac_f32 v138, v98, v98\n"
      "v_mul_f32 v111, v179, v111\n"
      "v_mfma_f32_16x16x32_bf16 v[184:187], a[116:119], v[180:183], v[184:187]\n"
      "v_fmac_f32 v138, v99, v99\n"
      "v_fma_f32 v139, v122, v122, s51\n"
      "v_mfma_f32_16x16x32_bf16 v[202:205], a[120:123], v[160:163], 0\n"
      "v_fmac_f32 v139, v123, v123\n"
      "v_mul_f32_e64 v195, v179, v195\n"
      "v_mfma_f32_16x16x32_bf16 v[202:205], a[124:127], v[188:191], v[202:205]\n"
      "v_fmac_f32 v139, v124, v124\n"
      "v_cvt_pk_bf16_f32 v117, v118, v119\n"
      "v_mfma_f32_16x16x32_bf16 v[202:205], a[128:131], v[148:151], v[202:205]\n"
      "v_fmac_f32 v138, v134, v134\n"
      "v_cvt_pk_bf16_f32 v217, v158, v159\n"
      "v_mfma_f32_16x16x32_bf16 v[202:205], a[132:135], v[164:167], v[202:205]\n"
      "v_cvt_pk_bf16_f32 v128, v108, v109\n"
      "v_fmac_f32_e64 v139, v125, v125\n"
      "v_mfma_f32_16x16x32_bf16 v[202:205], a[136:139], v[152:155], v[202:205]\n"
      "v_fmac_f32 v139, v184, v184\n"
      "v_mul_f32 v140, v76, v140\n"
      "v_mfma_f32_16x16x32_bf16 v[202:205], a[140:143], v[180:183], v[202:205]\n"
      "v_cvt_pk_bf16_f32 v140, v140, v141\n"
      "v_cvt_pk_bf16_f32 v192, v192, v193\n"
      "v_mfma_f32_16x16x32_bf16 v[144:147], v[160:163], a[144:147], 0\n"
      "ds_read_b128 v[156:159], v2 offset:24576\n"
      "v_fmac_f32_e64 v139, v185, v185\n"
      "v_mfma_f32_16x16x32_bf16 v[144:147], v[188:191], a[148:151], v[144:147]\n"
      "v_fmac_f32 v139, v186, v186\n"
      "v_mul_f32 v143, v76, v143\n"
      "v_mfma_f32_16x16x32_bf16 v[144:147], v[148:151], a[152:155], v[144:147]\n"
      "v_fmac_f32_e64 v139, v187, v187\n"
      "v_cvt_pk_bf16_f32 v141, v142, v143\n"
      "v_mfma_f32_16x16x32_bf16 v[144:147], v[164:167], a[156:159], v[144:147]\n"
      "ds_read_b128 v[176:179], v2 offset:18432\n"
      "v_fmac_f32_e64 v138, v135, v135\n"
      "v_mfma_f32_16x16x32_bf16 v[144:147], v[152:155], a[160:163], v[144:147]\n"
      "v_cvt_pk_bf16_f32 v118, v170, v171\n"
      "v_fmac_f32 v139, v202, v202\n"
      "v_mfma_f32_16x16x32_bf16 v[144:147], v[180:183], a[164:167], v[144:147]\n"
      "v_fmac_f32 v139, v203, v203\n"
      "v_fmac_f32 v138, v136, v136\n"
      "v_mfma_f32_16x16x32_bf16 v[168:171], v[160:163], a[168:171], 0\n"
      "v_cvt_pk_bf16_f32 v129, v110, v111\n"
      "v_mul_f32 v172, v76, v172\n"
      "v_mfma_f32_16x16x32_bf16 v[168:171], v[188:191], a[172:175], v[168:171]\n"
      "v_cvt_pk_bf16_f32 v193, v194, v195\n"
      "v_fmac_f32 v139, v204, v204\n"
      "v_mfma_f32_16x16x32_bf16 v[168:171], v[148:151], a[176:179], v[168:171]\n"
      "v_fmac_f32 v138, v137, v137\n"
      "v_fmac_f32_e64 v139, v205, v205\n"
      "v_mfma_f32_16x16x32_bf16 v[168:171], v[164:167], a[180:183], v[168:171]\n"
      "s_nop 0\n"
      "v_permlane16_swap_b32 v138, v139\n"
      "v_mfma_f32_16x16x32_bf16 v[168:171], v[152:155], a[184:187], v[168:171]\n"
      "s_add_u32 s43, s43, 1\n"
      "v_add_f32 v138, v138, v139\n"
      "v_mov_b32_e64 v139, v138\n"
      "v_mfma_f32_16x16x32_bf16 v[168:171], v[180:183], a[188:191], v[168:171]\n"
      "ds_read_b128 v[108:111], v2 offset:21504\n"
      "v_cvt_pk_bf16_f32 v119, v172, v173\n"
      "v_mfma_f32_16x16x32_bf16 v[172:175], v[160:163], a[192:195], 0\n"
      "v_permlane32_swap_b32_e64 v138, v139\n"
      "v_mfma_f32_16x16x32_bf16 v[172:175], v[188:191], a[196:199], v[172:175]\n"
      "ds_read_b128 v[160:163], v2 offset:30720\n"
      "v_cvt_pk_bf16_f32 v77, v146, v147\n"
      "v_mfma_f32_16x16x32_bf16 v[172:175], v[148:151], a[200:203], v[172:175]\n"
      "ds_read_b128 v[188:191], v2 offset:33792\n"
      "v_cvt_pk_bf16_f32 v76, v144, v145\n"
      "v_mfma_f32_16x16x32_bf16 v[172:175], v[164:167], a[204:207], v[172:175]\n"
      "ds_read_b128 v[142:145], v2 offset:27648\n"
      "v_add_f32 v138, v138, v139\n"
      "v_mfma_f32_16x16x32_bf16 v[172:175], v[152:155], a[208:211], v[172:175]\n"
      "v_rsq_f32 v138, v138\n"
      "v_mfma_f32_16x16x32_bf16 v[172:175], v[180:183], a[212:215], v[172:175]\n"
      "s_waitcnt lgkmcnt(3)\n"
      "v_mov_b32 v139, v138\n"
      "v_cvt_pk_bf16_f32 v80, v168, v169\n"
      "v_mfma_f32_16x16x32_bf16 v[148:151], a[216:219], v[176:179], 0\n"
      "v_permlane16_swap_b32 v138, v139\n"
      "v_mfma_f32_16x16x32_bf16 v[148:151], a[220:223], v[108:111], v[148:151]\n"
      "v_mul_f32 v138, s50, v138\n"
      "v_mul_f32 v99, v138, v99\n"
      "v_mfma_f32_16x16x32_bf16 v[148:151], a[224:227], v[156:159], v[148:151]\n"
      "s_and_b32 s86, s86, -4\n"
      "v_mul_f32 v125, v139, v125\n"
      "v_mul_f32 v185, v139, v185\n"
      "v_mfma_f32_16x16x32_bf16 v[100:103], a[240:243], v[176:179], 0\n"
      "v_mul_f32 v205, v139, v205\n"
      "v_mul_f32 v137, v138, v137\n"
      "v_mfma_f32_16x16x32_bf16 v[100:103], a[244:247], v[108:111], v[100:103]\n"
      "v_mul_f32 v221, v138, v221\n"
      "v_mul_f32 v122, v139, v122\n"
      "v_mfma_f32_16x16x32_bf16 v[100:103], a[248:251], v[156:159], v[100:103]\n"
      "ds_read_u16 v86, v11 offset:64\n"
      "v_mul_f32_e64 v220, v138, v220\n"
      "v_mfma_f32_16x16x32_bf16 v[164:167], v[232:235], v[176:179], 0\n"
      "v_mul_f32 v203, v139, v203\n"
      "v_mul_f32_e64 v98, v138, v98\n"
      "v_mfma_f32_16x16x32_bf16 v[164:167], v[236:239], v[108:111], v[164:167]\n"
      "s_waitcnt lgkmcnt(1)\n"
      "v_cvt_pk_bf16_f32 v81, v170, v171\n"
      "v_mul_f32 v97, v138, v97\n"
      "v_mfma_f32_16x16x32_bf16 v[164:167], v[240:243], v[156:159], v[164:167]\n"
      "ds_read_b128 v[178:181], v10 offset:32\n"
      "v_mul_f32 v123, v139, v123\n"
      "v_mfma_f32_16x16x32_bf16 v[148:151], a[228:231], v[142:145], v[148:151]\n"
      "ds_read_b128 v[106:109], v2 offset:18688\n"
      "v_mul_f32_e64 v187, v139, v187\n"
      "v_mfma_f32_16x16x32_bf16 v[148:151], a[232:235], v[160:163], v[148:151]\n"
      "ds_read_b128 v[152:155], v2 offset:21760\n"
      "v_cvt_pk_bf16_f32 v122, v122, v123\n"
      "v_mfma_f32_16x16x32_bf16 v[148:151], a[236:239], v[188:191], v[148:151]\n"
      "v_mul_f32_e64 v124, v139, v124\n"
      "v_cvt_pk_bf16_f32 v84, v172, v173\n"
      "v_mfma_f32_16x16x32_bf16 v[100:103], a[252:255], v[142:145], v[100:103]\n"
      "v_mul_f32 v186, v139, v186\n"
      "v_mul_f32 v223, v138, v223\n"
      "v_mfma_f32_16x16x32_bf16 v[100:103], v[224:227], v[160:163], v[100:103]\n"
      "v_mul_f32 v134, v138, v134\n"
      "v_cvt_pk_bf16_f32 v123, v124, v125\n"
      "v_mfma_f32_16x16x32_bf16 v[100:103], v[228:231], v[188:191], v[100:103]\n"
      "ds_read_b64 v[218:219], v10 offset:24\n"
      "v_mul_f32 v202, v139, v202\n"
      "v_mfma_f32_16x16x32_bf16 v[164:167], v[244:247], v[142:145], v[164:167]\n"
      "v_cvt_pk_bf16_f32 v85, v174, v175\n"
      "v_mul_f32 v184, v139, v184\n"
      "v_mfma_f32_16x16x32_bf16 v[164:167], v[248:251], v[160:163], v[164:167]\n"
      "v_cvt_pk_bf16_f32 v202, v202, v203\n"
      "v_mul_f32 v204, v139, v204\n"
      "v_mfma_f32_16x16x32_bf16 v[164:167], v[252:255], v[188:191], v[164:167]\n"
      "ds_read_u16 v82, v11 offset:32\n"
      "v_cvt_pk_bf16_f32 v125, v186, v187\n"
      "v_mfma_f32_16x16x32_bf16 v[188:191], v[206:209], v[112:115], v[40:43]\n"
      "ds_read_b128 v[174:177], v2 offset:24832\n"
      "v_cvt_pk_bf16_f32 v124, v184, v185\n"
      "v_mfma_f32_16x16x32_bf16 v[142:145], v[116:119], v[112:115], v[44:47]\n"
      "s_waitcnt lgkmcnt(3)\n"
      "v_cvt_pk_bf16_f32 v220, v220, v221\n"
      "v_cvt_pk_bf16_f32 v203, v204, v205\n"
      "v_mfma_f32_16x16x32_bf16 v[184:187], v[122:125], v[112:115], v[48:51]\n"
      "s_cmp_ge_u32 s95, 0x40000000\n"
      "s_cselect_b32 s94, -1, -2\n"
      "v_mul_f32 v96, v138, v96\n"
      "v_mfma_f32_16x16x32_bf16 v[170:173], v[178:181], v[112:115], v[24:27]\n"
      "s_waitcnt lgkmcnt(2)\n"
      "s_waitcnt vmcnt(1)\n"
      "v_or3_b32 v210, v210, v211, v212\n"
      "v_mul_f32 v222, v138, v222\n"
      "v_mfma_f32_16x16x16_bf16 v[188:191], v[104:105], v[200:201], v[188:191]\n"
      "v_fma_f32 v168, v148, v148, s52\n"
      "v_cvt_pk_bf16_f32 v221, v222, v223\n"
      "v_mul_f32 v136, v138, v136\n"
      "v_mfma_f32_16x16x16_bf16 v[142:145], v[140:141], v[200:201], v[142:145]\n"
      "v_mul_f32_e64 v135, v138, v135\n"
      "v_cvt_pk_bf16_f32 v134, v134, v135\n"
      "v_cvt_pk_bf16_f32 v135, v136, v137\n"
      "v_mfma_f32_16x16x16_bf16 v[184:187], v[202:203], v[200:201], v[184:187]\n"
      "v_exp_f32 v188, v188\n"
      "v_cvt_pk_bf16_f32 v222, v96, v97\n"
      "v_mfma_f32_16x16x16_bf16 v[170:173], v[218:219], v[200:201], v[170:173]\n"
      "v_exp_f32 v189, v189\n"
      "v_mul_f32 v169, v149, v149\n"
      "v_mfma_f32_16x16x32_bf16 v[156:159], a[216:219], v[106:109], 0\n"
      "v_exp_f32 v142, v142\n"
      "v_mfma_f32_16x16x32_bf16 v[156:159], a[220:223], v[152:155], v[156:159]\n"
      "s_waitcnt lgkmcnt(0)\n"
      "v_exp_f32 v145, v145\n"
      "v_mfma_f32_16x16x32_bf16 v[156:159], a[224:227], v[174:177], v[156:159]\n"
      "v_exp_f32 v191, v191\n"
      "v_mfma_f32_16x16x32_bf16 v[160:163], a[240:243], v[106:109], 0\n"
      "v_exp_f32 v184, v184\n"
      "v_mfma_f32_16x16x32_bf16 v[160:163], a[244:247], v[152:155], v[160:163]\n"
      "v_exp_f32 v185, v185\n"
      "v_mfma_f32_16x16x32_bf16 v[160:163], a[248:251], v[174:177], v[160:163]\n"
      "v_cvt_pk_bf16_f32 v188, v188, v189\n"
      "s_waitcnt vmcnt(0)\n"
      "v_or3_b32 v210, v210, v213, v88\n"
      "v_mfma_f32_16x16x32_bf16 v[136:139], v[232:235], v[106:109], 0\n"
      "v_exp_f32_e64 v187, v187\n"
      "v_mfma_f32_16x16x32_bf16 v[136:139], v[236:239], v[152:155], v[136:139]\n"
      "ds_read_b128 v[106:109], v2 offset:30976\n"
      "v_cvt_pk_bf16_f32 v88, v184, v185\n"
      "v_mfma_f32_16x16x32_bf16 v[136:139], v[240:243], v[174:177], v[136:139]\n"
      "v_exp_f32 v186, v186\n"
      "v_mfma_f32_16x16x32_bf16 v[174:177], v[206:209], v[126:129], v[52:55]\n"
      "v_exp_f32 v170, v170\n"
      "v_mfma_f32_16x16x32_bf16 v[112:115], v[116:119], v[126:129], v[56:59]\n"
      "v_exp_f32 v144, v144\n"
      "v_mfma_f32_16x16x32_bf16 v[182:185], v[122:125], v[126:129], v[60:63]\n"
      "v_exp_f32 v143, v143\n"
      "v_mfma_f32_16x16x32_bf16 v[152:155], v[178:181], v[126:129], v[26:29]\n"
      "s_cmp_gt_u32 s42, s44\n"
      "s_cselect_b32 s95, s94, s95\n"
      "v_cvt_pk_bf16_f32 v89, v186, v187\n"
      "v_mfma_f32_16x16x16_bf16 v[174:177], v[104:105], v[192:193], v[174:177]\n"
      "v_fmac_f32 v169, v151, v151\n"
      "v_exp_f32 v190, v190\n"
      "v_mfma_f32_16x16x16_bf16 v[112:115], v[140:141], v[192:193], v[112:115]\n"
      "v_cvt_pk_bf16_f32 v189, v190, v191\n"
      "v_cvt_pk_bf16_f32 v190, v142, v143\n"
      "v_fmac_f32_e64 v168, v150, v150\n"
      "v_mfma_f32_16x16x16_bf16 v[182:185], v[202:203], v[192:193], v[182:185]\n"
      "v_cvt_pk_bf16_f32 v191, v144, v145\n"
      "v_cvt_pk_bf16_f32 v223, v98, v99\n"
      "v_cvt_pk_bf16_f32 v90, v170, 0\n"
      "v_mfma_f32_16x16x16_bf16 v[152:155], v[218:219], v[192:193], v[152:155]\n"
      "v_exp_f32 v175, v175\n"
      "v_exp_f32 v113, v113\n"
      "v_mfma_f32_16x16x32_bf16 v[192:195], v[32:35], v[188:191], 0\n"
      "v_exp_f32_e64 v112, v112\n"
      "v_mfma_f32_16x16x32_bf16 v[192:195], v[32:35], v[88:91], v[192:195]\n"
      "v_exp_f32_e64 v174, v174\n"
      "v_mfma_f32_16x16x32_bf16 v[96:99], v[196:199], v[188:191], 0\n"
      "ds_write_b32 v31, v210 offset:4608\n"
      "v_mfma_f32_16x16x32_bf16 v[96:99], v[76:79], v[88:91], v[96:99]\n"
      "v_exp_f32 v176, v176\n"
      "v_mfma_f32_16x16x32_bf16 v[210:213], v[214:217], v[188:191], 0\n"
      "v_exp_f32 v177, v177\n"
      "v_mfma_f32_16x16x32_bf16 v[210:213], v[80:83], v[88:91], v[210:213]\n"
      "ds_read_b128 v[170:173], v2 offset:27904\n"
      "v_cvt_pk_bf16_f32 v174, v174, v175\n"
      "v_mfma_f32_16x16x32_bf16 v[142:145], v[130:133], v[188:191], 0\n"
      "v_exp_f32_e64 v114, v114\n"
      "v_mfma_f32_16x16x32_bf16 v[142:145], v[84:87], v[88:91], v[142:145]\n"
      "v_cvt_pk_bf16_f32 v175, v176, v177\n"
      "v_cvt_pk_bf16_f32 v176, v112, v113\n"
      "v_mfma_f32_16x16x32_bf16 v[126:129], v[206:209], v[220:223], v[64:67]\n"
      "v_exp_f32 v115, v115\n"
      "v_mfma_f32_16x16x32_bf16 v[186:189], v[116:119], v[220:223], v[68:71]\n"
      "v_cvt_pk_bf16_f32 v177, v114, v115\n"
      "v_rcp_f32 v192, v192\n"
      "v_exp_f32_e64 v184, v184\n"
      "v_pk_mul_f32 v[142:143], v[142:143], v[192:193] op_sel_hi:[1,0]\n"
      "v_pk_mul_f32 v[212:213], v[212:213], v[192:193] op_sel_hi:[1,0]\n"
      "v_pk_mul_f32 v[96:97], v[96:97], v[192:193] op_sel_hi:[1,0]\n"
      "v_pk_mul_f32 v[98:99], v[98:99], v[192:193] op_sel_hi:[1,0]\n"
      "v_pk_mul_f32 v[144:145], v[144:145], v[192:193] op_sel_hi:[1,0]\n"
      "v_pk_mul_f32 v[210:211], v[210:211], v[192:193] op_sel_hi:[1,0]\n"
      "v_mfma_f32_16x16x32_bf16 v[112:115], v[122:125], v[220:223], v[72:75]\n"
      "s_waitcnt lgkmcnt(0)\n"
      "v_exp_f32 v152, v152\n"
      "v_mfma_f32_16x16x32_bf16 v[190:193], v[178:181], v[220:223], v[28:31]\n"
      "ds_read_b128 v[220:223], v2 offset:34048\n"
      "v_cvt_pk_bf16_f32 v142, v142, v143\n"
      "v_mfma_f32_16x16x16_bf16 v[126:129], v[104:105], v[134:135], v[126:129]\n"
      "v_exp_f32 v183, v183\n"
      "v_cvt_pk_bf16_f32 v96, v96, v97\n"
      "v_mfma_f32_16x16x16_bf16 v[186:189], v[140:141], v[134:135], v[186:189]\n"
      "v_cvt_pk_bf16_f32 v97, v98, v99\n"
      "v_exp_f32 v182, v182\n"
      "v_mfma_f32_16x16x16_bf16 v[112:115], v[202:203], v[134:135], v[112:115]\n"
      "v_cvt_pk_bf16_f32 v99, v212, v213\n"
      "v_cvt_pk_bf16_f32 v143, v144, v145\n"
      "v_cvt_pk_bf16_f32 v92, v182, v183\n"
      "v_mfma_f32_16x16x16_bf16 v[190:193], v[218:219], v[134:135], v[190:193]\n"
      "v_cvt_pk_bf16_f32 v94, v152, 0\n"
      "v_exp_f32 v186, v186\n"
      "v_mfma_f32_16x16x32_bf16 v[156:159], a[228:231], v[170:173], v[156:159]\n"
      "s_nop 0\n"
      "v_exp_f32 v114, v114\n"
      "v_mfma_f32_16x16x32_bf16 v[156:159], a[232:235], v[106:109], v[156:159]\n"
      "s_waitcnt lgkmcnt(0)\n"
      "v_exp_f32 v126, v126\n"
      "v_mfma_f32_16x16x32_bf16 v[156:159], a[236:239], v[220:223], v[156:159]\n"
      "v_exp_f32 v188, v188\n"
      "v_mfma_f32_16x16x32_bf16 v[160:163], a[252:255], v[170:173], v[160:163]\n"
      "ds_read_b128 v[192:195], v10\n"
      "v_cvt_pk_bf16_f32 v98, v210, v211\n"
      "v_mfma_f32_16x16x32_bf16 v[160:163], v[224:227], v[106:109], v[160:163]\n"
      "v_exp_f32 v185, v185\n"
      "v_mfma_f32_16x16x32_bf16 v[160:163], v[228:231], v[220:223], v[160:163]\n"
      "v_exp_f32 v128, v128\n"
      "v_mfma_f32_16x16x32_bf16 v[136:139], v[244:247], v[170:173], v[136:139]\n"
      "v_exp_f32_e64 v127, v127\n"
      "v_mfma_f32_16x16x32_bf16 v[136:139], v[248:251], v[106:109], v[136:139]\n"
      "ds_read_b128 v[210:213], v7 offset:18432\n"
      "v_cvt_pk_bf16_f32 v93, v184, v185\n"
      "v_mfma_f32_16x16x32_bf16 v[136:139], v[252:255], v[220:223], v[136:139]\n"
      "v_exp_f32 v187, v187\n"
      "v_mfma_f32_16x16x32_bf16 v[170:173], v[32:35], v[174:177], 0\n"
      "v_exp_f32 v190, v190\n"
      "v_mfma_f32_16x16x32_bf16 v[170:173], v[32:35], v[92:95], v[170:173]\n"
      "ds_read_b64 v[88:89], v10 offset:16\n"
      "v_fma_f32 v134, v156, v156, s52\n"
      "v_mfma_f32_16x16x32_bf16 v[144:147], v[196:199], v[174:177], 0\n"
      "ds_read_b128 v[220:223], v7 offset:22528\n"
      "v_mul_f32_e64 v135, v157, v157\n"
      "v_mfma_f32_16x16x32_bf16 v[144:147], v[76:79], v[92:95], v[144:147]\n"
      "ds_write_b128 v3, v[96:99]\n"
      "v_mfma_f32_16x16x32_bf16 v[152:155], v[214:217], v[174:177], 0\n"
      "s_waitcnt lgkmcnt(3)\n"
      "v_rcp_f32 v170, v170\n"
      "v_mfma_f32_16x16x32_bf16 v[152:155], v[80:83], v[92:95], v[152:155]\n"
      "buffer_store_dwordx4 v[210:213], v20, s[24:27], s46 offen sc1\n"
      "v_pk_fma_f32 v[168:169], v[100:101], v[100:101], v[168:169]\n"
      "v_pk_fma_f32 v[134:135], v[158:159], v[158:159], v[134:135]\n"
      "v_mfma_f32_16x16x32_bf16 v[182:185], v[130:133], v[174:177], 0\n"
      "v_exp_f32 v129, v129\n"
      "v_mfma_f32_16x16x32_bf16 v[182:185], v[84:87], v[92:95], v[182:185]\n"
      "ds_read_b128 v[172:175], v2 offset:22016\n"
      "v_fmac_f32 v134, v160, v160\n"
      "v_mfma_f32_16x16x32_bf16 v[96:99], v[192:195], v[206:209], v[30:33]\n"
      "v_exp_f32 v115, v115\n"
      "v_mfma_f32_16x16x32_bf16 v[210:213], v[192:195], v[116:119], v[30:33]\n"
      "s_waitcnt lgkmcnt(1)\n"
      "v_exp_f32_e64 v113, v113\n"
      "v_mfma_f32_16x16x32_bf16 v[204:207], v[192:195], v[122:125], v[30:33]\n"
      "ds_read_b128 v[120:123], v2 offset:18944\n"
      "v_cvt_pk_bf16_f32 v126, v126, v127\n"
      "v_mfma_f32_16x16x32_bf16 v[108:111], v[192:195], v[178:181], v[30:33]\n"
      "ds_read_b128 v[178:181], v7 offset:26624\n"
      "v_fmac_f32_e64 v135, v161, v161\n"
      "v_mfma_f32_16x16x16_bf16 v[96:99], v[88:89], v[104:105], v[96:99]\n"
      "ds_read_b128 v[104:107], v2 offset:25088\n"
      "v_cvt_pk_bf16_f32 v90, v190, 0\n"
      "v_mfma_f32_16x16x16_bf16 v[210:213], v[88:89], v[140:141], v[210:213]\n"
      "buffer_store_dwordx4 v[220:223], v21, s[24:27], s46 offen sc1\n"
      "v_pk_mul_f32 v[144:145], v[144:145], v[170:171] op_sel_hi:[1,0]\n"
      "v_pk_mul_f32 v[182:183], v[182:183], v[170:171] op_sel_hi:[1,0]\n"
      "v_pk_mul_f32 v[154:155], v[154:155], v[170:171] op_sel_hi:[1,0]\n"
      "v_pk_mul_f32 v[184:185], v[184:185], v[170:171] op_sel_hi:[1,0]\n"
      "v_pk_fma_f32 v[168:169], v[102:103], v[102:103], v[168:169]\n"
      "v_pk_fma_f32 v[134:135], v[162:163], v[162:163], v[134:135]\n"
      "v_pk_fma_f32 v[134:135], v[136:137], v[136:137], v[134:135]\n"
      "v_pk_fma_f32 v[168:169], v[164:165], v[164:165], v[168:169]\n"
      "v_mfma_f32_16x16x16_bf16 v[204:207], v[88:89], v[202:203], v[204:207]\n"
      "v_exp_f32 v112, v112\n"
      "v_cvt_pk_bf16_f32 v127, v128, v129\n"
      "v_mfma_f32_16x16x16_bf16 v[108:111], v[88:89], v[218:219], v[108:111]\n"
      "v_fmac_f32 v169, v167, v167\n"
      "v_cvt_pk_bf16_f32 v182, v182, v183\n"
      "s_waitcnt lgkmcnt(0)\n"
      "v_mfma_f32_16x16x32_bf16 v[116:119], a[216:219], v[120:123], 0\n"
      "v_exp_f32 v189, v189\n"
      "v_mfma_f32_16x16x32_bf16 v[116:119], a[220:223], v[172:175], v[116:119]\n"
      "ds_read_b128 v[218:221], v7 offset:30720\n"
      "v_cndmask_b32 v96, v96, v210, s[58:59]\n"
      "v_mfma_f32_16x16x32_bf16 v[116:119], a[224:227], v[104:107], v[116:119]\n"
      "ds_write_b64 v4, v[142:143]\n"
      "v_mfma_f32_16x16x32_bf16 v[192:195], a[240:243], v[120:123], 0\n"
      "ds_read_b128 v[200:203], v2 offset:34304\n"
      "v_cvt_pk_bf16_f32 v89, v114, v115\n"
      "v_mfma_f32_16x16x32_bf16 v[192:195], a[244:247], v[172:175], v[192:195]\n"
      "buffer_store_dwordx4 v[178:181], v22, s[24:27], s46 offen sc1\n"
      "v_pk_fma_f32 v[134:135], v[138:139], v[138:139], v[134:135]\n"
      "v_mfma_f32_16x16x32_bf16 v[192:195], a[248:251], v[104:107], v[192:195]\n"
      "ds_read_b128 v[206:209], v2 offset:31232\n"
      "v_cndmask_b32 v96, v96, v204, s[60:61]\n"
      "v_mfma_f32_16x16x32_bf16 v[140:143], v[232:235], v[120:123], 0\n"
      "ds_read_b128 v[178:181], v2 offset:28160\n"
      "v_cndmask_b32 v96, v96, v108, s[62:63]\n"
      "v_mfma_f32_16x16x32_bf16 v[140:143], v[236:239], v[172:175], v[140:143]\n"
      "v_cvt_pk_bf16_f32 v129, v188, v189\n"
      "v_cvt_pk_bf16_f32 v128, v186, v187\n"
      "v_mfma_f32_16x16x32_bf16 v[140:143], v[240:243], v[104:107], v[140:143]\n"
      "v_cvt_pk_bf16_f32 v88, v112, v113\n"
      "v_add_f32 v134, v134, v135\n"
      "v_mfma_f32_16x16x32_bf16 v[104:107], v[32:35], v[126:129], 0\n"
      "v_exp_f32 v96, v96\n"
      "v_mfma_f32_16x16x32_bf16 v[104:107], v[32:35], v[88:91], v[104:107]\n"
      "v_cvt_pk_bf16_f32 v96, v96, v96\n"
      "v_fmac_f32_e64 v168, v166, v166\n"
      "v_mfma_f32_16x16x32_bf16 v[188:191], v[196:199], v[126:129], 0\n"
      "ds_write_b16 v13, v96\n"
      "v_mfma_f32_16x16x32_bf16 v[188:191], v[76:79], v[88:91], v[188:191]\n"
      "v_cvt_pk_bf16_f32 v183, v184, v185\n"
      "v_add_f32_e64 v168, v168, v169\n"
      "v_mfma_f32_16x16x32_bf16 v[96:99], v[214:217], v[126:129], 0\n"
      "ds_write_b64 v4, v[182:183] offset:256\n"
      "v_mfma_f32_16x16x32_bf16 v[96:99], v[80:83], v[88:91], v[96:99]\n"
      "v_rcp_f32 v104, v104\n"
      "v_mfma_f32_16x16x32_bf16 v[174:177], v[130:133], v[126:129], 0\n"
      "s_waitcnt lgkmcnt(2)\n"
      "v_permlane16_swap_b32_e64 v168, v134\n"
      "v_mfma_f32_16x16x32_bf16 v[174:177], v[84:87], v[88:91], v[174:177]\n"
      "ds_read_b128 v[210:213], v14\n"
      "v_cvt_pk_bf16_f32 v144, v144, v145\n"
      "v_mfma_f32_16x16x32_bf16 v[116:119], a[228:231], v[178:181], v[116:119]\n"
      "buffer_store_dwordx4 v[218:221], v23, s[24:27], s46 offen sc1\n"
      "v_pk_mul_f32 v[96:97], v[96:97], v[104:105] op_sel_hi:[1,0]\n"
      "v_pk_mul_f32 v[146:147], v[146:147], v[170:171] op_sel_hi:[1,0]\n"
      "v_pk_mul_f32 v[152:153], v[152:153], v[170:171] op_sel_hi:[1,0]\n"
      "v_pk_mul_f32 v[188:189], v[188:189], v[104:105] op_sel_hi:[1,0]\n"
      "v_mfma_f32_16x16x32_bf16 v[116:119], a[232:235], v[206:209], v[116:119]\n"
      "ds_read_b128 v[112:115], v7 offset:34816\n"
      "v_cvt_pk_bf16_f32 v145, v146, v147\n"
      "v_mfma_f32_16x16x32_bf16 v[116:119], a[236:239], v[200:203], v[116:119]\n"
      "v_cvt_pk_bf16_f32 v146, v152, v153\n"
      "v_cvt_pk_bf16_f32 v147, v154, v155\n"
      "v_mfma_f32_16x16x32_bf16 v[192:195], a[252:255], v[178:181], v[192:195]\n"
      "ds_write_b128 v3, v[144:147] offset:256\n"
      "v_pk_mul_f32 v[98:99], v[98:99], v[104:105] op_sel_hi:[1,0]\n"
      "v_pk_mul_f32 v[190:191], v[190:191], v[104:105] op_sel_hi:[1,0]\n"
      "v_mfma_f32_16x16x32_bf16 v[192:195], v[224:227], v[206:209], v[192:195]\n"
      "s_nop 0\n"
      "v_fma_f32 v182, v116, v116, s52\n"
      "v_cvt_pk_bf16_f32 v188, v188, v189\n"
      "v_mfma_f32_16x16x32_bf16 v[192:195], v[228:231], v[200:203], v[192:195]\n"
      "v_mul_f32 v183, v117, v117\n"
      "v_cvt_pk_bf16_f32 v189, v190, v191\n"
      "v_mfma_f32_16x16x32_bf16 v[140:143], v[244:247], v[178:181], v[140:143]\n"
      "ds_read_b128 v[106:109], v14 offset:64\n"
      "v_cvt_pk_bf16_f32 v190, v96, v97\n"
      "v_mfma_f32_16x16x32_bf16 v[140:143], v[248:251], v[206:209], v[140:143]\n"
      "s_waitcnt lgkmcnt(2)\n"
      "v_add_f32 v168, v168, v134\n"
      "v_cvt_pk_bf16_f32 v191, v98, v99\n"
      "v_mfma_f32_16x16x32_bf16 v[140:143], v[252:255], v[200:203], v[140:143]\n"
      "ds_write_b128 v3, v[188:191] offset:512\n"
      "v_pk_fma_f32 v[182:183], v[118:119], v[118:119], v[182:183]\n"
      "v_pk_fma_f32 v[182:183], v[192:193], v[192:193], v[182:183]\n"
      "v_pk_mul_f32 v[174:175], v[174:175], v[104:105] op_sel_hi:[1,0]\n"
      "v_cvt_pk_bf16_f32 v174, v174, v175\n"
      "v_pk_fma_f32 v[182:183], v[194:195], v[194:195], v[182:183]\n"
      "v_pk_mul_f32 v[176:177], v[176:177], v[104:105] op_sel_hi:[1,0]\n"
      "v_cvt_pk_bf16_f32 v175, v176, v177\n"
      "v_pk_fma_f32 v[182:183], v[140:141], v[140:141], v[182:183]\n"
      "v_pk_fma_f32 v[182:183], v[142:143], v[142:143], v[182:183]\n"
      "v_add_f32 v182, v182, v183\n"
      "v_mov_b32 v134, v182\n"
      "v_mfma_f32_16x16x32_bf16 v[218:221], v[210:213], v[32:35], 0\n"
      "ds_write_b64 v4, v[174:175] offset:512\n"
      "v_permlane16_swap_b32_e64 v182, v134\n"
      "v_add_f32_e64 v182, v182, v134\n"
      "s_cmp_ge_u32 s95, 0x40000000\n"
      "s_cselect_b64 vcc, -1, s[56:57]\n"
      "v_permlane32_swap_b32 v168, v182\n"
      "v_add_f32_e64 v168, v168, v182\n"
      "s_add_u32 s42, s42, 1\n"
      "ds_write_b32 v31, v168\n"
      "buffer_store_dwordx4 v[112:115], v25, s[24:27], s46 offen sc1\n"
      "s_waitcnt lgkmcnt(2)\n"
      "v_mfma_f32_16x16x32_bf16 v[218:221], v[106:109], v[32:35], v[218:221]\n"
      "s_waitcnt lgkmcnt(0)\n"
      "s_barrier\n"
      "s_cbranch_vccz L_loop1_%=\n"
      "s_branch L_disp1_%=\n"
      "s_nop 7\n"
      "s_nop 7\n"
      "L_max1_%=:\n"
      "v_mfma_f32_16x16x32_bf16 v[204:207], v[210:213], v[196:199], 0\n"
      "ds_read_b128 v[200:203], v0 offset:18432\n"
      "s_add_u32 m0, s64, 0\n"
      "v_mfma_f32_16x16x32_bf16 v[204:207], v[106:109], v[76:79], v[204:207]\n"
      "ds_read_b128 v[184:187], v9\n"
      "ds_read_b128 v[168:171], v0 offset:24576\n"
      "v_mfma_f32_16x16x32_bf16 v[176:179], v[210:213], v[214:217], 0\n"
      "ds_read_b128 v[152:155], v1 offset:18432\n"
      "v_readlane_b32 s45, v37, s43\n"
      "v_mfma_f32_16x16x32_bf16 v[176:179], v[106:109], v[80:83], v[176:179]\n"
      "buffer_load_dwordx4 v16, s[16:19], s84 offen  lds\n"
      "v_mfma_f32_16x16x32_bf16 v[96:99], v[210:213], v[130:133], 0\n"
      "ds_read_b128 v[206:209], v1 offset:24576\n"
      "v_readlane_b32 s46, v37, s42\n"
      "v_mfma_f32_16x16x32_bf16 v[96:99], v[106:109], v[84:87], v[96:99]\n"
      "ds_read_b128 v[144:147], v0 offset:30720\n"
      "s_waitcnt lgkmcnt(5)\n"
      "v_mfma_f32_16x16x32_bf16 v[104:107], a[0:3], v[200:203], 0\n"
      "ds_read_b128 v[108:111], v0 offset:26624\n"
      "v_cndmask_b32 v204, v204, v176, s[58:59]\n"
      "v_mfma_f32_16x16x32_bf16 v[112:115], a[24:27], v[200:203], 0\n"
      "s_waitcnt lgkmcnt(5)\n"
      "v_add_f32 v186, v186, v187\n"
      "v_add_f32 v184, v184, v185\n"
      "v_mfma_f32_16x16x32_bf16 v[172:175], a[48:51], v[200:203], 0\n"
      "s_add_u32 m0, s64, 4096\n"
      "v_mov_b32_dpp v218, v218 quad_perm:[0,0,0,0] row_mask:0xf bank_mask:0xf\n"
      "v_add_f32 v184, v184, v186\n"
      "v_mfma_f32_16x16x32_bf16 v[220:223], a[72:75], v[200:203], 0\n"
      "s_waitcnt lgkmcnt(3)\n"
      "v_rcp_f32_e64 v218, v218\n"
      "v_mfma_f32_16x16x32_bf16 v[210:213], a[96:99], v[200:203], 0\n"
      "buffer_load_dwordx4 v17, s[16:19], s84 offen  lds\n"
      "v_mfma_f32_16x16x32_bf16 v[122:125], a[120:123], v[200:203], 0\n"
      "s_add_u32 m0, s64, 8192\n"
      "v_rsq_f32_e64 v184, v184\n"
      "v_mfma_f32_16x16x32_bf16 v[104:107], a[4:7], v[152:155], v[104:107]\n"
      "ds_read_b128 v[180:183], v0 offset:20480\n"
      "v_mul_f32 v184, 0x415db3d7, v184\n"
      "v_mfma_f32_16x16x32_bf16 v[104:107], a[8:11], v[168:171], v[104:107]\n"
      "ds_read_b128 v[176:179], v1 offset:30720\n"
      "v_mov_b32 v186, v184\n"
      "v_mfma_f32_16x16x32_bf16 v[112:115], a[28:31], v[152:155], v[112:115]\n"
      "s_nop 0\n"
      "v_permlane32_swap_b32 v184, v186\n"
      "v_mfma_f32_16x16x32_bf16 v[112:115], a[32:35], v[168:171], v[112:115]\n"
      "v_mov_b32 v185, v184\n"
      "v_mul_f32_e64 v193, v186, v193\n"
      "v_mfma_f32_16x16x32_bf16 v[172:175], a[52:55], v[152:155], v[172:175]\n"
      "buffer_load_dwordx4 v18, s[16:19], s84 offen  lds\n"
      "v_mfma_f32_16x16x32_bf16 v[172:175], a[56:59], v[168:171], v[172:175]\n"
      "s_or_b32 s95, s45, s46\n"
      "v_permlane16_swap_b32 v184, v185\n"
      "v_mfma_f32_16x16x32_bf16 v[220:223], a[76:79], v[152:155], v[220:223]\n"
      "s_add_u32 m0, s64, 12288\n"
      "v_mul_f32 v164, v184, v164\n"
      "v_mul_f32 v103, v184, v103\n"
      "v_mfma_f32_16x16x32_bf16 v[220:223], a[80:83], v[168:171], v[220:223]\n"
      "v_mul_f32 v151, v184, v151\n"
      "v_mul_f32 v165, v184, v165\n"
      "v_mfma_f32_16x16x32_bf16 v[210:213], a[100:103], v[152:155], v[210:213]\n"
      "v_cvt_pk_bf16_f32 v164, v164, v165\n"
      "v_mul_f32_e64 v167, v184, v167\n"
      "v_mfma_f32_16x16x32_bf16 v[210:213], a[104:107], v[168:171], v[210:213]\n"
      "buffer_load_dwordx4 v19, s[16:19], s84 offen  lds\n"
      "v_mfma_f32_16x16x32_bf16 v[122:125], a[124:127], v[152:155], v[122:125]\n"
      "s_waitcnt lgkmcnt(0)\n"
      "v_mul_f32 v149, v184, v149\n"
      "v_mul_f32_e64 v148, v184, v148\n"
      "v_mfma_f32_16x16x32_bf16 v[122:125], a[128:131], v[168:171], v[122:125]\n"
      "ds_read_b128 v[188:191], v1 offset:26624\n"
      "v_mul_f32 v166, v184, v166\n"
      "v_mfma_f32_16x16x32_bf16 v[104:107], a[12:15], v[206:209], v[104:107]\n"
      "v_mul_f32 v101, v184, v101\n"
      "v_mul_f32 v150, v184, v150\n"
      "v_mfma_f32_16x16x32_bf16 v[104:107], a[16:19], v[144:147], v[104:107]\n"
      "s_add_u32 m0, s65, 16384\n"
      "v_cvt_pk_bf16_f32 v148, v148, v149\n"
      "v_mul_f32 v102, v184, v102\n"
      "v_mfma_f32_16x16x32_bf16 v[104:107], a[20:23], v[176:179], v[104:107]\n"
      "buffer_load_dwordx4 v15, s[16:19], s84 offen  lds\n"
      "v_mfma_f32_16x16x32_bf16 v[112:115], a[36:39], v[206:209], v[112:115]\n"
      "v_cvt_pk_bf16_f32 v149, v150, v151\n"
      "v_mul_f32 v100, v184, v100\n"
      "v_mfma_f32_16x16x32_bf16 v[112:115], a[40:43], v[144:147], v[112:115]\n"
      "s_or_b32 s95, s95, s48\n"
      "v_cvt_pk_bf16_f32 v151, v102, v103\n"
      "v_cvt_pk_bf16_f32 v150, v100, v101\n"
      "v_mfma_f32_16x16x32_bf16 v[112:115], a[44:47], v[176:179], v[112:115]\n"
      "ds_write_b128 v5, v[148:151] offset:18432\n"
      "v_pk_mul_f32 v[136:137], v[136:137], v[184:185] op_sel:[0,1]\n"
      "v_pk_mul_f32 v[156:157], v[156:157], v[184:185] op_sel:[0,1]\n"
      "v_pk_mul_f32 v[158:159], v[158:159], v[184:185] op_sel:[0,1]\n"
      "v_mfma_f32_16x16x32_bf16 v[172:175], a[60:63], v[206:209], v[172:175]\n"
      "buffer_load_dwordx4 v[126:129], v36, s[28:31], s86 offen\n"
      "v_mfma_f32_16x16x32_bf16 v[172:175], a[64:67], v[144:147], v[172:175]\n"
      "s_cmp_ge_u32 s95, 0x40000000\n"
      "s_cselect_b32 s94, -1, -2\n"
      "v_cvt_pk_bf16_f32 v165, v166, v167\n"
      "v_mfma_f32_16x16x32_bf16 v[172:175], a[68:71], v[176:179], v[172:175]\n"
      "v_mul_f32_e64 v160, v185, v160\n"
      "v_cvt_pk_bf16_f32 v156, v156, v157\n"
      "v_mfma_f32_16x16x32_bf16 v[220:223], a[84:87], v[206:209], v[220:223]\n"
      "v_mul_f32 v141, v186, v141\n"
      "v_mul_f32 v163, v185, v163\n"
      "v_mfma_f32_16x16x32_bf16 v[220:223], a[88:91], v[144:147], v[220:223]\n"
      "ds_write_b64 v6, v[164:165] offset:18432\n"
      "v_mfma_f32_16x16x32_bf16 v[220:223], a[92:95], v[176:179], v[220:223]\n"
      "v_cvt_pk_bf16_f32 v157, v158, v159\n"
      "v_mul_f32_e64 v143, v186, v143\n"
      "v_mfma_f32_16x16x32_bf16 v[210:213], a[108:111], v[206:209], v[210:213]\n"
      "buffer_load_dword v82, v29, s[28:31], s86 offen\n"
      "v_mfma_f32_16x16x32_bf16 v[210:213], a[112:115], v[144:147], v[210:213]\n"
      "v_mul_f32 v161, v185, v161\n"
      "v_mul_f32 v138, v185, v138\n"
      "v_mfma_f32_16x16x32_bf16 v[210:213], a[116:119], v[176:179], v[210:213]\n"
      "ds_read_b128 v[100:103], v1 offset:28672\n"
      "v_fma_f32 v205, v220, v220, s51\n"
      "v_mfma_f32_16x16x32_bf16 v[122:125], a[132:135], v[206:209], v[122:125]\n"
      "v_cvt_pk_bf16_f32 v158, v160, v161\n"
      "v_mul_f32 v119, v186, v119\n"
      "v_mfma_f32_16x16x32_bf16 v[122:125], a[136:139], v[144:147], v[122:125]\n"
      "v_mul_f32 v162, v185, v162\n"
      "v_fma_f32 v81, v104, v104, s51\n"
      "v_mfma_f32_16x16x32_bf16 v[122:125], a[140:143], v[176:179], v[122:125]\n"
      "v_cvt_pk_bf16_f32 v159, v162, v163\n"
      "v_fmac_f32_e64 v205, v221, v221\n"
      "v_mfma_f32_16x16x32_bf16 v[196:199], v[200:203], a[144:147], 0\n"
      "ds_write_b128 v5, v[156:159] offset:24576\n"
      "v_mfma_f32_16x16x32_bf16 v[196:199], v[152:155], a[148:151], v[196:199]\n"
      "v_fmac_f32_e64 v81, v105, v105\n"
      "v_cvt_pk_bf16_f32 v136, v136, v137\n"
      "v_mfma_f32_16x16x32_bf16 v[196:199], v[168:171], a[152:155], v[196:199]\n"
      "v_mul_f32 v140, v186, v140\n"
      "v_mul_f32 v139, v185, v139\n"
      "v_mfma_f32_16x16x32_bf16 v[196:199], v[206:209], a[156:159], v[196:199]\n"
      "ds_read_b128 v[156:159], v0 offset:32768\n"
      "v_cvt_pk_bf16_f32 v137, v138, v139\n"
      "v_mfma_f32_16x16x32_bf16 v[196:199], v[144:147], a[160:163], v[196:199]\n"
      "ds_write_b64 v6, v[136:137] offset:24576\n"
      "v_mfma_f32_16x16x32_bf16 v[196:199], v[176:179], a[164:167], v[196:199]\n"
      "v_fmac_f32 v205, v222, v222\n"
      "v_mul_f32 v118, v186, v118\n"
      "v_mfma_f32_16x16x32_bf16 v[214:217], v[200:203], a[168:171], 0\n"
      "v_cvt_pk_bf16_f32 v140, v140, v141\n"
      "v_cndmask_b32 v204, v204, v96, s[60:61]\n"
      "v_mfma_f32_16x16x32_bf16 v[214:217], v[152:155], a[172:175], v[214:217]\n"
      "ds_read_b128 v[96:99], v1 offset:32768\n"
      "v_mul_f32 v194, v186, v194\n"
      "v_mfma_f32_16x16x32_bf16 v[214:217], v[168:171], a[176:179], v[214:217]\n"
      "v_fmac_f32 v81, v106, v106\n"
      "v_fmac_f32 v81, v107, v107\n"
      "v_mfma_f32_16x16x32_bf16 v[214:217], v[206:209], a[180:183], v[214:217]\n"
      "v_mul_f32 v204, v218, v204\n"
      "v_mul_f32 v117, v186, v117\n"
      "v_mfma_f32_16x16x32_bf16 v[214:217], v[144:147], a[184:187], v[214:217]\n"
      "v_fmac_f32 v205, v223, v223\n"
      "v_fmac_f32 v81, v112, v112\n"
      "v_mfma_f32_16x16x32_bf16 v[214:217], v[176:179], a[188:191], v[214:217]\n"
      "ds_read_b128 v[148:151], v1 offset:20480\n"
      "v_mul_f32 v142, v186, v142\n"
      "v_mfma_f32_16x16x32_bf16 v[130:133], v[200:203], a[192:195], 0\n"
      "v_fmac_f32 v81, v113, v113\n"
      "v_fmac_f32 v205, v210, v210\n"
      "v_mfma_f32_16x16x32_bf16 v[130:133], v[152:155], a[196:199], v[130:133]\n"
      "ds_read_b128 v[200:203], v7\n"
      "v_mul_f32_e64 v116, v186, v116\n"
      "v_mfma_f32_16x16x32_bf16 v[130:133], v[168:171], a[200:203], v[130:133]\n"
      "v_fmac_f32 v205, v211, v211\n"
      "v_cvt_pk_bf16_f32 v116, v116, v117\n"
      "v_mfma_f32_16x16x32_bf16 v[130:133], v[206:209], a[204:207], v[130:133]\n"
      "v_mul_f32 v192, v186, v192\n"
      "v_cvt_pk_bf16_f32 v117, v118, v119\n"
      "v_mfma_f32_16x16x32_bf16 v[130:133], v[144:147], a[208:211], v[130:133]\n"
      "v_fmac_f32 v205, v212, v212\n"
      "v_fmac_f32 v205, v213, v213\n"
      "v_mfma_f32_16x16x32_bf16 v[130:133], v[176:179], a[212:215], v[130:133]\n"
      "ds_read_b128 v[152:155], v9 offset:4608\n"
      "v_cvt_pk_bf16_f32 v118, v192, v193\n"
      "v_mfma_f32_16x16x32_bf16 v[160:163], a[0:3], v[180:183], 0\n"
      "s_waitcnt lgkmcnt(2)\n"
      "v_mul_f32 v195, v186, v195\n"
      "v_cvt_pk_bf16_f32 v141, v142, v143\n"
      "v_mfma_f32_16x16x32_bf16 v[160:163], a[4:7], v[148:151], v[160:163]\n"
      "v_fmac_f32 v205, v122, v122\n"
      "v_fmac_f32 v81, v114, v114\n"
      "v_mfma_f32_16x16x32_bf16 v[160:163], a[8:11], v[108:111], v[160:163]\n"
      "s_cmp_gt_u32 s42, s44\n"
      "s_cselect_b32 s95, s94, s95\n"
      "v_fmac_f32 v81, v115, v115\n"
      "v_mfma_f32_16x16x32_bf16 v[160:163], a[12:15], v[188:191], v[160:163]\n"
      "v_fmac_f32 v205, v123, v123\n"
      "v_fmac_f32_e64 v205, v124, v124\n"
      "v_mfma_f32_16x16x32_bf16 v[160:163], a[16:19], v[156:159], v[160:163]\n"
      "v_cvt_pk_bf16_f32 v204, v204, v204\n"
      "v_cvt_pk_bf16_f32 v119, v194, v195\n"
      "v_mfma_f32_16x16x32_bf16 v[160:163], a[20:23], v[96:99], v[160:163]\n"
      "v_fmac_f32 v81, v172, v172\n"
      "v_fmac_f32 v81, v173, v173\n"
      "v_mfma_f32_16x16x32_bf16 v[176:179], a[24:27], v[180:183], 0\n"
      "ds_read_b128 v[184:187], v1 offset:22528\n"
      "v_cvt_pk_bf16_f32 v130, v130, v131\n"
      "v_mfma_f32_16x16x32_bf16 v[176:179], a[28:31], v[148:151], v[176:179]\n"
      "ds_write_b128 v5, v[116:119] offset:30720\n"
      "v_mfma_f32_16x16x32_bf16 v[176:179], a[32:35], v[108:111], v[176:179]\n"
      "s_waitcnt lgkmcnt(2)\n"
      "v_fmac_f32 v81, v174, v174\n"
      "v_or3_b32 v152, v152, v153, v154\n"
      "v_mfma_f32_16x16x32_bf16 v[176:179], a[36:39], v[188:191], v[176:179]\n"
      "v_fmac_f32 v205, v125, v125\n"
      "v_fmac_f32 v81, v175, v175\n"
      "v_mfma_f32_16x16x32_bf16 v[176:179], a[40:43], v[156:159], v[176:179]\n"
      "s_nop 0\n"
      "v_permlane16_swap_b32 v81, v205\n"
      "v_mfma_f32_16x16x32_bf16 v[176:179], a[44:47], v[96:99], v[176:179]\n"
      "ds_read_b128 v[164:167], v1 offset:34816\n"
      "v_add_f32_e64 v81, v81, v205\n"
      "v_mfma_f32_16x16x32_bf16 v[144:147], a[48:51], v[180:183], 0\n"
      "v_fma_f32 v85, v160, v160, s51\n"
      "v_mov_b32 v205, v81\n"
      "v_mfma_f32_16x16x32_bf16 v[144:147], a[52:55], v[148:151], v[144:147]\n"
      "s_nop 0\n"
      "v_permlane32_swap_b32 v81, v205\n"
      "v_mfma_f32_16x16x32_bf16 v[144:147], a[56:59], v[108:111], v[144:147]\n"
      "ds_read_b128 v[192:195], v0 offset:22528\n"
      "v_add_f32 v81, v81, v205\n"
      "v_mfma_f32_16x16x32_bf16 v[144:147], a[60:63], v[188:191], v[144:147]\n"
      "v_rsq_f32 v81, v81\n"
      "v_mfma_f32_16x16x32_bf16 v[144:147], a[64:67], v[156:159], v[144:147]\n"
      "v_bitop3_b32 v152, v152, v155, s33 bitop3:0xa8\n"
      "v_mov_b32 v205, v81\n"
      "v_mfma_f32_16x16x32_bf16 v[144:147], a[68:71], v[96:99], v[144:147]\n"
      "s_nop 0\n"
      "v_permlane16_swap_b32 v81, v205\n"
      "v_mfma_f32_16x16x32_bf16 v[118:121], a[72:75], v[180:183], 0\n"
      "ds_write_b64 v6, v[140:141] offset:30720\n"
      "v_mfma_f32_16x16x32_bf16 v[118:121], a[76:79], v[148:151], v[118:121]\n"
      "v_mul_f32 v81, s50, v81\n"
      "v_mul_f32 v112, v81, v112\n"
      "v_mfma_f32_16x16x32_bf16 v[118:121], a[80:83], v[108:111], v[118:121]\n"
      "v_cmp_ne_u32 s[56:57], 0, v152\n"
      "v_mul_f32_e64 v105, v81, v105\n"
      "v_mfma_f32_16x16x32_bf16 v[118:121], a[84:87], v[188:191], v[118:121]\n"
      "v_mul_f32 v104, v81, v104\n"
      "v_cvt_pk_bf16_f32 v196, v196, v197\n"
      "v_mfma_f32_16x16x32_bf16 v[118:121], a[88:91], v[156:159], v[118:121]\n"
      "v_mul_f32 v222, v205, v222\n"
      "v_mul_f32 v114, v81, v114\n"
      "v_mfma_f32_16x16x32_bf16 v[118:121], a[92:95], v[96:99], v[118:121]\n"
      "v_cvt_pk_bf16_f32 v104, v104, v105\n"
      "v_fmac_f32 v85, v161, v161\n"
      "v_mfma_f32_16x16x32_bf16 v[152:155], a[96:99], v[180:183], 0\n"
      "v_mul_f32 v124, v205, v124\n"
      "v_mul_f32 v211, v205, v211\n"
      "v_mfma_f32_16x16x32_bf16 v[152:155], a[100:103], v[148:151], v[152:155]\n"
      "v_cvt_pk_bf16_f32 v131, v132, v133\n"
      "v_mul_f32_e64 v173, v81, v173\n"
      "v_mfma_f32_16x16x32_bf16 v[152:155], a[104:107], v[108:111], v[152:155]\n"
      "ds_write_b16 v12, v204\n"
      "v_mfma_f32_16x16x32_bf16 v[152:155], a[108:111], v[188:191], v[152:155]\n"
      "v_mul_f32 v175, v81, v175\n"
      "v_mul_f32 v107, v81, v107\n"
      "v_mfma_f32_16x16x32_bf16 v[152:155], a[112:115], v[156:159], v[152:155]\n"
      "v_mul_f32_e64 v172, v81, v172\n"
      "v_readlane_b32 s47, v38, s43\n"
      "v_mfma_f32_16x16x32_bf16 v[152:155], a[116:119], v[96:99], v[152:155]\n"
      "v_fma_f32 v204, v118, v118, s51\n"
      "v_cvt_pk_bf16_f32 v197, v198, v199\n"
      "v_mfma_f32_16x16x32_bf16 v[168:171], a[120:123], v[180:183], 0\n"
      "v_cvt_pk_bf16_f32 v214, v214, v215\n"
      "v_mul_f32 v106, v81, v106\n"
      "v_mfma_f32_16x16x32_bf16 v[168:171], a[124:127], v[148:151], v[168:171]\n"
      "v_cvt_pk_bf16_f32 v215, v216, v217\n"
      "v_fmac_f32 v204, v119, v119\n"
      "v_mfma_f32_16x16x32_bf16 v[168:171], a[128:131], v[108:111], v[168:171]\n"
      "v_fmac_f32 v85, v162, v162\n"
      "v_mul_f32 v115, v81, v115\n"
      "v_mfma_f32_16x16x32_bf16 v[168:171], a[132:135], v[188:191], v[168:171]\n"
      "v_fmac_f32 v85, v163, v163\n"
      "v_fmac_f32 v85, v176, v176\n"
      "v_mfma_f32_16x16x32_bf16 v[168:171], a[136:139], v[156:159], v[168:171]\n"
      "v_fmac_f32 v204, v120, v120\n"
      "v_fmac_f32 v85, v177, v177\n"
      "v_mfma_f32_16x16x32_bf16 v[168:171], a[140:143], v[96:99], v[168:171]\n"
      "s_cmp_ge_u32 s95, 0x40000000\n"
      "s_cselect_b64 vcc, -1, s[56:57]\n"
      "v_fmac_f32_e64 v85, v178, v178\n"
      "v_mfma_f32_16x16x32_bf16 v[132:135], v[180:183], a[144:147], 0\n"
      "v_fmac_f32 v85, v179, v179\n"
      "v_fmac_f32 v204, v121, v121\n"
      "v_mfma_f32_16x16x32_bf16 v[132:135], v[148:151], a[148:151], v[132:135]\n"
      "v_mul_f32 v113, v81, v113\n"
      "v_mul_f32 v123, v205, v123\n"
      "v_mfma_f32_16x16x32_bf16 v[132:135], v[108:111], a[152:155], v[132:135]\n"
      "v_mul_f32 v210, v205, v210\n"
      "v_mul_f32 v174, v81, v174\n"
      "v_mfma_f32_16x16x32_bf16 v[132:135], v[188:191], a[156:159], v[132:135]\n"
      "v_cvt_pk_bf16_f32 v105, v106, v107\n"
      "v_fmac_f32 v85, v144, v144\n"
      "v_mfma_f32_16x16x32_bf16 v[132:135], v[156:159], a[160:163], v[132:135]\n"
      "v_fmac_f32 v204, v152, v152\n"
      "v_mul_f32 v221, v205, v221\n"
      "v_mfma_f32_16x16x32_bf16 v[132:135], v[96:99], a[164:167], v[132:135]\n"
      "v_mul_f32 v220, v205, v220\n"
      "v_mul_f32 v223, v205, v223\n"
      "v_mfma_f32_16x16x32_bf16 v[136:139], v[180:183], a[168:171], 0\n"
      "v_cvt_pk_bf16_f32 v220, v220, v221\n"
      "v_mul_f32 v122, v205, v122\n"
      "v_mfma_f32_16x16x32_bf16 v[136:139], v[148:151], a[172:175], v[136:139]\n"
      "v_cvt_pk_bf16_f32 v107, v114, v115\n"
      "v_fmac_f32 v204, v153, v153\n"
      "v_mfma_f32_16x16x32_bf16 v[136:139], v[108:111], a[176:179], v[136:139]\n"
      "v_mul_f32 v212, v205, v212\n"
      "v_fmac_f32_e64 v204, v154, v154\n"
      "v_mfma_f32_16x16x32_bf16 v[136:139], v[188:191], a[180:183], v[136:139]\n"
      "v_fmac_f32 v85, v145, v145\n"
      "v_fmac_f32 v85, v146, v146\n"
      "v_mfma_f32_16x16x32_bf16 v[136:139], v[156:159], a[184:187], v[136:139]\n"
      "v_fmac_f32 v204, v155, v155\n"
      "v_fmac_f32 v204, v168, v168\n"
      "v_mfma_f32_16x16x32_bf16 v[136:139], v[96:99], a[188:191], v[136:139]\n"
      "v_fmac_f32 v204, v169, v169\n"
      "v_cvt_pk_bf16_f32 v172, v172, v173\n"
      "v_mfma_f32_16x16x32_bf16 v[140:143], v[180:183], a[192:195], 0\n"
      "ds_read_b128 v[206:209], v0 offset:28672\n"
      "v_fmac_f32 v204, v170, v170\n"
      "v_mfma_f32_16x16x32_bf16 v[140:143], v[148:151], a[196:199], v[140:143]\n"
      "ds_read_b128 v[180:183], v7 offset:4096\n"
      "v_cvt_pk_bf16_f32 v106, v112, v113\n"
      "v_mfma_f32_16x16x32_bf16 v[140:143], v[108:111], a[200:203], v[140:143]\n"
      "ds_read_b128 v[148:151], v0 offset:34816\n"
      "v_fmac_f32 v85, v147, v147\n"
      "v_mfma_f32_16x16x32_bf16 v[140:143], v[188:191], a[204:207], v[140:143]\n"
      "v_fmac_f32 v204, v171, v171\n"
      "v_cvt_pk_bf16_f32 v221, v222, v223\n"
      "v_mfma_f32_16x16x32_bf16 v[140:143], v[156:159], a[208:211], v[140:143]\n"
      "v_permlane16_swap_b32 v85, v204\n"
      "v_mfma_f32_16x16x32_bf16 v[140:143], v[96:99], a[212:215], v[140:143]\n"
      "s_waitcnt lgkmcnt(3)\n"
      "v_add_f32 v85, v85, v204\n"
      "v_mov_b32 v204, v85\n"
      "v_mfma_f32_16x16x32_bf16 v[108:111], a[0:3], v[192:195], 0\n"
      "s_nop 0\n"
      "v_permlane32_swap_b32 v85, v204\n"
      "v_mfma_f32_16x16x32_bf16 v[108:111], a[4:7], v[184:187], v[108:111]\n"
      "s_waitcnt lgkmcnt(1)\n"
      "v_add_f32 v85, v85, v204\n"
      "v_cvt_pk_bf16_f32 v217, v138, v139\n"
      "v_mfma_f32_16x16x32_bf16 v[108:111], a[8:11], v[206:209], v[108:111]\n"
      "v_rsq_f32_e64 v85, v85\n"
      "v_mfma_f32_16x16x32_bf16 v[108:111], a[12:15], v[100:103], v[108:111]\n"
      "s_waitcnt lgkmcnt(0)\n"
      "v_mov_b32 v204, v85\n"
      "v_cvt_pk_bf16_f32 v199, v134, v135\n"
      "v_mfma_f32_16x16x32_bf16 v[108:111], a[16:19], v[148:151], v[108:111]\n"
      "v_permlane16_swap_b32 v85, v204\n"
      "v_mfma_f32_16x16x32_bf16 v[108:111], a[20:23], v[164:167], v[108:111]\n"
      "v_mul_f32 v169, v204, v169\n"
      "v_cvt_pk_bf16_f32 v198, v132, v133\n"
      "v_mfma_f32_16x16x32_bf16 v[156:159], a[24:27], v[192:195], 0\n"
      "v_mul_f32 v168, v204, v168\n"
      "v_mul_f32 v213, v205, v213\n"
      "v_mfma_f32_16x16x32_bf16 v[156:159], a[28:31], v[184:187], v[156:159]\n"
      "v_mul_f32_e64 v85, s50, v85\n"
      "v_cvt_pk_bf16_f32 v222, v210, v211\n"
      "v_mfma_f32_16x16x32_bf16 v[156:159], a[32:35], v[206:209], v[156:159]\n"
      "v_fma_f32 v116, v108, v108, s51\n"
      "v_mul_f32 v170, v204, v170\n"
      "v_mfma_f32_16x16x32_bf16 v[156:159], a[36:39], v[100:103], v[156:159]\n"
      "v_mul_f32 v118, v204, v118\n"
      "v_mul_f32 v144, v85, v144\n"
      "v_mfma_f32_16x16x32_bf16 v[156:159], a[40:43], v[148:151], v[156:159]\n"
      "v_mul_f32 v154, v204, v154\n"
      "v_mul_f32 v161, v85, v161\n"
      "v_mfma_f32_16x16x32_bf16 v[156:159], a[44:47], v[164:167], v[156:159]\n"
      "v_mul_f32 v125, v205, v125\n"
      "v_cvt_pk_bf16_f32 v132, v140, v141\n"
      "v_mfma_f32_16x16x32_bf16 v[112:115], a[48:51], v[192:195], 0\n"
      "v_mul_f32 v162, v85, v162\n"
      "v_fmac_f32 v116, v109, v109\n"
      "v_mfma_f32_16x16x32_bf16 v[112:115], a[52:55], v[184:187], v[112:115]\n"
      "v_fmac_f32 v116, v110, v110\n"
      "v_fmac_f32 v116, v111, v111\n"
      "v_mfma_f32_16x16x32_bf16 v[112:115], a[56:59], v[206:209], v[112:115]\n"
      "v_cvt_pk_bf16_f32 v223, v212, v213\n"
      "v_mul_f32_e64 v119, v204, v119\n"
      "v_mfma_f32_16x16x32_bf16 v[112:115], a[60:63], v[100:103], v[112:115]\n"
      "v_mul_f32_e64 v178, v85, v178\n"
      "v_cvt_pk_bf16_f32 v122, v122, v123\n"
      "v_mfma_f32_16x16x32_bf16 v[112:115], a[64:67], v[148:151], v[112:115]\n"
      "ds_read_b128 v[138:141], v7 offset:8192\n"
      "v_cvt_pk_bf16_f32 v118, v118, v119\n"
      "v_mfma_f32_16x16x32_bf16 v[112:115], a[68:71], v[164:167], v[112:115]\n"
      "v_fmac_f32 v116, v156, v156\n"
      "v_mul_f32 v121, v204, v121\n"
      "v_mul_f32_e64 v176, v85, v176\n"
      "v_mfma_f32_16x16x32_bf16 v[96:99], a[72:75], v[192:195], 0\n"
      "buffer_store_dwordx4 v[200:203], v20, s[24:27], s85 offen sc1\n"
      "v_pk_mul_f32 v[152:153], v[152:153], v[204:205] op_sel_hi:[1,0]\n"
      "v_mfma_f32_16x16x32_bf16 v[96:99], a[76:79], v[184:187], v[96:99]\n"
      "v_mul_f32 v171, v204, v171\n"
      "v_cvt_pk_bf16_f32 v173, v174, v175\n"
      "v_mfma_f32_16x16x32_bf16 v[96:99], a[80:83], v[206:209], v[96:99]\n"
      "v_mul_f32 v155, v204, v155\n"
      "v_mul_f32 v120, v204, v120\n"
      "v_mfma_f32_16x16x32_bf16 v[96:99], a[84:87], v[100:103], v[96:99]\n"
      "v_cvt_pk_bf16_f32 v123, v124, v125\n"
      "v_mul_f32 v163, v85, v163\n"
      "v_mfma_f32_16x16x32_bf16 v[96:99], a[88:91], v[148:151], v[96:99]\n"
      "ds_read_b128 v[210:213], v7 offset:16384\n"
      "v_cvt_pk_bf16_f32 v119, v120, v121\n"
      "v_mfma_f32_16x16x32_bf16 v[96:99], a[92:95], v[164:167], v[96:99]\n"
      "v_mul_f32 v145, v85, v145\n"
      "v_fmac_f32 v116, v157, v157\n"
      "v_mfma_f32_16x16x32_bf16 v[188:191], a[96:99], v[192:195], 0\n"
      "ds_read_b128 v[200:203], v7 offset:12288\n"
      "v_mul_f32_e64 v160, v85, v160\n"
      "v_mfma_f32_16x16x32_bf16 v[188:191], a[100:103], v[184:187], v[188:191]\n"
      "v_cvt_pk_bf16_f32 v160, v160, v161\n"
      "v_cvt_pk_bf16_f32 v161, v162, v163\n"
      "v_mfma_f32_16x16x32_bf16 v[188:191], a[104:107], v[206:209], v[188:191]\n"
      "v_fmac_f32_e64 v116, v158, v158\n"
      "v_fma_f32 v84, v96, v96, s51\n"
      "v_mfma_f32_16x16x32_bf16 v[188:191], a[108:111], v[100:103], v[188:191]\n"
      "buffer_store_dwordx4 v[180:183], v21, s[24:27], s85 offen sc1\n"
      "v_mfma_f32_16x16x32_bf16 v[188:191], a[112:115], v[148:151], v[188:191]\n"
      "v_cvt_pk_bf16_f32 v120, v152, v153\n"
      "v_cvt_pk_bf16_f32 v144, v144, v145\n"
      "v_mfma_f32_16x16x32_bf16 v[188:191], a[116:119], v[164:167], v[188:191]\n"
      "v_mul_f32 v177, v85, v177\n"
      "v_fmac_f32 v84, v97, v97\n"
      "v_mfma_f32_16x16x32_bf16 v[180:183], a[120:123], v[192:195], 0\n"
      "v_cvt_pk_bf16_f32 v168, v168, v169\n"
      "v_fmac_f32 v84, v98, v98\n"
      "v_mfma_f32_16x16x32_bf16 v[180:183], a[124:127], v[184:187], v[180:183]\n"
      "v_fmac_f32 v84, v99, v99\n"
      "v_cvt_pk_bf16_f32 v162, v176, v177\n"
      "v_mfma_f32_16x16x32_bf16 v[180:183], a[128:131], v[206:209], v[180:183]\n"
      "v_cvt_pk_bf16_f32 v133, v142, v143\n"
      "v_fmac_f32_e64 v116, v159, v159\n"
      "v_mfma_f32_16x16x32_bf16 v[180:183], a[132:135], v[100:103], v[180:183]\n"
      "v_fmac_f32 v84, v188, v188\n"
      "v_fmac_f32 v116, v112, v112\n"
      "v_mfma_f32_16x16x32_bf16 v[180:183], a[136:139], v[148:151], v[180:183]\n"
      "s_waitcnt lgkmcnt(0)\n"
      "v_fmac_f32 v116, v113, v113\n"
      "v_fmac_f32 v116, v114, v114\n"
      "v_mfma_f32_16x16x32_bf16 v[180:183], a[140:143], v[164:167], v[180:183]\n"
      "buffer_store_dwordx4 v[138:141], v22, s[24:27], s85 offen sc1\n"
      "s_nop 1\n"
      "v_mfma_f32_16x16x32_bf16 v[138:141], v[192:195], a[144:147], 0\n"
      "v_fmac_f32 v84, v189, v189\n"
      "v_fmac_f32 v84, v190, v190\n"
      "v_mfma_f32_16x16x32_bf16 v[138:141], v[184:187], a[148:151], v[138:141]\n"
      "v_fmac_f32 v84, v191, v191\n"
      "v_fmac_f32 v116, v115, v115\n"
      "v_mfma_f32_16x16x32_bf16 v[138:141], v[206:209], a[152:155], v[138:141]\n"
      "v_fmac_f32 v84, v180, v180\n"
      "v_fmac_f32 v84, v181, v181\n"
      "v_mfma_f32_16x16x32_bf16 v[138:141], v[100:103], a[156:159], v[138:141]\n"
      "v_fmac_f32 v84, v182, v182\n"
      "v_fmac_f32 v84, v183, v183\n"
      "v_mfma_f32_16x16x32_bf16 v[138:141], v[148:151], a[160:163], v[138:141]\n"
      "s_nop 0\n"
      "v_permlane16_swap_b32 v116, v84\n"
      "v_mfma_f32_16x16x32_bf16 v[138:141], v[164:167], a[164:167], v[138:141]\n"
      "v_add_f32 v116, v116, v84\n"
      "v_mov_b32 v84, v116\n"
      "v_mfma_f32_16x16x32_bf16 v[174:177], v[192:195], a[168:171], 0\n"
      "s_nop 0\n"
      "v_permlane32_swap_b32 v116, v84\n"
      "v_mfma_f32_16x16x32_bf16 v[174:177], v[184:187], a[172:175], v[174:177]\n"
      "buffer_store_dwordx4 v[200:203], v23, s[24:27], s85 offen sc1\n"
      "v_pk_mul_f32 v[146:147], v[146:147], v[84:85] op_sel:[0,1]\n"
      "v_mfma_f32_16x16x32_bf16 v[174:177], v[206:209], a[176:179], v[174:177]\n"
      "ds_read_b128 v[202:205], v2 offset:6144\n"
      "v_add_f32 v116, v116, v84\n"
      "v_mfma_f32_16x16x32_bf16 v[174:177], v[100:103], a[180:183], v[174:177]\n"
      "v_rsq_f32 v116, v116\n"
      "v_mfma_f32_16x16x32_bf16 v[174:177], v[148:151], a[184:187], v[174:177]\n"
      "v_cvt_pk_bf16_f32 v145, v146, v147\n"
      "v_cvt_pk_bf16_f32 v121, v154, v155\n"
      "v_mfma_f32_16x16x32_bf16 v[174:177], v[164:167], a[188:191], v[174:177]\n"
      "ds_read_b128 v[152:155], v10 offset:1200\n"
      "v_cvt_pk_bf16_f32 v216, v136, v137\n"
      "v_mfma_f32_16x16x32_bf16 v[134:137], v[192:195], a[192:195], 0\n"
      "ds_read_b128 v[192:195], v2\n"
      "v_mov_b32 v84, v116\n"
      "v_mfma_f32_16x16x32_bf16 v[134:137], v[184:187], a[196:199], v[134:137]\n"
      "s_nop 0\n"
      "v_permlane16_swap_b32_e64 v116, v84\n"
      "v_mfma_f32_16x16x32_bf16 v[134:137], v[206:209], a[200:203], v[134:137]\n"
      "ds_read_b128 v[184:187], v2 offset:3072\n"
      "v_mul_f32_e64 v116, s50, v116\n"
      "v_mfma_f32_16x16x32_bf16 v[134:137], v[100:103], a[204:207], v[134:137]\n"
      "ds_read_u16 v86, v11 offset:1232\n"
      "v_cvt_pk_bf16_f32 v81, v176, v177\n"
      "v_mfma_f32_16x16x32_bf16 v[134:137], v[148:151], a[208:211], v[134:137]\n"
      "buffer_store_dwordx4 v[210:213], v25, s[24:27], s85 offen sc1\n"
      "v_pk_mul_f32 v[158:159], v[158:159], v[116:117] op_sel_hi:[1,0]\n"
      "v_pk_mul_f32 v[114:115], v[114:115], v[116:117] op_sel_hi:[1,0]\n"
      "v_pk_mul_f32 v[180:181], v[180:181], v[84:85] op_sel_hi:[1,0]\n"
      "v_pk_mul_f32 v[108:109], v[108:109], v[116:117] op_sel_hi:[1,0]\n"
      "v_pk_mul_f32 v[96:97], v[96:97], v[84:85] op_sel_hi:[1,0]\n"
      "v_pk_mul_f32 v[112:113], v[112:113], v[116:117] op_sel_hi:[1,0]\n"
      "v_pk_mul_f32 v[188:189], v[188:189], v[84:85] op_sel_hi:[1,0]\n"
      "v_mfma_f32_16x16x32_bf16 v[134:137], v[164:167], a[212:215], v[134:137]\n"
      "s_waitcnt lgkmcnt(2)\n"
      "v_mul_f32 v182, v84, v182\n"
      "v_mul_f32 v99, v84, v99\n"
      "v_mfma_f32_16x16x32_bf16 v[148:151], a[216:219], v[192:195], 0\n"
      "s_waitcnt lgkmcnt(1)\n"
      "v_mul_f32 v157, v116, v157\n"
      "v_mul_f32 v183, v84, v183\n"
      "v_mfma_f32_16x16x32_bf16 v[148:151], a[220:223], v[184:187], v[148:151]\n"
      "v_cvt_pk_bf16_f32 v96, v96, v97\n"
      "v_mul_f32 v190, v84, v190\n"
      "v_mfma_f32_16x16x32_bf16 v[148:151], a[224:227], v[202:205], v[148:151]\n"
      "ds_read_b64 v[218:219], v10 offset:1192\n"
      "v_mul_f32 v98, v84, v98\n"
      "v_mfma_f32_16x16x32_bf16 v[100:103], a[240:243], v[192:195], 0\n"
      "v_cvt_pk_bf16_f32 v97, v98, v99\n"
      "v_cvt_pk_bf16_f32 v169, v170, v171\n"
      "v_mfma_f32_16x16x32_bf16 v[100:103], a[244:247], v[184:187], v[100:103]\n"
      "ds_read_b128 v[206:209], v2 offset:9216\n"
      "v_mul_f32_e64 v179, v85, v179\n"
      "v_mfma_f32_16x16x32_bf16 v[100:103], a[248:251], v[202:205], v[100:103]\n"
      "ds_read_b128 v[210:213], v2 offset:15360\n"
      "v_cvt_pk_bf16_f32 v112, v112, v113\n"
      "v_mfma_f32_16x16x32_bf16 v[164:167], v[232:235], v[192:195], 0\n"
      "ds_read_b128 v[192:195], v2 offset:12288\n"
      "v_mul_f32_e64 v191, v84, v191\n"
      "v_mfma_f32_16x16x32_bf16 v[164:167], v[236:239], v[184:187], v[164:167]\n"
      "v_cvt_pk_bf16_f32 v163, v178, v179\n"
      "v_cvt_pk_bf16_f32 v85, v136, v137\n"
      "v_mfma_f32_16x16x32_bf16 v[164:167], v[240:243], v[202:205], v[164:167]\n"
      "v_cvt_pk_bf16_f32 v99, v190, v191\n"
      "v_mul_f32 v110, v116, v110\n"
      "v_mfma_f32_16x16x32_bf16 v[176:179], v[220:223], v[104:107], v[40:43]\n"
      "v_cvt_pk_bf16_f32 v98, v188, v189\n"
      "v_mul_f32 v111, v116, v111\n"
      "v_mfma_f32_16x16x32_bf16 v[200:203], v[118:121], v[104:107], v[44:47]\n"
      "ds_read_b128 v[188:191], v2 offset:3328\n"
      "v_cvt_pk_bf16_f32 v84, v134, v135\n"
      "v_mfma_f32_16x16x32_bf16 v[184:187], v[96:99], v[104:107], v[48:51]\n"
      "s_waitcnt lgkmcnt(2)\n"
      "v_cvt_pk_bf16_f32 v76, v138, v139\n"
      "v_cvt_pk_bf16_f32 v108, v108, v109\n"
      "v_mfma_f32_16x16x32_bf16 v[134:137], v[152:155], v[104:107], v[24:27]\n"
      "ds_read_b128 v[104:107], v2 offset:256\n"
      "v_mul_f32 v156, v116, v156\n"
      "v_mfma_f32_16x16x16_bf16 v[176:179], v[122:123], v[172:173], v[176:179]\n"
      "v_cvt_pk_bf16_f32 v77, v140, v141\n"
      "v_cvt_pk_bf16_f32 v180, v180, v181\n"
      "v_cvt_pk_bf16_f32 v109, v110, v111\n"
      "v_mfma_f32_16x16x16_bf16 v[200:203], v[168:169], v[172:173], v[200:203]\n"
      "v_cvt_pk_bf16_f32 v181, v182, v183\n"
      "v_cvt_pk_bf16_f32 v111, v158, v159\n"
      "v_cvt_pk_bf16_f32 v80, v174, v175\n"
      "v_mfma_f32_16x16x16_bf16 v[184:187], v[180:181], v[172:173], v[184:187]\n"
      "v_cvt_pk_bf16_f32 v110, v156, v157\n"
      "v_cvt_pk_bf16_f32 v113, v114, v115\n"
      "s_waitcnt vmcnt(6)\n"
      "v_or3_b32 v126, v126, v127, v128\n"
      "v_mfma_f32_16x16x16_bf16 v[134:137], v[218:219], v[172:173], v[134:137]\n"
      "v_max3_f32 v157, v176, v177, v178\n"
      "v_max3_f32 v157, v157, v202, v203\n"
      "v_max3_f32 v140, v179, v200, v201\n"
      "v_mfma_f32_16x16x32_bf16 v[148:151], a[228:231], v[206:209], v[148:151]\n"
      "s_waitcnt lgkmcnt(1)\n"
      "v_max3_f32 v157, v157, v186, v187\n"
      "v_max3_f32 v140, v140, v184, v185\n"
      "v_mfma_f32_16x16x32_bf16 v[148:151], a[232:235], v[192:195], v[148:151]\n"
      "v_max3_f32 v157, v157, v140, v134\n"
      "v_mov_b32_e64 v140, v157\n"
      "v_mfma_f32_16x16x32_bf16 v[148:151], a[236:239], v[210:213], v[148:151]\n"
      "s_nop 0\n"
      "v_permlane16_swap_b32 v157, v140\n"
      "v_mfma_f32_16x16x32_bf16 v[100:103], a[252:255], v[206:209], v[100:103]\n"
      "v_max_f32 v157, v157, v140\n"
      "v_mov_b32 v140, v157\n"
      "v_mfma_f32_16x16x32_bf16 v[100:103], v[224:227], v[192:195], v[100:103]\n"
      "ds_read_u16 v78, v11 offset:1168\n"
      "v_permlane32_swap_b32 v157, v140\n"
      "v_mfma_f32_16x16x32_bf16 v[100:103], v[228:231], v[210:213], v[100:103]\n"
      "v_max_f32 v157, v157, v140\n"
      "v_sub_f32 v176, v176, v157\n"
      "v_mfma_f32_16x16x32_bf16 v[164:167], v[244:247], v[206:209], v[164:167]\n"
      "v_sub_f32 v184, v184, v157\n"
      "v_sub_f32 v177, v177, v157\n"
      "v_mfma_f32_16x16x32_bf16 v[164:167], v[248:251], v[192:195], v[164:167]\n"
      "s_waitcnt vmcnt(5)\n"
      "v_or3_b32 v126, v126, v129, v82\n"
      "v_sub_f32 v185, v185, v157\n"
      "v_mfma_f32_16x16x32_bf16 v[164:167], v[252:255], v[210:213], v[164:167]\n"
      "ds_read_b128 v[204:207], v2 offset:6400\n"
      "v_sub_f32 v179, v179, v157\n"
      "v_sub_f32 v203, v203, v157\n"
      "v_mfma_f32_16x16x32_bf16 v[140:143], v[220:223], v[160:163], v[52:55]\n"
      "v_exp_f32 v185, v185\n"
      "v_mfma_f32_16x16x32_bf16 v[208:211], v[118:121], v[160:163], v[56:59]\n"
      "v_sub_f32 v187, v187, v157\n"
      "v_sub_f32 v134, v134, v157\n"
      "v_mfma_f32_16x16x32_bf16 v[114:117], v[96:99], v[160:163], v[60:63]\n"
      "v_sub_f32 v178, v178, v157\n"
      "v_sub_f32 v186, v186, v157\n"
      "v_mfma_f32_16x16x32_bf16 v[170:173], v[152:155], v[160:163], v[26:29]\n"
      "s_waitcnt lgkmcnt(1)\n"
      "v_exp_f32 v177, v177\n"
      "v_exp_f32 v203, v203\n"
      "v_mfma_f32_16x16x16_bf16 v[140:143], v[122:123], v[144:145], v[140:143]\n"
      "v_exp_f32 v178, v178\n"
      "v_exp_f32 v176, v176\n"
      "v_mfma_f32_16x16x16_bf16 v[208:211], v[168:169], v[144:145], v[208:211]\n"
      "v_sub_f32 v200, v200, v157\n"
      "v_exp_f32 v187, v187\n"
      "v_cvt_pk_bf16_f32 v176, v176, v177\n"
      "v_exp_f32 v179, v179\n"
      "v_mfma_f32_16x16x16_bf16 v[114:117], v[180:181], v[144:145], v[114:117]\n"
      "v_exp_f32 v134, v134\n"
      "v_sub_f32 v202, v202, v157\n"
      "v_mfma_f32_16x16x16_bf16 v[170:173], v[218:219], v[144:145], v[170:173]\n"
      "v_max3_f32 v213, v140, v141, v142\n"
      "v_sub_f32_e64 v201, v201, v157\n"
      "v_cvt_pk_bf16_f32 v177, v178, v179\n"
      "v_mfma_f32_16x16x32_bf16 v[156:159], a[216:219], v[104:107], 0\n"
      "s_waitcnt lgkmcnt(0)\n"
      "v_exp_f32 v202, v202\n"
      "v_mfma_f32_16x16x32_bf16 v[156:159], a[220:223], v[188:191], v[156:159]\n"
      "s_add_u32 s43, s43, 1\n"
      "v_exp_f32 v200, v200\n"
      "v_mfma_f32_16x16x32_bf16 v[156:159], a[224:227], v[204:207], v[156:159]\n"
      "v_max3_f32 v213, v213, v210, v211\n"
      "v_exp_f32 v184, v184\n"
      "v_mfma_f32_16x16x32_bf16 v[160:163], a[240:243], v[104:107], 0\n"
      "v_exp_f32 v201, v201\n"
      "v_mfma_f32_16x16x32_bf16 v[160:163], a[244:247], v[188:191], v[160:163]\n"
      "v_exp_f32_e64 v186, v186\n"
      "v_cvt_pk_bf16_f32 v179, v202, v203\n"
      "v_mfma_f32_16x16x32_bf16 v[160:163], a[248:251], v[204:207], v[160:163]\n"
      "v_cvt_pk_bf16_f32 v88, v184, v185\n"
      "v_cvt_pk_bf16_f32 v178, v200, v201\n"
      "v_mfma_f32_16x16x32_bf16 v[136:139], v[232:235], v[104:107], 0\n"
      "ds_read_b128 v[192:195], v2 offset:9472\n"
      "v_max3_f32 v172, v143, v208, v209\n"
      "v_mfma_f32_16x16x32_bf16 v[136:139], v[236:239], v[188:191], v[136:139]\n"
      "ds_read_u16 v82, v11 offset:1200\n"
      "v_max3_f32 v213, v213, v116, v117\n"
      "v_mfma_f32_16x16x32_bf16 v[136:139], v[240:243], v[204:207], v[136:139]\n"
      "v_cvt_pk_bf16_f32 v90, v134, 0\n"
      "v_cvt_pk_bf16_f32 v89, v186, v187\n"
      "v_mfma_f32_16x16x32_bf16 v[188:191], v[32:35], v[176:179], 0\n"
      "s_and_b32 s47, s47, -4\n"
      "v_max3_f32 v172, v172, v114, v115\n"
      "v_max3_f32 v213, v213, v172, v170\n"
      "v_mov_b32 v206, v213\n"
      "v_mfma_f32_16x16x32_bf16 v[188:191], v[32:35], v[88:91], v[188:191]\n"
      "s_nop 0\n"
      "v_permlane16_swap_b32 v213, v206\n"
      "v_mfma_f32_16x16x32_bf16 v[172:175], v[196:199], v[176:179], 0\n"
      "v_max_f32 v213, v213, v206\n"
      "v_mov_b32 v206, v213\n"
      "v_mfma_f32_16x16x32_bf16 v[172:175], v[76:79], v[88:91], v[172:175]\n"
      "s_nop 0\n"
      "v_permlane32_swap_b32 v213, v206\n"
      "v_mfma_f32_16x16x32_bf16 v[200:203], v[214:217], v[176:179], 0\n"
      "s_waitcnt lgkmcnt(0)\n"
      "v_max_f32 v213, v213, v206\n"
      "v_sub_f32_e64 v140, v140, v213\n"
      "v_mfma_f32_16x16x32_bf16 v[200:203], v[80:83], v[88:91], v[200:203]\n"
      "v_sub_f32 v141, v141, v213\n"
      "v_rcp_f32 v188, v188\n"
      "v_sub_f32 v142, v142, v213\n"
      "v_sub_f32 v211, v211, v213\n"
      "v_sub_f32_e64 v143, v143, v213\n"
      "v_pk_mul_f32 v[174:175], v[174:175], v[188:189] op_sel_hi:[1,0]\n"
      "v_sub_f32 v209, v209, v213\n"
      "v_exp_f32 v209, v209\n"
      "v_sub_f32 v210, v210, v213\n"
      "v_mfma_f32_16x16x32_bf16 v[204:207], v[130:133], v[176:179], 0\n"
      "v_exp_f32 v142, v142\n"
      "v_exp_f32 v141, v141\n"
      "v_mfma_f32_16x16x32_bf16 v[204:207], v[84:87], v[88:91], v[204:207]\n"
      "v_exp_f32 v143, v143\n"
      "v_sub_f32 v208, v208, v213\n"
      "v_mfma_f32_16x16x32_bf16 v[104:107], v[220:223], v[108:111], v[64:67]\n"
      "v_exp_f32 v140, v140\n"
      "v_exp_f32_e64 v208, v208\n"
      "v_cvt_pk_bf16_f32 v140, v140, v141\n"
      "v_cvt_pk_bf16_f32 v141, v142, v143\n"
      "v_pk_mul_f32 v[202:203], v[202:203], v[188:189] op_sel_hi:[1,0]\n"
      "v_pk_mul_f32 v[204:205], v[204:205], v[188:189] op_sel_hi:[1,0]\n"
      "v_pk_mul_f32 v[206:207], v[206:207], v[188:189] op_sel_hi:[1,0]\n"
      "v_sub_f32_e64 v114, v114, v213\n"
      "v_pk_mul_f32 v[200:201], v[200:201], v[188:189] op_sel_hi:[1,0]\n"
      "v_pk_mul_f32 v[172:173], v[172:173], v[188:189] op_sel_hi:[1,0]\n"
      "v_exp_f32 v210, v210\n"
      "v_cvt_pk_bf16_f32 v142, v208, v209\n"
      "v_mfma_f32_16x16x32_bf16 v[144:147], v[118:121], v[108:111], v[68:71]\n"
      "v_exp_f32 v211, v211\n"
      "v_mfma_f32_16x16x32_bf16 v[176:179], v[96:99], v[108:111], v[72:75]\n"
      "ds_read_b128 v[182:185], v2 offset:12544\n"
      "v_cvt_pk_bf16_f32 v143, v210, v211\n"
      "v_mfma_f32_16x16x32_bf16 v[208:211], v[152:155], v[108:111], v[28:31]\n"
      "ds_read_b128 v[188:191], v2 offset:15616\n"
      "v_cvt_pk_bf16_f32 v204, v204, v205\n"
      "v_mfma_f32_16x16x16_bf16 v[104:107], v[122:123], v[112:113], v[104:107]\n"
      "v_sub_f32_e64 v170, v170, v213\n"
      "v_cvt_pk_bf16_f32 v205, v206, v207\n"
      "v_sub_f32 v115, v115, v213\n"
      "v_mfma_f32_16x16x16_bf16 v[144:147], v[168:169], v[112:113], v[144:147]\n"
      "v_sub_f32 v116, v116, v213\n"
      "v_sub_f32_e64 v117, v117, v213\n"
      "v_cvt_pk_bf16_f32 v172, v172, v173\n"
      "v_mfma_f32_16x16x16_bf16 v[176:179], v[180:181], v[112:113], v[176:179]\n"
      "v_cvt_pk_bf16_f32 v173, v174, v175\n"
      "v_exp_f32_e64 v116, v116\n"
      "v_mfma_f32_16x16x16_bf16 v[208:211], v[218:219], v[112:113], v[208:211]\n"
      "v_cvt_pk_bf16_f32 v175, v202, v203\n"
      "v_cvt_pk_bf16_f32 v174, v200, v201\n"
      "v_max3_f32 v90, v104, v105, v106\n"
      "v_mfma_f32_16x16x32_bf16 v[156:159], a[228:231], v[192:195], v[156:159]\n"
      "s_waitcnt lgkmcnt(1)\n"
      "v_exp_f32 v170, v170\n"
      "v_mfma_f32_16x16x32_bf16 v[156:159], a[232:235], v[182:185], v[156:159]\n"
      "s_waitcnt lgkmcnt(0)\n"
      "v_exp_f32 v115, v115\n"
      "v_mfma_f32_16x16x32_bf16 v[156:159], a[236:239], v[188:191], v[156:159]\n"
      "v_exp_f32 v117, v117\n"
      "v_mfma_f32_16x16x32_bf16 v[160:163], a[252:255], v[192:195], v[160:163]\n"
      "v_exp_f32 v114, v114\n"
      "v_mfma_f32_16x16x32_bf16 v[160:163], v[224:227], v[182:185], v[160:163]\n"
      "ds_read_b64 v[88:89], v10 offset:1184\n"
      "v_cvt_pk_bf16_f32 v94, v170, 0\n"
      "v_mfma_f32_16x16x32_bf16 v[160:163], v[228:231], v[188:191], v[160:163]\n"
      "v_cvt_pk_bf16_f32 v93, v116, v117\n"
      "v_max3_f32 v90, v90, v146, v147\n"
      "v_mfma_f32_16x16x32_bf16 v[136:139], v[244:247], v[192:195], v[136:139]\n"
      "v_max3_f32 v200, v107, v144, v145\n"
      "v_cvt_pk_bf16_f32 v92, v114, v115\n"
      "v_mfma_f32_16x16x32_bf16 v[136:139], v[248:251], v[182:185], v[136:139]\n"
      "v_max3_f32 v200, v200, v176, v177\n"
      "v_fma_f32 v170, v148, v148, s52\n"
      "v_mfma_f32_16x16x32_bf16 v[136:139], v[252:255], v[188:191], v[136:139]\n"
      "ds_read_b128 v[192:195], v10 offset:1168\n"
      "v_max3_f32 v90, v90, v178, v179\n"
      "v_mfma_f32_16x16x32_bf16 v[210:213], v[32:35], v[140:143], 0\n"
      "v_max3_f32 v90, v90, v200, v208\n"
      "v_mov_b32 v114, v90\n"
      "v_mfma_f32_16x16x32_bf16 v[210:213], v[32:35], v[92:95], v[210:213]\n"
      "s_nop 0\n"
      "v_permlane16_swap_b32_e64 v90, v114\n"
      "v_mfma_f32_16x16x32_bf16 v[182:185], v[196:199], v[140:143], 0\n"
      "ds_write_b64 v4, v[204:205] offset:18432\n"
      "v_mfma_f32_16x16x32_bf16 v[182:185], v[76:79], v[92:95], v[182:185]\n"
      "v_max_f32 v90, v90, v114\n"
      "v_mov_b32 v114, v90\n"
      "v_mfma_f32_16x16x32_bf16 v[200:203], v[214:217], v[140:143], 0\n"
      "s_nop 0\n"
      "v_permlane32_swap_b32 v90, v114\n"
      "v_mfma_f32_16x16x32_bf16 v[200:203], v[80:83], v[92:95], v[200:203]\n"
      "v_max_f32 v90, v90, v114\n"
      "v_sub_f32 v105, v105, v90\n"
      "v_mfma_f32_16x16x32_bf16 v[112:115], v[130:133], v[140:143], 0\n"
      "v_sub_f32 v107, v107, v90\n"
      "v_sub_f32 v179, v179, v90\n"
      "v_mfma_f32_16x16x32_bf16 v[112:115], v[84:87], v[92:95], v[112:115]\n"
      "s_waitcnt lgkmcnt(1)\n"
      "v_exp_f32 v105, v105\n"
      "v_mfma_f32_16x16x32_bf16 v[188:191], v[192:195], v[220:223], v[30:33]\n"
      "v_rcp_f32 v210, v210\n"
      "v_mfma_f32_16x16x32_bf16 v[204:207], v[192:195], v[118:121], v[30:33]\n"
      "ds_read_b128 v[108:111], v2 offset:512\n"
      "v_sub_f32 v145, v145, v90\n"
      "v_mfma_f32_16x16x32_bf16 v[140:143], v[192:195], v[96:99], v[30:33]\n"
      "ds_read_b128 v[96:99], v2 offset:6656\n"
      "v_sub_f32 v146, v146, v90\n"
      "v_mfma_f32_16x16x32_bf16 v[220:223], v[192:195], v[152:155], v[30:33]\n"
      "ds_read_b128 v[152:155], v2 offset:3584\n"
      "v_sub_f32 v178, v178, v90\n"
      "v_mfma_f32_16x16x16_bf16 v[188:191], v[88:89], v[122:123], v[188:191]\n"
      "v_sub_f32 v104, v104, v90\n"
      "v_mul_f32 v171, v149, v149\n"
      "v_exp_f32 v104, v104\n"
      "v_mfma_f32_16x16x16_bf16 v[204:207], v[88:89], v[168:169], v[204:207]\n"
      "v_sub_f32 v176, v176, v90\n"
      "v_exp_f32 v178, v178\n"
      "v_mfma_f32_16x16x16_bf16 v[140:143], v[88:89], v[180:181], v[140:143]\n"
      "v_sub_f32 v147, v147, v90\n"
      "v_mul_f32 v191, v157, v157\n"
      "v_exp_f32 v179, v179\n"
      "v_mfma_f32_16x16x16_bf16 v[220:223], v[88:89], v[218:219], v[220:223]\n"
      "v_fma_f32 v190, v156, v156, s52\n"
      "v_sub_f32 v106, v106, v90\n"
      "v_exp_f32 v107, v107\n"
      "v_sub_f32_e64 v208, v208, v90\n"
      "v_exp_f32_e64 v106, v106\n"
      "v_pk_mul_f32 v[182:183], v[182:183], v[210:211] op_sel_hi:[1,0]\n"
      "v_pk_fma_f32 v[190:191], v[158:159], v[158:159], v[190:191]\n"
      "v_pk_mul_f32 v[112:113], v[112:113], v[210:211] op_sel_hi:[1,0]\n"
      "v_pk_mul_f32 v[202:203], v[202:203], v[210:211] op_sel_hi:[1,0]\n"
      "v_pk_fma_f32 v[170:171], v[150:151], v[150:151], v[170:171]\n"
      "v_pk_fma_f32 v[190:191], v[160:161], v[160:161], v[190:191]\n"
      "v_cvt_pk_bf16_f32 v112, v112, v113\n"
      "v_pk_mul_f32 v[200:201], v[200:201], v[210:211] op_sel_hi:[1,0]\n"
      "v_pk_fma_f32 v[190:191], v[162:163], v[162:163], v[190:191]\n"
      "v_pk_mul_f32 v[114:115], v[114:115], v[210:211] op_sel_hi:[1,0]\n"
      "v_cvt_pk_bf16_f32 v89, v178, v179\n"
      "v_sub_f32_e64 v144, v144, v90\n"
      "v_pk_mul_f32 v[184:185], v[184:185], v[210:211] op_sel_hi:[1,0]\n"
      "v_pk_fma_f32 v[190:191], v[136:137], v[136:137], v[190:191]\n"
      "v_pk_fma_f32 v[190:191], v[138:139], v[138:139], v[190:191]\n"
      "v_pk_fma_f32 v[170:171], v[100:101], v[100:101], v[170:171]\n"
      "s_waitcnt lgkmcnt(0)\n"
      "v_mfma_f32_16x16x32_bf16 v[116:119], a[216:219], v[108:111], 0\n"
      "v_cndmask_b32 v188, v188, v204, s[58:59]\n"
      "v_sub_f32 v177, v177, v90\n"
      "v_mfma_f32_16x16x32_bf16 v[116:119], a[220:223], v[152:155], v[116:119]\n"
      "v_exp_f32_e64 v147, v147\n"
      "v_mfma_f32_16x16x32_bf16 v[116:119], a[224:227], v[96:99], v[116:119]\n"
      "ds_read_b128 v[210:213], v2 offset:12800\n"
      "v_cvt_pk_bf16_f32 v113, v114, v115\n"
      "v_mfma_f32_16x16x32_bf16 v[192:195], a[240:243], v[108:111], 0\n"
      "ds_read_b128 v[204:207], v2 offset:9728\n"
      "v_cndmask_b32 v188, v188, v140, s[60:61]\n"
      "v_mfma_f32_16x16x32_bf16 v[192:195], a[244:247], v[152:155], v[192:195]\n"
      "ds_read_b128 v[178:181], v2 offset:15872\n"
      "v_cvt_pk_bf16_f32 v182, v182, v183\n"
      "v_mfma_f32_16x16x32_bf16 v[192:195], a[248:251], v[96:99], v[192:195]\n"
      "ds_write_b128 v3, v[172:175] offset:18432\n"
      "v_pk_fma_f32 v[170:171], v[102:103], v[102:103], v[170:171]\n"
      "v_pk_fma_f32 v[170:171], v[164:165], v[164:165], v[170:171]\n"
      "v_cndmask_b32 v188, v188, v220, s[62:63]\n"
      "v_mfma_f32_16x16x32_bf16 v[140:143], v[232:235], v[108:111], 0\n"
      "v_exp_f32 v177, v177\n"
      "v_exp_f32 v176, v176\n"
      "v_max_f32_dpp v88, v188, v188 row_ror:8 row_mask:0xf bank_mask:0xf\n"
      "v_mfma_f32_16x16x32_bf16 v[140:143], v[236:239], v[152:155], v[140:143]\n"
      "s_nop 0\n"
      "v_max_f32_dpp v88, v88, v88 row_ror:4 row_mask:0xf bank_mask:0xf\n"
      "v_cvt_pk_bf16_f32 v104, v104, v105\n"
      "v_mfma_f32_16x16x32_bf16 v[140:143], v[240:243], v[96:99], v[140:143]\n"
      "s_waitcnt lgkmcnt(1)\n"
      "v_max_f32_dpp v88, v88, v88 row_ror:2 row_mask:0xf bank_mask:0xf\n"
      "v_cvt_pk_bf16_f32 v183, v184, v185\n"
      "v_add_f32 v190, v190, v191\n"
      "v_mfma_f32_16x16x32_bf16 v[116:119], a[228:231], v[204:207], v[116:119]\n"
      "v_exp_f32 v146, v146\n"
      "v_mfma_f32_16x16x32_bf16 v[116:119], a[232:235], v[210:213], v[116:119]\n"
      "ds_write_b64 v4, v[112:113] offset:18688\n"
      "v_mfma_f32_16x16x32_bf16 v[116:119], a[236:239], v[178:181], v[116:119]\n"
      "v_max_f32_dpp v88, v88, v88 row_ror:1 row_mask:0xf bank_mask:0xf\n"
      "v_exp_f32_e64 v144, v144\n"
      "v_mfma_f32_16x16x32_bf16 v[192:195], a[252:255], v[204:207], v[192:195]\n"
      "v_cvt_pk_bf16_f32 v185, v202, v203\n"
      "v_max_f32_dpp v88, v88, v88 row_bcast:15 row_mask:0xa bank_mask:0xf\n"
      "v_cvt_pk_bf16_f32 v184, v200, v201\n"
      "v_mfma_f32_16x16x32_bf16 v[192:195], v[224:227], v[210:213], v[192:195]\n"
      "v_cvt_pk_bf16_f32 v105, v106, v107\n"
      "v_cvt_pk_bf16_f32 v107, v146, v147\n"
      "v_mfma_f32_16x16x32_bf16 v[192:195], v[228:231], v[178:181], v[192:195]\n"
      "v_max_f32_dpp v88, v88, v88 row_bcast:31 row_mask:0xc bank_mask:0xf\n"
      "v_mul_f32_e64 v97, v117, v117\n"
      "v_readlane_b32 s72, v88, 63\n"
      "v_fma_f32 v96, v116, v116, s52\n"
      "v_mfma_f32_16x16x32_bf16 v[140:143], v[244:247], v[204:207], v[140:143]\n"
      "v_subrev_f32 v188, s72, v188\n"
      "v_cvt_pk_bf16_f32 v88, v176, v177\n"
      "v_mfma_f32_16x16x32_bf16 v[140:143], v[248:251], v[210:213], v[140:143]\n"
      "v_exp_f32 v145, v145\n"
      "v_mfma_f32_16x16x32_bf16 v[140:143], v[252:255], v[178:181], v[140:143]\n"
      "ds_write_b128 v3, v[182:185] offset:18688\n"
      "v_exp_f32_e64 v188, v188\n"
      "v_pk_fma_f32 v[96:97], v[118:119], v[118:119], v[96:97]\n"
      "v_pk_fma_f32 v[170:171], v[166:167], v[166:167], v[170:171]\n"
      "v_pk_fma_f32 v[96:97], v[192:193], v[192:193], v[96:97]\n"
      "v_exp_f32 v208, v208\n"
      "v_add_f32 v170, v170, v171\n"
      "v_cvt_pk_bf16_f32 v188, v188, v188\n"
      "v_cvt_pk_bf16_f32 v106, v144, v145\n"
      "v_pk_fma_f32 v[96:97], v[194:195], v[194:195], v[96:97]\n"
      "v_pk_fma_f32 v[96:97], v[140:141], v[140:141], v[96:97]\n"
      "v_pk_fma_f32 v[96:97], v[142:143], v[142:143], v[96:97]\n"
      "v_cvt_pk_bf16_f32 v90, v208, 0\n"
      "v_mfma_f32_16x16x32_bf16 v[204:207], v[32:35], v[104:107], 0\n"
      "v_permlane16_swap_b32 v170, v190\n"
      "v_mfma_f32_16x16x32_bf16 v[204:207], v[32:35], v[88:91], v[204:207]\n"
      "v_add_f32 v96, v96, v97\n"
      "v_add_f32 v170, v170, v190\n"
      "v_mov_b32 v190, v96\n"
      "v_mfma_f32_16x16x32_bf16 v[200:203], v[196:199], v[104:107], 0\n"
      "ds_write_b16 v13, v188\n"
      "v_permlane16_swap_b32_e64 v96, v190\n"
      "v_mfma_f32_16x16x32_bf16 v[200:203], v[76:79], v[88:91], v[200:203]\n"
      "s_nop 0\n"
      "v_rcp_f32 v204, v204\n"
      "v_mfma_f32_16x16x32_bf16 v[218:221], v[214:217], v[104:107], 0\n"
      "ds_read_b128 v[210:213], v14\n"
      "v_add_f32 v96, v96, v190\n"
      "v_mfma_f32_16x16x32_bf16 v[218:221], v[80:83], v[88:91], v[218:221]\n"
      "s_nop 0\n"
      "v_permlane32_swap_b32 v170, v96\n"
      "v_add_f32 v170, v170, v96\n"
      "v_mfma_f32_16x16x32_bf16 v[96:99], v[130:133], v[104:107], 0\n"
      "ds_write_b32 v31, v170 offset:1024\n"
      "v_mfma_f32_16x16x32_bf16 v[96:99], v[84:87], v[88:91], v[96:99]\n"
      "ds_read_b128 v[106:109], v14 offset:64\n"
      "ds_write_b32 v31, v126 offset:5632\n"
      "v_pk_mul_f32 v[220:221], v[220:221], v[204:205] op_sel_hi:[1,0]\n"
      "v_pk_mul_f32 v[200:201], v[200:201], v[204:205] op_sel_hi:[1,0]\n"
      "v_cvt_pk_bf16_f32 v200, v200, v201\n"
      "v_pk_mul_f32 v[218:219], v[218:219], v[204:205] op_sel_hi:[1,0]\n"
      "v_pk_mul_f32 v[202:203], v[202:203], v[204:205] op_sel_hi:[1,0]\n"
      "v_cvt_pk_bf16_f32 v201, v202, v203\n"
      "v_cvt_pk_bf16_f32 v202, v218, v219\n"
      "v_cvt_pk_bf16_f32 v203, v220, v221\n"
      "s_add_u32 s42, s42, 1\n"
      "ds_write_b128 v3, v[200:203] offset:18944\n"
      "v_pk_mul_f32 v[98:99], v[98:99], v[204:205] op_sel_hi:[1,0]\n"
      "v_pk_mul_f32 v[96:97], v[96:97], v[204:205] op_sel_hi:[1,0]\n"
      "v_cvt_pk_bf16_f32 v96, v96, v97\n"
      "s_waitcnt lgkmcnt(3)\n"
      "v_mfma_f32_16x16x32_bf16 v[218:221], v[210:213], v[32:35], 0\n"
      "s_waitcnt lgkmcnt(2)\n"
      "v_cvt_pk_bf16_f32 v97, v98, v99\n"
      "v_add_u32 v11, 2336, v11\n"
      "v_mfma_f32_16x16x32_bf16 v[218:221], v[106:109], v[32:35], v[218:221]\n"
      "ds_write_b64 v4, v[96:97] offset:18944\n"
      "v_add_u32 v10, 2336, v10\n"
      "s_waitcnt lgkmcnt(0)\n"
      "s_barrier\n"
      "s_cbranch_vccnz L_disp0_%=\n"
      "L_max0_%=:\n"
      "v_mfma_f32_16x16x32_bf16 v[204:207], v[210:213], v[196:199], 0\n"
      "ds_read_b128 v[180:183], v0\n"
      "v_readlane_b32 s85, v37, s42\n"
      "v_mfma_f32_16x16x32_bf16 v[204:207], v[106:109], v[76:79], v[204:207]\n"
      "ds_read_b128 v[112:115], v9 offset:1024\n"
      "s_add_u32 m0, s64, 18432\n"
      "v_mfma_f32_16x16x32_bf16 v[176:179], v[210:213], v[214:217], 0\n"
      "ds_read_b128 v[144:147], v1 offset:8192\n"
      "ds_read_b128 v[188:191], v0 offset:6144\n"
      "v_mfma_f32_16x16x32_bf16 v[176:179], v[106:109], v[80:83], v[176:179]\n"
      "buffer_load_dwordx4 v16, s[16:19], s45 offen  lds\n"
      "v_mfma_f32_16x16x32_bf16 v[96:99], v[210:213], v[130:133], 0\n"
      "s_add_u32 m0, s64, 22528\n"
      "ds_read_b128 v[184:187], v1\n"
      "v_mfma_f32_16x16x32_bf16 v[96:99], v[106:109], v[84:87], v[96:99]\n"
      "ds_read_b128 v[206:209], v0 offset:14336\n"
      "s_waitcnt lgkmcnt(5)\n"
      "v_mfma_f32_16x16x32_bf16 v[210:213], a[0:3], v[180:183], 0\n"
      "ds_read_b128 v[168:171], v1 offset:6144\n"
      "v_readlane_b32 s84, v37, s43\n"
      "v_mfma_f32_16x16x32_bf16 v[104:107], a[24:27], v[180:183], 0\n"
      "s_waitcnt lgkmcnt(5)\n"
      "v_add_f32 v114, v114, v115\n"
      "v_add_f32 v112, v112, v113\n"
      "v_mfma_f32_16x16x32_bf16 v[152:155], a[48:51], v[180:183], 0\n"
      "buffer_load_dwordx4 v17, s[16:19], s45 offen  lds\n"
      "v_mfma_f32_16x16x32_bf16 v[172:175], a[72:75], v[180:183], 0\n"
      "s_or_b32 s95, s84, s85\n"
      "v_add_f32 v112, v112, v114\n"
      "v_cndmask_b32 v204, v204, v176, s[58:59]\n"
      "v_mfma_f32_16x16x32_bf16 v[124:127], a[96:99], v[180:183], 0\n"
      "s_waitcnt lgkmcnt(2)\n"
      "v_rsq_f32 v112, v112\n"
      "v_mfma_f32_16x16x32_bf16 v[120:123], a[120:123], v[180:183], 0\n"
      "ds_read_b128 v[200:203], v1 offset:12288\n"
      "v_mfma_f32_16x16x32_bf16 v[210:213], a[4:7], v[184:187], v[210:213]\n"
      "ds_read_b128 v[176:179], v0 offset:12288\n"
      "v_mul_f32 v112, 0x415db3d7, v112\n"
      "v_mfma_f32_16x16x32_bf16 v[210:213], a[8:11], v[188:191], v[210:213]\n"
      "s_add_u32 m0, s64, 26624\n"
      "v_readlane_b32 s86, v38, s43\n"
      "v_mov_b32 v114, v112\n"
      "v_mfma_f32_16x16x32_bf16 v[104:107], a[28:31], v[184:187], v[104:107]\n"
      "s_or_b32 s95, s95, s48\n"
      "v_permlane32_swap_b32 v112, v114\n"
      "v_mfma_f32_16x16x32_bf16 v[104:107], a[32:35], v[188:191], v[104:107]\n"
      "s_cmp_ge_u32 s95, 0x40000000\n"
      "s_cselect_b32 s94, -1, -2\n"
      "v_mov_b32 v113, v112\n"
      "v_mfma_f32_16x16x32_bf16 v[152:155], a[52:55], v[184:187], v[152:155]\n"
      "buffer_load_dwordx4 v18, s[16:19], s45 offen  lds\n"
      "v_mfma_f32_16x16x32_bf16 v[152:155], a[56:59], v[188:191], v[152:155]\n"
      "v_permlane16_swap_b32_e64 v112, v113\n"
      "v_mfma_f32_16x16x32_bf16 v[172:175], a[76:79], v[184:187], v[172:175]\n"
      "s_add_u32 m0, s64, 30720\n"
      "v_mul_f32 v150, v112, v150\n"
      "v_mul_f32 v165, v112, v165\n"
      "v_mfma_f32_16x16x32_bf16 v[172:175], a[80:83], v[188:191], v[172:175]\n"
      "v_mul_f32 v148, v112, v148\n"
      "v_mul_f32 v100, v112, v100\n"
      "v_mfma_f32_16x16x32_bf16 v[124:127], a[100:103], v[184:187], v[124:127]\n"
      "v_mul_f32 v102, v112, v102\n"
      "v_mul_f32 v160, v113, v160\n"
      "v_mfma_f32_16x16x32_bf16 v[124:127], a[104:107], v[188:191], v[124:127]\n"
      "s_waitcnt lgkmcnt(0)\n"
      "v_mul_f32 v151, v112, v151\n"
      "v_mul_f32_e64 v136, v113, v136\n"
      "v_mfma_f32_16x16x32_bf16 v[120:123], a[124:127], v[184:187], v[120:123]\n"
      "buffer_load_dwordx4 v19, s[16:19], s45 offen  lds\n"
      "v_mfma_f32_16x16x32_bf16 v[120:123], a[128:131], v[188:191], v[120:123]\n"
      "s_add_u32 m0, s65, 34816\n"
      "v_mul_f32 v149, v112, v149\n"
      "v_mul_f32 v101, v112, v101\n"
      "v_mfma_f32_16x16x32_bf16 v[210:213], a[12:15], v[168:171], v[210:213]\n"
      "v_cvt_pk_bf16_f32 v148, v148, v149\n"
      "v_cvt_pk_bf16_f32 v149, v150, v151\n"
      "v_mfma_f32_16x16x32_bf16 v[210:213], a[16:19], v[176:179], v[210:213]\n"
      "v_cvt_pk_bf16_f32 v150, v100, v101\n"
      "v_mul_f32_e64 v103, v112, v103\n"
      "v_mfma_f32_16x16x32_bf16 v[210:213], a[20:23], v[200:203], v[210:213]\n"
      "v_cvt_pk_bf16_f32 v151, v102, v103\n"
      "v_mul_f32_e64 v164, v112, v164\n"
      "v_mfma_f32_16x16x32_bf16 v[104:107], a[36:39], v[168:171], v[104:107]\n"
      "buffer_load_dwordx4 v15, s[16:19], s45 offen  lds\n"
      "v_mfma_f32_16x16x32_bf16 v[104:107], a[40:43], v[176:179], v[104:107]\n"
      "s_cmp_gt_u32 s42, s44\n"
      "s_cselect_b32 s95, s94, s95\n"
      "v_cvt_pk_bf16_f32 v164, v164, v165\n"
      "v_mfma_f32_16x16x32_bf16 v[104:107], a[44:47], v[200:203], v[104:107]\n"
      "ds_write_b128 v5, v[148:151]\n"
      "v_pk_mul_f32 v[166:167], v[166:167], v[112:113] op_sel_hi:[1,0]\n"
      "v_pk_mul_f32 v[158:159], v[158:159], v[112:113] op_sel:[0,1]\n"
      "v_pk_mul_f32 v[156:157], v[156:157], v[112:113] op_sel:[0,1]\n"
      "v_pk_mul_f32 v[162:163], v[162:163], v[112:113] op_sel:[0,1]\n"
      "v_mfma_f32_16x16x32_bf16 v[152:155], a[60:63], v[168:171], v[152:155]\n"
      "buffer_load_dwordx4 v[108:111], v36, s[28:31], s47 offen\n"
      "v_mfma_f32_16x16x32_bf16 v[152:155], a[64:67], v[176:179], v[152:155]\n"
      "ds_read_b128 v[100:103], v1 offset:2048\n"
      "v_cvt_pk_bf16_f32 v165, v166, v167\n"
      "v_mfma_f32_16x16x32_bf16 v[152:155], a[68:71], v[200:203], v[152:155]\n"
      "ds_write_b64 v6, v[164:165]\n"
      "v_mfma_f32_16x16x32_bf16 v[172:175], a[84:87], v[168:171], v[172:175]\n"
      "ds_read_b128 v[148:151], v0 offset:2048\n"
      "v_mul_f32_e64 v139, v113, v139\n"
      "v_mfma_f32_16x16x32_bf16 v[172:175], a[88:91], v[176:179], v[172:175]\n"
      "v_cvt_pk_bf16_f32 v156, v156, v157\n"
      "v_mul_f32 v143, v114, v143\n"
      "v_mfma_f32_16x16x32_bf16 v[172:175], a[92:95], v[200:203], v[172:175]\n"
      "v_mul_f32 v116, v114, v116\n"
      "v_fma_f32 v205, v210, v210, s51\n"
      "v_mfma_f32_16x16x32_bf16 v[124:127], a[108:111], v[168:171], v[124:127]\n"
      "buffer_load_dword v90, v29, s[28:31], s47 offen\n"
      "v_mfma_f32_16x16x32_bf16 v[124:127], a[112:115], v[176:179], v[124:127]\n"
      "v_mov_b32_dpp v218, v218 quad_perm:[0,0,0,0] row_mask:0xf bank_mask:0xf\n"
      "v_mul_f32 v161, v113, v161\n"
      "v_mfma_f32_16x16x32_bf16 v[124:127], a[116:119], v[200:203], v[124:127]\n"
      "v_mul_f32 v117, v114, v117\n"
      "v_mul_f32_e64 v137, v113, v137\n"
      "v_mfma_f32_16x16x32_bf16 v[120:123], a[132:135], v[168:171], v[120:123]\n"
      "v_cvt_pk_bf16_f32 v157, v158, v159\n"
      "v_mul_f32_e64 v192, v114, v192\n"
      "v_mfma_f32_16x16x32_bf16 v[120:123], a[136:139], v[176:179], v[120:123]\n"
      "v_fma_f32 v82, v172, v172, s51\n"
      "v_mul_f32_e64 v138, v113, v138\n"
      "v_mfma_f32_16x16x32_bf16 v[120:123], a[140:143], v[200:203], v[120:123]\n"
      "v_cvt_pk_bf16_f32 v136, v136, v137\n"
      "v_cvt_pk_bf16_f32 v158, v160, v161\n"
      "v_mfma_f32_16x16x32_bf16 v[196:199], v[180:183], a[144:147], 0\n"
      "v_cvt_pk_bf16_f32 v159, v162, v163\n"
      "v_fmac_f32_e64 v205, v211, v211\n"
      "v_mfma_f32_16x16x32_bf16 v[196:199], v[184:187], a[148:151], v[196:199]\n"
      "ds_read_b128 v[160:163], v9 offset:5632\n"
      "v_fmac_f32_e64 v205, v212, v212\n"
      "v_mfma_f32_16x16x32_bf16 v[196:199], v[188:191], a[152:155], v[196:199]\n"
      "ds_write_b128 v5, v[156:159] offset:6144\n"
      "v_mfma_f32_16x16x32_bf16 v[196:199], v[168:171], a[156:159], v[196:199]\n"
      "v_fmac_f32 v205, v213, v213\n"
      "v_fmac_f32 v205, v104, v104\n"
      "v_mfma_f32_16x16x32_bf16 v[196:199], v[176:179], a[160:163], v[196:199]\n"
      "ds_read_b128 v[156:159], v0 offset:8192\n"
      "v_cvt_pk_bf16_f32 v137, v138, v139\n"
      "v_mfma_f32_16x16x32_bf16 v[196:199], v[200:203], a[164:167], v[196:199]\n"
      "v_rcp_f32_e64 v218, v218\n"
      "v_mfma_f32_16x16x32_bf16 v[214:217], v[180:183], a[168:171], 0\n"
      "v_mul_f32 v141, v114, v141\n"
      "v_mul_f32 v119, v114, v119\n"
      "v_mfma_f32_16x16x32_bf16 v[214:217], v[184:187], a[172:175], v[214:217]\n"
      "v_cndmask_b32 v204, v204, v96, s[60:61]\n"
      "v_fmac_f32 v205, v105, v105\n"
      "v_mfma_f32_16x16x32_bf16 v[214:217], v[188:191], a[176:179], v[214:217]\n"
      "v_fmac_f32 v82, v173, v173\n"
      "v_fmac_f32 v205, v106, v106\n"
      "v_mfma_f32_16x16x32_bf16 v[214:217], v[168:171], a[180:183], v[214:217]\n"
      "v_mul_f32 v140, v114, v140\n"
      "v_mul_f32 v142, v114, v142\n"
      "v_mfma_f32_16x16x32_bf16 v[214:217], v[176:179], a[184:187], v[214:217]\n"
      "v_fmac_f32 v205, v107, v107\n"
      "v_fmac_f32_e64 v82, v174, v174\n"
      "v_mfma_f32_16x16x32_bf16 v[214:217], v[200:203], a[188:191], v[214:217]\n"
      "ds_write_b64 v6, v[136:137] offset:6144\n"
      "v_mfma_f32_16x16x32_bf16 v[130:133], v[180:183], a[192:195], 0\n"
      "ds_read_b128 v[164:167], v1 offset:14336\n"
      "v_mul_f32_e64 v195, v114, v195\n"
      "v_mfma_f32_16x16x32_bf16 v[130:133], v[184:187], a[196:199], v[130:133]\n"
      "v_fmac_f32 v205, v152, v152\n"
      "v_fmac_f32 v82, v175, v175\n"
      "v_mfma_f32_16x16x32_bf16 v[130:133], v[188:191], a[200:203], v[130:133]\n"
      "v_cvt_pk_bf16_f32 v140, v140, v141\n"
      "v_mul_f32_e64 v194, v114, v194\n"
      "v_mfma_f32_16x16x32_bf16 v[130:133], v[168:171], a[204:207], v[130:133]\n"
      "ds_read_b128 v[180:183], v0 offset:16384\n"
      "v_cvt_pk_bf16_f32 v141, v142, v143\n"
      "v_mfma_f32_16x16x32_bf16 v[130:133], v[176:179], a[208:211], v[130:133]\n"
      "v_fmac_f32 v205, v153, v153\n"
      "v_mul_f32 v204, v218, v204\n"
      "v_mfma_f32_16x16x32_bf16 v[130:133], v[200:203], a[212:215], v[130:133]\n"
      "s_waitcnt lgkmcnt(3)\n"
      "v_fmac_f32 v82, v124, v124\n"
      "v_fmac_f32 v82, v125, v125\n"
      "v_mfma_f32_16x16x32_bf16 v[96:99], a[0:3], v[148:151], 0\n"
      "ds_read_b128 v[200:203], v0 offset:4096\n"
      "v_fmac_f32 v82, v126, v126\n"
      "v_mfma_f32_16x16x32_bf16 v[96:99], a[4:7], v[100:103], v[96:99]\n"
      "ds_read_b128 v[218:221], v1 offset:10240\n"
      "v_fmac_f32 v82, v127, v127\n"
      "v_mfma_f32_16x16x32_bf16 v[96:99], a[8:11], v[156:159], v[96:99]\n"
      "s_waitcnt lgkmcnt(3)\n"
      "v_mul_f32_e64 v193, v114, v193\n"
      "v_cvt_pk_bf16_f32 v116, v116, v117\n"
      "v_mfma_f32_16x16x32_bf16 v[96:99], a[12:15], v[144:147], v[96:99]\n"
      "ds_write_b64 v6, v[140:141] offset:12288\n"
      "v_mfma_f32_16x16x32_bf16 v[96:99], a[16:19], v[206:209], v[96:99]\n"
      "ds_read_b128 v[184:187], v1 offset:16384\n"
      "v_fmac_f32 v82, v120, v120\n"
      "v_mfma_f32_16x16x32_bf16 v[96:99], a[20:23], v[164:167], v[96:99]\n"
      "v_fmac_f32 v205, v154, v154\n"
      "v_cvt_pk_bf16_f32 v130, v130, v131\n"
      "v_mfma_f32_16x16x32_bf16 v[140:143], a[24:27], v[148:151], 0\n"
      "v_fmac_f32 v82, v121, v121\n"
      "v_fmac_f32 v205, v155, v155\n"
      "v_mfma_f32_16x16x32_bf16 v[140:143], a[28:31], v[100:103], v[140:143]\n"
      "v_fmac_f32 v82, v122, v122\n"
      "v_fmac_f32 v82, v123, v123\n"
      "v_mfma_f32_16x16x32_bf16 v[140:143], a[32:35], v[156:159], v[140:143]\n"
      "v_cvt_pk_bf16_f32 v204, v204, v204\n"
      "v_fma_f32 v80, v96, v96, s51\n"
      "v_mfma_f32_16x16x32_bf16 v[140:143], a[36:39], v[144:147], v[140:143]\n"
      "v_permlane16_swap_b32 v205, v82\n"
      "v_mfma_f32_16x16x32_bf16 v[140:143], a[40:43], v[206:209], v[140:143]\n"
      "v_cvt_pk_bf16_f32 v214, v214, v215\n"
      "v_mul_f32 v118, v114, v118\n"
      "v_mfma_f32_16x16x32_bf16 v[140:143], a[44:47], v[164:167], v[140:143]\n"
      "v_cvt_pk_bf16_f32 v117, v118, v119\n"
      "v_add_f32 v205, v205, v82\n"
      "v_mfma_f32_16x16x32_bf16 v[176:179], a[48:51], v[148:151], 0\n"
      "v_mov_b32 v82, v205\n"
      "v_cvt_pk_bf16_f32 v196, v196, v197\n"
      "v_mfma_f32_16x16x32_bf16 v[176:179], a[52:55], v[100:103], v[176:179]\n"
      "v_permlane32_swap_b32 v205, v82\n"
      "v_mfma_f32_16x16x32_bf16 v[176:179], a[56:59], v[156:159], v[176:179]\n"
      "v_cvt_pk_bf16_f32 v118, v192, v193\n"
      "v_add_f32 v205, v205, v82\n"
      "v_mfma_f32_16x16x32_bf16 v[176:179], a[60:63], v[144:147], v[176:179]\n"
      "v_fmac_f32_e64 v80, v97, v97\n"
      "v_cvt_pk_bf16_f32 v119, v194, v195\n"
      "v_mfma_f32_16x16x32_bf16 v[176:179], a[64:67], v[206:209], v[176:179]\n"
      "ds_write_b128 v5, v[116:119] offset:12288\n"
      "v_mfma_f32_16x16x32_bf16 v[176:179], a[68:71], v[164:167], v[176:179]\n"
      "v_rsq_f32 v205, v205\n"
      "v_mfma_f32_16x16x32_bf16 v[112:115], a[72:75], v[148:151], 0\n"
      "v_fmac_f32 v80, v98, v98\n"
      "v_mov_b32 v82, v205\n"
      "v_mfma_f32_16x16x32_bf16 v[112:115], a[76:79], v[100:103], v[112:115]\n"
      "ds_write_b16 v12, v204 offset:16\n"
      "v_mfma_f32_16x16x32_bf16 v[112:115], a[80:83], v[156:159], v[112:115]\n"
      "v_permlane16_swap_b32 v205, v82\n"
      "v_mfma_f32_16x16x32_bf16 v[112:115], a[84:87], v[144:147], v[112:115]\n"
      "v_mul_f32 v205, s50, v205\n"
      "v_mul_f32 v213, v205, v213\n"
      "v_mfma_f32_16x16x32_bf16 v[112:115], a[88:91], v[206:209], v[112:115]\n"
      "v_mul_f32 v155, v205, v155\n"
      "v_fmac_f32 v80, v99, v99\n"
      "v_mfma_f32_16x16x32_bf16 v[112:115], a[92:95], v[164:167], v[112:115]\n"
      "v_cvt_pk_bf16_f32 v131, v132, v133\n"
      "v_mul_f32 v154, v205, v154\n"
      "v_mfma_f32_16x16x32_bf16 v[116:119], a[96:99], v[148:151], 0\n"
      "v_mul_f32 v120, v82, v120\n"
      "v_or3_b32 v160, v160, v161, v162\n"
      "v_mfma_f32_16x16x32_bf16 v[116:119], a[100:103], v[100:103], v[116:119]\n"
      "v_mul_f32 v172, v82, v172\n"
      "v_mul_f32 v153, v205, v153\n"
      "v_mfma_f32_16x16x32_bf16 v[116:119], a[104:107], v[156:159], v[116:119]\n"
      "v_mul_f32 v173, v82, v173\n"
      "v_cvt_pk_bf16_f32 v172, v172, v173\n"
      "v_mfma_f32_16x16x32_bf16 v[116:119], a[108:111], v[144:147], v[116:119]\n"
      "v_mul_f32 v122, v82, v122\n"
      "v_mul_f32_e64 v104, v205, v104\n"
      "v_mfma_f32_16x16x32_bf16 v[116:119], a[112:115], v[206:209], v[116:119]\n"
      "v_fma_f32 v204, v112, v112, s51\n"
      "v_mul_f32 v174, v82, v174\n"
      "v_mfma_f32_16x16x32_bf16 v[116:119], a[116:119], v[164:167], v[116:119]\n"
      "v_fmac_f32 v204, v113, v113\n"
      "v_mul_f32 v152, v205, v152\n"
      "v_mfma_f32_16x16x32_bf16 v[192:195], a[120:123], v[148:151], 0\n"
      "v_mul_f32 v175, v82, v175\n"
      "v_mul_f32 v210, v205, v210\n"
      "v_mfma_f32_16x16x32_bf16 v[192:195], a[124:127], v[100:103], v[192:195]\n"
      "v_fmac_f32 v204, v114, v114\n"
      "v_mul_f32 v121, v82, v121\n"
      "v_mfma_f32_16x16x32_bf16 v[192:195], a[128:131], v[156:159], v[192:195]\n"
      "v_mul_f32 v107, v205, v107\n"
      "v_cvt_pk_bf16_f32 v215, v216, v217\n"
      "v_mfma_f32_16x16x32_bf16 v[192:195], a[132:135], v[144:147], v[192:195]\n"
      "ds_read_u16 v78, v11\n"
      "v_mul_f32 v211, v205, v211\n"
      "v_mfma_f32_16x16x32_bf16 v[192:195], a[136:139], v[206:209], v[192:195]\n"
      "v_mul_f32 v123, v82, v123\n"
      "v_fmac_f32_e64 v80, v140, v140\n"
      "v_mfma_f32_16x16x32_bf16 v[192:195], a[140:143], v[164:167], v[192:195]\n"
      "v_fmac_f32 v80, v141, v141\n"
      "v_mul_f32 v212, v205, v212\n"
      "v_mfma_f32_16x16x32_bf16 v[132:135], v[148:151], a[144:147], 0\n"
      "v_fmac_f32 v204, v115, v115\n"
      "v_mul_f32 v125, v82, v125\n"
      "v_mfma_f32_16x16x32_bf16 v[132:135], v[100:103], a[148:151], v[132:135]\n"
      "v_fmac_f32 v204, v116, v116\n"
      "v_cvt_pk_bf16_f32 v210, v210, v211\n"
      "v_mfma_f32_16x16x32_bf16 v[132:135], v[156:159], a[152:155], v[132:135]\n"
      "v_add_u32 v12, 32, v12\n"
      "v_fmac_f32 v204, v117, v117\n"
      "v_mfma_f32_16x16x32_bf16 v[132:135], v[144:147], a[156:159], v[132:135]\n"
      "v_cvt_pk_bf16_f32 v211, v212, v213\n"
      "v_fmac_f32 v204, v118, v118\n"
      "v_mfma_f32_16x16x32_bf16 v[132:135], v[206:209], a[160:163], v[132:135]\n"
      "v_bitop3_b32 v160, v160, v163, s33 bitop3:0xa8\n"
      "v_cvt_pk_bf16_f32 v152, v152, v153\n"
      "v_mfma_f32_16x16x32_bf16 v[132:135], v[164:167], a[164:167], v[132:135]\n"
      "v_fmac_f32 v80, v142, v142\n"
      "v_fmac_f32 v204, v119, v119\n"
      "v_mfma_f32_16x16x32_bf16 v[136:139], v[148:151], a[168:171], 0\n"
      "v_fmac_f32 v204, v192, v192\n"
      "v_fmac_f32 v80, v143, v143\n"
      "v_mfma_f32_16x16x32_bf16 v[136:139], v[100:103], a[172:175], v[136:139]\n"
      "v_cvt_pk_bf16_f32 v197, v198, v199\n"
      "v_cmp_ne_u32 s[56:57], 0, v160\n"
      "v_mfma_f32_16x16x32_bf16 v[136:139], v[156:159], a[176:179], v[136:139]\n"
      "v_mul_f32 v126, v82, v126\n"
      "v_fmac_f32 v80, v176, v176\n"
      "v_mfma_f32_16x16x32_bf16 v[136:139], v[144:147], a[180:183], v[136:139]\n"
      "v_fmac_f32 v80, v177, v177\n"
      "v_cvt_pk_bf16_f32 v173, v174, v175\n"
      "v_mfma_f32_16x16x32_bf16 v[136:139], v[206:209], a[184:187], v[136:139]\n"
      "ds_read_b128 v[188:191], v1 offset:4096\n"
      "v_fmac_f32 v80, v178, v178\n"
      "v_mfma_f32_16x16x32_bf16 v[136:139], v[164:167], a[188:191], v[136:139]\n"
      "v_fmac_f32 v204, v193, v193\n"
      "v_fmac_f32 v204, v194, v194\n"
      "v_mfma_f32_16x16x32_bf16 v[168:171], v[148:151], a[192:195], 0\n"
      "ds_read_b128 v[148:151], v0 offset:10240\n"
      "v_fmac_f32 v80, v179, v179\n"
      "v_mfma_f32_16x16x32_bf16 v[168:171], v[100:103], a[196:199], v[168:171]\n"
      "v_mul_f32 v127, v82, v127\n"
      "v_fmac_f32 v204, v195, v195\n"
      "v_mfma_f32_16x16x32_bf16 v[168:171], v[156:159], a[200:203], v[168:171]\n"
      "v_mul_f32 v105, v205, v105\n"
      "v_mul_f32 v124, v82, v124\n"
      "v_mfma_f32_16x16x32_bf16 v[168:171], v[144:147], a[204:207], v[168:171]\n"
      "ds_read_u16 v82, v11 offset:32\n"
      "v_mul_f32 v106, v205, v106\n"
      "v_mfma_f32_16x16x32_bf16 v[168:171], v[206:209], a[208:211], v[168:171]\n"
      "v_permlane16_swap_b32 v80, v204\n"
      "v_mfma_f32_16x16x32_bf16 v[168:171], v[164:167], a[212:215], v[168:171]\n"
      "s_waitcnt lgkmcnt(2)\n"
      "v_add_f32 v80, v80, v204\n"
      "v_mov_b32 v204, v80\n"
      "v_mfma_f32_16x16x32_bf16 v[144:147], a[0:3], v[200:203], 0\n"
      "s_nop 0\n"
      "v_permlane32_swap_b32 v80, v204\n"
      "v_add_f32 v80, v80, v204\n"
      "v_mfma_f32_16x16x32_bf16 v[144:147], a[4:7], v[188:191], v[144:147]\n"
      "s_waitcnt lgkmcnt(1)\n"
      "v_rsq_f32_e64 v80, v80\n"
      "v_mfma_f32_16x16x32_bf16 v[144:147], a[8:11], v[148:151], v[144:147]\n"
      "v_mov_b32 v204, v80\n"
      "v_cvt_pk_bf16_f32 v198, v132, v133\n"
      "v_mfma_f32_16x16x32_bf16 v[144:147], a[12:15], v[218:221], v[144:147]\n"
      "v_permlane16_swap_b32 v80, v204\n"
      "v_mfma_f32_16x16x32_bf16 v[144:147], a[16:19], v[180:183], v[144:147]\n"
      "v_mul_f32 v193, v204, v193\n"
      "v_mul_f32_e64 v80, s50, v80\n"
      "v_mfma_f32_16x16x32_bf16 v[144:147], a[20:23], v[184:187], v[144:147]\n"
      "s_add_u32 s43, s43, 1\n"
      "v_cvt_pk_bf16_f32 v153, v154, v155\n"
      "v_cvt_pk_bf16_f32 v120, v120, v121\n"
      "v_mfma_f32_16x16x32_bf16 v[158:161], a[24:27], v[200:203], 0\n"
      "v_mul_f32 v113, v204, v113\n"
      "v_mul_f32 v118, v204, v118\n"
      "v_mfma_f32_16x16x32_bf16 v[158:161], a[28:31], v[188:191], v[158:161]\n"
      "v_mul_f32_e64 v195, v204, v195\n"
      "v_fma_f32 v133, v144, v144, s51\n"
      "v_mfma_f32_16x16x32_bf16 v[158:161], a[32:35], v[148:151], v[158:161]\n"
      "ds_read_b128 v[100:103], v7 offset:18432\n"
      "v_mul_f32 v112, v204, v112\n"
      "v_mfma_f32_16x16x32_bf16 v[158:161], a[36:39], v[218:221], v[158:161]\n"
      "v_mul_f32 v192, v204, v192\n"
      "v_mul_f32_e64 v97, v80, v97\n"
      "v_mfma_f32_16x16x32_bf16 v[158:161], a[40:43], v[180:183], v[158:161]\n"
      "v_cvt_pk_bf16_f32 v213, v106, v107\n"
      "v_cvt_pk_bf16_f32 v199, v134, v135\n"
      "v_mfma_f32_16x16x32_bf16 v[158:161], a[44:47], v[184:187], v[158:161]\n"
      "v_mul_f32 v98, v80, v98\n"
      "v_mul_f32 v141, v80, v141\n"
      "v_mfma_f32_16x16x32_bf16 v[154:157], a[48:51], v[200:203], 0\n"
      "v_mul_f32_e64 v96, v80, v96\n"
      "v_cvt_pk_bf16_f32 v112, v112, v113\n"
      "v_mfma_f32_16x16x32_bf16 v[154:157], a[52:55], v[188:191], v[154:157]\n"
      "v_cvt_pk_bf16_f32 v192, v192, v193\n"
      "v_cvt_pk_bf16_f32 v212, v104, v105\n"
      "v_mfma_f32_16x16x32_bf16 v[154:157], a[56:59], v[148:151], v[154:157]\n"
      "ds_read_b128 v[164:167], v7 offset:30720\n"
      "v_cvt_pk_bf16_f32 v174, v124, v125\n"
      "v_mfma_f32_16x16x32_bf16 v[154:157], a[60:63], v[218:221], v[154:157]\n"
      "v_cvt_pk_bf16_f32 v121, v122, v123\n"
      "v_fmac_f32_e64 v133, v145, v145\n"
      "v_mfma_f32_16x16x32_bf16 v[154:157], a[64:67], v[180:183], v[154:157]\n"
      "ds_read_b128 v[104:107], v7 offset:22528\n"
      "v_cvt_pk_bf16_f32 v175, v126, v127\n"
      "v_mfma_f32_16x16x32_bf16 v[154:157], a[68:71], v[184:187], v[154:157]\n"
      "v_mul_f32 v99, v80, v99\n"
      "v_fmac_f32 v133, v146, v146\n"
      "v_mfma_f32_16x16x32_bf16 v[122:125], a[72:75], v[200:203], 0\n"
      "s_waitcnt lgkmcnt(2)\n"
      "v_fmac_f32 v133, v147, v147\n"
      "v_cvt_pk_bf16_f32 v216, v136, v137\n"
      "v_mfma_f32_16x16x32_bf16 v[122:125], a[76:79], v[188:191], v[122:125]\n"
      "buffer_store_dwordx4 v[100:103], v20, s[24:27], s46 offen sc1\n"
      "v_pk_mul_f32 v[116:117], v[116:117], v[204:205] op_sel_hi:[1,0]\n"
      "v_pk_mul_f32 v[176:177], v[176:177], v[80:81] op_sel_hi:[1,0]\n"
      "v_mfma_f32_16x16x32_bf16 v[122:125], a[80:83], v[148:151], v[122:125]\n"
      "v_mul_f32 v140, v80, v140\n"
      "v_fmac_f32 v133, v158, v158\n"
      "v_mfma_f32_16x16x32_bf16 v[122:125], a[84:87], v[218:221], v[122:125]\n"
      "ds_read_b128 v[100:103], v7 offset:26624\n"
      "v_cvt_pk_bf16_f32 v132, v168, v169\n"
      "v_mfma_f32_16x16x32_bf16 v[122:125], a[88:91], v[180:183], v[122:125]\n"
      "v_fmac_f32 v133, v159, v159\n"
      "v_mul_f32 v194, v204, v194\n"
      "v_mfma_f32_16x16x32_bf16 v[122:125], a[92:95], v[184:187], v[122:125]\n"
      "v_mul_f32_e64 v115, v204, v115\n"
      "v_cvt_pk_bf16_f32 v193, v194, v195\n"
      "v_mfma_f32_16x16x32_bf16 v[134:137], a[96:99], v[200:203], 0\n"
      "v_cvt_pk_bf16_f32 v96, v96, v97\n"
      "v_mul_f32 v114, v204, v114\n"
      "v_mfma_f32_16x16x32_bf16 v[134:137], a[100:103], v[188:191], v[134:137]\n"
      "s_waitcnt lgkmcnt(1)\n"
      "v_mul_f32_e64 v119, v204, v119\n"
      "v_cvt_pk_bf16_f32 v113, v114, v115\n"
      "v_mfma_f32_16x16x32_bf16 v[134:137], a[104:107], v[148:151], v[134:137]\n"
      "s_cmp_ge_u32 s95, 0x40000000\n"
      "s_cselect_b64 vcc, -1, s[56:57]\n"
      "v_fmac_f32_e64 v133, v160, v160\n"
      "v_mfma_f32_16x16x32_bf16 v[134:137], a[108:111], v[218:221], v[134:137]\n"
      "buffer_store_dwordx4 v[104:107], v21, s[24:27], s46 offen sc1\n"
      "v_pk_mul_f32 v[178:179], v[178:179], v[80:81] op_sel_hi:[1,0]\n"
      "v_mfma_f32_16x16x32_bf16 v[134:137], a[112:115], v[180:183], v[134:137]\n"
      "v_mul_f32 v143, v80, v143\n"
      "v_fmac_f32 v133, v161, v161\n"
      "v_mfma_f32_16x16x32_bf16 v[134:137], a[116:119], v[184:187], v[134:137]\n"
      "v_fma_f32 v208, v122, v122, s51\n"
      "v_fmac_f32_e64 v208, v123, v123\n"
      "v_mfma_f32_16x16x32_bf16 v[104:107], a[120:123], v[200:203], 0\n"
      "v_fmac_f32_e64 v133, v154, v154\n"
      "v_cvt_pk_bf16_f32 v114, v116, v117\n"
      "v_mfma_f32_16x16x32_bf16 v[104:107], a[124:127], v[188:191], v[104:107]\n"
      "v_cvt_pk_bf16_f32 v97, v98, v99\n"
      "v_fmac_f32 v208, v124, v124\n"
      "v_mfma_f32_16x16x32_bf16 v[104:107], a[128:131], v[148:151], v[104:107]\n"
      "v_fmac_f32 v208, v125, v125\n"
      "v_fmac_f32_e64 v133, v155, v155\n"
      "v_mfma_f32_16x16x32_bf16 v[104:107], a[132:135], v[218:221], v[104:107]\n"
      "v_cvt_pk_bf16_f32 v115, v118, v119\n"
      "v_fmac_f32 v208, v134, v134\n"
      "v_mfma_f32_16x16x32_bf16 v[104:107], a[136:139], v[180:183], v[104:107]\n"
      "s_waitcnt lgkmcnt(0)\n"
      "v_fmac_f32 v133, v156, v156\n"
      "v_fmac_f32 v208, v135, v135\n"
      "v_mfma_f32_16x16x32_bf16 v[104:107], a[140:143], v[184:187], v[104:107]\n"
      "buffer_store_dwordx4 v[100:103], v22, s[24:27], s46 offen sc1\n"
      "v_mfma_f32_16x16x32_bf16 v[126:129], v[200:203], a[144:147], 0\n"
      "v_mul_f32 v142, v80, v142\n"
      "v_fmac_f32 v133, v157, v157\n"
      "v_mfma_f32_16x16x32_bf16 v[126:129], v[188:191], a[148:151], v[126:129]\n"
      "v_fmac_f32 v208, v136, v136\n"
      "v_fmac_f32 v208, v137, v137\n"
      "v_mfma_f32_16x16x32_bf16 v[126:129], v[148:151], a[152:155], v[126:129]\n"
      "v_fmac_f32 v208, v104, v104\n"
      "v_fmac_f32 v208, v105, v105\n"
      "v_mfma_f32_16x16x32_bf16 v[126:129], v[218:221], a[156:159], v[126:129]\n"
      "v_fmac_f32 v208, v106, v106\n"
      "v_fmac_f32 v208, v107, v107\n"
      "v_mfma_f32_16x16x32_bf16 v[126:129], v[180:183], a[160:163], v[126:129]\n"
      "s_nop 0\n"
      "v_permlane16_swap_b32 v133, v208\n"
      "v_mfma_f32_16x16x32_bf16 v[126:129], v[184:187], a[164:167], v[126:129]\n"
      "v_add_f32 v133, v133, v208\n"
      "v_mov_b32 v208, v133\n"
      "v_mfma_f32_16x16x32_bf16 v[204:207], v[200:203], a[168:171], 0\n"
      "s_nop 0\n"
      "v_permlane32_swap_b32 v133, v208\n"
      "v_mfma_f32_16x16x32_bf16 v[204:207], v[188:191], a[172:175], v[204:207]\n"
      "buffer_store_dwordx4 v[164:167], v23, s[24:27], s46 offen sc1\n"
      "v_mfma_f32_16x16x32_bf16 v[204:207], v[148:151], a[176:179], v[204:207]\n"
      "ds_read_b128 v[100:103], v7 offset:34816\n"
      "v_add_f32 v133, v133, v208\n"
      "v_mfma_f32_16x16x32_bf16 v[204:207], v[218:221], a[180:183], v[204:207]\n"
      "v_rsq_f32 v133, v133\n"
      "v_mfma_f32_16x16x32_bf16 v[204:207], v[180:183], a[184:187], v[204:207]\n"
      "ds_read_b128 v[116:119], v2 offset:21504\n"
      "v_mov_b32 v208, v133\n"
      "v_mfma_f32_16x16x32_bf16 v[204:207], v[184:187], a[188:191], v[204:207]\n"
      "s_nop 0\n"
      "v_permlane16_swap_b32_e64 v133, v208\n"
      "v_mfma_f32_16x16x32_bf16 v[166:169], v[200:203], a[192:195], 0\n"
      "ds_read_b128 v[200:203], v2 offset:18432\n"
      "v_cvt_pk_bf16_f32 v176, v176, v177\n"
      "v_mfma_f32_16x16x32_bf16 v[166:169], v[188:191], a[196:199], v[166:169]\n"
      "ds_read_b128 v[188:191], v2 offset:24576\n"
      "v_mul_f32 v133, s50, v133\n"
      "v_mfma_f32_16x16x32_bf16 v[166:169], v[148:151], a[200:203], v[166:169]\n"
      "s_waitcnt lgkmcnt(3)\n"
      "v_mul_f32_e64 v161, v133, v161\n"
      "v_cvt_pk_bf16_f32 v217, v138, v139\n"
      "v_mfma_f32_16x16x32_bf16 v[166:169], v[218:221], a[204:207], v[166:169]\n"
      "ds_read_b128 v[220:223], v10 offset:32\n"
      "v_cvt_pk_bf16_f32 v98, v140, v141\n"
      "v_mfma_f32_16x16x32_bf16 v[166:169], v[180:183], a[208:211], v[166:169]\n"
      "buffer_store_dwordx4 v[100:103], v25, s[24:27], s46 offen sc1\n"
      "v_pk_mul_f32 v[104:105], v[104:105], v[208:209] op_sel_hi:[1,0]\n"
      "v_pk_mul_f32 v[136:137], v[136:137], v[208:209] op_sel_hi:[1,0]\n"
      "v_pk_mul_f32 v[144:145], v[144:145], v[132:133] op_sel:[0,1]\n"
      "v_pk_mul_f32 v[154:155], v[154:155], v[132:133] op_sel:[0,1]\n"
      "v_pk_mul_f32 v[124:125], v[124:125], v[208:209] op_sel_hi:[1,0]\n"
      "v_pk_mul_f32 v[134:135], v[134:135], v[208:209] op_sel_hi:[1,0]\n"
      "v_pk_mul_f32 v[122:123], v[122:123], v[208:209] op_sel_hi:[1,0]\n"
      "v_mfma_f32_16x16x32_bf16 v[166:169], v[184:187], a[212:215], v[166:169]\n"
      "s_waitcnt lgkmcnt(1)\n"
      "v_cvt_pk_bf16_f32 v80, v204, v205\n"
      "v_mul_f32 v146, v133, v146\n"
      "v_mfma_f32_16x16x32_bf16 v[148:151], a[216:219], v[200:203], 0\n"
      "v_cvt_pk_bf16_f32 v177, v178, v179\n"
      "v_mul_f32_e64 v160, v133, v160\n"
      "v_mfma_f32_16x16x32_bf16 v[148:151], a[220:223], v[116:119], v[148:151]\n"
      "ds_read_b64 v[218:219], v10 offset:24\n"
      "v_cvt_pk_bf16_f32 v76, v126, v127\n"
      "v_mfma_f32_16x16x32_bf16 v[148:151], a[224:227], v[188:191], v[148:151]\n"
      "ds_read_b128 v[138:141], v2 offset:30720\n"
      "v_cvt_pk_bf16_f32 v144, v144, v145\n"
      "v_mfma_f32_16x16x32_bf16 v[100:103], a[240:243], v[200:203], 0\n"
      "v_cvt_pk_bf16_f32 v99, v142, v143\n"
      "v_cvt_pk_bf16_f32 v84, v166, v167\n"
      "v_mfma_f32_16x16x32_bf16 v[100:103], a[244:247], v[116:119], v[100:103]\n"
      "ds_read_b128 v[184:187], v2 offset:27648\n"
      "v_cvt_pk_bf16_f32 v81, v206, v207\n"
      "v_mfma_f32_16x16x32_bf16 v[100:103], a[248:251], v[188:191], v[100:103]\n"
      "v_cvt_pk_bf16_f32 v122, v122, v123\n"
      "v_mul_f32 v147, v133, v147\n"
      "v_mfma_f32_16x16x32_bf16 v[164:167], v[232:235], v[200:203], 0\n"
      "v_mul_f32 v106, v208, v106\n"
      "v_mul_f32_e64 v156, v133, v156\n"
      "v_mfma_f32_16x16x32_bf16 v[164:167], v[236:239], v[116:119], v[164:167]\n"
      "v_cvt_pk_bf16_f32 v123, v124, v125\n"
      "v_mul_f32 v107, v208, v107\n"
      "v_mfma_f32_16x16x32_bf16 v[164:167], v[240:243], v[188:191], v[164:167]\n"
      "s_waitcnt lgkmcnt(1)\n"
      "v_cvt_pk_bf16_f32 v125, v136, v137\n"
      "v_cvt_pk_bf16_f32 v104, v104, v105\n"
      "v_mfma_f32_16x16x32_bf16 v[116:119], v[172:175], v[210:213], v[40:43]\n"
      "ds_read_u16 v86, v11 offset:64\n"
      "v_cvt_pk_bf16_f32 v124, v134, v135\n"
      "v_mfma_f32_16x16x32_bf16 v[202:205], v[112:115], v[210:213], v[44:47]\n"
      "ds_read_b128 v[206:209], v2 offset:21760\n"
      "v_mul_f32_e64 v159, v133, v159\n"
      "v_mfma_f32_16x16x32_bf16 v[134:137], v[122:125], v[210:213], v[48:51]\n"
      "ds_read_b128 v[188:191], v2 offset:33792\n"
      "v_cvt_pk_bf16_f32 v105, v106, v107\n"
      "v_mfma_f32_16x16x32_bf16 v[180:183], v[220:223], v[210:213], v[24:27]\n"
      "s_waitcnt lgkmcnt(3)\n"
      "v_mul_f32 v157, v133, v157\n"
      "v_cvt_pk_bf16_f32 v85, v168, v169\n"
      "v_mfma_f32_16x16x16_bf16 v[116:119], v[120:121], v[152:153], v[116:119]\n"
      "v_cvt_pk_bf16_f32 v154, v154, v155\n"
      "s_waitcnt vmcnt(6)\n"
      "v_or3_b32 v108, v108, v109, v110\n"
      "v_cvt_pk_bf16_f32 v145, v146, v147\n"
      "v_mfma_f32_16x16x16_bf16 v[202:205], v[192:193], v[152:153], v[202:205]\n"
      "v_mul_f32 v158, v133, v158\n"
      "v_cvt_pk_bf16_f32 v77, v128, v129\n"
      "v_cvt_pk_bf16_f32 v155, v156, v157\n"
      "v_mfma_f32_16x16x16_bf16 v[134:137], v[104:105], v[152:153], v[134:137]\n"
      "v_cvt_pk_bf16_f32 v146, v158, v159\n"
      "v_cvt_pk_bf16_f32 v147, v160, v161\n"
      "v_max3_f32 v158, v116, v117, v118\n"
      "v_mfma_f32_16x16x16_bf16 v[180:183], v[218:219], v[152:153], v[180:183]\n"
      "v_max3_f32 v212, v119, v202, v203\n"
      "v_cvt_pk_bf16_f32 v133, v170, v171\n"
      "v_max3_f32 v158, v158, v204, v205\n"
      "v_mfma_f32_16x16x32_bf16 v[148:151], a[228:231], v[184:187], v[148:151]\n"
      "s_waitcnt lgkmcnt(0)\n"
      "v_max3_f32 v158, v158, v136, v137\n"
      "v_max3_f32 v212, v212, v134, v135\n"
      "v_mfma_f32_16x16x32_bf16 v[148:151], a[232:235], v[138:141], v[148:151]\n"
      "v_max3_f32 v158, v158, v212, v180\n"
      "v_mov_b32 v170, v158\n"
      "v_mfma_f32_16x16x32_bf16 v[148:151], a[236:239], v[188:191], v[148:151]\n"
      "s_nop 0\n"
      "v_permlane16_swap_b32 v158, v170\n"
      "v_mfma_f32_16x16x32_bf16 v[100:103], a[252:255], v[184:187], v[100:103]\n"
      "ds_read_b128 v[210:213], v2 offset:18688\n"
      "v_max_f32 v158, v158, v170\n"
      "v_mfma_f32_16x16x32_bf16 v[100:103], v[224:227], v[138:141], v[100:103]\n"
      "s_waitcnt vmcnt(5)\n"
      "v_or3_b32 v108, v108, v111, v90\n"
      "v_mov_b32_e64 v170, v158\n"
      "v_mfma_f32_16x16x32_bf16 v[100:103], v[228:231], v[188:191], v[100:103]\n"
      "s_nop 0\n"
      "v_permlane32_swap_b32 v158, v170\n"
      "v_mfma_f32_16x16x32_bf16 v[164:167], v[244:247], v[184:187], v[164:167]\n"
      "v_max_f32 v158, v158, v170\n"
      "v_sub_f32 v203, v203, v158\n"
      "v_mfma_f32_16x16x32_bf16 v[164:167], v[248:251], v[138:141], v[164:167]\n"
      "ds_read_b128 v[140:143], v2 offset:24832\n"
      "v_sub_f32 v137, v137, v158\n"
      "v_mfma_f32_16x16x32_bf16 v[164:167], v[252:255], v[188:191], v[164:167]\n"
      "v_sub_f32 v136, v136, v158\n"
      "v_sub_f32 v117, v117, v158\n"
      "v_mfma_f32_16x16x32_bf16 v[168:171], v[172:175], v[96:99], v[52:55]\n"
      "v_sub_f32 v204, v204, v158\n"
      "v_sub_f32 v119, v119, v158\n"
      "v_sub_f32 v180, v180, v158\n"
      "v_sub_f32_e64 v116, v116, v158\n"
      "v_mfma_f32_16x16x32_bf16 v[188:191], v[112:115], v[96:99], v[56:59]\n"
      "v_exp_f32 v180, v180\n"
      "v_sub_f32 v202, v202, v158\n"
      "v_mfma_f32_16x16x32_bf16 v[184:187], v[122:125], v[96:99], v[60:63]\n"
      "v_exp_f32_e64 v203, v203\n"
      "v_mfma_f32_16x16x32_bf16 v[126:129], v[220:223], v[96:99], v[26:29]\n"
      "s_waitcnt lgkmcnt(1)\n"
      "v_cvt_pk_bf16_f32 v90, v180, 0\n"
      "v_sub_f32 v135, v135, v158\n"
      "v_mfma_f32_16x16x16_bf16 v[168:171], v[120:121], v[176:177], v[168:171]\n"
      "v_exp_f32 v137, v137\n"
      "v_exp_f32 v117, v117\n"
      "v_mfma_f32_16x16x16_bf16 v[188:191], v[192:193], v[176:177], v[188:191]\n"
      "v_exp_f32 v204, v204\n"
      "v_sub_f32 v134, v134, v158\n"
      "v_sub_f32_e64 v118, v118, v158\n"
      "v_mfma_f32_16x16x16_bf16 v[184:187], v[104:105], v[176:177], v[184:187]\n"
      "v_exp_f32 v116, v116\n"
      "v_exp_f32 v202, v202\n"
      "v_sub_f32 v205, v205, v158\n"
      "v_mfma_f32_16x16x16_bf16 v[126:129], v[218:219], v[176:177], v[126:129]\n"
      "v_exp_f32 v119, v119\n"
      "v_cvt_pk_bf16_f32 v116, v116, v117\n"
      "v_mfma_f32_16x16x32_bf16 v[156:159], a[216:219], v[210:213], 0\n"
      "v_exp_f32_e64 v136, v136\n"
      "v_max3_f32 v153, v168, v169, v170\n"
      "v_mfma_f32_16x16x32_bf16 v[156:159], a[220:223], v[206:209], v[156:159]\n"
      "s_waitcnt lgkmcnt(0)\n"
      "v_exp_f32 v118, v118\n"
      "v_mfma_f32_16x16x32_bf16 v[156:159], a[224:227], v[140:143], v[156:159]\n"
      "v_cvt_pk_bf16_f32 v89, v136, v137\n"
      "v_cvt_pk_bf16_f32 v117, v118, v119\n"
      "v_mfma_f32_16x16x32_bf16 v[160:163], a[240:243], v[210:213], 0\n"
      "v_exp_f32_e64 v134, v134\n"
      "v_mfma_f32_16x16x32_bf16 v[160:163], a[244:247], v[206:209], v[160:163]\n"
      "v_cvt_pk_bf16_f32 v118, v202, v203\n"
      "v_max3_f32 v203, v171, v188, v189\n"
      "v_mfma_f32_16x16x32_bf16 v[160:163], a[248:251], v[140:143], v[160:163]\n"
      "ds_write_b32 v31, v108 offset:4608\n"
      "v_mfma_f32_16x16x32_bf16 v[136:139], v[232:235], v[210:213], 0\n"
      "v_exp_f32_e64 v205, v205\n"
      "v_mfma_f32_16x16x32_bf16 v[136:139], v[236:239], v[206:209], v[136:139]\n"
      "v_max3_f32 v153, v153, v190, v191\n"
      "v_cvt_pk_bf16_f32 v119, v204, v205\n"
      "v_mfma_f32_16x16x32_bf16 v[136:139], v[240:243], v[140:143], v[136:139]\n"
      "v_exp_f32_e64 v135, v135\n"
      "v_mfma_f32_16x16x32_bf16 v[140:143], v[32:35], v[116:119], 0\n"
      "v_cvt_pk_bf16_f32 v88, v134, v135\n"
      "v_max3_f32 v153, v153, v186, v187\n"
      "v_max3_f32 v203, v203, v184, v185\n"
      "v_max3_f32 v153, v153, v203, v126\n"
      "v_mfma_f32_16x16x32_bf16 v[140:143], v[32:35], v[88:91], v[140:143]\n"
      "ds_read_b128 v[210:213], v2 offset:27904\n"
      "v_mov_b32 v97, v153\n"
      "v_mfma_f32_16x16x32_bf16 v[106:109], v[196:199], v[116:119], 0\n"
      "s_nop 0\n"
      "v_permlane16_swap_b32 v153, v97\n"
      "v_mfma_f32_16x16x32_bf16 v[106:109], v[76:79], v[88:91], v[106:109]\n"
      "v_max_f32 v153, v153, v97\n"
      "v_mov_b32 v97, v153\n"
      "v_mfma_f32_16x16x32_bf16 v[202:205], v[214:217], v[116:119], 0\n"
      "v_rcp_f32 v140, v140\n"
      "v_mfma_f32_16x16x32_bf16 v[202:205], v[80:83], v[88:91], v[202:205]\n"
      "v_permlane32_swap_b32 v153, v97\n"
      "v_max_f32 v153, v153, v97\n"
      "v_mfma_f32_16x16x32_bf16 v[96:99], v[130:133], v[116:119], 0\n"
      "v_sub_f32 v169, v169, v153\n"
      "v_sub_f32 v170, v170, v153\n"
      "v_mfma_f32_16x16x32_bf16 v[96:99], v[84:87], v[88:91], v[96:99]\n"
      "v_sub_f32 v188, v188, v153\n"
      "v_sub_f32 v186, v186, v153\n"
      "v_sub_f32 v185, v185, v153\n"
      "v_exp_f32 v186, v186\n"
      "v_pk_mul_f32 v[106:107], v[106:107], v[140:141] op_sel_hi:[1,0]\n"
      "v_pk_mul_f32 v[108:109], v[108:109], v[140:141] op_sel_hi:[1,0]\n"
      "v_sub_f32 v184, v184, v153\n"
      "v_pk_mul_f32 v[202:203], v[202:203], v[140:141] op_sel_hi:[1,0]\n"
      "v_exp_f32 v169, v169\n"
      "v_sub_f32_e64 v189, v189, v153\n"
      "v_pk_mul_f32 v[98:99], v[98:99], v[140:141] op_sel_hi:[1,0]\n"
      "v_sub_f32 v171, v171, v153\n"
      "v_sub_f32 v126, v126, v153\n"
      "v_pk_mul_f32 v[96:97], v[96:97], v[140:141] op_sel_hi:[1,0]\n"
      "v_cvt_pk_bf16_f32 v96, v96, v97\n"
      "v_pk_mul_f32 v[204:205], v[204:205], v[140:141] op_sel_hi:[1,0]\n"
      "v_cvt_pk_bf16_f32 v106, v106, v107\n"
      "v_cvt_pk_bf16_f32 v107, v108, v109\n"
      "v_mfma_f32_16x16x32_bf16 v[206:209], v[172:175], v[144:147], v[64:67]\n"
      "s_waitcnt lgkmcnt(0)\n"
      "v_cvt_pk_bf16_f32 v108, v202, v203\n"
      "v_exp_f32_e64 v184, v184\n"
      "v_mfma_f32_16x16x32_bf16 v[180:183], v[112:115], v[144:147], v[68:71]\n"
      "s_and_b32 s86, s86, -4\n"
      "v_cvt_pk_bf16_f32 v109, v204, v205\n"
      "v_cvt_pk_bf16_f32 v97, v98, v99\n"
      "v_mfma_f32_16x16x32_bf16 v[202:205], v[122:125], v[144:147], v[72:75]\n"
      "ds_read_b128 v[116:119], v2 offset:30976\n"
      "v_sub_f32 v191, v191, v153\n"
      "v_mfma_f32_16x16x32_bf16 v[176:179], v[220:223], v[144:147], v[28:31]\n"
      "ds_read_b128 v[140:143], v2 offset:34048\n"
      "v_exp_f32 v126, v126\n"
      "v_mfma_f32_16x16x16_bf16 v[206:209], v[120:121], v[154:155], v[206:209]\n"
      "v_sub_f32 v187, v187, v153\n"
      "v_exp_f32 v185, v185\n"
      "v_mfma_f32_16x16x16_bf16 v[180:183], v[192:193], v[154:155], v[180:183]\n"
      "v_exp_f32 v187, v187\n"
      "v_sub_f32 v190, v190, v153\n"
      "v_mfma_f32_16x16x16_bf16 v[202:205], v[104:105], v[154:155], v[202:205]\n"
      "v_sub_f32 v168, v168, v153\n"
      "v_exp_f32 v189, v189\n"
      "v_mfma_f32_16x16x16_bf16 v[176:179], v[218:219], v[154:155], v[176:179]\n"
      "v_exp_f32_e64 v168, v168\n"
      "v_cvt_pk_bf16_f32 v92, v184, v185\n"
      "v_mfma_f32_16x16x32_bf16 v[156:159], a[228:231], v[210:213], v[156:159]\n"
      "s_waitcnt lgkmcnt(1)\n"
      "v_exp_f32_e64 v188, v188\n"
      "v_mfma_f32_16x16x32_bf16 v[156:159], a[232:235], v[116:119], v[156:159]\n"
      "s_waitcnt lgkmcnt(0)\n"
      "v_cvt_pk_bf16_f32 v168, v168, v169\n"
      "v_exp_f32 v170, v170\n"
      "v_mfma_f32_16x16x32_bf16 v[156:159], a[236:239], v[140:143], v[156:159]\n"
      "v_exp_f32 v171, v171\n"
      "v_mfma_f32_16x16x32_bf16 v[160:163], a[252:255], v[210:213], v[160:163]\n"
      "v_exp_f32 v191, v191\n"
      "v_mfma_f32_16x16x32_bf16 v[160:163], v[224:227], v[116:119], v[160:163]\n"
      "v_exp_f32 v190, v190\n"
      "v_mfma_f32_16x16x32_bf16 v[160:163], v[228:231], v[140:143], v[160:163]\n"
      "v_max3_f32 v89, v206, v207, v208\n"
      "v_cvt_pk_bf16_f32 v169, v170, v171\n"
      "v_cvt_pk_bf16_f32 v171, v190, v191\n"
      "v_mfma_f32_16x16x32_bf16 v[136:139], v[244:247], v[210:213], v[136:139]\n"
      "ds_read_b128 v[210:213], v10\n"
      "v_cvt_pk_bf16_f32 v170, v188, v189\n"
      "v_mfma_f32_16x16x32_bf16 v[136:139], v[248:251], v[116:119], v[136:139]\n"
      "v_max3_f32 v189, v209, v180, v181\n"
      "v_cvt_pk_bf16_f32 v94, v126, 0\n"
      "v_mfma_f32_16x16x32_bf16 v[136:139], v[252:255], v[140:143], v[136:139]\n"
      "v_max3_f32 v189, v189, v202, v203\n"
      "v_cvt_pk_bf16_f32 v93, v186, v187\n"
      "v_mfma_f32_16x16x32_bf16 v[140:143], v[32:35], v[168:171], 0\n"
      "v_max3_f32 v89, v89, v182, v183\n"
      "v_max3_f32 v89, v89, v204, v205\n"
      "v_mfma_f32_16x16x32_bf16 v[140:143], v[32:35], v[92:95], v[140:143]\n"
      "v_max3_f32 v89, v89, v189, v176\n"
      "v_mov_b32 v116, v89\n"
      "v_mfma_f32_16x16x32_bf16 v[186:189], v[196:199], v[168:171], 0\n"
      "s_nop 0\n"
      "v_permlane16_swap_b32_e64 v89, v116\n"
      "v_mfma_f32_16x16x32_bf16 v[186:189], v[76:79], v[92:95], v[186:189]\n"
      "ds_read_b128 v[152:155], v2 offset:18944\n"
      "v_max_f32 v89, v89, v116\n"
      "v_mfma_f32_16x16x32_bf16 v[126:129], v[214:217], v[168:171], 0\n"
      "ds_read_b64 v[146:147], v10 offset:16\n"
      "v_mov_b32 v116, v89\n"
      "v_mfma_f32_16x16x32_bf16 v[126:129], v[80:83], v[92:95], v[126:129]\n"
      "ds_write_b64 v4, v[96:97]\n"
      "v_mfma_f32_16x16x32_bf16 v[96:99], v[130:133], v[168:171], 0\n"
      "v_permlane32_swap_b32_e64 v89, v116\n"
      "v_mfma_f32_16x16x32_bf16 v[96:99], v[84:87], v[92:95], v[96:99]\n"
      "s_waitcnt lgkmcnt(3)\n"
      "v_max_f32 v89, v89, v116\n"
      "v_sub_f32 v208, v208, v89\n"
      "v_mfma_f32_16x16x32_bf16 v[168:171], v[210:213], v[172:175], v[30:33]\n"
      "v_rcp_f32 v140, v140\n"
      "v_mfma_f32_16x16x32_bf16 v[116:119], v[210:213], v[112:115], v[30:33]\n"
      "ds_read_b128 v[112:115], v2 offset:25088\n"
      "v_sub_f32 v203, v203, v89\n"
      "v_mfma_f32_16x16x32_bf16 v[142:145], v[210:213], v[122:125], v[30:33]\n"
      "s_waitcnt lgkmcnt(2)\n"
      "v_sub_f32 v209, v209, v89\n"
      "v_sub_f32 v176, v176, v89\n"
      "v_mfma_f32_16x16x32_bf16 v[172:175], v[210:213], v[220:223], v[30:33]\n"
      "ds_read_b128 v[210:213], v2 offset:22016\n"
      "v_sub_f32_e64 v207, v207, v89\n"
      "v_mfma_f32_16x16x16_bf16 v[168:171], v[146:147], v[120:121], v[168:171]\n"
      "v_sub_f32 v181, v181, v89\n"
      "v_exp_f32 v208, v208\n"
      "v_mfma_f32_16x16x16_bf16 v[116:119], v[146:147], v[192:193], v[116:119]\n"
      "v_exp_f32 v209, v209\n"
      "v_sub_f32 v205, v205, v89\n"
      "v_fma_f32 v190, v148, v148, s52\n"
      "v_mfma_f32_16x16x16_bf16 v[142:145], v[146:147], v[104:105], v[142:145]\n"
      "v_sub_f32 v206, v206, v89\n"
      "v_mul_f32 v191, v149, v149\n"
      "v_exp_f32 v207, v207\n"
      "v_mul_f32 v179, v157, v157\n"
      "v_fma_f32 v178, v156, v156, s52\n"
      "v_sub_f32_e64 v202, v202, v89\n"
      "v_pk_fma_f32 v[190:191], v[150:151], v[150:151], v[190:191]\n"
      "v_pk_mul_f32 v[188:189], v[188:189], v[140:141] op_sel_hi:[1,0]\n"
      "v_pk_fma_f32 v[178:179], v[158:159], v[158:159], v[178:179]\n"
      "v_pk_mul_f32 v[98:99], v[98:99], v[140:141] op_sel_hi:[1,0]\n"
      "v_pk_mul_f32 v[126:127], v[126:127], v[140:141] op_sel_hi:[1,0]\n"
      "v_cndmask_b32 v168, v168, v116, s[58:59]\n"
      "v_mfma_f32_16x16x16_bf16 v[172:175], v[146:147], v[218:219], v[172:175]\n"
      "v_exp_f32 v203, v203\n"
      "v_sub_f32 v204, v204, v89\n"
      "v_mfma_f32_16x16x32_bf16 v[116:119], a[216:219], v[152:155], 0\n"
      "s_waitcnt lgkmcnt(0)\n"
      "v_exp_f32 v202, v202\n"
      "v_sub_f32 v180, v180, v89\n"
      "v_mfma_f32_16x16x32_bf16 v[116:119], a[220:223], v[210:213], v[116:119]\n"
      "ds_read_b128 v[218:221], v2 offset:31232\n"
      "v_sub_f32 v183, v183, v89\n"
      "v_mfma_f32_16x16x32_bf16 v[116:119], a[224:227], v[112:115], v[116:119]\n"
      "v_sub_f32 v182, v182, v89\n"
      "v_exp_f32 v206, v206\n"
      "v_mfma_f32_16x16x32_bf16 v[192:195], a[240:243], v[152:155], 0\n"
      "ds_read_b128 v[144:147], v2 offset:34304\n"
      "v_cndmask_b32 v168, v168, v142, s[60:61]\n"
      "v_cndmask_b32 v168, v168, v172, s[62:63]\n"
      "v_mfma_f32_16x16x32_bf16 v[192:195], a[244:247], v[210:213], v[192:195]\n"
      "ds_read_b128 v[170:173], v2 offset:28160\n"
      "v_cvt_pk_bf16_f32 v206, v206, v207\n"
      "v_mfma_f32_16x16x32_bf16 v[192:195], a[248:251], v[112:115], v[192:195]\n"
      "ds_write_b128 v3, v[106:109]\n"
      "v_pk_mul_f32 v[186:187], v[186:187], v[140:141] op_sel_hi:[1,0]\n"
      "v_pk_mul_f32 v[96:97], v[96:97], v[140:141] op_sel_hi:[1,0]\n"
      "v_pk_mul_f32 v[128:129], v[128:129], v[140:141] op_sel_hi:[1,0]\n"
      "v_pk_fma_f32 v[178:179], v[160:161], v[160:161], v[178:179]\n"
      "v_pk_fma_f32 v[190:191], v[100:101], v[100:101], v[190:191]\n"
      "v_mfma_f32_16x16x32_bf16 v[140:143], v[232:235], v[152:155], 0\n"
      "v_cvt_pk_bf16_f32 v207, v208, v209\n"
      "v_max_f32_dpp v153, v168, v168 row_ror:8 row_mask:0xf bank_mask:0xf\n"
      "v_mfma_f32_16x16x32_bf16 v[140:143], v[236:239], v[210:213], v[140:143]\n"
      "s_nop 0\n"
      "v_max_f32_dpp v153, v153, v153 row_ror:4 row_mask:0xf bank_mask:0xf\n"
      "v_exp_f32 v181, v181\n"
      "v_mfma_f32_16x16x32_bf16 v[140:143], v[240:243], v[112:115], v[140:143]\n"
      "s_waitcnt lgkmcnt(1)\n"
      "v_exp_f32 v176, v176\n"
      "v_mfma_f32_16x16x32_bf16 v[116:119], a[228:231], v[170:173], v[116:119]\n"
      "v_exp_f32 v180, v180\n"
      "v_max_f32_dpp v153, v153, v153 row_ror:2 row_mask:0xf bank_mask:0xf\n"
      "v_cvt_pk_bf16_f32 v186, v186, v187\n"
      "v_mfma_f32_16x16x32_bf16 v[116:119], a[232:235], v[218:221], v[116:119]\n"
      "v_cvt_pk_bf16_f32 v187, v188, v189\n"
      "v_max_f32_dpp v153, v153, v153 row_ror:1 row_mask:0xf bank_mask:0xf\n"
      "v_mfma_f32_16x16x32_bf16 v[116:119], a[236:239], v[144:147], v[116:119]\n"
      "s_nop 0\n"
      "v_max_f32_dpp v153, v153, v153 row_bcast:15 row_mask:0xa bank_mask:0xf\n"
      "v_cvt_pk_bf16_f32 v96, v96, v97\n"
      "v_mfma_f32_16x16x32_bf16 v[192:195], a[252:255], v[170:173], v[192:195]\n"
      "v_max_f32_dpp v153, v153, v153 row_bcast:31 row_mask:0xc bank_mask:0xf\n"
      "v_cvt_pk_bf16_f32 v189, v128, v129\n"
      "v_mfma_f32_16x16x32_bf16 v[192:195], v[224:227], v[218:221], v[192:195]\n"
      "v_exp_f32 v204, v204\n"
      "v_mfma_f32_16x16x32_bf16 v[192:195], v[228:231], v[144:147], v[192:195]\n"
      "v_exp_f32 v205, v205\n"
      "v_cvt_pk_bf16_f32 v188, v126, v127\n"
      "v_readlane_b32 s72, v153, 63\n"
      "v_mfma_f32_16x16x32_bf16 v[140:143], v[244:247], v[170:173], v[140:143]\n"
      "v_mul_f32 v153, v117, v117\n"
      "v_cvt_pk_bf16_f32 v89, v204, v205\n"
      "v_subrev_f32 v168, s72, v168\n"
      "v_fma_f32 v152, v116, v116, s52\n"
      "v_mfma_f32_16x16x32_bf16 v[140:143], v[248:251], v[218:221], v[140:143]\n"
      "v_cvt_pk_bf16_f32 v97, v98, v99\n"
      "v_exp_f32_e64 v168, v168\n"
      "v_mfma_f32_16x16x32_bf16 v[140:143], v[252:255], v[144:147], v[140:143]\n"
      "ds_write_b128 v3, v[186:189] offset:256\n"
      "v_pk_fma_f32 v[190:191], v[102:103], v[102:103], v[190:191]\n"
      "v_pk_fma_f32 v[152:153], v[118:119], v[118:119], v[152:153]\n"
      "v_exp_f32 v183, v183\n"
      "v_pk_fma_f32 v[178:179], v[162:163], v[162:163], v[178:179]\n"
      "v_exp_f32 v182, v182\n"
      "v_cvt_pk_bf16_f32 v88, v202, v203\n"
      "v_pk_fma_f32 v[152:153], v[192:193], v[192:193], v[152:153]\n"
      "v_cvt_pk_bf16_f32 v209, v182, v183\n"
      "v_pk_fma_f32 v[190:191], v[164:165], v[164:165], v[190:191]\n"
      "v_pk_fma_f32 v[152:153], v[194:195], v[194:195], v[152:153]\n"
      "v_cvt_pk_bf16_f32 v90, v176, 0\n"
      "v_pk_fma_f32 v[178:179], v[136:137], v[136:137], v[178:179]\n"
      "v_cvt_pk_bf16_f32 v208, v180, v181\n"
      "v_pk_fma_f32 v[152:153], v[140:141], v[140:141], v[152:153]\n"
      "v_pk_fma_f32 v[190:191], v[166:167], v[166:167], v[190:191]\n"
      "v_pk_fma_f32 v[178:179], v[138:139], v[138:139], v[178:179]\n"
      "v_pk_fma_f32 v[152:153], v[142:143], v[142:143], v[152:153]\n"
      "v_add_f32 v190, v190, v191\n"
      "v_mfma_f32_16x16x32_bf16 v[218:221], v[32:35], v[206:209], 0\n"
      "v_add_f32 v178, v178, v179\n"
      "v_cvt_pk_bf16_f32 v168, v168, v168\n"
      "v_mfma_f32_16x16x32_bf16 v[218:221], v[32:35], v[88:91], v[218:221]\n"
      "v_permlane16_swap_b32 v190, v178\n"
      "v_add_f32 v190, v190, v178\n"
      "v_add_f32 v152, v152, v153\n"
      "v_mov_b32 v178, v152\n"
      "v_mfma_f32_16x16x32_bf16 v[210:213], v[196:199], v[206:209], 0\n"
      "ds_write_b16 v13, v168\n"
      "v_mfma_f32_16x16x32_bf16 v[210:213], v[76:79], v[88:91], v[210:213]\n"
      "v_permlane16_swap_b32 v152, v178\n"
      "v_add_f32 v152, v152, v178\n"
      "v_mfma_f32_16x16x32_bf16 v[106:109], v[214:217], v[206:209], 0\n"
      "s_nop 0\n"
      "v_permlane32_swap_b32 v190, v152\n"
      "v_add_f32_e64 v190, v190, v152\n"
      "v_mfma_f32_16x16x32_bf16 v[106:109], v[80:83], v[88:91], v[106:109]\n"
      "ds_write_b32 v31, v190\n"
      "v_mfma_f32_16x16x32_bf16 v[202:205], v[130:133], v[206:209], 0\n"
      "v_rcp_f32_e64 v218, v218\n"
      "v_mfma_f32_16x16x32_bf16 v[202:205], v[84:87], v[88:91], v[202:205]\n"
      "ds_write_b64 v4, v[96:97] offset:256\n"
      "v_mul_f32 v210, v218, v210\n"
      "v_mul_f32_e64 v211, v218, v211\n"
      "v_pk_mul_f32 v[212:213], v[212:213], v[218:219] op_sel_hi:[1,0]\n"
      "v_cvt_pk_bf16_f32 v210, v210, v211\n"
      "v_cvt_pk_bf16_f32 v211, v212, v213\n"
      "v_pk_mul_f32 v[108:109], v[108:109], v[218:219] op_sel_hi:[1,0]\n"
      "v_cvt_pk_bf16_f32 v213, v108, v109\n"
      "v_pk_mul_f32 v[106:107], v[106:107], v[218:219] op_sel_hi:[1,0]\n"
      "v_cvt_pk_bf16_f32 v212, v106, v107\n"
      "s_add_u32 s42, s42, 1\n"
      "ds_write_b128 v3, v[210:213] offset:512\n"
      "ds_read_b128 v[210:213], v14\n"
      "v_pk_mul_f32 v[204:205], v[204:205], v[218:219] op_sel_hi:[1,0]\n"
      "v_pk_mul_f32 v[202:203], v[202:203], v[218:219] op_sel_hi:[1,0]\n"
      "v_cvt_pk_bf16_f32 v202, v202, v203\n"
      "v_cvt_pk_bf16_f32 v203, v204, v205\n"
      "ds_read_b128 v[106:109], v14 offset:64\n"
      "ds_write_b64 v4, v[202:203] offset:512\n"
      "s_waitcnt lgkmcnt(2)\n"
      "v_mfma_f32_16x16x32_bf16 v[218:221], v[210:213], v[32:35], 0\n"
      "s_waitcnt lgkmcnt(1)\n"
      "v_mfma_f32_16x16x32_bf16 v[218:221], v[106:109], v[32:35], v[218:221]\n"
      "s_waitcnt lgkmcnt(0)\n"
      "s_barrier\n"
      "s_cbranch_vccz L_max1_%=\n"
      "L_cold_%=:\n"
      "L_disp1_%=:\n"
      "s_cmp_lt_u32 s95, 0x40000000\n"
      "s_cbranch_scc1 L_nofix_11_%=\n"
      "s_cmp_eq_u32 s95, -2\n"
      "s_cbranch_scc1 L_tail1_%=\n"
      "s_nop 2\n"
      "v_lshlrev_b32 v204, 3, v36\n"
      "s_add_u32 s72, s98, L_cold_%=-L_ipf0_%=\n"
      "s_addc_u32 s73, s99, 0\n"
      "s_mov_b32 s74, L_cend_%=-L_cold_%=\n"
      "s_mov_b32 s75, 0x20000\n"
      "s_mov_b32 m0, 161792\n"
      "s_nop 0\n"
      "buffer_load_dwordx4 v204, s[72:75], 0 offen  lds\n"
      "s_lshr_b32 s92, s84, 30\n"
      "s_or_b32 s92, s92, s66\n"
      "s_cbranch_scc0 L_nf_12_%=\n"
      "s_bitcmp1_b32 s84, 31\n"
      "s_cselect_b32 s75, 100270464, 0\n"
      "s_bitcmp1_b32 s84, 30\n"
      "s_cselect_b32 s76, 196224, 0\n"
      "s_and_b32 s74, s84, 0x3fffffff\n"
      "s_cmp_ge_u32 s84, 0x40000000\n"
      "s_cselect_b32 s74, s74, 0\n"
      "s_cselect_b32 s84, 0, s84\n"
      "s_sub_u32 s74, s74, s66\n"
      "s_add_u32 s66, s66, s74\n"
      "s_sub_u32 s75, s75, s67\n"
      "s_add_u32 s67, s67, s75\n"
      "s_sub_u32 s76, s76, s68\n"
      "s_add_u32 s68, s68, s76\n"
      "v_add_u32 v16, s74, v16\n"
      "v_add_u32 v17, s74, v17\n"
      "v_add_u32 v18, s74, v18\n"
      "v_add_u32 v19, s74, v19\n"
      "v_add_u32 v15, s74, v15\n"
      "s_cmp_eq_u32 s75, 0\n"
      "s_cbranch_scc1 L_ax_13_%=\n"
      "v_bfe_i32 v204, v27, 0, 1\n"
      "v_and_b32 v204, s75, v204\n"
      "v_sub_u32 v16, v16, v204\n"
      "v_bfe_i32 v204, v27, 2, 1\n"
      "v_and_b32 v204, s75, v204\n"
      "v_sub_u32 v17, v17, v204\n"
      "v_bfe_i32 v204, v27, 4, 1\n"
      "v_and_b32 v204, s75, v204\n"
      "v_sub_u32 v18, v18, v204\n"
      "v_bfe_i32 v204, v27, 6, 1\n"
      "v_and_b32 v204, s75, v204\n"
      "v_sub_u32 v19, v19, v204\n"
      "v_bfe_i32 v204, v27, 8, 1\n"
      "v_and_b32 v204, s75, v204\n"
      "v_sub_u32 v15, v15, v204\n"
      "L_ax_13_%=:\n"
      "s_cmp_eq_u32 s76, 0\n"
      "s_cbranch_scc1 L_ax_14_%=\n"
      "v_bfe_i32 v204, v27, 1, 1\n"
      "v_and_b32 v204, s76, v204\n"
      "v_sub_u32 v16, v16, v204\n"
      "v_bfe_i32 v204, v27, 3, 1\n"
      "v_and_b32 v204, s76, v204\n"
      "v_sub_u32 v17, v17, v204\n"
      "v_bfe_i32 v204, v27, 5, 1\n"
      "v_and_b32 v204, s76, v204\n"
      "v_sub_u32 v18, v18, v204\n"
      "v_bfe_i32 v204, v27, 7, 1\n"
      "v_and_b32 v204, s76, v204\n"
      "v_sub_u32 v19, v19, v204\n"
      "v_bfe_i32 v204, v27, 9, 1\n"
      "v_and_b32 v204, s76, v204\n"
      "v_sub_u32 v15, v15, v204\n"
      "L_ax_14_%=:\n"
      "s_or_b32 s48, s66, s69\n"
      "s_cmp_lg_u32 s48, 0\n"
      "s_cselect_b32 s48, 0x40000000, 0\n"
      "L_nf_12_%=:\n"
      "s_lshr_b32 s92, s85, 30\n"
      "s_or_b32 s92, s92, s69\n"
      "s_cbranch_scc0 L_nf_15_%=\n"
      "s_bitcmp1_b32 s85, 31\n"
      "s_cselect_b32 s75, 100270464, 0\n"
      "s_bitcmp1_b32 s85, 30\n"
      "s_cselect_b32 s76, 196224, 0\n"
      "s_and_b32 s74, s85, 0x3fffffff\n"
      "s_cmp_ge_u32 s85, 0x40000000\n"
      "s_cselect_b32 s74, s74, 0\n"
      "s_cselect_b32 s85, 0, s85\n"
      "s_sub_u32 s74, s74, s69\n"
      "s_add_u32 s69, s69, s74\n"
      "s_sub_u32 s75, s75, s70\n"
      "s_add_u32 s70, s70, s75\n"
      "s_sub_u32 s76, s76, s71\n"
      "s_add_u32 s71, s71, s76\n"
      "v_add_u32 v20, s74, v20\n"
      "v_add_u32 v21, s74, v21\n"
      "v_add_u32 v22, s74, v22\n"
      "v_add_u32 v23, s74, v23\n"
      "v_add_u32 v25, s74, v25\n"
      "s_cmp_eq_u32 s75, 0\n"
      "s_cbranch_scc1 L_ax_16_%=\n"
      "v_bfe_i32 v204, v27, 10, 1\n"
      "v_and_b32 v204, s75, v204\n"
      "v_sub_u32 v20, v20, v204\n"
      "v_bfe_i32 v204, v27, 12, 1\n"
      "v_and_b32 v204, s75, v204\n"
      "v_sub_u32 v21, v21, v204\n"
      "v_bfe_i32 v204, v27, 14, 1\n"
      "v_and_b32 v204, s75, v204\n"
      "v_sub_u32 v22, v22, v204\n"
      "v_bfe_i32 v204, v27, 16, 1\n"
      "v_and_b32 v204, s75, v204\n"
      "v_sub_u32 v23, v23, v204\n"
      "v_bfe_i32 v204, v27, 18, 1\n"
      "v_and_b32 v204, s75, v204\n"
      "v_sub_u32 v25, v25, v204\n"
      "L_ax_16_%=:\n"
      "s_cmp_eq_u32 s76, 0\n"
      "s_cbranch_scc1 L_ax_17_%=\n"
      "v_bfe_i32 v204, v27, 11, 1\n"
      "v_and_b32 v204, s76, v204\n"
      "v_sub_u32 v20, v20, v204\n"
      "v_bfe_i32 v204, v27, 13, 1\n"
      "v_and_b32 v204, s76, v204\n"
      "v_sub_u32 v21, v21, v204\n"
      "v_bfe_i32 v204, v27, 15, 1\n"
      "v_and_b32 v204, s76, v204\n"
      "v_sub_u32 v22, v22, v204\n"
      "v_bfe_i32 v204, v27, 17, 1\n"
      "v_and_b32 v204, s76, v204\n"
      "v_sub_u32 v23, v23, v204\n"
      "v_bfe_i32 v204, v27, 19, 1\n"
      "v_and_b32 v204, s76, v204\n"
      "v_sub_u32 v25, v25, v204\n"
      "L_ax_17_%=:\n"
      "s_or_b32 s48, s66, s69\n"
      "s_cmp_lg_u32 s48, 0\n"
      "s_cselect_b32 s48, 0x40000000, 0\n"
      "L_nf_15_%=:\n"
      "s_sub_u32 s92, s42, 1\n"
      "s_cmp_gt_u32 s92, s44\n"
      "s_cbranch_scc1 L_tail1_%=\n"
      "L_nofix_11_%=:\n"
      "s_cmp_lg_u64 s[56:57], 0\n"
      "s_cbranch_scc1 L_slow1_%=\n"
      "L_sel1_%=:\n"
      "s_cmp_lg_u64 s[54:55], 0\n"
      "s_cbranch_scc1 L_max1_%=\n"
      "s_branch L_loop1_%=\n"
      "L_slow1_%=:\n"
      "s_nop 4\n"
      "v_mfma_f32_16x16x32_bf16 v[204:207], v[210:213], v[196:199], 0\n"
      "ds_read_b128 v[188:191], v0 offset:18432\n"
      "v_subrev_u32 v168, 115456, v9\n"
      "v_mfma_f32_16x16x32_bf16 v[204:207], v[106:109], v[76:79], v[204:207]\n"
      "ds_read_b128 v[152:155], v9\n"
      "v_readlane_b32 s45, v37, s43\n"
      "v_mfma_f32_16x16x32_bf16 v[176:179], v[210:213], v[214:217], 0\n"
      "s_mov_b32 m0, 110592\n"
      "s_add_u32 s72, s42, 1\n"
      "v_readlane_b32 s77, v38, s72\n"
      "v_subrev_u32 v196, 115456, v9\n"
      "v_mfma_f32_16x16x32_bf16 v[176:179], v[106:109], v[80:83], v[176:179]\n"
      "s_and_b32 s73, s77, -16\n"
      "ds_read_b128 v[206:209], v0 offset:24576\n"
      "v_subrev_u32 v39, 115456, v9\n"
      "v_mfma_f32_16x16x32_bf16 v[96:99], v[210:213], v[130:133], 0\n"
      "ds_read_b128 v[112:115], v1 offset:18432\n"
      "v_readlane_b32 s46, v37, s42\n"
      "v_mfma_f32_16x16x32_bf16 v[96:99], v[106:109], v[84:87], v[96:99]\n"
      "ds_read_b128 v[108:111], v1 offset:24576\n"
      "s_waitcnt lgkmcnt(4)\n"
      "v_mfma_f32_16x16x32_bf16 v[200:203], a[0:3], v[188:191], 0\n"
      "ds_read_b128 v[144:147], v1 offset:30720\n"
      "v_mov_b32_dpp v218, v218 quad_perm:[0,0,0,0] row_mask:0xf bank_mask:0xf\n"
      "v_mfma_f32_16x16x32_bf16 v[210:213], a[24:27], v[188:191], 0\n"
      "buffer_load_dwordx4 v196, s[28:31], s73 offen  lds\n"
      "v_mfma_f32_16x16x32_bf16 v[104:107], a[48:51], v[188:191], 0\n"
      "s_waitcnt lgkmcnt(4)\n"
      "v_add_f32 v152, v152, v153\n"
      "v_add_f32 v154, v154, v155\n"
      "v_mfma_f32_16x16x32_bf16 v[180:183], a[72:75], v[188:191], 0\n"
      "ds_read_b128 v[170:173], v0 offset:30720\n"
      "v_add_f32 v152, v152, v154\n"
      "v_mfma_f32_16x16x32_bf16 v[184:187], a[96:99], v[188:191], 0\n"
      "s_mov_b32 m0, 111616\n"
      "v_rcp_f32 v218, v218\n"
      "v_mfma_f32_16x16x32_bf16 v[120:123], a[120:123], v[188:191], 0\n"
      "s_waitcnt lgkmcnt(2)\n"
      "v_rsq_f32 v152, v152\n"
      "v_mfma_f32_16x16x32_bf16 v[200:203], a[4:7], v[112:115], v[200:203]\n"
      "s_add_u32 s73, s73, 1024\n"
      "v_mul_f32 v152, 0x415db3d7, v152\n"
      "v_mov_b32_e64 v154, v152\n"
      "v_mfma_f32_16x16x32_bf16 v[200:203], a[8:11], v[206:209], v[200:203]\n"
      "buffer_load_dwordx4 v196, s[28:31], s73 offen  lds\n"
      "v_mfma_f32_16x16x32_bf16 v[210:213], a[28:31], v[112:115], v[210:213]\n"
      "s_add_u32 s73, s73, 1024\n"
      "v_permlane32_swap_b32_e64 v152, v154\n"
      "v_mfma_f32_16x16x32_bf16 v[210:213], a[32:35], v[206:209], v[210:213]\n"
      "s_mov_b32 m0, 112640\n"
      "v_mul_f32 v194, v154, v194\n"
      "v_mov_b32 v153, v152\n"
      "v_mfma_f32_16x16x32_bf16 v[104:107], a[52:55], v[112:115], v[104:107]\n"
      "s_nop 0\n"
      "v_permlane16_swap_b32 v152, v153\n"
      "v_mfma_f32_16x16x32_bf16 v[104:107], a[56:59], v[206:209], v[104:107]\n"
      "v_mul_f32 v149, v152, v149\n"
      "v_mul_f32 v148, v152, v148\n"
      "v_mfma_f32_16x16x32_bf16 v[180:183], a[76:79], v[112:115], v[180:183]\n"
      "buffer_load_dwordx4 v196, s[28:31], s73 offen  lds\n"
      "v_mfma_f32_16x16x32_bf16 v[180:183], a[80:83], v[206:209], v[180:183]\n"
      "v_mul_f32 v150, v152, v150\n"
      "v_mul_f32 v100, v152, v100\n"
      "v_mfma_f32_16x16x32_bf16 v[184:187], a[100:103], v[112:115], v[184:187]\n"
      "s_mov_b32 m0, 113664\n"
      "v_cvt_pk_bf16_f32 v148, v148, v149\n"
      "v_mul_f32 v102, v152, v102\n"
      "v_mfma_f32_16x16x32_bf16 v[184:187], a[104:107], v[206:209], v[184:187]\n"
      "s_add_u32 s73, s73, 1024\n"
      "v_mul_f32 v151, v152, v151\n"
      "v_mul_f32_e64 v101, v152, v101\n"
      "v_mfma_f32_16x16x32_bf16 v[120:123], a[124:127], v[112:115], v[120:123]\n"
      "ds_read_b128 v[126:129], v1 offset:20480\n"
      "v_cvt_pk_bf16_f32 v149, v150, v151\n"
      "v_mfma_f32_16x16x32_bf16 v[120:123], a[128:131], v[206:209], v[120:123]\n"
      "buffer_load_dwordx4 v196, s[28:31], s73 offen  lds\n"
      "v_mfma_f32_16x16x32_bf16 v[200:203], a[12:15], v[108:111], v[200:203]\n"
      "s_waitcnt lgkmcnt(1)\n"
      "v_mul_f32 v138, v153, v138\n"
      "v_mul_f32 v118, v154, v118\n"
      "v_mfma_f32_16x16x32_bf16 v[200:203], a[16:19], v[170:173], v[200:203]\n"
      "ds_read_b128 v[220:223], v0 offset:20480\n"
      "v_mul_f32 v103, v152, v103\n"
      "v_mfma_f32_16x16x32_bf16 v[200:203], a[20:23], v[144:147], v[200:203]\n"
      "s_add_u32 s73, s73, 1024\n"
      "v_mul_f32_e64 v160, v153, v160\n"
      "v_cvt_pk_bf16_f32 v150, v100, v101\n"
      "v_mfma_f32_16x16x32_bf16 v[210:213], a[36:39], v[108:111], v[210:213]\n"
      "v_mul_f32_e64 v161, v153, v161\n"
      "v_mul_f32_e64 v158, v153, v158\n"
      "v_mfma_f32_16x16x32_bf16 v[210:213], a[40:43], v[170:173], v[210:213]\n"
      "v_cvt_pk_bf16_f32 v151, v102, v103\n"
      "s_mov_b32 m0, 114688\n"
      "v_mfma_f32_16x16x32_bf16 v[210:213], a[44:47], v[144:147], v[210:213]\n"
      "s_mov_b32 exec_hi, 0xffff\n"
      "buffer_load_dwordx4 v196, s[28:31], s73 offen  lds\n"
      "s_mov_b32 exec_hi, -1\n"
      "v_pk_mul_f32 v[164:165], v[164:165], v[152:153] op_sel_hi:[1,0]\n"
      "v_pk_mul_f32 v[166:167], v[166:167], v[152:153] op_sel_hi:[1,0]\n"
      "v_pk_mul_f32 v[136:137], v[136:137], v[152:153] op_sel:[0,1]\n"
      "v_pk_mul_f32 v[156:157], v[156:157], v[152:153] op_sel:[0,1]\n"
      "v_pk_mul_f32 v[162:163], v[162:163], v[152:153] op_sel:[0,1]\n"
      "v_mfma_f32_16x16x32_bf16 v[104:107], a[60:63], v[108:111], v[104:107]\n"
      "ds_write_b128 v5, v[148:151] offset:18432\n"
      "v_mfma_f32_16x16x32_bf16 v[104:107], a[64:67], v[170:173], v[104:107]\n"
      "ds_read_b128 v[148:151], v0 offset:26624\n"
      "v_cvt_pk_bf16_f32 v164, v164, v165\n"
      "v_mfma_f32_16x16x32_bf16 v[104:107], a[68:71], v[144:147], v[104:107]\n"
      "s_add_u32 m0, s64, 0\n"
      "v_cvt_pk_bf16_f32 v156, v156, v157\n"
      "v_cvt_pk_bf16_f32 v165, v166, v167\n"
      "v_mfma_f32_16x16x32_bf16 v[180:183], a[84:87], v[108:111], v[180:183]\n"
      "ds_write_b64 v6, v[164:165] offset:18432\n"
      "v_mfma_f32_16x16x32_bf16 v[180:183], a[88:91], v[170:173], v[180:183]\n"
      "buffer_load_dwordx4 v16, s[16:19], s84 offen  lds\n"
      "v_mfma_f32_16x16x32_bf16 v[180:183], a[92:95], v[144:147], v[180:183]\n"
      "s_add_u32 s73, s73, 1024\n"
      "v_cvt_pk_bf16_f32 v136, v136, v137\n"
      "v_mul_f32_e64 v159, v153, v159\n"
      "v_mfma_f32_16x16x32_bf16 v[184:187], a[108:111], v[108:111], v[184:187]\n"
      "s_add_u32 m0, s64, 4096\n"
      "v_cvt_pk_bf16_f32 v157, v158, v159\n"
      "v_mul_f32 v142, v154, v142\n"
      "v_mfma_f32_16x16x32_bf16 v[184:187], a[112:115], v[170:173], v[184:187]\n"
      "v_mul_f32 v140, v154, v140\n"
      "v_cvt_pk_bf16_f32 v159, v162, v163\n"
      "v_mfma_f32_16x16x32_bf16 v[184:187], a[116:119], v[144:147], v[184:187]\n"
      "v_mul_f32 v141, v154, v141\n"
      "v_mul_f32 v116, v154, v116\n"
      "v_mfma_f32_16x16x32_bf16 v[120:123], a[132:135], v[108:111], v[120:123]\n"
      "buffer_load_dwordx4 v17, s[16:19], s84 offen  lds\n"
      "v_mfma_f32_16x16x32_bf16 v[120:123], a[136:139], v[170:173], v[120:123]\n"
      "v_bfe_u32 v78, v168, 4, 4\n"
      "v_mul_f32_e64 v139, v153, v139\n"
      "v_mfma_f32_16x16x32_bf16 v[120:123], a[140:143], v[144:147], v[120:123]\n"
      "s_add_u32 m0, s64, 8192\n"
      "v_cvt_pk_bf16_f32 v158, v160, v161\n"
      "v_fma_f32 v205, v180, v180, s51\n"
      "v_mfma_f32_16x16x32_bf16 v[196:199], v[188:191], a[144:147], 0\n"
      "ds_write_b128 v5, v[156:159] offset:24576\n"
      "v_mfma_f32_16x16x32_bf16 v[196:199], v[112:115], a[148:151], v[196:199]\n"
      "buffer_load_dwordx4 v18, s[16:19], s84 offen  lds\n"
      "v_mfma_f32_16x16x32_bf16 v[196:199], v[206:209], a[152:155], v[196:199]\n"
      "v_mul_f32_e64 v192, v154, v192\n"
      "v_cvt_pk_bf16_f32 v140, v140, v141\n"
      "v_mfma_f32_16x16x32_bf16 v[196:199], v[108:111], a[156:159], v[196:199]\n"
      "s_add_u32 m0, s64, 12288\n"
      "v_cvt_pk_bf16_f32 v137, v138, v139\n"
      "v_mul_f32 v195, v154, v195\n"
      "v_mfma_f32_16x16x32_bf16 v[196:199], v[170:173], a[160:163], v[196:199]\n"
      "v_mul_f32 v117, v154, v117\n"
      "v_fmac_f32_e64 v205, v181, v181\n"
      "v_mfma_f32_16x16x32_bf16 v[196:199], v[144:147], a[164:167], v[196:199]\n"
      "ds_write_b64 v6, v[136:137] offset:24576\n"
      "v_mfma_f32_16x16x32_bf16 v[214:217], v[188:191], a[168:171], 0\n"
      "buffer_load_dwordx4 v19, s[16:19], s84 offen  lds\n"
      "v_mfma_f32_16x16x32_bf16 v[214:217], v[112:115], a[172:175], v[214:217]\n"
      "v_cvt_pk_bf16_f32 v116, v116, v117\n"
      "v_lshrrev_b32_e64 v168, 8, v168\n"
      "v_mfma_f32_16x16x32_bf16 v[214:217], v[206:209], a[176:179], v[214:217]\n"
      "ds_read_b128 v[100:103], v1 offset:26624\n"
      "v_fma_f32 v169, v200, v200, s51\n"
      "v_mfma_f32_16x16x32_bf16 v[214:217], v[108:111], a[180:183], v[214:217]\n"
      "s_add_u32 m0, s65, 16384\n"
      "v_mul_f32 v119, v154, v119\n"
      "v_cvt_pk_bf16_f32 v117, v118, v119\n"
      "v_mfma_f32_16x16x32_bf16 v[214:217], v[170:173], a[184:187], v[214:217]\n"
      "ds_read_b128 v[164:167], v0 offset:32768\n"
      "v_fmac_f32 v205, v182, v182\n"
      "v_mfma_f32_16x16x32_bf16 v[214:217], v[144:147], a[188:191], v[214:217]\n"
      "buffer_load_dwordx4 v15, s[16:19], s84 offen  lds\n"
      "v_mfma_f32_16x16x32_bf16 v[130:133], v[188:191], a[192:195], 0\n"
      "v_cndmask_b32 v204, v204, v176, s[58:59]\n"
      "v_cvt_pk_bf16_f32 v119, v194, v195\n"
      "v_mfma_f32_16x16x32_bf16 v[130:133], v[112:115], a[196:199], v[130:133]\n"
      "s_waitcnt lgkmcnt(2)\n"
      "v_cndmask_b32 v204, v204, v96, s[60:61]\n"
      "v_mul_f32 v193, v154, v193\n"
      "v_mfma_f32_16x16x32_bf16 v[130:133], v[206:209], a[200:203], v[130:133]\n"
      "ds_read_b128 v[156:159], v1 offset:32768\n"
      "v_mul_f32 v143, v154, v143\n"
      "v_mfma_f32_16x16x32_bf16 v[130:133], v[108:111], a[204:207], v[130:133]\n"
      "s_and_b32 s77, s77, 15\n"
      "v_cvt_pk_bf16_f32 v118, v192, v193\n"
      "v_lshrrev_b32_e64 v39, 3, v39\n"
      "v_mfma_f32_16x16x32_bf16 v[130:133], v[170:173], a[208:211], v[130:133]\n"
      "buffer_load_dwordx4 v[192:195], v36, s[28:31], s86 offen\n"
      "v_mfma_f32_16x16x32_bf16 v[130:133], v[144:147], a[212:215], v[130:133]\n"
      "ds_write_b128 v5, v[116:119] offset:30720\n"
      "v_mfma_f32_16x16x32_bf16 v[116:119], a[0:3], v[220:223], 0\n"
      "s_or_b32 s95, s45, s46\n"
      "v_fmac_f32 v169, v201, v201\n"
      "v_fmac_f32 v205, v183, v183\n"
      "v_mfma_f32_16x16x32_bf16 v[116:119], a[4:7], v[126:129], v[116:119]\n"
      "s_waitcnt lgkmcnt(1)\n"
      "v_fmac_f32 v205, v184, v184\n"
      "v_fmac_f32 v169, v202, v202\n"
      "v_mfma_f32_16x16x32_bf16 v[116:119], a[8:11], v[148:151], v[116:119]\n"
      "buffer_load_dword v82, v29, s[28:31], s86 offen\n"
      "v_mfma_f32_16x16x32_bf16 v[116:119], a[12:15], v[100:103], v[116:119]\n"
      "v_fmac_f32_e64 v169, v203, v203\n"
      "v_cvt_pk_bf16_f32 v196, v196, v197\n"
      "v_mfma_f32_16x16x32_bf16 v[116:119], a[16:19], v[164:167], v[116:119]\n"
      "v_min_u32 v39, 96, v39\n"
      "v_fmac_f32 v205, v185, v185\n"
      "v_mfma_f32_16x16x32_bf16 v[116:119], a[20:23], v[156:159], v[116:119]\n"
      "v_fmac_f32 v169, v210, v210\n"
      "v_cvt_pk_bf16_f32 v141, v142, v143\n"
      "v_mfma_f32_16x16x32_bf16 v[160:163], a[24:27], v[220:223], 0\n"
      "v_fmac_f32_e64 v205, v186, v186\n"
      "v_mfma_f32_16x16x32_bf16 v[160:163], a[28:31], v[126:129], v[160:163]\n"
      "ds_write_b64 v6, v[140:141] offset:30720\n"
      "v_mfma_f32_16x16x32_bf16 v[160:163], a[32:35], v[148:151], v[160:163]\n"
      "s_or_b32 s95, s95, s48\n"
      "v_fmac_f32 v205, v187, v187\n"
      "v_fmac_f32_e64 v169, v211, v211\n"
      "v_mfma_f32_16x16x32_bf16 v[160:163], a[36:39], v[100:103], v[160:163]\n"
      "v_fma_f32 v77, v116, v116, s51\n"
      "v_mfma_f32_16x16x32_bf16 v[160:163], a[40:43], v[164:167], v[160:163]\n"
      "v_fmac_f32 v205, v120, v120\n"
      "v_fmac_f32 v169, v212, v212\n"
      "v_mfma_f32_16x16x32_bf16 v[160:163], a[44:47], v[156:159], v[160:163]\n"
      "ds_read_b128 v[136:139], v9 offset:4608\n"
      "v_cvt_pk_bf16_f32 v214, v214, v215\n"
      "v_mfma_f32_16x16x32_bf16 v[140:143], a[48:51], v[220:223], 0\n"
      "v_fmac_f32 v169, v213, v213\n"
      "v_fmac_f32 v169, v104, v104\n"
      "v_fmac_f32 v205, v121, v121\n"
      "v_mfma_f32_16x16x32_bf16 v[140:143], a[52:55], v[126:129], v[140:143]\n"
      "v_fmac_f32 v169, v105, v105\n"
      "v_fmac_f32_e64 v205, v122, v122\n"
      "v_mfma_f32_16x16x32_bf16 v[140:143], a[56:59], v[148:151], v[140:143]\n"
      "v_mul_u32_u24 v89, 98, v78\n"
      "v_fmac_f32 v169, v106, v106\n"
      "v_mfma_f32_16x16x32_bf16 v[140:143], a[60:63], v[100:103], v[140:143]\n"
      "v_fmac_f32 v169, v107, v107\n"
      "v_fmac_f32 v205, v123, v123\n"
      "v_mfma_f32_16x16x32_bf16 v[140:143], a[64:67], v[164:167], v[140:143]\n"
      "v_fmac_f32 v77, v117, v117\n"
      "v_cvt_pk_bf16_f32 v215, v216, v217\n"
      "v_mfma_f32_16x16x32_bf16 v[140:143], a[68:71], v[156:159], v[140:143]\n"
      "s_waitcnt lgkmcnt(0)\n"
      "v_permlane16_swap_b32 v169, v205\n"
      "v_mfma_f32_16x16x32_bf16 v[176:179], a[72:75], v[220:223], 0\n"
      "ds_read_b128 v[96:99], v1 offset:22528\n"
      "v_add_f32_e64 v169, v169, v205\n"
      "v_mfma_f32_16x16x32_bf16 v[176:179], a[76:79], v[126:129], v[176:179]\n"
      "v_or3_b32 v136, v136, v137, v138\n"
      "v_mov_b32 v205, v169\n"
      "v_mfma_f32_16x16x32_bf16 v[176:179], a[80:83], v[148:151], v[176:179]\n"
      "s_cmp_ge_u32 s95, 0x40000000\n"
      "s_cselect_b32 s94, -1, -2\n"
      "v_lshl_add_u32 v89, v168, 3, v89\n"
      "v_mfma_f32_16x16x32_bf16 v[176:179], a[84:87], v[100:103], v[176:179]\n"
      "ds_read_b128 v[108:111], v0 offset:22528\n"
      "v_fmac_f32 v77, v118, v118\n"
      "v_mfma_f32_16x16x32_bf16 v[176:179], a[88:91], v[164:167], v[176:179]\n"
      "v_fmac_f32 v77, v119, v119\n"
      "v_fmac_f32 v77, v160, v160\n"
      "v_mfma_f32_16x16x32_bf16 v[176:179], a[92:95], v[156:159], v[176:179]\n"
      "v_permlane32_swap_b32_e64 v169, v205\n"
      "v_mfma_f32_16x16x32_bf16 v[206:209], a[96:99], v[220:223], 0\n"
      "v_lshlrev_b32 v168, 2, v168\n"
      "v_add_f32 v169, v169, v205\n"
      "v_mfma_f32_16x16x32_bf16 v[206:209], a[100:103], v[126:129], v[206:209]\n"
      "v_rsq_f32_e64 v169, v169\n"
      "v_mfma_f32_16x16x32_bf16 v[206:209], a[104:107], v[148:151], v[206:209]\n"
      "s_nop 0\n"
      "v_fma_f32 v113, v176, v176, s51\n"
      "v_mfma_f32_16x16x32_bf16 v[206:209], a[108:111], v[100:103], v[206:209]\n"
      "v_mov_b32 v205, v169\n"
      "v_fmac_f32 v77, v161, v161\n"
      "v_mfma_f32_16x16x32_bf16 v[206:209], a[112:115], v[164:167], v[206:209]\n"
      "v_permlane16_swap_b32 v169, v205\n"
      "v_mfma_f32_16x16x32_bf16 v[206:209], a[116:119], v[156:159], v[206:209]\n"
      "v_mul_f32 v204, v218, v204\n"
      "v_mul_f32 v169, s50, v169\n"
      "v_mfma_f32_16x16x32_bf16 v[188:191], a[120:123], v[220:223], 0\n"
      "v_cvt_pk_bf16_f32 v197, v198, v199\n"
      "v_fmac_f32 v113, v177, v177\n"
      "v_mfma_f32_16x16x32_bf16 v[188:191], a[124:127], v[126:129], v[188:191]\n"
      "v_add_u32 v89, s77, v89\n"
      "v_add_u32 v39, s77, v39\n"
      "v_mfma_f32_16x16x32_bf16 v[188:191], a[128:131], v[148:151], v[188:191]\n"
      "v_fmac_f32 v77, v162, v162\n"
      "v_mul_f32 v104, v169, v104\n"
      "v_mfma_f32_16x16x32_bf16 v[188:191], a[132:135], v[100:103], v[188:191]\n"
      "v_fmac_f32 v113, v178, v178\n"
      "v_mul_f32 v201, v169, v201\n"
      "v_mfma_f32_16x16x32_bf16 v[188:191], a[136:139], v[164:167], v[188:191]\n"
      "v_cvt_pk_bf16_f32 v204, v204, v204\n"
      "v_fmac_f32 v113, v179, v179\n"
      "v_mfma_f32_16x16x32_bf16 v[188:191], a[140:143], v[156:159], v[188:191]\n"
      "v_mul_f32 v107, v169, v107\n"
      "v_mul_f32 v181, v205, v181\n"
      "v_mfma_f32_16x16x32_bf16 v[170:173], v[220:223], a[144:147], 0\n"
      "v_bitop3_b32 v136, v136, v139, s33 bitop3:0xa8\n"
      "v_mul_f32_e64 v202, v169, v202\n"
      "v_mfma_f32_16x16x32_bf16 v[170:173], v[126:129], a[148:151], v[170:173]\n"
      "v_mul_f32 v105, v169, v105\n"
      "v_fmac_f32 v77, v163, v163\n"
      "v_mfma_f32_16x16x32_bf16 v[170:173], v[148:151], a[152:155], v[170:173]\n"
      "v_fmac_f32 v113, v206, v206\n"
      "v_mul_f32 v213, v169, v213\n"
      "v_mfma_f32_16x16x32_bf16 v[170:173], v[100:103], a[156:159], v[170:173]\n"
      "v_sub_u32 v78, v78, v168\n"
      "v_fmac_f32 v113, v207, v207\n"
      "v_mfma_f32_16x16x32_bf16 v[170:173], v[164:167], a[160:163], v[170:173]\n"
      "v_fmac_f32 v77, v140, v140\n"
      "v_fmac_f32 v113, v208, v208\n"
      "v_mfma_f32_16x16x32_bf16 v[170:173], v[156:159], a[164:167], v[170:173]\n"
      "v_cvt_pk_bf16_f32 v104, v104, v105\n"
      "v_fmac_f32 v77, v141, v141\n"
      "v_fmac_f32 v113, v209, v209\n"
      "v_mfma_f32_16x16x32_bf16 v[144:147], v[220:223], a[168:171], 0\n"
      "ds_write_b16 v12, v204\n"
      "v_mfma_f32_16x16x32_bf16 v[144:147], v[126:129], a[172:175], v[144:147]\n"
      "v_lshlrev_b32 v78, 4, v78\n"
      "v_mov_b32 v168, 0x3fb9\n"
      "v_mfma_f32_16x16x32_bf16 v[144:147], v[148:151], a[176:179], v[144:147]\n"
      "v_fmac_f32 v77, v142, v142\n"
      "v_cvt_pk_bf16_f32 v130, v130, v131\n"
      "v_mfma_f32_16x16x32_bf16 v[144:147], v[100:103], a[180:183], v[144:147]\n"
      "s_waitcnt vmcnt(7)\n"
      "v_fmac_f32 v113, v188, v188\n"
      "v_mul_f32 v106, v169, v106\n"
      "v_mfma_f32_16x16x32_bf16 v[144:147], v[164:167], a[184:187], v[144:147]\n"
      "v_mul_f32 v187, v205, v187\n"
      "v_cmp_ne_u32 s[56:57], 0, v136\n"
      "v_mfma_f32_16x16x32_bf16 v[144:147], v[156:159], a[188:191], v[144:147]\n"
      "v_cvt_pk_bf16_f32 v131, v132, v133\n"
      "v_mul_f32 v121, v205, v121\n"
      "v_mfma_f32_16x16x32_bf16 v[136:139], v[220:223], a[192:195], 0\n"
      "ds_read_b128 v[218:221], v0 offset:28672\n"
      "v_fmac_f32 v113, v189, v189\n"
      "v_mfma_f32_16x16x32_bf16 v[136:139], v[126:129], a[196:199], v[136:139]\n"
      "ds_read_b128 v[126:129], v1 offset:28672\n"
      "v_cvt_pk_bf16_f32 v105, v106, v107\n"
      "v_mfma_f32_16x16x32_bf16 v[136:139], v[148:151], a[200:203], v[136:139]\n"
      "ds_read_b128 v[148:151], v1 offset:34816\n"
      "v_fmac_f32 v77, v143, v143\n"
      "v_mfma_f32_16x16x32_bf16 v[136:139], v[100:103], a[204:207], v[136:139]\n"
      "ds_read_b128 v[100:103], v0 offset:34816\n"
      "v_fmac_f32 v113, v190, v190\n"
      "v_mfma_f32_16x16x32_bf16 v[136:139], v[164:167], a[208:211], v[136:139]\n"
      "v_fmac_f32 v113, v191, v191\n"
      "v_cvt_pk_bf16_f32 v199, v172, v173\n"
      "v_mfma_f32_16x16x32_bf16 v[136:139], v[156:159], a[212:215], v[136:139]\n"
      "s_waitcnt lgkmcnt(4)\n"
      "v_permlane16_swap_b32 v77, v113\n"
      "v_mfma_f32_16x16x32_bf16 v[152:155], a[0:3], v[108:111], 0\n"
      "v_add_f32 v77, v77, v113\n"
      "v_mov_b32 v113, v77\n"
      "v_mfma_f32_16x16x32_bf16 v[152:155], a[4:7], v[96:99], v[152:155]\n"
      "s_waitcnt lgkmcnt(2)\n"
      "v_cvt_pk_bf16_f32 v217, v146, v147\n"
      "v_mul_f32 v184, v205, v184\n"
      "v_cmp_gt_u32 s[72:73], 64, v78\n"
      "v_mfma_f32_16x16x32_bf16 v[152:155], a[8:11], v[218:221], v[152:155]\n"
      "v_mul_f32 v185, v205, v185\n"
      "v_readlane_b32 s47, v38, s43\n"
      "v_mfma_f32_16x16x32_bf16 v[152:155], a[12:15], v[126:129], v[152:155]\n"
      "s_waitcnt lgkmcnt(0)\n"
      "v_permlane32_swap_b32 v77, v113\n"
      "v_add_f32_e64 v77, v77, v113\n"
      "v_mfma_f32_16x16x32_bf16 v[152:155], a[16:19], v[100:103], v[152:155]\n"
      "v_mul_f32 v200, v169, v200\n"
      "v_mul_f32 v203, v169, v203\n"
      "v_mfma_f32_16x16x32_bf16 v[152:155], a[20:23], v[148:151], v[152:155]\n"
      "v_rsq_f32_e64 v77, v77\n"
      "v_mfma_f32_16x16x32_bf16 v[156:159], a[24:27], v[108:111], 0\n"
      "v_mov_b32 v113, v77\n"
      "v_mul_f32 v212, v169, v212\n"
      "v_mfma_f32_16x16x32_bf16 v[156:159], a[28:31], v[96:99], v[156:159]\n"
      "v_permlane16_swap_b32_e64 v77, v113\n"
      "v_mfma_f32_16x16x32_bf16 v[156:159], a[32:35], v[218:221], v[156:159]\n"
      "v_and_b32 v92, 2, v89\n"
      "v_add_u32 v39, 115296, v39\n"
      "v_mfma_f32_16x16x32_bf16 v[156:159], a[36:39], v[126:129], v[156:159]\n"
      "v_mul_f32 v120, v205, v120\n"
      "v_cvt_pk_bf16_f32 v216, v144, v145\n"
      "v_cvt_pk_bf16_f32 v200, v200, v201\n"
      "v_cvt_pk_bf16_f32 v201, v202, v203\n"
      "v_fma_f32 v223, v152, v152, s51\n"
      "v_pk_mul_f32 v[210:211], v[210:211], v[168:169] op_sel:[0,1]\n"
      "v_mul_f32 v77, s50, v77\n"
      "v_pk_mul_f32 v[122:123], v[122:123], v[204:205] op_sel:[0,1]\n"
      "v_pk_mul_f32 v[188:189], v[188:189], v[112:113] op_sel:[0,1]\n"
      "v_mul_f32 v207, v113, v207\n"
      "v_pk_mul_f32 v[176:177], v[176:177], v[112:113] op_sel:[0,1]\n"
      "v_pk_mul_f32 v[178:179], v[178:179], v[112:113] op_sel:[0,1]\n"
      "v_pk_mul_f32 v[140:141], v[140:141], v[76:77] op_sel:[0,1]\n"
      "v_pk_mul_f32 v[182:183], v[182:183], v[204:205] op_sel:[0,1]\n"
      "v_pk_mul_f32 v[116:117], v[116:117], v[76:77] op_sel:[0,1]\n"
      "v_pk_mul_f32 v[118:119], v[118:119], v[76:77] op_sel:[0,1]\n"
      "v_mul_f32_e64 v206, v113, v206\n"
      "v_pk_mul_f32 v[190:191], v[190:191], v[112:113] op_sel:[0,1]\n"
      "v_mfma_f32_16x16x32_bf16 v[156:159], a[40:43], v[100:103], v[156:159]\n"
      "v_mov_b32 v169, 0\n"
      "v_mul_f32 v142, v77, v142\n"
      "v_mfma_f32_16x16x32_bf16 v[156:159], a[44:47], v[148:151], v[156:159]\n"
      "v_cvt_pk_bf16_f32 v133, v138, v139\n"
      "v_mul_f32 v186, v205, v186\n"
      "v_mfma_f32_16x16x32_bf16 v[144:147], a[48:51], v[108:111], 0\n"
      "v_fmac_f32 v223, v153, v153\n"
      "v_mul_f32 v160, v77, v160\n"
      "v_mfma_f32_16x16x32_bf16 v[144:147], a[52:55], v[96:99], v[144:147]\n"
      "v_fmac_f32 v223, v154, v154\n"
      "v_fmac_f32 v223, v155, v155\n"
      "v_mfma_f32_16x16x32_bf16 v[144:147], a[56:59], v[218:221], v[144:147]\n"
      "v_mul_f32 v143, v77, v143\n"
      "v_mul_f32 v180, v205, v180\n"
      "v_mfma_f32_16x16x32_bf16 v[144:147], a[60:63], v[126:129], v[144:147]\n"
      "s_cmp_gt_u32 s42, s44\n"
      "s_cselect_b32 s95, s94, s95\n"
      "v_cvt_pk_bf16_f32 v180, v180, v181\n"
      "v_mfma_f32_16x16x32_bf16 v[144:147], a[64:67], v[100:103], v[144:147]\n"
      "v_mul_f32 v209, v113, v209\n"
      "v_mul_f32_e64 v208, v113, v208\n"
      "v_mfma_f32_16x16x32_bf16 v[144:147], a[68:71], v[148:151], v[144:147]\n"
      "v_cvt_pk_bf16_f32 v181, v182, v183\n"
      "v_fmac_f32_e64 v223, v156, v156\n"
      "v_mfma_f32_16x16x32_bf16 v[112:115], a[72:75], v[108:111], 0\n"
      "v_cvt_pk_bf16_f32 v188, v188, v189\n"
      "v_cvt_pk_bf16_f32 v132, v136, v137\n"
      "v_mfma_f32_16x16x32_bf16 v[112:115], a[76:79], v[96:99], v[112:115]\n"
      "v_lshlrev_b64 v[168:169], v78, v[168:169]\n"
      "v_cvt_pk_bf16_f32 v182, v184, v185\n"
      "v_mfma_f32_16x16x32_bf16 v[112:115], a[80:83], v[218:221], v[112:115]\n"
      "v_cvt_pk_bf16_f32 v198, v170, v171\n"
      "v_mul_f32_e64 v163, v77, v163\n"
      "v_mfma_f32_16x16x32_bf16 v[112:115], a[84:87], v[126:129], v[112:115]\n"
      "s_cmp_ge_u32 s95, 0x40000000\n"
      "s_cselect_b64 vcc, -1, s[56:57]\n"
      "v_cvt_pk_bf16_f32 v176, v176, v177\n"
      "v_cvt_pk_bf16_f32 v189, v190, v191\n"
      "v_mfma_f32_16x16x32_bf16 v[112:115], a[88:91], v[100:103], v[112:115]\n"
      "v_fmac_f32 v223, v157, v157\n"
      "v_fmac_f32 v223, v158, v158\n"
      "v_mfma_f32_16x16x32_bf16 v[112:115], a[92:95], v[148:151], v[112:115]\n"
      "v_cvt_pk_bf16_f32 v177, v178, v179\n"
      "v_cvt_pk_bf16_f32 v202, v210, v211\n"
      "v_and_b32 v89, -4, v89\n"
      "v_mfma_f32_16x16x32_bf16 v[136:139], a[96:99], v[108:111], 0\n"
      "v_cndmask_b32_e64 v168, 0, v168, s[72:73]\n"
      "v_fmac_f32 v223, v159, v159\n"
      "v_mfma_f32_16x16x32_bf16 v[136:139], a[100:103], v[96:99], v[136:139]\n"
      "v_cvt_pk_bf16_f32 v120, v120, v121\n"
      "v_cvt_pk_bf16_f32 v116, v116, v117\n"
      "v_mfma_f32_16x16x32_bf16 v[136:139], a[104:107], v[218:221], v[136:139]\n"
      "ds_read_u16 v39, v39\n"
      "v_fma_f32 v222, v112, v112, s51\n"
      "v_mfma_f32_16x16x32_bf16 v[136:139], a[108:111], v[126:129], v[136:139]\n"
      "v_cvt_pk_bf16_f32 v140, v140, v141\n"
      "v_fmac_f32 v222, v113, v113\n"
      "v_mfma_f32_16x16x32_bf16 v[136:139], a[112:115], v[100:103], v[136:139]\n"
      "v_fmac_f32 v223, v144, v144\n"
      "v_cvt_pk_bf16_f32 v203, v212, v213\n"
      "v_cvt_pk_bf16_f32 v121, v122, v123\n"
      "v_mfma_f32_16x16x32_bf16 v[136:139], a[116:119], v[148:151], v[136:139]\n"
      "v_mul_f32 v161, v77, v161\n"
      "v_fmac_f32 v222, v114, v114\n"
      "v_mfma_f32_16x16x32_bf16 v[210:213], a[120:123], v[108:111], 0\n"
      "v_mul_f32 v162, v77, v162\n"
      "v_cvt_pk_bf16_f32 v141, v142, v143\n"
      "v_mfma_f32_16x16x32_bf16 v[210:213], a[124:127], v[96:99], v[210:213]\n"
      "ds_read_b128 v[164:167], v7\n"
      "v_fmac_f32 v223, v145, v145\n"
      "v_mfma_f32_16x16x32_bf16 v[210:213], a[128:131], v[218:221], v[210:213]\n"
      "v_fmac_f32 v222, v115, v115\n"
      "v_cvt_pk_bf16_f32 v117, v118, v119\n"
      "v_mfma_f32_16x16x32_bf16 v[210:213], a[132:135], v[126:129], v[210:213]\n"
      "v_cvt_pk_bf16_f32 v118, v160, v161\n"
      "v_fmac_f32 v223, v146, v146\n"
      "v_mfma_f32_16x16x32_bf16 v[210:213], a[136:139], v[100:103], v[210:213]\n"
      "v_cvt_pk_bf16_f32 v183, v186, v187\n"
      "v_fmac_f32 v222, v136, v136\n"
      "v_mfma_f32_16x16x32_bf16 v[210:213], a[140:143], v[148:151], v[210:213]\n"
      "v_fmac_f32 v222, v137, v137\n"
      "v_fmac_f32 v222, v138, v138\n"
      "v_mfma_f32_16x16x32_bf16 v[184:187], v[108:111], a[144:147], 0\n"
      "v_fmac_f32 v222, v139, v139\n"
      "v_cvt_pk_bf16_f32 v178, v206, v207\n"
      "v_mfma_f32_16x16x32_bf16 v[184:187], v[96:99], a[148:151], v[184:187]\n"
      "v_fmac_f32 v223, v147, v147\n"
      "v_cvt_pk_bf16_f32 v179, v208, v209\n"
      "v_mfma_f32_16x16x32_bf16 v[184:187], v[218:221], a[152:155], v[184:187]\n"
      "v_fmac_f32 v222, v210, v210\n"
      "v_fmac_f32_e64 v222, v211, v211\n"
      "v_mfma_f32_16x16x32_bf16 v[184:187], v[126:129], a[156:159], v[184:187]\n"
      "v_fmac_f32 v222, v212, v212\n"
      "v_fmac_f32 v222, v213, v213\n"
      "v_mfma_f32_16x16x32_bf16 v[184:187], v[100:103], a[160:163], v[184:187]\n"
      "s_nop 0\n"
      "v_permlane16_swap_b32 v223, v222\n"
      "v_mfma_f32_16x16x32_bf16 v[184:187], v[148:151], a[164:167], v[184:187]\n"
      "v_add_f32 v223, v223, v222\n"
      "v_mov_b32 v222, v223\n"
      "v_mfma_f32_16x16x32_bf16 v[204:207], v[108:111], a[168:171], 0\n"
      "s_nop 0\n"
      "v_permlane32_swap_b32 v223, v222\n"
      "v_mfma_f32_16x16x32_bf16 v[204:207], v[96:99], a[172:175], v[204:207]\n"
      "v_add_f32 v223, v223, v222\n"
      "v_cvt_pk_bf16_f32 v119, v162, v163\n"
      "v_mfma_f32_16x16x32_bf16 v[204:207], v[218:221], a[176:179], v[204:207]\n"
      "v_rsq_f32 v223, v223\n"
      "v_mfma_f32_16x16x32_bf16 v[204:207], v[126:129], a[180:183], v[204:207]\n"
      "v_mov_b32 v222, v223\n"
      "v_cvt_pk_bf16_f32 v77, v186, v187\n"
      "v_mfma_f32_16x16x32_bf16 v[204:207], v[100:103], a[184:187], v[204:207]\n"
      "s_waitcnt lgkmcnt(0)\n"
      "v_permlane16_swap_b32_e64 v223, v222\n"
      "v_mfma_f32_16x16x32_bf16 v[204:207], v[148:151], a[188:191], v[204:207]\n"
      "ds_read_b128 v[160:163], v2 offset:3072\n"
      "v_cndmask_b32_e64 v169, 0, v169, s[72:73]\n"
      "v_mfma_f32_16x16x32_bf16 v[170:173], v[108:111], a[192:195], 0\n"
      "ds_read_b128 v[106:109], v7 offset:4096\n"
      "v_mfma_f32_16x16x32_bf16 v[170:173], v[96:99], a[196:199], v[170:173]\n"
      "ds_read_b128 v[96:99], v2 offset:6144\n"
      "v_mul_f32_e64 v223, s50, v223\n"
      "v_mfma_f32_16x16x32_bf16 v[170:173], v[218:221], a[200:203], v[170:173]\n"
      "ds_read_b128 v[218:221], v2 offset:9216\n"
      "v_cvt_pk_bf16_f32 v81, v206, v207\n"
      "v_mfma_f32_16x16x32_bf16 v[170:173], v[126:129], a[204:207], v[170:173]\n"
      "ds_read_b128 v[206:209], v2\n"
      "v_mul_f32_e64 v113, v222, v113\n"
      "v_mfma_f32_16x16x32_bf16 v[170:173], v[100:103], a[208:211], v[170:173]\n"
      "buffer_store_dwordx4 v[164:167], v20, s[24:27], s85 offen sc1\n"
      "v_pk_mul_f32 v[138:139], v[138:139], v[222:223] op_sel_hi:[1,0]\n"
      "v_pk_mul_f32 v[144:145], v[144:145], v[222:223] op_sel:[0,1]\n"
      "v_pk_mul_f32 v[136:137], v[136:137], v[222:223] op_sel_hi:[1,0]\n"
      "v_pk_mul_f32 v[210:211], v[210:211], v[222:223] op_sel_hi:[1,0]\n"
      "v_pk_mul_f32 v[114:115], v[114:115], v[222:223] op_sel_hi:[1,0]\n"
      "v_mul_f32_e64 v112, v222, v112\n"
      "v_pk_mul_f32 v[158:159], v[158:159], v[222:223] op_sel:[0,1]\n"
      "v_pk_mul_f32 v[156:157], v[156:157], v[222:223] op_sel:[0,1]\n"
      "v_pk_mul_f32 v[152:153], v[152:153], v[222:223] op_sel:[0,1]\n"
      "v_pk_mul_f32 v[146:147], v[146:147], v[222:223] op_sel:[0,1]\n"
      "v_pk_mul_f32 v[154:155], v[154:155], v[222:223] op_sel:[0,1]\n"
      "v_pk_mul_f32 v[212:213], v[212:213], v[222:223] op_sel_hi:[1,0]\n"
      "v_mfma_f32_16x16x32_bf16 v[170:173], v[148:151], a[212:215], v[170:173]\n"
      "s_waitcnt lgkmcnt(0)\n"
      "v_cvt_pk_bf16_f32 v76, v184, v185\n"
      "v_add_u32 v142, 110592, v89\n"
      "v_cvt_pk_bf16_f32 v80, v204, v205\n"
      "v_mfma_f32_16x16x32_bf16 v[148:151], a[216:219], v[206:209], 0\n"
      "v_cvt_pk_bf16_f32 v210, v210, v211\n"
      "v_cvt_pk_bf16_f32 v112, v112, v113\n"
      "v_mfma_f32_16x16x32_bf16 v[148:151], a[220:223], v[160:163], v[148:151]\n"
      "ds_read_b128 v[184:187], v2 offset:12288\n"
      "v_mfma_f32_16x16x32_bf16 v[148:151], a[224:227], v[96:99], v[148:151]\n"
      "v_cvt_pk_bf16_f32 v84, v170, v171\n"
      "v_cvt_pk_bf16_f32 v85, v172, v173\n"
      "v_mfma_f32_16x16x32_bf16 v[100:103], a[240:243], v[206:209], 0\n"
      "buffer_store_dwordx4 v[106:109], v21, s[24:27], s85 offen sc1\n"
      "v_mfma_f32_16x16x32_bf16 v[100:103], a[244:247], v[160:163], v[100:103]\n"
      "s_nop 0\n"
      "ds_read_b128 v[106:109], v2 offset:15360\n"
      "v_cvt_pk_bf16_f32 v211, v212, v213\n"
      "v_mfma_f32_16x16x32_bf16 v[100:103], a[248:251], v[96:99], v[100:103]\n"
      "ds_read2_b32 v[128:129], v142 offset0:16 offset1:17\n"
      "v_cmp_gt_u32 s[72:73], 64, v78\n"
      "v_mfma_f32_16x16x32_bf16 v[164:167], v[232:235], v[206:209], 0\n"
      "v_cvt_pk_bf16_f32 v144, v144, v145\n"
      "v_cvt_pk_bf16_f32 v152, v152, v153\n"
      "v_mfma_f32_16x16x32_bf16 v[164:167], v[236:239], v[160:163], v[164:167]\n"
      "ds_read_b128 v[160:163], v7 offset:8192\n"
      "v_add_u32 v110, 112160, v89\n"
      "v_mfma_f32_16x16x32_bf16 v[164:167], v[240:243], v[96:99], v[164:167]\n"
      "ds_read2_b32 v[172:173], v142 offset0:0 offset1:1\n"
      "v_cvt_pk_bf16_f32 v153, v154, v155\n"
      "v_mfma_f32_16x16x32_bf16 v[148:151], a[228:231], v[218:221], v[148:151]\n"
      "s_waitcnt lgkmcnt(4)\n"
      "v_mov_b32_e64 v223, 0\n"
      "v_cvt_pk_bf16_f32 v113, v114, v115\n"
      "v_mfma_f32_16x16x32_bf16 v[148:151], a[232:235], v[184:187], v[148:151]\n"
      "s_waitcnt lgkmcnt(3)\n"
      "v_cvt_pk_bf16_f32 v115, v138, v139\n"
      "v_cvt_pk_bf16_f32 v114, v136, v137\n"
      "v_mfma_f32_16x16x32_bf16 v[148:151], a[236:239], v[106:109], v[148:151]\n"
      "ds_read_b128 v[204:207], v10 offset:1200\n"
      "v_cvt_pk_bf16_f32 v155, v158, v159\n"
      "v_cvt_pk_bf16_f32 v154, v156, v157\n"
      "v_mfma_f32_16x16x32_bf16 v[100:103], a[252:255], v[218:221], v[100:103]\n"
      "ds_read2_b32 v[170:171], v142 offset0:2 offset1:10\n"
      "v_mov_b32 v222, 0xbb2b\n"
      "v_mfma_f32_16x16x32_bf16 v[100:103], v[224:227], v[184:187], v[100:103]\n"
      "ds_read_b64 v[212:213], v10 offset:1192\n"
      "v_cvt_pk_bf16_f32 v145, v146, v147\n"
      "v_mul_f32 v209, v149, v149\n"
      "v_mfma_f32_16x16x32_bf16 v[100:103], v[228:231], v[106:109], v[100:103]\n"
      "s_waitcnt lgkmcnt(3)\n"
      "buffer_store_dwordx4 v[160:163], v22, s[24:27], s85 offen sc1\n"
      "v_mfma_f32_16x16x32_bf16 v[164:167], v[244:247], v[218:221], v[164:167]\n"
      "ds_read_b128 v[156:159], v7 offset:12288\n"
      "v_fma_f32 v208, v148, v148, s52\n"
      "v_mfma_f32_16x16x32_bf16 v[164:167], v[248:251], v[184:187], v[164:167]\n"
      "ds_read2_b32 v[138:139], v142 offset0:24 offset1:18\n"
      "v_fmac_f32 v209, v151, v151\n"
      "v_mfma_f32_16x16x32_bf16 v[164:167], v[252:255], v[106:109], v[164:167]\n"
      "s_waitcnt lgkmcnt(3)\n"
      "v_fmac_f32_e64 v208, v150, v150\n"
      "v_alignbyte_b32 v172, v173, v172, v92\n"
      "v_mfma_f32_16x16x32_bf16 v[106:109], v[180:183], v[200:203], v[40:43]\n"
      "ds_read2_b32 v[122:123], v142 offset0:8 offset1:9\n"
      "v_lshlrev_b64 v[222:223], v78, v[222:223]\n"
      "v_mfma_f32_16x16x32_bf16 v[218:221], v[176:179], v[200:203], v[44:47]\n"
      "ds_read2_b32 v[142:143], v110 offset0:8 offset1:9\n"
      "v_alignbyte_b32 v128, v129, v128, v92\n"
      "v_mfma_f32_16x16x32_bf16 v[96:99], v[112:115], v[200:203], v[48:51]\n"
      "ds_read_b128 v[160:163], v7 offset:16384\n"
      "v_fmac_f32 v209, v101, v101\n"
      "v_mfma_f32_16x16x32_bf16 v[134:137], v[204:207], v[200:203], v[24:27]\n"
      "v_alignbyte_b32 v173, v170, v173, v92\n"
      "v_fmac_f32 v208, v100, v100\n"
      "v_mfma_f32_16x16x16_bf16 v[106:109], v[120:121], v[104:105], v[106:109]\n"
      "s_waitcnt lgkmcnt(3)\n"
      "v_alignbyte_b32 v129, v139, v129, v92\n"
      "v_mfma_f32_16x16x16_bf16 v[218:221], v[188:189], v[104:105], v[218:221]\n"
      "buffer_store_dwordx4 v[156:159], v23, s[24:27], s85 offen sc1\n"
      "v_mfma_f32_16x16x16_bf16 v[96:99], v[210:211], v[104:105], v[96:99]\n"
      "v_alignbyte_b32 v138, v138, v138, v92\n"
      "v_mfma_f32_16x16x16_bf16 v[134:137], v[212:213], v[104:105], v[134:137]\n"
      "s_waitcnt lgkmcnt(0)\n"
      "v_alignbyte_b32 v122, v123, v122, v92\n"
      "v_cndmask_b32_e64 v222, 0, v222, s[72:73]\n"
      "v_alignbyte_b32 v123, v171, v123, v92\n"
      "v_mfma_f32_16x16x16_bf16 v[106:109], v[168:169], v[172:173], v[106:109]\n"
      "ds_read2_b32 v[202:203], v110 offset0:0 offset1:1\n"
      "v_cndmask_b32_e64 v223, 0, v223, s[72:73]\n"
      "v_mfma_f32_16x16x16_bf16 v[218:221], v[168:169], v[122:123], v[218:221]\n"
      "v_fmac_f32_e64 v208, v102, v102\n"
      "v_alignbyte_b32 v142, v143, v142, v92\n"
      "v_fmac_f32_e64 v209, v103, v103\n"
      "v_mfma_f32_16x16x16_bf16 v[96:99], v[168:169], v[128:129], v[96:99]\n"
      "ds_read_b128 v[124:127], v2 offset:256\n"
      "v_fmac_f32_e64 v209, v165, v165\n"
      "v_mfma_f32_16x16x16_bf16 v[134:137], v[168:169], v[138:139], v[134:137]\n"
      "ds_read_b128 v[184:187], v2 offset:6400\n"
      "v_mfma_f32_16x16x16_bf16 v[106:109], v[222:223], v[172:173], v[106:109]\n"
      "ds_read_b128 v[170:173], v2 offset:3328\n"
      "v_fmac_f32 v208, v164, v164\n"
      "v_mfma_f32_16x16x16_bf16 v[218:221], v[222:223], v[122:123], v[218:221]\n"
      "buffer_store_dwordx4 v[160:163], v25, s[24:27], s85 offen sc1\n"
      "v_pk_fma_f32 v[208:209], v[166:167], v[166:167], v[208:209]\n"
      "v_mfma_f32_16x16x16_bf16 v[96:99], v[222:223], v[128:129], v[96:99]\n"
      "ds_read2_b32 v[128:129], v110 offset0:2 offset1:10\n"
      "v_add_u32 v111, 113728, v89\n"
      "v_mfma_f32_16x16x16_bf16 v[134:137], v[222:223], v[138:139], v[134:137]\n"
      "s_waitcnt lgkmcnt(3)\n"
      "v_alignbyte_b32 v202, v203, v202, v92\n"
      "v_max3_f32 v146, v106, v107, v108\n"
      "v_mfma_f32_16x16x32_bf16 v[156:159], a[216:219], v[124:127], 0\n"
      "ds_read2_b32 v[200:201], v110 offset0:16 offset1:17\n"
      "v_add_f32 v208, v208, v209\n"
      "s_waitcnt lgkmcnt(2)\n"
      "v_mfma_f32_16x16x32_bf16 v[156:159], a[220:223], v[170:173], v[156:159]\n"
      "ds_read2_b32 v[190:191], v110 offset0:24 offset1:18\n"
      "v_max3_f32 v161, v109, v218, v219\n"
      "v_max3_f32 v146, v146, v220, v221\n"
      "s_waitcnt vmcnt(6)\n"
      "v_or3_b32 v192, v192, v193, v194\n"
      "v_mfma_f32_16x16x32_bf16 v[156:159], a[224:227], v[184:187], v[156:159]\n"
      "v_max3_f32 v161, v161, v96, v97\n"
      "v_max3_f32 v146, v146, v98, v99\n"
      "v_max3_f32 v146, v146, v161, v134\n"
      "v_mfma_f32_16x16x32_bf16 v[160:163], a[240:243], v[124:127], 0\n"
      "s_waitcnt lgkmcnt(2)\n"
      "v_alignbyte_b32 v143, v129, v143, v92\n"
      "v_alignbyte_b32 v203, v128, v203, v92\n"
      "v_mfma_f32_16x16x32_bf16 v[160:163], a[244:247], v[170:173], v[160:163]\n"
      "ds_read_u16 v78, v11 offset:1168\n"
      "v_mov_b32_e64 v122, v146\n"
      "v_mfma_f32_16x16x32_bf16 v[160:163], a[248:251], v[184:187], v[160:163]\n"
      "s_waitcnt lgkmcnt(2)\n"
      "v_alignbyte_b32 v200, v201, v200, v92\n"
      "s_waitcnt vmcnt(5)\n"
      "v_or3_b32 v192, v192, v195, v82\n"
      "v_mfma_f32_16x16x32_bf16 v[136:139], v[232:235], v[124:127], 0\n"
      "ds_write_b32 v31, v192 offset:5632\n"
      "v_mfma_f32_16x16x32_bf16 v[136:139], v[236:239], v[170:173], v[136:139]\n"
      "s_waitcnt lgkmcnt(2)\n"
      "v_alignbyte_b32 v201, v191, v201, v92\n"
      "v_alignbyte_b32 v190, v190, v190, v92\n"
      "v_mfma_f32_16x16x32_bf16 v[136:139], v[240:243], v[184:187], v[136:139]\n"
      "v_permlane16_swap_b32 v146, v122\n"
      "v_mfma_f32_16x16x32_bf16 v[126:129], v[180:183], v[116:119], v[52:55]\n"
      "v_max_f32 v146, v146, v122\n"
      "v_mov_b32 v122, v146\n"
      "v_mfma_f32_16x16x32_bf16 v[192:195], v[176:179], v[116:119], v[56:59]\n"
      "s_nop 0\n"
      "v_permlane32_swap_b32 v146, v122\n"
      "v_max_f32 v146, v146, v122\n"
      "v_mfma_f32_16x16x32_bf16 v[170:173], v[112:115], v[116:119], v[60:63]\n"
      "v_sub_f32 v99, v99, v146\n"
      "v_sub_f32 v221, v221, v146\n"
      "v_mfma_f32_16x16x32_bf16 v[122:125], v[204:207], v[116:119], v[26:29]\n"
      "v_exp_f32 v221, v221\n"
      "v_mfma_f32_16x16x16_bf16 v[126:129], v[120:121], v[140:141], v[126:129]\n"
      "v_exp_f32 v99, v99\n"
      "v_sub_f32 v106, v106, v146\n"
      "v_mfma_f32_16x16x16_bf16 v[192:195], v[188:189], v[140:141], v[192:195]\n"
      "v_sub_f32 v220, v220, v146\n"
      "v_sub_f32 v97, v97, v146\n"
      "v_sub_f32 v98, v98, v146\n"
      "v_mfma_f32_16x16x16_bf16 v[170:173], v[210:211], v[140:141], v[170:173]\n"
      "v_sub_f32 v107, v107, v146\n"
      "v_sub_f32 v109, v109, v146\n"
      "v_sub_f32 v108, v108, v146\n"
      "v_mfma_f32_16x16x16_bf16 v[122:125], v[212:213], v[140:141], v[122:125]\n"
      "v_exp_f32 v98, v98\n"
      "v_exp_f32 v220, v220\n"
      "v_mfma_f32_16x16x16_bf16 v[126:129], v[168:169], v[202:203], v[126:129]\n"
      "v_exp_f32 v107, v107\n"
      "v_sub_f32 v96, v96, v146\n"
      "v_mfma_f32_16x16x16_bf16 v[192:195], v[168:169], v[142:143], v[192:195]\n"
      "v_cvt_pk_bf16_f32 v89, v98, v99\n"
      "v_exp_f32 v106, v106\n"
      "v_mfma_f32_16x16x16_bf16 v[170:173], v[168:169], v[200:201], v[170:173]\n"
      "v_exp_f32 v109, v109\n"
      "v_sub_f32_e64 v218, v218, v146\n"
      "v_mfma_f32_16x16x16_bf16 v[122:125], v[168:169], v[190:191], v[122:125]\n"
      "v_cvt_pk_bf16_f32 v106, v106, v107\n"
      "v_exp_f32_e64 v108, v108\n"
      "v_mfma_f32_16x16x16_bf16 v[126:129], v[222:223], v[202:203], v[126:129]\n"
      "ds_read_b128 v[116:119], v2 offset:9472\n"
      "v_exp_f32 v97, v97\n"
      "v_mfma_f32_16x16x16_bf16 v[192:195], v[222:223], v[142:143], v[192:195]\n"
      "v_sub_f32 v219, v219, v146\n"
      "v_exp_f32 v219, v219\n"
      "v_sub_f32 v134, v134, v146\n"
      "v_mfma_f32_16x16x16_bf16 v[170:173], v[222:223], v[200:201], v[170:173]\n"
      "ds_read_b128 v[140:143], v2 offset:12544\n"
      "ds_read_b128 v[184:187], v2 offset:15616\n"
      "v_mfma_f32_16x16x16_bf16 v[122:125], v[222:223], v[190:191], v[122:125]\n"
      "v_exp_f32 v96, v96\n"
      "v_exp_f32 v218, v218\n"
      "v_cvt_pk_bf16_f32 v107, v108, v109\n"
      "v_cvt_pk_bf16_f32 v108, v218, v219\n"
      "v_max3_f32 v219, v126, v127, v128\n"
      "v_cvt_pk_bf16_f32 v88, v96, v97\n"
      "v_max3_f32 v219, v219, v194, v195\n"
      "v_exp_f32_e64 v134, v134\n"
      "v_cvt_pk_bf16_f32 v109, v220, v221\n"
      "v_max3_f32 v125, v129, v192, v193\n"
      "v_max3_f32 v219, v219, v172, v173\n"
      "v_max3_f32 v125, v125, v170, v171\n"
      "v_max3_f32 v219, v219, v125, v122\n"
      "v_mov_b32 v125, v219\n"
      "s_waitcnt lgkmcnt(2)\n"
      "v_mfma_f32_16x16x32_bf16 v[156:159], a[228:231], v[116:119], v[156:159]\n"
      "s_waitcnt lgkmcnt(1)\n"
      "v_permlane16_swap_b32 v219, v125\n"
      "v_mfma_f32_16x16x32_bf16 v[156:159], a[232:235], v[140:143], v[156:159]\n"
      "s_waitcnt lgkmcnt(0)\n"
      "v_max_f32 v219, v219, v125\n"
      "v_mov_b32 v125, v219\n"
      "v_mfma_f32_16x16x32_bf16 v[156:159], a[236:239], v[184:187], v[156:159]\n"
      "s_nop 0\n"
      "v_permlane32_swap_b32_e64 v219, v125\n"
      "v_mfma_f32_16x16x32_bf16 v[160:163], a[252:255], v[116:119], v[160:163]\n"
      "v_max_f32 v219, v219, v125\n"
      "v_sub_f32 v126, v126, v219\n"
      "v_mfma_f32_16x16x32_bf16 v[160:163], v[224:227], v[140:143], v[160:163]\n"
      "ds_read2_b32 v[124:125], v111 offset0:8 offset1:9\n"
      "v_sub_f32 v195, v195, v219\n"
      "v_mfma_f32_16x16x32_bf16 v[160:163], v[228:231], v[184:187], v[160:163]\n"
      "ds_read_u16 v82, v11 offset:1200\n"
      "v_sub_f32 v128, v128, v219\n"
      "v_mfma_f32_16x16x32_bf16 v[136:139], v[244:247], v[116:119], v[136:139]\n"
      "ds_read2_b32 v[146:147], v111 offset0:16 offset1:17\n"
      "v_sub_f32 v127, v127, v219\n"
      "v_mfma_f32_16x16x32_bf16 v[136:139], v[248:251], v[140:143], v[136:139]\n"
      "ds_read2_b32 v[200:201], v111 offset0:2 offset1:10\n"
      "v_sub_f32 v129, v129, v219\n"
      "v_sub_f32 v172, v172, v219\n"
      "v_mfma_f32_16x16x32_bf16 v[136:139], v[252:255], v[184:187], v[136:139]\n"
      "ds_read_u16 v86, v11 offset:1232\n"
      "v_sub_f32 v122, v122, v219\n"
      "v_sub_f32 v193, v193, v219\n"
      "v_sub_f32 v170, v170, v219\n"
      "v_exp_f32 v129, v129\n"
      "v_sub_f32 v171, v171, v219\n"
      "v_cvt_pk_bf16_f32 v90, v134, 0\n"
      "v_mfma_f32_16x16x32_bf16 v[116:119], v[32:35], v[106:109], 0\n"
      "ds_read2_b32 v[174:175], v111 offset0:0 offset1:1\n"
      "v_exp_f32 v127, v127\n"
      "v_mfma_f32_16x16x32_bf16 v[116:119], v[32:35], v[88:91], v[116:119]\n"
      "ds_read2_b32 v[104:105], v111 offset0:24 offset1:18\n"
      "v_sub_f32 v192, v192, v219\n"
      "v_exp_f32 v126, v126\n"
      "v_exp_f32 v195, v195\n"
      "v_exp_f32 v128, v128\n"
      "v_sub_f32 v173, v173, v219\n"
      "v_sub_f32 v194, v194, v219\n"
      "v_mfma_f32_16x16x32_bf16 v[140:143], v[196:199], v[106:109], 0\n"
      "s_waitcnt lgkmcnt(2)\n"
      "v_alignbyte_b32 v146, v147, v146, v92\n"
      "v_alignbyte_b32 v124, v125, v124, v92\n"
      "v_mfma_f32_16x16x32_bf16 v[140:143], v[76:79], v[88:91], v[140:143]\n"
      "v_exp_f32_e64 v193, v193\n"
      "v_mfma_f32_16x16x32_bf16 v[218:221], v[214:217], v[106:109], 0\n"
      "s_waitcnt lgkmcnt(1)\n"
      "v_alignbyte_b32 v125, v201, v125, v92\n"
      "v_alignbyte_b32 v174, v175, v174, v92\n"
      "v_mfma_f32_16x16x32_bf16 v[218:221], v[80:83], v[88:91], v[218:221]\n"
      "s_waitcnt lgkmcnt(0)\n"
      "v_alignbyte_b32 v175, v200, v175, v92\n"
      "v_exp_f32_e64 v194, v194\n"
      "v_mfma_f32_16x16x32_bf16 v[184:187], v[130:133], v[106:109], 0\n"
      "v_alignbyte_b32 v147, v105, v147, v92\n"
      "v_cvt_pk_bf16_f32 v126, v126, v127\n"
      "v_mfma_f32_16x16x32_bf16 v[184:187], v[84:87], v[88:91], v[184:187]\n"
      "v_cvt_pk_bf16_f32 v127, v128, v129\n"
      "v_alignbyte_b32 v104, v104, v104, v92\n"
      "v_mfma_f32_16x16x32_bf16 v[106:109], v[180:183], v[152:155], v[64:67]\n"
      "v_exp_f32_e64 v192, v192\n"
      "v_mfma_f32_16x16x32_bf16 v[200:203], v[176:179], v[152:155], v[68:71]\n"
      "v_cvt_pk_bf16_f32 v128, v192, v193\n"
      "v_cvt_pk_bf16_f32 v129, v194, v195\n"
      "v_mfma_f32_16x16x32_bf16 v[96:99], v[112:115], v[152:155], v[72:75]\n"
      "v_rcp_f32 v116, v116\n"
      "v_mfma_f32_16x16x32_bf16 v[190:193], v[204:207], v[152:155], v[28:31]\n"
      "v_exp_f32 v171, v171\n"
      "v_mfma_f32_16x16x16_bf16 v[106:109], v[120:121], v[144:145], v[106:109]\n"
      "v_exp_f32 v172, v172\n"
      "v_mul_f32 v185, v116, v185\n"
      "v_mfma_f32_16x16x16_bf16 v[200:203], v[188:189], v[144:145], v[200:203]\n"
      "v_mul_f32 v218, v116, v218\n"
      "v_exp_f32 v173, v173\n"
      "v_mfma_f32_16x16x16_bf16 v[96:99], v[210:211], v[144:145], v[96:99]\n"
      "v_exp_f32 v170, v170\n"
      "v_mul_f32 v143, v116, v143\n"
      "v_mfma_f32_16x16x16_bf16 v[190:193], v[212:213], v[144:145], v[190:193]\n"
      "v_mul_f32 v219, v116, v219\n"
      "v_mul_f32 v140, v116, v140\n"
      "v_cvt_pk_bf16_f32 v92, v170, v171\n"
      "v_mfma_f32_16x16x16_bf16 v[106:109], v[168:169], v[174:175], v[106:109]\n"
      "v_mul_f32 v141, v116, v141\n"
      "v_exp_f32 v122, v122\n"
      "v_mfma_f32_16x16x16_bf16 v[200:203], v[168:169], v[124:125], v[200:203]\n"
      "v_mul_f32 v184, v116, v184\n"
      "v_fma_f32 v110, v156, v156, s52\n"
      "v_cvt_pk_bf16_f32 v93, v172, v173\n"
      "v_mfma_f32_16x16x16_bf16 v[96:99], v[168:169], v[146:147], v[96:99]\n"
      "v_mul_f32 v111, v157, v157\n"
      "v_mul_f32 v142, v116, v142\n"
      "v_cvt_pk_bf16_f32 v140, v140, v141\n"
      "v_mfma_f32_16x16x16_bf16 v[190:193], v[168:169], v[104:105], v[190:193]\n"
      "v_fmac_f32 v110, v158, v158\n"
      "v_cvt_pk_bf16_f32 v141, v142, v143\n"
      "v_mul_f32 v186, v116, v186\n"
      "v_mfma_f32_16x16x16_bf16 v[106:109], v[222:223], v[174:175], v[106:109]\n"
      "ds_read_b128 v[170:173], v2 offset:512\n"
      "v_mul_f32 v187, v116, v187\n"
      "v_mfma_f32_16x16x16_bf16 v[200:203], v[222:223], v[124:125], v[200:203]\n"
      "v_cvt_pk_bf16_f32 v184, v184, v185\n"
      "v_cvt_pk_bf16_f32 v94, v122, 0\n"
      "v_cvt_pk_bf16_f32 v142, v218, v219\n"
      "v_mfma_f32_16x16x16_bf16 v[96:99], v[222:223], v[146:147], v[96:99]\n"
      "ds_read_b128 v[152:155], v2 offset:3584\n"
      "v_cvt_pk_bf16_f32 v185, v186, v187\n"
      "v_mfma_f32_16x16x16_bf16 v[190:193], v[222:223], v[104:105], v[190:193]\n"
      "ds_read_b128 v[122:125], v2 offset:6656\n"
      "v_fmac_f32_e64 v111, v159, v159\n"
      "v_max3_f32 v90, v106, v107, v108\n"
      "v_max3_f32 v90, v90, v202, v203\n"
      "v_max3_f32 v118, v109, v200, v201\n"
      "v_max3_f32 v90, v90, v98, v99\n"
      "v_pk_mul_f32 v[220:221], v[220:221], v[116:117] op_sel_hi:[1,0]\n"
      "v_cvt_pk_bf16_f32 v143, v220, v221\n"
      "v_pk_fma_f32 v[110:111], v[160:161], v[160:161], v[110:111]\n"
      "v_max3_f32 v118, v118, v96, v97\n"
      "v_pk_fma_f32 v[110:111], v[162:163], v[162:163], v[110:111]\n"
      "v_max3_f32 v90, v90, v118, v190\n"
      "v_pk_fma_f32 v[110:111], v[136:137], v[136:137], v[110:111]\n"
      "v_mov_b32 v192, v90\n"
      "v_pk_fma_f32 v[110:111], v[138:139], v[138:139], v[110:111]\n"
      "v_add_f32 v110, v110, v111\n"
      "v_permlane16_swap_b32 v90, v192\n"
      "v_max_f32 v90, v90, v192\n"
      "v_mov_b32 v192, v90\n"
      "s_waitcnt lgkmcnt(1)\n"
      "v_mfma_f32_16x16x32_bf16 v[116:119], a[216:219], v[170:173], 0\n"
      "v_permlane32_swap_b32 v90, v192\n"
      "v_mfma_f32_16x16x32_bf16 v[116:119], a[220:223], v[152:155], v[116:119]\n"
      "s_waitcnt lgkmcnt(0)\n"
      "v_max_f32 v90, v90, v192\n"
      "v_sub_f32 v202, v202, v90\n"
      "v_mfma_f32_16x16x32_bf16 v[116:119], a[224:227], v[122:125], v[116:119]\n"
      "ds_write_b128 v3, v[140:143] offset:18432\n"
      "v_mfma_f32_16x16x32_bf16 v[192:195], a[240:243], v[170:173], 0\n"
      "v_sub_f32 v108, v108, v90\n"
      "v_sub_f32 v106, v106, v90\n"
      "v_mfma_f32_16x16x32_bf16 v[192:195], a[244:247], v[152:155], v[192:195]\n"
      "v_exp_f32 v108, v108\n"
      "v_mfma_f32_16x16x32_bf16 v[192:195], a[248:251], v[122:125], v[192:195]\n"
      "v_sub_f32 v200, v200, v90\n"
      "v_sub_f32 v107, v107, v90\n"
      "v_mfma_f32_16x16x32_bf16 v[140:143], v[232:235], v[170:173], 0\n"
      "v_sub_f32 v109, v109, v90\n"
      "v_exp_f32 v200, v200\n"
      "v_mfma_f32_16x16x32_bf16 v[140:143], v[236:239], v[152:155], v[140:143]\n"
      "v_exp_f32 v109, v109\n"
      "v_mfma_f32_16x16x32_bf16 v[140:143], v[240:243], v[122:125], v[140:143]\n"
      "v_exp_f32 v106, v106\n"
      "v_mfma_f32_16x16x32_bf16 v[218:221], v[32:35], v[126:129], 0\n"
      "v_exp_f32 v107, v107\n"
      "v_mfma_f32_16x16x32_bf16 v[218:221], v[32:35], v[92:95], v[218:221]\n"
      "ds_read_b128 v[122:125], v10 offset:1168\n"
      "v_sub_f32_e64 v201, v201, v90\n"
      "v_mfma_f32_16x16x32_bf16 v[152:155], v[196:199], v[126:129], 0\n"
      "v_cvt_pk_bf16_f32 v106, v106, v107\n"
      "v_cvt_pk_bf16_f32 v107, v108, v109\n"
      "v_mfma_f32_16x16x32_bf16 v[152:155], v[76:79], v[92:95], v[152:155]\n"
      "ds_read_b64 v[88:89], v10 offset:1184\n"
      "v_sub_f32 v99, v99, v90\n"
      "v_mfma_f32_16x16x32_bf16 v[220:223], v[214:217], v[126:129], 0\n"
      "v_exp_f32 v201, v201\n"
      "v_mfma_f32_16x16x32_bf16 v[220:223], v[80:83], v[92:95], v[220:223]\n"
      "v_sub_f32 v203, v203, v90\n"
      "v_cvt_pk_bf16_f32 v108, v200, v201\n"
      "v_mfma_f32_16x16x32_bf16 v[170:173], v[130:133], v[126:129], 0\n"
      "v_rcp_f32 v218, v218\n"
      "v_mfma_f32_16x16x32_bf16 v[170:173], v[84:87], v[92:95], v[170:173]\n"
      "s_waitcnt lgkmcnt(1)\n"
      "v_exp_f32 v203, v203\n"
      "v_mfma_f32_16x16x32_bf16 v[126:129], v[122:125], v[180:183], v[30:33]\n"
      "v_exp_f32_e64 v202, v202\n"
      "v_mfma_f32_16x16x32_bf16 v[144:147], v[122:125], v[176:179], v[30:33]\n"
      "s_waitcnt lgkmcnt(0)\n"
      "v_cvt_pk_bf16_f32 v109, v202, v203\n"
      "v_sub_f32 v97, v97, v90\n"
      "v_mfma_f32_16x16x32_bf16 v[200:203], v[122:125], v[112:115], v[30:33]\n"
      "ds_read_b128 v[112:115], v2 offset:9728\n"
      "v_sub_f32 v96, v96, v90\n"
      "v_mfma_f32_16x16x32_bf16 v[180:183], v[122:125], v[204:207], v[30:33]\n"
      "ds_read_b128 v[204:207], v2 offset:12800\n"
      "v_sub_f32 v98, v98, v90\n"
      "v_mfma_f32_16x16x16_bf16 v[126:129], v[88:89], v[120:121], v[126:129]\n"
      "ds_read_b128 v[122:125], v2 offset:15872\n"
      "v_sub_f32 v190, v190, v90\n"
      "v_mfma_f32_16x16x16_bf16 v[144:147], v[88:89], v[188:189], v[144:147]\n"
      "v_exp_f32 v97, v97\n"
      "v_exp_f32 v99, v99\n"
      "v_mfma_f32_16x16x16_bf16 v[200:203], v[88:89], v[210:211], v[200:203]\n"
      "v_exp_f32 v98, v98\n"
      "v_exp_f32_e64 v96, v96\n"
      "v_exp_f32_e64 v190, v190\n"
      "v_pk_mul_f32 v[170:171], v[170:171], v[218:219] op_sel_hi:[1,0]\n"
      "v_pk_mul_f32 v[220:221], v[220:221], v[218:219] op_sel_hi:[1,0]\n"
      "v_pk_mul_f32 v[222:223], v[222:223], v[218:219] op_sel_hi:[1,0]\n"
      "v_pk_mul_f32 v[154:155], v[154:155], v[218:219] op_sel_hi:[1,0]\n"
      "v_pk_mul_f32 v[152:153], v[152:153], v[218:219] op_sel_hi:[1,0]\n"
      "v_mfma_f32_16x16x16_bf16 v[180:183], v[88:89], v[212:213], v[180:183]\n"
      "v_cndmask_b32 v126, v126, v144, s[58:59]\n"
      "v_cndmask_b32 v126, v126, v200, s[60:61]\n"
      "s_waitcnt lgkmcnt(1)\n"
      "v_mfma_f32_16x16x32_bf16 v[116:119], a[228:231], v[112:115], v[116:119]\n"
      "s_waitcnt lgkmcnt(0)\n"
      "v_cvt_pk_bf16_f32 v88, v96, v97\n"
      "v_lshlrev_b32_e64 v39, 16, v39\n"
      "v_mfma_f32_16x16x32_bf16 v[116:119], a[232:235], v[204:207], v[116:119]\n"
      "v_cndmask_b32 v126, v126, v180, s[62:63]\n"
      "v_cvt_pk_bf16_f32 v89, v98, v99\n"
      "v_mfma_f32_16x16x32_bf16 v[116:119], a[236:239], v[122:125], v[116:119]\n"
      "v_fmac_f32_e64 v126, s53, v39\n"
      "v_cvt_pk_bf16_f32 v152, v152, v153\n"
      "v_mfma_f32_16x16x32_bf16 v[192:195], a[252:255], v[112:115], v[192:195]\n"
      "ds_write_b64 v4, v[184:185] offset:18432\n"
      "v_mfma_f32_16x16x32_bf16 v[192:195], v[224:227], v[204:207], v[192:195]\n"
      "v_max_f32_dpp v213, v126, v126 row_ror:8 row_mask:0xf bank_mask:0xf\n"
      "v_cvt_pk_bf16_f32 v153, v154, v155\n"
      "v_mfma_f32_16x16x32_bf16 v[192:195], v[228:231], v[122:125], v[192:195]\n"
      "v_cvt_pk_bf16_f32 v90, v190, 0\n"
      "v_max_f32_dpp v213, v213, v213 row_ror:4 row_mask:0xf bank_mask:0xf\n"
      "v_mfma_f32_16x16x32_bf16 v[140:143], v[244:247], v[112:115], v[140:143]\n"
      "v_mul_f32_e64 v185, v117, v117\n"
      "v_max_f32_dpp v213, v213, v213 row_ror:2 row_mask:0xf bank_mask:0xf\n"
      "v_mfma_f32_16x16x32_bf16 v[140:143], v[248:251], v[204:207], v[140:143]\n"
      "v_cvt_pk_bf16_f32 v155, v222, v223\n"
      "v_max_f32_dpp v213, v213, v213 row_ror:1 row_mask:0xf bank_mask:0xf\n"
      "v_mfma_f32_16x16x32_bf16 v[140:143], v[252:255], v[122:125], v[140:143]\n"
      "v_fma_f32 v184, v116, v116, s52\n"
      "v_max_f32_dpp v213, v213, v213 row_bcast:15 row_mask:0xa bank_mask:0xf\n"
      "v_mfma_f32_16x16x32_bf16 v[202:205], v[32:35], v[106:109], 0\n"
      "s_nop 0\n"
      "v_max_f32_dpp v213, v213, v213 row_bcast:31 row_mask:0xc bank_mask:0xf\n"
      "v_cvt_pk_bf16_f32 v154, v220, v221\n"
      "v_mfma_f32_16x16x32_bf16 v[202:205], v[32:35], v[88:91], v[202:205]\n"
      "v_cvt_pk_bf16_f32 v170, v170, v171\n"
      "v_readlane_b32 s72, v213, 63\n"
      "v_mfma_f32_16x16x32_bf16 v[210:213], v[196:199], v[106:109], 0\n"
      "ds_write_b128 v3, v[152:155] offset:18688\n"
      "v_subrev_f32 v126, s72, v126\n"
      "v_pk_fma_f32 v[184:185], v[118:119], v[118:119], v[184:185]\n"
      "v_mfma_f32_16x16x32_bf16 v[210:213], v[76:79], v[88:91], v[210:213]\n"
      "s_nop 0\n"
      "v_rcp_f32 v202, v202\n"
      "v_mfma_f32_16x16x32_bf16 v[204:207], v[214:217], v[106:109], 0\n"
      "v_exp_f32 v126, v126\n"
      "v_mfma_f32_16x16x32_bf16 v[204:207], v[80:83], v[88:91], v[204:207]\n"
      "v_permlane16_swap_b32 v208, v110\n"
      "v_add_f32_e64 v208, v208, v110\n"
      "v_add_u32 v10, 2336, v10\n"
      "v_cvt_pk_bf16_f32 v126, v126, v126\n"
      "v_pk_mul_f32 v[210:211], v[210:211], v[202:203] op_sel_hi:[1,0]\n"
      "v_pk_mul_f32 v[212:213], v[212:213], v[202:203] op_sel_hi:[1,0]\n"
      "v_pk_fma_f32 v[184:185], v[192:193], v[192:193], v[184:185]\n"
      "v_pk_fma_f32 v[184:185], v[194:195], v[194:195], v[184:185]\n"
      "v_pk_mul_f32 v[204:205], v[204:205], v[202:203] op_sel_hi:[1,0]\n"
      "v_pk_fma_f32 v[184:185], v[140:141], v[140:141], v[184:185]\n"
      "v_pk_fma_f32 v[184:185], v[142:143], v[142:143], v[184:185]\n"
      "v_cvt_pk_bf16_f32 v210, v210, v211\n"
      "v_cvt_pk_bf16_f32 v211, v212, v213\n"
      "v_pk_mul_f32 v[206:207], v[206:207], v[202:203] op_sel_hi:[1,0]\n"
      "v_cvt_pk_bf16_f32 v213, v206, v207\n"
      "v_mfma_f32_16x16x32_bf16 v[96:99], v[130:133], v[106:109], 0\n"
      "ds_write_b16 v13, v126\n"
      "v_cvt_pk_bf16_f32 v212, v204, v205\n"
      "v_mfma_f32_16x16x32_bf16 v[96:99], v[84:87], v[88:91], v[96:99]\n"
      "ds_read_b128 v[106:109], v14 offset:64\n"
      "ds_write_b128 v3, v[210:213] offset:18944\n"
      "v_add_f32 v184, v184, v185\n"
      "v_mov_b32 v110, v184\n"
      "v_pk_mul_f32 v[172:173], v[172:173], v[218:219] op_sel_hi:[1,0]\n"
      "v_cvt_pk_bf16_f32 v171, v172, v173\n"
      "v_permlane16_swap_b32 v184, v110\n"
      "v_add_f32_e64 v184, v184, v110\n"
      "v_pk_mul_f32 v[98:99], v[98:99], v[202:203] op_sel_hi:[1,0]\n"
      "s_and_b32 s47, s47, -4\n"
      "ds_write_b64 v4, v[170:171] offset:18688\n"
      "v_permlane32_swap_b32 v208, v184\n"
      "v_add_f32_e64 v208, v208, v184\n"
      "s_add_u32 s42, s42, 1\n"
      "ds_read_b128 v[210:213], v14\n"
      "ds_write_b32 v31, v208 offset:1024\n"
      "v_pk_mul_f32 v[96:97], v[96:97], v[202:203] op_sel_hi:[1,0]\n"
      "v_cvt_pk_bf16_f32 v96, v96, v97\n"
      "v_cvt_pk_bf16_f32 v97, v98, v99\n"
      "s_add_u32 s43, s43, 1\n"
      "ds_write_b64 v4, v[96:97] offset:18944\n"
      "s_waitcnt lgkmcnt(2)\n"
      "v_mfma_f32_16x16x32_bf16 v[218:221], v[210:213], v[32:35], 0\n"
      "v_mfma_f32_16x16x32_bf16 v[218:221], v[106:109], v[32:35], v[218:221]\n"
      "v_add_u32 v11, 2336, v11\n"
      "s_waitcnt lgkmcnt(0)\n"
      "s_barrier\n"
      "s_cbranch_vccz L_sel0_%=\n"
      "L_disp0_%=:\n"
      "s_cmp_lt_u32 s95, 0x40000000\n"
      "s_cbranch_scc1 L_nofix_18_%=\n"
      "s_cmp_eq_u32 s95, -2\n"
      "s_cbranch_scc1 L_tail0_%=\n"
      "s_nop 2\n"
      "v_lshlrev_b32 v205, 3, v36\n"
      "s_add_u32 s72, s98, L_cold_%=-L_ipf0_%=\n"
      "s_addc_u32 s73, s99, 0\n"
      "s_mov_b32 s74, L_cend_%=-L_cold_%=\n"
      "s_mov_b32 s75, 0x20000\n"
      "s_mov_b32 m0, 161792\n"
      "s_nop 0\n"
      "buffer_load_dwordx4 v205, s[72:75], 0 offen  lds\n"
      "s_lshr_b32 s92, s45, 30\n"
      "s_or_b32 s92, s92, s66\n"
      "s_cbranch_scc0 L_nf_19_%=\n"
      "s_bitcmp1_b32 s45, 31\n"
      "s_cselect_b32 s75, 100270464, 0\n"
      "s_bitcmp1_b32 s45, 30\n"
      "s_cselect_b32 s76, 196224, 0\n"
      "s_and_b32 s74, s45, 0x3fffffff\n"
      "s_cmp_ge_u32 s45, 0x40000000\n"
      "s_cselect_b32 s74, s74, 0\n"
      "s_cselect_b32 s45, 0, s45\n"
      "s_sub_u32 s74, s74, s66\n"
      "s_add_u32 s66, s66, s74\n"
      "s_sub_u32 s75, s75, s67\n"
      "s_add_u32 s67, s67, s75\n"
      "s_sub_u32 s76, s76, s68\n"
      "s_add_u32 s68, s68, s76\n"
      "v_add_u32 v16, s74, v16\n"
      "v_add_u32 v17, s74, v17\n"
      "v_add_u32 v18, s74, v18\n"
      "v_add_u32 v19, s74, v19\n"
      "v_add_u32 v15, s74, v15\n"
      "s_cmp_eq_u32 s75, 0\n"
      "s_cbranch_scc1 L_ax_20_%=\n"
      "v_bfe_i32 v205, v27, 0, 1\n"
      "v_and_b32 v205, s75, v205\n"
      "v_sub_u32 v16, v16, v205\n"
      "v_bfe_i32 v205, v27, 2, 1\n"
      "v_and_b32 v205, s75, v205\n"
      "v_sub_u32 v17, v17, v205\n"
      "v_bfe_i32 v205, v27, 4, 1\n"
      "v_and_b32 v205, s75, v205\n"
      "v_sub_u32 v18, v18, v205\n"
      "v_bfe_i32 v205, v27, 6, 1\n"
      "v_and_b32 v205, s75, v205\n"
      "v_sub_u32 v19, v19, v205\n"
      "v_bfe_i32 v205, v27, 8, 1\n"
      "v_and_b32 v205, s75, v205\n"
      "v_sub_u32 v15, v15, v205\n"
      "L_ax_20_%=:\n"
      "s_cmp_eq_u32 s76, 0\n"
      "s_cbranch_scc1 L_ax_21_%=\n"
      "v_bfe_i32 v205, v27, 1, 1\n"
      "v_and_b32 v205, s76, v205\n"
      "v_sub_u32 v16, v16, v205\n"
      "v_bfe_i32 v205, v27, 3, 1\n"
      "v_and_b32 v205, s76, v205\n"
      "v_sub_u32 v17, v17, v205\n"
      "v_bfe_i32 v205, v27, 5, 1\n"
      "v_and_b32 v205, s76, v205\n"
      "v_sub_u32 v18, v18, v205\n"
      "v_bfe_i32 v205, v27, 7, 1\n"
      "v_and_b32 v205, s76, v205\n"
      "v_sub_u32 v19, v19, v205\n"
      "v_bfe_i32 v205, v27, 9, 1\n"
      "v_and_b32 v205, s76, v205\n"
      "v_sub_u32 v15, v15, v205\n"
      "L_ax_21_%=:\n"
      "s_or_b32 s48, s66, s69\n"
      "s_cmp_lg_u32 s48, 0\n"
      "s_cselect_b32 s48, 0x40000000, 0\n"
      "L_nf_19_%=:\n"
      "s_lshr_b32 s92, s46, 30\n"
      "s_or_b32 s92, s92, s69\n"
      "s_cbranch_scc0 L_nf_22_%=\n"
      "s_bitcmp1_b32 s46, 31\n"
      "s_cselect_b32 s75, 100270464, 0\n"
      "s_bitcmp1_b32 s46, 30\n"
      "s_cselect_b32 s76, 196224, 0\n"
      "s_and_b32 s74, s46, 0x3fffffff\n"
      "s_cmp_ge_u32 s46, 0x40000000\n"
      "s_cselect_b32 s74, s74, 0\n"
      "s_cselect_b32 s46, 0, s46\n"
      "s_sub_u32 s74, s74, s69\n"
      "s_add_u32 s69, s69, s74\n"
      "s_sub_u32 s75, s75, s70\n"
      "s_add_u32 s70, s70, s75\n"
      "s_sub_u32 s76, s76, s71\n"
      "s_add_u32 s71, s71, s76\n"
      "v_add_u32 v20, s74, v20\n"
      "v_add_u32 v21, s74, v21\n"
      "v_add_u32 v22, s74, v22\n"
      "v_add_u32 v23, s74, v23\n"
      "v_add_u32 v25, s74, v25\n"
      "s_cmp_eq_u32 s75, 0\n"
      "s_cbranch_scc1 L_ax_23_%=\n"
      "v_bfe_i32 v205, v27, 10, 1\n"
      "v_and_b32 v205, s75, v205\n"
      "v_sub_u32 v20, v20, v205\n"
      "v_bfe_i32 v205, v27, 12, 1\n"
      "v_and_b32 v205, s75, v205\n"
      "v_sub_u32 v21, v21, v205\n"
      "v_bfe_i32 v205, v27, 14, 1\n"
      "v_and_b32 v205, s75, v205\n"
      "v_sub_u32 v22, v22, v205\n"
      "v_bfe_i32 v205, v27, 16, 1\n"
      "v_and_b32 v205, s75, v205\n"
      "v_sub_u32 v23, v23, v205\n"
      "v_bfe_i32 v205, v27, 18, 1\n"
      "v_and_b32 v205, s75, v205\n"
      "v_sub_u32 v25, v25, v205\n"
      "L_ax_23_%=:\n"
      "s_cmp_eq_u32 s76, 0\n"
      "s_cbranch_scc1 L_ax_24_%=\n"
      "v_bfe_i32 v205, v27, 11, 1\n"
      "v_and_b32 v205, s76, v205\n"
      "v_sub_u32 v20, v20, v205\n"
      "v_bfe_i32 v205, v27, 13, 1\n"
      "v_and_b32 v205, s76, v205\n"
      "v_sub_u32 v21, v21, v205\n"
      "v_bfe_i32 v205, v27, 15, 1\n"
      "v_and_b32 v205, s76, v205\n"
      "v_sub_u32 v22, v22, v205\n"
      "v_bfe_i32 v205, v27, 17, 1\n"
      "v_and_b32 v205, s76, v205\n"
      "v_sub_u32 v23, v23, v205\n"
      "v_bfe_i32 v205, v27, 19, 1\n"
      "v_and_b32_e64 v205, s76, v205\n"
      "v_sub_u32_e64 v25, v25, v205\n"
      "L_ax_24_%=:\n"
      "s_or_b32 s48, s66, s69\n"
      "s_cmp_lg_u32 s48, 0\n"
      "s_cselect_b32 s48, 0x40000000, 0\n"
      "L_nf_22_%=:\n"
      "s_sub_u32 s92, s42, 1\n"
      "s_cmp_gt_u32 s92, s44\n"
      "s_cbranch_scc1 L_tail0_%=\n"
      "L_nofix_18_%=:\n"
      "s_cmp_lg_u64 s[56:57], 0\n"
      "s_cbranch_scc1 L_slow0_%=\n"
      "L_sel0_%=:\n"
      "s_cmp_lg_u64 s[54:55], 0\n"
      "s_cbranch_scc1 L_max0_%=\n"
      "s_branch L_loop0_%=\n"
      "L_slow0_%=:\n"
      "s_nop 4\n"
      "v_mfma_f32_16x16x32_bf16 v[204:207], v[210:213], v[196:199], 0\n"
      "ds_read_b128 v[120:123], v0\n"
      "ds_read_b128 v[184:187], v9 offset:1024\n"
      "v_mfma_f32_16x16x32_bf16 v[204:207], v[106:109], v[76:79], v[204:207]\n"
      "ds_read_b128 v[200:203], v0 offset:6144\n"
      "v_readlane_b32 s85, v37, s42\n"
      "v_mfma_f32_16x16x32_bf16 v[176:179], v[210:213], v[214:217], 0\n"
      "ds_read_b128 v[168:171], v1\n"
      "v_mov_b32 v104, 0x3fb9\n"
      "v_mfma_f32_16x16x32_bf16 v[176:179], v[106:109], v[80:83], v[176:179]\n"
      "ds_read_b128 v[172:175], v1 offset:2048\n"
      "v_subrev_u32 v198, 115456, v9\n"
      "v_mfma_f32_16x16x32_bf16 v[96:99], v[210:213], v[130:133], 0\n"
      "s_add_u32 s72, s42, 1\n"
      "v_readlane_b32 s84, v37, s43\n"
      "v_readlane_b32 s77, v38, s72\n"
      "s_mov_b32 m0, 110592\n"
      "v_mfma_f32_16x16x32_bf16 v[96:99], v[106:109], v[84:87], v[96:99]\n"
      "s_and_b32 s73, s77, -16\n"
      "v_mov_b32_dpp v218, v218 quad_perm:[0,0,0,0] row_mask:0xf bank_mask:0xf\n"
      "s_waitcnt lgkmcnt(4)\n"
      "v_mfma_f32_16x16x32_bf16 v[210:213], a[0:3], v[120:123], 0\n"
      "buffer_load_dwordx4 v198, s[28:31], s73 offen  lds\n"
      "v_mfma_f32_16x16x32_bf16 v[144:147], a[24:27], v[120:123], 0\n"
      "s_waitcnt lgkmcnt(3)\n"
      "v_rcp_f32 v218, v218\n"
      "v_add_f32 v186, v186, v187\n"
      "v_mfma_f32_16x16x32_bf16 v[206:209], a[48:51], v[120:123], 0\n"
      "v_add_f32 v184, v184, v185\n"
      "v_add_f32 v184, v184, v186\n"
      "v_mfma_f32_16x16x32_bf16 v[126:129], a[72:75], v[120:123], 0\n"
      "s_waitcnt lgkmcnt(0)\n"
      "v_rsq_f32 v184, v184\n"
      "v_mfma_f32_16x16x32_bf16 v[106:109], a[96:99], v[120:123], 0\n"
      "s_add_u32 s73, s73, 1024\n"
      "v_readlane_b32 s86, v38, s43\n"
      "v_mul_f32 v184, 0x415db3d7, v184\n"
      "v_mfma_f32_16x16x32_bf16 v[180:183], a[120:123], v[120:123], 0\n"
      "ds_read_b128 v[152:155], v1 offset:6144\n"
      "v_mov_b32_e64 v186, v184\n"
      "v_mfma_f32_16x16x32_bf16 v[210:213], a[4:7], v[168:171], v[210:213]\n"
      "s_mov_b32 m0, 111616\n"
      "v_permlane32_swap_b32_e64 v184, v186\n"
      "v_mfma_f32_16x16x32_bf16 v[210:213], a[8:11], v[200:203], v[210:213]\n"
      "buffer_load_dwordx4 v198, s[28:31], s73 offen  lds\n"
      "v_mfma_f32_16x16x32_bf16 v[144:147], a[28:31], v[168:171], v[144:147]\n"
      "ds_read_b128 v[110:113], v1 offset:12288\n"
      "v_mov_b32_e64 v185, v184\n"
      "v_mfma_f32_16x16x32_bf16 v[144:147], a[32:35], v[200:203], v[144:147]\n"
      "ds_read_b128 v[188:191], v0 offset:12288\n"
      "v_mul_f32_e64 v195, v186, v195\n"
      "v_mfma_f32_16x16x32_bf16 v[206:209], a[52:55], v[168:171], v[206:209]\n"
      "s_mov_b32 m0, 112640\n"
      "v_permlane16_swap_b32 v184, v185\n"
      "v_mfma_f32_16x16x32_bf16 v[206:209], a[56:59], v[200:203], v[206:209]\n"
      "s_add_u32 s73, s73, 1024\n"
      "v_mul_f32 v157, v185, v157\n"
      "v_mul_f32 v100, v184, v100\n"
      "v_mfma_f32_16x16x32_bf16 v[126:129], a[76:79], v[168:171], v[126:129]\n"
      "buffer_load_dwordx4 v198, s[28:31], s73 offen  lds\n"
      "v_mfma_f32_16x16x32_bf16 v[126:129], a[80:83], v[200:203], v[126:129]\n"
      "s_and_b32 s77, s77, 15\n"
      "v_mul_f32 v151, v184, v151\n"
      "v_mul_f32 v136, v185, v136\n"
      "v_mfma_f32_16x16x32_bf16 v[106:109], a[100:103], v[168:171], v[106:109]\n"
      "v_mul_f32 v148, v184, v148\n"
      "v_mul_f32 v103, v184, v103\n"
      "v_mfma_f32_16x16x32_bf16 v[106:109], a[104:107], v[200:203], v[106:109]\n"
      "s_waitcnt lgkmcnt(0)\n"
      "v_mul_f32 v118, v186, v118\n"
      "v_mul_f32 v149, v184, v149\n"
      "v_mfma_f32_16x16x32_bf16 v[180:183], a[124:127], v[168:171], v[180:183]\n"
      "s_mov_b32 m0, 113664\n"
      "v_mul_f32 v150, v184, v150\n"
      "v_cvt_pk_bf16_f32 v148, v148, v149\n"
      "v_mfma_f32_16x16x32_bf16 v[180:183], a[128:131], v[200:203], v[180:183]\n"
      "s_add_u32 s73, s73, 1024\n"
      "v_cvt_pk_bf16_f32 v149, v150, v151\n"
      "v_mul_f32 v101, v184, v101\n"
      "v_mfma_f32_16x16x32_bf16 v[210:213], a[12:15], v[152:155], v[210:213]\n"
      "buffer_load_dwordx4 v198, s[28:31], s73 offen  lds\n"
      "v_mfma_f32_16x16x32_bf16 v[210:213], a[16:19], v[188:191], v[210:213]\n"
      "v_mul_f32 v160, v185, v160\n"
      "v_mul_f32_e64 v156, v185, v156\n"
      "v_mfma_f32_16x16x32_bf16 v[210:213], a[20:23], v[110:113], v[210:213]\n"
      "s_mov_b32 m0, 114688\n"
      "v_mul_f32_e64 v102, v184, v102\n"
      "v_cvt_pk_bf16_f32 v150, v100, v101\n"
      "v_mfma_f32_16x16x32_bf16 v[144:147], a[36:39], v[152:155], v[144:147]\n"
      "s_add_u32 s73, s73, 1024\n"
      "v_cvt_pk_bf16_f32 v151, v102, v103\n"
      "v_mul_f32 v161, v185, v161\n"
      "v_mfma_f32_16x16x32_bf16 v[144:147], a[40:43], v[188:191], v[144:147]\n"
      "v_cndmask_b32 v204, v204, v176, s[58:59]\n"
      "v_cvt_pk_bf16_f32 v156, v156, v157\n"
      "v_mfma_f32_16x16x32_bf16 v[144:147], a[44:47], v[110:113], v[144:147]\n"
      "s_mov_b32 exec_hi, 0xffff\n"
      "buffer_load_dwordx4 v198, s[28:31], s73 offen  lds\n"
      "s_mov_b32 exec_hi, -1\n"
      "v_pk_mul_f32 v[116:117], v[116:117], v[186:187] op_sel_hi:[1,0]\n"
      "v_pk_mul_f32 v[142:143], v[142:143], v[186:187] op_sel_hi:[1,0]\n"
      "v_pk_mul_f32 v[158:159], v[158:159], v[184:185] op_sel:[0,1]\n"
      "v_mfma_f32_16x16x32_bf16 v[206:209], a[60:63], v[152:155], v[206:209]\n"
      "ds_write_b128 v5, v[148:151]\n"
      "v_pk_mul_f32 v[164:165], v[164:165], v[184:185] op_sel_hi:[1,0]\n"
      "v_pk_mul_f32 v[166:167], v[166:167], v[184:185] op_sel_hi:[1,0]\n"
      "v_mfma_f32_16x16x32_bf16 v[206:209], a[64:67], v[188:191], v[206:209]\n"
      "v_cvt_pk_bf16_f32 v164, v164, v165\n"
      "v_mul_f32_e64 v162, v185, v162\n"
      "v_mfma_f32_16x16x32_bf16 v[206:209], a[68:71], v[110:113], v[206:209]\n"
      "s_add_u32 m0, s64, 18432\n"
      "v_cvt_pk_bf16_f32 v165, v166, v167\n"
      "v_cvt_pk_bf16_f32 v157, v158, v159\n"
      "v_mfma_f32_16x16x32_bf16 v[126:129], a[84:87], v[152:155], v[126:129]\n"
      "ds_write_b64 v6, v[164:165]\n"
      "v_mfma_f32_16x16x32_bf16 v[126:129], a[88:91], v[188:191], v[126:129]\n"
      "v_fma_f32 v77, v210, v210, s51\n"
      "v_mul_f32_e64 v138, v185, v138\n"
      "v_mfma_f32_16x16x32_bf16 v[126:129], a[92:95], v[110:113], v[126:129]\n"
      "buffer_load_dwordx4 v16, s[16:19], s45 offen  lds\n"
      "v_mfma_f32_16x16x32_bf16 v[106:109], a[108:111], v[152:155], v[106:109]\n"
      "s_or_b32 s95, s84, s85\n"
      "v_cvt_pk_bf16_f32 v158, v160, v161\n"
      "v_mul_f32 v193, v186, v193\n"
      "v_mfma_f32_16x16x32_bf16 v[106:109], a[112:115], v[188:191], v[106:109]\n"
      "s_add_u32 m0, s64, 22528\n"
      "v_mul_f32 v139, v185, v139\n"
      "v_fma_f32 v81, v126, v126, s51\n"
      "v_mfma_f32_16x16x32_bf16 v[106:109], a[116:119], v[110:113], v[106:109]\n"
      "v_mul_f32 v163, v185, v163\n"
      "v_mul_f32_e64 v137, v185, v137\n"
      "v_mfma_f32_16x16x32_bf16 v[180:183], a[132:135], v[152:155], v[180:183]\n"
      "ds_read_b128 v[164:167], v0 offset:14336\n"
      "v_cvt_pk_bf16_f32 v159, v162, v163\n"
      "v_mfma_f32_16x16x32_bf16 v[180:183], a[136:139], v[188:191], v[180:183]\n"
      "buffer_load_dwordx4 v17, s[16:19], s45 offen  lds\n"
      "v_mfma_f32_16x16x32_bf16 v[180:183], a[140:143], v[110:113], v[180:183]\n"
      "ds_read_b128 v[220:223], v0 offset:10240\n"
      "v_mul_f32 v119, v186, v119\n"
      "v_mfma_f32_16x16x32_bf16 v[196:199], v[120:123], a[144:147], 0\n"
      "s_add_u32 m0, s64, 26624\n"
      "v_cvt_pk_bf16_f32 v136, v136, v137\n"
      "v_mul_f32 v192, v186, v192\n"
      "v_mfma_f32_16x16x32_bf16 v[196:199], v[168:171], a[148:151], v[196:199]\n"
      "ds_write_b128 v5, v[156:159] offset:6144\n"
      "v_mfma_f32_16x16x32_bf16 v[196:199], v[200:203], a[152:155], v[196:199]\n"
      "buffer_load_dwordx4 v18, s[16:19], s45 offen  lds\n"
      "v_fmac_f32_e64 v81, v127, v127\n"
      "v_mfma_f32_16x16x32_bf16 v[196:199], v[152:155], a[156:159], v[196:199]\n"
      "ds_read_b128 v[160:163], v0 offset:2048\n"
      "v_cvt_pk_bf16_f32 v137, v138, v139\n"
      "v_mfma_f32_16x16x32_bf16 v[196:199], v[188:191], a[160:163], v[196:199]\n"
      "s_add_u32 m0, s64, 30720\n"
      "v_mul_f32 v141, v186, v141\n"
      "v_mov_b32 v105, 0\n"
      "v_fmac_f32_e64 v77, v211, v211\n"
      "v_mfma_f32_16x16x32_bf16 v[196:199], v[110:113], a[164:167], v[196:199]\n"
      "ds_write_b64 v6, v[136:137] offset:6144\n"
      "v_mfma_f32_16x16x32_bf16 v[214:217], v[120:123], a[168:171], 0\n"
      "buffer_load_dwordx4 v19, s[16:19], s45 offen  lds\n"
      "v_mfma_f32_16x16x32_bf16 v[214:217], v[168:171], a[172:175], v[214:217]\n"
      "v_cvt_pk_bf16_f32 v116, v116, v117\n"
      "v_mul_f32_e64 v194, v186, v194\n"
      "v_mfma_f32_16x16x32_bf16 v[214:217], v[200:203], a[176:179], v[214:217]\n"
      "s_add_u32 m0, s65, 34816\n"
      "v_fmac_f32 v77, v212, v212\n"
      "v_mul_f32 v140, v186, v140\n"
      "v_mfma_f32_16x16x32_bf16 v[214:217], v[152:155], a[180:183], v[214:217]\n"
      "v_fmac_f32 v77, v213, v213\n"
      "v_fmac_f32 v81, v128, v128\n"
      "v_mfma_f32_16x16x32_bf16 v[214:217], v[188:191], a[184:187], v[214:217]\n"
      "ds_read_b128 v[156:159], v0 offset:8192\n"
      "v_cvt_pk_bf16_f32 v117, v118, v119\n"
      "v_mfma_f32_16x16x32_bf16 v[214:217], v[110:113], a[188:191], v[214:217]\n"
      "buffer_load_dwordx4 v15, s[16:19], s45 offen  lds\n"
      "v_mfma_f32_16x16x32_bf16 v[130:133], v[120:123], a[192:195], 0\n"
      "s_or_b32 s95, s95, s48\n"
      "v_cvt_pk_bf16_f32 v119, v194, v195\n"
      "v_cvt_pk_bf16_f32 v118, v192, v193\n"
      "v_mfma_f32_16x16x32_bf16 v[130:133], v[168:171], a[196:199], v[130:133]\n"
      "s_waitcnt lgkmcnt(1)\n"
      "v_fmac_f32 v81, v129, v129\n"
      "v_fmac_f32 v77, v144, v144\n"
      "v_mfma_f32_16x16x32_bf16 v[130:133], v[200:203], a[200:203], v[130:133]\n"
      "ds_read_b128 v[192:195], v1 offset:8192\n"
      "v_fmac_f32_e64 v77, v145, v145\n"
      "v_mfma_f32_16x16x32_bf16 v[130:133], v[152:155], a[204:207], v[130:133]\n"
      "ds_read_b128 v[184:187], v1 offset:14336\n"
      "v_cvt_pk_bf16_f32 v214, v214, v215\n"
      "v_mfma_f32_16x16x32_bf16 v[130:133], v[188:191], a[208:211], v[130:133]\n"
      "buffer_load_dwordx4 v[176:179], v36, s[28:31], s47 offen\n"
      "v_mfma_f32_16x16x32_bf16 v[130:133], v[110:113], a[212:215], v[130:133]\n"
      "ds_write_b128 v5, v[116:119] offset:12288\n"
      "v_mfma_f32_16x16x32_bf16 v[188:191], a[0:3], v[160:163], 0\n"
      "s_add_u32 s73, s73, 1024\n"
      "v_fmac_f32 v77, v146, v146\n"
      "v_fmac_f32 v81, v106, v106\n"
      "v_mfma_f32_16x16x32_bf16 v[188:191], a[4:7], v[172:175], v[188:191]\n"
      "s_waitcnt lgkmcnt(2)\n"
      "v_fmac_f32 v77, v147, v147\n"
      "v_fmac_f32_e64 v77, v206, v206\n"
      "v_mfma_f32_16x16x32_bf16 v[188:191], a[8:11], v[156:159], v[188:191]\n"
      "buffer_load_dword v78, v29, s[28:31], s47 offen\n"
      "v_mfma_f32_16x16x32_bf16 v[188:191], a[12:15], v[192:195], v[188:191]\n"
      "v_cvt_pk_bf16_f32 v140, v140, v141\n"
      "v_cvt_pk_bf16_f32 v196, v196, v197\n"
      "v_mfma_f32_16x16x32_bf16 v[188:191], a[16:19], v[164:167], v[188:191]\n"
      "s_waitcnt lgkmcnt(1)\n"
      "v_fmac_f32 v77, v207, v207\n"
      "v_cndmask_b32 v204, v204, v96, s[60:61]\n"
      "v_mfma_f32_16x16x32_bf16 v[188:191], a[20:23], v[184:187], v[188:191]\n"
      "v_cvt_pk_bf16_f32 v215, v216, v217\n"
      "v_cvt_pk_bf16_f32 v197, v198, v199\n"
      "v_mfma_f32_16x16x32_bf16 v[116:119], a[24:27], v[160:163], 0\n"
      "v_fmac_f32_e64 v77, v208, v208\n"
      "v_subrev_u32 v217, 115456, v9\n"
      "v_mfma_f32_16x16x32_bf16 v[116:119], a[28:31], v[172:175], v[116:119]\n"
      "v_cvt_pk_bf16_f32 v141, v142, v143\n"
      "v_fmac_f32_e64 v81, v107, v107\n"
      "v_mfma_f32_16x16x32_bf16 v[116:119], a[32:35], v[156:159], v[116:119]\n"
      "v_fma_f32 v76, v188, v188, s51\n"
      "v_bfe_u32 v80, v217, 4, 4\n"
      "v_mfma_f32_16x16x32_bf16 v[116:119], a[36:39], v[192:195], v[116:119]\n"
      "v_fmac_f32 v81, v108, v108\n"
      "v_mul_u32_u24 v89, 98, v80\n"
      "v_mfma_f32_16x16x32_bf16 v[116:119], a[40:43], v[164:167], v[116:119]\n"
      "v_lshrrev_b32 v217, 8, v217\n"
      "v_fmac_f32_e64 v76, v189, v189\n"
      "v_mfma_f32_16x16x32_bf16 v[116:119], a[44:47], v[184:187], v[116:119]\n"
      "s_cmp_ge_u32 s95, 0x40000000\n"
      "s_cselect_b32 s94, -1, -2\n"
      "v_fmac_f32 v81, v109, v109\n"
      "v_mfma_f32_16x16x32_bf16 v[120:123], a[48:51], v[160:163], 0\n"
      "v_fmac_f32 v81, v180, v180\n"
      "v_fmac_f32 v81, v181, v181\n"
      "v_mfma_f32_16x16x32_bf16 v[120:123], a[52:55], v[172:175], v[120:123]\n"
      "v_fmac_f32 v77, v209, v209\n"
      "v_fmac_f32_e64 v81, v182, v182\n"
      "v_mfma_f32_16x16x32_bf16 v[120:123], a[56:59], v[156:159], v[120:123]\n"
      "v_cvt_pk_bf16_f32 v130, v130, v131\n"
      "v_fmac_f32 v76, v190, v190\n"
      "v_mfma_f32_16x16x32_bf16 v[120:123], a[60:63], v[192:195], v[120:123]\n"
      "s_cmp_gt_u32 s42, s44\n"
      "s_cselect_b32 s95, s94, s95\n"
      "v_fmac_f32 v81, v183, v183\n"
      "v_mfma_f32_16x16x32_bf16 v[120:123], a[64:67], v[164:167], v[120:123]\n"
      "ds_read_b128 v[198:201], v9 offset:5632\n"
      "v_cvt_pk_bf16_f32 v131, v132, v133\n"
      "v_mfma_f32_16x16x32_bf16 v[120:123], a[68:71], v[184:187], v[120:123]\n"
      "v_permlane16_swap_b32 v77, v81\n"
      "v_mfma_f32_16x16x32_bf16 v[168:171], a[72:75], v[160:163], 0\n"
      "v_add_f32 v77, v77, v81\n"
      "v_mov_b32 v81, v77\n"
      "v_mfma_f32_16x16x32_bf16 v[168:171], a[76:79], v[172:175], v[168:171]\n"
      "s_nop 0\n"
      "v_permlane32_swap_b32 v77, v81\n"
      "v_mfma_f32_16x16x32_bf16 v[168:171], a[80:83], v[156:159], v[168:171]\n"
      "v_fmac_f32 v76, v191, v191\n"
      "v_add_f32 v77, v77, v81\n"
      "v_mfma_f32_16x16x32_bf16 v[168:171], a[84:87], v[192:195], v[168:171]\n"
      "v_rsq_f32 v77, v77\n"
      "v_mfma_f32_16x16x32_bf16 v[168:171], a[88:91], v[164:167], v[168:171]\n"
      "v_fmac_f32 v76, v116, v116\n"
      "v_mul_f32 v204, v218, v204\n"
      "v_mfma_f32_16x16x32_bf16 v[168:171], a[92:95], v[184:187], v[168:171]\n"
      "v_mov_b32 v81, v77\n"
      "v_fmac_f32 v76, v117, v117\n"
      "v_mfma_f32_16x16x32_bf16 v[136:139], a[96:99], v[160:163], 0\n"
      "v_lshl_add_u32 v89, v217, 3, v89\n"
      "v_lshlrev_b32 v217, 2, v217\n"
      "v_mfma_f32_16x16x32_bf16 v[136:139], a[100:103], v[172:175], v[136:139]\n"
      "v_permlane16_swap_b32 v77, v81\n"
      "v_mfma_f32_16x16x32_bf16 v[136:139], a[104:107], v[156:159], v[136:139]\n"
      "v_mul_f32 v77, s50, v77\n"
      "v_fmac_f32 v76, v118, v118\n"
      "v_mfma_f32_16x16x32_bf16 v[136:139], a[108:111], v[192:195], v[136:139]\n"
      "v_mul_f32 v180, v81, v180\n"
      "v_mul_f32 v208, v77, v208\n"
      "v_mfma_f32_16x16x32_bf16 v[136:139], a[112:115], v[164:167], v[136:139]\n"
      "v_mul_f32 v128, v81, v128\n"
      "v_mul_f32 v212, v77, v212\n"
      "v_mfma_f32_16x16x32_bf16 v[136:139], a[116:119], v[184:187], v[136:139]\n"
      "v_fma_f32 v133, v168, v168, s51\n"
      "v_mul_f32 v108, v81, v108\n"
      "v_mfma_f32_16x16x32_bf16 v[110:113], a[120:123], v[160:163], 0\n"
      "v_fmac_f32 v133, v169, v169\n"
      "v_sub_u32_e64 v80, v80, v217\n"
      "v_mfma_f32_16x16x32_bf16 v[110:113], a[124:127], v[172:175], v[110:113]\n"
      "ds_read_b128 v[100:103], v1 offset:16384\n"
      "v_fmac_f32 v76, v119, v119\n"
      "v_mfma_f32_16x16x32_bf16 v[110:113], a[128:131], v[156:159], v[110:113]\n"
      "s_waitcnt lgkmcnt(1)\n"
      "v_fmac_f32_e64 v133, v170, v170\n"
      "v_or3_b32 v198, v198, v199, v200\n"
      "v_mfma_f32_16x16x32_bf16 v[110:113], a[132:135], v[192:195], v[110:113]\n"
      "v_mul_f32 v109, v81, v109\n"
      "v_mul_f32 v206, v77, v206\n"
      "v_mfma_f32_16x16x32_bf16 v[110:113], a[136:139], v[164:167], v[110:113]\n"
      "v_mul_f32 v146, v77, v146\n"
      "v_mul_f32 v209, v77, v209\n"
      "v_mfma_f32_16x16x32_bf16 v[110:113], a[140:143], v[184:187], v[110:113]\n"
      "ds_write_b64 v6, v[140:141] offset:12288\n"
      "v_mfma_f32_16x16x32_bf16 v[216:219], v[160:163], a[144:147], 0\n"
      "v_fmac_f32 v133, v171, v171\n"
      "v_cvt_pk_bf16_f32 v204, v204, v204\n"
      "v_mfma_f32_16x16x32_bf16 v[216:219], v[172:175], a[148:151], v[216:219]\n"
      "ds_read_b128 v[152:155], v1 offset:4096\n"
      "v_fmac_f32 v133, v136, v136\n"
      "v_mfma_f32_16x16x32_bf16 v[216:219], v[156:159], a[152:155], v[216:219]\n"
      "ds_write_b16 v12, v204 offset:16\n"
      "v_mfma_f32_16x16x32_bf16 v[216:219], v[192:195], a[156:159], v[216:219]\n"
      "v_mul_f32 v127, v81, v127\n"
      "v_mul_f32 v144, v77, v144\n"
      "v_mfma_f32_16x16x32_bf16 v[216:219], v[164:167], a[160:163], v[216:219]\n"
      "v_fmac_f32 v76, v120, v120\n"
      "v_fmac_f32 v133, v137, v137\n"
      "v_mfma_f32_16x16x32_bf16 v[216:219], v[184:187], a[164:167], v[216:219]\n"
      "v_fmac_f32 v133, v138, v138\n"
      "v_mul_f32 v183, v81, v183\n"
      "v_mfma_f32_16x16x32_bf16 v[148:151], v[160:163], a[168:171], 0\n"
      "v_mul_f32 v181, v81, v181\n"
      "v_bitop3_b32 v198, v198, v201, s33 bitop3:0xa8\n"
      "v_mfma_f32_16x16x32_bf16 v[148:151], v[172:175], a[172:175], v[148:151]\n"
      "v_mul_f32 v210, v77, v210\n"
      "v_fmac_f32 v133, v139, v139\n"
      "v_mfma_f32_16x16x32_bf16 v[148:151], v[156:159], a[176:179], v[148:151]\n"
      "ds_read_b128 v[202:205], v1 offset:10240\n"
      "v_add_u32 v89, s77, v89\n"
      "v_mfma_f32_16x16x32_bf16 v[148:151], v[192:195], a[180:183], v[148:151]\n"
      "v_fmac_f32 v133, v110, v110\n"
      "v_cmp_ne_u32 s[56:57], 0, v198\n"
      "v_mfma_f32_16x16x32_bf16 v[148:151], v[164:167], a[184:187], v[148:151]\n"
      "v_mul_f32 v129, v81, v129\n"
      "v_mul_f32_e64 v107, v81, v107\n"
      "v_mfma_f32_16x16x32_bf16 v[148:151], v[184:187], a[188:191], v[148:151]\n"
      "ds_read_b128 v[140:143], v0 offset:4096\n"
      "v_mul_f32 v145, v77, v145\n"
      "v_mfma_f32_16x16x32_bf16 v[198:201], v[160:163], a[192:195], 0\n"
      "v_add_u32 v12, 32, v12\n"
      "v_fmac_f32 v76, v121, v121\n"
      "v_mfma_f32_16x16x32_bf16 v[198:201], v[172:175], a[196:199], v[198:201]\n"
      "v_fmac_f32 v133, v111, v111\n"
      "v_fmac_f32 v76, v122, v122\n"
      "v_mfma_f32_16x16x32_bf16 v[198:201], v[156:159], a[200:203], v[198:201]\n"
      "v_fmac_f32 v133, v112, v112\n"
      "v_mul_f32 v126, v81, v126\n"
      "v_mfma_f32_16x16x32_bf16 v[198:201], v[192:195], a[204:207], v[198:201]\n"
      "ds_read_b128 v[156:159], v0 offset:16384\n"
      "v_mul_f32 v147, v77, v147\n"
      "v_mfma_f32_16x16x32_bf16 v[198:201], v[164:167], a[208:211], v[198:201]\n"
      "s_add_u32 s43, s43, 1\n"
      "v_fmac_f32 v76, v123, v123\n"
      "v_mul_f32 v211, v77, v211\n"
      "v_mfma_f32_16x16x32_bf16 v[198:201], v[184:187], a[212:215], v[198:201]\n"
      "s_waitcnt lgkmcnt(1)\n"
      "v_cvt_pk_bf16_f32 v210, v210, v211\n"
      "v_cvt_pk_bf16_f32 v126, v126, v127\n"
      "v_mfma_f32_16x16x32_bf16 v[96:99], a[0:3], v[140:143], 0\n"
      "v_cvt_pk_bf16_f32 v127, v128, v129\n"
      "v_fmac_f32_e64 v133, v113, v113\n"
      "v_mfma_f32_16x16x32_bf16 v[96:99], a[4:7], v[152:155], v[96:99]\n"
      "s_nop 0\n"
      "v_permlane16_swap_b32 v76, v133\n"
      "v_mfma_f32_16x16x32_bf16 v[96:99], a[8:11], v[220:223], v[96:99]\n"
      "v_lshlrev_b32 v80, 4, v80\n"
      "v_add_f32 v76, v76, v133\n"
      "v_mfma_f32_16x16x32_bf16 v[96:99], a[12:15], v[202:205], v[96:99]\n"
      "s_waitcnt lgkmcnt(0)\n"
      "v_mul_f32 v106, v81, v106\n"
      "v_mov_b32 v133, v76\n"
      "v_mfma_f32_16x16x32_bf16 v[96:99], a[16:19], v[156:159], v[96:99]\n"
      "s_nop 0\n"
      "v_permlane32_swap_b32_e64 v76, v133\n"
      "v_mfma_f32_16x16x32_bf16 v[96:99], a[20:23], v[100:103], v[96:99]\n"
      "v_cvt_pk_bf16_f32 v132, v198, v199\n"
      "v_cvt_pk_bf16_f32 v198, v216, v217\n"
      "v_mfma_f32_16x16x32_bf16 v[160:163], a[24:27], v[140:143], 0\n"
      "v_cvt_pk_bf16_f32 v180, v180, v181\n"
      "v_add_f32 v76, v76, v133\n"
      "v_mfma_f32_16x16x32_bf16 v[160:163], a[28:31], v[152:155], v[160:163]\n"
      "v_rsq_f32 v76, v76\n"
      "v_mfma_f32_16x16x32_bf16 v[160:163], a[32:35], v[220:223], v[160:163]\n"
      "v_mul_f32 v213, v77, v213\n"
      "v_fma_f32 v186, v96, v96, s51\n"
      "v_mfma_f32_16x16x32_bf16 v[160:163], a[36:39], v[202:205], v[160:163]\n"
      "s_waitcnt vmcnt(7)\n"
      "v_mov_b32 v133, v76\n"
      "v_cvt_pk_bf16_f32 v129, v108, v109\n"
      "v_mfma_f32_16x16x32_bf16 v[160:163], a[40:43], v[156:159], v[160:163]\n"
      "v_permlane16_swap_b32 v76, v133\n"
      "v_mfma_f32_16x16x32_bf16 v[160:163], a[44:47], v[100:103], v[160:163]\n"
      "v_and_b32 v92, 2, v89\n"
      "v_mul_f32 v136, v133, v136\n"
      "v_mfma_f32_16x16x32_bf16 v[192:195], a[48:51], v[140:143], 0\n"
      "v_cvt_pk_bf16_f32 v216, v148, v149\n"
      "v_mul_f32_e64 v182, v81, v182\n"
      "v_mfma_f32_16x16x32_bf16 v[192:195], a[52:55], v[152:155], v[192:195]\n"
      "v_fmac_f32 v186, v97, v97\n"
      "v_fmac_f32 v186, v98, v98\n"
      "v_mfma_f32_16x16x32_bf16 v[192:195], a[56:59], v[220:223], v[192:195]\n"
      "v_mul_f32 v137, v133, v137\n"
      "v_mul_f32 v76, s50, v76\n"
      "v_mfma_f32_16x16x32_bf16 v[192:195], a[60:63], v[202:205], v[192:195]\n"
      "v_cvt_pk_bf16_f32 v128, v106, v107\n"
      "v_mul_f32 v189, v76, v189\n"
      "v_mfma_f32_16x16x32_bf16 v[192:195], a[64:67], v[156:159], v[192:195]\n"
      "v_mul_f32 v169, v133, v169\n"
      "v_cvt_pk_bf16_f32 v211, v212, v213\n"
      "v_mfma_f32_16x16x32_bf16 v[192:195], a[68:71], v[100:103], v[192:195]\n"
      "v_mul_f32_e64 v188, v76, v188\n"
      "v_cvt_pk_bf16_f32 v188, v188, v189\n"
      "v_mfma_f32_16x16x32_bf16 v[106:109], a[72:75], v[140:143], 0\n"
      "v_mul_f32_e64 v116, v76, v116\n"
      "v_cvt_pk_bf16_f32 v199, v218, v219\n"
      "v_mfma_f32_16x16x32_bf16 v[106:109], a[76:79], v[152:155], v[106:109]\n"
      "v_cvt_pk_bf16_f32 v217, v150, v151\n"
      "v_mul_f32 v110, v133, v110\n"
      "v_mfma_f32_16x16x32_bf16 v[106:109], a[80:83], v[220:223], v[106:109]\n"
      "v_mul_f32 v111, v133, v111\n"
      "v_fmac_f32_e64 v186, v99, v99\n"
      "v_mfma_f32_16x16x32_bf16 v[106:109], a[84:87], v[202:205], v[106:109]\n"
      "v_mul_f32_e64 v207, v77, v207\n"
      "v_cvt_pk_bf16_f32 v206, v206, v207\n"
      "v_mfma_f32_16x16x32_bf16 v[106:109], a[88:91], v[156:159], v[106:109]\n"
      "v_cvt_pk_bf16_f32 v110, v110, v111\n"
      "v_and_b32 v89, -4, v89\n"
      "v_mfma_f32_16x16x32_bf16 v[106:109], a[92:95], v[100:103], v[106:109]\n"
      "v_mul_f32 v117, v76, v117\n"
      "v_fmac_f32_e64 v186, v160, v160\n"
      "v_mfma_f32_16x16x32_bf16 v[164:167], a[96:99], v[140:143], 0\n"
      "v_cvt_pk_bf16_f32 v181, v182, v183\n"
      "v_mul_f32_e64 v168, v133, v168\n"
      "v_mfma_f32_16x16x32_bf16 v[164:167], a[100:103], v[152:155], v[164:167]\n"
      "v_cmp_gt_u32 s[72:73], 64, v80\n"
      "v_fmac_f32 v186, v161, v161\n"
      "v_mfma_f32_16x16x32_bf16 v[164:167], a[104:107], v[220:223], v[164:167]\n"
      "v_fma_f32 v183, v106, v106, s51\n"
      "v_fmac_f32 v183, v107, v107\n"
      "v_fmac_f32 v186, v162, v162\n"
      "v_fmac_f32 v186, v163, v163\n"
      "v_cvt_pk_bf16_f32 v168, v168, v169\n"
      "v_pk_mul_f32 v[122:123], v[122:123], v[76:77] op_sel_hi:[1,0]\n"
      "v_pk_mul_f32 v[138:139], v[138:139], v[132:133] op_sel:[0,1]\n"
      "v_pk_mul_f32 v[190:191], v[190:191], v[76:77] op_sel_hi:[1,0]\n"
      "v_pk_mul_f32 v[112:113], v[112:113], v[132:133] op_sel:[0,1]\n"
      "v_pk_mul_f32 v[118:119], v[118:119], v[76:77] op_sel_hi:[1,0]\n"
      "v_pk_mul_f32 v[120:121], v[120:121], v[76:77] op_sel_hi:[1,0]\n"
      "v_pk_mul_f32 v[170:171], v[170:171], v[132:133] op_sel:[0,1]\n"
      "v_mfma_f32_16x16x32_bf16 v[164:167], a[108:111], v[202:205], v[164:167]\n"
      "v_cvt_pk_bf16_f32 v169, v170, v171\n"
      "v_fmac_f32_e64 v183, v108, v108\n"
      "v_mfma_f32_16x16x32_bf16 v[164:167], a[112:115], v[156:159], v[164:167]\n"
      "v_cvt_pk_bf16_f32 v207, v208, v209\n"
      "v_fmac_f32 v186, v192, v192\n"
      "v_mfma_f32_16x16x32_bf16 v[164:167], a[116:119], v[100:103], v[164:167]\n"
      "s_add_u32 s42, s42, 1\n"
      "v_cvt_pk_bf16_f32 v111, v112, v113\n"
      "v_cvt_pk_bf16_f32 v189, v190, v191\n"
      "v_mfma_f32_16x16x32_bf16 v[172:175], a[120:123], v[140:143], 0\n"
      "v_fmac_f32 v186, v193, v193\n"
      "v_fmac_f32 v183, v109, v109\n"
      "v_mfma_f32_16x16x32_bf16 v[172:175], a[124:127], v[152:155], v[172:175]\n"
      "v_cvt_pk_bf16_f32 v120, v120, v121\n"
      "v_fmac_f32 v186, v194, v194\n"
      "v_mfma_f32_16x16x32_bf16 v[172:175], a[128:131], v[220:223], v[172:175]\n"
      "v_fmac_f32 v183, v164, v164\n"
      "v_fmac_f32_e64 v183, v165, v165\n"
      "v_mfma_f32_16x16x32_bf16 v[172:175], a[132:135], v[202:205], v[172:175]\n"
      "v_cvt_pk_bf16_f32 v212, v144, v145\n"
      "v_cvt_pk_bf16_f32 v170, v136, v137\n"
      "v_mfma_f32_16x16x32_bf16 v[172:175], a[136:139], v[156:159], v[172:175]\n"
      "v_cvt_pk_bf16_f32 v190, v116, v117\n"
      "v_fmac_f32_e64 v183, v166, v166\n"
      "v_mfma_f32_16x16x32_bf16 v[172:175], a[140:143], v[100:103], v[172:175]\n"
      "v_cvt_pk_bf16_f32 v213, v146, v147\n"
      "v_fmac_f32 v186, v195, v195\n"
      "v_mfma_f32_16x16x32_bf16 v[148:151], v[140:143], a[144:147], 0\n"
      "s_cmp_ge_u32 s95, 0x40000000\n"
      "s_cselect_b64 vcc, -1, s[56:57]\n"
      "v_fmac_f32 v183, v167, v167\n"
      "v_mfma_f32_16x16x32_bf16 v[148:151], v[152:155], a[148:151], v[148:151]\n"
      "s_nop 0\n"
      "v_fmac_f32 v183, v172, v172\n"
      "v_fmac_f32 v183, v173, v173\n"
      "v_mfma_f32_16x16x32_bf16 v[148:151], v[220:223], a[152:155], v[148:151]\n"
      "v_fmac_f32 v183, v174, v174\n"
      "v_fmac_f32 v183, v175, v175\n"
      "v_mfma_f32_16x16x32_bf16 v[148:151], v[202:205], a[156:159], v[148:151]\n"
      "s_nop 0\n"
      "v_permlane16_swap_b32 v186, v183\n"
      "v_mfma_f32_16x16x32_bf16 v[148:151], v[156:159], a[160:163], v[148:151]\n"
      "v_add_f32 v186, v186, v183\n"
      "v_mov_b32 v183, v186\n"
      "v_mfma_f32_16x16x32_bf16 v[148:151], v[100:103], a[164:167], v[148:151]\n"
      "s_nop 0\n"
      "v_permlane32_swap_b32 v186, v183\n"
      "v_add_f32 v186, v186, v183\n"
      "v_mfma_f32_16x16x32_bf16 v[112:115], v[140:143], a[168:171], 0\n"
      "v_rsq_f32 v186, v186\n"
      "v_mfma_f32_16x16x32_bf16 v[112:115], v[152:155], a[172:175], v[112:115]\n"
      "v_cvt_pk_bf16_f32 v191, v118, v119\n"
      "v_mov_b32 v183, v186\n"
      "v_mfma_f32_16x16x32_bf16 v[112:115], v[220:223], a[176:179], v[112:115]\n"
      "s_nop 0\n"
      "v_permlane16_swap_b32 v186, v183\n"
      "v_mfma_f32_16x16x32_bf16 v[112:115], v[202:205], a[180:183], v[112:115]\n"
      "ds_read_b128 v[134:137], v7 offset:18432\n"
      "v_cvt_pk_bf16_f32 v121, v122, v123\n"
      "v_lshlrev_b64 v[104:105], v80, v[104:105]\n"
      "v_cvt_pk_bf16_f32 v77, v150, v151\n"
      "v_mul_f32_e64 v186, s50, v186\n"
      "v_pk_mul_f32 v[174:175], v[174:175], v[182:183] op_sel:[0,1]\n"
      "v_pk_mul_f32 v[172:173], v[172:173], v[182:183] op_sel:[0,1]\n"
      "v_pk_mul_f32 v[96:97], v[96:97], v[186:187] op_sel_hi:[1,0]\n"
      "v_pk_mul_f32 v[108:109], v[108:109], v[182:183] op_sel:[0,1]\n"
      "v_pk_mul_f32 v[166:167], v[166:167], v[182:183] op_sel:[0,1]\n"
      "v_pk_mul_f32 v[192:193], v[192:193], v[186:187] op_sel_hi:[1,0]\n"
      "v_pk_mul_f32 v[164:165], v[164:165], v[182:183] op_sel:[0,1]\n"
      "v_pk_mul_f32 v[106:107], v[106:107], v[182:183] op_sel:[0,1]\n"
      "v_pk_mul_f32 v[194:195], v[194:195], v[186:187] op_sel_hi:[1,0]\n"
      "v_mfma_f32_16x16x32_bf16 v[112:115], v[156:159], a[184:187], v[112:115]\n"
      "ds_read_b128 v[116:119], v2 offset:18432\n"
      "v_cvt_pk_bf16_f32 v106, v106, v107\n"
      "v_mfma_f32_16x16x32_bf16 v[112:115], v[100:103], a[188:191], v[112:115]\n"
      "ds_read_b128 v[122:125], v2 offset:21504\n"
      "v_add_u32 v218, 110592, v89\n"
      "v_mfma_f32_16x16x32_bf16 v[182:185], v[140:143], a[192:195], 0\n"
      "ds_read_b128 v[144:147], v2 offset:24576\n"
      "v_cvt_pk_bf16_f32 v107, v108, v109\n"
      "v_mfma_f32_16x16x32_bf16 v[182:185], v[152:155], a[196:199], v[182:185]\n"
      "ds_read_b128 v[152:155], v2 offset:30720\n"
      "v_cvt_pk_bf16_f32 v171, v138, v139\n"
      "v_mfma_f32_16x16x32_bf16 v[182:185], v[220:223], a[200:203], v[182:185]\n"
      "ds_read_b128 v[138:141], v2 offset:27648\n"
      "v_cvt_pk_bf16_f32 v192, v192, v193\n"
      "v_mfma_f32_16x16x32_bf16 v[182:185], v[202:205], a[204:207], v[182:185]\n"
      "ds_read_b128 v[202:205], v2 offset:33792\n"
      "v_cvt_pk_bf16_f32 v81, v114, v115\n"
      "v_mfma_f32_16x16x32_bf16 v[182:185], v[156:159], a[208:211], v[182:185]\n"
      "ds_read_b128 v[156:159], v7 offset:22528\n"
      "v_cvt_pk_bf16_f32 v109, v166, v167\n"
      "v_mfma_f32_16x16x32_bf16 v[182:185], v[100:103], a[212:215], v[182:185]\n"
      "s_waitcnt lgkmcnt(5)\n"
      "v_cvt_pk_bf16_f32 v76, v148, v149\n"
      "v_mov_b32 v223, 0\n"
      "v_mfma_f32_16x16x32_bf16 v[148:151], a[216:219], v[116:119], 0\n"
      "buffer_store_dwordx4 v[134:137], v20, s[24:27], s46 offen sc1\n"
      "v_mfma_f32_16x16x32_bf16 v[148:151], a[220:223], v[122:125], v[148:151]\n"
      "s_waitcnt lgkmcnt(2)\n"
      "v_mul_f32 v99, v186, v99\n"
      "v_mul_f32 v162, v186, v162\n"
      "v_mfma_f32_16x16x32_bf16 v[148:151], a[224:227], v[144:147], v[148:151]\n"
      "v_cvt_pk_bf16_f32 v172, v172, v173\n"
      "v_cvt_pk_bf16_f32 v173, v174, v175\n"
      "v_mfma_f32_16x16x32_bf16 v[100:103], a[240:243], v[116:119], 0\n"
      "ds_read_b64 v[134:135], v10 offset:24\n"
      "v_cndmask_b32_e64 v104, 0, v104, s[72:73]\n"
      "v_mfma_f32_16x16x32_bf16 v[100:103], a[244:247], v[122:125], v[100:103]\n"
      "s_waitcnt lgkmcnt(1)\n"
      "v_cndmask_b32_e64 v105, 0, v105, s[72:73]\n"
      "v_cvt_pk_bf16_f32 v108, v164, v165\n"
      "v_mfma_f32_16x16x32_bf16 v[100:103], a[248:251], v[144:147], v[100:103]\n"
      "ds_read2_b32 v[136:137], v218 offset0:24 offset1:18\n"
      "v_mov_b32 v222, 0xbb2b\n"
      "v_mfma_f32_16x16x32_bf16 v[164:167], v[232:235], v[116:119], 0\n"
      "ds_read2_b32 v[208:209], v218 offset0:2 offset1:10\n"
      "v_lshlrev_b64 v[222:223], v80, v[222:223]\n"
      "v_mfma_f32_16x16x32_bf16 v[164:167], v[236:239], v[122:125], v[164:167]\n"
      "ds_read_b128 v[116:119], v7 offset:26624\n"
      "v_cvt_pk_bf16_f32 v133, v200, v201\n"
      "v_mfma_f32_16x16x32_bf16 v[164:167], v[240:243], v[144:147], v[164:167]\n"
      "buffer_store_dwordx4 v[156:159], v21, s[24:27], s46 offen sc1\n"
      "v_pk_mul_f32 v[160:161], v[160:161], v[186:187] op_sel_hi:[1,0]\n"
      "v_mfma_f32_16x16x32_bf16 v[148:151], a[228:231], v[138:141], v[148:151]\n"
      "ds_read2_b32 v[174:175], v218 offset0:0 offset1:1\n"
      "v_mul_f32 v163, v186, v163\n"
      "v_mfma_f32_16x16x32_bf16 v[148:151], a[232:235], v[152:155], v[148:151]\n"
      "s_waitcnt lgkmcnt(3)\n"
      "v_mul_f32 v98, v186, v98\n"
      "v_cvt_pk_bf16_f32 v84, v182, v183\n"
      "v_mfma_f32_16x16x32_bf16 v[148:151], a[236:239], v[202:205], v[148:151]\n"
      "ds_read2_b32 v[114:115], v218 offset0:8 offset1:9\n"
      "v_cvt_pk_bf16_f32 v96, v96, v97\n"
      "v_mfma_f32_16x16x32_bf16 v[100:103], a[252:255], v[138:141], v[100:103]\n"
      "ds_read2_b32 v[186:187], v218 offset0:16 offset1:17\n"
      "v_alignbyte_b32 v136, v136, v136, v92\n"
      "v_mfma_f32_16x16x32_bf16 v[100:103], v[224:227], v[152:155], v[100:103]\n"
      "s_waitcnt lgkmcnt(3)\n"
      "v_cvt_pk_bf16_f32 v97, v98, v99\n"
      "v_cvt_pk_bf16_f32 v85, v184, v185\n"
      "v_mfma_f32_16x16x32_bf16 v[100:103], v[228:231], v[202:205], v[100:103]\n"
      "ds_read_b128 v[156:159], v7 offset:30720\n"
      "v_fma_f32 v200, v148, v148, s52\n"
      "v_mfma_f32_16x16x32_bf16 v[164:167], v[244:247], v[138:141], v[164:167]\n"
      "ds_read_b128 v[144:147], v10 offset:32\n"
      "v_mul_f32_e64 v201, v149, v149\n"
      "v_mfma_f32_16x16x32_bf16 v[164:167], v[248:251], v[152:155], v[164:167]\n"
      "buffer_store_dwordx4 v[116:119], v22, s[24:27], s46 offen sc1\n"
      "v_mfma_f32_16x16x32_bf16 v[164:167], v[252:255], v[202:205], v[164:167]\n"
      "v_add_u32 v154, 112160, v89\n"
      "v_cmp_gt_u32 s[72:73], 64, v80\n"
      "v_mfma_f32_16x16x32_bf16 v[122:125], v[126:129], v[210:213], v[40:43]\n"
      "s_waitcnt lgkmcnt(2)\n"
      "v_cvt_pk_bf16_f32 v98, v160, v161\n"
      "v_cvt_pk_bf16_f32 v193, v194, v195\n"
      "v_mfma_f32_16x16x32_bf16 v[140:143], v[168:171], v[210:213], v[44:47]\n"
      "ds_read_b128 v[202:205], v2 offset:21760\n"
      "v_cvt_pk_bf16_f32 v99, v162, v163\n"
      "v_mfma_f32_16x16x32_bf16 v[182:185], v[106:109], v[210:213], v[48:51]\n"
      "s_waitcnt lgkmcnt(1)\n"
      "v_alignbyte_b32 v114, v115, v114, v92\n"
      "v_alignbyte_b32 v174, v175, v174, v92\n"
      "v_mfma_f32_16x16x32_bf16 v[116:119], v[144:147], v[210:213], v[24:27]\n"
      "ds_read_b128 v[160:163], v7 offset:34816\n"
      "v_fmac_f32_e64 v201, v151, v151\n"
      "v_mfma_f32_16x16x16_bf16 v[122:125], v[180:181], v[206:207], v[122:125]\n"
      "v_cvt_pk_bf16_f32 v80, v112, v113\n"
      "v_alignbyte_b32 v175, v208, v175, v92\n"
      "v_cndmask_b32_e64 v222, 0, v222, s[72:73]\n"
      "v_mfma_f32_16x16x16_bf16 v[140:143], v[110:111], v[206:207], v[140:143]\n"
      "ds_read_b128 v[218:221], v2 offset:18688\n"
      "v_alignbyte_b32 v115, v209, v115, v92\n"
      "v_mfma_f32_16x16x16_bf16 v[182:185], v[172:173], v[206:207], v[182:185]\n"
      "buffer_store_dwordx4 v[156:159], v23, s[24:27], s46 offen sc1\n"
      "v_mfma_f32_16x16x16_bf16 v[116:119], v[134:135], v[206:207], v[116:119]\n"
      "v_alignbyte_b32 v186, v187, v186, v92\n"
      "v_subrev_u32 v39, 115456, v9\n"
      "v_cndmask_b32_e64 v223, 0, v223, s[72:73]\n"
      "v_mfma_f32_16x16x16_bf16 v[122:125], v[104:105], v[174:175], v[122:125]\n"
      "v_fmac_f32 v200, v150, v150\n"
      "v_lshrrev_b32 v39, 3, v39\n"
      "v_alignbyte_b32 v187, v137, v187, v92\n"
      "v_mfma_f32_16x16x16_bf16 v[140:143], v[104:105], v[114:115], v[140:143]\n"
      "v_fmac_f32_e64 v200, v100, v100\n"
      "v_fmac_f32_e64 v201, v101, v101\n"
      "v_min_u32 v39, 96, v39\n"
      "v_mfma_f32_16x16x16_bf16 v[182:185], v[104:105], v[186:187], v[182:185]\n"
      "v_add_u32 v93, 113728, v89\n"
      "v_fmac_f32_e64 v201, v103, v103\n"
      "v_fmac_f32 v200, v102, v102\n"
      "v_mfma_f32_16x16x16_bf16 v[116:119], v[104:105], v[136:137], v[116:119]\n"
      "ds_read2_b32 v[112:113], v154 offset0:0 offset1:1\n"
      "v_add_u32_e64 v39, s77, v39\n"
      "v_mfma_f32_16x16x16_bf16 v[122:125], v[222:223], v[174:175], v[122:125]\n"
      "ds_read_b128 v[210:213], v2 offset:24832\n"
      "v_add_u32 v39, 115296, v39\n"
      "v_mfma_f32_16x16x16_bf16 v[140:143], v[222:223], v[114:115], v[140:143]\n"
      "ds_read2_b32 v[174:175], v154 offset0:8 offset1:9\n"
      "ds_read2_b32 v[138:139], v154 offset0:2 offset1:10\n"
      "v_mfma_f32_16x16x16_bf16 v[182:185], v[222:223], v[186:187], v[182:185]\n"
      "s_waitcnt lgkmcnt(4)\n"
      "buffer_store_dwordx4 v[160:163], v25, s[24:27], s46 offen sc1\n"
      "v_pk_fma_f32 v[200:201], v[164:165], v[164:165], v[200:201]\n"
      "v_pk_fma_f32 v[200:201], v[166:167], v[166:167], v[200:201]\n"
      "v_mfma_f32_16x16x16_bf16 v[116:119], v[222:223], v[136:137], v[116:119]\n"
      "v_max3_f32 v115, v122, v123, v124\n"
      "v_max3_f32 v163, v125, v140, v141\n"
      "v_max3_f32 v115, v115, v142, v143\n"
      "v_add_f32_e64 v200, v200, v201\n"
      "v_mfma_f32_16x16x32_bf16 v[156:159], a[216:219], v[218:221], 0\n"
      "s_waitcnt lgkmcnt(2)\n"
      "v_max3_f32 v115, v115, v184, v185\n"
      "v_alignbyte_b32 v112, v113, v112, v92\n"
      "v_mfma_f32_16x16x32_bf16 v[156:159], a[220:223], v[202:205], v[156:159]\n"
      "s_waitcnt lgkmcnt(1)\n"
      "v_max3_f32 v163, v163, v182, v183\n"
      "v_alignbyte_b32 v174, v175, v174, v92\n"
      "v_max3_f32 v115, v115, v163, v116\n"
      "v_mfma_f32_16x16x32_bf16 v[156:159], a[224:227], v[210:213], v[156:159]\n"
      "s_waitcnt lgkmcnt(0)\n"
      "v_alignbyte_b32 v113, v138, v113, v92\n"
      "v_mov_b32 v206, v115\n"
      "v_mfma_f32_16x16x32_bf16 v[160:163], a[240:243], v[218:221], 0\n"
      "s_nop 0\n"
      "v_permlane16_swap_b32 v115, v206\n"
      "v_mfma_f32_16x16x32_bf16 v[160:163], a[244:247], v[202:205], v[160:163]\n"
      "s_waitcnt vmcnt(6)\n"
      "v_or3_b32 v176, v176, v177, v178\n"
      "v_max_f32 v115, v115, v206\n"
      "v_mfma_f32_16x16x32_bf16 v[160:163], a[248:251], v[210:213], v[160:163]\n"
      "ds_read2_b32 v[186:187], v154 offset0:16 offset1:17\n"
      "v_alignbyte_b32 v175, v139, v175, v92\n"
      "v_mfma_f32_16x16x32_bf16 v[136:139], v[232:235], v[218:221], 0\n"
      "ds_read2_b32 v[118:119], v154 offset0:24 offset1:18\n"
      "v_mov_b32 v206, v115\n"
      "v_mfma_f32_16x16x32_bf16 v[136:139], v[236:239], v[202:205], v[136:139]\n"
      "s_nop 0\n"
      "v_permlane32_swap_b32 v115, v206\n"
      "v_mfma_f32_16x16x32_bf16 v[136:139], v[240:243], v[210:213], v[136:139]\n"
      "v_max_f32 v115, v115, v206\n"
      "v_sub_f32 v184, v184, v115\n"
      "v_sub_f32 v123, v123, v115\n"
      "v_sub_f32 v122, v122, v115\n"
      "v_exp_f32 v184, v184\n"
      "v_mfma_f32_16x16x32_bf16 v[206:209], v[126:129], v[188:191], v[52:55]\n"
      "v_sub_f32 v140, v140, v115\n"
      "s_waitcnt vmcnt(5)\n"
      "v_or3_b32 v176, v176, v179, v78\n"
      "v_sub_f32 v182, v182, v115\n"
      "v_mfma_f32_16x16x32_bf16 v[218:221], v[168:171], v[188:191], v[56:59]\n"
      "s_waitcnt lgkmcnt(1)\n"
      "v_sub_f32 v141, v141, v115\n"
      "v_sub_f32 v185, v185, v115\n"
      "v_mfma_f32_16x16x32_bf16 v[202:205], v[106:109], v[188:191], v[60:63]\n"
      "ds_write_b32 v31, v176 offset:4608\n"
      "v_mfma_f32_16x16x32_bf16 v[152:155], v[144:147], v[188:191], v[26:29]\n"
      "s_waitcnt lgkmcnt(1)\n"
      "v_alignbyte_b32 v186, v187, v186, v92\n"
      "v_sub_f32 v142, v142, v115\n"
      "v_mfma_f32_16x16x16_bf16 v[206:209], v[180:181], v[120:121], v[206:209]\n"
      "v_sub_f32 v125, v125, v115\n"
      "v_exp_f32 v142, v142\n"
      "v_mfma_f32_16x16x16_bf16 v[218:221], v[110:111], v[120:121], v[218:221]\n"
      "v_exp_f32 v182, v182\n"
      "v_alignbyte_b32 v118, v118, v118, v92\n"
      "v_mfma_f32_16x16x16_bf16 v[202:205], v[172:173], v[120:121], v[202:205]\n"
      "v_alignbyte_b32 v187, v119, v187, v92\n"
      "v_exp_f32 v185, v185\n"
      "v_mfma_f32_16x16x16_bf16 v[152:155], v[134:135], v[120:121], v[152:155]\n"
      "v_exp_f32 v123, v123\n"
      "v_sub_f32 v116, v116, v115\n"
      "v_mfma_f32_16x16x16_bf16 v[206:209], v[104:105], v[112:113], v[206:209]\n"
      "v_exp_f32 v140, v140\n"
      "v_sub_f32 v143, v143, v115\n"
      "v_mfma_f32_16x16x16_bf16 v[218:221], v[104:105], v[174:175], v[218:221]\n"
      "v_exp_f32 v122, v122\n"
      "v_sub_f32 v183, v183, v115\n"
      "v_mfma_f32_16x16x16_bf16 v[202:205], v[104:105], v[186:187], v[202:205]\n"
      "v_exp_f32_e64 v125, v125\n"
      "v_cvt_pk_bf16_f32 v89, v184, v185\n"
      "v_mfma_f32_16x16x16_bf16 v[152:155], v[104:105], v[118:119], v[152:155]\n"
      "v_exp_f32 v143, v143\n"
      "v_sub_f32 v124, v124, v115\n"
      "v_mfma_f32_16x16x16_bf16 v[206:209], v[222:223], v[112:113], v[206:209]\n"
      "ds_read_b128 v[112:115], v2 offset:27904\n"
      "v_cvt_pk_bf16_f32 v122, v122, v123\n"
      "v_mfma_f32_16x16x16_bf16 v[218:221], v[222:223], v[174:175], v[218:221]\n"
      "ds_read_b128 v[174:177], v2 offset:30976\n"
      "v_exp_f32 v124, v124\n"
      "v_mfma_f32_16x16x16_bf16 v[202:205], v[222:223], v[186:187], v[202:205]\n"
      "ds_read_b128 v[184:187], v2 offset:34048\n"
      "v_exp_f32 v141, v141\n"
      "v_cvt_pk_bf16_f32 v123, v124, v125\n"
      "v_mfma_f32_16x16x16_bf16 v[152:155], v[222:223], v[118:119], v[152:155]\n"
      "v_cvt_pk_bf16_f32 v125, v142, v143\n"
      "v_max3_f32 v118, v206, v207, v208\n"
      "v_max3_f32 v118, v118, v220, v221\n"
      "v_exp_f32 v116, v116\n"
      "v_exp_f32 v183, v183\n"
      "v_max3_f32 v118, v118, v204, v205\n"
      "v_max3_f32 v213, v209, v218, v219\n"
      "v_cvt_pk_bf16_f32 v124, v140, v141\n"
      "v_max3_f32 v213, v213, v202, v203\n"
      "v_max3_f32 v118, v118, v213, v152\n"
      "s_waitcnt lgkmcnt(2)\n"
      "v_mfma_f32_16x16x32_bf16 v[156:159], a[228:231], v[112:115], v[156:159]\n"
      "s_waitcnt lgkmcnt(1)\n"
      "v_mov_b32 v141, v118\n"
      "v_cvt_pk_bf16_f32 v88, v182, v183\n"
      "v_mfma_f32_16x16x32_bf16 v[156:159], a[232:235], v[174:177], v[156:159]\n"
      "s_waitcnt lgkmcnt(0)\n"
      "v_permlane16_swap_b32_e64 v118, v141\n"
      "v_mfma_f32_16x16x32_bf16 v[156:159], a[236:239], v[184:187], v[156:159]\n"
      "ds_read2_b32 v[212:213], v93 offset0:2 offset1:10\n"
      "v_max_f32_e64 v118, v118, v141\n"
      "v_mfma_f32_16x16x32_bf16 v[160:163], a[252:255], v[112:115], v[160:163]\n"
      "ds_read2_b32 v[182:183], v93 offset0:8 offset1:9\n"
      "v_cvt_pk_bf16_f32 v90, v116, 0\n"
      "v_mfma_f32_16x16x32_bf16 v[160:163], v[224:227], v[174:177], v[160:163]\n"
      "ds_read2_b32 v[154:155], v93 offset0:0 offset1:1\n"
      "v_mov_b32 v141, v118\n"
      "v_mfma_f32_16x16x32_bf16 v[160:163], v[228:231], v[184:187], v[160:163]\n"
      "s_nop 0\n"
      "v_permlane32_swap_b32_e64 v118, v141\n"
      "v_mfma_f32_16x16x32_bf16 v[136:139], v[244:247], v[112:115], v[136:139]\n"
      "ds_read_u16 v78, v11\n"
      "v_max_f32 v118, v118, v141\n"
      "v_mfma_f32_16x16x32_bf16 v[136:139], v[248:251], v[174:177], v[136:139]\n"
      "v_sub_f32 v221, v221, v118\n"
      "v_sub_f32 v152, v152, v118\n"
      "v_sub_f32 v207, v207, v118\n"
      "v_mfma_f32_16x16x32_bf16 v[136:139], v[252:255], v[184:187], v[136:139]\n"
      "ds_read_u16 v82, v11 offset:32\n"
      "v_sub_f32 v203, v203, v118\n"
      "v_sub_f32 v208, v208, v118\n"
      "v_sub_f32 v206, v206, v118\n"
      "v_sub_f32 v220, v220, v118\n"
      "v_mfma_f32_16x16x32_bf16 v[140:143], v[32:35], v[122:125], 0\n"
      "s_waitcnt lgkmcnt(2)\n"
      "v_sub_f32 v209, v209, v118\n"
      "v_sub_f32_e64 v204, v204, v118\n"
      "v_mfma_f32_16x16x32_bf16 v[140:143], v[32:35], v[88:91], v[140:143]\n"
      "ds_read_u16 v86, v11 offset:64\n"
      "v_exp_f32 v208, v208\n"
      "v_exp_f32 v207, v207\n"
      "v_sub_f32 v219, v219, v118\n"
      "v_sub_f32 v202, v202, v118\n"
      "v_alignbyte_b32 v154, v155, v154, v92\n"
      "v_exp_f32 v206, v206\n"
      "v_alignbyte_b32 v155, v212, v155, v92\n"
      "v_alignbyte_b32 v182, v183, v182, v92\n"
      "v_sub_f32 v205, v205, v118\n"
      "v_mfma_f32_16x16x32_bf16 v[174:177], v[196:199], v[122:125], 0\n"
      "s_waitcnt lgkmcnt(1)\n"
      "v_sub_f32 v218, v218, v118\n"
      "v_exp_f32 v209, v209\n"
      "v_mfma_f32_16x16x32_bf16 v[174:177], v[76:79], v[88:91], v[174:177]\n"
      "ds_read2_b32 v[142:143], v93 offset0:16 offset1:17\n"
      "v_exp_f32 v218, v218\n"
      "v_mfma_f32_16x16x32_bf16 v[116:119], v[214:217], v[122:125], 0\n"
      "ds_read2_b32 v[194:195], v93 offset0:24 offset1:18\n"
      "v_exp_f32_e64 v219, v219\n"
      "v_mfma_f32_16x16x32_bf16 v[116:119], v[80:83], v[88:91], v[116:119]\n"
      "v_alignbyte_b32 v183, v213, v183, v92\n"
      "v_cvt_pk_bf16_f32 v206, v206, v207\n"
      "v_mfma_f32_16x16x32_bf16 v[210:213], v[130:133], v[122:125], 0\n"
      "s_waitcnt lgkmcnt(2)\n"
      "v_exp_f32 v220, v220\n"
      "v_mfma_f32_16x16x32_bf16 v[210:213], v[84:87], v[88:91], v[210:213]\n"
      "ds_read_u16 v39, v39\n"
      "v_exp_f32_e64 v221, v221\n"
      "v_cvt_pk_bf16_f32 v207, v208, v209\n"
      "v_cvt_pk_bf16_f32 v208, v218, v219\n"
      "v_mfma_f32_16x16x32_bf16 v[112:115], v[126:129], v[96:99], v[64:67]\n"
      "s_waitcnt lgkmcnt(2)\n"
      "v_cvt_pk_bf16_f32 v209, v220, v221\n"
      "v_alignbyte_b32 v142, v143, v142, v92\n"
      "v_mfma_f32_16x16x32_bf16 v[218:221], v[168:171], v[96:99], v[68:71]\n"
      "s_waitcnt lgkmcnt(1)\n"
      "v_alignbyte_b32 v143, v195, v143, v92\n"
      "v_alignbyte_b32 v194, v194, v194, v92\n"
      "v_mfma_f32_16x16x32_bf16 v[188:191], v[106:109], v[96:99], v[72:75]\n"
      "v_rcp_f32 v140, v140\n"
      "v_mfma_f32_16x16x32_bf16 v[120:123], v[144:147], v[96:99], v[28:31]\n"
      "v_exp_f32 v202, v202\n"
      "v_mfma_f32_16x16x16_bf16 v[112:115], v[180:181], v[192:193], v[112:115]\n"
      "v_exp_f32 v203, v203\n"
      "v_mul_f32 v175, v140, v175\n"
      "v_mfma_f32_16x16x16_bf16 v[218:221], v[110:111], v[192:193], v[218:221]\n"
      "v_exp_f32 v204, v204\n"
      "v_mul_f32 v211, v140, v211\n"
      "v_mfma_f32_16x16x16_bf16 v[188:191], v[172:173], v[192:193], v[188:191]\n"
      "v_exp_f32 v205, v205\n"
      "v_mul_f32 v119, v140, v119\n"
      "v_mfma_f32_16x16x16_bf16 v[120:123], v[134:135], v[192:193], v[120:123]\n"
      "v_mul_f32 v118, v140, v118\n"
      "v_cvt_pk_bf16_f32 v92, v202, v203\n"
      "v_mul_f32 v210, v140, v210\n"
      "v_mfma_f32_16x16x16_bf16 v[112:115], v[104:105], v[154:155], v[112:115]\n"
      "v_exp_f32 v152, v152\n"
      "v_fma_f32 v178, v156, v156, s52\n"
      "v_mfma_f32_16x16x16_bf16 v[218:221], v[104:105], v[182:183], v[218:221]\n"
      "v_mul_f32 v176, v140, v176\n"
      "v_cvt_pk_bf16_f32 v93, v204, v205\n"
      "v_mul_f32 v174, v140, v174\n"
      "v_mfma_f32_16x16x16_bf16 v[188:191], v[104:105], v[142:143], v[188:191]\n"
      "v_mul_f32 v179, v157, v157\n"
      "v_fmac_f32 v179, v159, v159\n"
      "v_mul_f32 v177, v140, v177\n"
      "v_mfma_f32_16x16x16_bf16 v[120:123], v[104:105], v[194:195], v[120:123]\n"
      "ds_read_b128 v[202:205], v2 offset:22016\n"
      "v_cvt_pk_bf16_f32 v210, v210, v211\n"
      "v_mfma_f32_16x16x16_bf16 v[112:115], v[222:223], v[154:155], v[112:115]\n"
      "v_fmac_f32_e64 v178, v158, v158\n"
      "v_cvt_pk_bf16_f32 v174, v174, v175\n"
      "v_cvt_pk_bf16_f32 v175, v176, v177\n"
      "v_mfma_f32_16x16x16_bf16 v[218:221], v[222:223], v[182:183], v[218:221]\n"
      "ds_read_b128 v[182:185], v2 offset:18944\n"
      "v_fmac_f32 v178, v160, v160\n"
      "v_mfma_f32_16x16x16_bf16 v[188:191], v[222:223], v[142:143], v[188:191]\n"
      "v_cvt_pk_bf16_f32 v177, v118, v119\n"
      "v_fmac_f32 v179, v161, v161\n"
      "v_cvt_pk_bf16_f32 v94, v152, 0\n"
      "v_mfma_f32_16x16x16_bf16 v[120:123], v[222:223], v[194:195], v[120:123]\n"
      "ds_read_b128 v[152:155], v2 offset:25088\n"
      "v_max3_f32 v118, v115, v218, v219\n"
      "v_max3_f32 v97, v112, v113, v114\n"
      "v_max3_f32 v97, v97, v220, v221\n"
      "v_max3_f32 v97, v97, v190, v191\n"
      "v_pk_mul_f32 v[116:117], v[116:117], v[140:141] op_sel_hi:[1,0]\n"
      "v_pk_mul_f32 v[212:213], v[212:213], v[140:141] op_sel_hi:[1,0]\n"
      "v_max3_f32 v118, v118, v188, v189\n"
      "v_max3_f32 v97, v97, v118, v120\n"
      "v_pk_fma_f32 v[178:179], v[162:163], v[162:163], v[178:179]\n"
      "v_cvt_pk_bf16_f32 v176, v116, v117\n"
      "v_mov_b32_e64 v140, v97\n"
      "v_pk_fma_f32 v[178:179], v[136:137], v[136:137], v[178:179]\n"
      "v_cvt_pk_bf16_f32 v211, v212, v213\n"
      "v_permlane16_swap_b32 v97, v140\n"
      "v_pk_fma_f32 v[178:179], v[138:139], v[138:139], v[178:179]\n"
      "s_waitcnt lgkmcnt(1)\n"
      "v_mfma_f32_16x16x32_bf16 v[116:119], a[216:219], v[182:185], 0\n"
      "ds_write_b128 v3, v[174:177]\n"
      "v_mfma_f32_16x16x32_bf16 v[116:119], a[220:223], v[202:205], v[116:119]\n"
      "s_waitcnt lgkmcnt(1)\n"
      "v_max_f32 v97, v97, v140\n"
      "v_mov_b32 v140, v97\n"
      "v_mfma_f32_16x16x32_bf16 v[116:119], a[224:227], v[152:155], v[116:119]\n"
      "s_nop 0\n"
      "v_permlane32_swap_b32 v97, v140\n"
      "v_mfma_f32_16x16x32_bf16 v[192:195], a[240:243], v[182:185], 0\n"
      "v_add_f32 v178, v178, v179\n"
      "v_max_f32 v97, v97, v140\n"
      "v_mfma_f32_16x16x32_bf16 v[192:195], a[244:247], v[202:205], v[192:195]\n"
      "v_sub_f32 v220, v220, v97\n"
      "v_sub_f32 v190, v190, v97\n"
      "v_mfma_f32_16x16x32_bf16 v[192:195], a[248:251], v[152:155], v[192:195]\n"
      "v_sub_f32 v221, v221, v97\n"
      "v_sub_f32 v218, v218, v97\n"
      "v_mfma_f32_16x16x32_bf16 v[140:143], v[232:235], v[182:185], 0\n"
      "v_sub_f32 v219, v219, v97\n"
      "v_sub_f32 v114, v114, v97\n"
      "v_mfma_f32_16x16x32_bf16 v[140:143], v[236:239], v[202:205], v[140:143]\n"
      "v_sub_f32 v120, v120, v97\n"
      "v_sub_f32 v191, v191, v97\n"
      "v_mfma_f32_16x16x32_bf16 v[140:143], v[240:243], v[152:155], v[140:143]\n"
      "v_sub_f32 v115, v115, v97\n"
      "v_sub_f32 v189, v189, v97\n"
      "v_mfma_f32_16x16x32_bf16 v[202:205], v[32:35], v[206:209], 0\n"
      "v_exp_f32 v219, v219\n"
      "v_sub_f32 v113, v113, v97\n"
      "v_sub_f32 v188, v188, v97\n"
      "v_sub_f32_e64 v112, v112, v97\n"
      "v_mfma_f32_16x16x32_bf16 v[202:205], v[32:35], v[92:95], v[202:205]\n"
      "ds_read_b128 v[96:99], v10\n"
      "v_exp_f32 v189, v189\n"
      "v_mfma_f32_16x16x32_bf16 v[174:177], v[196:199], v[206:209], 0\n"
      "v_exp_f32 v188, v188\n"
      "v_mfma_f32_16x16x32_bf16 v[174:177], v[76:79], v[92:95], v[174:177]\n"
      "v_exp_f32 v114, v114\n"
      "v_mfma_f32_16x16x32_bf16 v[182:185], v[214:217], v[206:209], 0\n"
      "v_exp_f32 v115, v115\n"
      "v_mfma_f32_16x16x32_bf16 v[182:185], v[80:83], v[92:95], v[182:185]\n"
      "v_rcp_f32 v202, v202\n"
      "v_mfma_f32_16x16x32_bf16 v[152:155], v[130:133], v[206:209], 0\n"
      "ds_read_b64 v[208:209], v10 offset:16\n"
      "v_exp_f32 v113, v113\n"
      "v_mfma_f32_16x16x32_bf16 v[152:155], v[84:87], v[92:95], v[152:155]\n"
      "s_waitcnt lgkmcnt(1)\n"
      "v_exp_f32 v112, v112\n"
      "v_mfma_f32_16x16x32_bf16 v[204:207], v[96:99], v[126:129], v[30:33]\n"
      "v_cvt_pk_bf16_f32 v88, v188, v189\n"
      "v_exp_f32_e64 v218, v218\n"
      "v_mfma_f32_16x16x32_bf16 v[124:127], v[96:99], v[168:171], v[30:33]\n"
      "v_cvt_pk_bf16_f32 v112, v112, v113\n"
      "v_cvt_pk_bf16_f32 v113, v114, v115\n"
      "v_mfma_f32_16x16x32_bf16 v[168:171], v[96:99], v[106:109], v[30:33]\n"
      "ds_read_b128 v[104:107], v2 offset:28160\n"
      "v_cvt_pk_bf16_f32 v114, v218, v219\n"
      "v_mfma_f32_16x16x32_bf16 v[186:189], v[96:99], v[144:147], v[30:33]\n"
      "ds_read_b128 v[96:99], v2 offset:31232\n"
      "s_waitcnt lgkmcnt(2)\n"
      "v_mfma_f32_16x16x16_bf16 v[204:207], v[208:209], v[180:181], v[204:207]\n"
      "ds_read_b128 v[144:147], v2 offset:34304\n"
      "v_mul_f32 v177, v202, v177\n"
      "v_mfma_f32_16x16x16_bf16 v[124:127], v[208:209], v[110:111], v[124:127]\n"
      "v_exp_f32 v191, v191\n"
      "v_exp_f32 v221, v221\n"
      "v_mfma_f32_16x16x16_bf16 v[168:171], v[208:209], v[172:173], v[168:171]\n"
      "v_exp_f32 v120, v120\n"
      "v_exp_f32 v190, v190\n"
      "v_exp_f32_e64 v220, v220\n"
      "v_pk_mul_f32 v[174:175], v[174:175], v[202:203] op_sel_hi:[1,0]\n"
      "v_pk_mul_f32 v[182:183], v[182:183], v[202:203] op_sel_hi:[1,0]\n"
      "v_pk_mul_f32 v[184:185], v[184:185], v[202:203] op_sel_hi:[1,0]\n"
      "v_pk_mul_f32 v[152:153], v[152:153], v[202:203] op_sel_hi:[1,0]\n"
      "v_mfma_f32_16x16x16_bf16 v[186:189], v[208:209], v[134:135], v[186:189]\n"
      "v_cndmask_b32 v204, v204, v124, s[58:59]\n"
      "v_lshlrev_b32 v39, 16, v39\n"
      "s_waitcnt lgkmcnt(1)\n"
      "v_mfma_f32_16x16x32_bf16 v[116:119], a[228:231], v[104:107], v[116:119]\n"
      "v_cndmask_b32 v204, v204, v168, s[60:61]\n"
      "v_mul_f32 v176, v202, v176\n"
      "v_mfma_f32_16x16x32_bf16 v[116:119], a[232:235], v[96:99], v[116:119]\n"
      "s_waitcnt lgkmcnt(0)\n"
      "v_cvt_pk_bf16_f32 v115, v220, v221\n"
      "v_cndmask_b32 v204, v204, v186, s[62:63]\n"
      "v_mfma_f32_16x16x32_bf16 v[116:119], a[236:239], v[144:147], v[116:119]\n"
      "v_fmac_f32_e64 v204, s53, v39\n"
      "v_cvt_pk_bf16_f32 v89, v190, v191\n"
      "v_mfma_f32_16x16x32_bf16 v[192:195], a[252:255], v[104:107], v[192:195]\n"
      "v_max_f32_dpp v209, v204, v204 row_ror:8 row_mask:0xf bank_mask:0xf\n"
      "v_cvt_pk_bf16_f32 v174, v174, v175\n"
      "v_mfma_f32_16x16x32_bf16 v[192:195], v[224:227], v[96:99], v[192:195]\n"
      "v_cvt_pk_bf16_f32 v175, v176, v177\n"
      "v_cvt_pk_bf16_f32 v90, v120, 0\n"
      "v_mfma_f32_16x16x32_bf16 v[192:195], v[228:231], v[144:147], v[192:195]\n"
      "v_max_f32_dpp v209, v209, v209 row_ror:4 row_mask:0xf bank_mask:0xf\n"
      "v_cvt_pk_bf16_f32 v176, v182, v183\n"
      "v_mfma_f32_16x16x32_bf16 v[140:143], v[244:247], v[104:107], v[140:143]\n"
      "v_fma_f32 v182, v116, v116, s52\n"
      "v_max_f32_dpp v209, v209, v209 row_ror:2 row_mask:0xf bank_mask:0xf\n"
      "v_mfma_f32_16x16x32_bf16 v[140:143], v[248:251], v[96:99], v[140:143]\n"
      "v_cvt_pk_bf16_f32 v177, v184, v185\n"
      "v_max_f32_dpp v209, v209, v209 row_ror:1 row_mask:0xf bank_mask:0xf\n"
      "v_mfma_f32_16x16x32_bf16 v[140:143], v[252:255], v[144:147], v[140:143]\n"
      "v_permlane16_swap_b32_e64 v200, v178\n"
      "v_mfma_f32_16x16x32_bf16 v[106:109], v[32:35], v[112:115], 0\n"
      "ds_write_b128 v3, v[174:177] offset:256\n"
      "v_mfma_f32_16x16x32_bf16 v[106:109], v[32:35], v[88:91], v[106:109]\n"
      "v_max_f32_dpp v209, v209, v209 row_bcast:15 row_mask:0xa bank_mask:0xf\n"
      "v_mul_f32 v183, v117, v117\n"
      "v_mfma_f32_16x16x32_bf16 v[218:221], v[196:199], v[112:115], 0\n"
      "v_add_f32 v200, v200, v178\n"
      "v_max_f32_dpp v209, v209, v209 row_bcast:31 row_mask:0xc bank_mask:0xf\n"
      "v_mfma_f32_16x16x32_bf16 v[218:221], v[76:79], v[88:91], v[218:221]\n"
      "v_readlane_b32 s72, v209, 63\n"
      "v_cvt_pk_bf16_f32 v152, v152, v153\n"
      "v_mfma_f32_16x16x32_bf16 v[96:99], v[214:217], v[112:115], 0\n"
      "v_rcp_f32 v106, v106\n"
      "v_mfma_f32_16x16x32_bf16 v[96:99], v[80:83], v[88:91], v[96:99]\n"
      "ds_write_b64 v4, v[210:211]\n"
      "v_subrev_f32 v204, s72, v204\n"
      "v_exp_f32_e64 v204, v204\n"
      "v_pk_fma_f32 v[182:183], v[118:119], v[118:119], v[182:183]\n"
      "v_pk_mul_f32 v[218:219], v[218:219], v[106:107] op_sel_hi:[1,0]\n"
      "v_pk_fma_f32 v[182:183], v[192:193], v[192:193], v[182:183]\n"
      "v_pk_mul_f32 v[220:221], v[220:221], v[106:107] op_sel_hi:[1,0]\n"
      "v_cvt_pk_bf16_f32 v204, v204, v204\n"
      "v_pk_mul_f32 v[96:97], v[96:97], v[106:107] op_sel_hi:[1,0]\n"
      "v_pk_mul_f32 v[98:99], v[98:99], v[106:107] op_sel_hi:[1,0]\n"
      "v_mfma_f32_16x16x32_bf16 v[206:209], v[130:133], v[112:115], 0\n"
      "ds_write_b16 v13, v204\n"
      "v_cvt_pk_bf16_f32 v218, v218, v219\n"
      "v_cvt_pk_bf16_f32 v219, v220, v221\n"
      "v_cvt_pk_bf16_f32 v220, v96, v97\n"
      "v_pk_fma_f32 v[182:183], v[194:195], v[194:195], v[182:183]\n"
      "v_cvt_pk_bf16_f32 v221, v98, v99\n"
      "v_mfma_f32_16x16x32_bf16 v[206:209], v[84:87], v[88:91], v[206:209]\n"
      "ds_write_b128 v3, v[218:221] offset:512\n"
      "v_pk_fma_f32 v[182:183], v[140:141], v[140:141], v[182:183]\n"
      "v_pk_fma_f32 v[182:183], v[142:143], v[142:143], v[182:183]\n"
      "v_pk_mul_f32 v[154:155], v[154:155], v[202:203] op_sel_hi:[1,0]\n"
      "v_add_f32 v182, v182, v183\n"
      "v_mov_b32 v178, v182\n"
      "v_cvt_pk_bf16_f32 v153, v154, v155\n"
      "ds_read_b128 v[210:213], v14\n"
      "ds_write_b64 v4, v[152:153] offset:256\n"
      "v_pk_mul_f32 v[206:207], v[206:207], v[106:107] op_sel_hi:[1,0]\n"
      "v_permlane16_swap_b32 v182, v178\n"
      "v_add_f32 v182, v182, v178\n"
      "v_pk_mul_f32 v[208:209], v[208:209], v[106:107] op_sel_hi:[1,0]\n"
      "v_cvt_pk_bf16_f32 v206, v206, v207\n"
      "v_cvt_pk_bf16_f32 v207, v208, v209\n"
      "ds_write_b64 v4, v[206:207] offset:512\n"
      "ds_read_b128 v[106:109], v14 offset:64\n"
      "v_permlane32_swap_b32 v200, v182\n"
      "v_add_f32 v200, v200, v182\n"
      "s_and_b32 s86, s86, -4\n"
      "ds_write_b32 v31, v200\n"
      "s_waitcnt lgkmcnt(4)\n"
      "v_mfma_f32_16x16x32_bf16 v[218:221], v[210:213], v[32:35], 0\n"
      "s_waitcnt lgkmcnt(1)\n"
      "v_mfma_f32_16x16x32_bf16 v[218:221], v[106:109], v[32:35], v[218:221]\n"
      "s_waitcnt lgkmcnt(0)\n"
      "s_barrier\n"
      "s_cbranch_vccz L_sel1_%=\n"
      "s_branch L_disp1_%=\n"
      "L_tail1_%=:\n"
      "s_nop 4\n"
      "ds_read_b128 v[182:185], v9\n"
      "v_mfma_f32_16x16x32_bf16 v[204:207], v[210:213], v[196:199], 0\n"
      "ds_read_b128 v[196:199], v2 offset:12288\n"
      "ds_read_b128 v[110:113], v2 offset:15616\n"
      "v_mfma_f32_16x16x32_bf16 v[204:207], v[106:109], v[76:79], v[204:207]\n"
      "ds_read_b128 v[188:191], v2 offset:3328\n"
      "v_mfma_f32_16x16x32_bf16 v[176:179], v[210:213], v[214:217], 0\n"
      "ds_read_b128 v[214:217], v2 offset:9216\n"
      "ds_read_b128 v[152:155], v2 offset:3072\n"
      "ds_read_b128 v[172:175], v2 offset:6144\n"
      "v_subrev_u32 v78, 115456, v9\n"
      "v_mfma_f32_16x16x32_bf16 v[176:179], v[106:109], v[80:83], v[176:179]\n"
      "ds_read_b128 v[168:171], v2\n"
      "v_subrev_u32 v206, 115456, v9\n"
      "v_mfma_f32_16x16x32_bf16 v[96:99], v[210:213], v[130:133], 0\n"
      "s_waitcnt lgkmcnt(7)\n"
      "v_add_f32 v182, v182, v183\n"
      "v_add_f32 v184, v184, v185\n"
      "v_add_f32 v182, v182, v184\n"
      "v_rsq_f32 v182, v182\n"
      "s_nop 0\n"
      "v_mul_f32 v182, 0x415db3d7, v182\n"
      "v_mov_b32 v184, v182\n"
      "v_cndmask_b32 v204, v204, v176, s[58:59]\n"
      "v_and_b32 v211, 0x300, v78\n"
      "v_permlane32_swap_b32 v182, v184\n"
      "v_mov_b32 v183, v182\n"
      "v_pk_mul_f32 v[116:117], v[116:117], v[184:185] op_sel_hi:[1,0]\n"
      "v_pk_mul_f32 v[192:193], v[192:193], v[184:185] op_sel_hi:[1,0]\n"
      "v_permlane16_swap_b32 v182, v183\n"
      "v_pk_mul_f32 v[166:167], v[166:167], v[182:183] op_sel_hi:[1,0]\n"
      "v_pk_mul_f32 v[162:163], v[162:163], v[182:183] op_sel:[0,1]\n"
      "v_pk_mul_f32 v[150:151], v[150:151], v[182:183] op_sel_hi:[1,0]\n"
      "v_pk_mul_f32 v[156:157], v[156:157], v[182:183] op_sel:[0,1]\n"
      "v_pk_mul_f32 v[194:195], v[194:195], v[184:185] op_sel_hi:[1,0]\n"
      "v_pk_mul_f32 v[158:159], v[158:159], v[182:183] op_sel:[0,1]\n"
      "v_pk_mul_f32 v[138:139], v[138:139], v[182:183] op_sel:[0,1]\n"
      "v_pk_mul_f32 v[140:141], v[140:141], v[184:185] op_sel_hi:[1,0]\n"
      "v_pk_mul_f32 v[148:149], v[148:149], v[182:183] op_sel_hi:[1,0]\n"
      "v_pk_mul_f32 v[136:137], v[136:137], v[182:183] op_sel:[0,1]\n"
      "v_pk_mul_f32 v[142:143], v[142:143], v[184:185] op_sel_hi:[1,0]\n"
      "v_cvt_pk_bf16_f32 v116, v116, v117\n"
      "v_pk_mul_f32 v[118:119], v[118:119], v[184:185] op_sel_hi:[1,0]\n"
      "v_pk_mul_f32 v[164:165], v[164:165], v[182:183] op_sel_hi:[1,0]\n"
      "v_cvt_pk_bf16_f32 v156, v156, v157\n"
      "v_cvt_pk_bf16_f32 v136, v136, v137\n"
      "v_cvt_pk_bf16_f32 v140, v140, v141\n"
      "v_cvt_pk_bf16_f32 v117, v118, v119\n"
      "v_cvt_pk_bf16_f32 v157, v158, v159\n"
      "v_pk_mul_f32 v[160:161], v[160:161], v[182:183] op_sel:[0,1]\n"
      "v_cvt_pk_bf16_f32 v164, v164, v165\n"
      "v_cvt_pk_bf16_f32 v159, v162, v163\n"
      "v_pk_mul_f32 v[100:101], v[100:101], v[182:183] op_sel_hi:[1,0]\n"
      "v_cvt_pk_bf16_f32 v119, v194, v195\n"
      "v_cvt_pk_bf16_f32 v141, v142, v143\n"
      "v_cvt_pk_bf16_f32 v148, v148, v149\n"
      "v_cvt_pk_bf16_f32 v149, v150, v151\n"
      "v_cvt_pk_bf16_f32 v118, v192, v193\n"
      "v_pk_mul_f32 v[102:103], v[102:103], v[182:183] op_sel_hi:[1,0]\n"
      "v_cvt_pk_bf16_f32 v165, v166, v167\n"
      "v_cvt_pk_bf16_f32 v137, v138, v139\n"
      "v_cvt_pk_bf16_f32 v158, v160, v161\n"
      "v_cvt_pk_bf16_f32 v150, v100, v101\n"
      "v_cvt_pk_bf16_f32 v151, v102, v103\n"
      "v_mfma_f32_16x16x32_bf16 v[96:99], v[106:109], v[84:87], v[96:99]\n"
      "ds_write_b128 v5, v[116:119] offset:30720\n"
      "ds_write_b64 v6, v[164:165] offset:18432\n"
      "ds_write_b128 v5, v[156:159] offset:24576\n"
      "ds_write_b128 v5, v[148:151] offset:18432\n"
      "ds_write_b64 v6, v[136:137] offset:24576\n"
      "ds_write_b64 v6, v[140:141] offset:30720\n"
      "s_waitcnt lgkmcnt(5)\n"
      "v_mfma_f32_16x16x32_bf16 v[148:151], a[216:219], v[168:171], 0\n"
      "ds_read_b128 v[156:159], v2 offset:15360\n"
      "v_subrev_u32 v84, 73728, v6\n"
      "v_mfma_f32_16x16x32_bf16 v[148:151], a[220:223], v[152:155], v[148:151]\n"
      "ds_read_b128 v[116:119], v2 offset:9472\n"
      "v_mfma_f32_16x16x32_bf16 v[100:103], a[240:243], v[168:171], 0\n"
      "ds_read_b128 v[192:195], v2 offset:256\n"
      "v_add_u32 v78, v78, v211\n"
      "v_mfma_f32_16x16x32_bf16 v[100:103], a[244:247], v[152:155], v[100:103]\n"
      "ds_read_b128 v[140:143], v7 offset:8192\n"
      "s_mul_i32 s92, s40, 2048\n"
      "v_mfma_f32_16x16x32_bf16 v[164:167], v[232:235], v[168:171], 0\n"
      "ds_read_b128 v[106:109], v7 offset:16384\n"
      "v_mfma_f32_16x16x32_bf16 v[164:167], v[236:239], v[152:155], v[164:167]\n"
      "v_mfma_f32_16x16x32_bf16 v[148:151], a[224:227], v[172:175], v[148:151]\n"
      "ds_read_b128 v[210:213], v7\n"
      "v_add_u32 v169, s92, v206\n"
      "v_mfma_f32_16x16x32_bf16 v[148:151], a[228:231], v[214:217], v[148:151]\n"
      "v_lshrrev_b32 v206, 4, v169\n"
      "v_mfma_f32_16x16x32_bf16 v[100:103], a[248:251], v[172:175], v[100:103]\n"
      "ds_read_b128 v[130:133], v2 offset:15872\n"
      "v_mfma_f32_16x16x32_bf16 v[100:103], a[252:255], v[214:217], v[100:103]\n"
      "v_mov_b32_dpp v218, v218 quad_perm:[0,0,0,0] row_mask:0xf bank_mask:0xf\n"
      "v_add_u32_e64 v168, 0, v206\n"
      "v_mfma_f32_16x16x32_bf16 v[164:167], v[240:243], v[172:175], v[164:167]\n"
      "v_ashrrev_i32 v86, 31, v37\n"
      "v_cndmask_b32 v204, v204, v96, s[60:61]\n"
      "v_mfma_f32_16x16x32_bf16 v[164:167], v[244:247], v[214:217], v[164:167]\n"
      "ds_read_b128 v[214:217], v2 offset:6656\n"
      "v_mfma_f32_16x16x32_bf16 v[148:151], a[232:235], v[196:199], v[148:151]\n"
      "s_waitcnt lgkmcnt(6)\n"
      "v_and_b32 v85, 0x3fffffff, v37\n"
      "v_mfma_f32_16x16x32_bf16 v[148:151], a[236:239], v[156:159], v[148:151]\n"
      "ds_read_b128 v[96:99], v2 offset:12800\n"
      "v_mfma_f32_16x16x32_bf16 v[100:103], v[224:227], v[196:199], v[100:103]\n"
      "v_mfma_f32_16x16x32_bf16 v[100:103], v[228:231], v[156:159], v[100:103]\n"
      "ds_read_b128 v[200:203], v2 offset:6400\n"
      "v_add_u32 v85, 1179648, v85\n"
      "v_mfma_f32_16x16x32_bf16 v[164:167], v[248:251], v[196:199], v[164:167]\n"
      "ds_read_b128 v[196:199], v2 offset:3584\n"
      "v_add_u32 v78, 149504, v78\n"
      "v_mfma_f32_16x16x32_bf16 v[164:167], v[252:255], v[156:159], v[164:167]\n"
      "s_waitcnt lgkmcnt(4)\n"
      "v_add_u32 v80, 64, v206\n"
      "v_mfma_f32_16x16x32_bf16 v[156:159], a[216:219], v[192:195], 0\n"
      "ds_read_b128 v[206:209], v2 offset:12544\n"
      "v_mul_u32_u24 v82, 2731, v168\n"
      "v_mfma_f32_16x16x32_bf16 v[156:159], a[220:223], v[188:191], v[156:159]\n"
      "ds_read_b128 v[220:223], v7 offset:4096\n"
      "v_mul_u32_u24 v81, 2731, v80\n"
      "v_mfma_f32_16x16x32_bf16 v[160:163], a[240:243], v[192:195], 0\n"
      "v_rcp_f32_e64 v218, v218\n"
      "v_mfma_f32_16x16x32_bf16 v[160:163], a[244:247], v[188:191], v[160:163]\n"
      "ds_read_b128 v[182:185], v2 offset:512\n"
      "v_mfma_f32_16x16x32_bf16 v[136:139], v[232:235], v[192:195], 0\n"
      "ds_read_b128 v[124:127], v2 offset:9728\n"
      "v_mfma_f32_16x16x32_bf16 v[136:139], v[236:239], v[188:191], v[136:139]\n"
      "s_waitcnt lgkmcnt(4)\n"
      "v_fma_f32 v174, v148, v148, s52\n"
      "v_mul_f32 v175, v149, v149\n"
      "v_mfma_f32_16x16x32_bf16 v[156:159], a[224:227], v[200:203], v[156:159]\n"
      "v_fmac_f32 v175, v151, v151\n"
      "v_fmac_f32 v174, v150, v150\n"
      "v_mfma_f32_16x16x32_bf16 v[156:159], a[228:231], v[116:119], v[156:159]\n"
      "buffer_store_dwordx4 v[210:213], v20, s[24:27], s85 offen sc1\n"
      "v_mfma_f32_16x16x32_bf16 v[160:163], a[248:251], v[200:203], v[160:163]\n"
      "s_nop 0\n"
      "ds_read_b128 v[210:213], v78\n"
      "v_lshrrev_b32 v81, 16, v81\n"
      "v_mfma_f32_16x16x32_bf16 v[160:163], a[252:255], v[116:119], v[160:163]\n"
      "v_lshrrev_b32 v82, 16, v82\n"
      "v_fmac_f32 v174, v100, v100\n"
      "v_mfma_f32_16x16x32_bf16 v[136:139], v[240:243], v[200:203], v[136:139]\n"
      "s_waitcnt lgkmcnt(1)\n"
      "v_fmac_f32 v175, v101, v101\n"
      "v_fmac_f32_e64 v174, v102, v102\n"
      "v_mfma_f32_16x16x32_bf16 v[136:139], v[244:247], v[116:119], v[136:139]\n"
      "buffer_store_dwordx4 v[220:223], v21, s[24:27], s85 offen sc1\n"
      "v_mfma_f32_16x16x32_bf16 v[156:159], a[232:235], v[206:209], v[156:159]\n"
      "v_and_b32 v86, 100270464, v86\n"
      "v_fmac_f32 v175, v103, v103\n"
      "v_mfma_f32_16x16x32_bf16 v[156:159], a[236:239], v[110:113], v[156:159]\n"
      "buffer_store_dwordx4 v[140:143], v22, s[24:27], s85 offen sc1\n"
      "v_mfma_f32_16x16x32_bf16 v[160:163], v[224:227], v[206:209], v[160:163]\n"
      "v_fmac_f32 v175, v165, v165\n"
      "v_mfma_f32_16x16x32_bf16 v[160:163], v[228:231], v[110:113], v[160:163]\n"
      "ds_read_b128 v[200:203], v7 offset:12288\n"
      "v_fmac_f32 v174, v164, v164\n"
      "v_mfma_f32_16x16x32_bf16 v[136:139], v[248:251], v[206:209], v[136:139]\n"
      "s_nop 0\n"
      "v_fma_f32 v190, v156, v156, s52\n"
      "v_fmac_f32 v174, v166, v166\n"
      "v_mfma_f32_16x16x32_bf16 v[136:139], v[252:255], v[110:113], v[136:139]\n"
      "v_mul_f32 v191, v157, v157\n"
      "v_mfma_f32_16x16x32_bf16 v[116:119], a[216:219], v[182:185], 0\n"
      "v_fmac_f32 v175, v167, v167\n"
      "v_mul_u32_u24 v76, 24, v81\n"
      "v_mfma_f32_16x16x32_bf16 v[116:119], a[220:223], v[196:199], v[116:119]\n"
      "ds_read_b128 v[220:223], v78 offset:6144\n"
      "v_fmac_f32_e64 v190, v158, v158\n"
      "v_mfma_f32_16x16x32_bf16 v[192:195], a[240:243], v[182:185], 0\n"
      "ds_read_b128 v[206:209], v78 offset:2048\n"
      "v_fmac_f32 v191, v159, v159\n"
      "v_mfma_f32_16x16x32_bf16 v[192:195], a[244:247], v[196:199], v[192:195]\n"
      "v_add_f32 v174, v174, v175\n"
      "v_mfma_f32_16x16x32_bf16 v[140:143], v[232:235], v[182:185], 0\n"
      "v_fmac_f32 v190, v160, v160\n"
      "v_mul_f32 v204, v218, v204\n"
      "v_mfma_f32_16x16x32_bf16 v[140:143], v[236:239], v[196:199], v[140:143]\n"
      "ds_read_b128 v[196:199], v78 offset:10240\n"
      "v_mfma_f32_16x16x32_bf16 v[116:119], a[224:227], v[214:217], v[116:119]\n"
      "v_fmac_f32 v191, v161, v161\n"
      "v_sub_u32 v85, v85, v86\n"
      "v_mfma_f32_16x16x32_bf16 v[116:119], a[228:231], v[124:127], v[116:119]\n"
      "v_fmac_f32_e64 v190, v162, v162\n"
      "v_cvt_pk_bf16_f32 v204, v204, v204\n"
      "v_mfma_f32_16x16x32_bf16 v[192:195], a[248:251], v[214:217], v[192:195]\n"
      "v_fmac_f32 v191, v163, v163\n"
      "v_fmac_f32 v190, v136, v136\n"
      "v_mfma_f32_16x16x32_bf16 v[192:195], a[252:255], v[124:127], v[192:195]\n"
      "ds_read_b128 v[170:173], v78 offset:4096\n"
      "v_mfma_f32_16x16x32_bf16 v[140:143], v[240:243], v[214:217], v[140:143]\n"
      "v_fmac_f32 v191, v137, v137\n"
      "v_fmac_f32 v191, v139, v139\n"
      "v_mfma_f32_16x16x32_bf16 v[140:143], v[244:247], v[124:127], v[140:143]\n"
      "ds_write_b16 v12, v204\n"
      "v_mfma_f32_16x16x32_bf16 v[116:119], a[232:235], v[96:99], v[116:119]\n"
      "s_waitcnt lgkmcnt(2)\n"
      "v_fmac_f32 v190, v138, v138\n"
      "v_add_f32_e64 v190, v190, v191\n"
      "v_mfma_f32_16x16x32_bf16 v[116:119], a[236:239], v[130:133], v[116:119]\n"
      "s_nop 0\n"
      "v_permlane16_swap_b32 v174, v190\n"
      "v_mfma_f32_16x16x32_bf16 v[192:195], v[224:227], v[96:99], v[192:195]\n"
      "buffer_store_dwordx4 v[200:203], v23, s[24:27], s85 offen sc1\n"
      "v_mfma_f32_16x16x32_bf16 v[192:195], v[228:231], v[130:133], v[192:195]\n"
      "v_sub_u32 v76, v80, v76\n"
      "v_add_f32 v174, v174, v190\n"
      "v_mfma_f32_16x16x32_bf16 v[140:143], v[248:251], v[96:99], v[140:143]\n"
      "v_mul_f32 v97, v117, v117\n"
      "v_fma_f32 v96, v116, v116, s52\n"
      "v_mfma_f32_16x16x32_bf16 v[140:143], v[252:255], v[130:133], v[140:143]\n"
      "v_fmac_f32 v97, v119, v119\n"
      "v_fmac_f32 v96, v118, v118\n"
      "v_mfma_f32_16x16x32_bf16 v[130:133], a[216:219], v[210:213], 0\n"
      "v_fmac_f32 v97, v193, v193\n"
      "v_fmac_f32 v96, v192, v192\n"
      "v_mfma_f32_16x16x32_bf16 v[130:133], a[220:223], v[206:209], v[130:133]\n"
      "v_fmac_f32 v97, v195, v195\n"
      "v_fmac_f32 v96, v194, v194\n"
      "v_mfma_f32_16x16x32_bf16 v[214:217], a[240:243], v[210:213], 0\n"
      "v_fmac_f32 v97, v141, v141\n"
      "v_fmac_f32 v96, v140, v140\n"
      "v_mfma_f32_16x16x32_bf16 v[214:217], a[244:247], v[206:209], v[214:217]\n"
      "v_fmac_f32 v97, v143, v143\n"
      "v_fmac_f32 v96, v142, v142\n"
      "v_mfma_f32_16x16x32_bf16 v[200:203], v[232:235], v[210:213], 0\n"
      "s_waitcnt lgkmcnt(0)\n"
      "v_add_f32 v96, v96, v97\n"
      "v_mov_b32 v190, v96\n"
      "v_mfma_f32_16x16x32_bf16 v[200:203], v[236:239], v[206:209], v[200:203]\n"
      "s_nop 0\n"
      "v_permlane16_swap_b32 v96, v190\n"
      "v_mfma_f32_16x16x32_bf16 v[130:133], a[224:227], v[170:173], v[130:133]\n"
      "ds_read_b128 v[210:213], v78 offset:8192\n"
      "v_add_f32 v96, v96, v190\n"
      "v_mfma_f32_16x16x32_bf16 v[130:133], a[228:231], v[220:223], v[130:133]\n"
      "s_nop 0\n"
      "v_permlane32_swap_b32 v174, v96\n"
      "v_add_f32 v174, v174, v96\n"
      "v_mfma_f32_16x16x32_bf16 v[214:217], a[248:251], v[170:173], v[214:217]\n"
      "ds_write_b32 v31, v174 offset:1024\n"
      "v_mfma_f32_16x16x32_bf16 v[214:217], a[252:255], v[220:223], v[214:217]\n"
      "buffer_store_dwordx4 v[106:109], v25, s[24:27], s85 offen sc1\n"
      "v_mfma_f32_16x16x32_bf16 v[200:203], v[240:243], v[170:173], v[200:203]\n"
      "v_mul_u32_u24_e64 v127, 24, v82\n"
      "v_readlane_b32 s85, v37, s42\n"
      "v_bfe_i32 v86, v37, 30, 1\n"
      "v_mfma_f32_16x16x32_bf16 v[200:203], v[244:247], v[220:223], v[200:203]\n"
      "s_or_b32 s95, s85, s48\n"
      "v_and_b32 v86, 196224, v86\n"
      "s_waitcnt lgkmcnt(1)\n"
      "v_mfma_f32_16x16x32_bf16 v[130:133], a[232:235], v[210:213], v[130:133]\n"
      "s_cmp_ge_u32 s95, 0x40000000\n"
      "s_cbranch_scc1 L_rare_27_%=\n"
      "L_back_28_%=:\n"
      "v_mfma_f32_16x16x32_bf16 v[130:133], a[236:239], v[196:199], v[130:133]\n"
      "s_waitcnt lgkmcnt(0)\n"
      "s_barrier\n"
      "ds_read_b128 v[106:109], v9 offset:1024\n"
      "ds_read_b128 v[218:221], v78 offset:256\n"
      "v_mfma_f32_16x16x32_bf16 v[214:217], v[224:227], v[210:213], v[214:217]\n"
      "ds_read_b128 v[204:207], v78 offset:2304\n"
      "v_mfma_f32_16x16x32_bf16 v[214:217], v[228:231], v[196:199], v[214:217]\n"
      "ds_read_b128 v[96:99], v7 offset:18432\n"
      "v_mul_f32_e64 v171, v131, v131\n"
      "v_mfma_f32_16x16x32_bf16 v[200:203], v[248:251], v[210:213], v[200:203]\n"
      "ds_read_b128 v[210:213], v7 offset:22528\n"
      "v_fma_f32 v170, v130, v130, s52\n"
      "v_mfma_f32_16x16x32_bf16 v[200:203], v[252:255], v[196:199], v[200:203]\n"
      "ds_read_b128 v[196:199], v78 offset:4352\n"
      "ds_read_b128 v[182:185], v78 offset:6400\n"
      "ds_read_b128 v[172:175], v7 offset:26624\n"
      "v_pk_fma_f32 v[170:171], v[132:133], v[132:133], v[170:171]\n"
      "v_pk_fma_f32 v[170:171], v[214:215], v[214:215], v[170:171]\n"
      "s_waitcnt lgkmcnt(7)\n"
      "v_add_f32 v108, v108, v109\n"
      "v_add_f32 v106, v106, v107\n"
      "v_add_f32 v106, v106, v108\n"
      "v_pk_fma_f32 v[170:171], v[216:217], v[216:217], v[170:171]\n"
      "s_waitcnt lgkmcnt(6)\n"
      "v_mfma_f32_16x16x32_bf16 v[188:191], a[216:219], v[218:221], 0\n"
      "s_waitcnt lgkmcnt(5)\n"
      "v_rsq_f32 v106, v106\n"
      "v_mfma_f32_16x16x32_bf16 v[188:191], a[220:223], v[204:207], v[188:191]\n"
      "s_waitcnt lgkmcnt(4)\n"
      "v_mul_f32 v106, 0x415db3d7, v106\n"
      "v_mov_b32_e64 v108, v106\n"
      "v_mfma_f32_16x16x32_bf16 v[110:113], a[240:243], v[218:221], 0\n"
      "s_nop 0\n"
      "v_permlane32_swap_b32 v106, v108\n"
      "v_mfma_f32_16x16x32_bf16 v[110:113], a[244:247], v[204:207], v[110:113]\n"
      "buffer_store_dwordx4 v[96:99], v20, s[24:27], s85 offen sc1\n"
      "v_mov_b32 v107, v106\n"
      "v_pk_mul_f32 v[140:141], v[140:141], v[108:109] op_sel_hi:[1,0]\n"
      "v_mfma_f32_16x16x32_bf16 v[96:99], v[232:235], v[218:221], 0\n"
      "s_waitcnt lgkmcnt(0)\n"
      "v_permlane16_swap_b32_e64 v106, v107\n"
      "v_mfma_f32_16x16x32_bf16 v[96:99], v[236:239], v[204:207], v[96:99]\n"
      "buffer_store_dwordx4 v[210:213], v21, s[24:27], s85 offen sc1\n"
      "v_pk_mul_f32 v[136:137], v[136:137], v[106:107] op_sel:[0,1]\n"
      "v_pk_mul_f32 v[150:151], v[150:151], v[106:107] op_sel_hi:[1,0]\n"
      "v_pk_mul_f32 v[164:165], v[164:165], v[106:107] op_sel_hi:[1,0]\n"
      "v_pk_mul_f32 v[166:167], v[166:167], v[106:107] op_sel_hi:[1,0]\n"
      "v_pk_mul_f32 v[160:161], v[160:161], v[106:107] op_sel:[0,1]\n"
      "v_pk_fma_f32 v[170:171], v[200:201], v[200:201], v[170:171]\n"
      "v_pk_mul_f32 v[158:159], v[158:159], v[106:107] op_sel:[0,1]\n"
      "v_pk_mul_f32 v[162:163], v[162:163], v[106:107] op_sel:[0,1]\n"
      "v_pk_mul_f32 v[142:143], v[142:143], v[108:109] op_sel_hi:[1,0]\n"
      "v_pk_mul_f32 v[102:103], v[102:103], v[106:107] op_sel_hi:[1,0]\n"
      "v_pk_mul_f32 v[138:139], v[138:139], v[106:107] op_sel:[0,1]\n"
      "v_pk_mul_f32 v[148:149], v[148:149], v[106:107] op_sel_hi:[1,0]\n"
      "v_pk_mul_f32 v[156:157], v[156:157], v[106:107] op_sel:[0,1]\n"
      "v_mfma_f32_16x16x32_bf16 v[188:191], a[224:227], v[196:199], v[188:191]\n"
      "ds_read_b128 v[210:213], v78 offset:10496\n"
      "v_cvt_pk_bf16_f32 v148, v148, v149\n"
      "v_mfma_f32_16x16x32_bf16 v[188:191], a[228:231], v[182:185], v[188:191]\n"
      "ds_read_b128 v[218:221], v78 offset:8448\n"
      "v_cvt_pk_bf16_f32 v164, v164, v165\n"
      "v_mfma_f32_16x16x32_bf16 v[110:113], a[248:251], v[196:199], v[110:113]\n"
      "buffer_store_dwordx4 v[172:175], v22, s[24:27], s85 offen sc1\n"
      "v_pk_mul_f32 v[100:101], v[100:101], v[106:107] op_sel_hi:[1,0]\n"
      "v_mfma_f32_16x16x32_bf16 v[110:113], a[252:255], v[182:185], v[110:113]\n"
      "ds_read_b128 v[204:207], v7 offset:30720\n"
      "v_cvt_pk_bf16_f32 v149, v150, v151\n"
      "v_cvt_pk_bf16_f32 v140, v140, v141\n"
      "v_cvt_pk_bf16_f32 v156, v156, v157\n"
      "v_cvt_pk_bf16_f32 v157, v158, v159\n"
      "v_pk_fma_f32 v[170:171], v[202:203], v[202:203], v[170:171]\n"
      "v_mfma_f32_16x16x32_bf16 v[96:99], v[240:243], v[196:199], v[96:99]\n"
      "v_cvt_pk_bf16_f32 v165, v166, v167\n"
      "v_cvt_pk_bf16_f32 v159, v162, v163\n"
      "v_cvt_pk_bf16_f32 v141, v142, v143\n"
      "v_mfma_f32_16x16x32_bf16 v[96:99], v[244:247], v[182:185], v[96:99]\n"
      "s_waitcnt lgkmcnt(1)\n"
      "v_cvt_pk_bf16_f32 v136, v136, v137\n"
      "v_cvt_pk_bf16_f32 v150, v100, v101\n"
      "v_cvt_pk_bf16_f32 v137, v138, v139\n"
      "v_mfma_f32_16x16x32_bf16 v[188:191], a[232:235], v[218:221], v[188:191]\n"
      "ds_write_b64 v6, v[164:165]\n"
      "v_mfma_f32_16x16x32_bf16 v[188:191], a[236:239], v[210:213], v[188:191]\n"
      "ds_write_b64 v6, v[136:137] offset:6144\n"
      "v_mfma_f32_16x16x32_bf16 v[110:113], v[224:227], v[218:221], v[110:113]\n"
      "ds_write_b64 v6, v[140:141] offset:12288\n"
      "v_cvt_pk_bf16_f32 v151, v102, v103\n"
      "v_mfma_f32_16x16x32_bf16 v[110:113], v[228:231], v[210:213], v[110:113]\n"
      "ds_write_b128 v5, v[148:151]\n"
      "v_cvt_pk_bf16_f32 v158, v160, v161\n"
      "v_mfma_f32_16x16x32_bf16 v[96:99], v[248:251], v[218:221], v[96:99]\n"
      "ds_read_b128 v[148:151], v7 offset:34816\n"
      "v_fma_f32 v136, v188, v188, s52\n"
      "v_mfma_f32_16x16x32_bf16 v[96:99], v[252:255], v[210:213], v[96:99]\n"
      "ds_write_b128 v5, v[156:159] offset:6144\n"
      "v_mul_f32 v137, v189, v189\n"
      "v_pk_fma_f32 v[136:137], v[190:191], v[190:191], v[136:137]\n"
      "v_pk_mul_f32 v[118:119], v[118:119], v[108:109] op_sel_hi:[1,0]\n"
      "v_pk_fma_f32 v[136:137], v[110:111], v[110:111], v[136:137]\n"
      "v_pk_mul_f32 v[116:117], v[116:117], v[108:109] op_sel_hi:[1,0]\n"
      "v_pk_fma_f32 v[136:137], v[112:113], v[112:113], v[136:137]\n"
      "v_pk_mul_f32 v[194:195], v[194:195], v[108:109] op_sel_hi:[1,0]\n"
      "v_pk_fma_f32 v[136:137], v[96:97], v[96:97], v[136:137]\n"
      "v_add_f32 v170, v170, v171\n"
      "v_pk_fma_f32 v[136:137], v[98:99], v[98:99], v[136:137]\n"
      "v_add_f32 v136, v136, v137\n"
      "v_cvt_pk_bf16_f32 v116, v116, v117\n"
      "v_pk_mul_f32 v[192:193], v[192:193], v[108:109] op_sel_hi:[1,0]\n"
      "v_permlane16_swap_b32 v170, v136\n"
      "v_cvt_pk_bf16_f32 v117, v118, v119\n"
      "v_add_f32 v170, v170, v136\n"
      "v_mov_b32 v136, v170\n"
      "v_cvt_pk_bf16_f32 v118, v192, v193\n"
      "v_cvt_pk_bf16_f32 v119, v194, v195\n"
      "v_permlane32_swap_b32 v170, v136\n"
      "v_add_f32 v170, v170, v136\n"
      "s_add_u32 s42, s42, 1\n"
      "ds_write_b128 v5, v[116:119] offset:12288\n"
      "ds_write_b32 v31, v170 offset:4608\n"
      "s_waitcnt lgkmcnt(2)\n"
      "buffer_store_dwordx4 v[204:207], v23, s[24:27], s85 offen sc1\n"
      "v_sub_u32 v127, v168, v127\n"
      "buffer_store_dwordx4 v[148:151], v25, s[24:27], s85 offen sc1\n"
      "v_and_b32_e64 v80, 7, v81\n"
      "s_waitcnt lgkmcnt(0)\n"
      "s_barrier\n"
      "ds_read_b128 v[148:151], v9 offset:4608\n"
      "v_lshl_add_u32 v81, v81, 2, 12\n"
      "v_and_b32 v168, 7, v82\n"
      "v_readlane_b32 s85, v37, s42\n"
      "ds_read_b128 v[116:119], v7\n"
      "s_or_b32 s95, s85, s48\n"
      "v_xor_b32_e64 v127, v127, v168\n"
      "s_cmp_ge_u32 s95, 0x40000000\n"
      "s_cbranch_scc1 L_rare_31_%=\n"
      "L_back_32_%=:\n"
      "ds_read_b128 v[192:195], v7 offset:4096\n"
      "ds_read_b128 v[204:207], v7 offset:16384\n"
      "ds_read_b128 v[156:159], v7 offset:8192\n"
      "ds_read_b128 v[210:213], v7 offset:12288\n"
      "s_waitcnt lgkmcnt(5)\n"
      "v_add_f32 v150, v150, v151\n"
      "v_add_f32 v148, v148, v149\n"
      "v_add_f32 v148, v148, v150\n"
      "v_rsq_f32 v148, v148\n"
      "s_nop 0\n"
      "v_mul_f32 v148, 0x415db3d7, v148\n"
      "v_mov_b32 v150, v148\n"
      "v_subrev_u32 v151, 73728, v5\n"
      "s_nop 0\n"
      "v_permlane32_swap_b32 v148, v150\n"
      "v_mov_b32 v149, v148\n"
      "v_sub_u32 v85, v85, v86\n"
      "v_lshl_add_u32 v82, v82, 2, 12\n"
      "v_permlane16_swap_b32 v148, v149\n"
      "v_pk_mul_f32 v[188:189], v[188:189], v[148:149] op_sel:[0,1]\n"
      "v_pk_mul_f32 v[96:97], v[96:97], v[148:149] op_sel:[0,1]\n"
      "v_pk_mul_f32 v[112:113], v[112:113], v[148:149] op_sel:[0,1]\n"
      "v_pk_mul_f32 v[132:133], v[132:133], v[148:149] op_sel_hi:[1,0]\n"
      "v_pk_mul_f32 v[130:131], v[130:131], v[148:149] op_sel_hi:[1,0]\n"
      "v_pk_mul_f32 v[202:203], v[202:203], v[148:149] op_sel_hi:[1,0]\n"
      "v_cvt_pk_bf16_f32 v96, v96, v97\n"
      "v_cvt_pk_bf16_f32 v188, v188, v189\n"
      "v_pk_mul_f32 v[190:191], v[190:191], v[148:149] op_sel:[0,1]\n"
      "v_pk_mul_f32 v[200:201], v[200:201], v[148:149] op_sel_hi:[1,0]\n"
      "v_cvt_pk_bf16_f32 v200, v200, v201\n"
      "v_pk_mul_f32 v[214:215], v[214:215], v[148:149] op_sel_hi:[1,0]\n"
      "v_cvt_pk_bf16_f32 v189, v190, v191\n"
      "v_pk_mul_f32 v[110:111], v[110:111], v[148:149] op_sel:[0,1]\n"
      "v_cvt_pk_bf16_f32 v130, v130, v131\n"
      "v_pk_mul_f32 v[216:217], v[216:217], v[148:149] op_sel_hi:[1,0]\n"
      "v_pk_mul_f32 v[98:99], v[98:99], v[148:149] op_sel:[0,1]\n"
      "v_cvt_pk_bf16_f32 v191, v112, v113\n"
      "v_cvt_pk_bf16_f32 v131, v132, v133\n"
      "v_cvt_pk_bf16_f32 v132, v214, v215\n"
      "v_cvt_pk_bf16_f32 v201, v202, v203\n"
      "v_cvt_pk_bf16_f32 v190, v110, v111\n"
      "v_cvt_pk_bf16_f32 v97, v98, v99\n"
      "s_add_u32 s42, s42, 1\n"
      "ds_write_b128 v151, v[188:191] offset:6144\n"
      "ds_write_b64 v84, v[200:201]\n"
      "ds_write_b64 v84, v[96:97] offset:6144\n"
      "v_cvt_pk_bf16_f32 v133, v216, v217\n"
      "s_waitcnt lgkmcnt(3)\n"
      "buffer_store_dwordx4 v[116:119], v20, s[24:27], s85 offen sc1\n"
      "ds_write_b128 v151, v[130:133]\n"
      "buffer_store_dwordx4 v[192:195], v21, s[24:27], s85 offen sc1\n"
      "v_xor_b32_e64 v76, v76, v80\n"
      "ds_bpermute_b32 v80, v82, v85\n"
      "ds_bpermute_b32 v82, v81, v85\n"
      "buffer_store_dwordx4 v[156:159], v22, s[24:27], s85 offen sc1\n"
      "s_waitcnt lgkmcnt(2)\n"
      "s_barrier\n"
      "ds_read_b128 v[156:159], v169\n"
      "buffer_store_dwordx4 v[210:213], v23, s[24:27], s85 offen sc1\n"
      "s_nop 1\n"
      "ds_read_b128 v[210:213], v169 offset:1024\n"
      "buffer_store_dwordx4 v[204:207], v25, s[24:27], s85 offen sc1\n"
      "s_waitcnt lgkmcnt(2)\n"
      "v_lshl_add_u32 v80, v127, 4, v80\n"
      "v_lshl_add_u32 v82, v76, 4, v82\n"
      "s_waitcnt lgkmcnt(1)\n"
      "buffer_store_dwordx4 v[156:159], v80, s[24:27], 0 offen sc1\n"
      "s_waitcnt lgkmcnt(0)\n"
      "buffer_store_dwordx4 v[210:213], v82, s[24:27], 0 offen sc1\n"
      "s_endpgm\n"
      "L_tail0_%=:\n"
      "s_nop 4\n"
      "ds_read_b128 v[168:171], v9 offset:1024\n"
      "v_and_b32 v208, 0x3fffffff, v37\n"
      "v_mfma_f32_16x16x32_bf16 v[204:207], v[210:213], v[196:199], 0\n"
      "ds_read_b128 v[188:191], v2 offset:18432\n"
      "ds_read_b128 v[110:113], v2 offset:24576\n"
      "ds_read_b128 v[196:199], v2 offset:27904\n"
      "v_mfma_f32_16x16x32_bf16 v[204:207], v[106:109], v[76:79], v[204:207]\n"
      "ds_read_b128 v[124:127], v2 offset:27648\n"
      "ds_read_b128 v[182:185], v7 offset:30720\n"
      "v_mfma_f32_16x16x32_bf16 v[176:179], v[210:213], v[214:217], 0\n"
      "ds_read_b128 v[172:175], v7 offset:18432\n"
      "ds_read_b128 v[214:217], v2 offset:18688\n"
      "v_mfma_f32_16x16x32_bf16 v[176:179], v[106:109], v[80:83], v[176:179]\n"
      "ds_read_b128 v[144:147], v2 offset:21504\n"
      "v_subrev_u32 v115, 115456, v9\n"
      "s_waitcnt lgkmcnt(8)\n"
      "v_add_f32 v168, v168, v169\n"
      "v_add_f32 v170, v170, v171\n"
      "v_add_f32 v168, v168, v170\n"
      "v_rsq_f32_e64 v168, v168\n"
      "v_mfma_f32_16x16x32_bf16 v[96:99], v[210:213], v[130:133], 0\n"
      "s_waitcnt lgkmcnt(7)\n"
      "v_mul_f32 v168, 0x415db3d7, v168\n"
      "v_mov_b32 v170, v168\n"
      "v_cndmask_b32 v204, v204, v176, s[58:59]\n"
      "s_nop 0\n"
      "v_permlane32_swap_b32 v168, v170\n"
      "v_mov_b32 v169, v168\n"
      "v_pk_mul_f32 v[142:143], v[142:143], v[170:171] op_sel_hi:[1,0]\n"
      "v_pk_mul_f32 v[116:117], v[116:117], v[170:171] op_sel_hi:[1,0]\n"
      "v_permlane16_swap_b32 v168, v169\n"
      "v_pk_mul_f32 v[140:141], v[140:141], v[170:171] op_sel_hi:[1,0]\n"
      "v_pk_mul_f32 v[166:167], v[166:167], v[168:169] op_sel_hi:[1,0]\n"
      "v_pk_mul_f32 v[192:193], v[192:193], v[170:171] op_sel_hi:[1,0]\n"
      "v_pk_mul_f32 v[164:165], v[164:165], v[168:169] op_sel_hi:[1,0]\n"
      "v_pk_mul_f32 v[156:157], v[156:157], v[168:169] op_sel:[0,1]\n"
      "v_pk_mul_f32 v[194:195], v[194:195], v[170:171] op_sel_hi:[1,0]\n"
      "v_pk_mul_f32 v[148:149], v[148:149], v[168:169] op_sel_hi:[1,0]\n"
      "v_pk_mul_f32 v[118:119], v[118:119], v[170:171] op_sel_hi:[1,0]\n"
      "v_cvt_pk_bf16_f32 v140, v140, v141\n"
      "v_pk_mul_f32 v[158:159], v[158:159], v[168:169] op_sel:[0,1]\n"
      "v_pk_mul_f32 v[150:151], v[150:151], v[168:169] op_sel_hi:[1,0]\n"
      "v_pk_mul_f32 v[100:101], v[100:101], v[168:169] op_sel_hi:[1,0]\n"
      "v_cvt_pk_bf16_f32 v116, v116, v117\n"
      "v_pk_mul_f32 v[136:137], v[136:137], v[168:169] op_sel:[0,1]\n"
      "v_pk_mul_f32 v[102:103], v[102:103], v[168:169] op_sel_hi:[1,0]\n"
      "v_pk_mul_f32 v[160:161], v[160:161], v[168:169] op_sel:[0,1]\n"
      "v_cvt_pk_bf16_f32 v148, v148, v149\n"
      "v_pk_mul_f32 v[138:139], v[138:139], v[168:169] op_sel:[0,1]\n"
      "v_cvt_pk_bf16_f32 v136, v136, v137\n"
      "v_pk_mul_f32 v[162:163], v[162:163], v[168:169] op_sel:[0,1]\n"
      "v_cvt_pk_bf16_f32 v149, v150, v151\n"
      "v_cvt_pk_bf16_f32 v150, v100, v101\n"
      "v_cvt_pk_bf16_f32 v117, v118, v119\n"
      "v_cvt_pk_bf16_f32 v156, v156, v157\n"
      "v_cvt_pk_bf16_f32 v157, v158, v159\n"
      "v_cvt_pk_bf16_f32 v119, v194, v195\n"
      "v_cvt_pk_bf16_f32 v164, v164, v165\n"
      "v_cvt_pk_bf16_f32 v118, v192, v193\n"
      "v_cvt_pk_bf16_f32 v159, v162, v163\n"
      "v_cvt_pk_bf16_f32 v158, v160, v161\n"
      "v_cvt_pk_bf16_f32 v141, v142, v143\n"
      "v_cvt_pk_bf16_f32 v165, v166, v167\n"
      "v_cvt_pk_bf16_f32 v137, v138, v139\n"
      "v_cvt_pk_bf16_f32 v151, v102, v103\n"
      "v_mfma_f32_16x16x32_bf16 v[96:99], v[106:109], v[84:87], v[96:99]\n"
      "ds_write_b128 v5, v[116:119] offset:12288\n"
      "ds_write_b64 v6, v[140:141] offset:12288\n"
      "ds_write_b128 v5, v[156:159] offset:6144\n"
      "ds_write_b64 v6, v[164:165]\n"
      "ds_write_b64 v6, v[136:137] offset:6144\n"
      "ds_write_b128 v5, v[148:151]\n"
      "v_mfma_f32_16x16x32_bf16 v[148:151], a[216:219], v[188:191], 0\n"
      "ds_read_b128 v[156:159], v2 offset:30720\n"
      "s_waitcnt lgkmcnt(5)\n"
      "v_mfma_f32_16x16x32_bf16 v[148:151], a[220:223], v[144:147], v[148:151]\n"
      "ds_read_b128 v[160:163], v2 offset:33792\n"
      "v_mfma_f32_16x16x32_bf16 v[100:103], a[240:243], v[188:191], 0\n"
      "v_mov_b32_dpp v218, v218 quad_perm:[0,0,0,0] row_mask:0xf bank_mask:0xf\n"
      "v_mfma_f32_16x16x32_bf16 v[100:103], a[244:247], v[144:147], v[100:103]\n"
      "ds_read_b128 v[116:119], v2 offset:21760\n"
      "v_mfma_f32_16x16x32_bf16 v[164:167], v[232:235], v[188:191], 0\n"
      "ds_read_b128 v[200:203], v2 offset:34304\n"
      "v_mfma_f32_16x16x32_bf16 v[164:167], v[236:239], v[144:147], v[164:167]\n"
      "ds_read_b128 v[192:195], v2 offset:34048\n"
      "v_subrev_u32 v78, 115456, v9\n"
      "v_mfma_f32_16x16x32_bf16 v[148:151], a[224:227], v[110:113], v[148:151]\n"
      "ds_read_b128 v[130:133], v2 offset:31232\n"
      "v_add_u32 v208, 1179648, v208\n"
      "v_mfma_f32_16x16x32_bf16 v[148:151], a[228:231], v[124:127], v[148:151]\n"
      "s_mul_i32 s92, s40, 2048\n"
      "ds_read_b128 v[210:213], v2 offset:22016\n"
      "v_mfma_f32_16x16x32_bf16 v[100:103], a[248:251], v[110:113], v[100:103]\n"
      "ds_read_b128 v[140:143], v2 offset:30976\n"
      "v_mfma_f32_16x16x32_bf16 v[100:103], a[252:255], v[124:127], v[100:103]\n"
      "buffer_store_dwordx4 v[172:175], v20, s[24:27], s46 offen sc1\n"
      "v_mfma_f32_16x16x32_bf16 v[164:167], v[240:243], v[110:113], v[164:167]\n"
      "s_nop 0\n"
      "ds_read_b128 v[170:173], v2 offset:24832\n"
      "v_add_u32_e64 v76, s92, v115\n"
      "v_mfma_f32_16x16x32_bf16 v[164:167], v[244:247], v[124:127], v[164:167]\n"
      "s_waitcnt lgkmcnt(7)\n"
      "v_ashrrev_i32 v86, 31, v37\n"
      "v_and_b32 v189, 0x300, v78\n"
      "v_mfma_f32_16x16x32_bf16 v[148:151], a[232:235], v[156:159], v[148:151]\n"
      "v_rcp_f32_e64 v218, v218\n"
      "v_mfma_f32_16x16x32_bf16 v[148:151], a[236:239], v[160:163], v[148:151]\n"
      "ds_read_b128 v[110:113], v7 offset:26624\n"
      "v_cndmask_b32 v204, v204, v96, s[60:61]\n"
      "v_mfma_f32_16x16x32_bf16 v[100:103], v[224:227], v[156:159], v[100:103]\n"
      "ds_read_b128 v[124:127], v7 offset:22528\n"
      "v_mfma_f32_16x16x32_bf16 v[100:103], v[228:231], v[160:163], v[100:103]\n"
      "v_add_u32_e64 v78, v78, v189\n"
      "v_mfma_f32_16x16x32_bf16 v[164:167], v[248:251], v[156:159], v[164:167]\n"
      "v_add_u32 v78, 149504, v78\n"
      "v_mfma_f32_16x16x32_bf16 v[164:167], v[252:255], v[160:163], v[164:167]\n"
      "ds_read_b128 v[188:191], v78\n"
      "v_mfma_f32_16x16x32_bf16 v[156:159], a[216:219], v[214:217], 0\n"
      "s_waitcnt lgkmcnt(3)\n"
      "v_mul_f32 v204, v218, v204\n"
      "v_lshrrev_b32 v115, 4, v76\n"
      "v_mfma_f32_16x16x32_bf16 v[156:159], a[220:223], v[116:119], v[156:159]\n"
      "v_add_u32 v85, 0, v115\n"
      "v_fma_f32 v220, v148, v148, s52\n"
      "v_mfma_f32_16x16x32_bf16 v[160:163], a[240:243], v[214:217], 0\n"
      "s_waitcnt lgkmcnt(1)\n"
      "v_mul_f32 v221, v149, v149\n"
      "v_mfma_f32_16x16x32_bf16 v[160:163], a[244:247], v[116:119], v[160:163]\n"
      "ds_read_b128 v[96:99], v2 offset:18944\n"
      "v_mfma_f32_16x16x32_bf16 v[136:139], v[232:235], v[214:217], 0\n"
      "buffer_store_dwordx4 v[124:127], v21, s[24:27], s46 offen sc1\n"
      "v_mfma_f32_16x16x32_bf16 v[136:139], v[236:239], v[116:119], v[136:139]\n"
      "v_mfma_f32_16x16x32_bf16 v[156:159], a[224:227], v[170:173], v[156:159]\n"
      "v_fmac_f32 v221, v151, v151\n"
      "v_mfma_f32_16x16x32_bf16 v[156:159], a[228:231], v[196:199], v[156:159]\n"
      "ds_read_b128 v[216:219], v78 offset:8192\n"
      "v_fmac_f32 v220, v150, v150\n"
      "v_mfma_f32_16x16x32_bf16 v[160:163], a[248:251], v[170:173], v[160:163]\n"
      "ds_read_b128 v[106:109], v2 offset:28160\n"
      "v_fmac_f32_e64 v221, v101, v101\n"
      "v_mfma_f32_16x16x32_bf16 v[160:163], a[252:255], v[196:199], v[160:163]\n"
      "buffer_store_dwordx4 v[110:113], v22, s[24:27], s46 offen sc1\n"
      "v_mfma_f32_16x16x32_bf16 v[136:139], v[240:243], v[170:173], v[136:139]\n"
      "ds_read_b128 v[124:127], v78 offset:6144\n"
      "v_mfma_f32_16x16x32_bf16 v[136:139], v[244:247], v[196:199], v[136:139]\n"
      "ds_read_b128 v[172:175], v2 offset:25088\n"
      "v_cvt_pk_bf16_f32 v204, v204, v204\n"
      "v_mfma_f32_16x16x32_bf16 v[156:159], a[232:235], v[140:143], v[156:159]\n"
      "v_add_u32_e64 v111, 64, v115\n"
      "v_mfma_f32_16x16x32_bf16 v[156:159], a[236:239], v[192:195], v[156:159]\n"
      "ds_read_b128 v[144:147], v78 offset:10240\n"
      "v_mul_u32_u24 v179, 2731, v85\n"
      "v_mfma_f32_16x16x32_bf16 v[160:163], v[224:227], v[140:143], v[160:163]\n"
      "ds_write_b16 v12, v204 offset:16\n"
      "v_mfma_f32_16x16x32_bf16 v[160:163], v[228:231], v[192:195], v[160:163]\n"
      "v_mul_u32_u24 v80, 2731, v111\n"
      "v_mfma_f32_16x16x32_bf16 v[136:139], v[248:251], v[140:143], v[136:139]\n"
      "s_nop 0\n"
      "v_fma_f32 v170, v156, v156, s52\n"
      "v_mul_f32 v171, v157, v157\n"
      "v_mfma_f32_16x16x32_bf16 v[136:139], v[252:255], v[192:195], v[136:139]\n"
      "s_waitcnt lgkmcnt(2)\n"
      "v_lshrrev_b32 v80, 16, v80\n"
      "v_fmac_f32 v220, v100, v100\n"
      "v_mfma_f32_16x16x32_bf16 v[116:119], a[216:219], v[96:99], 0\n"
      "v_fmac_f32 v171, v159, v159\n"
      "v_fmac_f32 v220, v102, v102\n"
      "v_mfma_f32_16x16x32_bf16 v[116:119], a[220:223], v[210:213], v[116:119]\n"
      "v_fmac_f32 v170, v158, v158\n"
      "v_fmac_f32 v170, v160, v160\n"
      "v_mfma_f32_16x16x32_bf16 v[192:195], a[240:243], v[96:99], 0\n"
      "v_fmac_f32 v221, v103, v103\n"
      "v_fmac_f32_e64 v171, v161, v161\n"
      "v_mfma_f32_16x16x32_bf16 v[192:195], a[244:247], v[210:213], v[192:195]\n"
      "buffer_store_dwordx4 v[182:185], v23, s[24:27], s46 offen sc1\n"
      "v_mfma_f32_16x16x32_bf16 v[140:143], v[232:235], v[96:99], 0\n"
      "v_fmac_f32 v170, v162, v162\n"
      "v_fmac_f32 v220, v164, v164\n"
      "v_mfma_f32_16x16x32_bf16 v[140:143], v[236:239], v[210:213], v[140:143]\n"
      "v_subrev_u32 v110, 73728, v6\n"
      "v_fmac_f32 v221, v165, v165\n"
      "v_mfma_f32_16x16x32_bf16 v[116:119], a[224:227], v[172:175], v[116:119]\n"
      "ds_read_b128 v[212:215], v78 offset:2048\n"
      "v_lshrrev_b32 v179, 16, v179\n"
      "v_mfma_f32_16x16x32_bf16 v[116:119], a[228:231], v[106:109], v[116:119]\n"
      "v_fmac_f32_e64 v171, v163, v163\n"
      "v_and_b32 v86, 100270464, v86\n"
      "v_mfma_f32_16x16x32_bf16 v[192:195], a[248:251], v[172:175], v[192:195]\n"
      "ds_read_b128 v[196:199], v78 offset:4096\n"
      "v_fmac_f32 v171, v137, v137\n"
      "v_mfma_f32_16x16x32_bf16 v[192:195], a[252:255], v[106:109], v[192:195]\n"
      "v_fmac_f32 v170, v136, v136\n"
      "v_fmac_f32_e64 v170, v138, v138\n"
      "v_mfma_f32_16x16x32_bf16 v[140:143], v[240:243], v[172:175], v[140:143]\n"
      "v_fmac_f32 v171, v139, v139\n"
      "v_sub_u32 v208, v208, v86\n"
      "v_mfma_f32_16x16x32_bf16 v[140:143], v[244:247], v[106:109], v[140:143]\n"
      "ds_read_b128 v[96:99], v7 offset:34816\n"
      "v_fmac_f32 v221, v167, v167\n"
      "v_mfma_f32_16x16x32_bf16 v[116:119], a[232:235], v[130:133], v[116:119]\n"
      "v_subrev_u32 v77, 73728, v5\n"
      "v_add_f32 v170, v170, v171\n"
      "v_mfma_f32_16x16x32_bf16 v[116:119], a[236:239], v[200:203], v[116:119]\n"
      "v_fmac_f32 v220, v166, v166\n"
      "v_add_f32 v220, v220, v221\n"
      "v_mfma_f32_16x16x32_bf16 v[192:195], v[224:227], v[130:133], v[192:195]\n"
      "s_nop 0\n"
      "v_permlane16_swap_b32 v220, v170\n"
      "v_mfma_f32_16x16x32_bf16 v[192:195], v[228:231], v[200:203], v[192:195]\n"
      "s_nop 1\n"
      "v_fma_f32 v182, v116, v116, s52\n"
      "v_mfma_f32_16x16x32_bf16 v[140:143], v[248:251], v[130:133], v[140:143]\n"
      "v_mul_f32 v183, v117, v117\n"
      "v_fmac_f32_e64 v183, v119, v119\n"
      "v_mfma_f32_16x16x32_bf16 v[140:143], v[252:255], v[200:203], v[140:143]\n"
      "v_fmac_f32 v182, v118, v118\n"
      "v_fmac_f32 v182, v192, v192\n"
      "v_mfma_f32_16x16x32_bf16 v[106:109], a[216:219], v[188:191], 0\n"
      "s_waitcnt lgkmcnt(0)\n"
      "v_fmac_f32 v183, v193, v193\n"
      "v_fmac_f32 v182, v194, v194\n"
      "v_mfma_f32_16x16x32_bf16 v[106:109], a[220:223], v[212:215], v[106:109]\n"
      "v_add_f32 v220, v220, v170\n"
      "v_fmac_f32 v183, v195, v195\n"
      "v_mfma_f32_16x16x32_bf16 v[200:203], a[240:243], v[188:191], 0\n"
      "v_fmac_f32 v182, v140, v140\n"
      "v_fmac_f32 v183, v141, v141\n"
      "v_mfma_f32_16x16x32_bf16 v[200:203], a[244:247], v[212:215], v[200:203]\n"
      "v_fmac_f32 v182, v142, v142\n"
      "v_fmac_f32 v183, v143, v143\n"
      "v_mfma_f32_16x16x32_bf16 v[130:133], v[232:235], v[188:191], 0\n"
      "v_add_f32 v182, v182, v183\n"
      "v_mov_b32 v170, v182\n"
      "v_mfma_f32_16x16x32_bf16 v[130:133], v[236:239], v[212:215], v[130:133]\n"
      "s_nop 0\n"
      "v_permlane16_swap_b32_e64 v182, v170\n"
      "v_mfma_f32_16x16x32_bf16 v[106:109], a[224:227], v[196:199], v[106:109]\n"
      "v_add_f32 v182, v182, v170\n"
      "v_mfma_f32_16x16x32_bf16 v[106:109], a[228:231], v[124:127], v[106:109]\n"
      "s_nop 0\n"
      "v_permlane32_swap_b32_e64 v220, v182\n"
      "v_mfma_f32_16x16x32_bf16 v[200:203], a[248:251], v[196:199], v[200:203]\n"
      "v_add_f32_e64 v220, v220, v182\n"
      "v_mfma_f32_16x16x32_bf16 v[200:203], a[252:255], v[124:127], v[200:203]\n"
      "ds_write_b32 v31, v220\n"
      "v_mfma_f32_16x16x32_bf16 v[130:133], v[240:243], v[196:199], v[130:133]\n"
      "buffer_store_dwordx4 v[96:99], v25, s[24:27], s46 offen sc1\n"
      "v_mfma_f32_16x16x32_bf16 v[130:133], v[244:247], v[124:127], v[130:133]\n"
      "v_mfma_f32_16x16x32_bf16 v[106:109], a[232:235], v[216:219], v[106:109]\n"
      "v_readlane_b32 s46, v37, s42\n"
      "s_nop 0\n"
      "s_or_b32 s95, s46, s48\n"
      "v_mfma_f32_16x16x32_bf16 v[106:109], a[236:239], v[144:147], v[106:109]\n"
      "s_cmp_ge_u32 s95, 0x40000000\n"
      "s_cbranch_scc1 L_rare_35_%=\n"
      "L_back_36_%=:\n"
      "v_mfma_f32_16x16x32_bf16 v[200:203], v[224:227], v[216:219], v[200:203]\n"
      "s_waitcnt lgkmcnt(0)\n"
      "s_barrier\n"
      "ds_read_b128 v[124:127], v9\n"
      "v_mfma_f32_16x16x32_bf16 v[200:203], v[228:231], v[144:147], v[200:203]\n"
      "ds_read_b128 v[96:99], v7\n"
      "ds_read_b128 v[212:215], v78 offset:256\n"
      "ds_read_b128 v[182:185], v78 offset:2304\n"
      "v_mul_f32_e64 v177, v107, v107\n"
      "v_fma_f32 v176, v106, v106, s52\n"
      "v_mfma_f32_16x16x32_bf16 v[130:133], v[248:251], v[216:219], v[130:133]\n"
      "ds_read_b128 v[196:199], v78 offset:6400\n"
      "ds_read_b128 v[218:221], v7 offset:8192\n"
      "ds_read_b128 v[172:175], v78 offset:4352\n"
      "ds_read_b128 v[168:171], v7 offset:12288\n"
      "v_pk_fma_f32 v[176:177], v[108:109], v[108:109], v[176:177]\n"
      "v_mfma_f32_16x16x32_bf16 v[130:133], v[252:255], v[144:147], v[130:133]\n"
      "ds_read_b128 v[144:147], v7 offset:4096\n"
      "s_waitcnt lgkmcnt(8)\n"
      "v_add_f32 v126, v126, v127\n"
      "v_add_f32_e64 v124, v124, v125\n"
      "v_add_f32_e64 v124, v124, v126\n"
      "v_pk_fma_f32 v[176:177], v[200:201], v[200:201], v[176:177]\n"
      "v_pk_fma_f32 v[176:177], v[202:203], v[202:203], v[176:177]\n"
      "v_rsq_f32_e64 v124, v124\n"
      "v_pk_fma_f32 v[176:177], v[130:131], v[130:131], v[176:177]\n"
      "v_pk_fma_f32 v[176:177], v[132:133], v[132:133], v[176:177]\n"
      "s_waitcnt lgkmcnt(6)\n"
      "v_mfma_f32_16x16x32_bf16 v[112:115], a[216:219], v[212:215], 0\n"
      "s_waitcnt lgkmcnt(5)\n"
      "v_mul_f32 v124, 0x415db3d7, v124\n"
      "v_mov_b32_e64 v126, v124\n"
      "v_mfma_f32_16x16x32_bf16 v[112:115], a[220:223], v[182:185], v[112:115]\n"
      "s_nop 0\n"
      "v_permlane32_swap_b32 v124, v126\n"
      "v_mfma_f32_16x16x32_bf16 v[188:191], a[240:243], v[212:215], 0\n"
      "ds_read_b128 v[204:207], v78 offset:8448\n"
      "v_mov_b32 v125, v124\n"
      "v_mfma_f32_16x16x32_bf16 v[188:191], a[244:247], v[182:185], v[188:191]\n"
      "buffer_store_dwordx4 v[96:99], v20, s[24:27], s46 offen sc1\n"
      "v_pk_mul_f32 v[142:143], v[142:143], v[126:127] op_sel_hi:[1,0]\n"
      "s_nop 0\n"
      "v_mfma_f32_16x16x32_bf16 v[96:99], v[232:235], v[212:215], 0\n"
      "s_waitcnt lgkmcnt(1)\n"
      "v_permlane16_swap_b32 v124, v125\n"
      "v_mfma_f32_16x16x32_bf16 v[96:99], v[236:239], v[182:185], v[96:99]\n"
      "buffer_store_dwordx4 v[144:147], v21, s[24:27], s46 offen sc1\n"
      "v_pk_mul_f32 v[100:101], v[100:101], v[124:125] op_sel_hi:[1,0]\n"
      "v_pk_mul_f32 v[162:163], v[162:163], v[124:125] op_sel:[0,1]\n"
      "v_pk_mul_f32 v[160:161], v[160:161], v[124:125] op_sel:[0,1]\n"
      "v_pk_mul_f32 v[136:137], v[136:137], v[124:125] op_sel:[0,1]\n"
      "v_pk_mul_f32 v[156:157], v[156:157], v[124:125] op_sel:[0,1]\n"
      "v_pk_mul_f32 v[148:149], v[148:149], v[124:125] op_sel_hi:[1,0]\n"
      "v_pk_mul_f32 v[140:141], v[140:141], v[126:127] op_sel_hi:[1,0]\n"
      "v_pk_mul_f32 v[164:165], v[164:165], v[124:125] op_sel_hi:[1,0]\n"
      "v_cvt_pk_bf16_f32 v164, v164, v165\n"
      "v_pk_mul_f32 v[192:193], v[192:193], v[126:127] op_sel_hi:[1,0]\n"
      "v_pk_mul_f32 v[158:159], v[158:159], v[124:125] op_sel:[0,1]\n"
      "v_pk_mul_f32 v[138:139], v[138:139], v[124:125] op_sel:[0,1]\n"
      "v_pk_mul_f32 v[116:117], v[116:117], v[126:127] op_sel_hi:[1,0]\n"
      "v_pk_mul_f32 v[102:103], v[102:103], v[124:125] op_sel_hi:[1,0]\n"
      "v_pk_mul_f32 v[166:167], v[166:167], v[124:125] op_sel_hi:[1,0]\n"
      "v_mfma_f32_16x16x32_bf16 v[112:115], a[224:227], v[172:175], v[112:115]\n"
      "v_cvt_pk_bf16_f32 v165, v166, v167\n"
      "v_add_f32_e64 v176, v176, v177\n"
      "v_cvt_pk_bf16_f32 v148, v148, v149\n"
      "v_mfma_f32_16x16x32_bf16 v[112:115], a[228:231], v[196:199], v[112:115]\n"
      "s_waitcnt lgkmcnt(0)\n"
      "v_cvt_pk_bf16_f32 v156, v156, v157\n"
      "v_cvt_pk_bf16_f32 v136, v136, v137\n"
      "v_mfma_f32_16x16x32_bf16 v[188:191], a[248:251], v[172:175], v[188:191]\n"
      "ds_read_b128 v[214:217], v78 offset:10496\n"
      "v_cvt_pk_bf16_f32 v137, v138, v139\n"
      "v_mfma_f32_16x16x32_bf16 v[188:191], a[252:255], v[196:199], v[188:191]\n"
      "ds_write_b64 v6, v[164:165] offset:18432\n"
      "v_cvt_pk_bf16_f32 v157, v158, v159\n"
      "v_mfma_f32_16x16x32_bf16 v[96:99], v[240:243], v[172:175], v[96:99]\n"
      "buffer_store_dwordx4 v[218:221], v22, s[24:27], s46 offen sc1\n"
      "v_pk_mul_f32 v[150:151], v[150:151], v[124:125] op_sel_hi:[1,0]\n"
      "v_mfma_f32_16x16x32_bf16 v[96:99], v[244:247], v[196:199], v[96:99]\n"
      "ds_write_b64 v6, v[136:137] offset:24576\n"
      "v_mfma_f32_16x16x32_bf16 v[112:115], a[232:235], v[204:207], v[112:115]\n"
      "s_waitcnt lgkmcnt(2)\n"
      "v_cvt_pk_bf16_f32 v149, v150, v151\n"
      "v_cvt_pk_bf16_f32 v140, v140, v141\n"
      "v_mfma_f32_16x16x32_bf16 v[112:115], a[236:239], v[214:217], v[112:115]\n"
      "v_cvt_pk_bf16_f32 v159, v162, v163\n"
      "v_cvt_pk_bf16_f32 v158, v160, v161\n"
      "v_cvt_pk_bf16_f32 v150, v100, v101\n"
      "v_mfma_f32_16x16x32_bf16 v[188:191], v[224:227], v[204:207], v[188:191]\n"
      "ds_write_b128 v5, v[156:159] offset:24576\n"
      "v_cvt_pk_bf16_f32 v151, v102, v103\n"
      "v_mfma_f32_16x16x32_bf16 v[188:191], v[228:231], v[214:217], v[188:191]\n"
      "ds_read_b128 v[164:167], v7 offset:16384\n"
      "v_cvt_pk_bf16_f32 v141, v142, v143\n"
      "v_mfma_f32_16x16x32_bf16 v[96:99], v[248:251], v[204:207], v[96:99]\n"
      "ds_write_b64 v6, v[140:141] offset:30720\n"
      "v_mfma_f32_16x16x32_bf16 v[96:99], v[252:255], v[214:217], v[96:99]\n"
      "ds_write_b128 v5, v[148:151] offset:18432\n"
      "v_cvt_pk_bf16_f32 v116, v116, v117\n"
      "v_mul_f32_e64 v205, v113, v113\n"
      "v_pk_mul_f32 v[194:195], v[194:195], v[126:127] op_sel_hi:[1,0]\n"
      "v_fma_f32 v204, v112, v112, s52\n"
      "v_pk_fma_f32 v[204:205], v[114:115], v[114:115], v[204:205]\n"
      "v_pk_fma_f32 v[204:205], v[188:189], v[188:189], v[204:205]\n"
      "v_pk_mul_f32 v[118:119], v[118:119], v[126:127] op_sel_hi:[1,0]\n"
      "v_pk_fma_f32 v[204:205], v[190:191], v[190:191], v[204:205]\n"
      "v_pk_fma_f32 v[204:205], v[96:97], v[96:97], v[204:205]\n"
      "v_pk_fma_f32 v[204:205], v[98:99], v[98:99], v[204:205]\n"
      "v_add_f32 v204, v204, v205\n"
      "v_cvt_pk_bf16_f32 v117, v118, v119\n"
      "v_cvt_pk_bf16_f32 v118, v192, v193\n"
      "v_permlane16_swap_b32 v176, v204\n"
      "v_add_f32 v176, v176, v204\n"
      "v_mov_b32 v204, v176\n"
      "v_cvt_pk_bf16_f32 v119, v194, v195\n"
      "ds_write_b128 v5, v[116:119] offset:30720\n"
      "v_permlane32_swap_b32 v176, v204\n"
      "v_add_f32 v176, v176, v204\n"
      "s_add_u32 s42, s42, 1\n"
      "ds_write_b32 v31, v176 offset:4608\n"
      "buffer_store_dwordx4 v[168:171], v23, s[24:27], s46 offen sc1\n"
      "v_mul_u32_u24 v163, 24, v80\n"
      "v_mul_u32_u24 v140, 24, v179\n"
      "v_sub_u32 v163, v111, v163\n"
      "v_add_u32 v12, 32, v12\n"
      "v_bfe_i32 v86, v37, 30, 1\n"
      "s_waitcnt lgkmcnt(2)\n"
      "buffer_store_dwordx4 v[164:167], v25, s[24:27], s46 offen sc1\n"
      "v_readlane_b32 s46, v37, s42\n"
      "s_waitcnt lgkmcnt(0)\n"
      "s_barrier\n"
      "ds_read_b128 v[204:207], v9 offset:4608\n"
      "v_sub_u32_e64 v140, v85, v140\n"
      "ds_read_b128 v[168:171], v7 offset:18432\n"
      "v_and_b32 v86, 196224, v86\n"
      "v_sub_u32 v208, v208, v86\n"
      "v_and_b32_e64 v85, 7, v179\n"
      "ds_read_b128 v[142:145], v7 offset:26624\n"
      "s_or_b32 s95, s46, s48\n"
      "ds_read_b128 v[148:151], v7 offset:30720\n"
      "ds_read_b128 v[192:195], v7 offset:22528\n"
      "v_and_b32 v111, 7, v80\n"
      "s_cmp_ge_u32 s95, 0x40000000\n"
      "s_cbranch_scc1 L_rare_39_%=\n"
      "L_back_40_%=:\n"
      "s_waitcnt lgkmcnt(4)\n"
      "v_add_f32 v206, v206, v207\n"
      "v_add_f32_e64 v204, v204, v205\n"
      "v_add_f32_e64 v204, v204, v206\n"
      "v_rsq_f32_e64 v204, v204\n"
      "s_nop 0\n"
      "v_mul_f32 v204, 0x415db3d7, v204\n"
      "v_mov_b32 v206, v204\n"
      "v_lshl_add_u32 v179, v179, 2, 12\n"
      "s_nop 0\n"
      "v_permlane32_swap_b32_e64 v204, v206\n"
      "v_mov_b32 v205, v204\n"
      "v_lshl_add_u32 v80, v80, 2, 12\n"
      "s_nop 0\n"
      "v_permlane16_swap_b32_e64 v204, v205\n"
      "v_pk_mul_f32 v[106:107], v[106:107], v[204:205] op_sel_hi:[1,0]\n"
      "v_pk_mul_f32 v[96:97], v[96:97], v[204:205] op_sel:[0,1]\n"
      "v_pk_mul_f32 v[114:115], v[114:115], v[204:205] op_sel:[0,1]\n"
      "v_pk_mul_f32 v[108:109], v[108:109], v[204:205] op_sel_hi:[1,0]\n"
      "v_pk_mul_f32 v[200:201], v[200:201], v[204:205] op_sel_hi:[1,0]\n"
      "v_pk_mul_f32 v[190:191], v[190:191], v[204:205] op_sel:[0,1]\n"
      "v_pk_mul_f32 v[112:113], v[112:113], v[204:205] op_sel:[0,1]\n"
      "v_pk_mul_f32 v[98:99], v[98:99], v[204:205] op_sel:[0,1]\n"
      "v_pk_mul_f32 v[132:133], v[132:133], v[204:205] op_sel_hi:[1,0]\n"
      "v_pk_mul_f32 v[130:131], v[130:131], v[204:205] op_sel_hi:[1,0]\n"
      "v_cvt_pk_bf16_f32 v106, v106, v107\n"
      "v_cvt_pk_bf16_f32 v107, v108, v109\n"
      "v_cvt_pk_bf16_f32 v96, v96, v97\n"
      "v_pk_mul_f32 v[202:203], v[202:203], v[204:205] op_sel_hi:[1,0]\n"
      "v_cvt_pk_bf16_f32 v112, v112, v113\n"
      "v_cvt_pk_bf16_f32 v97, v98, v99\n"
      "v_cvt_pk_bf16_f32 v109, v202, v203\n"
      "v_pk_mul_f32 v[188:189], v[188:189], v[204:205] op_sel:[0,1]\n"
      "v_cvt_pk_bf16_f32 v130, v130, v131\n"
      "v_cvt_pk_bf16_f32 v113, v114, v115\n"
      "v_cvt_pk_bf16_f32 v131, v132, v133\n"
      "v_cvt_pk_bf16_f32 v108, v200, v201\n"
      "s_add_u32 s42, s42, 1\n"
      "ds_write_b128 v77, v[106:109]\n"
      "ds_write_b64 v110, v[96:97] offset:6144\n"
      "ds_write_b64 v110, v[130:131]\n"
      "v_cvt_pk_bf16_f32 v115, v190, v191\n"
      "v_cvt_pk_bf16_f32 v114, v188, v189\n"
      "s_waitcnt lgkmcnt(3)\n"
      "buffer_store_dwordx4 v[168:171], v20, s[24:27], s46 offen sc1\n"
      "ds_write_b128 v77, v[112:115] offset:6144\n"
      "ds_bpermute_b32 v191, v179, v208\n"
      "buffer_store_dwordx4 v[192:195], v21, s[24:27], s46 offen sc1\n"
      "v_xor_b32_e64 v163, v163, v111\n"
      "v_xor_b32 v140, v140, v85\n"
      "ds_bpermute_b32 v132, v80, v208\n"
      "ds_read_b128 v[168:171], v7 offset:34816\n"
      "buffer_store_dwordx4 v[142:145], v22, s[24:27], s46 offen sc1\n"
      "s_waitcnt lgkmcnt(3)\n"
      "s_barrier\n"
      "ds_read_b128 v[200:203], v76\n"
      "buffer_store_dwordx4 v[148:151], v23, s[24:27], s46 offen sc1\n"
      "ds_read_b128 v[112:115], v76 offset:1024\n"
      "s_waitcnt lgkmcnt(3)\n"
      "v_lshl_add_u32 v132, v163, 4, v132\n"
      "s_waitcnt lgkmcnt(2)\n"
      "buffer_store_dwordx4 v[168:171], v25, s[24:27], s46 offen sc1\n"
      "v_lshl_add_u32 v191, v140, 4, v191\n"
      "s_waitcnt lgkmcnt(1)\n"
      "buffer_store_dwordx4 v[200:203], v191, s[24:27], 0 offen sc1\n"
      "s_waitcnt lgkmcnt(0)\n"
      "buffer_store_dwordx4 v[112:115], v132, s[24:27], 0 offen sc1\n"
      "s_endpgm\n"
      "L_rare_1_%=:\n"
      "s_nop 7\n"
      "s_waitcnt lgkmcnt(0)\n"
      "s_add_u32 s72, s42, 1\n"
      "v_readlane_b32 s77, v38, s72\n"
      "v_subrev_u32 v103, 115456, v9\n"
      "s_and_b32 s73, s77, -16\n"
      "s_mov_b32 m0, 110592\n"
      "s_nop 0\n"
      "buffer_load_dwordx4 v103, s[28:31], s73 offen  lds\n"
      "s_add_u32 s73, s73, 1024\n"
      "s_mov_b32 m0, 111616\n"
      "s_nop 0\n"
      "buffer_load_dwordx4 v103, s[28:31], s73 offen  lds\n"
      "s_add_u32 s73, s73, 1024\n"
      "s_mov_b32 m0, 112640\n"
      "s_nop 0\n"
      "buffer_load_dwordx4 v103, s[28:31], s73 offen  lds\n"
      "s_add_u32 s73, s73, 1024\n"
      "s_mov_b32 m0, 113664\n"
      "s_nop 0\n"
      "buffer_load_dwordx4 v103, s[28:31], s73 offen  lds\n"
      "s_add_u32 s73, s73, 1024\n"
      "s_mov_b32 m0, 114688\n"
      "s_mov_b32 exec_hi, 0xffff\n"
      "buffer_load_dwordx4 v103, s[28:31], s73 offen  lds\n"
      "s_mov_b32 exec_hi, -1\n"
      "s_add_u32 s73, s73, 1024\n"
      "s_and_b32 s77, s77, 15\n"
      "s_waitcnt lgkmcnt(0)\n"
      "s_nop 4\n"
      "s_branch L_back_2_%=\n"
      "L_rare_3_%=:\n"
      "s_nop 7\n"
      "s_waitcnt lgkmcnt(0)\n"
      "s_waitcnt vmcnt(0)\n"
      "v_subrev_u32 v155, 115456, v9\n"
      "v_bfe_u32 v101, v155, 4, 4\n"
      "v_mul_u32_u24 v101, 98, v101\n"
      "v_lshrrev_b32 v155, 8, v155\n"
      "v_lshl_add_u32 v101, v155, 3, v101\n"
      "v_add_u32 v101, s77, v101\n"
      "v_and_b32 v154, 2, v101\n"
      "v_and_b32 v101, -4, v101\n"
      "v_add_u32 v101, 110592, v101\n"
      "ds_read2_b32 v[110:111], v101 offset0:0 offset1:1\n"
      "ds_read2_b32 v[136:137], v101 offset0:8 offset1:9\n"
      "ds_read2_b32 v[134:135], v101 offset0:16 offset1:17\n"
      "ds_read2_b32 v[102:103], v101 offset0:2 offset1:10\n"
      "ds_read2_b32 v[156:157], v101 offset0:18 offset1:24\n"
      "s_waitcnt lgkmcnt(4)\n"
      "v_alignbyte_b32 v110, v111, v110, v154\n"
      "s_waitcnt lgkmcnt(1)\n"
      "v_alignbyte_b32 v111, v102, v111, v154\n"
      "v_lshlrev_b32 v155, 16, v110\n"
      "v_fmac_f32 v150, s53, v155\n"
      "v_and_b32 v155, 0xffff0000, v110\n"
      "v_fmac_f32 v151, s53, v155\n"
      "v_lshlrev_b32 v155, 16, v111\n"
      "v_fmac_f32_e64 v152, s53, v155\n"
      "v_and_b32 v155, 0xffff0000, v111\n"
      "v_fmac_f32 v153, s53, v155\n"
      "v_alignbyte_b32 v136, v137, v136, v154\n"
      "v_alignbyte_b32 v137, v103, v137, v154\n"
      "v_lshlrev_b32 v155, 16, v136\n"
      "v_fmac_f32 v206, s53, v155\n"
      "v_and_b32 v155, 0xffff0000, v136\n"
      "v_fmac_f32 v207, s53, v155\n"
      "v_lshlrev_b32 v155, 16, v137\n"
      "v_fmac_f32 v208, s53, v155\n"
      "v_and_b32 v155, 0xffff0000, v137\n"
      "v_fmac_f32 v209, s53, v155\n"
      "v_alignbyte_b32 v134, v135, v134, v154\n"
      "s_waitcnt lgkmcnt(0)\n"
      "v_alignbyte_b32 v135, v156, v135, v154\n"
      "v_lshlrev_b32 v155, 16, v134\n"
      "v_fmac_f32 v192, s53, v155\n"
      "v_and_b32 v155, 0xffff0000, v134\n"
      "v_fmac_f32 v193, s53, v155\n"
      "v_lshlrev_b32 v155, 16, v135\n"
      "v_fmac_f32 v194, s53, v155\n"
      "v_and_b32 v155, 0xffff0000, v135\n"
      "v_fmac_f32 v195, s53, v155\n"
      "v_alignbyte_b32 v155, v157, v157, v154\n"
      "v_lshlrev_b32 v155, 16, v155\n"
      "v_fmac_f32 v140, s53, v155\n"
      "v_max3_f32 v101, v150, v151, v152\n"
      "v_max3_f32 v154, v153, v206, v207\n"
      "v_max3_f32 v101, v101, v208, v209\n"
      "v_max3_f32 v154, v154, v192, v193\n"
      "v_max3_f32 v101, v101, v194, v195\n"
      "v_max3_f32 v101, v101, v154, v140\n"
      "v_mov_b32 v155, v101\n"
      "s_nop 1\n"
      "v_permlane16_swap_b32 v101, v155\n"
      "v_max_f32 v101, v101, v155\n"
      "v_mov_b32 v155, v101\n"
      "s_nop 1\n"
      "v_permlane32_swap_b32 v101, v155\n"
      "v_max_f32 v101, v101, v155\n"
      "v_sub_f32 v150, v150, v101\n"
      "v_sub_f32 v151, v151, v101\n"
      "v_sub_f32 v152, v152, v101\n"
      "v_sub_f32 v153, v153, v101\n"
      "v_sub_f32 v206, v206, v101\n"
      "v_sub_f32 v207, v207, v101\n"
      "v_sub_f32 v208, v208, v101\n"
      "v_sub_f32 v209, v209, v101\n"
      "v_sub_f32 v192, v192, v101\n"
      "v_sub_f32 v193, v193, v101\n"
      "v_sub_f32 v194, v194, v101\n"
      "v_sub_f32 v195, v195, v101\n"
      "v_sub_f32_e64 v140, v140, v101\n"
      "s_waitcnt lgkmcnt(0)\n"
      "s_nop 4\n"
      "s_branch L_back_4_%=\n"
      "L_rare_5_%=:\n"
      "s_nop 7\n"
      "s_waitcnt lgkmcnt(0)\n"
      "s_waitcnt vmcnt(0)\n"
      "v_subrev_u32 v212, 115456, v9\n"
      "v_bfe_u32 v157, v212, 4, 4\n"
      "v_mul_u32_u24 v157, 98, v157\n"
      "v_lshrrev_b32 v212, 8, v212\n"
      "v_lshl_add_u32 v157, v212, 3, v157\n"
      "v_add_u32 v157, s77, v157\n"
      "v_and_b32 v78, 2, v157\n"
      "v_and_b32 v157, -4, v157\n"
      "v_add_u32 v157, 112160, v157\n"
      "ds_read2_b32 v[220:221], v157 offset0:0 offset1:1\n"
      "ds_read2_b32 v[218:219], v157 offset0:8 offset1:9\n"
      "ds_read2_b32 v[210:211], v157 offset0:16 offset1:17\n"
      "ds_read2_b32 v[158:159], v157 offset0:2 offset1:10\n"
      "ds_read2_b32 v[204:205], v157 offset0:18 offset1:24\n"
      "s_waitcnt lgkmcnt(4)\n"
      "v_alignbyte_b32 v220, v221, v220, v78\n"
      "s_waitcnt lgkmcnt(1)\n"
      "v_alignbyte_b32 v221, v158, v221, v78\n"
      "v_lshlrev_b32 v212, 16, v220\n"
      "v_fmac_f32 v166, s53, v212\n"
      "v_and_b32 v212, 0xffff0000, v220\n"
      "v_fmac_f32 v167, s53, v212\n"
      "v_lshlrev_b32 v212, 16, v221\n"
      "v_fmac_f32 v168, s53, v212\n"
      "v_and_b32 v212, 0xffff0000, v221\n"
      "v_fmac_f32 v169, s53, v212\n"
      "v_alignbyte_b32 v218, v219, v218, v78\n"
      "v_alignbyte_b32 v219, v159, v219, v78\n"
      "v_lshlrev_b32 v212, 16, v218\n"
      "v_fmac_f32 v144, s53, v212\n"
      "v_and_b32 v212, 0xffff0000, v218\n"
      "v_fmac_f32 v145, s53, v212\n"
      "v_lshlrev_b32 v212, 16, v219\n"
      "v_fmac_f32_e64 v146, s53, v212\n"
      "v_and_b32 v212, 0xffff0000, v219\n"
      "v_fmac_f32 v147, s53, v212\n"
      "v_alignbyte_b32 v210, v211, v210, v78\n"
      "s_waitcnt lgkmcnt(0)\n"
      "v_alignbyte_b32 v211, v204, v211, v78\n"
      "v_lshlrev_b32 v212, 16, v210\n"
      "v_fmac_f32 v134, s53, v212\n"
      "v_and_b32 v212, 0xffff0000, v210\n"
      "v_fmac_f32 v135, s53, v212\n"
      "v_lshlrev_b32 v212, 16, v211\n"
      "v_fmac_f32_e64 v136, s53, v212\n"
      "v_and_b32 v212, 0xffff0000, v211\n"
      "v_fmac_f32 v137, s53, v212\n"
      "v_alignbyte_b32 v212, v205, v205, v78\n"
      "v_lshlrev_b32 v212, 16, v212\n"
      "v_fmac_f32_e64 v202, s53, v212\n"
      "v_max3_f32 v157, v166, v167, v168\n"
      "v_max3_f32 v78, v169, v144, v145\n"
      "v_max3_f32 v157, v157, v146, v147\n"
      "v_max3_f32 v78, v78, v134, v135\n"
      "v_max3_f32 v157, v157, v136, v137\n"
      "v_max3_f32 v157, v157, v78, v202\n"
      "v_mov_b32 v212, v157\n"
      "s_nop 1\n"
      "v_permlane16_swap_b32 v157, v212\n"
      "v_max_f32 v157, v157, v212\n"
      "v_mov_b32 v212, v157\n"
      "s_nop 1\n"
      "v_permlane32_swap_b32 v157, v212\n"
      "v_max_f32 v157, v157, v212\n"
      "v_sub_f32 v166, v166, v157\n"
      "v_sub_f32 v167, v167, v157\n"
      "v_sub_f32 v168, v168, v157\n"
      "v_sub_f32 v169, v169, v157\n"
      "v_sub_f32 v144, v144, v157\n"
      "v_sub_f32 v145, v145, v157\n"
      "v_sub_f32 v146, v146, v157\n"
      "v_sub_f32 v147, v147, v157\n"
      "v_sub_f32 v134, v134, v157\n"
      "v_sub_f32 v135, v135, v157\n"
      "v_sub_f32 v136, v136, v157\n"
      "v_sub_f32 v137, v137, v157\n"
      "v_sub_f32_e64 v202, v202, v157\n"
      "s_waitcnt lgkmcnt(0)\n"
      "s_nop 4\n"
      "s_branch L_back_6_%=\n"
      "L_rare_7_%=:\n"
      "s_nop 7\n"
      "s_waitcnt lgkmcnt(0)\n"
      "s_waitcnt vmcnt(0)\n"
      "v_subrev_u32 v180, 115456, v9\n"
      "v_bfe_u32 v190, v180, 4, 4\n"
      "v_mul_u32_u24 v190, 98, v190\n"
      "v_lshrrev_b32_e64 v180, 8, v180\n"
      "v_lshl_add_u32 v190, v180, 3, v190\n"
      "v_add_u32 v190, s77, v190\n"
      "v_and_b32 v154, 2, v190\n"
      "v_and_b32_e64 v190, -4, v190\n"
      "v_add_u32 v190, 113728, v190\n"
      "ds_read2_b32 v[136:137], v190 offset0:0 offset1:1\n"
      "ds_read2_b32 v[100:101], v190 offset0:8 offset1:9\n"
      "ds_read2_b32 v[102:103], v190 offset0:16 offset1:17\n"
      "ds_read2_b32 v[138:139], v190 offset0:2 offset1:10\n"
      "ds_read2_b32 v[142:143], v190 offset0:18 offset1:24\n"
      "s_waitcnt lgkmcnt(4)\n"
      "v_alignbyte_b32 v136, v137, v136, v154\n"
      "s_waitcnt lgkmcnt(1)\n"
      "v_alignbyte_b32 v137, v138, v137, v154\n"
      "v_lshlrev_b32 v180, 16, v136\n"
      "v_fmac_f32 v156, s53, v180\n"
      "v_and_b32 v180, 0xffff0000, v136\n"
      "v_fmac_f32 v157, s53, v180\n"
      "v_lshlrev_b32 v180, 16, v137\n"
      "v_fmac_f32_e64 v158, s53, v180\n"
      "v_and_b32 v180, 0xffff0000, v137\n"
      "v_fmac_f32 v159, s53, v180\n"
      "v_alignbyte_b32 v100, v101, v100, v154\n"
      "v_alignbyte_b32 v101, v139, v101, v154\n"
      "v_lshlrev_b32 v180, 16, v100\n"
      "v_fmac_f32 v106, s53, v180\n"
      "v_and_b32 v180, 0xffff0000, v100\n"
      "v_fmac_f32 v107, s53, v180\n"
      "v_lshlrev_b32 v180, 16, v101\n"
      "v_fmac_f32 v108, s53, v180\n"
      "v_and_b32 v180, 0xffff0000, v101\n"
      "v_fmac_f32 v109, s53, v180\n"
      "v_alignbyte_b32 v102, v103, v102, v154\n"
      "s_waitcnt lgkmcnt(0)\n"
      "v_alignbyte_b32 v103, v142, v103, v154\n"
      "v_lshlrev_b32 v180, 16, v102\n"
      "v_fmac_f32 v204, s53, v180\n"
      "v_and_b32 v180, 0xffff0000, v102\n"
      "v_fmac_f32 v205, s53, v180\n"
      "v_lshlrev_b32 v180, 16, v103\n"
      "v_fmac_f32 v206, s53, v180\n"
      "v_and_b32 v180, 0xffff0000, v103\n"
      "v_fmac_f32 v207, s53, v180\n"
      "v_alignbyte_b32 v180, v143, v143, v154\n"
      "v_lshlrev_b32 v180, 16, v180\n"
      "v_fmac_f32 v208, s53, v180\n"
      "v_max3_f32 v190, v156, v157, v158\n"
      "v_max3_f32 v154, v159, v106, v107\n"
      "v_max3_f32 v190, v190, v108, v109\n"
      "v_max3_f32 v154, v154, v204, v205\n"
      "v_max3_f32 v190, v190, v206, v207\n"
      "v_max3_f32 v190, v190, v154, v208\n"
      "v_mov_b32 v180, v190\n"
      "s_nop 1\n"
      "v_permlane16_swap_b32 v190, v180\n"
      "v_max_f32 v190, v190, v180\n"
      "v_mov_b32 v180, v190\n"
      "s_nop 1\n"
      "v_permlane32_swap_b32 v190, v180\n"
      "v_max_f32 v190, v190, v180\n"
      "v_sub_f32 v156, v156, v190\n"
      "v_sub_f32 v157, v157, v190\n"
      "v_sub_f32 v158, v158, v190\n"
      "v_sub_f32 v159, v159, v190\n"
      "v_sub_f32 v106, v106, v190\n"
      "v_sub_f32 v107, v107, v190\n"
      "v_sub_f32 v108, v108, v190\n"
      "v_sub_f32 v109, v109, v190\n"
      "v_sub_f32 v204, v204, v190\n"
      "v_sub_f32 v205, v205, v190\n"
      "v_sub_f32 v206, v206, v190\n"
      "v_sub_f32 v207, v207, v190\n"
      "v_sub_f32 v208, v208, v190\n"
      "s_waitcnt lgkmcnt(0)\n"
      "s_nop 4\n"
      "s_branch L_back_8_%=\n"
      "L_rare_9_%=:\n"
      "s_nop 7\n"
      "s_waitcnt lgkmcnt(0)\n"
      "s_waitcnt vmcnt(0)\n"
      "v_subrev_u32 v88, 115456, v9\n"
      "v_lshrrev_b32 v88, 3, v88\n"
      "v_min_u32 v88, 96, v88\n"
      "v_add_u32 v88, s77, v88\n"
      "v_add_u32 v88, 115296, v88\n"
      "ds_read_u16 v88, v88\n"
      "s_waitcnt lgkmcnt(0)\n"
      "v_lshlrev_b32_e64 v88, 16, v88\n"
      "v_fmac_f32 v218, s53, v88\n"
      "s_nop 1\n"
      "v_max_f32_dpp v200, v218, v218 row_ror:8 row_mask:0xf bank_mask:0xf\n"
      "s_nop 1\n"
      "v_max_f32_dpp v200, v200, v200 row_ror:4 row_mask:0xf bank_mask:0xf\n"
      "s_nop 1\n"
      "v_max_f32_dpp v200, v200, v200 row_ror:2 row_mask:0xf bank_mask:0xf\n"
      "s_nop 1\n"
      "v_max_f32_dpp v200, v200, v200 row_ror:1 row_mask:0xf bank_mask:0xf\n"
      "s_nop 1\n"
      "v_max_f32_dpp v200, v200, v200 row_bcast:15 row_mask:0xa bank_mask:0xf\n"
      "s_nop 1\n"
      "v_max_f32_dpp v200, v200, v200 row_bcast:31 row_mask:0xc bank_mask:0xf\n"
      "s_nop 0\n"
      "v_readlane_b32 s72, v200, 63\n"
      "s_nop 1\n"
      "v_subrev_f32 v218, s72, v218\n"
      "s_waitcnt lgkmcnt(0)\n"
      "s_nop 4\n"
      "s_branch L_back_10_%=\n"
      "L_rare_27_%=:\n"
      "s_nop 7\n"
      "s_waitcnt lgkmcnt(0)\n"
      "s_bitcmp1_b32 s85, 31\n"
      "s_cselect_b32 s75, 100270464, 0\n"
      "s_bitcmp1_b32 s85, 30\n"
      "s_cselect_b32 s76, 196224, 0\n"
      "s_and_b32 s74, s85, 0x3fffffff\n"
      "s_cmp_ge_u32 s85, 0x40000000\n"
      "s_cselect_b32 s74, s74, 0\n"
      "s_cselect_b32 s85, 0, s85\n"
      "s_sub_u32 s74, s74, s69\n"
      "s_add_u32 s69, s69, s74\n"
      "s_sub_u32 s75, s75, s70\n"
      "s_add_u32 s70, s70, s75\n"
      "s_sub_u32 s76, s76, s71\n"
      "s_add_u32 s71, s71, s76\n"
      "v_add_u32 v20, s74, v20\n"
      "v_add_u32 v21, s74, v21\n"
      "v_add_u32 v22, s74, v22\n"
      "v_add_u32 v23, s74, v23\n"
      "v_add_u32 v25, s74, v25\n"
      "s_cmp_eq_u32 s75, 0\n"
      "s_cbranch_scc1 L_ax_25_%=\n"
      "v_bfe_i32 v220, v27, 10, 1\n"
      "v_and_b32 v220, s75, v220\n"
      "v_sub_u32 v20, v20, v220\n"
      "v_bfe_i32 v220, v27, 12, 1\n"
      "v_and_b32 v220, s75, v220\n"
      "v_sub_u32 v21, v21, v220\n"
      "v_bfe_i32 v220, v27, 14, 1\n"
      "v_and_b32 v220, s75, v220\n"
      "v_sub_u32 v22, v22, v220\n"
      "v_bfe_i32 v220, v27, 16, 1\n"
      "v_and_b32 v220, s75, v220\n"
      "v_sub_u32 v23, v23, v220\n"
      "v_bfe_i32 v220, v27, 18, 1\n"
      "v_and_b32 v220, s75, v220\n"
      "v_sub_u32 v25, v25, v220\n"
      "L_ax_25_%=:\n"
      "s_cmp_eq_u32 s76, 0\n"
      "s_cbranch_scc1 L_ax_26_%=\n"
      "v_bfe_i32 v220, v27, 11, 1\n"
      "v_and_b32 v220, s76, v220\n"
      "v_sub_u32_e64 v20, v20, v220\n"
      "v_bfe_i32 v220, v27, 13, 1\n"
      "v_and_b32 v220, s76, v220\n"
      "v_sub_u32 v21, v21, v220\n"
      "v_bfe_i32 v220, v27, 15, 1\n"
      "v_and_b32 v220, s76, v220\n"
      "v_sub_u32 v22, v22, v220\n"
      "v_bfe_i32 v220, v27, 17, 1\n"
      "v_and_b32 v220, s76, v220\n"
      "v_sub_u32 v23, v23, v220\n"
      "v_bfe_i32 v220, v27, 19, 1\n"
      "v_and_b32 v220, s76, v220\n"
      "v_sub_u32 v25, v25, v220\n"
      "L_ax_26_%=:\n"
      "s_or_b32 s48, s66, s69\n"
      "s_cmp_lg_u32 s48, 0\n"
      "s_cselect_b32 s48, 0x40000000, 0\n"
      "s_waitcnt lgkmcnt(0)\n"
      "s_nop 4\n"
      "s_branch L_back_28_%=\n"
      "L_rare_31_%=:\n"
      "s_nop 7\n"
      "s_waitcnt lgkmcnt(0)\n"
      "s_bitcmp1_b32 s85, 31\n"
      "s_cselect_b32 s75, 100270464, 0\n"
      "s_bitcmp1_b32 s85, 30\n"
      "s_cselect_b32 s76, 196224, 0\n"
      "s_and_b32 s74, s85, 0x3fffffff\n"
      "s_cmp_ge_u32 s85, 0x40000000\n"
      "s_cselect_b32 s74, s74, 0\n"
      "s_cselect_b32 s85, 0, s85\n"
      "s_sub_u32 s74, s74, s69\n"
      "s_add_u32 s69, s69, s74\n"
      "s_sub_u32 s75, s75, s70\n"
      "s_add_u32 s70, s70, s75\n"
      "s_sub_u32 s76, s76, s71\n"
      "s_add_u32 s71, s71, s76\n"
      "v_add_u32 v20, s74, v20\n"
      "v_add_u32 v21, s74, v21\n"
      "v_add_u32 v22, s74, v22\n"
      "v_add_u32 v23, s74, v23\n"
      "v_add_u32 v25, s74, v25\n"
      "s_cmp_eq_u32 s75, 0\n"
      "s_cbranch_scc1 L_ax_29_%=\n"
      "v_bfe_i32 v207, v27, 10, 1\n"
      "v_and_b32 v207, s75, v207\n"
      "v_sub_u32 v20, v20, v207\n"
      "v_bfe_i32 v207, v27, 12, 1\n"
      "v_and_b32 v207, s75, v207\n"
      "v_sub_u32 v21, v21, v207\n"
      "v_bfe_i32 v207, v27, 14, 1\n"
      "v_and_b32 v207, s75, v207\n"
      "v_sub_u32 v22, v22, v207\n"
      "v_bfe_i32 v207, v27, 16, 1\n"
      "v_and_b32 v207, s75, v207\n"
      "v_sub_u32 v23, v23, v207\n"
      "v_bfe_i32 v207, v27, 18, 1\n"
      "v_and_b32 v207, s75, v207\n"
      "v_sub_u32 v25, v25, v207\n"
      "L_ax_29_%=:\n"
      "s_cmp_eq_u32 s76, 0\n"
      "s_cbranch_scc1 L_ax_30_%=\n"
      "v_bfe_i32 v207, v27, 11, 1\n"
      "v_and_b32 v207, s76, v207\n"
      "v_sub_u32 v20, v20, v207\n"
      "v_bfe_i32 v207, v27, 13, 1\n"
      "v_and_b32 v207, s76, v207\n"
      "v_sub_u32 v21, v21, v207\n"
      "v_bfe_i32 v207, v27, 15, 1\n"
      "v_and_b32 v207, s76, v207\n"
      "v_sub_u32 v22, v22, v207\n"
      "v_bfe_i32 v207, v27, 17, 1\n"
      "v_and_b32 v207, s76, v207\n"
      "v_sub_u32 v23, v23, v207\n"
      "v_bfe_i32 v207, v27, 19, 1\n"
      "v_and_b32 v207, s76, v207\n"
      "v_sub_u32 v25, v25, v207\n"
      "L_ax_30_%=:\n"
      "s_or_b32 s48, s66, s69\n"
      "s_cmp_lg_u32 s48, 0\n"
      "s_cselect_b32 s48, 0x40000000, 0\n"
      "s_waitcnt lgkmcnt(0)\n"
      "s_nop 4\n"
      "s_branch L_back_32_%=\n"
      "L_rare_35_%=:\n"
      "s_nop 7\n"
      "s_waitcnt lgkmcnt(0)\n"
      "s_bitcmp1_b32 s46, 31\n"
      "s_cselect_b32 s75, 100270464, 0\n"
      "s_bitcmp1_b32 s46, 30\n"
      "s_cselect_b32 s76, 196224, 0\n"
      "s_and_b32 s74, s46, 0x3fffffff\n"
      "s_cmp_ge_u32 s46, 0x40000000\n"
      "s_cselect_b32 s74, s74, 0\n"
      "s_cselect_b32 s46, 0, s46\n"
      "s_sub_u32 s74, s74, s69\n"
      "s_add_u32 s69, s69, s74\n"
      "s_sub_u32 s75, s75, s70\n"
      "s_add_u32 s70, s70, s75\n"
      "s_sub_u32 s76, s76, s71\n"
      "s_add_u32 s71, s71, s76\n"
      "v_add_u32 v20, s74, v20\n"
      "v_add_u32 v21, s74, v21\n"
      "v_add_u32 v22, s74, v22\n"
      "v_add_u32 v23, s74, v23\n"
      "v_add_u32 v25, s74, v25\n"
      "s_cmp_eq_u32 s75, 0\n"
      "s_cbranch_scc1 L_ax_33_%=\n"
      "v_bfe_i32 v126, v27, 10, 1\n"
      "v_and_b32 v126, s75, v126\n"
      "v_sub_u32 v20, v20, v126\n"
      "v_bfe_i32 v126, v27, 12, 1\n"
      "v_and_b32 v126, s75, v126\n"
      "v_sub_u32 v21, v21, v126\n"
      "v_bfe_i32 v126, v27, 14, 1\n"
      "v_and_b32 v126, s75, v126\n"
      "v_sub_u32 v22, v22, v126\n"
      "v_bfe_i32 v126, v27, 16, 1\n"
      "v_and_b32 v126, s75, v126\n"
      "v_sub_u32 v23, v23, v126\n"
      "v_bfe_i32 v126, v27, 18, 1\n"
      "v_and_b32 v126, s75, v126\n"
      "v_sub_u32 v25, v25, v126\n"
      "L_ax_33_%=:\n"
      "s_cmp_eq_u32 s76, 0\n"
      "s_cbranch_scc1 L_ax_34_%=\n"
      "v_bfe_i32 v126, v27, 11, 1\n"
      "v_and_b32 v126, s76, v126\n"
      "v_sub_u32 v20, v20, v126\n"
      "v_bfe_i32 v126, v27, 13, 1\n"
      "v_and_b32 v126, s76, v126\n"
      "v_sub_u32 v21, v21, v126\n"
      "v_bfe_i32 v126, v27, 15, 1\n"
      "v_and_b32 v126, s76, v126\n"
      "v_sub_u32 v22, v22, v126\n"
      "v_bfe_i32 v126, v27, 17, 1\n"
      "v_and_b32 v126, s76, v126\n"
      "v_sub_u32 v23, v23, v126\n"
      "v_bfe_i32 v126, v27, 19, 1\n"
      "v_and_b32 v126, s76, v126\n"
      "v_sub_u32 v25, v25, v126\n"
      "L_ax_34_%=:\n"
      "s_or_b32 s48, s66, s69\n"
      "s_cmp_lg_u32 s48, 0\n"
      "s_cselect_b32 s48, 0x40000000, 0\n"
      "s_waitcnt lgkmcnt(0)\n"
      "s_nop 4\n"
      "s_branch L_back_36_%=\n"
      "L_rare_39_%=:\n"
      "s_nop 7\n"
      "s_waitcnt lgkmcnt(0)\n"
      "s_bitcmp1_b32 s46, 31\n"
      "s_cselect_b32 s75, 100270464, 0\n"
      "s_bitcmp1_b32 s46, 30\n"
      "s_cselect_b32 s76, 196224, 0\n"
      "s_and_b32 s74, s46, 0x3fffffff\n"
      "s_cmp_ge_u32 s46, 0x40000000\n"
      "s_cselect_b32 s74, s74, 0\n"
      "s_cselect_b32 s46, 0, s46\n"
      "s_sub_u32 s74, s74, s69\n"
      "s_add_u32 s69, s69, s74\n"
      "s_sub_u32 s75, s75, s70\n"
      "s_add_u32 s70, s70, s75\n"
      "s_sub_u32 s76, s76, s71\n"
      "s_add_u32 s71, s71, s76\n"
      "v_add_u32 v20, s74, v20\n"
      "v_add_u32 v21, s74, v21\n"
      "v_add_u32 v22, s74, v22\n"
      "v_add_u32 v23, s74, v23\n"
      "v_add_u32 v25, s74, v25\n"
      "s_cmp_eq_u32 s75, 0\n"
      "s_cbranch_scc1 L_ax_37_%=\n"
      "v_bfe_i32 v100, v27, 10, 1\n"
      "v_and_b32 v100, s75, v100\n"
      "v_sub_u32 v20, v20, v100\n"
      "v_bfe_i32 v100, v27, 12, 1\n"
      "v_and_b32 v100, s75, v100\n"
      "v_sub_u32 v21, v21, v100\n"
      "v_bfe_i32 v100, v27, 14, 1\n"
      "v_and_b32 v100, s75, v100\n"
      "v_sub_u32 v22, v22, v100\n"
      "v_bfe_i32 v100, v27, 16, 1\n"
      "v_and_b32 v100, s75, v100\n"
      "v_sub_u32 v23, v23, v100\n"
      "v_bfe_i32 v100, v27, 18, 1\n"
      "v_and_b32 v100, s75, v100\n"
      "v_sub_u32 v25, v25, v100\n"
      "L_ax_37_%=:\n"
      "s_cmp_eq_u32 s76, 0\n"
      "s_cbranch_scc1 L_ax_38_%=\n"
      "v_bfe_i32 v100, v27, 11, 1\n"
      "v_and_b32 v100, s76, v100\n"
      "v_sub_u32 v20, v20, v100\n"
      "v_bfe_i32 v100, v27, 13, 1\n"
      "v_and_b32 v100, s76, v100\n"
      "v_sub_u32 v21, v21, v100\n"
      "v_bfe_i32 v100, v27, 15, 1\n"
      "v_and_b32 v100, s76, v100\n"
      "v_sub_u32 v22, v22, v100\n"
      "v_bfe_i32 v100, v27, 17, 1\n"
      "v_and_b32 v100, s76, v100\n"
      "v_sub_u32 v23, v23, v100\n"
      "v_bfe_i32 v100, v27, 19, 1\n"
      "v_and_b32 v100, s76, v100\n"
      "v_sub_u32 v25, v25, v100\n"
      "L_ax_38_%=:\n"
      "s_or_b32 s48, s66, s69\n"
      "s_cmp_lg_u32 s48, 0\n"
      "s_cselect_b32 s48, 0x40000000, 0\n"
      "s_waitcnt lgkmcnt(0)\n"
      "s_nop 4\n"
      "s_branch L_back_40_%=\n"
      "L_cend_%=:\n"
      :
      : "s"(__builtin_amdgcn_kernarg_segment_ptr()), "s"(wave), "s"(blockIdx.x), "s"(CT_TAB), "s"(lds0)
      : "v0","v1","v2","v3","v4","v5","v6","v7","v8","v9","v10","v11","v12","v13","v14","v15","v16","v17","v18","v19","v20","v21","v22","v23","v24","v25","v26","v27","v28","v29","v30","v31","v32","v33","v34","v35","v36","v37","v38","v39","v40","v41","v42","v43","v44","v45","v46","v47","v48","v49","v50","v51","v52","v53","v54","v55","v56","v57","v58","v59","v60","v61","v62","v63","v64","v65","v66","v67","v68","v69","v70","v71","v72","v73","v74","v75","v76","v77","v78","v79","v80","v81","v82","v83","v84","v85","v86","v87","v88","v89","v90","v91","v92","v93","v94","v95","v96","v97","v98","v99","v100","v101","v102","v103","v104","v105","v106","v107","v108","v109","v110","v111","v112","v113","v114","v115","v116","v117","v118","v119","v120","v121","v122","v123","v124","v125","v126","v127","v128","v129","v130","v131","v132","v133","v134","v135","v136","v137","v138","v139","v140","v141","v142","v143","v144","v145","v146","v147","v148","v149","v150","v151","v152","v153","v154","v155","v156","v157","v158","v159","v160","v161","v162","v163","v164","v165","v166","v167","v168","v169","v170","v171","v172","v173","v174","v175","v176","v177","v178","v179","v180","v181","v182","v183","v184","v185","v186","v187","v188","v189","v190","v191","v192","v193","v194","v195","v196","v197","v198","v199","v200","v201","v202","v203","v204","v205","v206","v207","v208","v209","v210","v211","v212","v213","v214","v215","v216","v217","v218","v219","v220","v221","v222","v223","v224","v225","v226","v227","v228","v229","v230","v231","v232","v233","v234","v235","v236","v237","v238","v239","v240","v241","v242","v243","v244","v245","v246","v247","v248","v249","v250","v251","v252","v253","v254","v255","a0","a1","a2","a3","a4","a5","a6","a7","a8","a9","a10","a11","a12","a13","a14","a15","a16","a17","a18","a19","a20","a21","a22","a23","a24","a25","a26","a27","a28","a29","a30","a31","a32","a33","a34","a35","a36","a37","a38","a39","a40","a41","a42","a43","a44","a45","a46","a47","a48","a49","a50","a51","a52","a53","a54","a55","a56","a57","a58","a59","a60","a61","a62","a63","a64","a65","a66","a67","a68","a69","a70","a71","a72","a73","a74","a75","a76","a77","a78","a79","a80","a81","a82","a83","a84","a85","a86","a87","a88","a89","a90","a91","a92","a93","a94","a95","a96","a97","a98","a99","a100","a101","a102","a103","a104","a105","a106","a107","a108","a109","a110","a111","a112","a113","a114","a115","a116","a117","a118","a119","a120","a121","a122","a123","a124","a125","a126","a127","a128","a129","a130","a131","a132","a133","a134","a135","a136","a137","a138","a139","a140","a141","a142","a143","a144","a145","a146","a147","a148","a149","a150","a151","a152","a153","a154","a155","a156","a157","a158","a159","a160","a161","a162","a163","a164","a165","a166","a167","a168","a169","a170","a171","a172","a173","a174","a175","a176","a177","a178","a179","a180","a181","a182","a183","a184","a185","a186","a187","a188","a189","a190","a191","a192","a193","a194","a195","a196","a197","a198","a199","a200","a201","a202","a203","a204","a205","a206","a207","a208","a209","a210","a211","a212","a213","a214","a215","a216","a217","a218","a219","a220","a221","a222","a223","a224","a225","a226","a227","a228","a229","a230","a231","a232","a233","a234","a235","a236","a237","a238","a239","a240","a241","a242","a243","a244","a245","a246","a247","a248","a249","a250","a251","a252","a253","a254","a255","s16","s17","s18","s19","s20","s21","s22","s23","s24","s25","s26","s27","s28","s29","s30","s31","s33","s34","s35","s36","s37","s38","s39","s40","s41","s42","s43","s44","s45","s46","s47","s48","s49","s50","s51","s52","s53","s54","s55","s56","s57","s58","s59","s60","s61","s62","s63","s64","s65","s66","s67","s68","s69","s70","s71","s72","s73","s74","s75","s76","s77","s78","s79","s80","s81","s82","s83","s84","s85","s86","s87","s88","s89","s90","s91","s92","s93","s94","s95","s96","s97","s98","s99","s100","s101", "vcc", "scc", "memory");
}

}

extern "C" void kernel_launch(void* const* d_in, const int* in_sizes, int n_in, void* d_out, int out_size, void* d_ws,
                              size_t ws_size, hipStream_t stream) {
  const uint16_t* x = (const uint16_t*)d_in[0];
  const uint16_t* wqkv = (const uint16_t*)d_in[1];
  const uint16_t* wo = (const uint16_t*)d_in[2];
  const uint16_t* ls = (const uint16_t*)d_in[3];
  const uint16_t* w1 = (const uint16_t*)d_in[4];
  const uint16_t* w2 = (const uint16_t*)d_in[5];
  const uint16_t* rc = (const uint16_t*)d_in[6];
  const int* ridx = (const int*)d_in[7];
  const uint16_t* mask = (const uint16_t*)d_in[8];
  swin::swin_main<<<256, 256, 0, stream>>>(x, mask, (char*)d_ws, (uint16_t*)d_out, wqkv, wo, ls, w1, w2, rc, ridx);
}
```

```cpp
#include <hip/hip_runtime.h>
#include <hip/hip_bf16.h>
#include <stdint.h>

namespace swin {

constexpr unsigned CT_WORDS = 8192u;
constexpr unsigned LDS_BYTES = 163840u;

__device__ __attribute__((aligned(256))) const unsigned CT_TAB[CT_WORDS] = {0x4800u,0x4820u,0x4840u,0x4800u,0x4808u,0x4828u,0x4848u,0x4810u,0x4810u,0x4830u,0x4850u,0x4820u,0x4818u,0x4838u,0x4858u,0x4830u,0x4880u,0x48a0u,0x48c0u,0x4880u,0x4888u,0x48a8u,0x48c8u,0x4890u,0x4890u,0x48b0u,0x48d0u,0x48a0u,0x4898u,0x48b8u,0x48d8u,0x48b0u,0x4920u,0x4900u,0x4960u,0x4920u,0x4928u,0x4908u,0x4968u,0x4930u,0x4930u,0x4910u,0x4970u,0x4900u,0x4938u,0x4918u,0x4978u,0x4910u,0x49a0u,0x4980u,0x49e0u,0x49a0u,0x49a8u,0x4988u,0x49e8u,0x49b0u,0x49b0u,0x4990u,0x49f0u,0x4980u,0x49b8u,0x4998u,0x49f8u,0x4990u,0x4c40u,0x4c60u,0x4c00u,0x4a00u,0x4c48u,0x4c68u,0x4c08u,0x4a10u,0x4c50u,0x4c70u,0x4c10u,0x4a20u,0x4c58u,0x4c78u,0x4c18u,0x4a30u,0x4cc0u,0x4ce0u,0x4c80u,0x4a80u,0x4cc8u,0x4ce8u,0x4c88u,0x4a90u,0x4cd0u,0x4cf0u,0x4c90u,0x4aa0u,0x4cd8u,0x4cf8u,0x4c98u,0x4ab0u,0x4d60u,0x4d40u,0x4d20u,0x4b20u,0x4d68u,0x4d48u,0x4d28u,0x4b30u,0x4d70u,0x4d50u,0x4d30u,0x4b00u,0x4d78u,0x4d58u,0x4d38u,0x4b10u,0x4de0u,0x4dc0u,0x4da0u,0x4ba0u,0x4de8u,0x4dc8u,0x4da8u,0x4bb0u,0x4df0u,0x4dd0u,0x4db0u,0x4b80u,0x4df8u,0x4dd8u,0x4db8u,0x4b90u,0x5000u,0x5020u,0x5040u,0x4c40u,0x5008u,0x5028u,0x5048u,0x4c50u,0x5010u,0x5030u,0x5050u,0x4c60u,0x5018u,0x5038u,0x5058u,0x4c70u,0x5080u,0x50a0u,0x50c0u,0x4cc0u,0x5088u,0x50a8u,0x50c8u,0x4cd0u,0x5090u,0x50b0u,0x50d0u,0x4ce0u,0x5098u,0x50b8u,0x50d8u,0x4cf0u,0x5120u,0x5100u,0x5160u,0x4d60u,0x5128u,0x5108u,0x5168u,0x4d70u,0x5130u,0x5110u,0x5170u,0x4d40u,0x5138u,0x5118u,0x5178u,0x4d50u,0x51a0u,0x5180u,0x51e0u,0x4de0u,0x51a8u,0x5188u,0x51e8u,0x4df0u,0x51b0u,0x5190u,0x51f0u,0x4dc0u,0x51b8u,0x5198u,0x51f8u,0x4dd0u,0x5440u,0x5460u,0x5400u,0x4e40u,0x5448u,0x5468u,0x5408u,0x4e50u,0x5450u,0x5470u,0x5410u,0x4e60u,0x5458u,0x5478u,0x5418u,0x4e70u,0x54c0u,0x54e0u,0x5480u,0x4ec0u,0x54c8u,0x54e8u,0x5488u,0x4ed0u,0x54d0u,0x54f0u,0x5490u,0x4ee0u,0x54d8u,0x54f8u,0x5498u,0x4ef0u,0x5560u,0x5540u,0x5520u,0x4f60u,0x5568u,0x5548u,0x5528u,0x4f70u,0x5570u,0x5550u,0x5530u,0x4f40u,0x5578u,0x5558u,0x5538u,0x4f50u,0x55e0u,0x55c0u,0x55a0u,0x4fe0u,0x55e8u,0x55c8u,0x55a8u,0x4ff0u,0x55f0u,0x55d0u,0x55b0u,0x4fc0u,0x55f8u,0x55d8u,0x55b8u,0x4fd0u,0x4840u,0x1e800u,0x1eb00u,0x0u,0x4848u,0x1ec90u,0x1ef90u,0x10u,0x4850u,0x1f120u,0x1f420u,0x20u,0x4858u,0x1f5b0u,0x1f8b0u,0x30u,0x48c0u,0x1fa40u,0x1fd40u,0x40u,0x48c8u,0x1fed0u,0x201d0u,0x50u,0x48d0u,0x20360u,0x20660u,0x60u,0x48d8u,0x207f0u,0x20af0u,0x70u,0x4960u,0x20c80u,0x20f80u,0x180u,0x4968u,0x21110u,0x21410u,0x190u,0x4970u,0x215a0u,0x218a0u,0x1a0u,0x4978u,0x21a30u,0x21d30u,0x1b0u,0x49e0u,0x21ec0u,0x221c0u,0x1c0u,0x49e8u,0x22350u,0x22650u,0x1d0u,0x49f0u,0x227e0u,0x22ae0u,0x1e0u,0x49f8u,0x22c70u,0x22f70u,0x1f0u,0x4a40u,0x1e830u,0x1eb08u,0x320u,0x4a48u,0x1ecc0u,0x1ef98u,0x330u,0x4a50u,0x1f150u,0x1f428u,0x300u,0x4a58u,0x1f5e0u,0x1f8b8u,0x310u,0x4ac0u,0x1fa70u,0x1fd48u,0x360u,0x4ac8u,0x1ff00u,0x201d8u,0x370u,0x4ad0u,0x20390u,0x20668u,0x340u,0x4ad8u,0x20820u,0x20af8u,0x350u,0x4b60u,0x20cb0u,0x20f88u,0x4a0u,0x4b68u,0x21140u,0x21418u,0x4b0u,0x4b70u,0x215d0u,0x218a8u,0x480u,0x4b78u,0x21a60u,0x21d38u,0x490u,0x4be0u,0x21ef0u,0x221c8u,0x4e0u,0x4be8u,0x22380u,0x22658u,0x4f0u,0x4bf0u,0x22810u,0x22ae8u,0x4c0u,0x4bf8u,0x22ca0u,0x22f78u,0x4d0u,0x4c00u,0x1e860u,0x1eb10u,0x600u,0x4c08u,0x1ecf0u,0x1efa0u,0x610u,0x4c10u,0x1f180u,0x1f430u,0x620u,0x4c18u,0x1f610u,0x1f8c0u,0x630u,0x4c80u,0x1faa0u,0x1fd50u,0x640u,0x4c88u,0x1ff30u,0x201e0u,0x650u,0x4c90u,0x203c0u,0x20670u,0x660u,0x4c98u,0x20850u,0x20b00u,0x670u,0x4d20u,0x20ce0u,0x20f90u,0x780u,0x4d28u,0x21170u,0x21420u,0x790u,0x4d30u,0x21600u,0x218b0u,0x7a0u,0x4d38u,0x21a90u,0x21d40u,0x7b0u,0x4da0u,0x21f20u,0x221d0u,0x7c0u,0x4da8u,0x223b0u,0x22660u,0x7d0u,0x4db0u,0x22840u,0x22af0u,0x7e0u,0x4db8u,0x22cd0u,0x22f80u,0x7f0u,0x4e00u,0x1e890u,0x1eb18u,0x920u,0x4e08u,0x1ed20u,0x1efa8u,0x930u,0x4e10u,0x1f1b0u,0x1f438u,0x900u,0x4e18u,0x1f640u,0x1f8c8u,0x910u,0x4e80u,0x1fad0u,0x1fd58u,0x960u,0x4e88u,0x1ff60u,0x201e8u,0x970u,0x4e90u,0x203f0u,0x20678u,0x940u,0x4e98u,0x20880u,0x20b08u,0x950u,0x4f20u,0x20d10u,0x20f98u,0xaa0u,0x4f28u,0x211a0u,0x21428u,0xab0u,0x4f30u,0x21630u,0x218b8u,0xa80u,0x4f38u,0x21ac0u,0x21d48u,0xa90u,0x4fa0u,0x21f50u,0x221d8u,0xae0u,0x4fa8u,0x223e0u,0x22668u,0xaf0u,0x4fb0u,0x22870u,0x22af8u,0xac0u,0x4fb8u,0x22d00u,0x22f88u,0xad0u,0x0u,0x40u,0x9000u,0x9000u,0x80u,0xc0u,0x9010u,0x9010u,0x110u,0x150u,0x9020u,0x9020u,0x190u,0x1d0u,0x9030u,0x9030u,0x220u,0x260u,0x9040u,0x9040u,0x2a0u,0x2e0u,0x9050u,0x9050u,0x330u,0x370u,0x9060u,0x9060u,0x3b0u,0x3f0u,0x9070u,0x9070u,0x440u,0x400u,0x9080u,0x9080u,0x4c0u,0x480u,0x9090u,0x9090u,0x550u,0x510u,0x90a0u,0x90a0u,0x5d0u,0x590u,0x90b0u,0x90b0u,0x660u,0x620u,0x90c0u,0x90c0u,0x6e0u,0x6a0u,0x90d0u,0x90d0u,0x770u,0x730u,0x90e0u,0x90e0u,0x7f0u,0x7b0u,0x90f0u,0x90f0u,0x10u,0x50u,0x9300u,0x9300u,0x90u,0xd0u,0x9310u,0x9310u,0x100u,0x140u,0x9320u,0x9320u,0x180u,0x1c0u,0x9330u,0x9330u,0x230u,0x270u,0x9340u,0x9340u,0x2b0u,0x2f0u,0x9350u,0x9350u,0x320u,0x360u,0x9360u,0x9360u,0x3a0u,0x3e0u,0x9370u,0x9370u,0x450u,0x410u,0x9380u,0x9380u,0x4d0u,0x490u,0x9390u,0x9390u,0x540u,0x500u,0x93a0u,0x93a0u,0x5c0u,0x580u,0x93b0u,0x93b0u,0x670u,0x630u,0x93c0u,0x93c0u,0x6f0u,0x6b0u,0x93d0u,0x93d0u,0x760u,0x720u,0x93e0u,0x93e0u,0x7e0u,0x7a0u,0x93f0u,0x93f0u,0x20u,0x60u,0x9600u,0x9600u,0xa0u,0xe0u,0x9610u,0x9610u,0x130u,0x170u,0x9620u,0x9620u,0x1b0u,0x1f0u,0x9630u,0x9630u,0x200u,0x240u,0x9640u,0x9640u,0x280u,0x2c0u,0x9650u,0x9650u,0x310u,0x350u,0x9660u,0x9660u,0x390u,0x3d0u,0x9670u,0x9670u,0x460u,0x420u,0x9680u,0x9680u,0x4e0u,0x4a0u,0x9690u,0x9690u,0x570u,0x530u,0x96a0u,0x96a0u,0x5f0u,0x5b0u,0x96b0u,0x96b0u,0x640u,0x600u,0x96c0u,0x96c0u,0x6c0u,0x680u,0x96d0u,0x96d0u,0x750u,0x710u,0x96e0u,0x96e0u,0x7d0u,0x790u,0x96f0u,0x96f0u,0x30u,0x70u,0x9900u,0x9900u,0xb0u,0xf0u,0x9910u,0x9910u,0x120u,0x160u,0x9920u,0x9920u,0x1a0u,0x1e0u,0x9930u,0x9930u,0x210u,0x250u,0x9940u,0x9940u,0x290u,0x2d0u,0x9950u,0x9950u,0x300u,0x340u,0x9960u,0x9960u,0x380u,0x3c0u,0x9970u,0x9970u,0x470u,0x430u,0x9980u,0x9980u,0x4f0u,0x4b0u,0x9990u,0x9990u,0x560u,0x520u,0x99a0u,0x99a0u,0x5e0u,0x5a0u,0x99b0u,0x99b0u,0x650u,0x610u,0x99c0u,0x99c0u,0x6d0u,0x690u,0x99d0u,0x99d0u,0x740u,0x700u,0x99e0u,0x99e0u,0x7c0u,0x780u,0x99f0u,0x99f0u,0x9c00u,0x12000u,0x12040u,0x12000u,0x9c10u,0x12190u,0x121d0u,0x12010u,0x9c20u,0x12320u,0x12360u,0x12020u,0x9c30u,0x124b0u,0x124f0u,0x12030u,0x9c40u,0x12640u,0x12600u,0x12040u,0x9c50u,0x127d0u,0x12790u,0x12050u,0x9c60u,0x12960u,0x12920u,0x12060u,0x9c70u,0x12af0u,0x12ab0u,0x12070u,0x9c80u,0x12c00u,0x12c40u,0x12080u,0x9c90u,0x12d90u,0x12dd0u,0x12090u,0x9ca0u,0x12f20u,0x12f60u,0x120a0u,0x9cb0u,0x130b0u,0x130f0u,0x120b0u,0x9cc0u,0x13240u,0x13200u,0x120c0u,0x9cd0u,0x133d0u,0x13390u,0x120d0u,0x9ce0u,0x13560u,0x13520u,0x120e0u,0x9cf0u,0x136f0u,0x136b0u,0x120f0u,0x9f00u,0x12010u,0x12048u,0x12100u,0x9f10u,0x12180u,0x121d8u,0x12110u,0x9f20u,0x12330u,0x12368u,0x12120u,0x9f30u,0x124a0u,0x124f8u,0x12130u,0x9f40u,0x12650u,0x12608u,0x12140u,0x9f50u,0x127c0u,0x12798u,0x12150u,0x9f60u,0x12970u,0x12928u,0x12160u,0x9f70u,0x12ae0u,0x12ab8u,0x12170u,0x9f80u,0x12c10u,0x12c48u,0x12180u,0x9f90u,0x12d80u,0x12dd8u,0x12190u,0x9fa0u,0x12f30u,0x12f68u,0x121a0u,0x9fb0u,0x130a0u,0x130f8u,0x121b0u,0x9fc0u,0x13250u,0x13208u,0x121c0u,0x9fd0u,0x133c0u,0x13398u,0x121d0u,0x9fe0u,0x13570u,0x13528u,0x121e0u,0x9ff0u,0x136e0u,0x136b8u,0x121f0u,0xa200u,0x12020u,0x12050u,0x12200u,0xa210u,0x121b0u,0x121c0u,0x12210u,0xa220u,0x12300u,0x12370u,0x12220u,0xa230u,0x12490u,0x124e0u,0x12230u,0xa240u,0x12660u,0x12610u,0x12240u,0xa250u,0x127f0u,0x12780u,0x12250u,0xa260u,0x12940u,0x12930u,0x12260u,0xa270u,0x12ad0u,0x12aa0u,0x12270u,0xa280u,0x12c20u,0x12c50u,0x12280u,0xa290u,0x12db0u,0x12dc0u,0x12290u,0xa2a0u,0x12f00u,0x12f70u,0x122a0u,0xa2b0u,0x13090u,0x130e0u,0x122b0u,0xa2c0u,0x13260u,0x13210u,0x122c0u,0xa2d0u,0x133f0u,0x13380u,0x122d0u,0xa2e0u,0x13540u,0x13530u,0x122e0u,0xa2f0u,0x136d0u,0x136a0u,0x122f0u,0xa500u,0x12030u,0x12058u,0x12300u,0xa510u,0x121a0u,0x121c8u,0x12310u,0xa520u,0x12310u,0x12378u,0x12320u,0xa530u,0x12480u,0x124e8u,0x12330u,0xa540u,0x12670u,0x12618u,0x12340u,0xa550u,0x127e0u,0x12788u,0x12350u,0xa560u,0x12950u,0x12938u,0x12360u,0xa570u,0x12ac0u,0x12aa8u,0x12370u,0xa580u,0x12c30u,0x12c58u,0x12380u,0xa590u,0x12da0u,0x12dc8u,0x12390u,0xa5a0u,0x12f10u,0x12f78u,0x123a0u,0xa5b0u,0x13080u,0x130e8u,0x123b0u,0xa5c0u,0x13270u,0x13218u,0x123c0u,0xa5d0u,0x133e0u,0x13388u,0x123d0u,0xa5e0u,0x13550u,0x13538u,0x123e0u,0xa5f0u,0x136c0u,0x136a8u,0x123f0u,0x4800u,0x1c300u,0x1e800u,0x1eb00u,0x4880u,0x1c310u,0x100000u,0x1eb02u,0x4910u,0x1c320u,0x100000u,0x1eb04u,0x4990u,0x1c330u,0x100000u,0x1eb06u,0x4a20u,0x1c340u,0x1e800u,0x1eb08u,0x4aa0u,0x1c350u,0x100000u,0x1eb0au,0x4b30u,0x1c360u,0x100000u,0x1eb0cu,0x4bb0u,0x1c370u,0x100000u,0x1eb0eu,0x4c40u,0x1c380u,0x1e800u,0x1eb10u,0x4cc0u,0x1c390u,0x100000u,0x1eb12u,0x4d50u,0x1c3a0u,0x100000u,0x1eb14u,0x4dd0u,0x1c3b0u,0x100000u,0x1eb16u,0x4e60u,0x1c3c0u,0x1e800u,0x1eb18u,0x4ee0u,0x1c3d0u,0x100000u,0x1eb1au,0x4f70u,0x1c3e0u,0x100000u,0x1eb1cu,0x4ff0u,0x1c3f0u,0x100000u,0x1eb1eu,0x4810u,0x1c400u,0x1e830u,0x1eb00u,0x4890u,0x1c410u,0x100000u,0x1eb02u,0x4900u,0x1c420u,0x100000u,0x1eb04u,0x4980u,0x1c430u,0x100000u,0x1eb06u,0x4a30u,0x1c440u,0x1e830u,0x1eb08u,0x4ab0u,0x1c450u,0x100000u,0x1eb0au,0x4b20u,0x1c460u,0x100000u,0x1eb0cu,0x4ba0u,0x1c470u,0x100000u,0x1eb0eu,0x4c50u,0x1c480u,0x1e830u,0x1eb10u,0x4cd0u,0x1c490u,0x100000u,0x1eb12u,0x4d40u,0x1c4a0u,0x100000u,0x1eb14u,0x4dc0u,0x1c4b0u,0x100000u,0x1eb16u,0x4e70u,0x1c4c0u,0x1e830u,0x1eb18u,0x4ef0u,0x1c4d0u,0x100000u,0x1eb1au,0x4f60u,0x1c4e0u,0x100000u,0x1eb1cu,0x4fe0u,0x1c4f0u,0x100000u,0x1eb1eu,0x4820u,0x1c500u,0x1e860u,0x1eb00u,0x48a0u,0x1c510u,0x100000u,0x1eb02u,0x4930u,0x1c520u,0x100000u,0x1eb04u,0x49b0u,0x1c530u,0x100000u,0x1eb06u,0x4a00u,0x1c540u,0x1e860u,0x1eb08u,0x4a80u,0x1c550u,0x100000u,0x1eb0au,0x4b10u,0x1c560u,0x100000u,0x1eb0cu,0x4b90u,0x1c570u,0x100000u,0x1eb0eu,0x4c60u,0x1c580u,0x1e860u,0x1eb10u,0x4ce0u,0x1c590u,0x100000u,0x1eb12u,0x4d70u,0x1c5a0u,0x100000u,0x1eb14u,0x4df0u,0x1c5b0u,0x100000u,0x1eb16u,0x4e40u,0x1c5c0u,0x1e860u,0x1eb18u,0x4ec0u,0x1c5d0u,0x100000u,0x1eb1au,0x4f50u,0x1c5e0u,0x100000u,0x1eb1cu,0x4fd0u,0x1c5f0u,0x100000u,0x1eb1eu,0x4830u,0x1c600u,0x1e890u,0x1eb00u,0x48b0u,0x1c610u,0x100000u,0x1eb02u,0x4920u,0x1c620u,0x100000u,0x1eb04u,0x49a0u,0x1c630u,0x100000u,0x1eb06u,0x4a10u,0x1c640u,0x1e890u,0x1eb08u,0x4a90u,0x1c650u,0x100000u,0x1eb0au,0x4b00u,0x1c660u,0x100000u,0x1eb0cu,0x4b80u,0x1c670u,0x100000u,0x1eb0eu,0x4c70u,0x1c680u,0x1e890u,0x1eb10u,0x4cf0u,0x1c690u,0x100000u,0x1eb12u,0x4d60u,0x1c6a0u,0x100000u,0x1eb14u,0x4de0u,0x1c6b0u,0x100000u,0x1eb16u,0x4e50u,0x1c6c0u,0x1e890u,0x1eb18u,0x4ed0u,0x1c6d0u,0x100000u,0x1eb1au,0x4f40u,0x1c6e0u,0x100000u,0x1eb1cu,0x4fc0u,0x1c6f0u,0x100000u,0x1eb1eu,0x24800u,0x1d300u,0x1d300u,0xc0100u,0x24802u,0x1d302u,0x100000u,0xc0110u,0x24804u,0x1d304u,0x100000u,0xc0120u,0x24806u,0x1d306u,0x100000u,0xc0130u,0x24a00u,0x1d310u,0x1d300u,0xc0140u,0x24a02u,0x1d312u,0x100000u,0xc0150u,0x24a04u,0x1d314u,0x100000u,0xc0160u,0x24a06u,0x1d316u,0x100000u,0xc0170u,0x24c00u,0x1d320u,0x1d300u,0xc0280u,0x24c02u,0x1d322u,0x100000u,0xc0290u,0x24c04u,0x1d324u,0x100000u,0xc02a0u,0x24c06u,0x1d326u,0x100000u,0xc02b0u,0x24e00u,0x1d330u,0x1d300u,0xc02c0u,0x24e02u,0x1d332u,0x100000u,0xc02d0u,0x24e04u,0x1d334u,0x100000u,0xc02e0u,0x24e06u,0x1d336u,0x100000u,0xc02f0u,0x24808u,0x1d308u,0x1d310u,0xc0410u,0x2480au,0x1d30au,0x100000u,0xc0400u,0x2480cu,0x1d30cu,0x100000u,0xc0430u,0x2480eu,0x1d30eu,0x100000u,0xc0420u,0x24a08u,0x1d318u,0x1d310u,0xc0450u,0x24a0au,0x1d31au,0x100000u,0xc0440u,0x24a0cu,0x1d31cu,0x100000u,0xc0470u,0x24a0eu,0x1d31eu,0x100000u,0xc0460u,0x24c08u,0x1d328u,0x1d310u,0xef990u,0x24c0au,0x1d32au,0x100000u,0xef980u,0x24c0cu,0x1d32cu,0x100000u,0xef9b0u,0x24c0eu,0x1d32eu,0x100000u,0xef9a0u,0x24e08u,0x1d338u,0x1d310u,0xef9d0u,0x24e0au,0x1d33au,0x100000u,0xef9c0u,0x24e0cu,0x1d33cu,0x100000u,0xef9f0u,0x24e0eu,0x1d33eu,0x100000u,0xef9e0u,0x25000u,0x1d340u,0x1d320u,0xefb20u,0x25002u,0x1d342u,0x100000u,0xefb30u,0x25004u,0x1d344u,0x100000u,0xefb00u,0x25006u,0x1d346u,0x100000u,0xefb10u,0x25200u,0x1d350u,0x1d320u,0xefb60u,0x25202u,0x1d352u,0x100000u,0xefb70u,0x25204u,0x1d354u,0x100000u,0xefb40u,0x25206u,0x1d356u,0x100000u,0xefb50u,0x25400u,0x1d360u,0x1d320u,0xefca0u,0x25402u,0x1d362u,0x100000u,0xefcb0u,0x25404u,0x1d364u,0x100000u,0xefc80u,0x25406u,0x1d366u,0x100000u,0xefc90u,0x25600u,0x1d370u,0x1d320u,0xefce0u,0x25602u,0x1d372u,0x100000u,0xefcf0u,0x25604u,0x1d374u,0x100000u,0xefcc0u,0x25606u,0x1d376u,0x100000u,0xefcd0u,0x100000u,0x1d348u,0x1d330u,0xefe30u,0x100000u,0x1d34au,0x100000u,0xefe20u,0x100000u,0x1d34cu,0x100000u,0xefe10u,0x100000u,0x1d34eu,0x100000u,0xefe00u,0x100000u,0x1d358u,0x1d330u,0xefe70u,0x100000u,0x1d35au,0x100000u,0xefe60u,0x100000u,0x1d35cu,0x100000u,0xefe50u,0x100000u,0x1d35eu,0x100000u,0xefe40u,0x100000u,0x1d368u,0x1d330u,0xeffb0u,0x100000u,0x1d36au,0x100000u,0xeffa0u,0x100000u,0x1d36cu,0x100000u,0xeff90u,0x100000u,0x1d36eu,0x100000u,0xeff80u,0x100000u,0x1d378u,0x1d330u,0xefff0u,0x100000u,0x1d37au,0x100000u,0xeffe0u,0x100000u,0x1d37cu,0x100000u,0xeffd0u,0x100000u,0x1d37eu,0x100000u,0xeffc0u,0x0u,0x30420u,0x8fc50u,0xc0000u,0x10u,0x30430u,0x8fc40u,0xc0010u,0x20u,0x30400u,0x8fc70u,0xc0020u,0x30u,0x30410u,0x8fc60u,0xc0030u,0x40u,0x30460u,0x8fc10u,0xc0040u,0x50u,0x30470u,0x8fc00u,0xc0050u,0x60u,0x30440u,0x8fc30u,0xc0060u,0x70u,0x30450u,0x8fc20u,0xc0070u,0x80u,0x304b0u,0x8fcd0u,0xc0080u,0x90u,0x304a0u,0x8fcc0u,0xc0090u,0xa0u,0x30490u,0x8fcf0u,0xc00a0u,0xb0u,0x30480u,0x8fce0u,0xc00b0u,0xc0u,0x304f0u,0x8fc90u,0xc00c0u,0xd0u,0x304e0u,0x8fc80u,0xc00d0u,0xe0u,0x304d0u,0x8fcb0u,0xc00e0u,0xf0u,0x304c0u,0x8fca0u,0xc00f0u,0x100u,0x30530u,0x8fd60u,0xc0100u,0x110u,0x30520u,0x8fd70u,0xc0110u,0x120u,0x30510u,0x8fd40u,0xc0120u,0x130u,0x30500u,0x8fd50u,0xc0130u,0x140u,0x30570u,0x8fd20u,0xc0140u,0x150u,0x30560u,0x8fd30u,0xc0150u,0x160u,0x30550u,0x8fd00u,0xc0160u,0x170u,0x30540u,0x8fd10u,0xc0170u,0x190u,0x305b0u,0x8fde0u,0xc0190u,0x180u,0x305a0u,0x8fdf0u,0xc0180u,0x1b0u,0x30590u,0x8fdc0u,0xc01b0u,0x1a0u,0x30580u,0x8fdd0u,0xc01a0u,0x1d0u,0x305f0u,0x8fda0u,0xc01d0u,0x1c0u,0x305e0u,0x8fdb0u,0xc01c0u,0x1f0u,0x305d0u,0x8fd80u,0xc01f0u,0x1e0u,0x305c0u,0x8fd90u,0xc01e0u,0x210u,0x30640u,0x8fe60u,0xc0210u,0x200u,0x30650u,0x8fe70u,0xc0200u,0x230u,0x30660u,0x8fe40u,0xc0230u,0x220u,0x30670u,0x8fe50u,0xc0220u,0x250u,0x30600u,0x8fe20u,0xc0250u,0x240u,0x30610u,0x8fe30u,0xc0240u,0x270u,0x30620u,0x8fe00u,0xc0270u,0x260u,0x30630u,0x8fe10u,0xc0260u,0x290u,0x306c0u,0x8fef0u,0xc0290u,0x280u,0x306d0u,0x8fee0u,0xc0280u,0x2b0u,0x306e0u,0x8fed0u,0xc02b0u,0x2a0u,0x306f0u,0x8fec0u,0xc02a0u,0x2d0u,0x30680u,0x8feb0u,0xc02d0u,0x2c0u,0x30690u,0x8fea0u,0xc02c0u,0x2f0u,0x306a0u,0x8fe90u,0xc02f0u,0x2e0u,0x306b0u,0x8fe80u,0xc02e0u,0x320u,0x30740u,0x8ff70u,0xc0320u,0x330u,0x30750u,0x8ff60u,0xc0330u,0x300u,0x30760u,0x8ff50u,0xc0300u,0x310u,0x30770u,0x8ff40u,0xc0310u,0x360u,0x30700u,0x8ff30u,0xc0360u,0x370u,0x30710u,0x8ff20u,0xc0370u,0x340u,0x30720u,0x8ff10u,0xc0340u,0x350u,0x30730u,0x8ff00u,0xc0350u,0x3a0u,0x307d0u,0x8fff0u,0xc03a0u,0x3b0u,0x307c0u,0x8ffe0u,0xc03b0u,0x380u,0x307f0u,0x8ffd0u,0xc0380u,0x390u,0x307e0u,0x8ffc0u,0xc0390u,0x3e0u,0x30790u,0x8ffb0u,0xc03e0u,0x3f0u,0x30780u,0x8ffa0u,0xc03f0u,0x3c0u,0x307b0u,0x8ff90u,0xc03c0u,0x3d0u,0x307a0u,0x8ff80u,0xc03d0u,0x0u,0x11f820u,0x0u,0x7030cu,0x0u,0x11f830u,0x0u,0x7030cu,0x0u,0x11f800u,0x0u,0x7030cu,0x0u,0x11f810u,0x0u,0x7030cu,0x0u,0x11f860u,0x0u,0x7030cu,0x0u,0x11f870u,0x0u,0x7030cu,0x0u,0x11f840u,0x0u,0x7030cu,0x0u,0x11f850u,0x0u,0x7030cu,0x0u,0x11f8b0u,0x0u,0x7230cu,0x0u,0x11f8a0u,0x0u,0x7230cu,0x0u,0x11f890u,0x0u,0x7230cu,0x0u,0x11f880u,0x0u,0x7230cu,0x0u,0x11f8f0u,0x0u,0x7230cu,0x0u,0x11f8e0u,0x0u,0x7230cu,0x0u,0x11f8d0u,0x0u,0x7230cu,0x0u,0x11f8c0u,0x0u,0x7230cu,0x0u,0x11f930u,0x0u,0x7232cu,0x0u,0x11f920u,0x0u,0x7232cu,0x0u,0x11f910u,0x0u,0x7232cu,0x0u,0x11f900u,0x0u,0x7232cu,0x0u,0x11f970u,0x0u,0x7232cu,0x0u,0x11f960u,0x0u,0x7232cu,0x0u,0x11f950u,0x0u,0x7232cu,0x0u,0x11f940u,0x0u,0x7232cu,0x0u,0x11f9b0u,0x0u,0x72124u,0x0u,0x11f9a0u,0x0u,0x72124u,0x0u,0x11f990u,0x0u,0x72124u,0x0u,0x11f980u,0x0u,0x72124u,0x0u,0x11f9f0u,0x0u,0x72124u,0x0u,0x11f9e0u,0x0u,0x72124u,0x0u,0x11f9d0u,0x0u,0x72124u,0x0u,0x11f9c0u,0x0u,0x72124u,0x0u,0x11fa40u,0x0u,0x721a6u,0x0u,0x11fa50u,0x0u,0x721a6u,0x0u,0x11fa60u,0x0u,0x721a6u,0x0u,0x11fa70u,0x0u,0x721a6u,0x0u,0x11fa00u,0x0u,0x721a6u,0x0u,0x11fa10u,0x0u,0x721a6u,0x0u,0x11fa20u,0x0u,0x721a6u,0x0u,0x11fa30u,0x0u,0x721a6u,0x0u,0x11fac0u,0x0u,0x72186u,0x0u,0x11fad0u,0x0u,0x72186u,0x0u,0x11fae0u,0x0u,0x72186u,0x0u,0x11faf0u,0x0u,0x72186u,0x0u,0x11fa80u,0x0u,0x72186u,0x0u,0x11fa90u,0x0u,0x72186u,0x0u,0x11faa0u,0x0u,0x72186u,0x0u,0x11fab0u,0x0u,0x72186u,0x0u,0x11fb40u,0x0u,0x72186u,0x0u,0x11fb50u,0x0u,0x72186u,0x0u,0x11fb60u,0x0u,0x72186u,0x0u,0x11fb70u,0x0u,0x72186u,0x0u,0x11fb00u,0x0u,0x72186u,0x0u,0x11fb10u,0x0u,0x72186u,0x0u,0x11fb20u,0x0u,0x72186u,0x0u,0x11fb30u,0x0u,0x72186u,0x0u,0x11fbd0u,0x0u,0x7230cu,0x0u,0x11fbc0u,0x0u,0x7230cu,0x0u,0x11fbf0u,0x0u,0x7230cu,0x0u,0x11fbe0u,0x0u,0x7230cu,0x0u,0x11fb90u,0x0u,0x7230cu,0x0u,0x11fb80u,0x0u,0x7230cu,0x0u,0x11fbb0u,0x0u,0x7230cu,0x0u,0x11fba0u,0x0u,0x7230cu,0x5800u,0x5820u,0x4860u,0x4860u,0x5808u,0x5828u,0x4868u,0x4870u,0x5810u,0x5830u,0x4870u,0x5800u,0x5818u,0x5838u,0x4878u,0x5810u,0x5880u,0x58a0u,0x48e0u,0x48e0u,0x5888u,0x58a8u,0x48e8u,0x48f0u,0x5890u,0x58b0u,0x48f0u,0x5880u,0x5898u,0x58b8u,0x48f8u,0x5890u,0x5920u,0x5900u,0x4940u,0x4940u,0x5928u,0x5908u,0x4948u,0x4950u,0x5930u,0x5910u,0x4950u,0x5920u,0x5938u,0x5918u,0x4958u,0x5930u,0x59a0u,0x5980u,0x49c0u,0x49c0u,0x59a8u,0x5988u,0x49c8u,0x49d0u,0x59b0u,0x5990u,0x49d0u,0x59a0u,0x59b8u,0x5998u,0x49d8u,0x59b0u,0x5c40u,0x5c60u,0x4c20u,0x4a60u,0x5c48u,0x5c68u,0x4c28u,0x4a70u,0x5c50u,0x5c70u,0x4c30u,0x5a00u,0x5c58u,0x5c78u,0x4c38u,0x5a10u,0x5cc0u,0x5ce0u,0x4ca0u,0x4ae0u,0x5cc8u,0x5ce8u,0x4ca8u,0x4af0u,0x5cd0u,0x5cf0u,0x4cb0u,0x5a80u,0x5cd8u,0x5cf8u,0x4cb8u,0x5a90u,0x5d60u,0x5d40u,0x4d00u,0x4b40u,0x5d68u,0x5d48u,0x4d08u,0x4b50u,0x5d70u,0x5d50u,0x4d10u,0x5b20u,0x5d78u,0x5d58u,0x4d18u,0x5b30u,0x5de0u,0x5dc0u,0x4d80u,0x4bc0u,0x5de8u,0x5dc8u,0x4d88u,0x4bd0u,0x5df0u,0x5dd0u,0x4d90u,0x5ba0u,0x5df8u,0x5dd8u,0x4d98u,0x5bb0u,0x6000u,0x6020u,0x5060u,0x4c20u,0x6008u,0x6028u,0x5068u,0x4c30u,0x6010u,0x6030u,0x5070u,0x5c40u,0x6018u,0x6038u,0x5078u,0x5c50u,0x6080u,0x60a0u,0x50e0u,0x4ca0u,0x6088u,0x60a8u,0x50e8u,0x4cb0u,0x6090u,0x60b0u,0x50f0u,0x5cc0u,0x6098u,0x60b8u,0x50f8u,0x5cd0u,0x6120u,0x6100u,0x5140u,0x4d00u,0x6128u,0x6108u,0x5148u,0x4d10u,0x6130u,0x6110u,0x5150u,0x5d60u,0x6138u,0x6118u,0x5158u,0x5d70u,0x61a0u,0x6180u,0x51c0u,0x4d80u,0x61a8u,0x6188u,0x51c8u,0x4d90u,0x61b0u,0x6190u,0x51d0u,0x5de0u,0x61b8u,0x6198u,0x51d8u,0x5df0u,0x6440u,0x6460u,0x5420u,0x4e20u,0x6448u,0x6468u,0x5428u,0x4e30u,0x6450u,0x6470u,0x5430u,0x5e40u,0x6458u,0x6478u,0x5438u,0x5e50u,0x64c0u,0x64e0u,0x54a0u,0x4ea0u,0x64c8u,0x64e8u,0x54a8u,0x4eb0u,0x64d0u,0x64f0u,0x54b0u,0x5ec0u,0x64d8u,0x64f8u,0x54b8u,0x5ed0u,0x6560u,0x6540u,0x5500u,0x4f00u,0x6568u,0x6548u,0x5508u,0x4f10u,0x6570u,0x6550u,0x5510u,0x5f60u,0x6578u,0x6558u,0x5518u,0x5f70u,0x65e0u,0x65c0u,0x5580u,0x4f80u,0x65e8u,0x65c8u,0x5588u,0x4f90u,0x65f0u,0x65d0u,0x5590u,0x5fe0u,0x65f8u,0x65d8u,0x5598u,0x5ff0u,0x5820u,0x1e8c0u,0x1eb60u,0xc40u,0x5828u,0x1ed50u,0x1eff0u,0xc50u,0x5830u,0x1f1e0u,0x1f480u,0xc60u,0x5838u,0x1f670u,0x1f910u,0xc70u,0x58a0u,0x1fb00u,0x1fda0u,0xc00u,0x58a8u,0x1ff90u,0x20230u,0xc10u,0x58b0u,0x20420u,0x206c0u,0xc20u,0x58b8u,0x208b0u,0x20b50u,0xc30u,0x5900u,0x20d40u,0x20fe0u,0xdc0u,0x5908u,0x211d0u,0x21470u,0xdd0u,0x5910u,0x21660u,0x21900u,0xde0u,0x5918u,0x21af0u,0x21d90u,0xdf0u,0x5980u,0x21f80u,0x22220u,0xd80u,0x5988u,0x22410u,0x226b0u,0xd90u,0x5990u,0x228a0u,0x22b40u,0xda0u,0x5998u,0x22d30u,0x22fd0u,0xdb0u,0x5a20u,0x1e8f0u,0x1eb68u,0xf60u,0x5a28u,0x1ed80u,0x1eff8u,0xf70u,0x5a30u,0x1f210u,0x1f488u,0xf40u,0x5a38u,0x1f6a0u,0x1f918u,0xf50u,0x5aa0u,0x1fb30u,0x1fda8u,0xf20u,0x5aa8u,0x1ffc0u,0x20238u,0xf30u,0x5ab0u,0x20450u,0x206c8u,0xf00u,0x5ab8u,0x208e0u,0x20b58u,0xf10u,0x5b00u,0x20d70u,0x20fe8u,0x10e0u,0x5b08u,0x21200u,0x21478u,0x10f0u,0x5b10u,0x21690u,0x21908u,0x10c0u,0x5b18u,0x21b20u,0x21d98u,0x10d0u,0x5b80u,0x21fb0u,0x22228u,0x10a0u,0x5b88u,0x22440u,0x226b8u,0x10b0u,0x5b90u,0x228d0u,0x22b48u,0x1080u,0x5b98u,0x22d60u,0x22fd8u,0x1090u,0x5c60u,0x1e920u,0x1eb70u,0x1240u,0x5c68u,0x1edb0u,0x1f000u,0x1250u,0x5c70u,0x1f240u,0x1f490u,0x1260u,0x5c78u,0x1f6d0u,0x1f920u,0x1270u,0x5ce0u,0x1fb60u,0x1fdb0u,0x1200u,0x5ce8u,0x1fff0u,0x20240u,0x1210u,0x5cf0u,0x20480u,0x206d0u,0x1220u,0x5cf8u,0x20910u,0x20b60u,0x1230u,0x5d40u,0x20da0u,0x20ff0u,0x13c0u,0x5d48u,0x21230u,0x21480u,0x13d0u,0x5d50u,0x216c0u,0x21910u,0x13e0u,0x5d58u,0x21b50u,0x21da0u,0x13f0u,0x5dc0u,0x21fe0u,0x22230u,0x1380u,0x5dc8u,0x22470u,0x226c0u,0x1390u,0x5dd0u,0x22900u,0x22b50u,0x13a0u,0x5dd8u,0x22d90u,0x22fe0u,0x13b0u,0x5e60u,0x1e950u,0x1eb78u,0x1560u,0x5e68u,0x1ede0u,0x1f008u,0x1570u,0x5e70u,0x1f270u,0x1f498u,0x1540u,0x5e78u,0x1f700u,0x1f928u,0x1550u,0x5ee0u,0x1fb90u,0x1fdb8u,0x1520u,0x5ee8u,0x20020u,0x20248u,0x1530u,0x5ef0u,0x204b0u,0x206d8u,0x1500u,0x5ef8u,0x20940u,0x20b68u,0x1510u,0x5f40u,0x20dd0u,0x20ff8u,0x16e0u,0x5f48u,0x21260u,0x21488u,0x16f0u,0x5f50u,0x216f0u,0x21918u,0x16c0u,0x5f58u,0x21b80u,0x21da8u,0x16d0u,0x5fc0u,0x22010u,0x22238u,0x16a0u,0x5fc8u,0x224a0u,0x226c8u,0x16b0u,0x5fd0u,0x22930u,0x22b58u,0x1680u,0x5fd8u,0x22dc0u,0x22fe8u,0x1690u,0x0u,0x40u,0x9000u,0xa800u,0x80u,0xc0u,0x9010u,0xa810u,0x110u,0x150u,0x9020u,0xa820u,0x190u,0x1d0u,0x9030u,0xa830u,0x220u,0x260u,0x9040u,0xa840u,0x2a0u,0x2e0u,0x9050u,0xa850u,0x330u,0x370u,0x9060u,0xa860u,0x3b0u,0x3f0u,0x9070u,0xa870u,0x440u,0x400u,0x9080u,0xa880u,0x4c0u,0x480u,0x9090u,0xa890u,0x550u,0x510u,0x90a0u,0xa8a0u,0x5d0u,0x590u,0x90b0u,0xa8b0u,0x660u,0x620u,0x90c0u,0xa8c0u,0x6e0u,0x6a0u,0x90d0u,0xa8d0u,0x770u,0x730u,0x90e0u,0xa8e0u,0x7f0u,0x7b0u,0x90f0u,0xa8f0u,0x10u,0x50u,0x9300u,0xab00u,0x90u,0xd0u,0x9310u,0xab10u,0x100u,0x140u,0x9320u,0xab20u,0x180u,0x1c0u,0x9330u,0xab30u,0x230u,0x270u,0x9340u,0xab40u,0x2b0u,0x2f0u,0x9350u,0xab50u,0x320u,0x360u,0x9360u,0xab60u,0x3a0u,0x3e0u,0x9370u,0xab70u,0x450u,0x410u,0x9380u,0xab80u,0x4d0u,0x490u,0x9390u,0xab90u,0x540u,0x500u,0x93a0u,0xaba0u,0x5c0u,0x580u,0x93b0u,0xabb0u,0x670u,0x630u,0x93c0u,0xabc0u,0x6f0u,0x6b0u,0x93d0u,0xabd0u,0x760u,0x720u,0x93e0u,0xabe0u,0x7e0u,0x7a0u,0x93f0u,0xabf0u,0x20u,0x60u,0x9600u,0xae00u,0xa0u,0xe0u,0x9610u,0xae10u,0x130u,0x170u,0x9620u,0xae20u,0x1b0u,0x1f0u,0x9630u,0xae30u,0x200u,0x240u,0x9640u,0xae40u,0x280u,0x2c0u,0x9650u,0xae50u,0x310u,0x350u,0x9660u,0xae60u,0x390u,0x3d0u,0x9670u,0xae70u,0x460u,0x420u,0x9680u,0xae80u,0x4e0u,0x4a0u,0x9690u,0xae90u,0x570u,0x530u,0x96a0u,0xaea0u,0x5f0u,0x5b0u,0x96b0u,0xaeb0u,0x640u,0x600u,0x96c0u,0xaec0u,0x6c0u,0x680u,0x96d0u,0xaed0u,0x750u,0x710u,0x96e0u,0xaee0u,0x7d0u,0x790u,0x96f0u,0xaef0u,0x30u,0x70u,0x9900u,0xb100u,0xb0u,0xf0u,0x9910u,0xb110u,0x120u,0x160u,0x9920u,0xb120u,0x1a0u,0x1e0u,0x9930u,0xb130u,0x210u,0x250u,0x9940u,0xb140u,0x290u,0x2d0u,0x9950u,0xb150u,0x300u,0x340u,0x9960u,0xb160u,0x380u,0x3c0u,0x9970u,0xb170u,0x470u,0x430u,0x9980u,0xb180u,0x4f0u,0x4b0u,0x9990u,0xb190u,0x560u,0x520u,0x99a0u,0xb1a0u,0x5e0u,0x5a0u,0x99b0u,0xb1b0u,0x650u,0x610u,0x99c0u,0xb1c0u,0x6d0u,0x690u,0x99d0u,0xb1d0u,0x740u,0x700u,0x99e0u,0xb1e0u,0x7c0u,0x780u,0x99f0u,0xb1f0u,0x9c08u,0x12060u,0x120a0u,0x12400u,0x9c18u,0x121f0u,0x12230u,0x12410u,0x9c28u,0x12340u,0x12380u,0x12420u,0x9c38u,0x124d0u,0x12510u,0x12430u,0x9c48u,0x12620u,0x126e0u,0x12440u,0x9c58u,0x127b0u,0x12870u,0x12450u,0x9c68u,0x12900u,0x129c0u,0x12460u,0x9c78u,0x12a90u,0x12b50u,0x12470u,0x9c88u,0x12c60u,0x12ca0u,0x12480u,0x9c98u,0x12df0u,0x12e30u,0x12490u,0x9ca8u,0x12f40u,0x12f80u,0x124a0u,0x9cb8u,0x130d0u,0x13110u,0x124b0u,0x9cc8u,0x13220u,0x132e0u,0x124c0u,0x9cd8u,0x133b0u,0x13470u,0x124d0u,0x9ce8u,0x13500u,0x135c0u,0x124e0u,0x9cf8u,0x13690u,0x13750u,0x124f0u,0x9f08u,0x12070u,0x120a8u,0x12500u,0x9f18u,0x121e0u,0x12238u,0x12510u,0x9f28u,0x12350u,0x12388u,0x12520u,0x9f38u,0x124c0u,0x12518u,0x12530u,0x9f48u,0x12630u,0x126e8u,0x12540u,0x9f58u,0x127a0u,0x12878u,0x12550u,0x9f68u,0x12910u,0x129c8u,0x12560u,0x9f78u,0x12a80u,0x12b58u,0x12570u,0x9f88u,0x12c70u,0x12ca8u,0x12580u,0x9f98u,0x12de0u,0x12e38u,0x12590u,0x9fa8u,0x12f50u,0x12f88u,0x125a0u,0x9fb8u,0x130c0u,0x13118u,0x125b0u,0x9fc8u,0x13230u,0x132e8u,0x125c0u,0x9fd8u,0x133a0u,0x13478u,0x125d0u,0x9fe8u,0x13510u,0x135c8u,0x125e0u,0x9ff8u,0x13680u,0x13758u,0x125f0u,0xa208u,0x12080u,0x120b0u,0x12600u,0xa218u,0x12210u,0x12220u,0x12610u,0xa228u,0x123a0u,0x12390u,0x12620u,0xa238u,0x12530u,0x12500u,0x12630u,0xa248u,0x126c0u,0x126f0u,0x12640u,0xa258u,0x12850u,0x12860u,0x12650u,0xa268u,0x129e0u,0x129d0u,0x12660u,0xa278u,0x12b70u,0x12b40u,0x12670u,0xa288u,0x12c80u,0x12cb0u,0x12680u,0xa298u,0x12e10u,0x12e20u,0x12690u,0xa2a8u,0x12fa0u,0x12f90u,0x126a0u,0xa2b8u,0x13130u,0x13100u,0x126b0u,0xa2c8u,0x132c0u,0x132f0u,0x126c0u,0xa2d8u,0x13450u,0x13460u,0x126d0u,0xa2e8u,0x135e0u,0x135d0u,0x126e0u,0xa2f8u,0x13770u,0x13740u,0x126f0u,0xa508u,0x12090u,0x120b8u,0x12700u,0xa518u,0x12200u,0x12228u,0x12710u,0xa528u,0x123b0u,0x12398u,0x12720u,0xa538u,0x12520u,0x12508u,0x12730u,0xa548u,0x126d0u,0x126f8u,0x12740u,0xa558u,0x12840u,0x12868u,0x12750u,0xa568u,0x129f0u,0x129d8u,0x12760u,0xa578u,0x12b60u,0x12b48u,0x12770u,0xa588u,0x12c90u,0x12cb8u,0x12780u,0xa598u,0x12e00u,0x12e28u,0x12790u,0xa5a8u,0x12fb0u,0x12f98u,0x127a0u,0xa5b8u,0x13120u,0x13108u,0x127b0u,0xa5c8u,0x132d0u,0x132f8u,0x127c0u,0xa5d8u,0x13440u,0x13468u,0x127d0u,0xa5e8u,0x135f0u,0x135d8u,0x127e0u,0xa5f8u,0x13760u,0x13748u,0x127f0u,0x5000u,0x1c300u,0x1e8c0u,0x1eb60u,0x5080u,0x1c310u,0x100000u,0x1eb62u,0x5110u,0x1c320u,0x100000u,0x1eb64u,0x5190u,0x1c330u,0x100000u,0x1eb66u,0x5220u,0x1c340u,0x1e8c0u,0x1eb68u,0x52a0u,0x1c350u,0x100000u,0x1eb6au,0x5330u,0x1c360u,0x100000u,0x1eb6cu,0x53b0u,0x1c370u,0x100000u,0x1eb6eu,0x5440u,0x1c380u,0x1e8c0u,0x1eb70u,0x54c0u,0x1c390u,0x100000u,0x1eb72u,0x5550u,0x1c3a0u,0x100000u,0x1eb74u,0x55d0u,0x1c3b0u,0x100000u,0x1eb76u,0x5660u,0x1c3c0u,0x1e8c0u,0x1eb78u,0x56e0u,0x1c3d0u,0x100000u,0x1eb7au,0x5770u,0x1c3e0u,0x100000u,0x1eb7cu,0x57f0u,0x1c3f0u,0x100000u,0x1eb7eu,0x5010u,0x1c400u,0x1e8f0u,0x1eb60u,0x5090u,0x1c410u,0x100000u,0x1eb62u,0x5100u,0x1c420u,0x100000u,0x1eb64u,0x5180u,0x1c430u,0x100000u,0x1eb66u,0x5230u,0x1c440u,0x1e8f0u,0x1eb68u,0x52b0u,0x1c450u,0x100000u,0x1eb6au,0x5320u,0x1c460u,0x100000u,0x1eb6cu,0x53a0u,0x1c470u,0x100000u,0x1eb6eu,0x5450u,0x1c480u,0x1e8f0u,0x1eb70u,0x54d0u,0x1c490u,0x100000u,0x1eb72u,0x5540u,0x1c4a0u,0x100000u,0x1eb74u,0x55c0u,0x1c4b0u,0x100000u,0x1eb76u,0x5670u,0x1c4c0u,0x1e8f0u,0x1eb78u,0x56f0u,0x1c4d0u,0x100000u,0x1eb7au,0x5760u,0x1c4e0u,0x100000u,0x1eb7cu,0x57e0u,0x1c4f0u,0x100000u,0x1eb7eu,0x5020u,0x1c500u,0x1e920u,0x1eb60u,0x50a0u,0x1c510u,0x100000u,0x1eb62u,0x5130u,0x1c520u,0x100000u,0x1eb64u,0x51b0u,0x1c530u,0x100000u,0x1eb66u,0x5200u,0x1c540u,0x1e920u,0x1eb68u,0x5280u,0x1c550u,0x100000u,0x1eb6au,0x5310u,0x1c560u,0x100000u,0x1eb6cu,0x5390u,0x1c570u,0x100000u,0x1eb6eu,0x5460u,0x1c580u,0x1e920u,0x1eb70u,0x54e0u,0x1c590u,0x100000u,0x1eb72u,0x5570u,0x1c5a0u,0x100000u,0x1eb74u,0x55f0u,0x1c5b0u,0x100000u,0x1eb76u,0x5640u,0x1c5c0u,0x1e920u,0x1eb78u,0x56c0u,0x1c5d0u,0x100000u,0x1eb7au,0x5750u,0x1c5e0u,0x100000u,0x1eb7cu,0x57d0u,0x1c5f0u,0x100000u,0x1eb7eu,0x5030u,0x1c600u,0x1e950u,0x1eb60u,0x50b0u,0x1c610u,0x100000u,0x1eb62u,0x5120u,0x1c620u,0x100000u,0x1eb64u,0x51a0u,0x1c630u,0x100000u,0x1eb66u,0x5210u,0x1c640u,0x1e950u,0x1eb68u,0x5290u,0x1c650u,0x100000u,0x1eb6au,0x5300u,0x1c660u,0x100000u,0x1eb6cu,0x5380u,0x1c670u,0x100000u,0x1eb6eu,0x5470u,0x1c680u,0x1e950u,0x1eb70u,0x54f0u,0x1c690u,0x100000u,0x1eb72u,0x5560u,0x1c6a0u,0x100000u,0x1eb74u,0x55e0u,0x1c6b0u,0x100000u,0x1eb76u,0x5650u,0x1c6c0u,0x1e950u,0x1eb78u,0x56d0u,0x1c6d0u,0x100000u,0x1eb7au,0x5740u,0x1c6e0u,0x100000u,0x1eb7cu,0x57c0u,0x1c6f0u,0x100000u,0x1eb7eu,0x25800u,0x1d380u,0x1d380u,0xf0140u,0x25802u,0x1d382u,0x100000u,0xf0150u,0x25804u,0x1d384u,0x100000u,0xf0160u,0x25806u,0x1d386u,0x100000u,0xf0170u,0x25a00u,0x1d390u,0x1d380u,0xf0100u,0x25a02u,0x1d392u,0x100000u,0xf0110u,0x25a04u,0x1d394u,0x100000u,0xf0120u,0x25a06u,0x1d396u,0x100000u,0xf0130u,0x25c00u,0x1d3a0u,0x1d380u,0xf02c0u,0x25c02u,0x1d3a2u,0x100000u,0xf02d0u,0x25c04u,0x1d3a4u,0x100000u,0xf02e0u,0x25c06u,0x1d3a6u,0x100000u,0xf02f0u,0x25e00u,0x1d3b0u,0x1d380u,0xf0280u,0x25e02u,0x1d3b2u,0x100000u,0xf0290u,0x25e04u,0x1d3b4u,0x100000u,0xf02a0u,0x25e06u,0x1d3b6u,0x100000u,0xf02b0u,0x25808u,0x1d388u,0x1d390u,0x11f850u,0x2580au,0x1d38au,0x100000u,0x11f840u,0x2580cu,0x1d38cu,0x100000u,0x11f870u,0x2580eu,0x1d38eu,0x100000u,0x11f860u,0x25a08u,0x1d398u,0x1d390u,0x11f810u,0x25a0au,0x1d39au,0x100000u,0x11f800u,0x25a0cu,0x1d39cu,0x100000u,0x11f830u,0x25a0eu,0x1d39eu,0x100000u,0x11f820u,0x25c08u,0x1d3a8u,0x1d390u,0x11f9d0u,0x25c0au,0x1d3aau,0x100000u,0x11f9c0u,0x25c0cu,0x1d3acu,0x100000u,0x11f9f0u,0x25c0eu,0x1d3aeu,0x100000u,0x11f9e0u,0x25e08u,0x1d3b8u,0x1d390u,0x11f990u,0x25e0au,0x1d3bau,0x100000u,0x11f980u,0x25e0cu,0x1d3bcu,0x100000u,0x11f9b0u,0x25e0eu,0x1d3beu,0x100000u,0x11f9a0u,0x25008u,0x1d3c0u,0x1d3a0u,0x11fb60u,0x2500au,0x1d3c2u,0x100000u,0x11fb70u,0x2500cu,0x1d3c4u,0x100000u,0x11fb40u,0x2500eu,0x1d3c6u,0x100000u,0x11fb50u,0x25208u,0x1d3d0u,0x1d3a0u,0x11fb20u,0x2520au,0x1d3d2u,0x100000u,0x11fb30u,0x2520cu,0x1d3d4u,0x100000u,0x11fb00u,0x2520eu,0x1d3d6u,0x100000u,0x11fb10u,0x25408u,0x1d3e0u,0x1d3a0u,0x11fce0u,0x2540au,0x1d3e2u,0x100000u,0x11fcf0u,0x2540cu,0x1d3e4u,0x100000u,0x11fcc0u,0x2540eu,0x1d3e6u,0x100000u,0x11fcd0u,0x25608u,0x1d3f0u,0x1d3a0u,0x11fca0u,0x2560au,0x1d3f2u,0x100000u,0x11fcb0u,0x2560cu,0x1d3f4u,0x100000u,0x11fc80u,0x2560eu,0x1d3f6u,0x100000u,0x11fc90u,0x100000u,0x1d3c8u,0x1d3b0u,0x11fe70u,0x100000u,0x1d3cau,0x100000u,0x11fe60u,0x100000u,0x1d3ccu,0x100000u,0x11fe50u,0x100000u,0x1d3ceu,0x100000u,0x11fe40u,0x100000u,0x1d3d8u,0x1d3b0u,0x11fe30u,0x100000u,0x1d3dau,0x100000u,0x11fe20u,0x100000u,0x1d3dcu,0x100000u,0x11fe10u,0x100000u,0x1d3deu,0x100000u,0x11fe00u,0x100000u,0x1d3e8u,0x1d3b0u,0x11fff0u,0x100000u,0x1d3eau,0x100000u,0x11ffe0u,0x100000u,0x1d3ecu,0x100000u,0x11ffd0u,0x100000u,0x1d3eeu,0x100000u,0x11ffc0u,0x100000u,0x1d3f8u,0x1d3b0u,0x11ffb0u,0x100000u,0x1d3fau,0x100000u,0x11ffa0u,0x100000u,0x1d3fcu,0x100000u,0x11ff90u,0x100000u,0x1d3feu,0x100000u,0x11ff80u,0x420u,0x30850u,0x90000u,0xc0420u,0x430u,0x30840u,0x90010u,0xc0430u,0x400u,0x30870u,0x90020u,0xc0400u,0x410u,0x30860u,0x90030u,0xc0410u,0x460u,0x30810u,0x90040u,0xc0460u,0x470u,0x30800u,0x90050u,0xc0470u,0x440u,0x30830u,0x90060u,0xc0440u,0x450u,0x30820u,0x90070u,0xc0450u,0x4b0u,0x308d0u,0x90080u,0xef8b0u,0x4a0u,0x308c0u,0x90090u,0xef8a0u,0x490u,0x308f0u,0x900a0u,0xef890u,0x480u,0x308e0u,0x900b0u,0xef880u,0x4f0u,0x30890u,0x900c0u,0xef8f0u,0x4e0u,0x30880u,0x900d0u,0xef8e0u,0x4d0u,0x308b0u,0x900e0u,0xef8d0u,0x4c0u,0x308a0u,0x900f0u,0xef8c0u,0x530u,0x5fd60u,0x90100u,0xef930u,0x520u,0x5fd70u,0x90110u,0xef920u,0x510u,0x5fd40u,0x90120u,0xef910u,0x500u,0x5fd50u,0x90130u,0xef900u,0x570u,0x5fd20u,0x90140u,0xef970u,0x560u,0x5fd30u,0x90150u,0xef960u,0x550u,0x5fd00u,0x90160u,0xef950u,0x540u,0x5fd10u,0x90170u,0xef940u,0x5b0u,0x5fde0u,0x90190u,0xef9b0u,0x5a0u,0x5fdf0u,0x90180u,0xef9a0u,0x590u,0x5fdc0u,0x901b0u,0xef990u,0x580u,0x5fdd0u,0x901a0u,0xef980u,0x5f0u,0x5fda0u,0x901d0u,0xef9f0u,0x5e0u,0x5fdb0u,0x901c0u,0xef9e0u,0x5d0u,0x5fd80u,0x901f0u,0xef9d0u,0x5c0u,0x5fd90u,0x901e0u,0xef9c0u,0x640u,0x5fe60u,0x90210u,0xefa40u,0x650u,0x5fe70u,0x90200u,0xefa50u,0x660u,0x5fe40u,0x90230u,0xefa60u,0x670u,0x5fe50u,0x90220u,0xefa70u,0x600u,0x5fe20u,0x90250u,0xefa00u,0x610u,0x5fe30u,0x90240u,0xefa10u,0x620u,0x5fe00u,0x90270u,0xefa20u,0x630u,0x5fe10u,0x90260u,0xefa30u,0x6c0u,0x5fef0u,0x90290u,0xefac0u,0x6d0u,0x5fee0u,0x90280u,0xefad0u,0x6e0u,0x5fed0u,0x902b0u,0xefae0u,0x6f0u,0x5fec0u,0x902a0u,0xefaf0u,0x680u,0x5feb0u,0x902d0u,0xefa80u,0x690u,0x5fea0u,0x902c0u,0xefa90u,0x6a0u,0x5fe90u,0x902f0u,0xefaa0u,0x6b0u,0x5fe80u,0x902e0u,0xefab0u,0x740u,0x5ff70u,0x90320u,0xefb40u,0x750u,0x5ff60u,0x90330u,0xefb50u,0x760u,0x5ff50u,0x90300u,0xefb60u,0x770u,0x5ff40u,0x90310u,0xefb70u,0x700u,0x5ff30u,0x90360u,0xefb00u,0x710u,0x5ff20u,0x90370u,0xefb10u,0x720u,0x5ff10u,0x90340u,0xefb20u,0x730u,0x5ff00u,0x90350u,0xefb30u,0x7d0u,0x5fff0u,0x903a0u,0xefbd0u,0x7c0u,0x5ffe0u,0x903b0u,0xefbc0u,0x7f0u,0x5ffd0u,0x90380u,0xefbf0u,0x7e0u,0x5ffc0u,0x90390u,0xefbe0u,0x790u,0x5ffb0u,0x903e0u,0xefb90u,0x780u,0x5ffa0u,0x903f0u,0xefb80u,0x7b0u,0x5ff90u,0x903c0u,0xefbb0u,0x7a0u,0x5ff80u,0x903d0u,0xefba0u,0x0u,0x11fc50u,0x0u,0x7230cu,0x0u,0x11fc40u,0x0u,0x7230cu,0x0u,0x11fc70u,0x0u,0x7230cu,0x0u,0x11fc60u,0x0u,0x7230cu,0x0u,0x11fc10u,0x0u,0x7230cu,0x0u,0x11fc00u,0x0u,0x7230cu,0x0u,0x11fc30u,0x0u,0x7230cu,0x0u,0x11fc20u,0x0u,0x7230cu,0x0u,0x11fcd0u,0x0u,0x5232cu,0x0u,0x11fcc0u,0x0u,0x5232cu,0x0u,0x11fcf0u,0x0u,0x5232cu,0x0u,0x11fce0u,0x0u,0x5232cu,0x0u,0x11fc90u,0x0u,0x5232cu,0x0u,0x11fc80u,0x0u,0x5232cu,0x0u,0x11fcb0u,0x0u,0x5232cu,0x0u,0x11fca0u,0x0u,0x5232cu,0x0u,0x11fd60u,0x0u,0xd0124u,0x0u,0x11fd70u,0x0u,0xd0124u,0x0u,0x11fd40u,0x0u,0xd0124u,0x0u,0x11fd50u,0x0u,0xd0124u,0x0u,0x11fd20u,0x0u,0xd0124u,0x0u,0x11fd30u,0x0u,0xd0124u,0x0u,0x11fd00u,0x0u,0xd0124u,0x0u,0x11fd10u,0x0u,0xd0124u,0x0u,0x11fde0u,0x0u,0xd81a6u,0x0u,0x11fdf0u,0x0u,0xd81a6u,0x0u,0x11fdc0u,0x0u,0xd81a6u,0x0u,0x11fdd0u,0x0u,0xd81a6u,0x0u,0x11fda0u,0x0u,0xd81a6u,0x0u,0x11fdb0u,0x0u,0xd81a6u,0x0u,0x11fd80u,0x0u,0xd81a6u,0x0u,0x11fd90u,0x0u,0xd81a6u,0x0u,0x11fe60u,0x0u,0xd8996u,0x0u,0x11fe70u,0x0u,0xd8996u,0x0u,0x11fe40u,0x0u,0xd8996u,0x0u,0x11fe50u,0x0u,0xd8996u,0x0u,0x11fe20u,0x0u,0xd8996u,0x0u,0x11fe30u,0x0u,0xd8996u,0x0u,0x11fe00u,0x0u,0xd8996u,0x0u,0x11fe10u,0x0u,0xd8996u,0x0u,0x11fef0u,0x0u,0xd8996u,0x0u,0x11fee0u,0x0u,0xd8996u,0x0u,0x11fed0u,0x0u,0xd8996u,0x0u,0x11fec0u,0x0u,0xd8996u,0x0u,0x11feb0u,0x0u,0xd8996u,0x0u,0x11fea0u,0x0u,0xd8996u,0x0u,0x11fe90u,0x0u,0xd8996u,0x0u,0x11fe80u,0x0u,0xd8996u,0x0u,0x11ff70u,0x0u,0xd8b1cu,0x0u,0x11ff60u,0x0u,0xd8b1cu,0x0u,0x11ff50u,0x0u,0xd8b1cu,0x0u,0x11ff40u,0x0u,0xd8b1cu,0x0u,0x11ff30u,0x0u,0xd8b1cu,0x0u,0x11ff20u,0x0u,0xd8b1cu,0x0u,0x11ff10u,0x0u,0xd8b1cu,0x0u,0x11ff00u,0x0u,0xd8b1cu,0x0u,0x11fff0u,0x0u,0xd8b1cu,0x0u,0x11ffe0u,0x0u,0xd8b1cu,0x0u,0x11ffd0u,0x0u,0xd8b1cu,0x0u,0x11ffc0u,0x0u,0xd8b1cu,0x0u,0x11ffb0u,0x0u,0xd8b1cu,0x0u,0x11ffa0u,0x0u,0xd8b1cu,0x0u,0x11ff90u,0x0u,0xd8b1cu,0x0u,0x11ff80u,0x0u,0xd8b1cu,0x5840u,0x5860u,0x6800u,0x5840u,0x5848u,0x5868u,0x6808u,0x5850u,0x5850u,0x5870u,0x6810u,0x5860u,0x5858u,0x5878u,0x6818u,0x5870u,0x58c0u,0x58e0u,0x6880u,0x58c0u,0x58c8u,0x58e8u,0x6888u,0x58d0u,0x58d0u,0x58f0u,0x6890u,0x58e0u,0x58d8u,0x58f8u,0x6898u,0x58f0u,0x5960u,0x5940u,0x6920u,0x5960u,0x5968u,0x5948u,0x6928u,0x5970u,0x5970u,0x5950u,0x6930u,0x5940u,0x5978u,0x5958u,0x6938u,0x5950u,0x59e0u,0x59c0u,0x69a0u,0x59e0u,0x59e8u,0x59c8u,0x69a8u,0x59f0u,0x59f0u,0x59d0u,0x69b0u,0x59c0u,0x59f8u,0x59d8u,0x69b8u,0x59d0u,0x5c00u,0x5c20u,0x6c40u,0x5a40u,0x5c08u,0x5c28u,0x6c48u,0x5a50u,0x5c10u,0x5c30u,0x6c50u,0x5a60u,0x5c18u,0x5c38u,0x6c58u,0x5a70u,0x5c80u,0x5ca0u,0x6cc0u,0x5ac0u,0x5c88u,0x5ca8u,0x6cc8u,0x5ad0u,0x5c90u,0x5cb0u,0x6cd0u,0x5ae0u,0x5c98u,0x5cb8u,0x6cd8u,0x5af0u,0x5d20u,0x5d00u,0x6d60u,0x5b60u,0x5d28u,0x5d08u,0x6d68u,0x5b70u,0x5d30u,0x5d10u,0x6d70u,0x5b40u,0x5d38u,0x5d18u,0x6d78u,0x5b50u,0x5da0u,0x5d80u,0x6de0u,0x5be0u,0x5da8u,0x5d88u,0x6de8u,0x5bf0u,0x5db0u,0x5d90u,0x6df0u,0x5bc0u,0x5db8u,0x5d98u,0x6df8u,0x5bd0u,0x6040u,0x6060u,0x7000u,0x5c00u,0x6048u,0x6068u,0x7008u,0x5c10u,0x6050u,0x6070u,0x7010u,0x5c20u,0x6058u,0x6078u,0x7018u,0x5c30u,0x60c0u,0x60e0u,0x7080u,0x5c80u,0x60c8u,0x60e8u,0x7088u,0x5c90u,0x60d0u,0x60f0u,0x7090u,0x5ca0u,0x60d8u,0x60f8u,0x7098u,0x5cb0u,0x6160u,0x6140u,0x7120u,0x5d20u,0x6168u,0x6148u,0x7128u,0x5d30u,0x6170u,0x6150u,0x7130u,0x5d00u,0x6178u,0x6158u,0x7138u,0x5d10u,0x61e0u,0x61c0u,0x71a0u,0x5da0u,0x61e8u,0x61c8u,0x71a8u,0x5db0u,0x61f0u,0x61d0u,0x71b0u,0x5d80u,0x61f8u,0x61d8u,0x71b8u,0x5d90u,0x6400u,0x6420u,0x7440u,0x5e00u,0x6408u,0x6428u,0x7448u,0x5e10u,0x6410u,0x6430u,0x7450u,0x5e20u,0x6418u,0x6438u,0x7458u,0x5e30u,0x6480u,0x64a0u,0x74c0u,0x5e80u,0x6488u,0x64a8u,0x74c8u,0x5e90u,0x6490u,0x64b0u,0x74d0u,0x5ea0u,0x6498u,0x64b8u,0x74d8u,0x5eb0u,0x6520u,0x6500u,0x7560u,0x5f20u,0x6528u,0x6508u,0x7568u,0x5f30u,0x6530u,0x6510u,0x7570u,0x5f00u,0x6538u,0x6518u,0x7578u,0x5f10u,0x65a0u,0x6580u,0x75e0u,0x5fa0u,0x65a8u,0x6588u,0x75e8u,0x5fb0u,0x65b0u,0x6590u,0x75f0u,0x5f80u,0x65b8u,0x6598u,0x75f8u,0x5f90u,0x6800u,0x1e980u,0x1ebc0u,0x1800u,0x6808u,0x1ee10u,0x1f050u,0x1810u,0x6810u,0x1f2a0u,0x1f4e0u,0x1820u,0x6818u,0x1f730u,0x1f970u,0x1830u,0x6880u,0x1fbc0u,0x1fe00u,0x1840u,0x6888u,0x20050u,0x20290u,0x1850u,0x6890u,0x204e0u,0x20720u,0x1860u,0x6898u,0x20970u,0x20bb0u,0x1870u,0x6920u,0x20e00u,0x21040u,0x1980u,0x6928u,0x21290u,0x214d0u,0x1990u,0x6930u,0x21720u,0x21960u,0x19a0u,0x6938u,0x21bb0u,0x21df0u,0x19b0u,0x69a0u,0x22040u,0x22280u,0x19c0u,0x69a8u,0x224d0u,0x22710u,0x19d0u,0x69b0u,0x22960u,0x22ba0u,0x19e0u,0x69b8u,0x22df0u,0x23030u,0x19f0u,0x6a00u,0x1e9b0u,0x1ebc8u,0x1b20u,0x6a08u,0x1ee40u,0x1f058u,0x1b30u,0x6a10u,0x1f2d0u,0x1f4e8u,0x1b00u,0x6a18u,0x1f760u,0x1f978u,0x1b10u,0x6a80u,0x1fbf0u,0x1fe08u,0x1b60u,0x6a88u,0x20080u,0x20298u,0x1b70u,0x6a90u,0x20510u,0x20728u,0x1b40u,0x6a98u,0x209a0u,0x20bb8u,0x1b50u,0x6b20u,0x20e30u,0x21048u,0x1ca0u,0x6b28u,0x212c0u,0x214d8u,0x1cb0u,0x6b30u,0x21750u,0x21968u,0x1c80u,0x6b38u,0x21be0u,0x21df8u,0x1c90u,0x6ba0u,0x22070u,0x22288u,0x1ce0u,0x6ba8u,0x22500u,0x22718u,0x1cf0u,0x6bb0u,0x22990u,0x22ba8u,0x1cc0u,0x6bb8u,0x22e20u,0x23038u,0x1cd0u,0x6c40u,0x1e9e0u,0x1ebd0u,0x1e00u,0x6c48u,0x1ee70u,0x1f060u,0x1e10u,0x6c50u,0x1f300u,0x1f4f0u,0x1e20u,0x6c58u,0x1f790u,0x1f980u,0x1e30u,0x6cc0u,0x1fc20u,0x1fe10u,0x1e40u,0x6cc8u,0x200b0u,0x202a0u,0x1e50u,0x6cd0u,0x20540u,0x20730u,0x1e60u,0x6cd8u,0x209d0u,0x20bc0u,0x1e70u,0x6d60u,0x20e60u,0x21050u,0x1f80u,0x6d68u,0x212f0u,0x214e0u,0x1f90u,0x6d70u,0x21780u,0x21970u,0x1fa0u,0x6d78u,0x21c10u,0x21e00u,0x1fb0u,0x6de0u,0x220a0u,0x22290u,0x1fc0u,0x6de8u,0x22530u,0x22720u,0x1fd0u,0x6df0u,0x229c0u,0x22bb0u,0x1fe0u,0x6df8u,0x22e50u,0x23040u,0x1ff0u,0x6e40u,0x1ea10u,0x1ebd8u,0x2120u,0x6e48u,0x1eea0u,0x1f068u,0x2130u,0x6e50u,0x1f330u,0x1f4f8u,0x2100u,0x6e58u,0x1f7c0u,0x1f988u,0x2110u,0x6ec0u,0x1fc50u,0x1fe18u,0x2160u,0x6ec8u,0x200e0u,0x202a8u,0x2170u,0x6ed0u,0x20570u,0x20738u,0x2140u,0x6ed8u,0x20a00u,0x20bc8u,0x2150u,0x6f60u,0x20e90u,0x21058u,0x22a0u,0x6f68u,0x21320u,0x214e8u,0x22b0u,0x6f70u,0x217b0u,0x21978u,0x2280u,0x6f78u,0x21c40u,0x21e08u,0x2290u,0x6fe0u,0x220d0u,0x22298u,0x22e0u,0x6fe8u,0x22560u,0x22728u,0x22f0u,0x6ff0u,0x229f0u,0x22bb8u,0x22c0u,0x6ff8u,0x22e80u,0x23048u,0x22d0u,0x0u,0x40u,0x9000u,0xb400u,0x80u,0xc0u,0x9010u,0xb410u,0x110u,0x150u,0x9020u,0xb420u,0x190u,0x1d0u,0x9030u,0xb430u,0x220u,0x260u,0x9040u,0xb440u,0x2a0u,0x2e0u,0x9050u,0xb450u,0x330u,0x370u,0x9060u,0xb460u,0x3b0u,0x3f0u,0x9070u,0xb470u,0x440u,0x400u,0x9080u,0xb480u,0x4c0u,0x480u,0x9090u,0xb490u,0x550u,0x510u,0x90a0u,0xb4a0u,0x5d0u,0x590u,0x90b0u,0xb4b0u,0x660u,0x620u,0x90c0u,0xb4c0u,0x6e0u,0x6a0u,0x90d0u,0xb4d0u,0x770u,0x730u,0x90e0u,0xb4e0u,0x7f0u,0x7b0u,0x90f0u,0xb4f0u,0x10u,0x50u,0x9300u,0xb700u,0x90u,0xd0u,0x9310u,0xb710u,0x100u,0x140u,0x9320u,0xb720u,0x180u,0x1c0u,0x9330u,0xb730u,0x230u,0x270u,0x9340u,0xb740u,0x2b0u,0x2f0u,0x9350u,0xb750u,0x320u,0x360u,0x9360u,0xb760u,0x3a0u,0x3e0u,0x9370u,0xb770u,0x450u,0x410u,0x9380u,0xb780u,0x4d0u,0x490u,0x9390u,0xb790u,0x540u,0x500u,0x93a0u,0xb7a0u,0x5c0u,0x580u,0x93b0u,0xb7b0u,0x670u,0x630u,0x93c0u,0xb7c0u,0x6f0u,0x6b0u,0x93d0u,0xb7d0u,0x760u,0x720u,0x93e0u,0xb7e0u,0x7e0u,0x7a0u,0x93f0u,0xb7f0u,0x20u,0x60u,0x9600u,0xba00u,0xa0u,0xe0u,0x9610u,0xba10u,0x130u,0x170u,0x9620u,0xba20u,0x1b0u,0x1f0u,0x9630u,0xba30u,0x200u,0x240u,0x9640u,0xba40u,0x280u,0x2c0u,0x9650u,0xba50u,0x310u,0x350u,0x9660u,0xba60u,0x390u,0x3d0u,0x9670u,0xba70u,0x460u,0x420u,0x9680u,0xba80u,0x4e0u,0x4a0u,0x9690u,0xba90u,0x570u,0x530u,0x96a0u,0xbaa0u,0x5f0u,0x5b0u,0x96b0u,0xbab0u,0x640u,0x600u,0x96c0u,0xbac0u,0x6c0u,0x680u,0x96d0u,0xbad0u,0x750u,0x710u,0x96e0u,0xbae0u,0x7d0u,0x790u,0x96f0u,0xbaf0u,0x30u,0x70u,0x9900u,0xbd00u,0xb0u,0xf0u,0x9910u,0xbd10u,0x120u,0x160u,0x9920u,0xbd20u,0x1a0u,0x1e0u,0x9930u,0xbd30u,0x210u,0x250u,0x9940u,0xbd40u,0x290u,0x2d0u,0x9950u,0xbd50u,0x300u,0x340u,0x9960u,0xbd60u,0x380u,0x3c0u,0x9970u,0xbd70u,0x470u,0x430u,0x9980u,0xbd80u,0x4f0u,0x4b0u,0x9990u,0xbd90u,0x560u,0x520u,0x99a0u,0xbda0u,0x5e0u,0x5a0u,0x99b0u,0xbdb0u,0x650u,0x610u,0x99c0u,0xbdc0u,0x6d0u,0x690u,0x99d0u,0xbdd0u,0x740u,0x700u,0x99e0u,0xbde0u,0x7c0u,0x780u,0x99f0u,0xbdf0u,0xc000u,0x120c0u,0x12100u,0x12800u,0xc010u,0x12250u,0x12290u,0x12810u,0xc020u,0x123e0u,0x12420u,0x12820u,0xc030u,0x12570u,0x125b0u,0x12830u,0xc040u,0x12680u,0x12740u,0x12840u,0xc050u,0x12810u,0x128d0u,0x12850u,0xc060u,0x129a0u,0x12a60u,0x12860u,0xc070u,0x12b30u,0x12bf0u,0x12870u,0xc080u,0x12cc0u,0x12d00u,0x12880u,0xc090u,0x12e50u,0x12e90u,0x12890u,0xc0a0u,0x12fe0u,0x13020u,0x128a0u,0xc0b0u,0x13170u,0x131b0u,0x128b0u,0xc0c0u,0x13280u,0x13340u,0x128c0u,0xc0d0u,0x13410u,0x134d0u,0x128d0u,0xc0e0u,0x135a0u,0x13660u,0x128e0u,0xc0f0u,0x13730u,0x137f0u,0x128f0u,0xc300u,0x120d0u,0x12108u,0x12900u,0xc310u,0x12240u,0x12298u,0x12910u,0xc320u,0x123f0u,0x12428u,0x12920u,0xc330u,0x12560u,0x125b8u,0x12930u,0xc340u,0x12690u,0x12748u,0x12940u,0xc350u,0x12800u,0x128d8u,0x12950u,0xc360u,0x129b0u,0x12a68u,0x12960u,0xc370u,0x12b20u,0x12bf8u,0x12970u,0xc380u,0x12cd0u,0x12d08u,0x12980u,0xc390u,0x12e40u,0x12e98u,0x12990u,0xc3a0u,0x12ff0u,0x13028u,0x129a0u,0xc3b0u,0x13160u,0x131b8u,0x129b0u,0xc3c0u,0x13290u,0x13348u,0x129c0u,0xc3d0u,0x13400u,0x134d8u,0x129d0u,0xc3e0u,0x135b0u,0x13668u,0x129e0u,0xc3f0u,0x13720u,0x137f8u,0x129f0u,0xc600u,0x120e0u,0x12110u,0x12a00u,0xc610u,0x12270u,0x12280u,0x12a10u,0xc620u,0x123c0u,0x12430u,0x12a20u,0xc630u,0x12550u,0x125a0u,0x12a30u,0xc640u,0x126a0u,0x12750u,0x12a40u,0xc650u,0x12830u,0x128c0u,0x12a50u,0xc660u,0x12980u,0x12a70u,0x12a60u,0xc670u,0x12b10u,0x12be0u,0x12a70u,0xc680u,0x12ce0u,0x12d10u,0x12a80u,0xc690u,0x12e70u,0x12e80u,0x12a90u,0xc6a0u,0x12fc0u,0x13030u,0x12aa0u,0xc6b0u,0x13150u,0x131a0u,0x12ab0u,0xc6c0u,0x132a0u,0x13350u,0x12ac0u,0xc6d0u,0x13430u,0x134c0u,0x12ad0u,0xc6e0u,0x13580u,0x13670u,0x12ae0u,0xc6f0u,0x13710u,0x137e0u,0x12af0u,0xc900u,0x120f0u,0x12118u,0x12b00u,0xc910u,0x12260u,0x12288u,0x12b10u,0xc920u,0x123d0u,0x12438u,0x12b20u,0xc930u,0x12540u,0x125a8u,0x12b30u,0xc940u,0x126b0u,0x12758u,0x12b40u,0xc950u,0x12820u,0x128c8u,0x12b50u,0xc960u,0x12990u,0x12a78u,0x12b60u,0xc970u,0x12b00u,0x12be8u,0x12b70u,0xc980u,0x12cf0u,0x12d18u,0x12b80u,0xc990u,0x12e60u,0x12e88u,0x12b90u,0xc9a0u,0x12fd0u,0x13038u,0x12ba0u,0xc9b0u,0x13140u,0x131a8u,0x12bb0u,0xc9c0u,0x132b0u,0x13358u,0x12bc0u,0xc9d0u,0x13420u,0x134c8u,0x12bd0u,0xc9e0u,0x13590u,0x13678u,0x12be0u,0xc9f0u,0x13700u,0x137e8u,0x12bf0u,0x7800u,0x1c300u,0x1e980u,0x1ebc0u,0x7880u,0x1c310u,0x100000u,0x1ebc2u,0x7910u,0x1c320u,0x100000u,0x1ebc4u,0x7990u,0x1c330u,0x100000u,0x1ebc6u,0x7a20u,0x1c340u,0x1e980u,0x1ebc8u,0x7aa0u,0x1c350u,0x100000u,0x1ebcau,0x7b30u,0x1c360u,0x100000u,0x1ebccu,0x7bb0u,0x1c370u,0x100000u,0x1ebceu,0x7c40u,0x1c380u,0x1e980u,0x1ebd0u,0x7cc0u,0x1c390u,0x100000u,0x1ebd2u,0x7d50u,0x1c3a0u,0x100000u,0x1ebd4u,0x7dd0u,0x1c3b0u,0x100000u,0x1ebd6u,0x7e60u,0x1c3c0u,0x1e980u,0x1ebd8u,0x7ee0u,0x1c3d0u,0x100000u,0x1ebdau,0x7f70u,0x1c3e0u,0x100000u,0x1ebdcu,0x7ff0u,0x1c3f0u,0x100000u,0x1ebdeu,0x7810u,0x1c400u,0x1e9b0u,0x1ebc0u,0x7890u,0x1c410u,0x100000u,0x1ebc2u,0x7900u,0x1c420u,0x100000u,0x1ebc4u,0x7980u,0x1c430u,0x100000u,0x1ebc6u,0x7a30u,0x1c440u,0x1e9b0u,0x1ebc8u,0x7ab0u,0x1c450u,0x100000u,0x1ebcau,0x7b20u,0x1c460u,0x100000u,0x1ebccu,0x7ba0u,0x1c470u,0x100000u,0x1ebceu,0x7c50u,0x1c480u,0x1e9b0u,0x1ebd0u,0x7cd0u,0x1c490u,0x100000u,0x1ebd2u,0x7d40u,0x1c4a0u,0x100000u,0x1ebd4u,0x7dc0u,0x1c4b0u,0x100000u,0x1ebd6u,0x7e70u,0x1c4c0u,0x1e9b0u,0x1ebd8u,0x7ef0u,0x1c4d0u,0x100000u,0x1ebdau,0x7f60u,0x1c4e0u,0x100000u,0x1ebdcu,0x7fe0u,0x1c4f0u,0x100000u,0x1ebdeu,0x7820u,0x1c500u,0x1e9e0u,0x1ebc0u,0x78a0u,0x1c510u,0x100000u,0x1ebc2u,0x7930u,0x1c520u,0x100000u,0x1ebc4u,0x79b0u,0x1c530u,0x100000u,0x1ebc6u,0x7a00u,0x1c540u,0x1e9e0u,0x1ebc8u,0x7a80u,0x1c550u,0x100000u,0x1ebcau,0x7b10u,0x1c560u,0x100000u,0x1ebccu,0x7b90u,0x1c570u,0x100000u,0x1ebceu,0x7c60u,0x1c580u,0x1e9e0u,0x1ebd0u,0x7ce0u,0x1c590u,0x100000u,0x1ebd2u,0x7d70u,0x1c5a0u,0x100000u,0x1ebd4u,0x7df0u,0x1c5b0u,0x100000u,0x1ebd6u,0x7e40u,0x1c5c0u,0x1e9e0u,0x1ebd8u,0x7ec0u,0x1c5d0u,0x100000u,0x1ebdau,0x7f50u,0x1c5e0u,0x100000u,0x1ebdcu,0x7fd0u,0x1c5f0u,0x100000u,0x1ebdeu,0x7830u,0x1c600u,0x1ea10u,0x1ebc0u,0x78b0u,0x1c610u,0x100000u,0x1ebc2u,0x7920u,0x1c620u,0x100000u,0x1ebc4u,0x79a0u,0x1c630u,0x100000u,0x1ebc6u,0x7a10u,0x1c640u,0x1ea10u,0x1ebc8u,0x7a90u,0x1c650u,0x100000u,0x1ebcau,0x7b00u,0x1c660u,0x100000u,0x1ebccu,0x7b80u,0x1c670u,0x100000u,0x1ebceu,0x7c70u,0x1c680u,0x1ea10u,0x1ebd0u,0x7cf0u,0x1c690u,0x100000u,0x1ebd2u,0x7d60u,0x1c6a0u,0x100000u,0x1ebd4u,0x7de0u,0x1c6b0u,0x100000u,0x1ebd6u,0x7e50u,0x1c6c0u,0x1ea10u,0x1ebd8u,0x7ed0u,0x1c6d0u,0x100000u,0x1ebdau,0x7f40u,0x1c6e0u,0x100000u,0x1ebdcu,0x7fc0u,0x1c6f0u,0x100000u,0x1ebdeu,0x26000u,0x1d400u,0x1d400u,0x80000000u,0x26002u,0x1d402u,0x100000u,0x80000000u,0x26004u,0x1d404u,0x100000u,0x80000000u,0x26006u,0x1d406u,0x100000u,0x80000000u,0x26200u,0x1d410u,0x1d400u,0x80000000u,0x26202u,0x1d412u,0x100000u,0x80000000u,0x26204u,0x1d414u,0x100000u,0x80000000u,0x26206u,0x1d416u,0x100000u,0x80000000u,0x26400u,0x1d420u,0x1d400u,0x80000000u,0x26402u,0x1d422u,0x100000u,0x80000000u,0x26404u,0x1d424u,0x100000u,0x80000000u,0x26406u,0x1d426u,0x100000u,0x80000000u,0x26600u,0x1d430u,0x1d400u,0x80000000u,0x26602u,0x1d432u,0x100000u,0x80000000u,0x26604u,0x1d434u,0x100000u,0x80000000u,0x26606u,0x1d436u,0x100000u,0x80000000u,0x26008u,0x1d408u,0x1d410u,0x80000000u,0x2600au,0x1d40au,0x100000u,0x80000000u,0x2600cu,0x1d40cu,0x100000u,0x80000000u,0x2600eu,0x1d40eu,0x100000u,0x80000000u,0x26208u,0x1d418u,0x1d410u,0x80000000u,0x2620au,0x1d41au,0x100000u,0x80000000u,0x2620cu,0x1d41cu,0x100000u,0x80000000u,0x2620eu,0x1d41eu,0x100000u,0x80000000u,0x26408u,0x1d428u,0x1d410u,0x80000000u,0x2640au,0x1d42au,0x100000u,0x80000000u,0x2640cu,0x1d42cu,0x100000u,0x80000000u,0x2640eu,0x1d42eu,0x100000u,0x80000000u,0x26608u,0x1d438u,0x1d410u,0x80000000u,0x2660au,0x1d43au,0x100000u,0x80000000u,0x2660cu,0x1d43cu,0x100000u,0x80000000u,0x2660eu,0x1d43eu,0x100000u,0x80000000u,0x26800u,0x1d440u,0x1d420u,0x80000000u,0x26802u,0x1d442u,0x100000u,0x80000000u,0x26804u,0x1d444u,0x100000u,0x80000000u,0x26806u,0x1d446u,0x100000u,0x80000000u,0x26a00u,0x1d450u,0x1d420u,0x80000000u,0x26a02u,0x1d452u,0x100000u,0x80000000u,0x26a04u,0x1d454u,0x100000u,0x80000000u,0x26a06u,0x1d456u,0x100000u,0x80000000u,0x26c00u,0x1d460u,0x1d420u,0x80000000u,0x26c02u,0x1d462u,0x100000u,0x80000000u,0x26c04u,0x1d464u,0x100000u,0x80000000u,0x26c06u,0x1d466u,0x100000u,0x80000000u,0x26e00u,0x1d470u,0x1d420u,0x80000000u,0x26e02u,0x1d472u,0x100000u,0x80000000u,0x26e04u,0x1d474u,0x100000u,0x80000000u,0x26e06u,0x1d476u,0x100000u,0x80000000u,0x100000u,0x1d448u,0x1d430u,0x80000000u,0x100000u,0x1d44au,0x100000u,0x80000000u,0x100000u,0x1d44cu,0x100000u,0x80000000u,0x100000u,0x1d44eu,0x100000u,0x80000000u,0x100000u,0x1d458u,0x1d430u,0x80000000u,0x100000u,0x1d45au,0x100000u,0x80000000u,0x100000u,0x1d45cu,0x100000u,0x80000000u,0x100000u,0x1d45eu,0x100000u,0x80000000u,0x100000u,0x1d468u,0x1d430u,0x80000000u,0x100000u,0x1d46au,0x100000u,0x80000000u,0x100000u,0x1d46cu,0x100000u,0x80000000u,0x100000u,0x1d46eu,0x100000u,0x80000000u,0x100000u,0x1d478u,0x1d430u,0x80000000u,0x100000u,0x1d47au,0x100000u,0x80000000u,0x100000u,0x1d47cu,0x100000u,0x80000000u,0x100000u,0x1d47eu,0x100000u,0x80000000u,0x850u,0x60000u,0x90420u,0xefc50u,0x840u,0x60010u,0x90430u,0xefc40u,0x870u,0x60020u,0x90400u,0xefc70u,0x860u,0x60030u,0x90410u,0xefc60u,0x810u,0x60040u,0x90460u,0xefc10u,0x800u,0x60050u,0x90470u,0xefc00u,0x830u,0x60060u,0x90440u,0xefc30u,0x820u,0x60070u,0x90450u,0xefc20u,0x8d0u,0x60080u,0x904b0u,0xefcd0u,0x8c0u,0x60090u,0x904a0u,0xefcc0u,0x8f0u,0x600a0u,0x90490u,0xefcf0u,0x8e0u,0x600b0u,0x90480u,0xefce0u,0x890u,0x600c0u,0x904f0u,0xefc90u,0x880u,0x600d0u,0x904e0u,0xefc80u,0x8b0u,0x600e0u,0x904d0u,0xefcb0u,0x8a0u,0x600f0u,0x904c0u,0xefca0u,0x960u,0x60100u,0x90530u,0xefd60u,0x970u,0x60110u,0x90520u,0xefd70u,0x940u,0x60120u,0x90510u,0xefd40u,0x950u,0x60130u,0x90500u,0xefd50u,0x920u,0x60140u,0x90570u,0xefd20u,0x930u,0x60150u,0x90560u,0xefd30u,0x900u,0x60160u,0x90550u,0xefd00u,0x910u,0x60170u,0x90540u,0xefd10u,0x9e0u,0x60190u,0x905b0u,0xefde0u,0x9f0u,0x60180u,0x905a0u,0xefdf0u,0x9c0u,0x601b0u,0x90590u,0xefdc0u,0x9d0u,0x601a0u,0x90580u,0xefdd0u,0x9a0u,0x601d0u,0x905f0u,0xefda0u,0x9b0u,0x601c0u,0x905e0u,0xefdb0u,0x980u,0x601f0u,0x905d0u,0xefd80u,0x990u,0x601e0u,0x905c0u,0xefd90u,0xa60u,0x60210u,0xbfa40u,0xefe60u,0xa70u,0x60200u,0xbfa50u,0xefe70u,0xa40u,0x60230u,0xbfa60u,0xefe40u,0xa50u,0x60220u,0xbfa70u,0xefe50u,0xa20u,0x60250u,0xbfa00u,0xefe20u,0xa30u,0x60240u,0xbfa10u,0xefe30u,0xa00u,0x60270u,0xbfa20u,0xefe00u,0xa10u,0x60260u,0xbfa30u,0xefe10u,0x2fef0u,0x60290u,0xbfac0u,0xefef0u,0x2fee0u,0x60280u,0xbfad0u,0xefee0u,0x2fed0u,0x602b0u,0xbfae0u,0xefed0u,0x2fec0u,0x602a0u,0xbfaf0u,0xefec0u,0x2feb0u,0x602d0u,0xbfa80u,0xefeb0u,0x2fea0u,0x602c0u,0xbfa90u,0xefea0u,0x2fe90u,0x602f0u,0xbfaa0u,0xefe90u,0x2fe80u,0x602e0u,0xbfab0u,0xefe80u,0x2ff70u,0x60320u,0xbfb40u,0xeff70u,0x2ff60u,0x60330u,0xbfb50u,0xeff60u,0x2ff50u,0x60300u,0xbfb60u,0xeff50u,0x2ff40u,0x60310u,0xbfb70u,0xeff40u,0x2ff30u,0x60360u,0xbfb00u,0xeff30u,0x2ff20u,0x60370u,0xbfb10u,0xeff20u,0x2ff10u,0x60340u,0xbfb20u,0xeff10u,0x2ff00u,0x60350u,0xbfb30u,0xeff00u,0x2fff0u,0x603a0u,0xbfbd0u,0xefff0u,0x2ffe0u,0x603b0u,0xbfbc0u,0xeffe0u,0x2ffd0u,0x60380u,0xbfbf0u,0xeffd0u,0x2ffc0u,0x60390u,0xbfbe0u,0xeffc0u,0x2ffb0u,0x603e0u,0xbfb90u,0xeffb0u,0x2ffa0u,0x603f0u,0xbfb80u,0xeffa0u,0x2ff90u,0x603c0u,0xbfbb0u,0xeff90u,0x2ff80u,0x603d0u,0xbfba0u,0xeff80u,0x0u,0x80000000u,0x0u,0x18830u,0x0u,0x80000000u,0x0u,0x18830u,0x0u,0x80000000u,0x0u,0x18830u,0x0u,0x80000000u,0x0u,0x18830u,0x0u,0x80000000u,0x0u,0x18830u,0x0u,0x80000000u,0x0u,0x18830u,0x0u,0x80000000u,0x0u,0x18830u,0x0u,0x80000000u,0x0u,0x18830u,0x0u,0x80000000u,0x0u,0x18830u,0x0u,0x80000000u,0x0u,0x18830u,0x0u,0x80000000u,0x0u,0x18830u,0x0u,0x80000000u,0x0u,0x18830u,0x0u,0x80000000u,0x0u,0x18830u,0x0u,0x80000000u,0x0u,0x18830u,0x0u,0x80000000u,0x0u,0x18830u,0x0u,0x80000000u,0x0u,0x18830u,0x0u,0x80000000u,0x0u,0x188b2u,0x0u,0x80000000u,0x0u,0x188b2u,0x0u,0x80000000u,0x0u,0x188b2u,0x0u,0x80000000u,0x0u,0x188b2u,0x0u,0x80000000u,0x0u,0x188b2u,0x0u,0x80000000u,0x0u,0x188b2u,0x0u,0x80000000u,0x0u,0x188b2u,0x0u,0x80000000u,0x0u,0x188b2u,0x0u,0x80000000u,0x0u,0x18892u,0x0u,0x80000000u,0x0u,0x18892u,0x0u,0x80000000u,0x0u,0x18892u,0x0u,0x80000000u,0x0u,0x18892u,0x0u,0x80000000u,0x0u,0x18892u,0x0u,0x80000000u,0x0u,0x18892u,0x0u,0x80000000u,0x0u,0x18892u,0x0u,0x80000000u,0x0u,0x18892u,0x0u,0x80000000u,0x0u,0x1489au,0x0u,0x80000000u,0x0u,0x1489au,0x0u,0x80000000u,0x0u,0x1489au,0x0u,0x80000000u,0x0u,0x1489au,0x0u,0x80000000u,0x0u,0x1489au,0x0u,0x80000000u,0x0u,0x1489au,0x0u,0x80000000u,0x0u,0x1489au,0x0u,0x80000000u,0x0u,0x1489au,0x0u,0x80000000u,0x0u,0x34018u,0x0u,0x80000000u,0x0u,0x34018u,0x0u,0x80000000u,0x0u,0x34018u,0x0u,0x80000000u,0x0u,0x34018u,0x0u,0x80000000u,0x0u,0x34018u,0x0u,0x80000000u,0x0u,0x34018u,0x0u,0x80000000u,0x0u,0x34018u,0x0u,0x80000000u,0x0u,0x34018u,0x0u,0x80000000u,0x0u,0x36018u,0x0u,0x80000000u,0x0u,0x36018u,0x0u,0x80000000u,0x0u,0x36018u,0x0u,0x80000000u,0x0u,0x36018u,0x0u,0x80000000u,0x0u,0x36018u,0x0u,0x80000000u,0x0u,0x36018u,0x0u,0x80000000u,0x0u,0x36018u,0x0u,0x80000000u,0x0u,0x36018u,0x0u,0x80000000u,0x0u,0x36030u,0x0u,0x80000000u,0x0u,0x36030u,0x0u,0x80000000u,0x0u,0x36030u,0x0u,0x80000000u,0x0u,0x36030u,0x0u,0x80000000u,0x0u,0x36030u,0x0u,0x80000000u,0x0u,0x36030u,0x0u,0x80000000u,0x0u,0x36030u,0x0u,0x80000000u,0x0u,0x36030u,0x6840u,0x6860u,0x6820u,0x6820u,0x6848u,0x6868u,0x6828u,0x6830u,0x6850u,0x6870u,0x6830u,0x6840u,0x6858u,0x6878u,0x6838u,0x6850u,0x68c0u,0x68e0u,0x68a0u,0x68a0u,0x68c8u,0x68e8u,0x68a8u,0x68b0u,0x68d0u,0x68f0u,0x68b0u,0x68c0u,0x68d8u,0x68f8u,0x68b8u,0x68d0u,0x6960u,0x6940u,0x6900u,0x6900u,0x6968u,0x6948u,0x6908u,0x6910u,0x6970u,0x6950u,0x6910u,0x6960u,0x6978u,0x6958u,0x6918u,0x6970u,0x69e0u,0x69c0u,0x6980u,0x6980u,0x69e8u,0x69c8u,0x6988u,0x6990u,0x69f0u,0x69d0u,0x6990u,0x69e0u,0x69f8u,0x69d8u,0x6998u,0x69f0u,0x6c00u,0x6c20u,0x6c60u,0x6a20u,0x6c08u,0x6c28u,0x6c68u,0x6a30u,0x6c10u,0x6c30u,0x6c70u,0x6a40u,0x6c18u,0x6c38u,0x6c78u,0x6a50u,0x6c80u,0x6ca0u,0x6ce0u,0x6aa0u,0x6c88u,0x6ca8u,0x6ce8u,0x6ab0u,0x6c90u,0x6cb0u,0x6cf0u,0x6ac0u,0x6c98u,0x6cb8u,0x6cf8u,0x6ad0u,0x6d20u,0x6d00u,0x6d40u,0x6b00u,0x6d28u,0x6d08u,0x6d48u,0x6b10u,0x6d30u,0x6d10u,0x6d50u,0x6b60u,0x6d38u,0x6d18u,0x6d58u,0x6b70u,0x6da0u,0x6d80u,0x6dc0u,0x6b80u,0x6da8u,0x6d88u,0x6dc8u,0x6b90u,0x6db0u,0x6d90u,0x6dd0u,0x6be0u,0x6db8u,0x6d98u,0x6dd8u,0x6bf0u,0x7040u,0x7060u,0x7020u,0x6c60u,0x7048u,0x7068u,0x7028u,0x6c70u,0x7050u,0x7070u,0x7030u,0x6c00u,0x7058u,0x7078u,0x7038u,0x6c10u,0x70c0u,0x70e0u,0x70a0u,0x6ce0u,0x70c8u,0x70e8u,0x70a8u,0x6cf0u,0x70d0u,0x70f0u,0x70b0u,0x6c80u,0x70d8u,0x70f8u,0x70b8u,0x6c90u,0x7160u,0x7140u,0x7100u,0x6d40u,0x7168u,0x7148u,0x7108u,0x6d50u,0x7170u,0x7150u,0x7110u,0x6d20u,0x7178u,0x7158u,0x7118u,0x6d30u,0x71e0u,0x71c0u,0x7180u,0x6dc0u,0x71e8u,0x71c8u,0x7188u,0x6dd0u,0x71f0u,0x71d0u,0x7190u,0x6da0u,0x71f8u,0x71d8u,0x7198u,0x6db0u,0x7400u,0x7420u,0x7460u,0x6e60u,0x7408u,0x7428u,0x7468u,0x6e70u,0x7410u,0x7430u,0x7470u,0x6e00u,0x7418u,0x7438u,0x7478u,0x6e10u,0x7480u,0x74a0u,0x74e0u,0x6ee0u,0x7488u,0x74a8u,0x74e8u,0x6ef0u,0x7490u,0x74b0u,0x74f0u,0x6e80u,0x7498u,0x74b8u,0x74f8u,0x6e90u,0x7520u,0x7500u,0x7540u,0x6f40u,0x7528u,0x7508u,0x7548u,0x6f50u,0x7530u,0x7510u,0x7550u,0x6f20u,0x7538u,0x7518u,0x7558u,0x6f30u,0x75a0u,0x7580u,0x75c0u,0x6fc0u,0x75a8u,0x7588u,0x75c8u,0x6fd0u,0x75b0u,0x7590u,0x75d0u,0x6fa0u,0x75b8u,0x7598u,0x75d8u,0x6fb0u,0x6860u,0x1ea40u,0x1ec20u,0x2440u,0x6868u,0x1eed0u,0x1f0b0u,0x2450u,0x6870u,0x1f360u,0x1f540u,0x2460u,0x6878u,0x1f7f0u,0x1f9d0u,0x2470u,0x68e0u,0x1fc80u,0x1fe60u,0x2400u,0x68e8u,0x20110u,0x202f0u,0x2410u,0x68f0u,0x205a0u,0x20780u,0x2420u,0x68f8u,0x20a30u,0x20c10u,0x2430u,0x6940u,0x20ec0u,0x210a0u,0x25c0u,0x6948u,0x21350u,0x21530u,0x25d0u,0x6950u,0x217e0u,0x219c0u,0x25e0u,0x6958u,0x21c70u,0x21e50u,0x25f0u,0x69c0u,0x22100u,0x222e0u,0x2580u,0x69c8u,0x22590u,0x22770u,0x2590u,0x69d0u,0x22a20u,0x22c00u,0x25a0u,0x69d8u,0x22eb0u,0x23090u,0x25b0u,0x6a60u,0x1ea70u,0x1ec28u,0x2760u,0x6a68u,0x1ef00u,0x1f0b8u,0x2770u,0x6a70u,0x1f390u,0x1f548u,0x2740u,0x6a78u,0x1f820u,0x1f9d8u,0x2750u,0x6ae0u,0x1fcb0u,0x1fe68u,0x2720u,0x6ae8u,0x20140u,0x202f8u,0x2730u,0x6af0u,0x205d0u,0x20788u,0x2700u,0x6af8u,0x20a60u,0x20c18u,0x2710u,0x6b40u,0x20ef0u,0x210a8u,0x28e0u,0x6b48u,0x21380u,0x21538u,0x28f0u,0x6b50u,0x21810u,0x219c8u,0x28c0u,0x6b58u,0x21ca0u,0x21e58u,0x28d0u,0x6bc0u,0x22130u,0x222e8u,0x28a0u,0x6bc8u,0x225c0u,0x22778u,0x28b0u,0x6bd0u,0x22a50u,0x22c08u,0x2880u,0x6bd8u,0x22ee0u,0x23098u,0x2890u,0x6c20u,0x1eaa0u,0x1ec30u,0x2a40u,0x6c28u,0x1ef30u,0x1f0c0u,0x2a50u,0x6c30u,0x1f3c0u,0x1f550u,0x2a60u,0x6c38u,0x1f850u,0x1f9e0u,0x2a70u,0x6ca0u,0x1fce0u,0x1fe70u,0x2a00u,0x6ca8u,0x20170u,0x20300u,0x2a10u,0x6cb0u,0x20600u,0x20790u,0x2a20u,0x6cb8u,0x20a90u,0x20c20u,0x2a30u,0x6d00u,0x20f20u,0x210b0u,0x2bc0u,0x6d08u,0x213b0u,0x21540u,0x2bd0u,0x6d10u,0x21840u,0x219d0u,0x2be0u,0x6d18u,0x21cd0u,0x21e60u,0x2bf0u,0x6d80u,0x22160u,0x222f0u,0x2b80u,0x6d88u,0x225f0u,0x22780u,0x2b90u,0x6d90u,0x22a80u,0x22c10u,0x2ba0u,0x6d98u,0x22f10u,0x230a0u,0x2bb0u,0x6e20u,0x1ead0u,0x1ec38u,0x2d60u,0x6e28u,0x1ef60u,0x1f0c8u,0x2d70u,0x6e30u,0x1f3f0u,0x1f558u,0x2d40u,0x6e38u,0x1f880u,0x1f9e8u,0x2d50u,0x6ea0u,0x1fd10u,0x1fe78u,0x2d20u,0x6ea8u,0x201a0u,0x20308u,0x2d30u,0x6eb0u,0x20630u,0x20798u,0x2d00u,0x6eb8u,0x20ac0u,0x20c28u,0x2d10u,0x6f00u,0x20f50u,0x210b8u,0x2ee0u,0x6f08u,0x213e0u,0x21548u,0x2ef0u,0x6f10u,0x21870u,0x219d8u,0x2ec0u,0x6f18u,0x21d00u,0x21e68u,0x2ed0u,0x6f80u,0x22190u,0x222f8u,0x2ea0u,0x6f88u,0x22620u,0x22788u,0x2eb0u,0x6f90u,0x22ab0u,0x22c18u,0x2e80u,0x6f98u,0x22f40u,0x230a8u,0x2e90u,0x0u,0x40u,0x9000u,0xcc00u,0x80u,0xc0u,0x9010u,0xcc10u,0x110u,0x150u,0x9020u,0xcc20u,0x190u,0x1d0u,0x9030u,0xcc30u,0x220u,0x260u,0x9040u,0xcc40u,0x2a0u,0x2e0u,0x9050u,0xcc50u,0x330u,0x370u,0x9060u,0xcc60u,0x3b0u,0x3f0u,0x9070u,0xcc70u,0x440u,0x400u,0x9080u,0xcc80u,0x4c0u,0x480u,0x9090u,0xcc90u,0x550u,0x510u,0x90a0u,0xcca0u,0x5d0u,0x590u,0x90b0u,0xccb0u,0x660u,0x620u,0x90c0u,0xccc0u,0x6e0u,0x6a0u,0x90d0u,0xccd0u,0x770u,0x730u,0x90e0u,0xcce0u,0x7f0u,0x7b0u,0x90f0u,0xccf0u,0x10u,0x50u,0x9300u,0xcf00u,0x90u,0xd0u,0x9310u,0xcf10u,0x100u,0x140u,0x9320u,0xcf20u,0x180u,0x1c0u,0x9330u,0xcf30u,0x230u,0x270u,0x9340u,0xcf40u,0x2b0u,0x2f0u,0x9350u,0xcf50u,0x320u,0x360u,0x9360u,0xcf60u,0x3a0u,0x3e0u,0x9370u,0xcf70u,0x450u,0x410u,0x9380u,0xcf80u,0x4d0u,0x490u,0x9390u,0xcf90u,0x540u,0x500u,0x93a0u,0xcfa0u,0x5c0u,0x580u,0x93b0u,0xcfb0u,0x670u,0x630u,0x93c0u,0xcfc0u,0x6f0u,0x6b0u,0x93d0u,0xcfd0u,0x760u,0x720u,0x93e0u,0xcfe0u,0x7e0u,0x7a0u,0x93f0u,0xcff0u,0x20u,0x60u,0x9600u,0xd200u,0xa0u,0xe0u,0x9610u,0xd210u,0x130u,0x170u,0x9620u,0xd220u,0x1b0u,0x1f0u,0x9630u,0xd230u,0x200u,0x240u,0x9640u,0xd240u,0x280u,0x2c0u,0x9650u,0xd250u,0x310u,0x350u,0x9660u,0xd260u,0x390u,0x3d0u,0x9670u,0xd270u,0x460u,0x420u,0x9680u,0xd280u,0x4e0u,0x4a0u,0x9690u,0xd290u,0x570u,0x530u,0x96a0u,0xd2a0u,0x5f0u,0x5b0u,0x96b0u,0xd2b0u,0x640u,0x600u,0x96c0u,0xd2c0u,0x6c0u,0x680u,0x96d0u,0xd2d0u,0x750u,0x710u,0x96e0u,0xd2e0u,0x7d0u,0x790u,0x96f0u,0xd2f0u,0x30u,0x70u,0x9900u,0xd500u,0xb0u,0xf0u,0x9910u,0xd510u,0x120u,0x160u,0x9920u,0xd520u,0x1a0u,0x1e0u,0x9930u,0xd530u,0x210u,0x250u,0x9940u,0xd540u,0x290u,0x2d0u,0x9950u,0xd550u,0x300u,0x340u,0x9960u,0xd560u,0x380u,0x3c0u,0x9970u,0xd570u,0x470u,0x430u,0x9980u,0xd580u,0x4f0u,0x4b0u,0x9990u,0xd590u,0x560u,0x520u,0x99a0u,0xd5a0u,0x5e0u,0x5a0u,0x99b0u,0xd5b0u,0x650u,0x610u,0x99c0u,0xd5c0u,0x6d0u,0x690u,0x99d0u,0xd5d0u,0x740u,0x700u,0x99e0u,0xd5e0u,0x7c0u,0x780u,0x99f0u,0xd5f0u,0xc008u,0x12120u,0x12160u,0x12c00u,0xc018u,0x122b0u,0x122f0u,0x12c10u,0xc028u,0x12400u,0x12440u,0x12c20u,0xc038u,0x12590u,0x125d0u,0x12c30u,0xc048u,0x12760u,0x12720u,0x12c40u,0xc058u,0x128f0u,0x128b0u,0x12c50u,0xc068u,0x12a40u,0x12a00u,0x12c60u,0xc078u,0x12bd0u,0x12b90u,0x12c70u,0xc088u,0x12d20u,0x12d60u,0x12c80u,0xc098u,0x12eb0u,0x12ef0u,0x12c90u,0xc0a8u,0x13000u,0x13040u,0x12ca0u,0xc0b8u,0x13190u,0x131d0u,0x12cb0u,0xc0c8u,0x13360u,0x13320u,0x12cc0u,0xc0d8u,0x134f0u,0x134b0u,0x12cd0u,0xc0e8u,0x13640u,0x13600u,0x12ce0u,0xc0f8u,0x137d0u,0x13790u,0x12cf0u,0xc308u,0x12130u,0x12168u,0x12d00u,0xc318u,0x122a0u,0x122f8u,0x12d10u,0xc328u,0x12410u,0x12448u,0x12d20u,0xc338u,0x12580u,0x125d8u,0x12d30u,0xc348u,0x12770u,0x12728u,0x12d40u,0xc358u,0x128e0u,0x128b8u,0x12d50u,0xc368u,0x12a50u,0x12a08u,0x12d60u,0xc378u,0x12bc0u,0x12b98u,0x12d70u,0xc388u,0x12d30u,0x12d68u,0x12d80u,0xc398u,0x12ea0u,0x12ef8u,0x12d90u,0xc3a8u,0x13010u,0x13048u,0x12da0u,0xc3b8u,0x13180u,0x131d8u,0x12db0u,0xc3c8u,0x13370u,0x13328u,0x12dc0u,0xc3d8u,0x134e0u,0x134b8u,0x12dd0u,0xc3e8u,0x13650u,0x13608u,0x12de0u,0xc3f8u,0x137c0u,0x13798u,0x12df0u,0xc608u,0x12140u,0x12170u,0x12e00u,0xc618u,0x122d0u,0x122e0u,0x12e10u,0xc628u,0x12460u,0x12450u,0x12e20u,0xc638u,0x125f0u,0x125c0u,0x12e30u,0xc648u,0x12700u,0x12730u,0x12e40u,0xc658u,0x12890u,0x128a0u,0x12e50u,0xc668u,0x12a20u,0x12a10u,0x12e60u,0xc678u,0x12bb0u,0x12b80u,0x12e70u,0xc688u,0x12d40u,0x12d70u,0x12e80u,0xc698u,0x12ed0u,0x12ee0u,0x12e90u,0xc6a8u,0x13060u,0x13050u,0x12ea0u,0xc6b8u,0x131f0u,0x131c0u,0x12eb0u,0xc6c8u,0x13300u,0x13330u,0x12ec0u,0xc6d8u,0x13490u,0x134a0u,0x12ed0u,0xc6e8u,0x13620u,0x13610u,0x12ee0u,0xc6f8u,0x137b0u,0x13780u,0x12ef0u,0xc908u,0x12150u,0x12178u,0x12f00u,0xc918u,0x122c0u,0x122e8u,0x12f10u,0xc928u,0x12470u,0x12458u,0x12f20u,0xc938u,0x125e0u,0x125c8u,0x12f30u,0xc948u,0x12710u,0x12738u,0x12f40u,0xc958u,0x12880u,0x128a8u,0x12f50u,0xc968u,0x12a30u,0x12a18u,0x12f60u,0xc978u,0x12ba0u,0x12b88u,0x12f70u,0xc988u,0x12d50u,0x12d78u,0x12f80u,0xc998u,0x12ec0u,0x12ee8u,0x12f90u,0xc9a8u,0x13070u,0x13058u,0x12fa0u,0xc9b8u,0x131e0u,0x131c8u,0x12fb0u,0xc9c8u,0x13310u,0x13338u,0x12fc0u,0xc9d8u,0x13480u,0x134a8u,0x12fd0u,0xc9e8u,0x13630u,0x13618u,0x12fe0u,0xc9f8u,0x137a0u,0x13788u,0x12ff0u,0x8000u,0x1c300u,0x1ea40u,0x1ec20u,0x8080u,0x1c310u,0x100000u,0x1ec22u,0x8110u,0x1c320u,0x100000u,0x1ec24u,0x8190u,0x1c330u,0x100000u,0x1ec26u,0x8220u,0x1c340u,0x1ea40u,0x1ec28u,0x82a0u,0x1c350u,0x100000u,0x1ec2au,0x8330u,0x1c360u,0x100000u,0x1ec2cu,0x83b0u,0x1c370u,0x100000u,0x1ec2eu,0x8440u,0x1c380u,0x1ea40u,0x1ec30u,0x84c0u,0x1c390u,0x100000u,0x1ec32u,0x8550u,0x1c3a0u,0x100000u,0x1ec34u,0x85d0u,0x1c3b0u,0x100000u,0x1ec36u,0x8660u,0x1c3c0u,0x1ea40u,0x1ec38u,0x86e0u,0x1c3d0u,0x100000u,0x1ec3au,0x8770u,0x1c3e0u,0x100000u,0x1ec3cu,0x87f0u,0x1c3f0u,0x100000u,0x1ec3eu,0x8010u,0x1c400u,0x1ea70u,0x1ec20u,0x8090u,0x1c410u,0x100000u,0x1ec22u,0x8100u,0x1c420u,0x100000u,0x1ec24u,0x8180u,0x1c430u,0x100000u,0x1ec26u,0x8230u,0x1c440u,0x1ea70u,0x1ec28u,0x82b0u,0x1c450u,0x100000u,0x1ec2au,0x8320u,0x1c460u,0x100000u,0x1ec2cu,0x83a0u,0x1c470u,0x100000u,0x1ec2eu,0x8450u,0x1c480u,0x1ea70u,0x1ec30u,0x84d0u,0x1c490u,0x100000u,0x1ec32u,0x8540u,0x1c4a0u,0x100000u,0x1ec34u,0x85c0u,0x1c4b0u,0x100000u,0x1ec36u,0x8670u,0x1c4c0u,0x1ea70u,0x1ec38u,0x86f0u,0x1c4d0u,0x100000u,0x1ec3au,0x8760u,0x1c4e0u,0x100000u,0x1ec3cu,0x87e0u,0x1c4f0u,0x100000u,0x1ec3eu,0x8020u,0x1c500u,0x1eaa0u,0x1ec20u,0x80a0u,0x1c510u,0x100000u,0x1ec22u,0x8130u,0x1c520u,0x100000u,0x1ec24u,0x81b0u,0x1c530u,0x100000u,0x1ec26u,0x8200u,0x1c540u,0x1eaa0u,0x1ec28u,0x8280u,0x1c550u,0x100000u,0x1ec2au,0x8310u,0x1c560u,0x100000u,0x1ec2cu,0x8390u,0x1c570u,0x100000u,0x1ec2eu,0x8460u,0x1c580u,0x1eaa0u,0x1ec30u,0x84e0u,0x1c590u,0x100000u,0x1ec32u,0x8570u,0x1c5a0u,0x100000u,0x1ec34u,0x85f0u,0x1c5b0u,0x100000u,0x1ec36u,0x8640u,0x1c5c0u,0x1eaa0u,0x1ec38u,0x86c0u,0x1c5d0u,0x100000u,0x1ec3au,0x8750u,0x1c5e0u,0x100000u,0x1ec3cu,0x87d0u,0x1c5f0u,0x100000u,0x1ec3eu,0x8030u,0x1c600u,0x1ead0u,0x1ec20u,0x80b0u,0x1c610u,0x100000u,0x1ec22u,0x8120u,0x1c620u,0x100000u,0x1ec24u,0x81a0u,0x1c630u,0x100000u,0x1ec26u,0x8210u,0x1c640u,0x1ead0u,0x1ec28u,0x8290u,0x1c650u,0x100000u,0x1ec2au,0x8300u,0x1c660u,0x100000u,0x1ec2cu,0x8380u,0x1c670u,0x100000u,0x1ec2eu,0x8470u,0x1c680u,0x1ead0u,0x1ec30u,0x84f0u,0x1c690u,0x100000u,0x1ec32u,0x8560u,0x1c6a0u,0x100000u,0x1ec34u,0x85e0u,0x1c6b0u,0x100000u,0x1ec36u,0x8650u,0x1c6c0u,0x1ead0u,0x1ec38u,0x86d0u,0x1c6d0u,0x100000u,0x1ec3au,0x8740u,0x1c6e0u,0x100000u,0x1ec3cu,0x87c0u,0x1c6f0u,0x100000u,0x1ec3eu,0x27000u,0x1d480u,0x1d480u,0x80000000u,0x27002u,0x1d482u,0x100000u,0x80000000u,0x27004u,0x1d484u,0x100000u,0x80000000u,0x27006u,0x1d486u,0x100000u,0x80000000u,0x27200u,0x1d490u,0x1d480u,0x80000000u,0x27202u,0x1d492u,0x100000u,0x80000000u,0x27204u,0x1d494u,0x100000u,0x80000000u,0x27206u,0x1d496u,0x100000u,0x80000000u,0x27400u,0x1d4a0u,0x1d480u,0x80000000u,0x27402u,0x1d4a2u,0x100000u,0x80000000u,0x27404u,0x1d4a4u,0x100000u,0x80000000u,0x27406u,0x1d4a6u,0x100000u,0x80000000u,0x27600u,0x1d4b0u,0x1d480u,0x80000000u,0x27602u,0x1d4b2u,0x100000u,0x80000000u,0x27604u,0x1d4b4u,0x100000u,0x80000000u,0x27606u,0x1d4b6u,0x100000u,0x80000000u,0x27008u,0x1d488u,0x1d490u,0x80000000u,0x2700au,0x1d48au,0x100000u,0x80000000u,0x2700cu,0x1d48cu,0x100000u,0x80000000u,0x2700eu,0x1d48eu,0x100000u,0x80000000u,0x27208u,0x1d498u,0x1d490u,0x80000000u,0x2720au,0x1d49au,0x100000u,0x80000000u,0x2720cu,0x1d49cu,0x100000u,0x80000000u,0x2720eu,0x1d49eu,0x100000u,0x80000000u,0x27408u,0x1d4a8u,0x1d490u,0x80000000u,0x2740au,0x1d4aau,0x100000u,0x80000000u,0x2740cu,0x1d4acu,0x100000u,0x80000000u,0x2740eu,0x1d4aeu,0x100000u,0x80000000u,0x27608u,0x1d4b8u,0x1d490u,0x80000000u,0x2760au,0x1d4bau,0x100000u,0x80000000u,0x2760cu,0x1d4bcu,0x100000u,0x80000000u,0x2760eu,0x1d4beu,0x100000u,0x80000000u,0x26808u,0x1d4c0u,0x1d4a0u,0x80000000u,0x2680au,0x1d4c2u,0x100000u,0x80000000u,0x2680cu,0x1d4c4u,0x100000u,0x80000000u,0x2680eu,0x1d4c6u,0x100000u,0x80000000u,0x26a08u,0x1d4d0u,0x1d4a0u,0x80000000u,0x26a0au,0x1d4d2u,0x100000u,0x80000000u,0x26a0cu,0x1d4d4u,0x100000u,0x80000000u,0x26a0eu,0x1d4d6u,0x100000u,0x80000000u,0x26c08u,0x1d4e0u,0x1d4a0u,0x80000000u,0x26c0au,0x1d4e2u,0x100000u,0x80000000u,0x26c0cu,0x1d4e4u,0x100000u,0x80000000u,0x26c0eu,0x1d4e6u,0x100000u,0x80000000u,0x26e08u,0x1d4f0u,0x1d4a0u,0x80000000u,0x26e0au,0x1d4f2u,0x100000u,0x80000000u,0x26e0cu,0x1d4f4u,0x100000u,0x80000000u,0x26e0eu,0x1d4f6u,0x100000u,0x80000000u,0x100000u,0x1d4c8u,0x1d4b0u,0x80000000u,0x100000u,0x1d4cau,0x100000u,0x80000000u,0x100000u,0x1d4ccu,0x100000u,0x80000000u,0x100000u,0x1d4ceu,0x100000u,0x80000000u,0x100000u,0x1d4d8u,0x1d4b0u,0x80000000u,0x100000u,0x1d4dau,0x100000u,0x80000000u,0x100000u,0x1d4dcu,0x100000u,0x80000000u,0x100000u,0x1d4deu,0x100000u,0x80000000u,0x100000u,0x1d4e8u,0x1d4b0u,0x80000000u,0x100000u,0x1d4eau,0x100000u,0x80000000u,0x100000u,0x1d4ecu,0x100000u,0x80000000u,0x100000u,0x1d4eeu,0x100000u,0x80000000u,0x100000u,0x1d4f8u,0x1d4b0u,0x80000000u,0x100000u,0x1d4fau,0x100000u,0x80000000u,0x100000u,0x1d4fcu,0x100000u,0x80000000u,0x100000u,0x1d4feu,0x100000u,0x80000000u,0x30000u,0x60420u,0xbfc50u,0xf0000u,0x30010u,0x60430u,0xbfc40u,0xf0010u,0x30020u,0x60400u,0xbfc70u,0xf0020u,0x30030u,0x60410u,0xbfc60u,0xf0030u,0x30040u,0x60460u,0xbfc10u,0xf0040u,0x30050u,0x60470u,0xbfc00u,0xf0050u,0x30060u,0x60440u,0xbfc30u,0xf0060u,0x30070u,0x60450u,0xbfc20u,0xf0070u,0x30080u,0x604b0u,0xbfcd0u,0xf0080u,0x30090u,0x604a0u,0xbfcc0u,0xf0090u,0x300a0u,0x60490u,0xbfcf0u,0xf00a0u,0x300b0u,0x60480u,0xbfce0u,0xf00b0u,0x300c0u,0x604f0u,0xbfc90u,0xf00c0u,0x300d0u,0x604e0u,0xbfc80u,0xf00d0u,0x300e0u,0x604d0u,0xbfcb0u,0xf00e0u,0x300f0u,0x604c0u,0xbfca0u,0xf00f0u,0x30100u,0x60530u,0xbfd60u,0xf0100u,0x30110u,0x60520u,0xbfd70u,0xf0110u,0x30120u,0x60510u,0xbfd40u,0xf0120u,0x30130u,0x60500u,0xbfd50u,0xf0130u,0x30140u,0x60570u,0xbfd20u,0xf0140u,0x30150u,0x60560u,0xbfd30u,0xf0150u,0x30160u,0x60550u,0xbfd00u,0xf0160u,0x30170u,0x60540u,0xbfd10u,0xf0170u,0x30190u,0x605b0u,0xbfde0u,0xf0190u,0x30180u,0x605a0u,0xbfdf0u,0xf0180u,0x301b0u,0x60590u,0xbfdc0u,0xf01b0u,0x301a0u,0x60580u,0xbfdd0u,0xf01a0u,0x301d0u,0x605f0u,0xbfda0u,0xf01d0u,0x301c0u,0x605e0u,0xbfdb0u,0xf01c0u,0x301f0u,0x605d0u,0xbfd80u,0xf01f0u,0x301e0u,0x605c0u,0xbfd90u,0xf01e0u,0x30210u,0x60640u,0xbfe60u,0xf0210u,0x30200u,0x60650u,0xbfe70u,0xf0200u,0x30230u,0x60660u,0xbfe40u,0xf0230u,0x30220u,0x60670u,0xbfe50u,0xf0220u,0x30250u,0x60600u,0xbfe20u,0xf0250u,0x30240u,0x60610u,0xbfe30u,0xf0240u,0x30270u,0x60620u,0xbfe00u,0xf0270u,0x30260u,0x60630u,0xbfe10u,0xf0260u,0x30290u,0x606c0u,0xbfef0u,0xf0290u,0x30280u,0x606d0u,0xbfee0u,0xf0280u,0x302b0u,0x606e0u,0xbfed0u,0xf02b0u,0x302a0u,0x606f0u,0xbfec0u,0xf02a0u,0x302d0u,0x60680u,0xbfeb0u,0xf02d0u,0x302c0u,0x60690u,0xbfea0u,0xf02c0u,0x302f0u,0x606a0u,0xbfe90u,0xf02f0u,0x302e0u,0x606b0u,0xbfe80u,0xf02e0u,0x30320u,0x60740u,0xbff70u,0x11f720u,0x30330u,0x60750u,0xbff60u,0x11f730u,0x30300u,0x60760u,0xbff50u,0x11f700u,0x30310u,0x60770u,0xbff40u,0x11f710u,0x30360u,0x60700u,0xbff30u,0x11f760u,0x30370u,0x60710u,0xbff20u,0x11f770u,0x30340u,0x60720u,0xbff10u,0x11f740u,0x30350u,0x60730u,0xbff00u,0x11f750u,0x303a0u,0x8fbd0u,0xbfff0u,0x11f7a0u,0x303b0u,0x8fbc0u,0xbffe0u,0x11f7b0u,0x30380u,0x8fbf0u,0xbffd0u,0x11f780u,0x30390u,0x8fbe0u,0xbffc0u,0x11f790u,0x303e0u,0x8fb90u,0xbffb0u,0x11f7e0u,0x303f0u,0x8fb80u,0xbffa0u,0x11f7f0u,0x303c0u,0x8fbb0u,0xbff90u,0x11f7c0u,0x303d0u,0x8fba0u,0xbff80u,0x11f7d0u,0x0u,0x80000000u,0x0u,0x36030u,0x0u,0x80000000u,0x0u,0x36030u,0x0u,0x80000000u,0x0u,0x36030u,0x0u,0x80000000u,0x0u,0x36030u,0x0u,0x80000000u,0x0u,0x36030u,0x0u,0x80000000u,0x0u,0x36030u,0x0u,0x80000000u,0x0u,0x36030u,0x0u,0x80000000u,0x0u,0x36030u,0x0u,0x80000000u,0x0u,0x360b2u,0x0u,0x80000000u,0x0u,0x360b2u,0x0u,0x80000000u,0x0u,0x360b2u,0x0u,0x80000000u,0x0u,0x360b2u,0x0u,0x80000000u,0x0u,0x360b2u,0x0u,0x80000000u,0x0u,0x360b2u,0x0u,0x80000000u,0x0u,0x360b2u,0x0u,0x80000000u,0x0u,0x360b2u,0x0u,0x80000000u,0x0u,0x36092u,0x0u,0x80000000u,0x0u,0x36092u,0x0u,0x80000000u,0x0u,0x36092u,0x0u,0x80000000u,0x0u,0x36092u,0x0u,0x80000000u,0x0u,0x36092u,0x0u,0x80000000u,0x0u,0x36092u,0x0u,0x80000000u,0x0u,0x36092u,0x0u,0x80000000u,0x0u,0x36092u,0x0u,0x80000000u,0x0u,0x3609au,0x0u,0x80000000u,0x0u,0x3609au,0x0u,0x80000000u,0x0u,0x3609au,0x0u,0x80000000u,0x0u,0x3609au,0x0u,0x80000000u,0x0u,0x3609au,0x0u,0x80000000u,0x0u,0x3609au,0x0u,0x80000000u,0x0u,0x3609au,0x0u,0x80000000u,0x0u,0x3609au,0x0u,0x80000000u,0x0u,0x36059u,0x0u,0x80000000u,0x0u,0x36059u,0x0u,0x80000000u,0x0u,0x36059u,0x0u,0x80000000u,0x0u,0x36059u,0x0u,0x80000000u,0x0u,0x36059u,0x0u,0x80000000u,0x0u,0x36059u,0x0u,0x80000000u,0x0u,0x36059u,0x0u,0x80000000u,0x0u,0x36059u,0x0u,0x80000000u,0x0u,0x36059u,0x0u,0x80000000u,0x0u,0x36059u,0x0u,0x80000000u,0x0u,0x36059u,0x0u,0x80000000u,0x0u,0x36059u,0x0u,0x80000000u,0x0u,0x36059u,0x0u,0x80000000u,0x0u,0x36059u,0x0u,0x80000000u,0x0u,0x36059u,0x0u,0x80000000u,0x0u,0x36059u,0x0u,0x80000000u,0x0u,0x16071u,0x0u,0x80000000u,0x0u,0x16071u,0x0u,0x80000000u,0x0u,0x16071u,0x0u,0x80000000u,0x0u,0x16071u,0x0u,0x80000000u,0x0u,0x16071u,0x0u,0x80000000u,0x0u,0x16071u,0x0u,0x80000000u,0x0u,0x16071u,0x0u,0x80000000u,0x0u,0x16071u,0x0u,0x80000000u,0x0u,0x14071u,0x0u,0x80000000u,0x0u,0x14071u,0x0u,0x80000000u,0x0u,0x14071u,0x0u,0x80000000u,0x0u,0x14071u,0x0u,0x80000000u,0x0u,0x14071u,0x0u,0x80000000u,0x0u,0x14071u,0x0u,0x80000000u,0x0u,0x14071u,0x0u,0x80000000u,0x0u,0x14071u};

__global__ __launch_bounds__(256, 1) void swin_main(const uint16_t* x, const uint16_t* mask, char* ws, uint16_t* out,
                                                    const uint16_t* wqkv, const uint16_t* wo, const uint16_t* ls,
                                                    const uint16_t* w1, const uint16_t* w2, const uint16_t* rc,
                                                    const int* ridx) {
  __shared__ __attribute__((aligned(16))) char smem[LDS_BYTES];
  const int wave = __builtin_amdgcn_readfirstlane(threadIdx.x >> 6);
  const unsigned lds0 = (unsigned)(size_t)smem;
  asm volatile(
      ".p2align 5\n"
      "s_mov_b64 s[92:93], %0\n"
      "s_mov_b32 s40, %1\n"
      "s_mov_b32 s41, %2\n"
      "s_mov_b64 s[88:89], %3\n"
      "s_load_dwordx2 s[16:17], s[92:93], 0\n"
      "s_load_dwordx2 s[20:21], s[92:93], 32\n"
      "s_load_dwordx2 s[46:47], s[92:93], 40\n"
      "s_load_dwordx8 s[80:87], s[92:93], 48\n"
      "s_load_dwordx2 s[76:77], s[92:93], 80\n"
      "s_mov_b64 s[90:91], s[92:93]\n"
      "s_getpc_b64 s[98:99]\n"
      "L_ipf0_%=:\n"
      "v_mbcnt_lo_u32_b32 v8, -1, 0\n"
      "v_mbcnt_hi_u32_b32 v8, -1, v8\n"
      "v_lshlrev_b32 v239, 2, v8\n"
      "v_lshlrev_b32 v220, 4, v8\n"
      "s_cmp_lt_u32 s41, 209\n"
      "s_cselect_b32 s44, 20, 19\n"
      "s_lshl_b32 s92, s40, 8\n"
      "v_add_u32_e64 v69, s92, v239\n"
      "v_min_u32 v29, 704, v69\n"
      "v_add_u32 v29, 4096, v29\n"
      "v_min_u32 v69, 672, v69\n"
      "v_add_u32 v232, 122112, v69\n"
      "s_lshr_b32 s92, s41, 3\n"
      "s_lshl_b32 s92, s92, 7\n"
      "v_lshlrev_b32 v236, 7, v8\n"
      "s_mul_i32 s93, s92, 18\n"
      "s_mul_i32 s94, s40, 640\n"
      "v_min_u32 v237, 512, v236\n"
      "v_add_u32 v237, s94, v237\n"
      "v_min_u32 v237, 2176, v237\n"
      "v_add_u32 v237, s93, v237\n"
      "s_cmp_eq_u32 s40, 1\n"
      "s_cselect_b32 s93, 4096, 0\n"
      "s_add_u32 s92, s92, s93\n"
      "s_cmp_eq_u32 s40, 2\n"
      "s_cselect_b32 s93, 9, 8\n"
      "s_cselect_b64 s[72:73], s[98:99], s[88:89]\n"
      "s_cmp_lt_u32 s40, 2\n"
      "s_cselect_b32 s93, 27, s93\n"
      "s_mul_i32 s92, s92, s93\n"
      "s_sub_u32 s93, s93, 1\n"
      "s_lshl_b32 s93, s93, 7\n"
      "v_min_u32 v236, s93, v236\n"
      "v_add_u32 v236, s92, v236\n"
      "s_lshl_b32 s95, s40, 3\n"
      "v_lshrrev_b32 v238, 3, v8\n"
      "v_and_b32 v146, 7, v8\n"
      "v_add_u32 v147, s95, v238\n"
      "v_bfe_u32 v148, v147, 1, 3\n"
      "v_cmp_lt_u32 s[70:71], s44, v147\n"
      "v_min_u32_e64 v147, s44, v147\n"
      "s_mov_b32 s18, 100270464\n"
      "s_mov_b32 s19, 0x20000\n"
      "s_mov_b32 s78, 9604\n"
      "s_mov_b32 s79, 0x20000\n"
      "s_mov_b32 s22, 221184\n"
      "s_mov_b32 s23, 0x20000\n"
      "s_lshl_b32 s64, s40, 10\n"
      "v_add_u32 v36, s64, v220\n"
      "s_add_u32 s75, s64, 18432\n"
      "s_and_b32 s92, s40, 1\n"
      "s_lshl_b32 s92, s92, 2\n"
      "s_mul_i32 s93, s40, 9216\n"
      "v_and_b32 v149, 2, v238\n"
      "v_xor_b32 v149, v149, v146\n"
      "v_xor_b32_e64 v149, s92, v149\n"
      "v_lshl_add_u32 v149, v149, 4, s93\n"
      "v_mul_u32_u24 v96, 1152, v238\n"
      "v_add_u32 v96, v96, v149\n"
      "s_cmp_lt_u32 s40, 2\n"
      "s_cselect_b32 s65, s64, 163840\n"
      "s_waitcnt lgkmcnt(0)\n"
      "s_cselect_b64 s[72:73], s[20:21], s[72:73]\n"
      "global_load_dword v69, v69, s[86:87]\n"
      "global_load_dword a255, v236, s[72:73]\n"
      "global_load_dword a253, v237, s[46:47]\n"
      "s_mul_i32 s92, s40, 8192\n"
      "s_add_u32 s88, s88, s92\n"
      "s_addc_u32 s89, s89, 0\n"
      "s_add_u32 m0, s64, 124928\n"
      "s_movk_i32 s92, 0\n"
      "buffer_load_dwordx4 v36, s[76:79], s92 offen  lds\n"
      "s_add_u32 m0, s64, 129024\n"
      "s_movk_i32 s92, 4096\n"
      "buffer_load_dwordx4 v36, s[76:79], s92 offen  lds\n"
      "s_add_u32 m0, s64, 133120\n"
      "s_cmp_lt_u32 s40, 2\n"
      "s_cselect_b32 m0, m0, s65\n"
      "s_movk_i32 s92, 8192\n"
      "buffer_load_dwordx4 v36, s[76:79], s92 offen  lds\n"
      "s_load_dwordx16 s[0:15], s[82:83], 0\n"
      "s_load_dwordx16 s[48:63], s[84:85], 0\n"
      "s_load_dwordx16 s[24:39], s[84:85], 64\n"
      "s_load_dwordx2 s[66:67], s[80:81], 0\n"
      "s_movk_i32 s93, 4180\n"
      "s_cmp_lt_u32 s41, 209\n"
      "s_cselect_b32 s92, 209, 47\n"
      "s_cselect_b32 s93, 795, s93\n"
      "s_cmp_lt_u32 s41, 252\n"
      "s_cselect_b32 s94, 16, 15\n"
      "s_add_u32 s93, s93, s41\n"
      "v_lshl_add_u32 v236, v147, 8, s41\n"
      "v_mul_u32_u24 v237, s92, v147\n"
      "v_add_u32 v237, s93, v237\n"
      "v_cmp_lt_u32 vcc, s94, v147\n"
      "v_subrev_u32 v149, 5184, v237\n"
      "s_nop 0\n"
      "v_cndmask_b32 v236, v236, v237, vcc\n"
      "v_cmp_le_u32 vcc, 5184, v236\n"
      "v_mul_u32_u24 v68, 58255, v236\n"
      "v_lshrrev_b32 v68, 22, v68\n"
      "v_mul_u32_u24 v183, 72, v68\n"
      "v_sub_u32 v183, v236, v183\n"
      "v_min_u32 v237, 72, v149\n"
      "v_cndmask_b32 v68, v68, v237, vcc\n"
      "v_subrev_u32 v237, 72, v149\n"
      "v_min_u32 v237, 72, v237\n"
      "v_cndmask_b32 v183, v183, v237, vcc\n"
      "v_mad_u32_u24 v237, v68, 7, 9\n"
      "v_subrev_u32 v236, 0x1ff, v237\n"
      "v_min_u32 v237, v237, v236\n"
      "v_mad_u32_u24 v236, v183, 7, 9\n"
      "v_subrev_u32 v149, 0x1ff, v236\n"
      "v_min_u32 v236, v236, v149\n"
      "v_mul_u32_u24 v237, 0x1ff, v237\n"
      "v_add_u32 v237, v237, v236\n"
      "v_mul_u32_u24 v236, 0x180, v237\n"
      "v_bfrev_b32 v147, 1\n"
      "v_cndmask_b32 v236, v236, v147, s[70:71]\n"
      "v_xor_b32 v148, v148, v146\n"
      "v_lshl_add_u32 v236, v148, 4, v236\n"
      "s_add_u32 m0, s64, 149504\n"
      "s_nop 0\n"
      "buffer_load_dwordx4 v236, s[16:19], 0 offen  lds\n"
      "s_add_u32 m0, s64, 153472\n"
      "s_nop 0\n"
      "buffer_load_dwordx4 v236, s[16:19], 0 offen offset:128  lds\n"
      "s_add_u32 m0, s64, 157440\n"
      "s_nop 0\n"
      "buffer_load_dwordx4 v236, s[16:19], 0 offen offset:256  lds\n"
      "s_movk_i32 s95, 504\n"
      "s_and_b32 s92, s40, 1\n"
      "s_lshl_b32 s92, s92, 2\n"
      "v_lshrrev_b32 v236, 1, v238\n"
      "v_xor_b32 v236, v236, v146\n"
      "v_xor_b32 v236, s92, v236\n"
      "v_lshlrev_b32 v236, 4, v236\n"
      "s_lshl_b32 s92, s40, 3\n"
      "s_brev_b32 s43, 1\n"
      "s_cmp_lt_u32 s40, 2\n"
      "s_cselect_b32 s93, 32, -16\n"
      "s_cselect_b32 s94, 0, 128\n"
      "s_cselect_b32 s43, 256, s43\n"
      "v_add_u32 v237, s92, v238\n"
      "v_mul_u32_u24 v238, 37, v237\n"
      "v_lshrrev_b32_e64 v238, 8, v238\n"
      "v_mad_u32_u24 v238, v238, s95, v237\n"
      "v_mul_u32_u24 v238, 384, v238\n"
      "v_add_u32 v16, v238, v236\n"
      "v_add_u32 v238, s93, v237\n"
      "v_mul_u32_u24 v146, 37, v238\n"
      "v_lshrrev_b32 v146, 8, v146\n"
      "v_mad_u32_u24 v146, v146, s95, v238\n"
      "v_mul_u32_u24 v146, 384, v146\n"
      "v_add3_u32 v17, v146, v236, s94\n"
      "v_add_u32_e64 v238, 16, v237\n"
      "v_mul_u32_u24_e64 v237, 37, v238\n"
      "v_lshrrev_b32_e64 v237, 8, v237\n"
      "v_mad_u32_u24 v237, v237, s95, v238\n"
      "v_mul_u32_u24 v237, 384, v237\n"
      "s_movk_i32 s94, 128\n"
      "v_add3_u32 v18, v237, v236, s94\n"
      "v_add_u32 v19, 256, v16\n"
      "v_add_u32_e64 v15, s43, v17\n"
      "s_add_u32 s92, s41, 0\n"
      "s_mul_i32 s93, s92, 58255\n"
      "s_lshr_b32 s93, s93, 22\n"
      "s_mul_i32 s93, s93, 439\n"
      "s_add_u32 s45, s92, s93\n"
      "s_mul_i32 s45, s45, 2688\n"
      "s_add_u32 s45, s45, 589824\n"
      "s_add_u32 m0, s64, 0\n"
      "s_nop 0\n"
      "buffer_load_dwordx4 v16, s[16:19], s45 offen  lds\n"
      "s_add_u32 m0, s64, 4096\n"
      "s_nop 0\n"
      "buffer_load_dwordx4 v17, s[16:19], s45 offen  lds\n"
      "s_add_u32 m0, s64, 8192\n"
      "s_nop 0\n"
      "buffer_load_dwordx4 v18, s[16:19], s45 offen  lds\n"
      "s_add_u32 m0, s64, 12288\n"
      "s_nop 0\n"
      "buffer_load_dwordx4 v19, s[16:19], s45 offen  lds\n"
      "s_add_u32 m0, s65, 16384\n"
      "s_nop 0\n"
      "buffer_load_dwordx4 v15, s[16:19], s45 offen  lds\n"
      "s_add_u32 s92, s41, 0\n"
      "s_mul_i32 s93, s92, 58255\n"
      "s_lshr_b32 s93, s93, 22\n"
      "s_add_u32 s47, s92, s93\n"
      "s_mul_i32 s47, s47, 4802\n"
      "s_and_b32 s47, s47, -4\n"
      "s_add_u32 s92, s41, 256\n"
      "s_mul_i32 s93, s92, 58255\n"
      "s_lshr_b32 s93, s93, 22\n"
      "s_add_u32 s74, s92, s93\n"
      "s_mul_i32 s74, s74, 4802\n"
      "s_and_b32 s74, s74, -4\n"
      "s_mul_i32 s93, s93, 439\n"
      "s_add_u32 s46, s92, s93\n"
      "s_mul_i32 s46, s46, 2688\n"
      "s_add_u32 s46, s46, 589824\n"
      "v_subrev_u32 v237, 2, v8\n"
      "v_cmp_ge_u32 s[72:73], s44, v237\n"
      "v_min_u32_e64 v237, s44, v237\n"
      "s_movk_i32 s93, 4180\n"
      "s_cmp_lt_u32 s41, 209\n"
      "s_cselect_b32 s92, 209, 47\n"
      "s_cselect_b32 s93, 795, s93\n"
      "s_cmp_lt_u32 s41, 252\n"
      "s_cselect_b32 s94, 16, 15\n"
      "s_add_u32 s93, s93, s41\n"
      "v_lshl_add_u32 v236, v237, 8, s41\n"
      "v_mul_u32_u24 v238, s92, v237\n"
      "v_add_u32_e64 v238, s93, v238\n"
      "v_cmp_lt_u32 vcc, s94, v237\n"
      "v_subrev_u32 v146, 5184, v238\n"
      "s_nop 0\n"
      "v_cndmask_b32 v236, v236, v238, vcc\n"
      "v_cmp_le_u32 vcc, 5184, v236\n"
      "v_mul_u32_u24 v147, 58255, v236\n"
      "v_lshrrev_b32 v147, 22, v147\n"
      "v_mul_u32_u24 v148, 72, v147\n"
      "v_sub_u32_e64 v148, v236, v148\n"
      "v_min_u32 v238, 72, v146\n"
      "v_cndmask_b32 v147, v147, v238, vcc\n"
      "v_subrev_u32 v238, 72, v146\n"
      "v_min_u32 v238, 72, v238\n"
      "v_cndmask_b32 v148, v148, v238, vcc\n"
      "v_mul_u32_u24 v38, 73, v147\n"
      "v_add_u32 v38, v38, v148\n"
      "v_mul_u32_u24 v38, 4802, v38\n"
      "v_mul_u32_u24 v237, 0x1ff, v147\n"
      "v_add_u32_e64 v237, v237, v148\n"
      "v_mul_u32_u24 v237, 2688, v237\n"
      "v_add_u32 v237, 589824, v237\n"
      "v_cmp_eq_u32 vcc, 72, v147\n"
      "v_or_b32 v68, 0x80000000, v237\n"
      "s_nop 0\n"
      "v_cndmask_b32 v237, v237, v68, vcc\n"
      "v_cmp_eq_u32 vcc, 72, v148\n"
      "v_or_b32_e64 v68, 0x40000000, v237\n"
      "s_nop 0\n"
      "v_cndmask_b32 v237, v237, v68, vcc\n"
      "v_mov_b32 v238, 0x20000000\n"
      "v_cndmask_b32 v237, v238, v237, s[72:73]\n"
      "v_cndmask_b32 v38, v238, v38, s[72:73]\n"
      "v_mov_b32 v37, v238\n"
      "s_nop 1\n"
      "v_mov_b32_dpp v37, v237 wave_shr:1 row_mask:0xf bank_mask:0xf\n"
      "v_mov_b32 v79, 0\n"
      "v_mov_b32 v83, 0\n"
      "v_mov_b32 v87, 0\n"
      "v_mov_b32 v91, 0\n"
      "v_mov_b32 v95, 0\n"
      "s_lshl_b32 s94, s40, 2\n"
      "s_add_u32 s94, s94, 115456\n"
      "v_add_u32 v31, s94, v220\n"
      "v_and_b32 v68, 3, v8\n"
      "v_cmp_ne_u32 vcc, 0, v68\n"
      "v_mov_b32 v68, 0x3f803f80\n"
      "s_nop 0\n"
      "v_cndmask_b32_e64 v32, v68, 0, vcc\n"
      "v_mov_b32 v33, v32\n"
      "v_mov_b32 v34, v32\n"
      "v_mov_b32 v35, v32\n"
      "v_and_b32 v123, 15, v8\n"
      "v_lshrrev_b32_e64 v66, 4, v8\n"
      "v_mul_u32_u24 v123, 196, v123\n"
      "v_add_u32 v123, 124928, v123\n"
      "v_lshl_add_u32 v66, v66, 4, v123\n"
      "v_min_u32 v62, 192, v239\n"
      "v_add_u32 v62, 124928, v62\n"
      "global_load_dwordx4 v[236:239], v220, s[88:89]\n"
      "s_waitcnt lgkmcnt(0)\n"
      "s_pack_ll_b32_b16 s92, s0, s8\n"
      "s_waitcnt vmcnt(14)\n"
      "v_dot2_f32_bf16 v183, s92, v69, 0\n"
      "s_pack_hh_b32_b16 s93, s0, s8\n"
      "v_dot2_f32_bf16 v233, s93, v69, 0\n"
      "s_pack_ll_b32_b16 s94, s1, s9\n"
      "v_dot2_f32_bf16 v234, s94, v69, 0\n"
      "s_pack_hh_b32_b16 s95, s1, s9\n"
      "v_dot2_f32_bf16 v196, s95, v69, 0\n"
      "s_pack_ll_b32_b16 s92, s2, s10\n"
      "v_dot2_f32_bf16 v181, s92, v69, 0\n"
      "s_pack_hh_b32_b16 s93, s2, s10\n"
      "v_dot2_f32_bf16 v235, s93, v69, 0\n"
      "s_pack_ll_b32_b16 s94, s3, s11\n"
      "v_dot2_f32_bf16 v131, s94, v69, 0\n"
      "s_pack_hh_b32_b16 s95, s3, s11\n"
      "v_dot2_f32_bf16 v182, s95, v69, 0\n"
      "s_pack_ll_b32_b16 s92, s4, s12\n"
      "v_dot2_f32_bf16 v130, s92, v69, 0\n"
      "s_pack_hh_b32_b16 s93, s4, s12\n"
      "v_dot2_f32_bf16 v133, s93, v69, 0\n"
      "s_pack_ll_b32_b16 s94, s5, s13\n"
      "v_dot2_f32_bf16 v132, s94, v69, 0\n"
      "global_load_dwordx4 v[146:149], v220, s[88:89] offset:1024\n"
      "s_pack_hh_b32_b16 s95, s5, s13\n"
      "v_dot2_f32_bf16 v197, s95, v69, 0\n"
      "s_pack_ll_b32_b16 s92, s6, s14\n"
      "v_dot2_f32_bf16 v194, s92, v69, 0\n"
      "s_pack_hh_b32_b16 s93, s6, s14\n"
      "v_dot2_f32_bf16 v180, s93, v69, 0\n"
      "s_pack_ll_b32_b16 s94, s7, s15\n"
      "v_dot2_f32_bf16 v58, s94, v69, 0\n"
      "s_pack_hh_b32_b16 s95, s7, s15\n"
      "v_dot2_f32_bf16 v195, s95, v69, 0\n"
      "s_lshl_b32 s92, s40, 4\n"
      "s_lshr_b64 s[66:67], s[66:67], s92\n"
      "s_lshl_b32 s66, s66, 16\n"
      "v_mov_b32 v69, s66\n"
      "v_max_f32 v69, 0xc2c80000, v69\n"
      "v_min_f32 v69, 0x40930000, v69\n"
      "v_mul_f32 v69, 0x3fb8aa3b, v69\n"
      "v_exp_f32 v69, v69\n"
      "s_nop 0\n"
      "v_cvt_pk_bf16_f32 v69, v69, v69\n"
      "v_lshlrev_b32 v69, 16, v69\n"
      "v_max_f32 v183, 0, v183\n"
      "v_max_f32_e64 v233, 0, v233\n"
      "global_load_dwordx4 v[0:3], v220, s[88:89] offset:2048\n"
      "v_cvt_pk_bf16_f32 v183, v183, v233\n"
      "v_max_f32 v234, 0, v234\n"
      "v_max_f32 v196, 0, v196\n"
      "v_cvt_pk_bf16_f32 v234, v234, v196\n"
      "v_max_f32 v181, 0, v181\n"
      "v_max_f32 v235, 0, v235\n"
      "v_cvt_pk_bf16_f32 v181, v181, v235\n"
      "v_max_f32 v131, 0, v131\n"
      "v_max_f32 v182, 0, v182\n"
      "v_cvt_pk_bf16_f32 v131, v131, v182\n"
      "v_max_f32 v130, 0, v130\n"
      "v_max_f32 v133, 0, v133\n"
      "v_cvt_pk_bf16_f32 v130, v130, v133\n"
      "v_max_f32 v132, 0, v132\n"
      "v_max_f32 v197, 0, v197\n"
      "v_cvt_pk_bf16_f32 v132, v132, v197\n"
      "v_max_f32 v194, 0, v194\n"
      "v_max_f32 v180, 0, v180\n"
      "v_cvt_pk_bf16_f32 v194, v194, v180\n"
      "v_max_f32 v58, 0, v58\n"
      "v_max_f32 v195, 0, v195\n"
      "v_cvt_pk_bf16_f32 v58, v58, v195\n"
      "s_mov_b32 s42, 0\n"
      "s_add_u32 m0, s75, 0\n"
      "s_nop 0\n"
      "buffer_load_dwordx4 v96, s[20:23], s42 offen  lds\n"
      "s_pack_ll_b32_b16 s92, s48, s50\n"
      "v_dot2_f32_bf16 v180, s92, v183, 0\n"
      "s_pack_hh_b32_b16 s93, s48, s50\n"
      "v_dot2_f32_bf16 v233, s93, v183, 0\n"
      "s_pack_ll_b32_b16 s94, s49, s51\n"
      "v_dot2_f32_bf16 v235, s94, v183, 0\n"
      "s_pack_hh_b32_b16 s95, s49, s51\n"
      "v_dot2_f32_bf16 v182, s95, v183, 0\n"
      "s_pack_ll_b32_b16 s92, s52, s54\n"
      "v_dot2_f32_bf16 v180, s92, v234, v180\n"
      "s_pack_hh_b32_b16 s93, s52, s54\n"
      "v_dot2_f32_bf16 v233, s93, v234, v233\n"
      "s_pack_ll_b32_b16 s94, s53, s55\n"
      "v_dot2_f32_bf16 v235, s94, v234, v235\n"
      "s_pack_hh_b32_b16 s95, s53, s55\n"
      "v_dot2_f32_bf16 v182, s95, v234, v182\n"
      "s_pack_ll_b32_b16 s92, s56, s58\n"
      "v_dot2_f32_bf16 v180, s92, v181, v180\n"
      "s_pack_hh_b32_b16 s93, s56, s58\n"
      "v_dot2_f32_bf16 v233, s93, v181, v233\n"
      "s_pack_ll_b32_b16 s94, s57, s59\n"
      "v_dot2_f32_bf16 v235, s94, v181, v235\n"
      "s_add_u32 m0, s75, 3968\n"
      "s_nop 0\n"
      "buffer_load_dwordx4 v96, s[20:23], s42 offen offset:128  lds\n"
      "s_pack_hh_b32_b16 s95, s57, s59\n"
      "v_dot2_f32_bf16 v182, s95, v181, v182\n"
      "s_pack_ll_b32_b16 s92, s60, s62\n"
      "v_dot2_f32_bf16 v180, s92, v131, v180\n"
      "s_pack_hh_b32_b16 s93, s60, s62\n"
      "v_dot2_f32_bf16 v233, s93, v131, v233\n"
      "s_pack_ll_b32_b16 s94, s61, s63\n"
      "v_dot2_f32_bf16 v235, s94, v131, v235\n"
      "s_pack_hh_b32_b16 s95, s61, s63\n"
      "v_dot2_f32_bf16 v182, s95, v131, v182\n"
      "s_pack_ll_b32_b16 s92, s24, s26\n"
      "v_dot2_f32_bf16 v180, s92, v130, v180\n"
      "s_pack_hh_b32_b16 s93, s24, s26\n"
      "v_dot2_f32_bf16 v233, s93, v130, v233\n"
      "s_pack_ll_b32_b16 s94, s25, s27\n"
      "v_dot2_f32_bf16 v235, s94, v130, v235\n"
      "s_pack_hh_b32_b16 s95, s25, s27\n"
      "v_dot2_f32_bf16 v182, s95, v130, v182\n"
      "s_pack_ll_b32_b16 s92, s28, s30\n"
      "v_dot2_f32_bf16 v180, s92, v132, v180\n"
      "s_pack_hh_b32_b16 s93, s28, s30\n"
      "v_dot2_f32_bf16 v233, s93, v132, v233\n"
      "s_pack_ll_b32_b16 s94, s29, s31\n"
      "s_add_u32 m0, s75, 7936\n"
      "s_nop 0\n"
      "buffer_load_dwordx4 v96, s[20:23], s42 offen offset:256  lds\n"
      "v_dot2_f32_bf16 v235, s94, v132, v235\n"
      "s_pack_hh_b32_b16 s95, s29, s31\n"
      "v_dot2_f32_bf16 v182, s95, v132, v182\n"
      "s_pack_ll_b32_b16 s92, s32, s34\n"
      "v_dot2_f32_bf16 v180, s92, v194, v180\n"
      "s_pack_hh_b32_b16 s93, s32, s34\n"
      "v_dot2_f32_bf16 v233, s93, v194, v233\n"
      "s_pack_ll_b32_b16 s94, s33, s35\n"
      "v_dot2_f32_bf16 v235, s94, v194, v235\n"
      "s_pack_hh_b32_b16 s95, s33, s35\n"
      "v_dot2_f32_bf16 v182, s95, v194, v182\n"
      "s_pack_ll_b32_b16 s92, s36, s38\n"
      "v_dot2_f32_bf16 v180, s92, v58, v180\n"
      "s_pack_hh_b32_b16 s93, s36, s38\n"
      "v_dot2_f32_bf16 v233, s93, v58, v233\n"
      "s_pack_ll_b32_b16 s94, s37, s39\n"
      "v_dot2_f32_bf16 v235, s94, v58, v235\n"
      "s_pack_hh_b32_b16 s95, s37, s39\n"
      "v_dot2_f32_bf16 v182, s95, v58, v182\n"
      "s_load_dwordx2 s[28:29], s[90:91], 8\n"
      "s_load_dwordx2 s[34:35], s[90:91], 16\n"
      "s_load_dwordx2 s[24:25], s[90:91], 24\n"
      "s_add_u32 m0, s75, 11904\n"
      "s_nop 0\n"
      "buffer_load_dwordx4 v96, s[20:23], s42 offen offset:384  lds\n"
      "s_load_dwordx2 s[36:37], s[90:91], 40\n"
      "s_mov_b32 s26, 100270464\n"
      "s_mov_b32 s27, 0x20000\n"
      "s_mov_b32 s38, 73728\n"
      "s_mov_b32 s39, 0x20000\n"
      "s_mov_b32 s30, 25589860\n"
      "s_mov_b32 s31, 0x20000\n"
      "s_mov_b32 s53, 0x3fb8aa3b\n"
      "s_mov_b32 s51, 0x2a8cbccc\n"
      "s_mov_b32 s52, 0x38fba882\n"
      "s_mov_b32 s58, 0xffff0000\n"
      "s_mov_b32 s59, 0x0\n"
      "s_mov_b32 s60, 0x0\n"
      "s_mov_b32 s61, 0xffff\n"
      "s_mov_b32 s62, 0x0\n"
      "s_mov_b32 s63, 0xffff0000\n"
      "v_mov_b32 v68, 0x41f00000\n"
      "v_cmp_nle_f32 s[54:55], v69, v68\n"
      "v_mul_f32 v69, s53, v69\n"
      "s_nop 0\n"
      "v_readfirstlane_b32 s50, v69\n"
      "v_cvt_pk_bf16_f32 v180, v180, v180\n"
      "v_lshlrev_b32 v180, 16, v180\n"
      "s_add_u32 m0, s75, 15872\n"
      "s_nop 0\n"
      "buffer_load_dwordx4 v96, s[20:23], s42 offen offset:512  lds\n"
      "v_mul_f32 v180, 0xbfb8aa3b, v180\n"
      "v_exp_f32 v180, v180\n"
      "s_nop 0\n"
      "v_add_f32 v180, 1.0, v180\n"
      "v_rcp_f32 v180, v180\n"
      "s_nop 0\n"
      "v_cvt_pk_bf16_f32 v180, v180, v180\n"
      "v_lshlrev_b32 v180, 16, v180\n"
      "v_add_f32 v180, -0.5, v180\n"
      "v_mul_f32 v180, 0x41b8aa3b, v180\n"
      "ds_write_b32 v232, v180\n"
      "v_cvt_pk_bf16_f32 v233, v233, v233\n"
      "v_lshlrev_b32 v233, 16, v233\n"
      "v_mul_f32 v233, 0xbfb8aa3b, v233\n"
      "v_exp_f32 v233, v233\n"
      "s_nop 0\n"
      "v_add_f32 v233, 1.0, v233\n"
      "v_rcp_f32_e64 v233, v233\n"
      "s_nop 0\n"
      "v_cvt_pk_bf16_f32 v233, v233, v233\n"
      "v_lshlrev_b32 v233, 16, v233\n"
      "v_add_f32 v233, -0.5, v233\n"
      "v_mul_f32 v233, 0x41b8aa3b, v233\n"
      "ds_write_b32 v232, v233 offset:704\n"
      "v_cvt_pk_bf16_f32 v235, v235, v235\n"
      "v_lshlrev_b32 v235, 16, v235\n"
      "s_add_u32 m0, s75, 19840\n"
      "s_nop 0\n"
      "buffer_load_dwordx4 v96, s[20:23], s42 offen offset:640  lds\n"
      "v_mul_f32 v235, 0xbfb8aa3b, v235\n"
      "v_exp_f32 v235, v235\n"
      "s_nop 0\n"
      "v_add_f32 v235, 1.0, v235\n"
      "v_rcp_f32_e64 v235, v235\n"
      "s_nop 0\n"
      "v_cvt_pk_bf16_f32 v235, v235, v235\n"
      "v_lshlrev_b32 v235, 16, v235\n"
      "v_add_f32 v235, -0.5, v235\n"
      "v_mul_f32 v235, 0x41b8aa3b, v235\n"
      "ds_write_b32 v232, v235 offset:1408\n"
      "v_cvt_pk_bf16_f32 v182, v182, v182\n"
      "v_lshlrev_b32 v182, 16, v182\n"
      "v_mul_f32 v182, 0xbfb8aa3b, v182\n"
      "v_exp_f32 v182, v182\n"
      "s_nop 0\n"
      "v_add_f32 v182, 1.0, v182\n"
      "v_rcp_f32 v182, v182\n"
      "s_nop 0\n"
      "v_cvt_pk_bf16_f32 v182, v182, v182\n"
      "v_lshlrev_b32 v182, 16, v182\n"
      "v_add_f32 v182, -0.5, v182\n"
      "v_mul_f32 v182, 0x41b8aa3b, v182\n"
      "ds_write_b32 v232, v182 offset:2112\n"
      "s_mul_i32 s76, s40, 704\n"
      "s_add_u32 s76, s76, 122112\n"
      "s_mov_b32 s43, 36864\n"
      "s_add_u32 m0, s75, 24576\n"
      "s_nop 0\n"
      "buffer_load_dwordx4 v96, s[20:23], s43 offen  lds\n"
      "s_add_u32 m0, s75, 28544\n"
      "s_nop 0\n"
      "buffer_load_dwordx4 v96, s[20:23], s43 offen offset:128  lds\n"
      "s_add_u32 m0, s75, 32512\n"
      "s_nop 0\n"
      "buffer_load_dwordx4 v96, s[20:23], s43 offen offset:256  lds\n"
      "s_add_u32 m0, s75, 36480\n"
      "s_nop 0\n"
      "buffer_load_dwordx4 v96, s[20:23], s43 offen offset:384  lds\n"
      "s_add_u32 m0, s75, 40448\n"
      "s_nop 0\n"
      "buffer_load_dwordx4 v96, s[20:23], s43 offen offset:512  lds\n"
      "s_add_u32 m0, s75, 44416\n"
      "s_nop 0\n"
      "buffer_load_dwordx4 v96, s[20:23], s43 offen offset:640  lds\n"
      "s_mov_b32 s45, s46\n"
      "s_mov_b32 s49, s47\n"
      "s_mov_b32 s66, 0\n"
      "s_mov_b32 s67, 0\n"
      "s_mov_b32 s68, 0\n"
      "s_mov_b32 s69, 0\n"
      "s_mov_b32 s70, 0\n"
      "s_mov_b32 s71, 0\n"
      "s_mov_b32 s48, 0\n"
      "s_mov_b32 s33, 0x7fff7fff\n"
      "s_mov_b32 s42, 1\n"
      "s_mov_b32 s43, 5\n"
      "v_readlane_b32 s47, v38, 4\n"
      "s_nop 0\n"
      "s_and_b32 s47, s47, -4\n"
      "s_waitcnt vmcnt(12)\n"
      "v_add_u32 v116, 149504, v0\n"
      "v_add_u32 v143, 149504, v1\n"
      "v_add_u32 v76, 49152, v236\n"
      "v_add_u32 v77, 49152, v237\n"
      "v_add_u32 v108, 49152, v238\n"
      "v_add_u32 v78, 49152, v239\n"
      "v_add_u32 v82, 49152, v146\n"
      "s_waitcnt vmcnt(6)\n"
      "s_waitcnt lgkmcnt(0)\n"
      "s_barrier\n"
      "s_add_u32 m0, s75, 49152\n"
      "s_mov_b32 s78, 73728\n"
      "buffer_load_dwordx4 v96, s[20:23], s78 offen  lds\n"
      "s_add_u32 m0, s75, 53120\n"
      "ds_read_b128 a[216:219], v116\n"
      "ds_read_b64_tr_b16 a[48:49], v238\n"
      "ds_read_b128 a[220:223], v0\n"
      "ds_read_b64_tr_b16 a[2:3], v236 offset:512\n"
      "ds_read_b64_tr_b16 a[72:73], v236 offset:12288\n"
      "ds_read_b64_tr_b16 a[74:75], v236 offset:12800\n"
      "buffer_load_dwordx4 v96, s[20:23], s78 offen offset:128  lds\n"
      "ds_read_b64_tr_b16 a[0:1], v236\n"
      "ds_read_b64_tr_b16 a[50:51], v238 offset:512\n"
      "ds_read_b64_tr_b16 a[24:25], v237\n"
      "v_add_u32 v156, 6272, v66\n"
      "s_add_u32 m0, s75, 57088\n"
      "ds_read_b128 a[224:227], v0 offset:2048\n"
      "ds_read_b64_tr_b16 a[26:27], v237 offset:512\n"
      "buffer_load_dwordx4 v96, s[20:23], s78 offen offset:256  lds\n"
      "ds_read2_b32 v[68:69], v156 offset0:16 offset1:17\n"
      "ds_read_b32 v150, v123 offset:3328\n"
      "ds_read_b128 a[244:247], v1 offset:4096\n"
      "ds_read_b64_tr_b16 a[120:121], v238 offset:12288\n"
      "s_add_u32 m0, s75, 61056\n"
      "ds_read_b64_tr_b16 a[122:123], v238 offset:12800\n"
      "s_waitcnt lgkmcnt(8)\n"
      "v_mfma_f32_16x16x32_bf16 v[232:235], a[48:51], a[216:219], 0\n"
      "buffer_load_dwordx4 v96, s[20:23], s78 offen offset:384  lds\n"
      "ds_read_b64_tr_b16 a[98:99], v237 offset:12800\n"
      "v_mfma_f32_16x16x32_bf16 v[180:183], a[0:3], a[220:223], 0\n"
      "s_mov_b32 s79, 110592\n"
      "v_add_u32 v121, 3136, v66\n"
      "s_waitcnt lgkmcnt(6)\n"
      "v_mfma_f32_16x16x32_bf16 v[130:133], a[24:27], a[224:227], 0\n"
      "s_add_u32 m0, s75, 65024\n"
      "ds_read_b128 a[228:231], v0 offset:4096\n"
      "ds_read_b64_tr_b16 a[96:97], v237 offset:12288\n"
      "v_mfma_f32_16x16x32_bf16 v[194:197], a[24:27], a[220:223], 0\n"
      "buffer_load_dwordx4 v96, s[20:23], s78 offen offset:512  lds\n"
      "v_mfma_f32_16x16x32_bf16 v[170:173], a[48:51], a[224:227], 0\n"
      "ds_read_b128 a[236:239], v1\n"
      "ds_read2_b32 v[58:59], v121 offset0:18 offset1:19\n"
      "v_mfma_f32_16x16x32_bf16 v[100:103], a[0:3], a[224:227], 0\n"
      "ds_read_b128 a[232:235], v143\n"
      "s_add_u32 m0, s75, 68992\n"
      "ds_read2_b32 v[70:71], v156 offset0:18 offset1:19\n"
      "v_mfma_f32_16x16x32_bf16 v[208:211], a[72:75], a[220:223], 0\n"
      "buffer_load_dwordx4 v96, s[20:23], s78 offen offset:640  lds\n"
      "v_mfma_f32_16x16x32_bf16 v[244:247], a[0:3], a[216:219], 0\n"
      "ds_read_b128 a[252:255], v0 offset:6144\n"
      "s_waitcnt lgkmcnt(5)\n"
      "v_mfma_f32_16x16x32_bf16 v[240:243], a[96:99], a[224:227], 0\n"
      "ds_read2_b32 v[72:73], v156 offset0:32 offset1:33\n"
      "ds_read_b128 a[240:243], v1 offset:2048\n"
      "v_mfma_f32_16x16x32_bf16 v[252:255], a[96:99], a[228:231], 0\n"
      "s_waitcnt vmcnt(6)\n"
      "s_barrier\n"
      "global_load_dwordx4 v[4:7], v220, s[88:89] offset:3072\n"
      "ds_read_b64_tr_b16 a[4:5], v236 offset:24576\n"
      "ds_read_b64_tr_b16 a[28:29], v237 offset:24576\n"
      "s_add_u32 s88, s88, 4096\n"
      "s_addc_u32 s89, s89, 0\n"
      "ds_read_b64_tr_b16 a[54:55], v238 offset:25088\n"
      "ds_read_b64_tr_b16 a[124:125], v238 offset:36864\n"
      "ds_read_b64_tr_b16 a[126:127], v238 offset:37376\n"
      "ds_read_b64_tr_b16 a[100:101], v237 offset:36864\n"
      "v_mfma_f32_16x16x32_bf16 v[200:203], a[72:75], a[224:227], 0\n"
      "ds_read_b64_tr_b16 a[102:103], v237 offset:37376\n"
      "global_load_dwordx4 v[8:11], v220, s[88:89]\n"
      "ds_read_b64_tr_b16 a[52:53], v238 offset:24576\n"
      "ds_read_b64_tr_b16 a[76:77], v236 offset:36864\n"
      "v_mfma_f32_16x16x32_bf16 v[138:141], a[120:123], a[224:227], 0\n"
      "ds_read_b64_tr_b16 a[30:31], v237 offset:25088\n"
      "ds_read_b64_tr_b16 a[78:79], v236 offset:37376\n"
      "ds_read_b64_tr_b16 a[6:7], v236 offset:25088\n"
      "v_mfma_f32_16x16x32_bf16 v[224:227], a[96:99], a[216:219], 0\n"
      "global_load_dwordx4 v[12:15], v220, s[88:89] offset:1024\n"
      "v_mfma_f32_16x16x32_bf16 v[248:251], a[120:123], a[216:219], 0\n"
      "ds_read2_b32 v[50:51], v66 offset0:34 offset1:35\n"
      "ds_read2_b32 v[52:53], v121 offset0:0 offset1:1\n"
      "ds_read_b32 v104, v123 offset:192\n"
      "v_mfma_f32_16x16x32_bf16 v[216:219], a[72:75], a[216:219], 0\n"
      "ds_read_b128 a[224:227], v143 offset:4096\n"
      "v_lshl_add_u32 v150, v150, 2, s76\n"
      "v_mfma_f32_16x16x32_bf16 v[162:165], a[0:3], a[228:231], 0\n"
      "global_load_dwordx4 v[20:23], v220, s[88:89] offset:2048\n"
      "ds_read_b32 v155, v62 offset:9408\n"
      "v_mfma_f32_16x16x32_bf16 v[212:215], a[120:123], a[220:223], 0\n"
      "ds_read2_b32 v[42:43], v66 offset0:2 offset1:3\n"
      "s_waitcnt lgkmcnt(6)\n"
      "v_mfma_f32_16x16x32_bf16 v[212:215], a[124:127], a[236:239], v[212:215]\n"
      "ds_read2_b32 v[62:63], v121 offset0:34 offset1:35\n"
      "ds_read_b128 a[248:251], v116 offset:4096\n"
      "v_mfma_f32_16x16x32_bf16 v[190:193], a[96:99], a[220:223], 0\n"
      "global_load_dwordx4 v[24:27], v220, s[88:89] offset:3072\n"
      "v_mfma_f32_16x16x32_bf16 v[220:223], a[48:51], a[220:223], 0\n"
      "ds_read2_b32 v[44:45], v66 offset0:16 offset1:17\n"
      "s_mov_b32 s82, 0xffff\n"
      "ds_read2_b32 v[46:47], v66 offset0:18 offset1:19\n"
      "v_mfma_f32_16x16x32_bf16 v[220:223], a[52:55], a[236:239], v[220:223]\n"
      "ds_read2_b32 v[60:61], v121 offset0:32 offset1:33\n"
      "v_lshl_add_u32 v73, v73, 2, s76\n"
      "s_add_u32 m0, s75, 73728\n"
      "v_mfma_f32_16x16x32_bf16 v[190:193], a[100:103], a[236:239], v[190:193]\n"
      "buffer_load_dwordx4 v96, s[20:23], s79 offen  lds\n"
      "v_mfma_f32_16x16x32_bf16 v[166:169], a[24:27], a[216:219], 0\n"
      "s_add_u32 m0, s75, 77696\n"
      "ds_read2_b32 v[54:55], v121 offset0:2 offset1:3\n"
      "v_mfma_f32_16x16x32_bf16 v[180:183], a[4:7], a[236:239], v[180:183]\n"
      "ds_read2_b32 v[48:49], v66 offset0:32 offset1:33\n"
      "s_mov_b32 s78, 147456\n"
      "v_mfma_f32_16x16x32_bf16 v[194:197], a[28:31], a[236:239], v[194:197]\n"
      "ds_read2_b32 v[56:57], v121 offset0:16 offset1:17\n"
      "ds_read2_b32 v[74:75], v156 offset0:34 offset1:35\n"
      "v_mfma_f32_16x16x32_bf16 v[232:235], a[52:55], a[232:235], v[232:235]\n"
      "buffer_load_dwordx4 v96, s[20:23], s79 offen offset:128  lds\n"
      "v_mfma_f32_16x16x32_bf16 v[166:169], a[28:31], a[232:235], v[166:169]\n"
      "ds_read_b32 v157, v123 offset:6464\n"
      "v_lshl_add_u32 v70, v70, 2, s76\n"
      "v_mfma_f32_16x16x32_bf16 v[120:123], a[72:75], a[228:231], 0\n"
      "ds_read2_b32 v[64:65], v156 offset0:0 offset1:1\n"
      "s_add_u32 m0, s75, 81664\n"
      "v_mfma_f32_16x16x32_bf16 v[100:103], a[4:7], a[240:243], v[100:103]\n"
      "ds_read_b128 a[216:219], v0 offset:8192\n"
      "s_mov_b32 s91, 0x1ffff\n"
      "v_mfma_f32_16x16x32_bf16 v[244:247], a[4:7], a[232:235], v[244:247]\n"
      "buffer_load_dwordx4 v96, s[20:23], s79 offen offset:256  lds\n"
      "v_mfma_f32_16x16x32_bf16 v[240:243], a[100:103], a[240:243], v[240:243]\n"
      "s_mov_b32 s83, 0x0\n"
      "s_waitcnt lgkmcnt(4)\n"
      "v_lshl_add_u32 v63, v63, 2, s76\n"
      "v_mfma_f32_16x16x32_bf16 v[216:219], a[76:79], a[232:235], v[216:219]\n"
      "s_mov_b32 s90, 0xffffffff\n"
      "v_lshl_add_u32 v155, v155, 2, s76\n"
      "v_lshl_add_u32 v46, v46, 2, s76\n"
      "v_mfma_f32_16x16x32_bf16 v[208:211], a[76:79], a[236:239], v[208:211]\n"
      "ds_read_b128 a[236:239], v1 offset:10240\n"
      "v_lshl_add_u32 v54, v54, 2, s76\n"
      "s_add_u32 m0, s75, 85632\n"
      "v_mfma_f32_16x16x32_bf16 v[124:127], a[120:123], a[228:231], 0\n"
      "buffer_load_dwordx4 v96, s[20:23], s79 offen offset:384  lds\n"
      "s_add_u32 m0, s75, 89600\n"
      "v_mfma_f32_16x16x32_bf16 v[124:127], a[124:127], a[244:247], v[124:127]\n"
      "s_waitcnt vmcnt(9)\n"
      "s_barrier\n"
      "ds_read_b64_tr_b16 a[8:9], v76\n"
      "ds_read_b64_tr_b16 a[104:105], v77 offset:12288\n"
      "ds_read_b64_tr_b16 a[128:129], v108 offset:12288\n"
      "ds_read_b64_tr_b16 a[10:11], v76 offset:512\n"
      "ds_read_b64_tr_b16 a[32:33], v77\n"
      "v_mfma_f32_16x16x32_bf16 v[158:161], a[48:51], a[228:231], 0\n"
      "ds_read_b64_tr_b16 a[56:57], v108\n"
      "ds_read_b64_tr_b16 a[34:35], v77 offset:512\n"
      "buffer_load_dwordx4 v96, s[20:23], s79 offen offset:512  lds\n"
      "ds_read_b64_tr_b16 a[80:81], v76 offset:12288\n"
      "v_mfma_f32_16x16x32_bf16 v[130:133], a[28:31], a[240:243], v[130:133]\n"
      "ds_read_b64_tr_b16 a[130:131], v108 offset:12800\n"
      "ds_read_b64_tr_b16 a[58:59], v108 offset:512\n"
      "s_add_u32 m0, s75, 93568\n"
      "ds_read_b64_tr_b16 a[106:107], v77 offset:12800\n"
      "ds_read_b64_tr_b16 a[82:83], v76 offset:12800\n"
      "buffer_load_dwordx4 v96, s[20:23], s79 offen offset:640  lds\n"
      "v_lshl_add_u32 v49, v49, 2, s76\n"
      "v_lshl_add_u32 v55, v55, 2, s76\n"
      "v_mfma_f32_16x16x32_bf16 v[134:137], a[24:27], a[228:231], 0\n"
      "ds_read_b32 v155, v155\n"
      "v_lshl_add_u32 v50, v50, 2, s76\n"
      "v_mfma_f32_16x16x32_bf16 v[224:227], a[100:103], a[232:235], v[224:227]\n"
      "ds_read2_b32 v[40:41], v66 offset0:0 offset1:1\n"
      "s_add_u32 m0, s75, 0\n"
      "v_lshl_add_u32 v71, v71, 2, s76\n"
      "v_mfma_f32_16x16x32_bf16 v[252:255], a[100:103], a[244:247], v[252:255]\n"
      "buffer_load_dwordx4 v96, s[20:23], s78 offen  lds\n"
      "v_lshl_add_u32 v104, v104, 2, s76\n"
      "v_mfma_f32_16x16x32_bf16 v[200:203], a[76:79], a[240:243], v[200:203]\n"
      "s_add_u32 m0, s75, 3968\n"
      "v_lshl_add_u32 v61, v61, 2, s76\n"
      "v_lshl_add_u32 v58, v58, 2, s76\n"
      "v_mfma_f32_16x16x32_bf16 v[170:173], a[52:55], a[240:243], v[170:173]\n"
      "ds_read_b128 a[220:223], v0 offset:10240\n"
      "s_waitcnt lgkmcnt(3)\n"
      "v_lshl_add_u32 v65, v65, 2, s76\n"
      "v_lshl_add_u32 v72, v72, 2, s76\n"
      "v_mfma_f32_16x16x32_bf16 v[248:251], a[124:127], a[232:235], v[248:251]\n"
      "buffer_load_dwordx4 v96, s[20:23], s78 offen offset:128  lds\n"
      "v_mfma_f32_16x16x32_bf16 v[248:251], a[128:131], a[248:251], v[248:251]\n"
      "ds_read_b128 a[228:231], v1 offset:6144\n"
      "v_lshl_add_u32 v69, v69, 2, s76\n"
      "v_mfma_f32_16x16x32_bf16 v[100:103], a[8:11], a[216:219], v[100:103]\n"
      "ds_read2_b32 v[66:67], v156 offset0:2 offset1:3\n"
      "v_lshl_add_u32 v43, v43, 2, s76\n"
      "v_mfma_f32_16x16x32_bf16 v[138:141], a[124:127], a[240:243], v[138:141]\n"
      "s_add_u32 m0, s75, 7936\n"
      "ds_read_b32 v73, v73\n"
      "v_mfma_f32_16x16x32_bf16 v[162:165], a[4:7], a[244:247], v[162:165]\n"
      "buffer_load_dwordx4 v96, s[20:23], s78 offen offset:256  lds\n"
      "v_mfma_f32_16x16x32_bf16 v[190:193], a[104:107], a[252:255], v[190:193]\n"
      "ds_read_b32 v72, v72\n"
      "s_waitcnt lgkmcnt(4)\n"
      "v_mfma_f32_16x16x32_bf16 v[124:127], a[128:131], a[220:223], v[124:127]\n"
      "ds_read_b128 a[232:235], v1 offset:8192\n"
      "v_lshl_add_u32 v41, v41, 2, s76\n"
      "v_mfma_f32_16x16x32_bf16 v[220:223], a[56:59], a[252:255], v[220:223]\n"
      "s_add_u32 m0, s75, 11904\n"
      "ds_read_b32 v50, v50\n"
      "v_lshl_add_u32 v51, v51, 2, s76\n"
      "v_mfma_f32_16x16x32_bf16 v[120:123], a[76:79], a[244:247], v[120:123]\n"
      "buffer_load_dwordx4 v96, s[20:23], s78 offen offset:384  lds\n"
      "v_mfma_f32_16x16x32_bf16 v[240:243], a[104:107], a[216:219], v[240:243]\n"
      "s_add_u32 m0, s75, 15872\n"
      "ds_read_b32 v71, v71\n"
      "v_mfma_f32_16x16x32_bf16 v[224:227], a[104:107], a[248:251], v[224:227]\n"
      "ds_read_b32 v61, v61\n"
      "v_lshl_add_u32 v47, v47, 2, s76\n"
      "v_mfma_f32_16x16x32_bf16 v[200:203], a[80:83], a[216:219], v[200:203]\n"
      "ds_read_b128 a[240:243], v116 offset:8192\n"
      "v_readlane_b32 s86, v38, s43\n"
      "v_mfma_f32_16x16x32_bf16 v[170:173], a[56:59], a[216:219], v[170:173]\n"
      "buffer_load_dwordx4 v96, s[20:23], s78 offen offset:512  lds\n"
      "v_lshl_add_u32 v42, v42, 2, s76\n"
      "s_mov_b32 s79, 184320\n"
      "v_mfma_f32_16x16x32_bf16 v[166:169], a[32:35], a[248:251], v[166:169]\n"
      "s_add_u32 m0, s75, 19840\n"
      "v_lshl_add_u32 v40, v40, 2, s76\n"
      "v_lshl_add_u32 v75, v75, 2, s76\n"
      "v_mov_b32 v105, 0xf149f2ca\n"
      "v_mfma_f32_16x16x32_bf16 v[120:123], a[80:83], a[220:223], v[120:123]\n"
      "ds_read_b32 v69, v69\n"
      "v_lshl_add_u32 v48, v48, 2, s76\n"
      "v_lshl_add_u32 v44, v44, 2, s76\n"
      "v_mfma_f32_16x16x32_bf16 v[180:183], a[8:11], a[252:255], v[180:183]\n"
      "buffer_load_dwordx4 v96, s[20:23], s78 offen offset:640  lds\n"
      "v_mfma_f32_16x16x32_bf16 v[194:197], a[32:35], a[252:255], v[194:197]\n"
      "s_add_u32 m0, s75, 24576\n"
      "v_lshl_add_u32 v64, v64, 2, s76\n"
      "v_lshl_add_u32 v157, v157, 2, s76\n"
      "v_mfma_f32_16x16x32_bf16 v[232:235], a[56:59], a[248:251], v[232:235]\n"
      "s_waitcnt vmcnt(6)\n"
      "s_barrier\n"
      "ds_read_b64_tr_b16 a[108:109], v77 offset:36864\n"
      "ds_read_b64_tr_b16 a[132:133], v108 offset:36864\n"
      "v_mfma_f32_16x16x32_bf16 v[244:247], a[8:11], a[248:251], v[244:247]\n"
      "ds_read_b64_tr_b16 a[110:111], v77 offset:37376\n"
      "ds_read_b64_tr_b16 a[38:39], v77 offset:25088\n"
      "v_mfma_f32_16x16x32_bf16 v[158:161], a[52:55], a[244:247], v[158:161]\n"
      "ds_read_b64_tr_b16 a[134:135], v108 offset:37376\n"
      "ds_read_b64_tr_b16 a[84:85], v76 offset:36864\n"
      "ds_read_b64_tr_b16 a[12:13], v76 offset:24576\n"
      "ds_read_b64_tr_b16 a[36:37], v77 offset:24576\n"
      "ds_read_b64_tr_b16 a[60:61], v108 offset:24576\n"
      "v_mfma_f32_16x16x32_bf16 v[158:161], a[56:59], a[220:223], v[158:161]\n"
      "ds_read_b64_tr_b16 a[14:15], v76 offset:25088\n"
      "ds_read_b64_tr_b16 a[62:63], v108 offset:25088\n"
      "ds_read_b64_tr_b16 a[86:87], v76 offset:37376\n"
      "v_mfma_f32_16x16x32_bf16 v[216:219], a[80:83], a[248:251], v[216:219]\n"
      "buffer_load_dwordx4 v96, s[20:23], s79 offen  lds\n"
      "v_mfma_f32_16x16x32_bf16 v[134:137], a[28:31], a[244:247], v[134:137]\n"
      "ds_read_b32 v43, v43\n"
      "s_add_u32 m0, s75, 28544\n"
      "v_lshl_add_u32 v45, v45, 2, s76\n"
      "v_mfma_f32_16x16x32_bf16 v[134:137], a[32:35], a[220:223], v[134:137]\n"
      "ds_read_b128 a[244:247], v0 offset:12288\n"
      "v_lshl_add_u32 v56, v56, 2, s76\n"
      "v_mfma_f32_16x16x32_bf16 v[130:133], a[32:35], a[216:219], v[130:133]\n"
      "ds_read_b32 v65, v65\n"
      "buffer_load_dwordx4 v96, s[20:23], s79 offen offset:128  lds\n"
      "s_mov_b32 s78, 0\n"
      "v_mfma_f32_16x16x32_bf16 v[138:141], a[128:131], a[216:219], v[138:141]\n"
      "s_add_u32 m0, s75, 32512\n"
      "v_lshl_add_u32 v52, v52, 2, s76\n"
      "s_waitcnt lgkmcnt(3)\n"
      "v_mfma_f32_16x16x32_bf16 v[170:173], a[60:63], a[232:235], v[170:173]\n"
      "ds_read_b32 v104, v104\n"
      "ds_read_b32 v40, v40\n"
      "v_lshl_add_u32 v60, v60, 2, s76\n"
      "v_mfma_f32_16x16x32_bf16 v[212:215], a[128:131], a[252:255], v[212:215]\n"
      "buffer_load_dwordx4 v96, s[20:23], s79 offen offset:256  lds\n"
      "v_mfma_f32_16x16x32_bf16 v[208:211], a[80:83], a[252:255], v[208:211]\n"
      "ds_read_b32 v44, v44\n"
      "v_lshl_add_u32 v67, v67, 2, s76\n"
      "v_mfma_f32_16x16x32_bf16 v[208:211], a[84:87], a[228:231], v[208:211]\n"
      "ds_read_b128 a[216:219], v143 offset:8192\n"
      "v_lshl_add_u32 v74, v74, 2, s76\n"
      "v_mfma_f32_16x16x32_bf16 v[252:255], a[104:107], a[220:223], v[252:255]\n"
      "ds_read_b32 v48, v48\n"
      "s_add_u32 m0, s75, 36480\n"
      "v_mfma_f32_16x16x32_bf16 v[252:255], a[108:111], a[236:239], v[252:255]\n"
      "buffer_load_dwordx4 v96, s[20:23], s79 offen offset:384  lds\n"
      "v_cndmask_b32 v30, v105, v155, s[90:91]\n"
      "v_mfma_f32_16x16x32_bf16 v[124:127], a[132:135], a[236:239], v[124:127]\n"
      "ds_read_b32 v60, v60\n"
      "v_lshl_add_u32 v66, v66, 2, s76\n"
      "v_mfma_f32_16x16x32_bf16 v[120:123], a[84:87], a[236:239], v[120:123]\n"
      "s_add_u32 m0, s75, 40448\n"
      "ds_read_b32 v75, v75\n"
      "v_mfma_f32_16x16x32_bf16 v[244:247], a[12:15], a[224:227], v[244:247]\n"
      "ds_read_b32 v157, v157\n"
      "v_lshl_add_u32 v57, v57, 2, s76\n"
      "v_mfma_f32_16x16x32_bf16 v[162:165], a[8:11], a[220:223], v[162:165]\n"
      "buffer_load_dwordx4 v96, s[20:23], s79 offen offset:512  lds\n"
      "v_mfma_f32_16x16x32_bf16 v[158:161], a[60:63], a[236:239], v[158:161]\n"
      "ds_read_b128 a[248:251], v0 offset:14336\n"
      "s_add_u32 m0, s75, 44416\n"
      "ds_read_b32 v45, v45\n"
      "v_mfma_f32_16x16x32_bf16 v[240:243], a[108:111], a[232:235], v[240:243]\n"
      "ds_read_b32 v64, v64\n"
      "ds_read_b32 v70, v70\n"
      "v_mfma_f32_16x16x32_bf16 v[166:169], a[36:39], a[224:227], v[166:169]\n"
      "buffer_load_dwordx4 v96, s[20:23], s79 offen offset:640  lds\n"
      "v_mfma_f32_16x16x32_bf16 v[248:251], a[132:135], a[224:227], v[248:251]\n"
      "ds_read_b128 a[252:255], v0 offset:16384\n"
      "s_add_u32 m0, s75, 48384\n"
      "v_mfma_f32_16x16x32_bf16 v[212:215], a[132:135], a[228:231], v[212:215]\n"
      "ds_read_b32 v150, v150\n"
      "v_lshl_add_u32 v53, v53, 2, s76\n"
      "v_mfma_f32_16x16x32_bf16 v[134:137], a[36:39], a[236:239], v[134:137]\n"
      "ds_read_b32 v63, v63\n"
      "ds_read_b32 v53, v53\n"
      "v_mfma_f32_16x16x32_bf16 v[162:165], a[12:15], a[236:239], v[162:165]\n"
      "buffer_load_dwordx4 v96, s[20:23], s78 offen offset:768  lds\n"
      "v_mfma_f32_16x16x32_bf16 v[194:197], a[36:39], a[228:231], v[194:197]\n"
      "s_add_u32 m0, s75, 52352\n"
      "s_waitcnt vmcnt(7)\n"
      "s_barrier\n"
      "ds_read_b64_tr_b16 a[136:137], v238 offset:12288\n"
      "v_mfma_f32_16x16x32_bf16 v[224:227], a[108:111], a[224:227], v[224:227]\n"
      "ds_read_b64_tr_b16 a[88:89], v236 offset:12288\n"
      "ds_read_b64_tr_b16 a[66:67], v238 offset:512\n"
      "ds_read_b64_tr_b16 a[90:91], v236 offset:12800\n"
      "v_mfma_f32_16x16x32_bf16 v[180:183], a[12:15], a[228:231], v[180:183]\n"
      "ds_read_b64_tr_b16 a[16:17], v236\n"
      "ds_read_b64_tr_b16 a[18:19], v236 offset:512\n"
      "v_mfma_f32_16x16x32_bf16 v[232:235], a[60:63], a[224:227], v[232:235]\n"
      "buffer_load_dwordx4 v96, s[20:23], s78 offen offset:896  lds\n"
      "v_mfma_f32_16x16x32_bf16 v[100:103], a[12:15], a[232:235], v[100:103]\n"
      "ds_read_b64_tr_b16 a[138:139], v238 offset:12800\n"
      "ds_read_b64_tr_b16 a[42:43], v237 offset:512\n"
      "ds_read_b64_tr_b16 a[64:65], v238\n"
      "v_mfma_f32_16x16x32_bf16 v[138:141], a[132:135], a[232:235], v[138:141]\n"
      "ds_read_b64_tr_b16 a[112:113], v237 offset:12288\n"
      "ds_read_b64_tr_b16 a[114:115], v237 offset:12800\n"
      "v_mfma_f32_16x16x32_bf16 v[130:133], a[36:39], a[232:235], v[130:133]\n"
      "ds_read_b64_tr_b16 a[40:41], v237\n"
      "s_add_u32 m0, s75, 56320\n"
      "v_mfma_f32_16x16x32_bf16 v[200:203], a[84:87], a[232:235], v[200:203]\n"
      "buffer_load_dwordx4 v96, s[20:23], s78 offen offset:1024  lds\n"
      "v_mfma_f32_16x16x32_bf16 v[216:219], a[84:87], a[224:227], v[216:219]\n"
      "ds_read_b32 v67, v67\n"
      "s_waitcnt lgkmcnt(7)\n"
      "v_mfma_f32_16x16x32_bf16 v[216:219], a[88:91], a[240:243], v[216:219]\n"
      "ds_read_b32 v47, v47\n"
      "v_readlane_b32 s84, v37, s43\n"
      "v_mfma_f32_16x16x32_bf16 v[220:223], a[60:63], a[228:231], v[220:223]\n"
      "s_mov_b32 s78, 36864\n"
      "v_lshl_add_u32 v68, v68, 2, s76\n"
      "s_add_u32 m0, s75, 60672\n"
      "v_mfma_f32_16x16x32_bf16 v[100:103], a[16:19], a[248:251], v[100:103]\n"
      "buffer_load_dwordx4 v96, s[20:23], s78 offen offset:768  lds\n"
      "s_waitcnt lgkmcnt(2)\n"
      "v_mfma_f32_16x16x32_bf16 v[252:255], a[112:115], a[252:255], v[252:255]\n"
      "s_add_u32 m0, s75, 64640\n"
      "ds_read_b128 a[232:235], v116 offset:2048\n"
      "s_mov_b32 s79, 73728\n"
      "v_mfma_f32_16x16x32_bf16 v[212:215], a[136:139], a[244:247], v[212:215]\n"
      "ds_read_b32 v46, v46\n"
      "ds_read_b32 v54, v54\n"
      "v_mfma_f32_16x16x32_bf16 v[232:235], a[64:67], a[240:243], v[232:235]\n"
      "buffer_load_dwordx4 v96, s[20:23], s78 offen offset:896  lds\n"
      "v_mfma_f32_16x16x32_bf16 v[208:211], a[88:91], a[244:247], v[208:211]\n"
      "s_add_u32 m0, s75, 68608\n"
      "ds_read_b32 v68, v68\n"
      "v_mfma_f32_16x16x32_bf16 v[190:193], a[108:111], a[228:231], v[190:193]\n"
      "ds_read_b32 v58, v58\n"
      "ds_read_b32 v41, v41\n"
      "ds_read_b128 a[236:239], v143 offset:2048\n"
      "v_mfma_f32_16x16x32_bf16 v[124:127], a[136:139], a[252:255], v[124:127]\n"
      "buffer_load_dwordx4 v96, s[20:23], s78 offen offset:1024  lds\n"
      "v_mfma_f32_16x16x32_bf16 v[244:247], a[16:19], a[240:243], v[244:247]\n"
      "ds_read_b32 v49, v49\n"
      "v_lshl_add_u32 v62, v62, 2, s76\n"
      "v_mfma_f32_16x16x32_bf16 v[248:251], a[136:139], a[240:243], v[248:251]\n"
      "ds_read_b32 v57, v57\n"
      "v_cndmask_b32 v28, v105, v157, s[82:83]\n"
      "v_mfma_f32_16x16x32_bf16 v[180:183], a[16:19], a[244:247], v[180:183]\n"
      "ds_read_b32 v52, v52\n"
      "s_add_u32 m0, s75, 72960\n"
      "v_mfma_f32_16x16x32_bf16 v[120:123], a[88:91], a[252:255], v[120:123]\n"
      "buffer_load_dwordx4 v96, s[20:23], s79 offen offset:768  lds\n"
      "s_waitcnt lgkmcnt(4)\n"
      "v_mfma_f32_16x16x32_bf16 v[154:157], a[72:75], a[232:235], 0\n"
      "ds_read_b32 v55, v55\n"
      "v_readlane_b32 s85, v37, s42\n"
      "v_mfma_f32_16x16x32_bf16 v[220:223], a[64:67], a[244:247], v[220:223]\n"
      "ds_read_b32 v42, v42\n"
      "ds_read_b32 v74, v74\n"
      "v_mfma_f32_16x16x32_bf16 v[194:197], a[40:43], a[244:247], v[194:197]\n"
      "s_add_u32 m0, s75, 76928\n"
      "ds_read_b32 v56, v56\n"
      "v_mfma_f32_16x16x32_bf16 v[130:133], a[40:43], a[248:251], v[130:133]\n"
      "buffer_load_dwordx4 v96, s[20:23], s79 offen offset:896  lds\n"
      "v_mfma_f32_16x16x32_bf16 v[158:161], a[64:67], a[252:255], v[158:161]\n"
      "ds_read_b128 a[224:227], v1 offset:14336\n"
      "s_add_u32 m0, s75, 80896\n"
      "ds_read_b128 a[228:231], v1 offset:16384\n"
      "v_mfma_f32_16x16x32_bf16 v[134:137], a[40:43], a[252:255], v[134:137]\n"
      "ds_read_b32 v66, v66\n"
      "ds_read_b128 a[220:223], v1 offset:12288\n"
      "v_mfma_f32_16x16x32_bf16 v[162:165], a[16:19], a[252:255], v[162:165]\n"
      "buffer_load_dwordx4 v96, s[20:23], s79 offen offset:1024  lds\n"
      "v_mfma_f32_16x16x32_bf16 v[200:203], a[88:91], a[248:251], v[200:203]\n"
      "s_waitcnt vmcnt(9)\n"
      "s_barrier\n"
      "ds_read_b64_tr_b16 a[94:95], v236 offset:37376\n"
      "v_mfma_f32_16x16x32_bf16 v[190:193], a[112:115], a[244:247], v[190:193]\n"
      "ds_read_b64_tr_b16 a[20:21], v236 offset:24576\n"
      "ds_read_b64_tr_b16 a[92:93], v236 offset:36864\n"
      "v_mfma_f32_16x16x32_bf16 v[174:177], a[0:3], a[232:235], 0\n"
      "ds_read_b64_tr_b16 a[22:23], v236 offset:25088\n"
      "s_mov_b32 s79, 110592\n"
      "v_mfma_f32_16x16x32_bf16 v[224:227], a[112:115], a[240:243], v[224:227]\n"
      "ds_read_b64_tr_b16 a[116:117], v237 offset:36864\n"
      "ds_read_b64_tr_b16 a[68:69], v238 offset:24576\n"
      "v_mfma_f32_16x16x32_bf16 v[170:173], a[64:67], a[248:251], v[170:173]\n"
      "s_add_u32 m0, s75, 85248\n"
      "ds_read_b64_tr_b16 a[44:45], v237 offset:24576\n"
      "v_mfma_f32_16x16x32_bf16 v[166:169], a[40:43], a[240:243], v[166:169]\n"
      "ds_read_b64_tr_b16 a[70:71], v238 offset:25088\n"
      "ds_read_b64_tr_b16 a[140:141], v238 offset:36864\n"
      "v_mfma_f32_16x16x32_bf16 v[112:115], a[48:51], a[232:235], 0\n"
      "buffer_load_dwordx4 v96, s[20:23], s79 offen offset:768  lds\n"
      "v_mfma_f32_16x16x32_bf16 v[138:141], a[136:139], a[248:251], v[138:141]\n"
      "ds_read_b64_tr_b16 a[46:47], v237 offset:25088\n"
      "s_waitcnt lgkmcnt(6)\n"
      "v_mfma_f32_16x16x32_bf16 v[180:183], a[20:23], a[220:223], v[180:183]\n"
      "s_add_u32 m0, s75, 89216\n"
      "ds_read_b64_tr_b16 a[118:119], v237 offset:37376\n"
      "v_mfma_f32_16x16x32_bf16 v[244:247], a[20:23], a[216:219], v[244:247]\n"
      "ds_read_b64_tr_b16 a[142:143], v238 offset:37376\n"
      "v_lshl_add_u32 v59, v59, 2, s76\n"
      "v_mfma_f32_16x16x32_bf16 v[216:219], a[92:95], a[216:219], v[216:219]\n"
      "buffer_load_dwordx4 v96, s[20:23], s79 offen offset:896  lds\n"
      "v_mfma_f32_16x16x32_bf16 v[120:123], a[92:95], a[228:231], v[120:123]\n"
      "s_add_u32 m0, s75, 93184\n"
      "ds_read_b128 a[240:243], v116 offset:6144\n"
      "v_mfma_f32_16x16x32_bf16 v[200:203], a[92:95], a[224:227], v[200:203]\n"
      "ds_read_b128 a[244:247], v143 offset:6144\n"
      "v_mul_f32_e64 v107, v245, v245\n"
      "v_mfma_f32_16x16x32_bf16 v[154:157], a[76:79], a[236:239], v[154:157]\n"
      "s_mov_b32 s78, 147456\n"
      "v_mul_f32_e64 v185, v217, v217\n"
      "v_fma_f32 v106, v244, v244, s51\n"
      "v_mfma_f32_16x16x32_bf16 v[208:211], a[92:95], a[220:223], v[208:211]\n"
      "buffer_load_dwordx4 v96, s[20:23], s79 offen offset:1024  lds\n"
      "v_mfma_f32_16x16x32_bf16 v[162:165], a[20:23], a[228:231], v[162:165]\n"
      "s_waitcnt lgkmcnt(2)\n"
      "v_fma_f32 v184, v216, v216, s51\n"
      "s_add_u32 m0, s75, -768\n"
      "v_mfma_f32_16x16x32_bf16 v[166:169], a[44:47], a[216:219], v[166:169]\n"
      "ds_read_b32 v51, v51\n"
      "v_cndmask_b32 v26, v105, v150, s[82:83]\n"
      "v_mfma_f32_16x16x32_bf16 v[138:141], a[140:143], a[224:227], v[138:141]\n"
      "s_waitcnt lgkmcnt(2)\n"
      "v_cndmask_b32 v24, v105, v104, s[82:83]\n"
      "v_mul_f32_e64 v105, v201, v201\n"
      "v_mfma_f32_16x16x32_bf16 v[100:103], a[20:23], a[224:227], v[100:103]\n"
      "buffer_load_dwordx4 v96, s[20:23], s78 offen offset:768  lds\n"
      "v_mfma_f32_16x16x32_bf16 v[174:177], a[4:7], a[236:239], v[174:177]\n"
      "ds_read_b32 v59, v59\n"
      "v_fma_f32 v104, v200, v200, s51\n"
      "v_mfma_f32_16x16x32_bf16 v[174:177], a[8:11], a[240:243], v[174:177]\n"
      "s_lshl_b32 s92, s40, 6\n"
      "v_fma_f32 v150, v208, v208, s51\n"
      "v_mul_f32 v153, v181, v181\n"
      "v_mfma_f32_16x16x32_bf16 v[130:133], a[44:47], a[224:227], v[130:133]\n"
      "s_add_u32 m0, s75, 3200\n"
      "v_mul_f32 v145, v163, v163\n"
      "v_mul_f32 v151, v209, v209\n"
      "v_mfma_f32_16x16x32_bf16 v[190:193], a[116:119], a[220:223], v[190:193]\n"
      "buffer_load_dwordx4 v96, s[20:23], s78 offen offset:896  lds\n"
      "v_fma_f32 v230, v120, v120, s51\n"
      "v_mul_f32_e64 v231, v121, v121\n"
      "v_pk_fma_f32 v[104:105], v[202:203], v[202:203], v[104:105]\n"
      "v_pk_fma_f32 v[184:185], v[218:219], v[218:219], v[184:185]\n"
      "v_pk_fma_f32 v[106:107], v[246:247], v[246:247], v[106:107]\n"
      "v_pk_fma_f32 v[150:151], v[210:211], v[210:211], v[150:151]\n"
      "v_pk_fma_f32 v[106:107], v[166:167], v[166:167], v[106:107]\n"
      "v_mfma_f32_16x16x32_bf16 v[232:235], a[68:71], a[216:219], v[232:235]\n"
      "s_add_u32 m0, s75, 7168\n"
      "ds_read_b128 a[252:255], v143 offset:10240\n"
      "v_mfma_f32_16x16x32_bf16 v[224:227], a[116:119], a[216:219], v[224:227]\n"
      "buffer_load_dwordx4 v96, s[20:23], s78 offen offset:1024  lds\n"
      "v_mfma_f32_16x16x32_bf16 v[194:197], a[44:47], a[220:223], v[194:197]\n"
      "s_add_u32 s43, s43, 1\n"
      "v_fma_f32 v152, v180, v180, s51\n"
      "s_mov_b32 s78, 184320\n"
      "v_mfma_f32_16x16x32_bf16 v[240:243], a[112:115], a[248:251], v[240:243]\n"
      "s_add_u32 m0, s75, 11520\n"
      "v_fma_f32 v144, v162, v162, s51\n"
      "v_mul_f32 v207, v101, v101\n"
      "v_fma_f32 v206, v100, v100, s51\n"
      "v_mfma_f32_16x16x32_bf16 v[240:243], a[116:119], a[224:227], v[240:243]\n"
      "ds_read_b128 a[248:251], v116 offset:10240\n"
      "buffer_load_dwordx4 v96, s[20:23], s78 offen offset:768  lds\n"
      "v_pk_fma_f32 v[106:107], v[168:169], v[168:169], v[106:107]\n"
      "v_pk_fma_f32 v[184:185], v[224:225], v[224:225], v[184:185]\n"
      "s_add_u32 m0, s75, 15488\n"
      "v_pk_fma_f32 v[150:151], v[190:191], v[190:191], v[150:151]\n"
      "v_pk_fma_f32 v[184:185], v[226:227], v[226:227], v[184:185]\n"
      "v_pk_fma_f32 v[106:107], v[232:233], v[232:233], v[106:107]\n"
      "v_pk_fma_f32 v[144:145], v[164:165], v[164:165], v[144:145]\n"
      "v_pk_fma_f32 v[230:231], v[122:123], v[122:123], v[230:231]\n"
      "v_pk_fma_f32 v[152:153], v[182:183], v[182:183], v[152:153]\n"
      "v_pk_fma_f32 v[106:107], v[234:235], v[234:235], v[106:107]\n"
      "v_pk_fma_f32 v[206:207], v[102:103], v[102:103], v[206:207]\n"
      "v_pk_fma_f32 v[152:153], v[194:195], v[194:195], v[152:153]\n"
      "v_pk_fma_f32 v[104:105], v[240:241], v[240:241], v[104:105]\n"
      "v_mfma_f32_16x16x32_bf16 v[248:251], a[140:143], a[216:219], v[248:251]\n"
      "buffer_load_dwordx4 v96, s[20:23], s78 offen offset:896  lds\n"
      "v_mfma_f32_16x16x32_bf16 v[158:161], a[68:71], a[228:231], v[158:161]\n"
      "s_add_u32 m0, s75, 19456\n"
      "v_add_f32 v106, v106, v107\n"
      "s_waitcnt vmcnt(11)\n"
      "s_barrier\n"
      "v_mfma_f32_16x16x32_bf16 v[124:127], a[140:143], a[228:231], v[124:127]\n"
      "s_mov_b32 s79, 0\n"
      "ds_read_b64_tr_b16 a[146:147], v76 offset:512\n"
      "v_mfma_f32_16x16x32_bf16 v[112:115], a[52:55], a[236:239], v[112:115]\n"
      "ds_read_b64_tr_b16 a[196:197], v108 offset:12288\n"
      "ds_read_b64_tr_b16 a[172:173], v77 offset:12288\n"
      "v_mfma_f32_16x16x32_bf16 v[212:215], a[140:143], a[220:223], v[212:215]\n"
      "buffer_load_dwordx4 v96, s[20:23], s78 offen offset:1024  lds\n"
      "v_mfma_f32_16x16x32_bf16 v[252:255], a[116:119], a[228:231], v[252:255]\n"
      "ds_read_b64_tr_b16 a[144:145], v76\n"
      "ds_read_b64_tr_b16 a[168:169], v77\n"
      "v_mfma_f32_16x16x32_bf16 v[112:115], a[56:59], a[240:243], v[112:115]\n"
      "ds_read_b64_tr_b16 a[174:175], v77 offset:12800\n"
      "s_add_u32 m0, s75, 24576\n"
      "v_mfma_f32_16x16x32_bf16 v[220:223], a[68:71], a[220:223], v[220:223]\n"
      "ds_read_b64_tr_b16 a[170:171], v77 offset:512\n"
      "ds_read_b64_tr_b16 a[198:199], v108 offset:12800\n"
      "v_mfma_f32_16x16x32_bf16 v[134:137], a[44:47], a[228:231], v[134:137]\n"
      "ds_read_b64_tr_b16 a[150:151], v76 offset:12800\n"
      "ds_read_b64_tr_b16 a[194:195], v108 offset:512\n"
      "ds_read_b64_tr_b16 a[148:149], v76 offset:12288\n"
      "v_pk_fma_f32 v[104:105], v[242:243], v[242:243], v[104:105]\n"
      "v_pk_fma_f32 v[152:153], v[196:197], v[196:197], v[152:153]\n"
      "v_pk_fma_f32 v[104:105], v[138:139], v[138:139], v[104:105]\n"
      "v_pk_fma_f32 v[184:185], v[248:249], v[248:249], v[184:185]\n"
      "v_pk_fma_f32 v[206:207], v[130:131], v[130:131], v[206:207]\n"
      "v_mfma_f32_16x16x32_bf16 v[170:173], a[68:71], a[224:227], v[170:173]\n"
      "ds_read_b64_tr_b16 a[192:193], v108\n"
      "buffer_load_dwordx4 v149, s[36:39], s79 offen  lds\n"
      "v_pk_fma_f32 v[230:231], v[252:253], v[252:253], v[230:231]\n"
      "v_pk_fma_f32 v[206:207], v[132:133], v[132:133], v[206:207]\n"
      "v_pk_fma_f32 v[184:185], v[250:251], v[250:251], v[184:185]\n"
      "v_pk_fma_f32 v[104:105], v[140:141], v[140:141], v[104:105]\n"
      "v_add_f32 v184, v184, v185\n"
      "v_add_f32 v104, v104, v105\n"
      "v_pk_fma_f32 v[152:153], v[220:221], v[220:221], v[152:153]\n"
      "v_pk_fma_f32 v[150:151], v[192:193], v[192:193], v[150:151]\n"
      "v_pk_fma_f32 v[230:231], v[254:255], v[254:255], v[230:231]\n"
      "v_permlane16_swap_b32 v106, v184\n"
      "v_pk_fma_f32 v[206:207], v[170:171], v[170:171], v[206:207]\n"
      "v_add_f32 v106, v106, v184\n"
      "v_pk_fma_f32 v[230:231], v[124:125], v[124:125], v[230:231]\n"
      "v_mov_b32 v184, v106\n"
      "v_pk_fma_f32 v[206:207], v[172:173], v[172:173], v[206:207]\n"
      "v_pk_fma_f32 v[150:151], v[212:213], v[212:213], v[150:151]\n"
      "v_pk_fma_f32 v[152:153], v[222:223], v[222:223], v[152:153]\n"
      "v_add_f32 v206, v206, v207\n"
      "v_pk_fma_f32 v[144:145], v[134:135], v[134:135], v[144:145]\n"
      "v_pk_fma_f32 v[150:151], v[214:215], v[214:215], v[150:151]\n"
      "v_permlane16_swap_b32 v206, v104\n"
      "v_add_f32 v206, v206, v104\n"
      "v_mov_b32 v104, v206\n"
      "v_add_f32 v152, v152, v153\n"
      "v_mfma_f32_16x16x32_bf16 v[96:99], a[24:27], a[232:235], 0\n"
      "s_waitcnt lgkmcnt(0)\n"
      "v_add_f32 v150, v150, v151\n"
      "v_permlane32_swap_b32_e64 v106, v184\n"
      "v_mfma_f32_16x16x32_bf16 v[174:177], a[12:15], a[244:247], v[174:177]\n"
      "s_add_u32 m0, s75, 28544\n"
      "v_permlane16_swap_b32 v152, v150\n"
      "v_permlane32_swap_b32 v206, v104\n"
      "v_add_f32 v106, v106, v184\n"
      "v_pk_fma_f32 v[230:231], v[126:127], v[126:127], v[230:231]\n"
      "v_add_f32 v206, v206, v104\n"
      "v_add_f32 v230, v230, v231\n"
      "v_add_f32 v152, v152, v150\n"
      "v_mfma_f32_16x16x32_bf16 v[174:177], a[16:19], a[248:251], v[174:177]\n"
      "buffer_load_dwordx4 v149, s[36:39], s79 offen offset:128  lds\n"
      "v_rsq_f32_e64 v206, v206\n"
      "v_pk_fma_f32 v[144:145], v[136:137], v[136:137], v[144:145]\n"
      "v_mfma_f32_16x16x32_bf16 v[174:177], a[20:23], a[252:255], v[174:177]\n"
      "ds_read_b32 v62, v62\n"
      "v_mov_b32 v150, v152\n"
      "v_rsq_f32 v106, v106\n"
      "v_mov_b32 v104, v206\n"
      "v_mfma_f32_16x16x32_bf16 v[154:157], a[80:83], a[240:243], v[154:157]\n"
      "s_add_u32 m0, s75, 32512\n"
      "v_permlane32_swap_b32 v152, v150\n"
      "v_mfma_f32_16x16x32_bf16 v[154:157], a[84:87], a[244:247], v[154:157]\n"
      "buffer_load_dwordx4 v149, s[36:39], s79 offen offset:256  lds\n"
      "v_add_f32 v152, v152, v150\n"
      "v_permlane16_swap_b32 v206, v104\n"
      "v_mfma_f32_16x16x32_bf16 v[154:157], a[88:91], a[248:251], v[154:157]\n"
      "s_add_u32 m0, s75, 36864\n"
      "v_mov_b32 v184, v106\n"
      "v_mul_f32 v206, s50, v206\n"
      "v_mfma_f32_16x16x32_bf16 v[154:157], a[92:95], a[252:255], v[154:157]\n"
      "s_mov_b32 s79, 12288\n"
      "v_rsq_f32 v152, v152\n"
      "v_permlane16_swap_b32 v106, v184\n"
      "v_mov_b32_e64 v150, v152\n"
      "v_pk_mul_f32 v[100:101], v[100:101], v[206:207] op_sel_hi:[1,0]\n"
      "v_pk_fma_f32 v[144:145], v[158:159], v[158:159], v[144:145]\n"
      "v_pk_mul_f32 v[138:139], v[138:139], v[104:105] op_sel_hi:[1,0]\n"
      "v_pk_mul_f32 v[132:133], v[132:133], v[206:207] op_sel_hi:[1,0]\n"
      "v_pk_mul_f32 v[218:219], v[218:219], v[184:185] op_sel_hi:[1,0]\n"
      "v_mul_f32_e64 v106, s50, v106\n"
      "v_pk_mul_f32 v[216:217], v[216:217], v[184:185] op_sel_hi:[1,0]\n"
      "v_pk_fma_f32 v[144:145], v[160:161], v[160:161], v[144:145]\n"
      "v_pk_mul_f32 v[172:173], v[172:173], v[206:207] op_sel_hi:[1,0]\n"
      "v_mfma_f32_16x16x32_bf16 v[112:115], a[60:63], a[244:247], v[112:115]\n"
      "buffer_load_dwordx4 v149, s[36:39], s79 offen  lds\n"
      "v_add_f32 v144, v144, v145\n"
      "v_permlane16_swap_b32 v152, v150\n"
      "v_pk_mul_f32 v[226:227], v[226:227], v[184:185] op_sel_hi:[1,0]\n"
      "v_pk_mul_f32 v[232:233], v[232:233], v[106:107] op_sel_hi:[1,0]\n"
      "v_pk_mul_f32 v[248:249], v[248:249], v[184:185] op_sel_hi:[1,0]\n"
      "v_pk_mul_f32 v[246:247], v[246:247], v[106:107] op_sel_hi:[1,0]\n"
      "v_pk_mul_f32 v[190:191], v[190:191], v[150:151] op_sel_hi:[1,0]\n"
      "v_mfma_f32_16x16x32_bf16 v[96:99], a[28:31], a[236:239], v[96:99]\n"
      "s_add_u32 m0, s75, 40832\n"
      "v_permlane16_swap_b32 v144, v230\n"
      "v_add_f32 v144, v144, v230\n"
      "v_cvt_pk_bf16_f32 v216, v216, v217\n"
      "v_mov_b32 v230, v144\n"
      "v_mfma_f32_16x16x32_bf16 v[96:99], a[32:35], a[240:243], v[96:99]\n"
      "s_waitcnt vmcnt(10)\n"
      "s_barrier\n"
      "ds_read_b64_tr_b16 a[204:205], v108 offset:36864\n"
      "v_mfma_f32_16x16x32_bf16 v[96:99], a[36:39], a[244:247], v[96:99]\n"
      "buffer_load_dwordx4 v149, s[36:39], s79 offen offset:128  lds\n"
      "v_permlane32_swap_b32 v144, v230\n"
      "v_cvt_pk_bf16_f32 v217, v218, v219\n"
      "v_pk_mul_f32 v[250:251], v[250:251], v[184:185] op_sel_hi:[1,0]\n"
      "v_pk_mul_f32 v[200:201], v[200:201], v[104:105] op_sel_hi:[1,0]\n"
      "v_pk_mul_f32 v[168:169], v[168:169], v[106:107] op_sel_hi:[1,0]\n"
      "v_pk_mul_f32 v[224:225], v[224:225], v[184:185] op_sel_hi:[1,0]\n"
      "v_pk_mul_f32 v[140:141], v[140:141], v[104:105] op_sel_hi:[1,0]\n"
      "v_add_f32_e64 v144, v144, v230\n"
      "v_cvt_pk_bf16_f32 v219, v226, v227\n"
      "v_cvt_pk_bf16_f32 v218, v224, v225\n"
      "v_pk_mul_f32 v[244:245], v[244:245], v[106:107] op_sel_hi:[1,0]\n"
      "v_pk_mul_f32 v[242:243], v[242:243], v[104:105] op_sel_hi:[1,0]\n"
      "v_mfma_f32_16x16x32_bf16 v[226:229], a[120:123], a[232:235], 0\n"
      "ds_read_b64_tr_b16 a[176:177], v77 offset:24576\n"
      "ds_read_b64_tr_b16 a[182:183], v77 offset:37376\n"
      "ds_read_b64_tr_b16 a[154:155], v76 offset:25088\n"
      "v_pk_mul_f32 v[192:193], v[192:193], v[150:151] op_sel_hi:[1,0]\n"
      "v_mul_f32_e64 v109, v155, v155\n"
      "v_cvt_pk_bf16_f32 v248, v248, v249\n"
      "v_pk_mul_f32 v[130:131], v[130:131], v[206:207] op_sel_hi:[1,0]\n"
      "v_mfma_f32_16x16x32_bf16 v[96:99], a[40:43], a[248:251], v[96:99]\n"
      "ds_read_b64_tr_b16 a[152:153], v76 offset:24576\n"
      "ds_read_b64_tr_b16 a[202:203], v108 offset:25088\n"
      "v_cvt_pk_bf16_f32 v249, v250, v251\n"
      "v_mfma_f32_16x16x32_bf16 v[184:187], a[96:99], a[232:235], 0\n"
      "ds_read_b64_tr_b16 a[200:201], v108 offset:24576\n"
      "v_rsq_f32_e64 v144, v144\n"
      "v_mfma_f32_16x16x32_bf16 v[184:187], a[100:103], a[236:239], v[184:187]\n"
      "ds_read_b64_tr_b16 a[206:207], v108 offset:37376\n"
      "ds_read_b64_tr_b16 a[178:179], v77 offset:25088\n"
      "v_mfma_f32_16x16x32_bf16 v[184:187], a[104:107], a[240:243], v[184:187]\n"
      "ds_read_b64_tr_b16 a[158:159], v76 offset:37376\n"
      "v_mul_f32_e64 v152, s50, v152\n"
      "v_mfma_f32_16x16x32_bf16 v[226:229], a[124:127], a[236:239], v[226:229]\n"
      "ds_read_b64_tr_b16 a[156:157], v76 offset:36864\n"
      "s_add_u32 m0, s75, 44800\n"
      "v_mfma_f32_16x16x32_bf16 v[226:229], a[128:131], a[240:243], v[226:229]\n"
      "ds_read_b64_tr_b16 a[180:181], v77 offset:36864\n"
      "v_mul_f32 v77, v175, v175\n"
      "v_mov_b32 v230, v144\n"
      "v_cvt_pk_bf16_f32 v244, v244, v245\n"
      "v_mfma_f32_16x16x32_bf16 v[184:187], a[108:111], a[244:247], v[184:187]\n"
      "s_mov_b32 s78, 24576\n"
      "ds_write_b128 v147, v[216:219] offset:32\n"
      "v_mfma_f32_16x16x32_bf16 v[184:187], a[112:115], a[248:251], v[184:187]\n"
      "ds_read_b128 v[216:219], v143\n"
      "v_cvt_pk_bf16_f32 v245, v246, v247\n"
      "v_mfma_f32_16x16x32_bf16 v[184:187], a[116:119], a[252:255], v[184:187]\n"
      "buffer_load_dwordx4 v149, s[36:39], s79 offen offset:256  lds\n"
      "v_fma_f32 v76, v174, v174, s51\n"
      "v_mfma_f32_16x16x32_bf16 v[96:99], a[44:47], a[252:255], v[96:99]\n"
      "ds_write_b64 v147, v[248:249] offset:24\n"
      "v_permlane16_swap_b32_e64 v144, v230\n"
      "v_cvt_pk_bf16_f32 v232, v232, v233\n"
      "v_pk_mul_f32 v[234:235], v[234:235], v[106:107] op_sel_hi:[1,0]\n"
      "v_pk_mul_f32 v[212:213], v[212:213], v[150:151] op_sel_hi:[1,0]\n"
      "v_mul_f32_e64 v144, s50, v144\n"
      "v_pk_fma_f32 v[76:77], v[176:177], v[176:177], v[76:77]\n"
      "v_pk_mul_f32 v[214:215], v[214:215], v[150:151] op_sel_hi:[1,0]\n"
      "v_pk_mul_f32 v[180:181], v[180:181], v[152:153] op_sel_hi:[1,0]\n"
      "v_pk_mul_f32 v[166:167], v[166:167], v[106:107] op_sel_hi:[1,0]\n"
      "v_cvt_pk_bf16_f32 v247, v168, v169\n"
      "v_pk_mul_f32 v[120:121], v[120:121], v[230:231] op_sel_hi:[1,0]\n"
      "v_mfma_f32_16x16x32_bf16 v[226:229], a[132:135], a[244:247], v[226:229]\n"
      "s_add_u32 m0, s75, 49152\n"
      "ds_read_b128 v[248:251], v143 offset:4096\n"
      "buffer_load_dwordx4 v149, s[36:39], s78 offen  lds\n"
      "v_pk_mul_f32 v[196:197], v[196:197], v[152:153] op_sel_hi:[1,0]\n"
      "v_fma_f32 v108, v154, v154, s51\n"
      "v_pk_mul_f32 v[194:195], v[194:195], v[152:153] op_sel_hi:[1,0]\n"
      "v_pk_fma_f32 v[108:109], v[156:157], v[156:157], v[108:109]\n"
      "v_pk_fma_f32 v[76:77], v[96:97], v[96:97], v[76:77]\n"
      "v_pk_mul_f32 v[208:209], v[208:209], v[150:151] op_sel_hi:[1,0]\n"
      "v_pk_mul_f32 v[254:255], v[254:255], v[230:231] op_sel_hi:[1,0]\n"
      "v_cvt_pk_bf16_f32 v233, v234, v235\n"
      "v_pk_mul_f32 v[170:171], v[170:171], v[206:207] op_sel_hi:[1,0]\n"
      "v_pk_mul_f32 v[240:241], v[240:241], v[104:105] op_sel_hi:[1,0]\n"
      "v_pk_mul_f32 v[202:203], v[202:203], v[104:105] op_sel_hi:[1,0]\n"
      "v_cvt_pk_bf16_f32 v246, v166, v167\n"
      "v_pk_mul_f32 v[222:223], v[222:223], v[152:153] op_sel_hi:[1,0]\n"
      "v_pk_mul_f32 v[124:125], v[124:125], v[230:231] op_sel_hi:[1,0]\n"
      "v_pk_mul_f32 v[182:183], v[182:183], v[152:153] op_sel_hi:[1,0]\n"
      "s_add_u32 m0, s75, 53120\n"
      "v_mfma_f32_16x16x32_bf16 v[226:229], a[136:139], a[248:251], v[226:229]\n"
      "ds_write_b128 v147, v[244:247]\n"
      "v_pk_fma_f32 v[108:109], v[184:185], v[184:185], v[108:109]\n"
      "v_cvt_pk_bf16_f32 v200, v200, v201\n"
      "v_cvt_pk_bf16_f32 v201, v202, v203\n"
      "v_cvt_pk_bf16_f32 v120, v120, v121\n"
      "v_mfma_f32_16x16x32_bf16 v[226:229], a[140:143], a[252:255], v[226:229]\n"
      "ds_read_b128 v[244:247], v116 offset:4096\n"
      "v_cvt_pk_bf16_f32 v170, v170, v171\n"
      "v_mfma_f32_16x16x32_bf16 v[112:115], a[64:67], a[248:251], v[112:115]\n"
      "ds_read_b128 v[166:169], v116\n"
      "buffer_load_dwordx4 v149, s[36:39], s78 offen offset:128  lds\n"
      "v_pk_mul_f32 v[162:163], v[162:163], v[144:145] op_sel_hi:[1,0]\n"
      "v_pk_mul_f32 v[102:103], v[102:103], v[206:207] op_sel_hi:[1,0]\n"
      "v_cvt_pk_bf16_f32 v100, v100, v101\n"
      "v_cvt_pk_bf16_f32 v203, v242, v243\n"
      "s_add_u32 m0, s75, 57088\n"
      "v_pk_mul_f32 v[122:123], v[122:123], v[230:231] op_sel_hi:[1,0]\n"
      "v_pk_fma_f32 v[76:77], v[98:99], v[98:99], v[76:77]\n"
      "v_pk_mul_f32 v[252:253], v[252:253], v[230:231] op_sel_hi:[1,0]\n"
      "v_pk_mul_f32 v[210:211], v[210:211], v[150:151] op_sel_hi:[1,0]\n"
      "v_pk_mul_f32 v[220:221], v[220:221], v[152:153] op_sel_hi:[1,0]\n"
      "v_pk_mul_f32 v[160:161], v[160:161], v[144:145] op_sel_hi:[1,0]\n"
      "v_cvt_pk_bf16_f32 v101, v102, v103\n"
      "v_cvt_pk_bf16_f32 v180, v180, v181\n"
      "v_cvt_pk_bf16_f32 v181, v182, v183\n"
      "v_mfma_f32_16x16x32_bf16 v[112:115], a[68:71], a[252:255], v[112:115]\n"
      "buffer_load_dwordx4 v149, s[36:39], s78 offen offset:256  lds\n"
      "s_mov_b32 s78, 36864\n"
      "v_cvt_pk_bf16_f32 v182, v194, v195\n"
      "s_waitcnt lgkmcnt(0)\n"
      "s_waitcnt vmcnt(9)\n"
      "s_barrier\n"
      "v_pk_mul_f32 v[158:159], v[158:159], v[144:145] op_sel_hi:[1,0]\n"
      "v_cvt_pk_bf16_f32 v183, v196, v197\n"
      "v_pk_fma_f32 v[108:109], v[186:187], v[186:187], v[108:109]\n"
      "s_add_u32 m0, s75, 61440\n"
      "ds_read_b64_tr_b16 a[188:189], v237 offset:12288\n"
      "v_pk_mul_f32 v[136:137], v[136:137], v[144:145] op_sel_hi:[1,0]\n"
      "v_mfma_f32_16x16x32_bf16 v[194:197], a[168:171], v[166:169], 0\n"
      "ds_read_b64_tr_b16 a[212:213], v238 offset:12288\n"
      "ds_read_b64_tr_b16 a[190:191], v237 offset:12800\n"
      "v_mfma_f32_16x16x32_bf16 v[194:197], a[172:175], v[216:219], v[194:197]\n"
      "ds_read_b64_tr_b16 a[186:187], v237 offset:512\n"
      "buffer_load_dwordx4 v149, s[36:39], s78 offen  lds\n"
      "v_mfma_f32_16x16x32_bf16 v[194:197], a[176:179], v[244:247], v[194:197]\n"
      "ds_read_b128 v[104:107], v116 offset:8192\n"
      "v_cvt_pk_bf16_f32 v103, v132, v133\n"
      "v_mfma_f32_16x16x32_bf16 v[194:197], a[180:183], v[248:251], v[194:197]\n"
      "ds_read_b64_tr_b16 a[160:161], v236\n"
      "ds_read_b64_tr_b16 a[214:215], v238 offset:12800\n"
      "v_mfma_f32_16x16x32_bf16 v[204:207], a[192:195], v[166:169], 0\n"
      "ds_read_b64_tr_b16 a[184:185], v237\n"
      "s_add_u32 m0, s75, 65408\n"
      "v_mfma_f32_16x16x32_bf16 v[204:207], a[196:199], v[216:219], v[204:207]\n"
      "ds_read_b64_tr_b16 a[210:211], v238 offset:512\n"
      "v_cvt_pk_bf16_f32 v102, v130, v131\n"
      "v_mfma_f32_16x16x32_bf16 v[204:207], a[200:203], v[244:247], v[204:207]\n"
      "ds_read_b64_tr_b16 a[164:165], v236 offset:12288\n"
      "buffer_load_dwordx4 v149, s[36:39], s78 offen offset:128  lds\n"
      "v_cvt_pk_bf16_f32 v138, v138, v139\n"
      "s_mov_b32 s79, 49152\n"
      "v_pk_fma_f32 v[76:77], v[112:113], v[112:113], v[76:77]\n"
      "v_pk_mul_f32 v[126:127], v[126:127], v[230:231] op_sel_hi:[1,0]\n"
      "v_mfma_f32_16x16x32_bf16 v[204:207], a[204:207], v[248:251], v[204:207]\n"
      "ds_read_b64_tr_b16 a[166:167], v236 offset:12800\n"
      "ds_read_b64_tr_b16 a[208:209], v238\n"
      "v_mfma_f32_16x16x32_bf16 v[150:153], a[144:147], v[166:169], 0\n"
      "ds_read_b128 v[130:133], v143 offset:8192\n"
      "v_cvt_pk_bf16_f32 v212, v212, v213\n"
      "s_add_u32 m0, s75, 69376\n"
      "ds_read_b64_tr_b16 a[162:163], v236 offset:512\n"
      "v_mfma_f32_16x16x32_bf16 v[150:153], a[148:151], v[216:219], v[150:153]\n"
      "buffer_load_dwordx4 v149, s[36:39], s78 offen offset:256  lds\n"
      "ds_read_b128 v[234:237], v143 offset:2048\n"
      "v_pk_mul_f32 v[164:165], v[164:165], v[144:145] op_sel_hi:[1,0]\n"
      "v_pk_fma_f32 v[76:77], v[114:115], v[114:115], v[76:77]\n"
      "v_pk_fma_f32 v[108:109], v[226:227], v[226:227], v[108:109]\n"
      "v_mfma_f32_16x16x32_bf16 v[150:153], a[152:155], v[244:247], v[150:153]\n"
      "ds_read_b128 v[242:245], v116 offset:2048\n"
      "v_cvt_pk_bf16_f32 v121, v122, v123\n"
      "v_cvt_pk_bf16_f32 v208, v208, v209\n"
      "v_mfma_f32_16x16x32_bf16 v[150:153], a[156:159], v[248:251], v[150:153]\n"
      "s_add_u32 m0, s75, 73728\n"
      "ds_read_b128 v[246:249], v116 offset:10240\n"
      "buffer_load_dwordx4 v149, s[36:39], s79 offen  lds\n"
      "v_pk_fma_f32 v[108:109], v[228:229], v[228:229], v[108:109]\n"
      "s_waitcnt lgkmcnt(3)\n"
      "v_mfma_f32_16x16x32_bf16 v[150:153], a[160:163], v[104:107], v[150:153]\n"
      "ds_write_b64 v147, v[232:233] offset:16\n"
      "v_mfma_f32_16x16x32_bf16 v[194:197], a[184:187], v[104:107], v[194:197]\n"
      "s_add_u32 m0, s75, 77696\n"
      "ds_read_b128 v[230:233], v116 offset:6144\n"
      "v_mfma_f32_16x16x32_bf16 v[194:197], a[188:191], v[130:133], v[194:197]\n"
      "buffer_load_dwordx4 v149, s[36:39], s79 offen offset:128  lds\n"
      "v_cvt_pk_bf16_f32 v124, v124, v125\n"
      "v_cvt_pk_bf16_f32 v123, v254, v255\n"
      "v_add_f32 v76, v76, v77\n"
      "v_cvt_pk_bf16_f32 v162, v162, v163\n"
      "v_cvt_pk_bf16_f32 v202, v240, v241\n"
      "v_add_f32 v108, v108, v109\n"
      "v_mfma_f32_16x16x32_bf16 v[204:207], a[208:211], v[104:107], v[204:207]\n"
      "s_add_u32 m0, s75, 81664\n"
      "ds_read_b128 v[166:169], v143 offset:6144\n"
      "v_mfma_f32_16x16x32_bf16 v[204:207], a[212:215], v[130:133], v[204:207]\n"
      "ds_read_b128 v[104:107], v0 offset:10240\n"
      "v_cvt_pk_bf16_f32 v171, v172, v173\n"
      "v_cvt_pk_bf16_f32 v125, v126, v127\n"
      "v_cvt_pk_bf16_f32 v194, v194, v195\n"
      "v_mfma_f32_16x16x32_bf16 v[150:153], a[164:167], v[130:133], v[150:153]\n"
      "buffer_load_dwordx4 v149, s[36:39], s79 offen offset:256  lds\n"
      "s_waitcnt lgkmcnt(2)\n"
      "v_mfma_f32_16x16x32_bf16 v[126:129], a[144:147], v[242:245], 0\n"
      "ds_read_b128 v[116:119], v0 offset:4096\n"
      "v_cvt_pk_bf16_f32 v213, v214, v215\n"
      "v_cvt_pk_bf16_f32 v195, v196, v197\n"
      "v_mfma_f32_16x16x32_bf16 v[196:199], a[192:195], v[242:245], 0\n"
      "s_mov_b32 s79, 61440\n"
      "v_permlane16_swap_b32 v76, v108\n"
      "v_add_f32_e64 v76, v76, v108\n"
      "s_add_u32 m0, s75, 86016\n"
      "v_mfma_f32_16x16x32_bf16 v[196:199], a[196:199], v[234:237], v[196:199]\n"
      "buffer_load_dwordx4 v149, s[36:39], s79 offen  lds\n"
      "v_mfma_f32_16x16x32_bf16 v[214:217], a[168:171], v[242:245], 0\n"
      "ds_read_b128 v[242:245], v143 offset:10240\n"
      "v_mov_b32_e64 v108, v76\n"
      "v_cvt_pk_bf16_f32 v122, v252, v253\n"
      "v_mfma_f32_16x16x32_bf16 v[126:129], a[148:151], v[234:237], v[126:129]\n"
      "s_add_u32 m0, s75, 89984\n"
      "v_permlane32_swap_b32_e64 v76, v108\n"
      "v_mfma_f32_16x16x32_bf16 v[126:129], a[152:155], v[230:233], v[126:129]\n"
      "ds_write_b64 v148, v[194:195] offset:32\n"
      "v_add_f32 v76, v76, v108\n"
      "v_xor_b32 v241, 64, v8\n"
      "v_cvt_pk_bf16_f32 v209, v210, v211\n"
      "v_mfma_f32_16x16x32_bf16 v[196:199], a[200:203], v[230:233], v[196:199]\n"
      "buffer_load_dwordx4 v149, s[36:39], s79 offen offset:128  lds\n"
      "v_mfma_f32_16x16x32_bf16 v[214:217], a[172:175], v[234:237], v[214:217]\n"
      "ds_read_b128 v[130:133], v1 offset:4096\n"
      "v_rsq_f32_e64 v76, v76\n"
      "v_mfma_f32_16x16x32_bf16 v[214:217], a[176:179], v[230:233], v[214:217]\n"
      "s_waitcnt lgkmcnt(2)\n"
      "v_add_u32 v172, 49152, v241\n"
      "v_mov_b32 v108, v76\n"
      "v_mfma_f32_16x16x32_bf16 v[214:217], a[180:183], v[166:169], v[214:217]\n"
      "s_add_u32 m0, s75, 93952\n"
      "v_permlane16_swap_b32_e64 v76, v108\n"
      "v_mfma_f32_16x16x32_bf16 v[126:129], a[156:159], v[166:169], v[126:129]\n"
      "buffer_load_dwordx4 v149, s[36:39], s79 offen offset:256  lds\n"
      "s_waitcnt vmcnt(12)\n"
      "s_barrier\n"
      "ds_read_b64_tr_b16 a[222:223], v239 offset:38912\n"
      "v_pk_mul_f32 v[134:135], v[134:135], v[144:145] op_sel_hi:[1,0]\n"
      "v_mfma_f32_16x16x32_bf16 v[126:129], a[160:163], v[246:249], v[126:129]\n"
      "ds_read_b64_tr_b16 v[232:233], v146 offset:24576\n"
      "ds_read_b64_tr_b16 a[216:217], v239 offset:24576\n"
      "ds_read_b64_tr_b16 a[242:243], v239 offset:26632\n"
      "v_mfma_f32_16x16x32_bf16 v[126:129], a[164:167], v[242:245], v[126:129]\n"
      "buffer_load_dwordx4 v[142:145], v36, s[28:31], s49 offen\n"
      "v_mfma_f32_16x16x32_bf16 v[196:199], a[204:207], v[166:169], v[196:199]\n"
      "ds_read_b128 v[250:253], v1 offset:10240\n"
      "v_cvt_pk_bf16_f32 v220, v220, v221\n"
      "v_mfma_f32_16x16x32_bf16 v[196:199], a[208:211], v[246:249], v[196:199]\n"
      "ds_read_b64_tr_b16 a[246:247], v239 offset:38920\n"
      "ds_read_b64_tr_b16 a[240:241], v239 offset:24584\n"
      "v_mul_f32 v76, s50, v76\n"
      "v_mfma_f32_16x16x32_bf16 v[214:217], a[184:187], v[246:249], v[214:217]\n"
      "ds_read_b64_tr_b16 v[236:237], v146 offset:36864\n"
      "ds_read_b64_tr_b16 a[244:245], v239 offset:36872\n"
      "v_mfma_f32_16x16x32_bf16 v[214:217], a[188:191], v[242:245], v[214:217]\n"
      "ds_read_b64_tr_b16 a[220:221], v239 offset:36864\n"
      "buffer_load_dword v255, v29, s[28:31], s49 offen\n"
      "v_pk_mul_f32 v[112:113], v[112:113], v[76:77] op_sel_hi:[1,0]\n"
      "v_pk_mul_f32 v[96:97], v[96:97], v[76:77] op_sel_hi:[1,0]\n"
      "v_cvt_pk_bf16_f32 v210, v190, v191\n"
      "v_mfma_f32_16x16x32_bf16 v[196:199], a[212:215], v[242:245], v[196:199]\n"
      "ds_read_b64_tr_b16 a[218:219], v239 offset:26624\n"
      "v_cvt_pk_bf16_f32 v163, v164, v165\n"
      "v_mfma_f32_16x16x32_bf16 v[188:191], v[116:119], a[168:171], 0\n"
      "ds_read_b64_tr_b16 v[238:239], v146 offset:38912\n"
      "s_waitcnt lgkmcnt(5)\n"
      "v_mfma_f32_16x16x32_bf16 v[188:191], v[130:133], a[172:175], v[188:191]\n"
      "buffer_load_dwordx4 v[166:169], v36, s[28:31], s74 offen\n"
      "ds_read_b64_tr_b16 v[234:235], v146 offset:26624\n"
      "v_cvt_pk_bf16_f32 v158, v158, v159\n"
      "v_cvt_pk_bf16_f32 v214, v214, v215\n"
      "v_cvt_pk_bf16_f32 v164, v134, v135\n"
      "v_mov_b32 v109, 0xbc80bc80\n"
      "v_pk_mul_f32 v[156:157], v[156:157], v[108:109] op_sel_hi:[1,0]\n"
      "v_cvt_pk_bf16_f32 v159, v160, v161\n"
      "v_mov_b32 v110, 0xbc80bc80\n"
      "v_pk_mul_f32 v[154:155], v[154:155], v[108:109] op_sel_hi:[1,0]\n"
      "v_mov_b32 v111, 0xbc80bc80\n"
      "v_cvt_pk_bf16_f32 v126, v126, v127\n"
      "v_cvt_pk_bf16_f32 v127, v128, v129\n"
      "v_pk_mul_f32 v[174:175], v[174:175], v[76:77] op_sel_hi:[1,0]\n"
      "v_pk_mul_f32 v[176:177], v[176:177], v[76:77] op_sel_hi:[1,0]\n"
      "v_cvt_pk_bf16_f32 v211, v192, v193\n"
      "v_cvt_pk_bf16_f32 v112, v112, v113\n"
      "v_cvt_pk_bf16_f32 v221, v222, v223\n"
      "v_mfma_f32_16x16x32_bf16 v[222:225], v[116:119], a[144:147], 0\n"
      "buffer_load_dword v254, v29, s[28:31], s74 offen\n"
      "v_cvt_pk_bf16_f32 v215, v216, v217\n"
      "s_add_u32 m0, s64, 18432\n"
      "v_pk_mul_f32 v[186:187], v[186:187], v[108:109] op_sel_hi:[1,0]\n"
      "v_pk_mul_f32 v[228:229], v[228:229], v[108:109] op_sel_hi:[1,0]\n"
      "v_pk_mul_f32 v[226:227], v[226:227], v[108:109] op_sel_hi:[1,0]\n"
      "v_cvt_pk_bf16_f32 v165, v136, v137\n"
      "v_pk_mul_f32 v[184:185], v[184:185], v[108:109] op_sel_hi:[1,0]\n"
      "v_cvt_pk_bf16_f32 v196, v196, v197\n"
      "v_pk_mul_f32 v[114:115], v[114:115], v[76:77] op_sel_hi:[1,0]\n"
      "v_cvt_pk_bf16_f32 v113, v114, v115\n"
      "v_pk_mul_f32 v[98:99], v[98:99], v[76:77] op_sel_hi:[1,0]\n"
      "v_cvt_pk_bf16_f32 v204, v204, v205\n"
      "v_cvt_pk_bf16_f32 v197, v198, v199\n"
      "v_cvt_pk_bf16_f32 v226, v226, v227\n"
      "v_mfma_f32_16x16x32_bf16 v[222:225], v[130:133], a[148:151], v[222:225]\n"
      "buffer_load_dwordx4 v16, s[16:19], s45 offen  lds\n"
      "v_subrev_u32 v39, 115456, v9\n"
      "v_cvt_pk_bf16_f32 v154, v154, v155\n"
      "v_mfma_f32_16x16x32_bf16 v[222:225], v[104:107], a[152:155], v[222:225]\n"
      "s_add_u32 m0, s64, 22528\n"
      "v_cvt_pk_bf16_f32 v227, v228, v229\n"
      "v_cvt_pk_bf16_f32 v174, v174, v175\n"
      "v_mfma_f32_16x16x32_bf16 v[222:225], v[250:253], a[156:159], v[222:225]\n"
      "ds_read_b128 v[242:245], v8 offset:24576\n"
      "ds_read_b128 v[246:249], v241 offset:24576\n"
      "v_mfma_f32_16x16x32_bf16 v[188:191], v[104:107], a[176:179], v[188:191]\n"
      "buffer_load_dwordx4 v17, s[16:19], s45 offen  lds\n"
      "v_cvt_pk_bf16_f32 v139, v140, v141\n"
      "v_mfma_f32_16x16x32_bf16 v[134:137], v[116:119], a[192:195], 0\n"
      "s_add_u32 m0, s64, 26624\n"
      "ds_read_b128 v[192:195], v8 offset:28672\n"
      "v_cvt_pk_bf16_f32 v155, v156, v157\n"
      "v_mfma_f32_16x16x32_bf16 v[134:137], v[130:133], a[196:199], v[134:137]\n"
      "ds_read_b128 v[130:133], v241 offset:28672\n"
      "v_lshrrev_b32_e64 v39, 4, v39\n"
      "v_mfma_f32_16x16x32_bf16 v[134:137], v[104:107], a[200:203], v[134:137]\n"
      "ds_read_b128 v[228:231], v0 offset:2048\n"
      "v_cvt_pk_bf16_f32 v175, v176, v177\n"
      "v_mfma_f32_16x16x32_bf16 v[134:137], v[250:253], a[204:207], v[134:137]\n"
      "ds_read_b128 v[104:107], v1 offset:2048\n"
      "buffer_load_dwordx4 v18, s[16:19], s45 offen  lds\n"
      "v_mfma_f32_16x16x32_bf16 v[188:191], v[250:253], a[180:183], v[188:191]\n"
      "s_mov_b32 exec_lo, 0x1f001f\n"
      "s_mov_b32 exec_hi, 0x1f001f\n"
      "ds_write_b64 v148, v[126:127] offset:18688\n"
      "ds_write_b64 v148, v[214:215] offset:18720\n"
      "ds_write_b64 v148, v[196:197] offset:18752\n"
      "s_mov_b64 exec, -1\n"
      "s_add_u32 m0, s64, 30720\n"
      "s_waitcnt lgkmcnt(3)\n"
      "v_mfma_f32_16x16x32_bf16 v[196:199], v[228:231], a[168:171], 0\n"
      "ds_read_b128 v[126:129], v241 offset:32768\n"
      "v_cvt_pk_bf16_f32 v177, v98, v99\n"
      "v_mov_b32 v108, 0xbc80bc80\n"
      "v_cvt_pk_bf16_f32 v156, v184, v185\n"
      "v_mfma_f32_16x16x32_bf16 v[196:199], v[104:107], a[172:175], v[196:199]\n"
      "ds_read_b128 v[114:117], v0 offset:16384\n"
      "v_cvt_pk_bf16_f32 v157, v186, v187\n"
      "v_mfma_f32_16x16x32_bf16 v[184:187], v[242:245], v[108:111], 0\n"
      "buffer_load_dwordx4 v19, s[16:19], s45 offen  lds\n"
      "v_mfma_f32_16x16x32_bf16 v[184:187], v[246:249], v[108:111], v[184:187]\n"
      "s_waitcnt vmcnt(14)\n"
      "s_barrier\n"
      "s_add_u32 m0, s65, 34816\n"
      "v_cvt_pk_bf16_f32 v176, v96, v97\n"
      "v_mfma_f32_16x16x32_bf16 v[184:187], v[192:195], v[108:111], v[184:187]\n"
      "ds_read_b128 v[96:99], v8 offset:32768\n"
      "ds_read_b128 v[250:253], v0 offset:8192\n"
      "v_mfma_f32_16x16x32_bf16 v[184:187], v[130:133], v[108:111], v[184:187]\n"
      "ds_read_b64_tr_b16 a[228:229], v78 offset:12288\n"
      "v_cvt_pk_bf16_f32 v150, v150, v151\n"
      "v_mfma_f32_16x16x32_bf16 v[214:217], v[228:231], a[192:195], 0\n"
      "buffer_load_dwordx4 v15, s[16:19], s45 offen  lds\n"
      "ds_read_b64_tr_b16 a[230:231], v78 offset:14336\n"
      "ds_read_b64_tr_b16 v[242:243], v82 offset:2048\n"
      "v_mfma_f32_16x16x32_bf16 v[130:133], v[228:231], a[144:147], 0\n"
      "ds_read_b64_tr_b16 a[250:251], v78 offset:2056\n"
      "v_cvt_pk_bf16_f32 v151, v152, v153\n"
      "v_mfma_f32_16x16x32_bf16 v[130:133], v[104:107], a[148:151], v[130:133]\n"
      "ds_read_b64_tr_b16 a[254:255], v78 offset:14344\n"
      "ds_read_b64_tr_b16 a[252:253], v78 offset:12296\n"
      "v_mfma_f32_16x16x32_bf16 v[214:217], v[104:107], a[196:199], v[214:217]\n"
      "ds_read_b64_tr_b16 v[246:247], v82 offset:14336\n"
      "ds_read_b64_tr_b16 a[224:225], v78\n"
      "s_waitcnt lgkmcnt(7)\n"
      "v_mfma_f32_16x16x32_bf16 v[214:217], v[250:253], a[200:203], v[214:217]\n"
      "ds_read_b128 v[228:231], v1 offset:8192\n"
      "ds_read_b64_tr_b16 v[240:241], v82\n"
      "v_mfma_f32_16x16x32_bf16 v[184:187], v[96:99], v[108:111], v[184:187]\n"
      "ds_read_b64_tr_b16 v[244:245], v82 offset:12288\n"
      "ds_read_b64_tr_b16 a[226:227], v78 offset:2048\n"
      "v_mfma_f32_16x16x32_bf16 v[184:187], v[126:129], v[108:111], v[184:187]\n"
      "ds_read_b64_tr_b16 a[248:249], v78 offset:8\n"
      "v_cvt_pk_bf16_f32 v205, v206, v207\n"
      "v_mfma_f32_16x16x32_bf16 v[196:199], v[250:253], a[176:179], v[196:199]\n"
      "ds_read_b128 v[126:129], v1 offset:16384\n"
      "v_and_b32 v39, 48, v39\n"
      "v_mfma_f32_16x16x32_bf16 v[188:191], v[114:117], a[184:187], v[188:191]\n"
      "ds_read_b128 v[96:99], v0 offset:14336\n"
      "ds_read_b128 v[104:107], v1 offset:14336\n"
      "v_mfma_f32_16x16x32_bf16 v[222:225], v[114:117], a[160:163], v[222:225]\n"
      "s_mov_b32 exec_lo, 0x1f001f\n"
      "s_mov_b32 exec_hi, 0x1f001f\n"
      "ds_write_b128 v147, v[174:177] offset:18688\n"
      "ds_write_b64 v147, v[112:113] offset:18704\n"
      "ds_write_b64 v147, v[226:227] offset:18712\n"
      "ds_write_b128 v147, v[154:157] offset:18720\n"
      "s_mov_b64 exec, -1\n"
      "v_mfma_f32_16x16x32_bf16 v[134:137], v[114:117], a[208:211], v[134:137]\n"
      "ds_read_b128 v[174:177], v172\n"
      "v_mul_f32 v187, 0x3eaaaaab, v187\n"
      "v_mfma_f32_16x16x32_bf16 v[130:133], v[250:253], a[152:155], v[130:133]\n"
      "s_waitcnt lgkmcnt(5)\n"
      "v_add_u32 v156, 49152, v8\n"
      "v_mul_f32 v186, 0x3eaaaaab, v186\n"
      "v_mfma_f32_16x16x32_bf16 v[130:133], v[228:231], a[156:159], v[130:133]\n"
      "ds_read_b128 v[250:253], v156 offset:4096\n"
      "v_add_u32 v39, 123136, v39\n"
      "v_mfma_f32_16x16x32_bf16 v[196:199], v[228:231], a[180:183], v[196:199]\n"
      "buffer_load_dwordx4 v[116:119], v36, s[28:31], s47 offen\n"
      "v_mfma_f32_16x16x32_bf16 v[222:225], v[126:129], a[164:167], v[222:225]\n"
      "ds_write_b64 v148, v[150:151]\n"
      "v_mfma_f32_16x16x32_bf16 v[214:217], v[228:231], a[204:207], v[214:217]\n"
      "ds_read_b128 v[228:231], v156\n"
      "v_add_u32_e64 v219, s92, v39\n"
      "v_mfma_f32_16x16x32_bf16 v[214:217], v[96:99], a[208:211], v[214:217]\n"
      "ds_read_b128 v[112:115], v156 offset:8192\n"
      "v_mul_f32 v185, 0x3eaaaaab, v185\n"
      "v_mul_f32 v184, 0x3eaaaaab, v184\n"
      "v_mfma_f32_16x16x32_bf16 v[214:217], v[104:107], a[212:215], v[214:217]\n"
      "ds_read_b128 v[152:155], v172 offset:8192\n"
      "v_cvt_pk_bf16_f32 v76, v222, v223\n"
      "v_mfma_f32_16x16x32_bf16 v[188:191], v[126:129], a[188:191], v[188:191]\n"
      "ds_write_b64 v148, v[204:205] offset:64\n"
      "v_mfma_f32_16x16x32_bf16 v[134:137], v[126:129], a[212:215], v[134:137]\n"
      "ds_read_b128 v[126:129], v172 offset:4096\n"
      "v_cvt_pk_bf16_f32 v77, v224, v225\n"
      "v_mfma_f32_16x16x32_bf16 v[204:207], v[200:203], v[180:183], v[44:47]\n"
      "buffer_load_dword v223, v29, s[28:31], s47 offen\n"
      "v_mfma_f32_16x16x32_bf16 v[196:199], v[96:99], a[184:187], v[196:199]\n"
      "ds_read_b128 v[224:227], v1\n"
      "v_cvt_pk_bf16_f32 v217, v216, v217\n"
      "v_mfma_f32_16x16x32_bf16 v[130:133], v[96:99], a[160:163], v[130:133]\n"
      "ds_read_b128 v[146:149], v0\n"
      "v_cvt_pk_bf16_f32 v81, v190, v191\n"
      "v_mfma_f32_16x16x32_bf16 v[130:133], v[104:107], a[164:167], v[130:133]\n"
      "s_waitcnt lgkmcnt(5)\n"
      "v_cvt_pk_bf16_f32 v216, v214, v215\n"
      "v_subrev_u32 v218, 115456, v9\n"
      "v_mfma_f32_16x16x32_bf16 v[196:199], v[104:107], a[188:191], v[196:199]\n"
      "ds_write_b128 v219, v[184:187]\n"
      "v_mfma_f32_16x16x32_bf16 v[184:187], v[228:231], v[108:111], 0\n"
      "s_waitcnt vmcnt(11)\n"
      "s_barrier\n"
      "v_cvt_pk_bf16_f32 v80, v188, v189\n"
      "v_mfma_f32_16x16x32_bf16 v[184:187], v[174:177], v[108:111], v[184:187]\n"
      "ds_read_b128 v[228:231], v156 offset:28672\n"
      "ds_read_b128 v[174:177], v156 offset:24576\n"
      "v_mfma_f32_16x16x32_bf16 v[184:187], v[250:253], v[108:111], v[184:187]\n"
      "ds_read_b128 v[248:251], v172 offset:24576\n"
      "s_waitcnt lgkmcnt(4)\n"
      "v_mfma_f32_16x16x32_bf16 v[184:187], v[126:129], v[108:111], v[184:187]\n"
      "ds_read_b128 v[188:191], v172 offset:32768\n"
      "v_cvt_pk_bf16_f32 v199, v198, v199\n"
      "v_bfe_u32 v214, v218, 8, 2\n"
      "v_mfma_f32_16x16x32_bf16 v[184:187], v[112:115], v[108:111], v[184:187]\n"
      "ds_read_b128 v[126:129], v172 offset:28672\n"
      "v_cvt_pk_bf16_f32 v84, v134, v135\n"
      "v_mfma_f32_16x16x32_bf16 v[184:187], v[152:155], v[108:111], v[184:187]\n"
      "ds_read_b128 v[192:195], v156 offset:32768\n"
      "v_cvt_pk_bf16_f32 v198, v196, v197\n"
      "v_mfma_f32_16x16x16_bf16 v[204:207], v[138:139], v[220:221], v[204:207]\n"
      "v_cvt_pk_bf16_f32 v85, v136, v137\n"
      "v_lshlrev_b32_e64 v214, 2, v214\n"
      "v_cvt_pk_bf16_f32 v133, v132, v133\n"
      "v_mfma_f32_16x16x32_bf16 v[154:157], v[146:149], a[144:147], 0\n"
      "ds_read_b128 v[134:137], v0 offset:6144\n"
      "v_bfe_u32 v218, v218, 4, 4\n"
      "v_mfma_f32_16x16x32_bf16 v[154:157], v[224:227], a[148:151], v[154:157]\n"
      "ds_read_b64_tr_b16 a[238:239], v78 offset:38912\n"
      "s_waitcnt lgkmcnt(5)\n"
      "v_mfma_f32_16x16x32_bf16 v[96:99], v[174:177], v[108:111], 0\n"
      "ds_read_b64_tr_b16 v[252:253], v82 offset:36864\n"
      "s_waitcnt vmcnt(10)\n"
      "v_or3_b32 v142, v142, v143, v144\n"
      "v_mfma_f32_16x16x32_bf16 v[96:99], v[248:251], v[108:111], v[96:99]\n"
      "ds_read_b128 v[174:177], v10 offset:32\n"
      "v_mul_f32 v187, 0x3eaaaaab, v187\n"
      "v_mfma_f32_16x16x32_bf16 v[96:99], v[228:231], v[108:111], v[96:99]\n"
      "ds_read_b128 v[104:107], v1 offset:6144\n"
      "s_waitcnt lgkmcnt(5)\n"
      "v_mfma_f32_16x16x32_bf16 v[96:99], v[126:129], v[108:111], v[96:99]\n"
      "ds_read_b64_tr_b16 a[236:237], v78 offset:36864\n"
      "v_cvt_pk_bf16_f32 v132, v130, v131\n"
      "v_mfma_f32_16x16x32_bf16 v[96:99], v[192:195], v[108:111], v[96:99]\n"
      "ds_read_b64_tr_b16 v[230:231], v78 offset:38920\n"
      "v_sub_u32 v218, v218, v214\n"
      "v_mfma_f32_16x16x32_bf16 v[96:99], v[188:191], v[108:111], v[96:99]\n"
      "ds_read_b64_tr_b16 v[228:229], v78 offset:36872\n"
      "v_mul_f32 v184, 0x3eaaaaab, v184\n"
      "v_mfma_f32_16x16x32_bf16 v[188:191], v[146:149], a[192:195], 0\n"
      "ds_read_b64 v[172:173], v10 offset:24\n"
      "v_mul_f32 v185, 0x3eaaaaab, v185\n"
      "v_mov_b32 v215, 0\n"
      "v_mfma_f32_16x16x32_bf16 v[188:191], v[224:227], a[196:199], v[188:191]\n"
      "s_waitcnt lgkmcnt(5)\n"
      "s_waitcnt vmcnt(9)\n"
      "v_or3_b32 v142, v142, v145, v255\n"
      "v_mul_f32 v96, 0x3eaaaaab, v96\n"
      "v_mfma_f32_16x16x32_bf16 v[108:111], v[146:149], a[168:171], 0\n"
      "ds_read_b128 v[144:147], v1 offset:12288\n"
      "v_mul_f32 v97, 0x3eaaaaab, v97\n"
      "v_mfma_f32_16x16x32_bf16 v[108:111], v[224:227], a[172:175], v[108:111]\n"
      "ds_read_b64_tr_b16 v[250:251], v82 offset:26624\n"
      "v_mul_f32 v98, 0x3eaaaaab, v98\n"
      "v_mfma_f32_16x16x32_bf16 v[108:111], v[134:137], a[176:179], v[108:111]\n"
      "ds_read_b128 v[126:129], v0 offset:12288\n"
      "s_waitcnt vmcnt(8)\n"
      "v_or3_b32 v166, v166, v167, v168\n"
      "v_mfma_f32_16x16x32_bf16 v[150:153], v[208:211], v[180:183], v[40:43]\n"
      "ds_write_b32 v31, v142 offset:6656\n"
      "v_mfma_f32_16x16x32_bf16 v[140:143], v[174:177], v[180:183], v[24:27]\n"
      "ds_read_b64_tr_b16 a[234:235], v78 offset:26624\n"
      "v_mul_f32 v99, 0x3eaaaaab, v99\n"
      "v_mfma_f32_16x16x32_bf16 v[154:157], v[134:137], a[152:155], v[154:157]\n"
      "ds_read_b64_tr_b16 v[224:225], v78 offset:24584\n"
      "v_mul_f32 v186, 0x3eaaaaab, v186\n"
      "v_mfma_f32_16x16x32_bf16 v[188:191], v[134:137], a[200:203], v[188:191]\n"
      "ds_write_b128 v219, v[184:187] offset:256\n"
      "v_mfma_f32_16x16x32_bf16 v[192:195], v[174:177], v[100:103], v[26:29]\n"
      "ds_read_b64_tr_b16 a[232:233], v78 offset:24576\n"
      "s_waitcnt lgkmcnt(5)\n"
      "v_mfma_f32_16x16x32_bf16 v[154:157], v[104:107], a[156:159], v[154:157]\n"
      "ds_read_b64_tr_b16 v[248:249], v82 offset:24576\n"
      "s_waitcnt vmcnt(7)\n"
      "v_or3_b32 v166, v166, v169, v254\n"
      "v_mfma_f32_16x16x32_bf16 v[108:111], v[104:107], a[180:183], v[108:111]\n"
      "ds_write_b128 v219, v[96:99] offset:512\n"
      "v_mfma_f32_16x16x32_bf16 v[108:111], v[126:129], a[184:187], v[108:111]\n"
      "ds_read_b64_tr_b16 v[226:227], v78 offset:26632\n"
      "v_mov_b32 v214, 0x3f80\n"
      "v_mfma_f32_16x16x32_bf16 v[134:137], v[120:123], v[180:183], v[48:51]\n"
      "ds_read_b64_tr_b16 v[254:255], v82 offset:38912\n"
      "v_lshlrev_b32_e64 v218, 4, v218\n"
      "v_mfma_f32_16x16x32_bf16 v[154:157], v[126:129], a[160:163], v[154:157]\n"
      "ds_write_b32 v31, v166 offset:5632\n"
      "v_mfma_f32_16x16x32_bf16 v[154:157], v[144:147], a[164:167], v[154:157]\n"
      "s_waitcnt lgkmcnt(0)\n"
      "s_barrier\n"
      "v_mov_b32_e64 v99, 0\n"
      "v_mfma_f32_16x16x16_bf16 v[150:153], v[212:213], v[220:221], v[150:153]\n"
      "ds_read_b128 v[166:169], v9 offset:6656\n"
      "v_lshlrev_b64 v[214:215], v218, v[214:215]\n"
      "v_mfma_f32_16x16x32_bf16 v[108:111], v[144:147], a[188:191], v[108:111]\n"
      "ds_read_b128 v[112:115], v9 offset:5632\n"
      "v_cmp_gt_u32 s[80:81], 64, v218\n"
      "v_mfma_f32_16x16x32_bf16 v[188:191], v[104:107], a[204:207], v[188:191]\n"
      "ds_read_b128 v[104:107], v39 offset:64\n"
      "v_cndmask_b32_e64 v184, 0, v214, s[80:81]\n"
      "v_mfma_f32_16x16x16_bf16 v[134:137], v[124:125], v[220:221], v[134:137]\n"
      "v_mov_b32_e64 v183, 0\n"
      "v_cndmask_b32_e64 v97, 0, v215, s[80:81]\n"
      "v_cvt_pk_bf16_f32 v130, v154, v155\n"
      "v_mfma_f32_16x16x16_bf16 v[140:143], v[172:173], v[220:221], v[140:143]\n"
      "v_cndmask_b32_e64 v96, 0, v214, s[80:81]\n"
      "v_cvt_pk_bf16_f32 v196, v108, v109\n"
      "v_mov_b32_e64 v182, 0\n"
      "v_mfma_f32_16x16x32_bf16 v[188:191], v[126:129], a[208:211], v[188:191]\n"
      "ds_read_b128 v[126:129], v39\n"
      "v_cndmask_b32_e64 v185, 0, v215, s[80:81]\n"
      "v_mfma_f32_16x16x32_bf16 v[188:191], v[144:147], a[212:215], v[188:191]\n"
      "s_waitcnt lgkmcnt(3)\n"
      "v_or3_b32 v166, v166, v167, v168\n"
      "v_mov_b32 v98, 0\n"
      "v_mfma_f32_16x16x16_bf16 v[192:195], v[172:173], v[170:171], v[192:195]\n"
      "v_cvt_pk_bf16_f32 v131, v156, v157\n"
      "v_bitop3_b32 v166, v166, v169, s33 bitop3:0xa8\n"
      "v_cmp_ne_u32 s[56:57], 0, v166\n"
      "v_mfma_f32_16x16x32_bf16 v[166:169], v[208:211], v[100:103], v[52:55]\n"
      "s_waitcnt lgkmcnt(1)\n"
      "v_cvt_pk_bf16_f32 v215, v190, v191\n"
      "v_or3_b32 v112, v112, v113, v114\n"
      "v_mfma_f32_16x16x32_bf16 v[144:147], v[182:185], v[232:235], v[104:107]\n"
      "s_or_b64 vcc, s[56:57], s[54:55]\n"
      "v_cvt_pk_bf16_f32 v214, v188, v189\n"
      "v_bitop3_b32 v112, v112, v115, s33 bitop3:0xa8\n"
      "v_mfma_f32_16x16x32_bf16 v[188:191], v[182:185], a[216:219], v[104:107]\n"
      "s_cbranch_vccnz L_rare_1_%=\n"
      "L_back_2_%=:\n"
      "v_mfma_f32_16x16x32_bf16 v[218:221], v[182:185], a[240:243], v[104:107]\n"
      "s_nop 2\n"
      "s_cbranch_vccnz L_rare_3_%=\n"
      "L_back_4_%=:\n"
      "v_mfma_f32_16x16x32_bf16 v[106:109], v[120:123], v[100:103], v[60:63]\n"
      "s_waitcnt lgkmcnt(0)\n"
      "v_cvt_pk_bf16_f32 v197, v110, v111\n"
      "v_cmp_ne_u32 s[56:57], 0, v112\n"
      "v_mfma_f32_16x16x32_bf16 v[110:113], v[96:99], v[232:235], v[126:129]\n"
      "v_cvt_pk_bf16_f32 v234, v144, v145\n"
      "v_cvt_pk_bf16_f32 v235, v146, v147\n"
      "v_exp_f32 v151, v151\n"
      "v_cvt_pk_bf16_f32 v218, v218, v219\n"
      "v_mfma_f32_16x16x16_bf16 v[166:169], v[212:213], v[170:171], v[166:169]\n"
      "v_exp_f32 v137, v137\n"
      "v_cvt_pk_bf16_f32 v220, v220, v221\n"
      "v_mfma_f32_16x16x16_bf16 v[106:109], v[124:125], v[170:171], v[106:109]\n"
      "v_cvt_pk_bf16_f32 v233, v112, v113\n"
      "v_exp_f32 v153, v153\n"
      "v_exp_f32 v150, v150\n"
      "v_cvt_pk_bf16_f32 v188, v188, v189\n"
      "v_mfma_f32_16x16x32_bf16 v[144:147], v[200:203], v[100:103], v[56:59]\n"
      "ds_read_b128 v[100:103], v10\n"
      "v_cvt_pk_bf16_f32 v232, v110, v111\n"
      "v_mfma_f32_16x16x32_bf16 v[110:113], v[96:99], a[216:219], v[126:129]\n"
      "v_exp_f32_e64 v207, v207\n"
      "v_mfma_f32_16x16x32_bf16 v[154:157], v[96:99], a[240:243], v[126:129]\n"
      "ds_read_b64 v[180:181], v10 offset:16\n"
      "v_accvgpr_write_b32 a243, v220\n"
      "v_cvt_pk_bf16_f32 v150, v150, v151\n"
      "v_mfma_f32_16x16x16_bf16 v[144:147], v[138:139], v[170:171], v[144:147]\n"
      "v_accvgpr_write_b32 a218, v188\n"
      "v_accvgpr_write_b32 a242, v218\n"
      "v_cvt_pk_bf16_f32 v110, v110, v111\n"
      "v_exp_f32 v206, v206\n"
      "v_exp_f32 v140, v140\n"
      "v_cvt_pk_bf16_f32 v112, v112, v113\n"
      "v_cvt_pk_bf16_f32 v154, v154, v155\n"
      "v_cvt_pk_bf16_f32 v190, v190, v191\n"
      "v_mfma_f32_16x16x32_bf16 v[186:189], v[200:203], v[162:165], v[68:71]\n"
      "s_waitcnt lgkmcnt(1)\n"
      "v_exp_f32 v204, v204\n"
      "v_exp_f32 v152, v152\n"
      "v_accvgpr_write_b32 a240, v154\n"
      "v_mfma_f32_16x16x32_bf16 v[126:129], v[100:103], v[200:203], v[30:33]\n"
      "s_cbranch_vccnz L_rare_5_%=\n"
      "L_back_6_%=:\n"
      "v_accvgpr_write_b32 a217, v112\n"
      "v_exp_f32 v135, v135\n"
      "v_mfma_f32_16x16x16_bf16 v[186:189], v[138:139], v[158:159], v[186:189]\n"
      "v_cvt_pk_bf16_f32 v151, v152, v153\n"
      "v_exp_f32 v147, v147\n"
      "v_exp_f32_e64 v134, v134\n"
      "v_mfma_f32_16x16x32_bf16 v[218:221], v[208:211], v[162:165], v[64:67]\n"
      "s_waitcnt lgkmcnt(0)\n"
      "v_exp_f32 v205, v205\n"
      "v_mfma_f32_16x16x16_bf16 v[126:129], v[180:181], v[138:139], v[126:129]\n"
      "v_exp_f32_e64 v107, v107\n"
      "v_cvt_pk_bf16_f32 v153, v206, v207\n"
      "v_mfma_f32_16x16x32_bf16 v[200:203], v[100:103], v[208:211], v[30:33]\n"
      "ds_read_u16 v82, v11 offset:32\n"
      "v_exp_f32_e64 v192, v192\n"
      "v_mfma_f32_16x16x32_bf16 v[208:211], v[174:177], v[162:165], v[28:31]\n"
      "ds_read_u16 v78, v11\n"
      "v_cvt_pk_bf16_f32 v152, v204, v205\n"
      "v_mfma_f32_16x16x16_bf16 v[218:221], v[212:213], v[158:159], v[218:221]\n"
      "v_exp_f32 v109, v109\n"
      "v_accvgpr_write_b32 a216, v110\n"
      "v_exp_f32 v136, v136\n"
      "v_mfma_f32_16x16x16_bf16 v[208:211], v[172:173], v[158:159], v[208:211]\n"
      "v_exp_f32_e64 v167, v167\n"
      "v_cvt_pk_bf16_f32 v90, v140, 0\n"
      "v_mfma_f32_16x16x32_bf16 v[204:207], v[120:123], v[162:165], v[72:75]\n"
      "ds_read_u16 v86, v11 offset:64\n"
      "v_exp_f32_e64 v108, v108\n"
      "v_mfma_f32_16x16x32_bf16 v[140:143], v[100:103], v[174:177], v[30:33]\n"
      "ds_read_b128 v[174:177], v39 offset:128\n"
      "v_cvt_pk_bf16_f32 v88, v134, v135\n"
      "v_mfma_f32_16x16x16_bf16 v[200:203], v[180:181], v[212:213], v[200:203]\n"
      "v_exp_f32 v169, v169\n"
      "v_exp_f32 v166, v166\n"
      "v_cvt_pk_bf16_f32 v89, v136, v137\n"
      "v_mfma_f32_16x16x32_bf16 v[210:213], v[100:103], v[120:123], v[30:33]\n"
      "ds_read_b128 v[160:163], v39 offset:192\n"
      "v_exp_f32 v145, v145\n"
      "v_mfma_f32_16x16x16_bf16 v[140:143], v[180:181], v[172:173], v[140:143]\n"
      "v_cvt_pk_bf16_f32 v156, v156, v157\n"
      "v_cvt_pk_bf16_f32 v94, v192, 0\n"
      "s_waitcnt vmcnt(1)\n"
      "v_or3_b32 v116, v116, v117, v118\n"
      "v_mfma_f32_16x16x32_bf16 v[110:113], v[130:133], v[150:153], 0\n"
      "s_waitcnt lgkmcnt(2)\n"
      "v_exp_f32 v168, v168\n"
      "v_mfma_f32_16x16x32_bf16 v[110:113], v[76:79], v[88:91], v[110:113]\n"
      "v_exp_f32_e64 v146, v146\n"
      "v_mfma_f32_16x16x16_bf16 v[204:207], v[124:125], v[158:159], v[204:207]\n"
      "v_cvt_pk_bf16_f32 v166, v166, v167\n"
      "v_exp_f32_e64 v144, v144\n"
      "v_mfma_f32_16x16x32_bf16 v[100:103], v[214:217], v[150:153], 0\n"
      "v_cvt_pk_bf16_f32 v93, v108, v109\n"
      "v_cndmask_b32 v200, v200, v126, s[58:59]\n"
      "v_mfma_f32_16x16x32_bf16 v[100:103], v[84:87], v[88:91], v[100:103]\n"
      "s_waitcnt lgkmcnt(0)\n"
      "v_cvt_pk_bf16_f32 v167, v168, v169\n"
      "v_accvgpr_write_b32 a219, v190\n"
      "v_mfma_f32_16x16x32_bf16 v[126:129], v[182:185], v[236:239], v[160:163]\n"
      "s_cbranch_vccnz L_rare_7_%=\n"
      "L_back_8_%=:\n"
      "v_mfma_f32_16x16x32_bf16 v[190:193], v[32:35], v[150:153], 0\n"
      "ds_read_b128 v[136:139], v39 offset:320\n"
      "v_exp_f32_e64 v188, v188\n"
      "v_cvt_pk_bf16_f32 v168, v144, v145\n"
      "v_mfma_f32_16x16x32_bf16 v[190:193], v[32:35], v[88:91], v[190:193]\n"
      "s_or_b32 s95, s84, s85\n"
      "v_exp_f32 v189, v189\n"
      "v_mfma_f32_16x16x16_bf16 v[210:213], v[180:181], v[124:125], v[210:213]\n"
      "v_exp_f32_e64 v106, v106\n"
      "v_cvt_pk_bf16_f32 v169, v146, v147\n"
      "v_mfma_f32_16x16x32_bf16 v[142:145], v[96:99], v[236:239], v[174:177]\n"
      "v_exp_f32 v205, v205\n"
      "v_mfma_f32_16x16x32_bf16 v[122:125], v[96:99], a[220:223], v[174:177]\n"
      "v_rcp_f32 v190, v190\n"
      "v_mfma_f32_16x16x32_bf16 v[146:149], v[196:199], v[150:153], 0\n"
      "ds_read_b128 v[152:155], v39 offset:256\n"
      "v_exp_f32 v218, v218\n"
      "v_mfma_f32_16x16x32_bf16 v[146:149], v[80:83], v[88:91], v[146:149]\n"
      "v_exp_f32 v206, v206\n"
      "v_mfma_f32_16x16x32_bf16 v[178:181], v[96:99], a[244:247], v[174:177]\n"
      "s_waitcnt lgkmcnt(1)\n"
      "v_exp_f32 v204, v204\n"
      "v_mfma_f32_16x16x32_bf16 v[170:173], v[182:185], a[248:251], v[136:139]\n"
      "v_cvt_pk_bf16_f32 v122, v122, v123\n"
      "v_exp_f32 v187, v187\n"
      "v_cvt_pk_bf16_f32 v92, v106, v107\n"
      "v_exp_f32 v219, v219\n"
      "v_pk_mul_f32 v[110:111], v[110:111], v[190:191] op_sel_hi:[1,0]\n"
      "v_exp_f32_e64 v207, v207\n"
      "v_pk_mul_f32 v[148:149], v[148:149], v[190:191] op_sel_hi:[1,0]\n"
      "v_cvt_pk_bf16_f32 v124, v124, v125\n"
      "v_pk_mul_f32 v[112:113], v[112:113], v[190:191] op_sel_hi:[1,0]\n"
      "v_cvt_pk_bf16_f32 v236, v142, v143\n"
      "v_mfma_f32_16x16x32_bf16 v[174:177], v[182:185], a[244:247], v[160:163]\n"
      "s_or_b32 s95, s95, s48\n"
      "v_exp_f32 v221, v221\n"
      "v_mfma_f32_16x16x32_bf16 v[106:109], v[182:185], a[220:223], v[160:163]\n"
      "s_cmp_ge_u32 s95, 0x40000000\n"
      "s_cselect_b32 s94, -1, -2\n"
      "v_cndmask_b32 v200, v200, v210, s[60:61]\n"
      "v_cvt_pk_bf16_f32 v89, v206, v207\n"
      "v_accvgpr_write_b32 a220, v122\n"
      "v_cvt_pk_bf16_f32 v239, v128, v129\n"
      "v_pk_mul_f32 v[100:101], v[100:101], v[190:191] op_sel_hi:[1,0]\n"
      "v_exp_f32_e64 v208, v208\n"
      "v_pk_mul_f32 v[146:147], v[146:147], v[190:191] op_sel_hi:[1,0]\n"
      "v_mfma_f32_16x16x32_bf16 v[210:213], v[182:185], v[240:243], v[136:139]\n"
      "v_cndmask_b32 v200, v200, v140, s[62:63]\n"
      "v_cvt_pk_bf16_f32 v176, v176, v177\n"
      "v_cvt_pk_bf16_f32 v172, v172, v173\n"
      "v_mfma_f32_16x16x32_bf16 v[160:163], v[182:185], a[224:227], v[136:139]\n"
      "s_waitcnt lgkmcnt(0)\n"
      "v_exp_f32_e64 v186, v186\n"
      "v_mfma_f32_16x16x32_bf16 v[140:143], v[96:99], a[224:227], v[152:155]\n"
      "s_waitcnt vmcnt(0)\n"
      "v_or3_b32 v116, v116, v119, v223\n"
      "v_cvt_pk_bf16_f32 v108, v108, v109\n"
      "v_mfma_f32_16x16x32_bf16 v[118:121], v[96:99], v[240:243], v[152:155]\n"
      "v_exp_f32 v220, v220\n"
      "v_cvt_pk_bf16_f32 v237, v144, v145\n"
      "v_cvt_pk_bf16_f32 v218, v218, v219\n"
      "v_mfma_f32_16x16x32_bf16 v[134:137], v[96:99], a[248:251], v[152:155]\n"
      "v_cvt_pk_bf16_f32 v180, v180, v181\n"
      "v_cvt_pk_bf16_f32 v110, v110, v111\n"
      "v_mfma_f32_16x16x32_bf16 v[150:153], v[32:35], v[166:169], 0\n"
      "s_cbranch_vccnz L_rare_9_%=\n"
      "L_back_10_%=:\n"
      "v_cvt_pk_bf16_f32 v240, v118, v119\n"
      "v_mfma_f32_16x16x32_bf16 v[150:153], v[32:35], v[92:95], v[150:153]\n"
      "v_cvt_pk_bf16_f32 v162, v162, v163\n"
      "v_cvt_pk_bf16_f32 v219, v220, v221\n"
      "v_cvt_pk_bf16_f32 v111, v112, v113\n"
      "v_cvt_pk_bf16_f32 v220, v186, v187\n"
      "v_mfma_f32_16x16x32_bf16 v[192:195], v[130:133], v[166:169], 0\n"
      "s_cmp_gt_u32 s42, s44\n"
      "s_cselect_b32 s95, s94, s95\n"
      "v_cvt_pk_bf16_f32 v88, v204, v205\n"
      "v_mfma_f32_16x16x32_bf16 v[192:195], v[76:79], v[92:95], v[192:195]\n"
      "v_exp_f32_e64 v200, v200\n"
      "v_cvt_pk_bf16_f32 v241, v120, v121\n"
      "v_mfma_f32_16x16x32_bf16 v[204:207], v[214:217], v[166:169], 0\n"
      "ds_read_b128 v[120:123], v39 offset:448\n"
      "v_cvt_pk_bf16_f32 v242, v210, v211\n"
      "v_mfma_f32_16x16x32_bf16 v[204:207], v[84:87], v[92:95], v[204:207]\n"
      "s_and_b32 s86, s86, -4\n"
      "v_cvt_pk_bf16_f32 v170, v170, v171\n"
      "v_accvgpr_write_b32 a245, v180\n"
      "v_accvgpr_write_b32 a241, v156\n"
      "v_rcp_f32 v150, v150\n"
      "v_cvt_pk_bf16_f32 v112, v146, v147\n"
      "v_pk_mul_f32 v[192:193], v[192:193], v[150:151] op_sel_hi:[1,0]\n"
      "v_cvt_pk_bf16_f32 v142, v142, v143\n"
      "v_pk_mul_f32 v[194:195], v[194:195], v[150:151] op_sel_hi:[1,0]\n"
      "v_cvt_pk_bf16_f32 v221, v188, v189\n"
      "v_pk_mul_f32 v[206:207], v[206:207], v[150:151] op_sel_hi:[1,0]\n"
      "v_pk_mul_f32 v[204:205], v[204:205], v[150:151] op_sel_hi:[1,0]\n"
      "v_mfma_f32_16x16x32_bf16 v[186:189], v[196:199], v[166:169], 0\n"
      "v_cvt_pk_bf16_f32 v136, v136, v137\n"
      "v_cvt_pk_bf16_f32 v200, v200, v200\n"
      "v_cvt_pk_bf16_f32 v178, v178, v179\n"
      "v_mfma_f32_16x16x32_bf16 v[186:189], v[80:83], v[92:95], v[186:189]\n"
      "v_cvt_pk_bf16_f32 v90, v208, 0\n"
      "v_accvgpr_write_b32 a249, v136\n"
      "v_cvt_pk_bf16_f32 v113, v148, v149\n"
      "v_mfma_f32_16x16x32_bf16 v[208:211], v[32:35], v[218:221], 0\n"
      "ds_read_b128 v[166:169], v39 offset:384\n"
      "v_cvt_pk_bf16_f32 v106, v106, v107\n"
      "v_mfma_f32_16x16x32_bf16 v[208:211], v[32:35], v[88:91], v[208:211]\n"
      "ds_write_b128 v3, v[110:113]\n"
      "v_pk_mul_f32 v[102:103], v[102:103], v[190:191] op_sel_hi:[1,0]\n"
      "v_mfma_f32_16x16x32_bf16 v[144:147], v[196:199], v[218:221], 0\n"
      "ds_read_b128 v[110:113], v39 offset:576\n"
      "v_cvt_pk_bf16_f32 v174, v174, v175\n"
      "v_cvt_pk_bf16_f32 v192, v192, v193\n"
      "v_accvgpr_write_b32 a247, v176\n"
      "v_mfma_f32_16x16x32_bf16 v[144:147], v[80:83], v[88:91], v[144:147]\n"
      "s_waitcnt lgkmcnt(3)\n"
      "v_rcp_f32_e64 v208, v208\n"
      "v_cvt_pk_bf16_f32 v243, v212, v213\n"
      "v_mfma_f32_16x16x32_bf16 v[154:157], v[130:133], v[218:221], 0\n"
      "ds_read_b128 v[136:139], v39 offset:640\n"
      "v_cvt_pk_bf16_f32 v140, v140, v141\n"
      "v_accvgpr_write_b32 a246, v174\n"
      "v_mfma_f32_16x16x32_bf16 v[154:157], v[76:79], v[88:91], v[154:157]\n"
      "ds_write_b16 v13, v200\n"
      "v_mfma_f32_16x16x32_bf16 v[174:177], v[182:185], a[228:231], v[120:123]\n"
      "ds_read_b128 v[200:203], v39 offset:512\n"
      "v_accvgpr_write_b32 a244, v178\n"
      "v_mfma_f32_16x16x32_bf16 v[178:181], v[214:217], v[218:221], 0\n"
      "v_accvgpr_write_b32 a225, v142\n"
      "v_cvt_pk_bf16_f32 v100, v100, v101\n"
      "v_mfma_f32_16x16x32_bf16 v[178:181], v[84:87], v[88:91], v[178:181]\n"
      "v_cvt_pk_bf16_f32 v160, v160, v161\n"
      "v_cvt_pk_bf16_f32 v101, v102, v103\n"
      "v_mfma_f32_16x16x32_bf16 v[210:213], v[182:185], a[252:255], v[120:123]\n"
      "s_waitcnt lgkmcnt(3)\n"
      "v_cvt_pk_bf16_f32 v134, v134, v135\n"
      "v_cvt_pk_bf16_f32 v238, v126, v127\n"
      "v_cvt_pk_bf16_f32 v176, v176, v177\n"
      "v_mfma_f32_16x16x32_bf16 v[126:129], v[96:99], a[228:231], v[166:169]\n"
      "ds_write_b64 v4, v[100:101]\n"
      "v_accvgpr_write_b32 a248, v134\n"
      "v_accvgpr_write_b32 a231, v176\n"
      "v_pk_mul_f32 v[186:187], v[186:187], v[150:151] op_sel_hi:[1,0]\n"
      "v_pk_mul_f32 v[188:189], v[188:189], v[150:151] op_sel_hi:[1,0]\n"
      "v_pk_mul_f32 v[154:155], v[154:155], v[208:209] op_sel_hi:[1,0]\n"
      "v_cvt_pk_bf16_f32 v154, v154, v155\n"
      "v_pk_mul_f32 v[180:181], v[180:181], v[208:209] op_sel_hi:[1,0]\n"
      "v_cvt_pk_bf16_f32 v174, v174, v175\n"
      "v_cvt_pk_bf16_f32 v193, v194, v195\n"
      "v_accvgpr_write_b32 a227, v162\n"
      "v_pk_mul_f32 v[178:179], v[178:179], v[208:209] op_sel_hi:[1,0]\n"
      "v_mfma_f32_16x16x32_bf16 v[162:165], v[96:99], v[244:247], v[166:169]\n"
      "ds_read_b128 v[150:153], v39 offset:704\n"
      "v_cvt_pk_bf16_f32 v128, v128, v129\n"
      "v_cvt_pk_bf16_f32 v212, v212, v213\n"
      "v_mfma_f32_16x16x32_bf16 v[102:105], v[182:185], v[244:247], v[120:123]\n"
      "s_waitcnt lgkmcnt(2)\n"
      "v_accvgpr_write_b32 a221, v124\n"
      "v_cvt_pk_bf16_f32 v194, v186, v187\n"
      "v_mfma_f32_16x16x32_bf16 v[122:125], v[96:99], a[236:239], v[136:139]\n"
      "s_add_u32 s42, s42, 1\n"
      "v_cvt_pk_bf16_f32 v195, v188, v189\n"
      "v_accvgpr_write_b32 a224, v140\n"
      "v_cvt_pk_bf16_f32 v204, v204, v205\n"
      "v_cvt_pk_bf16_f32 v205, v206, v207\n"
      "v_mfma_f32_16x16x32_bf16 v[186:189], v[96:99], a[252:255], v[166:169]\n"
      "s_cmp_ge_u32 s95, 0x40000000\n"
      "s_cselect_b64 vcc, -1, s[56:57]\n"
      "v_accvgpr_write_b32 a226, v160\n"
      "v_cvt_pk_bf16_f32 v124, v124, v125\n"
      "v_mfma_f32_16x16x32_bf16 v[166:169], v[182:185], a[232:235], v[110:113]\n"
      "ds_write_b128 v3, v[192:195] offset:256\n"
      "v_pk_mul_f32 v[156:157], v[156:157], v[208:209] op_sel_hi:[1,0]\n"
      "v_cvt_pk_bf16_f32 v210, v210, v211\n"
      "v_pk_mul_f32 v[146:147], v[146:147], v[208:209] op_sel_hi:[1,0]\n"
      "v_pk_mul_f32 v[144:145], v[144:145], v[208:209] op_sel_hi:[1,0]\n"
      "v_mfma_f32_16x16x32_bf16 v[206:209], v[96:99], v[224:227], v[200:203]\n"
      "ds_read_b128 v[218:221], v14\n"
      "v_cvt_pk_bf16_f32 v186, v186, v187\n"
      "v_cvt_pk_bf16_f32 v126, v126, v127\n"
      "v_cvt_pk_bf16_f32 v178, v178, v179\n"
      "v_mfma_f32_16x16x32_bf16 v[140:143], v[96:99], a[232:235], v[200:203]\n"
      "ds_write_b32 v31, v116 offset:4608\n"
      "v_cvt_pk_bf16_f32 v168, v168, v169\n"
      "v_cvt_pk_bf16_f32 v179, v180, v181\n"
      "v_mfma_f32_16x16x32_bf16 v[158:161], v[96:99], v[252:255], v[136:139]\n"
      "v_cvt_pk_bf16_f32 v166, v166, v167\n"
      "v_cvt_pk_bf16_f32 v247, v104, v105\n"
      "v_cvt_pk_bf16_f32 v155, v156, v157\n"
      "v_accvgpr_write_b32 a228, v126\n"
      "v_mfma_f32_16x16x32_bf16 v[190:193], v[182:185], v[224:227], v[110:113]\n"
      "ds_write_b64 v4, v[178:179] offset:512\n"
      "v_mfma_f32_16x16x32_bf16 v[178:181], v[96:99], v[248:251], v[200:203]\n"
      "s_waitcnt lgkmcnt(3)\n"
      "v_accvgpr_write_b32 a230, v174\n"
      "v_cvt_pk_bf16_f32 v244, v162, v163\n"
      "v_mfma_f32_16x16x32_bf16 v[200:203], v[182:185], a[236:239], v[150:153]\n"
      "ds_read_b128 v[174:177], v14 offset:64\n"
      "v_cvt_pk_bf16_f32 v140, v140, v141\n"
      "v_cvt_pk_bf16_f32 v122, v122, v123\n"
      "v_cvt_pk_bf16_f32 v226, v190, v191\n"
      "v_cvt_pk_bf16_f32 v188, v188, v189\n"
      "v_cvt_pk_bf16_f32 v157, v146, v147\n"
      "v_accvgpr_write_b32 a222, v106\n"
      "v_mfma_f32_16x16x32_bf16 v[146:149], v[182:185], v[228:231], v[150:153]\n"
      "ds_write_b64 v4, v[204:205] offset:256\n"
      "v_mfma_f32_16x16x32_bf16 v[118:121], v[96:99], v[228:231], v[136:139]\n"
      "v_cvt_pk_bf16_f32 v227, v192, v193\n"
      "v_cvt_pk_bf16_f32 v156, v144, v145\n"
      "v_cvt_pk_bf16_f32 v246, v102, v103\n"
      "v_mfma_f32_16x16x32_bf16 v[190:193], v[182:185], v[248:251], v[110:113]\n"
      "ds_write_b128 v3, v[154:157] offset:512\n"
      "v_cvt_pk_bf16_f32 v202, v202, v203\n"
      "v_mfma_f32_16x16x32_bf16 v[110:113], v[182:185], v[252:255], v[150:153]\n"
      "s_waitcnt lgkmcnt(2)\n"
      "v_accvgpr_write_b32 a250, v170\n"
      "v_accvgpr_write_b32 a254, v210\n"
      "v_cvt_pk_bf16_f32 v225, v208, v209\n"
      "v_cvt_pk_bf16_f32 v224, v206, v207\n"
      "v_cvt_pk_bf16_f32 v231, v148, v149\n"
      "v_accvgpr_write_b32 a252, v186\n"
      "v_accvgpr_write_b32 a251, v172\n"
      "v_cvt_pk_bf16_f32 v230, v146, v147\n"
      "v_accvgpr_write_b32 a239, v202\n"
      "v_cvt_pk_bf16_f32 v254, v110, v111\n"
      "v_accvgpr_write_b32 a236, v122\n"
      "v_cvt_pk_bf16_f32 v253, v160, v161\n"
      "v_cvt_pk_bf16_f32 v228, v118, v119\n"
      "v_cvt_pk_bf16_f32 v255, v112, v113\n"
      "v_cvt_pk_bf16_f32 v142, v142, v143\n"
      "v_cvt_pk_bf16_f32 v252, v158, v159\n"
      "v_accvgpr_write_b32 a234, v166\n"
      "v_accvgpr_write_b32 a253, v188\n"
      "v_accvgpr_write_b32 a237, v124\n"
      "v_accvgpr_write_b32 a235, v168\n"
      "v_cvt_pk_bf16_f32 v249, v180, v181\n"
      "v_accvgpr_write_b32 a223, v108\n"
      "v_cvt_pk_bf16_f32 v200, v200, v201\n"
      "v_accvgpr_write_b32 a238, v200\n"
      "v_accvgpr_write_b32 a233, v142\n"
      "v_cvt_pk_bf16_f32 v248, v178, v179\n"
      "v_cvt_pk_bf16_f32 v229, v120, v121\n"
      "v_cvt_pk_bf16_f32 v250, v190, v191\n"
      "v_accvgpr_write_b32 a255, v212\n"
      "v_mfma_f32_16x16x32_bf16 v[208:211], v[218:221], v[32:35], 0\n"
      "v_cvt_pk_bf16_f32 v245, v164, v165\n"
      "v_accvgpr_write_b32 a229, v128\n"
      "v_cvt_pk_bf16_f32 v251, v192, v193\n"
      "v_accvgpr_write_b32 a232, v140\n"
      "v_mfma_f32_16x16x32_bf16 v[208:211], v[174:177], v[32:35], v[208:211]\n"
      "s_waitcnt lgkmcnt(0)\n"
      "s_barrier\n"
      "s_or_b64 vcc, vcc, s[54:55]\n"
      "s_cbranch_vccnz L_disp1_%=\n"
      "s_nop 7\n"
      "s_nop 7\n"
      "L_loop1_%=:\n"
      "v_mfma_f32_16x16x32_bf16 v[168:171], v[218:221], v[130:133], 0\n"
      "ds_read_b128 v[164:167], v0 offset:18432\n"
      "ds_read_b128 v[178:181], v9\n"
      "v_mfma_f32_16x16x32_bf16 v[168:171], v[174:177], v[76:79], v[168:171]\n"
      "ds_read_b128 v[108:111], v0 offset:30720\n"
      "v_readlane_b32 s46, v37, s42\n"
      "v_mfma_f32_16x16x32_bf16 v[150:153], v[218:221], v[196:199], 0\n"
      "ds_read_b128 v[142:145], v1 offset:18432\n"
      "s_add_u32 m0, s64, 0\n"
      "v_mfma_f32_16x16x32_bf16 v[150:153], v[174:177], v[80:83], v[150:153]\n"
      "buffer_load_dwordx4 v16, s[16:19], s84 offen  lds\n"
      "v_mfma_f32_16x16x32_bf16 v[194:197], v[218:221], v[214:217], 0\n"
      "ds_read_b128 v[190:193], v0 offset:24576\n"
      "v_readlane_b32 s47, v38, s43\n"
      "v_mfma_f32_16x16x32_bf16 v[194:197], v[174:177], v[84:87], v[194:197]\n"
      "ds_read_b128 v[170:173], v0 offset:32768\n"
      "s_waitcnt lgkmcnt(5)\n"
      "v_mfma_f32_16x16x32_bf16 v[124:127], a[0:3], v[164:167], 0\n"
      "ds_read_b128 v[128:131], v9 offset:4608\n"
      "v_cndmask_b32 v168, v168, v150, s[58:59]\n"
      "v_mfma_f32_16x16x32_bf16 v[182:185], a[24:27], v[164:167], 0\n"
      "s_add_u32 m0, s64, 4096\n"
      "s_waitcnt lgkmcnt(5)\n"
      "v_add_f32 v180, v180, v181\n"
      "v_mfma_f32_16x16x32_bf16 v[138:141], a[48:51], v[164:167], 0\n"
      "buffer_load_dwordx4 v17, s[16:19], s84 offen  lds\n"
      "v_mfma_f32_16x16x32_bf16 v[152:155], a[72:75], v[164:167], 0\n"
      "v_add_f32 v178, v178, v179\n"
      "v_add_f32 v178, v178, v180\n"
      "v_mfma_f32_16x16x32_bf16 v[174:177], a[96:99], v[164:167], 0\n"
      "s_waitcnt lgkmcnt(2)\n"
      "v_rsq_f32 v178, v178\n"
      "v_mfma_f32_16x16x32_bf16 v[218:221], a[120:123], v[164:167], 0\n"
      "s_add_u32 m0, s64, 8192\n"
      "v_readlane_b32 s45, v37, s43\n"
      "v_mul_f32 v178, 0x415db3d7, v178\n"
      "v_mfma_f32_16x16x32_bf16 v[124:127], a[4:7], v[142:145], v[124:127]\n"
      "ds_read_b128 v[186:189], v1 offset:24576\n"
      "v_mov_b32 v180, v178\n"
      "v_mfma_f32_16x16x32_bf16 v[124:127], a[8:11], v[190:193], v[124:127]\n"
      "buffer_load_dwordx4 v18, s[16:19], s84 offen  lds\n"
      "v_mfma_f32_16x16x32_bf16 v[182:185], a[28:31], v[142:145], v[182:185]\n"
      "v_permlane32_swap_b32 v178, v180\n"
      "v_mfma_f32_16x16x32_bf16 v[182:185], a[32:35], v[190:193], v[182:185]\n"
      "ds_read_b128 v[210:213], v1 offset:30720\n"
      "v_mov_b32_e64 v179, v178\n"
      "v_mfma_f32_16x16x32_bf16 v[138:141], a[52:55], v[142:145], v[138:141]\n"
      "s_add_u32 m0, s64, 12288\n"
      "v_permlane16_swap_b32 v178, v179\n"
      "v_mfma_f32_16x16x32_bf16 v[138:141], a[56:59], v[190:193], v[138:141]\n"
      "v_mul_f32 v200, v179, v200\n"
      "v_mul_f32_e64 v201, v179, v201\n"
      "v_mfma_f32_16x16x32_bf16 v[152:155], a[76:79], v[142:145], v[152:155]\n"
      "buffer_load_dwordx4 v19, s[16:19], s84 offen  lds\n"
      "v_mfma_f32_16x16x32_bf16 v[152:155], a[80:83], v[190:193], v[152:155]\n"
      "v_mul_f32 v202, v179, v202\n"
      "v_mul_f32 v115, v179, v115\n"
      "v_mfma_f32_16x16x32_bf16 v[174:177], a[100:103], v[142:145], v[174:177]\n"
      "ds_read_b128 v[104:107], v1 offset:26624\n"
      "v_mul_f32_e64 v135, v178, v135\n"
      "v_mfma_f32_16x16x32_bf16 v[174:177], a[104:107], v[190:193], v[174:177]\n"
      "s_add_u32 m0, s65, 16384\n"
      "v_mul_f32 v205, v179, v205\n"
      "v_mul_f32 v137, v178, v137\n"
      "v_mfma_f32_16x16x32_bf16 v[218:221], a[124:127], v[142:145], v[218:221]\n"
      "s_or_b32 s95, s45, s46\n"
      "v_mul_f32 v163, v180, v163\n"
      "v_mul_f32 v114, v179, v114\n"
      "v_mfma_f32_16x16x32_bf16 v[218:221], a[128:131], v[190:193], v[218:221]\n"
      "s_waitcnt lgkmcnt(1)\n"
      "v_mul_f32 v158, v180, v158\n"
      "v_mul_f32 v203, v179, v203\n"
      "v_mfma_f32_16x16x32_bf16 v[124:127], a[12:15], v[186:189], v[124:127]\n"
      "v_mul_f32 v134, v178, v134\n"
      "v_mul_f32 v117, v178, v117\n"
      "v_mfma_f32_16x16x32_bf16 v[124:127], a[16:19], v[108:111], v[124:127]\n"
      "buffer_load_dwordx4 v15, s[16:19], s84 offen  lds\n"
      "v_mfma_f32_16x16x32_bf16 v[124:127], a[20:23], v[210:213], v[124:127]\n"
      "v_mul_f32 v161, v180, v161\n"
      "v_mul_f32 v97, v178, v97\n"
      "v_mfma_f32_16x16x32_bf16 v[182:185], a[36:39], v[186:189], v[182:185]\n"
      "v_mul_f32 v96, v178, v96\n"
      "v_mul_f32 v116, v178, v116\n"
      "v_mfma_f32_16x16x32_bf16 v[182:185], a[40:43], v[108:111], v[182:185]\n"
      "v_mul_f32 v147, v180, v147\n"
      "v_mul_f32 v207, v179, v207\n"
      "v_mfma_f32_16x16x32_bf16 v[182:185], a[44:47], v[210:213], v[182:185]\n"
      "v_mul_f32_e64 v99, v178, v99\n"
      "v_cvt_pk_bf16_f32 v96, v96, v97\n"
      "v_mfma_f32_16x16x32_bf16 v[138:141], a[60:63], v[186:189], v[138:141]\n"
      "ds_read_b128 v[120:123], v1 offset:20480\n"
      "v_or3_b32 v128, v128, v129, v130\n"
      "v_mfma_f32_16x16x32_bf16 v[138:141], a[64:67], v[108:111], v[138:141]\n"
      "v_mul_f32 v206, v179, v206\n"
      "v_mul_f32 v157, v180, v157\n"
      "v_mfma_f32_16x16x32_bf16 v[138:141], a[68:71], v[210:213], v[138:141]\n"
      "buffer_load_dwordx4 v[100:103], v36, s[28:31], s86 offen\n"
      "v_mfma_f32_16x16x32_bf16 v[152:155], a[84:87], v[186:189], v[152:155]\n"
      "s_or_b32 s95, s95, s48\n"
      "v_mul_f32 v98, v178, v98\n"
      "v_fma_f32 v150, v124, v124, s51\n"
      "v_mfma_f32_16x16x32_bf16 v[152:155], a[88:91], v[108:111], v[152:155]\n"
      "v_cvt_pk_bf16_f32 v134, v134, v135\n"
      "v_mul_f32 v204, v179, v204\n"
      "v_mfma_f32_16x16x32_bf16 v[152:155], a[92:95], v[210:213], v[152:155]\n"
      "v_fmac_f32 v150, v125, v125\n"
      "v_mul_f32 v146, v180, v146\n"
      "v_mfma_f32_16x16x32_bf16 v[174:177], a[108:111], v[186:189], v[174:177]\n"
      "v_mul_f32 v119, v178, v119\n"
      "v_cvt_pk_bf16_f32 v97, v98, v99\n"
      "v_mfma_f32_16x16x32_bf16 v[174:177], a[112:115], v[108:111], v[174:177]\n"
      "v_mul_f32_e64 v113, v179, v113\n"
      "v_cvt_pk_bf16_f32 v204, v204, v205\n"
      "v_mfma_f32_16x16x32_bf16 v[174:177], a[116:119], v[210:213], v[174:177]\n"
      "buffer_load_dword v86, v29, s[28:31], s86 offen\n"
      "v_mfma_f32_16x16x32_bf16 v[218:221], a[132:135], v[186:189], v[218:221]\n"
      "v_cvt_pk_bf16_f32 v200, v200, v201\n"
      "v_mul_f32_e64 v136, v178, v136\n"
      "v_mfma_f32_16x16x32_bf16 v[218:221], a[136:139], v[108:111], v[218:221]\n"
      "v_bitop3_b32 v128, v128, v131, s33 bitop3:0xa8\n"
      "v_fma_f32 v151, v152, v152, s51\n"
      "v_mfma_f32_16x16x32_bf16 v[218:221], a[140:143], v[210:213], v[218:221]\n"
      "v_cvt_pk_bf16_f32 v98, v116, v117\n"
      "v_fmac_f32_e64 v150, v126, v126\n"
      "v_mfma_f32_16x16x32_bf16 v[130:133], v[164:167], a[144:147], 0\n"
      "v_cvt_pk_bf16_f32 v205, v206, v207\n"
      "v_fmac_f32 v151, v153, v153\n"
      "v_mfma_f32_16x16x32_bf16 v[130:133], v[142:145], a[148:151], v[130:133]\n"
      "v_mul_f32 v118, v178, v118\n"
      "v_cvt_pk_bf16_f32 v99, v118, v119\n"
      "v_mfma_f32_16x16x32_bf16 v[130:133], v[190:193], a[152:155], v[130:133]\n"
      "ds_read_b128 v[116:119], v0 offset:20480\n"
      "v_cvt_pk_bf16_f32 v201, v202, v203\n"
      "v_mfma_f32_16x16x32_bf16 v[130:133], v[186:189], a[156:159], v[130:133]\n"
      "ds_write_b128 v5, v[96:99] offset:18432\n"
      "v_mfma_f32_16x16x32_bf16 v[130:133], v[108:111], a[160:163], v[130:133]\n"
      "v_mul_f32 v112, v179, v112\n"
      "v_fmac_f32 v151, v154, v154\n"
      "v_mfma_f32_16x16x32_bf16 v[130:133], v[210:213], a[164:167], v[130:133]\n"
      "v_cvt_pk_bf16_f32 v202, v112, v113\n"
      "v_fmac_f32 v151, v155, v155\n"
      "v_mfma_f32_16x16x32_bf16 v[196:199], v[164:167], a[168:171], 0\n"
      "ds_read_b128 v[96:99], v0 offset:26624\n"
      "v_fmac_f32 v151, v174, v174\n"
      "v_mfma_f32_16x16x32_bf16 v[196:199], v[142:145], a[172:175], v[196:199]\n"
      "v_cvt_pk_bf16_f32 v203, v114, v115\n"
      "v_fmac_f32_e64 v151, v175, v175\n"
      "v_mfma_f32_16x16x32_bf16 v[196:199], v[190:193], a[176:179], v[196:199]\n"
      "ds_write_b128 v5, v[200:203] offset:24576\n"
      "v_mfma_f32_16x16x32_bf16 v[196:199], v[186:189], a[180:183], v[196:199]\n"
      "v_mul_f32 v160, v180, v160\n"
      "v_fmac_f32 v150, v127, v127\n"
      "v_mfma_f32_16x16x32_bf16 v[196:199], v[108:111], a[184:187], v[196:199]\n"
      "v_fmac_f32 v151, v176, v176\n"
      "v_fmac_f32 v150, v182, v182\n"
      "v_mfma_f32_16x16x32_bf16 v[196:199], v[210:213], a[188:191], v[196:199]\n"
      "v_cvt_pk_bf16_f32 v130, v130, v131\n"
      "v_mul_f32_e64 v162, v180, v162\n"
      "v_mfma_f32_16x16x32_bf16 v[214:217], v[164:167], a[192:195], 0\n"
      "v_fmac_f32 v151, v177, v177\n"
      "v_cvt_pk_bf16_f32 v160, v160, v161\n"
      "v_mfma_f32_16x16x32_bf16 v[214:217], v[142:145], a[196:199], v[214:217]\n"
      "v_mul_f32 v149, v180, v149\n"
      "v_cndmask_b32 v168, v168, v194, s[60:61]\n"
      "v_mfma_f32_16x16x32_bf16 v[214:217], v[190:193], a[200:203], v[214:217]\n"
      "ds_read_b128 v[192:195], v0 offset:22528\n"
      "v_mul_f32_e64 v156, v180, v156\n"
      "v_mfma_f32_16x16x32_bf16 v[214:217], v[186:189], a[204:207], v[214:217]\n"
      "ds_read_b128 v[112:115], v1 offset:32768\n"
      "v_cvt_pk_bf16_f32 v156, v156, v157\n"
      "v_mfma_f32_16x16x32_bf16 v[214:217], v[108:111], a[208:211], v[214:217]\n"
      "ds_read_b128 v[200:203], v0 offset:28672\n"
      "v_mul_f32 v159, v180, v159\n"
      "v_mfma_f32_16x16x32_bf16 v[214:217], v[210:213], a[212:215], v[214:217]\n"
      "s_waitcnt lgkmcnt(3)\n"
      "v_fmac_f32 v151, v218, v218\n"
      "v_fmac_f32 v150, v183, v183\n"
      "v_mfma_f32_16x16x32_bf16 v[210:213], a[0:3], v[116:119], 0\n"
      "v_cvt_pk_bf16_f32 v135, v136, v137\n"
      "v_fmac_f32 v150, v184, v184\n"
      "v_mfma_f32_16x16x32_bf16 v[210:213], a[4:7], v[120:123], v[210:213]\n"
      "v_fmac_f32 v150, v185, v185\n"
      "v_cvt_pk_bf16_f32 v131, v132, v133\n"
      "v_mfma_f32_16x16x32_bf16 v[210:213], a[8:11], v[96:99], v[210:213]\n"
      "ds_write_b64 v6, v[204:205] offset:24576\n"
      "v_mfma_f32_16x16x32_bf16 v[210:213], a[12:15], v[104:107], v[210:213]\n"
      "ds_read_b128 v[186:189], v1 offset:22528\n"
      "v_cvt_pk_bf16_f32 v157, v158, v159\n"
      "v_mfma_f32_16x16x32_bf16 v[210:213], a[16:19], v[170:173], v[210:213]\n"
      "s_waitcnt lgkmcnt(2)\n"
      "v_fmac_f32 v150, v138, v138\n"
      "v_cvt_pk_bf16_f32 v158, v146, v147\n"
      "v_mfma_f32_16x16x32_bf16 v[210:213], a[20:23], v[112:115], v[210:213]\n"
      "ds_write_b64 v6, v[134:135] offset:18432\n"
      "v_mfma_f32_16x16x32_bf16 v[142:145], a[24:27], v[116:119], 0\n"
      "s_cmp_eq_u32 s43, 21\n"
      "s_cselect_b32 s95, -1, s95\n"
      "v_fmac_f32 v151, v219, v219\n"
      "v_mfma_f32_16x16x32_bf16 v[142:145], a[28:31], v[120:123], v[142:145]\n"
      "v_fmac_f32 v151, v220, v220\n"
      "v_fmac_f32_e64 v150, v139, v139\n"
      "v_mfma_f32_16x16x32_bf16 v[142:145], a[32:35], v[96:99], v[142:145]\n"
      "v_fmac_f32 v151, v221, v221\n"
      "v_fmac_f32 v150, v140, v140\n"
      "v_mfma_f32_16x16x32_bf16 v[142:145], a[36:39], v[104:107], v[142:145]\n"
      "s_cmp_ge_u32 s95, 0x40000000\n"
      "s_cselect_b32 s94, -1, -2\n"
      "v_fmac_f32_e64 v150, v141, v141\n"
      "v_mfma_f32_16x16x32_bf16 v[142:145], a[40:43], v[170:173], v[142:145]\n"
      "v_mov_b32_dpp v208, v208 quad_perm:[0,0,0,0] row_mask:0xf bank_mask:0xf\n"
      "v_fma_f32 v129, v210, v210, s51\n"
      "v_mfma_f32_16x16x32_bf16 v[142:145], a[44:47], v[112:115], v[142:145]\n"
      "v_permlane16_swap_b32 v150, v151\n"
      "v_mfma_f32_16x16x32_bf16 v[108:111], a[48:51], v[116:119], 0\n"
      "ds_read_b128 v[134:137], v1 offset:28672\n"
      "v_fmac_f32 v129, v211, v211\n"
      "v_mfma_f32_16x16x32_bf16 v[108:111], a[52:55], v[120:123], v[108:111]\n"
      "v_fmac_f32 v129, v212, v212\n"
      "v_add_f32 v150, v150, v151\n"
      "v_mfma_f32_16x16x32_bf16 v[108:111], a[56:59], v[96:99], v[108:111]\n"
      "v_mul_f32 v148, v180, v148\n"
      "v_mov_b32 v151, v150\n"
      "v_mfma_f32_16x16x32_bf16 v[108:111], a[60:63], v[104:107], v[108:111]\n"
      "v_cvt_pk_bf16_f32 v214, v214, v215\n"
      "v_cvt_pk_bf16_f32 v159, v148, v149\n"
      "v_mfma_f32_16x16x32_bf16 v[108:111], a[64:67], v[170:173], v[108:111]\n"
      "v_permlane32_swap_b32 v150, v151\n"
      "v_mfma_f32_16x16x32_bf16 v[108:111], a[68:71], v[112:115], v[108:111]\n"
      "v_fmac_f32 v129, v213, v213\n"
      "v_add_f32 v150, v150, v151\n"
      "v_mfma_f32_16x16x32_bf16 v[178:181], a[72:75], v[116:119], 0\n"
      "v_rsq_f32 v150, v150\n"
      "v_mfma_f32_16x16x32_bf16 v[178:181], a[76:79], v[120:123], v[178:181]\n"
      "v_mov_b32 v151, v150\n"
      "v_fmac_f32 v129, v142, v142\n"
      "v_mfma_f32_16x16x32_bf16 v[178:181], a[80:83], v[96:99], v[178:181]\n"
      "v_permlane16_swap_b32_e64 v150, v151\n"
      "v_mfma_f32_16x16x32_bf16 v[178:181], a[84:87], v[104:107], v[178:181]\n"
      "v_cvt_pk_bf16_f32 v161, v162, v163\n"
      "v_cvt_pk_bf16_f32 v196, v196, v197\n"
      "v_mfma_f32_16x16x32_bf16 v[178:181], a[88:91], v[170:173], v[178:181]\n"
      "v_rcp_f32 v208, v208\n"
      "v_mfma_f32_16x16x32_bf16 v[178:181], a[92:95], v[112:115], v[178:181]\n"
      "v_mul_f32 v168, v208, v168\n"
      "v_mul_f32_e64 v150, s50, v150\n"
      "v_mfma_f32_16x16x32_bf16 v[206:209], a[96:99], v[116:119], 0\n"
      "ds_write_b64 v6, v[160:161] offset:30720\n"
      "v_mfma_f32_16x16x32_bf16 v[206:209], a[100:103], v[120:123], v[206:209]\n"
      "v_fmac_f32_e64 v129, v143, v143\n"
      "v_cvt_pk_bf16_f32 v215, v216, v217\n"
      "v_mfma_f32_16x16x32_bf16 v[206:209], a[104:107], v[96:99], v[206:209]\n"
      "v_fmac_f32 v129, v144, v144\n"
      "v_mul_f32 v138, v150, v138\n"
      "v_mfma_f32_16x16x32_bf16 v[206:209], a[108:111], v[104:107], v[206:209]\n"
      "v_fma_f32 v204, v178, v178, s51\n"
      "v_cvt_pk_bf16_f32 v168, v168, v168\n"
      "v_mfma_f32_16x16x32_bf16 v[206:209], a[112:115], v[170:173], v[206:209]\n"
      "v_cmp_ne_u32 s[56:57], 0, v128\n"
      "v_fmac_f32_e64 v129, v145, v145\n"
      "v_mfma_f32_16x16x32_bf16 v[206:209], a[116:119], v[112:115], v[206:209]\n"
      "ds_write_b128 v5, v[156:159] offset:30720\n"
      "v_mfma_f32_16x16x32_bf16 v[156:159], a[120:123], v[116:119], 0\n"
      "v_mul_f32 v220, v151, v220\n"
      "v_fmac_f32 v204, v179, v179\n"
      "v_mfma_f32_16x16x32_bf16 v[156:159], a[124:127], v[120:123], v[156:159]\n"
      "v_cvt_pk_bf16_f32 v197, v198, v199\n"
      "v_mul_f32 v219, v151, v219\n"
      "v_mfma_f32_16x16x32_bf16 v[156:159], a[128:131], v[96:99], v[156:159]\n"
      "v_fmac_f32 v204, v180, v180\n"
      "v_mul_f32 v139, v150, v139\n"
      "v_mfma_f32_16x16x32_bf16 v[156:159], a[132:135], v[104:107], v[156:159]\n"
      "v_mul_f32 v184, v150, v184\n"
      "v_mul_f32 v124, v150, v124\n"
      "v_mfma_f32_16x16x32_bf16 v[156:159], a[136:139], v[170:173], v[156:159]\n"
      "v_mul_f32 v218, v151, v218\n"
      "v_cvt_pk_bf16_f32 v138, v138, v139\n"
      "v_mfma_f32_16x16x32_bf16 v[156:159], a[140:143], v[112:115], v[156:159]\n"
      "v_mul_f32 v141, v150, v141\n"
      "v_mul_f32 v154, v151, v154\n"
      "v_mfma_f32_16x16x32_bf16 v[146:149], v[116:119], a[144:147], 0\n"
      "v_mul_f32 v221, v151, v221\n"
      "v_fmac_f32 v204, v181, v181\n"
      "v_mfma_f32_16x16x32_bf16 v[146:149], v[120:123], a[148:151], v[146:149]\n"
      "ds_write_b16 v12, v168\n"
      "v_mfma_f32_16x16x32_bf16 v[146:149], v[96:99], a[152:155], v[146:149]\n"
      "v_mul_f32 v126, v150, v126\n"
      "v_fmac_f32 v129, v108, v108\n"
      "v_mfma_f32_16x16x32_bf16 v[146:149], v[104:107], a[156:159], v[146:149]\n"
      "v_mul_f32 v125, v150, v125\n"
      "v_fmac_f32 v204, v206, v206\n"
      "v_mfma_f32_16x16x32_bf16 v[146:149], v[170:173], a[160:163], v[146:149]\n"
      "v_fmac_f32 v129, v109, v109\n"
      "v_fmac_f32 v204, v207, v207\n"
      "v_mfma_f32_16x16x32_bf16 v[146:149], v[112:115], a[164:167], v[146:149]\n"
      "v_fmac_f32 v204, v208, v208\n"
      "v_mul_f32 v153, v151, v153\n"
      "v_mfma_f32_16x16x32_bf16 v[166:169], v[116:119], a[168:171], 0\n"
      "v_mul_f32 v127, v150, v127\n"
      "v_mul_f32 v152, v151, v152\n"
      "v_mfma_f32_16x16x32_bf16 v[166:169], v[120:123], a[172:175], v[166:169]\n"
      "v_cvt_pk_bf16_f32 v218, v218, v219\n"
      "v_fmac_f32_e64 v204, v209, v209\n"
      "v_mfma_f32_16x16x32_bf16 v[166:169], v[96:99], a[176:179], v[166:169]\n"
      "v_fmac_f32 v129, v110, v110\n"
      "v_fmac_f32 v204, v156, v156\n"
      "v_mfma_f32_16x16x32_bf16 v[166:169], v[104:107], a[180:183], v[166:169]\n"
      "v_mul_f32 v140, v150, v140\n"
      "v_mul_f32 v155, v151, v155\n"
      "v_mfma_f32_16x16x32_bf16 v[166:169], v[170:173], a[184:187], v[166:169]\n"
      "v_mul_f32 v182, v150, v182\n"
      "v_fmac_f32 v204, v157, v157\n"
      "v_mfma_f32_16x16x32_bf16 v[166:169], v[112:115], a[188:191], v[166:169]\n"
      "v_cvt_pk_bf16_f32 v219, v220, v221\n"
      "v_mul_f32 v175, v151, v175\n"
      "v_mfma_f32_16x16x32_bf16 v[160:163], v[116:119], a[192:195], 0\n"
      "ds_read_b128 v[220:223], v2 offset:6144\n"
      "v_mul_f32 v174, v151, v174\n"
      "v_mfma_f32_16x16x32_bf16 v[160:163], v[120:123], a[196:199], v[160:163]\n"
      "ds_read_b128 v[120:123], v1 offset:34816\n"
      "v_fmac_f32 v129, v111, v111\n"
      "v_mfma_f32_16x16x32_bf16 v[160:163], v[96:99], a[200:203], v[160:163]\n"
      "v_fmac_f32 v204, v158, v158\n"
      "v_fmac_f32 v204, v159, v159\n"
      "v_mfma_f32_16x16x32_bf16 v[160:163], v[104:107], a[204:207], v[160:163]\n"
      "ds_read_b128 v[116:119], v0 offset:34816\n"
      "v_cvt_pk_bf16_f32 v124, v124, v125\n"
      "v_mfma_f32_16x16x32_bf16 v[160:163], v[170:173], a[208:211], v[160:163]\n"
      "v_permlane16_swap_b32 v129, v204\n"
      "v_mul_f32 v185, v150, v185\n"
      "v_mfma_f32_16x16x32_bf16 v[160:163], v[112:115], a[212:215], v[160:163]\n"
      "s_waitcnt lgkmcnt(3)\n"
      "v_add_f32 v129, v129, v204\n"
      "v_mov_b32 v204, v129\n"
      "v_mfma_f32_16x16x32_bf16 v[170:173], a[0:3], v[192:195], 0\n"
      "s_nop 0\n"
      "v_permlane32_swap_b32 v129, v204\n"
      "v_mfma_f32_16x16x32_bf16 v[170:173], a[4:7], v[186:189], v[170:173]\n"
      "v_cvt_pk_bf16_f32 v152, v152, v153\n"
      "v_cvt_pk_bf16_f32 v125, v126, v127\n"
      "v_mfma_f32_16x16x32_bf16 v[170:173], a[8:11], v[200:203], v[170:173]\n"
      "v_add_f32 v129, v129, v204\n"
      "v_cvt_pk_bf16_f32 v198, v166, v167\n"
      "v_mfma_f32_16x16x32_bf16 v[170:173], a[12:15], v[134:137], v[170:173]\n"
      "s_waitcnt lgkmcnt(0)\n"
      "v_rsq_f32 v129, v129\n"
      "v_mfma_f32_16x16x32_bf16 v[170:173], a[16:19], v[116:119], v[170:173]\n"
      "v_mov_b32 v204, v129\n"
      "v_mul_f32 v183, v150, v183\n"
      "v_mfma_f32_16x16x32_bf16 v[170:173], a[20:23], v[120:123], v[170:173]\n"
      "v_mul_f32 v176, v151, v176\n"
      "v_cvt_pk_bf16_f32 v153, v154, v155\n"
      "v_mfma_f32_16x16x32_bf16 v[112:115], a[24:27], v[192:195], 0\n"
      "v_permlane16_swap_b32_e64 v129, v204\n"
      "v_mfma_f32_16x16x32_bf16 v[112:115], a[28:31], v[186:189], v[112:115]\n"
      "v_cvt_pk_bf16_f32 v127, v184, v185\n"
      "v_mul_f32 v129, s50, v129\n"
      "v_mfma_f32_16x16x32_bf16 v[112:115], a[32:35], v[200:203], v[112:115]\n"
      "v_mul_f32 v177, v151, v177\n"
      "v_mul_f32_e64 v157, v204, v157\n"
      "v_mfma_f32_16x16x32_bf16 v[112:115], a[36:39], v[134:137], v[112:115]\n"
      "v_mul_f32 v212, v129, v212\n"
      "v_fma_f32 v81, v170, v170, s51\n"
      "v_mfma_f32_16x16x32_bf16 v[112:115], a[40:43], v[116:119], v[112:115]\n"
      "v_mul_f32 v109, v129, v109\n"
      "v_cvt_pk_bf16_f32 v139, v140, v141\n"
      "v_mfma_f32_16x16x32_bf16 v[112:115], a[44:47], v[120:123], v[112:115]\n"
      "v_fmac_f32 v81, v171, v171\n"
      "v_fmac_f32 v81, v172, v172\n"
      "v_mfma_f32_16x16x32_bf16 v[104:107], a[48:51], v[192:195], 0\n"
      "v_cvt_pk_bf16_f32 v217, v162, v163\n"
      "v_mul_f32 v159, v204, v159\n"
      "v_mfma_f32_16x16x32_bf16 v[104:107], a[52:55], v[186:189], v[104:107]\n"
      "v_cvt_pk_bf16_f32 v216, v160, v161\n"
      "v_mul_f32 v179, v204, v179\n"
      "v_mfma_f32_16x16x32_bf16 v[104:107], a[56:59], v[200:203], v[104:107]\n"
      "v_mul_f32 v206, v204, v206\n"
      "v_mul_f32 v213, v129, v213\n"
      "v_mfma_f32_16x16x32_bf16 v[104:107], a[60:63], v[134:137], v[104:107]\n"
      "v_fmac_f32_e64 v81, v173, v173\n"
      "v_cvt_pk_bf16_f32 v154, v174, v175\n"
      "v_mfma_f32_16x16x32_bf16 v[104:107], a[64:67], v[116:119], v[104:107]\n"
      "v_mul_f32 v181, v204, v181\n"
      "v_mul_f32 v145, v129, v145\n"
      "v_mfma_f32_16x16x32_bf16 v[104:107], a[68:71], v[120:123], v[104:107]\n"
      "v_mul_f32 v207, v204, v207\n"
      "v_mul_f32 v211, v129, v211\n"
      "v_mfma_f32_16x16x32_bf16 v[162:165], a[72:75], v[192:195], 0\n"
      "v_cvt_pk_bf16_f32 v199, v168, v169\n"
      "v_fmac_f32_e64 v81, v112, v112\n"
      "v_mfma_f32_16x16x32_bf16 v[162:165], a[76:79], v[186:189], v[162:165]\n"
      "v_mul_f32 v180, v204, v180\n"
      "v_fmac_f32 v81, v113, v113\n"
      "v_mfma_f32_16x16x32_bf16 v[162:165], a[80:83], v[200:203], v[162:165]\n"
      "v_fmac_f32 v81, v114, v114\n"
      "v_mul_f32 v156, v204, v156\n"
      "v_mfma_f32_16x16x32_bf16 v[162:165], a[84:87], v[134:137], v[162:165]\n"
      "v_mul_f32 v208, v204, v208\n"
      "v_fmac_f32 v81, v115, v115\n"
      "v_mfma_f32_16x16x32_bf16 v[162:165], a[88:91], v[116:119], v[162:165]\n"
      "v_mul_f32 v158, v204, v158\n"
      "v_mul_f32 v144, v129, v144\n"
      "v_mfma_f32_16x16x32_bf16 v[162:165], a[92:95], v[120:123], v[162:165]\n"
      "v_mul_f32 v210, v129, v210\n"
      "v_cvt_pk_bf16_f32 v210, v210, v211\n"
      "v_mfma_f32_16x16x32_bf16 v[166:169], a[96:99], v[192:195], 0\n"
      "v_fmac_f32 v81, v104, v104\n"
      "v_mul_f32_e64 v111, v129, v111\n"
      "v_mfma_f32_16x16x32_bf16 v[166:169], a[100:103], v[186:189], v[166:169]\n"
      "v_cvt_pk_bf16_f32 v211, v212, v213\n"
      "v_cvt_pk_bf16_f32 v126, v182, v183\n"
      "v_mfma_f32_16x16x32_bf16 v[166:169], a[104:107], v[200:203], v[166:169]\n"
      "v_fma_f32 v84, v162, v162, s51\n"
      "v_fmac_f32 v81, v105, v105\n"
      "v_mfma_f32_16x16x32_bf16 v[166:169], a[108:111], v[134:137], v[166:169]\n"
      "v_fmac_f32 v84, v163, v163\n"
      "v_mul_f32 v178, v204, v178\n"
      "v_mfma_f32_16x16x32_bf16 v[166:169], a[112:115], v[116:119], v[166:169]\n"
      "v_mul_f32 v143, v129, v143\n"
      "v_mul_f32 v142, v129, v142\n"
      "v_mfma_f32_16x16x32_bf16 v[166:169], a[116:119], v[120:123], v[166:169]\n"
      "v_fmac_f32 v81, v106, v106\n"
      "v_fmac_f32_e64 v84, v164, v164\n"
      "v_mfma_f32_16x16x32_bf16 v[182:185], a[120:123], v[192:195], 0\n"
      "v_cvt_pk_bf16_f32 v178, v178, v179\n"
      "v_cvt_pk_bf16_f32 v156, v156, v157\n"
      "v_mfma_f32_16x16x32_bf16 v[182:185], a[124:127], v[186:189], v[182:185]\n"
      "v_cvt_pk_bf16_f32 v179, v180, v181\n"
      "v_cvt_pk_bf16_f32 v133, v148, v149\n"
      "v_mfma_f32_16x16x32_bf16 v[182:185], a[128:131], v[200:203], v[182:185]\n"
      "v_cvt_pk_bf16_f32 v155, v176, v177\n"
      "v_fmac_f32 v84, v165, v165\n"
      "v_mfma_f32_16x16x32_bf16 v[182:185], a[132:135], v[134:137], v[182:185]\n"
      "v_fmac_f32 v84, v166, v166\n"
      "v_fmac_f32_e64 v81, v107, v107\n"
      "v_mfma_f32_16x16x32_bf16 v[182:185], a[136:139], v[116:119], v[182:185]\n"
      "v_cvt_pk_bf16_f32 v213, v144, v145\n"
      "v_cvt_pk_bf16_f32 v132, v146, v147\n"
      "v_mfma_f32_16x16x32_bf16 v[182:185], a[140:143], v[120:123], v[182:185]\n"
      "v_fmac_f32 v84, v167, v167\n"
      "v_fmac_f32 v84, v168, v168\n"
      "v_mfma_f32_16x16x32_bf16 v[144:147], v[192:195], a[144:147], 0\n"
      "s_cmp_gt_u32 s42, s44\n"
      "s_cselect_b32 s95, s94, s95\n"
      "v_fmac_f32_e64 v84, v169, v169\n"
      "v_mfma_f32_16x16x32_bf16 v[144:147], v[186:189], a[148:151], v[144:147]\n"
      "s_nop 0\n"
      "v_fmac_f32 v84, v182, v182\n"
      "v_fmac_f32 v84, v183, v183\n"
      "v_mfma_f32_16x16x32_bf16 v[144:147], v[200:203], a[152:155], v[144:147]\n"
      "s_add_u32 s42, s42, 1\n"
      "v_fmac_f32 v84, v184, v184\n"
      "v_fmac_f32 v84, v185, v185\n"
      "v_mfma_f32_16x16x32_bf16 v[144:147], v[134:137], a[156:159], v[144:147]\n"
      "s_nop 0\n"
      "v_permlane16_swap_b32 v81, v84\n"
      "v_mfma_f32_16x16x32_bf16 v[144:147], v[116:119], a[160:163], v[144:147]\n"
      "v_add_f32 v81, v81, v84\n"
      "v_cvt_pk_bf16_f32 v157, v158, v159\n"
      "v_mfma_f32_16x16x32_bf16 v[144:147], v[120:123], a[164:167], v[144:147]\n"
      "v_cvt_pk_bf16_f32 v212, v142, v143\n"
      "v_mov_b32 v84, v81\n"
      "v_mfma_f32_16x16x32_bf16 v[148:151], v[192:195], a[168:171], 0\n"
      "s_nop 0\n"
      "v_permlane32_swap_b32 v81, v84\n"
      "v_mfma_f32_16x16x32_bf16 v[148:151], v[186:189], a[172:175], v[148:151]\n"
      "v_cvt_pk_bf16_f32 v180, v206, v207\n"
      "v_add_f32 v81, v81, v84\n"
      "v_mfma_f32_16x16x32_bf16 v[148:151], v[200:203], a[176:179], v[148:151]\n"
      "v_rsq_f32 v81, v81\n"
      "v_mfma_f32_16x16x32_bf16 v[148:151], v[134:137], a[180:183], v[148:151]\n"
      "ds_read_b128 v[174:177], v2\n"
      "v_mov_b32 v84, v81\n"
      "v_mfma_f32_16x16x32_bf16 v[148:151], v[116:119], a[184:187], v[148:151]\n"
      "s_nop 0\n"
      "v_permlane16_swap_b32 v81, v84\n"
      "v_mfma_f32_16x16x32_bf16 v[148:151], v[120:123], a[188:191], v[148:151]\n"
      "v_mul_f32 v162, v84, v162\n"
      "v_mul_f32_e64 v81, s50, v81\n"
      "v_mfma_f32_16x16x32_bf16 v[158:161], v[192:195], a[192:195], 0\n"
      "ds_read_b128 v[190:193], v2 offset:3072\n"
      "v_cvt_pk_bf16_f32 v77, v146, v147\n"
      "v_mfma_f32_16x16x32_bf16 v[158:161], v[186:189], a[196:199], v[158:161]\n"
      "ds_read_u16 v82, v11 offset:1200\n"
      "v_mul_f32_e64 v112, v81, v112\n"
      "v_mfma_f32_16x16x32_bf16 v[158:161], v[200:203], a[200:203], v[158:161]\n"
      "ds_read_b128 v[140:143], v2 offset:256\n"
      "v_mul_f32 v164, v84, v164\n"
      "v_mfma_f32_16x16x32_bf16 v[158:161], v[134:137], a[204:207], v[158:161]\n"
      "ds_read_b128 v[200:203], v2 offset:15360\n"
      "v_mul_f32 v108, v129, v108\n"
      "v_mfma_f32_16x16x32_bf16 v[158:161], v[116:119], a[208:211], v[158:161]\n"
      "v_mul_f32 v163, v84, v163\n"
      "v_mul_f32 v184, v84, v184\n"
      "v_mfma_f32_16x16x32_bf16 v[158:161], v[120:123], a[212:215], v[158:161]\n"
      "s_waitcnt lgkmcnt(4)\n"
      "v_mul_f32 v209, v204, v209\n"
      "v_mul_f32 v104, v81, v104\n"
      "v_mfma_f32_16x16x32_bf16 v[96:99], a[216:219], v[174:177], 0\n"
      "s_waitcnt lgkmcnt(3)\n"
      "v_mul_f32 v185, v84, v185\n"
      "v_mul_f32 v114, v81, v114\n"
      "v_mfma_f32_16x16x32_bf16 v[96:99], a[220:223], v[190:193], v[96:99]\n"
      "ds_read_b128 v[204:207], v2 offset:9216\n"
      "v_mul_f32_e64 v183, v84, v183\n"
      "v_mfma_f32_16x16x32_bf16 v[96:99], a[224:227], v[220:223], v[96:99]\n"
      "ds_read_b128 v[186:189], v2 offset:12288\n"
      "v_cvt_pk_bf16_f32 v181, v208, v209\n"
      "v_mfma_f32_16x16x32_bf16 v[116:119], a[240:243], v[174:177], 0\n"
      "v_mul_f32_e64 v165, v84, v165\n"
      "v_cvt_pk_bf16_f32 v162, v162, v163\n"
      "v_mfma_f32_16x16x32_bf16 v[116:119], a[244:247], v[190:193], v[116:119]\n"
      "v_mul_f32 v171, v81, v171\n"
      "v_mul_f32 v167, v84, v167\n"
      "v_mfma_f32_16x16x32_bf16 v[116:119], a[248:251], v[220:223], v[116:119]\n"
      "v_mul_f32 v168, v84, v168\n"
      "v_mul_f32 v182, v84, v182\n"
      "v_mfma_f32_16x16x32_bf16 v[134:137], v[232:235], v[174:177], 0\n"
      "v_mul_f32 v110, v129, v110\n"
      "v_mul_f32 v169, v84, v169\n"
      "v_mfma_f32_16x16x32_bf16 v[134:137], v[236:239], v[190:193], v[134:137]\n"
      "v_cvt_pk_bf16_f32 v108, v108, v109\n"
      "v_mul_f32 v170, v81, v170\n"
      "v_mfma_f32_16x16x32_bf16 v[134:137], v[240:243], v[220:223], v[134:137]\n"
      "s_waitcnt lgkmcnt(0)\n"
      "v_cvt_pk_bf16_f32 v182, v182, v183\n"
      "v_cvt_pk_bf16_f32 v163, v164, v165\n"
      "v_mfma_f32_16x16x32_bf16 v[96:99], a[228:231], v[204:207], v[96:99]\n"
      "ds_read_b128 v[220:223], v2 offset:3328\n"
      "v_cvt_pk_bf16_f32 v183, v184, v185\n"
      "v_mfma_f32_16x16x32_bf16 v[96:99], a[232:235], v[186:189], v[96:99]\n"
      "v_cvt_pk_bf16_f32 v165, v168, v169\n"
      "v_cvt_pk_bf16_f32 v85, v160, v161\n"
      "v_mfma_f32_16x16x32_bf16 v[96:99], a[236:239], v[200:203], v[96:99]\n"
      "ds_read_b128 v[174:177], v2 offset:9472\n"
      "v_mul_f32 v115, v81, v115\n"
      "v_mfma_f32_16x16x32_bf16 v[116:119], a[252:255], v[204:207], v[116:119]\n"
      "ds_read_b64 v[128:129], v10 offset:1192\n"
      "v_mul_f32 v173, v81, v173\n"
      "v_mfma_f32_16x16x32_bf16 v[116:119], v[224:227], v[186:189], v[116:119]\n"
      "ds_read_b128 v[120:123], v10 offset:1200\n"
      "v_mul_f32 v107, v81, v107\n"
      "v_mfma_f32_16x16x32_bf16 v[116:119], v[228:231], v[200:203], v[116:119]\n"
      "v_mul_f32 v166, v84, v166\n"
      "v_mul_f32_e64 v172, v81, v172\n"
      "v_mfma_f32_16x16x32_bf16 v[134:137], v[244:247], v[204:207], v[134:137]\n"
      "v_cvt_pk_bf16_f32 v84, v158, v159\n"
      "v_mul_f32_e64 v105, v81, v105\n"
      "v_mfma_f32_16x16x32_bf16 v[134:137], v[248:251], v[186:189], v[134:137]\n"
      "ds_read_u16 v78, v11 offset:1168\n"
      "v_cvt_pk_bf16_f32 v76, v144, v145\n"
      "v_mfma_f32_16x16x32_bf16 v[134:137], v[252:255], v[200:203], v[134:137]\n"
      "v_mul_f32_e64 v113, v81, v113\n"
      "v_cvt_pk_bf16_f32 v104, v104, v105\n"
      "v_mfma_f32_16x16x32_bf16 v[186:189], v[152:155], v[124:127], v[40:43]\n"
      "v_cvt_pk_bf16_f32 v164, v166, v167\n"
      "v_cvt_pk_bf16_f32 v170, v170, v171\n"
      "v_mfma_f32_16x16x32_bf16 v[144:147], v[178:181], v[124:127], v[44:47]\n"
      "s_waitcnt lgkmcnt(1)\n"
      "v_cvt_pk_bf16_f32 v80, v148, v149\n"
      "v_mul_f32 v106, v81, v106\n"
      "v_mfma_f32_16x16x32_bf16 v[166:169], v[162:165], v[124:127], v[48:51]\n"
      "s_add_u32 s43, s43, 1\n"
      "v_fma_f32 v184, v96, v96, s52\n"
      "v_mul_f32 v185, v97, v97\n"
      "v_mfma_f32_16x16x32_bf16 v[192:195], v[120:123], v[124:127], v[24:27]\n"
      "ds_read_b128 v[158:161], v2 offset:6400\n"
      "v_cvt_pk_bf16_f32 v171, v172, v173\n"
      "v_mfma_f32_16x16x16_bf16 v[186:189], v[218:219], v[138:139], v[186:189]\n"
      "v_fmac_f32 v184, v98, v98\n"
      "s_waitcnt vmcnt(1)\n"
      "v_or3_b32 v100, v100, v101, v102\n"
      "v_cvt_pk_bf16_f32 v173, v114, v115\n"
      "v_mfma_f32_16x16x16_bf16 v[144:147], v[156:157], v[138:139], v[144:147]\n"
      "v_cvt_pk_bf16_f32 v172, v112, v113\n"
      "v_fmac_f32 v185, v99, v99\n"
      "v_fmac_f32 v185, v117, v117\n"
      "v_mfma_f32_16x16x16_bf16 v[166:169], v[182:183], v[138:139], v[166:169]\n"
      "v_exp_f32_e64 v187, v187\n"
      "v_cvt_pk_bf16_f32 v81, v150, v151\n"
      "v_mfma_f32_16x16x16_bf16 v[192:195], v[128:129], v[138:139], v[192:195]\n"
      "v_cvt_pk_bf16_f32 v109, v110, v111\n"
      "v_exp_f32 v189, v189\n"
      "v_mfma_f32_16x16x32_bf16 v[200:203], a[216:219], v[140:143], 0\n"
      "v_exp_f32 v145, v145\n"
      "v_mfma_f32_16x16x32_bf16 v[200:203], a[220:223], v[220:223], v[200:203]\n"
      "s_waitcnt lgkmcnt(0)\n"
      "v_exp_f32 v147, v147\n"
      "v_mfma_f32_16x16x32_bf16 v[200:203], a[224:227], v[158:161], v[200:203]\n"
      "s_and_b32 s47, s47, -4\n"
      "v_exp_f32 v146, v146\n"
      "v_mfma_f32_16x16x32_bf16 v[112:115], a[240:243], v[140:143], 0\n"
      "v_exp_f32_e64 v188, v188\n"
      "v_mfma_f32_16x16x32_bf16 v[112:115], a[244:247], v[220:223], v[112:115]\n"
      "v_exp_f32_e64 v168, v168\n"
      "v_mfma_f32_16x16x32_bf16 v[112:115], a[248:251], v[158:161], v[112:115]\n"
      "v_exp_f32_e64 v144, v144\n"
      "v_mfma_f32_16x16x32_bf16 v[204:207], v[232:235], v[140:143], 0\n"
      "v_exp_f32_e64 v167, v167\n"
      "v_mfma_f32_16x16x32_bf16 v[204:207], v[236:239], v[220:223], v[204:207]\n"
      "v_exp_f32 v186, v186\n"
      "v_mfma_f32_16x16x32_bf16 v[204:207], v[240:243], v[158:161], v[204:207]\n"
      "ds_read_b128 v[158:161], v2 offset:12544\n"
      "s_waitcnt vmcnt(0)\n"
      "v_or3_b32 v100, v100, v103, v86\n"
      "v_mfma_f32_16x16x32_bf16 v[148:151], v[152:155], v[210:213], v[52:55]\n"
      "ds_read_u16 v86, v11 offset:1232\n"
      "v_cvt_pk_bf16_f32 v186, v186, v187\n"
      "v_mfma_f32_16x16x32_bf16 v[220:223], v[178:181], v[210:213], v[56:59]\n"
      "v_exp_f32_e64 v169, v169\n"
      "v_mfma_f32_16x16x32_bf16 v[124:127], v[162:165], v[210:213], v[60:63]\n"
      "v_cvt_pk_bf16_f32 v187, v188, v189\n"
      "v_cvt_pk_bf16_f32 v188, v144, v145\n"
      "v_mfma_f32_16x16x32_bf16 v[140:143], v[120:123], v[210:213], v[26:29]\n"
      "v_exp_f32 v192, v192\n"
      "v_mfma_f32_16x16x16_bf16 v[148:151], v[218:219], v[108:109], v[148:151]\n"
      "v_cvt_pk_bf16_f32 v189, v146, v147\n"
      "v_exp_f32 v166, v166\n"
      "v_mfma_f32_16x16x16_bf16 v[220:223], v[156:157], v[108:109], v[220:223]\n"
      "v_cvt_pk_bf16_f32 v105, v106, v107\n"
      "v_cvt_pk_bf16_f32 v90, v192, 0\n"
      "v_cvt_pk_bf16_f32 v88, v166, v167\n"
      "v_mfma_f32_16x16x16_bf16 v[124:127], v[182:183], v[108:109], v[124:127]\n"
      "v_cvt_pk_bf16_f32 v89, v168, v169\n"
      "v_exp_f32 v150, v150\n"
      "v_mfma_f32_16x16x16_bf16 v[140:143], v[128:129], v[108:109], v[140:143]\n"
      "v_exp_f32 v148, v148\n"
      "v_fmac_f32 v184, v116, v116\n"
      "v_mfma_f32_16x16x32_bf16 v[144:147], v[32:35], v[186:189], 0\n"
      "v_exp_f32 v223, v223\n"
      "v_mfma_f32_16x16x32_bf16 v[144:147], v[32:35], v[88:91], v[144:147]\n"
      "v_exp_f32_e64 v151, v151\n"
      "v_mfma_f32_16x16x32_bf16 v[166:169], v[130:133], v[186:189], 0\n"
      "v_exp_f32 v124, v124\n"
      "v_mfma_f32_16x16x32_bf16 v[166:169], v[76:79], v[88:91], v[166:169]\n"
      "v_exp_f32 v222, v222\n"
      "v_mfma_f32_16x16x32_bf16 v[210:213], v[196:199], v[186:189], 0\n"
      "v_exp_f32 v149, v149\n"
      "v_mfma_f32_16x16x32_bf16 v[210:213], v[80:83], v[88:91], v[210:213]\n"
      "v_exp_f32 v127, v127\n"
      "v_mfma_f32_16x16x32_bf16 v[190:193], v[214:217], v[186:189], 0\n"
      "s_waitcnt lgkmcnt(0)\n"
      "v_exp_f32 v125, v125\n"
      "v_mfma_f32_16x16x32_bf16 v[190:193], v[84:87], v[88:91], v[190:193]\n"
      "v_rcp_f32_e64 v144, v144\n"
      "v_cvt_pk_bf16_f32 v148, v148, v149\n"
      "v_mfma_f32_16x16x32_bf16 v[108:111], v[152:155], v[170:173], v[64:67]\n"
      "v_cvt_pk_bf16_f32 v92, v124, v125\n"
      "v_exp_f32_e64 v126, v126\n"
      "v_cvt_pk_bf16_f32 v149, v150, v151\n"
      "v_cvt_pk_bf16_f32 v151, v222, v223\n"
      "v_pk_mul_f32 v[210:211], v[210:211], v[144:145] op_sel_hi:[1,0]\n"
      "v_pk_mul_f32 v[168:169], v[168:169], v[144:145] op_sel_hi:[1,0]\n"
      "v_pk_mul_f32 v[190:191], v[190:191], v[144:145] op_sel_hi:[1,0]\n"
      "v_pk_mul_f32 v[166:167], v[166:167], v[144:145] op_sel_hi:[1,0]\n"
      "v_pk_mul_f32 v[212:213], v[212:213], v[144:145] op_sel_hi:[1,0]\n"
      "v_pk_mul_f32 v[192:193], v[192:193], v[144:145] op_sel_hi:[1,0]\n"
      "v_mfma_f32_16x16x32_bf16 v[186:189], v[178:181], v[170:173], v[68:71]\n"
      "ds_write_b32 v31, v100 offset:5632\n"
      "v_mfma_f32_16x16x32_bf16 v[144:147], v[162:165], v[170:173], v[72:75]\n"
      "v_cvt_pk_bf16_f32 v93, v126, v127\n"
      "v_cvt_pk_bf16_f32 v166, v166, v167\n"
      "v_mfma_f32_16x16x32_bf16 v[124:127], v[120:123], v[170:173], v[28:31]\n"
      "ds_read_b128 v[100:103], v2 offset:15616\n"
      "v_cvt_pk_bf16_f32 v167, v168, v169\n"
      "v_mfma_f32_16x16x16_bf16 v[108:111], v[218:219], v[104:105], v[108:111]\n"
      "v_exp_f32_e64 v220, v220\n"
      "v_cvt_pk_bf16_f32 v168, v210, v211\n"
      "v_mfma_f32_16x16x16_bf16 v[186:189], v[156:157], v[104:105], v[186:189]\n"
      "v_cvt_pk_bf16_f32 v169, v212, v213\n"
      "v_exp_f32_e64 v221, v221\n"
      "v_mfma_f32_16x16x16_bf16 v[144:147], v[182:183], v[104:105], v[144:147]\n"
      "v_cvt_pk_bf16_f32 v150, v220, v221\n"
      "v_exp_f32 v140, v140\n"
      "v_mfma_f32_16x16x16_bf16 v[124:127], v[128:129], v[104:105], v[124:127]\n"
      "v_exp_f32 v108, v108\n"
      "v_cvt_pk_bf16_f32 v190, v190, v191\n"
      "v_mfma_f32_16x16x32_bf16 v[200:203], a[228:231], v[174:177], v[200:203]\n"
      "v_exp_f32 v186, v186\n"
      "v_mfma_f32_16x16x32_bf16 v[200:203], a[232:235], v[158:161], v[200:203]\n"
      "s_waitcnt lgkmcnt(0)\n"
      "v_exp_f32 v109, v109\n"
      "v_mfma_f32_16x16x32_bf16 v[200:203], a[236:239], v[100:103], v[200:203]\n"
      "v_exp_f32 v146, v146\n"
      "v_mfma_f32_16x16x32_bf16 v[112:115], a[252:255], v[174:177], v[112:115]\n"
      "v_exp_f32_e64 v144, v144\n"
      "v_mfma_f32_16x16x32_bf16 v[112:115], v[224:227], v[158:161], v[112:115]\n"
      "v_exp_f32 v147, v147\n"
      "v_mfma_f32_16x16x32_bf16 v[112:115], v[228:231], v[100:103], v[112:115]\n"
      "v_exp_f32 v145, v145\n"
      "v_mfma_f32_16x16x32_bf16 v[204:207], v[244:247], v[174:177], v[204:207]\n"
      "ds_read_b128 v[174:177], v7\n"
      "v_cvt_pk_bf16_f32 v108, v108, v109\n"
      "v_mfma_f32_16x16x32_bf16 v[204:207], v[248:251], v[158:161], v[204:207]\n"
      "v_exp_f32_e64 v110, v110\n"
      "v_mfma_f32_16x16x32_bf16 v[204:207], v[252:255], v[100:103], v[204:207]\n"
      "ds_read_b64 v[126:127], v10 offset:1184\n"
      "v_cvt_pk_bf16_f32 v94, v140, 0\n"
      "v_mfma_f32_16x16x32_bf16 v[158:161], v[32:35], v[148:151], 0\n"
      "v_exp_f32_e64 v187, v187\n"
      "v_mfma_f32_16x16x32_bf16 v[158:161], v[32:35], v[92:95], v[158:161]\n"
      "ds_read_b128 v[208:211], v10 offset:1168\n"
      "v_cvt_pk_bf16_f32 v88, v144, v145\n"
      "v_cvt_pk_bf16_f32 v89, v146, v147\n"
      "v_mfma_f32_16x16x32_bf16 v[170:173], v[130:133], v[148:151], 0\n"
      "v_exp_f32_e64 v124, v124\n"
      "v_mfma_f32_16x16x32_bf16 v[170:173], v[76:79], v[92:95], v[170:173]\n"
      "ds_write_b128 v3, v[166:169] offset:18432\n"
      "v_mfma_f32_16x16x32_bf16 v[144:147], v[196:199], v[148:151], 0\n"
      "v_cvt_pk_bf16_f32 v191, v192, v193\n"
      "v_fma_f32 v138, v200, v200, s52\n"
      "v_mfma_f32_16x16x32_bf16 v[144:147], v[80:83], v[92:95], v[144:147]\n"
      "ds_read_b128 v[166:169], v7 offset:4096\n"
      "v_mul_f32 v139, v201, v201\n"
      "v_mfma_f32_16x16x32_bf16 v[100:103], v[214:217], v[148:151], 0\n"
      "s_waitcnt lgkmcnt(2)\n"
      "v_rcp_f32_e64 v158, v158\n"
      "v_mfma_f32_16x16x32_bf16 v[100:103], v[84:87], v[92:95], v[100:103]\n"
      "buffer_store_dwordx4 v[174:177], v20, s[24:27], s85 offen sc1\n"
      "v_pk_fma_f32 v[138:139], v[202:203], v[202:203], v[138:139]\n"
      "v_pk_fma_f32 v[138:139], v[112:113], v[112:113], v[138:139]\n"
      "v_pk_mul_f32 v[170:171], v[170:171], v[158:159] op_sel_hi:[1,0]\n"
      "v_pk_mul_f32 v[172:173], v[172:173], v[158:159] op_sel_hi:[1,0]\n"
      "v_pk_mul_f32 v[144:145], v[144:145], v[158:159] op_sel_hi:[1,0]\n"
      "v_pk_fma_f32 v[184:185], v[118:119], v[118:119], v[184:185]\n"
      "v_pk_fma_f32 v[184:185], v[134:135], v[134:135], v[184:185]\n"
      "v_mfma_f32_16x16x32_bf16 v[174:177], v[208:211], v[152:155], v[30:33]\n"
      "ds_read_b128 v[104:107], v2 offset:512\n"
      "v_cvt_pk_bf16_f32 v170, v170, v171\n"
      "v_mfma_f32_16x16x32_bf16 v[148:151], v[208:211], v[178:181], v[30:33]\n"
      "s_waitcnt lgkmcnt(1)\n"
      "v_exp_f32 v111, v111\n"
      "v_mfma_f32_16x16x32_bf16 v[140:143], v[208:211], v[162:165], v[30:33]\n"
      "ds_read_b128 v[220:223], v2 offset:3584\n"
      "v_cvt_pk_bf16_f32 v171, v172, v173\n"
      "v_mfma_f32_16x16x32_bf16 v[192:195], v[208:211], v[120:123], v[30:33]\n"
      "ds_read_b128 v[208:211], v2 offset:6656\n"
      "v_cvt_pk_bf16_f32 v172, v144, v145\n"
      "v_mfma_f32_16x16x16_bf16 v[174:177], v[126:127], v[218:219], v[174:177]\n"
      "v_cvt_pk_bf16_f32 v109, v110, v111\n"
      "v_exp_f32_e64 v189, v189\n"
      "v_mfma_f32_16x16x16_bf16 v[148:151], v[126:127], v[156:157], v[148:151]\n"
      "ds_read_b128 v[178:181], v7 offset:8192\n"
      "v_cvt_pk_bf16_f32 v110, v186, v187\n"
      "v_mfma_f32_16x16x16_bf16 v[140:143], v[126:127], v[182:183], v[140:143]\n"
      "buffer_store_dwordx4 v[166:169], v21, s[24:27], s85 offen sc1\n"
      "v_pk_mul_f32 v[100:101], v[100:101], v[158:159] op_sel_hi:[1,0]\n"
      "v_pk_fma_f32 v[138:139], v[114:115], v[114:115], v[138:139]\n"
      "v_pk_fma_f32 v[138:139], v[204:205], v[204:205], v[138:139]\n"
      "v_pk_mul_f32 v[146:147], v[146:147], v[158:159] op_sel_hi:[1,0]\n"
      "v_pk_mul_f32 v[102:103], v[102:103], v[158:159] op_sel_hi:[1,0]\n"
      "v_mfma_f32_16x16x16_bf16 v[192:195], v[126:127], v[128:129], v[192:195]\n"
      "v_exp_f32_e64 v188, v188\n"
      "s_waitcnt lgkmcnt(1)\n"
      "v_mfma_f32_16x16x32_bf16 v[156:159], a[216:219], v[104:107], 0\n"
      "v_cndmask_b32 v174, v174, v148, s[58:59]\n"
      "v_cndmask_b32 v174, v174, v140, s[60:61]\n"
      "v_mfma_f32_16x16x32_bf16 v[156:159], a[220:223], v[220:223], v[156:159]\n"
      "ds_read_b128 v[166:169], v7 offset:12288\n"
      "v_cvt_pk_bf16_f32 v173, v146, v147\n"
      "v_mfma_f32_16x16x32_bf16 v[156:159], a[224:227], v[208:211], v[156:159]\n"
      "ds_write_b128 v3, v[170:173] offset:18688\n"
      "v_pk_fma_f32 v[138:139], v[206:207], v[206:207], v[138:139]\n"
      "v_mfma_f32_16x16x32_bf16 v[146:149], a[240:243], v[104:107], 0\n"
      "s_waitcnt lgkmcnt(2)\n"
      "v_cvt_pk_bf16_f32 v100, v100, v101\n"
      "v_cvt_pk_bf16_f32 v101, v102, v103\n"
      "v_mfma_f32_16x16x32_bf16 v[146:149], a[244:247], v[220:223], v[146:149]\n"
      "buffer_store_dwordx4 v[178:181], v22, s[24:27], s85 offen sc1\n"
      "v_pk_fma_f32 v[184:185], v[136:137], v[136:137], v[184:185]\n"
      "v_mfma_f32_16x16x32_bf16 v[146:149], a[248:251], v[208:211], v[146:149]\n"
      "ds_write_b64 v4, v[190:191] offset:18432\n"
      "v_mfma_f32_16x16x32_bf16 v[160:163], v[232:235], v[104:107], 0\n"
      "ds_read_b128 v[126:129], v2 offset:12800\n"
      "v_cndmask_b32 v174, v174, v192, s[62:63]\n"
      "v_mfma_f32_16x16x32_bf16 v[160:163], v[236:239], v[220:223], v[160:163]\n"
      "ds_read_b128 v[150:153], v2 offset:9728\n"
      "v_cvt_pk_bf16_f32 v111, v188, v189\n"
      "v_mfma_f32_16x16x32_bf16 v[160:163], v[240:243], v[208:211], v[160:163]\n"
      "ds_read_b128 v[142:145], v7 offset:16384\n"
      "v_cvt_pk_bf16_f32 v90, v124, 0\n"
      "v_mfma_f32_16x16x32_bf16 v[208:211], v[32:35], v[108:111], 0\n"
      "ds_read_b128 v[170:173], v2 offset:15872\n"
      "v_add_f32 v138, v138, v139\n"
      "v_mfma_f32_16x16x32_bf16 v[208:211], v[32:35], v[88:91], v[208:211]\n"
      "v_exp_f32 v174, v174\n"
      "v_mfma_f32_16x16x32_bf16 v[190:193], v[130:133], v[108:111], 0\n"
      "s_waitcnt lgkmcnt(5)\n"
      "v_cvt_pk_bf16_f32 v174, v174, v174\n"
      "v_add_f32 v184, v184, v185\n"
      "v_mfma_f32_16x16x32_bf16 v[190:193], v[76:79], v[88:91], v[190:193]\n"
      "ds_write_b16 v13, v174\n"
      "v_mfma_f32_16x16x32_bf16 v[102:105], v[196:199], v[108:111], 0\n"
      "buffer_store_dwordx4 v[166:169], v23, s[24:27], s85 offen sc1\n"
      "v_mfma_f32_16x16x32_bf16 v[102:105], v[80:83], v[88:91], v[102:105]\n"
      "s_waitcnt lgkmcnt(2)\n"
      "v_rcp_f32 v208, v208\n"
      "v_mfma_f32_16x16x32_bf16 v[166:169], v[214:217], v[108:111], 0\n"
      "ds_write_b64 v4, v[100:101] offset:18688\n"
      "v_mfma_f32_16x16x32_bf16 v[166:169], v[84:87], v[88:91], v[166:169]\n"
      "ds_read_b128 v[174:177], v14 offset:64\n"
      "v_mul_f32 v190, v208, v190\n"
      "v_mfma_f32_16x16x32_bf16 v[156:159], a[228:231], v[150:153], v[156:159]\n"
      "s_waitcnt lgkmcnt(3)\n"
      "v_mul_f32 v191, v208, v191\n"
      "v_mul_f32 v102, v208, v102\n"
      "v_mfma_f32_16x16x32_bf16 v[156:159], a[232:235], v[126:129], v[156:159]\n"
      "ds_read_b128 v[218:221], v14\n"
      "v_cvt_pk_bf16_f32 v190, v190, v191\n"
      "v_mfma_f32_16x16x32_bf16 v[156:159], a[236:239], v[170:173], v[156:159]\n"
      "v_mul_f32 v104, v208, v104\n"
      "v_mul_f32 v193, v208, v193\n"
      "v_mfma_f32_16x16x32_bf16 v[146:149], a[252:255], v[150:153], v[146:149]\n"
      "v_permlane16_swap_b32 v184, v138\n"
      "v_mfma_f32_16x16x32_bf16 v[146:149], v[224:227], v[126:129], v[146:149]\n"
      "v_mul_f32 v192, v208, v192\n"
      "v_mul_f32_e64 v103, v208, v103\n"
      "v_mfma_f32_16x16x32_bf16 v[146:149], v[228:231], v[170:173], v[146:149]\n"
      "v_cvt_pk_bf16_f32 v191, v192, v193\n"
      "v_mul_f32_e64 v181, v157, v157\n"
      "v_mfma_f32_16x16x32_bf16 v[160:163], v[244:247], v[150:153], v[160:163]\n"
      "v_mul_f32 v105, v208, v105\n"
      "v_fma_f32 v180, v156, v156, s52\n"
      "v_mfma_f32_16x16x32_bf16 v[160:163], v[248:251], v[126:129], v[160:163]\n"
      "s_waitcnt lgkmcnt(0)\n"
      "v_cvt_pk_bf16_f32 v193, v104, v105\n"
      "v_cvt_pk_bf16_f32 v192, v102, v103\n"
      "v_mfma_f32_16x16x32_bf16 v[160:163], v[252:255], v[170:173], v[160:163]\n"
      "ds_write_b128 v3, v[190:193] offset:18944\n"
      "v_pk_mul_f32 v[168:169], v[168:169], v[208:209] op_sel_hi:[1,0]\n"
      "v_pk_mul_f32 v[166:167], v[166:167], v[208:209] op_sel_hi:[1,0]\n"
      "v_pk_fma_f32 v[180:181], v[158:159], v[158:159], v[180:181]\n"
      "v_cvt_pk_bf16_f32 v166, v166, v167\n"
      "v_add_f32_e64 v184, v184, v138\n"
      "v_pk_fma_f32 v[180:181], v[146:147], v[146:147], v[180:181]\n"
      "v_pk_fma_f32 v[180:181], v[148:149], v[148:149], v[180:181]\n"
      "v_cvt_pk_bf16_f32 v167, v168, v169\n"
      "v_pk_fma_f32 v[180:181], v[160:161], v[160:161], v[180:181]\n"
      "v_pk_fma_f32 v[180:181], v[162:163], v[162:163], v[180:181]\n"
      "v_add_f32_e64 v180, v180, v181\n"
      "v_mov_b32_e64 v138, v180\n"
      "v_mfma_f32_16x16x32_bf16 v[208:211], v[218:221], v[32:35], 0\n"
      "ds_write_b64 v4, v[166:167] offset:18944\n"
      "v_permlane16_swap_b32 v180, v138\n"
      "v_add_f32_e64 v180, v180, v138\n"
      "s_nop 1\n"
      "v_permlane32_swap_b32_e64 v184, v180\n"
      "v_add_f32 v184, v184, v180\n"
      "v_mfma_f32_16x16x32_bf16 v[208:211], v[174:177], v[32:35], v[208:211]\n"
      "ds_write_b32 v31, v184 offset:1024\n"
      "buffer_store_dwordx4 v[142:145], v25, s[24:27], s85 offen sc1\n"
      "s_cmp_ge_u32 s95, 0x40000000\n"
      "s_cselect_b64 vcc, -1, s[56:57]\n"
      "v_add_u32 v10, 2336, v10\n"
      "v_add_u32 v11, 2336, v11\n"
      "s_waitcnt lgkmcnt(0)\n"
      "s_barrier\n"
      "s_cbranch_vccnz L_disp0_%=\n"
      "L_loop0_%=:\n"
      "v_mfma_f32_16x16x32_bf16 v[168:171], v[218:221], v[130:133], 0\n"
      "ds_read_b128 v[142:145], v0\n"
      "ds_read_b128 v[178:181], v9 offset:1024\n"
      "v_mfma_f32_16x16x32_bf16 v[168:171], v[174:177], v[76:79], v[168:171]\n"
      "ds_read_b128 v[182:185], v1\n"
      "v_mfma_f32_16x16x32_bf16 v[150:153], v[218:221], v[196:199], 0\n"
      "ds_read_b128 v[138:141], v9 offset:5632\n"
      "s_add_u32 m0, s64, 18432\n"
      "v_mfma_f32_16x16x32_bf16 v[150:153], v[174:177], v[80:83], v[150:153]\n"
      "ds_read_b128 v[126:129], v0 offset:6144\n"
      "v_readlane_b32 s85, v37, s42\n"
      "v_mfma_f32_16x16x32_bf16 v[194:197], v[218:221], v[214:217], 0\n"
      "buffer_load_dwordx4 v16, s[16:19], s45 offen  lds\n"
      "v_mfma_f32_16x16x32_bf16 v[194:197], v[174:177], v[84:87], v[194:197]\n"
      "ds_read_b128 v[218:221], v1 offset:12288\n"
      "s_waitcnt lgkmcnt(5)\n"
      "v_mfma_f32_16x16x32_bf16 v[174:177], a[0:3], v[142:145], 0\n"
      "ds_read_b128 v[170:173], v0 offset:12288\n"
      "v_readlane_b32 s84, v37, s43\n"
      "v_mfma_f32_16x16x32_bf16 v[100:103], a[24:27], v[142:145], 0\n"
      "s_waitcnt lgkmcnt(5)\n"
      "v_readlane_b32 s86, v38, s43\n"
      "v_add_f32 v178, v178, v179\n"
      "v_mfma_f32_16x16x32_bf16 v[190:193], a[48:51], v[142:145], 0\n"
      "s_add_u32 m0, s64, 22528\n"
      "v_add_f32 v180, v180, v181\n"
      "v_add_f32 v178, v178, v180\n"
      "v_mfma_f32_16x16x32_bf16 v[164:167], a[72:75], v[142:145], 0\n"
      "v_rsq_f32 v178, v178\n"
      "v_mfma_f32_16x16x32_bf16 v[120:123], a[96:99], v[142:145], 0\n"
      "buffer_load_dwordx4 v17, s[16:19], s45 offen  lds\n"
      "v_mfma_f32_16x16x32_bf16 v[186:189], a[120:123], v[142:145], 0\n"
      "s_waitcnt lgkmcnt(1)\n"
      "v_mul_f32 v178, 0x415db3d7, v178\n"
      "v_mov_b32 v180, v178\n"
      "v_mfma_f32_16x16x32_bf16 v[174:177], a[4:7], v[182:185], v[174:177]\n"
      "s_add_u32 m0, s64, 26624\n"
      "v_permlane32_swap_b32 v178, v180\n"
      "v_mfma_f32_16x16x32_bf16 v[174:177], a[8:11], v[126:129], v[174:177]\n"
      "ds_read_b128 v[210:213], v1 offset:6144\n"
      "v_mov_b32 v179, v178\n"
      "v_mfma_f32_16x16x32_bf16 v[100:103], a[28:31], v[182:185], v[100:103]\n"
      "s_nop 0\n"
      "v_permlane16_swap_b32 v178, v179\n"
      "v_mfma_f32_16x16x32_bf16 v[100:103], a[32:35], v[126:129], v[100:103]\n"
      "buffer_load_dwordx4 v18, s[16:19], s45 offen  lds\n"
      "v_mfma_f32_16x16x32_bf16 v[190:193], a[52:55], v[182:185], v[190:193]\n"
      "s_add_u32 m0, s64, 30720\n"
      "v_cndmask_b32 v168, v168, v150, s[58:59]\n"
      "v_mul_f32 v119, v178, v119\n"
      "v_mfma_f32_16x16x32_bf16 v[190:193], a[56:59], v[126:129], v[190:193]\n"
      "v_mul_f32 v99, v178, v99\n"
      "v_mul_f32 v162, v180, v162\n"
      "v_mfma_f32_16x16x32_bf16 v[164:167], a[76:79], v[182:185], v[164:167]\n"
      "v_mul_f32 v117, v178, v117\n"
      "v_mov_b32_dpp v208, v208 quad_perm:[0,0,0,0] row_mask:0xf bank_mask:0xf\n"
      "v_mfma_f32_16x16x32_bf16 v[164:167], a[80:83], v[126:129], v[164:167]\n"
      "v_mul_f32 v97, v178, v97\n"
      "v_mul_f32 v96, v178, v96\n"
      "v_mfma_f32_16x16x32_bf16 v[120:123], a[100:103], v[182:185], v[120:123]\n"
      "buffer_load_dwordx4 v19, s[16:19], s45 offen  lds\n"
      "v_mfma_f32_16x16x32_bf16 v[120:123], a[104:107], v[126:129], v[120:123]\n"
      "v_cndmask_b32 v168, v168, v194, s[60:61]\n"
      "v_mul_f32 v203, v179, v203\n"
      "v_mfma_f32_16x16x32_bf16 v[186:189], a[124:127], v[182:185], v[186:189]\n"
      "v_mul_f32 v112, v179, v112\n"
      "v_mul_f32 v98, v178, v98\n"
      "v_mfma_f32_16x16x32_bf16 v[186:189], a[128:131], v[126:129], v[186:189]\n"
      "s_waitcnt lgkmcnt(0)\n"
      "v_mul_f32 v147, v180, v147\n"
      "v_mul_f32 v113, v179, v113\n"
      "v_mfma_f32_16x16x32_bf16 v[174:177], a[12:15], v[210:213], v[174:177]\n"
      "s_add_u32 m0, s65, 34816\n"
      "v_mul_f32 v206, v179, v206\n"
      "v_mul_f32 v201, v179, v201\n"
      "v_mfma_f32_16x16x32_bf16 v[174:177], a[16:19], v[170:173], v[174:177]\n"
      "buffer_load_dwordx4 v15, s[16:19], s45 offen  lds\n"
      "v_mfma_f32_16x16x32_bf16 v[174:177], a[20:23], v[218:221], v[174:177]\n"
      "v_mul_f32 v115, v179, v115\n"
      "v_mul_f32 v205, v179, v205\n"
      "v_mfma_f32_16x16x32_bf16 v[100:103], a[36:39], v[210:213], v[100:103]\n"
      "s_or_b32 s95, s84, s85\n"
      "v_mul_f32 v118, v178, v118\n"
      "v_mul_f32 v158, v180, v158\n"
      "v_mfma_f32_16x16x32_bf16 v[100:103], a[40:43], v[170:173], v[100:103]\n"
      "v_cvt_pk_bf16_f32 v96, v96, v97\n"
      "v_mul_f32 v116, v178, v116\n"
      "v_mfma_f32_16x16x32_bf16 v[100:103], a[44:47], v[218:221], v[100:103]\n"
      "v_rcp_f32_e64 v208, v208\n"
      "v_mfma_f32_16x16x32_bf16 v[190:193], a[60:63], v[210:213], v[190:193]\n"
      "v_cvt_pk_bf16_f32 v97, v98, v99\n"
      "v_cvt_pk_bf16_f32 v98, v116, v117\n"
      "v_mfma_f32_16x16x32_bf16 v[190:193], a[64:67], v[170:173], v[190:193]\n"
      "ds_read_b128 v[104:107], v1 offset:2048\n"
      "v_cvt_pk_bf16_f32 v99, v118, v119\n"
      "v_mfma_f32_16x16x32_bf16 v[190:193], a[68:71], v[218:221], v[190:193]\n"
      "buffer_load_dwordx4 v[108:111], v36, s[28:31], s47 offen\n"
      "v_mfma_f32_16x16x32_bf16 v[164:167], a[84:87], v[210:213], v[164:167]\n"
      "ds_write_b128 v5, v[96:99]\n"
      "v_mfma_f32_16x16x32_bf16 v[164:167], a[88:91], v[170:173], v[164:167]\n"
      "v_mul_f32 v148, v180, v148\n"
      "v_mul_f32 v157, v180, v157\n"
      "v_mfma_f32_16x16x32_bf16 v[164:167], a[92:95], v[218:221], v[164:167]\n"
      "v_mul_f32_e64 v200, v179, v200\n"
      "v_or3_b32 v138, v138, v139, v140\n"
      "v_mfma_f32_16x16x32_bf16 v[120:123], a[108:111], v[210:213], v[120:123]\n"
      "buffer_load_dword v90, v29, s[28:31], s47 offen\n"
      "v_mfma_f32_16x16x32_bf16 v[120:123], a[112:115], v[170:173], v[120:123]\n"
      "v_mul_f32_e64 v161, v180, v161\n"
      "v_fma_f32 v78, v174, v174, s51\n"
      "v_mfma_f32_16x16x32_bf16 v[120:123], a[116:119], v[218:221], v[120:123]\n"
      "v_fmac_f32 v78, v175, v175\n"
      "v_mul_f32 v168, v208, v168\n"
      "v_mfma_f32_16x16x32_bf16 v[186:189], a[132:135], v[210:213], v[186:189]\n"
      "s_or_b32 s95, s95, s48\n"
      "v_mul_f32 v159, v180, v159\n"
      "v_mul_f32 v160, v180, v160\n"
      "v_mfma_f32_16x16x32_bf16 v[186:189], a[136:139], v[170:173], v[186:189]\n"
      "v_fmac_f32 v78, v176, v176\n"
      "v_fma_f32 v194, v164, v164, s51\n"
      "v_mfma_f32_16x16x32_bf16 v[186:189], a[140:143], v[218:221], v[186:189]\n"
      "ds_read_b128 v[116:119], v1 offset:8192\n"
      "v_fmac_f32_e64 v194, v165, v165\n"
      "v_mfma_f32_16x16x32_bf16 v[130:133], v[142:145], a[144:147], 0\n"
      "v_cvt_pk_bf16_f32 v168, v168, v168\n"
      "v_bitop3_b32 v138, v138, v141, s33 bitop3:0xa8\n"
      "v_mfma_f32_16x16x32_bf16 v[130:133], v[182:185], a[148:151], v[130:133]\n"
      "v_fmac_f32 v78, v177, v177\n"
      "v_fmac_f32 v194, v166, v166\n"
      "v_mfma_f32_16x16x32_bf16 v[130:133], v[126:129], a[152:155], v[130:133]\n"
      "ds_read_b128 v[150:153], v0 offset:8192\n"
      "v_fmac_f32 v194, v167, v167\n"
      "v_mfma_f32_16x16x32_bf16 v[130:133], v[210:213], a[156:159], v[130:133]\n"
      "v_mul_f32 v135, v178, v135\n"
      "v_mul_f32 v202, v179, v202\n"
      "v_mfma_f32_16x16x32_bf16 v[130:133], v[170:173], a[160:163], v[130:133]\n"
      "v_cvt_pk_bf16_f32 v200, v200, v201\n"
      "v_fmac_f32 v78, v100, v100\n"
      "v_mfma_f32_16x16x32_bf16 v[130:133], v[218:221], a[164:167], v[130:133]\n"
      "v_mul_f32 v207, v179, v207\n"
      "v_fmac_f32 v194, v120, v120\n"
      "v_mfma_f32_16x16x32_bf16 v[196:199], v[142:145], a[168:171], 0\n"
      "v_fmac_f32_e64 v78, v101, v101\n"
      "v_cmp_ne_u32 s[56:57], 0, v138\n"
      "v_mfma_f32_16x16x32_bf16 v[196:199], v[182:185], a[172:175], v[196:199]\n"
      "ds_write_b16 v12, v168 offset:16\n"
      "v_mfma_f32_16x16x32_bf16 v[196:199], v[126:129], a[176:179], v[196:199]\n"
      "v_fmac_f32 v78, v102, v102\n"
      "v_mul_f32 v204, v179, v204\n"
      "v_mfma_f32_16x16x32_bf16 v[196:199], v[210:213], a[180:183], v[196:199]\n"
      "ds_read_b128 v[138:141], v0 offset:2048\n"
      "v_fmac_f32 v78, v103, v103\n"
      "v_mfma_f32_16x16x32_bf16 v[196:199], v[170:173], a[184:187], v[196:199]\n"
      "v_fmac_f32 v78, v190, v190\n"
      "v_cvt_pk_bf16_f32 v201, v202, v203\n"
      "v_mfma_f32_16x16x32_bf16 v[196:199], v[218:221], a[188:191], v[196:199]\n"
      "v_mul_f32 v114, v179, v114\n"
      "v_fmac_f32 v194, v121, v121\n"
      "v_mfma_f32_16x16x32_bf16 v[214:217], v[142:145], a[192:195], 0\n"
      "s_cmp_eq_u32 s43, 21\n"
      "s_cselect_b32 s95, -1, s95\n"
      "v_fmac_f32_e64 v194, v122, v122\n"
      "v_mfma_f32_16x16x32_bf16 v[214:217], v[182:185], a[196:199], v[214:217]\n"
      "v_cvt_pk_bf16_f32 v204, v204, v205\n"
      "v_cvt_pk_bf16_f32 v205, v206, v207\n"
      "v_mfma_f32_16x16x32_bf16 v[214:217], v[126:129], a[200:203], v[214:217]\n"
      "ds_read_b128 v[126:129], v0 offset:14336\n"
      "v_mul_f32_e64 v156, v180, v156\n"
      "v_mfma_f32_16x16x32_bf16 v[214:217], v[210:213], a[204:207], v[214:217]\n"
      "v_fmac_f32 v78, v191, v191\n"
      "v_fmac_f32 v194, v123, v123\n"
      "v_mfma_f32_16x16x32_bf16 v[214:217], v[170:173], a[208:211], v[214:217]\n"
      "ds_read_b128 v[210:213], v1 offset:14336\n"
      "v_cvt_pk_bf16_f32 v156, v156, v157\n"
      "v_mfma_f32_16x16x32_bf16 v[214:217], v[218:221], a[212:215], v[214:217]\n"
      "s_waitcnt lgkmcnt(2)\n"
      "v_cvt_pk_bf16_f32 v157, v158, v159\n"
      "v_fmac_f32 v78, v192, v192\n"
      "v_mfma_f32_16x16x32_bf16 v[142:145], a[0:3], v[138:141], 0\n"
      "v_fmac_f32 v194, v186, v186\n"
      "v_mul_f32 v146, v180, v146\n"
      "v_mfma_f32_16x16x32_bf16 v[142:145], a[4:7], v[104:107], v[142:145]\n"
      "v_cvt_pk_bf16_f32 v130, v130, v131\n"
      "v_mul_f32 v163, v180, v163\n"
      "v_mfma_f32_16x16x32_bf16 v[142:145], a[8:11], v[150:153], v[142:145]\n"
      "v_fmac_f32 v194, v187, v187\n"
      "v_cvt_pk_bf16_f32 v196, v196, v197\n"
      "v_mfma_f32_16x16x32_bf16 v[142:145], a[12:15], v[116:119], v[142:145]\n"
      "s_waitcnt lgkmcnt(1)\n"
      "v_mul_f32 v136, v178, v136\n"
      "v_fmac_f32 v78, v193, v193\n"
      "v_mfma_f32_16x16x32_bf16 v[142:145], a[16:19], v[126:129], v[142:145]\n"
      "s_waitcnt lgkmcnt(0)\n"
      "v_mul_f32_e64 v137, v178, v137\n"
      "v_cvt_pk_bf16_f32 v131, v132, v133\n"
      "v_mfma_f32_16x16x32_bf16 v[142:145], a[20:23], v[210:213], v[142:145]\n"
      "v_cvt_pk_bf16_f32 v214, v214, v215\n"
      "v_mul_f32_e64 v134, v178, v134\n"
      "v_mfma_f32_16x16x32_bf16 v[182:185], a[24:27], v[138:141], 0\n"
      "v_cvt_pk_bf16_f32 v197, v198, v199\n"
      "v_mfma_f32_16x16x32_bf16 v[182:185], a[28:31], v[104:107], v[182:185]\n"
      "v_fmac_f32 v194, v188, v188\n"
      "v_fmac_f32 v194, v189, v189\n"
      "v_mfma_f32_16x16x32_bf16 v[182:185], a[32:35], v[150:153], v[182:185]\n"
      "v_cvt_pk_bf16_f32 v134, v134, v135\n"
      "v_mul_f32_e64 v149, v180, v149\n"
      "v_mfma_f32_16x16x32_bf16 v[182:185], a[36:39], v[116:119], v[182:185]\n"
      "ds_write_b64 v6, v[204:205] offset:6144\n"
      "v_mfma_f32_16x16x32_bf16 v[182:185], a[40:43], v[126:129], v[182:185]\n"
      "v_fma_f32 v82, v142, v142, s51\n"
      "v_cvt_pk_bf16_f32 v159, v148, v149\n"
      "v_mfma_f32_16x16x32_bf16 v[182:185], a[44:47], v[210:213], v[182:185]\n"
      "v_permlane16_swap_b32 v78, v194\n"
      "v_mfma_f32_16x16x32_bf16 v[220:223], a[48:51], v[138:141], 0\n"
      "v_cvt_pk_bf16_f32 v160, v160, v161\n"
      "v_add_f32 v78, v78, v194\n"
      "v_mfma_f32_16x16x32_bf16 v[220:223], a[52:55], v[104:107], v[220:223]\n"
      "v_fmac_f32 v82, v143, v143\n"
      "v_mov_b32 v194, v78\n"
      "v_mfma_f32_16x16x32_bf16 v[220:223], a[56:59], v[150:153], v[220:223]\n"
      "v_cvt_pk_bf16_f32 v158, v146, v147\n"
      "v_fmac_f32 v82, v144, v144\n"
      "v_mfma_f32_16x16x32_bf16 v[220:223], a[60:63], v[116:119], v[220:223]\n"
      "v_permlane32_swap_b32 v78, v194\n"
      "v_mfma_f32_16x16x32_bf16 v[220:223], a[64:67], v[126:129], v[220:223]\n"
      "ds_write_b128 v5, v[156:159] offset:12288\n"
      "v_mfma_f32_16x16x32_bf16 v[220:223], a[68:71], v[210:213], v[220:223]\n"
      "v_add_f32 v78, v78, v194\n"
      "v_cvt_pk_bf16_f32 v161, v162, v163\n"
      "v_mfma_f32_16x16x32_bf16 v[146:149], a[72:75], v[138:141], 0\n"
      "v_rsq_f32 v78, v78\n"
      "v_mfma_f32_16x16x32_bf16 v[146:149], a[76:79], v[104:107], v[146:149]\n"
      "v_fmac_f32_e64 v82, v145, v145\n"
      "v_cvt_pk_bf16_f32 v203, v114, v115\n"
      "v_mfma_f32_16x16x32_bf16 v[146:149], a[80:83], v[150:153], v[146:149]\n"
      "ds_read_b128 v[156:159], v0 offset:4096\n"
      "v_cvt_pk_bf16_f32 v135, v136, v137\n"
      "v_mfma_f32_16x16x32_bf16 v[146:149], a[84:87], v[116:119], v[146:149]\n"
      "v_fmac_f32 v82, v182, v182\n"
      "v_mov_b32 v194, v78\n"
      "v_mfma_f32_16x16x32_bf16 v[146:149], a[88:91], v[126:129], v[146:149]\n"
      "v_fmac_f32 v82, v183, v183\n"
      "v_fmac_f32 v82, v184, v184\n"
      "v_mfma_f32_16x16x32_bf16 v[146:149], a[92:95], v[210:213], v[146:149]\n"
      "ds_read_b128 v[178:181], v1 offset:10240\n"
      "v_fmac_f32 v82, v185, v185\n"
      "v_mfma_f32_16x16x32_bf16 v[204:207], a[96:99], v[138:141], 0\n"
      "ds_write_b64 v6, v[134:135]\n"
      "v_mfma_f32_16x16x32_bf16 v[204:207], a[100:103], v[104:107], v[204:207]\n"
      "v_permlane16_swap_b32 v78, v194\n"
      "v_mfma_f32_16x16x32_bf16 v[204:207], a[104:107], v[150:153], v[204:207]\n"
      "s_nop 0\n"
      "v_fma_f32 v173, v146, v146, s51\n"
      "v_mul_f32 v78, s50, v78\n"
      "v_mfma_f32_16x16x32_bf16 v[204:207], a[108:111], v[116:119], v[204:207]\n"
      "v_mul_f32 v187, v194, v187\n"
      "v_cvt_pk_bf16_f32 v202, v112, v113\n"
      "v_mfma_f32_16x16x32_bf16 v[204:207], a[112:115], v[126:129], v[204:207]\n"
      "v_mul_f32 v177, v78, v177\n"
      "v_mul_f32_e64 v175, v78, v175\n"
      "v_mfma_f32_16x16x32_bf16 v[204:207], a[116:119], v[210:213], v[204:207]\n"
      "v_mul_f32 v190, v78, v190\n"
      "v_mul_f32 v192, v78, v192\n"
      "v_mfma_f32_16x16x32_bf16 v[168:171], a[120:123], v[138:141], 0\n"
      "ds_read_b128 v[134:137], v1 offset:4096\n"
      "v_fmac_f32 v173, v147, v147\n"
      "v_mfma_f32_16x16x32_bf16 v[168:171], a[124:127], v[104:107], v[168:171]\n"
      "v_fmac_f32 v82, v220, v220\n"
      "v_fmac_f32_e64 v173, v148, v148\n"
      "v_mfma_f32_16x16x32_bf16 v[168:171], a[128:131], v[150:153], v[168:171]\n"
      "ds_read_b128 v[96:99], v0 offset:16384\n"
      "v_fmac_f32 v173, v149, v149\n"
      "v_mfma_f32_16x16x32_bf16 v[168:171], a[132:135], v[116:119], v[168:171]\n"
      "v_cvt_pk_bf16_f32 v215, v216, v217\n"
      "v_mul_f32 v191, v78, v191\n"
      "v_mfma_f32_16x16x32_bf16 v[168:171], a[136:139], v[126:129], v[168:171]\n"
      "v_mul_f32 v122, v194, v122\n"
      "v_fmac_f32 v173, v204, v204\n"
      "v_mfma_f32_16x16x32_bf16 v[168:171], a[140:143], v[210:213], v[168:171]\n"
      "ds_write_b64 v6, v[160:161] offset:12288\n"
      "v_mfma_f32_16x16x32_bf16 v[160:163], v[138:141], a[144:147], 0\n"
      "s_cmp_ge_u32 s95, 0x40000000\n"
      "s_cselect_b32 s94, -1, -2\n"
      "v_mul_f32 v166, v194, v166\n"
      "v_mfma_f32_16x16x32_bf16 v[160:163], v[104:107], a[148:151], v[160:163]\n"
      "s_cmp_gt_u32 s42, s44\n"
      "s_cselect_b32 s95, s94, s95\n"
      "v_fmac_f32 v173, v205, v205\n"
      "v_mfma_f32_16x16x32_bf16 v[160:163], v[150:153], a[152:155], v[160:163]\n"
      "v_mul_f32 v120, v194, v120\n"
      "v_mul_f32 v193, v78, v193\n"
      "v_mfma_f32_16x16x32_bf16 v[160:163], v[116:119], a[156:159], v[160:163]\n"
      "v_mul_f32 v121, v194, v121\n"
      "v_mul_f32 v167, v194, v167\n"
      "v_mfma_f32_16x16x32_bf16 v[160:163], v[126:129], a[160:163], v[160:163]\n"
      "v_mul_f32 v101, v78, v101\n"
      "v_fmac_f32 v173, v206, v206\n"
      "v_mfma_f32_16x16x32_bf16 v[160:163], v[210:213], a[164:167], v[160:163]\n"
      "v_fmac_f32 v82, v221, v221\n"
      "v_mul_f32 v102, v78, v102\n"
      "v_mfma_f32_16x16x32_bf16 v[112:115], v[138:141], a[168:171], 0\n"
      "v_fmac_f32 v173, v207, v207\n"
      "v_mul_f32 v164, v194, v164\n"
      "v_mfma_f32_16x16x32_bf16 v[112:115], v[104:107], a[172:175], v[112:115]\n"
      "v_fmac_f32 v173, v168, v168\n"
      "v_mul_f32 v174, v78, v174\n"
      "v_mfma_f32_16x16x32_bf16 v[112:115], v[150:153], a[176:179], v[112:115]\n"
      "v_cvt_pk_bf16_f32 v132, v160, v161\n"
      "v_fmac_f32_e64 v173, v169, v169\n"
      "v_mfma_f32_16x16x32_bf16 v[112:115], v[116:119], a[180:183], v[112:115]\n"
      "ds_write_b128 v5, v[200:203] offset:6144\n"
      "v_mfma_f32_16x16x32_bf16 v[112:115], v[126:129], a[184:187], v[112:115]\n"
      "v_mul_f32 v176, v78, v176\n"
      "v_fmac_f32 v173, v170, v170\n"
      "v_mfma_f32_16x16x32_bf16 v[112:115], v[210:213], a[188:191], v[112:115]\n"
      "ds_read_u16 v86, v11 offset:64\n"
      "v_fmac_f32 v82, v222, v222\n"
      "v_mfma_f32_16x16x32_bf16 v[200:203], v[138:141], a[192:195], 0\n"
      "v_mul_f32 v165, v194, v165\n"
      "v_mul_f32 v186, v194, v186\n"
      "v_mfma_f32_16x16x32_bf16 v[200:203], v[104:107], a[196:199], v[200:203]\n"
      "ds_read_b128 v[138:141], v0 offset:10240\n"
      "v_fmac_f32 v173, v171, v171\n"
      "v_mfma_f32_16x16x32_bf16 v[200:203], v[150:153], a[200:203], v[200:203]\n"
      "ds_read_b128 v[104:107], v2 offset:24576\n"
      "v_fmac_f32 v82, v223, v223\n"
      "v_mfma_f32_16x16x32_bf16 v[200:203], v[116:119], a[204:207], v[200:203]\n"
      "s_nop 0\n"
      "v_permlane16_swap_b32_e64 v82, v173\n"
      "v_mfma_f32_16x16x32_bf16 v[200:203], v[126:129], a[208:211], v[200:203]\n"
      "ds_read_b128 v[116:119], v1 offset:16384\n"
      "v_mul_f32 v188, v194, v188\n"
      "v_mfma_f32_16x16x32_bf16 v[200:203], v[210:213], a[212:215], v[200:203]\n"
      "s_waitcnt lgkmcnt(3)\n"
      "v_mul_f32 v189, v194, v189\n"
      "v_add_f32 v82, v82, v173\n"
      "v_mfma_f32_16x16x32_bf16 v[210:213], a[0:3], v[156:159], 0\n"
      "v_mul_f32 v100, v78, v100\n"
      "v_mul_f32 v103, v78, v103\n"
      "v_mfma_f32_16x16x32_bf16 v[210:213], a[4:7], v[134:137], v[210:213]\n"
      "s_waitcnt lgkmcnt(2)\n"
      "v_cvt_pk_bf16_f32 v164, v164, v165\n"
      "v_cvt_pk_bf16_f32 v190, v190, v191\n"
      "v_mfma_f32_16x16x32_bf16 v[210:213], a[8:11], v[138:141], v[210:213]\n"
      "v_mov_b32 v173, v82\n"
      "v_cvt_pk_bf16_f32 v133, v162, v163\n"
      "v_mfma_f32_16x16x32_bf16 v[210:213], a[12:15], v[178:181], v[210:213]\n"
      "v_cvt_pk_bf16_f32 v165, v166, v167\n"
      "v_cvt_pk_bf16_f32 v174, v174, v175\n"
      "v_mfma_f32_16x16x32_bf16 v[210:213], a[16:19], v[96:99], v[210:213]\n"
      "s_waitcnt lgkmcnt(0)\n"
      "v_permlane32_swap_b32 v82, v173\n"
      "v_mfma_f32_16x16x32_bf16 v[210:213], a[20:23], v[116:119], v[210:213]\n"
      "v_add_f32 v82, v82, v173\n"
      "v_mul_f32 v123, v194, v123\n"
      "v_mfma_f32_16x16x32_bf16 v[150:153], a[24:27], v[156:159], 0\n"
      "v_rsq_f32 v82, v82\n"
      "v_mfma_f32_16x16x32_bf16 v[150:153], a[28:31], v[134:137], v[150:153]\n"
      "v_mov_b32 v173, v82\n"
      "v_cvt_pk_bf16_f32 v186, v186, v187\n"
      "v_mfma_f32_16x16x32_bf16 v[150:153], a[32:35], v[138:141], v[150:153]\n"
      "v_permlane16_swap_b32 v82, v173\n"
      "v_mfma_f32_16x16x32_bf16 v[150:153], a[36:39], v[178:181], v[150:153]\n"
      "v_fma_f32 v81, v210, v210, s51\n"
      "v_mul_f32 v168, v173, v168\n"
      "v_mfma_f32_16x16x32_bf16 v[150:153], a[40:43], v[96:99], v[150:153]\n"
      "v_mul_f32_e64 v169, v173, v169\n"
      "v_cvt_pk_bf16_f32 v167, v122, v123\n"
      "v_mfma_f32_16x16x32_bf16 v[150:153], a[44:47], v[116:119], v[150:153]\n"
      "v_cvt_pk_bf16_f32 v175, v176, v177\n"
      "v_mul_f32 v82, s50, v82\n"
      "v_mfma_f32_16x16x32_bf16 v[126:129], a[48:51], v[156:159], 0\n"
      "v_cvt_pk_bf16_f32 v198, v112, v113\n"
      "v_fmac_f32 v81, v211, v211\n"
      "v_mfma_f32_16x16x32_bf16 v[126:129], a[52:55], v[134:137], v[126:129]\n"
      "v_mul_f32 v147, v173, v147\n"
      "v_mul_f32 v143, v82, v143\n"
      "v_mfma_f32_16x16x32_bf16 v[126:129], a[56:59], v[138:141], v[126:129]\n"
      "v_mul_f32 v183, v82, v183\n"
      "v_mul_f32 v146, v173, v146\n"
      "v_mfma_f32_16x16x32_bf16 v[126:129], a[60:63], v[178:181], v[126:129]\n"
      "v_mul_f32_e64 v171, v173, v171\n"
      "v_cvt_pk_bf16_f32 v146, v146, v147\n"
      "v_mfma_f32_16x16x32_bf16 v[126:129], a[64:67], v[96:99], v[126:129]\n"
      "v_cvt_pk_bf16_f32 v176, v100, v101\n"
      "v_fmac_f32 v81, v212, v212\n"
      "v_mfma_f32_16x16x32_bf16 v[126:129], a[68:71], v[116:119], v[126:129]\n"
      "v_mul_f32 v204, v173, v204\n"
      "v_cvt_pk_bf16_f32 v177, v102, v103\n"
      "v_mfma_f32_16x16x32_bf16 v[160:163], a[72:75], v[156:159], 0\n"
      "v_cvt_pk_bf16_f32 v168, v168, v169\n"
      "v_mul_f32 v221, v82, v221\n"
      "v_mfma_f32_16x16x32_bf16 v[160:163], a[76:79], v[134:137], v[160:163]\n"
      "v_mul_f32 v149, v173, v149\n"
      "v_fmac_f32 v81, v213, v213\n"
      "v_mfma_f32_16x16x32_bf16 v[160:163], a[80:83], v[138:141], v[160:163]\n"
      "v_mul_f32 v205, v173, v205\n"
      "v_cvt_pk_bf16_f32 v199, v114, v115\n"
      "v_mfma_f32_16x16x32_bf16 v[160:163], a[84:87], v[178:181], v[160:163]\n"
      "v_cvt_pk_bf16_f32 v187, v188, v189\n"
      "v_mul_f32 v220, v82, v220\n"
      "v_mfma_f32_16x16x32_bf16 v[160:163], a[88:91], v[96:99], v[160:163]\n"
      "v_fmac_f32 v81, v150, v150\n"
      "v_cvt_pk_bf16_f32 v216, v200, v201\n"
      "v_mfma_f32_16x16x32_bf16 v[160:163], a[92:95], v[116:119], v[160:163]\n"
      "v_mul_f32 v222, v82, v222\n"
      "v_mul_f32 v148, v173, v148\n"
      "v_mfma_f32_16x16x32_bf16 v[100:103], a[96:99], v[156:159], 0\n"
      "v_fmac_f32_e64 v81, v151, v151\n"
      "v_cvt_pk_bf16_f32 v147, v148, v149\n"
      "v_mfma_f32_16x16x32_bf16 v[100:103], a[100:103], v[134:137], v[100:103]\n"
      "v_mul_f32 v223, v82, v223\n"
      "v_mfma_f32_16x16x32_bf16 v[100:103], a[104:107], v[138:141], v[100:103]\n"
      "v_fmac_f32 v81, v152, v152\n"
      "v_cvt_pk_bf16_f32 v166, v120, v121\n"
      "v_mfma_f32_16x16x32_bf16 v[100:103], a[108:111], v[178:181], v[100:103]\n"
      "v_cvt_pk_bf16_f32 v217, v202, v203\n"
      "v_fma_f32 v189, v160, v160, s51\n"
      "v_mfma_f32_16x16x32_bf16 v[100:103], a[112:115], v[96:99], v[100:103]\n"
      "v_fmac_f32 v81, v153, v153\n"
      "v_fmac_f32 v81, v126, v126\n"
      "v_mfma_f32_16x16x32_bf16 v[100:103], a[116:119], v[116:119], v[100:103]\n"
      "v_fmac_f32_e64 v189, v161, v161\n"
      "v_cvt_pk_bf16_f32 v148, v204, v205\n"
      "v_mfma_f32_16x16x32_bf16 v[120:123], a[120:123], v[156:159], 0\n"
      "v_cvt_pk_bf16_f32 v191, v192, v193\n"
      "v_fmac_f32_e64 v189, v162, v162\n"
      "v_mfma_f32_16x16x32_bf16 v[120:123], a[124:127], v[134:137], v[120:123]\n"
      "v_cvt_pk_bf16_f32 v220, v220, v221\n"
      "v_fmac_f32 v81, v127, v127\n"
      "v_mfma_f32_16x16x32_bf16 v[120:123], a[128:131], v[138:141], v[120:123]\n"
      "v_mul_f32 v170, v173, v170\n"
      "v_fmac_f32 v189, v163, v163\n"
      "v_mfma_f32_16x16x32_bf16 v[120:123], a[132:135], v[178:181], v[120:123]\n"
      "v_fmac_f32 v81, v128, v128\n"
      "v_fmac_f32 v81, v129, v129\n"
      "v_mfma_f32_16x16x32_bf16 v[120:123], a[136:139], v[96:99], v[120:123]\n"
      "v_fmac_f32 v189, v100, v100\n"
      "v_fmac_f32 v189, v101, v101\n"
      "v_mfma_f32_16x16x32_bf16 v[120:123], a[140:143], v[116:119], v[120:123]\n"
      "s_add_u32 s42, s42, 1\n"
      "v_mul_f32_e64 v144, v82, v144\n"
      "v_cvt_pk_bf16_f32 v169, v170, v171\n"
      "v_mfma_f32_16x16x32_bf16 v[200:203], v[156:159], a[144:147], 0\n"
      "v_cvt_pk_bf16_f32 v221, v222, v223\n"
      "v_fmac_f32_e64 v189, v102, v102\n"
      "v_mfma_f32_16x16x32_bf16 v[200:203], v[134:137], a[148:151], v[200:203]\n"
      "ds_read_u16 v78, v11\n"
      "v_fmac_f32 v189, v103, v103\n"
      "v_mfma_f32_16x16x32_bf16 v[200:203], v[138:141], a[152:155], v[200:203]\n"
      "v_fmac_f32 v189, v120, v120\n"
      "v_fmac_f32_e64 v189, v121, v121\n"
      "v_mfma_f32_16x16x32_bf16 v[200:203], v[178:181], a[156:159], v[200:203]\n"
      "v_fmac_f32 v189, v122, v122\n"
      "v_fmac_f32 v189, v123, v123\n"
      "v_mfma_f32_16x16x32_bf16 v[200:203], v[96:99], a[160:163], v[200:203]\n"
      "s_nop 0\n"
      "v_permlane16_swap_b32 v81, v189\n"
      "v_mfma_f32_16x16x32_bf16 v[200:203], v[116:119], a[164:167], v[200:203]\n"
      "v_add_f32 v81, v81, v189\n"
      "v_mov_b32 v189, v81\n"
      "v_mfma_f32_16x16x32_bf16 v[112:115], v[156:159], a[168:171], 0\n"
      "s_nop 0\n"
      "v_permlane32_swap_b32 v81, v189\n"
      "v_mfma_f32_16x16x32_bf16 v[112:115], v[134:137], a[172:175], v[112:115]\n"
      "v_mul_f32 v184, v82, v184\n"
      "v_add_f32 v81, v81, v189\n"
      "v_mfma_f32_16x16x32_bf16 v[112:115], v[138:141], a[176:179], v[112:115]\n"
      "v_mul_f32 v207, v173, v207\n"
      "v_mul_f32 v206, v173, v206\n"
      "v_mfma_f32_16x16x32_bf16 v[112:115], v[178:181], a[180:183], v[112:115]\n"
      "v_rsq_f32 v81, v81\n"
      "v_mfma_f32_16x16x32_bf16 v[112:115], v[96:99], a[184:187], v[112:115]\n"
      "ds_read_b128 v[192:195], v2 offset:18432\n"
      "v_mov_b32 v189, v81\n"
      "v_mfma_f32_16x16x32_bf16 v[112:115], v[116:119], a[188:191], v[112:115]\n"
      "s_nop 0\n"
      "v_permlane16_swap_b32 v81, v189\n"
      "v_mfma_f32_16x16x32_bf16 v[170:173], v[156:159], a[192:195], 0\n"
      "ds_read_b128 v[156:159], v2 offset:21504\n"
      "v_cvt_pk_bf16_f32 v149, v206, v207\n"
      "v_mfma_f32_16x16x32_bf16 v[170:173], v[134:137], a[196:199], v[170:173]\n"
      "v_mul_f32 v163, v189, v163\n"
      "v_mul_f32 v120, v189, v120\n"
      "v_mfma_f32_16x16x32_bf16 v[170:173], v[138:141], a[200:203], v[170:173]\n"
      "v_mul_f32 v182, v82, v182\n"
      "v_mul_f32 v161, v189, v161\n"
      "v_mfma_f32_16x16x32_bf16 v[170:173], v[178:181], a[204:207], v[170:173]\n"
      "ds_read_b128 v[204:207], v2 offset:30720\n"
      "v_mul_f32_e64 v162, v189, v162\n"
      "v_mfma_f32_16x16x32_bf16 v[170:173], v[96:99], a[208:211], v[170:173]\n"
      "v_cvt_pk_bf16_f32 v76, v200, v201\n"
      "v_mul_f32 v122, v189, v122\n"
      "v_mfma_f32_16x16x32_bf16 v[170:173], v[116:119], a[212:215], v[170:173]\n"
      "s_waitcnt lgkmcnt(2)\n"
      "v_mul_f32 v142, v82, v142\n"
      "v_cvt_pk_bf16_f32 v77, v202, v203\n"
      "v_mfma_f32_16x16x32_bf16 v[96:99], a[216:219], v[192:195], 0\n"
      "s_waitcnt lgkmcnt(1)\n"
      "v_mul_f32 v121, v189, v121\n"
      "v_mul_f32 v81, s50, v81\n"
      "v_mfma_f32_16x16x32_bf16 v[96:99], a[220:223], v[156:159], v[96:99]\n"
      "ds_read_b128 v[200:203], v2 offset:27648\n"
      "v_mul_f32 v145, v82, v145\n"
      "v_mfma_f32_16x16x32_bf16 v[96:99], a[224:227], v[104:107], v[96:99]\n"
      "v_mul_f32 v160, v189, v160\n"
      "v_mul_f32_e64 v210, v81, v210\n"
      "v_mfma_f32_16x16x32_bf16 v[116:119], a[240:243], v[192:195], 0\n"
      "ds_read_b128 v[178:181], v2 offset:33792\n"
      "v_cvt_pk_bf16_f32 v84, v170, v171\n"
      "v_mfma_f32_16x16x32_bf16 v[116:119], a[244:247], v[156:159], v[116:119]\n"
      "v_cvt_pk_bf16_f32 v142, v142, v143\n"
      "v_mul_f32 v152, v81, v152\n"
      "v_mfma_f32_16x16x32_bf16 v[116:119], a[248:251], v[104:107], v[116:119]\n"
      "v_mul_f32 v101, v189, v101\n"
      "v_cvt_pk_bf16_f32 v143, v144, v145\n"
      "v_mfma_f32_16x16x32_bf16 v[134:137], v[232:235], v[192:195], 0\n"
      "v_mul_f32 v127, v81, v127\n"
      "v_mul_f32 v213, v81, v213\n"
      "v_mfma_f32_16x16x32_bf16 v[134:137], v[236:239], v[156:159], v[134:137]\n"
      "v_mul_f32 v100, v189, v100\n"
      "v_mul_f32 v123, v189, v123\n"
      "v_mfma_f32_16x16x32_bf16 v[134:137], v[240:243], v[104:107], v[134:137]\n"
      "s_waitcnt lgkmcnt(1)\n"
      "v_mul_f32 v102, v189, v102\n"
      "v_mul_f32_e64 v151, v81, v151\n"
      "v_mfma_f32_16x16x32_bf16 v[96:99], a[228:231], v[200:203], v[96:99]\n"
      "ds_read_b128 v[192:195], v10 offset:32\n"
      "v_cvt_pk_bf16_f32 v160, v160, v161\n"
      "v_mfma_f32_16x16x32_bf16 v[96:99], a[232:235], v[204:207], v[96:99]\n"
      "s_waitcnt lgkmcnt(1)\n"
      "v_cvt_pk_bf16_f32 v120, v120, v121\n"
      "v_mul_f32 v185, v82, v185\n"
      "v_mfma_f32_16x16x32_bf16 v[96:99], a[236:239], v[178:181], v[96:99]\n"
      "ds_read_b64 v[222:223], v10 offset:24\n"
      "v_mul_f32_e64 v128, v81, v128\n"
      "v_mfma_f32_16x16x32_bf16 v[116:119], a[252:255], v[200:203], v[116:119]\n"
      "ds_read_u16 v82, v11 offset:32\n"
      "v_cvt_pk_bf16_f32 v145, v184, v185\n"
      "v_mfma_f32_16x16x32_bf16 v[116:119], v[224:227], v[204:207], v[116:119]\n"
      "v_mul_f32 v103, v189, v103\n"
      "v_mul_f32 v129, v81, v129\n"
      "v_mfma_f32_16x16x32_bf16 v[116:119], v[228:231], v[178:181], v[116:119]\n"
      "v_mul_f32_e64 v126, v81, v126\n"
      "v_cvt_pk_bf16_f32 v161, v162, v163\n"
      "v_mfma_f32_16x16x32_bf16 v[134:137], v[244:247], v[200:203], v[134:137]\n"
      "ds_read_b128 v[138:141], v2 offset:18688\n"
      "v_mul_f32_e64 v211, v81, v211\n"
      "v_mfma_f32_16x16x32_bf16 v[134:137], v[248:251], v[204:207], v[134:137]\n"
      "v_cvt_pk_bf16_f32 v163, v102, v103\n"
      "v_cvt_pk_bf16_f32 v144, v182, v183\n"
      "v_mfma_f32_16x16x32_bf16 v[134:137], v[252:255], v[178:181], v[134:137]\n"
      "v_cvt_pk_bf16_f32 v80, v112, v113\n"
      "v_cvt_pk_bf16_f32 v162, v100, v101\n"
      "v_mfma_f32_16x16x32_bf16 v[104:107], v[164:167], v[174:177], v[40:43]\n"
      "ds_read_b128 v[100:103], v2 offset:21760\n"
      "v_cvt_pk_bf16_f32 v210, v210, v211\n"
      "v_mfma_f32_16x16x32_bf16 v[154:157], v[146:149], v[174:177], v[44:47]\n"
      "s_waitcnt lgkmcnt(2)\n"
      "v_mul_f32 v212, v81, v212\n"
      "v_cvt_pk_bf16_f32 v121, v122, v123\n"
      "v_mfma_f32_16x16x32_bf16 v[182:185], v[160:163], v[174:177], v[48:51]\n"
      "ds_read_b128 v[122:125], v2 offset:24832\n"
      "v_mul_f32 v150, v81, v150\n"
      "v_mfma_f32_16x16x32_bf16 v[178:181], v[192:195], v[174:177], v[24:27]\n"
      "s_waitcnt lgkmcnt(2)\n"
      "v_cvt_pk_bf16_f32 v126, v126, v127\n"
      "v_cvt_pk_bf16_f32 v211, v212, v213\n"
      "v_mfma_f32_16x16x16_bf16 v[104:107], v[186:187], v[190:191], v[104:107]\n"
      "v_mul_f32_e64 v153, v81, v153\n"
      "v_cvt_pk_bf16_f32 v212, v150, v151\n"
      "v_cvt_pk_bf16_f32 v81, v114, v115\n"
      "v_mfma_f32_16x16x16_bf16 v[154:157], v[168:169], v[190:191], v[154:157]\n"
      "v_fma_f32 v150, v96, v96, s52\n"
      "s_waitcnt vmcnt(1)\n"
      "v_or3_b32 v108, v108, v109, v110\n"
      "v_mul_f32 v151, v97, v97\n"
      "v_mfma_f32_16x16x16_bf16 v[182:185], v[120:121], v[190:191], v[182:185]\n"
      "v_exp_f32_e64 v105, v105\n"
      "v_cvt_pk_bf16_f32 v213, v152, v153\n"
      "v_mfma_f32_16x16x16_bf16 v[178:181], v[222:223], v[190:191], v[178:181]\n"
      "v_cvt_pk_bf16_f32 v85, v172, v173\n"
      "v_exp_f32_e64 v107, v107\n"
      "v_mfma_f32_16x16x32_bf16 v[200:203], a[216:219], v[138:141], 0\n"
      "s_waitcnt lgkmcnt(0)\n"
      "v_exp_f32 v157, v157\n"
      "v_mfma_f32_16x16x32_bf16 v[200:203], a[220:223], v[100:103], v[200:203]\n"
      "v_exp_f32_e64 v156, v156\n"
      "v_mfma_f32_16x16x32_bf16 v[200:203], a[224:227], v[122:125], v[200:203]\n"
      "v_exp_f32 v104, v104\n"
      "v_mfma_f32_16x16x32_bf16 v[112:115], a[240:243], v[138:141], 0\n"
      "v_exp_f32 v154, v154\n"
      "v_mfma_f32_16x16x32_bf16 v[112:115], a[244:247], v[100:103], v[112:115]\n"
      "v_exp_f32 v183, v183\n"
      "v_mfma_f32_16x16x32_bf16 v[112:115], a[248:251], v[122:125], v[112:115]\n"
      "v_cvt_pk_bf16_f32 v104, v104, v105\n"
      "v_fmac_f32 v151, v99, v99\n"
      "v_mfma_f32_16x16x32_bf16 v[204:207], v[232:235], v[138:141], 0\n"
      "v_exp_f32 v178, v178\n"
      "v_mfma_f32_16x16x32_bf16 v[204:207], v[236:239], v[100:103], v[204:207]\n"
      "v_exp_f32_e64 v106, v106\n"
      "v_mfma_f32_16x16x32_bf16 v[204:207], v[240:243], v[122:125], v[204:207]\n"
      "s_waitcnt vmcnt(0)\n"
      "v_or3_b32 v108, v108, v111, v90\n"
      "v_cvt_pk_bf16_f32 v127, v128, v129\n"
      "v_mfma_f32_16x16x32_bf16 v[174:177], v[164:167], v[142:145], v[52:55]\n"
      "v_exp_f32_e64 v182, v182\n"
      "v_mfma_f32_16x16x32_bf16 v[170:173], v[146:149], v[142:145], v[56:59]\n"
      "v_exp_f32 v184, v184\n"
      "v_mfma_f32_16x16x32_bf16 v[122:125], v[160:163], v[142:145], v[60:63]\n"
      "v_exp_f32 v185, v185\n"
      "v_mfma_f32_16x16x32_bf16 v[188:191], v[192:195], v[142:145], v[26:29]\n"
      "v_cvt_pk_bf16_f32 v105, v106, v107\n"
      "v_cvt_pk_bf16_f32 v90, v178, 0\n"
      "v_mfma_f32_16x16x16_bf16 v[174:177], v[186:187], v[220:221], v[174:177]\n"
      "v_exp_f32 v155, v155\n"
      "v_fmac_f32 v150, v98, v98\n"
      "v_mfma_f32_16x16x16_bf16 v[170:173], v[168:169], v[220:221], v[170:173]\n"
      "v_cvt_pk_bf16_f32 v106, v154, v155\n"
      "v_cvt_pk_bf16_f32 v107, v156, v157\n"
      "v_cvt_pk_bf16_f32 v88, v182, v183\n"
      "v_mfma_f32_16x16x16_bf16 v[122:125], v[120:121], v[220:221], v[122:125]\n"
      "v_cvt_pk_bf16_f32 v89, v184, v185\n"
      "v_exp_f32 v174, v174\n"
      "v_mfma_f32_16x16x16_bf16 v[188:191], v[222:223], v[220:221], v[188:191]\n"
      "v_exp_f32 v175, v175\n"
      "v_exp_f32 v176, v176\n"
      "v_mfma_f32_16x16x32_bf16 v[156:159], v[32:35], v[104:107], 0\n"
      "v_exp_f32 v170, v170\n"
      "v_mfma_f32_16x16x32_bf16 v[156:159], v[32:35], v[88:91], v[156:159]\n"
      "v_exp_f32_e64 v173, v173\n"
      "v_mfma_f32_16x16x32_bf16 v[218:221], v[130:133], v[104:107], 0\n"
      "v_exp_f32 v172, v172\n"
      "v_mfma_f32_16x16x32_bf16 v[218:221], v[76:79], v[88:91], v[218:221]\n"
      "v_exp_f32 v171, v171\n"
      "v_mfma_f32_16x16x32_bf16 v[138:141], v[196:199], v[104:107], 0\n"
      "v_exp_f32_e64 v177, v177\n"
      "v_mfma_f32_16x16x32_bf16 v[138:141], v[80:83], v[88:91], v[138:141]\n"
      "ds_read_b128 v[178:181], v2 offset:30976\n"
      "v_cvt_pk_bf16_f32 v174, v174, v175\n"
      "v_mfma_f32_16x16x32_bf16 v[152:155], v[214:217], v[104:107], 0\n"
      "ds_read_b128 v[104:107], v2 offset:27904\n"
      "v_cvt_pk_bf16_f32 v175, v176, v177\n"
      "v_mfma_f32_16x16x32_bf16 v[152:155], v[84:87], v[88:91], v[152:155]\n"
      "ds_write_b32 v31, v108 offset:4608\n"
      "v_mfma_f32_16x16x32_bf16 v[142:145], v[164:167], v[210:213], v[64:67]\n"
      "v_rcp_f32 v156, v156\n"
      "v_exp_f32 v188, v188\n"
      "v_cvt_pk_bf16_f32 v177, v172, v173\n"
      "v_pk_fma_f32 v[150:151], v[116:117], v[116:117], v[150:151]\n"
      "v_pk_mul_f32 v[138:139], v[138:139], v[156:157] op_sel_hi:[1,0]\n"
      "v_pk_mul_f32 v[218:219], v[218:219], v[156:157] op_sel_hi:[1,0]\n"
      "v_pk_mul_f32 v[152:153], v[152:153], v[156:157] op_sel_hi:[1,0]\n"
      "v_pk_mul_f32 v[220:221], v[220:221], v[156:157] op_sel_hi:[1,0]\n"
      "v_mfma_f32_16x16x32_bf16 v[108:111], v[146:149], v[210:213], v[68:71]\n"
      "v_cvt_pk_bf16_f32 v176, v170, v171\n"
      "v_cvt_pk_bf16_f32 v218, v218, v219\n"
      "v_mfma_f32_16x16x32_bf16 v[170:173], v[160:163], v[210:213], v[72:75]\n"
      "s_waitcnt lgkmcnt(1)\n"
      "v_mul_f32 v141, v156, v141\n"
      "v_mul_f32_e64 v154, v156, v154\n"
      "v_mfma_f32_16x16x32_bf16 v[100:103], v[192:195], v[210:213], v[28:31]\n"
      "ds_read_b128 v[208:211], v2 offset:34048\n"
      "v_mul_f32 v155, v156, v155\n"
      "v_mfma_f32_16x16x16_bf16 v[142:145], v[186:187], v[126:127], v[142:145]\n"
      "v_mul_f32 v140, v156, v140\n"
      "v_cvt_pk_bf16_f32 v219, v220, v221\n"
      "v_exp_f32 v124, v124\n"
      "v_mfma_f32_16x16x16_bf16 v[108:111], v[168:169], v[126:127], v[108:111]\n"
      "v_cvt_pk_bf16_f32 v220, v138, v139\n"
      "v_exp_f32 v125, v125\n"
      "v_mfma_f32_16x16x16_bf16 v[170:173], v[120:121], v[126:127], v[170:173]\n"
      "v_cvt_pk_bf16_f32 v221, v140, v141\n"
      "v_exp_f32 v144, v144\n"
      "v_mfma_f32_16x16x16_bf16 v[100:103], v[222:223], v[126:127], v[100:103]\n"
      "v_cvt_pk_bf16_f32 v93, v124, v125\n"
      "v_exp_f32 v122, v122\n"
      "v_mfma_f32_16x16x32_bf16 v[200:203], a[228:231], v[104:107], v[200:203]\n"
      "v_cvt_pk_bf16_f32 v94, v188, 0\n"
      "v_cvt_pk_bf16_f32 v152, v152, v153\n"
      "v_mfma_f32_16x16x32_bf16 v[200:203], a[232:235], v[178:181], v[200:203]\n"
      "s_waitcnt lgkmcnt(0)\n"
      "v_exp_f32 v123, v123\n"
      "v_mfma_f32_16x16x32_bf16 v[200:203], a[236:239], v[208:211], v[200:203]\n"
      "v_exp_f32 v170, v170\n"
      "v_mfma_f32_16x16x32_bf16 v[112:115], a[252:255], v[104:107], v[112:115]\n"
      "v_exp_f32 v172, v172\n"
      "v_mfma_f32_16x16x32_bf16 v[112:115], v[224:227], v[178:181], v[112:115]\n"
      "v_exp_f32 v171, v171\n"
      "v_mfma_f32_16x16x32_bf16 v[112:115], v[228:231], v[208:211], v[112:115]\n"
      "ds_read_b128 v[156:159], v7 offset:18432\n"
      "v_exp_f32 v145, v145\n"
      "v_mfma_f32_16x16x32_bf16 v[204:207], v[244:247], v[104:107], v[204:207]\n"
      "ds_read_b128 v[104:107], v2 offset:18944\n"
      "v_cvt_pk_bf16_f32 v92, v122, v123\n"
      "v_mfma_f32_16x16x32_bf16 v[204:207], v[248:251], v[178:181], v[204:207]\n"
      "ds_read_b64 v[102:103], v10 offset:16\n"
      "v_cvt_pk_bf16_f32 v88, v170, v171\n"
      "v_mfma_f32_16x16x32_bf16 v[204:207], v[252:255], v[208:211], v[204:207]\n"
      "ds_read_b128 v[208:211], v10\n"
      "v_cvt_pk_bf16_f32 v153, v154, v155\n"
      "v_mfma_f32_16x16x32_bf16 v[126:129], v[32:35], v[174:177], 0\n"
      "ds_write_b128 v3, v[218:221]\n"
      "v_mfma_f32_16x16x32_bf16 v[126:129], v[32:35], v[92:95], v[126:129]\n"
      "s_add_u32 s43, s43, 1\n"
      "v_exp_f32 v109, v109\n"
      "v_mfma_f32_16x16x32_bf16 v[218:221], v[130:133], v[174:177], 0\n"
      "v_exp_f32_e64 v100, v100\n"
      "v_mfma_f32_16x16x32_bf16 v[218:221], v[76:79], v[92:95], v[218:221]\n"
      "ds_read_b128 v[178:181], v7 offset:22528\n"
      "v_fmac_f32 v150, v118, v118\n"
      "v_mfma_f32_16x16x32_bf16 v[138:141], v[196:199], v[174:177], 0\n"
      "s_waitcnt lgkmcnt(2)\n"
      "v_exp_f32_e64 v143, v143\n"
      "v_mfma_f32_16x16x32_bf16 v[138:141], v[80:83], v[92:95], v[138:141]\n"
      "buffer_store_dwordx4 v[156:159], v20, s[24:27], s46 offen sc1\n"
      "v_mfma_f32_16x16x32_bf16 v[182:185], v[214:217], v[174:177], 0\n"
      "v_exp_f32_e64 v173, v173\n"
      "v_mfma_f32_16x16x32_bf16 v[182:185], v[84:87], v[92:95], v[182:185]\n"
      "v_rcp_f32 v126, v126\n"
      "v_mfma_f32_16x16x32_bf16 v[174:177], v[208:211], v[164:167], v[30:33]\n"
      "v_mul_f32 v155, v201, v201\n"
      "v_fma_f32 v154, v200, v200, s52\n"
      "v_mfma_f32_16x16x32_bf16 v[156:159], v[208:211], v[146:149], v[30:33]\n"
      "ds_read_b128 v[164:167], v7 offset:26624\n"
      "v_fmac_f32 v151, v119, v119\n"
      "v_mfma_f32_16x16x32_bf16 v[188:191], v[208:211], v[160:163], v[30:33]\n"
      "s_waitcnt lgkmcnt(1)\n"
      "v_exp_f32_e64 v142, v142\n"
      "v_mfma_f32_16x16x32_bf16 v[122:125], v[208:211], v[192:195], v[30:33]\n"
      "ds_read_b128 v[208:211], v2 offset:25088\n"
      "v_cvt_pk_bf16_f32 v142, v142, v143\n"
      "v_mfma_f32_16x16x16_bf16 v[174:177], v[102:103], v[186:187], v[174:177]\n"
      "ds_read_b128 v[192:195], v2 offset:22016\n"
      "v_cvt_pk_bf16_f32 v143, v144, v145\n"
      "v_mfma_f32_16x16x16_bf16 v[156:159], v[102:103], v[168:169], v[156:159]\n"
      "buffer_store_dwordx4 v[178:181], v21, s[24:27], s46 offen sc1\n"
      "v_pk_fma_f32 v[154:155], v[202:203], v[202:203], v[154:155]\n"
      "v_pk_mul_f32 v[184:185], v[184:185], v[126:127] op_sel_hi:[1,0]\n"
      "v_pk_fma_f32 v[150:151], v[134:135], v[134:135], v[150:151]\n"
      "v_pk_mul_f32 v[220:221], v[220:221], v[126:127] op_sel_hi:[1,0]\n"
      "v_pk_mul_f32 v[218:219], v[218:219], v[126:127] op_sel_hi:[1,0]\n"
      "v_pk_mul_f32 v[140:141], v[140:141], v[126:127] op_sel_hi:[1,0]\n"
      "v_pk_fma_f32 v[154:155], v[112:113], v[112:113], v[154:155]\n"
      "v_pk_fma_f32 v[154:155], v[114:115], v[114:115], v[154:155]\n"
      "v_mfma_f32_16x16x16_bf16 v[188:191], v[102:103], v[120:121], v[188:191]\n"
      "v_exp_f32_e64 v110, v110\n"
      "v_cndmask_b32 v174, v174, v156, s[58:59]\n"
      "v_mfma_f32_16x16x16_bf16 v[122:125], v[102:103], v[222:223], v[122:125]\n"
      "v_cvt_pk_bf16_f32 v218, v218, v219\n"
      "v_exp_f32 v111, v111\n"
      "v_mfma_f32_16x16x32_bf16 v[156:159], a[216:219], v[104:107], 0\n"
      "s_waitcnt lgkmcnt(0)\n"
      "v_exp_f32_e64 v108, v108\n"
      "v_mfma_f32_16x16x32_bf16 v[156:159], a[220:223], v[192:195], v[156:159]\n"
      "v_cvt_pk_bf16_f32 v89, v172, v173\n"
      "v_cvt_pk_bf16_f32 v90, v100, 0\n"
      "v_mfma_f32_16x16x32_bf16 v[156:159], a[224:227], v[208:211], v[156:159]\n"
      "ds_read_b128 v[168:171], v7 offset:30720\n"
      "v_cvt_pk_bf16_f32 v144, v108, v109\n"
      "v_mfma_f32_16x16x32_bf16 v[146:149], a[240:243], v[104:107], 0\n"
      "buffer_store_dwordx4 v[164:167], v22, s[24:27], s46 offen sc1\n"
      "v_pk_mul_f32 v[182:183], v[182:183], v[126:127] op_sel_hi:[1,0]\n"
      "v_pk_fma_f32 v[150:151], v[136:137], v[136:137], v[150:151]\n"
      "v_pk_fma_f32 v[154:155], v[204:205], v[204:205], v[154:155]\n"
      "v_pk_mul_f32 v[138:139], v[138:139], v[126:127] op_sel_hi:[1,0]\n"
      "v_pk_fma_f32 v[154:155], v[206:207], v[206:207], v[154:155]\n"
      "v_mfma_f32_16x16x32_bf16 v[146:149], a[244:247], v[192:195], v[146:149]\n"
      "ds_read_b128 v[176:179], v2 offset:28160\n"
      "v_cvt_pk_bf16_f32 v219, v220, v221\n"
      "v_mfma_f32_16x16x32_bf16 v[146:149], a[248:251], v[208:211], v[146:149]\n"
      "v_cvt_pk_bf16_f32 v221, v140, v141\n"
      "v_cvt_pk_bf16_f32 v220, v138, v139\n"
      "v_mfma_f32_16x16x32_bf16 v[160:163], v[232:235], v[104:107], 0\n"
      "ds_write_b128 v3, v[218:221] offset:256\n"
      "v_mfma_f32_16x16x32_bf16 v[160:163], v[236:239], v[192:195], v[160:163]\n"
      "v_cndmask_b32 v174, v174, v188, s[60:61]\n"
      "v_cvt_pk_bf16_f32 v145, v110, v111\n"
      "v_mfma_f32_16x16x32_bf16 v[160:163], v[240:243], v[208:211], v[160:163]\n"
      "ds_read_b128 v[208:211], v2 offset:31232\n"
      "v_cndmask_b32 v174, v174, v122, s[62:63]\n"
      "v_mfma_f32_16x16x32_bf16 v[104:107], v[32:35], v[142:145], 0\n"
      "ds_read_b128 v[126:129], v2 offset:34304\n"
      "v_add_f32 v150, v150, v151\n"
      "v_mfma_f32_16x16x32_bf16 v[104:107], v[32:35], v[88:91], v[104:107]\n"
      "s_waitcnt lgkmcnt(3)\n"
      "v_exp_f32_e64 v174, v174\n"
      "v_mfma_f32_16x16x32_bf16 v[188:191], v[130:133], v[142:145], 0\n"
      "ds_write_b64 v4, v[152:153]\n"
      "v_mfma_f32_16x16x32_bf16 v[188:191], v[76:79], v[88:91], v[188:191]\n"
      "buffer_store_dwordx4 v[168:171], v23, s[24:27], s46 offen sc1\n"
      "s_nop 1\n"
      "v_mfma_f32_16x16x32_bf16 v[168:171], v[196:199], v[142:145], 0\n"
      "v_add_f32 v154, v154, v155\n"
      "v_cvt_pk_bf16_f32 v174, v174, v174\n"
      "v_mfma_f32_16x16x32_bf16 v[168:171], v[80:83], v[88:91], v[168:171]\n"
      "ds_write_b16 v13, v174\n"
      "v_mfma_f32_16x16x32_bf16 v[106:109], v[214:217], v[142:145], 0\n"
      "v_rcp_f32_e64 v104, v104\n"
      "v_mfma_f32_16x16x32_bf16 v[106:109], v[84:87], v[88:91], v[106:109]\n"
      "s_waitcnt lgkmcnt(2)\n"
      "v_permlane16_swap_b32 v150, v154\n"
      "v_mfma_f32_16x16x32_bf16 v[156:159], a[228:231], v[176:179], v[156:159]\n"
      "ds_read_b128 v[218:221], v14\n"
      "v_mul_f32_e64 v190, v104, v190\n"
      "v_mfma_f32_16x16x32_bf16 v[156:159], a[232:235], v[208:211], v[156:159]\n"
      "ds_read_b128 v[140:143], v7 offset:34816\n"
      "v_cvt_pk_bf16_f32 v182, v182, v183\n"
      "v_mfma_f32_16x16x32_bf16 v[156:159], a[236:239], v[126:129], v[156:159]\n"
      "v_cvt_pk_bf16_f32 v183, v184, v185\n"
      "v_mul_f32 v189, v104, v189\n"
      "v_mfma_f32_16x16x32_bf16 v[146:149], a[252:255], v[176:179], v[146:149]\n"
      "v_mul_f32 v191, v104, v191\n"
      "v_mul_f32 v188, v104, v188\n"
      "v_mfma_f32_16x16x32_bf16 v[146:149], v[224:227], v[208:211], v[146:149]\n"
      "ds_write_b64 v4, v[182:183] offset:256\n"
      "v_cvt_pk_bf16_f32 v188, v188, v189\n"
      "v_mul_f32 v183, v157, v157\n"
      "v_pk_mul_f32 v[168:169], v[168:169], v[104:105] op_sel_hi:[1,0]\n"
      "v_pk_mul_f32 v[170:171], v[170:171], v[104:105] op_sel_hi:[1,0]\n"
      "v_mfma_f32_16x16x32_bf16 v[146:149], v[228:231], v[126:129], v[146:149]\n"
      "v_cvt_pk_bf16_f32 v189, v190, v191\n"
      "v_cvt_pk_bf16_f32 v191, v170, v171\n"
      "v_mfma_f32_16x16x32_bf16 v[160:163], v[244:247], v[176:179], v[160:163]\n"
      "ds_read_b128 v[174:177], v14 offset:64\n"
      "v_fma_f32 v182, v156, v156, s52\n"
      "v_mfma_f32_16x16x32_bf16 v[160:163], v[248:251], v[208:211], v[160:163]\n"
      "s_waitcnt lgkmcnt(2)\n"
      "v_cvt_pk_bf16_f32 v190, v168, v169\n"
      "v_add_f32 v150, v150, v154\n"
      "v_mfma_f32_16x16x32_bf16 v[160:163], v[252:255], v[126:129], v[160:163]\n"
      "ds_write_b128 v3, v[188:191] offset:512\n"
      "v_pk_fma_f32 v[182:183], v[158:159], v[158:159], v[182:183]\n"
      "v_pk_mul_f32 v[106:107], v[106:107], v[104:105] op_sel_hi:[1,0]\n"
      "v_pk_fma_f32 v[182:183], v[146:147], v[146:147], v[182:183]\n"
      "v_pk_mul_f32 v[108:109], v[108:109], v[104:105] op_sel_hi:[1,0]\n"
      "v_cvt_pk_bf16_f32 v106, v106, v107\n"
      "v_pk_fma_f32 v[182:183], v[148:149], v[148:149], v[182:183]\n"
      "v_cvt_pk_bf16_f32 v107, v108, v109\n"
      "v_pk_fma_f32 v[182:183], v[160:161], v[160:161], v[182:183]\n"
      "v_pk_fma_f32 v[182:183], v[162:163], v[162:163], v[182:183]\n"
      "v_add_f32 v182, v182, v183\n"
      "v_mov_b32 v154, v182\n"
      "v_mfma_f32_16x16x32_bf16 v[208:211], v[218:221], v[32:35], 0\n"
      "ds_write_b64 v4, v[106:107] offset:512\n"
      "v_permlane16_swap_b32 v182, v154\n"
      "v_add_f32 v182, v182, v154\n"
      "s_nop 1\n"
      "v_permlane32_swap_b32 v150, v182\n"
      "v_add_f32 v150, v150, v182\n"
      "s_and_b32 s86, s86, -4\n"
      "ds_write_b32 v31, v150\n"
      "s_cmp_ge_u32 s95, 0x40000000\n"
      "s_cselect_b64 vcc, -1, s[56:57]\n"
      "v_add_u32 v12, 32, v12\n"
      "buffer_store_dwordx4 v[140:143], v25, s[24:27], s46 offen sc1\n"
      "s_waitcnt lgkmcnt(2)\n"
      "v_mfma_f32_16x16x32_bf16 v[208:211], v[174:177], v[32:35], v[208:211]\n"
      "s_waitcnt lgkmcnt(0)\n"
      "s_barrier\n"
      "s_cbranch_vccz L_loop1_%=\n"
      "s_branch L_disp1_%=\n"
      "s_nop 7\n"
      "s_nop 7\n"
      "L_max1_%=:\n"
      "v_mfma_f32_16x16x32_bf16 v[168:171], v[218:221], v[130:133], 0\n"
      "ds_read_b128 v[124:127], v0 offset:18432\n"
      "ds_read_b128 v[108:111], v9\n"
      "v_mfma_f32_16x16x32_bf16 v[168:171], v[174:177], v[76:79], v[168:171]\n"
      "s_add_u32 m0, s64, 0\n"
      "ds_read_b128 v[120:123], v0 offset:24576\n"
      "v_mfma_f32_16x16x32_bf16 v[150:153], v[218:221], v[196:199], 0\n"
      "buffer_load_dwordx4 v16, s[16:19], s84 offen  lds\n"
      "v_mfma_f32_16x16x32_bf16 v[150:153], v[174:177], v[80:83], v[150:153]\n"
      "ds_read_b128 v[182:185], v1 offset:18432\n"
      "v_mfma_f32_16x16x32_bf16 v[194:197], v[218:221], v[214:217], 0\n"
      "ds_read_b128 v[142:145], v0 offset:20480\n"
      "v_readlane_b32 s47, v38, s43\n"
      "v_mfma_f32_16x16x32_bf16 v[194:197], v[174:177], v[84:87], v[194:197]\n"
      "s_add_u32 m0, s64, 4096\n"
      "s_waitcnt lgkmcnt(4)\n"
      "v_mfma_f32_16x16x32_bf16 v[100:103], a[0:3], v[124:127], 0\n"
      "ds_read_b128 v[178:181], v0 offset:26624\n"
      "v_cndmask_b32 v168, v168, v150, s[58:59]\n"
      "v_mfma_f32_16x16x32_bf16 v[164:167], a[24:27], v[124:127], 0\n"
      "s_waitcnt lgkmcnt(4)\n"
      "v_add_f32 v110, v110, v111\n"
      "v_add_f32_e64 v108, v108, v109\n"
      "v_mfma_f32_16x16x32_bf16 v[138:141], a[48:51], v[124:127], 0\n"
      "buffer_load_dwordx4 v17, s[16:19], s84 offen  lds\n"
      "v_mfma_f32_16x16x32_bf16 v[170:173], a[72:75], v[124:127], 0\n"
      "ds_read_b128 v[190:193], v0 offset:30720\n"
      "v_add_f32 v108, v108, v110\n"
      "v_mfma_f32_16x16x32_bf16 v[186:189], a[96:99], v[124:127], 0\n"
      "s_waitcnt lgkmcnt(2)\n"
      "v_rsq_f32_e64 v108, v108\n"
      "v_mfma_f32_16x16x32_bf16 v[174:177], a[120:123], v[124:127], 0\n"
      "ds_read_b128 v[152:155], v1 offset:30720\n"
      "v_mul_f32 v108, 0x415db3d7, v108\n"
      "v_mfma_f32_16x16x32_bf16 v[100:103], a[4:7], v[182:185], v[100:103]\n"
      "ds_read_b128 v[220:223], v1 offset:20480\n"
      "v_mov_b32 v110, v108\n"
      "v_mfma_f32_16x16x32_bf16 v[100:103], a[8:11], v[120:123], v[100:103]\n"
      "s_nop 0\n"
      "v_permlane32_swap_b32_e64 v108, v110\n"
      "v_mfma_f32_16x16x32_bf16 v[164:167], a[28:31], v[182:185], v[164:167]\n"
      "ds_read_b128 v[104:107], v1 offset:24576\n"
      "v_mov_b32_e64 v109, v108\n"
      "v_mfma_f32_16x16x32_bf16 v[164:167], a[32:35], v[120:123], v[164:167]\n"
      "s_add_u32 m0, s64, 8192\n"
      "v_mul_f32 v163, v110, v163\n"
      "v_mul_f32 v157, v110, v157\n"
      "v_mfma_f32_16x16x32_bf16 v[138:141], a[52:55], v[182:185], v[138:141]\n"
      "buffer_load_dwordx4 v18, s[16:19], s84 offen  lds\n"
      "v_mfma_f32_16x16x32_bf16 v[138:141], a[56:59], v[120:123], v[138:141]\n"
      "v_permlane16_swap_b32 v108, v109\n"
      "v_mfma_f32_16x16x32_bf16 v[170:173], a[76:79], v[182:185], v[170:173]\n"
      "s_add_u32 m0, s64, 12288\n"
      "v_mul_f32 v200, v109, v200\n"
      "v_cndmask_b32 v168, v168, v194, s[60:61]\n"
      "v_mfma_f32_16x16x32_bf16 v[170:173], a[80:83], v[120:123], v[170:173]\n"
      "v_mul_f32 v201, v109, v201\n"
      "v_mul_f32 v115, v109, v115\n"
      "v_mfma_f32_16x16x32_bf16 v[186:189], a[100:103], v[182:185], v[186:189]\n"
      "v_mul_f32 v136, v108, v136\n"
      "v_mul_f32 v96, v108, v96\n"
      "v_mfma_f32_16x16x32_bf16 v[186:189], a[104:107], v[120:123], v[186:189]\n"
      "buffer_load_dwordx4 v19, s[16:19], s84 offen  lds\n"
      "v_mfma_f32_16x16x32_bf16 v[174:177], a[124:127], v[182:185], v[174:177]\n"
      "s_add_u32 m0, s65, 16384\n"
      "v_mul_f32 v97, v108, v97\n"
      "v_cvt_pk_bf16_f32 v200, v200, v201\n"
      "v_mfma_f32_16x16x32_bf16 v[174:177], a[128:131], v[120:123], v[174:177]\n"
      "s_waitcnt lgkmcnt(0)\n"
      "v_mul_f32 v99, v108, v99\n"
      "v_mul_f32 v146, v110, v146\n"
      "v_mfma_f32_16x16x32_bf16 v[100:103], a[12:15], v[104:107], v[100:103]\n"
      "v_mul_f32 v202, v109, v202\n"
      "v_mul_f32 v162, v110, v162\n"
      "v_mfma_f32_16x16x32_bf16 v[100:103], a[16:19], v[190:193], v[100:103]\n"
      "v_mul_f32 v119, v108, v119\n"
      "v_mul_f32 v156, v110, v156\n"
      "v_mfma_f32_16x16x32_bf16 v[100:103], a[20:23], v[152:155], v[100:103]\n"
      "buffer_load_dwordx4 v15, s[16:19], s84 offen  lds\n"
      "v_mfma_f32_16x16x32_bf16 v[164:167], a[36:39], v[104:107], v[164:167]\n"
      "v_mul_f32 v134, v108, v134\n"
      "v_mul_f32 v135, v108, v135\n"
      "v_mfma_f32_16x16x32_bf16 v[164:167], a[40:43], v[190:193], v[164:167]\n"
      "v_mul_f32 v113, v109, v113\n"
      "v_mul_f32 v203, v109, v203\n"
      "v_mfma_f32_16x16x32_bf16 v[164:167], a[44:47], v[152:155], v[164:167]\n"
      "v_mul_f32 v112, v109, v112\n"
      "v_mul_f32 v207, v109, v207\n"
      "v_mfma_f32_16x16x32_bf16 v[138:141], a[60:63], v[104:107], v[138:141]\n"
      "v_cvt_pk_bf16_f32 v96, v96, v97\n"
      "v_readlane_b32 s45, v37, s43\n"
      "v_mfma_f32_16x16x32_bf16 v[138:141], a[64:67], v[190:193], v[138:141]\n"
      "v_mul_f32 v205, v109, v205\n"
      "v_mul_f32 v160, v110, v160\n"
      "v_mfma_f32_16x16x32_bf16 v[138:141], a[68:71], v[152:155], v[138:141]\n"
      "v_mul_f32_e64 v98, v108, v98\n"
      "v_fma_f32 v129, v100, v100, s51\n"
      "v_mfma_f32_16x16x32_bf16 v[170:173], a[84:87], v[104:107], v[170:173]\n"
      "buffer_load_dwordx4 v[210:213], v36, s[28:31], s86 offen\n"
      "v_mfma_f32_16x16x32_bf16 v[170:173], a[88:91], v[190:193], v[170:173]\n"
      "v_cvt_pk_bf16_f32 v156, v156, v157\n"
      "v_fmac_f32 v129, v101, v101\n"
      "v_mfma_f32_16x16x32_bf16 v[170:173], a[92:95], v[152:155], v[170:173]\n"
      "v_fmac_f32 v129, v102, v102\n"
      "v_mul_f32_e64 v116, v108, v116\n"
      "v_mfma_f32_16x16x32_bf16 v[186:189], a[108:111], v[104:107], v[186:189]\n"
      "s_and_b32 s47, s47, -4\n"
      "v_cvt_pk_bf16_f32 v201, v202, v203\n"
      "v_mul_f32 v118, v108, v118\n"
      "v_mfma_f32_16x16x32_bf16 v[186:189], a[112:115], v[190:193], v[186:189]\n"
      "s_nop 0\n"
      "v_fma_f32 v128, v170, v170, s51\n"
      "v_mul_f32 v159, v110, v159\n"
      "v_mfma_f32_16x16x32_bf16 v[186:189], a[116:119], v[152:155], v[186:189]\n"
      "v_mul_f32 v158, v110, v158\n"
      "v_mul_f32 v161, v110, v161\n"
      "v_mfma_f32_16x16x32_bf16 v[174:177], a[132:135], v[104:107], v[174:177]\n"
      "v_cvt_pk_bf16_f32 v97, v98, v99\n"
      "v_fmac_f32 v128, v171, v171\n"
      "v_mfma_f32_16x16x32_bf16 v[174:177], a[136:139], v[190:193], v[174:177]\n"
      "v_mul_f32 v148, v110, v148\n"
      "v_mul_f32 v117, v108, v117\n"
      "v_mfma_f32_16x16x32_bf16 v[174:177], a[140:143], v[152:155], v[174:177]\n"
      "v_mul_f32 v204, v109, v204\n"
      "v_fmac_f32_e64 v129, v103, v103\n"
      "v_mfma_f32_16x16x32_bf16 v[130:133], v[124:127], a[144:147], 0\n"
      "v_cvt_pk_bf16_f32 v98, v116, v117\n"
      "v_mov_b32_dpp v208, v208 quad_perm:[0,0,0,0] row_mask:0xf bank_mask:0xf\n"
      "v_mfma_f32_16x16x32_bf16 v[130:133], v[182:185], a[148:151], v[130:133]\n"
      "v_rcp_f32_e64 v208, v208\n"
      "v_mfma_f32_16x16x32_bf16 v[130:133], v[120:123], a[152:155], v[130:133]\n"
      "v_fmac_f32 v128, v172, v172\n"
      "v_fmac_f32 v129, v164, v164\n"
      "v_mfma_f32_16x16x32_bf16 v[130:133], v[104:107], a[156:159], v[130:133]\n"
      "v_fmac_f32 v129, v165, v165\n"
      "v_mul_f32 v114, v109, v114\n"
      "v_mfma_f32_16x16x32_bf16 v[130:133], v[190:193], a[160:163], v[130:133]\n"
      "v_fmac_f32 v128, v173, v173\n"
      "v_mul_f32 v206, v109, v206\n"
      "v_mfma_f32_16x16x32_bf16 v[130:133], v[152:155], a[164:167], v[130:133]\n"
      "v_fmac_f32_e64 v129, v166, v166\n"
      "v_cvt_pk_bf16_f32 v202, v112, v113\n"
      "v_mfma_f32_16x16x32_bf16 v[196:199], v[124:127], a[168:171], 0\n"
      "v_cvt_pk_bf16_f32 v157, v158, v159\n"
      "v_cvt_pk_bf16_f32 v99, v118, v119\n"
      "v_mfma_f32_16x16x32_bf16 v[196:199], v[182:185], a[172:175], v[196:199]\n"
      "v_cvt_pk_bf16_f32 v203, v114, v115\n"
      "v_cvt_pk_bf16_f32 v204, v204, v205\n"
      "v_mfma_f32_16x16x32_bf16 v[196:199], v[120:123], a[176:179], v[196:199]\n"
      "v_fmac_f32 v128, v186, v186\n"
      "v_mul_f32 v137, v108, v137\n"
      "v_mfma_f32_16x16x32_bf16 v[196:199], v[104:107], a[180:183], v[196:199]\n"
      "ds_write_b128 v5, v[200:203] offset:24576\n"
      "v_mfma_f32_16x16x32_bf16 v[196:199], v[190:193], a[184:187], v[196:199]\n"
      "buffer_load_dword v94, v29, s[28:31], s86 offen\n"
      "v_mfma_f32_16x16x32_bf16 v[196:199], v[152:155], a[188:191], v[196:199]\n"
      "ds_read_b128 v[112:115], v0 offset:32768\n"
      "v_cvt_pk_bf16_f32 v205, v206, v207\n"
      "v_mfma_f32_16x16x32_bf16 v[214:217], v[124:127], a[192:195], 0\n"
      "v_mul_f32 v149, v110, v149\n"
      "v_fmac_f32 v128, v187, v187\n"
      "v_mfma_f32_16x16x32_bf16 v[214:217], v[182:185], a[196:199], v[214:217]\n"
      "ds_read_b128 v[182:185], v1 offset:26624\n"
      "v_mul_f32 v147, v110, v147\n"
      "v_mfma_f32_16x16x32_bf16 v[214:217], v[120:123], a[200:203], v[214:217]\n"
      "ds_read_b128 v[120:123], v1 offset:32768\n"
      "v_fmac_f32 v128, v188, v188\n"
      "v_mfma_f32_16x16x32_bf16 v[214:217], v[104:107], a[204:207], v[214:217]\n"
      "v_fmac_f32 v129, v167, v167\n"
      "v_mul_f32 v168, v208, v168\n"
      "v_mfma_f32_16x16x32_bf16 v[214:217], v[190:193], a[208:211], v[214:217]\n"
      "ds_read_b128 v[116:119], v1 offset:22528\n"
      "v_fmac_f32_e64 v129, v138, v138\n"
      "v_mfma_f32_16x16x32_bf16 v[214:217], v[152:155], a[212:215], v[214:217]\n"
      "ds_write_b128 v5, v[96:99] offset:18432\n"
      "v_mfma_f32_16x16x32_bf16 v[150:153], a[0:3], v[142:145], 0\n"
      "ds_read_b128 v[190:193], v9 offset:4608\n"
      "v_fmac_f32 v129, v139, v139\n"
      "v_mfma_f32_16x16x32_bf16 v[150:153], a[4:7], v[220:223], v[150:153]\n"
      "v_fmac_f32 v128, v189, v189\n"
      "v_fmac_f32 v128, v174, v174\n"
      "v_mfma_f32_16x16x32_bf16 v[150:153], a[8:11], v[178:181], v[150:153]\n"
      "s_waitcnt lgkmcnt(3)\n"
      "v_cvt_pk_bf16_f32 v134, v134, v135\n"
      "v_cvt_pk_bf16_f32 v168, v168, v168\n"
      "v_mfma_f32_16x16x32_bf16 v[150:153], a[12:15], v[182:185], v[150:153]\n"
      "v_fmac_f32 v128, v175, v175\n"
      "v_fmac_f32 v129, v140, v140\n"
      "v_mfma_f32_16x16x32_bf16 v[150:153], a[16:19], v[112:115], v[150:153]\n"
      "v_cvt_pk_bf16_f32 v135, v136, v137\n"
      "v_fmac_f32 v128, v176, v176\n"
      "v_mfma_f32_16x16x32_bf16 v[150:153], a[20:23], v[120:123], v[150:153]\n"
      "v_readlane_b32 s46, v37, s42\n"
      "v_fmac_f32 v129, v141, v141\n"
      "v_mfma_f32_16x16x32_bf16 v[200:203], a[24:27], v[142:145], 0\n"
      "v_cvt_pk_bf16_f32 v196, v196, v197\n"
      "v_cvt_pk_bf16_f32 v160, v160, v161\n"
      "v_mfma_f32_16x16x32_bf16 v[200:203], a[28:31], v[220:223], v[200:203]\n"
      "v_fmac_f32 v128, v177, v177\n"
      "v_cvt_pk_bf16_f32 v158, v146, v147\n"
      "v_mfma_f32_16x16x32_bf16 v[200:203], a[32:35], v[178:181], v[200:203]\n"
      "v_permlane16_swap_b32 v129, v128\n"
      "v_mfma_f32_16x16x32_bf16 v[200:203], a[36:39], v[182:185], v[200:203]\n"
      "v_cvt_pk_bf16_f32 v161, v162, v163\n"
      "v_cvt_pk_bf16_f32 v214, v214, v215\n"
      "v_mfma_f32_16x16x32_bf16 v[200:203], a[40:43], v[112:115], v[200:203]\n"
      "ds_write_b64 v6, v[160:161] offset:30720\n"
      "v_mfma_f32_16x16x32_bf16 v[200:203], a[44:47], v[120:123], v[200:203]\n"
      "v_add_f32_e64 v129, v129, v128\n"
      "v_cvt_pk_bf16_f32 v215, v216, v217\n"
      "v_mfma_f32_16x16x32_bf16 v[206:209], a[48:51], v[142:145], 0\n"
      "v_fma_f32 v169, v150, v150, s51\n"
      "v_cvt_pk_bf16_f32 v130, v130, v131\n"
      "v_mfma_f32_16x16x32_bf16 v[206:209], a[52:55], v[220:223], v[206:209]\n"
      "v_fmac_f32 v169, v151, v151\n"
      "v_mov_b32 v128, v129\n"
      "v_mfma_f32_16x16x32_bf16 v[206:209], a[56:59], v[178:181], v[206:209]\n"
      "s_nop 0\n"
      "v_permlane32_swap_b32 v129, v128\n"
      "v_mfma_f32_16x16x32_bf16 v[206:209], a[60:63], v[182:185], v[206:209]\n"
      "v_add_f32 v129, v129, v128\n"
      "v_cvt_pk_bf16_f32 v159, v148, v149\n"
      "v_mfma_f32_16x16x32_bf16 v[206:209], a[64:67], v[112:115], v[206:209]\n"
      "v_rsq_f32 v129, v129\n"
      "v_mfma_f32_16x16x32_bf16 v[206:209], a[68:71], v[120:123], v[206:209]\n"
      "v_fmac_f32 v169, v152, v152\n"
      "v_mov_b32 v128, v129\n"
      "v_mfma_f32_16x16x32_bf16 v[160:163], a[72:75], v[142:145], 0\n"
      "s_nop 0\n"
      "v_permlane16_swap_b32 v129, v128\n"
      "v_mfma_f32_16x16x32_bf16 v[160:163], a[76:79], v[220:223], v[160:163]\n"
      "ds_read_b128 v[96:99], v1 offset:34816\n"
      "v_fmac_f32 v169, v153, v153\n"
      "v_mfma_f32_16x16x32_bf16 v[160:163], a[80:83], v[178:181], v[160:163]\n"
      "v_fmac_f32 v169, v200, v200\n"
      "v_mul_f32_e64 v129, s50, v129\n"
      "v_mfma_f32_16x16x32_bf16 v[160:163], a[84:87], v[182:185], v[160:163]\n"
      "v_mul_f32_e64 v166, v129, v166\n"
      "v_cvt_pk_bf16_f32 v197, v198, v199\n"
      "v_mfma_f32_16x16x32_bf16 v[160:163], a[88:91], v[112:115], v[160:163]\n"
      "ds_read_b128 v[108:111], v1 offset:28672\n"
      "v_fmac_f32 v169, v201, v201\n"
      "v_mfma_f32_16x16x32_bf16 v[160:163], a[92:95], v[120:123], v[160:163]\n"
      "v_fmac_f32 v169, v202, v202\n"
      "v_mul_f32_e64 v102, v129, v102\n"
      "v_mfma_f32_16x16x32_bf16 v[104:107], a[96:99], v[142:145], 0\n"
      "s_waitcnt lgkmcnt(2)\n"
      "v_mul_f32 v138, v129, v138\n"
      "v_or3_b32 v190, v190, v191, v192\n"
      "v_mfma_f32_16x16x32_bf16 v[104:107], a[100:103], v[220:223], v[104:107]\n"
      "v_mul_f32 v164, v129, v164\n"
      "v_mul_f32 v174, v128, v174\n"
      "v_mfma_f32_16x16x32_bf16 v[104:107], a[104:107], v[178:181], v[104:107]\n"
      "v_mul_f32_e64 v171, v128, v171\n"
      "v_fma_f32 v85, v160, v160, s51\n"
      "v_mfma_f32_16x16x32_bf16 v[104:107], a[108:111], v[182:185], v[104:107]\n"
      "v_fmac_f32 v85, v161, v161\n"
      "v_fmac_f32 v169, v203, v203\n"
      "v_mfma_f32_16x16x32_bf16 v[104:107], a[112:115], v[112:115], v[104:107]\n"
      "ds_write_b64 v6, v[134:135] offset:18432\n"
      "v_mfma_f32_16x16x32_bf16 v[104:107], a[116:119], v[120:123], v[104:107]\n"
      "v_cvt_pk_bf16_f32 v131, v132, v133\n"
      "v_fmac_f32 v85, v162, v162\n"
      "v_mfma_f32_16x16x32_bf16 v[124:127], a[120:123], v[142:145], 0\n"
      "v_fmac_f32 v85, v163, v163\n"
      "v_fmac_f32 v169, v206, v206\n"
      "v_mfma_f32_16x16x32_bf16 v[124:127], a[124:127], v[220:223], v[124:127]\n"
      "ds_write_b64 v6, v[204:205] offset:24576\n"
      "v_mfma_f32_16x16x32_bf16 v[124:127], a[128:131], v[178:181], v[124:127]\n"
      "v_mul_f32 v139, v129, v139\n"
      "v_fmac_f32 v169, v207, v207\n"
      "v_mfma_f32_16x16x32_bf16 v[124:127], a[132:135], v[182:185], v[124:127]\n"
      "v_fmac_f32 v85, v104, v104\n"
      "v_mul_f32 v173, v128, v173\n"
      "v_mfma_f32_16x16x32_bf16 v[124:127], a[136:139], v[112:115], v[124:127]\n"
      "v_mul_f32 v175, v128, v175\n"
      "v_mul_f32 v165, v129, v165\n"
      "v_mfma_f32_16x16x32_bf16 v[124:127], a[140:143], v[120:123], v[124:127]\n"
      "v_bitop3_b32 v190, v190, v193, s33 bitop3:0xa8\n"
      "v_fmac_f32 v85, v105, v105\n"
      "v_mfma_f32_16x16x32_bf16 v[216:219], v[142:145], a[144:147], 0\n"
      "v_mul_f32 v101, v129, v101\n"
      "v_mul_f32 v170, v128, v170\n"
      "v_mfma_f32_16x16x32_bf16 v[216:219], v[220:223], a[148:151], v[216:219]\n"
      "ds_write_b128 v5, v[156:159] offset:30720\n"
      "v_mfma_f32_16x16x32_bf16 v[216:219], v[178:181], a[152:155], v[216:219]\n"
      "v_mul_f32 v100, v129, v100\n"
      "v_fmac_f32 v85, v106, v106\n"
      "v_mfma_f32_16x16x32_bf16 v[216:219], v[182:185], a[156:159], v[216:219]\n"
      "v_fmac_f32_e64 v169, v208, v208\n"
      "v_cvt_pk_bf16_f32 v100, v100, v101\n"
      "v_mfma_f32_16x16x32_bf16 v[216:219], v[112:115], a[160:163], v[216:219]\n"
      "v_fmac_f32 v85, v107, v107\n"
      "v_fmac_f32 v85, v124, v124\n"
      "v_mfma_f32_16x16x32_bf16 v[216:219], v[120:123], a[164:167], v[216:219]\n"
      "v_mul_f32 v189, v128, v189\n"
      "v_fmac_f32 v85, v125, v125\n"
      "v_mfma_f32_16x16x32_bf16 v[192:195], v[142:145], a[168:171], 0\n"
      "v_fmac_f32 v169, v209, v209\n"
      "v_fmac_f32 v85, v126, v126\n"
      "v_mfma_f32_16x16x32_bf16 v[192:195], v[220:223], a[172:175], v[192:195]\n"
      "v_mul_f32 v167, v129, v167\n"
      "v_fmac_f32 v85, v127, v127\n"
      "v_mfma_f32_16x16x32_bf16 v[192:195], v[178:181], a[176:179], v[192:195]\n"
      "s_nop 0\n"
      "v_permlane16_swap_b32 v169, v85\n"
      "v_mfma_f32_16x16x32_bf16 v[192:195], v[182:185], a[180:183], v[192:195]\n"
      "v_add_f32 v169, v169, v85\n"
      "v_mul_f32 v176, v128, v176\n"
      "v_mfma_f32_16x16x32_bf16 v[192:195], v[112:115], a[184:187], v[192:195]\n"
      "v_mul_f32_e64 v140, v129, v140\n"
      "v_cvt_pk_bf16_f32 v132, v216, v217\n"
      "v_mfma_f32_16x16x32_bf16 v[192:195], v[120:123], a[188:191], v[192:195]\n"
      "ds_read_b128 v[146:149], v0 offset:22528\n"
      "v_mov_b32 v85, v169\n"
      "v_mfma_f32_16x16x32_bf16 v[156:159], v[142:145], a[192:195], 0\n"
      "ds_read_b128 v[134:137], v0 offset:28672\n"
      "v_mul_f32 v188, v128, v188\n"
      "v_mfma_f32_16x16x32_bf16 v[156:159], v[220:223], a[196:199], v[156:159]\n"
      "v_permlane32_swap_b32 v169, v85\n"
      "v_mfma_f32_16x16x32_bf16 v[156:159], v[178:181], a[200:203], v[156:159]\n"
      "ds_read_b128 v[178:181], v0 offset:34816\n"
      "v_add_f32 v169, v169, v85\n"
      "v_mfma_f32_16x16x32_bf16 v[156:159], v[182:185], a[204:207], v[156:159]\n"
      "v_rsq_f32 v169, v169\n"
      "v_mfma_f32_16x16x32_bf16 v[156:159], v[112:115], a[208:211], v[156:159]\n"
      "ds_read_u16 v78, v11 offset:1168\n"
      "v_mov_b32 v85, v169\n"
      "v_mfma_f32_16x16x32_bf16 v[156:159], v[120:123], a[212:215], v[156:159]\n"
      "s_waitcnt lgkmcnt(3)\n"
      "v_cvt_pk_bf16_f32 v170, v170, v171\n"
      "v_mul_f32 v172, v128, v172\n"
      "v_mfma_f32_16x16x32_bf16 v[182:185], a[0:3], v[146:149], 0\n"
      "s_waitcnt lgkmcnt(2)\n"
      "v_permlane16_swap_b32 v169, v85\n"
      "v_mfma_f32_16x16x32_bf16 v[182:185], a[4:7], v[116:119], v[182:185]\n"
      "s_or_b32 s95, s45, s46\n"
      "v_mul_f32 v103, v129, v103\n"
      "v_mul_f32_e64 v127, v85, v127\n"
      "v_mfma_f32_16x16x32_bf16 v[182:185], a[8:11], v[134:137], v[182:185]\n"
      "v_mul_f32 v187, v128, v187\n"
      "v_mul_f32 v169, s50, v169\n"
      "v_mfma_f32_16x16x32_bf16 v[182:185], a[12:15], v[108:111], v[182:185]\n"
      "s_waitcnt lgkmcnt(1)\n"
      "v_mul_f32 v124, v85, v124\n"
      "v_cvt_pk_bf16_f32 v101, v102, v103\n"
      "v_mfma_f32_16x16x32_bf16 v[182:185], a[16:19], v[178:181], v[182:185]\n"
      "v_cvt_pk_bf16_f32 v171, v172, v173\n"
      "v_mul_f32_e64 v104, v85, v104\n"
      "v_mfma_f32_16x16x32_bf16 v[182:185], a[20:23], v[96:99], v[182:185]\n"
      "v_mul_f32 v107, v85, v107\n"
      "v_mul_f32 v141, v129, v141\n"
      "v_mfma_f32_16x16x32_bf16 v[142:145], a[24:27], v[146:149], 0\n"
      "v_mul_f32 v125, v85, v125\n"
      "v_mul_f32 v150, v169, v150\n"
      "v_mfma_f32_16x16x32_bf16 v[142:145], a[28:31], v[116:119], v[142:145]\n"
      "ds_write_b16 v12, v168\n"
      "v_mfma_f32_16x16x32_bf16 v[142:145], a[32:35], v[134:137], v[142:145]\n"
      "v_cvt_pk_bf16_f32 v173, v188, v189\n"
      "v_mul_f32 v177, v128, v177\n"
      "v_mfma_f32_16x16x32_bf16 v[142:145], a[36:39], v[108:111], v[142:145]\n"
      "v_mul_f32 v151, v169, v151\n"
      "v_cvt_pk_bf16_f32 v103, v166, v167\n"
      "v_mfma_f32_16x16x32_bf16 v[142:145], a[40:43], v[178:181], v[142:145]\n"
      "v_fma_f32 v191, v182, v182, s51\n"
      "v_mul_f32 v161, v85, v161\n"
      "v_mfma_f32_16x16x32_bf16 v[142:145], a[44:47], v[96:99], v[142:145]\n"
      "v_mul_f32 v106, v85, v106\n"
      "v_mul_f32_e64 v160, v85, v160\n"
      "v_mfma_f32_16x16x32_bf16 v[220:223], a[48:51], v[146:149], 0\n"
      "v_mul_f32 v126, v85, v126\n"
      "v_mul_f32 v200, v169, v200\n"
      "v_mfma_f32_16x16x32_bf16 v[220:223], a[52:55], v[116:119], v[220:223]\n"
      "v_cmp_ne_u32 s[56:57], 0, v190\n"
      "v_mul_f32 v186, v128, v186\n"
      "v_mfma_f32_16x16x32_bf16 v[220:223], a[56:59], v[134:137], v[220:223]\n"
      "v_cvt_pk_bf16_f32 v216, v156, v157\n"
      "v_mul_f32 v209, v169, v209\n"
      "v_mfma_f32_16x16x32_bf16 v[220:223], a[60:63], v[108:111], v[220:223]\n"
      "v_fmac_f32 v191, v183, v183\n"
      "v_cvt_pk_bf16_f32 v172, v186, v187\n"
      "v_mfma_f32_16x16x32_bf16 v[220:223], a[64:67], v[178:181], v[220:223]\n"
      "v_cvt_pk_bf16_f32 v150, v150, v151\n"
      "v_mul_f32 v163, v85, v163\n"
      "v_mfma_f32_16x16x32_bf16 v[220:223], a[68:71], v[96:99], v[220:223]\n"
      "v_mul_f32 v203, v169, v203\n"
      "v_cvt_pk_bf16_f32 v124, v124, v125\n"
      "v_mfma_f32_16x16x32_bf16 v[120:123], a[72:75], v[146:149], 0\n"
      "v_mul_f32 v152, v169, v152\n"
      "v_fmac_f32 v191, v184, v184\n"
      "v_mfma_f32_16x16x32_bf16 v[120:123], a[76:79], v[116:119], v[120:123]\n"
      "v_fmac_f32 v191, v185, v185\n"
      "v_cvt_pk_bf16_f32 v125, v126, v127\n"
      "v_mfma_f32_16x16x32_bf16 v[120:123], a[80:83], v[134:137], v[120:123]\n"
      "v_mul_f32_e64 v207, v169, v207\n"
      "v_mul_f32_e64 v153, v169, v153\n"
      "v_mfma_f32_16x16x32_bf16 v[120:123], a[84:87], v[108:111], v[120:123]\n"
      "v_cvt_pk_bf16_f32 v133, v218, v219\n"
      "v_fmac_f32 v191, v142, v142\n"
      "v_mfma_f32_16x16x32_bf16 v[120:123], a[88:91], v[178:181], v[120:123]\n"
      "s_or_b32 s95, s95, s48\n"
      "v_cvt_pk_bf16_f32 v199, v194, v195\n"
      "v_cvt_pk_bf16_f32 v160, v160, v161\n"
      "v_mfma_f32_16x16x32_bf16 v[120:123], a[92:95], v[96:99], v[120:123]\n"
      "v_cvt_pk_bf16_f32 v174, v174, v175\n"
      "v_fmac_f32 v191, v143, v143\n"
      "v_mfma_f32_16x16x32_bf16 v[154:157], a[96:99], v[146:149], 0\n"
      "v_fmac_f32 v191, v144, v144\n"
      "v_mul_f32 v206, v169, v206\n"
      "v_mfma_f32_16x16x32_bf16 v[154:157], a[100:103], v[116:119], v[154:157]\n"
      "v_mul_f32 v162, v85, v162\n"
      "v_cvt_pk_bf16_f32 v138, v138, v139\n"
      "v_mfma_f32_16x16x32_bf16 v[154:157], a[104:107], v[134:137], v[154:157]\n"
      "v_mul_f32_e64 v201, v169, v201\n"
      "v_fma_f32 v190, v120, v120, s51\n"
      "v_mfma_f32_16x16x32_bf16 v[154:157], a[108:111], v[108:111], v[154:157]\n"
      "v_cvt_pk_bf16_f32 v102, v164, v165\n"
      "v_cvt_pk_bf16_f32 v198, v192, v193\n"
      "v_mfma_f32_16x16x32_bf16 v[154:157], a[112:115], v[178:181], v[154:157]\n"
      "v_fmac_f32 v190, v121, v121\n"
      "v_fmac_f32 v191, v145, v145\n"
      "v_mfma_f32_16x16x32_bf16 v[154:157], a[116:119], v[96:99], v[154:157]\n"
      "v_fmac_f32 v191, v220, v220\n"
      "v_fmac_f32 v191, v221, v221\n"
      "v_mfma_f32_16x16x32_bf16 v[126:129], a[120:123], v[146:149], 0\n"
      "v_fmac_f32_e64 v190, v122, v122\n"
      "v_cvt_pk_bf16_f32 v151, v152, v153\n"
      "v_mfma_f32_16x16x32_bf16 v[126:129], a[124:127], v[116:119], v[126:129]\n"
      "v_fmac_f32 v190, v123, v123\n"
      "v_mul_f32 v105, v85, v105\n"
      "v_mfma_f32_16x16x32_bf16 v[126:129], a[128:131], v[134:137], v[126:129]\n"
      "v_mul_f32_e64 v202, v169, v202\n"
      "v_cvt_pk_bf16_f32 v161, v162, v163\n"
      "v_mfma_f32_16x16x32_bf16 v[126:129], a[132:135], v[108:111], v[126:129]\n"
      "v_cvt_pk_bf16_f32 v217, v158, v159\n"
      "v_cvt_pk_bf16_f32 v162, v104, v105\n"
      "v_mfma_f32_16x16x32_bf16 v[126:129], a[136:139], v[178:181], v[126:129]\n"
      "v_cvt_pk_bf16_f32 v175, v176, v177\n"
      "v_fmac_f32 v191, v222, v222\n"
      "v_mfma_f32_16x16x32_bf16 v[126:129], a[140:143], v[96:99], v[126:129]\n"
      "v_fmac_f32 v190, v154, v154\n"
      "v_cvt_pk_bf16_f32 v152, v200, v201\n"
      "v_mfma_f32_16x16x32_bf16 v[192:195], v[146:149], a[144:147], 0\n"
      "v_fmac_f32 v190, v155, v155\n"
      "v_fmac_f32 v190, v156, v156\n"
      "v_mfma_f32_16x16x32_bf16 v[192:195], v[116:119], a[148:151], v[192:195]\n"
      "v_cvt_pk_bf16_f32 v153, v202, v203\n"
      "v_fmac_f32 v190, v157, v157\n"
      "v_mfma_f32_16x16x32_bf16 v[192:195], v[134:137], a[152:155], v[192:195]\n"
      "v_mul_f32 v208, v169, v208\n"
      "v_fmac_f32_e64 v190, v126, v126\n"
      "v_mfma_f32_16x16x32_bf16 v[192:195], v[108:111], a[156:159], v[192:195]\n"
      "ds_read_b128 v[200:203], v2\n"
      "v_fmac_f32 v190, v127, v127\n"
      "v_mfma_f32_16x16x32_bf16 v[192:195], v[178:181], a[160:163], v[192:195]\n"
      "v_fmac_f32 v191, v223, v223\n"
      "v_fmac_f32 v190, v128, v128\n"
      "v_mfma_f32_16x16x32_bf16 v[192:195], v[96:99], a[164:167], v[192:195]\n"
      "v_fmac_f32 v190, v129, v129\n"
      "v_cvt_pk_bf16_f32 v206, v206, v207\n"
      "v_mfma_f32_16x16x32_bf16 v[166:169], v[146:149], a[168:171], 0\n"
      "v_permlane16_swap_b32 v191, v190\n"
      "v_mfma_f32_16x16x32_bf16 v[166:169], v[116:119], a[172:175], v[166:169]\n"
      "v_cvt_pk_bf16_f32 v163, v106, v107\n"
      "v_add_f32 v191, v191, v190\n"
      "v_mfma_f32_16x16x32_bf16 v[166:169], v[134:137], a[176:179], v[166:169]\n"
      "ds_read_b128 v[112:115], v2 offset:6144\n"
      "v_mov_b32 v190, v191\n"
      "v_mfma_f32_16x16x32_bf16 v[166:169], v[108:111], a[180:183], v[166:169]\n"
      "s_nop 0\n"
      "v_permlane32_swap_b32_e64 v191, v190\n"
      "v_mfma_f32_16x16x32_bf16 v[166:169], v[178:181], a[184:187], v[166:169]\n"
      "s_cmp_ge_u32 s95, 0x40000000\n"
      "s_cselect_b32 s94, -1, -2\n"
      "v_add_f32 v191, v191, v190\n"
      "v_mfma_f32_16x16x32_bf16 v[166:169], v[96:99], a[188:191], v[166:169]\n"
      "v_rsq_f32 v191, v191\n"
      "v_mfma_f32_16x16x32_bf16 v[104:107], v[146:149], a[192:195], 0\n"
      "ds_read_b128 v[146:149], v2 offset:3072\n"
      "v_mov_b32 v190, v191\n"
      "v_mfma_f32_16x16x32_bf16 v[104:107], v[116:119], a[196:199], v[104:107]\n"
      "s_nop 0\n"
      "v_permlane16_swap_b32 v191, v190\n"
      "v_mfma_f32_16x16x32_bf16 v[104:107], v[134:137], a[200:203], v[104:107]\n"
      "ds_read_b128 v[186:189], v10 offset:1200\n"
      "v_mul_f32 v191, s50, v191\n"
      "v_mfma_f32_16x16x32_bf16 v[104:107], v[108:111], a[204:207], v[104:107]\n"
      "v_mul_f32 v120, v190, v120\n"
      "v_mul_f32 v154, v190, v154\n"
      "v_mfma_f32_16x16x32_bf16 v[104:107], v[178:181], a[208:211], v[104:107]\n"
      "ds_read_b64 v[218:219], v10 offset:1192\n"
      "v_mul_f32_e64 v121, v190, v121\n"
      "v_mfma_f32_16x16x32_bf16 v[104:107], v[96:99], a[212:215], v[104:107]\n"
      "s_waitcnt lgkmcnt(3)\n"
      "v_mul_f32 v156, v190, v156\n"
      "v_mul_f32 v129, v190, v129\n"
      "v_mfma_f32_16x16x32_bf16 v[96:99], a[216:219], v[200:203], 0\n"
      "s_waitcnt lgkmcnt(2)\n"
      "v_mul_f32 v157, v190, v157\n"
      "v_cvt_pk_bf16_f32 v139, v140, v141\n"
      "v_mfma_f32_16x16x32_bf16 v[96:99], a[220:223], v[146:149], v[96:99]\n"
      "v_mul_f32 v123, v190, v123\n"
      "v_mul_f32_e64 v155, v190, v155\n"
      "v_mfma_f32_16x16x32_bf16 v[96:99], a[224:227], v[112:115], v[96:99]\n"
      "ds_read_b128 v[176:179], v2 offset:9216\n"
      "v_mul_f32 v122, v190, v122\n"
      "v_mfma_f32_16x16x32_bf16 v[116:119], a[240:243], v[200:203], 0\n"
      "s_cmp_gt_u32 s42, s44\n"
      "s_cselect_b32 s95, s94, s95\n"
      "v_cvt_pk_bf16_f32 v85, v106, v107\n"
      "v_mfma_f32_16x16x32_bf16 v[116:119], a[244:247], v[146:149], v[116:119]\n"
      "v_mul_f32 v128, v190, v128\n"
      "v_mul_f32_e64 v182, v191, v182\n"
      "v_mfma_f32_16x16x32_bf16 v[116:119], a[248:251], v[112:115], v[116:119]\n"
      "v_cvt_pk_bf16_f32 v120, v120, v121\n"
      "v_cvt_pk_bf16_f32 v121, v122, v123\n"
      "v_mfma_f32_16x16x32_bf16 v[134:137], v[232:235], v[200:203], 0\n"
      "ds_read_u16 v82, v11 offset:1200\n"
      "v_cvt_pk_bf16_f32 v84, v104, v105\n"
      "v_mfma_f32_16x16x32_bf16 v[134:137], v[236:239], v[146:149], v[134:137]\n"
      "v_cvt_pk_bf16_f32 v123, v156, v157\n"
      "v_cvt_pk_bf16_f32 v122, v154, v155\n"
      "v_mfma_f32_16x16x32_bf16 v[134:137], v[240:243], v[112:115], v[134:137]\n"
      "v_cvt_pk_bf16_f32 v77, v194, v195\n"
      "v_cvt_pk_bf16_f32 v80, v166, v167\n"
      "v_mfma_f32_16x16x32_bf16 v[146:149], v[170:173], v[100:103], v[40:43]\n"
      "s_waitcnt lgkmcnt(1)\n"
      "v_cvt_pk_bf16_f32 v76, v192, v193\n"
      "v_mul_f32 v126, v190, v126\n"
      "v_mfma_f32_16x16x32_bf16 v[104:107], v[160:163], v[100:103], v[44:47]\n"
      "ds_read_b128 v[200:203], v2 offset:12288\n"
      "v_cvt_pk_bf16_f32 v207, v208, v209\n"
      "v_mfma_f32_16x16x32_bf16 v[108:111], v[120:123], v[100:103], v[48:51]\n"
      "ds_read_b128 v[112:115], v2 offset:15360\n"
      "v_cvt_pk_bf16_f32 v81, v168, v169\n"
      "v_mfma_f32_16x16x32_bf16 v[164:167], v[186:189], v[100:103], v[24:27]\n"
      "v_mul_f32 v145, v191, v145\n"
      "v_mul_f32 v127, v190, v127\n"
      "v_mfma_f32_16x16x16_bf16 v[146:149], v[174:175], v[138:139], v[146:149]\n"
      "v_mul_f32 v184, v191, v184\n"
      "v_cvt_pk_bf16_f32 v126, v126, v127\n"
      "v_mul_f32 v221, v191, v221\n"
      "v_mfma_f32_16x16x16_bf16 v[104:107], v[124:125], v[138:139], v[104:107]\n"
      "v_cvt_pk_bf16_f32 v127, v128, v129\n"
      "v_mul_f32 v144, v191, v144\n"
      "v_mul_f32 v220, v191, v220\n"
      "v_mfma_f32_16x16x16_bf16 v[108:111], v[126:127], v[138:139], v[108:111]\n"
      "v_mul_f32 v185, v191, v185\n"
      "v_mul_f32 v183, v191, v183\n"
      "v_max3_f32 v181, v146, v147, v148\n"
      "v_mfma_f32_16x16x16_bf16 v[164:167], v[218:219], v[138:139], v[164:167]\n"
      "v_max3_f32 v169, v149, v104, v105\n"
      "v_max3_f32 v181, v181, v106, v107\n"
      "v_cvt_pk_bf16_f32 v220, v220, v221\n"
      "v_mfma_f32_16x16x32_bf16 v[96:99], a[228:231], v[176:179], v[96:99]\n"
      "s_waitcnt lgkmcnt(0)\n"
      "v_max3_f32 v169, v169, v108, v109\n"
      "v_max3_f32 v181, v181, v110, v111\n"
      "v_mfma_f32_16x16x32_bf16 v[96:99], a[232:235], v[200:203], v[96:99]\n"
      "s_add_u32 s42, s42, 1\n"
      "v_max3_f32 v181, v181, v169, v164\n"
      "v_mov_b32 v138, v181\n"
      "v_mfma_f32_16x16x32_bf16 v[96:99], a[236:239], v[112:115], v[96:99]\n"
      "s_nop 0\n"
      "v_permlane16_swap_b32 v181, v138\n"
      "v_mfma_f32_16x16x32_bf16 v[116:119], a[252:255], v[176:179], v[116:119]\n"
      "ds_read_b128 v[166:169], v2 offset:3328\n"
      "v_max_f32 v181, v181, v138\n"
      "v_mfma_f32_16x16x32_bf16 v[116:119], v[224:227], v[200:203], v[116:119]\n"
      "v_cvt_pk_bf16_f32 v182, v182, v183\n"
      "v_mov_b32 v138, v181\n"
      "v_mfma_f32_16x16x32_bf16 v[116:119], v[228:231], v[112:115], v[116:119]\n"
      "s_nop 0\n"
      "v_permlane32_swap_b32_e64 v181, v138\n"
      "v_max_f32 v181, v181, v138\n"
      "v_cvt_pk_bf16_f32 v183, v184, v185\n"
      "s_waitcnt vmcnt(1)\n"
      "v_or3_b32 v210, v210, v211, v212\n"
      "v_pk_add_f32 v[106:107], v[106:107], v[180:181] op_sel:[0,1] neg_lo:[0,1] neg_hi:[0,1]\n"
      "v_pk_add_f32 v[104:105], v[104:105], v[180:181] op_sel:[0,1] neg_lo:[0,1] neg_hi:[0,1]\n"
      "v_pk_add_f32 v[148:149], v[148:149], v[180:181] op_sel:[0,1] neg_lo:[0,1] neg_hi:[0,1]\n"
      "v_pk_mul_f32 v[142:143], v[142:143], v[190:191] op_sel:[0,1]\n"
      "v_pk_add_f32 v[110:111], v[110:111], v[180:181] op_sel:[0,1] neg_lo:[0,1] neg_hi:[0,1]\n"
      "v_pk_add_f32 v[146:147], v[146:147], v[180:181] op_sel:[0,1] neg_lo:[0,1] neg_hi:[0,1]\n"
      "v_pk_add_f32 v[108:109], v[108:109], v[180:181] op_sel:[0,1] neg_lo:[0,1] neg_hi:[0,1]\n"
      "v_pk_mul_f32 v[222:223], v[222:223], v[190:191] op_sel:[0,1]\n"
      "v_mfma_f32_16x16x32_bf16 v[134:137], v[244:247], v[176:179], v[134:137]\n"
      "ds_read_b128 v[138:141], v2 offset:256\n"
      "v_cvt_pk_bf16_f32 v184, v142, v143\n"
      "v_mfma_f32_16x16x32_bf16 v[134:137], v[248:251], v[200:203], v[134:137]\n"
      "v_exp_f32 v104, v104\n"
      "v_mfma_f32_16x16x32_bf16 v[134:137], v[252:255], v[112:115], v[134:137]\n"
      "v_exp_f32 v146, v146\n"
      "v_mfma_f32_16x16x32_bf16 v[100:103], v[170:173], v[150:153], v[52:55]\n"
      "ds_read_b128 v[154:157], v2 offset:6400\n"
      "v_cvt_pk_bf16_f32 v185, v144, v145\n"
      "v_mfma_f32_16x16x32_bf16 v[190:193], v[160:163], v[150:153], v[56:59]\n"
      "v_exp_f32 v106, v106\n"
      "v_mfma_f32_16x16x32_bf16 v[142:145], v[120:123], v[150:153], v[60:63]\n"
      "v_exp_f32 v108, v108\n"
      "v_mfma_f32_16x16x32_bf16 v[176:179], v[186:189], v[150:153], v[26:29]\n"
      "s_waitcnt lgkmcnt(1)\n"
      "v_exp_f32 v147, v147\n"
      "v_mfma_f32_16x16x16_bf16 v[100:103], v[174:175], v[206:207], v[100:103]\n"
      "v_cvt_pk_bf16_f32 v221, v222, v223\n"
      "v_exp_f32_e64 v111, v111\n"
      "v_mfma_f32_16x16x16_bf16 v[190:193], v[124:125], v[206:207], v[190:193]\n"
      "v_cvt_pk_bf16_f32 v146, v146, v147\n"
      "v_exp_f32 v110, v110\n"
      "v_mfma_f32_16x16x16_bf16 v[142:145], v[126:127], v[206:207], v[142:145]\n"
      "v_mul_f32 v195, v97, v97\n"
      "v_exp_f32 v148, v148\n"
      "v_mfma_f32_16x16x16_bf16 v[176:179], v[218:219], v[206:207], v[176:179]\n"
      "v_cvt_pk_bf16_f32 v89, v110, v111\n"
      "v_exp_f32 v149, v149\n"
      "v_mfma_f32_16x16x32_bf16 v[200:203], a[216:219], v[138:141], 0\n"
      "v_sub_f32 v164, v164, v181\n"
      "v_cvt_pk_bf16_f32 v147, v148, v149\n"
      "v_mfma_f32_16x16x32_bf16 v[200:203], a[220:223], v[166:169], v[200:203]\n"
      "s_waitcnt lgkmcnt(0)\n"
      "v_exp_f32_e64 v107, v107\n"
      "v_mfma_f32_16x16x32_bf16 v[200:203], a[224:227], v[154:157], v[200:203]\n"
      "v_exp_f32 v164, v164\n"
      "v_mfma_f32_16x16x32_bf16 v[112:115], a[240:243], v[138:141], 0\n"
      "v_exp_f32 v105, v105\n"
      "v_mfma_f32_16x16x32_bf16 v[112:115], a[244:247], v[166:169], v[112:115]\n"
      "v_cvt_pk_bf16_f32 v149, v106, v107\n"
      "v_cvt_pk_bf16_f32 v90, v164, 0\n"
      "v_mfma_f32_16x16x32_bf16 v[112:115], a[248:251], v[154:157], v[112:115]\n"
      "ds_read_u16 v86, v11 offset:1232\n"
      "v_cvt_pk_bf16_f32 v148, v104, v105\n"
      "v_mfma_f32_16x16x32_bf16 v[204:207], v[232:235], v[138:141], 0\n"
      "v_exp_f32_e64 v109, v109\n"
      "v_mfma_f32_16x16x32_bf16 v[204:207], v[236:239], v[166:169], v[204:207]\n"
      "v_max3_f32 v104, v100, v101, v102\n"
      "v_max3_f32 v167, v103, v190, v191\n"
      "v_mfma_f32_16x16x32_bf16 v[204:207], v[240:243], v[154:157], v[204:207]\n"
      "v_cvt_pk_bf16_f32 v88, v108, v109\n"
      "v_max3_f32 v104, v104, v192, v193\n"
      "v_mfma_f32_16x16x32_bf16 v[156:159], v[32:35], v[146:149], 0\n"
      "v_max3_f32 v167, v167, v142, v143\n"
      "v_max3_f32 v104, v104, v144, v145\n"
      "v_mfma_f32_16x16x32_bf16 v[156:159], v[32:35], v[88:91], v[156:159]\n"
      "s_waitcnt vmcnt(0)\n"
      "v_or3_b32 v210, v210, v213, v94\n"
      "v_max3_f32 v104, v104, v167, v176\n"
      "v_mfma_f32_16x16x32_bf16 v[178:181], v[130:133], v[146:149], 0\n"
      "ds_read_b128 v[164:167], v2 offset:12544\n"
      "v_mov_b32 v155, v104\n"
      "v_mfma_f32_16x16x32_bf16 v[178:181], v[76:79], v[88:91], v[178:181]\n"
      "s_nop 0\n"
      "v_permlane16_swap_b32 v104, v155\n"
      "v_mfma_f32_16x16x32_bf16 v[138:141], v[196:199], v[146:149], 0\n"
      "v_max_f32 v104, v104, v155\n"
      "v_mov_b32 v155, v104\n"
      "v_mfma_f32_16x16x32_bf16 v[138:141], v[80:83], v[88:91], v[138:141]\n"
      "v_rcp_f32_e64 v156, v156\n"
      "v_mfma_f32_16x16x32_bf16 v[150:153], v[214:217], v[146:149], 0\n"
      "s_waitcnt lgkmcnt(1)\n"
      "v_permlane32_swap_b32 v104, v155\n"
      "v_mfma_f32_16x16x32_bf16 v[150:153], v[84:87], v[88:91], v[150:153]\n"
      "ds_read_b128 v[146:149], v2 offset:9472\n"
      "v_max_f32 v104, v104, v155\n"
      "v_sub_f32 v176, v176, v104\n"
      "v_exp_f32_e64 v176, v176\n"
      "v_pk_mul_f32 v[140:141], v[140:141], v[156:157] op_sel_hi:[1,0]\n"
      "v_pk_mul_f32 v[178:179], v[178:179], v[156:157] op_sel_hi:[1,0]\n"
      "v_pk_mul_f32 v[180:181], v[180:181], v[156:157] op_sel_hi:[1,0]\n"
      "v_cvt_pk_bf16_f32 v178, v178, v179\n"
      "v_pk_mul_f32 v[150:151], v[150:151], v[156:157] op_sel_hi:[1,0]\n"
      "v_pk_mul_f32 v[138:139], v[138:139], v[156:157] op_sel_hi:[1,0]\n"
      "v_cvt_pk_bf16_f32 v179, v180, v181\n"
      "v_cvt_pk_bf16_f32 v150, v150, v151\n"
      "v_fma_f32 v194, v96, v96, s52\n"
      "v_pk_mul_f32 v[152:153], v[152:153], v[156:157] op_sel_hi:[1,0]\n"
      "v_pk_add_f32 v[190:191], v[190:191], v[104:105] op_sel_hi:[1,0] neg_lo:[0,1] neg_hi:[0,1]\n"
      "v_pk_add_f32 v[144:145], v[144:145], v[104:105] op_sel_hi:[1,0] neg_lo:[0,1] neg_hi:[0,1]\n"
      "v_cvt_pk_bf16_f32 v151, v152, v153\n"
      "v_cvt_pk_bf16_f32 v181, v140, v141\n"
      "v_pk_add_f32 v[192:193], v[192:193], v[104:105] op_sel_hi:[1,0] neg_lo:[0,1] neg_hi:[0,1]\n"
      "v_pk_add_f32 v[142:143], v[142:143], v[104:105] op_sel_hi:[1,0] neg_lo:[0,1] neg_hi:[0,1]\n"
      "v_pk_fma_f32 v[194:195], v[98:99], v[98:99], v[194:195]\n"
      "v_cvt_pk_bf16_f32 v180, v138, v139\n"
      "v_exp_f32_e64 v192, v192\n"
      "v_pk_add_f32 v[100:101], v[100:101], v[104:105] op_sel_hi:[1,0] neg_lo:[0,1] neg_hi:[0,1]\n"
      "v_pk_add_f32 v[102:103], v[102:103], v[104:105] op_sel_hi:[1,0] neg_lo:[0,1] neg_hi:[0,1]\n"
      "v_mfma_f32_16x16x32_bf16 v[138:141], v[170:173], v[182:185], v[64:67]\n"
      "ds_write_b64 v4, v[150:151] offset:18432\n"
      "v_mfma_f32_16x16x32_bf16 v[152:155], v[160:163], v[182:185], v[68:71]\n"
      "s_waitcnt lgkmcnt(1)\n"
      "v_exp_f32 v102, v102\n"
      "v_mfma_f32_16x16x32_bf16 v[104:107], v[120:123], v[182:185], v[72:75]\n"
      "ds_read_b128 v[156:159], v2 offset:15616\n"
      "v_exp_f32_e64 v144, v144\n"
      "v_mfma_f32_16x16x32_bf16 v[108:111], v[186:189], v[182:185], v[28:31]\n"
      "ds_write_b128 v3, v[178:181] offset:18432\n"
      "v_mfma_f32_16x16x16_bf16 v[138:141], v[174:175], v[220:221], v[138:141]\n"
      "v_exp_f32 v100, v100\n"
      "v_exp_f32 v101, v101\n"
      "v_mfma_f32_16x16x16_bf16 v[152:155], v[124:125], v[220:221], v[152:155]\n"
      "v_cvt_pk_bf16_f32 v100, v100, v101\n"
      "v_exp_f32 v143, v143\n"
      "v_mfma_f32_16x16x16_bf16 v[104:107], v[126:127], v[220:221], v[104:107]\n"
      "v_exp_f32 v142, v142\n"
      "v_exp_f32 v190, v190\n"
      "v_mfma_f32_16x16x16_bf16 v[108:111], v[218:219], v[220:221], v[108:111]\n"
      "v_exp_f32 v191, v191\n"
      "v_max3_f32 v88, v138, v139, v140\n"
      "v_mfma_f32_16x16x32_bf16 v[200:203], a[228:231], v[146:149], v[200:203]\n"
      "v_exp_f32 v103, v103\n"
      "v_mfma_f32_16x16x32_bf16 v[200:203], a[232:235], v[164:167], v[200:203]\n"
      "s_waitcnt lgkmcnt(1)\n"
      "v_cvt_pk_bf16_f32 v94, v176, 0\n"
      "v_cvt_pk_bf16_f32 v101, v102, v103\n"
      "v_mfma_f32_16x16x32_bf16 v[200:203], a[236:239], v[156:159], v[200:203]\n"
      "v_exp_f32_e64 v145, v145\n"
      "v_mfma_f32_16x16x32_bf16 v[112:115], a[252:255], v[146:149], v[112:115]\n"
      "v_cvt_pk_bf16_f32 v102, v190, v191\n"
      "v_cvt_pk_bf16_f32 v92, v142, v143\n"
      "v_mfma_f32_16x16x32_bf16 v[112:115], v[224:227], v[164:167], v[112:115]\n"
      "v_max3_f32 v88, v88, v154, v155\n"
      "v_max3_f32 v180, v141, v152, v153\n"
      "v_mfma_f32_16x16x32_bf16 v[112:115], v[228:231], v[156:159], v[112:115]\n"
      "v_exp_f32_e64 v193, v193\n"
      "v_mfma_f32_16x16x32_bf16 v[204:207], v[244:247], v[146:149], v[204:207]\n"
      "v_max3_f32 v88, v88, v106, v107\n"
      "v_max3_f32 v180, v180, v104, v105\n"
      "v_mfma_f32_16x16x32_bf16 v[204:207], v[248:251], v[164:167], v[204:207]\n"
      "ds_read_b128 v[146:149], v7\n"
      "v_cvt_pk_bf16_f32 v103, v192, v193\n"
      "v_mfma_f32_16x16x32_bf16 v[204:207], v[252:255], v[156:159], v[204:207]\n"
      "v_cvt_pk_bf16_f32 v93, v144, v145\n"
      "v_max3_f32 v88, v88, v180, v108\n"
      "v_mfma_f32_16x16x32_bf16 v[180:183], v[32:35], v[100:103], 0\n"
      "ds_read_b64 v[192:193], v10 offset:1184\n"
      "v_mov_b32_e64 v144, v88\n"
      "v_mfma_f32_16x16x32_bf16 v[180:183], v[32:35], v[92:95], v[180:183]\n"
      "ds_read_b128 v[156:159], v10 offset:1168\n"
      "v_permlane16_swap_b32 v88, v144\n"
      "v_mfma_f32_16x16x32_bf16 v[176:179], v[130:133], v[100:103], 0\n"
      "v_max_f32 v88, v88, v144\n"
      "v_mov_b32 v144, v88\n"
      "v_mfma_f32_16x16x32_bf16 v[176:179], v[76:79], v[92:95], v[176:179]\n"
      "s_nop 0\n"
      "v_permlane32_swap_b32 v88, v144\n"
      "v_mfma_f32_16x16x32_bf16 v[166:169], v[196:199], v[100:103], 0\n"
      "s_waitcnt lgkmcnt(2)\n"
      "v_rcp_f32_e64 v180, v180\n"
      "v_mfma_f32_16x16x32_bf16 v[166:169], v[80:83], v[92:95], v[166:169]\n"
      "ds_read_b128 v[220:223], v7 offset:4096\n"
      "v_max_f32_e64 v88, v88, v144\n"
      "v_mfma_f32_16x16x32_bf16 v[142:145], v[214:217], v[100:103], 0\n"
      "buffer_store_dwordx4 v[146:149], v20, s[24:27], s85 offen sc1\n"
      "v_pk_add_f32 v[138:139], v[138:139], v[88:89] op_sel_hi:[1,0] neg_lo:[0,1] neg_hi:[0,1]\n"
      "v_pk_mul_f32 v[178:179], v[178:179], v[180:181] op_sel_hi:[1,0]\n"
      "v_pk_add_f32 v[154:155], v[154:155], v[88:89] op_sel_hi:[1,0] neg_lo:[0,1] neg_hi:[0,1]\n"
      "v_pk_fma_f32 v[194:195], v[116:117], v[116:117], v[194:195]\n"
      "v_pk_mul_f32 v[176:177], v[176:177], v[180:181] op_sel_hi:[1,0]\n"
      "v_pk_add_f32 v[106:107], v[106:107], v[88:89] op_sel_hi:[1,0] neg_lo:[0,1] neg_hi:[0,1]\n"
      "v_pk_mul_f32 v[166:167], v[166:167], v[180:181] op_sel_hi:[1,0]\n"
      "v_pk_fma_f32 v[194:195], v[118:119], v[118:119], v[194:195]\n"
      "v_pk_add_f32 v[152:153], v[152:153], v[88:89] op_sel_hi:[1,0] neg_lo:[0,1] neg_hi:[0,1]\n"
      "v_mfma_f32_16x16x32_bf16 v[142:145], v[84:87], v[92:95], v[142:145]\n"
      "s_waitcnt lgkmcnt(1)\n"
      "v_cvt_pk_bf16_f32 v176, v176, v177\n"
      "v_cvt_pk_bf16_f32 v177, v178, v179\n"
      "v_mfma_f32_16x16x32_bf16 v[182:185], v[156:159], v[170:173], v[30:33]\n"
      "s_waitcnt lgkmcnt(0)\n"
      "v_exp_f32_e64 v138, v138\n"
      "v_mfma_f32_16x16x32_bf16 v[146:149], v[156:159], v[160:163], v[30:33]\n"
      "ds_read_b128 v[170:173], v2 offset:512\n"
      "v_cvt_pk_bf16_f32 v178, v166, v167\n"
      "v_mfma_f32_16x16x32_bf16 v[160:163], v[156:159], v[120:123], v[30:33]\n"
      "ds_read_b128 v[164:167], v2 offset:6656\n"
      "v_fma_f32 v110, v200, v200, s52\n"
      "v_mfma_f32_16x16x32_bf16 v[120:123], v[156:159], v[186:189], v[30:33]\n"
      "ds_read_b128 v[100:103], v2 offset:3584\n"
      "v_sub_f32 v108, v108, v88\n"
      "v_mfma_f32_16x16x16_bf16 v[182:185], v[192:193], v[174:175], v[182:185]\n"
      "buffer_store_dwordx4 v[220:223], v21, s[24:27], s85 offen sc1\n"
      "v_exp_f32 v108, v108\n"
      "v_mul_f32_e64 v111, v201, v201\n"
      "v_pk_fma_f32 v[110:111], v[202:203], v[202:203], v[110:111]\n"
      "v_pk_mul_f32 v[168:169], v[168:169], v[180:181] op_sel_hi:[1,0]\n"
      "v_pk_fma_f32 v[110:111], v[112:113], v[112:113], v[110:111]\n"
      "v_pk_add_f32 v[104:105], v[104:105], v[88:89] op_sel_hi:[1,0] neg_lo:[0,1] neg_hi:[0,1]\n"
      "v_pk_add_f32 v[140:141], v[140:141], v[88:89] op_sel_hi:[1,0] neg_lo:[0,1] neg_hi:[0,1]\n"
      "v_mfma_f32_16x16x16_bf16 v[146:149], v[192:193], v[124:125], v[146:149]\n"
      "v_exp_f32 v104, v104\n"
      "v_exp_f32 v139, v139\n"
      "v_exp_f32 v155, v155\n"
      "v_mfma_f32_16x16x16_bf16 v[160:163], v[192:193], v[126:127], v[160:163]\n"
      "v_exp_f32 v106, v106\n"
      "v_exp_f32 v105, v105\n"
      "v_mfma_f32_16x16x16_bf16 v[120:123], v[192:193], v[218:219], v[120:123]\n"
      "v_exp_f32 v154, v154\n"
      "s_waitcnt lgkmcnt(0)\n"
      "v_mfma_f32_16x16x32_bf16 v[156:159], a[216:219], v[170:173], 0\n"
      "v_exp_f32 v140, v140\n"
      "v_mfma_f32_16x16x32_bf16 v[156:159], a[220:223], v[100:103], v[156:159]\n"
      "ds_read_b128 v[220:223], v2 offset:15872\n"
      "v_cndmask_b32 v182, v182, v146, s[58:59]\n"
      "v_mfma_f32_16x16x32_bf16 v[156:159], a[224:227], v[164:167], v[156:159]\n"
      "v_cndmask_b32 v182, v182, v160, s[60:61]\n"
      "v_cvt_pk_bf16_f32 v179, v168, v169\n"
      "v_cndmask_b32 v182, v182, v120, s[62:63]\n"
      "v_mfma_f32_16x16x32_bf16 v[146:149], a[240:243], v[170:173], 0\n"
      "ds_read_b128 v[126:129], v2 offset:9728\n"
      "v_max_f32_dpp v218, v182, v182 row_ror:8 row_mask:0xf bank_mask:0xf\n"
      "v_mfma_f32_16x16x32_bf16 v[146:149], a[244:247], v[100:103], v[146:149]\n"
      "ds_read_b128 v[190:193], v2 offset:12800\n"
      "v_max_f32_dpp v218, v218, v218 row_ror:4 row_mask:0xf bank_mask:0xf\n"
      "v_mfma_f32_16x16x32_bf16 v[146:149], a[248:251], v[164:167], v[146:149]\n"
      "ds_read_b128 v[122:125], v7 offset:8192\n"
      "v_max_f32_dpp v218, v218, v218 row_ror:2 row_mask:0xf bank_mask:0xf\n"
      "v_mfma_f32_16x16x32_bf16 v[160:163], v[232:235], v[170:173], 0\n"
      "ds_write_b128 v3, v[176:179] offset:18688\n"
      "v_pk_fma_f32 v[110:111], v[114:115], v[114:115], v[110:111]\n"
      "v_pk_mul_f32 v[144:145], v[144:145], v[180:181] op_sel_hi:[1,0]\n"
      "v_max_f32_dpp v218, v218, v218 row_ror:1 row_mask:0xf bank_mask:0xf\n"
      "v_mfma_f32_16x16x32_bf16 v[160:163], v[236:239], v[100:103], v[160:163]\n"
      "ds_read_b128 v[168:171], v7 offset:16384\n"
      "v_exp_f32 v153, v153\n"
      "v_exp_f32_e64 v141, v141\n"
      "v_pk_mul_f32 v[142:143], v[142:143], v[180:181] op_sel_hi:[1,0]\n"
      "v_pk_fma_f32 v[110:111], v[204:205], v[204:205], v[110:111]\n"
      "v_pk_fma_f32 v[194:195], v[134:135], v[134:135], v[194:195]\n"
      "v_mfma_f32_16x16x32_bf16 v[160:163], v[240:243], v[164:167], v[160:163]\n"
      "s_waitcnt lgkmcnt(2)\n"
      "v_cvt_pk_bf16_f32 v88, v104, v105\n"
      "v_max_f32_dpp v218, v218, v218 row_bcast:15 row_mask:0xa bank_mask:0xf\n"
      "v_mfma_f32_16x16x32_bf16 v[156:159], a[228:231], v[126:129], v[156:159]\n"
      "ds_read_b128 v[102:105], v7 offset:12288\n"
      "v_max_f32_dpp v218, v218, v218 row_bcast:31 row_mask:0xc bank_mask:0xf\n"
      "v_mfma_f32_16x16x32_bf16 v[156:159], a[232:235], v[190:193], v[156:159]\n"
      "buffer_store_dwordx4 v[122:125], v22, s[24:27], s85 offen sc1\n"
      "v_readlane_b32 s72, v218, 63\n"
      "v_pk_fma_f32 v[194:195], v[136:137], v[136:137], v[194:195]\n"
      "v_mfma_f32_16x16x32_bf16 v[156:159], a[236:239], v[220:223], v[156:159]\n"
      "v_exp_f32 v107, v107\n"
      "v_subrev_f32 v182, s72, v182\n"
      "v_mfma_f32_16x16x32_bf16 v[146:149], a[252:255], v[126:129], v[146:149]\n"
      "v_exp_f32_e64 v182, v182\n"
      "v_mfma_f32_16x16x32_bf16 v[146:149], v[224:227], v[190:193], v[146:149]\n"
      "v_exp_f32 v152, v152\n"
      "v_mfma_f32_16x16x32_bf16 v[146:149], v[228:231], v[220:223], v[146:149]\n"
      "s_nop 0\n"
      "v_fma_f32 v166, v156, v156, s52\n"
      "v_cvt_pk_bf16_f32 v182, v182, v182\n"
      "v_cvt_pk_bf16_f32 v138, v138, v139\n"
      "v_cvt_pk_bf16_f32 v139, v140, v141\n"
      "v_cvt_pk_bf16_f32 v141, v154, v155\n"
      "v_mfma_f32_16x16x32_bf16 v[160:163], v[244:247], v[126:129], v[160:163]\n"
      "ds_write_b16 v13, v182\n"
      "v_cvt_pk_bf16_f32 v90, v108, 0\n"
      "v_mfma_f32_16x16x32_bf16 v[160:163], v[248:251], v[190:193], v[160:163]\n"
      "s_waitcnt lgkmcnt(1)\n"
      "v_cvt_pk_bf16_f32 v89, v106, v107\n"
      "v_cvt_pk_bf16_f32 v140, v152, v153\n"
      "v_mfma_f32_16x16x32_bf16 v[160:163], v[252:255], v[220:223], v[160:163]\n"
      "ds_write_b32 v31, v210 offset:5632\n"
      "v_cvt_pk_bf16_f32 v142, v142, v143\n"
      "v_mfma_f32_16x16x32_bf16 v[208:211], v[32:35], v[138:141], 0\n"
      "ds_read_b128 v[218:221], v14\n"
      "v_mul_f32 v167, v157, v157\n"
      "v_mfma_f32_16x16x32_bf16 v[208:211], v[32:35], v[88:91], v[208:211]\n"
      "ds_read_b128 v[174:177], v14 offset:64\n"
      "v_cvt_pk_bf16_f32 v143, v144, v145\n"
      "v_mfma_f32_16x16x32_bf16 v[106:109], v[130:133], v[138:141], 0\n"
      "buffer_store_dwordx4 v[102:105], v23, s[24:27], s85 offen sc1\n"
      "v_pk_fma_f32 v[110:111], v[206:207], v[206:207], v[110:111]\n"
      "v_pk_fma_f32 v[166:167], v[158:159], v[158:159], v[166:167]\n"
      "v_mfma_f32_16x16x32_bf16 v[106:109], v[76:79], v[88:91], v[106:109]\n"
      "s_nop 0\n"
      "v_rcp_f32 v208, v208\n"
      "v_mfma_f32_16x16x32_bf16 v[126:129], v[196:199], v[138:141], 0\n"
      "v_add_f32 v110, v110, v111\n"
      "v_add_f32 v194, v194, v195\n"
      "v_mfma_f32_16x16x32_bf16 v[126:129], v[80:83], v[88:91], v[126:129]\n"
      "ds_write_b64 v4, v[142:143] offset:18688\n"
      "v_permlane16_swap_b32_e64 v194, v110\n"
      "v_pk_fma_f32 v[166:167], v[146:147], v[146:147], v[166:167]\n"
      "v_pk_mul_f32 v[106:107], v[106:107], v[208:209] op_sel_hi:[1,0]\n"
      "v_pk_fma_f32 v[166:167], v[148:149], v[148:149], v[166:167]\n"
      "v_add_f32_e64 v194, v194, v110\n"
      "v_cvt_pk_bf16_f32 v106, v106, v107\n"
      "v_pk_mul_f32 v[108:109], v[108:109], v[208:209] op_sel_hi:[1,0]\n"
      "v_pk_mul_f32 v[126:127], v[126:127], v[208:209] op_sel_hi:[1,0]\n"
      "v_pk_mul_f32 v[128:129], v[128:129], v[208:209] op_sel_hi:[1,0]\n"
      "v_pk_fma_f32 v[166:167], v[160:161], v[160:161], v[166:167]\n"
      "v_pk_fma_f32 v[166:167], v[162:163], v[162:163], v[166:167]\n"
      "v_cvt_pk_bf16_f32 v107, v108, v109\n"
      "v_cvt_pk_bf16_f32 v108, v126, v127\n"
      "v_cvt_pk_bf16_f32 v109, v128, v129\n"
      "v_mfma_f32_16x16x32_bf16 v[126:129], v[214:217], v[138:141], 0\n"
      "s_waitcnt lgkmcnt(1)\n"
      "v_add_f32 v166, v166, v167\n"
      "v_mov_b32 v110, v166\n"
      "v_mfma_f32_16x16x32_bf16 v[126:129], v[84:87], v[88:91], v[126:129]\n"
      "ds_write_b128 v3, v[106:109] offset:18944\n"
      "v_permlane16_swap_b32 v166, v110\n"
      "v_add_f32 v166, v166, v110\n"
      "s_cmp_ge_u32 s95, 0x40000000\n"
      "s_cselect_b64 vcc, -1, s[56:57]\n"
      "v_permlane32_swap_b32_e64 v194, v166\n"
      "v_add_f32_e64 v194, v194, v166\n"
      "s_add_u32 s43, s43, 1\n"
      "ds_write_b32 v31, v194 offset:1024\n"
      "v_pk_mul_f32 v[126:127], v[126:127], v[208:209] op_sel_hi:[1,0]\n"
      "v_cvt_pk_bf16_f32 v126, v126, v127\n"
      "v_pk_mul_f32 v[128:129], v[128:129], v[208:209] op_sel_hi:[1,0]\n"
      "v_cvt_pk_bf16_f32 v127, v128, v129\n"
      "v_mfma_f32_16x16x32_bf16 v[208:211], v[218:221], v[32:35], 0\n"
      "ds_write_b64 v4, v[126:127] offset:18944\n"
      "v_mfma_f32_16x16x32_bf16 v[208:211], v[174:177], v[32:35], v[208:211]\n"
      "buffer_store_dwordx4 v[168:171], v25, s[24:27], s85 offen sc1\n"
      "v_add_u32 v11, 2336, v11\n"
      "v_add_u32 v10, 2336, v10\n"
      "s_waitcnt lgkmcnt(0)\n"
      "s_barrier\n"
      "s_cbranch_vccnz L_disp0_%=\n"
      "L_max0_%=:\n"
      "v_mfma_f32_16x16x32_bf16 v[168:171], v[218:221], v[130:133], 0\n"
      "ds_read_b128 v[164:167], v0\n"
      "s_add_u32 m0, s64, 18432\n"
      "v_mfma_f32_16x16x32_bf16 v[168:171], v[174:177], v[76:79], v[168:171]\n"
      "ds_read_b128 v[142:145], v9 offset:1024\n"
      "ds_read_b128 v[108:111], v1\n"
      "v_mfma_f32_16x16x32_bf16 v[150:153], v[218:221], v[196:199], 0\n"
      "buffer_load_dwordx4 v16, s[16:19], s45 offen  lds\n"
      "v_mfma_f32_16x16x32_bf16 v[150:153], v[174:177], v[80:83], v[150:153]\n"
      "ds_read_b128 v[124:127], v1 offset:12288\n"
      "v_mfma_f32_16x16x32_bf16 v[194:197], v[218:221], v[214:217], 0\n"
      "ds_read_b128 v[184:187], v0 offset:6144\n"
      "v_readlane_b32 s85, v37, s42\n"
      "v_mfma_f32_16x16x32_bf16 v[194:197], v[174:177], v[84:87], v[194:197]\n"
      "ds_read_b128 v[100:103], v0 offset:12288\n"
      "s_waitcnt lgkmcnt(5)\n"
      "v_mfma_f32_16x16x32_bf16 v[220:223], a[0:3], v[164:167], 0\n"
      "v_cndmask_b32 v168, v168, v150, s[58:59]\n"
      "v_readlane_b32 s86, v38, s43\n"
      "v_mfma_f32_16x16x32_bf16 v[172:175], a[24:27], v[164:167], 0\n"
      "s_waitcnt lgkmcnt(4)\n"
      "v_mov_b32_dpp v208, v208 quad_perm:[0,0,0,0] row_mask:0xf bank_mask:0xf\n"
      "v_add_f32 v144, v144, v145\n"
      "v_mfma_f32_16x16x32_bf16 v[176:179], a[48:51], v[164:167], 0\n"
      "s_add_u32 m0, s64, 22528\n"
      "v_add_f32 v142, v142, v143\n"
      "v_add_f32 v142, v142, v144\n"
      "v_mfma_f32_16x16x32_bf16 v[210:213], a[72:75], v[164:167], 0\n"
      "buffer_load_dwordx4 v17, s[16:19], s45 offen  lds\n"
      "v_mfma_f32_16x16x32_bf16 v[180:183], a[96:99], v[164:167], 0\n"
      "s_add_u32 m0, s64, 26624\n"
      "v_rcp_f32 v208, v208\n"
      "v_mfma_f32_16x16x32_bf16 v[138:141], a[120:123], v[164:167], 0\n"
      "s_waitcnt lgkmcnt(0)\n"
      "v_rsq_f32_e64 v142, v142\n"
      "v_mfma_f32_16x16x32_bf16 v[220:223], a[4:7], v[108:111], v[220:223]\n"
      "ds_read_b128 v[214:217], v9 offset:5632\n"
      "v_mul_f32 v142, 0x415db3d7, v142\n"
      "v_mfma_f32_16x16x32_bf16 v[220:223], a[8:11], v[184:187], v[220:223]\n"
      "ds_read_b128 v[150:153], v1 offset:6144\n"
      "v_mov_b32_e64 v144, v142\n"
      "v_mfma_f32_16x16x32_bf16 v[172:175], a[28:31], v[108:111], v[172:175]\n"
      "buffer_load_dwordx4 v18, s[16:19], s45 offen  lds\n"
      "v_mfma_f32_16x16x32_bf16 v[172:175], a[32:35], v[184:187], v[172:175]\n"
      "s_add_u32 m0, s64, 30720\n"
      "v_permlane32_swap_b32_e64 v142, v144\n"
      "v_mfma_f32_16x16x32_bf16 v[176:179], a[52:55], v[108:111], v[176:179]\n"
      "ds_read_b128 v[188:191], v1 offset:2048\n"
      "v_mul_f32 v160, v144, v160\n"
      "v_mfma_f32_16x16x32_bf16 v[176:179], a[56:59], v[184:187], v[176:179]\n"
      "ds_read_u16 v78, v11\n"
      "v_mul_f32 v158, v144, v158\n"
      "v_mfma_f32_16x16x32_bf16 v[210:213], a[76:79], v[108:111], v[210:213]\n"
      "ds_read_b128 v[120:123], v0 offset:8192\n"
      "v_mov_b32_e64 v143, v142\n"
      "v_mfma_f32_16x16x32_bf16 v[210:213], a[80:83], v[184:187], v[210:213]\n"
      "buffer_load_dwordx4 v19, s[16:19], s45 offen  lds\n"
      "v_mfma_f32_16x16x32_bf16 v[180:183], a[100:103], v[108:111], v[180:183]\n"
      "v_permlane16_swap_b32 v142, v143\n"
      "v_mfma_f32_16x16x32_bf16 v[180:183], a[104:107], v[184:187], v[180:183]\n"
      "v_mul_f32 v134, v142, v134\n"
      "v_mul_f32 v200, v143, v200\n"
      "v_mfma_f32_16x16x32_bf16 v[138:141], a[124:127], v[108:111], v[138:141]\n"
      "v_mul_f32 v137, v142, v137\n"
      "v_mul_f32 v207, v143, v207\n"
      "v_mfma_f32_16x16x32_bf16 v[138:141], a[128:131], v[184:187], v[138:141]\n"
      "s_waitcnt lgkmcnt(1)\n"
      "v_mul_f32 v116, v142, v116\n"
      "v_mul_f32 v157, v144, v157\n"
      "v_mfma_f32_16x16x32_bf16 v[220:223], a[12:15], v[150:153], v[220:223]\n"
      "v_mul_f32 v163, v144, v163\n"
      "v_mul_f32 v97, v142, v97\n"
      "v_mfma_f32_16x16x32_bf16 v[220:223], a[16:19], v[100:103], v[220:223]\n"
      "v_mul_f32 v135, v142, v135\n"
      "v_mul_f32 v204, v143, v204\n"
      "v_mfma_f32_16x16x32_bf16 v[220:223], a[20:23], v[124:127], v[220:223]\n"
      "v_mul_f32 v205, v143, v205\n"
      "v_mul_f32 v113, v143, v113\n"
      "v_mfma_f32_16x16x32_bf16 v[172:175], a[36:39], v[150:153], v[172:175]\n"
      "v_mul_f32 v115, v143, v115\n"
      "v_mul_f32 v202, v143, v202\n"
      "v_mfma_f32_16x16x32_bf16 v[172:175], a[40:43], v[100:103], v[172:175]\n"
      "s_add_u32 m0, s65, 34816\n"
      "v_cvt_pk_bf16_f32 v134, v134, v135\n"
      "v_mul_f32 v161, v144, v161\n"
      "v_mfma_f32_16x16x32_bf16 v[172:175], a[44:47], v[124:127], v[172:175]\n"
      "v_mul_f32 v203, v143, v203\n"
      "v_mul_f32_e64 v206, v143, v206\n"
      "v_mfma_f32_16x16x32_bf16 v[176:179], a[60:63], v[150:153], v[176:179]\n"
      "buffer_load_dwordx4 v15, s[16:19], s45 offen  lds\n"
      "v_mfma_f32_16x16x32_bf16 v[176:179], a[64:67], v[100:103], v[176:179]\n"
      "v_mul_f32_e64 v162, v144, v162\n"
      "v_cvt_pk_bf16_f32 v204, v204, v205\n"
      "v_mfma_f32_16x16x32_bf16 v[176:179], a[68:71], v[124:127], v[176:179]\n"
      "ds_read_b128 v[104:107], v0 offset:14336\n"
      "v_mul_f32 v136, v142, v136\n"
      "v_mfma_f32_16x16x32_bf16 v[210:213], a[84:87], v[150:153], v[210:213]\n"
      "v_mul_f32 v99, v142, v99\n"
      "v_cvt_pk_bf16_f32 v160, v160, v161\n"
      "v_mfma_f32_16x16x32_bf16 v[210:213], a[88:91], v[100:103], v[210:213]\n"
      "v_or3_b32 v214, v214, v215, v216\n"
      "v_bitop3_b32 v214, v214, v217, s33 bitop3:0xa8\n"
      "v_mfma_f32_16x16x32_bf16 v[210:213], a[92:95], v[124:127], v[210:213]\n"
      "v_fma_f32 v170, v220, v220, s51\n"
      "v_cndmask_b32 v168, v168, v194, s[60:61]\n"
      "v_mfma_f32_16x16x32_bf16 v[180:183], a[108:111], v[150:153], v[180:183]\n"
      "v_mul_f32 v156, v144, v156\n"
      "v_mfma_f32_16x16x32_bf16 v[180:183], a[112:115], v[100:103], v[180:183]\n"
      "buffer_load_dwordx4 v[192:195], v36, s[28:31], s47 offen\n"
      "v_mfma_f32_16x16x32_bf16 v[180:183], a[116:119], v[124:127], v[180:183]\n"
      "s_nop 0\n"
      "v_fma_f32 v82, v210, v210, s51\n"
      "v_cvt_pk_bf16_f32 v135, v136, v137\n"
      "v_mfma_f32_16x16x32_bf16 v[138:141], a[132:135], v[150:153], v[138:141]\n"
      "v_fmac_f32 v82, v211, v211\n"
      "v_fmac_f32 v170, v221, v221\n"
      "v_mfma_f32_16x16x32_bf16 v[138:141], a[136:139], v[100:103], v[138:141]\n"
      "v_mul_f32 v96, v142, v96\n"
      "v_cvt_pk_bf16_f32 v161, v162, v163\n"
      "v_mfma_f32_16x16x32_bf16 v[138:141], a[140:143], v[124:127], v[138:141]\n"
      "v_fmac_f32 v82, v212, v212\n"
      "v_fmac_f32 v82, v213, v213\n"
      "v_mfma_f32_16x16x32_bf16 v[130:133], v[164:167], a[144:147], 0\n"
      "v_fmac_f32 v170, v222, v222\n"
      "v_cvt_pk_bf16_f32 v96, v96, v97\n"
      "v_mfma_f32_16x16x32_bf16 v[130:133], v[108:111], a[148:151], v[130:133]\n"
      "v_fmac_f32 v82, v180, v180\n"
      "v_cvt_pk_bf16_f32 v205, v206, v207\n"
      "v_mfma_f32_16x16x32_bf16 v[130:133], v[184:187], a[152:155], v[130:133]\n"
      "v_mul_f32 v146, v144, v146\n"
      "v_mul_f32_e64 v114, v143, v114\n"
      "v_mfma_f32_16x16x32_bf16 v[130:133], v[150:153], a[156:159], v[130:133]\n"
      "v_fmac_f32 v82, v181, v181\n"
      "v_fmac_f32 v170, v223, v223\n"
      "v_mfma_f32_16x16x32_bf16 v[130:133], v[100:103], a[160:163], v[130:133]\n"
      "ds_write_b64 v6, v[134:135]\n"
      "v_mfma_f32_16x16x32_bf16 v[130:133], v[124:127], a[164:167], v[130:133]\n"
      "v_readlane_b32 s84, v37, s43\n"
      "v_mul_f32_e64 v149, v144, v149\n"
      "v_mfma_f32_16x16x32_bf16 v[196:199], v[164:167], a[168:171], 0\n"
      "buffer_load_dword v89, v29, s[28:31], s47 offen\n"
      "v_mfma_f32_16x16x32_bf16 v[196:199], v[108:111], a[172:175], v[196:199]\n"
      "ds_read_b128 v[134:137], v0 offset:2048\n"
      "v_mul_f32_e64 v98, v142, v98\n"
      "v_mfma_f32_16x16x32_bf16 v[196:199], v[184:187], a[176:179], v[196:199]\n"
      "v_cmp_ne_u32 s[56:57], 0, v214\n"
      "v_mul_f32 v159, v144, v159\n"
      "v_mfma_f32_16x16x32_bf16 v[196:199], v[150:153], a[180:183], v[196:199]\n"
      "v_fmac_f32 v170, v172, v172\n"
      "v_mul_f32 v112, v143, v112\n"
      "v_mfma_f32_16x16x32_bf16 v[196:199], v[100:103], a[184:187], v[196:199]\n"
      "v_cvt_pk_bf16_f32 v156, v156, v157\n"
      "v_fmac_f32 v82, v182, v182\n"
      "v_mfma_f32_16x16x32_bf16 v[196:199], v[124:127], a[188:191], v[196:199]\n"
      "v_mul_f32 v147, v144, v147\n"
      "v_mul_f32 v201, v143, v201\n"
      "v_mfma_f32_16x16x32_bf16 v[214:217], v[164:167], a[192:195], 0\n"
      "v_fmac_f32_e64 v82, v183, v183\n"
      "v_mul_f32_e64 v118, v142, v118\n"
      "v_mfma_f32_16x16x32_bf16 v[214:217], v[108:111], a[196:199], v[214:217]\n"
      "v_cvt_pk_bf16_f32 v157, v158, v159\n"
      "v_fmac_f32_e64 v170, v173, v173\n"
      "v_mfma_f32_16x16x32_bf16 v[214:217], v[184:187], a[200:203], v[214:217]\n"
      "ds_read_b128 v[164:167], v1 offset:8192\n"
      "v_cvt_pk_bf16_f32 v200, v200, v201\n"
      "v_mfma_f32_16x16x32_bf16 v[214:217], v[150:153], a[204:207], v[214:217]\n"
      "ds_read_b128 v[150:153], v1 offset:14336\n"
      "v_mul_f32 v119, v142, v119\n"
      "v_mfma_f32_16x16x32_bf16 v[214:217], v[100:103], a[208:211], v[214:217]\n"
      "ds_write_b64 v6, v[204:205] offset:6144\n"
      "v_mfma_f32_16x16x32_bf16 v[214:217], v[124:127], a[212:215], v[214:217]\n"
      "s_waitcnt lgkmcnt(3)\n"
      "v_cvt_pk_bf16_f32 v201, v202, v203\n"
      "v_cvt_pk_bf16_f32 v97, v98, v99\n"
      "v_mfma_f32_16x16x32_bf16 v[184:187], a[0:3], v[134:137], 0\n"
      "v_mul_f32_e64 v148, v144, v148\n"
      "v_cvt_pk_bf16_f32 v130, v130, v131\n"
      "v_mfma_f32_16x16x32_bf16 v[184:187], a[4:7], v[188:191], v[184:187]\n"
      "s_or_b32 s95, s84, s85\n"
      "v_fmac_f32 v82, v138, v138\n"
      "v_cvt_pk_bf16_f32 v202, v112, v113\n"
      "v_mfma_f32_16x16x32_bf16 v[184:187], a[8:11], v[120:123], v[184:187]\n"
      "s_waitcnt lgkmcnt(2)\n"
      "v_fmac_f32 v170, v174, v174\n"
      "v_fmac_f32_e64 v170, v175, v175\n"
      "v_mfma_f32_16x16x32_bf16 v[184:187], a[12:15], v[164:167], v[184:187]\n"
      "v_cvt_pk_bf16_f32 v214, v214, v215\n"
      "v_fmac_f32 v82, v139, v139\n"
      "v_mfma_f32_16x16x32_bf16 v[184:187], a[16:19], v[104:107], v[184:187]\n"
      "s_waitcnt lgkmcnt(1)\n"
      "v_fmac_f32_e64 v170, v176, v176\n"
      "v_cvt_pk_bf16_f32 v215, v216, v217\n"
      "v_mfma_f32_16x16x32_bf16 v[184:187], a[20:23], v[150:153], v[184:187]\n"
      "v_fmac_f32_e64 v82, v140, v140\n"
      "v_cvt_pk_bf16_f32 v196, v196, v197\n"
      "v_mfma_f32_16x16x32_bf16 v[204:207], a[24:27], v[134:137], 0\n"
      "v_mul_f32 v117, v142, v117\n"
      "v_fmac_f32 v82, v141, v141\n"
      "v_mfma_f32_16x16x32_bf16 v[204:207], a[28:31], v[188:191], v[204:207]\n"
      "v_mul_f32 v168, v208, v168\n"
      "v_fmac_f32 v170, v177, v177\n"
      "v_mfma_f32_16x16x32_bf16 v[204:207], a[32:35], v[120:123], v[204:207]\n"
      "v_fmac_f32_e64 v170, v178, v178\n"
      "v_fma_f32 v208, v184, v184, s51\n"
      "v_mfma_f32_16x16x32_bf16 v[204:207], a[36:39], v[164:167], v[204:207]\n"
      "ds_write_b64 v6, v[160:161] offset:12288\n"
      "v_mfma_f32_16x16x32_bf16 v[204:207], a[40:43], v[104:107], v[204:207]\n"
      "v_cvt_pk_bf16_f32 v197, v198, v199\n"
      "v_fmac_f32_e64 v208, v185, v185\n"
      "v_mfma_f32_16x16x32_bf16 v[204:207], a[44:47], v[150:153], v[204:207]\n"
      "v_cvt_pk_bf16_f32 v158, v146, v147\n"
      "v_fmac_f32 v170, v179, v179\n"
      "v_mfma_f32_16x16x32_bf16 v[108:111], a[48:51], v[134:137], 0\n"
      "s_nop 0\n"
      "v_permlane16_swap_b32 v170, v82\n"
      "v_mfma_f32_16x16x32_bf16 v[108:111], a[52:55], v[188:191], v[108:111]\n"
      "v_cvt_pk_bf16_f32 v203, v114, v115\n"
      "v_add_f32 v170, v170, v82\n"
      "v_mfma_f32_16x16x32_bf16 v[108:111], a[56:59], v[120:123], v[108:111]\n"
      "v_mov_b32 v82, v170\n"
      "v_cvt_pk_bf16_f32 v159, v148, v149\n"
      "v_mfma_f32_16x16x32_bf16 v[108:111], a[60:63], v[164:167], v[108:111]\n"
      "v_permlane32_swap_b32 v170, v82\n"
      "v_mfma_f32_16x16x32_bf16 v[108:111], a[64:67], v[104:107], v[108:111]\n"
      "v_cvt_pk_bf16_f32 v131, v132, v133\n"
      "v_fmac_f32 v208, v186, v186\n"
      "v_mfma_f32_16x16x32_bf16 v[108:111], a[68:71], v[150:153], v[108:111]\n"
      "v_add_f32 v170, v170, v82\n"
      "v_fmac_f32_e64 v208, v187, v187\n"
      "v_mfma_f32_16x16x32_bf16 v[146:149], a[72:75], v[134:137], 0\n"
      "v_rsq_f32 v170, v170\n"
      "v_mfma_f32_16x16x32_bf16 v[146:149], a[76:79], v[188:191], v[146:149]\n"
      "v_cvt_pk_bf16_f32 v168, v168, v168\n"
      "v_mov_b32 v82, v170\n"
      "v_mfma_f32_16x16x32_bf16 v[146:149], a[80:83], v[120:123], v[146:149]\n"
      "ds_read_b128 v[142:145], v0 offset:4096\n"
      "v_fmac_f32_e64 v208, v204, v204\n"
      "v_mfma_f32_16x16x32_bf16 v[146:149], a[84:87], v[164:167], v[146:149]\n"
      "ds_read_b128 v[112:115], v1 offset:4096\n"
      "v_fmac_f32 v208, v205, v205\n"
      "v_mfma_f32_16x16x32_bf16 v[146:149], a[88:91], v[104:107], v[146:149]\n"
      "v_permlane16_swap_b32 v170, v82\n"
      "v_mfma_f32_16x16x32_bf16 v[146:149], a[92:95], v[150:153], v[146:149]\n"
      "v_cvt_pk_bf16_f32 v99, v118, v119\n"
      "v_mul_f32 v211, v82, v211\n"
      "v_mfma_f32_16x16x32_bf16 v[100:103], a[96:99], v[134:137], 0\n"
      "v_fmac_f32 v208, v206, v206\n"
      "v_mul_f32_e64 v170, s50, v170\n"
      "v_mfma_f32_16x16x32_bf16 v[100:103], a[100:103], v[188:191], v[100:103]\n"
      "v_mul_f32 v182, v82, v182\n"
      "v_mul_f32 v141, v82, v141\n"
      "v_mfma_f32_16x16x32_bf16 v[100:103], a[104:107], v[120:123], v[100:103]\n"
      "v_fma_f32 v155, v146, v146, s51\n"
      "v_fmac_f32 v155, v147, v147\n"
      "v_mfma_f32_16x16x32_bf16 v[100:103], a[108:111], v[164:167], v[100:103]\n"
      "v_mul_f32 v173, v170, v173\n"
      "v_fmac_f32 v155, v148, v148\n"
      "v_mfma_f32_16x16x32_bf16 v[100:103], a[112:115], v[104:107], v[100:103]\n"
      "v_mul_f32 v138, v82, v138\n"
      "v_mul_f32 v180, v82, v180\n"
      "v_mfma_f32_16x16x32_bf16 v[100:103], a[116:119], v[150:153], v[100:103]\n"
      "v_fmac_f32 v208, v207, v207\n"
      "v_mul_f32 v210, v82, v210\n"
      "v_mfma_f32_16x16x32_bf16 v[160:163], a[120:123], v[134:137], 0\n"
      "v_fmac_f32 v208, v108, v108\n"
      "v_fmac_f32 v155, v149, v149\n"
      "v_mfma_f32_16x16x32_bf16 v[160:163], a[124:127], v[188:191], v[160:163]\n"
      "v_mul_f32 v221, v170, v221\n"
      "v_mul_f32 v177, v170, v177\n"
      "v_mfma_f32_16x16x32_bf16 v[160:163], a[128:131], v[120:123], v[160:163]\n"
      "v_mul_f32 v212, v82, v212\n"
      "v_fmac_f32 v208, v109, v109\n"
      "v_mfma_f32_16x16x32_bf16 v[160:163], a[132:135], v[164:167], v[160:163]\n"
      "v_fmac_f32 v155, v100, v100\n"
      "v_fmac_f32 v208, v110, v110\n"
      "v_mfma_f32_16x16x32_bf16 v[160:163], a[136:139], v[104:107], v[160:163]\n"
      "v_fmac_f32 v155, v101, v101\n"
      "v_mul_f32 v220, v170, v220\n"
      "v_mfma_f32_16x16x32_bf16 v[160:163], a[140:143], v[150:153], v[160:163]\n"
      "v_fmac_f32 v155, v102, v102\n"
      "v_mul_f32 v181, v82, v181\n"
      "v_mfma_f32_16x16x32_bf16 v[216:219], v[134:137], a[144:147], 0\n"
      "v_mul_f32 v139, v82, v139\n"
      "v_mul_f32 v176, v170, v176\n"
      "v_mfma_f32_16x16x32_bf16 v[216:219], v[188:191], a[148:151], v[216:219]\n"
      "ds_read_b128 v[124:127], v0 offset:10240\n"
      "v_fmac_f32 v155, v103, v103\n"
      "v_mfma_f32_16x16x32_bf16 v[216:219], v[120:123], a[152:155], v[216:219]\n"
      "ds_write_b128 v5, v[156:159] offset:12288\n"
      "v_pk_mul_f32 v[222:223], v[222:223], v[170:171] op_sel_hi:[1,0]\n"
      "v_mfma_f32_16x16x32_bf16 v[216:219], v[164:167], a[156:159], v[216:219]\n"
      "v_fmac_f32 v208, v111, v111\n"
      "v_mul_f32 v213, v82, v213\n"
      "v_mfma_f32_16x16x32_bf16 v[216:219], v[104:107], a[160:163], v[216:219]\n"
      "v_fmac_f32 v155, v160, v160\n"
      "v_mul_f32 v140, v82, v140\n"
      "v_mfma_f32_16x16x32_bf16 v[216:219], v[150:153], a[164:167], v[216:219]\n"
      "v_fmac_f32_e64 v155, v161, v161\n"
      "v_cvt_pk_bf16_f32 v210, v210, v211\n"
      "v_mfma_f32_16x16x32_bf16 v[156:159], v[134:137], a[168:171], 0\n"
      "ds_write_b128 v5, v[200:203] offset:6144\n"
      "v_mfma_f32_16x16x32_bf16 v[156:159], v[188:191], a[172:175], v[156:159]\n"
      "v_cvt_pk_bf16_f32 v98, v116, v117\n"
      "v_cvt_pk_bf16_f32 v176, v176, v177\n"
      "v_mfma_f32_16x16x32_bf16 v[156:159], v[120:123], a[176:179], v[156:159]\n"
      "v_mul_f32 v179, v170, v179\n"
      "v_fmac_f32 v155, v162, v162\n"
      "v_mfma_f32_16x16x32_bf16 v[156:159], v[164:167], a[180:183], v[156:159]\n"
      "ds_read_b128 v[116:119], v1 offset:16384\n"
      "v_mul_f32_e64 v178, v170, v178\n"
      "v_mfma_f32_16x16x32_bf16 v[156:159], v[104:107], a[184:187], v[156:159]\n"
      "v_cvt_pk_bf16_f32 v132, v216, v217\n"
      "v_mfma_f32_16x16x32_bf16 v[156:159], v[150:153], a[188:191], v[156:159]\n"
      "ds_write_b128 v5, v[96:99]\n"
      "v_mfma_f32_16x16x32_bf16 v[96:99], v[134:137], a[192:195], 0\n"
      "ds_read_b128 v[134:137], v1 offset:10240\n"
      "v_mul_f32 v183, v82, v183\n"
      "v_mfma_f32_16x16x32_bf16 v[96:99], v[188:191], a[196:199], v[96:99]\n"
      "v_fmac_f32 v155, v163, v163\n"
      "v_cvt_pk_bf16_f32 v138, v138, v139\n"
      "v_mfma_f32_16x16x32_bf16 v[96:99], v[120:123], a[200:203], v[96:99]\n"
      "v_permlane16_swap_b32 v208, v155\n"
      "v_mfma_f32_16x16x32_bf16 v[96:99], v[164:167], a[204:207], v[96:99]\n"
      "ds_read_b128 v[200:203], v0 offset:16384\n"
      "v_add_f32 v208, v208, v155\n"
      "v_mfma_f32_16x16x32_bf16 v[96:99], v[104:107], a[208:211], v[96:99]\n"
      "s_waitcnt lgkmcnt(3)\n"
      "v_mov_b32 v155, v208\n"
      "v_cvt_pk_bf16_f32 v198, v156, v157\n"
      "v_mfma_f32_16x16x32_bf16 v[96:99], v[150:153], a[212:215], v[96:99]\n"
      "s_or_b32 s95, s95, s48\n"
      "v_permlane32_swap_b32 v208, v155\n"
      "v_mfma_f32_16x16x32_bf16 v[104:107], a[0:3], v[142:145], 0\n"
      "v_cvt_pk_bf16_f32 v177, v178, v179\n"
      "v_add_f32_e64 v208, v208, v155\n"
      "v_mfma_f32_16x16x32_bf16 v[104:107], a[4:7], v[112:115], v[104:107]\n"
      "s_waitcnt lgkmcnt(1)\n"
      "v_cvt_pk_bf16_f32 v211, v212, v213\n"
      "v_mul_f32 v172, v170, v172\n"
      "v_mfma_f32_16x16x32_bf16 v[104:107], a[8:11], v[124:127], v[104:107]\n"
      "ds_read_u16 v82, v11 offset:32\n"
      "v_mul_f32 v174, v170, v174\n"
      "v_mfma_f32_16x16x32_bf16 v[104:107], a[12:15], v[134:137], v[104:107]\n"
      "s_waitcnt lgkmcnt(1)\n"
      "v_rsq_f32_e64 v208, v208\n"
      "v_mfma_f32_16x16x32_bf16 v[104:107], a[16:19], v[200:203], v[104:107]\n"
      "v_mov_b32 v155, v208\n"
      "v_cvt_pk_bf16_f32 v139, v140, v141\n"
      "v_mfma_f32_16x16x32_bf16 v[104:107], a[20:23], v[116:119], v[104:107]\n"
      "v_permlane16_swap_b32 v208, v155\n"
      "v_mfma_f32_16x16x32_bf16 v[188:191], a[24:27], v[142:145], 0\n"
      "v_cvt_pk_bf16_f32 v220, v220, v221\n"
      "v_mul_f32 v147, v155, v147\n"
      "v_mfma_f32_16x16x32_bf16 v[188:191], a[28:31], v[112:115], v[188:191]\n"
      "s_cmp_ge_u32 s95, 0x40000000\n"
      "s_cselect_b32 s94, -1, -2\n"
      "v_mul_f32 v175, v170, v175\n"
      "v_mfma_f32_16x16x32_bf16 v[188:191], a[32:35], v[124:127], v[188:191]\n"
      "v_mul_f32 v161, v155, v161\n"
      "v_mul_f32 v208, s50, v208\n"
      "v_mfma_f32_16x16x32_bf16 v[188:191], a[36:39], v[134:137], v[188:191]\n"
      "v_mul_f32 v100, v155, v100\n"
      "v_mul_f32 v163, v155, v163\n"
      "v_mfma_f32_16x16x32_bf16 v[188:191], a[40:43], v[200:203], v[188:191]\n"
      "v_cvt_pk_bf16_f32 v216, v96, v97\n"
      "v_cvt_pk_bf16_f32 v221, v222, v223\n"
      "v_mfma_f32_16x16x32_bf16 v[188:191], a[44:47], v[116:119], v[188:191]\n"
      "v_cvt_pk_bf16_f32 v133, v218, v219\n"
      "v_mul_f32 v184, v208, v184\n"
      "v_mfma_f32_16x16x32_bf16 v[120:123], a[48:51], v[142:145], 0\n"
      "v_mul_f32 v148, v155, v148\n"
      "v_mul_f32_e64 v185, v208, v185\n"
      "v_mfma_f32_16x16x32_bf16 v[120:123], a[52:55], v[112:115], v[120:123]\n"
      "v_fma_f32 v219, v104, v104, s51\n"
      "v_cvt_pk_bf16_f32 v217, v98, v99\n"
      "v_mfma_f32_16x16x32_bf16 v[120:123], a[56:59], v[124:127], v[120:123]\n"
      "v_fmac_f32 v219, v105, v105\n"
      "v_mul_f32 v160, v155, v160\n"
      "v_mfma_f32_16x16x32_bf16 v[120:123], a[60:63], v[134:137], v[120:123]\n"
      "v_fmac_f32_e64 v219, v106, v106\n"
      "v_cvt_pk_bf16_f32 v212, v180, v181\n"
      "v_mfma_f32_16x16x32_bf16 v[120:123], a[64:67], v[200:203], v[120:123]\n"
      "s_and_b32 s86, s86, -4\n"
      "v_cvt_pk_bf16_f32 v160, v160, v161\n"
      "v_mul_f32 v111, v208, v111\n"
      "v_mfma_f32_16x16x32_bf16 v[120:123], a[68:71], v[116:119], v[120:123]\n"
      "v_mul_f32 v162, v155, v162\n"
      "v_fmac_f32 v219, v107, v107\n"
      "v_mfma_f32_16x16x32_bf16 v[150:153], a[72:75], v[142:145], 0\n"
      "v_fmac_f32 v219, v188, v188\n"
      "v_mul_f32 v103, v155, v103\n"
      "v_mfma_f32_16x16x32_bf16 v[150:153], a[76:79], v[112:115], v[150:153]\n"
      "v_mul_f32_e64 v101, v155, v101\n"
      "v_cvt_pk_bf16_f32 v223, v174, v175\n"
      "v_mfma_f32_16x16x32_bf16 v[150:153], a[80:83], v[124:127], v[150:153]\n"
      "ds_write_b16 v12, v168 offset:16\n"
      "v_mfma_f32_16x16x32_bf16 v[150:153], a[84:87], v[134:137], v[150:153]\n"
      "v_mul_f32 v146, v155, v146\n"
      "v_fmac_f32 v219, v189, v189\n"
      "v_mfma_f32_16x16x32_bf16 v[150:153], a[88:91], v[200:203], v[150:153]\n"
      "v_mul_f32 v149, v155, v149\n"
      "v_cvt_pk_bf16_f32 v199, v158, v159\n"
      "v_mfma_f32_16x16x32_bf16 v[150:153], a[92:95], v[116:119], v[150:153]\n"
      "v_mul_f32 v108, v208, v108\n"
      "v_mul_f32_e64 v207, v208, v207\n"
      "v_mfma_f32_16x16x32_bf16 v[168:171], a[96:99], v[142:145], 0\n"
      "v_mul_f32 v204, v208, v204\n"
      "v_mul_f32 v110, v208, v110\n"
      "v_mfma_f32_16x16x32_bf16 v[168:171], a[100:103], v[112:115], v[168:171]\n"
      "v_mul_f32 v109, v208, v109\n"
      "v_mul_f32 v187, v208, v187\n"
      "v_mfma_f32_16x16x32_bf16 v[168:171], a[104:107], v[124:127], v[168:171]\n"
      "v_cvt_pk_bf16_f32 v146, v146, v147\n"
      "v_fmac_f32 v219, v190, v190\n"
      "v_mfma_f32_16x16x32_bf16 v[168:171], a[108:111], v[134:137], v[168:171]\n"
      "v_mul_f32 v102, v155, v102\n"
      "v_mul_f32 v206, v208, v206\n"
      "v_mfma_f32_16x16x32_bf16 v[168:171], a[112:115], v[200:203], v[168:171]\n"
      "v_mul_f32 v186, v208, v186\n"
      "v_fma_f32 v140, v150, v150, s51\n"
      "v_mfma_f32_16x16x32_bf16 v[168:171], a[116:119], v[116:119], v[168:171]\n"
      "v_cvt_pk_bf16_f32 v161, v162, v163\n"
      "v_cvt_pk_bf16_f32 v147, v148, v149\n"
      "v_mfma_f32_16x16x32_bf16 v[154:157], a[120:123], v[142:145], 0\n"
      "v_mul_f32 v205, v208, v205\n"
      "v_fmac_f32 v219, v191, v191\n"
      "v_mfma_f32_16x16x32_bf16 v[154:157], a[124:127], v[112:115], v[154:157]\n"
      "v_cvt_pk_bf16_f32 v148, v100, v101\n"
      "v_fmac_f32_e64 v140, v151, v151\n"
      "v_mfma_f32_16x16x32_bf16 v[154:157], a[128:131], v[124:127], v[154:157]\n"
      "v_cvt_pk_bf16_f32 v184, v184, v185\n"
      "v_cvt_pk_bf16_f32 v185, v186, v187\n"
      "v_mfma_f32_16x16x32_bf16 v[154:157], a[132:135], v[134:137], v[154:157]\n"
      "v_fmac_f32 v140, v152, v152\n"
      "v_fmac_f32 v219, v120, v120\n"
      "v_mfma_f32_16x16x32_bf16 v[154:157], a[136:139], v[200:203], v[154:157]\n"
      "v_cvt_pk_bf16_f32 v222, v172, v173\n"
      "v_fmac_f32 v140, v153, v153\n"
      "v_mfma_f32_16x16x32_bf16 v[154:157], a[140:143], v[116:119], v[154:157]\n"
      "v_fmac_f32 v140, v168, v168\n"
      "v_fmac_f32 v140, v169, v169\n"
      "v_mfma_f32_16x16x32_bf16 v[96:99], v[142:145], a[144:147], 0\n"
      "v_fmac_f32 v140, v170, v170\n"
      "v_fmac_f32_e64 v219, v121, v121\n"
      "v_mfma_f32_16x16x32_bf16 v[96:99], v[112:115], a[148:151], v[96:99]\n"
      "v_cvt_pk_bf16_f32 v149, v102, v103\n"
      "v_fmac_f32 v219, v122, v122\n"
      "v_mfma_f32_16x16x32_bf16 v[96:99], v[124:127], a[152:155], v[96:99]\n"
      "v_cvt_pk_bf16_f32 v213, v182, v183\n"
      "v_fmac_f32 v140, v171, v171\n"
      "v_mfma_f32_16x16x32_bf16 v[96:99], v[134:137], a[156:159], v[96:99]\n"
      "v_fmac_f32 v219, v123, v123\n"
      "v_fmac_f32 v140, v154, v154\n"
      "v_mfma_f32_16x16x32_bf16 v[96:99], v[200:203], a[160:163], v[96:99]\n"
      "v_fmac_f32 v140, v155, v155\n"
      "v_fmac_f32 v140, v156, v156\n"
      "v_mfma_f32_16x16x32_bf16 v[96:99], v[116:119], a[164:167], v[96:99]\n"
      "v_cvt_pk_bf16_f32 v187, v206, v207\n"
      "v_fmac_f32 v140, v157, v157\n"
      "v_mfma_f32_16x16x32_bf16 v[206:209], v[142:145], a[168:171], 0\n"
      "s_nop 0\n"
      "v_permlane16_swap_b32 v219, v140\n"
      "v_mfma_f32_16x16x32_bf16 v[206:209], v[112:115], a[172:175], v[206:209]\n"
      "v_add_f32 v219, v219, v140\n"
      "v_mov_b32 v140, v219\n"
      "v_mfma_f32_16x16x32_bf16 v[206:209], v[124:127], a[176:179], v[206:209]\n"
      "s_nop 0\n"
      "v_permlane32_swap_b32 v219, v140\n"
      "v_mfma_f32_16x16x32_bf16 v[206:209], v[134:137], a[180:183], v[206:209]\n"
      "ds_read_b128 v[172:175], v2 offset:18432\n"
      "v_add_u32 v12, 32, v12\n"
      "v_mfma_f32_16x16x32_bf16 v[206:209], v[200:203], a[184:187], v[206:209]\n"
      "ds_read_b128 v[100:103], v2 offset:24576\n"
      "v_add_f32 v219, v219, v140\n"
      "v_mfma_f32_16x16x32_bf16 v[206:209], v[116:119], a[188:191], v[206:209]\n"
      "v_rsq_f32 v219, v219\n"
      "v_mfma_f32_16x16x32_bf16 v[178:181], v[142:145], a[192:195], 0\n"
      "ds_read_b128 v[162:165], v2 offset:21504\n"
      "v_mov_b32 v140, v219\n"
      "v_mfma_f32_16x16x32_bf16 v[178:181], v[112:115], a[196:199], v[178:181]\n"
      "s_nop 0\n"
      "v_permlane16_swap_b32_e64 v219, v140\n"
      "v_mfma_f32_16x16x32_bf16 v[178:181], v[124:127], a[200:203], v[178:181]\n"
      "v_mul_f32 v170, v140, v170\n"
      "v_cvt_pk_bf16_f32 v77, v98, v99\n"
      "v_mfma_f32_16x16x32_bf16 v[178:181], v[134:137], a[204:207], v[178:181]\n"
      "s_waitcnt lgkmcnt(2)\n"
      "v_cvt_pk_bf16_f32 v76, v96, v97\n"
      "v_cvt_pk_bf16_f32 v108, v108, v109\n"
      "v_mfma_f32_16x16x32_bf16 v[178:181], v[200:203], a[208:211], v[178:181]\n"
      "ds_read_b128 v[126:129], v10 offset:32\n"
      "v_mul_f32 v151, v140, v151\n"
      "v_mfma_f32_16x16x32_bf16 v[178:181], v[116:119], a[212:215], v[178:181]\n"
      "s_cmp_gt_u32 s42, s44\n"
      "s_cselect_b32 s95, s94, s95\n"
      "v_mul_f32 v171, v140, v171\n"
      "v_mfma_f32_16x16x32_bf16 v[96:99], a[216:219], v[172:175], 0\n"
      "s_waitcnt lgkmcnt(1)\n"
      "v_mul_f32 v155, v140, v155\n"
      "v_cvt_pk_bf16_f32 v80, v206, v207\n"
      "v_mfma_f32_16x16x32_bf16 v[96:99], a[220:223], v[162:165], v[96:99]\n"
      "v_mul_f32_e64 v219, s50, v219\n"
      "v_cvt_pk_bf16_f32 v186, v204, v205\n"
      "v_mfma_f32_16x16x32_bf16 v[96:99], a[224:227], v[100:103], v[96:99]\n"
      "ds_read_b64 v[124:125], v10 offset:24\n"
      "v_cvt_pk_bf16_f32 v109, v110, v111\n"
      "v_mfma_f32_16x16x32_bf16 v[116:119], a[240:243], v[172:175], 0\n"
      "v_mul_f32 v152, v140, v152\n"
      "v_mul_f32 v169, v140, v169\n"
      "v_mfma_f32_16x16x32_bf16 v[116:119], a[244:247], v[162:165], v[116:119]\n"
      "v_mul_f32 v153, v140, v153\n"
      "v_mul_f32 v154, v140, v154\n"
      "v_mfma_f32_16x16x32_bf16 v[116:119], a[248:251], v[100:103], v[116:119]\n"
      "ds_read_b128 v[200:203], v2 offset:27648\n"
      "v_mul_f32 v150, v140, v150\n"
      "v_mfma_f32_16x16x32_bf16 v[134:137], v[232:235], v[172:175], 0\n"
      "ds_read_b128 v[172:175], v2 offset:18688\n"
      "v_cvt_pk_bf16_f32 v150, v150, v151\n"
      "v_mfma_f32_16x16x32_bf16 v[134:137], v[236:239], v[162:165], v[134:137]\n"
      "v_mul_f32 v168, v140, v168\n"
      "v_cvt_pk_bf16_f32 v151, v152, v153\n"
      "v_mfma_f32_16x16x32_bf16 v[134:137], v[240:243], v[100:103], v[134:137]\n"
      "s_add_u32 s42, s42, 1\n"
      "v_mul_f32 v157, v140, v157\n"
      "v_mul_f32 v156, v140, v156\n"
      "v_mfma_f32_16x16x32_bf16 v[162:165], v[210:213], v[220:223], v[40:43]\n"
      "s_waitcnt lgkmcnt(2)\n"
      "v_cvt_pk_bf16_f32 v152, v168, v169\n"
      "v_cvt_pk_bf16_f32 v153, v170, v171\n"
      "v_mfma_f32_16x16x32_bf16 v[204:207], v[146:149], v[220:223], v[44:47]\n"
      "ds_read_b128 v[112:115], v2 offset:33792\n"
      "v_mul_f32 v104, v219, v104\n"
      "v_mfma_f32_16x16x32_bf16 v[168:171], v[150:153], v[220:223], v[48:51]\n"
      "ds_read_b128 v[100:103], v2 offset:30720\n"
      "v_mul_f32 v105, v219, v105\n"
      "v_mfma_f32_16x16x32_bf16 v[140:143], v[126:129], v[220:223], v[24:27]\n"
      "s_waitcnt lgkmcnt(2)\n"
      "v_cvt_pk_bf16_f32 v81, v208, v209\n"
      "v_cvt_pk_bf16_f32 v154, v154, v155\n"
      "v_mfma_f32_16x16x16_bf16 v[162:165], v[138:139], v[176:177], v[162:165]\n"
      "v_mul_f32 v121, v219, v121\n"
      "v_cvt_pk_bf16_f32 v84, v178, v179\n"
      "v_cvt_pk_bf16_f32 v155, v156, v157\n"
      "v_mfma_f32_16x16x16_bf16 v[204:207], v[160:161], v[176:177], v[204:207]\n"
      "v_mul_f32_e64 v106, v219, v106\n"
      "v_cvt_pk_bf16_f32 v85, v180, v181\n"
      "v_cvt_pk_bf16_f32 v104, v104, v105\n"
      "v_mfma_f32_16x16x16_bf16 v[168:171], v[154:155], v[176:177], v[168:171]\n"
      "v_mul_f32 v120, v219, v120\n"
      "v_mul_f32 v107, v219, v107\n"
      "v_max3_f32 v156, v162, v163, v164\n"
      "v_mfma_f32_16x16x16_bf16 v[140:143], v[124:125], v[176:177], v[140:143]\n"
      "v_cvt_pk_bf16_f32 v120, v120, v121\n"
      "v_max3_f32 v156, v156, v206, v207\n"
      "v_max3_f32 v177, v165, v204, v205\n"
      "v_mfma_f32_16x16x32_bf16 v[96:99], a[228:231], v[200:203], v[96:99]\n"
      "s_waitcnt lgkmcnt(0)\n"
      "v_max3_f32 v177, v177, v168, v169\n"
      "v_max3_f32 v156, v156, v170, v171\n"
      "v_mfma_f32_16x16x32_bf16 v[96:99], a[232:235], v[100:103], v[96:99]\n"
      "v_max3_f32 v156, v156, v177, v140\n"
      "v_mov_b32 v157, v156\n"
      "v_mfma_f32_16x16x32_bf16 v[96:99], a[236:239], v[112:115], v[96:99]\n"
      "ds_read_b128 v[176:179], v2 offset:21760\n"
      "v_cvt_pk_bf16_f32 v105, v106, v107\n"
      "v_mfma_f32_16x16x32_bf16 v[116:119], a[252:255], v[200:203], v[116:119]\n"
      "v_permlane16_swap_b32_e64 v156, v157\n"
      "v_mfma_f32_16x16x32_bf16 v[116:119], v[224:227], v[100:103], v[116:119]\n"
      "v_max_f32 v156, v156, v157\n"
      "v_mov_b32 v157, v156\n"
      "v_mfma_f32_16x16x32_bf16 v[116:119], v[228:231], v[112:115], v[116:119]\n"
      "ds_read_b128 v[142:145], v2 offset:24832\n"
      "v_permlane32_swap_b32 v156, v157\n"
      "v_max_f32 v156, v156, v157\n"
      "v_sub_f32_e64 v140, v140, v156\n"
      "v_pk_mul_f32 v[122:123], v[122:123], v[218:219] op_sel:[0,1]\n"
      "v_pk_mul_f32 v[188:189], v[188:189], v[218:219] op_sel:[0,1]\n"
      "v_cvt_pk_bf16_f32 v106, v188, v189\n"
      "v_pk_add_f32 v[164:165], v[164:165], v[156:157] op_sel_hi:[1,0] neg_lo:[0,1] neg_hi:[0,1]\n"
      "v_pk_add_f32 v[204:205], v[204:205], v[156:157] op_sel_hi:[1,0] neg_lo:[0,1] neg_hi:[0,1]\n"
      "v_pk_mul_f32 v[190:191], v[190:191], v[218:219] op_sel:[0,1]\n"
      "v_cvt_pk_bf16_f32 v107, v190, v191\n"
      "v_pk_add_f32 v[206:207], v[206:207], v[156:157] op_sel_hi:[1,0] neg_lo:[0,1] neg_hi:[0,1]\n"
      "v_pk_add_f32 v[170:171], v[170:171], v[156:157] op_sel_hi:[1,0] neg_lo:[0,1] neg_hi:[0,1]\n"
      "v_pk_add_f32 v[168:169], v[168:169], v[156:157] op_sel_hi:[1,0] neg_lo:[0,1] neg_hi:[0,1]\n"
      "v_pk_add_f32 v[162:163], v[162:163], v[156:157] op_sel_hi:[1,0] neg_lo:[0,1] neg_hi:[0,1]\n"
      "v_mfma_f32_16x16x32_bf16 v[134:137], v[244:247], v[200:203], v[134:137]\n"
      "v_exp_f32 v162, v162\n"
      "v_mfma_f32_16x16x32_bf16 v[134:137], v[248:251], v[100:103], v[134:137]\n"
      "v_exp_f32 v164, v164\n"
      "v_mfma_f32_16x16x32_bf16 v[134:137], v[252:255], v[112:115], v[134:137]\n"
      "v_exp_f32 v206, v206\n"
      "v_mfma_f32_16x16x32_bf16 v[188:191], v[210:213], v[184:187], v[52:55]\n"
      "v_exp_f32 v207, v207\n"
      "v_mfma_f32_16x16x32_bf16 v[156:159], v[146:149], v[184:187], v[56:59]\n"
      "v_exp_f32 v140, v140\n"
      "v_mfma_f32_16x16x32_bf16 v[100:103], v[150:153], v[184:187], v[60:63]\n"
      "v_exp_f32 v163, v163\n"
      "v_mfma_f32_16x16x32_bf16 v[110:113], v[126:129], v[184:187], v[26:29]\n"
      "v_exp_f32_e64 v204, v204\n"
      "v_mfma_f32_16x16x16_bf16 v[188:191], v[138:139], v[108:109], v[188:191]\n"
      "v_exp_f32_e64 v168, v168\n"
      "v_cvt_pk_bf16_f32 v90, v140, 0\n"
      "v_mfma_f32_16x16x16_bf16 v[156:159], v[160:161], v[108:109], v[156:159]\n"
      "v_cvt_pk_bf16_f32 v162, v162, v163\n"
      "v_exp_f32 v165, v165\n"
      "v_mfma_f32_16x16x16_bf16 v[100:103], v[154:155], v[108:109], v[100:103]\n"
      "v_cvt_pk_bf16_f32 v163, v164, v165\n"
      "v_exp_f32 v169, v169\n"
      "v_mfma_f32_16x16x16_bf16 v[110:113], v[124:125], v[108:109], v[110:113]\n"
      "v_cvt_pk_bf16_f32 v165, v206, v207\n"
      "s_waitcnt vmcnt(1)\n"
      "v_or3_b32 v192, v192, v193, v194\n"
      "v_max3_f32 v187, v188, v189, v190\n"
      "v_mfma_f32_16x16x32_bf16 v[200:203], a[216:219], v[172:175], 0\n"
      "s_waitcnt lgkmcnt(0)\n"
      "v_max3_f32 v187, v187, v158, v159\n"
      "v_max3_f32 v187, v187, v102, v103\n"
      "v_mfma_f32_16x16x32_bf16 v[200:203], a[220:223], v[176:179], v[200:203]\n"
      "v_exp_f32 v205, v205\n"
      "v_mfma_f32_16x16x32_bf16 v[200:203], a[224:227], v[142:145], v[200:203]\n"
      "s_waitcnt vmcnt(0)\n"
      "v_or3_b32 v192, v192, v195, v89\n"
      "v_max3_f32 v219, v191, v156, v157\n"
      "v_mfma_f32_16x16x32_bf16 v[112:115], a[240:243], v[172:175], 0\n"
      "v_exp_f32 v170, v170\n"
      "v_mfma_f32_16x16x32_bf16 v[112:115], a[244:247], v[176:179], v[112:115]\n"
      "v_exp_f32 v171, v171\n"
      "v_mfma_f32_16x16x32_bf16 v[112:115], a[248:251], v[142:145], v[112:115]\n"
      "v_max3_f32 v219, v219, v100, v101\n"
      "v_cvt_pk_bf16_f32 v164, v204, v205\n"
      "v_mfma_f32_16x16x32_bf16 v[204:207], v[232:235], v[172:175], 0\n"
      "v_cvt_pk_bf16_f32 v89, v170, v171\n"
      "v_max3_f32 v187, v187, v219, v110\n"
      "v_mfma_f32_16x16x32_bf16 v[204:207], v[236:239], v[176:179], v[204:207]\n"
      "v_cvt_pk_bf16_f32 v88, v168, v169\n"
      "v_mov_b32 v219, v187\n"
      "v_mfma_f32_16x16x32_bf16 v[204:207], v[240:243], v[142:145], v[204:207]\n"
      "s_nop 0\n"
      "v_permlane16_swap_b32 v187, v219\n"
      "v_mfma_f32_16x16x32_bf16 v[174:177], v[32:35], v[162:165], 0\n"
      "v_max_f32 v187, v187, v219\n"
      "v_mov_b32 v219, v187\n"
      "v_mfma_f32_16x16x32_bf16 v[174:177], v[32:35], v[88:91], v[174:177]\n"
      "s_nop 0\n"
      "v_permlane32_swap_b32_e64 v187, v219\n"
      "v_mfma_f32_16x16x32_bf16 v[168:171], v[130:133], v[162:165], 0\n"
      "ds_read_u16 v86, v11 offset:64\n"
      "v_max_f32_e64 v187, v187, v219\n"
      "v_mfma_f32_16x16x32_bf16 v[168:171], v[76:79], v[88:91], v[168:171]\n"
      "ds_write_b32 v31, v192 offset:4608\n"
      "v_mfma_f32_16x16x32_bf16 v[218:221], v[196:199], v[162:165], 0\n"
      "v_rcp_f32 v174, v174\n"
      "v_mfma_f32_16x16x32_bf16 v[218:221], v[80:83], v[88:91], v[218:221]\n"
      "ds_read_b128 v[176:179], v2 offset:27904\n"
      "v_sub_f32 v110, v110, v187\n"
      "v_exp_f32_e64 v110, v110\n"
      "v_cvt_pk_bf16_f32 v121, v122, v123\n"
      "v_pk_add_f32 v[190:191], v[190:191], v[186:187] op_sel:[0,1] neg_lo:[0,1] neg_hi:[0,1]\n"
      "v_pk_mul_f32 v[170:171], v[170:171], v[174:175] op_sel_hi:[1,0]\n"
      "v_pk_add_f32 v[100:101], v[100:101], v[186:187] op_sel:[0,1] neg_lo:[0,1] neg_hi:[0,1]\n"
      "v_pk_add_f32 v[158:159], v[158:159], v[186:187] op_sel:[0,1] neg_lo:[0,1] neg_hi:[0,1]\n"
      "v_pk_add_f32 v[156:157], v[156:157], v[186:187] op_sel:[0,1] neg_lo:[0,1] neg_hi:[0,1]\n"
      "v_pk_mul_f32 v[220:221], v[220:221], v[174:175] op_sel_hi:[1,0]\n"
      "v_pk_mul_f32 v[168:169], v[168:169], v[174:175] op_sel_hi:[1,0]\n"
      "v_pk_mul_f32 v[218:219], v[218:219], v[174:175] op_sel_hi:[1,0]\n"
      "v_pk_add_f32 v[102:103], v[102:103], v[186:187] op_sel:[0,1] neg_lo:[0,1] neg_hi:[0,1]\n"
      "v_pk_add_f32 v[188:189], v[188:189], v[186:187] op_sel:[0,1] neg_lo:[0,1] neg_hi:[0,1]\n"
      "v_mfma_f32_16x16x32_bf16 v[184:187], v[214:217], v[162:165], 0\n"
      "s_waitcnt lgkmcnt(2)\n"
      "v_exp_f32 v103, v103\n"
      "v_cvt_pk_bf16_f32 v168, v168, v169\n"
      "v_mfma_f32_16x16x32_bf16 v[184:187], v[84:87], v[88:91], v[184:187]\n"
      "v_exp_f32 v100, v100\n"
      "v_mfma_f32_16x16x32_bf16 v[142:145], v[210:213], v[104:107], v[64:67]\n"
      "v_exp_f32_e64 v102, v102\n"
      "v_mfma_f32_16x16x32_bf16 v[180:183], v[146:149], v[104:107], v[68:71]\n"
      "s_waitcnt lgkmcnt(0)\n"
      "v_cvt_pk_bf16_f32 v169, v170, v171\n"
      "v_cvt_pk_bf16_f32 v170, v218, v219\n"
      "v_mfma_f32_16x16x32_bf16 v[164:167], v[150:153], v[104:107], v[72:75]\n"
      "ds_read_b128 v[192:195], v2 offset:30976\n"
      "v_cvt_pk_bf16_f32 v171, v220, v221\n"
      "v_mfma_f32_16x16x32_bf16 v[218:221], v[126:129], v[104:107], v[28:31]\n"
      "ds_read_b128 v[104:107], v2 offset:34048\n"
      "v_exp_f32_e64 v101, v101\n"
      "v_mfma_f32_16x16x16_bf16 v[142:145], v[138:139], v[120:121], v[142:145]\n"
      "v_cvt_pk_bf16_f32 v93, v102, v103\n"
      "v_exp_f32 v188, v188\n"
      "v_mfma_f32_16x16x16_bf16 v[180:183], v[160:161], v[120:121], v[180:183]\n"
      "v_exp_f32 v156, v156\n"
      "v_cvt_pk_bf16_f32 v92, v100, v101\n"
      "v_mfma_f32_16x16x16_bf16 v[164:167], v[154:155], v[120:121], v[164:167]\n"
      "v_cvt_pk_bf16_f32 v94, v110, 0\n"
      "v_exp_f32_e64 v189, v189\n"
      "v_mfma_f32_16x16x16_bf16 v[218:221], v[124:125], v[120:121], v[218:221]\n"
      "v_exp_f32 v190, v190\n"
      "v_cvt_pk_bf16_f32 v188, v188, v189\n"
      "v_mfma_f32_16x16x32_bf16 v[200:203], a[228:231], v[176:179], v[200:203]\n"
      "s_waitcnt lgkmcnt(1)\n"
      "v_exp_f32 v191, v191\n"
      "v_mfma_f32_16x16x32_bf16 v[200:203], a[232:235], v[192:195], v[200:203]\n"
      "s_waitcnt lgkmcnt(0)\n"
      "v_max3_f32 v162, v142, v143, v144\n"
      "v_cvt_pk_bf16_f32 v189, v190, v191\n"
      "v_mfma_f32_16x16x32_bf16 v[200:203], a[236:239], v[104:107], v[200:203]\n"
      "v_exp_f32 v158, v158\n"
      "v_mfma_f32_16x16x32_bf16 v[112:115], a[252:255], v[176:179], v[112:115]\n"
      "v_exp_f32 v159, v159\n"
      "v_mfma_f32_16x16x32_bf16 v[112:115], v[224:227], v[192:195], v[112:115]\n"
      "v_exp_f32_e64 v157, v157\n"
      "v_max3_f32 v162, v162, v182, v183\n"
      "v_mfma_f32_16x16x32_bf16 v[112:115], v[228:231], v[104:107], v[112:115]\n"
      "v_cvt_pk_bf16_f32 v191, v158, v159\n"
      "v_max3_f32 v121, v145, v180, v181\n"
      "v_mfma_f32_16x16x32_bf16 v[204:207], v[244:247], v[176:179], v[204:207]\n"
      "v_max3_f32 v162, v162, v166, v167\n"
      "v_cvt_pk_bf16_f32 v190, v156, v157\n"
      "v_mfma_f32_16x16x32_bf16 v[204:207], v[248:251], v[192:195], v[204:207]\n"
      "ds_read_b128 v[156:159], v7 offset:18432\n"
      "v_max3_f32 v121, v121, v164, v165\n"
      "v_mfma_f32_16x16x32_bf16 v[204:207], v[252:255], v[104:107], v[204:207]\n"
      "v_max3_f32 v162, v162, v121, v218\n"
      "v_mov_b32 v221, v162\n"
      "v_mfma_f32_16x16x32_bf16 v[104:107], v[32:35], v[188:191], 0\n"
      "s_nop 0\n"
      "v_permlane16_swap_b32_e64 v162, v221\n"
      "v_mfma_f32_16x16x32_bf16 v[104:107], v[32:35], v[92:95], v[104:107]\n"
      "ds_read_b128 v[176:179], v10\n"
      "v_max_f32 v162, v162, v221\n"
      "v_mfma_f32_16x16x32_bf16 v[120:123], v[130:133], v[188:191], 0\n"
      "v_mov_b32 v221, v162\n"
      "v_mul_f32 v195, v201, v201\n"
      "v_mfma_f32_16x16x32_bf16 v[120:123], v[76:79], v[92:95], v[120:123]\n"
      "ds_read_b64 v[102:103], v10 offset:16\n"
      "v_mul_f32 v111, v97, v97\n"
      "v_mfma_f32_16x16x32_bf16 v[106:109], v[196:199], v[188:191], 0\n"
      "v_permlane32_swap_b32 v162, v221\n"
      "v_mfma_f32_16x16x32_bf16 v[106:109], v[80:83], v[92:95], v[106:109]\n"
      "ds_write_b128 v3, v[168:171]\n"
      "v_pk_mul_f32 v[184:185], v[184:185], v[174:175] op_sel_hi:[1,0]\n"
      "v_max_f32 v162, v162, v221\n"
      "v_pk_mul_f32 v[186:187], v[186:187], v[174:175] op_sel_hi:[1,0]\n"
      "v_mfma_f32_16x16x32_bf16 v[168:171], v[214:217], v[188:191], 0\n"
      "s_waitcnt lgkmcnt(2)\n"
      "v_rcp_f32 v104, v104\n"
      "v_mfma_f32_16x16x32_bf16 v[168:171], v[84:87], v[92:95], v[168:171]\n"
      "ds_read_b128 v[172:175], v7 offset:22528\n"
      "v_cvt_pk_bf16_f32 v184, v184, v185\n"
      "v_mfma_f32_16x16x32_bf16 v[220:223], v[176:179], v[210:213], v[30:33]\n"
      "buffer_store_dwordx4 v[156:159], v20, s[24:27], s46 offen sc1\n"
      "v_cvt_pk_bf16_f32 v185, v186, v187\n"
      "v_pk_add_f32 v[180:181], v[180:181], v[162:163] op_sel_hi:[1,0] neg_lo:[0,1] neg_hi:[0,1]\n"
      "v_pk_mul_f32 v[108:109], v[108:109], v[104:105] op_sel_hi:[1,0]\n"
      "v_fma_f32 v110, v96, v96, s52\n"
      "v_pk_mul_f32 v[168:169], v[168:169], v[104:105] op_sel_hi:[1,0]\n"
      "v_pk_add_f32 v[166:167], v[166:167], v[162:163] op_sel_hi:[1,0] neg_lo:[0,1] neg_hi:[0,1]\n"
      "v_pk_mul_f32 v[120:121], v[120:121], v[104:105] op_sel_hi:[1,0]\n"
      "v_pk_add_f32 v[142:143], v[142:143], v[162:163] op_sel_hi:[1,0] neg_lo:[0,1] neg_hi:[0,1]\n"
      "v_pk_add_f32 v[182:183], v[182:183], v[162:163] op_sel_hi:[1,0] neg_lo:[0,1] neg_hi:[0,1]\n"
      "v_pk_add_f32 v[164:165], v[164:165], v[162:163] op_sel_hi:[1,0] neg_lo:[0,1] neg_hi:[0,1]\n"
      "v_pk_fma_f32 v[110:111], v[98:99], v[98:99], v[110:111]\n"
      "v_pk_fma_f32 v[110:111], v[116:117], v[116:117], v[110:111]\n"
      "v_mfma_f32_16x16x32_bf16 v[156:159], v[176:179], v[146:149], v[30:33]\n"
      "s_waitcnt lgkmcnt(0)\n"
      "v_exp_f32 v142, v142\n"
      "v_mfma_f32_16x16x32_bf16 v[146:149], v[176:179], v[150:153], v[30:33]\n"
      "ds_read_b128 v[190:193], v2 offset:22016\n"
      "v_cvt_pk_bf16_f32 v120, v120, v121\n"
      "v_mfma_f32_16x16x32_bf16 v[186:189], v[176:179], v[126:129], v[30:33]\n"
      "ds_read_b128 v[176:179], v2 offset:25088\n"
      "v_fma_f32 v194, v200, v200, s52\n"
      "v_mfma_f32_16x16x16_bf16 v[220:223], v[102:103], v[138:139], v[220:223]\n"
      "v_exp_f32 v166, v166\n"
      "v_sub_f32 v218, v218, v162\n"
      "v_mfma_f32_16x16x16_bf16 v[156:159], v[102:103], v[160:161], v[156:159]\n"
      "ds_read_b128 v[208:211], v2 offset:18944\n"
      "buffer_store_dwordx4 v[172:175], v21, s[24:27], s46 offen sc1\n"
      "v_pk_mul_f32 v[122:123], v[122:123], v[104:105] op_sel_hi:[1,0]\n"
      "v_pk_add_f32 v[144:145], v[144:145], v[162:163] op_sel_hi:[1,0] neg_lo:[0,1] neg_hi:[0,1]\n"
      "v_pk_fma_f32 v[110:111], v[118:119], v[118:119], v[110:111]\n"
      "v_pk_mul_f32 v[106:107], v[106:107], v[104:105] op_sel_hi:[1,0]\n"
      "v_pk_fma_f32 v[110:111], v[134:135], v[134:135], v[110:111]\n"
      "v_pk_mul_f32 v[170:171], v[170:171], v[104:105] op_sel_hi:[1,0]\n"
      "v_pk_fma_f32 v[110:111], v[136:137], v[136:137], v[110:111]\n"
      "v_pk_fma_f32 v[194:195], v[202:203], v[202:203], v[194:195]\n"
      "v_mfma_f32_16x16x16_bf16 v[146:149], v[102:103], v[154:155], v[146:149]\n"
      "v_cndmask_b32 v220, v220, v156, s[58:59]\n"
      "v_exp_f32_e64 v143, v143\n"
      "v_exp_f32 v164, v164\n"
      "v_mfma_f32_16x16x16_bf16 v[186:189], v[102:103], v[124:125], v[186:189]\n"
      "v_exp_f32_e64 v167, v167\n"
      "s_waitcnt lgkmcnt(0)\n"
      "v_mfma_f32_16x16x32_bf16 v[156:159], a[216:219], v[208:211], 0\n"
      "ds_read_b128 v[172:175], v7 offset:26624\n"
      "v_cndmask_b32 v220, v220, v146, s[60:61]\n"
      "v_mfma_f32_16x16x32_bf16 v[156:159], a[220:223], v[190:193], v[156:159]\n"
      "v_exp_f32_e64 v144, v144\n"
      "v_mfma_f32_16x16x32_bf16 v[156:159], a[224:227], v[176:179], v[156:159]\n"
      "ds_read_b128 v[126:129], v2 offset:31232\n"
      "v_cvt_pk_bf16_f32 v121, v122, v123\n"
      "v_mfma_f32_16x16x32_bf16 v[146:149], a[240:243], v[208:211], 0\n"
      "ds_read_b128 v[102:105], v2 offset:28160\n"
      "v_cvt_pk_bf16_f32 v123, v108, v109\n"
      "v_mfma_f32_16x16x32_bf16 v[146:149], a[244:247], v[190:193], v[146:149]\n"
      "ds_write_b64 v4, v[184:185]\n"
      "v_exp_f32_e64 v182, v182\n"
      "v_cndmask_b32 v220, v220, v186, s[62:63]\n"
      "v_pk_fma_f32 v[194:195], v[112:113], v[112:113], v[194:195]\n"
      "v_mfma_f32_16x16x32_bf16 v[146:149], a[248:251], v[176:179], v[146:149]\n"
      "ds_read_b128 v[138:141], v2 offset:34304\n"
      "v_exp_f32_e64 v218, v218\n"
      "v_cvt_pk_bf16_f32 v142, v142, v143\n"
      "v_max_f32_dpp v109, v220, v220 row_ror:8 row_mask:0xf bank_mask:0xf\n"
      "v_pk_fma_f32 v[194:195], v[114:115], v[114:115], v[194:195]\n"
      "v_mfma_f32_16x16x32_bf16 v[160:163], v[232:235], v[208:211], 0\n"
      "v_add_f32_e64 v110, v110, v111\n"
      "v_max_f32_dpp v109, v109, v109 row_ror:4 row_mask:0xf bank_mask:0xf\n"
      "v_mfma_f32_16x16x32_bf16 v[160:163], v[236:239], v[190:193], v[160:163]\n"
      "s_waitcnt lgkmcnt(2)\n"
      "v_cvt_pk_bf16_f32 v168, v168, v169\n"
      "v_exp_f32 v180, v180\n"
      "v_max_f32_dpp v109, v109, v109 row_ror:2 row_mask:0xf bank_mask:0xf\n"
      "v_mfma_f32_16x16x32_bf16 v[160:163], v[240:243], v[176:179], v[160:163]\n"
      "buffer_store_dwordx4 v[172:175], v22, s[24:27], s46 offen sc1\n"
      "v_mfma_f32_16x16x32_bf16 v[156:159], a[228:231], v[102:105], v[156:159]\n"
      "v_exp_f32_e64 v145, v145\n"
      "v_mfma_f32_16x16x32_bf16 v[156:159], a[232:235], v[126:129], v[156:159]\n"
      "s_waitcnt lgkmcnt(0)\n"
      "v_exp_f32 v165, v165\n"
      "v_max_f32_dpp v109, v109, v109 row_ror:1 row_mask:0xf bank_mask:0xf\n"
      "v_mfma_f32_16x16x32_bf16 v[156:159], a[236:239], v[138:141], v[156:159]\n"
      "v_cvt_pk_bf16_f32 v90, v218, 0\n"
      "v_max_f32_dpp v109, v109, v109 row_bcast:15 row_mask:0xa bank_mask:0xf\n"
      "v_exp_f32_e64 v181, v181\n"
      "v_cvt_pk_bf16_f32 v89, v166, v167\n"
      "v_mfma_f32_16x16x32_bf16 v[146:149], a[252:255], v[102:105], v[146:149]\n"
      "v_max_f32_dpp v109, v109, v109 row_bcast:31 row_mask:0xc bank_mask:0xf\n"
      "v_cvt_pk_bf16_f32 v169, v170, v171\n"
      "v_mfma_f32_16x16x32_bf16 v[146:149], v[224:227], v[126:129], v[146:149]\n"
      "v_readlane_b32 s72, v109, 63\n"
      "v_mul_f32_e64 v193, v157, v157\n"
      "v_mfma_f32_16x16x32_bf16 v[146:149], v[228:231], v[138:141], v[146:149]\n"
      "ds_read_b128 v[208:211], v7 offset:30720\n"
      "v_exp_f32 v183, v183\n"
      "v_mfma_f32_16x16x32_bf16 v[160:163], v[244:247], v[102:105], v[160:163]\n"
      "v_subrev_f32 v220, s72, v220\n"
      "v_cvt_pk_bf16_f32 v143, v144, v145\n"
      "v_mfma_f32_16x16x32_bf16 v[160:163], v[248:251], v[126:129], v[160:163]\n"
      "ds_read_b128 v[102:105], v7 offset:34816\n"
      "v_cvt_pk_bf16_f32 v144, v180, v181\n"
      "v_mfma_f32_16x16x32_bf16 v[160:163], v[252:255], v[138:141], v[160:163]\n"
      "v_cvt_pk_bf16_f32 v88, v164, v165\n"
      "v_exp_f32_e64 v220, v220\n"
      "v_cvt_pk_bf16_f32 v145, v182, v183\n"
      "v_cvt_pk_bf16_f32 v220, v220, v220\n"
      "v_pk_fma_f32 v[194:195], v[204:205], v[204:205], v[194:195]\n"
      "v_mfma_f32_16x16x32_bf16 v[170:173], v[32:35], v[142:145], 0\n"
      "ds_write_b16 v13, v220\n"
      "v_mfma_f32_16x16x32_bf16 v[170:173], v[32:35], v[88:91], v[170:173]\n"
      "ds_read_b128 v[218:221], v14\n"
      "v_cvt_pk_bf16_f32 v122, v106, v107\n"
      "v_mfma_f32_16x16x32_bf16 v[106:109], v[130:133], v[142:145], 0\n"
      "ds_write_b64 v4, v[168:169] offset:256\n"
      "v_mfma_f32_16x16x32_bf16 v[106:109], v[76:79], v[88:91], v[106:109]\n"
      "ds_read_b128 v[174:177], v14 offset:64\n"
      "v_fma_f32 v192, v156, v156, s52\n"
      "v_mfma_f32_16x16x32_bf16 v[126:129], v[196:199], v[142:145], 0\n"
      "s_waitcnt lgkmcnt(4)\n"
      "v_rcp_f32 v170, v170\n"
      "v_mfma_f32_16x16x32_bf16 v[126:129], v[80:83], v[88:91], v[126:129]\n"
      "ds_write_b128 v3, v[120:123] offset:256\n"
      "v_pk_fma_f32 v[192:193], v[158:159], v[158:159], v[192:193]\n"
      "v_mfma_f32_16x16x32_bf16 v[166:169], v[214:217], v[142:145], 0\n"
      "buffer_store_dwordx4 v[208:211], v23, s[24:27], s46 offen sc1\n"
      "v_pk_fma_f32 v[192:193], v[146:147], v[146:147], v[192:193]\n"
      "v_pk_mul_f32 v[106:107], v[106:107], v[170:171] op_sel_hi:[1,0]\n"
      "v_pk_fma_f32 v[192:193], v[148:149], v[148:149], v[192:193]\n"
      "v_pk_mul_f32 v[108:109], v[108:109], v[170:171] op_sel_hi:[1,0]\n"
      "v_pk_fma_f32 v[192:193], v[160:161], v[160:161], v[192:193]\n"
      "v_pk_mul_f32 v[126:127], v[126:127], v[170:171] op_sel_hi:[1,0]\n"
      "v_pk_fma_f32 v[194:195], v[206:207], v[206:207], v[194:195]\n"
      "v_pk_mul_f32 v[128:129], v[128:129], v[170:171] op_sel_hi:[1,0]\n"
      "v_cvt_pk_bf16_f32 v106, v106, v107\n"
      "v_add_f32_e64 v194, v194, v195\n"
      "v_pk_fma_f32 v[192:193], v[162:163], v[162:163], v[192:193]\n"
      "v_mfma_f32_16x16x32_bf16 v[166:169], v[84:87], v[88:91], v[166:169]\n"
      "s_waitcnt lgkmcnt(1)\n"
      "v_permlane16_swap_b32 v110, v194\n"
      "v_add_f32_e64 v192, v192, v193\n"
      "v_cvt_pk_bf16_f32 v107, v108, v109\n"
      "v_cvt_pk_bf16_f32 v108, v126, v127\n"
      "v_add_f32 v110, v110, v194\n"
      "v_cvt_pk_bf16_f32 v109, v128, v129\n"
      "v_mov_b32_e64 v194, v192\n"
      "s_add_u32 s43, s43, 1\n"
      "ds_write_b128 v3, v[106:109] offset:512\n"
      "v_permlane16_swap_b32 v192, v194\n"
      "v_add_f32 v192, v192, v194\n"
      "v_pk_mul_f32 v[168:169], v[168:169], v[170:171] op_sel_hi:[1,0]\n"
      "v_pk_mul_f32 v[166:167], v[166:167], v[170:171] op_sel_hi:[1,0]\n"
      "v_permlane32_swap_b32 v110, v192\n"
      "v_add_f32 v110, v110, v192\n"
      "v_mfma_f32_16x16x32_bf16 v[208:211], v[218:221], v[32:35], 0\n"
      "ds_write_b32 v31, v110\n"
      "v_cvt_pk_bf16_f32 v166, v166, v167\n"
      "v_cvt_pk_bf16_f32 v167, v168, v169\n"
      "v_mfma_f32_16x16x32_bf16 v[208:211], v[174:177], v[32:35], v[208:211]\n"
      "ds_write_b64 v4, v[166:167] offset:512\n"
      "buffer_store_dwordx4 v[102:105], v25, s[24:27], s46 offen sc1\n"
      "s_cmp_ge_u32 s95, 0x40000000\n"
      "s_cselect_b64 vcc, -1, s[56:57]\n"
      "s_waitcnt lgkmcnt(0)\n"
      "s_barrier\n"
      "s_cbranch_vccz L_max1_%=\n"
      "L_cold_%=:\n"
      "L_disp1_%=:\n"
      "s_cmp_lt_u32 s95, 0x40000000\n"
      "s_cbranch_scc1 L_nofix_11_%=\n"
      "s_cmp_eq_u32 s95, -2\n"
      "s_cbranch_scc1 L_tail1_%=\n"
      "s_nop 2\n"
      "v_lshlrev_b32 v169, 3, v36\n"
      "s_add_u32 s72, s98, L_cold_%=-L_ipf0_%=\n"
      "s_addc_u32 s73, s99, 0\n"
      "s_mov_b32 s74, L_cend_%=-L_cold_%=\n"
      "s_mov_b32 s75, 0x20000\n"
      "s_mov_b32 m0, 161792\n"
      "s_nop 0\n"
      "buffer_load_dwordx4 v169, s[72:75], 0 offen  lds\n"
      "s_lshr_b32 s92, s84, 30\n"
      "s_or_b32 s92, s92, s66\n"
      "s_cbranch_scc0 L_nf_12_%=\n"
      "s_bitcmp1_b32 s84, 31\n"
      "s_cselect_b32 s75, 100270464, 0\n"
      "s_bitcmp1_b32 s84, 30\n"
      "s_cselect_b32 s76, 196224, 0\n"
      "s_and_b32 s74, s84, 0x3fffffff\n"
      "s_cmp_ge_u32 s84, 0x40000000\n"
      "s_cselect_b32 s74, s74, 0\n"
      "s_cselect_b32 s84, 0, s84\n"
      "s_sub_u32 s74, s74, s66\n"
      "s_add_u32 s66, s66, s74\n"
      "s_sub_u32 s75, s75, s67\n"
      "s_add_u32 s67, s67, s75\n"
      "s_sub_u32 s76, s76, s68\n"
      "s_add_u32 s68, s68, s76\n"
      "v_add_u32 v16, s74, v16\n"
      "v_add_u32 v17, s74, v17\n"
      "v_add_u32 v18, s74, v18\n"
      "v_add_u32 v19, s74, v19\n"
      "v_add_u32 v15, s74, v15\n"
      "s_cmp_eq_u32 s75, 0\n"
      "s_cbranch_scc1 L_ax_13_%=\n"
      "v_bfe_i32 v169, v27, 0, 1\n"
      "v_and_b32 v169, s75, v169\n"
      "v_sub_u32 v16, v16, v169\n"
      "v_bfe_i32 v169, v27, 2, 1\n"
      "v_and_b32 v169, s75, v169\n"
      "v_sub_u32 v17, v17, v169\n"
      "v_bfe_i32 v169, v27, 4, 1\n"
      "v_and_b32 v169, s75, v169\n"
      "v_sub_u32 v18, v18, v169\n"
      "v_bfe_i32 v169, v27, 6, 1\n"
      "v_and_b32 v169, s75, v169\n"
      "v_sub_u32 v19, v19, v169\n"
      "v_bfe_i32 v169, v27, 8, 1\n"
      "v_and_b32 v169, s75, v169\n"
      "v_sub_u32 v15, v15, v169\n"
      "L_ax_13_%=:\n"
      "s_cmp_eq_u32 s76, 0\n"
      "s_cbranch_scc1 L_ax_14_%=\n"
      "v_bfe_i32 v169, v27, 1, 1\n"
      "v_and_b32 v169, s76, v169\n"
      "v_sub_u32 v16, v16, v169\n"
      "v_bfe_i32 v169, v27, 3, 1\n"
      "v_and_b32 v169, s76, v169\n"
      "v_sub_u32 v17, v17, v169\n"
      "v_bfe_i32 v169, v27, 5, 1\n"
      "v_and_b32 v169, s76, v169\n"
      "v_sub_u32 v18, v18, v169\n"
      "v_bfe_i32 v169, v27, 7, 1\n"
      "v_and_b32 v169, s76, v169\n"
      "v_sub_u32 v19, v19, v169\n"
      "v_bfe_i32 v169, v27, 9, 1\n"
      "v_and_b32 v169, s76, v169\n"
      "v_sub_u32 v15, v15, v169\n"
      "L_ax_14_%=:\n"
      "s_or_b32 s48, s66, s69\n"
      "s_cmp_lg_u32 s48, 0\n"
      "s_cselect_b32 s48, 0x40000000, 0\n"
      "L_nf_12_%=:\n"
      "s_lshr_b32 s92, s85, 30\n"
      "s_or_b32 s92, s92, s69\n"
      "s_cbranch_scc0 L_nf_15_%=\n"
      "s_bitcmp1_b32 s85, 31\n"
      "s_cselect_b32 s75, 100270464, 0\n"
      "s_bitcmp1_b32 s85, 30\n"
      "s_cselect_b32 s76, 196224, 0\n"
      "s_and_b32 s74, s85, 0x3fffffff\n"
      "s_cmp_ge_u32 s85, 0x40000000\n"
      "s_cselect_b32 s74, s74, 0\n"
      "s_cselect_b32 s85, 0, s85\n"
      "s_sub_u32 s74, s74, s69\n"
      "s_add_u32 s69, s69, s74\n"
      "s_sub_u32 s75, s75, s70\n"
      "s_add_u32 s70, s70, s75\n"
      "s_sub_u32 s76, s76, s71\n"
      "s_add_u32 s71, s71, s76\n"
      "v_add_u32 v20, s74, v20\n"
      "v_add_u32 v21, s74, v21\n"
      "v_add_u32 v22, s74, v22\n"
      "v_add_u32 v23, s74, v23\n"
      "v_add_u32 v25, s74, v25\n"
      "s_cmp_eq_u32 s75, 0\n"
      "s_cbranch_scc1 L_ax_16_%=\n"
      "v_bfe_i32 v169, v27, 10, 1\n"
      "v_and_b32 v169, s75, v169\n"
      "v_sub_u32 v20, v20, v169\n"
      "v_bfe_i32 v169, v27, 12, 1\n"
      "v_and_b32 v169, s75, v169\n"
      "v_sub_u32 v21, v21, v169\n"
      "v_bfe_i32 v169, v27, 14, 1\n"
      "v_and_b32 v169, s75, v169\n"
      "v_sub_u32 v22, v22, v169\n"
      "v_bfe_i32 v169, v27, 16, 1\n"
      "v_and_b32 v169, s75, v169\n"
      "v_sub_u32 v23, v23, v169\n"
      "v_bfe_i32 v169, v27, 18, 1\n"
      "v_and_b32 v169, s75, v169\n"
      "v_sub_u32 v25, v25, v169\n"
      "L_ax_16_%=:\n"
      "s_cmp_eq_u32 s76, 0\n"
      "s_cbranch_scc1 L_ax_17_%=\n"
      "v_bfe_i32 v169, v27, 11, 1\n"
      "v_and_b32 v169, s76, v169\n"
      "v_sub_u32 v20, v20, v169\n"
      "v_bfe_i32 v169, v27, 13, 1\n"
      "v_and_b32 v169, s76, v169\n"
      "v_sub_u32 v21, v21, v169\n"
      "v_bfe_i32 v169, v27, 15, 1\n"
      "v_and_b32 v169, s76, v169\n"
      "v_sub_u32 v22, v22, v169\n"
      "v_bfe_i32 v169, v27, 17, 1\n"
      "v_and_b32 v169, s76, v169\n"
      "v_sub_u32 v23, v23, v169\n"
      "v_bfe_i32 v169, v27, 19, 1\n"
      "v_and_b32 v169, s76, v169\n"
      "v_sub_u32_e64 v25, v25, v169\n"
      "L_ax_17_%=:\n"
      "s_or_b32 s48, s66, s69\n"
      "s_cmp_lg_u32 s48, 0\n"
      "s_cselect_b32 s48, 0x40000000, 0\n"
      "L_nf_15_%=:\n"
      "s_sub_u32 s92, s42, 1\n"
      "s_cmp_gt_u32 s92, s44\n"
      "s_cbranch_scc1 L_tail1_%=\n"
      "L_nofix_11_%=:\n"
      "s_cmp_lg_u64 s[56:57], 0\n"
      "s_cbranch_scc1 L_slow1_%=\n"
      "L_sel1_%=:\n"
      "s_cmp_lg_u64 s[54:55], 0\n"
      "s_cbranch_scc1 L_max1_%=\n"
      "s_branch L_loop1_%=\n"
      "L_slow1_%=:\n"
      "s_nop 4\n"
      "v_mfma_f32_16x16x32_bf16 v[168:171], v[218:221], v[130:133], 0\n"
      "ds_read_b128 v[182:185], v0 offset:18432\n"
      "ds_read_b128 v[190:193], v9\n"
      "v_mfma_f32_16x16x32_bf16 v[168:171], v[174:177], v[76:79], v[168:171]\n"
      "ds_read_b128 v[142:145], v0 offset:24576\n"
      "v_subrev_u32 v132, 115456, v9\n"
      "v_subrev_u32 v223, 115456, v9\n"
      "v_mfma_f32_16x16x32_bf16 v[150:153], v[218:221], v[196:199], 0\n"
      "ds_read_b128 v[108:111], v1 offset:18432\n"
      "s_add_u32 s72, s42, 1\n"
      "v_mfma_f32_16x16x32_bf16 v[150:153], v[174:177], v[80:83], v[150:153]\n"
      "ds_read_b128 v[164:167], v9 offset:4608\n"
      "v_bfe_u32 v77, v223, 4, 4\n"
      "v_mfma_f32_16x16x32_bf16 v[194:197], v[218:221], v[214:217], 0\n"
      "ds_read_b128 v[218:221], v1 offset:24576\n"
      "v_readlane_b32 s77, v38, s72\n"
      "v_mfma_f32_16x16x32_bf16 v[194:197], v[174:177], v[84:87], v[194:197]\n"
      "s_and_b32 s73, s77, -16\n"
      "s_mov_b32 m0, 110592\n"
      "s_waitcnt lgkmcnt(5)\n"
      "v_mfma_f32_16x16x32_bf16 v[174:177], a[0:3], v[182:185], 0\n"
      "buffer_load_dwordx4 v132, s[28:31], s73 offen  lds\n"
      "v_mfma_f32_16x16x32_bf16 v[170:173], a[24:27], v[182:185], 0\n"
      "ds_read_b128 v[186:189], v1 offset:30720\n"
      "v_subrev_u32 v39, 115456, v9\n"
      "v_mfma_f32_16x16x32_bf16 v[120:123], a[48:51], v[182:185], 0\n"
      "s_waitcnt lgkmcnt(4)\n"
      "v_add_f32 v192, v192, v193\n"
      "s_mov_b32 m0, 111616\n"
      "v_mfma_f32_16x16x32_bf16 v[178:181], a[72:75], v[182:185], 0\n"
      "s_add_u32 s73, s73, 1024\n"
      "v_add_f32 v190, v190, v191\n"
      "v_add_f32 v190, v190, v192\n"
      "v_mfma_f32_16x16x32_bf16 v[138:141], a[96:99], v[182:185], 0\n"
      "s_waitcnt lgkmcnt(2)\n"
      "v_rsq_f32 v190, v190\n"
      "v_mfma_f32_16x16x32_bf16 v[124:127], a[120:123], v[182:185], 0\n"
      "ds_read_b128 v[100:103], v1 offset:26624\n"
      "v_mul_f32 v190, 0x415db3d7, v190\n"
      "v_mfma_f32_16x16x32_bf16 v[174:177], a[4:7], v[108:111], v[174:177]\n"
      "ds_read_b128 v[152:155], v0 offset:30720\n"
      "v_mov_b32_e64 v192, v190\n"
      "v_mfma_f32_16x16x32_bf16 v[174:177], a[8:11], v[142:145], v[174:177]\n"
      "buffer_load_dwordx4 v132, s[28:31], s73 offen  lds\n"
      "v_mfma_f32_16x16x32_bf16 v[170:173], a[28:31], v[108:111], v[170:173]\n"
      "v_permlane32_swap_b32 v190, v192\n"
      "v_mfma_f32_16x16x32_bf16 v[170:173], a[32:35], v[142:145], v[170:173]\n"
      "s_add_u32 s73, s73, 1024\n"
      "v_lshrrev_b32 v223, 8, v223\n"
      "v_mov_b32 v191, v190\n"
      "v_mfma_f32_16x16x32_bf16 v[120:123], a[52:55], v[108:111], v[120:123]\n"
      "s_mov_b32 m0, 112640\n"
      "v_permlane16_swap_b32 v190, v191\n"
      "v_mfma_f32_16x16x32_bf16 v[120:123], a[56:59], v[142:145], v[120:123]\n"
      "v_mul_f32 v203, v191, v203\n"
      "v_mul_f32 v201, v191, v201\n"
      "v_mfma_f32_16x16x32_bf16 v[178:181], a[76:79], v[108:111], v[178:181]\n"
      "buffer_load_dwordx4 v132, s[28:31], s73 offen  lds\n"
      "v_mfma_f32_16x16x32_bf16 v[178:181], a[80:83], v[142:145], v[178:181]\n"
      "v_mul_f32 v200, v191, v200\n"
      "v_cvt_pk_bf16_f32 v200, v200, v201\n"
      "v_mfma_f32_16x16x32_bf16 v[138:141], a[100:103], v[108:111], v[138:141]\n"
      "s_waitcnt lgkmcnt(0)\n"
      "v_mul_u32_u24 v78, 98, v77\n"
      "v_mul_f32 v118, v190, v118\n"
      "v_mfma_f32_16x16x32_bf16 v[138:141], a[104:107], v[142:145], v[138:141]\n"
      "s_mov_b32 m0, 113664\n"
      "v_mul_f32 v98, v190, v98\n"
      "v_mul_f32_e64 v135, v190, v135\n"
      "v_mfma_f32_16x16x32_bf16 v[124:127], a[124:127], v[108:111], v[124:127]\n"
      "s_add_u32 s73, s73, 1024\n"
      "v_mul_f32_e64 v134, v190, v134\n"
      "v_mov_b32_dpp v208, v208 quad_perm:[0,0,0,0] row_mask:0xf bank_mask:0xf\n"
      "v_mfma_f32_16x16x32_bf16 v[124:127], a[128:131], v[142:145], v[124:127]\n"
      "ds_read_b128 v[210:213], v1 offset:32768\n"
      "v_mul_f32_e64 v157, v192, v157\n"
      "v_mfma_f32_16x16x32_bf16 v[174:177], a[12:15], v[218:221], v[174:177]\n"
      "v_cvt_pk_bf16_f32 v134, v134, v135\n"
      "v_mul_f32 v119, v190, v119\n"
      "v_mfma_f32_16x16x32_bf16 v[174:177], a[16:19], v[152:155], v[174:177]\n"
      "v_mul_f32 v202, v191, v202\n"
      "v_mul_f32_e64 v99, v190, v99\n"
      "v_mfma_f32_16x16x32_bf16 v[174:177], a[20:23], v[186:189], v[174:177]\n"
      "buffer_load_dwordx4 v132, s[28:31], s73 offen  lds\n"
      "v_mfma_f32_16x16x32_bf16 v[170:173], a[36:39], v[218:221], v[170:173]\n"
      "s_mov_b32 m0, 114688\n"
      "v_mul_f32 v156, v192, v156\n"
      "v_mul_f32 v163, v192, v163\n"
      "v_mfma_f32_16x16x32_bf16 v[170:173], a[40:43], v[152:155], v[170:173]\n"
      "s_add_u32 s73, s73, 1024\n"
      "v_readlane_b32 s45, v37, s43\n"
      "v_cndmask_b32 v168, v168, v150, s[58:59]\n"
      "v_mfma_f32_16x16x32_bf16 v[170:173], a[44:47], v[186:189], v[170:173]\n"
      "s_mov_b32 exec_hi, 0xffff\n"
      "buffer_load_dwordx4 v132, s[28:31], s73 offen  lds\n"
      "s_mov_b32 exec_hi, -1\n"
      "v_pk_mul_f32 v[112:113], v[112:113], v[190:191] op_sel:[0,1]\n"
      "v_pk_mul_f32 v[206:207], v[206:207], v[190:191] op_sel:[0,1]\n"
      "v_pk_mul_f32 v[116:117], v[116:117], v[190:191] op_sel_hi:[1,0]\n"
      "v_pk_mul_f32 v[204:205], v[204:205], v[190:191] op_sel:[0,1]\n"
      "v_pk_mul_f32 v[136:137], v[136:137], v[190:191] op_sel_hi:[1,0]\n"
      "v_pk_mul_f32 v[96:97], v[96:97], v[190:191] op_sel_hi:[1,0]\n"
      "v_mfma_f32_16x16x32_bf16 v[120:123], a[60:63], v[218:221], v[120:123]\n"
      "s_add_u32 m0, s64, 0\n"
      "v_cvt_pk_bf16_f32 v96, v96, v97\n"
      "v_mul_f32_e64 v115, v191, v115\n"
      "v_mfma_f32_16x16x32_bf16 v[120:123], a[64:67], v[152:155], v[120:123]\n"
      "v_cvt_pk_bf16_f32 v135, v136, v137\n"
      "v_cvt_pk_bf16_f32 v97, v98, v99\n"
      "v_mfma_f32_16x16x32_bf16 v[120:123], a[68:71], v[186:189], v[120:123]\n"
      "buffer_load_dwordx4 v16, s[16:19], s84 offen  lds\n"
      "v_mfma_f32_16x16x32_bf16 v[178:181], a[84:87], v[218:221], v[178:181]\n"
      "s_add_u32 m0, s64, 4096\n"
      "v_cvt_pk_bf16_f32 v201, v202, v203\n"
      "v_mul_f32 v149, v192, v149\n"
      "v_mfma_f32_16x16x32_bf16 v[178:181], a[88:91], v[152:155], v[178:181]\n"
      "ds_write_b64 v6, v[134:135] offset:18432\n"
      "v_mfma_f32_16x16x32_bf16 v[178:181], a[92:95], v[186:189], v[178:181]\n"
      "v_mul_f32 v148, v192, v148\n"
      "v_cvt_pk_bf16_f32 v99, v118, v119\n"
      "v_mfma_f32_16x16x32_bf16 v[138:141], a[108:111], v[218:221], v[138:141]\n"
      "ds_read_b128 v[134:137], v1 offset:20480\n"
      "v_cvt_pk_bf16_f32 v98, v116, v117\n"
      "v_mfma_f32_16x16x32_bf16 v[138:141], a[112:115], v[152:155], v[138:141]\n"
      "v_mul_f32_e64 v114, v191, v114\n"
      "v_cvt_pk_bf16_f32 v202, v112, v113\n"
      "v_mfma_f32_16x16x32_bf16 v[138:141], a[116:119], v[186:189], v[138:141]\n"
      "buffer_load_dwordx4 v17, s[16:19], s84 offen  lds\n"
      "v_mfma_f32_16x16x32_bf16 v[124:127], a[132:135], v[218:221], v[124:127]\n"
      "ds_write_b128 v5, v[96:99] offset:18432\n"
      "v_pk_mul_f32 v[160:161], v[160:161], v[192:193] op_sel_hi:[1,0]\n"
      "v_mfma_f32_16x16x32_bf16 v[124:127], a[136:139], v[152:155], v[124:127]\n"
      "v_fma_f32 v195, v178, v178, s51\n"
      "v_mul_f32_e64 v162, v192, v162\n"
      "v_mfma_f32_16x16x32_bf16 v[124:127], a[140:143], v[186:189], v[124:127]\n"
      "s_add_u32 m0, s64, 8192\n"
      "v_fma_f32 v80, v174, v174, s51\n"
      "v_cvt_pk_bf16_f32 v203, v114, v115\n"
      "v_mfma_f32_16x16x32_bf16 v[130:133], v[182:185], a[144:147], 0\n"
      "ds_write_b128 v5, v[200:203] offset:24576\n"
      "v_mfma_f32_16x16x32_bf16 v[130:133], v[108:111], a[148:151], v[130:133]\n"
      "buffer_load_dwordx4 v18, s[16:19], s84 offen  lds\n"
      "v_mfma_f32_16x16x32_bf16 v[130:133], v[142:145], a[152:155], v[130:133]\n"
      "v_mul_f32_e64 v159, v192, v159\n"
      "v_or3_b32 v164, v164, v165, v166\n"
      "v_mfma_f32_16x16x32_bf16 v[130:133], v[218:221], a[156:159], v[130:133]\n"
      "v_mul_f32_e64 v158, v192, v158\n"
      "v_cvt_pk_bf16_f32 v156, v156, v157\n"
      "v_mfma_f32_16x16x32_bf16 v[130:133], v[152:155], a[160:163], v[130:133]\n"
      "ds_read_b128 v[96:99], v0 offset:32768\n"
      "v_mul_f32 v147, v192, v147\n"
      "v_mfma_f32_16x16x32_bf16 v[130:133], v[186:189], a[164:167], v[130:133]\n"
      "s_add_u32 m0, s64, 12288\n"
      "v_fmac_f32 v80, v175, v175\n"
      "v_cndmask_b32 v168, v168, v194, s[60:61]\n"
      "v_mfma_f32_16x16x32_bf16 v[196:199], v[182:185], a[168:171], 0\n"
      "buffer_load_dwordx4 v19, s[16:19], s84 offen  lds\n"
      "v_mfma_f32_16x16x32_bf16 v[196:199], v[108:111], a[172:175], v[196:199]\n"
      "s_add_u32 m0, s65, 16384\n"
      "v_bitop3_b32 v164, v164, v167, s33 bitop3:0xa8\n"
      "v_fmac_f32_e64 v195, v179, v179\n"
      "v_mfma_f32_16x16x32_bf16 v[196:199], v[142:145], a[176:179], v[196:199]\n"
      "v_cvt_pk_bf16_f32 v204, v204, v205\n"
      "v_fmac_f32_e64 v195, v180, v180\n"
      "v_mfma_f32_16x16x32_bf16 v[196:199], v[218:221], a[180:183], v[196:199]\n"
      "ds_read_b128 v[116:119], v0 offset:20480\n"
      "v_cvt_pk_bf16_f32 v130, v130, v131\n"
      "v_mfma_f32_16x16x32_bf16 v[196:199], v[152:155], a[184:187], v[196:199]\n"
      "ds_read_b128 v[200:203], v0 offset:28672\n"
      "v_fmac_f32_e64 v80, v176, v176\n"
      "v_mfma_f32_16x16x32_bf16 v[196:199], v[186:189], a[188:191], v[196:199]\n"
      "buffer_load_dwordx4 v15, s[16:19], s84 offen  lds\n"
      "v_mfma_f32_16x16x32_bf16 v[214:217], v[182:185], a[192:195], 0\n"
      "ds_read_b128 v[112:115], v0 offset:26624\n"
      "v_fmac_f32 v195, v181, v181\n"
      "v_mfma_f32_16x16x32_bf16 v[214:217], v[108:111], a[196:199], v[214:217]\n"
      "v_fmac_f32 v195, v138, v138\n"
      "v_cvt_pk_bf16_f32 v157, v158, v159\n"
      "v_mfma_f32_16x16x32_bf16 v[214:217], v[142:145], a[200:203], v[214:217]\n"
      "v_fmac_f32 v80, v177, v177\n"
      "v_fmac_f32 v195, v139, v139\n"
      "v_mfma_f32_16x16x32_bf16 v[214:217], v[218:221], a[204:207], v[214:217]\n"
      "v_cvt_pk_bf16_f32 v159, v148, v149\n"
      "v_cvt_pk_bf16_f32 v205, v206, v207\n"
      "v_mfma_f32_16x16x32_bf16 v[214:217], v[152:155], a[208:211], v[214:217]\n"
      "buffer_load_dwordx4 v[218:221], v36, s[28:31], s86 offen\n"
      "v_mfma_f32_16x16x32_bf16 v[214:217], v[186:189], a[212:215], v[214:217]\n"
      "s_waitcnt lgkmcnt(1)\n"
      "v_rcp_f32 v208, v208\n"
      "v_mfma_f32_16x16x32_bf16 v[104:107], a[0:3], v[116:119], 0\n"
      "v_lshl_add_u32 v78, v223, 3, v78\n"
      "v_fmac_f32 v80, v170, v170\n"
      "v_mfma_f32_16x16x32_bf16 v[104:107], a[4:7], v[134:137], v[104:107]\n"
      "s_waitcnt lgkmcnt(0)\n"
      "v_fmac_f32 v195, v140, v140\n"
      "v_mov_b32 v222, 0x3fb9\n"
      "v_mfma_f32_16x16x32_bf16 v[104:107], a[8:11], v[112:115], v[104:107]\n"
      "v_fmac_f32 v195, v141, v141\n"
      "v_fmac_f32_e64 v80, v171, v171\n"
      "v_mfma_f32_16x16x32_bf16 v[104:107], a[12:15], v[100:103], v[104:107]\n"
      "v_fmac_f32 v80, v172, v172\n"
      "v_fmac_f32 v195, v124, v124\n"
      "v_mfma_f32_16x16x32_bf16 v[104:107], a[16:19], v[96:99], v[104:107]\n"
      "buffer_load_dword v82, v29, s[28:31], s86 offen\n"
      "v_mfma_f32_16x16x32_bf16 v[104:107], a[20:23], v[210:213], v[104:107]\n"
      "v_mul_f32 v168, v208, v168\n"
      "v_fmac_f32 v195, v125, v125\n"
      "v_mfma_f32_16x16x32_bf16 v[206:209], a[24:27], v[116:119], 0\n"
      "ds_write_b64 v6, v[204:205] offset:24576\n"
      "v_mfma_f32_16x16x32_bf16 v[206:209], a[28:31], v[134:137], v[206:209]\n"
      "v_fmac_f32 v80, v173, v173\n"
      "v_cmp_ne_u32 s[56:57], 0, v164\n"
      "v_mfma_f32_16x16x32_bf16 v[206:209], a[32:35], v[112:115], v[206:209]\n"
      "v_fmac_f32 v195, v126, v126\n"
      "v_mul_f32 v146, v192, v146\n"
      "v_mfma_f32_16x16x32_bf16 v[206:209], a[36:39], v[100:103], v[206:209]\n"
      "v_cvt_pk_bf16_f32 v158, v146, v147\n"
      "v_fmac_f32 v80, v120, v120\n"
      "v_mfma_f32_16x16x32_bf16 v[206:209], a[40:43], v[96:99], v[206:209]\n"
      "v_fmac_f32 v80, v121, v121\n"
      "v_lshlrev_b32 v223, 2, v223\n"
      "v_mfma_f32_16x16x32_bf16 v[206:209], a[44:47], v[210:213], v[206:209]\n"
      "v_fmac_f32_e64 v195, v127, v127\n"
      "v_fma_f32 v204, v104, v104, s51\n"
      "v_mfma_f32_16x16x32_bf16 v[146:149], a[48:51], v[116:119], 0\n"
      "v_fmac_f32 v80, v122, v122\n"
      "v_lshrrev_b32 v39, 3, v39\n"
      "v_mfma_f32_16x16x32_bf16 v[146:149], a[52:55], v[134:137], v[146:149]\n"
      "v_fmac_f32 v204, v105, v105\n"
      "v_fmac_f32 v204, v106, v106\n"
      "v_mfma_f32_16x16x32_bf16 v[146:149], a[56:59], v[112:115], v[146:149]\n"
      "s_add_u32 s73, s73, 1024\n"
      "v_fmac_f32 v80, v123, v123\n"
      "v_fmac_f32 v204, v107, v107\n"
      "v_mfma_f32_16x16x32_bf16 v[146:149], a[60:63], v[100:103], v[146:149]\n"
      "v_permlane16_swap_b32 v80, v195\n"
      "v_mfma_f32_16x16x32_bf16 v[146:149], a[64:67], v[96:99], v[146:149]\n"
      "v_fmac_f32 v204, v206, v206\n"
      "v_add_f32_e64 v80, v80, v195\n"
      "v_mfma_f32_16x16x32_bf16 v[146:149], a[68:71], v[210:213], v[146:149]\n"
      "ds_write_b128 v5, v[156:159] offset:30720\n"
      "v_mfma_f32_16x16x32_bf16 v[186:189], a[72:75], v[116:119], 0\n"
      "v_mov_b32_e64 v195, v80\n"
      "v_cvt_pk_bf16_f32 v168, v168, v168\n"
      "v_mfma_f32_16x16x32_bf16 v[186:189], a[76:79], v[134:137], v[186:189]\n"
      "v_permlane32_swap_b32 v80, v195\n"
      "v_mfma_f32_16x16x32_bf16 v[186:189], a[80:83], v[112:115], v[186:189]\n"
      "v_fmac_f32 v204, v207, v207\n"
      "v_add_f32_e64 v80, v80, v195\n"
      "v_mfma_f32_16x16x32_bf16 v[186:189], a[84:87], v[100:103], v[186:189]\n"
      "v_rsq_f32 v80, v80\n"
      "v_mfma_f32_16x16x32_bf16 v[186:189], a[88:91], v[96:99], v[186:189]\n"
      "v_fmac_f32 v204, v208, v208\n"
      "v_cvt_pk_bf16_f32 v196, v196, v197\n"
      "v_mfma_f32_16x16x32_bf16 v[186:189], a[92:95], v[210:213], v[186:189]\n"
      "v_sub_u32_e64 v77, v77, v223\n"
      "v_cvt_pk_bf16_f32 v160, v160, v161\n"
      "v_mfma_f32_16x16x32_bf16 v[156:159], a[96:99], v[116:119], 0\n"
      "v_mov_b32 v194, 0xbb2b\n"
      "v_cvt_pk_bf16_f32 v131, v132, v133\n"
      "v_mfma_f32_16x16x32_bf16 v[156:159], a[100:103], v[134:137], v[156:159]\n"
      "v_fmac_f32_e64 v204, v209, v209\n"
      "v_cvt_pk_bf16_f32 v161, v162, v163\n"
      "v_mfma_f32_16x16x32_bf16 v[156:159], a[104:107], v[112:115], v[156:159]\n"
      "ds_write_b16 v12, v168\n"
      "v_mfma_f32_16x16x32_bf16 v[156:159], a[108:111], v[100:103], v[156:159]\n"
      "v_cvt_pk_bf16_f32 v197, v198, v199\n"
      "v_mov_b32_e64 v195, v80\n"
      "v_mfma_f32_16x16x32_bf16 v[156:159], a[112:115], v[96:99], v[156:159]\n"
      "v_fma_f32 v205, v186, v186, s51\n"
      "v_min_u32 v39, 96, v39\n"
      "v_mfma_f32_16x16x32_bf16 v[156:159], a[116:119], v[210:213], v[156:159]\n"
      "v_permlane16_swap_b32 v80, v195\n"
      "v_mfma_f32_16x16x32_bf16 v[166:169], a[120:123], v[116:119], 0\n"
      "v_fmac_f32 v205, v187, v187\n"
      "v_fmac_f32 v205, v188, v188\n"
      "v_mfma_f32_16x16x32_bf16 v[166:169], a[124:127], v[134:137], v[166:169]\n"
      "v_fmac_f32 v204, v146, v146\n"
      "v_mul_f32 v80, s50, v80\n"
      "v_mfma_f32_16x16x32_bf16 v[166:169], a[128:131], v[112:115], v[166:169]\n"
      "v_fmac_f32 v204, v147, v147\n"
      "v_cvt_pk_bf16_f32 v214, v214, v215\n"
      "v_mfma_f32_16x16x32_bf16 v[166:169], a[132:135], v[100:103], v[166:169]\n"
      "v_fmac_f32 v205, v189, v189\n"
      "v_mul_f32 v175, v80, v175\n"
      "v_mfma_f32_16x16x32_bf16 v[166:169], a[136:139], v[96:99], v[166:169]\n"
      "s_and_b32 s77, s77, 15\n"
      "v_mul_f32 v178, v195, v178\n"
      "v_mul_f32 v171, v80, v171\n"
      "v_mfma_f32_16x16x32_bf16 v[166:169], a[140:143], v[210:213], v[166:169]\n"
      "ds_read_b128 v[162:165], v1 offset:22528\n"
      "v_fmac_f32 v204, v148, v148\n"
      "v_mfma_f32_16x16x32_bf16 v[152:155], v[116:119], a[144:147], 0\n"
      "v_mul_f32 v180, v195, v180\n"
      "v_cvt_pk_bf16_f32 v215, v216, v217\n"
      "v_mfma_f32_16x16x32_bf16 v[152:155], v[134:137], a[148:151], v[152:155]\n"
      "v_fmac_f32 v205, v156, v156\n"
      "v_mul_f32 v121, v80, v121\n"
      "v_mfma_f32_16x16x32_bf16 v[152:155], v[112:115], a[152:155], v[152:155]\n"
      "v_fmac_f32 v205, v157, v157\n"
      "v_mul_f32 v179, v195, v179\n"
      "v_mfma_f32_16x16x32_bf16 v[152:155], v[100:103], a[156:159], v[152:155]\n"
      "v_fmac_f32 v205, v158, v158\n"
      "v_fmac_f32 v205, v159, v159\n"
      "v_mfma_f32_16x16x32_bf16 v[152:155], v[96:99], a[160:163], v[152:155]\n"
      "v_fmac_f32 v204, v149, v149\n"
      "v_mul_f32 v127, v195, v127\n"
      "v_mfma_f32_16x16x32_bf16 v[152:155], v[210:213], a[164:167], v[152:155]\n"
      "v_mul_f32 v124, v195, v124\n"
      "v_fmac_f32 v205, v166, v166\n"
      "v_mfma_f32_16x16x32_bf16 v[142:145], v[116:119], a[168:171], 0\n"
      "v_fmac_f32 v205, v167, v167\n"
      "v_fmac_f32_e64 v205, v168, v168\n"
      "v_mfma_f32_16x16x32_bf16 v[142:145], v[134:137], a[172:175], v[142:145]\n"
      "ds_write_b64 v6, v[160:161] offset:30720\n"
      "v_mfma_f32_16x16x32_bf16 v[142:145], v[112:115], a[176:179], v[142:145]\n"
      "v_fmac_f32 v205, v169, v169\n"
      "v_mov_b32 v223, 0\n"
      "v_mfma_f32_16x16x32_bf16 v[142:145], v[100:103], a[180:183], v[142:145]\n"
      "v_permlane16_swap_b32 v204, v205\n"
      "v_mfma_f32_16x16x32_bf16 v[142:145], v[96:99], a[184:187], v[142:145]\n"
      "ds_read_b128 v[108:111], v0 offset:22528\n"
      "v_mul_f32 v125, v195, v125\n"
      "v_mfma_f32_16x16x32_bf16 v[142:145], v[210:213], a[188:191], v[142:145]\n"
      "v_mul_f32 v139, v195, v139\n"
      "v_cvt_pk_bf16_f32 v124, v124, v125\n"
      "v_mfma_f32_16x16x32_bf16 v[182:185], v[116:119], a[192:195], 0\n"
      "ds_read_b128 v[116:119], v1 offset:34816\n"
      "v_mul_f32 v120, v80, v120\n"
      "v_mfma_f32_16x16x32_bf16 v[182:185], v[134:137], a[196:199], v[182:185]\n"
      "ds_read_b128 v[134:137], v1 offset:28672\n"
      "v_add_f32_e64 v204, v204, v205\n"
      "v_mfma_f32_16x16x32_bf16 v[182:185], v[112:115], a[200:203], v[182:185]\n"
      "ds_read_b128 v[112:115], v0 offset:34816\n"
      "v_add_u32 v78, s77, v78\n"
      "v_mfma_f32_16x16x32_bf16 v[182:185], v[100:103], a[204:207], v[182:185]\n"
      "v_mul_f32 v174, v80, v174\n"
      "v_mul_f32_e64 v181, v195, v181\n"
      "v_mfma_f32_16x16x32_bf16 v[182:185], v[96:99], a[208:211], v[182:185]\n"
      "v_mov_b32 v205, v204\n"
      "v_cvt_pk_bf16_f32 v120, v120, v121\n"
      "v_add_u32 v39, s77, v39\n"
      "v_mfma_f32_16x16x32_bf16 v[182:185], v[210:213], a[212:215], v[182:185]\n"
      "s_waitcnt lgkmcnt(3)\n"
      "v_permlane32_swap_b32 v204, v205\n"
      "v_mfma_f32_16x16x32_bf16 v[210:213], a[0:3], v[108:111], 0\n"
      "s_waitcnt vmcnt(7)\n"
      "v_mul_f32 v122, v80, v122\n"
      "v_mul_f32_e64 v126, v195, v126\n"
      "v_mfma_f32_16x16x32_bf16 v[210:213], a[4:7], v[162:165], v[210:213]\n"
      "v_mul_f32 v123, v80, v123\n"
      "v_and_b32 v93, 2, v78\n"
      "v_mfma_f32_16x16x32_bf16 v[210:213], a[8:11], v[200:203], v[210:213]\n"
      "s_waitcnt lgkmcnt(0)\n"
      "v_add_f32 v204, v204, v205\n"
      "v_cvt_pk_bf16_f32 v178, v178, v179\n"
      "v_mfma_f32_16x16x32_bf16 v[210:213], a[12:15], v[134:137], v[210:213]\n"
      "v_mul_f32 v138, v195, v138\n"
      "v_mul_f32 v173, v80, v173\n"
      "v_mfma_f32_16x16x32_bf16 v[210:213], a[16:19], v[112:115], v[210:213]\n"
      "v_rsq_f32_e64 v204, v204\n"
      "v_mfma_f32_16x16x32_bf16 v[210:213], a[20:23], v[116:119], v[210:213]\n"
      "v_cvt_pk_bf16_f32 v199, v144, v145\n"
      "v_cvt_pk_bf16_f32 v121, v122, v123\n"
      "v_mfma_f32_16x16x32_bf16 v[190:193], a[24:27], v[108:111], 0\n"
      "v_lshlrev_b32 v77, 4, v77\n"
      "v_cvt_pk_bf16_f32 v132, v152, v153\n"
      "v_mfma_f32_16x16x32_bf16 v[190:193], a[28:31], v[162:165], v[190:193]\n"
      "v_mov_b32 v205, v204\n"
      "v_cvt_pk_bf16_f32 v198, v142, v143\n"
      "v_mfma_f32_16x16x32_bf16 v[190:193], a[32:35], v[200:203], v[190:193]\n"
      "v_permlane16_swap_b32 v204, v205\n"
      "v_mfma_f32_16x16x32_bf16 v[190:193], a[36:39], v[134:137], v[190:193]\n"
      "v_mul_f32 v204, s50, v204\n"
      "v_mul_f32 v172, v80, v172\n"
      "v_mfma_f32_16x16x32_bf16 v[190:193], a[40:43], v[112:115], v[190:193]\n"
      "v_mul_f32 v207, v204, v207\n"
      "v_cvt_pk_bf16_f32 v174, v174, v175\n"
      "v_mfma_f32_16x16x32_bf16 v[190:193], a[44:47], v[116:119], v[190:193]\n"
      "v_cvt_pk_bf16_f32 v125, v126, v127\n"
      "v_cvt_pk_bf16_f32 v217, v184, v185\n"
      "v_mfma_f32_16x16x32_bf16 v[126:129], a[48:51], v[108:111], 0\n"
      "v_cmp_gt_u32 s[72:73], 64, v77\n"
      "v_fma_f32 v123, v210, v210, s51\n"
      "v_mfma_f32_16x16x32_bf16 v[126:129], a[52:55], v[162:165], v[126:129]\n"
      "v_fmac_f32_e64 v123, v211, v211\n"
      "v_readlane_b32 s46, v37, s42\n"
      "v_mfma_f32_16x16x32_bf16 v[126:129], a[56:59], v[200:203], v[126:129]\n"
      "v_mul_f32 v148, v204, v148\n"
      "v_mul_f32 v170, v80, v170\n"
      "v_mfma_f32_16x16x32_bf16 v[126:129], a[60:63], v[134:137], v[126:129]\n"
      "v_cvt_pk_bf16_f32 v133, v154, v155\n"
      "v_cvt_pk_bf16_f32 v179, v180, v181\n"
      "v_mfma_f32_16x16x32_bf16 v[126:129], a[64:67], v[112:115], v[126:129]\n"
      "v_cvt_pk_bf16_f32 v216, v182, v183\n"
      "v_lshlrev_b64 v[222:223], v77, v[222:223]\n"
      "v_mfma_f32_16x16x32_bf16 v[126:129], a[68:71], v[116:119], v[126:129]\n"
      "v_mul_f32 v149, v204, v149\n"
      "v_fmac_f32 v123, v212, v212\n"
      "v_mul_f32 v206, v204, v206\n"
      "v_and_b32 v78, -4, v78\n"
      "v_fmac_f32_e64 v123, v213, v213\n"
      "v_pk_mul_f32 v[156:157], v[156:157], v[204:205] op_sel:[0,1]\n"
      "v_pk_mul_f32 v[186:187], v[186:187], v[204:205] op_sel:[0,1]\n"
      "v_pk_mul_f32 v[188:189], v[188:189], v[204:205] op_sel:[0,1]\n"
      "v_pk_mul_f32 v[208:209], v[208:209], v[204:205] op_sel_hi:[1,0]\n"
      "v_pk_mul_f32 v[166:167], v[166:167], v[204:205] op_sel:[0,1]\n"
      "v_pk_mul_f32 v[104:105], v[104:105], v[204:205] op_sel_hi:[1,0]\n"
      "v_pk_mul_f32 v[176:177], v[176:177], v[80:81] op_sel_hi:[1,0]\n"
      "v_pk_mul_f32 v[140:141], v[140:141], v[194:195] op_sel:[0,1]\n"
      "v_pk_mul_f32 v[106:107], v[106:107], v[204:205] op_sel_hi:[1,0]\n"
      "v_mfma_f32_16x16x32_bf16 v[182:185], a[72:75], v[108:111], 0\n"
      "v_mul_f32 v168, v205, v168\n"
      "v_mul_f32 v146, v204, v146\n"
      "v_mfma_f32_16x16x32_bf16 v[182:185], a[76:79], v[162:165], v[182:185]\n"
      "v_mul_f32 v158, v205, v158\n"
      "v_cvt_pk_bf16_f32 v181, v140, v141\n"
      "v_mfma_f32_16x16x32_bf16 v[182:185], a[80:83], v[200:203], v[182:185]\n"
      "v_mul_f32 v169, v205, v169\n"
      "v_fmac_f32_e64 v123, v190, v190\n"
      "v_mfma_f32_16x16x32_bf16 v[182:185], a[84:87], v[134:137], v[182:185]\n"
      "v_fmac_f32_e64 v123, v191, v191\n"
      "v_cndmask_b32_e64 v223, 0, v223, s[72:73]\n"
      "v_mfma_f32_16x16x32_bf16 v[182:185], a[88:91], v[112:115], v[182:185]\n"
      "v_cvt_pk_bf16_f32 v180, v138, v139\n"
      "v_fmac_f32 v123, v192, v192\n"
      "v_mfma_f32_16x16x32_bf16 v[182:185], a[92:95], v[116:119], v[182:185]\n"
      "v_cvt_pk_bf16_f32 v175, v176, v177\n"
      "v_fmac_f32 v123, v193, v193\n"
      "v_mfma_f32_16x16x32_bf16 v[152:155], a[96:99], v[108:111], 0\n"
      "v_cvt_pk_bf16_f32 v104, v104, v105\n"
      "v_mul_f32_e64 v147, v204, v147\n"
      "v_mfma_f32_16x16x32_bf16 v[152:155], a[100:103], v[162:165], v[152:155]\n"
      "v_cvt_pk_bf16_f32 v186, v186, v187\n"
      "v_fmac_f32 v123, v126, v126\n"
      "v_mfma_f32_16x16x32_bf16 v[152:155], a[104:107], v[200:203], v[152:155]\n"
      "v_mov_b32 v195, 0\n"
      "v_fma_f32 v122, v182, v182, s51\n"
      "v_mfma_f32_16x16x32_bf16 v[152:155], a[108:111], v[134:137], v[152:155]\n"
      "v_fmac_f32 v122, v183, v183\n"
      "v_cvt_pk_bf16_f32 v187, v188, v189\n"
      "v_mfma_f32_16x16x32_bf16 v[152:155], a[112:115], v[112:115], v[152:155]\n"
      "v_fmac_f32 v122, v184, v184\n"
      "v_cvt_pk_bf16_f32 v105, v106, v107\n"
      "v_mfma_f32_16x16x32_bf16 v[152:155], a[116:119], v[116:119], v[152:155]\n"
      "v_mul_f32_e64 v159, v205, v159\n"
      "v_cvt_pk_bf16_f32 v106, v206, v207\n"
      "v_mfma_f32_16x16x32_bf16 v[142:145], a[120:123], v[108:111], 0\n"
      "v_cvt_pk_bf16_f32 v107, v208, v209\n"
      "v_fmac_f32 v123, v127, v127\n"
      "v_mfma_f32_16x16x32_bf16 v[142:145], a[124:127], v[162:165], v[142:145]\n"
      "v_fmac_f32 v123, v128, v128\n"
      "v_fmac_f32 v122, v185, v185\n"
      "v_mfma_f32_16x16x32_bf16 v[142:145], a[128:131], v[200:203], v[142:145]\n"
      "v_fmac_f32 v123, v129, v129\n"
      "v_cvt_pk_bf16_f32 v166, v166, v167\n"
      "v_mfma_f32_16x16x32_bf16 v[142:145], a[132:135], v[134:137], v[142:145]\n"
      "s_or_b32 s95, s45, s46\n"
      "v_cvt_pk_bf16_f32 v177, v172, v173\n"
      "v_cndmask_b32_e64 v222, 0, v222, s[72:73]\n"
      "v_mfma_f32_16x16x32_bf16 v[142:145], a[136:139], v[112:115], v[142:145]\n"
      "v_fmac_f32 v122, v152, v152\n"
      "v_fmac_f32 v122, v153, v153\n"
      "v_mfma_f32_16x16x32_bf16 v[142:145], a[140:143], v[116:119], v[142:145]\n"
      "s_or_b32 s95, s95, s48\n"
      "v_cvt_pk_bf16_f32 v189, v158, v159\n"
      "v_fmac_f32_e64 v122, v154, v154\n"
      "v_mfma_f32_16x16x32_bf16 v[204:207], v[108:111], a[144:147], 0\n"
      "v_cvt_pk_bf16_f32 v146, v146, v147\n"
      "v_fmac_f32 v122, v155, v155\n"
      "v_mfma_f32_16x16x32_bf16 v[204:207], v[162:165], a[148:151], v[204:207]\n"
      "s_nop 0\n"
      "v_fmac_f32 v122, v142, v142\n"
      "v_lshlrev_b64 v[194:195], v77, v[194:195]\n"
      "v_mfma_f32_16x16x32_bf16 v[204:207], v[200:203], a[152:155], v[204:207]\n"
      "v_fmac_f32 v122, v143, v143\n"
      "v_cmp_gt_u32 s[72:73], 64, v77\n"
      "v_mfma_f32_16x16x32_bf16 v[204:207], v[134:137], a[156:159], v[204:207]\n"
      "v_cvt_pk_bf16_f32 v167, v168, v169\n"
      "v_fmac_f32 v122, v144, v144\n"
      "v_mfma_f32_16x16x32_bf16 v[204:207], v[112:115], a[160:163], v[204:207]\n"
      "v_fmac_f32 v122, v145, v145\n"
      "v_cvt_pk_bf16_f32 v176, v170, v171\n"
      "v_mfma_f32_16x16x32_bf16 v[204:207], v[116:119], a[164:167], v[204:207]\n"
      "v_permlane16_swap_b32 v123, v122\n"
      "v_mfma_f32_16x16x32_bf16 v[158:161], v[108:111], a[168:171], 0\n"
      "v_add_f32 v123, v123, v122\n"
      "v_mov_b32 v122, v123\n"
      "v_mfma_f32_16x16x32_bf16 v[158:161], v[162:165], a[172:175], v[158:161]\n"
      "s_nop 0\n"
      "v_permlane32_swap_b32 v123, v122\n"
      "v_mfma_f32_16x16x32_bf16 v[158:161], v[200:203], a[176:179], v[158:161]\n"
      "v_add_f32 v123, v123, v122\n"
      "v_cvt_pk_bf16_f32 v188, v156, v157\n"
      "v_mfma_f32_16x16x32_bf16 v[158:161], v[134:137], a[180:183], v[158:161]\n"
      "v_rsq_f32_e64 v123, v123\n"
      "v_mfma_f32_16x16x32_bf16 v[158:161], v[112:115], a[184:187], v[158:161]\n"
      "ds_read_b128 v[168:171], v2\n"
      "v_mov_b32 v122, v123\n"
      "v_mfma_f32_16x16x32_bf16 v[158:161], v[116:119], a[188:191], v[158:161]\n"
      "ds_read_b128 v[100:103], v2 offset:3072\n"
      "v_permlane16_swap_b32 v123, v122\n"
      "v_mfma_f32_16x16x32_bf16 v[138:141], v[108:111], a[192:195], 0\n"
      "ds_read_b128 v[108:111], v7\n"
      "v_cvt_pk_bf16_f32 v77, v206, v207\n"
      "v_mfma_f32_16x16x32_bf16 v[138:141], v[162:165], a[196:199], v[138:141]\n"
      "ds_read_b128 v[206:209], v2 offset:6144\n"
      "v_mul_f32 v183, v122, v183\n"
      "v_mfma_f32_16x16x32_bf16 v[138:141], v[200:203], a[200:203], v[138:141]\n"
      "ds_read_b128 v[200:203], v7 offset:4096\n"
      "v_mul_f32 v154, v122, v154\n"
      "v_mul_f32 v123, s50, v123\n"
      "v_mul_f32 v182, v122, v182\n"
      "v_cvt_pk_bf16_f32 v147, v148, v149\n"
      "v_pk_mul_f32 v[212:213], v[212:213], v[122:123] op_sel:[0,1]\n"
      "v_pk_mul_f32 v[190:191], v[190:191], v[122:123] op_sel:[0,1]\n"
      "v_pk_mul_f32 v[144:145], v[144:145], v[122:123] op_sel_hi:[1,0]\n"
      "v_pk_mul_f32 v[210:211], v[210:211], v[122:123] op_sel:[0,1]\n"
      "v_pk_mul_f32 v[142:143], v[142:143], v[122:123] op_sel_hi:[1,0]\n"
      "v_mfma_f32_16x16x32_bf16 v[138:141], v[134:137], a[204:207], v[138:141]\n"
      "v_cvt_pk_bf16_f32 v80, v158, v159\n"
      "v_add_u32 v84, 110592, v78\n"
      "v_mfma_f32_16x16x32_bf16 v[138:141], v[112:115], a[208:211], v[138:141]\n"
      "v_cvt_pk_bf16_f32 v182, v182, v183\n"
      "v_mul_f32 v192, v123, v192\n"
      "v_mfma_f32_16x16x32_bf16 v[138:141], v[116:119], a[212:215], v[138:141]\n"
      "s_waitcnt lgkmcnt(1)\n"
      "v_cvt_pk_bf16_f32 v142, v142, v143\n"
      "v_cvt_pk_bf16_f32 v143, v144, v145\n"
      "v_mfma_f32_16x16x32_bf16 v[96:99], a[216:219], v[168:171], 0\n"
      "buffer_store_dwordx4 v[108:111], v20, s[24:27], s85 offen sc1\n"
      "v_pk_mul_f32 v[184:185], v[184:185], v[122:123] op_sel_hi:[1,0]\n"
      "v_pk_mul_f32 v[128:129], v[128:129], v[122:123] op_sel:[0,1]\n"
      "v_pk_mul_f32 v[152:153], v[152:153], v[122:123] op_sel_hi:[1,0]\n"
      "v_pk_mul_f32 v[126:127], v[126:127], v[122:123] op_sel:[0,1]\n"
      "v_mfma_f32_16x16x32_bf16 v[96:99], a[220:223], v[100:103], v[96:99]\n"
      "ds_read_b128 v[112:115], v2 offset:9216\n"
      "v_mul_f32_e64 v155, v122, v155\n"
      "v_mfma_f32_16x16x32_bf16 v[96:99], a[224:227], v[206:209], v[96:99]\n"
      "ds_read_b128 v[156:159], v2 offset:15360\n"
      "v_cvt_pk_bf16_f32 v85, v140, v141\n"
      "v_mfma_f32_16x16x32_bf16 v[116:119], a[240:243], v[168:171], 0\n"
      "ds_read_b128 v[162:165], v2 offset:12288\n"
      "v_cvt_pk_bf16_f32 v183, v184, v185\n"
      "v_mfma_f32_16x16x32_bf16 v[116:119], a[244:247], v[100:103], v[116:119]\n"
      "ds_read_b128 v[148:151], v7 offset:8192\n"
      "v_cvt_pk_bf16_f32 v184, v152, v153\n"
      "v_mfma_f32_16x16x32_bf16 v[116:119], a[248:251], v[206:209], v[116:119]\n"
      "s_cmp_ge_u32 s95, 0x40000000\n"
      "s_cselect_b32 s94, -1, -2\n"
      "v_cvt_pk_bf16_f32 v185, v154, v155\n"
      "v_mfma_f32_16x16x32_bf16 v[134:137], v[232:235], v[168:171], 0\n"
      "ds_read_b64 v[172:173], v10 offset:1192\n"
      "v_cvt_pk_bf16_f32 v76, v204, v205\n"
      "v_mfma_f32_16x16x32_bf16 v[134:137], v[236:239], v[100:103], v[134:137]\n"
      "s_waitcnt lgkmcnt(4)\n"
      "v_cvt_pk_bf16_f32 v210, v210, v211\n"
      "v_cvt_pk_bf16_f32 v211, v212, v213\n"
      "v_mfma_f32_16x16x32_bf16 v[134:137], v[240:243], v[206:209], v[134:137]\n"
      "buffer_store_dwordx4 v[200:203], v21, s[24:27], s85 offen sc1\n"
      "v_mfma_f32_16x16x32_bf16 v[96:99], a[228:231], v[112:115], v[96:99]\n"
      "s_nop 0\n"
      "ds_read_b128 v[200:203], v7 offset:12288\n"
      "s_waitcnt lgkmcnt(2)\n"
      "v_mfma_f32_16x16x32_bf16 v[96:99], a[232:235], v[162:165], v[96:99]\n"
      "ds_read_b128 v[168:171], v10 offset:1200\n"
      "v_cvt_pk_bf16_f32 v212, v190, v191\n"
      "v_mfma_f32_16x16x32_bf16 v[96:99], a[236:239], v[156:159], v[96:99]\n"
      "ds_read2_b32 v[206:207], v84 offset0:8 offset1:9\n"
      "v_mul_f32 v193, v123, v193\n"
      "v_mfma_f32_16x16x32_bf16 v[116:119], a[252:255], v[112:115], v[116:119]\n"
      "ds_read2_b32 v[140:141], v84 offset0:2 offset1:10\n"
      "v_cvt_pk_bf16_f32 v213, v192, v193\n"
      "v_mfma_f32_16x16x32_bf16 v[116:119], v[224:227], v[162:165], v[116:119]\n"
      "ds_read2_b32 v[204:205], v84 offset0:0 offset1:1\n"
      "v_cndmask_b32_e64 v195, 0, v195, s[72:73]\n"
      "v_mfma_f32_16x16x32_bf16 v[116:119], v[228:231], v[156:159], v[116:119]\n"
      "v_cvt_pk_bf16_f32 v126, v126, v127\n"
      "v_add_u32 v39, 115296, v39\n"
      "v_mfma_f32_16x16x32_bf16 v[134:137], v[244:247], v[112:115], v[134:137]\n"
      "ds_read_b128 v[112:115], v7 offset:16384\n"
      "v_cvt_pk_bf16_f32 v127, v128, v129\n"
      "v_mfma_f32_16x16x32_bf16 v[134:137], v[248:251], v[162:165], v[134:137]\n"
      "buffer_store_dwordx4 v[148:151], v22, s[24:27], s85 offen sc1\n"
      "v_mfma_f32_16x16x32_bf16 v[134:137], v[252:255], v[156:159], v[134:137]\n"
      "ds_read_b128 v[156:159], v2 offset:3328\n"
      "v_fma_f32 v144, v96, v96, s52\n"
      "v_mfma_f32_16x16x32_bf16 v[162:165], v[178:181], v[174:177], v[40:43]\n"
      "s_waitcnt lgkmcnt(2)\n"
      "v_alignbyte_b32 v204, v205, v204, v93\n"
      "v_cvt_pk_bf16_f32 v81, v160, v161\n"
      "v_mfma_f32_16x16x32_bf16 v[148:151], v[186:189], v[174:177], v[44:47]\n"
      "ds_read2_b32 v[152:153], v84 offset0:16 offset1:17\n"
      "v_alignbyte_b32 v205, v140, v205, v93\n"
      "v_mfma_f32_16x16x32_bf16 v[190:193], v[182:185], v[174:177], v[48:51]\n"
      "ds_read2_b32 v[122:123], v84 offset0:24 offset1:18\n"
      "v_alignbyte_b32 v206, v207, v206, v93\n"
      "v_mfma_f32_16x16x32_bf16 v[100:103], v[168:171], v[174:177], v[24:27]\n"
      "s_cmp_gt_u32 s42, s44\n"
      "s_cselect_b32 s95, s94, s95\n"
      "v_mul_f32 v145, v97, v97\n"
      "v_mfma_f32_16x16x16_bf16 v[162:165], v[124:125], v[120:121], v[162:165]\n"
      "v_fmac_f32 v145, v99, v99\n"
      "v_fmac_f32 v144, v98, v98\n"
      "v_fmac_f32 v145, v117, v117\n"
      "v_mfma_f32_16x16x16_bf16 v[148:151], v[166:167], v[120:121], v[148:151]\n"
      "v_fmac_f32 v144, v116, v116\n"
      "v_alignbyte_b32 v207, v141, v207, v93\n"
      "v_fmac_f32 v145, v119, v119\n"
      "v_mfma_f32_16x16x16_bf16 v[190:193], v[142:143], v[120:121], v[190:193]\n"
      "buffer_store_dwordx4 v[200:203], v23, s[24:27], s85 offen sc1\n"
      "v_mfma_f32_16x16x16_bf16 v[100:103], v[172:173], v[120:121], v[100:103]\n"
      "v_cvt_pk_bf16_f32 v84, v138, v139\n"
      "s_waitcnt lgkmcnt(0)\n"
      "v_alignbyte_b32 v152, v153, v152, v93\n"
      "v_mfma_f32_16x16x16_bf16 v[162:165], v[222:223], v[204:205], v[162:165]\n"
      "v_cndmask_b32_e64 v194, 0, v194, s[72:73]\n"
      "v_fmac_f32 v144, v118, v118\n"
      "v_alignbyte_b32 v153, v123, v153, v93\n"
      "v_mfma_f32_16x16x16_bf16 v[148:151], v[222:223], v[206:207], v[148:151]\n"
      "v_alignbyte_b32 v122, v122, v122, v93\n"
      "v_fmac_f32 v144, v134, v134\n"
      "v_fmac_f32_e64 v145, v135, v135\n"
      "v_mfma_f32_16x16x16_bf16 v[190:193], v[222:223], v[152:153], v[190:193]\n"
      "ds_read_b128 v[174:177], v2 offset:256\n"
      "v_add_u32 v108, 113728, v78\n"
      "v_mfma_f32_16x16x16_bf16 v[100:103], v[222:223], v[122:123], v[100:103]\n"
      "ds_read_b128 v[138:141], v2 offset:6400\n"
      "v_add_u32 v120, 112160, v78\n"
      "v_mfma_f32_16x16x16_bf16 v[162:165], v[194:195], v[204:205], v[162:165]\n"
      "ds_read2_b32 v[154:155], v120 offset0:0 offset1:1\n"
      "v_fmac_f32 v145, v137, v137\n"
      "v_mfma_f32_16x16x16_bf16 v[148:151], v[194:195], v[206:207], v[148:151]\n"
      "ds_read_u16 v39, v39\n"
      "v_fmac_f32 v144, v136, v136\n"
      "v_mfma_f32_16x16x16_bf16 v[190:193], v[194:195], v[152:153], v[190:193]\n"
      "buffer_store_dwordx4 v[112:115], v25, s[24:27], s85 offen sc1\n"
      "v_mfma_f32_16x16x16_bf16 v[100:103], v[194:195], v[122:123], v[100:103]\n"
      "v_add_f32_e64 v144, v144, v145\n"
      "v_max3_f32 v121, v162, v163, v164\n"
      "s_waitcnt lgkmcnt(3)\n"
      "v_mfma_f32_16x16x32_bf16 v[200:203], a[216:219], v[174:177], 0\n"
      "v_max3_f32 v115, v165, v148, v149\n"
      "v_max3_f32 v121, v121, v150, v151\n"
      "v_mfma_f32_16x16x32_bf16 v[200:203], a[220:223], v[156:159], v[200:203]\n"
      "s_waitcnt lgkmcnt(1)\n"
      "v_alignbyte_b32 v154, v155, v154, v93\n"
      "v_max3_f32 v121, v121, v192, v193\n"
      "v_mfma_f32_16x16x32_bf16 v[200:203], a[224:227], v[138:141], v[200:203]\n"
      "v_max3_f32 v115, v115, v190, v191\n"
      "v_max3_f32 v121, v121, v115, v100\n"
      "v_mfma_f32_16x16x32_bf16 v[112:115], a[240:243], v[174:177], 0\n"
      "ds_read2_b32 v[102:103], v120 offset0:16 offset1:17\n"
      "v_mov_b32 v78, v121\n"
      "v_mfma_f32_16x16x32_bf16 v[112:115], a[244:247], v[156:159], v[112:115]\n"
      "ds_read2_b32 v[152:153], v120 offset0:8 offset1:9\n"
      "v_permlane16_swap_b32 v121, v78\n"
      "v_mfma_f32_16x16x32_bf16 v[112:115], a[248:251], v[138:141], v[112:115]\n"
      "v_max_f32 v121, v121, v78\n"
      "s_waitcnt vmcnt(6)\n"
      "v_or3_b32 v218, v218, v219, v220\n"
      "v_mfma_f32_16x16x32_bf16 v[204:207], v[232:235], v[174:177], 0\n"
      "ds_read2_b32 v[122:123], v120 offset0:2 offset1:10\n"
      "v_mov_b32 v78, v121\n"
      "v_mfma_f32_16x16x32_bf16 v[204:207], v[236:239], v[156:159], v[204:207]\n"
      "ds_read2_b32 v[110:111], v120 offset0:24 offset1:18\n"
      "v_permlane32_swap_b32 v121, v78\n"
      "v_max_f32 v121, v121, v78\n"
      "v_sub_f32 v100, v100, v121\n"
      "v_pk_add_f32 v[150:151], v[150:151], v[120:121] op_sel:[0,1] neg_lo:[0,1] neg_hi:[0,1]\n"
      "v_pk_add_f32 v[148:149], v[148:149], v[120:121] op_sel:[0,1] neg_lo:[0,1] neg_hi:[0,1]\n"
      "v_pk_add_f32 v[164:165], v[164:165], v[120:121] op_sel:[0,1] neg_lo:[0,1] neg_hi:[0,1]\n"
      "v_pk_add_f32 v[190:191], v[190:191], v[120:121] op_sel:[0,1] neg_lo:[0,1] neg_hi:[0,1]\n"
      "v_pk_add_f32 v[162:163], v[162:163], v[120:121] op_sel:[0,1] neg_lo:[0,1] neg_hi:[0,1]\n"
      "v_pk_add_f32 v[192:193], v[192:193], v[120:121] op_sel:[0,1] neg_lo:[0,1] neg_hi:[0,1]\n"
      "v_mfma_f32_16x16x32_bf16 v[204:207], v[240:243], v[138:141], v[204:207]\n"
      "s_waitcnt lgkmcnt(2)\n"
      "v_alignbyte_b32 v152, v153, v152, v93\n"
      "s_waitcnt vmcnt(5)\n"
      "v_or3_b32 v218, v218, v221, v82\n"
      "v_mfma_f32_16x16x32_bf16 v[174:177], v[178:181], v[104:107], v[52:55]\n"
      "ds_write_b32 v31, v218 offset:5632\n"
      "v_mfma_f32_16x16x32_bf16 v[156:159], v[186:189], v[104:107], v[56:59]\n"
      "s_waitcnt lgkmcnt(2)\n"
      "v_exp_f32 v151, v151\n"
      "v_mfma_f32_16x16x32_bf16 v[218:221], v[182:185], v[104:107], v[60:63]\n"
      "s_waitcnt lgkmcnt(1)\n"
      "v_alignbyte_b32 v153, v123, v153, v93\n"
      "v_alignbyte_b32 v155, v122, v155, v93\n"
      "v_mfma_f32_16x16x32_bf16 v[120:123], v[168:171], v[104:107], v[26:29]\n"
      "ds_read_b128 v[104:107], v2 offset:9472\n"
      "v_exp_f32 v162, v162\n"
      "v_mfma_f32_16x16x16_bf16 v[174:177], v[124:125], v[146:147], v[174:177]\n"
      "v_exp_f32 v148, v148\n"
      "v_exp_f32 v163, v163\n"
      "v_mfma_f32_16x16x16_bf16 v[156:159], v[166:167], v[146:147], v[156:159]\n"
      "v_alignbyte_b32 v110, v110, v110, v93\n"
      "v_exp_f32 v164, v164\n"
      "v_mfma_f32_16x16x16_bf16 v[218:221], v[142:143], v[146:147], v[218:221]\n"
      "v_exp_f32 v165, v165\n"
      "v_cvt_pk_bf16_f32 v162, v162, v163\n"
      "v_mfma_f32_16x16x16_bf16 v[120:123], v[172:173], v[146:147], v[120:123]\n"
      "v_alignbyte_b32 v102, v103, v102, v93\n"
      "v_exp_f32_e64 v193, v193\n"
      "v_mfma_f32_16x16x16_bf16 v[174:177], v[222:223], v[154:155], v[174:177]\n"
      "v_alignbyte_b32 v103, v111, v103, v93\n"
      "v_exp_f32 v149, v149\n"
      "v_mfma_f32_16x16x16_bf16 v[156:159], v[222:223], v[152:153], v[156:159]\n"
      "v_cvt_pk_bf16_f32 v163, v164, v165\n"
      "v_exp_f32 v192, v192\n"
      "v_mfma_f32_16x16x16_bf16 v[218:221], v[222:223], v[102:103], v[218:221]\n"
      "v_cvt_pk_bf16_f32 v164, v148, v149\n"
      "v_exp_f32 v150, v150\n"
      "v_mfma_f32_16x16x16_bf16 v[120:123], v[222:223], v[110:111], v[120:123]\n"
      "v_exp_f32 v190, v190\n"
      "v_cvt_pk_bf16_f32 v89, v192, v193\n"
      "v_mfma_f32_16x16x16_bf16 v[174:177], v[194:195], v[154:155], v[174:177]\n"
      "ds_read_b128 v[146:149], v2 offset:12544\n"
      "v_cvt_pk_bf16_f32 v165, v150, v151\n"
      "v_mfma_f32_16x16x16_bf16 v[156:159], v[194:195], v[152:153], v[156:159]\n"
      "ds_read_b128 v[150:153], v2 offset:15616\n"
      "v_exp_f32_e64 v191, v191\n"
      "v_mfma_f32_16x16x16_bf16 v[218:221], v[194:195], v[102:103], v[218:221]\n"
      "v_readlane_b32 s47, v38, s43\n"
      "v_exp_f32 v100, v100\n"
      "v_cvt_pk_bf16_f32 v88, v190, v191\n"
      "v_mfma_f32_16x16x16_bf16 v[120:123], v[194:195], v[110:111], v[120:123]\n"
      "v_cvt_pk_bf16_f32 v90, v100, 0\n"
      "v_max3_f32 v191, v174, v175, v176\n"
      "v_max3_f32 v191, v191, v158, v159\n"
      "v_max3_f32 v82, v177, v156, v157\n"
      "s_waitcnt lgkmcnt(2)\n"
      "v_mfma_f32_16x16x32_bf16 v[200:203], a[228:231], v[104:107], v[200:203]\n"
      "ds_read_u16 v78, v11 offset:1168\n"
      "v_max3_f32 v82, v82, v218, v219\n"
      "v_max3_f32 v191, v191, v220, v221\n"
      "v_max3_f32 v191, v191, v82, v120\n"
      "v_mov_b32 v190, v191\n"
      "s_waitcnt lgkmcnt(2)\n"
      "v_mfma_f32_16x16x32_bf16 v[200:203], a[232:235], v[146:149], v[200:203]\n"
      "s_waitcnt lgkmcnt(1)\n"
      "v_permlane16_swap_b32 v191, v190\n"
      "v_mfma_f32_16x16x32_bf16 v[200:203], a[236:239], v[150:153], v[200:203]\n"
      "v_max_f32 v191, v191, v190\n"
      "v_mov_b32 v190, v191\n"
      "v_mfma_f32_16x16x32_bf16 v[112:115], a[252:255], v[104:107], v[112:115]\n"
      "ds_read_u16 v82, v11 offset:1200\n"
      "v_permlane32_swap_b32 v191, v190\n"
      "v_max_f32 v191, v191, v190\n"
      "v_sub_f32_e64 v120, v120, v191\n"
      "v_pk_add_f32 v[220:221], v[220:221], v[190:191] op_sel:[0,1] neg_lo:[0,1] neg_hi:[0,1]\n"
      "v_pk_add_f32 v[156:157], v[156:157], v[190:191] op_sel:[0,1] neg_lo:[0,1] neg_hi:[0,1]\n"
      "v_pk_add_f32 v[218:219], v[218:219], v[190:191] op_sel:[0,1] neg_lo:[0,1] neg_hi:[0,1]\n"
      "v_pk_add_f32 v[174:175], v[174:175], v[190:191] op_sel:[0,1] neg_lo:[0,1] neg_hi:[0,1]\n"
      "v_pk_add_f32 v[176:177], v[176:177], v[190:191] op_sel:[0,1] neg_lo:[0,1] neg_hi:[0,1]\n"
      "v_pk_add_f32 v[158:159], v[158:159], v[190:191] op_sel:[0,1] neg_lo:[0,1] neg_hi:[0,1]\n"
      "v_mfma_f32_16x16x32_bf16 v[112:115], v[224:227], v[146:149], v[112:115]\n"
      "v_exp_f32 v175, v175\n"
      "v_mfma_f32_16x16x32_bf16 v[112:115], v[228:231], v[150:153], v[112:115]\n"
      "v_exp_f32 v156, v156\n"
      "v_mfma_f32_16x16x32_bf16 v[204:207], v[244:247], v[104:107], v[204:207]\n"
      "ds_read2_b32 v[128:129], v108 offset0:0 offset1:1\n"
      "v_exp_f32 v158, v158\n"
      "v_mfma_f32_16x16x32_bf16 v[204:207], v[248:251], v[146:149], v[204:207]\n"
      "ds_read2_b32 v[190:191], v108 offset0:2 offset1:10\n"
      "v_exp_f32 v218, v218\n"
      "v_mfma_f32_16x16x32_bf16 v[204:207], v[252:255], v[150:153], v[204:207]\n"
      "v_exp_f32_e64 v174, v174\n"
      "v_mfma_f32_16x16x32_bf16 v[146:149], v[32:35], v[162:165], 0\n"
      "ds_read_u16 v86, v11 offset:1232\n"
      "v_exp_f32 v219, v219\n"
      "v_mfma_f32_16x16x32_bf16 v[146:149], v[32:35], v[88:91], v[146:149]\n"
      "s_waitcnt lgkmcnt(3)\n"
      "v_exp_f32_e64 v176, v176\n"
      "v_mfma_f32_16x16x32_bf16 v[104:107], v[130:133], v[162:165], 0\n"
      "ds_read2_b32 v[208:209], v108 offset0:8 offset1:9\n"
      "v_cvt_pk_bf16_f32 v174, v174, v175\n"
      "v_mfma_f32_16x16x32_bf16 v[104:107], v[76:79], v[88:91], v[104:107]\n"
      "ds_read2_b32 v[160:161], v108 offset0:24 offset1:18\n"
      "v_exp_f32 v157, v157\n"
      "v_mfma_f32_16x16x32_bf16 v[148:151], v[196:199], v[162:165], 0\n"
      "s_waitcnt lgkmcnt(3)\n"
      "v_alignbyte_b32 v128, v129, v128, v93\n"
      "v_cvt_pk_bf16_f32 v92, v218, v219\n"
      "v_mfma_f32_16x16x32_bf16 v[148:151], v[80:83], v[88:91], v[148:151]\n"
      "ds_read2_b32 v[122:123], v108 offset0:16 offset1:17\n"
      "v_alignbyte_b32 v129, v190, v129, v93\n"
      "v_mfma_f32_16x16x32_bf16 v[152:155], v[214:217], v[162:165], 0\n"
      "s_waitcnt lgkmcnt(3)\n"
      "v_exp_f32 v159, v159\n"
      "v_mfma_f32_16x16x32_bf16 v[152:155], v[84:87], v[88:91], v[152:155]\n"
      "v_exp_f32 v177, v177\n"
      "v_mfma_f32_16x16x32_bf16 v[100:103], v[178:181], v[210:213], v[64:67]\n"
      "s_waitcnt lgkmcnt(2)\n"
      "v_cvt_pk_bf16_f32 v175, v176, v177\n"
      "v_cvt_pk_bf16_f32 v177, v158, v159\n"
      "v_mfma_f32_16x16x32_bf16 v[108:111], v[186:189], v[210:213], v[68:71]\n"
      "v_alignbyte_b32 v208, v209, v208, v93\n"
      "v_cvt_pk_bf16_f32 v176, v156, v157\n"
      "v_alignbyte_b32 v209, v191, v209, v93\n"
      "v_mfma_f32_16x16x32_bf16 v[190:193], v[182:185], v[210:213], v[72:75]\n"
      "v_rcp_f32_e64 v146, v146\n"
      "v_mfma_f32_16x16x32_bf16 v[138:141], v[168:171], v[210:213], v[28:31]\n"
      "s_waitcnt lgkmcnt(0)\n"
      "v_mul_f32 v149, v146, v149\n"
      "v_mul_f32 v151, v146, v151\n"
      "v_mfma_f32_16x16x16_bf16 v[100:103], v[124:125], v[126:127], v[100:103]\n"
      "v_mul_f32 v105, v146, v105\n"
      "v_alignbyte_b32 v160, v160, v160, v93\n"
      "v_alignbyte_b32 v122, v123, v122, v93\n"
      "v_mfma_f32_16x16x16_bf16 v[108:111], v[166:167], v[126:127], v[108:111]\n"
      "v_alignbyte_b32 v123, v161, v123, v93\n"
      "v_mul_f32 v153, v146, v153\n"
      "v_mul_f32 v150, v146, v150\n"
      "v_mfma_f32_16x16x16_bf16 v[190:193], v[142:143], v[126:127], v[190:193]\n"
      "v_exp_f32 v220, v220\n"
      "v_mul_f32 v107, v146, v107\n"
      "v_mfma_f32_16x16x16_bf16 v[138:141], v[172:173], v[126:127], v[138:141]\n"
      "v_mul_f32 v104, v146, v104\n"
      "v_mul_f32 v165, v201, v201\n"
      "v_mul_f32 v106, v146, v106\n"
      "v_mfma_f32_16x16x16_bf16 v[100:103], v[222:223], v[128:129], v[100:103]\n"
      "v_exp_f32 v120, v120\n"
      "v_lshlrev_b32_e64 v39, 16, v39\n"
      "v_mfma_f32_16x16x16_bf16 v[108:111], v[222:223], v[208:209], v[108:111]\n"
      "v_mul_f32 v152, v146, v152\n"
      "v_exp_f32 v221, v221\n"
      "v_mfma_f32_16x16x16_bf16 v[190:193], v[222:223], v[122:123], v[190:193]\n"
      "v_cvt_pk_bf16_f32 v93, v220, v221\n"
      "v_fma_f32 v164, v200, v200, s52\n"
      "v_cvt_pk_bf16_f32 v104, v104, v105\n"
      "v_mfma_f32_16x16x16_bf16 v[138:141], v[222:223], v[160:161], v[138:141]\n"
      "ds_read_b128 v[218:221], v2 offset:3584\n"
      "v_mul_f32_e64 v148, v146, v148\n"
      "v_mfma_f32_16x16x16_bf16 v[100:103], v[194:195], v[128:129], v[100:103]\n"
      "v_cvt_pk_bf16_f32 v105, v106, v107\n"
      "v_cvt_pk_bf16_f32 v106, v148, v149\n"
      "v_cvt_pk_bf16_f32 v152, v152, v153\n"
      "v_mfma_f32_16x16x16_bf16 v[108:111], v[194:195], v[208:209], v[108:111]\n"
      "ds_read_b128 v[208:211], v2 offset:6656\n"
      "v_cvt_pk_bf16_f32 v94, v120, 0\n"
      "v_mfma_f32_16x16x16_bf16 v[190:193], v[194:195], v[122:123], v[190:193]\n"
      "ds_read_b128 v[126:129], v2 offset:512\n"
      "v_fmac_f32_e64 v164, v202, v202\n"
      "v_mfma_f32_16x16x16_bf16 v[138:141], v[194:195], v[160:161], v[138:141]\n"
      "v_cvt_pk_bf16_f32 v107, v150, v151\n"
      "v_max3_f32 v222, v100, v101, v102\n"
      "v_max3_f32 v222, v222, v110, v111\n"
      "v_fmac_f32_e64 v165, v203, v203\n"
      "v_max3_f32 v156, v103, v108, v109\n"
      "v_max3_f32 v156, v156, v190, v191\n"
      "v_pk_mul_f32 v[154:155], v[154:155], v[146:147] op_sel_hi:[1,0]\n"
      "v_max3_f32 v222, v222, v192, v193\n"
      "v_max3_f32 v222, v222, v156, v138\n"
      "v_mov_b32 v156, v222\n"
      "v_pk_fma_f32 v[164:165], v[112:113], v[112:113], v[164:165]\n"
      "v_pk_fma_f32 v[164:165], v[114:115], v[114:115], v[164:165]\n"
      "v_permlane16_swap_b32 v222, v156\n"
      "v_max_f32 v222, v222, v156\n"
      "v_mov_b32 v156, v222\n"
      "v_cvt_pk_bf16_f32 v153, v154, v155\n"
      "v_pk_fma_f32 v[164:165], v[204:205], v[204:205], v[164:165]\n"
      "v_permlane32_swap_b32_e64 v222, v156\n"
      "v_max_f32 v222, v222, v156\n"
      "s_waitcnt lgkmcnt(0)\n"
      "v_mfma_f32_16x16x32_bf16 v[156:159], a[216:219], v[126:129], 0\n"
      "ds_write_b128 v3, v[104:107] offset:18432\n"
      "v_pk_add_f32 v[102:103], v[102:103], v[222:223] op_sel_hi:[1,0] neg_lo:[0,1] neg_hi:[0,1]\n"
      "v_pk_add_f32 v[100:101], v[100:101], v[222:223] op_sel_hi:[1,0] neg_lo:[0,1] neg_hi:[0,1]\n"
      "v_pk_add_f32 v[190:191], v[190:191], v[222:223] op_sel_hi:[1,0] neg_lo:[0,1] neg_hi:[0,1]\n"
      "v_pk_add_f32 v[108:109], v[108:109], v[222:223] op_sel_hi:[1,0] neg_lo:[0,1] neg_hi:[0,1]\n"
      "v_pk_add_f32 v[192:193], v[192:193], v[222:223] op_sel_hi:[1,0] neg_lo:[0,1] neg_hi:[0,1]\n"
      "v_pk_fma_f32 v[164:165], v[206:207], v[206:207], v[164:165]\n"
      "v_mfma_f32_16x16x32_bf16 v[156:159], a[220:223], v[218:221], v[156:159]\n"
      "v_exp_f32_e64 v108, v108\n"
      "v_mfma_f32_16x16x32_bf16 v[156:159], a[224:227], v[208:211], v[156:159]\n"
      "ds_write_b64 v4, v[152:153] offset:18432\n"
      "v_mfma_f32_16x16x32_bf16 v[146:149], a[240:243], v[126:129], 0\n"
      "v_sub_f32 v110, v110, v222\n"
      "v_add_f32 v164, v164, v165\n"
      "v_mfma_f32_16x16x32_bf16 v[146:149], a[244:247], v[218:221], v[146:149]\n"
      "v_exp_f32 v193, v193\n"
      "v_mfma_f32_16x16x32_bf16 v[146:149], a[248:251], v[208:211], v[146:149]\n"
      "v_exp_f32 v192, v192\n"
      "v_mfma_f32_16x16x32_bf16 v[160:163], v[232:235], v[126:129], 0\n"
      "ds_read_b128 v[104:107], v10 offset:1168\n"
      "v_sub_f32 v138, v138, v222\n"
      "v_mfma_f32_16x16x32_bf16 v[160:163], v[236:239], v[218:221], v[160:163]\n"
      "ds_read_b64 v[212:213], v10 offset:1184\n"
      "v_sub_f32 v111, v111, v222\n"
      "v_mfma_f32_16x16x32_bf16 v[160:163], v[240:243], v[208:211], v[160:163]\n"
      "v_exp_f32 v111, v111\n"
      "v_mfma_f32_16x16x32_bf16 v[218:221], v[32:35], v[174:177], 0\n"
      "v_exp_f32 v191, v191\n"
      "v_mfma_f32_16x16x32_bf16 v[218:221], v[32:35], v[92:95], v[218:221]\n"
      "v_exp_f32_e64 v102, v102\n"
      "v_mfma_f32_16x16x32_bf16 v[208:211], v[130:133], v[174:177], 0\n"
      "v_exp_f32 v100, v100\n"
      "v_mfma_f32_16x16x32_bf16 v[208:211], v[76:79], v[92:95], v[208:211]\n"
      "v_exp_f32 v110, v110\n"
      "v_mfma_f32_16x16x32_bf16 v[126:129], v[196:199], v[174:177], 0\n"
      "v_exp_f32 v101, v101\n"
      "v_mfma_f32_16x16x32_bf16 v[126:129], v[80:83], v[92:95], v[126:129]\n"
      "v_rcp_f32 v218, v218\n"
      "v_mfma_f32_16x16x32_bf16 v[220:223], v[214:217], v[174:177], 0\n"
      "s_waitcnt lgkmcnt(0)\n"
      "v_exp_f32 v138, v138\n"
      "v_mfma_f32_16x16x32_bf16 v[220:223], v[84:87], v[92:95], v[220:223]\n"
      "ds_read_b128 v[174:177], v2 offset:9728\n"
      "v_cvt_pk_bf16_f32 v89, v192, v193\n"
      "v_mfma_f32_16x16x32_bf16 v[152:155], v[104:107], v[178:181], v[30:33]\n"
      "v_exp_f32 v190, v190\n"
      "v_mfma_f32_16x16x32_bf16 v[178:181], v[104:107], v[186:189], v[30:33]\n"
      "v_cvt_pk_bf16_f32 v100, v100, v101\n"
      "v_mul_f32 v208, v218, v208\n"
      "v_mfma_f32_16x16x32_bf16 v[186:189], v[104:107], v[182:185], v[30:33]\n"
      "v_exp_f32 v103, v103\n"
      "v_mfma_f32_16x16x32_bf16 v[182:185], v[104:107], v[168:171], v[30:33]\n"
      "ds_read_b128 v[168:171], v2 offset:12800\n"
      "v_mul_f32 v129, v218, v129\n"
      "v_mfma_f32_16x16x16_bf16 v[152:155], v[212:213], v[124:125], v[152:155]\n"
      "ds_read_b128 v[104:107], v2 offset:15872\n"
      "v_cvt_pk_bf16_f32 v101, v102, v103\n"
      "v_mfma_f32_16x16x16_bf16 v[178:181], v[212:213], v[166:167], v[178:181]\n"
      "v_mul_f32 v211, v218, v211\n"
      "v_exp_f32 v109, v109\n"
      "v_permlane16_swap_b32_e64 v144, v164\n"
      "v_mul_f32_e64 v210, v218, v210\n"
      "v_pk_mul_f32 v[126:127], v[126:127], v[218:219] op_sel_hi:[1,0]\n"
      "v_mfma_f32_16x16x16_bf16 v[186:189], v[212:213], v[142:143], v[186:189]\n"
      "v_mul_f32_e64 v209, v218, v209\n"
      "v_mul_f32_e64 v128, v218, v128\n"
      "v_mul_f32_e64 v223, v218, v223\n"
      "v_mfma_f32_16x16x16_bf16 v[182:185], v[212:213], v[172:173], v[182:185]\n"
      "v_cndmask_b32 v152, v152, v178, s[58:59]\n"
      "v_cvt_pk_bf16_f32 v103, v110, v111\n"
      "s_waitcnt lgkmcnt(2)\n"
      "v_mfma_f32_16x16x32_bf16 v[156:159], a[228:231], v[174:177], v[156:159]\n"
      "s_waitcnt lgkmcnt(1)\n"
      "v_cvt_pk_bf16_f32 v208, v208, v209\n"
      "v_cndmask_b32 v152, v152, v186, s[60:61]\n"
      "v_mfma_f32_16x16x32_bf16 v[156:159], a[232:235], v[168:171], v[156:159]\n"
      "s_waitcnt lgkmcnt(0)\n"
      "v_cvt_pk_bf16_f32 v209, v210, v211\n"
      "v_cndmask_b32 v152, v152, v182, s[62:63]\n"
      "v_mfma_f32_16x16x32_bf16 v[156:159], a[236:239], v[104:107], v[156:159]\n"
      "v_fmac_f32 v152, s53, v39\n"
      "v_cvt_pk_bf16_f32 v90, v138, 0\n"
      "v_mfma_f32_16x16x32_bf16 v[146:149], a[252:255], v[174:177], v[146:149]\n"
      "v_cvt_pk_bf16_f32 v88, v190, v191\n"
      "v_max_f32_dpp v213, v152, v152 row_ror:8 row_mask:0xf bank_mask:0xf\n"
      "v_mfma_f32_16x16x32_bf16 v[146:149], v[224:227], v[168:171], v[146:149]\n"
      "s_nop 0\n"
      "v_max_f32_dpp v213, v213, v213 row_ror:4 row_mask:0xf bank_mask:0xf\n"
      "v_fma_f32 v190, v156, v156, s52\n"
      "v_mfma_f32_16x16x32_bf16 v[146:149], v[228:231], v[104:107], v[146:149]\n"
      "v_mul_f32 v191, v157, v157\n"
      "v_cvt_pk_bf16_f32 v210, v126, v127\n"
      "v_mfma_f32_16x16x32_bf16 v[160:163], v[244:247], v[174:177], v[160:163]\n"
      "v_cvt_pk_bf16_f32 v102, v108, v109\n"
      "v_max_f32_dpp v213, v213, v213 row_ror:2 row_mask:0xf bank_mask:0xf\n"
      "v_mfma_f32_16x16x32_bf16 v[160:163], v[248:251], v[168:171], v[160:163]\n"
      "v_cvt_pk_bf16_f32 v211, v128, v129\n"
      "v_max_f32_dpp v213, v213, v213 row_ror:1 row_mask:0xf bank_mask:0xf\n"
      "v_mfma_f32_16x16x32_bf16 v[160:163], v[252:255], v[104:107], v[160:163]\n"
      "ds_write_b128 v3, v[208:211] offset:18688\n"
      "v_pk_fma_f32 v[190:191], v[158:159], v[158:159], v[190:191]\n"
      "v_pk_mul_f32 v[220:221], v[220:221], v[218:219] op_sel_hi:[1,0]\n"
      "v_pk_fma_f32 v[190:191], v[146:147], v[146:147], v[190:191]\n"
      "v_mfma_f32_16x16x32_bf16 v[208:211], v[32:35], v[100:103], 0\n"
      "v_max_f32_dpp v213, v213, v213 row_bcast:15 row_mask:0xa bank_mask:0xf\n"
      "v_mul_f32_e64 v222, v218, v222\n"
      "v_mfma_f32_16x16x32_bf16 v[208:211], v[32:35], v[88:91], v[208:211]\n"
      "v_cvt_pk_bf16_f32 v220, v220, v221\n"
      "v_max_f32_dpp v213, v213, v213 row_bcast:31 row_mask:0xc bank_mask:0xf\n"
      "v_mfma_f32_16x16x32_bf16 v[168:171], v[130:133], v[100:103], 0\n"
      "v_readlane_b32 s72, v213, 63\n"
      "v_add_f32 v144, v144, v164\n"
      "v_mfma_f32_16x16x32_bf16 v[168:171], v[76:79], v[88:91], v[168:171]\n"
      "v_cvt_pk_bf16_f32 v221, v222, v223\n"
      "v_subrev_f32 v152, s72, v152\n"
      "v_mfma_f32_16x16x32_bf16 v[104:107], v[196:199], v[100:103], 0\n"
      "v_exp_f32 v152, v152\n"
      "v_rcp_f32 v208, v208\n"
      "v_cvt_pk_bf16_f32 v152, v152, v152\n"
      "v_pk_fma_f32 v[190:191], v[148:149], v[148:149], v[190:191]\n"
      "v_pk_fma_f32 v[190:191], v[160:161], v[160:161], v[190:191]\n"
      "v_pk_mul_f32 v[168:169], v[168:169], v[208:209] op_sel_hi:[1,0]\n"
      "v_pk_fma_f32 v[190:191], v[162:163], v[162:163], v[190:191]\n"
      "v_pk_mul_f32 v[170:171], v[170:171], v[208:209] op_sel_hi:[1,0]\n"
      "v_mfma_f32_16x16x32_bf16 v[104:107], v[80:83], v[88:91], v[104:107]\n"
      "ds_write_b16 v13, v152\n"
      "v_mfma_f32_16x16x32_bf16 v[210:213], v[214:217], v[100:103], 0\n"
      "ds_read_b128 v[174:177], v14 offset:64\n"
      "v_cvt_pk_bf16_f32 v168, v168, v169\n"
      "v_add_f32 v190, v190, v191\n"
      "v_mfma_f32_16x16x32_bf16 v[210:213], v[84:87], v[88:91], v[210:213]\n"
      "ds_write_b64 v4, v[220:221] offset:18688\n"
      "v_mov_b32 v164, v190\n"
      "v_cvt_pk_bf16_f32 v169, v170, v171\n"
      "v_pk_mul_f32 v[104:105], v[104:105], v[208:209] op_sel_hi:[1,0]\n"
      "v_pk_mul_f32 v[106:107], v[106:107], v[208:209] op_sel_hi:[1,0]\n"
      "v_cvt_pk_bf16_f32 v170, v104, v105\n"
      "v_cvt_pk_bf16_f32 v171, v106, v107\n"
      "s_add_u32 s43, s43, 1\n"
      "ds_write_b128 v3, v[168:171] offset:18944\n"
      "v_permlane16_swap_b32 v190, v164\n"
      "v_add_f32 v190, v190, v164\n"
      "v_pk_mul_f32 v[210:211], v[210:211], v[208:209] op_sel_hi:[1,0]\n"
      "v_pk_mul_f32 v[212:213], v[212:213], v[208:209] op_sel_hi:[1,0]\n"
      "v_permlane32_swap_b32 v144, v190\n"
      "v_add_f32 v144, v144, v190\n"
      "s_and_b32 s47, s47, -4\n"
      "ds_write_b32 v31, v144 offset:1024\n"
      "v_cvt_pk_bf16_f32 v210, v210, v211\n"
      "v_cvt_pk_bf16_f32 v211, v212, v213\n"
      "ds_read_b128 v[218:221], v14\n"
      "ds_write_b64 v4, v[210:211] offset:18944\n"
      "s_cmp_ge_u32 s95, 0x40000000\n"
      "s_cselect_b64 vcc, -1, s[56:57]\n"
      "v_add_u32 v11, 2336, v11\n"
      "s_waitcnt lgkmcnt(1)\n"
      "v_mfma_f32_16x16x32_bf16 v[208:211], v[218:221], v[32:35], 0\n"
      "s_add_u32 s42, s42, 1\n"
      "v_add_u32 v10, 2336, v10\n"
      "v_mfma_f32_16x16x32_bf16 v[208:211], v[174:177], v[32:35], v[208:211]\n"
      "s_waitcnt lgkmcnt(0)\n"
      "s_barrier\n"
      "s_cbranch_vccz L_sel0_%=\n"
      "L_disp0_%=:\n"
      "s_cmp_lt_u32 s95, 0x40000000\n"
      "s_cbranch_scc1 L_nofix_18_%=\n"
      "s_cmp_eq_u32 s95, -2\n"
      "s_cbranch_scc1 L_tail0_%=\n"
      "s_nop 2\n"
      "v_lshlrev_b32 v169, 3, v36\n"
      "s_add_u32 s72, s98, L_cold_%=-L_ipf0_%=\n"
      "s_addc_u32 s73, s99, 0\n"
      "s_mov_b32 s74, L_cend_%=-L_cold_%=\n"
      "s_mov_b32 s75, 0x20000\n"
      "s_mov_b32 m0, 161792\n"
      "s_nop 0\n"
      "buffer_load_dwordx4 v169, s[72:75], 0 offen  lds\n"
      "s_lshr_b32 s92, s45, 30\n"
      "s_or_b32 s92, s92, s66\n"
      "s_cbranch_scc0 L_nf_19_%=\n"
      "s_bitcmp1_b32 s45, 31\n"
      "s_cselect_b32 s75, 100270464, 0\n"
      "s_bitcmp1_b32 s45, 30\n"
      "s_cselect_b32 s76, 196224, 0\n"
      "s_and_b32 s74, s45, 0x3fffffff\n"
      "s_cmp_ge_u32 s45, 0x40000000\n"
      "s_cselect_b32 s74, s74, 0\n"
      "s_cselect_b32 s45, 0, s45\n"
      "s_sub_u32 s74, s74, s66\n"
      "s_add_u32 s66, s66, s74\n"
      "s_sub_u32 s75, s75, s67\n"
      "s_add_u32 s67, s67, s75\n"
      "s_sub_u32 s76, s76, s68\n"
      "s_add_u32 s68, s68, s76\n"
      "v_add_u32 v16, s74, v16\n"
      "v_add_u32 v17, s74, v17\n"
      "v_add_u32 v18, s74, v18\n"
      "v_add_u32 v19, s74, v19\n"
      "v_add_u32 v15, s74, v15\n"
      "s_cmp_eq_u32 s75, 0\n"
      "s_cbranch_scc1 L_ax_20_%=\n"
      "v_bfe_i32 v169, v27, 0, 1\n"
      "v_and_b32 v169, s75, v169\n"
      "v_sub_u32 v16, v16, v169\n"
      "v_bfe_i32 v169, v27, 2, 1\n"
      "v_and_b32 v169, s75, v169\n"
      "v_sub_u32 v17, v17, v169\n"
      "v_bfe_i32 v169, v27, 4, 1\n"
      "v_and_b32 v169, s75, v169\n"
      "v_sub_u32 v18, v18, v169\n"
      "v_bfe_i32 v169, v27, 6, 1\n"
      "v_and_b32 v169, s75, v169\n"
      "v_sub_u32 v19, v19, v169\n"
      "v_bfe_i32 v169, v27, 8, 1\n"
      "v_and_b32 v169, s75, v169\n"
      "v_sub_u32 v15, v15, v169\n"
      "L_ax_20_%=:\n"
      "s_cmp_eq_u32 s76, 0\n"
      "s_cbranch_scc1 L_ax_21_%=\n"
      "v_bfe_i32 v169, v27, 1, 1\n"
      "v_and_b32 v169, s76, v169\n"
      "v_sub_u32 v16, v16, v169\n"
      "v_bfe_i32 v169, v27, 3, 1\n"
      "v_and_b32 v169, s76, v169\n"
      "v_sub_u32 v17, v17, v169\n"
      "v_bfe_i32 v169, v27, 5, 1\n"
      "v_and_b32 v169, s76, v169\n"
      "v_sub_u32 v18, v18, v169\n"
      "v_bfe_i32 v169, v27, 7, 1\n"
      "v_and_b32 v169, s76, v169\n"
      "v_sub_u32 v19, v19, v169\n"
      "v_bfe_i32 v169, v27, 9, 1\n"
      "v_and_b32 v169, s76, v169\n"
      "v_sub_u32 v15, v15, v169\n"
      "L_ax_21_%=:\n"
      "s_or_b32 s48, s66, s69\n"
      "s_cmp_lg_u32 s48, 0\n"
      "s_cselect_b32 s48, 0x40000000, 0\n"
      "L_nf_19_%=:\n"
      "s_lshr_b32 s92, s46, 30\n"
      "s_or_b32 s92, s92, s69\n"
      "s_cbranch_scc0 L_nf_22_%=\n"
      "s_bitcmp1_b32 s46, 31\n"
      "s_cselect_b32 s75, 100270464, 0\n"
      "s_bitcmp1_b32 s46, 30\n"
      "s_cselect_b32 s76, 196224, 0\n"
      "s_and_b32 s74, s46, 0x3fffffff\n"
      "s_cmp_ge_u32 s46, 0x40000000\n"
      "s_cselect_b32 s74, s74, 0\n"
      "s_cselect_b32 s46, 0, s46\n"
      "s_sub_u32 s74, s74, s69\n"
      "s_add_u32 s69, s69, s74\n"
      "s_sub_u32 s75, s75, s70\n"
      "s_add_u32 s70, s70, s75\n"
      "s_sub_u32 s76, s76, s71\n"
      "s_add_u32 s71, s71, s76\n"
      "v_add_u32 v20, s74, v20\n"
      "v_add_u32 v21, s74, v21\n"
      "v_add_u32 v22, s74, v22\n"
      "v_add_u32 v23, s74, v23\n"
      "v_add_u32 v25, s74, v25\n"
      "s_cmp_eq_u32 s75, 0\n"
      "s_cbranch_scc1 L_ax_23_%=\n"
      "v_bfe_i32 v169, v27, 10, 1\n"
      "v_and_b32 v169, s75, v169\n"
      "v_sub_u32 v20, v20, v169\n"
      "v_bfe_i32 v169, v27, 12, 1\n"
      "v_and_b32 v169, s75, v169\n"
      "v_sub_u32 v21, v21, v169\n"
      "v_bfe_i32 v169, v27, 14, 1\n"
      "v_and_b32 v169, s75, v169\n"
      "v_sub_u32 v22, v22, v169\n"
      "v_bfe_i32 v169, v27, 16, 1\n"
      "v_and_b32 v169, s75, v169\n"
      "v_sub_u32 v23, v23, v169\n"
      "v_bfe_i32 v169, v27, 18, 1\n"
      "v_and_b32 v169, s75, v169\n"
      "v_sub_u32 v25, v25, v169\n"
      "L_ax_23_%=:\n"
      "s_cmp_eq_u32 s76, 0\n"
      "s_cbranch_scc1 L_ax_24_%=\n"
      "v_bfe_i32 v169, v27, 11, 1\n"
      "v_and_b32 v169, s76, v169\n"
      "v_sub_u32 v20, v20, v169\n"
      "v_bfe_i32 v169, v27, 13, 1\n"
      "v_and_b32 v169, s76, v169\n"
      "v_sub_u32 v21, v21, v169\n"
      "v_bfe_i32 v169, v27, 15, 1\n"
      "v_and_b32 v169, s76, v169\n"
      "v_sub_u32 v22, v22, v169\n"
      "v_bfe_i32 v169, v27, 17, 1\n"
      "v_and_b32 v169, s76, v169\n"
      "v_sub_u32 v23, v23, v169\n"
      "v_bfe_i32 v169, v27, 19, 1\n"
      "v_and_b32 v169, s76, v169\n"
      "v_sub_u32 v25, v25, v169\n"
      "L_ax_24_%=:\n"
      "s_or_b32 s48, s66, s69\n"
      "s_cmp_lg_u32 s48, 0\n"
      "s_cselect_b32 s48, 0x40000000, 0\n"
      "L_nf_22_%=:\n"
      "s_sub_u32 s92, s42, 1\n"
      "s_cmp_gt_u32 s92, s44\n"
      "s_cbranch_scc1 L_tail0_%=\n"
      "L_nofix_18_%=:\n"
      "s_cmp_lg_u64 s[56:57], 0\n"
      "s_cbranch_scc1 L_slow0_%=\n"
      "L_sel0_%=:\n"
      "s_cmp_lg_u64 s[54:55], 0\n"
      "s_cbranch_scc1 L_max0_%=\n"
      "s_branch L_loop0_%=\n"
      "L_slow0_%=:\n"
      "s_nop 4\n"
      "v_mfma_f32_16x16x32_bf16 v[168:171], v[218:221], v[130:133], 0\n"
      "ds_read_b128 v[138:141], v0\n"
      "s_mov_b32 m0, 110592\n"
      "v_mfma_f32_16x16x32_bf16 v[168:171], v[174:177], v[76:79], v[168:171]\n"
      "ds_read_b128 v[190:193], v9 offset:1024\n"
      "v_subrev_u32 v76, 115456, v9\n"
      "v_mfma_f32_16x16x32_bf16 v[150:153], v[218:221], v[196:199], 0\n"
      "s_add_u32 s72, s42, 1\n"
      "v_readlane_b32 s77, v38, s72\n"
      "v_subrev_u32 v131, 115456, v9\n"
      "v_mfma_f32_16x16x32_bf16 v[150:153], v[174:177], v[80:83], v[150:153]\n"
      "ds_read_b128 v[108:111], v0 offset:6144\n"
      "v_subrev_u32 v39, 115456, v9\n"
      "v_mfma_f32_16x16x32_bf16 v[194:197], v[218:221], v[214:217], 0\n"
      "ds_read_b128 v[164:167], v1\n"
      "s_and_b32 s73, s77, -16\n"
      "v_mfma_f32_16x16x32_bf16 v[194:197], v[174:177], v[84:87], v[194:197]\n"
      "buffer_load_dwordx4 v131, s[28:31], s73 offen  lds\n"
      "s_waitcnt lgkmcnt(3)\n"
      "v_mfma_f32_16x16x32_bf16 v[152:155], a[0:3], v[138:141], 0\n"
      "s_add_u32 s73, s73, 1024\n"
      "ds_read_b128 v[122:125], v0 offset:4096\n"
      "v_mfma_f32_16x16x32_bf16 v[210:213], a[24:27], v[138:141], 0\n"
      "s_waitcnt lgkmcnt(3)\n"
      "v_add_f32 v190, v190, v191\n"
      "v_add_f32 v192, v192, v193\n"
      "v_mfma_f32_16x16x32_bf16 v[174:177], a[48:51], v[138:141], 0\n"
      "ds_read_b128 v[170:173], v1 offset:12288\n"
      "v_add_f32 v190, v190, v192\n"
      "v_mfma_f32_16x16x32_bf16 v[104:107], a[72:75], v[138:141], 0\n"
      "s_mov_b32 m0, 111616\n"
      "v_rsq_f32 v190, v190\n"
      "v_mfma_f32_16x16x32_bf16 v[126:129], a[96:99], v[138:141], 0\n"
      "s_waitcnt lgkmcnt(2)\n"
      "v_mov_b32 v93, 0\n"
      "v_mul_f32 v190, 0x415db3d7, v190\n"
      "v_mfma_f32_16x16x32_bf16 v[178:181], a[120:123], v[138:141], 0\n"
      "ds_read_b128 v[186:189], v1 offset:6144\n"
      "v_mov_b32_e64 v192, v190\n"
      "v_mfma_f32_16x16x32_bf16 v[152:155], a[4:7], v[164:167], v[152:155]\n"
      "buffer_load_dwordx4 v131, s[28:31], s73 offen  lds\n"
      "v_mfma_f32_16x16x32_bf16 v[152:155], a[8:11], v[108:111], v[152:155]\n"
      "v_permlane32_swap_b32 v190, v192\n"
      "v_mfma_f32_16x16x32_bf16 v[210:213], a[28:31], v[164:167], v[210:213]\n"
      "v_mov_b32 v191, v190\n"
      "v_readlane_b32 s84, v37, s43\n"
      "v_mfma_f32_16x16x32_bf16 v[210:213], a[32:35], v[108:111], v[210:213]\n"
      "v_permlane16_swap_b32 v190, v191\n"
      "v_mfma_f32_16x16x32_bf16 v[174:177], a[52:55], v[164:167], v[174:177]\n"
      "s_add_u32 s73, s73, 1024\n"
      "v_mul_f32 v200, v191, v200\n"
      "v_mul_f32 v201, v191, v201\n"
      "v_mfma_f32_16x16x32_bf16 v[174:177], a[56:59], v[108:111], v[174:177]\n"
      "ds_read_b128 v[182:185], v0 offset:12288\n"
      "v_mul_f32 v98, v190, v98\n"
      "v_mfma_f32_16x16x32_bf16 v[104:107], a[76:79], v[164:167], v[104:107]\n"
      "s_mov_b32 m0, 112640\n"
      "v_bfe_u32 v78, v76, 4, 4\n"
      "v_mul_f32 v112, v191, v112\n"
      "v_mfma_f32_16x16x32_bf16 v[104:107], a[80:83], v[108:111], v[104:107]\n"
      "v_mul_f32 v203, v191, v203\n"
      "v_mul_f32_e64 v157, v192, v157\n"
      "v_mfma_f32_16x16x32_bf16 v[126:129], a[100:103], v[164:167], v[126:129]\n"
      "buffer_load_dwordx4 v131, s[28:31], s73 offen  lds\n"
      "v_mfma_f32_16x16x32_bf16 v[126:129], a[104:107], v[108:111], v[126:129]\n"
      "v_mov_b32_dpp v208, v208 quad_perm:[0,0,0,0] row_mask:0xf bank_mask:0xf\n"
      "v_mul_f32 v158, v192, v158\n"
      "v_mfma_f32_16x16x32_bf16 v[178:181], a[124:127], v[164:167], v[178:181]\n"
      "s_mov_b32 m0, 113664\n"
      "v_mul_f32 v118, v190, v118\n"
      "v_mul_f32 v160, v192, v160\n"
      "v_mfma_f32_16x16x32_bf16 v[178:181], a[128:131], v[108:111], v[178:181]\n"
      "s_waitcnt lgkmcnt(0)\n"
      "v_mul_f32 v115, v191, v115\n"
      "v_mul_f32 v202, v191, v202\n"
      "v_mfma_f32_16x16x32_bf16 v[152:155], a[12:15], v[186:189], v[152:155]\n"
      "s_add_u32 s73, s73, 1024\n"
      "v_mul_f32_e64 v114, v191, v114\n"
      "v_cvt_pk_bf16_f32 v200, v200, v201\n"
      "v_mfma_f32_16x16x32_bf16 v[152:155], a[16:19], v[182:185], v[152:155]\n"
      "buffer_load_dwordx4 v131, s[28:31], s73 offen  lds\n"
      "v_mfma_f32_16x16x32_bf16 v[152:155], a[20:23], v[170:173], v[152:155]\n"
      "s_add_u32 s73, s73, 1024\n"
      "v_mul_f32_e64 v99, v190, v99\n"
      "v_mul_f32_e64 v205, v191, v205\n"
      "v_mfma_f32_16x16x32_bf16 v[210:213], a[36:39], v[186:189], v[210:213]\n"
      "s_mov_b32 m0, 114688\n"
      "v_mul_f32_e64 v137, v190, v137\n"
      "v_mul_f32 v119, v190, v119\n"
      "v_mfma_f32_16x16x32_bf16 v[210:213], a[40:43], v[182:185], v[210:213]\n"
      "s_mov_b32 exec_hi, 0xffff\n"
      "buffer_load_dwordx4 v131, s[28:31], s73 offen  lds\n"
      "s_mov_b32 exec_hi, -1\n"
      "v_pk_mul_f32 v[96:97], v[96:97], v[190:191] op_sel_hi:[1,0]\n"
      "v_pk_mul_f32 v[116:117], v[116:117], v[190:191] op_sel_hi:[1,0]\n"
      "v_mfma_f32_16x16x32_bf16 v[210:213], a[44:47], v[170:173], v[210:213]\n"
      "v_mul_f32_e64 v147, v192, v147\n"
      "v_cvt_pk_bf16_f32 v96, v96, v97\n"
      "v_mfma_f32_16x16x32_bf16 v[174:177], a[60:63], v[186:189], v[174:177]\n"
      "ds_read_b128 v[100:103], v1 offset:2048\n"
      "v_mul_f32_e64 v159, v192, v159\n"
      "v_mfma_f32_16x16x32_bf16 v[174:177], a[64:67], v[182:185], v[174:177]\n"
      "s_add_u32 m0, s64, 18432\n"
      "v_cvt_pk_bf16_f32 v201, v202, v203\n"
      "v_cvt_pk_bf16_f32 v97, v98, v99\n"
      "v_mfma_f32_16x16x32_bf16 v[174:177], a[68:71], v[170:173], v[174:177]\n"
      "buffer_load_dwordx4 v16, s[16:19], s45 offen  lds\n"
      "v_mfma_f32_16x16x32_bf16 v[104:107], a[84:87], v[186:189], v[104:107]\n"
      "s_add_u32 m0, s64, 22528\n"
      "v_mul_f32 v149, v192, v149\n"
      "v_mul_f32 v204, v191, v204\n"
      "v_mfma_f32_16x16x32_bf16 v[104:107], a[88:91], v[182:185], v[104:107]\n"
      "v_cvt_pk_bf16_f32 v99, v118, v119\n"
      "v_cvt_pk_bf16_f32 v203, v114, v115\n"
      "v_mfma_f32_16x16x32_bf16 v[104:107], a[92:95], v[170:173], v[104:107]\n"
      "ds_read_b128 v[118:121], v1 offset:8192\n"
      "v_mul_f32_e64 v113, v191, v113\n"
      "v_mfma_f32_16x16x32_bf16 v[126:129], a[108:111], v[186:189], v[126:129]\n"
      "v_fma_f32 v169, v152, v152, s51\n"
      "v_cvt_pk_bf16_f32 v98, v116, v117\n"
      "v_mfma_f32_16x16x32_bf16 v[126:129], a[112:115], v[182:185], v[126:129]\n"
      "ds_read_b128 v[220:223], v1 offset:14336\n"
      "v_mul_f32_e64 v162, v192, v162\n"
      "v_mfma_f32_16x16x32_bf16 v[126:129], a[116:119], v[170:173], v[126:129]\n"
      "ds_write_b128 v5, v[96:99]\n"
      "v_mfma_f32_16x16x32_bf16 v[178:181], a[132:135], v[186:189], v[178:181]\n"
      "buffer_load_dwordx4 v17, s[16:19], s45 offen  lds\n"
      "v_mfma_f32_16x16x32_bf16 v[178:181], a[136:139], v[182:185], v[178:181]\n"
      "s_add_u32 m0, s64, 26624\n"
      "v_fma_f32 v209, v104, v104, s51\n"
      "v_mul_f32 v146, v192, v146\n"
      "v_mfma_f32_16x16x32_bf16 v[178:181], a[140:143], v[170:173], v[178:181]\n"
      "ds_read_b128 v[114:117], v0 offset:2048\n"
      "v_fmac_f32 v209, v105, v105\n"
      "v_mfma_f32_16x16x32_bf16 v[130:133], v[138:141], a[144:147], 0\n"
      "ds_read_b128 v[142:145], v0 offset:14336\n"
      "v_mul_f32_e64 v156, v192, v156\n"
      "v_mfma_f32_16x16x32_bf16 v[130:133], v[164:167], a[148:151], v[130:133]\n"
      "v_cvt_pk_bf16_f32 v204, v204, v205\n"
      "v_cvt_pk_bf16_f32 v156, v156, v157\n"
      "v_mfma_f32_16x16x32_bf16 v[130:133], v[108:111], a[152:155], v[130:133]\n"
      "buffer_load_dwordx4 v18, s[16:19], s45 offen  lds\n"
      "v_mfma_f32_16x16x32_bf16 v[130:133], v[186:189], a[156:159], v[130:133]\n"
      "v_mul_f32 v134, v190, v134\n"
      "v_cvt_pk_bf16_f32 v157, v158, v159\n"
      "v_mfma_f32_16x16x32_bf16 v[130:133], v[182:185], a[160:163], v[130:133]\n"
      "s_add_u32 m0, s64, 30720\n"
      "v_mul_f32 v135, v190, v135\n"
      "v_cvt_pk_bf16_f32 v202, v112, v113\n"
      "v_mfma_f32_16x16x32_bf16 v[130:133], v[170:173], a[164:167], v[130:133]\n"
      "ds_write_b128 v5, v[200:203] offset:6144\n"
      "v_mfma_f32_16x16x32_bf16 v[196:199], v[138:141], a[168:171], 0\n"
      "buffer_load_dwordx4 v19, s[16:19], s45 offen  lds\n"
      "v_mfma_f32_16x16x32_bf16 v[196:199], v[164:167], a[172:175], v[196:199]\n"
      "v_rcp_f32_e64 v208, v208\n"
      "v_mfma_f32_16x16x32_bf16 v[196:199], v[108:111], a[176:179], v[196:199]\n"
      "ds_read_b128 v[200:203], v0 offset:8192\n"
      "v_cvt_pk_bf16_f32 v134, v134, v135\n"
      "v_mfma_f32_16x16x32_bf16 v[196:199], v[186:189], a[180:183], v[196:199]\n"
      "v_fmac_f32 v209, v106, v106\n"
      "v_mul_f32 v148, v192, v148\n"
      "v_mfma_f32_16x16x32_bf16 v[196:199], v[182:185], a[184:187], v[196:199]\n"
      "s_add_u32 m0, s65, 34816\n"
      "v_fmac_f32_e64 v209, v107, v107\n"
      "v_cvt_pk_bf16_f32 v158, v146, v147\n"
      "v_mfma_f32_16x16x32_bf16 v[196:199], v[170:173], a[188:191], v[196:199]\n"
      "buffer_load_dwordx4 v15, s[16:19], s45 offen  lds\n"
      "v_mfma_f32_16x16x32_bf16 v[214:217], v[138:141], a[192:195], 0\n"
      "v_cndmask_b32 v168, v168, v150, s[58:59]\n"
      "v_fmac_f32 v169, v153, v153\n"
      "v_mfma_f32_16x16x32_bf16 v[214:217], v[164:167], a[196:199], v[214:217]\n"
      "v_cvt_pk_bf16_f32 v159, v148, v149\n"
      "v_fmac_f32 v169, v154, v154\n"
      "v_mfma_f32_16x16x32_bf16 v[214:217], v[108:111], a[200:203], v[214:217]\n"
      "v_cvt_pk_bf16_f32 v130, v130, v131\n"
      "v_fmac_f32 v169, v155, v155\n"
      "v_mfma_f32_16x16x32_bf16 v[214:217], v[186:189], a[204:207], v[214:217]\n"
      "v_fmac_f32 v169, v210, v210\n"
      "v_fmac_f32_e64 v169, v211, v211\n"
      "v_mfma_f32_16x16x32_bf16 v[214:217], v[182:185], a[208:211], v[214:217]\n"
      "buffer_load_dwordx4 v[108:111], v36, s[28:31], s47 offen\n"
      "v_mfma_f32_16x16x32_bf16 v[214:217], v[170:173], a[212:215], v[214:217]\n"
      "s_waitcnt lgkmcnt(0)\n"
      "v_fmac_f32 v169, v212, v212\n"
      "v_fmac_f32 v209, v126, v126\n"
      "v_mfma_f32_16x16x32_bf16 v[138:141], a[0:3], v[114:117], 0\n"
      "ds_write_b128 v5, v[156:159] offset:12288\n"
      "v_mfma_f32_16x16x32_bf16 v[138:141], a[4:7], v[100:103], v[138:141]\n"
      "v_mul_f32 v163, v192, v163\n"
      "v_mul_f32 v161, v192, v161\n"
      "v_mfma_f32_16x16x32_bf16 v[138:141], a[8:11], v[200:203], v[138:141]\n"
      "buffer_load_dword v82, v29, s[28:31], s47 offen\n"
      "v_mfma_f32_16x16x32_bf16 v[138:141], a[12:15], v[118:121], v[138:141]\n"
      "v_fmac_f32 v209, v127, v127\n"
      "v_fmac_f32 v209, v128, v128\n"
      "v_mfma_f32_16x16x32_bf16 v[138:141], a[16:19], v[142:145], v[138:141]\n"
      "ds_read_b128 v[156:159], v9 offset:5632\n"
      "v_fmac_f32 v209, v129, v129\n"
      "v_mfma_f32_16x16x32_bf16 v[138:141], a[20:23], v[220:223], v[138:141]\n"
      "ds_read_b128 v[96:99], v1 offset:4096\n"
      "v_fmac_f32_e64 v209, v178, v178\n"
      "v_mfma_f32_16x16x32_bf16 v[186:189], a[24:27], v[114:117], 0\n"
      "v_readlane_b32 s85, v37, s42\n"
      "v_cvt_pk_bf16_f32 v196, v196, v197\n"
      "v_mfma_f32_16x16x32_bf16 v[186:189], a[28:31], v[100:103], v[186:189]\n"
      "s_and_b32 s77, s77, 15\n"
      "v_cvt_pk_bf16_f32 v214, v214, v215\n"
      "v_fmac_f32 v169, v213, v213\n"
      "v_mfma_f32_16x16x32_bf16 v[186:189], a[32:35], v[200:203], v[186:189]\n"
      "v_fmac_f32 v169, v174, v174\n"
      "v_fmac_f32 v169, v175, v175\n"
      "v_mfma_f32_16x16x32_bf16 v[186:189], a[36:39], v[118:121], v[186:189]\n"
      "v_fmac_f32_e64 v209, v179, v179\n"
      "v_fma_f32 v81, v138, v138, s51\n"
      "v_mfma_f32_16x16x32_bf16 v[186:189], a[40:43], v[142:145], v[186:189]\n"
      "v_fmac_f32 v169, v176, v176\n"
      "v_fmac_f32 v169, v177, v177\n"
      "v_mfma_f32_16x16x32_bf16 v[186:189], a[44:47], v[220:223], v[186:189]\n"
      "s_waitcnt lgkmcnt(0)\n"
      "v_mul_f32 v136, v190, v136\n"
      "v_cvt_pk_bf16_f32 v131, v132, v133\n"
      "v_mfma_f32_16x16x32_bf16 v[148:151], a[48:51], v[114:117], 0\n"
      "v_or3_b32 v156, v156, v157, v158\n"
      "v_fmac_f32 v209, v180, v180\n"
      "v_mfma_f32_16x16x32_bf16 v[148:151], a[52:55], v[100:103], v[148:151]\n"
      "v_fmac_f32 v209, v181, v181\n"
      "v_cndmask_b32 v168, v168, v194, s[60:61]\n"
      "v_mfma_f32_16x16x32_bf16 v[148:151], a[56:59], v[200:203], v[148:151]\n"
      "v_permlane16_swap_b32_e64 v169, v209\n"
      "v_mfma_f32_16x16x32_bf16 v[148:151], a[60:63], v[118:121], v[148:151]\n"
      "v_mul_u32_u24 v86, 98, v78\n"
      "v_cvt_pk_bf16_f32 v160, v160, v161\n"
      "v_mfma_f32_16x16x32_bf16 v[148:151], a[64:67], v[142:145], v[148:151]\n"
      "v_add_f32 v169, v169, v209\n"
      "v_lshrrev_b32 v76, 8, v76\n"
      "v_mfma_f32_16x16x32_bf16 v[148:151], a[68:71], v[220:223], v[148:151]\n"
      "v_fmac_f32 v81, v139, v139\n"
      "v_mov_b32 v209, v169\n"
      "v_mfma_f32_16x16x32_bf16 v[170:173], a[72:75], v[114:117], 0\n"
      "s_nop 0\n"
      "v_permlane32_swap_b32 v169, v209\n"
      "v_mfma_f32_16x16x32_bf16 v[170:173], a[76:79], v[100:103], v[170:173]\n"
      "v_cvt_pk_bf16_f32 v161, v162, v163\n"
      "v_fmac_f32 v81, v140, v140\n"
      "v_mfma_f32_16x16x32_bf16 v[170:173], a[80:83], v[200:203], v[170:173]\n"
      "v_lshl_add_u32 v86, v76, 3, v86\n"
      "v_fmac_f32 v81, v141, v141\n"
      "v_mfma_f32_16x16x32_bf16 v[170:173], a[84:87], v[118:121], v[170:173]\n"
      "v_add_f32 v169, v169, v209\n"
      "v_cvt_pk_bf16_f32 v197, v198, v199\n"
      "v_mfma_f32_16x16x32_bf16 v[170:173], a[88:91], v[142:145], v[170:173]\n"
      "s_or_b32 s95, s84, s85\n"
      "v_lshlrev_b32 v76, 2, v76\n"
      "v_fmac_f32 v81, v186, v186\n"
      "v_mfma_f32_16x16x32_bf16 v[170:173], a[92:95], v[220:223], v[170:173]\n"
      "v_mul_f32 v206, v191, v206\n"
      "v_fmac_f32 v81, v187, v187\n"
      "v_mfma_f32_16x16x32_bf16 v[182:185], a[96:99], v[114:117], 0\n"
      "ds_write_b64 v6, v[160:161] offset:12288\n"
      "v_mfma_f32_16x16x32_bf16 v[182:185], a[100:103], v[100:103], v[182:185]\n"
      "v_rsq_f32 v169, v169\n"
      "v_mfma_f32_16x16x32_bf16 v[182:185], a[104:107], v[200:203], v[182:185]\n"
      "v_mul_f32 v207, v191, v207\n"
      "v_mul_f32 v168, v208, v168\n"
      "v_mfma_f32_16x16x32_bf16 v[182:185], a[108:111], v[118:121], v[182:185]\n"
      "v_mov_b32 v209, v169\n"
      "v_fma_f32 v158, v170, v170, s51\n"
      "v_mfma_f32_16x16x32_bf16 v[182:185], a[112:115], v[142:145], v[182:185]\n"
      "v_cvt_pk_bf16_f32 v135, v136, v137\n"
      "v_fmac_f32 v158, v171, v171\n"
      "v_mfma_f32_16x16x32_bf16 v[182:185], a[116:119], v[220:223], v[182:185]\n"
      "v_permlane16_swap_b32 v169, v209\n"
      "v_mfma_f32_16x16x32_bf16 v[164:167], a[120:123], v[114:117], 0\n"
      "v_cvt_pk_bf16_f32 v168, v168, v168\n"
      "v_mul_f32 v169, s50, v169\n"
      "v_mfma_f32_16x16x32_bf16 v[164:167], a[124:127], v[100:103], v[164:167]\n"
      "v_cvt_pk_bf16_f32 v215, v216, v217\n"
      "v_mul_f32 v174, v169, v174\n"
      "v_mfma_f32_16x16x32_bf16 v[164:167], a[128:131], v[200:203], v[164:167]\n"
      "v_fmac_f32 v158, v172, v172\n"
      "v_mul_f32 v211, v169, v211\n"
      "v_mfma_f32_16x16x32_bf16 v[164:167], a[132:135], v[118:121], v[164:167]\n"
      "v_lshrrev_b32 v39, 3, v39\n"
      "v_fmac_f32 v158, v173, v173\n"
      "v_mfma_f32_16x16x32_bf16 v[164:167], a[136:139], v[142:145], v[164:167]\n"
      "v_cvt_pk_bf16_f32 v205, v206, v207\n"
      "v_fmac_f32 v81, v188, v188\n"
      "v_mfma_f32_16x16x32_bf16 v[164:167], a[140:143], v[220:223], v[164:167]\n"
      "v_mul_f32 v152, v169, v152\n"
      "v_mul_f32_e64 v105, v209, v105\n"
      "v_mfma_f32_16x16x32_bf16 v[216:219], v[114:117], a[144:147], 0\n"
      "ds_write_b16 v12, v168 offset:16\n"
      "v_mfma_f32_16x16x32_bf16 v[216:219], v[100:103], a[148:151], v[216:219]\n"
      "v_mul_f32 v107, v209, v107\n"
      "v_fmac_f32 v158, v182, v182\n"
      "v_mfma_f32_16x16x32_bf16 v[216:219], v[200:203], a[152:155], v[216:219]\n"
      "ds_write_b64 v6, v[204:205] offset:6144\n"
      "v_mfma_f32_16x16x32_bf16 v[216:219], v[118:121], a[156:159], v[216:219]\n"
      "v_mul_f32 v153, v169, v153\n"
      "v_mul_f32 v180, v209, v180\n"
      "v_mfma_f32_16x16x32_bf16 v[216:219], v[142:145], a[160:163], v[216:219]\n"
      "v_mul_f32 v178, v209, v178\n"
      "v_fmac_f32 v158, v183, v183\n"
      "v_mfma_f32_16x16x32_bf16 v[216:219], v[220:223], a[164:167], v[216:219]\n"
      "s_or_b32 s95, s95, s48\n"
      "v_fmac_f32 v81, v189, v189\n"
      "v_fmac_f32 v81, v148, v148\n"
      "v_mfma_f32_16x16x32_bf16 v[204:207], v[114:117], a[168:171], 0\n"
      "ds_write_b64 v6, v[134:135]\n"
      "v_mfma_f32_16x16x32_bf16 v[204:207], v[100:103], a[172:175], v[204:207]\n"
      "v_mul_f32 v104, v209, v104\n"
      "v_sub_u32 v78, v78, v76\n"
      "v_mfma_f32_16x16x32_bf16 v[204:207], v[200:203], a[176:179], v[204:207]\n"
      "v_fmac_f32 v81, v149, v149\n"
      "v_fmac_f32 v158, v184, v184\n"
      "v_mfma_f32_16x16x32_bf16 v[204:207], v[118:121], a[180:183], v[204:207]\n"
      "v_mul_f32 v210, v169, v210\n"
      "v_lshlrev_b32 v78, 4, v78\n"
      "v_mfma_f32_16x16x32_bf16 v[204:207], v[142:145], a[184:187], v[204:207]\n"
      "ds_read_b128 v[192:195], v0 offset:10240\n"
      "v_mul_f32 v154, v169, v154\n"
      "v_mfma_f32_16x16x32_bf16 v[204:207], v[220:223], a[188:191], v[204:207]\n"
      "v_mul_f32 v181, v209, v181\n"
      "v_fmac_f32 v158, v185, v185\n"
      "v_mfma_f32_16x16x32_bf16 v[134:137], v[114:117], a[192:195], 0\n"
      "ds_read_b128 v[112:115], v1 offset:16384\n"
      "v_fmac_f32_e64 v158, v164, v164\n"
      "v_mfma_f32_16x16x32_bf16 v[134:137], v[100:103], a[196:199], v[134:137]\n"
      "v_mul_f32 v179, v209, v179\n"
      "v_fmac_f32 v158, v165, v165\n"
      "v_mfma_f32_16x16x32_bf16 v[134:137], v[200:203], a[200:203], v[134:137]\n"
      "ds_read_b128 v[200:203], v1 offset:10240\n"
      "v_mul_f32_e64 v177, v169, v177\n"
      "v_mfma_f32_16x16x32_bf16 v[134:137], v[118:121], a[204:207], v[134:137]\n"
      "ds_read_b128 v[116:119], v0 offset:16384\n"
      "v_cvt_pk_bf16_f32 v152, v152, v153\n"
      "v_mfma_f32_16x16x32_bf16 v[134:137], v[142:145], a[208:211], v[134:137]\n"
      "v_fmac_f32 v81, v150, v150\n"
      "v_fmac_f32 v158, v166, v166\n"
      "v_mfma_f32_16x16x32_bf16 v[134:137], v[220:223], a[212:215], v[134:137]\n"
      "v_mul_f32 v106, v209, v106\n"
      "v_fmac_f32 v81, v151, v151\n"
      "v_mfma_f32_16x16x32_bf16 v[144:147], a[0:3], v[122:125], 0\n"
      "v_fmac_f32 v158, v167, v167\n"
      "v_cvt_pk_bf16_f32 v198, v204, v205\n"
      "v_mfma_f32_16x16x32_bf16 v[144:147], a[4:7], v[96:99], v[144:147]\n"
      "s_waitcnt lgkmcnt(2)\n"
      "v_permlane16_swap_b32 v81, v158\n"
      "v_mfma_f32_16x16x32_bf16 v[144:147], a[8:11], v[192:195], v[144:147]\n"
      "s_waitcnt lgkmcnt(1)\n"
      "v_bitop3_b32 v156, v156, v159, s33 bitop3:0xa8\n"
      "v_mul_f32_e64 v155, v169, v155\n"
      "v_mfma_f32_16x16x32_bf16 v[144:147], a[12:15], v[200:203], v[144:147]\n"
      "s_waitcnt lgkmcnt(0)\n"
      "v_add_f32 v81, v81, v158\n"
      "v_mov_b32 v158, v81\n"
      "v_mfma_f32_16x16x32_bf16 v[144:147], a[16:19], v[116:119], v[144:147]\n"
      "s_nop 0\n"
      "v_permlane32_swap_b32 v81, v158\n"
      "v_mfma_f32_16x16x32_bf16 v[144:147], a[20:23], v[112:115], v[144:147]\n"
      "v_add_f32 v81, v81, v158\n"
      "v_cvt_pk_bf16_f32 v153, v154, v155\n"
      "v_mfma_f32_16x16x32_bf16 v[220:223], a[24:27], v[122:125], 0\n"
      "v_rsq_f32 v81, v81\n"
      "v_mfma_f32_16x16x32_bf16 v[220:223], a[28:31], v[96:99], v[220:223]\n"
      "v_mov_b32 v158, v81\n"
      "v_cvt_pk_bf16_f32 v154, v210, v211\n"
      "v_mfma_f32_16x16x32_bf16 v[220:223], a[32:35], v[192:195], v[220:223]\n"
      "s_add_u32 s73, s73, 1024\n"
      "v_fma_f32 v204, v144, v144, s51\n"
      "v_add_u32 v86, s77, v86\n"
      "v_mfma_f32_16x16x32_bf16 v[220:223], a[36:39], v[200:203], v[220:223]\n"
      "v_permlane16_swap_b32 v81, v158\n"
      "v_mfma_f32_16x16x32_bf16 v[220:223], a[40:43], v[116:119], v[220:223]\n"
      "v_mul_f32_e64 v81, s50, v81\n"
      "v_cvt_pk_bf16_f32 v178, v178, v179\n"
      "v_mfma_f32_16x16x32_bf16 v[220:223], a[44:47], v[112:115], v[220:223]\n"
      "v_cvt_pk_bf16_f32 v104, v104, v105\n"
      "v_mul_f32 v175, v169, v175\n"
      "v_mfma_f32_16x16x32_bf16 v[160:163], a[48:51], v[122:125], 0\n"
      "v_fmac_f32 v204, v145, v145\n"
      "v_mul_f32_e64 v189, v81, v189\n"
      "v_mfma_f32_16x16x32_bf16 v[160:163], a[52:55], v[96:99], v[160:163]\n"
      "v_mul_f32 v176, v169, v176\n"
      "v_mul_f32 v188, v81, v188\n"
      "v_mfma_f32_16x16x32_bf16 v[160:163], a[56:59], v[192:195], v[160:163]\n"
      "v_min_u32 v39, 96, v39\n"
      "v_fmac_f32 v204, v146, v146\n"
      "v_mfma_f32_16x16x32_bf16 v[160:163], a[60:63], v[200:203], v[160:163]\n"
      "v_cvt_pk_bf16_f32 v132, v216, v217\n"
      "v_cvt_pk_bf16_f32 v179, v180, v181\n"
      "v_add_u32 v39, s77, v39\n"
      "v_cvt_pk_bf16_f32 v105, v106, v107\n"
      "v_cvt_pk_bf16_f32 v133, v218, v219\n"
      "v_pk_mul_f32 v[170:171], v[170:171], v[158:159] op_sel_hi:[1,0]\n"
      "v_pk_mul_f32 v[150:151], v[150:151], v[80:81] op_sel:[0,1]\n"
      "v_pk_mul_f32 v[128:129], v[128:129], v[208:209] op_sel:[0,1]\n"
      "v_pk_mul_f32 v[126:127], v[126:127], v[208:209] op_sel:[0,1]\n"
      "v_pk_mul_f32 v[164:165], v[164:165], v[158:159] op_sel_hi:[1,0]\n"
      "v_pk_mul_f32 v[182:183], v[182:183], v[158:159] op_sel_hi:[1,0]\n"
      "v_pk_mul_f32 v[138:139], v[138:139], v[80:81] op_sel:[0,1]\n"
      "v_pk_mul_f32 v[140:141], v[140:141], v[80:81] op_sel:[0,1]\n"
      "v_pk_mul_f32 v[166:167], v[166:167], v[158:159] op_sel_hi:[1,0]\n"
      "v_pk_mul_f32 v[184:185], v[184:185], v[158:159] op_sel_hi:[1,0]\n"
      "v_pk_mul_f32 v[212:213], v[212:213], v[168:169] op_sel:[0,1]\n"
      "v_pk_mul_f32 v[172:173], v[172:173], v[158:159] op_sel_hi:[1,0]\n"
      "v_mfma_f32_16x16x32_bf16 v[160:163], a[64:67], v[116:119], v[160:163]\n"
      "v_fmac_f32_e64 v204, v147, v147\n"
      "v_cvt_pk_bf16_f32 v216, v134, v135\n"
      "v_mfma_f32_16x16x32_bf16 v[160:163], a[68:71], v[112:115], v[160:163]\n"
      "v_fmac_f32_e64 v204, v220, v220\n"
      "v_cvt_pk_bf16_f32 v138, v138, v139\n"
      "v_mfma_f32_16x16x32_bf16 v[100:103], a[72:75], v[122:125], 0\n"
      "s_cmp_ge_u32 s95, 0x40000000\n"
      "s_cselect_b32 s94, -1, -2\n"
      "v_mul_f32_e64 v187, v81, v187\n"
      "v_mfma_f32_16x16x32_bf16 v[100:103], a[76:79], v[96:99], v[100:103]\n"
      "v_cmp_ne_u32 s[56:57], 0, v156\n"
      "v_cvt_pk_bf16_f32 v139, v140, v141\n"
      "v_mfma_f32_16x16x32_bf16 v[100:103], a[80:83], v[192:195], v[100:103]\n"
      "v_fmac_f32_e64 v204, v221, v221\n"
      "v_cvt_pk_bf16_f32 v170, v170, v171\n"
      "v_mfma_f32_16x16x32_bf16 v[100:103], a[84:87], v[200:203], v[100:103]\n"
      "v_mul_f32_e64 v186, v81, v186\n"
      "v_cvt_pk_bf16_f32 v106, v126, v127\n"
      "v_mfma_f32_16x16x32_bf16 v[100:103], a[88:91], v[116:119], v[100:103]\n"
      "v_add_u32 v39, 115296, v39\n"
      "v_fmac_f32_e64 v204, v222, v222\n"
      "v_mfma_f32_16x16x32_bf16 v[100:103], a[92:95], v[112:115], v[100:103]\n"
      "v_cvt_pk_bf16_f32 v140, v186, v187\n"
      "v_fmac_f32_e64 v204, v223, v223\n"
      "v_mfma_f32_16x16x32_bf16 v[156:159], a[96:99], v[122:125], 0\n"
      "v_cvt_pk_bf16_f32 v217, v136, v137\n"
      "v_cvt_pk_bf16_f32 v174, v174, v175\n"
      "v_mfma_f32_16x16x32_bf16 v[156:159], a[100:103], v[96:99], v[156:159]\n"
      "v_fmac_f32_e64 v204, v160, v160\n"
      "v_cvt_pk_bf16_f32 v171, v172, v173\n"
      "v_mfma_f32_16x16x32_bf16 v[156:159], a[104:107], v[192:195], v[156:159]\n"
      "v_cmp_gt_u32 s[72:73], 64, v78\n"
      "v_mul_f32 v148, v81, v148\n"
      "v_mfma_f32_16x16x32_bf16 v[156:159], a[108:111], v[200:203], v[156:159]\n"
      "v_fmac_f32 v204, v161, v161\n"
      "v_cvt_pk_bf16_f32 v155, v212, v213\n"
      "v_mfma_f32_16x16x32_bf16 v[156:159], a[112:115], v[116:119], v[156:159]\n"
      "v_cvt_pk_bf16_f32 v107, v128, v129\n"
      "v_fmac_f32_e64 v204, v162, v162\n"
      "v_mfma_f32_16x16x32_bf16 v[156:159], a[116:119], v[112:115], v[156:159]\n"
      "ds_read_b128 v[210:213], v7 offset:18432\n"
      "v_fma_f32 v219, v100, v100, s51\n"
      "v_mfma_f32_16x16x32_bf16 v[126:129], a[120:123], v[122:125], 0\n"
      "v_cvt_pk_bf16_f32 v173, v184, v185\n"
      "v_mul_f32 v149, v81, v149\n"
      "v_mfma_f32_16x16x32_bf16 v[126:129], a[124:127], v[96:99], v[126:129]\n"
      "v_fmac_f32 v219, v101, v101\n"
      "v_fmac_f32_e64 v219, v102, v102\n"
      "v_mfma_f32_16x16x32_bf16 v[126:129], a[128:131], v[192:195], v[126:129]\n"
      "v_fmac_f32 v219, v103, v103\n"
      "v_fmac_f32 v219, v156, v156\n"
      "v_mfma_f32_16x16x32_bf16 v[126:129], a[132:135], v[200:203], v[126:129]\n"
      "v_fmac_f32 v204, v163, v163\n"
      "v_fmac_f32 v219, v157, v157\n"
      "v_mfma_f32_16x16x32_bf16 v[126:129], a[136:139], v[116:119], v[126:129]\n"
      "ds_read_b128 v[184:187], v2 offset:21504\n"
      "v_cvt_pk_bf16_f32 v172, v182, v183\n"
      "v_mfma_f32_16x16x32_bf16 v[126:129], a[140:143], v[112:115], v[126:129]\n"
      "v_fmac_f32 v219, v158, v158\n"
      "v_cvt_pk_bf16_f32 v141, v188, v189\n"
      "v_mfma_f32_16x16x32_bf16 v[188:191], v[122:125], a[144:147], 0\n"
      "v_cvt_pk_bf16_f32 v175, v176, v177\n"
      "v_and_b32 v94, 2, v86\n"
      "v_mfma_f32_16x16x32_bf16 v[188:191], v[96:99], a[148:151], v[188:191]\n"
      "v_cvt_pk_bf16_f32 v164, v164, v165\n"
      "v_fmac_f32_e64 v219, v159, v159\n"
      "v_mfma_f32_16x16x32_bf16 v[188:191], v[192:195], a[152:155], v[188:191]\n"
      "v_cvt_pk_bf16_f32 v199, v206, v207\n"
      "v_cvt_pk_bf16_f32 v165, v166, v167\n"
      "v_mfma_f32_16x16x32_bf16 v[188:191], v[200:203], a[156:159], v[188:191]\n"
      "v_fmac_f32 v219, v126, v126\n"
      "v_fmac_f32 v219, v127, v127\n"
      "v_mfma_f32_16x16x32_bf16 v[188:191], v[116:119], a[160:163], v[188:191]\n"
      "s_waitcnt vmcnt(7)\n"
      "v_fmac_f32 v219, v128, v128\n"
      "v_fmac_f32 v219, v129, v129\n"
      "v_mfma_f32_16x16x32_bf16 v[188:191], v[112:115], a[164:167], v[188:191]\n"
      "s_nop 0\n"
      "v_permlane16_swap_b32 v204, v219\n"
      "v_mfma_f32_16x16x32_bf16 v[166:169], v[122:125], a[168:171], 0\n"
      "v_add_f32 v204, v204, v219\n"
      "v_mov_b32 v219, v204\n"
      "v_mfma_f32_16x16x32_bf16 v[166:169], v[96:99], a[172:175], v[166:169]\n"
      "s_nop 0\n"
      "v_permlane32_swap_b32_e64 v204, v219\n"
      "v_mfma_f32_16x16x32_bf16 v[166:169], v[192:195], a[176:179], v[166:169]\n"
      "v_mov_b32 v92, 0x3fb9\n"
      "v_add_f32_e64 v204, v204, v219\n"
      "v_mfma_f32_16x16x32_bf16 v[166:169], v[200:203], a[180:183], v[166:169]\n"
      "s_waitcnt lgkmcnt(0)\n"
      "v_rsq_f32 v204, v204\n"
      "v_mfma_f32_16x16x32_bf16 v[166:169], v[116:119], a[184:187], v[166:169]\n"
      "ds_read_b128 v[134:137], v7 offset:22528\n"
      "v_mov_b32 v219, v204\n"
      "v_mfma_f32_16x16x32_bf16 v[166:169], v[112:115], a[188:191], v[166:169]\n"
      "s_nop 0\n"
      "v_permlane16_swap_b32_e64 v204, v219\n"
      "v_mfma_f32_16x16x32_bf16 v[206:209], v[122:125], a[192:195], 0\n"
      "ds_read_b128 v[120:123], v2 offset:24576\n"
      "v_and_b32_e64 v86, -4, v86\n"
      "v_mfma_f32_16x16x32_bf16 v[206:209], v[96:99], a[196:199], v[206:209]\n"
      "ds_read_b128 v[180:183], v2 offset:18432\n"
      "v_cvt_pk_bf16_f32 v148, v148, v149\n"
      "v_mfma_f32_16x16x32_bf16 v[206:209], v[192:195], a[200:203], v[206:209]\n"
      "ds_read_b128 v[192:195], v2 offset:27648\n"
      "v_mul_f32_e64 v204, s50, v204\n"
      "v_mfma_f32_16x16x32_bf16 v[206:209], v[200:203], a[204:207], v[206:209]\n"
      "buffer_store_dwordx4 v[210:213], v20, s[24:27], s46 offen sc1\n"
      "v_pk_mul_f32 v[158:159], v[158:159], v[218:219] op_sel:[0,1]\n"
      "v_pk_mul_f32 v[128:129], v[128:129], v[218:219] op_sel:[0,1]\n"
      "v_pk_mul_f32 v[100:101], v[100:101], v[218:219] op_sel:[0,1]\n"
      "v_pk_mul_f32 v[160:161], v[160:161], v[204:205] op_sel_hi:[1,0]\n"
      "v_pk_mul_f32 v[146:147], v[146:147], v[204:205] op_sel_hi:[1,0]\n"
      "v_pk_mul_f32 v[220:221], v[220:221], v[204:205] op_sel_hi:[1,0]\n"
      "v_pk_mul_f32 v[102:103], v[102:103], v[218:219] op_sel:[0,1]\n"
      "v_pk_mul_f32 v[222:223], v[222:223], v[204:205] op_sel_hi:[1,0]\n"
      "v_pk_mul_f32 v[144:145], v[144:145], v[204:205] op_sel_hi:[1,0]\n"
      "v_pk_mul_f32 v[156:157], v[156:157], v[218:219] op_sel:[0,1]\n"
      "v_pk_mul_f32 v[126:127], v[126:127], v[218:219] op_sel:[0,1]\n"
      "v_mfma_f32_16x16x32_bf16 v[206:209], v[116:119], a[208:211], v[206:209]\n"
      "s_waitcnt lgkmcnt(1)\n"
      "v_cvt_pk_bf16_f32 v149, v150, v151\n"
      "v_add_u32 v124, 110592, v86\n"
      "v_mfma_f32_16x16x32_bf16 v[206:209], v[112:115], a[212:215], v[206:209]\n"
      "ds_read2_b32 v[218:219], v124 offset0:0 offset1:1\n"
      "v_cvt_pk_bf16_f32 v81, v168, v169\n"
      "v_mfma_f32_16x16x32_bf16 v[96:99], a[216:219], v[180:183], 0\n"
      "ds_read_b128 v[200:203], v2 offset:33792\n"
      "v_mul_f32 v162, v204, v162\n"
      "v_mfma_f32_16x16x32_bf16 v[96:99], a[220:223], v[184:187], v[96:99]\n"
      "ds_read_b128 v[112:115], v2 offset:30720\n"
      "v_cvt_pk_bf16_f32 v126, v126, v127\n"
      "v_mfma_f32_16x16x32_bf16 v[96:99], a[224:227], v[120:123], v[96:99]\n"
      "v_cvt_pk_bf16_f32 v127, v128, v129\n"
      "v_cvt_pk_bf16_f32 v100, v100, v101\n"
      "v_mfma_f32_16x16x32_bf16 v[116:119], a[240:243], v[180:183], 0\n"
      "v_mul_f32_e64 v163, v204, v163\n"
      "v_cvt_pk_bf16_f32 v80, v166, v167\n"
      "v_mfma_f32_16x16x32_bf16 v[116:119], a[244:247], v[184:187], v[116:119]\n"
      "ds_read_b128 v[210:213], v7 offset:26624\n"
      "v_lshlrev_b64 v[92:93], v78, v[92:93]\n"
      "v_mfma_f32_16x16x32_bf16 v[116:119], a[248:251], v[120:123], v[116:119]\n"
      "buffer_store_dwordx4 v[134:137], v21, s[24:27], s46 offen sc1\n"
      "s_nop 1\n"
      "v_mfma_f32_16x16x32_bf16 v[134:137], v[232:235], v[180:183], 0\n"
      "s_waitcnt lgkmcnt(1)\n"
      "v_cvt_pk_bf16_f32 v85, v208, v209\n"
      "v_cvt_pk_bf16_f32 v144, v144, v145\n"
      "v_mfma_f32_16x16x32_bf16 v[134:137], v[236:239], v[184:187], v[134:137]\n"
      "ds_read_b128 v[166:169], v7 offset:30720\n"
      "v_cvt_pk_bf16_f32 v84, v206, v207\n"
      "v_mfma_f32_16x16x32_bf16 v[134:137], v[240:243], v[120:123], v[134:137]\n"
      "ds_read2_b32 v[150:151], v124 offset0:2 offset1:10\n"
      "v_mov_b32 v125, 0\n"
      "v_mfma_f32_16x16x32_bf16 v[96:99], a[228:231], v[192:195], v[96:99]\n"
      "s_waitcnt lgkmcnt(2)\n"
      "v_cvt_pk_bf16_f32 v101, v102, v103\n"
      "v_cvt_pk_bf16_f32 v76, v188, v189\n"
      "v_mfma_f32_16x16x32_bf16 v[96:99], a[232:235], v[112:115], v[96:99]\n"
      "ds_read2_b32 v[206:207], v124 offset0:24 offset1:18\n"
      "v_cndmask_b32_e64 v93, 0, v93, s[72:73]\n"
      "v_mfma_f32_16x16x32_bf16 v[96:99], a[236:239], v[200:203], v[96:99]\n"
      "ds_read_b128 v[184:187], v10 offset:32\n"
      "v_cvt_pk_bf16_f32 v160, v160, v161\n"
      "v_mfma_f32_16x16x32_bf16 v[116:119], a[252:255], v[192:195], v[116:119]\n"
      "ds_read2_b32 v[204:205], v124 offset0:8 offset1:9\n"
      "v_alignbyte_b32 v218, v219, v218, v94\n"
      "v_mfma_f32_16x16x32_bf16 v[116:119], v[224:227], v[112:115], v[116:119]\n"
      "buffer_store_dwordx4 v[210:213], v22, s[24:27], s46 offen sc1\n"
      "v_mfma_f32_16x16x32_bf16 v[116:119], v[228:231], v[200:203], v[116:119]\n"
      "v_cvt_pk_bf16_f32 v102, v156, v157\n"
      "v_cvt_pk_bf16_f32 v145, v146, v147\n"
      "v_mfma_f32_16x16x32_bf16 v[134:137], v[244:247], v[192:195], v[134:137]\n"
      "v_cvt_pk_bf16_f32 v161, v162, v163\n"
      "v_cvt_pk_bf16_f32 v103, v158, v159\n"
      "v_mfma_f32_16x16x32_bf16 v[134:137], v[248:251], v[112:115], v[134:137]\n"
      "ds_read2_b32 v[212:213], v124 offset0:16 offset1:17\n"
      "v_mul_f32 v129, v97, v97\n"
      "v_mfma_f32_16x16x32_bf16 v[134:137], v[252:255], v[200:203], v[134:137]\n"
      "s_waitcnt lgkmcnt(1)\n"
      "v_alignbyte_b32 v204, v205, v204, v94\n"
      "v_fma_f32 v128, v96, v96, s52\n"
      "v_mfma_f32_16x16x32_bf16 v[156:159], v[104:107], v[152:155], v[40:43]\n"
      "v_alignbyte_b32 v205, v151, v205, v94\n"
      "v_cndmask_b32_e64 v92, 0, v92, s[72:73]\n"
      "v_mfma_f32_16x16x32_bf16 v[208:211], v[170:173], v[152:155], v[44:47]\n"
      "ds_read_b64 v[122:123], v10 offset:24\n"
      "v_fmac_f32 v129, v99, v99\n"
      "v_mfma_f32_16x16x32_bf16 v[192:195], v[100:103], v[152:155], v[48:51]\n"
      "v_fmac_f32 v128, v98, v98\n"
      "v_cvt_pk_bf16_f32 v77, v190, v191\n"
      "v_mfma_f32_16x16x32_bf16 v[180:183], v[184:187], v[152:155], v[24:27]\n"
      "ds_read_b128 v[112:115], v7 offset:34816\n"
      "v_mov_b32 v124, 0xbb2b\n"
      "v_mfma_f32_16x16x16_bf16 v[156:159], v[178:179], v[174:175], v[156:159]\n"
      "v_alignbyte_b32 v219, v150, v219, v94\n"
      "v_add_u32 v120, 113728, v86\n"
      "v_cmp_gt_u32 s[72:73], 64, v78\n"
      "v_mfma_f32_16x16x16_bf16 v[208:211], v[164:165], v[174:175], v[208:211]\n"
      "buffer_store_dwordx4 v[166:169], v23, s[24:27], s46 offen sc1\n"
      "v_pk_fma_f32 v[128:129], v[116:117], v[116:117], v[128:129]\n"
      "v_mfma_f32_16x16x16_bf16 v[192:195], v[126:127], v[174:175], v[192:195]\n"
      "ds_read_b128 v[150:153], v2 offset:21760\n"
      "s_waitcnt lgkmcnt(2)\n"
      "v_mfma_f32_16x16x16_bf16 v[180:183], v[122:123], v[174:175], v[180:183]\n"
      "v_lshlrev_b64 v[124:125], v78, v[124:125]\n"
      "v_cvt_pk_bf16_f32 v146, v220, v221\n"
      "v_alignbyte_b32 v212, v213, v212, v94\n"
      "v_mfma_f32_16x16x16_bf16 v[156:159], v[92:93], v[218:219], v[156:159]\n"
      "v_alignbyte_b32 v213, v207, v213, v94\n"
      "v_fmac_f32 v129, v119, v119\n"
      "v_alignbyte_b32 v206, v206, v206, v94\n"
      "v_mfma_f32_16x16x16_bf16 v[208:211], v[92:93], v[204:205], v[208:211]\n"
      "ds_read_b128 v[174:177], v2 offset:18688\n"
      "v_cndmask_b32_e64 v124, 0, v124, s[72:73]\n"
      "v_mfma_f32_16x16x16_bf16 v[192:195], v[92:93], v[212:213], v[192:195]\n"
      "v_add_u32 v166, 112160, v86\n"
      "v_cvt_pk_bf16_f32 v147, v222, v223\n"
      "v_cndmask_b32_e64 v125, 0, v125, s[72:73]\n"
      "v_mfma_f32_16x16x16_bf16 v[180:183], v[92:93], v[206:207], v[180:183]\n"
      "ds_read2_b32 v[190:191], v166 offset0:8 offset1:9\n"
      "v_fmac_f32 v128, v118, v118\n"
      "v_mfma_f32_16x16x16_bf16 v[156:159], v[124:125], v[218:219], v[156:159]\n"
      "ds_read_b128 v[218:221], v2 offset:24832\n"
      "v_fmac_f32 v129, v135, v135\n"
      "v_mfma_f32_16x16x16_bf16 v[208:211], v[124:125], v[204:205], v[208:211]\n"
      "ds_read2_b32 v[188:189], v166 offset0:0 offset1:1\n"
      "v_fmac_f32 v128, v134, v134\n"
      "v_mfma_f32_16x16x16_bf16 v[192:195], v[124:125], v[212:213], v[192:195]\n"
      "v_fmac_f32 v128, v136, v136\n"
      "v_fmac_f32 v129, v137, v137\n"
      "v_max3_f32 v213, v156, v157, v158\n"
      "v_mfma_f32_16x16x16_bf16 v[180:183], v[124:125], v[206:207], v[180:183]\n"
      "v_add_f32 v128, v128, v129\n"
      "v_max3_f32 v213, v213, v210, v211\n"
      "s_waitcnt lgkmcnt(3)\n"
      "v_mfma_f32_16x16x32_bf16 v[200:203], a[216:219], v[174:177], 0\n"
      "buffer_store_dwordx4 v[112:115], v25, s[24:27], s46 offen sc1\n"
      "v_mfma_f32_16x16x32_bf16 v[200:203], a[220:223], v[150:153], v[200:203]\n"
      "s_waitcnt lgkmcnt(1)\n"
      "v_alignbyte_b32 v190, v191, v190, v94\n"
      "v_max3_f32 v206, v159, v208, v209\n"
      "v_mfma_f32_16x16x32_bf16 v[200:203], a[224:227], v[218:221], v[200:203]\n"
      "ds_read2_b32 v[168:169], v166 offset0:2 offset1:10\n"
      "v_max3_f32 v213, v213, v194, v195\n"
      "v_mfma_f32_16x16x32_bf16 v[112:115], a[240:243], v[174:177], 0\n"
      "ds_read2_b32 v[154:155], v166 offset0:24 offset1:18\n"
      "v_max3_f32 v206, v206, v192, v193\n"
      "v_mfma_f32_16x16x32_bf16 v[112:115], a[244:247], v[150:153], v[112:115]\n"
      "ds_read_u16 v39, v39\n"
      "s_waitcnt vmcnt(6)\n"
      "v_or3_b32 v108, v108, v109, v110\n"
      "v_mfma_f32_16x16x32_bf16 v[112:115], a[248:251], v[218:221], v[112:115]\n"
      "s_waitcnt lgkmcnt(3)\n"
      "v_max3_f32 v213, v213, v206, v180\n"
      "v_alignbyte_b32 v188, v189, v188, v94\n"
      "v_mfma_f32_16x16x32_bf16 v[204:207], v[232:235], v[174:177], 0\n"
      "ds_read2_b32 v[162:163], v166 offset0:16 offset1:17\n"
      "v_mov_b32 v110, v213\n"
      "v_mfma_f32_16x16x32_bf16 v[204:207], v[236:239], v[150:153], v[204:207]\n"
      "s_nop 0\n"
      "v_permlane16_swap_b32_e64 v213, v110\n"
      "v_mfma_f32_16x16x32_bf16 v[204:207], v[240:243], v[218:221], v[204:207]\n"
      "s_waitcnt lgkmcnt(3)\n"
      "v_max_f32 v213, v213, v110\n"
      "v_alignbyte_b32 v191, v169, v191, v94\n"
      "v_mfma_f32_16x16x32_bf16 v[218:221], v[104:107], v[138:141], v[52:55]\n"
      "v_alignbyte_b32 v189, v168, v189, v94\n"
      "v_mov_b32 v110, v213\n"
      "s_waitcnt vmcnt(5)\n"
      "v_or3_b32 v108, v108, v111, v82\n"
      "v_mfma_f32_16x16x32_bf16 v[174:177], v[170:173], v[138:141], v[56:59]\n"
      "v_permlane32_swap_b32_e64 v213, v110\n"
      "v_mfma_f32_16x16x32_bf16 v[150:153], v[100:103], v[138:141], v[60:63]\n"
      "ds_write_b32 v31, v108 offset:4608\n"
      "v_mfma_f32_16x16x32_bf16 v[166:169], v[184:187], v[138:141], v[26:29]\n"
      "ds_read_u16 v78, v11\n"
      "v_max_f32 v213, v213, v110\n"
      "v_sub_f32_e64 v209, v209, v213\n"
      "s_waitcnt lgkmcnt(2)\n"
      "v_alignbyte_b32 v162, v163, v162, v94\n"
      "v_pk_add_f32 v[156:157], v[156:157], v[212:213] op_sel:[0,1] neg_lo:[0,1] neg_hi:[0,1]\n"
      "v_pk_add_f32 v[194:195], v[194:195], v[212:213] op_sel:[0,1] neg_lo:[0,1] neg_hi:[0,1]\n"
      "v_pk_add_f32 v[158:159], v[158:159], v[212:213] op_sel:[0,1] neg_lo:[0,1] neg_hi:[0,1]\n"
      "v_pk_add_f32 v[210:211], v[210:211], v[212:213] op_sel:[0,1] neg_lo:[0,1] neg_hi:[0,1]\n"
      "v_pk_add_f32 v[192:193], v[192:193], v[212:213] op_sel:[0,1] neg_lo:[0,1] neg_hi:[0,1]\n"
      "v_mfma_f32_16x16x16_bf16 v[218:221], v[178:179], v[148:149], v[218:221]\n"
      "v_exp_f32 v192, v192\n"
      "v_exp_f32 v156, v156\n"
      "v_mfma_f32_16x16x16_bf16 v[174:177], v[164:165], v[148:149], v[174:177]\n"
      "v_exp_f32 v193, v193\n"
      "v_alignbyte_b32 v163, v155, v163, v94\n"
      "v_mfma_f32_16x16x16_bf16 v[150:153], v[126:127], v[148:149], v[150:153]\n"
      "v_cvt_pk_bf16_f32 v88, v192, v193\n"
      "v_exp_f32 v158, v158\n"
      "v_mfma_f32_16x16x16_bf16 v[166:169], v[122:123], v[148:149], v[166:169]\n"
      "v_sub_f32 v180, v180, v213\n"
      "v_exp_f32 v157, v157\n"
      "v_mfma_f32_16x16x16_bf16 v[218:221], v[92:93], v[188:189], v[218:221]\n"
      "v_cvt_pk_bf16_f32 v156, v156, v157\n"
      "v_exp_f32 v210, v210\n"
      "v_mfma_f32_16x16x16_bf16 v[174:177], v[92:93], v[190:191], v[174:177]\n"
      "v_exp_f32 v159, v159\n"
      "v_alignbyte_b32 v154, v154, v154, v94\n"
      "v_mfma_f32_16x16x16_bf16 v[150:153], v[92:93], v[162:163], v[150:153]\n"
      "ds_read_b128 v[138:141], v2 offset:27904\n"
      "v_cvt_pk_bf16_f32 v157, v158, v159\n"
      "v_mfma_f32_16x16x16_bf16 v[166:169], v[92:93], v[154:155], v[166:169]\n"
      "v_exp_f32 v211, v211\n"
      "v_sub_f32 v208, v208, v213\n"
      "v_mfma_f32_16x16x16_bf16 v[218:221], v[124:125], v[188:189], v[218:221]\n"
      "ds_read_b128 v[108:111], v2 offset:30976\n"
      "v_cvt_pk_bf16_f32 v159, v210, v211\n"
      "v_mfma_f32_16x16x16_bf16 v[174:177], v[124:125], v[190:191], v[174:177]\n"
      "ds_read_b128 v[210:213], v2 offset:34048\n"
      "v_exp_f32 v208, v208\n"
      "v_exp_f32 v209, v209\n"
      "v_mfma_f32_16x16x16_bf16 v[150:153], v[124:125], v[162:163], v[150:153]\n"
      "v_cvt_pk_bf16_f32 v158, v208, v209\n"
      "v_exp_f32 v180, v180\n"
      "v_mfma_f32_16x16x16_bf16 v[166:169], v[124:125], v[154:155], v[166:169]\n"
      "v_exp_f32 v194, v194\n"
      "v_exp_f32_e64 v195, v195\n"
      "v_max3_f32 v193, v218, v219, v220\n"
      "v_max3_f32 v163, v221, v174, v175\n"
      "v_max3_f32 v193, v193, v176, v177\n"
      "s_waitcnt lgkmcnt(2)\n"
      "v_mfma_f32_16x16x32_bf16 v[200:203], a[228:231], v[138:141], v[200:203]\n"
      "s_waitcnt lgkmcnt(1)\n"
      "v_cvt_pk_bf16_f32 v89, v194, v195\n"
      "v_max3_f32 v163, v163, v150, v151\n"
      "v_mfma_f32_16x16x32_bf16 v[200:203], a[232:235], v[108:111], v[200:203]\n"
      "s_waitcnt lgkmcnt(0)\n"
      "v_max3_f32 v193, v193, v152, v153\n"
      "v_max3_f32 v193, v193, v163, v166\n"
      "v_mfma_f32_16x16x32_bf16 v[200:203], a[236:239], v[210:213], v[200:203]\n"
      "ds_read_u16 v82, v11 offset:32\n"
      "v_mov_b32 v209, v193\n"
      "v_mfma_f32_16x16x32_bf16 v[112:115], a[252:255], v[138:141], v[112:115]\n"
      "ds_read2_b32 v[168:169], v120 offset0:8 offset1:9\n"
      "v_permlane16_swap_b32_e64 v193, v209\n"
      "v_mfma_f32_16x16x32_bf16 v[112:115], v[224:227], v[108:111], v[112:115]\n"
      "ds_read2_b32 v[148:149], v120 offset0:16 offset1:17\n"
      "v_max_f32 v193, v193, v209\n"
      "v_mfma_f32_16x16x32_bf16 v[112:115], v[228:231], v[210:213], v[112:115]\n"
      "ds_read2_b32 v[162:163], v120 offset0:0 offset1:1\n"
      "v_mov_b32 v209, v193\n"
      "v_mfma_f32_16x16x32_bf16 v[204:207], v[244:247], v[138:141], v[204:207]\n"
      "ds_read_u16 v86, v11 offset:64\n"
      "v_permlane32_swap_b32 v193, v209\n"
      "v_cvt_pk_bf16_f32 v90, v180, 0\n"
      "v_max_f32 v193, v193, v209\n"
      "v_pk_add_f32 v[220:221], v[220:221], v[192:193] op_sel:[0,1] neg_lo:[0,1] neg_hi:[0,1]\n"
      "v_pk_add_f32 v[174:175], v[174:175], v[192:193] op_sel:[0,1] neg_lo:[0,1] neg_hi:[0,1]\n"
      "v_pk_add_f32 v[218:219], v[218:219], v[192:193] op_sel:[0,1] neg_lo:[0,1] neg_hi:[0,1]\n"
      "v_mfma_f32_16x16x32_bf16 v[204:207], v[248:251], v[108:111], v[204:207]\n"
      "v_exp_f32 v219, v219\n"
      "v_exp_f32 v218, v218\n"
      "v_mfma_f32_16x16x32_bf16 v[204:207], v[252:255], v[210:213], v[204:207]\n"
      "ds_read2_b32 v[108:109], v120 offset0:2 offset1:10\n"
      "v_sub_f32 v166, v166, v193\n"
      "v_mfma_f32_16x16x32_bf16 v[208:211], v[32:35], v[156:159], 0\n"
      "v_exp_f32 v221, v221\n"
      "v_mfma_f32_16x16x32_bf16 v[208:211], v[32:35], v[88:91], v[208:211]\n"
      "s_waitcnt lgkmcnt(1)\n"
      "v_exp_f32 v220, v220\n"
      "v_alignbyte_b32 v162, v163, v162, v94\n"
      "v_alignbyte_b32 v148, v149, v148, v94\n"
      "v_cvt_pk_bf16_f32 v218, v218, v219\n"
      "v_pk_add_f32 v[176:177], v[176:177], v[192:193] op_sel:[0,1] neg_lo:[0,1] neg_hi:[0,1]\n"
      "v_pk_add_f32 v[150:151], v[150:151], v[192:193] op_sel:[0,1] neg_lo:[0,1] neg_hi:[0,1]\n"
      "v_pk_add_f32 v[152:153], v[152:153], v[192:193] op_sel:[0,1] neg_lo:[0,1] neg_hi:[0,1]\n"
      "v_mfma_f32_16x16x32_bf16 v[210:213], v[130:133], v[156:159], 0\n"
      "v_exp_f32_e64 v177, v177\n"
      "v_mfma_f32_16x16x32_bf16 v[210:213], v[76:79], v[88:91], v[210:213]\n"
      "ds_read2_b32 v[180:181], v120 offset0:24 offset1:18\n"
      "v_exp_f32_e64 v176, v176\n"
      "v_mfma_f32_16x16x32_bf16 v[192:195], v[196:199], v[156:159], 0\n"
      "v_cvt_pk_bf16_f32 v219, v220, v221\n"
      "v_cvt_pk_bf16_f32 v221, v176, v177\n"
      "v_mfma_f32_16x16x32_bf16 v[192:195], v[80:83], v[88:91], v[192:195]\n"
      "v_exp_f32 v175, v175\n"
      "v_mfma_f32_16x16x32_bf16 v[138:141], v[214:217], v[156:159], 0\n"
      "s_waitcnt lgkmcnt(1)\n"
      "v_exp_f32_e64 v174, v174\n"
      "v_mfma_f32_16x16x32_bf16 v[138:141], v[84:87], v[88:91], v[138:141]\n"
      "v_alignbyte_b32 v163, v108, v163, v94\n"
      "v_alignbyte_b32 v168, v169, v168, v94\n"
      "v_mfma_f32_16x16x32_bf16 v[188:191], v[104:107], v[144:147], v[64:67]\n"
      "v_alignbyte_b32 v169, v109, v169, v94\n"
      "v_cvt_pk_bf16_f32 v220, v174, v175\n"
      "v_mfma_f32_16x16x32_bf16 v[174:177], v[170:173], v[144:147], v[68:71]\n"
      "s_waitcnt lgkmcnt(0)\n"
      "v_alignbyte_b32 v149, v181, v149, v94\n"
      "v_alignbyte_b32 v180, v180, v180, v94\n"
      "v_mfma_f32_16x16x32_bf16 v[108:111], v[100:103], v[144:147], v[72:75]\n"
      "v_rcp_f32 v208, v208\n"
      "v_mfma_f32_16x16x32_bf16 v[154:157], v[184:187], v[144:147], v[28:31]\n"
      "v_mul_f32 v212, v208, v212\n"
      "v_lshlrev_b32 v39, 16, v39\n"
      "v_mfma_f32_16x16x16_bf16 v[188:191], v[178:179], v[160:161], v[188:191]\n"
      "v_mul_f32 v210, v208, v210\n"
      "v_exp_f32 v150, v150\n"
      "v_mfma_f32_16x16x16_bf16 v[174:177], v[164:165], v[160:161], v[174:177]\n"
      "v_exp_f32 v166, v166\n"
      "v_mul_f32 v193, v208, v193\n"
      "v_mfma_f32_16x16x16_bf16 v[108:111], v[126:127], v[160:161], v[108:111]\n"
      "v_mul_f32 v211, v208, v211\n"
      "v_mul_f32 v195, v208, v195\n"
      "v_mul_f32 v213, v208, v213\n"
      "v_mfma_f32_16x16x16_bf16 v[154:157], v[122:123], v[160:161], v[154:157]\n"
      "v_mul_f32 v194, v208, v194\n"
      "v_cvt_pk_bf16_f32 v210, v210, v211\n"
      "v_mul_f32_e64 v138, v208, v138\n"
      "v_mfma_f32_16x16x16_bf16 v[188:191], v[92:93], v[162:163], v[188:191]\n"
      "v_cvt_pk_bf16_f32 v211, v212, v213\n"
      "v_exp_f32_e64 v152, v152\n"
      "v_mfma_f32_16x16x16_bf16 v[174:177], v[92:93], v[168:169], v[174:177]\n"
      "v_cvt_pk_bf16_f32 v213, v194, v195\n"
      "v_exp_f32 v153, v153\n"
      "v_mfma_f32_16x16x16_bf16 v[108:111], v[92:93], v[148:149], v[108:111]\n"
      "v_exp_f32 v151, v151\n"
      "v_fma_f32 v182, v200, v200, s52\n"
      "v_mfma_f32_16x16x16_bf16 v[154:157], v[92:93], v[180:181], v[154:157]\n"
      "v_mul_f32_e64 v192, v208, v192\n"
      "v_cvt_pk_bf16_f32 v93, v152, v153\n"
      "v_cvt_pk_bf16_f32 v92, v150, v151\n"
      "v_mfma_f32_16x16x16_bf16 v[188:191], v[124:125], v[162:163], v[188:191]\n"
      "ds_read_b128 v[150:153], v2 offset:25088\n"
      "v_cvt_pk_bf16_f32 v212, v192, v193\n"
      "v_mfma_f32_16x16x16_bf16 v[174:177], v[124:125], v[168:169], v[174:177]\n"
      "ds_read_b128 v[192:195], v2 offset:18944\n"
      "v_cvt_pk_bf16_f32 v94, v166, 0\n"
      "v_mfma_f32_16x16x16_bf16 v[108:111], v[124:125], v[148:149], v[108:111]\n"
      "ds_read_b128 v[166:169], v2 offset:22016\n"
      "v_mul_f32 v139, v208, v139\n"
      "v_mfma_f32_16x16x16_bf16 v[154:157], v[124:125], v[180:181], v[154:157]\n"
      "v_max3_f32 v209, v188, v189, v190\n"
      "v_cvt_pk_bf16_f32 v138, v138, v139\n"
      "v_mul_f32 v183, v201, v201\n"
      "v_max3_f32 v209, v209, v176, v177\n"
      "v_max3_f32 v149, v191, v174, v175\n"
      "v_max3_f32 v149, v149, v108, v109\n"
      "v_max3_f32 v209, v209, v110, v111\n"
      "v_pk_mul_f32 v[140:141], v[140:141], v[208:209] op_sel_hi:[1,0]\n"
      "v_cvt_pk_bf16_f32 v139, v140, v141\n"
      "v_max3_f32 v209, v209, v149, v154\n"
      "v_mov_b32_e64 v156, v209\n"
      "v_pk_fma_f32 v[182:183], v[202:203], v[202:203], v[182:183]\n"
      "v_pk_fma_f32 v[182:183], v[112:113], v[112:113], v[182:183]\n"
      "v_permlane16_swap_b32_e64 v209, v156\n"
      "v_max_f32_e64 v209, v209, v156\n"
      "v_mov_b32_e64 v156, v209\n"
      "v_pk_fma_f32 v[182:183], v[114:115], v[114:115], v[182:183]\n"
      "v_pk_fma_f32 v[182:183], v[204:205], v[204:205], v[182:183]\n"
      "v_permlane32_swap_b32_e64 v209, v156\n"
      "v_max_f32 v209, v209, v156\n"
      "v_pk_add_f32 v[176:177], v[176:177], v[208:209] op_sel:[0,1] neg_lo:[0,1] neg_hi:[0,1]\n"
      "v_pk_add_f32 v[110:111], v[110:111], v[208:209] op_sel:[0,1] neg_lo:[0,1] neg_hi:[0,1]\n"
      "v_pk_add_f32 v[174:175], v[174:175], v[208:209] op_sel:[0,1] neg_lo:[0,1] neg_hi:[0,1]\n"
      "s_waitcnt lgkmcnt(0)\n"
      "v_mfma_f32_16x16x32_bf16 v[156:159], a[216:219], v[192:195], 0\n"
      "ds_write_b128 v3, v[210:213]\n"
      "v_pk_fma_f32 v[182:183], v[206:207], v[206:207], v[182:183]\n"
      "v_pk_add_f32 v[190:191], v[190:191], v[208:209] op_sel:[0,1] neg_lo:[0,1] neg_hi:[0,1]\n"
      "v_pk_add_f32 v[108:109], v[108:109], v[208:209] op_sel:[0,1] neg_lo:[0,1] neg_hi:[0,1]\n"
      "v_pk_add_f32 v[188:189], v[188:189], v[208:209] op_sel:[0,1] neg_lo:[0,1] neg_hi:[0,1]\n"
      "v_mfma_f32_16x16x32_bf16 v[156:159], a[220:223], v[166:169], v[156:159]\n"
      "v_add_f32 v182, v182, v183\n"
      "v_exp_f32 v191, v191\n"
      "v_mfma_f32_16x16x32_bf16 v[156:159], a[224:227], v[150:153], v[156:159]\n"
      "v_exp_f32 v189, v189\n"
      "v_mfma_f32_16x16x32_bf16 v[146:149], a[240:243], v[192:195], 0\n"
      "v_exp_f32 v188, v188\n"
      "v_mfma_f32_16x16x32_bf16 v[146:149], a[244:247], v[166:169], v[146:149]\n"
      "v_exp_f32 v177, v177\n"
      "v_mfma_f32_16x16x32_bf16 v[146:149], a[248:251], v[150:153], v[146:149]\n"
      "ds_write_b64 v4, v[138:139]\n"
      "v_mfma_f32_16x16x32_bf16 v[160:163], v[232:235], v[192:195], 0\n"
      "v_exp_f32 v190, v190\n"
      "v_mfma_f32_16x16x32_bf16 v[160:163], v[236:239], v[166:169], v[160:163]\n"
      "ds_read_b64 v[180:181], v10 offset:16\n"
      "v_sub_f32 v154, v154, v209\n"
      "v_mfma_f32_16x16x32_bf16 v[160:163], v[240:243], v[150:153], v[160:163]\n"
      "s_cmp_gt_u32 s42, s44\n"
      "s_cselect_b32 s95, s94, s95\n"
      "v_exp_f32 v176, v176\n"
      "v_mfma_f32_16x16x32_bf16 v[208:211], v[32:35], v[218:221], 0\n"
      "v_exp_f32_e64 v111, v111\n"
      "v_mfma_f32_16x16x32_bf16 v[208:211], v[32:35], v[92:95], v[208:211]\n"
      "ds_read_b128 v[150:153], v10\n"
      "v_cvt_pk_bf16_f32 v188, v188, v189\n"
      "v_mfma_f32_16x16x32_bf16 v[192:195], v[130:133], v[218:221], 0\n"
      "v_exp_f32_e64 v175, v175\n"
      "v_mfma_f32_16x16x32_bf16 v[192:195], v[76:79], v[92:95], v[192:195]\n"
      "v_exp_f32_e64 v174, v174\n"
      "v_mfma_f32_16x16x32_bf16 v[166:169], v[196:199], v[218:221], 0\n"
      "v_cvt_pk_bf16_f32 v189, v190, v191\n"
      "v_cvt_pk_bf16_f32 v191, v176, v177\n"
      "v_mfma_f32_16x16x32_bf16 v[166:169], v[80:83], v[92:95], v[166:169]\n"
      "v_exp_f32 v109, v109\n"
      "v_mfma_f32_16x16x32_bf16 v[210:213], v[214:217], v[218:221], 0\n"
      "v_exp_f32 v108, v108\n"
      "v_mfma_f32_16x16x32_bf16 v[210:213], v[84:87], v[92:95], v[210:213]\n"
      "s_waitcnt lgkmcnt(0)\n"
      "v_rcp_f32 v208, v208\n"
      "v_mfma_f32_16x16x32_bf16 v[218:221], v[150:153], v[104:107], v[30:33]\n"
      "v_cvt_pk_bf16_f32 v190, v174, v175\n"
      "v_cvt_pk_bf16_f32 v88, v108, v109\n"
      "v_mfma_f32_16x16x32_bf16 v[174:177], v[150:153], v[170:173], v[30:33]\n"
      "ds_read_b128 v[170:173], v2 offset:28160\n"
      "v_mul_f32_e64 v194, v208, v194\n"
      "v_mfma_f32_16x16x32_bf16 v[138:141], v[150:153], v[100:103], v[30:33]\n"
      "ds_read_b128 v[100:103], v2 offset:31232\n"
      "v_mul_f32_e64 v169, v208, v169\n"
      "v_mfma_f32_16x16x32_bf16 v[106:109], v[150:153], v[184:187], v[30:33]\n"
      "ds_read_b128 v[150:153], v2 offset:34304\n"
      "v_mfma_f32_16x16x16_bf16 v[218:221], v[180:181], v[178:179], v[218:221]\n"
      "v_exp_f32 v154, v154\n"
      "v_mul_f32 v167, v208, v167\n"
      "v_mfma_f32_16x16x16_bf16 v[174:177], v[180:181], v[164:165], v[174:177]\n"
      "v_mul_f32 v192, v208, v192\n"
      "v_exp_f32 v110, v110\n"
      "v_mfma_f32_16x16x16_bf16 v[138:141], v[180:181], v[126:127], v[138:141]\n"
      "v_mul_f32_e64 v193, v208, v193\n"
      "v_mul_f32_e64 v195, v208, v195\n"
      "v_mul_f32 v166, v208, v166\n"
      "v_mfma_f32_16x16x16_bf16 v[106:109], v[180:181], v[122:123], v[106:109]\n"
      "v_permlane16_swap_b32_e64 v128, v182\n"
      "s_waitcnt lgkmcnt(2)\n"
      "v_mfma_f32_16x16x32_bf16 v[156:159], a[228:231], v[170:173], v[156:159]\n"
      "s_waitcnt lgkmcnt(1)\n"
      "v_cndmask_b32 v218, v218, v174, s[58:59]\n"
      "v_cvt_pk_bf16_f32 v89, v110, v111\n"
      "v_mfma_f32_16x16x32_bf16 v[156:159], a[232:235], v[100:103], v[156:159]\n"
      "s_waitcnt lgkmcnt(0)\n"
      "v_cvt_pk_bf16_f32 v192, v192, v193\n"
      "v_cndmask_b32 v218, v218, v138, s[60:61]\n"
      "v_mfma_f32_16x16x32_bf16 v[156:159], a[236:239], v[150:153], v[156:159]\n"
      "v_cndmask_b32 v218, v218, v106, s[62:63]\n"
      "v_fmac_f32 v218, s53, v39\n"
      "v_mfma_f32_16x16x32_bf16 v[146:149], a[252:255], v[170:173], v[146:149]\n"
      "s_nop 0\n"
      "v_max_f32_dpp v209, v218, v218 row_ror:8 row_mask:0xf bank_mask:0xf\n"
      "v_cvt_pk_bf16_f32 v90, v154, 0\n"
      "v_mfma_f32_16x16x32_bf16 v[146:149], v[224:227], v[100:103], v[146:149]\n"
      "v_max_f32_dpp v209, v209, v209 row_ror:4 row_mask:0xf bank_mask:0xf\n"
      "v_fma_f32 v164, v156, v156, s52\n"
      "v_mfma_f32_16x16x32_bf16 v[146:149], v[228:231], v[150:153], v[146:149]\n"
      "s_add_u32 s42, s42, 1\n"
      "v_max_f32_dpp v209, v209, v209 row_ror:2 row_mask:0xf bank_mask:0xf\n"
      "v_add_f32 v128, v128, v182\n"
      "v_mfma_f32_16x16x32_bf16 v[160:163], v[244:247], v[170:173], v[160:163]\n"
      "v_max_f32_dpp v209, v209, v209 row_ror:1 row_mask:0xf bank_mask:0xf\n"
      "v_cvt_pk_bf16_f32 v193, v194, v195\n"
      "v_mfma_f32_16x16x32_bf16 v[160:163], v[248:251], v[100:103], v[160:163]\n"
      "v_max_f32_dpp v209, v209, v209 row_bcast:15 row_mask:0xa bank_mask:0xf\n"
      "v_mul_f32_e64 v165, v157, v157\n"
      "v_mfma_f32_16x16x32_bf16 v[160:163], v[252:255], v[150:153], v[160:163]\n"
      "v_max_f32_dpp v209, v209, v209 row_bcast:31 row_mask:0xc bank_mask:0xf\n"
      "v_mul_f32_e64 v168, v208, v168\n"
      "v_mfma_f32_16x16x32_bf16 v[150:153], v[32:35], v[188:191], 0\n"
      "v_cvt_pk_bf16_f32 v195, v168, v169\n"
      "v_cvt_pk_bf16_f32 v194, v166, v167\n"
      "v_mfma_f32_16x16x32_bf16 v[150:153], v[32:35], v[88:91], v[150:153]\n"
      "ds_write_b128 v3, v[192:195] offset:256\n"
      "v_pk_mul_f32 v[210:211], v[210:211], v[208:209] op_sel_hi:[1,0]\n"
      "v_pk_fma_f32 v[164:165], v[158:159], v[158:159], v[164:165]\n"
      "v_pk_mul_f32 v[212:213], v[212:213], v[208:209] op_sel_hi:[1,0]\n"
      "v_pk_fma_f32 v[164:165], v[146:147], v[146:147], v[164:165]\n"
      "v_mfma_f32_16x16x32_bf16 v[174:177], v[130:133], v[188:191], 0\n"
      "v_cvt_pk_bf16_f32 v210, v210, v211\n"
      "v_readlane_b32 s72, v209, 63\n"
      "v_mfma_f32_16x16x32_bf16 v[174:177], v[76:79], v[88:91], v[174:177]\n"
      "s_nop 0\n"
      "v_subrev_f32 v218, s72, v218\n"
      "v_cvt_pk_bf16_f32 v211, v212, v213\n"
      "v_mfma_f32_16x16x32_bf16 v[168:171], v[196:199], v[188:191], 0\n"
      "v_exp_f32_e64 v218, v218\n"
      "v_mfma_f32_16x16x32_bf16 v[168:171], v[80:83], v[88:91], v[168:171]\n"
      "ds_write_b64 v4, v[210:211] offset:256\n"
      "v_rcp_f32_e64 v150, v150\n"
      "v_pk_fma_f32 v[164:165], v[148:149], v[148:149], v[164:165]\n"
      "v_pk_mul_f32 v[174:175], v[174:175], v[150:151] op_sel_hi:[1,0]\n"
      "v_pk_fma_f32 v[164:165], v[160:161], v[160:161], v[164:165]\n"
      "v_cvt_pk_bf16_f32 v218, v218, v218\n"
      "v_pk_mul_f32 v[176:177], v[176:177], v[150:151] op_sel_hi:[1,0]\n"
      "v_pk_fma_f32 v[164:165], v[162:163], v[162:163], v[164:165]\n"
      "v_pk_mul_f32 v[168:169], v[168:169], v[150:151] op_sel_hi:[1,0]\n"
      "v_add_f32 v164, v164, v165\n"
      "v_cvt_pk_bf16_f32 v174, v174, v175\n"
      "v_mov_b32 v182, v164\n"
      "v_mfma_f32_16x16x32_bf16 v[208:211], v[214:217], v[188:191], 0\n"
      "ds_write_b16 v13, v218\n"
      "v_cvt_pk_bf16_f32 v175, v176, v177\n"
      "v_permlane16_swap_b32_e64 v164, v182\n"
      "v_pk_mul_f32 v[170:171], v[170:171], v[150:151] op_sel_hi:[1,0]\n"
      "v_cvt_pk_bf16_f32 v177, v170, v171\n"
      "v_add_f32_e64 v164, v164, v182\n"
      "v_cvt_pk_bf16_f32 v176, v168, v169\n"
      "v_mfma_f32_16x16x32_bf16 v[208:211], v[84:87], v[88:91], v[208:211]\n"
      "ds_write_b128 v3, v[174:177] offset:512\n"
      "ds_read_b128 v[218:221], v14\n"
      "ds_read_b128 v[174:177], v14 offset:64\n"
      "v_permlane32_swap_b32 v128, v164\n"
      "v_add_f32_e64 v128, v128, v164\n"
      "v_readlane_b32 s86, v38, s43\n"
      "s_add_u32 s43, s43, 1\n"
      "ds_write_b32 v31, v128\n"
      "v_pk_mul_f32 v[208:209], v[208:209], v[150:151] op_sel_hi:[1,0]\n"
      "v_cvt_pk_bf16_f32 v208, v208, v209\n"
      "v_pk_mul_f32 v[210:211], v[210:211], v[150:151] op_sel_hi:[1,0]\n"
      "v_cvt_pk_bf16_f32 v209, v210, v211\n"
      "s_and_b32 s86, s86, -4\n"
      "ds_write_b64 v4, v[208:209] offset:512\n"
      "v_add_u32_e64 v12, 32, v12\n"
      "s_cmp_ge_u32 s95, 0x40000000\n"
      "s_cselect_b64 vcc, -1, s[56:57]\n"
      "s_waitcnt lgkmcnt(3)\n"
      "v_mfma_f32_16x16x32_bf16 v[208:211], v[218:221], v[32:35], 0\n"
      "s_waitcnt lgkmcnt(2)\n"
      "v_mfma_f32_16x16x32_bf16 v[208:211], v[174:177], v[32:35], v[208:211]\n"
      "s_waitcnt lgkmcnt(0)\n"
      "s_barrier\n"
      "s_cbranch_vccz L_sel1_%=\n"
      "s_branch L_disp1_%=\n"
      "L_tail1_%=:\n"
      "s_nop 4\n"
      "ds_read_b128 v[164:167], v9\n"
      "v_mfma_f32_16x16x32_bf16 v[168:171], v[218:221], v[130:133], 0\n"
      "ds_read_b128 v[130:133], v2 offset:12288\n"
      "ds_read_b128 v[186:189], v2 offset:15616\n"
      "v_mfma_f32_16x16x32_bf16 v[168:171], v[174:177], v[76:79], v[168:171]\n"
      "ds_read_b128 v[104:107], v2 offset:3328\n"
      "v_mfma_f32_16x16x32_bf16 v[150:153], v[218:221], v[196:199], 0\n"
      "ds_read_b128 v[178:181], v2 offset:9216\n"
      "ds_read_b128 v[138:141], v2 offset:3072\n"
      "ds_read_b128 v[190:193], v2 offset:6144\n"
      "v_subrev_u32 v76, 115456, v9\n"
      "v_mfma_f32_16x16x32_bf16 v[150:153], v[174:177], v[80:83], v[150:153]\n"
      "ds_read_b128 v[108:111], v2\n"
      "v_subrev_u32 v171, 115456, v9\n"
      "v_mfma_f32_16x16x32_bf16 v[194:197], v[218:221], v[214:217], 0\n"
      "s_waitcnt lgkmcnt(7)\n"
      "v_add_f32 v164, v164, v165\n"
      "v_add_f32 v166, v166, v167\n"
      "v_add_f32 v164, v164, v166\n"
      "v_rsq_f32 v164, v164\n"
      "s_nop 0\n"
      "v_mul_f32 v164, 0x415db3d7, v164\n"
      "v_mov_b32 v166, v164\n"
      "v_cndmask_b32 v168, v168, v150, s[58:59]\n"
      "v_and_b32 v217, 0x300, v76\n"
      "v_permlane32_swap_b32 v164, v166\n"
      "v_mov_b32_e64 v165, v164\n"
      "v_pk_mul_f32 v[156:157], v[156:157], v[166:167] op_sel_hi:[1,0]\n"
      "v_pk_mul_f32 v[146:147], v[146:147], v[166:167] op_sel_hi:[1,0]\n"
      "v_permlane16_swap_b32_e64 v164, v165\n"
      "v_pk_mul_f32 v[136:137], v[136:137], v[164:165] op_sel_hi:[1,0]\n"
      "v_pk_mul_f32 v[114:115], v[114:115], v[164:165] op_sel:[0,1]\n"
      "v_pk_mul_f32 v[98:99], v[98:99], v[164:165] op_sel_hi:[1,0]\n"
      "v_pk_mul_f32 v[200:201], v[200:201], v[164:165] op_sel:[0,1]\n"
      "v_pk_mul_f32 v[148:149], v[148:149], v[166:167] op_sel_hi:[1,0]\n"
      "v_pk_mul_f32 v[202:203], v[202:203], v[164:165] op_sel:[0,1]\n"
      "v_pk_mul_f32 v[206:207], v[206:207], v[164:165] op_sel:[0,1]\n"
      "v_pk_mul_f32 v[160:161], v[160:161], v[166:167] op_sel_hi:[1,0]\n"
      "v_pk_mul_f32 v[96:97], v[96:97], v[164:165] op_sel_hi:[1,0]\n"
      "v_pk_mul_f32 v[204:205], v[204:205], v[164:165] op_sel:[0,1]\n"
      "v_pk_mul_f32 v[162:163], v[162:163], v[166:167] op_sel_hi:[1,0]\n"
      "v_cvt_pk_bf16_f32 v156, v156, v157\n"
      "v_pk_mul_f32 v[158:159], v[158:159], v[166:167] op_sel_hi:[1,0]\n"
      "v_pk_mul_f32 v[134:135], v[134:135], v[164:165] op_sel_hi:[1,0]\n"
      "v_cvt_pk_bf16_f32 v200, v200, v201\n"
      "v_cvt_pk_bf16_f32 v204, v204, v205\n"
      "v_cvt_pk_bf16_f32 v160, v160, v161\n"
      "v_cvt_pk_bf16_f32 v157, v158, v159\n"
      "v_cvt_pk_bf16_f32 v201, v202, v203\n"
      "v_pk_mul_f32 v[112:113], v[112:113], v[164:165] op_sel:[0,1]\n"
      "v_cvt_pk_bf16_f32 v134, v134, v135\n"
      "v_cvt_pk_bf16_f32 v203, v114, v115\n"
      "v_pk_mul_f32 v[116:117], v[116:117], v[164:165] op_sel_hi:[1,0]\n"
      "v_cvt_pk_bf16_f32 v159, v148, v149\n"
      "v_cvt_pk_bf16_f32 v161, v162, v163\n"
      "v_cvt_pk_bf16_f32 v96, v96, v97\n"
      "v_cvt_pk_bf16_f32 v97, v98, v99\n"
      "v_cvt_pk_bf16_f32 v158, v146, v147\n"
      "v_pk_mul_f32 v[118:119], v[118:119], v[164:165] op_sel_hi:[1,0]\n"
      "v_cvt_pk_bf16_f32 v135, v136, v137\n"
      "v_cvt_pk_bf16_f32 v205, v206, v207\n"
      "v_cvt_pk_bf16_f32 v202, v112, v113\n"
      "v_cvt_pk_bf16_f32 v98, v116, v117\n"
      "v_cvt_pk_bf16_f32 v99, v118, v119\n"
      "v_mfma_f32_16x16x32_bf16 v[194:197], v[174:177], v[84:87], v[194:197]\n"
      "ds_write_b128 v5, v[156:159] offset:30720\n"
      "ds_write_b64 v6, v[134:135] offset:18432\n"
      "ds_write_b128 v5, v[200:203] offset:24576\n"
      "ds_write_b128 v5, v[96:99] offset:18432\n"
      "ds_write_b64 v6, v[204:205] offset:24576\n"
      "ds_write_b64 v6, v[160:161] offset:30720\n"
      "s_waitcnt lgkmcnt(5)\n"
      "v_mfma_f32_16x16x32_bf16 v[96:99], a[216:219], v[108:111], 0\n"
      "ds_read_b128 v[200:203], v2 offset:15360\n"
      "v_subrev_u32 v86, 73728, v6\n"
      "v_mfma_f32_16x16x32_bf16 v[96:99], a[220:223], v[138:141], v[96:99]\n"
      "ds_read_b128 v[156:159], v2 offset:9472\n"
      "v_mfma_f32_16x16x32_bf16 v[116:119], a[240:243], v[108:111], 0\n"
      "ds_read_b128 v[146:149], v2 offset:256\n"
      "v_add_u32 v76, v76, v217\n"
      "v_mfma_f32_16x16x32_bf16 v[116:119], a[244:247], v[138:141], v[116:119]\n"
      "ds_read_b128 v[160:163], v7 offset:8192\n"
      "s_mul_i32 s92, s40, 2048\n"
      "v_mfma_f32_16x16x32_bf16 v[134:137], v[232:235], v[108:111], 0\n"
      "ds_read_b128 v[174:177], v7 offset:16384\n"
      "v_mfma_f32_16x16x32_bf16 v[134:137], v[236:239], v[138:141], v[134:137]\n"
      "v_mfma_f32_16x16x32_bf16 v[96:99], a[224:227], v[190:193], v[96:99]\n"
      "ds_read_b128 v[214:217], v7\n"
      "v_add_u32 v85, s92, v171\n"
      "v_mfma_f32_16x16x32_bf16 v[96:99], a[228:231], v[178:181], v[96:99]\n"
      "v_lshrrev_b32 v171, 4, v85\n"
      "v_mfma_f32_16x16x32_bf16 v[116:119], a[248:251], v[190:193], v[116:119]\n"
      "ds_read_b128 v[196:199], v2 offset:15872\n"
      "v_mfma_f32_16x16x32_bf16 v[116:119], a[252:255], v[178:181], v[116:119]\n"
      "v_mov_b32_dpp v208, v208 quad_perm:[0,0,0,0] row_mask:0xf bank_mask:0xf\n"
      "v_add_u32_e64 v84, 0, v171\n"
      "v_mfma_f32_16x16x32_bf16 v[134:137], v[240:243], v[190:193], v[134:137]\n"
      "v_ashrrev_i32 v190, 31, v37\n"
      "v_cndmask_b32 v168, v168, v194, s[60:61]\n"
      "v_mfma_f32_16x16x32_bf16 v[134:137], v[244:247], v[178:181], v[134:137]\n"
      "ds_read_b128 v[218:221], v2 offset:6656\n"
      "v_mfma_f32_16x16x32_bf16 v[96:99], a[232:235], v[130:133], v[96:99]\n"
      "s_waitcnt lgkmcnt(6)\n"
      "v_and_b32 v191, 0x3fffffff, v37\n"
      "v_mfma_f32_16x16x32_bf16 v[96:99], a[236:239], v[200:203], v[96:99]\n"
      "ds_read_b128 v[150:153], v2 offset:12800\n"
      "v_mfma_f32_16x16x32_bf16 v[116:119], v[224:227], v[130:133], v[116:119]\n"
      "v_mfma_f32_16x16x32_bf16 v[116:119], v[228:231], v[200:203], v[116:119]\n"
      "ds_read_b128 v[210:213], v2 offset:6400\n"
      "v_add_u32 v191, 1179648, v191\n"
      "v_mfma_f32_16x16x32_bf16 v[134:137], v[248:251], v[130:133], v[134:137]\n"
      "ds_read_b128 v[130:133], v2 offset:3584\n"
      "v_add_u32 v76, 149504, v76\n"
      "v_mfma_f32_16x16x32_bf16 v[134:137], v[252:255], v[200:203], v[134:137]\n"
      "s_waitcnt lgkmcnt(4)\n"
      "v_add_u32 v181, 64, v171\n"
      "v_mfma_f32_16x16x32_bf16 v[200:203], a[216:219], v[146:149], 0\n"
      "ds_read_b128 v[192:195], v2 offset:12544\n"
      "v_mul_u32_u24 v180, 2731, v84\n"
      "v_mfma_f32_16x16x32_bf16 v[200:203], a[220:223], v[104:107], v[200:203]\n"
      "ds_read_b128 v[170:173], v7 offset:4096\n"
      "v_mul_u32_u24 v178, 2731, v181\n"
      "v_mfma_f32_16x16x32_bf16 v[112:115], a[240:243], v[146:149], 0\n"
      "v_rcp_f32_e64 v208, v208\n"
      "v_mfma_f32_16x16x32_bf16 v[112:115], a[244:247], v[104:107], v[112:115]\n"
      "ds_read_b128 v[164:167], v2 offset:512\n"
      "v_mfma_f32_16x16x32_bf16 v[204:207], v[232:235], v[146:149], 0\n"
      "ds_read_b128 v[108:111], v2 offset:9728\n"
      "v_mfma_f32_16x16x32_bf16 v[204:207], v[236:239], v[104:107], v[204:207]\n"
      "s_waitcnt lgkmcnt(4)\n"
      "v_fma_f32 v140, v96, v96, s52\n"
      "v_mul_f32 v141, v97, v97\n"
      "v_mfma_f32_16x16x32_bf16 v[200:203], a[224:227], v[210:213], v[200:203]\n"
      "v_fmac_f32 v141, v99, v99\n"
      "v_fmac_f32 v140, v98, v98\n"
      "v_mfma_f32_16x16x32_bf16 v[200:203], a[228:231], v[156:159], v[200:203]\n"
      "buffer_store_dwordx4 v[214:217], v20, s[24:27], s85 offen sc1\n"
      "v_mfma_f32_16x16x32_bf16 v[112:115], a[248:251], v[210:213], v[112:115]\n"
      "s_nop 0\n"
      "ds_read_b128 v[214:217], v76\n"
      "v_lshrrev_b32 v178, 16, v178\n"
      "v_mfma_f32_16x16x32_bf16 v[112:115], a[252:255], v[156:159], v[112:115]\n"
      "v_lshrrev_b32 v180, 16, v180\n"
      "v_fmac_f32 v140, v116, v116\n"
      "v_mfma_f32_16x16x32_bf16 v[204:207], v[240:243], v[210:213], v[204:207]\n"
      "s_waitcnt lgkmcnt(1)\n"
      "v_fmac_f32 v141, v117, v117\n"
      "v_fmac_f32_e64 v140, v118, v118\n"
      "v_mfma_f32_16x16x32_bf16 v[204:207], v[244:247], v[156:159], v[204:207]\n"
      "buffer_store_dwordx4 v[170:173], v21, s[24:27], s85 offen sc1\n"
      "v_mfma_f32_16x16x32_bf16 v[200:203], a[232:235], v[192:195], v[200:203]\n"
      "v_and_b32 v190, 100270464, v190\n"
      "v_fmac_f32 v141, v119, v119\n"
      "v_mfma_f32_16x16x32_bf16 v[200:203], a[236:239], v[186:189], v[200:203]\n"
      "buffer_store_dwordx4 v[160:163], v22, s[24:27], s85 offen sc1\n"
      "v_mfma_f32_16x16x32_bf16 v[112:115], v[224:227], v[192:195], v[112:115]\n"
      "v_fmac_f32 v141, v135, v135\n"
      "v_mfma_f32_16x16x32_bf16 v[112:115], v[228:231], v[186:189], v[112:115]\n"
      "ds_read_b128 v[210:213], v7 offset:12288\n"
      "v_fmac_f32 v140, v134, v134\n"
      "v_mfma_f32_16x16x32_bf16 v[204:207], v[248:251], v[192:195], v[204:207]\n"
      "s_nop 0\n"
      "v_fma_f32 v138, v200, v200, s52\n"
      "v_fmac_f32 v140, v136, v136\n"
      "v_mfma_f32_16x16x32_bf16 v[204:207], v[252:255], v[186:189], v[204:207]\n"
      "v_mul_f32 v139, v201, v201\n"
      "v_mfma_f32_16x16x32_bf16 v[156:159], a[216:219], v[164:167], 0\n"
      "v_fmac_f32 v141, v137, v137\n"
      "v_mul_u32_u24 v179, 24, v178\n"
      "v_mfma_f32_16x16x32_bf16 v[156:159], a[220:223], v[130:133], v[156:159]\n"
      "ds_read_b128 v[192:195], v76 offset:6144\n"
      "v_fmac_f32_e64 v138, v202, v202\n"
      "v_mfma_f32_16x16x32_bf16 v[146:149], a[240:243], v[164:167], 0\n"
      "ds_read_b128 v[170:173], v76 offset:2048\n"
      "v_fmac_f32 v139, v203, v203\n"
      "v_mfma_f32_16x16x32_bf16 v[146:149], a[244:247], v[130:133], v[146:149]\n"
      "v_add_f32 v140, v140, v141\n"
      "v_mfma_f32_16x16x32_bf16 v[160:163], v[232:235], v[164:167], 0\n"
      "v_fmac_f32 v138, v112, v112\n"
      "v_mul_f32 v168, v208, v168\n"
      "v_mfma_f32_16x16x32_bf16 v[160:163], v[236:239], v[130:133], v[160:163]\n"
      "ds_read_b128 v[130:133], v76 offset:10240\n"
      "v_mfma_f32_16x16x32_bf16 v[156:159], a[224:227], v[218:221], v[156:159]\n"
      "v_fmac_f32 v139, v113, v113\n"
      "v_sub_u32 v191, v191, v190\n"
      "v_mfma_f32_16x16x32_bf16 v[156:159], a[228:231], v[108:111], v[156:159]\n"
      "v_fmac_f32_e64 v138, v114, v114\n"
      "v_cvt_pk_bf16_f32 v168, v168, v168\n"
      "v_mfma_f32_16x16x32_bf16 v[146:149], a[248:251], v[218:221], v[146:149]\n"
      "v_fmac_f32 v139, v115, v115\n"
      "v_fmac_f32 v138, v204, v204\n"
      "v_mfma_f32_16x16x32_bf16 v[146:149], a[252:255], v[108:111], v[146:149]\n"
      "ds_read_b128 v[102:105], v76 offset:4096\n"
      "v_mfma_f32_16x16x32_bf16 v[160:163], v[240:243], v[218:221], v[160:163]\n"
      "v_fmac_f32 v139, v205, v205\n"
      "v_fmac_f32 v139, v207, v207\n"
      "v_mfma_f32_16x16x32_bf16 v[160:163], v[244:247], v[108:111], v[160:163]\n"
      "ds_write_b16 v12, v168\n"
      "v_mfma_f32_16x16x32_bf16 v[156:159], a[232:235], v[150:153], v[156:159]\n"
      "s_waitcnt lgkmcnt(2)\n"
      "v_fmac_f32 v138, v206, v206\n"
      "v_add_f32_e64 v138, v138, v139\n"
      "v_mfma_f32_16x16x32_bf16 v[156:159], a[236:239], v[196:199], v[156:159]\n"
      "s_nop 0\n"
      "v_permlane16_swap_b32 v140, v138\n"
      "v_mfma_f32_16x16x32_bf16 v[146:149], v[224:227], v[150:153], v[146:149]\n"
      "buffer_store_dwordx4 v[210:213], v23, s[24:27], s85 offen sc1\n"
      "v_mfma_f32_16x16x32_bf16 v[146:149], v[228:231], v[196:199], v[146:149]\n"
      "v_sub_u32 v179, v181, v179\n"
      "v_add_f32 v140, v140, v138\n"
      "v_mfma_f32_16x16x32_bf16 v[160:163], v[248:251], v[150:153], v[160:163]\n"
      "v_mul_f32 v153, v157, v157\n"
      "v_fma_f32 v152, v156, v156, s52\n"
      "v_mfma_f32_16x16x32_bf16 v[160:163], v[252:255], v[196:199], v[160:163]\n"
      "v_fmac_f32 v153, v159, v159\n"
      "v_fmac_f32 v152, v158, v158\n"
      "v_mfma_f32_16x16x32_bf16 v[196:199], a[216:219], v[214:217], 0\n"
      "v_fmac_f32 v153, v147, v147\n"
      "v_fmac_f32 v152, v146, v146\n"
      "v_mfma_f32_16x16x32_bf16 v[196:199], a[220:223], v[170:173], v[196:199]\n"
      "v_fmac_f32 v153, v149, v149\n"
      "v_fmac_f32 v152, v148, v148\n"
      "v_mfma_f32_16x16x32_bf16 v[218:221], a[240:243], v[214:217], 0\n"
      "v_fmac_f32 v153, v161, v161\n"
      "v_fmac_f32 v152, v160, v160\n"
      "v_mfma_f32_16x16x32_bf16 v[218:221], a[244:247], v[170:173], v[218:221]\n"
      "v_fmac_f32 v153, v163, v163\n"
      "v_fmac_f32 v152, v162, v162\n"
      "v_mfma_f32_16x16x32_bf16 v[208:211], v[232:235], v[214:217], 0\n"
      "s_waitcnt lgkmcnt(0)\n"
      "v_add_f32 v152, v152, v153\n"
      "v_mov_b32 v138, v152\n"
      "v_mfma_f32_16x16x32_bf16 v[208:211], v[236:239], v[170:173], v[208:211]\n"
      "s_nop 0\n"
      "v_permlane16_swap_b32 v152, v138\n"
      "v_mfma_f32_16x16x32_bf16 v[196:199], a[224:227], v[102:105], v[196:199]\n"
      "ds_read_b128 v[214:217], v76 offset:8192\n"
      "v_add_f32 v152, v152, v138\n"
      "v_mfma_f32_16x16x32_bf16 v[196:199], a[228:231], v[192:195], v[196:199]\n"
      "s_nop 0\n"
      "v_permlane32_swap_b32 v140, v152\n"
      "v_add_f32 v140, v140, v152\n"
      "v_mfma_f32_16x16x32_bf16 v[218:221], a[248:251], v[102:105], v[218:221]\n"
      "ds_write_b32 v31, v140 offset:1024\n"
      "v_mfma_f32_16x16x32_bf16 v[218:221], a[252:255], v[192:195], v[218:221]\n"
      "buffer_store_dwordx4 v[174:177], v25, s[24:27], s85 offen sc1\n"
      "v_mfma_f32_16x16x32_bf16 v[208:211], v[240:243], v[102:105], v[208:211]\n"
      "v_mul_u32_u24_e64 v165, 24, v180\n"
      "v_readlane_b32 s85, v37, s42\n"
      "v_bfe_i32 v190, v37, 30, 1\n"
      "v_mfma_f32_16x16x32_bf16 v[208:211], v[244:247], v[192:195], v[208:211]\n"
      "s_or_b32 s95, s85, s48\n"
      "v_and_b32 v190, 196224, v190\n"
      "s_waitcnt lgkmcnt(1)\n"
      "v_mfma_f32_16x16x32_bf16 v[196:199], a[232:235], v[214:217], v[196:199]\n"
      "s_cmp_ge_u32 s95, 0x40000000\n"
      "s_cbranch_scc1 L_rare_27_%=\n"
      "L_back_28_%=:\n"
      "v_mfma_f32_16x16x32_bf16 v[196:199], a[236:239], v[130:133], v[196:199]\n"
      "s_waitcnt lgkmcnt(0)\n"
      "s_barrier\n"
      "ds_read_b128 v[174:177], v9 offset:1024\n"
      "ds_read_b128 v[150:153], v76 offset:256\n"
      "v_mfma_f32_16x16x32_bf16 v[218:221], v[224:227], v[214:217], v[218:221]\n"
      "ds_read_b128 v[168:171], v76 offset:2304\n"
      "v_mfma_f32_16x16x32_bf16 v[218:221], v[228:231], v[130:133], v[218:221]\n"
      "ds_read_b128 v[192:195], v7 offset:18432\n"
      "v_mul_f32_e64 v167, v197, v197\n"
      "v_mfma_f32_16x16x32_bf16 v[208:211], v[248:251], v[214:217], v[208:211]\n"
      "ds_read_b128 v[214:217], v7 offset:22528\n"
      "v_fma_f32 v166, v196, v196, s52\n"
      "v_mfma_f32_16x16x32_bf16 v[208:211], v[252:255], v[130:133], v[208:211]\n"
      "ds_read_b128 v[130:133], v76 offset:4352\n"
      "ds_read_b128 v[102:105], v76 offset:6400\n"
      "ds_read_b128 v[108:111], v7 offset:26624\n"
      "v_pk_fma_f32 v[166:167], v[198:199], v[198:199], v[166:167]\n"
      "v_pk_fma_f32 v[166:167], v[218:219], v[218:219], v[166:167]\n"
      "s_waitcnt lgkmcnt(7)\n"
      "v_add_f32 v176, v176, v177\n"
      "v_add_f32 v174, v174, v175\n"
      "v_add_f32 v174, v174, v176\n"
      "v_pk_fma_f32 v[166:167], v[220:221], v[220:221], v[166:167]\n"
      "s_waitcnt lgkmcnt(6)\n"
      "v_mfma_f32_16x16x32_bf16 v[186:189], a[216:219], v[150:153], 0\n"
      "s_waitcnt lgkmcnt(5)\n"
      "v_rsq_f32 v174, v174\n"
      "v_mfma_f32_16x16x32_bf16 v[186:189], a[220:223], v[168:171], v[186:189]\n"
      "s_waitcnt lgkmcnt(4)\n"
      "v_mul_f32 v174, 0x415db3d7, v174\n"
      "v_mov_b32_e64 v176, v174\n"
      "v_mfma_f32_16x16x32_bf16 v[138:141], a[240:243], v[150:153], 0\n"
      "s_nop 0\n"
      "v_permlane32_swap_b32 v174, v176\n"
      "v_mfma_f32_16x16x32_bf16 v[138:141], a[244:247], v[168:171], v[138:141]\n"
      "buffer_store_dwordx4 v[192:195], v20, s[24:27], s85 offen sc1\n"
      "v_mov_b32 v175, v174\n"
      "v_pk_mul_f32 v[160:161], v[160:161], v[176:177] op_sel_hi:[1,0]\n"
      "v_mfma_f32_16x16x32_bf16 v[192:195], v[232:235], v[150:153], 0\n"
      "s_waitcnt lgkmcnt(0)\n"
      "v_permlane16_swap_b32_e64 v174, v175\n"
      "v_mfma_f32_16x16x32_bf16 v[192:195], v[236:239], v[168:171], v[192:195]\n"
      "buffer_store_dwordx4 v[214:217], v21, s[24:27], s85 offen sc1\n"
      "v_pk_mul_f32 v[204:205], v[204:205], v[174:175] op_sel:[0,1]\n"
      "v_pk_mul_f32 v[98:99], v[98:99], v[174:175] op_sel_hi:[1,0]\n"
      "v_pk_mul_f32 v[134:135], v[134:135], v[174:175] op_sel_hi:[1,0]\n"
      "v_pk_mul_f32 v[136:137], v[136:137], v[174:175] op_sel_hi:[1,0]\n"
      "v_pk_mul_f32 v[112:113], v[112:113], v[174:175] op_sel:[0,1]\n"
      "v_pk_fma_f32 v[166:167], v[208:209], v[208:209], v[166:167]\n"
      "v_pk_mul_f32 v[202:203], v[202:203], v[174:175] op_sel:[0,1]\n"
      "v_pk_mul_f32 v[114:115], v[114:115], v[174:175] op_sel:[0,1]\n"
      "v_pk_mul_f32 v[162:163], v[162:163], v[176:177] op_sel_hi:[1,0]\n"
      "v_pk_mul_f32 v[118:119], v[118:119], v[174:175] op_sel_hi:[1,0]\n"
      "v_pk_mul_f32 v[206:207], v[206:207], v[174:175] op_sel:[0,1]\n"
      "v_pk_mul_f32 v[96:97], v[96:97], v[174:175] op_sel_hi:[1,0]\n"
      "v_pk_mul_f32 v[200:201], v[200:201], v[174:175] op_sel:[0,1]\n"
      "v_mfma_f32_16x16x32_bf16 v[186:189], a[224:227], v[130:133], v[186:189]\n"
      "ds_read_b128 v[214:217], v76 offset:10496\n"
      "v_cvt_pk_bf16_f32 v96, v96, v97\n"
      "v_mfma_f32_16x16x32_bf16 v[186:189], a[228:231], v[102:105], v[186:189]\n"
      "ds_read_b128 v[168:171], v76 offset:8448\n"
      "v_cvt_pk_bf16_f32 v134, v134, v135\n"
      "v_mfma_f32_16x16x32_bf16 v[138:141], a[248:251], v[130:133], v[138:141]\n"
      "buffer_store_dwordx4 v[108:111], v22, s[24:27], s85 offen sc1\n"
      "v_pk_mul_f32 v[116:117], v[116:117], v[174:175] op_sel_hi:[1,0]\n"
      "v_mfma_f32_16x16x32_bf16 v[138:141], a[252:255], v[102:105], v[138:141]\n"
      "ds_read_b128 v[150:153], v7 offset:30720\n"
      "v_cvt_pk_bf16_f32 v97, v98, v99\n"
      "v_cvt_pk_bf16_f32 v160, v160, v161\n"
      "v_cvt_pk_bf16_f32 v200, v200, v201\n"
      "v_cvt_pk_bf16_f32 v201, v202, v203\n"
      "v_pk_fma_f32 v[166:167], v[210:211], v[210:211], v[166:167]\n"
      "v_mfma_f32_16x16x32_bf16 v[192:195], v[240:243], v[130:133], v[192:195]\n"
      "v_cvt_pk_bf16_f32 v135, v136, v137\n"
      "v_cvt_pk_bf16_f32 v203, v114, v115\n"
      "v_cvt_pk_bf16_f32 v161, v162, v163\n"
      "v_mfma_f32_16x16x32_bf16 v[192:195], v[244:247], v[102:105], v[192:195]\n"
      "s_waitcnt lgkmcnt(1)\n"
      "v_cvt_pk_bf16_f32 v204, v204, v205\n"
      "v_cvt_pk_bf16_f32 v98, v116, v117\n"
      "v_cvt_pk_bf16_f32 v205, v206, v207\n"
      "v_mfma_f32_16x16x32_bf16 v[186:189], a[232:235], v[168:171], v[186:189]\n"
      "ds_write_b64 v6, v[134:135]\n"
      "v_mfma_f32_16x16x32_bf16 v[186:189], a[236:239], v[214:217], v[186:189]\n"
      "ds_write_b64 v6, v[204:205] offset:6144\n"
      "v_mfma_f32_16x16x32_bf16 v[138:141], v[224:227], v[168:171], v[138:141]\n"
      "ds_write_b64 v6, v[160:161] offset:12288\n"
      "v_cvt_pk_bf16_f32 v99, v118, v119\n"
      "v_mfma_f32_16x16x32_bf16 v[138:141], v[228:231], v[214:217], v[138:141]\n"
      "ds_write_b128 v5, v[96:99]\n"
      "v_cvt_pk_bf16_f32 v202, v112, v113\n"
      "v_mfma_f32_16x16x32_bf16 v[192:195], v[248:251], v[168:171], v[192:195]\n"
      "ds_read_b128 v[96:99], v7 offset:34816\n"
      "v_fma_f32 v204, v186, v186, s52\n"
      "v_mfma_f32_16x16x32_bf16 v[192:195], v[252:255], v[214:217], v[192:195]\n"
      "ds_write_b128 v5, v[200:203] offset:6144\n"
      "v_mul_f32 v205, v187, v187\n"
      "v_pk_fma_f32 v[204:205], v[188:189], v[188:189], v[204:205]\n"
      "v_pk_mul_f32 v[158:159], v[158:159], v[176:177] op_sel_hi:[1,0]\n"
      "v_pk_fma_f32 v[204:205], v[138:139], v[138:139], v[204:205]\n"
      "v_pk_mul_f32 v[156:157], v[156:157], v[176:177] op_sel_hi:[1,0]\n"
      "v_pk_fma_f32 v[204:205], v[140:141], v[140:141], v[204:205]\n"
      "v_pk_mul_f32 v[148:149], v[148:149], v[176:177] op_sel_hi:[1,0]\n"
      "v_pk_fma_f32 v[204:205], v[192:193], v[192:193], v[204:205]\n"
      "v_add_f32 v166, v166, v167\n"
      "v_pk_fma_f32 v[204:205], v[194:195], v[194:195], v[204:205]\n"
      "v_add_f32 v204, v204, v205\n"
      "v_cvt_pk_bf16_f32 v156, v156, v157\n"
      "v_pk_mul_f32 v[146:147], v[146:147], v[176:177] op_sel_hi:[1,0]\n"
      "v_permlane16_swap_b32 v166, v204\n"
      "v_cvt_pk_bf16_f32 v157, v158, v159\n"
      "v_add_f32 v166, v166, v204\n"
      "v_mov_b32 v204, v166\n"
      "v_cvt_pk_bf16_f32 v158, v146, v147\n"
      "v_cvt_pk_bf16_f32 v159, v148, v149\n"
      "v_permlane32_swap_b32 v166, v204\n"
      "v_add_f32 v166, v166, v204\n"
      "s_add_u32 s42, s42, 1\n"
      "ds_write_b128 v5, v[156:159] offset:12288\n"
      "ds_write_b32 v31, v166 offset:4608\n"
      "s_waitcnt lgkmcnt(2)\n"
      "buffer_store_dwordx4 v[150:153], v23, s[24:27], s85 offen sc1\n"
      "v_sub_u32 v165, v84, v165\n"
      "buffer_store_dwordx4 v[96:99], v25, s[24:27], s85 offen sc1\n"
      "v_and_b32_e64 v181, 7, v178\n"
      "s_waitcnt lgkmcnt(0)\n"
      "s_barrier\n"
      "ds_read_b128 v[96:99], v9 offset:4608\n"
      "v_lshl_add_u32 v178, v178, 2, 12\n"
      "v_and_b32 v84, 7, v180\n"
      "v_readlane_b32 s85, v37, s42\n"
      "ds_read_b128 v[156:159], v7\n"
      "s_or_b32 s95, s85, s48\n"
      "v_xor_b32_e64 v165, v165, v84\n"
      "s_cmp_ge_u32 s95, 0x40000000\n"
      "s_cbranch_scc1 L_rare_31_%=\n"
      "L_back_32_%=:\n"
      "ds_read_b128 v[146:149], v7 offset:4096\n"
      "ds_read_b128 v[200:203], v7 offset:16384\n"
      "ds_read_b128 v[214:217], v7 offset:8192\n"
      "ds_read_b128 v[204:207], v7 offset:12288\n"
      "s_waitcnt lgkmcnt(5)\n"
      "v_add_f32 v98, v98, v99\n"
      "v_add_f32 v96, v96, v97\n"
      "v_add_f32 v96, v96, v98\n"
      "v_rsq_f32 v96, v96\n"
      "s_nop 0\n"
      "v_mul_f32 v96, 0x415db3d7, v96\n"
      "v_mov_b32 v98, v96\n"
      "v_subrev_u32 v99, 73728, v5\n"
      "s_nop 0\n"
      "v_permlane32_swap_b32 v96, v98\n"
      "v_mov_b32 v97, v96\n"
      "v_sub_u32 v191, v191, v190\n"
      "v_lshl_add_u32 v180, v180, 2, 12\n"
      "v_permlane16_swap_b32 v96, v97\n"
      "v_pk_mul_f32 v[186:187], v[186:187], v[96:97] op_sel:[0,1]\n"
      "v_pk_mul_f32 v[192:193], v[192:193], v[96:97] op_sel:[0,1]\n"
      "v_pk_mul_f32 v[140:141], v[140:141], v[96:97] op_sel:[0,1]\n"
      "v_pk_mul_f32 v[198:199], v[198:199], v[96:97] op_sel_hi:[1,0]\n"
      "v_pk_mul_f32 v[196:197], v[196:197], v[96:97] op_sel_hi:[1,0]\n"
      "v_pk_mul_f32 v[210:211], v[210:211], v[96:97] op_sel_hi:[1,0]\n"
      "v_cvt_pk_bf16_f32 v192, v192, v193\n"
      "v_cvt_pk_bf16_f32 v186, v186, v187\n"
      "v_pk_mul_f32 v[188:189], v[188:189], v[96:97] op_sel:[0,1]\n"
      "v_pk_mul_f32 v[208:209], v[208:209], v[96:97] op_sel_hi:[1,0]\n"
      "v_cvt_pk_bf16_f32 v208, v208, v209\n"
      "v_pk_mul_f32 v[218:219], v[218:219], v[96:97] op_sel_hi:[1,0]\n"
      "v_cvt_pk_bf16_f32 v187, v188, v189\n"
      "v_pk_mul_f32 v[138:139], v[138:139], v[96:97] op_sel:[0,1]\n"
      "v_cvt_pk_bf16_f32 v196, v196, v197\n"
      "v_pk_mul_f32 v[220:221], v[220:221], v[96:97] op_sel_hi:[1,0]\n"
      "v_pk_mul_f32 v[194:195], v[194:195], v[96:97] op_sel:[0,1]\n"
      "v_cvt_pk_bf16_f32 v189, v140, v141\n"
      "v_cvt_pk_bf16_f32 v197, v198, v199\n"
      "v_cvt_pk_bf16_f32 v198, v218, v219\n"
      "v_cvt_pk_bf16_f32 v209, v210, v211\n"
      "v_cvt_pk_bf16_f32 v188, v138, v139\n"
      "v_cvt_pk_bf16_f32 v193, v194, v195\n"
      "s_add_u32 s42, s42, 1\n"
      "ds_write_b128 v99, v[186:189] offset:6144\n"
      "ds_write_b64 v86, v[208:209]\n"
      "ds_write_b64 v86, v[192:193] offset:6144\n"
      "v_cvt_pk_bf16_f32 v199, v220, v221\n"
      "s_waitcnt lgkmcnt(3)\n"
      "buffer_store_dwordx4 v[156:159], v20, s[24:27], s85 offen sc1\n"
      "ds_write_b128 v99, v[196:199]\n"
      "buffer_store_dwordx4 v[146:149], v21, s[24:27], s85 offen sc1\n"
      "v_xor_b32 v179, v179, v181\n"
      "s_nop 0\n"
      "ds_bpermute_b32 v149, v180, v191\n"
      "ds_bpermute_b32 v148, v178, v191\n"
      "buffer_store_dwordx4 v[214:217], v22, s[24:27], s85 offen sc1\n"
      "s_waitcnt lgkmcnt(2)\n"
      "s_barrier\n"
      "ds_read_b128 v[214:217], v85\n"
      "buffer_store_dwordx4 v[204:207], v23, s[24:27], s85 offen sc1\n"
      "s_nop 1\n"
      "ds_read_b128 v[204:207], v85 offset:1024\n"
      "buffer_store_dwordx4 v[200:203], v25, s[24:27], s85 offen sc1\n"
      "s_waitcnt lgkmcnt(2)\n"
      "v_lshl_add_u32 v149, v165, 4, v149\n"
      "v_lshl_add_u32 v148, v179, 4, v148\n"
      "s_waitcnt lgkmcnt(1)\n"
      "buffer_store_dwordx4 v[214:217], v149, s[24:27], 0 offen sc1\n"
      "s_waitcnt lgkmcnt(0)\n"
      "buffer_store_dwordx4 v[204:207], v148, s[24:27], 0 offen sc1\n"
      "s_endpgm\n"
      "L_tail0_%=:\n"
      "s_nop 4\n"
      "ds_read_b128 v[186:189], v9 offset:1024\n"
      "v_and_b32 v173, 0x3fffffff, v37\n"
      "v_mfma_f32_16x16x32_bf16 v[168:171], v[218:221], v[130:133], 0\n"
      "ds_read_b128 v[102:105], v2 offset:18432\n"
      "ds_read_b128 v[138:141], v2 offset:24576\n"
      "ds_read_b128 v[130:133], v2 offset:27904\n"
      "v_mfma_f32_16x16x32_bf16 v[168:171], v[174:177], v[76:79], v[168:171]\n"
      "ds_read_b128 v[108:111], v2 offset:27648\n"
      "ds_read_b128 v[190:193], v7 offset:30720\n"
      "v_mfma_f32_16x16x32_bf16 v[150:153], v[218:221], v[196:199], 0\n"
      "ds_read_b128 v[164:167], v7 offset:18432\n"
      "ds_read_b128 v[178:181], v2 offset:18688\n"
      "v_mfma_f32_16x16x32_bf16 v[150:153], v[174:177], v[80:83], v[150:153]\n"
      "ds_read_b128 v[182:185], v2 offset:21504\n"
      "v_subrev_u32 v198, 115456, v9\n"
      "s_waitcnt lgkmcnt(8)\n"
      "v_add_f32 v186, v186, v187\n"
      "v_add_f32 v188, v188, v189\n"
      "v_add_f32 v186, v186, v188\n"
      "v_rsq_f32_e64 v186, v186\n"
      "v_mfma_f32_16x16x32_bf16 v[194:197], v[218:221], v[214:217], 0\n"
      "s_waitcnt lgkmcnt(7)\n"
      "v_mul_f32 v186, 0x415db3d7, v186\n"
      "v_mov_b32 v188, v186\n"
      "v_cndmask_b32 v168, v168, v150, s[58:59]\n"
      "s_nop 0\n"
      "v_permlane32_swap_b32 v186, v188\n"
      "v_mov_b32_e64 v187, v186\n"
      "v_pk_mul_f32 v[162:163], v[162:163], v[188:189] op_sel_hi:[1,0]\n"
      "v_pk_mul_f32 v[156:157], v[156:157], v[188:189] op_sel_hi:[1,0]\n"
      "v_permlane16_swap_b32_e64 v186, v187\n"
      "v_pk_mul_f32 v[160:161], v[160:161], v[188:189] op_sel_hi:[1,0]\n"
      "v_pk_mul_f32 v[136:137], v[136:137], v[186:187] op_sel_hi:[1,0]\n"
      "v_pk_mul_f32 v[146:147], v[146:147], v[188:189] op_sel_hi:[1,0]\n"
      "v_pk_mul_f32 v[134:135], v[134:135], v[186:187] op_sel_hi:[1,0]\n"
      "v_pk_mul_f32 v[200:201], v[200:201], v[186:187] op_sel:[0,1]\n"
      "v_pk_mul_f32 v[148:149], v[148:149], v[188:189] op_sel_hi:[1,0]\n"
      "v_pk_mul_f32 v[96:97], v[96:97], v[186:187] op_sel_hi:[1,0]\n"
      "v_pk_mul_f32 v[158:159], v[158:159], v[188:189] op_sel_hi:[1,0]\n"
      "v_cvt_pk_bf16_f32 v160, v160, v161\n"
      "v_pk_mul_f32 v[202:203], v[202:203], v[186:187] op_sel:[0,1]\n"
      "v_pk_mul_f32 v[98:99], v[98:99], v[186:187] op_sel_hi:[1,0]\n"
      "v_pk_mul_f32 v[116:117], v[116:117], v[186:187] op_sel_hi:[1,0]\n"
      "v_cvt_pk_bf16_f32 v156, v156, v157\n"
      "v_pk_mul_f32 v[204:205], v[204:205], v[186:187] op_sel:[0,1]\n"
      "v_pk_mul_f32 v[118:119], v[118:119], v[186:187] op_sel_hi:[1,0]\n"
      "v_pk_mul_f32 v[112:113], v[112:113], v[186:187] op_sel:[0,1]\n"
      "v_cvt_pk_bf16_f32 v96, v96, v97\n"
      "v_pk_mul_f32 v[206:207], v[206:207], v[186:187] op_sel:[0,1]\n"
      "v_cvt_pk_bf16_f32 v204, v204, v205\n"
      "v_pk_mul_f32 v[114:115], v[114:115], v[186:187] op_sel:[0,1]\n"
      "v_cvt_pk_bf16_f32 v97, v98, v99\n"
      "v_cvt_pk_bf16_f32 v98, v116, v117\n"
      "v_cvt_pk_bf16_f32 v157, v158, v159\n"
      "v_cvt_pk_bf16_f32 v200, v200, v201\n"
      "v_cvt_pk_bf16_f32 v201, v202, v203\n"
      "v_cvt_pk_bf16_f32 v159, v148, v149\n"
      "v_cvt_pk_bf16_f32 v134, v134, v135\n"
      "v_cvt_pk_bf16_f32 v158, v146, v147\n"
      "v_cvt_pk_bf16_f32 v203, v114, v115\n"
      "v_cvt_pk_bf16_f32 v202, v112, v113\n"
      "v_cvt_pk_bf16_f32 v161, v162, v163\n"
      "v_cvt_pk_bf16_f32 v135, v136, v137\n"
      "v_cvt_pk_bf16_f32 v205, v206, v207\n"
      "v_cvt_pk_bf16_f32 v99, v118, v119\n"
      "v_mfma_f32_16x16x32_bf16 v[194:197], v[174:177], v[84:87], v[194:197]\n"
      "ds_write_b128 v5, v[156:159] offset:12288\n"
      "ds_write_b64 v6, v[160:161] offset:12288\n"
      "ds_write_b128 v5, v[200:203] offset:6144\n"
      "ds_write_b64 v6, v[134:135]\n"
      "ds_write_b64 v6, v[204:205] offset:6144\n"
      "ds_write_b128 v5, v[96:99]\n"
      "v_mfma_f32_16x16x32_bf16 v[96:99], a[216:219], v[102:105], 0\n"
      "ds_read_b128 v[200:203], v2 offset:30720\n"
      "s_waitcnt lgkmcnt(5)\n"
      "v_mfma_f32_16x16x32_bf16 v[96:99], a[220:223], v[182:185], v[96:99]\n"
      "ds_read_b128 v[112:115], v2 offset:33792\n"
      "v_mfma_f32_16x16x32_bf16 v[116:119], a[240:243], v[102:105], 0\n"
      "v_mov_b32_dpp v208, v208 quad_perm:[0,0,0,0] row_mask:0xf bank_mask:0xf\n"
      "v_mfma_f32_16x16x32_bf16 v[116:119], a[244:247], v[182:185], v[116:119]\n"
      "ds_read_b128 v[156:159], v2 offset:21760\n"
      "v_mfma_f32_16x16x32_bf16 v[134:137], v[232:235], v[102:105], 0\n"
      "ds_read_b128 v[186:189], v2 offset:34304\n"
      "v_mfma_f32_16x16x32_bf16 v[134:137], v[236:239], v[182:185], v[134:137]\n"
      "ds_read_b128 v[146:149], v2 offset:34048\n"
      "v_subrev_u32 v85, 115456, v9\n"
      "v_mfma_f32_16x16x32_bf16 v[96:99], a[224:227], v[138:141], v[96:99]\n"
      "ds_read_b128 v[174:177], v2 offset:31232\n"
      "v_add_u32 v173, 1179648, v173\n"
      "v_mfma_f32_16x16x32_bf16 v[96:99], a[228:231], v[108:111], v[96:99]\n"
      "s_mul_i32 s92, s40, 2048\n"
      "ds_read_b128 v[150:153], v2 offset:22016\n"
      "v_mfma_f32_16x16x32_bf16 v[116:119], a[248:251], v[138:141], v[116:119]\n"
      "ds_read_b128 v[160:163], v2 offset:30976\n"
      "v_mfma_f32_16x16x32_bf16 v[116:119], a[252:255], v[108:111], v[116:119]\n"
      "buffer_store_dwordx4 v[164:167], v20, s[24:27], s46 offen sc1\n"
      "v_mfma_f32_16x16x32_bf16 v[134:137], v[240:243], v[138:141], v[134:137]\n"
      "ds_read_b128 v[218:221], v2 offset:24832\n"
      "v_add_u32 v81, s92, v198\n"
      "v_mfma_f32_16x16x32_bf16 v[134:137], v[244:247], v[108:111], v[134:137]\n"
      "s_waitcnt lgkmcnt(7)\n"
      "v_ashrrev_i32 v171, 31, v37\n"
      "v_and_b32 v164, 0x300, v85\n"
      "v_mfma_f32_16x16x32_bf16 v[96:99], a[232:235], v[200:203], v[96:99]\n"
      "v_rcp_f32_e64 v208, v208\n"
      "v_mfma_f32_16x16x32_bf16 v[96:99], a[236:239], v[112:115], v[96:99]\n"
      "ds_read_b128 v[108:111], v7 offset:26624\n"
      "v_cndmask_b32 v168, v168, v194, s[60:61]\n"
      "v_mfma_f32_16x16x32_bf16 v[116:119], v[224:227], v[200:203], v[116:119]\n"
      "ds_read_b128 v[138:141], v7 offset:22528\n"
      "v_mfma_f32_16x16x32_bf16 v[116:119], v[228:231], v[112:115], v[116:119]\n"
      "v_add_u32_e64 v85, v85, v164\n"
      "v_mfma_f32_16x16x32_bf16 v[134:137], v[248:251], v[200:203], v[134:137]\n"
      "v_add_u32 v85, 149504, v85\n"
      "v_mfma_f32_16x16x32_bf16 v[134:137], v[252:255], v[112:115], v[134:137]\n"
      "ds_read_b128 v[214:217], v85\n"
      "v_mfma_f32_16x16x32_bf16 v[200:203], a[216:219], v[178:181], 0\n"
      "s_waitcnt lgkmcnt(3)\n"
      "v_mul_f32 v168, v208, v168\n"
      "v_lshrrev_b32_e64 v198, 4, v81\n"
      "v_mfma_f32_16x16x32_bf16 v[200:203], a[220:223], v[156:159], v[200:203]\n"
      "v_add_u32 v82, 0, v198\n"
      "v_fma_f32 v164, v96, v96, s52\n"
      "v_mfma_f32_16x16x32_bf16 v[112:115], a[240:243], v[178:181], 0\n"
      "s_waitcnt lgkmcnt(1)\n"
      "v_mul_f32_e64 v165, v97, v97\n"
      "v_mfma_f32_16x16x32_bf16 v[112:115], a[244:247], v[156:159], v[112:115]\n"
      "ds_read_b128 v[182:185], v2 offset:18944\n"
      "v_mfma_f32_16x16x32_bf16 v[204:207], v[232:235], v[178:181], 0\n"
      "buffer_store_dwordx4 v[138:141], v21, s[24:27], s46 offen sc1\n"
      "v_mfma_f32_16x16x32_bf16 v[204:207], v[236:239], v[156:159], v[204:207]\n"
      "v_mfma_f32_16x16x32_bf16 v[200:203], a[224:227], v[218:221], v[200:203]\n"
      "v_fmac_f32_e64 v165, v99, v99\n"
      "v_mfma_f32_16x16x32_bf16 v[200:203], a[228:231], v[130:133], v[200:203]\n"
      "ds_read_b128 v[208:211], v85 offset:8192\n"
      "v_fmac_f32_e64 v164, v98, v98\n"
      "v_mfma_f32_16x16x32_bf16 v[112:115], a[248:251], v[218:221], v[112:115]\n"
      "ds_read_b128 v[102:105], v2 offset:28160\n"
      "v_fmac_f32 v165, v117, v117\n"
      "v_mfma_f32_16x16x32_bf16 v[112:115], a[252:255], v[130:133], v[112:115]\n"
      "buffer_store_dwordx4 v[108:111], v22, s[24:27], s46 offen sc1\n"
      "v_mfma_f32_16x16x32_bf16 v[204:207], v[240:243], v[218:221], v[204:207]\n"
      "s_nop 0\n"
      "ds_read_b128 v[108:111], v85 offset:6144\n"
      "v_mfma_f32_16x16x32_bf16 v[204:207], v[244:247], v[130:133], v[204:207]\n"
      "ds_read_b128 v[130:133], v2 offset:25088\n"
      "v_cvt_pk_bf16_f32 v168, v168, v168\n"
      "v_mfma_f32_16x16x32_bf16 v[200:203], a[232:235], v[160:163], v[200:203]\n"
      "v_add_u32_e64 v170, 64, v198\n"
      "v_mfma_f32_16x16x32_bf16 v[200:203], a[236:239], v[146:149], v[200:203]\n"
      "ds_read_b128 v[138:141], v85 offset:10240\n"
      "v_mul_u32_u24 v78, 2731, v82\n"
      "v_mfma_f32_16x16x32_bf16 v[112:115], v[224:227], v[160:163], v[112:115]\n"
      "ds_write_b16 v12, v168 offset:16\n"
      "v_mfma_f32_16x16x32_bf16 v[112:115], v[228:231], v[146:149], v[112:115]\n"
      "v_mul_u32_u24 v86, 2731, v170\n"
      "v_mfma_f32_16x16x32_bf16 v[204:207], v[248:251], v[160:163], v[204:207]\n"
      "s_nop 0\n"
      "v_fma_f32 v178, v200, v200, s52\n"
      "v_mul_f32 v179, v201, v201\n"
      "v_mfma_f32_16x16x32_bf16 v[204:207], v[252:255], v[146:149], v[204:207]\n"
      "s_waitcnt lgkmcnt(2)\n"
      "v_lshrrev_b32 v86, 16, v86\n"
      "v_fmac_f32 v164, v116, v116\n"
      "v_mfma_f32_16x16x32_bf16 v[156:159], a[216:219], v[182:185], 0\n"
      "v_fmac_f32 v179, v203, v203\n"
      "v_fmac_f32 v164, v118, v118\n"
      "v_mfma_f32_16x16x32_bf16 v[156:159], a[220:223], v[150:153], v[156:159]\n"
      "v_fmac_f32 v178, v202, v202\n"
      "v_fmac_f32 v178, v112, v112\n"
      "v_mfma_f32_16x16x32_bf16 v[146:149], a[240:243], v[182:185], 0\n"
      "v_fmac_f32 v165, v119, v119\n"
      "v_fmac_f32 v179, v113, v113\n"
      "v_mfma_f32_16x16x32_bf16 v[146:149], a[244:247], v[150:153], v[146:149]\n"
      "buffer_store_dwordx4 v[190:193], v23, s[24:27], s46 offen sc1\n"
      "v_mfma_f32_16x16x32_bf16 v[160:163], v[232:235], v[182:185], 0\n"
      "v_fmac_f32 v178, v114, v114\n"
      "v_fmac_f32 v164, v134, v134\n"
      "v_mfma_f32_16x16x32_bf16 v[160:163], v[236:239], v[150:153], v[160:163]\n"
      "v_subrev_u32 v76, 73728, v6\n"
      "v_fmac_f32 v165, v135, v135\n"
      "v_mfma_f32_16x16x32_bf16 v[156:159], a[224:227], v[130:133], v[156:159]\n"
      "ds_read_b128 v[182:185], v85 offset:2048\n"
      "v_lshrrev_b32 v78, 16, v78\n"
      "v_mfma_f32_16x16x32_bf16 v[156:159], a[228:231], v[102:105], v[156:159]\n"
      "v_fmac_f32 v179, v115, v115\n"
      "v_and_b32 v171, 100270464, v171\n"
      "v_mfma_f32_16x16x32_bf16 v[146:149], a[248:251], v[130:133], v[146:149]\n"
      "ds_read_b128 v[150:153], v85 offset:4096\n"
      "v_fmac_f32 v179, v205, v205\n"
      "v_mfma_f32_16x16x32_bf16 v[146:149], a[252:255], v[102:105], v[146:149]\n"
      "v_fmac_f32 v178, v204, v204\n"
      "v_fmac_f32 v178, v206, v206\n"
      "v_mfma_f32_16x16x32_bf16 v[160:163], v[240:243], v[130:133], v[160:163]\n"
      "v_fmac_f32 v179, v207, v207\n"
      "v_sub_u32_e64 v173, v173, v171\n"
      "v_mfma_f32_16x16x32_bf16 v[160:163], v[244:247], v[102:105], v[160:163]\n"
      "ds_read_b128 v[218:221], v7 offset:34816\n"
      "v_fmac_f32 v165, v137, v137\n"
      "v_mfma_f32_16x16x32_bf16 v[156:159], a[232:235], v[174:177], v[156:159]\n"
      "v_subrev_u32 v168, 73728, v5\n"
      "v_add_f32 v178, v178, v179\n"
      "v_mfma_f32_16x16x32_bf16 v[156:159], a[236:239], v[186:189], v[156:159]\n"
      "v_fmac_f32 v164, v136, v136\n"
      "v_add_f32 v164, v164, v165\n"
      "v_mfma_f32_16x16x32_bf16 v[146:149], v[224:227], v[174:177], v[146:149]\n"
      "s_nop 0\n"
      "v_permlane16_swap_b32 v164, v178\n"
      "v_mfma_f32_16x16x32_bf16 v[146:149], v[228:231], v[186:189], v[146:149]\n"
      "s_nop 1\n"
      "v_fma_f32 v180, v156, v156, s52\n"
      "v_mfma_f32_16x16x32_bf16 v[160:163], v[248:251], v[174:177], v[160:163]\n"
      "v_mul_f32 v181, v157, v157\n"
      "v_fmac_f32 v181, v159, v159\n"
      "v_mfma_f32_16x16x32_bf16 v[160:163], v[252:255], v[186:189], v[160:163]\n"
      "v_fmac_f32 v180, v158, v158\n"
      "v_fmac_f32 v180, v146, v146\n"
      "v_mfma_f32_16x16x32_bf16 v[190:193], a[216:219], v[214:217], 0\n"
      "s_waitcnt lgkmcnt(0)\n"
      "v_fmac_f32 v181, v147, v147\n"
      "v_fmac_f32 v180, v148, v148\n"
      "v_mfma_f32_16x16x32_bf16 v[190:193], a[220:223], v[182:185], v[190:193]\n"
      "v_add_f32 v164, v164, v178\n"
      "v_fmac_f32 v181, v149, v149\n"
      "v_mfma_f32_16x16x32_bf16 v[174:177], a[240:243], v[214:217], 0\n"
      "v_fmac_f32 v180, v160, v160\n"
      "v_fmac_f32 v181, v161, v161\n"
      "v_mfma_f32_16x16x32_bf16 v[174:177], a[244:247], v[182:185], v[174:177]\n"
      "v_fmac_f32 v180, v162, v162\n"
      "v_fmac_f32 v181, v163, v163\n"
      "v_mfma_f32_16x16x32_bf16 v[102:105], v[232:235], v[214:217], 0\n"
      "v_add_f32 v180, v180, v181\n"
      "v_mov_b32 v178, v180\n"
      "v_mfma_f32_16x16x32_bf16 v[102:105], v[236:239], v[182:185], v[102:105]\n"
      "s_nop 0\n"
      "v_permlane16_swap_b32 v180, v178\n"
      "v_mfma_f32_16x16x32_bf16 v[190:193], a[224:227], v[150:153], v[190:193]\n"
      "v_add_f32 v180, v180, v178\n"
      "v_mfma_f32_16x16x32_bf16 v[190:193], a[228:231], v[108:111], v[190:193]\n"
      "s_nop 0\n"
      "v_permlane32_swap_b32_e64 v164, v180\n"
      "v_mfma_f32_16x16x32_bf16 v[174:177], a[248:251], v[150:153], v[174:177]\n"
      "v_add_f32_e64 v164, v164, v180\n"
      "v_mfma_f32_16x16x32_bf16 v[174:177], a[252:255], v[108:111], v[174:177]\n"
      "ds_write_b32 v31, v164\n"
      "v_mfma_f32_16x16x32_bf16 v[102:105], v[240:243], v[150:153], v[102:105]\n"
      "buffer_store_dwordx4 v[218:221], v25, s[24:27], s46 offen sc1\n"
      "v_mfma_f32_16x16x32_bf16 v[102:105], v[244:247], v[108:111], v[102:105]\n"
      "v_mfma_f32_16x16x32_bf16 v[190:193], a[232:235], v[208:211], v[190:193]\n"
      "v_readlane_b32 s46, v37, s42\n"
      "s_nop 0\n"
      "s_or_b32 s95, s46, s48\n"
      "v_mfma_f32_16x16x32_bf16 v[190:193], a[236:239], v[138:141], v[190:193]\n"
      "s_cmp_ge_u32 s95, 0x40000000\n"
      "s_cbranch_scc1 L_rare_35_%=\n"
      "L_back_36_%=:\n"
      "v_mfma_f32_16x16x32_bf16 v[174:177], v[224:227], v[208:211], v[174:177]\n"
      "s_waitcnt lgkmcnt(0)\n"
      "s_barrier\n"
      "ds_read_b128 v[186:189], v9\n"
      "v_mfma_f32_16x16x32_bf16 v[174:177], v[228:231], v[138:141], v[174:177]\n"
      "ds_read_b128 v[178:181], v7\n"
      "ds_read_b128 v[218:221], v85 offset:256\n"
      "ds_read_b128 v[150:153], v85 offset:2304\n"
      "v_mul_f32_e64 v195, v191, v191\n"
      "v_fma_f32 v194, v190, v190, s52\n"
      "v_mfma_f32_16x16x32_bf16 v[102:105], v[248:251], v[208:211], v[102:105]\n"
      "ds_read_b128 v[108:111], v85 offset:6400\n"
      "ds_read_b128 v[196:199], v7 offset:8192\n"
      "ds_read_b128 v[214:217], v85 offset:4352\n"
      "ds_read_b128 v[164:167], v7 offset:12288\n"
      "v_pk_fma_f32 v[194:195], v[192:193], v[192:193], v[194:195]\n"
      "v_mfma_f32_16x16x32_bf16 v[102:105], v[252:255], v[138:141], v[102:105]\n"
      "ds_read_b128 v[182:185], v7 offset:4096\n"
      "s_waitcnt lgkmcnt(8)\n"
      "v_add_f32 v188, v188, v189\n"
      "v_add_f32_e64 v186, v186, v187\n"
      "v_add_f32_e64 v186, v186, v188\n"
      "v_pk_fma_f32 v[194:195], v[174:175], v[174:175], v[194:195]\n"
      "v_pk_fma_f32 v[194:195], v[176:177], v[176:177], v[194:195]\n"
      "v_rsq_f32_e64 v186, v186\n"
      "v_pk_fma_f32 v[194:195], v[102:103], v[102:103], v[194:195]\n"
      "v_pk_fma_f32 v[194:195], v[104:105], v[104:105], v[194:195]\n"
      "s_waitcnt lgkmcnt(6)\n"
      "v_mfma_f32_16x16x32_bf16 v[208:211], a[216:219], v[218:221], 0\n"
      "s_waitcnt lgkmcnt(5)\n"
      "v_mul_f32 v186, 0x415db3d7, v186\n"
      "v_mov_b32_e64 v188, v186\n"
      "v_mfma_f32_16x16x32_bf16 v[208:211], a[220:223], v[150:153], v[208:211]\n"
      "s_nop 0\n"
      "v_permlane32_swap_b32 v186, v188\n"
      "v_mfma_f32_16x16x32_bf16 v[138:141], a[240:243], v[218:221], 0\n"
      "ds_read_b128 v[130:133], v85 offset:8448\n"
      "v_mov_b32 v187, v186\n"
      "v_mfma_f32_16x16x32_bf16 v[138:141], a[244:247], v[150:153], v[138:141]\n"
      "buffer_store_dwordx4 v[178:181], v20, s[24:27], s46 offen sc1\n"
      "v_pk_mul_f32 v[162:163], v[162:163], v[188:189] op_sel_hi:[1,0]\n"
      "s_nop 0\n"
      "v_mfma_f32_16x16x32_bf16 v[178:181], v[232:235], v[218:221], 0\n"
      "s_waitcnt lgkmcnt(1)\n"
      "v_permlane16_swap_b32 v186, v187\n"
      "v_mfma_f32_16x16x32_bf16 v[178:181], v[236:239], v[150:153], v[178:181]\n"
      "buffer_store_dwordx4 v[182:185], v21, s[24:27], s46 offen sc1\n"
      "v_pk_mul_f32 v[116:117], v[116:117], v[186:187] op_sel_hi:[1,0]\n"
      "v_pk_mul_f32 v[114:115], v[114:115], v[186:187] op_sel:[0,1]\n"
      "v_pk_mul_f32 v[112:113], v[112:113], v[186:187] op_sel:[0,1]\n"
      "v_pk_mul_f32 v[204:205], v[204:205], v[186:187] op_sel:[0,1]\n"
      "v_pk_mul_f32 v[200:201], v[200:201], v[186:187] op_sel:[0,1]\n"
      "v_pk_mul_f32 v[96:97], v[96:97], v[186:187] op_sel_hi:[1,0]\n"
      "v_pk_mul_f32 v[160:161], v[160:161], v[188:189] op_sel_hi:[1,0]\n"
      "v_pk_mul_f32 v[134:135], v[134:135], v[186:187] op_sel_hi:[1,0]\n"
      "v_cvt_pk_bf16_f32 v134, v134, v135\n"
      "v_pk_mul_f32 v[146:147], v[146:147], v[188:189] op_sel_hi:[1,0]\n"
      "v_pk_mul_f32 v[202:203], v[202:203], v[186:187] op_sel:[0,1]\n"
      "v_pk_mul_f32 v[206:207], v[206:207], v[186:187] op_sel:[0,1]\n"
      "v_pk_mul_f32 v[156:157], v[156:157], v[188:189] op_sel_hi:[1,0]\n"
      "v_pk_mul_f32 v[118:119], v[118:119], v[186:187] op_sel_hi:[1,0]\n"
      "v_pk_mul_f32 v[136:137], v[136:137], v[186:187] op_sel_hi:[1,0]\n"
      "v_mfma_f32_16x16x32_bf16 v[208:211], a[224:227], v[214:217], v[208:211]\n"
      "v_cvt_pk_bf16_f32 v135, v136, v137\n"
      "v_add_f32_e64 v194, v194, v195\n"
      "v_cvt_pk_bf16_f32 v96, v96, v97\n"
      "v_mfma_f32_16x16x32_bf16 v[208:211], a[228:231], v[108:111], v[208:211]\n"
      "s_waitcnt lgkmcnt(0)\n"
      "v_cvt_pk_bf16_f32 v200, v200, v201\n"
      "v_cvt_pk_bf16_f32 v204, v204, v205\n"
      "v_mfma_f32_16x16x32_bf16 v[138:141], a[248:251], v[214:217], v[138:141]\n"
      "ds_read_b128 v[218:221], v85 offset:10496\n"
      "v_cvt_pk_bf16_f32 v205, v206, v207\n"
      "v_mfma_f32_16x16x32_bf16 v[138:141], a[252:255], v[108:111], v[138:141]\n"
      "ds_write_b64 v6, v[134:135] offset:18432\n"
      "v_cvt_pk_bf16_f32 v201, v202, v203\n"
      "v_mfma_f32_16x16x32_bf16 v[178:181], v[240:243], v[214:217], v[178:181]\n"
      "buffer_store_dwordx4 v[196:199], v22, s[24:27], s46 offen sc1\n"
      "v_pk_mul_f32 v[98:99], v[98:99], v[186:187] op_sel_hi:[1,0]\n"
      "v_mfma_f32_16x16x32_bf16 v[178:181], v[244:247], v[108:111], v[178:181]\n"
      "ds_write_b64 v6, v[204:205] offset:24576\n"
      "v_mfma_f32_16x16x32_bf16 v[208:211], a[232:235], v[130:133], v[208:211]\n"
      "s_waitcnt lgkmcnt(2)\n"
      "v_cvt_pk_bf16_f32 v97, v98, v99\n"
      "v_cvt_pk_bf16_f32 v160, v160, v161\n"
      "v_mfma_f32_16x16x32_bf16 v[208:211], a[236:239], v[218:221], v[208:211]\n"
      "v_cvt_pk_bf16_f32 v203, v114, v115\n"
      "v_cvt_pk_bf16_f32 v202, v112, v113\n"
      "v_cvt_pk_bf16_f32 v98, v116, v117\n"
      "v_mfma_f32_16x16x32_bf16 v[138:141], v[224:227], v[130:133], v[138:141]\n"
      "ds_write_b128 v5, v[200:203] offset:24576\n"
      "v_cvt_pk_bf16_f32 v99, v118, v119\n"
      "v_mfma_f32_16x16x32_bf16 v[138:141], v[228:231], v[218:221], v[138:141]\n"
      "ds_read_b128 v[198:201], v7 offset:16384\n"
      "v_cvt_pk_bf16_f32 v161, v162, v163\n"
      "v_mfma_f32_16x16x32_bf16 v[178:181], v[248:251], v[130:133], v[178:181]\n"
      "ds_write_b64 v6, v[160:161] offset:30720\n"
      "v_mfma_f32_16x16x32_bf16 v[178:181], v[252:255], v[218:221], v[178:181]\n"
      "ds_write_b128 v5, v[96:99] offset:18432\n"
      "v_cvt_pk_bf16_f32 v156, v156, v157\n"
      "v_mul_f32_e64 v161, v209, v209\n"
      "v_pk_mul_f32 v[148:149], v[148:149], v[188:189] op_sel_hi:[1,0]\n"
      "v_fma_f32 v160, v208, v208, s52\n"
      "v_pk_fma_f32 v[160:161], v[210:211], v[210:211], v[160:161]\n"
      "v_pk_fma_f32 v[160:161], v[138:139], v[138:139], v[160:161]\n"
      "v_pk_mul_f32 v[158:159], v[158:159], v[188:189] op_sel_hi:[1,0]\n"
      "v_pk_fma_f32 v[160:161], v[140:141], v[140:141], v[160:161]\n"
      "v_pk_fma_f32 v[160:161], v[178:179], v[178:179], v[160:161]\n"
      "v_pk_fma_f32 v[160:161], v[180:181], v[180:181], v[160:161]\n"
      "v_add_f32 v160, v160, v161\n"
      "v_cvt_pk_bf16_f32 v157, v158, v159\n"
      "v_cvt_pk_bf16_f32 v158, v146, v147\n"
      "v_permlane16_swap_b32 v194, v160\n"
      "v_add_f32 v194, v194, v160\n"
      "v_mov_b32 v160, v194\n"
      "v_cvt_pk_bf16_f32 v159, v148, v149\n"
      "ds_write_b128 v5, v[156:159] offset:30720\n"
      "v_permlane32_swap_b32 v194, v160\n"
      "v_add_f32 v194, v194, v160\n"
      "s_add_u32 s42, s42, 1\n"
      "ds_write_b32 v31, v194 offset:4608\n"
      "buffer_store_dwordx4 v[164:167], v23, s[24:27], s46 offen sc1\n"
      "v_mul_u32_u24 v194, 24, v86\n"
      "v_mul_u32_u24 v205, 24, v78\n"
      "v_sub_u32 v194, v170, v194\n"
      "v_add_u32 v12, 32, v12\n"
      "v_bfe_i32 v171, v37, 30, 1\n"
      "s_waitcnt lgkmcnt(2)\n"
      "buffer_store_dwordx4 v[198:201], v25, s[24:27], s46 offen sc1\n"
      "v_readlane_b32 s46, v37, s42\n"
      "s_waitcnt lgkmcnt(0)\n"
      "s_barrier\n"
      "ds_read_b128 v[198:201], v9 offset:4608\n"
      "v_sub_u32_e64 v205, v82, v205\n"
      "ds_read_b128 v[158:161], v7 offset:18432\n"
      "v_and_b32 v171, 196224, v171\n"
      "v_sub_u32 v173, v173, v171\n"
      "v_and_b32_e64 v82, 7, v78\n"
      "ds_read_b128 v[96:99], v7 offset:26624\n"
      "s_or_b32 s95, s46, s48\n"
      "ds_read_b128 v[112:115], v7 offset:30720\n"
      "ds_read_b128 v[130:133], v7 offset:22528\n"
      "v_and_b32 v170, 7, v86\n"
      "s_cmp_ge_u32 s95, 0x40000000\n"
      "s_cbranch_scc1 L_rare_39_%=\n"
      "L_back_40_%=:\n"
      "s_waitcnt lgkmcnt(4)\n"
      "v_add_f32 v200, v200, v201\n"
      "v_add_f32_e64 v198, v198, v199\n"
      "v_add_f32_e64 v198, v198, v200\n"
      "v_rsq_f32_e64 v198, v198\n"
      "s_nop 0\n"
      "v_mul_f32 v198, 0x415db3d7, v198\n"
      "v_mov_b32 v200, v198\n"
      "v_lshl_add_u32 v78, v78, 2, 12\n"
      "s_nop 0\n"
      "v_permlane32_swap_b32_e64 v198, v200\n"
      "v_mov_b32 v199, v198\n"
      "v_lshl_add_u32 v86, v86, 2, 12\n"
      "s_nop 0\n"
      "v_permlane16_swap_b32_e64 v198, v199\n"
      "v_pk_mul_f32 v[190:191], v[190:191], v[198:199] op_sel_hi:[1,0]\n"
      "v_pk_mul_f32 v[178:179], v[178:179], v[198:199] op_sel:[0,1]\n"
      "v_pk_mul_f32 v[210:211], v[210:211], v[198:199] op_sel:[0,1]\n"
      "v_pk_mul_f32 v[192:193], v[192:193], v[198:199] op_sel_hi:[1,0]\n"
      "v_pk_mul_f32 v[174:175], v[174:175], v[198:199] op_sel_hi:[1,0]\n"
      "v_pk_mul_f32 v[140:141], v[140:141], v[198:199] op_sel:[0,1]\n"
      "v_pk_mul_f32 v[208:209], v[208:209], v[198:199] op_sel:[0,1]\n"
      "v_pk_mul_f32 v[180:181], v[180:181], v[198:199] op_sel:[0,1]\n"
      "v_pk_mul_f32 v[104:105], v[104:105], v[198:199] op_sel_hi:[1,0]\n"
      "v_pk_mul_f32 v[102:103], v[102:103], v[198:199] op_sel_hi:[1,0]\n"
      "v_cvt_pk_bf16_f32 v190, v190, v191\n"
      "v_cvt_pk_bf16_f32 v191, v192, v193\n"
      "v_cvt_pk_bf16_f32 v178, v178, v179\n"
      "v_pk_mul_f32 v[176:177], v[176:177], v[198:199] op_sel_hi:[1,0]\n"
      "v_cvt_pk_bf16_f32 v208, v208, v209\n"
      "v_cvt_pk_bf16_f32 v179, v180, v181\n"
      "v_cvt_pk_bf16_f32 v193, v176, v177\n"
      "v_pk_mul_f32 v[138:139], v[138:139], v[198:199] op_sel:[0,1]\n"
      "v_cvt_pk_bf16_f32 v102, v102, v103\n"
      "v_cvt_pk_bf16_f32 v209, v210, v211\n"
      "v_cvt_pk_bf16_f32 v103, v104, v105\n"
      "v_cvt_pk_bf16_f32 v192, v174, v175\n"
      "s_add_u32 s42, s42, 1\n"
      "ds_write_b128 v168, v[190:193]\n"
      "ds_write_b64 v76, v[178:179] offset:6144\n"
      "ds_write_b64 v76, v[102:103]\n"
      "v_cvt_pk_bf16_f32 v211, v140, v141\n"
      "v_cvt_pk_bf16_f32 v210, v138, v139\n"
      "s_waitcnt lgkmcnt(3)\n"
      "buffer_store_dwordx4 v[158:161], v20, s[24:27], s46 offen sc1\n"
      "ds_write_b128 v168, v[208:211] offset:6144\n"
      "ds_bpermute_b32 v211, v78, v173\n"
      "buffer_store_dwordx4 v[130:133], v21, s[24:27], s46 offen sc1\n"
      "v_xor_b32_e64 v194, v194, v170\n"
      "v_xor_b32 v205, v205, v82\n"
      "ds_bpermute_b32 v170, v86, v173\n"
      "ds_read_b128 v[178:181], v7 offset:34816\n"
      "buffer_store_dwordx4 v[96:99], v22, s[24:27], s46 offen sc1\n"
      "s_waitcnt lgkmcnt(3)\n"
      "s_barrier\n"
      "ds_read_b128 v[130:133], v81\n"
      "buffer_store_dwordx4 v[112:115], v23, s[24:27], s46 offen sc1\n"
      "ds_read_b128 v[190:193], v81 offset:1024\n"
      "s_waitcnt lgkmcnt(3)\n"
      "v_lshl_add_u32 v170, v194, 4, v170\n"
      "s_waitcnt lgkmcnt(2)\n"
      "buffer_store_dwordx4 v[178:181], v25, s[24:27], s46 offen sc1\n"
      "v_lshl_add_u32 v211, v205, 4, v211\n"
      "s_waitcnt lgkmcnt(1)\n"
      "buffer_store_dwordx4 v[130:133], v211, s[24:27], 0 offen sc1\n"
      "s_waitcnt lgkmcnt(0)\n"
      "buffer_store_dwordx4 v[190:193], v170, s[24:27], 0 offen sc1\n"
      "s_endpgm\n"
      "L_rare_1_%=:\n"
      "s_nop 7\n"
      "s_waitcnt lgkmcnt(0)\n"
      "s_add_u32 s72, s42, 1\n"
      "v_readlane_b32 s77, v38, s72\n"
      "v_subrev_u32 v157, 115456, v9\n"
      "s_and_b32 s73, s77, -16\n"
      "s_mov_b32 m0, 110592\n"
      "s_nop 0\n"
      "buffer_load_dwordx4 v157, s[28:31], s73 offen  lds\n"
      "s_add_u32 s73, s73, 1024\n"
      "s_mov_b32 m0, 111616\n"
      "s_nop 0\n"
      "buffer_load_dwordx4 v157, s[28:31], s73 offen  lds\n"
      "s_add_u32 s73, s73, 1024\n"
      "s_mov_b32 m0, 112640\n"
      "s_nop 0\n"
      "buffer_load_dwordx4 v157, s[28:31], s73 offen  lds\n"
      "s_add_u32 s73, s73, 1024\n"
      "s_mov_b32 m0, 113664\n"
      "s_nop 0\n"
      "buffer_load_dwordx4 v157, s[28:31], s73 offen  lds\n"
      "s_add_u32 s73, s73, 1024\n"
      "s_mov_b32 m0, 114688\n"
      "s_mov_b32 exec_hi, 0xffff\n"
      "buffer_load_dwordx4 v157, s[28:31], s73 offen  lds\n"
      "s_mov_b32 exec_hi, -1\n"
      "s_add_u32 s73, s73, 1024\n"
      "s_and_b32 s77, s77, 15\n"
      "s_waitcnt lgkmcnt(0)\n"
      "s_nop 4\n"
      "s_branch L_back_2_%=\n"
      "L_rare_3_%=:\n"
      "s_nop 7\n"
      "s_waitcnt lgkmcnt(0)\n"
      "s_waitcnt vmcnt(0)\n"
      "v_subrev_u32 v180, 115456, v9\n"
      "v_bfe_u32 v113, v180, 4, 4\n"
      "v_mul_u32_u24 v113, 98, v113\n"
      "v_lshrrev_b32 v180, 8, v180\n"
      "v_lshl_add_u32 v113, v180, 3, v113\n"
      "v_add_u32 v113, s77, v113\n"
      "v_and_b32 v197, 2, v113\n"
      "v_and_b32 v113, -4, v113\n"
      "v_add_u32 v113, 110592, v113\n"
      "ds_read2_b32 v[106:107], v113 offset0:0 offset1:1\n"
      "ds_read2_b32 v[108:109], v113 offset0:8 offset1:9\n"
      "ds_read2_b32 v[156:157], v113 offset0:16 offset1:17\n"
      "ds_read2_b32 v[154:155], v113 offset0:2 offset1:10\n"
      "ds_read2_b32 v[186:187], v113 offset0:18 offset1:24\n"
      "s_waitcnt lgkmcnt(4)\n"
      "v_alignbyte_b32 v106, v107, v106, v197\n"
      "s_waitcnt lgkmcnt(1)\n"
      "v_alignbyte_b32 v107, v154, v107, v197\n"
      "v_lshlrev_b32 v180, 16, v106\n"
      "v_fmac_f32 v150, s53, v180\n"
      "v_and_b32 v180, 0xffff0000, v106\n"
      "v_fmac_f32 v151, s53, v180\n"
      "v_lshlrev_b32 v180, 16, v107\n"
      "v_fmac_f32_e64 v152, s53, v180\n"
      "v_and_b32 v180, 0xffff0000, v107\n"
      "v_fmac_f32 v153, s53, v180\n"
      "v_alignbyte_b32 v108, v109, v108, v197\n"
      "v_alignbyte_b32 v109, v155, v109, v197\n"
      "v_lshlrev_b32 v180, 16, v108\n"
      "v_fmac_f32 v204, s53, v180\n"
      "v_and_b32 v180, 0xffff0000, v108\n"
      "v_fmac_f32 v205, s53, v180\n"
      "v_lshlrev_b32 v180, 16, v109\n"
      "v_fmac_f32 v206, s53, v180\n"
      "v_and_b32 v180, 0xffff0000, v109\n"
      "v_fmac_f32 v207, s53, v180\n"
      "v_alignbyte_b32 v156, v157, v156, v197\n"
      "s_waitcnt lgkmcnt(0)\n"
      "v_alignbyte_b32 v157, v186, v157, v197\n"
      "v_lshlrev_b32 v180, 16, v156\n"
      "v_fmac_f32 v134, s53, v180\n"
      "v_and_b32 v180, 0xffff0000, v156\n"
      "v_fmac_f32 v135, s53, v180\n"
      "v_lshlrev_b32 v180, 16, v157\n"
      "v_fmac_f32 v136, s53, v180\n"
      "v_and_b32 v180, 0xffff0000, v157\n"
      "v_fmac_f32 v137, s53, v180\n"
      "v_alignbyte_b32 v180, v187, v187, v197\n"
      "v_lshlrev_b32 v180, 16, v180\n"
      "v_fmac_f32 v140, s53, v180\n"
      "v_max3_f32 v113, v150, v151, v152\n"
      "v_max3_f32 v197, v153, v204, v205\n"
      "v_max3_f32 v113, v113, v206, v207\n"
      "v_max3_f32 v197, v197, v134, v135\n"
      "v_max3_f32 v113, v113, v136, v137\n"
      "v_max3_f32 v113, v113, v197, v140\n"
      "v_mov_b32 v180, v113\n"
      "s_nop 1\n"
      "v_permlane16_swap_b32 v113, v180\n"
      "v_max_f32 v113, v113, v180\n"
      "v_mov_b32 v180, v113\n"
      "s_nop 1\n"
      "v_permlane32_swap_b32 v113, v180\n"
      "v_max_f32 v113, v113, v180\n"
      "v_sub_f32 v150, v150, v113\n"
      "v_sub_f32 v151, v151, v113\n"
      "v_sub_f32 v152, v152, v113\n"
      "v_sub_f32 v153, v153, v113\n"
      "v_sub_f32 v204, v204, v113\n"
      "v_sub_f32 v205, v205, v113\n"
      "v_sub_f32 v206, v206, v113\n"
      "v_sub_f32 v207, v207, v113\n"
      "v_sub_f32 v134, v134, v113\n"
      "v_sub_f32 v135, v135, v113\n"
      "v_sub_f32 v136, v136, v113\n"
      "v_sub_f32 v137, v137, v113\n"
      "v_sub_f32_e64 v140, v140, v113\n"
      "s_waitcnt lgkmcnt(0)\n"
      "s_nop 4\n"
      "s_branch L_back_4_%=\n"
      "L_rare_5_%=:\n"
      "s_nop 7\n"
      "s_waitcnt lgkmcnt(0)\n"
      "s_waitcnt vmcnt(0)\n"
      "v_subrev_u32 v141, 115456, v9\n"
      "v_bfe_u32 v82, v141, 4, 4\n"
      "v_mul_u32_u24 v82, 98, v82\n"
      "v_lshrrev_b32 v141, 8, v141\n"
      "v_lshl_add_u32 v82, v141, 3, v82\n"
      "v_add_u32 v82, s77, v82\n"
      "v_and_b32 v78, 2, v82\n"
      "v_and_b32 v82, -4, v82\n"
      "v_add_u32 v82, 112160, v82\n"
      "ds_read2_b32 v[220:221], v82 offset0:0 offset1:1\n"
      "ds_read2_b32 v[218:219], v82 offset0:8 offset1:9\n"
      "ds_read2_b32 v[200:201], v82 offset0:16 offset1:17\n"
      "ds_read2_b32 v[202:203], v82 offset0:2 offset1:10\n"
      "ds_read2_b32 v[142:143], v82 offset0:18 offset1:24\n"
      "s_waitcnt lgkmcnt(4)\n"
      "v_alignbyte_b32 v220, v221, v220, v78\n"
      "s_waitcnt lgkmcnt(1)\n"
      "v_alignbyte_b32 v221, v202, v221, v78\n"
      "v_lshlrev_b32 v141, 16, v220\n"
      "v_fmac_f32 v166, s53, v141\n"
      "v_and_b32 v141, 0xffff0000, v220\n"
      "v_fmac_f32 v167, s53, v141\n"
      "v_lshlrev_b32 v141, 16, v221\n"
      "v_fmac_f32 v168, s53, v141\n"
      "v_and_b32 v141, 0xffff0000, v221\n"
      "v_fmac_f32 v169, s53, v141\n"
      "v_alignbyte_b32 v218, v219, v218, v78\n"
      "v_alignbyte_b32 v219, v203, v219, v78\n"
      "v_lshlrev_b32 v141, 16, v218\n"
      "v_fmac_f32 v144, s53, v141\n"
      "v_and_b32 v141, 0xffff0000, v218\n"
      "v_fmac_f32 v145, s53, v141\n"
      "v_lshlrev_b32 v141, 16, v219\n"
      "v_fmac_f32_e64 v146, s53, v141\n"
      "v_and_b32 v141, 0xffff0000, v219\n"
      "v_fmac_f32 v147, s53, v141\n"
      "v_alignbyte_b32 v200, v201, v200, v78\n"
      "s_waitcnt lgkmcnt(0)\n"
      "v_alignbyte_b32 v201, v142, v201, v78\n"
      "v_lshlrev_b32 v141, 16, v200\n"
      "v_fmac_f32 v106, s53, v141\n"
      "v_and_b32 v141, 0xffff0000, v200\n"
      "v_fmac_f32 v107, s53, v141\n"
      "v_lshlrev_b32 v141, 16, v201\n"
      "v_fmac_f32_e64 v108, s53, v141\n"
      "v_and_b32 v141, 0xffff0000, v201\n"
      "v_fmac_f32 v109, s53, v141\n"
      "v_alignbyte_b32 v141, v143, v143, v78\n"
      "v_lshlrev_b32 v141, 16, v141\n"
      "v_fmac_f32_e64 v192, s53, v141\n"
      "v_max3_f32 v82, v166, v167, v168\n"
      "v_max3_f32 v78, v169, v144, v145\n"
      "v_max3_f32 v82, v82, v146, v147\n"
      "v_max3_f32 v78, v78, v106, v107\n"
      "v_max3_f32 v82, v82, v108, v109\n"
      "v_max3_f32 v82, v82, v78, v192\n"
      "v_mov_b32 v141, v82\n"
      "s_nop 1\n"
      "v_permlane16_swap_b32 v82, v141\n"
      "v_max_f32 v82, v82, v141\n"
      "v_mov_b32 v141, v82\n"
      "s_nop 1\n"
      "v_permlane32_swap_b32 v82, v141\n"
      "v_max_f32 v82, v82, v141\n"
      "v_sub_f32 v166, v166, v82\n"
      "v_sub_f32 v167, v167, v82\n"
      "v_sub_f32 v168, v168, v82\n"
      "v_sub_f32 v169, v169, v82\n"
      "v_sub_f32 v144, v144, v82\n"
      "v_sub_f32 v145, v145, v82\n"
      "v_sub_f32 v146, v146, v82\n"
      "v_sub_f32 v147, v147, v82\n"
      "v_sub_f32 v106, v106, v82\n"
      "v_sub_f32 v107, v107, v82\n"
      "v_sub_f32 v108, v108, v82\n"
      "v_sub_f32 v109, v109, v82\n"
      "v_sub_f32_e64 v192, v192, v82\n"
      "s_waitcnt lgkmcnt(0)\n"
      "s_nop 4\n"
      "s_branch L_back_6_%=\n"
      "L_rare_7_%=:\n"
      "s_nop 7\n"
      "s_waitcnt lgkmcnt(0)\n"
      "s_waitcnt vmcnt(0)\n"
      "v_subrev_u32 v173, 115456, v9\n"
      "v_bfe_u32 v123, v173, 4, 4\n"
      "v_mul_u32_u24 v123, 98, v123\n"
      "v_lshrrev_b32_e64 v173, 8, v173\n"
      "v_lshl_add_u32 v123, v173, 3, v123\n"
      "v_add_u32 v123, s77, v123\n"
      "v_and_b32 v122, 2, v123\n"
      "v_and_b32_e64 v123, -4, v123\n"
      "v_add_u32 v123, 113728, v123\n"
      "ds_read2_b32 v[136:137], v123 offset0:0 offset1:1\n"
      "ds_read2_b32 v[190:191], v123 offset0:8 offset1:9\n"
      "ds_read2_b32 v[138:139], v123 offset0:16 offset1:17\n"
      "ds_read2_b32 v[142:143], v123 offset0:2 offset1:10\n"
      "ds_read2_b32 v[192:193], v123 offset0:18 offset1:24\n"
      "s_waitcnt lgkmcnt(4)\n"
      "v_alignbyte_b32 v136, v137, v136, v122\n"
      "s_waitcnt lgkmcnt(1)\n"
      "v_alignbyte_b32 v137, v142, v137, v122\n"
      "v_lshlrev_b32 v173, 16, v136\n"
      "v_fmac_f32 v218, s53, v173\n"
      "v_and_b32 v173, 0xffff0000, v136\n"
      "v_fmac_f32 v219, s53, v173\n"
      "v_lshlrev_b32 v173, 16, v137\n"
      "v_fmac_f32_e64 v220, s53, v173\n"
      "v_and_b32 v173, 0xffff0000, v137\n"
      "v_fmac_f32 v221, s53, v173\n"
      "v_alignbyte_b32 v190, v191, v190, v122\n"
      "v_alignbyte_b32 v191, v143, v191, v122\n"
      "v_lshlrev_b32 v173, 16, v190\n"
      "v_fmac_f32 v186, s53, v173\n"
      "v_and_b32 v173, 0xffff0000, v190\n"
      "v_fmac_f32 v187, s53, v173\n"
      "v_lshlrev_b32 v173, 16, v191\n"
      "v_fmac_f32 v188, s53, v173\n"
      "v_and_b32 v173, 0xffff0000, v191\n"
      "v_fmac_f32 v189, s53, v173\n"
      "v_alignbyte_b32 v138, v139, v138, v122\n"
      "s_waitcnt lgkmcnt(0)\n"
      "v_alignbyte_b32 v139, v192, v139, v122\n"
      "v_lshlrev_b32 v173, 16, v138\n"
      "v_fmac_f32 v204, s53, v173\n"
      "v_and_b32 v173, 0xffff0000, v138\n"
      "v_fmac_f32 v205, s53, v173\n"
      "v_lshlrev_b32 v173, 16, v139\n"
      "v_fmac_f32 v206, s53, v173\n"
      "v_and_b32 v173, 0xffff0000, v139\n"
      "v_fmac_f32 v207, s53, v173\n"
      "v_alignbyte_b32 v173, v193, v193, v122\n"
      "v_lshlrev_b32 v173, 16, v173\n"
      "v_fmac_f32 v208, s53, v173\n"
      "v_max3_f32 v123, v218, v219, v220\n"
      "v_max3_f32 v122, v221, v186, v187\n"
      "v_max3_f32 v123, v123, v188, v189\n"
      "v_max3_f32 v122, v122, v204, v205\n"
      "v_max3_f32 v123, v123, v206, v207\n"
      "v_max3_f32 v123, v123, v122, v208\n"
      "v_mov_b32 v173, v123\n"
      "s_nop 1\n"
      "v_permlane16_swap_b32 v123, v173\n"
      "v_max_f32 v123, v123, v173\n"
      "v_mov_b32 v173, v123\n"
      "s_nop 1\n"
      "v_permlane32_swap_b32 v123, v173\n"
      "v_max_f32 v123, v123, v173\n"
      "v_sub_f32 v218, v218, v123\n"
      "v_sub_f32 v219, v219, v123\n"
      "v_sub_f32 v220, v220, v123\n"
      "v_sub_f32 v221, v221, v123\n"
      "v_sub_f32 v186, v186, v123\n"
      "v_sub_f32 v187, v187, v123\n"
      "v_sub_f32 v188, v188, v123\n"
      "v_sub_f32 v189, v189, v123\n"
      "v_sub_f32 v204, v204, v123\n"
      "v_sub_f32 v205, v205, v123\n"
      "v_sub_f32 v206, v206, v123\n"
      "v_sub_f32 v207, v207, v123\n"
      "v_sub_f32 v208, v208, v123\n"
      "s_waitcnt lgkmcnt(0)\n"
      "s_nop 4\n"
      "s_branch L_back_8_%=\n"
      "L_rare_9_%=:\n"
      "s_nop 7\n"
      "s_waitcnt lgkmcnt(0)\n"
      "s_waitcnt vmcnt(0)\n"
      "v_subrev_u32 v207, 115456, v9\n"
      "v_lshrrev_b32 v207, 3, v207\n"
      "v_min_u32 v207, 96, v207\n"
      "v_add_u32 v207, s77, v207\n"
      "v_add_u32 v207, 115296, v207\n"
      "ds_read_u16 v207, v207\n"
      "s_waitcnt lgkmcnt(0)\n"
      "v_lshlrev_b32_e64 v207, 16, v207\n"
      "v_fmac_f32 v200, s53, v207\n"
      "s_nop 1\n"
      "v_max_f32_dpp v206, v200, v200 row_ror:8 row_mask:0xf bank_mask:0xf\n"
      "s_nop 1\n"
      "v_max_f32_dpp v206, v206, v206 row_ror:4 row_mask:0xf bank_mask:0xf\n"
      "s_nop 1\n"
      "v_max_f32_dpp v206, v206, v206 row_ror:2 row_mask:0xf bank_mask:0xf\n"
      "s_nop 1\n"
      "v_max_f32_dpp v206, v206, v206 row_ror:1 row_mask:0xf bank_mask:0xf\n"
      "s_nop 1\n"
      "v_max_f32_dpp v206, v206, v206 row_bcast:15 row_mask:0xa bank_mask:0xf\n"
      "s_nop 1\n"
      "v_max_f32_dpp v206, v206, v206 row_bcast:31 row_mask:0xc bank_mask:0xf\n"
      "s_nop 0\n"
      "v_readlane_b32 s72, v206, 63\n"
      "s_nop 1\n"
      "v_subrev_f32 v200, s72, v200\n"
      "s_waitcnt lgkmcnt(0)\n"
      "s_nop 4\n"
      "s_branch L_back_10_%=\n"
      "L_rare_27_%=:\n"
      "s_nop 7\n"
      "s_waitcnt lgkmcnt(0)\n"
      "s_bitcmp1_b32 s85, 31\n"
      "s_cselect_b32 s75, 100270464, 0\n"
      "s_bitcmp1_b32 s85, 30\n"
      "s_cselect_b32 s76, 196224, 0\n"
      "s_and_b32 s74, s85, 0x3fffffff\n"
      "s_cmp_ge_u32 s85, 0x40000000\n"
      "s_cselect_b32 s74, s74, 0\n"
      "s_cselect_b32 s85, 0, s85\n"
      "s_sub_u32 s74, s74, s69\n"
      "s_add_u32 s69, s69, s74\n"
      "s_sub_u32 s75, s75, s70\n"
      "s_add_u32 s70, s70, s75\n"
      "s_sub_u32 s76, s76, s71\n"
      "s_add_u32 s71, s71, s76\n"
      "v_add_u32 v20, s74, v20\n"
      "v_add_u32 v21, s74, v21\n"
      "v_add_u32 v22, s74, v22\n"
      "v_add_u32 v23, s74, v23\n"
      "v_add_u32 v25, s74, v25\n"
      "s_cmp_eq_u32 s75, 0\n"
      "s_cbranch_scc1 L_ax_25_%=\n"
      "v_bfe_i32 v175, v27, 10, 1\n"
      "v_and_b32 v175, s75, v175\n"
      "v_sub_u32 v20, v20, v175\n"
      "v_bfe_i32 v175, v27, 12, 1\n"
      "v_and_b32 v175, s75, v175\n"
      "v_sub_u32 v21, v21, v175\n"
      "v_bfe_i32 v175, v27, 14, 1\n"
      "v_and_b32 v175, s75, v175\n"
      "v_sub_u32 v22, v22, v175\n"
      "v_bfe_i32 v175, v27, 16, 1\n"
      "v_and_b32 v175, s75, v175\n"
      "v_sub_u32 v23, v23, v175\n"
      "v_bfe_i32 v175, v27, 18, 1\n"
      "v_and_b32 v175, s75, v175\n"
      "v_sub_u32 v25, v25, v175\n"
      "L_ax_25_%=:\n"
      "s_cmp_eq_u32 s76, 0\n"
      "s_cbranch_scc1 L_ax_26_%=\n"
      "v_bfe_i32 v175, v27, 11, 1\n"
      "v_and_b32 v175, s76, v175\n"
      "v_sub_u32_e64 v20, v20, v175\n"
      "v_bfe_i32 v175, v27, 13, 1\n"
      "v_and_b32 v175, s76, v175\n"
      "v_sub_u32 v21, v21, v175\n"
      "v_bfe_i32 v175, v27, 15, 1\n"
      "v_and_b32 v175, s76, v175\n"
      "v_sub_u32 v22, v22, v175\n"
      "v_bfe_i32 v175, v27, 17, 1\n"
      "v_and_b32 v175, s76, v175\n"
      "v_sub_u32 v23, v23, v175\n"
      "v_bfe_i32 v175, v27, 19, 1\n"
      "v_and_b32 v175, s76, v175\n"
      "v_sub_u32 v25, v25, v175\n"
      "L_ax_26_%=:\n"
      "s_or_b32 s48, s66, s69\n"
      "s_cmp_lg_u32 s48, 0\n"
      "s_cselect_b32 s48, 0x40000000, 0\n"
      "s_waitcnt lgkmcnt(0)\n"
      "s_nop 4\n"
      "s_branch L_back_28_%=\n"
      "L_rare_31_%=:\n"
      "s_nop 7\n"
      "s_waitcnt lgkmcnt(0)\n"
      "s_bitcmp1_b32 s85, 31\n"
      "s_cselect_b32 s75, 100270464, 0\n"
      "s_bitcmp1_b32 s85, 30\n"
      "s_cselect_b32 s76, 196224, 0\n"
      "s_and_b32 s74, s85, 0x3fffffff\n"
      "s_cmp_ge_u32 s85, 0x40000000\n"
      "s_cselect_b32 s74, s74, 0\n"
      "s_cselect_b32 s85, 0, s85\n"
      "s_sub_u32 s74, s74, s69\n"
      "s_add_u32 s69, s69, s74\n"
      "s_sub_u32 s75, s75, s70\n"
      "s_add_u32 s70, s70, s75\n"
      "s_sub_u32 s76, s76, s71\n"
      "s_add_u32 s71, s71, s76\n"
      "v_add_u32 v20, s74, v20\n"
      "v_add_u32 v21, s74, v21\n"
      "v_add_u32 v22, s74, v22\n"
      "v_add_u32 v23, s74, v23\n"
      "v_add_u32 v25, s74, v25\n"
      "s_cmp_eq_u32 s75, 0\n"
      "s_cbranch_scc1 L_ax_29_%=\n"
      "v_bfe_i32 v148, v27, 10, 1\n"
      "v_and_b32 v148, s75, v148\n"
      "v_sub_u32 v20, v20, v148\n"
      "v_bfe_i32 v148, v27, 12, 1\n"
      "v_and_b32 v148, s75, v148\n"
      "v_sub_u32 v21, v21, v148\n"
      "v_bfe_i32 v148, v27, 14, 1\n"
      "v_and_b32 v148, s75, v148\n"
      "v_sub_u32 v22, v22, v148\n"
      "v_bfe_i32 v148, v27, 16, 1\n"
      "v_and_b32 v148, s75, v148\n"
      "v_sub_u32 v23, v23, v148\n"
      "v_bfe_i32 v148, v27, 18, 1\n"
      "v_and_b32 v148, s75, v148\n"
      "v_sub_u32 v25, v25, v148\n"
      "L_ax_29_%=:\n"
      "s_cmp_eq_u32 s76, 0\n"
      "s_cbranch_scc1 L_ax_30_%=\n"
      "v_bfe_i32 v148, v27, 11, 1\n"
      "v_and_b32 v148, s76, v148\n"
      "v_sub_u32 v20, v20, v148\n"
      "v_bfe_i32 v148, v27, 13, 1\n"
      "v_and_b32 v148, s76, v148\n"
      "v_sub_u32 v21, v21, v148\n"
      "v_bfe_i32 v148, v27, 15, 1\n"
      "v_and_b32 v148, s76, v148\n"
      "v_sub_u32 v22, v22, v148\n"
      "v_bfe_i32 v148, v27, 17, 1\n"
      "v_and_b32 v148, s76, v148\n"
      "v_sub_u32 v23, v23, v148\n"
      "v_bfe_i32 v148, v27, 19, 1\n"
      "v_and_b32 v148, s76, v148\n"
      "v_sub_u32 v25, v25, v148\n"
      "L_ax_30_%=:\n"
      "s_or_b32 s48, s66, s69\n"
      "s_cmp_lg_u32 s48, 0\n"
      "s_cselect_b32 s48, 0x40000000, 0\n"
      "s_waitcnt lgkmcnt(0)\n"
      "s_nop 4\n"
      "s_branch L_back_32_%=\n"
      "L_rare_35_%=:\n"
      "s_nop 7\n"
      "s_waitcnt lgkmcnt(0)\n"
      "s_bitcmp1_b32 s46, 31\n"
      "s_cselect_b32 s75, 100270464, 0\n"
      "s_bitcmp1_b32 s46, 30\n"
      "s_cselect_b32 s76, 196224, 0\n"
      "s_and_b32 s74, s46, 0x3fffffff\n"
      "s_cmp_ge_u32 s46, 0x40000000\n"
      "s_cselect_b32 s74, s74, 0\n"
      "s_cselect_b32 s46, 0, s46\n"
      "s_sub_u32 s74, s74, s69\n"
      "s_add_u32 s69, s69, s74\n"
      "s_sub_u32 s75, s75, s70\n"
      "s_add_u32 s70, s70, s75\n"
      "s_sub_u32 s76, s76, s71\n"
      "s_add_u32 s71, s71, s76\n"
      "v_add_u32 v20, s74, v20\n"
      "v_add_u32 v21, s74, v21\n"
      "v_add_u32 v22, s74, v22\n"
      "v_add_u32 v23, s74, v23\n"
      "v_add_u32 v25, s74, v25\n"
      "s_cmp_eq_u32 s75, 0\n"
      "s_cbranch_scc1 L_ax_33_%=\n"
      "v_bfe_i32 v219, v27, 10, 1\n"
      "v_and_b32 v219, s75, v219\n"
      "v_sub_u32 v20, v20, v219\n"
      "v_bfe_i32 v219, v27, 12, 1\n"
      "v_and_b32 v219, s75, v219\n"
      "v_sub_u32 v21, v21, v219\n"
      "v_bfe_i32 v219, v27, 14, 1\n"
      "v_and_b32 v219, s75, v219\n"
      "v_sub_u32 v22, v22, v219\n"
      "v_bfe_i32 v219, v27, 16, 1\n"
      "v_and_b32 v219, s75, v219\n"
      "v_sub_u32 v23, v23, v219\n"
      "v_bfe_i32 v219, v27, 18, 1\n"
      "v_and_b32 v219, s75, v219\n"
      "v_sub_u32 v25, v25, v219\n"
      "L_ax_33_%=:\n"
      "s_cmp_eq_u32 s76, 0\n"
      "s_cbranch_scc1 L_ax_34_%=\n"
      "v_bfe_i32 v219, v27, 11, 1\n"
      "v_and_b32 v219, s76, v219\n"
      "v_sub_u32 v20, v20, v219\n"
      "v_bfe_i32 v219, v27, 13, 1\n"
      "v_and_b32 v219, s76, v219\n"
      "v_sub_u32 v21, v21, v219\n"
      "v_bfe_i32 v219, v27, 15, 1\n"
      "v_and_b32 v219, s76, v219\n"
      "v_sub_u32 v22, v22, v219\n"
      "v_bfe_i32 v219, v27, 17, 1\n"
      "v_and_b32 v219, s76, v219\n"
      "v_sub_u32 v23, v23, v219\n"
      "v_bfe_i32 v219, v27, 19, 1\n"
      "v_and_b32 v219, s76, v219\n"
      "v_sub_u32 v25, v25, v219\n"
      "L_ax_34_%=:\n"
      "s_or_b32 s48, s66, s69\n"
      "s_cmp_lg_u32 s48, 0\n"
      "s_cselect_b32 s48, 0x40000000, 0\n"
      "s_waitcnt lgkmcnt(0)\n"
      "s_nop 4\n"
      "s_branch L_back_36_%=\n"
      "L_rare_39_%=:\n"
      "s_nop 7\n"
      "s_waitcnt lgkmcnt(0)\n"
      "s_bitcmp1_b32 s46, 31\n"
      "s_cselect_b32 s75, 100270464, 0\n"
      "s_bitcmp1_b32 s46, 30\n"
      "s_cselect_b32 s76, 196224, 0\n"
      "s_and_b32 s74, s46, 0x3fffffff\n"
      "s_cmp_ge_u32 s46, 0x40000000\n"
      "s_cselect_b32 s74, s74, 0\n"
      "s_cselect_b32 s46, 0, s46\n"
      "s_sub_u32 s74, s74, s69\n"
      "s_add_u32 s69, s69, s74\n"
      "s_sub_u32 s75, s75, s70\n"
      "s_add_u32 s70, s70, s75\n"
      "s_sub_u32 s76, s76, s71\n"
      "s_add_u32 s71, s71, s76\n"
      "v_add_u32 v20, s74, v20\n"
      "v_add_u32 v21, s74, v21\n"
      "v_add_u32 v22, s74, v22\n"
      "v_add_u32 v23, s74, v23\n"
      "v_add_u32 v25, s74, v25\n"
      "s_cmp_eq_u32 s75, 0\n"
      "s_cbranch_scc1 L_ax_37_%=\n"
      "v_bfe_i32 v146, v27, 10, 1\n"
      "v_and_b32 v146, s75, v146\n"
      "v_sub_u32 v20, v20, v146\n"
      "v_bfe_i32 v146, v27, 12, 1\n"
      "v_and_b32 v146, s75, v146\n"
      "v_sub_u32 v21, v21, v146\n"
      "v_bfe_i32 v146, v27, 14, 1\n"
      "v_and_b32 v146, s75, v146\n"
      "v_sub_u32 v22, v22, v146\n"
      "v_bfe_i32 v146, v27, 16, 1\n"
      "v_and_b32 v146, s75, v146\n"
      "v_sub_u32 v23, v23, v146\n"
      "v_bfe_i32 v146, v27, 18, 1\n"
      "v_and_b32 v146, s75, v146\n"
      "v_sub_u32 v25, v25, v146\n"
      "L_ax_37_%=:\n"
      "s_cmp_eq_u32 s76, 0\n"
      "s_cbranch_scc1 L_ax_38_%=\n"
      "v_bfe_i32 v146, v27, 11, 1\n"
      "v_and_b32 v146, s76, v146\n"
      "v_sub_u32 v20, v20, v146\n"
      "v_bfe_i32 v146, v27, 13, 1\n"
      "v_and_b32 v146, s76, v146\n"
      "v_sub_u32 v21, v21, v146\n"
      "v_bfe_i32 v146, v27, 15, 1\n"
      "v_and_b32 v146, s76, v146\n"
      "v_sub_u32 v22, v22, v146\n"
      "v_bfe_i32 v146, v27, 17, 1\n"
      "v_and_b32 v146, s76, v146\n"
      "v_sub_u32 v23, v23, v146\n"
      "v_bfe_i32 v146, v27, 19, 1\n"
      "v_and_b32 v146, s76, v146\n"
      "v_sub_u32 v25, v25, v146\n"
      "L_ax_38_%=:\n"
      "s_or_b32 s48, s66, s69\n"
      "s_cmp_lg_u32 s48, 0\n"
      "s_cselect_b32 s48, 0x40000000, 0\n"
      "s_waitcnt lgkmcnt(0)\n"
      "s_nop 4\n"
      "s_branch L_back_40_%=\n"
      "L_cend_%=:\n"
      :
      : "s"(__builtin_amdgcn_kernarg_segment_ptr()), "s"(wave), "s"(blockIdx.x), "s"(CT_TAB), "s"(lds0)
      : "v0","v1","v2","v3","v4","v5","v6","v7","v8","v9","v10","v11","v12","v13","v14","v15","v16","v17","v18","v19","v20","v21","v22","v23","v24","v25","v26","v27","v28","v29","v30","v31","v32","v33","v34","v35","v36","v37","v38","v39","v40","v41","v42","v43","v44","v45","v46","v47","v48","v49","v50","v51","v52","v53","v54","v55","v56","v57","v58","v59","v60","v61","v62","v63","v64","v65","v66","v67","v68","v69","v70","v71","v72","v73","v74","v75","v76","v77","v78","v79","v80","v81","v82","v83","v84","v85","v86","v87","v88","v89","v90","v91","v92","v93","v94","v95","v96","v97","v98","v99","v100","v101","v102","v103","v104","v105","v106","v107","v108","v109","v110","v111","v112","v113","v114","v115","v116","v117","v118","v119","v120","v121","v122","v123","v124","v125","v126","v127","v128","v129","v130","v131","v132","v133","v134","v135","v136","v137","v138","v139","v140","v141","v142","v143","v144","v145","v146","v147","v148","v149","v150","v151","v152","v153","v154","v155","v156","v157","v158","v159","v160","v161","v162","v163","v164","v165","v166","v167","v168","v169","v170","v171","v172","v173","v174","v175","v176","v177","v178","v179","v180","v181","v182","v183","v184","v185","v186","v187","v188","v189","v190","v191","v192","v193","v194","v195","v196","v197","v198","v199","v200","v201","v202","v203","v204","v205","v206","v207","v208","v209","v210","v211","v212","v213","v214","v215","v216","v217","v218","v219","v220","v221","v222","v223","v224","v225","v226","v227","v228","v229","v230","v231","v232","v233","v234","v235","v236","v237","v238","v239","v240","v241","v242","v243","v244","v245","v246","v247","v248","v249","v250","v251","v252","v253","v254","v255","a0","a1","a2","a3","a4","a5","a6","a7","a8","a9","a10","a11","a12","a13","a14","a15","a16","a17","a18","a19","a20","a21","a22","a23","a24","a25","a26","a27","a28","a29","a30","a31","a32","a33","a34","a35","a36","a37","a38","a39","a40","a41","a42","a43","a44","a45","a46","a47","a48","a49","a50","a51","a52","a53","a54","a55","a56","a57","a58","a59","a60","a61","a62","a63","a64","a65","a66","a67","a68","a69","a70","a71","a72","a73","a74","a75","a76","a77","a78","a79","a80","a81","a82","a83","a84","a85","a86","a87","a88","a89","a90","a91","a92","a93","a94","a95","a96","a97","a98","a99","a100","a101","a102","a103","a104","a105","a106","a107","a108","a109","a110","a111","a112","a113","a114","a115","a116","a117","a118","a119","a120","a121","a122","a123","a124","a125","a126","a127","a128","a129","a130","a131","a132","a133","a134","a135","a136","a137","a138","a139","a140","a141","a142","a143","a144","a145","a146","a147","a148","a149","a150","a151","a152","a153","a154","a155","a156","a157","a158","a159","a160","a161","a162","a163","a164","a165","a166","a167","a168","a169","a170","a171","a172","a173","a174","a175","a176","a177","a178","a179","a180","a181","a182","a183","a184","a185","a186","a187","a188","a189","a190","a191","a192","a193","a194","a195","a196","a197","a198","a199","a200","a201","a202","a203","a204","a205","a206","a207","a208","a209","a210","a211","a212","a213","a214","a215","a216","a217","a218","a219","a220","a221","a222","a223","a224","a225","a226","a227","a228","a229","a230","a231","a232","a233","a234","a235","a236","a237","a238","a239","a240","a241","a242","a243","a244","a245","a246","a247","a248","a249","a250","a251","a252","a253","a254","a255","s16","s17","s18","s19","s20","s21","s22","s23","s24","s25","s26","s27","s28","s29","s30","s31","s33","s34","s35","s36","s37","s38","s39","s40","s41","s42","s43","s44","s45","s46","s47","s48","s49","s50","s51","s52","s53","s54","s55","s56","s57","s58","s59","s60","s61","s62","s63","s64","s65","s66","s67","s68","s69","s70","s71","s72","s73","s74","s75","s76","s77","s78","s79","s80","s81","s82","s83","s84","s85","s86","s87","s88","s89","s90","s91","s92","s93","s94","s95","s96","s97","s98","s99","s100","s101", "vcc", "scc", "memory");
}

}

extern "C" void kernel_launch(void* const* d_in, const int* in_sizes, int n_in, void* d_out, int out_size, void* d_ws,
                              size_t ws_size, hipStream_t stream) {
  const uint16_t* x = (const uint16_t*)d_in[0];
  const uint16_t* wqkv = (const uint16_t*)d_in[1];
  const uint16_t* wo = (const uint16_t*)d_in[2];
  const uint16_t* ls = (const uint16_t*)d_in[3];
  const uint16_t* w1 = (const uint16_t*)d_in[4];
  const uint16_t* w2 = (const uint16_t*)d_in[5];
  const uint16_t* rc = (const uint16_t*)d_in[6];
  const int* ridx = (const int*)d_in[7];
  const uint16_t* mask = (const uint16_t*)d_in[8];
  swin::swin_main<<<256, 256, 0, stream>>>(x, mask, (char*)d_ws, (uint16_t*)d_out, wqkv, wo, ls, w1, w2, rc, ridx);
}
```

```cpp
#include <hip/hip_runtime.h>
#include <hip/hip_bf16.h>
#include <stdint.h>

namespace swin {

constexpr unsigned CT_WORDS = 8192u;
constexpr unsigned LDS_BYTES = 163840u;

__device__ __attribute__((aligned(256))) const unsigned CT_TAB[CT_WORDS] = {0x4800u,0x4820u,0x4840u,0x4800u,0x4808u,0x4828u,0x4848u,0x4810u,0x4810u,0x4830u,0x4850u,0x4820u,0x4818u,0x4838u,0x4858u,0x4830u,0x4880u,0x48a0u,0x48c0u,0x4880u,0x4888u,0x48a8u,0x48c8u,0x4890u,0x4890u,0x48b0u,0x48d0u,0x48a0u,0x4898u,0x48b8u,0x48d8u,0x48b0u,0x4920u,0x4900u,0x4960u,0x4920u,0x4928u,0x4908u,0x4968u,0x4930u,0x4930u,0x4910u,0x4970u,0x4900u,0x4938u,0x4918u,0x4978u,0x4910u,0x49a0u,0x4980u,0x49e0u,0x49a0u,0x49a8u,0x4988u,0x49e8u,0x49b0u,0x49b0u,0x4990u,0x49f0u,0x4980u,0x49b8u,0x4998u,0x49f8u,0x4990u,0x4c40u,0x4c60u,0x4c00u,0x4a00u,0x4c48u,0x4c68u,0x4c08u,0x4a10u,0x4c50u,0x4c70u,0x4c10u,0x4a20u,0x4c58u,0x4c78u,0x4c18u,0x4a30u,0x4cc0u,0x4ce0u,0x4c80u,0x4a80u,0x4cc8u,0x4ce8u,0x4c88u,0x4a90u,0x4cd0u,0x4cf0u,0x4c90u,0x4aa0u,0x4cd8u,0x4cf8u,0x4c98u,0x4ab0u,0x4d60u,0x4d40u,0x4d20u,0x4b20u,0x4d68u,0x4d48u,0x4d28u,0x4b30u,0x4d70u,0x4d50u,0x4d30u,0x4b00u,0x4d78u,0x4d58u,0x4d38u,0x4b10u,0x4de0u,0x4dc0u,0x4da0u,0x4ba0u,0x4de8u,0x4dc8u,0x4da8u,0x4bb0u,0x4df0u,0x4dd0u,0x4db0u,0x4b80u,0x4df8u,0x4dd8u,0x4db8u,0x4b90u,0x5000u,0x5020u,0x5040u,0x4c40u,0x5008u,0x5028u,0x5048u,0x4c50u,0x5010u,0x5030u,0x5050u,0x4c60u,0x5018u,0x5038u,0x5058u,0x4c70u,0x5080u,0x50a0u,0x50c0u,0x4cc0u,0x5088u,0x50a8u,0x50c8u,0x4cd0u,0x5090u,0x50b0u,0x50d0u,0x4ce0u,0x5098u,0x50b8u,0x50d8u,0x4cf0u,0x5120u,0x5100u,0x5160u,0x4d60u,0x5128u,0x5108u,0x5168u,0x4d70u,0x5130u,0x5110u,0x5170u,0x4d40u,0x5138u,0x5118u,0x5178u,0x4d50u,0x51a0u,0x5180u,0x51e0u,0x4de0u,0x51a8u,0x5188u,0x51e8u,0x4df0u,0x51b0u,0x5190u,0x51f0u,0x4dc0u,0x51b8u,0x5198u,0x51f8u,0x4dd0u,0x5440u,0x5460u,0x5400u,0x4e40u,0x5448u,0x5468u,0x5408u,0x4e50u,0x5450u,0x5470u,0x5410u,0x4e60u,0x5458u,0x5478u,0x5418u,0x4e70u,0x54c0u,0x54e0u,0x5480u,0x4ec0u,0x54c8u,0x54e8u,0x5488u,0x4ed0u,0x54d0u,0x54f0u,0x5490u,0x4ee0u,0x54d8u,0x54f8u,0x5498u,0x4ef0u,0x5560u,0x5540u,0x5520u,0x4f60u,0x5568u,0x5548u,0x5528u,0x4f70u,0x5570u,0x5550u,0x5530u,0x4f40u,0x5578u,0x5558u,0x5538u,0x4f50u,0x55e0u,0x55c0u,0x55a0u,0x4fe0u,0x55e8u,0x55c8u,0x55a8u,0x4ff0u,0x55f0u,0x55d0u,0x55b0u,0x4fc0u,0x55f8u,0x55d8u,0x55b8u,0x4fd0u,0x4840u,0x1e800u,0x1eb00u,0x0u,0x4848u,0x1ec90u,0x1ef90u,0x10u,0x4850u,0x1f120u,0x1f420u,0x20u,0x4858u,0x1f5b0u,0x1f8b0u,0x30u,0x48c0u,0x1fa40u,0x1fd40u,0x40u,0x48c8u,0x1fed0u,0x201d0u,0x50u,0x48d0u,0x20360u,0x20660u,0x60u,0x48d8u,0x207f0u,0x20af0u,0x70u,0x4960u,0x20c80u,0x20f80u,0x180u,0x4968u,0x21110u,0x21410u,0x190u,0x4970u,0x215a0u,0x218a0u,0x1a0u,0x4978u,0x21a30u,0x21d30u,0x1b0u,0x49e0u,0x21ec0u,0x221c0u,0x1c0u,0x49e8u,0x22350u,0x22650u,0x1d0u,0x49f0u,0x227e0u,0x22ae0u,0x1e0u,0x49f8u,0x22c70u,0x22f70u,0x1f0u,0x4a40u,0x1e830u,0x1eb08u,0x320u,0x4a48u,0x1ecc0u,0x1ef98u,0x330u,0x4a50u,0x1f150u,0x1f428u,0x300u,0x4a58u,0x1f5e0u,0x1f8b8u,0x310u,0x4ac0u,0x1fa70u,0x1fd48u,0x360u,0x4ac8u,0x1ff00u,0x201d8u,0x370u,0x4ad0u,0x20390u,0x20668u,0x340u,0x4ad8u,0x20820u,0x20af8u,0x350u,0x4b60u,0x20cb0u,0x20f88u,0x4a0u,0x4b68u,0x21140u,0x21418u,0x4b0u,0x4b70u,0x215d0u,0x218a8u,0x480u,0x4b78u,0x21a60u,0x21d38u,0x490u,0x4be0u,0x21ef0u,0x221c8u,0x4e0u,0x4be8u,0x22380u,0x22658u,0x4f0u,0x4bf0u,0x22810u,0x22ae8u,0x4c0u,0x4bf8u,0x22ca0u,0x22f78u,0x4d0u,0x4c00u,0x1e860u,0x1eb10u,0x600u,0x4c08u,0x1ecf0u,0x1efa0u,0x610u,0x4c10u,0x1f180u,0x1f430u,0x620u,0x4c18u,0x1f610u,0x1f8c0u,0x630u,0x4c80u,0x1faa0u,0x1fd50u,0x640u,0x4c88u,0x1ff30u,0x201e0u,0x650u,0x4c90u,0x203c0u,0x20670u,0x660u,0x4c98u,0x20850u,0x20b00u,0x670u,0x4d20u,0x20ce0u,0x20f90u,0x780u,0x4d28u,0x21170u,0x21420u,0x790u,0x4d30u,0x21600u,0x218b0u,0x7a0u,0x4d38u,0x21a90u,0x21d40u,0x7b0u,0x4da0u,0x21f20u,0x221d0u,0x7c0u,0x4da8u,0x223b0u,0x22660u,0x7d0u,0x4db0u,0x22840u,0x22af0u,0x7e0u,0x4db8u,0x22cd0u,0x22f80u,0x7f0u,0x4e00u,0x1e890u,0x1eb18u,0x920u,0x4e08u,0x1ed20u,0x1efa8u,0x930u,0x4e10u,0x1f1b0u,0x1f438u,0x900u,0x4e18u,0x1f640u,0x1f8c8u,0x910u,0x4e80u,0x1fad0u,0x1fd58u,0x960u,0x4e88u,0x1ff60u,0x201e8u,0x970u,0x4e90u,0x203f0u,0x20678u,0x940u,0x4e98u,0x20880u,0x20b08u,0x950u,0x4f20u,0x20d10u,0x20f98u,0xaa0u,0x4f28u,0x211a0u,0x21428u,0xab0u,0x4f30u,0x21630u,0x218b8u,0xa80u,0x4f38u,0x21ac0u,0x21d48u,0xa90u,0x4fa0u,0x21f50u,0x221d8u,0xae0u,0x4fa8u,0x223e0u,0x22668u,0xaf0u,0x4fb0u,0x22870u,0x22af8u,0xac0u,0x4fb8u,0x22d00u,0x22f88u,0xad0u,0x0u,0x40u,0x9000u,0x9000u,0x80u,0xc0u,0x9010u,0x9010u,0x110u,0x150u,0x9020u,0x9020u,0x190u,0x1d0u,0x9030u,0x9030u,0x220u,0x260u,0x9040u,0x9040u,0x2a0u,0x2e0u,0x9050u,0x9050u,0x330u,0x370u,0x9060u,0x9060u,0x3b0u,0x3f0u,0x9070u,0x9070u,0x440u,0x400u,0x9080u,0x9080u,0x4c0u,0x480u,0x9090u,0x9090u,0x550u,0x510u,0x90a0u,0x90a0u,0x5d0u,0x590u,0x90b0u,0x90b0u,0x660u,0x620u,0x90c0u,0x90c0u,0x6e0u,0x6a0u,0x90d0u,0x90d0u,0x770u,0x730u,0x90e0u,0x90e0u,0x7f0u,0x7b0u,0x90f0u,0x90f0u,0x10u,0x50u,0x9300u,0x9300u,0x90u,0xd0u,0x9310u,0x9310u,0x100u,0x140u,0x9320u,0x9320u,0x180u,0x1c0u,0x9330u,0x9330u,0x230u,0x270u,0x9340u,0x9340u,0x2b0u,0x2f0u,0x9350u,0x9350u,0x320u,0x360u,0x9360u,0x9360u,0x3a0u,0x3e0u,0x9370u,0x9370u,0x450u,0x410u,0x9380u,0x9380u,0x4d0u,0x490u,0x9390u,0x9390u,0x540u,0x500u,0x93a0u,0x93a0u,0x5c0u,0x580u,0x93b0u,0x93b0u,0x670u,0x630u,0x93c0u,0x93c0u,0x6f0u,0x6b0u,0x93d0u,0x93d0u,0x760u,0x720u,0x93e0u,0x93e0u,0x7e0u,0x7a0u,0x93f0u,0x93f0u,0x20u,0x60u,0x9600u,0x9600u,0xa0u,0xe0u,0x9610u,0x9610u,0x130u,0x170u,0x9620u,0x9620u,0x1b0u,0x1f0u,0x9630u,0x9630u,0x200u,0x240u,0x9640u,0x9640u,0x280u,0x2c0u,0x9650u,0x9650u,0x310u,0x350u,0x9660u,0x9660u,0x390u,0x3d0u,0x9670u,0x9670u,0x460u,0x420u,0x9680u,0x9680u,0x4e0u,0x4a0u,0x9690u,0x9690u,0x570u,0x530u,0x96a0u,0x96a0u,0x5f0u,0x5b0u,0x96b0u,0x96b0u,0x640u,0x600u,0x96c0u,0x96c0u,0x6c0u,0x680u,0x96d0u,0x96d0u,0x750u,0x710u,0x96e0u,0x96e0u,0x7d0u,0x790u,0x96f0u,0x96f0u,0x30u,0x70u,0x9900u,0x9900u,0xb0u,0xf0u,0x9910u,0x9910u,0x120u,0x160u,0x9920u,0x9920u,0x1a0u,0x1e0u,0x9930u,0x9930u,0x210u,0x250u,0x9940u,0x9940u,0x290u,0x2d0u,0x9950u,0x9950u,0x300u,0x340u,0x9960u,0x9960u,0x380u,0x3c0u,0x9970u,0x9970u,0x470u,0x430u,0x9980u,0x9980u,0x4f0u,0x4b0u,0x9990u,0x9990u,0x560u,0x520u,0x99a0u,0x99a0u,0x5e0u,0x5a0u,0x99b0u,0x99b0u,0x650u,0x610u,0x99c0u,0x99c0u,0x6d0u,0x690u,0x99d0u,0x99d0u,0x740u,0x700u,0x99e0u,0x99e0u,0x7c0u,0x780u,0x99f0u,0x99f0u,0x9c00u,0x12000u,0x12040u,0x12000u,0x9c10u,0x12190u,0x121d0u,0x12010u,0x9c20u,0x12320u,0x12360u,0x12020u,0x9c30u,0x124b0u,0x124f0u,0x12030u,0x9c40u,0x12640u,0x12600u,0x12040u,0x9c50u,0x127d0u,0x12790u,0x12050u,0x9c60u,0x12960u,0x12920u,0x12060u,0x9c70u,0x12af0u,0x12ab0u,0x12070u,0x9c80u,0x12c00u,0x12c40u,0x12080u,0x9c90u,0x12d90u,0x12dd0u,0x12090u,0x9ca0u,0x12f20u,0x12f60u,0x120a0u,0x9cb0u,0x130b0u,0x130f0u,0x120b0u,0x9cc0u,0x13240u,0x13200u,0x120c0u,0x9cd0u,0x133d0u,0x13390u,0x120d0u,0x9ce0u,0x13560u,0x13520u,0x120e0u,0x9cf0u,0x136f0u,0x136b0u,0x120f0u,0x9f00u,0x12010u,0x12048u,0x12100u,0x9f10u,0x12180u,0x121d8u,0x12110u,0x9f20u,0x12330u,0x12368u,0x12120u,0x9f30u,0x124a0u,0x124f8u,0x12130u,0x9f40u,0x12650u,0x12608u,0x12140u,0x9f50u,0x127c0u,0x12798u,0x12150u,0x9f60u,0x12970u,0x12928u,0x12160u,0x9f70u,0x12ae0u,0x12ab8u,0x12170u,0x9f80u,0x12c10u,0x12c48u,0x12180u,0x9f90u,0x12d80u,0x12dd8u,0x12190u,0x9fa0u,0x12f30u,0x12f68u,0x121a0u,0x9fb0u,0x130a0u,0x130f8u,0x121b0u,0x9fc0u,0x13250u,0x13208u,0x121c0u,0x9fd0u,0x133c0u,0x13398u,0x121d0u,0x9fe0u,0x13570u,0x13528u,0x121e0u,0x9ff0u,0x136e0u,0x136b8u,0x121f0u,0xa200u,0x12020u,0x12050u,0x12200u,0xa210u,0x121b0u,0x121c0u,0x12210u,0xa220u,0x12300u,0x12370u,0x12220u,0xa230u,0x12490u,0x124e0u,0x12230u,0xa240u,0x12660u,0x12610u,0x12240u,0xa250u,0x127f0u,0x12780u,0x12250u,0xa260u,0x12940u,0x12930u,0x12260u,0xa270u,0x12ad0u,0x12aa0u,0x12270u,0xa280u,0x12c20u,0x12c50u,0x12280u,0xa290u,0x12db0u,0x12dc0u,0x12290u,0xa2a0u,0x12f00u,0x12f70u,0x122a0u,0xa2b0u,0x13090u,0x130e0u,0x122b0u,0xa2c0u,0x13260u,0x13210u,0x122c0u,0xa2d0u,0x133f0u,0x13380u,0x122d0u,0xa2e0u,0x13540u,0x13530u,0x122e0u,0xa2f0u,0x136d0u,0x136a0u,0x122f0u,0xa500u,0x12030u,0x12058u,0x12300u,0xa510u,0x121a0u,0x121c8u,0x12310u,0xa520u,0x12310u,0x12378u,0x12320u,0xa530u,0x12480u,0x124e8u,0x12330u,0xa540u,0x12670u,0x12618u,0x12340u,0xa550u,0x127e0u,0x12788u,0x12350u,0xa560u,0x12950u,0x12938u,0x12360u,0xa570u,0x12ac0u,0x12aa8u,0x12370u,0xa580u,0x12c30u,0x12c58u,0x12380u,0xa590u,0x12da0u,0x12dc8u,0x12390u,0xa5a0u,0x12f10u,0x12f78u,0x123a0u,0xa5b0u,0x13080u,0x130e8u,0x123b0u,0xa5c0u,0x13270u,0x13218u,0x123c0u,0xa5d0u,0x133e0u,0x13388u,0x123d0u,0xa5e0u,0x13550u,0x13538u,0x123e0u,0xa5f0u,0x136c0u,0x136a8u,0x123f0u,0x4800u,0x1c300u,0x1e800u,0x1eb00u,0x4880u,0x1c310u,0x100000u,0x1eb02u,0x4910u,0x1c320u,0x100000u,0x1eb04u,0x4990u,0x1c330u,0x100000u,0x1eb06u,0x4a20u,0x1c340u,0x1e800u,0x1eb08u,0x4aa0u,0x1c350u,0x100000u,0x1eb0au,0x4b30u,0x1c360u,0x100000u,0x1eb0cu,0x4bb0u,0x1c370u,0x100000u,0x1eb0eu,0x4c40u,0x1c380u,0x1e800u,0x1eb10u,0x4cc0u,0x1c390u,0x100000u,0x1eb12u,0x4d50u,0x1c3a0u,0x100000u,0x1eb14u,0x4dd0u,0x1c3b0u,0x100000u,0x1eb16u,0x4e60u,0x1c3c0u,0x1e800u,0x1eb18u,0x4ee0u,0x1c3d0u,0x100000u,0x1eb1au,0x4f70u,0x1c3e0u,0x100000u,0x1eb1cu,0x4ff0u,0x1c3f0u,0x100000u,0x1eb1eu,0x4810u,0x1c400u,0x1e830u,0x1eb00u,0x4890u,0x1c410u,0x100000u,0x1eb02u,0x4900u,0x1c420u,0x100000u,0x1eb04u,0x4980u,0x1c430u,0x100000u,0x1eb06u,0x4a30u,0x1c440u,0x1e830u,0x1eb08u,0x4ab0u,0x1c450u,0x100000u,0x1eb0au,0x4b20u,0x1c460u,0x100000u,0x1eb0cu,0x4ba0u,0x1c470u,0x100000u,0x1eb0eu,0x4c50u,0x1c480u,0x1e830u,0x1eb10u,0x4cd0u,0x1c490u,0x100000u,0x1eb12u,0x4d40u,0x1c4a0u,0x100000u,0x1eb14u,0x4dc0u,0x1c4b0u,0x100000u,0x1eb16u,0x4e70u,0x1c4c0u,0x1e830u,0x1eb18u,0x4ef0u,0x1c4d0u,0x100000u,0x1eb1au,0x4f60u,0x1c4e0u,0x100000u,0x1eb1cu,0x4fe0u,0x1c4f0u,0x100000u,0x1eb1eu,0x4820u,0x1c500u,0x1e860u,0x1eb00u,0x48a0u,0x1c510u,0x100000u,0x1eb02u,0x4930u,0x1c520u,0x100000u,0x1eb04u,0x49b0u,0x1c530u,0x100000u,0x1eb06u,0x4a00u,0x1c540u,0x1e860u,0x1eb08u,0x4a80u,0x1c550u,0x100000u,0x1eb0au,0x4b10u,0x1c560u,0x100000u,0x1eb0cu,0x4b90u,0x1c570u,0x100000u,0x1eb0eu,0x4c60u,0x1c580u,0x1e860u,0x1eb10u,0x4ce0u,0x1c590u,0x100000u,0x1eb12u,0x4d70u,0x1c5a0u,0x100000u,0x1eb14u,0x4df0u,0x1c5b0u,0x100000u,0x1eb16u,0x4e40u,0x1c5c0u,0x1e860u,0x1eb18u,0x4ec0u,0x1c5d0u,0x100000u,0x1eb1au,0x4f50u,0x1c5e0u,0x100000u,0x1eb1cu,0x4fd0u,0x1c5f0u,0x100000u,0x1eb1eu,0x4830u,0x1c600u,0x1e890u,0x1eb00u,0x48b0u,0x1c610u,0x100000u,0x1eb02u,0x4920u,0x1c620u,0x100000u,0x1eb04u,0x49a0u,0x1c630u,0x100000u,0x1eb06u,0x4a10u,0x1c640u,0x1e890u,0x1eb08u,0x4a90u,0x1c650u,0x100000u,0x1eb0au,0x4b00u,0x1c660u,0x100000u,0x1eb0cu,0x4b80u,0x1c670u,0x100000u,0x1eb0eu,0x4c70u,0x1c680u,0x1e890u,0x1eb10u,0x4cf0u,0x1c690u,0x100000u,0x1eb12u,0x4d60u,0x1c6a0u,0x100000u,0x1eb14u,0x4de0u,0x1c6b0u,0x100000u,0x1eb16u,0x4e50u,0x1c6c0u,0x1e890u,0x1eb18u,0x4ed0u,0x1c6d0u,0x100000u,0x1eb1au,0x4f40u,0x1c6e0u,0x100000u,0x1eb1cu,0x4fc0u,0x1c6f0u,0x100000u,0x1eb1eu,0x24d00u,0x1d300u,0x1d300u,0xc0100u,0x24d02u,0x1d302u,0x100000u,0xc0110u,0x24d04u,0x1d304u,0x100000u,0xc0120u,0x24d06u,0x1d306u,0x100000u,0xc0130u,0x24f00u,0x1d310u,0x1d300u,0xc0140u,0x24f02u,0x1d312u,0x100000u,0xc0150u,0x24f04u,0x1d314u,0x100000u,0xc0160u,0x24f06u,0x1d316u,0x100000u,0xc0170u,0x25100u,0x1d320u,0x1d300u,0xc0280u,0x25102u,0x1d322u,0x100000u,0xc0290u,0x25104u,0x1d324u,0x100000u,0xc02a0u,0x25106u,0x1d326u,0x100000u,0xc02b0u,0x25300u,0x1d330u,0x1d300u,0xc02c0u,0x25302u,0x1d332u,0x100000u,0xc02d0u,0x25304u,0x1d334u,0x100000u,0xc02e0u,0x25306u,0x1d336u,0x100000u,0xc02f0u,0x24d08u,0x1d308u,0x1d310u,0xc0410u,0x24d0au,0x1d30au,0x100000u,0xc0400u,0x24d0cu,0x1d30cu,0x100000u,0xc0430u,0x24d0eu,0x1d30eu,0x100000u,0xc0420u,0x24f08u,0x1d318u,0x1d310u,0xc0450u,0x24f0au,0x1d31au,0x100000u,0xc0440u,0x24f0cu,0x1d31cu,0x100000u,0xc0470u,0x24f0eu,0x1d31eu,0x100000u,0xc0460u,0x25108u,0x1d328u,0x1d310u,0xef990u,0x2510au,0x1d32au,0x100000u,0xef980u,0x2510cu,0x1d32cu,0x100000u,0xef9b0u,0x2510eu,0x1d32eu,0x100000u,0xef9a0u,0x25308u,0x1d338u,0x1d310u,0xef9d0u,0x2530au,0x1d33au,0x100000u,0xef9c0u,0x2530cu,0x1d33cu,0x100000u,0xef9f0u,0x2530eu,0x1d33eu,0x100000u,0xef9e0u,0x25500u,0x1d340u,0x1d320u,0xefb20u,0x25502u,0x1d342u,0x100000u,0xefb30u,0x25504u,0x1d344u,0x100000u,0xefb00u,0x25506u,0x1d346u,0x100000u,0xefb10u,0x25700u,0x1d350u,0x1d320u,0xefb60u,0x25702u,0x1d352u,0x100000u,0xefb70u,0x25704u,0x1d354u,0x100000u,0xefb40u,0x25706u,0x1d356u,0x100000u,0xefb50u,0x25900u,0x1d360u,0x1d320u,0xefca0u,0x25902u,0x1d362u,0x100000u,0xefcb0u,0x25904u,0x1d364u,0x100000u,0xefc80u,0x25906u,0x1d366u,0x100000u,0xefc90u,0x25b00u,0x1d370u,0x1d320u,0xefce0u,0x25b02u,0x1d372u,0x100000u,0xefcf0u,0x25b04u,0x1d374u,0x100000u,0xefcc0u,0x25b06u,0x1d376u,0x100000u,0xefcd0u,0x100000u,0x1d348u,0x1d330u,0xefe30u,0x100000u,0x1d34au,0x100000u,0xefe20u,0x100000u,0x1d34cu,0x100000u,0xefe10u,0x100000u,0x1d34eu,0x100000u,0xefe00u,0x100000u,0x1d358u,0x1d330u,0xefe70u,0x100000u,0x1d35au,0x100000u,0xefe60u,0x100000u,0x1d35cu,0x100000u,0xefe50u,0x100000u,0x1d35eu,0x100000u,0xefe40u,0x100000u,0x1d368u,0x1d330u,0xeffb0u,0x100000u,0x1d36au,0x100000u,0xeffa0u,0x100000u,0x1d36cu,0x100000u,0xeff90u,0x100000u,0x1d36eu,0x100000u,0xeff80u,0x100000u,0x1d378u,0x1d330u,0xefff0u,0x100000u,0x1d37au,0x100000u,0xeffe0u,0x100000u,0x1d37cu,0x100000u,0xeffd0u,0x100000u,0x1d37eu,0x100000u,0xeffc0u,0x0u,0x30420u,0x8fc50u,0xc0000u,0x10u,0x30430u,0x8fc40u,0xc0010u,0x20u,0x30400u,0x8fc70u,0xc0020u,0x30u,0x30410u,0x8fc60u,0xc0030u,0x40u,0x30460u,0x8fc10u,0xc0040u,0x50u,0x30470u,0x8fc00u,0xc0050u,0x60u,0x30440u,0x8fc30u,0xc0060u,0x70u,0x30450u,0x8fc20u,0xc0070u,0x80u,0x304b0u,0x8fcd0u,0xc0080u,0x90u,0x304a0u,0x8fcc0u,0xc0090u,0xa0u,0x30490u,0x8fcf0u,0xc00a0u,0xb0u,0x30480u,0x8fce0u,0xc00b0u,0xc0u,0x304f0u,0x8fc90u,0xc00c0u,0xd0u,0x304e0u,0x8fc80u,0xc00d0u,0xe0u,0x304d0u,0x8fcb0u,0xc00e0u,0xf0u,0x304c0u,0x8fca0u,0xc00f0u,0x100u,0x30530u,0x8fd60u,0xc0100u,0x110u,0x30520u,0x8fd70u,0xc0110u,0x120u,0x30510u,0x8fd40u,0xc0120u,0x130u,0x30500u,0x8fd50u,0xc0130u,0x140u,0x30570u,0x8fd20u,0xc0140u,0x150u,0x30560u,0x8fd30u,0xc0150u,0x160u,0x30550u,0x8fd00u,0xc0160u,0x170u,0x30540u,0x8fd10u,0xc0170u,0x190u,0x305b0u,0x8fde0u,0xc0190u,0x180u,0x305a0u,0x8fdf0u,0xc0180u,0x1b0u,0x30590u,0x8fdc0u,0xc01b0u,0x1a0u,0x30580u,0x8fdd0u,0xc01a0u,0x1d0u,0x305f0u,0x8fda0u,0xc01d0u,0x1c0u,0x305e0u,0x8fdb0u,0xc01c0u,0x1f0u,0x305d0u,0x8fd80u,0xc01f0u,0x1e0u,0x305c0u,0x8fd90u,0xc01e0u,0x210u,0x30640u,0x8fe60u,0xc0210u,0x200u,0x30650u,0x8fe70u,0xc0200u,0x230u,0x30660u,0x8fe40u,0xc0230u,0x220u,0x30670u,0x8fe50u,0xc0220u,0x250u,0x30600u,0x8fe20u,0xc0250u,0x240u,0x30610u,0x8fe30u,0xc0240u,0x270u,0x30620u,0x8fe00u,0xc0270u,0x260u,0x30630u,0x8fe10u,0xc0260u,0x290u,0x306c0u,0x8fef0u,0xc0290u,0x280u,0x306d0u,0x8fee0u,0xc0280u,0x2b0u,0x306e0u,0x8fed0u,0xc02b0u,0x2a0u,0x306f0u,0x8fec0u,0xc02a0u,0x2d0u,0x30680u,0x8feb0u,0xc02d0u,0x2c0u,0x30690u,0x8fea0u,0xc02c0u,0x2f0u,0x306a0u,0x8fe90u,0xc02f0u,0x2e0u,0x306b0u,0x8fe80u,0xc02e0u,0x320u,0x30740u,0x8ff70u,0xc0320u,0x330u,0x30750u,0x8ff60u,0xc0330u,0x300u,0x30760u,0x8ff50u,0xc0300u,0x310u,0x30770u,0x8ff40u,0xc0310u,0x360u,0x30700u,0x8ff30u,0xc0360u,0x370u,0x30710u,0x8ff20u,0xc0370u,0x340u,0x30720u,0x8ff10u,0xc0340u,0x350u,0x30730u,0x8ff00u,0xc0350u,0x3a0u,0x307d0u,0x8fff0u,0xc03a0u,0x3b0u,0x307c0u,0x8ffe0u,0xc03b0u,0x380u,0x307f0u,0x8ffd0u,0xc0380u,0x390u,0x307e0u,0x8ffc0u,0xc0390u,0x3e0u,0x30790u,0x8ffb0u,0xc03e0u,0x3f0u,0x30780u,0x8ffa0u,0xc03f0u,0x3c0u,0x307b0u,0x8ff90u,0xc03c0u,0x3d0u,0x307a0u,0x8ff80u,0xc03d0u,0x0u,0x11f820u,0x0u,0x7030cu,0x0u,0x11f830u,0x0u,0x7030cu,0x0u,0x11f800u,0x0u,0x7030cu,0x0u,0x11f810u,0x0u,0x7030cu,0x0u,0x11f860u,0x0u,0x7030cu,0x0u,0x11f870u,0x0u,0x7030cu,0x0u,0x11f840u,0x0u,0x7030cu,0x0u,0x11f850u,0x0u,0x7030cu,0x0u,0x11f8b0u,0x0u,0x7230cu,0x0u,0x11f8a0u,0x0u,0x7230cu,0x0u,0x11f890u,0x0u,0x7230cu,0x0u,0x11f880u,0x0u,0x7230cu,0x0u,0x11f8f0u,0x0u,0x7230cu,0x0u,0x11f8e0u,0x0u,0x7230cu,0x0u,0x11f8d0u,0x0u,0x7230cu,0x0u,0x11f8c0u,0x0u,0x7230cu,0x0u,0x11f930u,0x0u,0x7232cu,0x0u,0x11f920u,0x0u,0x7232cu,0x0u,0x11f910u,0x0u,0x7232cu,0x0u,0x11f900u,0x0u,0x7232cu,0x0u,0x11f970u,0x0u,0x7232cu,0x0u,0x11f960u,0x0u,0x7232cu,0x0u,0x11f950u,0x0u,0x7232cu,0x0u,0x11f940u,0x0u,0x7232cu,0x0u,0x11f9b0u,0x0u,0x72124u,0x0u,0x11f9a0u,0x0u,0x72124u,0x0u,0x11f990u,0x0u,0x72124u,0x0u,0x11f980u,0x0u,0x72124u,0x0u,0x11f9f0u,0x0u,0x72124u,0x0u,0x11f9e0u,0x0u,0x72124u,0x0u,0x11f9d0u,0x0u,0x72124u,0x0u,0x11f9c0u,0x0u,0x72124u,0x0u,0x11fa40u,0x0u,0x721a6u,0x0u,0x11fa50u,0x0u,0x721a6u,0x0u,0x11fa60u,0x0u,0x721a6u,0x0u,0x11fa70u,0x0u,0x721a6u,0x0u,0x11fa00u,0x0u,0x721a6u,0x0u,0x11fa10u,0x0u,0x721a6u,0x0u,0x11fa20u,0x0u,0x721a6u,0x0u,0x11fa30u,0x0u,0x721a6u,0x0u,0x11fac0u,0x0u,0x72186u,0x0u,0x11fad0u,0x0u,0x72186u,0x0u,0x11fae0u,0x0u,0x72186u,0x0u,0x11faf0u,0x0u,0x72186u,0x0u,0x11fa80u,0x0u,0x72186u,0x0u,0x11fa90u,0x0u,0x72186u,0x0u,0x11faa0u,0x0u,0x72186u,0x0u,0x11fab0u,0x0u,0x72186u,0x0u,0x11fb40u,0x0u,0x72186u,0x0u,0x11fb50u,0x0u,0x72186u,0x0u,0x11fb60u,0x0u,0x72186u,0x0u,0x11fb70u,0x0u,0x72186u,0x0u,0x11fb00u,0x0u,0x72186u,0x0u,0x11fb10u,0x0u,0x72186u,0x0u,0x11fb20u,0x0u,0x72186u,0x0u,0x11fb30u,0x0u,0x72186u,0x0u,0x11fbd0u,0x0u,0x7230cu,0x0u,0x11fbc0u,0x0u,0x7230cu,0x0u,0x11fbf0u,0x0u,0x7230cu,0x0u,0x11fbe0u,0x0u,0x7230cu,0x0u,0x11fb90u,0x0u,0x7230cu,0x0u,0x11fb80u,0x0u,0x7230cu,0x0u,0x11fbb0u,0x0u,0x7230cu,0x0u,0x11fba0u,0x0u,0x7230cu,0x5800u,0x5820u,0x4860u,0x4860u,0x5808u,0x5828u,0x4868u,0x4870u,0x5810u,0x5830u,0x4870u,0x5800u,0x5818u,0x5838u,0x4878u,0x5810u,0x5880u,0x58a0u,0x48e0u,0x48e0u,0x5888u,0x58a8u,0x48e8u,0x48f0u,0x5890u,0x58b0u,0x48f0u,0x5880u,0x5898u,0x58b8u,0x48f8u,0x5890u,0x5920u,0x5900u,0x4940u,0x4940u,0x5928u,0x5908u,0x4948u,0x4950u,0x5930u,0x5910u,0x4950u,0x5920u,0x5938u,0x5918u,0x4958u,0x5930u,0x59a0u,0x5980u,0x49c0u,0x49c0u,0x59a8u,0x5988u,0x49c8u,0x49d0u,0x59b0u,0x5990u,0x49d0u,0x59a0u,0x59b8u,0x5998u,0x49d8u,0x59b0u,0x5c40u,0x5c60u,0x4c20u,0x4a60u,0x5c48u,0x5c68u,0x4c28u,0x4a70u,0x5c50u,0x5c70u,0x4c30u,0x5a00u,0x5c58u,0x5c78u,0x4c38u,0x5a10u,0x5cc0u,0x5ce0u,0x4ca0u,0x4ae0u,0x5cc8u,0x5ce8u,0x4ca8u,0x4af0u,0x5cd0u,0x5cf0u,0x4cb0u,0x5a80u,0x5cd8u,0x5cf8u,0x4cb8u,0x5a90u,0x5d60u,0x5d40u,0x4d00u,0x4b40u,0x5d68u,0x5d48u,0x4d08u,0x4b50u,0x5d70u,0x5d50u,0x4d10u,0x5b20u,0x5d78u,0x5d58u,0x4d18u,0x5b30u,0x5de0u,0x5dc0u,0x4d80u,0x4bc0u,0x5de8u,0x5dc8u,0x4d88u,0x4bd0u,0x5df0u,0x5dd0u,0x4d90u,0x5ba0u,0x5df8u,0x5dd8u,0x4d98u,0x5bb0u,0x6000u,0x6020u,0x5060u,0x4c20u,0x6008u,0x6028u,0x5068u,0x4c30u,0x6010u,0x6030u,0x5070u,0x5c40u,0x6018u,0x6038u,0x5078u,0x5c50u,0x6080u,0x60a0u,0x50e0u,0x4ca0u,0x6088u,0x60a8u,0x50e8u,0x4cb0u,0x6090u,0x60b0u,0x50f0u,0x5cc0u,0x6098u,0x60b8u,0x50f8u,0x5cd0u,0x6120u,0x6100u,0x5140u,0x4d00u,0x6128u,0x6108u,0x5148u,0x4d10u,0x6130u,0x6110u,0x5150u,0x5d60u,0x6138u,0x6118u,0x5158u,0x5d70u,0x61a0u,0x6180u,0x51c0u,0x4d80u,0x61a8u,0x6188u,0x51c8u,0x4d90u,0x61b0u,0x6190u,0x51d0u,0x5de0u,0x61b8u,0x6198u,0x51d8u,0x5df0u,0x6440u,0x6460u,0x5420u,0x4e20u,0x6448u,0x6468u,0x5428u,0x4e30u,0x6450u,0x6470u,0x5430u,0x5e40u,0x6458u,0x6478u,0x5438u,0x5e50u,0x64c0u,0x64e0u,0x54a0u,0x4ea0u,0x64c8u,0x64e8u,0x54a8u,0x4eb0u,0x64d0u,0x64f0u,0x54b0u,0x5ec0u,0x64d8u,0x64f8u,0x54b8u,0x5ed0u,0x6560u,0x6540u,0x5500u,0x4f00u,0x6568u,0x6548u,0x5508u,0x4f10u,0x6570u,0x6550u,0x5510u,0x5f60u,0x6578u,0x6558u,0x5518u,0x5f70u,0x65e0u,0x65c0u,0x5580u,0x4f80u,0x65e8u,0x65c8u,0x5588u,0x4f90u,0x65f0u,0x65d0u,0x5590u,0x5fe0u,0x65f8u,0x65d8u,0x5598u,0x5ff0u,0x5820u,0x1e8c0u,0x1eb60u,0xc40u,0x5828u,0x1ed50u,0x1eff0u,0xc50u,0x5830u,0x1f1e0u,0x1f480u,0xc60u,0x5838u,0x1f670u,0x1f910u,0xc70u,0x58a0u,0x1fb00u,0x1fda0u,0xc00u,0x58a8u,0x1ff90u,0x20230u,0xc10u,0x58b0u,0x20420u,0x206c0u,0xc20u,0x58b8u,0x208b0u,0x20b50u,0xc30u,0x5900u,0x20d40u,0x20fe0u,0xdc0u,0x5908u,0x211d0u,0x21470u,0xdd0u,0x5910u,0x21660u,0x21900u,0xde0u,0x5918u,0x21af0u,0x21d90u,0xdf0u,0x5980u,0x21f80u,0x22220u,0xd80u,0x5988u,0x22410u,0x226b0u,0xd90u,0x5990u,0x228a0u,0x22b40u,0xda0u,0x5998u,0x22d30u,0x22fd0u,0xdb0u,0x5a20u,0x1e8f0u,0x1eb68u,0xf60u,0x5a28u,0x1ed80u,0x1eff8u,0xf70u,0x5a30u,0x1f210u,0x1f488u,0xf40u,0x5a38u,0x1f6a0u,0x1f918u,0xf50u,0x5aa0u,0x1fb30u,0x1fda8u,0xf20u,0x5aa8u,0x1ffc0u,0x20238u,0xf30u,0x5ab0u,0x20450u,0x206c8u,0xf00u,0x5ab8u,0x208e0u,0x20b58u,0xf10u,0x5b00u,0x20d70u,0x20fe8u,0x10e0u,0x5b08u,0x21200u,0x21478u,0x10f0u,0x5b10u,0x21690u,0x21908u,0x10c0u,0x5b18u,0x21b20u,0x21d98u,0x10d0u,0x5b80u,0x21fb0u,0x22228u,0x10a0u,0x5b88u,0x22440u,0x226b8u,0x10b0u,0x5b90u,0x228d0u,0x22b48u,0x1080u,0x5b98u,0x22d60u,0x22fd8u,0x1090u,0x5c60u,0x1e920u,0x1eb70u,0x1240u,0x5c68u,0x1edb0u,0x1f000u,0x1250u,0x5c70u,0x1f240u,0x1f490u,0x1260u,0x5c78u,0x1f6d0u,0x1f920u,0x1270u,0x5ce0u,0x1fb60u,0x1fdb0u,0x1200u,0x5ce8u,0x1fff0u,0x20240u,0x1210u,0x5cf0u,0x20480u,0x206d0u,0x1220u,0x5cf8u,0x20910u,0x20b60u,0x1230u,0x5d40u,0x20da0u,0x20ff0u,0x13c0u,0x5d48u,0x21230u,0x21480u,0x13d0u,0x5d50u,0x216c0u,0x21910u,0x13e0u,0x5d58u,0x21b50u,0x21da0u,0x13f0u,0x5dc0u,0x21fe0u,0x22230u,0x1380u,0x5dc8u,0x22470u,0x226c0u,0x1390u,0x5dd0u,0x22900u,0x22b50u,0x13a0u,0x5dd8u,0x22d90u,0x22fe0u,0x13b0u,0x5e60u,0x1e950u,0x1eb78u,0x1560u,0x5e68u,0x1ede0u,0x1f008u,0x1570u,0x5e70u,0x1f270u,0x1f498u,0x1540u,0x5e78u,0x1f700u,0x1f928u,0x1550u,0x5ee0u,0x1fb90u,0x1fdb8u,0x1520u,0x5ee8u,0x20020u,0x20248u,0x1530u,0x5ef0u,0x204b0u,0x206d8u,0x1500u,0x5ef8u,0x20940u,0x20b68u,0x1510u,0x5f40u,0x20dd0u,0x20ff8u,0x16e0u,0x5f48u,0x21260u,0x21488u,0x16f0u,0x5f50u,0x216f0u,0x21918u,0x16c0u,0x5f58u,0x21b80u,0x21da8u,0x16d0u,0x5fc0u,0x22010u,0x22238u,0x16a0u,0x5fc8u,0x224a0u,0x226c8u,0x16b0u,0x5fd0u,0x22930u,0x22b58u,0x1680u,0x5fd8u,0x22dc0u,0x22fe8u,0x1690u,0x0u,0x40u,0x9000u,0xa800u,0x80u,0xc0u,0x9010u,0xa810u,0x110u,0x150u,0x9020u,0xa820u,0x190u,0x1d0u,0x9030u,0xa830u,0x220u,0x260u,0x9040u,0xa840u,0x2a0u,0x2e0u,0x9050u,0xa850u,0x330u,0x370u,0x9060u,0xa860u,0x3b0u,0x3f0u,0x9070u,0xa870u,0x440u,0x400u,0x9080u,0xa880u,0x4c0u,0x480u,0x9090u,0xa890u,0x550u,0x510u,0x90a0u,0xa8a0u,0x5d0u,0x590u,0x90b0u,0xa8b0u,0x660u,0x620u,0x90c0u,0xa8c0u,0x6e0u,0x6a0u,0x90d0u,0xa8d0u,0x770u,0x730u,0x90e0u,0xa8e0u,0x7f0u,0x7b0u,0x90f0u,0xa8f0u,0x10u,0x50u,0x9300u,0xab00u,0x90u,0xd0u,0x9310u,0xab10u,0x100u,0x140u,0x9320u,0xab20u,0x180u,0x1c0u,0x9330u,0xab30u,0x230u,0x270u,0x9340u,0xab40u,0x2b0u,0x2f0u,0x9350u,0xab50u,0x320u,0x360u,0x9360u,0xab60u,0x3a0u,0x3e0u,0x9370u,0xab70u,0x450u,0x410u,0x9380u,0xab80u,0x4d0u,0x490u,0x9390u,0xab90u,0x540u,0x500u,0x93a0u,0xaba0u,0x5c0u,0x580u,0x93b0u,0xabb0u,0x670u,0x630u,0x93c0u,0xabc0u,0x6f0u,0x6b0u,0x93d0u,0xabd0u,0x760u,0x720u,0x93e0u,0xabe0u,0x7e0u,0x7a0u,0x93f0u,0xabf0u,0x20u,0x60u,0x9600u,0xae00u,0xa0u,0xe0u,0x9610u,0xae10u,0x130u,0x170u,0x9620u,0xae20u,0x1b0u,0x1f0u,0x9630u,0xae30u,0x200u,0x240u,0x9640u,0xae40u,0x280u,0x2c0u,0x9650u,0xae50u,0x310u,0x350u,0x9660u,0xae60u,0x390u,0x3d0u,0x9670u,0xae70u,0x460u,0x420u,0x9680u,0xae80u,0x4e0u,0x4a0u,0x9690u,0xae90u,0x570u,0x530u,0x96a0u,0xaea0u,0x5f0u,0x5b0u,0x96b0u,0xaeb0u,0x640u,0x600u,0x96c0u,0xaec0u,0x6c0u,0x680u,0x96d0u,0xaed0u,0x750u,0x710u,0x96e0u,0xaee0u,0x7d0u,0x790u,0x96f0u,0xaef0u,0x30u,0x70u,0x9900u,0xb100u,0xb0u,0xf0u,0x9910u,0xb110u,0x120u,0x160u,0x9920u,0xb120u,0x1a0u,0x1e0u,0x9930u,0xb130u,0x210u,0x250u,0x9940u,0xb140u,0x290u,0x2d0u,0x9950u,0xb150u,0x300u,0x340u,0x9960u,0xb160u,0x380u,0x3c0u,0x9970u,0xb170u,0x470u,0x430u,0x9980u,0xb180u,0x4f0u,0x4b0u,0x9990u,0xb190u,0x560u,0x520u,0x99a0u,0xb1a0u,0x5e0u,0x5a0u,0x99b0u,0xb1b0u,0x650u,0x610u,0x99c0u,0xb1c0u,0x6d0u,0x690u,0x99d0u,0xb1d0u,0x740u,0x700u,0x99e0u,0xb1e0u,0x7c0u,0x780u,0x99f0u,0xb1f0u,0x9c08u,0x12060u,0x120a0u,0x12400u,0x9c18u,0x121f0u,0x12230u,0x12410u,0x9c28u,0x12340u,0x12380u,0x12420u,0x9c38u,0x124d0u,0x12510u,0x12430u,0x9c48u,0x12620u,0x126e0u,0x12440u,0x9c58u,0x127b0u,0x12870u,0x12450u,0x9c68u,0x12900u,0x129c0u,0x12460u,0x9c78u,0x12a90u,0x12b50u,0x12470u,0x9c88u,0x12c60u,0x12ca0u,0x12480u,0x9c98u,0x12df0u,0x12e30u,0x12490u,0x9ca8u,0x12f40u,0x12f80u,0x124a0u,0x9cb8u,0x130d0u,0x13110u,0x124b0u,0x9cc8u,0x13220u,0x132e0u,0x124c0u,0x9cd8u,0x133b0u,0x13470u,0x124d0u,0x9ce8u,0x13500u,0x135c0u,0x124e0u,0x9cf8u,0x13690u,0x13750u,0x124f0u,0x9f08u,0x12070u,0x120a8u,0x12500u,0x9f18u,0x121e0u,0x12238u,0x12510u,0x9f28u,0x12350u,0x12388u,0x12520u,0x9f38u,0x124c0u,0x12518u,0x12530u,0x9f48u,0x12630u,0x126e8u,0x12540u,0x9f58u,0x127a0u,0x12878u,0x12550u,0x9f68u,0x12910u,0x129c8u,0x12560u,0x9f78u,0x12a80u,0x12b58u,0x12570u,0x9f88u,0x12c70u,0x12ca8u,0x12580u,0x9f98u,0x12de0u,0x12e38u,0x12590u,0x9fa8u,0x12f50u,0x12f88u,0x125a0u,0x9fb8u,0x130c0u,0x13118u,0x125b0u,0x9fc8u,0x13230u,0x132e8u,0x125c0u,0x9fd8u,0x133a0u,0x13478u,0x125d0u,0x9fe8u,0x13510u,0x135c8u,0x125e0u,0x9ff8u,0x13680u,0x13758u,0x125f0u,0xa208u,0x12080u,0x120b0u,0x12600u,0xa218u,0x12210u,0x12220u,0x12610u,0xa228u,0x123a0u,0x12390u,0x12620u,0xa238u,0x12530u,0x12500u,0x12630u,0xa248u,0x126c0u,0x126f0u,0x12640u,0xa258u,0x12850u,0x12860u,0x12650u,0xa268u,0x129e0u,0x129d0u,0x12660u,0xa278u,0x12b70u,0x12b40u,0x12670u,0xa288u,0x12c80u,0x12cb0u,0x12680u,0xa298u,0x12e10u,0x12e20u,0x12690u,0xa2a8u,0x12fa0u,0x12f90u,0x126a0u,0xa2b8u,0x13130u,0x13100u,0x126b0u,0xa2c8u,0x132c0u,0x132f0u,0x126c0u,0xa2d8u,0x13450u,0x13460u,0x126d0u,0xa2e8u,0x135e0u,0x135d0u,0x126e0u,0xa2f8u,0x13770u,0x13740u,0x126f0u,0xa508u,0x12090u,0x120b8u,0x12700u,0xa518u,0x12200u,0x12228u,0x12710u,0xa528u,0x123b0u,0x12398u,0x12720u,0xa538u,0x12520u,0x12508u,0x12730u,0xa548u,0x126d0u,0x126f8u,0x12740u,0xa558u,0x12840u,0x12868u,0x12750u,0xa568u,0x129f0u,0x129d8u,0x12760u,0xa578u,0x12b60u,0x12b48u,0x12770u,0xa588u,0x12c90u,0x12cb8u,0x12780u,0xa598u,0x12e00u,0x12e28u,0x12790u,0xa5a8u,0x12fb0u,0x12f98u,0x127a0u,0xa5b8u,0x13120u,0x13108u,0x127b0u,0xa5c8u,0x132d0u,0x132f8u,0x127c0u,0xa5d8u,0x13440u,0x13468u,0x127d0u,0xa5e8u,0x135f0u,0x135d8u,0x127e0u,0xa5f8u,0x13760u,0x13748u,0x127f0u,0x5000u,0x1c300u,0x1e8c0u,0x1eb60u,0x5080u,0x1c310u,0x100000u,0x1eb62u,0x5110u,0x1c320u,0x100000u,0x1eb64u,0x5190u,0x1c330u,0x100000u,0x1eb66u,0x5220u,0x1c340u,0x1e8c0u,0x1eb68u,0x52a0u,0x1c350u,0x100000u,0x1eb6au,0x5330u,0x1c360u,0x100000u,0x1eb6cu,0x53b0u,0x1c370u,0x100000u,0x1eb6eu,0x5440u,0x1c380u,0x1e8c0u,0x1eb70u,0x54c0u,0x1c390u,0x100000u,0x1eb72u,0x5550u,0x1c3a0u,0x100000u,0x1eb74u,0x55d0u,0x1c3b0u,0x100000u,0x1eb76u,0x5660u,0x1c3c0u,0x1e8c0u,0x1eb78u,0x56e0u,0x1c3d0u,0x100000u,0x1eb7au,0x5770u,0x1c3e0u,0x100000u,0x1eb7cu,0x57f0u,0x1c3f0u,0x100000u,0x1eb7eu,0x5010u,0x1c400u,0x1e8f0u,0x1eb60u,0x5090u,0x1c410u,0x100000u,0x1eb62u,0x5100u,0x1c420u,0x100000u,0x1eb64u,0x5180u,0x1c430u,0x100000u,0x1eb66u,0x5230u,0x1c440u,0x1e8f0u,0x1eb68u,0x52b0u,0x1c450u,0x100000u,0x1eb6au,0x5320u,0x1c460u,0x100000u,0x1eb6cu,0x53a0u,0x1c470u,0x100000u,0x1eb6eu,0x5450u,0x1c480u,0x1e8f0u,0x1eb70u,0x54d0u,0x1c490u,0x100000u,0x1eb72u,0x5540u,0x1c4a0u,0x100000u,0x1eb74u,0x55c0u,0x1c4b0u,0x100000u,0x1eb76u,0x5670u,0x1c4c0u,0x1e8f0u,0x1eb78u,0x56f0u,0x1c4d0u,0x100000u,0x1eb7au,0x5760u,0x1c4e0u,0x100000u,0x1eb7cu,0x57e0u,0x1c4f0u,0x100000u,0x1eb7eu,0x5020u,0x1c500u,0x1e920u,0x1eb60u,0x50a0u,0x1c510u,0x100000u,0x1eb62u,0x5130u,0x1c520u,0x100000u,0x1eb64u,0x51b0u,0x1c530u,0x100000u,0x1eb66u,0x5200u,0x1c540u,0x1e920u,0x1eb68u,0x5280u,0x1c550u,0x100000u,0x1eb6au,0x5310u,0x1c560u,0x100000u,0x1eb6cu,0x5390u,0x1c570u,0x100000u,0x1eb6eu,0x5460u,0x1c580u,0x1e920u,0x1eb70u,0x54e0u,0x1c590u,0x100000u,0x1eb72u,0x5570u,0x1c5a0u,0x100000u,0x1eb74u,0x55f0u,0x1c5b0u,0x100000u,0x1eb76u,0x5640u,0x1c5c0u,0x1e920u,0x1eb78u,0x56c0u,0x1c5d0u,0x100000u,0x1eb7au,0x5750u,0x1c5e0u,0x100000u,0x1eb7cu,0x57d0u,0x1c5f0u,0x100000u,0x1eb7eu,0x5030u,0x1c600u,0x1e950u,0x1eb60u,0x50b0u,0x1c610u,0x100000u,0x1eb62u,0x5120u,0x1c620u,0x100000u,0x1eb64u,0x51a0u,0x1c630u,0x100000u,0x1eb66u,0x5210u,0x1c640u,0x1e950u,0x1eb68u,0x5290u,0x1c650u,0x100000u,0x1eb6au,0x5300u,0x1c660u,0x100000u,0x1eb6cu,0x5380u,0x1c670u,0x100000u,0x1eb6eu,0x5470u,0x1c680u,0x1e950u,0x1eb70u,0x54f0u,0x1c690u,0x100000u,0x1eb72u,0x5560u,0x1c6a0u,0x100000u,0x1eb74u,0x55e0u,0x1c6b0u,0x100000u,0x1eb76u,0x5650u,0x1c6c0u,0x1e950u,0x1eb78u,0x56d0u,0x1c6d0u,0x100000u,0x1eb7au,0x5740u,0x1c6e0u,0x100000u,0x1eb7cu,0x57c0u,0x1c6f0u,0x100000u,0x1eb7eu,0x25d00u,0x1d380u,0x1d380u,0xf0140u,0x25d02u,0x1d382u,0x100000u,0xf0150u,0x25d04u,0x1d384u,0x100000u,0xf0160u,0x25d06u,0x1d386u,0x100000u,0xf0170u,0x25f00u,0x1d390u,0x1d380u,0xf0100u,0x25f02u,0x1d392u,0x100000u,0xf0110u,0x25f04u,0x1d394u,0x100000u,0xf0120u,0x25f06u,0x1d396u,0x100000u,0xf0130u,0x26100u,0x1d3a0u,0x1d380u,0xf02c0u,0x26102u,0x1d3a2u,0x100000u,0xf02d0u,0x26104u,0x1d3a4u,0x100000u,0xf02e0u,0x26106u,0x1d3a6u,0x100000u,0xf02f0u,0x26300u,0x1d3b0u,0x1d380u,0xf0280u,0x26302u,0x1d3b2u,0x100000u,0xf0290u,0x26304u,0x1d3b4u,0x100000u,0xf02a0u,0x26306u,0x1d3b6u,0x100000u,0xf02b0u,0x25d08u,0x1d388u,0x1d390u,0x11f850u,0x25d0au,0x1d38au,0x100000u,0x11f840u,0x25d0cu,0x1d38cu,0x100000u,0x11f870u,0x25d0eu,0x1d38eu,0x100000u,0x11f860u,0x25f08u,0x1d398u,0x1d390u,0x11f810u,0x25f0au,0x1d39au,0x100000u,0x11f800u,0x25f0cu,0x1d39cu,0x100000u,0x11f830u,0x25f0eu,0x1d39eu,0x100000u,0x11f820u,0x26108u,0x1d3a8u,0x1d390u,0x11f9d0u,0x2610au,0x1d3aau,0x100000u,0x11f9c0u,0x2610cu,0x1d3acu,0x100000u,0x11f9f0u,0x2610eu,0x1d3aeu,0x100000u,0x11f9e0u,0x26308u,0x1d3b8u,0x1d390u,0x11f990u,0x2630au,0x1d3bau,0x100000u,0x11f980u,0x2630cu,0x1d3bcu,0x100000u,0x11f9b0u,0x2630eu,0x1d3beu,0x100000u,0x11f9a0u,0x25508u,0x1d3c0u,0x1d3a0u,0x11fb60u,0x2550au,0x1d3c2u,0x100000u,0x11fb70u,0x2550cu,0x1d3c4u,0x100000u,0x11fb40u,0x2550eu,0x1d3c6u,0x100000u,0x11fb50u,0x25708u,0x1d3d0u,0x1d3a0u,0x11fb20u,0x2570au,0x1d3d2u,0x100000u,0x11fb30u,0x2570cu,0x1d3d4u,0x100000u,0x11fb00u,0x2570eu,0x1d3d6u,0x100000u,0x11fb10u,0x25908u,0x1d3e0u,0x1d3a0u,0x11fce0u,0x2590au,0x1d3e2u,0x100000u,0x11fcf0u,0x2590cu,0x1d3e4u,0x100000u,0x11fcc0u,0x2590eu,0x1d3e6u,0x100000u,0x11fcd0u,0x25b08u,0x1d3f0u,0x1d3a0u,0x11fca0u,0x25b0au,0x1d3f2u,0x100000u,0x11fcb0u,0x25b0cu,0x1d3f4u,0x100000u,0x11fc80u,0x25b0eu,0x1d3f6u,0x100000u,0x11fc90u,0x100000u,0x1d3c8u,0x1d3b0u,0x11fe70u,0x100000u,0x1d3cau,0x100000u,0x11fe60u,0x100000u,0x1d3ccu,0x100000u,0x11fe50u,0x100000u,0x1d3ceu,0x100000u,0x11fe40u,0x100000u,0x1d3d8u,0x1d3b0u,0x11fe30u,0x100000u,0x1d3dau,0x100000u,0x11fe20u,0x100000u,0x1d3dcu,0x100000u,0x11fe10u,0x100000u,0x1d3deu,0x100000u,0x11fe00u,0x100000u,0x1d3e8u,0x1d3b0u,0x11fff0u,0x100000u,0x1d3eau,0x100000u,0x11ffe0u,0x100000u,0x1d3ecu,0x100000u,0x11ffd0u,0x100000u,0x1d3eeu,0x100000u,0x11ffc0u,0x100000u,0x1d3f8u,0x1d3b0u,0x11ffb0u,0x100000u,0x1d3fau,0x100000u,0x11ffa0u,0x100000u,0x1d3fcu,0x100000u,0x11ff90u,0x100000u,0x1d3feu,0x100000u,0x11ff80u,0x420u,0x30850u,0x90000u,0xc0420u,0x430u,0x30840u,0x90010u,0xc0430u,0x400u,0x30870u,0x90020u,0xc0400u,0x410u,0x30860u,0x90030u,0xc0410u,0x460u,0x30810u,0x90040u,0xc0460u,0x470u,0x30800u,0x90050u,0xc0470u,0x440u,0x30830u,0x90060u,0xc0440u,0x450u,0x30820u,0x90070u,0xc0450u,0x4b0u,0x308d0u,0x90080u,0xef8b0u,0x4a0u,0x308c0u,0x90090u,0xef8a0u,0x490u,0x308f0u,0x900a0u,0xef890u,0x480u,0x308e0u,0x900b0u,0xef880u,0x4f0u,0x30890u,0x900c0u,0xef8f0u,0x4e0u,0x30880u,0x900d0u,0xef8e0u,0x4d0u,0x308b0u,0x900e0u,0xef8d0u,0x4c0u,0x308a0u,0x900f0u,0xef8c0u,0x530u,0x5fd60u,0x90100u,0xef930u,0x520u,0x5fd70u,0x90110u,0xef920u,0x510u,0x5fd40u,0x90120u,0xef910u,0x500u,0x5fd50u,0x90130u,0xef900u,0x570u,0x5fd20u,0x90140u,0xef970u,0x560u,0x5fd30u,0x90150u,0xef960u,0x550u,0x5fd00u,0x90160u,0xef950u,0x540u,0x5fd10u,0x90170u,0xef940u,0x5b0u,0x5fde0u,0x90190u,0xef9b0u,0x5a0u,0x5fdf0u,0x90180u,0xef9a0u,0x590u,0x5fdc0u,0x901b0u,0xef990u,0x580u,0x5fdd0u,0x901a0u,0xef980u,0x5f0u,0x5fda0u,0x901d0u,0xef9f0u,0x5e0u,0x5fdb0u,0x901c0u,0xef9e0u,0x5d0u,0x5fd80u,0x901f0u,0xef9d0u,0x5c0u,0x5fd90u,0x901e0u,0xef9c0u,0x640u,0x5fe60u,0x90210u,0xefa40u,0x650u,0x5fe70u,0x90200u,0xefa50u,0x660u,0x5fe40u,0x90230u,0xefa60u,0x670u,0x5fe50u,0x90220u,0xefa70u,0x600u,0x5fe20u,0x90250u,0xefa00u,0x610u,0x5fe30u,0x90240u,0xefa10u,0x620u,0x5fe00u,0x90270u,0xefa20u,0x630u,0x5fe10u,0x90260u,0xefa30u,0x6c0u,0x5fef0u,0x90290u,0xefac0u,0x6d0u,0x5fee0u,0x90280u,0xefad0u,0x6e0u,0x5fed0u,0x902b0u,0xefae0u,0x6f0u,0x5fec0u,0x902a0u,0xefaf0u,0x680u,0x5feb0u,0x902d0u,0xefa80u,0x690u,0x5fea0u,0x902c0u,0xefa90u,0x6a0u,0x5fe90u,0x902f0u,0xefaa0u,0x6b0u,0x5fe80u,0x902e0u,0xefab0u,0x740u,0x5ff70u,0x90320u,0xefb40u,0x750u,0x5ff60u,0x90330u,0xefb50u,0x760u,0x5ff50u,0x90300u,0xefb60u,0x770u,0x5ff40u,0x90310u,0xefb70u,0x700u,0x5ff30u,0x90360u,0xefb00u,0x710u,0x5ff20u,0x90370u,0xefb10u,0x720u,0x5ff10u,0x90340u,0xefb20u,0x730u,0x5ff00u,0x90350u,0xefb30u,0x7d0u,0x5fff0u,0x903a0u,0xefbd0u,0x7c0u,0x5ffe0u,0x903b0u,0xefbc0u,0x7f0u,0x5ffd0u,0x90380u,0xefbf0u,0x7e0u,0x5ffc0u,0x90390u,0xefbe0u,0x790u,0x5ffb0u,0x903e0u,0xefb90u,0x780u,0x5ffa0u,0x903f0u,0xefb80u,0x7b0u,0x5ff90u,0x903c0u,0xefbb0u,0x7a0u,0x5ff80u,0x903d0u,0xefba0u,0x0u,0x11fc50u,0x0u,0x7230cu,0x0u,0x11fc40u,0x0u,0x7230cu,0x0u,0x11fc70u,0x0u,0x7230cu,0x0u,0x11fc60u,0x0u,0x7230cu,0x0u,0x11fc10u,0x0u,0x7230cu,0x0u,0x11fc00u,0x0u,0x7230cu,0x0u,0x11fc30u,0x0u,0x7230cu,0x0u,0x11fc20u,0x0u,0x7230cu,0x0u,0x11fcd0u,0x0u,0x5232cu,0x0u,0x11fcc0u,0x0u,0x5232cu,0x0u,0x11fcf0u,0x0u,0x5232cu,0x0u,0x11fce0u,0x0u,0x5232cu,0x0u,0x11fc90u,0x0u,0x5232cu,0x0u,0x11fc80u,0x0u,0x5232cu,0x0u,0x11fcb0u,0x0u,0x5232cu,0x0u,0x11fca0u,0x0u,0x5232cu,0x0u,0x11fd60u,0x0u,0xd0124u,0x0u,0x11fd70u,0x0u,0xd0124u,0x0u,0x11fd40u,0x0u,0xd0124u,0x0u,0x11fd50u,0x0u,0xd0124u,0x0u,0x11fd20u,0x0u,0xd0124u,0x0u,0x11fd30u,0x0u,0xd0124u,0x0u,0x11fd00u,0x0u,0xd0124u,0x0u,0x11fd10u,0x0u,0xd0124u,0x0u,0x11fde0u,0x0u,0xd81a6u,0x0u,0x11fdf0u,0x0u,0xd81a6u,0x0u,0x11fdc0u,0x0u,0xd81a6u,0x0u,0x11fdd0u,0x0u,0xd81a6u,0x0u,0x11fda0u,0x0u,0xd81a6u,0x0u,0x11fdb0u,0x0u,0xd81a6u,0x0u,0x11fd80u,0x0u,0xd81a6u,0x0u,0x11fd90u,0x0u,0xd81a6u,0x0u,0x11fe60u,0x0u,0xd8996u,0x0u,0x11fe70u,0x0u,0xd8996u,0x0u,0x11fe40u,0x0u,0xd8996u,0x0u,0x11fe50u,0x0u,0xd8996u,0x0u,0x11fe20u,0x0u,0xd8996u,0x0u,0x11fe30u,0x0u,0xd8996u,0x0u,0x11fe00u,0x0u,0xd8996u,0x0u,0x11fe10u,0x0u,0xd8996u,0x0u,0x11fef0u,0x0u,0xd8996u,0x0u,0x11fee0u,0x0u,0xd8996u,0x0u,0x11fed0u,0x0u,0xd8996u,0x0u,0x11fec0u,0x0u,0xd8996u,0x0u,0x11feb0u,0x0u,0xd8996u,0x0u,0x11fea0u,0x0u,0xd8996u,0x0u,0x11fe90u,0x0u,0xd8996u,0x0u,0x11fe80u,0x0u,0xd8996u,0x0u,0x11ff70u,0x0u,0xd8b1cu,0x0u,0x11ff60u,0x0u,0xd8b1cu,0x0u,0x11ff50u,0x0u,0xd8b1cu,0x0u,0x11ff40u,0x0u,0xd8b1cu,0x0u,0x11ff30u,0x0u,0xd8b1cu,0x0u,0x11ff20u,0x0u,0xd8b1cu,0x0u,0x11ff10u,0x0u,0xd8b1cu,0x0u,0x11ff00u,0x0u,0xd8b1cu,0x0u,0x11fff0u,0x0u,0xd8b1cu,0x0u,0x11ffe0u,0x0u,0xd8b1cu,0x0u,0x11ffd0u,0x0u,0xd8b1cu,0x0u,0x11ffc0u,0x0u,0xd8b1cu,0x0u,0x11ffb0u,0x0u,0xd8b1cu,0x0u,0x11ffa0u,0x0u,0xd8b1cu,0x0u,0x11ff90u,0x0u,0xd8b1cu,0x0u,0x11ff80u,0x0u,0xd8b1cu,0x5840u,0x5860u,0x6800u,0x5840u,0x5848u,0x5868u,0x6808u,0x5850u,0x5850u,0x5870u,0x6810u,0x5860u,0x5858u,0x5878u,0x6818u,0x5870u,0x58c0u,0x58e0u,0x6880u,0x58c0u,0x58c8u,0x58e8u,0x6888u,0x58d0u,0x58d0u,0x58f0u,0x6890u,0x58e0u,0x58d8u,0x58f8u,0x6898u,0x58f0u,0x5960u,0x5940u,0x6920u,0x5960u,0x5968u,0x5948u,0x6928u,0x5970u,0x5970u,0x5950u,0x6930u,0x5940u,0x5978u,0x5958u,0x6938u,0x5950u,0x59e0u,0x59c0u,0x69a0u,0x59e0u,0x59e8u,0x59c8u,0x69a8u,0x59f0u,0x59f0u,0x59d0u,0x69b0u,0x59c0u,0x59f8u,0x59d8u,0x69b8u,0x59d0u,0x5c00u,0x5c20u,0x6c40u,0x5a40u,0x5c08u,0x5c28u,0x6c48u,0x5a50u,0x5c10u,0x5c30u,0x6c50u,0x5a60u,0x5c18u,0x5c38u,0x6c58u,0x5a70u,0x5c80u,0x5ca0u,0x6cc0u,0x5ac0u,0x5c88u,0x5ca8u,0x6cc8u,0x5ad0u,0x5c90u,0x5cb0u,0x6cd0u,0x5ae0u,0x5c98u,0x5cb8u,0x6cd8u,0x5af0u,0x5d20u,0x5d00u,0x6d60u,0x5b60u,0x5d28u,0x5d08u,0x6d68u,0x5b70u,0x5d30u,0x5d10u,0x6d70u,0x5b40u,0x5d38u,0x5d18u,0x6d78u,0x5b50u,0x5da0u,0x5d80u,0x6de0u,0x5be0u,0x5da8u,0x5d88u,0x6de8u,0x5bf0u,0x5db0u,0x5d90u,0x6df0u,0x5bc0u,0x5db8u,0x5d98u,0x6df8u,0x5bd0u,0x6040u,0x6060u,0x7000u,0x5c00u,0x6048u,0x6068u,0x7008u,0x5c10u,0x6050u,0x6070u,0x7010u,0x5c20u,0x6058u,0x6078u,0x7018u,0x5c30u,0x60c0u,0x60e0u,0x7080u,0x5c80u,0x60c8u,0x60e8u,0x7088u,0x5c90u,0x60d0u,0x60f0u,0x7090u,0x5ca0u,0x60d8u,0x60f8u,0x7098u,0x5cb0u,0x6160u,0x6140u,0x7120u,0x5d20u,0x6168u,0x6148u,0x7128u,0x5d30u,0x6170u,0x6150u,0x7130u,0x5d00u,0x6178u,0x6158u,0x7138u,0x5d10u,0x61e0u,0x61c0u,0x71a0u,0x5da0u,0x61e8u,0x61c8u,0x71a8u,0x5db0u,0x61f0u,0x61d0u,0x71b0u,0x5d80u,0x61f8u,0x61d8u,0x71b8u,0x5d90u,0x6400u,0x6420u,0x7440u,0x5e00u,0x6408u,0x6428u,0x7448u,0x5e10u,0x6410u,0x6430u,0x7450u,0x5e20u,0x6418u,0x6438u,0x7458u,0x5e30u,0x6480u,0x64a0u,0x74c0u,0x5e80u,0x6488u,0x64a8u,0x74c8u,0x5e90u,0x6490u,0x64b0u,0x74d0u,0x5ea0u,0x6498u,0x64b8u,0x74d8u,0x5eb0u,0x6520u,0x6500u,0x7560u,0x5f20u,0x6528u,0x6508u,0x7568u,0x5f30u,0x6530u,0x6510u,0x7570u,0x5f00u,0x6538u,0x6518u,0x7578u,0x5f10u,0x65a0u,0x6580u,0x75e0u,0x5fa0u,0x65a8u,0x6588u,0x75e8u,0x5fb0u,0x65b0u,0x6590u,0x75f0u,0x5f80u,0x65b8u,0x6598u,0x75f8u,0x5f90u,0x6800u,0x1e980u,0x1ebc0u,0x1800u,0x6808u,0x1ee10u,0x1f050u,0x1810u,0x6810u,0x1f2a0u,0x1f4e0u,0x1820u,0x6818u,0x1f730u,0x1f970u,0x1830u,0x6880u,0x1fbc0u,0x1fe00u,0x1840u,0x6888u,0x20050u,0x20290u,0x1850u,0x6890u,0x204e0u,0x20720u,0x1860u,0x6898u,0x20970u,0x20bb0u,0x1870u,0x6920u,0x20e00u,0x21040u,0x1980u,0x6928u,0x21290u,0x214d0u,0x1990u,0x6930u,0x21720u,0x21960u,0x19a0u,0x6938u,0x21bb0u,0x21df0u,0x19b0u,0x69a0u,0x22040u,0x22280u,0x19c0u,0x69a8u,0x224d0u,0x22710u,0x19d0u,0x69b0u,0x22960u,0x22ba0u,0x19e0u,0x69b8u,0x22df0u,0x23030u,0x19f0u,0x6a00u,0x1e9b0u,0x1ebc8u,0x1b20u,0x6a08u,0x1ee40u,0x1f058u,0x1b30u,0x6a10u,0x1f2d0u,0x1f4e8u,0x1b00u,0x6a18u,0x1f760u,0x1f978u,0x1b10u,0x6a80u,0x1fbf0u,0x1fe08u,0x1b60u,0x6a88u,0x20080u,0x20298u,0x1b70u,0x6a90u,0x20510u,0x20728u,0x1b40u,0x6a98u,0x209a0u,0x20bb8u,0x1b50u,0x6b20u,0x20e30u,0x21048u,0x1ca0u,0x6b28u,0x212c0u,0x214d8u,0x1cb0u,0x6b30u,0x21750u,0x21968u,0x1c80u,0x6b38u,0x21be0u,0x21df8u,0x1c90u,0x6ba0u,0x22070u,0x22288u,0x1ce0u,0x6ba8u,0x22500u,0x22718u,0x1cf0u,0x6bb0u,0x22990u,0x22ba8u,0x1cc0u,0x6bb8u,0x22e20u,0x23038u,0x1cd0u,0x6c40u,0x1e9e0u,0x1ebd0u,0x1e00u,0x6c48u,0x1ee70u,0x1f060u,0x1e10u,0x6c50u,0x1f300u,0x1f4f0u,0x1e20u,0x6c58u,0x1f790u,0x1f980u,0x1e30u,0x6cc0u,0x1fc20u,0x1fe10u,0x1e40u,0x6cc8u,0x200b0u,0x202a0u,0x1e50u,0x6cd0u,0x20540u,0x20730u,0x1e60u,0x6cd8u,0x209d0u,0x20bc0u,0x1e70u,0x6d60u,0x20e60u,0x21050u,0x1f80u,0x6d68u,0x212f0u,0x214e0u,0x1f90u,0x6d70u,0x21780u,0x21970u,0x1fa0u,0x6d78u,0x21c10u,0x21e00u,0x1fb0u,0x6de0u,0x220a0u,0x22290u,0x1fc0u,0x6de8u,0x22530u,0x22720u,0x1fd0u,0x6df0u,0x229c0u,0x22bb0u,0x1fe0u,0x6df8u,0x22e50u,0x23040u,0x1ff0u,0x6e40u,0x1ea10u,0x1ebd8u,0x2120u,0x6e48u,0x1eea0u,0x1f068u,0x2130u,0x6e50u,0x1f330u,0x1f4f8u,0x2100u,0x6e58u,0x1f7c0u,0x1f988u,0x2110u,0x6ec0u,0x1fc50u,0x1fe18u,0x2160u,0x6ec8u,0x200e0u,0x202a8u,0x2170u,0x6ed0u,0x20570u,0x20738u,0x2140u,0x6ed8u,0x20a00u,0x20bc8u,0x2150u,0x6f60u,0x20e90u,0x21058u,0x22a0u,0x6f68u,0x21320u,0x214e8u,0x22b0u,0x6f70u,0x217b0u,0x21978u,0x2280u,0x6f78u,0x21c40u,0x21e08u,0x2290u,0x6fe0u,0x220d0u,0x22298u,0x22e0u,0x6fe8u,0x22560u,0x22728u,0x22f0u,0x6ff0u,0x229f0u,0x22bb8u,0x22c0u,0x6ff8u,0x22e80u,0x23048u,0x22d0u,0x0u,0x40u,0x9000u,0xb400u,0x80u,0xc0u,0x9010u,0xb410u,0x110u,0x150u,0x9020u,0xb420u,0x190u,0x1d0u,0x9030u,0xb430u,0x220u,0x260u,0x9040u,0xb440u,0x2a0u,0x2e0u,0x9050u,0xb450u,0x330u,0x370u,0x9060u,0xb460u,0x3b0u,0x3f0u,0x9070u,0xb470u,0x440u,0x400u,0x9080u,0xb480u,0x4c0u,0x480u,0x9090u,0xb490u,0x550u,0x510u,0x90a0u,0xb4a0u,0x5d0u,0x590u,0x90b0u,0xb4b0u,0x660u,0x620u,0x90c0u,0xb4c0u,0x6e0u,0x6a0u,0x90d0u,0xb4d0u,0x770u,0x730u,0x90e0u,0xb4e0u,0x7f0u,0x7b0u,0x90f0u,0xb4f0u,0x10u,0x50u,0x9300u,0xb700u,0x90u,0xd0u,0x9310u,0xb710u,0x100u,0x140u,0x9320u,0xb720u,0x180u,0x1c0u,0x9330u,0xb730u,0x230u,0x270u,0x9340u,0xb740u,0x2b0u,0x2f0u,0x9350u,0xb750u,0x320u,0x360u,0x9360u,0xb760u,0x3a0u,0x3e0u,0x9370u,0xb770u,0x450u,0x410u,0x9380u,0xb780u,0x4d0u,0x490u,0x9390u,0xb790u,0x540u,0x500u,0x93a0u,0xb7a0u,0x5c0u,0x580u,0x93b0u,0xb7b0u,0x670u,0x630u,0x93c0u,0xb7c0u,0x6f0u,0x6b0u,0x93d0u,0xb7d0u,0x760u,0x720u,0x93e0u,0xb7e0u,0x7e0u,0x7a0u,0x93f0u,0xb7f0u,0x20u,0x60u,0x9600u,0xba00u,0xa0u,0xe0u,0x9610u,0xba10u,0x130u,0x170u,0x9620u,0xba20u,0x1b0u,0x1f0u,0x9630u,0xba30u,0x200u,0x240u,0x9640u,0xba40u,0x280u,0x2c0u,0x9650u,0xba50u,0x310u,0x350u,0x9660u,0xba60u,0x390u,0x3d0u,0x9670u,0xba70u,0x460u,0x420u,0x9680u,0xba80u,0x4e0u,0x4a0u,0x9690u,0xba90u,0x570u,0x530u,0x96a0u,0xbaa0u,0x5f0u,0x5b0u,0x96b0u,0xbab0u,0x640u,0x600u,0x96c0u,0xbac0u,0x6c0u,0x680u,0x96d0u,0xbad0u,0x750u,0x710u,0x96e0u,0xbae0u,0x7d0u,0x790u,0x96f0u,0xbaf0u,0x30u,0x70u,0x9900u,0xbd00u,0xb0u,0xf0u,0x9910u,0xbd10u,0x120u,0x160u,0x9920u,0xbd20u,0x1a0u,0x1e0u,0x9930u,0xbd30u,0x210u,0x250u,0x9940u,0xbd40u,0x290u,0x2d0u,0x9950u,0xbd50u,0x300u,0x340u,0x9960u,0xbd60u,0x380u,0x3c0u,0x9970u,0xbd70u,0x470u,0x430u,0x9980u,0xbd80u,0x4f0u,0x4b0u,0x9990u,0xbd90u,0x560u,0x520u,0x99a0u,0xbda0u,0x5e0u,0x5a0u,0x99b0u,0xbdb0u,0x650u,0x610u,0x99c0u,0xbdc0u,0x6d0u,0x690u,0x99d0u,0xbdd0u,0x740u,0x700u,0x99e0u,0xbde0u,0x7c0u,0x780u,0x99f0u,0xbdf0u,0xc000u,0x120c0u,0x12100u,0x12800u,0xc010u,0x12250u,0x12290u,0x12810u,0xc020u,0x123e0u,0x12420u,0x12820u,0xc030u,0x12570u,0x125b0u,0x12830u,0xc040u,0x12680u,0x12740u,0x12840u,0xc050u,0x12810u,0x128d0u,0x12850u,0xc060u,0x129a0u,0x12a60u,0x12860u,0xc070u,0x12b30u,0x12bf0u,0x12870u,0xc080u,0x12cc0u,0x12d00u,0x12880u,0xc090u,0x12e50u,0x12e90u,0x12890u,0xc0a0u,0x12fe0u,0x13020u,0x128a0u,0xc0b0u,0x13170u,0x131b0u,0x128b0u,0xc0c0u,0x13280u,0x13340u,0x128c0u,0xc0d0u,0x13410u,0x134d0u,0x128d0u,0xc0e0u,0x135a0u,0x13660u,0x128e0u,0xc0f0u,0x13730u,0x137f0u,0x128f0u,0xc300u,0x120d0u,0x12108u,0x12900u,0xc310u,0x12240u,0x12298u,0x12910u,0xc320u,0x123f0u,0x12428u,0x12920u,0xc330u,0x12560u,0x125b8u,0x12930u,0xc340u,0x12690u,0x12748u,0x12940u,0xc350u,0x12800u,0x128d8u,0x12950u,0xc360u,0x129b0u,0x12a68u,0x12960u,0xc370u,0x12b20u,0x12bf8u,0x12970u,0xc380u,0x12cd0u,0x12d08u,0x12980u,0xc390u,0x12e40u,0x12e98u,0x12990u,0xc3a0u,0x12ff0u,0x13028u,0x129a0u,0xc3b0u,0x13160u,0x131b8u,0x129b0u,0xc3c0u,0x13290u,0x13348u,0x129c0u,0xc3d0u,0x13400u,0x134d8u,0x129d0u,0xc3e0u,0x135b0u,0x13668u,0x129e0u,0xc3f0u,0x13720u,0x137f8u,0x129f0u,0xc600u,0x120e0u,0x12110u,0x12a00u,0xc610u,0x12270u,0x12280u,0x12a10u,0xc620u,0x123c0u,0x12430u,0x12a20u,0xc630u,0x12550u,0x125a0u,0x12a30u,0xc640u,0x126a0u,0x12750u,0x12a40u,0xc650u,0x12830u,0x128c0u,0x12a50u,0xc660u,0x12980u,0x12a70u,0x12a60u,0xc670u,0x12b10u,0x12be0u,0x12a70u,0xc680u,0x12ce0u,0x12d10u,0x12a80u,0xc690u,0x12e70u,0x12e80u,0x12a90u,0xc6a0u,0x12fc0u,0x13030u,0x12aa0u,0xc6b0u,0x13150u,0x131a0u,0x12ab0u,0xc6c0u,0x132a0u,0x13350u,0x12ac0u,0xc6d0u,0x13430u,0x134c0u,0x12ad0u,0xc6e0u,0x13580u,0x13670u,0x12ae0u,0xc6f0u,0x13710u,0x137e0u,0x12af0u,0xc900u,0x120f0u,0x12118u,0x12b00u,0xc910u,0x12260u,0x12288u,0x12b10u,0xc920u,0x123d0u,0x12438u,0x12b20u,0xc930u,0x12540u,0x125a8u,0x12b30u,0xc940u,0x126b0u,0x12758u,0x12b40u,0xc950u,0x12820u,0x128c8u,0x12b50u,0xc960u,0x12990u,0x12a78u,0x12b60u,0xc970u,0x12b00u,0x12be8u,0x12b70u,0xc980u,0x12cf0u,0x12d18u,0x12b80u,0xc990u,0x12e60u,0x12e88u,0x12b90u,0xc9a0u,0x12fd0u,0x13038u,0x12ba0u,0xc9b0u,0x13140u,0x131a8u,0x12bb0u,0xc9c0u,0x132b0u,0x13358u,0x12bc0u,0xc9d0u,0x13420u,0x134c8u,0x12bd0u,0xc9e0u,0x13590u,0x13678u,0x12be0u,0xc9f0u,0x13700u,0x137e8u,0x12bf0u,0x7800u,0x1c300u,0x1e980u,0x1ebc0u,0x7880u,0x1c310u,0x100000u,0x1ebc2u,0x7910u,0x1c320u,0x100000u,0x1ebc4u,0x7990u,0x1c330u,0x100000u,0x1ebc6u,0x7a20u,0x1c340u,0x1e980u,0x1ebc8u,0x7aa0u,0x1c350u,0x100000u,0x1ebcau,0x7b30u,0x1c360u,0x100000u,0x1ebccu,0x7bb0u,0x1c370u,0x100000u,0x1ebceu,0x7c40u,0x1c380u,0x1e980u,0x1ebd0u,0x7cc0u,0x1c390u,0x100000u,0x1ebd2u,0x7d50u,0x1c3a0u,0x100000u,0x1ebd4u,0x7dd0u,0x1c3b0u,0x100000u,0x1ebd6u,0x7e60u,0x1c3c0u,0x1e980u,0x1ebd8u,0x7ee0u,0x1c3d0u,0x100000u,0x1ebdau,0x7f70u,0x1c3e0u,0x100000u,0x1ebdcu,0x7ff0u,0x1c3f0u,0x100000u,0x1ebdeu,0x7810u,0x1c400u,0x1e9b0u,0x1ebc0u,0x7890u,0x1c410u,0x100000u,0x1ebc2u,0x7900u,0x1c420u,0x100000u,0x1ebc4u,0x7980u,0x1c430u,0x100000u,0x1ebc6u,0x7a30u,0x1c440u,0x1e9b0u,0x1ebc8u,0x7ab0u,0x1c450u,0x100000u,0x1ebcau,0x7b20u,0x1c460u,0x100000u,0x1ebccu,0x7ba0u,0x1c470u,0x100000u,0x1ebceu,0x7c50u,0x1c480u,0x1e9b0u,0x1ebd0u,0x7cd0u,0x1c490u,0x100000u,0x1ebd2u,0x7d40u,0x1c4a0u,0x100000u,0x1ebd4u,0x7dc0u,0x1c4b0u,0x100000u,0x1ebd6u,0x7e70u,0x1c4c0u,0x1e9b0u,0x1ebd8u,0x7ef0u,0x1c4d0u,0x100000u,0x1ebdau,0x7f60u,0x1c4e0u,0x100000u,0x1ebdcu,0x7fe0u,0x1c4f0u,0x100000u,0x1ebdeu,0x7820u,0x1c500u,0x1e9e0u,0x1ebc0u,0x78a0u,0x1c510u,0x100000u,0x1ebc2u,0x7930u,0x1c520u,0x100000u,0x1ebc4u,0x79b0u,0x1c530u,0x100000u,0x1ebc6u,0x7a00u,0x1c540u,0x1e9e0u,0x1ebc8u,0x7a80u,0x1c550u,0x100000u,0x1ebcau,0x7b10u,0x1c560u,0x100000u,0x1ebccu,0x7b90u,0x1c570u,0x100000u,0x1ebceu,0x7c60u,0x1c580u,0x1e9e0u,0x1ebd0u,0x7ce0u,0x1c590u,0x100000u,0x1ebd2u,0x7d70u,0x1c5a0u,0x100000u,0x1ebd4u,0x7df0u,0x1c5b0u,0x100000u,0x1ebd6u,0x7e40u,0x1c5c0u,0x1e9e0u,0x1ebd8u,0x7ec0u,0x1c5d0u,0x100000u,0x1ebdau,0x7f50u,0x1c5e0u,0x100000u,0x1ebdcu,0x7fd0u,0x1c5f0u,0x100000u,0x1ebdeu,0x7830u,0x1c600u,0x1ea10u,0x1ebc0u,0x78b0u,0x1c610u,0x100000u,0x1ebc2u,0x7920u,0x1c620u,0x100000u,0x1ebc4u,0x79a0u,0x1c630u,0x100000u,0x1ebc6u,0x7a10u,0x1c640u,0x1ea10u,0x1ebc8u,0x7a90u,0x1c650u,0x100000u,0x1ebcau,0x7b00u,0x1c660u,0x100000u,0x1ebccu,0x7b80u,0x1c670u,0x100000u,0x1ebceu,0x7c70u,0x1c680u,0x1ea10u,0x1ebd0u,0x7cf0u,0x1c690u,0x100000u,0x1ebd2u,0x7d60u,0x1c6a0u,0x100000u,0x1ebd4u,0x7de0u,0x1c6b0u,0x100000u,0x1ebd6u,0x7e50u,0x1c6c0u,0x1ea10u,0x1ebd8u,0x7ed0u,0x1c6d0u,0x100000u,0x1ebdau,0x7f40u,0x1c6e0u,0x100000u,0x1ebdcu,0x7fc0u,0x1c6f0u,0x100000u,0x1ebdeu,0x26500u,0x1d400u,0x1d400u,0x80000000u,0x26502u,0x1d402u,0x100000u,0x80000000u,0x26504u,0x1d404u,0x100000u,0x80000000u,0x26506u,0x1d406u,0x100000u,0x80000000u,0x26700u,0x1d410u,0x1d400u,0x80000000u,0x26702u,0x1d412u,0x100000u,0x80000000u,0x26704u,0x1d414u,0x100000u,0x80000000u,0x26706u,0x1d416u,0x100000u,0x80000000u,0x26900u,0x1d420u,0x1d400u,0x80000000u,0x26902u,0x1d422u,0x100000u,0x80000000u,0x26904u,0x1d424u,0x100000u,0x80000000u,0x26906u,0x1d426u,0x100000u,0x80000000u,0x26b00u,0x1d430u,0x1d400u,0x80000000u,0x26b02u,0x1d432u,0x100000u,0x80000000u,0x26b04u,0x1d434u,0x100000u,0x80000000u,0x26b06u,0x1d436u,0x100000u,0x80000000u,0x26508u,0x1d408u,0x1d410u,0x80000000u,0x2650au,0x1d40au,0x100000u,0x80000000u,0x2650cu,0x1d40cu,0x100000u,0x80000000u,0x2650eu,0x1d40eu,0x100000u,0x80000000u,0x26708u,0x1d418u,0x1d410u,0x80000000u,0x2670au,0x1d41au,0x100000u,0x80000000u,0x2670cu,0x1d41cu,0x100000u,0x80000000u,0x2670eu,0x1d41eu,0x100000u,0x80000000u,0x26908u,0x1d428u,0x1d410u,0x80000000u,0x2690au,0x1d42au,0x100000u,0x80000000u,0x2690cu,0x1d42cu,0x100000u,0x80000000u,0x2690eu,0x1d42eu,0x100000u,0x80000000u,0x26b08u,0x1d438u,0x1d410u,0x80000000u,0x26b0au,0x1d43au,0x100000u,0x80000000u,0x26b0cu,0x1d43cu,0x100000u,0x80000000u,0x26b0eu,0x1d43eu,0x100000u,0x80000000u,0x26d00u,0x1d440u,0x1d420u,0x80000000u,0x26d02u,0x1d442u,0x100000u,0x80000000u,0x26d04u,0x1d444u,0x100000u,0x80000000u,0x26d06u,0x1d446u,0x100000u,0x80000000u,0x26f00u,0x1d450u,0x1d420u,0x80000000u,0x26f02u,0x1d452u,0x100000u,0x80000000u,0x26f04u,0x1d454u,0x100000u,0x80000000u,0x26f06u,0x1d456u,0x100000u,0x80000000u,0x27100u,0x1d460u,0x1d420u,0x80000000u,0x27102u,0x1d462u,0x100000u,0x80000000u,0x27104u,0x1d464u,0x100000u,0x80000000u,0x27106u,0x1d466u,0x100000u,0x80000000u,0x27300u,0x1d470u,0x1d420u,0x80000000u,0x27302u,0x1d472u,0x100000u,0x80000000u,0x27304u,0x1d474u,0x100000u,0x80000000u,0x27306u,0x1d476u,0x100000u,0x80000000u,0x100000u,0x1d448u,0x1d430u,0x80000000u,0x100000u,0x1d44au,0x100000u,0x80000000u,0x100000u,0x1d44cu,0x100000u,0x80000000u,0x100000u,0x1d44eu,0x100000u,0x80000000u,0x100000u,0x1d458u,0x1d430u,0x80000000u,0x100000u,0x1d45au,0x100000u,0x80000000u,0x100000u,0x1d45cu,0x100000u,0x80000000u,0x100000u,0x1d45eu,0x100000u,0x80000000u,0x100000u,0x1d468u,0x1d430u,0x80000000u,0x100000u,0x1d46au,0x100000u,0x80000000u,0x100000u,0x1d46cu,0x100000u,0x80000000u,0x100000u,0x1d46eu,0x100000u,0x80000000u,0x100000u,0x1d478u,0x1d430u,0x80000000u,0x100000u,0x1d47au,0x100000u,0x80000000u,0x100000u,0x1d47cu,0x100000u,0x80000000u,0x100000u,0x1d47eu,0x100000u,0x80000000u,0x850u,0x60000u,0x90420u,0xefc50u,0x840u,0x60010u,0x90430u,0xefc40u,0x870u,0x60020u,0x90400u,0xefc70u,0x860u,0x60030u,0x90410u,0xefc60u,0x810u,0x60040u,0x90460u,0xefc10u,0x800u,0x60050u,0x90470u,0xefc00u,0x830u,0x60060u,0x90440u,0xefc30u,0x820u,0x60070u,0x90450u,0xefc20u,0x8d0u,0x60080u,0x904b0u,0xefcd0u,0x8c0u,0x60090u,0x904a0u,0xefcc0u,0x8f0u,0x600a0u,0x90490u,0xefcf0u,0x8e0u,0x600b0u,0x90480u,0xefce0u,0x890u,0x600c0u,0x904f0u,0xefc90u,0x880u,0x600d0u,0x904e0u,0xefc80u,0x8b0u,0x600e0u,0x904d0u,0xefcb0u,0x8a0u,0x600f0u,0x904c0u,0xefca0u,0x960u,0x60100u,0x90530u,0xefd60u,0x970u,0x60110u,0x90520u,0xefd70u,0x940u,0x60120u,0x90510u,0xefd40u,0x950u,0x60130u,0x90500u,0xefd50u,0x920u,0x60140u,0x90570u,0xefd20u,0x930u,0x60150u,0x90560u,0xefd30u,0x900u,0x60160u,0x90550u,0xefd00u,0x910u,0x60170u,0x90540u,0xefd10u,0x9e0u,0x60190u,0x905b0u,0xefde0u,0x9f0u,0x60180u,0x905a0u,0xefdf0u,0x9c0u,0x601b0u,0x90590u,0xefdc0u,0x9d0u,0x601a0u,0x90580u,0xefdd0u,0x9a0u,0x601d0u,0x905f0u,0xefda0u,0x9b0u,0x601c0u,0x905e0u,0xefdb0u,0x980u,0x601f0u,0x905d0u,0xefd80u,0x990u,0x601e0u,0x905c0u,0xefd90u,0xa60u,0x60210u,0xbfa40u,0xefe60u,0xa70u,0x60200u,0xbfa50u,0xefe70u,0xa40u,0x60230u,0xbfa60u,0xefe40u,0xa50u,0x60220u,0xbfa70u,0xefe50u,0xa20u,0x60250u,0xbfa00u,0xefe20u,0xa30u,0x60240u,0xbfa10u,0xefe30u,0xa00u,0x60270u,0xbfa20u,0xefe00u,0xa10u,0x60260u,0xbfa30u,0xefe10u,0x2fef0u,0x60290u,0xbfac0u,0xefef0u,0x2fee0u,0x60280u,0xbfad0u,0xefee0u,0x2fed0u,0x602b0u,0xbfae0u,0xefed0u,0x2fec0u,0x602a0u,0xbfaf0u,0xefec0u,0x2feb0u,0x602d0u,0xbfa80u,0xefeb0u,0x2fea0u,0x602c0u,0xbfa90u,0xefea0u,0x2fe90u,0x602f0u,0xbfaa0u,0xefe90u,0x2fe80u,0x602e0u,0xbfab0u,0xefe80u,0x2ff70u,0x60320u,0xbfb40u,0xeff70u,0x2ff60u,0x60330u,0xbfb50u,0xeff60u,0x2ff50u,0x60300u,0xbfb60u,0xeff50u,0x2ff40u,0x60310u,0xbfb70u,0xeff40u,0x2ff30u,0x60360u,0xbfb00u,0xeff30u,0x2ff20u,0x60370u,0xbfb10u,0xeff20u,0x2ff10u,0x60340u,0xbfb20u,0xeff10u,0x2ff00u,0x60350u,0xbfb30u,0xeff00u,0x2fff0u,0x603a0u,0xbfbd0u,0xefff0u,0x2ffe0u,0x603b0u,0xbfbc0u,0xeffe0u,0x2ffd0u,0x60380u,0xbfbf0u,0xeffd0u,0x2ffc0u,0x60390u,0xbfbe0u,0xeffc0u,0x2ffb0u,0x603e0u,0xbfb90u,0xeffb0u,0x2ffa0u,0x603f0u,0xbfb80u,0xeffa0u,0x2ff90u,0x603c0u,0xbfbb0u,0xeff90u,0x2ff80u,0x603d0u,0xbfba0u,0xeff80u,0x0u,0x80000000u,0x0u,0x18830u,0x0u,0x80000000u,0x0u,0x18830u,0x0u,0x80000000u,0x0u,0x18830u,0x0u,0x80000000u,0x0u,0x18830u,0x0u,0x80000000u,0x0u,0x18830u,0x0u,0x80000000u,0x0u,0x18830u,0x0u,0x80000000u,0x0u,0x18830u,0x0u,0x80000000u,0x0u,0x18830u,0x0u,0x80000000u,0x0u,0x18830u,0x0u,0x80000000u,0x0u,0x18830u,0x0u,0x80000000u,0x0u,0x18830u,0x0u,0x80000000u,0x0u,0x18830u,0x0u,0x80000000u,0x0u,0x18830u,0x0u,0x80000000u,0x0u,0x18830u,0x0u,0x80000000u,0x0u,0x18830u,0x0u,0x80000000u,0x0u,0x18830u,0x0u,0x80000000u,0x0u,0x188b2u,0x0u,0x80000000u,0x0u,0x188b2u,0x0u,0x80000000u,0x0u,0x188b2u,0x0u,0x80000000u,0x0u,0x188b2u,0x0u,0x80000000u,0x0u,0x188b2u,0x0u,0x80000000u,0x0u,0x188b2u,0x0u,0x80000000u,0x0u,0x188b2u,0x0u,0x80000000u,0x0u,0x188b2u,0x0u,0x80000000u,0x0u,0x18892u,0x0u,0x80000000u,0x0u,0x18892u,0x0u,0x80000000u,0x0u,0x18892u,0x0u,0x80000000u,0x0u,0x18892u,0x0u,0x80000000u,0x0u,0x18892u,0x0u,0x80000000u,0x0u,0x18892u,0x0u,0x80000000u,0x0u,0x18892u,0x0u,0x80000000u,0x0u,0x18892u,0x0u,0x80000000u,0x0u,0x1489au,0x0u,0x80000000u,0x0u,0x1489au,0x0u,0x80000000u,0x0u,0x1489au,0x0u,0x80000000u,0x0u,0x1489au,0x0u,0x80000000u,0x0u,0x1489au,0x0u,0x80000000u,0x0u,0x1489au,0x0u,0x80000000u,0x0u,0x1489au,0x0u,0x80000000u,0x0u,0x1489au,0x0u,0x80000000u,0x0u,0x34018u,0x0u,0x80000000u,0x0u,0x34018u,0x0u,0x80000000u,0x0u,0x34018u,0x0u,0x80000000u,0x0u,0x34018u,0x0u,0x80000000u,0x0u,0x34018u,0x0u,0x80000000u,0x0u,0x34018u,0x0u,0x80000000u,0x0u,0x34018u,0x0u,0x80000000u,0x0u,0x34018u,0x0u,0x80000000u,0x0u,0x36018u,0x0u,0x80000000u,0x0u,0x36018u,0x0u,0x80000000u,0x0u,0x36018u,0x0u,0x80000000u,0x0u,0x36018u,0x0u,0x80000000u,0x0u,0x36018u,0x0u,0x80000000u,0x0u,0x36018u,0x0u,0x80000000u,0x0u,0x36018u,0x0u,0x80000000u,0x0u,0x36018u,0x0u,0x80000000u,0x0u,0x36030u,0x0u,0x80000000u,0x0u,0x36030u,0x0u,0x80000000u,0x0u,0x36030u,0x0u,0x80000000u,0x0u,0x36030u,0x0u,0x80000000u,0x0u,0x36030u,0x0u,0x80000000u,0x0u,0x36030u,0x0u,0x80000000u,0x0u,0x36030u,0x0u,0x80000000u,0x0u,0x36030u,0x6840u,0x6860u,0x6820u,0x6820u,0x6848u,0x6868u,0x6828u,0x6830u,0x6850u,0x6870u,0x6830u,0x6840u,0x6858u,0x6878u,0x6838u,0x6850u,0x68c0u,0x68e0u,0x68a0u,0x68a0u,0x68c8u,0x68e8u,0x68a8u,0x68b0u,0x68d0u,0x68f0u,0x68b0u,0x68c0u,0x68d8u,0x68f8u,0x68b8u,0x68d0u,0x6960u,0x6940u,0x6900u,0x6900u,0x6968u,0x6948u,0x6908u,0x6910u,0x6970u,0x6950u,0x6910u,0x6960u,0x6978u,0x6958u,0x6918u,0x6970u,0x69e0u,0x69c0u,0x6980u,0x6980u,0x69e8u,0x69c8u,0x6988u,0x6990u,0x69f0u,0x69d0u,0x6990u,0x69e0u,0x69f8u,0x69d8u,0x6998u,0x69f0u,0x6c00u,0x6c20u,0x6c60u,0x6a20u,0x6c08u,0x6c28u,0x6c68u,0x6a30u,0x6c10u,0x6c30u,0x6c70u,0x6a40u,0x6c18u,0x6c38u,0x6c78u,0x6a50u,0x6c80u,0x6ca0u,0x6ce0u,0x6aa0u,0x6c88u,0x6ca8u,0x6ce8u,0x6ab0u,0x6c90u,0x6cb0u,0x6cf0u,0x6ac0u,0x6c98u,0x6cb8u,0x6cf8u,0x6ad0u,0x6d20u,0x6d00u,0x6d40u,0x6b00u,0x6d28u,0x6d08u,0x6d48u,0x6b10u,0x6d30u,0x6d10u,0x6d50u,0x6b60u,0x6d38u,0x6d18u,0x6d58u,0x6b70u,0x6da0u,0x6d80u,0x6dc0u,0x6b80u,0x6da8u,0x6d88u,0x6dc8u,0x6b90u,0x6db0u,0x6d90u,0x6dd0u,0x6be0u,0x6db8u,0x6d98u,0x6dd8u,0x6bf0u,0x7040u,0x7060u,0x7020u,0x6c60u,0x7048u,0x7068u,0x7028u,0x6c70u,0x7050u,0x7070u,0x7030u,0x6c00u,0x7058u,0x7078u,0x7038u,0x6c10u,0x70c0u,0x70e0u,0x70a0u,0x6ce0u,0x70c8u,0x70e8u,0x70a8u,0x6cf0u,0x70d0u,0x70f0u,0x70b0u,0x6c80u,0x70d8u,0x70f8u,0x70b8u,0x6c90u,0x7160u,0x7140u,0x7100u,0x6d40u,0x7168u,0x7148u,0x7108u,0x6d50u,0x7170u,0x7150u,0x7110u,0x6d20u,0x7178u,0x7158u,0x7118u,0x6d30u,0x71e0u,0x71c0u,0x7180u,0x6dc0u,0x71e8u,0x71c8u,0x7188u,0x6dd0u,0x71f0u,0x71d0u,0x7190u,0x6da0u,0x71f8u,0x71d8u,0x7198u,0x6db0u,0x7400u,0x7420u,0x7460u,0x6e60u,0x7408u,0x7428u,0x7468u,0x6e70u,0x7410u,0x7430u,0x7470u,0x6e00u,0x7418u,0x7438u,0x7478u,0x6e10u,0x7480u,0x74a0u,0x74e0u,0x6ee0u,0x7488u,0x74a8u,0x74e8u,0x6ef0u,0x7490u,0x74b0u,0x74f0u,0x6e80u,0x7498u,0x74b8u,0x74f8u,0x6e90u,0x7520u,0x7500u,0x7540u,0x6f40u,0x7528u,0x7508u,0x7548u,0x6f50u,0x7530u,0x7510u,0x7550u,0x6f20u,0x7538u,0x7518u,0x7558u,0x6f30u,0x75a0u,0x7580u,0x75c0u,0x6fc0u,0x75a8u,0x7588u,0x75c8u,0x6fd0u,0x75b0u,0x7590u,0x75d0u,0x6fa0u,0x75b8u,0x7598u,0x75d8u,0x6fb0u,0x6860u,0x1ea40u,0x1ec20u,0x2440u,0x6868u,0x1eed0u,0x1f0b0u,0x2450u,0x6870u,0x1f360u,0x1f540u,0x2460u,0x6878u,0x1f7f0u,0x1f9d0u,0x2470u,0x68e0u,0x1fc80u,0x1fe60u,0x2400u,0x68e8u,0x20110u,0x202f0u,0x2410u,0x68f0u,0x205a0u,0x20780u,0x2420u,0x68f8u,0x20a30u,0x20c10u,0x2430u,0x6940u,0x20ec0u,0x210a0u,0x25c0u,0x6948u,0x21350u,0x21530u,0x25d0u,0x6950u,0x217e0u,0x219c0u,0x25e0u,0x6958u,0x21c70u,0x21e50u,0x25f0u,0x69c0u,0x22100u,0x222e0u,0x2580u,0x69c8u,0x22590u,0x22770u,0x2590u,0x69d0u,0x22a20u,0x22c00u,0x25a0u,0x69d8u,0x22eb0u,0x23090u,0x25b0u,0x6a60u,0x1ea70u,0x1ec28u,0x2760u,0x6a68u,0x1ef00u,0x1f0b8u,0x2770u,0x6a70u,0x1f390u,0x1f548u,0x2740u,0x6a78u,0x1f820u,0x1f9d8u,0x2750u,0x6ae0u,0x1fcb0u,0x1fe68u,0x2720u,0x6ae8u,0x20140u,0x202f8u,0x2730u,0x6af0u,0x205d0u,0x20788u,0x2700u,0x6af8u,0x20a60u,0x20c18u,0x2710u,0x6b40u,0x20ef0u,0x210a8u,0x28e0u,0x6b48u,0x21380u,0x21538u,0x28f0u,0x6b50u,0x21810u,0x219c8u,0x28c0u,0x6b58u,0x21ca0u,0x21e58u,0x28d0u,0x6bc0u,0x22130u,0x222e8u,0x28a0u,0x6bc8u,0x225c0u,0x22778u,0x28b0u,0x6bd0u,0x22a50u,0x22c08u,0x2880u,0x6bd8u,0x22ee0u,0x23098u,0x2890u,0x6c20u,0x1eaa0u,0x1ec30u,0x2a40u,0x6c28u,0x1ef30u,0x1f0c0u,0x2a50u,0x6c30u,0x1f3c0u,0x1f550u,0x2a60u,0x6c38u,0x1f850u,0x1f9e0u,0x2a70u,0x6ca0u,0x1fce0u,0x1fe70u,0x2a00u,0x6ca8u,0x20170u,0x20300u,0x2a10u,0x6cb0u,0x20600u,0x20790u,0x2a20u,0x6cb8u,0x20a90u,0x20c20u,0x2a30u,0x6d00u,0x20f20u,0x210b0u,0x2bc0u,0x6d08u,0x213b0u,0x21540u,0x2bd0u,0x6d10u,0x21840u,0x219d0u,0x2be0u,0x6d18u,0x21cd0u,0x21e60u,0x2bf0u,0x6d80u,0x22160u,0x222f0u,0x2b80u,0x6d88u,0x225f0u,0x22780u,0x2b90u,0x6d90u,0x22a80u,0x22c10u,0x2ba0u,0x6d98u,0x22f10u,0x230a0u,0x2bb0u,0x6e20u,0x1ead0u,0x1ec38u,0x2d60u,0x6e28u,0x1ef60u,0x1f0c8u,0x2d70u,0x6e30u,0x1f3f0u,0x1f558u,0x2d40u,0x6e38u,0x1f880u,0x1f9e8u,0x2d50u,0x6ea0u,0x1fd10u,0x1fe78u,0x2d20u,0x6ea8u,0x201a0u,0x20308u,0x2d30u,0x6eb0u,0x20630u,0x20798u,0x2d00u,0x6eb8u,0x20ac0u,0x20c28u,0x2d10u,0x6f00u,0x20f50u,0x210b8u,0x2ee0u,0x6f08u,0x213e0u,0x21548u,0x2ef0u,0x6f10u,0x21870u,0x219d8u,0x2ec0u,0x6f18u,0x21d00u,0x21e68u,0x2ed0u,0x6f80u,0x22190u,0x222f8u,0x2ea0u,0x6f88u,0x22620u,0x22788u,0x2eb0u,0x6f90u,0x22ab0u,0x22c18u,0x2e80u,0x6f98u,0x22f40u,0x230a8u,0x2e90u,0x0u,0x40u,0x9000u,0xcc00u,0x80u,0xc0u,0x9010u,0xcc10u,0x110u,0x150u,0x9020u,0xcc20u,0x190u,0x1d0u,0x9030u,0xcc30u,0x220u,0x260u,0x9040u,0xcc40u,0x2a0u,0x2e0u,0x9050u,0xcc50u,0x330u,0x370u,0x9060u,0xcc60u,0x3b0u,0x3f0u,0x9070u,0xcc70u,0x440u,0x400u,0x9080u,0xcc80u,0x4c0u,0x480u,0x9090u,0xcc90u,0x550u,0x510u,0x90a0u,0xcca0u,0x5d0u,0x590u,0x90b0u,0xccb0u,0x660u,0x620u,0x90c0u,0xccc0u,0x6e0u,0x6a0u,0x90d0u,0xccd0u,0x770u,0x730u,0x90e0u,0xcce0u,0x7f0u,0x7b0u,0x90f0u,0xccf0u,0x10u,0x50u,0x9300u,0xcf00u,0x90u,0xd0u,0x9310u,0xcf10u,0x100u,0x140u,0x9320u,0xcf20u,0x180u,0x1c0u,0x9330u,0xcf30u,0x230u,0x270u,0x9340u,0xcf40u,0x2b0u,0x2f0u,0x9350u,0xcf50u,0x320u,0x360u,0x9360u,0xcf60u,0x3a0u,0x3e0u,0x9370u,0xcf70u,0x450u,0x410u,0x9380u,0xcf80u,0x4d0u,0x490u,0x9390u,0xcf90u,0x540u,0x500u,0x93a0u,0xcfa0u,0x5c0u,0x580u,0x93b0u,0xcfb0u,0x670u,0x630u,0x93c0u,0xcfc0u,0x6f0u,0x6b0u,0x93d0u,0xcfd0u,0x760u,0x720u,0x93e0u,0xcfe0u,0x7e0u,0x7a0u,0x93f0u,0xcff0u,0x20u,0x60u,0x9600u,0xd200u,0xa0u,0xe0u,0x9610u,0xd210u,0x130u,0x170u,0x9620u,0xd220u,0x1b0u,0x1f0u,0x9630u,0xd230u,0x200u,0x240u,0x9640u,0xd240u,0x280u,0x2c0u,0x9650u,0xd250u,0x310u,0x350u,0x9660u,0xd260u,0x390u,0x3d0u,0x9670u,0xd270u,0x460u,0x420u,0x9680u,0xd280u,0x4e0u,0x4a0u,0x9690u,0xd290u,0x570u,0x530u,0x96a0u,0xd2a0u,0x5f0u,0x5b0u,0x96b0u,0xd2b0u,0x640u,0x600u,0x96c0u,0xd2c0u,0x6c0u,0x680u,0x96d0u,0xd2d0u,0x750u,0x710u,0x96e0u,0xd2e0u,0x7d0u,0x790u,0x96f0u,0xd2f0u,0x30u,0x70u,0x9900u,0xd500u,0xb0u,0xf0u,0x9910u,0xd510u,0x120u,0x160u,0x9920u,0xd520u,0x1a0u,0x1e0u,0x9930u,0xd530u,0x210u,0x250u,0x9940u,0xd540u,0x290u,0x2d0u,0x9950u,0xd550u,0x300u,0x340u,0x9960u,0xd560u,0x380u,0x3c0u,0x9970u,0xd570u,0x470u,0x430u,0x9980u,0xd580u,0x4f0u,0x4b0u,0x9990u,0xd590u,0x560u,0x520u,0x99a0u,0xd5a0u,0x5e0u,0x5a0u,0x99b0u,0xd5b0u,0x650u,0x610u,0x99c0u,0xd5c0u,0x6d0u,0x690u,0x99d0u,0xd5d0u,0x740u,0x700u,0x99e0u,0xd5e0u,0x7c0u,0x780u,0x99f0u,0xd5f0u,0xc008u,0x12120u,0x12160u,0x12c00u,0xc018u,0x122b0u,0x122f0u,0x12c10u,0xc028u,0x12400u,0x12440u,0x12c20u,0xc038u,0x12590u,0x125d0u,0x12c30u,0xc048u,0x12760u,0x12720u,0x12c40u,0xc058u,0x128f0u,0x128b0u,0x12c50u,0xc068u,0x12a40u,0x12a00u,0x12c60u,0xc078u,0x12bd0u,0x12b90u,0x12c70u,0xc088u,0x12d20u,0x12d60u,0x12c80u,0xc098u,0x12eb0u,0x12ef0u,0x12c90u,0xc0a8u,0x13000u,0x13040u,0x12ca0u,0xc0b8u,0x13190u,0x131d0u,0x12cb0u,0xc0c8u,0x13360u,0x13320u,0x12cc0u,0xc0d8u,0x134f0u,0x134b0u,0x12cd0u,0xc0e8u,0x13640u,0x13600u,0x12ce0u,0xc0f8u,0x137d0u,0x13790u,0x12cf0u,0xc308u,0x12130u,0x12168u,0x12d00u,0xc318u,0x122a0u,0x122f8u,0x12d10u,0xc328u,0x12410u,0x12448u,0x12d20u,0xc338u,0x12580u,0x125d8u,0x12d30u,0xc348u,0x12770u,0x12728u,0x12d40u,0xc358u,0x128e0u,0x128b8u,0x12d50u,0xc368u,0x12a50u,0x12a08u,0x12d60u,0xc378u,0x12bc0u,0x12b98u,0x12d70u,0xc388u,0x12d30u,0x12d68u,0x12d80u,0xc398u,0x12ea0u,0x12ef8u,0x12d90u,0xc3a8u,0x13010u,0x13048u,0x12da0u,0xc3b8u,0x13180u,0x131d8u,0x12db0u,0xc3c8u,0x13370u,0x13328u,0x12dc0u,0xc3d8u,0x134e0u,0x134b8u,0x12dd0u,0xc3e8u,0x13650u,0x13608u,0x12de0u,0xc3f8u,0x137c0u,0x13798u,0x12df0u,0xc608u,0x12140u,0x12170u,0x12e00u,0xc618u,0x122d0u,0x122e0u,0x12e10u,0xc628u,0x12460u,0x12450u,0x12e20u,0xc638u,0x125f0u,0x125c0u,0x12e30u,0xc648u,0x12700u,0x12730u,0x12e40u,0xc658u,0x12890u,0x128a0u,0x12e50u,0xc668u,0x12a20u,0x12a10u,0x12e60u,0xc678u,0x12bb0u,0x12b80u,0x12e70u,0xc688u,0x12d40u,0x12d70u,0x12e80u,0xc698u,0x12ed0u,0x12ee0u,0x12e90u,0xc6a8u,0x13060u,0x13050u,0x12ea0u,0xc6b8u,0x131f0u,0x131c0u,0x12eb0u,0xc6c8u,0x13300u,0x13330u,0x12ec0u,0xc6d8u,0x13490u,0x134a0u,0x12ed0u,0xc6e8u,0x13620u,0x13610u,0x12ee0u,0xc6f8u,0x137b0u,0x13780u,0x12ef0u,0xc908u,0x12150u,0x12178u,0x12f00u,0xc918u,0x122c0u,0x122e8u,0x12f10u,0xc928u,0x12470u,0x12458u,0x12f20u,0xc938u,0x125e0u,0x125c8u,0x12f30u,0xc948u,0x12710u,0x12738u,0x12f40u,0xc958u,0x12880u,0x128a8u,0x12f50u,0xc968u,0x12a30u,0x12a18u,0x12f60u,0xc978u,0x12ba0u,0x12b88u,0x12f70u,0xc988u,0x12d50u,0x12d78u,0x12f80u,0xc998u,0x12ec0u,0x12ee8u,0x12f90u,0xc9a8u,0x13070u,0x13058u,0x12fa0u,0xc9b8u,0x131e0u,0x131c8u,0x12fb0u,0xc9c8u,0x13310u,0x13338u,0x12fc0u,0xc9d8u,0x13480u,0x134a8u,0x12fd0u,0xc9e8u,0x13630u,0x13618u,0x12fe0u,0xc9f8u,0x137a0u,0x13788u,0x12ff0u,0x8000u,0x1c300u,0x1ea40u,0x1ec20u,0x8080u,0x1c310u,0x100000u,0x1ec22u,0x8110u,0x1c320u,0x100000u,0x1ec24u,0x8190u,0x1c330u,0x100000u,0x1ec26u,0x8220u,0x1c340u,0x1ea40u,0x1ec28u,0x82a0u,0x1c350u,0x100000u,0x1ec2au,0x8330u,0x1c360u,0x100000u,0x1ec2cu,0x83b0u,0x1c370u,0x100000u,0x1ec2eu,0x8440u,0x1c380u,0x1ea40u,0x1ec30u,0x84c0u,0x1c390u,0x100000u,0x1ec32u,0x8550u,0x1c3a0u,0x100000u,0x1ec34u,0x85d0u,0x1c3b0u,0x100000u,0x1ec36u,0x8660u,0x1c3c0u,0x1ea40u,0x1ec38u,0x86e0u,0x1c3d0u,0x100000u,0x1ec3au,0x8770u,0x1c3e0u,0x100000u,0x1ec3cu,0x87f0u,0x1c3f0u,0x100000u,0x1ec3eu,0x8010u,0x1c400u,0x1ea70u,0x1ec20u,0x8090u,0x1c410u,0x100000u,0x1ec22u,0x8100u,0x1c420u,0x100000u,0x1ec24u,0x8180u,0x1c430u,0x100000u,0x1ec26u,0x8230u,0x1c440u,0x1ea70u,0x1ec28u,0x82b0u,0x1c450u,0x100000u,0x1ec2au,0x8320u,0x1c460u,0x100000u,0x1ec2cu,0x83a0u,0x1c470u,0x100000u,0x1ec2eu,0x8450u,0x1c480u,0x1ea70u,0x1ec30u,0x84d0u,0x1c490u,0x100000u,0x1ec32u,0x8540u,0x1c4a0u,0x100000u,0x1ec34u,0x85c0u,0x1c4b0u,0x100000u,0x1ec36u,0x8670u,0x1c4c0u,0x1ea70u,0x1ec38u,0x86f0u,0x1c4d0u,0x100000u,0x1ec3au,0x8760u,0x1c4e0u,0x100000u,0x1ec3cu,0x87e0u,0x1c4f0u,0x100000u,0x1ec3eu,0x8020u,0x1c500u,0x1eaa0u,0x1ec20u,0x80a0u,0x1c510u,0x100000u,0x1ec22u,0x8130u,0x1c520u,0x100000u,0x1ec24u,0x81b0u,0x1c530u,0x100000u,0x1ec26u,0x8200u,0x1c540u,0x1eaa0u,0x1ec28u,0x8280u,0x1c550u,0x100000u,0x1ec2au,0x8310u,0x1c560u,0x100000u,0x1ec2cu,0x8390u,0x1c570u,0x100000u,0x1ec2eu,0x8460u,0x1c580u,0x1eaa0u,0x1ec30u,0x84e0u,0x1c590u,0x100000u,0x1ec32u,0x8570u,0x1c5a0u,0x100000u,0x1ec34u,0x85f0u,0x1c5b0u,0x100000u,0x1ec36u,0x8640u,0x1c5c0u,0x1eaa0u,0x1ec38u,0x86c0u,0x1c5d0u,0x100000u,0x1ec3au,0x8750u,0x1c5e0u,0x100000u,0x1ec3cu,0x87d0u,0x1c5f0u,0x100000u,0x1ec3eu,0x8030u,0x1c600u,0x1ead0u,0x1ec20u,0x80b0u,0x1c610u,0x100000u,0x1ec22u,0x8120u,0x1c620u,0x100000u,0x1ec24u,0x81a0u,0x1c630u,0x100000u,0x1ec26u,0x8210u,0x1c640u,0x1ead0u,0x1ec28u,0x8290u,0x1c650u,0x100000u,0x1ec2au,0x8300u,0x1c660u,0x100000u,0x1ec2cu,0x8380u,0x1c670u,0x100000u,0x1ec2eu,0x8470u,0x1c680u,0x1ead0u,0x1ec30u,0x84f0u,0x1c690u,0x100000u,0x1ec32u,0x8560u,0x1c6a0u,0x100000u,0x1ec34u,0x85e0u,0x1c6b0u,0x100000u,0x1ec36u,0x8650u,0x1c6c0u,0x1ead0u,0x1ec38u,0x86d0u,0x1c6d0u,0x100000u,0x1ec3au,0x8740u,0x1c6e0u,0x100000u,0x1ec3cu,0x87c0u,0x1c6f0u,0x100000u,0x1ec3eu,0x27500u,0x1d480u,0x1d480u,0x80000000u,0x27502u,0x1d482u,0x100000u,0x80000000u,0x27504u,0x1d484u,0x100000u,0x80000000u,0x27506u,0x1d486u,0x100000u,0x80000000u,0x27700u,0x1d490u,0x1d480u,0x80000000u,0x27702u,0x1d492u,0x100000u,0x80000000u,0x27704u,0x1d494u,0x100000u,0x80000000u,0x27706u,0x1d496u,0x100000u,0x80000000u,0x27900u,0x1d4a0u,0x1d480u,0x80000000u,0x27902u,0x1d4a2u,0x100000u,0x80000000u,0x27904u,0x1d4a4u,0x100000u,0x80000000u,0x27906u,0x1d4a6u,0x100000u,0x80000000u,0x27b00u,0x1d4b0u,0x1d480u,0x80000000u,0x27b02u,0x1d4b2u,0x100000u,0x80000000u,0x27b04u,0x1d4b4u,0x100000u,0x80000000u,0x27b06u,0x1d4b6u,0x100000u,0x80000000u,0x27508u,0x1d488u,0x1d490u,0x80000000u,0x2750au,0x1d48au,0x100000u,0x80000000u,0x2750cu,0x1d48cu,0x100000u,0x80000000u,0x2750eu,0x1d48eu,0x100000u,0x80000000u,0x27708u,0x1d498u,0x1d490u,0x80000000u,0x2770au,0x1d49au,0x100000u,0x80000000u,0x2770cu,0x1d49cu,0x100000u,0x80000000u,0x2770eu,0x1d49eu,0x100000u,0x80000000u,0x27908u,0x1d4a8u,0x1d490u,0x80000000u,0x2790au,0x1d4aau,0x100000u,0x80000000u,0x2790cu,0x1d4acu,0x100000u,0x80000000u,0x2790eu,0x1d4aeu,0x100000u,0x80000000u,0x27b08u,0x1d4b8u,0x1d490u,0x80000000u,0x27b0au,0x1d4bau,0x100000u,0x80000000u,0x27b0cu,0x1d4bcu,0x100000u,0x80000000u,0x27b0eu,0x1d4beu,0x100000u,0x80000000u,0x26d08u,0x1d4c0u,0x1d4a0u,0x80000000u,0x26d0au,0x1d4c2u,0x100000u,0x80000000u,0x26d0cu,0x1d4c4u,0x100000u,0x80000000u,0x26d0eu,0x1d4c6u,0x100000u,0x80000000u,0x26f08u,0x1d4d0u,0x1d4a0u,0x80000000u,0x26f0au,0x1d4d2u,0x100000u,0x80000000u,0x26f0cu,0x1d4d4u,0x100000u,0x80000000u,0x26f0eu,0x1d4d6u,0x100000u,0x80000000u,0x27108u,0x1d4e0u,0x1d4a0u,0x80000000u,0x2710au,0x1d4e2u,0x100000u,0x80000000u,0x2710cu,0x1d4e4u,0x100000u,0x80000000u,0x2710eu,0x1d4e6u,0x100000u,0x80000000u,0x27308u,0x1d4f0u,0x1d4a0u,0x80000000u,0x2730au,0x1d4f2u,0x100000u,0x80000000u,0x2730cu,0x1d4f4u,0x100000u,0x80000000u,0x2730eu,0x1d4f6u,0x100000u,0x80000000u,0x100000u,0x1d4c8u,0x1d4b0u,0x80000000u,0x100000u,0x1d4cau,0x100000u,0x80000000u,0x100000u,0x1d4ccu,0x100000u,0x80000000u,0x100000u,0x1d4ceu,0x100000u,0x80000000u,0x100000u,0x1d4d8u,0x1d4b0u,0x80000000u,0x100000u,0x1d4dau,0x100000u,0x80000000u,0x100000u,0x1d4dcu,0x100000u,0x80000000u,0x100000u,0x1d4deu,0x100000u,0x80000000u,0x100000u,0x1d4e8u,0x1d4b0u,0x80000000u,0x100000u,0x1d4eau,0x100000u,0x80000000u,0x100000u,0x1d4ecu,0x100000u,0x80000000u,0x100000u,0x1d4eeu,0x100000u,0x80000000u,0x100000u,0x1d4f8u,0x1d4b0u,0x80000000u,0x100000u,0x1d4fau,0x100000u,0x80000000u,0x100000u,0x1d4fcu,0x100000u,0x80000000u,0x100000u,0x1d4feu,0x100000u,0x80000000u,0x30000u,0x60420u,0xbfc50u,0xf0000u,0x30010u,0x60430u,0xbfc40u,0xf0010u,0x30020u,0x60400u,0xbfc70u,0xf0020u,0x30030u,0x60410u,0xbfc60u,0xf0030u,0x30040u,0x60460u,0xbfc10u,0xf0040u,0x30050u,0x60470u,0xbfc00u,0xf0050u,0x30060u,0x60440u,0xbfc30u,0xf0060u,0x30070u,0x60450u,0xbfc20u,0xf0070u,0x30080u,0x604b0u,0xbfcd0u,0xf0080u,0x30090u,0x604a0u,0xbfcc0u,0xf0090u,0x300a0u,0x60490u,0xbfcf0u,0xf00a0u,0x300b0u,0x60480u,0xbfce0u,0xf00b0u,0x300c0u,0x604f0u,0xbfc90u,0xf00c0u,0x300d0u,0x604e0u,0xbfc80u,0xf00d0u,0x300e0u,0x604d0u,0xbfcb0u,0xf00e0u,0x300f0u,0x604c0u,0xbfca0u,0xf00f0u,0x30100u,0x60530u,0xbfd60u,0xf0100u,0x30110u,0x60520u,0xbfd70u,0xf0110u,0x30120u,0x60510u,0xbfd40u,0xf0120u,0x30130u,0x60500u,0xbfd50u,0xf0130u,0x30140u,0x60570u,0xbfd20u,0xf0140u,0x30150u,0x60560u,0xbfd30u,0xf0150u,0x30160u,0x60550u,0xbfd00u,0xf0160u,0x30170u,0x60540u,0xbfd10u,0xf0170u,0x30190u,0x605b0u,0xbfde0u,0xf0190u,0x30180u,0x605a0u,0xbfdf0u,0xf0180u,0x301b0u,0x60590u,0xbfdc0u,0xf01b0u,0x301a0u,0x60580u,0xbfdd0u,0xf01a0u,0x301d0u,0x605f0u,0xbfda0u,0xf01d0u,0x301c0u,0x605e0u,0xbfdb0u,0xf01c0u,0x301f0u,0x605d0u,0xbfd80u,0xf01f0u,0x301e0u,0x605c0u,0xbfd90u,0xf01e0u,0x30210u,0x60640u,0xbfe60u,0xf0210u,0x30200u,0x60650u,0xbfe70u,0xf0200u,0x30230u,0x60660u,0xbfe40u,0xf0230u,0x30220u,0x60670u,0xbfe50u,0xf0220u,0x30250u,0x60600u,0xbfe20u,0xf0250u,0x30240u,0x60610u,0xbfe30u,0xf0240u,0x30270u,0x60620u,0xbfe00u,0xf0270u,0x30260u,0x60630u,0xbfe10u,0xf0260u,0x30290u,0x606c0u,0xbfef0u,0xf0290u,0x30280u,0x606d0u,0xbfee0u,0xf0280u,0x302b0u,0x606e0u,0xbfed0u,0xf02b0u,0x302a0u,0x606f0u,0xbfec0u,0xf02a0u,0x302d0u,0x60680u,0xbfeb0u,0xf02d0u,0x302c0u,0x60690u,0xbfea0u,0xf02c0u,0x302f0u,0x606a0u,0xbfe90u,0xf02f0u,0x302e0u,0x606b0u,0xbfe80u,0xf02e0u,0x30320u,0x60740u,0xbff70u,0x11f720u,0x30330u,0x60750u,0xbff60u,0x11f730u,0x30300u,0x60760u,0xbff50u,0x11f700u,0x30310u,0x60770u,0xbff40u,0x11f710u,0x30360u,0x60700u,0xbff30u,0x11f760u,0x30370u,0x60710u,0xbff20u,0x11f770u,0x30340u,0x60720u,0xbff10u,0x11f740u,0x30350u,0x60730u,0xbff00u,0x11f750u,0x303a0u,0x8fbd0u,0xbfff0u,0x11f7a0u,0x303b0u,0x8fbc0u,0xbffe0u,0x11f7b0u,0x30380u,0x8fbf0u,0xbffd0u,0x11f780u,0x30390u,0x8fbe0u,0xbffc0u,0x11f790u,0x303e0u,0x8fb90u,0xbffb0u,0x11f7e0u,0x303f0u,0x8fb80u,0xbffa0u,0x11f7f0u,0x303c0u,0x8fbb0u,0xbff90u,0x11f7c0u,0x303d0u,0x8fba0u,0xbff80u,0x11f7d0u,0x0u,0x80000000u,0x0u,0x36030u,0x0u,0x80000000u,0x0u,0x36030u,0x0u,0x80000000u,0x0u,0x36030u,0x0u,0x80000000u,0x0u,0x36030u,0x0u,0x80000000u,0x0u,0x36030u,0x0u,0x80000000u,0x0u,0x36030u,0x0u,0x80000000u,0x0u,0x36030u,0x0u,0x80000000u,0x0u,0x36030u,0x0u,0x80000000u,0x0u,0x360b2u,0x0u,0x80000000u,0x0u,0x360b2u,0x0u,0x80000000u,0x0u,0x360b2u,0x0u,0x80000000u,0x0u,0x360b2u,0x0u,0x80000000u,0x0u,0x360b2u,0x0u,0x80000000u,0x0u,0x360b2u,0x0u,0x80000000u,0x0u,0x360b2u,0x0u,0x80000000u,0x0u,0x360b2u,0x0u,0x80000000u,0x0u,0x36092u,0x0u,0x80000000u,0x0u,0x36092u,0x0u,0x80000000u,0x0u,0x36092u,0x0u,0x80000000u,0x0u,0x36092u,0x0u,0x80000000u,0x0u,0x36092u,0x0u,0x80000000u,0x0u,0x36092u,0x0u,0x80000000u,0x0u,0x36092u,0x0u,0x80000000u,0x0u,0x36092u,0x0u,0x80000000u,0x0u,0x3609au,0x0u,0x80000000u,0x0u,0x3609au,0x0u,0x80000000u,0x0u,0x3609au,0x0u,0x80000000u,0x0u,0x3609au,0x0u,0x80000000u,0x0u,0x3609au,0x0u,0x80000000u,0x0u,0x3609au,0x0u,0x80000000u,0x0u,0x3609au,0x0u,0x80000000u,0x0u,0x3609au,0x0u,0x80000000u,0x0u,0x36059u,0x0u,0x80000000u,0x0u,0x36059u,0x0u,0x80000000u,0x0u,0x36059u,0x0u,0x80000000u,0x0u,0x36059u,0x0u,0x80000000u,0x0u,0x36059u,0x0u,0x80000000u,0x0u,0x36059u,0x0u,0x80000000u,0x0u,0x36059u,0x0u,0x80000000u,0x0u,0x36059u,0x0u,0x80000000u,0x0u,0x36059u,0x0u,0x80000000u,0x0u,0x36059u,0x0u,0x80000000u,0x0u,0x36059u,0x0u,0x80000000u,0x0u,0x36059u,0x0u,0x80000000u,0x0u,0x36059u,0x0u,0x80000000u,0x0u,0x36059u,0x0u,0x80000000u,0x0u,0x36059u,0x0u,0x80000000u,0x0u,0x36059u,0x0u,0x80000000u,0x0u,0x16071u,0x0u,0x80000000u,0x0u,0x16071u,0x0u,0x80000000u,0x0u,0x16071u,0x0u,0x80000000u,0x0u,0x16071u,0x0u,0x80000000u,0x0u,0x16071u,0x0u,0x80000000u,0x0u,0x16071u,0x0u,0x80000000u,0x0u,0x16071u,0x0u,0x80000000u,0x0u,0x16071u,0x0u,0x80000000u,0x0u,0x14071u,0x0u,0x80000000u,0x0u,0x14071u,0x0u,0x80000000u,0x0u,0x14071u,0x0u,0x80000000u,0x0u,0x14071u,0x0u,0x80000000u,0x0u,0x14071u,0x0u,0x80000000u,0x0u,0x14071u,0x0u,0x80000000u,0x0u,0x14071u,0x0u,0x80000000u,0x0u,0x14071u};

__global__ __launch_bounds__(256, 1) void swin_main(const uint16_t* x, const uint16_t* mask, char* ws, uint16_t* out,
                                                    const uint16_t* wqkv, const uint16_t* wo, const uint16_t* ls,
                                                    const uint16_t* w1, const uint16_t* w2, const uint16_t* rc,
                                                    const int* ridx) {
  __shared__ __attribute__((aligned(16))) char smem[LDS_BYTES];
  const int wave = __builtin_amdgcn_readfirstlane(threadIdx.x >> 6);
  const unsigned lds0 = (unsigned)(size_t)smem;
  asm volatile(
      ".p2align 5\n"
      "s_mov_b64 s[92:93], %0\n"
      "s_mov_b32 s40, %1\n"
      "s_mov_b32 s41, %2\n"
      "s_mov_b64 s[88:89], %3\n"
      "s_load_dwordx2 s[16:17], s[92:93], 0\n"
      "s_load_dwordx2 s[20:21], s[92:93], 32\n"
      "s_load_dwordx2 s[46:47], s[92:93], 40\n"
      "s_load_dwordx8 s[80:87], s[92:93], 48\n"
      "s_load_dwordx2 s[76:77], s[92:93], 80\n"
      "s_mov_b64 s[90:91], s[92:93]\n"
      "s_getpc_b64 s[98:99]\n"
      "L_ipf0_%=:\n"
      "v_mbcnt_lo_u32_b32 v238, -1, 0\n"
      "v_mbcnt_hi_u32_b32 v238, -1, v238\n"
      "v_lshlrev_b32 v237, 2, v238\n"
      "v_lshlrev_b32_e64 v221, 4, v238\n"
      "s_lshr_b32 s49, s41, 3\n"
      "s_mov_b32 s72, 0x3100117e\n"
      "s_mov_b32 s73, 0xe0bec09e\n"
      "s_mov_b32 s70, 0x315e117e\n"
      "s_mov_b32 s71, 0xe04fc09e\n"
      "s_and_b32 s92, s41, 7\n"
      "s_cmp_eq_u32 s92, 6\n"
      "s_cselect_b32 s93, 2, 17\n"
      "s_cmp_lt_u32 s49, s93\n"
      "s_cselect_b64 s[72:73], s[72:73], s[70:71]\n"
      "s_lshl_b32 s92, s92, 3\n"
      "s_lshr_b64 s[72:73], s[72:73], s92\n"
      "s_add_u32 s41, s72, s49\n"
      "s_and_b32 s41, s41, 255\n"
      "s_movk_i32 s96, 4992\n"
      "s_cmp_lt_u32 s41, 192\n"
      "s_cselect_b32 s44, 20, 19\n"
      "s_cselect_b32 s96, 5137, s96\n"
      "s_cmp_lt_u32 s41, 111\n"
      "s_cselect_b32 s96, 5056, s96\n"
      "s_cmp_lt_u32 s41, 17\n"
      "s_cselect_b32 s44, 21, s44\n"
      "s_cselect_b32 s96, 5167, s96\n"
      "s_add_u32 s96, s96, s41\n"
      "s_lshl_b32 s92, s40, 8\n"
      "v_add_u32_e64 v69, s92, v237\n"
      "v_min_u32 v29, 704, v69\n"
      "v_add_u32 v29, 4096, v29\n"
      "v_min_u32 v69, 672, v69\n"
      "v_add_u32 v130, 122112, v69\n"
      "s_lshl_b32 s92, s49, 7\n"
      "v_lshlrev_b32_e64 v8, 7, v238\n"
      "s_mul_i32 s93, s92, 18\n"
      "s_mul_i32 s94, s40, 640\n"
      "v_min_u32 v236, 512, v8\n"
      "v_add_u32 v236, s94, v236\n"
      "v_min_u32 v236, 2176, v236\n"
      "v_add_u32 v236, s93, v236\n"
      "s_cmp_eq_u32 s40, 1\n"
      "s_cselect_b32 s93, 4096, 0\n"
      "s_add_u32 s92, s92, s93\n"
      "s_cmp_eq_u32 s40, 2\n"
      "s_cselect_b32 s93, 9, 8\n"
      "s_cselect_b64 s[72:73], s[98:99], s[88:89]\n"
      "s_cmp_lt_u32 s40, 2\n"
      "s_cselect_b32 s93, 27, s93\n"
      "s_mul_i32 s92, s92, s93\n"
      "s_sub_u32 s93, s93, 1\n"
      "s_lshl_b32 s93, s93, 7\n"
      "v_min_u32 v8, s93, v8\n"
      "v_add_u32 v8, s92, v8\n"
      "s_lshl_b32 s95, s40, 3\n"
      "v_lshrrev_b32 v239, 3, v238\n"
      "v_and_b32_e64 v147, 7, v238\n"
      "v_add_u32_e64 v148, s95, v239\n"
      "v_bfe_u32 v149, v148, 1, 3\n"
      "v_cmp_lt_u32 s[70:71], s44, v148\n"
      "v_min_u32_e64 v148, s44, v148\n"
      "s_mov_b32 s18, 100270464\n"
      "s_mov_b32 s19, 0x20000\n"
      "s_mov_b32 s78, 9604\n"
      "s_mov_b32 s79, 0x20000\n"
      "s_mov_b32 s22, 221184\n"
      "s_mov_b32 s23, 0x20000\n"
      "s_lshl_b32 s64, s40, 10\n"
      "v_add_u32 v36, s64, v221\n"
      "s_add_u32 s75, s64, 18432\n"
      "s_and_b32 s92, s40, 1\n"
      "s_lshl_b32 s92, s92, 2\n"
      "s_mul_i32 s93, s40, 9216\n"
      "v_and_b32_e64 v146, 2, v239\n"
      "v_xor_b32_e64 v146, v146, v147\n"
      "v_xor_b32_e64 v146, s92, v146\n"
      "v_lshl_add_u32 v146, v146, 4, s93\n"
      "v_mul_u32_u24 v99, 1152, v239\n"
      "v_add_u32_e64 v99, v99, v146\n"
      "s_cmp_lt_u32 s40, 2\n"
      "s_cselect_b32 s65, s64, 163840\n"
      "s_waitcnt lgkmcnt(0)\n"
      "s_cselect_b64 s[72:73], s[20:21], s[72:73]\n"
      "global_load_dword v69, v69, s[86:87]\n"
      "global_load_dword a255, v8, s[72:73]\n"
      "global_load_dword a253, v236, s[46:47]\n"
      "s_mul_i32 s92, s40, 8192\n"
      "s_add_u32 s88, s88, s92\n"
      "s_addc_u32 s89, s89, 0\n"
      "s_add_u32 m0, s64, 124928\n"
      "s_movk_i32 s92, 0\n"
      "buffer_load_dwordx4 v36, s[76:79], s92 offen  lds\n"
      "s_add_u32 m0, s64, 129024\n"
      "s_movk_i32 s92, 4096\n"
      "buffer_load_dwordx4 v36, s[76:79], s92 offen  lds\n"
      "s_add_u32 m0, s64, 133120\n"
      "s_cmp_lt_u32 s40, 2\n"
      "s_cselect_b32 m0, m0, s65\n"
      "s_movk_i32 s92, 8192\n"
      "buffer_load_dwordx4 v36, s[76:79], s92 offen  lds\n"
      "s_load_dwordx16 s[0:15], s[82:83], 0\n"
      "s_load_dwordx16 s[48:63], s[84:85], 0\n"
      "s_load_dwordx16 s[24:39], s[84:85], 64\n"
      "s_load_dwordx2 s[66:67], s[80:81], 0\n"
      "s_add_u32 s93, s41, 1216\n"
      "v_lshl_add_u32 v236, v148, 8, s41\n"
      "v_mul_u32_u24 v8, 192, v148\n"
      "v_add_u32 v8, s93, v8\n"
      "v_cmp_lt_u32 vcc, 18, v148\n"
      "v_mov_b32 v146, s96\n"
      "s_nop 0\n"
      "v_cndmask_b32 v236, v236, v8, vcc\n"
      "v_cmp_eq_u32 vcc, s44, v148\n"
      "s_nop 1\n"
      "v_cndmask_b32 v236, v236, v146, vcc\n"
      "v_subrev_u32 v146, 5184, v236\n"
      "v_cmp_le_u32 vcc, 5184, v236\n"
      "v_mul_u32_u24 v235, 58255, v236\n"
      "v_lshrrev_b32_e64 v235, 22, v235\n"
      "v_mul_u32_u24 v68, 72, v235\n"
      "v_sub_u32 v68, v236, v68\n"
      "v_min_u32 v8, 72, v146\n"
      "v_cndmask_b32 v235, v235, v8, vcc\n"
      "v_subrev_u32 v8, 72, v146\n"
      "v_min_u32 v8, 72, v8\n"
      "v_cndmask_b32 v68, v68, v8, vcc\n"
      "v_mad_u32_u24 v8, v235, 7, 9\n"
      "v_subrev_u32 v236, 0x1ff, v8\n"
      "v_min_u32 v8, v8, v236\n"
      "v_mad_u32_u24 v236, v68, 7, 9\n"
      "v_subrev_u32 v146, 0x1ff, v236\n"
      "v_min_u32 v236, v236, v146\n"
      "v_mul_u32_u24 v8, 0x1ff, v8\n"
      "v_add_u32 v8, v8, v236\n"
      "v_mul_u32_u24 v236, 0x180, v8\n"
      "v_bfrev_b32 v148, 1\n"
      "v_cndmask_b32 v236, v236, v148, s[70:71]\n"
      "v_xor_b32 v149, v149, v147\n"
      "v_lshl_add_u32 v236, v149, 4, v236\n"
      "s_add_u32 m0, s64, 150784\n"
      "s_nop 0\n"
      "buffer_load_dwordx4 v236, s[16:19], 0 offen  lds\n"
      "s_add_u32 m0, s64, 154752\n"
      "s_nop 0\n"
      "buffer_load_dwordx4 v236, s[16:19], 0 offen offset:128  lds\n"
      "s_add_u32 m0, s64, 158720\n"
      "s_nop 0\n"
      "buffer_load_dwordx4 v236, s[16:19], 0 offen offset:256  lds\n"
      "s_movk_i32 s95, 504\n"
      "s_and_b32 s92, s40, 1\n"
      "s_lshl_b32 s92, s92, 2\n"
      "v_lshrrev_b32 v236, 1, v239\n"
      "v_xor_b32 v236, v236, v147\n"
      "v_xor_b32 v236, s92, v236\n"
      "v_lshlrev_b32 v236, 4, v236\n"
      "s_lshl_b32 s92, s40, 3\n"
      "s_brev_b32 s43, 1\n"
      "s_cmp_lt_u32 s40, 2\n"
      "s_cselect_b32 s93, 32, -16\n"
      "s_cselect_b32 s94, 0, 128\n"
      "s_cselect_b32 s43, 256, s43\n"
      "v_add_u32 v8, s92, v239\n"
      "v_mul_u32_u24 v239, 37, v8\n"
      "v_lshrrev_b32 v239, 8, v239\n"
      "v_mad_u32_u24 v239, v239, s95, v8\n"
      "v_mul_u32_u24 v239, 384, v239\n"
      "v_add_u32 v16, v239, v236\n"
      "v_add_u32 v239, s93, v8\n"
      "v_mul_u32_u24 v147, 37, v239\n"
      "v_lshrrev_b32 v147, 8, v147\n"
      "v_mad_u32_u24 v147, v147, s95, v239\n"
      "v_mul_u32_u24 v147, 384, v147\n"
      "v_add3_u32 v17, v147, v236, s94\n"
      "v_add_u32 v239, 16, v8\n"
      "v_mul_u32_u24 v8, 37, v239\n"
      "v_lshrrev_b32_e64 v8, 8, v8\n"
      "v_mad_u32_u24 v8, v8, s95, v239\n"
      "v_mul_u32_u24 v8, 384, v8\n"
      "s_movk_i32 s94, 128\n"
      "v_add3_u32 v18, v8, v236, s94\n"
      "v_add_u32 v19, 256, v16\n"
      "v_add_u32 v15, s43, v17\n"
      "s_add_u32 s92, s41, 0\n"
      "s_mul_i32 s93, s92, 58255\n"
      "s_lshr_b32 s93, s93, 22\n"
      "s_mul_i32 s93, s93, 439\n"
      "s_add_u32 s45, s92, s93\n"
      "s_mul_i32 s45, s45, 2688\n"
      "s_add_u32 s45, s45, 589824\n"
      "s_add_u32 m0, s64, 0\n"
      "s_nop 0\n"
      "buffer_load_dwordx4 v16, s[16:19], s45 offen  lds\n"
      "s_add_u32 m0, s64, 4096\n"
      "s_nop 0\n"
      "buffer_load_dwordx4 v17, s[16:19], s45 offen  lds\n"
      "s_add_u32 m0, s64, 8192\n"
      "s_nop 0\n"
      "buffer_load_dwordx4 v18, s[16:19], s45 offen  lds\n"
      "s_add_u32 m0, s64, 12288\n"
      "s_nop 0\n"
      "buffer_load_dwordx4 v19, s[16:19], s45 offen  lds\n"
      "s_add_u32 m0, s65, 16384\n"
      "s_nop 0\n"
      "buffer_load_dwordx4 v15, s[16:19], s45 offen  lds\n"
      "s_add_u32 s92, s41, 0\n"
      "s_mul_i32 s93, s92, 58255\n"
      "s_lshr_b32 s93, s93, 22\n"
      "s_add_u32 s47, s92, s93\n"
      "s_mul_i32 s47, s47, 4802\n"
      "s_and_b32 s47, s47, -4\n"
      "s_add_u32 s92, s41, 256\n"
      "s_mul_i32 s93, s92, 58255\n"
      "s_lshr_b32 s93, s93, 22\n"
      "s_add_u32 s74, s92, s93\n"
      "s_mul_i32 s74, s74, 4802\n"
      "s_and_b32 s74, s74, -4\n"
      "s_mul_i32 s93, s93, 439\n"
      "s_add_u32 s46, s92, s93\n"
      "s_mul_i32 s46, s46, 2688\n"
      "s_add_u32 s46, s46, 589824\n"
      "v_subrev_u32 v8, 2, v238\n"
      "v_cmp_ge_u32 s[72:73], s44, v8\n"
      "v_min_u32 v8, s44, v8\n"
      "s_add_u32 s93, s41, 1216\n"
      "v_lshl_add_u32 v236, v8, 8, s41\n"
      "v_mul_u32_u24 v239, 192, v8\n"
      "v_add_u32 v239, s93, v239\n"
      "v_cmp_lt_u32 vcc, 18, v8\n"
      "v_mov_b32 v147, s96\n"
      "s_nop 0\n"
      "v_cndmask_b32 v236, v236, v239, vcc\n"
      "v_cmp_eq_u32 vcc, s44, v8\n"
      "s_nop 1\n"
      "v_cndmask_b32 v236, v236, v147, vcc\n"
      "v_subrev_u32 v147, 5184, v236\n"
      "v_cmp_le_u32 vcc, 5184, v236\n"
      "v_mul_u32_u24 v148, 58255, v236\n"
      "v_lshrrev_b32 v148, 22, v148\n"
      "v_mul_u32_u24 v149, 72, v148\n"
      "v_sub_u32_e64 v149, v236, v149\n"
      "v_min_u32 v239, 72, v147\n"
      "v_cndmask_b32 v148, v148, v239, vcc\n"
      "v_subrev_u32 v239, 72, v147\n"
      "v_min_u32 v239, 72, v239\n"
      "v_cndmask_b32 v149, v149, v239, vcc\n"
      "v_mul_u32_u24 v38, 73, v148\n"
      "v_add_u32 v38, v38, v149\n"
      "v_mul_u32_u24 v38, 4802, v38\n"
      "v_mul_u32_u24 v8, 0x1ff, v148\n"
      "v_add_u32_e64 v8, v8, v149\n"
      "v_mul_u32_u24 v8, 2688, v8\n"
      "v_add_u32 v8, 589824, v8\n"
      "v_cmp_eq_u32 vcc, 72, v148\n"
      "v_or_b32 v68, 0x80000000, v8\n"
      "s_nop 0\n"
      "v_cndmask_b32 v8, v8, v68, vcc\n"
      "v_cmp_eq_u32 vcc, 72, v149\n"
      "v_or_b32_e64 v68, 0x40000000, v8\n"
      "s_nop 0\n"
      "v_cndmask_b32 v8, v8, v68, vcc\n"
      "v_mov_b32 v239, 0x20000000\n"
      "v_cndmask_b32 v8, v239, v8, s[72:73]\n"
      "v_cndmask_b32 v38, v239, v38, s[72:73]\n"
      "v_mov_b32 v37, v239\n"
      "s_nop 1\n"
      "v_mov_b32_dpp v37, v8 wave_shr:1 row_mask:0xf bank_mask:0xf\n"
      "v_mov_b32 v79, 0\n"
      "v_mov_b32 v83, 0\n"
      "v_mov_b32 v87, 0\n"
      "v_mov_b32 v91, 0\n"
      "v_mov_b32 v95, 0\n"
      "s_lshl_b32 s94, s40, 2\n"
      "s_add_u32 s94, s94, 115456\n"
      "v_add_u32 v31, s94, v221\n"
      "v_and_b32 v68, 3, v238\n"
      "v_cmp_ne_u32 vcc, 0, v68\n"
      "v_mov_b32 v68, 0x3f803f80\n"
      "s_nop 0\n"
      "v_cndmask_b32_e64 v32, v68, 0, vcc\n"
      "v_mov_b32 v33, v32\n"
      "v_mov_b32 v34, v32\n"
      "v_mov_b32 v35, v32\n"
      "v_and_b32 v121, 15, v238\n"
      "v_lshrrev_b32_e64 v66, 4, v238\n"
      "v_mul_u32_u24 v121, 196, v121\n"
      "v_add_u32 v121, 124928, v121\n"
      "v_lshl_add_u32 v66, v66, 4, v121\n"
      "v_min_u32 v213, 192, v237\n"
      "v_add_u32 v213, 124928, v213\n"
      "global_load_dwordx4 v[236:239], v221, s[88:89]\n"
      "s_waitcnt lgkmcnt(0)\n"
      "s_pack_ll_b32_b16 s92, s0, s8\n"
      "s_waitcnt vmcnt(14)\n"
      "v_dot2_f32_bf16 v235, s92, v69, 0\n"
      "s_pack_hh_b32_b16 s93, s0, s8\n"
      "v_dot2_f32_bf16 v232, s93, v69, 0\n"
      "s_pack_ll_b32_b16 s94, s1, s9\n"
      "v_dot2_f32_bf16 v182, s94, v69, 0\n"
      "s_pack_hh_b32_b16 s95, s1, s9\n"
      "v_dot2_f32_bf16 v233, s95, v69, 0\n"
      "s_pack_ll_b32_b16 s92, s2, s10\n"
      "v_dot2_f32_bf16 v181, s92, v69, 0\n"
      "s_pack_hh_b32_b16 s93, s2, s10\n"
      "v_dot2_f32_bf16 v194, s93, v69, 0\n"
      "s_pack_ll_b32_b16 s94, s3, s11\n"
      "v_dot2_f32_bf16 v132, s94, v69, 0\n"
      "s_pack_hh_b32_b16 s95, s3, s11\n"
      "v_dot2_f32_bf16 v180, s95, v69, 0\n"
      "s_pack_ll_b32_b16 s92, s4, s12\n"
      "v_dot2_f32_bf16 v100, s92, v69, 0\n"
      "s_pack_hh_b32_b16 s93, s4, s12\n"
      "v_dot2_f32_bf16 v133, s93, v69, 0\n"
      "s_pack_ll_b32_b16 s94, s5, s13\n"
      "v_dot2_f32_bf16 v171, s94, v69, 0\n"
      "global_load_dwordx4 v[146:149], v221, s[88:89] offset:1024\n"
      "s_pack_hh_b32_b16 s95, s5, s13\n"
      "v_dot2_f32_bf16 v196, s95, v69, 0\n"
      "s_pack_ll_b32_b16 s92, s6, s14\n"
      "v_dot2_f32_bf16 v172, s92, v69, 0\n"
      "s_pack_hh_b32_b16 s93, s6, s14\n"
      "v_dot2_f32_bf16 v208, s93, v69, 0\n"
      "s_pack_ll_b32_b16 s94, s7, s15\n"
      "v_dot2_f32_bf16 v183, s94, v69, 0\n"
      "s_pack_hh_b32_b16 s95, s7, s15\n"
      "v_dot2_f32_bf16 v131, s95, v69, 0\n"
      "s_lshl_b32 s92, s40, 4\n"
      "s_lshr_b64 s[66:67], s[66:67], s92\n"
      "s_lshl_b32 s66, s66, 16\n"
      "v_mov_b32 v69, s66\n"
      "v_max_f32 v69, 0xc2c80000, v69\n"
      "v_min_f32 v69, 0x40930000, v69\n"
      "v_mul_f32 v69, 0x3fb8aa3b, v69\n"
      "v_exp_f32 v69, v69\n"
      "s_nop 0\n"
      "v_cvt_pk_bf16_f32 v69, v69, v69\n"
      "v_lshlrev_b32 v69, 16, v69\n"
      "v_max_f32 v235, 0, v235\n"
      "v_max_f32_e64 v232, 0, v232\n"
      "global_load_dwordx4 v[0:3], v221, s[88:89] offset:2048\n"
      "v_cvt_pk_bf16_f32 v235, v235, v232\n"
      "v_max_f32 v182, 0, v182\n"
      "v_max_f32 v233, 0, v233\n"
      "v_cvt_pk_bf16_f32 v182, v182, v233\n"
      "v_max_f32 v181, 0, v181\n"
      "v_max_f32 v194, 0, v194\n"
      "v_cvt_pk_bf16_f32 v181, v181, v194\n"
      "v_max_f32 v132, 0, v132\n"
      "v_max_f32 v180, 0, v180\n"
      "v_cvt_pk_bf16_f32 v132, v132, v180\n"
      "v_max_f32 v100, 0, v100\n"
      "v_max_f32 v133, 0, v133\n"
      "v_cvt_pk_bf16_f32 v100, v100, v133\n"
      "v_max_f32 v171, 0, v171\n"
      "v_max_f32 v196, 0, v196\n"
      "v_cvt_pk_bf16_f32 v171, v171, v196\n"
      "v_max_f32 v172, 0, v172\n"
      "v_max_f32 v208, 0, v208\n"
      "v_cvt_pk_bf16_f32 v172, v172, v208\n"
      "v_max_f32 v183, 0, v183\n"
      "v_max_f32 v131, 0, v131\n"
      "v_cvt_pk_bf16_f32 v183, v183, v131\n"
      "s_mov_b32 s42, 0\n"
      "s_add_u32 m0, s75, 0\n"
      "s_nop 0\n"
      "buffer_load_dwordx4 v99, s[20:23], s42 offen  lds\n"
      "s_pack_ll_b32_b16 s92, s48, s50\n"
      "v_dot2_f32_bf16 v233, s92, v235, 0\n"
      "s_pack_hh_b32_b16 s93, s48, s50\n"
      "v_dot2_f32_bf16 v232, s93, v235, 0\n"
      "s_pack_ll_b32_b16 s94, s49, s51\n"
      "v_dot2_f32_bf16 v180, s94, v235, 0\n"
      "s_pack_hh_b32_b16 s95, s49, s51\n"
      "v_dot2_f32_bf16 v131, s95, v235, 0\n"
      "s_pack_ll_b32_b16 s92, s52, s54\n"
      "v_dot2_f32_bf16 v233, s92, v182, v233\n"
      "s_pack_hh_b32_b16 s93, s52, s54\n"
      "v_dot2_f32_bf16 v232, s93, v182, v232\n"
      "s_pack_ll_b32_b16 s94, s53, s55\n"
      "v_dot2_f32_bf16 v180, s94, v182, v180\n"
      "s_pack_hh_b32_b16 s95, s53, s55\n"
      "v_dot2_f32_bf16 v131, s95, v182, v131\n"
      "s_pack_ll_b32_b16 s92, s56, s58\n"
      "v_dot2_f32_bf16 v233, s92, v181, v233\n"
      "s_pack_hh_b32_b16 s93, s56, s58\n"
      "v_dot2_f32_bf16 v232, s93, v181, v232\n"
      "s_pack_ll_b32_b16 s94, s57, s59\n"
      "v_dot2_f32_bf16 v180, s94, v181, v180\n"
      "s_add_u32 m0, s75, 3968\n"
      "s_nop 0\n"
      "buffer_load_dwordx4 v99, s[20:23], s42 offen offset:128  lds\n"
      "s_pack_hh_b32_b16 s95, s57, s59\n"
      "v_dot2_f32_bf16 v131, s95, v181, v131\n"
      "s_pack_ll_b32_b16 s92, s60, s62\n"
      "v_dot2_f32_bf16 v233, s92, v132, v233\n"
      "s_pack_hh_b32_b16 s93, s60, s62\n"
      "v_dot2_f32_bf16 v232, s93, v132, v232\n"
      "s_pack_ll_b32_b16 s94, s61, s63\n"
      "v_dot2_f32_bf16 v180, s94, v132, v180\n"
      "s_pack_hh_b32_b16 s95, s61, s63\n"
      "v_dot2_f32_bf16 v131, s95, v132, v131\n"
      "s_pack_ll_b32_b16 s92, s24, s26\n"
      "v_dot2_f32_bf16 v233, s92, v100, v233\n"
      "s_pack_hh_b32_b16 s93, s24, s26\n"
      "v_dot2_f32_bf16 v232, s93, v100, v232\n"
      "s_pack_ll_b32_b16 s94, s25, s27\n"
      "v_dot2_f32_bf16 v180, s94, v100, v180\n"
      "s_pack_hh_b32_b16 s95, s25, s27\n"
      "v_dot2_f32_bf16 v131, s95, v100, v131\n"
      "s_pack_ll_b32_b16 s92, s28, s30\n"
      "v_dot2_f32_bf16 v233, s92, v171, v233\n"
      "s_pack_hh_b32_b16 s93, s28, s30\n"
      "v_dot2_f32_bf16 v232, s93, v171, v232\n"
      "s_pack_ll_b32_b16 s94, s29, s31\n"
      "s_add_u32 m0, s75, 7936\n"
      "s_nop 0\n"
      "buffer_load_dwordx4 v99, s[20:23], s42 offen offset:256  lds\n"
      "v_dot2_f32_bf16 v180, s94, v171, v180\n"
      "s_pack_hh_b32_b16 s95, s29, s31\n"
      "v_dot2_f32_bf16 v131, s95, v171, v131\n"
      "s_pack_ll_b32_b16 s92, s32, s34\n"
      "v_dot2_f32_bf16 v233, s92, v172, v233\n"
      "s_pack_hh_b32_b16 s93, s32, s34\n"
      "v_dot2_f32_bf16 v232, s93, v172, v232\n"
      "s_pack_ll_b32_b16 s94, s33, s35\n"
      "v_dot2_f32_bf16 v180, s94, v172, v180\n"
      "s_pack_hh_b32_b16 s95, s33, s35\n"
      "v_dot2_f32_bf16 v131, s95, v172, v131\n"
      "s_pack_ll_b32_b16 s92, s36, s38\n"
      "v_dot2_f32_bf16 v233, s92, v183, v233\n"
      "s_pack_hh_b32_b16 s93, s36, s38\n"
      "v_dot2_f32_bf16 v232, s93, v183, v232\n"
      "s_pack_ll_b32_b16 s94, s37, s39\n"
      "v_dot2_f32_bf16 v180, s94, v183, v180\n"
      "s_pack_hh_b32_b16 s95, s37, s39\n"
      "v_dot2_f32_bf16 v131, s95, v183, v131\n"
      "s_load_dwordx2 s[28:29], s[90:91], 8\n"
      "s_load_dwordx2 s[34:35], s[90:91], 16\n"
      "s_load_dwordx2 s[24:25], s[90:91], 24\n"
      "s_add_u32 m0, s75, 11904\n"
      "s_nop 0\n"
      "buffer_load_dwordx4 v99, s[20:23], s42 offen offset:384  lds\n"
      "s_load_dwordx2 s[36:37], s[90:91], 40\n"
      "s_mov_b32 s26, 100270464\n"
      "s_mov_b32 s27, 0x20000\n"
      "s_mov_b32 s38, 73728\n"
      "s_mov_b32 s39, 0x20000\n"
      "s_mov_b32 s30, 25589860\n"
      "s_mov_b32 s31, 0x20000\n"
      "s_mov_b32 s53, 0x3fb8aa3b\n"
      "s_mov_b32 s51, 0x2a8cbccc\n"
      "s_mov_b32 s52, 0x38fba882\n"
      "s_mov_b32 s58, 0xffff0000\n"
      "s_mov_b32 s59, 0x0\n"
      "s_mov_b32 s60, 0x0\n"
      "s_mov_b32 s61, 0xffff\n"
      "s_mov_b32 s62, 0x0\n"
      "s_mov_b32 s63, 0xffff0000\n"
      "v_mov_b32 v68, 0x41f00000\n"
      "v_cmp_nle_f32 s[54:55], v69, v68\n"
      "v_mul_f32 v69, s53, v69\n"
      "s_nop 0\n"
      "v_readfirstlane_b32 s50, v69\n"
      "v_cvt_pk_bf16_f32 v233, v233, v233\n"
      "v_lshlrev_b32 v233, 16, v233\n"
      "s_add_u32 m0, s75, 15872\n"
      "s_nop 0\n"
      "buffer_load_dwordx4 v99, s[20:23], s42 offen offset:512  lds\n"
      "v_mul_f32 v233, 0xbfb8aa3b, v233\n"
      "v_exp_f32 v233, v233\n"
      "s_nop 0\n"
      "v_add_f32 v233, 1.0, v233\n"
      "v_rcp_f32 v233, v233\n"
      "s_nop 0\n"
      "v_cvt_pk_bf16_f32 v233, v233, v233\n"
      "v_lshlrev_b32 v233, 16, v233\n"
      "v_add_f32 v233, -0.5, v233\n"
      "v_mul_f32 v233, 0x41b8aa3b, v233\n"
      "ds_write_b32 v130, v233\n"
      "v_cvt_pk_bf16_f32 v232, v232, v232\n"
      "v_lshlrev_b32 v232, 16, v232\n"
      "v_mul_f32 v232, 0xbfb8aa3b, v232\n"
      "v_exp_f32 v232, v232\n"
      "s_nop 0\n"
      "v_add_f32 v232, 1.0, v232\n"
      "v_rcp_f32_e64 v232, v232\n"
      "s_nop 0\n"
      "v_cvt_pk_bf16_f32 v232, v232, v232\n"
      "v_lshlrev_b32 v232, 16, v232\n"
      "v_add_f32 v232, -0.5, v232\n"
      "v_mul_f32 v232, 0x41b8aa3b, v232\n"
      "ds_write_b32 v130, v232 offset:704\n"
      "v_cvt_pk_bf16_f32 v180, v180, v180\n"
      "v_lshlrev_b32 v180, 16, v180\n"
      "s_add_u32 m0, s75, 19840\n"
      "s_nop 0\n"
      "buffer_load_dwordx4 v99, s[20:23], s42 offen offset:640  lds\n"
      "v_mul_f32 v180, 0xbfb8aa3b, v180\n"
      "v_exp_f32 v180, v180\n"
      "s_nop 0\n"
      "v_add_f32 v180, 1.0, v180\n"
      "v_rcp_f32_e64 v180, v180\n"
      "s_nop 0\n"
      "v_cvt_pk_bf16_f32 v180, v180, v180\n"
      "v_lshlrev_b32 v180, 16, v180\n"
      "v_add_f32 v180, -0.5, v180\n"
      "v_mul_f32 v180, 0x41b8aa3b, v180\n"
      "ds_write_b32 v130, v180 offset:1408\n"
      "v_cvt_pk_bf16_f32 v131, v131, v131\n"
      "v_lshlrev_b32 v131, 16, v131\n"
      "v_mul_f32 v131, 0xbfb8aa3b, v131\n"
      "v_exp_f32 v131, v131\n"
      "s_nop 0\n"
      "v_add_f32 v131, 1.0, v131\n"
      "v_rcp_f32 v131, v131\n"
      "s_nop 0\n"
      "v_cvt_pk_bf16_f32 v131, v131, v131\n"
      "v_lshlrev_b32 v131, 16, v131\n"
      "v_add_f32 v131, -0.5, v131\n"
      "v_mul_f32 v131, 0x41b8aa3b, v131\n"
      "ds_write_b32 v130, v131 offset:2112\n"
      "s_mul_i32 s76, s40, 704\n"
      "s_add_u32 s76, s76, 122112\n"
      "s_mov_b32 s43, 36864\n"
      "s_add_u32 m0, s75, 24576\n"
      "s_nop 0\n"
      "buffer_load_dwordx4 v99, s[20:23], s43 offen  lds\n"
      "s_add_u32 m0, s75, 28544\n"
      "s_nop 0\n"
      "buffer_load_dwordx4 v99, s[20:23], s43 offen offset:128  lds\n"
      "s_add_u32 m0, s75, 32512\n"
      "s_nop 0\n"
      "buffer_load_dwordx4 v99, s[20:23], s43 offen offset:256  lds\n"
      "s_add_u32 m0, s75, 36480\n"
      "s_nop 0\n"
      "buffer_load_dwordx4 v99, s[20:23], s43 offen offset:384  lds\n"
      "s_add_u32 m0, s75, 40448\n"
      "s_nop 0\n"
      "buffer_load_dwordx4 v99, s[20:23], s43 offen offset:512  lds\n"
      "s_add_u32 m0, s75, 44416\n"
      "s_nop 0\n"
      "buffer_load_dwordx4 v99, s[20:23], s43 offen offset:640  lds\n"
      "s_mov_b32 s45, s46\n"
      "s_mov_b32 s49, s47\n"
      "s_mov_b32 s66, 0\n"
      "s_mov_b32 s67, 0\n"
      "s_mov_b32 s68, 0\n"
      "s_mov_b32 s69, 0\n"
      "s_mov_b32 s70, 0\n"
      "s_mov_b32 s71, 0\n"
      "s_mov_b32 s48, 0\n"
      "s_mov_b32 s33, 0x7fff7fff\n"
      "s_mov_b32 s42, 1\n"
      "s_mov_b32 s43, 5\n"
      "v_readlane_b32 s47, v38, 4\n"
      "s_nop 0\n"
      "s_and_b32 s47, s47, -4\n"
      "s_waitcnt vmcnt(12)\n"
      "v_add_u32 v116, 150784, v0\n"
      "v_add_u32 v143, 150784, v1\n"
      "v_add_u32 v77, 49152, v236\n"
      "v_add_u32 v76, 49152, v237\n"
      "v_add_u32 v108, 49152, v238\n"
      "v_add_u32 v90, 49152, v239\n"
      "v_add_u32 v82, 49152, v146\n"
      "s_waitcnt vmcnt(6)\n"
      "s_waitcnt lgkmcnt(0)\n"
      "s_barrier\n"
      "s_add_u32 m0, s75, 49152\n"
      "s_mov_b32 s78, 73728\n"
      "buffer_load_dwordx4 v99, s[20:23], s78 offen  lds\n"
      "s_add_u32 m0, s75, 53120\n"
      "ds_read_b128 a[216:219], v116\n"
      "ds_read_b64_tr_b16 a[48:49], v238\n"
      "ds_read_b128 a[220:223], v0\n"
      "ds_read_b64_tr_b16 a[2:3], v236 offset:512\n"
      "ds_read_b64_tr_b16 a[72:73], v236 offset:12288\n"
      "ds_read_b64_tr_b16 a[74:75], v236 offset:12800\n"
      "buffer_load_dwordx4 v99, s[20:23], s78 offen offset:128  lds\n"
      "ds_read_b64_tr_b16 a[0:1], v236\n"
      "ds_read_b64_tr_b16 a[50:51], v238 offset:512\n"
      "ds_read_b64_tr_b16 a[24:25], v237\n"
      "v_add_u32 v155, 6272, v66\n"
      "s_add_u32 m0, s75, 57088\n"
      "ds_read_b128 a[224:227], v0 offset:2048\n"
      "ds_read_b64_tr_b16 a[26:27], v237 offset:512\n"
      "buffer_load_dwordx4 v99, s[20:23], s78 offen offset:256  lds\n"
      "ds_read2_b32 v[68:69], v155 offset0:16 offset1:17\n"
      "ds_read_b32 v105, v121 offset:3328\n"
      "ds_read_b128 a[244:247], v1 offset:4096\n"
      "ds_read_b64_tr_b16 a[120:121], v238 offset:12288\n"
      "s_add_u32 m0, s75, 61056\n"
      "ds_read_b64_tr_b16 a[122:123], v238 offset:12800\n"
      "s_waitcnt lgkmcnt(8)\n"
      "v_mfma_f32_16x16x32_bf16 v[232:235], a[48:51], a[216:219], 0\n"
      "buffer_load_dwordx4 v99, s[20:23], s78 offen offset:384  lds\n"
      "ds_read_b64_tr_b16 a[98:99], v237 offset:12800\n"
      "v_mfma_f32_16x16x32_bf16 v[180:183], a[0:3], a[220:223], 0\n"
      "s_mov_b32 s79, 110592\n"
      "v_add_u32 v120, 3136, v66\n"
      "s_waitcnt lgkmcnt(6)\n"
      "v_mfma_f32_16x16x32_bf16 v[130:133], a[24:27], a[224:227], 0\n"
      "s_add_u32 m0, s75, 65024\n"
      "ds_read_b128 a[228:231], v0 offset:4096\n"
      "ds_read_b64_tr_b16 a[96:97], v237 offset:12288\n"
      "v_mfma_f32_16x16x32_bf16 v[194:197], a[24:27], a[220:223], 0\n"
      "buffer_load_dwordx4 v99, s[20:23], s78 offen offset:512  lds\n"
      "v_mfma_f32_16x16x32_bf16 v[170:173], a[48:51], a[224:227], 0\n"
      "ds_read_b128 a[236:239], v1\n"
      "ds_read2_b32 v[58:59], v120 offset0:18 offset1:19\n"
      "v_mfma_f32_16x16x32_bf16 v[100:103], a[0:3], a[224:227], 0\n"
      "ds_read_b128 a[232:235], v143\n"
      "s_add_u32 m0, s75, 68992\n"
      "ds_read2_b32 v[70:71], v155 offset0:18 offset1:19\n"
      "v_mfma_f32_16x16x32_bf16 v[208:211], a[72:75], a[220:223], 0\n"
      "buffer_load_dwordx4 v99, s[20:23], s78 offen offset:640  lds\n"
      "v_mfma_f32_16x16x32_bf16 v[244:247], a[0:3], a[216:219], 0\n"
      "ds_read_b128 a[252:255], v0 offset:6144\n"
      "s_waitcnt lgkmcnt(5)\n"
      "v_mfma_f32_16x16x32_bf16 v[240:243], a[96:99], a[224:227], 0\n"
      "ds_read2_b32 v[72:73], v155 offset0:32 offset1:33\n"
      "ds_read_b128 a[240:243], v1 offset:2048\n"
      "v_mfma_f32_16x16x32_bf16 v[252:255], a[96:99], a[228:231], 0\n"
      "s_waitcnt vmcnt(6)\n"
      "s_barrier\n"
      "global_load_dwordx4 v[4:7], v221, s[88:89] offset:3072\n"
      "ds_read_b64_tr_b16 a[4:5], v236 offset:24576\n"
      "ds_read_b64_tr_b16 a[28:29], v237 offset:24576\n"
      "s_add_u32 s88, s88, 4096\n"
      "s_addc_u32 s89, s89, 0\n"
      "ds_read_b64_tr_b16 a[54:55], v238 offset:25088\n"
      "ds_read_b64_tr_b16 a[124:125], v238 offset:36864\n"
      "ds_read_b64_tr_b16 a[126:127], v238 offset:37376\n"
      "ds_read_b64_tr_b16 a[100:101], v237 offset:36864\n"
      "v_mfma_f32_16x16x32_bf16 v[200:203], a[72:75], a[224:227], 0\n"
      "ds_read_b64_tr_b16 a[102:103], v237 offset:37376\n"
      "global_load_dwordx4 v[8:11], v221, s[88:89]\n"
      "ds_read_b64_tr_b16 a[52:53], v238 offset:24576\n"
      "ds_read_b64_tr_b16 a[76:77], v236 offset:36864\n"
      "v_mfma_f32_16x16x32_bf16 v[138:141], a[120:123], a[224:227], 0\n"
      "ds_read_b64_tr_b16 a[30:31], v237 offset:25088\n"
      "ds_read_b64_tr_b16 a[78:79], v236 offset:37376\n"
      "ds_read_b64_tr_b16 a[6:7], v236 offset:25088\n"
      "v_mfma_f32_16x16x32_bf16 v[224:227], a[96:99], a[216:219], 0\n"
      "global_load_dwordx4 v[12:15], v221, s[88:89] offset:1024\n"
      "v_mfma_f32_16x16x32_bf16 v[248:251], a[120:123], a[216:219], 0\n"
      "ds_read2_b32 v[50:51], v66 offset0:34 offset1:35\n"
      "ds_read2_b32 v[52:53], v120 offset0:0 offset1:1\n"
      "ds_read_b32 v104, v121 offset:192\n"
      "v_mfma_f32_16x16x32_bf16 v[216:219], a[72:75], a[216:219], 0\n"
      "ds_read_b128 a[224:227], v143 offset:4096\n"
      "v_lshl_add_u32 v105, v105, 2, s76\n"
      "v_mfma_f32_16x16x32_bf16 v[162:165], a[0:3], a[228:231], 0\n"
      "global_load_dwordx4 v[20:23], v221, s[88:89] offset:2048\n"
      "ds_read_b32 v154, v213 offset:9408\n"
      "v_mfma_f32_16x16x32_bf16 v[212:215], a[120:123], a[220:223], 0\n"
      "ds_read2_b32 v[42:43], v66 offset0:2 offset1:3\n"
      "s_waitcnt lgkmcnt(6)\n"
      "v_mfma_f32_16x16x32_bf16 v[212:215], a[124:127], a[236:239], v[212:215]\n"
      "ds_read2_b32 v[62:63], v120 offset0:34 offset1:35\n"
      "ds_read_b128 a[248:251], v116 offset:4096\n"
      "v_mfma_f32_16x16x32_bf16 v[190:193], a[96:99], a[220:223], 0\n"
      "global_load_dwordx4 v[24:27], v221, s[88:89] offset:3072\n"
      "v_mfma_f32_16x16x32_bf16 v[220:223], a[48:51], a[220:223], 0\n"
      "ds_read2_b32 v[44:45], v66 offset0:16 offset1:17\n"
      "s_mov_b32 s82, 0xffff\n"
      "ds_read2_b32 v[46:47], v66 offset0:18 offset1:19\n"
      "v_mfma_f32_16x16x32_bf16 v[220:223], a[52:55], a[236:239], v[220:223]\n"
      "ds_read2_b32 v[60:61], v120 offset0:32 offset1:33\n"
      "v_lshl_add_u32 v73, v73, 2, s76\n"
      "s_add_u32 m0, s75, 73728\n"
      "v_mfma_f32_16x16x32_bf16 v[190:193], a[100:103], a[236:239], v[190:193]\n"
      "buffer_load_dwordx4 v99, s[20:23], s79 offen  lds\n"
      "v_mfma_f32_16x16x32_bf16 v[166:169], a[24:27], a[216:219], 0\n"
      "s_add_u32 m0, s75, 77696\n"
      "ds_read2_b32 v[54:55], v120 offset0:2 offset1:3\n"
      "v_mfma_f32_16x16x32_bf16 v[180:183], a[4:7], a[236:239], v[180:183]\n"
      "ds_read2_b32 v[48:49], v66 offset0:32 offset1:33\n"
      "s_mov_b32 s78, 147456\n"
      "v_mfma_f32_16x16x32_bf16 v[194:197], a[28:31], a[236:239], v[194:197]\n"
      "ds_read2_b32 v[56:57], v120 offset0:16 offset1:17\n"
      "ds_read2_b32 v[74:75], v155 offset0:34 offset1:35\n"
      "v_mfma_f32_16x16x32_bf16 v[232:235], a[52:55], a[232:235], v[232:235]\n"
      "buffer_load_dwordx4 v99, s[20:23], s79 offen offset:128  lds\n"
      "v_mfma_f32_16x16x32_bf16 v[166:169], a[28:31], a[232:235], v[166:169]\n"
      "ds_read_b32 v157, v121 offset:6464\n"
      "v_lshl_add_u32 v70, v70, 2, s76\n"
      "v_mfma_f32_16x16x32_bf16 v[120:123], a[72:75], a[228:231], 0\n"
      "ds_read2_b32 v[64:65], v155 offset0:0 offset1:1\n"
      "s_add_u32 m0, s75, 81664\n"
      "v_mfma_f32_16x16x32_bf16 v[100:103], a[4:7], a[240:243], v[100:103]\n"
      "ds_read_b128 a[216:219], v0 offset:8192\n"
      "s_mov_b32 s91, 0x1ffff\n"
      "v_mfma_f32_16x16x32_bf16 v[244:247], a[4:7], a[232:235], v[244:247]\n"
      "buffer_load_dwordx4 v99, s[20:23], s79 offen offset:256  lds\n"
      "v_mfma_f32_16x16x32_bf16 v[240:243], a[100:103], a[240:243], v[240:243]\n"
      "s_mov_b32 s83, 0x0\n"
      "s_waitcnt lgkmcnt(4)\n"
      "v_lshl_add_u32 v63, v63, 2, s76\n"
      "v_mfma_f32_16x16x32_bf16 v[216:219], a[76:79], a[232:235], v[216:219]\n"
      "s_mov_b32 s90, 0xffffffff\n"
      "v_lshl_add_u32 v154, v154, 2, s76\n"
      "v_lshl_add_u32 v46, v46, 2, s76\n"
      "v_mfma_f32_16x16x32_bf16 v[208:211], a[76:79], a[236:239], v[208:211]\n"
      "ds_read_b128 a[236:239], v1 offset:10240\n"
      "v_lshl_add_u32 v54, v54, 2, s76\n"
      "s_add_u32 m0, s75, 85632\n"
      "v_mfma_f32_16x16x32_bf16 v[124:127], a[120:123], a[228:231], 0\n"
      "buffer_load_dwordx4 v99, s[20:23], s79 offen offset:384  lds\n"
      "s_add_u32 m0, s75, 89600\n"
      "v_mfma_f32_16x16x32_bf16 v[124:127], a[124:127], a[244:247], v[124:127]\n"
      "s_waitcnt vmcnt(9)\n"
      "s_barrier\n"
      "ds_read_b64_tr_b16 a[8:9], v77\n"
      "ds_read_b64_tr_b16 a[104:105], v76 offset:12288\n"
      "ds_read_b64_tr_b16 a[128:129], v108 offset:12288\n"
      "ds_read_b64_tr_b16 a[10:11], v77 offset:512\n"
      "ds_read_b64_tr_b16 a[32:33], v76\n"
      "v_mfma_f32_16x16x32_bf16 v[158:161], a[48:51], a[228:231], 0\n"
      "ds_read_b64_tr_b16 a[56:57], v108\n"
      "ds_read_b64_tr_b16 a[34:35], v76 offset:512\n"
      "buffer_load_dwordx4 v99, s[20:23], s79 offen offset:512  lds\n"
      "ds_read_b64_tr_b16 a[80:81], v77 offset:12288\n"
      "v_mfma_f32_16x16x32_bf16 v[130:133], a[28:31], a[240:243], v[130:133]\n"
      "ds_read_b64_tr_b16 a[130:131], v108 offset:12800\n"
      "ds_read_b64_tr_b16 a[58:59], v108 offset:512\n"
      "s_add_u32 m0, s75, 93568\n"
      "ds_read_b64_tr_b16 a[106:107], v76 offset:12800\n"
      "ds_read_b64_tr_b16 a[82:83], v77 offset:12800\n"
      "buffer_load_dwordx4 v99, s[20:23], s79 offen offset:640  lds\n"
      "v_lshl_add_u32 v49, v49, 2, s76\n"
      "v_lshl_add_u32 v55, v55, 2, s76\n"
      "v_mfma_f32_16x16x32_bf16 v[134:137], a[24:27], a[228:231], 0\n"
      "ds_read_b32 v154, v154\n"
      "v_lshl_add_u32 v50, v50, 2, s76\n"
      "v_mfma_f32_16x16x32_bf16 v[224:227], a[100:103], a[232:235], v[224:227]\n"
      "ds_read2_b32 v[40:41], v66 offset0:0 offset1:1\n"
      "s_add_u32 m0, s75, 0\n"
      "v_lshl_add_u32 v71, v71, 2, s76\n"
      "v_mfma_f32_16x16x32_bf16 v[252:255], a[100:103], a[244:247], v[252:255]\n"
      "buffer_load_dwordx4 v99, s[20:23], s78 offen  lds\n"
      "v_lshl_add_u32 v104, v104, 2, s76\n"
      "v_mfma_f32_16x16x32_bf16 v[200:203], a[76:79], a[240:243], v[200:203]\n"
      "s_add_u32 m0, s75, 3968\n"
      "v_lshl_add_u32 v61, v61, 2, s76\n"
      "v_lshl_add_u32 v58, v58, 2, s76\n"
      "v_mfma_f32_16x16x32_bf16 v[170:173], a[52:55], a[240:243], v[170:173]\n"
      "ds_read_b128 a[220:223], v0 offset:10240\n"
      "s_waitcnt lgkmcnt(3)\n"
      "v_lshl_add_u32 v65, v65, 2, s76\n"
      "v_lshl_add_u32 v72, v72, 2, s76\n"
      "v_mfma_f32_16x16x32_bf16 v[248:251], a[124:127], a[232:235], v[248:251]\n"
      "buffer_load_dwordx4 v99, s[20:23], s78 offen offset:128  lds\n"
      "v_mfma_f32_16x16x32_bf16 v[248:251], a[128:131], a[248:251], v[248:251]\n"
      "ds_read_b128 a[228:231], v1 offset:6144\n"
      "v_lshl_add_u32 v69, v69, 2, s76\n"
      "v_mfma_f32_16x16x32_bf16 v[100:103], a[8:11], a[216:219], v[100:103]\n"
      "ds_read2_b32 v[66:67], v155 offset0:2 offset1:3\n"
      "v_lshl_add_u32 v43, v43, 2, s76\n"
      "v_mfma_f32_16x16x32_bf16 v[138:141], a[124:127], a[240:243], v[138:141]\n"
      "s_add_u32 m0, s75, 7936\n"
      "ds_read_b32 v73, v73\n"
      "v_mfma_f32_16x16x32_bf16 v[162:165], a[4:7], a[244:247], v[162:165]\n"
      "buffer_load_dwordx4 v99, s[20:23], s78 offen offset:256  lds\n"
      "v_mfma_f32_16x16x32_bf16 v[190:193], a[104:107], a[252:255], v[190:193]\n"
      "ds_read_b32 v72, v72\n"
      "s_waitcnt lgkmcnt(4)\n"
      "v_mfma_f32_16x16x32_bf16 v[124:127], a[128:131], a[220:223], v[124:127]\n"
      "ds_read_b128 a[232:235], v1 offset:8192\n"
      "v_lshl_add_u32 v41, v41, 2, s76\n"
      "v_mfma_f32_16x16x32_bf16 v[220:223], a[56:59], a[252:255], v[220:223]\n"
      "s_add_u32 m0, s75, 11904\n"
      "ds_read_b32 v50, v50\n"
      "v_lshl_add_u32 v51, v51, 2, s76\n"
      "v_mfma_f32_16x16x32_bf16 v[120:123], a[76:79], a[244:247], v[120:123]\n"
      "buffer_load_dwordx4 v99, s[20:23], s78 offen offset:384  lds\n"
      "v_mfma_f32_16x16x32_bf16 v[240:243], a[104:107], a[216:219], v[240:243]\n"
      "s_add_u32 m0, s75, 15872\n"
      "ds_read_b32 v71, v71\n"
      "v_mfma_f32_16x16x32_bf16 v[224:227], a[104:107], a[248:251], v[224:227]\n"
      "ds_read_b32 v61, v61\n"
      "v_lshl_add_u32 v47, v47, 2, s76\n"
      "v_mfma_f32_16x16x32_bf16 v[200:203], a[80:83], a[216:219], v[200:203]\n"
      "ds_read_b128 a[240:243], v116 offset:8192\n"
      "v_readlane_b32 s86, v38, s43\n"
      "v_mfma_f32_16x16x32_bf16 v[170:173], a[56:59], a[216:219], v[170:173]\n"
      "buffer_load_dwordx4 v99, s[20:23], s78 offen offset:512  lds\n"
      "v_lshl_add_u32 v42, v42, 2, s76\n"
      "s_mov_b32 s79, 184320\n"
      "v_mfma_f32_16x16x32_bf16 v[166:169], a[32:35], a[248:251], v[166:169]\n"
      "s_add_u32 m0, s75, 19840\n"
      "v_lshl_add_u32 v40, v40, 2, s76\n"
      "v_lshl_add_u32 v75, v75, 2, s76\n"
      "v_mov_b32 v205, 0xf149f2ca\n"
      "v_mfma_f32_16x16x32_bf16 v[120:123], a[80:83], a[220:223], v[120:123]\n"
      "ds_read_b32 v69, v69\n"
      "v_lshl_add_u32 v48, v48, 2, s76\n"
      "v_lshl_add_u32 v44, v44, 2, s76\n"
      "v_mfma_f32_16x16x32_bf16 v[180:183], a[8:11], a[252:255], v[180:183]\n"
      "buffer_load_dwordx4 v99, s[20:23], s78 offen offset:640  lds\n"
      "v_mfma_f32_16x16x32_bf16 v[194:197], a[32:35], a[252:255], v[194:197]\n"
      "s_add_u32 m0, s75, 24576\n"
      "v_lshl_add_u32 v64, v64, 2, s76\n"
      "v_lshl_add_u32 v157, v157, 2, s76\n"
      "v_mfma_f32_16x16x32_bf16 v[232:235], a[56:59], a[248:251], v[232:235]\n"
      "s_waitcnt vmcnt(6)\n"
      "s_barrier\n"
      "ds_read_b64_tr_b16 a[108:109], v76 offset:36864\n"
      "ds_read_b64_tr_b16 a[132:133], v108 offset:36864\n"
      "v_mfma_f32_16x16x32_bf16 v[244:247], a[8:11], a[248:251], v[244:247]\n"
      "ds_read_b64_tr_b16 a[110:111], v76 offset:37376\n"
      "ds_read_b64_tr_b16 a[38:39], v76 offset:25088\n"
      "v_mfma_f32_16x16x32_bf16 v[158:161], a[52:55], a[244:247], v[158:161]\n"
      "ds_read_b64_tr_b16 a[134:135], v108 offset:37376\n"
      "ds_read_b64_tr_b16 a[84:85], v77 offset:36864\n"
      "ds_read_b64_tr_b16 a[12:13], v77 offset:24576\n"
      "ds_read_b64_tr_b16 a[36:37], v76 offset:24576\n"
      "ds_read_b64_tr_b16 a[60:61], v108 offset:24576\n"
      "v_mfma_f32_16x16x32_bf16 v[158:161], a[56:59], a[220:223], v[158:161]\n"
      "ds_read_b64_tr_b16 a[14:15], v77 offset:25088\n"
      "ds_read_b64_tr_b16 a[62:63], v108 offset:25088\n"
      "ds_read_b64_tr_b16 a[86:87], v77 offset:37376\n"
      "v_mfma_f32_16x16x32_bf16 v[216:219], a[80:83], a[248:251], v[216:219]\n"
      "buffer_load_dwordx4 v99, s[20:23], s79 offen  lds\n"
      "v_mfma_f32_16x16x32_bf16 v[134:137], a[28:31], a[244:247], v[134:137]\n"
      "ds_read_b32 v43, v43\n"
      "s_add_u32 m0, s75, 28544\n"
      "v_lshl_add_u32 v45, v45, 2, s76\n"
      "v_mfma_f32_16x16x32_bf16 v[134:137], a[32:35], a[220:223], v[134:137]\n"
      "ds_read_b128 a[244:247], v0 offset:12288\n"
      "v_lshl_add_u32 v56, v56, 2, s76\n"
      "v_mfma_f32_16x16x32_bf16 v[130:133], a[32:35], a[216:219], v[130:133]\n"
      "ds_read_b32 v65, v65\n"
      "buffer_load_dwordx4 v99, s[20:23], s79 offen offset:128  lds\n"
      "s_mov_b32 s78, 0\n"
      "v_mfma_f32_16x16x32_bf16 v[138:141], a[128:131], a[216:219], v[138:141]\n"
      "s_add_u32 m0, s75, 32512\n"
      "v_lshl_add_u32 v52, v52, 2, s76\n"
      "s_waitcnt lgkmcnt(3)\n"
      "v_mfma_f32_16x16x32_bf16 v[170:173], a[60:63], a[232:235], v[170:173]\n"
      "ds_read_b32 v104, v104\n"
      "ds_read_b32 v40, v40\n"
      "v_lshl_add_u32 v60, v60, 2, s76\n"
      "v_mfma_f32_16x16x32_bf16 v[212:215], a[128:131], a[252:255], v[212:215]\n"
      "buffer_load_dwordx4 v99, s[20:23], s79 offen offset:256  lds\n"
      "v_mfma_f32_16x16x32_bf16 v[208:211], a[80:83], a[252:255], v[208:211]\n"
      "ds_read_b32 v44, v44\n"
      "v_lshl_add_u32 v67, v67, 2, s76\n"
      "v_mfma_f32_16x16x32_bf16 v[208:211], a[84:87], a[228:231], v[208:211]\n"
      "ds_read_b128 a[216:219], v143 offset:8192\n"
      "v_lshl_add_u32 v74, v74, 2, s76\n"
      "v_mfma_f32_16x16x32_bf16 v[252:255], a[104:107], a[220:223], v[252:255]\n"
      "ds_read_b32 v48, v48\n"
      "s_add_u32 m0, s75, 36480\n"
      "v_mfma_f32_16x16x32_bf16 v[252:255], a[108:111], a[236:239], v[252:255]\n"
      "buffer_load_dwordx4 v99, s[20:23], s79 offen offset:384  lds\n"
      "v_cndmask_b32 v30, v205, v154, s[90:91]\n"
      "v_mfma_f32_16x16x32_bf16 v[124:127], a[132:135], a[236:239], v[124:127]\n"
      "ds_read_b32 v60, v60\n"
      "v_lshl_add_u32 v66, v66, 2, s76\n"
      "v_mfma_f32_16x16x32_bf16 v[120:123], a[84:87], a[236:239], v[120:123]\n"
      "s_add_u32 m0, s75, 40448\n"
      "ds_read_b32 v75, v75\n"
      "v_mfma_f32_16x16x32_bf16 v[244:247], a[12:15], a[224:227], v[244:247]\n"
      "ds_read_b32 v157, v157\n"
      "v_lshl_add_u32 v57, v57, 2, s76\n"
      "v_mfma_f32_16x16x32_bf16 v[162:165], a[8:11], a[220:223], v[162:165]\n"
      "buffer_load_dwordx4 v99, s[20:23], s79 offen offset:512  lds\n"
      "v_mfma_f32_16x16x32_bf16 v[158:161], a[60:63], a[236:239], v[158:161]\n"
      "ds_read_b128 a[248:251], v0 offset:14336\n"
      "s_add_u32 m0, s75, 44416\n"
      "ds_read_b32 v45, v45\n"
      "v_mfma_f32_16x16x32_bf16 v[240:243], a[108:111], a[232:235], v[240:243]\n"
      "ds_read_b32 v64, v64\n"
      "ds_read_b32 v70, v70\n"
      "v_mfma_f32_16x16x32_bf16 v[166:169], a[36:39], a[224:227], v[166:169]\n"
      "buffer_load_dwordx4 v99, s[20:23], s79 offen offset:640  lds\n"
      "v_mfma_f32_16x16x32_bf16 v[248:251], a[132:135], a[224:227], v[248:251]\n"
      "ds_read_b128 a[252:255], v0 offset:16384\n"
      "s_add_u32 m0, s75, 48384\n"
      "v_mfma_f32_16x16x32_bf16 v[212:215], a[132:135], a[228:231], v[212:215]\n"
      "ds_read_b32 v105, v105\n"
      "v_lshl_add_u32 v53, v53, 2, s76\n"
      "v_mfma_f32_16x16x32_bf16 v[134:137], a[36:39], a[236:239], v[134:137]\n"
      "ds_read_b32 v63, v63\n"
      "ds_read_b32 v53, v53\n"
      "v_mfma_f32_16x16x32_bf16 v[162:165], a[12:15], a[236:239], v[162:165]\n"
      "buffer_load_dwordx4 v99, s[20:23], s78 offen offset:768  lds\n"
      "v_mfma_f32_16x16x32_bf16 v[194:197], a[36:39], a[228:231], v[194:197]\n"
      "s_add_u32 m0, s75, 52352\n"
      "s_waitcnt vmcnt(7)\n"
      "s_barrier\n"
      "ds_read_b64_tr_b16 a[136:137], v238 offset:12288\n"
      "v_mfma_f32_16x16x32_bf16 v[224:227], a[108:111], a[224:227], v[224:227]\n"
      "ds_read_b64_tr_b16 a[88:89], v236 offset:12288\n"
      "ds_read_b64_tr_b16 a[66:67], v238 offset:512\n"
      "ds_read_b64_tr_b16 a[90:91], v236 offset:12800\n"
      "v_mfma_f32_16x16x32_bf16 v[180:183], a[12:15], a[228:231], v[180:183]\n"
      "ds_read_b64_tr_b16 a[16:17], v236\n"
      "ds_read_b64_tr_b16 a[18:19], v236 offset:512\n"
      "v_mfma_f32_16x16x32_bf16 v[232:235], a[60:63], a[224:227], v[232:235]\n"
      "buffer_load_dwordx4 v99, s[20:23], s78 offen offset:896  lds\n"
      "v_mfma_f32_16x16x32_bf16 v[100:103], a[12:15], a[232:235], v[100:103]\n"
      "ds_read_b64_tr_b16 a[138:139], v238 offset:12800\n"
      "ds_read_b64_tr_b16 a[42:43], v237 offset:512\n"
      "ds_read_b64_tr_b16 a[64:65], v238\n"
      "v_mfma_f32_16x16x32_bf16 v[138:141], a[132:135], a[232:235], v[138:141]\n"
      "ds_read_b64_tr_b16 a[112:113], v237 offset:12288\n"
      "ds_read_b64_tr_b16 a[114:115], v237 offset:12800\n"
      "v_mfma_f32_16x16x32_bf16 v[130:133], a[36:39], a[232:235], v[130:133]\n"
      "ds_read_b64_tr_b16 a[40:41], v237\n"
      "s_add_u32 m0, s75, 56320\n"
      "v_mfma_f32_16x16x32_bf16 v[200:203], a[84:87], a[232:235], v[200:203]\n"
      "buffer_load_dwordx4 v99, s[20:23], s78 offen offset:1024  lds\n"
      "v_mfma_f32_16x16x32_bf16 v[216:219], a[84:87], a[224:227], v[216:219]\n"
      "ds_read_b32 v67, v67\n"
      "s_waitcnt lgkmcnt(7)\n"
      "v_mfma_f32_16x16x32_bf16 v[216:219], a[88:91], a[240:243], v[216:219]\n"
      "ds_read_b32 v47, v47\n"
      "v_readlane_b32 s84, v37, s43\n"
      "v_mfma_f32_16x16x32_bf16 v[220:223], a[60:63], a[228:231], v[220:223]\n"
      "s_mov_b32 s78, 36864\n"
      "v_lshl_add_u32 v68, v68, 2, s76\n"
      "s_add_u32 m0, s75, 60672\n"
      "v_mfma_f32_16x16x32_bf16 v[100:103], a[16:19], a[248:251], v[100:103]\n"
      "buffer_load_dwordx4 v99, s[20:23], s78 offen offset:768  lds\n"
      "s_waitcnt lgkmcnt(2)\n"
      "v_mfma_f32_16x16x32_bf16 v[252:255], a[112:115], a[252:255], v[252:255]\n"
      "s_add_u32 m0, s75, 64640\n"
      "ds_read_b128 a[232:235], v116 offset:2048\n"
      "s_mov_b32 s79, 73728\n"
      "v_mfma_f32_16x16x32_bf16 v[212:215], a[136:139], a[244:247], v[212:215]\n"
      "ds_read_b32 v46, v46\n"
      "ds_read_b32 v54, v54\n"
      "v_mfma_f32_16x16x32_bf16 v[232:235], a[64:67], a[240:243], v[232:235]\n"
      "buffer_load_dwordx4 v99, s[20:23], s78 offen offset:896  lds\n"
      "v_mfma_f32_16x16x32_bf16 v[208:211], a[88:91], a[244:247], v[208:211]\n"
      "s_add_u32 m0, s75, 68608\n"
      "ds_read_b32 v68, v68\n"
      "v_mfma_f32_16x16x32_bf16 v[190:193], a[108:111], a[228:231], v[190:193]\n"
      "ds_read_b32 v58, v58\n"
      "ds_read_b32 v41, v41\n"
      "ds_read_b128 a[236:239], v143 offset:2048\n"
      "v_mfma_f32_16x16x32_bf16 v[124:127], a[136:139], a[252:255], v[124:127]\n"
      "buffer_load_dwordx4 v99, s[20:23], s78 offen offset:1024  lds\n"
      "v_mfma_f32_16x16x32_bf16 v[244:247], a[16:19], a[240:243], v[244:247]\n"
      "ds_read_b32 v49, v49\n"
      "v_lshl_add_u32 v62, v62, 2, s76\n"
      "v_mfma_f32_16x16x32_bf16 v[248:251], a[136:139], a[240:243], v[248:251]\n"
      "ds_read_b32 v57, v57\n"
      "v_cndmask_b32 v28, v205, v157, s[82:83]\n"
      "v_mfma_f32_16x16x32_bf16 v[180:183], a[16:19], a[244:247], v[180:183]\n"
      "ds_read_b32 v52, v52\n"
      "s_add_u32 m0, s75, 72960\n"
      "v_mfma_f32_16x16x32_bf16 v[120:123], a[88:91], a[252:255], v[120:123]\n"
      "buffer_load_dwordx4 v99, s[20:23], s79 offen offset:768  lds\n"
      "s_waitcnt lgkmcnt(4)\n"
      "v_mfma_f32_16x16x32_bf16 v[154:157], a[72:75], a[232:235], 0\n"
      "ds_read_b32 v55, v55\n"
      "v_readlane_b32 s85, v37, s42\n"
      "v_mfma_f32_16x16x32_bf16 v[220:223], a[64:67], a[244:247], v[220:223]\n"
      "ds_read_b32 v42, v42\n"
      "ds_read_b32 v74, v74\n"
      "v_mfma_f32_16x16x32_bf16 v[194:197], a[40:43], a[244:247], v[194:197]\n"
      "s_add_u32 m0, s75, 76928\n"
      "ds_read_b32 v56, v56\n"
      "v_mfma_f32_16x16x32_bf16 v[130:133], a[40:43], a[248:251], v[130:133]\n"
      "buffer_load_dwordx4 v99, s[20:23], s79 offen offset:896  lds\n"
      "v_mfma_f32_16x16x32_bf16 v[158:161], a[64:67], a[252:255], v[158:161]\n"
      "ds_read_b128 a[224:227], v1 offset:14336\n"
      "s_add_u32 m0, s75, 80896\n"
      "ds_read_b128 a[228:231], v1 offset:16384\n"
      "v_mfma_f32_16x16x32_bf16 v[134:137], a[40:43], a[252:255], v[134:137]\n"
      "ds_read_b32 v66, v66\n"
      "ds_read_b128 a[220:223], v1 offset:12288\n"
      "v_mfma_f32_16x16x32_bf16 v[162:165], a[16:19], a[252:255], v[162:165]\n"
      "buffer_load_dwordx4 v99, s[20:23], s79 offen offset:1024  lds\n"
      "v_mfma_f32_16x16x32_bf16 v[200:203], a[88:91], a[248:251], v[200:203]\n"
      "s_waitcnt vmcnt(9)\n"
      "s_barrier\n"
      "ds_read_b64_tr_b16 a[94:95], v236 offset:37376\n"
      "v_mfma_f32_16x16x32_bf16 v[190:193], a[112:115], a[244:247], v[190:193]\n"
      "ds_read_b64_tr_b16 a[20:21], v236 offset:24576\n"
      "ds_read_b64_tr_b16 a[92:93], v236 offset:36864\n"
      "v_mfma_f32_16x16x32_bf16 v[174:177], a[0:3], a[232:235], 0\n"
      "ds_read_b64_tr_b16 a[22:23], v236 offset:25088\n"
      "s_mov_b32 s79, 110592\n"
      "v_mfma_f32_16x16x32_bf16 v[224:227], a[112:115], a[240:243], v[224:227]\n"
      "ds_read_b64_tr_b16 a[116:117], v237 offset:36864\n"
      "ds_read_b64_tr_b16 a[68:69], v238 offset:24576\n"
      "v_mfma_f32_16x16x32_bf16 v[170:173], a[64:67], a[248:251], v[170:173]\n"
      "s_add_u32 m0, s75, 85248\n"
      "ds_read_b64_tr_b16 a[44:45], v237 offset:24576\n"
      "v_mfma_f32_16x16x32_bf16 v[166:169], a[40:43], a[240:243], v[166:169]\n"
      "ds_read_b64_tr_b16 a[70:71], v238 offset:25088\n"
      "ds_read_b64_tr_b16 a[140:141], v238 offset:36864\n"
      "v_mfma_f32_16x16x32_bf16 v[112:115], a[48:51], a[232:235], 0\n"
      "buffer_load_dwordx4 v99, s[20:23], s79 offen offset:768  lds\n"
      "v_mfma_f32_16x16x32_bf16 v[138:141], a[136:139], a[248:251], v[138:141]\n"
      "ds_read_b64_tr_b16 a[46:47], v237 offset:25088\n"
      "s_waitcnt lgkmcnt(6)\n"
      "v_mfma_f32_16x16x32_bf16 v[180:183], a[20:23], a[220:223], v[180:183]\n"
      "s_add_u32 m0, s75, 89216\n"
      "ds_read_b64_tr_b16 a[118:119], v237 offset:37376\n"
      "v_mfma_f32_16x16x32_bf16 v[244:247], a[20:23], a[216:219], v[244:247]\n"
      "ds_read_b64_tr_b16 a[142:143], v238 offset:37376\n"
      "v_lshl_add_u32 v59, v59, 2, s76\n"
      "v_mfma_f32_16x16x32_bf16 v[216:219], a[92:95], a[216:219], v[216:219]\n"
      "buffer_load_dwordx4 v99, s[20:23], s79 offen offset:896  lds\n"
      "v_mfma_f32_16x16x32_bf16 v[120:123], a[92:95], a[228:231], v[120:123]\n"
      "s_add_u32 m0, s75, 93184\n"
      "ds_read_b128 a[240:243], v116 offset:6144\n"
      "v_mfma_f32_16x16x32_bf16 v[200:203], a[92:95], a[224:227], v[200:203]\n"
      "ds_read_b128 a[244:247], v143 offset:6144\n"
      "v_mul_f32_e64 v107, v245, v245\n"
      "v_mfma_f32_16x16x32_bf16 v[154:157], a[76:79], a[236:239], v[154:157]\n"
      "s_mov_b32 s78, 147456\n"
      "v_mul_f32_e64 v229, v217, v217\n"
      "v_fma_f32 v106, v244, v244, s51\n"
      "v_mfma_f32_16x16x32_bf16 v[208:211], a[92:95], a[220:223], v[208:211]\n"
      "buffer_load_dwordx4 v99, s[20:23], s79 offen offset:1024  lds\n"
      "v_mfma_f32_16x16x32_bf16 v[162:165], a[20:23], a[228:231], v[162:165]\n"
      "s_waitcnt lgkmcnt(2)\n"
      "v_fma_f32 v228, v216, v216, s51\n"
      "s_add_u32 m0, s75, -768\n"
      "v_mfma_f32_16x16x32_bf16 v[166:169], a[44:47], a[216:219], v[166:169]\n"
      "ds_read_b32 v51, v51\n"
      "v_cndmask_b32 v26, v205, v105, s[82:83]\n"
      "v_mfma_f32_16x16x32_bf16 v[138:141], a[140:143], a[224:227], v[138:141]\n"
      "s_waitcnt lgkmcnt(2)\n"
      "v_cndmask_b32 v24, v205, v104, s[82:83]\n"
      "v_mul_f32_e64 v105, v201, v201\n"
      "v_mfma_f32_16x16x32_bf16 v[100:103], a[20:23], a[224:227], v[100:103]\n"
      "buffer_load_dwordx4 v99, s[20:23], s78 offen offset:768  lds\n"
      "v_mfma_f32_16x16x32_bf16 v[174:177], a[4:7], a[236:239], v[174:177]\n"
      "ds_read_b32 v59, v59\n"
      "v_fma_f32 v104, v200, v200, s51\n"
      "v_mfma_f32_16x16x32_bf16 v[174:177], a[8:11], a[240:243], v[174:177]\n"
      "s_lshl_b32 s92, s40, 6\n"
      "v_fma_f32 v204, v208, v208, s51\n"
      "v_mul_f32 v207, v181, v181\n"
      "v_mfma_f32_16x16x32_bf16 v[130:133], a[44:47], a[224:227], v[130:133]\n"
      "s_add_u32 m0, s75, 3200\n"
      "v_mul_f32 v145, v163, v163\n"
      "v_mul_f32 v205, v209, v209\n"
      "v_mfma_f32_16x16x32_bf16 v[190:193], a[116:119], a[220:223], v[190:193]\n"
      "buffer_load_dwordx4 v99, s[20:23], s78 offen offset:896  lds\n"
      "v_fma_f32 v152, v120, v120, s51\n"
      "v_mul_f32_e64 v153, v121, v121\n"
      "v_pk_fma_f32 v[104:105], v[202:203], v[202:203], v[104:105]\n"
      "v_pk_fma_f32 v[228:229], v[218:219], v[218:219], v[228:229]\n"
      "v_pk_fma_f32 v[106:107], v[246:247], v[246:247], v[106:107]\n"
      "v_pk_fma_f32 v[204:205], v[210:211], v[210:211], v[204:205]\n"
      "v_pk_fma_f32 v[106:107], v[166:167], v[166:167], v[106:107]\n"
      "v_mfma_f32_16x16x32_bf16 v[232:235], a[68:71], a[216:219], v[232:235]\n"
      "s_add_u32 m0, s75, 7168\n"
      "ds_read_b128 a[252:255], v143 offset:10240\n"
      "v_mfma_f32_16x16x32_bf16 v[224:227], a[116:119], a[216:219], v[224:227]\n"
      "buffer_load_dwordx4 v99, s[20:23], s78 offen offset:1024  lds\n"
      "v_mfma_f32_16x16x32_bf16 v[194:197], a[44:47], a[220:223], v[194:197]\n"
      "s_add_u32 s43, s43, 1\n"
      "v_fma_f32 v206, v180, v180, s51\n"
      "s_mov_b32 s78, 184320\n"
      "v_mfma_f32_16x16x32_bf16 v[240:243], a[112:115], a[248:251], v[240:243]\n"
      "s_add_u32 m0, s75, 11520\n"
      "v_fma_f32 v144, v162, v162, s51\n"
      "v_mul_f32 v151, v101, v101\n"
      "v_fma_f32 v150, v100, v100, s51\n"
      "v_mfma_f32_16x16x32_bf16 v[240:243], a[116:119], a[224:227], v[240:243]\n"
      "ds_read_b128 a[248:251], v116 offset:10240\n"
      "buffer_load_dwordx4 v99, s[20:23], s78 offen offset:768  lds\n"
      "v_pk_fma_f32 v[106:107], v[168:169], v[168:169], v[106:107]\n"
      "v_pk_fma_f32 v[228:229], v[224:225], v[224:225], v[228:229]\n"
      "s_add_u32 m0, s75, 15488\n"
      "v_pk_fma_f32 v[204:205], v[190:191], v[190:191], v[204:205]\n"
      "v_pk_fma_f32 v[228:229], v[226:227], v[226:227], v[228:229]\n"
      "v_pk_fma_f32 v[106:107], v[232:233], v[232:233], v[106:107]\n"
      "v_pk_fma_f32 v[144:145], v[164:165], v[164:165], v[144:145]\n"
      "v_pk_fma_f32 v[152:153], v[122:123], v[122:123], v[152:153]\n"
      "v_pk_fma_f32 v[206:207], v[182:183], v[182:183], v[206:207]\n"
      "v_pk_fma_f32 v[106:107], v[234:235], v[234:235], v[106:107]\n"
      "v_pk_fma_f32 v[150:151], v[102:103], v[102:103], v[150:151]\n"
      "v_pk_fma_f32 v[206:207], v[194:195], v[194:195], v[206:207]\n"
      "v_pk_fma_f32 v[104:105], v[240:241], v[240:241], v[104:105]\n"
      "v_mfma_f32_16x16x32_bf16 v[248:251], a[140:143], a[216:219], v[248:251]\n"
      "buffer_load_dwordx4 v99, s[20:23], s78 offen offset:896  lds\n"
      "v_mfma_f32_16x16x32_bf16 v[158:161], a[68:71], a[228:231], v[158:161]\n"
      "s_add_u32 m0, s75, 19456\n"
      "v_add_f32 v106, v106, v107\n"
      "s_waitcnt vmcnt(11)\n"
      "s_barrier\n"
      "v_mfma_f32_16x16x32_bf16 v[124:127], a[140:143], a[228:231], v[124:127]\n"
      "s_mov_b32 s79, 0\n"
      "ds_read_b64_tr_b16 a[146:147], v77 offset:512\n"
      "v_mfma_f32_16x16x32_bf16 v[112:115], a[52:55], a[236:239], v[112:115]\n"
      "ds_read_b64_tr_b16 a[196:197], v108 offset:12288\n"
      "ds_read_b64_tr_b16 a[172:173], v76 offset:12288\n"
      "v_mfma_f32_16x16x32_bf16 v[212:215], a[140:143], a[220:223], v[212:215]\n"
      "buffer_load_dwordx4 v99, s[20:23], s78 offen offset:1024  lds\n"
      "v_mfma_f32_16x16x32_bf16 v[252:255], a[116:119], a[228:231], v[252:255]\n"
      "ds_read_b64_tr_b16 a[144:145], v77\n"
      "ds_read_b64_tr_b16 a[168:169], v76\n"
      "v_mfma_f32_16x16x32_bf16 v[112:115], a[56:59], a[240:243], v[112:115]\n"
      "ds_read_b64_tr_b16 a[174:175], v76 offset:12800\n"
      "s_add_u32 m0, s75, 24576\n"
      "v_mfma_f32_16x16x32_bf16 v[220:223], a[68:71], a[220:223], v[220:223]\n"
      "ds_read_b64_tr_b16 a[170:171], v76 offset:512\n"
      "ds_read_b64_tr_b16 a[198:199], v108 offset:12800\n"
      "v_mfma_f32_16x16x32_bf16 v[134:137], a[44:47], a[228:231], v[134:137]\n"
      "ds_read_b64_tr_b16 a[150:151], v77 offset:12800\n"
      "ds_read_b64_tr_b16 a[194:195], v108 offset:512\n"
      "ds_read_b64_tr_b16 a[148:149], v77 offset:12288\n"
      "v_pk_fma_f32 v[104:105], v[242:243], v[242:243], v[104:105]\n"
      "v_pk_fma_f32 v[206:207], v[196:197], v[196:197], v[206:207]\n"
      "v_pk_fma_f32 v[104:105], v[138:139], v[138:139], v[104:105]\n"
      "v_pk_fma_f32 v[228:229], v[248:249], v[248:249], v[228:229]\n"
      "v_pk_fma_f32 v[150:151], v[130:131], v[130:131], v[150:151]\n"
      "v_mfma_f32_16x16x32_bf16 v[170:173], a[68:71], a[224:227], v[170:173]\n"
      "ds_read_b64_tr_b16 a[192:193], v108\n"
      "buffer_load_dwordx4 v149, s[36:39], s79 offen  lds\n"
      "v_pk_fma_f32 v[152:153], v[252:253], v[252:253], v[152:153]\n"
      "v_pk_fma_f32 v[150:151], v[132:133], v[132:133], v[150:151]\n"
      "v_pk_fma_f32 v[228:229], v[250:251], v[250:251], v[228:229]\n"
      "v_pk_fma_f32 v[104:105], v[140:141], v[140:141], v[104:105]\n"
      "v_add_f32 v228, v228, v229\n"
      "v_add_f32 v104, v104, v105\n"
      "v_pk_fma_f32 v[206:207], v[220:221], v[220:221], v[206:207]\n"
      "v_pk_fma_f32 v[204:205], v[192:193], v[192:193], v[204:205]\n"
      "v_pk_fma_f32 v[152:153], v[254:255], v[254:255], v[152:153]\n"
      "v_permlane16_swap_b32 v106, v228\n"
      "v_pk_fma_f32 v[150:151], v[170:171], v[170:171], v[150:151]\n"
      "v_add_f32 v106, v106, v228\n"
      "v_pk_fma_f32 v[152:153], v[124:125], v[124:125], v[152:153]\n"
      "v_mov_b32 v228, v106\n"
      "v_pk_fma_f32 v[150:151], v[172:173], v[172:173], v[150:151]\n"
      "v_pk_fma_f32 v[204:205], v[212:213], v[212:213], v[204:205]\n"
      "v_pk_fma_f32 v[206:207], v[222:223], v[222:223], v[206:207]\n"
      "v_add_f32 v150, v150, v151\n"
      "v_pk_fma_f32 v[144:145], v[134:135], v[134:135], v[144:145]\n"
      "v_pk_fma_f32 v[204:205], v[214:215], v[214:215], v[204:205]\n"
      "v_permlane16_swap_b32 v150, v104\n"
      "v_add_f32 v150, v150, v104\n"
      "v_mov_b32 v104, v150\n"
      "v_add_f32 v206, v206, v207\n"
      "v_mfma_f32_16x16x32_bf16 v[96:99], a[24:27], a[232:235], 0\n"
      "s_waitcnt lgkmcnt(0)\n"
      "v_add_f32 v204, v204, v205\n"
      "v_permlane32_swap_b32_e64 v106, v228\n"
      "v_mfma_f32_16x16x32_bf16 v[174:177], a[12:15], a[244:247], v[174:177]\n"
      "s_add_u32 m0, s75, 28544\n"
      "v_permlane16_swap_b32 v206, v204\n"
      "v_permlane32_swap_b32 v150, v104\n"
      "v_add_f32 v106, v106, v228\n"
      "v_pk_fma_f32 v[152:153], v[126:127], v[126:127], v[152:153]\n"
      "v_add_f32 v150, v150, v104\n"
      "v_add_f32 v152, v152, v153\n"
      "v_add_f32 v206, v206, v204\n"
      "v_mfma_f32_16x16x32_bf16 v[174:177], a[16:19], a[248:251], v[174:177]\n"
      "buffer_load_dwordx4 v149, s[36:39], s79 offen offset:128  lds\n"
      "v_rsq_f32_e64 v150, v150\n"
      "v_pk_fma_f32 v[144:145], v[136:137], v[136:137], v[144:145]\n"
      "v_mfma_f32_16x16x32_bf16 v[174:177], a[20:23], a[252:255], v[174:177]\n"
      "ds_read_b32 v62, v62\n"
      "v_mov_b32 v204, v206\n"
      "v_rsq_f32 v106, v106\n"
      "v_mov_b32 v104, v150\n"
      "v_mfma_f32_16x16x32_bf16 v[154:157], a[80:83], a[240:243], v[154:157]\n"
      "s_add_u32 m0, s75, 32512\n"
      "v_permlane32_swap_b32 v206, v204\n"
      "v_mfma_f32_16x16x32_bf16 v[154:157], a[84:87], a[244:247], v[154:157]\n"
      "buffer_load_dwordx4 v149, s[36:39], s79 offen offset:256  lds\n"
      "v_add_f32 v206, v206, v204\n"
      "v_permlane16_swap_b32 v150, v104\n"
      "v_mfma_f32_16x16x32_bf16 v[154:157], a[88:91], a[248:251], v[154:157]\n"
      "s_add_u32 m0, s75, 36864\n"
      "v_mov_b32 v228, v106\n"
      "v_mul_f32 v150, s50, v150\n"
      "v_mfma_f32_16x16x32_bf16 v[154:157], a[92:95], a[252:255], v[154:157]\n"
      "s_mov_b32 s79, 12288\n"
      "v_rsq_f32 v206, v206\n"
      "v_permlane16_swap_b32 v106, v228\n"
      "v_mov_b32_e64 v204, v206\n"
      "v_pk_mul_f32 v[100:101], v[100:101], v[150:151] op_sel_hi:[1,0]\n"
      "v_pk_fma_f32 v[144:145], v[158:159], v[158:159], v[144:145]\n"
      "v_pk_mul_f32 v[138:139], v[138:139], v[104:105] op_sel_hi:[1,0]\n"
      "v_pk_mul_f32 v[132:133], v[132:133], v[150:151] op_sel_hi:[1,0]\n"
      "v_pk_mul_f32 v[218:219], v[218:219], v[228:229] op_sel_hi:[1,0]\n"
      "v_mul_f32_e64 v106, s50, v106\n"
      "v_pk_mul_f32 v[216:217], v[216:217], v[228:229] op_sel_hi:[1,0]\n"
      "v_pk_fma_f32 v[144:145], v[160:161], v[160:161], v[144:145]\n"
      "v_pk_mul_f32 v[172:173], v[172:173], v[150:151] op_sel_hi:[1,0]\n"
      "v_mfma_f32_16x16x32_bf16 v[112:115], a[60:63], a[244:247], v[112:115]\n"
      "buffer_load_dwordx4 v149, s[36:39], s79 offen  lds\n"
      "v_add_f32 v144, v144, v145\n"
      "v_permlane16_swap_b32 v206, v204\n"
      "v_pk_mul_f32 v[226:227], v[226:227], v[228:229] op_sel_hi:[1,0]\n"
      "v_pk_mul_f32 v[232:233], v[232:233], v[106:107] op_sel_hi:[1,0]\n"
      "v_pk_mul_f32 v[248:249], v[248:249], v[228:229] op_sel_hi:[1,0]\n"
      "v_pk_mul_f32 v[246:247], v[246:247], v[106:107] op_sel_hi:[1,0]\n"
      "v_pk_mul_f32 v[190:191], v[190:191], v[204:205] op_sel_hi:[1,0]\n"
      "v_mfma_f32_16x16x32_bf16 v[96:99], a[28:31], a[236:239], v[96:99]\n"
      "s_add_u32 m0, s75, 40832\n"
      "v_permlane16_swap_b32 v144, v152\n"
      "v_add_f32 v144, v144, v152\n"
      "v_cvt_pk_bf16_f32 v216, v216, v217\n"
      "v_mov_b32 v152, v144\n"
      "v_mfma_f32_16x16x32_bf16 v[96:99], a[32:35], a[240:243], v[96:99]\n"
      "s_waitcnt vmcnt(10)\n"
      "s_barrier\n"
      "ds_read_b64_tr_b16 a[204:205], v108 offset:36864\n"
      "v_mfma_f32_16x16x32_bf16 v[96:99], a[36:39], a[244:247], v[96:99]\n"
      "buffer_load_dwordx4 v149, s[36:39], s79 offen offset:128  lds\n"
      "v_permlane32_swap_b32 v144, v152\n"
      "v_cvt_pk_bf16_f32 v217, v218, v219\n"
      "v_pk_mul_f32 v[250:251], v[250:251], v[228:229] op_sel_hi:[1,0]\n"
      "v_pk_mul_f32 v[200:201], v[200:201], v[104:105] op_sel_hi:[1,0]\n"
      "v_pk_mul_f32 v[168:169], v[168:169], v[106:107] op_sel_hi:[1,0]\n"
      "v_pk_mul_f32 v[224:225], v[224:225], v[228:229] op_sel_hi:[1,0]\n"
      "v_pk_mul_f32 v[140:141], v[140:141], v[104:105] op_sel_hi:[1,0]\n"
      "v_add_f32_e64 v144, v144, v152\n"
      "v_cvt_pk_bf16_f32 v219, v226, v227\n"
      "v_cvt_pk_bf16_f32 v218, v224, v225\n"
      "v_pk_mul_f32 v[244:245], v[244:245], v[106:107] op_sel_hi:[1,0]\n"
      "v_pk_mul_f32 v[242:243], v[242:243], v[104:105] op_sel_hi:[1,0]\n"
      "v_mfma_f32_16x16x32_bf16 v[226:229], a[120:123], a[232:235], 0\n"
      "ds_read_b64_tr_b16 a[176:177], v76 offset:24576\n"
      "ds_read_b64_tr_b16 a[182:183], v76 offset:37376\n"
      "ds_read_b64_tr_b16 a[154:155], v77 offset:25088\n"
      "v_pk_mul_f32 v[192:193], v[192:193], v[204:205] op_sel_hi:[1,0]\n"
      "v_mul_f32_e64 v109, v155, v155\n"
      "v_cvt_pk_bf16_f32 v248, v248, v249\n"
      "v_pk_mul_f32 v[130:131], v[130:131], v[150:151] op_sel_hi:[1,0]\n"
      "v_mfma_f32_16x16x32_bf16 v[96:99], a[40:43], a[248:251], v[96:99]\n"
      "ds_read_b64_tr_b16 a[152:153], v77 offset:24576\n"
      "ds_read_b64_tr_b16 a[202:203], v108 offset:25088\n"
      "v_cvt_pk_bf16_f32 v249, v250, v251\n"
      "v_mfma_f32_16x16x32_bf16 v[184:187], a[96:99], a[232:235], 0\n"
      "ds_read_b64_tr_b16 a[200:201], v108 offset:24576\n"
      "v_rsq_f32_e64 v144, v144\n"
      "v_mfma_f32_16x16x32_bf16 v[184:187], a[100:103], a[236:239], v[184:187]\n"
      "ds_read_b64_tr_b16 a[206:207], v108 offset:37376\n"
      "ds_read_b64_tr_b16 a[178:179], v76 offset:25088\n"
      "v_mfma_f32_16x16x32_bf16 v[184:187], a[104:107], a[240:243], v[184:187]\n"
      "ds_read_b64_tr_b16 a[158:159], v77 offset:37376\n"
      "v_mul_f32_e64 v206, s50, v206\n"
      "v_mfma_f32_16x16x32_bf16 v[226:229], a[124:127], a[236:239], v[226:229]\n"
      "ds_read_b64_tr_b16 a[156:157], v77 offset:36864\n"
      "s_add_u32 m0, s75, 44800\n"
      "v_mfma_f32_16x16x32_bf16 v[226:229], a[128:131], a[240:243], v[226:229]\n"
      "ds_read_b64_tr_b16 a[180:181], v76 offset:36864\n"
      "v_mul_f32 v77, v175, v175\n"
      "v_mov_b32 v152, v144\n"
      "v_cvt_pk_bf16_f32 v244, v244, v245\n"
      "v_mfma_f32_16x16x32_bf16 v[184:187], a[108:111], a[244:247], v[184:187]\n"
      "s_mov_b32 s78, 24576\n"
      "ds_write_b128 v147, v[216:219] offset:32\n"
      "v_mfma_f32_16x16x32_bf16 v[184:187], a[112:115], a[248:251], v[184:187]\n"
      "ds_read_b128 v[216:219], v143\n"
      "v_cvt_pk_bf16_f32 v245, v246, v247\n"
      "v_mfma_f32_16x16x32_bf16 v[184:187], a[116:119], a[252:255], v[184:187]\n"
      "buffer_load_dwordx4 v149, s[36:39], s79 offen offset:256  lds\n"
      "v_fma_f32 v76, v174, v174, s51\n"
      "v_mfma_f32_16x16x32_bf16 v[96:99], a[44:47], a[252:255], v[96:99]\n"
      "ds_write_b64 v147, v[248:249] offset:24\n"
      "v_permlane16_swap_b32_e64 v144, v152\n"
      "v_cvt_pk_bf16_f32 v232, v232, v233\n"
      "v_pk_mul_f32 v[234:235], v[234:235], v[106:107] op_sel_hi:[1,0]\n"
      "v_pk_mul_f32 v[212:213], v[212:213], v[204:205] op_sel_hi:[1,0]\n"
      "v_mul_f32_e64 v144, s50, v144\n"
      "v_pk_fma_f32 v[76:77], v[176:177], v[176:177], v[76:77]\n"
      "v_pk_mul_f32 v[214:215], v[214:215], v[204:205] op_sel_hi:[1,0]\n"
      "v_pk_mul_f32 v[180:181], v[180:181], v[206:207] op_sel_hi:[1,0]\n"
      "v_pk_mul_f32 v[166:167], v[166:167], v[106:107] op_sel_hi:[1,0]\n"
      "v_cvt_pk_bf16_f32 v247, v168, v169\n"
      "v_pk_mul_f32 v[120:121], v[120:121], v[152:153] op_sel_hi:[1,0]\n"
      "v_mfma_f32_16x16x32_bf16 v[226:229], a[132:135], a[244:247], v[226:229]\n"
      "s_add_u32 m0, s75, 49152\n"
      "ds_read_b128 v[248:251], v143 offset:4096\n"
      "buffer_load_dwordx4 v149, s[36:39], s78 offen  lds\n"
      "v_pk_mul_f32 v[196:197], v[196:197], v[206:207] op_sel_hi:[1,0]\n"
      "v_fma_f32 v108, v154, v154, s51\n"
      "v_pk_mul_f32 v[194:195], v[194:195], v[206:207] op_sel_hi:[1,0]\n"
      "v_pk_fma_f32 v[108:109], v[156:157], v[156:157], v[108:109]\n"
      "v_pk_fma_f32 v[76:77], v[96:97], v[96:97], v[76:77]\n"
      "v_pk_mul_f32 v[208:209], v[208:209], v[204:205] op_sel_hi:[1,0]\n"
      "v_pk_mul_f32 v[254:255], v[254:255], v[152:153] op_sel_hi:[1,0]\n"
      "v_cvt_pk_bf16_f32 v233, v234, v235\n"
      "v_pk_mul_f32 v[170:171], v[170:171], v[150:151] op_sel_hi:[1,0]\n"
      "v_pk_mul_f32 v[240:241], v[240:241], v[104:105] op_sel_hi:[1,0]\n"
      "v_pk_mul_f32 v[202:203], v[202:203], v[104:105] op_sel_hi:[1,0]\n"
      "v_cvt_pk_bf16_f32 v246, v166, v167\n"
      "v_pk_mul_f32 v[222:223], v[222:223], v[206:207] op_sel_hi:[1,0]\n"
      "v_pk_mul_f32 v[124:125], v[124:125], v[152:153] op_sel_hi:[1,0]\n"
      "v_pk_mul_f32 v[182:183], v[182:183], v[206:207] op_sel_hi:[1,0]\n"
      "s_add_u32 m0, s75, 53120\n"
      "v_mfma_f32_16x16x32_bf16 v[226:229], a[136:139], a[248:251], v[226:229]\n"
      "ds_write_b128 v147, v[244:247]\n"
      "v_pk_fma_f32 v[108:109], v[184:185], v[184:185], v[108:109]\n"
      "v_cvt_pk_bf16_f32 v200, v200, v201\n"
      "v_cvt_pk_bf16_f32 v201, v202, v203\n"
      "v_cvt_pk_bf16_f32 v120, v120, v121\n"
      "v_mfma_f32_16x16x32_bf16 v[226:229], a[140:143], a[252:255], v[226:229]\n"
      "ds_read_b128 v[244:247], v116 offset:4096\n"
      "v_cvt_pk_bf16_f32 v170, v170, v171\n"
      "v_mfma_f32_16x16x32_bf16 v[112:115], a[64:67], a[248:251], v[112:115]\n"
      "ds_read_b128 v[166:169], v116\n"
      "buffer_load_dwordx4 v149, s[36:39], s78 offen offset:128  lds\n"
      "v_pk_mul_f32 v[162:163], v[162:163], v[144:145] op_sel_hi:[1,0]\n"
      "v_pk_mul_f32 v[102:103], v[102:103], v[150:151] op_sel_hi:[1,0]\n"
      "v_cvt_pk_bf16_f32 v100, v100, v101\n"
      "v_cvt_pk_bf16_f32 v203, v242, v243\n"
      "s_add_u32 m0, s75, 57088\n"
      "v_pk_mul_f32 v[122:123], v[122:123], v[152:153] op_sel_hi:[1,0]\n"
      "v_pk_fma_f32 v[76:77], v[98:99], v[98:99], v[76:77]\n"
      "v_pk_mul_f32 v[252:253], v[252:253], v[152:153] op_sel_hi:[1,0]\n"
      "v_pk_mul_f32 v[210:211], v[210:211], v[204:205] op_sel_hi:[1,0]\n"
      "v_pk_mul_f32 v[220:221], v[220:221], v[206:207] op_sel_hi:[1,0]\n"
      "v_pk_mul_f32 v[160:161], v[160:161], v[144:145] op_sel_hi:[1,0]\n"
      "v_cvt_pk_bf16_f32 v101, v102, v103\n"
      "v_cvt_pk_bf16_f32 v180, v180, v181\n"
      "v_cvt_pk_bf16_f32 v181, v182, v183\n"
      "v_mfma_f32_16x16x32_bf16 v[112:115], a[68:71], a[252:255], v[112:115]\n"
      "buffer_load_dwordx4 v149, s[36:39], s78 offen offset:256  lds\n"
      "s_mov_b32 s78, 36864\n"
      "v_cvt_pk_bf16_f32 v182, v194, v195\n"
      "s_waitcnt lgkmcnt(0)\n"
      "s_waitcnt vmcnt(9)\n"
      "s_barrier\n"
      "v_pk_mul_f32 v[158:159], v[158:159], v[144:145] op_sel_hi:[1,0]\n"
      "v_cvt_pk_bf16_f32 v183, v196, v197\n"
      "v_pk_fma_f32 v[108:109], v[186:187], v[186:187], v[108:109]\n"
      "s_add_u32 m0, s75, 61440\n"
      "ds_read_b64_tr_b16 a[188:189], v237 offset:12288\n"
      "v_pk_mul_f32 v[136:137], v[136:137], v[144:145] op_sel_hi:[1,0]\n"
      "v_mfma_f32_16x16x32_bf16 v[194:197], a[168:171], v[166:169], 0\n"
      "ds_read_b64_tr_b16 a[212:213], v238 offset:12288\n"
      "ds_read_b64_tr_b16 a[190:191], v237 offset:12800\n"
      "v_mfma_f32_16x16x32_bf16 v[194:197], a[172:175], v[216:219], v[194:197]\n"
      "ds_read_b64_tr_b16 a[186:187], v237 offset:512\n"
      "buffer_load_dwordx4 v149, s[36:39], s78 offen  lds\n"
      "v_mfma_f32_16x16x32_bf16 v[194:197], a[176:179], v[244:247], v[194:197]\n"
      "ds_read_b128 v[104:107], v116 offset:8192\n"
      "v_cvt_pk_bf16_f32 v103, v132, v133\n"
      "v_mfma_f32_16x16x32_bf16 v[194:197], a[180:183], v[248:251], v[194:197]\n"
      "ds_read_b64_tr_b16 a[160:161], v236\n"
      "ds_read_b64_tr_b16 a[214:215], v238 offset:12800\n"
      "v_mfma_f32_16x16x32_bf16 v[204:207], a[192:195], v[166:169], 0\n"
      "ds_read_b64_tr_b16 a[184:185], v237\n"
      "s_add_u32 m0, s75, 65408\n"
      "v_mfma_f32_16x16x32_bf16 v[204:207], a[196:199], v[216:219], v[204:207]\n"
      "ds_read_b64_tr_b16 a[210:211], v238 offset:512\n"
      "v_cvt_pk_bf16_f32 v102, v130, v131\n"
      "v_mfma_f32_16x16x32_bf16 v[204:207], a[200:203], v[244:247], v[204:207]\n"
      "ds_read_b64_tr_b16 a[164:165], v236 offset:12288\n"
      "buffer_load_dwordx4 v149, s[36:39], s78 offen offset:128  lds\n"
      "v_cvt_pk_bf16_f32 v138, v138, v139\n"
      "s_mov_b32 s79, 49152\n"
      "v_pk_fma_f32 v[76:77], v[112:113], v[112:113], v[76:77]\n"
      "v_pk_mul_f32 v[126:127], v[126:127], v[152:153] op_sel_hi:[1,0]\n"
      "v_mfma_f32_16x16x32_bf16 v[204:207], a[204:207], v[248:251], v[204:207]\n"
      "ds_read_b64_tr_b16 a[166:167], v236 offset:12800\n"
      "ds_read_b64_tr_b16 a[208:209], v238\n"
      "v_mfma_f32_16x16x32_bf16 v[150:153], a[144:147], v[166:169], 0\n"
      "ds_read_b128 v[130:133], v143 offset:8192\n"
      "v_cvt_pk_bf16_f32 v212, v212, v213\n"
      "s_add_u32 m0, s75, 69376\n"
      "ds_read_b64_tr_b16 a[162:163], v236 offset:512\n"
      "v_mfma_f32_16x16x32_bf16 v[150:153], a[148:151], v[216:219], v[150:153]\n"
      "buffer_load_dwordx4 v149, s[36:39], s78 offen offset:256  lds\n"
      "ds_read_b128 v[234:237], v143 offset:2048\n"
      "v_pk_mul_f32 v[164:165], v[164:165], v[144:145] op_sel_hi:[1,0]\n"
      "v_pk_fma_f32 v[76:77], v[114:115], v[114:115], v[76:77]\n"
      "v_pk_fma_f32 v[108:109], v[226:227], v[226:227], v[108:109]\n"
      "v_mfma_f32_16x16x32_bf16 v[150:153], a[152:155], v[244:247], v[150:153]\n"
      "ds_read_b128 v[242:245], v116 offset:2048\n"
      "v_cvt_pk_bf16_f32 v121, v122, v123\n"
      "v_cvt_pk_bf16_f32 v208, v208, v209\n"
      "v_mfma_f32_16x16x32_bf16 v[150:153], a[156:159], v[248:251], v[150:153]\n"
      "s_add_u32 m0, s75, 73728\n"
      "ds_read_b128 v[246:249], v116 offset:10240\n"
      "buffer_load_dwordx4 v149, s[36:39], s79 offen  lds\n"
      "v_pk_fma_f32 v[108:109], v[228:229], v[228:229], v[108:109]\n"
      "s_waitcnt lgkmcnt(3)\n"
      "v_mfma_f32_16x16x32_bf16 v[150:153], a[160:163], v[104:107], v[150:153]\n"
      "ds_write_b64 v147, v[232:233] offset:16\n"
      "v_mfma_f32_16x16x32_bf16 v[194:197], a[184:187], v[104:107], v[194:197]\n"
      "s_add_u32 m0, s75, 77696\n"
      "ds_read_b128 v[230:233], v116 offset:6144\n"
      "v_mfma_f32_16x16x32_bf16 v[194:197], a[188:191], v[130:133], v[194:197]\n"
      "buffer_load_dwordx4 v149, s[36:39], s79 offen offset:128  lds\n"
      "v_cvt_pk_bf16_f32 v124, v124, v125\n"
      "v_cvt_pk_bf16_f32 v123, v254, v255\n"
      "v_add_f32 v76, v76, v77\n"
      "v_cvt_pk_bf16_f32 v162, v162, v163\n"
      "v_cvt_pk_bf16_f32 v202, v240, v241\n"
      "v_add_f32 v108, v108, v109\n"
      "v_mfma_f32_16x16x32_bf16 v[204:207], a[208:211], v[104:107], v[204:207]\n"
      "s_add_u32 m0, s75, 81664\n"
      "ds_read_b128 v[166:169], v143 offset:6144\n"
      "v_mfma_f32_16x16x32_bf16 v[204:207], a[212:215], v[130:133], v[204:207]\n"
      "ds_read_b128 v[104:107], v0 offset:10240\n"
      "v_cvt_pk_bf16_f32 v171, v172, v173\n"
      "v_cvt_pk_bf16_f32 v125, v126, v127\n"
      "v_cvt_pk_bf16_f32 v194, v194, v195\n"
      "v_mfma_f32_16x16x32_bf16 v[150:153], a[164:167], v[130:133], v[150:153]\n"
      "buffer_load_dwordx4 v149, s[36:39], s79 offen offset:256  lds\n"
      "s_waitcnt lgkmcnt(2)\n"
      "v_mfma_f32_16x16x32_bf16 v[126:129], a[144:147], v[242:245], 0\n"
      "ds_read_b128 v[116:119], v0 offset:4096\n"
      "v_cvt_pk_bf16_f32 v213, v214, v215\n"
      "v_cvt_pk_bf16_f32 v195, v196, v197\n"
      "v_mfma_f32_16x16x32_bf16 v[196:199], a[192:195], v[242:245], 0\n"
      "s_mov_b32 s79, 61440\n"
      "v_permlane16_swap_b32 v76, v108\n"
      "v_add_f32_e64 v76, v76, v108\n"
      "s_add_u32 m0, s75, 86016\n"
      "v_mfma_f32_16x16x32_bf16 v[196:199], a[196:199], v[234:237], v[196:199]\n"
      "buffer_load_dwordx4 v149, s[36:39], s79 offen  lds\n"
      "v_mfma_f32_16x16x32_bf16 v[214:217], a[168:171], v[242:245], 0\n"
      "ds_read_b128 v[242:245], v143 offset:10240\n"
      "v_mov_b32_e64 v108, v76\n"
      "v_cvt_pk_bf16_f32 v122, v252, v253\n"
      "v_mfma_f32_16x16x32_bf16 v[126:129], a[148:151], v[234:237], v[126:129]\n"
      "s_add_u32 m0, s75, 89984\n"
      "v_permlane32_swap_b32_e64 v76, v108\n"
      "v_mfma_f32_16x16x32_bf16 v[126:129], a[152:155], v[230:233], v[126:129]\n"
      "ds_write_b64 v148, v[194:195] offset:32\n"
      "v_add_f32 v76, v76, v108\n"
      "v_xor_b32 v241, 64, v8\n"
      "v_cvt_pk_bf16_f32 v209, v210, v211\n"
      "v_mfma_f32_16x16x32_bf16 v[196:199], a[200:203], v[230:233], v[196:199]\n"
      "buffer_load_dwordx4 v149, s[36:39], s79 offen offset:128  lds\n"
      "v_mfma_f32_16x16x32_bf16 v[214:217], a[172:175], v[234:237], v[214:217]\n"
      "ds_read_b128 v[130:133], v1 offset:4096\n"
      "v_rsq_f32_e64 v76, v76\n"
      "v_mfma_f32_16x16x32_bf16 v[214:217], a[176:179], v[230:233], v[214:217]\n"
      "s_waitcnt lgkmcnt(2)\n"
      "v_add_u32 v172, 49152, v241\n"
      "v_mov_b32 v108, v76\n"
      "v_mfma_f32_16x16x32_bf16 v[214:217], a[180:183], v[166:169], v[214:217]\n"
      "s_add_u32 m0, s75, 93952\n"
      "v_permlane16_swap_b32_e64 v76, v108\n"
      "v_mfma_f32_16x16x32_bf16 v[126:129], a[156:159], v[166:169], v[126:129]\n"
      "buffer_load_dwordx4 v149, s[36:39], s79 offen offset:256  lds\n"
      "s_waitcnt vmcnt(12)\n"
      "s_barrier\n"
      "ds_read_b64_tr_b16 a[222:223], v239 offset:38912\n"
      "v_pk_mul_f32 v[134:135], v[134:135], v[144:145] op_sel_hi:[1,0]\n"
      "v_mfma_f32_16x16x32_bf16 v[126:129], a[160:163], v[246:249], v[126:129]\n"
      "ds_read_b64_tr_b16 v[232:233], v146 offset:24576\n"
      "ds_read_b64_tr_b16 a[216:217], v239 offset:24576\n"
      "ds_read_b64_tr_b16 a[242:243], v239 offset:26632\n"
      "v_mfma_f32_16x16x32_bf16 v[126:129], a[164:167], v[242:245], v[126:129]\n"
      "buffer_load_dwordx4 v[142:145], v36, s[28:31], s49 offen\n"
      "v_mfma_f32_16x16x32_bf16 v[196:199], a[204:207], v[166:169], v[196:199]\n"
      "ds_read_b128 v[250:253], v1 offset:10240\n"
      "v_cvt_pk_bf16_f32 v220, v220, v221\n"
      "v_mfma_f32_16x16x32_bf16 v[196:199], a[208:211], v[246:249], v[196:199]\n"
      "ds_read_b64_tr_b16 a[246:247], v239 offset:38920\n"
      "ds_read_b64_tr_b16 a[240:241], v239 offset:24584\n"
      "v_mul_f32 v76, s50, v76\n"
      "v_mfma_f32_16x16x32_bf16 v[214:217], a[184:187], v[246:249], v[214:217]\n"
      "ds_read_b64_tr_b16 v[236:237], v146 offset:36864\n"
      "ds_read_b64_tr_b16 a[244:245], v239 offset:36872\n"
      "v_mfma_f32_16x16x32_bf16 v[214:217], a[188:191], v[242:245], v[214:217]\n"
      "ds_read_b64_tr_b16 a[220:221], v239 offset:36864\n"
      "buffer_load_dword v255, v29, s[28:31], s49 offen\n"
      "v_pk_mul_f32 v[112:113], v[112:113], v[76:77] op_sel_hi:[1,0]\n"
      "v_pk_mul_f32 v[96:97], v[96:97], v[76:77] op_sel_hi:[1,0]\n"
      "v_cvt_pk_bf16_f32 v210, v190, v191\n"
      "v_mfma_f32_16x16x32_bf16 v[196:199], a[212:215], v[242:245], v[196:199]\n"
      "ds_read_b64_tr_b16 a[218:219], v239 offset:26624\n"
      "v_cvt_pk_bf16_f32 v163, v164, v165\n"
      "v_mfma_f32_16x16x32_bf16 v[188:191], v[116:119], a[168:171], 0\n"
      "ds_read_b64_tr_b16 v[238:239], v146 offset:38912\n"
      "s_waitcnt lgkmcnt(5)\n"
      "v_mfma_f32_16x16x32_bf16 v[188:191], v[130:133], a[172:175], v[188:191]\n"
      "buffer_load_dwordx4 v[166:169], v36, s[28:31], s74 offen\n"
      "ds_read_b64_tr_b16 v[234:235], v146 offset:26624\n"
      "v_cvt_pk_bf16_f32 v158, v158, v159\n"
      "v_cvt_pk_bf16_f32 v214, v214, v215\n"
      "v_cvt_pk_bf16_f32 v164, v134, v135\n"
      "v_mov_b32 v109, 0xbc80bc80\n"
      "v_pk_mul_f32 v[156:157], v[156:157], v[108:109] op_sel_hi:[1,0]\n"
      "v_cvt_pk_bf16_f32 v159, v160, v161\n"
      "v_mov_b32 v110, 0xbc80bc80\n"
      "v_pk_mul_f32 v[154:155], v[154:155], v[108:109] op_sel_hi:[1,0]\n"
      "v_mov_b32 v111, 0xbc80bc80\n"
      "v_cvt_pk_bf16_f32 v126, v126, v127\n"
      "v_cvt_pk_bf16_f32 v127, v128, v129\n"
      "v_pk_mul_f32 v[174:175], v[174:175], v[76:77] op_sel_hi:[1,0]\n"
      "v_pk_mul_f32 v[176:177], v[176:177], v[76:77] op_sel_hi:[1,0]\n"
      "v_cvt_pk_bf16_f32 v211, v192, v193\n"
      "v_cvt_pk_bf16_f32 v112, v112, v113\n"
      "v_cvt_pk_bf16_f32 v221, v222, v223\n"
      "v_mfma_f32_16x16x32_bf16 v[222:225], v[116:119], a[144:147], 0\n"
      "buffer_load_dword v254, v29, s[28:31], s74 offen\n"
      "v_cvt_pk_bf16_f32 v215, v216, v217\n"
      "s_add_u32 m0, s64, 18432\n"
      "v_pk_mul_f32 v[186:187], v[186:187], v[108:109] op_sel_hi:[1,0]\n"
      "v_pk_mul_f32 v[228:229], v[228:229], v[108:109] op_sel_hi:[1,0]\n"
      "v_pk_mul_f32 v[226:227], v[226:227], v[108:109] op_sel_hi:[1,0]\n"
      "v_cvt_pk_bf16_f32 v165, v136, v137\n"
      "v_pk_mul_f32 v[184:185], v[184:185], v[108:109] op_sel_hi:[1,0]\n"
      "v_cvt_pk_bf16_f32 v196, v196, v197\n"
      "v_pk_mul_f32 v[114:115], v[114:115], v[76:77] op_sel_hi:[1,0]\n"
      "v_cvt_pk_bf16_f32 v113, v114, v115\n"
      "v_pk_mul_f32 v[98:99], v[98:99], v[76:77] op_sel_hi:[1,0]\n"
      "v_cvt_pk_bf16_f32 v204, v204, v205\n"
      "v_cvt_pk_bf16_f32 v197, v198, v199\n"
      "v_cvt_pk_bf16_f32 v226, v226, v227\n"
      "v_mfma_f32_16x16x32_bf16 v[222:225], v[130:133], a[148:151], v[222:225]\n"
      "buffer_load_dwordx4 v16, s[16:19], s45 offen  lds\n"
      "v_subrev_u32 v39, 115456, v9\n"
      "v_cvt_pk_bf16_f32 v154, v154, v155\n"
      "v_mfma_f32_16x16x32_bf16 v[222:225], v[104:107], a[152:155], v[222:225]\n"
      "s_add_u32 m0, s64, 22528\n"
      "v_cvt_pk_bf16_f32 v227, v228, v229\n"
      "v_cvt_pk_bf16_f32 v174, v174, v175\n"
      "v_mfma_f32_16x16x32_bf16 v[222:225], v[250:253], a[156:159], v[222:225]\n"
      "ds_read_b128 v[242:245], v8 offset:24576\n"
      "ds_read_b128 v[246:249], v241 offset:24576\n"
      "v_mfma_f32_16x16x32_bf16 v[188:191], v[104:107], a[176:179], v[188:191]\n"
      "buffer_load_dwordx4 v17, s[16:19], s45 offen  lds\n"
      "v_cvt_pk_bf16_f32 v139, v140, v141\n"
      "v_mfma_f32_16x16x32_bf16 v[134:137], v[116:119], a[192:195], 0\n"
      "s_add_u32 m0, s64, 26624\n"
      "ds_read_b128 v[192:195], v8 offset:28672\n"
      "v_cvt_pk_bf16_f32 v155, v156, v157\n"
      "v_mfma_f32_16x16x32_bf16 v[134:137], v[130:133], a[196:199], v[134:137]\n"
      "ds_read_b128 v[130:133], v241 offset:28672\n"
      "v_lshrrev_b32_e64 v39, 4, v39\n"
      "v_mfma_f32_16x16x32_bf16 v[134:137], v[104:107], a[200:203], v[134:137]\n"
      "ds_read_b128 v[228:231], v0 offset:2048\n"
      "v_cvt_pk_bf16_f32 v175, v176, v177\n"
      "v_mfma_f32_16x16x32_bf16 v[134:137], v[250:253], a[204:207], v[134:137]\n"
      "ds_read_b128 v[104:107], v1 offset:2048\n"
      "buffer_load_dwordx4 v18, s[16:19], s45 offen  lds\n"
      "v_mfma_f32_16x16x32_bf16 v[188:191], v[250:253], a[180:183], v[188:191]\n"
      "s_mov_b32 exec_lo, 0x3f003f\n"
      "s_mov_b32 exec_hi, 0x3f003f\n"
      "ds_write_b64 v148, v[126:127] offset:18688\n"
      "ds_write_b64 v148, v[214:215] offset:18720\n"
      "ds_write_b64 v148, v[196:197] offset:18752\n"
      "s_mov_b64 exec, -1\n"
      "s_add_u32 m0, s64, 30720\n"
      "s_waitcnt lgkmcnt(3)\n"
      "v_mfma_f32_16x16x32_bf16 v[196:199], v[228:231], a[168:171], 0\n"
      "ds_read_b128 v[126:129], v241 offset:32768\n"
      "v_cvt_pk_bf16_f32 v177, v98, v99\n"
      "v_mov_b32 v108, 0xbc80bc80\n"
      "v_cvt_pk_bf16_f32 v156, v184, v185\n"
      "v_mfma_f32_16x16x32_bf16 v[196:199], v[104:107], a[172:175], v[196:199]\n"
      "ds_read_b128 v[114:117], v0 offset:16384\n"
      "v_cvt_pk_bf16_f32 v157, v186, v187\n"
      "v_mfma_f32_16x16x32_bf16 v[184:187], v[242:245], v[108:111], 0\n"
      "buffer_load_dwordx4 v19, s[16:19], s45 offen  lds\n"
      "v_mfma_f32_16x16x32_bf16 v[184:187], v[246:249], v[108:111], v[184:187]\n"
      "s_waitcnt vmcnt(14)\n"
      "s_barrier\n"
      "s_add_u32 m0, s65, 34816\n"
      "v_cvt_pk_bf16_f32 v176, v96, v97\n"
      "v_mfma_f32_16x16x32_bf16 v[184:187], v[192:195], v[108:111], v[184:187]\n"
      "ds_read_b128 v[96:99], v8 offset:32768\n"
      "ds_read_b128 v[250:253], v0 offset:8192\n"
      "v_mfma_f32_16x16x32_bf16 v[184:187], v[130:133], v[108:111], v[184:187]\n"
      "ds_read_b64_tr_b16 a[228:229], v90 offset:12288\n"
      "v_cvt_pk_bf16_f32 v150, v150, v151\n"
      "v_mfma_f32_16x16x32_bf16 v[214:217], v[228:231], a[192:195], 0\n"
      "buffer_load_dwordx4 v15, s[16:19], s45 offen  lds\n"
      "ds_read_b64_tr_b16 a[230:231], v90 offset:14336\n"
      "ds_read_b64_tr_b16 v[242:243], v82 offset:2048\n"
      "v_mfma_f32_16x16x32_bf16 v[130:133], v[228:231], a[144:147], 0\n"
      "ds_read_b64_tr_b16 a[250:251], v90 offset:2056\n"
      "v_cvt_pk_bf16_f32 v151, v152, v153\n"
      "v_mfma_f32_16x16x32_bf16 v[130:133], v[104:107], a[148:151], v[130:133]\n"
      "ds_read_b64_tr_b16 a[254:255], v90 offset:14344\n"
      "ds_read_b64_tr_b16 a[252:253], v90 offset:12296\n"
      "v_mfma_f32_16x16x32_bf16 v[214:217], v[104:107], a[196:199], v[214:217]\n"
      "ds_read_b64_tr_b16 v[246:247], v82 offset:14336\n"
      "ds_read_b64_tr_b16 a[224:225], v90\n"
      "s_waitcnt lgkmcnt(7)\n"
      "v_mfma_f32_16x16x32_bf16 v[214:217], v[250:253], a[200:203], v[214:217]\n"
      "ds_read_b128 v[228:231], v1 offset:8192\n"
      "ds_read_b64_tr_b16 v[240:241], v82\n"
      "v_mfma_f32_16x16x32_bf16 v[184:187], v[96:99], v[108:111], v[184:187]\n"
      "ds_read_b64_tr_b16 v[244:245], v82 offset:12288\n"
      "ds_read_b64_tr_b16 a[226:227], v90 offset:2048\n"
      "v_mfma_f32_16x16x32_bf16 v[184:187], v[126:129], v[108:111], v[184:187]\n"
      "ds_read_b64_tr_b16 a[248:249], v90 offset:8\n"
      "v_cvt_pk_bf16_f32 v205, v206, v207\n"
      "v_mfma_f32_16x16x32_bf16 v[196:199], v[250:253], a[176:179], v[196:199]\n"
      "ds_read_b128 v[126:129], v1 offset:16384\n"
      "v_and_b32 v39, 48, v39\n"
      "v_mfma_f32_16x16x32_bf16 v[188:191], v[114:117], a[184:187], v[188:191]\n"
      "ds_read_b128 v[96:99], v0 offset:14336\n"
      "ds_read_b128 v[104:107], v1 offset:14336\n"
      "v_mfma_f32_16x16x32_bf16 v[222:225], v[114:117], a[160:163], v[222:225]\n"
      "s_mov_b32 exec_lo, 0x3f003f\n"
      "s_mov_b32 exec_hi, 0x3f003f\n"
      "ds_write_b128 v147, v[174:177] offset:18688\n"
      "ds_write_b64 v147, v[112:113] offset:18704\n"
      "ds_write_b64 v147, v[226:227] offset:18712\n"
      "ds_write_b128 v147, v[154:157] offset:18720\n"
      "s_mov_b64 exec, -1\n"
      "v_mfma_f32_16x16x32_bf16 v[134:137], v[114:117], a[208:211], v[134:137]\n"
      "ds_read_b128 v[174:177], v172\n"
      "v_mul_f32 v187, 0x3eaaaaab, v187\n"
      "v_mfma_f32_16x16x32_bf16 v[130:133], v[250:253], a[152:155], v[130:133]\n"
      "s_waitcnt lgkmcnt(5)\n"
      "v_add_u32 v157, 49152, v8\n"
      "v_mul_f32 v186, 0x3eaaaaab, v186\n"
      "v_mfma_f32_16x16x32_bf16 v[130:133], v[228:231], a[156:159], v[130:133]\n"
      "ds_read_b128 v[250:253], v157 offset:4096\n"
      "v_add_u32 v39, 123136, v39\n"
      "v_mfma_f32_16x16x32_bf16 v[196:199], v[228:231], a[180:183], v[196:199]\n"
      "buffer_load_dwordx4 v[116:119], v36, s[28:31], s47 offen\n"
      "v_mfma_f32_16x16x32_bf16 v[222:225], v[126:129], a[164:167], v[222:225]\n"
      "ds_write_b64 v148, v[150:151]\n"
      "v_mfma_f32_16x16x32_bf16 v[214:217], v[228:231], a[204:207], v[214:217]\n"
      "ds_read_b128 v[228:231], v157\n"
      "v_add_u32_e64 v219, s92, v39\n"
      "v_mfma_f32_16x16x32_bf16 v[214:217], v[96:99], a[208:211], v[214:217]\n"
      "ds_read_b128 v[112:115], v157 offset:8192\n"
      "v_mul_f32 v185, 0x3eaaaaab, v185\n"
      "v_mul_f32 v184, 0x3eaaaaab, v184\n"
      "v_mfma_f32_16x16x32_bf16 v[214:217], v[104:107], a[212:215], v[214:217]\n"
      "ds_read_b128 v[152:155], v172 offset:8192\n"
      "v_cvt_pk_bf16_f32 v76, v222, v223\n"
      "v_mfma_f32_16x16x32_bf16 v[188:191], v[126:129], a[188:191], v[188:191]\n"
      "ds_write_b64 v148, v[204:205] offset:64\n"
      "v_mfma_f32_16x16x32_bf16 v[134:137], v[126:129], a[212:215], v[134:137]\n"
      "ds_read_b128 v[126:129], v172 offset:4096\n"
      "v_cvt_pk_bf16_f32 v77, v224, v225\n"
      "v_mfma_f32_16x16x32_bf16 v[204:207], v[200:203], v[180:183], v[44:47]\n"
      "buffer_load_dword v223, v29, s[28:31], s47 offen\n"
      "v_mfma_f32_16x16x32_bf16 v[196:199], v[96:99], a[184:187], v[196:199]\n"
      "ds_read_b128 v[224:227], v1\n"
      "v_cvt_pk_bf16_f32 v217, v216, v217\n"
      "v_mfma_f32_16x16x32_bf16 v[130:133], v[96:99], a[160:163], v[130:133]\n"
      "ds_read_b128 v[146:149], v0\n"
      "v_cvt_pk_bf16_f32 v81, v190, v191\n"
      "v_mfma_f32_16x16x32_bf16 v[130:133], v[104:107], a[164:167], v[130:133]\n"
      "s_waitcnt lgkmcnt(5)\n"
      "v_cvt_pk_bf16_f32 v216, v214, v215\n"
      "v_subrev_u32 v218, 115456, v9\n"
      "v_mfma_f32_16x16x32_bf16 v[196:199], v[104:107], a[188:191], v[196:199]\n"
      "ds_write_b128 v219, v[184:187]\n"
      "v_mfma_f32_16x16x32_bf16 v[184:187], v[228:231], v[108:111], 0\n"
      "s_waitcnt vmcnt(11)\n"
      "s_barrier\n"
      "v_cvt_pk_bf16_f32 v80, v188, v189\n"
      "v_mfma_f32_16x16x32_bf16 v[184:187], v[174:177], v[108:111], v[184:187]\n"
      "ds_read_b128 v[228:231], v157 offset:28672\n"
      "ds_read_b128 v[174:177], v157 offset:24576\n"
      "v_mfma_f32_16x16x32_bf16 v[184:187], v[250:253], v[108:111], v[184:187]\n"
      "ds_read_b128 v[248:251], v172 offset:24576\n"
      "s_waitcnt lgkmcnt(4)\n"
      "v_mfma_f32_16x16x32_bf16 v[184:187], v[126:129], v[108:111], v[184:187]\n"
      "ds_read_b128 v[188:191], v172 offset:32768\n"
      "v_cvt_pk_bf16_f32 v199, v198, v199\n"
      "v_bfe_u32 v214, v218, 8, 2\n"
      "v_mfma_f32_16x16x32_bf16 v[184:187], v[112:115], v[108:111], v[184:187]\n"
      "ds_read_b128 v[126:129], v172 offset:28672\n"
      "v_cvt_pk_bf16_f32 v84, v134, v135\n"
      "v_mfma_f32_16x16x32_bf16 v[184:187], v[152:155], v[108:111], v[184:187]\n"
      "ds_read_b128 v[192:195], v157 offset:32768\n"
      "v_cvt_pk_bf16_f32 v198, v196, v197\n"
      "v_mfma_f32_16x16x16_bf16 v[204:207], v[138:139], v[220:221], v[204:207]\n"
      "v_cvt_pk_bf16_f32 v85, v136, v137\n"
      "v_lshlrev_b32_e64 v214, 2, v214\n"
      "v_cvt_pk_bf16_f32 v133, v132, v133\n"
      "v_mfma_f32_16x16x32_bf16 v[154:157], v[146:149], a[144:147], 0\n"
      "ds_read_b128 v[134:137], v0 offset:6144\n"
      "v_bfe_u32 v218, v218, 4, 4\n"
      "v_mfma_f32_16x16x32_bf16 v[154:157], v[224:227], a[148:151], v[154:157]\n"
      "ds_read_b64_tr_b16 a[238:239], v90 offset:38912\n"
      "s_waitcnt lgkmcnt(5)\n"
      "v_mfma_f32_16x16x32_bf16 v[96:99], v[174:177], v[108:111], 0\n"
      "ds_read_b64_tr_b16 v[252:253], v82 offset:36864\n"
      "s_waitcnt vmcnt(10)\n"
      "v_or3_b32 v142, v142, v143, v144\n"
      "v_mfma_f32_16x16x32_bf16 v[96:99], v[248:251], v[108:111], v[96:99]\n"
      "ds_read_b128 v[174:177], v10 offset:32\n"
      "v_mul_f32 v187, 0x3eaaaaab, v187\n"
      "v_mfma_f32_16x16x32_bf16 v[96:99], v[228:231], v[108:111], v[96:99]\n"
      "ds_read_b128 v[104:107], v1 offset:6144\n"
      "s_waitcnt lgkmcnt(5)\n"
      "v_mfma_f32_16x16x32_bf16 v[96:99], v[126:129], v[108:111], v[96:99]\n"
      "ds_read_b64_tr_b16 a[236:237], v90 offset:36864\n"
      "v_cvt_pk_bf16_f32 v132, v130, v131\n"
      "v_mfma_f32_16x16x32_bf16 v[96:99], v[192:195], v[108:111], v[96:99]\n"
      "ds_read_b64_tr_b16 v[230:231], v90 offset:38920\n"
      "v_sub_u32 v218, v218, v214\n"
      "v_mfma_f32_16x16x32_bf16 v[96:99], v[188:191], v[108:111], v[96:99]\n"
      "ds_read_b64_tr_b16 v[228:229], v90 offset:36872\n"
      "v_mul_f32 v184, 0x3eaaaaab, v184\n"
      "v_mfma_f32_16x16x32_bf16 v[188:191], v[146:149], a[192:195], 0\n"
      "ds_read_b64 v[172:173], v10 offset:24\n"
      "v_mul_f32 v185, 0x3eaaaaab, v185\n"
      "v_mov_b32 v215, 0\n"
      "v_mfma_f32_16x16x32_bf16 v[188:191], v[224:227], a[196:199], v[188:191]\n"
      "s_waitcnt lgkmcnt(5)\n"
      "s_waitcnt vmcnt(9)\n"
      "v_or3_b32 v142, v142, v145, v255\n"
      "v_mul_f32 v96, 0x3eaaaaab, v96\n"
      "v_mfma_f32_16x16x32_bf16 v[108:111], v[146:149], a[168:171], 0\n"
      "ds_read_b128 v[144:147], v1 offset:12288\n"
      "v_mul_f32 v97, 0x3eaaaaab, v97\n"
      "v_mfma_f32_16x16x32_bf16 v[108:111], v[224:227], a[172:175], v[108:111]\n"
      "ds_read_b64_tr_b16 v[250:251], v82 offset:26624\n"
      "v_mul_f32 v98, 0x3eaaaaab, v98\n"
      "v_mfma_f32_16x16x32_bf16 v[108:111], v[134:137], a[176:179], v[108:111]\n"
      "ds_read_b128 v[126:129], v0 offset:12288\n"
      "s_waitcnt vmcnt(8)\n"
      "v_or3_b32 v166, v166, v167, v168\n"
      "v_mfma_f32_16x16x32_bf16 v[150:153], v[208:211], v[180:183], v[40:43]\n"
      "ds_write_b32 v31, v142 offset:6656\n"
      "v_mfma_f32_16x16x32_bf16 v[140:143], v[174:177], v[180:183], v[24:27]\n"
      "ds_read_b64_tr_b16 a[234:235], v90 offset:26624\n"
      "v_mul_f32 v99, 0x3eaaaaab, v99\n"
      "v_mfma_f32_16x16x32_bf16 v[154:157], v[134:137], a[152:155], v[154:157]\n"
      "ds_read_b64_tr_b16 v[224:225], v90 offset:24584\n"
      "v_mul_f32 v186, 0x3eaaaaab, v186\n"
      "v_mfma_f32_16x16x32_bf16 v[188:191], v[134:137], a[200:203], v[188:191]\n"
      "ds_write_b128 v219, v[184:187] offset:256\n"
      "v_mfma_f32_16x16x32_bf16 v[192:195], v[174:177], v[100:103], v[26:29]\n"
      "ds_read_b64_tr_b16 a[232:233], v90 offset:24576\n"
      "s_waitcnt lgkmcnt(5)\n"
      "v_mfma_f32_16x16x32_bf16 v[154:157], v[104:107], a[156:159], v[154:157]\n"
      "ds_read_b64_tr_b16 v[248:249], v82 offset:24576\n"
      "s_waitcnt vmcnt(7)\n"
      "v_or3_b32 v166, v166, v169, v254\n"
      "v_mfma_f32_16x16x32_bf16 v[108:111], v[104:107], a[180:183], v[108:111]\n"
      "ds_write_b128 v219, v[96:99] offset:512\n"
      "v_mfma_f32_16x16x32_bf16 v[108:111], v[126:129], a[184:187], v[108:111]\n"
      "ds_read_b64_tr_b16 v[226:227], v90 offset:26632\n"
      "v_mov_b32 v214, 0x3f80\n"
      "v_mfma_f32_16x16x32_bf16 v[134:137], v[120:123], v[180:183], v[48:51]\n"
      "ds_read_b64_tr_b16 v[254:255], v82 offset:38912\n"
      "v_lshlrev_b32_e64 v218, 4, v218\n"
      "v_mfma_f32_16x16x32_bf16 v[154:157], v[126:129], a[160:163], v[154:157]\n"
      "ds_write_b32 v31, v166 offset:5632\n"
      "v_mfma_f32_16x16x32_bf16 v[154:157], v[144:147], a[164:167], v[154:157]\n"
      "s_waitcnt lgkmcnt(0)\n"
      "s_barrier\n"
      "v_mov_b32_e64 v99, 0\n"
      "v_mfma_f32_16x16x16_bf16 v[150:153], v[212:213], v[220:221], v[150:153]\n"
      "ds_read_b128 v[166:169], v9 offset:6656\n"
      "v_lshlrev_b64 v[214:215], v218, v[214:215]\n"
      "v_mfma_f32_16x16x32_bf16 v[108:111], v[144:147], a[188:191], v[108:111]\n"
      "ds_read_b128 v[112:115], v9 offset:5632\n"
      "v_cmp_gt_u32 s[80:81], 64, v218\n"
      "v_mfma_f32_16x16x32_bf16 v[188:191], v[104:107], a[204:207], v[188:191]\n"
      "ds_read_b128 v[104:107], v39 offset:64\n"
      "v_cndmask_b32_e64 v184, 0, v214, s[80:81]\n"
      "v_mfma_f32_16x16x16_bf16 v[134:137], v[124:125], v[220:221], v[134:137]\n"
      "v_mov_b32_e64 v183, 0\n"
      "v_cndmask_b32_e64 v97, 0, v215, s[80:81]\n"
      "v_cvt_pk_bf16_f32 v130, v154, v155\n"
      "v_mfma_f32_16x16x16_bf16 v[140:143], v[172:173], v[220:221], v[140:143]\n"
      "v_cndmask_b32_e64 v96, 0, v214, s[80:81]\n"
      "v_cvt_pk_bf16_f32 v196, v108, v109\n"
      "v_mov_b32_e64 v182, 0\n"
      "v_mfma_f32_16x16x32_bf16 v[188:191], v[126:129], a[208:211], v[188:191]\n"
      "ds_read_b128 v[126:129], v39\n"
      "v_cndmask_b32_e64 v185, 0, v215, s[80:81]\n"
      "v_mfma_f32_16x16x32_bf16 v[188:191], v[144:147], a[212:215], v[188:191]\n"
      "s_waitcnt lgkmcnt(3)\n"
      "v_or3_b32 v166, v166, v167, v168\n"
      "v_mov_b32 v98, 0\n"
      "v_mfma_f32_16x16x16_bf16 v[192:195], v[172:173], v[170:171], v[192:195]\n"
      "v_cvt_pk_bf16_f32 v131, v156, v157\n"
      "v_bitop3_b32 v166, v166, v169, s33 bitop3:0xa8\n"
      "v_cmp_ne_u32 s[56:57], 0, v166\n"
      "v_mfma_f32_16x16x32_bf16 v[166:169], v[208:211], v[100:103], v[52:55]\n"
      "s_waitcnt lgkmcnt(1)\n"
      "v_cvt_pk_bf16_f32 v215, v190, v191\n"
      "v_or3_b32 v112, v112, v113, v114\n"
      "v_mfma_f32_16x16x32_bf16 v[144:147], v[182:185], v[232:235], v[104:107]\n"
      "s_or_b64 vcc, s[56:57], s[54:55]\n"
      "v_cvt_pk_bf16_f32 v214, v188, v189\n"
      "v_bitop3_b32 v112, v112, v115, s33 bitop3:0xa8\n"
      "v_mfma_f32_16x16x32_bf16 v[188:191], v[182:185], a[216:219], v[104:107]\n"
      "s_cbranch_vccnz L_rare_1_%=\n"
      "L_back_2_%=:\n"
      "v_mfma_f32_16x16x32_bf16 v[218:221], v[182:185], a[240:243], v[104:107]\n"
      "s_nop 2\n"
      "s_cbranch_vccnz L_rare_3_%=\n"
      "L_back_4_%=:\n"
      "v_mfma_f32_16x16x32_bf16 v[106:109], v[120:123], v[100:103], v[60:63]\n"
      "s_waitcnt lgkmcnt(0)\n"
      "v_cvt_pk_bf16_f32 v197, v110, v111\n"
      "v_cmp_ne_u32 s[56:57], 0, v112\n"
      "v_mfma_f32_16x16x32_bf16 v[110:113], v[96:99], v[232:235], v[126:129]\n"
      "v_cvt_pk_bf16_f32 v234, v144, v145\n"
      "v_cvt_pk_bf16_f32 v235, v146, v147\n"
      "v_exp_f32 v151, v151\n"
      "v_cvt_pk_bf16_f32 v218, v218, v219\n"
      "v_mfma_f32_16x16x16_bf16 v[166:169], v[212:213], v[170:171], v[166:169]\n"
      "v_exp_f32 v137, v137\n"
      "v_cvt_pk_bf16_f32 v220, v220, v221\n"
      "v_mfma_f32_16x16x16_bf16 v[106:109], v[124:125], v[170:171], v[106:109]\n"
      "v_cvt_pk_bf16_f32 v233, v112, v113\n"
      "v_exp_f32 v153, v153\n"
      "v_exp_f32 v150, v150\n"
      "v_cvt_pk_bf16_f32 v188, v188, v189\n"
      "v_mfma_f32_16x16x32_bf16 v[144:147], v[200:203], v[100:103], v[56:59]\n"
      "ds_read_b128 v[100:103], v10\n"
      "v_cvt_pk_bf16_f32 v232, v110, v111\n"
      "v_mfma_f32_16x16x32_bf16 v[110:113], v[96:99], a[216:219], v[126:129]\n"
      "v_exp_f32_e64 v207, v207\n"
      "v_mfma_f32_16x16x32_bf16 v[154:157], v[96:99], a[240:243], v[126:129]\n"
      "ds_read_b64 v[180:181], v10 offset:16\n"
      "v_accvgpr_write_b32 a243, v220\n"
      "v_cvt_pk_bf16_f32 v150, v150, v151\n"
      "v_mfma_f32_16x16x16_bf16 v[144:147], v[138:139], v[170:171], v[144:147]\n"
      "v_accvgpr_write_b32 a218, v188\n"
      "v_accvgpr_write_b32 a242, v218\n"
      "v_cvt_pk_bf16_f32 v110, v110, v111\n"
      "v_exp_f32 v206, v206\n"
      "v_exp_f32 v140, v140\n"
      "v_cvt_pk_bf16_f32 v112, v112, v113\n"
      "v_cvt_pk_bf16_f32 v154, v154, v155\n"
      "v_cvt_pk_bf16_f32 v190, v190, v191\n"
      "v_mfma_f32_16x16x32_bf16 v[186:189], v[200:203], v[162:165], v[68:71]\n"
      "s_waitcnt lgkmcnt(1)\n"
      "v_exp_f32 v204, v204\n"
      "v_exp_f32 v152, v152\n"
      "v_accvgpr_write_b32 a240, v154\n"
      "v_mfma_f32_16x16x32_bf16 v[126:129], v[100:103], v[200:203], v[30:33]\n"
      "s_cbranch_vccnz L_rare_5_%=\n"
      "L_back_6_%=:\n"
      "v_accvgpr_write_b32 a217, v112\n"
      "v_exp_f32 v135, v135\n"
      "v_mfma_f32_16x16x16_bf16 v[186:189], v[138:139], v[158:159], v[186:189]\n"
      "v_cvt_pk_bf16_f32 v151, v152, v153\n"
      "v_exp_f32 v147, v147\n"
      "v_exp_f32_e64 v134, v134\n"
      "v_mfma_f32_16x16x32_bf16 v[218:221], v[208:211], v[162:165], v[64:67]\n"
      "s_waitcnt lgkmcnt(0)\n"
      "v_exp_f32 v205, v205\n"
      "v_mfma_f32_16x16x16_bf16 v[126:129], v[180:181], v[138:139], v[126:129]\n"
      "v_exp_f32_e64 v107, v107\n"
      "v_cvt_pk_bf16_f32 v153, v206, v207\n"
      "v_mfma_f32_16x16x32_bf16 v[200:203], v[100:103], v[208:211], v[30:33]\n"
      "ds_read_u16 v82, v11 offset:32\n"
      "v_exp_f32_e64 v192, v192\n"
      "v_mfma_f32_16x16x32_bf16 v[208:211], v[174:177], v[162:165], v[28:31]\n"
      "ds_read_u16 v78, v11\n"
      "v_cvt_pk_bf16_f32 v152, v204, v205\n"
      "v_mfma_f32_16x16x16_bf16 v[218:221], v[212:213], v[158:159], v[218:221]\n"
      "v_exp_f32 v109, v109\n"
      "v_accvgpr_write_b32 a216, v110\n"
      "v_exp_f32 v136, v136\n"
      "v_mfma_f32_16x16x16_bf16 v[208:211], v[172:173], v[158:159], v[208:211]\n"
      "v_exp_f32_e64 v167, v167\n"
      "v_cvt_pk_bf16_f32 v90, v140, 0\n"
      "v_mfma_f32_16x16x32_bf16 v[204:207], v[120:123], v[162:165], v[72:75]\n"
      "ds_read_u16 v86, v11 offset:64\n"
      "v_exp_f32_e64 v108, v108\n"
      "v_mfma_f32_16x16x32_bf16 v[140:143], v[100:103], v[174:177], v[30:33]\n"
      "ds_read_b128 v[174:177], v39 offset:128\n"
      "v_cvt_pk_bf16_f32 v88, v134, v135\n"
      "v_mfma_f32_16x16x16_bf16 v[200:203], v[180:181], v[212:213], v[200:203]\n"
      "v_exp_f32 v169, v169\n"
      "v_exp_f32 v166, v166\n"
      "v_cvt_pk_bf16_f32 v89, v136, v137\n"
      "v_mfma_f32_16x16x32_bf16 v[210:213], v[100:103], v[120:123], v[30:33]\n"
      "ds_read_b128 v[160:163], v39 offset:192\n"
      "v_exp_f32 v145, v145\n"
      "v_mfma_f32_16x16x16_bf16 v[140:143], v[180:181], v[172:173], v[140:143]\n"
      "v_cvt_pk_bf16_f32 v156, v156, v157\n"
      "v_cvt_pk_bf16_f32 v94, v192, 0\n"
      "s_waitcnt vmcnt(1)\n"
      "v_or3_b32 v116, v116, v117, v118\n"
      "v_mfma_f32_16x16x32_bf16 v[110:113], v[130:133], v[150:153], 0\n"
      "s_waitcnt lgkmcnt(2)\n"
      "v_exp_f32 v168, v168\n"
      "v_mfma_f32_16x16x32_bf16 v[110:113], v[76:79], v[88:91], v[110:113]\n"
      "v_exp_f32_e64 v146, v146\n"
      "v_mfma_f32_16x16x16_bf16 v[204:207], v[124:125], v[158:159], v[204:207]\n"
      "v_cvt_pk_bf16_f32 v166, v166, v167\n"
      "v_exp_f32_e64 v144, v144\n"
      "v_mfma_f32_16x16x32_bf16 v[100:103], v[214:217], v[150:153], 0\n"
      "v_cvt_pk_bf16_f32 v93, v108, v109\n"
      "v_cndmask_b32 v200, v200, v126, s[58:59]\n"
      "v_mfma_f32_16x16x32_bf16 v[100:103], v[84:87], v[88:91], v[100:103]\n"
      "s_waitcnt lgkmcnt(0)\n"
      "v_cvt_pk_bf16_f32 v167, v168, v169\n"
      "v_accvgpr_write_b32 a219, v190\n"
      "v_mfma_f32_16x16x32_bf16 v[126:129], v[182:185], v[236:239], v[160:163]\n"
      "s_cbranch_vccnz L_rare_7_%=\n"
      "L_back_8_%=:\n"
      "v_mfma_f32_16x16x32_bf16 v[190:193], v[32:35], v[150:153], 0\n"
      "ds_read_b128 v[136:139], v39 offset:320\n"
      "v_exp_f32_e64 v188, v188\n"
      "v_cvt_pk_bf16_f32 v168, v144, v145\n"
      "v_mfma_f32_16x16x32_bf16 v[190:193], v[32:35], v[88:91], v[190:193]\n"
      "s_or_b32 s95, s84, s85\n"
      "v_exp_f32 v189, v189\n"
      "v_mfma_f32_16x16x16_bf16 v[210:213], v[180:181], v[124:125], v[210:213]\n"
      "v_exp_f32_e64 v106, v106\n"
      "v_cvt_pk_bf16_f32 v169, v146, v147\n"
      "v_mfma_f32_16x16x32_bf16 v[142:145], v[96:99], v[236:239], v[174:177]\n"
      "v_exp_f32 v205, v205\n"
      "v_mfma_f32_16x16x32_bf16 v[122:125], v[96:99], a[220:223], v[174:177]\n"
      "v_rcp_f32 v190, v190\n"
      "v_mfma_f32_16x16x32_bf16 v[146:149], v[196:199], v[150:153], 0\n"
      "ds_read_b128 v[152:155], v39 offset:256\n"
      "v_exp_f32 v218, v218\n"
      "v_mfma_f32_16x16x32_bf16 v[146:149], v[80:83], v[88:91], v[146:149]\n"
      "v_exp_f32 v206, v206\n"
      "v_mfma_f32_16x16x32_bf16 v[178:181], v[96:99], a[244:247], v[174:177]\n"
      "s_waitcnt lgkmcnt(1)\n"
      "v_exp_f32 v204, v204\n"
      "v_mfma_f32_16x16x32_bf16 v[170:173], v[182:185], a[248:251], v[136:139]\n"
      "v_cvt_pk_bf16_f32 v122, v122, v123\n"
      "v_exp_f32 v187, v187\n"
      "v_cvt_pk_bf16_f32 v92, v106, v107\n"
      "v_exp_f32 v219, v219\n"
      "v_pk_mul_f32 v[110:111], v[110:111], v[190:191] op_sel_hi:[1,0]\n"
      "v_exp_f32_e64 v207, v207\n"
      "v_pk_mul_f32 v[148:149], v[148:149], v[190:191] op_sel_hi:[1,0]\n"
      "v_cvt_pk_bf16_f32 v124, v124, v125\n"
      "v_pk_mul_f32 v[112:113], v[112:113], v[190:191] op_sel_hi:[1,0]\n"
      "v_cvt_pk_bf16_f32 v236, v142, v143\n"
      "v_mfma_f32_16x16x32_bf16 v[174:177], v[182:185], a[244:247], v[160:163]\n"
      "s_or_b32 s95, s95, s48\n"
      "v_exp_f32 v221, v221\n"
      "v_mfma_f32_16x16x32_bf16 v[106:109], v[182:185], a[220:223], v[160:163]\n"
      "s_cmp_ge_u32 s95, 0x40000000\n"
      "s_cselect_b32 s94, -1, -2\n"
      "v_cndmask_b32 v200, v200, v210, s[60:61]\n"
      "v_cvt_pk_bf16_f32 v89, v206, v207\n"
      "v_accvgpr_write_b32 a220, v122\n"
      "v_cvt_pk_bf16_f32 v239, v128, v129\n"
      "v_pk_mul_f32 v[100:101], v[100:101], v[190:191] op_sel_hi:[1,0]\n"
      "v_exp_f32_e64 v208, v208\n"
      "v_pk_mul_f32 v[146:147], v[146:147], v[190:191] op_sel_hi:[1,0]\n"
      "v_mfma_f32_16x16x32_bf16 v[210:213], v[182:185], v[240:243], v[136:139]\n"
      "v_cndmask_b32 v200, v200, v140, s[62:63]\n"
      "v_cvt_pk_bf16_f32 v176, v176, v177\n"
      "v_cvt_pk_bf16_f32 v172, v172, v173\n"
      "v_mfma_f32_16x16x32_bf16 v[160:163], v[182:185], a[224:227], v[136:139]\n"
      "s_waitcnt lgkmcnt(0)\n"
      "v_exp_f32_e64 v186, v186\n"
      "v_mfma_f32_16x16x32_bf16 v[140:143], v[96:99], a[224:227], v[152:155]\n"
      "s_waitcnt vmcnt(0)\n"
      "v_or3_b32 v116, v116, v119, v223\n"
      "v_cvt_pk_bf16_f32 v108, v108, v109\n"
      "v_mfma_f32_16x16x32_bf16 v[118:121], v[96:99], v[240:243], v[152:155]\n"
      "v_exp_f32 v220, v220\n"
      "v_cvt_pk_bf16_f32 v237, v144, v145\n"
      "v_cvt_pk_bf16_f32 v218, v218, v219\n"
      "v_mfma_f32_16x16x32_bf16 v[134:137], v[96:99], a[248:251], v[152:155]\n"
      "v_cvt_pk_bf16_f32 v180, v180, v181\n"
      "v_cvt_pk_bf16_f32 v110, v110, v111\n"
      "v_mfma_f32_16x16x32_bf16 v[150:153], v[32:35], v[166:169], 0\n"
      "s_cbranch_vccnz L_rare_9_%=\n"
      "L_back_10_%=:\n"
      "v_cvt_pk_bf16_f32 v240, v118, v119\n"
      "v_mfma_f32_16x16x32_bf16 v[150:153], v[32:35], v[92:95], v[150:153]\n"
      "v_cvt_pk_bf16_f32 v162, v162, v163\n"
      "v_cvt_pk_bf16_f32 v219, v220, v221\n"
      "v_cvt_pk_bf16_f32 v111, v112, v113\n"
      "v_cvt_pk_bf16_f32 v220, v186, v187\n"
      "v_mfma_f32_16x16x32_bf16 v[192:195], v[130:133], v[166:169], 0\n"
      "s_cmp_gt_u32 s42, s44\n"
      "s_cselect_b32 s95, s94, s95\n"
      "v_cvt_pk_bf16_f32 v88, v204, v205\n"
      "v_mfma_f32_16x16x32_bf16 v[192:195], v[76:79], v[92:95], v[192:195]\n"
      "v_exp_f32_e64 v200, v200\n"
      "v_cvt_pk_bf16_f32 v241, v120, v121\n"
      "v_mfma_f32_16x16x32_bf16 v[204:207], v[214:217], v[166:169], 0\n"
      "ds_read_b128 v[120:123], v39 offset:448\n"
      "v_cvt_pk_bf16_f32 v242, v210, v211\n"
      "v_mfma_f32_16x16x32_bf16 v[204:207], v[84:87], v[92:95], v[204:207]\n"
      "s_and_b32 s86, s86, -4\n"
      "v_cvt_pk_bf16_f32 v170, v170, v171\n"
      "v_accvgpr_write_b32 a245, v180\n"
      "v_accvgpr_write_b32 a241, v156\n"
      "v_rcp_f32 v150, v150\n"
      "v_cvt_pk_bf16_f32 v112, v146, v147\n"
      "v_pk_mul_f32 v[192:193], v[192:193], v[150:151] op_sel_hi:[1,0]\n"
      "v_cvt_pk_bf16_f32 v142, v142, v143\n"
      "v_pk_mul_f32 v[194:195], v[194:195], v[150:151] op_sel_hi:[1,0]\n"
      "v_cvt_pk_bf16_f32 v221, v188, v189\n"
      "v_pk_mul_f32 v[206:207], v[206:207], v[150:151] op_sel_hi:[1,0]\n"
      "v_pk_mul_f32 v[204:205], v[204:205], v[150:151] op_sel_hi:[1,0]\n"
      "v_mfma_f32_16x16x32_bf16 v[186:189], v[196:199], v[166:169], 0\n"
      "v_cvt_pk_bf16_f32 v136, v136, v137\n"
      "v_cvt_pk_bf16_f32 v200, v200, v200\n"
      "v_cvt_pk_bf16_f32 v178, v178, v179\n"
      "v_mfma_f32_16x16x32_bf16 v[186:189], v[80:83], v[92:95], v[186:189]\n"
      "v_cvt_pk_bf16_f32 v90, v208, 0\n"
      "v_accvgpr_write_b32 a249, v136\n"
      "v_cvt_pk_bf16_f32 v113, v148, v149\n"
      "v_mfma_f32_16x16x32_bf16 v[208:211], v[32:35], v[218:221], 0\n"
      "ds_read_b128 v[166:169], v39 offset:384\n"
      "v_cvt_pk_bf16_f32 v106, v106, v107\n"
      "v_mfma_f32_16x16x32_bf16 v[208:211], v[32:35], v[88:91], v[208:211]\n"
      "ds_write_b128 v3, v[110:113]\n"
      "v_pk_mul_f32 v[102:103], v[102:103], v[190:191] op_sel_hi:[1,0]\n"
      "v_mfma_f32_16x16x32_bf16 v[144:147], v[196:199], v[218:221], 0\n"
      "ds_read_b128 v[110:113], v39 offset:576\n"
      "v_cvt_pk_bf16_f32 v174, v174, v175\n"
      "v_cvt_pk_bf16_f32 v192, v192, v193\n"
      "v_accvgpr_write_b32 a247, v176\n"
      "v_mfma_f32_16x16x32_bf16 v[144:147], v[80:83], v[88:91], v[144:147]\n"
      "s_waitcnt lgkmcnt(3)\n"
      "v_rcp_f32_e64 v208, v208\n"
      "v_cvt_pk_bf16_f32 v243, v212, v213\n"
      "v_mfma_f32_16x16x32_bf16 v[154:157], v[130:133], v[218:221], 0\n"
      "ds_read_b128 v[136:139], v39 offset:640\n"
      "v_cvt_pk_bf16_f32 v140, v140, v141\n"
      "v_accvgpr_write_b32 a246, v174\n"
      "v_mfma_f32_16x16x32_bf16 v[154:157], v[76:79], v[88:91], v[154:157]\n"
      "ds_write_b16 v13, v200\n"
      "v_mfma_f32_16x16x32_bf16 v[174:177], v[182:185], a[228:231], v[120:123]\n"
      "ds_read_b128 v[200:203], v39 offset:512\n"
      "v_accvgpr_write_b32 a244, v178\n"
      "v_mfma_f32_16x16x32_bf16 v[178:181], v[214:217], v[218:221], 0\n"
      "v_accvgpr_write_b32 a225, v142\n"
      "v_cvt_pk_bf16_f32 v100, v100, v101\n"
      "v_mfma_f32_16x16x32_bf16 v[178:181], v[84:87], v[88:91], v[178:181]\n"
      "v_cvt_pk_bf16_f32 v160, v160, v161\n"
      "v_cvt_pk_bf16_f32 v101, v102, v103\n"
      "v_mfma_f32_16x16x32_bf16 v[210:213], v[182:185], a[252:255], v[120:123]\n"
      "s_waitcnt lgkmcnt(3)\n"
      "v_cvt_pk_bf16_f32 v134, v134, v135\n"
      "v_cvt_pk_bf16_f32 v238, v126, v127\n"
      "v_cvt_pk_bf16_f32 v176, v176, v177\n"
      "v_mfma_f32_16x16x32_bf16 v[126:129], v[96:99], a[228:231], v[166:169]\n"
      "ds_write_b64 v4, v[100:101]\n"
      "v_accvgpr_write_b32 a248, v134\n"
      "v_accvgpr_write_b32 a231, v176\n"
      "v_pk_mul_f32 v[186:187], v[186:187], v[150:151] op_sel_hi:[1,0]\n"
      "v_pk_mul_f32 v[188:189], v[188:189], v[150:151] op_sel_hi:[1,0]\n"
      "v_pk_mul_f32 v[154:155], v[154:155], v[208:209] op_sel_hi:[1,0]\n"
      "v_cvt_pk_bf16_f32 v154, v154, v155\n"
      "v_pk_mul_f32 v[180:181], v[180:181], v[208:209] op_sel_hi:[1,0]\n"
      "v_cvt_pk_bf16_f32 v174, v174, v175\n"
      "v_cvt_pk_bf16_f32 v193, v194, v195\n"
      "v_accvgpr_write_b32 a227, v162\n"
      "v_pk_mul_f32 v[178:179], v[178:179], v[208:209] op_sel_hi:[1,0]\n"
      "v_mfma_f32_16x16x32_bf16 v[162:165], v[96:99], v[244:247], v[166:169]\n"
      "ds_read_b128 v[150:153], v39 offset:704\n"
      "v_cvt_pk_bf16_f32 v128, v128, v129\n"
      "v_cvt_pk_bf16_f32 v212, v212, v213\n"
      "v_mfma_f32_16x16x32_bf16 v[102:105], v[182:185], v[244:247], v[120:123]\n"
      "s_waitcnt lgkmcnt(2)\n"
      "v_accvgpr_write_b32 a221, v124\n"
      "v_cvt_pk_bf16_f32 v194, v186, v187\n"
      "v_mfma_f32_16x16x32_bf16 v[122:125], v[96:99], a[236:239], v[136:139]\n"
      "s_add_u32 s42, s42, 1\n"
      "v_cvt_pk_bf16_f32 v195, v188, v189\n"
      "v_accvgpr_write_b32 a224, v140\n"
      "v_cvt_pk_bf16_f32 v204, v204, v205\n"
      "v_cvt_pk_bf16_f32 v205, v206, v207\n"
      "v_mfma_f32_16x16x32_bf16 v[186:189], v[96:99], a[252:255], v[166:169]\n"
      "s_cmp_ge_u32 s95, 0x40000000\n"
      "s_cselect_b64 vcc, -1, s[56:57]\n"
      "v_accvgpr_write_b32 a226, v160\n"
      "v_cvt_pk_bf16_f32 v124, v124, v125\n"
      "v_mfma_f32_16x16x32_bf16 v[166:169], v[182:185], a[232:235], v[110:113]\n"
      "ds_write_b128 v3, v[192:195] offset:256\n"
      "v_pk_mul_f32 v[156:157], v[156:157], v[208:209] op_sel_hi:[1,0]\n"
      "v_cvt_pk_bf16_f32 v210, v210, v211\n"
      "v_pk_mul_f32 v[146:147], v[146:147], v[208:209] op_sel_hi:[1,0]\n"
      "v_pk_mul_f32 v[144:145], v[144:145], v[208:209] op_sel_hi:[1,0]\n"
      "v_mfma_f32_16x16x32_bf16 v[206:209], v[96:99], v[224:227], v[200:203]\n"
      "ds_read_b128 v[218:221], v14\n"
      "v_cvt_pk_bf16_f32 v186, v186, v187\n"
      "v_cvt_pk_bf16_f32 v126, v126, v127\n"
      "v_cvt_pk_bf16_f32 v178, v178, v179\n"
      "v_mfma_f32_16x16x32_bf16 v[140:143], v[96:99], a[232:235], v[200:203]\n"
      "ds_write_b32 v31, v116 offset:4608\n"
      "v_cvt_pk_bf16_f32 v168, v168, v169\n"
      "v_cvt_pk_bf16_f32 v179, v180, v181\n"
      "v_mfma_f32_16x16x32_bf16 v[158:161], v[96:99], v[252:255], v[136:139]\n"
      "v_cvt_pk_bf16_f32 v166, v166, v167\n"
      "v_cvt_pk_bf16_f32 v247, v104, v105\n"
      "v_cvt_pk_bf16_f32 v155, v156, v157\n"
      "v_accvgpr_write_b32 a228, v126\n"
      "v_mfma_f32_16x16x32_bf16 v[190:193], v[182:185], v[224:227], v[110:113]\n"
      "ds_write_b64 v4, v[178:179] offset:512\n"
      "v_mfma_f32_16x16x32_bf16 v[178:181], v[96:99], v[248:251], v[200:203]\n"
      "s_waitcnt lgkmcnt(3)\n"
      "v_accvgpr_write_b32 a230, v174\n"
      "v_cvt_pk_bf16_f32 v244, v162, v163\n"
      "v_mfma_f32_16x16x32_bf16 v[200:203], v[182:185], a[236:239], v[150:153]\n"
      "ds_read_b128 v[174:177], v14 offset:64\n"
      "v_cvt_pk_bf16_f32 v140, v140, v141\n"
      "v_cvt_pk_bf16_f32 v122, v122, v123\n"
      "v_cvt_pk_bf16_f32 v226, v190, v191\n"
      "v_cvt_pk_bf16_f32 v188, v188, v189\n"
      "v_cvt_pk_bf16_f32 v157, v146, v147\n"
      "v_accvgpr_write_b32 a222, v106\n"
      "v_mfma_f32_16x16x32_bf16 v[146:149], v[182:185], v[228:231], v[150:153]\n"
      "ds_write_b64 v4, v[204:205] offset:256\n"
      "v_mfma_f32_16x16x32_bf16 v[118:121], v[96:99], v[228:231], v[136:139]\n"
      "v_cvt_pk_bf16_f32 v227, v192, v193\n"
      "v_cvt_pk_bf16_f32 v156, v144, v145\n"
      "v_cvt_pk_bf16_f32 v246, v102, v103\n"
      "v_mfma_f32_16x16x32_bf16 v[190:193], v[182:185], v[248:251], v[110:113]\n"
      "ds_write_b128 v3, v[154:157] offset:512\n"
      "v_cvt_pk_bf16_f32 v202, v202, v203\n"
      "v_mfma_f32_16x16x32_bf16 v[110:113], v[182:185], v[252:255], v[150:153]\n"
      "s_waitcnt lgkmcnt(2)\n"
      "v_accvgpr_write_b32 a250, v170\n"
      "v_accvgpr_write_b32 a254, v210\n"
      "v_cvt_pk_bf16_f32 v225, v208, v209\n"
      "v_cvt_pk_bf16_f32 v224, v206, v207\n"
      "v_cvt_pk_bf16_f32 v231, v148, v149\n"
      "v_accvgpr_write_b32 a252, v186\n"
      "v_accvgpr_write_b32 a251, v172\n"
      "v_cvt_pk_bf16_f32 v230, v146, v147\n"
      "v_accvgpr_write_b32 a239, v202\n"
      "v_cvt_pk_bf16_f32 v254, v110, v111\n"
      "v_accvgpr_write_b32 a236, v122\n"
      "v_cvt_pk_bf16_f32 v253, v160, v161\n"
      "v_cvt_pk_bf16_f32 v228, v118, v119\n"
      "v_cvt_pk_bf16_f32 v255, v112, v113\n"
      "v_cvt_pk_bf16_f32 v142, v142, v143\n"
      "v_cvt_pk_bf16_f32 v252, v158, v159\n"
      "v_accvgpr_write_b32 a234, v166\n"
      "v_accvgpr_write_b32 a253, v188\n"
      "v_accvgpr_write_b32 a237, v124\n"
      "v_accvgpr_write_b32 a235, v168\n"
      "v_cvt_pk_bf16_f32 v249, v180, v181\n"
      "v_accvgpr_write_b32 a223, v108\n"
      "v_cvt_pk_bf16_f32 v200, v200, v201\n"
      "v_accvgpr_write_b32 a238, v200\n"
      "v_accvgpr_write_b32 a233, v142\n"
      "v_cvt_pk_bf16_f32 v248, v178, v179\n"
      "v_cvt_pk_bf16_f32 v229, v120, v121\n"
      "v_cvt_pk_bf16_f32 v250, v190, v191\n"
      "v_accvgpr_write_b32 a255, v212\n"
      "v_mfma_f32_16x16x32_bf16 v[208:211], v[218:221], v[32:35], 0\n"
      "v_cvt_pk_bf16_f32 v245, v164, v165\n"
      "v_accvgpr_write_b32 a229, v128\n"
      "v_cvt_pk_bf16_f32 v251, v192, v193\n"
      "v_accvgpr_write_b32 a232, v140\n"
      "v_mfma_f32_16x16x32_bf16 v[208:211], v[174:177], v[32:35], v[208:211]\n"
      "s_waitcnt lgkmcnt(0)\n"
      "s_barrier\n"
      "s_or_b64 vcc, vcc, s[54:55]\n"
      "s_cbranch_vccnz L_disp1_%=\n"
      "s_nop 7\n"
      "s_nop 7\n"
      "L_loop1_%=:\n"
      "v_mfma_f32_16x16x32_bf16 v[168:171], v[218:221], v[130:133], 0\n"
      "ds_read_b128 v[164:167], v0 offset:18432\n"
      "ds_read_b128 v[178:181], v9\n"
      "v_mfma_f32_16x16x32_bf16 v[168:171], v[174:177], v[76:79], v[168:171]\n"
      "ds_read_b128 v[108:111], v0 offset:30720\n"
      "v_readlane_b32 s46, v37, s42\n"
      "v_mfma_f32_16x16x32_bf16 v[150:153], v[218:221], v[196:199], 0\n"
      "ds_read_b128 v[142:145], v1 offset:18432\n"
      "s_add_u32 m0, s64, 0\n"
      "v_mfma_f32_16x16x32_bf16 v[150:153], v[174:177], v[80:83], v[150:153]\n"
      "buffer_load_dwordx4 v16, s[16:19], s84 offen  lds\n"
      "v_mfma_f32_16x16x32_bf16 v[194:197], v[218:221], v[214:217], 0\n"
      "ds_read_b128 v[190:193], v0 offset:24576\n"
      "v_readlane_b32 s47, v38, s43\n"
      "v_mfma_f32_16x16x32_bf16 v[194:197], v[174:177], v[84:87], v[194:197]\n"
      "ds_read_b128 v[170:173], v0 offset:32768\n"
      "s_waitcnt lgkmcnt(5)\n"
      "v_mfma_f32_16x16x32_bf16 v[124:127], a[0:3], v[164:167], 0\n"
      "ds_read_b128 v[128:131], v9 offset:4608\n"
      "v_cndmask_b32 v168, v168, v150, s[58:59]\n"
      "v_mfma_f32_16x16x32_bf16 v[182:185], a[24:27], v[164:167], 0\n"
      "s_add_u32 m0, s64, 4096\n"
      "s_waitcnt lgkmcnt(5)\n"
      "v_add_f32 v180, v180, v181\n"
      "v_mfma_f32_16x16x32_bf16 v[138:141], a[48:51], v[164:167], 0\n"
      "buffer_load_dwordx4 v17, s[16:19], s84 offen  lds\n"
      "v_mfma_f32_16x16x32_bf16 v[152:155], a[72:75], v[164:167], 0\n"
      "v_add_f32 v178, v178, v179\n"
      "v_add_f32 v178, v178, v180\n"
      "v_mfma_f32_16x16x32_bf16 v[174:177], a[96:99], v[164:167], 0\n"
      "s_waitcnt lgkmcnt(2)\n"
      "v_rsq_f32 v178, v178\n"
      "v_mfma_f32_16x16x32_bf16 v[218:221], a[120:123], v[164:167], 0\n"
      "s_add_u32 m0, s64, 8192\n"
      "v_readlane_b32 s45, v37, s43\n"
      "v_mul_f32 v178, 0x415db3d7, v178\n"
      "v_mfma_f32_16x16x32_bf16 v[124:127], a[4:7], v[142:145], v[124:127]\n"
      "ds_read_b128 v[186:189], v1 offset:24576\n"
      "v_mov_b32 v180, v178\n"
      "v_mfma_f32_16x16x32_bf16 v[124:127], a[8:11], v[190:193], v[124:127]\n"
      "buffer_load_dwordx4 v18, s[16:19], s84 offen  lds\n"
      "v_mfma_f32_16x16x32_bf16 v[182:185], a[28:31], v[142:145], v[182:185]\n"
      "v_permlane32_swap_b32 v178, v180\n"
      "v_mfma_f32_16x16x32_bf16 v[182:185], a[32:35], v[190:193], v[182:185]\n"
      "ds_read_b128 v[210:213], v1 offset:30720\n"
      "v_mov_b32_e64 v179, v178\n"
      "v_mfma_f32_16x16x32_bf16 v[138:141], a[52:55], v[142:145], v[138:141]\n"
      "s_add_u32 m0, s64, 12288\n"
      "v_permlane16_swap_b32 v178, v179\n"
      "v_mfma_f32_16x16x32_bf16 v[138:141], a[56:59], v[190:193], v[138:141]\n"
      "v_mul_f32 v200, v179, v200\n"
      "v_mul_f32_e64 v201, v179, v201\n"
      "v_mfma_f32_16x16x32_bf16 v[152:155], a[76:79], v[142:145], v[152:155]\n"
      "buffer_load_dwordx4 v19, s[16:19], s84 offen  lds\n"
      "v_mfma_f32_16x16x32_bf16 v[152:155], a[80:83], v[190:193], v[152:155]\n"
      "v_mul_f32 v202, v179, v202\n"
      "v_mul_f32 v115, v179, v115\n"
      "v_mfma_f32_16x16x32_bf16 v[174:177], a[100:103], v[142:145], v[174:177]\n"
      "ds_read_b128 v[104:107], v1 offset:26624\n"
      "v_mul_f32_e64 v135, v178, v135\n"
      "v_mfma_f32_16x16x32_bf16 v[174:177], a[104:107], v[190:193], v[174:177]\n"
      "s_add_u32 m0, s65, 16384\n"
      "v_mul_f32 v205, v179, v205\n"
      "v_mul_f32 v137, v178, v137\n"
      "v_mfma_f32_16x16x32_bf16 v[218:221], a[124:127], v[142:145], v[218:221]\n"
      "s_or_b32 s95, s45, s46\n"
      "v_mul_f32 v163, v180, v163\n"
      "v_mul_f32 v114, v179, v114\n"
      "v_mfma_f32_16x16x32_bf16 v[218:221], a[128:131], v[190:193], v[218:221]\n"
      "s_waitcnt lgkmcnt(1)\n"
      "v_mul_f32 v158, v180, v158\n"
      "v_mul_f32 v203, v179, v203\n"
      "v_mfma_f32_16x16x32_bf16 v[124:127], a[12:15], v[186:189], v[124:127]\n"
      "v_mul_f32 v134, v178, v134\n"
      "v_mul_f32 v117, v178, v117\n"
      "v_mfma_f32_16x16x32_bf16 v[124:127], a[16:19], v[108:111], v[124:127]\n"
      "buffer_load_dwordx4 v15, s[16:19], s84 offen  lds\n"
      "v_mfma_f32_16x16x32_bf16 v[124:127], a[20:23], v[210:213], v[124:127]\n"
      "v_mul_f32 v161, v180, v161\n"
      "v_mul_f32 v97, v178, v97\n"
      "v_mfma_f32_16x16x32_bf16 v[182:185], a[36:39], v[186:189], v[182:185]\n"
      "v_mul_f32 v96, v178, v96\n"
      "v_mul_f32 v116, v178, v116\n"
      "v_mfma_f32_16x16x32_bf16 v[182:185], a[40:43], v[108:111], v[182:185]\n"
      "v_mul_f32 v147, v180, v147\n"
      "v_mul_f32 v207, v179, v207\n"
      "v_mfma_f32_16x16x32_bf16 v[182:185], a[44:47], v[210:213], v[182:185]\n"
      "v_mul_f32_e64 v99, v178, v99\n"
      "v_cvt_pk_bf16_f32 v96, v96, v97\n"
      "v_mfma_f32_16x16x32_bf16 v[138:141], a[60:63], v[186:189], v[138:141]\n"
      "ds_read_b128 v[120:123], v1 offset:20480\n"
      "v_or3_b32 v128, v128, v129, v130\n"
      "v_mfma_f32_16x16x32_bf16 v[138:141], a[64:67], v[108:111], v[138:141]\n"
      "v_mul_f32 v206, v179, v206\n"
      "v_mul_f32 v157, v180, v157\n"
      "v_mfma_f32_16x16x32_bf16 v[138:141], a[68:71], v[210:213], v[138:141]\n"
      "buffer_load_dwordx4 v[100:103], v36, s[28:31], s86 offen\n"
      "v_mfma_f32_16x16x32_bf16 v[152:155], a[84:87], v[186:189], v[152:155]\n"
      "s_or_b32 s95, s95, s48\n"
      "v_mul_f32 v98, v178, v98\n"
      "v_fma_f32 v150, v124, v124, s51\n"
      "v_mfma_f32_16x16x32_bf16 v[152:155], a[88:91], v[108:111], v[152:155]\n"
      "v_cvt_pk_bf16_f32 v134, v134, v135\n"
      "v_mul_f32 v204, v179, v204\n"
      "v_mfma_f32_16x16x32_bf16 v[152:155], a[92:95], v[210:213], v[152:155]\n"
      "v_fmac_f32 v150, v125, v125\n"
      "v_mul_f32 v146, v180, v146\n"
      "v_mfma_f32_16x16x32_bf16 v[174:177], a[108:111], v[186:189], v[174:177]\n"
      "v_mul_f32 v119, v178, v119\n"
      "v_cvt_pk_bf16_f32 v97, v98, v99\n"
      "v_mfma_f32_16x16x32_bf16 v[174:177], a[112:115], v[108:111], v[174:177]\n"
      "v_mul_f32_e64 v113, v179, v113\n"
      "v_cvt_pk_bf16_f32 v204, v204, v205\n"
      "v_mfma_f32_16x16x32_bf16 v[174:177], a[116:119], v[210:213], v[174:177]\n"
      "buffer_load_dword v86, v29, s[28:31], s86 offen\n"
      "v_mfma_f32_16x16x32_bf16 v[218:221], a[132:135], v[186:189], v[218:221]\n"
      "v_cvt_pk_bf16_f32 v200, v200, v201\n"
      "v_mul_f32_e64 v136, v178, v136\n"
      "v_mfma_f32_16x16x32_bf16 v[218:221], a[136:139], v[108:111], v[218:221]\n"
      "v_bitop3_b32 v128, v128, v131, s33 bitop3:0xa8\n"
      "v_fma_f32 v151, v152, v152, s51\n"
      "v_mfma_f32_16x16x32_bf16 v[218:221], a[140:143], v[210:213], v[218:221]\n"
      "v_cvt_pk_bf16_f32 v98, v116, v117\n"
      "v_fmac_f32_e64 v150, v126, v126\n"
      "v_mfma_f32_16x16x32_bf16 v[130:133], v[164:167], a[144:147], 0\n"
      "v_cvt_pk_bf16_f32 v205, v206, v207\n"
      "v_fmac_f32 v151, v153, v153\n"
      "v_mfma_f32_16x16x32_bf16 v[130:133], v[142:145], a[148:151], v[130:133]\n"
      "v_mul_f32 v118, v178, v118\n"
      "v_cvt_pk_bf16_f32 v99, v118, v119\n"
      "v_mfma_f32_16x16x32_bf16 v[130:133], v[190:193], a[152:155], v[130:133]\n"
      "ds_read_b128 v[116:119], v0 offset:20480\n"
      "v_cvt_pk_bf16_f32 v201, v202, v203\n"
      "v_mfma_f32_16x16x32_bf16 v[130:133], v[186:189], a[156:159], v[130:133]\n"
      "ds_write_b128 v5, v[96:99] offset:18432\n"
      "v_mfma_f32_16x16x32_bf16 v[130:133], v[108:111], a[160:163], v[130:133]\n"
      "v_mul_f32 v112, v179, v112\n"
      "v_fmac_f32 v151, v154, v154\n"
      "v_mfma_f32_16x16x32_bf16 v[130:133], v[210:213], a[164:167], v[130:133]\n"
      "v_cvt_pk_bf16_f32 v202, v112, v113\n"
      "v_fmac_f32 v151, v155, v155\n"
      "v_mfma_f32_16x16x32_bf16 v[196:199], v[164:167], a[168:171], 0\n"
      "ds_read_b128 v[96:99], v0 offset:26624\n"
      "v_fmac_f32 v151, v174, v174\n"
      "v_mfma_f32_16x16x32_bf16 v[196:199], v[142:145], a[172:175], v[196:199]\n"
      "v_cvt_pk_bf16_f32 v203, v114, v115\n"
      "v_fmac_f32_e64 v151, v175, v175\n"
      "v_mfma_f32_16x16x32_bf16 v[196:199], v[190:193], a[176:179], v[196:199]\n"
      "ds_write_b128 v5, v[200:203] offset:24576\n"
      "v_mfma_f32_16x16x32_bf16 v[196:199], v[186:189], a[180:183], v[196:199]\n"
      "v_mul_f32 v160, v180, v160\n"
      "v_fmac_f32 v150, v127, v127\n"
      "v_mfma_f32_16x16x32_bf16 v[196:199], v[108:111], a[184:187], v[196:199]\n"
      "v_fmac_f32 v151, v176, v176\n"
      "v_fmac_f32 v150, v182, v182\n"
      "v_mfma_f32_16x16x32_bf16 v[196:199], v[210:213], a[188:191], v[196:199]\n"
      "v_cvt_pk_bf16_f32 v130, v130, v131\n"
      "v_mul_f32_e64 v162, v180, v162\n"
      "v_mfma_f32_16x16x32_bf16 v[214:217], v[164:167], a[192:195], 0\n"
      "v_fmac_f32 v151, v177, v177\n"
      "v_cvt_pk_bf16_f32 v160, v160, v161\n"
      "v_mfma_f32_16x16x32_bf16 v[214:217], v[142:145], a[196:199], v[214:217]\n"
      "v_mul_f32 v149, v180, v149\n"
      "v_cndmask_b32 v168, v168, v194, s[60:61]\n"
      "v_mfma_f32_16x16x32_bf16 v[214:217], v[190:193], a[200:203], v[214:217]\n"
      "ds_read_b128 v[192:195], v0 offset:22528\n"
      "v_mul_f32_e64 v156, v180, v156\n"
      "v_mfma_f32_16x16x32_bf16 v[214:217], v[186:189], a[204:207], v[214:217]\n"
      "ds_read_b128 v[112:115], v1 offset:32768\n"
      "v_cvt_pk_bf16_f32 v156, v156, v157\n"
      "v_mfma_f32_16x16x32_bf16 v[214:217], v[108:111], a[208:211], v[214:217]\n"
      "ds_read_b128 v[200:203], v0 offset:28672\n"
      "v_mul_f32 v159, v180, v159\n"
      "v_mfma_f32_16x16x32_bf16 v[214:217], v[210:213], a[212:215], v[214:217]\n"
      "s_waitcnt lgkmcnt(3)\n"
      "v_fmac_f32 v151, v218, v218\n"
      "v_fmac_f32 v150, v183, v183\n"
      "v_mfma_f32_16x16x32_bf16 v[210:213], a[0:3], v[116:119], 0\n"
      "v_cvt_pk_bf16_f32 v135, v136, v137\n"
      "v_fmac_f32 v150, v184, v184\n"
      "v_mfma_f32_16x16x32_bf16 v[210:213], a[4:7], v[120:123], v[210:213]\n"
      "v_fmac_f32 v150, v185, v185\n"
      "v_cvt_pk_bf16_f32 v131, v132, v133\n"
      "v_mfma_f32_16x16x32_bf16 v[210:213], a[8:11], v[96:99], v[210:213]\n"
      "ds_write_b64 v6, v[204:205] offset:24576\n"
      "v_mfma_f32_16x16x32_bf16 v[210:213], a[12:15], v[104:107], v[210:213]\n"
      "ds_read_b128 v[186:189], v1 offset:22528\n"
      "v_cvt_pk_bf16_f32 v157, v158, v159\n"
      "v_mfma_f32_16x16x32_bf16 v[210:213], a[16:19], v[170:173], v[210:213]\n"
      "s_waitcnt lgkmcnt(2)\n"
      "v_fmac_f32 v150, v138, v138\n"
      "v_cvt_pk_bf16_f32 v158, v146, v147\n"
      "v_mfma_f32_16x16x32_bf16 v[210:213], a[20:23], v[112:115], v[210:213]\n"
      "ds_write_b64 v6, v[134:135] offset:18432\n"
      "v_mfma_f32_16x16x32_bf16 v[142:145], a[24:27], v[116:119], 0\n"
      "s_cmp_eq_u32 s43, 21\n"
      "s_cselect_b32 s95, -1, s95\n"
      "v_fmac_f32 v151, v219, v219\n"
      "v_mfma_f32_16x16x32_bf16 v[142:145], a[28:31], v[120:123], v[142:145]\n"
      "v_fmac_f32 v151, v220, v220\n"
      "v_fmac_f32_e64 v150, v139, v139\n"
      "v_mfma_f32_16x16x32_bf16 v[142:145], a[32:35], v[96:99], v[142:145]\n"
      "v_fmac_f32 v151, v221, v221\n"
      "v_fmac_f32 v150, v140, v140\n"
      "v_mfma_f32_16x16x32_bf16 v[142:145], a[36:39], v[104:107], v[142:145]\n"
      "s_cmp_ge_u32 s95, 0x40000000\n"
      "s_cselect_b32 s94, -1, -2\n"
      "v_fmac_f32_e64 v150, v141, v141\n"
      "v_mfma_f32_16x16x32_bf16 v[142:145], a[40:43], v[170:173], v[142:145]\n"
      "v_mov_b32_dpp v208, v208 quad_perm:[0,0,0,0] row_mask:0xf bank_mask:0xf\n"
      "v_fma_f32 v129, v210, v210, s51\n"
      "v_mfma_f32_16x16x32_bf16 v[142:145], a[44:47], v[112:115], v[142:145]\n"
      "v_permlane16_swap_b32 v150, v151\n"
      "v_mfma_f32_16x16x32_bf16 v[108:111], a[48:51], v[116:119], 0\n"
      "ds_read_b128 v[134:137], v1 offset:28672\n"
      "v_fmac_f32 v129, v211, v211\n"
      "v_mfma_f32_16x16x32_bf16 v[108:111], a[52:55], v[120:123], v[108:111]\n"
      "v_fmac_f32 v129, v212, v212\n"
      "v_add_f32 v150, v150, v151\n"
      "v_mfma_f32_16x16x32_bf16 v[108:111], a[56:59], v[96:99], v[108:111]\n"
      "v_mul_f32 v148, v180, v148\n"
      "v_mov_b32 v151, v150\n"
      "v_mfma_f32_16x16x32_bf16 v[108:111], a[60:63], v[104:107], v[108:111]\n"
      "v_cvt_pk_bf16_f32 v214, v214, v215\n"
      "v_cvt_pk_bf16_f32 v159, v148, v149\n"
      "v_mfma_f32_16x16x32_bf16 v[108:111], a[64:67], v[170:173], v[108:111]\n"
      "v_permlane32_swap_b32 v150, v151\n"
      "v_mfma_f32_16x16x32_bf16 v[108:111], a[68:71], v[112:115], v[108:111]\n"
      "v_fmac_f32 v129, v213, v213\n"
      "v_add_f32 v150, v150, v151\n"
      "v_mfma_f32_16x16x32_bf16 v[178:181], a[72:75], v[116:119], 0\n"
      "v_rsq_f32 v150, v150\n"
      "v_mfma_f32_16x16x32_bf16 v[178:181], a[76:79], v[120:123], v[178:181]\n"
      "v_mov_b32 v151, v150\n"
      "v_fmac_f32 v129, v142, v142\n"
      "v_mfma_f32_16x16x32_bf16 v[178:181], a[80:83], v[96:99], v[178:181]\n"
      "v_permlane16_swap_b32_e64 v150, v151\n"
      "v_mfma_f32_16x16x32_bf16 v[178:181], a[84:87], v[104:107], v[178:181]\n"
      "v_cvt_pk_bf16_f32 v161, v162, v163\n"
      "v_cvt_pk_bf16_f32 v196, v196, v197\n"
      "v_mfma_f32_16x16x32_bf16 v[178:181], a[88:91], v[170:173], v[178:181]\n"
      "v_rcp_f32 v208, v208\n"
      "v_mfma_f32_16x16x32_bf16 v[178:181], a[92:95], v[112:115], v[178:181]\n"
      "v_mul_f32 v168, v208, v168\n"
      "v_mul_f32_e64 v150, s50, v150\n"
      "v_mfma_f32_16x16x32_bf16 v[206:209], a[96:99], v[116:119], 0\n"
      "ds_write_b64 v6, v[160:161] offset:30720\n"
      "v_mfma_f32_16x16x32_bf16 v[206:209], a[100:103], v[120:123], v[206:209]\n"
      "v_fmac_f32_e64 v129, v143, v143\n"
      "v_cvt_pk_bf16_f32 v215, v216, v217\n"
      "v_mfma_f32_16x16x32_bf16 v[206:209], a[104:107], v[96:99], v[206:209]\n"
      "v_fmac_f32 v129, v144, v144\n"
      "v_mul_f32 v138, v150, v138\n"
      "v_mfma_f32_16x16x32_bf16 v[206:209], a[108:111], v[104:107], v[206:209]\n"
      "v_fma_f32 v204, v178, v178, s51\n"
      "v_cvt_pk_bf16_f32 v168, v168, v168\n"
      "v_mfma_f32_16x16x32_bf16 v[206:209], a[112:115], v[170:173], v[206:209]\n"
      "v_cmp_ne_u32 s[56:57], 0, v128\n"
      "v_fmac_f32_e64 v129, v145, v145\n"
      "v_mfma_f32_16x16x32_bf16 v[206:209], a[116:119], v[112:115], v[206:209]\n"
      "ds_write_b128 v5, v[156:159] offset:30720\n"
      "v_mfma_f32_16x16x32_bf16 v[156:159], a[120:123], v[116:119], 0\n"
      "v_mul_f32 v220, v151, v220\n"
      "v_fmac_f32 v204, v179, v179\n"
      "v_mfma_f32_16x16x32_bf16 v[156:159], a[124:127], v[120:123], v[156:159]\n"
      "v_cvt_pk_bf16_f32 v197, v198, v199\n"
      "v_mul_f32 v219, v151, v219\n"
      "v_mfma_f32_16x16x32_bf16 v[156:159], a[128:131], v[96:99], v[156:159]\n"
      "v_fmac_f32 v204, v180, v180\n"
      "v_mul_f32 v139, v150, v139\n"
      "v_mfma_f32_16x16x32_bf16 v[156:159], a[132:135], v[104:107], v[156:159]\n"
      "v_mul_f32 v184, v150, v184\n"
      "v_mul_f32 v124, v150, v124\n"
      "v_mfma_f32_16x16x32_bf16 v[156:159], a[136:139], v[170:173], v[156:159]\n"
      "v_mul_f32 v218, v151, v218\n"
      "v_cvt_pk_bf16_f32 v138, v138, v139\n"
      "v_mfma_f32_16x16x32_bf16 v[156:159], a[140:143], v[112:115], v[156:159]\n"
      "v_mul_f32 v141, v150, v141\n"
      "v_mul_f32 v154, v151, v154\n"
      "v_mfma_f32_16x16x32_bf16 v[146:149], v[116:119], a[144:147], 0\n"
      "v_mul_f32 v221, v151, v221\n"
      "v_fmac_f32 v204, v181, v181\n"
      "v_mfma_f32_16x16x32_bf16 v[146:149], v[120:123], a[148:151], v[146:149]\n"
      "ds_write_b16 v12, v168\n"
      "v_mfma_f32_16x16x32_bf16 v[146:149], v[96:99], a[152:155], v[146:149]\n"
      "v_mul_f32 v126, v150, v126\n"
      "v_fmac_f32 v129, v108, v108\n"
      "v_mfma_f32_16x16x32_bf16 v[146:149], v[104:107], a[156:159], v[146:149]\n"
      "v_mul_f32 v125, v150, v125\n"
      "v_fmac_f32 v204, v206, v206\n"
      "v_mfma_f32_16x16x32_bf16 v[146:149], v[170:173], a[160:163], v[146:149]\n"
      "v_fmac_f32 v129, v109, v109\n"
      "v_fmac_f32 v204, v207, v207\n"
      "v_mfma_f32_16x16x32_bf16 v[146:149], v[112:115], a[164:167], v[146:149]\n"
      "v_fmac_f32 v204, v208, v208\n"
      "v_mul_f32 v153, v151, v153\n"
      "v_mfma_f32_16x16x32_bf16 v[166:169], v[116:119], a[168:171], 0\n"
      "v_mul_f32 v127, v150, v127\n"
      "v_mul_f32 v152, v151, v152\n"
      "v_mfma_f32_16x16x32_bf16 v[166:169], v[120:123], a[172:175], v[166:169]\n"
      "v_cvt_pk_bf16_f32 v218, v218, v219\n"
      "v_fmac_f32_e64 v204, v209, v209\n"
      "v_mfma_f32_16x16x32_bf16 v[166:169], v[96:99], a[176:179], v[166:169]\n"
      "v_fmac_f32 v129, v110, v110\n"
      "v_fmac_f32 v204, v156, v156\n"
      "v_mfma_f32_16x16x32_bf16 v[166:169], v[104:107], a[180:183], v[166:169]\n"
      "v_mul_f32 v140, v150, v140\n"
      "v_mul_f32 v155, v151, v155\n"
      "v_mfma_f32_16x16x32_bf16 v[166:169], v[170:173], a[184:187], v[166:169]\n"
      "v_mul_f32 v182, v150, v182\n"
      "v_fmac_f32 v204, v157, v157\n"
      "v_mfma_f32_16x16x32_bf16 v[166:169], v[112:115], a[188:191], v[166:169]\n"
      "v_cvt_pk_bf16_f32 v219, v220, v221\n"
      "v_mul_f32 v175, v151, v175\n"
      "v_mfma_f32_16x16x32_bf16 v[160:163], v[116:119], a[192:195], 0\n"
      "ds_read_b128 v[220:223], v2 offset:6144\n"
      "v_mul_f32 v174, v151, v174\n"
      "v_mfma_f32_16x16x32_bf16 v[160:163], v[120:123], a[196:199], v[160:163]\n"
      "ds_read_b128 v[120:123], v1 offset:34816\n"
      "v_fmac_f32 v129, v111, v111\n"
      "v_mfma_f32_16x16x32_bf16 v[160:163], v[96:99], a[200:203], v[160:163]\n"
      "v_fmac_f32 v204, v158, v158\n"
      "v_fmac_f32 v204, v159, v159\n"
      "v_mfma_f32_16x16x32_bf16 v[160:163], v[104:107], a[204:207], v[160:163]\n"
      "ds_read_b128 v[116:119], v0 offset:34816\n"
      "v_cvt_pk_bf16_f32 v124, v124, v125\n"
      "v_mfma_f32_16x16x32_bf16 v[160:163], v[170:173], a[208:211], v[160:163]\n"
      "v_permlane16_swap_b32 v129, v204\n"
      "v_mul_f32 v185, v150, v185\n"
      "v_mfma_f32_16x16x32_bf16 v[160:163], v[112:115], a[212:215], v[160:163]\n"
      "s_waitcnt lgkmcnt(3)\n"
      "v_add_f32 v129, v129, v204\n"
      "v_mov_b32 v204, v129\n"
      "v_mfma_f32_16x16x32_bf16 v[170:173], a[0:3], v[192:195], 0\n"
      "s_nop 0\n"
      "v_permlane32_swap_b32 v129, v204\n"
      "v_mfma_f32_16x16x32_bf16 v[170:173], a[4:7], v[186:189], v[170:173]\n"
      "v_cvt_pk_bf16_f32 v152, v152, v153\n"
      "v_cvt_pk_bf16_f32 v125, v126, v127\n"
      "v_mfma_f32_16x16x32_bf16 v[170:173], a[8:11], v[200:203], v[170:173]\n"
      "v_add_f32 v129, v129, v204\n"
      "v_cvt_pk_bf16_f32 v198, v166, v167\n"
      "v_mfma_f32_16x16x32_bf16 v[170:173], a[12:15], v[134:137], v[170:173]\n"
      "s_waitcnt lgkmcnt(0)\n"
      "v_rsq_f32 v129, v129\n"
      "v_mfma_f32_16x16x32_bf16 v[170:173], a[16:19], v[116:119], v[170:173]\n"
      "v_mov_b32 v204, v129\n"
      "v_mul_f32 v183, v150, v183\n"
      "v_mfma_f32_16x16x32_bf16 v[170:173], a[20:23], v[120:123], v[170:173]\n"
      "v_mul_f32 v176, v151, v176\n"
      "v_cvt_pk_bf16_f32 v153, v154, v155\n"
      "v_mfma_f32_16x16x32_bf16 v[112:115], a[24:27], v[192:195], 0\n"
      "v_permlane16_swap_b32_e64 v129, v204\n"
      "v_mfma_f32_16x16x32_bf16 v[112:115], a[28:31], v[186:189], v[112:115]\n"
      "v_cvt_pk_bf16_f32 v127, v184, v185\n"
      "v_mul_f32 v129, s50, v129\n"
      "v_mfma_f32_16x16x32_bf16 v[112:115], a[32:35], v[200:203], v[112:115]\n"
      "v_mul_f32 v177, v151, v177\n"
      "v_mul_f32_e64 v157, v204, v157\n"
      "v_mfma_f32_16x16x32_bf16 v[112:115], a[36:39], v[134:137], v[112:115]\n"
      "v_mul_f32 v212, v129, v212\n"
      "v_fma_f32 v81, v170, v170, s51\n"
      "v_mfma_f32_16x16x32_bf16 v[112:115], a[40:43], v[116:119], v[112:115]\n"
      "v_mul_f32 v109, v129, v109\n"
      "v_cvt_pk_bf16_f32 v139, v140, v141\n"
      "v_mfma_f32_16x16x32_bf16 v[112:115], a[44:47], v[120:123], v[112:115]\n"
      "v_fmac_f32 v81, v171, v171\n"
      "v_fmac_f32 v81, v172, v172\n"
      "v_mfma_f32_16x16x32_bf16 v[104:107], a[48:51], v[192:195], 0\n"
      "v_cvt_pk_bf16_f32 v217, v162, v163\n"
      "v_mul_f32 v159, v204, v159\n"
      "v_mfma_f32_16x16x32_bf16 v[104:107], a[52:55], v[186:189], v[104:107]\n"
      "v_cvt_pk_bf16_f32 v216, v160, v161\n"
      "v_mul_f32 v179, v204, v179\n"
      "v_mfma_f32_16x16x32_bf16 v[104:107], a[56:59], v[200:203], v[104:107]\n"
      "v_mul_f32 v206, v204, v206\n"
      "v_mul_f32 v213, v129, v213\n"
      "v_mfma_f32_16x16x32_bf16 v[104:107], a[60:63], v[134:137], v[104:107]\n"
      "v_fmac_f32_e64 v81, v173, v173\n"
      "v_cvt_pk_bf16_f32 v154, v174, v175\n"
      "v_mfma_f32_16x16x32_bf16 v[104:107], a[64:67], v[116:119], v[104:107]\n"
      "v_mul_f32 v181, v204, v181\n"
      "v_mul_f32 v145, v129, v145\n"
      "v_mfma_f32_16x16x32_bf16 v[104:107], a[68:71], v[120:123], v[104:107]\n"
      "v_mul_f32 v207, v204, v207\n"
      "v_mul_f32 v211, v129, v211\n"
      "v_mfma_f32_16x16x32_bf16 v[162:165], a[72:75], v[192:195], 0\n"
      "v_cvt_pk_bf16_f32 v199, v168, v169\n"
      "v_fmac_f32_e64 v81, v112, v112\n"
      "v_mfma_f32_16x16x32_bf16 v[162:165], a[76:79], v[186:189], v[162:165]\n"
      "v_mul_f32 v180, v204, v180\n"
      "v_fmac_f32 v81, v113, v113\n"
      "v_mfma_f32_16x16x32_bf16 v[162:165], a[80:83], v[200:203], v[162:165]\n"
      "v_fmac_f32 v81, v114, v114\n"
      "v_mul_f32 v156, v204, v156\n"
      "v_mfma_f32_16x16x32_bf16 v[162:165], a[84:87], v[134:137], v[162:165]\n"
      "v_mul_f32 v208, v204, v208\n"
      "v_fmac_f32 v81, v115, v115\n"
      "v_mfma_f32_16x16x32_bf16 v[162:165], a[88:91], v[116:119], v[162:165]\n"
      "v_mul_f32 v158, v204, v158\n"
      "v_mul_f32 v144, v129, v144\n"
      "v_mfma_f32_16x16x32_bf16 v[162:165], a[92:95], v[120:123], v[162:165]\n"
      "v_mul_f32 v210, v129, v210\n"
      "v_cvt_pk_bf16_f32 v210, v210, v211\n"
      "v_mfma_f32_16x16x32_bf16 v[166:169], a[96:99], v[192:195], 0\n"
      "v_fmac_f32 v81, v104, v104\n"
      "v_mul_f32_e64 v111, v129, v111\n"
      "v_mfma_f32_16x16x32_bf16 v[166:169], a[100:103], v[186:189], v[166:169]\n"
      "v_cvt_pk_bf16_f32 v211, v212, v213\n"
      "v_cvt_pk_bf16_f32 v126, v182, v183\n"
      "v_mfma_f32_16x16x32_bf16 v[166:169], a[104:107], v[200:203], v[166:169]\n"
      "v_fma_f32 v84, v162, v162, s51\n"
      "v_fmac_f32 v81, v105, v105\n"
      "v_mfma_f32_16x16x32_bf16 v[166:169], a[108:111], v[134:137], v[166:169]\n"
      "v_fmac_f32 v84, v163, v163\n"
      "v_mul_f32 v178, v204, v178\n"
      "v_mfma_f32_16x16x32_bf16 v[166:169], a[112:115], v[116:119], v[166:169]\n"
      "v_mul_f32 v143, v129, v143\n"
      "v_mul_f32 v142, v129, v142\n"
      "v_mfma_f32_16x16x32_bf16 v[166:169], a[116:119], v[120:123], v[166:169]\n"
      "v_fmac_f32 v81, v106, v106\n"
      "v_fmac_f32_e64 v84, v164, v164\n"
      "v_mfma_f32_16x16x32_bf16 v[182:185], a[120:123], v[192:195], 0\n"
      "v_cvt_pk_bf16_f32 v178, v178, v179\n"
      "v_cvt_pk_bf16_f32 v156, v156, v157\n"
      "v_mfma_f32_16x16x32_bf16 v[182:185], a[124:127], v[186:189], v[182:185]\n"
      "v_cvt_pk_bf16_f32 v179, v180, v181\n"
      "v_cvt_pk_bf16_f32 v133, v148, v149\n"
      "v_mfma_f32_16x16x32_bf16 v[182:185], a[128:131], v[200:203], v[182:185]\n"
      "v_cvt_pk_bf16_f32 v155, v176, v177\n"
      "v_fmac_f32 v84, v165, v165\n"
      "v_mfma_f32_16x16x32_bf16 v[182:185], a[132:135], v[134:137], v[182:185]\n"
      "v_fmac_f32 v84, v166, v166\n"
      "v_fmac_f32_e64 v81, v107, v107\n"
      "v_mfma_f32_16x16x32_bf16 v[182:185], a[136:139], v[116:119], v[182:185]\n"
      "v_cvt_pk_bf16_f32 v213, v144, v145\n"
      "v_cvt_pk_bf16_f32 v132, v146, v147\n"
      "v_mfma_f32_16x16x32_bf16 v[182:185], a[140:143], v[120:123], v[182:185]\n"
      "v_fmac_f32 v84, v167, v167\n"
      "v_fmac_f32 v84, v168, v168\n"
      "v_mfma_f32_16x16x32_bf16 v[144:147], v[192:195], a[144:147], 0\n"
      "s_cmp_gt_u32 s42, s44\n"
      "s_cselect_b32 s95, s94, s95\n"
      "v_fmac_f32_e64 v84, v169, v169\n"
      "v_mfma_f32_16x16x32_bf16 v[144:147], v[186:189], a[148:151], v[144:147]\n"
      "s_nop 0\n"
      "v_fmac_f32 v84, v182, v182\n"
      "v_fmac_f32 v84, v183, v183\n"
      "v_mfma_f32_16x16x32_bf16 v[144:147], v[200:203], a[152:155], v[144:147]\n"
      "s_add_u32 s42, s42, 1\n"
      "v_fmac_f32 v84, v184, v184\n"
      "v_fmac_f32 v84, v185, v185\n"
      "v_mfma_f32_16x16x32_bf16 v[144:147], v[134:137], a[156:159], v[144:147]\n"
      "s_nop 0\n"
      "v_permlane16_swap_b32 v81, v84\n"
      "v_mfma_f32_16x16x32_bf16 v[144:147], v[116:119], a[160:163], v[144:147]\n"
      "v_add_f32 v81, v81, v84\n"
      "v_cvt_pk_bf16_f32 v157, v158, v159\n"
      "v_mfma_f32_16x16x32_bf16 v[144:147], v[120:123], a[164:167], v[144:147]\n"
      "v_cvt_pk_bf16_f32 v212, v142, v143\n"
      "v_mov_b32 v84, v81\n"
      "v_mfma_f32_16x16x32_bf16 v[148:151], v[192:195], a[168:171], 0\n"
      "s_nop 0\n"
      "v_permlane32_swap_b32 v81, v84\n"
      "v_mfma_f32_16x16x32_bf16 v[148:151], v[186:189], a[172:175], v[148:151]\n"
      "v_cvt_pk_bf16_f32 v180, v206, v207\n"
      "v_add_f32 v81, v81, v84\n"
      "v_mfma_f32_16x16x32_bf16 v[148:151], v[200:203], a[176:179], v[148:151]\n"
      "v_rsq_f32 v81, v81\n"
      "v_mfma_f32_16x16x32_bf16 v[148:151], v[134:137], a[180:183], v[148:151]\n"
      "ds_read_b128 v[174:177], v2\n"
      "v_mov_b32 v84, v81\n"
      "v_mfma_f32_16x16x32_bf16 v[148:151], v[116:119], a[184:187], v[148:151]\n"
      "s_nop 0\n"
      "v_permlane16_swap_b32 v81, v84\n"
      "v_mfma_f32_16x16x32_bf16 v[148:151], v[120:123], a[188:191], v[148:151]\n"
      "v_mul_f32 v162, v84, v162\n"
      "v_mul_f32_e64 v81, s50, v81\n"
      "v_mfma_f32_16x16x32_bf16 v[158:161], v[192:195], a[192:195], 0\n"
      "ds_read_b128 v[190:193], v2 offset:3072\n"
      "v_cvt_pk_bf16_f32 v77, v146, v147\n"
      "v_mfma_f32_16x16x32_bf16 v[158:161], v[186:189], a[196:199], v[158:161]\n"
      "ds_read_u16 v82, v11 offset:1200\n"
      "v_mul_f32_e64 v112, v81, v112\n"
      "v_mfma_f32_16x16x32_bf16 v[158:161], v[200:203], a[200:203], v[158:161]\n"
      "ds_read_b128 v[140:143], v2 offset:256\n"
      "v_mul_f32 v164, v84, v164\n"
      "v_mfma_f32_16x16x32_bf16 v[158:161], v[134:137], a[204:207], v[158:161]\n"
      "ds_read_b128 v[200:203], v2 offset:15360\n"
      "v_mul_f32 v108, v129, v108\n"
      "v_mfma_f32_16x16x32_bf16 v[158:161], v[116:119], a[208:211], v[158:161]\n"
      "v_mul_f32 v163, v84, v163\n"
      "v_mul_f32 v184, v84, v184\n"
      "v_mfma_f32_16x16x32_bf16 v[158:161], v[120:123], a[212:215], v[158:161]\n"
      "s_waitcnt lgkmcnt(4)\n"
      "v_mul_f32 v209, v204, v209\n"
      "v_mul_f32 v104, v81, v104\n"
      "v_mfma_f32_16x16x32_bf16 v[96:99], a[216:219], v[174:177], 0\n"
      "s_waitcnt lgkmcnt(3)\n"
      "v_mul_f32 v185, v84, v185\n"
      "v_mul_f32 v114, v81, v114\n"
      "v_mfma_f32_16x16x32_bf16 v[96:99], a[220:223], v[190:193], v[96:99]\n"
      "ds_read_b128 v[204:207], v2 offset:9216\n"
      "v_mul_f32_e64 v183, v84, v183\n"
      "v_mfma_f32_16x16x32_bf16 v[96:99], a[224:227], v[220:223], v[96:99]\n"
      "ds_read_b128 v[186:189], v2 offset:12288\n"
      "v_cvt_pk_bf16_f32 v181, v208, v209\n"
      "v_mfma_f32_16x16x32_bf16 v[116:119], a[240:243], v[174:177], 0\n"
      "v_mul_f32_e64 v165, v84, v165\n"
      "v_cvt_pk_bf16_f32 v162, v162, v163\n"
      "v_mfma_f32_16x16x32_bf16 v[116:119], a[244:247], v[190:193], v[116:119]\n"
      "v_mul_f32 v171, v81, v171\n"
      "v_mul_f32 v167, v84, v167\n"
      "v_mfma_f32_16x16x32_bf16 v[116:119], a[248:251], v[220:223], v[116:119]\n"
      "v_mul_f32 v168, v84, v168\n"
      "v_mul_f32 v182, v84, v182\n"
      "v_mfma_f32_16x16x32_bf16 v[134:137], v[232:235], v[174:177], 0\n"
      "v_mul_f32 v110, v129, v110\n"
      "v_mul_f32 v169, v84, v169\n"
      "v_mfma_f32_16x16x32_bf16 v[134:137], v[236:239], v[190:193], v[134:137]\n"
      "v_cvt_pk_bf16_f32 v108, v108, v109\n"
      "v_mul_f32 v170, v81, v170\n"
      "v_mfma_f32_16x16x32_bf16 v[134:137], v[240:243], v[220:223], v[134:137]\n"
      "s_waitcnt lgkmcnt(0)\n"
      "v_cvt_pk_bf16_f32 v182, v182, v183\n"
      "v_cvt_pk_bf16_f32 v163, v164, v165\n"
      "v_mfma_f32_16x16x32_bf16 v[96:99], a[228:231], v[204:207], v[96:99]\n"
      "ds_read_b128 v[220:223], v2 offset:3328\n"
      "v_cvt_pk_bf16_f32 v183, v184, v185\n"
      "v_mfma_f32_16x16x32_bf16 v[96:99], a[232:235], v[186:189], v[96:99]\n"
      "v_cvt_pk_bf16_f32 v165, v168, v169\n"
      "v_cvt_pk_bf16_f32 v85, v160, v161\n"
      "v_mfma_f32_16x16x32_bf16 v[96:99], a[236:239], v[200:203], v[96:99]\n"
      "ds_read_b128 v[174:177], v2 offset:9472\n"
      "v_mul_f32 v115, v81, v115\n"
      "v_mfma_f32_16x16x32_bf16 v[116:119], a[252:255], v[204:207], v[116:119]\n"
      "ds_read_b64 v[128:129], v10 offset:1192\n"
      "v_mul_f32 v173, v81, v173\n"
      "v_mfma_f32_16x16x32_bf16 v[116:119], v[224:227], v[186:189], v[116:119]\n"
      "ds_read_b128 v[120:123], v10 offset:1200\n"
      "v_mul_f32 v107, v81, v107\n"
      "v_mfma_f32_16x16x32_bf16 v[116:119], v[228:231], v[200:203], v[116:119]\n"
      "v_mul_f32 v166, v84, v166\n"
      "v_mul_f32_e64 v172, v81, v172\n"
      "v_mfma_f32_16x16x32_bf16 v[134:137], v[244:247], v[204:207], v[134:137]\n"
      "v_cvt_pk_bf16_f32 v84, v158, v159\n"
      "v_mul_f32_e64 v105, v81, v105\n"
      "v_mfma_f32_16x16x32_bf16 v[134:137], v[248:251], v[186:189], v[134:137]\n"
      "ds_read_u16 v78, v11 offset:1168\n"
      "v_cvt_pk_bf16_f32 v76, v144, v145\n"
      "v_mfma_f32_16x16x32_bf16 v[134:137], v[252:255], v[200:203], v[134:137]\n"
      "v_mul_f32_e64 v113, v81, v113\n"
      "v_cvt_pk_bf16_f32 v104, v104, v105\n"
      "v_mfma_f32_16x16x32_bf16 v[186:189], v[152:155], v[124:127], v[40:43]\n"
      "v_cvt_pk_bf16_f32 v164, v166, v167\n"
      "v_cvt_pk_bf16_f32 v170, v170, v171\n"
      "v_mfma_f32_16x16x32_bf16 v[144:147], v[178:181], v[124:127], v[44:47]\n"
      "s_waitcnt lgkmcnt(1)\n"
      "v_cvt_pk_bf16_f32 v80, v148, v149\n"
      "v_mul_f32 v106, v81, v106\n"
      "v_mfma_f32_16x16x32_bf16 v[166:169], v[162:165], v[124:127], v[48:51]\n"
      "s_add_u32 s43, s43, 1\n"
      "v_fma_f32 v184, v96, v96, s52\n"
      "v_mul_f32 v185, v97, v97\n"
      "v_mfma_f32_16x16x32_bf16 v[192:195], v[120:123], v[124:127], v[24:27]\n"
      "ds_read_b128 v[158:161], v2 offset:6400\n"
      "v_cvt_pk_bf16_f32 v171, v172, v173\n"
      "v_mfma_f32_16x16x16_bf16 v[186:189], v[218:219], v[138:139], v[186:189]\n"
      "v_fmac_f32 v184, v98, v98\n"
      "s_waitcnt vmcnt(1)\n"
      "v_or3_b32 v100, v100, v101, v102\n"
      "v_cvt_pk_bf16_f32 v173, v114, v115\n"
      "v_mfma_f32_16x16x16_bf16 v[144:147], v[156:157], v[138:139], v[144:147]\n"
      "v_cvt_pk_bf16_f32 v172, v112, v113\n"
      "v_fmac_f32 v185, v99, v99\n"
      "v_fmac_f32 v185, v117, v117\n"
      "v_mfma_f32_16x16x16_bf16 v[166:169], v[182:183], v[138:139], v[166:169]\n"
      "v_exp_f32_e64 v187, v187\n"
      "v_cvt_pk_bf16_f32 v81, v150, v151\n"
      "v_mfma_f32_16x16x16_bf16 v[192:195], v[128:129], v[138:139], v[192:195]\n"
      "v_cvt_pk_bf16_f32 v109, v110, v111\n"
      "v_exp_f32 v189, v189\n"
      "v_mfma_f32_16x16x32_bf16 v[200:203], a[216:219], v[140:143], 0\n"
      "v_exp_f32 v145, v145\n"
      "v_mfma_f32_16x16x32_bf16 v[200:203], a[220:223], v[220:223], v[200:203]\n"
      "s_waitcnt lgkmcnt(0)\n"
      "v_exp_f32 v147, v147\n"
      "v_mfma_f32_16x16x32_bf16 v[200:203], a[224:227], v[158:161], v[200:203]\n"
      "s_and_b32 s47, s47, -4\n"
      "v_exp_f32 v146, v146\n"
      "v_mfma_f32_16x16x32_bf16 v[112:115], a[240:243], v[140:143], 0\n"
      "v_exp_f32_e64 v188, v188\n"
      "v_mfma_f32_16x16x32_bf16 v[112:115], a[244:247], v[220:223], v[112:115]\n"
      "v_exp_f32_e64 v168, v168\n"
      "v_mfma_f32_16x16x32_bf16 v[112:115], a[248:251], v[158:161], v[112:115]\n"
      "v_exp_f32_e64 v144, v144\n"
      "v_mfma_f32_16x16x32_bf16 v[204:207], v[232:235], v[140:143], 0\n"
      "v_exp_f32_e64 v167, v167\n"
      "v_mfma_f32_16x16x32_bf16 v[204:207], v[236:239], v[220:223], v[204:207]\n"
      "v_exp_f32 v186, v186\n"
      "v_mfma_f32_16x16x32_bf16 v[204:207], v[240:243], v[158:161], v[204:207]\n"
      "ds_read_b128 v[158:161], v2 offset:12544\n"
      "s_waitcnt vmcnt(0)\n"
      "v_or3_b32 v100, v100, v103, v86\n"
      "v_mfma_f32_16x16x32_bf16 v[148:151], v[152:155], v[210:213], v[52:55]\n"
      "ds_read_u16 v86, v11 offset:1232\n"
      "v_cvt_pk_bf16_f32 v186, v186, v187\n"
      "v_mfma_f32_16x16x32_bf16 v[220:223], v[178:181], v[210:213], v[56:59]\n"
      "v_exp_f32_e64 v169, v169\n"
      "v_mfma_f32_16x16x32_bf16 v[124:127], v[162:165], v[210:213], v[60:63]\n"
      "v_cvt_pk_bf16_f32 v187, v188, v189\n"
      "v_cvt_pk_bf16_f32 v188, v144, v145\n"
      "v_mfma_f32_16x16x32_bf16 v[140:143], v[120:123], v[210:213], v[26:29]\n"
      "v_exp_f32 v192, v192\n"
      "v_mfma_f32_16x16x16_bf16 v[148:151], v[218:219], v[108:109], v[148:151]\n"
      "v_cvt_pk_bf16_f32 v189, v146, v147\n"
      "v_exp_f32 v166, v166\n"
      "v_mfma_f32_16x16x16_bf16 v[220:223], v[156:157], v[108:109], v[220:223]\n"
      "v_cvt_pk_bf16_f32 v105, v106, v107\n"
      "v_cvt_pk_bf16_f32 v90, v192, 0\n"
      "v_cvt_pk_bf16_f32 v88, v166, v167\n"
      "v_mfma_f32_16x16x16_bf16 v[124:127], v[182:183], v[108:109], v[124:127]\n"
      "v_cvt_pk_bf16_f32 v89, v168, v169\n"
      "v_exp_f32 v150, v150\n"
      "v_mfma_f32_16x16x16_bf16 v[140:143], v[128:129], v[108:109], v[140:143]\n"
      "v_exp_f32 v148, v148\n"
      "v_fmac_f32 v184, v116, v116\n"
      "v_mfma_f32_16x16x32_bf16 v[144:147], v[32:35], v[186:189], 0\n"
      "v_exp_f32 v223, v223\n"
      "v_mfma_f32_16x16x32_bf16 v[144:147], v[32:35], v[88:91], v[144:147]\n"
      "v_exp_f32_e64 v151, v151\n"
      "v_mfma_f32_16x16x32_bf16 v[166:169], v[130:133], v[186:189], 0\n"
      "v_exp_f32 v124, v124\n"
      "v_mfma_f32_16x16x32_bf16 v[166:169], v[76:79], v[88:91], v[166:169]\n"
      "v_exp_f32 v222, v222\n"
      "v_mfma_f32_16x16x32_bf16 v[210:213], v[196:199], v[186:189], 0\n"
      "v_exp_f32 v149, v149\n"
      "v_mfma_f32_16x16x32_bf16 v[210:213], v[80:83], v[88:91], v[210:213]\n"
      "v_exp_f32 v127, v127\n"
      "v_mfma_f32_16x16x32_bf16 v[190:193], v[214:217], v[186:189], 0\n"
      "s_waitcnt lgkmcnt(0)\n"
      "v_exp_f32 v125, v125\n"
      "v_mfma_f32_16x16x32_bf16 v[190:193], v[84:87], v[88:91], v[190:193]\n"
      "v_rcp_f32_e64 v144, v144\n"
      "v_cvt_pk_bf16_f32 v148, v148, v149\n"
      "v_mfma_f32_16x16x32_bf16 v[108:111], v[152:155], v[170:173], v[64:67]\n"
      "v_cvt_pk_bf16_f32 v92, v124, v125\n"
      "v_exp_f32_e64 v126, v126\n"
      "v_cvt_pk_bf16_f32 v149, v150, v151\n"
      "v_cvt_pk_bf16_f32 v151, v222, v223\n"
      "v_pk_mul_f32 v[210:211], v[210:211], v[144:145] op_sel_hi:[1,0]\n"
      "v_pk_mul_f32 v[168:169], v[168:169], v[144:145] op_sel_hi:[1,0]\n"
      "v_pk_mul_f32 v[190:191], v[190:191], v[144:145] op_sel_hi:[1,0]\n"
      "v_pk_mul_f32 v[166:167], v[166:167], v[144:145] op_sel_hi:[1,0]\n"
      "v_pk_mul_f32 v[212:213], v[212:213], v[144:145] op_sel_hi:[1,0]\n"
      "v_pk_mul_f32 v[192:193], v[192:193], v[144:145] op_sel_hi:[1,0]\n"
      "v_mfma_f32_16x16x32_bf16 v[186:189], v[178:181], v[170:173], v[68:71]\n"
      "ds_write_b32 v31, v100 offset:5632\n"
      "v_mfma_f32_16x16x32_bf16 v[144:147], v[162:165], v[170:173], v[72:75]\n"
      "v_cvt_pk_bf16_f32 v93, v126, v127\n"
      "v_cvt_pk_bf16_f32 v166, v166, v167\n"
      "v_mfma_f32_16x16x32_bf16 v[124:127], v[120:123], v[170:173], v[28:31]\n"
      "ds_read_b128 v[100:103], v2 offset:15616\n"
      "v_cvt_pk_bf16_f32 v167, v168, v169\n"
      "v_mfma_f32_16x16x16_bf16 v[108:111], v[218:219], v[104:105], v[108:111]\n"
      "v_exp_f32_e64 v220, v220\n"
      "v_cvt_pk_bf16_f32 v168, v210, v211\n"
      "v_mfma_f32_16x16x16_bf16 v[186:189], v[156:157], v[104:105], v[186:189]\n"
      "v_cvt_pk_bf16_f32 v169, v212, v213\n"
      "v_exp_f32_e64 v221, v221\n"
      "v_mfma_f32_16x16x16_bf16 v[144:147], v[182:183], v[104:105], v[144:147]\n"
      "v_cvt_pk_bf16_f32 v150, v220, v221\n"
      "v_exp_f32 v140, v140\n"
      "v_mfma_f32_16x16x16_bf16 v[124:127], v[128:129], v[104:105], v[124:127]\n"
      "v_exp_f32 v108, v108\n"
      "v_cvt_pk_bf16_f32 v190, v190, v191\n"
      "v_mfma_f32_16x16x32_bf16 v[200:203], a[228:231], v[174:177], v[200:203]\n"
      "v_exp_f32 v186, v186\n"
      "v_mfma_f32_16x16x32_bf16 v[200:203], a[232:235], v[158:161], v[200:203]\n"
      "s_waitcnt lgkmcnt(0)\n"
      "v_exp_f32 v109, v109\n"
      "v_mfma_f32_16x16x32_bf16 v[200:203], a[236:239], v[100:103], v[200:203]\n"
      "v_exp_f32 v146, v146\n"
      "v_mfma_f32_16x16x32_bf16 v[112:115], a[252:255], v[174:177], v[112:115]\n"
      "v_exp_f32_e64 v144, v144\n"
      "v_mfma_f32_16x16x32_bf16 v[112:115], v[224:227], v[158:161], v[112:115]\n"
      "v_exp_f32 v147, v147\n"
      "v_mfma_f32_16x16x32_bf16 v[112:115], v[228:231], v[100:103], v[112:115]\n"
      "v_exp_f32 v145, v145\n"
      "v_mfma_f32_16x16x32_bf16 v[204:207], v[244:247], v[174:177], v[204:207]\n"
      "ds_read_b128 v[174:177], v7\n"
      "v_cvt_pk_bf16_f32 v108, v108, v109\n"
      "v_mfma_f32_16x16x32_bf16 v[204:207], v[248:251], v[158:161], v[204:207]\n"
      "v_exp_f32_e64 v110, v110\n"
      "v_mfma_f32_16x16x32_bf16 v[204:207], v[252:255], v[100:103], v[204:207]\n"
      "ds_read_b64 v[126:127], v10 offset:1184\n"
      "v_cvt_pk_bf16_f32 v94, v140, 0\n"
      "v_mfma_f32_16x16x32_bf16 v[158:161], v[32:35], v[148:151], 0\n"
      "v_exp_f32_e64 v187, v187\n"
      "v_mfma_f32_16x16x32_bf16 v[158:161], v[32:35], v[92:95], v[158:161]\n"
      "ds_read_b128 v[208:211], v10 offset:1168\n"
      "v_cvt_pk_bf16_f32 v88, v144, v145\n"
      "v_cvt_pk_bf16_f32 v89, v146, v147\n"
      "v_mfma_f32_16x16x32_bf16 v[170:173], v[130:133], v[148:151], 0\n"
      "v_exp_f32_e64 v124, v124\n"
      "v_mfma_f32_16x16x32_bf16 v[170:173], v[76:79], v[92:95], v[170:173]\n"
      "ds_write_b128 v3, v[166:169] offset:18432\n"
      "v_mfma_f32_16x16x32_bf16 v[144:147], v[196:199], v[148:151], 0\n"
      "v_cvt_pk_bf16_f32 v191, v192, v193\n"
      "v_fma_f32 v138, v200, v200, s52\n"
      "v_mfma_f32_16x16x32_bf16 v[144:147], v[80:83], v[92:95], v[144:147]\n"
      "ds_read_b128 v[166:169], v7 offset:4096\n"
      "v_mul_f32 v139, v201, v201\n"
      "v_mfma_f32_16x16x32_bf16 v[100:103], v[214:217], v[148:151], 0\n"
      "s_waitcnt lgkmcnt(2)\n"
      "v_rcp_f32_e64 v158, v158\n"
      "v_mfma_f32_16x16x32_bf16 v[100:103], v[84:87], v[92:95], v[100:103]\n"
      "buffer_store_dwordx4 v[174:177], v20, s[24:27], s85 offen sc1\n"
      "v_pk_fma_f32 v[138:139], v[202:203], v[202:203], v[138:139]\n"
      "v_pk_fma_f32 v[138:139], v[112:113], v[112:113], v[138:139]\n"
      "v_pk_mul_f32 v[170:171], v[170:171], v[158:159] op_sel_hi:[1,0]\n"
      "v_pk_mul_f32 v[172:173], v[172:173], v[158:159] op_sel_hi:[1,0]\n"
      "v_pk_mul_f32 v[144:145], v[144:145], v[158:159] op_sel_hi:[1,0]\n"
      "v_pk_fma_f32 v[184:185], v[118:119], v[118:119], v[184:185]\n"
      "v_pk_fma_f32 v[184:185], v[134:135], v[134:135], v[184:185]\n"
      "v_mfma_f32_16x16x32_bf16 v[174:177], v[208:211], v[152:155], v[30:33]\n"
      "ds_read_b128 v[104:107], v2 offset:512\n"
      "v_cvt_pk_bf16_f32 v170, v170, v171\n"
      "v_mfma_f32_16x16x32_bf16 v[148:151], v[208:211], v[178:181], v[30:33]\n"
      "s_waitcnt lgkmcnt(1)\n"
      "v_exp_f32 v111, v111\n"
      "v_mfma_f32_16x16x32_bf16 v[140:143], v[208:211], v[162:165], v[30:33]\n"
      "ds_read_b128 v[220:223], v2 offset:3584\n"
      "v_cvt_pk_bf16_f32 v171, v172, v173\n"
      "v_mfma_f32_16x16x32_bf16 v[192:195], v[208:211], v[120:123], v[30:33]\n"
      "ds_read_b128 v[208:211], v2 offset:6656\n"
      "v_cvt_pk_bf16_f32 v172, v144, v145\n"
      "v_mfma_f32_16x16x16_bf16 v[174:177], v[126:127], v[218:219], v[174:177]\n"
      "v_cvt_pk_bf16_f32 v109, v110, v111\n"
      "v_exp_f32_e64 v189, v189\n"
      "v_mfma_f32_16x16x16_bf16 v[148:151], v[126:127], v[156:157], v[148:151]\n"
      "ds_read_b128 v[178:181], v7 offset:8192\n"
      "v_cvt_pk_bf16_f32 v110, v186, v187\n"
      "v_mfma_f32_16x16x16_bf16 v[140:143], v[126:127], v[182:183], v[140:143]\n"
      "buffer_store_dwordx4 v[166:169], v21, s[24:27], s85 offen sc1\n"
      "v_pk_mul_f32 v[100:101], v[100:101], v[158:159] op_sel_hi:[1,0]\n"
      "v_pk_fma_f32 v[138:139], v[114:115], v[114:115], v[138:139]\n"
      "v_pk_fma_f32 v[138:139], v[204:205], v[204:205], v[138:139]\n"
      "v_pk_mul_f32 v[146:147], v[146:147], v[158:159] op_sel_hi:[1,0]\n"
      "v_pk_mul_f32 v[102:103], v[102:103], v[158:159] op_sel_hi:[1,0]\n"
      "v_mfma_f32_16x16x16_bf16 v[192:195], v[126:127], v[128:129], v[192:195]\n"
      "v_exp_f32_e64 v188, v188\n"
      "s_waitcnt lgkmcnt(1)\n"
      "v_mfma_f32_16x16x32_bf16 v[156:159], a[216:219], v[104:107], 0\n"
      "v_cndmask_b32 v174, v174, v148, s[58:59]\n"
      "v_cndmask_b32 v174, v174, v140, s[60:61]\n"
      "v_mfma_f32_16x16x32_bf16 v[156:159], a[220:223], v[220:223], v[156:159]\n"
      "ds_read_b128 v[166:169], v7 offset:12288\n"
      "v_cvt_pk_bf16_f32 v173, v146, v147\n"
      "v_mfma_f32_16x16x32_bf16 v[156:159], a[224:227], v[208:211], v[156:159]\n"
      "ds_write_b128 v3, v[170:173] offset:18688\n"
      "v_pk_fma_f32 v[138:139], v[206:207], v[206:207], v[138:139]\n"
      "v_mfma_f32_16x16x32_bf16 v[146:149], a[240:243], v[104:107], 0\n"
      "s_waitcnt lgkmcnt(2)\n"
      "v_cvt_pk_bf16_f32 v100, v100, v101\n"
      "v_cvt_pk_bf16_f32 v101, v102, v103\n"
      "v_mfma_f32_16x16x32_bf16 v[146:149], a[244:247], v[220:223], v[146:149]\n"
      "buffer_store_dwordx4 v[178:181], v22, s[24:27], s85 offen sc1\n"
      "v_pk_fma_f32 v[184:185], v[136:137], v[136:137], v[184:185]\n"
      "v_mfma_f32_16x16x32_bf16 v[146:149], a[248:251], v[208:211], v[146:149]\n"
      "ds_write_b64 v4, v[190:191] offset:18432\n"
      "v_mfma_f32_16x16x32_bf16 v[160:163], v[232:235], v[104:107], 0\n"
      "ds_read_b128 v[126:129], v2 offset:12800\n"
      "v_cndmask_b32 v174, v174, v192, s[62:63]\n"
      "v_mfma_f32_16x16x32_bf16 v[160:163], v[236:239], v[220:223], v[160:163]\n"
      "ds_read_b128 v[150:153], v2 offset:9728\n"
      "v_cvt_pk_bf16_f32 v111, v188, v189\n"
      "v_mfma_f32_16x16x32_bf16 v[160:163], v[240:243], v[208:211], v[160:163]\n"
      "ds_read_b128 v[142:145], v7 offset:16384\n"
      "v_cvt_pk_bf16_f32 v90, v124, 0\n"
      "v_mfma_f32_16x16x32_bf16 v[208:211], v[32:35], v[108:111], 0\n"
      "ds_read_b128 v[170:173], v2 offset:15872\n"
      "v_add_f32 v138, v138, v139\n"
      "v_mfma_f32_16x16x32_bf16 v[208:211], v[32:35], v[88:91], v[208:211]\n"
      "v_exp_f32 v174, v174\n"
      "v_mfma_f32_16x16x32_bf16 v[190:193], v[130:133], v[108:111], 0\n"
      "s_waitcnt lgkmcnt(5)\n"
      "v_cvt_pk_bf16_f32 v174, v174, v174\n"
      "v_add_f32 v184, v184, v185\n"
      "v_mfma_f32_16x16x32_bf16 v[190:193], v[76:79], v[88:91], v[190:193]\n"
      "ds_write_b16 v13, v174\n"
      "v_mfma_f32_16x16x32_bf16 v[102:105], v[196:199], v[108:111], 0\n"
      "buffer_store_dwordx4 v[166:169], v23, s[24:27], s85 offen sc1\n"
      "v_mfma_f32_16x16x32_bf16 v[102:105], v[80:83], v[88:91], v[102:105]\n"
      "s_waitcnt lgkmcnt(2)\n"
      "v_rcp_f32 v208, v208\n"
      "v_mfma_f32_16x16x32_bf16 v[166:169], v[214:217], v[108:111], 0\n"
      "ds_write_b64 v4, v[100:101] offset:18688\n"
      "v_mfma_f32_16x16x32_bf16 v[166:169], v[84:87], v[88:91], v[166:169]\n"
      "ds_read_b128 v[174:177], v14 offset:64\n"
      "v_mul_f32 v190, v208, v190\n"
      "v_mfma_f32_16x16x32_bf16 v[156:159], a[228:231], v[150:153], v[156:159]\n"
      "s_waitcnt lgkmcnt(3)\n"
      "v_mul_f32 v191, v208, v191\n"
      "v_mul_f32 v102, v208, v102\n"
      "v_mfma_f32_16x16x32_bf16 v[156:159], a[232:235], v[126:129], v[156:159]\n"
      "ds_read_b128 v[218:221], v14\n"
      "v_cvt_pk_bf16_f32 v190, v190, v191\n"
      "v_mfma_f32_16x16x32_bf16 v[156:159], a[236:239], v[170:173], v[156:159]\n"
      "v_mul_f32 v104, v208, v104\n"
      "v_mul_f32 v193, v208, v193\n"
      "v_mfma_f32_16x16x32_bf16 v[146:149], a[252:255], v[150:153], v[146:149]\n"
      "v_permlane16_swap_b32 v184, v138\n"
      "v_mfma_f32_16x16x32_bf16 v[146:149], v[224:227], v[126:129], v[146:149]\n"
      "v_mul_f32 v192, v208, v192\n"
      "v_mul_f32_e64 v103, v208, v103\n"
      "v_mfma_f32_16x16x32_bf16 v[146:149], v[228:231], v[170:173], v[146:149]\n"
      "v_cvt_pk_bf16_f32 v191, v192, v193\n"
      "v_mul_f32_e64 v179, v157, v157\n"
      "v_mfma_f32_16x16x32_bf16 v[160:163], v[244:247], v[150:153], v[160:163]\n"
      "v_mul_f32 v105, v208, v105\n"
      "v_fma_f32 v178, v156, v156, s52\n"
      "v_mfma_f32_16x16x32_bf16 v[160:163], v[248:251], v[126:129], v[160:163]\n"
      "s_waitcnt lgkmcnt(0)\n"
      "v_cvt_pk_bf16_f32 v193, v104, v105\n"
      "v_cvt_pk_bf16_f32 v192, v102, v103\n"
      "v_mfma_f32_16x16x32_bf16 v[160:163], v[252:255], v[170:173], v[160:163]\n"
      "ds_write_b128 v3, v[190:193] offset:18944\n"
      "v_pk_mul_f32 v[168:169], v[168:169], v[208:209] op_sel_hi:[1,0]\n"
      "v_pk_mul_f32 v[166:167], v[166:167], v[208:209] op_sel_hi:[1,0]\n"
      "v_pk_fma_f32 v[178:179], v[158:159], v[158:159], v[178:179]\n"
      "v_cvt_pk_bf16_f32 v166, v166, v167\n"
      "v_add_f32_e64 v184, v184, v138\n"
      "v_pk_fma_f32 v[178:179], v[146:147], v[146:147], v[178:179]\n"
      "v_pk_fma_f32 v[178:179], v[148:149], v[148:149], v[178:179]\n"
      "v_cvt_pk_bf16_f32 v167, v168, v169\n"
      "v_pk_fma_f32 v[178:179], v[160:161], v[160:161], v[178:179]\n"
      "v_pk_fma_f32 v[178:179], v[162:163], v[162:163], v[178:179]\n"
      "v_add_f32_e64 v178, v178, v179\n"
      "v_mov_b32_e64 v138, v178\n"
      "v_mfma_f32_16x16x32_bf16 v[208:211], v[218:221], v[32:35], 0\n"
      "ds_write_b64 v4, v[166:167] offset:18944\n"
      "v_permlane16_swap_b32 v178, v138\n"
      "v_add_f32_e64 v178, v178, v138\n"
      "s_nop 1\n"
      "v_permlane32_swap_b32_e64 v184, v178\n"
      "v_add_f32 v184, v184, v178\n"
      "v_mfma_f32_16x16x32_bf16 v[208:211], v[174:177], v[32:35], v[208:211]\n"
      "ds_write_b32 v31, v184 offset:1024\n"
      "buffer_store_dwordx4 v[142:145], v25, s[24:27], s85 offen sc1\n"
      "s_cmp_ge_u32 s95, 0x40000000\n"
      "s_cselect_b64 vcc, -1, s[56:57]\n"
      "v_add_u32 v10, 2336, v10\n"
      "v_add_u32 v11, 2336, v11\n"
      "s_waitcnt lgkmcnt(0)\n"
      "s_barrier\n"
      "s_cbranch_vccnz L_disp0_%=\n"
      "L_loop0_%=:\n"
      "v_mfma_f32_16x16x32_bf16 v[168:171], v[218:221], v[130:133], 0\n"
      "ds_read_b128 v[142:145], v0\n"
      "ds_read_b128 v[178:181], v9 offset:1024\n"
      "v_mfma_f32_16x16x32_bf16 v[168:171], v[174:177], v[76:79], v[168:171]\n"
      "ds_read_b128 v[182:185], v1\n"
      "v_mfma_f32_16x16x32_bf16 v[150:153], v[218:221], v[196:199], 0\n"
      "ds_read_b128 v[138:141], v9 offset:5632\n"
      "s_add_u32 m0, s64, 18432\n"
      "v_mfma_f32_16x16x32_bf16 v[150:153], v[174:177], v[80:83], v[150:153]\n"
      "ds_read_b128 v[126:129], v0 offset:6144\n"
      "v_readlane_b32 s85, v37, s42\n"
      "v_mfma_f32_16x16x32_bf16 v[194:197], v[218:221], v[214:217], 0\n"
      "buffer_load_dwordx4 v16, s[16:19], s45 offen  lds\n"
      "v_mfma_f32_16x16x32_bf16 v[194:197], v[174:177], v[84:87], v[194:197]\n"
      "ds_read_b128 v[218:221], v1 offset:12288\n"
      "s_waitcnt lgkmcnt(5)\n"
      "v_mfma_f32_16x16x32_bf16 v[174:177], a[0:3], v[142:145], 0\n"
      "ds_read_b128 v[170:173], v0 offset:12288\n"
      "v_readlane_b32 s84, v37, s43\n"
      "v_mfma_f32_16x16x32_bf16 v[100:103], a[24:27], v[142:145], 0\n"
      "s_waitcnt lgkmcnt(5)\n"
      "v_readlane_b32 s86, v38, s43\n"
      "v_add_f32 v178, v178, v179\n"
      "v_mfma_f32_16x16x32_bf16 v[190:193], a[48:51], v[142:145], 0\n"
      "s_add_u32 m0, s64, 22528\n"
      "v_add_f32 v180, v180, v181\n"
      "v_add_f32 v178, v178, v180\n"
      "v_mfma_f32_16x16x32_bf16 v[164:167], a[72:75], v[142:145], 0\n"
      "v_rsq_f32 v178, v178\n"
      "v_mfma_f32_16x16x32_bf16 v[120:123], a[96:99], v[142:145], 0\n"
      "buffer_load_dwordx4 v17, s[16:19], s45 offen  lds\n"
      "v_mfma_f32_16x16x32_bf16 v[186:189], a[120:123], v[142:145], 0\n"
      "s_waitcnt lgkmcnt(1)\n"
      "v_mul_f32 v178, 0x415db3d7, v178\n"
      "v_mov_b32 v180, v178\n"
      "v_mfma_f32_16x16x32_bf16 v[174:177], a[4:7], v[182:185], v[174:177]\n"
      "s_add_u32 m0, s64, 26624\n"
      "v_permlane32_swap_b32 v178, v180\n"
      "v_mfma_f32_16x16x32_bf16 v[174:177], a[8:11], v[126:129], v[174:177]\n"
      "ds_read_b128 v[210:213], v1 offset:6144\n"
      "v_mov_b32 v179, v178\n"
      "v_mfma_f32_16x16x32_bf16 v[100:103], a[28:31], v[182:185], v[100:103]\n"
      "s_nop 0\n"
      "v_permlane16_swap_b32 v178, v179\n"
      "v_mfma_f32_16x16x32_bf16 v[100:103], a[32:35], v[126:129], v[100:103]\n"
      "buffer_load_dwordx4 v18, s[16:19], s45 offen  lds\n"
      "v_mfma_f32_16x16x32_bf16 v[190:193], a[52:55], v[182:185], v[190:193]\n"
      "s_add_u32 m0, s64, 30720\n"
      "v_cndmask_b32 v168, v168, v150, s[58:59]\n"
      "v_mul_f32 v119, v178, v119\n"
      "v_mfma_f32_16x16x32_bf16 v[190:193], a[56:59], v[126:129], v[190:193]\n"
      "v_mul_f32 v99, v178, v99\n"
      "v_mul_f32 v162, v180, v162\n"
      "v_mfma_f32_16x16x32_bf16 v[164:167], a[76:79], v[182:185], v[164:167]\n"
      "v_mul_f32 v117, v178, v117\n"
      "v_mov_b32_dpp v208, v208 quad_perm:[0,0,0,0] row_mask:0xf bank_mask:0xf\n"
      "v_mfma_f32_16x16x32_bf16 v[164:167], a[80:83], v[126:129], v[164:167]\n"
      "v_mul_f32 v97, v178, v97\n"
      "v_mul_f32 v96, v178, v96\n"
      "v_mfma_f32_16x16x32_bf16 v[120:123], a[100:103], v[182:185], v[120:123]\n"
      "buffer_load_dwordx4 v19, s[16:19], s45 offen  lds\n"
      "v_mfma_f32_16x16x32_bf16 v[120:123], a[104:107], v[126:129], v[120:123]\n"
      "v_cndmask_b32 v168, v168, v194, s[60:61]\n"
      "v_mul_f32 v203, v179, v203\n"
      "v_mfma_f32_16x16x32_bf16 v[186:189], a[124:127], v[182:185], v[186:189]\n"
      "v_mul_f32 v112, v179, v112\n"
      "v_mul_f32 v98, v178, v98\n"
      "v_mfma_f32_16x16x32_bf16 v[186:189], a[128:131], v[126:129], v[186:189]\n"
      "s_waitcnt lgkmcnt(0)\n"
      "v_mul_f32 v147, v180, v147\n"
      "v_mul_f32 v113, v179, v113\n"
      "v_mfma_f32_16x16x32_bf16 v[174:177], a[12:15], v[210:213], v[174:177]\n"
      "s_add_u32 m0, s65, 34816\n"
      "v_mul_f32 v206, v179, v206\n"
      "v_mul_f32 v201, v179, v201\n"
      "v_mfma_f32_16x16x32_bf16 v[174:177], a[16:19], v[170:173], v[174:177]\n"
      "buffer_load_dwordx4 v15, s[16:19], s45 offen  lds\n"
      "v_mfma_f32_16x16x32_bf16 v[174:177], a[20:23], v[218:221], v[174:177]\n"
      "v_mul_f32 v115, v179, v115\n"
      "v_mul_f32 v205, v179, v205\n"
      "v_mfma_f32_16x16x32_bf16 v[100:103], a[36:39], v[210:213], v[100:103]\n"
      "s_or_b32 s95, s84, s85\n"
      "v_mul_f32 v118, v178, v118\n"
      "v_mul_f32 v158, v180, v158\n"
      "v_mfma_f32_16x16x32_bf16 v[100:103], a[40:43], v[170:173], v[100:103]\n"
      "v_cvt_pk_bf16_f32 v96, v96, v97\n"
      "v_mul_f32 v116, v178, v116\n"
      "v_mfma_f32_16x16x32_bf16 v[100:103], a[44:47], v[218:221], v[100:103]\n"
      "v_rcp_f32_e64 v208, v208\n"
      "v_mfma_f32_16x16x32_bf16 v[190:193], a[60:63], v[210:213], v[190:193]\n"
      "v_cvt_pk_bf16_f32 v97, v98, v99\n"
      "v_cvt_pk_bf16_f32 v98, v116, v117\n"
      "v_mfma_f32_16x16x32_bf16 v[190:193], a[64:67], v[170:173], v[190:193]\n"
      "ds_read_b128 v[104:107], v1 offset:2048\n"
      "v_cvt_pk_bf16_f32 v99, v118, v119\n"
      "v_mfma_f32_16x16x32_bf16 v[190:193], a[68:71], v[218:221], v[190:193]\n"
      "buffer_load_dwordx4 v[108:111], v36, s[28:31], s47 offen\n"
      "v_mfma_f32_16x16x32_bf16 v[164:167], a[84:87], v[210:213], v[164:167]\n"
      "ds_write_b128 v5, v[96:99]\n"
      "v_mfma_f32_16x16x32_bf16 v[164:167], a[88:91], v[170:173], v[164:167]\n"
      "v_mul_f32 v148, v180, v148\n"
      "v_mul_f32 v157, v180, v157\n"
      "v_mfma_f32_16x16x32_bf16 v[164:167], a[92:95], v[218:221], v[164:167]\n"
      "v_mul_f32_e64 v200, v179, v200\n"
      "v_or3_b32 v138, v138, v139, v140\n"
      "v_mfma_f32_16x16x32_bf16 v[120:123], a[108:111], v[210:213], v[120:123]\n"
      "buffer_load_dword v90, v29, s[28:31], s47 offen\n"
      "v_mfma_f32_16x16x32_bf16 v[120:123], a[112:115], v[170:173], v[120:123]\n"
      "v_mul_f32_e64 v161, v180, v161\n"
      "v_fma_f32 v78, v174, v174, s51\n"
      "v_mfma_f32_16x16x32_bf16 v[120:123], a[116:119], v[218:221], v[120:123]\n"
      "v_fmac_f32 v78, v175, v175\n"
      "v_mul_f32 v168, v208, v168\n"
      "v_mfma_f32_16x16x32_bf16 v[186:189], a[132:135], v[210:213], v[186:189]\n"
      "s_or_b32 s95, s95, s48\n"
      "v_mul_f32 v159, v180, v159\n"
      "v_mul_f32 v160, v180, v160\n"
      "v_mfma_f32_16x16x32_bf16 v[186:189], a[136:139], v[170:173], v[186:189]\n"
      "v_fmac_f32 v78, v176, v176\n"
      "v_fma_f32 v194, v164, v164, s51\n"
      "v_mfma_f32_16x16x32_bf16 v[186:189], a[140:143], v[218:221], v[186:189]\n"
      "ds_read_b128 v[116:119], v1 offset:8192\n"
      "v_fmac_f32_e64 v194, v165, v165\n"
      "v_mfma_f32_16x16x32_bf16 v[130:133], v[142:145], a[144:147], 0\n"
      "v_cvt_pk_bf16_f32 v168, v168, v168\n"
      "v_bitop3_b32 v138, v138, v141, s33 bitop3:0xa8\n"
      "v_mfma_f32_16x16x32_bf16 v[130:133], v[182:185], a[148:151], v[130:133]\n"
      "v_fmac_f32 v78, v177, v177\n"
      "v_fmac_f32 v194, v166, v166\n"
      "v_mfma_f32_16x16x32_bf16 v[130:133], v[126:129], a[152:155], v[130:133]\n"
      "ds_read_b128 v[150:153], v0 offset:8192\n"
      "v_fmac_f32 v194, v167, v167\n"
      "v_mfma_f32_16x16x32_bf16 v[130:133], v[210:213], a[156:159], v[130:133]\n"
      "v_mul_f32 v135, v178, v135\n"
      "v_mul_f32 v202, v179, v202\n"
      "v_mfma_f32_16x16x32_bf16 v[130:133], v[170:173], a[160:163], v[130:133]\n"
      "v_cvt_pk_bf16_f32 v200, v200, v201\n"
      "v_fmac_f32 v78, v100, v100\n"
      "v_mfma_f32_16x16x32_bf16 v[130:133], v[218:221], a[164:167], v[130:133]\n"
      "v_mul_f32 v207, v179, v207\n"
      "v_fmac_f32 v194, v120, v120\n"
      "v_mfma_f32_16x16x32_bf16 v[196:199], v[142:145], a[168:171], 0\n"
      "v_fmac_f32_e64 v78, v101, v101\n"
      "v_cmp_ne_u32 s[56:57], 0, v138\n"
      "v_mfma_f32_16x16x32_bf16 v[196:199], v[182:185], a[172:175], v[196:199]\n"
      "ds_write_b16 v12, v168 offset:16\n"
      "v_mfma_f32_16x16x32_bf16 v[196:199], v[126:129], a[176:179], v[196:199]\n"
      "v_fmac_f32 v78, v102, v102\n"
      "v_mul_f32 v204, v179, v204\n"
      "v_mfma_f32_16x16x32_bf16 v[196:199], v[210:213], a[180:183], v[196:199]\n"
      "ds_read_b128 v[138:141], v0 offset:2048\n"
      "v_fmac_f32 v78, v103, v103\n"
      "v_mfma_f32_16x16x32_bf16 v[196:199], v[170:173], a[184:187], v[196:199]\n"
      "v_fmac_f32 v78, v190, v190\n"
      "v_cvt_pk_bf16_f32 v201, v202, v203\n"
      "v_mfma_f32_16x16x32_bf16 v[196:199], v[218:221], a[188:191], v[196:199]\n"
      "v_mul_f32 v114, v179, v114\n"
      "v_fmac_f32 v194, v121, v121\n"
      "v_mfma_f32_16x16x32_bf16 v[214:217], v[142:145], a[192:195], 0\n"
      "s_cmp_eq_u32 s43, 21\n"
      "s_cselect_b32 s95, -1, s95\n"
      "v_fmac_f32_e64 v194, v122, v122\n"
      "v_mfma_f32_16x16x32_bf16 v[214:217], v[182:185], a[196:199], v[214:217]\n"
      "v_cvt_pk_bf16_f32 v204, v204, v205\n"
      "v_cvt_pk_bf16_f32 v205, v206, v207\n"
      "v_mfma_f32_16x16x32_bf16 v[214:217], v[126:129], a[200:203], v[214:217]\n"
      "ds_read_b128 v[126:129], v0 offset:14336\n"
      "v_mul_f32_e64 v156, v180, v156\n"
      "v_mfma_f32_16x16x32_bf16 v[214:217], v[210:213], a[204:207], v[214:217]\n"
      "v_fmac_f32 v78, v191, v191\n"
      "v_fmac_f32 v194, v123, v123\n"
      "v_mfma_f32_16x16x32_bf16 v[214:217], v[170:173], a[208:211], v[214:217]\n"
      "ds_read_b128 v[210:213], v1 offset:14336\n"
      "v_cvt_pk_bf16_f32 v156, v156, v157\n"
      "v_mfma_f32_16x16x32_bf16 v[214:217], v[218:221], a[212:215], v[214:217]\n"
      "s_waitcnt lgkmcnt(2)\n"
      "v_cvt_pk_bf16_f32 v157, v158, v159\n"
      "v_fmac_f32 v78, v192, v192\n"
      "v_mfma_f32_16x16x32_bf16 v[142:145], a[0:3], v[138:141], 0\n"
      "v_fmac_f32 v194, v186, v186\n"
      "v_mul_f32 v146, v180, v146\n"
      "v_mfma_f32_16x16x32_bf16 v[142:145], a[4:7], v[104:107], v[142:145]\n"
      "v_cvt_pk_bf16_f32 v130, v130, v131\n"
      "v_mul_f32 v163, v180, v163\n"
      "v_mfma_f32_16x16x32_bf16 v[142:145], a[8:11], v[150:153], v[142:145]\n"
      "v_fmac_f32 v194, v187, v187\n"
      "v_cvt_pk_bf16_f32 v196, v196, v197\n"
      "v_mfma_f32_16x16x32_bf16 v[142:145], a[12:15], v[116:119], v[142:145]\n"
      "s_waitcnt lgkmcnt(1)\n"
      "v_mul_f32 v136, v178, v136\n"
      "v_fmac_f32 v78, v193, v193\n"
      "v_mfma_f32_16x16x32_bf16 v[142:145], a[16:19], v[126:129], v[142:145]\n"
      "s_waitcnt lgkmcnt(0)\n"
      "v_mul_f32_e64 v137, v178, v137\n"
      "v_cvt_pk_bf16_f32 v131, v132, v133\n"
      "v_mfma_f32_16x16x32_bf16 v[142:145], a[20:23], v[210:213], v[142:145]\n"
      "v_cvt_pk_bf16_f32 v214, v214, v215\n"
      "v_mul_f32_e64 v134, v178, v134\n"
      "v_mfma_f32_16x16x32_bf16 v[182:185], a[24:27], v[138:141], 0\n"
      "v_cvt_pk_bf16_f32 v197, v198, v199\n"
      "v_mfma_f32_16x16x32_bf16 v[182:185], a[28:31], v[104:107], v[182:185]\n"
      "v_fmac_f32 v194, v188, v188\n"
      "v_fmac_f32 v194, v189, v189\n"
      "v_mfma_f32_16x16x32_bf16 v[182:185], a[32:35], v[150:153], v[182:185]\n"
      "v_cvt_pk_bf16_f32 v134, v134, v135\n"
      "v_mul_f32_e64 v149, v180, v149\n"
      "v_mfma_f32_16x16x32_bf16 v[182:185], a[36:39], v[116:119], v[182:185]\n"
      "ds_write_b64 v6, v[204:205] offset:6144\n"
      "v_mfma_f32_16x16x32_bf16 v[182:185], a[40:43], v[126:129], v[182:185]\n"
      "v_fma_f32 v82, v142, v142, s51\n"
      "v_cvt_pk_bf16_f32 v159, v148, v149\n"
      "v_mfma_f32_16x16x32_bf16 v[182:185], a[44:47], v[210:213], v[182:185]\n"
      "v_permlane16_swap_b32 v78, v194\n"
      "v_mfma_f32_16x16x32_bf16 v[220:223], a[48:51], v[138:141], 0\n"
      "v_cvt_pk_bf16_f32 v160, v160, v161\n"
      "v_add_f32 v78, v78, v194\n"
      "v_mfma_f32_16x16x32_bf16 v[220:223], a[52:55], v[104:107], v[220:223]\n"
      "v_fmac_f32 v82, v143, v143\n"
      "v_mov_b32 v194, v78\n"
      "v_mfma_f32_16x16x32_bf16 v[220:223], a[56:59], v[150:153], v[220:223]\n"
      "v_cvt_pk_bf16_f32 v158, v146, v147\n"
      "v_fmac_f32 v82, v144, v144\n"
      "v_mfma_f32_16x16x32_bf16 v[220:223], a[60:63], v[116:119], v[220:223]\n"
      "v_permlane32_swap_b32 v78, v194\n"
      "v_mfma_f32_16x16x32_bf16 v[220:223], a[64:67], v[126:129], v[220:223]\n"
      "ds_write_b128 v5, v[156:159] offset:12288\n"
      "v_mfma_f32_16x16x32_bf16 v[220:223], a[68:71], v[210:213], v[220:223]\n"
      "v_add_f32 v78, v78, v194\n"
      "v_cvt_pk_bf16_f32 v161, v162, v163\n"
      "v_mfma_f32_16x16x32_bf16 v[146:149], a[72:75], v[138:141], 0\n"
      "v_rsq_f32 v78, v78\n"
      "v_mfma_f32_16x16x32_bf16 v[146:149], a[76:79], v[104:107], v[146:149]\n"
      "v_fmac_f32_e64 v82, v145, v145\n"
      "v_cvt_pk_bf16_f32 v203, v114, v115\n"
      "v_mfma_f32_16x16x32_bf16 v[146:149], a[80:83], v[150:153], v[146:149]\n"
      "ds_read_b128 v[156:159], v0 offset:4096\n"
      "v_cvt_pk_bf16_f32 v135, v136, v137\n"
      "v_mfma_f32_16x16x32_bf16 v[146:149], a[84:87], v[116:119], v[146:149]\n"
      "v_fmac_f32 v82, v182, v182\n"
      "v_mov_b32 v194, v78\n"
      "v_mfma_f32_16x16x32_bf16 v[146:149], a[88:91], v[126:129], v[146:149]\n"
      "v_fmac_f32 v82, v183, v183\n"
      "v_fmac_f32 v82, v184, v184\n"
      "v_mfma_f32_16x16x32_bf16 v[146:149], a[92:95], v[210:213], v[146:149]\n"
      "ds_read_b128 v[178:181], v1 offset:10240\n"
      "v_fmac_f32 v82, v185, v185\n"
      "v_mfma_f32_16x16x32_bf16 v[204:207], a[96:99], v[138:141], 0\n"
      "ds_write_b64 v6, v[134:135]\n"
      "v_mfma_f32_16x16x32_bf16 v[204:207], a[100:103], v[104:107], v[204:207]\n"
      "v_permlane16_swap_b32 v78, v194\n"
      "v_mfma_f32_16x16x32_bf16 v[204:207], a[104:107], v[150:153], v[204:207]\n"
      "s_nop 0\n"
      "v_fma_f32 v172, v146, v146, s51\n"
      "v_mul_f32 v78, s50, v78\n"
      "v_mfma_f32_16x16x32_bf16 v[204:207], a[108:111], v[116:119], v[204:207]\n"
      "v_mul_f32 v187, v194, v187\n"
      "v_cvt_pk_bf16_f32 v202, v112, v113\n"
      "v_mfma_f32_16x16x32_bf16 v[204:207], a[112:115], v[126:129], v[204:207]\n"
      "v_mul_f32 v177, v78, v177\n"
      "v_mul_f32_e64 v175, v78, v175\n"
      "v_mfma_f32_16x16x32_bf16 v[204:207], a[116:119], v[210:213], v[204:207]\n"
      "v_mul_f32 v190, v78, v190\n"
      "v_mul_f32 v192, v78, v192\n"
      "v_mfma_f32_16x16x32_bf16 v[168:171], a[120:123], v[138:141], 0\n"
      "ds_read_b128 v[134:137], v1 offset:4096\n"
      "v_fmac_f32 v172, v147, v147\n"
      "v_mfma_f32_16x16x32_bf16 v[168:171], a[124:127], v[104:107], v[168:171]\n"
      "v_fmac_f32 v82, v220, v220\n"
      "v_fmac_f32_e64 v172, v148, v148\n"
      "v_mfma_f32_16x16x32_bf16 v[168:171], a[128:131], v[150:153], v[168:171]\n"
      "ds_read_b128 v[96:99], v0 offset:16384\n"
      "v_fmac_f32 v172, v149, v149\n"
      "v_mfma_f32_16x16x32_bf16 v[168:171], a[132:135], v[116:119], v[168:171]\n"
      "v_cvt_pk_bf16_f32 v215, v216, v217\n"
      "v_mul_f32 v191, v78, v191\n"
      "v_mfma_f32_16x16x32_bf16 v[168:171], a[136:139], v[126:129], v[168:171]\n"
      "v_mul_f32 v122, v194, v122\n"
      "v_fmac_f32 v172, v204, v204\n"
      "v_mfma_f32_16x16x32_bf16 v[168:171], a[140:143], v[210:213], v[168:171]\n"
      "ds_write_b64 v6, v[160:161] offset:12288\n"
      "v_mfma_f32_16x16x32_bf16 v[160:163], v[138:141], a[144:147], 0\n"
      "s_cmp_ge_u32 s95, 0x40000000\n"
      "s_cselect_b32 s94, -1, -2\n"
      "v_mul_f32 v166, v194, v166\n"
      "v_mfma_f32_16x16x32_bf16 v[160:163], v[104:107], a[148:151], v[160:163]\n"
      "s_cmp_gt_u32 s42, s44\n"
      "s_cselect_b32 s95, s94, s95\n"
      "v_fmac_f32 v172, v205, v205\n"
      "v_mfma_f32_16x16x32_bf16 v[160:163], v[150:153], a[152:155], v[160:163]\n"
      "v_mul_f32 v120, v194, v120\n"
      "v_mul_f32 v193, v78, v193\n"
      "v_mfma_f32_16x16x32_bf16 v[160:163], v[116:119], a[156:159], v[160:163]\n"
      "v_mul_f32 v121, v194, v121\n"
      "v_mul_f32 v167, v194, v167\n"
      "v_mfma_f32_16x16x32_bf16 v[160:163], v[126:129], a[160:163], v[160:163]\n"
      "v_mul_f32 v101, v78, v101\n"
      "v_fmac_f32 v172, v206, v206\n"
      "v_mfma_f32_16x16x32_bf16 v[160:163], v[210:213], a[164:167], v[160:163]\n"
      "v_fmac_f32 v82, v221, v221\n"
      "v_mul_f32 v102, v78, v102\n"
      "v_mfma_f32_16x16x32_bf16 v[112:115], v[138:141], a[168:171], 0\n"
      "v_fmac_f32 v172, v207, v207\n"
      "v_mul_f32 v164, v194, v164\n"
      "v_mfma_f32_16x16x32_bf16 v[112:115], v[104:107], a[172:175], v[112:115]\n"
      "v_fmac_f32 v172, v168, v168\n"
      "v_mul_f32 v174, v78, v174\n"
      "v_mfma_f32_16x16x32_bf16 v[112:115], v[150:153], a[176:179], v[112:115]\n"
      "v_cvt_pk_bf16_f32 v132, v160, v161\n"
      "v_fmac_f32_e64 v172, v169, v169\n"
      "v_mfma_f32_16x16x32_bf16 v[112:115], v[116:119], a[180:183], v[112:115]\n"
      "ds_write_b128 v5, v[200:203] offset:6144\n"
      "v_mfma_f32_16x16x32_bf16 v[112:115], v[126:129], a[184:187], v[112:115]\n"
      "v_mul_f32 v176, v78, v176\n"
      "v_fmac_f32 v172, v170, v170\n"
      "v_mfma_f32_16x16x32_bf16 v[112:115], v[210:213], a[188:191], v[112:115]\n"
      "ds_read_u16 v86, v11 offset:64\n"
      "v_fmac_f32 v82, v222, v222\n"
      "v_mfma_f32_16x16x32_bf16 v[200:203], v[138:141], a[192:195], 0\n"
      "v_mul_f32 v165, v194, v165\n"
      "v_mul_f32 v186, v194, v186\n"
      "v_mfma_f32_16x16x32_bf16 v[200:203], v[104:107], a[196:199], v[200:203]\n"
      "ds_read_b128 v[138:141], v0 offset:10240\n"
      "v_fmac_f32 v172, v171, v171\n"
      "v_mfma_f32_16x16x32_bf16 v[200:203], v[150:153], a[200:203], v[200:203]\n"
      "ds_read_b128 v[104:107], v2 offset:24576\n"
      "v_fmac_f32 v82, v223, v223\n"
      "v_mfma_f32_16x16x32_bf16 v[200:203], v[116:119], a[204:207], v[200:203]\n"
      "s_nop 0\n"
      "v_permlane16_swap_b32_e64 v82, v172\n"
      "v_mfma_f32_16x16x32_bf16 v[200:203], v[126:129], a[208:211], v[200:203]\n"
      "ds_read_b128 v[116:119], v1 offset:16384\n"
      "v_mul_f32 v188, v194, v188\n"
      "v_mfma_f32_16x16x32_bf16 v[200:203], v[210:213], a[212:215], v[200:203]\n"
      "s_waitcnt lgkmcnt(3)\n"
      "v_mul_f32 v189, v194, v189\n"
      "v_add_f32 v82, v82, v172\n"
      "v_mfma_f32_16x16x32_bf16 v[210:213], a[0:3], v[156:159], 0\n"
      "v_mul_f32 v100, v78, v100\n"
      "v_mul_f32 v103, v78, v103\n"
      "v_mfma_f32_16x16x32_bf16 v[210:213], a[4:7], v[134:137], v[210:213]\n"
      "s_waitcnt lgkmcnt(2)\n"
      "v_cvt_pk_bf16_f32 v164, v164, v165\n"
      "v_cvt_pk_bf16_f32 v190, v190, v191\n"
      "v_mfma_f32_16x16x32_bf16 v[210:213], a[8:11], v[138:141], v[210:213]\n"
      "v_mov_b32 v172, v82\n"
      "v_cvt_pk_bf16_f32 v133, v162, v163\n"
      "v_mfma_f32_16x16x32_bf16 v[210:213], a[12:15], v[178:181], v[210:213]\n"
      "v_cvt_pk_bf16_f32 v165, v166, v167\n"
      "v_cvt_pk_bf16_f32 v174, v174, v175\n"
      "v_mfma_f32_16x16x32_bf16 v[210:213], a[16:19], v[96:99], v[210:213]\n"
      "s_waitcnt lgkmcnt(0)\n"
      "v_permlane32_swap_b32 v82, v172\n"
      "v_mfma_f32_16x16x32_bf16 v[210:213], a[20:23], v[116:119], v[210:213]\n"
      "v_add_f32 v82, v82, v172\n"
      "v_mul_f32 v123, v194, v123\n"
      "v_mfma_f32_16x16x32_bf16 v[150:153], a[24:27], v[156:159], 0\n"
      "v_rsq_f32 v82, v82\n"
      "v_mfma_f32_16x16x32_bf16 v[150:153], a[28:31], v[134:137], v[150:153]\n"
      "v_mov_b32 v172, v82\n"
      "v_cvt_pk_bf16_f32 v186, v186, v187\n"
      "v_mfma_f32_16x16x32_bf16 v[150:153], a[32:35], v[138:141], v[150:153]\n"
      "v_permlane16_swap_b32 v82, v172\n"
      "v_mfma_f32_16x16x32_bf16 v[150:153], a[36:39], v[178:181], v[150:153]\n"
      "v_fma_f32 v81, v210, v210, s51\n"
      "v_mul_f32 v168, v172, v168\n"
      "v_mfma_f32_16x16x32_bf16 v[150:153], a[40:43], v[96:99], v[150:153]\n"
      "v_mul_f32_e64 v169, v172, v169\n"
      "v_cvt_pk_bf16_f32 v167, v122, v123\n"
      "v_mfma_f32_16x16x32_bf16 v[150:153], a[44:47], v[116:119], v[150:153]\n"
      "v_cvt_pk_bf16_f32 v175, v176, v177\n"
      "v_mul_f32 v82, s50, v82\n"
      "v_mfma_f32_16x16x32_bf16 v[126:129], a[48:51], v[156:159], 0\n"
      "v_cvt_pk_bf16_f32 v198, v112, v113\n"
      "v_fmac_f32 v81, v211, v211\n"
      "v_mfma_f32_16x16x32_bf16 v[126:129], a[52:55], v[134:137], v[126:129]\n"
      "v_mul_f32 v147, v172, v147\n"
      "v_mul_f32 v143, v82, v143\n"
      "v_mfma_f32_16x16x32_bf16 v[126:129], a[56:59], v[138:141], v[126:129]\n"
      "v_mul_f32 v183, v82, v183\n"
      "v_mul_f32 v146, v172, v146\n"
      "v_mfma_f32_16x16x32_bf16 v[126:129], a[60:63], v[178:181], v[126:129]\n"
      "v_mul_f32_e64 v171, v172, v171\n"
      "v_cvt_pk_bf16_f32 v146, v146, v147\n"
      "v_mfma_f32_16x16x32_bf16 v[126:129], a[64:67], v[96:99], v[126:129]\n"
      "v_cvt_pk_bf16_f32 v176, v100, v101\n"
      "v_fmac_f32 v81, v212, v212\n"
      "v_mfma_f32_16x16x32_bf16 v[126:129], a[68:71], v[116:119], v[126:129]\n"
      "v_mul_f32 v204, v172, v204\n"
      "v_cvt_pk_bf16_f32 v177, v102, v103\n"
      "v_mfma_f32_16x16x32_bf16 v[160:163], a[72:75], v[156:159], 0\n"
      "v_cvt_pk_bf16_f32 v168, v168, v169\n"
      "v_mul_f32 v221, v82, v221\n"
      "v_mfma_f32_16x16x32_bf16 v[160:163], a[76:79], v[134:137], v[160:163]\n"
      "v_mul_f32 v149, v172, v149\n"
      "v_fmac_f32 v81, v213, v213\n"
      "v_mfma_f32_16x16x32_bf16 v[160:163], a[80:83], v[138:141], v[160:163]\n"
      "v_mul_f32 v205, v172, v205\n"
      "v_cvt_pk_bf16_f32 v199, v114, v115\n"
      "v_mfma_f32_16x16x32_bf16 v[160:163], a[84:87], v[178:181], v[160:163]\n"
      "v_cvt_pk_bf16_f32 v187, v188, v189\n"
      "v_mul_f32 v220, v82, v220\n"
      "v_mfma_f32_16x16x32_bf16 v[160:163], a[88:91], v[96:99], v[160:163]\n"
      "v_fmac_f32 v81, v150, v150\n"
      "v_cvt_pk_bf16_f32 v216, v200, v201\n"
      "v_mfma_f32_16x16x32_bf16 v[160:163], a[92:95], v[116:119], v[160:163]\n"
      "v_mul_f32 v222, v82, v222\n"
      "v_mul_f32 v148, v172, v148\n"
      "v_mfma_f32_16x16x32_bf16 v[100:103], a[96:99], v[156:159], 0\n"
      "v_fmac_f32_e64 v81, v151, v151\n"
      "v_cvt_pk_bf16_f32 v147, v148, v149\n"
      "v_mfma_f32_16x16x32_bf16 v[100:103], a[100:103], v[134:137], v[100:103]\n"
      "v_mul_f32 v223, v82, v223\n"
      "v_mfma_f32_16x16x32_bf16 v[100:103], a[104:107], v[138:141], v[100:103]\n"
      "v_fmac_f32 v81, v152, v152\n"
      "v_cvt_pk_bf16_f32 v166, v120, v121\n"
      "v_mfma_f32_16x16x32_bf16 v[100:103], a[108:111], v[178:181], v[100:103]\n"
      "v_cvt_pk_bf16_f32 v217, v202, v203\n"
      "v_fma_f32 v188, v160, v160, s51\n"
      "v_mfma_f32_16x16x32_bf16 v[100:103], a[112:115], v[96:99], v[100:103]\n"
      "v_fmac_f32 v81, v153, v153\n"
      "v_fmac_f32 v81, v126, v126\n"
      "v_mfma_f32_16x16x32_bf16 v[100:103], a[116:119], v[116:119], v[100:103]\n"
      "v_fmac_f32_e64 v188, v161, v161\n"
      "v_cvt_pk_bf16_f32 v148, v204, v205\n"
      "v_mfma_f32_16x16x32_bf16 v[120:123], a[120:123], v[156:159], 0\n"
      "v_cvt_pk_bf16_f32 v191, v192, v193\n"
      "v_fmac_f32_e64 v188, v162, v162\n"
      "v_mfma_f32_16x16x32_bf16 v[120:123], a[124:127], v[134:137], v[120:123]\n"
      "v_cvt_pk_bf16_f32 v220, v220, v221\n"
      "v_fmac_f32 v81, v127, v127\n"
      "v_mfma_f32_16x16x32_bf16 v[120:123], a[128:131], v[138:141], v[120:123]\n"
      "v_mul_f32 v170, v172, v170\n"
      "v_fmac_f32 v188, v163, v163\n"
      "v_mfma_f32_16x16x32_bf16 v[120:123], a[132:135], v[178:181], v[120:123]\n"
      "v_fmac_f32 v81, v128, v128\n"
      "v_fmac_f32 v81, v129, v129\n"
      "v_mfma_f32_16x16x32_bf16 v[120:123], a[136:139], v[96:99], v[120:123]\n"
      "v_fmac_f32 v188, v100, v100\n"
      "v_fmac_f32 v188, v101, v101\n"
      "v_mfma_f32_16x16x32_bf16 v[120:123], a[140:143], v[116:119], v[120:123]\n"
      "s_add_u32 s42, s42, 1\n"
      "v_mul_f32_e64 v144, v82, v144\n"
      "v_cvt_pk_bf16_f32 v169, v170, v171\n"
      "v_mfma_f32_16x16x32_bf16 v[200:203], v[156:159], a[144:147], 0\n"
      "v_cvt_pk_bf16_f32 v221, v222, v223\n"
      "v_fmac_f32_e64 v188, v102, v102\n"
      "v_mfma_f32_16x16x32_bf16 v[200:203], v[134:137], a[148:151], v[200:203]\n"
      "ds_read_u16 v78, v11\n"
      "v_fmac_f32 v188, v103, v103\n"
      "v_mfma_f32_16x16x32_bf16 v[200:203], v[138:141], a[152:155], v[200:203]\n"
      "v_fmac_f32 v188, v120, v120\n"
      "v_fmac_f32_e64 v188, v121, v121\n"
      "v_mfma_f32_16x16x32_bf16 v[200:203], v[178:181], a[156:159], v[200:203]\n"
      "v_fmac_f32 v188, v122, v122\n"
      "v_fmac_f32 v188, v123, v123\n"
      "v_mfma_f32_16x16x32_bf16 v[200:203], v[96:99], a[160:163], v[200:203]\n"
      "s_nop 0\n"
      "v_permlane16_swap_b32 v81, v188\n"
      "v_mfma_f32_16x16x32_bf16 v[200:203], v[116:119], a[164:167], v[200:203]\n"
      "v_add_f32 v81, v81, v188\n"
      "v_mov_b32 v188, v81\n"
      "v_mfma_f32_16x16x32_bf16 v[112:115], v[156:159], a[168:171], 0\n"
      "s_nop 0\n"
      "v_permlane32_swap_b32 v81, v188\n"
      "v_mfma_f32_16x16x32_bf16 v[112:115], v[134:137], a[172:175], v[112:115]\n"
      "v_mul_f32 v184, v82, v184\n"
      "v_add_f32 v81, v81, v188\n"
      "v_mfma_f32_16x16x32_bf16 v[112:115], v[138:141], a[176:179], v[112:115]\n"
      "v_mul_f32 v207, v172, v207\n"
      "v_mul_f32 v206, v172, v206\n"
      "v_mfma_f32_16x16x32_bf16 v[112:115], v[178:181], a[180:183], v[112:115]\n"
      "v_rsq_f32 v81, v81\n"
      "v_mfma_f32_16x16x32_bf16 v[112:115], v[96:99], a[184:187], v[112:115]\n"
      "ds_read_b128 v[192:195], v2 offset:18432\n"
      "v_mov_b32 v188, v81\n"
      "v_mfma_f32_16x16x32_bf16 v[112:115], v[116:119], a[188:191], v[112:115]\n"
      "s_nop 0\n"
      "v_permlane16_swap_b32 v81, v188\n"
      "v_mfma_f32_16x16x32_bf16 v[170:173], v[156:159], a[192:195], 0\n"
      "ds_read_b128 v[156:159], v2 offset:21504\n"
      "v_cvt_pk_bf16_f32 v149, v206, v207\n"
      "v_mfma_f32_16x16x32_bf16 v[170:173], v[134:137], a[196:199], v[170:173]\n"
      "v_mul_f32 v163, v188, v163\n"
      "v_mul_f32 v120, v188, v120\n"
      "v_mfma_f32_16x16x32_bf16 v[170:173], v[138:141], a[200:203], v[170:173]\n"
      "v_mul_f32 v182, v82, v182\n"
      "v_mul_f32 v161, v188, v161\n"
      "v_mfma_f32_16x16x32_bf16 v[170:173], v[178:181], a[204:207], v[170:173]\n"
      "ds_read_b128 v[204:207], v2 offset:30720\n"
      "v_mul_f32_e64 v162, v188, v162\n"
      "v_mfma_f32_16x16x32_bf16 v[170:173], v[96:99], a[208:211], v[170:173]\n"
      "v_cvt_pk_bf16_f32 v76, v200, v201\n"
      "v_mul_f32 v122, v188, v122\n"
      "v_mfma_f32_16x16x32_bf16 v[170:173], v[116:119], a[212:215], v[170:173]\n"
      "s_waitcnt lgkmcnt(2)\n"
      "v_mul_f32 v142, v82, v142\n"
      "v_cvt_pk_bf16_f32 v77, v202, v203\n"
      "v_mfma_f32_16x16x32_bf16 v[96:99], a[216:219], v[192:195], 0\n"
      "s_waitcnt lgkmcnt(1)\n"
      "v_mul_f32 v121, v188, v121\n"
      "v_mul_f32 v81, s50, v81\n"
      "v_mfma_f32_16x16x32_bf16 v[96:99], a[220:223], v[156:159], v[96:99]\n"
      "ds_read_b128 v[200:203], v2 offset:27648\n"
      "v_mul_f32 v145, v82, v145\n"
      "v_mfma_f32_16x16x32_bf16 v[96:99], a[224:227], v[104:107], v[96:99]\n"
      "v_mul_f32 v160, v188, v160\n"
      "v_mul_f32_e64 v210, v81, v210\n"
      "v_mfma_f32_16x16x32_bf16 v[116:119], a[240:243], v[192:195], 0\n"
      "ds_read_b128 v[178:181], v2 offset:33792\n"
      "v_cvt_pk_bf16_f32 v84, v170, v171\n"
      "v_mfma_f32_16x16x32_bf16 v[116:119], a[244:247], v[156:159], v[116:119]\n"
      "v_cvt_pk_bf16_f32 v142, v142, v143\n"
      "v_mul_f32 v152, v81, v152\n"
      "v_mfma_f32_16x16x32_bf16 v[116:119], a[248:251], v[104:107], v[116:119]\n"
      "v_mul_f32 v101, v188, v101\n"
      "v_cvt_pk_bf16_f32 v143, v144, v145\n"
      "v_mfma_f32_16x16x32_bf16 v[134:137], v[232:235], v[192:195], 0\n"
      "v_mul_f32 v127, v81, v127\n"
      "v_mul_f32 v213, v81, v213\n"
      "v_mfma_f32_16x16x32_bf16 v[134:137], v[236:239], v[156:159], v[134:137]\n"
      "v_mul_f32 v100, v188, v100\n"
      "v_mul_f32 v123, v188, v123\n"
      "v_mfma_f32_16x16x32_bf16 v[134:137], v[240:243], v[104:107], v[134:137]\n"
      "s_waitcnt lgkmcnt(1)\n"
      "v_mul_f32 v102, v188, v102\n"
      "v_mul_f32_e64 v151, v81, v151\n"
      "v_mfma_f32_16x16x32_bf16 v[96:99], a[228:231], v[200:203], v[96:99]\n"
      "ds_read_b128 v[192:195], v10 offset:32\n"
      "v_cvt_pk_bf16_f32 v160, v160, v161\n"
      "v_mfma_f32_16x16x32_bf16 v[96:99], a[232:235], v[204:207], v[96:99]\n"
      "s_waitcnt lgkmcnt(1)\n"
      "v_cvt_pk_bf16_f32 v120, v120, v121\n"
      "v_mul_f32 v185, v82, v185\n"
      "v_mfma_f32_16x16x32_bf16 v[96:99], a[236:239], v[178:181], v[96:99]\n"
      "ds_read_b64 v[222:223], v10 offset:24\n"
      "v_mul_f32_e64 v128, v81, v128\n"
      "v_mfma_f32_16x16x32_bf16 v[116:119], a[252:255], v[200:203], v[116:119]\n"
      "ds_read_u16 v82, v11 offset:32\n"
      "v_cvt_pk_bf16_f32 v145, v184, v185\n"
      "v_mfma_f32_16x16x32_bf16 v[116:119], v[224:227], v[204:207], v[116:119]\n"
      "v_mul_f32 v103, v188, v103\n"
      "v_mul_f32 v129, v81, v129\n"
      "v_mfma_f32_16x16x32_bf16 v[116:119], v[228:231], v[178:181], v[116:119]\n"
      "v_mul_f32_e64 v126, v81, v126\n"
      "v_cvt_pk_bf16_f32 v161, v162, v163\n"
      "v_mfma_f32_16x16x32_bf16 v[134:137], v[244:247], v[200:203], v[134:137]\n"
      "ds_read_b128 v[138:141], v2 offset:18688\n"
      "v_mul_f32_e64 v211, v81, v211\n"
      "v_mfma_f32_16x16x32_bf16 v[134:137], v[248:251], v[204:207], v[134:137]\n"
      "v_cvt_pk_bf16_f32 v163, v102, v103\n"
      "v_cvt_pk_bf16_f32 v144, v182, v183\n"
      "v_mfma_f32_16x16x32_bf16 v[134:137], v[252:255], v[178:181], v[134:137]\n"
      "v_cvt_pk_bf16_f32 v80, v112, v113\n"
      "v_cvt_pk_bf16_f32 v162, v100, v101\n"
      "v_mfma_f32_16x16x32_bf16 v[104:107], v[164:167], v[174:177], v[40:43]\n"
      "ds_read_b128 v[100:103], v2 offset:21760\n"
      "v_cvt_pk_bf16_f32 v210, v210, v211\n"
      "v_mfma_f32_16x16x32_bf16 v[154:157], v[146:149], v[174:177], v[44:47]\n"
      "s_waitcnt lgkmcnt(2)\n"
      "v_mul_f32 v212, v81, v212\n"
      "v_cvt_pk_bf16_f32 v121, v122, v123\n"
      "v_mfma_f32_16x16x32_bf16 v[182:185], v[160:163], v[174:177], v[48:51]\n"
      "ds_read_b128 v[122:125], v2 offset:24832\n"
      "v_mul_f32 v150, v81, v150\n"
      "v_mfma_f32_16x16x32_bf16 v[178:181], v[192:195], v[174:177], v[24:27]\n"
      "s_waitcnt lgkmcnt(2)\n"
      "v_cvt_pk_bf16_f32 v126, v126, v127\n"
      "v_cvt_pk_bf16_f32 v211, v212, v213\n"
      "v_mfma_f32_16x16x16_bf16 v[104:107], v[186:187], v[190:191], v[104:107]\n"
      "v_mul_f32_e64 v153, v81, v153\n"
      "v_cvt_pk_bf16_f32 v212, v150, v151\n"
      "v_cvt_pk_bf16_f32 v81, v114, v115\n"
      "v_mfma_f32_16x16x16_bf16 v[154:157], v[168:169], v[190:191], v[154:157]\n"
      "v_fma_f32 v150, v96, v96, s52\n"
      "s_waitcnt vmcnt(1)\n"
      "v_or3_b32 v108, v108, v109, v110\n"
      "v_mul_f32 v151, v97, v97\n"
      "v_mfma_f32_16x16x16_bf16 v[182:185], v[120:121], v[190:191], v[182:185]\n"
      "v_exp_f32_e64 v105, v105\n"
      "v_cvt_pk_bf16_f32 v213, v152, v153\n"
      "v_mfma_f32_16x16x16_bf16 v[178:181], v[222:223], v[190:191], v[178:181]\n"
      "v_cvt_pk_bf16_f32 v85, v172, v173\n"
      "v_exp_f32_e64 v107, v107\n"
      "v_mfma_f32_16x16x32_bf16 v[200:203], a[216:219], v[138:141], 0\n"
      "s_waitcnt lgkmcnt(0)\n"
      "v_exp_f32 v157, v157\n"
      "v_mfma_f32_16x16x32_bf16 v[200:203], a[220:223], v[100:103], v[200:203]\n"
      "v_exp_f32_e64 v156, v156\n"
      "v_mfma_f32_16x16x32_bf16 v[200:203], a[224:227], v[122:125], v[200:203]\n"
      "v_exp_f32 v104, v104\n"
      "v_mfma_f32_16x16x32_bf16 v[112:115], a[240:243], v[138:141], 0\n"
      "v_exp_f32 v154, v154\n"
      "v_mfma_f32_16x16x32_bf16 v[112:115], a[244:247], v[100:103], v[112:115]\n"
      "v_exp_f32 v183, v183\n"
      "v_mfma_f32_16x16x32_bf16 v[112:115], a[248:251], v[122:125], v[112:115]\n"
      "v_cvt_pk_bf16_f32 v104, v104, v105\n"
      "v_fmac_f32 v151, v99, v99\n"
      "v_mfma_f32_16x16x32_bf16 v[204:207], v[232:235], v[138:141], 0\n"
      "v_exp_f32 v178, v178\n"
      "v_mfma_f32_16x16x32_bf16 v[204:207], v[236:239], v[100:103], v[204:207]\n"
      "v_exp_f32_e64 v106, v106\n"
      "v_mfma_f32_16x16x32_bf16 v[204:207], v[240:243], v[122:125], v[204:207]\n"
      "s_waitcnt vmcnt(0)\n"
      "v_or3_b32 v108, v108, v111, v90\n"
      "v_cvt_pk_bf16_f32 v127, v128, v129\n"
      "v_mfma_f32_16x16x32_bf16 v[174:177], v[164:167], v[142:145], v[52:55]\n"
      "v_exp_f32_e64 v182, v182\n"
      "v_mfma_f32_16x16x32_bf16 v[170:173], v[146:149], v[142:145], v[56:59]\n"
      "v_exp_f32 v184, v184\n"
      "v_mfma_f32_16x16x32_bf16 v[122:125], v[160:163], v[142:145], v[60:63]\n"
      "v_exp_f32 v185, v185\n"
      "v_mfma_f32_16x16x32_bf16 v[100:103], v[192:195], v[142:145], v[26:29]\n"
      "v_cvt_pk_bf16_f32 v105, v106, v107\n"
      "v_cvt_pk_bf16_f32 v90, v178, 0\n"
      "v_mfma_f32_16x16x16_bf16 v[174:177], v[186:187], v[220:221], v[174:177]\n"
      "v_exp_f32 v155, v155\n"
      "v_fmac_f32 v150, v98, v98\n"
      "v_mfma_f32_16x16x16_bf16 v[170:173], v[168:169], v[220:221], v[170:173]\n"
      "v_cvt_pk_bf16_f32 v106, v154, v155\n"
      "v_cvt_pk_bf16_f32 v107, v156, v157\n"
      "v_cvt_pk_bf16_f32 v88, v182, v183\n"
      "v_mfma_f32_16x16x16_bf16 v[122:125], v[120:121], v[220:221], v[122:125]\n"
      "v_cvt_pk_bf16_f32 v89, v184, v185\n"
      "v_exp_f32 v174, v174\n"
      "v_mfma_f32_16x16x16_bf16 v[100:103], v[222:223], v[220:221], v[100:103]\n"
      "v_exp_f32 v175, v175\n"
      "v_exp_f32 v176, v176\n"
      "v_mfma_f32_16x16x32_bf16 v[156:159], v[32:35], v[104:107], 0\n"
      "v_exp_f32 v170, v170\n"
      "v_mfma_f32_16x16x32_bf16 v[156:159], v[32:35], v[88:91], v[156:159]\n"
      "v_exp_f32_e64 v173, v173\n"
      "v_mfma_f32_16x16x32_bf16 v[218:221], v[130:133], v[104:107], 0\n"
      "v_exp_f32 v172, v172\n"
      "v_mfma_f32_16x16x32_bf16 v[218:221], v[76:79], v[88:91], v[218:221]\n"
      "v_exp_f32 v171, v171\n"
      "v_mfma_f32_16x16x32_bf16 v[138:141], v[196:199], v[104:107], 0\n"
      "v_exp_f32_e64 v177, v177\n"
      "v_mfma_f32_16x16x32_bf16 v[138:141], v[80:83], v[88:91], v[138:141]\n"
      "ds_read_b128 v[178:181], v2 offset:30976\n"
      "v_cvt_pk_bf16_f32 v174, v174, v175\n"
      "v_mfma_f32_16x16x32_bf16 v[182:185], v[214:217], v[104:107], 0\n"
      "ds_read_b128 v[104:107], v2 offset:27904\n"
      "v_cvt_pk_bf16_f32 v175, v176, v177\n"
      "v_mfma_f32_16x16x32_bf16 v[182:185], v[84:87], v[88:91], v[182:185]\n"
      "ds_write_b32 v31, v108 offset:4608\n"
      "v_mfma_f32_16x16x32_bf16 v[152:155], v[164:167], v[210:213], v[64:67]\n"
      "v_rcp_f32 v156, v156\n"
      "v_exp_f32 v100, v100\n"
      "v_cvt_pk_bf16_f32 v177, v172, v173\n"
      "v_pk_fma_f32 v[150:151], v[116:117], v[116:117], v[150:151]\n"
      "v_pk_mul_f32 v[138:139], v[138:139], v[156:157] op_sel_hi:[1,0]\n"
      "v_pk_mul_f32 v[218:219], v[218:219], v[156:157] op_sel_hi:[1,0]\n"
      "v_pk_mul_f32 v[182:183], v[182:183], v[156:157] op_sel_hi:[1,0]\n"
      "v_pk_mul_f32 v[220:221], v[220:221], v[156:157] op_sel_hi:[1,0]\n"
      "v_mfma_f32_16x16x32_bf16 v[188:191], v[146:149], v[210:213], v[68:71]\n"
      "v_cvt_pk_bf16_f32 v176, v170, v171\n"
      "v_cvt_pk_bf16_f32 v218, v218, v219\n"
      "v_mfma_f32_16x16x32_bf16 v[170:173], v[160:163], v[210:213], v[72:75]\n"
      "s_waitcnt lgkmcnt(1)\n"
      "v_mul_f32 v141, v156, v141\n"
      "v_mul_f32_e64 v184, v156, v184\n"
      "v_mfma_f32_16x16x32_bf16 v[108:111], v[192:195], v[210:213], v[28:31]\n"
      "ds_read_b128 v[208:211], v2 offset:34048\n"
      "v_mul_f32 v185, v156, v185\n"
      "v_mfma_f32_16x16x16_bf16 v[152:155], v[186:187], v[126:127], v[152:155]\n"
      "v_mul_f32 v140, v156, v140\n"
      "v_cvt_pk_bf16_f32 v219, v220, v221\n"
      "v_exp_f32 v124, v124\n"
      "v_mfma_f32_16x16x16_bf16 v[188:191], v[168:169], v[126:127], v[188:191]\n"
      "v_cvt_pk_bf16_f32 v220, v138, v139\n"
      "v_exp_f32 v125, v125\n"
      "v_mfma_f32_16x16x16_bf16 v[170:173], v[120:121], v[126:127], v[170:173]\n"
      "v_cvt_pk_bf16_f32 v221, v140, v141\n"
      "v_exp_f32 v154, v154\n"
      "v_mfma_f32_16x16x16_bf16 v[108:111], v[222:223], v[126:127], v[108:111]\n"
      "v_cvt_pk_bf16_f32 v93, v124, v125\n"
      "v_exp_f32 v122, v122\n"
      "v_mfma_f32_16x16x32_bf16 v[200:203], a[228:231], v[104:107], v[200:203]\n"
      "v_cvt_pk_bf16_f32 v94, v100, 0\n"
      "v_cvt_pk_bf16_f32 v182, v182, v183\n"
      "v_mfma_f32_16x16x32_bf16 v[200:203], a[232:235], v[178:181], v[200:203]\n"
      "s_waitcnt lgkmcnt(0)\n"
      "v_exp_f32 v123, v123\n"
      "v_mfma_f32_16x16x32_bf16 v[200:203], a[236:239], v[208:211], v[200:203]\n"
      "v_exp_f32 v170, v170\n"
      "v_mfma_f32_16x16x32_bf16 v[112:115], a[252:255], v[104:107], v[112:115]\n"
      "v_exp_f32 v172, v172\n"
      "v_mfma_f32_16x16x32_bf16 v[112:115], v[224:227], v[178:181], v[112:115]\n"
      "v_exp_f32 v171, v171\n"
      "v_mfma_f32_16x16x32_bf16 v[112:115], v[228:231], v[208:211], v[112:115]\n"
      "ds_read_b128 v[156:159], v7 offset:18432\n"
      "v_exp_f32 v155, v155\n"
      "v_mfma_f32_16x16x32_bf16 v[204:207], v[244:247], v[104:107], v[204:207]\n"
      "ds_read_b128 v[104:107], v2 offset:18944\n"
      "v_cvt_pk_bf16_f32 v92, v122, v123\n"
      "v_mfma_f32_16x16x32_bf16 v[204:207], v[248:251], v[178:181], v[204:207]\n"
      "ds_read_b64 v[110:111], v10 offset:16\n"
      "v_cvt_pk_bf16_f32 v88, v170, v171\n"
      "v_mfma_f32_16x16x32_bf16 v[204:207], v[252:255], v[208:211], v[204:207]\n"
      "ds_read_b128 v[208:211], v10\n"
      "v_cvt_pk_bf16_f32 v183, v184, v185\n"
      "v_mfma_f32_16x16x32_bf16 v[126:129], v[32:35], v[174:177], 0\n"
      "ds_write_b128 v3, v[218:221]\n"
      "v_mfma_f32_16x16x32_bf16 v[126:129], v[32:35], v[92:95], v[126:129]\n"
      "s_add_u32 s43, s43, 1\n"
      "v_exp_f32 v189, v189\n"
      "v_mfma_f32_16x16x32_bf16 v[218:221], v[130:133], v[174:177], 0\n"
      "v_exp_f32_e64 v108, v108\n"
      "v_mfma_f32_16x16x32_bf16 v[218:221], v[76:79], v[92:95], v[218:221]\n"
      "ds_read_b128 v[178:181], v7 offset:22528\n"
      "v_fmac_f32 v150, v118, v118\n"
      "v_mfma_f32_16x16x32_bf16 v[138:141], v[196:199], v[174:177], 0\n"
      "s_waitcnt lgkmcnt(2)\n"
      "v_exp_f32_e64 v153, v153\n"
      "v_mfma_f32_16x16x32_bf16 v[138:141], v[80:83], v[92:95], v[138:141]\n"
      "buffer_store_dwordx4 v[156:159], v20, s[24:27], s46 offen sc1\n"
      "v_mfma_f32_16x16x32_bf16 v[122:125], v[214:217], v[174:177], 0\n"
      "v_exp_f32_e64 v173, v173\n"
      "v_mfma_f32_16x16x32_bf16 v[122:125], v[84:87], v[92:95], v[122:125]\n"
      "v_rcp_f32 v126, v126\n"
      "v_mfma_f32_16x16x32_bf16 v[142:145], v[208:211], v[164:167], v[30:33]\n"
      "v_mul_f32 v185, v201, v201\n"
      "v_fma_f32 v184, v200, v200, s52\n"
      "v_mfma_f32_16x16x32_bf16 v[156:159], v[208:211], v[146:149], v[30:33]\n"
      "ds_read_b128 v[164:167], v7 offset:26624\n"
      "v_fmac_f32 v151, v119, v119\n"
      "v_mfma_f32_16x16x32_bf16 v[174:177], v[208:211], v[160:163], v[30:33]\n"
      "s_waitcnt lgkmcnt(1)\n"
      "v_exp_f32_e64 v152, v152\n"
      "v_mfma_f32_16x16x32_bf16 v[100:103], v[208:211], v[192:195], v[30:33]\n"
      "ds_read_b128 v[208:211], v2 offset:25088\n"
      "v_cvt_pk_bf16_f32 v152, v152, v153\n"
      "v_mfma_f32_16x16x16_bf16 v[142:145], v[110:111], v[186:187], v[142:145]\n"
      "ds_read_b128 v[192:195], v2 offset:22016\n"
      "v_cvt_pk_bf16_f32 v153, v154, v155\n"
      "v_mfma_f32_16x16x16_bf16 v[156:159], v[110:111], v[168:169], v[156:159]\n"
      "buffer_store_dwordx4 v[178:181], v21, s[24:27], s46 offen sc1\n"
      "v_pk_fma_f32 v[184:185], v[202:203], v[202:203], v[184:185]\n"
      "v_pk_mul_f32 v[124:125], v[124:125], v[126:127] op_sel_hi:[1,0]\n"
      "v_pk_fma_f32 v[150:151], v[134:135], v[134:135], v[150:151]\n"
      "v_pk_mul_f32 v[220:221], v[220:221], v[126:127] op_sel_hi:[1,0]\n"
      "v_pk_mul_f32 v[218:219], v[218:219], v[126:127] op_sel_hi:[1,0]\n"
      "v_pk_mul_f32 v[140:141], v[140:141], v[126:127] op_sel_hi:[1,0]\n"
      "v_pk_fma_f32 v[184:185], v[112:113], v[112:113], v[184:185]\n"
      "v_pk_fma_f32 v[184:185], v[114:115], v[114:115], v[184:185]\n"
      "v_mfma_f32_16x16x16_bf16 v[174:177], v[110:111], v[120:121], v[174:177]\n"
      "v_exp_f32_e64 v190, v190\n"
      "v_cndmask_b32 v142, v142, v156, s[58:59]\n"
      "v_mfma_f32_16x16x16_bf16 v[100:103], v[110:111], v[222:223], v[100:103]\n"
      "v_cvt_pk_bf16_f32 v218, v218, v219\n"
      "v_exp_f32 v191, v191\n"
      "v_mfma_f32_16x16x32_bf16 v[156:159], a[216:219], v[104:107], 0\n"
      "s_waitcnt lgkmcnt(0)\n"
      "v_exp_f32_e64 v188, v188\n"
      "v_mfma_f32_16x16x32_bf16 v[156:159], a[220:223], v[192:195], v[156:159]\n"
      "v_cvt_pk_bf16_f32 v89, v172, v173\n"
      "v_cvt_pk_bf16_f32 v90, v108, 0\n"
      "v_mfma_f32_16x16x32_bf16 v[156:159], a[224:227], v[208:211], v[156:159]\n"
      "ds_read_b128 v[168:171], v7 offset:30720\n"
      "v_cvt_pk_bf16_f32 v154, v188, v189\n"
      "v_mfma_f32_16x16x32_bf16 v[146:149], a[240:243], v[104:107], 0\n"
      "buffer_store_dwordx4 v[164:167], v22, s[24:27], s46 offen sc1\n"
      "v_pk_mul_f32 v[122:123], v[122:123], v[126:127] op_sel_hi:[1,0]\n"
      "v_pk_fma_f32 v[150:151], v[136:137], v[136:137], v[150:151]\n"
      "v_pk_fma_f32 v[184:185], v[204:205], v[204:205], v[184:185]\n"
      "v_pk_mul_f32 v[138:139], v[138:139], v[126:127] op_sel_hi:[1,0]\n"
      "v_pk_fma_f32 v[184:185], v[206:207], v[206:207], v[184:185]\n"
      "v_mfma_f32_16x16x32_bf16 v[146:149], a[244:247], v[192:195], v[146:149]\n"
      "ds_read_b128 v[176:179], v2 offset:28160\n"
      "v_cvt_pk_bf16_f32 v219, v220, v221\n"
      "v_mfma_f32_16x16x32_bf16 v[146:149], a[248:251], v[208:211], v[146:149]\n"
      "v_cvt_pk_bf16_f32 v221, v140, v141\n"
      "v_cvt_pk_bf16_f32 v220, v138, v139\n"
      "v_mfma_f32_16x16x32_bf16 v[160:163], v[232:235], v[104:107], 0\n"
      "ds_write_b128 v3, v[218:221] offset:256\n"
      "v_mfma_f32_16x16x32_bf16 v[160:163], v[236:239], v[192:195], v[160:163]\n"
      "v_cndmask_b32 v142, v142, v174, s[60:61]\n"
      "v_cvt_pk_bf16_f32 v155, v190, v191\n"
      "v_mfma_f32_16x16x32_bf16 v[160:163], v[240:243], v[208:211], v[160:163]\n"
      "ds_read_b128 v[208:211], v2 offset:31232\n"
      "v_cndmask_b32 v142, v142, v100, s[62:63]\n"
      "v_mfma_f32_16x16x32_bf16 v[104:107], v[32:35], v[152:155], 0\n"
      "ds_read_b128 v[126:129], v2 offset:34304\n"
      "v_add_f32 v150, v150, v151\n"
      "v_mfma_f32_16x16x32_bf16 v[104:107], v[32:35], v[88:91], v[104:107]\n"
      "s_waitcnt lgkmcnt(3)\n"
      "v_exp_f32_e64 v142, v142\n"
      "v_mfma_f32_16x16x32_bf16 v[188:191], v[130:133], v[152:155], 0\n"
      "ds_write_b64 v4, v[182:183]\n"
      "v_mfma_f32_16x16x32_bf16 v[188:191], v[76:79], v[88:91], v[188:191]\n"
      "buffer_store_dwordx4 v[168:171], v23, s[24:27], s46 offen sc1\n"
      "s_nop 1\n"
      "v_mfma_f32_16x16x32_bf16 v[168:171], v[196:199], v[152:155], 0\n"
      "v_add_f32 v184, v184, v185\n"
      "v_cvt_pk_bf16_f32 v142, v142, v142\n"
      "v_mfma_f32_16x16x32_bf16 v[168:171], v[80:83], v[88:91], v[168:171]\n"
      "ds_write_b16 v13, v142\n"
      "v_mfma_f32_16x16x32_bf16 v[106:109], v[214:217], v[152:155], 0\n"
      "v_rcp_f32_e64 v104, v104\n"
      "v_mfma_f32_16x16x32_bf16 v[106:109], v[84:87], v[88:91], v[106:109]\n"
      "s_waitcnt lgkmcnt(2)\n"
      "v_permlane16_swap_b32 v150, v184\n"
      "v_mfma_f32_16x16x32_bf16 v[156:159], a[228:231], v[176:179], v[156:159]\n"
      "ds_read_b128 v[218:221], v14\n"
      "v_mul_f32_e64 v190, v104, v190\n"
      "v_mfma_f32_16x16x32_bf16 v[156:159], a[232:235], v[208:211], v[156:159]\n"
      "ds_read_b128 v[140:143], v7 offset:34816\n"
      "v_cvt_pk_bf16_f32 v122, v122, v123\n"
      "v_mfma_f32_16x16x32_bf16 v[156:159], a[236:239], v[126:129], v[156:159]\n"
      "v_cvt_pk_bf16_f32 v123, v124, v125\n"
      "v_mul_f32 v189, v104, v189\n"
      "v_mfma_f32_16x16x32_bf16 v[146:149], a[252:255], v[176:179], v[146:149]\n"
      "v_mul_f32 v191, v104, v191\n"
      "v_mul_f32 v188, v104, v188\n"
      "v_mfma_f32_16x16x32_bf16 v[146:149], v[224:227], v[208:211], v[146:149]\n"
      "ds_write_b64 v4, v[122:123] offset:256\n"
      "v_cvt_pk_bf16_f32 v188, v188, v189\n"
      "v_mul_f32 v125, v157, v157\n"
      "v_pk_mul_f32 v[168:169], v[168:169], v[104:105] op_sel_hi:[1,0]\n"
      "v_pk_mul_f32 v[170:171], v[170:171], v[104:105] op_sel_hi:[1,0]\n"
      "v_mfma_f32_16x16x32_bf16 v[146:149], v[228:231], v[126:129], v[146:149]\n"
      "v_cvt_pk_bf16_f32 v189, v190, v191\n"
      "v_cvt_pk_bf16_f32 v191, v170, v171\n"
      "v_mfma_f32_16x16x32_bf16 v[160:163], v[244:247], v[176:179], v[160:163]\n"
      "ds_read_b128 v[174:177], v14 offset:64\n"
      "v_fma_f32 v124, v156, v156, s52\n"
      "v_mfma_f32_16x16x32_bf16 v[160:163], v[248:251], v[208:211], v[160:163]\n"
      "s_waitcnt lgkmcnt(2)\n"
      "v_cvt_pk_bf16_f32 v190, v168, v169\n"
      "v_add_f32 v150, v150, v184\n"
      "v_mfma_f32_16x16x32_bf16 v[160:163], v[252:255], v[126:129], v[160:163]\n"
      "ds_write_b128 v3, v[188:191] offset:512\n"
      "v_pk_fma_f32 v[124:125], v[158:159], v[158:159], v[124:125]\n"
      "v_pk_mul_f32 v[106:107], v[106:107], v[104:105] op_sel_hi:[1,0]\n"
      "v_pk_fma_f32 v[124:125], v[146:147], v[146:147], v[124:125]\n"
      "v_pk_mul_f32 v[108:109], v[108:109], v[104:105] op_sel_hi:[1,0]\n"
      "v_cvt_pk_bf16_f32 v106, v106, v107\n"
      "v_pk_fma_f32 v[124:125], v[148:149], v[148:149], v[124:125]\n"
      "v_cvt_pk_bf16_f32 v107, v108, v109\n"
      "v_pk_fma_f32 v[124:125], v[160:161], v[160:161], v[124:125]\n"
      "v_pk_fma_f32 v[124:125], v[162:163], v[162:163], v[124:125]\n"
      "v_add_f32 v124, v124, v125\n"
      "v_mov_b32 v184, v124\n"
      "v_mfma_f32_16x16x32_bf16 v[208:211], v[218:221], v[32:35], 0\n"
      "ds_write_b64 v4, v[106:107] offset:512\n"
      "v_permlane16_swap_b32 v124, v184\n"
      "v_add_f32 v124, v124, v184\n"
      "s_nop 1\n"
      "v_permlane32_swap_b32 v150, v124\n"
      "v_add_f32 v150, v150, v124\n"
      "s_and_b32 s86, s86, -4\n"
      "ds_write_b32 v31, v150\n"
      "s_cmp_ge_u32 s95, 0x40000000\n"
      "s_cselect_b64 vcc, -1, s[56:57]\n"
      "v_add_u32 v12, 32, v12\n"
      "buffer_store_dwordx4 v[140:143], v25, s[24:27], s46 offen sc1\n"
      "s_waitcnt lgkmcnt(2)\n"
      "v_mfma_f32_16x16x32_bf16 v[208:211], v[174:177], v[32:35], v[208:211]\n"
      "s_waitcnt lgkmcnt(0)\n"
      "s_barrier\n"
      "s_cbranch_vccz L_loop1_%=\n"
      "s_branch L_disp1_%=\n"
      "s_nop 7\n"
      "s_nop 7\n"
      "L_max1_%=:\n"
      "v_mfma_f32_16x16x32_bf16 v[168:171], v[218:221], v[130:133], 0\n"
      "ds_read_b128 v[124:127], v0 offset:18432\n"
      "ds_read_b128 v[108:111], v9\n"
      "v_mfma_f32_16x16x32_bf16 v[168:171], v[174:177], v[76:79], v[168:171]\n"
      "s_add_u32 m0, s64, 0\n"
      "ds_read_b128 v[120:123], v0 offset:24576\n"
      "v_mfma_f32_16x16x32_bf16 v[150:153], v[218:221], v[196:199], 0\n"
      "buffer_load_dwordx4 v16, s[16:19], s84 offen  lds\n"
      "v_mfma_f32_16x16x32_bf16 v[150:153], v[174:177], v[80:83], v[150:153]\n"
      "ds_read_b128 v[182:185], v1 offset:18432\n"
      "v_mfma_f32_16x16x32_bf16 v[194:197], v[218:221], v[214:217], 0\n"
      "ds_read_b128 v[142:145], v0 offset:20480\n"
      "v_readlane_b32 s47, v38, s43\n"
      "v_mfma_f32_16x16x32_bf16 v[194:197], v[174:177], v[84:87], v[194:197]\n"
      "s_add_u32 m0, s64, 4096\n"
      "s_waitcnt lgkmcnt(4)\n"
      "v_mfma_f32_16x16x32_bf16 v[100:103], a[0:3], v[124:127], 0\n"
      "ds_read_b128 v[178:181], v0 offset:26624\n"
      "v_cndmask_b32 v168, v168, v150, s[58:59]\n"
      "v_mfma_f32_16x16x32_bf16 v[164:167], a[24:27], v[124:127], 0\n"
      "s_waitcnt lgkmcnt(4)\n"
      "v_add_f32 v110, v110, v111\n"
      "v_add_f32_e64 v108, v108, v109\n"
      "v_mfma_f32_16x16x32_bf16 v[138:141], a[48:51], v[124:127], 0\n"
      "buffer_load_dwordx4 v17, s[16:19], s84 offen  lds\n"
      "v_mfma_f32_16x16x32_bf16 v[170:173], a[72:75], v[124:127], 0\n"
      "ds_read_b128 v[190:193], v0 offset:30720\n"
      "v_add_f32 v108, v108, v110\n"
      "v_mfma_f32_16x16x32_bf16 v[186:189], a[96:99], v[124:127], 0\n"
      "s_waitcnt lgkmcnt(2)\n"
      "v_rsq_f32_e64 v108, v108\n"
      "v_mfma_f32_16x16x32_bf16 v[174:177], a[120:123], v[124:127], 0\n"
      "ds_read_b128 v[152:155], v1 offset:30720\n"
      "v_mul_f32 v108, 0x415db3d7, v108\n"
      "v_mfma_f32_16x16x32_bf16 v[100:103], a[4:7], v[182:185], v[100:103]\n"
      "ds_read_b128 v[220:223], v1 offset:20480\n"
      "v_mov_b32 v110, v108\n"
      "v_mfma_f32_16x16x32_bf16 v[100:103], a[8:11], v[120:123], v[100:103]\n"
      "s_nop 0\n"
      "v_permlane32_swap_b32_e64 v108, v110\n"
      "v_mfma_f32_16x16x32_bf16 v[164:167], a[28:31], v[182:185], v[164:167]\n"
      "ds_read_b128 v[104:107], v1 offset:24576\n"
      "v_mov_b32_e64 v109, v108\n"
      "v_mfma_f32_16x16x32_bf16 v[164:167], a[32:35], v[120:123], v[164:167]\n"
      "s_add_u32 m0, s64, 8192\n"
      "v_mul_f32 v163, v110, v163\n"
      "v_mul_f32 v157, v110, v157\n"
      "v_mfma_f32_16x16x32_bf16 v[138:141], a[52:55], v[182:185], v[138:141]\n"
      "buffer_load_dwordx4 v18, s[16:19], s84 offen  lds\n"
      "v_mfma_f32_16x16x32_bf16 v[138:141], a[56:59], v[120:123], v[138:141]\n"
      "v_permlane16_swap_b32 v108, v109\n"
      "v_mfma_f32_16x16x32_bf16 v[170:173], a[76:79], v[182:185], v[170:173]\n"
      "s_add_u32 m0, s64, 12288\n"
      "v_mul_f32 v200, v109, v200\n"
      "v_cndmask_b32 v168, v168, v194, s[60:61]\n"
      "v_mfma_f32_16x16x32_bf16 v[170:173], a[80:83], v[120:123], v[170:173]\n"
      "v_mul_f32 v201, v109, v201\n"
      "v_mul_f32 v115, v109, v115\n"
      "v_mfma_f32_16x16x32_bf16 v[186:189], a[100:103], v[182:185], v[186:189]\n"
      "v_mul_f32 v136, v108, v136\n"
      "v_mul_f32 v96, v108, v96\n"
      "v_mfma_f32_16x16x32_bf16 v[186:189], a[104:107], v[120:123], v[186:189]\n"
      "buffer_load_dwordx4 v19, s[16:19], s84 offen  lds\n"
      "v_mfma_f32_16x16x32_bf16 v[174:177], a[124:127], v[182:185], v[174:177]\n"
      "s_add_u32 m0, s65, 16384\n"
      "v_mul_f32 v97, v108, v97\n"
      "v_cvt_pk_bf16_f32 v200, v200, v201\n"
      "v_mfma_f32_16x16x32_bf16 v[174:177], a[128:131], v[120:123], v[174:177]\n"
      "s_waitcnt lgkmcnt(0)\n"
      "v_mul_f32 v99, v108, v99\n"
      "v_mul_f32 v146, v110, v146\n"
      "v_mfma_f32_16x16x32_bf16 v[100:103], a[12:15], v[104:107], v[100:103]\n"
      "v_mul_f32 v202, v109, v202\n"
      "v_mul_f32 v162, v110, v162\n"
      "v_mfma_f32_16x16x32_bf16 v[100:103], a[16:19], v[190:193], v[100:103]\n"
      "v_mul_f32 v119, v108, v119\n"
      "v_mul_f32 v156, v110, v156\n"
      "v_mfma_f32_16x16x32_bf16 v[100:103], a[20:23], v[152:155], v[100:103]\n"
      "buffer_load_dwordx4 v15, s[16:19], s84 offen  lds\n"
      "v_mfma_f32_16x16x32_bf16 v[164:167], a[36:39], v[104:107], v[164:167]\n"
      "v_mul_f32 v134, v108, v134\n"
      "v_mul_f32 v135, v108, v135\n"
      "v_mfma_f32_16x16x32_bf16 v[164:167], a[40:43], v[190:193], v[164:167]\n"
      "v_mul_f32 v113, v109, v113\n"
      "v_mul_f32 v203, v109, v203\n"
      "v_mfma_f32_16x16x32_bf16 v[164:167], a[44:47], v[152:155], v[164:167]\n"
      "v_mul_f32 v112, v109, v112\n"
      "v_mul_f32 v207, v109, v207\n"
      "v_mfma_f32_16x16x32_bf16 v[138:141], a[60:63], v[104:107], v[138:141]\n"
      "v_cvt_pk_bf16_f32 v96, v96, v97\n"
      "v_readlane_b32 s45, v37, s43\n"
      "v_mfma_f32_16x16x32_bf16 v[138:141], a[64:67], v[190:193], v[138:141]\n"
      "v_mul_f32 v205, v109, v205\n"
      "v_mul_f32 v160, v110, v160\n"
      "v_mfma_f32_16x16x32_bf16 v[138:141], a[68:71], v[152:155], v[138:141]\n"
      "v_mul_f32_e64 v98, v108, v98\n"
      "v_fma_f32 v128, v100, v100, s51\n"
      "v_mfma_f32_16x16x32_bf16 v[170:173], a[84:87], v[104:107], v[170:173]\n"
      "buffer_load_dwordx4 v[210:213], v36, s[28:31], s86 offen\n"
      "v_mfma_f32_16x16x32_bf16 v[170:173], a[88:91], v[190:193], v[170:173]\n"
      "v_cvt_pk_bf16_f32 v156, v156, v157\n"
      "v_fmac_f32 v128, v101, v101\n"
      "v_mfma_f32_16x16x32_bf16 v[170:173], a[92:95], v[152:155], v[170:173]\n"
      "v_fmac_f32 v128, v102, v102\n"
      "v_mul_f32_e64 v116, v108, v116\n"
      "v_mfma_f32_16x16x32_bf16 v[186:189], a[108:111], v[104:107], v[186:189]\n"
      "s_and_b32 s47, s47, -4\n"
      "v_cvt_pk_bf16_f32 v201, v202, v203\n"
      "v_mul_f32 v118, v108, v118\n"
      "v_mfma_f32_16x16x32_bf16 v[186:189], a[112:115], v[190:193], v[186:189]\n"
      "s_nop 0\n"
      "v_fma_f32 v129, v170, v170, s51\n"
      "v_mul_f32 v159, v110, v159\n"
      "v_mfma_f32_16x16x32_bf16 v[186:189], a[116:119], v[152:155], v[186:189]\n"
      "v_mul_f32 v158, v110, v158\n"
      "v_mul_f32 v161, v110, v161\n"
      "v_mfma_f32_16x16x32_bf16 v[174:177], a[132:135], v[104:107], v[174:177]\n"
      "v_cvt_pk_bf16_f32 v97, v98, v99\n"
      "v_fmac_f32 v129, v171, v171\n"
      "v_mfma_f32_16x16x32_bf16 v[174:177], a[136:139], v[190:193], v[174:177]\n"
      "v_mul_f32 v148, v110, v148\n"
      "v_mul_f32 v117, v108, v117\n"
      "v_mfma_f32_16x16x32_bf16 v[174:177], a[140:143], v[152:155], v[174:177]\n"
      "v_mul_f32 v204, v109, v204\n"
      "v_fmac_f32_e64 v128, v103, v103\n"
      "v_mfma_f32_16x16x32_bf16 v[130:133], v[124:127], a[144:147], 0\n"
      "v_cvt_pk_bf16_f32 v98, v116, v117\n"
      "v_mov_b32_dpp v208, v208 quad_perm:[0,0,0,0] row_mask:0xf bank_mask:0xf\n"
      "v_mfma_f32_16x16x32_bf16 v[130:133], v[182:185], a[148:151], v[130:133]\n"
      "v_rcp_f32_e64 v208, v208\n"
      "v_mfma_f32_16x16x32_bf16 v[130:133], v[120:123], a[152:155], v[130:133]\n"
      "v_fmac_f32 v129, v172, v172\n"
      "v_fmac_f32 v128, v164, v164\n"
      "v_mfma_f32_16x16x32_bf16 v[130:133], v[104:107], a[156:159], v[130:133]\n"
      "v_fmac_f32 v128, v165, v165\n"
      "v_mul_f32 v114, v109, v114\n"
      "v_mfma_f32_16x16x32_bf16 v[130:133], v[190:193], a[160:163], v[130:133]\n"
      "v_fmac_f32 v129, v173, v173\n"
      "v_mul_f32 v206, v109, v206\n"
      "v_mfma_f32_16x16x32_bf16 v[130:133], v[152:155], a[164:167], v[130:133]\n"
      "v_fmac_f32_e64 v128, v166, v166\n"
      "v_cvt_pk_bf16_f32 v202, v112, v113\n"
      "v_mfma_f32_16x16x32_bf16 v[196:199], v[124:127], a[168:171], 0\n"
      "v_cvt_pk_bf16_f32 v157, v158, v159\n"
      "v_cvt_pk_bf16_f32 v99, v118, v119\n"
      "v_mfma_f32_16x16x32_bf16 v[196:199], v[182:185], a[172:175], v[196:199]\n"
      "v_cvt_pk_bf16_f32 v203, v114, v115\n"
      "v_cvt_pk_bf16_f32 v204, v204, v205\n"
      "v_mfma_f32_16x16x32_bf16 v[196:199], v[120:123], a[176:179], v[196:199]\n"
      "v_fmac_f32 v129, v186, v186\n"
      "v_mul_f32 v137, v108, v137\n"
      "v_mfma_f32_16x16x32_bf16 v[196:199], v[104:107], a[180:183], v[196:199]\n"
      "ds_write_b128 v5, v[200:203] offset:24576\n"
      "v_mfma_f32_16x16x32_bf16 v[196:199], v[190:193], a[184:187], v[196:199]\n"
      "buffer_load_dword v94, v29, s[28:31], s86 offen\n"
      "v_mfma_f32_16x16x32_bf16 v[196:199], v[152:155], a[188:191], v[196:199]\n"
      "ds_read_b128 v[112:115], v0 offset:32768\n"
      "v_cvt_pk_bf16_f32 v205, v206, v207\n"
      "v_mfma_f32_16x16x32_bf16 v[214:217], v[124:127], a[192:195], 0\n"
      "v_mul_f32 v149, v110, v149\n"
      "v_fmac_f32 v129, v187, v187\n"
      "v_mfma_f32_16x16x32_bf16 v[214:217], v[182:185], a[196:199], v[214:217]\n"
      "ds_read_b128 v[182:185], v1 offset:26624\n"
      "v_mul_f32 v147, v110, v147\n"
      "v_mfma_f32_16x16x32_bf16 v[214:217], v[120:123], a[200:203], v[214:217]\n"
      "ds_read_b128 v[120:123], v1 offset:32768\n"
      "v_fmac_f32 v129, v188, v188\n"
      "v_mfma_f32_16x16x32_bf16 v[214:217], v[104:107], a[204:207], v[214:217]\n"
      "v_fmac_f32 v128, v167, v167\n"
      "v_mul_f32 v168, v208, v168\n"
      "v_mfma_f32_16x16x32_bf16 v[214:217], v[190:193], a[208:211], v[214:217]\n"
      "ds_read_b128 v[116:119], v1 offset:22528\n"
      "v_fmac_f32_e64 v128, v138, v138\n"
      "v_mfma_f32_16x16x32_bf16 v[214:217], v[152:155], a[212:215], v[214:217]\n"
      "ds_write_b128 v5, v[96:99] offset:18432\n"
      "v_mfma_f32_16x16x32_bf16 v[150:153], a[0:3], v[142:145], 0\n"
      "ds_read_b128 v[190:193], v9 offset:4608\n"
      "v_fmac_f32 v128, v139, v139\n"
      "v_mfma_f32_16x16x32_bf16 v[150:153], a[4:7], v[220:223], v[150:153]\n"
      "v_fmac_f32 v129, v189, v189\n"
      "v_fmac_f32 v129, v174, v174\n"
      "v_mfma_f32_16x16x32_bf16 v[150:153], a[8:11], v[178:181], v[150:153]\n"
      "s_waitcnt lgkmcnt(3)\n"
      "v_cvt_pk_bf16_f32 v134, v134, v135\n"
      "v_cvt_pk_bf16_f32 v168, v168, v168\n"
      "v_mfma_f32_16x16x32_bf16 v[150:153], a[12:15], v[182:185], v[150:153]\n"
      "v_fmac_f32 v129, v175, v175\n"
      "v_fmac_f32 v128, v140, v140\n"
      "v_mfma_f32_16x16x32_bf16 v[150:153], a[16:19], v[112:115], v[150:153]\n"
      "v_cvt_pk_bf16_f32 v135, v136, v137\n"
      "v_fmac_f32 v129, v176, v176\n"
      "v_mfma_f32_16x16x32_bf16 v[150:153], a[20:23], v[120:123], v[150:153]\n"
      "v_readlane_b32 s46, v37, s42\n"
      "v_fmac_f32 v128, v141, v141\n"
      "v_mfma_f32_16x16x32_bf16 v[200:203], a[24:27], v[142:145], 0\n"
      "v_cvt_pk_bf16_f32 v196, v196, v197\n"
      "v_cvt_pk_bf16_f32 v160, v160, v161\n"
      "v_mfma_f32_16x16x32_bf16 v[200:203], a[28:31], v[220:223], v[200:203]\n"
      "v_fmac_f32 v129, v177, v177\n"
      "v_cvt_pk_bf16_f32 v158, v146, v147\n"
      "v_mfma_f32_16x16x32_bf16 v[200:203], a[32:35], v[178:181], v[200:203]\n"
      "v_permlane16_swap_b32 v128, v129\n"
      "v_mfma_f32_16x16x32_bf16 v[200:203], a[36:39], v[182:185], v[200:203]\n"
      "v_cvt_pk_bf16_f32 v161, v162, v163\n"
      "v_cvt_pk_bf16_f32 v214, v214, v215\n"
      "v_mfma_f32_16x16x32_bf16 v[200:203], a[40:43], v[112:115], v[200:203]\n"
      "ds_write_b64 v6, v[160:161] offset:30720\n"
      "v_mfma_f32_16x16x32_bf16 v[200:203], a[44:47], v[120:123], v[200:203]\n"
      "v_add_f32_e64 v128, v128, v129\n"
      "v_cvt_pk_bf16_f32 v215, v216, v217\n"
      "v_mfma_f32_16x16x32_bf16 v[206:209], a[48:51], v[142:145], 0\n"
      "v_fma_f32 v169, v150, v150, s51\n"
      "v_cvt_pk_bf16_f32 v130, v130, v131\n"
      "v_mfma_f32_16x16x32_bf16 v[206:209], a[52:55], v[220:223], v[206:209]\n"
      "v_fmac_f32 v169, v151, v151\n"
      "v_mov_b32 v129, v128\n"
      "v_mfma_f32_16x16x32_bf16 v[206:209], a[56:59], v[178:181], v[206:209]\n"
      "s_nop 0\n"
      "v_permlane32_swap_b32 v128, v129\n"
      "v_mfma_f32_16x16x32_bf16 v[206:209], a[60:63], v[182:185], v[206:209]\n"
      "v_add_f32 v128, v128, v129\n"
      "v_cvt_pk_bf16_f32 v159, v148, v149\n"
      "v_mfma_f32_16x16x32_bf16 v[206:209], a[64:67], v[112:115], v[206:209]\n"
      "v_rsq_f32 v128, v128\n"
      "v_mfma_f32_16x16x32_bf16 v[206:209], a[68:71], v[120:123], v[206:209]\n"
      "v_fmac_f32 v169, v152, v152\n"
      "v_mov_b32 v129, v128\n"
      "v_mfma_f32_16x16x32_bf16 v[160:163], a[72:75], v[142:145], 0\n"
      "s_nop 0\n"
      "v_permlane16_swap_b32 v128, v129\n"
      "v_mfma_f32_16x16x32_bf16 v[160:163], a[76:79], v[220:223], v[160:163]\n"
      "ds_read_b128 v[96:99], v1 offset:34816\n"
      "v_fmac_f32 v169, v153, v153\n"
      "v_mfma_f32_16x16x32_bf16 v[160:163], a[80:83], v[178:181], v[160:163]\n"
      "v_fmac_f32 v169, v200, v200\n"
      "v_mul_f32_e64 v128, s50, v128\n"
      "v_mfma_f32_16x16x32_bf16 v[160:163], a[84:87], v[182:185], v[160:163]\n"
      "v_mul_f32_e64 v166, v128, v166\n"
      "v_cvt_pk_bf16_f32 v197, v198, v199\n"
      "v_mfma_f32_16x16x32_bf16 v[160:163], a[88:91], v[112:115], v[160:163]\n"
      "ds_read_b128 v[108:111], v1 offset:28672\n"
      "v_fmac_f32 v169, v201, v201\n"
      "v_mfma_f32_16x16x32_bf16 v[160:163], a[92:95], v[120:123], v[160:163]\n"
      "v_fmac_f32 v169, v202, v202\n"
      "v_mul_f32_e64 v102, v128, v102\n"
      "v_mfma_f32_16x16x32_bf16 v[104:107], a[96:99], v[142:145], 0\n"
      "s_waitcnt lgkmcnt(2)\n"
      "v_mul_f32 v138, v128, v138\n"
      "v_or3_b32 v190, v190, v191, v192\n"
      "v_mfma_f32_16x16x32_bf16 v[104:107], a[100:103], v[220:223], v[104:107]\n"
      "v_mul_f32 v164, v128, v164\n"
      "v_mul_f32 v174, v129, v174\n"
      "v_mfma_f32_16x16x32_bf16 v[104:107], a[104:107], v[178:181], v[104:107]\n"
      "v_mul_f32_e64 v171, v129, v171\n"
      "v_fma_f32 v85, v160, v160, s51\n"
      "v_mfma_f32_16x16x32_bf16 v[104:107], a[108:111], v[182:185], v[104:107]\n"
      "v_fmac_f32 v85, v161, v161\n"
      "v_fmac_f32 v169, v203, v203\n"
      "v_mfma_f32_16x16x32_bf16 v[104:107], a[112:115], v[112:115], v[104:107]\n"
      "ds_write_b64 v6, v[134:135] offset:18432\n"
      "v_mfma_f32_16x16x32_bf16 v[104:107], a[116:119], v[120:123], v[104:107]\n"
      "v_cvt_pk_bf16_f32 v131, v132, v133\n"
      "v_fmac_f32 v85, v162, v162\n"
      "v_mfma_f32_16x16x32_bf16 v[124:127], a[120:123], v[142:145], 0\n"
      "v_fmac_f32 v85, v163, v163\n"
      "v_fmac_f32 v169, v206, v206\n"
      "v_mfma_f32_16x16x32_bf16 v[124:127], a[124:127], v[220:223], v[124:127]\n"
      "ds_write_b64 v6, v[204:205] offset:24576\n"
      "v_mfma_f32_16x16x32_bf16 v[124:127], a[128:131], v[178:181], v[124:127]\n"
      "v_mul_f32 v139, v128, v139\n"
      "v_fmac_f32 v169, v207, v207\n"
      "v_mfma_f32_16x16x32_bf16 v[124:127], a[132:135], v[182:185], v[124:127]\n"
      "v_fmac_f32 v85, v104, v104\n"
      "v_mul_f32 v173, v129, v173\n"
      "v_mfma_f32_16x16x32_bf16 v[124:127], a[136:139], v[112:115], v[124:127]\n"
      "v_mul_f32 v175, v129, v175\n"
      "v_mul_f32 v165, v128, v165\n"
      "v_mfma_f32_16x16x32_bf16 v[124:127], a[140:143], v[120:123], v[124:127]\n"
      "v_bitop3_b32 v190, v190, v193, s33 bitop3:0xa8\n"
      "v_fmac_f32 v85, v105, v105\n"
      "v_mfma_f32_16x16x32_bf16 v[216:219], v[142:145], a[144:147], 0\n"
      "v_mul_f32 v101, v128, v101\n"
      "v_mul_f32 v170, v129, v170\n"
      "v_mfma_f32_16x16x32_bf16 v[216:219], v[220:223], a[148:151], v[216:219]\n"
      "ds_write_b128 v5, v[156:159] offset:30720\n"
      "v_mfma_f32_16x16x32_bf16 v[216:219], v[178:181], a[152:155], v[216:219]\n"
      "v_mul_f32 v100, v128, v100\n"
      "v_fmac_f32 v85, v106, v106\n"
      "v_mfma_f32_16x16x32_bf16 v[216:219], v[182:185], a[156:159], v[216:219]\n"
      "v_fmac_f32_e64 v169, v208, v208\n"
      "v_cvt_pk_bf16_f32 v100, v100, v101\n"
      "v_mfma_f32_16x16x32_bf16 v[216:219], v[112:115], a[160:163], v[216:219]\n"
      "v_fmac_f32 v85, v107, v107\n"
      "v_fmac_f32 v85, v124, v124\n"
      "v_mfma_f32_16x16x32_bf16 v[216:219], v[120:123], a[164:167], v[216:219]\n"
      "v_mul_f32 v189, v129, v189\n"
      "v_fmac_f32 v85, v125, v125\n"
      "v_mfma_f32_16x16x32_bf16 v[192:195], v[142:145], a[168:171], 0\n"
      "v_fmac_f32 v169, v209, v209\n"
      "v_fmac_f32 v85, v126, v126\n"
      "v_mfma_f32_16x16x32_bf16 v[192:195], v[220:223], a[172:175], v[192:195]\n"
      "v_mul_f32 v167, v128, v167\n"
      "v_fmac_f32 v85, v127, v127\n"
      "v_mfma_f32_16x16x32_bf16 v[192:195], v[178:181], a[176:179], v[192:195]\n"
      "s_nop 0\n"
      "v_permlane16_swap_b32 v169, v85\n"
      "v_mfma_f32_16x16x32_bf16 v[192:195], v[182:185], a[180:183], v[192:195]\n"
      "v_add_f32 v169, v169, v85\n"
      "v_mul_f32 v176, v129, v176\n"
      "v_mfma_f32_16x16x32_bf16 v[192:195], v[112:115], a[184:187], v[192:195]\n"
      "v_mul_f32_e64 v140, v128, v140\n"
      "v_cvt_pk_bf16_f32 v132, v216, v217\n"
      "v_mfma_f32_16x16x32_bf16 v[192:195], v[120:123], a[188:191], v[192:195]\n"
      "ds_read_b128 v[146:149], v0 offset:22528\n"
      "v_mov_b32 v85, v169\n"
      "v_mfma_f32_16x16x32_bf16 v[156:159], v[142:145], a[192:195], 0\n"
      "ds_read_b128 v[134:137], v0 offset:28672\n"
      "v_mul_f32 v188, v129, v188\n"
      "v_mfma_f32_16x16x32_bf16 v[156:159], v[220:223], a[196:199], v[156:159]\n"
      "v_permlane32_swap_b32 v169, v85\n"
      "v_mfma_f32_16x16x32_bf16 v[156:159], v[178:181], a[200:203], v[156:159]\n"
      "ds_read_b128 v[178:181], v0 offset:34816\n"
      "v_add_f32 v169, v169, v85\n"
      "v_mfma_f32_16x16x32_bf16 v[156:159], v[182:185], a[204:207], v[156:159]\n"
      "v_rsq_f32 v169, v169\n"
      "v_mfma_f32_16x16x32_bf16 v[156:159], v[112:115], a[208:211], v[156:159]\n"
      "ds_read_u16 v78, v11 offset:1168\n"
      "v_mov_b32 v85, v169\n"
      "v_mfma_f32_16x16x32_bf16 v[156:159], v[120:123], a[212:215], v[156:159]\n"
      "s_waitcnt lgkmcnt(3)\n"
      "v_cvt_pk_bf16_f32 v170, v170, v171\n"
      "v_mul_f32 v172, v129, v172\n"
      "v_mfma_f32_16x16x32_bf16 v[182:185], a[0:3], v[146:149], 0\n"
      "s_waitcnt lgkmcnt(2)\n"
      "v_permlane16_swap_b32 v169, v85\n"
      "v_mfma_f32_16x16x32_bf16 v[182:185], a[4:7], v[116:119], v[182:185]\n"
      "s_or_b32 s95, s45, s46\n"
      "v_mul_f32 v103, v128, v103\n"
      "v_mul_f32_e64 v127, v85, v127\n"
      "v_mfma_f32_16x16x32_bf16 v[182:185], a[8:11], v[134:137], v[182:185]\n"
      "v_mul_f32 v187, v129, v187\n"
      "v_mul_f32 v169, s50, v169\n"
      "v_mfma_f32_16x16x32_bf16 v[182:185], a[12:15], v[108:111], v[182:185]\n"
      "s_waitcnt lgkmcnt(1)\n"
      "v_mul_f32 v124, v85, v124\n"
      "v_cvt_pk_bf16_f32 v101, v102, v103\n"
      "v_mfma_f32_16x16x32_bf16 v[182:185], a[16:19], v[178:181], v[182:185]\n"
      "v_cvt_pk_bf16_f32 v171, v172, v173\n"
      "v_mul_f32_e64 v104, v85, v104\n"
      "v_mfma_f32_16x16x32_bf16 v[182:185], a[20:23], v[96:99], v[182:185]\n"
      "v_mul_f32 v107, v85, v107\n"
      "v_mul_f32 v141, v128, v141\n"
      "v_mfma_f32_16x16x32_bf16 v[142:145], a[24:27], v[146:149], 0\n"
      "v_mul_f32 v125, v85, v125\n"
      "v_mul_f32 v150, v169, v150\n"
      "v_mfma_f32_16x16x32_bf16 v[142:145], a[28:31], v[116:119], v[142:145]\n"
      "ds_write_b16 v12, v168\n"
      "v_mfma_f32_16x16x32_bf16 v[142:145], a[32:35], v[134:137], v[142:145]\n"
      "v_cvt_pk_bf16_f32 v173, v188, v189\n"
      "v_mul_f32 v177, v129, v177\n"
      "v_mfma_f32_16x16x32_bf16 v[142:145], a[36:39], v[108:111], v[142:145]\n"
      "v_mul_f32 v151, v169, v151\n"
      "v_cvt_pk_bf16_f32 v103, v166, v167\n"
      "v_mfma_f32_16x16x32_bf16 v[142:145], a[40:43], v[178:181], v[142:145]\n"
      "v_fma_f32 v191, v182, v182, s51\n"
      "v_mul_f32 v161, v85, v161\n"
      "v_mfma_f32_16x16x32_bf16 v[142:145], a[44:47], v[96:99], v[142:145]\n"
      "v_mul_f32 v106, v85, v106\n"
      "v_mul_f32_e64 v160, v85, v160\n"
      "v_mfma_f32_16x16x32_bf16 v[220:223], a[48:51], v[146:149], 0\n"
      "v_mul_f32 v126, v85, v126\n"
      "v_mul_f32 v200, v169, v200\n"
      "v_mfma_f32_16x16x32_bf16 v[220:223], a[52:55], v[116:119], v[220:223]\n"
      "v_cmp_ne_u32 s[56:57], 0, v190\n"
      "v_mul_f32 v186, v129, v186\n"
      "v_mfma_f32_16x16x32_bf16 v[220:223], a[56:59], v[134:137], v[220:223]\n"
      "v_cvt_pk_bf16_f32 v216, v156, v157\n"
      "v_mul_f32 v209, v169, v209\n"
      "v_mfma_f32_16x16x32_bf16 v[220:223], a[60:63], v[108:111], v[220:223]\n"
      "v_fmac_f32 v191, v183, v183\n"
      "v_cvt_pk_bf16_f32 v172, v186, v187\n"
      "v_mfma_f32_16x16x32_bf16 v[220:223], a[64:67], v[178:181], v[220:223]\n"
      "v_cvt_pk_bf16_f32 v150, v150, v151\n"
      "v_mul_f32 v163, v85, v163\n"
      "v_mfma_f32_16x16x32_bf16 v[220:223], a[68:71], v[96:99], v[220:223]\n"
      "v_mul_f32 v203, v169, v203\n"
      "v_cvt_pk_bf16_f32 v124, v124, v125\n"
      "v_mfma_f32_16x16x32_bf16 v[120:123], a[72:75], v[146:149], 0\n"
      "v_mul_f32 v152, v169, v152\n"
      "v_fmac_f32 v191, v184, v184\n"
      "v_mfma_f32_16x16x32_bf16 v[120:123], a[76:79], v[116:119], v[120:123]\n"
      "v_fmac_f32 v191, v185, v185\n"
      "v_cvt_pk_bf16_f32 v125, v126, v127\n"
      "v_mfma_f32_16x16x32_bf16 v[120:123], a[80:83], v[134:137], v[120:123]\n"
      "v_mul_f32_e64 v207, v169, v207\n"
      "v_mul_f32_e64 v153, v169, v153\n"
      "v_mfma_f32_16x16x32_bf16 v[120:123], a[84:87], v[108:111], v[120:123]\n"
      "v_cvt_pk_bf16_f32 v133, v218, v219\n"
      "v_fmac_f32 v191, v142, v142\n"
      "v_mfma_f32_16x16x32_bf16 v[120:123], a[88:91], v[178:181], v[120:123]\n"
      "s_or_b32 s95, s95, s48\n"
      "v_cvt_pk_bf16_f32 v199, v194, v195\n"
      "v_cvt_pk_bf16_f32 v160, v160, v161\n"
      "v_mfma_f32_16x16x32_bf16 v[120:123], a[92:95], v[96:99], v[120:123]\n"
      "v_cvt_pk_bf16_f32 v174, v174, v175\n"
      "v_fmac_f32 v191, v143, v143\n"
      "v_mfma_f32_16x16x32_bf16 v[154:157], a[96:99], v[146:149], 0\n"
      "v_fmac_f32 v191, v144, v144\n"
      "v_mul_f32 v206, v169, v206\n"
      "v_mfma_f32_16x16x32_bf16 v[154:157], a[100:103], v[116:119], v[154:157]\n"
      "v_mul_f32 v162, v85, v162\n"
      "v_cvt_pk_bf16_f32 v138, v138, v139\n"
      "v_mfma_f32_16x16x32_bf16 v[154:157], a[104:107], v[134:137], v[154:157]\n"
      "v_mul_f32_e64 v201, v169, v201\n"
      "v_fma_f32 v190, v120, v120, s51\n"
      "v_mfma_f32_16x16x32_bf16 v[154:157], a[108:111], v[108:111], v[154:157]\n"
      "v_cvt_pk_bf16_f32 v102, v164, v165\n"
      "v_cvt_pk_bf16_f32 v198, v192, v193\n"
      "v_mfma_f32_16x16x32_bf16 v[154:157], a[112:115], v[178:181], v[154:157]\n"
      "v_fmac_f32 v190, v121, v121\n"
      "v_fmac_f32 v191, v145, v145\n"
      "v_mfma_f32_16x16x32_bf16 v[154:157], a[116:119], v[96:99], v[154:157]\n"
      "v_fmac_f32 v191, v220, v220\n"
      "v_fmac_f32 v191, v221, v221\n"
      "v_mfma_f32_16x16x32_bf16 v[126:129], a[120:123], v[146:149], 0\n"
      "v_fmac_f32_e64 v190, v122, v122\n"
      "v_cvt_pk_bf16_f32 v151, v152, v153\n"
      "v_mfma_f32_16x16x32_bf16 v[126:129], a[124:127], v[116:119], v[126:129]\n"
      "v_fmac_f32 v190, v123, v123\n"
      "v_mul_f32 v105, v85, v105\n"
      "v_mfma_f32_16x16x32_bf16 v[126:129], a[128:131], v[134:137], v[126:129]\n"
      "v_mul_f32_e64 v202, v169, v202\n"
      "v_cvt_pk_bf16_f32 v161, v162, v163\n"
      "v_mfma_f32_16x16x32_bf16 v[126:129], a[132:135], v[108:111], v[126:129]\n"
      "v_cvt_pk_bf16_f32 v217, v158, v159\n"
      "v_cvt_pk_bf16_f32 v162, v104, v105\n"
      "v_mfma_f32_16x16x32_bf16 v[126:129], a[136:139], v[178:181], v[126:129]\n"
      "v_cvt_pk_bf16_f32 v175, v176, v177\n"
      "v_fmac_f32 v191, v222, v222\n"
      "v_mfma_f32_16x16x32_bf16 v[126:129], a[140:143], v[96:99], v[126:129]\n"
      "v_fmac_f32 v190, v154, v154\n"
      "v_cvt_pk_bf16_f32 v152, v200, v201\n"
      "v_mfma_f32_16x16x32_bf16 v[192:195], v[146:149], a[144:147], 0\n"
      "v_fmac_f32 v190, v155, v155\n"
      "v_fmac_f32 v190, v156, v156\n"
      "v_mfma_f32_16x16x32_bf16 v[192:195], v[116:119], a[148:151], v[192:195]\n"
      "v_cvt_pk_bf16_f32 v153, v202, v203\n"
      "v_fmac_f32 v190, v157, v157\n"
      "v_mfma_f32_16x16x32_bf16 v[192:195], v[134:137], a[152:155], v[192:195]\n"
      "v_mul_f32 v208, v169, v208\n"
      "v_fmac_f32_e64 v190, v126, v126\n"
      "v_mfma_f32_16x16x32_bf16 v[192:195], v[108:111], a[156:159], v[192:195]\n"
      "ds_read_b128 v[200:203], v2\n"
      "v_fmac_f32 v190, v127, v127\n"
      "v_mfma_f32_16x16x32_bf16 v[192:195], v[178:181], a[160:163], v[192:195]\n"
      "v_fmac_f32 v191, v223, v223\n"
      "v_fmac_f32 v190, v128, v128\n"
      "v_mfma_f32_16x16x32_bf16 v[192:195], v[96:99], a[164:167], v[192:195]\n"
      "v_fmac_f32 v190, v129, v129\n"
      "v_cvt_pk_bf16_f32 v206, v206, v207\n"
      "v_mfma_f32_16x16x32_bf16 v[166:169], v[146:149], a[168:171], 0\n"
      "v_permlane16_swap_b32 v191, v190\n"
      "v_mfma_f32_16x16x32_bf16 v[166:169], v[116:119], a[172:175], v[166:169]\n"
      "v_cvt_pk_bf16_f32 v163, v106, v107\n"
      "v_add_f32 v191, v191, v190\n"
      "v_mfma_f32_16x16x32_bf16 v[166:169], v[134:137], a[176:179], v[166:169]\n"
      "ds_read_b128 v[112:115], v2 offset:6144\n"
      "v_mov_b32 v190, v191\n"
      "v_mfma_f32_16x16x32_bf16 v[166:169], v[108:111], a[180:183], v[166:169]\n"
      "s_nop 0\n"
      "v_permlane32_swap_b32_e64 v191, v190\n"
      "v_mfma_f32_16x16x32_bf16 v[166:169], v[178:181], a[184:187], v[166:169]\n"
      "s_cmp_ge_u32 s95, 0x40000000\n"
      "s_cselect_b32 s94, -1, -2\n"
      "v_add_f32 v191, v191, v190\n"
      "v_mfma_f32_16x16x32_bf16 v[166:169], v[96:99], a[188:191], v[166:169]\n"
      "v_rsq_f32 v191, v191\n"
      "v_mfma_f32_16x16x32_bf16 v[104:107], v[146:149], a[192:195], 0\n"
      "ds_read_b128 v[146:149], v2 offset:3072\n"
      "v_mov_b32 v190, v191\n"
      "v_mfma_f32_16x16x32_bf16 v[104:107], v[116:119], a[196:199], v[104:107]\n"
      "s_nop 0\n"
      "v_permlane16_swap_b32 v191, v190\n"
      "v_mfma_f32_16x16x32_bf16 v[104:107], v[134:137], a[200:203], v[104:107]\n"
      "ds_read_b128 v[186:189], v10 offset:1200\n"
      "v_mul_f32 v191, s50, v191\n"
      "v_mfma_f32_16x16x32_bf16 v[104:107], v[108:111], a[204:207], v[104:107]\n"
      "v_mul_f32 v120, v190, v120\n"
      "v_mul_f32 v154, v190, v154\n"
      "v_mfma_f32_16x16x32_bf16 v[104:107], v[178:181], a[208:211], v[104:107]\n"
      "ds_read_b64 v[218:219], v10 offset:1192\n"
      "v_mul_f32_e64 v121, v190, v121\n"
      "v_mfma_f32_16x16x32_bf16 v[104:107], v[96:99], a[212:215], v[104:107]\n"
      "s_waitcnt lgkmcnt(3)\n"
      "v_mul_f32 v156, v190, v156\n"
      "v_mul_f32 v129, v190, v129\n"
      "v_mfma_f32_16x16x32_bf16 v[96:99], a[216:219], v[200:203], 0\n"
      "s_waitcnt lgkmcnt(2)\n"
      "v_mul_f32 v157, v190, v157\n"
      "v_cvt_pk_bf16_f32 v139, v140, v141\n"
      "v_mfma_f32_16x16x32_bf16 v[96:99], a[220:223], v[146:149], v[96:99]\n"
      "v_mul_f32 v123, v190, v123\n"
      "v_mul_f32_e64 v155, v190, v155\n"
      "v_mfma_f32_16x16x32_bf16 v[96:99], a[224:227], v[112:115], v[96:99]\n"
      "ds_read_b128 v[176:179], v2 offset:9216\n"
      "v_mul_f32 v122, v190, v122\n"
      "v_mfma_f32_16x16x32_bf16 v[116:119], a[240:243], v[200:203], 0\n"
      "s_cmp_gt_u32 s42, s44\n"
      "s_cselect_b32 s95, s94, s95\n"
      "v_cvt_pk_bf16_f32 v85, v106, v107\n"
      "v_mfma_f32_16x16x32_bf16 v[116:119], a[244:247], v[146:149], v[116:119]\n"
      "v_mul_f32 v128, v190, v128\n"
      "v_mul_f32_e64 v182, v191, v182\n"
      "v_mfma_f32_16x16x32_bf16 v[116:119], a[248:251], v[112:115], v[116:119]\n"
      "v_cvt_pk_bf16_f32 v120, v120, v121\n"
      "v_cvt_pk_bf16_f32 v121, v122, v123\n"
      "v_mfma_f32_16x16x32_bf16 v[134:137], v[232:235], v[200:203], 0\n"
      "ds_read_u16 v82, v11 offset:1200\n"
      "v_cvt_pk_bf16_f32 v84, v104, v105\n"
      "v_mfma_f32_16x16x32_bf16 v[134:137], v[236:239], v[146:149], v[134:137]\n"
      "v_cvt_pk_bf16_f32 v123, v156, v157\n"
      "v_cvt_pk_bf16_f32 v122, v154, v155\n"
      "v_mfma_f32_16x16x32_bf16 v[134:137], v[240:243], v[112:115], v[134:137]\n"
      "v_cvt_pk_bf16_f32 v77, v194, v195\n"
      "v_cvt_pk_bf16_f32 v80, v166, v167\n"
      "v_mfma_f32_16x16x32_bf16 v[146:149], v[170:173], v[100:103], v[40:43]\n"
      "s_waitcnt lgkmcnt(1)\n"
      "v_cvt_pk_bf16_f32 v76, v192, v193\n"
      "v_mul_f32 v126, v190, v126\n"
      "v_mfma_f32_16x16x32_bf16 v[104:107], v[160:163], v[100:103], v[44:47]\n"
      "ds_read_b128 v[200:203], v2 offset:12288\n"
      "v_cvt_pk_bf16_f32 v207, v208, v209\n"
      "v_mfma_f32_16x16x32_bf16 v[108:111], v[120:123], v[100:103], v[48:51]\n"
      "ds_read_b128 v[112:115], v2 offset:15360\n"
      "v_cvt_pk_bf16_f32 v81, v168, v169\n"
      "v_mfma_f32_16x16x32_bf16 v[164:167], v[186:189], v[100:103], v[24:27]\n"
      "v_mul_f32 v145, v191, v145\n"
      "v_mul_f32 v127, v190, v127\n"
      "v_mfma_f32_16x16x16_bf16 v[146:149], v[174:175], v[138:139], v[146:149]\n"
      "v_mul_f32 v184, v191, v184\n"
      "v_cvt_pk_bf16_f32 v126, v126, v127\n"
      "v_mul_f32 v221, v191, v221\n"
      "v_mfma_f32_16x16x16_bf16 v[104:107], v[124:125], v[138:139], v[104:107]\n"
      "v_cvt_pk_bf16_f32 v127, v128, v129\n"
      "v_mul_f32 v144, v191, v144\n"
      "v_mul_f32 v220, v191, v220\n"
      "v_mfma_f32_16x16x16_bf16 v[108:111], v[126:127], v[138:139], v[108:111]\n"
      "v_mul_f32 v185, v191, v185\n"
      "v_mul_f32 v183, v191, v183\n"
      "v_max3_f32 v181, v146, v147, v148\n"
      "v_mfma_f32_16x16x16_bf16 v[164:167], v[218:219], v[138:139], v[164:167]\n"
      "v_max3_f32 v168, v149, v104, v105\n"
      "v_max3_f32 v181, v181, v106, v107\n"
      "v_cvt_pk_bf16_f32 v220, v220, v221\n"
      "v_mfma_f32_16x16x32_bf16 v[96:99], a[228:231], v[176:179], v[96:99]\n"
      "s_waitcnt lgkmcnt(0)\n"
      "v_max3_f32 v168, v168, v108, v109\n"
      "v_max3_f32 v181, v181, v110, v111\n"
      "v_mfma_f32_16x16x32_bf16 v[96:99], a[232:235], v[200:203], v[96:99]\n"
      "s_add_u32 s42, s42, 1\n"
      "v_max3_f32 v181, v181, v168, v164\n"
      "v_mov_b32 v139, v181\n"
      "v_mfma_f32_16x16x32_bf16 v[96:99], a[236:239], v[112:115], v[96:99]\n"
      "s_nop 0\n"
      "v_permlane16_swap_b32 v181, v139\n"
      "v_mfma_f32_16x16x32_bf16 v[116:119], a[252:255], v[176:179], v[116:119]\n"
      "ds_read_b128 v[166:169], v2 offset:3328\n"
      "v_max_f32 v181, v181, v139\n"
      "v_mfma_f32_16x16x32_bf16 v[116:119], v[224:227], v[200:203], v[116:119]\n"
      "v_cvt_pk_bf16_f32 v182, v182, v183\n"
      "v_mov_b32 v139, v181\n"
      "v_mfma_f32_16x16x32_bf16 v[116:119], v[228:231], v[112:115], v[116:119]\n"
      "s_nop 0\n"
      "v_permlane32_swap_b32_e64 v181, v139\n"
      "v_max_f32 v181, v181, v139\n"
      "v_cvt_pk_bf16_f32 v183, v184, v185\n"
      "s_waitcnt vmcnt(1)\n"
      "v_or3_b32 v210, v210, v211, v212\n"
      "v_pk_add_f32 v[106:107], v[106:107], v[180:181] op_sel:[0,1] neg_lo:[0,1] neg_hi:[0,1]\n"
      "v_pk_add_f32 v[104:105], v[104:105], v[180:181] op_sel:[0,1] neg_lo:[0,1] neg_hi:[0,1]\n"
      "v_pk_add_f32 v[148:149], v[148:149], v[180:181] op_sel:[0,1] neg_lo:[0,1] neg_hi:[0,1]\n"
      "v_pk_mul_f32 v[142:143], v[142:143], v[190:191] op_sel:[0,1]\n"
      "v_pk_add_f32 v[110:111], v[110:111], v[180:181] op_sel:[0,1] neg_lo:[0,1] neg_hi:[0,1]\n"
      "v_pk_add_f32 v[146:147], v[146:147], v[180:181] op_sel:[0,1] neg_lo:[0,1] neg_hi:[0,1]\n"
      "v_pk_add_f32 v[108:109], v[108:109], v[180:181] op_sel:[0,1] neg_lo:[0,1] neg_hi:[0,1]\n"
      "v_pk_mul_f32 v[222:223], v[222:223], v[190:191] op_sel:[0,1]\n"
      "v_mfma_f32_16x16x32_bf16 v[134:137], v[244:247], v[176:179], v[134:137]\n"
      "ds_read_b128 v[138:141], v2 offset:256\n"
      "v_cvt_pk_bf16_f32 v184, v142, v143\n"
      "v_mfma_f32_16x16x32_bf16 v[134:137], v[248:251], v[200:203], v[134:137]\n"
      "v_exp_f32 v104, v104\n"
      "v_mfma_f32_16x16x32_bf16 v[134:137], v[252:255], v[112:115], v[134:137]\n"
      "v_exp_f32 v146, v146\n"
      "v_mfma_f32_16x16x32_bf16 v[100:103], v[170:173], v[150:153], v[52:55]\n"
      "ds_read_b128 v[154:157], v2 offset:6400\n"
      "v_cvt_pk_bf16_f32 v185, v144, v145\n"
      "v_mfma_f32_16x16x32_bf16 v[190:193], v[160:163], v[150:153], v[56:59]\n"
      "v_exp_f32 v106, v106\n"
      "v_mfma_f32_16x16x32_bf16 v[142:145], v[120:123], v[150:153], v[60:63]\n"
      "v_exp_f32 v108, v108\n"
      "v_mfma_f32_16x16x32_bf16 v[176:179], v[186:189], v[150:153], v[26:29]\n"
      "s_waitcnt lgkmcnt(1)\n"
      "v_exp_f32 v147, v147\n"
      "v_mfma_f32_16x16x16_bf16 v[100:103], v[174:175], v[206:207], v[100:103]\n"
      "v_cvt_pk_bf16_f32 v221, v222, v223\n"
      "v_exp_f32_e64 v111, v111\n"
      "v_mfma_f32_16x16x16_bf16 v[190:193], v[124:125], v[206:207], v[190:193]\n"
      "v_cvt_pk_bf16_f32 v146, v146, v147\n"
      "v_exp_f32 v110, v110\n"
      "v_mfma_f32_16x16x16_bf16 v[142:145], v[126:127], v[206:207], v[142:145]\n"
      "v_mul_f32 v195, v97, v97\n"
      "v_exp_f32 v148, v148\n"
      "v_mfma_f32_16x16x16_bf16 v[176:179], v[218:219], v[206:207], v[176:179]\n"
      "v_cvt_pk_bf16_f32 v89, v110, v111\n"
      "v_exp_f32 v149, v149\n"
      "v_mfma_f32_16x16x32_bf16 v[200:203], a[216:219], v[138:141], 0\n"
      "v_sub_f32 v164, v164, v181\n"
      "v_cvt_pk_bf16_f32 v147, v148, v149\n"
      "v_mfma_f32_16x16x32_bf16 v[200:203], a[220:223], v[166:169], v[200:203]\n"
      "s_waitcnt lgkmcnt(0)\n"
      "v_exp_f32_e64 v107, v107\n"
      "v_mfma_f32_16x16x32_bf16 v[200:203], a[224:227], v[154:157], v[200:203]\n"
      "v_exp_f32 v164, v164\n"
      "v_mfma_f32_16x16x32_bf16 v[112:115], a[240:243], v[138:141], 0\n"
      "v_exp_f32 v105, v105\n"
      "v_mfma_f32_16x16x32_bf16 v[112:115], a[244:247], v[166:169], v[112:115]\n"
      "v_cvt_pk_bf16_f32 v149, v106, v107\n"
      "v_cvt_pk_bf16_f32 v90, v164, 0\n"
      "v_mfma_f32_16x16x32_bf16 v[112:115], a[248:251], v[154:157], v[112:115]\n"
      "ds_read_u16 v86, v11 offset:1232\n"
      "v_cvt_pk_bf16_f32 v148, v104, v105\n"
      "v_mfma_f32_16x16x32_bf16 v[204:207], v[232:235], v[138:141], 0\n"
      "v_exp_f32_e64 v109, v109\n"
      "v_mfma_f32_16x16x32_bf16 v[204:207], v[236:239], v[166:169], v[204:207]\n"
      "v_max3_f32 v105, v100, v101, v102\n"
      "v_max3_f32 v167, v103, v190, v191\n"
      "v_mfma_f32_16x16x32_bf16 v[204:207], v[240:243], v[154:157], v[204:207]\n"
      "v_cvt_pk_bf16_f32 v88, v108, v109\n"
      "v_max3_f32 v105, v105, v192, v193\n"
      "v_mfma_f32_16x16x32_bf16 v[156:159], v[32:35], v[146:149], 0\n"
      "v_max3_f32 v167, v167, v142, v143\n"
      "v_max3_f32 v105, v105, v144, v145\n"
      "v_mfma_f32_16x16x32_bf16 v[156:159], v[32:35], v[88:91], v[156:159]\n"
      "s_waitcnt vmcnt(0)\n"
      "v_or3_b32 v210, v210, v213, v94\n"
      "v_max3_f32 v105, v105, v167, v176\n"
      "v_mfma_f32_16x16x32_bf16 v[178:181], v[130:133], v[146:149], 0\n"
      "ds_read_b128 v[164:167], v2 offset:12544\n"
      "v_mov_b32 v155, v105\n"
      "v_mfma_f32_16x16x32_bf16 v[178:181], v[76:79], v[88:91], v[178:181]\n"
      "s_nop 0\n"
      "v_permlane16_swap_b32 v105, v155\n"
      "v_mfma_f32_16x16x32_bf16 v[138:141], v[196:199], v[146:149], 0\n"
      "v_max_f32 v105, v105, v155\n"
      "v_mov_b32 v155, v105\n"
      "v_mfma_f32_16x16x32_bf16 v[138:141], v[80:83], v[88:91], v[138:141]\n"
      "v_rcp_f32_e64 v156, v156\n"
      "v_mfma_f32_16x16x32_bf16 v[150:153], v[214:217], v[146:149], 0\n"
      "s_waitcnt lgkmcnt(1)\n"
      "v_permlane32_swap_b32 v105, v155\n"
      "v_mfma_f32_16x16x32_bf16 v[150:153], v[84:87], v[88:91], v[150:153]\n"
      "ds_read_b128 v[146:149], v2 offset:9472\n"
      "v_max_f32 v105, v105, v155\n"
      "v_sub_f32 v176, v176, v105\n"
      "v_exp_f32_e64 v176, v176\n"
      "v_pk_mul_f32 v[140:141], v[140:141], v[156:157] op_sel_hi:[1,0]\n"
      "v_pk_mul_f32 v[178:179], v[178:179], v[156:157] op_sel_hi:[1,0]\n"
      "v_pk_mul_f32 v[180:181], v[180:181], v[156:157] op_sel_hi:[1,0]\n"
      "v_cvt_pk_bf16_f32 v178, v178, v179\n"
      "v_pk_mul_f32 v[150:151], v[150:151], v[156:157] op_sel_hi:[1,0]\n"
      "v_pk_mul_f32 v[138:139], v[138:139], v[156:157] op_sel_hi:[1,0]\n"
      "v_cvt_pk_bf16_f32 v179, v180, v181\n"
      "v_cvt_pk_bf16_f32 v150, v150, v151\n"
      "v_fma_f32 v194, v96, v96, s52\n"
      "v_pk_mul_f32 v[152:153], v[152:153], v[156:157] op_sel_hi:[1,0]\n"
      "v_pk_add_f32 v[190:191], v[190:191], v[104:105] op_sel:[0,1] neg_lo:[0,1] neg_hi:[0,1]\n"
      "v_pk_add_f32 v[144:145], v[144:145], v[104:105] op_sel:[0,1] neg_lo:[0,1] neg_hi:[0,1]\n"
      "v_cvt_pk_bf16_f32 v151, v152, v153\n"
      "v_cvt_pk_bf16_f32 v181, v140, v141\n"
      "v_pk_add_f32 v[192:193], v[192:193], v[104:105] op_sel:[0,1] neg_lo:[0,1] neg_hi:[0,1]\n"
      "v_pk_add_f32 v[142:143], v[142:143], v[104:105] op_sel:[0,1] neg_lo:[0,1] neg_hi:[0,1]\n"
      "v_pk_fma_f32 v[194:195], v[98:99], v[98:99], v[194:195]\n"
      "v_cvt_pk_bf16_f32 v180, v138, v139\n"
      "v_exp_f32_e64 v192, v192\n"
      "v_pk_add_f32 v[100:101], v[100:101], v[104:105] op_sel:[0,1] neg_lo:[0,1] neg_hi:[0,1]\n"
      "v_pk_add_f32 v[102:103], v[102:103], v[104:105] op_sel:[0,1] neg_lo:[0,1] neg_hi:[0,1]\n"
      "v_mfma_f32_16x16x32_bf16 v[138:141], v[170:173], v[182:185], v[64:67]\n"
      "ds_write_b64 v4, v[150:151] offset:18432\n"
      "v_mfma_f32_16x16x32_bf16 v[152:155], v[160:163], v[182:185], v[68:71]\n"
      "s_waitcnt lgkmcnt(1)\n"
      "v_exp_f32 v102, v102\n"
      "v_mfma_f32_16x16x32_bf16 v[104:107], v[120:123], v[182:185], v[72:75]\n"
      "ds_read_b128 v[156:159], v2 offset:15616\n"
      "v_exp_f32_e64 v144, v144\n"
      "v_mfma_f32_16x16x32_bf16 v[108:111], v[186:189], v[182:185], v[28:31]\n"
      "ds_write_b128 v3, v[178:181] offset:18432\n"
      "v_mfma_f32_16x16x16_bf16 v[138:141], v[174:175], v[220:221], v[138:141]\n"
      "v_exp_f32 v100, v100\n"
      "v_exp_f32 v101, v101\n"
      "v_mfma_f32_16x16x16_bf16 v[152:155], v[124:125], v[220:221], v[152:155]\n"
      "v_cvt_pk_bf16_f32 v100, v100, v101\n"
      "v_exp_f32 v143, v143\n"
      "v_mfma_f32_16x16x16_bf16 v[104:107], v[126:127], v[220:221], v[104:107]\n"
      "v_exp_f32 v142, v142\n"
      "v_exp_f32 v190, v190\n"
      "v_mfma_f32_16x16x16_bf16 v[108:111], v[218:219], v[220:221], v[108:111]\n"
      "v_exp_f32 v191, v191\n"
      "v_max3_f32 v88, v138, v139, v140\n"
      "v_mfma_f32_16x16x32_bf16 v[200:203], a[228:231], v[146:149], v[200:203]\n"
      "v_exp_f32 v103, v103\n"
      "v_mfma_f32_16x16x32_bf16 v[200:203], a[232:235], v[164:167], v[200:203]\n"
      "s_waitcnt lgkmcnt(1)\n"
      "v_cvt_pk_bf16_f32 v94, v176, 0\n"
      "v_cvt_pk_bf16_f32 v101, v102, v103\n"
      "v_mfma_f32_16x16x32_bf16 v[200:203], a[236:239], v[156:159], v[200:203]\n"
      "v_exp_f32_e64 v145, v145\n"
      "v_mfma_f32_16x16x32_bf16 v[112:115], a[252:255], v[146:149], v[112:115]\n"
      "v_cvt_pk_bf16_f32 v102, v190, v191\n"
      "v_cvt_pk_bf16_f32 v92, v142, v143\n"
      "v_mfma_f32_16x16x32_bf16 v[112:115], v[224:227], v[164:167], v[112:115]\n"
      "v_max3_f32 v88, v88, v154, v155\n"
      "v_max3_f32 v181, v141, v152, v153\n"
      "v_mfma_f32_16x16x32_bf16 v[112:115], v[228:231], v[156:159], v[112:115]\n"
      "v_exp_f32_e64 v193, v193\n"
      "v_mfma_f32_16x16x32_bf16 v[204:207], v[244:247], v[146:149], v[204:207]\n"
      "v_max3_f32 v88, v88, v106, v107\n"
      "v_max3_f32 v181, v181, v104, v105\n"
      "v_mfma_f32_16x16x32_bf16 v[204:207], v[248:251], v[164:167], v[204:207]\n"
      "ds_read_b128 v[146:149], v7\n"
      "v_cvt_pk_bf16_f32 v103, v192, v193\n"
      "v_mfma_f32_16x16x32_bf16 v[204:207], v[252:255], v[156:159], v[204:207]\n"
      "v_cvt_pk_bf16_f32 v93, v144, v145\n"
      "v_max3_f32 v88, v88, v181, v108\n"
      "v_mfma_f32_16x16x32_bf16 v[180:183], v[32:35], v[100:103], 0\n"
      "ds_read_b64 v[192:193], v10 offset:1184\n"
      "v_mov_b32_e64 v145, v88\n"
      "v_mfma_f32_16x16x32_bf16 v[180:183], v[32:35], v[92:95], v[180:183]\n"
      "ds_read_b128 v[156:159], v10 offset:1168\n"
      "v_permlane16_swap_b32 v88, v145\n"
      "v_mfma_f32_16x16x32_bf16 v[176:179], v[130:133], v[100:103], 0\n"
      "v_max_f32 v88, v88, v145\n"
      "v_mov_b32 v145, v88\n"
      "v_mfma_f32_16x16x32_bf16 v[176:179], v[76:79], v[92:95], v[176:179]\n"
      "s_nop 0\n"
      "v_permlane32_swap_b32 v88, v145\n"
      "v_mfma_f32_16x16x32_bf16 v[166:169], v[196:199], v[100:103], 0\n"
      "s_waitcnt lgkmcnt(2)\n"
      "v_rcp_f32_e64 v180, v180\n"
      "v_mfma_f32_16x16x32_bf16 v[166:169], v[80:83], v[92:95], v[166:169]\n"
      "ds_read_b128 v[220:223], v7 offset:4096\n"
      "v_max_f32_e64 v88, v88, v145\n"
      "v_mfma_f32_16x16x32_bf16 v[142:145], v[214:217], v[100:103], 0\n"
      "buffer_store_dwordx4 v[146:149], v20, s[24:27], s85 offen sc1\n"
      "v_pk_add_f32 v[138:139], v[138:139], v[88:89] op_sel_hi:[1,0] neg_lo:[0,1] neg_hi:[0,1]\n"
      "v_pk_mul_f32 v[178:179], v[178:179], v[180:181] op_sel_hi:[1,0]\n"
      "v_pk_add_f32 v[154:155], v[154:155], v[88:89] op_sel_hi:[1,0] neg_lo:[0,1] neg_hi:[0,1]\n"
      "v_pk_fma_f32 v[194:195], v[116:117], v[116:117], v[194:195]\n"
      "v_pk_mul_f32 v[176:177], v[176:177], v[180:181] op_sel_hi:[1,0]\n"
      "v_pk_add_f32 v[106:107], v[106:107], v[88:89] op_sel_hi:[1,0] neg_lo:[0,1] neg_hi:[0,1]\n"
      "v_pk_mul_f32 v[166:167], v[166:167], v[180:181] op_sel_hi:[1,0]\n"
      "v_pk_fma_f32 v[194:195], v[118:119], v[118:119], v[194:195]\n"
      "v_pk_add_f32 v[152:153], v[152:153], v[88:89] op_sel_hi:[1,0] neg_lo:[0,1] neg_hi:[0,1]\n"
      "v_mfma_f32_16x16x32_bf16 v[142:145], v[84:87], v[92:95], v[142:145]\n"
      "s_waitcnt lgkmcnt(1)\n"
      "v_cvt_pk_bf16_f32 v176, v176, v177\n"
      "v_cvt_pk_bf16_f32 v177, v178, v179\n"
      "v_mfma_f32_16x16x32_bf16 v[182:185], v[156:159], v[170:173], v[30:33]\n"
      "s_waitcnt lgkmcnt(0)\n"
      "v_exp_f32_e64 v138, v138\n"
      "v_mfma_f32_16x16x32_bf16 v[146:149], v[156:159], v[160:163], v[30:33]\n"
      "ds_read_b128 v[170:173], v2 offset:512\n"
      "v_cvt_pk_bf16_f32 v178, v166, v167\n"
      "v_mfma_f32_16x16x32_bf16 v[160:163], v[156:159], v[120:123], v[30:33]\n"
      "ds_read_b128 v[164:167], v2 offset:6656\n"
      "v_fma_f32 v110, v200, v200, s52\n"
      "v_mfma_f32_16x16x32_bf16 v[120:123], v[156:159], v[186:189], v[30:33]\n"
      "ds_read_b128 v[100:103], v2 offset:3584\n"
      "v_sub_f32 v108, v108, v88\n"
      "v_mfma_f32_16x16x16_bf16 v[182:185], v[192:193], v[174:175], v[182:185]\n"
      "buffer_store_dwordx4 v[220:223], v21, s[24:27], s85 offen sc1\n"
      "v_exp_f32 v108, v108\n"
      "v_mul_f32_e64 v111, v201, v201\n"
      "v_pk_fma_f32 v[110:111], v[202:203], v[202:203], v[110:111]\n"
      "v_pk_mul_f32 v[168:169], v[168:169], v[180:181] op_sel_hi:[1,0]\n"
      "v_pk_fma_f32 v[110:111], v[112:113], v[112:113], v[110:111]\n"
      "v_pk_add_f32 v[104:105], v[104:105], v[88:89] op_sel_hi:[1,0] neg_lo:[0,1] neg_hi:[0,1]\n"
      "v_pk_add_f32 v[140:141], v[140:141], v[88:89] op_sel_hi:[1,0] neg_lo:[0,1] neg_hi:[0,1]\n"
      "v_mfma_f32_16x16x16_bf16 v[146:149], v[192:193], v[124:125], v[146:149]\n"
      "v_exp_f32 v104, v104\n"
      "v_exp_f32 v139, v139\n"
      "v_exp_f32 v155, v155\n"
      "v_mfma_f32_16x16x16_bf16 v[160:163], v[192:193], v[126:127], v[160:163]\n"
      "v_exp_f32 v106, v106\n"
      "v_exp_f32 v105, v105\n"
      "v_mfma_f32_16x16x16_bf16 v[120:123], v[192:193], v[218:219], v[120:123]\n"
      "v_exp_f32 v154, v154\n"
      "s_waitcnt lgkmcnt(0)\n"
      "v_mfma_f32_16x16x32_bf16 v[156:159], a[216:219], v[170:173], 0\n"
      "v_exp_f32 v140, v140\n"
      "v_mfma_f32_16x16x32_bf16 v[156:159], a[220:223], v[100:103], v[156:159]\n"
      "ds_read_b128 v[220:223], v2 offset:15872\n"
      "v_cndmask_b32 v182, v182, v146, s[58:59]\n"
      "v_mfma_f32_16x16x32_bf16 v[156:159], a[224:227], v[164:167], v[156:159]\n"
      "v_cndmask_b32 v182, v182, v160, s[60:61]\n"
      "v_cvt_pk_bf16_f32 v179, v168, v169\n"
      "v_cndmask_b32 v182, v182, v120, s[62:63]\n"
      "v_mfma_f32_16x16x32_bf16 v[146:149], a[240:243], v[170:173], 0\n"
      "ds_read_b128 v[126:129], v2 offset:9728\n"
      "v_max_f32_dpp v218, v182, v182 row_ror:8 row_mask:0xf bank_mask:0xf\n"
      "v_mfma_f32_16x16x32_bf16 v[146:149], a[244:247], v[100:103], v[146:149]\n"
      "ds_read_b128 v[190:193], v2 offset:12800\n"
      "v_max_f32_dpp v218, v218, v218 row_ror:4 row_mask:0xf bank_mask:0xf\n"
      "v_mfma_f32_16x16x32_bf16 v[146:149], a[248:251], v[164:167], v[146:149]\n"
      "ds_read_b128 v[122:125], v7 offset:8192\n"
      "v_max_f32_dpp v218, v218, v218 row_ror:2 row_mask:0xf bank_mask:0xf\n"
      "v_mfma_f32_16x16x32_bf16 v[160:163], v[232:235], v[170:173], 0\n"
      "ds_write_b128 v3, v[176:179] offset:18688\n"
      "v_pk_fma_f32 v[110:111], v[114:115], v[114:115], v[110:111]\n"
      "v_pk_mul_f32 v[144:145], v[144:145], v[180:181] op_sel_hi:[1,0]\n"
      "v_max_f32_dpp v218, v218, v218 row_ror:1 row_mask:0xf bank_mask:0xf\n"
      "v_mfma_f32_16x16x32_bf16 v[160:163], v[236:239], v[100:103], v[160:163]\n"
      "ds_read_b128 v[168:171], v7 offset:16384\n"
      "v_exp_f32 v153, v153\n"
      "v_exp_f32_e64 v141, v141\n"
      "v_pk_mul_f32 v[142:143], v[142:143], v[180:181] op_sel_hi:[1,0]\n"
      "v_pk_fma_f32 v[110:111], v[204:205], v[204:205], v[110:111]\n"
      "v_pk_fma_f32 v[194:195], v[134:135], v[134:135], v[194:195]\n"
      "v_mfma_f32_16x16x32_bf16 v[160:163], v[240:243], v[164:167], v[160:163]\n"
      "s_waitcnt lgkmcnt(2)\n"
      "v_cvt_pk_bf16_f32 v88, v104, v105\n"
      "v_max_f32_dpp v218, v218, v218 row_bcast:15 row_mask:0xa bank_mask:0xf\n"
      "v_mfma_f32_16x16x32_bf16 v[156:159], a[228:231], v[126:129], v[156:159]\n"
      "ds_read_b128 v[102:105], v7 offset:12288\n"
      "v_max_f32_dpp v218, v218, v218 row_bcast:31 row_mask:0xc bank_mask:0xf\n"
      "v_mfma_f32_16x16x32_bf16 v[156:159], a[232:235], v[190:193], v[156:159]\n"
      "buffer_store_dwordx4 v[122:125], v22, s[24:27], s85 offen sc1\n"
      "v_readlane_b32 s72, v218, 63\n"
      "v_pk_fma_f32 v[194:195], v[136:137], v[136:137], v[194:195]\n"
      "v_mfma_f32_16x16x32_bf16 v[156:159], a[236:239], v[220:223], v[156:159]\n"
      "v_exp_f32 v107, v107\n"
      "v_subrev_f32 v182, s72, v182\n"
      "v_mfma_f32_16x16x32_bf16 v[146:149], a[252:255], v[126:129], v[146:149]\n"
      "v_exp_f32_e64 v182, v182\n"
      "v_mfma_f32_16x16x32_bf16 v[146:149], v[224:227], v[190:193], v[146:149]\n"
      "v_exp_f32 v152, v152\n"
      "v_mfma_f32_16x16x32_bf16 v[146:149], v[228:231], v[220:223], v[146:149]\n"
      "s_nop 0\n"
      "v_fma_f32 v166, v156, v156, s52\n"
      "v_cvt_pk_bf16_f32 v182, v182, v182\n"
      "v_cvt_pk_bf16_f32 v138, v138, v139\n"
      "v_cvt_pk_bf16_f32 v139, v140, v141\n"
      "v_cvt_pk_bf16_f32 v141, v154, v155\n"
      "v_mfma_f32_16x16x32_bf16 v[160:163], v[244:247], v[126:129], v[160:163]\n"
      "ds_write_b16 v13, v182\n"
      "v_cvt_pk_bf16_f32 v90, v108, 0\n"
      "v_mfma_f32_16x16x32_bf16 v[160:163], v[248:251], v[190:193], v[160:163]\n"
      "s_waitcnt lgkmcnt(1)\n"
      "v_cvt_pk_bf16_f32 v89, v106, v107\n"
      "v_cvt_pk_bf16_f32 v140, v152, v153\n"
      "v_mfma_f32_16x16x32_bf16 v[160:163], v[252:255], v[220:223], v[160:163]\n"
      "ds_write_b32 v31, v210 offset:5632\n"
      "v_cvt_pk_bf16_f32 v142, v142, v143\n"
      "v_mfma_f32_16x16x32_bf16 v[208:211], v[32:35], v[138:141], 0\n"
      "ds_read_b128 v[218:221], v14\n"
      "v_mul_f32 v167, v157, v157\n"
      "v_mfma_f32_16x16x32_bf16 v[208:211], v[32:35], v[88:91], v[208:211]\n"
      "ds_read_b128 v[174:177], v14 offset:64\n"
      "v_cvt_pk_bf16_f32 v143, v144, v145\n"
      "v_mfma_f32_16x16x32_bf16 v[106:109], v[130:133], v[138:141], 0\n"
      "buffer_store_dwordx4 v[102:105], v23, s[24:27], s85 offen sc1\n"
      "v_pk_fma_f32 v[110:111], v[206:207], v[206:207], v[110:111]\n"
      "v_pk_fma_f32 v[166:167], v[158:159], v[158:159], v[166:167]\n"
      "v_mfma_f32_16x16x32_bf16 v[106:109], v[76:79], v[88:91], v[106:109]\n"
      "s_nop 0\n"
      "v_rcp_f32 v208, v208\n"
      "v_mfma_f32_16x16x32_bf16 v[126:129], v[196:199], v[138:141], 0\n"
      "v_add_f32 v110, v110, v111\n"
      "v_add_f32 v194, v194, v195\n"
      "v_mfma_f32_16x16x32_bf16 v[126:129], v[80:83], v[88:91], v[126:129]\n"
      "ds_write_b64 v4, v[142:143] offset:18688\n"
      "v_permlane16_swap_b32_e64 v194, v110\n"
      "v_pk_fma_f32 v[166:167], v[146:147], v[146:147], v[166:167]\n"
      "v_pk_mul_f32 v[106:107], v[106:107], v[208:209] op_sel_hi:[1,0]\n"
      "v_pk_fma_f32 v[166:167], v[148:149], v[148:149], v[166:167]\n"
      "v_add_f32_e64 v194, v194, v110\n"
      "v_cvt_pk_bf16_f32 v106, v106, v107\n"
      "v_pk_mul_f32 v[108:109], v[108:109], v[208:209] op_sel_hi:[1,0]\n"
      "v_pk_mul_f32 v[126:127], v[126:127], v[208:209] op_sel_hi:[1,0]\n"
      "v_pk_mul_f32 v[128:129], v[128:129], v[208:209] op_sel_hi:[1,0]\n"
      "v_pk_fma_f32 v[166:167], v[160:161], v[160:161], v[166:167]\n"
      "v_pk_fma_f32 v[166:167], v[162:163], v[162:163], v[166:167]\n"
      "v_cvt_pk_bf16_f32 v107, v108, v109\n"
      "v_cvt_pk_bf16_f32 v108, v126, v127\n"
      "v_cvt_pk_bf16_f32 v109, v128, v129\n"
      "v_mfma_f32_16x16x32_bf16 v[126:129], v[214:217], v[138:141], 0\n"
      "s_waitcnt lgkmcnt(1)\n"
      "v_add_f32 v166, v166, v167\n"
      "v_mov_b32 v110, v166\n"
      "v_mfma_f32_16x16x32_bf16 v[126:129], v[84:87], v[88:91], v[126:129]\n"
      "ds_write_b128 v3, v[106:109] offset:18944\n"
      "v_permlane16_swap_b32 v166, v110\n"
      "v_add_f32 v166, v166, v110\n"
      "s_cmp_ge_u32 s95, 0x40000000\n"
      "s_cselect_b64 vcc, -1, s[56:57]\n"
      "v_permlane32_swap_b32_e64 v194, v166\n"
      "v_add_f32_e64 v194, v194, v166\n"
      "s_add_u32 s43, s43, 1\n"
      "ds_write_b32 v31, v194 offset:1024\n"
      "v_pk_mul_f32 v[126:127], v[126:127], v[208:209] op_sel_hi:[1,0]\n"
      "v_cvt_pk_bf16_f32 v126, v126, v127\n"
      "v_pk_mul_f32 v[128:129], v[128:129], v[208:209] op_sel_hi:[1,0]\n"
      "v_cvt_pk_bf16_f32 v127, v128, v129\n"
      "v_mfma_f32_16x16x32_bf16 v[208:211], v[218:221], v[32:35], 0\n"
      "ds_write_b64 v4, v[126:127] offset:18944\n"
      "v_mfma_f32_16x16x32_bf16 v[208:211], v[174:177], v[32:35], v[208:211]\n"
      "buffer_store_dwordx4 v[168:171], v25, s[24:27], s85 offen sc1\n"
      "v_add_u32 v11, 2336, v11\n"
      "v_add_u32 v10, 2336, v10\n"
      "s_waitcnt lgkmcnt(0)\n"
      "s_barrier\n"
      "s_cbranch_vccnz L_disp0_%=\n"
      "L_max0_%=:\n"
      "v_mfma_f32_16x16x32_bf16 v[168:171], v[218:221], v[130:133], 0\n"
      "ds_read_b128 v[164:167], v0\n"
      "s_add_u32 m0, s64, 18432\n"
      "v_mfma_f32_16x16x32_bf16 v[168:171], v[174:177], v[76:79], v[168:171]\n"
      "ds_read_b128 v[142:145], v9 offset:1024\n"
      "ds_read_b128 v[108:111], v1\n"
      "v_mfma_f32_16x16x32_bf16 v[150:153], v[218:221], v[196:199], 0\n"
      "buffer_load_dwordx4 v16, s[16:19], s45 offen  lds\n"
      "v_mfma_f32_16x16x32_bf16 v[150:153], v[174:177], v[80:83], v[150:153]\n"
      "ds_read_b128 v[124:127], v1 offset:12288\n"
      "v_mfma_f32_16x16x32_bf16 v[194:197], v[218:221], v[214:217], 0\n"
      "ds_read_b128 v[184:187], v0 offset:6144\n"
      "v_readlane_b32 s85, v37, s42\n"
      "v_mfma_f32_16x16x32_bf16 v[194:197], v[174:177], v[84:87], v[194:197]\n"
      "ds_read_b128 v[100:103], v0 offset:12288\n"
      "s_waitcnt lgkmcnt(5)\n"
      "v_mfma_f32_16x16x32_bf16 v[220:223], a[0:3], v[164:167], 0\n"
      "v_cndmask_b32 v168, v168, v150, s[58:59]\n"
      "v_readlane_b32 s86, v38, s43\n"
      "v_mfma_f32_16x16x32_bf16 v[172:175], a[24:27], v[164:167], 0\n"
      "s_waitcnt lgkmcnt(4)\n"
      "v_mov_b32_dpp v208, v208 quad_perm:[0,0,0,0] row_mask:0xf bank_mask:0xf\n"
      "v_add_f32 v144, v144, v145\n"
      "v_mfma_f32_16x16x32_bf16 v[176:179], a[48:51], v[164:167], 0\n"
      "s_add_u32 m0, s64, 22528\n"
      "v_add_f32 v142, v142, v143\n"
      "v_add_f32 v142, v142, v144\n"
      "v_mfma_f32_16x16x32_bf16 v[210:213], a[72:75], v[164:167], 0\n"
      "buffer_load_dwordx4 v17, s[16:19], s45 offen  lds\n"
      "v_mfma_f32_16x16x32_bf16 v[180:183], a[96:99], v[164:167], 0\n"
      "s_add_u32 m0, s64, 26624\n"
      "v_rcp_f32 v208, v208\n"
      "v_mfma_f32_16x16x32_bf16 v[138:141], a[120:123], v[164:167], 0\n"
      "s_waitcnt lgkmcnt(0)\n"
      "v_rsq_f32_e64 v142, v142\n"
      "v_mfma_f32_16x16x32_bf16 v[220:223], a[4:7], v[108:111], v[220:223]\n"
      "ds_read_b128 v[214:217], v9 offset:5632\n"
      "v_mul_f32 v142, 0x415db3d7, v142\n"
      "v_mfma_f32_16x16x32_bf16 v[220:223], a[8:11], v[184:187], v[220:223]\n"
      "ds_read_b128 v[150:153], v1 offset:6144\n"
      "v_mov_b32_e64 v144, v142\n"
      "v_mfma_f32_16x16x32_bf16 v[172:175], a[28:31], v[108:111], v[172:175]\n"
      "buffer_load_dwordx4 v18, s[16:19], s45 offen  lds\n"
      "v_mfma_f32_16x16x32_bf16 v[172:175], a[32:35], v[184:187], v[172:175]\n"
      "s_add_u32 m0, s64, 30720\n"
      "v_permlane32_swap_b32_e64 v142, v144\n"
      "v_mfma_f32_16x16x32_bf16 v[176:179], a[52:55], v[108:111], v[176:179]\n"
      "ds_read_b128 v[188:191], v1 offset:2048\n"
      "v_mul_f32 v160, v144, v160\n"
      "v_mfma_f32_16x16x32_bf16 v[176:179], a[56:59], v[184:187], v[176:179]\n"
      "ds_read_u16 v78, v11\n"
      "v_mul_f32 v158, v144, v158\n"
      "v_mfma_f32_16x16x32_bf16 v[210:213], a[76:79], v[108:111], v[210:213]\n"
      "ds_read_b128 v[120:123], v0 offset:8192\n"
      "v_mov_b32_e64 v143, v142\n"
      "v_mfma_f32_16x16x32_bf16 v[210:213], a[80:83], v[184:187], v[210:213]\n"
      "buffer_load_dwordx4 v19, s[16:19], s45 offen  lds\n"
      "v_mfma_f32_16x16x32_bf16 v[180:183], a[100:103], v[108:111], v[180:183]\n"
      "v_permlane16_swap_b32 v142, v143\n"
      "v_mfma_f32_16x16x32_bf16 v[180:183], a[104:107], v[184:187], v[180:183]\n"
      "v_mul_f32 v134, v142, v134\n"
      "v_mul_f32 v200, v143, v200\n"
      "v_mfma_f32_16x16x32_bf16 v[138:141], a[124:127], v[108:111], v[138:141]\n"
      "v_mul_f32 v137, v142, v137\n"
      "v_mul_f32 v207, v143, v207\n"
      "v_mfma_f32_16x16x32_bf16 v[138:141], a[128:131], v[184:187], v[138:141]\n"
      "s_waitcnt lgkmcnt(1)\n"
      "v_mul_f32 v116, v142, v116\n"
      "v_mul_f32 v157, v144, v157\n"
      "v_mfma_f32_16x16x32_bf16 v[220:223], a[12:15], v[150:153], v[220:223]\n"
      "v_mul_f32 v163, v144, v163\n"
      "v_mul_f32 v97, v142, v97\n"
      "v_mfma_f32_16x16x32_bf16 v[220:223], a[16:19], v[100:103], v[220:223]\n"
      "v_mul_f32 v135, v142, v135\n"
      "v_mul_f32 v204, v143, v204\n"
      "v_mfma_f32_16x16x32_bf16 v[220:223], a[20:23], v[124:127], v[220:223]\n"
      "v_mul_f32 v205, v143, v205\n"
      "v_mul_f32 v113, v143, v113\n"
      "v_mfma_f32_16x16x32_bf16 v[172:175], a[36:39], v[150:153], v[172:175]\n"
      "v_mul_f32 v115, v143, v115\n"
      "v_mul_f32 v202, v143, v202\n"
      "v_mfma_f32_16x16x32_bf16 v[172:175], a[40:43], v[100:103], v[172:175]\n"
      "s_add_u32 m0, s65, 34816\n"
      "v_cvt_pk_bf16_f32 v134, v134, v135\n"
      "v_mul_f32 v161, v144, v161\n"
      "v_mfma_f32_16x16x32_bf16 v[172:175], a[44:47], v[124:127], v[172:175]\n"
      "v_mul_f32 v203, v143, v203\n"
      "v_mul_f32_e64 v206, v143, v206\n"
      "v_mfma_f32_16x16x32_bf16 v[176:179], a[60:63], v[150:153], v[176:179]\n"
      "buffer_load_dwordx4 v15, s[16:19], s45 offen  lds\n"
      "v_mfma_f32_16x16x32_bf16 v[176:179], a[64:67], v[100:103], v[176:179]\n"
      "v_mul_f32_e64 v162, v144, v162\n"
      "v_cvt_pk_bf16_f32 v204, v204, v205\n"
      "v_mfma_f32_16x16x32_bf16 v[176:179], a[68:71], v[124:127], v[176:179]\n"
      "ds_read_b128 v[104:107], v0 offset:14336\n"
      "v_mul_f32 v136, v142, v136\n"
      "v_mfma_f32_16x16x32_bf16 v[210:213], a[84:87], v[150:153], v[210:213]\n"
      "v_mul_f32 v99, v142, v99\n"
      "v_cvt_pk_bf16_f32 v160, v160, v161\n"
      "v_mfma_f32_16x16x32_bf16 v[210:213], a[88:91], v[100:103], v[210:213]\n"
      "v_or3_b32 v214, v214, v215, v216\n"
      "v_bitop3_b32 v214, v214, v217, s33 bitop3:0xa8\n"
      "v_mfma_f32_16x16x32_bf16 v[210:213], a[92:95], v[124:127], v[210:213]\n"
      "v_fma_f32 v169, v220, v220, s51\n"
      "v_cndmask_b32 v168, v168, v194, s[60:61]\n"
      "v_mfma_f32_16x16x32_bf16 v[180:183], a[108:111], v[150:153], v[180:183]\n"
      "v_mul_f32 v156, v144, v156\n"
      "v_mfma_f32_16x16x32_bf16 v[180:183], a[112:115], v[100:103], v[180:183]\n"
      "buffer_load_dwordx4 v[192:195], v36, s[28:31], s47 offen\n"
      "v_mfma_f32_16x16x32_bf16 v[180:183], a[116:119], v[124:127], v[180:183]\n"
      "s_nop 0\n"
      "v_fma_f32 v82, v210, v210, s51\n"
      "v_cvt_pk_bf16_f32 v135, v136, v137\n"
      "v_mfma_f32_16x16x32_bf16 v[138:141], a[132:135], v[150:153], v[138:141]\n"
      "v_fmac_f32 v82, v211, v211\n"
      "v_fmac_f32 v169, v221, v221\n"
      "v_mfma_f32_16x16x32_bf16 v[138:141], a[136:139], v[100:103], v[138:141]\n"
      "v_mul_f32 v96, v142, v96\n"
      "v_cvt_pk_bf16_f32 v161, v162, v163\n"
      "v_mfma_f32_16x16x32_bf16 v[138:141], a[140:143], v[124:127], v[138:141]\n"
      "v_fmac_f32 v82, v212, v212\n"
      "v_fmac_f32 v82, v213, v213\n"
      "v_mfma_f32_16x16x32_bf16 v[130:133], v[164:167], a[144:147], 0\n"
      "v_fmac_f32 v169, v222, v222\n"
      "v_cvt_pk_bf16_f32 v96, v96, v97\n"
      "v_mfma_f32_16x16x32_bf16 v[130:133], v[108:111], a[148:151], v[130:133]\n"
      "v_fmac_f32 v82, v180, v180\n"
      "v_cvt_pk_bf16_f32 v205, v206, v207\n"
      "v_mfma_f32_16x16x32_bf16 v[130:133], v[184:187], a[152:155], v[130:133]\n"
      "v_mul_f32 v146, v144, v146\n"
      "v_mul_f32_e64 v114, v143, v114\n"
      "v_mfma_f32_16x16x32_bf16 v[130:133], v[150:153], a[156:159], v[130:133]\n"
      "v_fmac_f32 v82, v181, v181\n"
      "v_fmac_f32 v169, v223, v223\n"
      "v_mfma_f32_16x16x32_bf16 v[130:133], v[100:103], a[160:163], v[130:133]\n"
      "ds_write_b64 v6, v[134:135]\n"
      "v_mfma_f32_16x16x32_bf16 v[130:133], v[124:127], a[164:167], v[130:133]\n"
      "v_readlane_b32 s84, v37, s43\n"
      "v_mul_f32_e64 v149, v144, v149\n"
      "v_mfma_f32_16x16x32_bf16 v[196:199], v[164:167], a[168:171], 0\n"
      "buffer_load_dword v89, v29, s[28:31], s47 offen\n"
      "v_mfma_f32_16x16x32_bf16 v[196:199], v[108:111], a[172:175], v[196:199]\n"
      "ds_read_b128 v[134:137], v0 offset:2048\n"
      "v_mul_f32_e64 v98, v142, v98\n"
      "v_mfma_f32_16x16x32_bf16 v[196:199], v[184:187], a[176:179], v[196:199]\n"
      "v_cmp_ne_u32 s[56:57], 0, v214\n"
      "v_mul_f32 v159, v144, v159\n"
      "v_mfma_f32_16x16x32_bf16 v[196:199], v[150:153], a[180:183], v[196:199]\n"
      "v_fmac_f32 v169, v172, v172\n"
      "v_mul_f32 v112, v143, v112\n"
      "v_mfma_f32_16x16x32_bf16 v[196:199], v[100:103], a[184:187], v[196:199]\n"
      "v_cvt_pk_bf16_f32 v156, v156, v157\n"
      "v_fmac_f32 v82, v182, v182\n"
      "v_mfma_f32_16x16x32_bf16 v[196:199], v[124:127], a[188:191], v[196:199]\n"
      "v_mul_f32 v147, v144, v147\n"
      "v_mul_f32 v201, v143, v201\n"
      "v_mfma_f32_16x16x32_bf16 v[214:217], v[164:167], a[192:195], 0\n"
      "v_fmac_f32_e64 v82, v183, v183\n"
      "v_mul_f32_e64 v118, v142, v118\n"
      "v_mfma_f32_16x16x32_bf16 v[214:217], v[108:111], a[196:199], v[214:217]\n"
      "v_cvt_pk_bf16_f32 v157, v158, v159\n"
      "v_fmac_f32_e64 v169, v173, v173\n"
      "v_mfma_f32_16x16x32_bf16 v[214:217], v[184:187], a[200:203], v[214:217]\n"
      "ds_read_b128 v[164:167], v1 offset:8192\n"
      "v_cvt_pk_bf16_f32 v200, v200, v201\n"
      "v_mfma_f32_16x16x32_bf16 v[214:217], v[150:153], a[204:207], v[214:217]\n"
      "ds_read_b128 v[150:153], v1 offset:14336\n"
      "v_mul_f32 v119, v142, v119\n"
      "v_mfma_f32_16x16x32_bf16 v[214:217], v[100:103], a[208:211], v[214:217]\n"
      "ds_write_b64 v6, v[204:205] offset:6144\n"
      "v_mfma_f32_16x16x32_bf16 v[214:217], v[124:127], a[212:215], v[214:217]\n"
      "s_waitcnt lgkmcnt(3)\n"
      "v_cvt_pk_bf16_f32 v201, v202, v203\n"
      "v_cvt_pk_bf16_f32 v97, v98, v99\n"
      "v_mfma_f32_16x16x32_bf16 v[184:187], a[0:3], v[134:137], 0\n"
      "v_mul_f32_e64 v148, v144, v148\n"
      "v_cvt_pk_bf16_f32 v130, v130, v131\n"
      "v_mfma_f32_16x16x32_bf16 v[184:187], a[4:7], v[188:191], v[184:187]\n"
      "s_or_b32 s95, s84, s85\n"
      "v_fmac_f32 v82, v138, v138\n"
      "v_cvt_pk_bf16_f32 v202, v112, v113\n"
      "v_mfma_f32_16x16x32_bf16 v[184:187], a[8:11], v[120:123], v[184:187]\n"
      "s_waitcnt lgkmcnt(2)\n"
      "v_fmac_f32 v169, v174, v174\n"
      "v_fmac_f32_e64 v169, v175, v175\n"
      "v_mfma_f32_16x16x32_bf16 v[184:187], a[12:15], v[164:167], v[184:187]\n"
      "v_cvt_pk_bf16_f32 v214, v214, v215\n"
      "v_fmac_f32 v82, v139, v139\n"
      "v_mfma_f32_16x16x32_bf16 v[184:187], a[16:19], v[104:107], v[184:187]\n"
      "s_waitcnt lgkmcnt(1)\n"
      "v_fmac_f32_e64 v169, v176, v176\n"
      "v_cvt_pk_bf16_f32 v215, v216, v217\n"
      "v_mfma_f32_16x16x32_bf16 v[184:187], a[20:23], v[150:153], v[184:187]\n"
      "v_fmac_f32_e64 v82, v140, v140\n"
      "v_cvt_pk_bf16_f32 v196, v196, v197\n"
      "v_mfma_f32_16x16x32_bf16 v[204:207], a[24:27], v[134:137], 0\n"
      "v_mul_f32 v117, v142, v117\n"
      "v_fmac_f32 v82, v141, v141\n"
      "v_mfma_f32_16x16x32_bf16 v[204:207], a[28:31], v[188:191], v[204:207]\n"
      "v_mul_f32 v168, v208, v168\n"
      "v_fmac_f32 v169, v177, v177\n"
      "v_mfma_f32_16x16x32_bf16 v[204:207], a[32:35], v[120:123], v[204:207]\n"
      "v_fmac_f32_e64 v169, v178, v178\n"
      "v_fma_f32 v208, v184, v184, s51\n"
      "v_mfma_f32_16x16x32_bf16 v[204:207], a[36:39], v[164:167], v[204:207]\n"
      "ds_write_b64 v6, v[160:161] offset:12288\n"
      "v_mfma_f32_16x16x32_bf16 v[204:207], a[40:43], v[104:107], v[204:207]\n"
      "v_cvt_pk_bf16_f32 v197, v198, v199\n"
      "v_fmac_f32_e64 v208, v185, v185\n"
      "v_mfma_f32_16x16x32_bf16 v[204:207], a[44:47], v[150:153], v[204:207]\n"
      "v_cvt_pk_bf16_f32 v158, v146, v147\n"
      "v_fmac_f32 v169, v179, v179\n"
      "v_mfma_f32_16x16x32_bf16 v[108:111], a[48:51], v[134:137], 0\n"
      "s_nop 0\n"
      "v_permlane16_swap_b32 v169, v82\n"
      "v_mfma_f32_16x16x32_bf16 v[108:111], a[52:55], v[188:191], v[108:111]\n"
      "v_cvt_pk_bf16_f32 v203, v114, v115\n"
      "v_add_f32 v169, v169, v82\n"
      "v_mfma_f32_16x16x32_bf16 v[108:111], a[56:59], v[120:123], v[108:111]\n"
      "v_mov_b32 v82, v169\n"
      "v_cvt_pk_bf16_f32 v159, v148, v149\n"
      "v_mfma_f32_16x16x32_bf16 v[108:111], a[60:63], v[164:167], v[108:111]\n"
      "v_permlane32_swap_b32 v169, v82\n"
      "v_mfma_f32_16x16x32_bf16 v[108:111], a[64:67], v[104:107], v[108:111]\n"
      "v_cvt_pk_bf16_f32 v131, v132, v133\n"
      "v_fmac_f32 v208, v186, v186\n"
      "v_mfma_f32_16x16x32_bf16 v[108:111], a[68:71], v[150:153], v[108:111]\n"
      "v_add_f32 v169, v169, v82\n"
      "v_fmac_f32_e64 v208, v187, v187\n"
      "v_mfma_f32_16x16x32_bf16 v[146:149], a[72:75], v[134:137], 0\n"
      "v_rsq_f32 v169, v169\n"
      "v_mfma_f32_16x16x32_bf16 v[146:149], a[76:79], v[188:191], v[146:149]\n"
      "v_cvt_pk_bf16_f32 v168, v168, v168\n"
      "v_mov_b32 v82, v169\n"
      "v_mfma_f32_16x16x32_bf16 v[146:149], a[80:83], v[120:123], v[146:149]\n"
      "ds_read_b128 v[142:145], v0 offset:4096\n"
      "v_fmac_f32_e64 v208, v204, v204\n"
      "v_mfma_f32_16x16x32_bf16 v[146:149], a[84:87], v[164:167], v[146:149]\n"
      "ds_read_b128 v[112:115], v1 offset:4096\n"
      "v_fmac_f32 v208, v205, v205\n"
      "v_mfma_f32_16x16x32_bf16 v[146:149], a[88:91], v[104:107], v[146:149]\n"
      "v_permlane16_swap_b32 v169, v82\n"
      "v_mfma_f32_16x16x32_bf16 v[146:149], a[92:95], v[150:153], v[146:149]\n"
      "v_cvt_pk_bf16_f32 v99, v118, v119\n"
      "v_mul_f32 v211, v82, v211\n"
      "v_mfma_f32_16x16x32_bf16 v[100:103], a[96:99], v[134:137], 0\n"
      "v_fmac_f32 v208, v206, v206\n"
      "v_mul_f32_e64 v169, s50, v169\n"
      "v_mfma_f32_16x16x32_bf16 v[100:103], a[100:103], v[188:191], v[100:103]\n"
      "v_mul_f32 v182, v82, v182\n"
      "v_mul_f32 v141, v82, v141\n"
      "v_mfma_f32_16x16x32_bf16 v[100:103], a[104:107], v[120:123], v[100:103]\n"
      "v_fma_f32 v154, v146, v146, s51\n"
      "v_fmac_f32 v154, v147, v147\n"
      "v_mfma_f32_16x16x32_bf16 v[100:103], a[108:111], v[164:167], v[100:103]\n"
      "v_mul_f32 v173, v169, v173\n"
      "v_fmac_f32 v154, v148, v148\n"
      "v_mfma_f32_16x16x32_bf16 v[100:103], a[112:115], v[104:107], v[100:103]\n"
      "v_mul_f32 v138, v82, v138\n"
      "v_mul_f32 v180, v82, v180\n"
      "v_mfma_f32_16x16x32_bf16 v[100:103], a[116:119], v[150:153], v[100:103]\n"
      "v_fmac_f32 v208, v207, v207\n"
      "v_mul_f32 v210, v82, v210\n"
      "v_mfma_f32_16x16x32_bf16 v[160:163], a[120:123], v[134:137], 0\n"
      "v_fmac_f32 v208, v108, v108\n"
      "v_fmac_f32 v154, v149, v149\n"
      "v_mfma_f32_16x16x32_bf16 v[160:163], a[124:127], v[188:191], v[160:163]\n"
      "v_mul_f32 v221, v169, v221\n"
      "v_mul_f32 v177, v169, v177\n"
      "v_mfma_f32_16x16x32_bf16 v[160:163], a[128:131], v[120:123], v[160:163]\n"
      "v_mul_f32 v212, v82, v212\n"
      "v_fmac_f32 v208, v109, v109\n"
      "v_mfma_f32_16x16x32_bf16 v[160:163], a[132:135], v[164:167], v[160:163]\n"
      "v_fmac_f32 v154, v100, v100\n"
      "v_fmac_f32 v208, v110, v110\n"
      "v_mfma_f32_16x16x32_bf16 v[160:163], a[136:139], v[104:107], v[160:163]\n"
      "v_fmac_f32 v154, v101, v101\n"
      "v_mul_f32 v220, v169, v220\n"
      "v_mfma_f32_16x16x32_bf16 v[160:163], a[140:143], v[150:153], v[160:163]\n"
      "v_fmac_f32 v154, v102, v102\n"
      "v_mul_f32 v181, v82, v181\n"
      "v_mfma_f32_16x16x32_bf16 v[216:219], v[134:137], a[144:147], 0\n"
      "v_mul_f32 v139, v82, v139\n"
      "v_mul_f32 v176, v169, v176\n"
      "v_mfma_f32_16x16x32_bf16 v[216:219], v[188:191], a[148:151], v[216:219]\n"
      "ds_read_b128 v[124:127], v0 offset:10240\n"
      "v_fmac_f32 v154, v103, v103\n"
      "v_mfma_f32_16x16x32_bf16 v[216:219], v[120:123], a[152:155], v[216:219]\n"
      "ds_write_b128 v5, v[156:159] offset:12288\n"
      "v_pk_mul_f32 v[222:223], v[222:223], v[168:169] op_sel:[0,1]\n"
      "v_mfma_f32_16x16x32_bf16 v[216:219], v[164:167], a[156:159], v[216:219]\n"
      "v_fmac_f32 v208, v111, v111\n"
      "v_mul_f32 v213, v82, v213\n"
      "v_mfma_f32_16x16x32_bf16 v[216:219], v[104:107], a[160:163], v[216:219]\n"
      "v_fmac_f32 v154, v160, v160\n"
      "v_mul_f32 v140, v82, v140\n"
      "v_mfma_f32_16x16x32_bf16 v[216:219], v[150:153], a[164:167], v[216:219]\n"
      "v_fmac_f32_e64 v154, v161, v161\n"
      "v_cvt_pk_bf16_f32 v210, v210, v211\n"
      "v_mfma_f32_16x16x32_bf16 v[156:159], v[134:137], a[168:171], 0\n"
      "ds_write_b128 v5, v[200:203] offset:6144\n"
      "v_mfma_f32_16x16x32_bf16 v[156:159], v[188:191], a[172:175], v[156:159]\n"
      "v_cvt_pk_bf16_f32 v98, v116, v117\n"
      "v_cvt_pk_bf16_f32 v176, v176, v177\n"
      "v_mfma_f32_16x16x32_bf16 v[156:159], v[120:123], a[176:179], v[156:159]\n"
      "v_mul_f32 v179, v169, v179\n"
      "v_fmac_f32 v154, v162, v162\n"
      "v_mfma_f32_16x16x32_bf16 v[156:159], v[164:167], a[180:183], v[156:159]\n"
      "ds_read_b128 v[116:119], v1 offset:16384\n"
      "v_mul_f32_e64 v178, v169, v178\n"
      "v_mfma_f32_16x16x32_bf16 v[156:159], v[104:107], a[184:187], v[156:159]\n"
      "v_cvt_pk_bf16_f32 v132, v216, v217\n"
      "v_mfma_f32_16x16x32_bf16 v[156:159], v[150:153], a[188:191], v[156:159]\n"
      "ds_write_b128 v5, v[96:99]\n"
      "v_mfma_f32_16x16x32_bf16 v[96:99], v[134:137], a[192:195], 0\n"
      "ds_read_b128 v[134:137], v1 offset:10240\n"
      "v_mul_f32 v183, v82, v183\n"
      "v_mfma_f32_16x16x32_bf16 v[96:99], v[188:191], a[196:199], v[96:99]\n"
      "v_fmac_f32 v154, v163, v163\n"
      "v_cvt_pk_bf16_f32 v138, v138, v139\n"
      "v_mfma_f32_16x16x32_bf16 v[96:99], v[120:123], a[200:203], v[96:99]\n"
      "v_permlane16_swap_b32 v208, v154\n"
      "v_mfma_f32_16x16x32_bf16 v[96:99], v[164:167], a[204:207], v[96:99]\n"
      "ds_read_b128 v[200:203], v0 offset:16384\n"
      "v_add_f32 v208, v208, v154\n"
      "v_mfma_f32_16x16x32_bf16 v[96:99], v[104:107], a[208:211], v[96:99]\n"
      "s_waitcnt lgkmcnt(3)\n"
      "v_mov_b32 v154, v208\n"
      "v_cvt_pk_bf16_f32 v198, v156, v157\n"
      "v_mfma_f32_16x16x32_bf16 v[96:99], v[150:153], a[212:215], v[96:99]\n"
      "s_or_b32 s95, s95, s48\n"
      "v_permlane32_swap_b32 v208, v154\n"
      "v_mfma_f32_16x16x32_bf16 v[104:107], a[0:3], v[142:145], 0\n"
      "v_cvt_pk_bf16_f32 v177, v178, v179\n"
      "v_add_f32_e64 v208, v208, v154\n"
      "v_mfma_f32_16x16x32_bf16 v[104:107], a[4:7], v[112:115], v[104:107]\n"
      "s_waitcnt lgkmcnt(1)\n"
      "v_cvt_pk_bf16_f32 v211, v212, v213\n"
      "v_mul_f32 v172, v169, v172\n"
      "v_mfma_f32_16x16x32_bf16 v[104:107], a[8:11], v[124:127], v[104:107]\n"
      "ds_read_u16 v82, v11 offset:32\n"
      "v_mul_f32 v174, v169, v174\n"
      "v_mfma_f32_16x16x32_bf16 v[104:107], a[12:15], v[134:137], v[104:107]\n"
      "s_waitcnt lgkmcnt(1)\n"
      "v_rsq_f32_e64 v208, v208\n"
      "v_mfma_f32_16x16x32_bf16 v[104:107], a[16:19], v[200:203], v[104:107]\n"
      "v_mov_b32 v154, v208\n"
      "v_cvt_pk_bf16_f32 v139, v140, v141\n"
      "v_mfma_f32_16x16x32_bf16 v[104:107], a[20:23], v[116:119], v[104:107]\n"
      "v_permlane16_swap_b32 v208, v154\n"
      "v_mfma_f32_16x16x32_bf16 v[188:191], a[24:27], v[142:145], 0\n"
      "v_cvt_pk_bf16_f32 v220, v220, v221\n"
      "v_mul_f32 v147, v154, v147\n"
      "v_mfma_f32_16x16x32_bf16 v[188:191], a[28:31], v[112:115], v[188:191]\n"
      "s_cmp_ge_u32 s95, 0x40000000\n"
      "s_cselect_b32 s94, -1, -2\n"
      "v_mul_f32 v175, v169, v175\n"
      "v_mfma_f32_16x16x32_bf16 v[188:191], a[32:35], v[124:127], v[188:191]\n"
      "v_mul_f32 v161, v154, v161\n"
      "v_mul_f32 v208, s50, v208\n"
      "v_mfma_f32_16x16x32_bf16 v[188:191], a[36:39], v[134:137], v[188:191]\n"
      "v_mul_f32 v100, v154, v100\n"
      "v_mul_f32 v163, v154, v163\n"
      "v_mfma_f32_16x16x32_bf16 v[188:191], a[40:43], v[200:203], v[188:191]\n"
      "v_cvt_pk_bf16_f32 v216, v96, v97\n"
      "v_cvt_pk_bf16_f32 v221, v222, v223\n"
      "v_mfma_f32_16x16x32_bf16 v[188:191], a[44:47], v[116:119], v[188:191]\n"
      "v_cvt_pk_bf16_f32 v133, v218, v219\n"
      "v_mul_f32 v184, v208, v184\n"
      "v_mfma_f32_16x16x32_bf16 v[120:123], a[48:51], v[142:145], 0\n"
      "v_mul_f32 v148, v154, v148\n"
      "v_mul_f32_e64 v185, v208, v185\n"
      "v_mfma_f32_16x16x32_bf16 v[120:123], a[52:55], v[112:115], v[120:123]\n"
      "v_fma_f32 v218, v104, v104, s51\n"
      "v_cvt_pk_bf16_f32 v217, v98, v99\n"
      "v_mfma_f32_16x16x32_bf16 v[120:123], a[56:59], v[124:127], v[120:123]\n"
      "v_fmac_f32 v218, v105, v105\n"
      "v_mul_f32 v160, v154, v160\n"
      "v_mfma_f32_16x16x32_bf16 v[120:123], a[60:63], v[134:137], v[120:123]\n"
      "v_fmac_f32_e64 v218, v106, v106\n"
      "v_cvt_pk_bf16_f32 v212, v180, v181\n"
      "v_mfma_f32_16x16x32_bf16 v[120:123], a[64:67], v[200:203], v[120:123]\n"
      "s_and_b32 s86, s86, -4\n"
      "v_cvt_pk_bf16_f32 v160, v160, v161\n"
      "v_mul_f32 v111, v208, v111\n"
      "v_mfma_f32_16x16x32_bf16 v[120:123], a[68:71], v[116:119], v[120:123]\n"
      "v_mul_f32 v162, v154, v162\n"
      "v_fmac_f32 v218, v107, v107\n"
      "v_mfma_f32_16x16x32_bf16 v[150:153], a[72:75], v[142:145], 0\n"
      "v_fmac_f32 v218, v188, v188\n"
      "v_mul_f32 v103, v154, v103\n"
      "v_mfma_f32_16x16x32_bf16 v[150:153], a[76:79], v[112:115], v[150:153]\n"
      "v_mul_f32_e64 v101, v154, v101\n"
      "v_cvt_pk_bf16_f32 v223, v174, v175\n"
      "v_mfma_f32_16x16x32_bf16 v[150:153], a[80:83], v[124:127], v[150:153]\n"
      "ds_write_b16 v12, v168 offset:16\n"
      "v_mfma_f32_16x16x32_bf16 v[150:153], a[84:87], v[134:137], v[150:153]\n"
      "v_mul_f32 v146, v154, v146\n"
      "v_fmac_f32 v218, v189, v189\n"
      "v_mfma_f32_16x16x32_bf16 v[150:153], a[88:91], v[200:203], v[150:153]\n"
      "v_mul_f32 v149, v154, v149\n"
      "v_cvt_pk_bf16_f32 v199, v158, v159\n"
      "v_mfma_f32_16x16x32_bf16 v[150:153], a[92:95], v[116:119], v[150:153]\n"
      "v_mul_f32 v108, v208, v108\n"
      "v_mul_f32_e64 v207, v208, v207\n"
      "v_mfma_f32_16x16x32_bf16 v[168:171], a[96:99], v[142:145], 0\n"
      "v_mul_f32 v204, v208, v204\n"
      "v_mul_f32 v110, v208, v110\n"
      "v_mfma_f32_16x16x32_bf16 v[168:171], a[100:103], v[112:115], v[168:171]\n"
      "v_mul_f32 v109, v208, v109\n"
      "v_mul_f32 v187, v208, v187\n"
      "v_mfma_f32_16x16x32_bf16 v[168:171], a[104:107], v[124:127], v[168:171]\n"
      "v_cvt_pk_bf16_f32 v146, v146, v147\n"
      "v_fmac_f32 v218, v190, v190\n"
      "v_mfma_f32_16x16x32_bf16 v[168:171], a[108:111], v[134:137], v[168:171]\n"
      "v_mul_f32 v102, v154, v102\n"
      "v_mul_f32 v206, v208, v206\n"
      "v_mfma_f32_16x16x32_bf16 v[168:171], a[112:115], v[200:203], v[168:171]\n"
      "v_mul_f32 v186, v208, v186\n"
      "v_fma_f32 v141, v150, v150, s51\n"
      "v_mfma_f32_16x16x32_bf16 v[168:171], a[116:119], v[116:119], v[168:171]\n"
      "v_cvt_pk_bf16_f32 v161, v162, v163\n"
      "v_cvt_pk_bf16_f32 v147, v148, v149\n"
      "v_mfma_f32_16x16x32_bf16 v[154:157], a[120:123], v[142:145], 0\n"
      "v_mul_f32 v205, v208, v205\n"
      "v_fmac_f32 v218, v191, v191\n"
      "v_mfma_f32_16x16x32_bf16 v[154:157], a[124:127], v[112:115], v[154:157]\n"
      "v_cvt_pk_bf16_f32 v148, v100, v101\n"
      "v_fmac_f32_e64 v141, v151, v151\n"
      "v_mfma_f32_16x16x32_bf16 v[154:157], a[128:131], v[124:127], v[154:157]\n"
      "v_cvt_pk_bf16_f32 v184, v184, v185\n"
      "v_cvt_pk_bf16_f32 v185, v186, v187\n"
      "v_mfma_f32_16x16x32_bf16 v[154:157], a[132:135], v[134:137], v[154:157]\n"
      "v_fmac_f32 v141, v152, v152\n"
      "v_fmac_f32 v218, v120, v120\n"
      "v_mfma_f32_16x16x32_bf16 v[154:157], a[136:139], v[200:203], v[154:157]\n"
      "v_cvt_pk_bf16_f32 v222, v172, v173\n"
      "v_fmac_f32 v141, v153, v153\n"
      "v_mfma_f32_16x16x32_bf16 v[154:157], a[140:143], v[116:119], v[154:157]\n"
      "v_fmac_f32 v141, v168, v168\n"
      "v_fmac_f32 v141, v169, v169\n"
      "v_mfma_f32_16x16x32_bf16 v[96:99], v[142:145], a[144:147], 0\n"
      "v_fmac_f32 v141, v170, v170\n"
      "v_fmac_f32_e64 v218, v121, v121\n"
      "v_mfma_f32_16x16x32_bf16 v[96:99], v[112:115], a[148:151], v[96:99]\n"
      "v_cvt_pk_bf16_f32 v149, v102, v103\n"
      "v_fmac_f32 v218, v122, v122\n"
      "v_mfma_f32_16x16x32_bf16 v[96:99], v[124:127], a[152:155], v[96:99]\n"
      "v_cvt_pk_bf16_f32 v213, v182, v183\n"
      "v_fmac_f32 v141, v171, v171\n"
      "v_mfma_f32_16x16x32_bf16 v[96:99], v[134:137], a[156:159], v[96:99]\n"
      "v_fmac_f32 v218, v123, v123\n"
      "v_fmac_f32 v141, v154, v154\n"
      "v_mfma_f32_16x16x32_bf16 v[96:99], v[200:203], a[160:163], v[96:99]\n"
      "v_fmac_f32 v141, v155, v155\n"
      "v_fmac_f32 v141, v156, v156\n"
      "v_mfma_f32_16x16x32_bf16 v[96:99], v[116:119], a[164:167], v[96:99]\n"
      "v_cvt_pk_bf16_f32 v187, v206, v207\n"
      "v_fmac_f32 v141, v157, v157\n"
      "v_mfma_f32_16x16x32_bf16 v[206:209], v[142:145], a[168:171], 0\n"
      "s_nop 0\n"
      "v_permlane16_swap_b32 v218, v141\n"
      "v_mfma_f32_16x16x32_bf16 v[206:209], v[112:115], a[172:175], v[206:209]\n"
      "v_add_f32 v218, v218, v141\n"
      "v_mov_b32 v141, v218\n"
      "v_mfma_f32_16x16x32_bf16 v[206:209], v[124:127], a[176:179], v[206:209]\n"
      "s_nop 0\n"
      "v_permlane32_swap_b32 v218, v141\n"
      "v_mfma_f32_16x16x32_bf16 v[206:209], v[134:137], a[180:183], v[206:209]\n"
      "ds_read_b128 v[172:175], v2 offset:18432\n"
      "v_add_u32 v12, 32, v12\n"
      "v_mfma_f32_16x16x32_bf16 v[206:209], v[200:203], a[184:187], v[206:209]\n"
      "ds_read_b128 v[100:103], v2 offset:24576\n"
      "v_add_f32 v218, v218, v141\n"
      "v_mfma_f32_16x16x32_bf16 v[206:209], v[116:119], a[188:191], v[206:209]\n"
      "v_rsq_f32 v218, v218\n"
      "v_mfma_f32_16x16x32_bf16 v[178:181], v[142:145], a[192:195], 0\n"
      "ds_read_b128 v[162:165], v2 offset:21504\n"
      "v_mov_b32 v141, v218\n"
      "v_mfma_f32_16x16x32_bf16 v[178:181], v[112:115], a[196:199], v[178:181]\n"
      "s_nop 0\n"
      "v_permlane16_swap_b32_e64 v218, v141\n"
      "v_mfma_f32_16x16x32_bf16 v[178:181], v[124:127], a[200:203], v[178:181]\n"
      "v_mul_f32 v170, v141, v170\n"
      "v_cvt_pk_bf16_f32 v77, v98, v99\n"
      "v_mfma_f32_16x16x32_bf16 v[178:181], v[134:137], a[204:207], v[178:181]\n"
      "s_waitcnt lgkmcnt(2)\n"
      "v_cvt_pk_bf16_f32 v76, v96, v97\n"
      "v_cvt_pk_bf16_f32 v108, v108, v109\n"
      "v_mfma_f32_16x16x32_bf16 v[178:181], v[200:203], a[208:211], v[178:181]\n"
      "ds_read_b128 v[126:129], v10 offset:32\n"
      "v_mul_f32 v151, v141, v151\n"
      "v_mfma_f32_16x16x32_bf16 v[178:181], v[116:119], a[212:215], v[178:181]\n"
      "s_cmp_gt_u32 s42, s44\n"
      "s_cselect_b32 s95, s94, s95\n"
      "v_mul_f32 v171, v141, v171\n"
      "v_mfma_f32_16x16x32_bf16 v[96:99], a[216:219], v[172:175], 0\n"
      "s_waitcnt lgkmcnt(1)\n"
      "v_mul_f32 v155, v141, v155\n"
      "v_cvt_pk_bf16_f32 v80, v206, v207\n"
      "v_mfma_f32_16x16x32_bf16 v[96:99], a[220:223], v[162:165], v[96:99]\n"
      "v_mul_f32_e64 v218, s50, v218\n"
      "v_cvt_pk_bf16_f32 v186, v204, v205\n"
      "v_mfma_f32_16x16x32_bf16 v[96:99], a[224:227], v[100:103], v[96:99]\n"
      "ds_read_b64 v[124:125], v10 offset:24\n"
      "v_cvt_pk_bf16_f32 v109, v110, v111\n"
      "v_mfma_f32_16x16x32_bf16 v[116:119], a[240:243], v[172:175], 0\n"
      "v_mul_f32 v152, v141, v152\n"
      "v_mul_f32 v169, v141, v169\n"
      "v_mfma_f32_16x16x32_bf16 v[116:119], a[244:247], v[162:165], v[116:119]\n"
      "v_mul_f32 v153, v141, v153\n"
      "v_mul_f32 v154, v141, v154\n"
      "v_mfma_f32_16x16x32_bf16 v[116:119], a[248:251], v[100:103], v[116:119]\n"
      "ds_read_b128 v[200:203], v2 offset:27648\n"
      "v_mul_f32 v150, v141, v150\n"
      "v_mfma_f32_16x16x32_bf16 v[134:137], v[232:235], v[172:175], 0\n"
      "ds_read_b128 v[172:175], v2 offset:18688\n"
      "v_cvt_pk_bf16_f32 v150, v150, v151\n"
      "v_mfma_f32_16x16x32_bf16 v[134:137], v[236:239], v[162:165], v[134:137]\n"
      "v_mul_f32 v168, v141, v168\n"
      "v_cvt_pk_bf16_f32 v151, v152, v153\n"
      "v_mfma_f32_16x16x32_bf16 v[134:137], v[240:243], v[100:103], v[134:137]\n"
      "s_add_u32 s42, s42, 1\n"
      "v_mul_f32 v157, v141, v157\n"
      "v_mul_f32 v156, v141, v156\n"
      "v_mfma_f32_16x16x32_bf16 v[162:165], v[210:213], v[220:223], v[40:43]\n"
      "s_waitcnt lgkmcnt(2)\n"
      "v_cvt_pk_bf16_f32 v152, v168, v169\n"
      "v_cvt_pk_bf16_f32 v153, v170, v171\n"
      "v_mfma_f32_16x16x32_bf16 v[204:207], v[146:149], v[220:223], v[44:47]\n"
      "ds_read_b128 v[112:115], v2 offset:33792\n"
      "v_mul_f32 v104, v218, v104\n"
      "v_mfma_f32_16x16x32_bf16 v[168:171], v[150:153], v[220:223], v[48:51]\n"
      "ds_read_b128 v[100:103], v2 offset:30720\n"
      "v_mul_f32 v105, v218, v105\n"
      "v_mfma_f32_16x16x32_bf16 v[140:143], v[126:129], v[220:223], v[24:27]\n"
      "s_waitcnt lgkmcnt(2)\n"
      "v_cvt_pk_bf16_f32 v81, v208, v209\n"
      "v_cvt_pk_bf16_f32 v154, v154, v155\n"
      "v_mfma_f32_16x16x16_bf16 v[162:165], v[138:139], v[176:177], v[162:165]\n"
      "v_mul_f32 v121, v218, v121\n"
      "v_cvt_pk_bf16_f32 v84, v178, v179\n"
      "v_cvt_pk_bf16_f32 v155, v156, v157\n"
      "v_mfma_f32_16x16x16_bf16 v[204:207], v[160:161], v[176:177], v[204:207]\n"
      "v_mul_f32_e64 v106, v218, v106\n"
      "v_cvt_pk_bf16_f32 v85, v180, v181\n"
      "v_cvt_pk_bf16_f32 v104, v104, v105\n"
      "v_mfma_f32_16x16x16_bf16 v[168:171], v[154:155], v[176:177], v[168:171]\n"
      "v_mul_f32 v120, v218, v120\n"
      "v_mul_f32 v107, v218, v107\n"
      "v_max3_f32 v156, v162, v163, v164\n"
      "v_mfma_f32_16x16x16_bf16 v[140:143], v[124:125], v[176:177], v[140:143]\n"
      "v_cvt_pk_bf16_f32 v120, v120, v121\n"
      "v_max3_f32 v156, v156, v206, v207\n"
      "v_max3_f32 v176, v165, v204, v205\n"
      "v_mfma_f32_16x16x32_bf16 v[96:99], a[228:231], v[200:203], v[96:99]\n"
      "s_waitcnt lgkmcnt(0)\n"
      "v_max3_f32 v176, v176, v168, v169\n"
      "v_max3_f32 v156, v156, v170, v171\n"
      "v_mfma_f32_16x16x32_bf16 v[96:99], a[232:235], v[100:103], v[96:99]\n"
      "v_max3_f32 v156, v156, v176, v140\n"
      "v_mov_b32 v157, v156\n"
      "v_mfma_f32_16x16x32_bf16 v[96:99], a[236:239], v[112:115], v[96:99]\n"
      "ds_read_b128 v[176:179], v2 offset:21760\n"
      "v_cvt_pk_bf16_f32 v105, v106, v107\n"
      "v_mfma_f32_16x16x32_bf16 v[116:119], a[252:255], v[200:203], v[116:119]\n"
      "v_permlane16_swap_b32_e64 v156, v157\n"
      "v_mfma_f32_16x16x32_bf16 v[116:119], v[224:227], v[100:103], v[116:119]\n"
      "v_max_f32 v156, v156, v157\n"
      "v_mov_b32 v157, v156\n"
      "v_mfma_f32_16x16x32_bf16 v[116:119], v[228:231], v[112:115], v[116:119]\n"
      "ds_read_b128 v[142:145], v2 offset:24832\n"
      "v_permlane32_swap_b32 v156, v157\n"
      "v_max_f32 v156, v156, v157\n"
      "v_sub_f32_e64 v140, v140, v156\n"
      "v_pk_mul_f32 v[122:123], v[122:123], v[218:219] op_sel_hi:[1,0]\n"
      "v_pk_mul_f32 v[188:189], v[188:189], v[218:219] op_sel_hi:[1,0]\n"
      "v_cvt_pk_bf16_f32 v106, v188, v189\n"
      "v_pk_add_f32 v[164:165], v[164:165], v[156:157] op_sel_hi:[1,0] neg_lo:[0,1] neg_hi:[0,1]\n"
      "v_pk_add_f32 v[204:205], v[204:205], v[156:157] op_sel_hi:[1,0] neg_lo:[0,1] neg_hi:[0,1]\n"
      "v_pk_mul_f32 v[190:191], v[190:191], v[218:219] op_sel_hi:[1,0]\n"
      "v_cvt_pk_bf16_f32 v107, v190, v191\n"
      "v_pk_add_f32 v[206:207], v[206:207], v[156:157] op_sel_hi:[1,0] neg_lo:[0,1] neg_hi:[0,1]\n"
      "v_pk_add_f32 v[170:171], v[170:171], v[156:157] op_sel_hi:[1,0] neg_lo:[0,1] neg_hi:[0,1]\n"
      "v_pk_add_f32 v[168:169], v[168:169], v[156:157] op_sel_hi:[1,0] neg_lo:[0,1] neg_hi:[0,1]\n"
      "v_pk_add_f32 v[162:163], v[162:163], v[156:157] op_sel_hi:[1,0] neg_lo:[0,1] neg_hi:[0,1]\n"
      "v_mfma_f32_16x16x32_bf16 v[134:137], v[244:247], v[200:203], v[134:137]\n"
      "v_exp_f32 v162, v162\n"
      "v_mfma_f32_16x16x32_bf16 v[134:137], v[248:251], v[100:103], v[134:137]\n"
      "v_exp_f32 v164, v164\n"
      "v_mfma_f32_16x16x32_bf16 v[134:137], v[252:255], v[112:115], v[134:137]\n"
      "v_exp_f32 v206, v206\n"
      "v_mfma_f32_16x16x32_bf16 v[188:191], v[210:213], v[184:187], v[52:55]\n"
      "v_exp_f32 v207, v207\n"
      "v_mfma_f32_16x16x32_bf16 v[156:159], v[146:149], v[184:187], v[56:59]\n"
      "v_exp_f32 v140, v140\n"
      "v_mfma_f32_16x16x32_bf16 v[100:103], v[150:153], v[184:187], v[60:63]\n"
      "v_exp_f32 v163, v163\n"
      "v_mfma_f32_16x16x32_bf16 v[110:113], v[126:129], v[184:187], v[26:29]\n"
      "v_exp_f32_e64 v204, v204\n"
      "v_mfma_f32_16x16x16_bf16 v[188:191], v[138:139], v[108:109], v[188:191]\n"
      "v_exp_f32_e64 v168, v168\n"
      "v_cvt_pk_bf16_f32 v90, v140, 0\n"
      "v_mfma_f32_16x16x16_bf16 v[156:159], v[160:161], v[108:109], v[156:159]\n"
      "v_cvt_pk_bf16_f32 v162, v162, v163\n"
      "v_exp_f32 v165, v165\n"
      "v_mfma_f32_16x16x16_bf16 v[100:103], v[154:155], v[108:109], v[100:103]\n"
      "v_cvt_pk_bf16_f32 v163, v164, v165\n"
      "v_exp_f32 v169, v169\n"
      "v_mfma_f32_16x16x16_bf16 v[110:113], v[124:125], v[108:109], v[110:113]\n"
      "v_cvt_pk_bf16_f32 v165, v206, v207\n"
      "s_waitcnt vmcnt(1)\n"
      "v_or3_b32 v192, v192, v193, v194\n"
      "v_max3_f32 v187, v188, v189, v190\n"
      "v_mfma_f32_16x16x32_bf16 v[200:203], a[216:219], v[172:175], 0\n"
      "s_waitcnt lgkmcnt(0)\n"
      "v_max3_f32 v187, v187, v158, v159\n"
      "v_max3_f32 v187, v187, v102, v103\n"
      "v_mfma_f32_16x16x32_bf16 v[200:203], a[220:223], v[176:179], v[200:203]\n"
      "v_exp_f32 v205, v205\n"
      "v_mfma_f32_16x16x32_bf16 v[200:203], a[224:227], v[142:145], v[200:203]\n"
      "s_waitcnt vmcnt(0)\n"
      "v_or3_b32 v192, v192, v195, v89\n"
      "v_max3_f32 v218, v191, v156, v157\n"
      "v_mfma_f32_16x16x32_bf16 v[112:115], a[240:243], v[172:175], 0\n"
      "v_exp_f32 v170, v170\n"
      "v_mfma_f32_16x16x32_bf16 v[112:115], a[244:247], v[176:179], v[112:115]\n"
      "v_exp_f32 v171, v171\n"
      "v_mfma_f32_16x16x32_bf16 v[112:115], a[248:251], v[142:145], v[112:115]\n"
      "v_max3_f32 v218, v218, v100, v101\n"
      "v_cvt_pk_bf16_f32 v164, v204, v205\n"
      "v_mfma_f32_16x16x32_bf16 v[204:207], v[232:235], v[172:175], 0\n"
      "v_cvt_pk_bf16_f32 v89, v170, v171\n"
      "v_max3_f32 v187, v187, v218, v110\n"
      "v_mfma_f32_16x16x32_bf16 v[204:207], v[236:239], v[176:179], v[204:207]\n"
      "v_cvt_pk_bf16_f32 v88, v168, v169\n"
      "v_mov_b32 v218, v187\n"
      "v_mfma_f32_16x16x32_bf16 v[204:207], v[240:243], v[142:145], v[204:207]\n"
      "s_nop 0\n"
      "v_permlane16_swap_b32 v187, v218\n"
      "v_mfma_f32_16x16x32_bf16 v[174:177], v[32:35], v[162:165], 0\n"
      "v_max_f32 v187, v187, v218\n"
      "v_mov_b32 v218, v187\n"
      "v_mfma_f32_16x16x32_bf16 v[174:177], v[32:35], v[88:91], v[174:177]\n"
      "s_nop 0\n"
      "v_permlane32_swap_b32_e64 v187, v218\n"
      "v_mfma_f32_16x16x32_bf16 v[168:171], v[130:133], v[162:165], 0\n"
      "ds_read_u16 v86, v11 offset:64\n"
      "v_max_f32_e64 v187, v187, v218\n"
      "v_mfma_f32_16x16x32_bf16 v[168:171], v[76:79], v[88:91], v[168:171]\n"
      "ds_write_b32 v31, v192 offset:4608\n"
      "v_mfma_f32_16x16x32_bf16 v[218:221], v[196:199], v[162:165], 0\n"
      "v_rcp_f32 v174, v174\n"
      "v_mfma_f32_16x16x32_bf16 v[218:221], v[80:83], v[88:91], v[218:221]\n"
      "ds_read_b128 v[176:179], v2 offset:27904\n"
      "v_sub_f32 v110, v110, v187\n"
      "v_exp_f32_e64 v110, v110\n"
      "v_cvt_pk_bf16_f32 v121, v122, v123\n"
      "v_pk_add_f32 v[190:191], v[190:191], v[186:187] op_sel:[0,1] neg_lo:[0,1] neg_hi:[0,1]\n"
      "v_pk_mul_f32 v[170:171], v[170:171], v[174:175] op_sel_hi:[1,0]\n"
      "v_pk_add_f32 v[100:101], v[100:101], v[186:187] op_sel:[0,1] neg_lo:[0,1] neg_hi:[0,1]\n"
      "v_pk_add_f32 v[158:159], v[158:159], v[186:187] op_sel:[0,1] neg_lo:[0,1] neg_hi:[0,1]\n"
      "v_pk_add_f32 v[156:157], v[156:157], v[186:187] op_sel:[0,1] neg_lo:[0,1] neg_hi:[0,1]\n"
      "v_pk_mul_f32 v[220:221], v[220:221], v[174:175] op_sel_hi:[1,0]\n"
      "v_pk_mul_f32 v[168:169], v[168:169], v[174:175] op_sel_hi:[1,0]\n"
      "v_pk_mul_f32 v[218:219], v[218:219], v[174:175] op_sel_hi:[1,0]\n"
      "v_pk_add_f32 v[102:103], v[102:103], v[186:187] op_sel:[0,1] neg_lo:[0,1] neg_hi:[0,1]\n"
      "v_pk_add_f32 v[188:189], v[188:189], v[186:187] op_sel:[0,1] neg_lo:[0,1] neg_hi:[0,1]\n"
      "v_mfma_f32_16x16x32_bf16 v[184:187], v[214:217], v[162:165], 0\n"
      "s_waitcnt lgkmcnt(2)\n"
      "v_exp_f32 v103, v103\n"
      "v_cvt_pk_bf16_f32 v168, v168, v169\n"
      "v_mfma_f32_16x16x32_bf16 v[184:187], v[84:87], v[88:91], v[184:187]\n"
      "v_exp_f32 v100, v100\n"
      "v_mfma_f32_16x16x32_bf16 v[142:145], v[210:213], v[104:107], v[64:67]\n"
      "v_exp_f32_e64 v102, v102\n"
      "v_mfma_f32_16x16x32_bf16 v[180:183], v[146:149], v[104:107], v[68:71]\n"
      "s_waitcnt lgkmcnt(0)\n"
      "v_cvt_pk_bf16_f32 v169, v170, v171\n"
      "v_cvt_pk_bf16_f32 v170, v218, v219\n"
      "v_mfma_f32_16x16x32_bf16 v[164:167], v[150:153], v[104:107], v[72:75]\n"
      "ds_read_b128 v[192:195], v2 offset:30976\n"
      "v_cvt_pk_bf16_f32 v171, v220, v221\n"
      "v_mfma_f32_16x16x32_bf16 v[218:221], v[126:129], v[104:107], v[28:31]\n"
      "ds_read_b128 v[104:107], v2 offset:34048\n"
      "v_exp_f32_e64 v101, v101\n"
      "v_mfma_f32_16x16x16_bf16 v[142:145], v[138:139], v[120:121], v[142:145]\n"
      "v_cvt_pk_bf16_f32 v93, v102, v103\n"
      "v_exp_f32 v188, v188\n"
      "v_mfma_f32_16x16x16_bf16 v[180:183], v[160:161], v[120:121], v[180:183]\n"
      "v_exp_f32 v156, v156\n"
      "v_cvt_pk_bf16_f32 v92, v100, v101\n"
      "v_mfma_f32_16x16x16_bf16 v[164:167], v[154:155], v[120:121], v[164:167]\n"
      "v_cvt_pk_bf16_f32 v94, v110, 0\n"
      "v_exp_f32_e64 v189, v189\n"
      "v_mfma_f32_16x16x16_bf16 v[218:221], v[124:125], v[120:121], v[218:221]\n"
      "v_exp_f32 v190, v190\n"
      "v_cvt_pk_bf16_f32 v188, v188, v189\n"
      "v_mfma_f32_16x16x32_bf16 v[200:203], a[228:231], v[176:179], v[200:203]\n"
      "s_waitcnt lgkmcnt(1)\n"
      "v_exp_f32 v191, v191\n"
      "v_mfma_f32_16x16x32_bf16 v[200:203], a[232:235], v[192:195], v[200:203]\n"
      "s_waitcnt lgkmcnt(0)\n"
      "v_max3_f32 v163, v142, v143, v144\n"
      "v_cvt_pk_bf16_f32 v189, v190, v191\n"
      "v_mfma_f32_16x16x32_bf16 v[200:203], a[236:239], v[104:107], v[200:203]\n"
      "v_exp_f32 v158, v158\n"
      "v_mfma_f32_16x16x32_bf16 v[112:115], a[252:255], v[176:179], v[112:115]\n"
      "v_exp_f32 v159, v159\n"
      "v_mfma_f32_16x16x32_bf16 v[112:115], v[224:227], v[192:195], v[112:115]\n"
      "v_exp_f32_e64 v157, v157\n"
      "v_max3_f32 v163, v163, v182, v183\n"
      "v_mfma_f32_16x16x32_bf16 v[112:115], v[228:231], v[104:107], v[112:115]\n"
      "v_cvt_pk_bf16_f32 v191, v158, v159\n"
      "v_max3_f32 v120, v145, v180, v181\n"
      "v_mfma_f32_16x16x32_bf16 v[204:207], v[244:247], v[176:179], v[204:207]\n"
      "v_max3_f32 v163, v163, v166, v167\n"
      "v_cvt_pk_bf16_f32 v190, v156, v157\n"
      "v_mfma_f32_16x16x32_bf16 v[204:207], v[248:251], v[192:195], v[204:207]\n"
      "ds_read_b128 v[156:159], v7 offset:18432\n"
      "v_max3_f32 v120, v120, v164, v165\n"
      "v_mfma_f32_16x16x32_bf16 v[204:207], v[252:255], v[104:107], v[204:207]\n"
      "v_max3_f32 v163, v163, v120, v218\n"
      "v_mov_b32 v221, v163\n"
      "v_mfma_f32_16x16x32_bf16 v[104:107], v[32:35], v[188:191], 0\n"
      "s_nop 0\n"
      "v_permlane16_swap_b32_e64 v163, v221\n"
      "v_mfma_f32_16x16x32_bf16 v[104:107], v[32:35], v[92:95], v[104:107]\n"
      "ds_read_b128 v[176:179], v10\n"
      "v_max_f32 v163, v163, v221\n"
      "v_mfma_f32_16x16x32_bf16 v[120:123], v[130:133], v[188:191], 0\n"
      "v_mov_b32 v221, v163\n"
      "v_mul_f32 v195, v201, v201\n"
      "v_mfma_f32_16x16x32_bf16 v[120:123], v[76:79], v[92:95], v[120:123]\n"
      "ds_read_b64 v[102:103], v10 offset:16\n"
      "v_mul_f32 v111, v97, v97\n"
      "v_mfma_f32_16x16x32_bf16 v[106:109], v[196:199], v[188:191], 0\n"
      "v_permlane32_swap_b32 v163, v221\n"
      "v_mfma_f32_16x16x32_bf16 v[106:109], v[80:83], v[92:95], v[106:109]\n"
      "ds_write_b128 v3, v[168:171]\n"
      "v_pk_mul_f32 v[184:185], v[184:185], v[174:175] op_sel_hi:[1,0]\n"
      "v_max_f32 v163, v163, v221\n"
      "v_pk_mul_f32 v[186:187], v[186:187], v[174:175] op_sel_hi:[1,0]\n"
      "v_mfma_f32_16x16x32_bf16 v[168:171], v[214:217], v[188:191], 0\n"
      "s_waitcnt lgkmcnt(2)\n"
      "v_rcp_f32 v104, v104\n"
      "v_mfma_f32_16x16x32_bf16 v[168:171], v[84:87], v[92:95], v[168:171]\n"
      "ds_read_b128 v[172:175], v7 offset:22528\n"
      "v_cvt_pk_bf16_f32 v184, v184, v185\n"
      "v_mfma_f32_16x16x32_bf16 v[220:223], v[176:179], v[210:213], v[30:33]\n"
      "buffer_store_dwordx4 v[156:159], v20, s[24:27], s46 offen sc1\n"
      "v_cvt_pk_bf16_f32 v185, v186, v187\n"
      "v_pk_add_f32 v[180:181], v[180:181], v[162:163] op_sel:[0,1] neg_lo:[0,1] neg_hi:[0,1]\n"
      "v_pk_mul_f32 v[108:109], v[108:109], v[104:105] op_sel_hi:[1,0]\n"
      "v_fma_f32 v110, v96, v96, s52\n"
      "v_pk_mul_f32 v[168:169], v[168:169], v[104:105] op_sel_hi:[1,0]\n"
      "v_pk_add_f32 v[166:167], v[166:167], v[162:163] op_sel:[0,1] neg_lo:[0,1] neg_hi:[0,1]\n"
      "v_pk_mul_f32 v[120:121], v[120:121], v[104:105] op_sel_hi:[1,0]\n"
      "v_pk_add_f32 v[142:143], v[142:143], v[162:163] op_sel:[0,1] neg_lo:[0,1] neg_hi:[0,1]\n"
      "v_pk_add_f32 v[182:183], v[182:183], v[162:163] op_sel:[0,1] neg_lo:[0,1] neg_hi:[0,1]\n"
      "v_pk_add_f32 v[164:165], v[164:165], v[162:163] op_sel:[0,1] neg_lo:[0,1] neg_hi:[0,1]\n"
      "v_pk_fma_f32 v[110:111], v[98:99], v[98:99], v[110:111]\n"
      "v_pk_fma_f32 v[110:111], v[116:117], v[116:117], v[110:111]\n"
      "v_mfma_f32_16x16x32_bf16 v[156:159], v[176:179], v[146:149], v[30:33]\n"
      "s_waitcnt lgkmcnt(0)\n"
      "v_exp_f32 v142, v142\n"
      "v_mfma_f32_16x16x32_bf16 v[146:149], v[176:179], v[150:153], v[30:33]\n"
      "ds_read_b128 v[190:193], v2 offset:22016\n"
      "v_cvt_pk_bf16_f32 v120, v120, v121\n"
      "v_mfma_f32_16x16x32_bf16 v[186:189], v[176:179], v[126:129], v[30:33]\n"
      "ds_read_b128 v[176:179], v2 offset:25088\n"
      "v_fma_f32 v194, v200, v200, s52\n"
      "v_mfma_f32_16x16x16_bf16 v[220:223], v[102:103], v[138:139], v[220:223]\n"
      "v_exp_f32 v166, v166\n"
      "v_sub_f32 v218, v218, v163\n"
      "v_mfma_f32_16x16x16_bf16 v[156:159], v[102:103], v[160:161], v[156:159]\n"
      "ds_read_b128 v[208:211], v2 offset:18944\n"
      "buffer_store_dwordx4 v[172:175], v21, s[24:27], s46 offen sc1\n"
      "v_pk_mul_f32 v[122:123], v[122:123], v[104:105] op_sel_hi:[1,0]\n"
      "v_pk_add_f32 v[144:145], v[144:145], v[162:163] op_sel:[0,1] neg_lo:[0,1] neg_hi:[0,1]\n"
      "v_pk_fma_f32 v[110:111], v[118:119], v[118:119], v[110:111]\n"
      "v_pk_mul_f32 v[106:107], v[106:107], v[104:105] op_sel_hi:[1,0]\n"
      "v_pk_fma_f32 v[110:111], v[134:135], v[134:135], v[110:111]\n"
      "v_pk_mul_f32 v[170:171], v[170:171], v[104:105] op_sel_hi:[1,0]\n"
      "v_pk_fma_f32 v[110:111], v[136:137], v[136:137], v[110:111]\n"
      "v_pk_fma_f32 v[194:195], v[202:203], v[202:203], v[194:195]\n"
      "v_mfma_f32_16x16x16_bf16 v[146:149], v[102:103], v[154:155], v[146:149]\n"
      "v_cndmask_b32 v220, v220, v156, s[58:59]\n"
      "v_exp_f32_e64 v143, v143\n"
      "v_exp_f32 v164, v164\n"
      "v_mfma_f32_16x16x16_bf16 v[186:189], v[102:103], v[124:125], v[186:189]\n"
      "v_exp_f32_e64 v167, v167\n"
      "s_waitcnt lgkmcnt(0)\n"
      "v_mfma_f32_16x16x32_bf16 v[156:159], a[216:219], v[208:211], 0\n"
      "ds_read_b128 v[172:175], v7 offset:26624\n"
      "v_cndmask_b32 v220, v220, v146, s[60:61]\n"
      "v_mfma_f32_16x16x32_bf16 v[156:159], a[220:223], v[190:193], v[156:159]\n"
      "v_exp_f32_e64 v144, v144\n"
      "v_mfma_f32_16x16x32_bf16 v[156:159], a[224:227], v[176:179], v[156:159]\n"
      "ds_read_b128 v[126:129], v2 offset:31232\n"
      "v_cvt_pk_bf16_f32 v121, v122, v123\n"
      "v_mfma_f32_16x16x32_bf16 v[146:149], a[240:243], v[208:211], 0\n"
      "ds_read_b128 v[102:105], v2 offset:28160\n"
      "v_cvt_pk_bf16_f32 v123, v108, v109\n"
      "v_mfma_f32_16x16x32_bf16 v[146:149], a[244:247], v[190:193], v[146:149]\n"
      "ds_write_b64 v4, v[184:185]\n"
      "v_exp_f32_e64 v182, v182\n"
      "v_cndmask_b32 v220, v220, v186, s[62:63]\n"
      "v_pk_fma_f32 v[194:195], v[112:113], v[112:113], v[194:195]\n"
      "v_mfma_f32_16x16x32_bf16 v[146:149], a[248:251], v[176:179], v[146:149]\n"
      "ds_read_b128 v[138:141], v2 offset:34304\n"
      "v_exp_f32_e64 v218, v218\n"
      "v_cvt_pk_bf16_f32 v142, v142, v143\n"
      "v_max_f32_dpp v185, v220, v220 row_ror:8 row_mask:0xf bank_mask:0xf\n"
      "v_pk_fma_f32 v[194:195], v[114:115], v[114:115], v[194:195]\n"
      "v_mfma_f32_16x16x32_bf16 v[160:163], v[232:235], v[208:211], 0\n"
      "v_add_f32_e64 v110, v110, v111\n"
      "v_max_f32_dpp v185, v185, v185 row_ror:4 row_mask:0xf bank_mask:0xf\n"
      "v_mfma_f32_16x16x32_bf16 v[160:163], v[236:239], v[190:193], v[160:163]\n"
      "s_waitcnt lgkmcnt(2)\n"
      "v_cvt_pk_bf16_f32 v168, v168, v169\n"
      "v_exp_f32 v180, v180\n"
      "v_max_f32_dpp v185, v185, v185 row_ror:2 row_mask:0xf bank_mask:0xf\n"
      "v_mfma_f32_16x16x32_bf16 v[160:163], v[240:243], v[176:179], v[160:163]\n"
      "buffer_store_dwordx4 v[172:175], v22, s[24:27], s46 offen sc1\n"
      "v_mfma_f32_16x16x32_bf16 v[156:159], a[228:231], v[102:105], v[156:159]\n"
      "v_exp_f32_e64 v145, v145\n"
      "v_mfma_f32_16x16x32_bf16 v[156:159], a[232:235], v[126:129], v[156:159]\n"
      "s_waitcnt lgkmcnt(0)\n"
      "v_exp_f32 v165, v165\n"
      "v_max_f32_dpp v185, v185, v185 row_ror:1 row_mask:0xf bank_mask:0xf\n"
      "v_mfma_f32_16x16x32_bf16 v[156:159], a[236:239], v[138:141], v[156:159]\n"
      "v_cvt_pk_bf16_f32 v90, v218, 0\n"
      "v_max_f32_dpp v185, v185, v185 row_bcast:15 row_mask:0xa bank_mask:0xf\n"
      "v_exp_f32_e64 v181, v181\n"
      "v_cvt_pk_bf16_f32 v89, v166, v167\n"
      "v_mfma_f32_16x16x32_bf16 v[146:149], a[252:255], v[102:105], v[146:149]\n"
      "v_max_f32_dpp v185, v185, v185 row_bcast:31 row_mask:0xc bank_mask:0xf\n"
      "v_cvt_pk_bf16_f32 v169, v170, v171\n"
      "v_mfma_f32_16x16x32_bf16 v[146:149], v[224:227], v[126:129], v[146:149]\n"
      "v_readlane_b32 s72, v185, 63\n"
      "v_mul_f32_e64 v185, v157, v157\n"
      "v_mfma_f32_16x16x32_bf16 v[146:149], v[228:231], v[138:141], v[146:149]\n"
      "ds_read_b128 v[208:211], v7 offset:30720\n"
      "v_exp_f32 v183, v183\n"
      "v_mfma_f32_16x16x32_bf16 v[160:163], v[244:247], v[102:105], v[160:163]\n"
      "v_subrev_f32 v220, s72, v220\n"
      "v_cvt_pk_bf16_f32 v143, v144, v145\n"
      "v_mfma_f32_16x16x32_bf16 v[160:163], v[248:251], v[126:129], v[160:163]\n"
      "ds_read_b128 v[102:105], v7 offset:34816\n"
      "v_cvt_pk_bf16_f32 v144, v180, v181\n"
      "v_mfma_f32_16x16x32_bf16 v[160:163], v[252:255], v[138:141], v[160:163]\n"
      "v_cvt_pk_bf16_f32 v88, v164, v165\n"
      "v_exp_f32_e64 v220, v220\n"
      "v_cvt_pk_bf16_f32 v145, v182, v183\n"
      "v_cvt_pk_bf16_f32 v220, v220, v220\n"
      "v_pk_fma_f32 v[194:195], v[204:205], v[204:205], v[194:195]\n"
      "v_mfma_f32_16x16x32_bf16 v[170:173], v[32:35], v[142:145], 0\n"
      "ds_write_b16 v13, v220\n"
      "v_mfma_f32_16x16x32_bf16 v[170:173], v[32:35], v[88:91], v[170:173]\n"
      "ds_read_b128 v[218:221], v14\n"
      "v_cvt_pk_bf16_f32 v122, v106, v107\n"
      "v_mfma_f32_16x16x32_bf16 v[106:109], v[130:133], v[142:145], 0\n"
      "ds_write_b64 v4, v[168:169] offset:256\n"
      "v_mfma_f32_16x16x32_bf16 v[106:109], v[76:79], v[88:91], v[106:109]\n"
      "ds_read_b128 v[174:177], v14 offset:64\n"
      "v_fma_f32 v184, v156, v156, s52\n"
      "v_mfma_f32_16x16x32_bf16 v[126:129], v[196:199], v[142:145], 0\n"
      "s_waitcnt lgkmcnt(4)\n"
      "v_rcp_f32 v170, v170\n"
      "v_mfma_f32_16x16x32_bf16 v[126:129], v[80:83], v[88:91], v[126:129]\n"
      "ds_write_b128 v3, v[120:123] offset:256\n"
      "v_pk_fma_f32 v[184:185], v[158:159], v[158:159], v[184:185]\n"
      "v_mfma_f32_16x16x32_bf16 v[166:169], v[214:217], v[142:145], 0\n"
      "buffer_store_dwordx4 v[208:211], v23, s[24:27], s46 offen sc1\n"
      "v_pk_fma_f32 v[184:185], v[146:147], v[146:147], v[184:185]\n"
      "v_pk_mul_f32 v[106:107], v[106:107], v[170:171] op_sel_hi:[1,0]\n"
      "v_pk_fma_f32 v[184:185], v[148:149], v[148:149], v[184:185]\n"
      "v_pk_mul_f32 v[108:109], v[108:109], v[170:171] op_sel_hi:[1,0]\n"
      "v_pk_fma_f32 v[184:185], v[160:161], v[160:161], v[184:185]\n"
      "v_pk_mul_f32 v[126:127], v[126:127], v[170:171] op_sel_hi:[1,0]\n"
      "v_pk_fma_f32 v[194:195], v[206:207], v[206:207], v[194:195]\n"
      "v_pk_mul_f32 v[128:129], v[128:129], v[170:171] op_sel_hi:[1,0]\n"
      "v_cvt_pk_bf16_f32 v106, v106, v107\n"
      "v_add_f32_e64 v194, v194, v195\n"
      "v_pk_fma_f32 v[184:185], v[162:163], v[162:163], v[184:185]\n"
      "v_mfma_f32_16x16x32_bf16 v[166:169], v[84:87], v[88:91], v[166:169]\n"
      "s_waitcnt lgkmcnt(1)\n"
      "v_permlane16_swap_b32 v110, v194\n"
      "v_add_f32_e64 v184, v184, v185\n"
      "v_cvt_pk_bf16_f32 v107, v108, v109\n"
      "v_cvt_pk_bf16_f32 v108, v126, v127\n"
      "v_add_f32 v110, v110, v194\n"
      "v_cvt_pk_bf16_f32 v109, v128, v129\n"
      "v_mov_b32_e64 v194, v184\n"
      "s_add_u32 s43, s43, 1\n"
      "ds_write_b128 v3, v[106:109] offset:512\n"
      "v_permlane16_swap_b32 v184, v194\n"
      "v_add_f32 v184, v184, v194\n"
      "v_pk_mul_f32 v[168:169], v[168:169], v[170:171] op_sel_hi:[1,0]\n"
      "v_pk_mul_f32 v[166:167], v[166:167], v[170:171] op_sel_hi:[1,0]\n"
      "v_permlane32_swap_b32 v110, v184\n"
      "v_add_f32 v110, v110, v184\n"
      "v_mfma_f32_16x16x32_bf16 v[208:211], v[218:221], v[32:35], 0\n"
      "ds_write_b32 v31, v110\n"
      "v_cvt_pk_bf16_f32 v166, v166, v167\n"
      "v_cvt_pk_bf16_f32 v167, v168, v169\n"
      "v_mfma_f32_16x16x32_bf16 v[208:211], v[174:177], v[32:35], v[208:211]\n"
      "ds_write_b64 v4, v[166:167] offset:512\n"
      "buffer_store_dwordx4 v[102:105], v25, s[24:27], s46 offen sc1\n"
      "s_cmp_ge_u32 s95, 0x40000000\n"
      "s_cselect_b64 vcc, -1, s[56:57]\n"
      "s_waitcnt lgkmcnt(0)\n"
      "s_barrier\n"
      "s_cbranch_vccz L_max1_%=\n"
      "L_cold_%=:\n"
      "L_disp1_%=:\n"
      "s_cmp_lt_u32 s95, 0x40000000\n"
      "s_cbranch_scc1 L_nofix_11_%=\n"
      "s_cmp_eq_u32 s95, -2\n"
      "s_cbranch_scc1 L_tail1_%=\n"
      "s_nop 2\n"
      "v_lshlrev_b32 v169, 3, v36\n"
      "s_add_u32 s72, s98, L_cold_%=-L_ipf0_%=\n"
      "s_addc_u32 s73, s99, 0\n"
      "s_mov_b32 s74, L_cend_%=-L_cold_%=\n"
      "s_mov_b32 s75, 0x20000\n"
      "s_mov_b32 m0, 122112\n"
      "s_nop 0\n"
      "buffer_load_dwordx4 v169, s[72:75], 0 offen  lds\n"
      "s_lshr_b32 s92, s84, 30\n"
      "s_or_b32 s92, s92, s66\n"
      "s_cbranch_scc0 L_nf_12_%=\n"
      "s_bitcmp1_b32 s84, 31\n"
      "s_cselect_b32 s75, 100270464, 0\n"
      "s_bitcmp1_b32 s84, 30\n"
      "s_cselect_b32 s76, 196224, 0\n"
      "s_and_b32 s74, s84, 0x3fffffff\n"
      "s_cmp_ge_u32 s84, 0x40000000\n"
      "s_cselect_b32 s74, s74, 0\n"
      "s_cselect_b32 s84, 0, s84\n"
      "s_sub_u32 s74, s74, s66\n"
      "s_add_u32 s66, s66, s74\n"
      "s_sub_u32 s75, s75, s67\n"
      "s_add_u32 s67, s67, s75\n"
      "s_sub_u32 s76, s76, s68\n"
      "s_add_u32 s68, s68, s76\n"
      "v_add_u32 v16, s74, v16\n"
      "v_add_u32 v17, s74, v17\n"
      "v_add_u32 v18, s74, v18\n"
      "v_add_u32 v19, s74, v19\n"
      "v_add_u32 v15, s74, v15\n"
      "s_cmp_eq_u32 s75, 0\n"
      "s_cbranch_scc1 L_ax_13_%=\n"
      "v_bfe_i32 v169, v27, 0, 1\n"
      "v_and_b32 v169, s75, v169\n"
      "v_sub_u32 v16, v16, v169\n"
      "v_bfe_i32 v169, v27, 2, 1\n"
      "v_and_b32 v169, s75, v169\n"
      "v_sub_u32 v17, v17, v169\n"
      "v_bfe_i32 v169, v27, 4, 1\n"
      "v_and_b32 v169, s75, v169\n"
      "v_sub_u32 v18, v18, v169\n"
      "v_bfe_i32 v169, v27, 6, 1\n"
      "v_and_b32 v169, s75, v169\n"
      "v_sub_u32 v19, v19, v169\n"
      "v_bfe_i32 v169, v27, 8, 1\n"
      "v_and_b32 v169, s75, v169\n"
      "v_sub_u32 v15, v15, v169\n"
      "L_ax_13_%=:\n"
      "s_cmp_eq_u32 s76, 0\n"
      "s_cbranch_scc1 L_ax_14_%=\n"
      "v_bfe_i32 v169, v27, 1, 1\n"
      "v_and_b32 v169, s76, v169\n"
      "v_sub_u32 v16, v16, v169\n"
      "v_bfe_i32 v169, v27, 3, 1\n"
      "v_and_b32 v169, s76, v169\n"
      "v_sub_u32 v17, v17, v169\n"
      "v_bfe_i32 v169, v27, 5, 1\n"
      "v_and_b32 v169, s76, v169\n"
      "v_sub_u32 v18, v18, v169\n"
      "v_bfe_i32 v169, v27, 7, 1\n"
      "v_and_b32 v169, s76, v169\n"
      "v_sub_u32 v19, v19, v169\n"
      "v_bfe_i32 v169, v27, 9, 1\n"
      "v_and_b32 v169, s76, v169\n"
      "v_sub_u32 v15, v15, v169\n"
      "L_ax_14_%=:\n"
      "s_or_b32 s48, s66, s69\n"
      "s_cmp_lg_u32 s48, 0\n"
      "s_cselect_b32 s48, 0x40000000, 0\n"
      "L_nf_12_%=:\n"
      "s_lshr_b32 s92, s85, 30\n"
      "s_or_b32 s92, s92, s69\n"
      "s_cbranch_scc0 L_nf_15_%=\n"
      "s_bitcmp1_b32 s85, 31\n"
      "s_cselect_b32 s75, 100270464, 0\n"
      "s_bitcmp1_b32 s85, 30\n"
      "s_cselect_b32 s76, 196224, 0\n"
      "s_and_b32 s74, s85, 0x3fffffff\n"
      "s_cmp_ge_u32 s85, 0x40000000\n"
      "s_cselect_b32 s74, s74, 0\n"
      "s_cselect_b32 s85, 0, s85\n"
      "s_sub_u32 s74, s74, s69\n"
      "s_add_u32 s69, s69, s74\n"
      "s_sub_u32 s75, s75, s70\n"
      "s_add_u32 s70, s70, s75\n"
      "s_sub_u32 s76, s76, s71\n"
      "s_add_u32 s71, s71, s76\n"
      "v_add_u32 v20, s74, v20\n"
      "v_add_u32 v21, s74, v21\n"
      "v_add_u32 v22, s74, v22\n"
      "v_add_u32 v23, s74, v23\n"
      "v_add_u32 v25, s74, v25\n"
      "s_cmp_eq_u32 s75, 0\n"
      "s_cbranch_scc1 L_ax_16_%=\n"
      "v_bfe_i32 v169, v27, 10, 1\n"
      "v_and_b32 v169, s75, v169\n"
      "v_sub_u32 v20, v20, v169\n"
      "v_bfe_i32 v169, v27, 12, 1\n"
      "v_and_b32 v169, s75, v169\n"
      "v_sub_u32 v21, v21, v169\n"
      "v_bfe_i32 v169, v27, 14, 1\n"
      "v_and_b32 v169, s75, v169\n"
      "v_sub_u32 v22, v22, v169\n"
      "v_bfe_i32 v169, v27, 16, 1\n"
      "v_and_b32 v169, s75, v169\n"
      "v_sub_u32 v23, v23, v169\n"
      "v_bfe_i32 v169, v27, 18, 1\n"
      "v_and_b32 v169, s75, v169\n"
      "v_sub_u32 v25, v25, v169\n"
      "L_ax_16_%=:\n"
      "s_cmp_eq_u32 s76, 0\n"
      "s_cbranch_scc1 L_ax_17_%=\n"
      "v_bfe_i32 v169, v27, 11, 1\n"
      "v_and_b32 v169, s76, v169\n"
      "v_sub_u32 v20, v20, v169\n"
      "v_bfe_i32 v169, v27, 13, 1\n"
      "v_and_b32 v169, s76, v169\n"
      "v_sub_u32 v21, v21, v169\n"
      "v_bfe_i32 v169, v27, 15, 1\n"
      "v_and_b32 v169, s76, v169\n"
      "v_sub_u32 v22, v22, v169\n"
      "v_bfe_i32 v169, v27, 17, 1\n"
      "v_and_b32 v169, s76, v169\n"
      "v_sub_u32 v23, v23, v169\n"
      "v_bfe_i32 v169, v27, 19, 1\n"
      "v_and_b32 v169, s76, v169\n"
      "v_sub_u32_e64 v25, v25, v169\n"
      "L_ax_17_%=:\n"
      "s_or_b32 s48, s66, s69\n"
      "s_cmp_lg_u32 s48, 0\n"
      "s_cselect_b32 s48, 0x40000000, 0\n"
      "L_nf_15_%=:\n"
      "s_sub_u32 s92, s42, 1\n"
      "s_cmp_gt_u32 s92, s44\n"
      "s_cbranch_scc1 L_tail1_%=\n"
      "L_nofix_11_%=:\n"
      "s_cmp_lg_u64 s[56:57], 0\n"
      "s_cbranch_scc1 L_slow1_%=\n"
      "L_sel1_%=:\n"
      "s_cmp_lg_u64 s[54:55], 0\n"
      "s_cbranch_scc1 L_max1_%=\n"
      "s_branch L_loop1_%=\n"
      "L_slow1_%=:\n"
      "s_nop 4\n"
      "v_mfma_f32_16x16x32_bf16 v[168:171], v[218:221], v[130:133], 0\n"
      "ds_read_b128 v[182:185], v0 offset:18432\n"
      "ds_read_b128 v[190:193], v9\n"
      "v_mfma_f32_16x16x32_bf16 v[168:171], v[174:177], v[76:79], v[168:171]\n"
      "ds_read_b128 v[142:145], v0 offset:24576\n"
      "v_subrev_u32 v131, 115456, v9\n"
      "v_subrev_u32 v223, 115456, v9\n"
      "v_mfma_f32_16x16x32_bf16 v[150:153], v[218:221], v[196:199], 0\n"
      "ds_read_b128 v[108:111], v1 offset:18432\n"
      "s_add_u32 s72, s42, 1\n"
      "v_mfma_f32_16x16x32_bf16 v[150:153], v[174:177], v[80:83], v[150:153]\n"
      "ds_read_b128 v[164:167], v9 offset:4608\n"
      "v_bfe_u32 v77, v223, 4, 4\n"
      "v_mfma_f32_16x16x32_bf16 v[194:197], v[218:221], v[214:217], 0\n"
      "ds_read_b128 v[218:221], v1 offset:24576\n"
      "v_readlane_b32 s77, v38, s72\n"
      "v_mfma_f32_16x16x32_bf16 v[194:197], v[174:177], v[84:87], v[194:197]\n"
      "s_and_b32 s73, s77, -16\n"
      "s_mov_b32 m0, 110592\n"
      "s_waitcnt lgkmcnt(5)\n"
      "v_mfma_f32_16x16x32_bf16 v[174:177], a[0:3], v[182:185], 0\n"
      "buffer_load_dwordx4 v131, s[28:31], s73 offen  lds\n"
      "v_mfma_f32_16x16x32_bf16 v[170:173], a[24:27], v[182:185], 0\n"
      "ds_read_b128 v[186:189], v1 offset:30720\n"
      "v_subrev_u32 v8, 115456, v9\n"
      "v_mfma_f32_16x16x32_bf16 v[120:123], a[48:51], v[182:185], 0\n"
      "s_waitcnt lgkmcnt(4)\n"
      "v_add_f32 v192, v192, v193\n"
      "s_mov_b32 m0, 111616\n"
      "v_mfma_f32_16x16x32_bf16 v[178:181], a[72:75], v[182:185], 0\n"
      "s_add_u32 s73, s73, 1024\n"
      "v_add_f32 v190, v190, v191\n"
      "v_add_f32 v190, v190, v192\n"
      "v_mfma_f32_16x16x32_bf16 v[138:141], a[96:99], v[182:185], 0\n"
      "s_waitcnt lgkmcnt(2)\n"
      "v_rsq_f32 v190, v190\n"
      "v_mfma_f32_16x16x32_bf16 v[124:127], a[120:123], v[182:185], 0\n"
      "ds_read_b128 v[100:103], v1 offset:26624\n"
      "v_mul_f32 v190, 0x415db3d7, v190\n"
      "v_mfma_f32_16x16x32_bf16 v[174:177], a[4:7], v[108:111], v[174:177]\n"
      "ds_read_b128 v[152:155], v0 offset:30720\n"
      "v_mov_b32_e64 v192, v190\n"
      "v_mfma_f32_16x16x32_bf16 v[174:177], a[8:11], v[142:145], v[174:177]\n"
      "buffer_load_dwordx4 v131, s[28:31], s73 offen  lds\n"
      "v_mfma_f32_16x16x32_bf16 v[170:173], a[28:31], v[108:111], v[170:173]\n"
      "v_permlane32_swap_b32 v190, v192\n"
      "v_mfma_f32_16x16x32_bf16 v[170:173], a[32:35], v[142:145], v[170:173]\n"
      "s_add_u32 s73, s73, 1024\n"
      "v_lshrrev_b32 v223, 8, v223\n"
      "v_mov_b32 v191, v190\n"
      "v_mfma_f32_16x16x32_bf16 v[120:123], a[52:55], v[108:111], v[120:123]\n"
      "s_mov_b32 m0, 112640\n"
      "v_permlane16_swap_b32 v190, v191\n"
      "v_mfma_f32_16x16x32_bf16 v[120:123], a[56:59], v[142:145], v[120:123]\n"
      "v_mul_f32 v203, v191, v203\n"
      "v_mul_f32 v201, v191, v201\n"
      "v_mfma_f32_16x16x32_bf16 v[178:181], a[76:79], v[108:111], v[178:181]\n"
      "buffer_load_dwordx4 v131, s[28:31], s73 offen  lds\n"
      "v_mfma_f32_16x16x32_bf16 v[178:181], a[80:83], v[142:145], v[178:181]\n"
      "v_mul_f32 v200, v191, v200\n"
      "v_cvt_pk_bf16_f32 v200, v200, v201\n"
      "v_mfma_f32_16x16x32_bf16 v[138:141], a[100:103], v[108:111], v[138:141]\n"
      "s_waitcnt lgkmcnt(0)\n"
      "v_mul_u32_u24 v78, 98, v77\n"
      "v_mul_f32 v118, v190, v118\n"
      "v_mfma_f32_16x16x32_bf16 v[138:141], a[104:107], v[142:145], v[138:141]\n"
      "s_mov_b32 m0, 113664\n"
      "v_mul_f32 v98, v190, v98\n"
      "v_mul_f32_e64 v135, v190, v135\n"
      "v_mfma_f32_16x16x32_bf16 v[124:127], a[124:127], v[108:111], v[124:127]\n"
      "s_add_u32 s73, s73, 1024\n"
      "v_mul_f32_e64 v134, v190, v134\n"
      "v_mov_b32_dpp v208, v208 quad_perm:[0,0,0,0] row_mask:0xf bank_mask:0xf\n"
      "v_mfma_f32_16x16x32_bf16 v[124:127], a[128:131], v[142:145], v[124:127]\n"
      "ds_read_b128 v[210:213], v1 offset:32768\n"
      "v_mul_f32_e64 v157, v192, v157\n"
      "v_mfma_f32_16x16x32_bf16 v[174:177], a[12:15], v[218:221], v[174:177]\n"
      "v_cvt_pk_bf16_f32 v134, v134, v135\n"
      "v_mul_f32 v119, v190, v119\n"
      "v_mfma_f32_16x16x32_bf16 v[174:177], a[16:19], v[152:155], v[174:177]\n"
      "v_mul_f32 v202, v191, v202\n"
      "v_mul_f32_e64 v99, v190, v99\n"
      "v_mfma_f32_16x16x32_bf16 v[174:177], a[20:23], v[186:189], v[174:177]\n"
      "buffer_load_dwordx4 v131, s[28:31], s73 offen  lds\n"
      "v_mfma_f32_16x16x32_bf16 v[170:173], a[36:39], v[218:221], v[170:173]\n"
      "s_mov_b32 m0, 114688\n"
      "v_mul_f32 v156, v192, v156\n"
      "v_mul_f32 v163, v192, v163\n"
      "v_mfma_f32_16x16x32_bf16 v[170:173], a[40:43], v[152:155], v[170:173]\n"
      "s_add_u32 s73, s73, 1024\n"
      "v_readlane_b32 s45, v37, s43\n"
      "v_cndmask_b32 v168, v168, v150, s[58:59]\n"
      "v_mfma_f32_16x16x32_bf16 v[170:173], a[44:47], v[186:189], v[170:173]\n"
      "s_mov_b32 exec_hi, 0xffff\n"
      "buffer_load_dwordx4 v131, s[28:31], s73 offen  lds\n"
      "s_mov_b32 exec_hi, -1\n"
      "v_pk_mul_f32 v[112:113], v[112:113], v[190:191] op_sel:[0,1]\n"
      "v_pk_mul_f32 v[206:207], v[206:207], v[190:191] op_sel:[0,1]\n"
      "v_pk_mul_f32 v[116:117], v[116:117], v[190:191] op_sel_hi:[1,0]\n"
      "v_pk_mul_f32 v[204:205], v[204:205], v[190:191] op_sel:[0,1]\n"
      "v_pk_mul_f32 v[136:137], v[136:137], v[190:191] op_sel_hi:[1,0]\n"
      "v_pk_mul_f32 v[96:97], v[96:97], v[190:191] op_sel_hi:[1,0]\n"
      "v_mfma_f32_16x16x32_bf16 v[120:123], a[60:63], v[218:221], v[120:123]\n"
      "s_add_u32 m0, s64, 0\n"
      "v_cvt_pk_bf16_f32 v96, v96, v97\n"
      "v_mul_f32_e64 v115, v191, v115\n"
      "v_mfma_f32_16x16x32_bf16 v[120:123], a[64:67], v[152:155], v[120:123]\n"
      "v_cvt_pk_bf16_f32 v135, v136, v137\n"
      "v_cvt_pk_bf16_f32 v97, v98, v99\n"
      "v_mfma_f32_16x16x32_bf16 v[120:123], a[68:71], v[186:189], v[120:123]\n"
      "buffer_load_dwordx4 v16, s[16:19], s84 offen  lds\n"
      "v_mfma_f32_16x16x32_bf16 v[178:181], a[84:87], v[218:221], v[178:181]\n"
      "s_add_u32 m0, s64, 4096\n"
      "v_cvt_pk_bf16_f32 v201, v202, v203\n"
      "v_mul_f32 v149, v192, v149\n"
      "v_mfma_f32_16x16x32_bf16 v[178:181], a[88:91], v[152:155], v[178:181]\n"
      "ds_write_b64 v6, v[134:135] offset:18432\n"
      "v_mfma_f32_16x16x32_bf16 v[178:181], a[92:95], v[186:189], v[178:181]\n"
      "v_mul_f32 v148, v192, v148\n"
      "v_cvt_pk_bf16_f32 v99, v118, v119\n"
      "v_mfma_f32_16x16x32_bf16 v[138:141], a[108:111], v[218:221], v[138:141]\n"
      "ds_read_b128 v[134:137], v1 offset:20480\n"
      "v_cvt_pk_bf16_f32 v98, v116, v117\n"
      "v_mfma_f32_16x16x32_bf16 v[138:141], a[112:115], v[152:155], v[138:141]\n"
      "v_mul_f32_e64 v114, v191, v114\n"
      "v_cvt_pk_bf16_f32 v202, v112, v113\n"
      "v_mfma_f32_16x16x32_bf16 v[138:141], a[116:119], v[186:189], v[138:141]\n"
      "buffer_load_dwordx4 v17, s[16:19], s84 offen  lds\n"
      "v_mfma_f32_16x16x32_bf16 v[124:127], a[132:135], v[218:221], v[124:127]\n"
      "ds_write_b128 v5, v[96:99] offset:18432\n"
      "v_pk_mul_f32 v[160:161], v[160:161], v[192:193] op_sel_hi:[1,0]\n"
      "v_mfma_f32_16x16x32_bf16 v[124:127], a[136:139], v[152:155], v[124:127]\n"
      "v_fma_f32 v195, v178, v178, s51\n"
      "v_mul_f32_e64 v162, v192, v162\n"
      "v_mfma_f32_16x16x32_bf16 v[124:127], a[140:143], v[186:189], v[124:127]\n"
      "s_add_u32 m0, s64, 8192\n"
      "v_fma_f32 v80, v174, v174, s51\n"
      "v_cvt_pk_bf16_f32 v203, v114, v115\n"
      "v_mfma_f32_16x16x32_bf16 v[130:133], v[182:185], a[144:147], 0\n"
      "ds_write_b128 v5, v[200:203] offset:24576\n"
      "v_mfma_f32_16x16x32_bf16 v[130:133], v[108:111], a[148:151], v[130:133]\n"
      "buffer_load_dwordx4 v18, s[16:19], s84 offen  lds\n"
      "v_mfma_f32_16x16x32_bf16 v[130:133], v[142:145], a[152:155], v[130:133]\n"
      "v_mul_f32_e64 v159, v192, v159\n"
      "v_or3_b32 v164, v164, v165, v166\n"
      "v_mfma_f32_16x16x32_bf16 v[130:133], v[218:221], a[156:159], v[130:133]\n"
      "v_mul_f32_e64 v158, v192, v158\n"
      "v_cvt_pk_bf16_f32 v156, v156, v157\n"
      "v_mfma_f32_16x16x32_bf16 v[130:133], v[152:155], a[160:163], v[130:133]\n"
      "ds_read_b128 v[96:99], v0 offset:32768\n"
      "v_mul_f32 v147, v192, v147\n"
      "v_mfma_f32_16x16x32_bf16 v[130:133], v[186:189], a[164:167], v[130:133]\n"
      "s_add_u32 m0, s64, 12288\n"
      "v_fmac_f32 v80, v175, v175\n"
      "v_cndmask_b32 v168, v168, v194, s[60:61]\n"
      "v_mfma_f32_16x16x32_bf16 v[196:199], v[182:185], a[168:171], 0\n"
      "buffer_load_dwordx4 v19, s[16:19], s84 offen  lds\n"
      "v_mfma_f32_16x16x32_bf16 v[196:199], v[108:111], a[172:175], v[196:199]\n"
      "s_add_u32 m0, s65, 16384\n"
      "v_bitop3_b32 v164, v164, v167, s33 bitop3:0xa8\n"
      "v_fmac_f32_e64 v195, v179, v179\n"
      "v_mfma_f32_16x16x32_bf16 v[196:199], v[142:145], a[176:179], v[196:199]\n"
      "v_cvt_pk_bf16_f32 v204, v204, v205\n"
      "v_fmac_f32_e64 v195, v180, v180\n"
      "v_mfma_f32_16x16x32_bf16 v[196:199], v[218:221], a[180:183], v[196:199]\n"
      "ds_read_b128 v[116:119], v0 offset:20480\n"
      "v_cvt_pk_bf16_f32 v130, v130, v131\n"
      "v_mfma_f32_16x16x32_bf16 v[196:199], v[152:155], a[184:187], v[196:199]\n"
      "ds_read_b128 v[200:203], v0 offset:28672\n"
      "v_fmac_f32_e64 v80, v176, v176\n"
      "v_mfma_f32_16x16x32_bf16 v[196:199], v[186:189], a[188:191], v[196:199]\n"
      "buffer_load_dwordx4 v15, s[16:19], s84 offen  lds\n"
      "v_mfma_f32_16x16x32_bf16 v[214:217], v[182:185], a[192:195], 0\n"
      "ds_read_b128 v[112:115], v0 offset:26624\n"
      "v_fmac_f32 v195, v181, v181\n"
      "v_mfma_f32_16x16x32_bf16 v[214:217], v[108:111], a[196:199], v[214:217]\n"
      "v_fmac_f32 v195, v138, v138\n"
      "v_cvt_pk_bf16_f32 v157, v158, v159\n"
      "v_mfma_f32_16x16x32_bf16 v[214:217], v[142:145], a[200:203], v[214:217]\n"
      "v_fmac_f32 v80, v177, v177\n"
      "v_fmac_f32 v195, v139, v139\n"
      "v_mfma_f32_16x16x32_bf16 v[214:217], v[218:221], a[204:207], v[214:217]\n"
      "v_cvt_pk_bf16_f32 v159, v148, v149\n"
      "v_cvt_pk_bf16_f32 v205, v206, v207\n"
      "v_mfma_f32_16x16x32_bf16 v[214:217], v[152:155], a[208:211], v[214:217]\n"
      "buffer_load_dwordx4 v[218:221], v36, s[28:31], s86 offen\n"
      "v_mfma_f32_16x16x32_bf16 v[214:217], v[186:189], a[212:215], v[214:217]\n"
      "s_waitcnt lgkmcnt(1)\n"
      "v_rcp_f32 v208, v208\n"
      "v_mfma_f32_16x16x32_bf16 v[104:107], a[0:3], v[116:119], 0\n"
      "v_lshl_add_u32 v78, v223, 3, v78\n"
      "v_fmac_f32 v80, v170, v170\n"
      "v_mfma_f32_16x16x32_bf16 v[104:107], a[4:7], v[134:137], v[104:107]\n"
      "s_waitcnt lgkmcnt(0)\n"
      "v_fmac_f32 v195, v140, v140\n"
      "v_mov_b32 v222, 0x3fb9\n"
      "v_mfma_f32_16x16x32_bf16 v[104:107], a[8:11], v[112:115], v[104:107]\n"
      "v_fmac_f32 v195, v141, v141\n"
      "v_fmac_f32_e64 v80, v171, v171\n"
      "v_mfma_f32_16x16x32_bf16 v[104:107], a[12:15], v[100:103], v[104:107]\n"
      "v_fmac_f32 v80, v172, v172\n"
      "v_fmac_f32 v195, v124, v124\n"
      "v_mfma_f32_16x16x32_bf16 v[104:107], a[16:19], v[96:99], v[104:107]\n"
      "buffer_load_dword v82, v29, s[28:31], s86 offen\n"
      "v_mfma_f32_16x16x32_bf16 v[104:107], a[20:23], v[210:213], v[104:107]\n"
      "v_mul_f32 v168, v208, v168\n"
      "v_fmac_f32 v195, v125, v125\n"
      "v_mfma_f32_16x16x32_bf16 v[206:209], a[24:27], v[116:119], 0\n"
      "ds_write_b64 v6, v[204:205] offset:24576\n"
      "v_mfma_f32_16x16x32_bf16 v[206:209], a[28:31], v[134:137], v[206:209]\n"
      "v_fmac_f32 v80, v173, v173\n"
      "v_cmp_ne_u32 s[56:57], 0, v164\n"
      "v_mfma_f32_16x16x32_bf16 v[206:209], a[32:35], v[112:115], v[206:209]\n"
      "v_fmac_f32 v195, v126, v126\n"
      "v_mul_f32 v146, v192, v146\n"
      "v_mfma_f32_16x16x32_bf16 v[206:209], a[36:39], v[100:103], v[206:209]\n"
      "v_cvt_pk_bf16_f32 v158, v146, v147\n"
      "v_fmac_f32 v80, v120, v120\n"
      "v_mfma_f32_16x16x32_bf16 v[206:209], a[40:43], v[96:99], v[206:209]\n"
      "v_fmac_f32 v80, v121, v121\n"
      "v_lshlrev_b32 v223, 2, v223\n"
      "v_mfma_f32_16x16x32_bf16 v[206:209], a[44:47], v[210:213], v[206:209]\n"
      "v_fmac_f32_e64 v195, v127, v127\n"
      "v_fma_f32 v204, v104, v104, s51\n"
      "v_mfma_f32_16x16x32_bf16 v[146:149], a[48:51], v[116:119], 0\n"
      "v_fmac_f32 v80, v122, v122\n"
      "v_lshrrev_b32 v8, 3, v8\n"
      "v_mfma_f32_16x16x32_bf16 v[146:149], a[52:55], v[134:137], v[146:149]\n"
      "v_fmac_f32 v204, v105, v105\n"
      "v_fmac_f32 v204, v106, v106\n"
      "v_mfma_f32_16x16x32_bf16 v[146:149], a[56:59], v[112:115], v[146:149]\n"
      "s_add_u32 s73, s73, 1024\n"
      "v_fmac_f32 v80, v123, v123\n"
      "v_fmac_f32 v204, v107, v107\n"
      "v_mfma_f32_16x16x32_bf16 v[146:149], a[60:63], v[100:103], v[146:149]\n"
      "v_permlane16_swap_b32 v80, v195\n"
      "v_mfma_f32_16x16x32_bf16 v[146:149], a[64:67], v[96:99], v[146:149]\n"
      "v_fmac_f32 v204, v206, v206\n"
      "v_add_f32_e64 v80, v80, v195\n"
      "v_mfma_f32_16x16x32_bf16 v[146:149], a[68:71], v[210:213], v[146:149]\n"
      "ds_write_b128 v5, v[156:159] offset:30720\n"
      "v_mfma_f32_16x16x32_bf16 v[186:189], a[72:75], v[116:119], 0\n"
      "v_mov_b32_e64 v195, v80\n"
      "v_cvt_pk_bf16_f32 v168, v168, v168\n"
      "v_mfma_f32_16x16x32_bf16 v[186:189], a[76:79], v[134:137], v[186:189]\n"
      "v_permlane32_swap_b32 v80, v195\n"
      "v_mfma_f32_16x16x32_bf16 v[186:189], a[80:83], v[112:115], v[186:189]\n"
      "v_fmac_f32 v204, v207, v207\n"
      "v_add_f32_e64 v80, v80, v195\n"
      "v_mfma_f32_16x16x32_bf16 v[186:189], a[84:87], v[100:103], v[186:189]\n"
      "v_rsq_f32 v80, v80\n"
      "v_mfma_f32_16x16x32_bf16 v[186:189], a[88:91], v[96:99], v[186:189]\n"
      "v_fmac_f32 v204, v208, v208\n"
      "v_cvt_pk_bf16_f32 v196, v196, v197\n"
      "v_mfma_f32_16x16x32_bf16 v[186:189], a[92:95], v[210:213], v[186:189]\n"
      "v_sub_u32_e64 v77, v77, v223\n"
      "v_cvt_pk_bf16_f32 v160, v160, v161\n"
      "v_mfma_f32_16x16x32_bf16 v[156:159], a[96:99], v[116:119], 0\n"
      "v_mov_b32 v194, 0xbb2b\n"
      "v_cvt_pk_bf16_f32 v131, v132, v133\n"
      "v_mfma_f32_16x16x32_bf16 v[156:159], a[100:103], v[134:137], v[156:159]\n"
      "v_fmac_f32_e64 v204, v209, v209\n"
      "v_cvt_pk_bf16_f32 v161, v162, v163\n"
      "v_mfma_f32_16x16x32_bf16 v[156:159], a[104:107], v[112:115], v[156:159]\n"
      "ds_write_b16 v12, v168\n"
      "v_mfma_f32_16x16x32_bf16 v[156:159], a[108:111], v[100:103], v[156:159]\n"
      "v_cvt_pk_bf16_f32 v197, v198, v199\n"
      "v_mov_b32_e64 v195, v80\n"
      "v_mfma_f32_16x16x32_bf16 v[156:159], a[112:115], v[96:99], v[156:159]\n"
      "v_fma_f32 v205, v186, v186, s51\n"
      "v_min_u32 v8, 96, v8\n"
      "v_mfma_f32_16x16x32_bf16 v[156:159], a[116:119], v[210:213], v[156:159]\n"
      "v_permlane16_swap_b32 v80, v195\n"
      "v_mfma_f32_16x16x32_bf16 v[166:169], a[120:123], v[116:119], 0\n"
      "v_fmac_f32 v205, v187, v187\n"
      "v_fmac_f32 v205, v188, v188\n"
      "v_mfma_f32_16x16x32_bf16 v[166:169], a[124:127], v[134:137], v[166:169]\n"
      "v_fmac_f32 v204, v146, v146\n"
      "v_mul_f32 v80, s50, v80\n"
      "v_mfma_f32_16x16x32_bf16 v[166:169], a[128:131], v[112:115], v[166:169]\n"
      "v_fmac_f32 v204, v147, v147\n"
      "v_cvt_pk_bf16_f32 v214, v214, v215\n"
      "v_mfma_f32_16x16x32_bf16 v[166:169], a[132:135], v[100:103], v[166:169]\n"
      "v_fmac_f32 v205, v189, v189\n"
      "v_mul_f32 v175, v80, v175\n"
      "v_mfma_f32_16x16x32_bf16 v[166:169], a[136:139], v[96:99], v[166:169]\n"
      "s_and_b32 s77, s77, 15\n"
      "v_mul_f32 v178, v195, v178\n"
      "v_mul_f32 v171, v80, v171\n"
      "v_mfma_f32_16x16x32_bf16 v[166:169], a[140:143], v[210:213], v[166:169]\n"
      "ds_read_b128 v[162:165], v1 offset:22528\n"
      "v_fmac_f32 v204, v148, v148\n"
      "v_mfma_f32_16x16x32_bf16 v[152:155], v[116:119], a[144:147], 0\n"
      "v_mul_f32 v180, v195, v180\n"
      "v_cvt_pk_bf16_f32 v215, v216, v217\n"
      "v_mfma_f32_16x16x32_bf16 v[152:155], v[134:137], a[148:151], v[152:155]\n"
      "v_fmac_f32 v205, v156, v156\n"
      "v_mul_f32 v121, v80, v121\n"
      "v_mfma_f32_16x16x32_bf16 v[152:155], v[112:115], a[152:155], v[152:155]\n"
      "v_fmac_f32 v205, v157, v157\n"
      "v_mul_f32 v179, v195, v179\n"
      "v_mfma_f32_16x16x32_bf16 v[152:155], v[100:103], a[156:159], v[152:155]\n"
      "v_fmac_f32 v205, v158, v158\n"
      "v_fmac_f32 v205, v159, v159\n"
      "v_mfma_f32_16x16x32_bf16 v[152:155], v[96:99], a[160:163], v[152:155]\n"
      "v_fmac_f32 v204, v149, v149\n"
      "v_mul_f32 v127, v195, v127\n"
      "v_mfma_f32_16x16x32_bf16 v[152:155], v[210:213], a[164:167], v[152:155]\n"
      "v_mul_f32 v124, v195, v124\n"
      "v_fmac_f32 v205, v166, v166\n"
      "v_mfma_f32_16x16x32_bf16 v[142:145], v[116:119], a[168:171], 0\n"
      "v_fmac_f32 v205, v167, v167\n"
      "v_fmac_f32_e64 v205, v168, v168\n"
      "v_mfma_f32_16x16x32_bf16 v[142:145], v[134:137], a[172:175], v[142:145]\n"
      "ds_write_b64 v6, v[160:161] offset:30720\n"
      "v_mfma_f32_16x16x32_bf16 v[142:145], v[112:115], a[176:179], v[142:145]\n"
      "v_fmac_f32 v205, v169, v169\n"
      "v_mov_b32 v223, 0\n"
      "v_mfma_f32_16x16x32_bf16 v[142:145], v[100:103], a[180:183], v[142:145]\n"
      "v_permlane16_swap_b32 v204, v205\n"
      "v_mfma_f32_16x16x32_bf16 v[142:145], v[96:99], a[184:187], v[142:145]\n"
      "ds_read_b128 v[108:111], v0 offset:22528\n"
      "v_mul_f32 v125, v195, v125\n"
      "v_mfma_f32_16x16x32_bf16 v[142:145], v[210:213], a[188:191], v[142:145]\n"
      "v_mul_f32 v139, v195, v139\n"
      "v_cvt_pk_bf16_f32 v124, v124, v125\n"
      "v_mfma_f32_16x16x32_bf16 v[182:185], v[116:119], a[192:195], 0\n"
      "ds_read_b128 v[116:119], v1 offset:34816\n"
      "v_mul_f32 v120, v80, v120\n"
      "v_mfma_f32_16x16x32_bf16 v[182:185], v[134:137], a[196:199], v[182:185]\n"
      "ds_read_b128 v[134:137], v1 offset:28672\n"
      "v_add_f32_e64 v204, v204, v205\n"
      "v_mfma_f32_16x16x32_bf16 v[182:185], v[112:115], a[200:203], v[182:185]\n"
      "ds_read_b128 v[112:115], v0 offset:34816\n"
      "v_add_u32 v78, s77, v78\n"
      "v_mfma_f32_16x16x32_bf16 v[182:185], v[100:103], a[204:207], v[182:185]\n"
      "v_mul_f32 v174, v80, v174\n"
      "v_mul_f32_e64 v181, v195, v181\n"
      "v_mfma_f32_16x16x32_bf16 v[182:185], v[96:99], a[208:211], v[182:185]\n"
      "v_mov_b32 v205, v204\n"
      "v_cvt_pk_bf16_f32 v120, v120, v121\n"
      "v_add_u32 v8, s77, v8\n"
      "v_mfma_f32_16x16x32_bf16 v[182:185], v[210:213], a[212:215], v[182:185]\n"
      "s_waitcnt lgkmcnt(3)\n"
      "v_permlane32_swap_b32 v204, v205\n"
      "v_mfma_f32_16x16x32_bf16 v[210:213], a[0:3], v[108:111], 0\n"
      "s_waitcnt vmcnt(7)\n"
      "v_mul_f32 v122, v80, v122\n"
      "v_mul_f32_e64 v126, v195, v126\n"
      "v_mfma_f32_16x16x32_bf16 v[210:213], a[4:7], v[162:165], v[210:213]\n"
      "v_mul_f32 v123, v80, v123\n"
      "v_and_b32 v93, 2, v78\n"
      "v_mfma_f32_16x16x32_bf16 v[210:213], a[8:11], v[200:203], v[210:213]\n"
      "s_waitcnt lgkmcnt(0)\n"
      "v_add_f32 v204, v204, v205\n"
      "v_cvt_pk_bf16_f32 v178, v178, v179\n"
      "v_mfma_f32_16x16x32_bf16 v[210:213], a[12:15], v[134:137], v[210:213]\n"
      "v_mul_f32 v138, v195, v138\n"
      "v_mul_f32 v173, v80, v173\n"
      "v_mfma_f32_16x16x32_bf16 v[210:213], a[16:19], v[112:115], v[210:213]\n"
      "v_rsq_f32_e64 v204, v204\n"
      "v_mfma_f32_16x16x32_bf16 v[210:213], a[20:23], v[116:119], v[210:213]\n"
      "v_cvt_pk_bf16_f32 v199, v144, v145\n"
      "v_cvt_pk_bf16_f32 v121, v122, v123\n"
      "v_mfma_f32_16x16x32_bf16 v[190:193], a[24:27], v[108:111], 0\n"
      "v_lshlrev_b32 v77, 4, v77\n"
      "v_cvt_pk_bf16_f32 v132, v152, v153\n"
      "v_mfma_f32_16x16x32_bf16 v[190:193], a[28:31], v[162:165], v[190:193]\n"
      "v_mov_b32 v205, v204\n"
      "v_cvt_pk_bf16_f32 v198, v142, v143\n"
      "v_mfma_f32_16x16x32_bf16 v[190:193], a[32:35], v[200:203], v[190:193]\n"
      "v_permlane16_swap_b32 v204, v205\n"
      "v_mfma_f32_16x16x32_bf16 v[190:193], a[36:39], v[134:137], v[190:193]\n"
      "v_mul_f32 v204, s50, v204\n"
      "v_mul_f32 v172, v80, v172\n"
      "v_mfma_f32_16x16x32_bf16 v[190:193], a[40:43], v[112:115], v[190:193]\n"
      "v_mul_f32 v207, v204, v207\n"
      "v_cvt_pk_bf16_f32 v174, v174, v175\n"
      "v_mfma_f32_16x16x32_bf16 v[190:193], a[44:47], v[116:119], v[190:193]\n"
      "v_cvt_pk_bf16_f32 v125, v126, v127\n"
      "v_cvt_pk_bf16_f32 v217, v184, v185\n"
      "v_mfma_f32_16x16x32_bf16 v[126:129], a[48:51], v[108:111], 0\n"
      "v_cmp_gt_u32 s[72:73], 64, v77\n"
      "v_fma_f32 v123, v210, v210, s51\n"
      "v_mfma_f32_16x16x32_bf16 v[126:129], a[52:55], v[162:165], v[126:129]\n"
      "v_fmac_f32_e64 v123, v211, v211\n"
      "v_readlane_b32 s46, v37, s42\n"
      "v_mfma_f32_16x16x32_bf16 v[126:129], a[56:59], v[200:203], v[126:129]\n"
      "v_mul_f32 v148, v204, v148\n"
      "v_mul_f32 v170, v80, v170\n"
      "v_mfma_f32_16x16x32_bf16 v[126:129], a[60:63], v[134:137], v[126:129]\n"
      "v_cvt_pk_bf16_f32 v133, v154, v155\n"
      "v_cvt_pk_bf16_f32 v179, v180, v181\n"
      "v_mfma_f32_16x16x32_bf16 v[126:129], a[64:67], v[112:115], v[126:129]\n"
      "v_cvt_pk_bf16_f32 v216, v182, v183\n"
      "v_lshlrev_b64 v[222:223], v77, v[222:223]\n"
      "v_mfma_f32_16x16x32_bf16 v[126:129], a[68:71], v[116:119], v[126:129]\n"
      "v_mul_f32 v149, v204, v149\n"
      "v_fmac_f32 v123, v212, v212\n"
      "v_mul_f32 v206, v204, v206\n"
      "v_and_b32 v78, -4, v78\n"
      "v_fmac_f32_e64 v123, v213, v213\n"
      "v_pk_mul_f32 v[156:157], v[156:157], v[204:205] op_sel:[0,1]\n"
      "v_pk_mul_f32 v[186:187], v[186:187], v[204:205] op_sel:[0,1]\n"
      "v_pk_mul_f32 v[188:189], v[188:189], v[204:205] op_sel:[0,1]\n"
      "v_pk_mul_f32 v[208:209], v[208:209], v[204:205] op_sel_hi:[1,0]\n"
      "v_pk_mul_f32 v[166:167], v[166:167], v[204:205] op_sel:[0,1]\n"
      "v_pk_mul_f32 v[104:105], v[104:105], v[204:205] op_sel_hi:[1,0]\n"
      "v_pk_mul_f32 v[176:177], v[176:177], v[80:81] op_sel_hi:[1,0]\n"
      "v_pk_mul_f32 v[140:141], v[140:141], v[194:195] op_sel:[0,1]\n"
      "v_pk_mul_f32 v[106:107], v[106:107], v[204:205] op_sel_hi:[1,0]\n"
      "v_mfma_f32_16x16x32_bf16 v[182:185], a[72:75], v[108:111], 0\n"
      "v_mul_f32 v168, v205, v168\n"
      "v_mul_f32 v146, v204, v146\n"
      "v_mfma_f32_16x16x32_bf16 v[182:185], a[76:79], v[162:165], v[182:185]\n"
      "v_mul_f32 v158, v205, v158\n"
      "v_cvt_pk_bf16_f32 v181, v140, v141\n"
      "v_mfma_f32_16x16x32_bf16 v[182:185], a[80:83], v[200:203], v[182:185]\n"
      "v_mul_f32 v169, v205, v169\n"
      "v_fmac_f32_e64 v123, v190, v190\n"
      "v_mfma_f32_16x16x32_bf16 v[182:185], a[84:87], v[134:137], v[182:185]\n"
      "v_fmac_f32_e64 v123, v191, v191\n"
      "v_cndmask_b32_e64 v223, 0, v223, s[72:73]\n"
      "v_mfma_f32_16x16x32_bf16 v[182:185], a[88:91], v[112:115], v[182:185]\n"
      "v_cvt_pk_bf16_f32 v180, v138, v139\n"
      "v_fmac_f32 v123, v192, v192\n"
      "v_mfma_f32_16x16x32_bf16 v[182:185], a[92:95], v[116:119], v[182:185]\n"
      "v_cvt_pk_bf16_f32 v175, v176, v177\n"
      "v_fmac_f32 v123, v193, v193\n"
      "v_mfma_f32_16x16x32_bf16 v[152:155], a[96:99], v[108:111], 0\n"
      "v_cvt_pk_bf16_f32 v104, v104, v105\n"
      "v_mul_f32_e64 v147, v204, v147\n"
      "v_mfma_f32_16x16x32_bf16 v[152:155], a[100:103], v[162:165], v[152:155]\n"
      "v_cvt_pk_bf16_f32 v186, v186, v187\n"
      "v_fmac_f32 v123, v126, v126\n"
      "v_mfma_f32_16x16x32_bf16 v[152:155], a[104:107], v[200:203], v[152:155]\n"
      "v_mov_b32 v195, 0\n"
      "v_fma_f32 v122, v182, v182, s51\n"
      "v_mfma_f32_16x16x32_bf16 v[152:155], a[108:111], v[134:137], v[152:155]\n"
      "v_fmac_f32 v122, v183, v183\n"
      "v_cvt_pk_bf16_f32 v187, v188, v189\n"
      "v_mfma_f32_16x16x32_bf16 v[152:155], a[112:115], v[112:115], v[152:155]\n"
      "v_fmac_f32 v122, v184, v184\n"
      "v_cvt_pk_bf16_f32 v105, v106, v107\n"
      "v_mfma_f32_16x16x32_bf16 v[152:155], a[116:119], v[116:119], v[152:155]\n"
      "v_mul_f32_e64 v159, v205, v159\n"
      "v_cvt_pk_bf16_f32 v106, v206, v207\n"
      "v_mfma_f32_16x16x32_bf16 v[142:145], a[120:123], v[108:111], 0\n"
      "v_cvt_pk_bf16_f32 v107, v208, v209\n"
      "v_fmac_f32 v123, v127, v127\n"
      "v_mfma_f32_16x16x32_bf16 v[142:145], a[124:127], v[162:165], v[142:145]\n"
      "v_fmac_f32 v123, v128, v128\n"
      "v_fmac_f32 v122, v185, v185\n"
      "v_mfma_f32_16x16x32_bf16 v[142:145], a[128:131], v[200:203], v[142:145]\n"
      "v_fmac_f32 v123, v129, v129\n"
      "v_cvt_pk_bf16_f32 v166, v166, v167\n"
      "v_mfma_f32_16x16x32_bf16 v[142:145], a[132:135], v[134:137], v[142:145]\n"
      "s_or_b32 s95, s45, s46\n"
      "v_cvt_pk_bf16_f32 v177, v172, v173\n"
      "v_cndmask_b32_e64 v222, 0, v222, s[72:73]\n"
      "v_mfma_f32_16x16x32_bf16 v[142:145], a[136:139], v[112:115], v[142:145]\n"
      "v_fmac_f32 v122, v152, v152\n"
      "v_fmac_f32 v122, v153, v153\n"
      "v_mfma_f32_16x16x32_bf16 v[142:145], a[140:143], v[116:119], v[142:145]\n"
      "s_or_b32 s95, s95, s48\n"
      "v_cvt_pk_bf16_f32 v189, v158, v159\n"
      "v_fmac_f32_e64 v122, v154, v154\n"
      "v_mfma_f32_16x16x32_bf16 v[204:207], v[108:111], a[144:147], 0\n"
      "v_cvt_pk_bf16_f32 v146, v146, v147\n"
      "v_fmac_f32 v122, v155, v155\n"
      "v_mfma_f32_16x16x32_bf16 v[204:207], v[162:165], a[148:151], v[204:207]\n"
      "s_nop 0\n"
      "v_fmac_f32 v122, v142, v142\n"
      "v_lshlrev_b64 v[194:195], v77, v[194:195]\n"
      "v_mfma_f32_16x16x32_bf16 v[204:207], v[200:203], a[152:155], v[204:207]\n"
      "v_fmac_f32 v122, v143, v143\n"
      "v_cmp_gt_u32 s[72:73], 64, v77\n"
      "v_mfma_f32_16x16x32_bf16 v[204:207], v[134:137], a[156:159], v[204:207]\n"
      "v_cvt_pk_bf16_f32 v167, v168, v169\n"
      "v_fmac_f32 v122, v144, v144\n"
      "v_mfma_f32_16x16x32_bf16 v[204:207], v[112:115], a[160:163], v[204:207]\n"
      "v_fmac_f32 v122, v145, v145\n"
      "v_cvt_pk_bf16_f32 v176, v170, v171\n"
      "v_mfma_f32_16x16x32_bf16 v[204:207], v[116:119], a[164:167], v[204:207]\n"
      "v_permlane16_swap_b32 v123, v122\n"
      "v_mfma_f32_16x16x32_bf16 v[158:161], v[108:111], a[168:171], 0\n"
      "v_add_f32 v123, v123, v122\n"
      "v_mov_b32 v122, v123\n"
      "v_mfma_f32_16x16x32_bf16 v[158:161], v[162:165], a[172:175], v[158:161]\n"
      "s_nop 0\n"
      "v_permlane32_swap_b32 v123, v122\n"
      "v_mfma_f32_16x16x32_bf16 v[158:161], v[200:203], a[176:179], v[158:161]\n"
      "v_add_f32 v123, v123, v122\n"
      "v_cvt_pk_bf16_f32 v188, v156, v157\n"
      "v_mfma_f32_16x16x32_bf16 v[158:161], v[134:137], a[180:183], v[158:161]\n"
      "v_rsq_f32_e64 v123, v123\n"
      "v_mfma_f32_16x16x32_bf16 v[158:161], v[112:115], a[184:187], v[158:161]\n"
      "ds_read_b128 v[168:171], v2\n"
      "v_mov_b32 v122, v123\n"
      "v_mfma_f32_16x16x32_bf16 v[158:161], v[116:119], a[188:191], v[158:161]\n"
      "ds_read_b128 v[100:103], v2 offset:3072\n"
      "v_permlane16_swap_b32 v123, v122\n"
      "v_mfma_f32_16x16x32_bf16 v[138:141], v[108:111], a[192:195], 0\n"
      "ds_read_b128 v[108:111], v7\n"
      "v_cvt_pk_bf16_f32 v77, v206, v207\n"
      "v_mfma_f32_16x16x32_bf16 v[138:141], v[162:165], a[196:199], v[138:141]\n"
      "ds_read_b128 v[206:209], v2 offset:6144\n"
      "v_mul_f32 v183, v122, v183\n"
      "v_mfma_f32_16x16x32_bf16 v[138:141], v[200:203], a[200:203], v[138:141]\n"
      "ds_read_b128 v[200:203], v7 offset:4096\n"
      "v_mul_f32 v154, v122, v154\n"
      "v_mul_f32 v123, s50, v123\n"
      "v_mul_f32 v182, v122, v182\n"
      "v_cvt_pk_bf16_f32 v147, v148, v149\n"
      "v_pk_mul_f32 v[212:213], v[212:213], v[122:123] op_sel:[0,1]\n"
      "v_pk_mul_f32 v[190:191], v[190:191], v[122:123] op_sel:[0,1]\n"
      "v_pk_mul_f32 v[144:145], v[144:145], v[122:123] op_sel_hi:[1,0]\n"
      "v_pk_mul_f32 v[210:211], v[210:211], v[122:123] op_sel:[0,1]\n"
      "v_pk_mul_f32 v[142:143], v[142:143], v[122:123] op_sel_hi:[1,0]\n"
      "v_mfma_f32_16x16x32_bf16 v[138:141], v[134:137], a[204:207], v[138:141]\n"
      "v_cvt_pk_bf16_f32 v80, v158, v159\n"
      "v_add_u32 v84, 110592, v78\n"
      "v_mfma_f32_16x16x32_bf16 v[138:141], v[112:115], a[208:211], v[138:141]\n"
      "v_cvt_pk_bf16_f32 v182, v182, v183\n"
      "v_mul_f32 v192, v123, v192\n"
      "v_mfma_f32_16x16x32_bf16 v[138:141], v[116:119], a[212:215], v[138:141]\n"
      "s_waitcnt lgkmcnt(1)\n"
      "v_cvt_pk_bf16_f32 v142, v142, v143\n"
      "v_cvt_pk_bf16_f32 v143, v144, v145\n"
      "v_mfma_f32_16x16x32_bf16 v[96:99], a[216:219], v[168:171], 0\n"
      "buffer_store_dwordx4 v[108:111], v20, s[24:27], s85 offen sc1\n"
      "v_pk_mul_f32 v[184:185], v[184:185], v[122:123] op_sel_hi:[1,0]\n"
      "v_pk_mul_f32 v[128:129], v[128:129], v[122:123] op_sel:[0,1]\n"
      "v_pk_mul_f32 v[152:153], v[152:153], v[122:123] op_sel_hi:[1,0]\n"
      "v_pk_mul_f32 v[126:127], v[126:127], v[122:123] op_sel:[0,1]\n"
      "v_mfma_f32_16x16x32_bf16 v[96:99], a[220:223], v[100:103], v[96:99]\n"
      "ds_read_b128 v[112:115], v2 offset:9216\n"
      "v_mul_f32_e64 v155, v122, v155\n"
      "v_mfma_f32_16x16x32_bf16 v[96:99], a[224:227], v[206:209], v[96:99]\n"
      "ds_read_b128 v[156:159], v2 offset:15360\n"
      "v_cvt_pk_bf16_f32 v85, v140, v141\n"
      "v_mfma_f32_16x16x32_bf16 v[116:119], a[240:243], v[168:171], 0\n"
      "ds_read_b128 v[162:165], v2 offset:12288\n"
      "v_cvt_pk_bf16_f32 v183, v184, v185\n"
      "v_mfma_f32_16x16x32_bf16 v[116:119], a[244:247], v[100:103], v[116:119]\n"
      "ds_read_b128 v[148:151], v7 offset:8192\n"
      "v_cvt_pk_bf16_f32 v184, v152, v153\n"
      "v_mfma_f32_16x16x32_bf16 v[116:119], a[248:251], v[206:209], v[116:119]\n"
      "s_cmp_ge_u32 s95, 0x40000000\n"
      "s_cselect_b32 s94, -1, -2\n"
      "v_cvt_pk_bf16_f32 v185, v154, v155\n"
      "v_mfma_f32_16x16x32_bf16 v[134:137], v[232:235], v[168:171], 0\n"
      "ds_read_b64 v[172:173], v10 offset:1192\n"
      "v_cvt_pk_bf16_f32 v76, v204, v205\n"
      "v_mfma_f32_16x16x32_bf16 v[134:137], v[236:239], v[100:103], v[134:137]\n"
      "s_waitcnt lgkmcnt(4)\n"
      "v_cvt_pk_bf16_f32 v210, v210, v211\n"
      "v_cvt_pk_bf16_f32 v211, v212, v213\n"
      "v_mfma_f32_16x16x32_bf16 v[134:137], v[240:243], v[206:209], v[134:137]\n"
      "buffer_store_dwordx4 v[200:203], v21, s[24:27], s85 offen sc1\n"
      "v_mfma_f32_16x16x32_bf16 v[96:99], a[228:231], v[112:115], v[96:99]\n"
      "s_nop 0\n"
      "ds_read_b128 v[200:203], v7 offset:12288\n"
      "s_waitcnt lgkmcnt(2)\n"
      "v_mfma_f32_16x16x32_bf16 v[96:99], a[232:235], v[162:165], v[96:99]\n"
      "ds_read_b128 v[168:171], v10 offset:1200\n"
      "v_cvt_pk_bf16_f32 v212, v190, v191\n"
      "v_mfma_f32_16x16x32_bf16 v[96:99], a[236:239], v[156:159], v[96:99]\n"
      "ds_read2_b32 v[206:207], v84 offset0:8 offset1:9\n"
      "v_mul_f32 v193, v123, v193\n"
      "v_mfma_f32_16x16x32_bf16 v[116:119], a[252:255], v[112:115], v[116:119]\n"
      "ds_read2_b32 v[140:141], v84 offset0:2 offset1:10\n"
      "v_cvt_pk_bf16_f32 v213, v192, v193\n"
      "v_mfma_f32_16x16x32_bf16 v[116:119], v[224:227], v[162:165], v[116:119]\n"
      "ds_read2_b32 v[204:205], v84 offset0:0 offset1:1\n"
      "v_cndmask_b32_e64 v195, 0, v195, s[72:73]\n"
      "v_mfma_f32_16x16x32_bf16 v[116:119], v[228:231], v[156:159], v[116:119]\n"
      "v_cvt_pk_bf16_f32 v126, v126, v127\n"
      "v_add_u32 v8, 115296, v8\n"
      "v_mfma_f32_16x16x32_bf16 v[134:137], v[244:247], v[112:115], v[134:137]\n"
      "ds_read_b128 v[112:115], v7 offset:16384\n"
      "v_cvt_pk_bf16_f32 v127, v128, v129\n"
      "v_mfma_f32_16x16x32_bf16 v[134:137], v[248:251], v[162:165], v[134:137]\n"
      "buffer_store_dwordx4 v[148:151], v22, s[24:27], s85 offen sc1\n"
      "v_mfma_f32_16x16x32_bf16 v[134:137], v[252:255], v[156:159], v[134:137]\n"
      "ds_read_b128 v[156:159], v2 offset:3328\n"
      "v_fma_f32 v144, v96, v96, s52\n"
      "v_mfma_f32_16x16x32_bf16 v[162:165], v[178:181], v[174:177], v[40:43]\n"
      "s_waitcnt lgkmcnt(2)\n"
      "v_alignbyte_b32 v204, v205, v204, v93\n"
      "v_cvt_pk_bf16_f32 v81, v160, v161\n"
      "v_mfma_f32_16x16x32_bf16 v[148:151], v[186:189], v[174:177], v[44:47]\n"
      "ds_read2_b32 v[152:153], v84 offset0:16 offset1:17\n"
      "v_alignbyte_b32 v205, v140, v205, v93\n"
      "v_mfma_f32_16x16x32_bf16 v[190:193], v[182:185], v[174:177], v[48:51]\n"
      "ds_read2_b32 v[122:123], v84 offset0:24 offset1:18\n"
      "v_alignbyte_b32 v206, v207, v206, v93\n"
      "v_mfma_f32_16x16x32_bf16 v[100:103], v[168:171], v[174:177], v[24:27]\n"
      "s_cmp_gt_u32 s42, s44\n"
      "s_cselect_b32 s95, s94, s95\n"
      "v_mul_f32 v145, v97, v97\n"
      "v_mfma_f32_16x16x16_bf16 v[162:165], v[124:125], v[120:121], v[162:165]\n"
      "v_fmac_f32 v145, v99, v99\n"
      "v_fmac_f32 v144, v98, v98\n"
      "v_fmac_f32 v145, v117, v117\n"
      "v_mfma_f32_16x16x16_bf16 v[148:151], v[166:167], v[120:121], v[148:151]\n"
      "v_fmac_f32 v144, v116, v116\n"
      "v_alignbyte_b32 v207, v141, v207, v93\n"
      "v_fmac_f32 v145, v119, v119\n"
      "v_mfma_f32_16x16x16_bf16 v[190:193], v[142:143], v[120:121], v[190:193]\n"
      "buffer_store_dwordx4 v[200:203], v23, s[24:27], s85 offen sc1\n"
      "v_mfma_f32_16x16x16_bf16 v[100:103], v[172:173], v[120:121], v[100:103]\n"
      "v_cvt_pk_bf16_f32 v84, v138, v139\n"
      "s_waitcnt lgkmcnt(0)\n"
      "v_alignbyte_b32 v152, v153, v152, v93\n"
      "v_mfma_f32_16x16x16_bf16 v[162:165], v[222:223], v[204:205], v[162:165]\n"
      "v_cndmask_b32_e64 v194, 0, v194, s[72:73]\n"
      "v_fmac_f32 v144, v118, v118\n"
      "v_alignbyte_b32 v153, v123, v153, v93\n"
      "v_mfma_f32_16x16x16_bf16 v[148:151], v[222:223], v[206:207], v[148:151]\n"
      "v_alignbyte_b32 v122, v122, v122, v93\n"
      "v_fmac_f32 v144, v134, v134\n"
      "v_fmac_f32_e64 v145, v135, v135\n"
      "v_mfma_f32_16x16x16_bf16 v[190:193], v[222:223], v[152:153], v[190:193]\n"
      "ds_read_b128 v[174:177], v2 offset:256\n"
      "v_add_u32 v109, 113728, v78\n"
      "v_mfma_f32_16x16x16_bf16 v[100:103], v[222:223], v[122:123], v[100:103]\n"
      "ds_read_b128 v[138:141], v2 offset:6400\n"
      "v_add_u32 v121, 112160, v78\n"
      "v_mfma_f32_16x16x16_bf16 v[162:165], v[194:195], v[204:205], v[162:165]\n"
      "ds_read2_b32 v[154:155], v121 offset0:0 offset1:1\n"
      "v_fmac_f32 v145, v137, v137\n"
      "v_mfma_f32_16x16x16_bf16 v[148:151], v[194:195], v[206:207], v[148:151]\n"
      "ds_read_u16 v8, v8\n"
      "v_fmac_f32 v144, v136, v136\n"
      "v_mfma_f32_16x16x16_bf16 v[190:193], v[194:195], v[152:153], v[190:193]\n"
      "buffer_store_dwordx4 v[112:115], v25, s[24:27], s85 offen sc1\n"
      "v_mfma_f32_16x16x16_bf16 v[100:103], v[194:195], v[122:123], v[100:103]\n"
      "v_add_f32_e64 v144, v144, v145\n"
      "v_max3_f32 v120, v162, v163, v164\n"
      "s_waitcnt lgkmcnt(3)\n"
      "v_mfma_f32_16x16x32_bf16 v[200:203], a[216:219], v[174:177], 0\n"
      "v_max3_f32 v113, v165, v148, v149\n"
      "v_max3_f32 v120, v120, v150, v151\n"
      "v_mfma_f32_16x16x32_bf16 v[200:203], a[220:223], v[156:159], v[200:203]\n"
      "s_waitcnt lgkmcnt(1)\n"
      "v_alignbyte_b32 v154, v155, v154, v93\n"
      "v_max3_f32 v120, v120, v192, v193\n"
      "v_mfma_f32_16x16x32_bf16 v[200:203], a[224:227], v[138:141], v[200:203]\n"
      "v_max3_f32 v113, v113, v190, v191\n"
      "v_max3_f32 v120, v120, v113, v100\n"
      "v_mfma_f32_16x16x32_bf16 v[112:115], a[240:243], v[174:177], 0\n"
      "ds_read2_b32 v[102:103], v121 offset0:16 offset1:17\n"
      "v_mov_b32 v78, v120\n"
      "v_mfma_f32_16x16x32_bf16 v[112:115], a[244:247], v[156:159], v[112:115]\n"
      "ds_read2_b32 v[152:153], v121 offset0:8 offset1:9\n"
      "v_permlane16_swap_b32 v120, v78\n"
      "v_mfma_f32_16x16x32_bf16 v[112:115], a[248:251], v[138:141], v[112:115]\n"
      "v_max_f32 v120, v120, v78\n"
      "s_waitcnt vmcnt(6)\n"
      "v_or3_b32 v218, v218, v219, v220\n"
      "v_mfma_f32_16x16x32_bf16 v[204:207], v[232:235], v[174:177], 0\n"
      "ds_read2_b32 v[122:123], v121 offset0:2 offset1:10\n"
      "v_mov_b32 v78, v120\n"
      "v_mfma_f32_16x16x32_bf16 v[204:207], v[236:239], v[156:159], v[204:207]\n"
      "ds_read2_b32 v[110:111], v121 offset0:24 offset1:18\n"
      "v_permlane32_swap_b32 v120, v78\n"
      "v_max_f32 v120, v120, v78\n"
      "v_sub_f32 v100, v100, v120\n"
      "v_pk_add_f32 v[150:151], v[150:151], v[120:121] op_sel_hi:[1,0] neg_lo:[0,1] neg_hi:[0,1]\n"
      "v_pk_add_f32 v[148:149], v[148:149], v[120:121] op_sel_hi:[1,0] neg_lo:[0,1] neg_hi:[0,1]\n"
      "v_pk_add_f32 v[164:165], v[164:165], v[120:121] op_sel_hi:[1,0] neg_lo:[0,1] neg_hi:[0,1]\n"
      "v_pk_add_f32 v[190:191], v[190:191], v[120:121] op_sel_hi:[1,0] neg_lo:[0,1] neg_hi:[0,1]\n"
      "v_pk_add_f32 v[162:163], v[162:163], v[120:121] op_sel_hi:[1,0] neg_lo:[0,1] neg_hi:[0,1]\n"
      "v_pk_add_f32 v[192:193], v[192:193], v[120:121] op_sel_hi:[1,0] neg_lo:[0,1] neg_hi:[0,1]\n"
      "v_mfma_f32_16x16x32_bf16 v[204:207], v[240:243], v[138:141], v[204:207]\n"
      "s_waitcnt lgkmcnt(2)\n"
      "v_alignbyte_b32 v152, v153, v152, v93\n"
      "s_waitcnt vmcnt(5)\n"
      "v_or3_b32 v218, v218, v221, v82\n"
      "v_mfma_f32_16x16x32_bf16 v[174:177], v[178:181], v[104:107], v[52:55]\n"
      "ds_write_b32 v31, v218 offset:5632\n"
      "v_mfma_f32_16x16x32_bf16 v[156:159], v[186:189], v[104:107], v[56:59]\n"
      "s_waitcnt lgkmcnt(2)\n"
      "v_exp_f32 v151, v151\n"
      "v_mfma_f32_16x16x32_bf16 v[218:221], v[182:185], v[104:107], v[60:63]\n"
      "s_waitcnt lgkmcnt(1)\n"
      "v_alignbyte_b32 v153, v123, v153, v93\n"
      "v_alignbyte_b32 v155, v122, v155, v93\n"
      "v_mfma_f32_16x16x32_bf16 v[120:123], v[168:171], v[104:107], v[26:29]\n"
      "ds_read_b128 v[104:107], v2 offset:9472\n"
      "v_exp_f32 v162, v162\n"
      "v_mfma_f32_16x16x16_bf16 v[174:177], v[124:125], v[146:147], v[174:177]\n"
      "v_exp_f32 v148, v148\n"
      "v_exp_f32 v163, v163\n"
      "v_mfma_f32_16x16x16_bf16 v[156:159], v[166:167], v[146:147], v[156:159]\n"
      "v_alignbyte_b32 v110, v110, v110, v93\n"
      "v_exp_f32 v164, v164\n"
      "v_mfma_f32_16x16x16_bf16 v[218:221], v[142:143], v[146:147], v[218:221]\n"
      "v_exp_f32 v165, v165\n"
      "v_cvt_pk_bf16_f32 v162, v162, v163\n"
      "v_mfma_f32_16x16x16_bf16 v[120:123], v[172:173], v[146:147], v[120:123]\n"
      "v_alignbyte_b32 v102, v103, v102, v93\n"
      "v_exp_f32_e64 v193, v193\n"
      "v_mfma_f32_16x16x16_bf16 v[174:177], v[222:223], v[154:155], v[174:177]\n"
      "v_alignbyte_b32 v103, v111, v103, v93\n"
      "v_exp_f32 v149, v149\n"
      "v_mfma_f32_16x16x16_bf16 v[156:159], v[222:223], v[152:153], v[156:159]\n"
      "v_cvt_pk_bf16_f32 v163, v164, v165\n"
      "v_exp_f32 v192, v192\n"
      "v_mfma_f32_16x16x16_bf16 v[218:221], v[222:223], v[102:103], v[218:221]\n"
      "v_cvt_pk_bf16_f32 v164, v148, v149\n"
      "v_exp_f32 v150, v150\n"
      "v_mfma_f32_16x16x16_bf16 v[120:123], v[222:223], v[110:111], v[120:123]\n"
      "v_exp_f32 v190, v190\n"
      "v_cvt_pk_bf16_f32 v89, v192, v193\n"
      "v_mfma_f32_16x16x16_bf16 v[174:177], v[194:195], v[154:155], v[174:177]\n"
      "ds_read_b128 v[146:149], v2 offset:12544\n"
      "v_cvt_pk_bf16_f32 v165, v150, v151\n"
      "v_mfma_f32_16x16x16_bf16 v[156:159], v[194:195], v[152:153], v[156:159]\n"
      "ds_read_b128 v[150:153], v2 offset:15616\n"
      "v_exp_f32_e64 v191, v191\n"
      "v_mfma_f32_16x16x16_bf16 v[218:221], v[194:195], v[102:103], v[218:221]\n"
      "v_readlane_b32 s47, v38, s43\n"
      "v_exp_f32 v100, v100\n"
      "v_cvt_pk_bf16_f32 v88, v190, v191\n"
      "v_mfma_f32_16x16x16_bf16 v[120:123], v[194:195], v[110:111], v[120:123]\n"
      "v_cvt_pk_bf16_f32 v90, v100, 0\n"
      "v_max3_f32 v191, v174, v175, v176\n"
      "v_max3_f32 v191, v191, v158, v159\n"
      "v_max3_f32 v82, v177, v156, v157\n"
      "s_waitcnt lgkmcnt(2)\n"
      "v_mfma_f32_16x16x32_bf16 v[200:203], a[228:231], v[104:107], v[200:203]\n"
      "ds_read_u16 v78, v11 offset:1168\n"
      "v_max3_f32 v82, v82, v218, v219\n"
      "v_max3_f32 v191, v191, v220, v221\n"
      "v_max3_f32 v191, v191, v82, v120\n"
      "v_mov_b32 v190, v191\n"
      "s_waitcnt lgkmcnt(2)\n"
      "v_mfma_f32_16x16x32_bf16 v[200:203], a[232:235], v[146:149], v[200:203]\n"
      "s_waitcnt lgkmcnt(1)\n"
      "v_permlane16_swap_b32 v191, v190\n"
      "v_mfma_f32_16x16x32_bf16 v[200:203], a[236:239], v[150:153], v[200:203]\n"
      "v_max_f32 v191, v191, v190\n"
      "v_mov_b32 v190, v191\n"
      "v_mfma_f32_16x16x32_bf16 v[112:115], a[252:255], v[104:107], v[112:115]\n"
      "ds_read_u16 v82, v11 offset:1200\n"
      "v_permlane32_swap_b32 v191, v190\n"
      "v_max_f32 v191, v191, v190\n"
      "v_sub_f32_e64 v120, v120, v191\n"
      "v_pk_add_f32 v[220:221], v[220:221], v[190:191] op_sel:[0,1] neg_lo:[0,1] neg_hi:[0,1]\n"
      "v_pk_add_f32 v[156:157], v[156:157], v[190:191] op_sel:[0,1] neg_lo:[0,1] neg_hi:[0,1]\n"
      "v_pk_add_f32 v[218:219], v[218:219], v[190:191] op_sel:[0,1] neg_lo:[0,1] neg_hi:[0,1]\n"
      "v_pk_add_f32 v[174:175], v[174:175], v[190:191] op_sel:[0,1] neg_lo:[0,1] neg_hi:[0,1]\n"
      "v_pk_add_f32 v[176:177], v[176:177], v[190:191] op_sel:[0,1] neg_lo:[0,1] neg_hi:[0,1]\n"
      "v_pk_add_f32 v[158:159], v[158:159], v[190:191] op_sel:[0,1] neg_lo:[0,1] neg_hi:[0,1]\n"
      "v_mfma_f32_16x16x32_bf16 v[112:115], v[224:227], v[146:149], v[112:115]\n"
      "v_exp_f32 v175, v175\n"
      "v_mfma_f32_16x16x32_bf16 v[112:115], v[228:231], v[150:153], v[112:115]\n"
      "v_exp_f32 v156, v156\n"
      "v_mfma_f32_16x16x32_bf16 v[204:207], v[244:247], v[104:107], v[204:207]\n"
      "ds_read2_b32 v[128:129], v109 offset0:0 offset1:1\n"
      "v_exp_f32 v158, v158\n"
      "v_mfma_f32_16x16x32_bf16 v[204:207], v[248:251], v[146:149], v[204:207]\n"
      "ds_read2_b32 v[190:191], v109 offset0:2 offset1:10\n"
      "v_exp_f32 v218, v218\n"
      "v_mfma_f32_16x16x32_bf16 v[204:207], v[252:255], v[150:153], v[204:207]\n"
      "v_exp_f32_e64 v174, v174\n"
      "v_mfma_f32_16x16x32_bf16 v[146:149], v[32:35], v[162:165], 0\n"
      "ds_read_u16 v86, v11 offset:1232\n"
      "v_exp_f32 v219, v219\n"
      "v_mfma_f32_16x16x32_bf16 v[146:149], v[32:35], v[88:91], v[146:149]\n"
      "s_waitcnt lgkmcnt(3)\n"
      "v_exp_f32_e64 v176, v176\n"
      "v_mfma_f32_16x16x32_bf16 v[104:107], v[130:133], v[162:165], 0\n"
      "ds_read2_b32 v[208:209], v109 offset0:8 offset1:9\n"
      "v_cvt_pk_bf16_f32 v174, v174, v175\n"
      "v_mfma_f32_16x16x32_bf16 v[104:107], v[76:79], v[88:91], v[104:107]\n"
      "ds_read2_b32 v[160:161], v109 offset0:24 offset1:18\n"
      "v_exp_f32 v157, v157\n"
      "v_mfma_f32_16x16x32_bf16 v[148:151], v[196:199], v[162:165], 0\n"
      "s_waitcnt lgkmcnt(3)\n"
      "v_alignbyte_b32 v128, v129, v128, v93\n"
      "v_cvt_pk_bf16_f32 v92, v218, v219\n"
      "v_mfma_f32_16x16x32_bf16 v[148:151], v[80:83], v[88:91], v[148:151]\n"
      "ds_read2_b32 v[122:123], v109 offset0:16 offset1:17\n"
      "v_alignbyte_b32 v129, v190, v129, v93\n"
      "v_mfma_f32_16x16x32_bf16 v[152:155], v[214:217], v[162:165], 0\n"
      "s_waitcnt lgkmcnt(3)\n"
      "v_exp_f32 v159, v159\n"
      "v_mfma_f32_16x16x32_bf16 v[152:155], v[84:87], v[88:91], v[152:155]\n"
      "v_exp_f32 v177, v177\n"
      "v_mfma_f32_16x16x32_bf16 v[100:103], v[178:181], v[210:213], v[64:67]\n"
      "s_waitcnt lgkmcnt(2)\n"
      "v_cvt_pk_bf16_f32 v175, v176, v177\n"
      "v_cvt_pk_bf16_f32 v177, v158, v159\n"
      "v_mfma_f32_16x16x32_bf16 v[108:111], v[186:189], v[210:213], v[68:71]\n"
      "v_alignbyte_b32 v208, v209, v208, v93\n"
      "v_cvt_pk_bf16_f32 v176, v156, v157\n"
      "v_alignbyte_b32 v209, v191, v209, v93\n"
      "v_mfma_f32_16x16x32_bf16 v[190:193], v[182:185], v[210:213], v[72:75]\n"
      "v_rcp_f32_e64 v146, v146\n"
      "v_mfma_f32_16x16x32_bf16 v[138:141], v[168:171], v[210:213], v[28:31]\n"
      "s_waitcnt lgkmcnt(0)\n"
      "v_mul_f32 v149, v146, v149\n"
      "v_mul_f32 v151, v146, v151\n"
      "v_mfma_f32_16x16x16_bf16 v[100:103], v[124:125], v[126:127], v[100:103]\n"
      "v_mul_f32 v105, v146, v105\n"
      "v_alignbyte_b32 v160, v160, v160, v93\n"
      "v_alignbyte_b32 v122, v123, v122, v93\n"
      "v_mfma_f32_16x16x16_bf16 v[108:111], v[166:167], v[126:127], v[108:111]\n"
      "v_alignbyte_b32 v123, v161, v123, v93\n"
      "v_mul_f32 v153, v146, v153\n"
      "v_mul_f32 v150, v146, v150\n"
      "v_mfma_f32_16x16x16_bf16 v[190:193], v[142:143], v[126:127], v[190:193]\n"
      "v_exp_f32 v220, v220\n"
      "v_mul_f32 v107, v146, v107\n"
      "v_mfma_f32_16x16x16_bf16 v[138:141], v[172:173], v[126:127], v[138:141]\n"
      "v_mul_f32 v104, v146, v104\n"
      "v_mul_f32 v165, v201, v201\n"
      "v_mul_f32 v106, v146, v106\n"
      "v_mfma_f32_16x16x16_bf16 v[100:103], v[222:223], v[128:129], v[100:103]\n"
      "v_exp_f32 v120, v120\n"
      "v_lshlrev_b32_e64 v8, 16, v8\n"
      "v_mfma_f32_16x16x16_bf16 v[108:111], v[222:223], v[208:209], v[108:111]\n"
      "v_mul_f32 v152, v146, v152\n"
      "v_exp_f32 v221, v221\n"
      "v_mfma_f32_16x16x16_bf16 v[190:193], v[222:223], v[122:123], v[190:193]\n"
      "v_cvt_pk_bf16_f32 v93, v220, v221\n"
      "v_fma_f32 v164, v200, v200, s52\n"
      "v_cvt_pk_bf16_f32 v104, v104, v105\n"
      "v_mfma_f32_16x16x16_bf16 v[138:141], v[222:223], v[160:161], v[138:141]\n"
      "ds_read_b128 v[218:221], v2 offset:3584\n"
      "v_mul_f32_e64 v148, v146, v148\n"
      "v_mfma_f32_16x16x16_bf16 v[100:103], v[194:195], v[128:129], v[100:103]\n"
      "v_cvt_pk_bf16_f32 v105, v106, v107\n"
      "v_cvt_pk_bf16_f32 v106, v148, v149\n"
      "v_cvt_pk_bf16_f32 v152, v152, v153\n"
      "v_mfma_f32_16x16x16_bf16 v[108:111], v[194:195], v[208:209], v[108:111]\n"
      "ds_read_b128 v[208:211], v2 offset:6656\n"
      "v_cvt_pk_bf16_f32 v94, v120, 0\n"
      "v_mfma_f32_16x16x16_bf16 v[190:193], v[194:195], v[122:123], v[190:193]\n"
      "ds_read_b128 v[126:129], v2 offset:512\n"
      "v_fmac_f32_e64 v164, v202, v202\n"
      "v_mfma_f32_16x16x16_bf16 v[138:141], v[194:195], v[160:161], v[138:141]\n"
      "v_cvt_pk_bf16_f32 v107, v150, v151\n"
      "v_max3_f32 v223, v100, v101, v102\n"
      "v_max3_f32 v223, v223, v110, v111\n"
      "v_fmac_f32_e64 v165, v203, v203\n"
      "v_max3_f32 v156, v103, v108, v109\n"
      "v_max3_f32 v156, v156, v190, v191\n"
      "v_pk_mul_f32 v[154:155], v[154:155], v[146:147] op_sel_hi:[1,0]\n"
      "v_max3_f32 v223, v223, v192, v193\n"
      "v_max3_f32 v223, v223, v156, v138\n"
      "v_mov_b32 v156, v223\n"
      "v_pk_fma_f32 v[164:165], v[112:113], v[112:113], v[164:165]\n"
      "v_pk_fma_f32 v[164:165], v[114:115], v[114:115], v[164:165]\n"
      "v_permlane16_swap_b32 v223, v156\n"
      "v_max_f32 v223, v223, v156\n"
      "v_mov_b32 v156, v223\n"
      "v_cvt_pk_bf16_f32 v153, v154, v155\n"
      "v_pk_fma_f32 v[164:165], v[204:205], v[204:205], v[164:165]\n"
      "v_permlane32_swap_b32_e64 v223, v156\n"
      "v_max_f32 v223, v223, v156\n"
      "s_waitcnt lgkmcnt(0)\n"
      "v_mfma_f32_16x16x32_bf16 v[156:159], a[216:219], v[126:129], 0\n"
      "ds_write_b128 v3, v[104:107] offset:18432\n"
      "v_pk_add_f32 v[102:103], v[102:103], v[222:223] op_sel:[0,1] neg_lo:[0,1] neg_hi:[0,1]\n"
      "v_pk_add_f32 v[100:101], v[100:101], v[222:223] op_sel:[0,1] neg_lo:[0,1] neg_hi:[0,1]\n"
      "v_pk_add_f32 v[190:191], v[190:191], v[222:223] op_sel:[0,1] neg_lo:[0,1] neg_hi:[0,1]\n"
      "v_pk_add_f32 v[108:109], v[108:109], v[222:223] op_sel:[0,1] neg_lo:[0,1] neg_hi:[0,1]\n"
      "v_pk_add_f32 v[192:193], v[192:193], v[222:223] op_sel:[0,1] neg_lo:[0,1] neg_hi:[0,1]\n"
      "v_pk_fma_f32 v[164:165], v[206:207], v[206:207], v[164:165]\n"
      "v_mfma_f32_16x16x32_bf16 v[156:159], a[220:223], v[218:221], v[156:159]\n"
      "v_exp_f32_e64 v108, v108\n"
      "v_mfma_f32_16x16x32_bf16 v[156:159], a[224:227], v[208:211], v[156:159]\n"
      "ds_write_b64 v4, v[152:153] offset:18432\n"
      "v_mfma_f32_16x16x32_bf16 v[146:149], a[240:243], v[126:129], 0\n"
      "v_sub_f32 v110, v110, v223\n"
      "v_add_f32 v164, v164, v165\n"
      "v_mfma_f32_16x16x32_bf16 v[146:149], a[244:247], v[218:221], v[146:149]\n"
      "v_exp_f32 v193, v193\n"
      "v_mfma_f32_16x16x32_bf16 v[146:149], a[248:251], v[208:211], v[146:149]\n"
      "v_exp_f32 v192, v192\n"
      "v_mfma_f32_16x16x32_bf16 v[160:163], v[232:235], v[126:129], 0\n"
      "ds_read_b128 v[104:107], v10 offset:1168\n"
      "v_sub_f32 v138, v138, v223\n"
      "v_mfma_f32_16x16x32_bf16 v[160:163], v[236:239], v[218:221], v[160:163]\n"
      "ds_read_b64 v[212:213], v10 offset:1184\n"
      "v_sub_f32 v111, v111, v223\n"
      "v_mfma_f32_16x16x32_bf16 v[160:163], v[240:243], v[208:211], v[160:163]\n"
      "v_exp_f32 v111, v111\n"
      "v_mfma_f32_16x16x32_bf16 v[218:221], v[32:35], v[174:177], 0\n"
      "v_exp_f32 v191, v191\n"
      "v_mfma_f32_16x16x32_bf16 v[218:221], v[32:35], v[92:95], v[218:221]\n"
      "v_exp_f32_e64 v102, v102\n"
      "v_mfma_f32_16x16x32_bf16 v[208:211], v[130:133], v[174:177], 0\n"
      "v_exp_f32 v100, v100\n"
      "v_mfma_f32_16x16x32_bf16 v[208:211], v[76:79], v[92:95], v[208:211]\n"
      "v_exp_f32 v110, v110\n"
      "v_mfma_f32_16x16x32_bf16 v[126:129], v[196:199], v[174:177], 0\n"
      "v_exp_f32 v101, v101\n"
      "v_mfma_f32_16x16x32_bf16 v[126:129], v[80:83], v[92:95], v[126:129]\n"
      "v_rcp_f32 v218, v218\n"
      "v_mfma_f32_16x16x32_bf16 v[220:223], v[214:217], v[174:177], 0\n"
      "s_waitcnt lgkmcnt(0)\n"
      "v_exp_f32 v138, v138\n"
      "v_mfma_f32_16x16x32_bf16 v[220:223], v[84:87], v[92:95], v[220:223]\n"
      "ds_read_b128 v[174:177], v2 offset:9728\n"
      "v_cvt_pk_bf16_f32 v89, v192, v193\n"
      "v_mfma_f32_16x16x32_bf16 v[152:155], v[104:107], v[178:181], v[30:33]\n"
      "v_exp_f32 v190, v190\n"
      "v_mfma_f32_16x16x32_bf16 v[178:181], v[104:107], v[186:189], v[30:33]\n"
      "v_cvt_pk_bf16_f32 v100, v100, v101\n"
      "v_mul_f32 v208, v218, v208\n"
      "v_mfma_f32_16x16x32_bf16 v[186:189], v[104:107], v[182:185], v[30:33]\n"
      "v_exp_f32 v103, v103\n"
      "v_mfma_f32_16x16x32_bf16 v[182:185], v[104:107], v[168:171], v[30:33]\n"
      "ds_read_b128 v[168:171], v2 offset:12800\n"
      "v_mul_f32 v129, v218, v129\n"
      "v_mfma_f32_16x16x16_bf16 v[152:155], v[212:213], v[124:125], v[152:155]\n"
      "ds_read_b128 v[104:107], v2 offset:15872\n"
      "v_cvt_pk_bf16_f32 v101, v102, v103\n"
      "v_mfma_f32_16x16x16_bf16 v[178:181], v[212:213], v[166:167], v[178:181]\n"
      "v_mul_f32 v211, v218, v211\n"
      "v_exp_f32 v109, v109\n"
      "v_permlane16_swap_b32_e64 v144, v164\n"
      "v_mul_f32_e64 v210, v218, v210\n"
      "v_pk_mul_f32 v[126:127], v[126:127], v[218:219] op_sel_hi:[1,0]\n"
      "v_mfma_f32_16x16x16_bf16 v[186:189], v[212:213], v[142:143], v[186:189]\n"
      "v_mul_f32_e64 v209, v218, v209\n"
      "v_mul_f32_e64 v128, v218, v128\n"
      "v_mul_f32_e64 v223, v218, v223\n"
      "v_mfma_f32_16x16x16_bf16 v[182:185], v[212:213], v[172:173], v[182:185]\n"
      "v_cndmask_b32 v152, v152, v178, s[58:59]\n"
      "v_cvt_pk_bf16_f32 v103, v110, v111\n"
      "s_waitcnt lgkmcnt(2)\n"
      "v_mfma_f32_16x16x32_bf16 v[156:159], a[228:231], v[174:177], v[156:159]\n"
      "s_waitcnt lgkmcnt(1)\n"
      "v_cvt_pk_bf16_f32 v208, v208, v209\n"
      "v_cndmask_b32 v152, v152, v186, s[60:61]\n"
      "v_mfma_f32_16x16x32_bf16 v[156:159], a[232:235], v[168:171], v[156:159]\n"
      "s_waitcnt lgkmcnt(0)\n"
      "v_cvt_pk_bf16_f32 v209, v210, v211\n"
      "v_cndmask_b32 v152, v152, v182, s[62:63]\n"
      "v_mfma_f32_16x16x32_bf16 v[156:159], a[236:239], v[104:107], v[156:159]\n"
      "v_fmac_f32 v152, s53, v8\n"
      "v_cvt_pk_bf16_f32 v90, v138, 0\n"
      "v_mfma_f32_16x16x32_bf16 v[146:149], a[252:255], v[174:177], v[146:149]\n"
      "v_cvt_pk_bf16_f32 v88, v190, v191\n"
      "v_max_f32_dpp v212, v152, v152 row_ror:8 row_mask:0xf bank_mask:0xf\n"
      "v_mfma_f32_16x16x32_bf16 v[146:149], v[224:227], v[168:171], v[146:149]\n"
      "s_nop 0\n"
      "v_max_f32_dpp v212, v212, v212 row_ror:4 row_mask:0xf bank_mask:0xf\n"
      "v_fma_f32 v190, v156, v156, s52\n"
      "v_mfma_f32_16x16x32_bf16 v[146:149], v[228:231], v[104:107], v[146:149]\n"
      "v_mul_f32 v191, v157, v157\n"
      "v_cvt_pk_bf16_f32 v210, v126, v127\n"
      "v_mfma_f32_16x16x32_bf16 v[160:163], v[244:247], v[174:177], v[160:163]\n"
      "v_cvt_pk_bf16_f32 v102, v108, v109\n"
      "v_max_f32_dpp v212, v212, v212 row_ror:2 row_mask:0xf bank_mask:0xf\n"
      "v_mfma_f32_16x16x32_bf16 v[160:163], v[248:251], v[168:171], v[160:163]\n"
      "v_cvt_pk_bf16_f32 v211, v128, v129\n"
      "v_max_f32_dpp v212, v212, v212 row_ror:1 row_mask:0xf bank_mask:0xf\n"
      "v_mfma_f32_16x16x32_bf16 v[160:163], v[252:255], v[104:107], v[160:163]\n"
      "ds_write_b128 v3, v[208:211] offset:18688\n"
      "v_pk_fma_f32 v[190:191], v[158:159], v[158:159], v[190:191]\n"
      "v_pk_mul_f32 v[220:221], v[220:221], v[218:219] op_sel_hi:[1,0]\n"
      "v_pk_fma_f32 v[190:191], v[146:147], v[146:147], v[190:191]\n"
      "v_mfma_f32_16x16x32_bf16 v[208:211], v[32:35], v[100:103], 0\n"
      "v_max_f32_dpp v212, v212, v212 row_bcast:15 row_mask:0xa bank_mask:0xf\n"
      "v_mul_f32_e64 v222, v218, v222\n"
      "v_mfma_f32_16x16x32_bf16 v[208:211], v[32:35], v[88:91], v[208:211]\n"
      "v_cvt_pk_bf16_f32 v220, v220, v221\n"
      "v_max_f32_dpp v212, v212, v212 row_bcast:31 row_mask:0xc bank_mask:0xf\n"
      "v_mfma_f32_16x16x32_bf16 v[168:171], v[130:133], v[100:103], 0\n"
      "v_readlane_b32 s72, v212, 63\n"
      "v_add_f32 v144, v144, v164\n"
      "v_mfma_f32_16x16x32_bf16 v[168:171], v[76:79], v[88:91], v[168:171]\n"
      "v_cvt_pk_bf16_f32 v221, v222, v223\n"
      "v_subrev_f32 v152, s72, v152\n"
      "v_mfma_f32_16x16x32_bf16 v[104:107], v[196:199], v[100:103], 0\n"
      "v_exp_f32 v152, v152\n"
      "v_rcp_f32 v208, v208\n"
      "v_cvt_pk_bf16_f32 v152, v152, v152\n"
      "v_pk_fma_f32 v[190:191], v[148:149], v[148:149], v[190:191]\n"
      "v_pk_fma_f32 v[190:191], v[160:161], v[160:161], v[190:191]\n"
      "v_pk_mul_f32 v[168:169], v[168:169], v[208:209] op_sel_hi:[1,0]\n"
      "v_pk_fma_f32 v[190:191], v[162:163], v[162:163], v[190:191]\n"
      "v_pk_mul_f32 v[170:171], v[170:171], v[208:209] op_sel_hi:[1,0]\n"
      "v_mfma_f32_16x16x32_bf16 v[104:107], v[80:83], v[88:91], v[104:107]\n"
      "ds_write_b16 v13, v152\n"
      "v_mfma_f32_16x16x32_bf16 v[210:213], v[214:217], v[100:103], 0\n"
      "ds_read_b128 v[174:177], v14 offset:64\n"
      "v_cvt_pk_bf16_f32 v168, v168, v169\n"
      "v_add_f32 v190, v190, v191\n"
      "v_mfma_f32_16x16x32_bf16 v[210:213], v[84:87], v[88:91], v[210:213]\n"
      "ds_write_b64 v4, v[220:221] offset:18688\n"
      "v_mov_b32 v164, v190\n"
      "v_cvt_pk_bf16_f32 v169, v170, v171\n"
      "v_pk_mul_f32 v[104:105], v[104:105], v[208:209] op_sel_hi:[1,0]\n"
      "v_pk_mul_f32 v[106:107], v[106:107], v[208:209] op_sel_hi:[1,0]\n"
      "v_cvt_pk_bf16_f32 v170, v104, v105\n"
      "v_cvt_pk_bf16_f32 v171, v106, v107\n"
      "s_add_u32 s43, s43, 1\n"
      "ds_write_b128 v3, v[168:171] offset:18944\n"
      "v_permlane16_swap_b32 v190, v164\n"
      "v_add_f32 v190, v190, v164\n"
      "v_pk_mul_f32 v[210:211], v[210:211], v[208:209] op_sel_hi:[1,0]\n"
      "v_pk_mul_f32 v[212:213], v[212:213], v[208:209] op_sel_hi:[1,0]\n"
      "v_permlane32_swap_b32 v144, v190\n"
      "v_add_f32 v144, v144, v190\n"
      "s_and_b32 s47, s47, -4\n"
      "ds_write_b32 v31, v144 offset:1024\n"
      "v_cvt_pk_bf16_f32 v210, v210, v211\n"
      "v_cvt_pk_bf16_f32 v211, v212, v213\n"
      "ds_read_b128 v[218:221], v14\n"
      "ds_write_b64 v4, v[210:211] offset:18944\n"
      "s_cmp_ge_u32 s95, 0x40000000\n"
      "s_cselect_b64 vcc, -1, s[56:57]\n"
      "v_add_u32 v11, 2336, v11\n"
      "s_waitcnt lgkmcnt(1)\n"
      "v_mfma_f32_16x16x32_bf16 v[208:211], v[218:221], v[32:35], 0\n"
      "s_add_u32 s42, s42, 1\n"
      "v_add_u32 v10, 2336, v10\n"
      "v_mfma_f32_16x16x32_bf16 v[208:211], v[174:177], v[32:35], v[208:211]\n"
      "s_waitcnt lgkmcnt(0)\n"
      "s_barrier\n"
      "s_cbranch_vccz L_sel0_%=\n"
      "L_disp0_%=:\n"
      "s_cmp_lt_u32 s95, 0x40000000\n"
      "s_cbranch_scc1 L_nofix_18_%=\n"
      "s_cmp_eq_u32 s95, -2\n"
      "s_cbranch_scc1 L_tail0_%=\n"
      "s_nop 2\n"
      "v_lshlrev_b32 v171, 3, v36\n"
      "s_add_u32 s72, s98, L_cold_%=-L_ipf0_%=\n"
      "s_addc_u32 s73, s99, 0\n"
      "s_mov_b32 s74, L_cend_%=-L_cold_%=\n"
      "s_mov_b32 s75, 0x20000\n"
      "s_mov_b32 m0, 122112\n"
      "s_nop 0\n"
      "buffer_load_dwordx4 v171, s[72:75], 0 offen  lds\n"
      "s_lshr_b32 s92, s45, 30\n"
      "s_or_b32 s92, s92, s66\n"
      "s_cbranch_scc0 L_nf_19_%=\n"
      "s_bitcmp1_b32 s45, 31\n"
      "s_cselect_b32 s75, 100270464, 0\n"
      "s_bitcmp1_b32 s45, 30\n"
      "s_cselect_b32 s76, 196224, 0\n"
      "s_and_b32 s74, s45, 0x3fffffff\n"
      "s_cmp_ge_u32 s45, 0x40000000\n"
      "s_cselect_b32 s74, s74, 0\n"
      "s_cselect_b32 s45, 0, s45\n"
      "s_sub_u32 s74, s74, s66\n"
      "s_add_u32 s66, s66, s74\n"
      "s_sub_u32 s75, s75, s67\n"
      "s_add_u32 s67, s67, s75\n"
      "s_sub_u32 s76, s76, s68\n"
      "s_add_u32 s68, s68, s76\n"
      "v_add_u32 v16, s74, v16\n"
      "v_add_u32 v17, s74, v17\n"
      "v_add_u32 v18, s74, v18\n"
      "v_add_u32 v19, s74, v19\n"
      "v_add_u32 v15, s74, v15\n"
      "s_cmp_eq_u32 s75, 0\n"
      "s_cbranch_scc1 L_ax_20_%=\n"
      "v_bfe_i32 v171, v27, 0, 1\n"
      "v_and_b32 v171, s75, v171\n"
      "v_sub_u32 v16, v16, v171\n"
      "v_bfe_i32 v171, v27, 2, 1\n"
      "v_and_b32 v171, s75, v171\n"
      "v_sub_u32 v17, v17, v171\n"
      "v_bfe_i32 v171, v27, 4, 1\n"
      "v_and_b32 v171, s75, v171\n"
      "v_sub_u32 v18, v18, v171\n"
      "v_bfe_i32 v171, v27, 6, 1\n"
      "v_and_b32 v171, s75, v171\n"
      "v_sub_u32 v19, v19, v171\n"
      "v_bfe_i32 v171, v27, 8, 1\n"
      "v_and_b32 v171, s75, v171\n"
      "v_sub_u32 v15, v15, v171\n"
      "L_ax_20_%=:\n"
      "s_cmp_eq_u32 s76, 0\n"
      "s_cbranch_scc1 L_ax_21_%=\n"
      "v_bfe_i32 v171, v27, 1, 1\n"
      "v_and_b32 v171, s76, v171\n"
      "v_sub_u32 v16, v16, v171\n"
      "v_bfe_i32 v171, v27, 3, 1\n"
      "v_and_b32 v171, s76, v171\n"
      "v_sub_u32 v17, v17, v171\n"
      "v_bfe_i32 v171, v27, 5, 1\n"
      "v_and_b32 v171, s76, v171\n"
      "v_sub_u32 v18, v18, v171\n"
      "v_bfe_i32 v171, v27, 7, 1\n"
      "v_and_b32 v171, s76, v171\n"
      "v_sub_u32 v19, v19, v171\n"
      "v_bfe_i32 v171, v27, 9, 1\n"
      "v_and_b32 v171, s76, v171\n"
      "v_sub_u32 v15, v15, v171\n"
      "L_ax_21_%=:\n"
      "s_or_b32 s48, s66, s69\n"
      "s_cmp_lg_u32 s48, 0\n"
      "s_cselect_b32 s48, 0x40000000, 0\n"
      "L_nf_19_%=:\n"
      "s_lshr_b32 s92, s46, 30\n"
      "s_or_b32 s92, s92, s69\n"
      "s_cbranch_scc0 L_nf_22_%=\n"
      "s_bitcmp1_b32 s46, 31\n"
      "s_cselect_b32 s75, 100270464, 0\n"
      "s_bitcmp1_b32 s46, 30\n"
      "s_cselect_b32 s76, 196224, 0\n"
      "s_and_b32 s74, s46, 0x3fffffff\n"
      "s_cmp_ge_u32 s46, 0x40000000\n"
      "s_cselect_b32 s74, s74, 0\n"
      "s_cselect_b32 s46, 0, s46\n"
      "s_sub_u32 s74, s74, s69\n"
      "s_add_u32 s69, s69, s74\n"
      "s_sub_u32 s75, s75, s70\n"
      "s_add_u32 s70, s70, s75\n"
      "s_sub_u32 s76, s76, s71\n"
      "s_add_u32 s71, s71, s76\n"
      "v_add_u32 v20, s74, v20\n"
      "v_add_u32 v21, s74, v21\n"
      "v_add_u32 v22, s74, v22\n"
      "v_add_u32 v23, s74, v23\n"
      "v_add_u32 v25, s74, v25\n"
      "s_cmp_eq_u32 s75, 0\n"
      "s_cbranch_scc1 L_ax_23_%=\n"
      "v_bfe_i32 v171, v27, 10, 1\n"
      "v_and_b32 v171, s75, v171\n"
      "v_sub_u32 v20, v20, v171\n"
      "v_bfe_i32 v171, v27, 12, 1\n"
      "v_and_b32 v171, s75, v171\n"
      "v_sub_u32 v21, v21, v171\n"
      "v_bfe_i32 v171, v27, 14, 1\n"
      "v_and_b32 v171, s75, v171\n"
      "v_sub_u32 v22, v22, v171\n"
      "v_bfe_i32 v171, v27, 16, 1\n"
      "v_and_b32 v171, s75, v171\n"
      "v_sub_u32 v23, v23, v171\n"
      "v_bfe_i32 v171, v27, 18, 1\n"
      "v_and_b32 v171, s75, v171\n"
      "v_sub_u32 v25, v25, v171\n"
      "L_ax_23_%=:\n"
      "s_cmp_eq_u32 s76, 0\n"
      "s_cbranch_scc1 L_ax_24_%=\n"
      "v_bfe_i32 v171, v27, 11, 1\n"
      "v_and_b32 v171, s76, v171\n"
      "v_sub_u32 v20, v20, v171\n"
      "v_bfe_i32 v171, v27, 13, 1\n"
      "v_and_b32 v171, s76, v171\n"
      "v_sub_u32 v21, v21, v171\n"
      "v_bfe_i32 v171, v27, 15, 1\n"
      "v_and_b32 v171, s76, v171\n"
      "v_sub_u32 v22, v22, v171\n"
      "v_bfe_i32 v171, v27, 17, 1\n"
      "v_and_b32 v171, s76, v171\n"
      "v_sub_u32 v23, v23, v171\n"
      "v_bfe_i32 v171, v27, 19, 1\n"
      "v_and_b32 v171, s76, v171\n"
      "v_sub_u32 v25, v25, v171\n"
      "L_ax_24_%=:\n"
      "s_or_b32 s48, s66, s69\n"
      "s_cmp_lg_u32 s48, 0\n"
      "s_cselect_b32 s48, 0x40000000, 0\n"
      "L_nf_22_%=:\n"
      "s_sub_u32 s92, s42, 1\n"
      "s_cmp_gt_u32 s92, s44\n"
      "s_cbranch_scc1 L_tail0_%=\n"
      "L_nofix_18_%=:\n"
      "s_cmp_lg_u64 s[56:57], 0\n"
      "s_cbranch_scc1 L_slow0_%=\n"
      "L_sel0_%=:\n"
      "s_cmp_lg_u64 s[54:55], 0\n"
      "s_cbranch_scc1 L_max0_%=\n"
      "s_branch L_loop0_%=\n"
      "L_slow0_%=:\n"
      "s_nop 4\n"
      "v_mfma_f32_16x16x32_bf16 v[168:171], v[218:221], v[130:133], 0\n"
      "ds_read_b128 v[138:141], v0\n"
      "s_mov_b32 m0, 110592\n"
      "v_mfma_f32_16x16x32_bf16 v[168:171], v[174:177], v[76:79], v[168:171]\n"
      "ds_read_b128 v[190:193], v9 offset:1024\n"
      "v_subrev_u32 v76, 115456, v9\n"
      "v_mfma_f32_16x16x32_bf16 v[150:153], v[218:221], v[196:199], 0\n"
      "s_add_u32 s72, s42, 1\n"
      "v_readlane_b32 s77, v38, s72\n"
      "v_subrev_u32 v132, 115456, v9\n"
      "v_mfma_f32_16x16x32_bf16 v[150:153], v[174:177], v[80:83], v[150:153]\n"
      "ds_read_b128 v[108:111], v0 offset:6144\n"
      "v_subrev_u32 v8, 115456, v9\n"
      "v_mfma_f32_16x16x32_bf16 v[194:197], v[218:221], v[214:217], 0\n"
      "ds_read_b128 v[164:167], v1\n"
      "s_and_b32 s73, s77, -16\n"
      "v_mfma_f32_16x16x32_bf16 v[194:197], v[174:177], v[84:87], v[194:197]\n"
      "buffer_load_dwordx4 v132, s[28:31], s73 offen  lds\n"
      "s_waitcnt lgkmcnt(3)\n"
      "v_mfma_f32_16x16x32_bf16 v[152:155], a[0:3], v[138:141], 0\n"
      "s_add_u32 s73, s73, 1024\n"
      "ds_read_b128 v[122:125], v0 offset:4096\n"
      "v_mfma_f32_16x16x32_bf16 v[210:213], a[24:27], v[138:141], 0\n"
      "s_waitcnt lgkmcnt(3)\n"
      "v_add_f32 v190, v190, v191\n"
      "v_add_f32 v192, v192, v193\n"
      "v_mfma_f32_16x16x32_bf16 v[174:177], a[48:51], v[138:141], 0\n"
      "ds_read_b128 v[170:173], v1 offset:12288\n"
      "v_add_f32 v190, v190, v192\n"
      "v_mfma_f32_16x16x32_bf16 v[104:107], a[72:75], v[138:141], 0\n"
      "s_mov_b32 m0, 111616\n"
      "v_rsq_f32 v190, v190\n"
      "v_mfma_f32_16x16x32_bf16 v[126:129], a[96:99], v[138:141], 0\n"
      "s_waitcnt lgkmcnt(2)\n"
      "v_mov_b32 v93, 0\n"
      "v_mul_f32 v190, 0x415db3d7, v190\n"
      "v_mfma_f32_16x16x32_bf16 v[178:181], a[120:123], v[138:141], 0\n"
      "ds_read_b128 v[186:189], v1 offset:6144\n"
      "v_mov_b32_e64 v192, v190\n"
      "v_mfma_f32_16x16x32_bf16 v[152:155], a[4:7], v[164:167], v[152:155]\n"
      "buffer_load_dwordx4 v132, s[28:31], s73 offen  lds\n"
      "v_mfma_f32_16x16x32_bf16 v[152:155], a[8:11], v[108:111], v[152:155]\n"
      "v_permlane32_swap_b32 v190, v192\n"
      "v_mfma_f32_16x16x32_bf16 v[210:213], a[28:31], v[164:167], v[210:213]\n"
      "v_mov_b32 v191, v190\n"
      "v_readlane_b32 s84, v37, s43\n"
      "v_mfma_f32_16x16x32_bf16 v[210:213], a[32:35], v[108:111], v[210:213]\n"
      "v_permlane16_swap_b32 v190, v191\n"
      "v_mfma_f32_16x16x32_bf16 v[174:177], a[52:55], v[164:167], v[174:177]\n"
      "s_add_u32 s73, s73, 1024\n"
      "v_mul_f32 v200, v191, v200\n"
      "v_mul_f32 v201, v191, v201\n"
      "v_mfma_f32_16x16x32_bf16 v[174:177], a[56:59], v[108:111], v[174:177]\n"
      "ds_read_b128 v[182:185], v0 offset:12288\n"
      "v_mul_f32 v98, v190, v98\n"
      "v_mfma_f32_16x16x32_bf16 v[104:107], a[76:79], v[164:167], v[104:107]\n"
      "s_mov_b32 m0, 112640\n"
      "v_bfe_u32 v78, v76, 4, 4\n"
      "v_mul_f32 v112, v191, v112\n"
      "v_mfma_f32_16x16x32_bf16 v[104:107], a[80:83], v[108:111], v[104:107]\n"
      "v_mul_f32 v203, v191, v203\n"
      "v_mul_f32_e64 v157, v192, v157\n"
      "v_mfma_f32_16x16x32_bf16 v[126:129], a[100:103], v[164:167], v[126:129]\n"
      "buffer_load_dwordx4 v132, s[28:31], s73 offen  lds\n"
      "v_mfma_f32_16x16x32_bf16 v[126:129], a[104:107], v[108:111], v[126:129]\n"
      "v_mov_b32_dpp v208, v208 quad_perm:[0,0,0,0] row_mask:0xf bank_mask:0xf\n"
      "v_mul_f32 v158, v192, v158\n"
      "v_mfma_f32_16x16x32_bf16 v[178:181], a[124:127], v[164:167], v[178:181]\n"
      "s_mov_b32 m0, 113664\n"
      "v_mul_f32 v118, v190, v118\n"
      "v_mul_f32 v160, v192, v160\n"
      "v_mfma_f32_16x16x32_bf16 v[178:181], a[128:131], v[108:111], v[178:181]\n"
      "s_waitcnt lgkmcnt(0)\n"
      "v_mul_f32 v115, v191, v115\n"
      "v_mul_f32 v202, v191, v202\n"
      "v_mfma_f32_16x16x32_bf16 v[152:155], a[12:15], v[186:189], v[152:155]\n"
      "s_add_u32 s73, s73, 1024\n"
      "v_mul_f32_e64 v114, v191, v114\n"
      "v_cvt_pk_bf16_f32 v200, v200, v201\n"
      "v_mfma_f32_16x16x32_bf16 v[152:155], a[16:19], v[182:185], v[152:155]\n"
      "buffer_load_dwordx4 v132, s[28:31], s73 offen  lds\n"
      "v_mfma_f32_16x16x32_bf16 v[152:155], a[20:23], v[170:173], v[152:155]\n"
      "s_add_u32 s73, s73, 1024\n"
      "v_mul_f32_e64 v99, v190, v99\n"
      "v_mul_f32_e64 v205, v191, v205\n"
      "v_mfma_f32_16x16x32_bf16 v[210:213], a[36:39], v[186:189], v[210:213]\n"
      "s_mov_b32 m0, 114688\n"
      "v_mul_f32_e64 v137, v190, v137\n"
      "v_mul_f32 v119, v190, v119\n"
      "v_mfma_f32_16x16x32_bf16 v[210:213], a[40:43], v[182:185], v[210:213]\n"
      "s_mov_b32 exec_hi, 0xffff\n"
      "buffer_load_dwordx4 v132, s[28:31], s73 offen  lds\n"
      "s_mov_b32 exec_hi, -1\n"
      "v_pk_mul_f32 v[96:97], v[96:97], v[190:191] op_sel_hi:[1,0]\n"
      "v_pk_mul_f32 v[116:117], v[116:117], v[190:191] op_sel_hi:[1,0]\n"
      "v_mfma_f32_16x16x32_bf16 v[210:213], a[44:47], v[170:173], v[210:213]\n"
      "v_mul_f32_e64 v147, v192, v147\n"
      "v_cvt_pk_bf16_f32 v96, v96, v97\n"
      "v_mfma_f32_16x16x32_bf16 v[174:177], a[60:63], v[186:189], v[174:177]\n"
      "ds_read_b128 v[100:103], v1 offset:2048\n"
      "v_mul_f32_e64 v159, v192, v159\n"
      "v_mfma_f32_16x16x32_bf16 v[174:177], a[64:67], v[182:185], v[174:177]\n"
      "s_add_u32 m0, s64, 18432\n"
      "v_cvt_pk_bf16_f32 v201, v202, v203\n"
      "v_cvt_pk_bf16_f32 v97, v98, v99\n"
      "v_mfma_f32_16x16x32_bf16 v[174:177], a[68:71], v[170:173], v[174:177]\n"
      "buffer_load_dwordx4 v16, s[16:19], s45 offen  lds\n"
      "v_mfma_f32_16x16x32_bf16 v[104:107], a[84:87], v[186:189], v[104:107]\n"
      "s_add_u32 m0, s64, 22528\n"
      "v_mul_f32 v149, v192, v149\n"
      "v_mul_f32 v204, v191, v204\n"
      "v_mfma_f32_16x16x32_bf16 v[104:107], a[88:91], v[182:185], v[104:107]\n"
      "v_cvt_pk_bf16_f32 v99, v118, v119\n"
      "v_cvt_pk_bf16_f32 v203, v114, v115\n"
      "v_mfma_f32_16x16x32_bf16 v[104:107], a[92:95], v[170:173], v[104:107]\n"
      "ds_read_b128 v[118:121], v1 offset:8192\n"
      "v_mul_f32_e64 v113, v191, v113\n"
      "v_mfma_f32_16x16x32_bf16 v[126:129], a[108:111], v[186:189], v[126:129]\n"
      "v_fma_f32 v169, v152, v152, s51\n"
      "v_cvt_pk_bf16_f32 v98, v116, v117\n"
      "v_mfma_f32_16x16x32_bf16 v[126:129], a[112:115], v[182:185], v[126:129]\n"
      "ds_read_b128 v[220:223], v1 offset:14336\n"
      "v_mul_f32_e64 v162, v192, v162\n"
      "v_mfma_f32_16x16x32_bf16 v[126:129], a[116:119], v[170:173], v[126:129]\n"
      "ds_write_b128 v5, v[96:99]\n"
      "v_mfma_f32_16x16x32_bf16 v[178:181], a[132:135], v[186:189], v[178:181]\n"
      "buffer_load_dwordx4 v17, s[16:19], s45 offen  lds\n"
      "v_mfma_f32_16x16x32_bf16 v[178:181], a[136:139], v[182:185], v[178:181]\n"
      "s_add_u32 m0, s64, 26624\n"
      "v_fma_f32 v209, v104, v104, s51\n"
      "v_mul_f32 v146, v192, v146\n"
      "v_mfma_f32_16x16x32_bf16 v[178:181], a[140:143], v[170:173], v[178:181]\n"
      "ds_read_b128 v[114:117], v0 offset:2048\n"
      "v_fmac_f32 v209, v105, v105\n"
      "v_mfma_f32_16x16x32_bf16 v[130:133], v[138:141], a[144:147], 0\n"
      "ds_read_b128 v[142:145], v0 offset:14336\n"
      "v_mul_f32_e64 v156, v192, v156\n"
      "v_mfma_f32_16x16x32_bf16 v[130:133], v[164:167], a[148:151], v[130:133]\n"
      "v_cvt_pk_bf16_f32 v204, v204, v205\n"
      "v_cvt_pk_bf16_f32 v156, v156, v157\n"
      "v_mfma_f32_16x16x32_bf16 v[130:133], v[108:111], a[152:155], v[130:133]\n"
      "buffer_load_dwordx4 v18, s[16:19], s45 offen  lds\n"
      "v_mfma_f32_16x16x32_bf16 v[130:133], v[186:189], a[156:159], v[130:133]\n"
      "v_mul_f32 v134, v190, v134\n"
      "v_cvt_pk_bf16_f32 v157, v158, v159\n"
      "v_mfma_f32_16x16x32_bf16 v[130:133], v[182:185], a[160:163], v[130:133]\n"
      "s_add_u32 m0, s64, 30720\n"
      "v_mul_f32 v135, v190, v135\n"
      "v_cvt_pk_bf16_f32 v202, v112, v113\n"
      "v_mfma_f32_16x16x32_bf16 v[130:133], v[170:173], a[164:167], v[130:133]\n"
      "ds_write_b128 v5, v[200:203] offset:6144\n"
      "v_mfma_f32_16x16x32_bf16 v[196:199], v[138:141], a[168:171], 0\n"
      "buffer_load_dwordx4 v19, s[16:19], s45 offen  lds\n"
      "v_mfma_f32_16x16x32_bf16 v[196:199], v[164:167], a[172:175], v[196:199]\n"
      "v_rcp_f32_e64 v208, v208\n"
      "v_mfma_f32_16x16x32_bf16 v[196:199], v[108:111], a[176:179], v[196:199]\n"
      "ds_read_b128 v[200:203], v0 offset:8192\n"
      "v_cvt_pk_bf16_f32 v134, v134, v135\n"
      "v_mfma_f32_16x16x32_bf16 v[196:199], v[186:189], a[180:183], v[196:199]\n"
      "v_fmac_f32 v209, v106, v106\n"
      "v_mul_f32 v148, v192, v148\n"
      "v_mfma_f32_16x16x32_bf16 v[196:199], v[182:185], a[184:187], v[196:199]\n"
      "s_add_u32 m0, s65, 34816\n"
      "v_fmac_f32_e64 v209, v107, v107\n"
      "v_cvt_pk_bf16_f32 v158, v146, v147\n"
      "v_mfma_f32_16x16x32_bf16 v[196:199], v[170:173], a[188:191], v[196:199]\n"
      "buffer_load_dwordx4 v15, s[16:19], s45 offen  lds\n"
      "v_mfma_f32_16x16x32_bf16 v[214:217], v[138:141], a[192:195], 0\n"
      "v_cndmask_b32 v168, v168, v150, s[58:59]\n"
      "v_fmac_f32 v169, v153, v153\n"
      "v_mfma_f32_16x16x32_bf16 v[214:217], v[164:167], a[196:199], v[214:217]\n"
      "v_cvt_pk_bf16_f32 v159, v148, v149\n"
      "v_fmac_f32 v169, v154, v154\n"
      "v_mfma_f32_16x16x32_bf16 v[214:217], v[108:111], a[200:203], v[214:217]\n"
      "v_cvt_pk_bf16_f32 v130, v130, v131\n"
      "v_fmac_f32 v169, v155, v155\n"
      "v_mfma_f32_16x16x32_bf16 v[214:217], v[186:189], a[204:207], v[214:217]\n"
      "v_fmac_f32 v169, v210, v210\n"
      "v_fmac_f32_e64 v169, v211, v211\n"
      "v_mfma_f32_16x16x32_bf16 v[214:217], v[182:185], a[208:211], v[214:217]\n"
      "buffer_load_dwordx4 v[108:111], v36, s[28:31], s47 offen\n"
      "v_mfma_f32_16x16x32_bf16 v[214:217], v[170:173], a[212:215], v[214:217]\n"
      "s_waitcnt lgkmcnt(0)\n"
      "v_fmac_f32 v169, v212, v212\n"
      "v_fmac_f32 v209, v126, v126\n"
      "v_mfma_f32_16x16x32_bf16 v[138:141], a[0:3], v[114:117], 0\n"
      "ds_write_b128 v5, v[156:159] offset:12288\n"
      "v_mfma_f32_16x16x32_bf16 v[138:141], a[4:7], v[100:103], v[138:141]\n"
      "v_mul_f32 v163, v192, v163\n"
      "v_mul_f32 v161, v192, v161\n"
      "v_mfma_f32_16x16x32_bf16 v[138:141], a[8:11], v[200:203], v[138:141]\n"
      "buffer_load_dword v82, v29, s[28:31], s47 offen\n"
      "v_mfma_f32_16x16x32_bf16 v[138:141], a[12:15], v[118:121], v[138:141]\n"
      "v_fmac_f32 v209, v127, v127\n"
      "v_fmac_f32 v209, v128, v128\n"
      "v_mfma_f32_16x16x32_bf16 v[138:141], a[16:19], v[142:145], v[138:141]\n"
      "ds_read_b128 v[156:159], v9 offset:5632\n"
      "v_fmac_f32 v209, v129, v129\n"
      "v_mfma_f32_16x16x32_bf16 v[138:141], a[20:23], v[220:223], v[138:141]\n"
      "ds_read_b128 v[96:99], v1 offset:4096\n"
      "v_fmac_f32_e64 v209, v178, v178\n"
      "v_mfma_f32_16x16x32_bf16 v[186:189], a[24:27], v[114:117], 0\n"
      "v_readlane_b32 s85, v37, s42\n"
      "v_cvt_pk_bf16_f32 v196, v196, v197\n"
      "v_mfma_f32_16x16x32_bf16 v[186:189], a[28:31], v[100:103], v[186:189]\n"
      "s_and_b32 s77, s77, 15\n"
      "v_cvt_pk_bf16_f32 v214, v214, v215\n"
      "v_fmac_f32 v169, v213, v213\n"
      "v_mfma_f32_16x16x32_bf16 v[186:189], a[32:35], v[200:203], v[186:189]\n"
      "v_fmac_f32 v169, v174, v174\n"
      "v_fmac_f32 v169, v175, v175\n"
      "v_mfma_f32_16x16x32_bf16 v[186:189], a[36:39], v[118:121], v[186:189]\n"
      "v_fmac_f32_e64 v209, v179, v179\n"
      "v_fma_f32 v81, v138, v138, s51\n"
      "v_mfma_f32_16x16x32_bf16 v[186:189], a[40:43], v[142:145], v[186:189]\n"
      "v_fmac_f32 v169, v176, v176\n"
      "v_fmac_f32 v169, v177, v177\n"
      "v_mfma_f32_16x16x32_bf16 v[186:189], a[44:47], v[220:223], v[186:189]\n"
      "s_waitcnt lgkmcnt(0)\n"
      "v_mul_f32 v136, v190, v136\n"
      "v_cvt_pk_bf16_f32 v131, v132, v133\n"
      "v_mfma_f32_16x16x32_bf16 v[148:151], a[48:51], v[114:117], 0\n"
      "v_or3_b32 v156, v156, v157, v158\n"
      "v_fmac_f32 v209, v180, v180\n"
      "v_mfma_f32_16x16x32_bf16 v[148:151], a[52:55], v[100:103], v[148:151]\n"
      "v_fmac_f32 v209, v181, v181\n"
      "v_cndmask_b32 v168, v168, v194, s[60:61]\n"
      "v_mfma_f32_16x16x32_bf16 v[148:151], a[56:59], v[200:203], v[148:151]\n"
      "v_permlane16_swap_b32_e64 v169, v209\n"
      "v_mfma_f32_16x16x32_bf16 v[148:151], a[60:63], v[118:121], v[148:151]\n"
      "v_mul_u32_u24 v88, 98, v78\n"
      "v_cvt_pk_bf16_f32 v160, v160, v161\n"
      "v_mfma_f32_16x16x32_bf16 v[148:151], a[64:67], v[142:145], v[148:151]\n"
      "v_add_f32 v169, v169, v209\n"
      "v_lshrrev_b32 v76, 8, v76\n"
      "v_mfma_f32_16x16x32_bf16 v[148:151], a[68:71], v[220:223], v[148:151]\n"
      "v_fmac_f32 v81, v139, v139\n"
      "v_mov_b32 v209, v169\n"
      "v_mfma_f32_16x16x32_bf16 v[170:173], a[72:75], v[114:117], 0\n"
      "s_nop 0\n"
      "v_permlane32_swap_b32 v169, v209\n"
      "v_mfma_f32_16x16x32_bf16 v[170:173], a[76:79], v[100:103], v[170:173]\n"
      "v_cvt_pk_bf16_f32 v161, v162, v163\n"
      "v_fmac_f32 v81, v140, v140\n"
      "v_mfma_f32_16x16x32_bf16 v[170:173], a[80:83], v[200:203], v[170:173]\n"
      "v_lshl_add_u32 v88, v76, 3, v88\n"
      "v_fmac_f32 v81, v141, v141\n"
      "v_mfma_f32_16x16x32_bf16 v[170:173], a[84:87], v[118:121], v[170:173]\n"
      "v_add_f32 v169, v169, v209\n"
      "v_cvt_pk_bf16_f32 v197, v198, v199\n"
      "v_mfma_f32_16x16x32_bf16 v[170:173], a[88:91], v[142:145], v[170:173]\n"
      "s_or_b32 s95, s84, s85\n"
      "v_lshlrev_b32 v76, 2, v76\n"
      "v_fmac_f32 v81, v186, v186\n"
      "v_mfma_f32_16x16x32_bf16 v[170:173], a[92:95], v[220:223], v[170:173]\n"
      "v_mul_f32 v206, v191, v206\n"
      "v_fmac_f32 v81, v187, v187\n"
      "v_mfma_f32_16x16x32_bf16 v[182:185], a[96:99], v[114:117], 0\n"
      "ds_write_b64 v6, v[160:161] offset:12288\n"
      "v_mfma_f32_16x16x32_bf16 v[182:185], a[100:103], v[100:103], v[182:185]\n"
      "v_rsq_f32 v169, v169\n"
      "v_mfma_f32_16x16x32_bf16 v[182:185], a[104:107], v[200:203], v[182:185]\n"
      "v_mul_f32 v207, v191, v207\n"
      "v_mul_f32 v168, v208, v168\n"
      "v_mfma_f32_16x16x32_bf16 v[182:185], a[108:111], v[118:121], v[182:185]\n"
      "v_mov_b32 v209, v169\n"
      "v_fma_f32 v158, v170, v170, s51\n"
      "v_mfma_f32_16x16x32_bf16 v[182:185], a[112:115], v[142:145], v[182:185]\n"
      "v_cvt_pk_bf16_f32 v135, v136, v137\n"
      "v_fmac_f32 v158, v171, v171\n"
      "v_mfma_f32_16x16x32_bf16 v[182:185], a[116:119], v[220:223], v[182:185]\n"
      "v_permlane16_swap_b32 v169, v209\n"
      "v_mfma_f32_16x16x32_bf16 v[164:167], a[120:123], v[114:117], 0\n"
      "v_cvt_pk_bf16_f32 v168, v168, v168\n"
      "v_mul_f32 v169, s50, v169\n"
      "v_mfma_f32_16x16x32_bf16 v[164:167], a[124:127], v[100:103], v[164:167]\n"
      "v_cvt_pk_bf16_f32 v215, v216, v217\n"
      "v_mul_f32 v174, v169, v174\n"
      "v_mfma_f32_16x16x32_bf16 v[164:167], a[128:131], v[200:203], v[164:167]\n"
      "v_fmac_f32 v158, v172, v172\n"
      "v_mul_f32 v211, v169, v211\n"
      "v_mfma_f32_16x16x32_bf16 v[164:167], a[132:135], v[118:121], v[164:167]\n"
      "v_lshrrev_b32 v8, 3, v8\n"
      "v_fmac_f32 v158, v173, v173\n"
      "v_mfma_f32_16x16x32_bf16 v[164:167], a[136:139], v[142:145], v[164:167]\n"
      "v_cvt_pk_bf16_f32 v205, v206, v207\n"
      "v_fmac_f32 v81, v188, v188\n"
      "v_mfma_f32_16x16x32_bf16 v[164:167], a[140:143], v[220:223], v[164:167]\n"
      "v_mul_f32 v152, v169, v152\n"
      "v_mul_f32_e64 v105, v209, v105\n"
      "v_mfma_f32_16x16x32_bf16 v[216:219], v[114:117], a[144:147], 0\n"
      "ds_write_b16 v12, v168 offset:16\n"
      "v_mfma_f32_16x16x32_bf16 v[216:219], v[100:103], a[148:151], v[216:219]\n"
      "v_mul_f32 v107, v209, v107\n"
      "v_fmac_f32 v158, v182, v182\n"
      "v_mfma_f32_16x16x32_bf16 v[216:219], v[200:203], a[152:155], v[216:219]\n"
      "ds_write_b64 v6, v[204:205] offset:6144\n"
      "v_mfma_f32_16x16x32_bf16 v[216:219], v[118:121], a[156:159], v[216:219]\n"
      "v_mul_f32 v153, v169, v153\n"
      "v_mul_f32 v180, v209, v180\n"
      "v_mfma_f32_16x16x32_bf16 v[216:219], v[142:145], a[160:163], v[216:219]\n"
      "v_mul_f32 v178, v209, v178\n"
      "v_fmac_f32 v158, v183, v183\n"
      "v_mfma_f32_16x16x32_bf16 v[216:219], v[220:223], a[164:167], v[216:219]\n"
      "s_or_b32 s95, s95, s48\n"
      "v_fmac_f32 v81, v189, v189\n"
      "v_fmac_f32 v81, v148, v148\n"
      "v_mfma_f32_16x16x32_bf16 v[204:207], v[114:117], a[168:171], 0\n"
      "ds_write_b64 v6, v[134:135]\n"
      "v_mfma_f32_16x16x32_bf16 v[204:207], v[100:103], a[172:175], v[204:207]\n"
      "v_mul_f32 v104, v209, v104\n"
      "v_sub_u32 v78, v78, v76\n"
      "v_mfma_f32_16x16x32_bf16 v[204:207], v[200:203], a[176:179], v[204:207]\n"
      "v_fmac_f32 v81, v149, v149\n"
      "v_fmac_f32 v158, v184, v184\n"
      "v_mfma_f32_16x16x32_bf16 v[204:207], v[118:121], a[180:183], v[204:207]\n"
      "v_mul_f32 v210, v169, v210\n"
      "v_lshlrev_b32 v78, 4, v78\n"
      "v_mfma_f32_16x16x32_bf16 v[204:207], v[142:145], a[184:187], v[204:207]\n"
      "ds_read_b128 v[192:195], v0 offset:10240\n"
      "v_mul_f32 v154, v169, v154\n"
      "v_mfma_f32_16x16x32_bf16 v[204:207], v[220:223], a[188:191], v[204:207]\n"
      "v_mul_f32 v181, v209, v181\n"
      "v_fmac_f32 v158, v185, v185\n"
      "v_mfma_f32_16x16x32_bf16 v[134:137], v[114:117], a[192:195], 0\n"
      "ds_read_b128 v[112:115], v1 offset:16384\n"
      "v_fmac_f32_e64 v158, v164, v164\n"
      "v_mfma_f32_16x16x32_bf16 v[134:137], v[100:103], a[196:199], v[134:137]\n"
      "v_mul_f32 v179, v209, v179\n"
      "v_fmac_f32 v158, v165, v165\n"
      "v_mfma_f32_16x16x32_bf16 v[134:137], v[200:203], a[200:203], v[134:137]\n"
      "ds_read_b128 v[200:203], v1 offset:10240\n"
      "v_mul_f32_e64 v177, v169, v177\n"
      "v_mfma_f32_16x16x32_bf16 v[134:137], v[118:121], a[204:207], v[134:137]\n"
      "ds_read_b128 v[116:119], v0 offset:16384\n"
      "v_cvt_pk_bf16_f32 v152, v152, v153\n"
      "v_mfma_f32_16x16x32_bf16 v[134:137], v[142:145], a[208:211], v[134:137]\n"
      "v_fmac_f32 v81, v150, v150\n"
      "v_fmac_f32 v158, v166, v166\n"
      "v_mfma_f32_16x16x32_bf16 v[134:137], v[220:223], a[212:215], v[134:137]\n"
      "v_mul_f32 v106, v209, v106\n"
      "v_fmac_f32 v81, v151, v151\n"
      "v_mfma_f32_16x16x32_bf16 v[144:147], a[0:3], v[122:125], 0\n"
      "v_fmac_f32 v158, v167, v167\n"
      "v_cvt_pk_bf16_f32 v198, v204, v205\n"
      "v_mfma_f32_16x16x32_bf16 v[144:147], a[4:7], v[96:99], v[144:147]\n"
      "s_waitcnt lgkmcnt(2)\n"
      "v_permlane16_swap_b32 v81, v158\n"
      "v_mfma_f32_16x16x32_bf16 v[144:147], a[8:11], v[192:195], v[144:147]\n"
      "s_waitcnt lgkmcnt(1)\n"
      "v_bitop3_b32 v156, v156, v159, s33 bitop3:0xa8\n"
      "v_mul_f32_e64 v155, v169, v155\n"
      "v_mfma_f32_16x16x32_bf16 v[144:147], a[12:15], v[200:203], v[144:147]\n"
      "s_waitcnt lgkmcnt(0)\n"
      "v_add_f32 v81, v81, v158\n"
      "v_mov_b32 v158, v81\n"
      "v_mfma_f32_16x16x32_bf16 v[144:147], a[16:19], v[116:119], v[144:147]\n"
      "s_nop 0\n"
      "v_permlane32_swap_b32 v81, v158\n"
      "v_mfma_f32_16x16x32_bf16 v[144:147], a[20:23], v[112:115], v[144:147]\n"
      "v_add_f32 v81, v81, v158\n"
      "v_cvt_pk_bf16_f32 v153, v154, v155\n"
      "v_mfma_f32_16x16x32_bf16 v[220:223], a[24:27], v[122:125], 0\n"
      "v_rsq_f32 v81, v81\n"
      "v_mfma_f32_16x16x32_bf16 v[220:223], a[28:31], v[96:99], v[220:223]\n"
      "v_mov_b32 v158, v81\n"
      "v_cvt_pk_bf16_f32 v154, v210, v211\n"
      "v_mfma_f32_16x16x32_bf16 v[220:223], a[32:35], v[192:195], v[220:223]\n"
      "s_add_u32 s73, s73, 1024\n"
      "v_fma_f32 v204, v144, v144, s51\n"
      "v_add_u32 v88, s77, v88\n"
      "v_mfma_f32_16x16x32_bf16 v[220:223], a[36:39], v[200:203], v[220:223]\n"
      "v_permlane16_swap_b32 v81, v158\n"
      "v_mfma_f32_16x16x32_bf16 v[220:223], a[40:43], v[116:119], v[220:223]\n"
      "v_mul_f32_e64 v81, s50, v81\n"
      "v_cvt_pk_bf16_f32 v178, v178, v179\n"
      "v_mfma_f32_16x16x32_bf16 v[220:223], a[44:47], v[112:115], v[220:223]\n"
      "v_cvt_pk_bf16_f32 v104, v104, v105\n"
      "v_mul_f32 v175, v169, v175\n"
      "v_mfma_f32_16x16x32_bf16 v[160:163], a[48:51], v[122:125], 0\n"
      "v_fmac_f32 v204, v145, v145\n"
      "v_mul_f32_e64 v189, v81, v189\n"
      "v_mfma_f32_16x16x32_bf16 v[160:163], a[52:55], v[96:99], v[160:163]\n"
      "v_mul_f32 v176, v169, v176\n"
      "v_mul_f32 v188, v81, v188\n"
      "v_mfma_f32_16x16x32_bf16 v[160:163], a[56:59], v[192:195], v[160:163]\n"
      "v_min_u32 v8, 96, v8\n"
      "v_fmac_f32 v204, v146, v146\n"
      "v_mfma_f32_16x16x32_bf16 v[160:163], a[60:63], v[200:203], v[160:163]\n"
      "v_cvt_pk_bf16_f32 v132, v216, v217\n"
      "v_cvt_pk_bf16_f32 v179, v180, v181\n"
      "v_add_u32 v8, s77, v8\n"
      "v_cvt_pk_bf16_f32 v105, v106, v107\n"
      "v_cvt_pk_bf16_f32 v133, v218, v219\n"
      "v_pk_mul_f32 v[170:171], v[170:171], v[158:159] op_sel_hi:[1,0]\n"
      "v_pk_mul_f32 v[150:151], v[150:151], v[80:81] op_sel:[0,1]\n"
      "v_pk_mul_f32 v[128:129], v[128:129], v[208:209] op_sel:[0,1]\n"
      "v_pk_mul_f32 v[126:127], v[126:127], v[208:209] op_sel:[0,1]\n"
      "v_pk_mul_f32 v[164:165], v[164:165], v[158:159] op_sel_hi:[1,0]\n"
      "v_pk_mul_f32 v[182:183], v[182:183], v[158:159] op_sel_hi:[1,0]\n"
      "v_pk_mul_f32 v[138:139], v[138:139], v[80:81] op_sel:[0,1]\n"
      "v_pk_mul_f32 v[140:141], v[140:141], v[80:81] op_sel:[0,1]\n"
      "v_pk_mul_f32 v[166:167], v[166:167], v[158:159] op_sel_hi:[1,0]\n"
      "v_pk_mul_f32 v[184:185], v[184:185], v[158:159] op_sel_hi:[1,0]\n"
      "v_pk_mul_f32 v[212:213], v[212:213], v[168:169] op_sel:[0,1]\n"
      "v_pk_mul_f32 v[172:173], v[172:173], v[158:159] op_sel_hi:[1,0]\n"
      "v_mfma_f32_16x16x32_bf16 v[160:163], a[64:67], v[116:119], v[160:163]\n"
      "v_fmac_f32_e64 v204, v147, v147\n"
      "v_cvt_pk_bf16_f32 v216, v134, v135\n"
      "v_mfma_f32_16x16x32_bf16 v[160:163], a[68:71], v[112:115], v[160:163]\n"
      "v_fmac_f32_e64 v204, v220, v220\n"
      "v_cvt_pk_bf16_f32 v138, v138, v139\n"
      "v_mfma_f32_16x16x32_bf16 v[100:103], a[72:75], v[122:125], 0\n"
      "s_cmp_ge_u32 s95, 0x40000000\n"
      "s_cselect_b32 s94, -1, -2\n"
      "v_mul_f32_e64 v187, v81, v187\n"
      "v_mfma_f32_16x16x32_bf16 v[100:103], a[76:79], v[96:99], v[100:103]\n"
      "v_cmp_ne_u32 s[56:57], 0, v156\n"
      "v_cvt_pk_bf16_f32 v139, v140, v141\n"
      "v_mfma_f32_16x16x32_bf16 v[100:103], a[80:83], v[192:195], v[100:103]\n"
      "v_fmac_f32_e64 v204, v221, v221\n"
      "v_cvt_pk_bf16_f32 v170, v170, v171\n"
      "v_mfma_f32_16x16x32_bf16 v[100:103], a[84:87], v[200:203], v[100:103]\n"
      "v_mul_f32_e64 v186, v81, v186\n"
      "v_cvt_pk_bf16_f32 v106, v126, v127\n"
      "v_mfma_f32_16x16x32_bf16 v[100:103], a[88:91], v[116:119], v[100:103]\n"
      "v_add_u32 v8, 115296, v8\n"
      "v_fmac_f32_e64 v204, v222, v222\n"
      "v_mfma_f32_16x16x32_bf16 v[100:103], a[92:95], v[112:115], v[100:103]\n"
      "v_cvt_pk_bf16_f32 v140, v186, v187\n"
      "v_fmac_f32_e64 v204, v223, v223\n"
      "v_mfma_f32_16x16x32_bf16 v[156:159], a[96:99], v[122:125], 0\n"
      "v_cvt_pk_bf16_f32 v217, v136, v137\n"
      "v_cvt_pk_bf16_f32 v174, v174, v175\n"
      "v_mfma_f32_16x16x32_bf16 v[156:159], a[100:103], v[96:99], v[156:159]\n"
      "v_fmac_f32_e64 v204, v160, v160\n"
      "v_cvt_pk_bf16_f32 v171, v172, v173\n"
      "v_mfma_f32_16x16x32_bf16 v[156:159], a[104:107], v[192:195], v[156:159]\n"
      "v_cmp_gt_u32 s[72:73], 64, v78\n"
      "v_mul_f32 v148, v81, v148\n"
      "v_mfma_f32_16x16x32_bf16 v[156:159], a[108:111], v[200:203], v[156:159]\n"
      "v_fmac_f32 v204, v161, v161\n"
      "v_cvt_pk_bf16_f32 v155, v212, v213\n"
      "v_mfma_f32_16x16x32_bf16 v[156:159], a[112:115], v[116:119], v[156:159]\n"
      "v_cvt_pk_bf16_f32 v107, v128, v129\n"
      "v_fmac_f32_e64 v204, v162, v162\n"
      "v_mfma_f32_16x16x32_bf16 v[156:159], a[116:119], v[112:115], v[156:159]\n"
      "ds_read_b128 v[210:213], v7 offset:18432\n"
      "v_fma_f32 v218, v100, v100, s51\n"
      "v_mfma_f32_16x16x32_bf16 v[126:129], a[120:123], v[122:125], 0\n"
      "v_cvt_pk_bf16_f32 v173, v184, v185\n"
      "v_mul_f32 v149, v81, v149\n"
      "v_mfma_f32_16x16x32_bf16 v[126:129], a[124:127], v[96:99], v[126:129]\n"
      "v_fmac_f32 v218, v101, v101\n"
      "v_fmac_f32_e64 v218, v102, v102\n"
      "v_mfma_f32_16x16x32_bf16 v[126:129], a[128:131], v[192:195], v[126:129]\n"
      "v_fmac_f32 v218, v103, v103\n"
      "v_fmac_f32 v218, v156, v156\n"
      "v_mfma_f32_16x16x32_bf16 v[126:129], a[132:135], v[200:203], v[126:129]\n"
      "v_fmac_f32 v204, v163, v163\n"
      "v_fmac_f32 v218, v157, v157\n"
      "v_mfma_f32_16x16x32_bf16 v[126:129], a[136:139], v[116:119], v[126:129]\n"
      "ds_read_b128 v[184:187], v2 offset:21504\n"
      "v_cvt_pk_bf16_f32 v172, v182, v183\n"
      "v_mfma_f32_16x16x32_bf16 v[126:129], a[140:143], v[112:115], v[126:129]\n"
      "v_fmac_f32 v218, v158, v158\n"
      "v_cvt_pk_bf16_f32 v141, v188, v189\n"
      "v_mfma_f32_16x16x32_bf16 v[188:191], v[122:125], a[144:147], 0\n"
      "v_cvt_pk_bf16_f32 v175, v176, v177\n"
      "v_and_b32 v94, 2, v88\n"
      "v_mfma_f32_16x16x32_bf16 v[188:191], v[96:99], a[148:151], v[188:191]\n"
      "v_cvt_pk_bf16_f32 v164, v164, v165\n"
      "v_fmac_f32_e64 v218, v159, v159\n"
      "v_mfma_f32_16x16x32_bf16 v[188:191], v[192:195], a[152:155], v[188:191]\n"
      "v_cvt_pk_bf16_f32 v199, v206, v207\n"
      "v_cvt_pk_bf16_f32 v165, v166, v167\n"
      "v_mfma_f32_16x16x32_bf16 v[188:191], v[200:203], a[156:159], v[188:191]\n"
      "v_fmac_f32 v218, v126, v126\n"
      "v_fmac_f32 v218, v127, v127\n"
      "v_mfma_f32_16x16x32_bf16 v[188:191], v[116:119], a[160:163], v[188:191]\n"
      "s_waitcnt vmcnt(7)\n"
      "v_fmac_f32 v218, v128, v128\n"
      "v_fmac_f32 v218, v129, v129\n"
      "v_mfma_f32_16x16x32_bf16 v[188:191], v[112:115], a[164:167], v[188:191]\n"
      "s_nop 0\n"
      "v_permlane16_swap_b32 v204, v218\n"
      "v_mfma_f32_16x16x32_bf16 v[166:169], v[122:125], a[168:171], 0\n"
      "v_add_f32 v204, v204, v218\n"
      "v_mov_b32 v218, v204\n"
      "v_mfma_f32_16x16x32_bf16 v[166:169], v[96:99], a[172:175], v[166:169]\n"
      "s_nop 0\n"
      "v_permlane32_swap_b32_e64 v204, v218\n"
      "v_mfma_f32_16x16x32_bf16 v[166:169], v[192:195], a[176:179], v[166:169]\n"
      "v_mov_b32 v92, 0x3fb9\n"
      "v_add_f32_e64 v204, v204, v218\n"
      "v_mfma_f32_16x16x32_bf16 v[166:169], v[200:203], a[180:183], v[166:169]\n"
      "s_waitcnt lgkmcnt(0)\n"
      "v_rsq_f32 v204, v204\n"
      "v_mfma_f32_16x16x32_bf16 v[166:169], v[116:119], a[184:187], v[166:169]\n"
      "ds_read_b128 v[134:137], v7 offset:22528\n"
      "v_mov_b32 v218, v204\n"
      "v_mfma_f32_16x16x32_bf16 v[166:169], v[112:115], a[188:191], v[166:169]\n"
      "s_nop 0\n"
      "v_permlane16_swap_b32_e64 v204, v218\n"
      "v_mfma_f32_16x16x32_bf16 v[206:209], v[122:125], a[192:195], 0\n"
      "ds_read_b128 v[120:123], v2 offset:24576\n"
      "v_and_b32_e64 v88, -4, v88\n"
      "v_mfma_f32_16x16x32_bf16 v[206:209], v[96:99], a[196:199], v[206:209]\n"
      "ds_read_b128 v[180:183], v2 offset:18432\n"
      "v_cvt_pk_bf16_f32 v148, v148, v149\n"
      "v_mfma_f32_16x16x32_bf16 v[206:209], v[192:195], a[200:203], v[206:209]\n"
      "ds_read_b128 v[192:195], v2 offset:27648\n"
      "v_mul_f32_e64 v204, s50, v204\n"
      "v_mfma_f32_16x16x32_bf16 v[206:209], v[200:203], a[204:207], v[206:209]\n"
      "buffer_store_dwordx4 v[210:213], v20, s[24:27], s46 offen sc1\n"
      "v_pk_mul_f32 v[158:159], v[158:159], v[218:219] op_sel_hi:[1,0]\n"
      "v_pk_mul_f32 v[128:129], v[128:129], v[218:219] op_sel_hi:[1,0]\n"
      "v_pk_mul_f32 v[100:101], v[100:101], v[218:219] op_sel_hi:[1,0]\n"
      "v_pk_mul_f32 v[160:161], v[160:161], v[204:205] op_sel_hi:[1,0]\n"
      "v_pk_mul_f32 v[146:147], v[146:147], v[204:205] op_sel_hi:[1,0]\n"
      "v_pk_mul_f32 v[220:221], v[220:221], v[204:205] op_sel_hi:[1,0]\n"
      "v_pk_mul_f32 v[102:103], v[102:103], v[218:219] op_sel_hi:[1,0]\n"
      "v_pk_mul_f32 v[222:223], v[222:223], v[204:205] op_sel_hi:[1,0]\n"
      "v_pk_mul_f32 v[144:145], v[144:145], v[204:205] op_sel_hi:[1,0]\n"
      "v_pk_mul_f32 v[156:157], v[156:157], v[218:219] op_sel_hi:[1,0]\n"
      "v_pk_mul_f32 v[126:127], v[126:127], v[218:219] op_sel_hi:[1,0]\n"
      "v_mfma_f32_16x16x32_bf16 v[206:209], v[116:119], a[208:211], v[206:209]\n"
      "s_waitcnt lgkmcnt(1)\n"
      "v_cvt_pk_bf16_f32 v149, v150, v151\n"
      "v_add_u32 v125, 110592, v88\n"
      "v_mfma_f32_16x16x32_bf16 v[206:209], v[112:115], a[212:215], v[206:209]\n"
      "ds_read2_b32 v[218:219], v125 offset0:0 offset1:1\n"
      "v_cvt_pk_bf16_f32 v81, v168, v169\n"
      "v_mfma_f32_16x16x32_bf16 v[96:99], a[216:219], v[180:183], 0\n"
      "ds_read_b128 v[200:203], v2 offset:33792\n"
      "v_mul_f32 v162, v204, v162\n"
      "v_mfma_f32_16x16x32_bf16 v[96:99], a[220:223], v[184:187], v[96:99]\n"
      "ds_read_b128 v[112:115], v2 offset:30720\n"
      "v_cvt_pk_bf16_f32 v126, v126, v127\n"
      "v_mfma_f32_16x16x32_bf16 v[96:99], a[224:227], v[120:123], v[96:99]\n"
      "v_cvt_pk_bf16_f32 v127, v128, v129\n"
      "v_cvt_pk_bf16_f32 v100, v100, v101\n"
      "v_mfma_f32_16x16x32_bf16 v[116:119], a[240:243], v[180:183], 0\n"
      "v_mul_f32_e64 v163, v204, v163\n"
      "v_cvt_pk_bf16_f32 v80, v166, v167\n"
      "v_mfma_f32_16x16x32_bf16 v[116:119], a[244:247], v[184:187], v[116:119]\n"
      "ds_read_b128 v[210:213], v7 offset:26624\n"
      "v_lshlrev_b64 v[92:93], v78, v[92:93]\n"
      "v_mfma_f32_16x16x32_bf16 v[116:119], a[248:251], v[120:123], v[116:119]\n"
      "buffer_store_dwordx4 v[134:137], v21, s[24:27], s46 offen sc1\n"
      "s_nop 1\n"
      "v_mfma_f32_16x16x32_bf16 v[134:137], v[232:235], v[180:183], 0\n"
      "s_waitcnt lgkmcnt(1)\n"
      "v_cvt_pk_bf16_f32 v85, v208, v209\n"
      "v_cvt_pk_bf16_f32 v144, v144, v145\n"
      "v_mfma_f32_16x16x32_bf16 v[134:137], v[236:239], v[184:187], v[134:137]\n"
      "ds_read_b128 v[166:169], v7 offset:30720\n"
      "v_cvt_pk_bf16_f32 v84, v206, v207\n"
      "v_mfma_f32_16x16x32_bf16 v[134:137], v[240:243], v[120:123], v[134:137]\n"
      "ds_read2_b32 v[150:151], v125 offset0:2 offset1:10\n"
      "v_mov_b32 v123, 0\n"
      "v_mfma_f32_16x16x32_bf16 v[96:99], a[228:231], v[192:195], v[96:99]\n"
      "s_waitcnt lgkmcnt(2)\n"
      "v_cvt_pk_bf16_f32 v101, v102, v103\n"
      "v_cvt_pk_bf16_f32 v76, v188, v189\n"
      "v_mfma_f32_16x16x32_bf16 v[96:99], a[232:235], v[112:115], v[96:99]\n"
      "ds_read2_b32 v[206:207], v125 offset0:24 offset1:18\n"
      "v_cndmask_b32_e64 v93, 0, v93, s[72:73]\n"
      "v_mfma_f32_16x16x32_bf16 v[96:99], a[236:239], v[200:203], v[96:99]\n"
      "ds_read_b128 v[184:187], v10 offset:32\n"
      "v_cvt_pk_bf16_f32 v160, v160, v161\n"
      "v_mfma_f32_16x16x32_bf16 v[116:119], a[252:255], v[192:195], v[116:119]\n"
      "ds_read2_b32 v[204:205], v125 offset0:8 offset1:9\n"
      "v_alignbyte_b32 v218, v219, v218, v94\n"
      "v_mfma_f32_16x16x32_bf16 v[116:119], v[224:227], v[112:115], v[116:119]\n"
      "buffer_store_dwordx4 v[210:213], v22, s[24:27], s46 offen sc1\n"
      "v_mfma_f32_16x16x32_bf16 v[116:119], v[228:231], v[200:203], v[116:119]\n"
      "v_cvt_pk_bf16_f32 v102, v156, v157\n"
      "v_cvt_pk_bf16_f32 v145, v146, v147\n"
      "v_mfma_f32_16x16x32_bf16 v[134:137], v[244:247], v[192:195], v[134:137]\n"
      "v_cvt_pk_bf16_f32 v161, v162, v163\n"
      "v_cvt_pk_bf16_f32 v103, v158, v159\n"
      "v_mfma_f32_16x16x32_bf16 v[134:137], v[248:251], v[112:115], v[134:137]\n"
      "ds_read2_b32 v[212:213], v125 offset0:16 offset1:17\n"
      "v_mul_f32 v129, v97, v97\n"
      "v_mfma_f32_16x16x32_bf16 v[134:137], v[252:255], v[200:203], v[134:137]\n"
      "s_waitcnt lgkmcnt(1)\n"
      "v_alignbyte_b32 v204, v205, v204, v94\n"
      "v_fma_f32 v128, v96, v96, s52\n"
      "v_mfma_f32_16x16x32_bf16 v[156:159], v[104:107], v[152:155], v[40:43]\n"
      "v_alignbyte_b32 v205, v151, v205, v94\n"
      "v_cndmask_b32_e64 v92, 0, v92, s[72:73]\n"
      "v_mfma_f32_16x16x32_bf16 v[208:211], v[170:173], v[152:155], v[44:47]\n"
      "ds_read_b64 v[124:125], v10 offset:24\n"
      "v_fmac_f32 v129, v99, v99\n"
      "v_mfma_f32_16x16x32_bf16 v[192:195], v[100:103], v[152:155], v[48:51]\n"
      "v_fmac_f32 v128, v98, v98\n"
      "v_cvt_pk_bf16_f32 v77, v190, v191\n"
      "v_mfma_f32_16x16x32_bf16 v[180:183], v[184:187], v[152:155], v[24:27]\n"
      "ds_read_b128 v[112:115], v7 offset:34816\n"
      "v_mov_b32 v122, 0xbb2b\n"
      "v_mfma_f32_16x16x16_bf16 v[156:159], v[178:179], v[174:175], v[156:159]\n"
      "v_alignbyte_b32 v219, v150, v219, v94\n"
      "v_add_u32 v121, 113728, v88\n"
      "v_cmp_gt_u32 s[72:73], 64, v78\n"
      "v_mfma_f32_16x16x16_bf16 v[208:211], v[164:165], v[174:175], v[208:211]\n"
      "buffer_store_dwordx4 v[166:169], v23, s[24:27], s46 offen sc1\n"
      "v_pk_fma_f32 v[128:129], v[116:117], v[116:117], v[128:129]\n"
      "v_mfma_f32_16x16x16_bf16 v[192:195], v[126:127], v[174:175], v[192:195]\n"
      "ds_read_b128 v[150:153], v2 offset:21760\n"
      "s_waitcnt lgkmcnt(2)\n"
      "v_mfma_f32_16x16x16_bf16 v[180:183], v[124:125], v[174:175], v[180:183]\n"
      "v_lshlrev_b64 v[122:123], v78, v[122:123]\n"
      "v_cvt_pk_bf16_f32 v146, v220, v221\n"
      "v_alignbyte_b32 v212, v213, v212, v94\n"
      "v_mfma_f32_16x16x16_bf16 v[156:159], v[92:93], v[218:219], v[156:159]\n"
      "v_alignbyte_b32 v213, v207, v213, v94\n"
      "v_fmac_f32 v129, v119, v119\n"
      "v_alignbyte_b32 v206, v206, v206, v94\n"
      "v_mfma_f32_16x16x16_bf16 v[208:211], v[92:93], v[204:205], v[208:211]\n"
      "ds_read_b128 v[174:177], v2 offset:18688\n"
      "v_cndmask_b32_e64 v122, 0, v122, s[72:73]\n"
      "v_mfma_f32_16x16x16_bf16 v[192:195], v[92:93], v[212:213], v[192:195]\n"
      "v_add_u32 v78, 112160, v88\n"
      "v_cvt_pk_bf16_f32 v147, v222, v223\n"
      "v_cndmask_b32_e64 v123, 0, v123, s[72:73]\n"
      "v_mfma_f32_16x16x16_bf16 v[180:183], v[92:93], v[206:207], v[180:183]\n"
      "ds_read2_b32 v[190:191], v78 offset0:8 offset1:9\n"
      "v_fmac_f32 v128, v118, v118\n"
      "v_mfma_f32_16x16x16_bf16 v[156:159], v[122:123], v[218:219], v[156:159]\n"
      "ds_read_b128 v[218:221], v2 offset:24832\n"
      "v_fmac_f32 v129, v135, v135\n"
      "v_mfma_f32_16x16x16_bf16 v[208:211], v[122:123], v[204:205], v[208:211]\n"
      "ds_read2_b32 v[188:189], v78 offset0:0 offset1:1\n"
      "v_fmac_f32 v128, v134, v134\n"
      "v_mfma_f32_16x16x16_bf16 v[192:195], v[122:123], v[212:213], v[192:195]\n"
      "v_fmac_f32 v128, v136, v136\n"
      "v_fmac_f32 v129, v137, v137\n"
      "v_max3_f32 v212, v156, v157, v158\n"
      "v_mfma_f32_16x16x16_bf16 v[180:183], v[122:123], v[206:207], v[180:183]\n"
      "v_add_f32 v128, v128, v129\n"
      "v_max3_f32 v212, v212, v210, v211\n"
      "s_waitcnt lgkmcnt(3)\n"
      "v_mfma_f32_16x16x32_bf16 v[200:203], a[216:219], v[174:177], 0\n"
      "buffer_store_dwordx4 v[112:115], v25, s[24:27], s46 offen sc1\n"
      "v_mfma_f32_16x16x32_bf16 v[200:203], a[220:223], v[150:153], v[200:203]\n"
      "s_waitcnt lgkmcnt(1)\n"
      "v_alignbyte_b32 v190, v191, v190, v94\n"
      "v_max3_f32 v206, v159, v208, v209\n"
      "v_mfma_f32_16x16x32_bf16 v[200:203], a[224:227], v[218:221], v[200:203]\n"
      "ds_read2_b32 v[166:167], v78 offset0:2 offset1:10\n"
      "v_max3_f32 v212, v212, v194, v195\n"
      "v_mfma_f32_16x16x32_bf16 v[112:115], a[240:243], v[174:177], 0\n"
      "ds_read2_b32 v[154:155], v78 offset0:24 offset1:18\n"
      "v_max3_f32 v206, v206, v192, v193\n"
      "v_mfma_f32_16x16x32_bf16 v[112:115], a[244:247], v[150:153], v[112:115]\n"
      "ds_read_u16 v8, v8\n"
      "s_waitcnt vmcnt(6)\n"
      "v_or3_b32 v108, v108, v109, v110\n"
      "v_mfma_f32_16x16x32_bf16 v[112:115], a[248:251], v[218:221], v[112:115]\n"
      "s_waitcnt lgkmcnt(3)\n"
      "v_max3_f32 v212, v212, v206, v180\n"
      "v_alignbyte_b32 v188, v189, v188, v94\n"
      "v_mfma_f32_16x16x32_bf16 v[204:207], v[232:235], v[174:177], 0\n"
      "ds_read2_b32 v[162:163], v78 offset0:16 offset1:17\n"
      "v_mov_b32 v88, v212\n"
      "v_mfma_f32_16x16x32_bf16 v[204:207], v[236:239], v[150:153], v[204:207]\n"
      "s_nop 0\n"
      "v_permlane16_swap_b32_e64 v212, v88\n"
      "v_mfma_f32_16x16x32_bf16 v[204:207], v[240:243], v[218:221], v[204:207]\n"
      "s_waitcnt lgkmcnt(3)\n"
      "v_max_f32 v212, v212, v88\n"
      "v_alignbyte_b32 v191, v167, v191, v94\n"
      "v_mfma_f32_16x16x32_bf16 v[218:221], v[104:107], v[138:141], v[52:55]\n"
      "v_alignbyte_b32 v189, v166, v189, v94\n"
      "v_mov_b32 v88, v212\n"
      "s_waitcnt vmcnt(5)\n"
      "v_or3_b32 v108, v108, v111, v82\n"
      "v_mfma_f32_16x16x32_bf16 v[174:177], v[170:173], v[138:141], v[56:59]\n"
      "v_permlane32_swap_b32_e64 v212, v88\n"
      "v_mfma_f32_16x16x32_bf16 v[150:153], v[100:103], v[138:141], v[60:63]\n"
      "ds_write_b32 v31, v108 offset:4608\n"
      "v_mfma_f32_16x16x32_bf16 v[166:169], v[184:187], v[138:141], v[26:29]\n"
      "ds_read_u16 v78, v11\n"
      "v_max_f32 v212, v212, v88\n"
      "v_sub_f32_e64 v209, v209, v212\n"
      "s_waitcnt lgkmcnt(2)\n"
      "v_alignbyte_b32 v162, v163, v162, v94\n"
      "v_pk_add_f32 v[156:157], v[156:157], v[212:213] op_sel_hi:[1,0] neg_lo:[0,1] neg_hi:[0,1]\n"
      "v_pk_add_f32 v[194:195], v[194:195], v[212:213] op_sel_hi:[1,0] neg_lo:[0,1] neg_hi:[0,1]\n"
      "v_pk_add_f32 v[158:159], v[158:159], v[212:213] op_sel_hi:[1,0] neg_lo:[0,1] neg_hi:[0,1]\n"
      "v_pk_add_f32 v[210:211], v[210:211], v[212:213] op_sel_hi:[1,0] neg_lo:[0,1] neg_hi:[0,1]\n"
      "v_pk_add_f32 v[192:193], v[192:193], v[212:213] op_sel_hi:[1,0] neg_lo:[0,1] neg_hi:[0,1]\n"
      "v_mfma_f32_16x16x16_bf16 v[218:221], v[178:179], v[148:149], v[218:221]\n"
      "v_exp_f32 v192, v192\n"
      "v_exp_f32 v156, v156\n"
      "v_mfma_f32_16x16x16_bf16 v[174:177], v[164:165], v[148:149], v[174:177]\n"
      "v_exp_f32 v193, v193\n"
      "v_alignbyte_b32 v163, v155, v163, v94\n"
      "v_mfma_f32_16x16x16_bf16 v[150:153], v[126:127], v[148:149], v[150:153]\n"
      "v_cvt_pk_bf16_f32 v88, v192, v193\n"
      "v_exp_f32 v158, v158\n"
      "v_mfma_f32_16x16x16_bf16 v[166:169], v[124:125], v[148:149], v[166:169]\n"
      "v_sub_f32 v180, v180, v212\n"
      "v_exp_f32 v157, v157\n"
      "v_mfma_f32_16x16x16_bf16 v[218:221], v[92:93], v[188:189], v[218:221]\n"
      "v_cvt_pk_bf16_f32 v156, v156, v157\n"
      "v_exp_f32 v210, v210\n"
      "v_mfma_f32_16x16x16_bf16 v[174:177], v[92:93], v[190:191], v[174:177]\n"
      "v_exp_f32 v159, v159\n"
      "v_alignbyte_b32 v154, v154, v154, v94\n"
      "v_mfma_f32_16x16x16_bf16 v[150:153], v[92:93], v[162:163], v[150:153]\n"
      "ds_read_b128 v[138:141], v2 offset:27904\n"
      "v_cvt_pk_bf16_f32 v157, v158, v159\n"
      "v_mfma_f32_16x16x16_bf16 v[166:169], v[92:93], v[154:155], v[166:169]\n"
      "v_exp_f32 v211, v211\n"
      "v_sub_f32 v208, v208, v212\n"
      "v_mfma_f32_16x16x16_bf16 v[218:221], v[122:123], v[188:189], v[218:221]\n"
      "ds_read_b128 v[108:111], v2 offset:30976\n"
      "v_cvt_pk_bf16_f32 v159, v210, v211\n"
      "v_mfma_f32_16x16x16_bf16 v[174:177], v[122:123], v[190:191], v[174:177]\n"
      "ds_read_b128 v[210:213], v2 offset:34048\n"
      "v_exp_f32 v208, v208\n"
      "v_exp_f32 v209, v209\n"
      "v_mfma_f32_16x16x16_bf16 v[150:153], v[122:123], v[162:163], v[150:153]\n"
      "v_cvt_pk_bf16_f32 v158, v208, v209\n"
      "v_exp_f32 v180, v180\n"
      "v_mfma_f32_16x16x16_bf16 v[166:169], v[122:123], v[154:155], v[166:169]\n"
      "v_exp_f32 v194, v194\n"
      "v_exp_f32_e64 v195, v195\n"
      "v_max3_f32 v193, v218, v219, v220\n"
      "v_max3_f32 v163, v221, v174, v175\n"
      "v_max3_f32 v193, v193, v176, v177\n"
      "s_waitcnt lgkmcnt(2)\n"
      "v_mfma_f32_16x16x32_bf16 v[200:203], a[228:231], v[138:141], v[200:203]\n"
      "s_waitcnt lgkmcnt(1)\n"
      "v_cvt_pk_bf16_f32 v89, v194, v195\n"
      "v_max3_f32 v163, v163, v150, v151\n"
      "v_mfma_f32_16x16x32_bf16 v[200:203], a[232:235], v[108:111], v[200:203]\n"
      "s_waitcnt lgkmcnt(0)\n"
      "v_max3_f32 v193, v193, v152, v153\n"
      "v_max3_f32 v193, v193, v163, v166\n"
      "v_mfma_f32_16x16x32_bf16 v[200:203], a[236:239], v[210:213], v[200:203]\n"
      "ds_read_u16 v82, v11 offset:32\n"
      "v_mov_b32 v209, v193\n"
      "v_mfma_f32_16x16x32_bf16 v[112:115], a[252:255], v[138:141], v[112:115]\n"
      "ds_read2_b32 v[168:169], v121 offset0:8 offset1:9\n"
      "v_permlane16_swap_b32_e64 v193, v209\n"
      "v_mfma_f32_16x16x32_bf16 v[112:115], v[224:227], v[108:111], v[112:115]\n"
      "ds_read2_b32 v[162:163], v121 offset0:16 offset1:17\n"
      "v_max_f32 v193, v193, v209\n"
      "v_mfma_f32_16x16x32_bf16 v[112:115], v[228:231], v[210:213], v[112:115]\n"
      "ds_read2_b32 v[148:149], v121 offset0:0 offset1:1\n"
      "v_mov_b32 v209, v193\n"
      "v_mfma_f32_16x16x32_bf16 v[204:207], v[244:247], v[138:141], v[204:207]\n"
      "ds_read_u16 v86, v11 offset:64\n"
      "v_permlane32_swap_b32 v193, v209\n"
      "v_cvt_pk_bf16_f32 v90, v180, 0\n"
      "v_max_f32 v193, v193, v209\n"
      "v_pk_add_f32 v[220:221], v[220:221], v[192:193] op_sel:[0,1] neg_lo:[0,1] neg_hi:[0,1]\n"
      "v_pk_add_f32 v[174:175], v[174:175], v[192:193] op_sel:[0,1] neg_lo:[0,1] neg_hi:[0,1]\n"
      "v_pk_add_f32 v[218:219], v[218:219], v[192:193] op_sel:[0,1] neg_lo:[0,1] neg_hi:[0,1]\n"
      "v_mfma_f32_16x16x32_bf16 v[204:207], v[248:251], v[108:111], v[204:207]\n"
      "v_exp_f32 v219, v219\n"
      "v_exp_f32 v218, v218\n"
      "v_mfma_f32_16x16x32_bf16 v[204:207], v[252:255], v[210:213], v[204:207]\n"
      "ds_read2_b32 v[108:109], v121 offset0:2 offset1:10\n"
      "v_sub_f32 v166, v166, v193\n"
      "v_mfma_f32_16x16x32_bf16 v[208:211], v[32:35], v[156:159], 0\n"
      "v_exp_f32 v221, v221\n"
      "v_mfma_f32_16x16x32_bf16 v[208:211], v[32:35], v[88:91], v[208:211]\n"
      "s_waitcnt lgkmcnt(1)\n"
      "v_exp_f32 v220, v220\n"
      "v_alignbyte_b32 v148, v149, v148, v94\n"
      "v_alignbyte_b32 v162, v163, v162, v94\n"
      "v_cvt_pk_bf16_f32 v218, v218, v219\n"
      "v_pk_add_f32 v[176:177], v[176:177], v[192:193] op_sel:[0,1] neg_lo:[0,1] neg_hi:[0,1]\n"
      "v_pk_add_f32 v[150:151], v[150:151], v[192:193] op_sel:[0,1] neg_lo:[0,1] neg_hi:[0,1]\n"
      "v_pk_add_f32 v[152:153], v[152:153], v[192:193] op_sel:[0,1] neg_lo:[0,1] neg_hi:[0,1]\n"
      "v_mfma_f32_16x16x32_bf16 v[210:213], v[130:133], v[156:159], 0\n"
      "v_exp_f32_e64 v177, v177\n"
      "v_mfma_f32_16x16x32_bf16 v[210:213], v[76:79], v[88:91], v[210:213]\n"
      "ds_read2_b32 v[180:181], v121 offset0:24 offset1:18\n"
      "v_exp_f32_e64 v176, v176\n"
      "v_mfma_f32_16x16x32_bf16 v[192:195], v[196:199], v[156:159], 0\n"
      "v_cvt_pk_bf16_f32 v219, v220, v221\n"
      "v_cvt_pk_bf16_f32 v221, v176, v177\n"
      "v_mfma_f32_16x16x32_bf16 v[192:195], v[80:83], v[88:91], v[192:195]\n"
      "v_exp_f32 v175, v175\n"
      "v_mfma_f32_16x16x32_bf16 v[138:141], v[214:217], v[156:159], 0\n"
      "s_waitcnt lgkmcnt(1)\n"
      "v_exp_f32_e64 v174, v174\n"
      "v_mfma_f32_16x16x32_bf16 v[138:141], v[84:87], v[88:91], v[138:141]\n"
      "v_alignbyte_b32 v149, v108, v149, v94\n"
      "v_alignbyte_b32 v168, v169, v168, v94\n"
      "v_mfma_f32_16x16x32_bf16 v[188:191], v[104:107], v[144:147], v[64:67]\n"
      "v_alignbyte_b32 v169, v109, v169, v94\n"
      "v_cvt_pk_bf16_f32 v220, v174, v175\n"
      "v_mfma_f32_16x16x32_bf16 v[174:177], v[170:173], v[144:147], v[68:71]\n"
      "s_waitcnt lgkmcnt(0)\n"
      "v_alignbyte_b32 v163, v181, v163, v94\n"
      "v_alignbyte_b32 v180, v180, v180, v94\n"
      "v_mfma_f32_16x16x32_bf16 v[108:111], v[100:103], v[144:147], v[72:75]\n"
      "v_rcp_f32 v208, v208\n"
      "v_mfma_f32_16x16x32_bf16 v[154:157], v[184:187], v[144:147], v[28:31]\n"
      "v_mul_f32 v212, v208, v212\n"
      "v_lshlrev_b32 v8, 16, v8\n"
      "v_mfma_f32_16x16x16_bf16 v[188:191], v[178:179], v[160:161], v[188:191]\n"
      "v_mul_f32 v210, v208, v210\n"
      "v_exp_f32 v150, v150\n"
      "v_mfma_f32_16x16x16_bf16 v[174:177], v[164:165], v[160:161], v[174:177]\n"
      "v_exp_f32 v166, v166\n"
      "v_mul_f32 v193, v208, v193\n"
      "v_mfma_f32_16x16x16_bf16 v[108:111], v[126:127], v[160:161], v[108:111]\n"
      "v_mul_f32 v211, v208, v211\n"
      "v_mul_f32 v195, v208, v195\n"
      "v_mul_f32 v213, v208, v213\n"
      "v_mfma_f32_16x16x16_bf16 v[154:157], v[124:125], v[160:161], v[154:157]\n"
      "v_mul_f32 v194, v208, v194\n"
      "v_cvt_pk_bf16_f32 v210, v210, v211\n"
      "v_mul_f32_e64 v138, v208, v138\n"
      "v_mfma_f32_16x16x16_bf16 v[188:191], v[92:93], v[148:149], v[188:191]\n"
      "v_cvt_pk_bf16_f32 v211, v212, v213\n"
      "v_exp_f32_e64 v152, v152\n"
      "v_mfma_f32_16x16x16_bf16 v[174:177], v[92:93], v[168:169], v[174:177]\n"
      "v_cvt_pk_bf16_f32 v213, v194, v195\n"
      "v_exp_f32 v153, v153\n"
      "v_mfma_f32_16x16x16_bf16 v[108:111], v[92:93], v[162:163], v[108:111]\n"
      "v_exp_f32 v151, v151\n"
      "v_fma_f32 v222, v200, v200, s52\n"
      "v_mfma_f32_16x16x16_bf16 v[154:157], v[92:93], v[180:181], v[154:157]\n"
      "v_mul_f32_e64 v192, v208, v192\n"
      "v_cvt_pk_bf16_f32 v93, v152, v153\n"
      "v_cvt_pk_bf16_f32 v92, v150, v151\n"
      "v_mfma_f32_16x16x16_bf16 v[188:191], v[122:123], v[148:149], v[188:191]\n"
      "ds_read_b128 v[150:153], v2 offset:25088\n"
      "v_cvt_pk_bf16_f32 v212, v192, v193\n"
      "v_mfma_f32_16x16x16_bf16 v[174:177], v[122:123], v[168:169], v[174:177]\n"
      "ds_read_b128 v[192:195], v2 offset:18944\n"
      "v_cvt_pk_bf16_f32 v94, v166, 0\n"
      "v_mfma_f32_16x16x16_bf16 v[108:111], v[122:123], v[162:163], v[108:111]\n"
      "ds_read_b128 v[166:169], v2 offset:22016\n"
      "v_mul_f32 v139, v208, v139\n"
      "v_mfma_f32_16x16x16_bf16 v[154:157], v[122:123], v[180:181], v[154:157]\n"
      "v_max3_f32 v209, v188, v189, v190\n"
      "v_cvt_pk_bf16_f32 v138, v138, v139\n"
      "v_mul_f32 v223, v201, v201\n"
      "v_max3_f32 v209, v209, v176, v177\n"
      "v_max3_f32 v159, v191, v174, v175\n"
      "v_max3_f32 v159, v159, v108, v109\n"
      "v_max3_f32 v209, v209, v110, v111\n"
      "v_pk_mul_f32 v[140:141], v[140:141], v[208:209] op_sel_hi:[1,0]\n"
      "v_cvt_pk_bf16_f32 v139, v140, v141\n"
      "v_max3_f32 v209, v209, v159, v154\n"
      "v_mov_b32_e64 v159, v209\n"
      "v_pk_fma_f32 v[222:223], v[202:203], v[202:203], v[222:223]\n"
      "v_pk_fma_f32 v[222:223], v[112:113], v[112:113], v[222:223]\n"
      "v_permlane16_swap_b32_e64 v209, v159\n"
      "v_max_f32_e64 v209, v209, v159\n"
      "v_mov_b32_e64 v159, v209\n"
      "v_pk_fma_f32 v[222:223], v[114:115], v[114:115], v[222:223]\n"
      "v_pk_fma_f32 v[222:223], v[204:205], v[204:205], v[222:223]\n"
      "v_permlane32_swap_b32_e64 v209, v159\n"
      "v_max_f32 v209, v209, v159\n"
      "v_pk_add_f32 v[176:177], v[176:177], v[208:209] op_sel:[0,1] neg_lo:[0,1] neg_hi:[0,1]\n"
      "v_pk_add_f32 v[110:111], v[110:111], v[208:209] op_sel:[0,1] neg_lo:[0,1] neg_hi:[0,1]\n"
      "v_pk_add_f32 v[174:175], v[174:175], v[208:209] op_sel:[0,1] neg_lo:[0,1] neg_hi:[0,1]\n"
      "s_waitcnt lgkmcnt(0)\n"
      "v_mfma_f32_16x16x32_bf16 v[156:159], a[216:219], v[192:195], 0\n"
      "ds_write_b128 v3, v[210:213]\n"
      "v_pk_fma_f32 v[222:223], v[206:207], v[206:207], v[222:223]\n"
      "v_pk_add_f32 v[190:191], v[190:191], v[208:209] op_sel:[0,1] neg_lo:[0,1] neg_hi:[0,1]\n"
      "v_pk_add_f32 v[108:109], v[108:109], v[208:209] op_sel:[0,1] neg_lo:[0,1] neg_hi:[0,1]\n"
      "v_pk_add_f32 v[188:189], v[188:189], v[208:209] op_sel:[0,1] neg_lo:[0,1] neg_hi:[0,1]\n"
      "v_mfma_f32_16x16x32_bf16 v[156:159], a[220:223], v[166:169], v[156:159]\n"
      "v_add_f32 v222, v222, v223\n"
      "v_exp_f32 v191, v191\n"
      "v_mfma_f32_16x16x32_bf16 v[156:159], a[224:227], v[150:153], v[156:159]\n"
      "v_exp_f32 v189, v189\n"
      "v_mfma_f32_16x16x32_bf16 v[146:149], a[240:243], v[192:195], 0\n"
      "v_exp_f32 v188, v188\n"
      "v_mfma_f32_16x16x32_bf16 v[146:149], a[244:247], v[166:169], v[146:149]\n"
      "v_exp_f32 v177, v177\n"
      "v_mfma_f32_16x16x32_bf16 v[146:149], a[248:251], v[150:153], v[146:149]\n"
      "ds_write_b64 v4, v[138:139]\n"
      "v_mfma_f32_16x16x32_bf16 v[160:163], v[232:235], v[192:195], 0\n"
      "v_exp_f32 v190, v190\n"
      "v_mfma_f32_16x16x32_bf16 v[160:163], v[236:239], v[166:169], v[160:163]\n"
      "ds_read_b64 v[180:181], v10 offset:16\n"
      "v_sub_f32 v154, v154, v209\n"
      "v_mfma_f32_16x16x32_bf16 v[160:163], v[240:243], v[150:153], v[160:163]\n"
      "s_cmp_gt_u32 s42, s44\n"
      "s_cselect_b32 s95, s94, s95\n"
      "v_exp_f32 v176, v176\n"
      "v_mfma_f32_16x16x32_bf16 v[208:211], v[32:35], v[218:221], 0\n"
      "v_exp_f32_e64 v111, v111\n"
      "v_mfma_f32_16x16x32_bf16 v[208:211], v[32:35], v[92:95], v[208:211]\n"
      "ds_read_b128 v[150:153], v10\n"
      "v_cvt_pk_bf16_f32 v188, v188, v189\n"
      "v_mfma_f32_16x16x32_bf16 v[192:195], v[130:133], v[218:221], 0\n"
      "v_exp_f32_e64 v175, v175\n"
      "v_mfma_f32_16x16x32_bf16 v[192:195], v[76:79], v[92:95], v[192:195]\n"
      "v_exp_f32_e64 v174, v174\n"
      "v_mfma_f32_16x16x32_bf16 v[166:169], v[196:199], v[218:221], 0\n"
      "v_cvt_pk_bf16_f32 v189, v190, v191\n"
      "v_cvt_pk_bf16_f32 v191, v176, v177\n"
      "v_mfma_f32_16x16x32_bf16 v[166:169], v[80:83], v[92:95], v[166:169]\n"
      "v_exp_f32 v109, v109\n"
      "v_mfma_f32_16x16x32_bf16 v[210:213], v[214:217], v[218:221], 0\n"
      "v_exp_f32 v108, v108\n"
      "v_mfma_f32_16x16x32_bf16 v[210:213], v[84:87], v[92:95], v[210:213]\n"
      "s_waitcnt lgkmcnt(0)\n"
      "v_rcp_f32 v208, v208\n"
      "v_mfma_f32_16x16x32_bf16 v[218:221], v[150:153], v[104:107], v[30:33]\n"
      "v_cvt_pk_bf16_f32 v190, v174, v175\n"
      "v_cvt_pk_bf16_f32 v88, v108, v109\n"
      "v_mfma_f32_16x16x32_bf16 v[174:177], v[150:153], v[170:173], v[30:33]\n"
      "ds_read_b128 v[170:173], v2 offset:28160\n"
      "v_mul_f32_e64 v194, v208, v194\n"
      "v_mfma_f32_16x16x32_bf16 v[138:141], v[150:153], v[100:103], v[30:33]\n"
      "ds_read_b128 v[100:103], v2 offset:31232\n"
      "v_mul_f32_e64 v169, v208, v169\n"
      "v_mfma_f32_16x16x32_bf16 v[106:109], v[150:153], v[184:187], v[30:33]\n"
      "ds_read_b128 v[150:153], v2 offset:34304\n"
      "v_mfma_f32_16x16x16_bf16 v[218:221], v[180:181], v[178:179], v[218:221]\n"
      "v_exp_f32 v154, v154\n"
      "v_mul_f32 v167, v208, v167\n"
      "v_mfma_f32_16x16x16_bf16 v[174:177], v[180:181], v[164:165], v[174:177]\n"
      "v_mul_f32 v192, v208, v192\n"
      "v_exp_f32 v110, v110\n"
      "v_mfma_f32_16x16x16_bf16 v[138:141], v[180:181], v[126:127], v[138:141]\n"
      "v_mul_f32_e64 v193, v208, v193\n"
      "v_mul_f32_e64 v195, v208, v195\n"
      "v_mul_f32 v166, v208, v166\n"
      "v_mfma_f32_16x16x16_bf16 v[106:109], v[180:181], v[124:125], v[106:109]\n"
      "v_permlane16_swap_b32_e64 v128, v222\n"
      "s_waitcnt lgkmcnt(2)\n"
      "v_mfma_f32_16x16x32_bf16 v[156:159], a[228:231], v[170:173], v[156:159]\n"
      "s_waitcnt lgkmcnt(1)\n"
      "v_cndmask_b32 v218, v218, v174, s[58:59]\n"
      "v_cvt_pk_bf16_f32 v89, v110, v111\n"
      "v_mfma_f32_16x16x32_bf16 v[156:159], a[232:235], v[100:103], v[156:159]\n"
      "s_waitcnt lgkmcnt(0)\n"
      "v_cvt_pk_bf16_f32 v192, v192, v193\n"
      "v_cndmask_b32 v218, v218, v138, s[60:61]\n"
      "v_mfma_f32_16x16x32_bf16 v[156:159], a[236:239], v[150:153], v[156:159]\n"
      "v_cndmask_b32 v218, v218, v106, s[62:63]\n"
      "v_fmac_f32 v218, s53, v8\n"
      "v_mfma_f32_16x16x32_bf16 v[146:149], a[252:255], v[170:173], v[146:149]\n"
      "s_nop 0\n"
      "v_max_f32_dpp v209, v218, v218 row_ror:8 row_mask:0xf bank_mask:0xf\n"
      "v_cvt_pk_bf16_f32 v90, v154, 0\n"
      "v_mfma_f32_16x16x32_bf16 v[146:149], v[224:227], v[100:103], v[146:149]\n"
      "v_max_f32_dpp v209, v209, v209 row_ror:4 row_mask:0xf bank_mask:0xf\n"
      "v_fma_f32 v164, v156, v156, s52\n"
      "v_mfma_f32_16x16x32_bf16 v[146:149], v[228:231], v[150:153], v[146:149]\n"
      "s_add_u32 s42, s42, 1\n"
      "v_max_f32_dpp v209, v209, v209 row_ror:2 row_mask:0xf bank_mask:0xf\n"
      "v_add_f32 v128, v128, v222\n"
      "v_mfma_f32_16x16x32_bf16 v[160:163], v[244:247], v[170:173], v[160:163]\n"
      "v_max_f32_dpp v209, v209, v209 row_ror:1 row_mask:0xf bank_mask:0xf\n"
      "v_cvt_pk_bf16_f32 v193, v194, v195\n"
      "v_mfma_f32_16x16x32_bf16 v[160:163], v[248:251], v[100:103], v[160:163]\n"
      "v_max_f32_dpp v209, v209, v209 row_bcast:15 row_mask:0xa bank_mask:0xf\n"
      "v_mul_f32_e64 v165, v157, v157\n"
      "v_mfma_f32_16x16x32_bf16 v[160:163], v[252:255], v[150:153], v[160:163]\n"
      "v_max_f32_dpp v209, v209, v209 row_bcast:31 row_mask:0xc bank_mask:0xf\n"
      "v_mul_f32_e64 v168, v208, v168\n"
      "v_mfma_f32_16x16x32_bf16 v[150:153], v[32:35], v[188:191], 0\n"
      "v_cvt_pk_bf16_f32 v195, v168, v169\n"
      "v_cvt_pk_bf16_f32 v194, v166, v167\n"
      "v_mfma_f32_16x16x32_bf16 v[150:153], v[32:35], v[88:91], v[150:153]\n"
      "ds_write_b128 v3, v[192:195] offset:256\n"
      "v_pk_mul_f32 v[210:211], v[210:211], v[208:209] op_sel_hi:[1,0]\n"
      "v_pk_fma_f32 v[164:165], v[158:159], v[158:159], v[164:165]\n"
      "v_pk_mul_f32 v[212:213], v[212:213], v[208:209] op_sel_hi:[1,0]\n"
      "v_pk_fma_f32 v[164:165], v[146:147], v[146:147], v[164:165]\n"
      "v_mfma_f32_16x16x32_bf16 v[174:177], v[130:133], v[188:191], 0\n"
      "v_cvt_pk_bf16_f32 v210, v210, v211\n"
      "v_readlane_b32 s72, v209, 63\n"
      "v_mfma_f32_16x16x32_bf16 v[174:177], v[76:79], v[88:91], v[174:177]\n"
      "s_nop 0\n"
      "v_subrev_f32 v218, s72, v218\n"
      "v_cvt_pk_bf16_f32 v211, v212, v213\n"
      "v_mfma_f32_16x16x32_bf16 v[168:171], v[196:199], v[188:191], 0\n"
      "v_exp_f32_e64 v218, v218\n"
      "v_mfma_f32_16x16x32_bf16 v[168:171], v[80:83], v[88:91], v[168:171]\n"
      "ds_write_b64 v4, v[210:211] offset:256\n"
      "v_rcp_f32_e64 v150, v150\n"
      "v_pk_fma_f32 v[164:165], v[148:149], v[148:149], v[164:165]\n"
      "v_pk_mul_f32 v[174:175], v[174:175], v[150:151] op_sel_hi:[1,0]\n"
      "v_pk_fma_f32 v[164:165], v[160:161], v[160:161], v[164:165]\n"
      "v_cvt_pk_bf16_f32 v218, v218, v218\n"
      "v_pk_mul_f32 v[176:177], v[176:177], v[150:151] op_sel_hi:[1,0]\n"
      "v_pk_fma_f32 v[164:165], v[162:163], v[162:163], v[164:165]\n"
      "v_pk_mul_f32 v[168:169], v[168:169], v[150:151] op_sel_hi:[1,0]\n"
      "v_add_f32 v164, v164, v165\n"
      "v_cvt_pk_bf16_f32 v174, v174, v175\n"
      "v_mov_b32 v222, v164\n"
      "v_mfma_f32_16x16x32_bf16 v[208:211], v[214:217], v[188:191], 0\n"
      "ds_write_b16 v13, v218\n"
      "v_cvt_pk_bf16_f32 v175, v176, v177\n"
      "v_permlane16_swap_b32_e64 v164, v222\n"
      "v_pk_mul_f32 v[170:171], v[170:171], v[150:151] op_sel_hi:[1,0]\n"
      "v_cvt_pk_bf16_f32 v177, v170, v171\n"
      "v_add_f32_e64 v164, v164, v222\n"
      "v_cvt_pk_bf16_f32 v176, v168, v169\n"
      "v_mfma_f32_16x16x32_bf16 v[208:211], v[84:87], v[88:91], v[208:211]\n"
      "ds_write_b128 v3, v[174:177] offset:512\n"
      "ds_read_b128 v[218:221], v14\n"
      "ds_read_b128 v[174:177], v14 offset:64\n"
      "v_permlane32_swap_b32 v128, v164\n"
      "v_add_f32_e64 v128, v128, v164\n"
      "v_readlane_b32 s86, v38, s43\n"
      "s_add_u32 s43, s43, 1\n"
      "ds_write_b32 v31, v128\n"
      "v_pk_mul_f32 v[208:209], v[208:209], v[150:151] op_sel_hi:[1,0]\n"
      "v_cvt_pk_bf16_f32 v208, v208, v209\n"
      "v_pk_mul_f32 v[210:211], v[210:211], v[150:151] op_sel_hi:[1,0]\n"
      "v_cvt_pk_bf16_f32 v209, v210, v211\n"
      "s_and_b32 s86, s86, -4\n"
      "ds_write_b64 v4, v[208:209] offset:512\n"
      "v_add_u32_e64 v12, 32, v12\n"
      "s_cmp_ge_u32 s95, 0x40000000\n"
      "s_cselect_b64 vcc, -1, s[56:57]\n"
      "s_waitcnt lgkmcnt(3)\n"
      "v_mfma_f32_16x16x32_bf16 v[208:211], v[218:221], v[32:35], 0\n"
      "s_waitcnt lgkmcnt(2)\n"
      "v_mfma_f32_16x16x32_bf16 v[208:211], v[174:177], v[32:35], v[208:211]\n"
      "s_waitcnt lgkmcnt(0)\n"
      "s_barrier\n"
      "s_cbranch_vccz L_sel1_%=\n"
      "s_branch L_disp1_%=\n"
      "L_tail1_%=:\n"
      "s_nop 4\n"
      "ds_read_b128 v[164:167], v9\n"
      "v_mfma_f32_16x16x32_bf16 v[168:171], v[218:221], v[130:133], 0\n"
      "ds_read_b128 v[130:133], v2\n"
      "ds_read_b128 v[186:189], v2 offset:6144\n"
      "v_mfma_f32_16x16x32_bf16 v[168:171], v[174:177], v[76:79], v[168:171]\n"
      "ds_read_b128 v[104:107], v2 offset:6400\n"
      "ds_read_b128 v[178:181], v2 offset:3072\n"
      "v_mfma_f32_16x16x32_bf16 v[150:153], v[218:221], v[196:199], 0\n"
      "ds_read_b128 v[138:141], v7\n"
      "ds_read_b128 v[190:193], v2 offset:12288\n"
      "ds_read_b128 v[108:111], v2 offset:3328\n"
      "v_mfma_f32_16x16x32_bf16 v[150:153], v[174:177], v[80:83], v[150:153]\n"
      "ds_read_b128 v[100:103], v2 offset:9216\n"
      "v_mfma_f32_16x16x32_bf16 v[194:197], v[218:221], v[214:217], 0\n"
      "s_waitcnt lgkmcnt(8)\n"
      "v_add_f32 v166, v166, v167\n"
      "v_add_f32 v164, v164, v165\n"
      "v_add_f32 v164, v164, v166\n"
      "v_rsq_f32 v164, v164\n"
      "v_mfma_f32_16x16x32_bf16 v[194:197], v[174:177], v[84:87], v[194:197]\n"
      "s_waitcnt lgkmcnt(7)\n"
      "v_mul_f32 v164, 0x415db3d7, v164\n"
      "v_mov_b32 v166, v164\n"
      "s_nop 1\n"
      "v_permlane32_swap_b32_e64 v164, v166\n"
      "v_pk_mul_f32 v[160:161], v[160:161], v[166:167] op_sel_hi:[1,0]\n"
      "v_mov_b32_e64 v165, v164\n"
      "v_pk_mul_f32 v[158:159], v[158:159], v[166:167] op_sel_hi:[1,0]\n"
      "v_pk_mul_f32 v[162:163], v[162:163], v[166:167] op_sel_hi:[1,0]\n"
      "v_permlane16_swap_b32_e64 v164, v165\n"
      "v_pk_mul_f32 v[200:201], v[200:201], v[164:165] op_sel:[0,1]\n"
      "v_pk_mul_f32 v[136:137], v[136:137], v[164:165] op_sel_hi:[1,0]\n"
      "v_pk_mul_f32 v[148:149], v[148:149], v[166:167] op_sel_hi:[1,0]\n"
      "v_pk_mul_f32 v[206:207], v[206:207], v[164:165] op_sel:[0,1]\n"
      "v_pk_mul_f32 v[96:97], v[96:97], v[164:165] op_sel_hi:[1,0]\n"
      "v_pk_mul_f32 v[134:135], v[134:135], v[164:165] op_sel_hi:[1,0]\n"
      "v_pk_mul_f32 v[156:157], v[156:157], v[166:167] op_sel_hi:[1,0]\n"
      "v_pk_mul_f32 v[146:147], v[146:147], v[166:167] op_sel_hi:[1,0]\n"
      "v_pk_mul_f32 v[98:99], v[98:99], v[164:165] op_sel_hi:[1,0]\n"
      "v_cvt_pk_bf16_f32 v134, v134, v135\n"
      "v_pk_mul_f32 v[112:113], v[112:113], v[164:165] op_sel:[0,1]\n"
      "v_cvt_pk_bf16_f32 v200, v200, v201\n"
      "v_cvt_pk_bf16_f32 v156, v156, v157\n"
      "v_pk_mul_f32 v[114:115], v[114:115], v[164:165] op_sel:[0,1]\n"
      "v_cvt_pk_bf16_f32 v96, v96, v97\n"
      "v_pk_mul_f32 v[202:203], v[202:203], v[164:165] op_sel:[0,1]\n"
      "v_pk_mul_f32 v[204:205], v[204:205], v[164:165] op_sel:[0,1]\n"
      "v_cvt_pk_bf16_f32 v204, v204, v205\n"
      "v_cvt_pk_bf16_f32 v201, v202, v203\n"
      "v_pk_mul_f32 v[116:117], v[116:117], v[164:165] op_sel_hi:[1,0]\n"
      "v_pk_mul_f32 v[118:119], v[118:119], v[164:165] op_sel_hi:[1,0]\n"
      "v_cvt_pk_bf16_f32 v135, v136, v137\n"
      "v_cvt_pk_bf16_f32 v157, v158, v159\n"
      "v_cvt_pk_bf16_f32 v203, v114, v115\n"
      "v_cvt_pk_bf16_f32 v205, v206, v207\n"
      "v_cvt_pk_bf16_f32 v160, v160, v161\n"
      "v_cvt_pk_bf16_f32 v97, v98, v99\n"
      "v_cvt_pk_bf16_f32 v99, v118, v119\n"
      "v_cvt_pk_bf16_f32 v158, v146, v147\n"
      "v_cvt_pk_bf16_f32 v161, v162, v163\n"
      "v_cvt_pk_bf16_f32 v202, v112, v113\n"
      "v_cvt_pk_bf16_f32 v159, v148, v149\n"
      "v_cvt_pk_bf16_f32 v98, v116, v117\n"
      "s_mul_i32 s92, s40, 3072\n"
      "ds_write_b128 v5, v[156:159] offset:30720\n"
      "ds_write_b128 v5, v[200:203] offset:24576\n"
      "ds_write_b64 v6, v[160:161] offset:30720\n"
      "ds_write_b64 v6, v[134:135] offset:18432\n"
      "ds_write_b128 v5, v[96:99] offset:18432\n"
      "ds_write_b64 v6, v[204:205] offset:24576\n"
      "v_mfma_f32_16x16x32_bf16 v[96:99], a[216:219], v[130:133], 0\n"
      "ds_read_b128 v[200:203], v2 offset:15360\n"
      "s_waitcnt lgkmcnt(6)\n"
      "v_mfma_f32_16x16x32_bf16 v[96:99], a[220:223], v[178:181], v[96:99]\n"
      "ds_read_b128 v[196:199], v2 offset:15872\n"
      "v_mov_b32_dpp v208, v208 quad_perm:[0,0,0,0] row_mask:0xf bank_mask:0xf\n"
      "v_mfma_f32_16x16x32_bf16 v[116:119], a[240:243], v[130:133], 0\n"
      "ds_read_b128 v[156:159], v7 offset:8192\n"
      "v_subrev_u32 v210, 115456, v9\n"
      "v_mfma_f32_16x16x32_bf16 v[116:119], a[244:247], v[178:181], v[116:119]\n"
      "ds_read_b128 v[216:219], v2 offset:12800\n"
      "v_subrev_u32 v81, 115456, v9\n"
      "v_mfma_f32_16x16x32_bf16 v[134:137], v[232:235], v[130:133], 0\n"
      "v_add_u32 v85, s92, v210\n"
      "v_mfma_f32_16x16x32_bf16 v[134:137], v[236:239], v[178:181], v[134:137]\n"
      "buffer_store_dwordx4 v[138:141], v20, s[24:27], s85 offen sc1\n"
      "v_mfma_f32_16x16x32_bf16 v[96:99], a[224:227], v[186:189], v[96:99]\n"
      "ds_read_b128 v[146:149], v2 offset:256\n"
      "v_lshrrev_b32_e64 v210, 4, v85\n"
      "v_mfma_f32_16x16x32_bf16 v[96:99], a[228:231], v[100:103], v[96:99]\n"
      "v_and_b32 v178, 0x3fffffff, v37\n"
      "v_mfma_f32_16x16x32_bf16 v[116:119], a[248:251], v[186:189], v[116:119]\n"
      "ds_read_b128 v[160:163], v2 offset:15616\n"
      "v_mfma_f32_16x16x32_bf16 v[116:119], a[252:255], v[100:103], v[116:119]\n"
      "ds_read_b128 v[130:133], v7 offset:4096\n"
      "v_mfma_f32_16x16x32_bf16 v[134:137], v[240:243], v[186:189], v[134:137]\n"
      "ds_read_b128 v[174:177], v2 offset:12544\n"
      "v_mfma_f32_16x16x32_bf16 v[134:137], v[244:247], v[100:103], v[134:137]\n"
      "ds_read_b128 v[212:215], v2 offset:6656\n"
      "v_mfma_f32_16x16x32_bf16 v[96:99], a[232:235], v[190:193], v[96:99]\n"
      "s_waitcnt lgkmcnt(6)\n"
      "v_add_u32 v180, 128, v210\n"
      "v_add_u32 v179, 64, v210\n"
      "v_mfma_f32_16x16x32_bf16 v[96:99], a[236:239], v[200:203], v[96:99]\n"
      "ds_read_b128 v[152:155], v2 offset:9472\n"
      "v_mfma_f32_16x16x32_bf16 v[116:119], v[224:227], v[190:193], v[116:119]\n"
      "v_ashrrev_i32 v84, 31, v37\n"
      "v_and_b32 v102, 0x300, v81\n"
      "v_mfma_f32_16x16x32_bf16 v[116:119], v[228:231], v[200:203], v[116:119]\n"
      "ds_read_b128 v[170:173], v2 offset:512\n"
      "v_add_u32 v81, v81, v102\n"
      "v_mfma_f32_16x16x32_bf16 v[134:137], v[248:251], v[190:193], v[134:137]\n"
      "v_cndmask_b32 v168, v168, v150, s[58:59]\n"
      "v_mfma_f32_16x16x32_bf16 v[134:137], v[252:255], v[200:203], v[134:137]\n"
      "s_waitcnt lgkmcnt(3)\n"
      "v_add_u32 v81, 150784, v81\n"
      "v_fma_f32 v102, v96, v96, s52\n"
      "v_mfma_f32_16x16x32_bf16 v[200:203], a[216:219], v[146:149], 0\n"
      "v_mul_f32 v103, v97, v97\n"
      "v_mul_u32_u24 v82, 2731, v180\n"
      "v_mfma_f32_16x16x32_bf16 v[200:203], a[220:223], v[108:111], v[200:203]\n"
      "v_subrev_u32 v86, 73728, v6\n"
      "v_fmac_f32_e64 v103, v99, v99\n"
      "v_mfma_f32_16x16x32_bf16 v[112:115], a[240:243], v[146:149], 0\n"
      "ds_read_b128 v[220:223], v2 offset:9728\n"
      "v_fmac_f32 v102, v98, v98\n"
      "v_mfma_f32_16x16x32_bf16 v[112:115], a[244:247], v[108:111], v[112:115]\n"
      "v_fmac_f32 v103, v117, v117\n"
      "v_fmac_f32 v102, v116, v116\n"
      "v_mfma_f32_16x16x32_bf16 v[204:207], v[232:235], v[146:149], 0\n"
      "ds_read_b128 v[164:167], v2 offset:3584\n"
      "v_fmac_f32 v103, v119, v119\n"
      "v_mfma_f32_16x16x32_bf16 v[204:207], v[236:239], v[108:111], v[204:207]\n"
      "s_waitcnt lgkmcnt(2)\n"
      "v_add_u32 v178, 1179648, v178\n"
      "v_lshrrev_b32 v82, 16, v82\n"
      "v_mfma_f32_16x16x32_bf16 v[200:203], a[224:227], v[104:107], v[200:203]\n"
      "ds_read_b128 v[108:111], v81 offset:4096\n"
      "v_mfma_f32_16x16x32_bf16 v[200:203], a[228:231], v[152:155], v[200:203]\n"
      "buffer_store_dwordx4 v[130:133], v21, s[24:27], s85 offen sc1\n"
      "v_mfma_f32_16x16x32_bf16 v[112:115], a[248:251], v[104:107], v[112:115]\n"
      "s_nop 0\n"
      "ds_read_b128 v[130:133], v7 offset:16384\n"
      "v_and_b32 v84, 100270464, v84\n"
      "v_mfma_f32_16x16x32_bf16 v[112:115], a[252:255], v[152:155], v[112:115]\n"
      "v_fmac_f32 v102, v118, v118\n"
      "v_sub_u32_e64 v178, v178, v84\n"
      "v_mfma_f32_16x16x32_bf16 v[204:207], v[240:243], v[104:107], v[204:207]\n"
      "v_rcp_f32 v208, v208\n"
      "v_mfma_f32_16x16x32_bf16 v[204:207], v[244:247], v[152:155], v[204:207]\n"
      "v_mul_u32_u24 v80, 2731, v179\n"
      "v_fmac_f32 v102, v134, v134\n"
      "v_mfma_f32_16x16x32_bf16 v[200:203], a[232:235], v[174:177], v[200:203]\n"
      "v_cndmask_b32 v168, v168, v194, s[60:61]\n"
      "v_fmac_f32 v103, v135, v135\n"
      "v_mfma_f32_16x16x32_bf16 v[200:203], a[236:239], v[160:163], v[200:203]\n"
      "v_add_u32 v78, 0, v210\n"
      "v_mul_u32_u24 v76, 24, v82\n"
      "v_mfma_f32_16x16x32_bf16 v[112:115], v[224:227], v[174:177], v[112:115]\n"
      "v_mul_u32_u24 v77, 2731, v78\n"
      "v_mul_f32 v168, v208, v168\n"
      "v_mfma_f32_16x16x32_bf16 v[112:115], v[228:231], v[160:163], v[112:115]\n"
      "buffer_store_dwordx4 v[156:159], v22, s[24:27], s85 offen sc1\n"
      "v_mfma_f32_16x16x32_bf16 v[204:207], v[248:251], v[174:177], v[204:207]\n"
      "ds_read_b128 v[174:177], v81 offset:6144\n"
      "v_mul_f32_e64 v189, v201, v201\n"
      "v_mfma_f32_16x16x32_bf16 v[204:207], v[252:255], v[160:163], v[204:207]\n"
      "v_fma_f32 v188, v200, v200, s52\n"
      "v_fmac_f32 v189, v203, v203\n"
      "v_mfma_f32_16x16x32_bf16 v[156:159], a[216:219], v[170:173], 0\n"
      "s_waitcnt lgkmcnt(1)\n"
      "v_cvt_pk_bf16_f32 v168, v168, v168\n"
      "v_fmac_f32 v102, v136, v136\n"
      "v_mfma_f32_16x16x32_bf16 v[156:159], a[220:223], v[164:167], v[156:159]\n"
      "v_fmac_f32 v188, v202, v202\n"
      "v_mfma_f32_16x16x32_bf16 v[146:149], a[240:243], v[170:173], 0\n"
      "ds_read_b128 v[208:211], v81 offset:10240\n"
      "v_fmac_f32 v188, v112, v112\n"
      "v_mfma_f32_16x16x32_bf16 v[146:149], a[244:247], v[164:167], v[146:149]\n"
      "ds_read_b128 v[150:153], v81 offset:8192\n"
      "v_lshrrev_b32 v77, 16, v77\n"
      "v_mfma_f32_16x16x32_bf16 v[160:163], v[232:235], v[170:173], 0\n"
      "ds_write_b16 v12, v168\n"
      "v_mfma_f32_16x16x32_bf16 v[160:163], v[236:239], v[164:167], v[160:163]\n"
      "ds_read_b128 v[168:171], v7 offset:12288\n"
      "v_fmac_f32 v189, v113, v113\n"
      "v_mfma_f32_16x16x32_bf16 v[156:159], a[224:227], v[212:215], v[156:159]\n"
      "v_fmac_f32 v189, v115, v115\n"
      "v_fmac_f32 v188, v114, v114\n"
      "v_mfma_f32_16x16x32_bf16 v[156:159], a[228:231], v[220:223], v[156:159]\n"
      "v_fmac_f32 v103, v137, v137\n"
      "v_mfma_f32_16x16x32_bf16 v[146:149], a[248:251], v[212:215], v[146:149]\n"
      "ds_read_b128 v[192:195], v81\n"
      "v_mfma_f32_16x16x32_bf16 v[146:149], a[252:255], v[220:223], v[146:149]\n"
      "ds_read_b128 v[104:107], v81 offset:2048\n"
      "v_add_f32 v102, v102, v103\n"
      "v_mfma_f32_16x16x32_bf16 v[160:163], v[240:243], v[212:215], v[160:163]\n"
      "v_fmac_f32 v188, v204, v204\n"
      "v_fmac_f32 v189, v205, v205\n"
      "v_mfma_f32_16x16x32_bf16 v[160:163], v[244:247], v[220:223], v[160:163]\n"
      "v_fmac_f32 v189, v207, v207\n"
      "v_fmac_f32 v188, v206, v206\n"
      "v_mfma_f32_16x16x32_bf16 v[156:159], a[232:235], v[216:219], v[156:159]\n"
      "s_waitcnt lgkmcnt(2)\n"
      "v_add_f32 v188, v188, v189\n"
      "v_mul_u32_u24 v172, 24, v77\n"
      "v_mfma_f32_16x16x32_bf16 v[156:159], a[236:239], v[196:199], v[156:159]\n"
      "buffer_store_dwordx4 v[168:171], v23, s[24:27], s85 offen sc1\n"
      "v_mfma_f32_16x16x32_bf16 v[146:149], v[224:227], v[216:219], v[146:149]\n"
      "v_permlane16_swap_b32_e64 v102, v188\n"
      "v_mfma_f32_16x16x32_bf16 v[146:149], v[228:231], v[196:199], v[146:149]\n"
      "v_bfe_i32 v84, v37, 30, 1\n"
      "v_add_f32 v102, v102, v188\n"
      "v_mfma_f32_16x16x32_bf16 v[160:163], v[248:251], v[216:219], v[160:163]\n"
      "s_nop 0\n"
      "v_mul_f32_e64 v169, v157, v157\n"
      "v_fma_f32 v168, v156, v156, s52\n"
      "v_mfma_f32_16x16x32_bf16 v[160:163], v[252:255], v[196:199], v[160:163]\n"
      "s_waitcnt lgkmcnt(0)\n"
      "v_fmac_f32 v169, v159, v159\n"
      "v_fmac_f32_e64 v168, v158, v158\n"
      "v_mfma_f32_16x16x32_bf16 v[196:199], a[216:219], v[192:195], 0\n"
      "v_fmac_f32 v169, v147, v147\n"
      "v_fmac_f32 v168, v146, v146\n"
      "v_mfma_f32_16x16x32_bf16 v[196:199], a[220:223], v[104:107], v[196:199]\n"
      "v_fmac_f32 v169, v149, v149\n"
      "v_fmac_f32 v168, v148, v148\n"
      "v_mfma_f32_16x16x32_bf16 v[216:219], a[240:243], v[192:195], 0\n"
      "buffer_store_dwordx4 v[130:133], v25, s[24:27], s85 offen sc1\n"
      "v_pk_fma_f32 v[168:169], v[160:161], v[160:161], v[168:169]\n"
      "v_pk_fma_f32 v[168:169], v[162:163], v[162:163], v[168:169]\n"
      "v_mfma_f32_16x16x32_bf16 v[216:219], a[244:247], v[104:107], v[216:219]\n"
      "v_add_f32 v168, v168, v169\n"
      "v_mov_b32 v188, v168\n"
      "v_mfma_f32_16x16x32_bf16 v[130:133], v[232:235], v[192:195], 0\n"
      "s_nop 0\n"
      "v_permlane16_swap_b32 v168, v188\n"
      "v_mfma_f32_16x16x32_bf16 v[130:133], v[236:239], v[104:107], v[130:133]\n"
      "v_add_f32 v168, v168, v188\n"
      "v_mfma_f32_16x16x32_bf16 v[196:199], a[224:227], v[108:111], v[196:199]\n"
      "s_nop 0\n"
      "v_permlane32_swap_b32 v102, v168\n"
      "v_add_f32 v102, v102, v168\n"
      "v_mfma_f32_16x16x32_bf16 v[196:199], a[228:231], v[174:177], v[196:199]\n"
      "ds_write_b32 v31, v102 offset:1024\n"
      "v_mfma_f32_16x16x32_bf16 v[216:219], a[248:251], v[108:111], v[216:219]\n"
      "v_subrev_u32 v173, 73728, v5\n"
      "v_mfma_f32_16x16x32_bf16 v[216:219], a[252:255], v[174:177], v[216:219]\n"
      "v_sub_u32 v76, v180, v76\n"
      "v_readlane_b32 s85, v37, s42\n"
      "v_mfma_f32_16x16x32_bf16 v[130:133], v[240:243], v[108:111], v[130:133]\n"
      "s_or_b32 s95, s85, s48\n"
      "v_lshrrev_b32 v80, 16, v80\n"
      "v_sub_u32 v172, v78, v172\n"
      "v_and_b32 v180, 7, v82\n"
      "v_mfma_f32_16x16x32_bf16 v[130:133], v[244:247], v[174:177], v[130:133]\n"
      "v_xor_b32 v76, v76, v180\n"
      "v_mfma_f32_16x16x32_bf16 v[196:199], a[232:235], v[150:153], v[196:199]\n"
      "s_cmp_ge_u32 s95, 0x40000000\n"
      "s_cbranch_scc1 L_rare_27_%=\n"
      "L_back_28_%=:\n"
      "v_mfma_f32_16x16x32_bf16 v[196:199], a[236:239], v[208:211], v[196:199]\n"
      "s_waitcnt lgkmcnt(0)\n"
      "s_barrier\n"
      "ds_read_b128 v[174:177], v9 offset:1024\n"
      "v_mfma_f32_16x16x32_bf16 v[216:219], v[224:227], v[150:153], v[216:219]\n"
      "ds_read_b128 v[168:171], v81 offset:256\n"
      "ds_read_b128 v[212:215], v81 offset:2304\n"
      "v_mfma_f32_16x16x32_bf16 v[216:219], v[228:231], v[208:211], v[216:219]\n"
      "ds_read_b128 v[220:223], v7 offset:18432\n"
      "v_fma_f32 v180, v196, v196, s52\n"
      "v_mul_f32_e64 v181, v197, v197\n"
      "v_mfma_f32_16x16x32_bf16 v[130:133], v[248:251], v[150:153], v[130:133]\n"
      "ds_read_b128 v[150:153], v7 offset:22528\n"
      "ds_read_b128 v[192:195], v7 offset:26624\n"
      "ds_read_b128 v[102:105], v81 offset:4352\n"
      "v_pk_fma_f32 v[180:181], v[198:199], v[198:199], v[180:181]\n"
      "v_pk_fma_f32 v[180:181], v[216:217], v[216:217], v[180:181]\n"
      "v_mfma_f32_16x16x32_bf16 v[130:133], v[252:255], v[208:211], v[130:133]\n"
      "ds_read_b128 v[208:211], v7 offset:34816\n"
      "s_waitcnt lgkmcnt(7)\n"
      "v_add_f32 v174, v174, v175\n"
      "v_add_f32 v176, v176, v177\n"
      "v_add_f32 v174, v174, v176\n"
      "v_rsq_f32_e64 v174, v174\n"
      "s_waitcnt lgkmcnt(6)\n"
      "v_mfma_f32_16x16x32_bf16 v[106:109], a[216:219], v[168:171], 0\n"
      "s_waitcnt lgkmcnt(5)\n"
      "v_mul_f32 v174, 0x415db3d7, v174\n"
      "v_mov_b32_e64 v176, v174\n"
      "v_mfma_f32_16x16x32_bf16 v[106:109], a[220:223], v[212:215], v[106:109]\n"
      "ds_read_b128 v[138:141], v81 offset:6400\n"
      "ds_read_b128 v[164:167], v81 offset:8448\n"
      "v_mfma_f32_16x16x32_bf16 v[186:189], a[240:243], v[168:171], 0\n"
      "s_waitcnt lgkmcnt(5)\n"
      "v_permlane32_swap_b32 v174, v176\n"
      "v_mfma_f32_16x16x32_bf16 v[186:189], a[244:247], v[212:215], v[186:189]\n"
      "buffer_store_dwordx4 v[220:223], v20, s[24:27], s85 offen sc1\n"
      "v_mov_b32_e64 v175, v174\n"
      "v_pk_mul_f32 v[156:157], v[156:157], v[176:177] op_sel_hi:[1,0]\n"
      "v_pk_mul_f32 v[158:159], v[158:159], v[176:177] op_sel_hi:[1,0]\n"
      "v_mfma_f32_16x16x32_bf16 v[220:223], v[232:235], v[168:171], 0\n"
      "s_waitcnt lgkmcnt(2)\n"
      "v_permlane16_swap_b32_e64 v174, v175\n"
      "v_mfma_f32_16x16x32_bf16 v[220:223], v[236:239], v[212:215], v[220:223]\n"
      "buffer_store_dwordx4 v[150:153], v21, s[24:27], s85 offen sc1\n"
      "v_pk_fma_f32 v[180:181], v[218:219], v[218:219], v[180:181]\n"
      "v_pk_mul_f32 v[162:163], v[162:163], v[176:177] op_sel_hi:[1,0]\n"
      "v_pk_mul_f32 v[146:147], v[146:147], v[176:177] op_sel_hi:[1,0]\n"
      "v_pk_mul_f32 v[96:97], v[96:97], v[174:175] op_sel_hi:[1,0]\n"
      "v_pk_mul_f32 v[98:99], v[98:99], v[174:175] op_sel_hi:[1,0]\n"
      "v_pk_mul_f32 v[160:161], v[160:161], v[176:177] op_sel_hi:[1,0]\n"
      "v_pk_mul_f32 v[134:135], v[134:135], v[174:175] op_sel_hi:[1,0]\n"
      "v_pk_mul_f32 v[206:207], v[206:207], v[174:175] op_sel:[0,1]\n"
      "v_mfma_f32_16x16x32_bf16 v[106:109], a[224:227], v[102:105], v[106:109]\n"
      "s_waitcnt lgkmcnt(0)\n"
      "v_cvt_pk_bf16_f32 v96, v96, v97\n"
      "v_cvt_pk_bf16_f32 v160, v160, v161\n"
      "v_mfma_f32_16x16x32_bf16 v[106:109], a[228:231], v[138:141], v[106:109]\n"
      "buffer_store_dwordx4 v[192:195], v22, s[24:27], s85 offen sc1\n"
      "v_pk_mul_f32 v[202:203], v[202:203], v[174:175] op_sel:[0,1]\n"
      "v_pk_mul_f32 v[116:117], v[116:117], v[174:175] op_sel_hi:[1,0]\n"
      "v_pk_mul_f32 v[204:205], v[204:205], v[174:175] op_sel:[0,1]\n"
      "v_mfma_f32_16x16x32_bf16 v[186:189], a[248:251], v[102:105], v[186:189]\n"
      "ds_read_b128 v[168:171], v81 offset:10496\n"
      "v_cvt_pk_bf16_f32 v97, v98, v99\n"
      "v_mfma_f32_16x16x32_bf16 v[186:189], a[252:255], v[138:141], v[186:189]\n"
      "v_cvt_pk_bf16_f32 v161, v162, v163\n"
      "v_cvt_pk_bf16_f32 v204, v204, v205\n"
      "v_cvt_pk_bf16_f32 v98, v116, v117\n"
      "v_cvt_pk_bf16_f32 v156, v156, v157\n"
      "v_cvt_pk_bf16_f32 v157, v158, v159\n"
      "v_pk_mul_f32 v[136:137], v[136:137], v[174:175] op_sel_hi:[1,0]\n"
      "v_pk_mul_f32 v[148:149], v[148:149], v[176:177] op_sel_hi:[1,0]\n"
      "v_pk_mul_f32 v[118:119], v[118:119], v[174:175] op_sel_hi:[1,0]\n"
      "v_pk_mul_f32 v[200:201], v[200:201], v[174:175] op_sel:[0,1]\n"
      "v_mfma_f32_16x16x32_bf16 v[220:223], v[240:243], v[102:105], v[220:223]\n"
      "ds_read_b128 v[150:153], v7 offset:30720\n"
      "v_cvt_pk_bf16_f32 v158, v146, v147\n"
      "v_mfma_f32_16x16x32_bf16 v[220:223], v[244:247], v[138:141], v[220:223]\n"
      "ds_write_b64 v6, v[160:161] offset:12288\n"
      "v_cvt_pk_bf16_f32 v159, v148, v149\n"
      "v_mfma_f32_16x16x32_bf16 v[106:109], a[232:235], v[164:167], v[106:109]\n"
      "s_waitcnt lgkmcnt(2)\n"
      "v_cvt_pk_bf16_f32 v134, v134, v135\n"
      "v_cvt_pk_bf16_f32 v99, v118, v119\n"
      "v_mfma_f32_16x16x32_bf16 v[106:109], a[236:239], v[168:171], v[106:109]\n"
      "ds_write_b128 v5, v[96:99]\n"
      "v_mfma_f32_16x16x32_bf16 v[186:189], v[224:227], v[164:167], v[186:189]\n"
      "v_cvt_pk_bf16_f32 v135, v136, v137\n"
      "v_cvt_pk_bf16_f32 v205, v206, v207\n"
      "v_mfma_f32_16x16x32_bf16 v[186:189], v[228:231], v[168:171], v[186:189]\n"
      "ds_write_b64 v6, v[204:205] offset:6144\n"
      "v_mfma_f32_16x16x32_bf16 v[220:223], v[248:251], v[164:167], v[220:223]\n"
      "ds_write_b64 v6, v[134:135]\n"
      "v_mfma_f32_16x16x32_bf16 v[220:223], v[252:255], v[168:171], v[220:223]\n"
      "ds_write_b128 v5, v[156:159] offset:12288\n"
      "v_fma_f32 v158, v106, v106, s52\n"
      "v_mul_f32 v159, v107, v107\n"
      "v_pk_fma_f32 v[158:159], v[108:109], v[108:109], v[158:159]\n"
      "v_pk_fma_f32 v[158:159], v[186:187], v[186:187], v[158:159]\n"
      "v_pk_fma_f32 v[158:159], v[188:189], v[188:189], v[158:159]\n"
      "v_pk_fma_f32 v[180:181], v[130:131], v[130:131], v[180:181]\n"
      "v_pk_fma_f32 v[180:181], v[132:133], v[132:133], v[180:181]\n"
      "v_pk_fma_f32 v[158:159], v[220:221], v[220:221], v[158:159]\n"
      "v_pk_fma_f32 v[158:159], v[222:223], v[222:223], v[158:159]\n"
      "v_add_f32 v180, v180, v181\n"
      "v_add_f32 v158, v158, v159\n"
      "v_cvt_pk_bf16_f32 v200, v200, v201\n"
      "v_pk_mul_f32 v[112:113], v[112:113], v[174:175] op_sel:[0,1]\n"
      "v_cvt_pk_bf16_f32 v201, v202, v203\n"
      "v_permlane16_swap_b32 v180, v158\n"
      "v_add_f32 v180, v180, v158\n"
      "v_mov_b32 v158, v180\n"
      "v_pk_mul_f32 v[114:115], v[114:115], v[174:175] op_sel:[0,1]\n"
      "v_cvt_pk_bf16_f32 v203, v114, v115\n"
      "v_cvt_pk_bf16_f32 v202, v112, v113\n"
      "v_permlane32_swap_b32 v180, v158\n"
      "v_add_f32_e64 v180, v180, v158\n"
      "s_add_u32 s42, s42, 1\n"
      "ds_write_b128 v5, v[200:203] offset:6144\n"
      "ds_write_b32 v31, v180 offset:4608\n"
      "s_waitcnt lgkmcnt(2)\n"
      "buffer_store_dwordx4 v[150:153], v23, s[24:27], s85 offen sc1\n"
      "v_mul_u32_u24 v175, 24, v80\n"
      "v_and_b32 v84, 196224, v84\n"
      "v_and_b32 v78, 7, v77\n"
      "v_sub_u32_e64 v175, v179, v175\n"
      "v_lshl_add_u32 v77, v77, 2, 12\n"
      "v_and_b32_e64 v179, 7, v80\n"
      "buffer_store_dwordx4 v[208:211], v25, s[24:27], s85 offen sc1\n"
      "s_waitcnt lgkmcnt(0)\n"
      "s_barrier\n"
      "ds_read_b128 v[208:211], v9 offset:4608\n"
      "v_lshl_add_u32 v82, v82, 2, 12\n"
      "v_xor_b32_e64 v175, v175, v179\n"
      "v_readlane_b32 s85, v37, s42\n"
      "v_sub_u32 v178, v178, v84\n"
      "s_or_b32 s95, s85, s48\n"
      "ds_read_b128 v[200:203], v7\n"
      "ds_read_b128 v[112:115], v7 offset:8192\n"
      "ds_bpermute_b32 v84, v82, v178\n"
      "v_lshl_add_u32 v80, v80, 2, 12\n"
      "s_cmp_ge_u32 s95, 0x40000000\n"
      "s_cbranch_scc1 L_rare_31_%=\n"
      "L_back_32_%=:\n"
      "ds_read_b128 v[134:137], v7 offset:12288\n"
      "ds_read_b128 v[156:159], v7 offset:4096\n"
      "s_waitcnt lgkmcnt(5)\n"
      "v_add_f32 v210, v210, v211\n"
      "v_add_f32 v208, v208, v209\n"
      "v_add_f32 v208, v208, v210\n"
      "v_rsq_f32 v208, v208\n"
      "s_nop 0\n"
      "v_mul_f32 v208, 0x415db3d7, v208\n"
      "v_mov_b32 v210, v208\n"
      "s_nop 1\n"
      "v_permlane32_swap_b32 v208, v210\n"
      "v_mov_b32_e64 v209, v208\n"
      "s_waitcnt lgkmcnt(2)\n"
      "buffer_store_dwordx4 v[200:203], v20, s[24:27], s85 offen sc1\n"
      "v_permlane16_swap_b32_e64 v208, v209\n"
      "v_pk_mul_f32 v[198:199], v[198:199], v[208:209] op_sel_hi:[1,0]\n"
      "v_pk_mul_f32 v[196:197], v[196:197], v[208:209] op_sel_hi:[1,0]\n"
      "v_pk_mul_f32 v[188:189], v[188:189], v[208:209] op_sel:[0,1]\n"
      "v_pk_mul_f32 v[108:109], v[108:109], v[208:209] op_sel:[0,1]\n"
      "v_pk_mul_f32 v[218:219], v[218:219], v[208:209] op_sel_hi:[1,0]\n"
      "v_cvt_pk_bf16_f32 v196, v196, v197\n"
      "v_pk_mul_f32 v[106:107], v[106:107], v[208:209] op_sel:[0,1]\n"
      "v_pk_mul_f32 v[186:187], v[186:187], v[208:209] op_sel:[0,1]\n"
      "v_cvt_pk_bf16_f32 v197, v198, v199\n"
      "v_cvt_pk_bf16_f32 v106, v106, v107\n"
      "v_pk_mul_f32 v[222:223], v[222:223], v[208:209] op_sel:[0,1]\n"
      "v_pk_mul_f32 v[216:217], v[216:217], v[208:209] op_sel_hi:[1,0]\n"
      "v_cvt_pk_bf16_f32 v199, v218, v219\n"
      "v_pk_mul_f32 v[220:221], v[220:221], v[208:209] op_sel:[0,1]\n"
      "v_pk_mul_f32 v[130:131], v[130:131], v[208:209] op_sel_hi:[1,0]\n"
      "v_cvt_pk_bf16_f32 v107, v108, v109\n"
      "v_cvt_pk_bf16_f32 v198, v216, v217\n"
      "ds_write_b128 v173, v[196:199]\n"
      "v_cvt_pk_bf16_f32 v220, v220, v221\n"
      "v_cvt_pk_bf16_f32 v108, v186, v187\n"
      "v_cvt_pk_bf16_f32 v109, v188, v189\n"
      "v_cvt_pk_bf16_f32 v130, v130, v131\n"
      "v_cvt_pk_bf16_f32 v221, v222, v223\n"
      "ds_write_b128 v173, v[106:109] offset:6144\n"
      "ds_write_b64 v86, v[220:221] offset:6144\n"
      "v_pk_mul_f32 v[132:133], v[132:133], v[208:209] op_sel_hi:[1,0]\n"
      "v_cvt_pk_bf16_f32 v131, v132, v133\n"
      "s_add_u32 s42, s42, 1\n"
      "ds_write_b64 v86, v[130:131]\n"
      "v_xor_b32_e64 v172, v172, v78\n"
      "s_waitcnt lgkmcnt(3)\n"
      "buffer_store_dwordx4 v[156:159], v21, s[24:27], s85 offen sc1\n"
      "s_nop 1\n"
      "ds_read_b128 v[156:159], v7 offset:16384\n"
      "ds_bpermute_b32 v130, v77, v178\n"
      "buffer_store_dwordx4 v[112:115], v22, s[24:27], s85 offen sc1\n"
      "s_waitcnt lgkmcnt(2)\n"
      "s_barrier\n"
      "ds_read_b128 v[112:115], v85\n"
      "buffer_store_dwordx4 v[134:137], v23, s[24:27], s85 offen sc1\n"
      "ds_bpermute_b32 v77, v80, v178\n"
      "s_nop 0\n"
      "ds_read_b128 v[134:137], v85 offset:1024\n"
      "s_waitcnt lgkmcnt(3)\n"
      "buffer_store_dwordx4 v[156:159], v25, s[24:27], s85 offen sc1\n"
      "s_nop 1\n"
      "ds_read_b128 v[156:159], v85 offset:2048\n"
      "v_lshl_add_u32 v130, v172, 4, v130\n"
      "v_lshl_add_u32 v84, v76, 4, v84\n"
      "s_waitcnt lgkmcnt(3)\n"
      "buffer_store_dwordx4 v[112:115], v130, s[24:27], 0 offen sc1\n"
      "s_waitcnt lgkmcnt(2)\n"
      "v_lshl_add_u32 v77, v175, 4, v77\n"
      "s_waitcnt lgkmcnt(1)\n"
      "buffer_store_dwordx4 v[134:137], v77, s[24:27], 0 offen sc1\n"
      "s_waitcnt lgkmcnt(0)\n"
      "buffer_store_dwordx4 v[156:159], v84, s[24:27], 0 offen sc1\n"
      "s_endpgm\n"
      "L_tail0_%=:\n"
      "s_nop 4\n"
      "ds_read_b128 v[102:105], v9 offset:1024\n"
      "v_mfma_f32_16x16x32_bf16 v[168:171], v[218:221], v[130:133], 0\n"
      "ds_read_b128 v[138:141], v2 offset:18432\n"
      "v_subrev_u32 v212, 115456, v9\n"
      "v_mfma_f32_16x16x32_bf16 v[168:171], v[174:177], v[76:79], v[168:171]\n"
      "ds_read_b128 v[130:133], v2 offset:27648\n"
      "ds_read_b128 v[106:109], v2 offset:30720\n"
      "ds_read_b128 v[164:167], v2 offset:24576\n"
      "v_mfma_f32_16x16x32_bf16 v[150:153], v[218:221], v[196:199], 0\n"
      "ds_read_b128 v[186:189], v2 offset:18688\n"
      "v_and_b32 v155, 0x3fffffff, v37\n"
      "v_mfma_f32_16x16x32_bf16 v[150:153], v[174:177], v[80:83], v[150:153]\n"
      "ds_read_b128 v[178:181], v2 offset:21504\n"
      "v_mfma_f32_16x16x32_bf16 v[194:197], v[218:221], v[214:217], 0\n"
      "ds_read_b128 v[216:219], v2 offset:33792\n"
      "s_waitcnt lgkmcnt(7)\n"
      "v_add_f32 v102, v102, v103\n"
      "v_add_f32 v104, v104, v105\n"
      "v_add_f32 v102, v102, v104\n"
      "v_rsq_f32_e64 v102, v102\n"
      "v_cndmask_b32 v168, v168, v150, s[58:59]\n"
      "v_mul_f32 v102, 0x415db3d7, v102\n"
      "v_mov_b32 v104, v102\n"
      "s_mul_i32 s92, s40, 3072\n"
      "v_ashrrev_i32 v81, 31, v37\n"
      "v_permlane32_swap_b32 v102, v104\n"
      "v_mov_b32 v103, v102\n"
      "v_pk_mul_f32 v[156:157], v[156:157], v[104:105] op_sel_hi:[1,0]\n"
      "v_pk_mul_f32 v[146:147], v[146:147], v[104:105] op_sel_hi:[1,0]\n"
      "v_pk_mul_f32 v[148:149], v[148:149], v[104:105] op_sel_hi:[1,0]\n"
      "v_permlane16_swap_b32_e64 v102, v103\n"
      "v_pk_mul_f32 v[202:203], v[202:203], v[102:103] op_sel:[0,1]\n"
      "v_pk_mul_f32 v[114:115], v[114:115], v[102:103] op_sel:[0,1]\n"
      "v_cvt_pk_bf16_f32 v156, v156, v157\n"
      "v_pk_mul_f32 v[200:201], v[200:201], v[102:103] op_sel:[0,1]\n"
      "v_pk_mul_f32 v[160:161], v[160:161], v[104:105] op_sel_hi:[1,0]\n"
      "v_pk_mul_f32 v[116:117], v[116:117], v[102:103] op_sel_hi:[1,0]\n"
      "v_pk_mul_f32 v[98:99], v[98:99], v[102:103] op_sel_hi:[1,0]\n"
      "v_pk_mul_f32 v[204:205], v[204:205], v[102:103] op_sel:[0,1]\n"
      "v_pk_mul_f32 v[96:97], v[96:97], v[102:103] op_sel_hi:[1,0]\n"
      "v_cvt_pk_bf16_f32 v96, v96, v97\n"
      "v_cvt_pk_bf16_f32 v204, v204, v205\n"
      "v_pk_mul_f32 v[134:135], v[134:135], v[102:103] op_sel_hi:[1,0]\n"
      "v_cvt_pk_bf16_f32 v134, v134, v135\n"
      "v_pk_mul_f32 v[158:159], v[158:159], v[104:105] op_sel_hi:[1,0]\n"
      "v_cvt_pk_bf16_f32 v160, v160, v161\n"
      "v_cvt_pk_bf16_f32 v157, v158, v159\n"
      "v_pk_mul_f32 v[112:113], v[112:113], v[102:103] op_sel:[0,1]\n"
      "v_cvt_pk_bf16_f32 v200, v200, v201\n"
      "v_pk_mul_f32 v[206:207], v[206:207], v[102:103] op_sel:[0,1]\n"
      "v_cvt_pk_bf16_f32 v201, v202, v203\n"
      "v_pk_mul_f32 v[136:137], v[136:137], v[102:103] op_sel_hi:[1,0]\n"
      "v_cvt_pk_bf16_f32 v205, v206, v207\n"
      "v_pk_mul_f32 v[118:119], v[118:119], v[102:103] op_sel_hi:[1,0]\n"
      "v_pk_mul_f32 v[162:163], v[162:163], v[104:105] op_sel_hi:[1,0]\n"
      "v_cvt_pk_bf16_f32 v203, v114, v115\n"
      "v_cvt_pk_bf16_f32 v97, v98, v99\n"
      "v_cvt_pk_bf16_f32 v98, v116, v117\n"
      "v_cvt_pk_bf16_f32 v135, v136, v137\n"
      "v_cvt_pk_bf16_f32 v202, v112, v113\n"
      "v_cvt_pk_bf16_f32 v99, v118, v119\n"
      "v_cvt_pk_bf16_f32 v161, v162, v163\n"
      "v_cvt_pk_bf16_f32 v159, v148, v149\n"
      "v_cvt_pk_bf16_f32 v158, v146, v147\n"
      "v_mfma_f32_16x16x32_bf16 v[194:197], v[174:177], v[84:87], v[194:197]\n"
      "ds_write_b64 v6, v[160:161] offset:12288\n"
      "ds_write_b128 v5, v[96:99]\n"
      "ds_write_b64 v6, v[204:205] offset:6144\n"
      "ds_write_b64 v6, v[134:135]\n"
      "ds_write_b128 v5, v[200:203] offset:6144\n"
      "ds_write_b128 v5, v[156:159] offset:12288\n"
      "s_waitcnt lgkmcnt(5)\n"
      "v_mfma_f32_16x16x32_bf16 v[96:99], a[216:219], v[138:141], 0\n"
      "ds_read_b128 v[156:159], v7 offset:18432\n"
      "v_mov_b32_dpp v208, v208 quad_perm:[0,0,0,0] row_mask:0xf bank_mask:0xf\n"
      "v_mfma_f32_16x16x32_bf16 v[96:99], a[220:223], v[178:181], v[96:99]\n"
      "ds_read_b128 v[150:153], v2 offset:18944\n"
      "v_and_b32 v81, 100270464, v81\n"
      "v_mfma_f32_16x16x32_bf16 v[116:119], a[240:243], v[138:141], 0\n"
      "ds_read_b128 v[146:149], v2 offset:27904\n"
      "v_add_u32 v84, s92, v212\n"
      "v_mfma_f32_16x16x32_bf16 v[116:119], a[244:247], v[178:181], v[116:119]\n"
      "v_rcp_f32_e64 v208, v208\n"
      "v_mfma_f32_16x16x32_bf16 v[134:137], v[232:235], v[138:141], 0\n"
      "v_subrev_u32 v213, 115456, v9\n"
      "v_and_b32 v141, 0x300, v213\n"
      "v_mfma_f32_16x16x32_bf16 v[134:137], v[236:239], v[178:181], v[134:137]\n"
      "v_add_u32_e64 v213, v213, v141\n"
      "v_mfma_f32_16x16x32_bf16 v[96:99], a[224:227], v[164:167], v[96:99]\n"
      "ds_read_b128 v[176:179], v2 offset:25088\n"
      "v_mfma_f32_16x16x32_bf16 v[96:99], a[228:231], v[130:133], v[96:99]\n"
      "ds_read_b128 v[138:141], v7 offset:26624\n"
      "v_lshrrev_b32_e64 v212, 4, v84\n"
      "v_mfma_f32_16x16x32_bf16 v[116:119], a[248:251], v[164:167], v[116:119]\n"
      "ds_read_b128 v[160:163], v2 offset:30976\n"
      "v_mfma_f32_16x16x32_bf16 v[116:119], a[252:255], v[130:133], v[116:119]\n"
      "ds_read_b128 v[196:199], v2 offset:21760\n"
      "v_add_u32_e64 v180, 64, v212\n"
      "v_mfma_f32_16x16x32_bf16 v[134:137], v[240:243], v[164:167], v[134:137]\n"
      "v_add_u32 v213, 150784, v213\n"
      "v_mfma_f32_16x16x32_bf16 v[134:137], v[244:247], v[130:133], v[134:137]\n"
      "ds_read_b128 v[220:223], v213 offset:4096\n"
      "v_cndmask_b32 v168, v168, v194, s[60:61]\n"
      "v_mfma_f32_16x16x32_bf16 v[96:99], a[232:235], v[106:109], v[96:99]\n"
      "ds_read_b128 v[130:133], v2 offset:24832\n"
      "v_mfma_f32_16x16x32_bf16 v[96:99], a[236:239], v[216:219], v[96:99]\n"
      "v_mul_u32_u24 v181, 2731, v180\n"
      "v_mfma_f32_16x16x32_bf16 v[116:119], v[224:227], v[106:109], v[116:119]\n"
      "v_add_u32 v82, 128, v212\n"
      "v_mfma_f32_16x16x32_bf16 v[116:119], v[228:231], v[216:219], v[116:119]\n"
      "ds_read_b128 v[164:167], v213 offset:2048\n"
      "v_mfma_f32_16x16x32_bf16 v[134:137], v[248:251], v[106:109], v[134:137]\n"
      "ds_read_b128 v[106:109], v213\n"
      "v_mfma_f32_16x16x32_bf16 v[134:137], v[252:255], v[216:219], v[134:137]\n"
      "ds_read_b128 v[214:217], v2 offset:34048\n"
      "v_mfma_f32_16x16x32_bf16 v[200:203], a[216:219], v[186:189], 0\n"
      "s_waitcnt lgkmcnt(5)\n"
      "v_fma_f32 v194, v96, v96, s52\n"
      "v_mfma_f32_16x16x32_bf16 v[200:203], a[220:223], v[196:199], v[200:203]\n"
      "ds_read_b128 v[170:173], v2 offset:34304\n"
      "v_mfma_f32_16x16x32_bf16 v[112:115], a[240:243], v[186:189], 0\n"
      "v_mul_f32 v195, v97, v97\n"
      "v_mul_f32 v168, v208, v168\n"
      "v_mfma_f32_16x16x32_bf16 v[112:115], a[244:247], v[196:199], v[112:115]\n"
      "v_fmac_f32 v194, v98, v98\n"
      "v_add_u32 v155, 1179648, v155\n"
      "v_mfma_f32_16x16x32_bf16 v[204:207], v[232:235], v[186:189], 0\n"
      "s_waitcnt lgkmcnt(2)\n"
      "v_fmac_f32 v195, v99, v99\n"
      "v_mfma_f32_16x16x32_bf16 v[204:207], v[236:239], v[196:199], v[204:207]\n"
      "ds_read_b128 v[208:211], v7 offset:34816\n"
      "v_add_u32_e64 v80, 0, v212\n"
      "v_mfma_f32_16x16x32_bf16 v[200:203], a[224:227], v[130:133], v[200:203]\n"
      "ds_read_b128 v[102:105], v7 offset:22528\n"
      "v_mul_u32_u24 v86, 2731, v80\n"
      "v_mfma_f32_16x16x32_bf16 v[200:203], a[228:231], v[146:149], v[200:203]\n"
      "v_sub_u32 v155, v155, v81\n"
      "v_lshrrev_b32 v181, 16, v181\n"
      "v_mfma_f32_16x16x32_bf16 v[112:115], a[248:251], v[130:133], v[112:115]\n"
      "buffer_store_dwordx4 v[156:159], v20, s[24:27], s46 offen sc1\n"
      "v_mfma_f32_16x16x32_bf16 v[112:115], a[252:255], v[146:149], v[112:115]\n"
      "ds_read_b128 v[196:199], v2 offset:22016\n"
      "v_fmac_f32_e64 v194, v116, v116\n"
      "v_mfma_f32_16x16x32_bf16 v[204:207], v[240:243], v[130:133], v[204:207]\n"
      "ds_read_b128 v[130:133], v2 offset:28160\n"
      "v_fmac_f32 v195, v117, v117\n"
      "v_mfma_f32_16x16x32_bf16 v[204:207], v[244:247], v[146:149], v[204:207]\n"
      "ds_read_b128 v[186:189], v7 offset:30720\n"
      "v_fmac_f32 v194, v118, v118\n"
      "v_mfma_f32_16x16x32_bf16 v[200:203], a[232:235], v[160:163], v[200:203]\n"
      "s_waitcnt lgkmcnt(3)\n"
      "v_cvt_pk_bf16_f32 v168, v168, v168\n"
      "v_lshrrev_b32 v86, 16, v86\n"
      "v_mfma_f32_16x16x32_bf16 v[200:203], a[236:239], v[214:217], v[200:203]\n"
      "ds_write_b16 v12, v168 offset:16\n"
      "v_mfma_f32_16x16x32_bf16 v[112:115], v[224:227], v[160:163], v[112:115]\n"
      "v_mul_u32_u24 v78, 24, v86\n"
      "v_mfma_f32_16x16x32_bf16 v[112:115], v[228:231], v[214:217], v[112:115]\n"
      "v_fmac_f32 v195, v119, v119\n"
      "v_fmac_f32 v195, v135, v135\n"
      "v_mfma_f32_16x16x32_bf16 v[204:207], v[248:251], v[160:163], v[204:207]\n"
      "s_nop 0\n"
      "v_fma_f32 v168, v200, v200, s52\n"
      "v_mul_u32_u24_e64 v85, 24, v181\n"
      "v_mfma_f32_16x16x32_bf16 v[204:207], v[252:255], v[214:217], v[204:207]\n"
      "s_waitcnt lgkmcnt(1)\n"
      "v_mul_u32_u24 v154, 2731, v82\n"
      "v_mul_f32 v169, v201, v201\n"
      "v_mfma_f32_16x16x32_bf16 v[156:159], a[216:219], v[150:153], 0\n"
      "ds_read_b128 v[216:219], v2 offset:31232\n"
      "v_mfma_f32_16x16x32_bf16 v[156:159], a[220:223], v[196:199], v[156:159]\n"
      "buffer_store_dwordx4 v[102:105], v21, s[24:27], s46 offen sc1\n"
      "v_mfma_f32_16x16x32_bf16 v[146:149], a[240:243], v[150:153], 0\n"
      "v_fmac_f32 v169, v203, v203\n"
      "v_fmac_f32 v194, v134, v134\n"
      "v_mfma_f32_16x16x32_bf16 v[146:149], a[244:247], v[196:199], v[146:149]\n"
      "v_fmac_f32 v168, v202, v202\n"
      "v_fmac_f32 v169, v113, v113\n"
      "v_mfma_f32_16x16x32_bf16 v[160:163], v[232:235], v[150:153], 0\n"
      "v_fmac_f32 v168, v112, v112\n"
      "v_fmac_f32 v195, v137, v137\n"
      "v_mfma_f32_16x16x32_bf16 v[160:163], v[236:239], v[196:199], v[160:163]\n"
      "buffer_store_dwordx4 v[138:141], v22, s[24:27], s46 offen sc1\n"
      "v_mfma_f32_16x16x32_bf16 v[156:159], a[224:227], v[176:179], v[156:159]\n"
      "s_nop 0\n"
      "ds_read_b128 v[138:141], v213 offset:10240\n"
      "v_bfe_i32 v81, v37, 30, 1\n"
      "v_mfma_f32_16x16x32_bf16 v[156:159], a[228:231], v[130:133], v[156:159]\n"
      "v_fmac_f32 v168, v114, v114\n"
      "v_fmac_f32 v169, v115, v115\n"
      "v_mfma_f32_16x16x32_bf16 v[146:149], a[248:251], v[176:179], v[146:149]\n"
      "ds_read_b128 v[196:199], v213 offset:6144\n"
      "v_fmac_f32 v194, v136, v136\n"
      "v_mfma_f32_16x16x32_bf16 v[146:149], a[252:255], v[130:133], v[146:149]\n"
      "buffer_store_dwordx4 v[186:189], v23, s[24:27], s46 offen sc1\n"
      "v_mfma_f32_16x16x32_bf16 v[160:163], v[240:243], v[176:179], v[160:163]\n"
      "ds_read_b128 v[102:105], v213 offset:8192\n"
      "v_fmac_f32_e64 v169, v205, v205\n"
      "v_mfma_f32_16x16x32_bf16 v[160:163], v[244:247], v[130:133], v[160:163]\n"
      "s_waitcnt lgkmcnt(2)\n"
      "v_lshrrev_b32 v154, 16, v154\n"
      "v_fmac_f32 v168, v204, v204\n"
      "v_mfma_f32_16x16x32_bf16 v[156:159], a[232:235], v[216:219], v[156:159]\n"
      "v_add_f32 v194, v194, v195\n"
      "v_mfma_f32_16x16x32_bf16 v[156:159], a[236:239], v[170:173], v[156:159]\n"
      "v_fmac_f32 v169, v207, v207\n"
      "v_fmac_f32 v168, v206, v206\n"
      "v_mfma_f32_16x16x32_bf16 v[146:149], v[224:227], v[216:219], v[146:149]\n"
      "v_sub_u32 v78, v80, v78\n"
      "v_add_f32 v168, v168, v169\n"
      "v_mfma_f32_16x16x32_bf16 v[146:149], v[228:231], v[170:173], v[146:149]\n"
      "s_nop 0\n"
      "v_permlane16_swap_b32 v194, v168\n"
      "v_mfma_f32_16x16x32_bf16 v[160:163], v[248:251], v[216:219], v[160:163]\n"
      "v_mul_f32_e64 v177, v157, v157\n"
      "v_fma_f32 v176, v156, v156, s52\n"
      "v_mfma_f32_16x16x32_bf16 v[160:163], v[252:255], v[170:173], v[160:163]\n"
      "v_fmac_f32 v176, v158, v158\n"
      "v_fmac_f32 v177, v159, v159\n"
      "v_mfma_f32_16x16x32_bf16 v[130:133], a[216:219], v[106:109], 0\n"
      "buffer_store_dwordx4 v[208:211], v25, s[24:27], s46 offen sc1\n"
      "v_pk_fma_f32 v[176:177], v[146:147], v[146:147], v[176:177]\n"
      "v_pk_fma_f32 v[176:177], v[148:149], v[148:149], v[176:177]\n"
      "v_mfma_f32_16x16x32_bf16 v[130:133], a[220:223], v[164:167], v[130:133]\n"
      "s_nop 0\n"
      "v_fmac_f32 v177, v161, v161\n"
      "v_readlane_b32 s46, v37, s42\n"
      "v_mfma_f32_16x16x32_bf16 v[216:219], a[240:243], v[106:109], 0\n"
      "v_fmac_f32 v176, v160, v160\n"
      "v_fmac_f32 v176, v162, v162\n"
      "v_mfma_f32_16x16x32_bf16 v[216:219], a[244:247], v[164:167], v[216:219]\n"
      "v_fmac_f32 v177, v163, v163\n"
      "v_add_f32 v194, v194, v168\n"
      "v_mfma_f32_16x16x32_bf16 v[172:175], v[232:235], v[106:109], 0\n"
      "s_waitcnt lgkmcnt(0)\n"
      "v_add_f32 v176, v176, v177\n"
      "v_mov_b32 v168, v176\n"
      "v_mfma_f32_16x16x32_bf16 v[172:175], v[236:239], v[164:167], v[172:175]\n"
      "s_nop 0\n"
      "v_permlane16_swap_b32 v176, v168\n"
      "v_mfma_f32_16x16x32_bf16 v[130:133], a[224:227], v[220:223], v[130:133]\n"
      "s_or_b32 s95, s46, s48\n"
      "v_add_f32 v176, v176, v168\n"
      "v_mfma_f32_16x16x32_bf16 v[130:133], a[228:231], v[196:199], v[130:133]\n"
      "s_nop 0\n"
      "v_permlane32_swap_b32 v194, v176\n"
      "v_add_f32 v194, v194, v176\n"
      "v_mfma_f32_16x16x32_bf16 v[216:219], a[248:251], v[220:223], v[216:219]\n"
      "ds_write_b32 v31, v194\n"
      "v_mfma_f32_16x16x32_bf16 v[216:219], a[252:255], v[196:199], v[216:219]\n"
      "v_sub_u32 v85, v180, v85\n"
      "v_mfma_f32_16x16x32_bf16 v[172:175], v[240:243], v[220:223], v[172:175]\n"
      "v_and_b32 v180, 7, v181\n"
      "v_lshl_add_u32 v181, v181, 2, 12\n"
      "v_mfma_f32_16x16x32_bf16 v[172:175], v[244:247], v[196:199], v[172:175]\n"
      "v_and_b32 v80, 7, v86\n"
      "v_mfma_f32_16x16x32_bf16 v[130:133], a[232:235], v[102:105], v[130:133]\n"
      "s_cmp_ge_u32 s95, 0x40000000\n"
      "s_cbranch_scc1 L_rare_35_%=\n"
      "L_back_36_%=:\n"
      "v_mfma_f32_16x16x32_bf16 v[130:133], a[236:239], v[138:141], v[130:133]\n"
      "v_mul_u32_u24 v90, 24, v154\n"
      "v_sub_u32_e64 v90, v82, v90\n"
      "v_mfma_f32_16x16x32_bf16 v[216:219], v[224:227], v[102:105], v[216:219]\n"
      "s_waitcnt lgkmcnt(0)\n"
      "s_barrier\n"
      "ds_read_b128 v[176:179], v9\n"
      "ds_read_b128 v[106:109], v7\n"
      "v_mfma_f32_16x16x32_bf16 v[216:219], v[228:231], v[138:141], v[216:219]\n"
      "ds_read_b128 v[150:153], v213 offset:256\n"
      "ds_read_b128 v[196:199], v213 offset:2304\n"
      "ds_read_b128 v[220:223], v213 offset:8448\n"
      "v_fma_f32 v76, v130, v130, s52\n"
      "v_mul_f32_e64 v77, v131, v131\n"
      "v_pk_fma_f32 v[76:77], v[132:133], v[132:133], v[76:77]\n"
      "v_mfma_f32_16x16x32_bf16 v[172:175], v[248:251], v[102:105], v[172:175]\n"
      "ds_read_b128 v[164:167], v213 offset:6400\n"
      "ds_read_b128 v[102:105], v7 offset:4096\n"
      "ds_read_b128 v[186:189], v7 offset:8192\n"
      "ds_read_b128 v[208:211], v213 offset:4352\n"
      "v_pk_fma_f32 v[76:77], v[216:217], v[216:217], v[76:77]\n"
      "v_mfma_f32_16x16x32_bf16 v[172:175], v[252:255], v[138:141], v[172:175]\n"
      "s_waitcnt lgkmcnt(8)\n"
      "v_add_f32 v178, v178, v179\n"
      "v_add_f32_e64 v176, v176, v177\n"
      "v_add_f32_e64 v176, v176, v178\n"
      "v_rsq_f32_e64 v176, v176\n"
      "v_pk_fma_f32 v[76:77], v[218:219], v[218:219], v[76:77]\n"
      "v_mul_f32 v176, 0x415db3d7, v176\n"
      "v_mov_b32_e64 v178, v176\n"
      "v_pk_fma_f32 v[76:77], v[172:173], v[172:173], v[76:77]\n"
      "s_waitcnt lgkmcnt(6)\n"
      "v_mfma_f32_16x16x32_bf16 v[138:141], a[216:219], v[150:153], 0\n"
      "s_waitcnt lgkmcnt(5)\n"
      "v_permlane32_swap_b32_e64 v176, v178\n"
      "v_mfma_f32_16x16x32_bf16 v[138:141], a[220:223], v[196:199], v[138:141]\n"
      "ds_read_b128 v[168:171], v213 offset:10496\n"
      "v_mov_b32_e64 v177, v176\n"
      "v_mfma_f32_16x16x32_bf16 v[212:215], a[240:243], v[150:153], 0\n"
      "buffer_store_dwordx4 v[106:109], v20, s[24:27], s46 offen sc1\n"
      "v_pk_mul_f32 v[146:147], v[146:147], v[178:179] op_sel_hi:[1,0]\n"
      "v_pk_fma_f32 v[76:77], v[174:175], v[174:175], v[76:77]\n"
      "v_pk_mul_f32 v[156:157], v[156:157], v[178:179] op_sel_hi:[1,0]\n"
      "v_pk_mul_f32 v[158:159], v[158:159], v[178:179] op_sel_hi:[1,0]\n"
      "v_mfma_f32_16x16x32_bf16 v[212:215], a[244:247], v[196:199], v[212:215]\n"
      "s_waitcnt lgkmcnt(1)\n"
      "v_permlane16_swap_b32 v176, v177\n"
      "v_mfma_f32_16x16x32_bf16 v[108:111], v[232:235], v[150:153], 0\n"
      "ds_read_b128 v[192:195], v7 offset:12288\n"
      "v_cvt_pk_bf16_f32 v156, v156, v157\n"
      "v_mfma_f32_16x16x32_bf16 v[108:111], v[236:239], v[196:199], v[108:111]\n"
      "ds_read_b128 v[196:199], v7 offset:16384\n"
      "v_cvt_pk_bf16_f32 v157, v158, v159\n"
      "v_mfma_f32_16x16x32_bf16 v[138:141], a[224:227], v[208:211], v[138:141]\n"
      "buffer_store_dwordx4 v[102:105], v21, s[24:27], s46 offen sc1\n"
      "v_pk_mul_f32 v[118:119], v[118:119], v[176:177] op_sel_hi:[1,0]\n"
      "v_pk_mul_f32 v[112:113], v[112:113], v[176:177] op_sel:[0,1]\n"
      "v_pk_mul_f32 v[148:149], v[148:149], v[178:179] op_sel_hi:[1,0]\n"
      "v_pk_mul_f32 v[136:137], v[136:137], v[176:177] op_sel_hi:[1,0]\n"
      "v_pk_mul_f32 v[206:207], v[206:207], v[176:177] op_sel:[0,1]\n"
      "v_pk_mul_f32 v[200:201], v[200:201], v[176:177] op_sel:[0,1]\n"
      "v_pk_mul_f32 v[98:99], v[98:99], v[176:177] op_sel_hi:[1,0]\n"
      "v_mfma_f32_16x16x32_bf16 v[138:141], a[228:231], v[164:167], v[138:141]\n"
      "v_cvt_pk_bf16_f32 v158, v146, v147\n"
      "v_cvt_pk_bf16_f32 v159, v148, v149\n"
      "v_mfma_f32_16x16x32_bf16 v[212:215], a[248:251], v[208:211], v[212:215]\n"
      "buffer_store_dwordx4 v[186:189], v22, s[24:27], s46 offen sc1\n"
      "v_pk_mul_f32 v[134:135], v[134:135], v[176:177] op_sel_hi:[1,0]\n"
      "v_pk_mul_f32 v[96:97], v[96:97], v[176:177] op_sel_hi:[1,0]\n"
      "v_pk_mul_f32 v[202:203], v[202:203], v[176:177] op_sel:[0,1]\n"
      "v_pk_mul_f32 v[204:205], v[204:205], v[176:177] op_sel:[0,1]\n"
      "v_pk_mul_f32 v[116:117], v[116:117], v[176:177] op_sel_hi:[1,0]\n"
      "v_pk_mul_f32 v[114:115], v[114:115], v[176:177] op_sel:[0,1]\n"
      "v_mfma_f32_16x16x32_bf16 v[212:215], a[252:255], v[164:167], v[212:215]\n"
      "v_cvt_pk_bf16_f32 v200, v200, v201\n"
      "v_cvt_pk_bf16_f32 v96, v96, v97\n"
      "v_cvt_pk_bf16_f32 v204, v204, v205\n"
      "v_mfma_f32_16x16x32_bf16 v[108:111], v[240:243], v[208:211], v[108:111]\n"
      "v_cvt_pk_bf16_f32 v201, v202, v203\n"
      "v_cvt_pk_bf16_f32 v97, v98, v99\n"
      "v_cvt_pk_bf16_f32 v205, v206, v207\n"
      "v_mfma_f32_16x16x32_bf16 v[108:111], v[244:247], v[164:167], v[108:111]\n"
      "ds_write_b64 v6, v[204:205] offset:24576\n"
      "v_mfma_f32_16x16x32_bf16 v[138:141], a[232:235], v[220:223], v[138:141]\n"
      "s_waitcnt lgkmcnt(1)\n"
      "v_cvt_pk_bf16_f32 v134, v134, v135\n"
      "v_cvt_pk_bf16_f32 v202, v112, v113\n"
      "v_mfma_f32_16x16x32_bf16 v[138:141], a[236:239], v[168:171], v[138:141]\n"
      "v_cvt_pk_bf16_f32 v203, v114, v115\n"
      "v_cvt_pk_bf16_f32 v99, v118, v119\n"
      "v_cvt_pk_bf16_f32 v135, v136, v137\n"
      "v_mfma_f32_16x16x32_bf16 v[212:215], v[224:227], v[220:223], v[212:215]\n"
      "ds_write_b128 v5, v[200:203] offset:24576\n"
      "v_cvt_pk_bf16_f32 v98, v116, v117\n"
      "v_mfma_f32_16x16x32_bf16 v[212:215], v[228:231], v[168:171], v[212:215]\n"
      "ds_write_b64 v6, v[134:135] offset:18432\n"
      "v_mfma_f32_16x16x32_bf16 v[108:111], v[248:251], v[220:223], v[108:111]\n"
      "ds_write_b128 v5, v[156:159] offset:30720\n"
      "v_mfma_f32_16x16x32_bf16 v[108:111], v[252:255], v[168:171], v[108:111]\n"
      "ds_write_b128 v5, v[96:99] offset:18432\n"
      "v_mul_f32 v159, v139, v139\n"
      "v_fma_f32 v158, v138, v138, s52\n"
      "v_pk_mul_f32 v[160:161], v[160:161], v[178:179] op_sel_hi:[1,0]\n"
      "v_add_f32_e64 v76, v76, v77\n"
      "v_pk_fma_f32 v[158:159], v[140:141], v[140:141], v[158:159]\n"
      "v_pk_fma_f32 v[158:159], v[212:213], v[212:213], v[158:159]\n"
      "v_pk_fma_f32 v[158:159], v[214:215], v[214:215], v[158:159]\n"
      "v_pk_fma_f32 v[158:159], v[108:109], v[108:109], v[158:159]\n"
      "v_pk_fma_f32 v[158:159], v[110:111], v[110:111], v[158:159]\n"
      "v_add_f32 v158, v158, v159\n"
      "v_cvt_pk_bf16_f32 v160, v160, v161\n"
      "v_pk_mul_f32 v[162:163], v[162:163], v[178:179] op_sel_hi:[1,0]\n"
      "v_permlane16_swap_b32 v76, v158\n"
      "v_cvt_pk_bf16_f32 v161, v162, v163\n"
      "v_add_f32 v76, v76, v158\n"
      "v_mov_b32_e64 v158, v76\n"
      "s_add_u32 s42, s42, 1\n"
      "ds_write_b64 v6, v[160:161] offset:30720\n"
      "v_permlane32_swap_b32_e64 v76, v158\n"
      "v_add_f32 v76, v76, v158\n"
      "ds_write_b32 v31, v76 offset:4608\n"
      "buffer_store_dwordx4 v[192:195], v23, s[24:27], s46 offen sc1\n"
      "v_and_b32 v81, 196224, v81\n"
      "s_nop 0\n"
      "v_subrev_u32 v192, 73728, v5\n"
      "v_sub_u32_e64 v155, v155, v81\n"
      "ds_bpermute_b32 v163, v181, v155\n"
      "buffer_store_dwordx4 v[196:199], v25, s[24:27], s46 offen sc1\n"
      "s_waitcnt lgkmcnt(1)\n"
      "s_barrier\n"
      "ds_read_b128 v[158:161], v9 offset:4608\n"
      "v_subrev_u32 v196, 73728, v6\n"
      "ds_read_b128 v[198:201], v7 offset:22528\n"
      "ds_read_b128 v[96:99], v7 offset:18432\n"
      "ds_read_b128 v[204:207], v7 offset:30720\n"
      "v_readlane_b32 s46, v37, s42\n"
      "ds_read_b128 v[166:169], v7 offset:26624\n"
      "v_lshl_add_u32 v86, v86, 2, 12\n"
      "v_add_u32 v12, 32, v12\n"
      "s_or_b32 s95, s46, s48\n"
      "s_cmp_ge_u32 s95, 0x40000000\n"
      "s_cbranch_scc1 L_rare_39_%=\n"
      "L_back_40_%=:\n"
      "v_xor_b32 v78, v78, v80\n"
      "s_waitcnt lgkmcnt(4)\n"
      "v_add_f32 v160, v160, v161\n"
      "v_add_f32 v158, v158, v159\n"
      "v_add_f32 v158, v158, v160\n"
      "v_rsq_f32 v158, v158\n"
      "v_and_b32 v82, 7, v154\n"
      "v_mul_f32 v158, 0x415db3d7, v158\n"
      "v_mov_b32 v160, v158\n"
      "v_lshl_add_u32 v154, v154, 2, 12\n"
      "s_nop 0\n"
      "v_permlane32_swap_b32 v158, v160\n"
      "v_mov_b32 v159, v158\n"
      "s_waitcnt lgkmcnt(1)\n"
      "buffer_store_dwordx4 v[96:99], v20, s[24:27], s46 offen sc1\n"
      "v_permlane16_swap_b32_e64 v158, v159\n"
      "v_pk_mul_f32 v[132:133], v[132:133], v[158:159] op_sel_hi:[1,0]\n"
      "v_pk_mul_f32 v[216:217], v[216:217], v[158:159] op_sel_hi:[1,0]\n"
      "v_pk_mul_f32 v[130:131], v[130:131], v[158:159] op_sel_hi:[1,0]\n"
      "v_pk_mul_f32 v[138:139], v[138:139], v[158:159] op_sel:[0,1]\n"
      "v_pk_mul_f32 v[140:141], v[140:141], v[158:159] op_sel:[0,1]\n"
      "v_pk_mul_f32 v[212:213], v[212:213], v[158:159] op_sel:[0,1]\n"
      "v_pk_mul_f32 v[108:109], v[108:109], v[158:159] op_sel:[0,1]\n"
      "v_cvt_pk_bf16_f32 v138, v138, v139\n"
      "v_pk_mul_f32 v[214:215], v[214:215], v[158:159] op_sel:[0,1]\n"
      "v_pk_mul_f32 v[172:173], v[172:173], v[158:159] op_sel_hi:[1,0]\n"
      "v_cvt_pk_bf16_f32 v139, v140, v141\n"
      "v_cvt_pk_bf16_f32 v108, v108, v109\n"
      "v_cvt_pk_bf16_f32 v140, v212, v213\n"
      "v_cvt_pk_bf16_f32 v130, v130, v131\n"
      "v_pk_mul_f32 v[110:111], v[110:111], v[158:159] op_sel:[0,1]\n"
      "v_pk_mul_f32 v[218:219], v[218:219], v[158:159] op_sel_hi:[1,0]\n"
      "v_cvt_pk_bf16_f32 v141, v214, v215\n"
      "s_add_u32 s42, s42, 1\n"
      "ds_write_b128 v192, v[138:141] offset:6144\n"
      "v_cvt_pk_bf16_f32 v131, v132, v133\n"
      "v_cvt_pk_bf16_f32 v133, v218, v219\n"
      "v_cvt_pk_bf16_f32 v172, v172, v173\n"
      "v_cvt_pk_bf16_f32 v132, v216, v217\n"
      "v_cvt_pk_bf16_f32 v109, v110, v111\n"
      "ds_write_b128 v192, v[130:133]\n"
      "ds_write_b64 v196, v[108:109] offset:6144\n"
      "v_pk_mul_f32 v[174:175], v[174:175], v[158:159] op_sel_hi:[1,0]\n"
      "v_cvt_pk_bf16_f32 v173, v174, v175\n"
      "ds_write_b64 v196, v[172:173]\n"
      "buffer_store_dwordx4 v[198:201], v21, s[24:27], s46 offen sc1\n"
      "ds_bpermute_b32 v76, v154, v155\n"
      "s_nop 0\n"
      "ds_read_b128 v[198:201], v7 offset:34816\n"
      "ds_bpermute_b32 v217, v86, v155\n"
      "s_waitcnt lgkmcnt(5)\n"
      "buffer_store_dwordx4 v[166:169], v22, s[24:27], s46 offen sc1\n"
      "v_xor_b32 v85, v85, v180\n"
      "v_xor_b32 v90, v90, v82\n"
      "v_lshl_add_u32 v163, v85, 4, v163\n"
      "s_waitcnt lgkmcnt(3)\n"
      "s_barrier\n"
      "ds_read_b128 v[166:169], v84\n"
      "buffer_store_dwordx4 v[204:207], v23, s[24:27], s46 offen sc1\n"
      "ds_read_b128 v[172:175], v84 offset:1024\n"
      "s_waitcnt lgkmcnt(2)\n"
      "buffer_store_dwordx4 v[198:201], v25, s[24:27], s46 offen sc1\n"
      "ds_read_b128 v[108:111], v84 offset:2048\n"
      "v_lshl_add_u32 v76, v90, 4, v76\n"
      "v_lshl_add_u32 v217, v78, 4, v217\n"
      "s_waitcnt lgkmcnt(2)\n"
      "buffer_store_dwordx4 v[166:169], v217, s[24:27], 0 offen sc1\n"
      "s_waitcnt lgkmcnt(1)\n"
      "buffer_store_dwordx4 v[172:175], v163, s[24:27], 0 offen sc1\n"
      "s_waitcnt lgkmcnt(0)\n"
      "buffer_store_dwordx4 v[108:111], v76, s[24:27], 0 offen sc1\n"
      "s_endpgm\n"
      "L_rare_1_%=:\n"
      "s_nop 7\n"
      "s_waitcnt lgkmcnt(0)\n"
      "s_add_u32 s72, s42, 1\n"
      "v_readlane_b32 s77, v38, s72\n"
      "v_subrev_u32 v157, 115456, v9\n"
      "s_and_b32 s73, s77, -16\n"
      "s_mov_b32 m0, 110592\n"
      "s_nop 0\n"
      "buffer_load_dwordx4 v157, s[28:31], s73 offen  lds\n"
      "s_add_u32 s73, s73, 1024\n"
      "s_mov_b32 m0, 111616\n"
      "s_nop 0\n"
      "buffer_load_dwordx4 v157, s[28:31], s73 offen  lds\n"
      "s_add_u32 s73, s73, 1024\n"
      "s_mov_b32 m0, 112640\n"
      "s_nop 0\n"
      "buffer_load_dwordx4 v157, s[28:31], s73 offen  lds\n"
      "s_add_u32 s73, s73, 1024\n"
      "s_mov_b32 m0, 113664\n"
      "s_nop 0\n"
      "buffer_load_dwordx4 v157, s[28:31], s73 offen  lds\n"
      "s_add_u32 s73, s73, 1024\n"
      "s_mov_b32 m0, 114688\n"
      "s_mov_b32 exec_hi, 0xffff\n"
      "buffer_load_dwordx4 v157, s[28:31], s73 offen  lds\n"
      "s_mov_b32 exec_hi, -1\n"
      "s_add_u32 s73, s73, 1024\n"
      "s_and_b32 s77, s77, 15\n"
      "s_waitcnt lgkmcnt(0)\n"
      "s_nop 4\n"
      "s_branch L_back_2_%=\n"
      "L_rare_3_%=:\n"
      "s_nop 7\n"
      "s_waitcnt lgkmcnt(0)\n"
      "s_waitcnt vmcnt(0)\n"
      "v_subrev_u32 v181, 115456, v9\n"
      "v_bfe_u32 v113, v181, 4, 4\n"
      "v_mul_u32_u24 v113, 98, v113\n"
      "v_lshrrev_b32 v181, 8, v181\n"
      "v_lshl_add_u32 v113, v181, 3, v113\n"
      "v_add_u32 v113, s77, v113\n"
      "v_and_b32 v197, 2, v113\n"
      "v_and_b32 v113, -4, v113\n"
      "v_add_u32 v113, 110592, v113\n"
      "ds_read2_b32 v[106:107], v113 offset0:0 offset1:1\n"
      "ds_read2_b32 v[108:109], v113 offset0:8 offset1:9\n"
      "ds_read2_b32 v[156:157], v113 offset0:16 offset1:17\n"
      "ds_read2_b32 v[154:155], v113 offset0:2 offset1:10\n"
      "ds_read2_b32 v[186:187], v113 offset0:18 offset1:24\n"
      "s_waitcnt lgkmcnt(4)\n"
      "v_alignbyte_b32 v106, v107, v106, v197\n"
      "s_waitcnt lgkmcnt(1)\n"
      "v_alignbyte_b32 v107, v154, v107, v197\n"
      "v_lshlrev_b32 v181, 16, v106\n"
      "v_fmac_f32 v150, s53, v181\n"
      "v_and_b32 v181, 0xffff0000, v106\n"
      "v_fmac_f32 v151, s53, v181\n"
      "v_lshlrev_b32 v181, 16, v107\n"
      "v_fmac_f32_e64 v152, s53, v181\n"
      "v_and_b32 v181, 0xffff0000, v107\n"
      "v_fmac_f32 v153, s53, v181\n"
      "v_alignbyte_b32 v108, v109, v108, v197\n"
      "v_alignbyte_b32 v109, v155, v109, v197\n"
      "v_lshlrev_b32 v181, 16, v108\n"
      "v_fmac_f32 v204, s53, v181\n"
      "v_and_b32 v181, 0xffff0000, v108\n"
      "v_fmac_f32 v205, s53, v181\n"
      "v_lshlrev_b32 v181, 16, v109\n"
      "v_fmac_f32 v206, s53, v181\n"
      "v_and_b32 v181, 0xffff0000, v109\n"
      "v_fmac_f32 v207, s53, v181\n"
      "v_alignbyte_b32 v156, v157, v156, v197\n"
      "s_waitcnt lgkmcnt(0)\n"
      "v_alignbyte_b32 v157, v186, v157, v197\n"
      "v_lshlrev_b32 v181, 16, v156\n"
      "v_fmac_f32 v134, s53, v181\n"
      "v_and_b32 v181, 0xffff0000, v156\n"
      "v_fmac_f32 v135, s53, v181\n"
      "v_lshlrev_b32 v181, 16, v157\n"
      "v_fmac_f32 v136, s53, v181\n"
      "v_and_b32 v181, 0xffff0000, v157\n"
      "v_fmac_f32 v137, s53, v181\n"
      "v_alignbyte_b32 v181, v187, v187, v197\n"
      "v_lshlrev_b32 v181, 16, v181\n"
      "v_fmac_f32 v140, s53, v181\n"
      "v_max3_f32 v113, v150, v151, v152\n"
      "v_max3_f32 v197, v153, v204, v205\n"
      "v_max3_f32 v113, v113, v206, v207\n"
      "v_max3_f32 v197, v197, v134, v135\n"
      "v_max3_f32 v113, v113, v136, v137\n"
      "v_max3_f32 v113, v113, v197, v140\n"
      "v_mov_b32 v181, v113\n"
      "s_nop 1\n"
      "v_permlane16_swap_b32 v113, v181\n"
      "v_max_f32 v113, v113, v181\n"
      "v_mov_b32 v181, v113\n"
      "s_nop 1\n"
      "v_permlane32_swap_b32 v113, v181\n"
      "v_max_f32 v113, v113, v181\n"
      "v_sub_f32 v150, v150, v113\n"
      "v_sub_f32 v151, v151, v113\n"
      "v_sub_f32 v152, v152, v113\n"
      "v_sub_f32 v153, v153, v113\n"
      "v_sub_f32 v204, v204, v113\n"
      "v_sub_f32 v205, v205, v113\n"
      "v_sub_f32 v206, v206, v113\n"
      "v_sub_f32 v207, v207, v113\n"
      "v_sub_f32 v134, v134, v113\n"
      "v_sub_f32 v135, v135, v113\n"
      "v_sub_f32 v136, v136, v113\n"
      "v_sub_f32 v137, v137, v113\n"
      "v_sub_f32_e64 v140, v140, v113\n"
      "s_waitcnt lgkmcnt(0)\n"
      "s_nop 4\n"
      "s_branch L_back_4_%=\n"
      "L_rare_5_%=:\n"
      "s_nop 7\n"
      "s_waitcnt lgkmcnt(0)\n"
      "s_waitcnt vmcnt(0)\n"
      "v_subrev_u32 v113, 115456, v9\n"
      "v_bfe_u32 v82, v113, 4, 4\n"
      "v_mul_u32_u24 v82, 98, v82\n"
      "v_lshrrev_b32 v113, 8, v113\n"
      "v_lshl_add_u32 v82, v113, 3, v82\n"
      "v_add_u32 v82, s77, v82\n"
      "v_and_b32 v141, 2, v82\n"
      "v_and_b32 v82, -4, v82\n"
      "v_add_u32 v82, 112160, v82\n"
      "ds_read2_b32 v[218:219], v82 offset0:0 offset1:1\n"
      "ds_read2_b32 v[220:221], v82 offset0:8 offset1:9\n"
      "ds_read2_b32 v[200:201], v82 offset0:16 offset1:17\n"
      "ds_read2_b32 v[202:203], v82 offset0:2 offset1:10\n"
      "ds_read2_b32 v[142:143], v82 offset0:18 offset1:24\n"
      "s_waitcnt lgkmcnt(4)\n"
      "v_alignbyte_b32 v218, v219, v218, v141\n"
      "s_waitcnt lgkmcnt(1)\n"
      "v_alignbyte_b32 v219, v202, v219, v141\n"
      "v_lshlrev_b32 v113, 16, v218\n"
      "v_fmac_f32 v166, s53, v113\n"
      "v_and_b32 v113, 0xffff0000, v218\n"
      "v_fmac_f32 v167, s53, v113\n"
      "v_lshlrev_b32 v113, 16, v219\n"
      "v_fmac_f32 v168, s53, v113\n"
      "v_and_b32 v113, 0xffff0000, v219\n"
      "v_fmac_f32 v169, s53, v113\n"
      "v_alignbyte_b32 v220, v221, v220, v141\n"
      "v_alignbyte_b32 v221, v203, v221, v141\n"
      "v_lshlrev_b32 v113, 16, v220\n"
      "v_fmac_f32 v144, s53, v113\n"
      "v_and_b32 v113, 0xffff0000, v220\n"
      "v_fmac_f32 v145, s53, v113\n"
      "v_lshlrev_b32 v113, 16, v221\n"
      "v_fmac_f32_e64 v146, s53, v113\n"
      "v_and_b32 v113, 0xffff0000, v221\n"
      "v_fmac_f32 v147, s53, v113\n"
      "v_alignbyte_b32 v200, v201, v200, v141\n"
      "s_waitcnt lgkmcnt(0)\n"
      "v_alignbyte_b32 v201, v142, v201, v141\n"
      "v_lshlrev_b32 v113, 16, v200\n"
      "v_fmac_f32 v106, s53, v113\n"
      "v_and_b32 v113, 0xffff0000, v200\n"
      "v_fmac_f32 v107, s53, v113\n"
      "v_lshlrev_b32 v113, 16, v201\n"
      "v_fmac_f32_e64 v108, s53, v113\n"
      "v_and_b32 v113, 0xffff0000, v201\n"
      "v_fmac_f32 v109, s53, v113\n"
      "v_alignbyte_b32 v113, v143, v143, v141\n"
      "v_lshlrev_b32 v113, 16, v113\n"
      "v_fmac_f32_e64 v192, s53, v113\n"
      "v_max3_f32 v82, v166, v167, v168\n"
      "v_max3_f32 v141, v169, v144, v145\n"
      "v_max3_f32 v82, v82, v146, v147\n"
      "v_max3_f32 v141, v141, v106, v107\n"
      "v_max3_f32 v82, v82, v108, v109\n"
      "v_max3_f32 v82, v82, v141, v192\n"
      "v_mov_b32 v113, v82\n"
      "s_nop 1\n"
      "v_permlane16_swap_b32 v82, v113\n"
      "v_max_f32 v82, v82, v113\n"
      "v_mov_b32 v113, v82\n"
      "s_nop 1\n"
      "v_permlane32_swap_b32 v82, v113\n"
      "v_max_f32 v82, v82, v113\n"
      "v_sub_f32 v166, v166, v82\n"
      "v_sub_f32 v167, v167, v82\n"
      "v_sub_f32 v168, v168, v82\n"
      "v_sub_f32 v169, v169, v82\n"
      "v_sub_f32 v144, v144, v82\n"
      "v_sub_f32 v145, v145, v82\n"
      "v_sub_f32 v146, v146, v82\n"
      "v_sub_f32 v147, v147, v82\n"
      "v_sub_f32 v106, v106, v82\n"
      "v_sub_f32 v107, v107, v82\n"
      "v_sub_f32 v108, v108, v82\n"
      "v_sub_f32 v109, v109, v82\n"
      "v_sub_f32_e64 v192, v192, v82\n"
      "s_waitcnt lgkmcnt(0)\n"
      "s_nop 4\n"
      "s_branch L_back_6_%=\n"
      "L_rare_7_%=:\n"
      "s_nop 7\n"
      "s_waitcnt lgkmcnt(0)\n"
      "s_waitcnt vmcnt(0)\n"
      "v_subrev_u32 v173, 115456, v9\n"
      "v_bfe_u32 v123, v173, 4, 4\n"
      "v_mul_u32_u24 v123, 98, v123\n"
      "v_lshrrev_b32_e64 v173, 8, v173\n"
      "v_lshl_add_u32 v123, v173, 3, v123\n"
      "v_add_u32 v123, s77, v123\n"
      "v_and_b32 v122, 2, v123\n"
      "v_and_b32_e64 v123, -4, v123\n"
      "v_add_u32 v123, 113728, v123\n"
      "ds_read2_b32 v[136:137], v123 offset0:0 offset1:1\n"
      "ds_read2_b32 v[190:191], v123 offset0:8 offset1:9\n"
      "ds_read2_b32 v[142:143], v123 offset0:16 offset1:17\n"
      "ds_read2_b32 v[138:139], v123 offset0:2 offset1:10\n"
      "ds_read2_b32 v[192:193], v123 offset0:18 offset1:24\n"
      "s_waitcnt lgkmcnt(4)\n"
      "v_alignbyte_b32 v136, v137, v136, v122\n"
      "s_waitcnt lgkmcnt(1)\n"
      "v_alignbyte_b32 v137, v138, v137, v122\n"
      "v_lshlrev_b32 v173, 16, v136\n"
      "v_fmac_f32 v218, s53, v173\n"
      "v_and_b32 v173, 0xffff0000, v136\n"
      "v_fmac_f32 v219, s53, v173\n"
      "v_lshlrev_b32 v173, 16, v137\n"
      "v_fmac_f32_e64 v220, s53, v173\n"
      "v_and_b32 v173, 0xffff0000, v137\n"
      "v_fmac_f32 v221, s53, v173\n"
      "v_alignbyte_b32 v190, v191, v190, v122\n"
      "v_alignbyte_b32 v191, v139, v191, v122\n"
      "v_lshlrev_b32 v173, 16, v190\n"
      "v_fmac_f32 v186, s53, v173\n"
      "v_and_b32 v173, 0xffff0000, v190\n"
      "v_fmac_f32 v187, s53, v173\n"
      "v_lshlrev_b32 v173, 16, v191\n"
      "v_fmac_f32 v188, s53, v173\n"
      "v_and_b32 v173, 0xffff0000, v191\n"
      "v_fmac_f32 v189, s53, v173\n"
      "v_alignbyte_b32 v142, v143, v142, v122\n"
      "s_waitcnt lgkmcnt(0)\n"
      "v_alignbyte_b32 v143, v192, v143, v122\n"
      "v_lshlrev_b32 v173, 16, v142\n"
      "v_fmac_f32 v204, s53, v173\n"
      "v_and_b32 v173, 0xffff0000, v142\n"
      "v_fmac_f32 v205, s53, v173\n"
      "v_lshlrev_b32 v173, 16, v143\n"
      "v_fmac_f32 v206, s53, v173\n"
      "v_and_b32 v173, 0xffff0000, v143\n"
      "v_fmac_f32 v207, s53, v173\n"
      "v_alignbyte_b32 v173, v193, v193, v122\n"
      "v_lshlrev_b32 v173, 16, v173\n"
      "v_fmac_f32 v208, s53, v173\n"
      "v_max3_f32 v123, v218, v219, v220\n"
      "v_max3_f32 v122, v221, v186, v187\n"
      "v_max3_f32 v123, v123, v188, v189\n"
      "v_max3_f32 v122, v122, v204, v205\n"
      "v_max3_f32 v123, v123, v206, v207\n"
      "v_max3_f32 v123, v123, v122, v208\n"
      "v_mov_b32 v173, v123\n"
      "s_nop 1\n"
      "v_permlane16_swap_b32 v123, v173\n"
      "v_max_f32 v123, v123, v173\n"
      "v_mov_b32 v173, v123\n"
      "s_nop 1\n"
      "v_permlane32_swap_b32 v123, v173\n"
      "v_max_f32 v123, v123, v173\n"
      "v_sub_f32 v218, v218, v123\n"
      "v_sub_f32 v219, v219, v123\n"
      "v_sub_f32 v220, v220, v123\n"
      "v_sub_f32 v221, v221, v123\n"
      "v_sub_f32 v186, v186, v123\n"
      "v_sub_f32 v187, v187, v123\n"
      "v_sub_f32 v188, v188, v123\n"
      "v_sub_f32 v189, v189, v123\n"
      "v_sub_f32 v204, v204, v123\n"
      "v_sub_f32 v205, v205, v123\n"
      "v_sub_f32 v206, v206, v123\n"
      "v_sub_f32 v207, v207, v123\n"
      "v_sub_f32 v208, v208, v123\n"
      "s_waitcnt lgkmcnt(0)\n"
      "s_nop 4\n"
      "s_branch L_back_8_%=\n"
      "L_rare_9_%=:\n"
      "s_nop 7\n"
      "s_waitcnt lgkmcnt(0)\n"
      "s_waitcnt vmcnt(0)\n"
      "v_subrev_u32 v207, 115456, v9\n"
      "v_lshrrev_b32 v207, 3, v207\n"
      "v_min_u32 v207, 96, v207\n"
      "v_add_u32 v207, s77, v207\n"
      "v_add_u32 v207, 115296, v207\n"
      "ds_read_u16 v207, v207\n"
      "s_waitcnt lgkmcnt(0)\n"
      "v_lshlrev_b32_e64 v207, 16, v207\n"
      "v_fmac_f32 v200, s53, v207\n"
      "s_nop 1\n"
      "v_max_f32_dpp v192, v200, v200 row_ror:8 row_mask:0xf bank_mask:0xf\n"
      "s_nop 1\n"
      "v_max_f32_dpp v192, v192, v192 row_ror:4 row_mask:0xf bank_mask:0xf\n"
      "s_nop 1\n"
      "v_max_f32_dpp v192, v192, v192 row_ror:2 row_mask:0xf bank_mask:0xf\n"
      "s_nop 1\n"
      "v_max_f32_dpp v192, v192, v192 row_ror:1 row_mask:0xf bank_mask:0xf\n"
      "s_nop 1\n"
      "v_max_f32_dpp v192, v192, v192 row_bcast:15 row_mask:0xa bank_mask:0xf\n"
      "s_nop 1\n"
      "v_max_f32_dpp v192, v192, v192 row_bcast:31 row_mask:0xc bank_mask:0xf\n"
      "s_nop 0\n"
      "v_readlane_b32 s72, v192, 63\n"
      "s_nop 1\n"
      "v_subrev_f32 v200, s72, v200\n"
      "s_waitcnt lgkmcnt(0)\n"
      "s_nop 4\n"
      "s_branch L_back_10_%=\n"
      "L_rare_27_%=:\n"
      "s_nop 7\n"
      "s_waitcnt lgkmcnt(0)\n"
      "s_bitcmp1_b32 s85, 31\n"
      "s_cselect_b32 s75, 100270464, 0\n"
      "s_bitcmp1_b32 s85, 30\n"
      "s_cselect_b32 s76, 196224, 0\n"
      "s_and_b32 s74, s85, 0x3fffffff\n"
      "s_cmp_ge_u32 s85, 0x40000000\n"
      "s_cselect_b32 s74, s74, 0\n"
      "s_cselect_b32 s85, 0, s85\n"
      "s_sub_u32 s74, s74, s69\n"
      "s_add_u32 s69, s69, s74\n"
      "s_sub_u32 s75, s75, s70\n"
      "s_add_u32 s70, s70, s75\n"
      "s_sub_u32 s76, s76, s71\n"
      "s_add_u32 s71, s71, s76\n"
      "v_add_u32 v20, s74, v20\n"
      "v_add_u32 v21, s74, v21\n"
      "v_add_u32 v22, s74, v22\n"
      "v_add_u32 v23, s74, v23\n"
      "v_add_u32 v25, s74, v25\n"
      "s_cmp_eq_u32 s75, 0\n"
      "s_cbranch_scc1 L_ax_25_%=\n"
      "v_bfe_i32 v176, v27, 10, 1\n"
      "v_and_b32 v176, s75, v176\n"
      "v_sub_u32 v20, v20, v176\n"
      "v_bfe_i32 v176, v27, 12, 1\n"
      "v_and_b32 v176, s75, v176\n"
      "v_sub_u32 v21, v21, v176\n"
      "v_bfe_i32 v176, v27, 14, 1\n"
      "v_and_b32 v176, s75, v176\n"
      "v_sub_u32 v22, v22, v176\n"
      "v_bfe_i32 v176, v27, 16, 1\n"
      "v_and_b32 v176, s75, v176\n"
      "v_sub_u32 v23, v23, v176\n"
      "v_bfe_i32 v176, v27, 18, 1\n"
      "v_and_b32 v176, s75, v176\n"
      "v_sub_u32 v25, v25, v176\n"
      "L_ax_25_%=:\n"
      "s_cmp_eq_u32 s76, 0\n"
      "s_cbranch_scc1 L_ax_26_%=\n"
      "v_bfe_i32 v176, v27, 11, 1\n"
      "v_and_b32 v176, s76, v176\n"
      "v_sub_u32_e64 v20, v20, v176\n"
      "v_bfe_i32 v176, v27, 13, 1\n"
      "v_and_b32 v176, s76, v176\n"
      "v_sub_u32 v21, v21, v176\n"
      "v_bfe_i32 v176, v27, 15, 1\n"
      "v_and_b32 v176, s76, v176\n"
      "v_sub_u32 v22, v22, v176\n"
      "v_bfe_i32 v176, v27, 17, 1\n"
      "v_and_b32 v176, s76, v176\n"
      "v_sub_u32 v23, v23, v176\n"
      "v_bfe_i32 v176, v27, 19, 1\n"
      "v_and_b32 v176, s76, v176\n"
      "v_sub_u32 v25, v25, v176\n"
      "L_ax_26_%=:\n"
      "s_or_b32 s48, s66, s69\n"
      "s_cmp_lg_u32 s48, 0\n"
      "s_cselect_b32 s48, 0x40000000, 0\n"
      "s_waitcnt lgkmcnt(0)\n"
      "s_nop 4\n"
      "s_branch L_back_28_%=\n"
      "L_rare_31_%=:\n"
      "s_nop 7\n"
      "s_waitcnt lgkmcnt(0)\n"
      "s_bitcmp1_b32 s85, 31\n"
      "s_cselect_b32 s75, 100270464, 0\n"
      "s_bitcmp1_b32 s85, 30\n"
      "s_cselect_b32 s76, 196224, 0\n"
      "s_and_b32 s74, s85, 0x3fffffff\n"
      "s_cmp_ge_u32 s85, 0x40000000\n"
      "s_cselect_b32 s74, s74, 0\n"
      "s_cselect_b32 s85, 0, s85\n"
      "s_sub_u32 s74, s74, s69\n"
      "s_add_u32 s69, s69, s74\n"
      "s_sub_u32 s75, s75, s70\n"
      "s_add_u32 s70, s70, s75\n"
      "s_sub_u32 s76, s76, s71\n"
      "s_add_u32 s71, s71, s76\n"
      "v_add_u32 v20, s74, v20\n"
      "v_add_u32 v21, s74, v21\n"
      "v_add_u32 v22, s74, v22\n"
      "v_add_u32 v23, s74, v23\n"
      "v_add_u32 v25, s74, v25\n"
      "s_cmp_eq_u32 s75, 0\n"
      "s_cbranch_scc1 L_ax_29_%=\n"
      "v_bfe_i32 v158, v27, 10, 1\n"
      "v_and_b32 v158, s75, v158\n"
      "v_sub_u32 v20, v20, v158\n"
      "v_bfe_i32 v158, v27, 12, 1\n"
      "v_and_b32 v158, s75, v158\n"
      "v_sub_u32 v21, v21, v158\n"
      "v_bfe_i32 v158, v27, 14, 1\n"
      "v_and_b32 v158, s75, v158\n"
      "v_sub_u32 v22, v22, v158\n"
      "v_bfe_i32 v158, v27, 16, 1\n"
      "v_and_b32 v158, s75, v158\n"
      "v_sub_u32 v23, v23, v158\n"
      "v_bfe_i32 v158, v27, 18, 1\n"
      "v_and_b32 v158, s75, v158\n"
      "v_sub_u32 v25, v25, v158\n"
      "L_ax_29_%=:\n"
      "s_cmp_eq_u32 s76, 0\n"
      "s_cbranch_scc1 L_ax_30_%=\n"
      "v_bfe_i32 v158, v27, 11, 1\n"
      "v_and_b32 v158, s76, v158\n"
      "v_sub_u32 v20, v20, v158\n"
      "v_bfe_i32 v158, v27, 13, 1\n"
      "v_and_b32 v158, s76, v158\n"
      "v_sub_u32 v21, v21, v158\n"
      "v_bfe_i32 v158, v27, 15, 1\n"
      "v_and_b32 v158, s76, v158\n"
      "v_sub_u32 v22, v22, v158\n"
      "v_bfe_i32 v158, v27, 17, 1\n"
      "v_and_b32 v158, s76, v158\n"
      "v_sub_u32 v23, v23, v158\n"
      "v_bfe_i32 v158, v27, 19, 1\n"
      "v_and_b32 v158, s76, v158\n"
      "v_sub_u32 v25, v25, v158\n"
      "L_ax_30_%=:\n"
      "s_or_b32 s48, s66, s69\n"
      "s_cmp_lg_u32 s48, 0\n"
      "s_cselect_b32 s48, 0x40000000, 0\n"
      "s_waitcnt lgkmcnt(0)\n"
      "s_nop 4\n"
      "s_branch L_back_32_%=\n"
      "L_rare_35_%=:\n"
      "s_nop 7\n"
      "s_waitcnt lgkmcnt(0)\n"
      "s_bitcmp1_b32 s46, 31\n"
      "s_cselect_b32 s75, 100270464, 0\n"
      "s_bitcmp1_b32 s46, 30\n"
      "s_cselect_b32 s76, 196224, 0\n"
      "s_and_b32 s74, s46, 0x3fffffff\n"
      "s_cmp_ge_u32 s46, 0x40000000\n"
      "s_cselect_b32 s74, s74, 0\n"
      "s_cselect_b32 s46, 0, s46\n"
      "s_sub_u32 s74, s74, s69\n"
      "s_add_u32 s69, s69, s74\n"
      "s_sub_u32 s75, s75, s70\n"
      "s_add_u32 s70, s70, s75\n"
      "s_sub_u32 s76, s76, s71\n"
      "s_add_u32 s71, s71, s76\n"
      "v_add_u32 v20, s74, v20\n"
      "v_add_u32 v21, s74, v21\n"
      "v_add_u32 v22, s74, v22\n"
      "v_add_u32 v23, s74, v23\n"
      "v_add_u32 v25, s74, v25\n"
      "s_cmp_eq_u32 s75, 0\n"
      "s_cbranch_scc1 L_ax_33_%=\n"
      "v_bfe_i32 v176, v27, 10, 1\n"
      "v_and_b32 v176, s75, v176\n"
      "v_sub_u32 v20, v20, v176\n"
      "v_bfe_i32 v176, v27, 12, 1\n"
      "v_and_b32 v176, s75, v176\n"
      "v_sub_u32 v21, v21, v176\n"
      "v_bfe_i32 v176, v27, 14, 1\n"
      "v_and_b32 v176, s75, v176\n"
      "v_sub_u32 v22, v22, v176\n"
      "v_bfe_i32 v176, v27, 16, 1\n"
      "v_and_b32 v176, s75, v176\n"
      "v_sub_u32 v23, v23, v176\n"
      "v_bfe_i32 v176, v27, 18, 1\n"
      "v_and_b32 v176, s75, v176\n"
      "v_sub_u32 v25, v25, v176\n"
      "L_ax_33_%=:\n"
      "s_cmp_eq_u32 s76, 0\n"
      "s_cbranch_scc1 L_ax_34_%=\n"
      "v_bfe_i32 v176, v27, 11, 1\n"
      "v_and_b32 v176, s76, v176\n"
      "v_sub_u32 v20, v20, v176\n"
      "v_bfe_i32 v176, v27, 13, 1\n"
      "v_and_b32 v176, s76, v176\n"
      "v_sub_u32 v21, v21, v176\n"
      "v_bfe_i32 v176, v27, 15, 1\n"
      "v_and_b32 v176, s76, v176\n"
      "v_sub_u32 v22, v22, v176\n"
      "v_bfe_i32 v176, v27, 17, 1\n"
      "v_and_b32 v176, s76, v176\n"
      "v_sub_u32 v23, v23, v176\n"
      "v_bfe_i32 v176, v27, 19, 1\n"
      "v_and_b32 v176, s76, v176\n"
      "v_sub_u32 v25, v25, v176\n"
      "L_ax_34_%=:\n"
      "s_or_b32 s48, s66, s69\n"
      "s_cmp_lg_u32 s48, 0\n"
      "s_cselect_b32 s48, 0x40000000, 0\n"
      "s_waitcnt lgkmcnt(0)\n"
      "s_nop 4\n"
      "s_branch L_back_36_%=\n"
      "L_rare_39_%=:\n"
      "s_nop 7\n"
      "s_waitcnt lgkmcnt(0)\n"
      "s_bitcmp1_b32 s46, 31\n"
      "s_cselect_b32 s75, 100270464, 0\n"
      "s_bitcmp1_b32 s46, 30\n"
      "s_cselect_b32 s76, 196224, 0\n"
      "s_and_b32 s74, s46, 0x3fffffff\n"
      "s_cmp_ge_u32 s46, 0x40000000\n"
      "s_cselect_b32 s74, s74, 0\n"
      "s_cselect_b32 s46, 0, s46\n"
      "s_sub_u32 s74, s74, s69\n"
      "s_add_u32 s69, s69, s74\n"
      "s_sub_u32 s75, s75, s70\n"
      "s_add_u32 s70, s70, s75\n"
      "s_sub_u32 s76, s76, s71\n"
      "s_add_u32 s71, s71, s76\n"
      "v_add_u32 v20, s74, v20\n"
      "v_add_u32 v21, s74, v21\n"
      "v_add_u32 v22, s74, v22\n"
      "v_add_u32 v23, s74, v23\n"
      "v_add_u32 v25, s74, v25\n"
      "s_cmp_eq_u32 s75, 0\n"
      "s_cbranch_scc1 L_ax_37_%=\n"
      "v_bfe_i32 v223, v27, 10, 1\n"
      "v_and_b32 v223, s75, v223\n"
      "v_sub_u32 v20, v20, v223\n"
      "v_bfe_i32 v223, v27, 12, 1\n"
      "v_and_b32 v223, s75, v223\n"
      "v_sub_u32 v21, v21, v223\n"
      "v_bfe_i32 v223, v27, 14, 1\n"
      "v_and_b32 v223, s75, v223\n"
      "v_sub_u32 v22, v22, v223\n"
      "v_bfe_i32 v223, v27, 16, 1\n"
      "v_and_b32 v223, s75, v223\n"
      "v_sub_u32 v23, v23, v223\n"
      "v_bfe_i32 v223, v27, 18, 1\n"
      "v_and_b32 v223, s75, v223\n"
      "v_sub_u32 v25, v25, v223\n"
      "L_ax_37_%=:\n"
      "s_cmp_eq_u32 s76, 0\n"
      "s_cbranch_scc1 L_ax_38_%=\n"
      "v_bfe_i32 v223, v27, 11, 1\n"
      "v_and_b32 v223, s76, v223\n"
      "v_sub_u32 v20, v20, v223\n"
      "v_bfe_i32 v223, v27, 13, 1\n"
      "v_and_b32 v223, s76, v223\n"
      "v_sub_u32 v21, v21, v223\n"
      "v_bfe_i32 v223, v27, 15, 1\n"
      "v_and_b32 v223, s76, v223\n"
      "v_sub_u32 v22, v22, v223\n"
      "v_bfe_i32 v223, v27, 17, 1\n"
      "v_and_b32 v223, s76, v223\n"
      "v_sub_u32 v23, v23, v223\n"
      "v_bfe_i32 v223, v27, 19, 1\n"
      "v_and_b32 v223, s76, v223\n"
      "v_sub_u32 v25, v25, v223\n"
      "L_ax_38_%=:\n"
      "s_or_b32 s48, s66, s69\n"
      "s_cmp_lg_u32 s48, 0\n"
      "s_cselect_b32 s48, 0x40000000, 0\n"
      "s_waitcnt lgkmcnt(0)\n"
      "s_nop 4\n"
      "s_branch L_back_40_%=\n"
      "L_cend_%=:\n"
      :
      : "s"(__builtin_amdgcn_kernarg_segment_ptr()), "s"(wave), "s"(blockIdx.x), "s"(CT_TAB), "s"(lds0)
      : "v0","v1","v2","v3","v4","v5","v6","v7","v8","v9","v10","v11","v12","v13","v14","v15","v16","v17","v18","v19","v20","v21","v22","v23","v24","v25","v26","v27","v28","v29","v30","v31","v32","v33","v34","v35","v36","v37","v38","v39","v40","v41","v42","v43","v44","v45","v46","v47","v48","v49","v50","v51","v52","v53","v54","v55","v56","v57","v58","v59","v60","v61","v62","v63","v64","v65","v66","v67","v68","v69","v70","v71","v72","v73","v74","v75","v76","v77","v78","v79","v80","v81","v82","v83","v84","v85","v86","v87","v88","v89","v90","v91","v92","v93","v94","v95","v96","v97","v98","v99","v100","v101","v102","v103","v104","v105","v106","v107","v108","v109","v110","v111","v112","v113","v114","v115","v116","v117","v118","v119","v120","v121","v122","v123","v124","v125","v126","v127","v128","v129","v130","v131","v132","v133","v134","v135","v136","v137","v138","v139","v140","v141","v142","v143","v144","v145","v146","v147","v148","v149","v150","v151","v152","v153","v154","v155","v156","v157","v158","v159","v160","v161","v162","v163","v164","v165","v166","v167","v168","v169","v170","v171","v172","v173","v174","v175","v176","v177","v178","v179","v180","v181","v182","v183","v184","v185","v186","v187","v188","v189","v190","v191","v192","v193","v194","v195","v196","v197","v198","v199","v200","v201","v202","v203","v204","v205","v206","v207","v208","v209","v210","v211","v212","v213","v214","v215","v216","v217","v218","v219","v220","v221","v222","v223","v224","v225","v226","v227","v228","v229","v230","v231","v232","v233","v234","v235","v236","v237","v238","v239","v240","v241","v242","v243","v244","v245","v246","v247","v248","v249","v250","v251","v252","v253","v254","v255","a0","a1","a2","a3","a4","a5","a6","a7","a8","a9","a10","a11","a12","a13","a14","a15","a16","a17","a18","a19","a20","a21","a22","a23","a24","a25","a26","a27","a28","a29","a30","a31","a32","a33","a34","a35","a36","a37","a38","a39","a40","a41","a42","a43","a44","a45","a46","a47","a48","a49","a50","a51","a52","a53","a54","a55","a56","a57","a58","a59","a60","a61","a62","a63","a64","a65","a66","a67","a68","a69","a70","a71","a72","a73","a74","a75","a76","a77","a78","a79","a80","a81","a82","a83","a84","a85","a86","a87","a88","a89","a90","a91","a92","a93","a94","a95","a96","a97","a98","a99","a100","a101","a102","a103","a104","a105","a106","a107","a108","a109","a110","a111","a112","a113","a114","a115","a116","a117","a118","a119","a120","a121","a122","a123","a124","a125","a126","a127","a128","a129","a130","a131","a132","a133","a134","a135","a136","a137","a138","a139","a140","a141","a142","a143","a144","a145","a146","a147","a148","a149","a150","a151","a152","a153","a154","a155","a156","a157","a158","a159","a160","a161","a162","a163","a164","a165","a166","a167","a168","a169","a170","a171","a172","a173","a174","a175","a176","a177","a178","a179","a180","a181","a182","a183","a184","a185","a186","a187","a188","a189","a190","a191","a192","a193","a194","a195","a196","a197","a198","a199","a200","a201","a202","a203","a204","a205","a206","a207","a208","a209","a210","a211","a212","a213","a214","a215","a216","a217","a218","a219","a220","a221","a222","a223","a224","a225","a226","a227","a228","a229","a230","a231","a232","a233","a234","a235","a236","a237","a238","a239","a240","a241","a242","a243","a244","a245","a246","a247","a248","a249","a250","a251","a252","a253","a254","a255","s16","s17","s18","s19","s20","s21","s22","s23","s24","s25","s26","s27","s28","s29","s30","s31","s33","s34","s35","s36","s37","s38","s39","s40","s41","s42","s43","s44","s45","s46","s47","s48","s49","s50","s51","s52","s53","s54","s55","s56","s57","s58","s59","s60","s61","s62","s63","s64","s65","s66","s67","s68","s69","s70","s71","s72","s73","s74","s75","s76","s77","s78","s79","s80","s81","s82","s83","s84","s85","s86","s87","s88","s89","s90","s91","s92","s93","s94","s95","s96","s97","s98","s99","s100","s101", "vcc", "scc", "memory");
}

}

extern "C" void kernel_launch(void* const* d_in, const int* in_sizes, int n_in, void* d_out, int out_size, void* d_ws,
                              size_t ws_size, hipStream_t stream) {
  const uint16_t* x = (const uint16_t*)d_in[0];
  const uint16_t* wqkv = (const uint16_t*)d_in[1];
  const uint16_t* wo = (const uint16_t*)d_in[2];
  const uint16_t* ls = (const uint16_t*)d_in[3];
  const uint16_t* w1 = (const uint16_t*)d_in[4];
  const uint16_t* w2 = (const uint16_t*)d_in[5];
  const uint16_t* rc = (const uint16_t*)d_in[6];
  const int* ridx = (const int*)d_in[7];
  const uint16_t* mask = (const uint16_t*)d_in[8];
  swin::swin_main<<<256, 256, 0, stream>>>(x, mask, (char*)d_ws, (uint16_t*)d_out, wqkv, wo, ls, w1, w2, rc, ridx);
}
```

```cpp
#include <hip/hip_runtime.h>
#include <hip/hip_bf16.h>
#include <stdint.h>

namespace swin {

constexpr unsigned CT_WORDS = 8192u;
constexpr unsigned LDS_BYTES = 163840u;

__device__ __attribute__((aligned(256))) const unsigned CT_TAB[CT_WORDS] = {0x4800u,0x4820u,0x4840u,0x4800u,0x4808u,0x4828u,0x4848u,0x4810u,0x4810u,0x4830u,0x4850u,0x4820u,0x4818u,0x4838u,0x4858u,0x4830u,0x4880u,0x48a0u,0x48c0u,0x4880u,0x4888u,0x48a8u,0x48c8u,0x4890u,0x4890u,0x48b0u,0x48d0u,0x48a0u,0x4898u,0x48b8u,0x48d8u,0x48b0u,0x4920u,0x4900u,0x4960u,0x4920u,0x4928u,0x4908u,0x4968u,0x4930u,0x4930u,0x4910u,0x4970u,0x4900u,0x4938u,0x4918u,0x4978u,0x4910u,0x49a0u,0x4980u,0x49e0u,0x49a0u,0x49a8u,0x4988u,0x49e8u,0x49b0u,0x49b0u,0x4990u,0x49f0u,0x4980u,0x49b8u,0x4998u,0x49f8u,0x4990u,0x4c40u,0x4c60u,0x4c00u,0x4a00u,0x4c48u,0x4c68u,0x4c08u,0x4a10u,0x4c50u,0x4c70u,0x4c10u,0x4a20u,0x4c58u,0x4c78u,0x4c18u,0x4a30u,0x4cc0u,0x4ce0u,0x4c80u,0x4a80u,0x4cc8u,0x4ce8u,0x4c88u,0x4a90u,0x4cd0u,0x4cf0u,0x4c90u,0x4aa0u,0x4cd8u,0x4cf8u,0x4c98u,0x4ab0u,0x4d60u,0x4d40u,0x4d20u,0x4b20u,0x4d68u,0x4d48u,0x4d28u,0x4b30u,0x4d70u,0x4d50u,0x4d30u,0x4b00u,0x4d78u,0x4d58u,0x4d38u,0x4b10u,0x4de0u,0x4dc0u,0x4da0u,0x4ba0u,0x4de8u,0x4dc8u,0x4da8u,0x4bb0u,0x4df0u,0x4dd0u,0x4db0u,0x4b80u,0x4df8u,0x4dd8u,0x4db8u,0x4b90u,0x5000u,0x5020u,0x5040u,0x4c40u,0x5008u,0x5028u,0x5048u,0x4c50u,0x5010u,0x5030u,0x5050u,0x4c60u,0x5018u,0x5038u,0x5058u,0x4c70u,0x5080u,0x50a0u,0x50c0u,0x4cc0u,0x5088u,0x50a8u,0x50c8u,0x4cd0u,0x5090u,0x50b0u,0x50d0u,0x4ce0u,0x5098u,0x50b8u,0x50d8u,0x4cf0u,0x5120u,0x5100u,0x5160u,0x4d60u,0x5128u,0x5108u,0x5168u,0x4d70u,0x5130u,0x5110u,0x5170u,0x4d40u,0x5138u,0x5118u,0x5178u,0x4d50u,0x51a0u,0x5180u,0x51e0u,0x4de0u,0x51a8u,0x5188u,0x51e8u,0x4df0u,0x51b0u,0x5190u,0x51f0u,0x4dc0u,0x51b8u,0x5198u,0x51f8u,0x4dd0u,0x5440u,0x5460u,0x5400u,0x4e40u,0x5448u,0x5468u,0x5408u,0x4e50u,0x5450u,0x5470u,0x5410u,0x4e60u,0x5458u,0x5478u,0x5418u,0x4e70u,0x54c0u,0x54e0u,0x5480u,0x4ec0u,0x54c8u,0x54e8u,0x5488u,0x4ed0u,0x54d0u,0x54f0u,0x5490u,0x4ee0u,0x54d8u,0x54f8u,0x5498u,0x4ef0u,0x5560u,0x5540u,0x5520u,0x4f60u,0x5568u,0x5548u,0x5528u,0x4f70u,0x5570u,0x5550u,0x5530u,0x4f40u,0x5578u,0x5558u,0x5538u,0x4f50u,0x55e0u,0x55c0u,0x55a0u,0x4fe0u,0x55e8u,0x55c8u,0x55a8u,0x4ff0u,0x55f0u,0x55d0u,0x55b0u,0x4fc0u,0x55f8u,0x55d8u,0x55b8u,0x4fd0u,0x4840u,0x1e800u,0x1eb00u,0x0u,0x4848u,0x1ec90u,0x1ef90u,0x10u,0x4850u,0x1f120u,0x1f420u,0x20u,0x4858u,0x1f5b0u,0x1f8b0u,0x30u,0x48c0u,0x1fa40u,0x1fd40u,0x40u,0x48c8u,0x1fed0u,0x201d0u,0x50u,0x48d0u,0x20360u,0x20660u,0x60u,0x48d8u,0x207f0u,0x20af0u,0x70u,0x4960u,0x20c80u,0x20f80u,0x180u,0x4968u,0x21110u,0x21410u,0x190u,0x4970u,0x215a0u,0x218a0u,0x1a0u,0x4978u,0x21a30u,0x21d30u,0x1b0u,0x49e0u,0x21ec0u,0x221c0u,0x1c0u,0x49e8u,0x22350u,0x22650u,0x1d0u,0x49f0u,0x227e0u,0x22ae0u,0x1e0u,0x49f8u,0x22c70u,0x22f70u,0x1f0u,0x4a40u,0x1e830u,0x1eb08u,0x320u,0x4a48u,0x1ecc0u,0x1ef98u,0x330u,0x4a50u,0x1f150u,0x1f428u,0x300u,0x4a58u,0x1f5e0u,0x1f8b8u,0x310u,0x4ac0u,0x1fa70u,0x1fd48u,0x360u,0x4ac8u,0x1ff00u,0x201d8u,0x370u,0x4ad0u,0x20390u,0x20668u,0x340u,0x4ad8u,0x20820u,0x20af8u,0x350u,0x4b60u,0x20cb0u,0x20f88u,0x4a0u,0x4b68u,0x21140u,0x21418u,0x4b0u,0x4b70u,0x215d0u,0x218a8u,0x480u,0x4b78u,0x21a60u,0x21d38u,0x490u,0x4be0u,0x21ef0u,0x221c8u,0x4e0u,0x4be8u,0x22380u,0x22658u,0x4f0u,0x4bf0u,0x22810u,0x22ae8u,0x4c0u,0x4bf8u,0x22ca0u,0x22f78u,0x4d0u,0x4c00u,0x1e860u,0x1eb10u,0x600u,0x4c08u,0x1ecf0u,0x1efa0u,0x610u,0x4c10u,0x1f180u,0x1f430u,0x620u,0x4c18u,0x1f610u,0x1f8c0u,0x630u,0x4c80u,0x1faa0u,0x1fd50u,0x640u,0x4c88u,0x1ff30u,0x201e0u,0x650u,0x4c90u,0x203c0u,0x20670u,0x660u,0x4c98u,0x20850u,0x20b00u,0x670u,0x4d20u,0x20ce0u,0x20f90u,0x780u,0x4d28u,0x21170u,0x21420u,0x790u,0x4d30u,0x21600u,0x218b0u,0x7a0u,0x4d38u,0x21a90u,0x21d40u,0x7b0u,0x4da0u,0x21f20u,0x221d0u,0x7c0u,0x4da8u,0x223b0u,0x22660u,0x7d0u,0x4db0u,0x22840u,0x22af0u,0x7e0u,0x4db8u,0x22cd0u,0x22f80u,0x7f0u,0x4e00u,0x1e890u,0x1eb18u,0x920u,0x4e08u,0x1ed20u,0x1efa8u,0x930u,0x4e10u,0x1f1b0u,0x1f438u,0x900u,0x4e18u,0x1f640u,0x1f8c8u,0x910u,0x4e80u,0x1fad0u,0x1fd58u,0x960u,0x4e88u,0x1ff60u,0x201e8u,0x970u,0x4e90u,0x203f0u,0x20678u,0x940u,0x4e98u,0x20880u,0x20b08u,0x950u,0x4f20u,0x20d10u,0x20f98u,0xaa0u,0x4f28u,0x211a0u,0x21428u,0xab0u,0x4f30u,0x21630u,0x218b8u,0xa80u,0x4f38u,0x21ac0u,0x21d48u,0xa90u,0x4fa0u,0x21f50u,0x221d8u,0xae0u,0x4fa8u,0x223e0u,0x22668u,0xaf0u,0x4fb0u,0x22870u,0x22af8u,0xac0u,0x4fb8u,0x22d00u,0x22f88u,0xad0u,0x0u,0x40u,0x9000u,0x9000u,0x80u,0xc0u,0x9010u,0x9010u,0x110u,0x150u,0x9020u,0x9020u,0x190u,0x1d0u,0x9030u,0x9030u,0x220u,0x260u,0x9040u,0x9040u,0x2a0u,0x2e0u,0x9050u,0x9050u,0x330u,0x370u,0x9060u,0x9060u,0x3b0u,0x3f0u,0x9070u,0x9070u,0x440u,0x400u,0x9080u,0x9080u,0x4c0u,0x480u,0x9090u,0x9090u,0x550u,0x510u,0x90a0u,0x90a0u,0x5d0u,0x590u,0x90b0u,0x90b0u,0x660u,0x620u,0x90c0u,0x90c0u,0x6e0u,0x6a0u,0x90d0u,0x90d0u,0x770u,0x730u,0x90e0u,0x90e0u,0x7f0u,0x7b0u,0x90f0u,0x90f0u,0x10u,0x50u,0x9300u,0x9300u,0x90u,0xd0u,0x9310u,0x9310u,0x100u,0x140u,0x9320u,0x9320u,0x180u,0x1c0u,0x9330u,0x9330u,0x230u,0x270u,0x9340u,0x9340u,0x2b0u,0x2f0u,0x9350u,0x9350u,0x320u,0x360u,0x9360u,0x9360u,0x3a0u,0x3e0u,0x9370u,0x9370u,0x450u,0x410u,0x9380u,0x9380u,0x4d0u,0x490u,0x9390u,0x9390u,0x540u,0x500u,0x93a0u,0x93a0u,0x5c0u,0x580u,0x93b0u,0x93b0u,0x670u,0x630u,0x93c0u,0x93c0u,0x6f0u,0x6b0u,0x93d0u,0x93d0u,0x760u,0x720u,0x93e0u,0x93e0u,0x7e0u,0x7a0u,0x93f0u,0x93f0u,0x20u,0x60u,0x9600u,0x9600u,0xa0u,0xe0u,0x9610u,0x9610u,0x130u,0x170u,0x9620u,0x9620u,0x1b0u,0x1f0u,0x9630u,0x9630u,0x200u,0x240u,0x9640u,0x9640u,0x280u,0x2c0u,0x9650u,0x9650u,0x310u,0x350u,0x9660u,0x9660u,0x390u,0x3d0u,0x9670u,0x9670u,0x460u,0x420u,0x9680u,0x9680u,0x4e0u,0x4a0u,0x9690u,0x9690u,0x570u,0x530u,0x96a0u,0x96a0u,0x5f0u,0x5b0u,0x96b0u,0x96b0u,0x640u,0x600u,0x96c0u,0x96c0u,0x6c0u,0x680u,0x96d0u,0x96d0u,0x750u,0x710u,0x96e0u,0x96e0u,0x7d0u,0x790u,0x96f0u,0x96f0u,0x30u,0x70u,0x9900u,0x9900u,0xb0u,0xf0u,0x9910u,0x9910u,0x120u,0x160u,0x9920u,0x9920u,0x1a0u,0x1e0u,0x9930u,0x9930u,0x210u,0x250u,0x9940u,0x9940u,0x290u,0x2d0u,0x9950u,0x9950u,0x300u,0x340u,0x9960u,0x9960u,0x380u,0x3c0u,0x9970u,0x9970u,0x470u,0x430u,0x9980u,0x9980u,0x4f0u,0x4b0u,0x9990u,0x9990u,0x560u,0x520u,0x99a0u,0x99a0u,0x5e0u,0x5a0u,0x99b0u,0x99b0u,0x650u,0x610u,0x99c0u,0x99c0u,0x6d0u,0x690u,0x99d0u,0x99d0u,0x740u,0x700u,0x99e0u,0x99e0u,0x7c0u,0x780u,0x99f0u,0x99f0u,0x9c00u,0x12000u,0x12040u,0x12000u,0x9c10u,0x12190u,0x121d0u,0x12010u,0x9c20u,0x12320u,0x12360u,0x12020u,0x9c30u,0x124b0u,0x124f0u,0x12030u,0x9c40u,0x12640u,0x12600u,0x12040u,0x9c50u,0x127d0u,0x12790u,0x12050u,0x9c60u,0x12960u,0x12920u,0x12060u,0x9c70u,0x12af0u,0x12ab0u,0x12070u,0x9c80u,0x12c00u,0x12c40u,0x12080u,0x9c90u,0x12d90u,0x12dd0u,0x12090u,0x9ca0u,0x12f20u,0x12f60u,0x120a0u,0x9cb0u,0x130b0u,0x130f0u,0x120b0u,0x9cc0u,0x13240u,0x13200u,0x120c0u,0x9cd0u,0x133d0u,0x13390u,0x120d0u,0x9ce0u,0x13560u,0x13520u,0x120e0u,0x9cf0u,0x136f0u,0x136b0u,0x120f0u,0x9f00u,0x12010u,0x12048u,0x12100u,0x9f10u,0x12180u,0x121d8u,0x12110u,0x9f20u,0x12330u,0x12368u,0x12120u,0x9f30u,0x124a0u,0x124f8u,0x12130u,0x9f40u,0x12650u,0x12608u,0x12140u,0x9f50u,0x127c0u,0x12798u,0x12150u,0x9f60u,0x12970u,0x12928u,0x12160u,0x9f70u,0x12ae0u,0x12ab8u,0x12170u,0x9f80u,0x12c10u,0x12c48u,0x12180u,0x9f90u,0x12d80u,0x12dd8u,0x12190u,0x9fa0u,0x12f30u,0x12f68u,0x121a0u,0x9fb0u,0x130a0u,0x130f8u,0x121b0u,0x9fc0u,0x13250u,0x13208u,0x121c0u,0x9fd0u,0x133c0u,0x13398u,0x121d0u,0x9fe0u,0x13570u,0x13528u,0x121e0u,0x9ff0u,0x136e0u,0x136b8u,0x121f0u,0xa200u,0x12020u,0x12050u,0x12200u,0xa210u,0x121b0u,0x121c0u,0x12210u,0xa220u,0x12300u,0x12370u,0x12220u,0xa230u,0x12490u,0x124e0u,0x12230u,0xa240u,0x12660u,0x12610u,0x12240u,0xa250u,0x127f0u,0x12780u,0x12250u,0xa260u,0x12940u,0x12930u,0x12260u,0xa270u,0x12ad0u,0x12aa0u,0x12270u,0xa280u,0x12c20u,0x12c50u,0x12280u,0xa290u,0x12db0u,0x12dc0u,0x12290u,0xa2a0u,0x12f00u,0x12f70u,0x122a0u,0xa2b0u,0x13090u,0x130e0u,0x122b0u,0xa2c0u,0x13260u,0x13210u,0x122c0u,0xa2d0u,0x133f0u,0x13380u,0x122d0u,0xa2e0u,0x13540u,0x13530u,0x122e0u,0xa2f0u,0x136d0u,0x136a0u,0x122f0u,0xa500u,0x12030u,0x12058u,0x12300u,0xa510u,0x121a0u,0x121c8u,0x12310u,0xa520u,0x12310u,0x12378u,0x12320u,0xa530u,0x12480u,0x124e8u,0x12330u,0xa540u,0x12670u,0x12618u,0x12340u,0xa550u,0x127e0u,0x12788u,0x12350u,0xa560u,0x12950u,0x12938u,0x12360u,0xa570u,0x12ac0u,0x12aa8u,0x12370u,0xa580u,0x12c30u,0x12c58u,0x12380u,0xa590u,0x12da0u,0x12dc8u,0x12390u,0xa5a0u,0x12f10u,0x12f78u,0x123a0u,0xa5b0u,0x13080u,0x130e8u,0x123b0u,0xa5c0u,0x13270u,0x13218u,0x123c0u,0xa5d0u,0x133e0u,0x13388u,0x123d0u,0xa5e0u,0x13550u,0x13538u,0x123e0u,0xa5f0u,0x136c0u,0x136a8u,0x123f0u,0x4800u,0x1c300u,0x1e800u,0x1eb00u,0x4880u,0x1c310u,0x100000u,0x1eb02u,0x4910u,0x1c320u,0x100000u,0x1eb04u,0x4990u,0x1c330u,0x100000u,0x1eb06u,0x4a20u,0x1c340u,0x1e800u,0x1eb08u,0x4aa0u,0x1c350u,0x100000u,0x1eb0au,0x4b30u,0x1c360u,0x100000u,0x1eb0cu,0x4bb0u,0x1c370u,0x100000u,0x1eb0eu,0x4c40u,0x1c380u,0x1e800u,0x1eb10u,0x4cc0u,0x1c390u,0x100000u,0x1eb12u,0x4d50u,0x1c3a0u,0x100000u,0x1eb14u,0x4dd0u,0x1c3b0u,0x100000u,0x1eb16u,0x4e60u,0x1c3c0u,0x1e800u,0x1eb18u,0x4ee0u,0x1c3d0u,0x100000u,0x1eb1au,0x4f70u,0x1c3e0u,0x100000u,0x1eb1cu,0x4ff0u,0x1c3f0u,0x100000u,0x1eb1eu,0x4810u,0x1c400u,0x1e830u,0x1eb00u,0x4890u,0x1c410u,0x100000u,0x1eb02u,0x4900u,0x1c420u,0x100000u,0x1eb04u,0x4980u,0x1c430u,0x100000u,0x1eb06u,0x4a30u,0x1c440u,0x1e830u,0x1eb08u,0x4ab0u,0x1c450u,0x100000u,0x1eb0au,0x4b20u,0x1c460u,0x100000u,0x1eb0cu,0x4ba0u,0x1c470u,0x100000u,0x1eb0eu,0x4c50u,0x1c480u,0x1e830u,0x1eb10u,0x4cd0u,0x1c490u,0x100000u,0x1eb12u,0x4d40u,0x1c4a0u,0x100000u,0x1eb14u,0x4dc0u,0x1c4b0u,0x100000u,0x1eb16u,0x4e70u,0x1c4c0u,0x1e830u,0x1eb18u,0x4ef0u,0x1c4d0u,0x100000u,0x1eb1au,0x4f60u,0x1c4e0u,0x100000u,0x1eb1cu,0x4fe0u,0x1c4f0u,0x100000u,0x1eb1eu,0x4820u,0x1c500u,0x1e860u,0x1eb00u,0x48a0u,0x1c510u,0x100000u,0x1eb02u,0x4930u,0x1c520u,0x100000u,0x1eb04u,0x49b0u,0x1c530u,0x100000u,0x1eb06u,0x4a00u,0x1c540u,0x1e860u,0x1eb08u,0x4a80u,0x1c550u,0x100000u,0x1eb0au,0x4b10u,0x1c560u,0x100000u,0x1eb0cu,0x4b90u,0x1c570u,0x100000u,0x1eb0eu,0x4c60u,0x1c580u,0x1e860u,0x1eb10u,0x4ce0u,0x1c590u,0x100000u,0x1eb12u,0x4d70u,0x1c5a0u,0x100000u,0x1eb14u,0x4df0u,0x1c5b0u,0x100000u,0x1eb16u,0x4e40u,0x1c5c0u,0x1e860u,0x1eb18u,0x4ec0u,0x1c5d0u,0x100000u,0x1eb1au,0x4f50u,0x1c5e0u,0x100000u,0x1eb1cu,0x4fd0u,0x1c5f0u,0x100000u,0x1eb1eu,0x4830u,0x1c600u,0x1e890u,0x1eb00u,0x48b0u,0x1c610u,0x100000u,0x1eb02u,0x4920u,0x1c620u,0x100000u,0x1eb04u,0x49a0u,0x1c630u,0x100000u,0x1eb06u,0x4a10u,0x1c640u,0x1e890u,0x1eb08u,0x4a90u,0x1c650u,0x100000u,0x1eb0au,0x4b00u,0x1c660u,0x100000u,0x1eb0cu,0x4b80u,0x1c670u,0x100000u,0x1eb0eu,0x4c70u,0x1c680u,0x1e890u,0x1eb10u,0x4cf0u,0x1c690u,0x100000u,0x1eb12u,0x4d60u,0x1c6a0u,0x100000u,0x1eb14u,0x4de0u,0x1c6b0u,0x100000u,0x1eb16u,0x4e50u,0x1c6c0u,0x1e890u,0x1eb18u,0x4ed0u,0x1c6d0u,0x100000u,0x1eb1au,0x4f40u,0x1c6e0u,0x100000u,0x1eb1cu,0x4fc0u,0x1c6f0u,0x100000u,0x1eb1eu,0x24d00u,0x1d300u,0x1d300u,0xc0100u,0x24d02u,0x1d302u,0x100000u,0xc0110u,0x24d04u,0x1d304u,0x100000u,0xc0120u,0x24d06u,0x1d306u,0x100000u,0xc0130u,0x24f00u,0x1d310u,0x1d300u,0xc0140u,0x24f02u,0x1d312u,0x100000u,0xc0150u,0x24f04u,0x1d314u,0x100000u,0xc0160u,0x24f06u,0x1d316u,0x100000u,0xc0170u,0x25100u,0x1d320u,0x1d300u,0xc0280u,0x25102u,0x1d322u,0x100000u,0xc0290u,0x25104u,0x1d324u,0x100000u,0xc02a0u,0x25106u,0x1d326u,0x100000u,0xc02b0u,0x25300u,0x1d330u,0x1d300u,0xc02c0u,0x25302u,0x1d332u,0x100000u,0xc02d0u,0x25304u,0x1d334u,0x100000u,0xc02e0u,0x25306u,0x1d336u,0x100000u,0xc02f0u,0x24d08u,0x1d308u,0x1d310u,0xc0410u,0x24d0au,0x1d30au,0x100000u,0xc0400u,0x24d0cu,0x1d30cu,0x100000u,0xc0430u,0x24d0eu,0x1d30eu,0x100000u,0xc0420u,0x24f08u,0x1d318u,0x1d310u,0xc0450u,0x24f0au,0x1d31au,0x100000u,0xc0440u,0x24f0cu,0x1d31cu,0x100000u,0xc0470u,0x24f0eu,0x1d31eu,0x100000u,0xc0460u,0x25108u,0x1d328u,0x1d310u,0xef990u,0x2510au,0x1d32au,0x100000u,0xef980u,0x2510cu,0x1d32cu,0x100000u,0xef9b0u,0x2510eu,0x1d32eu,0x100000u,0xef9a0u,0x25308u,0x1d338u,0x1d310u,0xef9d0u,0x2530au,0x1d33au,0x100000u,0xef9c0u,0x2530cu,0x1d33cu,0x100000u,0xef9f0u,0x2530eu,0x1d33eu,0x100000u,0xef9e0u,0x25500u,0x1d340u,0x1d320u,0xefb20u,0x25502u,0x1d342u,0x100000u,0xefb30u,0x25504u,0x1d344u,0x100000u,0xefb00u,0x25506u,0x1d346u,0x100000u,0xefb10u,0x25700u,0x1d350u,0x1d320u,0xefb60u,0x25702u,0x1d352u,0x100000u,0xefb70u,0x25704u,0x1d354u,0x100000u,0xefb40u,0x25706u,0x1d356u,0x100000u,0xefb50u,0x25900u,0x1d360u,0x1d320u,0xefca0u,0x25902u,0x1d362u,0x100000u,0xefcb0u,0x25904u,0x1d364u,0x100000u,0xefc80u,0x25906u,0x1d366u,0x100000u,0xefc90u,0x25b00u,0x1d370u,0x1d320u,0xefce0u,0x25b02u,0x1d372u,0x100000u,0xefcf0u,0x25b04u,0x1d374u,0x100000u,0xefcc0u,0x25b06u,0x1d376u,0x100000u,0xefcd0u,0x100000u,0x1d348u,0x1d330u,0xefe30u,0x100000u,0x1d34au,0x100000u,0xefe20u,0x100000u,0x1d34cu,0x100000u,0xefe10u,0x100000u,0x1d34eu,0x100000u,0xefe00u,0x100000u,0x1d358u,0x1d330u,0xefe70u,0x100000u,0x1d35au,0x100000u,0xefe60u,0x100000u,0x1d35cu,0x100000u,0xefe50u,0x100000u,0x1d35eu,0x100000u,0xefe40u,0x100000u,0x1d368u,0x1d330u,0xeffb0u,0x100000u,0x1d36au,0x100000u,0xeffa0u,0x100000u,0x1d36cu,0x100000u,0xeff90u,0x100000u,0x1d36eu,0x100000u,0xeff80u,0x100000u,0x1d378u,0x1d330u,0xefff0u,0x100000u,0x1d37au,0x100000u,0xeffe0u,0x100000u,0x1d37cu,0x100000u,0xeffd0u,0x100000u,0x1d37eu,0x100000u,0xeffc0u,0x0u,0x30420u,0x8fc50u,0xc0000u,0x10u,0x30430u,0x8fc40u,0xc0010u,0x20u,0x30400u,0x8fc70u,0xc0020u,0x30u,0x30410u,0x8fc60u,0xc0030u,0x40u,0x30460u,0x8fc10u,0xc0040u,0x50u,0x30470u,0x8fc00u,0xc0050u,0x60u,0x30440u,0x8fc30u,0xc0060u,0x70u,0x30450u,0x8fc20u,0xc0070u,0x80u,0x304b0u,0x8fcd0u,0xc0080u,0x90u,0x304a0u,0x8fcc0u,0xc0090u,0xa0u,0x30490u,0x8fcf0u,0xc00a0u,0xb0u,0x30480u,0x8fce0u,0xc00b0u,0xc0u,0x304f0u,0x8fc90u,0xc00c0u,0xd0u,0x304e0u,0x8fc80u,0xc00d0u,0xe0u,0x304d0u,0x8fcb0u,0xc00e0u,0xf0u,0x304c0u,0x8fca0u,0xc00f0u,0x100u,0x30530u,0x8fd60u,0xc0100u,0x110u,0x30520u,0x8fd70u,0xc0110u,0x120u,0x30510u,0x8fd40u,0xc0120u,0x130u,0x30500u,0x8fd50u,0xc0130u,0x140u,0x30570u,0x8fd20u,0xc0140u,0x150u,0x30560u,0x8fd30u,0xc0150u,0x160u,0x30550u,0x8fd00u,0xc0160u,0x170u,0x30540u,0x8fd10u,0xc0170u,0x190u,0x305b0u,0x8fde0u,0xc0190u,0x180u,0x305a0u,0x8fdf0u,0xc0180u,0x1b0u,0x30590u,0x8fdc0u,0xc01b0u,0x1a0u,0x30580u,0x8fdd0u,0xc01a0u,0x1d0u,0x305f0u,0x8fda0u,0xc01d0u,0x1c0u,0x305e0u,0x8fdb0u,0xc01c0u,0x1f0u,0x305d0u,0x8fd80u,0xc01f0u,0x1e0u,0x305c0u,0x8fd90u,0xc01e0u,0x210u,0x30640u,0x8fe60u,0xc0210u,0x200u,0x30650u,0x8fe70u,0xc0200u,0x230u,0x30660u,0x8fe40u,0xc0230u,0x220u,0x30670u,0x8fe50u,0xc0220u,0x250u,0x30600u,0x8fe20u,0xc0250u,0x240u,0x30610u,0x8fe30u,0xc0240u,0x270u,0x30620u,0x8fe00u,0xc0270u,0x260u,0x30630u,0x8fe10u,0xc0260u,0x290u,0x306c0u,0x8fef0u,0xc0290u,0x280u,0x306d0u,0x8fee0u,0xc0280u,0x2b0u,0x306e0u,0x8fed0u,0xc02b0u,0x2a0u,0x306f0u,0x8fec0u,0xc02a0u,0x2d0u,0x30680u,0x8feb0u,0xc02d0u,0x2c0u,0x30690u,0x8fea0u,0xc02c0u,0x2f0u,0x306a0u,0x8fe90u,0xc02f0u,0x2e0u,0x306b0u,0x8fe80u,0xc02e0u,0x320u,0x30740u,0x8ff70u,0xc0320u,0x330u,0x30750u,0x8ff60u,0xc0330u,0x300u,0x30760u,0x8ff50u,0xc0300u,0x310u,0x30770u,0x8ff40u,0xc0310u,0x360u,0x30700u,0x8ff30u,0xc0360u,0x370u,0x30710u,0x8ff20u,0xc0370u,0x340u,0x30720u,0x8ff10u,0xc0340u,0x350u,0x30730u,0x8ff00u,0xc0350u,0x3a0u,0x307d0u,0x8fff0u,0xc03a0u,0x3b0u,0x307c0u,0x8ffe0u,0xc03b0u,0x380u,0x307f0u,0x8ffd0u,0xc0380u,0x390u,0x307e0u,0x8ffc0u,0xc0390u,0x3e0u,0x30790u,0x8ffb0u,0xc03e0u,0x3f0u,0x30780u,0x8ffa0u,0xc03f0u,0x3c0u,0x307b0u,0x8ff90u,0xc03c0u,0x3d0u,0x307a0u,0x8ff80u,0xc03d0u,0x0u,0x11f820u,0x0u,0x7030cu,0x0u,0x11f830u,0x0u,0x7030cu,0x0u,0x11f800u,0x0u,0x7030cu,0x0u,0x11f810u,0x0u,0x7030cu,0x0u,0x11f860u,0x0u,0x7030cu,0x0u,0x11f870u,0x0u,0x7030cu,0x0u,0x11f840u,0x0u,0x7030cu,0x0u,0x11f850u,0x0u,0x7030cu,0x0u,0x11f8b0u,0x0u,0x7230cu,0x0u,0x11f8a0u,0x0u,0x7230cu,0x0u,0x11f890u,0x0u,0x7230cu,0x0u,0x11f880u,0x0u,0x7230cu,0x0u,0x11f8f0u,0x0u,0x7230cu,0x0u,0x11f8e0u,0x0u,0x7230cu,0x0u,0x11f8d0u,0x0u,0x7230cu,0x0u,0x11f8c0u,0x0u,0x7230cu,0x0u,0x11f930u,0x0u,0x7232cu,0x0u,0x11f920u,0x0u,0x7232cu,0x0u,0x11f910u,0x0u,0x7232cu,0x0u,0x11f900u,0x0u,0x7232cu,0x0u,0x11f970u,0x0u,0x7232cu,0x0u,0x11f960u,0x0u,0x7232cu,0x0u,0x11f950u,0x0u,0x7232cu,0x0u,0x11f940u,0x0u,0x7232cu,0x0u,0x11f9b0u,0x0u,0x72124u,0x0u,0x11f9a0u,0x0u,0x72124u,0x0u,0x11f990u,0x0u,0x72124u,0x0u,0x11f980u,0x0u,0x72124u,0x0u,0x11f9f0u,0x0u,0x72124u,0x0u,0x11f9e0u,0x0u,0x72124u,0x0u,0x11f9d0u,0x0u,0x72124u,0x0u,0x11f9c0u,0x0u,0x72124u,0x0u,0x11fa40u,0x0u,0x721a6u,0x0u,0x11fa50u,0x0u,0x721a6u,0x0u,0x11fa60u,0x0u,0x721a6u,0x0u,0x11fa70u,0x0u,0x721a6u,0x0u,0x11fa00u,0x0u,0x721a6u,0x0u,0x11fa10u,0x0u,0x721a6u,0x0u,0x11fa20u,0x0u,0x721a6u,0x0u,0x11fa30u,0x0u,0x721a6u,0x0u,0x11fac0u,0x0u,0x72186u,0x0u,0x11fad0u,0x0u,0x72186u,0x0u,0x11fae0u,0x0u,0x72186u,0x0u,0x11faf0u,0x0u,0x72186u,0x0u,0x11fa80u,0x0u,0x72186u,0x0u,0x11fa90u,0x0u,0x72186u,0x0u,0x11faa0u,0x0u,0x72186u,0x0u,0x11fab0u,0x0u,0x72186u,0x0u,0x11fb40u,0x0u,0x72186u,0x0u,0x11fb50u,0x0u,0x72186u,0x0u,0x11fb60u,0x0u,0x72186u,0x0u,0x11fb70u,0x0u,0x72186u,0x0u,0x11fb00u,0x0u,0x72186u,0x0u,0x11fb10u,0x0u,0x72186u,0x0u,0x11fb20u,0x0u,0x72186u,0x0u,0x11fb30u,0x0u,0x72186u,0x0u,0x11fbd0u,0x0u,0x7230cu,0x0u,0x11fbc0u,0x0u,0x7230cu,0x0u,0x11fbf0u,0x0u,0x7230cu,0x0u,0x11fbe0u,0x0u,0x7230cu,0x0u,0x11fb90u,0x0u,0x7230cu,0x0u,0x11fb80u,0x0u,0x7230cu,0x0u,0x11fbb0u,0x0u,0x7230cu,0x0u,0x11fba0u,0x0u,0x7230cu,0x5800u,0x5820u,0x4860u,0x4860u,0x5808u,0x5828u,0x4868u,0x4870u,0x5810u,0x5830u,0x4870u,0x5800u,0x5818u,0x5838u,0x4878u,0x5810u,0x5880u,0x58a0u,0x48e0u,0x48e0u,0x5888u,0x58a8u,0x48e8u,0x48f0u,0x5890u,0x58b0u,0x48f0u,0x5880u,0x5898u,0x58b8u,0x48f8u,0x5890u,0x5920u,0x5900u,0x4940u,0x4940u,0x5928u,0x5908u,0x4948u,0x4950u,0x5930u,0x5910u,0x4950u,0x5920u,0x5938u,0x5918u,0x4958u,0x5930u,0x59a0u,0x5980u,0x49c0u,0x49c0u,0x59a8u,0x5988u,0x49c8u,0x49d0u,0x59b0u,0x5990u,0x49d0u,0x59a0u,0x59b8u,0x5998u,0x49d8u,0x59b0u,0x5c40u,0x5c60u,0x4c20u,0x4a60u,0x5c48u,0x5c68u,0x4c28u,0x4a70u,0x5c50u,0x5c70u,0x4c30u,0x5a00u,0x5c58u,0x5c78u,0x4c38u,0x5a10u,0x5cc0u,0x5ce0u,0x4ca0u,0x4ae0u,0x5cc8u,0x5ce8u,0x4ca8u,0x4af0u,0x5cd0u,0x5cf0u,0x4cb0u,0x5a80u,0x5cd8u,0x5cf8u,0x4cb8u,0x5a90u,0x5d60u,0x5d40u,0x4d00u,0x4b40u,0x5d68u,0x5d48u,0x4d08u,0x4b50u,0x5d70u,0x5d50u,0x4d10u,0x5b20u,0x5d78u,0x5d58u,0x4d18u,0x5b30u,0x5de0u,0x5dc0u,0x4d80u,0x4bc0u,0x5de8u,0x5dc8u,0x4d88u,0x4bd0u,0x5df0u,0x5dd0u,0x4d90u,0x5ba0u,0x5df8u,0x5dd8u,0x4d98u,0x5bb0u,0x6000u,0x6020u,0x5060u,0x4c20u,0x6008u,0x6028u,0x5068u,0x4c30u,0x6010u,0x6030u,0x5070u,0x5c40u,0x6018u,0x6038u,0x5078u,0x5c50u,0x6080u,0x60a0u,0x50e0u,0x4ca0u,0x6088u,0x60a8u,0x50e8u,0x4cb0u,0x6090u,0x60b0u,0x50f0u,0x5cc0u,0x6098u,0x60b8u,0x50f8u,0x5cd0u,0x6120u,0x6100u,0x5140u,0x4d00u,0x6128u,0x6108u,0x5148u,0x4d10u,0x6130u,0x6110u,0x5150u,0x5d60u,0x6138u,0x6118u,0x5158u,0x5d70u,0x61a0u,0x6180u,0x51c0u,0x4d80u,0x61a8u,0x6188u,0x51c8u,0x4d90u,0x61b0u,0x6190u,0x51d0u,0x5de0u,0x61b8u,0x6198u,0x51d8u,0x5df0u,0x6440u,0x6460u,0x5420u,0x4e20u,0x6448u,0x6468u,0x5428u,0x4e30u,0x6450u,0x6470u,0x5430u,0x5e40u,0x6458u,0x6478u,0x5438u,0x5e50u,0x64c0u,0x64e0u,0x54a0u,0x4ea0u,0x64c8u,0x64e8u,0x54a8u,0x4eb0u,0x64d0u,0x64f0u,0x54b0u,0x5ec0u,0x64d8u,0x64f8u,0x54b8u,0x5ed0u,0x6560u,0x6540u,0x5500u,0x4f00u,0x6568u,0x6548u,0x5508u,0x4f10u,0x6570u,0x6550u,0x5510u,0x5f60u,0x6578u,0x6558u,0x5518u,0x5f70u,0x65e0u,0x65c0u,0x5580u,0x4f80u,0x65e8u,0x65c8u,0x5588u,0x4f90u,0x65f0u,0x65d0u,0x5590u,0x5fe0u,0x65f8u,0x65d8u,0x5598u,0x5ff0u,0x5820u,0x1e8c0u,0x1eb60u,0xc40u,0x5828u,0x1ed50u,0x1eff0u,0xc50u,0x5830u,0x1f1e0u,0x1f480u,0xc60u,0x5838u,0x1f670u,0x1f910u,0xc70u,0x58a0u,0x1fb00u,0x1fda0u,0xc00u,0x58a8u,0x1ff90u,0x20230u,0xc10u,0x58b0u,0x20420u,0x206c0u,0xc20u,0x58b8u,0x208b0u,0x20b50u,0xc30u,0x5900u,0x20d40u,0x20fe0u,0xdc0u,0x5908u,0x211d0u,0x21470u,0xdd0u,0x5910u,0x21660u,0x21900u,0xde0u,0x5918u,0x21af0u,0x21d90u,0xdf0u,0x5980u,0x21f80u,0x22220u,0xd80u,0x5988u,0x22410u,0x226b0u,0xd90u,0x5990u,0x228a0u,0x22b40u,0xda0u,0x5998u,0x22d30u,0x22fd0u,0xdb0u,0x5a20u,0x1e8f0u,0x1eb68u,0xf60u,0x5a28u,0x1ed80u,0x1eff8u,0xf70u,0x5a30u,0x1f210u,0x1f488u,0xf40u,0x5a38u,0x1f6a0u,0x1f918u,0xf50u,0x5aa0u,0x1fb30u,0x1fda8u,0xf20u,0x5aa8u,0x1ffc0u,0x20238u,0xf30u,0x5ab0u,0x20450u,0x206c8u,0xf00u,0x5ab8u,0x208e0u,0x20b58u,0xf10u,0x5b00u,0x20d70u,0x20fe8u,0x10e0u,0x5b08u,0x21200u,0x21478u,0x10f0u,0x5b10u,0x21690u,0x21908u,0x10c0u,0x5b18u,0x21b20u,0x21d98u,0x10d0u,0x5b80u,0x21fb0u,0x22228u,0x10a0u,0x5b88u,0x22440u,0x226b8u,0x10b0u,0x5b90u,0x228d0u,0x22b48u,0x1080u,0x5b98u,0x22d60u,0x22fd8u,0x1090u,0x5c60u,0x1e920u,0x1eb70u,0x1240u,0x5c68u,0x1edb0u,0x1f000u,0x1250u,0x5c70u,0x1f240u,0x1f490u,0x1260u,0x5c78u,0x1f6d0u,0x1f920u,0x1270u,0x5ce0u,0x1fb60u,0x1fdb0u,0x1200u,0x5ce8u,0x1fff0u,0x20240u,0x1210u,0x5cf0u,0x20480u,0x206d0u,0x1220u,0x5cf8u,0x20910u,0x20b60u,0x1230u,0x5d40u,0x20da0u,0x20ff0u,0x13c0u,0x5d48u,0x21230u,0x21480u,0x13d0u,0x5d50u,0x216c0u,0x21910u,0x13e0u,0x5d58u,0x21b50u,0x21da0u,0x13f0u,0x5dc0u,0x21fe0u,0x22230u,0x1380u,0x5dc8u,0x22470u,0x226c0u,0x1390u,0x5dd0u,0x22900u,0x22b50u,0x13a0u,0x5dd8u,0x22d90u,0x22fe0u,0x13b0u,0x5e60u,0x1e950u,0x1eb78u,0x1560u,0x5e68u,0x1ede0u,0x1f008u,0x1570u,0x5e70u,0x1f270u,0x1f498u,0x1540u,0x5e78u,0x1f700u,0x1f928u,0x1550u,0x5ee0u,0x1fb90u,0x1fdb8u,0x1520u,0x5ee8u,0x20020u,0x20248u,0x1530u,0x5ef0u,0x204b0u,0x206d8u,0x1500u,0x5ef8u,0x20940u,0x20b68u,0x1510u,0x5f40u,0x20dd0u,0x20ff8u,0x16e0u,0x5f48u,0x21260u,0x21488u,0x16f0u,0x5f50u,0x216f0u,0x21918u,0x16c0u,0x5f58u,0x21b80u,0x21da8u,0x16d0u,0x5fc0u,0x22010u,0x22238u,0x16a0u,0x5fc8u,0x224a0u,0x226c8u,0x16b0u,0x5fd0u,0x22930u,0x22b58u,0x1680u,0x5fd8u,0x22dc0u,0x22fe8u,0x1690u,0x0u,0x40u,0x9000u,0xa800u,0x80u,0xc0u,0x9010u,0xa810u,0x110u,0x150u,0x9020u,0xa820u,0x190u,0x1d0u,0x9030u,0xa830u,0x220u,0x260u,0x9040u,0xa840u,0x2a0u,0x2e0u,0x9050u,0xa850u,0x330u,0x370u,0x9060u,0xa860u,0x3b0u,0x3f0u,0x9070u,0xa870u,0x440u,0x400u,0x9080u,0xa880u,0x4c0u,0x480u,0x9090u,0xa890u,0x550u,0x510u,0x90a0u,0xa8a0u,0x5d0u,0x590u,0x90b0u,0xa8b0u,0x660u,0x620u,0x90c0u,0xa8c0u,0x6e0u,0x6a0u,0x90d0u,0xa8d0u,0x770u,0x730u,0x90e0u,0xa8e0u,0x7f0u,0x7b0u,0x90f0u,0xa8f0u,0x10u,0x50u,0x9300u,0xab00u,0x90u,0xd0u,0x9310u,0xab10u,0x100u,0x140u,0x9320u,0xab20u,0x180u,0x1c0u,0x9330u,0xab30u,0x230u,0x270u,0x9340u,0xab40u,0x2b0u,0x2f0u,0x9350u,0xab50u,0x320u,0x360u,0x9360u,0xab60u,0x3a0u,0x3e0u,0x9370u,0xab70u,0x450u,0x410u,0x9380u,0xab80u,0x4d0u,0x490u,0x9390u,0xab90u,0x540u,0x500u,0x93a0u,0xaba0u,0x5c0u,0x580u,0x93b0u,0xabb0u,0x670u,0x630u,0x93c0u,0xabc0u,0x6f0u,0x6b0u,0x93d0u,0xabd0u,0x760u,0x720u,0x93e0u,0xabe0u,0x7e0u,0x7a0u,0x93f0u,0xabf0u,0x20u,0x60u,0x9600u,0xae00u,0xa0u,0xe0u,0x9610u,0xae10u,0x130u,0x170u,0x9620u,0xae20u,0x1b0u,0x1f0u,0x9630u,0xae30u,0x200u,0x240u,0x9640u,0xae40u,0x280u,0x2c0u,0x9650u,0xae50u,0x310u,0x350u,0x9660u,0xae60u,0x390u,0x3d0u,0x9670u,0xae70u,0x460u,0x420u,0x9680u,0xae80u,0x4e0u,0x4a0u,0x9690u,0xae90u,0x570u,0x530u,0x96a0u,0xaea0u,0x5f0u,0x5b0u,0x96b0u,0xaeb0u,0x640u,0x600u,0x96c0u,0xaec0u,0x6c0u,0x680u,0x96d0u,0xaed0u,0x750u,0x710u,0x96e0u,0xaee0u,0x7d0u,0x790u,0x96f0u,0xaef0u,0x30u,0x70u,0x9900u,0xb100u,0xb0u,0xf0u,0x9910u,0xb110u,0x120u,0x160u,0x9920u,0xb120u,0x1a0u,0x1e0u,0x9930u,0xb130u,0x210u,0x250u,0x9940u,0xb140u,0x290u,0x2d0u,0x9950u,0xb150u,0x300u,0x340u,0x9960u,0xb160u,0x380u,0x3c0u,0x9970u,0xb170u,0x470u,0x430u,0x9980u,0xb180u,0x4f0u,0x4b0u,0x9990u,0xb190u,0x560u,0x520u,0x99a0u,0xb1a0u,0x5e0u,0x5a0u,0x99b0u,0xb1b0u,0x650u,0x610u,0x99c0u,0xb1c0u,0x6d0u,0x690u,0x99d0u,0xb1d0u,0x740u,0x700u,0x99e0u,0xb1e0u,0x7c0u,0x780u,0x99f0u,0xb1f0u,0x9c08u,0x12060u,0x120a0u,0x12400u,0x9c18u,0x121f0u,0x12230u,0x12410u,0x9c28u,0x12340u,0x12380u,0x12420u,0x9c38u,0x124d0u,0x12510u,0x12430u,0x9c48u,0x12620u,0x126e0u,0x12440u,0x9c58u,0x127b0u,0x12870u,0x12450u,0x9c68u,0x12900u,0x129c0u,0x12460u,0x9c78u,0x12a90u,0x12b50u,0x12470u,0x9c88u,0x12c60u,0x12ca0u,0x12480u,0x9c98u,0x12df0u,0x12e30u,0x12490u,0x9ca8u,0x12f40u,0x12f80u,0x124a0u,0x9cb8u,0x130d0u,0x13110u,0x124b0u,0x9cc8u,0x13220u,0x132e0u,0x124c0u,0x9cd8u,0x133b0u,0x13470u,0x124d0u,0x9ce8u,0x13500u,0x135c0u,0x124e0u,0x9cf8u,0x13690u,0x13750u,0x124f0u,0x9f08u,0x12070u,0x120a8u,0x12500u,0x9f18u,0x121e0u,0x12238u,0x12510u,0x9f28u,0x12350u,0x12388u,0x12520u,0x9f38u,0x124c0u,0x12518u,0x12530u,0x9f48u,0x12630u,0x126e8u,0x12540u,0x9f58u,0x127a0u,0x12878u,0x12550u,0x9f68u,0x12910u,0x129c8u,0x12560u,0x9f78u,0x12a80u,0x12b58u,0x12570u,0x9f88u,0x12c70u,0x12ca8u,0x12580u,0x9f98u,0x12de0u,0x12e38u,0x12590u,0x9fa8u,0x12f50u,0x12f88u,0x125a0u,0x9fb8u,0x130c0u,0x13118u,0x125b0u,0x9fc8u,0x13230u,0x132e8u,0x125c0u,0x9fd8u,0x133a0u,0x13478u,0x125d0u,0x9fe8u,0x13510u,0x135c8u,0x125e0u,0x9ff8u,0x13680u,0x13758u,0x125f0u,0xa208u,0x12080u,0x120b0u,0x12600u,0xa218u,0x12210u,0x12220u,0x12610u,0xa228u,0x123a0u,0x12390u,0x12620u,0xa238u,0x12530u,0x12500u,0x12630u,0xa248u,0x126c0u,0x126f0u,0x12640u,0xa258u,0x12850u,0x12860u,0x12650u,0xa268u,0x129e0u,0x129d0u,0x12660u,0xa278u,0x12b70u,0x12b40u,0x12670u,0xa288u,0x12c80u,0x12cb0u,0x12680u,0xa298u,0x12e10u,0x12e20u,0x12690u,0xa2a8u,0x12fa0u,0x12f90u,0x126a0u,0xa2b8u,0x13130u,0x13100u,0x126b0u,0xa2c8u,0x132c0u,0x132f0u,0x126c0u,0xa2d8u,0x13450u,0x13460u,0x126d0u,0xa2e8u,0x135e0u,0x135d0u,0x126e0u,0xa2f8u,0x13770u,0x13740u,0x126f0u,0xa508u,0x12090u,0x120b8u,0x12700u,0xa518u,0x12200u,0x12228u,0x12710u,0xa528u,0x123b0u,0x12398u,0x12720u,0xa538u,0x12520u,0x12508u,0x12730u,0xa548u,0x126d0u,0x126f8u,0x12740u,0xa558u,0x12840u,0x12868u,0x12750u,0xa568u,0x129f0u,0x129d8u,0x12760u,0xa578u,0x12b60u,0x12b48u,0x12770u,0xa588u,0x12c90u,0x12cb8u,0x12780u,0xa598u,0x12e00u,0x12e28u,0x12790u,0xa5a8u,0x12fb0u,0x12f98u,0x127a0u,0xa5b8u,0x13120u,0x13108u,0x127b0u,0xa5c8u,0x132d0u,0x132f8u,0x127c0u,0xa5d8u,0x13440u,0x13468u,0x127d0u,0xa5e8u,0x135f0u,0x135d8u,0x127e0u,0xa5f8u,0x13760u,0x13748u,0x127f0u,0x5000u,0x1c300u,0x1e8c0u,0x1eb60u,0x5080u,0x1c310u,0x100000u,0x1eb62u,0x5110u,0x1c320u,0x100000u,0x1eb64u,0x5190u,0x1c330u,0x100000u,0x1eb66u,0x5220u,0x1c340u,0x1e8c0u,0x1eb68u,0x52a0u,0x1c350u,0x100000u,0x1eb6au,0x5330u,0x1c360u,0x100000u,0x1eb6cu,0x53b0u,0x1c370u,0x100000u,0x1eb6eu,0x5440u,0x1c380u,0x1e8c0u,0x1eb70u,0x54c0u,0x1c390u,0x100000u,0x1eb72u,0x5550u,0x1c3a0u,0x100000u,0x1eb74u,0x55d0u,0x1c3b0u,0x100000u,0x1eb76u,0x5660u,0x1c3c0u,0x1e8c0u,0x1eb78u,0x56e0u,0x1c3d0u,0x100000u,0x1eb7au,0x5770u,0x1c3e0u,0x100000u,0x1eb7cu,0x57f0u,0x1c3f0u,0x100000u,0x1eb7eu,0x5010u,0x1c400u,0x1e8f0u,0x1eb60u,0x5090u,0x1c410u,0x100000u,0x1eb62u,0x5100u,0x1c420u,0x100000u,0x1eb64u,0x5180u,0x1c430u,0x100000u,0x1eb66u,0x5230u,0x1c440u,0x1e8f0u,0x1eb68u,0x52b0u,0x1c450u,0x100000u,0x1eb6au,0x5320u,0x1c460u,0x100000u,0x1eb6cu,0x53a0u,0x1c470u,0x100000u,0x1eb6eu,0x5450u,0x1c480u,0x1e8f0u,0x1eb70u,0x54d0u,0x1c490u,0x100000u,0x1eb72u,0x5540u,0x1c4a0u,0x100000u,0x1eb74u,0x55c0u,0x1c4b0u,0x100000u,0x1eb76u,0x5670u,0x1c4c0u,0x1e8f0u,0x1eb78u,0x56f0u,0x1c4d0u,0x100000u,0x1eb7au,0x5760u,0x1c4e0u,0x100000u,0x1eb7cu,0x57e0u,0x1c4f0u,0x100000u,0x1eb7eu,0x5020u,0x1c500u,0x1e920u,0x1eb60u,0x50a0u,0x1c510u,0x100000u,0x1eb62u,0x5130u,0x1c520u,0x100000u,0x1eb64u,0x51b0u,0x1c530u,0x100000u,0x1eb66u,0x5200u,0x1c540u,0x1e920u,0x1eb68u,0x5280u,0x1c550u,0x100000u,0x1eb6au,0x5310u,0x1c560u,0x100000u,0x1eb6cu,0x5390u,0x1c570u,0x100000u,0x1eb6eu,0x5460u,0x1c580u,0x1e920u,0x1eb70u,0x54e0u,0x1c590u,0x100000u,0x1eb72u,0x5570u,0x1c5a0u,0x100000u,0x1eb74u,0x55f0u,0x1c5b0u,0x100000u,0x1eb76u,0x5640u,0x1c5c0u,0x1e920u,0x1eb78u,0x56c0u,0x1c5d0u,0x100000u,0x1eb7au,0x5750u,0x1c5e0u,0x100000u,0x1eb7cu,0x57d0u,0x1c5f0u,0x100000u,0x1eb7eu,0x5030u,0x1c600u,0x1e950u,0x1eb60u,0x50b0u,0x1c610u,0x100000u,0x1eb62u,0x5120u,0x1c620u,0x100000u,0x1eb64u,0x51a0u,0x1c630u,0x100000u,0x1eb66u,0x5210u,0x1c640u,0x1e950u,0x1eb68u,0x5290u,0x1c650u,0x100000u,0x1eb6au,0x5300u,0x1c660u,0x100000u,0x1eb6cu,0x5380u,0x1c670u,0x100000u,0x1eb6eu,0x5470u,0x1c680u,0x1e950u,0x1eb70u,0x54f0u,0x1c690u,0x100000u,0x1eb72u,0x5560u,0x1c6a0u,0x100000u,0x1eb74u,0x55e0u,0x1c6b0u,0x100000u,0x1eb76u,0x5650u,0x1c6c0u,0x1e950u,0x1eb78u,0x56d0u,0x1c6d0u,0x100000u,0x1eb7au,0x5740u,0x1c6e0u,0x100000u,0x1eb7cu,0x57c0u,0x1c6f0u,0x100000u,0x1eb7eu,0x25d00u,0x1d380u,0x1d380u,0xf0140u,0x25d02u,0x1d382u,0x100000u,0xf0150u,0x25d04u,0x1d384u,0x100000u,0xf0160u,0x25d06u,0x1d386u,0x100000u,0xf0170u,0x25f00u,0x1d390u,0x1d380u,0xf0100u,0x25f02u,0x1d392u,0x100000u,0xf0110u,0x25f04u,0x1d394u,0x100000u,0xf0120u,0x25f06u,0x1d396u,0x100000u,0xf0130u,0x26100u,0x1d3a0u,0x1d380u,0xf02c0u,0x26102u,0x1d3a2u,0x100000u,0xf02d0u,0x26104u,0x1d3a4u,0x100000u,0xf02e0u,0x26106u,0x1d3a6u,0x100000u,0xf02f0u,0x26300u,0x1d3b0u,0x1d380u,0xf0280u,0x26302u,0x1d3b2u,0x100000u,0xf0290u,0x26304u,0x1d3b4u,0x100000u,0xf02a0u,0x26306u,0x1d3b6u,0x100000u,0xf02b0u,0x25d08u,0x1d388u,0x1d390u,0x11f850u,0x25d0au,0x1d38au,0x100000u,0x11f840u,0x25d0cu,0x1d38cu,0x100000u,0x11f870u,0x25d0eu,0x1d38eu,0x100000u,0x11f860u,0x25f08u,0x1d398u,0x1d390u,0x11f810u,0x25f0au,0x1d39au,0x100000u,0x11f800u,0x25f0cu,0x1d39cu,0x100000u,0x11f830u,0x25f0eu,0x1d39eu,0x100000u,0x11f820u,0x26108u,0x1d3a8u,0x1d390u,0x11f9d0u,0x2610au,0x1d3aau,0x100000u,0x11f9c0u,0x2610cu,0x1d3acu,0x100000u,0x11f9f0u,0x2610eu,0x1d3aeu,0x100000u,0x11f9e0u,0x26308u,0x1d3b8u,0x1d390u,0x11f990u,0x2630au,0x1d3bau,0x100000u,0x11f980u,0x2630cu,0x1d3bcu,0x100000u,0x11f9b0u,0x2630eu,0x1d3beu,0x100000u,0x11f9a0u,0x25508u,0x1d3c0u,0x1d3a0u,0x11fb60u,0x2550au,0x1d3c2u,0x100000u,0x11fb70u,0x2550cu,0x1d3c4u,0x100000u,0x11fb40u,0x2550eu,0x1d3c6u,0x100000u,0x11fb50u,0x25708u,0x1d3d0u,0x1d3a0u,0x11fb20u,0x2570au,0x1d3d2u,0x100000u,0x11fb30u,0x2570cu,0x1d3d4u,0x100000u,0x11fb00u,0x2570eu,0x1d3d6u,0x100000u,0x11fb10u,0x25908u,0x1d3e0u,0x1d3a0u,0x11fce0u,0x2590au,0x1d3e2u,0x100000u,0x11fcf0u,0x2590cu,0x1d3e4u,0x100000u,0x11fcc0u,0x2590eu,0x1d3e6u,0x100000u,0x11fcd0u,0x25b08u,0x1d3f0u,0x1d3a0u,0x11fca0u,0x25b0au,0x1d3f2u,0x100000u,0x11fcb0u,0x25b0cu,0x1d3f4u,0x100000u,0x11fc80u,0x25b0eu,0x1d3f6u,0x100000u,0x11fc90u,0x100000u,0x1d3c8u,0x1d3b0u,0x11fe70u,0x100000u,0x1d3cau,0x100000u,0x11fe60u,0x100000u,0x1d3ccu,0x100000u,0x11fe50u,0x100000u,0x1d3ceu,0x100000u,0x11fe40u,0x100000u,0x1d3d8u,0x1d3b0u,0x11fe30u,0x100000u,0x1d3dau,0x100000u,0x11fe20u,0x100000u,0x1d3dcu,0x100000u,0x11fe10u,0x100000u,0x1d3deu,0x100000u,0x11fe00u,0x100000u,0x1d3e8u,0x1d3b0u,0x11fff0u,0x100000u,0x1d3eau,0x100000u,0x11ffe0u,0x100000u,0x1d3ecu,0x100000u,0x11ffd0u,0x100000u,0x1d3eeu,0x100000u,0x11ffc0u,0x100000u,0x1d3f8u,0x1d3b0u,0x11ffb0u,0x100000u,0x1d3fau,0x100000u,0x11ffa0u,0x100000u,0x1d3fcu,0x100000u,0x11ff90u,0x100000u,0x1d3feu,0x100000u,0x11ff80u,0x420u,0x30850u,0x90000u,0xc0420u,0x430u,0x30840u,0x90010u,0xc0430u,0x400u,0x30870u,0x90020u,0xc0400u,0x410u,0x30860u,0x90030u,0xc0410u,0x460u,0x30810u,0x90040u,0xc0460u,0x470u,0x30800u,0x90050u,0xc0470u,0x440u,0x30830u,0x90060u,0xc0440u,0x450u,0x30820u,0x90070u,0xc0450u,0x4b0u,0x308d0u,0x90080u,0xef8b0u,0x4a0u,0x308c0u,0x90090u,0xef8a0u,0x490u,0x308f0u,0x900a0u,0xef890u,0x480u,0x308e0u,0x900b0u,0xef880u,0x4f0u,0x30890u,0x900c0u,0xef8f0u,0x4e0u,0x30880u,0x900d0u,0xef8e0u,0x4d0u,0x308b0u,0x900e0u,0xef8d0u,0x4c0u,0x308a0u,0x900f0u,0xef8c0u,0x530u,0x5fd60u,0x90100u,0xef930u,0x520u,0x5fd70u,0x90110u,0xef920u,0x510u,0x5fd40u,0x90120u,0xef910u,0x500u,0x5fd50u,0x90130u,0xef900u,0x570u,0x5fd20u,0x90140u,0xef970u,0x560u,0x5fd30u,0x90150u,0xef960u,0x550u,0x5fd00u,0x90160u,0xef950u,0x540u,0x5fd10u,0x90170u,0xef940u,0x5b0u,0x5fde0u,0x90190u,0xef9b0u,0x5a0u,0x5fdf0u,0x90180u,0xef9a0u,0x590u,0x5fdc0u,0x901b0u,0xef990u,0x580u,0x5fdd0u,0x901a0u,0xef980u,0x5f0u,0x5fda0u,0x901d0u,0xef9f0u,0x5e0u,0x5fdb0u,0x901c0u,0xef9e0u,0x5d0u,0x5fd80u,0x901f0u,0xef9d0u,0x5c0u,0x5fd90u,0x901e0u,0xef9c0u,0x640u,0x5fe60u,0x90210u,0xefa40u,0x650u,0x5fe70u,0x90200u,0xefa50u,0x660u,0x5fe40u,0x90230u,0xefa60u,0x670u,0x5fe50u,0x90220u,0xefa70u,0x600u,0x5fe20u,0x90250u,0xefa00u,0x610u,0x5fe30u,0x90240u,0xefa10u,0x620u,0x5fe00u,0x90270u,0xefa20u,0x630u,0x5fe10u,0x90260u,0xefa30u,0x6c0u,0x5fef0u,0x90290u,0xefac0u,0x6d0u,0x5fee0u,0x90280u,0xefad0u,0x6e0u,0x5fed0u,0x902b0u,0xefae0u,0x6f0u,0x5fec0u,0x902a0u,0xefaf0u,0x680u,0x5feb0u,0x902d0u,0xefa80u,0x690u,0x5fea0u,0x902c0u,0xefa90u,0x6a0u,0x5fe90u,0x902f0u,0xefaa0u,0x6b0u,0x5fe80u,0x902e0u,0xefab0u,0x740u,0x5ff70u,0x90320u,0xefb40u,0x750u,0x5ff60u,0x90330u,0xefb50u,0x760u,0x5ff50u,0x90300u,0xefb60u,0x770u,0x5ff40u,0x90310u,0xefb70u,0x700u,0x5ff30u,0x90360u,0xefb00u,0x710u,0x5ff20u,0x90370u,0xefb10u,0x720u,0x5ff10u,0x90340u,0xefb20u,0x730u,0x5ff00u,0x90350u,0xefb30u,0x7d0u,0x5fff0u,0x903a0u,0xefbd0u,0x7c0u,0x5ffe0u,0x903b0u,0xefbc0u,0x7f0u,0x5ffd0u,0x90380u,0xefbf0u,0x7e0u,0x5ffc0u,0x90390u,0xefbe0u,0x790u,0x5ffb0u,0x903e0u,0xefb90u,0x780u,0x5ffa0u,0x903f0u,0xefb80u,0x7b0u,0x5ff90u,0x903c0u,0xefbb0u,0x7a0u,0x5ff80u,0x903d0u,0xefba0u,0x0u,0x11fc50u,0x0u,0x7230cu,0x0u,0x11fc40u,0x0u,0x7230cu,0x0u,0x11fc70u,0x0u,0x7230cu,0x0u,0x11fc60u,0x0u,0x7230cu,0x0u,0x11fc10u,0x0u,0x7230cu,0x0u,0x11fc00u,0x0u,0x7230cu,0x0u,0x11fc30u,0x0u,0x7230cu,0x0u,0x11fc20u,0x0u,0x7230cu,0x0u,0x11fcd0u,0x0u,0x5232cu,0x0u,0x11fcc0u,0x0u,0x5232cu,0x0u,0x11fcf0u,0x0u,0x5232cu,0x0u,0x11fce0u,0x0u,0x5232cu,0x0u,0x11fc90u,0x0u,0x5232cu,0x0u,0x11fc80u,0x0u,0x5232cu,0x0u,0x11fcb0u,0x0u,0x5232cu,0x0u,0x11fca0u,0x0u,0x5232cu,0x0u,0x11fd60u,0x0u,0xd0124u,0x0u,0x11fd70u,0x0u,0xd0124u,0x0u,0x11fd40u,0x0u,0xd0124u,0x0u,0x11fd50u,0x0u,0xd0124u,0x0u,0x11fd20u,0x0u,0xd0124u,0x0u,0x11fd30u,0x0u,0xd0124u,0x0u,0x11fd00u,0x0u,0xd0124u,0x0u,0x11fd10u,0x0u,0xd0124u,0x0u,0x11fde0u,0x0u,0xd81a6u,0x0u,0x11fdf0u,0x0u,0xd81a6u,0x0u,0x11fdc0u,0x0u,0xd81a6u,0x0u,0x11fdd0u,0x0u,0xd81a6u,0x0u,0x11fda0u,0x0u,0xd81a6u,0x0u,0x11fdb0u,0x0u,0xd81a6u,0x0u,0x11fd80u,0x0u,0xd81a6u,0x0u,0x11fd90u,0x0u,0xd81a6u,0x0u,0x11fe60u,0x0u,0xd8996u,0x0u,0x11fe70u,0x0u,0xd8996u,0x0u,0x11fe40u,0x0u,0xd8996u,0x0u,0x11fe50u,0x0u,0xd8996u,0x0u,0x11fe20u,0x0u,0xd8996u,0x0u,0x11fe30u,0x0u,0xd8996u,0x0u,0x11fe00u,0x0u,0xd8996u,0x0u,0x11fe10u,0x0u,0xd8996u,0x0u,0x11fef0u,0x0u,0xd8996u,0x0u,0x11fee0u,0x0u,0xd8996u,0x0u,0x11fed0u,0x0u,0xd8996u,0x0u,0x11fec0u,0x0u,0xd8996u,0x0u,0x11feb0u,0x0u,0xd8996u,0x0u,0x11fea0u,0x0u,0xd8996u,0x0u,0x11fe90u,0x0u,0xd8996u,0x0u,0x11fe80u,0x0u,0xd8996u,0x0u,0x11ff70u,0x0u,0xd8b1cu,0x0u,0x11ff60u,0x0u,0xd8b1cu,0x0u,0x11ff50u,0x0u,0xd8b1cu,0x0u,0x11ff40u,0x0u,0xd8b1cu,0x0u,0x11ff30u,0x0u,0xd8b1cu,0x0u,0x11ff20u,0x0u,0xd8b1cu,0x0u,0x11ff10u,0x0u,0xd8b1cu,0x0u,0x11ff00u,0x0u,0xd8b1cu,0x0u,0x11fff0u,0x0u,0xd8b1cu,0x0u,0x11ffe0u,0x0u,0xd8b1cu,0x0u,0x11ffd0u,0x0u,0xd8b1cu,0x0u,0x11ffc0u,0x0u,0xd8b1cu,0x0u,0x11ffb0u,0x0u,0xd8b1cu,0x0u,0x11ffa0u,0x0u,0xd8b1cu,0x0u,0x11ff90u,0x0u,0xd8b1cu,0x0u,0x11ff80u,0x0u,0xd8b1cu,0x5840u,0x5860u,0x6800u,0x5840u,0x5848u,0x5868u,0x6808u,0x5850u,0x5850u,0x5870u,0x6810u,0x5860u,0x5858u,0x5878u,0x6818u,0x5870u,0x58c0u,0x58e0u,0x6880u,0x58c0u,0x58c8u,0x58e8u,0x6888u,0x58d0u,0x58d0u,0x58f0u,0x6890u,0x58e0u,0x58d8u,0x58f8u,0x6898u,0x58f0u,0x5960u,0x5940u,0x6920u,0x5960u,0x5968u,0x5948u,0x6928u,0x5970u,0x5970u,0x5950u,0x6930u,0x5940u,0x5978u,0x5958u,0x6938u,0x5950u,0x59e0u,0x59c0u,0x69a0u,0x59e0u,0x59e8u,0x59c8u,0x69a8u,0x59f0u,0x59f0u,0x59d0u,0x69b0u,0x59c0u,0x59f8u,0x59d8u,0x69b8u,0x59d0u,0x5c00u,0x5c20u,0x6c40u,0x5a40u,0x5c08u,0x5c28u,0x6c48u,0x5a50u,0x5c10u,0x5c30u,0x6c50u,0x5a60u,0x5c18u,0x5c38u,0x6c58u,0x5a70u,0x5c80u,0x5ca0u,0x6cc0u,0x5ac0u,0x5c88u,0x5ca8u,0x6cc8u,0x5ad0u,0x5c90u,0x5cb0u,0x6cd0u,0x5ae0u,0x5c98u,0x5cb8u,0x6cd8u,0x5af0u,0x5d20u,0x5d00u,0x6d60u,0x5b60u,0x5d28u,0x5d08u,0x6d68u,0x5b70u,0x5d30u,0x5d10u,0x6d70u,0x5b40u,0x5d38u,0x5d18u,0x6d78u,0x5b50u,0x5da0u,0x5d80u,0x6de0u,0x5be0u,0x5da8u,0x5d88u,0x6de8u,0x5bf0u,0x5db0u,0x5d90u,0x6df0u,0x5bc0u,0x5db8u,0x5d98u,0x6df8u,0x5bd0u,0x6040u,0x6060u,0x7000u,0x5c00u,0x6048u,0x6068u,0x7008u,0x5c10u,0x6050u,0x6070u,0x7010u,0x5c20u,0x6058u,0x6078u,0x7018u,0x5c30u,0x60c0u,0x60e0u,0x7080u,0x5c80u,0x60c8u,0x60e8u,0x7088u,0x5c90u,0x60d0u,0x60f0u,0x7090u,0x5ca0u,0x60d8u,0x60f8u,0x7098u,0x5cb0u,0x6160u,0x6140u,0x7120u,0x5d20u,0x6168u,0x6148u,0x7128u,0x5d30u,0x6170u,0x6150u,0x7130u,0x5d00u,0x6178u,0x6158u,0x7138u,0x5d10u,0x61e0u,0x61c0u,0x71a0u,0x5da0u,0x61e8u,0x61c8u,0x71a8u,0x5db0u,0x61f0u,0x61d0u,0x71b0u,0x5d80u,0x61f8u,0x61d8u,0x71b8u,0x5d90u,0x6400u,0x6420u,0x7440u,0x5e00u,0x6408u,0x6428u,0x7448u,0x5e10u,0x6410u,0x6430u,0x7450u,0x5e20u,0x6418u,0x6438u,0x7458u,0x5e30u,0x6480u,0x64a0u,0x74c0u,0x5e80u,0x6488u,0x64a8u,0x74c8u,0x5e90u,0x6490u,0x64b0u,0x74d0u,0x5ea0u,0x6498u,0x64b8u,0x74d8u,0x5eb0u,0x6520u,0x6500u,0x7560u,0x5f20u,0x6528u,0x6508u,0x7568u,0x5f30u,0x6530u,0x6510u,0x7570u,0x5f00u,0x6538u,0x6518u,0x7578u,0x5f10u,0x65a0u,0x6580u,0x75e0u,0x5fa0u,0x65a8u,0x6588u,0x75e8u,0x5fb0u,0x65b0u,0x6590u,0x75f0u,0x5f80u,0x65b8u,0x6598u,0x75f8u,0x5f90u,0x6800u,0x1e980u,0x1ebc0u,0x1800u,0x6808u,0x1ee10u,0x1f050u,0x1810u,0x6810u,0x1f2a0u,0x1f4e0u,0x1820u,0x6818u,0x1f730u,0x1f970u,0x1830u,0x6880u,0x1fbc0u,0x1fe00u,0x1840u,0x6888u,0x20050u,0x20290u,0x1850u,0x6890u,0x204e0u,0x20720u,0x1860u,0x6898u,0x20970u,0x20bb0u,0x1870u,0x6920u,0x20e00u,0x21040u,0x1980u,0x6928u,0x21290u,0x214d0u,0x1990u,0x6930u,0x21720u,0x21960u,0x19a0u,0x6938u,0x21bb0u,0x21df0u,0x19b0u,0x69a0u,0x22040u,0x22280u,0x19c0u,0x69a8u,0x224d0u,0x22710u,0x19d0u,0x69b0u,0x22960u,0x22ba0u,0x19e0u,0x69b8u,0x22df0u,0x23030u,0x19f0u,0x6a00u,0x1e9b0u,0x1ebc8u,0x1b20u,0x6a08u,0x1ee40u,0x1f058u,0x1b30u,0x6a10u,0x1f2d0u,0x1f4e8u,0x1b00u,0x6a18u,0x1f760u,0x1f978u,0x1b10u,0x6a80u,0x1fbf0u,0x1fe08u,0x1b60u,0x6a88u,0x20080u,0x20298u,0x1b70u,0x6a90u,0x20510u,0x20728u,0x1b40u,0x6a98u,0x209a0u,0x20bb8u,0x1b50u,0x6b20u,0x20e30u,0x21048u,0x1ca0u,0x6b28u,0x212c0u,0x214d8u,0x1cb0u,0x6b30u,0x21750u,0x21968u,0x1c80u,0x6b38u,0x21be0u,0x21df8u,0x1c90u,0x6ba0u,0x22070u,0x22288u,0x1ce0u,0x6ba8u,0x22500u,0x22718u,0x1cf0u,0x6bb0u,0x22990u,0x22ba8u,0x1cc0u,0x6bb8u,0x22e20u,0x23038u,0x1cd0u,0x6c40u,0x1e9e0u,0x1ebd0u,0x1e00u,0x6c48u,0x1ee70u,0x1f060u,0x1e10u,0x6c50u,0x1f300u,0x1f4f0u,0x1e20u,0x6c58u,0x1f790u,0x1f980u,0x1e30u,0x6cc0u,0x1fc20u,0x1fe10u,0x1e40u,0x6cc8u,0x200b0u,0x202a0u,0x1e50u,0x6cd0u,0x20540u,0x20730u,0x1e60u,0x6cd8u,0x209d0u,0x20bc0u,0x1e70u,0x6d60u,0x20e60u,0x21050u,0x1f80u,0x6d68u,0x212f0u,0x214e0u,0x1f90u,0x6d70u,0x21780u,0x21970u,0x1fa0u,0x6d78u,0x21c10u,0x21e00u,0x1fb0u,0x6de0u,0x220a0u,0x22290u,0x1fc0u,0x6de8u,0x22530u,0x22720u,0x1fd0u,0x6df0u,0x229c0u,0x22bb0u,0x1fe0u,0x6df8u,0x22e50u,0x23040u,0x1ff0u,0x6e40u,0x1ea10u,0x1ebd8u,0x2120u,0x6e48u,0x1eea0u,0x1f068u,0x2130u,0x6e50u,0x1f330u,0x1f4f8u,0x2100u,0x6e58u,0x1f7c0u,0x1f988u,0x2110u,0x6ec0u,0x1fc50u,0x1fe18u,0x2160u,0x6ec8u,0x200e0u,0x202a8u,0x2170u,0x6ed0u,0x20570u,0x20738u,0x2140u,0x6ed8u,0x20a00u,0x20bc8u,0x2150u,0x6f60u,0x20e90u,0x21058u,0x22a0u,0x6f68u,0x21320u,0x214e8u,0x22b0u,0x6f70u,0x217b0u,0x21978u,0x2280u,0x6f78u,0x21c40u,0x21e08u,0x2290u,0x6fe0u,0x220d0u,0x22298u,0x22e0u,0x6fe8u,0x22560u,0x22728u,0x22f0u,0x6ff0u,0x229f0u,0x22bb8u,0x22c0u,0x6ff8u,0x22e80u,0x23048u,0x22d0u,0x0u,0x40u,0x9000u,0xb400u,0x80u,0xc0u,0x9010u,0xb410u,0x110u,0x150u,0x9020u,0xb420u,0x190u,0x1d0u,0x9030u,0xb430u,0x220u,0x260u,0x9040u,0xb440u,0x2a0u,0x2e0u,0x9050u,0xb450u,0x330u,0x370u,0x9060u,0xb460u,0x3b0u,0x3f0u,0x9070u,0xb470u,0x440u,0x400u,0x9080u,0xb480u,0x4c0u,0x480u,0x9090u,0xb490u,0x550u,0x510u,0x90a0u,0xb4a0u,0x5d0u,0x590u,0x90b0u,0xb4b0u,0x660u,0x620u,0x90c0u,0xb4c0u,0x6e0u,0x6a0u,0x90d0u,0xb4d0u,0x770u,0x730u,0x90e0u,0xb4e0u,0x7f0u,0x7b0u,0x90f0u,0xb4f0u,0x10u,0x50u,0x9300u,0xb700u,0x90u,0xd0u,0x9310u,0xb710u,0x100u,0x140u,0x9320u,0xb720u,0x180u,0x1c0u,0x9330u,0xb730u,0x230u,0x270u,0x9340u,0xb740u,0x2b0u,0x2f0u,0x9350u,0xb750u,0x320u,0x360u,0x9360u,0xb760u,0x3a0u,0x3e0u,0x9370u,0xb770u,0x450u,0x410u,0x9380u,0xb780u,0x4d0u,0x490u,0x9390u,0xb790u,0x540u,0x500u,0x93a0u,0xb7a0u,0x5c0u,0x580u,0x93b0u,0xb7b0u,0x670u,0x630u,0x93c0u,0xb7c0u,0x6f0u,0x6b0u,0x93d0u,0xb7d0u,0x760u,0x720u,0x93e0u,0xb7e0u,0x7e0u,0x7a0u,0x93f0u,0xb7f0u,0x20u,0x60u,0x9600u,0xba00u,0xa0u,0xe0u,0x9610u,0xba10u,0x130u,0x170u,0x9620u,0xba20u,0x1b0u,0x1f0u,0x9630u,0xba30u,0x200u,0x240u,0x9640u,0xba40u,0x280u,0x2c0u,0x9650u,0xba50u,0x310u,0x350u,0x9660u,0xba60u,0x390u,0x3d0u,0x9670u,0xba70u,0x460u,0x420u,0x9680u,0xba80u,0x4e0u,0x4a0u,0x9690u,0xba90u,0x570u,0x530u,0x96a0u,0xbaa0u,0x5f0u,0x5b0u,0x96b0u,0xbab0u,0x640u,0x600u,0x96c0u,0xbac0u,0x6c0u,0x680u,0x96d0u,0xbad0u,0x750u,0x710u,0x96e0u,0xbae0u,0x7d0u,0x790u,0x96f0u,0xbaf0u,0x30u,0x70u,0x9900u,0xbd00u,0xb0u,0xf0u,0x9910u,0xbd10u,0x120u,0x160u,0x9920u,0xbd20u,0x1a0u,0x1e0u,0x9930u,0xbd30u,0x210u,0x250u,0x9940u,0xbd40u,0x290u,0x2d0u,0x9950u,0xbd50u,0x300u,0x340u,0x9960u,0xbd60u,0x380u,0x3c0u,0x9970u,0xbd70u,0x470u,0x430u,0x9980u,0xbd80u,0x4f0u,0x4b0u,0x9990u,0xbd90u,0x560u,0x520u,0x99a0u,0xbda0u,0x5e0u,0x5a0u,0x99b0u,0xbdb0u,0x650u,0x610u,0x99c0u,0xbdc0u,0x6d0u,0x690u,0x99d0u,0xbdd0u,0x740u,0x700u,0x99e0u,0xbde0u,0x7c0u,0x780u,0x99f0u,0xbdf0u,0xc000u,0x120c0u,0x12100u,0x12800u,0xc010u,0x12250u,0x12290u,0x12810u,0xc020u,0x123e0u,0x12420u,0x12820u,0xc030u,0x12570u,0x125b0u,0x12830u,0xc040u,0x12680u,0x12740u,0x12840u,0xc050u,0x12810u,0x128d0u,0x12850u,0xc060u,0x129a0u,0x12a60u,0x12860u,0xc070u,0x12b30u,0x12bf0u,0x12870u,0xc080u,0x12cc0u,0x12d00u,0x12880u,0xc090u,0x12e50u,0x12e90u,0x12890u,0xc0a0u,0x12fe0u,0x13020u,0x128a0u,0xc0b0u,0x13170u,0x131b0u,0x128b0u,0xc0c0u,0x13280u,0x13340u,0x128c0u,0xc0d0u,0x13410u,0x134d0u,0x128d0u,0xc0e0u,0x135a0u,0x13660u,0x128e0u,0xc0f0u,0x13730u,0x137f0u,0x128f0u,0xc300u,0x120d0u,0x12108u,0x12900u,0xc310u,0x12240u,0x12298u,0x12910u,0xc320u,0x123f0u,0x12428u,0x12920u,0xc330u,0x12560u,0x125b8u,0x12930u,0xc340u,0x12690u,0x12748u,0x12940u,0xc350u,0x12800u,0x128d8u,0x12950u,0xc360u,0x129b0u,0x12a68u,0x12960u,0xc370u,0x12b20u,0x12bf8u,0x12970u,0xc380u,0x12cd0u,0x12d08u,0x12980u,0xc390u,0x12e40u,0x12e98u,0x12990u,0xc3a0u,0x12ff0u,0x13028u,0x129a0u,0xc3b0u,0x13160u,0x131b8u,0x129b0u,0xc3c0u,0x13290u,0x13348u,0x129c0u,0xc3d0u,0x13400u,0x134d8u,0x129d0u,0xc3e0u,0x135b0u,0x13668u,0x129e0u,0xc3f0u,0x13720u,0x137f8u,0x129f0u,0xc600u,0x120e0u,0x12110u,0x12a00u,0xc610u,0x12270u,0x12280u,0x12a10u,0xc620u,0x123c0u,0x12430u,0x12a20u,0xc630u,0x12550u,0x125a0u,0x12a30u,0xc640u,0x126a0u,0x12750u,0x12a40u,0xc650u,0x12830u,0x128c0u,0x12a50u,0xc660u,0x12980u,0x12a70u,0x12a60u,0xc670u,0x12b10u,0x12be0u,0x12a70u,0xc680u,0x12ce0u,0x12d10u,0x12a80u,0xc690u,0x12e70u,0x12e80u,0x12a90u,0xc6a0u,0x12fc0u,0x13030u,0x12aa0u,0xc6b0u,0x13150u,0x131a0u,0x12ab0u,0xc6c0u,0x132a0u,0x13350u,0x12ac0u,0xc6d0u,0x13430u,0x134c0u,0x12ad0u,0xc6e0u,0x13580u,0x13670u,0x12ae0u,0xc6f0u,0x13710u,0x137e0u,0x12af0u,0xc900u,0x120f0u,0x12118u,0x12b00u,0xc910u,0x12260u,0x12288u,0x12b10u,0xc920u,0x123d0u,0x12438u,0x12b20u,0xc930u,0x12540u,0x125a8u,0x12b30u,0xc940u,0x126b0u,0x12758u,0x12b40u,0xc950u,0x12820u,0x128c8u,0x12b50u,0xc960u,0x12990u,0x12a78u,0x12b60u,0xc970u,0x12b00u,0x12be8u,0x12b70u,0xc980u,0x12cf0u,0x12d18u,0x12b80u,0xc990u,0x12e60u,0x12e88u,0x12b90u,0xc9a0u,0x12fd0u,0x13038u,0x12ba0u,0xc9b0u,0x13140u,0x131a8u,0x12bb0u,0xc9c0u,0x132b0u,0x13358u,0x12bc0u,0xc9d0u,0x13420u,0x134c8u,0x12bd0u,0xc9e0u,0x13590u,0x13678u,0x12be0u,0xc9f0u,0x13700u,0x137e8u,0x12bf0u,0x7800u,0x1c300u,0x1e980u,0x1ebc0u,0x7880u,0x1c310u,0x100000u,0x1ebc2u,0x7910u,0x1c320u,0x100000u,0x1ebc4u,0x7990u,0x1c330u,0x100000u,0x1ebc6u,0x7a20u,0x1c340u,0x1e980u,0x1ebc8u,0x7aa0u,0x1c350u,0x100000u,0x1ebcau,0x7b30u,0x1c360u,0x100000u,0x1ebccu,0x7bb0u,0x1c370u,0x100000u,0x1ebceu,0x7c40u,0x1c380u,0x1e980u,0x1ebd0u,0x7cc0u,0x1c390u,0x100000u,0x1ebd2u,0x7d50u,0x1c3a0u,0x100000u,0x1ebd4u,0x7dd0u,0x1c3b0u,0x100000u,0x1ebd6u,0x7e60u,0x1c3c0u,0x1e980u,0x1ebd8u,0x7ee0u,0x1c3d0u,0x100000u,0x1ebdau,0x7f70u,0x1c3e0u,0x100000u,0x1ebdcu,0x7ff0u,0x1c3f0u,0x100000u,0x1ebdeu,0x7810u,0x1c400u,0x1e9b0u,0x1ebc0u,0x7890u,0x1c410u,0x100000u,0x1ebc2u,0x7900u,0x1c420u,0x100000u,0x1ebc4u,0x7980u,0x1c430u,0x100000u,0x1ebc6u,0x7a30u,0x1c440u,0x1e9b0u,0x1ebc8u,0x7ab0u,0x1c450u,0x100000u,0x1ebcau,0x7b20u,0x1c460u,0x100000u,0x1ebccu,0x7ba0u,0x1c470u,0x100000u,0x1ebceu,0x7c50u,0x1c480u,0x1e9b0u,0x1ebd0u,0x7cd0u,0x1c490u,0x100000u,0x1ebd2u,0x7d40u,0x1c4a0u,0x100000u,0x1ebd4u,0x7dc0u,0x1c4b0u,0x100000u,0x1ebd6u,0x7e70u,0x1c4c0u,0x1e9b0u,0x1ebd8u,0x7ef0u,0x1c4d0u,0x100000u,0x1ebdau,0x7f60u,0x1c4e0u,0x100000u,0x1ebdcu,0x7fe0u,0x1c4f0u,0x100000u,0x1ebdeu,0x7820u,0x1c500u,0x1e9e0u,0x1ebc0u,0x78a0u,0x1c510u,0x100000u,0x1ebc2u,0x7930u,0x1c520u,0x100000u,0x1ebc4u,0x79b0u,0x1c530u,0x100000u,0x1ebc6u,0x7a00u,0x1c540u,0x1e9e0u,0x1ebc8u,0x7a80u,0x1c550u,0x100000u,0x1ebcau,0x7b10u,0x1c560u,0x100000u,0x1ebccu,0x7b90u,0x1c570u,0x100000u,0x1ebceu,0x7c60u,0x1c580u,0x1e9e0u,0x1ebd0u,0x7ce0u,0x1c590u,0x100000u,0x1ebd2u,0x7d70u,0x1c5a0u,0x100000u,0x1ebd4u,0x7df0u,0x1c5b0u,0x100000u,0x1ebd6u,0x7e40u,0x1c5c0u,0x1e9e0u,0x1ebd8u,0x7ec0u,0x1c5d0u,0x100000u,0x1ebdau,0x7f50u,0x1c5e0u,0x100000u,0x1ebdcu,0x7fd0u,0x1c5f0u,0x100000u,0x1ebdeu,0x7830u,0x1c600u,0x1ea10u,0x1ebc0u,0x78b0u,0x1c610u,0x100000u,0x1ebc2u,0x7920u,0x1c620u,0x100000u,0x1ebc4u,0x79a0u,0x1c630u,0x100000u,0x1ebc6u,0x7a10u,0x1c640u,0x1ea10u,0x1ebc8u,0x7a90u,0x1c650u,0x100000u,0x1ebcau,0x7b00u,0x1c660u,0x100000u,0x1ebccu,0x7b80u,0x1c670u,0x100000u,0x1ebceu,0x7c70u,0x1c680u,0x1ea10u,0x1ebd0u,0x7cf0u,0x1c690u,0x100000u,0x1ebd2u,0x7d60u,0x1c6a0u,0x100000u,0x1ebd4u,0x7de0u,0x1c6b0u,0x100000u,0x1ebd6u,0x7e50u,0x1c6c0u,0x1ea10u,0x1ebd8u,0x7ed0u,0x1c6d0u,0x100000u,0x1ebdau,0x7f40u,0x1c6e0u,0x100000u,0x1ebdcu,0x7fc0u,0x1c6f0u,0x100000u,0x1ebdeu,0x26500u,0x1d400u,0x1d400u,0x80000000u,0x26502u,0x1d402u,0x100000u,0x80000000u,0x26504u,0x1d404u,0x100000u,0x80000000u,0x26506u,0x1d406u,0x100000u,0x80000000u,0x26700u,0x1d410u,0x1d400u,0x80000000u,0x26702u,0x1d412u,0x100000u,0x80000000u,0x26704u,0x1d414u,0x100000u,0x80000000u,0x26706u,0x1d416u,0x100000u,0x80000000u,0x26900u,0x1d420u,0x1d400u,0x80000000u,0x26902u,0x1d422u,0x100000u,0x80000000u,0x26904u,0x1d424u,0x100000u,0x80000000u,0x26906u,0x1d426u,0x100000u,0x80000000u,0x26b00u,0x1d430u,0x1d400u,0x80000000u,0x26b02u,0x1d432u,0x100000u,0x80000000u,0x26b04u,0x1d434u,0x100000u,0x80000000u,0x26b06u,0x1d436u,0x100000u,0x80000000u,0x26508u,0x1d408u,0x1d410u,0x80000000u,0x2650au,0x1d40au,0x100000u,0x80000000u,0x2650cu,0x1d40cu,0x100000u,0x80000000u,0x2650eu,0x1d40eu,0x100000u,0x80000000u,0x26708u,0x1d418u,0x1d410u,0x80000000u,0x2670au,0x1d41au,0x100000u,0x80000000u,0x2670cu,0x1d41cu,0x100000u,0x80000000u,0x2670eu,0x1d41eu,0x100000u,0x80000000u,0x26908u,0x1d428u,0x1d410u,0x80000000u,0x2690au,0x1d42au,0x100000u,0x80000000u,0x2690cu,0x1d42cu,0x100000u,0x80000000u,0x2690eu,0x1d42eu,0x100000u,0x80000000u,0x26b08u,0x1d438u,0x1d410u,0x80000000u,0x26b0au,0x1d43au,0x100000u,0x80000000u,0x26b0cu,0x1d43cu,0x100000u,0x80000000u,0x26b0eu,0x1d43eu,0x100000u,0x80000000u,0x26d00u,0x1d440u,0x1d420u,0x80000000u,0x26d02u,0x1d442u,0x100000u,0x80000000u,0x26d04u,0x1d444u,0x100000u,0x80000000u,0x26d06u,0x1d446u,0x100000u,0x80000000u,0x26f00u,0x1d450u,0x1d420u,0x80000000u,0x26f02u,0x1d452u,0x100000u,0x80000000u,0x26f04u,0x1d454u,0x100000u,0x80000000u,0x26f06u,0x1d456u,0x100000u,0x80000000u,0x27100u,0x1d460u,0x1d420u,0x80000000u,0x27102u,0x1d462u,0x100000u,0x80000000u,0x27104u,0x1d464u,0x100000u,0x80000000u,0x27106u,0x1d466u,0x100000u,0x80000000u,0x27300u,0x1d470u,0x1d420u,0x80000000u,0x27302u,0x1d472u,0x100000u,0x80000000u,0x27304u,0x1d474u,0x100000u,0x80000000u,0x27306u,0x1d476u,0x100000u,0x80000000u,0x100000u,0x1d448u,0x1d430u,0x80000000u,0x100000u,0x1d44au,0x100000u,0x80000000u,0x100000u,0x1d44cu,0x100000u,0x80000000u,0x100000u,0x1d44eu,0x100000u,0x80000000u,0x100000u,0x1d458u,0x1d430u,0x80000000u,0x100000u,0x1d45au,0x100000u,0x80000000u,0x100000u,0x1d45cu,0x100000u,0x80000000u,0x100000u,0x1d45eu,0x100000u,0x80000000u,0x100000u,0x1d468u,0x1d430u,0x80000000u,0x100000u,0x1d46au,0x100000u,0x80000000u,0x100000u,0x1d46cu,0x100000u,0x80000000u,0x100000u,0x1d46eu,0x100000u,0x80000000u,0x100000u,0x1d478u,0x1d430u,0x80000000u,0x100000u,0x1d47au,0x100000u,0x80000000u,0x100000u,0x1d47cu,0x100000u,0x80000000u,0x100000u,0x1d47eu,0x100000u,0x80000000u,0x850u,0x60000u,0x90420u,0xefc50u,0x840u,0x60010u,0x90430u,0xefc40u,0x870u,0x60020u,0x90400u,0xefc70u,0x860u,0x60030u,0x90410u,0xefc60u,0x810u,0x60040u,0x90460u,0xefc10u,0x800u,0x60050u,0x90470u,0xefc00u,0x830u,0x60060u,0x90440u,0xefc30u,0x820u,0x60070u,0x90450u,0xefc20u,0x8d0u,0x60080u,0x904b0u,0xefcd0u,0x8c0u,0x60090u,0x904a0u,0xefcc0u,0x8f0u,0x600a0u,0x90490u,0xefcf0u,0x8e0u,0x600b0u,0x90480u,0xefce0u,0x890u,0x600c0u,0x904f0u,0xefc90u,0x880u,0x600d0u,0x904e0u,0xefc80u,0x8b0u,0x600e0u,0x904d0u,0xefcb0u,0x8a0u,0x600f0u,0x904c0u,0xefca0u,0x960u,0x60100u,0x90530u,0xefd60u,0x970u,0x60110u,0x90520u,0xefd70u,0x940u,0x60120u,0x90510u,0xefd40u,0x950u,0x60130u,0x90500u,0xefd50u,0x920u,0x60140u,0x90570u,0xefd20u,0x930u,0x60150u,0x90560u,0xefd30u,0x900u,0x60160u,0x90550u,0xefd00u,0x910u,0x60170u,0x90540u,0xefd10u,0x9e0u,0x60190u,0x905b0u,0xefde0u,0x9f0u,0x60180u,0x905a0u,0xefdf0u,0x9c0u,0x601b0u,0x90590u,0xefdc0u,0x9d0u,0x601a0u,0x90580u,0xefdd0u,0x9a0u,0x601d0u,0x905f0u,0xefda0u,0x9b0u,0x601c0u,0x905e0u,0xefdb0u,0x980u,0x601f0u,0x905d0u,0xefd80u,0x990u,0x601e0u,0x905c0u,0xefd90u,0xa60u,0x60210u,0xbfa40u,0xefe60u,0xa70u,0x60200u,0xbfa50u,0xefe70u,0xa40u,0x60230u,0xbfa60u,0xefe40u,0xa50u,0x60220u,0xbfa70u,0xefe50u,0xa20u,0x60250u,0xbfa00u,0xefe20u,0xa30u,0x60240u,0xbfa10u,0xefe30u,0xa00u,0x60270u,0xbfa20u,0xefe00u,0xa10u,0x60260u,0xbfa30u,0xefe10u,0x2fef0u,0x60290u,0xbfac0u,0xefef0u,0x2fee0u,0x60280u,0xbfad0u,0xefee0u,0x2fed0u,0x602b0u,0xbfae0u,0xefed0u,0x2fec0u,0x602a0u,0xbfaf0u,0xefec0u,0x2feb0u,0x602d0u,0xbfa80u,0xefeb0u,0x2fea0u,0x602c0u,0xbfa90u,0xefea0u,0x2fe90u,0x602f0u,0xbfaa0u,0xefe90u,0x2fe80u,0x602e0u,0xbfab0u,0xefe80u,0x2ff70u,0x60320u,0xbfb40u,0xeff70u,0x2ff60u,0x60330u,0xbfb50u,0xeff60u,0x2ff50u,0x60300u,0xbfb60u,0xeff50u,0x2ff40u,0x60310u,0xbfb70u,0xeff40u,0x2ff30u,0x60360u,0xbfb00u,0xeff30u,0x2ff20u,0x60370u,0xbfb10u,0xeff20u,0x2ff10u,0x60340u,0xbfb20u,0xeff10u,0x2ff00u,0x60350u,0xbfb30u,0xeff00u,0x2fff0u,0x603a0u,0xbfbd0u,0xefff0u,0x2ffe0u,0x603b0u,0xbfbc0u,0xeffe0u,0x2ffd0u,0x60380u,0xbfbf0u,0xeffd0u,0x2ffc0u,0x60390u,0xbfbe0u,0xeffc0u,0x2ffb0u,0x603e0u,0xbfb90u,0xeffb0u,0x2ffa0u,0x603f0u,0xbfb80u,0xeffa0u,0x2ff90u,0x603c0u,0xbfbb0u,0xeff90u,0x2ff80u,0x603d0u,0xbfba0u,0xeff80u,0x0u,0x80000000u,0x0u,0x18830u,0x0u,0x80000000u,0x0u,0x18830u,0x0u,0x80000000u,0x0u,0x18830u,0x0u,0x80000000u,0x0u,0x18830u,0x0u,0x80000000u,0x0u,0x18830u,0x0u,0x80000000u,0x0u,0x18830u,0x0u,0x80000000u,0x0u,0x18830u,0x0u,0x80000000u,0x0u,0x18830u,0x0u,0x80000000u,0x0u,0x18830u,0x0u,0x80000000u,0x0u,0x18830u,0x0u,0x80000000u,0x0u,0x18830u,0x0u,0x80000000u,0x0u,0x18830u,0x0u,0x80000000u,0x0u,0x18830u,0x0u,0x80000000u,0x0u,0x18830u,0x0u,0x80000000u,0x0u,0x18830u,0x0u,0x80000000u,0x0u,0x18830u,0x0u,0x80000000u,0x0u,0x188b2u,0x0u,0x80000000u,0x0u,0x188b2u,0x0u,0x80000000u,0x0u,0x188b2u,0x0u,0x80000000u,0x0u,0x188b2u,0x0u,0x80000000u,0x0u,0x188b2u,0x0u,0x80000000u,0x0u,0x188b2u,0x0u,0x80000000u,0x0u,0x188b2u,0x0u,0x80000000u,0x0u,0x188b2u,0x0u,0x80000000u,0x0u,0x18892u,0x0u,0x80000000u,0x0u,0x18892u,0x0u,0x80000000u,0x0u,0x18892u,0x0u,0x80000000u,0x0u,0x18892u,0x0u,0x80000000u,0x0u,0x18892u,0x0u,0x80000000u,0x0u,0x18892u,0x0u,0x80000000u,0x0u,0x18892u,0x0u,0x80000000u,0x0u,0x18892u,0x0u,0x80000000u,0x0u,0x1489au,0x0u,0x80000000u,0x0u,0x1489au,0x0u,0x80000000u,0x0u,0x1489au,0x0u,0x80000000u,0x0u,0x1489au,0x0u,0x80000000u,0x0u,0x1489au,0x0u,0x80000000u,0x0u,0x1489au,0x0u,0x80000000u,0x0u,0x1489au,0x0u,0x80000000u,0x0u,0x1489au,0x0u,0x80000000u,0x0u,0x34018u,0x0u,0x80000000u,0x0u,0x34018u,0x0u,0x80000000u,0x0u,0x34018u,0x0u,0x80000000u,0x0u,0x34018u,0x0u,0x80000000u,0x0u,0x34018u,0x0u,0x80000000u,0x0u,0x34018u,0x0u,0x80000000u,0x0u,0x34018u,0x0u,0x80000000u,0x0u,0x34018u,0x0u,0x80000000u,0x0u,0x36018u,0x0u,0x80000000u,0x0u,0x36018u,0x0u,0x80000000u,0x0u,0x36018u,0x0u,0x80000000u,0x0u,0x36018u,0x0u,0x80000000u,0x0u,0x36018u,0x0u,0x80000000u,0x0u,0x36018u,0x0u,0x80000000u,0x0u,0x36018u,0x0u,0x80000000u,0x0u,0x36018u,0x0u,0x80000000u,0x0u,0x36030u,0x0u,0x80000000u,0x0u,0x36030u,0x0u,0x80000000u,0x0u,0x36030u,0x0u,0x80000000u,0x0u,0x36030u,0x0u,0x80000000u,0x0u,0x36030u,0x0u,0x80000000u,0x0u,0x36030u,0x0u,0x80000000u,0x0u,0x36030u,0x0u,0x80000000u,0x0u,0x36030u,0x6840u,0x6860u,0x6820u,0x6820u,0x6848u,0x6868u,0x6828u,0x6830u,0x6850u,0x6870u,0x6830u,0x6840u,0x6858u,0x6878u,0x6838u,0x6850u,0x68c0u,0x68e0u,0x68a0u,0x68a0u,0x68c8u,0x68e8u,0x68a8u,0x68b0u,0x68d0u,0x68f0u,0x68b0u,0x68c0u,0x68d8u,0x68f8u,0x68b8u,0x68d0u,0x6960u,0x6940u,0x6900u,0x6900u,0x6968u,0x6948u,0x6908u,0x6910u,0x6970u,0x6950u,0x6910u,0x6960u,0x6978u,0x6958u,0x6918u,0x6970u,0x69e0u,0x69c0u,0x6980u,0x6980u,0x69e8u,0x69c8u,0x6988u,0x6990u,0x69f0u,0x69d0u,0x6990u,0x69e0u,0x69f8u,0x69d8u,0x6998u,0x69f0u,0x6c00u,0x6c20u,0x6c60u,0x6a20u,0x6c08u,0x6c28u,0x6c68u,0x6a30u,0x6c10u,0x6c30u,0x6c70u,0x6a40u,0x6c18u,0x6c38u,0x6c78u,0x6a50u,0x6c80u,0x6ca0u,0x6ce0u,0x6aa0u,0x6c88u,0x6ca8u,0x6ce8u,0x6ab0u,0x6c90u,0x6cb0u,0x6cf0u,0x6ac0u,0x6c98u,0x6cb8u,0x6cf8u,0x6ad0u,0x6d20u,0x6d00u,0x6d40u,0x6b00u,0x6d28u,0x6d08u,0x6d48u,0x6b10u,0x6d30u,0x6d10u,0x6d50u,0x6b60u,0x6d38u,0x6d18u,0x6d58u,0x6b70u,0x6da0u,0x6d80u,0x6dc0u,0x6b80u,0x6da8u,0x6d88u,0x6dc8u,0x6b90u,0x6db0u,0x6d90u,0x6dd0u,0x6be0u,0x6db8u,0x6d98u,0x6dd8u,0x6bf0u,0x7040u,0x7060u,0x7020u,0x6c60u,0x7048u,0x7068u,0x7028u,0x6c70u,0x7050u,0x7070u,0x7030u,0x6c00u,0x7058u,0x7078u,0x7038u,0x6c10u,0x70c0u,0x70e0u,0x70a0u,0x6ce0u,0x70c8u,0x70e8u,0x70a8u,0x6cf0u,0x70d0u,0x70f0u,0x70b0u,0x6c80u,0x70d8u,0x70f8u,0x70b8u,0x6c90u,0x7160u,0x7140u,0x7100u,0x6d40u,0x7168u,0x7148u,0x7108u,0x6d50u,0x7170u,0x7150u,0x7110u,0x6d20u,0x7178u,0x7158u,0x7118u,0x6d30u,0x71e0u,0x71c0u,0x7180u,0x6dc0u,0x71e8u,0x71c8u,0x7188u,0x6dd0u,0x71f0u,0x71d0u,0x7190u,0x6da0u,0x71f8u,0x71d8u,0x7198u,0x6db0u,0x7400u,0x7420u,0x7460u,0x6e60u,0x7408u,0x7428u,0x7468u,0x6e70u,0x7410u,0x7430u,0x7470u,0x6e00u,0x7418u,0x7438u,0x7478u,0x6e10u,0x7480u,0x74a0u,0x74e0u,0x6ee0u,0x7488u,0x74a8u,0x74e8u,0x6ef0u,0x7490u,0x74b0u,0x74f0u,0x6e80u,0x7498u,0x74b8u,0x74f8u,0x6e90u,0x7520u,0x7500u,0x7540u,0x6f40u,0x7528u,0x7508u,0x7548u,0x6f50u,0x7530u,0x7510u,0x7550u,0x6f20u,0x7538u,0x7518u,0x7558u,0x6f30u,0x75a0u,0x7580u,0x75c0u,0x6fc0u,0x75a8u,0x7588u,0x75c8u,0x6fd0u,0x75b0u,0x7590u,0x75d0u,0x6fa0u,0x75b8u,0x7598u,0x75d8u,0x6fb0u,0x6860u,0x1ea40u,0x1ec20u,0x2440u,0x6868u,0x1eed0u,0x1f0b0u,0x2450u,0x6870u,0x1f360u,0x1f540u,0x2460u,0x6878u,0x1f7f0u,0x1f9d0u,0x2470u,0x68e0u,0x1fc80u,0x1fe60u,0x2400u,0x68e8u,0x20110u,0x202f0u,0x2410u,0x68f0u,0x205a0u,0x20780u,0x2420u,0x68f8u,0x20a30u,0x20c10u,0x2430u,0x6940u,0x20ec0u,0x210a0u,0x25c0u,0x6948u,0x21350u,0x21530u,0x25d0u,0x6950u,0x217e0u,0x219c0u,0x25e0u,0x6958u,0x21c70u,0x21e50u,0x25f0u,0x69c0u,0x22100u,0x222e0u,0x2580u,0x69c8u,0x22590u,0x22770u,0x2590u,0x69d0u,0x22a20u,0x22c00u,0x25a0u,0x69d8u,0x22eb0u,0x23090u,0x25b0u,0x6a60u,0x1ea70u,0x1ec28u,0x2760u,0x6a68u,0x1ef00u,0x1f0b8u,0x2770u,0x6a70u,0x1f390u,0x1f548u,0x2740u,0x6a78u,0x1f820u,0x1f9d8u,0x2750u,0x6ae0u,0x1fcb0u,0x1fe68u,0x2720u,0x6ae8u,0x20140u,0x202f8u,0x2730u,0x6af0u,0x205d0u,0x20788u,0x2700u,0x6af8u,0x20a60u,0x20c18u,0x2710u,0x6b40u,0x20ef0u,0x210a8u,0x28e0u,0x6b48u,0x21380u,0x21538u,0x28f0u,0x6b50u,0x21810u,0x219c8u,0x28c0u,0x6b58u,0x21ca0u,0x21e58u,0x28d0u,0x6bc0u,0x22130u,0x222e8u,0x28a0u,0x6bc8u,0x225c0u,0x22778u,0x28b0u,0x6bd0u,0x22a50u,0x22c08u,0x2880u,0x6bd8u,0x22ee0u,0x23098u,0x2890u,0x6c20u,0x1eaa0u,0x1ec30u,0x2a40u,0x6c28u,0x1ef30u,0x1f0c0u,0x2a50u,0x6c30u,0x1f3c0u,0x1f550u,0x2a60u,0x6c38u,0x1f850u,0x1f9e0u,0x2a70u,0x6ca0u,0x1fce0u,0x1fe70u,0x2a00u,0x6ca8u,0x20170u,0x20300u,0x2a10u,0x6cb0u,0x20600u,0x20790u,0x2a20u,0x6cb8u,0x20a90u,0x20c20u,0x2a30u,0x6d00u,0x20f20u,0x210b0u,0x2bc0u,0x6d08u,0x213b0u,0x21540u,0x2bd0u,0x6d10u,0x21840u,0x219d0u,0x2be0u,0x6d18u,0x21cd0u,0x21e60u,0x2bf0u,0x6d80u,0x22160u,0x222f0u,0x2b80u,0x6d88u,0x225f0u,0x22780u,0x2b90u,0x6d90u,0x22a80u,0x22c10u,0x2ba0u,0x6d98u,0x22f10u,0x230a0u,0x2bb0u,0x6e20u,0x1ead0u,0x1ec38u,0x2d60u,0x6e28u,0x1ef60u,0x1f0c8u,0x2d70u,0x6e30u,0x1f3f0u,0x1f558u,0x2d40u,0x6e38u,0x1f880u,0x1f9e8u,0x2d50u,0x6ea0u,0x1fd10u,0x1fe78u,0x2d20u,0x6ea8u,0x201a0u,0x20308u,0x2d30u,0x6eb0u,0x20630u,0x20798u,0x2d00u,0x6eb8u,0x20ac0u,0x20c28u,0x2d10u,0x6f00u,0x20f50u,0x210b8u,0x2ee0u,0x6f08u,0x213e0u,0x21548u,0x2ef0u,0x6f10u,0x21870u,0x219d8u,0x2ec0u,0x6f18u,0x21d00u,0x21e68u,0x2ed0u,0x6f80u,0x22190u,0x222f8u,0x2ea0u,0x6f88u,0x22620u,0x22788u,0x2eb0u,0x6f90u,0x22ab0u,0x22c18u,0x2e80u,0x6f98u,0x22f40u,0x230a8u,0x2e90u,0x0u,0x40u,0x9000u,0xcc00u,0x80u,0xc0u,0x9010u,0xcc10u,0x110u,0x150u,0x9020u,0xcc20u,0x190u,0x1d0u,0x9030u,0xcc30u,0x220u,0x260u,0x9040u,0xcc40u,0x2a0u,0x2e0u,0x9050u,0xcc50u,0x330u,0x370u,0x9060u,0xcc60u,0x3b0u,0x3f0u,0x9070u,0xcc70u,0x440u,0x400u,0x9080u,0xcc80u,0x4c0u,0x480u,0x9090u,0xcc90u,0x550u,0x510u,0x90a0u,0xcca0u,0x5d0u,0x590u,0x90b0u,0xccb0u,0x660u,0x620u,0x90c0u,0xccc0u,0x6e0u,0x6a0u,0x90d0u,0xccd0u,0x770u,0x730u,0x90e0u,0xcce0u,0x7f0u,0x7b0u,0x90f0u,0xccf0u,0x10u,0x50u,0x9300u,0xcf00u,0x90u,0xd0u,0x9310u,0xcf10u,0x100u,0x140u,0x9320u,0xcf20u,0x180u,0x1c0u,0x9330u,0xcf30u,0x230u,0x270u,0x9340u,0xcf40u,0x2b0u,0x2f0u,0x9350u,0xcf50u,0x320u,0x360u,0x9360u,0xcf60u,0x3a0u,0x3e0u,0x9370u,0xcf70u,0x450u,0x410u,0x9380u,0xcf80u,0x4d0u,0x490u,0x9390u,0xcf90u,0x540u,0x500u,0x93a0u,0xcfa0u,0x5c0u,0x580u,0x93b0u,0xcfb0u,0x670u,0x630u,0x93c0u,0xcfc0u,0x6f0u,0x6b0u,0x93d0u,0xcfd0u,0x760u,0x720u,0x93e0u,0xcfe0u,0x7e0u,0x7a0u,0x93f0u,0xcff0u,0x20u,0x60u,0x9600u,0xd200u,0xa0u,0xe0u,0x9610u,0xd210u,0x130u,0x170u,0x9620u,0xd220u,0x1b0u,0x1f0u,0x9630u,0xd230u,0x200u,0x240u,0x9640u,0xd240u,0x280u,0x2c0u,0x9650u,0xd250u,0x310u,0x350u,0x9660u,0xd260u,0x390u,0x3d0u,0x9670u,0xd270u,0x460u,0x420u,0x9680u,0xd280u,0x4e0u,0x4a0u,0x9690u,0xd290u,0x570u,0x530u,0x96a0u,0xd2a0u,0x5f0u,0x5b0u,0x96b0u,0xd2b0u,0x640u,0x600u,0x96c0u,0xd2c0u,0x6c0u,0x680u,0x96d0u,0xd2d0u,0x750u,0x710u,0x96e0u,0xd2e0u,0x7d0u,0x790u,0x96f0u,0xd2f0u,0x30u,0x70u,0x9900u,0xd500u,0xb0u,0xf0u,0x9910u,0xd510u,0x120u,0x160u,0x9920u,0xd520u,0x1a0u,0x1e0u,0x9930u,0xd530u,0x210u,0x250u,0x9940u,0xd540u,0x290u,0x2d0u,0x9950u,0xd550u,0x300u,0x340u,0x9960u,0xd560u,0x380u,0x3c0u,0x9970u,0xd570u,0x470u,0x430u,0x9980u,0xd580u,0x4f0u,0x4b0u,0x9990u,0xd590u,0x560u,0x520u,0x99a0u,0xd5a0u,0x5e0u,0x5a0u,0x99b0u,0xd5b0u,0x650u,0x610u,0x99c0u,0xd5c0u,0x6d0u,0x690u,0x99d0u,0xd5d0u,0x740u,0x700u,0x99e0u,0xd5e0u,0x7c0u,0x780u,0x99f0u,0xd5f0u,0xc008u,0x12120u,0x12160u,0x12c00u,0xc018u,0x122b0u,0x122f0u,0x12c10u,0xc028u,0x12400u,0x12440u,0x12c20u,0xc038u,0x12590u,0x125d0u,0x12c30u,0xc048u,0x12760u,0x12720u,0x12c40u,0xc058u,0x128f0u,0x128b0u,0x12c50u,0xc068u,0x12a40u,0x12a00u,0x12c60u,0xc078u,0x12bd0u,0x12b90u,0x12c70u,0xc088u,0x12d20u,0x12d60u,0x12c80u,0xc098u,0x12eb0u,0x12ef0u,0x12c90u,0xc0a8u,0x13000u,0x13040u,0x12ca0u,0xc0b8u,0x13190u,0x131d0u,0x12cb0u,0xc0c8u,0x13360u,0x13320u,0x12cc0u,0xc0d8u,0x134f0u,0x134b0u,0x12cd0u,0xc0e8u,0x13640u,0x13600u,0x12ce0u,0xc0f8u,0x137d0u,0x13790u,0x12cf0u,0xc308u,0x12130u,0x12168u,0x12d00u,0xc318u,0x122a0u,0x122f8u,0x12d10u,0xc328u,0x12410u,0x12448u,0x12d20u,0xc338u,0x12580u,0x125d8u,0x12d30u,0xc348u,0x12770u,0x12728u,0x12d40u,0xc358u,0x128e0u,0x128b8u,0x12d50u,0xc368u,0x12a50u,0x12a08u,0x12d60u,0xc378u,0x12bc0u,0x12b98u,0x12d70u,0xc388u,0x12d30u,0x12d68u,0x12d80u,0xc398u,0x12ea0u,0x12ef8u,0x12d90u,0xc3a8u,0x13010u,0x13048u,0x12da0u,0xc3b8u,0x13180u,0x131d8u,0x12db0u,0xc3c8u,0x13370u,0x13328u,0x12dc0u,0xc3d8u,0x134e0u,0x134b8u,0x12dd0u,0xc3e8u,0x13650u,0x13608u,0x12de0u,0xc3f8u,0x137c0u,0x13798u,0x12df0u,0xc608u,0x12140u,0x12170u,0x12e00u,0xc618u,0x122d0u,0x122e0u,0x12e10u,0xc628u,0x12460u,0x12450u,0x12e20u,0xc638u,0x125f0u,0x125c0u,0x12e30u,0xc648u,0x12700u,0x12730u,0x12e40u,0xc658u,0x12890u,0x128a0u,0x12e50u,0xc668u,0x12a20u,0x12a10u,0x12e60u,0xc678u,0x12bb0u,0x12b80u,0x12e70u,0xc688u,0x12d40u,0x12d70u,0x12e80u,0xc698u,0x12ed0u,0x12ee0u,0x12e90u,0xc6a8u,0x13060u,0x13050u,0x12ea0u,0xc6b8u,0x131f0u,0x131c0u,0x12eb0u,0xc6c8u,0x13300u,0x13330u,0x12ec0u,0xc6d8u,0x13490u,0x134a0u,0x12ed0u,0xc6e8u,0x13620u,0x13610u,0x12ee0u,0xc6f8u,0x137b0u,0x13780u,0x12ef0u,0xc908u,0x12150u,0x12178u,0x12f00u,0xc918u,0x122c0u,0x122e8u,0x12f10u,0xc928u,0x12470u,0x12458u,0x12f20u,0xc938u,0x125e0u,0x125c8u,0x12f30u,0xc948u,0x12710u,0x12738u,0x12f40u,0xc958u,0x12880u,0x128a8u,0x12f50u,0xc968u,0x12a30u,0x12a18u,0x12f60u,0xc978u,0x12ba0u,0x12b88u,0x12f70u,0xc988u,0x12d50u,0x12d78u,0x12f80u,0xc998u,0x12ec0u,0x12ee8u,0x12f90u,0xc9a8u,0x13070u,0x13058u,0x12fa0u,0xc9b8u,0x131e0u,0x131c8u,0x12fb0u,0xc9c8u,0x13310u,0x13338u,0x12fc0u,0xc9d8u,0x13480u,0x134a8u,0x12fd0u,0xc9e8u,0x13630u,0x13618u,0x12fe0u,0xc9f8u,0x137a0u,0x13788u,0x12ff0u,0x8000u,0x1c300u,0x1ea40u,0x1ec20u,0x8080u,0x1c310u,0x100000u,0x1ec22u,0x8110u,0x1c320u,0x100000u,0x1ec24u,0x8190u,0x1c330u,0x100000u,0x1ec26u,0x8220u,0x1c340u,0x1ea40u,0x1ec28u,0x82a0u,0x1c350u,0x100000u,0x1ec2au,0x8330u,0x1c360u,0x100000u,0x1ec2cu,0x83b0u,0x1c370u,0x100000u,0x1ec2eu,0x8440u,0x1c380u,0x1ea40u,0x1ec30u,0x84c0u,0x1c390u,0x100000u,0x1ec32u,0x8550u,0x1c3a0u,0x100000u,0x1ec34u,0x85d0u,0x1c3b0u,0x100000u,0x1ec36u,0x8660u,0x1c3c0u,0x1ea40u,0x1ec38u,0x86e0u,0x1c3d0u,0x100000u,0x1ec3au,0x8770u,0x1c3e0u,0x100000u,0x1ec3cu,0x87f0u,0x1c3f0u,0x100000u,0x1ec3eu,0x8010u,0x1c400u,0x1ea70u,0x1ec20u,0x8090u,0x1c410u,0x100000u,0x1ec22u,0x8100u,0x1c420u,0x100000u,0x1ec24u,0x8180u,0x1c430u,0x100000u,0x1ec26u,0x8230u,0x1c440u,0x1ea70u,0x1ec28u,0x82b0u,0x1c450u,0x100000u,0x1ec2au,0x8320u,0x1c460u,0x100000u,0x1ec2cu,0x83a0u,0x1c470u,0x100000u,0x1ec2eu,0x8450u,0x1c480u,0x1ea70u,0x1ec30u,0x84d0u,0x1c490u,0x100000u,0x1ec32u,0x8540u,0x1c4a0u,0x100000u,0x1ec34u,0x85c0u,0x1c4b0u,0x100000u,0x1ec36u,0x8670u,0x1c4c0u,0x1ea70u,0x1ec38u,0x86f0u,0x1c4d0u,0x100000u,0x1ec3au,0x8760u,0x1c4e0u,0x100000u,0x1ec3cu,0x87e0u,0x1c4f0u,0x100000u,0x1ec3eu,0x8020u,0x1c500u,0x1eaa0u,0x1ec20u,0x80a0u,0x1c510u,0x100000u,0x1ec22u,0x8130u,0x1c520u,0x100000u,0x1ec24u,0x81b0u,0x1c530u,0x100000u,0x1ec26u,0x8200u,0x1c540u,0x1eaa0u,0x1ec28u,0x8280u,0x1c550u,0x100000u,0x1ec2au,0x8310u,0x1c560u,0x100000u,0x1ec2cu,0x8390u,0x1c570u,0x100000u,0x1ec2eu,0x8460u,0x1c580u,0x1eaa0u,0x1ec30u,0x84e0u,0x1c590u,0x100000u,0x1ec32u,0x8570u,0x1c5a0u,0x100000u,0x1ec34u,0x85f0u,0x1c5b0u,0x100000u,0x1ec36u,0x8640u,0x1c5c0u,0x1eaa0u,0x1ec38u,0x86c0u,0x1c5d0u,0x100000u,0x1ec3au,0x8750u,0x1c5e0u,0x100000u,0x1ec3cu,0x87d0u,0x1c5f0u,0x100000u,0x1ec3eu,0x8030u,0x1c600u,0x1ead0u,0x1ec20u,0x80b0u,0x1c610u,0x100000u,0x1ec22u,0x8120u,0x1c620u,0x100000u,0x1ec24u,0x81a0u,0x1c630u,0x100000u,0x1ec26u,0x8210u,0x1c640u,0x1ead0u,0x1ec28u,0x8290u,0x1c650u,0x100000u,0x1ec2au,0x8300u,0x1c660u,0x100000u,0x1ec2cu,0x8380u,0x1c670u,0x100000u,0x1ec2eu,0x8470u,0x1c680u,0x1ead0u,0x1ec30u,0x84f0u,0x1c690u,0x100000u,0x1ec32u,0x8560u,0x1c6a0u,0x100000u,0x1ec34u,0x85e0u,0x1c6b0u,0x100000u,0x1ec36u,0x8650u,0x1c6c0u,0x1ead0u,0x1ec38u,0x86d0u,0x1c6d0u,0x100000u,0x1ec3au,0x8740u,0x1c6e0u,0x100000u,0x1ec3cu,0x87c0u,0x1c6f0u,0x100000u,0x1ec3eu,0x27500u,0x1d480u,0x1d480u,0x80000000u,0x27502u,0x1d482u,0x100000u,0x80000000u,0x27504u,0x1d484u,0x100000u,0x80000000u,0x27506u,0x1d486u,0x100000u,0x80000000u,0x27700u,0x1d490u,0x1d480u,0x80000000u,0x27702u,0x1d492u,0x100000u,0x80000000u,0x27704u,0x1d494u,0x100000u,0x80000000u,0x27706u,0x1d496u,0x100000u,0x80000000u,0x27900u,0x1d4a0u,0x1d480u,0x80000000u,0x27902u,0x1d4a2u,0x100000u,0x80000000u,0x27904u,0x1d4a4u,0x100000u,0x80000000u,0x27906u,0x1d4a6u,0x100000u,0x80000000u,0x27b00u,0x1d4b0u,0x1d480u,0x80000000u,0x27b02u,0x1d4b2u,0x100000u,0x80000000u,0x27b04u,0x1d4b4u,0x100000u,0x80000000u,0x27b06u,0x1d4b6u,0x100000u,0x80000000u,0x27508u,0x1d488u,0x1d490u,0x80000000u,0x2750au,0x1d48au,0x100000u,0x80000000u,0x2750cu,0x1d48cu,0x100000u,0x80000000u,0x2750eu,0x1d48eu,0x100000u,0x80000000u,0x27708u,0x1d498u,0x1d490u,0x80000000u,0x2770au,0x1d49au,0x100000u,0x80000000u,0x2770cu,0x1d49cu,0x100000u,0x80000000u,0x2770eu,0x1d49eu,0x100000u,0x80000000u,0x27908u,0x1d4a8u,0x1d490u,0x80000000u,0x2790au,0x1d4aau,0x100000u,0x80000000u,0x2790cu,0x1d4acu,0x100000u,0x80000000u,0x2790eu,0x1d4aeu,0x100000u,0x80000000u,0x27b08u,0x1d4b8u,0x1d490u,0x80000000u,0x27b0au,0x1d4bau,0x100000u,0x80000000u,0x27b0cu,0x1d4bcu,0x100000u,0x80000000u,0x27b0eu,0x1d4beu,0x100000u,0x80000000u,0x26d08u,0x1d4c0u,0x1d4a0u,0x80000000u,0x26d0au,0x1d4c2u,0x100000u,0x80000000u,0x26d0cu,0x1d4c4u,0x100000u,0x80000000u,0x26d0eu,0x1d4c6u,0x100000u,0x80000000u,0x26f08u,0x1d4d0u,0x1d4a0u,0x80000000u,0x26f0au,0x1d4d2u,0x100000u,0x80000000u,0x26f0cu,0x1d4d4u,0x100000u,0x80000000u,0x26f0eu,0x1d4d6u,0x100000u,0x80000000u,0x27108u,0x1d4e0u,0x1d4a0u,0x80000000u,0x2710au,0x1d4e2u,0x100000u,0x80000000u,0x2710cu,0x1d4e4u,0x100000u,0x80000000u,0x2710eu,0x1d4e6u,0x100000u,0x80000000u,0x27308u,0x1d4f0u,0x1d4a0u,0x80000000u,0x2730au,0x1d4f2u,0x100000u,0x80000000u,0x2730cu,0x1d4f4u,0x100000u,0x80000000u,0x2730eu,0x1d4f6u,0x100000u,0x80000000u,0x100000u,0x1d4c8u,0x1d4b0u,0x80000000u,0x100000u,0x1d4cau,0x100000u,0x80000000u,0x100000u,0x1d4ccu,0x100000u,0x80000000u,0x100000u,0x1d4ceu,0x100000u,0x80000000u,0x100000u,0x1d4d8u,0x1d4b0u,0x80000000u,0x100000u,0x1d4dau,0x100000u,0x80000000u,0x100000u,0x1d4dcu,0x100000u,0x80000000u,0x100000u,0x1d4deu,0x100000u,0x80000000u,0x100000u,0x1d4e8u,0x1d4b0u,0x80000000u,0x100000u,0x1d4eau,0x100000u,0x80000000u,0x100000u,0x1d4ecu,0x100000u,0x80000000u,0x100000u,0x1d4eeu,0x100000u,0x80000000u,0x100000u,0x1d4f8u,0x1d4b0u,0x80000000u,0x100000u,0x1d4fau,0x100000u,0x80000000u,0x100000u,0x1d4fcu,0x100000u,0x80000000u,0x100000u,0x1d4feu,0x100000u,0x80000000u,0x30000u,0x60420u,0xbfc50u,0xf0000u,0x30010u,0x60430u,0xbfc40u,0xf0010u,0x30020u,0x60400u,0xbfc70u,0xf0020u,0x30030u,0x60410u,0xbfc60u,0xf0030u,0x30040u,0x60460u,0xbfc10u,0xf0040u,0x30050u,0x60470u,0xbfc00u,0xf0050u,0x30060u,0x60440u,0xbfc30u,0xf0060u,0x30070u,0x60450u,0xbfc20u,0xf0070u,0x30080u,0x604b0u,0xbfcd0u,0xf0080u,0x30090u,0x604a0u,0xbfcc0u,0xf0090u,0x300a0u,0x60490u,0xbfcf0u,0xf00a0u,0x300b0u,0x60480u,0xbfce0u,0xf00b0u,0x300c0u,0x604f0u,0xbfc90u,0xf00c0u,0x300d0u,0x604e0u,0xbfc80u,0xf00d0u,0x300e0u,0x604d0u,0xbfcb0u,0xf00e0u,0x300f0u,0x604c0u,0xbfca0u,0xf00f0u,0x30100u,0x60530u,0xbfd60u,0xf0100u,0x30110u,0x60520u,0xbfd70u,0xf0110u,0x30120u,0x60510u,0xbfd40u,0xf0120u,0x30130u,0x60500u,0xbfd50u,0xf0130u,0x30140u,0x60570u,0xbfd20u,0xf0140u,0x30150u,0x60560u,0xbfd30u,0xf0150u,0x30160u,0x60550u,0xbfd00u,0xf0160u,0x30170u,0x60540u,0xbfd10u,0xf0170u,0x30190u,0x605b0u,0xbfde0u,0xf0190u,0x30180u,0x605a0u,0xbfdf0u,0xf0180u,0x301b0u,0x60590u,0xbfdc0u,0xf01b0u,0x301a0u,0x60580u,0xbfdd0u,0xf01a0u,0x301d0u,0x605f0u,0xbfda0u,0xf01d0u,0x301c0u,0x605e0u,0xbfdb0u,0xf01c0u,0x301f0u,0x605d0u,0xbfd80u,0xf01f0u,0x301e0u,0x605c0u,0xbfd90u,0xf01e0u,0x30210u,0x60640u,0xbfe60u,0xf0210u,0x30200u,0x60650u,0xbfe70u,0xf0200u,0x30230u,0x60660u,0xbfe40u,0xf0230u,0x30220u,0x60670u,0xbfe50u,0xf0220u,0x30250u,0x60600u,0xbfe20u,0xf0250u,0x30240u,0x60610u,0xbfe30u,0xf0240u,0x30270u,0x60620u,0xbfe00u,0xf0270u,0x30260u,0x60630u,0xbfe10u,0xf0260u,0x30290u,0x606c0u,0xbfef0u,0xf0290u,0x30280u,0x606d0u,0xbfee0u,0xf0280u,0x302b0u,0x606e0u,0xbfed0u,0xf02b0u,0x302a0u,0x606f0u,0xbfec0u,0xf02a0u,0x302d0u,0x60680u,0xbfeb0u,0xf02d0u,0x302c0u,0x60690u,0xbfea0u,0xf02c0u,0x302f0u,0x606a0u,0xbfe90u,0xf02f0u,0x302e0u,0x606b0u,0xbfe80u,0xf02e0u,0x30320u,0x60740u,0xbff70u,0x11f720u,0x30330u,0x60750u,0xbff60u,0x11f730u,0x30300u,0x60760u,0xbff50u,0x11f700u,0x30310u,0x60770u,0xbff40u,0x11f710u,0x30360u,0x60700u,0xbff30u,0x11f760u,0x30370u,0x60710u,0xbff20u,0x11f770u,0x30340u,0x60720u,0xbff10u,0x11f740u,0x30350u,0x60730u,0xbff00u,0x11f750u,0x303a0u,0x8fbd0u,0xbfff0u,0x11f7a0u,0x303b0u,0x8fbc0u,0xbffe0u,0x11f7b0u,0x30380u,0x8fbf0u,0xbffd0u,0x11f780u,0x30390u,0x8fbe0u,0xbffc0u,0x11f790u,0x303e0u,0x8fb90u,0xbffb0u,0x11f7e0u,0x303f0u,0x8fb80u,0xbffa0u,0x11f7f0u,0x303c0u,0x8fbb0u,0xbff90u,0x11f7c0u,0x303d0u,0x8fba0u,0xbff80u,0x11f7d0u,0x0u,0x80000000u,0x0u,0x36030u,0x0u,0x80000000u,0x0u,0x36030u,0x0u,0x80000000u,0x0u,0x36030u,0x0u,0x80000000u,0x0u,0x36030u,0x0u,0x80000000u,0x0u,0x36030u,0x0u,0x80000000u,0x0u,0x36030u,0x0u,0x80000000u,0x0u,0x36030u,0x0u,0x80000000u,0x0u,0x36030u,0x0u,0x80000000u,0x0u,0x360b2u,0x0u,0x80000000u,0x0u,0x360b2u,0x0u,0x80000000u,0x0u,0x360b2u,0x0u,0x80000000u,0x0u,0x360b2u,0x0u,0x80000000u,0x0u,0x360b2u,0x0u,0x80000000u,0x0u,0x360b2u,0x0u,0x80000000u,0x0u,0x360b2u,0x0u,0x80000000u,0x0u,0x360b2u,0x0u,0x80000000u,0x0u,0x36092u,0x0u,0x80000000u,0x0u,0x36092u,0x0u,0x80000000u,0x0u,0x36092u,0x0u,0x80000000u,0x0u,0x36092u,0x0u,0x80000000u,0x0u,0x36092u,0x0u,0x80000000u,0x0u,0x36092u,0x0u,0x80000000u,0x0u,0x36092u,0x0u,0x80000000u,0x0u,0x36092u,0x0u,0x80000000u,0x0u,0x3609au,0x0u,0x80000000u,0x0u,0x3609au,0x0u,0x80000000u,0x0u,0x3609au,0x0u,0x80000000u,0x0u,0x3609au,0x0u,0x80000000u,0x0u,0x3609au,0x0u,0x80000000u,0x0u,0x3609au,0x0u,0x80000000u,0x0u,0x3609au,0x0u,0x80000000u,0x0u,0x3609au,0x0u,0x80000000u,0x0u,0x36059u,0x0u,0x80000000u,0x0u,0x36059u,0x0u,0x80000000u,0x0u,0x36059u,0x0u,0x80000000u,0x0u,0x36059u,0x0u,0x80000000u,0x0u,0x36059u,0x0u,0x80000000u,0x0u,0x36059u,0x0u,0x80000000u,0x0u,0x36059u,0x0u,0x80000000u,0x0u,0x36059u,0x0u,0x80000000u,0x0u,0x36059u,0x0u,0x80000000u,0x0u,0x36059u,0x0u,0x80000000u,0x0u,0x36059u,0x0u,0x80000000u,0x0u,0x36059u,0x0u,0x80000000u,0x0u,0x36059u,0x0u,0x80000000u,0x0u,0x36059u,0x0u,0x80000000u,0x0u,0x36059u,0x0u,0x80000000u,0x0u,0x36059u,0x0u,0x80000000u,0x0u,0x16071u,0x0u,0x80000000u,0x0u,0x16071u,0x0u,0x80000000u,0x0u,0x16071u,0x0u,0x80000000u,0x0u,0x16071u,0x0u,0x80000000u,0x0u,0x16071u,0x0u,0x80000000u,0x0u,0x16071u,0x0u,0x80000000u,0x0u,0x16071u,0x0u,0x80000000u,0x0u,0x16071u,0x0u,0x80000000u,0x0u,0x14071u,0x0u,0x80000000u,0x0u,0x14071u,0x0u,0x80000000u,0x0u,0x14071u,0x0u,0x80000000u,0x0u,0x14071u,0x0u,0x80000000u,0x0u,0x14071u,0x0u,0x80000000u,0x0u,0x14071u,0x0u,0x80000000u,0x0u,0x14071u,0x0u,0x80000000u,0x0u,0x14071u};

__global__ __launch_bounds__(256, 1) void swin_main(const uint16_t* x, const uint16_t* mask, char* ws, uint16_t* out,
                                                    const uint16_t* wqkv, const uint16_t* wo, const uint16_t* ls,
                                                    const uint16_t* w1, const uint16_t* w2, const uint16_t* rc,
                                                    const int* ridx) {
  __shared__ __attribute__((aligned(16))) char smem[LDS_BYTES];
  const int wave = __builtin_amdgcn_readfirstlane(threadIdx.x >> 6);
  const unsigned lds0 = (unsigned)(size_t)smem;
  asm volatile(
      ".p2align 5\n"
      "s_mov_b64 s[92:93], %0\n"
      "s_mov_b32 s40, %1\n"
      "s_mov_b32 s41, %2\n"
      "s_mov_b64 s[88:89], %3\n"
      "s_load_dwordx2 s[16:17], s[92:93], 0\n"
      "s_load_dwordx2 s[20:21], s[92:93], 32\n"
      "s_load_dwordx2 s[46:47], s[92:93], 40\n"
      "s_load_dwordx8 s[80:87], s[92:93], 48\n"
      "s_load_dwordx2 s[76:77], s[92:93], 80\n"
      "s_mov_b64 s[90:91], s[92:93]\n"
      "s_getpc_b64 s[98:99]\n"
      "L_ipf0_%=:\n"
      "v_mbcnt_lo_u32_b32 v8, -1, 0\n"
      "v_mbcnt_hi_u32_b32 v8, -1, v8\n"
      "v_lshlrev_b32 v239, 2, v8\n"
      "v_lshlrev_b32_e64 v141, 4, v8\n"
      "s_lshr_b32 s68, s41, 3\n"
      "s_mov_b32 s72, 0x3100117e\n"
      "s_mov_b32 s73, 0xe0bec09e\n"
      "s_mov_b32 s70, 0x315e117e\n"
      "s_mov_b32 s71, 0xe04fc09e\n"
      "s_and_b32 s92, s41, 7\n"
      "s_cmp_eq_u32 s92, 6\n"
      "s_cselect_b32 s93, 2, 17\n"
      "s_cmp_lt_u32 s68, s93\n"
      "s_cselect_b64 s[72:73], s[72:73], s[70:71]\n"
      "s_lshl_b32 s92, s92, 3\n"
      "s_lshr_b64 s[72:73], s[72:73], s92\n"
      "s_add_u32 s41, s72, s68\n"
      "s_and_b32 s41, s41, 255\n"
      "s_movk_i32 s96, 4992\n"
      "s_cmp_lt_u32 s41, 192\n"
      "s_cselect_b32 s44, 20, 19\n"
      "s_cselect_b32 s96, 5137, s96\n"
      "s_cmp_lt_u32 s41, 111\n"
      "s_cselect_b32 s96, 5056, s96\n"
      "s_cmp_lt_u32 s41, 17\n"
      "s_cselect_b32 s44, 21, s44\n"
      "s_cselect_b32 s96, 5167, s96\n"
      "s_add_u32 s96, s96, s41\n"
      "s_lshl_b32 s92, s40, 8\n"
      "v_add_u32_e64 v154, s92, v239\n"
      "v_min_u32 v29, 704, v154\n"
      "v_add_u32 v29, 4096, v29\n"
      "v_min_u32 v154, 672, v154\n"
      "v_add_u32 v234, 122112, v154\n"
      "s_lshl_b32 s92, s68, 7\n"
      "v_lshlrev_b32_e64 v236, 7, v8\n"
      "s_mul_i32 s93, s92, 18\n"
      "s_mul_i32 s94, s40, 640\n"
      "v_min_u32 v238, 512, v236\n"
      "v_add_u32 v238, s94, v238\n"
      "v_min_u32 v238, 2176, v238\n"
      "v_add_u32 v238, s93, v238\n"
      "s_cmp_eq_u32 s40, 1\n"
      "s_cselect_b32 s93, 4096, 0\n"
      "s_add_u32 s92, s92, s93\n"
      "s_cmp_eq_u32 s40, 2\n"
      "s_cselect_b32 s93, 9, 8\n"
      "s_cselect_b64 s[72:73], s[98:99], s[88:89]\n"
      "s_cmp_lt_u32 s40, 2\n"
      "s_cselect_b32 s93, 27, s93\n"
      "s_mul_i32 s92, s92, s93\n"
      "s_lshl_b32 s93, s93, 7\n"
      "s_sub_u32 s93, s93, 128\n"
      "v_min_u32 v236, s93, v236\n"
      "v_add_u32 v236, s92, v236\n"
      "s_lshl_b32 s95, s40, 3\n"
      "v_lshrrev_b32 v210, 3, v8\n"
      "v_and_b32 v237, 7, v8\n"
      "v_add_u32_e64 v211, s95, v210\n"
      "v_bfe_u32 v208, v211, 1, 3\n"
      "v_cmp_lt_u32 s[70:71], s44, v211\n"
      "v_min_u32_e64 v211, s44, v211\n"
      "s_mov_b32 s18, 100270464\n"
      "s_mov_b32 s19, 0x20000\n"
      "s_mov_b32 s78, 9604\n"
      "s_mov_b32 s79, 0x20000\n"
      "s_mov_b32 s22, 221184\n"
      "s_mov_b32 s23, 0x20000\n"
      "s_lshl_b32 s64, s40, 10\n"
      "v_add_u32 v36, s64, v141\n"
      "s_add_u32 s75, s64, 18432\n"
      "s_and_b32 s92, s40, 1\n"
      "s_lshl_b32 s92, s92, 2\n"
      "s_mul_i32 s93, s40, 9216\n"
      "v_and_b32_e64 v209, 2, v210\n"
      "v_xor_b32_e64 v209, v209, v237\n"
      "v_xor_b32_e64 v209, s92, v209\n"
      "v_lshl_add_u32 v209, v209, 4, s93\n"
      "v_mul_u32_u24 v106, 1152, v210\n"
      "v_add_u32_e64 v106, v106, v209\n"
      "s_cmp_lt_u32 s40, 2\n"
      "s_cselect_b32 s65, s64, 163840\n"
      "s_waitcnt lgkmcnt(0)\n"
      "s_cselect_b64 s[72:73], s[20:21], s[72:73]\n"
      "global_load_dword v154, v154, s[86:87]\n"
      "global_load_dword a255, v236, s[72:73]\n"
      "global_load_dword a253, v238, s[46:47]\n"
      "s_mul_i32 s92, s40, 8192\n"
      "s_add_u32 s88, s88, s92\n"
      "s_addc_u32 s89, s89, 0\n"
      "s_add_u32 m0, s64, 124928\n"
      "s_movk_i32 s92, 0\n"
      "buffer_load_dwordx4 v36, s[76:79], s92 offen  lds\n"
      "s_add_u32 m0, s64, 129024\n"
      "s_movk_i32 s92, 4096\n"
      "buffer_load_dwordx4 v36, s[76:79], s92 offen  lds\n"
      "s_add_u32 m0, s64, 133120\n"
      "s_cmp_lt_u32 s40, 2\n"
      "s_cselect_b32 m0, m0, s65\n"
      "s_movk_i32 s92, 8192\n"
      "buffer_load_dwordx4 v36, s[76:79], s92 offen  lds\n"
      "s_load_dwordx16 s[0:15], s[82:83], 0\n"
      "s_load_dwordx16 s[48:63], s[84:85], 0\n"
      "s_load_dwordx16 s[24:39], s[84:85], 64\n"
      "s_load_dwordx2 s[66:67], s[80:81], 0\n"
      "s_add_u32 s93, s41, 1216\n"
      "v_lshl_add_u32 v238, v211, 8, s41\n"
      "v_mul_u32_u24 v236, 192, v211\n"
      "v_add_u32 v236, s93, v236\n"
      "v_cmp_lt_u32 vcc, 18, v211\n"
      "v_mov_b32 v209, s96\n"
      "s_nop 0\n"
      "v_cndmask_b32 v238, v238, v236, vcc\n"
      "v_cmp_eq_u32 vcc, s44, v211\n"
      "s_nop 1\n"
      "v_cndmask_b32 v238, v238, v209, vcc\n"
      "v_subrev_u32 v209, 5184, v238\n"
      "v_cmp_le_u32 vcc, 5184, v238\n"
      "v_mul_u32_u24 v233, 58255, v238\n"
      "v_lshrrev_b32_e64 v233, 22, v233\n"
      "v_mul_u32_u24 v69, 72, v233\n"
      "v_sub_u32 v69, v238, v69\n"
      "v_min_u32 v236, 72, v209\n"
      "v_cndmask_b32 v233, v233, v236, vcc\n"
      "v_subrev_u32 v236, 72, v209\n"
      "v_min_u32 v236, 72, v236\n"
      "v_cndmask_b32 v69, v69, v236, vcc\n"
      "v_mad_u32_u24 v236, v233, 7, 9\n"
      "v_subrev_u32 v238, 0x1ff, v236\n"
      "v_min_u32 v236, v236, v238\n"
      "v_mad_u32_u24 v238, v69, 7, 9\n"
      "v_subrev_u32 v209, 0x1ff, v238\n"
      "v_min_u32 v238, v238, v209\n"
      "v_mul_u32_u24 v236, 0x1ff, v236\n"
      "v_add_u32 v236, v236, v238\n"
      "v_mul_u32_u24 v238, 0x180, v236\n"
      "v_bfrev_b32 v211, 1\n"
      "v_cndmask_b32 v238, v238, v211, s[70:71]\n"
      "v_xor_b32 v208, v208, v237\n"
      "v_lshl_add_u32 v238, v208, 4, v238\n"
      "s_add_u32 m0, s64, 150784\n"
      "s_nop 0\n"
      "buffer_load_dwordx4 v238, s[16:19], 0 offen  lds\n"
      "s_add_u32 m0, s64, 154752\n"
      "s_nop 0\n"
      "buffer_load_dwordx4 v238, s[16:19], 0 offen offset:128  lds\n"
      "s_add_u32 m0, s64, 158720\n"
      "s_nop 0\n"
      "buffer_load_dwordx4 v238, s[16:19], 0 offen offset:256  lds\n"
      "s_movk_i32 s95, 504\n"
      "s_and_b32 s92, s40, 1\n"
      "s_lshl_b32 s92, s92, 2\n"
      "v_lshrrev_b32 v238, 1, v210\n"
      "v_xor_b32 v238, v238, v237\n"
      "v_xor_b32 v238, s92, v238\n"
      "v_lshlrev_b32 v238, 4, v238\n"
      "s_lshl_b32 s92, s40, 3\n"
      "s_brev_b32 s43, 1\n"
      "s_cmp_lt_u32 s40, 2\n"
      "s_cselect_b32 s93, 32, -16\n"
      "s_cselect_b32 s94, 0, 128\n"
      "s_cselect_b32 s43, 256, s43\n"
      "v_add_u32 v237, s92, v210\n"
      "v_mul_u32_u24 v236, 37, v237\n"
      "v_lshrrev_b32 v236, 8, v236\n"
      "v_mad_u32_u24 v236, v236, s95, v237\n"
      "v_mul_u32_u24 v236, 384, v236\n"
      "v_add_u32 v16, v236, v238\n"
      "v_add_u32 v236, s93, v237\n"
      "v_mul_u32_u24 v210, 37, v236\n"
      "v_lshrrev_b32 v210, 8, v210\n"
      "v_mad_u32_u24 v210, v210, s95, v236\n"
      "v_mul_u32_u24 v210, 384, v210\n"
      "v_add3_u32 v17, v210, v238, s94\n"
      "v_add_u32 v236, 16, v237\n"
      "v_mul_u32_u24 v237, 37, v236\n"
      "v_lshrrev_b32_e64 v237, 8, v237\n"
      "v_mad_u32_u24 v237, v237, s95, v236\n"
      "v_mul_u32_u24 v237, 384, v237\n"
      "s_movk_i32 s94, 128\n"
      "v_add3_u32 v18, v237, v238, s94\n"
      "v_add_u32 v19, 256, v16\n"
      "v_add_u32 v15, s43, v17\n"
      "s_add_u32 s92, s41, 0\n"
      "s_mul_i32 s93, s92, 58255\n"
      "s_lshr_b32 s93, s93, 22\n"
      "s_mul_i32 s93, s93, 439\n"
      "s_add_u32 s45, s92, s93\n"
      "s_mul_i32 s45, s45, 2688\n"
      "s_add_u32 s45, s45, 589824\n"
      "s_add_u32 m0, s64, 0\n"
      "s_nop 0\n"
      "buffer_load_dwordx4 v16, s[16:19], s45 offen  lds\n"
      "s_add_u32 m0, s64, 4096\n"
      "s_nop 0\n"
      "buffer_load_dwordx4 v17, s[16:19], s45 offen  lds\n"
      "s_add_u32 m0, s64, 8192\n"
      "s_nop 0\n"
      "buffer_load_dwordx4 v18, s[16:19], s45 offen  lds\n"
      "s_add_u32 m0, s64, 12288\n"
      "s_nop 0\n"
      "buffer_load_dwordx4 v19, s[16:19], s45 offen  lds\n"
      "s_add_u32 m0, s65, 16384\n"
      "s_nop 0\n"
      "buffer_load_dwordx4 v15, s[16:19], s45 offen  lds\n"
      "s_add_u32 s92, s41, 0\n"
      "s_mul_i32 s93, s92, 58255\n"
      "s_lshr_b32 s93, s93, 22\n"
      "s_add_u32 s47, s92, s93\n"
      "s_mul_i32 s47, s47, 4802\n"
      "s_and_b32 s47, s47, -4\n"
      "s_add_u32 s92, s41, 256\n"
      "s_mul_i32 s93, s92, 58255\n"
      "s_lshr_b32 s93, s93, 22\n"
      "s_add_u32 s74, s92, s93\n"
      "s_mul_i32 s74, s74, 4802\n"
      "s_and_b32 s74, s74, -4\n"
      "s_mul_i32 s93, s93, 439\n"
      "s_add_u32 s46, s92, s93\n"
      "s_mul_i32 s46, s46, 2688\n"
      "s_add_u32 s46, s46, 589824\n"
      "v_subrev_u32 v237, 2, v8\n"
      "v_cmp_ge_u32 s[72:73], s44, v237\n"
      "v_min_u32 v237, s44, v237\n"
      "s_add_u32 s93, s41, 1216\n"
      "v_lshl_add_u32 v238, v237, 8, s41\n"
      "v_mul_u32_u24 v236, 192, v237\n"
      "v_add_u32 v236, s93, v236\n"
      "v_cmp_lt_u32 vcc, 18, v237\n"
      "v_mov_b32 v210, s96\n"
      "s_nop 0\n"
      "v_cndmask_b32 v238, v238, v236, vcc\n"
      "v_cmp_eq_u32 vcc, s44, v237\n"
      "s_nop 1\n"
      "v_cndmask_b32 v238, v238, v210, vcc\n"
      "v_subrev_u32 v210, 5184, v238\n"
      "v_cmp_le_u32 vcc, 5184, v238\n"
      "v_mul_u32_u24 v211, 58255, v238\n"
      "v_lshrrev_b32 v211, 22, v211\n"
      "v_mul_u32_u24 v208, 72, v211\n"
      "v_sub_u32_e64 v208, v238, v208\n"
      "v_min_u32 v236, 72, v210\n"
      "v_cndmask_b32 v211, v211, v236, vcc\n"
      "v_subrev_u32 v236, 72, v210\n"
      "v_min_u32 v236, 72, v236\n"
      "v_cndmask_b32 v208, v208, v236, vcc\n"
      "v_mul_u32_u24 v38, 73, v211\n"
      "v_add_u32 v38, v38, v208\n"
      "v_mul_u32_u24 v38, 4802, v38\n"
      "v_mul_u32_u24 v237, 0x1ff, v211\n"
      "v_add_u32_e64 v237, v237, v208\n"
      "v_mul_u32_u24 v237, 2688, v237\n"
      "v_add_u32 v237, 589824, v237\n"
      "v_cmp_eq_u32 vcc, 72, v211\n"
      "v_or_b32 v69, 0x80000000, v237\n"
      "s_nop 0\n"
      "v_cndmask_b32 v237, v237, v69, vcc\n"
      "v_cmp_eq_u32 vcc, 72, v208\n"
      "v_or_b32_e64 v69, 0x40000000, v237\n"
      "s_nop 0\n"
      "v_cndmask_b32 v237, v237, v69, vcc\n"
      "v_mov_b32 v236, 0x20000000\n"
      "v_cndmask_b32 v237, v236, v237, s[72:73]\n"
      "v_cndmask_b32 v38, v236, v38, s[72:73]\n"
      "v_mov_b32 v37, v236\n"
      "s_nop 1\n"
      "v_mov_b32_dpp v37, v237 wave_shr:1 row_mask:0xf bank_mask:0xf\n"
      "v_mov_b32 v79, 0\n"
      "v_mov_b32 v83, 0\n"
      "v_mov_b32 v87, 0\n"
      "v_mov_b32 v91, 0\n"
      "v_mov_b32 v95, 0\n"
      "s_lshl_b32 s94, s40, 2\n"
      "s_add_u32 s94, s94, 115456\n"
      "v_add_u32 v31, s94, v141\n"
      "v_and_b32 v69, 3, v8\n"
      "v_cmp_ne_u32 vcc, 0, v69\n"
      "v_mov_b32 v69, 0x3f803f80\n"
      "s_nop 0\n"
      "v_cndmask_b32_e64 v32, v69, 0, vcc\n"
      "v_mov_b32 v33, v32\n"
      "v_mov_b32 v34, v32\n"
      "v_mov_b32 v35, v32\n"
      "v_and_b32 v115, 15, v8\n"
      "v_lshrrev_b32_e64 v66, 4, v8\n"
      "v_mul_u32_u24 v115, 196, v115\n"
      "v_add_u32 v115, 124928, v115\n"
      "v_lshl_add_u32 v66, v66, 4, v115\n"
      "v_min_u32 v171, 192, v239\n"
      "v_add_u32 v171, 124928, v171\n"
      "global_load_dwordx4 v[236:239], v141, s[88:89]\n"
      "s_waitcnt lgkmcnt(0)\n"
      "s_pack_ll_b32_b16 s92, s0, s8\n"
      "s_waitcnt vmcnt(14)\n"
      "v_dot2_f32_bf16 v233, s92, v154, 0\n"
      "s_pack_hh_b32_b16 s93, s0, s8\n"
      "v_dot2_f32_bf16 v68, s93, v154, 0\n"
      "s_pack_ll_b32_b16 s94, s1, s9\n"
      "v_dot2_f32_bf16 v155, s94, v154, 0\n"
      "s_pack_hh_b32_b16 s95, s1, s9\n"
      "v_dot2_f32_bf16 v232, s95, v154, 0\n"
      "s_pack_ll_b32_b16 s92, s2, s10\n"
      "v_dot2_f32_bf16 v235, s92, v154, 0\n"
      "s_pack_hh_b32_b16 s93, s2, s10\n"
      "v_dot2_f32_bf16 v132, s93, v154, 0\n"
      "s_pack_ll_b32_b16 s94, s3, s11\n"
      "v_dot2_f32_bf16 v130, s94, v154, 0\n"
      "s_pack_hh_b32_b16 s95, s3, s11\n"
      "v_dot2_f32_bf16 v157, s95, v154, 0\n"
      "s_pack_ll_b32_b16 s92, s4, s12\n"
      "v_dot2_f32_bf16 v131, s92, v154, 0\n"
      "s_pack_hh_b32_b16 s93, s4, s12\n"
      "v_dot2_f32_bf16 v133, s93, v154, 0\n"
      "s_pack_ll_b32_b16 s94, s5, s13\n"
      "v_dot2_f32_bf16 v197, s94, v154, 0\n"
      "global_load_dwordx4 v[208:211], v141, s[88:89] offset:1024\n"
      "s_pack_hh_b32_b16 s95, s5, s13\n"
      "v_dot2_f32_bf16 v156, s95, v154, 0\n"
      "s_pack_ll_b32_b16 s92, s6, s14\n"
      "v_dot2_f32_bf16 v196, s92, v154, 0\n"
      "s_pack_hh_b32_b16 s93, s6, s14\n"
      "v_dot2_f32_bf16 v147, s93, v154, 0\n"
      "s_pack_ll_b32_b16 s94, s7, s15\n"
      "v_dot2_f32_bf16 v194, s94, v154, 0\n"
      "s_pack_hh_b32_b16 s95, s7, s15\n"
      "v_dot2_f32_bf16 v146, s95, v154, 0\n"
      "s_lshl_b32 s92, s40, 4\n"
      "s_lshr_b64 s[66:67], s[66:67], s92\n"
      "s_lshl_b32 s66, s66, 16\n"
      "v_mov_b32 v154, s66\n"
      "v_max_f32 v154, 0xc2c80000, v154\n"
      "v_min_f32 v154, 0x40930000, v154\n"
      "v_mul_f32 v154, 0x3fb8aa3b, v154\n"
      "v_exp_f32 v154, v154\n"
      "s_nop 0\n"
      "v_cvt_pk_bf16_f32 v154, v154, v154\n"
      "v_lshlrev_b32 v154, 16, v154\n"
      "v_max_f32 v233, 0, v233\n"
      "v_max_f32_e64 v68, 0, v68\n"
      "global_load_dwordx4 v[0:3], v141, s[88:89] offset:2048\n"
      "v_cvt_pk_bf16_f32 v233, v233, v68\n"
      "v_max_f32 v155, 0, v155\n"
      "v_max_f32 v232, 0, v232\n"
      "v_cvt_pk_bf16_f32 v155, v155, v232\n"
      "v_max_f32 v235, 0, v235\n"
      "v_max_f32 v132, 0, v132\n"
      "v_cvt_pk_bf16_f32 v235, v235, v132\n"
      "v_max_f32 v130, 0, v130\n"
      "v_max_f32 v157, 0, v157\n"
      "v_cvt_pk_bf16_f32 v130, v130, v157\n"
      "v_max_f32 v131, 0, v131\n"
      "v_max_f32 v133, 0, v133\n"
      "v_cvt_pk_bf16_f32 v131, v131, v133\n"
      "v_max_f32 v197, 0, v197\n"
      "v_max_f32 v156, 0, v156\n"
      "v_cvt_pk_bf16_f32 v197, v197, v156\n"
      "v_max_f32 v196, 0, v196\n"
      "v_max_f32 v147, 0, v147\n"
      "v_cvt_pk_bf16_f32 v196, v196, v147\n"
      "v_max_f32 v194, 0, v194\n"
      "v_max_f32 v146, 0, v146\n"
      "v_cvt_pk_bf16_f32 v194, v194, v146\n"
      "s_mov_b32 s42, 0\n"
      "s_add_u32 m0, s75, 0\n"
      "s_nop 0\n"
      "buffer_load_dwordx4 v106, s[20:23], s42 offen  lds\n"
      "s_pack_ll_b32_b16 s92, s48, s50\n"
      "v_dot2_f32_bf16 v68, s92, v233, 0\n"
      "s_pack_hh_b32_b16 s93, s48, s50\n"
      "v_dot2_f32_bf16 v156, s93, v233, 0\n"
      "s_pack_ll_b32_b16 s94, s49, s51\n"
      "v_dot2_f32_bf16 v232, s94, v233, 0\n"
      "s_pack_hh_b32_b16 s95, s49, s51\n"
      "v_dot2_f32_bf16 v157, s95, v233, 0\n"
      "s_pack_ll_b32_b16 s92, s52, s54\n"
      "v_dot2_f32_bf16 v68, s92, v155, v68\n"
      "s_pack_hh_b32_b16 s93, s52, s54\n"
      "v_dot2_f32_bf16 v156, s93, v155, v156\n"
      "s_pack_ll_b32_b16 s94, s53, s55\n"
      "v_dot2_f32_bf16 v232, s94, v155, v232\n"
      "s_pack_hh_b32_b16 s95, s53, s55\n"
      "v_dot2_f32_bf16 v157, s95, v155, v157\n"
      "s_pack_ll_b32_b16 s92, s56, s58\n"
      "v_dot2_f32_bf16 v68, s92, v235, v68\n"
      "s_pack_hh_b32_b16 s93, s56, s58\n"
      "v_dot2_f32_bf16 v156, s93, v235, v156\n"
      "s_pack_ll_b32_b16 s94, s57, s59\n"
      "v_dot2_f32_bf16 v232, s94, v235, v232\n"
      "s_add_u32 m0, s75, 3968\n"
      "s_nop 0\n"
      "buffer_load_dwordx4 v106, s[20:23], s42 offen offset:128  lds\n"
      "s_pack_hh_b32_b16 s95, s57, s59\n"
      "v_dot2_f32_bf16 v157, s95, v235, v157\n"
      "s_pack_ll_b32_b16 s92, s60, s62\n"
      "v_dot2_f32_bf16 v68, s92, v130, v68\n"
      "s_pack_hh_b32_b16 s93, s60, s62\n"
      "v_dot2_f32_bf16 v156, s93, v130, v156\n"
      "s_pack_ll_b32_b16 s94, s61, s63\n"
      "v_dot2_f32_bf16 v232, s94, v130, v232\n"
      "s_pack_hh_b32_b16 s95, s61, s63\n"
      "v_dot2_f32_bf16 v157, s95, v130, v157\n"
      "s_pack_ll_b32_b16 s92, s24, s26\n"
      "v_dot2_f32_bf16 v68, s92, v131, v68\n"
      "s_pack_hh_b32_b16 s93, s24, s26\n"
      "v_dot2_f32_bf16 v156, s93, v131, v156\n"
      "s_pack_ll_b32_b16 s94, s25, s27\n"
      "v_dot2_f32_bf16 v232, s94, v131, v232\n"
      "s_pack_hh_b32_b16 s95, s25, s27\n"
      "v_dot2_f32_bf16 v157, s95, v131, v157\n"
      "s_pack_ll_b32_b16 s92, s28, s30\n"
      "v_dot2_f32_bf16 v68, s92, v197, v68\n"
      "s_pack_hh_b32_b16 s93, s28, s30\n"
      "v_dot2_f32_bf16 v156, s93, v197, v156\n"
      "s_pack_ll_b32_b16 s94, s29, s31\n"
      "s_add_u32 m0, s75, 7936\n"
      "s_nop 0\n"
      "buffer_load_dwordx4 v106, s[20:23], s42 offen offset:256  lds\n"
      "v_dot2_f32_bf16 v232, s94, v197, v232\n"
      "s_pack_hh_b32_b16 s95, s29, s31\n"
      "v_dot2_f32_bf16 v157, s95, v197, v157\n"
      "s_pack_ll_b32_b16 s92, s32, s34\n"
      "v_dot2_f32_bf16 v68, s92, v196, v68\n"
      "s_pack_hh_b32_b16 s93, s32, s34\n"
      "v_dot2_f32_bf16 v156, s93, v196, v156\n"
      "s_pack_ll_b32_b16 s94, s33, s35\n"
      "v_dot2_f32_bf16 v232, s94, v196, v232\n"
      "s_pack_hh_b32_b16 s95, s33, s35\n"
      "v_dot2_f32_bf16 v157, s95, v196, v157\n"
      "s_pack_ll_b32_b16 s92, s36, s38\n"
      "v_dot2_f32_bf16 v68, s92, v194, v68\n"
      "s_pack_hh_b32_b16 s93, s36, s38\n"
      "v_dot2_f32_bf16 v156, s93, v194, v156\n"
      "s_pack_ll_b32_b16 s94, s37, s39\n"
      "v_dot2_f32_bf16 v232, s94, v194, v232\n"
      "s_pack_hh_b32_b16 s95, s37, s39\n"
      "v_dot2_f32_bf16 v157, s95, v194, v157\n"
      "s_load_dwordx2 s[28:29], s[90:91], 8\n"
      "s_load_dwordx2 s[34:35], s[90:91], 16\n"
      "s_load_dwordx2 s[24:25], s[90:91], 24\n"
      "s_add_u32 m0, s75, 11904\n"
      "s_nop 0\n"
      "buffer_load_dwordx4 v106, s[20:23], s42 offen offset:384  lds\n"
      "s_load_dwordx2 s[36:37], s[90:91], 40\n"
      "s_mov_b32 s26, 100270464\n"
      "s_mov_b32 s27, 0x20000\n"
      "s_mov_b32 s38, 73728\n"
      "s_mov_b32 s39, 0x20000\n"
      "s_mov_b32 s30, 25589860\n"
      "s_mov_b32 s31, 0x20000\n"
      "s_mov_b32 s53, 0x3fb8aa3b\n"
      "s_mov_b32 s51, 0x2a8cbccc\n"
      "s_mov_b32 s52, 0x38fba882\n"
      "s_mov_b32 s58, 0xffff0000\n"
      "s_mov_b32 s59, 0x0\n"
      "s_mov_b32 s60, 0x0\n"
      "s_mov_b32 s61, 0xffff\n"
      "s_mov_b32 s62, 0x0\n"
      "s_mov_b32 s63, 0xffff0000\n"
      "v_mov_b32 v69, 0x41f00000\n"
      "v_cmp_nle_f32 s[54:55], v154, v69\n"
      "v_mul_f32 v154, s53, v154\n"
      "s_nop 0\n"
      "v_readfirstlane_b32 s50, v154\n"
      "v_cvt_pk_bf16_f32 v68, v68, v68\n"
      "v_lshlrev_b32 v68, 16, v68\n"
      "s_add_u32 m0, s75, 15872\n"
      "s_nop 0\n"
      "buffer_load_dwordx4 v106, s[20:23], s42 offen offset:512  lds\n"
      "v_mul_f32 v68, 0xbfb8aa3b, v68\n"
      "v_exp_f32 v68, v68\n"
      "s_nop 0\n"
      "v_add_f32 v68, 1.0, v68\n"
      "v_rcp_f32 v68, v68\n"
      "s_nop 0\n"
      "v_cvt_pk_bf16_f32 v68, v68, v68\n"
      "v_lshlrev_b32 v68, 16, v68\n"
      "v_add_f32 v68, -0.5, v68\n"
      "v_mul_f32 v68, 0x41b8aa3b, v68\n"
      "ds_write_b32 v234, v68\n"
      "v_cvt_pk_bf16_f32 v156, v156, v156\n"
      "v_lshlrev_b32 v156, 16, v156\n"
      "v_mul_f32 v156, 0xbfb8aa3b, v156\n"
      "v_exp_f32 v156, v156\n"
      "s_nop 0\n"
      "v_add_f32 v156, 1.0, v156\n"
      "v_rcp_f32_e64 v156, v156\n"
      "s_nop 0\n"
      "v_cvt_pk_bf16_f32 v156, v156, v156\n"
      "v_lshlrev_b32 v156, 16, v156\n"
      "v_add_f32 v156, -0.5, v156\n"
      "v_mul_f32 v156, 0x41b8aa3b, v156\n"
      "ds_write_b32 v234, v156 offset:704\n"
      "v_cvt_pk_bf16_f32 v232, v232, v232\n"
      "v_lshlrev_b32 v232, 16, v232\n"
      "s_add_u32 m0, s75, 19840\n"
      "s_nop 0\n"
      "buffer_load_dwordx4 v106, s[20:23], s42 offen offset:640  lds\n"
      "v_mul_f32 v232, 0xbfb8aa3b, v232\n"
      "v_exp_f32 v232, v232\n"
      "s_nop 0\n"
      "v_add_f32 v232, 1.0, v232\n"
      "v_rcp_f32_e64 v232, v232\n"
      "s_nop 0\n"
      "v_cvt_pk_bf16_f32 v232, v232, v232\n"
      "v_lshlrev_b32 v232, 16, v232\n"
      "v_add_f32 v232, -0.5, v232\n"
      "v_mul_f32 v232, 0x41b8aa3b, v232\n"
      "ds_write_b32 v234, v232 offset:1408\n"
      "v_cvt_pk_bf16_f32 v157, v157, v157\n"
      "v_lshlrev_b32 v157, 16, v157\n"
      "v_mul_f32 v157, 0xbfb8aa3b, v157\n"
      "v_exp_f32_e64 v157, v157\n"
      "s_nop 0\n"
      "v_add_f32 v157, 1.0, v157\n"
      "v_rcp_f32_e64 v157, v157\n"
      "s_nop 0\n"
      "v_cvt_pk_bf16_f32 v157, v157, v157\n"
      "v_lshlrev_b32_e64 v157, 16, v157\n"
      "v_add_f32_e64 v157, -0.5, v157\n"
      "v_mul_f32 v157, 0x41b8aa3b, v157\n"
      "ds_write_b32 v234, v157 offset:2112\n"
      "s_mul_i32 s76, s40, 704\n"
      "s_add_u32 s76, s76, 122112\n"
      "s_mov_b32 s43, 36864\n"
      "s_add_u32 m0, s75, 24576\n"
      "s_nop 0\n"
      "buffer_load_dwordx4 v106, s[20:23], s43 offen  lds\n"
      "s_add_u32 m0, s75, 28544\n"
      "s_nop 0\n"
      "buffer_load_dwordx4 v106, s[20:23], s43 offen offset:128  lds\n"
      "s_add_u32 m0, s75, 32512\n"
      "s_nop 0\n"
      "buffer_load_dwordx4 v106, s[20:23], s43 offen offset:256  lds\n"
      "s_add_u32 m0, s75, 36480\n"
      "s_nop 0\n"
      "buffer_load_dwordx4 v106, s[20:23], s43 offen offset:384  lds\n"
      "s_add_u32 m0, s75, 40448\n"
      "s_nop 0\n"
      "buffer_load_dwordx4 v106, s[20:23], s43 offen offset:512  lds\n"
      "s_add_u32 m0, s75, 44416\n"
      "s_nop 0\n"
      "buffer_load_dwordx4 v106, s[20:23], s43 offen offset:640  lds\n"
      "s_mov_b32 s45, s46\n"
      "s_lshl_b32 s9, s68, 1\n"
      "s_add_u32 s9, s9, s40\n"
      "s_mul_i32 s9, s9, 640\n"
      "s_mov_b32 s49, s47\n"
      "s_mov_b32 s66, 0\n"
      "s_mov_b32 s67, 0\n"
      "s_mov_b32 s68, 0\n"
      "s_mov_b32 s69, 0\n"
      "s_mov_b32 s70, 0\n"
      "s_mov_b32 s71, 0\n"
      "s_mov_b32 s48, 0\n"
      "s_mov_b32 s33, 0x7fff7fff\n"
      "s_mov_b32 s42, 1\n"
      "s_mov_b32 s43, 5\n"
      "v_readlane_b32 s47, v38, 4\n"
      "s_nop 0\n"
      "s_and_b32 s47, s47, -4\n"
      "s_waitcnt vmcnt(12)\n"
      "v_add_u32 v129, 150784, v0\n"
      "v_add_u32 v187, 150784, v1\n"
      "v_add_u32 v124, 49152, v236\n"
      "v_add_u32 v125, 49152, v237\n"
      "v_add_u32 v188, 49152, v238\n"
      "v_add_u32 v82, 49152, v239\n"
      "v_add_u32 v78, 49152, v208\n"
      "s_waitcnt vmcnt(6)\n"
      "s_waitcnt lgkmcnt(0)\n"
      "s_barrier\n"
      "s_add_u32 m0, s75, 49152\n"
      "s_mov_b32 s78, 73728\n"
      "buffer_load_dwordx4 v106, s[20:23], s78 offen  lds\n"
      "s_add_u32 m0, s75, 53120\n"
      "ds_read_b128 a[216:219], v129\n"
      "ds_read_b64_tr_b16 a[48:49], v238\n"
      "ds_read_b128 a[220:223], v0\n"
      "ds_read_b64_tr_b16 a[2:3], v236 offset:512\n"
      "ds_read_b64_tr_b16 a[72:73], v236 offset:12288\n"
      "ds_read_b64_tr_b16 a[74:75], v236 offset:12800\n"
      "buffer_load_dwordx4 v106, s[20:23], s78 offen offset:128  lds\n"
      "ds_read_b64_tr_b16 a[0:1], v236\n"
      "ds_read_b64_tr_b16 a[50:51], v238 offset:512\n"
      "ds_read_b64_tr_b16 a[24:25], v237\n"
      "v_add_u32 v176, 6272, v66\n"
      "s_add_u32 m0, s75, 57088\n"
      "ds_read_b128 a[224:227], v0 offset:2048\n"
      "ds_read_b64_tr_b16 a[26:27], v237 offset:512\n"
      "buffer_load_dwordx4 v106, s[20:23], s78 offen offset:256  lds\n"
      "ds_read2_b32 v[68:69], v176 offset0:16 offset1:17\n"
      "ds_read_b32 v152, v115 offset:3328\n"
      "ds_read_b128 a[244:247], v1 offset:4096\n"
      "ds_read_b64_tr_b16 a[120:121], v238 offset:12288\n"
      "s_add_u32 m0, s75, 61056\n"
      "ds_read_b64_tr_b16 a[122:123], v238 offset:12800\n"
      "s_waitcnt lgkmcnt(8)\n"
      "v_mfma_f32_16x16x32_bf16 v[232:235], a[48:51], a[216:219], 0\n"
      "buffer_load_dwordx4 v106, s[20:23], s78 offen offset:384  lds\n"
      "ds_read_b64_tr_b16 a[98:99], v237 offset:12800\n"
      "v_mfma_f32_16x16x32_bf16 v[154:157], a[0:3], a[220:223], 0\n"
      "s_mov_b32 s79, 110592\n"
      "v_add_u32 v75, 3136, v66\n"
      "s_waitcnt lgkmcnt(6)\n"
      "v_mfma_f32_16x16x32_bf16 v[130:133], a[24:27], a[224:227], 0\n"
      "s_add_u32 m0, s75, 65024\n"
      "ds_read_b128 a[228:231], v0 offset:4096\n"
      "ds_read_b64_tr_b16 a[96:97], v237 offset:12288\n"
      "v_mfma_f32_16x16x32_bf16 v[194:197], a[24:27], a[220:223], 0\n"
      "buffer_load_dwordx4 v106, s[20:23], s78 offen offset:512  lds\n"
      "v_mfma_f32_16x16x32_bf16 v[144:147], a[48:51], a[224:227], 0\n"
      "ds_read_b128 a[236:239], v1\n"
      "ds_read2_b32 v[58:59], v75 offset0:18 offset1:19\n"
      "v_mfma_f32_16x16x32_bf16 v[108:111], a[0:3], a[224:227], 0\n"
      "ds_read_b128 a[232:235], v187\n"
      "s_add_u32 m0, s75, 68992\n"
      "ds_read2_b32 v[70:71], v176 offset0:18 offset1:19\n"
      "v_mfma_f32_16x16x32_bf16 v[180:183], a[72:75], a[220:223], 0\n"
      "buffer_load_dwordx4 v106, s[20:23], s78 offen offset:640  lds\n"
      "v_mfma_f32_16x16x32_bf16 v[244:247], a[0:3], a[216:219], 0\n"
      "ds_read_b128 a[252:255], v0 offset:6144\n"
      "s_waitcnt lgkmcnt(5)\n"
      "v_mfma_f32_16x16x32_bf16 v[240:243], a[96:99], a[224:227], 0\n"
      "ds_read2_b32 v[72:73], v176 offset0:32 offset1:33\n"
      "ds_read_b128 a[240:243], v1 offset:2048\n"
      "v_mfma_f32_16x16x32_bf16 v[252:255], a[96:99], a[228:231], 0\n"
      "s_waitcnt vmcnt(6)\n"
      "s_barrier\n"
      "global_load_dwordx4 v[4:7], v141, s[88:89] offset:3072\n"
      "ds_read_b64_tr_b16 a[4:5], v236 offset:24576\n"
      "ds_read_b64_tr_b16 a[28:29], v237 offset:24576\n"
      "s_add_u32 s88, s88, 4096\n"
      "s_addc_u32 s89, s89, 0\n"
      "ds_read_b64_tr_b16 a[54:55], v238 offset:25088\n"
      "ds_read_b64_tr_b16 a[124:125], v238 offset:36864\n"
      "ds_read_b64_tr_b16 a[126:127], v238 offset:37376\n"
      "ds_read_b64_tr_b16 a[100:101], v237 offset:36864\n"
      "v_mfma_f32_16x16x32_bf16 v[166:169], a[72:75], a[224:227], 0\n"
      "ds_read_b64_tr_b16 a[102:103], v237 offset:37376\n"
      "global_load_dwordx4 v[8:11], v141, s[88:89]\n"
      "ds_read_b64_tr_b16 a[52:53], v238 offset:24576\n"
      "ds_read_b64_tr_b16 a[76:77], v236 offset:36864\n"
      "v_mfma_f32_16x16x32_bf16 v[120:123], a[120:123], a[224:227], 0\n"
      "ds_read_b64_tr_b16 a[30:31], v237 offset:25088\n"
      "ds_read_b64_tr_b16 a[78:79], v236 offset:37376\n"
      "ds_read_b64_tr_b16 a[6:7], v236 offset:25088\n"
      "v_mfma_f32_16x16x32_bf16 v[224:227], a[96:99], a[216:219], 0\n"
      "global_load_dwordx4 v[12:15], v141, s[88:89] offset:1024\n"
      "v_mfma_f32_16x16x32_bf16 v[248:251], a[120:123], a[216:219], 0\n"
      "ds_read2_b32 v[50:51], v66 offset0:34 offset1:35\n"
      "ds_read2_b32 v[52:53], v75 offset0:0 offset1:1\n"
      "ds_read_b32 v151, v115 offset:192\n"
      "v_mfma_f32_16x16x32_bf16 v[216:219], a[72:75], a[216:219], 0\n"
      "ds_read_b128 a[224:227], v187 offset:4096\n"
      "v_lshl_add_u32 v152, v152, 2, s76\n"
      "v_mfma_f32_16x16x32_bf16 v[100:103], a[0:3], a[228:231], 0\n"
      "global_load_dwordx4 v[20:23], v141, s[88:89] offset:2048\n"
      "ds_read_b32 v179, v171 offset:9408\n"
      "v_mfma_f32_16x16x32_bf16 v[170:173], a[120:123], a[220:223], 0\n"
      "ds_read2_b32 v[42:43], v66 offset0:2 offset1:3\n"
      "s_waitcnt lgkmcnt(6)\n"
      "v_mfma_f32_16x16x32_bf16 v[170:173], a[124:127], a[236:239], v[170:173]\n"
      "ds_read2_b32 v[62:63], v75 offset0:34 offset1:35\n"
      "ds_read_b128 a[248:251], v129 offset:4096\n"
      "v_mfma_f32_16x16x32_bf16 v[190:193], a[96:99], a[220:223], 0\n"
      "global_load_dwordx4 v[24:27], v141, s[88:89] offset:3072\n"
      "v_mfma_f32_16x16x32_bf16 v[138:141], a[48:51], a[220:223], 0\n"
      "ds_read2_b32 v[44:45], v66 offset0:16 offset1:17\n"
      "s_mov_b32 s82, 0xffff\n"
      "ds_read2_b32 v[46:47], v66 offset0:18 offset1:19\n"
      "v_mfma_f32_16x16x32_bf16 v[138:141], a[52:55], a[236:239], v[138:141]\n"
      "ds_read2_b32 v[60:61], v75 offset0:32 offset1:33\n"
      "v_lshl_add_u32 v73, v73, 2, s76\n"
      "s_add_u32 m0, s75, 73728\n"
      "v_mfma_f32_16x16x32_bf16 v[190:193], a[100:103], a[236:239], v[190:193]\n"
      "buffer_load_dwordx4 v106, s[20:23], s79 offen  lds\n"
      "v_mfma_f32_16x16x32_bf16 v[204:207], a[24:27], a[216:219], 0\n"
      "s_add_u32 m0, s75, 77696\n"
      "ds_read2_b32 v[54:55], v75 offset0:2 offset1:3\n"
      "v_mfma_f32_16x16x32_bf16 v[154:157], a[4:7], a[236:239], v[154:157]\n"
      "ds_read2_b32 v[48:49], v66 offset0:32 offset1:33\n"
      "s_mov_b32 s78, 147456\n"
      "v_mfma_f32_16x16x32_bf16 v[194:197], a[28:31], a[236:239], v[194:197]\n"
      "ds_read2_b32 v[56:57], v75 offset0:16 offset1:17\n"
      "ds_read2_b32 v[74:75], v176 offset0:34 offset1:35\n"
      "v_mfma_f32_16x16x32_bf16 v[232:235], a[52:55], a[232:235], v[232:235]\n"
      "buffer_load_dwordx4 v106, s[20:23], s79 offen offset:128  lds\n"
      "v_mfma_f32_16x16x32_bf16 v[204:207], a[28:31], a[232:235], v[204:207]\n"
      "ds_read_b32 v177, v115 offset:6464\n"
      "v_lshl_add_u32 v70, v70, 2, s76\n"
      "v_mfma_f32_16x16x32_bf16 v[112:115], a[72:75], a[228:231], 0\n"
      "ds_read2_b32 v[64:65], v176 offset0:0 offset1:1\n"
      "s_add_u32 m0, s75, 81664\n"
      "v_mfma_f32_16x16x32_bf16 v[108:111], a[4:7], a[240:243], v[108:111]\n"
      "ds_read_b128 a[216:219], v0 offset:8192\n"
      "s_mov_b32 s91, 0x1ffff\n"
      "v_mfma_f32_16x16x32_bf16 v[244:247], a[4:7], a[232:235], v[244:247]\n"
      "buffer_load_dwordx4 v106, s[20:23], s79 offen offset:256  lds\n"
      "v_mfma_f32_16x16x32_bf16 v[240:243], a[100:103], a[240:243], v[240:243]\n"
      "s_mov_b32 s83, 0x0\n"
      "s_waitcnt lgkmcnt(4)\n"
      "v_lshl_add_u32 v63, v63, 2, s76\n"
      "v_mfma_f32_16x16x32_bf16 v[216:219], a[76:79], a[232:235], v[216:219]\n"
      "s_mov_b32 s90, 0xffffffff\n"
      "v_lshl_add_u32 v179, v179, 2, s76\n"
      "v_lshl_add_u32 v46, v46, 2, s76\n"
      "v_mfma_f32_16x16x32_bf16 v[180:183], a[76:79], a[236:239], v[180:183]\n"
      "ds_read_b128 a[236:239], v1 offset:10240\n"
      "v_lshl_add_u32 v54, v54, 2, s76\n"
      "s_add_u32 m0, s75, 85632\n"
      "v_mfma_f32_16x16x32_bf16 v[212:215], a[120:123], a[228:231], 0\n"
      "buffer_load_dwordx4 v106, s[20:23], s79 offen offset:384  lds\n"
      "s_add_u32 m0, s75, 89600\n"
      "v_mfma_f32_16x16x32_bf16 v[212:215], a[124:127], a[244:247], v[212:215]\n"
      "s_waitcnt vmcnt(9)\n"
      "s_barrier\n"
      "ds_read_b64_tr_b16 a[8:9], v124\n"
      "ds_read_b64_tr_b16 a[104:105], v125 offset:12288\n"
      "ds_read_b64_tr_b16 a[128:129], v188 offset:12288\n"
      "ds_read_b64_tr_b16 a[10:11], v124 offset:512\n"
      "ds_read_b64_tr_b16 a[32:33], v125\n"
      "v_mfma_f32_16x16x32_bf16 v[116:119], a[48:51], a[228:231], 0\n"
      "ds_read_b64_tr_b16 a[56:57], v188\n"
      "ds_read_b64_tr_b16 a[34:35], v125 offset:512\n"
      "buffer_load_dwordx4 v106, s[20:23], s79 offen offset:512  lds\n"
      "ds_read_b64_tr_b16 a[80:81], v124 offset:12288\n"
      "v_mfma_f32_16x16x32_bf16 v[130:133], a[28:31], a[240:243], v[130:133]\n"
      "ds_read_b64_tr_b16 a[130:131], v188 offset:12800\n"
      "ds_read_b64_tr_b16 a[58:59], v188 offset:512\n"
      "s_add_u32 m0, s75, 93568\n"
      "ds_read_b64_tr_b16 a[106:107], v125 offset:12800\n"
      "ds_read_b64_tr_b16 a[82:83], v124 offset:12800\n"
      "buffer_load_dwordx4 v106, s[20:23], s79 offen offset:640  lds\n"
      "v_lshl_add_u32 v49, v49, 2, s76\n"
      "v_lshl_add_u32 v55, v55, 2, s76\n"
      "v_mfma_f32_16x16x32_bf16 v[96:99], a[24:27], a[228:231], 0\n"
      "ds_read_b32 v179, v179\n"
      "v_lshl_add_u32 v50, v50, 2, s76\n"
      "v_mfma_f32_16x16x32_bf16 v[224:227], a[100:103], a[232:235], v[224:227]\n"
      "ds_read2_b32 v[40:41], v66 offset0:0 offset1:1\n"
      "s_add_u32 m0, s75, 0\n"
      "v_lshl_add_u32 v71, v71, 2, s76\n"
      "v_mfma_f32_16x16x32_bf16 v[252:255], a[100:103], a[244:247], v[252:255]\n"
      "buffer_load_dwordx4 v106, s[20:23], s78 offen  lds\n"
      "v_lshl_add_u32 v151, v151, 2, s76\n"
      "v_mfma_f32_16x16x32_bf16 v[166:169], a[76:79], a[240:243], v[166:169]\n"
      "s_add_u32 m0, s75, 3968\n"
      "v_lshl_add_u32 v61, v61, 2, s76\n"
      "v_lshl_add_u32 v58, v58, 2, s76\n"
      "v_mfma_f32_16x16x32_bf16 v[144:147], a[52:55], a[240:243], v[144:147]\n"
      "ds_read_b128 a[220:223], v0 offset:10240\n"
      "s_waitcnt lgkmcnt(3)\n"
      "v_lshl_add_u32 v65, v65, 2, s76\n"
      "v_lshl_add_u32 v72, v72, 2, s76\n"
      "v_mfma_f32_16x16x32_bf16 v[248:251], a[124:127], a[232:235], v[248:251]\n"
      "buffer_load_dwordx4 v106, s[20:23], s78 offen offset:128  lds\n"
      "v_mfma_f32_16x16x32_bf16 v[248:251], a[128:131], a[248:251], v[248:251]\n"
      "ds_read_b128 a[228:231], v1 offset:6144\n"
      "v_lshl_add_u32 v69, v69, 2, s76\n"
      "v_mfma_f32_16x16x32_bf16 v[108:111], a[8:11], a[216:219], v[108:111]\n"
      "ds_read2_b32 v[66:67], v176 offset0:2 offset1:3\n"
      "v_lshl_add_u32 v43, v43, 2, s76\n"
      "v_mfma_f32_16x16x32_bf16 v[120:123], a[124:127], a[240:243], v[120:123]\n"
      "s_add_u32 m0, s75, 7936\n"
      "ds_read_b32 v73, v73\n"
      "v_mfma_f32_16x16x32_bf16 v[100:103], a[4:7], a[244:247], v[100:103]\n"
      "buffer_load_dwordx4 v106, s[20:23], s78 offen offset:256  lds\n"
      "v_mfma_f32_16x16x32_bf16 v[190:193], a[104:107], a[252:255], v[190:193]\n"
      "ds_read_b32 v72, v72\n"
      "s_waitcnt lgkmcnt(4)\n"
      "v_mfma_f32_16x16x32_bf16 v[212:215], a[128:131], a[220:223], v[212:215]\n"
      "ds_read_b128 a[232:235], v1 offset:8192\n"
      "v_lshl_add_u32 v41, v41, 2, s76\n"
      "v_mfma_f32_16x16x32_bf16 v[138:141], a[56:59], a[252:255], v[138:141]\n"
      "s_add_u32 m0, s75, 11904\n"
      "ds_read_b32 v50, v50\n"
      "v_lshl_add_u32 v51, v51, 2, s76\n"
      "v_mfma_f32_16x16x32_bf16 v[112:115], a[76:79], a[244:247], v[112:115]\n"
      "buffer_load_dwordx4 v106, s[20:23], s78 offen offset:384  lds\n"
      "v_mfma_f32_16x16x32_bf16 v[240:243], a[104:107], a[216:219], v[240:243]\n"
      "s_add_u32 m0, s75, 15872\n"
      "ds_read_b32 v71, v71\n"
      "v_mfma_f32_16x16x32_bf16 v[224:227], a[104:107], a[248:251], v[224:227]\n"
      "ds_read_b32 v61, v61\n"
      "v_lshl_add_u32 v47, v47, 2, s76\n"
      "v_mfma_f32_16x16x32_bf16 v[166:169], a[80:83], a[216:219], v[166:169]\n"
      "ds_read_b128 a[240:243], v129 offset:8192\n"
      "v_readlane_b32 s86, v38, s43\n"
      "v_mfma_f32_16x16x32_bf16 v[144:147], a[56:59], a[216:219], v[144:147]\n"
      "buffer_load_dwordx4 v106, s[20:23], s78 offen offset:512  lds\n"
      "v_lshl_add_u32 v42, v42, 2, s76\n"
      "s_mov_b32 s79, 184320\n"
      "v_mfma_f32_16x16x32_bf16 v[204:207], a[32:35], a[248:251], v[204:207]\n"
      "s_add_u32 m0, s75, 19840\n"
      "v_lshl_add_u32 v40, v40, 2, s76\n"
      "v_lshl_add_u32 v75, v75, 2, s76\n"
      "v_mov_b32 v134, 0xf149f2ca\n"
      "v_mfma_f32_16x16x32_bf16 v[112:115], a[80:83], a[220:223], v[112:115]\n"
      "ds_read_b32 v69, v69\n"
      "v_lshl_add_u32 v48, v48, 2, s76\n"
      "v_lshl_add_u32 v44, v44, 2, s76\n"
      "v_mfma_f32_16x16x32_bf16 v[154:157], a[8:11], a[252:255], v[154:157]\n"
      "buffer_load_dwordx4 v106, s[20:23], s78 offen offset:640  lds\n"
      "v_mfma_f32_16x16x32_bf16 v[194:197], a[32:35], a[252:255], v[194:197]\n"
      "s_add_u32 m0, s75, 24576\n"
      "v_lshl_add_u32 v64, v64, 2, s76\n"
      "v_lshl_add_u32 v177, v177, 2, s76\n"
      "v_mfma_f32_16x16x32_bf16 v[232:235], a[56:59], a[248:251], v[232:235]\n"
      "s_waitcnt vmcnt(6)\n"
      "s_barrier\n"
      "ds_read_b64_tr_b16 a[108:109], v125 offset:36864\n"
      "ds_read_b64_tr_b16 a[132:133], v188 offset:36864\n"
      "v_mfma_f32_16x16x32_bf16 v[244:247], a[8:11], a[248:251], v[244:247]\n"
      "ds_read_b64_tr_b16 a[110:111], v125 offset:37376\n"
      "ds_read_b64_tr_b16 a[38:39], v125 offset:25088\n"
      "v_mfma_f32_16x16x32_bf16 v[116:119], a[52:55], a[244:247], v[116:119]\n"
      "ds_read_b64_tr_b16 a[134:135], v188 offset:37376\n"
      "ds_read_b64_tr_b16 a[84:85], v124 offset:36864\n"
      "ds_read_b64_tr_b16 a[12:13], v124 offset:24576\n"
      "ds_read_b64_tr_b16 a[36:37], v125 offset:24576\n"
      "ds_read_b64_tr_b16 a[60:61], v188 offset:24576\n"
      "v_mfma_f32_16x16x32_bf16 v[116:119], a[56:59], a[220:223], v[116:119]\n"
      "ds_read_b64_tr_b16 a[14:15], v124 offset:25088\n"
      "ds_read_b64_tr_b16 a[62:63], v188 offset:25088\n"
      "ds_read_b64_tr_b16 a[86:87], v124 offset:37376\n"
      "v_mfma_f32_16x16x32_bf16 v[216:219], a[80:83], a[248:251], v[216:219]\n"
      "buffer_load_dwordx4 v106, s[20:23], s79 offen  lds\n"
      "v_mfma_f32_16x16x32_bf16 v[96:99], a[28:31], a[244:247], v[96:99]\n"
      "ds_read_b32 v43, v43\n"
      "s_add_u32 m0, s75, 28544\n"
      "v_lshl_add_u32 v45, v45, 2, s76\n"
      "v_mfma_f32_16x16x32_bf16 v[96:99], a[32:35], a[220:223], v[96:99]\n"
      "ds_read_b128 a[244:247], v0 offset:12288\n"
      "v_lshl_add_u32 v56, v56, 2, s76\n"
      "v_mfma_f32_16x16x32_bf16 v[130:133], a[32:35], a[216:219], v[130:133]\n"
      "ds_read_b32 v65, v65\n"
      "buffer_load_dwordx4 v106, s[20:23], s79 offen offset:128  lds\n"
      "s_mov_b32 s78, 0\n"
      "v_mfma_f32_16x16x32_bf16 v[120:123], a[128:131], a[216:219], v[120:123]\n"
      "s_add_u32 m0, s75, 32512\n"
      "v_lshl_add_u32 v52, v52, 2, s76\n"
      "s_waitcnt lgkmcnt(3)\n"
      "v_mfma_f32_16x16x32_bf16 v[144:147], a[60:63], a[232:235], v[144:147]\n"
      "ds_read_b32 v151, v151\n"
      "ds_read_b32 v40, v40\n"
      "v_lshl_add_u32 v60, v60, 2, s76\n"
      "v_mfma_f32_16x16x32_bf16 v[170:173], a[128:131], a[252:255], v[170:173]\n"
      "buffer_load_dwordx4 v106, s[20:23], s79 offen offset:256  lds\n"
      "v_mfma_f32_16x16x32_bf16 v[180:183], a[80:83], a[252:255], v[180:183]\n"
      "ds_read_b32 v44, v44\n"
      "v_lshl_add_u32 v67, v67, 2, s76\n"
      "v_mfma_f32_16x16x32_bf16 v[180:183], a[84:87], a[228:231], v[180:183]\n"
      "ds_read_b128 a[216:219], v187 offset:8192\n"
      "v_lshl_add_u32 v74, v74, 2, s76\n"
      "v_mfma_f32_16x16x32_bf16 v[252:255], a[104:107], a[220:223], v[252:255]\n"
      "ds_read_b32 v48, v48\n"
      "s_add_u32 m0, s75, 36480\n"
      "v_mfma_f32_16x16x32_bf16 v[252:255], a[108:111], a[236:239], v[252:255]\n"
      "buffer_load_dwordx4 v106, s[20:23], s79 offen offset:384  lds\n"
      "v_cndmask_b32 v30, v134, v179, s[90:91]\n"
      "v_mfma_f32_16x16x32_bf16 v[212:215], a[132:135], a[236:239], v[212:215]\n"
      "ds_read_b32 v60, v60\n"
      "v_lshl_add_u32 v66, v66, 2, s76\n"
      "v_mfma_f32_16x16x32_bf16 v[112:115], a[84:87], a[236:239], v[112:115]\n"
      "s_add_u32 m0, s75, 40448\n"
      "ds_read_b32 v75, v75\n"
      "v_mfma_f32_16x16x32_bf16 v[244:247], a[12:15], a[224:227], v[244:247]\n"
      "ds_read_b32 v177, v177\n"
      "v_lshl_add_u32 v57, v57, 2, s76\n"
      "v_mfma_f32_16x16x32_bf16 v[100:103], a[8:11], a[220:223], v[100:103]\n"
      "buffer_load_dwordx4 v106, s[20:23], s79 offen offset:512  lds\n"
      "v_mfma_f32_16x16x32_bf16 v[116:119], a[60:63], a[236:239], v[116:119]\n"
      "ds_read_b128 a[248:251], v0 offset:14336\n"
      "s_add_u32 m0, s75, 44416\n"
      "ds_read_b32 v45, v45\n"
      "v_mfma_f32_16x16x32_bf16 v[240:243], a[108:111], a[232:235], v[240:243]\n"
      "ds_read_b32 v64, v64\n"
      "ds_read_b32 v70, v70\n"
      "v_mfma_f32_16x16x32_bf16 v[204:207], a[36:39], a[224:227], v[204:207]\n"
      "buffer_load_dwordx4 v106, s[20:23], s79 offen offset:640  lds\n"
      "v_mfma_f32_16x16x32_bf16 v[248:251], a[132:135], a[224:227], v[248:251]\n"
      "ds_read_b128 a[252:255], v0 offset:16384\n"
      "s_add_u32 m0, s75, 48384\n"
      "v_mfma_f32_16x16x32_bf16 v[170:173], a[132:135], a[228:231], v[170:173]\n"
      "ds_read_b32 v152, v152\n"
      "v_lshl_add_u32 v53, v53, 2, s76\n"
      "v_mfma_f32_16x16x32_bf16 v[96:99], a[36:39], a[236:239], v[96:99]\n"
      "ds_read_b32 v63, v63\n"
      "ds_read_b32 v53, v53\n"
      "v_mfma_f32_16x16x32_bf16 v[100:103], a[12:15], a[236:239], v[100:103]\n"
      "buffer_load_dwordx4 v106, s[20:23], s78 offen offset:768  lds\n"
      "v_mfma_f32_16x16x32_bf16 v[194:197], a[36:39], a[228:231], v[194:197]\n"
      "s_add_u32 m0, s75, 52352\n"
      "s_waitcnt vmcnt(7)\n"
      "s_barrier\n"
      "ds_read_b64_tr_b16 a[136:137], v238 offset:12288\n"
      "v_mfma_f32_16x16x32_bf16 v[224:227], a[108:111], a[224:227], v[224:227]\n"
      "ds_read_b64_tr_b16 a[88:89], v236 offset:12288\n"
      "ds_read_b64_tr_b16 a[66:67], v238 offset:512\n"
      "ds_read_b64_tr_b16 a[90:91], v236 offset:12800\n"
      "v_mfma_f32_16x16x32_bf16 v[154:157], a[12:15], a[228:231], v[154:157]\n"
      "ds_read_b64_tr_b16 a[16:17], v236\n"
      "ds_read_b64_tr_b16 a[18:19], v236 offset:512\n"
      "v_mfma_f32_16x16x32_bf16 v[232:235], a[60:63], a[224:227], v[232:235]\n"
      "buffer_load_dwordx4 v106, s[20:23], s78 offen offset:896  lds\n"
      "v_mfma_f32_16x16x32_bf16 v[108:111], a[12:15], a[232:235], v[108:111]\n"
      "ds_read_b64_tr_b16 a[138:139], v238 offset:12800\n"
      "ds_read_b64_tr_b16 a[42:43], v237 offset:512\n"
      "ds_read_b64_tr_b16 a[64:65], v238\n"
      "v_mfma_f32_16x16x32_bf16 v[120:123], a[132:135], a[232:235], v[120:123]\n"
      "ds_read_b64_tr_b16 a[112:113], v237 offset:12288\n"
      "ds_read_b64_tr_b16 a[114:115], v237 offset:12800\n"
      "v_mfma_f32_16x16x32_bf16 v[130:133], a[36:39], a[232:235], v[130:133]\n"
      "ds_read_b64_tr_b16 a[40:41], v237\n"
      "s_add_u32 m0, s75, 56320\n"
      "v_mfma_f32_16x16x32_bf16 v[166:169], a[84:87], a[232:235], v[166:169]\n"
      "buffer_load_dwordx4 v106, s[20:23], s78 offen offset:1024  lds\n"
      "v_mfma_f32_16x16x32_bf16 v[216:219], a[84:87], a[224:227], v[216:219]\n"
      "ds_read_b32 v67, v67\n"
      "s_waitcnt lgkmcnt(7)\n"
      "v_mfma_f32_16x16x32_bf16 v[216:219], a[88:91], a[240:243], v[216:219]\n"
      "ds_read_b32 v47, v47\n"
      "v_readlane_b32 s84, v37, s43\n"
      "v_mfma_f32_16x16x32_bf16 v[138:141], a[60:63], a[228:231], v[138:141]\n"
      "s_mov_b32 s78, 36864\n"
      "v_lshl_add_u32 v68, v68, 2, s76\n"
      "s_add_u32 m0, s75, 60672\n"
      "v_mfma_f32_16x16x32_bf16 v[108:111], a[16:19], a[248:251], v[108:111]\n"
      "buffer_load_dwordx4 v106, s[20:23], s78 offen offset:768  lds\n"
      "s_waitcnt lgkmcnt(2)\n"
      "v_mfma_f32_16x16x32_bf16 v[252:255], a[112:115], a[252:255], v[252:255]\n"
      "s_add_u32 m0, s75, 64640\n"
      "ds_read_b128 a[232:235], v129 offset:2048\n"
      "s_mov_b32 s79, 73728\n"
      "v_mfma_f32_16x16x32_bf16 v[170:173], a[136:139], a[244:247], v[170:173]\n"
      "ds_read_b32 v46, v46\n"
      "ds_read_b32 v54, v54\n"
      "v_mfma_f32_16x16x32_bf16 v[232:235], a[64:67], a[240:243], v[232:235]\n"
      "buffer_load_dwordx4 v106, s[20:23], s78 offen offset:896  lds\n"
      "v_mfma_f32_16x16x32_bf16 v[180:183], a[88:91], a[244:247], v[180:183]\n"
      "s_add_u32 m0, s75, 68608\n"
      "ds_read_b32 v68, v68\n"
      "v_mfma_f32_16x16x32_bf16 v[190:193], a[108:111], a[228:231], v[190:193]\n"
      "ds_read_b32 v58, v58\n"
      "ds_read_b32 v41, v41\n"
      "ds_read_b128 a[236:239], v187 offset:2048\n"
      "v_mfma_f32_16x16x32_bf16 v[212:215], a[136:139], a[252:255], v[212:215]\n"
      "buffer_load_dwordx4 v106, s[20:23], s78 offen offset:1024  lds\n"
      "v_mfma_f32_16x16x32_bf16 v[244:247], a[16:19], a[240:243], v[244:247]\n"
      "ds_read_b32 v49, v49\n"
      "v_lshl_add_u32 v62, v62, 2, s76\n"
      "v_mfma_f32_16x16x32_bf16 v[248:251], a[136:139], a[240:243], v[248:251]\n"
      "ds_read_b32 v57, v57\n"
      "v_cndmask_b32 v28, v134, v177, s[82:83]\n"
      "v_mfma_f32_16x16x32_bf16 v[154:157], a[16:19], a[244:247], v[154:157]\n"
      "ds_read_b32 v52, v52\n"
      "s_add_u32 m0, s75, 72960\n"
      "v_mfma_f32_16x16x32_bf16 v[112:115], a[88:91], a[252:255], v[112:115]\n"
      "buffer_load_dwordx4 v106, s[20:23], s79 offen offset:768  lds\n"
      "s_waitcnt lgkmcnt(4)\n"
      "v_mfma_f32_16x16x32_bf16 v[176:179], a[72:75], a[232:235], 0\n"
      "ds_read_b32 v55, v55\n"
      "v_readlane_b32 s85, v37, s42\n"
      "v_mfma_f32_16x16x32_bf16 v[138:141], a[64:67], a[244:247], v[138:141]\n"
      "ds_read_b32 v42, v42\n"
      "ds_read_b32 v74, v74\n"
      "v_mfma_f32_16x16x32_bf16 v[194:197], a[40:43], a[244:247], v[194:197]\n"
      "s_add_u32 m0, s75, 76928\n"
      "ds_read_b32 v56, v56\n"
      "v_mfma_f32_16x16x32_bf16 v[130:133], a[40:43], a[248:251], v[130:133]\n"
      "buffer_load_dwordx4 v106, s[20:23], s79 offen offset:896  lds\n"
      "v_mfma_f32_16x16x32_bf16 v[116:119], a[64:67], a[252:255], v[116:119]\n"
      "ds_read_b128 a[224:227], v1 offset:14336\n"
      "s_add_u32 m0, s75, 80896\n"
      "ds_read_b128 a[228:231], v1 offset:16384\n"
      "v_mfma_f32_16x16x32_bf16 v[96:99], a[40:43], a[252:255], v[96:99]\n"
      "ds_read_b32 v66, v66\n"
      "ds_read_b128 a[220:223], v1 offset:12288\n"
      "v_mfma_f32_16x16x32_bf16 v[100:103], a[16:19], a[252:255], v[100:103]\n"
      "buffer_load_dwordx4 v106, s[20:23], s79 offen offset:1024  lds\n"
      "v_mfma_f32_16x16x32_bf16 v[166:169], a[88:91], a[248:251], v[166:169]\n"
      "s_waitcnt vmcnt(9)\n"
      "s_barrier\n"
      "ds_read_b64_tr_b16 a[94:95], v236 offset:37376\n"
      "v_mfma_f32_16x16x32_bf16 v[190:193], a[112:115], a[244:247], v[190:193]\n"
      "ds_read_b64_tr_b16 a[20:21], v236 offset:24576\n"
      "ds_read_b64_tr_b16 a[92:93], v236 offset:36864\n"
      "v_mfma_f32_16x16x32_bf16 v[158:161], a[0:3], a[232:235], 0\n"
      "ds_read_b64_tr_b16 a[22:23], v236 offset:25088\n"
      "s_mov_b32 s79, 110592\n"
      "v_mfma_f32_16x16x32_bf16 v[224:227], a[112:115], a[240:243], v[224:227]\n"
      "ds_read_b64_tr_b16 a[116:117], v237 offset:36864\n"
      "ds_read_b64_tr_b16 a[68:69], v238 offset:24576\n"
      "v_mfma_f32_16x16x32_bf16 v[144:147], a[64:67], a[248:251], v[144:147]\n"
      "s_add_u32 m0, s75, 85248\n"
      "ds_read_b64_tr_b16 a[44:45], v237 offset:24576\n"
      "v_mfma_f32_16x16x32_bf16 v[204:207], a[40:43], a[240:243], v[204:207]\n"
      "ds_read_b64_tr_b16 a[70:71], v238 offset:25088\n"
      "ds_read_b64_tr_b16 a[140:141], v238 offset:36864\n"
      "v_mfma_f32_16x16x32_bf16 v[162:165], a[48:51], a[232:235], 0\n"
      "buffer_load_dwordx4 v106, s[20:23], s79 offen offset:768  lds\n"
      "v_mfma_f32_16x16x32_bf16 v[120:123], a[136:139], a[248:251], v[120:123]\n"
      "ds_read_b64_tr_b16 a[46:47], v237 offset:25088\n"
      "s_waitcnt lgkmcnt(6)\n"
      "v_mfma_f32_16x16x32_bf16 v[154:157], a[20:23], a[220:223], v[154:157]\n"
      "s_add_u32 m0, s75, 89216\n"
      "ds_read_b64_tr_b16 a[118:119], v237 offset:37376\n"
      "v_mfma_f32_16x16x32_bf16 v[244:247], a[20:23], a[216:219], v[244:247]\n"
      "ds_read_b64_tr_b16 a[142:143], v238 offset:37376\n"
      "v_lshl_add_u32 v59, v59, 2, s76\n"
      "v_mfma_f32_16x16x32_bf16 v[216:219], a[92:95], a[216:219], v[216:219]\n"
      "buffer_load_dwordx4 v106, s[20:23], s79 offen offset:896  lds\n"
      "v_mfma_f32_16x16x32_bf16 v[112:115], a[92:95], a[228:231], v[112:115]\n"
      "s_add_u32 m0, s75, 93184\n"
      "ds_read_b128 a[240:243], v129 offset:6144\n"
      "v_mfma_f32_16x16x32_bf16 v[166:169], a[92:95], a[224:227], v[166:169]\n"
      "ds_read_b128 a[244:247], v187 offset:6144\n"
      "v_mul_f32_e64 v137, v245, v245\n"
      "v_mfma_f32_16x16x32_bf16 v[176:179], a[76:79], a[236:239], v[176:179]\n"
      "s_mov_b32 s78, 147456\n"
      "v_mul_f32_e64 v201, v217, v217\n"
      "v_fma_f32 v136, v244, v244, s51\n"
      "v_mfma_f32_16x16x32_bf16 v[180:183], a[92:95], a[220:223], v[180:183]\n"
      "buffer_load_dwordx4 v106, s[20:23], s79 offen offset:1024  lds\n"
      "v_mfma_f32_16x16x32_bf16 v[100:103], a[20:23], a[228:231], v[100:103]\n"
      "s_waitcnt lgkmcnt(2)\n"
      "v_fma_f32 v200, v216, v216, s51\n"
      "s_add_u32 m0, s75, -768\n"
      "v_mfma_f32_16x16x32_bf16 v[204:207], a[44:47], a[216:219], v[204:207]\n"
      "ds_read_b32 v51, v51\n"
      "v_cndmask_b32 v26, v134, v152, s[82:83]\n"
      "v_mfma_f32_16x16x32_bf16 v[120:123], a[140:143], a[224:227], v[120:123]\n"
      "s_waitcnt lgkmcnt(2)\n"
      "v_cndmask_b32 v24, v134, v151, s[82:83]\n"
      "v_mul_f32_e64 v135, v167, v167\n"
      "v_mfma_f32_16x16x32_bf16 v[108:111], a[20:23], a[224:227], v[108:111]\n"
      "buffer_load_dwordx4 v106, s[20:23], s78 offen offset:768  lds\n"
      "v_mfma_f32_16x16x32_bf16 v[158:161], a[4:7], a[236:239], v[158:161]\n"
      "ds_read_b32 v59, v59\n"
      "v_fma_f32 v134, v166, v166, s51\n"
      "v_mfma_f32_16x16x32_bf16 v[158:161], a[8:11], a[240:243], v[158:161]\n"
      "s_lshl_b32 s92, s40, 6\n"
      "v_fma_f32 v152, v180, v180, s51\n"
      "v_mul_f32 v151, v155, v155\n"
      "v_mfma_f32_16x16x32_bf16 v[130:133], a[44:47], a[224:227], v[130:133]\n"
      "s_add_u32 m0, s75, 3200\n"
      "v_mul_f32 v185, v101, v101\n"
      "v_mul_f32 v153, v181, v181\n"
      "v_mfma_f32_16x16x32_bf16 v[190:193], a[116:119], a[220:223], v[190:193]\n"
      "buffer_load_dwordx4 v106, s[20:23], s78 offen offset:896  lds\n"
      "v_fma_f32 v220, v112, v112, s51\n"
      "v_mul_f32_e64 v221, v113, v113\n"
      "v_pk_fma_f32 v[134:135], v[168:169], v[168:169], v[134:135]\n"
      "v_pk_fma_f32 v[200:201], v[218:219], v[218:219], v[200:201]\n"
      "v_pk_fma_f32 v[136:137], v[246:247], v[246:247], v[136:137]\n"
      "v_pk_fma_f32 v[152:153], v[182:183], v[182:183], v[152:153]\n"
      "v_pk_fma_f32 v[136:137], v[204:205], v[204:205], v[136:137]\n"
      "v_mfma_f32_16x16x32_bf16 v[232:235], a[68:71], a[216:219], v[232:235]\n"
      "s_add_u32 m0, s75, 7168\n"
      "ds_read_b128 a[252:255], v187 offset:10240\n"
      "v_mfma_f32_16x16x32_bf16 v[224:227], a[116:119], a[216:219], v[224:227]\n"
      "buffer_load_dwordx4 v106, s[20:23], s78 offen offset:1024  lds\n"
      "v_mfma_f32_16x16x32_bf16 v[194:197], a[44:47], a[220:223], v[194:197]\n"
      "s_add_u32 s43, s43, 1\n"
      "v_fma_f32 v150, v154, v154, s51\n"
      "s_mov_b32 s78, 184320\n"
      "v_mfma_f32_16x16x32_bf16 v[240:243], a[112:115], a[248:251], v[240:243]\n"
      "s_add_u32 m0, s75, 11520\n"
      "v_fma_f32 v184, v100, v100, s51\n"
      "v_mul_f32 v223, v109, v109\n"
      "v_fma_f32 v222, v108, v108, s51\n"
      "v_mfma_f32_16x16x32_bf16 v[240:243], a[116:119], a[224:227], v[240:243]\n"
      "ds_read_b128 a[248:251], v129 offset:10240\n"
      "buffer_load_dwordx4 v106, s[20:23], s78 offen offset:768  lds\n"
      "v_pk_fma_f32 v[136:137], v[206:207], v[206:207], v[136:137]\n"
      "v_pk_fma_f32 v[200:201], v[224:225], v[224:225], v[200:201]\n"
      "s_add_u32 m0, s75, 15488\n"
      "v_pk_fma_f32 v[152:153], v[190:191], v[190:191], v[152:153]\n"
      "v_pk_fma_f32 v[200:201], v[226:227], v[226:227], v[200:201]\n"
      "v_pk_fma_f32 v[136:137], v[232:233], v[232:233], v[136:137]\n"
      "v_pk_fma_f32 v[184:185], v[102:103], v[102:103], v[184:185]\n"
      "v_pk_fma_f32 v[220:221], v[114:115], v[114:115], v[220:221]\n"
      "v_pk_fma_f32 v[150:151], v[156:157], v[156:157], v[150:151]\n"
      "v_pk_fma_f32 v[136:137], v[234:235], v[234:235], v[136:137]\n"
      "v_pk_fma_f32 v[222:223], v[110:111], v[110:111], v[222:223]\n"
      "v_pk_fma_f32 v[150:151], v[194:195], v[194:195], v[150:151]\n"
      "v_pk_fma_f32 v[134:135], v[240:241], v[240:241], v[134:135]\n"
      "v_mfma_f32_16x16x32_bf16 v[248:251], a[140:143], a[216:219], v[248:251]\n"
      "buffer_load_dwordx4 v106, s[20:23], s78 offen offset:896  lds\n"
      "v_mfma_f32_16x16x32_bf16 v[116:119], a[68:71], a[228:231], v[116:119]\n"
      "s_add_u32 m0, s75, 19456\n"
      "v_add_f32 v136, v136, v137\n"
      "s_waitcnt vmcnt(11)\n"
      "s_barrier\n"
      "v_mfma_f32_16x16x32_bf16 v[212:215], a[140:143], a[228:231], v[212:215]\n"
      "s_mov_b32 s79, 0\n"
      "ds_read_b64_tr_b16 a[146:147], v124 offset:512\n"
      "v_mfma_f32_16x16x32_bf16 v[162:165], a[52:55], a[236:239], v[162:165]\n"
      "ds_read_b64_tr_b16 a[196:197], v188 offset:12288\n"
      "ds_read_b64_tr_b16 a[172:173], v125 offset:12288\n"
      "v_mfma_f32_16x16x32_bf16 v[170:173], a[140:143], a[220:223], v[170:173]\n"
      "buffer_load_dwordx4 v106, s[20:23], s78 offen offset:1024  lds\n"
      "v_mfma_f32_16x16x32_bf16 v[252:255], a[116:119], a[228:231], v[252:255]\n"
      "ds_read_b64_tr_b16 a[144:145], v124\n"
      "ds_read_b64_tr_b16 a[168:169], v125\n"
      "v_mfma_f32_16x16x32_bf16 v[162:165], a[56:59], a[240:243], v[162:165]\n"
      "ds_read_b64_tr_b16 a[174:175], v125 offset:12800\n"
      "s_add_u32 m0, s75, 24576\n"
      "v_mfma_f32_16x16x32_bf16 v[138:141], a[68:71], a[220:223], v[138:141]\n"
      "ds_read_b64_tr_b16 a[170:171], v125 offset:512\n"
      "ds_read_b64_tr_b16 a[198:199], v188 offset:12800\n"
      "v_mfma_f32_16x16x32_bf16 v[96:99], a[44:47], a[228:231], v[96:99]\n"
      "ds_read_b64_tr_b16 a[150:151], v124 offset:12800\n"
      "ds_read_b64_tr_b16 a[194:195], v188 offset:512\n"
      "ds_read_b64_tr_b16 a[148:149], v124 offset:12288\n"
      "v_pk_fma_f32 v[134:135], v[242:243], v[242:243], v[134:135]\n"
      "v_pk_fma_f32 v[150:151], v[196:197], v[196:197], v[150:151]\n"
      "v_pk_fma_f32 v[134:135], v[120:121], v[120:121], v[134:135]\n"
      "v_pk_fma_f32 v[200:201], v[248:249], v[248:249], v[200:201]\n"
      "v_pk_fma_f32 v[222:223], v[130:131], v[130:131], v[222:223]\n"
      "v_mfma_f32_16x16x32_bf16 v[144:147], a[68:71], a[224:227], v[144:147]\n"
      "ds_read_b64_tr_b16 a[192:193], v188\n"
      "buffer_load_dwordx4 v211, s[36:39], s79 offen  lds\n"
      "v_pk_fma_f32 v[220:221], v[252:253], v[252:253], v[220:221]\n"
      "v_pk_fma_f32 v[222:223], v[132:133], v[132:133], v[222:223]\n"
      "v_pk_fma_f32 v[200:201], v[250:251], v[250:251], v[200:201]\n"
      "v_pk_fma_f32 v[134:135], v[122:123], v[122:123], v[134:135]\n"
      "v_add_f32 v200, v200, v201\n"
      "v_add_f32 v134, v134, v135\n"
      "v_pk_fma_f32 v[150:151], v[138:139], v[138:139], v[150:151]\n"
      "v_pk_fma_f32 v[152:153], v[192:193], v[192:193], v[152:153]\n"
      "v_pk_fma_f32 v[220:221], v[254:255], v[254:255], v[220:221]\n"
      "v_permlane16_swap_b32 v136, v200\n"
      "v_pk_fma_f32 v[222:223], v[144:145], v[144:145], v[222:223]\n"
      "v_add_f32 v136, v136, v200\n"
      "v_pk_fma_f32 v[220:221], v[212:213], v[212:213], v[220:221]\n"
      "v_mov_b32 v200, v136\n"
      "v_pk_fma_f32 v[222:223], v[146:147], v[146:147], v[222:223]\n"
      "v_pk_fma_f32 v[152:153], v[170:171], v[170:171], v[152:153]\n"
      "v_pk_fma_f32 v[150:151], v[140:141], v[140:141], v[150:151]\n"
      "v_add_f32 v222, v222, v223\n"
      "v_pk_fma_f32 v[184:185], v[96:97], v[96:97], v[184:185]\n"
      "v_pk_fma_f32 v[152:153], v[172:173], v[172:173], v[152:153]\n"
      "v_permlane16_swap_b32 v222, v134\n"
      "v_add_f32 v222, v222, v134\n"
      "v_mov_b32 v134, v222\n"
      "v_add_f32 v150, v150, v151\n"
      "v_mfma_f32_16x16x32_bf16 v[104:107], a[24:27], a[232:235], 0\n"
      "s_waitcnt lgkmcnt(0)\n"
      "v_add_f32 v152, v152, v153\n"
      "v_permlane32_swap_b32_e64 v136, v200\n"
      "v_mfma_f32_16x16x32_bf16 v[158:161], a[12:15], a[244:247], v[158:161]\n"
      "s_add_u32 m0, s75, 28544\n"
      "v_permlane16_swap_b32 v150, v152\n"
      "v_permlane32_swap_b32 v222, v134\n"
      "v_add_f32 v136, v136, v200\n"
      "v_pk_fma_f32 v[220:221], v[214:215], v[214:215], v[220:221]\n"
      "v_add_f32 v222, v222, v134\n"
      "v_add_f32 v220, v220, v221\n"
      "v_add_f32 v150, v150, v152\n"
      "v_mfma_f32_16x16x32_bf16 v[158:161], a[16:19], a[248:251], v[158:161]\n"
      "buffer_load_dwordx4 v211, s[36:39], s79 offen offset:128  lds\n"
      "v_rsq_f32_e64 v222, v222\n"
      "v_pk_fma_f32 v[184:185], v[98:99], v[98:99], v[184:185]\n"
      "v_mfma_f32_16x16x32_bf16 v[158:161], a[20:23], a[252:255], v[158:161]\n"
      "ds_read_b32 v62, v62\n"
      "v_mov_b32 v152, v150\n"
      "v_rsq_f32 v136, v136\n"
      "v_mov_b32 v134, v222\n"
      "v_mfma_f32_16x16x32_bf16 v[176:179], a[80:83], a[240:243], v[176:179]\n"
      "s_add_u32 m0, s75, 32512\n"
      "v_permlane32_swap_b32 v150, v152\n"
      "v_mfma_f32_16x16x32_bf16 v[176:179], a[84:87], a[244:247], v[176:179]\n"
      "buffer_load_dwordx4 v211, s[36:39], s79 offen offset:256  lds\n"
      "v_add_f32 v150, v150, v152\n"
      "v_permlane16_swap_b32 v222, v134\n"
      "v_mfma_f32_16x16x32_bf16 v[176:179], a[88:91], a[248:251], v[176:179]\n"
      "s_add_u32 m0, s75, 36864\n"
      "v_mov_b32 v200, v136\n"
      "v_mul_f32 v222, s50, v222\n"
      "v_mfma_f32_16x16x32_bf16 v[176:179], a[92:95], a[252:255], v[176:179]\n"
      "s_mov_b32 s79, 12288\n"
      "v_rsq_f32 v150, v150\n"
      "v_permlane16_swap_b32 v136, v200\n"
      "v_mov_b32_e64 v152, v150\n"
      "v_pk_mul_f32 v[108:109], v[108:109], v[222:223] op_sel_hi:[1,0]\n"
      "v_pk_fma_f32 v[184:185], v[116:117], v[116:117], v[184:185]\n"
      "v_pk_mul_f32 v[120:121], v[120:121], v[134:135] op_sel_hi:[1,0]\n"
      "v_pk_mul_f32 v[132:133], v[132:133], v[222:223] op_sel_hi:[1,0]\n"
      "v_pk_mul_f32 v[218:219], v[218:219], v[200:201] op_sel_hi:[1,0]\n"
      "v_mul_f32_e64 v136, s50, v136\n"
      "v_pk_mul_f32 v[216:217], v[216:217], v[200:201] op_sel_hi:[1,0]\n"
      "v_pk_fma_f32 v[184:185], v[118:119], v[118:119], v[184:185]\n"
      "v_pk_mul_f32 v[146:147], v[146:147], v[222:223] op_sel_hi:[1,0]\n"
      "v_mfma_f32_16x16x32_bf16 v[162:165], a[60:63], a[244:247], v[162:165]\n"
      "buffer_load_dwordx4 v211, s[36:39], s79 offen  lds\n"
      "v_add_f32 v184, v184, v185\n"
      "v_permlane16_swap_b32 v150, v152\n"
      "v_pk_mul_f32 v[226:227], v[226:227], v[200:201] op_sel_hi:[1,0]\n"
      "v_pk_mul_f32 v[232:233], v[232:233], v[136:137] op_sel_hi:[1,0]\n"
      "v_pk_mul_f32 v[248:249], v[248:249], v[200:201] op_sel_hi:[1,0]\n"
      "v_pk_mul_f32 v[246:247], v[246:247], v[136:137] op_sel_hi:[1,0]\n"
      "v_pk_mul_f32 v[190:191], v[190:191], v[152:153] op_sel_hi:[1,0]\n"
      "v_mfma_f32_16x16x32_bf16 v[104:107], a[28:31], a[236:239], v[104:107]\n"
      "s_add_u32 m0, s75, 40832\n"
      "v_permlane16_swap_b32 v184, v220\n"
      "v_add_f32 v184, v184, v220\n"
      "v_cvt_pk_bf16_f32 v216, v216, v217\n"
      "v_mov_b32 v220, v184\n"
      "v_mfma_f32_16x16x32_bf16 v[104:107], a[32:35], a[240:243], v[104:107]\n"
      "s_waitcnt vmcnt(10)\n"
      "s_barrier\n"
      "ds_read_b64_tr_b16 a[204:205], v188 offset:36864\n"
      "v_mfma_f32_16x16x32_bf16 v[104:107], a[36:39], a[244:247], v[104:107]\n"
      "buffer_load_dwordx4 v211, s[36:39], s79 offen offset:128  lds\n"
      "v_permlane32_swap_b32 v184, v220\n"
      "v_cvt_pk_bf16_f32 v217, v218, v219\n"
      "v_pk_mul_f32 v[250:251], v[250:251], v[200:201] op_sel_hi:[1,0]\n"
      "v_pk_mul_f32 v[166:167], v[166:167], v[134:135] op_sel_hi:[1,0]\n"
      "v_pk_mul_f32 v[206:207], v[206:207], v[136:137] op_sel_hi:[1,0]\n"
      "v_pk_mul_f32 v[224:225], v[224:225], v[200:201] op_sel_hi:[1,0]\n"
      "v_pk_mul_f32 v[122:123], v[122:123], v[134:135] op_sel_hi:[1,0]\n"
      "v_add_f32_e64 v184, v184, v220\n"
      "v_cvt_pk_bf16_f32 v219, v226, v227\n"
      "v_cvt_pk_bf16_f32 v218, v224, v225\n"
      "v_pk_mul_f32 v[244:245], v[244:245], v[136:137] op_sel_hi:[1,0]\n"
      "v_pk_mul_f32 v[242:243], v[242:243], v[134:135] op_sel_hi:[1,0]\n"
      "v_mfma_f32_16x16x32_bf16 v[200:203], a[120:123], a[232:235], 0\n"
      "ds_read_b64_tr_b16 a[176:177], v125 offset:24576\n"
      "ds_read_b64_tr_b16 a[182:183], v125 offset:37376\n"
      "ds_read_b64_tr_b16 a[154:155], v124 offset:25088\n"
      "v_pk_mul_f32 v[192:193], v[192:193], v[152:153] op_sel_hi:[1,0]\n"
      "v_mul_f32_e64 v189, v177, v177\n"
      "v_cvt_pk_bf16_f32 v248, v248, v249\n"
      "v_pk_mul_f32 v[130:131], v[130:131], v[222:223] op_sel_hi:[1,0]\n"
      "v_mfma_f32_16x16x32_bf16 v[104:107], a[40:43], a[248:251], v[104:107]\n"
      "ds_read_b64_tr_b16 a[152:153], v124 offset:24576\n"
      "ds_read_b64_tr_b16 a[202:203], v188 offset:25088\n"
      "v_cvt_pk_bf16_f32 v249, v250, v251\n"
      "v_mfma_f32_16x16x32_bf16 v[228:231], a[96:99], a[232:235], 0\n"
      "ds_read_b64_tr_b16 a[200:201], v188 offset:24576\n"
      "v_rsq_f32_e64 v184, v184\n"
      "v_mfma_f32_16x16x32_bf16 v[228:231], a[100:103], a[236:239], v[228:231]\n"
      "ds_read_b64_tr_b16 a[206:207], v188 offset:37376\n"
      "ds_read_b64_tr_b16 a[178:179], v125 offset:25088\n"
      "v_mfma_f32_16x16x32_bf16 v[228:231], a[104:107], a[240:243], v[228:231]\n"
      "ds_read_b64_tr_b16 a[158:159], v124 offset:37376\n"
      "v_mul_f32_e64 v150, s50, v150\n"
      "v_mfma_f32_16x16x32_bf16 v[200:203], a[124:127], a[236:239], v[200:203]\n"
      "ds_read_b64_tr_b16 a[156:157], v124 offset:36864\n"
      "s_add_u32 m0, s75, 44800\n"
      "v_mfma_f32_16x16x32_bf16 v[200:203], a[128:131], a[240:243], v[200:203]\n"
      "ds_read_b64_tr_b16 a[180:181], v125 offset:36864\n"
      "v_mul_f32 v125, v159, v159\n"
      "v_mov_b32 v220, v184\n"
      "v_cvt_pk_bf16_f32 v244, v244, v245\n"
      "v_mfma_f32_16x16x32_bf16 v[228:231], a[108:111], a[244:247], v[228:231]\n"
      "s_mov_b32 s78, 24576\n"
      "ds_write_b128 v209, v[216:219] offset:32\n"
      "v_mfma_f32_16x16x32_bf16 v[228:231], a[112:115], a[248:251], v[228:231]\n"
      "ds_read_b128 v[216:219], v187\n"
      "v_cvt_pk_bf16_f32 v245, v246, v247\n"
      "v_mfma_f32_16x16x32_bf16 v[228:231], a[116:119], a[252:255], v[228:231]\n"
      "buffer_load_dwordx4 v211, s[36:39], s79 offen offset:256  lds\n"
      "v_fma_f32 v124, v158, v158, s51\n"
      "v_mfma_f32_16x16x32_bf16 v[104:107], a[44:47], a[252:255], v[104:107]\n"
      "ds_write_b64 v209, v[248:249] offset:24\n"
      "v_permlane16_swap_b32_e64 v184, v220\n"
      "v_cvt_pk_bf16_f32 v232, v232, v233\n"
      "v_pk_mul_f32 v[234:235], v[234:235], v[136:137] op_sel_hi:[1,0]\n"
      "v_pk_mul_f32 v[170:171], v[170:171], v[152:153] op_sel_hi:[1,0]\n"
      "v_mul_f32_e64 v184, s50, v184\n"
      "v_pk_fma_f32 v[124:125], v[160:161], v[160:161], v[124:125]\n"
      "v_pk_mul_f32 v[172:173], v[172:173], v[152:153] op_sel_hi:[1,0]\n"
      "v_pk_mul_f32 v[154:155], v[154:155], v[150:151] op_sel_hi:[1,0]\n"
      "v_pk_mul_f32 v[204:205], v[204:205], v[136:137] op_sel_hi:[1,0]\n"
      "v_cvt_pk_bf16_f32 v247, v206, v207\n"
      "v_pk_mul_f32 v[112:113], v[112:113], v[220:221] op_sel_hi:[1,0]\n"
      "v_mfma_f32_16x16x32_bf16 v[200:203], a[132:135], a[244:247], v[200:203]\n"
      "s_add_u32 m0, s75, 49152\n"
      "ds_read_b128 v[248:251], v187 offset:4096\n"
      "buffer_load_dwordx4 v211, s[36:39], s78 offen  lds\n"
      "v_pk_mul_f32 v[196:197], v[196:197], v[150:151] op_sel_hi:[1,0]\n"
      "v_fma_f32 v188, v176, v176, s51\n"
      "v_pk_mul_f32 v[194:195], v[194:195], v[150:151] op_sel_hi:[1,0]\n"
      "v_pk_fma_f32 v[188:189], v[178:179], v[178:179], v[188:189]\n"
      "v_pk_fma_f32 v[124:125], v[104:105], v[104:105], v[124:125]\n"
      "v_pk_mul_f32 v[180:181], v[180:181], v[152:153] op_sel_hi:[1,0]\n"
      "v_pk_mul_f32 v[254:255], v[254:255], v[220:221] op_sel_hi:[1,0]\n"
      "v_cvt_pk_bf16_f32 v233, v234, v235\n"
      "v_pk_mul_f32 v[144:145], v[144:145], v[222:223] op_sel_hi:[1,0]\n"
      "v_pk_mul_f32 v[240:241], v[240:241], v[134:135] op_sel_hi:[1,0]\n"
      "v_pk_mul_f32 v[168:169], v[168:169], v[134:135] op_sel_hi:[1,0]\n"
      "v_cvt_pk_bf16_f32 v246, v204, v205\n"
      "v_pk_mul_f32 v[140:141], v[140:141], v[150:151] op_sel_hi:[1,0]\n"
      "v_pk_mul_f32 v[212:213], v[212:213], v[220:221] op_sel_hi:[1,0]\n"
      "v_pk_mul_f32 v[156:157], v[156:157], v[150:151] op_sel_hi:[1,0]\n"
      "s_add_u32 m0, s75, 53120\n"
      "v_mfma_f32_16x16x32_bf16 v[200:203], a[136:139], a[248:251], v[200:203]\n"
      "ds_write_b128 v209, v[244:247]\n"
      "v_pk_fma_f32 v[188:189], v[228:229], v[228:229], v[188:189]\n"
      "v_cvt_pk_bf16_f32 v166, v166, v167\n"
      "v_cvt_pk_bf16_f32 v167, v168, v169\n"
      "v_cvt_pk_bf16_f32 v112, v112, v113\n"
      "v_mfma_f32_16x16x32_bf16 v[200:203], a[140:143], a[252:255], v[200:203]\n"
      "ds_read_b128 v[244:247], v129 offset:4096\n"
      "v_cvt_pk_bf16_f32 v144, v144, v145\n"
      "v_mfma_f32_16x16x32_bf16 v[162:165], a[64:67], a[248:251], v[162:165]\n"
      "ds_read_b128 v[204:207], v129\n"
      "buffer_load_dwordx4 v211, s[36:39], s78 offen offset:128  lds\n"
      "v_pk_mul_f32 v[100:101], v[100:101], v[184:185] op_sel_hi:[1,0]\n"
      "v_pk_mul_f32 v[110:111], v[110:111], v[222:223] op_sel_hi:[1,0]\n"
      "v_cvt_pk_bf16_f32 v108, v108, v109\n"
      "v_cvt_pk_bf16_f32 v169, v242, v243\n"
      "s_add_u32 m0, s75, 57088\n"
      "v_pk_mul_f32 v[114:115], v[114:115], v[220:221] op_sel_hi:[1,0]\n"
      "v_pk_fma_f32 v[124:125], v[106:107], v[106:107], v[124:125]\n"
      "v_pk_mul_f32 v[252:253], v[252:253], v[220:221] op_sel_hi:[1,0]\n"
      "v_pk_mul_f32 v[182:183], v[182:183], v[152:153] op_sel_hi:[1,0]\n"
      "v_pk_mul_f32 v[138:139], v[138:139], v[150:151] op_sel_hi:[1,0]\n"
      "v_pk_mul_f32 v[118:119], v[118:119], v[184:185] op_sel_hi:[1,0]\n"
      "v_cvt_pk_bf16_f32 v109, v110, v111\n"
      "v_cvt_pk_bf16_f32 v154, v154, v155\n"
      "v_cvt_pk_bf16_f32 v155, v156, v157\n"
      "v_mfma_f32_16x16x32_bf16 v[162:165], a[68:71], a[252:255], v[162:165]\n"
      "buffer_load_dwordx4 v211, s[36:39], s78 offen offset:256  lds\n"
      "s_mov_b32 s78, 36864\n"
      "v_cvt_pk_bf16_f32 v156, v194, v195\n"
      "s_waitcnt lgkmcnt(0)\n"
      "s_waitcnt vmcnt(9)\n"
      "s_barrier\n"
      "v_pk_mul_f32 v[116:117], v[116:117], v[184:185] op_sel_hi:[1,0]\n"
      "v_cvt_pk_bf16_f32 v157, v196, v197\n"
      "v_pk_fma_f32 v[188:189], v[230:231], v[230:231], v[188:189]\n"
      "s_add_u32 m0, s75, 61440\n"
      "ds_read_b64_tr_b16 a[188:189], v237 offset:12288\n"
      "v_pk_mul_f32 v[98:99], v[98:99], v[184:185] op_sel_hi:[1,0]\n"
      "v_mfma_f32_16x16x32_bf16 v[194:197], a[168:171], v[204:207], 0\n"
      "ds_read_b64_tr_b16 a[212:213], v238 offset:12288\n"
      "ds_read_b64_tr_b16 a[190:191], v237 offset:12800\n"
      "v_mfma_f32_16x16x32_bf16 v[194:197], a[172:175], v[216:219], v[194:197]\n"
      "ds_read_b64_tr_b16 a[186:187], v237 offset:512\n"
      "buffer_load_dwordx4 v211, s[36:39], s78 offen  lds\n"
      "v_mfma_f32_16x16x32_bf16 v[194:197], a[176:179], v[244:247], v[194:197]\n"
      "ds_read_b128 v[134:137], v129 offset:8192\n"
      "v_cvt_pk_bf16_f32 v111, v132, v133\n"
      "v_mfma_f32_16x16x32_bf16 v[194:197], a[180:183], v[248:251], v[194:197]\n"
      "ds_read_b64_tr_b16 a[160:161], v236\n"
      "ds_read_b64_tr_b16 a[214:215], v238 offset:12800\n"
      "v_mfma_f32_16x16x32_bf16 v[224:227], a[192:195], v[204:207], 0\n"
      "ds_read_b64_tr_b16 a[184:185], v237\n"
      "s_add_u32 m0, s75, 65408\n"
      "v_mfma_f32_16x16x32_bf16 v[224:227], a[196:199], v[216:219], v[224:227]\n"
      "ds_read_b64_tr_b16 a[210:211], v238 offset:512\n"
      "v_cvt_pk_bf16_f32 v110, v130, v131\n"
      "v_mfma_f32_16x16x32_bf16 v[224:227], a[200:203], v[244:247], v[224:227]\n"
      "ds_read_b64_tr_b16 a[164:165], v236 offset:12288\n"
      "buffer_load_dwordx4 v211, s[36:39], s78 offen offset:128  lds\n"
      "v_cvt_pk_bf16_f32 v120, v120, v121\n"
      "s_mov_b32 s79, 49152\n"
      "v_pk_fma_f32 v[124:125], v[162:163], v[162:163], v[124:125]\n"
      "v_pk_mul_f32 v[214:215], v[214:215], v[220:221] op_sel_hi:[1,0]\n"
      "v_mfma_f32_16x16x32_bf16 v[224:227], a[204:207], v[248:251], v[224:227]\n"
      "ds_read_b64_tr_b16 a[166:167], v236 offset:12800\n"
      "ds_read_b64_tr_b16 a[208:209], v238\n"
      "v_mfma_f32_16x16x32_bf16 v[150:153], a[144:147], v[204:207], 0\n"
      "ds_read_b128 v[130:133], v187 offset:8192\n"
      "v_cvt_pk_bf16_f32 v170, v170, v171\n"
      "s_add_u32 m0, s75, 69376\n"
      "ds_read_b64_tr_b16 a[162:163], v236 offset:512\n"
      "v_mfma_f32_16x16x32_bf16 v[150:153], a[148:151], v[216:219], v[150:153]\n"
      "buffer_load_dwordx4 v211, s[36:39], s78 offen offset:256  lds\n"
      "ds_read_b128 v[234:237], v187 offset:2048\n"
      "v_pk_mul_f32 v[102:103], v[102:103], v[184:185] op_sel_hi:[1,0]\n"
      "v_pk_fma_f32 v[124:125], v[164:165], v[164:165], v[124:125]\n"
      "v_pk_fma_f32 v[188:189], v[200:201], v[200:201], v[188:189]\n"
      "v_mfma_f32_16x16x32_bf16 v[150:153], a[152:155], v[244:247], v[150:153]\n"
      "ds_read_b128 v[242:245], v129 offset:2048\n"
      "v_cvt_pk_bf16_f32 v113, v114, v115\n"
      "v_cvt_pk_bf16_f32 v180, v180, v181\n"
      "v_mfma_f32_16x16x32_bf16 v[150:153], a[156:159], v[248:251], v[150:153]\n"
      "s_add_u32 m0, s75, 73728\n"
      "ds_read_b128 v[220:223], v129 offset:10240\n"
      "buffer_load_dwordx4 v211, s[36:39], s79 offen  lds\n"
      "v_pk_fma_f32 v[188:189], v[202:203], v[202:203], v[188:189]\n"
      "s_waitcnt lgkmcnt(3)\n"
      "v_mfma_f32_16x16x32_bf16 v[150:153], a[160:163], v[134:137], v[150:153]\n"
      "ds_write_b64 v209, v[232:233] offset:16\n"
      "v_mfma_f32_16x16x32_bf16 v[194:197], a[184:187], v[134:137], v[194:197]\n"
      "s_add_u32 m0, s75, 77696\n"
      "ds_read_b128 v[216:219], v129 offset:6144\n"
      "v_mfma_f32_16x16x32_bf16 v[194:197], a[188:191], v[130:133], v[194:197]\n"
      "buffer_load_dwordx4 v211, s[36:39], s79 offen offset:128  lds\n"
      "v_cvt_pk_bf16_f32 v212, v212, v213\n"
      "v_cvt_pk_bf16_f32 v115, v254, v255\n"
      "v_add_f32 v124, v124, v125\n"
      "v_cvt_pk_bf16_f32 v100, v100, v101\n"
      "v_cvt_pk_bf16_f32 v168, v240, v241\n"
      "v_add_f32 v188, v188, v189\n"
      "v_mfma_f32_16x16x32_bf16 v[224:227], a[208:211], v[134:137], v[224:227]\n"
      "s_add_u32 m0, s75, 81664\n"
      "ds_read_b128 v[204:207], v187 offset:6144\n"
      "v_mfma_f32_16x16x32_bf16 v[224:227], a[212:215], v[130:133], v[224:227]\n"
      "ds_read_b128 v[134:137], v0 offset:10240\n"
      "v_cvt_pk_bf16_f32 v145, v146, v147\n"
      "v_cvt_pk_bf16_f32 v213, v214, v215\n"
      "v_cvt_pk_bf16_f32 v194, v194, v195\n"
      "v_mfma_f32_16x16x32_bf16 v[150:153], a[164:167], v[130:133], v[150:153]\n"
      "buffer_load_dwordx4 v211, s[36:39], s79 offen offset:256  lds\n"
      "s_waitcnt lgkmcnt(2)\n"
      "v_mfma_f32_16x16x32_bf16 v[126:129], a[144:147], v[242:245], 0\n"
      "ds_read_b128 v[246:249], v0 offset:4096\n"
      "v_cvt_pk_bf16_f32 v171, v172, v173\n"
      "v_cvt_pk_bf16_f32 v195, v196, v197\n"
      "v_mfma_f32_16x16x32_bf16 v[196:199], a[192:195], v[242:245], 0\n"
      "s_mov_b32 s79, 61440\n"
      "v_permlane16_swap_b32 v124, v188\n"
      "v_add_f32_e64 v124, v124, v188\n"
      "s_add_u32 m0, s75, 86016\n"
      "v_mfma_f32_16x16x32_bf16 v[196:199], a[196:199], v[234:237], v[196:199]\n"
      "buffer_load_dwordx4 v211, s[36:39], s79 offen  lds\n"
      "v_mfma_f32_16x16x32_bf16 v[130:133], a[168:171], v[242:245], 0\n"
      "ds_read_b128 v[242:245], v187 offset:10240\n"
      "v_mov_b32_e64 v188, v124\n"
      "v_cvt_pk_bf16_f32 v114, v252, v253\n"
      "v_mfma_f32_16x16x32_bf16 v[126:129], a[148:151], v[234:237], v[126:129]\n"
      "s_add_u32 m0, s75, 89984\n"
      "v_permlane32_swap_b32_e64 v124, v188\n"
      "v_mfma_f32_16x16x32_bf16 v[126:129], a[152:155], v[216:219], v[126:129]\n"
      "ds_write_b64 v210, v[194:195] offset:32\n"
      "v_add_f32 v124, v124, v188\n"
      "v_xor_b32 v240, 64, v8\n"
      "v_cvt_pk_bf16_f32 v181, v182, v183\n"
      "v_mfma_f32_16x16x32_bf16 v[196:199], a[200:203], v[216:219], v[196:199]\n"
      "buffer_load_dwordx4 v211, s[36:39], s79 offen offset:128  lds\n"
      "v_mfma_f32_16x16x32_bf16 v[130:133], a[172:175], v[234:237], v[130:133]\n"
      "ds_read_b128 v[250:253], v1 offset:4096\n"
      "v_rsq_f32_e64 v124, v124\n"
      "v_mfma_f32_16x16x32_bf16 v[130:133], a[176:179], v[216:219], v[130:133]\n"
      "s_waitcnt lgkmcnt(2)\n"
      "v_add_u32 v85, 49152, v240\n"
      "v_mov_b32 v188, v124\n"
      "v_mfma_f32_16x16x32_bf16 v[130:133], a[180:183], v[204:207], v[130:133]\n"
      "s_add_u32 m0, s75, 93952\n"
      "v_permlane16_swap_b32_e64 v124, v188\n"
      "v_mfma_f32_16x16x32_bf16 v[126:129], a[156:159], v[204:207], v[126:129]\n"
      "buffer_load_dwordx4 v211, s[36:39], s79 offen offset:256  lds\n"
      "s_waitcnt vmcnt(12)\n"
      "s_barrier\n"
      "ds_read_b64_tr_b16 a[222:223], v239 offset:38912\n"
      "v_pk_mul_f32 v[96:97], v[96:97], v[184:185] op_sel_hi:[1,0]\n"
      "v_mfma_f32_16x16x32_bf16 v[126:129], a[160:163], v[220:223], v[126:129]\n"
      "ds_read_b64_tr_b16 v[232:233], v208 offset:24576\n"
      "ds_read_b64_tr_b16 a[216:217], v239 offset:24576\n"
      "ds_read_b64_tr_b16 a[242:243], v239 offset:26632\n"
      "v_mfma_f32_16x16x32_bf16 v[126:129], a[164:167], v[242:245], v[126:129]\n"
      "buffer_load_dwordx4 v[184:187], v36, s[28:31], s49 offen\n"
      "v_mfma_f32_16x16x32_bf16 v[196:199], a[204:207], v[204:207], v[196:199]\n"
      "ds_read_b128 v[214:217], v1 offset:10240\n"
      "v_cvt_pk_bf16_f32 v138, v138, v139\n"
      "v_mfma_f32_16x16x32_bf16 v[196:199], a[208:211], v[220:223], v[196:199]\n"
      "ds_read_b64_tr_b16 a[246:247], v239 offset:38920\n"
      "ds_read_b64_tr_b16 a[240:241], v239 offset:24584\n"
      "v_mul_f32 v124, s50, v124\n"
      "v_mfma_f32_16x16x32_bf16 v[130:133], a[184:187], v[220:223], v[130:133]\n"
      "ds_read_b64_tr_b16 v[236:237], v208 offset:36864\n"
      "ds_read_b64_tr_b16 a[244:245], v239 offset:36872\n"
      "v_mfma_f32_16x16x32_bf16 v[130:133], a[188:191], v[242:245], v[130:133]\n"
      "ds_read_b64_tr_b16 a[220:221], v239 offset:36864\n"
      "buffer_load_dword v207, v29, s[28:31], s49 offen\n"
      "v_pk_mul_f32 v[162:163], v[162:163], v[124:125] op_sel_hi:[1,0]\n"
      "v_pk_mul_f32 v[104:105], v[104:105], v[124:125] op_sel_hi:[1,0]\n"
      "v_cvt_pk_bf16_f32 v182, v190, v191\n"
      "v_mfma_f32_16x16x32_bf16 v[196:199], a[212:215], v[242:245], v[196:199]\n"
      "ds_read_b64_tr_b16 a[218:219], v239 offset:26624\n"
      "v_cvt_pk_bf16_f32 v101, v102, v103\n"
      "v_mfma_f32_16x16x32_bf16 v[172:175], v[246:249], a[168:171], 0\n"
      "ds_read_b64_tr_b16 v[238:239], v208 offset:38912\n"
      "s_waitcnt lgkmcnt(5)\n"
      "v_mfma_f32_16x16x32_bf16 v[172:175], v[250:253], a[172:175], v[172:175]\n"
      "buffer_load_dwordx4 v[146:149], v36, s[28:31], s74 offen\n"
      "ds_read_b64_tr_b16 v[234:235], v208 offset:26624\n"
      "v_cvt_pk_bf16_f32 v116, v116, v117\n"
      "v_cvt_pk_bf16_f32 v130, v130, v131\n"
      "v_cvt_pk_bf16_f32 v102, v96, v97\n"
      "v_mov_b32 v189, 0xbc80bc80\n"
      "v_pk_mul_f32 v[178:179], v[178:179], v[188:189] op_sel_hi:[1,0]\n"
      "v_cvt_pk_bf16_f32 v117, v118, v119\n"
      "v_mov_b32 v190, 0xbc80bc80\n"
      "v_pk_mul_f32 v[176:177], v[176:177], v[188:189] op_sel_hi:[1,0]\n"
      "v_mov_b32 v191, 0xbc80bc80\n"
      "v_cvt_pk_bf16_f32 v126, v126, v127\n"
      "v_cvt_pk_bf16_f32 v127, v128, v129\n"
      "v_pk_mul_f32 v[158:159], v[158:159], v[124:125] op_sel_hi:[1,0]\n"
      "v_pk_mul_f32 v[160:161], v[160:161], v[124:125] op_sel_hi:[1,0]\n"
      "v_cvt_pk_bf16_f32 v183, v192, v193\n"
      "v_cvt_pk_bf16_f32 v162, v162, v163\n"
      "v_cvt_pk_bf16_f32 v139, v140, v141\n"
      "v_mfma_f32_16x16x32_bf16 v[220:223], v[246:249], a[144:147], 0\n"
      "buffer_load_dword v118, v29, s[28:31], s74 offen\n"
      "v_cvt_pk_bf16_f32 v131, v132, v133\n"
      "s_add_u32 m0, s64, 18432\n"
      "v_pk_mul_f32 v[230:231], v[230:231], v[188:189] op_sel_hi:[1,0]\n"
      "v_pk_mul_f32 v[202:203], v[202:203], v[188:189] op_sel_hi:[1,0]\n"
      "v_pk_mul_f32 v[200:201], v[200:201], v[188:189] op_sel_hi:[1,0]\n"
      "v_cvt_pk_bf16_f32 v103, v98, v99\n"
      "v_pk_mul_f32 v[228:229], v[228:229], v[188:189] op_sel_hi:[1,0]\n"
      "v_cvt_pk_bf16_f32 v196, v196, v197\n"
      "v_pk_mul_f32 v[164:165], v[164:165], v[124:125] op_sel_hi:[1,0]\n"
      "v_cvt_pk_bf16_f32 v163, v164, v165\n"
      "v_pk_mul_f32 v[106:107], v[106:107], v[124:125] op_sel_hi:[1,0]\n"
      "v_cvt_pk_bf16_f32 v224, v224, v225\n"
      "v_cvt_pk_bf16_f32 v197, v198, v199\n"
      "v_cvt_pk_bf16_f32 v200, v200, v201\n"
      "v_mfma_f32_16x16x32_bf16 v[220:223], v[250:253], a[148:151], v[220:223]\n"
      "buffer_load_dwordx4 v16, s[16:19], s45 offen  lds\n"
      "v_subrev_u32 v39, 115456, v9\n"
      "v_cvt_pk_bf16_f32 v176, v176, v177\n"
      "v_mfma_f32_16x16x32_bf16 v[220:223], v[134:137], a[152:155], v[220:223]\n"
      "s_add_u32 m0, s64, 22528\n"
      "v_cvt_pk_bf16_f32 v201, v202, v203\n"
      "v_cvt_pk_bf16_f32 v158, v158, v159\n"
      "v_mfma_f32_16x16x32_bf16 v[220:223], v[214:217], a[156:159], v[220:223]\n"
      "ds_read_b128 v[202:205], v8 offset:24576\n"
      "ds_read_b128 v[192:195], v240 offset:24576\n"
      "v_mfma_f32_16x16x32_bf16 v[172:175], v[134:137], a[176:179], v[172:175]\n"
      "buffer_load_dwordx4 v17, s[16:19], s45 offen  lds\n"
      "v_cvt_pk_bf16_f32 v121, v122, v123\n"
      "v_mfma_f32_16x16x32_bf16 v[122:125], v[246:249], a[192:195], 0\n"
      "s_add_u32 m0, s64, 26624\n"
      "ds_read_b128 v[242:245], v8 offset:28672\n"
      "v_cvt_pk_bf16_f32 v177, v178, v179\n"
      "v_mfma_f32_16x16x32_bf16 v[122:125], v[250:253], a[196:199], v[122:125]\n"
      "ds_read_b128 v[246:249], v240 offset:28672\n"
      "v_lshrrev_b32_e64 v39, 4, v39\n"
      "v_mfma_f32_16x16x32_bf16 v[122:125], v[134:137], a[200:203], v[122:125]\n"
      "ds_read_b128 v[252:255], v0 offset:2048\n"
      "v_cvt_pk_bf16_f32 v159, v160, v161\n"
      "v_mfma_f32_16x16x32_bf16 v[122:125], v[214:217], a[204:207], v[122:125]\n"
      "ds_read_b128 v[134:137], v1 offset:2048\n"
      "buffer_load_dwordx4 v18, s[16:19], s45 offen  lds\n"
      "v_mfma_f32_16x16x32_bf16 v[172:175], v[214:217], a[180:183], v[172:175]\n"
      "s_mov_b32 exec_lo, 0x3f003f\n"
      "s_mov_b32 exec_hi, 0x3f003f\n"
      "ds_write_b64 v210, v[126:127] offset:18688\n"
      "ds_write_b64 v210, v[130:131] offset:18720\n"
      "ds_write_b64 v210, v[196:197] offset:18752\n"
      "s_mov_b64 exec, -1\n"
      "s_add_u32 m0, s64, 30720\n"
      "s_waitcnt lgkmcnt(3)\n"
      "v_mfma_f32_16x16x32_bf16 v[96:99], v[252:255], a[168:171], 0\n"
      "ds_read_b128 v[126:129], v240 offset:32768\n"
      "v_cvt_pk_bf16_f32 v161, v106, v107\n"
      "v_mov_b32 v188, 0xbc80bc80\n"
      "v_cvt_pk_bf16_f32 v178, v228, v229\n"
      "v_mfma_f32_16x16x32_bf16 v[96:99], v[134:137], a[172:175], v[96:99]\n"
      "ds_read_b128 v[214:217], v0 offset:16384\n"
      "v_cvt_pk_bf16_f32 v179, v230, v231\n"
      "v_mfma_f32_16x16x32_bf16 v[228:231], v[202:205], v[188:191], 0\n"
      "buffer_load_dwordx4 v19, s[16:19], s45 offen  lds\n"
      "v_mfma_f32_16x16x32_bf16 v[228:231], v[192:195], v[188:191], v[228:231]\n"
      "s_waitcnt vmcnt(14)\n"
      "s_barrier\n"
      "s_add_u32 m0, s65, 34816\n"
      "v_cvt_pk_bf16_f32 v160, v104, v105\n"
      "v_mfma_f32_16x16x32_bf16 v[228:231], v[242:245], v[188:191], v[228:231]\n"
      "ds_read_b128 v[130:133], v8 offset:32768\n"
      "ds_read_b128 v[202:205], v0 offset:8192\n"
      "v_mfma_f32_16x16x32_bf16 v[228:231], v[246:249], v[188:191], v[228:231]\n"
      "ds_read_b64_tr_b16 a[228:229], v82 offset:12288\n"
      "v_cvt_pk_bf16_f32 v150, v150, v151\n"
      "v_mfma_f32_16x16x32_bf16 v[104:107], v[252:255], a[192:195], 0\n"
      "buffer_load_dwordx4 v15, s[16:19], s45 offen  lds\n"
      "ds_read_b64_tr_b16 a[230:231], v82 offset:14336\n"
      "ds_read_b64_tr_b16 v[242:243], v78 offset:2048\n"
      "v_mfma_f32_16x16x32_bf16 v[194:197], v[252:255], a[144:147], 0\n"
      "ds_read_b64_tr_b16 a[250:251], v82 offset:2056\n"
      "v_cvt_pk_bf16_f32 v151, v152, v153\n"
      "v_mfma_f32_16x16x32_bf16 v[194:197], v[134:137], a[148:151], v[194:197]\n"
      "ds_read_b64_tr_b16 a[254:255], v82 offset:14344\n"
      "ds_read_b64_tr_b16 a[252:253], v82 offset:12296\n"
      "v_mfma_f32_16x16x32_bf16 v[104:107], v[134:137], a[196:199], v[104:107]\n"
      "ds_read_b64_tr_b16 v[246:247], v78 offset:14336\n"
      "ds_read_b64_tr_b16 a[224:225], v82\n"
      "s_waitcnt lgkmcnt(7)\n"
      "v_mfma_f32_16x16x32_bf16 v[104:107], v[202:205], a[200:203], v[104:107]\n"
      "ds_read_b128 v[250:253], v1 offset:8192\n"
      "ds_read_b64_tr_b16 v[240:241], v78\n"
      "v_mfma_f32_16x16x32_bf16 v[228:231], v[130:133], v[188:191], v[228:231]\n"
      "ds_read_b64_tr_b16 v[244:245], v78 offset:12288\n"
      "ds_read_b64_tr_b16 a[226:227], v82 offset:2048\n"
      "v_mfma_f32_16x16x32_bf16 v[228:231], v[126:129], v[188:191], v[228:231]\n"
      "ds_read_b64_tr_b16 a[248:249], v82 offset:8\n"
      "v_cvt_pk_bf16_f32 v225, v226, v227\n"
      "v_mfma_f32_16x16x32_bf16 v[96:99], v[202:205], a[176:179], v[96:99]\n"
      "ds_read_b128 v[126:129], v1 offset:16384\n"
      "v_and_b32 v39, 48, v39\n"
      "v_mfma_f32_16x16x32_bf16 v[172:175], v[214:217], a[184:187], v[172:175]\n"
      "ds_read_b128 v[130:133], v0 offset:14336\n"
      "ds_read_b128 v[134:137], v1 offset:14336\n"
      "v_mfma_f32_16x16x32_bf16 v[220:223], v[214:217], a[160:163], v[220:223]\n"
      "s_mov_b32 exec_lo, 0x3f003f\n"
      "s_mov_b32 exec_hi, 0x3f003f\n"
      "ds_write_b128 v209, v[158:161] offset:18688\n"
      "ds_write_b64 v209, v[162:163] offset:18704\n"
      "ds_write_b64 v209, v[200:201] offset:18712\n"
      "ds_write_b128 v209, v[176:179] offset:18720\n"
      "s_mov_b64 exec, -1\n"
      "v_mfma_f32_16x16x32_bf16 v[122:125], v[214:217], a[208:211], v[122:125]\n"
      "ds_read_b128 v[176:179], v85\n"
      "v_mul_f32 v231, 0x3eaaaaab, v231\n"
      "v_mfma_f32_16x16x32_bf16 v[194:197], v[202:205], a[152:155], v[194:197]\n"
      "s_waitcnt lgkmcnt(5)\n"
      "v_add_u32 v141, 49152, v8\n"
      "v_mul_f32 v230, 0x3eaaaaab, v230\n"
      "v_mfma_f32_16x16x32_bf16 v[194:197], v[250:253], a[156:159], v[194:197]\n"
      "ds_read_b128 v[202:205], v141 offset:4096\n"
      "v_add_u32 v39, 123136, v39\n"
      "v_mfma_f32_16x16x32_bf16 v[96:99], v[250:253], a[180:183], v[96:99]\n"
      "buffer_load_dwordx4 v[214:217], v36, s[28:31], s47 offen\n"
      "v_mfma_f32_16x16x32_bf16 v[220:223], v[126:129], a[164:167], v[220:223]\n"
      "ds_write_b64 v210, v[150:151]\n"
      "v_mfma_f32_16x16x32_bf16 v[104:107], v[250:253], a[204:207], v[104:107]\n"
      "ds_read_b128 v[250:253], v141\n"
      "v_add_u32_e64 v255, s92, v39\n"
      "v_mfma_f32_16x16x32_bf16 v[104:107], v[130:133], a[208:211], v[104:107]\n"
      "ds_read_b128 v[162:165], v141 offset:8192\n"
      "v_mul_f32 v229, 0x3eaaaaab, v229\n"
      "v_mul_f32 v228, 0x3eaaaaab, v228\n"
      "v_mfma_f32_16x16x32_bf16 v[104:107], v[134:137], a[212:215], v[104:107]\n"
      "ds_read_b128 v[198:201], v85 offset:8192\n"
      "v_cvt_pk_bf16_f32 v76, v220, v221\n"
      "v_mfma_f32_16x16x32_bf16 v[172:175], v[126:129], a[188:191], v[172:175]\n"
      "ds_write_b64 v210, v[224:225] offset:64\n"
      "v_mfma_f32_16x16x32_bf16 v[122:125], v[126:129], a[212:215], v[122:125]\n"
      "ds_read_b128 v[224:227], v85 offset:4096\n"
      "v_cvt_pk_bf16_f32 v77, v222, v223\n"
      "v_mfma_f32_16x16x32_bf16 v[126:129], v[166:169], v[154:157], v[44:47]\n"
      "buffer_load_dword v119, v29, s[28:31], s47 offen\n"
      "v_mfma_f32_16x16x32_bf16 v[96:99], v[130:133], a[184:187], v[96:99]\n"
      "ds_read_b128 v[158:161], v1\n"
      "v_cvt_pk_bf16_f32 v107, v106, v107\n"
      "v_mfma_f32_16x16x32_bf16 v[194:197], v[130:133], a[160:163], v[194:197]\n"
      "ds_read_b128 v[130:133], v0\n"
      "v_cvt_pk_bf16_f32 v81, v174, v175\n"
      "v_mfma_f32_16x16x32_bf16 v[194:197], v[134:137], a[164:167], v[194:197]\n"
      "s_waitcnt lgkmcnt(5)\n"
      "v_cvt_pk_bf16_f32 v106, v104, v105\n"
      "v_subrev_u32 v153, 115456, v9\n"
      "v_mfma_f32_16x16x32_bf16 v[96:99], v[134:137], a[188:191], v[96:99]\n"
      "ds_write_b128 v255, v[228:231]\n"
      "v_mfma_f32_16x16x32_bf16 v[208:211], v[250:253], v[188:191], 0\n"
      "s_waitcnt vmcnt(11)\n"
      "s_barrier\n"
      "v_cvt_pk_bf16_f32 v80, v172, v173\n"
      "v_mfma_f32_16x16x32_bf16 v[208:211], v[176:179], v[188:191], v[208:211]\n"
      "ds_read_b128 v[228:231], v141 offset:28672\n"
      "ds_read_b128 v[250:253], v141 offset:24576\n"
      "v_mfma_f32_16x16x32_bf16 v[208:211], v[202:205], v[188:191], v[208:211]\n"
      "ds_read_b128 v[202:205], v85 offset:24576\n"
      "s_waitcnt lgkmcnt(4)\n"
      "v_mfma_f32_16x16x32_bf16 v[208:211], v[224:227], v[188:191], v[208:211]\n"
      "ds_read_b128 v[224:227], v85 offset:32768\n"
      "v_cvt_pk_bf16_f32 v99, v98, v99\n"
      "v_bfe_u32 v104, v153, 8, 2\n"
      "v_mfma_f32_16x16x32_bf16 v[208:211], v[162:165], v[188:191], v[208:211]\n"
      "ds_read_b128 v[218:221], v85 offset:28672\n"
      "v_cvt_pk_bf16_f32 v84, v122, v123\n"
      "v_mfma_f32_16x16x32_bf16 v[208:211], v[198:201], v[188:191], v[208:211]\n"
      "ds_read_b128 v[134:137], v141 offset:32768\n"
      "v_cvt_pk_bf16_f32 v98, v96, v97\n"
      "v_mfma_f32_16x16x16_bf16 v[126:129], v[120:121], v[138:139], v[126:129]\n"
      "v_cvt_pk_bf16_f32 v85, v124, v125\n"
      "v_lshlrev_b32_e64 v104, 2, v104\n"
      "v_cvt_pk_bf16_f32 v197, v196, v197\n"
      "v_mfma_f32_16x16x32_bf16 v[140:143], v[130:133], a[144:147], 0\n"
      "ds_read_b128 v[176:179], v0 offset:6144\n"
      "v_bfe_u32 v153, v153, 4, 4\n"
      "v_mfma_f32_16x16x32_bf16 v[140:143], v[158:161], a[148:151], v[140:143]\n"
      "ds_read_b64_tr_b16 a[238:239], v82 offset:38912\n"
      "s_waitcnt lgkmcnt(5)\n"
      "v_mfma_f32_16x16x32_bf16 v[162:165], v[250:253], v[188:191], 0\n"
      "ds_read_b64_tr_b16 v[252:253], v78 offset:36864\n"
      "s_waitcnt vmcnt(10)\n"
      "v_or3_b32 v184, v184, v185, v186\n"
      "v_mfma_f32_16x16x32_bf16 v[162:165], v[202:205], v[188:191], v[162:165]\n"
      "ds_read_b128 v[202:205], v10 offset:32\n"
      "v_mul_f32 v211, 0x3eaaaaab, v211\n"
      "v_mfma_f32_16x16x32_bf16 v[162:165], v[228:231], v[188:191], v[162:165]\n"
      "ds_read_b128 v[198:201], v1 offset:6144\n"
      "s_waitcnt lgkmcnt(5)\n"
      "v_mfma_f32_16x16x32_bf16 v[162:165], v[218:221], v[188:191], v[162:165]\n"
      "ds_read_b64_tr_b16 a[236:237], v82 offset:36864\n"
      "v_cvt_pk_bf16_f32 v196, v194, v195\n"
      "v_mfma_f32_16x16x32_bf16 v[162:165], v[134:137], v[188:191], v[162:165]\n"
      "ds_read_b64_tr_b16 v[230:231], v82 offset:38920\n"
      "v_sub_u32 v153, v153, v104\n"
      "v_mfma_f32_16x16x32_bf16 v[162:165], v[224:227], v[188:191], v[162:165]\n"
      "ds_read_b64_tr_b16 v[228:229], v82 offset:36872\n"
      "v_mul_f32 v208, 0x3eaaaaab, v208\n"
      "v_mfma_f32_16x16x32_bf16 v[218:221], v[130:133], a[192:195], 0\n"
      "ds_read_b64 v[222:223], v10 offset:24\n"
      "v_mul_f32 v209, 0x3eaaaaab, v209\n"
      "v_mov_b32 v105, 0\n"
      "v_mfma_f32_16x16x32_bf16 v[218:221], v[158:161], a[196:199], v[218:221]\n"
      "s_waitcnt lgkmcnt(5)\n"
      "s_waitcnt vmcnt(9)\n"
      "v_or3_b32 v184, v184, v187, v207\n"
      "v_mul_f32 v162, 0x3eaaaaab, v162\n"
      "v_mfma_f32_16x16x32_bf16 v[122:125], v[130:133], a[168:171], 0\n"
      "ds_read_b128 v[134:137], v1 offset:12288\n"
      "v_mul_f32 v163, 0x3eaaaaab, v163\n"
      "v_mfma_f32_16x16x32_bf16 v[122:125], v[158:161], a[172:175], v[122:125]\n"
      "ds_read_b64_tr_b16 v[250:251], v78 offset:26624\n"
      "v_mul_f32 v164, 0x3eaaaaab, v164\n"
      "v_mfma_f32_16x16x32_bf16 v[122:125], v[176:179], a[176:179], v[122:125]\n"
      "ds_read_b128 v[130:133], v0 offset:12288\n"
      "s_waitcnt vmcnt(8)\n"
      "v_or3_b32 v146, v146, v147, v148\n"
      "v_mfma_f32_16x16x32_bf16 v[158:161], v[180:183], v[154:157], v[40:43]\n"
      "ds_write_b32 v31, v184 offset:6656\n"
      "v_mfma_f32_16x16x32_bf16 v[172:175], v[202:205], v[154:157], v[24:27]\n"
      "ds_read_b64_tr_b16 a[234:235], v82 offset:26624\n"
      "v_mul_f32 v165, 0x3eaaaaab, v165\n"
      "v_mfma_f32_16x16x32_bf16 v[140:143], v[176:179], a[152:155], v[140:143]\n"
      "ds_read_b64_tr_b16 v[224:225], v82 offset:24584\n"
      "v_mul_f32 v210, 0x3eaaaaab, v210\n"
      "v_mfma_f32_16x16x32_bf16 v[218:221], v[176:179], a[200:203], v[218:221]\n"
      "ds_write_b128 v255, v[208:211] offset:256\n"
      "v_mfma_f32_16x16x32_bf16 v[208:211], v[202:205], v[108:111], v[26:29]\n"
      "ds_read_b64_tr_b16 a[232:233], v82 offset:24576\n"
      "s_waitcnt lgkmcnt(5)\n"
      "v_mfma_f32_16x16x32_bf16 v[140:143], v[198:201], a[156:159], v[140:143]\n"
      "ds_read_b64_tr_b16 v[248:249], v78 offset:24576\n"
      "s_waitcnt vmcnt(7)\n"
      "v_or3_b32 v146, v146, v149, v118\n"
      "v_mfma_f32_16x16x32_bf16 v[122:125], v[198:201], a[180:183], v[122:125]\n"
      "ds_write_b128 v255, v[162:165] offset:512\n"
      "v_mfma_f32_16x16x32_bf16 v[122:125], v[130:133], a[184:187], v[122:125]\n"
      "ds_read_b64_tr_b16 v[226:227], v82 offset:26632\n"
      "v_mov_b32 v104, 0x3f80\n"
      "v_mfma_f32_16x16x32_bf16 v[176:179], v[112:115], v[154:157], v[48:51]\n"
      "ds_read_b64_tr_b16 v[254:255], v78 offset:38912\n"
      "v_lshlrev_b32_e64 v153, 4, v153\n"
      "v_mfma_f32_16x16x32_bf16 v[140:143], v[130:133], a[160:163], v[140:143]\n"
      "ds_write_b32 v31, v146 offset:5632\n"
      "v_mfma_f32_16x16x32_bf16 v[140:143], v[134:137], a[164:167], v[140:143]\n"
      "s_waitcnt lgkmcnt(0)\n"
      "s_barrier\n"
      "v_mov_b32_e64 v191, 0\n"
      "v_mfma_f32_16x16x16_bf16 v[158:161], v[170:171], v[138:139], v[158:161]\n"
      "ds_read_b128 v[184:187], v9 offset:6656\n"
      "v_lshlrev_b64 v[104:105], v153, v[104:105]\n"
      "v_mfma_f32_16x16x32_bf16 v[122:125], v[134:137], a[188:191], v[122:125]\n"
      "ds_read_b128 v[162:165], v9 offset:5632\n"
      "v_cmp_gt_u32 s[80:81], 64, v153\n"
      "v_mfma_f32_16x16x32_bf16 v[218:221], v[198:201], a[204:207], v[218:221]\n"
      "ds_read_b128 v[198:201], v39 offset:64\n"
      "v_cndmask_b32_e64 v154, 0, v104, s[80:81]\n"
      "v_mfma_f32_16x16x16_bf16 v[176:179], v[212:213], v[138:139], v[176:179]\n"
      "v_mov_b32_e64 v153, 0\n"
      "v_cndmask_b32_e64 v189, 0, v105, s[80:81]\n"
      "v_cvt_pk_bf16_f32 v194, v140, v141\n"
      "v_mfma_f32_16x16x16_bf16 v[172:175], v[222:223], v[138:139], v[172:175]\n"
      "v_cndmask_b32_e64 v188, 0, v104, s[80:81]\n"
      "v_cvt_pk_bf16_f32 v96, v122, v123\n"
      "v_mov_b32_e64 v152, 0\n"
      "v_mfma_f32_16x16x32_bf16 v[218:221], v[130:133], a[208:211], v[218:221]\n"
      "ds_read_b128 v[130:133], v39\n"
      "v_cndmask_b32_e64 v155, 0, v105, s[80:81]\n"
      "v_mfma_f32_16x16x32_bf16 v[218:221], v[134:137], a[212:215], v[218:221]\n"
      "s_waitcnt lgkmcnt(3)\n"
      "v_or3_b32 v184, v184, v185, v186\n"
      "v_mov_b32 v190, 0\n"
      "v_mfma_f32_16x16x16_bf16 v[208:211], v[222:223], v[144:145], v[208:211]\n"
      "v_cvt_pk_bf16_f32 v195, v142, v143\n"
      "v_bitop3_b32 v184, v184, v187, s33 bitop3:0xa8\n"
      "v_cmp_ne_u32 s[56:57], 0, v184\n"
      "v_mfma_f32_16x16x32_bf16 v[140:143], v[180:183], v[108:111], v[52:55]\n"
      "s_waitcnt lgkmcnt(1)\n"
      "v_cvt_pk_bf16_f32 v105, v220, v221\n"
      "v_or3_b32 v162, v162, v163, v164\n"
      "v_mfma_f32_16x16x32_bf16 v[184:187], v[152:155], v[232:235], v[198:201]\n"
      "s_or_b64 vcc, s[56:57], s[54:55]\n"
      "v_cvt_pk_bf16_f32 v104, v218, v219\n"
      "v_bitop3_b32 v162, v162, v165, s33 bitop3:0xa8\n"
      "v_mfma_f32_16x16x32_bf16 v[218:221], v[152:155], a[216:219], v[198:201]\n"
      "s_cbranch_vccnz L_rare_1_%=\n"
      "L_back_2_%=:\n"
      "v_mfma_f32_16x16x32_bf16 v[134:137], v[152:155], a[240:243], v[198:201]\n"
      "s_nop 2\n"
      "s_cbranch_vccnz L_rare_3_%=\n"
      "L_back_4_%=:\n"
      "v_mfma_f32_16x16x32_bf16 v[198:201], v[112:115], v[108:111], v[60:63]\n"
      "s_waitcnt lgkmcnt(0)\n"
      "v_cvt_pk_bf16_f32 v97, v124, v125\n"
      "v_cmp_ne_u32 s[56:57], 0, v162\n"
      "v_mfma_f32_16x16x32_bf16 v[122:125], v[188:191], v[232:235], v[130:133]\n"
      "v_cvt_pk_bf16_f32 v234, v184, v185\n"
      "v_cvt_pk_bf16_f32 v235, v186, v187\n"
      "v_exp_f32 v159, v159\n"
      "v_cvt_pk_bf16_f32 v134, v134, v135\n"
      "v_mfma_f32_16x16x16_bf16 v[140:143], v[170:171], v[144:145], v[140:143]\n"
      "v_exp_f32 v179, v179\n"
      "v_cvt_pk_bf16_f32 v136, v136, v137\n"
      "v_mfma_f32_16x16x16_bf16 v[198:201], v[212:213], v[144:145], v[198:201]\n"
      "v_cvt_pk_bf16_f32 v233, v124, v125\n"
      "v_exp_f32 v161, v161\n"
      "v_exp_f32 v158, v158\n"
      "v_cvt_pk_bf16_f32 v218, v218, v219\n"
      "v_mfma_f32_16x16x32_bf16 v[184:187], v[166:169], v[108:111], v[56:59]\n"
      "ds_read_b128 v[108:111], v10\n"
      "v_cvt_pk_bf16_f32 v232, v122, v123\n"
      "v_mfma_f32_16x16x32_bf16 v[162:165], v[188:191], a[216:219], v[130:133]\n"
      "v_exp_f32_e64 v129, v129\n"
      "v_mfma_f32_16x16x32_bf16 v[122:125], v[188:191], a[240:243], v[130:133]\n"
      "ds_read_b64 v[138:139], v10 offset:16\n"
      "v_accvgpr_write_b32 a243, v136\n"
      "v_cvt_pk_bf16_f32 v158, v158, v159\n"
      "v_mfma_f32_16x16x16_bf16 v[184:187], v[120:121], v[144:145], v[184:187]\n"
      "v_accvgpr_write_b32 a218, v218\n"
      "v_accvgpr_write_b32 a242, v134\n"
      "v_cvt_pk_bf16_f32 v162, v162, v163\n"
      "v_exp_f32 v128, v128\n"
      "v_exp_f32 v172, v172\n"
      "v_cvt_pk_bf16_f32 v164, v164, v165\n"
      "v_cvt_pk_bf16_f32 v122, v122, v123\n"
      "v_cvt_pk_bf16_f32 v220, v220, v221\n"
      "v_mfma_f32_16x16x32_bf16 v[130:133], v[166:169], v[100:103], v[68:71]\n"
      "s_waitcnt lgkmcnt(1)\n"
      "v_exp_f32 v126, v126\n"
      "v_exp_f32 v160, v160\n"
      "v_accvgpr_write_b32 a240, v122\n"
      "v_mfma_f32_16x16x32_bf16 v[134:137], v[108:111], v[166:169], v[30:33]\n"
      "s_cbranch_vccnz L_rare_5_%=\n"
      "L_back_6_%=:\n"
      "v_accvgpr_write_b32 a217, v164\n"
      "v_exp_f32 v177, v177\n"
      "v_mfma_f32_16x16x16_bf16 v[130:133], v[120:121], v[116:117], v[130:133]\n"
      "v_cvt_pk_bf16_f32 v159, v160, v161\n"
      "v_exp_f32 v187, v187\n"
      "v_exp_f32_e64 v176, v176\n"
      "v_mfma_f32_16x16x32_bf16 v[144:147], v[180:183], v[100:103], v[64:67]\n"
      "s_waitcnt lgkmcnt(0)\n"
      "v_exp_f32 v127, v127\n"
      "v_mfma_f32_16x16x16_bf16 v[134:137], v[138:139], v[120:121], v[134:137]\n"
      "v_exp_f32_e64 v199, v199\n"
      "v_cvt_pk_bf16_f32 v161, v128, v129\n"
      "v_mfma_f32_16x16x32_bf16 v[148:151], v[108:111], v[180:183], v[30:33]\n"
      "ds_read_u16 v82, v11 offset:32\n"
      "v_exp_f32_e64 v208, v208\n"
      "v_mfma_f32_16x16x32_bf16 v[180:183], v[202:205], v[100:103], v[28:31]\n"
      "ds_read_u16 v78, v11\n"
      "v_cvt_pk_bf16_f32 v160, v126, v127\n"
      "v_mfma_f32_16x16x16_bf16 v[144:147], v[170:171], v[116:117], v[144:147]\n"
      "v_exp_f32 v201, v201\n"
      "v_accvgpr_write_b32 a216, v162\n"
      "v_exp_f32 v178, v178\n"
      "v_mfma_f32_16x16x16_bf16 v[180:183], v[222:223], v[116:117], v[180:183]\n"
      "v_exp_f32_e64 v141, v141\n"
      "v_cvt_pk_bf16_f32 v90, v172, 0\n"
      "v_mfma_f32_16x16x32_bf16 v[126:129], v[112:115], v[100:103], v[72:75]\n"
      "ds_read_u16 v86, v11 offset:64\n"
      "v_exp_f32_e64 v200, v200\n"
      "v_mfma_f32_16x16x32_bf16 v[162:165], v[108:111], v[202:205], v[30:33]\n"
      "ds_read_b128 v[202:205], v39 offset:128\n"
      "v_cvt_pk_bf16_f32 v88, v176, v177\n"
      "v_mfma_f32_16x16x16_bf16 v[148:151], v[138:139], v[170:171], v[148:151]\n"
      "v_exp_f32 v143, v143\n"
      "v_exp_f32 v140, v140\n"
      "v_cvt_pk_bf16_f32 v89, v178, v179\n"
      "v_mfma_f32_16x16x32_bf16 v[174:177], v[108:111], v[112:115], v[30:33]\n"
      "ds_read_b128 v[108:111], v39 offset:192\n"
      "v_exp_f32 v185, v185\n"
      "v_mfma_f32_16x16x16_bf16 v[162:165], v[138:139], v[222:223], v[162:165]\n"
      "v_cvt_pk_bf16_f32 v124, v124, v125\n"
      "v_cvt_pk_bf16_f32 v94, v208, 0\n"
      "s_waitcnt vmcnt(1)\n"
      "v_or3_b32 v214, v214, v215, v216\n"
      "v_mfma_f32_16x16x32_bf16 v[208:211], v[194:197], v[158:161], 0\n"
      "s_waitcnt lgkmcnt(2)\n"
      "v_exp_f32 v142, v142\n"
      "v_mfma_f32_16x16x32_bf16 v[208:211], v[76:79], v[88:91], v[208:211]\n"
      "v_exp_f32_e64 v186, v186\n"
      "v_mfma_f32_16x16x16_bf16 v[126:129], v[212:213], v[116:117], v[126:129]\n"
      "v_cvt_pk_bf16_f32 v140, v140, v141\n"
      "v_exp_f32_e64 v184, v184\n"
      "v_mfma_f32_16x16x32_bf16 v[166:169], v[104:107], v[158:161], 0\n"
      "v_cvt_pk_bf16_f32 v93, v200, v201\n"
      "v_cndmask_b32 v148, v148, v134, s[58:59]\n"
      "v_mfma_f32_16x16x32_bf16 v[166:169], v[84:87], v[88:91], v[166:169]\n"
      "s_waitcnt lgkmcnt(0)\n"
      "v_cvt_pk_bf16_f32 v141, v142, v143\n"
      "v_accvgpr_write_b32 a219, v220\n"
      "v_mfma_f32_16x16x32_bf16 v[218:221], v[152:155], v[236:239], v[108:111]\n"
      "s_cbranch_vccnz L_rare_7_%=\n"
      "L_back_8_%=:\n"
      "v_mfma_f32_16x16x32_bf16 v[170:173], v[32:35], v[158:161], 0\n"
      "ds_read_b128 v[134:137], v39 offset:320\n"
      "v_exp_f32_e64 v132, v132\n"
      "v_cvt_pk_bf16_f32 v142, v184, v185\n"
      "v_mfma_f32_16x16x32_bf16 v[170:173], v[32:35], v[88:91], v[170:173]\n"
      "s_or_b32 s95, s84, s85\n"
      "v_exp_f32 v133, v133\n"
      "v_mfma_f32_16x16x16_bf16 v[174:177], v[138:139], v[212:213], v[174:177]\n"
      "v_exp_f32_e64 v198, v198\n"
      "v_cvt_pk_bf16_f32 v143, v186, v187\n"
      "v_mfma_f32_16x16x32_bf16 v[184:187], v[188:191], v[236:239], v[202:205]\n"
      "v_exp_f32 v127, v127\n"
      "v_mfma_f32_16x16x32_bf16 v[100:103], v[188:191], a[220:223], v[202:205]\n"
      "v_rcp_f32 v170, v170\n"
      "v_mfma_f32_16x16x32_bf16 v[120:123], v[96:99], v[158:161], 0\n"
      "ds_read_b128 v[158:161], v39 offset:256\n"
      "v_exp_f32 v144, v144\n"
      "v_mfma_f32_16x16x32_bf16 v[120:123], v[80:83], v[88:91], v[120:123]\n"
      "v_exp_f32 v128, v128\n"
      "v_mfma_f32_16x16x32_bf16 v[176:179], v[188:191], a[244:247], v[202:205]\n"
      "s_waitcnt lgkmcnt(1)\n"
      "v_exp_f32 v126, v126\n"
      "v_mfma_f32_16x16x32_bf16 v[202:205], v[152:155], a[248:251], v[134:137]\n"
      "v_cvt_pk_bf16_f32 v100, v100, v101\n"
      "v_exp_f32 v131, v131\n"
      "v_cvt_pk_bf16_f32 v92, v198, v199\n"
      "v_exp_f32 v145, v145\n"
      "v_pk_mul_f32 v[208:209], v[208:209], v[170:171] op_sel_hi:[1,0]\n"
      "v_exp_f32_e64 v129, v129\n"
      "v_pk_mul_f32 v[122:123], v[122:123], v[170:171] op_sel_hi:[1,0]\n"
      "v_cvt_pk_bf16_f32 v102, v102, v103\n"
      "v_pk_mul_f32 v[210:211], v[210:211], v[170:171] op_sel_hi:[1,0]\n"
      "v_cvt_pk_bf16_f32 v236, v184, v185\n"
      "v_mfma_f32_16x16x32_bf16 v[182:185], v[152:155], a[244:247], v[108:111]\n"
      "s_or_b32 s95, s95, s48\n"
      "v_exp_f32 v147, v147\n"
      "v_mfma_f32_16x16x32_bf16 v[198:201], v[152:155], a[220:223], v[108:111]\n"
      "s_cmp_ge_u32 s95, 0x40000000\n"
      "s_cselect_b32 s94, -1, -2\n"
      "v_cndmask_b32 v148, v148, v174, s[60:61]\n"
      "v_cvt_pk_bf16_f32 v89, v128, v129\n"
      "v_accvgpr_write_b32 a220, v100\n"
      "v_cvt_pk_bf16_f32 v239, v220, v221\n"
      "v_pk_mul_f32 v[166:167], v[166:167], v[170:171] op_sel_hi:[1,0]\n"
      "v_exp_f32 v180, v180\n"
      "v_pk_mul_f32 v[120:121], v[120:121], v[170:171] op_sel_hi:[1,0]\n"
      "v_mfma_f32_16x16x32_bf16 v[108:111], v[152:155], v[240:243], v[134:137]\n"
      "v_cndmask_b32 v148, v148, v162, s[62:63]\n"
      "v_cvt_pk_bf16_f32 v184, v184, v185\n"
      "v_cvt_pk_bf16_f32 v204, v204, v205\n"
      "v_mfma_f32_16x16x32_bf16 v[172:175], v[152:155], a[224:227], v[134:137]\n"
      "s_waitcnt lgkmcnt(0)\n"
      "v_exp_f32 v130, v130\n"
      "v_mfma_f32_16x16x32_bf16 v[162:165], v[188:191], a[224:227], v[158:161]\n"
      "s_waitcnt vmcnt(0)\n"
      "v_or3_b32 v214, v214, v217, v119\n"
      "v_cvt_pk_bf16_f32 v200, v200, v201\n"
      "v_mfma_f32_16x16x32_bf16 v[220:223], v[188:191], v[240:243], v[158:161]\n"
      "v_exp_f32 v146, v146\n"
      "v_cvt_pk_bf16_f32 v237, v186, v187\n"
      "v_cvt_pk_bf16_f32 v144, v144, v145\n"
      "v_mfma_f32_16x16x32_bf16 v[134:137], v[188:191], a[248:251], v[158:161]\n"
      "v_cvt_pk_bf16_f32 v178, v178, v179\n"
      "v_cvt_pk_bf16_f32 v208, v208, v209\n"
      "v_mfma_f32_16x16x32_bf16 v[158:161], v[32:35], v[140:143], 0\n"
      "s_cbranch_vccnz L_rare_9_%=\n"
      "L_back_10_%=:\n"
      "v_cvt_pk_bf16_f32 v240, v220, v221\n"
      "v_mfma_f32_16x16x32_bf16 v[158:161], v[32:35], v[92:95], v[158:161]\n"
      "v_cvt_pk_bf16_f32 v174, v174, v175\n"
      "v_cvt_pk_bf16_f32 v145, v146, v147\n"
      "v_cvt_pk_bf16_f32 v209, v210, v211\n"
      "v_cvt_pk_bf16_f32 v146, v130, v131\n"
      "v_mfma_f32_16x16x32_bf16 v[128:131], v[194:197], v[140:143], 0\n"
      "s_cmp_gt_u32 s42, s44\n"
      "s_cselect_b32 s95, s94, s95\n"
      "v_cvt_pk_bf16_f32 v88, v126, v127\n"
      "v_mfma_f32_16x16x32_bf16 v[128:131], v[76:79], v[92:95], v[128:131]\n"
      "v_exp_f32_e64 v148, v148\n"
      "v_cvt_pk_bf16_f32 v241, v222, v223\n"
      "v_mfma_f32_16x16x32_bf16 v[220:223], v[104:107], v[140:143], 0\n"
      "ds_read_b128 v[116:119], v39 offset:448\n"
      "v_cvt_pk_bf16_f32 v242, v108, v109\n"
      "v_mfma_f32_16x16x32_bf16 v[220:223], v[84:87], v[92:95], v[220:223]\n"
      "s_and_b32 s86, s86, -4\n"
      "v_cvt_pk_bf16_f32 v202, v202, v203\n"
      "v_accvgpr_write_b32 a245, v178\n"
      "v_accvgpr_write_b32 a241, v124\n"
      "v_rcp_f32 v158, v158\n"
      "v_cvt_pk_bf16_f32 v210, v120, v121\n"
      "v_pk_mul_f32 v[128:129], v[128:129], v[158:159] op_sel_hi:[1,0]\n"
      "v_cvt_pk_bf16_f32 v164, v164, v165\n"
      "v_pk_mul_f32 v[130:131], v[130:131], v[158:159] op_sel_hi:[1,0]\n"
      "v_cvt_pk_bf16_f32 v147, v132, v133\n"
      "v_pk_mul_f32 v[222:223], v[222:223], v[158:159] op_sel_hi:[1,0]\n"
      "v_pk_mul_f32 v[220:221], v[220:221], v[158:159] op_sel_hi:[1,0]\n"
      "v_mfma_f32_16x16x32_bf16 v[124:127], v[96:99], v[140:143], 0\n"
      "v_cvt_pk_bf16_f32 v136, v136, v137\n"
      "v_cvt_pk_bf16_f32 v148, v148, v148\n"
      "v_cvt_pk_bf16_f32 v176, v176, v177\n"
      "v_mfma_f32_16x16x32_bf16 v[124:127], v[80:83], v[92:95], v[124:127]\n"
      "v_cvt_pk_bf16_f32 v90, v180, 0\n"
      "v_accvgpr_write_b32 a249, v136\n"
      "v_cvt_pk_bf16_f32 v211, v122, v123\n"
      "v_mfma_f32_16x16x32_bf16 v[140:143], v[32:35], v[144:147], 0\n"
      "ds_read_b128 v[178:181], v39 offset:384\n"
      "v_cvt_pk_bf16_f32 v198, v198, v199\n"
      "v_mfma_f32_16x16x32_bf16 v[140:143], v[32:35], v[88:91], v[140:143]\n"
      "ds_write_b128 v3, v[208:211]\n"
      "v_pk_mul_f32 v[168:169], v[168:169], v[170:171] op_sel_hi:[1,0]\n"
      "v_mfma_f32_16x16x32_bf16 v[206:209], v[96:99], v[144:147], 0\n"
      "ds_read_b128 v[120:123], v39 offset:576\n"
      "v_cvt_pk_bf16_f32 v182, v182, v183\n"
      "v_cvt_pk_bf16_f32 v128, v128, v129\n"
      "v_accvgpr_write_b32 a247, v184\n"
      "v_mfma_f32_16x16x32_bf16 v[206:209], v[80:83], v[88:91], v[206:209]\n"
      "s_waitcnt lgkmcnt(3)\n"
      "v_rcp_f32_e64 v140, v140\n"
      "v_cvt_pk_bf16_f32 v243, v110, v111\n"
      "v_mfma_f32_16x16x32_bf16 v[108:111], v[194:197], v[144:147], 0\n"
      "ds_read_b128 v[184:187], v39 offset:640\n"
      "v_cvt_pk_bf16_f32 v162, v162, v163\n"
      "v_accvgpr_write_b32 a246, v182\n"
      "v_mfma_f32_16x16x32_bf16 v[108:111], v[76:79], v[88:91], v[108:111]\n"
      "ds_write_b16 v13, v148\n"
      "v_mfma_f32_16x16x32_bf16 v[148:151], v[152:155], a[228:231], v[116:119]\n"
      "ds_read_b128 v[136:139], v39 offset:512\n"
      "v_accvgpr_write_b32 a244, v176\n"
      "v_mfma_f32_16x16x32_bf16 v[112:115], v[104:107], v[144:147], 0\n"
      "v_accvgpr_write_b32 a225, v164\n"
      "v_cvt_pk_bf16_f32 v166, v166, v167\n"
      "v_mfma_f32_16x16x32_bf16 v[112:115], v[84:87], v[88:91], v[112:115]\n"
      "v_cvt_pk_bf16_f32 v172, v172, v173\n"
      "v_cvt_pk_bf16_f32 v167, v168, v169\n"
      "v_mfma_f32_16x16x32_bf16 v[144:147], v[152:155], a[252:255], v[116:119]\n"
      "s_waitcnt lgkmcnt(3)\n"
      "v_cvt_pk_bf16_f32 v134, v134, v135\n"
      "v_cvt_pk_bf16_f32 v238, v218, v219\n"
      "v_cvt_pk_bf16_f32 v150, v150, v151\n"
      "v_mfma_f32_16x16x32_bf16 v[210:213], v[188:191], a[228:231], v[178:181]\n"
      "ds_write_b64 v4, v[166:167]\n"
      "v_accvgpr_write_b32 a248, v134\n"
      "v_accvgpr_write_b32 a231, v150\n"
      "v_pk_mul_f32 v[124:125], v[124:125], v[158:159] op_sel_hi:[1,0]\n"
      "v_pk_mul_f32 v[126:127], v[126:127], v[158:159] op_sel_hi:[1,0]\n"
      "v_pk_mul_f32 v[108:109], v[108:109], v[140:141] op_sel_hi:[1,0]\n"
      "v_cvt_pk_bf16_f32 v108, v108, v109\n"
      "v_pk_mul_f32 v[114:115], v[114:115], v[140:141] op_sel_hi:[1,0]\n"
      "v_cvt_pk_bf16_f32 v148, v148, v149\n"
      "v_cvt_pk_bf16_f32 v129, v130, v131\n"
      "v_accvgpr_write_b32 a227, v174\n"
      "v_pk_mul_f32 v[112:113], v[112:113], v[140:141] op_sel_hi:[1,0]\n"
      "v_mfma_f32_16x16x32_bf16 v[216:219], v[188:191], v[244:247], v[178:181]\n"
      "ds_read_b128 v[164:167], v39 offset:704\n"
      "v_cvt_pk_bf16_f32 v212, v212, v213\n"
      "v_cvt_pk_bf16_f32 v146, v146, v147\n"
      "v_mfma_f32_16x16x32_bf16 v[158:161], v[152:155], v[244:247], v[116:119]\n"
      "s_waitcnt lgkmcnt(2)\n"
      "v_accvgpr_write_b32 a221, v102\n"
      "v_cvt_pk_bf16_f32 v130, v124, v125\n"
      "v_mfma_f32_16x16x32_bf16 v[132:135], v[188:191], a[236:239], v[184:187]\n"
      "s_add_u32 s42, s42, 1\n"
      "v_cvt_pk_bf16_f32 v131, v126, v127\n"
      "v_accvgpr_write_b32 a224, v162\n"
      "v_cvt_pk_bf16_f32 v220, v220, v221\n"
      "v_cvt_pk_bf16_f32 v221, v222, v223\n"
      "v_mfma_f32_16x16x32_bf16 v[124:127], v[188:191], a[252:255], v[178:181]\n"
      "s_cmp_ge_u32 s95, 0x40000000\n"
      "s_cselect_b64 vcc, -1, s[56:57]\n"
      "v_accvgpr_write_b32 a226, v172\n"
      "v_cvt_pk_bf16_f32 v134, v134, v135\n"
      "v_mfma_f32_16x16x32_bf16 v[174:177], v[152:155], a[232:235], v[120:123]\n"
      "ds_write_b128 v3, v[128:131] offset:256\n"
      "v_pk_mul_f32 v[110:111], v[110:111], v[140:141] op_sel_hi:[1,0]\n"
      "v_cvt_pk_bf16_f32 v144, v144, v145\n"
      "v_pk_mul_f32 v[208:209], v[208:209], v[140:141] op_sel_hi:[1,0]\n"
      "v_pk_mul_f32 v[206:207], v[206:207], v[140:141] op_sel_hi:[1,0]\n"
      "v_mfma_f32_16x16x32_bf16 v[140:143], v[188:191], v[224:227], v[136:139]\n"
      "ds_read_b128 v[178:181], v14\n"
      "v_cvt_pk_bf16_f32 v124, v124, v125\n"
      "v_cvt_pk_bf16_f32 v210, v210, v211\n"
      "v_cvt_pk_bf16_f32 v112, v112, v113\n"
      "v_mfma_f32_16x16x32_bf16 v[128:131], v[188:191], a[232:235], v[136:139]\n"
      "ds_write_b32 v31, v214 offset:4608\n"
      "v_cvt_pk_bf16_f32 v176, v176, v177\n"
      "v_cvt_pk_bf16_f32 v113, v114, v115\n"
      "v_mfma_f32_16x16x32_bf16 v[114:117], v[188:191], v[252:255], v[184:187]\n"
      "v_cvt_pk_bf16_f32 v174, v174, v175\n"
      "v_cvt_pk_bf16_f32 v247, v160, v161\n"
      "v_cvt_pk_bf16_f32 v109, v110, v111\n"
      "v_accvgpr_write_b32 a228, v210\n"
      "v_mfma_f32_16x16x32_bf16 v[160:163], v[152:155], v[224:227], v[120:123]\n"
      "ds_write_b64 v4, v[112:113] offset:512\n"
      "v_mfma_f32_16x16x32_bf16 v[168:171], v[188:191], v[248:251], v[136:139]\n"
      "s_waitcnt lgkmcnt(3)\n"
      "v_accvgpr_write_b32 a230, v148\n"
      "v_cvt_pk_bf16_f32 v244, v216, v217\n"
      "v_mfma_f32_16x16x32_bf16 v[136:139], v[152:155], a[236:239], v[164:167]\n"
      "ds_read_b128 v[100:103], v14 offset:64\n"
      "v_cvt_pk_bf16_f32 v128, v128, v129\n"
      "v_cvt_pk_bf16_f32 v132, v132, v133\n"
      "v_cvt_pk_bf16_f32 v226, v160, v161\n"
      "v_cvt_pk_bf16_f32 v126, v126, v127\n"
      "v_cvt_pk_bf16_f32 v111, v208, v209\n"
      "v_accvgpr_write_b32 a222, v198\n"
      "v_mfma_f32_16x16x32_bf16 v[148:151], v[152:155], v[228:231], v[164:167]\n"
      "ds_write_b64 v4, v[220:221] offset:256\n"
      "v_mfma_f32_16x16x32_bf16 v[220:223], v[188:191], v[228:231], v[184:187]\n"
      "v_cvt_pk_bf16_f32 v227, v162, v163\n"
      "v_cvt_pk_bf16_f32 v110, v206, v207\n"
      "v_cvt_pk_bf16_f32 v246, v158, v159\n"
      "v_mfma_f32_16x16x32_bf16 v[206:209], v[152:155], v[248:251], v[120:123]\n"
      "ds_write_b128 v3, v[108:111] offset:512\n"
      "v_cvt_pk_bf16_f32 v138, v138, v139\n"
      "v_mfma_f32_16x16x32_bf16 v[188:191], v[152:155], v[252:255], v[164:167]\n"
      "s_waitcnt lgkmcnt(2)\n"
      "v_accvgpr_write_b32 a250, v202\n"
      "v_accvgpr_write_b32 a254, v144\n"
      "v_cvt_pk_bf16_f32 v225, v142, v143\n"
      "v_cvt_pk_bf16_f32 v224, v140, v141\n"
      "v_cvt_pk_bf16_f32 v231, v150, v151\n"
      "v_accvgpr_write_b32 a252, v124\n"
      "v_accvgpr_write_b32 a251, v204\n"
      "v_cvt_pk_bf16_f32 v230, v148, v149\n"
      "v_accvgpr_write_b32 a239, v138\n"
      "v_cvt_pk_bf16_f32 v254, v188, v189\n"
      "v_accvgpr_write_b32 a236, v132\n"
      "v_cvt_pk_bf16_f32 v253, v116, v117\n"
      "v_cvt_pk_bf16_f32 v228, v220, v221\n"
      "v_cvt_pk_bf16_f32 v255, v190, v191\n"
      "v_cvt_pk_bf16_f32 v130, v130, v131\n"
      "v_cvt_pk_bf16_f32 v252, v114, v115\n"
      "v_accvgpr_write_b32 a234, v174\n"
      "v_accvgpr_write_b32 a253, v126\n"
      "v_accvgpr_write_b32 a237, v134\n"
      "v_accvgpr_write_b32 a235, v176\n"
      "v_cvt_pk_bf16_f32 v249, v170, v171\n"
      "v_accvgpr_write_b32 a223, v200\n"
      "v_cvt_pk_bf16_f32 v136, v136, v137\n"
      "v_accvgpr_write_b32 a238, v136\n"
      "v_accvgpr_write_b32 a233, v130\n"
      "v_cvt_pk_bf16_f32 v248, v168, v169\n"
      "v_cvt_pk_bf16_f32 v229, v222, v223\n"
      "v_cvt_pk_bf16_f32 v250, v206, v207\n"
      "v_accvgpr_write_b32 a255, v146\n"
      "v_mfma_f32_16x16x32_bf16 v[160:163], v[178:181], v[32:35], 0\n"
      "v_cvt_pk_bf16_f32 v245, v218, v219\n"
      "v_accvgpr_write_b32 a229, v212\n"
      "v_cvt_pk_bf16_f32 v251, v208, v209\n"
      "v_accvgpr_write_b32 a232, v128\n"
      "v_mfma_f32_16x16x32_bf16 v[160:163], v[100:103], v[32:35], v[160:163]\n"
      "s_add_u32 s10, s98, L_cold_%=-L_ipf0_%=\n"
      "s_addc_u32 s11, s99, 0\n"
      "s_mov_b32 s12, L_cend_%=-L_cold_%=\n"
      "s_sub_u32 s9, s9, 1280\n"
      "v_subrev_u32 v212, 115456, v9\n"
      "s_movk_i32 s13, 80\n"
      "v_cmp_gt_u32 s[14:15], s13, v212\n"
      "v_lshl_add_u32 v212, v212, 3, s9\n"
      "s_cmp_lt_u32 s40, 2\n"
      "s_cselect_b64 s[0:1], s[16:17], s[10:11]\n"
      "s_cselect_b32 s2, s18, s12\n"
      "s_cselect_b32 s8, -1, 0\n"
      "s_cselect_b64 s[14:15], 0, s[14:15]\n"
      "s_mov_b32 s3, s19\n"
      "s_add_u32 s6, s64, 16384\n"
      "s_cmp_lt_u32 s40, 2\n"
      "s_cselect_b32 s6, s6, 122112\n"
      "s_add_u32 s7, s64, 34816\n"
      "s_cmp_lt_u32 s40, 2\n"
      "s_cselect_b32 s7, s7, 122112\n"
      "s_and_b32 s5, s84, s8\n"
      "v_cndmask_b32 v15, v15, v212, s[14:15]\n"
      "s_waitcnt lgkmcnt(0)\n"
      "s_barrier\n"
      "s_or_b64 vcc, vcc, s[54:55]\n"
      "s_cbranch_vccnz L_disp1_%=\n"
      "s_nop 7\n"
      "s_nop 7\n"
      "L_loop1_%=:\n"
      "v_mfma_f32_16x16x32_bf16 v[112:115], v[178:181], v[194:197], 0\n"
      "ds_read_b128 v[156:159], v0 offset:18432\n"
      "s_add_u32 m0, s64, 0\n"
      "v_mfma_f32_16x16x32_bf16 v[112:115], v[100:103], v[76:79], v[112:115]\n"
      "ds_read_b128 v[108:111], v9\n"
      "v_mfma_f32_16x16x32_bf16 v[126:129], v[178:181], v[96:99], 0\n"
      "ds_read_b128 v[182:185], v1 offset:18432\n"
      "v_readlane_b32 s45, v37, s43\n"
      "v_mfma_f32_16x16x32_bf16 v[126:129], v[100:103], v[80:83], v[126:129]\n"
      "buffer_load_dwordx4 v16, s[16:19], s84 offen  lds\n"
      "v_mfma_f32_16x16x32_bf16 v[152:155], v[178:181], v[104:107], 0\n"
      "ds_read_b128 v[202:205], v0 offset:24576\n"
      "v_readlane_b32 s46, v37, s42\n"
      "v_readlane_b32 s47, v38, s43\n"
      "v_mfma_f32_16x16x32_bf16 v[152:155], v[100:103], v[84:87], v[152:155]\n"
      "ds_read_b128 v[198:201], v1 offset:24576\n"
      "s_waitcnt lgkmcnt(4)\n"
      "v_mfma_f32_16x16x32_bf16 v[114:117], a[0:3], v[156:159], 0\n"
      "ds_read_b128 v[206:209], v1 offset:20480\n"
      "s_add_u32 m0, s64, 4096\n"
      "v_mfma_f32_16x16x32_bf16 v[134:137], a[24:27], v[156:159], 0\n"
      "s_waitcnt lgkmcnt(4)\n"
      "v_add_f32 v108, v108, v109\n"
      "v_add_f32 v110, v110, v111\n"
      "v_mfma_f32_16x16x32_bf16 v[130:133], a[48:51], v[156:159], 0\n"
      "ds_read_b128 v[144:147], v0 offset:30720\n"
      "v_add_f32_e64 v108, v108, v110\n"
      "v_mfma_f32_16x16x32_bf16 v[190:193], a[72:75], v[156:159], 0\n"
      "buffer_load_dwordx4 v17, s[16:19], s84 offen  lds\n"
      "v_mfma_f32_16x16x32_bf16 v[100:103], a[96:99], v[156:159], 0\n"
      "s_add_u32 m0, s64, 8192\n"
      "v_rsq_f32 v108, v108\n"
      "v_mfma_f32_16x16x32_bf16 v[138:141], a[120:123], v[156:159], 0\n"
      "s_waitcnt lgkmcnt(2)\n"
      "v_mul_f32 v108, 0x415db3d7, v108\n"
      "v_mov_b32 v110, v108\n"
      "v_mfma_f32_16x16x32_bf16 v[114:117], a[4:7], v[182:185], v[114:117]\n"
      "s_nop 0\n"
      "v_permlane32_swap_b32_e64 v108, v110\n"
      "v_mfma_f32_16x16x32_bf16 v[114:117], a[8:11], v[202:205], v[114:117]\n"
      "ds_read_b128 v[170:173], v1 offset:30720\n"
      "v_mul_f32_e64 v220, v110, v220\n"
      "v_mfma_f32_16x16x32_bf16 v[134:137], a[28:31], v[182:185], v[134:137]\n"
      "buffer_load_dwordx4 v18, s[16:19], s84 offen  lds\n"
      "v_mfma_f32_16x16x32_bf16 v[134:137], a[32:35], v[202:205], v[134:137]\n"
      "v_mov_b32_dpp v160, v160 quad_perm:[0,0,0,0] row_mask:0xf bank_mask:0xf\n"
      "v_mov_b32_e64 v109, v108\n"
      "v_mfma_f32_16x16x32_bf16 v[130:133], a[52:55], v[182:185], v[130:133]\n"
      "v_mul_f32 v221, v110, v221\n"
      "v_mul_f32 v223, v110, v223\n"
      "v_mfma_f32_16x16x32_bf16 v[130:133], a[56:59], v[202:205], v[130:133]\n"
      "s_add_u32 m0, s64, 12288\n"
      "v_rcp_f32 v160, v160\n"
      "v_mfma_f32_16x16x32_bf16 v[190:193], a[76:79], v[182:185], v[190:193]\n"
      "v_permlane16_swap_b32 v108, v109\n"
      "v_mfma_f32_16x16x32_bf16 v[190:193], a[80:83], v[202:205], v[190:193]\n"
      "buffer_load_dwordx4 v19, s[16:19], s84 offen  lds\n"
      "v_mfma_f32_16x16x32_bf16 v[100:103], a[100:103], v[182:185], v[100:103]\n"
      "v_cndmask_b32 v112, v112, v126, s[58:59]\n"
      "v_mul_f32 v125, v109, v125\n"
      "v_mfma_f32_16x16x32_bf16 v[100:103], a[104:107], v[202:205], v[100:103]\n"
      "v_mul_f32 v213, v109, v213\n"
      "v_mul_f32 v149, v108, v149\n"
      "v_mfma_f32_16x16x32_bf16 v[138:141], a[124:127], v[182:185], v[138:141]\n"
      "s_mov_b32 m0, s6\n"
      "v_cvt_pk_bf16_f32 v220, v220, v221\n"
      "v_cndmask_b32 v112, v112, v152, s[60:61]\n"
      "v_mfma_f32_16x16x32_bf16 v[138:141], a[128:131], v[202:205], v[138:141]\n"
      "s_waitcnt lgkmcnt(0)\n"
      "v_mul_f32 v176, v108, v176\n"
      "v_mul_f32_e64 v188, v110, v188\n"
      "v_mfma_f32_16x16x32_bf16 v[114:117], a[12:15], v[198:201], v[114:117]\n"
      "buffer_load_dwordx4 v15, s[0:3], s5 offen  lds\n"
      "v_mfma_f32_16x16x32_bf16 v[114:117], a[16:19], v[144:147], v[114:117]\n"
      "ds_read_b128 v[178:181], v0 offset:20480\n"
      "v_mul_f32_e64 v189, v110, v189\n"
      "v_mfma_f32_16x16x32_bf16 v[114:117], a[20:23], v[170:173], v[114:117]\n"
      "v_mul_f32 v210, v109, v210\n"
      "v_mul_f32 v175, v108, v175\n"
      "v_mfma_f32_16x16x32_bf16 v[134:137], a[36:39], v[198:201], v[134:137]\n"
      "v_mul_f32 v174, v108, v174\n"
      "v_mul_f32 v215, v108, v215\n"
      "v_mfma_f32_16x16x32_bf16 v[134:137], a[40:43], v[144:147], v[134:137]\n"
      "v_mul_f32 v112, v160, v112\n"
      "v_mul_f32 v151, v108, v151\n"
      "v_mfma_f32_16x16x32_bf16 v[134:137], a[44:47], v[170:173], v[134:137]\n"
      "buffer_load_dwordx4 v[162:165], v36, s[28:31], s86 offen\n"
      "v_mfma_f32_16x16x32_bf16 v[130:133], a[60:63], v[198:201], v[130:133]\n"
      "v_mul_f32 v211, v109, v211\n"
      "v_mul_f32 v216, v108, v216\n"
      "v_mfma_f32_16x16x32_bf16 v[130:133], a[64:67], v[144:147], v[130:133]\n"
      "v_mul_f32 v177, v108, v177\n"
      "v_mul_f32 v222, v110, v222\n"
      "v_mfma_f32_16x16x32_bf16 v[130:133], a[68:71], v[170:173], v[130:133]\n"
      "v_fma_f32 v142, v114, v114, s51\n"
      "v_fmac_f32 v142, v115, v115\n"
      "v_mfma_f32_16x16x32_bf16 v[190:193], a[84:87], v[198:201], v[190:193]\n"
      "v_mul_f32 v168, v110, v168\n"
      "v_mul_f32 v214, v108, v214\n"
      "v_mfma_f32_16x16x32_bf16 v[190:193], a[88:91], v[144:147], v[190:193]\n"
      "v_cvt_pk_bf16_f32 v221, v222, v223\n"
      "v_mul_f32 v187, v110, v187\n"
      "v_mfma_f32_16x16x32_bf16 v[190:193], a[92:95], v[170:173], v[190:193]\n"
      "buffer_load_dword v90, v29, s[28:31], s86 offen\n"
      "v_mfma_f32_16x16x32_bf16 v[100:103], a[108:111], v[198:201], v[100:103]\n"
      "v_fmac_f32 v142, v116, v116\n"
      "v_mul_f32 v167, v110, v167\n"
      "v_mfma_f32_16x16x32_bf16 v[100:103], a[112:115], v[144:147], v[100:103]\n"
      "ds_read_b128 v[152:155], v1 offset:26624\n"
      "v_cvt_pk_bf16_f32 v174, v174, v175\n"
      "v_mfma_f32_16x16x32_bf16 v[100:103], a[116:119], v[170:173], v[100:103]\n"
      "v_fmac_f32_e64 v142, v117, v117\n"
      "v_fma_f32 v143, v190, v190, s51\n"
      "v_mfma_f32_16x16x32_bf16 v[138:141], a[132:135], v[198:201], v[138:141]\n"
      "v_fmac_f32_e64 v143, v191, v191\n"
      "v_cvt_pk_bf16_f32 v175, v176, v177\n"
      "v_mfma_f32_16x16x32_bf16 v[138:141], a[136:139], v[144:147], v[138:141]\n"
      "v_cvt_pk_bf16_f32 v112, v112, v112\n"
      "v_mul_f32 v166, v110, v166\n"
      "v_mfma_f32_16x16x32_bf16 v[138:141], a[140:143], v[170:173], v[138:141]\n"
      "v_fmac_f32 v143, v192, v192\n"
      "v_mul_f32 v119, v109, v119\n"
      "v_mfma_f32_16x16x32_bf16 v[194:197], v[156:159], a[144:147], 0\n"
      "v_cvt_pk_bf16_f32 v176, v214, v215\n"
      "v_fmac_f32 v143, v193, v193\n"
      "v_mfma_f32_16x16x32_bf16 v[194:197], v[182:185], a[148:151], v[194:197]\n"
      "ds_write_b16 v12, v112\n"
      "v_mfma_f32_16x16x32_bf16 v[194:197], v[202:205], a[152:155], v[194:197]\n"
      "v_mul_f32 v186, v110, v186\n"
      "v_mul_f32 v118, v109, v118\n"
      "v_mfma_f32_16x16x32_bf16 v[194:197], v[198:201], a[156:159], v[194:197]\n"
      "v_cvt_pk_bf16_f32 v222, v166, v167\n"
      "v_mul_f32 v217, v108, v217\n"
      "v_mfma_f32_16x16x32_bf16 v[194:197], v[144:147], a[160:163], v[194:197]\n"
      "v_mul_f32 v124, v109, v124\n"
      "v_fmac_f32_e64 v143, v100, v100\n"
      "v_mfma_f32_16x16x32_bf16 v[194:197], v[170:173], a[164:167], v[194:197]\n"
      "v_mul_f32 v148, v108, v148\n"
      "v_mul_f32 v212, v109, v212\n"
      "v_mfma_f32_16x16x32_bf16 v[96:99], v[156:159], a[168:171], 0\n"
      "v_cvt_pk_bf16_f32 v186, v186, v187\n"
      "v_fmac_f32 v142, v134, v134\n"
      "v_mfma_f32_16x16x32_bf16 v[96:99], v[182:185], a[172:175], v[96:99]\n"
      "v_mul_f32 v122, v109, v122\n"
      "v_mul_f32 v169, v110, v169\n"
      "v_mfma_f32_16x16x32_bf16 v[96:99], v[202:205], a[176:179], v[96:99]\n"
      "v_fmac_f32 v143, v101, v101\n"
      "v_fmac_f32_e64 v142, v135, v135\n"
      "v_mfma_f32_16x16x32_bf16 v[96:99], v[198:201], a[180:183], v[96:99]\n"
      "ds_read_b128 v[110:113], v0 offset:26624\n"
      "v_cvt_pk_bf16_f32 v177, v216, v217\n"
      "v_mfma_f32_16x16x32_bf16 v[96:99], v[144:147], a[184:187], v[96:99]\n"
      "v_fmac_f32 v142, v136, v136\n"
      "v_mul_f32 v121, v109, v121\n"
      "v_mfma_f32_16x16x32_bf16 v[96:99], v[170:173], a[188:191], v[96:99]\n"
      "ds_write_b128 v5, v[174:177] offset:18432\n"
      "v_mfma_f32_16x16x32_bf16 v[104:107], v[156:159], a[192:195], 0\n"
      "ds_read_b128 v[214:217], v1 offset:32768\n"
      "v_fmac_f32_e64 v143, v102, v102\n"
      "v_mfma_f32_16x16x32_bf16 v[104:107], v[182:185], a[196:199], v[104:107]\n"
      "ds_read_b128 v[182:185], v0 offset:32768\n"
      "v_cvt_pk_bf16_f32 v118, v118, v119\n"
      "v_mfma_f32_16x16x32_bf16 v[104:107], v[202:205], a[200:203], v[104:107]\n"
      "v_cvt_pk_bf16_f32 v210, v210, v211\n"
      "v_fmac_f32 v142, v137, v137\n"
      "v_mfma_f32_16x16x32_bf16 v[104:107], v[198:201], a[204:207], v[104:107]\n"
      "ds_read_b128 v[156:159], v9 offset:4608\n"
      "v_mul_f32 v123, v109, v123\n"
      "v_mfma_f32_16x16x32_bf16 v[104:107], v[144:147], a[208:211], v[104:107]\n"
      "v_cvt_pk_bf16_f32 v194, v194, v195\n"
      "v_fmac_f32 v143, v103, v103\n"
      "v_mfma_f32_16x16x32_bf16 v[104:107], v[170:173], a[212:215], v[104:107]\n"
      "s_waitcnt lgkmcnt(4)\n"
      "v_fmac_f32 v143, v138, v138\n"
      "v_fmac_f32 v142, v130, v130\n"
      "v_mfma_f32_16x16x32_bf16 v[202:205], a[0:3], v[178:181], 0\n"
      "v_cvt_pk_bf16_f32 v223, v168, v169\n"
      "v_mul_f32_e64 v150, v108, v150\n"
      "v_mfma_f32_16x16x32_bf16 v[202:205], a[4:7], v[206:209], v[202:205]\n"
      "ds_read_b128 v[174:177], v1 offset:22528\n"
      "v_cvt_pk_bf16_f32 v195, v196, v197\n"
      "v_mfma_f32_16x16x32_bf16 v[202:205], a[8:11], v[110:113], v[202:205]\n"
      "s_waitcnt lgkmcnt(2)\n"
      "v_fmac_f32 v143, v139, v139\n"
      "v_cvt_pk_bf16_f32 v148, v148, v149\n"
      "v_mfma_f32_16x16x32_bf16 v[202:205], a[12:15], v[152:155], v[202:205]\n"
      "s_or_b32 s95, s45, s46\n"
      "v_fmac_f32 v142, v131, v131\n"
      "v_cvt_pk_bf16_f32 v96, v96, v97\n"
      "v_mfma_f32_16x16x32_bf16 v[202:205], a[16:19], v[182:185], v[202:205]\n"
      "v_cvt_pk_bf16_f32 v187, v188, v189\n"
      "v_fmac_f32 v142, v132, v132\n"
      "v_mfma_f32_16x16x32_bf16 v[202:205], a[20:23], v[214:217], v[202:205]\n"
      "s_or_b32 s95, s95, s48\n"
      "v_fmac_f32_e64 v143, v140, v140\n"
      "v_cvt_pk_bf16_f32 v104, v104, v105\n"
      "v_mfma_f32_16x16x32_bf16 v[166:169], a[24:27], v[178:181], 0\n"
      "ds_write_b64 v6, v[186:187] offset:30720\n"
      "v_mfma_f32_16x16x32_bf16 v[166:169], a[28:31], v[206:209], v[166:169]\n"
      "v_fmac_f32 v142, v133, v133\n"
      "v_fmac_f32 v143, v141, v141\n"
      "v_mfma_f32_16x16x32_bf16 v[166:169], a[32:35], v[110:113], v[166:169]\n"
      "ds_read_b128 v[144:147], v1 offset:34816\n"
      "v_cvt_pk_bf16_f32 v105, v106, v107\n"
      "v_mfma_f32_16x16x32_bf16 v[166:169], a[36:39], v[152:155], v[166:169]\n"
      "v_fma_f32 v76, v202, v202, s51\n"
      "v_cvt_pk_bf16_f32 v211, v212, v213\n"
      "v_mfma_f32_16x16x32_bf16 v[166:169], a[40:43], v[182:185], v[166:169]\n"
      "v_permlane16_swap_b32 v142, v143\n"
      "v_mfma_f32_16x16x32_bf16 v[166:169], a[44:47], v[214:217], v[166:169]\n"
      "v_cvt_pk_bf16_f32 v149, v150, v151\n"
      "v_add_f32 v142, v142, v143\n"
      "v_mfma_f32_16x16x32_bf16 v[186:189], a[48:51], v[178:181], 0\n"
      "ds_read_b128 v[198:201], v0 offset:22528\n"
      "v_mul_f32_e64 v120, v109, v120\n"
      "v_mfma_f32_16x16x32_bf16 v[186:189], a[52:55], v[206:209], v[186:189]\n"
      "ds_write_b64 v6, v[148:149] offset:18432\n"
      "v_mfma_f32_16x16x32_bf16 v[186:189], a[56:59], v[110:113], v[186:189]\n"
      "s_waitcnt lgkmcnt(2)\n"
      "v_fmac_f32 v76, v203, v203\n"
      "v_mov_b32_e64 v143, v142\n"
      "v_mfma_f32_16x16x32_bf16 v[186:189], a[60:63], v[152:155], v[186:189]\n"
      "s_nop 0\n"
      "v_permlane32_swap_b32 v142, v143\n"
      "v_mfma_f32_16x16x32_bf16 v[186:189], a[64:67], v[182:185], v[186:189]\n"
      "v_or3_b32 v156, v156, v157, v158\n"
      "v_fmac_f32 v76, v204, v204\n"
      "v_mfma_f32_16x16x32_bf16 v[186:189], a[68:71], v[214:217], v[186:189]\n"
      "v_fmac_f32 v76, v205, v205\n"
      "v_add_f32 v142, v142, v143\n"
      "v_mfma_f32_16x16x32_bf16 v[126:129], a[72:75], v[178:181], 0\n"
      "v_rsq_f32 v142, v142\n"
      "v_mfma_f32_16x16x32_bf16 v[126:129], a[76:79], v[206:209], v[126:129]\n"
      "v_mov_b32 v143, v142\n"
      "v_fmac_f32 v76, v166, v166\n"
      "v_mfma_f32_16x16x32_bf16 v[126:129], a[80:83], v[110:113], v[126:129]\n"
      "s_cmp_ge_u32 s95, 0x40000000\n"
      "s_cselect_b32 s94, -1, -2\n"
      "v_cvt_pk_bf16_f32 v213, v124, v125\n"
      "v_mfma_f32_16x16x32_bf16 v[126:129], a[84:87], v[152:155], v[126:129]\n"
      "ds_write_b128 v5, v[220:223] offset:30720\n"
      "v_mfma_f32_16x16x32_bf16 v[126:129], a[88:91], v[182:185], v[126:129]\n"
      "v_permlane16_swap_b32 v142, v143\n"
      "v_mfma_f32_16x16x32_bf16 v[126:129], a[92:95], v[214:217], v[126:129]\n"
      "v_fmac_f32 v76, v167, v167\n"
      "v_mul_f32_e64 v142, s50, v142\n"
      "v_mfma_f32_16x16x32_bf16 v[218:221], a[96:99], v[178:181], 0\n"
      "v_cvt_pk_bf16_f32 v97, v98, v99\n"
      "v_cvt_pk_bf16_f32 v119, v120, v121\n"
      "v_mfma_f32_16x16x32_bf16 v[218:221], a[100:103], v[206:209], v[218:221]\n"
      "v_fmac_f32 v76, v168, v168\n"
      "v_mul_f32 v135, v142, v135\n"
      "v_mfma_f32_16x16x32_bf16 v[218:221], a[104:107], v[110:113], v[218:221]\n"
      "v_fma_f32 v98, v126, v126, s51\n"
      "v_cvt_pk_bf16_f32 v212, v122, v123\n"
      "v_mfma_f32_16x16x32_bf16 v[218:221], a[108:111], v[152:155], v[218:221]\n"
      "v_mul_f32 v192, v143, v192\n"
      "v_mul_f32 v139, v143, v139\n"
      "v_mfma_f32_16x16x32_bf16 v[218:221], a[112:115], v[182:185], v[218:221]\n"
      "v_mul_f32 v190, v143, v190\n"
      "v_mul_f32 v130, v142, v130\n"
      "v_mfma_f32_16x16x32_bf16 v[218:221], a[116:119], v[214:217], v[218:221]\n"
      "v_fmac_f32 v98, v127, v127\n"
      "v_mul_f32 v103, v143, v103\n"
      "v_mfma_f32_16x16x32_bf16 v[170:173], a[120:123], v[178:181], 0\n"
      "ds_read_b128 v[148:151], v1 offset:28672\n"
      "v_fmac_f32_e64 v98, v128, v128\n"
      "v_mfma_f32_16x16x32_bf16 v[170:173], a[124:127], v[206:209], v[170:173]\n"
      "ds_write_b128 v5, v[210:213] offset:24576\n"
      "v_mfma_f32_16x16x32_bf16 v[170:173], a[128:131], v[110:113], v[170:173]\n"
      "v_fmac_f32 v98, v129, v129\n"
      "v_fmac_f32 v98, v218, v218\n"
      "v_mfma_f32_16x16x32_bf16 v[170:173], a[132:135], v[152:155], v[170:173]\n"
      "v_fmac_f32 v98, v219, v219\n"
      "v_fmac_f32 v76, v169, v169\n"
      "v_mfma_f32_16x16x32_bf16 v[170:173], a[136:139], v[182:185], v[170:173]\n"
      "v_mul_f32 v140, v143, v140\n"
      "v_mul_f32 v117, v142, v117\n"
      "v_mfma_f32_16x16x32_bf16 v[170:173], a[140:143], v[214:217], v[170:173]\n"
      "v_mul_f32 v133, v142, v133\n"
      "v_mul_f32 v114, v142, v114\n"
      "v_mfma_f32_16x16x32_bf16 v[106:109], v[178:181], a[144:147], 0\n"
      "ds_write_b64 v6, v[118:119] offset:24576\n"
      "v_mfma_f32_16x16x32_bf16 v[106:109], v[206:209], a[148:151], v[106:109]\n"
      "ds_read_b128 v[210:213], v0 offset:34816\n"
      "v_mul_f32 v102, v143, v102\n"
      "v_mfma_f32_16x16x32_bf16 v[106:109], v[110:113], a[152:155], v[106:109]\n"
      "v_mul_f32 v193, v143, v193\n"
      "v_mul_f32 v116, v142, v116\n"
      "v_mfma_f32_16x16x32_bf16 v[106:109], v[152:155], a[156:159], v[106:109]\n"
      "v_fmac_f32 v98, v220, v220\n"
      "v_mul_f32_e64 v100, v143, v100\n"
      "v_mfma_f32_16x16x32_bf16 v[106:109], v[182:185], a[160:163], v[106:109]\n"
      "v_bitop3_b32 v156, v156, v159, s33 bitop3:0xa8\n"
      "v_mul_f32 v115, v142, v115\n"
      "v_mfma_f32_16x16x32_bf16 v[106:109], v[214:217], a[164:167], v[106:109]\n"
      "v_fmac_f32 v76, v186, v186\n"
      "v_fmac_f32_e64 v98, v221, v221\n"
      "v_mfma_f32_16x16x32_bf16 v[158:161], v[178:181], a[168:171], 0\n"
      "v_mul_f32 v131, v142, v131\n"
      "v_mfma_f32_16x16x32_bf16 v[158:161], v[206:209], a[172:175], v[158:161]\n"
      "v_fmac_f32 v76, v187, v187\n"
      "v_cvt_pk_bf16_f32 v130, v130, v131\n"
      "v_mfma_f32_16x16x32_bf16 v[158:161], v[110:113], a[176:179], v[158:161]\n"
      "v_mul_f32 v191, v143, v191\n"
      "v_mul_f32 v136, v142, v136\n"
      "v_mfma_f32_16x16x32_bf16 v[158:161], v[152:155], a[180:183], v[158:161]\n"
      "ds_read_b128 v[118:121], v0 offset:28672\n"
      "v_fmac_f32_e64 v76, v188, v188\n"
      "v_mfma_f32_16x16x32_bf16 v[158:161], v[182:185], a[184:187], v[158:161]\n"
      "v_cvt_pk_bf16_f32 v114, v114, v115\n"
      "v_cmp_ne_u32 s[56:57], 0, v156\n"
      "v_mfma_f32_16x16x32_bf16 v[158:161], v[214:217], a[188:191], v[158:161]\n"
      "v_mul_f32 v138, v143, v138\n"
      "v_fmac_f32 v98, v170, v170\n"
      "v_mfma_f32_16x16x32_bf16 v[122:125], v[178:181], a[192:195], 0\n"
      "v_fmac_f32 v76, v189, v189\n"
      "v_fmac_f32 v98, v171, v171\n"
      "v_mfma_f32_16x16x32_bf16 v[122:125], v[206:209], a[196:199], v[122:125]\n"
      "v_cvt_pk_bf16_f32 v190, v190, v191\n"
      "v_cvt_pk_bf16_f32 v115, v116, v117\n"
      "v_mfma_f32_16x16x32_bf16 v[122:125], v[110:113], a[200:203], v[122:125]\n"
      "ds_read_b128 v[178:181], v2\n"
      "v_fmac_f32 v98, v172, v172\n"
      "v_mfma_f32_16x16x32_bf16 v[122:125], v[152:155], a[204:207], v[122:125]\n"
      "v_fmac_f32 v98, v173, v173\n"
      "v_mul_f32_e64 v134, v142, v134\n"
      "v_mfma_f32_16x16x32_bf16 v[122:125], v[182:185], a[208:211], v[122:125]\n"
      "v_mul_f32 v132, v142, v132\n"
      "v_cvt_pk_bf16_f32 v196, v106, v107\n"
      "v_mfma_f32_16x16x32_bf16 v[122:125], v[214:217], a[212:215], v[122:125]\n"
      "s_waitcnt lgkmcnt(1)\n"
      "v_permlane16_swap_b32 v76, v98\n"
      "v_mfma_f32_16x16x32_bf16 v[182:185], a[0:3], v[198:201], 0\n"
      "s_cmp_gt_u32 s42, s44\n"
      "s_cselect_b32 s95, s94, s95\n"
      "v_add_f32 v76, v76, v98\n"
      "v_mfma_f32_16x16x32_bf16 v[182:185], a[4:7], v[174:177], v[182:185]\n"
      "v_mul_f32 v141, v143, v141\n"
      "v_mov_b32 v98, v76\n"
      "v_mfma_f32_16x16x32_bf16 v[182:185], a[8:11], v[118:121], v[182:185]\n"
      "s_nop 0\n"
      "v_permlane32_swap_b32 v76, v98\n"
      "v_mfma_f32_16x16x32_bf16 v[182:185], a[12:15], v[148:151], v[182:185]\n"
      "v_cvt_pk_bf16_f32 v197, v108, v109\n"
      "v_add_f32 v76, v76, v98\n"
      "v_mfma_f32_16x16x32_bf16 v[182:185], a[16:19], v[210:213], v[182:185]\n"
      "v_rsq_f32 v76, v76\n"
      "v_mfma_f32_16x16x32_bf16 v[182:185], a[20:23], v[144:147], v[182:185]\n"
      "v_cvt_pk_bf16_f32 v131, v132, v133\n"
      "v_mov_b32 v98, v76\n"
      "v_mfma_f32_16x16x32_bf16 v[110:113], a[24:27], v[198:201], 0\n"
      "s_nop 0\n"
      "v_permlane16_swap_b32_e64 v76, v98\n"
      "v_mfma_f32_16x16x32_bf16 v[110:113], a[28:31], v[174:177], v[110:113]\n"
      "v_cvt_pk_bf16_f32 v116, v134, v135\n"
      "v_cvt_pk_bf16_f32 v99, v160, v161\n"
      "v_mfma_f32_16x16x32_bf16 v[110:113], a[32:35], v[118:121], v[110:113]\n"
      "v_mul_f32 v221, v98, v221\n"
      "v_mul_f32 v101, v143, v101\n"
      "v_mfma_f32_16x16x32_bf16 v[110:113], a[36:39], v[148:151], v[110:113]\n"
      "v_mul_f32 v76, s50, v76\n"
      "v_mul_f32 v170, v98, v170\n"
      "v_mfma_f32_16x16x32_bf16 v[110:113], a[40:43], v[210:213], v[110:113]\n"
      "v_mul_f32 v137, v142, v137\n"
      "v_mul_f32 v171, v98, v171\n"
      "v_mfma_f32_16x16x32_bf16 v[110:113], a[44:47], v[144:147], v[110:113]\n"
      "v_fma_f32 v84, v182, v182, s51\n"
      "v_mul_f32_e64 v127, v98, v127\n"
      "v_mfma_f32_16x16x32_bf16 v[206:209], a[48:51], v[198:201], 0\n"
      "v_cvt_pk_bf16_f32 v117, v136, v137\n"
      "v_mul_f32 v189, v76, v189\n"
      "v_mfma_f32_16x16x32_bf16 v[206:209], a[52:55], v[174:177], v[206:209]\n"
      "v_mul_f32 v167, v76, v167\n"
      "v_mul_f32 v187, v76, v187\n"
      "v_mfma_f32_16x16x32_bf16 v[206:209], a[56:59], v[118:121], v[206:209]\n"
      "v_mul_f32 v218, v98, v218\n"
      "v_fmac_f32 v84, v183, v183\n"
      "v_mfma_f32_16x16x32_bf16 v[206:209], a[60:63], v[148:151], v[206:209]\n"
      "v_mul_f32 v126, v98, v126\n"
      "v_mul_f32 v128, v98, v128\n"
      "v_mfma_f32_16x16x32_bf16 v[206:209], a[64:67], v[210:213], v[206:209]\n"
      "v_fmac_f32 v84, v184, v184\n"
      "v_fmac_f32 v84, v185, v185\n"
      "v_mfma_f32_16x16x32_bf16 v[206:209], a[68:71], v[144:147], v[206:209]\n"
      "v_mul_f32 v186, v76, v186\n"
      "v_cvt_pk_bf16_f32 v107, v124, v125\n"
      "v_mfma_f32_16x16x32_bf16 v[152:155], a[72:75], v[198:201], 0\n"
      "v_mul_f32 v220, v98, v220\n"
      "v_fmac_f32 v84, v110, v110\n"
      "v_mfma_f32_16x16x32_bf16 v[152:155], a[76:79], v[174:177], v[152:155]\n"
      "v_fmac_f32 v84, v111, v111\n"
      "v_mul_f32 v219, v98, v219\n"
      "v_mfma_f32_16x16x32_bf16 v[152:155], a[80:83], v[118:121], v[152:155]\n"
      "v_mul_f32_e64 v173, v98, v173\n"
      "v_cvt_pk_bf16_f32 v126, v126, v127\n"
      "v_mfma_f32_16x16x32_bf16 v[152:155], a[84:87], v[148:151], v[152:155]\n"
      "v_fmac_f32 v84, v112, v112\n"
      "v_mul_f32 v129, v98, v129\n"
      "v_mfma_f32_16x16x32_bf16 v[152:155], a[88:91], v[210:213], v[152:155]\n"
      "v_cvt_pk_bf16_f32 v191, v192, v193\n"
      "v_cvt_pk_bf16_f32 v106, v122, v123\n"
      "v_mfma_f32_16x16x32_bf16 v[152:155], a[92:95], v[144:147], v[152:155]\n"
      "v_cvt_pk_bf16_f32 v138, v138, v139\n"
      "v_cvt_pk_bf16_f32 v127, v128, v129\n"
      "v_mfma_f32_16x16x32_bf16 v[122:125], a[96:99], v[198:201], 0\n"
      "v_cvt_pk_bf16_f32 v170, v170, v171\n"
      "v_mul_f32_e64 v172, v98, v172\n"
      "v_mfma_f32_16x16x32_bf16 v[122:125], a[100:103], v[174:177], v[122:125]\n"
      "v_cvt_pk_bf16_f32 v129, v220, v221\n"
      "v_mul_f32 v188, v76, v188\n"
      "v_mfma_f32_16x16x32_bf16 v[122:125], a[104:107], v[118:121], v[122:125]\n"
      "v_fma_f32 v160, v152, v152, s51\n"
      "v_fmac_f32 v84, v113, v113\n"
      "v_mfma_f32_16x16x32_bf16 v[122:125], a[108:111], v[148:151], v[122:125]\n"
      "v_fmac_f32 v160, v153, v153\n"
      "v_mul_f32 v166, v76, v166\n"
      "v_mfma_f32_16x16x32_bf16 v[122:125], a[112:115], v[210:213], v[122:125]\n"
      "v_fmac_f32 v84, v206, v206\n"
      "v_fmac_f32 v160, v154, v154\n"
      "v_mfma_f32_16x16x32_bf16 v[122:125], a[116:119], v[144:147], v[122:125]\n"
      "v_fmac_f32 v160, v155, v155\n"
      "v_cvt_pk_bf16_f32 v171, v172, v173\n"
      "v_mfma_f32_16x16x32_bf16 v[132:135], a[120:123], v[198:201], 0\n"
      "v_fmac_f32 v84, v207, v207\n"
      "v_cvt_pk_bf16_f32 v139, v140, v141\n"
      "v_mfma_f32_16x16x32_bf16 v[132:135], a[124:127], v[174:177], v[132:135]\n"
      "ds_read_b128 v[140:143], v2 offset:6144\n"
      "v_cvt_pk_bf16_f32 v193, v102, v103\n"
      "v_mfma_f32_16x16x32_bf16 v[132:135], a[128:131], v[118:121], v[132:135]\n"
      "v_mul_f32_e64 v204, v76, v204\n"
      "v_cvt_pk_bf16_f32 v128, v218, v219\n"
      "v_mfma_f32_16x16x32_bf16 v[132:135], a[132:135], v[148:151], v[132:135]\n"
      "v_cvt_pk_bf16_f32 v98, v158, v159\n"
      "v_fmac_f32 v84, v208, v208\n"
      "v_mfma_f32_16x16x32_bf16 v[132:135], a[136:139], v[210:213], v[132:135]\n"
      "v_fmac_f32 v160, v122, v122\n"
      "v_mul_f32_e64 v203, v76, v203\n"
      "v_mfma_f32_16x16x32_bf16 v[132:135], a[140:143], v[144:147], v[132:135]\n"
      "v_fmac_f32 v160, v123, v123\n"
      "v_fmac_f32 v160, v124, v124\n"
      "v_mfma_f32_16x16x32_bf16 v[216:219], v[198:201], a[144:147], 0\n"
      "s_add_u32 s42, s42, 1\n"
      "v_fmac_f32 v84, v209, v209\n"
      "v_fmac_f32 v160, v125, v125\n"
      "v_mfma_f32_16x16x32_bf16 v[216:219], v[174:177], a[148:151], v[216:219]\n"
      "v_cvt_pk_bf16_f32 v186, v186, v187\n"
      "v_fmac_f32 v160, v132, v132\n"
      "v_mfma_f32_16x16x32_bf16 v[216:219], v[118:121], a[152:155], v[216:219]\n"
      "v_fmac_f32 v160, v133, v133\n"
      "v_fmac_f32 v160, v134, v134\n"
      "v_mfma_f32_16x16x32_bf16 v[216:219], v[148:151], a[156:159], v[216:219]\n"
      "v_fmac_f32_e64 v160, v135, v135\n"
      "v_cvt_pk_bf16_f32 v192, v100, v101\n"
      "v_mfma_f32_16x16x32_bf16 v[216:219], v[210:213], a[160:163], v[216:219]\n"
      "v_permlane16_swap_b32 v84, v160\n"
      "v_mfma_f32_16x16x32_bf16 v[216:219], v[144:147], a[164:167], v[216:219]\n"
      "v_add_f32 v84, v84, v160\n"
      "v_mul_f32_e64 v205, v76, v205\n"
      "v_mfma_f32_16x16x32_bf16 v[156:159], v[198:201], a[168:171], 0\n"
      "v_mov_b32 v160, v84\n"
      "v_cvt_pk_bf16_f32 v187, v188, v189\n"
      "v_mfma_f32_16x16x32_bf16 v[156:159], v[174:177], a[172:175], v[156:159]\n"
      "v_permlane32_swap_b32 v84, v160\n"
      "v_mfma_f32_16x16x32_bf16 v[156:159], v[118:121], a[176:179], v[156:159]\n"
      "v_add_f32 v84, v84, v160\n"
      "v_cvt_pk_bf16_f32 v77, v218, v219\n"
      "v_mfma_f32_16x16x32_bf16 v[156:159], v[148:151], a[180:183], v[156:159]\n"
      "v_rsq_f32 v84, v84\n"
      "v_mfma_f32_16x16x32_bf16 v[156:159], v[210:213], a[184:187], v[156:159]\n"
      "ds_read_u16 v86, v11 offset:1232\n"
      "v_mov_b32 v160, v84\n"
      "v_mfma_f32_16x16x32_bf16 v[156:159], v[144:147], a[188:191], v[156:159]\n"
      "s_nop 0\n"
      "v_permlane16_swap_b32 v84, v160\n"
      "v_mfma_f32_16x16x32_bf16 v[218:221], v[198:201], a[192:195], 0\n"
      "ds_read_b128 v[198:201], v2 offset:3072\n"
      "v_mul_f32 v125, v160, v125\n"
      "v_mfma_f32_16x16x32_bf16 v[218:221], v[174:177], a[196:199], v[218:221]\n"
      "v_mul_f32 v153, v160, v153\n"
      "v_mul_f32 v84, s50, v84\n"
      "v_mfma_f32_16x16x32_bf16 v[218:221], v[118:121], a[200:203], v[218:221]\n"
      "v_mul_f32 v202, v76, v202\n"
      "v_mul_f32 v122, v160, v122\n"
      "v_mfma_f32_16x16x32_bf16 v[218:221], v[148:151], a[204:207], v[218:221]\n"
      "v_mul_f32 v168, v76, v168\n"
      "v_mul_f32 v123, v160, v123\n"
      "v_mfma_f32_16x16x32_bf16 v[218:221], v[210:213], a[208:211], v[218:221]\n"
      "ds_read_b128 v[100:103], v2 offset:9216\n"
      "v_mul_f32_e64 v124, v160, v124\n"
      "v_mfma_f32_16x16x32_bf16 v[218:221], v[144:147], a[212:215], v[218:221]\n"
      "s_waitcnt lgkmcnt(2)\n"
      "v_mul_f32 v209, v84, v209\n"
      "v_mul_f32 v169, v76, v169\n"
      "v_mfma_f32_16x16x32_bf16 v[174:177], a[216:219], v[178:181], 0\n"
      "s_waitcnt lgkmcnt(1)\n"
      "v_mul_f32 v134, v160, v134\n"
      "v_mul_f32 v184, v84, v184\n"
      "v_mfma_f32_16x16x32_bf16 v[174:177], a[220:223], v[198:201], v[174:177]\n"
      "ds_read_b128 v[210:213], v2 offset:15360\n"
      "v_cvt_pk_bf16_f32 v76, v216, v217\n"
      "v_mfma_f32_16x16x32_bf16 v[174:177], a[224:227], v[140:143], v[174:177]\n"
      "ds_read_b128 v[144:147], v2 offset:12288\n"
      "v_mul_f32 v132, v160, v132\n"
      "v_mfma_f32_16x16x32_bf16 v[214:217], a[240:243], v[178:181], 0\n"
      "v_mul_f32 v185, v84, v185\n"
      "v_mul_f32 v133, v160, v133\n"
      "v_mfma_f32_16x16x32_bf16 v[214:217], a[244:247], v[198:201], v[214:217]\n"
      "v_mul_f32 v207, v84, v207\n"
      "v_cvt_pk_bf16_f32 v81, v158, v159\n"
      "v_mfma_f32_16x16x32_bf16 v[214:217], a[248:251], v[140:143], v[214:217]\n"
      "v_mul_f32 v155, v160, v155\n"
      "v_mul_f32 v152, v160, v152\n"
      "v_mfma_f32_16x16x32_bf16 v[148:151], v[232:235], v[178:181], 0\n"
      "s_waitcnt lgkmcnt(2)\n"
      "v_cvt_pk_bf16_f32 v152, v152, v153\n"
      "v_mul_f32 v154, v160, v154\n"
      "v_mfma_f32_16x16x32_bf16 v[148:151], v[236:239], v[198:201], v[148:151]\n"
      "ds_read_b64 v[108:109], v10 offset:1192\n"
      "v_cvt_pk_bf16_f32 v202, v202, v203\n"
      "v_mfma_f32_16x16x32_bf16 v[148:151], v[240:243], v[140:143], v[148:151]\n"
      "ds_read_b128 v[178:181], v2 offset:256\n"
      "v_mul_f32 v206, v84, v206\n"
      "v_mfma_f32_16x16x32_bf16 v[174:177], a[228:231], v[100:103], v[174:177]\n"
      "s_waitcnt lgkmcnt(2)\n"
      "v_mul_f32_e64 v182, v84, v182\n"
      "v_cvt_pk_bf16_f32 v203, v204, v205\n"
      "v_mfma_f32_16x16x32_bf16 v[174:177], a[232:235], v[144:147], v[174:177]\n"
      "ds_read_u16 v78, v11 offset:1168\n"
      "v_cvt_pk_bf16_f32 v153, v154, v155\n"
      "v_mfma_f32_16x16x32_bf16 v[174:177], a[236:239], v[210:213], v[174:177]\n"
      "v_cvt_pk_bf16_f32 v205, v168, v169\n"
      "v_mul_f32_e64 v135, v160, v135\n"
      "v_mfma_f32_16x16x32_bf16 v[214:217], a[252:255], v[100:103], v[214:217]\n"
      "v_cvt_pk_bf16_f32 v132, v132, v133\n"
      "v_cvt_pk_bf16_f32 v133, v134, v135\n"
      "v_mfma_f32_16x16x32_bf16 v[214:217], v[224:227], v[144:147], v[214:217]\n"
      "ds_read_b128 v[198:201], v10 offset:1200\n"
      "v_cvt_pk_bf16_f32 v155, v124, v125\n"
      "v_mfma_f32_16x16x32_bf16 v[214:217], v[228:231], v[210:213], v[214:217]\n"
      "v_mul_f32 v110, v84, v110\n"
      "v_mul_f32 v113, v84, v113\n"
      "v_mfma_f32_16x16x32_bf16 v[148:151], v[244:247], v[100:103], v[148:151]\n"
      "ds_read_b128 v[158:161], v2 offset:3328\n"
      "v_cvt_pk_bf16_f32 v85, v220, v221\n"
      "v_mfma_f32_16x16x32_bf16 v[148:151], v[248:251], v[144:147], v[148:151]\n"
      "ds_read_u16 v82, v11 offset:1200\n"
      "v_mul_f32 v112, v84, v112\n"
      "v_mfma_f32_16x16x32_bf16 v[148:151], v[252:255], v[210:213], v[148:151]\n"
      "v_mul_f32 v111, v84, v111\n"
      "v_cvt_pk_bf16_f32 v154, v122, v123\n"
      "v_mfma_f32_16x16x32_bf16 v[134:137], v[190:193], v[114:117], v[40:43]\n"
      "ds_read_b128 v[144:147], v2 offset:6400\n"
      "v_cvt_pk_bf16_f32 v204, v166, v167\n"
      "v_mfma_f32_16x16x32_bf16 v[140:143], v[126:129], v[114:117], v[44:47]\n"
      "s_waitcnt lgkmcnt(3)\n"
      "v_mul_f32 v173, v175, v175\n"
      "v_mul_f32_e64 v183, v84, v183\n"
      "v_mfma_f32_16x16x32_bf16 v[220:223], v[152:155], v[114:117], v[48:51]\n"
      "ds_read_b128 v[100:103], v2 offset:15616\n"
      "v_cvt_pk_bf16_f32 v182, v182, v183\n"
      "v_mfma_f32_16x16x32_bf16 v[166:169], v[198:201], v[114:117], v[24:27]\n"
      "v_fma_f32 v172, v174, v174, s52\n"
      "v_cvt_pk_bf16_f32 v206, v206, v207\n"
      "v_mfma_f32_16x16x16_bf16 v[134:137], v[138:139], v[130:131], v[134:137]\n"
      "s_waitcnt vmcnt(1)\n"
      "v_or3_b32 v162, v162, v163, v164\n"
      "v_mul_f32 v208, v84, v208\n"
      "v_cvt_pk_bf16_f32 v84, v218, v219\n"
      "v_mfma_f32_16x16x16_bf16 v[140:143], v[170:171], v[130:131], v[140:143]\n"
      "v_cvt_pk_bf16_f32 v80, v156, v157\n"
      "v_cvt_pk_bf16_f32 v183, v184, v185\n"
      "v_fmac_f32_e64 v173, v177, v177\n"
      "v_mfma_f32_16x16x16_bf16 v[220:223], v[132:133], v[130:131], v[220:223]\n"
      "v_cvt_pk_bf16_f32 v184, v110, v111\n"
      "v_exp_f32 v136, v136\n"
      "v_mfma_f32_16x16x16_bf16 v[166:169], v[108:109], v[130:131], v[166:169]\n"
      "v_exp_f32_e64 v137, v137\n"
      "s_waitcnt vmcnt(0)\n"
      "v_or3_b32 v162, v162, v165, v90\n"
      "v_mfma_f32_16x16x32_bf16 v[210:213], a[216:219], v[178:181], 0\n"
      "s_waitcnt lgkmcnt(0)\n"
      "v_exp_f32 v143, v143\n"
      "v_mfma_f32_16x16x32_bf16 v[210:213], a[220:223], v[158:161], v[210:213]\n"
      "v_exp_f32 v141, v141\n"
      "v_mfma_f32_16x16x32_bf16 v[210:213], a[224:227], v[144:147], v[210:213]\n"
      "v_exp_f32 v220, v220\n"
      "v_mfma_f32_16x16x32_bf16 v[122:125], a[240:243], v[178:181], 0\n"
      "v_exp_f32_e64 v221, v221\n"
      "v_mfma_f32_16x16x32_bf16 v[122:125], a[244:247], v[158:161], v[122:125]\n"
      "v_exp_f32 v142, v142\n"
      "v_mfma_f32_16x16x32_bf16 v[122:125], a[248:251], v[144:147], v[122:125]\n"
      "v_exp_f32 v166, v166\n"
      "v_mfma_f32_16x16x32_bf16 v[118:121], v[232:235], v[178:181], 0\n"
      "s_and_b32 s47, s47, -4\n"
      "v_exp_f32 v134, v134\n"
      "v_mfma_f32_16x16x32_bf16 v[118:121], v[236:239], v[158:161], v[118:121]\n"
      "ds_write_b32 v31, v162 offset:5632\n"
      "v_mfma_f32_16x16x32_bf16 v[118:121], v[240:243], v[144:147], v[118:121]\n"
      "s_and_b32 s4, s45, s8\n"
      "v_exp_f32 v135, v135\n"
      "v_mfma_f32_16x16x32_bf16 v[160:163], v[190:193], v[202:205], v[52:55]\n"
      "v_exp_f32 v222, v222\n"
      "v_mfma_f32_16x16x32_bf16 v[178:181], v[126:129], v[202:205], v[56:59]\n"
      "v_exp_f32 v223, v223\n"
      "v_mfma_f32_16x16x32_bf16 v[156:159], v[152:155], v[202:205], v[60:63]\n"
      "v_cvt_pk_bf16_f32 v185, v112, v113\n"
      "v_cvt_pk_bf16_f32 v134, v134, v135\n"
      "v_mfma_f32_16x16x32_bf16 v[110:113], v[198:201], v[202:205], v[26:29]\n"
      "v_exp_f32 v140, v140\n"
      "v_mfma_f32_16x16x16_bf16 v[160:163], v[138:139], v[186:187], v[160:163]\n"
      "v_cvt_pk_bf16_f32 v90, v166, 0\n"
      "v_fmac_f32 v172, v176, v176\n"
      "v_cvt_pk_bf16_f32 v89, v222, v223\n"
      "v_mfma_f32_16x16x16_bf16 v[178:181], v[170:171], v[186:187], v[178:181]\n"
      "v_cvt_pk_bf16_f32 v135, v136, v137\n"
      "v_cvt_pk_bf16_f32 v88, v220, v221\n"
      "v_cvt_pk_bf16_f32 v137, v142, v143\n"
      "v_mfma_f32_16x16x16_bf16 v[156:159], v[132:133], v[186:187], v[156:159]\n"
      "v_cvt_pk_bf16_f32 v136, v140, v141\n"
      "v_exp_f32_e64 v162, v162\n"
      "v_mfma_f32_16x16x16_bf16 v[110:113], v[108:109], v[186:187], v[110:113]\n"
      "v_cvt_pk_bf16_f32 v207, v208, v209\n"
      "v_exp_f32 v161, v161\n"
      "v_mfma_f32_16x16x32_bf16 v[164:167], v[32:35], v[134:137], 0\n"
      "v_exp_f32 v160, v160\n"
      "v_mfma_f32_16x16x32_bf16 v[164:167], v[32:35], v[88:91], v[164:167]\n"
      "s_add_u32 s43, s43, 1\n"
      "v_exp_f32 v163, v163\n"
      "v_mfma_f32_16x16x32_bf16 v[220:223], v[194:197], v[134:137], 0\n"
      "v_exp_f32 v181, v181\n"
      "v_mfma_f32_16x16x32_bf16 v[220:223], v[76:79], v[88:91], v[220:223]\n"
      "v_exp_f32 v180, v180\n"
      "v_mfma_f32_16x16x32_bf16 v[202:205], v[96:99], v[134:137], 0\n"
      "ds_read_b128 v[186:189], v2 offset:9472\n"
      "v_cvt_pk_bf16_f32 v160, v160, v161\n"
      "v_mfma_f32_16x16x32_bf16 v[202:205], v[80:83], v[88:91], v[202:205]\n"
      "v_cvt_pk_bf16_f32 v161, v162, v163\n"
      "v_cvt_pk_bf16_f32 v163, v180, v181\n"
      "v_mfma_f32_16x16x32_bf16 v[114:117], v[104:107], v[134:137], 0\n"
      "v_rcp_f32_e64 v164, v164\n"
      "v_mfma_f32_16x16x32_bf16 v[114:117], v[84:87], v[88:91], v[114:117]\n"
      "v_exp_f32 v179, v179\n"
      "v_mfma_f32_16x16x32_bf16 v[134:137], v[190:193], v[182:185], v[64:67]\n"
      "v_mul_f32 v221, v164, v221\n"
      "v_mul_f32_e64 v222, v164, v222\n"
      "v_mfma_f32_16x16x32_bf16 v[144:147], v[126:129], v[182:185], v[68:71]\n"
      "v_exp_f32 v178, v178\n"
      "v_mfma_f32_16x16x32_bf16 v[140:143], v[152:155], v[182:185], v[72:75]\n"
      "s_waitcnt lgkmcnt(0)\n"
      "v_cvt_pk_bf16_f32 v162, v178, v179\n"
      "v_mul_f32_e64 v220, v164, v220\n"
      "v_mfma_f32_16x16x32_bf16 v[166:169], v[198:201], v[182:185], v[28:31]\n"
      "ds_read_b128 v[178:181], v2 offset:12544\n"
      "v_cvt_pk_bf16_f32 v220, v220, v221\n"
      "v_mfma_f32_16x16x16_bf16 v[134:137], v[138:139], v[206:207], v[134:137]\n"
      "v_exp_f32 v159, v159\n"
      "v_mul_f32 v223, v164, v223\n"
      "v_mfma_f32_16x16x16_bf16 v[144:147], v[170:171], v[206:207], v[144:147]\n"
      "v_cvt_pk_bf16_f32 v221, v222, v223\n"
      "v_exp_f32 v156, v156\n"
      "v_mfma_f32_16x16x16_bf16 v[140:143], v[132:133], v[206:207], v[140:143]\n"
      "v_exp_f32 v110, v110\n"
      "v_exp_f32 v157, v157\n"
      "v_exp_f32 v158, v158\n"
      "v_pk_mul_f32 v[202:203], v[202:203], v[164:165] op_sel_hi:[1,0]\n"
      "v_pk_mul_f32 v[204:205], v[204:205], v[164:165] op_sel_hi:[1,0]\n"
      "v_mfma_f32_16x16x16_bf16 v[166:169], v[108:109], v[206:207], v[166:169]\n"
      "v_exp_f32_e64 v134, v134\n"
      "v_cvt_pk_bf16_f32 v222, v202, v203\n"
      "v_mfma_f32_16x16x32_bf16 v[210:213], a[228:231], v[186:189], v[210:213]\n"
      "s_waitcnt lgkmcnt(0)\n"
      "v_exp_f32 v146, v146\n"
      "v_mfma_f32_16x16x32_bf16 v[210:213], a[232:235], v[178:181], v[210:213]\n"
      "v_exp_f32_e64 v135, v135\n"
      "v_mfma_f32_16x16x32_bf16 v[210:213], a[236:239], v[100:103], v[210:213]\n"
      "ds_read_b64 v[168:169], v10 offset:1184\n"
      "v_cvt_pk_bf16_f32 v93, v158, v159\n"
      "v_mfma_f32_16x16x32_bf16 v[122:125], a[252:255], v[186:189], v[122:125]\n"
      "ds_read_b128 v[206:209], v7\n"
      "v_cvt_pk_bf16_f32 v92, v156, v157\n"
      "v_mfma_f32_16x16x32_bf16 v[122:125], v[224:227], v[178:181], v[122:125]\n"
      "v_cvt_pk_bf16_f32 v223, v204, v205\n"
      "v_cvt_pk_bf16_f32 v134, v134, v135\n"
      "v_mfma_f32_16x16x32_bf16 v[122:125], v[228:231], v[100:103], v[122:125]\n"
      "ds_read_b128 v[182:185], v7 offset:4096\n"
      "v_cvt_pk_bf16_f32 v94, v110, 0\n"
      "v_mfma_f32_16x16x32_bf16 v[118:121], v[244:247], v[186:189], v[118:121]\n"
      "ds_read_b128 v[202:205], v2 offset:512\n"
      "v_mul_f32 v165, v211, v211\n"
      "v_mfma_f32_16x16x32_bf16 v[118:121], v[248:251], v[178:181], v[118:121]\n"
      "v_exp_f32 v144, v144\n"
      "v_mfma_f32_16x16x32_bf16 v[118:121], v[252:255], v[100:103], v[118:121]\n"
      "v_exp_f32 v137, v137\n"
      "v_mfma_f32_16x16x32_bf16 v[100:103], v[32:35], v[160:163], 0\n"
      "v_exp_f32 v136, v136\n"
      "v_mfma_f32_16x16x32_bf16 v[100:103], v[32:35], v[92:95], v[100:103]\n"
      "ds_write_b128 v3, v[220:223] offset:18432\n"
      "v_pk_mul_f32 v[114:115], v[114:115], v[164:165] op_sel_hi:[1,0]\n"
      "v_pk_mul_f32 v[116:117], v[116:117], v[164:165] op_sel_hi:[1,0]\n"
      "v_pk_fma_f32 v[172:173], v[214:215], v[214:215], v[172:173]\n"
      "v_mfma_f32_16x16x32_bf16 v[156:159], v[194:197], v[160:163], 0\n"
      "ds_read_b128 v[178:181], v10 offset:1168\n"
      "v_cvt_pk_bf16_f32 v114, v114, v115\n"
      "v_mfma_f32_16x16x32_bf16 v[156:159], v[76:79], v[92:95], v[156:159]\n"
      "v_fma_f32 v164, v210, v210, s52\n"
      "v_cvt_pk_bf16_f32 v115, v116, v117\n"
      "v_mfma_f32_16x16x32_bf16 v[218:221], v[96:99], v[160:163], 0\n"
      "s_waitcnt lgkmcnt(2)\n"
      "v_rcp_f32 v100, v100\n"
      "v_mfma_f32_16x16x32_bf16 v[218:221], v[80:83], v[92:95], v[218:221]\n"
      "buffer_store_dwordx4 v[206:209], v20, s[24:27], s85 offen sc1\n"
      "v_pk_fma_f32 v[164:165], v[212:213], v[212:213], v[164:165]\n"
      "v_pk_fma_f32 v[164:165], v[122:123], v[122:123], v[164:165]\n"
      "v_pk_mul_f32 v[156:157], v[156:157], v[100:101] op_sel_hi:[1,0]\n"
      "v_pk_fma_f32 v[172:173], v[216:217], v[216:217], v[172:173]\n"
      "v_mfma_f32_16x16x32_bf16 v[206:209], v[104:107], v[160:163], 0\n"
      "v_exp_f32_e64 v140, v140\n"
      "v_mfma_f32_16x16x32_bf16 v[206:209], v[84:87], v[92:95], v[206:209]\n"
      "s_waitcnt lgkmcnt(0)\n"
      "v_exp_f32 v141, v141\n"
      "v_mfma_f32_16x16x32_bf16 v[160:163], v[178:181], v[190:193], v[30:33]\n"
      "v_exp_f32_e64 v166, v166\n"
      "v_mfma_f32_16x16x32_bf16 v[186:189], v[178:181], v[126:129], v[30:33]\n"
      "v_exp_f32_e64 v147, v147\n"
      "v_mfma_f32_16x16x32_bf16 v[128:131], v[178:181], v[152:155], v[30:33]\n"
      "ds_read_b128 v[152:155], v2 offset:6656\n"
      "v_cvt_pk_bf16_f32 v156, v156, v157\n"
      "v_mfma_f32_16x16x32_bf16 v[190:193], v[178:181], v[198:201], v[30:33]\n"
      "ds_read_b128 v[198:201], v2 offset:3584\n"
      "v_cvt_pk_bf16_f32 v88, v140, v141\n"
      "v_mfma_f32_16x16x16_bf16 v[160:163], v[168:169], v[138:139], v[160:163]\n"
      "buffer_store_dwordx4 v[182:185], v21, s[24:27], s85 offen sc1\n"
      "v_pk_fma_f32 v[164:165], v[124:125], v[124:125], v[164:165]\n"
      "v_pk_mul_f32 v[208:209], v[208:209], v[100:101] op_sel_hi:[1,0]\n"
      "v_pk_mul_f32 v[158:159], v[158:159], v[100:101] op_sel_hi:[1,0]\n"
      "v_pk_mul_f32 v[220:221], v[220:221], v[100:101] op_sel_hi:[1,0]\n"
      "v_pk_fma_f32 v[172:173], v[148:149], v[148:149], v[172:173]\n"
      "v_pk_fma_f32 v[164:165], v[118:119], v[118:119], v[164:165]\n"
      "v_pk_mul_f32 v[218:219], v[218:219], v[100:101] op_sel_hi:[1,0]\n"
      "v_mfma_f32_16x16x16_bf16 v[186:189], v[168:169], v[170:171], v[186:189]\n"
      "v_cvt_pk_bf16_f32 v135, v136, v137\n"
      "v_exp_f32_e64 v142, v142\n"
      "v_mfma_f32_16x16x16_bf16 v[128:131], v[168:169], v[132:133], v[128:131]\n"
      "v_exp_f32_e64 v145, v145\n"
      "v_cvt_pk_bf16_f32 v157, v158, v159\n"
      "v_mfma_f32_16x16x16_bf16 v[190:193], v[168:169], v[108:109], v[190:193]\n"
      "v_exp_f32 v143, v143\n"
      "v_cvt_pk_bf16_f32 v159, v220, v221\n"
      "v_mfma_f32_16x16x32_bf16 v[220:223], a[216:219], v[202:205], 0\n"
      "s_waitcnt lgkmcnt(0)\n"
      "v_cvt_pk_bf16_f32 v90, v166, 0\n"
      "v_cndmask_b32 v160, v160, v186, s[58:59]\n"
      "v_mfma_f32_16x16x32_bf16 v[220:223], a[220:223], v[198:201], v[220:223]\n"
      "ds_read_b128 v[108:111], v7 offset:8192\n"
      "v_cvt_pk_bf16_f32 v136, v144, v145\n"
      "v_mfma_f32_16x16x32_bf16 v[220:223], a[224:227], v[152:155], v[220:223]\n"
      "v_cndmask_b32 v160, v160, v128, s[60:61]\n"
      "v_cvt_pk_bf16_f32 v158, v218, v219\n"
      "v_mfma_f32_16x16x32_bf16 v[166:169], a[240:243], v[202:205], 0\n"
      "ds_write_b128 v3, v[156:159] offset:18688\n"
      "v_pk_mul_f32 v[206:207], v[206:207], v[100:101] op_sel_hi:[1,0]\n"
      "v_pk_fma_f32 v[172:173], v[150:151], v[150:151], v[172:173]\n"
      "v_mfma_f32_16x16x32_bf16 v[166:169], a[244:247], v[198:201], v[166:169]\n"
      "ds_read_b128 v[182:185], v2 offset:15872\n"
      "v_cndmask_b32 v160, v160, v190, s[62:63]\n"
      "v_mfma_f32_16x16x32_bf16 v[166:169], a[248:251], v[152:155], v[166:169]\n"
      "ds_read_b128 v[156:159], v7 offset:12288\n"
      "v_cvt_pk_bf16_f32 v89, v142, v143\n"
      "v_mfma_f32_16x16x32_bf16 v[186:189], v[232:235], v[202:205], 0\n"
      "ds_read_b128 v[202:205], v2 offset:9728\n"
      "v_cvt_pk_bf16_f32 v137, v146, v147\n"
      "v_mfma_f32_16x16x32_bf16 v[186:189], v[236:239], v[198:201], v[186:189]\n"
      "v_exp_f32_e64 v160, v160\n"
      "v_mfma_f32_16x16x32_bf16 v[186:189], v[240:243], v[152:155], v[186:189]\n"
      "ds_write_b64 v4, v[114:115] offset:18432\n"
      "v_mfma_f32_16x16x32_bf16 v[152:155], v[32:35], v[134:137], 0\n"
      "ds_read_b128 v[198:201], v2 offset:12800\n"
      "v_add_f32 v172, v172, v173\n"
      "v_mfma_f32_16x16x32_bf16 v[152:155], v[32:35], v[88:91], v[152:155]\n"
      "s_waitcnt lgkmcnt(4)\n"
      "v_cvt_pk_bf16_f32 v160, v160, v160\n"
      "v_cvt_pk_bf16_f32 v206, v206, v207\n"
      "v_mfma_f32_16x16x32_bf16 v[112:115], v[194:197], v[134:137], 0\n"
      "ds_write_b16 v13, v160\n"
      "v_mfma_f32_16x16x32_bf16 v[112:115], v[76:79], v[88:91], v[112:115]\n"
      "ds_read_b128 v[100:103], v14 offset:64\n"
      "v_cvt_pk_bf16_f32 v207, v208, v209\n"
      "v_mfma_f32_16x16x32_bf16 v[138:141], v[96:99], v[134:137], 0\n"
      "buffer_store_dwordx4 v[108:111], v22, s[24:27], s85 offen sc1\n"
      "v_pk_fma_f32 v[164:165], v[120:121], v[120:121], v[164:165]\n"
      "v_mfma_f32_16x16x32_bf16 v[138:141], v[80:83], v[88:91], v[138:141]\n"
      "s_waitcnt lgkmcnt(3)\n"
      "v_rcp_f32 v152, v152\n"
      "v_mfma_f32_16x16x32_bf16 v[142:145], v[104:107], v[134:137], 0\n"
      "ds_write_b64 v4, v[206:207] offset:18688\n"
      "v_mfma_f32_16x16x32_bf16 v[142:145], v[84:87], v[88:91], v[142:145]\n"
      "ds_read_b128 v[178:181], v14\n"
      "v_add_f32 v164, v164, v165\n"
      "v_mfma_f32_16x16x32_bf16 v[220:223], a[228:231], v[202:205], v[220:223]\n"
      "s_waitcnt lgkmcnt(3)\n"
      "v_mul_f32 v113, v152, v113\n"
      "v_mul_f32 v112, v152, v112\n"
      "v_mfma_f32_16x16x32_bf16 v[220:223], a[232:235], v[198:201], v[220:223]\n"
      "v_mul_f32_e64 v114, v152, v114\n"
      "v_mul_f32_e64 v115, v152, v115\n"
      "v_mfma_f32_16x16x32_bf16 v[220:223], a[236:239], v[182:185], v[220:223]\n"
      "v_permlane16_swap_b32_e64 v172, v164\n"
      "v_mfma_f32_16x16x32_bf16 v[166:169], a[252:255], v[202:205], v[166:169]\n"
      "v_cvt_pk_bf16_f32 v112, v112, v113\n"
      "v_add_f32_e64 v172, v172, v164\n"
      "v_mfma_f32_16x16x32_bf16 v[166:169], v[224:227], v[198:201], v[166:169]\n"
      "buffer_store_dwordx4 v[156:159], v23, s[24:27], s85 offen sc1\n"
      "v_pk_mul_f32 v[140:141], v[140:141], v[152:153] op_sel_hi:[1,0]\n"
      "v_pk_mul_f32 v[138:139], v[138:139], v[152:153] op_sel_hi:[1,0]\n"
      "v_mfma_f32_16x16x32_bf16 v[166:169], v[228:231], v[182:185], v[166:169]\n"
      "ds_read_b128 v[128:131], v7 offset:16384\n"
      "v_cvt_pk_bf16_f32 v113, v114, v115\n"
      "v_mfma_f32_16x16x32_bf16 v[186:189], v[244:247], v[202:205], v[186:189]\n"
      "v_cvt_pk_bf16_f32 v115, v140, v141\n"
      "v_mul_f32 v141, v221, v221\n"
      "v_mfma_f32_16x16x32_bf16 v[186:189], v[248:251], v[198:201], v[186:189]\n"
      "s_waitcnt lgkmcnt(1)\n"
      "v_fma_f32 v140, v220, v220, s52\n"
      "v_cvt_pk_bf16_f32 v114, v138, v139\n"
      "v_mfma_f32_16x16x32_bf16 v[186:189], v[252:255], v[182:185], v[186:189]\n"
      "ds_write_b128 v3, v[112:115] offset:18944\n"
      "v_pk_fma_f32 v[140:141], v[222:223], v[222:223], v[140:141]\n"
      "v_pk_mul_f32 v[144:145], v[144:145], v[152:153] op_sel_hi:[1,0]\n"
      "v_pk_fma_f32 v[140:141], v[166:167], v[166:167], v[140:141]\n"
      "v_pk_mul_f32 v[142:143], v[142:143], v[152:153] op_sel_hi:[1,0]\n"
      "v_cvt_pk_bf16_f32 v142, v142, v143\n"
      "v_cvt_pk_bf16_f32 v143, v144, v145\n"
      "v_pk_fma_f32 v[140:141], v[168:169], v[168:169], v[140:141]\n"
      "v_pk_fma_f32 v[140:141], v[186:187], v[186:187], v[140:141]\n"
      "v_pk_fma_f32 v[140:141], v[188:189], v[188:189], v[140:141]\n"
      "v_add_f32 v140, v140, v141\n"
      "v_mov_b32 v164, v140\n"
      "v_mfma_f32_16x16x32_bf16 v[160:163], v[178:181], v[32:35], 0\n"
      "ds_write_b64 v4, v[142:143] offset:18944\n"
      "v_permlane16_swap_b32 v140, v164\n"
      "v_add_f32 v140, v140, v164\n"
      "s_nop 1\n"
      "v_permlane32_swap_b32 v172, v140\n"
      "v_add_f32_e64 v172, v172, v140\n"
      "v_mfma_f32_16x16x32_bf16 v[160:163], v[100:103], v[32:35], v[160:163]\n"
      "ds_write_b32 v31, v172 offset:1024\n"
      "v_add_u32 v10, 2336, v10\n"
      "s_cmp_ge_u32 s95, 0x40000000\n"
      "s_cselect_b64 vcc, -1, s[56:57]\n"
      "s_waitcnt lgkmcnt(2)\n"
      "buffer_store_dwordx4 v[128:131], v25, s[24:27], s85 offen sc1\n"
      "v_add_u32 v11, 2336, v11\n"
      "s_waitcnt lgkmcnt(0)\n"
      "s_barrier\n"
      "s_cbranch_vccnz L_disp0_%=\n"
      "L_loop0_%=:\n"
      "v_mfma_f32_16x16x32_bf16 v[112:115], v[178:181], v[194:197], 0\n"
      "ds_read_b128 v[140:143], v0\n"
      "ds_read_b128 v[108:111], v9 offset:1024\n"
      "v_mfma_f32_16x16x32_bf16 v[112:115], v[100:103], v[76:79], v[112:115]\n"
      "ds_read_b128 v[136:139], v1\n"
      "s_add_u32 m0, s64, 18432\n"
      "v_mfma_f32_16x16x32_bf16 v[126:129], v[178:181], v[96:99], 0\n"
      "buffer_load_dwordx4 v16, s[16:19], s45 offen  lds\n"
      "v_mfma_f32_16x16x32_bf16 v[126:129], v[100:103], v[80:83], v[126:129]\n"
      "ds_read_b128 v[190:193], v1 offset:6144\n"
      "s_add_u32 m0, s64, 22528\n"
      "v_mfma_f32_16x16x32_bf16 v[152:155], v[178:181], v[104:107], 0\n"
      "ds_read_b128 v[182:185], v1 offset:12288\n"
      "v_readlane_b32 s86, v38, s43\n"
      "v_mfma_f32_16x16x32_bf16 v[152:155], v[100:103], v[84:87], v[152:155]\n"
      "ds_read_b128 v[114:117], v0 offset:6144\n"
      "s_waitcnt lgkmcnt(5)\n"
      "v_mfma_f32_16x16x32_bf16 v[128:131], a[0:3], v[140:143], 0\n"
      "ds_read_b128 v[132:135], v0 offset:12288\n"
      "v_cndmask_b32 v112, v112, v126, s[58:59]\n"
      "v_mfma_f32_16x16x32_bf16 v[202:205], a[24:27], v[140:143], 0\n"
      "s_waitcnt lgkmcnt(5)\n"
      "v_add_f32 v108, v108, v109\n"
      "v_add_f32 v110, v110, v111\n"
      "v_mfma_f32_16x16x32_bf16 v[198:201], a[48:51], v[140:143], 0\n"
      "ds_read_b128 v[178:181], v0 offset:14336\n"
      "v_mov_b32_dpp v160, v160 quad_perm:[0,0,0,0] row_mask:0xf bank_mask:0xf\n"
      "v_mfma_f32_16x16x32_bf16 v[100:103], a[72:75], v[140:143], 0\n"
      "ds_read_b128 v[162:165], v0 offset:8192\n"
      "v_add_f32 v108, v108, v110\n"
      "v_mfma_f32_16x16x32_bf16 v[144:147], a[96:99], v[140:143], 0\n"
      "buffer_load_dwordx4 v17, s[16:19], s45 offen  lds\n"
      "v_mfma_f32_16x16x32_bf16 v[156:159], a[120:123], v[140:143], 0\n"
      "s_waitcnt lgkmcnt(3)\n"
      "v_rsq_f32 v108, v108\n"
      "v_mfma_f32_16x16x32_bf16 v[128:131], a[4:7], v[136:139], v[128:131]\n"
      "s_add_u32 m0, s64, 26624\n"
      "v_mul_f32 v108, 0x415db3d7, v108\n"
      "v_mov_b32 v110, v108\n"
      "v_mfma_f32_16x16x32_bf16 v[128:131], a[8:11], v[114:117], v[128:131]\n"
      "s_nop 0\n"
      "v_permlane32_swap_b32 v108, v110\n"
      "v_mfma_f32_16x16x32_bf16 v[202:205], a[28:31], v[136:139], v[202:205]\n"
      "v_cndmask_b32 v112, v112, v152, s[60:61]\n"
      "v_mov_b32 v109, v108\n"
      "v_mfma_f32_16x16x32_bf16 v[202:205], a[32:35], v[114:117], v[202:205]\n"
      "buffer_load_dwordx4 v18, s[16:19], s45 offen  lds\n"
      "v_mfma_f32_16x16x32_bf16 v[198:201], a[52:55], v[136:139], v[198:201]\n"
      "v_permlane16_swap_b32 v108, v109\n"
      "v_mfma_f32_16x16x32_bf16 v[198:201], a[56:59], v[114:117], v[198:201]\n"
      "ds_read_b128 v[170:173], v0 offset:2048\n"
      "v_mul_f32_e64 v148, v108, v148\n"
      "v_mfma_f32_16x16x32_bf16 v[100:103], a[76:79], v[136:139], v[100:103]\n"
      "s_add_u32 m0, s64, 30720\n"
      "v_rcp_f32 v160, v160\n"
      "v_mfma_f32_16x16x32_bf16 v[100:103], a[80:83], v[114:117], v[100:103]\n"
      "v_mul_f32 v211, v109, v211\n"
      "v_mul_f32_e64 v215, v108, v215\n"
      "v_mfma_f32_16x16x32_bf16 v[144:147], a[100:103], v[136:139], v[144:147]\n"
      "buffer_load_dwordx4 v19, s[16:19], s45 offen  lds\n"
      "v_mfma_f32_16x16x32_bf16 v[144:147], a[104:107], v[114:117], v[144:147]\n"
      "v_mul_f32 v166, v110, v166\n"
      "v_mul_f32 v220, v110, v220\n"
      "v_mfma_f32_16x16x32_bf16 v[156:159], a[124:127], v[136:139], v[156:159]\n"
      "v_mul_f32 v214, v108, v214\n"
      "v_mul_f32 v112, v160, v112\n"
      "v_mfma_f32_16x16x32_bf16 v[156:159], a[128:131], v[114:117], v[156:159]\n"
      "s_mov_b32 m0, s7\n"
      "v_mul_f32 v174, v108, v174\n"
      "v_mul_f32 v176, v108, v176\n"
      "v_mfma_f32_16x16x32_bf16 v[128:131], a[12:15], v[190:193], v[128:131]\n"
      "s_waitcnt lgkmcnt(0)\n"
      "v_mul_f32 v149, v108, v149\n"
      "v_mul_f32 v189, v110, v189\n"
      "v_mfma_f32_16x16x32_bf16 v[128:131], a[16:19], v[132:135], v[128:131]\n"
      "buffer_load_dwordx4 v15, s[0:3], s4 offen  lds\n"
      "v_mfma_f32_16x16x32_bf16 v[128:131], a[20:23], v[182:185], v[128:131]\n"
      "v_readlane_b32 s84, v37, s43\n"
      "v_mul_f32_e64 v175, v108, v175\n"
      "v_mfma_f32_16x16x32_bf16 v[202:205], a[36:39], v[190:193], v[202:205]\n"
      "v_cvt_pk_bf16_f32 v112, v112, v112\n"
      "v_cvt_pk_bf16_f32 v148, v148, v149\n"
      "v_mfma_f32_16x16x32_bf16 v[202:205], a[40:43], v[132:135], v[202:205]\n"
      "ds_read_b128 v[206:209], v1 offset:2048\n"
      "v_mul_f32 v212, v109, v212\n"
      "v_mfma_f32_16x16x32_bf16 v[202:205], a[44:47], v[182:185], v[202:205]\n"
      "v_mul_f32 v216, v108, v216\n"
      "v_mul_f32_e64 v167, v110, v167\n"
      "v_mfma_f32_16x16x32_bf16 v[198:201], a[60:63], v[190:193], v[198:201]\n"
      "v_mul_f32 v150, v108, v150\n"
      "v_mul_f32 v125, v109, v125\n"
      "v_mfma_f32_16x16x32_bf16 v[198:201], a[64:67], v[132:135], v[198:201]\n"
      "buffer_load_dwordx4 v[152:155], v36, s[28:31], s47 offen\n"
      "v_mfma_f32_16x16x32_bf16 v[198:201], a[68:71], v[182:185], v[198:201]\n"
      "v_mul_f32 v177, v108, v177\n"
      "v_mul_f32 v151, v108, v151\n"
      "v_mfma_f32_16x16x32_bf16 v[100:103], a[84:87], v[190:193], v[100:103]\n"
      "v_mul_f32_e64 v168, v110, v168\n"
      "v_cvt_pk_bf16_f32 v174, v174, v175\n"
      "v_mfma_f32_16x16x32_bf16 v[100:103], a[88:91], v[132:135], v[100:103]\n"
      "ds_write_b16 v12, v112 offset:16\n"
      "v_mfma_f32_16x16x32_bf16 v[100:103], a[92:95], v[182:185], v[100:103]\n"
      "v_mul_f32 v217, v108, v217\n"
      "v_mul_f32 v221, v110, v221\n"
      "v_mfma_f32_16x16x32_bf16 v[144:147], a[108:111], v[190:193], v[144:147]\n"
      "v_add_u32 v12, 32, v12\n"
      "v_mul_f32 v169, v110, v169\n"
      "v_mfma_f32_16x16x32_bf16 v[144:147], a[112:115], v[132:135], v[144:147]\n"
      "buffer_load_dword v89, v29, s[28:31], s47 offen\n"
      "v_mfma_f32_16x16x32_bf16 v[144:147], a[116:119], v[182:185], v[144:147]\n"
      "v_mul_f32_e64 v120, v109, v120\n"
      "v_fma_f32 v108, v100, v100, s51\n"
      "v_mfma_f32_16x16x32_bf16 v[156:159], a[132:135], v[190:193], v[156:159]\n"
      "v_cvt_pk_bf16_f32 v149, v150, v151\n"
      "v_fmac_f32 v108, v101, v101\n"
      "v_mfma_f32_16x16x32_bf16 v[156:159], a[136:139], v[132:135], v[156:159]\n"
      "v_mul_f32 v119, v109, v119\n"
      "v_cvt_pk_bf16_f32 v220, v220, v221\n"
      "v_mfma_f32_16x16x32_bf16 v[156:159], a[140:143], v[182:185], v[156:159]\n"
      "ds_write_b64 v6, v[148:149]\n"
      "v_mfma_f32_16x16x32_bf16 v[194:197], v[140:143], a[144:147], 0\n"
      "ds_read_b128 v[148:151], v9 offset:5632\n"
      "v_cvt_pk_bf16_f32 v175, v176, v177\n"
      "v_mfma_f32_16x16x32_bf16 v[194:197], v[136:139], a[148:151], v[194:197]\n"
      "v_fmac_f32 v108, v102, v102\n"
      "v_mul_f32 v210, v109, v210\n"
      "v_mfma_f32_16x16x32_bf16 v[194:197], v[114:117], a[152:155], v[194:197]\n"
      "v_cvt_pk_bf16_f32 v176, v214, v215\n"
      "v_mul_f32 v187, v110, v187\n"
      "v_mfma_f32_16x16x32_bf16 v[194:197], v[190:193], a[156:159], v[194:197]\n"
      "v_fmac_f32 v108, v103, v103\n"
      "v_fma_f32 v160, v128, v128, s51\n"
      "v_mfma_f32_16x16x32_bf16 v[194:197], v[132:135], a[160:163], v[194:197]\n"
      "v_mul_f32_e64 v222, v110, v222\n"
      "v_cvt_pk_bf16_f32 v177, v216, v217\n"
      "v_mfma_f32_16x16x32_bf16 v[194:197], v[182:185], a[164:167], v[194:197]\n"
      "v_mul_f32 v223, v110, v223\n"
      "v_fmac_f32 v160, v129, v129\n"
      "v_mfma_f32_16x16x32_bf16 v[96:99], v[140:143], a[168:171], 0\n"
      "ds_write_b128 v5, v[174:177]\n"
      "v_mfma_f32_16x16x32_bf16 v[96:99], v[136:139], a[172:175], v[96:99]\n"
      "ds_read_b128 v[174:177], v1 offset:14336\n"
      "v_fmac_f32_e64 v160, v130, v130\n"
      "v_mfma_f32_16x16x32_bf16 v[96:99], v[114:117], a[176:179], v[96:99]\n"
      "v_cvt_pk_bf16_f32 v221, v222, v223\n"
      "v_mul_f32 v186, v110, v186\n"
      "v_mfma_f32_16x16x32_bf16 v[96:99], v[190:193], a[180:183], v[96:99]\n"
      "v_readlane_b32 s85, v37, s42\n"
      "v_mul_f32 v118, v109, v118\n"
      "v_mfma_f32_16x16x32_bf16 v[96:99], v[132:135], a[184:187], v[96:99]\n"
      "s_waitcnt lgkmcnt(2)\n"
      "v_cvt_pk_bf16_f32 v210, v210, v211\n"
      "v_fmac_f32 v160, v131, v131\n"
      "v_mfma_f32_16x16x32_bf16 v[96:99], v[182:185], a[188:191], v[96:99]\n"
      "v_or3_b32 v148, v148, v149, v150\n"
      "v_fmac_f32_e64 v108, v144, v144\n"
      "v_mfma_f32_16x16x32_bf16 v[104:107], v[140:143], a[192:195], 0\n"
      "ds_read_b128 v[214:217], v1 offset:8192\n"
      "v_cvt_pk_bf16_f32 v186, v186, v187\n"
      "v_mfma_f32_16x16x32_bf16 v[104:107], v[136:139], a[196:199], v[104:107]\n"
      "ds_read_u16 v86, v11 offset:64\n"
      "v_fmac_f32_e64 v108, v145, v145\n"
      "v_mfma_f32_16x16x32_bf16 v[104:107], v[114:117], a[200:203], v[104:107]\n"
      "v_fmac_f32 v108, v146, v146\n"
      "v_mul_f32 v188, v110, v188\n"
      "v_mfma_f32_16x16x32_bf16 v[104:107], v[190:193], a[204:207], v[104:107]\n"
      "ds_read_b128 v[114:117], v0 offset:16384\n"
      "v_fmac_f32 v108, v147, v147\n"
      "v_mfma_f32_16x16x32_bf16 v[104:107], v[132:135], a[208:211], v[104:107]\n"
      "v_fmac_f32 v160, v202, v202\n"
      "v_fmac_f32 v160, v203, v203\n"
      "v_mfma_f32_16x16x32_bf16 v[104:107], v[182:185], a[212:215], v[104:107]\n"
      "v_fmac_f32 v108, v156, v156\n"
      "v_cvt_pk_bf16_f32 v194, v194, v195\n"
      "v_mfma_f32_16x16x32_bf16 v[182:185], a[0:3], v[170:173], 0\n"
      "v_cvt_pk_bf16_f32 v222, v166, v167\n"
      "v_fmac_f32 v160, v204, v204\n"
      "v_mfma_f32_16x16x32_bf16 v[182:185], a[4:7], v[206:209], v[182:185]\n"
      "v_cvt_pk_bf16_f32 v187, v188, v189\n"
      "v_mul_f32 v123, v109, v123\n"
      "v_mfma_f32_16x16x32_bf16 v[182:185], a[8:11], v[162:165], v[182:185]\n"
      "s_waitcnt lgkmcnt(1)\n"
      "v_mul_f32 v213, v109, v213\n"
      "v_fmac_f32 v160, v205, v205\n"
      "v_mfma_f32_16x16x32_bf16 v[182:185], a[12:15], v[214:217], v[182:185]\n"
      "s_or_b32 s95, s84, s85\n"
      "v_cvt_pk_bf16_f32 v223, v168, v169\n"
      "v_cvt_pk_bf16_f32 v96, v96, v97\n"
      "v_mfma_f32_16x16x32_bf16 v[182:185], a[16:19], v[178:181], v[182:185]\n"
      "v_mul_f32 v122, v109, v122\n"
      "v_fmac_f32 v160, v198, v198\n"
      "v_mfma_f32_16x16x32_bf16 v[182:185], a[20:23], v[174:177], v[182:185]\n"
      "v_cvt_pk_bf16_f32 v211, v212, v213\n"
      "v_bitop3_b32 v148, v148, v151, s33 bitop3:0xa8\n"
      "v_mfma_f32_16x16x32_bf16 v[166:169], a[24:27], v[170:173], 0\n"
      "v_fmac_f32 v160, v199, v199\n"
      "v_mul_f32 v121, v109, v121\n"
      "v_mfma_f32_16x16x32_bf16 v[166:169], a[28:31], v[206:209], v[166:169]\n"
      "v_fmac_f32 v108, v157, v157\n"
      "v_cvt_pk_bf16_f32 v104, v104, v105\n"
      "v_mfma_f32_16x16x32_bf16 v[166:169], a[32:35], v[162:165], v[166:169]\n"
      "v_fma_f32 v76, v182, v182, s51\n"
      "v_fmac_f32 v160, v200, v200\n"
      "v_mfma_f32_16x16x32_bf16 v[166:169], a[36:39], v[214:217], v[166:169]\n"
      "v_fmac_f32 v108, v158, v158\n"
      "v_cvt_pk_bf16_f32 v212, v122, v123\n"
      "v_mfma_f32_16x16x32_bf16 v[166:169], a[40:43], v[178:181], v[166:169]\n"
      "v_fmac_f32 v76, v183, v183\n"
      "v_cvt_pk_bf16_f32 v105, v106, v107\n"
      "v_mfma_f32_16x16x32_bf16 v[166:169], a[44:47], v[174:177], v[166:169]\n"
      "v_fmac_f32 v160, v201, v201\n"
      "v_mul_f32 v124, v109, v124\n"
      "v_mfma_f32_16x16x32_bf16 v[190:193], a[48:51], v[170:173], 0\n"
      "v_fmac_f32 v108, v159, v159\n"
      "v_fmac_f32 v76, v184, v184\n"
      "v_mfma_f32_16x16x32_bf16 v[190:193], a[52:55], v[206:209], v[190:193]\n"
      "ds_write_b64 v6, v[186:187] offset:12288\n"
      "v_mfma_f32_16x16x32_bf16 v[190:193], a[56:59], v[162:165], v[190:193]\n"
      "v_cvt_pk_bf16_f32 v213, v124, v125\n"
      "v_fmac_f32 v76, v185, v185\n"
      "v_mfma_f32_16x16x32_bf16 v[190:193], a[60:63], v[214:217], v[190:193]\n"
      "v_permlane16_swap_b32 v160, v108\n"
      "v_mfma_f32_16x16x32_bf16 v[190:193], a[64:67], v[178:181], v[190:193]\n"
      "v_fmac_f32 v76, v166, v166\n"
      "v_add_f32 v160, v160, v108\n"
      "v_mfma_f32_16x16x32_bf16 v[190:193], a[68:71], v[174:177], v[190:193]\n"
      "v_mov_b32 v108, v160\n"
      "v_fmac_f32 v76, v167, v167\n"
      "v_mfma_f32_16x16x32_bf16 v[186:189], a[72:75], v[170:173], 0\n"
      "v_permlane32_swap_b32 v160, v108\n"
      "v_mfma_f32_16x16x32_bf16 v[186:189], a[76:79], v[206:209], v[186:189]\n"
      "s_or_b32 s95, s95, s48\n"
      "v_cvt_pk_bf16_f32 v195, v196, v197\n"
      "v_add_f32_e64 v160, v160, v108\n"
      "v_mfma_f32_16x16x32_bf16 v[186:189], a[80:83], v[162:165], v[186:189]\n"
      "v_rsq_f32 v160, v160\n"
      "v_mfma_f32_16x16x32_bf16 v[186:189], a[84:87], v[214:217], v[186:189]\n"
      "ds_read_b128 v[138:141], v0 offset:4096\n"
      "v_fmac_f32 v76, v168, v168\n"
      "v_mfma_f32_16x16x32_bf16 v[186:189], a[88:91], v[178:181], v[186:189]\n"
      "v_fmac_f32 v76, v169, v169\n"
      "v_mov_b32 v108, v160\n"
      "v_mfma_f32_16x16x32_bf16 v[186:189], a[92:95], v[174:177], v[186:189]\n"
      "s_cmp_ge_u32 s95, 0x40000000\n"
      "s_cselect_b32 s94, -1, -2\n"
      "v_cvt_pk_bf16_f32 v97, v98, v99\n"
      "v_mfma_f32_16x16x32_bf16 v[110:113], a[96:99], v[170:173], 0\n"
      "v_permlane16_swap_b32_e64 v160, v108\n"
      "v_mfma_f32_16x16x32_bf16 v[110:113], a[100:103], v[206:209], v[110:113]\n"
      "ds_read_b128 v[134:137], v1 offset:10240\n"
      "v_fmac_f32 v76, v190, v190\n"
      "v_mfma_f32_16x16x32_bf16 v[110:113], a[104:107], v[162:165], v[110:113]\n"
      "v_fma_f32 v98, v186, v186, s51\n"
      "v_fmac_f32 v98, v187, v187\n"
      "v_mfma_f32_16x16x32_bf16 v[110:113], a[108:111], v[214:217], v[110:113]\n"
      "v_fmac_f32 v98, v188, v188\n"
      "v_mul_f32 v157, v108, v157\n"
      "v_mfma_f32_16x16x32_bf16 v[110:113], a[112:115], v[178:181], v[110:113]\n"
      "ds_write_b128 v5, v[220:223] offset:12288\n"
      "v_mfma_f32_16x16x32_bf16 v[110:113], a[116:119], v[174:177], v[110:113]\n"
      "v_fmac_f32_e64 v98, v189, v189\n"
      "v_cvt_pk_bf16_f32 v118, v118, v119\n"
      "v_mfma_f32_16x16x32_bf16 v[218:221], a[120:123], v[170:173], 0\n"
      "v_cmp_ne_u32 s[56:57], 0, v148\n"
      "v_mul_f32 v158, v108, v158\n"
      "v_mfma_f32_16x16x32_bf16 v[218:221], a[124:127], v[206:209], v[218:221]\n"
      "v_mul_f32 v160, s50, v160\n"
      "v_mul_f32_e64 v199, v160, v199\n"
      "v_mfma_f32_16x16x32_bf16 v[218:221], a[128:131], v[162:165], v[218:221]\n"
      "s_cmp_gt_u32 s42, s44\n"
      "s_cselect_b32 s95, s94, s95\n"
      "v_mul_f32 v100, v108, v100\n"
      "v_mfma_f32_16x16x32_bf16 v[218:221], a[132:135], v[214:217], v[218:221]\n"
      "v_mul_f32 v103, v108, v103\n"
      "v_fmac_f32 v98, v110, v110\n"
      "v_mfma_f32_16x16x32_bf16 v[218:221], a[136:139], v[178:181], v[218:221]\n"
      "v_mul_f32 v159, v108, v159\n"
      "v_fmac_f32_e64 v98, v111, v111\n"
      "v_mfma_f32_16x16x32_bf16 v[218:221], a[140:143], v[174:177], v[218:221]\n"
      "ds_write_b128 v5, v[210:213] offset:6144\n"
      "v_pk_mul_f32 v[146:147], v[146:147], v[108:109] op_sel_hi:[1,0]\n"
      "v_pk_mul_f32 v[130:131], v[130:131], v[160:161] op_sel_hi:[1,0]\n"
      "v_mfma_f32_16x16x32_bf16 v[148:151], v[170:173], a[144:147], 0\n"
      "v_fmac_f32 v76, v191, v191\n"
      "v_fmac_f32 v98, v112, v112\n"
      "v_mfma_f32_16x16x32_bf16 v[148:151], v[206:209], a[148:151], v[148:151]\n"
      "v_mul_f32_e64 v101, v108, v101\n"
      "v_cvt_pk_bf16_f32 v119, v120, v121\n"
      "v_mfma_f32_16x16x32_bf16 v[148:151], v[162:165], a[152:155], v[148:151]\n"
      "v_mul_f32 v102, v108, v102\n"
      "v_mul_f32 v156, v108, v156\n"
      "v_mfma_f32_16x16x32_bf16 v[148:151], v[214:217], a[156:159], v[148:151]\n"
      "v_mul_f32 v144, v108, v144\n"
      "v_fmac_f32 v98, v113, v113\n"
      "v_mfma_f32_16x16x32_bf16 v[148:151], v[178:181], a[160:163], v[148:151]\n"
      "v_mul_f32 v201, v160, v201\n"
      "v_mul_f32 v204, v160, v204\n"
      "v_mfma_f32_16x16x32_bf16 v[148:151], v[174:177], a[164:167], v[148:151]\n"
      "ds_write_b64 v6, v[118:119] offset:6144\n"
      "v_mfma_f32_16x16x32_bf16 v[122:125], v[170:173], a[168:171], 0\n"
      "v_fmac_f32 v98, v218, v218\n"
      "v_cvt_pk_bf16_f32 v100, v100, v101\n"
      "v_mfma_f32_16x16x32_bf16 v[122:125], v[206:209], a[172:175], v[122:125]\n"
      "v_mul_f32 v205, v160, v205\n"
      "v_fmac_f32 v98, v219, v219\n"
      "v_mfma_f32_16x16x32_bf16 v[122:125], v[162:165], a[176:179], v[122:125]\n"
      "ds_read_b128 v[118:121], v1 offset:4096\n"
      "v_mul_f32 v128, v160, v128\n"
      "v_mfma_f32_16x16x32_bf16 v[122:125], v[214:217], a[180:183], v[122:125]\n"
      "v_mul_f32 v129, v160, v129\n"
      "v_cvt_pk_bf16_f32 v197, v150, v151\n"
      "v_mfma_f32_16x16x32_bf16 v[122:125], v[178:181], a[184:187], v[122:125]\n"
      "v_fmac_f32 v98, v220, v220\n"
      "v_fmac_f32_e64 v76, v192, v192\n"
      "v_mfma_f32_16x16x32_bf16 v[122:125], v[174:177], a[188:191], v[122:125]\n"
      "v_fmac_f32_e64 v98, v221, v221\n"
      "v_cvt_pk_bf16_f32 v156, v156, v157\n"
      "v_mfma_f32_16x16x32_bf16 v[210:213], v[170:173], a[192:195], 0\n"
      "ds_read_b128 v[170:173], v0 offset:10240\n"
      "v_fmac_f32 v76, v193, v193\n"
      "v_mfma_f32_16x16x32_bf16 v[210:213], v[206:209], a[196:199], v[210:213]\n"
      "s_nop 0\n"
      "v_permlane16_swap_b32_e64 v76, v98\n"
      "v_mfma_f32_16x16x32_bf16 v[210:213], v[162:165], a[200:203], v[210:213]\n"
      "v_mul_f32 v198, v160, v198\n"
      "v_add_f32 v76, v76, v98\n"
      "v_mfma_f32_16x16x32_bf16 v[210:213], v[214:217], a[204:207], v[210:213]\n"
      "ds_read_b128 v[214:217], v1 offset:16384\n"
      "v_mov_b32 v98, v76\n"
      "v_mfma_f32_16x16x32_bf16 v[210:213], v[178:181], a[208:211], v[210:213]\n"
      "s_nop 0\n"
      "v_permlane32_swap_b32 v76, v98\n"
      "v_mfma_f32_16x16x32_bf16 v[210:213], v[174:177], a[212:215], v[210:213]\n"
      "s_waitcnt lgkmcnt(2)\n"
      "v_mul_f32 v145, v108, v145\n"
      "v_add_f32 v76, v76, v98\n"
      "v_mfma_f32_16x16x32_bf16 v[178:181], a[0:3], v[138:141], 0\n"
      "s_waitcnt lgkmcnt(1)\n"
      "v_rsq_f32 v76, v76\n"
      "v_mfma_f32_16x16x32_bf16 v[178:181], a[4:7], v[118:121], v[178:181]\n"
      "ds_read_b64 v[108:109], v10 offset:24\n"
      "v_mov_b32 v98, v76\n"
      "v_mfma_f32_16x16x32_bf16 v[178:181], a[8:11], v[170:173], v[178:181]\n"
      "s_and_b32 s86, s86, -4\n"
      "v_cvt_pk_bf16_f32 v198, v198, v199\n"
      "v_cvt_pk_bf16_f32 v128, v128, v129\n"
      "v_mfma_f32_16x16x32_bf16 v[178:181], a[12:15], v[134:137], v[178:181]\n"
      "v_permlane16_swap_b32 v76, v98\n"
      "v_mfma_f32_16x16x32_bf16 v[178:181], a[16:19], v[114:117], v[178:181]\n"
      "s_waitcnt lgkmcnt(1)\n"
      "v_cvt_pk_bf16_f32 v101, v102, v103\n"
      "v_mul_f32 v200, v160, v200\n"
      "v_mfma_f32_16x16x32_bf16 v[178:181], a[20:23], v[214:217], v[178:181]\n"
      "v_mul_f32 v218, v98, v218\n"
      "v_mul_f32_e64 v202, v160, v202\n"
      "v_mfma_f32_16x16x32_bf16 v[206:209], a[24:27], v[138:141], 0\n"
      "v_mul_f32 v113, v98, v113\n"
      "v_mul_f32 v76, s50, v76\n"
      "v_mfma_f32_16x16x32_bf16 v[206:209], a[28:31], v[118:121], v[206:209]\n"
      "v_mul_f32 v203, v160, v203\n"
      "v_mul_f32 v192, v76, v192\n"
      "v_mfma_f32_16x16x32_bf16 v[206:209], a[32:35], v[170:173], v[206:209]\n"
      "v_cvt_pk_bf16_f32 v157, v158, v159\n"
      "v_mul_f32_e64 v186, v98, v186\n"
      "v_mfma_f32_16x16x32_bf16 v[206:209], a[36:39], v[134:137], v[206:209]\n"
      "v_fma_f32 v142, v178, v178, s51\n"
      "v_mul_f32 v187, v98, v187\n"
      "v_mfma_f32_16x16x32_bf16 v[206:209], a[40:43], v[114:117], v[206:209]\n"
      "v_mul_f32 v220, v98, v220\n"
      "v_mul_f32_e64 v189, v98, v189\n"
      "v_mfma_f32_16x16x32_bf16 v[206:209], a[44:47], v[214:217], v[206:209]\n"
      "v_fmac_f32 v142, v179, v179\n"
      "v_mul_f32 v188, v98, v188\n"
      "v_mfma_f32_16x16x32_bf16 v[158:161], a[48:51], v[138:141], 0\n"
      "v_fmac_f32 v142, v180, v180\n"
      "v_mul_f32 v169, v76, v169\n"
      "v_mfma_f32_16x16x32_bf16 v[158:161], a[52:55], v[118:121], v[158:161]\n"
      "v_mul_f32 v219, v98, v219\n"
      "v_mul_f32 v183, v76, v183\n"
      "v_mfma_f32_16x16x32_bf16 v[158:161], a[56:59], v[170:173], v[158:161]\n"
      "v_mul_f32 v221, v98, v221\n"
      "v_fmac_f32 v142, v181, v181\n"
      "v_mfma_f32_16x16x32_bf16 v[158:161], a[60:63], v[134:137], v[158:161]\n"
      "v_fmac_f32 v142, v206, v206\n"
      "v_mul_f32 v110, v98, v110\n"
      "v_cvt_pk_bf16_f32 v129, v130, v131\n"
      "v_mfma_f32_16x16x32_bf16 v[158:161], a[64:67], v[114:117], v[158:161]\n"
      "v_mul_f32 v182, v76, v182\n"
      "v_mul_f32 v185, v76, v185\n"
      "v_mfma_f32_16x16x32_bf16 v[158:161], a[68:71], v[214:217], v[158:161]\n"
      "v_mul_f32_e64 v112, v98, v112\n"
      "v_cvt_pk_bf16_f32 v106, v210, v211\n"
      "v_mfma_f32_16x16x32_bf16 v[162:165], a[72:75], v[138:141], 0\n"
      "v_cvt_pk_bf16_f32 v103, v146, v147\n"
      "v_mul_f32 v166, v76, v166\n"
      "v_mfma_f32_16x16x32_bf16 v[162:165], a[76:79], v[118:121], v[162:165]\n"
      "v_mul_f32 v193, v76, v193\n"
      "v_cvt_pk_bf16_f32 v196, v148, v149\n"
      "v_mfma_f32_16x16x32_bf16 v[162:165], a[80:83], v[170:173], v[162:165]\n"
      "v_cvt_pk_bf16_f32 v199, v200, v201\n"
      "v_cvt_pk_bf16_f32 v107, v212, v213\n"
      "v_mfma_f32_16x16x32_bf16 v[162:165], a[84:87], v[134:137], v[162:165]\n"
      "v_fmac_f32_e64 v142, v207, v207\n"
      "v_fmac_f32_e64 v142, v208, v208\n"
      "v_mfma_f32_16x16x32_bf16 v[162:165], a[88:91], v[114:117], v[162:165]\n"
      "v_cvt_pk_bf16_f32 v186, v186, v187\n"
      "v_cvt_pk_bf16_f32 v182, v182, v183\n"
      "v_mfma_f32_16x16x32_bf16 v[162:165], a[92:95], v[214:217], v[162:165]\n"
      "v_cvt_pk_bf16_f32 v130, v202, v203\n"
      "v_mul_f32_e64 v111, v98, v111\n"
      "v_mfma_f32_16x16x32_bf16 v[200:203], a[96:99], v[138:141], 0\n"
      "v_cvt_pk_bf16_f32 v218, v218, v219\n"
      "v_cvt_pk_bf16_f32 v187, v188, v189\n"
      "v_mfma_f32_16x16x32_bf16 v[200:203], a[100:103], v[118:121], v[200:203]\n"
      "ds_read_b128 v[210:213], v2 offset:24576\n"
      "v_cvt_pk_bf16_f32 v98, v122, v123\n"
      "v_mfma_f32_16x16x32_bf16 v[200:203], a[104:107], v[170:173], v[200:203]\n"
      "s_add_u32 s42, s42, 1\n"
      "v_cvt_pk_bf16_f32 v102, v144, v145\n"
      "v_fma_f32 v133, v162, v162, s51\n"
      "v_mfma_f32_16x16x32_bf16 v[200:203], a[108:111], v[134:137], v[200:203]\n"
      "v_fmac_f32 v142, v209, v209\n"
      "v_mul_f32 v190, v76, v190\n"
      "v_mfma_f32_16x16x32_bf16 v[200:203], a[112:115], v[114:117], v[200:203]\n"
      "v_fmac_f32 v142, v158, v158\n"
      "v_cvt_pk_bf16_f32 v188, v110, v111\n"
      "v_mfma_f32_16x16x32_bf16 v[200:203], a[116:119], v[214:217], v[200:203]\n"
      "v_cvt_pk_bf16_f32 v99, v124, v125\n"
      "v_cvt_pk_bf16_f32 v219, v220, v221\n"
      "v_mfma_f32_16x16x32_bf16 v[220:223], a[120:123], v[138:141], 0\n"
      "v_mul_f32 v167, v76, v167\n"
      "v_mul_f32 v168, v76, v168\n"
      "v_mfma_f32_16x16x32_bf16 v[220:223], a[124:127], v[118:121], v[220:223]\n"
      "v_fmac_f32 v133, v163, v163\n"
      "v_mul_f32 v184, v76, v184\n"
      "v_mfma_f32_16x16x32_bf16 v[220:223], a[128:131], v[170:173], v[220:223]\n"
      "v_fmac_f32 v133, v164, v164\n"
      "v_fmac_f32 v142, v159, v159\n"
      "v_mfma_f32_16x16x32_bf16 v[220:223], a[132:135], v[134:137], v[220:223]\n"
      "v_fmac_f32 v133, v165, v165\n"
      "v_cvt_pk_bf16_f32 v183, v184, v185\n"
      "v_mfma_f32_16x16x32_bf16 v[220:223], a[136:139], v[114:117], v[220:223]\n"
      "v_fmac_f32 v133, v200, v200\n"
      "v_fmac_f32_e64 v133, v201, v201\n"
      "v_mfma_f32_16x16x32_bf16 v[220:223], a[140:143], v[214:217], v[220:223]\n"
      "ds_read_u16 v78, v11\n"
      "v_cvt_pk_bf16_f32 v131, v204, v205\n"
      "v_mfma_f32_16x16x32_bf16 v[148:151], v[138:141], a[144:147], 0\n"
      "v_mul_f32 v191, v76, v191\n"
      "v_fmac_f32 v142, v160, v160\n"
      "v_mfma_f32_16x16x32_bf16 v[148:151], v[118:121], a[148:151], v[148:151]\n"
      "v_cvt_pk_bf16_f32 v190, v190, v191\n"
      "v_fmac_f32 v133, v202, v202\n"
      "v_mfma_f32_16x16x32_bf16 v[148:151], v[170:173], a[152:155], v[148:151]\n"
      "v_fmac_f32 v133, v203, v203\n"
      "v_cvt_pk_bf16_f32 v189, v112, v113\n"
      "v_mfma_f32_16x16x32_bf16 v[148:151], v[134:137], a[156:159], v[148:151]\n"
      "v_fmac_f32 v133, v220, v220\n"
      "v_fmac_f32 v133, v221, v221\n"
      "v_mfma_f32_16x16x32_bf16 v[148:151], v[114:117], a[160:163], v[148:151]\n"
      "v_cvt_pk_bf16_f32 v185, v168, v169\n"
      "v_cvt_pk_bf16_f32 v191, v192, v193\n"
      "v_mfma_f32_16x16x32_bf16 v[148:151], v[214:217], a[164:167], v[148:151]\n"
      "v_fmac_f32 v133, v222, v222\n"
      "v_fmac_f32 v133, v223, v223\n"
      "v_mfma_f32_16x16x32_bf16 v[122:125], v[138:141], a[168:171], 0\n"
      "v_fmac_f32_e64 v142, v161, v161\n"
      "v_cvt_pk_bf16_f32 v184, v166, v167\n"
      "v_mfma_f32_16x16x32_bf16 v[122:125], v[118:121], a[172:175], v[122:125]\n"
      "v_permlane16_swap_b32 v142, v133\n"
      "v_mfma_f32_16x16x32_bf16 v[122:125], v[170:173], a[176:179], v[122:125]\n"
      "v_add_f32 v142, v142, v133\n"
      "v_mov_b32 v133, v142\n"
      "v_mfma_f32_16x16x32_bf16 v[122:125], v[134:137], a[180:183], v[122:125]\n"
      "s_nop 0\n"
      "v_permlane32_swap_b32_e64 v142, v133\n"
      "v_mfma_f32_16x16x32_bf16 v[122:125], v[114:117], a[184:187], v[122:125]\n"
      "ds_read_b128 v[166:169], v2 offset:18432\n"
      "v_add_f32 v142, v142, v133\n"
      "v_mfma_f32_16x16x32_bf16 v[122:125], v[214:217], a[188:191], v[122:125]\n"
      "v_rsq_f32 v142, v142\n"
      "v_mfma_f32_16x16x32_bf16 v[110:113], v[138:141], a[192:195], 0\n"
      "ds_read_b128 v[138:141], v2 offset:21504\n"
      "v_mov_b32 v133, v142\n"
      "v_mfma_f32_16x16x32_bf16 v[110:113], v[118:121], a[196:199], v[110:113]\n"
      "s_nop 0\n"
      "v_permlane16_swap_b32_e64 v142, v133\n"
      "v_mfma_f32_16x16x32_bf16 v[110:113], v[170:173], a[200:203], v[110:113]\n"
      "v_mul_f32 v201, v133, v201\n"
      "v_mul_f32 v162, v133, v162\n"
      "v_mfma_f32_16x16x32_bf16 v[110:113], v[134:137], a[204:207], v[110:113]\n"
      "ds_read_b128 v[118:121], v2 offset:30720\n"
      "v_mul_f32 v142, s50, v142\n"
      "v_mfma_f32_16x16x32_bf16 v[110:113], v[114:117], a[208:211], v[110:113]\n"
      "s_waitcnt lgkmcnt(2)\n"
      "v_cvt_pk_bf16_f32 v76, v148, v149\n"
      "v_mul_f32_e64 v208, v142, v208\n"
      "v_mfma_f32_16x16x32_bf16 v[110:113], v[214:217], a[212:215], v[110:113]\n"
      "ds_read_b128 v[170:173], v2 offset:27648\n"
      "v_cvt_pk_bf16_f32 v80, v122, v123\n"
      "v_mfma_f32_16x16x32_bf16 v[174:177], a[216:219], v[166:169], 0\n"
      "s_waitcnt lgkmcnt(2)\n"
      "v_mul_f32 v164, v133, v164\n"
      "v_mul_f32 v222, v133, v222\n"
      "v_mfma_f32_16x16x32_bf16 v[174:177], a[220:223], v[138:141], v[174:177]\n"
      "v_mul_f32 v209, v142, v209\n"
      "v_mul_f32_e64 v203, v133, v203\n"
      "v_mfma_f32_16x16x32_bf16 v[174:177], a[224:227], v[210:213], v[174:177]\n"
      "v_cvt_pk_bf16_f32 v77, v150, v151\n"
      "v_cvt_pk_bf16_f32 v85, v112, v113\n"
      "v_mfma_f32_16x16x32_bf16 v[214:217], a[240:243], v[166:169], 0\n"
      "v_mul_f32 v179, v142, v179\n"
      "v_mul_f32 v158, v142, v158\n"
      "v_mfma_f32_16x16x32_bf16 v[214:217], a[244:247], v[138:141], v[214:217]\n"
      "ds_read_b128 v[144:147], v2 offset:33792\n"
      "v_mul_f32_e64 v200, v133, v200\n"
      "v_mfma_f32_16x16x32_bf16 v[214:217], a[248:251], v[210:213], v[214:217]\n"
      "v_mul_f32 v165, v133, v165\n"
      "v_mul_f32 v206, v142, v206\n"
      "v_mfma_f32_16x16x32_bf16 v[148:151], v[232:235], v[166:169], 0\n"
      "v_mul_f32 v220, v133, v220\n"
      "v_mul_f32 v180, v142, v180\n"
      "v_mfma_f32_16x16x32_bf16 v[148:151], v[236:239], v[138:141], v[148:151]\n"
      "s_waitcnt lgkmcnt(1)\n"
      "v_mul_f32 v161, v142, v161\n"
      "v_mul_f32_e64 v207, v142, v207\n"
      "v_mfma_f32_16x16x32_bf16 v[148:151], v[240:243], v[210:213], v[148:151]\n"
      "ds_read_b128 v[166:169], v10 offset:32\n"
      "v_mul_f32 v163, v133, v163\n"
      "v_mfma_f32_16x16x32_bf16 v[174:177], a[228:231], v[170:173], v[174:177]\n"
      "v_mul_f32 v178, v142, v178\n"
      "v_mul_f32_e64 v221, v133, v221\n"
      "v_mfma_f32_16x16x32_bf16 v[174:177], a[232:235], v[118:121], v[174:177]\n"
      "s_waitcnt lgkmcnt(1)\n"
      "v_mul_f32 v223, v133, v223\n"
      "v_mul_f32_e64 v202, v133, v202\n"
      "v_mfma_f32_16x16x32_bf16 v[174:177], a[236:239], v[144:147], v[174:177]\n"
      "ds_read_b128 v[114:117], v2 offset:18688\n"
      "v_cvt_pk_bf16_f32 v84, v110, v111\n"
      "v_mfma_f32_16x16x32_bf16 v[214:217], a[252:255], v[170:173], v[214:217]\n"
      "ds_read_u16 v82, v11 offset:32\n"
      "v_cvt_pk_bf16_f32 v162, v162, v163\n"
      "v_mfma_f32_16x16x32_bf16 v[214:217], v[224:227], v[118:121], v[214:217]\n"
      "v_cvt_pk_bf16_f32 v178, v178, v179\n"
      "v_mul_f32_e64 v181, v142, v181\n"
      "v_mfma_f32_16x16x32_bf16 v[214:217], v[228:231], v[144:147], v[214:217]\n"
      "v_cvt_pk_bf16_f32 v220, v220, v221\n"
      "v_fma_f32 v126, v174, v174, s52\n"
      "v_mfma_f32_16x16x32_bf16 v[148:151], v[244:247], v[170:173], v[148:151]\n"
      "v_cvt_pk_bf16_f32 v163, v164, v165\n"
      "v_mul_f32 v127, v175, v175\n"
      "v_mfma_f32_16x16x32_bf16 v[148:151], v[248:251], v[118:121], v[148:151]\n"
      "v_cvt_pk_bf16_f32 v164, v200, v201\n"
      "v_fmac_f32 v127, v177, v177\n"
      "v_mfma_f32_16x16x32_bf16 v[148:151], v[252:255], v[144:147], v[148:151]\n"
      "v_cvt_pk_bf16_f32 v221, v222, v223\n"
      "v_cvt_pk_bf16_f32 v165, v202, v203\n"
      "v_mfma_f32_16x16x32_bf16 v[170:173], v[100:103], v[128:131], v[40:43]\n"
      "v_cvt_pk_bf16_f32 v179, v180, v181\n"
      "v_cvt_pk_bf16_f32 v180, v206, v207\n"
      "v_mfma_f32_16x16x32_bf16 v[200:203], v[186:189], v[128:131], v[44:47]\n"
      "s_waitcnt lgkmcnt(0)\n"
      "v_fmac_f32 v126, v176, v176\n"
      "v_cvt_pk_bf16_f32 v181, v208, v209\n"
      "v_mfma_f32_16x16x32_bf16 v[206:209], v[162:165], v[128:131], v[48:51]\n"
      "ds_read_b128 v[144:147], v2 offset:21760\n"
      "v_fmac_f32_e64 v127, v215, v215\n"
      "v_mfma_f32_16x16x32_bf16 v[138:141], v[166:169], v[128:131], v[24:27]\n"
      "ds_read_b128 v[130:133], v2 offset:24832\n"
      "v_fmac_f32 v126, v214, v214\n"
      "v_mfma_f32_16x16x16_bf16 v[170:173], v[156:157], v[198:199], v[170:173]\n"
      "v_mul_f32 v159, v142, v159\n"
      "s_waitcnt vmcnt(1)\n"
      "v_or3_b32 v152, v152, v153, v154\n"
      "v_fmac_f32 v126, v216, v216\n"
      "v_mfma_f32_16x16x16_bf16 v[200:203], v[218:219], v[198:199], v[200:203]\n"
      "v_fmac_f32 v127, v217, v217\n"
      "v_fmac_f32 v127, v149, v149\n"
      "v_cvt_pk_bf16_f32 v81, v124, v125\n"
      "v_mfma_f32_16x16x16_bf16 v[206:209], v[220:221], v[198:199], v[206:209]\n"
      "v_fmac_f32 v126, v148, v148\n"
      "v_exp_f32 v173, v173\n"
      "v_mfma_f32_16x16x16_bf16 v[138:141], v[108:109], v[198:199], v[138:141]\n"
      "v_mul_f32 v160, v142, v160\n"
      "v_exp_f32 v171, v171\n"
      "v_mfma_f32_16x16x32_bf16 v[210:213], a[216:219], v[114:117], 0\n"
      "s_waitcnt lgkmcnt(1)\n"
      "v_exp_f32 v200, v200\n"
      "v_mfma_f32_16x16x32_bf16 v[210:213], a[220:223], v[144:147], v[210:213]\n"
      "s_waitcnt lgkmcnt(0)\n"
      "v_exp_f32 v208, v208\n"
      "v_mfma_f32_16x16x32_bf16 v[210:213], a[224:227], v[130:133], v[210:213]\n"
      "v_exp_f32 v207, v207\n"
      "v_mfma_f32_16x16x32_bf16 v[122:125], a[240:243], v[114:117], 0\n"
      "v_exp_f32 v209, v209\n"
      "v_mfma_f32_16x16x32_bf16 v[122:125], a[244:247], v[144:147], v[122:125]\n"
      "v_exp_f32 v138, v138\n"
      "v_mfma_f32_16x16x32_bf16 v[122:125], a[248:251], v[130:133], v[122:125]\n"
      "v_cvt_pk_bf16_f32 v158, v158, v159\n"
      "s_waitcnt vmcnt(0)\n"
      "v_or3_b32 v152, v152, v155, v89\n"
      "v_mfma_f32_16x16x32_bf16 v[118:121], v[232:235], v[114:117], 0\n"
      "ds_write_b32 v31, v152 offset:4608\n"
      "v_mfma_f32_16x16x32_bf16 v[118:121], v[236:239], v[144:147], v[118:121]\n"
      "v_cvt_pk_bf16_f32 v159, v160, v161\n"
      "v_cvt_pk_bf16_f32 v89, v208, v209\n"
      "v_mfma_f32_16x16x32_bf16 v[118:121], v[240:243], v[130:133], v[118:121]\n"
      "v_exp_f32_e64 v206, v206\n"
      "v_mfma_f32_16x16x32_bf16 v[152:155], v[100:103], v[182:185], v[52:55]\n"
      "v_exp_f32 v170, v170\n"
      "v_mfma_f32_16x16x32_bf16 v[112:115], v[186:189], v[182:185], v[56:59]\n"
      "v_exp_f32 v201, v201\n"
      "v_mfma_f32_16x16x32_bf16 v[128:131], v[162:165], v[182:185], v[60:63]\n"
      "v_fmac_f32 v126, v150, v150\n"
      "v_cvt_pk_bf16_f32 v88, v206, v207\n"
      "v_mfma_f32_16x16x32_bf16 v[206:209], v[166:169], v[182:185], v[26:29]\n"
      "v_exp_f32 v203, v203\n"
      "v_mfma_f32_16x16x16_bf16 v[152:155], v[156:157], v[190:191], v[152:155]\n"
      "v_exp_f32 v172, v172\n"
      "v_cvt_pk_bf16_f32 v170, v170, v171\n"
      "v_mfma_f32_16x16x16_bf16 v[112:115], v[218:219], v[190:191], v[112:115]\n"
      "v_fmac_f32 v127, v151, v151\n"
      "v_exp_f32_e64 v202, v202\n"
      "v_mfma_f32_16x16x16_bf16 v[128:131], v[220:221], v[190:191], v[128:131]\n"
      "v_cvt_pk_bf16_f32 v171, v172, v173\n"
      "v_cvt_pk_bf16_f32 v173, v202, v203\n"
      "v_cvt_pk_bf16_f32 v172, v200, v201\n"
      "v_mfma_f32_16x16x16_bf16 v[206:209], v[108:109], v[190:191], v[206:209]\n"
      "v_exp_f32 v153, v153\n"
      "v_cvt_pk_bf16_f32 v90, v138, 0\n"
      "v_mfma_f32_16x16x32_bf16 v[200:203], v[32:35], v[170:173], 0\n"
      "v_exp_f32 v152, v152\n"
      "v_add_f32 v126, v126, v127\n"
      "v_mfma_f32_16x16x32_bf16 v[200:203], v[32:35], v[88:91], v[200:203]\n"
      "v_exp_f32 v129, v129\n"
      "v_mfma_f32_16x16x32_bf16 v[144:147], v[194:197], v[170:173], 0\n"
      "v_exp_f32_e64 v128, v128\n"
      "v_mfma_f32_16x16x32_bf16 v[144:147], v[76:79], v[88:91], v[144:147]\n"
      "ds_read_b128 v[140:143], v2 offset:27904\n"
      "v_cvt_pk_bf16_f32 v92, v128, v129\n"
      "v_mfma_f32_16x16x32_bf16 v[190:193], v[96:99], v[170:173], 0\n"
      "v_exp_f32 v154, v154\n"
      "v_mfma_f32_16x16x32_bf16 v[190:193], v[80:83], v[88:91], v[190:193]\n"
      "v_rcp_f32 v200, v200\n"
      "v_mfma_f32_16x16x32_bf16 v[202:205], v[104:107], v[170:173], 0\n"
      "v_exp_f32_e64 v114, v114\n"
      "v_mfma_f32_16x16x32_bf16 v[202:205], v[84:87], v[88:91], v[202:205]\n"
      "v_exp_f32 v115, v115\n"
      "v_mfma_f32_16x16x32_bf16 v[170:173], v[100:103], v[178:181], v[64:67]\n"
      "v_exp_f32 v155, v155\n"
      "v_mfma_f32_16x16x32_bf16 v[132:135], v[186:189], v[178:181], v[68:71]\n"
      "ds_read_b128 v[136:139], v2 offset:30976\n"
      "v_cvt_pk_bf16_f32 v152, v152, v153\n"
      "v_mfma_f32_16x16x32_bf16 v[182:185], v[162:165], v[178:181], v[72:75]\n"
      "s_waitcnt lgkmcnt(1)\n"
      "v_cvt_pk_bf16_f32 v153, v154, v155\n"
      "v_cvt_pk_bf16_f32 v155, v114, v115\n"
      "v_mfma_f32_16x16x32_bf16 v[114:117], v[166:169], v[178:181], v[28:31]\n"
      "ds_read_b128 v[178:181], v2 offset:34048\n"
      "v_mul_f32 v193, v200, v193\n"
      "v_mfma_f32_16x16x16_bf16 v[170:173], v[156:157], v[158:159], v[170:173]\n"
      "v_exp_f32 v131, v131\n"
      "v_exp_f32 v113, v113\n"
      "v_exp_f32_e64 v206, v206\n"
      "v_pk_mul_f32 v[144:145], v[144:145], v[200:201] op_sel_hi:[1,0]\n"
      "v_pk_mul_f32 v[202:203], v[202:203], v[200:201] op_sel_hi:[1,0]\n"
      "v_pk_mul_f32 v[204:205], v[204:205], v[200:201] op_sel_hi:[1,0]\n"
      "v_pk_mul_f32 v[146:147], v[146:147], v[200:201] op_sel_hi:[1,0]\n"
      "v_mfma_f32_16x16x16_bf16 v[132:135], v[218:219], v[158:159], v[132:135]\n"
      "v_exp_f32 v130, v130\n"
      "v_cvt_pk_bf16_f32 v144, v144, v145\n"
      "v_mfma_f32_16x16x16_bf16 v[182:185], v[220:221], v[158:159], v[182:185]\n"
      "v_exp_f32 v170, v170\n"
      "v_cvt_pk_bf16_f32 v202, v202, v203\n"
      "v_mfma_f32_16x16x16_bf16 v[114:117], v[108:109], v[158:159], v[114:117]\n"
      "v_exp_f32 v112, v112\n"
      "v_mul_f32 v190, v200, v190\n"
      "v_mfma_f32_16x16x32_bf16 v[210:213], a[228:231], v[140:143], v[210:213]\n"
      "s_waitcnt lgkmcnt(0)\n"
      "v_exp_f32 v173, v173\n"
      "v_mfma_f32_16x16x32_bf16 v[210:213], a[232:235], v[136:139], v[210:213]\n"
      "v_exp_f32_e64 v171, v171\n"
      "v_mfma_f32_16x16x32_bf16 v[210:213], a[236:239], v[178:181], v[210:213]\n"
      "v_cvt_pk_bf16_f32 v145, v146, v147\n"
      "v_cvt_pk_bf16_f32 v154, v112, v113\n"
      "v_mfma_f32_16x16x32_bf16 v[122:125], a[252:255], v[140:143], v[122:125]\n"
      "ds_read_b128 v[158:161], v7 offset:18432\n"
      "v_mul_f32_e64 v192, v200, v192\n"
      "v_mfma_f32_16x16x32_bf16 v[122:125], v[224:227], v[136:139], v[122:125]\n"
      "v_cvt_pk_bf16_f32 v147, v192, v193\n"
      "v_mul_f32 v191, v200, v191\n"
      "v_mfma_f32_16x16x32_bf16 v[122:125], v[228:231], v[178:181], v[122:125]\n"
      "v_exp_f32 v182, v182\n"
      "v_mfma_f32_16x16x32_bf16 v[118:121], v[244:247], v[140:143], v[118:121]\n"
      "ds_read_b128 v[198:201], v7 offset:22528\n"
      "v_cvt_pk_bf16_f32 v93, v130, v131\n"
      "v_mfma_f32_16x16x32_bf16 v[118:121], v[248:251], v[136:139], v[118:121]\n"
      "v_exp_f32_e64 v185, v185\n"
      "v_mfma_f32_16x16x32_bf16 v[118:121], v[252:255], v[178:181], v[118:121]\n"
      "ds_read_b64 v[222:223], v10 offset:16\n"
      "v_cvt_pk_bf16_f32 v94, v206, 0\n"
      "v_mfma_f32_16x16x32_bf16 v[178:181], v[32:35], v[152:155], 0\n"
      "ds_read_b128 v[206:209], v10\n"
      "v_cvt_pk_bf16_f32 v146, v190, v191\n"
      "v_mfma_f32_16x16x32_bf16 v[178:181], v[32:35], v[92:95], v[178:181]\n"
      "s_waitcnt lgkmcnt(3)\n"
      "v_exp_f32 v172, v172\n"
      "v_mfma_f32_16x16x32_bf16 v[128:131], v[194:197], v[152:155], 0\n"
      "buffer_store_dwordx4 v[158:161], v20, s[24:27], s46 offen sc1\n"
      "v_mfma_f32_16x16x32_bf16 v[128:131], v[76:79], v[92:95], v[128:131]\n"
      "s_nop 0\n"
      "ds_read_b128 v[158:161], v2 offset:18944\n"
      "v_cvt_pk_bf16_f32 v203, v204, v205\n"
      "v_mfma_f32_16x16x32_bf16 v[140:143], v[96:99], v[152:155], 0\n"
      "v_exp_f32 v184, v184\n"
      "v_mfma_f32_16x16x32_bf16 v[140:143], v[80:83], v[92:95], v[140:143]\n"
      "v_rcp_f32 v178, v178\n"
      "v_mfma_f32_16x16x32_bf16 v[136:139], v[104:107], v[152:155], 0\n"
      "s_waitcnt lgkmcnt(1)\n"
      "v_exp_f32_e64 v183, v183\n"
      "v_mfma_f32_16x16x32_bf16 v[136:139], v[84:87], v[92:95], v[136:139]\n"
      "ds_write_b128 v3, v[144:147]\n"
      "v_mfma_f32_16x16x32_bf16 v[152:155], v[206:209], v[100:103], v[30:33]\n"
      "s_waitcnt lgkmcnt(1)\n"
      "v_exp_f32 v133, v133\n"
      "v_mfma_f32_16x16x32_bf16 v[144:147], v[206:209], v[186:189], v[30:33]\n"
      "ds_read_b128 v[110:113], v7 offset:30720\n"
      "v_cvt_pk_bf16_f32 v170, v170, v171\n"
      "v_mfma_f32_16x16x32_bf16 v[100:103], v[206:209], v[162:165], v[30:33]\n"
      "ds_read_b128 v[162:165], v2 offset:25088\n"
      "v_cvt_pk_bf16_f32 v89, v184, v185\n"
      "v_mfma_f32_16x16x32_bf16 v[184:187], v[206:209], v[166:169], v[30:33]\n"
      "ds_read_b128 v[206:209], v2 offset:22016\n"
      "v_cvt_pk_bf16_f32 v171, v172, v173\n"
      "v_mfma_f32_16x16x16_bf16 v[152:155], v[222:223], v[156:157], v[152:155]\n"
      "v_exp_f32 v114, v114\n"
      "v_mul_f32 v191, v211, v211\n"
      "v_mfma_f32_16x16x16_bf16 v[144:147], v[222:223], v[218:219], v[144:147]\n"
      "v_fma_f32 v190, v210, v210, s52\n"
      "v_exp_f32_e64 v132, v132\n"
      "v_mfma_f32_16x16x16_bf16 v[100:103], v[222:223], v[220:221], v[100:103]\n"
      "buffer_store_dwordx4 v[198:201], v21, s[24:27], s46 offen sc1\n"
      "v_pk_mul_f32 v[130:131], v[130:131], v[178:179] op_sel_hi:[1,0]\n"
      "v_pk_fma_f32 v[190:191], v[212:213], v[212:213], v[190:191]\n"
      "v_pk_mul_f32 v[142:143], v[142:143], v[178:179] op_sel_hi:[1,0]\n"
      "v_pk_mul_f32 v[136:137], v[136:137], v[178:179] op_sel_hi:[1,0]\n"
      "v_pk_mul_f32 v[128:129], v[128:129], v[178:179] op_sel_hi:[1,0]\n"
      "v_pk_mul_f32 v[140:141], v[140:141], v[178:179] op_sel_hi:[1,0]\n"
      "v_pk_mul_f32 v[138:139], v[138:139], v[178:179] op_sel_hi:[1,0]\n"
      "v_pk_fma_f32 v[190:191], v[122:123], v[122:123], v[190:191]\n"
      "v_mfma_f32_16x16x16_bf16 v[184:187], v[222:223], v[108:109], v[184:187]\n"
      "v_cvt_pk_bf16_f32 v128, v128, v129\n"
      "v_exp_f32 v135, v135\n"
      "v_mfma_f32_16x16x32_bf16 v[220:223], a[216:219], v[158:161], 0\n"
      "s_waitcnt lgkmcnt(0)\n"
      "v_cvt_pk_bf16_f32 v129, v130, v131\n"
      "v_cvt_pk_bf16_f32 v131, v142, v143\n"
      "v_mfma_f32_16x16x32_bf16 v[220:223], a[220:223], v[206:209], v[220:223]\n"
      "ds_read_b128 v[178:181], v7 offset:26624\n"
      "v_cvt_pk_bf16_f32 v130, v140, v141\n"
      "v_mfma_f32_16x16x32_bf16 v[220:223], a[224:227], v[162:165], v[220:223]\n"
      "ds_write_b128 v3, v[128:131] offset:256\n"
      "v_pk_fma_f32 v[190:191], v[124:125], v[124:125], v[190:191]\n"
      "v_pk_fma_f32 v[190:191], v[118:119], v[118:119], v[190:191]\n"
      "v_mfma_f32_16x16x32_bf16 v[166:169], a[240:243], v[158:161], 0\n"
      "ds_read_b128 v[198:201], v2 offset:31232\n"
      "v_cvt_pk_bf16_f32 v172, v132, v133\n"
      "v_mfma_f32_16x16x32_bf16 v[166:169], a[244:247], v[206:209], v[166:169]\n"
      "v_exp_f32 v134, v134\n"
      "v_mfma_f32_16x16x32_bf16 v[166:169], a[248:251], v[162:165], v[166:169]\n"
      "ds_write_b64 v4, v[202:203]\n"
      "v_mfma_f32_16x16x32_bf16 v[186:189], v[232:235], v[158:161], 0\n"
      "s_waitcnt lgkmcnt(3)\n"
      "v_cvt_pk_bf16_f32 v173, v134, v135\n"
      "v_cvt_pk_bf16_f32 v90, v114, 0\n"
      "v_mfma_f32_16x16x32_bf16 v[186:189], v[236:239], v[206:209], v[186:189]\n"
      "buffer_store_dwordx4 v[178:181], v22, s[24:27], s46 offen sc1\n"
      "v_pk_fma_f32 v[190:191], v[120:121], v[120:121], v[190:191]\n"
      "v_mfma_f32_16x16x32_bf16 v[186:189], v[240:243], v[162:165], v[186:189]\n"
      "ds_read_b128 v[178:181], v2 offset:28160\n"
      "v_cvt_pk_bf16_f32 v88, v182, v183\n"
      "v_mfma_f32_16x16x32_bf16 v[160:163], v[32:35], v[170:173], 0\n"
      "v_cndmask_b32 v152, v152, v144, s[58:59]\n"
      "v_add_f32_e64 v190, v190, v191\n"
      "v_mfma_f32_16x16x32_bf16 v[160:163], v[32:35], v[88:91], v[160:163]\n"
      "s_nop 0\n"
      "v_permlane16_swap_b32 v126, v190\n"
      "v_mfma_f32_16x16x32_bf16 v[206:209], v[194:197], v[170:173], 0\n"
      "ds_read_b128 v[154:157], v2 offset:34304\n"
      "v_cvt_pk_bf16_f32 v136, v136, v137\n"
      "v_mfma_f32_16x16x32_bf16 v[206:209], v[76:79], v[88:91], v[206:209]\n"
      "v_cvt_pk_bf16_f32 v137, v138, v139\n"
      "v_cndmask_b32 v152, v152, v100, s[60:61]\n"
      "v_mfma_f32_16x16x32_bf16 v[202:205], v[96:99], v[170:173], 0\n"
      "ds_read_b128 v[162:165], v7 offset:34816\n"
      "v_add_f32 v126, v126, v190\n"
      "v_mfma_f32_16x16x32_bf16 v[202:205], v[80:83], v[88:91], v[202:205]\n"
      "v_rcp_f32 v160, v160\n"
      "v_mfma_f32_16x16x32_bf16 v[130:133], v[104:107], v[170:173], 0\n"
      "buffer_store_dwordx4 v[110:113], v23, s[24:27], s46 offen sc1\n"
      "v_pk_mul_f32 v[206:207], v[206:207], v[160:161] op_sel_hi:[1,0]\n"
      "v_pk_mul_f32 v[208:209], v[208:209], v[160:161] op_sel_hi:[1,0]\n"
      "v_cvt_pk_bf16_f32 v206, v206, v207\n"
      "v_mfma_f32_16x16x32_bf16 v[130:133], v[84:87], v[88:91], v[130:133]\n"
      "s_waitcnt lgkmcnt(1)\n"
      "v_cndmask_b32 v152, v152, v184, s[62:63]\n"
      "v_mul_f32 v203, v160, v203\n"
      "v_mfma_f32_16x16x32_bf16 v[220:223], a[228:231], v[178:181], v[220:223]\n"
      "v_cvt_pk_bf16_f32 v207, v208, v209\n"
      "v_mul_f32_e64 v204, v160, v204\n"
      "v_mfma_f32_16x16x32_bf16 v[220:223], a[232:235], v[198:201], v[220:223]\n"
      "ds_write_b64 v4, v[136:137] offset:256\n"
      "v_mfma_f32_16x16x32_bf16 v[220:223], a[236:239], v[154:157], v[220:223]\n"
      "v_exp_f32 v152, v152\n"
      "v_mfma_f32_16x16x32_bf16 v[166:169], a[252:255], v[178:181], v[166:169]\n"
      "v_mul_f32 v202, v160, v202\n"
      "v_mul_f32 v205, v160, v205\n"
      "v_mfma_f32_16x16x32_bf16 v[166:169], v[224:227], v[198:201], v[166:169]\n"
      "s_waitcnt lgkmcnt(1)\n"
      "v_cvt_pk_bf16_f32 v209, v204, v205\n"
      "v_cvt_pk_bf16_f32 v152, v152, v152\n"
      "v_mfma_f32_16x16x32_bf16 v[166:169], v[228:231], v[154:157], v[166:169]\n"
      "ds_write_b16 v13, v152\n"
      "v_mfma_f32_16x16x32_bf16 v[186:189], v[244:247], v[178:181], v[186:189]\n"
      "ds_read_b128 v[100:103], v14 offset:64\n"
      "v_fma_f32 v112, v220, v220, s52\n"
      "v_mfma_f32_16x16x32_bf16 v[186:189], v[248:251], v[198:201], v[186:189]\n"
      "ds_read_b128 v[178:181], v14\n"
      "v_cvt_pk_bf16_f32 v208, v202, v203\n"
      "v_mfma_f32_16x16x32_bf16 v[186:189], v[252:255], v[154:157], v[186:189]\n"
      "ds_write_b128 v3, v[206:209] offset:512\n"
      "v_mul_f32_e64 v113, v221, v221\n"
      "v_pk_mul_f32 v[130:131], v[130:131], v[160:161] op_sel_hi:[1,0]\n"
      "v_pk_fma_f32 v[112:113], v[222:223], v[222:223], v[112:113]\n"
      "v_pk_fma_f32 v[112:113], v[166:167], v[166:167], v[112:113]\n"
      "v_pk_mul_f32 v[132:133], v[132:133], v[160:161] op_sel_hi:[1,0]\n"
      "v_cvt_pk_bf16_f32 v130, v130, v131\n"
      "v_pk_fma_f32 v[112:113], v[168:169], v[168:169], v[112:113]\n"
      "v_pk_fma_f32 v[112:113], v[186:187], v[186:187], v[112:113]\n"
      "v_cvt_pk_bf16_f32 v131, v132, v133\n"
      "v_pk_fma_f32 v[112:113], v[188:189], v[188:189], v[112:113]\n"
      "v_add_f32 v112, v112, v113\n"
      "v_mov_b32 v190, v112\n"
      "s_and_b32 s5, s84, s8\n"
      "ds_write_b64 v4, v[130:131] offset:512\n"
      "v_permlane16_swap_b32 v112, v190\n"
      "v_add_f32 v112, v112, v190\n"
      "s_nop 1\n"
      "v_permlane32_swap_b32 v126, v112\n"
      "v_add_f32 v126, v126, v112\n"
      "s_add_u32 s43, s43, 1\n"
      "ds_write_b32 v31, v126\n"
      "buffer_store_dwordx4 v[162:165], v25, s[24:27], s46 offen sc1\n"
      "s_waitcnt lgkmcnt(2)\n"
      "s_nop 0\n"
      "v_mfma_f32_16x16x32_bf16 v[160:163], v[178:181], v[32:35], 0\n"
      "s_cmp_ge_u32 s95, 0x40000000\n"
      "s_cselect_b64 vcc, -1, s[56:57]\n"
      "v_mfma_f32_16x16x32_bf16 v[160:163], v[100:103], v[32:35], v[160:163]\n"
      "s_waitcnt lgkmcnt(0)\n"
      "s_barrier\n"
      "s_cbranch_vccz L_loop1_%=\n"
      "s_branch L_disp1_%=\n"
      "s_nop 7\n"
      "s_nop 7\n"
      "L_max1_%=:\n"
      "v_mfma_f32_16x16x32_bf16 v[112:115], v[178:181], v[194:197], 0\n"
      "ds_read_b128 v[140:143], v0 offset:18432\n"
      "s_add_u32 m0, s64, 0\n"
      "v_mfma_f32_16x16x32_bf16 v[112:115], v[100:103], v[76:79], v[112:115]\n"
      "ds_read_b128 v[156:159], v9\n"
      "v_mfma_f32_16x16x32_bf16 v[126:129], v[178:181], v[96:99], 0\n"
      "ds_read_b128 v[190:193], v1 offset:30720\n"
      "ds_read_b128 v[108:111], v1 offset:18432\n"
      "v_mfma_f32_16x16x32_bf16 v[126:129], v[100:103], v[80:83], v[126:129]\n"
      "buffer_load_dwordx4 v16, s[16:19], s84 offen  lds\n"
      "v_mfma_f32_16x16x32_bf16 v[152:155], v[178:181], v[104:107], 0\n"
      "ds_read_b128 v[144:147], v1 offset:24576\n"
      "v_readlane_b32 s45, v37, s43\n"
      "v_mfma_f32_16x16x32_bf16 v[152:155], v[100:103], v[84:87], v[152:155]\n"
      "ds_read_b128 v[114:117], v0 offset:24576\n"
      "s_waitcnt lgkmcnt(5)\n"
      "v_mfma_f32_16x16x32_bf16 v[206:209], a[0:3], v[140:143], 0\n"
      "ds_read_b128 v[182:185], v0 offset:26624\n"
      "v_cndmask_b32 v112, v112, v126, s[58:59]\n"
      "v_mfma_f32_16x16x32_bf16 v[198:201], a[24:27], v[140:143], 0\n"
      "s_waitcnt lgkmcnt(5)\n"
      "v_add_f32 v158, v158, v159\n"
      "v_add_f32 v156, v156, v157\n"
      "v_mfma_f32_16x16x32_bf16 v[128:131], a[48:51], v[140:143], 0\n"
      "ds_read_b128 v[132:135], v0 offset:30720\n"
      "v_add_f32 v156, v156, v158\n"
      "v_mfma_f32_16x16x32_bf16 v[202:205], a[72:75], v[140:143], 0\n"
      "s_add_u32 m0, s64, 4096\n"
      "v_rsq_f32 v156, v156\n"
      "v_mfma_f32_16x16x32_bf16 v[100:103], a[96:99], v[140:143], 0\n"
      "s_waitcnt lgkmcnt(3)\n"
      "v_mul_f32 v156, 0x415db3d7, v156\n"
      "v_mov_b32 v158, v156\n"
      "v_mfma_f32_16x16x32_bf16 v[162:165], a[120:123], v[140:143], 0\n"
      "buffer_load_dwordx4 v17, s[16:19], s84 offen  lds\n"
      "v_mfma_f32_16x16x32_bf16 v[206:209], a[4:7], v[108:111], v[206:209]\n"
      "s_waitcnt lgkmcnt(1)\n"
      "v_permlane32_swap_b32 v156, v158\n"
      "v_mfma_f32_16x16x32_bf16 v[206:209], a[8:11], v[114:117], v[206:209]\n"
      "ds_read_b128 v[178:181], v9 offset:4608\n"
      "v_mov_b32 v157, v156\n"
      "v_mfma_f32_16x16x32_bf16 v[198:201], a[28:31], v[108:111], v[198:201]\n"
      "s_nop 0\n"
      "v_permlane16_swap_b32_e64 v156, v157\n"
      "v_mfma_f32_16x16x32_bf16 v[198:201], a[32:35], v[114:117], v[198:201]\n"
      "v_mul_f32 v214, v156, v214\n"
      "v_mul_f32 v217, v156, v217\n"
      "v_mfma_f32_16x16x32_bf16 v[128:131], a[52:55], v[108:111], v[128:131]\n"
      "v_mul_f32 v175, v156, v175\n"
      "v_mul_f32 v215, v156, v215\n"
      "v_mfma_f32_16x16x32_bf16 v[128:131], a[56:59], v[114:117], v[128:131]\n"
      "s_add_u32 m0, s64, 8192\n"
      "v_mul_f32 v174, v156, v174\n"
      "v_mul_f32 v149, v156, v149\n"
      "v_mfma_f32_16x16x32_bf16 v[202:205], a[76:79], v[108:111], v[202:205]\n"
      "v_mul_f32 v123, v157, v123\n"
      "v_mul_f32 v120, v157, v120\n"
      "v_mfma_f32_16x16x32_bf16 v[202:205], a[80:83], v[114:117], v[202:205]\n"
      "buffer_load_dwordx4 v18, s[16:19], s84 offen  lds\n"
      "v_mfma_f32_16x16x32_bf16 v[100:103], a[100:103], v[108:111], v[100:103]\n"
      "v_mul_f32 v122, v157, v122\n"
      "v_cvt_pk_bf16_f32 v174, v174, v175\n"
      "v_mfma_f32_16x16x32_bf16 v[100:103], a[104:107], v[114:117], v[100:103]\n"
      "s_add_u32 m0, s64, 12288\n"
      "v_mul_f32 v177, v156, v177\n"
      "v_mul_f32 v186, v158, v186\n"
      "v_mfma_f32_16x16x32_bf16 v[162:165], a[124:127], v[108:111], v[162:165]\n"
      "s_waitcnt lgkmcnt(0)\n"
      "v_mul_f32 v220, v158, v220\n"
      "v_or3_b32 v178, v178, v179, v180\n"
      "v_mfma_f32_16x16x32_bf16 v[162:165], a[128:131], v[114:117], v[162:165]\n"
      "v_mul_f32 v221, v158, v221\n"
      "v_mul_f32 v216, v156, v216\n"
      "v_mul_f32 v121, v157, v121\n"
      "v_mfma_f32_16x16x32_bf16 v[206:209], a[12:15], v[144:147], v[206:209]\n"
      "buffer_load_dwordx4 v19, s[16:19], s84 offen  lds\n"
      "v_mfma_f32_16x16x32_bf16 v[206:209], a[16:19], v[132:135], v[206:209]\n"
      "v_mul_f32 v188, v158, v188\n"
      "v_mul_f32 v119, v157, v119\n"
      "v_mfma_f32_16x16x32_bf16 v[206:209], a[20:23], v[190:193], v[206:209]\n"
      "s_mov_b32 m0, s6\n"
      "v_mul_f32 v168, v158, v168\n"
      "v_mul_f32 v213, v157, v213\n"
      "v_mfma_f32_16x16x32_bf16 v[198:201], a[36:39], v[144:147], v[198:201]\n"
      "ds_read_b128 v[170:173], v0 offset:32768\n"
      "v_mul_f32 v125, v157, v125\n"
      "v_mfma_f32_16x16x32_bf16 v[198:201], a[40:43], v[132:135], v[198:201]\n"
      "v_mul_f32 v176, v156, v176\n"
      "v_mul_f32 v211, v157, v211\n"
      "v_mfma_f32_16x16x32_bf16 v[198:201], a[44:47], v[190:193], v[198:201]\n"
      "v_mul_f32_e64 v167, v158, v167\n"
      "v_readlane_b32 s46, v37, s42\n"
      "v_mfma_f32_16x16x32_bf16 v[128:131], a[60:63], v[144:147], v[128:131]\n"
      "v_mul_f32 v210, v157, v210\n"
      "v_cvt_pk_bf16_f32 v175, v176, v177\n"
      "v_mfma_f32_16x16x32_bf16 v[128:131], a[64:67], v[132:135], v[128:131]\n"
      "s_or_b32 s95, s45, s46\n"
      "v_mul_f32 v148, v156, v148\n"
      "v_mul_f32 v169, v158, v169\n"
      "v_mfma_f32_16x16x32_bf16 v[128:131], a[68:71], v[190:193], v[128:131]\n"
      "v_mul_f32_e64 v187, v158, v187\n"
      "v_fma_f32 v76, v206, v206, s51\n"
      "v_mfma_f32_16x16x32_bf16 v[202:205], a[84:87], v[144:147], v[202:205]\n"
      "v_cvt_pk_bf16_f32 v176, v214, v215\n"
      "v_mul_f32_e64 v118, v157, v118\n"
      "v_mfma_f32_16x16x32_bf16 v[202:205], a[88:91], v[132:135], v[202:205]\n"
      "buffer_load_dwordx4 v15, s[0:3], s5 offen  lds\n"
      "v_mfma_f32_16x16x32_bf16 v[202:205], a[92:95], v[190:193], v[202:205]\n"
      "v_mul_f32_e64 v212, v157, v212\n"
      "v_mov_b32_dpp v160, v160 quad_perm:[0,0,0,0] row_mask:0xf bank_mask:0xf\n"
      "v_mfma_f32_16x16x32_bf16 v[100:103], a[108:111], v[144:147], v[100:103]\n"
      "v_cvt_pk_bf16_f32 v118, v118, v119\n"
      "v_cvt_pk_bf16_f32 v177, v216, v217\n"
      "v_mfma_f32_16x16x32_bf16 v[100:103], a[112:115], v[132:135], v[100:103]\n"
      "ds_write_b128 v5, v[174:177] offset:18432\n"
      "v_mfma_f32_16x16x32_bf16 v[100:103], a[116:119], v[190:193], v[100:103]\n"
      "ds_read_b128 v[174:177], v1 offset:20480\n"
      "v_fmac_f32_e64 v76, v207, v207\n"
      "v_mfma_f32_16x16x32_bf16 v[162:165], a[132:135], v[144:147], v[162:165]\n"
      "v_fmac_f32 v76, v208, v208\n"
      "v_fma_f32 v153, v202, v202, s51\n"
      "v_mfma_f32_16x16x32_bf16 v[162:165], a[136:139], v[132:135], v[162:165]\n"
      "v_fmac_f32 v76, v209, v209\n"
      "v_cvt_pk_bf16_f32 v148, v148, v149\n"
      "v_mfma_f32_16x16x32_bf16 v[162:165], a[140:143], v[190:193], v[162:165]\n"
      "buffer_load_dwordx4 v[136:139], v36, s[28:31], s86 offen\n"
      "v_mfma_f32_16x16x32_bf16 v[194:197], v[140:143], a[144:147], 0\n"
      "v_cvt_pk_bf16_f32 v210, v210, v211\n"
      "v_mul_f32 v151, v156, v151\n"
      "v_mfma_f32_16x16x32_bf16 v[194:197], v[108:111], a[148:151], v[194:197]\n"
      "v_cvt_pk_bf16_f32 v211, v212, v213\n"
      "v_fmac_f32 v76, v198, v198\n"
      "v_mfma_f32_16x16x32_bf16 v[194:197], v[114:117], a[152:155], v[194:197]\n"
      "ds_read_b128 v[216:219], v0 offset:22528\n"
      "v_mul_f32 v166, v158, v166\n"
      "v_mfma_f32_16x16x32_bf16 v[194:197], v[144:147], a[156:159], v[194:197]\n"
      "v_mul_f32 v124, v157, v124\n"
      "v_mul_f32 v150, v156, v150\n"
      "v_mfma_f32_16x16x32_bf16 v[194:197], v[132:135], a[160:163], v[194:197]\n"
      "v_mul_f32 v189, v158, v189\n"
      "v_fmac_f32 v76, v199, v199\n"
      "v_mfma_f32_16x16x32_bf16 v[194:197], v[190:193], a[164:167], v[194:197]\n"
      "v_fmac_f32 v153, v203, v203\n"
      "v_fmac_f32 v76, v200, v200\n"
      "v_mfma_f32_16x16x32_bf16 v[96:99], v[140:143], a[168:171], 0\n"
      "v_fmac_f32 v153, v204, v204\n"
      "v_cvt_pk_bf16_f32 v186, v186, v187\n"
      "v_mfma_f32_16x16x32_bf16 v[96:99], v[108:111], a[172:175], v[96:99]\n"
      "v_cvt_pk_bf16_f32 v149, v150, v151\n"
      "v_cvt_pk_bf16_f32 v213, v124, v125\n"
      "v_mfma_f32_16x16x32_bf16 v[96:99], v[114:117], a[176:179], v[96:99]\n"
      "v_cvt_pk_bf16_f32 v119, v120, v121\n"
      "v_fmac_f32 v153, v205, v205\n"
      "v_mfma_f32_16x16x32_bf16 v[96:99], v[144:147], a[180:183], v[96:99]\n"
      "ds_read_b128 v[154:157], v1 offset:26624\n"
      "v_fmac_f32 v76, v201, v201\n"
      "v_mfma_f32_16x16x32_bf16 v[96:99], v[132:135], a[184:187], v[96:99]\n"
      "ds_read_b128 v[124:127], v0 offset:20480\n"
      "v_fmac_f32 v153, v100, v100\n"
      "v_mfma_f32_16x16x32_bf16 v[96:99], v[190:193], a[188:191], v[96:99]\n"
      "v_fmac_f32 v76, v128, v128\n"
      "v_cvt_pk_bf16_f32 v194, v194, v195\n"
      "v_mfma_f32_16x16x32_bf16 v[104:107], v[140:143], a[192:195], 0\n"
      "v_cvt_pk_bf16_f32 v187, v188, v189\n"
      "v_fmac_f32_e64 v153, v101, v101\n"
      "v_mfma_f32_16x16x32_bf16 v[104:107], v[108:111], a[196:199], v[104:107]\n"
      "ds_read_b128 v[108:111], v1 offset:32768\n"
      "v_cndmask_b32 v112, v112, v152, s[60:61]\n"
      "v_mfma_f32_16x16x32_bf16 v[104:107], v[114:117], a[200:203], v[104:107]\n"
      "v_rcp_f32_e64 v160, v160\n"
      "v_mfma_f32_16x16x32_bf16 v[104:107], v[144:147], a[204:207], v[104:107]\n"
      "ds_write_b64 v6, v[148:149] offset:18432\n"
      "v_mfma_f32_16x16x32_bf16 v[104:107], v[132:135], a[208:211], v[104:107]\n"
      "s_waitcnt lgkmcnt(2)\n"
      "v_fmac_f32 v153, v102, v102\n"
      "v_cvt_pk_bf16_f32 v220, v220, v221\n"
      "v_mfma_f32_16x16x32_bf16 v[104:107], v[190:193], a[212:215], v[104:107]\n"
      "buffer_load_dword v93, v29, s[28:31], s86 offen\n"
      "v_mfma_f32_16x16x32_bf16 v[190:193], a[0:3], v[124:127], 0\n"
      "v_fmac_f32 v153, v103, v103\n"
      "v_bitop3_b32 v178, v178, v181, s33 bitop3:0xa8\n"
      "v_mfma_f32_16x16x32_bf16 v[190:193], a[4:7], v[174:177], v[190:193]\n"
      "v_mul_f32 v112, v160, v112\n"
      "v_fmac_f32_e64 v153, v162, v162\n"
      "v_mfma_f32_16x16x32_bf16 v[190:193], a[8:11], v[182:185], v[190:193]\n"
      "v_fmac_f32_e64 v153, v163, v163\n"
      "v_cvt_pk_bf16_f32 v212, v122, v123\n"
      "v_mfma_f32_16x16x32_bf16 v[190:193], a[12:15], v[154:157], v[190:193]\n"
      "v_cvt_pk_bf16_f32 v104, v104, v105\n"
      "v_readlane_b32 s47, v38, s43\n"
      "v_mfma_f32_16x16x32_bf16 v[190:193], a[16:19], v[170:173], v[190:193]\n"
      "s_waitcnt lgkmcnt(0)\n"
      "v_fmac_f32 v76, v129, v129\n"
      "v_cvt_pk_bf16_f32 v105, v106, v107\n"
      "v_mfma_f32_16x16x32_bf16 v[190:193], a[20:23], v[108:111], v[190:193]\n"
      "v_fmac_f32_e64 v76, v130, v130\n"
      "v_cvt_pk_bf16_f32 v96, v96, v97\n"
      "v_mfma_f32_16x16x32_bf16 v[132:135], a[24:27], v[124:127], 0\n"
      "ds_read_b128 v[144:147], v1 offset:22528\n"
      "v_mul_f32_e64 v222, v158, v222\n"
      "v_mfma_f32_16x16x32_bf16 v[132:135], a[28:31], v[174:177], v[132:135]\n"
      "ds_read_b128 v[120:123], v1 offset:34816\n"
      "v_mul_f32_e64 v223, v158, v223\n"
      "v_mfma_f32_16x16x32_bf16 v[132:135], a[32:35], v[182:185], v[132:135]\n"
      "ds_write_b128 v5, v[210:213] offset:24576\n"
      "v_mfma_f32_16x16x32_bf16 v[132:135], a[36:39], v[154:157], v[132:135]\n"
      "ds_read_b128 v[140:143], v0 offset:28672\n"
      "v_fma_f32 v82, v190, v190, s51\n"
      "v_mfma_f32_16x16x32_bf16 v[132:135], a[40:43], v[170:173], v[132:135]\n"
      "v_fmac_f32 v153, v164, v164\n"
      "v_fmac_f32 v82, v191, v191\n"
      "v_mfma_f32_16x16x32_bf16 v[132:135], a[44:47], v[108:111], v[132:135]\n"
      "v_fmac_f32 v153, v165, v165\n"
      "v_fmac_f32 v76, v131, v131\n"
      "v_mfma_f32_16x16x32_bf16 v[158:161], a[48:51], v[124:127], 0\n"
      "v_fmac_f32 v82, v192, v192\n"
      "v_cvt_pk_bf16_f32 v112, v112, v112\n"
      "v_mfma_f32_16x16x32_bf16 v[158:161], a[52:55], v[174:177], v[158:161]\n"
      "v_permlane16_swap_b32 v76, v153\n"
      "v_mfma_f32_16x16x32_bf16 v[158:161], a[56:59], v[182:185], v[158:161]\n"
      "ds_write_b16 v12, v112\n"
      "v_mfma_f32_16x16x32_bf16 v[158:161], a[60:63], v[154:157], v[158:161]\n"
      "v_add_f32_e64 v76, v76, v153\n"
      "v_cvt_pk_bf16_f32 v195, v196, v197\n"
      "v_mfma_f32_16x16x32_bf16 v[158:161], a[64:67], v[170:173], v[158:161]\n"
      "ds_read_u16 v86, v11 offset:1232\n"
      "v_cvt_pk_bf16_f32 v221, v222, v223\n"
      "v_mfma_f32_16x16x32_bf16 v[158:161], a[68:71], v[108:111], v[158:161]\n"
      "ds_write_b64 v6, v[118:119] offset:24576\n"
      "v_mfma_f32_16x16x32_bf16 v[112:115], a[72:75], v[124:127], 0\n"
      "v_mov_b32 v153, v76\n"
      "v_fmac_f32 v82, v193, v193\n"
      "v_mfma_f32_16x16x32_bf16 v[112:115], a[76:79], v[174:177], v[112:115]\n"
      "v_permlane32_swap_b32 v76, v153\n"
      "v_mfma_f32_16x16x32_bf16 v[112:115], a[80:83], v[182:185], v[112:115]\n"
      "s_or_b32 s95, s95, s48\n"
      "v_fmac_f32_e64 v82, v132, v132\n"
      "v_cvt_pk_bf16_f32 v97, v98, v99\n"
      "v_mfma_f32_16x16x32_bf16 v[112:115], a[84:87], v[154:157], v[112:115]\n"
      "v_add_f32 v76, v76, v153\n"
      "v_fmac_f32 v82, v133, v133\n"
      "v_mfma_f32_16x16x32_bf16 v[112:115], a[88:91], v[170:173], v[112:115]\n"
      "v_rsq_f32_e64 v76, v76\n"
      "v_mfma_f32_16x16x32_bf16 v[112:115], a[92:95], v[108:111], v[112:115]\n"
      "s_cmp_ge_u32 s95, 0x40000000\n"
      "s_cselect_b32 s94, -1, -2\n"
      "v_mov_b32 v153, v76\n"
      "v_mfma_f32_16x16x32_bf16 v[148:151], a[96:99], v[124:127], 0\n"
      "s_nop 0\n"
      "v_permlane16_swap_b32 v76, v153\n"
      "v_mfma_f32_16x16x32_bf16 v[148:151], a[100:103], v[174:177], v[148:151]\n"
      "v_fmac_f32 v82, v134, v134\n"
      "v_fma_f32 v77, v112, v112, s51\n"
      "v_mfma_f32_16x16x32_bf16 v[148:151], a[104:107], v[182:185], v[148:151]\n"
      "v_fmac_f32 v77, v113, v113\n"
      "v_cvt_pk_bf16_f32 v223, v168, v169\n"
      "v_mfma_f32_16x16x32_bf16 v[148:151], a[108:111], v[154:157], v[148:151]\n"
      "v_mul_f32 v101, v153, v101\n"
      "v_mul_f32_e64 v205, v153, v205\n"
      "v_mfma_f32_16x16x32_bf16 v[148:151], a[112:115], v[170:173], v[148:151]\n"
      "v_cvt_pk_bf16_f32 v222, v166, v167\n"
      "v_mul_f32 v76, s50, v76\n"
      "v_mfma_f32_16x16x32_bf16 v[148:151], a[116:119], v[108:111], v[148:151]\n"
      "v_mul_f32 v129, v76, v129\n"
      "v_fmac_f32 v82, v135, v135\n"
      "v_mfma_f32_16x16x32_bf16 v[116:119], a[120:123], v[124:127], 0\n"
      "v_mul_f32 v207, v76, v207\n"
      "v_fmac_f32 v77, v114, v114\n"
      "v_mfma_f32_16x16x32_bf16 v[116:119], a[124:127], v[174:177], v[116:119]\n"
      "v_fmac_f32 v77, v115, v115\n"
      "v_mul_f32_e64 v128, v76, v128\n"
      "v_mfma_f32_16x16x32_bf16 v[116:119], a[128:131], v[182:185], v[116:119]\n"
      "ds_write_b128 v5, v[220:223] offset:30720\n"
      "v_pk_mul_f32 v[202:203], v[202:203], v[152:153] op_sel:[0,1]\n"
      "v_mfma_f32_16x16x32_bf16 v[116:119], a[132:135], v[154:157], v[116:119]\n"
      "v_fmac_f32 v77, v148, v148\n"
      "v_mul_f32 v162, v153, v162\n"
      "v_mfma_f32_16x16x32_bf16 v[116:119], a[136:139], v[170:173], v[116:119]\n"
      "v_fmac_f32 v77, v149, v149\n"
      "v_fmac_f32 v82, v158, v158\n"
      "v_mfma_f32_16x16x32_bf16 v[116:119], a[140:143], v[108:111], v[116:119]\n"
      "ds_write_b64 v6, v[186:187] offset:30720\n"
      "v_mfma_f32_16x16x32_bf16 v[212:215], v[124:127], a[144:147], 0\n"
      "v_mul_f32 v204, v153, v204\n"
      "v_mul_f32 v131, v76, v131\n"
      "v_mfma_f32_16x16x32_bf16 v[212:215], v[174:177], a[148:151], v[212:215]\n"
      "v_fmac_f32 v77, v150, v150\n"
      "v_mul_f32 v200, v76, v200\n"
      "v_mfma_f32_16x16x32_bf16 v[212:215], v[182:185], a[152:155], v[212:215]\n"
      "v_mul_f32 v165, v153, v165\n"
      "v_mul_f32 v100, v153, v100\n"
      "v_mfma_f32_16x16x32_bf16 v[212:215], v[154:157], a[156:159], v[212:215]\n"
      "v_fmac_f32 v82, v159, v159\n"
      "v_fmac_f32 v77, v151, v151\n"
      "v_mfma_f32_16x16x32_bf16 v[212:215], v[170:173], a[160:163], v[212:215]\n"
      "v_mul_f32 v163, v153, v163\n"
      "v_mul_f32 v209, v76, v209\n"
      "v_mfma_f32_16x16x32_bf16 v[212:215], v[108:111], a[164:167], v[212:215]\n"
      "v_mul_f32 v130, v76, v130\n"
      "v_mul_f32 v206, v76, v206\n"
      "v_mfma_f32_16x16x32_bf16 v[186:189], v[124:127], a[168:171], 0\n"
      "v_fmac_f32_e64 v82, v160, v160\n"
      "v_cvt_pk_bf16_f32 v162, v162, v163\n"
      "v_mfma_f32_16x16x32_bf16 v[186:189], v[174:177], a[172:175], v[186:189]\n"
      "v_mul_f32 v103, v153, v103\n"
      "v_mul_f32 v164, v153, v164\n"
      "v_mfma_f32_16x16x32_bf16 v[186:189], v[182:185], a[176:179], v[186:189]\n"
      "v_cvt_pk_bf16_f32 v196, v212, v213\n"
      "v_fmac_f32 v82, v161, v161\n"
      "v_mfma_f32_16x16x32_bf16 v[186:189], v[154:157], a[180:183], v[186:189]\n"
      "v_fmac_f32 v77, v116, v116\n"
      "v_mul_f32_e64 v208, v76, v208\n"
      "v_mfma_f32_16x16x32_bf16 v[186:189], v[170:173], a[184:187], v[186:189]\n"
      "v_cvt_pk_bf16_f32 v202, v202, v203\n"
      "v_fmac_f32_e64 v77, v117, v117\n"
      "v_mfma_f32_16x16x32_bf16 v[186:189], v[108:111], a[188:191], v[186:189]\n"
      "ds_read_b128 v[210:213], v1 offset:28672\n"
      "v_fmac_f32 v77, v118, v118\n"
      "v_mfma_f32_16x16x32_bf16 v[220:223], v[124:127], a[192:195], 0\n"
      "v_fmac_f32 v77, v119, v119\n"
      "v_cvt_pk_bf16_f32 v197, v214, v215\n"
      "v_mfma_f32_16x16x32_bf16 v[220:223], v[174:177], a[196:199], v[220:223]\n"
      "v_permlane16_swap_b32 v82, v77\n"
      "v_mfma_f32_16x16x32_bf16 v[220:223], v[182:185], a[200:203], v[220:223]\n"
      "ds_read_b128 v[174:177], v0 offset:34816\n"
      "v_mul_f32 v102, v153, v102\n"
      "v_mfma_f32_16x16x32_bf16 v[220:223], v[154:157], a[204:207], v[220:223]\n"
      "v_add_f32 v82, v82, v77\n"
      "v_mov_b32 v77, v82\n"
      "v_mfma_f32_16x16x32_bf16 v[220:223], v[170:173], a[208:211], v[220:223]\n"
      "s_cmp_gt_u32 s42, s44\n"
      "s_cselect_b32 s95, s94, s95\n"
      "v_mul_f32 v199, v76, v199\n"
      "v_mfma_f32_16x16x32_bf16 v[220:223], v[108:111], a[212:215], v[220:223]\n"
      "v_permlane32_swap_b32 v82, v77\n"
      "v_mfma_f32_16x16x32_bf16 v[182:185], a[0:3], v[216:219], 0\n"
      "s_waitcnt lgkmcnt(1)\n"
      "v_add_f32 v82, v82, v77\n"
      "v_cvt_pk_bf16_f32 v128, v128, v129\n"
      "v_mfma_f32_16x16x32_bf16 v[182:185], a[4:7], v[144:147], v[182:185]\n"
      "v_rsq_f32 v82, v82\n"
      "v_mfma_f32_16x16x32_bf16 v[182:185], a[8:11], v[140:143], v[182:185]\n"
      "v_mov_b32 v77, v82\n"
      "v_cvt_pk_bf16_f32 v206, v206, v207\n"
      "v_mfma_f32_16x16x32_bf16 v[182:185], a[12:15], v[210:213], v[182:185]\n"
      "s_waitcnt lgkmcnt(0)\n"
      "v_permlane16_swap_b32 v82, v77\n"
      "v_mfma_f32_16x16x32_bf16 v[182:185], a[16:19], v[174:177], v[182:185]\n"
      "v_mul_f32_e64 v117, v77, v117\n"
      "v_cvt_pk_bf16_f32 v129, v130, v131\n"
      "v_mfma_f32_16x16x32_bf16 v[182:185], a[20:23], v[120:123], v[182:185]\n"
      "v_cvt_pk_bf16_f32 v203, v204, v205\n"
      "v_cvt_pk_bf16_f32 v163, v164, v165\n"
      "v_mfma_f32_16x16x32_bf16 v[152:155], a[24:27], v[216:219], 0\n"
      "v_mul_f32 v201, v76, v201\n"
      "v_mul_f32 v82, s50, v82\n"
      "v_mfma_f32_16x16x32_bf16 v[152:155], a[28:31], v[144:147], v[152:155]\n"
      "v_mul_f32 v112, v77, v112\n"
      "v_mul_f32 v190, v82, v190\n"
      "v_mfma_f32_16x16x32_bf16 v[152:155], a[32:35], v[140:143], v[152:155]\n"
      "v_mul_f32 v159, v82, v159\n"
      "v_mul_f32 v113, v77, v113\n"
      "v_mfma_f32_16x16x32_bf16 v[152:155], a[36:39], v[210:213], v[152:155]\n"
      "v_fma_f32 v181, v182, v182, s51\n"
      "v_mul_f32 v116, v77, v116\n"
      "v_mfma_f32_16x16x32_bf16 v[152:155], a[40:43], v[174:177], v[152:155]\n"
      "v_mul_f32 v191, v82, v191\n"
      "v_mul_f32_e64 v133, v82, v133\n"
      "v_mfma_f32_16x16x32_bf16 v[152:155], a[44:47], v[120:123], v[152:155]\n"
      "v_mul_f32 v193, v82, v193\n"
      "v_fmac_f32 v181, v183, v183\n"
      "v_mfma_f32_16x16x32_bf16 v[166:169], a[48:51], v[216:219], 0\n"
      "v_cvt_pk_bf16_f32 v207, v208, v209\n"
      "v_fmac_f32 v181, v184, v184\n"
      "v_mfma_f32_16x16x32_bf16 v[166:169], a[52:55], v[144:147], v[166:169]\n"
      "v_fmac_f32 v181, v185, v185\n"
      "v_mul_f32 v158, v82, v158\n"
      "v_mfma_f32_16x16x32_bf16 v[166:169], a[56:59], v[140:143], v[166:169]\n"
      "v_mul_f32 v198, v76, v198\n"
      "v_mul_f32 v192, v82, v192\n"
      "v_mfma_f32_16x16x32_bf16 v[166:169], a[60:63], v[210:213], v[166:169]\n"
      "v_cvt_pk_bf16_f32 v106, v220, v221\n"
      "v_mul_f32 v160, v82, v160\n"
      "v_mfma_f32_16x16x32_bf16 v[166:169], a[64:67], v[174:177], v[166:169]\n"
      "v_cvt_pk_bf16_f32 v158, v158, v159\n"
      "v_cvt_pk_bf16_f32 v190, v190, v191\n"
      "v_mfma_f32_16x16x32_bf16 v[166:169], a[68:71], v[120:123], v[166:169]\n"
      "v_mul_f32 v134, v82, v134\n"
      "v_mul_f32 v148, v77, v148\n"
      "v_mfma_f32_16x16x32_bf16 v[170:173], a[72:75], v[216:219], 0\n"
      "v_cvt_pk_bf16_f32 v112, v112, v113\n"
      "v_cvt_pk_bf16_f32 v204, v100, v101\n"
      "v_mfma_f32_16x16x32_bf16 v[170:173], a[76:79], v[144:147], v[170:173]\n"
      "v_fmac_f32 v181, v152, v152\n"
      "v_fmac_f32 v181, v153, v153\n"
      "v_mfma_f32_16x16x32_bf16 v[170:173], a[80:83], v[140:143], v[170:173]\n"
      "v_cvt_pk_bf16_f32 v205, v102, v103\n"
      "v_cmp_ne_u32 s[56:57], 0, v178\n"
      "v_mfma_f32_16x16x32_bf16 v[170:173], a[84:87], v[210:213], v[170:173]\n"
      "v_mul_f32 v114, v77, v114\n"
      "v_mul_f32 v151, v77, v151\n"
      "v_mfma_f32_16x16x32_bf16 v[170:173], a[88:91], v[174:177], v[170:173]\n"
      "v_mul_f32_e64 v118, v77, v118\n"
      "v_cvt_pk_bf16_f32 v191, v192, v193\n"
      "v_mfma_f32_16x16x32_bf16 v[170:173], a[92:95], v[120:123], v[170:173]\n"
      "v_mul_f32 v132, v82, v132\n"
      "v_fmac_f32 v181, v154, v154\n"
      "v_mfma_f32_16x16x32_bf16 v[124:127], a[96:99], v[216:219], 0\n"
      "v_cvt_pk_bf16_f32 v208, v198, v199\n"
      "v_cvt_pk_bf16_f32 v99, v188, v189\n"
      "v_mfma_f32_16x16x32_bf16 v[124:127], a[100:103], v[144:147], v[124:127]\n"
      "v_fmac_f32 v181, v155, v155\n"
      "v_mul_f32 v115, v77, v115\n"
      "v_mfma_f32_16x16x32_bf16 v[124:127], a[104:107], v[140:143], v[124:127]\n"
      "s_and_b32 s47, s47, -4\n"
      "v_fmac_f32 v181, v166, v166\n"
      "v_fma_f32 v102, v170, v170, s51\n"
      "v_mfma_f32_16x16x32_bf16 v[124:127], a[108:111], v[210:213], v[124:127]\n"
      "v_mul_f32 v150, v77, v150\n"
      "v_cvt_pk_bf16_f32 v116, v116, v117\n"
      "v_mfma_f32_16x16x32_bf16 v[124:127], a[112:115], v[174:177], v[124:127]\n"
      "v_mul_f32 v149, v77, v149\n"
      "v_fmac_f32_e64 v102, v171, v171\n"
      "v_mfma_f32_16x16x32_bf16 v[124:127], a[116:119], v[120:123], v[124:127]\n"
      "v_fmac_f32 v102, v172, v172\n"
      "v_cvt_pk_bf16_f32 v113, v114, v115\n"
      "v_mfma_f32_16x16x32_bf16 v[108:111], a[120:123], v[216:219], 0\n"
      "v_fmac_f32 v181, v167, v167\n"
      "v_cvt_pk_bf16_f32 v115, v150, v151\n"
      "v_mfma_f32_16x16x32_bf16 v[108:111], a[124:127], v[144:147], v[108:111]\n"
      "v_cvt_pk_bf16_f32 v114, v148, v149\n"
      "v_fmac_f32_e64 v181, v168, v168\n"
      "v_mfma_f32_16x16x32_bf16 v[108:111], a[128:131], v[140:143], v[108:111]\n"
      "v_cvt_pk_bf16_f32 v209, v200, v201\n"
      "v_fmac_f32_e64 v102, v173, v173\n"
      "v_mfma_f32_16x16x32_bf16 v[108:111], a[132:135], v[210:213], v[108:111]\n"
      "v_cvt_pk_bf16_f32 v98, v186, v187\n"
      "v_fmac_f32 v102, v124, v124\n"
      "v_mfma_f32_16x16x32_bf16 v[108:111], a[136:139], v[174:177], v[108:111]\n"
      "v_cvt_pk_bf16_f32 v107, v222, v223\n"
      "v_fmac_f32 v102, v125, v125\n"
      "v_mfma_f32_16x16x32_bf16 v[108:111], a[140:143], v[120:123], v[108:111]\n"
      "v_fmac_f32 v181, v169, v169\n"
      "v_mul_f32 v119, v77, v119\n"
      "v_mfma_f32_16x16x32_bf16 v[148:151], v[216:219], a[144:147], 0\n"
      "v_fmac_f32 v102, v126, v126\n"
      "v_fmac_f32 v102, v127, v127\n"
      "v_mfma_f32_16x16x32_bf16 v[148:151], v[144:147], a[148:151], v[148:151]\n"
      "v_mul_f32 v135, v82, v135\n"
      "v_mul_f32 v161, v82, v161\n"
      "v_mfma_f32_16x16x32_bf16 v[148:151], v[140:143], a[152:155], v[148:151]\n"
      "ds_read_b128 v[220:223], v2 offset:3072\n"
      "v_cvt_pk_bf16_f32 v193, v134, v135\n"
      "v_mfma_f32_16x16x32_bf16 v[148:151], v[210:213], a[156:159], v[148:151]\n"
      "v_fmac_f32 v102, v108, v108\n"
      "v_fmac_f32 v102, v109, v109\n"
      "v_mfma_f32_16x16x32_bf16 v[148:151], v[174:177], a[160:163], v[148:151]\n"
      "v_cvt_pk_bf16_f32 v192, v132, v133\n"
      "v_fmac_f32 v102, v110, v110\n"
      "v_mfma_f32_16x16x32_bf16 v[148:151], v[120:123], a[164:167], v[148:151]\n"
      "v_cvt_pk_bf16_f32 v117, v118, v119\n"
      "v_fmac_f32 v102, v111, v111\n"
      "v_mfma_f32_16x16x32_bf16 v[186:189], v[216:219], a[168:171], 0\n"
      "s_nop 0\n"
      "v_permlane16_swap_b32 v181, v102\n"
      "v_mfma_f32_16x16x32_bf16 v[186:189], v[144:147], a[172:175], v[186:189]\n"
      "v_add_f32 v181, v181, v102\n"
      "v_mov_b32 v102, v181\n"
      "v_cvt_pk_bf16_f32 v76, v148, v149\n"
      "v_mfma_f32_16x16x32_bf16 v[186:189], v[140:143], a[176:179], v[186:189]\n"
      "v_permlane32_swap_b32_e64 v181, v102\n"
      "v_mfma_f32_16x16x32_bf16 v[186:189], v[210:213], a[180:183], v[186:189]\n"
      "ds_read_b128 v[198:201], v2\n"
      "v_add_f32 v181, v181, v102\n"
      "v_mfma_f32_16x16x32_bf16 v[186:189], v[174:177], a[184:187], v[186:189]\n"
      "v_rsq_f32 v181, v181\n"
      "v_mfma_f32_16x16x32_bf16 v[186:189], v[120:123], a[188:191], v[186:189]\n"
      "ds_read_u16 v82, v11 offset:1200\n"
      "v_mov_b32 v102, v181\n"
      "v_mfma_f32_16x16x32_bf16 v[132:135], v[216:219], a[192:195], 0\n"
      "s_nop 0\n"
      "v_permlane16_swap_b32_e64 v181, v102\n"
      "v_mfma_f32_16x16x32_bf16 v[132:135], v[144:147], a[196:199], v[132:135]\n"
      "ds_read_b128 v[144:147], v2 offset:6144\n"
      "v_mul_f32 v125, v102, v125\n"
      "v_mfma_f32_16x16x32_bf16 v[132:135], v[140:143], a[200:203], v[132:135]\n"
      "v_mul_f32 v170, v102, v170\n"
      "v_mul_f32 v173, v102, v173\n"
      "v_mfma_f32_16x16x32_bf16 v[132:135], v[210:213], a[204:207], v[132:135]\n"
      "ds_read_b128 v[210:213], v2 offset:9216\n"
      "v_mul_f32 v172, v102, v172\n"
      "v_mfma_f32_16x16x32_bf16 v[132:135], v[174:177], a[208:211], v[132:135]\n"
      "s_waitcnt lgkmcnt(3)\n"
      "v_cvt_pk_bf16_f32 v77, v150, v151\n"
      "v_mul_f32 v171, v102, v171\n"
      "v_mfma_f32_16x16x32_bf16 v[132:135], v[120:123], a[212:215], v[132:135]\n"
      "ds_read_u16 v78, v11 offset:1168\n"
      "v_mul_f32 v127, v102, v127\n"
      "v_mfma_f32_16x16x32_bf16 v[174:177], a[216:219], v[198:201], 0\n"
      "ds_read_b64 v[130:131], v10 offset:1192\n"
      "v_mul_f32 v111, v102, v111\n"
      "v_mfma_f32_16x16x32_bf16 v[174:177], a[220:223], v[220:223], v[174:177]\n"
      "s_waitcnt lgkmcnt(3)\n"
      "v_mul_f32 v108, v102, v108\n"
      "v_cvt_pk_bf16_f32 v159, v160, v161\n"
      "v_mfma_f32_16x16x32_bf16 v[174:177], a[224:227], v[144:147], v[174:177]\n"
      "ds_read_b128 v[140:143], v10 offset:1200\n"
      "v_cvt_pk_bf16_f32 v85, v134, v135\n"
      "v_mfma_f32_16x16x32_bf16 v[214:217], a[240:243], v[198:201], 0\n"
      "v_mul_f32 v126, v102, v126\n"
      "v_mul_f32 v109, v102, v109\n"
      "v_mfma_f32_16x16x32_bf16 v[214:217], a[244:247], v[220:223], v[214:217]\n"
      "v_cvt_pk_bf16_f32 v108, v108, v109\n"
      "v_cvt_pk_bf16_f32 v170, v170, v171\n"
      "v_mfma_f32_16x16x32_bf16 v[214:217], a[248:251], v[144:147], v[214:217]\n"
      "v_mul_f32 v124, v102, v124\n"
      "v_mul_f32 v181, s50, v181\n"
      "v_mfma_f32_16x16x32_bf16 v[148:151], v[232:235], v[198:201], 0\n"
      "v_mul_f32_e64 v154, v181, v154\n"
      "v_cvt_pk_bf16_f32 v171, v172, v173\n"
      "v_mfma_f32_16x16x32_bf16 v[148:151], v[236:239], v[220:223], v[148:151]\n"
      "v_cvt_pk_bf16_f32 v80, v186, v187\n"
      "v_mul_f32_e64 v110, v102, v110\n"
      "v_mfma_f32_16x16x32_bf16 v[148:151], v[240:243], v[144:147], v[148:151]\n"
      "v_cvt_pk_bf16_f32 v172, v124, v125\n"
      "v_cvt_pk_bf16_f32 v173, v126, v127\n"
      "v_mfma_f32_16x16x32_bf16 v[220:223], v[202:205], v[206:209], v[40:43]\n"
      "ds_read_b128 v[122:125], v2 offset:12288\n"
      "v_mul_f32_e64 v168, v181, v168\n"
      "v_mfma_f32_16x16x32_bf16 v[100:103], v[112:115], v[206:209], v[44:47]\n"
      "s_waitcnt lgkmcnt(1)\n"
      "v_cvt_pk_bf16_f32 v109, v110, v111\n"
      "v_mul_f32 v166, v181, v166\n"
      "v_mfma_f32_16x16x32_bf16 v[118:121], v[170:173], v[206:209], v[48:51]\n"
      "ds_read_b128 v[198:201], v2 offset:15360\n"
      "v_cvt_pk_bf16_f32 v81, v188, v189\n"
      "v_mfma_f32_16x16x32_bf16 v[144:147], v[140:143], v[206:209], v[24:27]\n"
      "v_mul_f32 v182, v181, v182\n"
      "v_mul_f32 v184, v181, v184\n"
      "v_mfma_f32_16x16x16_bf16 v[220:223], v[162:163], v[128:129], v[220:223]\n"
      "v_mul_f32 v167, v181, v167\n"
      "v_mul_f32 v155, v181, v155\n"
      "v_cvt_pk_bf16_f32 v84, v132, v133\n"
      "v_mfma_f32_16x16x16_bf16 v[100:103], v[116:117], v[128:129], v[100:103]\n"
      "v_mul_f32 v183, v181, v183\n"
      "v_cvt_pk_bf16_f32 v182, v182, v183\n"
      "v_mul_f32 v185, v181, v185\n"
      "v_mfma_f32_16x16x16_bf16 v[118:121], v[108:109], v[128:129], v[118:121]\n"
      "v_cvt_pk_bf16_f32 v183, v184, v185\n"
      "v_cvt_pk_bf16_f32 v166, v166, v167\n"
      "v_max3_f32 v206, v220, v221, v222\n"
      "v_mfma_f32_16x16x16_bf16 v[144:147], v[130:131], v[128:129], v[144:147]\n"
      "v_max3_f32 v132, v223, v100, v101\n"
      "v_mul_f32 v169, v181, v169\n"
      "v_cvt_pk_bf16_f32 v185, v154, v155\n"
      "v_mfma_f32_16x16x32_bf16 v[174:177], a[228:231], v[210:213], v[174:177]\n"
      "s_waitcnt lgkmcnt(0)\n"
      "v_max3_f32 v132, v132, v118, v119\n"
      "v_max3_f32 v206, v206, v102, v103\n"
      "v_mfma_f32_16x16x32_bf16 v[174:177], a[232:235], v[122:125], v[174:177]\n"
      "v_cvt_pk_bf16_f32 v167, v168, v169\n"
      "v_max3_f32 v206, v206, v120, v121\n"
      "v_mfma_f32_16x16x32_bf16 v[174:177], a[236:239], v[198:201], v[174:177]\n"
      "ds_read_b128 v[154:157], v2 offset:256\n"
      "v_max3_f32 v206, v206, v132, v144\n"
      "v_mfma_f32_16x16x32_bf16 v[214:217], a[252:255], v[210:213], v[214:217]\n"
      "ds_read_b128 v[132:135], v2 offset:6400\n"
      "v_mov_b32 v209, v206\n"
      "v_mfma_f32_16x16x32_bf16 v[214:217], v[224:227], v[122:125], v[214:217]\n"
      "s_nop 0\n"
      "v_permlane16_swap_b32_e64 v206, v209\n"
      "v_mfma_f32_16x16x32_bf16 v[214:217], v[228:231], v[198:201], v[214:217]\n"
      "v_max_f32 v206, v206, v209\n"
      "v_mov_b32 v209, v206\n"
      "v_mfma_f32_16x16x32_bf16 v[148:151], v[244:247], v[210:213], v[148:151]\n"
      "ds_read_b128 v[126:129], v2 offset:3328\n"
      "v_permlane32_swap_b32 v206, v209\n"
      "v_max_f32 v206, v206, v209\n"
      "v_sub_f32_e64 v144, v144, v206\n"
      "v_pk_add_f32 v[102:103], v[102:103], v[206:207] op_sel_hi:[1,0] neg_lo:[0,1] neg_hi:[0,1]\n"
      "v_pk_add_f32 v[220:221], v[220:221], v[206:207] op_sel_hi:[1,0] neg_lo:[0,1] neg_hi:[0,1]\n"
      "v_pk_add_f32 v[100:101], v[100:101], v[206:207] op_sel_hi:[1,0] neg_lo:[0,1] neg_hi:[0,1]\n"
      "v_pk_add_f32 v[120:121], v[120:121], v[206:207] op_sel_hi:[1,0] neg_lo:[0,1] neg_hi:[0,1]\n"
      "v_pk_mul_f32 v[152:153], v[152:153], v[180:181] op_sel:[0,1]\n"
      "v_pk_add_f32 v[118:119], v[118:119], v[206:207] op_sel_hi:[1,0] neg_lo:[0,1] neg_hi:[0,1]\n"
      "v_pk_add_f32 v[222:223], v[222:223], v[206:207] op_sel_hi:[1,0] neg_lo:[0,1] neg_hi:[0,1]\n"
      "v_mfma_f32_16x16x32_bf16 v[148:151], v[248:251], v[122:125], v[148:151]\n"
      "v_exp_f32 v118, v118\n"
      "v_mfma_f32_16x16x32_bf16 v[148:151], v[252:255], v[198:201], v[148:151]\n"
      "v_exp_f32 v120, v120\n"
      "v_mfma_f32_16x16x32_bf16 v[198:201], v[202:205], v[190:193], v[52:55]\n"
      "v_exp_f32 v222, v222\n"
      "v_mfma_f32_16x16x32_bf16 v[206:209], v[112:115], v[190:193], v[56:59]\n"
      "v_exp_f32 v119, v119\n"
      "v_mfma_f32_16x16x32_bf16 v[178:181], v[170:173], v[190:193], v[60:63]\n"
      "v_exp_f32 v121, v121\n"
      "v_mfma_f32_16x16x32_bf16 v[186:189], v[140:143], v[190:193], v[26:29]\n"
      "s_waitcnt lgkmcnt(0)\n"
      "v_exp_f32 v102, v102\n"
      "v_mfma_f32_16x16x16_bf16 v[198:201], v[162:163], v[158:159], v[198:201]\n"
      "v_exp_f32 v103, v103\n"
      "v_cvt_pk_bf16_f32 v184, v152, v153\n"
      "v_mfma_f32_16x16x16_bf16 v[206:209], v[116:117], v[158:159], v[206:209]\n"
      "v_exp_f32_e64 v223, v223\n"
      "v_cvt_pk_bf16_f32 v88, v118, v119\n"
      "v_mfma_f32_16x16x16_bf16 v[178:181], v[108:109], v[158:159], v[178:181]\n"
      "v_exp_f32 v221, v221\n"
      "v_exp_f32 v220, v220\n"
      "v_mfma_f32_16x16x16_bf16 v[186:189], v[130:131], v[158:159], v[186:189]\n"
      "v_cvt_pk_bf16_f32 v220, v220, v221\n"
      "v_exp_f32_e64 v100, v100\n"
      "v_mfma_f32_16x16x32_bf16 v[210:213], a[216:219], v[154:157], 0\n"
      "v_max3_f32 v169, v198, v199, v200\n"
      "v_max3_f32 v118, v201, v206, v207\n"
      "v_mfma_f32_16x16x32_bf16 v[210:213], a[220:223], v[126:129], v[210:213]\n"
      "v_max3_f32 v118, v118, v178, v179\n"
      "v_max3_f32 v169, v169, v208, v209\n"
      "v_mfma_f32_16x16x32_bf16 v[210:213], a[224:227], v[132:135], v[210:213]\n"
      "v_exp_f32_e64 v144, v144\n"
      "v_mfma_f32_16x16x32_bf16 v[122:125], a[240:243], v[154:157], 0\n"
      "v_cvt_pk_bf16_f32 v89, v120, v121\n"
      "v_max3_f32 v169, v169, v180, v181\n"
      "v_mfma_f32_16x16x32_bf16 v[122:125], a[244:247], v[126:129], v[122:125]\n"
      "v_cvt_pk_bf16_f32 v221, v222, v223\n"
      "v_cvt_pk_bf16_f32 v223, v102, v103\n"
      "v_mfma_f32_16x16x32_bf16 v[122:125], a[248:251], v[132:135], v[122:125]\n"
      "v_cvt_pk_bf16_f32 v90, v144, 0\n"
      "v_max3_f32 v169, v169, v118, v186\n"
      "v_mfma_f32_16x16x32_bf16 v[118:121], v[232:235], v[154:157], 0\n"
      "v_exp_f32_e64 v101, v101\n"
      "v_mfma_f32_16x16x32_bf16 v[118:121], v[236:239], v[126:129], v[118:121]\n"
      "v_mov_b32 v156, v169\n"
      "v_cvt_pk_bf16_f32 v222, v100, v101\n"
      "v_mfma_f32_16x16x32_bf16 v[118:121], v[240:243], v[132:135], v[118:121]\n"
      "v_permlane16_swap_b32 v169, v156\n"
      "v_mfma_f32_16x16x32_bf16 v[152:155], v[32:35], v[220:223], 0\n"
      "ds_read_b128 v[188:191], v2 offset:9472\n"
      "v_max_f32 v169, v169, v156\n"
      "v_mfma_f32_16x16x32_bf16 v[152:155], v[32:35], v[88:91], v[152:155]\n"
      "v_mov_b32 v156, v169\n"
      "s_waitcnt vmcnt(1)\n"
      "v_or3_b32 v136, v136, v137, v138\n"
      "v_mfma_f32_16x16x32_bf16 v[158:161], v[194:197], v[220:223], 0\n"
      "v_mul_f32 v193, v175, v175\n"
      "v_fma_f32 v192, v174, v174, s52\n"
      "v_mfma_f32_16x16x32_bf16 v[158:161], v[76:79], v[88:91], v[158:161]\n"
      "v_permlane32_swap_b32 v169, v156\n"
      "v_mfma_f32_16x16x32_bf16 v[144:147], v[96:99], v[220:223], 0\n"
      "v_max_f32 v169, v169, v156\n"
      "v_sub_f32 v186, v186, v169\n"
      "s_waitcnt vmcnt(0)\n"
      "v_or3_b32 v136, v136, v139, v93\n"
      "v_rcp_f32_e64 v152, v152\n"
      "v_pk_add_f32 v[180:181], v[180:181], v[168:169] op_sel:[0,1] neg_lo:[0,1] neg_hi:[0,1]\n"
      "v_pk_add_f32 v[198:199], v[198:199], v[168:169] op_sel:[0,1] neg_lo:[0,1] neg_hi:[0,1]\n"
      "v_pk_mul_f32 v[158:159], v[158:159], v[152:153] op_sel_hi:[1,0]\n"
      "v_pk_add_f32 v[206:207], v[206:207], v[168:169] op_sel:[0,1] neg_lo:[0,1] neg_hi:[0,1]\n"
      "v_pk_add_f32 v[200:201], v[200:201], v[168:169] op_sel:[0,1] neg_lo:[0,1] neg_hi:[0,1]\n"
      "v_pk_mul_f32 v[160:161], v[160:161], v[152:153] op_sel_hi:[1,0]\n"
      "v_exp_f32 v206, v206\n"
      "v_mfma_f32_16x16x32_bf16 v[144:147], v[80:83], v[88:91], v[144:147]\n"
      "v_exp_f32 v180, v180\n"
      "v_mfma_f32_16x16x32_bf16 v[154:157], v[104:107], v[220:223], 0\n"
      "v_exp_f32_e64 v181, v181\n"
      "v_mfma_f32_16x16x32_bf16 v[154:157], v[84:87], v[88:91], v[154:157]\n"
      "v_cvt_pk_bf16_f32 v158, v158, v159\n"
      "v_cvt_pk_bf16_f32 v93, v180, v181\n"
      "v_exp_f32_e64 v198, v198\n"
      "v_cvt_pk_bf16_f32 v159, v160, v161\n"
      "v_pk_fma_f32 v[192:193], v[176:177], v[176:177], v[192:193]\n"
      "v_pk_add_f32 v[178:179], v[178:179], v[168:169] op_sel:[0,1] neg_lo:[0,1] neg_hi:[0,1]\n"
      "v_pk_mul_f32 v[144:145], v[144:145], v[152:153] op_sel_hi:[1,0]\n"
      "v_pk_add_f32 v[208:209], v[208:209], v[168:169] op_sel:[0,1] neg_lo:[0,1] neg_hi:[0,1]\n"
      "v_pk_mul_f32 v[156:157], v[156:157], v[152:153] op_sel_hi:[1,0]\n"
      "v_pk_fma_f32 v[192:193], v[214:215], v[214:215], v[192:193]\n"
      "v_pk_mul_f32 v[146:147], v[146:147], v[152:153] op_sel_hi:[1,0]\n"
      "v_pk_mul_f32 v[154:155], v[154:155], v[152:153] op_sel_hi:[1,0]\n"
      "v_cvt_pk_bf16_f32 v160, v144, v145\n"
      "v_cvt_pk_bf16_f32 v161, v146, v147\n"
      "v_mfma_f32_16x16x32_bf16 v[132:135], v[202:205], v[182:185], v[64:67]\n"
      "v_exp_f32 v209, v209\n"
      "v_mfma_f32_16x16x32_bf16 v[126:129], v[112:115], v[182:185], v[68:71]\n"
      "s_waitcnt lgkmcnt(0)\n"
      "v_exp_f32_e64 v200, v200\n"
      "v_mfma_f32_16x16x32_bf16 v[144:147], v[170:173], v[182:185], v[72:75]\n"
      "ds_read_b128 v[220:223], v2 offset:12544\n"
      "v_cvt_pk_bf16_f32 v154, v154, v155\n"
      "v_mfma_f32_16x16x32_bf16 v[100:103], v[140:143], v[182:185], v[28:31]\n"
      "ds_read_b128 v[180:183], v2 offset:15616\n"
      "v_exp_f32 v201, v201\n"
      "v_mfma_f32_16x16x16_bf16 v[132:135], v[162:163], v[166:167], v[132:135]\n"
      "v_cvt_pk_bf16_f32 v155, v156, v157\n"
      "v_exp_f32 v199, v199\n"
      "v_mfma_f32_16x16x16_bf16 v[126:129], v[116:117], v[166:167], v[126:129]\n"
      "v_cvt_pk_bf16_f32 v198, v198, v199\n"
      "v_exp_f32 v207, v207\n"
      "v_mfma_f32_16x16x16_bf16 v[144:147], v[108:109], v[166:167], v[144:147]\n"
      "v_cvt_pk_bf16_f32 v199, v200, v201\n"
      "v_exp_f32 v186, v186\n"
      "v_mfma_f32_16x16x16_bf16 v[100:103], v[130:131], v[166:167], v[100:103]\n"
      "v_cvt_pk_bf16_f32 v200, v206, v207\n"
      "v_exp_f32 v208, v208\n"
      "v_mfma_f32_16x16x32_bf16 v[210:213], a[228:231], v[188:191], v[210:213]\n"
      "s_waitcnt lgkmcnt(1)\n"
      "v_exp_f32 v179, v179\n"
      "v_mfma_f32_16x16x32_bf16 v[210:213], a[232:235], v[220:223], v[210:213]\n"
      "s_waitcnt lgkmcnt(0)\n"
      "v_max3_f32 v184, v132, v133, v134\n"
      "v_cvt_pk_bf16_f32 v94, v186, 0\n"
      "v_mfma_f32_16x16x32_bf16 v[210:213], a[236:239], v[180:183], v[210:213]\n"
      "ds_write_b128 v3, v[158:161] offset:18432\n"
      "v_mfma_f32_16x16x32_bf16 v[122:125], a[252:255], v[188:191], v[122:125]\n"
      "v_cvt_pk_bf16_f32 v201, v208, v209\n"
      "v_max3_f32 v186, v135, v126, v127\n"
      "v_mfma_f32_16x16x32_bf16 v[122:125], v[224:227], v[220:223], v[122:125]\n"
      "ds_read_b64 v[102:103], v10 offset:1184\n"
      "v_max3_f32 v184, v184, v128, v129\n"
      "v_mfma_f32_16x16x32_bf16 v[122:125], v[228:231], v[180:183], v[122:125]\n"
      "v_max3_f32 v186, v186, v144, v145\n"
      "v_max3_f32 v184, v184, v146, v147\n"
      "v_mfma_f32_16x16x32_bf16 v[118:121], v[244:247], v[188:191], v[118:121]\n"
      "v_exp_f32_e64 v178, v178\n"
      "v_mfma_f32_16x16x32_bf16 v[118:121], v[248:251], v[220:223], v[118:121]\n"
      "ds_read_b128 v[220:223], v10 offset:1168\n"
      "v_max3_f32 v184, v184, v186, v100\n"
      "v_mfma_f32_16x16x32_bf16 v[118:121], v[252:255], v[180:183], v[118:121]\n"
      "ds_read_b128 v[186:189], v7\n"
      "v_cvt_pk_bf16_f32 v92, v178, v179\n"
      "v_mfma_f32_16x16x32_bf16 v[166:169], v[32:35], v[198:201], 0\n"
      "v_mul_f32 v183, v211, v211\n"
      "v_mov_b32 v209, v184\n"
      "v_mfma_f32_16x16x32_bf16 v[166:169], v[32:35], v[92:95], v[166:169]\n"
      "s_and_b32 s4, s45, s8\n"
      "v_permlane16_swap_b32 v184, v209\n"
      "v_mfma_f32_16x16x32_bf16 v[178:181], v[194:197], v[198:201], 0\n"
      "s_add_u32 s42, s42, 1\n"
      "v_max_f32 v184, v184, v209\n"
      "v_mov_b32 v209, v184\n"
      "v_mfma_f32_16x16x32_bf16 v[178:181], v[76:79], v[92:95], v[178:181]\n"
      "s_nop 0\n"
      "v_rcp_f32_e64 v166, v166\n"
      "v_mfma_f32_16x16x32_bf16 v[158:161], v[96:99], v[198:201], 0\n"
      "v_permlane32_swap_b32 v184, v209\n"
      "v_mfma_f32_16x16x32_bf16 v[158:161], v[80:83], v[92:95], v[158:161]\n"
      "v_max_f32 v184, v184, v209\n"
      "v_sub_f32_e64 v100, v100, v184\n"
      "v_mfma_f32_16x16x32_bf16 v[206:209], v[104:107], v[198:201], 0\n"
      "s_waitcnt lgkmcnt(0)\n"
      "v_exp_f32 v100, v100\n"
      "v_mfma_f32_16x16x32_bf16 v[206:209], v[84:87], v[92:95], v[206:209]\n"
      "buffer_store_dwordx4 v[186:189], v20, s[24:27], s85 offen sc1\n"
      "v_fma_f32 v182, v210, v210, s52\n"
      "v_pk_mul_f32 v[178:179], v[178:179], v[166:167] op_sel_hi:[1,0]\n"
      "v_pk_add_f32 v[146:147], v[146:147], v[184:185] op_sel_hi:[1,0] neg_lo:[0,1] neg_hi:[0,1]\n"
      "v_pk_add_f32 v[134:135], v[134:135], v[184:185] op_sel_hi:[1,0] neg_lo:[0,1] neg_hi:[0,1]\n"
      "v_pk_fma_f32 v[192:193], v[216:217], v[216:217], v[192:193]\n"
      "v_pk_fma_f32 v[182:183], v[212:213], v[212:213], v[182:183]\n"
      "v_pk_add_f32 v[126:127], v[126:127], v[184:185] op_sel_hi:[1,0] neg_lo:[0,1] neg_hi:[0,1]\n"
      "v_pk_mul_f32 v[180:181], v[180:181], v[166:167] op_sel_hi:[1,0]\n"
      "v_pk_fma_f32 v[182:183], v[122:123], v[122:123], v[182:183]\n"
      "v_pk_mul_f32 v[208:209], v[208:209], v[166:167] op_sel_hi:[1,0]\n"
      "v_pk_add_f32 v[144:145], v[144:145], v[184:185] op_sel_hi:[1,0] neg_lo:[0,1] neg_hi:[0,1]\n"
      "v_pk_mul_f32 v[206:207], v[206:207], v[166:167] op_sel_hi:[1,0]\n"
      "v_pk_mul_f32 v[160:161], v[160:161], v[166:167] op_sel_hi:[1,0]\n"
      "v_pk_add_f32 v[128:129], v[128:129], v[184:185] op_sel_hi:[1,0] neg_lo:[0,1] neg_hi:[0,1]\n"
      "v_pk_add_f32 v[132:133], v[132:133], v[184:185] op_sel_hi:[1,0] neg_lo:[0,1] neg_hi:[0,1]\n"
      "v_cvt_pk_bf16_f32 v178, v178, v179\n"
      "v_pk_mul_f32 v[158:159], v[158:159], v[166:167] op_sel_hi:[1,0]\n"
      "v_mfma_f32_16x16x32_bf16 v[184:187], v[220:223], v[202:205], v[30:33]\n"
      "ds_read_b128 v[200:203], v2 offset:3584\n"
      "v_cvt_pk_bf16_f32 v179, v180, v181\n"
      "v_mfma_f32_16x16x32_bf16 v[166:169], v[220:223], v[112:115], v[30:33]\n"
      "ds_read_b128 v[112:115], v2 offset:6656\n"
      "v_cvt_pk_bf16_f32 v180, v158, v159\n"
      "v_mfma_f32_16x16x32_bf16 v[188:191], v[220:223], v[170:173], v[30:33]\n"
      "ds_read_b128 v[170:173], v2 offset:512\n"
      "v_exp_f32_e64 v129, v129\n"
      "v_mfma_f32_16x16x32_bf16 v[156:159], v[220:223], v[140:143], v[30:33]\n"
      "ds_read_b128 v[138:141], v7 offset:4096\n"
      "v_cvt_pk_bf16_f32 v181, v160, v161\n"
      "v_mfma_f32_16x16x16_bf16 v[184:187], v[102:103], v[162:163], v[184:187]\n"
      "v_exp_f32 v132, v132\n"
      "v_exp_f32 v146, v146\n"
      "v_mfma_f32_16x16x16_bf16 v[166:169], v[102:103], v[116:117], v[166:169]\n"
      "v_exp_f32 v135, v135\n"
      "v_exp_f32 v147, v147\n"
      "v_mfma_f32_16x16x16_bf16 v[188:191], v[102:103], v[108:109], v[188:191]\n"
      "v_exp_f32 v133, v133\n"
      "v_exp_f32 v126, v126\n"
      "v_exp_f32_e64 v134, v134\n"
      "v_pk_fma_f32 v[192:193], v[148:149], v[148:149], v[192:193]\n"
      "v_mfma_f32_16x16x16_bf16 v[156:159], v[102:103], v[130:131], v[156:159]\n"
      "v_cndmask_b32 v184, v184, v166, s[58:59]\n"
      "v_cvt_pk_bf16_f32 v90, v100, 0\n"
      "s_waitcnt lgkmcnt(0)\n"
      "v_mfma_f32_16x16x32_bf16 v[220:223], a[216:219], v[170:173], 0\n"
      "ds_read_b128 v[160:163], v2 offset:12800\n"
      "v_cndmask_b32 v184, v184, v188, s[60:61]\n"
      "v_mfma_f32_16x16x32_bf16 v[220:223], a[220:223], v[200:203], v[220:223]\n"
      "buffer_store_dwordx4 v[138:141], v21, s[24:27], s85 offen sc1\n"
      "v_mfma_f32_16x16x32_bf16 v[220:223], a[224:227], v[112:115], v[220:223]\n"
      "s_nop 0\n"
      "ds_read_b128 v[138:141], v2 offset:9728\n"
      "v_cndmask_b32 v184, v184, v156, s[62:63]\n"
      "v_mfma_f32_16x16x32_bf16 v[166:169], a[240:243], v[170:173], 0\n"
      "ds_read_b128 v[156:159], v2 offset:15872\n"
      "v_exp_f32_e64 v144, v144\n"
      "v_mfma_f32_16x16x32_bf16 v[166:169], a[244:247], v[200:203], v[166:169]\n"
      "ds_read_b128 v[100:103], v7 offset:8192\n"
      "v_exp_f32_e64 v145, v145\n"
      "v_max_f32_dpp v131, v184, v184 row_ror:8 row_mask:0xf bank_mask:0xf\n"
      "v_mfma_f32_16x16x32_bf16 v[166:169], a[248:251], v[112:115], v[166:169]\n"
      "ds_write_b64 v4, v[154:155] offset:18432\n"
      "v_max_f32_dpp v131, v131, v131 row_ror:4 row_mask:0xf bank_mask:0xf\n"
      "v_mfma_f32_16x16x32_bf16 v[186:189], v[232:235], v[170:173], 0\n"
      "ds_read_b128 v[170:173], v7 offset:12288\n"
      "v_cvt_pk_bf16_f32 v89, v146, v147\n"
      "v_mfma_f32_16x16x32_bf16 v[186:189], v[236:239], v[200:203], v[186:189]\n"
      "v_max_f32_dpp v131, v131, v131 row_ror:2 row_mask:0xf bank_mask:0xf\n"
      "v_cvt_pk_bf16_f32 v88, v144, v145\n"
      "v_mfma_f32_16x16x32_bf16 v[186:189], v[240:243], v[112:115], v[186:189]\n"
      "s_waitcnt lgkmcnt(3)\n"
      "v_exp_f32 v127, v127\n"
      "v_mfma_f32_16x16x32_bf16 v[220:223], a[228:231], v[138:141], v[220:223]\n"
      "v_exp_f32 v128, v128\n"
      "v_mfma_f32_16x16x32_bf16 v[220:223], a[232:235], v[160:163], v[220:223]\n"
      "s_waitcnt lgkmcnt(2)\n"
      "v_cvt_pk_bf16_f32 v132, v132, v133\n"
      "v_max_f32_dpp v131, v131, v131 row_ror:1 row_mask:0xf bank_mask:0xf\n"
      "v_mfma_f32_16x16x32_bf16 v[220:223], a[236:239], v[156:159], v[220:223]\n"
      "ds_read_b128 v[112:115], v7 offset:16384\n"
      "v_max_f32_dpp v131, v131, v131 row_bcast:15 row_mask:0xa bank_mask:0xf\n"
      "v_mfma_f32_16x16x32_bf16 v[166:169], a[252:255], v[138:141], v[166:169]\n"
      "s_nop 0\n"
      "v_max_f32_dpp v131, v131, v131 row_bcast:31 row_mask:0xc bank_mask:0xf\n"
      "v_cvt_pk_bf16_f32 v133, v134, v135\n"
      "v_mfma_f32_16x16x32_bf16 v[166:169], v[224:227], v[160:163], v[166:169]\n"
      "buffer_store_dwordx4 v[100:103], v22, s[24:27], s85 offen sc1\n"
      "v_mfma_f32_16x16x32_bf16 v[166:169], v[228:231], v[156:159], v[166:169]\n"
      "v_readlane_b32 s72, v131, 63\n"
      "v_mul_f32 v131, v221, v221\n"
      "v_mfma_f32_16x16x32_bf16 v[186:189], v[244:247], v[138:141], v[186:189]\n"
      "ds_write_b128 v3, v[178:181] offset:18688\n"
      "v_subrev_f32 v184, s72, v184\n"
      "v_fma_f32 v130, v220, v220, s52\n"
      "v_pk_fma_f32 v[182:183], v[124:125], v[124:125], v[182:183]\n"
      "v_pk_fma_f32 v[182:183], v[118:119], v[118:119], v[182:183]\n"
      "v_exp_f32_e64 v184, v184\n"
      "v_pk_fma_f32 v[130:131], v[222:223], v[222:223], v[130:131]\n"
      "v_pk_fma_f32 v[192:193], v[150:151], v[150:151], v[192:193]\n"
      "v_cvt_pk_bf16_f32 v134, v126, v127\n"
      "v_mfma_f32_16x16x32_bf16 v[186:189], v[248:251], v[160:163], v[186:189]\n"
      "s_waitcnt lgkmcnt(1)\n"
      "v_cvt_pk_bf16_f32 v135, v128, v129\n"
      "v_cvt_pk_bf16_f32 v184, v184, v184\n"
      "v_mfma_f32_16x16x32_bf16 v[186:189], v[252:255], v[156:159], v[186:189]\n"
      "ds_write_b16 v13, v184\n"
      "v_mfma_f32_16x16x32_bf16 v[160:163], v[32:35], v[132:135], 0\n"
      "buffer_store_dwordx4 v[170:173], v23, s[24:27], s85 offen sc1\n"
      "v_pk_fma_f32 v[182:183], v[120:121], v[120:121], v[182:183]\n"
      "v_mfma_f32_16x16x32_bf16 v[160:163], v[32:35], v[88:91], v[160:163]\n"
      "v_add_f32 v182, v182, v183\n"
      "v_add_f32 v192, v192, v193\n"
      "v_mfma_f32_16x16x32_bf16 v[200:203], v[194:197], v[132:135], 0\n"
      "ds_read_b128 v[178:181], v14\n"
      "v_cvt_pk_bf16_f32 v206, v206, v207\n"
      "v_mfma_f32_16x16x32_bf16 v[200:203], v[76:79], v[88:91], v[200:203]\n"
      "ds_write_b32 v31, v136 offset:5632\n"
      "v_mfma_f32_16x16x32_bf16 v[152:155], v[96:99], v[132:135], 0\n"
      "v_rcp_f32_e64 v160, v160\n"
      "v_mfma_f32_16x16x32_bf16 v[152:155], v[80:83], v[88:91], v[152:155]\n"
      "ds_read_b128 v[100:103], v14 offset:64\n"
      "v_permlane16_swap_b32_e64 v192, v182\n"
      "v_cvt_pk_bf16_f32 v207, v208, v209\n"
      "v_add_f32_e64 v192, v192, v182\n"
      "v_pk_fma_f32 v[130:131], v[166:167], v[166:167], v[130:131]\n"
      "v_pk_mul_f32 v[200:201], v[200:201], v[160:161] op_sel_hi:[1,0]\n"
      "v_pk_fma_f32 v[130:131], v[168:169], v[168:169], v[130:131]\n"
      "v_pk_fma_f32 v[130:131], v[186:187], v[186:187], v[130:131]\n"
      "v_pk_fma_f32 v[130:131], v[188:189], v[188:189], v[130:131]\n"
      "v_mfma_f32_16x16x32_bf16 v[144:147], v[104:107], v[132:135], 0\n"
      "ds_write_b64 v4, v[206:207] offset:18688\n"
      "v_cvt_pk_bf16_f32 v200, v200, v201\n"
      "v_add_f32_e64 v130, v130, v131\n"
      "v_pk_mul_f32 v[152:153], v[152:153], v[160:161] op_sel_hi:[1,0]\n"
      "v_pk_mul_f32 v[154:155], v[154:155], v[160:161] op_sel_hi:[1,0]\n"
      "v_pk_mul_f32 v[202:203], v[202:203], v[160:161] op_sel_hi:[1,0]\n"
      "v_mfma_f32_16x16x32_bf16 v[144:147], v[84:87], v[88:91], v[144:147]\n"
      "s_waitcnt lgkmcnt(2)\n"
      "v_cvt_pk_bf16_f32 v201, v202, v203\n"
      "v_mov_b32 v182, v130\n"
      "v_cvt_pk_bf16_f32 v203, v154, v155\n"
      "v_cvt_pk_bf16_f32 v202, v152, v153\n"
      "s_add_u32 s43, s43, 1\n"
      "ds_write_b128 v3, v[200:203] offset:18944\n"
      "v_permlane16_swap_b32 v130, v182\n"
      "v_add_f32_e64 v130, v130, v182\n"
      "v_pk_mul_f32 v[144:145], v[144:145], v[160:161] op_sel_hi:[1,0]\n"
      "v_pk_mul_f32 v[146:147], v[146:147], v[160:161] op_sel_hi:[1,0]\n"
      "v_permlane32_swap_b32 v192, v130\n"
      "v_add_f32 v192, v192, v130\n"
      "v_mfma_f32_16x16x32_bf16 v[160:163], v[178:181], v[32:35], 0\n"
      "ds_write_b32 v31, v192 offset:1024\n"
      "v_cvt_pk_bf16_f32 v144, v144, v145\n"
      "v_cvt_pk_bf16_f32 v145, v146, v147\n"
      "s_waitcnt lgkmcnt(2)\n"
      "v_mfma_f32_16x16x32_bf16 v[160:163], v[100:103], v[32:35], v[160:163]\n"
      "ds_write_b64 v4, v[144:145] offset:18944\n"
      "v_add_u32 v10, 2336, v10\n"
      "v_add_u32 v11, 2336, v11\n"
      "buffer_store_dwordx4 v[112:115], v25, s[24:27], s85 offen sc1\n"
      "s_cmp_ge_u32 s95, 0x40000000\n"
      "s_cselect_b64 vcc, -1, s[56:57]\n"
      "s_waitcnt lgkmcnt(0)\n"
      "s_barrier\n"
      "s_cbranch_vccnz L_disp0_%=\n"
      "L_max0_%=:\n"
      "v_mfma_f32_16x16x32_bf16 v[112:115], v[178:181], v[194:197], 0\n"
      "ds_read_b128 v[130:133], v0\n"
      "v_mfma_f32_16x16x32_bf16 v[112:115], v[100:103], v[76:79], v[112:115]\n"
      "ds_read_b128 v[190:193], v9 offset:1024\n"
      "v_readlane_b32 s85, v37, s42\n"
      "v_mfma_f32_16x16x32_bf16 v[126:129], v[178:181], v[96:99], 0\n"
      "s_add_u32 m0, s64, 18432\n"
      "ds_read_b128 v[142:145], v1 offset:12288\n"
      "v_mfma_f32_16x16x32_bf16 v[126:129], v[100:103], v[80:83], v[126:129]\n"
      "buffer_load_dwordx4 v16, s[16:19], s45 offen  lds\n"
      "v_mfma_f32_16x16x32_bf16 v[152:155], v[178:181], v[104:107], 0\n"
      "ds_read_b128 v[114:117], v1\n"
      "ds_read_b128 v[108:111], v0 offset:6144\n"
      "v_mfma_f32_16x16x32_bf16 v[152:155], v[100:103], v[84:87], v[152:155]\n"
      "ds_read_b128 v[206:209], v0 offset:12288\n"
      "s_waitcnt lgkmcnt(5)\n"
      "v_mfma_f32_16x16x32_bf16 v[198:201], a[0:3], v[130:133], 0\n"
      "ds_read_b128 v[178:181], v1 offset:6144\n"
      "s_add_u32 m0, s64, 22528\n"
      "v_mfma_f32_16x16x32_bf16 v[134:137], a[24:27], v[130:133], 0\n"
      "s_waitcnt lgkmcnt(5)\n"
      "v_add_f32 v192, v192, v193\n"
      "v_add_f32 v190, v190, v191\n"
      "v_mfma_f32_16x16x32_bf16 v[170:173], a[48:51], v[130:133], 0\n"
      "buffer_load_dwordx4 v17, s[16:19], s45 offen  lds\n"
      "v_mfma_f32_16x16x32_bf16 v[100:103], a[72:75], v[130:133], 0\n"
      "s_add_u32 m0, s64, 26624\n"
      "v_mov_b32_dpp v160, v160 quad_perm:[0,0,0,0] row_mask:0xf bank_mask:0xf\n"
      "v_add_f32 v190, v190, v192\n"
      "v_mfma_f32_16x16x32_bf16 v[182:185], a[96:99], v[130:133], 0\n"
      "v_rcp_f32 v160, v160\n"
      "v_mfma_f32_16x16x32_bf16 v[154:157], a[120:123], v[130:133], 0\n"
      "s_waitcnt lgkmcnt(2)\n"
      "v_rsq_f32 v190, v190\n"
      "v_mfma_f32_16x16x32_bf16 v[198:201], a[4:7], v[114:117], v[198:201]\n"
      "ds_read_b128 v[162:165], v0 offset:2048\n"
      "v_mul_f32 v190, 0x415db3d7, v190\n"
      "v_mfma_f32_16x16x32_bf16 v[198:201], a[8:11], v[108:111], v[198:201]\n"
      "v_readlane_b32 s84, v37, s43\n"
      "v_mov_b32_e64 v192, v190\n"
      "v_mfma_f32_16x16x32_bf16 v[134:137], a[28:31], v[114:117], v[134:137]\n"
      "buffer_load_dwordx4 v18, s[16:19], s45 offen  lds\n"
      "v_mfma_f32_16x16x32_bf16 v[134:137], a[32:35], v[108:111], v[134:137]\n"
      "v_permlane32_swap_b32 v190, v192\n"
      "v_mfma_f32_16x16x32_bf16 v[170:173], a[52:55], v[114:117], v[170:173]\n"
      "v_cndmask_b32 v112, v112, v126, s[58:59]\n"
      "v_mov_b32 v191, v190\n"
      "v_mfma_f32_16x16x32_bf16 v[170:173], a[56:59], v[108:111], v[170:173]\n"
      "ds_read_b128 v[202:205], v9 offset:5632\n"
      "v_mul_f32_e64 v166, v192, v166\n"
      "v_mfma_f32_16x16x32_bf16 v[100:103], a[76:79], v[114:117], v[100:103]\n"
      "v_cndmask_b32 v112, v112, v152, s[60:61]\n"
      "v_mul_f32 v187, v192, v187\n"
      "v_mfma_f32_16x16x32_bf16 v[100:103], a[80:83], v[108:111], v[100:103]\n"
      "s_add_u32 m0, s64, 30720\n"
      "v_permlane16_swap_b32 v190, v191\n"
      "v_mfma_f32_16x16x32_bf16 v[182:185], a[100:103], v[114:117], v[182:185]\n"
      "v_mul_f32 v168, v192, v168\n"
      "v_mul_f32 v112, v160, v112\n"
      "v_mfma_f32_16x16x32_bf16 v[182:185], a[104:107], v[108:111], v[182:185]\n"
      "v_mul_f32 v151, v190, v151\n"
      "v_mul_f32 v176, v190, v176\n"
      "v_mfma_f32_16x16x32_bf16 v[154:157], a[124:127], v[114:117], v[154:157]\n"
      "s_waitcnt lgkmcnt(1)\n"
      "v_mul_f32 v175, v190, v175\n"
      "v_mul_f32_e64 v177, v190, v177\n"
      "v_mfma_f32_16x16x32_bf16 v[154:157], a[128:131], v[108:111], v[154:157]\n"
      "buffer_load_dwordx4 v19, s[16:19], s45 offen  lds\n"
      "v_mfma_f32_16x16x32_bf16 v[198:201], a[12:15], v[178:181], v[198:201]\n"
      "v_readlane_b32 s86, v38, s43\n"
      "v_mul_f32_e64 v217, v190, v217\n"
      "v_mfma_f32_16x16x32_bf16 v[198:201], a[16:19], v[206:209], v[198:201]\n"
      "v_mul_f32 v215, v190, v215\n"
      "v_mul_f32 v214, v190, v214\n"
      "v_mfma_f32_16x16x32_bf16 v[198:201], a[20:23], v[142:145], v[198:201]\n"
      "v_mul_f32 v148, v190, v148\n"
      "v_mul_f32 v149, v190, v149\n"
      "v_mfma_f32_16x16x32_bf16 v[134:137], a[36:39], v[178:181], v[134:137]\n"
      "s_mov_b32 m0, s7\n"
      "v_mul_f32 v174, v190, v174\n"
      "v_cvt_pk_bf16_f32 v174, v174, v175\n"
      "v_mfma_f32_16x16x32_bf16 v[134:137], a[40:43], v[206:209], v[134:137]\n"
      "s_nop 0\n"
      "v_fma_f32 v113, v198, v198, s51\n"
      "v_mul_f32 v212, v191, v212\n"
      "v_mfma_f32_16x16x32_bf16 v[134:137], a[44:47], v[142:145], v[134:137]\n"
      "ds_read_b128 v[138:141], v0 offset:8192\n"
      "v_cvt_pk_bf16_f32 v175, v176, v177\n"
      "v_mfma_f32_16x16x32_bf16 v[170:173], a[60:63], v[178:181], v[170:173]\n"
      "v_mul_f32 v216, v190, v216\n"
      "v_mul_f32 v150, v190, v150\n"
      "v_mfma_f32_16x16x32_bf16 v[170:173], a[64:67], v[206:209], v[170:173]\n"
      "buffer_load_dwordx4 v15, s[0:3], s4 offen  lds\n"
      "v_mfma_f32_16x16x32_bf16 v[170:173], a[68:71], v[142:145], v[170:173]\n"
      "s_or_b32 s95, s84, s85\n"
      "v_mul_f32 v221, v192, v221\n"
      "v_cvt_pk_bf16_f32 v176, v214, v215\n"
      "v_mfma_f32_16x16x32_bf16 v[100:103], a[84:87], v[178:181], v[100:103]\n"
      "v_cvt_pk_bf16_f32 v177, v216, v217\n"
      "v_mul_f32 v220, v192, v220\n"
      "v_mfma_f32_16x16x32_bf16 v[100:103], a[88:91], v[206:209], v[100:103]\n"
      "v_mul_f32 v118, v191, v118\n"
      "v_fmac_f32_e64 v113, v199, v199\n"
      "v_mfma_f32_16x16x32_bf16 v[100:103], a[92:95], v[142:145], v[100:103]\n"
      "ds_write_b128 v5, v[174:177]\n"
      "v_pk_mul_f32 v[120:121], v[120:121], v[190:191] op_sel:[0,1]\n"
      "v_mfma_f32_16x16x32_bf16 v[182:185], a[108:111], v[178:181], v[182:185]\n"
      "buffer_load_dwordx4 v[126:129], v36, s[28:31], s47 offen\n"
      "v_mfma_f32_16x16x32_bf16 v[182:185], a[112:115], v[206:209], v[182:185]\n"
      "v_mul_f32 v210, v191, v210\n"
      "v_mul_f32 v119, v191, v119\n"
      "v_mfma_f32_16x16x32_bf16 v[182:185], a[116:119], v[142:145], v[182:185]\n"
      "s_waitcnt lgkmcnt(1)\n"
      "v_mul_f32 v122, v191, v122\n"
      "v_or3_b32 v202, v202, v203, v204\n"
      "v_mfma_f32_16x16x32_bf16 v[154:157], a[132:135], v[178:181], v[154:157]\n"
      "v_fma_f32 v78, v100, v100, s51\n"
      "v_mul_f32 v186, v192, v186\n"
      "v_mfma_f32_16x16x32_bf16 v[154:157], a[136:139], v[206:209], v[154:157]\n"
      "v_fmac_f32 v78, v101, v101\n"
      "v_fmac_f32_e64 v113, v200, v200\n"
      "v_mfma_f32_16x16x32_bf16 v[154:157], a[140:143], v[142:145], v[154:157]\n"
      "ds_read_b128 v[174:177], v1 offset:2048\n"
      "v_fmac_f32 v78, v102, v102\n"
      "v_mfma_f32_16x16x32_bf16 v[194:197], v[130:133], a[144:147], 0\n"
      "v_fmac_f32 v78, v103, v103\n"
      "v_mul_f32_e64 v189, v192, v189\n"
      "v_mfma_f32_16x16x32_bf16 v[194:197], v[114:117], a[148:151], v[194:197]\n"
      "v_cvt_pk_bf16_f32 v118, v118, v119\n"
      "v_cvt_pk_bf16_f32 v220, v220, v221\n"
      "v_mfma_f32_16x16x32_bf16 v[194:197], v[108:111], a[152:155], v[194:197]\n"
      "v_fmac_f32 v78, v182, v182\n"
      "v_fmac_f32 v78, v183, v183\n"
      "v_mfma_f32_16x16x32_bf16 v[194:197], v[178:181], a[156:159], v[194:197]\n"
      "ds_read_b128 v[158:161], v1 offset:8192\n"
      "v_bitop3_b32 v202, v202, v205, s33 bitop3:0xa8\n"
      "v_mfma_f32_16x16x32_bf16 v[194:197], v[206:209], a[160:163], v[194:197]\n"
      "v_fmac_f32 v113, v201, v201\n"
      "v_mul_f32 v213, v191, v213\n"
      "v_mfma_f32_16x16x32_bf16 v[194:197], v[142:145], a[164:167], v[194:197]\n"
      "ds_read_b128 v[214:217], v0 offset:14336\n"
      "v_fmac_f32 v78, v184, v184\n"
      "v_mfma_f32_16x16x32_bf16 v[96:99], v[130:133], a[168:171], 0\n"
      "v_cvt_pk_bf16_f32 v148, v148, v149\n"
      "v_mul_f32 v125, v191, v125\n"
      "v_mfma_f32_16x16x32_bf16 v[96:99], v[114:117], a[172:175], v[96:99]\n"
      "v_cvt_pk_bf16_f32 v149, v150, v151\n"
      "v_fmac_f32 v78, v185, v185\n"
      "v_mfma_f32_16x16x32_bf16 v[96:99], v[108:111], a[176:179], v[96:99]\n"
      "v_fmac_f32 v78, v154, v154\n"
      "v_mul_f32 v223, v192, v223\n"
      "v_mfma_f32_16x16x32_bf16 v[96:99], v[178:181], a[180:183], v[96:99]\n"
      "v_mul_f32 v188, v192, v188\n"
      "v_fmac_f32 v113, v134, v134\n"
      "v_mfma_f32_16x16x32_bf16 v[96:99], v[206:209], a[184:187], v[96:99]\n"
      "v_fmac_f32 v78, v155, v155\n"
      "v_mul_f32 v123, v191, v123\n"
      "v_mfma_f32_16x16x32_bf16 v[96:99], v[142:145], a[188:191], v[96:99]\n"
      "v_fmac_f32 v113, v135, v135\n"
      "v_mul_f32_e64 v222, v192, v222\n"
      "v_mfma_f32_16x16x32_bf16 v[104:107], v[130:133], a[192:195], 0\n"
      "ds_read_b128 v[150:153], v1 offset:4096\n"
      "v_fmac_f32 v113, v136, v136\n"
      "v_mfma_f32_16x16x32_bf16 v[104:107], v[114:117], a[196:199], v[104:107]\n"
      "ds_read_b128 v[114:117], v1 offset:14336\n"
      "v_mul_f32 v124, v191, v124\n"
      "v_mfma_f32_16x16x32_bf16 v[104:107], v[108:111], a[200:203], v[104:107]\n"
      "s_or_b32 s95, s95, s48\n"
      "v_fmac_f32 v113, v137, v137\n"
      "v_cvt_pk_bf16_f32 v194, v194, v195\n"
      "v_mfma_f32_16x16x32_bf16 v[104:107], v[178:181], a[204:207], v[104:107]\n"
      "v_fmac_f32_e64 v113, v170, v170\n"
      "v_cvt_pk_bf16_f32 v195, v196, v197\n"
      "v_mfma_f32_16x16x32_bf16 v[104:107], v[206:209], a[208:211], v[104:107]\n"
      "buffer_load_dword v93, v29, s[28:31], s47 offen\n"
      "v_mfma_f32_16x16x32_bf16 v[104:107], v[142:145], a[212:215], v[104:107]\n"
      "v_cvt_pk_bf16_f32 v112, v112, v112\n"
      "v_mul_f32 v211, v191, v211\n"
      "v_mfma_f32_16x16x32_bf16 v[108:111], a[0:3], v[162:165], 0\n"
      "s_waitcnt lgkmcnt(1)\n"
      "v_cvt_pk_bf16_f32 v210, v210, v211\n"
      "v_fmac_f32_e64 v78, v156, v156\n"
      "v_mfma_f32_16x16x32_bf16 v[108:111], a[4:7], v[174:177], v[108:111]\n"
      "ds_write_b16 v12, v112 offset:16\n"
      "v_mfma_f32_16x16x32_bf16 v[108:111], a[8:11], v[138:141], v[108:111]\n"
      "v_cvt_pk_bf16_f32 v186, v186, v187\n"
      "v_fmac_f32_e64 v113, v171, v171\n"
      "v_mfma_f32_16x16x32_bf16 v[108:111], a[12:15], v[158:161], v[108:111]\n"
      "ds_write_b64 v6, v[148:149]\n"
      "v_mfma_f32_16x16x32_bf16 v[108:111], a[16:19], v[214:217], v[108:111]\n"
      "s_waitcnt lgkmcnt(2)\n"
      "v_fmac_f32 v78, v157, v157\n"
      "v_fmac_f32_e64 v113, v172, v172\n"
      "v_mfma_f32_16x16x32_bf16 v[108:111], a[20:23], v[114:117], v[108:111]\n"
      "v_mul_f32 v167, v192, v167\n"
      "v_cvt_pk_bf16_f32 v187, v188, v189\n"
      "v_mfma_f32_16x16x32_bf16 v[146:149], a[24:27], v[162:165], 0\n"
      "v_fmac_f32 v113, v173, v173\n"
      "v_cvt_pk_bf16_f32 v211, v212, v213\n"
      "v_mfma_f32_16x16x32_bf16 v[146:149], a[28:31], v[174:177], v[146:149]\n"
      "v_cvt_pk_bf16_f32 v104, v104, v105\n"
      "v_cvt_pk_bf16_f32 v221, v222, v223\n"
      "v_mfma_f32_16x16x32_bf16 v[146:149], a[32:35], v[138:141], v[146:149]\n"
      "v_permlane16_swap_b32 v113, v78\n"
      "v_mfma_f32_16x16x32_bf16 v[146:149], a[36:39], v[158:161], v[146:149]\n"
      "v_cvt_pk_bf16_f32 v96, v96, v97\n"
      "v_add_f32 v113, v113, v78\n"
      "v_mfma_f32_16x16x32_bf16 v[146:149], a[40:43], v[214:217], v[146:149]\n"
      "v_mov_b32 v78, v113\n"
      "v_cvt_pk_bf16_f32 v212, v122, v123\n"
      "v_mfma_f32_16x16x32_bf16 v[146:149], a[44:47], v[114:117], v[146:149]\n"
      "v_permlane32_swap_b32 v113, v78\n"
      "v_mfma_f32_16x16x32_bf16 v[142:145], a[48:51], v[162:165], 0\n"
      "v_cvt_pk_bf16_f32 v222, v166, v167\n"
      "v_cvt_pk_bf16_f32 v213, v124, v125\n"
      "v_mfma_f32_16x16x32_bf16 v[142:145], a[52:55], v[174:177], v[142:145]\n"
      "v_fma_f32 v76, v108, v108, s51\n"
      "v_add_f32 v113, v113, v78\n"
      "v_mfma_f32_16x16x32_bf16 v[142:145], a[56:59], v[138:141], v[142:145]\n"
      "v_rsq_f32 v113, v113\n"
      "v_mfma_f32_16x16x32_bf16 v[142:145], a[60:63], v[158:161], v[142:145]\n"
      "v_fmac_f32 v76, v109, v109\n"
      "v_mov_b32 v78, v113\n"
      "v_mfma_f32_16x16x32_bf16 v[142:145], a[64:67], v[214:217], v[142:145]\n"
      "v_mul_f32_e64 v169, v192, v169\n"
      "v_cvt_pk_bf16_f32 v97, v98, v99\n"
      "v_mfma_f32_16x16x32_bf16 v[142:145], a[68:71], v[114:117], v[142:145]\n"
      "v_permlane16_swap_b32_e64 v113, v78\n"
      "v_mfma_f32_16x16x32_bf16 v[206:209], a[72:75], v[162:165], 0\n"
      "ds_write_b128 v5, v[210:213] offset:6144\n"
      "v_mfma_f32_16x16x32_bf16 v[206:209], a[76:79], v[174:177], v[206:209]\n"
      "v_fmac_f32 v76, v110, v110\n"
      "v_fmac_f32 v76, v111, v111\n"
      "v_mfma_f32_16x16x32_bf16 v[206:209], a[80:83], v[138:141], v[206:209]\n"
      "ds_read_b128 v[130:133], v1 offset:10240\n"
      "v_mul_f32 v155, v78, v155\n"
      "v_mfma_f32_16x16x32_bf16 v[206:209], a[84:87], v[158:161], v[206:209]\n"
      "v_fmac_f32 v76, v146, v146\n"
      "v_cvt_pk_bf16_f32 v119, v120, v121\n"
      "v_mfma_f32_16x16x32_bf16 v[206:209], a[88:91], v[214:217], v[206:209]\n"
      "ds_read_b64 v[218:219], v10 offset:24\n"
      "v_mul_f32 v113, s50, v113\n"
      "v_mfma_f32_16x16x32_bf16 v[206:209], a[92:95], v[114:117], v[206:209]\n"
      "v_mul_f32 v135, v113, v135\n"
      "v_mul_f32_e64 v184, v78, v184\n"
      "v_mfma_f32_16x16x32_bf16 v[122:125], a[96:99], v[162:165], 0\n"
      "v_mul_f32_e64 v173, v113, v173\n"
      "v_cmp_ne_u32 s[56:57], 0, v202\n"
      "v_mfma_f32_16x16x32_bf16 v[122:125], a[100:103], v[174:177], v[122:125]\n"
      "ds_read_b128 v[210:213], v0 offset:10240\n"
      "v_mul_f32 v200, v113, v200\n"
      "v_mfma_f32_16x16x32_bf16 v[122:125], a[104:107], v[138:141], v[122:125]\n"
      "v_mul_f32 v185, v78, v185\n"
      "v_fma_f32 v196, v206, v206, s51\n"
      "v_mfma_f32_16x16x32_bf16 v[122:125], a[108:111], v[158:161], v[122:125]\n"
      "v_fmac_f32 v76, v147, v147\n"
      "v_fmac_f32 v76, v148, v148\n"
      "v_mfma_f32_16x16x32_bf16 v[122:125], a[112:115], v[214:217], v[122:125]\n"
      "v_fmac_f32 v76, v149, v149\n"
      "v_fmac_f32 v196, v207, v207\n"
      "v_mfma_f32_16x16x32_bf16 v[122:125], a[116:119], v[114:117], v[122:125]\n"
      "v_cvt_pk_bf16_f32 v105, v106, v107\n"
      "v_cvt_pk_bf16_f32 v223, v168, v169\n"
      "v_mfma_f32_16x16x32_bf16 v[166:169], a[120:123], v[162:165], 0\n"
      "ds_read_b128 v[202:205], v0 offset:4096\n"
      "v_mul_f32_e64 v170, v113, v170\n"
      "v_mfma_f32_16x16x32_bf16 v[166:169], a[124:127], v[174:177], v[166:169]\n"
      "ds_write_b64 v6, v[186:187] offset:12288\n"
      "v_mfma_f32_16x16x32_bf16 v[166:169], a[128:131], v[138:141], v[166:169]\n"
      "v_mul_f32 v171, v113, v171\n"
      "v_mul_f32 v156, v78, v156\n"
      "v_mfma_f32_16x16x32_bf16 v[166:169], a[132:135], v[158:161], v[166:169]\n"
      "v_mul_f32 v198, v113, v198\n"
      "v_fmac_f32 v76, v142, v142\n"
      "v_mfma_f32_16x16x32_bf16 v[166:169], a[136:139], v[214:217], v[166:169]\n"
      "v_fmac_f32 v76, v143, v143\n"
      "v_fmac_f32 v196, v208, v208\n"
      "v_mfma_f32_16x16x32_bf16 v[166:169], a[140:143], v[114:117], v[166:169]\n"
      "s_cmp_ge_u32 s95, 0x40000000\n"
      "s_cselect_b32 s94, -1, -2\n"
      "v_mul_f32 v172, v113, v172\n"
      "v_mfma_f32_16x16x32_bf16 v[178:181], v[162:165], a[144:147], 0\n"
      "v_mul_f32 v102, v78, v102\n"
      "v_mul_f32 v182, v78, v182\n"
      "v_mfma_f32_16x16x32_bf16 v[178:181], v[174:177], a[148:151], v[178:181]\n"
      "v_mul_f32 v199, v113, v199\n"
      "v_fmac_f32 v196, v209, v209\n"
      "v_mfma_f32_16x16x32_bf16 v[178:181], v[138:141], a[152:155], v[178:181]\n"
      "v_fmac_f32 v196, v122, v122\n"
      "v_mul_f32_e64 v183, v78, v183\n"
      "v_mfma_f32_16x16x32_bf16 v[178:181], v[158:161], a[156:159], v[178:181]\n"
      "ds_write_b64 v6, v[118:119] offset:6144\n"
      "v_mfma_f32_16x16x32_bf16 v[178:181], v[214:217], a[160:163], v[178:181]\n"
      "v_cvt_pk_bf16_f32 v198, v198, v199\n"
      "v_fmac_f32_e64 v196, v123, v123\n"
      "v_mfma_f32_16x16x32_bf16 v[178:181], v[114:117], a[164:167], v[178:181]\n"
      "v_fmac_f32 v196, v124, v124\n"
      "v_fmac_f32 v196, v125, v125\n"
      "v_mfma_f32_16x16x32_bf16 v[190:193], v[162:165], a[168:171], 0\n"
      "v_mul_f32 v100, v78, v100\n"
      "v_mul_f32 v101, v78, v101\n"
      "v_mfma_f32_16x16x32_bf16 v[190:193], v[174:177], a[172:175], v[190:193]\n"
      "v_mul_f32 v103, v78, v103\n"
      "v_mul_f32 v154, v78, v154\n"
      "v_mfma_f32_16x16x32_bf16 v[190:193], v[138:141], a[176:179], v[190:193]\n"
      "v_fmac_f32 v76, v144, v144\n"
      "v_fmac_f32 v196, v166, v166\n"
      "v_mfma_f32_16x16x32_bf16 v[190:193], v[158:161], a[180:183], v[190:193]\n"
      "v_mul_f32 v136, v113, v136\n"
      "v_fmac_f32 v196, v167, v167\n"
      "v_mfma_f32_16x16x32_bf16 v[190:193], v[214:217], a[184:187], v[190:193]\n"
      "ds_read_b128 v[186:189], v1 offset:16384\n"
      "v_mul_f32_e64 v157, v78, v157\n"
      "v_mfma_f32_16x16x32_bf16 v[190:193], v[114:117], a[188:191], v[190:193]\n"
      "ds_write_b128 v5, v[220:223] offset:12288\n"
      "v_mfma_f32_16x16x32_bf16 v[118:121], v[162:165], a[192:195], 0\n"
      "ds_read_b128 v[162:165], v0 offset:16384\n"
      "v_cvt_pk_bf16_f32 v170, v170, v171\n"
      "v_mfma_f32_16x16x32_bf16 v[118:121], v[174:177], a[196:199], v[118:121]\n"
      "v_fmac_f32 v196, v168, v168\n"
      "v_fmac_f32 v76, v145, v145\n"
      "v_mfma_f32_16x16x32_bf16 v[118:121], v[138:141], a[200:203], v[118:121]\n"
      "v_fmac_f32_e64 v196, v169, v169\n"
      "v_cvt_pk_bf16_f32 v154, v154, v155\n"
      "v_mfma_f32_16x16x32_bf16 v[118:121], v[158:161], a[204:207], v[118:121]\n"
      "v_cvt_pk_bf16_f32 v99, v192, v193\n"
      "v_mul_f32 v201, v113, v201\n"
      "v_mfma_f32_16x16x32_bf16 v[118:121], v[214:217], a[208:211], v[118:121]\n"
      "v_permlane16_swap_b32 v76, v196\n"
      "v_mfma_f32_16x16x32_bf16 v[118:121], v[114:117], a[212:215], v[118:121]\n"
      "s_waitcnt lgkmcnt(2)\n"
      "v_cvt_pk_bf16_f32 v98, v190, v191\n"
      "v_add_f32 v76, v76, v196\n"
      "v_mfma_f32_16x16x32_bf16 v[220:223], a[0:3], v[202:205], 0\n"
      "v_cvt_pk_bf16_f32 v199, v200, v201\n"
      "v_cvt_pk_bf16_f32 v197, v180, v181\n"
      "v_mfma_f32_16x16x32_bf16 v[220:223], a[4:7], v[150:153], v[220:223]\n"
      "v_mul_f32 v137, v113, v137\n"
      "v_mov_b32 v196, v76\n"
      "v_mfma_f32_16x16x32_bf16 v[220:223], a[8:11], v[210:213], v[220:223]\n"
      "v_cvt_pk_bf16_f32 v100, v100, v101\n"
      "v_mul_f32 v134, v113, v134\n"
      "v_mfma_f32_16x16x32_bf16 v[220:223], a[12:15], v[130:133], v[220:223]\n"
      "s_waitcnt lgkmcnt(0)\n"
      "v_permlane32_swap_b32 v76, v196\n"
      "v_add_f32 v76, v76, v196\n"
      "v_mfma_f32_16x16x32_bf16 v[220:223], a[16:19], v[162:165], v[220:223]\n"
      "v_rsq_f32 v76, v76\n"
      "v_mfma_f32_16x16x32_bf16 v[220:223], a[20:23], v[186:189], v[220:223]\n"
      "v_cvt_pk_bf16_f32 v106, v118, v119\n"
      "v_mov_b32 v196, v76\n"
      "v_mfma_f32_16x16x32_bf16 v[116:119], a[24:27], v[202:205], 0\n"
      "s_cmp_gt_u32 s42, s44\n"
      "s_cselect_b32 s95, s94, s95\n"
      "v_cvt_pk_bf16_f32 v200, v134, v135\n"
      "v_mfma_f32_16x16x32_bf16 v[116:119], a[28:31], v[150:153], v[116:119]\n"
      "v_permlane16_swap_b32 v76, v196\n"
      "v_mfma_f32_16x16x32_bf16 v[116:119], a[32:35], v[210:213], v[116:119]\n"
      "v_fma_f32 v90, v220, v220, s51\n"
      "v_fmac_f32 v90, v221, v221\n"
      "v_mfma_f32_16x16x32_bf16 v[116:119], a[36:39], v[130:133], v[116:119]\n"
      "v_fmac_f32 v90, v222, v222\n"
      "v_cvt_pk_bf16_f32 v201, v136, v137\n"
      "v_mfma_f32_16x16x32_bf16 v[116:119], a[40:43], v[162:165], v[116:119]\n"
      "v_mul_f32 v76, s50, v76\n"
      "v_cvt_pk_bf16_f32 v101, v102, v103\n"
      "v_mfma_f32_16x16x32_bf16 v[116:119], a[44:47], v[186:189], v[116:119]\n"
      "v_cvt_pk_bf16_f32 v171, v172, v173\n"
      "v_fmac_f32 v90, v223, v223\n"
      "v_mfma_f32_16x16x32_bf16 v[112:115], a[48:51], v[202:205], 0\n"
      "v_mul_f32 v144, v76, v144\n"
      "v_mul_f32 v209, v196, v209\n"
      "v_mfma_f32_16x16x32_bf16 v[112:115], a[52:55], v[150:153], v[112:115]\n"
      "v_mul_f32 v110, v76, v110\n"
      "v_mul_f32 v111, v76, v111\n"
      "v_mfma_f32_16x16x32_bf16 v[112:115], a[56:59], v[210:213], v[112:115]\n"
      "v_mul_f32 v208, v196, v208\n"
      "v_mul_f32 v206, v196, v206\n"
      "v_mfma_f32_16x16x32_bf16 v[112:115], a[60:63], v[130:133], v[112:115]\n"
      "v_fmac_f32 v90, v116, v116\n"
      "v_mul_f32 v169, v196, v169\n"
      "v_mfma_f32_16x16x32_bf16 v[112:115], a[64:67], v[162:165], v[112:115]\n"
      "v_mul_f32 v143, v76, v143\n"
      "v_mul_f32 v207, v196, v207\n"
      "v_mfma_f32_16x16x32_bf16 v[112:115], a[68:71], v[186:189], v[112:115]\n"
      "v_mul_f32 v168, v196, v168\n"
      "v_fmac_f32_e64 v90, v117, v117\n"
      "v_mfma_f32_16x16x32_bf16 v[158:161], a[72:75], v[202:205], 0\n"
      "v_cvt_pk_bf16_f32 v206, v206, v207\n"
      "v_cvt_pk_bf16_f32 v102, v182, v183\n"
      "v_mfma_f32_16x16x32_bf16 v[158:161], a[76:79], v[150:153], v[158:161]\n"
      "v_mul_f32 v149, v76, v149\n"
      "v_mul_f32 v108, v76, v108\n"
      "v_mfma_f32_16x16x32_bf16 v[158:161], a[80:83], v[210:213], v[158:161]\n"
      "v_mul_f32 v125, v196, v125\n"
      "v_mul_f32 v145, v76, v145\n"
      "v_mfma_f32_16x16x32_bf16 v[158:161], a[84:87], v[130:133], v[158:161]\n"
      "v_mul_f32 v123, v196, v123\n"
      "v_add_u32 v12, 32, v12\n"
      "v_mfma_f32_16x16x32_bf16 v[158:161], a[88:91], v[162:165], v[158:161]\n"
      "v_mul_f32 v167, v196, v167\n"
      "v_fmac_f32 v90, v118, v118\n"
      "v_mfma_f32_16x16x32_bf16 v[158:161], a[92:95], v[186:189], v[158:161]\n"
      "v_cvt_pk_bf16_f32 v207, v208, v209\n"
      "v_cvt_pk_bf16_f32 v103, v184, v185\n"
      "v_mfma_f32_16x16x32_bf16 v[190:193], a[96:99], v[202:205], 0\n"
      "v_mul_f32 v109, v76, v109\n"
      "v_mul_f32 v124, v196, v124\n"
      "v_mfma_f32_16x16x32_bf16 v[190:193], a[100:103], v[150:153], v[190:193]\n"
      "v_mul_f32 v147, v76, v147\n"
      "v_mul_f32 v166, v196, v166\n"
      "v_mfma_f32_16x16x32_bf16 v[190:193], a[104:107], v[210:213], v[190:193]\n"
      "ds_read_u16 v78, v11\n"
      "v_fmac_f32_e64 v90, v119, v119\n"
      "v_mfma_f32_16x16x32_bf16 v[190:193], a[108:111], v[130:133], v[190:193]\n"
      "v_cvt_pk_bf16_f32 v107, v120, v121\n"
      "v_cvt_pk_bf16_f32 v108, v108, v109\n"
      "v_mfma_f32_16x16x32_bf16 v[190:193], a[112:115], v[162:165], v[190:193]\n"
      "v_fma_f32 v77, v158, v158, s51\n"
      "v_fmac_f32 v90, v112, v112\n"
      "v_mfma_f32_16x16x32_bf16 v[190:193], a[116:119], v[186:189], v[190:193]\n"
      "v_mul_f32 v148, v76, v148\n"
      "v_fmac_f32_e64 v90, v113, v113\n"
      "v_mfma_f32_16x16x32_bf16 v[134:137], a[120:123], v[202:205], 0\n"
      "v_fmac_f32 v90, v114, v114\n"
      "v_fmac_f32 v77, v159, v159\n"
      "v_mfma_f32_16x16x32_bf16 v[134:137], a[124:127], v[150:153], v[134:137]\n"
      "v_cvt_pk_bf16_f32 v209, v124, v125\n"
      "v_fmac_f32 v77, v160, v160\n"
      "v_mfma_f32_16x16x32_bf16 v[134:137], a[128:131], v[210:213], v[134:137]\n"
      "v_mul_f32 v146, v76, v146\n"
      "v_mul_f32 v122, v196, v122\n"
      "v_mfma_f32_16x16x32_bf16 v[134:137], a[132:135], v[130:133], v[134:137]\n"
      "s_add_u32 s43, s43, 1\n"
      "v_fmac_f32 v77, v161, v161\n"
      "v_cvt_pk_bf16_f32 v155, v156, v157\n"
      "v_mfma_f32_16x16x32_bf16 v[134:137], a[136:139], v[162:165], v[134:137]\n"
      "v_mul_f32 v142, v76, v142\n"
      "v_cvt_pk_bf16_f32 v166, v166, v167\n"
      "v_mfma_f32_16x16x32_bf16 v[134:137], a[140:143], v[186:189], v[134:137]\n"
      "v_fmac_f32 v77, v190, v190\n"
      "v_cvt_pk_bf16_f32 v109, v110, v111\n"
      "v_mfma_f32_16x16x32_bf16 v[182:185], v[202:205], a[144:147], 0\n"
      "v_fmac_f32 v77, v191, v191\n"
      "v_fmac_f32_e64 v77, v192, v192\n"
      "v_mfma_f32_16x16x32_bf16 v[182:185], v[150:153], a[148:151], v[182:185]\n"
      "v_cvt_pk_bf16_f32 v110, v146, v147\n"
      "v_cvt_pk_bf16_f32 v111, v148, v149\n"
      "v_mfma_f32_16x16x32_bf16 v[182:185], v[210:213], a[152:155], v[182:185]\n"
      "v_fmac_f32 v77, v193, v193\n"
      "v_cvt_pk_bf16_f32 v208, v122, v123\n"
      "v_mfma_f32_16x16x32_bf16 v[182:185], v[130:133], a[156:159], v[182:185]\n"
      "v_fmac_f32 v90, v115, v115\n"
      "v_fmac_f32 v77, v134, v134\n"
      "v_mfma_f32_16x16x32_bf16 v[182:185], v[162:165], a[160:163], v[182:185]\n"
      "v_fmac_f32 v77, v135, v135\n"
      "v_fmac_f32_e64 v77, v136, v136\n"
      "v_mfma_f32_16x16x32_bf16 v[182:185], v[186:189], a[164:167], v[182:185]\n"
      "ds_read_b128 v[120:123], v2 offset:21504\n"
      "v_fmac_f32 v77, v137, v137\n"
      "v_mfma_f32_16x16x32_bf16 v[146:149], v[202:205], a[168:171], 0\n"
      "s_nop 0\n"
      "v_permlane16_swap_b32_e64 v90, v77\n"
      "v_mfma_f32_16x16x32_bf16 v[146:149], v[150:153], a[172:175], v[146:149]\n"
      "v_add_f32 v90, v90, v77\n"
      "v_mov_b32 v77, v90\n"
      "v_mfma_f32_16x16x32_bf16 v[146:149], v[210:213], a[176:179], v[146:149]\n"
      "s_nop 0\n"
      "v_permlane32_swap_b32 v90, v77\n"
      "v_mfma_f32_16x16x32_bf16 v[146:149], v[130:133], a[180:183], v[146:149]\n"
      "v_add_f32 v90, v90, v77\n"
      "v_cvt_pk_bf16_f32 v76, v182, v183\n"
      "v_mfma_f32_16x16x32_bf16 v[146:149], v[162:165], a[184:187], v[146:149]\n"
      "v_rsq_f32 v90, v90\n"
      "v_mfma_f32_16x16x32_bf16 v[146:149], v[186:189], a[188:191], v[146:149]\n"
      "ds_read_b128 v[138:141], v2 offset:18432\n"
      "v_mov_b32_e64 v77, v90\n"
      "v_mfma_f32_16x16x32_bf16 v[180:183], v[202:205], a[192:195], 0\n"
      "ds_read_b128 v[202:205], v2 offset:24576\n"
      "v_cvt_pk_bf16_f32 v142, v142, v143\n"
      "v_mfma_f32_16x16x32_bf16 v[180:183], v[150:153], a[196:199], v[180:183]\n"
      "v_permlane16_swap_b32_e64 v90, v77\n"
      "v_mfma_f32_16x16x32_bf16 v[180:183], v[210:213], a[200:203], v[180:183]\n"
      "v_mul_f32 v192, v77, v192\n"
      "v_mul_f32 v158, v77, v158\n"
      "v_mfma_f32_16x16x32_bf16 v[180:183], v[130:133], a[204:207], v[180:183]\n"
      "v_mul_f32 v134, v77, v134\n"
      "v_mul_f32 v161, v77, v161\n"
      "v_mfma_f32_16x16x32_bf16 v[180:183], v[162:165], a[208:211], v[180:183]\n"
      "v_mul_f32 v135, v77, v135\n"
      "v_mul_f32 v193, v77, v193\n"
      "v_mfma_f32_16x16x32_bf16 v[180:183], v[186:189], a[212:215], v[180:183]\n"
      "s_waitcnt lgkmcnt(1)\n"
      "v_mul_f32 v159, v77, v159\n"
      "v_cvt_pk_bf16_f32 v167, v168, v169\n"
      "v_mfma_f32_16x16x32_bf16 v[174:177], a[216:219], v[138:141], 0\n"
      "ds_read_b128 v[186:189], v10 offset:32\n"
      "v_cvt_pk_bf16_f32 v134, v134, v135\n"
      "v_mfma_f32_16x16x32_bf16 v[174:177], a[220:223], v[120:123], v[174:177]\n"
      "s_waitcnt lgkmcnt(1)\n"
      "v_mul_f32 v191, v77, v191\n"
      "v_mul_f32 v160, v77, v160\n"
      "v_mfma_f32_16x16x32_bf16 v[174:177], a[224:227], v[202:205], v[174:177]\n"
      "v_mul_f32 v90, s50, v90\n"
      "v_mul_f32 v114, v90, v114\n"
      "v_mfma_f32_16x16x32_bf16 v[214:217], a[240:243], v[138:141], 0\n"
      "ds_read_b128 v[210:213], v2 offset:27648\n"
      "v_mul_f32 v137, v77, v137\n"
      "v_mfma_f32_16x16x32_bf16 v[214:217], a[244:247], v[120:123], v[214:217]\n"
      "v_cvt_pk_bf16_f32 v158, v158, v159\n"
      "v_cvt_pk_bf16_f32 v159, v160, v161\n"
      "v_mfma_f32_16x16x32_bf16 v[214:217], a[248:251], v[202:205], v[214:217]\n"
      "v_cvt_pk_bf16_f32 v81, v148, v149\n"
      "v_mul_f32 v112, v90, v112\n"
      "v_mfma_f32_16x16x32_bf16 v[148:151], v[232:235], v[138:141], 0\n"
      "v_mul_f32 v190, v77, v190\n"
      "v_cvt_pk_bf16_f32 v160, v190, v191\n"
      "v_mfma_f32_16x16x32_bf16 v[148:151], v[236:239], v[120:123], v[148:151]\n"
      "ds_read_b128 v[120:123], v2 offset:33792\n"
      "v_cvt_pk_bf16_f32 v84, v180, v181\n"
      "v_mfma_f32_16x16x32_bf16 v[148:151], v[240:243], v[202:205], v[148:151]\n"
      "ds_read_u16 v82, v11 offset:32\n"
      "v_cvt_pk_bf16_f32 v80, v146, v147\n"
      "v_mfma_f32_16x16x32_bf16 v[162:165], v[100:103], v[198:201], v[40:43]\n"
      "v_cvt_pk_bf16_f32 v161, v192, v193\n"
      "v_mul_f32_e64 v136, v77, v136\n"
      "v_mfma_f32_16x16x32_bf16 v[202:205], v[206:209], v[198:201], v[44:47]\n"
      "s_waitcnt lgkmcnt(2)\n"
      "v_mul_f32 v220, v90, v220\n"
      "v_mul_f32_e64 v221, v90, v221\n"
      "v_mfma_f32_16x16x32_bf16 v[190:193], v[158:161], v[198:201], v[48:51]\n"
      "ds_read_b128 v[130:133], v2 offset:30720\n"
      "v_cvt_pk_bf16_f32 v135, v136, v137\n"
      "v_mfma_f32_16x16x32_bf16 v[136:139], v[186:189], v[198:201], v[24:27]\n"
      "v_mul_f32_e64 v113, v90, v113\n"
      "v_cvt_pk_bf16_f32 v77, v184, v185\n"
      "v_mfma_f32_16x16x16_bf16 v[162:165], v[154:155], v[170:171], v[162:165]\n"
      "v_mul_f32 v115, v90, v115\n"
      "v_mul_f32 v222, v90, v222\n"
      "v_mul_f32_e64 v223, v90, v223\n"
      "v_mfma_f32_16x16x16_bf16 v[202:205], v[166:167], v[170:171], v[202:205]\n"
      "v_cvt_pk_bf16_f32 v196, v178, v179\n"
      "v_cvt_pk_bf16_f32 v220, v220, v221\n"
      "v_cvt_pk_bf16_f32 v85, v182, v183\n"
      "v_mfma_f32_16x16x16_bf16 v[190:193], v[134:135], v[170:171], v[190:193]\n"
      "v_cvt_pk_bf16_f32 v112, v112, v113\n"
      "v_cvt_pk_bf16_f32 v143, v144, v145\n"
      "v_max3_f32 v145, v162, v163, v164\n"
      "v_mfma_f32_16x16x16_bf16 v[136:139], v[218:219], v[170:171], v[136:139]\n"
      "v_max3_f32 v145, v145, v204, v205\n"
      "v_cvt_pk_bf16_f32 v221, v222, v223\n"
      "v_max3_f32 v170, v165, v202, v203\n"
      "v_mfma_f32_16x16x32_bf16 v[174:177], a[228:231], v[210:213], v[174:177]\n"
      "s_waitcnt lgkmcnt(0)\n"
      "v_max3_f32 v170, v170, v190, v191\n"
      "v_max3_f32 v145, v145, v192, v193\n"
      "v_mfma_f32_16x16x32_bf16 v[174:177], a[232:235], v[130:133], v[174:177]\n"
      "v_max3_f32 v145, v145, v170, v136\n"
      "v_mov_b32 v170, v145\n"
      "v_mfma_f32_16x16x32_bf16 v[174:177], a[236:239], v[120:123], v[174:177]\n"
      "s_nop 0\n"
      "v_permlane16_swap_b32 v145, v170\n"
      "v_mfma_f32_16x16x32_bf16 v[214:217], a[252:255], v[210:213], v[214:217]\n"
      "v_max_f32 v145, v145, v170\n"
      "v_mov_b32 v170, v145\n"
      "v_mfma_f32_16x16x32_bf16 v[214:217], v[224:227], v[130:133], v[214:217]\n"
      "s_nop 0\n"
      "v_permlane32_swap_b32 v145, v170\n"
      "v_mfma_f32_16x16x32_bf16 v[214:217], v[228:231], v[120:123], v[214:217]\n"
      "v_max_f32 v145, v145, v170\n"
      "v_sub_f32 v163, v163, v145\n"
      "v_mfma_f32_16x16x32_bf16 v[148:151], v[244:247], v[210:213], v[148:151]\n"
      "ds_read_b128 v[168:171], v2 offset:18688\n"
      "v_sub_f32 v162, v162, v145\n"
      "v_mfma_f32_16x16x32_bf16 v[148:151], v[248:251], v[130:133], v[148:151]\n"
      "v_exp_f32 v162, v162\n"
      "v_mfma_f32_16x16x32_bf16 v[148:151], v[252:255], v[120:123], v[148:151]\n"
      "v_exp_f32_e64 v163, v163\n"
      "v_mfma_f32_16x16x32_bf16 v[178:181], v[100:103], v[108:111], v[52:55]\n"
      "ds_read_b128 v[130:133], v2 offset:24832\n"
      "v_sub_f32 v136, v136, v145\n"
      "s_waitcnt vmcnt(1)\n"
      "v_or3_b32 v126, v126, v127, v128\n"
      "v_exp_f32_e64 v136, v136\n"
      "v_pk_mul_f32 v[116:117], v[116:117], v[90:91] op_sel_hi:[1,0]\n"
      "v_pk_add_f32 v[164:165], v[164:165], v[144:145] op_sel:[0,1] neg_lo:[0,1] neg_hi:[0,1]\n"
      "v_pk_add_f32 v[204:205], v[204:205], v[144:145] op_sel:[0,1] neg_lo:[0,1] neg_hi:[0,1]\n"
      "v_pk_mul_f32 v[118:119], v[118:119], v[90:91] op_sel_hi:[1,0]\n"
      "v_pk_add_f32 v[190:191], v[190:191], v[144:145] op_sel:[0,1] neg_lo:[0,1] neg_hi:[0,1]\n"
      "v_pk_add_f32 v[202:203], v[202:203], v[144:145] op_sel:[0,1] neg_lo:[0,1] neg_hi:[0,1]\n"
      "v_pk_add_f32 v[192:193], v[192:193], v[144:145] op_sel:[0,1] neg_lo:[0,1] neg_hi:[0,1]\n"
      "v_mfma_f32_16x16x32_bf16 v[198:201], v[206:209], v[108:111], v[56:59]\n"
      "ds_read_b128 v[144:147], v2 offset:21760\n"
      "v_cvt_pk_bf16_f32 v223, v118, v119\n"
      "v_mfma_f32_16x16x32_bf16 v[182:185], v[158:161], v[108:111], v[60:63]\n"
      "v_exp_f32 v192, v192\n"
      "v_mfma_f32_16x16x32_bf16 v[138:141], v[186:189], v[108:111], v[26:29]\n"
      "s_waitcnt lgkmcnt(2)\n"
      "v_exp_f32_e64 v190, v190\n"
      "v_mfma_f32_16x16x16_bf16 v[178:181], v[154:155], v[142:143], v[178:181]\n"
      "v_cvt_pk_bf16_f32 v90, v136, 0\n"
      "v_exp_f32 v205, v205\n"
      "v_mfma_f32_16x16x16_bf16 v[198:201], v[166:167], v[142:143], v[198:201]\n"
      "v_exp_f32 v164, v164\n"
      "v_cvt_pk_bf16_f32 v113, v114, v115\n"
      "v_mfma_f32_16x16x16_bf16 v[182:185], v[134:135], v[142:143], v[182:185]\n"
      "v_exp_f32_e64 v203, v203\n"
      "v_cvt_pk_bf16_f32 v162, v162, v163\n"
      "v_mfma_f32_16x16x16_bf16 v[138:141], v[218:219], v[142:143], v[138:141]\n"
      "v_cvt_pk_bf16_f32 v222, v116, v117\n"
      "v_exp_f32 v165, v165\n"
      "v_mfma_f32_16x16x32_bf16 v[210:213], a[216:219], v[168:171], 0\n"
      "s_waitcnt lgkmcnt(0)\n"
      "v_cvt_pk_bf16_f32 v163, v164, v165\n"
      "v_max3_f32 v127, v178, v179, v180\n"
      "v_mfma_f32_16x16x32_bf16 v[210:213], a[220:223], v[144:147], v[210:213]\n"
      "v_exp_f32 v202, v202\n"
      "v_mfma_f32_16x16x32_bf16 v[210:213], a[224:227], v[130:133], v[210:213]\n"
      "v_exp_f32 v204, v204\n"
      "v_mfma_f32_16x16x32_bf16 v[122:125], a[240:243], v[168:171], 0\n"
      "v_cvt_pk_bf16_f32 v164, v202, v203\n"
      "v_cvt_pk_bf16_f32 v165, v204, v205\n"
      "v_mfma_f32_16x16x32_bf16 v[122:125], a[244:247], v[144:147], v[122:125]\n"
      "v_exp_f32 v193, v193\n"
      "v_mfma_f32_16x16x32_bf16 v[122:125], a[248:251], v[130:133], v[122:125]\n"
      "v_exp_f32 v191, v191\n"
      "v_mfma_f32_16x16x32_bf16 v[118:121], v[232:235], v[168:171], 0\n"
      "v_max3_f32 v170, v181, v198, v199\n"
      "v_max3_f32 v170, v170, v182, v183\n"
      "v_mfma_f32_16x16x32_bf16 v[118:121], v[236:239], v[144:147], v[118:121]\n"
      "v_cvt_pk_bf16_f32 v88, v190, v191\n"
      "v_max3_f32 v127, v127, v200, v201\n"
      "v_mfma_f32_16x16x32_bf16 v[118:121], v[240:243], v[130:133], v[118:121]\n"
      "v_max3_f32 v127, v127, v184, v185\n"
      "v_cvt_pk_bf16_f32 v89, v192, v193\n"
      "v_mfma_f32_16x16x32_bf16 v[130:133], v[32:35], v[162:165], 0\n"
      "ds_read_u16 v86, v11 offset:64\n"
      "v_fma_f32 v142, v174, v174, s52\n"
      "v_mfma_f32_16x16x32_bf16 v[130:133], v[32:35], v[88:91], v[130:133]\n"
      "v_max3_f32 v127, v127, v170, v138\n"
      "v_mov_b32 v143, v127\n"
      "v_mfma_f32_16x16x32_bf16 v[168:171], v[194:197], v[162:165], 0\n"
      "s_nop 0\n"
      "v_permlane16_swap_b32 v127, v143\n"
      "v_mfma_f32_16x16x32_bf16 v[168:171], v[76:79], v[88:91], v[168:171]\n"
      "ds_read_b128 v[202:205], v2 offset:27904\n"
      "s_waitcnt vmcnt(0)\n"
      "v_or3_b32 v126, v126, v129, v93\n"
      "v_mfma_f32_16x16x32_bf16 v[144:147], v[96:99], v[162:165], 0\n"
      "ds_write_b32 v31, v126 offset:4608\n"
      "v_mfma_f32_16x16x32_bf16 v[144:147], v[80:83], v[88:91], v[144:147]\n"
      "v_max_f32 v127, v127, v143\n"
      "v_mov_b32 v143, v127\n"
      "v_mfma_f32_16x16x32_bf16 v[190:193], v[104:107], v[162:165], 0\n"
      "s_waitcnt lgkmcnt(2)\n"
      "v_rcp_f32 v130, v130\n"
      "v_mfma_f32_16x16x32_bf16 v[190:193], v[84:87], v[88:91], v[190:193]\n"
      "ds_read_b128 v[162:165], v2 offset:30976\n"
      "v_permlane32_swap_b32 v127, v143\n"
      "v_max_f32 v127, v127, v143\n"
      "v_mul_f32_e64 v143, v175, v175\n"
      "v_pk_add_f32 v[178:179], v[178:179], v[126:127] op_sel:[0,1] neg_lo:[0,1] neg_hi:[0,1]\n"
      "v_pk_mul_f32 v[144:145], v[144:145], v[130:131] op_sel_hi:[1,0]\n"
      "v_pk_mul_f32 v[170:171], v[170:171], v[130:131] op_sel_hi:[1,0]\n"
      "v_pk_mul_f32 v[168:169], v[168:169], v[130:131] op_sel_hi:[1,0]\n"
      "v_sub_f32_e64 v138, v138, v127\n"
      "v_pk_mul_f32 v[146:147], v[146:147], v[130:131] op_sel_hi:[1,0]\n"
      "v_cvt_pk_bf16_f32 v168, v168, v169\n"
      "v_pk_mul_f32 v[190:191], v[190:191], v[130:131] op_sel_hi:[1,0]\n"
      "v_pk_fma_f32 v[142:143], v[176:177], v[176:177], v[142:143]\n"
      "v_cvt_pk_bf16_f32 v169, v170, v171\n"
      "v_pk_add_f32 v[198:199], v[198:199], v[126:127] op_sel:[0,1] neg_lo:[0,1] neg_hi:[0,1]\n"
      "v_pk_add_f32 v[180:181], v[180:181], v[126:127] op_sel:[0,1] neg_lo:[0,1] neg_hi:[0,1]\n"
      "v_pk_mul_f32 v[192:193], v[192:193], v[130:131] op_sel_hi:[1,0]\n"
      "v_pk_add_f32 v[182:183], v[182:183], v[126:127] op_sel:[0,1] neg_lo:[0,1] neg_hi:[0,1]\n"
      "v_pk_add_f32 v[200:201], v[200:201], v[126:127] op_sel:[0,1] neg_lo:[0,1] neg_hi:[0,1]\n"
      "v_cvt_pk_bf16_f32 v171, v146, v147\n"
      "v_pk_add_f32 v[184:185], v[184:185], v[126:127] op_sel:[0,1] neg_lo:[0,1] neg_hi:[0,1]\n"
      "v_pk_fma_f32 v[142:143], v[214:215], v[214:215], v[142:143]\n"
      "v_mfma_f32_16x16x32_bf16 v[130:133], v[100:103], v[220:223], v[64:67]\n"
      "v_cvt_pk_bf16_f32 v190, v190, v191\n"
      "v_cvt_pk_bf16_f32 v170, v144, v145\n"
      "v_cvt_pk_bf16_f32 v191, v192, v193\n"
      "v_mfma_f32_16x16x32_bf16 v[126:129], v[206:209], v[220:223], v[68:71]\n"
      "v_exp_f32_e64 v180, v180\n"
      "v_mfma_f32_16x16x32_bf16 v[144:147], v[158:161], v[220:223], v[72:75]\n"
      "s_waitcnt lgkmcnt(0)\n"
      "v_exp_f32 v185, v185\n"
      "v_mfma_f32_16x16x32_bf16 v[108:111], v[186:189], v[220:223], v[28:31]\n"
      "ds_read_b128 v[220:223], v2 offset:34048\n"
      "v_exp_f32 v184, v184\n"
      "v_mfma_f32_16x16x16_bf16 v[130:133], v[154:155], v[112:113], v[130:133]\n"
      "v_exp_f32 v183, v183\n"
      "v_exp_f32 v178, v178\n"
      "v_mfma_f32_16x16x16_bf16 v[126:129], v[166:167], v[112:113], v[126:129]\n"
      "v_exp_f32 v200, v200\n"
      "v_cvt_pk_bf16_f32 v93, v184, v185\n"
      "v_mfma_f32_16x16x16_bf16 v[144:147], v[134:135], v[112:113], v[144:147]\n"
      "v_exp_f32 v179, v179\n"
      "v_exp_f32 v182, v182\n"
      "v_mfma_f32_16x16x16_bf16 v[108:111], v[218:219], v[112:113], v[108:111]\n"
      "v_cvt_pk_bf16_f32 v178, v178, v179\n"
      "v_cvt_pk_bf16_f32 v92, v182, v183\n"
      "v_max3_f32 v136, v130, v131, v132\n"
      "v_mfma_f32_16x16x32_bf16 v[210:213], a[228:231], v[202:205], v[210:213]\n"
      "v_exp_f32 v138, v138\n"
      "v_mfma_f32_16x16x32_bf16 v[210:213], a[232:235], v[162:165], v[210:213]\n"
      "s_waitcnt lgkmcnt(0)\n"
      "v_exp_f32 v201, v201\n"
      "v_mfma_f32_16x16x32_bf16 v[210:213], a[236:239], v[220:223], v[210:213]\n"
      "v_exp_f32 v199, v199\n"
      "v_mfma_f32_16x16x32_bf16 v[122:125], a[252:255], v[202:205], v[122:125]\n"
      "v_exp_f32 v198, v198\n"
      "v_mfma_f32_16x16x32_bf16 v[122:125], v[224:227], v[162:165], v[122:125]\n"
      "v_exp_f32 v181, v181\n"
      "v_mfma_f32_16x16x32_bf16 v[122:125], v[228:231], v[220:223], v[122:125]\n"
      "v_cvt_pk_bf16_f32 v179, v180, v181\n"
      "v_cvt_pk_bf16_f32 v94, v138, 0\n"
      "v_mfma_f32_16x16x32_bf16 v[118:121], v[244:247], v[202:205], v[118:121]\n"
      "v_max3_f32 v136, v136, v128, v129\n"
      "v_cvt_pk_bf16_f32 v180, v198, v199\n"
      "v_mfma_f32_16x16x32_bf16 v[118:121], v[248:251], v[162:165], v[118:121]\n"
      "v_cvt_pk_bf16_f32 v181, v200, v201\n"
      "v_max3_f32 v165, v133, v126, v127\n"
      "v_mfma_f32_16x16x32_bf16 v[118:121], v[252:255], v[220:223], v[118:121]\n"
      "ds_read_b128 v[220:223], v10\n"
      "v_max3_f32 v165, v165, v144, v145\n"
      "v_mfma_f32_16x16x32_bf16 v[112:115], v[32:35], v[178:181], 0\n"
      "ds_read_b128 v[200:203], v7 offset:18432\n"
      "v_max3_f32 v136, v136, v146, v147\n"
      "v_mfma_f32_16x16x32_bf16 v[112:115], v[32:35], v[92:95], v[112:115]\n"
      "v_max3_f32 v136, v136, v165, v108\n"
      "v_mov_b32 v138, v136\n"
      "v_mfma_f32_16x16x32_bf16 v[162:165], v[194:197], v[178:181], 0\n"
      "s_nop 0\n"
      "v_permlane16_swap_b32_e64 v136, v138\n"
      "v_mfma_f32_16x16x32_bf16 v[162:165], v[76:79], v[92:95], v[162:165]\n"
      "ds_read_b64 v[204:205], v10 offset:16\n"
      "v_max_f32_e64 v136, v136, v138\n"
      "v_mfma_f32_16x16x32_bf16 v[182:185], v[96:99], v[178:181], 0\n"
      "ds_write_b128 v3, v[168:171]\n"
      "v_mfma_f32_16x16x32_bf16 v[182:185], v[80:83], v[92:95], v[182:185]\n"
      "ds_read_b128 v[170:173], v2 offset:22016\n"
      "v_mov_b32 v138, v136\n"
      "v_mfma_f32_16x16x32_bf16 v[114:117], v[104:107], v[178:181], 0\n"
      "s_nop 0\n"
      "v_permlane32_swap_b32 v136, v138\n"
      "v_mfma_f32_16x16x32_bf16 v[114:117], v[84:87], v[92:95], v[114:117]\n"
      "s_waitcnt lgkmcnt(3)\n"
      "v_rcp_f32_e64 v112, v112\n"
      "v_mfma_f32_16x16x32_bf16 v[178:181], v[220:223], v[100:103], v[30:33]\n"
      "ds_read_b128 v[100:103], v2 offset:18944\n"
      "v_max_f32_e64 v136, v136, v138\n"
      "v_mfma_f32_16x16x32_bf16 v[138:141], v[220:223], v[206:209], v[30:33]\n"
      "buffer_store_dwordx4 v[200:203], v20, s[24:27], s46 offen sc1\n"
      "v_pk_add_f32 v[144:145], v[144:145], v[136:137] op_sel_hi:[1,0] neg_lo:[0,1] neg_hi:[0,1]\n"
      "v_pk_add_f32 v[132:133], v[132:133], v[136:137] op_sel_hi:[1,0] neg_lo:[0,1] neg_hi:[0,1]\n"
      "v_pk_add_f32 v[126:127], v[126:127], v[136:137] op_sel_hi:[1,0] neg_lo:[0,1] neg_hi:[0,1]\n"
      "v_pk_mul_f32 v[164:165], v[164:165], v[112:113] op_sel_hi:[1,0]\n"
      "v_pk_add_f32 v[130:131], v[130:131], v[136:137] op_sel_hi:[1,0] neg_lo:[0,1] neg_hi:[0,1]\n"
      "v_pk_mul_f32 v[162:163], v[162:163], v[112:113] op_sel_hi:[1,0]\n"
      "v_pk_add_f32 v[146:147], v[146:147], v[136:137] op_sel_hi:[1,0] neg_lo:[0,1] neg_hi:[0,1]\n"
      "v_pk_mul_f32 v[116:117], v[116:117], v[112:113] op_sel_hi:[1,0]\n"
      "v_pk_mul_f32 v[184:185], v[184:185], v[112:113] op_sel_hi:[1,0]\n"
      "v_pk_fma_f32 v[142:143], v[216:217], v[216:217], v[142:143]\n"
      "v_pk_add_f32 v[128:129], v[128:129], v[136:137] op_sel_hi:[1,0] neg_lo:[0,1] neg_hi:[0,1]\n"
      "v_pk_mul_f32 v[182:183], v[182:183], v[112:113] op_sel_hi:[1,0]\n"
      "v_mfma_f32_16x16x32_bf16 v[200:203], v[220:223], v[158:161], v[30:33]\n"
      "s_waitcnt lgkmcnt(0)\n"
      "v_exp_f32 v144, v144\n"
      "v_mfma_f32_16x16x32_bf16 v[158:161], v[220:223], v[186:189], v[30:33]\n"
      "ds_read_b128 v[186:189], v7 offset:22528\n"
      "v_sub_f32_e64 v108, v108, v136\n"
      "v_mfma_f32_16x16x16_bf16 v[178:181], v[204:205], v[154:155], v[178:181]\n"
      "ds_read_b128 v[206:209], v2 offset:25088\n"
      "v_fma_f32 v136, v210, v210, s52\n"
      "v_mfma_f32_16x16x16_bf16 v[138:141], v[204:205], v[166:167], v[138:141]\n"
      "v_exp_f32 v130, v130\n"
      "v_cvt_pk_bf16_f32 v162, v162, v163\n"
      "v_mfma_f32_16x16x16_bf16 v[200:203], v[204:205], v[134:135], v[200:203]\n"
      "v_exp_f32 v145, v145\n"
      "v_exp_f32 v108, v108\n"
      "v_exp_f32 v131, v131\n"
      "v_mfma_f32_16x16x16_bf16 v[158:161], v[204:205], v[218:219], v[158:161]\n"
      "v_mul_f32_e64 v137, v211, v211\n"
      "v_cvt_pk_bf16_f32 v130, v130, v131\n"
      "v_exp_f32_e64 v127, v127\n"
      "v_mfma_f32_16x16x32_bf16 v[220:223], a[216:219], v[100:103], 0\n"
      "ds_read_b128 v[202:205], v2 offset:28160\n"
      "v_cndmask_b32 v178, v178, v138, s[58:59]\n"
      "v_exp_f32 v133, v133\n"
      "v_mfma_f32_16x16x32_bf16 v[220:223], a[220:223], v[170:173], v[220:223]\n"
      "s_waitcnt lgkmcnt(1)\n"
      "v_cndmask_b32 v178, v178, v200, s[60:61]\n"
      "v_cndmask_b32 v178, v178, v158, s[62:63]\n"
      "v_mfma_f32_16x16x32_bf16 v[220:223], a[224:227], v[206:209], v[220:223]\n"
      "buffer_store_dwordx4 v[186:189], v21, s[24:27], s46 offen sc1\n"
      "v_mfma_f32_16x16x32_bf16 v[166:169], a[240:243], v[100:103], 0\n"
      "ds_read_b128 v[158:161], v7 offset:26624\n"
      "v_exp_f32 v128, v128\n"
      "v_mfma_f32_16x16x32_bf16 v[166:169], a[244:247], v[170:173], v[166:169]\n"
      "ds_read_b128 v[138:141], v2 offset:31232\n"
      "v_exp_f32 v146, v146\n"
      "v_max_f32_dpp v200, v178, v178 row_ror:8 row_mask:0xf bank_mask:0xf\n"
      "v_cvt_pk_bf16_f32 v163, v164, v165\n"
      "v_pk_fma_f32 v[142:143], v[148:149], v[148:149], v[142:143]\n"
      "v_pk_fma_f32 v[136:137], v[212:213], v[212:213], v[136:137]\n"
      "v_max_f32_dpp v200, v200, v200 row_ror:4 row_mask:0xf bank_mask:0xf\n"
      "v_cvt_pk_bf16_f32 v165, v184, v185\n"
      "v_pk_fma_f32 v[136:137], v[122:123], v[122:123], v[136:137]\n"
      "v_max_f32_dpp v200, v200, v200 row_ror:2 row_mask:0xf bank_mask:0xf\n"
      "v_pk_fma_f32 v[136:137], v[124:125], v[124:125], v[136:137]\n"
      "v_cvt_pk_bf16_f32 v164, v182, v183\n"
      "v_pk_mul_f32 v[114:115], v[114:115], v[112:113] op_sel_hi:[1,0]\n"
      "v_mfma_f32_16x16x32_bf16 v[166:169], a[248:251], v[206:209], v[166:169]\n"
      "ds_read_b128 v[180:183], v2 offset:34304\n"
      "v_exp_f32 v147, v147\n"
      "v_mfma_f32_16x16x32_bf16 v[186:189], v[232:235], v[100:103], 0\n"
      "v_exp_f32_e64 v132, v132\n"
      "v_mfma_f32_16x16x32_bf16 v[186:189], v[236:239], v[170:173], v[186:189]\n"
      "s_waitcnt lgkmcnt(1)\n"
      "v_max_f32_dpp v200, v200, v200 row_ror:1 row_mask:0xf bank_mask:0xf\n"
      "v_exp_f32_e64 v126, v126\n"
      "v_mfma_f32_16x16x32_bf16 v[186:189], v[240:243], v[206:209], v[186:189]\n"
      "buffer_store_dwordx4 v[158:161], v22, s[24:27], s46 offen sc1\n"
      "v_mfma_f32_16x16x32_bf16 v[220:223], a[228:231], v[202:205], v[220:223]\n"
      "v_max_f32_dpp v200, v200, v200 row_bcast:15 row_mask:0xa bank_mask:0xf\n"
      "v_cvt_pk_bf16_f32 v131, v132, v133\n"
      "v_mfma_f32_16x16x32_bf16 v[220:223], a[232:235], v[138:141], v[220:223]\n"
      "s_waitcnt lgkmcnt(0)\n"
      "v_max_f32_dpp v200, v200, v200 row_bcast:31 row_mask:0xc bank_mask:0xf\n"
      "v_exp_f32 v129, v129\n"
      "v_mfma_f32_16x16x32_bf16 v[220:223], a[236:239], v[180:183], v[220:223]\n"
      "v_readlane_b32 s72, v200, 63\n"
      "v_cvt_pk_bf16_f32 v133, v128, v129\n"
      "v_mfma_f32_16x16x32_bf16 v[166:169], a[252:255], v[202:205], v[166:169]\n"
      "ds_read_b128 v[170:173], v7 offset:30720\n"
      "v_cvt_pk_bf16_f32 v132, v126, v127\n"
      "v_subrev_f32 v178, s72, v178\n"
      "v_mfma_f32_16x16x32_bf16 v[166:169], v[224:227], v[138:141], v[166:169]\n"
      "v_cvt_pk_bf16_f32 v90, v108, 0\n"
      "v_fma_f32 v126, v220, v220, s52\n"
      "v_mfma_f32_16x16x32_bf16 v[166:169], v[228:231], v[180:183], v[166:169]\n"
      "ds_write_b128 v3, v[162:165] offset:256\n"
      "v_pk_fma_f32 v[136:137], v[118:119], v[118:119], v[136:137]\n"
      "v_pk_fma_f32 v[142:143], v[150:151], v[150:151], v[142:143]\n"
      "v_mul_f32 v127, v221, v221\n"
      "v_mfma_f32_16x16x32_bf16 v[186:189], v[244:247], v[202:205], v[186:189]\n"
      "v_exp_f32 v178, v178\n"
      "v_mfma_f32_16x16x32_bf16 v[186:189], v[248:251], v[138:141], v[186:189]\n"
      "v_cvt_pk_bf16_f32 v88, v144, v145\n"
      "v_cvt_pk_bf16_f32 v89, v146, v147\n"
      "v_mfma_f32_16x16x32_bf16 v[186:189], v[252:255], v[180:183], v[186:189]\n"
      "s_waitcnt lgkmcnt(1)\n"
      "v_cvt_pk_bf16_f32 v114, v114, v115\n"
      "v_cvt_pk_bf16_f32 v178, v178, v178\n"
      "v_mfma_f32_16x16x32_bf16 v[160:163], v[32:35], v[130:133], 0\n"
      "ds_write_b16 v13, v178\n"
      "v_mfma_f32_16x16x32_bf16 v[160:163], v[32:35], v[88:91], v[160:163]\n"
      "buffer_store_dwordx4 v[170:173], v23, s[24:27], s46 offen sc1\n"
      "v_pk_fma_f32 v[126:127], v[222:223], v[222:223], v[126:127]\n"
      "v_pk_fma_f32 v[126:127], v[166:167], v[166:167], v[126:127]\n"
      "v_pk_fma_f32 v[136:137], v[120:121], v[120:121], v[136:137]\n"
      "v_mfma_f32_16x16x32_bf16 v[200:203], v[194:197], v[130:133], 0\n"
      "ds_write_b64 v4, v[190:191]\n"
      "v_mfma_f32_16x16x32_bf16 v[200:203], v[76:79], v[88:91], v[200:203]\n"
      "s_nop 0\n"
      "v_rcp_f32 v160, v160\n"
      "v_mfma_f32_16x16x32_bf16 v[152:155], v[96:99], v[130:133], 0\n"
      "ds_read_b128 v[110:113], v7 offset:34816\n"
      "v_add_f32 v142, v142, v143\n"
      "v_mfma_f32_16x16x32_bf16 v[152:155], v[80:83], v[88:91], v[152:155]\n"
      "ds_read_b128 v[178:181], v14\n"
      "v_cvt_pk_bf16_f32 v115, v116, v117\n"
      "v_mfma_f32_16x16x32_bf16 v[190:193], v[104:107], v[130:133], 0\n"
      "ds_read_b128 v[100:103], v14 offset:64\n"
      "v_add_f32 v136, v136, v137\n"
      "v_mfma_f32_16x16x32_bf16 v[190:193], v[84:87], v[88:91], v[190:193]\n"
      "ds_write_b64 v4, v[114:115] offset:256\n"
      "v_permlane16_swap_b32_e64 v142, v136\n"
      "v_pk_mul_f32 v[200:201], v[200:201], v[160:161] op_sel_hi:[1,0]\n"
      "v_pk_fma_f32 v[126:127], v[168:169], v[168:169], v[126:127]\n"
      "v_pk_mul_f32 v[202:203], v[202:203], v[160:161] op_sel_hi:[1,0]\n"
      "v_pk_mul_f32 v[152:153], v[152:153], v[160:161] op_sel_hi:[1,0]\n"
      "v_cvt_pk_bf16_f32 v200, v200, v201\n"
      "v_add_f32_e64 v142, v142, v136\n"
      "v_pk_fma_f32 v[126:127], v[186:187], v[186:187], v[126:127]\n"
      "v_cvt_pk_bf16_f32 v201, v202, v203\n"
      "v_pk_mul_f32 v[154:155], v[154:155], v[160:161] op_sel_hi:[1,0]\n"
      "v_pk_fma_f32 v[126:127], v[188:189], v[188:189], v[126:127]\n"
      "v_cvt_pk_bf16_f32 v202, v152, v153\n"
      "v_add_f32 v126, v126, v127\n"
      "v_cvt_pk_bf16_f32 v203, v154, v155\n"
      "v_mov_b32_e64 v136, v126\n"
      "s_add_u32 s42, s42, 1\n"
      "ds_write_b128 v3, v[200:203] offset:512\n"
      "v_permlane16_swap_b32 v126, v136\n"
      "v_add_f32 v126, v126, v136\n"
      "v_pk_mul_f32 v[190:191], v[190:191], v[160:161] op_sel_hi:[1,0]\n"
      "v_pk_mul_f32 v[192:193], v[192:193], v[160:161] op_sel_hi:[1,0]\n"
      "v_permlane32_swap_b32 v142, v126\n"
      "v_add_f32 v142, v142, v126\n"
      "s_and_b32 s86, s86, -4\n"
      "ds_write_b32 v31, v142\n"
      "v_cvt_pk_bf16_f32 v190, v190, v191\n"
      "v_cvt_pk_bf16_f32 v191, v192, v193\n"
      "s_waitcnt lgkmcnt(2)\n"
      "v_mfma_f32_16x16x32_bf16 v[160:163], v[178:181], v[32:35], 0\n"
      "ds_write_b64 v4, v[190:191] offset:512\n"
      "s_cmp_ge_u32 s95, 0x40000000\n"
      "s_cselect_b64 vcc, -1, s[56:57]\n"
      "s_and_b32 s5, s84, s8\n"
      "v_mfma_f32_16x16x32_bf16 v[160:163], v[100:103], v[32:35], v[160:163]\n"
      "buffer_store_dwordx4 v[110:113], v25, s[24:27], s46 offen sc1\n"
      "s_waitcnt lgkmcnt(0)\n"
      "s_barrier\n"
      "s_cbranch_vccz L_max1_%=\n"
      "L_cold_%=:\n"
      "L_disp1_%=:\n"
      "s_cmp_lt_u32 s95, 0x40000000\n"
      "s_cbranch_scc1 L_nofix_11_%=\n"
      "s_cmp_eq_u32 s95, -2\n"
      "s_cbranch_scc1 L_tail1_%=\n"
      "s_nop 2\n"
      "s_cmp_eq_u32 s84, 0x20000000\n"
      "s_cselect_b32 s66, 0, s66\n"
      "s_cselect_b32 s48, 0, s48\n"
      "s_lshr_b32 s92, s84, 30\n"
      "s_or_b32 s92, s92, s66\n"
      "s_cbranch_scc0 L_nf_12_%=\n"
      "s_bitcmp1_b32 s84, 31\n"
      "s_cselect_b32 s75, 100270464, 0\n"
      "s_bitcmp1_b32 s84, 30\n"
      "s_cselect_b32 s76, 196224, 0\n"
      "s_and_b32 s74, s84, 0x3fffffff\n"
      "s_cmp_ge_u32 s84, 0x40000000\n"
      "s_cselect_b32 s74, s74, 0\n"
      "s_cselect_b32 s84, 0, s84\n"
      "s_sub_u32 s74, s74, s66\n"
      "s_add_u32 s66, s66, s74\n"
      "s_sub_u32 s75, s75, s67\n"
      "s_add_u32 s67, s67, s75\n"
      "s_sub_u32 s76, s76, s68\n"
      "s_add_u32 s68, s68, s76\n"
      "v_add_u32 v16, s74, v16\n"
      "v_add_u32 v17, s74, v17\n"
      "v_add_u32 v18, s74, v18\n"
      "v_add_u32 v19, s74, v19\n"
      "v_add_u32 v15, s74, v15\n"
      "s_cmp_eq_u32 s75, 0\n"
      "s_cbranch_scc1 L_ax_13_%=\n"
      "v_bfe_i32 v113, v27, 0, 1\n"
      "v_and_b32 v113, s75, v113\n"
      "v_sub_u32 v16, v16, v113\n"
      "v_bfe_i32 v113, v27, 2, 1\n"
      "v_and_b32 v113, s75, v113\n"
      "v_sub_u32 v17, v17, v113\n"
      "v_bfe_i32 v113, v27, 4, 1\n"
      "v_and_b32 v113, s75, v113\n"
      "v_sub_u32 v18, v18, v113\n"
      "v_bfe_i32 v113, v27, 6, 1\n"
      "v_and_b32 v113, s75, v113\n"
      "v_sub_u32 v19, v19, v113\n"
      "v_bfe_i32 v113, v27, 8, 1\n"
      "v_and_b32 v113, s75, v113\n"
      "v_sub_u32 v15, v15, v113\n"
      "L_ax_13_%=:\n"
      "s_cmp_eq_u32 s76, 0\n"
      "s_cbranch_scc1 L_ax_14_%=\n"
      "v_bfe_i32 v113, v27, 1, 1\n"
      "v_and_b32 v113, s76, v113\n"
      "v_sub_u32 v16, v16, v113\n"
      "v_bfe_i32 v113, v27, 3, 1\n"
      "v_and_b32 v113, s76, v113\n"
      "v_sub_u32 v17, v17, v113\n"
      "v_bfe_i32 v113, v27, 5, 1\n"
      "v_and_b32 v113, s76, v113\n"
      "v_sub_u32 v18, v18, v113\n"
      "v_bfe_i32 v113, v27, 7, 1\n"
      "v_and_b32 v113, s76, v113\n"
      "v_sub_u32 v19, v19, v113\n"
      "v_bfe_i32 v113, v27, 9, 1\n"
      "v_and_b32 v113, s76, v113\n"
      "v_sub_u32 v15, v15, v113\n"
      "L_ax_14_%=:\n"
      "s_or_b32 s48, s66, s69\n"
      "s_cmp_lg_u32 s48, 0\n"
      "s_cselect_b32 s48, 0x40000000, 0\n"
      "L_nf_12_%=:\n"
      "s_lshr_b32 s92, s85, 30\n"
      "s_or_b32 s92, s92, s69\n"
      "s_cbranch_scc0 L_nf_15_%=\n"
      "s_bitcmp1_b32 s85, 31\n"
      "s_cselect_b32 s75, 100270464, 0\n"
      "s_bitcmp1_b32 s85, 30\n"
      "s_cselect_b32 s76, 196224, 0\n"
      "s_and_b32 s74, s85, 0x3fffffff\n"
      "s_cmp_ge_u32 s85, 0x40000000\n"
      "s_cselect_b32 s74, s74, 0\n"
      "s_cselect_b32 s85, 0, s85\n"
      "s_sub_u32 s74, s74, s69\n"
      "s_add_u32 s69, s69, s74\n"
      "s_sub_u32 s75, s75, s70\n"
      "s_add_u32 s70, s70, s75\n"
      "s_sub_u32 s76, s76, s71\n"
      "s_add_u32 s71, s71, s76\n"
      "v_add_u32 v20, s74, v20\n"
      "v_add_u32 v21, s74, v21\n"
      "v_add_u32 v22, s74, v22\n"
      "v_add_u32 v23, s74, v23\n"
      "v_add_u32 v25, s74, v25\n"
      "s_cmp_eq_u32 s75, 0\n"
      "s_cbranch_scc1 L_ax_16_%=\n"
      "v_bfe_i32 v113, v27, 10, 1\n"
      "v_and_b32 v113, s75, v113\n"
      "v_sub_u32 v20, v20, v113\n"
      "v_bfe_i32 v113, v27, 12, 1\n"
      "v_and_b32 v113, s75, v113\n"
      "v_sub_u32 v21, v21, v113\n"
      "v_bfe_i32 v113, v27, 14, 1\n"
      "v_and_b32 v113, s75, v113\n"
      "v_sub_u32 v22, v22, v113\n"
      "v_bfe_i32 v113, v27, 16, 1\n"
      "v_and_b32 v113, s75, v113\n"
      "v_sub_u32 v23, v23, v113\n"
      "v_bfe_i32 v113, v27, 18, 1\n"
      "v_and_b32 v113, s75, v113\n"
      "v_sub_u32 v25, v25, v113\n"
      "L_ax_16_%=:\n"
      "s_cmp_eq_u32 s76, 0\n"
      "s_cbranch_scc1 L_ax_17_%=\n"
      "v_bfe_i32 v113, v27, 11, 1\n"
      "v_and_b32 v113, s76, v113\n"
      "v_sub_u32 v20, v20, v113\n"
      "v_bfe_i32 v113, v27, 13, 1\n"
      "v_and_b32 v113, s76, v113\n"
      "v_sub_u32 v21, v21, v113\n"
      "v_bfe_i32 v113, v27, 15, 1\n"
      "v_and_b32 v113, s76, v113\n"
      "v_sub_u32 v22, v22, v113\n"
      "v_bfe_i32 v113, v27, 17, 1\n"
      "v_and_b32 v113, s76, v113\n"
      "v_sub_u32 v23, v23, v113\n"
      "v_bfe_i32 v113, v27, 19, 1\n"
      "v_and_b32 v113, s76, v113\n"
      "v_sub_u32 v25, v25, v113\n"
      "L_ax_17_%=:\n"
      "s_or_b32 s48, s66, s69\n"
      "s_cmp_lg_u32 s48, 0\n"
      "s_cselect_b32 s48, 0x40000000, 0\n"
      "L_nf_15_%=:\n"
      "s_and_b32 s5, s84, s8\n"
      "s_sub_u32 s92, s42, 1\n"
      "s_cmp_gt_u32 s92, s44\n"
      "s_cbranch_scc1 L_tail1_%=\n"
      "L_nofix_11_%=:\n"
      "s_cmp_lg_u64 s[56:57], 0\n"
      "s_cbranch_scc1 L_slow1_%=\n"
      "L_sel1_%=:\n"
      "s_cmp_lg_u64 s[54:55], 0\n"
      "s_cbranch_scc1 L_max1_%=\n"
      "s_branch L_loop1_%=\n"
      "L_slow1_%=:\n"
      "s_nop 4\n"
      "v_mfma_f32_16x16x32_bf16 v[112:115], v[178:181], v[194:197], 0\n"
      "ds_read_b128 v[140:143], v0 offset:18432\n"
      "s_add_u32 s72, s42, 1\n"
      "v_mfma_f32_16x16x32_bf16 v[112:115], v[100:103], v[76:79], v[112:115]\n"
      "ds_read_b128 v[130:133], v9\n"
      "v_readlane_b32 s77, v38, s72\n"
      "v_mfma_f32_16x16x32_bf16 v[126:129], v[178:181], v[96:99], 0\n"
      "ds_read_b128 v[108:111], v0 offset:24576\n"
      "s_mov_b32 m0, 110592\n"
      "v_mfma_f32_16x16x32_bf16 v[126:129], v[100:103], v[80:83], v[126:129]\n"
      "ds_read_b128 v[206:209], v1 offset:30720\n"
      "s_and_b32 s73, s77, -16\n"
      "v_mfma_f32_16x16x32_bf16 v[152:155], v[178:181], v[104:107], 0\n"
      "ds_read_b128 v[144:147], v1 offset:18432\n"
      "v_subrev_u32 v197, 115456, v9\n"
      "v_mfma_f32_16x16x32_bf16 v[152:155], v[100:103], v[84:87], v[152:155]\n"
      "buffer_load_dwordx4 v197, s[28:31], s73 offen  lds\n"
      "s_waitcnt lgkmcnt(4)\n"
      "v_mfma_f32_16x16x32_bf16 v[178:181], a[0:3], v[140:143], 0\n"
      "ds_read_b128 v[202:205], v1 offset:24576\n"
      "v_readlane_b32 s45, v37, s43\n"
      "v_mfma_f32_16x16x32_bf16 v[156:159], a[24:27], v[140:143], 0\n"
      "s_waitcnt lgkmcnt(4)\n"
      "v_add_f32 v130, v130, v131\n"
      "v_add_f32_e64 v132, v132, v133\n"
      "v_mfma_f32_16x16x32_bf16 v[190:193], a[48:51], v[140:143], 0\n"
      "s_add_u32 s73, s73, 1024\n"
      "v_add_f32_e64 v130, v130, v132\n"
      "v_mov_b32_dpp v160, v160 quad_perm:[0,0,0,0] row_mask:0xf bank_mask:0xf\n"
      "v_mfma_f32_16x16x32_bf16 v[182:185], a[72:75], v[140:143], 0\n"
      "s_mov_b32 m0, 111616\n"
      "v_rsq_f32 v130, v130\n"
      "v_mfma_f32_16x16x32_bf16 v[162:165], a[96:99], v[140:143], 0\n"
      "s_waitcnt lgkmcnt(1)\n"
      "v_mul_f32 v130, 0x415db3d7, v130\n"
      "v_mov_b32_e64 v132, v130\n"
      "v_mfma_f32_16x16x32_bf16 v[134:137], a[120:123], v[140:143], 0\n"
      "ds_read_b128 v[100:103], v0 offset:30720\n"
      "v_subrev_u32 v113, 115456, v9\n"
      "v_mfma_f32_16x16x32_bf16 v[178:181], a[4:7], v[144:147], v[178:181]\n"
      "buffer_load_dwordx4 v197, s[28:31], s73 offen  lds\n"
      "v_mfma_f32_16x16x32_bf16 v[178:181], a[8:11], v[108:111], v[178:181]\n"
      "s_mov_b32 m0, 112640\n"
      "v_permlane32_swap_b32 v130, v132\n"
      "v_mfma_f32_16x16x32_bf16 v[156:159], a[28:31], v[144:147], v[156:159]\n"
      "v_mul_f32 v221, v132, v221\n"
      "v_bfe_u32 v85, v113, 4, 4\n"
      "v_mfma_f32_16x16x32_bf16 v[156:159], a[32:35], v[108:111], v[156:159]\n"
      "s_add_u32 s73, s73, 1024\n"
      "v_mul_f32 v166, v132, v166\n"
      "v_mov_b32 v131, v130\n"
      "v_mfma_f32_16x16x32_bf16 v[190:193], a[52:55], v[144:147], v[190:193]\n"
      "s_nop 0\n"
      "v_permlane16_swap_b32 v130, v131\n"
      "v_mfma_f32_16x16x32_bf16 v[190:193], a[56:59], v[108:111], v[190:193]\n"
      "v_subrev_u32 v8, 115456, v9\n"
      "v_mul_f32_e64 v177, v130, v177\n"
      "v_mfma_f32_16x16x32_bf16 v[182:185], a[76:79], v[144:147], v[182:185]\n"
      "ds_read_b128 v[114:117], v9 offset:4608\n"
      "v_mul_f32_e64 v215, v130, v215\n"
      "v_mfma_f32_16x16x32_bf16 v[182:185], a[80:83], v[108:111], v[182:185]\n"
      "buffer_load_dwordx4 v197, s[28:31], s73 offen  lds\n"
      "v_mfma_f32_16x16x32_bf16 v[162:165], a[100:103], v[144:147], v[162:165]\n"
      "s_waitcnt lgkmcnt(1)\n"
      "v_mul_f32 v174, v130, v174\n"
      "v_mul_f32_e64 v176, v130, v176\n"
      "v_mfma_f32_16x16x32_bf16 v[162:165], a[104:107], v[108:111], v[162:165]\n"
      "s_add_u32 s73, s73, 1024\n"
      "v_mul_f32 v175, v130, v175\n"
      "v_mul_f32 v118, v131, v118\n"
      "v_mfma_f32_16x16x32_bf16 v[134:137], a[124:127], v[144:147], v[134:137]\n"
      "s_mov_b32 m0, 113664\n"
      "v_mul_f32 v217, v130, v217\n"
      "v_mul_f32 v213, v131, v213\n"
      "v_mfma_f32_16x16x32_bf16 v[134:137], a[128:131], v[108:111], v[134:137]\n"
      "ds_read_b128 v[198:201], v0 offset:32768\n"
      "v_mul_f32_e64 v216, v130, v216\n"
      "v_mfma_f32_16x16x32_bf16 v[178:181], a[12:15], v[202:205], v[178:181]\n"
      "buffer_load_dwordx4 v197, s[28:31], s73 offen  lds\n"
      "v_mfma_f32_16x16x32_bf16 v[178:181], a[16:19], v[100:103], v[178:181]\n"
      "s_add_u32 s73, s73, 1024\n"
      "v_cvt_pk_bf16_f32 v174, v174, v175\n"
      "v_cvt_pk_bf16_f32 v175, v176, v177\n"
      "v_mfma_f32_16x16x32_bf16 v[178:181], a[20:23], v[206:209], v[178:181]\n"
      "s_mov_b32 m0, 114688\n"
      "v_mul_f32 v214, v130, v214\n"
      "v_mul_f32 v125, v131, v125\n"
      "v_mfma_f32_16x16x32_bf16 v[156:159], a[36:39], v[202:205], v[156:159]\n"
      "s_mov_b32 exec_hi, 0xffff\n"
      "buffer_load_dwordx4 v197, s[28:31], s73 offen  lds\n"
      "s_mov_b32 exec_hi, -1\n"
      "v_mfma_f32_16x16x32_bf16 v[156:159], a[40:43], v[100:103], v[156:159]\n"
      "v_cvt_pk_bf16_f32 v176, v214, v215\n"
      "v_cvt_pk_bf16_f32 v177, v216, v217\n"
      "v_mfma_f32_16x16x32_bf16 v[156:159], a[44:47], v[206:209], v[156:159]\n"
      "v_mul_f32 v124, v131, v124\n"
      "v_fma_f32 v161, v178, v178, s51\n"
      "v_mfma_f32_16x16x32_bf16 v[190:193], a[60:63], v[202:205], v[190:193]\n"
      "ds_write_b128 v5, v[174:177] offset:18432\n"
      "v_pk_mul_f32 v[148:149], v[148:149], v[130:131] op_sel_hi:[1,0]\n"
      "v_pk_mul_f32 v[150:151], v[150:151], v[130:131] op_sel_hi:[1,0]\n"
      "v_pk_mul_f32 v[210:211], v[210:211], v[130:131] op_sel:[0,1]\n"
      "v_mfma_f32_16x16x32_bf16 v[190:193], a[64:67], v[100:103], v[190:193]\n"
      "s_add_u32 m0, s64, 0\n"
      "v_cvt_pk_bf16_f32 v148, v148, v149\n"
      "v_mul_f32 v121, v131, v121\n"
      "v_mfma_f32_16x16x32_bf16 v[190:193], a[68:71], v[206:209], v[190:193]\n"
      "buffer_load_dwordx4 v16, s[16:19], s84 offen  lds\n"
      "v_mfma_f32_16x16x32_bf16 v[182:185], a[84:87], v[202:205], v[182:185]\n"
      "ds_read_b128 v[174:177], v1 offset:26624\n"
      "v_mul_f32_e64 v212, v131, v212\n"
      "v_mfma_f32_16x16x32_bf16 v[182:185], a[88:91], v[100:103], v[182:185]\n"
      "ds_read_b128 v[216:219], v1 offset:20480\n"
      "v_cvt_pk_bf16_f32 v149, v150, v151\n"
      "v_mfma_f32_16x16x32_bf16 v[182:185], a[92:95], v[206:209], v[182:185]\n"
      "ds_write_b64 v6, v[148:149] offset:18432\n"
      "v_mfma_f32_16x16x32_bf16 v[162:165], a[108:111], v[202:205], v[162:165]\n"
      "v_fmac_f32 v161, v179, v179\n"
      "v_mul_f32 v123, v131, v123\n"
      "v_mfma_f32_16x16x32_bf16 v[162:165], a[112:115], v[100:103], v[162:165]\n"
      "v_cvt_pk_bf16_f32 v210, v210, v211\n"
      "v_mul_f32 v120, v131, v120\n"
      "v_mfma_f32_16x16x32_bf16 v[162:165], a[116:119], v[206:209], v[162:165]\n"
      "s_add_u32 m0, s64, 4096\n"
      "v_rcp_f32 v160, v160\n"
      "v_mfma_f32_16x16x32_bf16 v[134:137], a[132:135], v[202:205], v[134:137]\n"
      "buffer_load_dwordx4 v17, s[16:19], s84 offen  lds\n"
      "v_mfma_f32_16x16x32_bf16 v[134:137], a[136:139], v[100:103], v[134:137]\n"
      "ds_read_b128 v[170:173], v0 offset:20480\n"
      "v_mul_f32_e64 v119, v131, v119\n"
      "v_mfma_f32_16x16x32_bf16 v[134:137], a[140:143], v[206:209], v[134:137]\n"
      "ds_read_b128 v[148:151], v0 offset:26624\n"
      "v_readlane_b32 s46, v37, s42\n"
      "v_mfma_f32_16x16x32_bf16 v[194:197], v[140:143], a[144:147], 0\n"
      "v_cndmask_b32 v112, v112, v126, s[58:59]\n"
      "v_cvt_pk_bf16_f32 v211, v212, v213\n"
      "v_mfma_f32_16x16x32_bf16 v[194:197], v[144:147], a[148:151], v[194:197]\n"
      "s_add_u32 m0, s64, 8192\n"
      "v_mul_f32_e64 v122, v131, v122\n"
      "v_cvt_pk_bf16_f32 v213, v124, v125\n"
      "v_mfma_f32_16x16x32_bf16 v[194:197], v[108:111], a[152:155], v[194:197]\n"
      "buffer_load_dwordx4 v18, s[16:19], s84 offen  lds\n"
      "v_mfma_f32_16x16x32_bf16 v[194:197], v[202:205], a[156:159], v[194:197]\n"
      "s_add_u32 m0, s64, 12288\n"
      "v_fma_f32 v77, v182, v182, s51\n"
      "v_fmac_f32 v77, v183, v183\n"
      "v_mfma_f32_16x16x32_bf16 v[194:197], v[100:103], a[160:163], v[194:197]\n"
      "v_cvt_pk_bf16_f32 v118, v118, v119\n"
      "v_fmac_f32 v77, v184, v184\n"
      "v_mfma_f32_16x16x32_bf16 v[194:197], v[206:209], a[164:167], v[194:197]\n"
      "v_cndmask_b32 v112, v112, v152, s[60:61]\n"
      "v_mul_u32_u24 v78, 98, v85\n"
      "v_mfma_f32_16x16x32_bf16 v[96:99], v[140:143], a[168:171], 0\n"
      "v_cvt_pk_bf16_f32 v212, v122, v123\n"
      "v_mul_f32 v189, v132, v189\n"
      "v_mfma_f32_16x16x32_bf16 v[96:99], v[144:147], a[172:175], v[96:99]\n"
      "buffer_load_dwordx4 v19, s[16:19], s84 offen  lds\n"
      "v_mfma_f32_16x16x32_bf16 v[96:99], v[108:111], a[176:179], v[96:99]\n"
      "v_fmac_f32 v161, v180, v180\n"
      "v_fmac_f32 v161, v181, v181\n"
      "v_mfma_f32_16x16x32_bf16 v[96:99], v[202:205], a[180:183], v[96:99]\n"
      "v_cvt_pk_bf16_f32 v119, v120, v121\n"
      "v_mul_f32 v186, v132, v186\n"
      "v_mfma_f32_16x16x32_bf16 v[96:99], v[100:103], a[184:187], v[96:99]\n"
      "s_mov_b32 m0, s6\n"
      "v_fmac_f32 v77, v185, v185\n"
      "v_fmac_f32 v77, v162, v162\n"
      "v_mfma_f32_16x16x32_bf16 v[96:99], v[206:209], a[188:191], v[96:99]\n"
      "v_mul_f32 v167, v132, v167\n"
      "v_fmac_f32_e64 v77, v163, v163\n"
      "v_mfma_f32_16x16x32_bf16 v[104:107], v[140:143], a[192:195], 0\n"
      "buffer_load_dwordx4 v15, s[0:3], s5 offen  lds\n"
      "v_mfma_f32_16x16x32_bf16 v[104:107], v[144:147], a[196:199], v[104:107]\n"
      "ds_read_b128 v[152:155], v1 offset:32768\n"
      "v_mul_f32 v220, v132, v220\n"
      "v_mfma_f32_16x16x32_bf16 v[104:107], v[108:111], a[200:203], v[104:107]\n"
      "v_cvt_pk_bf16_f32 v96, v96, v97\n"
      "v_mul_f32 v169, v132, v169\n"
      "v_mfma_f32_16x16x32_bf16 v[104:107], v[202:205], a[204:207], v[104:107]\n"
      "ds_write_b128 v5, v[210:213] offset:24576\n"
      "v_pk_mul_f32 v[222:223], v[222:223], v[132:133] op_sel_hi:[1,0]\n"
      "v_mfma_f32_16x16x32_bf16 v[104:107], v[100:103], a[208:211], v[104:107]\n"
      "buffer_load_dwordx4 v[126:129], v36, s[28:31], s86 offen\n"
      "v_mfma_f32_16x16x32_bf16 v[104:107], v[206:209], a[212:215], v[104:107]\n"
      "s_waitcnt lgkmcnt(2)\n"
      "v_fmac_f32 v77, v164, v164\n"
      "v_mul_f32 v168, v132, v168\n"
      "v_mfma_f32_16x16x32_bf16 v[144:147], a[0:3], v[170:173], 0\n"
      "v_fmac_f32 v161, v156, v156\n"
      "v_cvt_pk_bf16_f32 v194, v194, v195\n"
      "v_mfma_f32_16x16x32_bf16 v[144:147], a[4:7], v[216:219], v[144:147]\n"
      "ds_write_b64 v6, v[118:119] offset:24576\n"
      "v_mfma_f32_16x16x32_bf16 v[144:147], a[8:11], v[148:151], v[144:147]\n"
      "v_fmac_f32 v161, v157, v157\n"
      "v_fmac_f32 v77, v165, v165\n"
      "v_mfma_f32_16x16x32_bf16 v[144:147], a[12:15], v[174:177], v[144:147]\n"
      "s_waitcnt lgkmcnt(1)\n"
      "v_fmac_f32 v161, v158, v158\n"
      "v_mul_f32_e64 v188, v132, v188\n"
      "v_mfma_f32_16x16x32_bf16 v[144:147], a[16:19], v[198:201], v[144:147]\n"
      "buffer_load_dword v86, v29, s[28:31], s86 offen\n"
      "v_mfma_f32_16x16x32_bf16 v[144:147], a[20:23], v[152:155], v[144:147]\n"
      "s_or_b32 s95, s45, s46\n"
      "v_fmac_f32 v77, v134, v134\n"
      "v_cvt_pk_bf16_f32 v104, v104, v105\n"
      "v_mfma_f32_16x16x32_bf16 v[122:125], a[24:27], v[170:173], 0\n"
      "ds_read_b128 v[206:209], v1 offset:22528\n"
      "v_fmac_f32_e64 v161, v159, v159\n"
      "v_mfma_f32_16x16x32_bf16 v[122:125], a[28:31], v[216:219], v[122:125]\n"
      "v_cvt_pk_bf16_f32 v220, v220, v221\n"
      "v_fma_f32 v76, v144, v144, s51\n"
      "v_mfma_f32_16x16x32_bf16 v[122:125], a[32:35], v[148:151], v[122:125]\n"
      "v_mul_f32 v112, v160, v112\n"
      "v_fmac_f32 v161, v190, v190\n"
      "v_mfma_f32_16x16x32_bf16 v[122:125], a[36:39], v[174:177], v[122:125]\n"
      "v_fmac_f32 v161, v191, v191\n"
      "v_fmac_f32 v77, v135, v135\n"
      "v_mfma_f32_16x16x32_bf16 v[122:125], a[40:43], v[198:201], v[122:125]\n"
      "ds_read_b128 v[210:213], v0 offset:22528\n"
      "v_fmac_f32 v77, v136, v136\n"
      "v_mfma_f32_16x16x32_bf16 v[122:125], a[44:47], v[152:155], v[122:125]\n"
      "v_fmac_f32 v77, v137, v137\n"
      "v_fmac_f32_e64 v161, v192, v192\n"
      "v_mfma_f32_16x16x32_bf16 v[138:141], a[48:51], v[170:173], 0\n"
      "v_fmac_f32_e64 v161, v193, v193\n"
      "v_cvt_pk_bf16_f32 v112, v112, v112\n"
      "v_mfma_f32_16x16x32_bf16 v[138:141], a[52:55], v[216:219], v[138:141]\n"
      "v_cvt_pk_bf16_f32 v195, v196, v197\n"
      "v_lshrrev_b32 v8, 3, v8\n"
      "v_mfma_f32_16x16x32_bf16 v[138:141], a[56:59], v[148:151], v[138:141]\n"
      "v_permlane16_swap_b32 v161, v77\n"
      "v_mfma_f32_16x16x32_bf16 v[138:141], a[60:63], v[174:177], v[138:141]\n"
      "v_add_f32 v161, v161, v77\n"
      "v_mov_b32 v77, v161\n"
      "v_mfma_f32_16x16x32_bf16 v[138:141], a[64:67], v[198:201], v[138:141]\n"
      "s_nop 0\n"
      "v_permlane32_swap_b32 v161, v77\n"
      "v_mfma_f32_16x16x32_bf16 v[138:141], a[68:71], v[152:155], v[138:141]\n"
      "v_lshrrev_b32 v113, 8, v113\n"
      "v_min_u32 v8, 96, v8\n"
      "v_mfma_f32_16x16x32_bf16 v[202:205], a[72:75], v[170:173], 0\n"
      "v_fmac_f32 v76, v145, v145\n"
      "v_readlane_b32 s47, v38, s43\n"
      "v_mfma_f32_16x16x32_bf16 v[202:205], a[76:79], v[216:219], v[202:205]\n"
      "v_or3_b32 v114, v114, v115, v116\n"
      "v_add_f32_e64 v161, v161, v77\n"
      "v_mfma_f32_16x16x32_bf16 v[202:205], a[80:83], v[148:151], v[202:205]\n"
      "ds_read_b128 v[118:121], v0 offset:28672\n"
      "v_fmac_f32 v76, v146, v146\n"
      "v_mfma_f32_16x16x32_bf16 v[202:205], a[84:87], v[174:177], v[202:205]\n"
      "s_and_b32 s77, s77, 15\n"
      "v_cvt_pk_bf16_f32 v105, v106, v107\n"
      "v_cvt_pk_bf16_f32 v97, v98, v99\n"
      "v_mfma_f32_16x16x32_bf16 v[202:205], a[88:91], v[198:201], v[202:205]\n"
      "v_rsq_f32 v161, v161\n"
      "v_mfma_f32_16x16x32_bf16 v[202:205], a[92:95], v[152:155], v[202:205]\n"
      "v_cvt_pk_bf16_f32 v221, v222, v223\n"
      "v_cvt_pk_bf16_f32 v222, v166, v167\n"
      "v_mfma_f32_16x16x32_bf16 v[100:103], a[96:99], v[170:173], 0\n"
      "v_cvt_pk_bf16_f32 v223, v168, v169\n"
      "v_fmac_f32 v76, v147, v147\n"
      "v_mfma_f32_16x16x32_bf16 v[100:103], a[100:103], v[216:219], v[100:103]\n"
      "s_or_b32 s95, s95, s48\n"
      "v_mov_b32 v77, v161\n"
      "v_fma_f32 v98, v202, v202, s51\n"
      "v_mfma_f32_16x16x32_bf16 v[100:103], a[104:107], v[148:151], v[100:103]\n"
      "v_permlane16_swap_b32 v161, v77\n"
      "v_mfma_f32_16x16x32_bf16 v[100:103], a[108:111], v[174:177], v[100:103]\n"
      "v_fmac_f32 v98, v203, v203\n"
      "v_mul_f32 v161, s50, v161\n"
      "v_mfma_f32_16x16x32_bf16 v[100:103], a[112:115], v[198:201], v[100:103]\n"
      "v_fmac_f32 v98, v204, v204\n"
      "v_fmac_f32 v76, v122, v122\n"
      "v_mfma_f32_16x16x32_bf16 v[100:103], a[116:119], v[152:155], v[100:103]\n"
      "v_mul_f32 v187, v132, v187\n"
      "v_fmac_f32 v76, v123, v123\n"
      "v_mfma_f32_16x16x32_bf16 v[108:111], a[120:123], v[170:173], 0\n"
      "v_mul_f32 v137, v77, v137\n"
      "v_fmac_f32 v76, v124, v124\n"
      "v_mfma_f32_16x16x32_bf16 v[108:111], a[124:127], v[216:219], v[108:111]\n"
      "ds_write_b128 v5, v[220:223] offset:30720\n"
      "v_mfma_f32_16x16x32_bf16 v[108:111], a[128:131], v[148:151], v[108:111]\n"
      "v_fmac_f32 v98, v205, v205\n"
      "v_mul_f32 v134, v77, v134\n"
      "v_mfma_f32_16x16x32_bf16 v[108:111], a[132:135], v[174:177], v[108:111]\n"
      "v_fmac_f32 v76, v125, v125\n"
      "v_fmac_f32 v98, v100, v100\n"
      "v_mfma_f32_16x16x32_bf16 v[108:111], a[136:139], v[198:201], v[108:111]\n"
      "v_mul_f32 v179, v161, v179\n"
      "v_cvt_pk_bf16_f32 v186, v186, v187\n"
      "v_mfma_f32_16x16x32_bf16 v[108:111], a[140:143], v[152:155], v[108:111]\n"
      "v_lshl_add_u32 v78, v113, 3, v78\n"
      "v_fmac_f32_e64 v98, v101, v101\n"
      "v_mfma_f32_16x16x32_bf16 v[130:133], v[170:173], a[144:147], 0\n"
      "v_mul_f32 v162, v77, v162\n"
      "v_mul_f32 v183, v77, v183\n"
      "v_mfma_f32_16x16x32_bf16 v[130:133], v[216:219], a[148:151], v[130:133]\n"
      "v_cvt_pk_bf16_f32 v187, v188, v189\n"
      "v_fmac_f32 v76, v138, v138\n"
      "v_mfma_f32_16x16x32_bf16 v[130:133], v[148:151], a[152:155], v[130:133]\n"
      "v_mul_f32 v178, v161, v178\n"
      "v_fmac_f32 v98, v102, v102\n"
      "v_mfma_f32_16x16x32_bf16 v[130:133], v[174:177], a[156:159], v[130:133]\n"
      "v_mul_f32 v193, v161, v193\n"
      "v_fmac_f32 v98, v103, v103\n"
      "v_mfma_f32_16x16x32_bf16 v[130:133], v[198:201], a[160:163], v[130:133]\n"
      "ds_write_b16 v12, v112\n"
      "v_mfma_f32_16x16x32_bf16 v[130:133], v[152:155], a[164:167], v[130:133]\n"
      "v_fmac_f32 v76, v139, v139\n"
      "v_lshlrev_b32 v113, 2, v113\n"
      "v_mfma_f32_16x16x32_bf16 v[166:169], v[170:173], a[168:171], 0\n"
      "v_fmac_f32 v98, v108, v108\n"
      "v_fmac_f32_e64 v98, v109, v109\n"
      "v_mfma_f32_16x16x32_bf16 v[166:169], v[216:219], a[172:175], v[166:169]\n"
      "ds_write_b64 v6, v[186:187] offset:30720\n"
      "v_mfma_f32_16x16x32_bf16 v[166:169], v[148:151], a[176:179], v[166:169]\n"
      "v_mul_f32 v135, v77, v135\n"
      "v_fmac_f32 v76, v140, v140\n"
      "v_mfma_f32_16x16x32_bf16 v[166:169], v[174:177], a[180:183], v[166:169]\n"
      "v_bitop3_b32 v114, v114, v117, s33 bitop3:0xa8\n"
      "v_cvt_pk_bf16_f32 v178, v178, v179\n"
      "v_mfma_f32_16x16x32_bf16 v[166:169], v[198:201], a[184:187], v[166:169]\n"
      "ds_read_b128 v[186:189], v1 offset:28672\n"
      "v_fmac_f32 v98, v110, v110\n"
      "v_mfma_f32_16x16x32_bf16 v[166:169], v[152:155], a[188:191], v[166:169]\n"
      "v_fmac_f32 v98, v111, v111\n"
      "v_fmac_f32 v76, v141, v141\n"
      "v_mfma_f32_16x16x32_bf16 v[220:223], v[170:173], a[192:195], 0\n"
      "v_mul_f32 v181, v161, v181\n"
      "v_mul_f32 v182, v77, v182\n"
      "v_mfma_f32_16x16x32_bf16 v[220:223], v[216:219], a[196:199], v[220:223]\n"
      "ds_read_b128 v[214:217], v0 offset:34816\n"
      "v_mul_f32 v191, v161, v191\n"
      "v_mfma_f32_16x16x32_bf16 v[220:223], v[148:151], a[200:203], v[220:223]\n"
      "s_waitcnt lgkmcnt(2)\n"
      "v_permlane16_swap_b32 v76, v98\n"
      "v_mfma_f32_16x16x32_bf16 v[220:223], v[174:177], a[204:207], v[220:223]\n"
      "ds_read_b128 v[174:177], v1 offset:34816\n"
      "v_add_f32_e64 v76, v76, v98\n"
      "v_mfma_f32_16x16x32_bf16 v[220:223], v[198:201], a[208:211], v[220:223]\n"
      "ds_read_b128 v[148:151], v7\n"
      "v_mov_b32 v98, v76\n"
      "v_mfma_f32_16x16x32_bf16 v[220:223], v[152:155], a[212:215], v[220:223]\n"
      "s_nop 0\n"
      "v_permlane32_swap_b32 v76, v98\n"
      "v_mfma_f32_16x16x32_bf16 v[170:173], a[0:3], v[210:213], 0\n"
      "v_add_f32 v76, v76, v98\n"
      "v_add_u32_e64 v8, s77, v8\n"
      "v_mfma_f32_16x16x32_bf16 v[170:173], a[4:7], v[206:209], v[170:173]\n"
      "v_rsq_f32 v76, v76\n"
      "v_mfma_f32_16x16x32_bf16 v[170:173], a[8:11], v[118:121], v[170:173]\n"
      "s_waitcnt lgkmcnt(2)\n"
      "v_cmp_ne_u32 s[56:57], 0, v114\n"
      "v_cvt_pk_bf16_f32 v182, v182, v183\n"
      "v_mfma_f32_16x16x32_bf16 v[170:173], a[12:15], v[186:189], v[170:173]\n"
      "s_waitcnt lgkmcnt(1)\n"
      "v_cvt_pk_bf16_f32 v99, v168, v169\n"
      "v_mov_b32 v98, v76\n"
      "v_mfma_f32_16x16x32_bf16 v[170:173], a[16:19], v[214:217], v[170:173]\n"
      "s_nop 0\n"
      "v_permlane16_swap_b32 v76, v98\n"
      "v_mfma_f32_16x16x32_bf16 v[170:173], a[20:23], v[174:177], v[170:173]\n"
      "v_sub_u32 v85, v85, v113\n"
      "v_mul_f32 v190, v161, v190\n"
      "v_mfma_f32_16x16x32_bf16 v[152:155], a[24:27], v[210:213], 0\n"
      "v_cvt_pk_bf16_f32 v197, v132, v133\n"
      "v_mul_f32 v76, s50, v76\n"
      "v_mfma_f32_16x16x32_bf16 v[152:155], a[28:31], v[206:209], v[152:155]\n"
      "s_and_b32 s47, s47, -4\n"
      "v_mul_f32 v136, v77, v136\n"
      "v_mul_f32 v101, v98, v101\n"
      "v_mfma_f32_16x16x32_bf16 v[152:155], a[32:35], v[118:121], v[152:155]\n"
      "s_waitcnt lgkmcnt(0)\n"
      "v_cvt_pk_bf16_f32 v196, v130, v131\n"
      "v_mul_f32 v180, v161, v180\n"
      "v_mfma_f32_16x16x32_bf16 v[152:155], a[36:39], v[186:189], v[152:155]\n"
      "buffer_store_dwordx4 v[148:151], v20, s[24:27], s85 offen sc1\n"
      "v_pk_mul_f32 v[122:123], v[122:123], v[76:77] op_sel_hi:[1,0]\n"
      "v_pk_mul_f32 v[204:205], v[204:205], v[98:99] op_sel_hi:[1,0]\n"
      "v_pk_mul_f32 v[110:111], v[110:111], v[98:99] op_sel_hi:[1,0]\n"
      "v_pk_mul_f32 v[156:157], v[156:157], v[160:161] op_sel:[0,1]\n"
      "v_pk_mul_f32 v[102:103], v[102:103], v[98:99] op_sel_hi:[1,0]\n"
      "v_pk_mul_f32 v[164:165], v[164:165], v[76:77] op_sel:[0,1]\n"
      "v_pk_mul_f32 v[158:159], v[158:159], v[160:161] op_sel:[0,1]\n"
      "v_pk_mul_f32 v[144:145], v[144:145], v[76:77] op_sel_hi:[1,0]\n"
      "v_mfma_f32_16x16x32_bf16 v[152:155], a[40:43], v[214:217], v[152:155]\n"
      "v_mul_f32 v192, v161, v192\n"
      "v_mul_f32 v185, v77, v185\n"
      "v_mfma_f32_16x16x32_bf16 v[152:155], a[44:47], v[174:177], v[152:155]\n"
      "s_add_u32 s73, s73, 1024\n"
      "v_mul_f32 v203, v98, v203\n"
      "v_fma_f32 v82, v170, v170, s51\n"
      "v_mfma_f32_16x16x32_bf16 v[112:115], a[48:51], v[210:213], 0\n"
      "v_mul_f32 v184, v77, v184\n"
      "v_cvt_pk_bf16_f32 v134, v134, v135\n"
      "v_mfma_f32_16x16x32_bf16 v[112:115], a[52:55], v[206:209], v[112:115]\n"
      "v_mul_f32 v100, v98, v100\n"
      "v_fmac_f32 v82, v171, v171\n"
      "v_mfma_f32_16x16x32_bf16 v[112:115], a[56:59], v[118:121], v[112:115]\n"
      "v_fmac_f32 v82, v172, v172\n"
      "v_mul_f32 v202, v98, v202\n"
      "v_mfma_f32_16x16x32_bf16 v[112:115], a[60:63], v[186:189], v[112:115]\n"
      "v_add_u32_e64 v78, s77, v78\n"
      "v_cvt_pk_bf16_f32 v135, v136, v137\n"
      "v_mfma_f32_16x16x32_bf16 v[112:115], a[64:67], v[214:217], v[112:115]\n"
      "v_lshlrev_b32 v85, 4, v85\n"
      "v_mul_f32 v146, v76, v146\n"
      "v_mfma_f32_16x16x32_bf16 v[112:115], a[68:71], v[174:177], v[112:115]\n"
      "v_cvt_pk_bf16_f32 v179, v180, v181\n"
      "v_cvt_pk_bf16_f32 v144, v144, v145\n"
      "v_mfma_f32_16x16x32_bf16 v[130:133], a[72:75], v[210:213], 0\n"
      "v_mul_f32 v147, v76, v147\n"
      "v_fmac_f32 v82, v173, v173\n"
      "v_mfma_f32_16x16x32_bf16 v[130:133], a[76:79], v[206:209], v[130:133]\n"
      "v_cvt_pk_bf16_f32 v180, v156, v157\n"
      "v_fmac_f32 v82, v152, v152\n"
      "v_mfma_f32_16x16x32_bf16 v[130:133], a[80:83], v[118:121], v[130:133]\n"
      "v_mul_f32 v124, v76, v124\n"
      "v_fmac_f32_e64 v82, v153, v153\n"
      "v_mfma_f32_16x16x32_bf16 v[130:133], a[84:87], v[186:189], v[130:133]\n"
      "v_mul_f32 v109, v98, v109\n"
      "v_mul_f32 v163, v77, v163\n"
      "v_mfma_f32_16x16x32_bf16 v[130:133], a[88:91], v[214:217], v[130:133]\n"
      "v_mul_f32_e64 v108, v98, v108\n"
      "v_cvt_pk_bf16_f32 v202, v202, v203\n"
      "v_mfma_f32_16x16x32_bf16 v[130:133], a[92:95], v[174:177], v[130:133]\n"
      "s_cmp_ge_u32 s95, 0x40000000\n"
      "s_cselect_b32 s94, -1, -2\n"
      "v_fmac_f32_e64 v82, v154, v154\n"
      "v_mfma_f32_16x16x32_bf16 v[198:201], a[96:99], v[210:213], 0\n"
      "v_fmac_f32_e64 v82, v155, v155\n"
      "v_cvt_pk_bf16_f32 v183, v184, v185\n"
      "v_mfma_f32_16x16x32_bf16 v[198:201], a[100:103], v[206:209], v[198:201]\n"
      "v_cvt_pk_bf16_f32 v181, v158, v159\n"
      "v_fma_f32 v88, v130, v130, s51\n"
      "v_mfma_f32_16x16x32_bf16 v[198:201], a[104:107], v[118:121], v[198:201]\n"
      "v_cvt_pk_bf16_f32 v98, v166, v167\n"
      "v_fmac_f32_e64 v88, v131, v131\n"
      "v_mfma_f32_16x16x32_bf16 v[198:201], a[108:111], v[186:189], v[198:201]\n"
      "v_cvt_pk_bf16_f32 v107, v222, v223\n"
      "v_cvt_pk_bf16_f32 v184, v162, v163\n"
      "v_mfma_f32_16x16x32_bf16 v[198:201], a[112:115], v[214:217], v[198:201]\n"
      "v_cvt_pk_bf16_f32 v190, v190, v191\n"
      "v_fmac_f32 v82, v112, v112\n"
      "v_mfma_f32_16x16x32_bf16 v[198:201], a[116:119], v[174:177], v[198:201]\n"
      "v_fmac_f32 v82, v113, v113\n"
      "v_cvt_pk_bf16_f32 v185, v164, v165\n"
      "v_mfma_f32_16x16x32_bf16 v[160:163], a[120:123], v[210:213], 0\n"
      "v_cvt_pk_bf16_f32 v145, v146, v147\n"
      "v_mov_b32 v136, 0x3fb9\n"
      "v_mfma_f32_16x16x32_bf16 v[160:163], a[124:127], v[206:209], v[160:163]\n"
      "v_cvt_pk_bf16_f32 v146, v122, v123\n"
      "v_fmac_f32 v82, v114, v114\n"
      "v_mfma_f32_16x16x32_bf16 v[160:163], a[128:131], v[118:121], v[160:163]\n"
      "v_fmac_f32 v88, v132, v132\n"
      "v_fmac_f32 v88, v133, v133\n"
      "v_mfma_f32_16x16x32_bf16 v[160:163], a[132:135], v[186:189], v[160:163]\n"
      "s_waitcnt vmcnt(8)\n"
      "v_fmac_f32 v88, v198, v198\n"
      "v_mul_f32 v125, v76, v125\n"
      "v_mfma_f32_16x16x32_bf16 v[160:163], a[136:139], v[214:217], v[160:163]\n"
      "v_cvt_pk_bf16_f32 v106, v220, v221\n"
      "v_fmac_f32 v82, v115, v115\n"
      "v_mfma_f32_16x16x32_bf16 v[160:163], a[140:143], v[174:177], v[160:163]\n"
      "v_fmac_f32 v88, v199, v199\n"
      "v_fmac_f32 v88, v200, v200\n"
      "v_fmac_f32 v88, v201, v201\n"
      "v_cvt_pk_bf16_f32 v147, v124, v125\n"
      "v_and_b32_e64 v93, 2, v78\n"
      "v_pk_mul_f32 v[138:139], v[138:139], v[76:77] op_sel_hi:[1,0]\n"
      "v_pk_mul_f32 v[140:141], v[140:141], v[76:77] op_sel_hi:[1,0]\n"
      "v_mfma_f32_16x16x32_bf16 v[148:151], v[210:213], a[144:147], 0\n"
      "v_fmac_f32 v88, v160, v160\n"
      "v_fmac_f32 v88, v161, v161\n"
      "v_fmac_f32 v88, v162, v162\n"
      "v_fmac_f32 v88, v163, v163\n"
      "v_mfma_f32_16x16x32_bf16 v[148:151], v[206:209], a[148:151], v[148:151]\n"
      "s_and_b32 s4, s45, s8\n"
      "v_permlane16_swap_b32 v82, v88\n"
      "v_mfma_f32_16x16x32_bf16 v[148:151], v[118:121], a[152:155], v[148:151]\n"
      "v_add_f32 v82, v82, v88\n"
      "v_mov_b32 v88, v82\n"
      "v_mfma_f32_16x16x32_bf16 v[148:151], v[186:189], a[156:159], v[148:151]\n"
      "s_nop 0\n"
      "v_permlane32_swap_b32 v82, v88\n"
      "v_mfma_f32_16x16x32_bf16 v[148:151], v[214:217], a[160:163], v[148:151]\n"
      "v_add_f32 v82, v82, v88\n"
      "v_rsq_f32 v82, v82\n"
      "v_mfma_f32_16x16x32_bf16 v[148:151], v[174:177], a[164:167], v[148:151]\n"
      "s_cmp_gt_u32 s42, s44\n"
      "s_cselect_b32 s95, s94, s95\n"
      "v_mov_b32 v88, v82\n"
      "v_mfma_f32_16x16x32_bf16 v[122:125], v[210:213], a[168:171], 0\n"
      "v_cvt_pk_bf16_f32 v191, v192, v193\n"
      "v_permlane16_swap_b32 v82, v88\n"
      "v_cvt_pk_bf16_f32 v108, v108, v109\n"
      "v_mul_f32_e64 v82, s50, v82\n"
      "v_pk_mul_f32 v[160:161], v[160:161], v[88:89] op_sel_hi:[1,0]\n"
      "v_pk_mul_f32 v[112:113], v[112:113], v[82:83] op_sel_hi:[1,0]\n"
      "v_pk_mul_f32 v[154:155], v[154:155], v[82:83] op_sel_hi:[1,0]\n"
      "v_pk_mul_f32 v[130:131], v[130:131], v[88:89] op_sel_hi:[1,0]\n"
      "v_pk_mul_f32 v[172:173], v[172:173], v[82:83] op_sel_hi:[1,0]\n"
      "v_pk_mul_f32 v[200:201], v[200:201], v[88:89] op_sel_hi:[1,0]\n"
      "v_pk_mul_f32 v[132:133], v[132:133], v[88:89] op_sel_hi:[1,0]\n"
      "v_pk_mul_f32 v[170:171], v[170:171], v[82:83] op_sel_hi:[1,0]\n"
      "v_mfma_f32_16x16x32_bf16 v[122:125], v[206:209], a[172:175], v[122:125]\n"
      "v_cvt_pk_bf16_f32 v203, v204, v205\n"
      "v_mul_f32_e64 v115, v82, v115\n"
      "v_mfma_f32_16x16x32_bf16 v[122:125], v[118:121], a[176:179], v[122:125]\n"
      "ds_read_b128 v[220:223], v2\n"
      "v_cvt_pk_bf16_f32 v77, v150, v151\n"
      "v_mfma_f32_16x16x32_bf16 v[122:125], v[186:189], a[180:183], v[122:125]\n"
      "v_cvt_pk_bf16_f32 v109, v110, v111\n"
      "v_cvt_pk_bf16_f32 v204, v100, v101\n"
      "v_mfma_f32_16x16x32_bf16 v[122:125], v[214:217], a[184:187], v[122:125]\n"
      "ds_read_b128 v[166:169], v2 offset:6144\n"
      "v_mov_b32_e64 v137, 0\n"
      "v_mfma_f32_16x16x32_bf16 v[122:125], v[174:177], a[188:191], v[122:125]\n"
      "v_cvt_pk_bf16_f32 v76, v148, v149\n"
      "v_and_b32_e64 v78, -4, v78\n"
      "v_mfma_f32_16x16x32_bf16 v[156:159], v[210:213], a[192:195], 0\n"
      "ds_read_b128 v[210:213], v2 offset:3072\n"
      "v_cvt_pk_bf16_f32 v205, v102, v103\n"
      "v_mfma_f32_16x16x32_bf16 v[156:159], v[206:209], a[196:199], v[156:159]\n"
      "ds_read_b128 v[148:151], v7 offset:4096\n"
      "v_mul_f32 v114, v82, v114\n"
      "v_mfma_f32_16x16x32_bf16 v[156:159], v[118:121], a[200:203], v[156:159]\n"
      "ds_read_b128 v[118:121], v2 offset:9216\n"
      "v_cvt_pk_bf16_f32 v130, v130, v131\n"
      "v_mfma_f32_16x16x32_bf16 v[156:159], v[186:189], a[204:207], v[156:159]\n"
      "s_waitcnt lgkmcnt(4)\n"
      "v_cvt_pk_bf16_f32 v138, v138, v139\n"
      "v_cvt_pk_bf16_f32 v81, v124, v125\n"
      "v_mfma_f32_16x16x32_bf16 v[156:159], v[214:217], a[208:211], v[156:159]\n"
      "ds_read_b128 v[186:189], v2 offset:12288\n"
      "v_mov_b32_e64 v103, 0\n"
      "v_mfma_f32_16x16x32_bf16 v[156:159], v[174:177], a[212:215], v[156:159]\n"
      "ds_read_b128 v[206:209], v2 offset:15360\n"
      "v_mul_f32 v198, v88, v198\n"
      "v_mfma_f32_16x16x32_bf16 v[174:177], a[216:219], v[220:223], 0\n"
      "s_waitcnt lgkmcnt(4)\n"
      "v_mul_f32 v199, v88, v199\n"
      "v_add_u32 v192, 110592, v78\n"
      "v_mfma_f32_16x16x32_bf16 v[174:177], a[220:223], v[210:213], v[174:177]\n"
      "s_waitcnt lgkmcnt(3)\n"
      "v_cvt_pk_bf16_f32 v131, v132, v133\n"
      "v_cvt_pk_bf16_f32 v112, v112, v113\n"
      "v_mfma_f32_16x16x32_bf16 v[174:177], a[224:227], v[166:169], v[174:177]\n"
      "buffer_store_dwordx4 v[148:151], v21, s[24:27], s85 offen sc1\n"
      "v_mfma_f32_16x16x32_bf16 v[214:217], a[240:243], v[220:223], 0\n"
      "ds_read2_b32 v[110:111], v192 offset0:2 offset1:10\n"
      "v_cvt_pk_bf16_f32 v84, v156, v157\n"
      "v_mfma_f32_16x16x32_bf16 v[214:217], a[244:247], v[210:213], v[214:217]\n"
      "s_cmp_ge_u32 s95, 0x40000000\n"
      "s_cselect_b64 vcc, -1, s[56:57]\n"
      "v_lshlrev_b64 v[136:137], v85, v[136:137]\n"
      "v_mfma_f32_16x16x32_bf16 v[214:217], a[248:251], v[166:169], v[214:217]\n"
      "s_waitcnt lgkmcnt(1)\n"
      "v_cvt_pk_bf16_f32 v133, v200, v201\n"
      "v_mul_f32 v153, v82, v153\n"
      "v_mfma_f32_16x16x32_bf16 v[148:151], v[232:235], v[220:223], 0\n"
      "ds_read2_b32 v[200:201], v192 offset0:16 offset1:17\n"
      "v_cvt_pk_bf16_f32 v80, v122, v123\n"
      "v_mfma_f32_16x16x32_bf16 v[148:151], v[236:239], v[210:213], v[148:151]\n"
      "ds_read_b128 v[210:213], v7 offset:8192\n"
      "v_cvt_pk_bf16_f32 v170, v170, v171\n"
      "v_mfma_f32_16x16x32_bf16 v[148:151], v[240:243], v[166:169], v[148:151]\n"
      "ds_read2_b32 v[218:219], v192 offset0:0 offset1:1\n"
      "v_cmp_gt_u32 s[72:73], 64, v85\n"
      "v_mfma_f32_16x16x32_bf16 v[174:177], a[228:231], v[118:121], v[174:177]\n"
      "v_cvt_pk_bf16_f32 v132, v198, v199\n"
      "v_cvt_pk_bf16_f32 v171, v172, v173\n"
      "v_mfma_f32_16x16x32_bf16 v[174:177], a[232:235], v[186:189], v[174:177]\n"
      "v_cvt_pk_bf16_f32 v113, v114, v115\n"
      "v_cndmask_b32_e64 v136, 0, v136, s[72:73]\n"
      "v_mfma_f32_16x16x32_bf16 v[174:177], a[236:239], v[206:209], v[174:177]\n"
      "ds_read_b128 v[114:117], v10 offset:1200\n"
      "v_cvt_pk_bf16_f32 v139, v140, v141\n"
      "v_mfma_f32_16x16x32_bf16 v[214:217], a[252:255], v[118:121], v[214:217]\n"
      "ds_read_b64 v[100:101], v10 offset:1192\n"
      "v_mul_f32_e64 v163, v88, v163\n"
      "v_mfma_f32_16x16x32_bf16 v[214:217], v[224:227], v[186:189], v[214:217]\n"
      "ds_read_b128 v[122:125], v7 offset:12288\n"
      "v_mov_b32 v102, 0xbb2b\n"
      "v_mfma_f32_16x16x32_bf16 v[214:217], v[228:231], v[206:209], v[214:217]\n"
      "ds_read2_b32 v[198:199], v192 offset0:8 offset1:9\n"
      "v_mul_f32 v162, v88, v162\n"
      "v_mfma_f32_16x16x32_bf16 v[148:151], v[244:247], v[118:121], v[148:151]\n"
      "s_waitcnt lgkmcnt(4)\n"
      "v_mul_f32 v152, v82, v152\n"
      "v_cvt_pk_bf16_f32 v173, v154, v155\n"
      "v_mfma_f32_16x16x32_bf16 v[148:151], v[248:251], v[186:189], v[148:151]\n"
      "buffer_store_dwordx4 v[210:213], v22, s[24:27], s85 offen sc1\n"
      "v_mfma_f32_16x16x32_bf16 v[148:151], v[252:255], v[206:209], v[148:151]\n"
      "ds_read2_b32 v[118:119], v192 offset0:24 offset1:18\n"
      "v_cndmask_b32_e64 v137, 0, v137, s[72:73]\n"
      "v_mfma_f32_16x16x32_bf16 v[220:223], v[182:185], v[178:181], v[40:43]\n"
      "v_alignbyte_b32 v218, v219, v218, v93\n"
      "v_cmp_gt_u32 s[72:73], 64, v85\n"
      "v_mfma_f32_16x16x32_bf16 v[186:189], v[202:205], v[178:181], v[44:47]\n"
      "s_waitcnt lgkmcnt(1)\n"
      "v_lshlrev_b64 v[102:103], v85, v[102:103]\n"
      "v_cvt_pk_bf16_f32 v160, v160, v161\n"
      "v_mfma_f32_16x16x32_bf16 v[166:169], v[130:133], v[178:181], v[48:51]\n"
      "ds_read_b128 v[154:157], v2 offset:256\n"
      "v_cvt_pk_bf16_f32 v161, v162, v163\n"
      "v_mfma_f32_16x16x32_bf16 v[162:165], v[114:117], v[178:181], v[24:27]\n"
      "ds_read_b128 v[210:213], v7 offset:16384\n"
      "v_alignbyte_b32 v200, v201, v200, v93\n"
      "v_mfma_f32_16x16x16_bf16 v[220:223], v[134:135], v[190:191], v[220:223]\n"
      "v_fma_f32 v192, v174, v174, s52\n"
      "v_alignbyte_b32 v198, v199, v198, v93\n"
      "v_add_u32 v8, 115296, v8\n"
      "v_mfma_f32_16x16x16_bf16 v[186:189], v[108:109], v[190:191], v[186:189]\n"
      "v_cvt_pk_bf16_f32 v172, v152, v153\n"
      "v_cndmask_b32_e64 v102, 0, v102, s[72:73]\n"
      "v_mul_f32_e64 v193, v175, v175\n"
      "v_mfma_f32_16x16x16_bf16 v[166:169], v[160:161], v[190:191], v[166:169]\n"
      "buffer_store_dwordx4 v[122:125], v23, s[24:27], s85 offen sc1\n"
      "v_mfma_f32_16x16x16_bf16 v[162:165], v[100:101], v[190:191], v[162:165]\n"
      "v_alignbyte_b32 v219, v110, v219, v93\n"
      "v_alignbyte_b32 v199, v111, v199, v93\n"
      "v_fmac_f32 v192, v176, v176\n"
      "v_mfma_f32_16x16x16_bf16 v[220:223], v[136:137], v[218:219], v[220:223]\n"
      "s_waitcnt lgkmcnt(0)\n"
      "v_alignbyte_b32 v201, v119, v201, v93\n"
      "v_add_u32 v152, 112160, v78\n"
      "v_mfma_f32_16x16x16_bf16 v[186:189], v[136:137], v[198:199], v[186:189]\n"
      "ds_read_b128 v[178:181], v2 offset:3328\n"
      "v_alignbyte_b32 v118, v118, v118, v93\n"
      "v_mfma_f32_16x16x16_bf16 v[166:169], v[136:137], v[200:201], v[166:169]\n"
      "ds_read2_b32 v[140:141], v152 offset0:8 offset1:9\n"
      "v_cndmask_b32_e64 v103, 0, v103, s[72:73]\n"
      "v_mfma_f32_16x16x16_bf16 v[162:165], v[136:137], v[118:119], v[162:165]\n"
      "ds_read2_b32 v[110:111], v152 offset0:0 offset1:1\n"
      "v_cvt_pk_bf16_f32 v85, v158, v159\n"
      "v_mfma_f32_16x16x16_bf16 v[220:223], v[102:103], v[218:219], v[220:223]\n"
      "ds_read_b128 v[206:209], v2 offset:6400\n"
      "v_fmac_f32_e64 v193, v177, v177\n"
      "v_mfma_f32_16x16x16_bf16 v[186:189], v[102:103], v[198:199], v[186:189]\n"
      "ds_read2_b32 v[120:121], v152 offset0:2 offset1:10\n"
      "v_fmac_f32_e64 v193, v215, v215\n"
      "v_mfma_f32_16x16x16_bf16 v[166:169], v[102:103], v[200:201], v[166:169]\n"
      "buffer_store_dwordx4 v[210:213], v25, s[24:27], s85 offen sc1\n"
      "v_mfma_f32_16x16x16_bf16 v[162:165], v[102:103], v[118:119], v[162:165]\n"
      "v_fmac_f32_e64 v192, v214, v214\n"
      "v_add_u32 v159, 113728, v78\n"
      "v_max3_f32 v78, v220, v221, v222\n"
      "v_mfma_f32_16x16x32_bf16 v[210:213], a[216:219], v[154:157], 0\n"
      "s_waitcnt lgkmcnt(2)\n"
      "v_alignbyte_b32 v140, v141, v140, v93\n"
      "v_max3_f32 v78, v78, v188, v189\n"
      "v_mfma_f32_16x16x32_bf16 v[210:213], a[220:223], v[178:181], v[210:213]\n"
      "s_waitcnt lgkmcnt(1)\n"
      "v_max3_f32 v165, v223, v186, v187\n"
      "v_max3_f32 v78, v78, v168, v169\n"
      "v_mfma_f32_16x16x32_bf16 v[210:213], a[224:227], v[206:209], v[210:213]\n"
      "ds_read_u16 v8, v8\n"
      "v_max3_f32 v165, v165, v166, v167\n"
      "v_mfma_f32_16x16x32_bf16 v[122:125], a[240:243], v[154:157], 0\n"
      "s_waitcnt lgkmcnt(1)\n"
      "v_max3_f32 v78, v78, v165, v162\n"
      "v_alignbyte_b32 v110, v111, v110, v93\n"
      "v_mfma_f32_16x16x32_bf16 v[122:125], a[244:247], v[178:181], v[122:125]\n"
      "ds_read2_b32 v[164:165], v152 offset0:16 offset1:17\n"
      "v_alignbyte_b32 v141, v121, v141, v93\n"
      "v_mfma_f32_16x16x32_bf16 v[122:125], a[248:251], v[206:209], v[122:125]\n"
      "s_waitcnt vmcnt(6)\n"
      "v_or3_b32 v126, v126, v127, v128\n"
      "v_alignbyte_b32 v111, v120, v111, v93\n"
      "v_mfma_f32_16x16x32_bf16 v[118:121], v[232:235], v[154:157], 0\n"
      "ds_read2_b32 v[156:157], v152 offset0:24 offset1:18\n"
      "v_mov_b32_e64 v201, v78\n"
      "v_mfma_f32_16x16x32_bf16 v[118:121], v[236:239], v[178:181], v[118:121]\n"
      "s_nop 0\n"
      "v_permlane16_swap_b32 v78, v201\n"
      "v_mfma_f32_16x16x32_bf16 v[118:121], v[240:243], v[206:209], v[118:121]\n"
      "v_max_f32 v78, v78, v201\n"
      "v_mov_b32 v201, v78\n"
      "v_mfma_f32_16x16x32_bf16 v[178:181], v[182:185], v[144:147], v[52:55]\n"
      "s_nop 0\n"
      "v_permlane32_swap_b32 v78, v201\n"
      "v_mfma_f32_16x16x32_bf16 v[152:155], v[202:205], v[144:147], v[56:59]\n"
      "s_waitcnt lgkmcnt(1)\n"
      "v_max_f32 v78, v78, v201\n"
      "v_sub_f32 v186, v186, v78\n"
      "v_alignbyte_b32 v164, v165, v164, v93\n"
      "v_lshlrev_b32_e64 v8, 16, v8\n"
      "s_waitcnt vmcnt(5)\n"
      "v_or3_b32 v126, v126, v129, v86\n"
      "v_pk_add_f32 v[166:167], v[166:167], v[78:79] op_sel_hi:[1,0] neg_lo:[0,1] neg_hi:[0,1]\n"
      "v_pk_add_f32 v[222:223], v[222:223], v[78:79] op_sel_hi:[1,0] neg_lo:[0,1] neg_hi:[0,1]\n"
      "v_pk_fma_f32 v[192:193], v[216:217], v[216:217], v[192:193]\n"
      "v_pk_add_f32 v[220:221], v[220:221], v[78:79] op_sel_hi:[1,0] neg_lo:[0,1] neg_hi:[0,1]\n"
      "v_pk_add_f32 v[168:169], v[168:169], v[78:79] op_sel_hi:[1,0] neg_lo:[0,1] neg_hi:[0,1]\n"
      "v_pk_add_f32 v[188:189], v[188:189], v[78:79] op_sel_hi:[1,0] neg_lo:[0,1] neg_hi:[0,1]\n"
      "v_mfma_f32_16x16x32_bf16 v[206:209], v[130:133], v[144:147], v[60:63]\n"
      "ds_write_b32 v31, v126 offset:5632\n"
      "v_mfma_f32_16x16x32_bf16 v[198:201], v[114:117], v[144:147], v[26:29]\n"
      "s_waitcnt lgkmcnt(1)\n"
      "v_exp_f32 v168, v168\n"
      "v_mfma_f32_16x16x16_bf16 v[178:181], v[134:135], v[138:139], v[178:181]\n"
      "v_exp_f32_e64 v221, v221\n"
      "v_alignbyte_b32 v165, v157, v165, v93\n"
      "v_mfma_f32_16x16x16_bf16 v[152:155], v[108:109], v[138:139], v[152:155]\n"
      "v_exp_f32 v166, v166\n"
      "v_sub_f32 v162, v162, v78\n"
      "v_mfma_f32_16x16x16_bf16 v[206:209], v[160:161], v[138:139], v[206:209]\n"
      "v_alignbyte_b32 v156, v156, v156, v93\n"
      "v_exp_f32 v188, v188\n"
      "v_mfma_f32_16x16x16_bf16 v[198:201], v[100:101], v[138:139], v[198:201]\n"
      "v_exp_f32 v167, v167\n"
      "v_exp_f32_e64 v220, v220\n"
      "v_mfma_f32_16x16x16_bf16 v[178:181], v[136:137], v[110:111], v[178:181]\n"
      "v_cvt_pk_bf16_f32 v88, v166, v167\n"
      "v_exp_f32_e64 v169, v169\n"
      "v_mfma_f32_16x16x16_bf16 v[152:155], v[136:137], v[140:141], v[152:155]\n"
      "v_cvt_pk_bf16_f32 v220, v220, v221\n"
      "v_exp_f32 v222, v222\n"
      "v_mfma_f32_16x16x16_bf16 v[206:209], v[136:137], v[164:165], v[206:209]\n"
      "v_exp_f32 v223, v223\n"
      "v_cvt_pk_bf16_f32 v89, v168, v169\n"
      "v_mfma_f32_16x16x16_bf16 v[198:201], v[136:137], v[156:157], v[198:201]\n"
      "ds_read_b128 v[166:169], v2 offset:9472\n"
      "v_sub_f32_e64 v187, v187, v78\n"
      "v_mfma_f32_16x16x16_bf16 v[178:181], v[102:103], v[110:111], v[178:181]\n"
      "ds_read_b128 v[144:147], v2 offset:12544\n"
      "v_exp_f32 v187, v187\n"
      "v_mfma_f32_16x16x16_bf16 v[152:155], v[102:103], v[140:141], v[152:155]\n"
      "v_exp_f32 v186, v186\n"
      "v_exp_f32_e64 v189, v189\n"
      "v_mfma_f32_16x16x16_bf16 v[206:209], v[102:103], v[164:165], v[206:209]\n"
      "ds_read_b128 v[138:141], v2 offset:15616\n"
      "v_cvt_pk_bf16_f32 v221, v222, v223\n"
      "v_cvt_pk_bf16_f32 v223, v188, v189\n"
      "v_mfma_f32_16x16x16_bf16 v[198:201], v[102:103], v[156:157], v[198:201]\n"
      "v_exp_f32_e64 v162, v162\n"
      "v_max3_f32 v78, v181, v152, v153\n"
      "v_max3_f32 v111, v178, v179, v180\n"
      "v_max3_f32 v111, v111, v154, v155\n"
      "v_max3_f32 v111, v111, v208, v209\n"
      "v_pk_fma_f32 v[192:193], v[148:149], v[148:149], v[192:193]\n"
      "v_max3_f32 v78, v78, v206, v207\n"
      "v_pk_fma_f32 v[192:193], v[150:151], v[150:151], v[192:193]\n"
      "v_max3_f32 v111, v111, v78, v198\n"
      "v_cvt_pk_bf16_f32 v90, v162, 0\n"
      "v_mov_b32 v110, v111\n"
      "s_waitcnt lgkmcnt(1)\n"
      "v_mfma_f32_16x16x32_bf16 v[210:213], a[228:231], v[166:169], v[210:213]\n"
      "ds_read_u16 v78, v11 offset:1168\n"
      "v_permlane16_swap_b32 v111, v110\n"
      "v_mfma_f32_16x16x32_bf16 v[210:213], a[232:235], v[144:147], v[210:213]\n"
      "s_waitcnt lgkmcnt(1)\n"
      "v_max_f32 v111, v111, v110\n"
      "v_mov_b32 v110, v111\n"
      "v_mfma_f32_16x16x32_bf16 v[210:213], a[236:239], v[138:141], v[210:213]\n"
      "ds_read_u16 v86, v11 offset:1232\n"
      "v_cvt_pk_bf16_f32 v222, v186, v187\n"
      "v_mfma_f32_16x16x32_bf16 v[122:125], a[252:255], v[166:169], v[122:125]\n"
      "ds_read_u16 v82, v11 offset:1200\n"
      "v_permlane32_swap_b32 v111, v110\n"
      "v_max_f32 v111, v111, v110\n"
      "v_sub_f32_e64 v198, v198, v111\n"
      "v_pk_add_f32 v[208:209], v[208:209], v[110:111] op_sel:[0,1] neg_lo:[0,1] neg_hi:[0,1]\n"
      "v_pk_add_f32 v[178:179], v[178:179], v[110:111] op_sel:[0,1] neg_lo:[0,1] neg_hi:[0,1]\n"
      "v_pk_add_f32 v[152:153], v[152:153], v[110:111] op_sel:[0,1] neg_lo:[0,1] neg_hi:[0,1]\n"
      "v_pk_add_f32 v[206:207], v[206:207], v[110:111] op_sel:[0,1] neg_lo:[0,1] neg_hi:[0,1]\n"
      "v_pk_add_f32 v[180:181], v[180:181], v[110:111] op_sel:[0,1] neg_lo:[0,1] neg_hi:[0,1]\n"
      "v_pk_add_f32 v[154:155], v[154:155], v[110:111] op_sel:[0,1] neg_lo:[0,1] neg_hi:[0,1]\n"
      "v_mfma_f32_16x16x32_bf16 v[122:125], v[224:227], v[144:147], v[122:125]\n"
      "ds_read2_b32 v[110:111], v159 offset0:0 offset1:1\n"
      "v_add_f32 v192, v192, v193\n"
      "v_mfma_f32_16x16x32_bf16 v[122:125], v[228:231], v[138:141], v[122:125]\n"
      "v_exp_f32 v178, v178\n"
      "v_mfma_f32_16x16x32_bf16 v[118:121], v[244:247], v[166:169], v[118:121]\n"
      "v_exp_f32_e64 v181, v181\n"
      "v_mfma_f32_16x16x32_bf16 v[118:121], v[248:251], v[144:147], v[118:121]\n"
      "ds_read2_b32 v[200:201], v159 offset0:8 offset1:9\n"
      "v_exp_f32_e64 v152, v152\n"
      "v_mfma_f32_16x16x32_bf16 v[118:121], v[252:255], v[138:141], v[118:121]\n"
      "ds_read2_b32 v[144:145], v159 offset0:2 offset1:10\n"
      "v_exp_f32_e64 v179, v179\n"
      "v_mfma_f32_16x16x32_bf16 v[166:169], v[32:35], v[220:223], 0\n"
      "ds_read2_b32 v[126:127], v159 offset0:16 offset1:17\n"
      "v_cvt_pk_bf16_f32 v178, v178, v179\n"
      "v_mfma_f32_16x16x32_bf16 v[166:169], v[32:35], v[88:91], v[166:169]\n"
      "ds_read2_b32 v[128:129], v159 offset0:24 offset1:18\n"
      "v_exp_f32_e64 v180, v180\n"
      "v_mfma_f32_16x16x32_bf16 v[186:189], v[194:197], v[220:223], 0\n"
      "s_waitcnt lgkmcnt(4)\n"
      "v_exp_f32 v155, v155\n"
      "v_mfma_f32_16x16x32_bf16 v[186:189], v[76:79], v[88:91], v[186:189]\n"
      "v_alignbyte_b32 v110, v111, v110, v93\n"
      "v_cvt_pk_bf16_f32 v179, v180, v181\n"
      "v_mfma_f32_16x16x32_bf16 v[162:165], v[96:99], v[220:223], 0\n"
      "v_exp_f32 v153, v153\n"
      "v_mfma_f32_16x16x32_bf16 v[162:165], v[80:83], v[88:91], v[162:165]\n"
      "s_waitcnt lgkmcnt(2)\n"
      "v_alignbyte_b32 v111, v144, v111, v93\n"
      "v_cvt_pk_bf16_f32 v180, v152, v153\n"
      "v_mfma_f32_16x16x32_bf16 v[138:141], v[104:107], v[220:223], 0\n"
      "s_waitcnt lgkmcnt(1)\n"
      "v_exp_f32 v154, v154\n"
      "v_mfma_f32_16x16x32_bf16 v[138:141], v[84:87], v[88:91], v[138:141]\n"
      "v_alignbyte_b32 v126, v127, v126, v93\n"
      "v_alignbyte_b32 v200, v201, v200, v93\n"
      "v_mfma_f32_16x16x32_bf16 v[156:159], v[182:185], v[170:173], v[64:67]\n"
      "s_waitcnt lgkmcnt(0)\n"
      "v_cvt_pk_bf16_f32 v181, v154, v155\n"
      "v_alignbyte_b32 v128, v128, v128, v93\n"
      "v_mfma_f32_16x16x32_bf16 v[152:155], v[202:205], v[170:173], v[68:71]\n"
      "v_alignbyte_b32 v127, v129, v127, v93\n"
      "v_alignbyte_b32 v201, v145, v201, v93\n"
      "v_mfma_f32_16x16x32_bf16 v[144:147], v[130:133], v[170:173], v[72:75]\n"
      "v_exp_f32 v208, v208\n"
      "v_mfma_f32_16x16x32_bf16 v[218:221], v[114:117], v[170:173], v[28:31]\n"
      "v_rcp_f32 v166, v166\n"
      "v_mfma_f32_16x16x16_bf16 v[156:159], v[134:135], v[112:113], v[156:159]\n"
      "v_exp_f32 v207, v207\n"
      "v_mul_f32 v163, v166, v163\n"
      "v_mfma_f32_16x16x16_bf16 v[152:155], v[108:109], v[112:113], v[152:155]\n"
      "v_exp_f32 v209, v209\n"
      "v_mul_f32 v164, v166, v164\n"
      "v_mfma_f32_16x16x16_bf16 v[144:147], v[160:161], v[112:113], v[144:147]\n"
      "v_mul_f32 v189, v166, v189\n"
      "v_mul_f32 v186, v166, v186\n"
      "v_mul_f32 v162, v166, v162\n"
      "v_mfma_f32_16x16x16_bf16 v[218:221], v[100:101], v[112:113], v[218:221]\n"
      "v_exp_f32 v206, v206\n"
      "v_cvt_pk_bf16_f32 v93, v208, v209\n"
      "v_mfma_f32_16x16x16_bf16 v[156:159], v[136:137], v[110:111], v[156:159]\n"
      "v_mul_f32 v138, v166, v138\n"
      "v_exp_f32_e64 v198, v198\n"
      "v_mfma_f32_16x16x16_bf16 v[152:155], v[136:137], v[200:201], v[152:155]\n"
      "v_mul_f32 v188, v166, v188\n"
      "v_mul_f32 v187, v166, v187\n"
      "v_cvt_pk_bf16_f32 v186, v186, v187\n"
      "v_mfma_f32_16x16x16_bf16 v[144:147], v[136:137], v[126:127], v[144:147]\n"
      "v_cvt_pk_bf16_f32 v187, v188, v189\n"
      "v_mul_f32_e64 v139, v166, v139\n"
      "v_cvt_pk_bf16_f32 v188, v162, v163\n"
      "v_mfma_f32_16x16x16_bf16 v[218:221], v[136:137], v[128:129], v[218:221]\n"
      "ds_read_b128 v[170:173], v2 offset:512\n"
      "v_mul_f32_e64 v165, v166, v165\n"
      "v_mfma_f32_16x16x16_bf16 v[156:159], v[102:103], v[110:111], v[156:159]\n"
      "ds_read_b128 v[110:113], v2 offset:3584\n"
      "v_cvt_pk_bf16_f32 v189, v164, v165\n"
      "v_mfma_f32_16x16x16_bf16 v[152:155], v[102:103], v[200:201], v[152:155]\n"
      "v_cvt_pk_bf16_f32 v94, v198, 0\n"
      "v_fma_f32 v136, v210, v210, s52\n"
      "v_mul_f32_e64 v137, v211, v211\n"
      "v_mfma_f32_16x16x16_bf16 v[144:147], v[102:103], v[126:127], v[144:147]\n"
      "ds_read_b128 v[162:165], v2 offset:6656\n"
      "v_max3_f32 v198, v156, v157, v158\n"
      "v_mfma_f32_16x16x16_bf16 v[218:221], v[102:103], v[128:129], v[218:221]\n"
      "v_fmac_f32 v137, v213, v213\n"
      "v_cvt_pk_bf16_f32 v138, v138, v139\n"
      "v_fmac_f32 v136, v212, v212\n"
      "v_max3_f32 v198, v198, v154, v155\n"
      "v_max3_f32 v222, v159, v152, v153\n"
      "v_max3_f32 v222, v222, v144, v145\n"
      "v_pk_fma_f32 v[136:137], v[122:123], v[122:123], v[136:137]\n"
      "v_max3_f32 v198, v198, v146, v147\n"
      "v_max3_f32 v198, v198, v222, v218\n"
      "v_pk_mul_f32 v[140:141], v[140:141], v[166:167] op_sel_hi:[1,0]\n"
      "v_pk_fma_f32 v[136:137], v[124:125], v[124:125], v[136:137]\n"
      "v_mov_b32 v166, v198\n"
      "s_waitcnt lgkmcnt(1)\n"
      "v_mfma_f32_16x16x32_bf16 v[220:223], a[216:219], v[170:173], 0\n"
      "v_permlane16_swap_b32 v198, v166\n"
      "v_max_f32 v198, v198, v166\n"
      "v_mov_b32_e64 v166, v198\n"
      "v_cvt_pk_bf16_f32 v92, v206, v207\n"
      "v_pk_fma_f32 v[136:137], v[118:119], v[118:119], v[136:137]\n"
      "v_permlane32_swap_b32 v198, v166\n"
      "v_max_f32 v198, v198, v166\n"
      "v_pk_fma_f32 v[136:137], v[120:121], v[120:121], v[136:137]\n"
      "v_pk_add_f32 v[158:159], v[158:159], v[198:199] op_sel_hi:[1,0] neg_lo:[0,1] neg_hi:[0,1]\n"
      "v_pk_add_f32 v[156:157], v[156:157], v[198:199] op_sel_hi:[1,0] neg_lo:[0,1] neg_hi:[0,1]\n"
      "v_mfma_f32_16x16x32_bf16 v[220:223], a[220:223], v[110:113], v[220:223]\n"
      "s_waitcnt lgkmcnt(0)\n"
      "v_exp_f32 v159, v159\n"
      "v_mfma_f32_16x16x32_bf16 v[220:223], a[224:227], v[162:165], v[220:223]\n"
      "ds_write_b128 v3, v[186:189] offset:18432\n"
      "v_pk_add_f32 v[152:153], v[152:153], v[198:199] op_sel_hi:[1,0] neg_lo:[0,1] neg_hi:[0,1]\n"
      "v_pk_add_f32 v[146:147], v[146:147], v[198:199] op_sel_hi:[1,0] neg_lo:[0,1] neg_hi:[0,1]\n"
      "v_pk_add_f32 v[144:145], v[144:145], v[198:199] op_sel_hi:[1,0] neg_lo:[0,1] neg_hi:[0,1]\n"
      "v_pk_add_f32 v[154:155], v[154:155], v[198:199] op_sel_hi:[1,0] neg_lo:[0,1] neg_hi:[0,1]\n"
      "v_mfma_f32_16x16x32_bf16 v[166:169], a[240:243], v[170:173], 0\n"
      "v_exp_f32 v156, v156\n"
      "v_mfma_f32_16x16x32_bf16 v[166:169], a[244:247], v[110:113], v[166:169]\n"
      "v_exp_f32 v145, v145\n"
      "v_mfma_f32_16x16x32_bf16 v[166:169], a[248:251], v[162:165], v[166:169]\n"
      "v_exp_f32 v157, v157\n"
      "v_mfma_f32_16x16x32_bf16 v[186:189], v[232:235], v[170:173], 0\n"
      "v_exp_f32 v158, v158\n"
      "v_mfma_f32_16x16x32_bf16 v[186:189], v[236:239], v[110:113], v[186:189]\n"
      "v_cvt_pk_bf16_f32 v156, v156, v157\n"
      "v_cvt_pk_bf16_f32 v139, v140, v141\n"
      "v_mfma_f32_16x16x32_bf16 v[186:189], v[240:243], v[162:165], v[186:189]\n"
      "ds_read_b128 v[170:173], v10 offset:1168\n"
      "v_sub_f32 v218, v218, v198\n"
      "v_mfma_f32_16x16x32_bf16 v[162:165], v[32:35], v[178:181], 0\n"
      "v_exp_f32 v144, v144\n"
      "v_mfma_f32_16x16x32_bf16 v[162:165], v[32:35], v[92:95], v[162:165]\n"
      "v_exp_f32_e64 v154, v154\n"
      "v_mfma_f32_16x16x32_bf16 v[110:113], v[194:197], v[178:181], 0\n"
      "ds_read_b64 v[102:103], v10 offset:1184\n"
      "v_cvt_pk_bf16_f32 v157, v158, v159\n"
      "v_mfma_f32_16x16x32_bf16 v[110:113], v[76:79], v[92:95], v[110:113]\n"
      "ds_write_b64 v4, v[138:139] offset:18432\n"
      "v_mfma_f32_16x16x32_bf16 v[198:201], v[96:99], v[178:181], 0\n"
      "v_exp_f32 v152, v152\n"
      "v_mfma_f32_16x16x32_bf16 v[198:201], v[80:83], v[92:95], v[198:201]\n"
      "v_add_f32 v136, v136, v137\n"
      "v_cvt_pk_bf16_f32 v88, v144, v145\n"
      "v_mfma_f32_16x16x32_bf16 v[138:141], v[104:107], v[178:181], 0\n"
      "s_waitcnt lgkmcnt(2)\n"
      "v_exp_f32 v155, v155\n"
      "v_mfma_f32_16x16x32_bf16 v[138:141], v[84:87], v[92:95], v[138:141]\n"
      "ds_read_b128 v[178:181], v2 offset:9728\n"
      "v_cvt_pk_bf16_f32 v159, v154, v155\n"
      "v_mfma_f32_16x16x32_bf16 v[206:209], v[170:173], v[182:185], v[30:33]\n"
      "v_rcp_f32_e64 v162, v162\n"
      "v_mfma_f32_16x16x32_bf16 v[182:185], v[170:173], v[202:205], v[30:33]\n"
      "s_waitcnt lgkmcnt(2)\n"
      "v_exp_f32 v153, v153\n"
      "v_mfma_f32_16x16x32_bf16 v[202:205], v[170:173], v[130:133], v[30:33]\n"
      "ds_read_b128 v[130:133], v2 offset:12800\n"
      "v_cvt_pk_bf16_f32 v158, v152, v153\n"
      "v_mfma_f32_16x16x32_bf16 v[152:155], v[170:173], v[114:117], v[30:33]\n"
      "ds_read_b128 v[170:173], v2 offset:15872\n"
      "v_mul_f32_e64 v201, v162, v201\n"
      "v_mfma_f32_16x16x16_bf16 v[206:209], v[102:103], v[134:135], v[206:209]\n"
      "v_mul_f32 v199, v162, v199\n"
      "v_permlane16_swap_b32 v192, v136\n"
      "v_mfma_f32_16x16x16_bf16 v[182:185], v[102:103], v[108:109], v[182:185]\n"
      "v_exp_f32 v147, v147\n"
      "v_mul_f32 v112, v162, v112\n"
      "v_mfma_f32_16x16x16_bf16 v[202:205], v[102:103], v[160:161], v[202:205]\n"
      "v_exp_f32_e64 v218, v218\n"
      "v_mul_f32_e64 v113, v162, v113\n"
      "v_mfma_f32_16x16x16_bf16 v[152:155], v[102:103], v[100:101], v[152:155]\n"
      "v_exp_f32_e64 v146, v146\n"
      "s_waitcnt lgkmcnt(2)\n"
      "v_mfma_f32_16x16x32_bf16 v[220:223], a[228:231], v[178:181], v[220:223]\n"
      "s_waitcnt lgkmcnt(1)\n"
      "v_cndmask_b32 v206, v206, v182, s[58:59]\n"
      "v_cvt_pk_bf16_f32 v90, v218, 0\n"
      "v_mfma_f32_16x16x32_bf16 v[220:223], a[232:235], v[130:133], v[220:223]\n"
      "s_waitcnt lgkmcnt(0)\n"
      "v_mul_f32 v110, v162, v110\n"
      "v_mul_f32_e64 v198, v162, v198\n"
      "v_mfma_f32_16x16x32_bf16 v[220:223], a[236:239], v[170:173], v[220:223]\n"
      "v_cndmask_b32 v206, v206, v202, s[60:61]\n"
      "v_cndmask_b32 v206, v206, v152, s[62:63]\n"
      "v_mfma_f32_16x16x32_bf16 v[166:169], a[252:255], v[178:181], v[166:169]\n"
      "v_mul_f32 v111, v162, v111\n"
      "v_fmac_f32_e64 v206, s53, v8\n"
      "v_mfma_f32_16x16x32_bf16 v[166:169], v[224:227], v[130:133], v[166:169]\n"
      "s_nop 0\n"
      "v_max_f32_dpp v160, v206, v206 row_ror:8 row_mask:0xf bank_mask:0xf\n"
      "v_cvt_pk_bf16_f32 v110, v110, v111\n"
      "v_mfma_f32_16x16x32_bf16 v[166:169], v[228:231], v[170:173], v[166:169]\n"
      "v_max_f32_dpp v160, v160, v160 row_ror:4 row_mask:0xf bank_mask:0xf\n"
      "v_mul_f32_e64 v200, v162, v200\n"
      "v_mfma_f32_16x16x32_bf16 v[186:189], v[244:247], v[178:181], v[186:189]\n"
      "v_cvt_pk_bf16_f32 v111, v112, v113\n"
      "v_max_f32_dpp v160, v160, v160 row_ror:2 row_mask:0xf bank_mask:0xf\n"
      "v_mfma_f32_16x16x32_bf16 v[186:189], v[248:251], v[130:133], v[186:189]\n"
      "s_nop 0\n"
      "v_max_f32_dpp v160, v160, v160 row_ror:1 row_mask:0xf bank_mask:0xf\n"
      "v_cvt_pk_bf16_f32 v89, v146, v147\n"
      "v_mfma_f32_16x16x32_bf16 v[186:189], v[252:255], v[170:173], v[186:189]\n"
      "v_mul_f32 v161, v221, v221\n"
      "v_max_f32_dpp v160, v160, v160 row_bcast:15 row_mask:0xa bank_mask:0xf\n"
      "v_mfma_f32_16x16x32_bf16 v[152:155], v[32:35], v[156:159], 0\n"
      "v_cvt_pk_bf16_f32 v112, v198, v199\n"
      "v_cvt_pk_bf16_f32 v113, v200, v201\n"
      "v_mfma_f32_16x16x32_bf16 v[152:155], v[32:35], v[88:91], v[152:155]\n"
      "v_max_f32_dpp v160, v160, v160 row_bcast:31 row_mask:0xc bank_mask:0xf\n"
      "v_add_f32_e64 v192, v192, v136\n"
      "v_mfma_f32_16x16x32_bf16 v[200:203], v[194:197], v[156:159], 0\n"
      "v_readlane_b32 s72, v160, 63\n"
      "v_fma_f32 v160, v220, v220, s52\n"
      "v_mfma_f32_16x16x32_bf16 v[200:203], v[76:79], v[88:91], v[200:203]\n"
      "ds_write_b128 v3, v[110:113] offset:18688\n"
      "v_pk_fma_f32 v[160:161], v[222:223], v[222:223], v[160:161]\n"
      "v_mfma_f32_16x16x32_bf16 v[112:115], v[96:99], v[156:159], 0\n"
      "v_rcp_f32 v152, v152\n"
      "v_subrev_f32 v206, s72, v206\n"
      "v_exp_f32_e64 v206, v206\n"
      "v_pk_fma_f32 v[160:161], v[166:167], v[166:167], v[160:161]\n"
      "v_pk_fma_f32 v[160:161], v[168:169], v[168:169], v[160:161]\n"
      "v_pk_mul_f32 v[200:201], v[200:201], v[152:153] op_sel_hi:[1,0]\n"
      "v_pk_mul_f32 v[202:203], v[202:203], v[152:153] op_sel_hi:[1,0]\n"
      "v_cvt_pk_bf16_f32 v206, v206, v206\n"
      "v_mfma_f32_16x16x32_bf16 v[112:115], v[80:83], v[88:91], v[112:115]\n"
      "ds_write_b16 v13, v206\n"
      "v_mfma_f32_16x16x32_bf16 v[108:111], v[104:107], v[156:159], 0\n"
      "ds_read_b128 v[178:181], v14\n"
      "ds_read_b128 v[100:103], v14 offset:64\n"
      "v_cvt_pk_bf16_f32 v200, v200, v201\n"
      "v_cvt_pk_bf16_f32 v201, v202, v203\n"
      "v_pk_mul_f32 v[138:139], v[138:139], v[162:163] op_sel_hi:[1,0]\n"
      "v_pk_fma_f32 v[160:161], v[186:187], v[186:187], v[160:161]\n"
      "v_pk_mul_f32 v[112:113], v[112:113], v[152:153] op_sel_hi:[1,0]\n"
      "v_pk_mul_f32 v[114:115], v[114:115], v[152:153] op_sel_hi:[1,0]\n"
      "v_cvt_pk_bf16_f32 v203, v114, v115\n"
      "v_cvt_pk_bf16_f32 v202, v112, v113\n"
      "v_cvt_pk_bf16_f32 v138, v138, v139\n"
      "v_mfma_f32_16x16x32_bf16 v[108:111], v[84:87], v[88:91], v[108:111]\n"
      "ds_write_b128 v3, v[200:203] offset:18944\n"
      "v_pk_fma_f32 v[160:161], v[188:189], v[188:189], v[160:161]\n"
      "v_add_f32 v160, v160, v161\n"
      "v_mov_b32 v136, v160\n"
      "v_pk_mul_f32 v[140:141], v[140:141], v[162:163] op_sel_hi:[1,0]\n"
      "v_cvt_pk_bf16_f32 v139, v140, v141\n"
      "v_permlane16_swap_b32 v160, v136\n"
      "v_add_f32 v160, v160, v136\n"
      "s_add_u32 s42, s42, 1\n"
      "ds_write_b64 v4, v[138:139] offset:18688\n"
      "v_permlane32_swap_b32 v192, v160\n"
      "v_pk_mul_f32 v[108:109], v[108:109], v[152:153] op_sel_hi:[1,0]\n"
      "v_add_f32_e64 v192, v192, v160\n"
      "v_pk_mul_f32 v[110:111], v[110:111], v[152:153] op_sel_hi:[1,0]\n"
      "s_add_u32 s43, s43, 1\n"
      "ds_write_b32 v31, v192 offset:1024\n"
      "v_cvt_pk_bf16_f32 v108, v108, v109\n"
      "v_cvt_pk_bf16_f32 v109, v110, v111\n"
      "s_waitcnt lgkmcnt(2)\n"
      "v_mfma_f32_16x16x32_bf16 v[160:163], v[178:181], v[32:35], 0\n"
      "ds_write_b64 v4, v[108:109] offset:18944\n"
      "v_add_u32 v11, 2336, v11\n"
      "v_mfma_f32_16x16x32_bf16 v[160:163], v[100:103], v[32:35], v[160:163]\n"
      "v_add_u32 v10, 2336, v10\n"
      "s_waitcnt lgkmcnt(0)\n"
      "s_barrier\n"
      "s_cbranch_vccz L_sel0_%=\n"
      "L_disp0_%=:\n"
      "s_cmp_lt_u32 s95, 0x40000000\n"
      "s_cbranch_scc1 L_nofix_18_%=\n"
      "s_cmp_eq_u32 s95, -2\n"
      "s_cbranch_scc1 L_tail0_%=\n"
      "s_nop 2\n"
      "s_cmp_eq_u32 s45, 0x20000000\n"
      "s_cselect_b32 s66, 0, s66\n"
      "s_cselect_b32 s48, 0, s48\n"
      "s_lshr_b32 s92, s45, 30\n"
      "s_or_b32 s92, s92, s66\n"
      "s_cbranch_scc0 L_nf_19_%=\n"
      "s_bitcmp1_b32 s45, 31\n"
      "s_cselect_b32 s75, 100270464, 0\n"
      "s_bitcmp1_b32 s45, 30\n"
      "s_cselect_b32 s76, 196224, 0\n"
      "s_and_b32 s74, s45, 0x3fffffff\n"
      "s_cmp_ge_u32 s45, 0x40000000\n"
      "s_cselect_b32 s74, s74, 0\n"
      "s_cselect_b32 s45, 0, s45\n"
      "s_sub_u32 s74, s74, s66\n"
      "s_add_u32 s66, s66, s74\n"
      "s_sub_u32 s75, s75, s67\n"
      "s_add_u32 s67, s67, s75\n"
      "s_sub_u32 s76, s76, s68\n"
      "s_add_u32 s68, s68, s76\n"
      "v_add_u32 v16, s74, v16\n"
      "v_add_u32 v17, s74, v17\n"
      "v_add_u32 v18, s74, v18\n"
      "v_add_u32 v19, s74, v19\n"
      "v_add_u32_e64 v15, s74, v15\n"
      "s_cmp_eq_u32 s75, 0\n"
      "s_cbranch_scc1 L_ax_20_%=\n"
      "v_bfe_i32 v136, v27, 0, 1\n"
      "v_and_b32 v136, s75, v136\n"
      "v_sub_u32 v16, v16, v136\n"
      "v_bfe_i32 v136, v27, 2, 1\n"
      "v_and_b32 v136, s75, v136\n"
      "v_sub_u32 v17, v17, v136\n"
      "v_bfe_i32 v136, v27, 4, 1\n"
      "v_and_b32 v136, s75, v136\n"
      "v_sub_u32 v18, v18, v136\n"
      "v_bfe_i32 v136, v27, 6, 1\n"
      "v_and_b32 v136, s75, v136\n"
      "v_sub_u32 v19, v19, v136\n"
      "v_bfe_i32 v136, v27, 8, 1\n"
      "v_and_b32 v136, s75, v136\n"
      "v_sub_u32 v15, v15, v136\n"
      "L_ax_20_%=:\n"
      "s_cmp_eq_u32 s76, 0\n"
      "s_cbranch_scc1 L_ax_21_%=\n"
      "v_bfe_i32 v136, v27, 1, 1\n"
      "v_and_b32 v136, s76, v136\n"
      "v_sub_u32 v16, v16, v136\n"
      "v_bfe_i32 v136, v27, 3, 1\n"
      "v_and_b32 v136, s76, v136\n"
      "v_sub_u32 v17, v17, v136\n"
      "v_bfe_i32 v136, v27, 5, 1\n"
      "v_and_b32 v136, s76, v136\n"
      "v_sub_u32 v18, v18, v136\n"
      "v_bfe_i32 v136, v27, 7, 1\n"
      "v_and_b32 v136, s76, v136\n"
      "v_sub_u32 v19, v19, v136\n"
      "v_bfe_i32 v136, v27, 9, 1\n"
      "v_and_b32 v136, s76, v136\n"
      "v_sub_u32 v15, v15, v136\n"
      "L_ax_21_%=:\n"
      "s_or_b32 s48, s66, s69\n"
      "s_cmp_lg_u32 s48, 0\n"
      "s_cselect_b32 s48, 0x40000000, 0\n"
      "L_nf_19_%=:\n"
      "s_lshr_b32 s92, s46, 30\n"
      "s_or_b32 s92, s92, s69\n"
      "s_cbranch_scc0 L_nf_22_%=\n"
      "s_bitcmp1_b32 s46, 31\n"
      "s_cselect_b32 s75, 100270464, 0\n"
      "s_bitcmp1_b32 s46, 30\n"
      "s_cselect_b32 s76, 196224, 0\n"
      "s_and_b32 s74, s46, 0x3fffffff\n"
      "s_cmp_ge_u32 s46, 0x40000000\n"
      "s_cselect_b32 s74, s74, 0\n"
      "s_cselect_b32 s46, 0, s46\n"
      "s_sub_u32 s74, s74, s69\n"
      "s_add_u32 s69, s69, s74\n"
      "s_sub_u32 s75, s75, s70\n"
      "s_add_u32 s70, s70, s75\n"
      "s_sub_u32 s76, s76, s71\n"
      "s_add_u32 s71, s71, s76\n"
      "v_add_u32 v20, s74, v20\n"
      "v_add_u32 v21, s74, v21\n"
      "v_add_u32 v22, s74, v22\n"
      "v_add_u32 v23, s74, v23\n"
      "v_add_u32 v25, s74, v25\n"
      "s_cmp_eq_u32 s75, 0\n"
      "s_cbranch_scc1 L_ax_23_%=\n"
      "v_bfe_i32 v136, v27, 10, 1\n"
      "v_and_b32 v136, s75, v136\n"
      "v_sub_u32 v20, v20, v136\n"
      "v_bfe_i32 v136, v27, 12, 1\n"
      "v_and_b32 v136, s75, v136\n"
      "v_sub_u32 v21, v21, v136\n"
      "v_bfe_i32 v136, v27, 14, 1\n"
      "v_and_b32 v136, s75, v136\n"
      "v_sub_u32 v22, v22, v136\n"
      "v_bfe_i32 v136, v27, 16, 1\n"
      "v_and_b32 v136, s75, v136\n"
      "v_sub_u32 v23, v23, v136\n"
      "v_bfe_i32 v136, v27, 18, 1\n"
      "v_and_b32 v136, s75, v136\n"
      "v_sub_u32 v25, v25, v136\n"
      "L_ax_23_%=:\n"
      "s_cmp_eq_u32 s76, 0\n"
      "s_cbranch_scc1 L_ax_24_%=\n"
      "v_bfe_i32 v136, v27, 11, 1\n"
      "v_and_b32 v136, s76, v136\n"
      "v_sub_u32 v20, v20, v136\n"
      "v_bfe_i32 v136, v27, 13, 1\n"
      "v_and_b32 v136, s76, v136\n"
      "v_sub_u32 v21, v21, v136\n"
      "v_bfe_i32 v136, v27, 15, 1\n"
      "v_and_b32 v136, s76, v136\n"
      "v_sub_u32 v22, v22, v136\n"
      "v_bfe_i32 v136, v27, 17, 1\n"
      "v_and_b32 v136, s76, v136\n"
      "v_sub_u32 v23, v23, v136\n"
      "v_bfe_i32 v136, v27, 19, 1\n"
      "v_and_b32 v136, s76, v136\n"
      "v_sub_u32 v25, v25, v136\n"
      "L_ax_24_%=:\n"
      "s_or_b32 s48, s66, s69\n"
      "s_cmp_lg_u32 s48, 0\n"
      "s_cselect_b32 s48, 0x40000000, 0\n"
      "L_nf_22_%=:\n"
      "s_and_b32 s4, s45, s8\n"
      "s_sub_u32 s92, s42, 1\n"
      "s_cmp_gt_u32 s92, s44\n"
      "s_cbranch_scc1 L_tail0_%=\n"
      "L_nofix_18_%=:\n"
      "s_cmp_lg_u64 s[56:57], 0\n"
      "s_cbranch_scc1 L_slow0_%=\n"
      "L_sel0_%=:\n"
      "s_cmp_lg_u64 s[54:55], 0\n"
      "s_cbranch_scc1 L_max0_%=\n"
      "s_branch L_loop0_%=\n"
      "L_slow0_%=:\n"
      "s_nop 4\n"
      "v_mfma_f32_16x16x32_bf16 v[112:115], v[178:181], v[194:197], 0\n"
      "ds_read_b128 v[136:139], v0\n"
      "v_subrev_u32 v195, 115456, v9\n"
      "v_mfma_f32_16x16x32_bf16 v[112:115], v[100:103], v[76:79], v[112:115]\n"
      "s_mov_b32 m0, 110592\n"
      "ds_read_b128 v[196:199], v9 offset:1024\n"
      "v_mfma_f32_16x16x32_bf16 v[126:129], v[178:181], v[96:99], 0\n"
      "ds_read_b128 v[108:111], v1\n"
      "s_add_u32 s72, s42, 1\n"
      "v_mfma_f32_16x16x32_bf16 v[126:129], v[100:103], v[80:83], v[126:129]\n"
      "ds_read_b128 v[132:135], v0 offset:6144\n"
      "v_readlane_b32 s77, v38, s72\n"
      "v_mfma_f32_16x16x32_bf16 v[152:155], v[178:181], v[104:107], 0\n"
      "ds_read_b128 v[204:207], v1 offset:6144\n"
      "s_and_b32 s73, s77, -16\n"
      "v_mfma_f32_16x16x32_bf16 v[152:155], v[100:103], v[84:87], v[152:155]\n"
      "buffer_load_dwordx4 v195, s[28:31], s73 offen  lds\n"
      "s_waitcnt lgkmcnt(4)\n"
      "v_mfma_f32_16x16x32_bf16 v[178:181], a[0:3], v[136:139], 0\n"
      "ds_read_b128 v[100:103], v1 offset:8192\n"
      "v_subrev_u32 v8, 115456, v9\n"
      "v_mfma_f32_16x16x32_bf16 v[128:131], a[24:27], v[136:139], 0\n"
      "s_waitcnt lgkmcnt(4)\n"
      "v_readlane_b32 s84, v37, s43\n"
      "v_add_f32 v198, v198, v199\n"
      "v_mfma_f32_16x16x32_bf16 v[156:159], a[48:51], v[136:139], 0\n"
      "s_add_u32 s73, s73, 1024\n"
      "v_add_f32_e64 v196, v196, v197\n"
      "v_add_f32_e64 v196, v196, v198\n"
      "v_mfma_f32_16x16x32_bf16 v[182:185], a[72:75], v[136:139], 0\n"
      "s_mov_b32 m0, 111616\n"
      "v_rsq_f32 v196, v196\n"
      "v_mfma_f32_16x16x32_bf16 v[144:147], a[96:99], v[136:139], 0\n"
      "buffer_load_dwordx4 v195, s[28:31], s73 offen  lds\n"
      "v_mfma_f32_16x16x32_bf16 v[140:143], a[120:123], v[136:139], 0\n"
      "s_waitcnt lgkmcnt(1)\n"
      "v_mul_f32 v196, 0x415db3d7, v196\n"
      "v_subrev_u32 v113, 115456, v9\n"
      "v_mfma_f32_16x16x32_bf16 v[178:181], a[4:7], v[108:111], v[178:181]\n"
      "ds_read_b128 v[170:173], v1 offset:12288\n"
      "v_mov_b32 v198, v196\n"
      "v_mfma_f32_16x16x32_bf16 v[178:181], a[8:11], v[132:135], v[178:181]\n"
      "s_mov_b32 m0, 112640\n"
      "v_permlane32_swap_b32 v196, v198\n"
      "v_mfma_f32_16x16x32_bf16 v[128:131], a[28:31], v[108:111], v[128:131]\n"
      "s_add_u32 s73, s73, 1024\n"
      "v_mov_b32 v197, v196\n"
      "v_mul_f32 v168, v198, v168\n"
      "v_mfma_f32_16x16x32_bf16 v[128:131], a[32:35], v[132:135], v[128:131]\n"
      "buffer_load_dwordx4 v195, s[28:31], s73 offen  lds\n"
      "v_mfma_f32_16x16x32_bf16 v[156:159], a[52:55], v[108:111], v[156:159]\n"
      "ds_read_b128 v[190:193], v0 offset:12288\n"
      "v_mul_f32_e64 v221, v198, v221\n"
      "v_mfma_f32_16x16x32_bf16 v[156:159], a[56:59], v[132:135], v[156:159]\n"
      "s_add_u32 s73, s73, 1024\n"
      "v_permlane16_swap_b32 v196, v197\n"
      "v_mfma_f32_16x16x32_bf16 v[182:185], a[76:79], v[108:111], v[182:185]\n"
      "s_mov_b32 m0, 113664\n"
      "v_mul_f32 v119, v197, v119\n"
      "v_mul_f32 v151, v196, v151\n"
      "v_mfma_f32_16x16x32_bf16 v[182:185], a[80:83], v[132:135], v[182:185]\n"
      "v_mul_f32 v177, v196, v177\n"
      "v_mul_f32 v118, v197, v118\n"
      "v_mfma_f32_16x16x32_bf16 v[144:147], a[100:103], v[108:111], v[144:147]\n"
      "v_mul_f32 v214, v196, v214\n"
      "v_mul_f32 v212, v197, v212\n"
      "v_mfma_f32_16x16x32_bf16 v[144:147], a[104:107], v[132:135], v[144:147]\n"
      "ds_read_b128 v[200:203], v0 offset:2048\n"
      "v_mul_f32 v176, v196, v176\n"
      "v_mfma_f32_16x16x32_bf16 v[140:143], a[124:127], v[108:111], v[140:143]\n"
      "buffer_load_dwordx4 v195, s[28:31], s73 offen  lds\n"
      "v_mfma_f32_16x16x32_bf16 v[140:143], a[128:131], v[132:135], v[140:143]\n"
      "s_waitcnt lgkmcnt(1)\n"
      "v_mul_f32 v150, v196, v150\n"
      "v_mul_f32_e64 v220, v198, v220\n"
      "v_mfma_f32_16x16x32_bf16 v[178:181], a[12:15], v[204:207], v[178:181]\n"
      "s_add_u32 s73, s73, 1024\n"
      "v_mul_f32 v187, v198, v187\n"
      "v_mul_f32 v223, v198, v223\n"
      "v_mfma_f32_16x16x32_bf16 v[178:181], a[16:19], v[190:193], v[178:181]\n"
      "ds_read_b128 v[114:117], v9 offset:5632\n"
      "v_mul_f32 v175, v196, v175\n"
      "v_mfma_f32_16x16x32_bf16 v[178:181], a[20:23], v[170:173], v[178:181]\n"
      "s_mov_b32 m0, 114688\n"
      "v_mul_f32 v174, v196, v174\n"
      "v_mul_f32 v215, v196, v215\n"
      "v_mfma_f32_16x16x32_bf16 v[128:131], a[36:39], v[204:207], v[128:131]\n"
      "v_cvt_pk_bf16_f32 v174, v174, v175\n"
      "v_cvt_pk_bf16_f32 v175, v176, v177\n"
      "v_mfma_f32_16x16x32_bf16 v[128:131], a[40:43], v[190:193], v[128:131]\n"
      "s_mov_b32 exec_hi, 0xffff\n"
      "buffer_load_dwordx4 v195, s[28:31], s73 offen  lds\n"
      "s_mov_b32 exec_hi, -1\n"
      "v_pk_mul_f32 v[210:211], v[210:211], v[196:197] op_sel:[0,1]\n"
      "v_pk_mul_f32 v[188:189], v[188:189], v[198:199] op_sel_hi:[1,0]\n"
      "v_pk_mul_f32 v[124:125], v[124:125], v[196:197] op_sel:[0,1]\n"
      "v_pk_mul_f32 v[216:217], v[216:217], v[196:197] op_sel_hi:[1,0]\n"
      "v_pk_mul_f32 v[120:121], v[120:121], v[196:197] op_sel:[0,1]\n"
      "v_mfma_f32_16x16x32_bf16 v[128:131], a[44:47], v[170:173], v[128:131]\n"
      "v_cvt_pk_bf16_f32 v210, v210, v211\n"
      "v_cvt_pk_bf16_f32 v177, v216, v217\n"
      "v_mfma_f32_16x16x32_bf16 v[156:159], a[60:63], v[204:207], v[156:159]\n"
      "s_add_u32 m0, s64, 18432\n"
      "v_lshrrev_b32_e64 v8, 3, v8\n"
      "v_cvt_pk_bf16_f32 v176, v214, v215\n"
      "v_mfma_f32_16x16x32_bf16 v[156:159], a[64:67], v[190:193], v[156:159]\n"
      "ds_write_b128 v5, v[174:177]\n"
      "v_pk_mul_f32 v[122:123], v[122:123], v[196:197] op_sel:[0,1]\n"
      "v_mfma_f32_16x16x32_bf16 v[156:159], a[68:71], v[170:173], v[156:159]\n"
      "buffer_load_dwordx4 v16, s[16:19], s45 offen  lds\n"
      "v_mfma_f32_16x16x32_bf16 v[182:185], a[84:87], v[204:207], v[182:185]\n"
      "s_and_b32 s77, s77, 15\n"
      "v_fma_f32 v199, v178, v178, s51\n"
      "v_mul_f32 v148, v196, v148\n"
      "v_mfma_f32_16x16x32_bf16 v[182:185], a[88:91], v[190:193], v[182:185]\n"
      "s_add_u32 m0, s64, 22528\n"
      "v_mul_f32 v213, v197, v213\n"
      "v_cvt_pk_bf16_f32 v118, v118, v119\n"
      "v_mfma_f32_16x16x32_bf16 v[182:185], a[92:95], v[170:173], v[182:185]\n"
      "v_mul_f32 v186, v198, v186\n"
      "v_cvt_pk_bf16_f32 v211, v212, v213\n"
      "v_mfma_f32_16x16x32_bf16 v[144:147], a[108:111], v[204:207], v[144:147]\n"
      "v_mul_f32 v149, v196, v149\n"
      "v_mul_f32 v169, v198, v169\n"
      "v_mfma_f32_16x16x32_bf16 v[144:147], a[112:115], v[190:193], v[144:147]\n"
      "buffer_load_dwordx4 v17, s[16:19], s45 offen  lds\n"
      "v_mfma_f32_16x16x32_bf16 v[144:147], a[116:119], v[170:173], v[144:147]\n"
      "ds_read_b128 v[174:177], v0 offset:14336\n"
      "v_cvt_pk_bf16_f32 v220, v220, v221\n"
      "v_mfma_f32_16x16x32_bf16 v[140:143], a[132:135], v[204:207], v[140:143]\n"
      "v_mul_f32 v166, v198, v166\n"
      "v_fmac_f32 v199, v179, v179\n"
      "v_mfma_f32_16x16x32_bf16 v[140:143], a[136:139], v[190:193], v[140:143]\n"
      "v_cvt_pk_bf16_f32 v119, v120, v121\n"
      "v_cvt_pk_bf16_f32 v213, v124, v125\n"
      "v_mfma_f32_16x16x32_bf16 v[140:143], a[140:143], v[170:173], v[140:143]\n"
      "s_add_u32 m0, s64, 26624\n"
      "v_fma_f32 v127, v182, v182, s51\n"
      "v_cvt_pk_bf16_f32 v212, v122, v123\n"
      "v_mfma_f32_16x16x32_bf16 v[194:197], v[136:139], a[144:147], 0\n"
      "buffer_load_dwordx4 v18, s[16:19], s45 offen  lds\n"
      "v_mfma_f32_16x16x32_bf16 v[194:197], v[108:111], a[148:151], v[194:197]\n"
      "ds_write_b128 v5, v[210:213] offset:6144\n"
      "v_mfma_f32_16x16x32_bf16 v[194:197], v[132:135], a[152:155], v[194:197]\n"
      "s_add_u32 m0, s64, 30720\n"
      "v_fmac_f32 v127, v183, v183\n"
      "v_fmac_f32 v127, v184, v184\n"
      "v_mfma_f32_16x16x32_bf16 v[194:197], v[204:207], a[156:159], v[194:197]\n"
      "v_min_u32 v8, 96, v8\n"
      "v_fmac_f32_e64 v127, v185, v185\n"
      "v_mfma_f32_16x16x32_bf16 v[194:197], v[190:193], a[160:163], v[194:197]\n"
      "ds_write_b64 v6, v[118:119] offset:6144\n"
      "v_mfma_f32_16x16x32_bf16 v[194:197], v[170:173], a[164:167], v[194:197]\n"
      "buffer_load_dwordx4 v19, s[16:19], s45 offen  lds\n"
      "v_mfma_f32_16x16x32_bf16 v[96:99], v[136:139], a[168:171], 0\n"
      "s_mov_b32 m0, s7\n"
      "v_cndmask_b32 v112, v112, v126, s[58:59]\n"
      "v_fmac_f32 v199, v180, v180\n"
      "v_mfma_f32_16x16x32_bf16 v[96:99], v[108:111], a[172:175], v[96:99]\n"
      "v_cvt_pk_bf16_f32 v148, v148, v149\n"
      "v_fmac_f32 v127, v144, v144\n"
      "v_mfma_f32_16x16x32_bf16 v[96:99], v[132:135], a[176:179], v[96:99]\n"
      "ds_read_b128 v[214:217], v1 offset:2048\n"
      "v_mul_f32 v222, v198, v222\n"
      "v_mfma_f32_16x16x32_bf16 v[96:99], v[204:207], a[180:183], v[96:99]\n"
      "ds_read_b128 v[210:213], v1 offset:14336\n"
      "v_fmac_f32_e64 v127, v145, v145\n"
      "v_mfma_f32_16x16x32_bf16 v[96:99], v[190:193], a[184:187], v[96:99]\n"
      "buffer_load_dwordx4 v15, s[0:3], s4 offen  lds\n"
      "v_mfma_f32_16x16x32_bf16 v[96:99], v[170:173], a[188:191], v[96:99]\n"
      "v_cvt_pk_bf16_f32 v149, v150, v151\n"
      "v_cvt_pk_bf16_f32 v186, v186, v187\n"
      "v_mfma_f32_16x16x32_bf16 v[104:107], v[136:139], a[192:195], 0\n"
      "ds_read_b128 v[122:125], v0 offset:8192\n"
      "v_readlane_b32 s86, v38, s43\n"
      "v_mfma_f32_16x16x32_bf16 v[104:107], v[108:111], a[196:199], v[104:107]\n"
      "v_fmac_f32 v199, v181, v181\n"
      "v_fmac_f32 v199, v128, v128\n"
      "v_mfma_f32_16x16x32_bf16 v[104:107], v[132:135], a[200:203], v[104:107]\n"
      "ds_write_b64 v6, v[148:149]\n"
      "v_mfma_f32_16x16x32_bf16 v[104:107], v[204:207], a[204:207], v[104:107]\n"
      "v_cvt_pk_bf16_f32 v221, v222, v223\n"
      "v_fmac_f32 v127, v146, v146\n"
      "v_mfma_f32_16x16x32_bf16 v[104:107], v[190:193], a[208:211], v[104:107]\n"
      "buffer_load_dwordx4 v[162:165], v36, s[28:31], s47 offen\n"
      "v_mfma_f32_16x16x32_bf16 v[104:107], v[170:173], a[212:215], v[104:107]\n"
      "s_waitcnt lgkmcnt(2)\n"
      "v_fmac_f32 v199, v129, v129\n"
      "v_fmac_f32 v127, v147, v147\n"
      "v_mfma_f32_16x16x32_bf16 v[170:173], a[0:3], v[200:203], 0\n"
      "v_or3_b32 v114, v114, v115, v116\n"
      "v_bfe_u32 v84, v113, 4, 4\n"
      "v_mfma_f32_16x16x32_bf16 v[170:173], a[4:7], v[214:217], v[170:173]\n"
      "s_waitcnt lgkmcnt(1)\n"
      "v_mov_b32_dpp v160, v160 quad_perm:[0,0,0,0] row_mask:0xf bank_mask:0xf\n"
      "v_fmac_f32 v127, v140, v140\n"
      "v_mfma_f32_16x16x32_bf16 v[170:173], a[8:11], v[122:125], v[170:173]\n"
      "v_cvt_pk_bf16_f32 v223, v168, v169\n"
      "v_cndmask_b32 v112, v112, v152, s[60:61]\n"
      "v_mfma_f32_16x16x32_bf16 v[170:173], a[12:15], v[100:103], v[170:173]\n"
      "ds_read_b128 v[148:151], v0 offset:4096\n"
      "v_fmac_f32_e64 v199, v130, v130\n"
      "v_mfma_f32_16x16x32_bf16 v[170:173], a[16:19], v[174:177], v[170:173]\n"
      "buffer_load_dword v78, v29, s[28:31], s47 offen\n"
      "v_mfma_f32_16x16x32_bf16 v[170:173], a[20:23], v[210:213], v[170:173]\n"
      "v_cvt_pk_bf16_f32 v187, v188, v189\n"
      "v_fmac_f32 v199, v131, v131\n"
      "v_mfma_f32_16x16x32_bf16 v[118:121], a[24:27], v[200:203], 0\n"
      "v_fmac_f32 v127, v141, v141\n"
      "v_lshrrev_b32_e64 v113, 8, v113\n"
      "v_mfma_f32_16x16x32_bf16 v[118:121], a[28:31], v[214:217], v[118:121]\n"
      "v_mul_f32_e64 v167, v198, v167\n"
      "v_cvt_pk_bf16_f32 v222, v166, v167\n"
      "v_mfma_f32_16x16x32_bf16 v[118:121], a[32:35], v[122:125], v[118:121]\n"
      "ds_write_b128 v5, v[220:223] offset:12288\n"
      "v_mfma_f32_16x16x32_bf16 v[118:121], a[36:39], v[100:103], v[118:121]\n"
      "v_fmac_f32_e64 v199, v156, v156\n"
      "v_fma_f32 v126, v170, v170, s51\n"
      "v_mfma_f32_16x16x32_bf16 v[118:121], a[40:43], v[174:177], v[118:121]\n"
      "v_cvt_pk_bf16_f32 v194, v194, v195\n"
      "v_fmac_f32_e64 v199, v157, v157\n"
      "v_mfma_f32_16x16x32_bf16 v[118:121], a[44:47], v[210:213], v[118:121]\n"
      "ds_write_b64 v6, v[186:187] offset:12288\n"
      "v_mfma_f32_16x16x32_bf16 v[220:223], a[48:51], v[200:203], 0\n"
      "v_fmac_f32 v127, v142, v142\n"
      "v_fmac_f32 v199, v158, v158\n"
      "v_mfma_f32_16x16x32_bf16 v[220:223], a[52:55], v[214:217], v[220:223]\n"
      "v_fmac_f32 v127, v143, v143\n"
      "v_fmac_f32 v126, v171, v171\n"
      "v_mfma_f32_16x16x32_bf16 v[220:223], a[56:59], v[122:125], v[220:223]\n"
      "v_cvt_pk_bf16_f32 v104, v104, v105\n"
      "v_fmac_f32_e64 v199, v159, v159\n"
      "v_mfma_f32_16x16x32_bf16 v[220:223], a[60:63], v[100:103], v[220:223]\n"
      "s_nop 0\n"
      "v_permlane16_swap_b32 v199, v127\n"
      "v_mfma_f32_16x16x32_bf16 v[220:223], a[64:67], v[174:177], v[220:223]\n"
      "v_fmac_f32 v126, v172, v172\n"
      "v_fmac_f32 v126, v173, v173\n"
      "v_mfma_f32_16x16x32_bf16 v[220:223], a[68:71], v[210:213], v[220:223]\n"
      "v_mul_u32_u24 v208, 98, v84\n"
      "v_add_f32 v199, v199, v127\n"
      "v_mfma_f32_16x16x32_bf16 v[152:155], a[72:75], v[200:203], 0\n"
      "v_mov_b32 v127, v199\n"
      "v_cvt_pk_bf16_f32 v105, v106, v107\n"
      "v_mfma_f32_16x16x32_bf16 v[152:155], a[76:79], v[214:217], v[152:155]\n"
      "v_bitop3_b32 v114, v114, v117, s33 bitop3:0xa8\n"
      "v_cvt_pk_bf16_f32 v195, v196, v197\n"
      "v_mfma_f32_16x16x32_bf16 v[152:155], a[80:83], v[122:125], v[152:155]\n"
      "v_permlane32_swap_b32 v199, v127\n"
      "v_mfma_f32_16x16x32_bf16 v[152:155], a[84:87], v[100:103], v[152:155]\n"
      "v_readlane_b32 s85, v37, s42\n"
      "v_add_f32 v199, v199, v127\n"
      "v_mfma_f32_16x16x32_bf16 v[152:155], a[88:91], v[174:177], v[152:155]\n"
      "v_rsq_f32 v199, v199\n"
      "v_mfma_f32_16x16x32_bf16 v[152:155], a[92:95], v[210:213], v[152:155]\n"
      "v_cvt_pk_bf16_f32 v96, v96, v97\n"
      "v_mov_b32 v127, v199\n"
      "v_mfma_f32_16x16x32_bf16 v[166:169], a[96:99], v[200:203], 0\n"
      "v_fmac_f32 v126, v118, v118\n"
      "v_lshl_add_u32 v208, v113, 3, v208\n"
      "v_mfma_f32_16x16x32_bf16 v[166:169], a[100:103], v[214:217], v[166:169]\n"
      "v_rcp_f32 v160, v160\n"
      "v_mfma_f32_16x16x32_bf16 v[166:169], a[104:107], v[122:125], v[166:169]\n"
      "v_fmac_f32 v126, v119, v119\n"
      "v_fma_f32 v197, v152, v152, s51\n"
      "v_mfma_f32_16x16x32_bf16 v[166:169], a[108:111], v[100:103], v[166:169]\n"
      "v_permlane16_swap_b32 v199, v127\n"
      "v_mfma_f32_16x16x32_bf16 v[166:169], a[112:115], v[174:177], v[166:169]\n"
      "v_fmac_f32 v197, v153, v153\n"
      "v_mul_f32 v140, v127, v140\n"
      "v_mfma_f32_16x16x32_bf16 v[166:169], a[116:119], v[210:213], v[166:169]\n"
      "v_cmp_ne_u32 s[56:57], 0, v114\n"
      "v_fmac_f32 v126, v120, v120\n"
      "v_mfma_f32_16x16x32_bf16 v[204:207], a[120:123], v[200:203], 0\n"
      "v_mul_f32 v183, v127, v183\n"
      "v_lshlrev_b32_e64 v113, 2, v113\n"
      "v_mfma_f32_16x16x32_bf16 v[204:207], a[124:127], v[214:217], v[204:207]\n"
      "ds_read_b128 v[186:189], v1 offset:4096\n"
      "v_mul_f32 v182, v127, v182\n"
      "v_mfma_f32_16x16x32_bf16 v[204:207], a[128:131], v[122:125], v[204:207]\n"
      "v_fmac_f32 v197, v154, v154\n"
      "v_sub_u32_e64 v84, v84, v113\n"
      "v_mfma_f32_16x16x32_bf16 v[204:207], a[132:135], v[100:103], v[204:207]\n"
      "s_or_b32 s95, s84, s85\n"
      "v_cvt_pk_bf16_f32 v97, v98, v99\n"
      "v_add_u32 v208, s77, v208\n"
      "v_mfma_f32_16x16x32_bf16 v[204:207], a[136:139], v[174:177], v[204:207]\n"
      "v_mul_f32 v112, v160, v112\n"
      "v_fmac_f32 v126, v121, v121\n"
      "v_mfma_f32_16x16x32_bf16 v[204:207], a[140:143], v[210:213], v[204:207]\n"
      "v_fmac_f32 v197, v155, v155\n"
      "v_fmac_f32 v197, v166, v166\n"
      "v_mfma_f32_16x16x32_bf16 v[106:109], v[200:203], a[144:147], 0\n"
      "v_fmac_f32 v197, v167, v167\n"
      "v_mfma_f32_16x16x32_bf16 v[106:109], v[214:217], a[148:151], v[106:109]\n"
      "v_cvt_pk_bf16_f32 v112, v112, v112\n"
      "v_fmac_f32 v126, v220, v220\n"
      "v_mfma_f32_16x16x32_bf16 v[106:109], v[122:125], a[152:155], v[106:109]\n"
      "ds_read_b128 v[114:117], v0 offset:16384\n"
      "v_fmac_f32 v197, v168, v168\n"
      "v_mfma_f32_16x16x32_bf16 v[106:109], v[100:103], a[156:159], v[106:109]\n"
      "v_mul_f32 v185, v127, v185\n"
      "v_mul_f32_e64 v199, s50, v199\n"
      "v_mfma_f32_16x16x32_bf16 v[106:109], v[174:177], a[160:163], v[106:109]\n"
      "v_fmac_f32 v126, v221, v221\n"
      "v_fmac_f32 v197, v169, v169\n"
      "v_mfma_f32_16x16x32_bf16 v[106:109], v[210:213], a[164:167], v[106:109]\n"
      "v_fmac_f32 v197, v204, v204\n"
      "v_fmac_f32 v197, v205, v205\n"
      "v_mfma_f32_16x16x32_bf16 v[136:139], v[200:203], a[168:171], 0\n"
      "v_fmac_f32 v126, v222, v222\n"
      "v_mul_f32 v178, v199, v178\n"
      "v_mfma_f32_16x16x32_bf16 v[136:139], v[214:217], a[172:175], v[136:139]\n"
      "v_mul_f32 v179, v199, v179\n"
      "v_mul_f32 v156, v199, v156\n"
      "v_mfma_f32_16x16x32_bf16 v[136:139], v[122:125], a[176:179], v[136:139]\n"
      "v_fmac_f32 v126, v223, v223\n"
      "v_fmac_f32 v197, v206, v206\n"
      "v_mfma_f32_16x16x32_bf16 v[136:139], v[100:103], a[180:183], v[136:139]\n"
      "v_mul_f32 v181, v199, v181\n"
      "v_mul_f32 v141, v127, v141\n"
      "v_mfma_f32_16x16x32_bf16 v[136:139], v[174:177], a[184:187], v[136:139]\n"
      "ds_read_b128 v[132:135], v0 offset:10240\n"
      "v_fmac_f32 v197, v207, v207\n"
      "v_mfma_f32_16x16x32_bf16 v[136:139], v[210:213], a[188:191], v[136:139]\n"
      "s_nop 0\n"
      "v_permlane16_swap_b32 v126, v197\n"
      "v_mfma_f32_16x16x32_bf16 v[190:193], v[200:203], a[192:195], 0\n"
      "ds_read_b128 v[200:203], v1 offset:10240\n"
      "v_lshlrev_b32 v84, 4, v84\n"
      "v_mfma_f32_16x16x32_bf16 v[190:193], v[214:217], a[196:199], v[190:193]\n"
      "v_add_f32 v126, v126, v197\n"
      "v_mov_b32 v198, 0x3fb9\n"
      "v_mfma_f32_16x16x32_bf16 v[190:193], v[122:125], a[200:203], v[190:193]\n"
      "ds_read_b128 v[214:217], v1 offset:16384\n"
      "v_mul_f32 v157, v199, v157\n"
      "v_mfma_f32_16x16x32_bf16 v[190:193], v[100:103], a[204:207], v[190:193]\n"
      "v_mul_f32 v159, v199, v159\n"
      "v_mul_f32 v145, v127, v145\n"
      "v_mfma_f32_16x16x32_bf16 v[190:193], v[174:177], a[208:211], v[190:193]\n"
      "s_waitcnt lgkmcnt(2)\n"
      "v_mul_f32 v184, v127, v184\n"
      "v_mov_b32 v197, v126\n"
      "v_mfma_f32_16x16x32_bf16 v[190:193], v[210:213], a[212:215], v[190:193]\n"
      "s_nop 0\n"
      "v_permlane32_swap_b32 v126, v197\n"
      "v_mfma_f32_16x16x32_bf16 v[100:103], a[0:3], v[148:151], 0\n"
      "v_add_f32 v126, v126, v197\n"
      "v_cvt_pk_bf16_f32 v140, v140, v141\n"
      "v_mfma_f32_16x16x32_bf16 v[100:103], a[4:7], v[186:189], v[100:103]\n"
      "v_cvt_pk_bf16_f32 v182, v182, v183\n"
      "v_mul_f32_e64 v180, v199, v180\n"
      "v_mfma_f32_16x16x32_bf16 v[100:103], a[8:11], v[132:135], v[100:103]\n"
      "s_waitcnt lgkmcnt(0)\n"
      "v_rsq_f32 v126, v126\n"
      "v_mfma_f32_16x16x32_bf16 v[100:103], a[12:15], v[200:203], v[100:103]\n"
      "v_mul_f32 v158, v199, v158\n"
      "v_mul_f32 v128, v199, v128\n"
      "v_mfma_f32_16x16x32_bf16 v[100:103], a[16:19], v[114:117], v[100:103]\n"
      "v_cvt_pk_bf16_f32 v183, v184, v185\n"
      "v_mov_b32 v197, v126\n"
      "v_mfma_f32_16x16x32_bf16 v[100:103], a[20:23], v[214:217], v[100:103]\n"
      "s_nop 0\n"
      "v_permlane16_swap_b32 v126, v197\n"
      "v_mfma_f32_16x16x32_bf16 v[210:213], a[24:27], v[148:151], 0\n"
      "ds_write_b16 v12, v112 offset:16\n"
      "v_mfma_f32_16x16x32_bf16 v[210:213], a[28:31], v[186:189], v[210:213]\n"
      "v_mul_f32 v126, s50, v126\n"
      "v_mul_f32 v144, v127, v144\n"
      "v_mfma_f32_16x16x32_bf16 v[210:213], a[32:35], v[132:135], v[210:213]\n"
      "v_mul_f32 v205, v197, v205\n"
      "v_fma_f32 v77, v100, v100, s51\n"
      "v_mfma_f32_16x16x32_bf16 v[210:213], a[36:39], v[200:203], v[210:213]\n"
      "v_cvt_pk_bf16_f32 v196, v106, v107\n"
      "v_mul_f32 v129, v199, v129\n"
      "v_mfma_f32_16x16x32_bf16 v[210:213], a[40:43], v[114:117], v[210:213]\n"
      "s_or_b32 s95, s95, s48\n"
      "v_fmac_f32 v77, v101, v101\n"
      "v_cvt_pk_bf16_f32 v156, v156, v157\n"
      "v_mfma_f32_16x16x32_bf16 v[210:213], a[44:47], v[214:217], v[210:213]\n"
      "v_cvt_pk_bf16_f32 v184, v144, v145\n"
      "v_mul_f32 v171, v126, v171\n"
      "v_mul_f32 v154, v197, v154\n"
      "v_fmac_f32 v77, v102, v102\n"
      "v_cvt_pk_bf16_f32 v106, v190, v191\n"
      "v_pk_mul_f32 v[166:167], v[166:167], v[196:197] op_sel:[0,1]\n"
      "v_pk_mul_f32 v[142:143], v[142:143], v[126:127] op_sel:[0,1]\n"
      "v_pk_mul_f32 v[130:131], v[130:131], v[198:199] op_sel:[0,1]\n"
      "v_pk_mul_f32 v[146:147], v[146:147], v[126:127] op_sel:[0,1]\n"
      "v_pk_mul_f32 v[168:169], v[168:169], v[196:197] op_sel:[0,1]\n"
      "v_pk_mul_f32 v[152:153], v[152:153], v[196:197] op_sel:[0,1]\n"
      "v_pk_mul_f32 v[220:221], v[220:221], v[126:127] op_sel_hi:[1,0]\n"
      "v_pk_mul_f32 v[120:121], v[120:121], v[126:127] op_sel_hi:[1,0]\n"
      "v_pk_mul_f32 v[118:119], v[118:119], v[126:127] op_sel_hi:[1,0]\n"
      "v_mfma_f32_16x16x32_bf16 v[110:113], a[48:51], v[148:151], 0\n"
      "s_add_u32 s73, s73, 1024\n"
      "v_cvt_pk_bf16_f32 v178, v178, v179\n"
      "v_fmac_f32 v77, v103, v103\n"
      "v_mfma_f32_16x16x32_bf16 v[110:113], a[52:55], v[186:189], v[110:113]\n"
      "v_mul_f32 v223, v126, v223\n"
      "v_cmp_gt_u32 s[72:73], 64, v84\n"
      "v_mfma_f32_16x16x32_bf16 v[110:113], a[56:59], v[132:135], v[110:113]\n"
      "v_mul_f32 v155, v197, v155\n"
      "v_fmac_f32 v77, v210, v210\n"
      "v_mfma_f32_16x16x32_bf16 v[110:113], a[60:63], v[200:203], v[110:113]\n"
      "v_cvt_pk_bf16_f32 v185, v146, v147\n"
      "v_mul_f32 v204, v197, v204\n"
      "v_mfma_f32_16x16x32_bf16 v[110:113], a[64:67], v[114:117], v[110:113]\n"
      "v_cvt_pk_bf16_f32 v179, v180, v181\n"
      "v_mul_f32 v172, v126, v172\n"
      "v_mfma_f32_16x16x32_bf16 v[110:113], a[68:71], v[214:217], v[110:113]\n"
      "v_cvt_pk_bf16_f32 v181, v130, v131\n"
      "v_and_b32 v92, 2, v208\n"
      "v_mfma_f32_16x16x32_bf16 v[144:147], a[72:75], v[148:151], 0\n"
      "v_cvt_pk_bf16_f32 v157, v158, v159\n"
      "v_fmac_f32 v77, v211, v211\n"
      "v_mfma_f32_16x16x32_bf16 v[144:147], a[76:79], v[186:189], v[144:147]\n"
      "v_cvt_pk_bf16_f32 v107, v192, v193\n"
      "v_cvt_pk_bf16_f32 v99, v138, v139\n"
      "v_mfma_f32_16x16x32_bf16 v[144:147], a[80:83], v[132:135], v[144:147]\n"
      "v_mul_f32 v170, v126, v170\n"
      "v_fmac_f32 v77, v212, v212\n"
      "v_mfma_f32_16x16x32_bf16 v[144:147], a[84:87], v[200:203], v[144:147]\n"
      "v_mul_f32 v173, v126, v173\n"
      "v_mul_f32 v206, v197, v206\n"
      "v_mfma_f32_16x16x32_bf16 v[144:147], a[88:91], v[114:117], v[144:147]\n"
      "v_mul_f32 v207, v197, v207\n"
      "v_cvt_pk_bf16_f32 v180, v128, v129\n"
      "v_mfma_f32_16x16x32_bf16 v[144:147], a[92:95], v[214:217], v[144:147]\n"
      "s_and_b32 s86, s86, -4\n"
      "v_add_u32 v8, s77, v8\n"
      "v_fmac_f32 v77, v213, v213\n"
      "v_mfma_f32_16x16x32_bf16 v[122:125], a[96:99], v[148:151], 0\n"
      "v_cvt_pk_bf16_f32 v220, v220, v221\n"
      "v_cvt_pk_bf16_f32 v141, v142, v143\n"
      "v_mfma_f32_16x16x32_bf16 v[122:125], a[100:103], v[186:189], v[122:125]\n"
      "s_cmp_ge_u32 s95, 0x40000000\n"
      "s_cselect_b32 s94, -1, -2\n"
      "v_fma_f32 v142, v144, v144, s51\n"
      "v_mfma_f32_16x16x32_bf16 v[122:125], a[104:107], v[132:135], v[122:125]\n"
      "v_mov_b32 v199, 0\n"
      "v_mul_f32 v222, v126, v222\n"
      "v_mfma_f32_16x16x32_bf16 v[122:125], a[108:111], v[200:203], v[122:125]\n"
      "v_fmac_f32 v142, v145, v145\n"
      "v_fmac_f32 v142, v146, v146\n"
      "v_mfma_f32_16x16x32_bf16 v[122:125], a[112:115], v[114:117], v[122:125]\n"
      "v_fmac_f32_e64 v77, v110, v110\n"
      "v_cvt_pk_bf16_f32 v152, v152, v153\n"
      "v_mfma_f32_16x16x32_bf16 v[122:125], a[116:119], v[214:217], v[122:125]\n"
      "v_cvt_pk_bf16_f32 v98, v136, v137\n"
      "v_and_b32_e64 v208, -4, v208\n"
      "v_mfma_f32_16x16x32_bf16 v[126:129], a[120:123], v[148:151], 0\n"
      "v_fmac_f32_e64 v77, v111, v111\n"
      "v_cvt_pk_bf16_f32 v153, v154, v155\n"
      "v_mfma_f32_16x16x32_bf16 v[126:129], a[124:127], v[186:189], v[126:129]\n"
      "v_cvt_pk_bf16_f32 v170, v170, v171\n"
      "v_cvt_pk_bf16_f32 v154, v166, v167\n"
      "v_mfma_f32_16x16x32_bf16 v[126:129], a[128:131], v[132:135], v[126:129]\n"
      "v_fmac_f32_e64 v142, v147, v147\n"
      "v_cvt_pk_bf16_f32 v197, v108, v109\n"
      "v_mfma_f32_16x16x32_bf16 v[126:129], a[132:135], v[200:203], v[126:129]\n"
      "ds_read_b128 v[174:177], v7 offset:18432\n"
      "v_fmac_f32_e64 v77, v112, v112\n"
      "v_mfma_f32_16x16x32_bf16 v[126:129], a[136:139], v[114:117], v[126:129]\n"
      "v_cvt_pk_bf16_f32 v171, v172, v173\n"
      "v_cvt_pk_bf16_f32 v173, v120, v121\n"
      "v_mfma_f32_16x16x32_bf16 v[126:129], a[140:143], v[214:217], v[126:129]\n"
      "v_cvt_pk_bf16_f32 v155, v168, v169\n"
      "v_fmac_f32 v142, v122, v122\n"
      "v_mfma_f32_16x16x32_bf16 v[166:169], v[148:151], a[144:147], 0\n"
      "s_waitcnt vmcnt(7)\n"
      "v_cvt_pk_bf16_f32 v204, v204, v205\n"
      "v_fmac_f32 v142, v123, v123\n"
      "v_mfma_f32_16x16x32_bf16 v[166:169], v[186:189], a[148:151], v[166:169]\n"
      "v_fmac_f32 v142, v124, v124\n"
      "v_cvt_pk_bf16_f32 v221, v222, v223\n"
      "v_mfma_f32_16x16x32_bf16 v[166:169], v[132:135], a[152:155], v[166:169]\n"
      "s_cmp_gt_u32 s42, s44\n"
      "s_cselect_b32 s95, s94, s95\n"
      "v_fmac_f32_e64 v142, v125, v125\n"
      "v_mfma_f32_16x16x32_bf16 v[166:169], v[200:203], a[156:159], v[166:169]\n"
      "v_lshlrev_b64 v[198:199], v84, v[198:199]\n"
      "v_cvt_pk_bf16_f32 v172, v118, v119\n"
      "v_mfma_f32_16x16x32_bf16 v[166:169], v[114:117], a[160:163], v[166:169]\n"
      "v_add_u32 v8, 115296, v8\n"
      "v_fmac_f32_e64 v142, v126, v126\n"
      "v_mfma_f32_16x16x32_bf16 v[166:169], v[214:217], a[164:167], v[166:169]\n"
      "v_fmac_f32 v142, v127, v127\n"
      "v_fmac_f32 v77, v113, v113\n"
      "v_mfma_f32_16x16x32_bf16 v[118:121], v[148:151], a[168:171], 0\n"
      "v_cvt_pk_bf16_f32 v205, v206, v207\n"
      "v_fmac_f32 v142, v128, v128\n"
      "v_mfma_f32_16x16x32_bf16 v[118:121], v[186:189], a[172:175], v[118:121]\n"
      "ds_read_b128 v[158:161], v2 offset:18432\n"
      "v_fmac_f32 v142, v129, v129\n"
      "v_mfma_f32_16x16x32_bf16 v[118:121], v[132:135], a[176:179], v[118:121]\n"
      "s_nop 0\n"
      "v_permlane16_swap_b32 v77, v142\n"
      "v_mfma_f32_16x16x32_bf16 v[118:121], v[200:203], a[180:183], v[118:121]\n"
      "v_add_f32 v77, v77, v142\n"
      "v_mov_b32 v142, v77\n"
      "v_mfma_f32_16x16x32_bf16 v[118:121], v[114:117], a[184:187], v[118:121]\n"
      "s_nop 0\n"
      "v_permlane32_swap_b32 v77, v142\n"
      "v_mfma_f32_16x16x32_bf16 v[118:121], v[214:217], a[188:191], v[118:121]\n"
      "ds_read_b128 v[136:139], v2 offset:21504\n"
      "v_add_f32_e64 v77, v77, v142\n"
      "v_mfma_f32_16x16x32_bf16 v[190:193], v[148:151], a[192:195], 0\n"
      "s_waitcnt lgkmcnt(2)\n"
      "v_rsq_f32 v77, v77\n"
      "v_mfma_f32_16x16x32_bf16 v[190:193], v[186:189], a[196:199], v[190:193]\n"
      "ds_read_b128 v[186:189], v2 offset:24576\n"
      "v_mov_b32 v142, v77\n"
      "v_mfma_f32_16x16x32_bf16 v[190:193], v[132:135], a[200:203], v[190:193]\n"
      "s_nop 0\n"
      "v_permlane16_swap_b32_e64 v77, v142\n"
      "v_mfma_f32_16x16x32_bf16 v[190:193], v[200:203], a[204:207], v[190:193]\n"
      "ds_read_b128 v[148:151], v7 offset:22528\n"
      "v_mul_f32_e64 v77, s50, v77\n"
      "v_mfma_f32_16x16x32_bf16 v[190:193], v[114:117], a[208:211], v[190:193]\n"
      "buffer_store_dwordx4 v[174:177], v20, s[24:27], s46 offen sc1\n"
      "v_pk_mul_f32 v[144:145], v[144:145], v[142:143] op_sel_hi:[1,0]\n"
      "v_pk_mul_f32 v[124:125], v[124:125], v[142:143] op_sel_hi:[1,0]\n"
      "v_pk_mul_f32 v[128:129], v[128:129], v[142:143] op_sel_hi:[1,0]\n"
      "v_pk_mul_f32 v[122:123], v[122:123], v[142:143] op_sel_hi:[1,0]\n"
      "v_pk_mul_f32 v[126:127], v[126:127], v[142:143] op_sel_hi:[1,0]\n"
      "v_pk_mul_f32 v[110:111], v[110:111], v[76:77] op_sel:[0,1]\n"
      "v_pk_mul_f32 v[100:101], v[100:101], v[76:77] op_sel:[0,1]\n"
      "v_pk_mul_f32 v[146:147], v[146:147], v[142:143] op_sel_hi:[1,0]\n"
      "v_mfma_f32_16x16x32_bf16 v[190:193], v[214:217], a[212:215], v[190:193]\n"
      "s_waitcnt lgkmcnt(1)\n"
      "v_cndmask_b32_e64 v198, 0, v198, s[72:73]\n"
      "v_cvt_pk_bf16_f32 v81, v120, v121\n"
      "v_mfma_f32_16x16x32_bf16 v[174:177], a[216:219], v[158:161], 0\n"
      "ds_read_b128 v[200:203], v2 offset:27648\n"
      "v_add_u32 v142, 110592, v208\n"
      "v_mfma_f32_16x16x32_bf16 v[174:177], a[220:223], v[136:139], v[174:177]\n"
      "s_waitcnt lgkmcnt(1)\n"
      "v_cvt_pk_bf16_f32 v80, v118, v119\n"
      "v_cvt_pk_bf16_f32 v144, v144, v145\n"
      "v_mfma_f32_16x16x32_bf16 v[174:177], a[224:227], v[186:189], v[174:177]\n"
      "ds_read2_b32 v[222:223], v142 offset0:0 offset1:1\n"
      "v_cndmask_b32_e64 v199, 0, v199, s[72:73]\n"
      "v_mfma_f32_16x16x32_bf16 v[214:217], a[240:243], v[158:161], 0\n"
      "ds_read_b128 v[118:121], v2 offset:33792\n"
      "v_cvt_pk_bf16_f32 v85, v192, v193\n"
      "v_mfma_f32_16x16x32_bf16 v[214:217], a[244:247], v[136:139], v[214:217]\n"
      "ds_read_b128 v[132:135], v2 offset:30720\n"
      "v_cvt_pk_bf16_f32 v145, v146, v147\n"
      "v_mfma_f32_16x16x32_bf16 v[214:217], a[248:251], v[186:189], v[214:217]\n"
      "buffer_store_dwordx4 v[148:151], v21, s[24:27], s46 offen sc1\n"
      "v_pk_mul_f32 v[112:113], v[112:113], v[76:77] op_sel:[0,1]\n"
      "v_pk_mul_f32 v[102:103], v[102:103], v[76:77] op_sel:[0,1]\n"
      "v_pk_mul_f32 v[212:213], v[212:213], v[76:77] op_sel:[0,1]\n"
      "v_pk_mul_f32 v[210:211], v[210:211], v[76:77] op_sel:[0,1]\n"
      "v_mfma_f32_16x16x32_bf16 v[148:151], v[232:235], v[158:161], 0\n"
      "ds_read2_b32 v[206:207], v142 offset0:24 offset1:18\n"
      "v_cvt_pk_bf16_f32 v110, v110, v111\n"
      "v_mfma_f32_16x16x32_bf16 v[148:151], v[236:239], v[136:139], v[148:151]\n"
      "ds_read_b128 v[158:161], v7 offset:26624\n"
      "v_cvt_pk_bf16_f32 v100, v100, v101\n"
      "v_mfma_f32_16x16x32_bf16 v[148:151], v[240:243], v[186:189], v[148:151]\n"
      "s_waitcnt lgkmcnt(2)\n"
      "v_alignbyte_b32 v222, v223, v222, v92\n"
      "v_cvt_pk_bf16_f32 v77, v168, v169\n"
      "v_mfma_f32_16x16x32_bf16 v[174:177], a[228:231], v[200:203], v[174:177]\n"
      "ds_read_b64 v[218:219], v10 offset:24\n"
      "v_cvt_pk_bf16_f32 v126, v126, v127\n"
      "v_mfma_f32_16x16x32_bf16 v[174:177], a[232:235], v[132:135], v[174:177]\n"
      "ds_read2_b32 v[192:193], v142 offset0:2 offset1:10\n"
      "v_cvt_pk_bf16_f32 v76, v166, v167\n"
      "v_mfma_f32_16x16x32_bf16 v[174:177], a[236:239], v[118:121], v[174:177]\n"
      "ds_read2_b32 v[136:137], v142 offset0:8 offset1:9\n"
      "v_cvt_pk_bf16_f32 v111, v112, v113\n"
      "v_mfma_f32_16x16x32_bf16 v[214:217], a[252:255], v[200:203], v[214:217]\n"
      "ds_read_b128 v[166:169], v7 offset:30720\n"
      "v_cvt_pk_bf16_f32 v146, v122, v123\n"
      "v_mfma_f32_16x16x32_bf16 v[214:217], v[224:227], v[132:135], v[214:217]\n"
      "s_waitcnt lgkmcnt(4)\n"
      "v_cvt_pk_bf16_f32 v147, v124, v125\n"
      "v_cvt_pk_bf16_f32 v101, v102, v103\n"
      "v_mfma_f32_16x16x32_bf16 v[214:217], v[228:231], v[118:121], v[214:217]\n"
      "ds_read2_b32 v[138:139], v142 offset0:16 offset1:17\n"
      "v_fma_f32 v142, v174, v174, s52\n"
      "v_mfma_f32_16x16x32_bf16 v[148:151], v[244:247], v[200:203], v[148:151]\n"
      "ds_read_b128 v[200:203], v10 offset:32\n"
      "v_add_u32 v188, 112160, v208\n"
      "v_mfma_f32_16x16x32_bf16 v[148:151], v[248:251], v[132:135], v[148:151]\n"
      "buffer_store_dwordx4 v[158:161], v22, s[24:27], s46 offen sc1\n"
      "v_mfma_f32_16x16x32_bf16 v[148:151], v[252:255], v[118:121], v[148:151]\n"
      "ds_read_u16 v8, v8\n"
      "v_mov_b32 v109, 0\n"
      "v_mfma_f32_16x16x32_bf16 v[132:135], v[182:185], v[178:181], v[40:43]\n"
      "s_waitcnt lgkmcnt(3)\n"
      "v_cmp_gt_u32 s[72:73], 64, v84\n"
      "v_alignbyte_b32 v223, v192, v223, v92\n"
      "v_mfma_f32_16x16x32_bf16 v[158:161], v[152:155], v[178:181], v[44:47]\n"
      "s_waitcnt lgkmcnt(2)\n"
      "v_mov_b32 v108, 0xbb2b\n"
      "v_cvt_pk_bf16_f32 v127, v128, v129\n"
      "v_mfma_f32_16x16x32_bf16 v[128:131], v[144:147], v[178:181], v[48:51]\n"
      "s_waitcnt lgkmcnt(1)\n"
      "v_alignbyte_b32 v138, v139, v138, v92\n"
      "v_lshlrev_b64 v[108:109], v84, v[108:109]\n"
      "v_mfma_f32_16x16x32_bf16 v[116:119], v[200:203], v[178:181], v[24:27]\n"
      "ds_read_b128 v[122:125], v7 offset:34816\n"
      "v_alignbyte_b32 v136, v137, v136, v92\n"
      "v_mfma_f32_16x16x16_bf16 v[132:135], v[140:141], v[156:157], v[132:135]\n"
      "v_cvt_pk_bf16_f32 v103, v212, v213\n"
      "v_alignbyte_b32 v139, v207, v139, v92\n"
      "v_mul_f32 v143, v175, v175\n"
      "v_mfma_f32_16x16x16_bf16 v[158:161], v[204:205], v[156:157], v[158:161]\n"
      "ds_read2_b32 v[112:113], v188 offset0:8 offset1:9\n"
      "v_alignbyte_b32 v137, v193, v137, v92\n"
      "v_mfma_f32_16x16x16_bf16 v[128:131], v[126:127], v[156:157], v[128:131]\n"
      "buffer_store_dwordx4 v[166:169], v23, s[24:27], s46 offen sc1\n"
      "v_mfma_f32_16x16x16_bf16 v[116:119], v[218:219], v[156:157], v[116:119]\n"
      "s_nop 0\n"
      "ds_read_b128 v[166:169], v2 offset:18688\n"
      "v_add_u32 v157, 113728, v208\n"
      "v_mfma_f32_16x16x16_bf16 v[132:135], v[198:199], v[222:223], v[132:135]\n"
      "v_cndmask_b32_e64 v108, 0, v108, s[72:73]\n"
      "v_fmac_f32_e64 v143, v177, v177\n"
      "v_alignbyte_b32 v206, v206, v206, v92\n"
      "v_mfma_f32_16x16x16_bf16 v[158:161], v[198:199], v[136:137], v[158:161]\n"
      "ds_read_b128 v[178:181], v2 offset:21760\n"
      "v_cvt_pk_bf16_f32 v102, v210, v211\n"
      "v_mfma_f32_16x16x16_bf16 v[128:131], v[198:199], v[138:139], v[128:131]\n"
      "v_cndmask_b32_e64 v109, 0, v109, s[72:73]\n"
      "v_fmac_f32_e64 v142, v176, v176\n"
      "v_cvt_pk_bf16_f32 v84, v190, v191\n"
      "v_mfma_f32_16x16x16_bf16 v[116:119], v[198:199], v[206:207], v[116:119]\n"
      "ds_read_b128 v[190:193], v2 offset:24832\n"
      "v_add_u32_e64 v12, 32, v12\n"
      "v_mfma_f32_16x16x16_bf16 v[132:135], v[108:109], v[222:223], v[132:135]\n"
      "ds_read2_b32 v[222:223], v188 offset0:2 offset1:10\n"
      "v_fmac_f32 v142, v214, v214\n"
      "v_mfma_f32_16x16x16_bf16 v[158:161], v[108:109], v[136:137], v[158:161]\n"
      "s_waitcnt lgkmcnt(3)\n"
      "v_lshlrev_b32 v8, 16, v8\n"
      "v_fmac_f32 v143, v215, v215\n"
      "v_mfma_f32_16x16x16_bf16 v[128:131], v[108:109], v[138:139], v[128:131]\n"
      "buffer_store_dwordx4 v[122:125], v25, s[24:27], s46 offen sc1\n"
      "v_pk_fma_f32 v[142:143], v[216:217], v[216:217], v[142:143]\n"
      "v_mfma_f32_16x16x16_bf16 v[116:119], v[108:109], v[206:207], v[116:119]\n"
      "ds_read2_b32 v[186:187], v188 offset0:0 offset1:1\n"
      "v_alignbyte_b32 v112, v113, v112, v92\n"
      "v_mfma_f32_16x16x32_bf16 v[210:213], a[216:219], v[166:169], 0\n"
      "s_waitcnt lgkmcnt(2)\n"
      "v_max3_f32 v189, v132, v133, v134\n"
      "v_max3_f32 v189, v189, v160, v161\n"
      "v_mfma_f32_16x16x32_bf16 v[210:213], a[220:223], v[178:181], v[210:213]\n"
      "s_waitcnt lgkmcnt(1)\n"
      "v_max3_f32 v118, v135, v158, v159\n"
      "v_max3_f32 v118, v118, v128, v129\n"
      "v_mfma_f32_16x16x32_bf16 v[210:213], a[224:227], v[190:193], v[210:213]\n"
      "v_alignbyte_b32 v113, v223, v113, v92\n"
      "v_max3_f32 v189, v189, v130, v131\n"
      "v_mfma_f32_16x16x32_bf16 v[122:125], a[240:243], v[166:169], 0\n"
      "v_max3_f32 v189, v189, v118, v116\n"
      "v_mov_b32_e64 v206, v189\n"
      "v_mfma_f32_16x16x32_bf16 v[122:125], a[244:247], v[178:181], v[122:125]\n"
      "s_nop 0\n"
      "v_permlane16_swap_b32 v189, v206\n"
      "v_mfma_f32_16x16x32_bf16 v[122:125], a[248:251], v[190:193], v[122:125]\n"
      "s_waitcnt lgkmcnt(0)\n"
      "v_max_f32 v189, v189, v206\n"
      "s_waitcnt vmcnt(6)\n"
      "v_or3_b32 v162, v162, v163, v164\n"
      "v_mfma_f32_16x16x32_bf16 v[118:121], v[232:235], v[166:169], 0\n"
      "ds_read2_b32 v[136:137], v188 offset0:16 offset1:17\n"
      "v_mov_b32 v206, v189\n"
      "v_mfma_f32_16x16x32_bf16 v[118:121], v[236:239], v[178:181], v[118:121]\n"
      "s_nop 0\n"
      "v_permlane32_swap_b32 v189, v206\n"
      "v_mfma_f32_16x16x32_bf16 v[118:121], v[240:243], v[190:193], v[118:121]\n"
      "ds_read2_b32 v[138:139], v188 offset0:24 offset1:18\n"
      "v_alignbyte_b32 v186, v187, v186, v92\n"
      "v_alignbyte_b32 v187, v222, v187, v92\n"
      "v_max_f32 v189, v189, v206\n"
      "v_sub_f32 v158, v158, v189\n"
      "v_pk_add_f32 v[134:135], v[134:135], v[188:189] op_sel:[0,1] neg_lo:[0,1] neg_hi:[0,1]\n"
      "v_pk_add_f32 v[132:133], v[132:133], v[188:189] op_sel:[0,1] neg_lo:[0,1] neg_hi:[0,1]\n"
      "v_pk_add_f32 v[160:161], v[160:161], v[188:189] op_sel:[0,1] neg_lo:[0,1] neg_hi:[0,1]\n"
      "v_pk_add_f32 v[130:131], v[130:131], v[188:189] op_sel:[0,1] neg_lo:[0,1] neg_hi:[0,1]\n"
      "v_pk_add_f32 v[128:129], v[128:129], v[188:189] op_sel:[0,1] neg_lo:[0,1] neg_hi:[0,1]\n"
      "v_mfma_f32_16x16x32_bf16 v[178:181], v[182:185], v[170:173], v[52:55]\n"
      "v_exp_f32 v133, v133\n"
      "s_waitcnt vmcnt(5)\n"
      "v_or3_b32 v162, v162, v165, v78\n"
      "v_mfma_f32_16x16x32_bf16 v[206:209], v[152:155], v[170:173], v[56:59]\n"
      "ds_write_b32 v31, v162 offset:4608\n"
      "v_mfma_f32_16x16x32_bf16 v[162:165], v[144:147], v[170:173], v[60:63]\n"
      "v_exp_f32 v132, v132\n"
      "v_mfma_f32_16x16x32_bf16 v[166:169], v[200:203], v[170:173], v[26:29]\n"
      "s_waitcnt lgkmcnt(1)\n"
      "v_exp_f32 v128, v128\n"
      "v_mfma_f32_16x16x16_bf16 v[178:181], v[140:141], v[220:221], v[178:181]\n"
      "v_alignbyte_b32 v136, v137, v136, v92\n"
      "v_exp_f32 v134, v134\n"
      "v_mfma_f32_16x16x16_bf16 v[206:209], v[204:205], v[220:221], v[206:209]\n"
      "v_exp_f32 v130, v130\n"
      "v_cvt_pk_bf16_f32 v132, v132, v133\n"
      "v_mfma_f32_16x16x16_bf16 v[162:165], v[126:127], v[220:221], v[162:165]\n"
      "v_sub_f32 v159, v159, v189\n"
      "v_exp_f32 v161, v161\n"
      "v_mfma_f32_16x16x16_bf16 v[166:169], v[218:219], v[220:221], v[166:169]\n"
      "v_exp_f32 v131, v131\n"
      "v_alignbyte_b32 v137, v139, v137, v92\n"
      "v_mfma_f32_16x16x16_bf16 v[178:181], v[198:199], v[186:187], v[178:181]\n"
      "v_alignbyte_b32 v138, v138, v138, v92\n"
      "v_exp_f32 v159, v159\n"
      "v_mfma_f32_16x16x16_bf16 v[206:209], v[198:199], v[112:113], v[206:209]\n"
      "v_exp_f32 v160, v160\n"
      "v_exp_f32 v135, v135\n"
      "v_mfma_f32_16x16x16_bf16 v[162:165], v[198:199], v[136:137], v[162:165]\n"
      "v_sub_f32 v116, v116, v189\n"
      "v_exp_f32_e64 v129, v129\n"
      "v_mfma_f32_16x16x16_bf16 v[166:169], v[198:199], v[138:139], v[166:169]\n"
      "ds_read_b128 v[220:223], v2 offset:30976\n"
      "v_cvt_pk_bf16_f32 v89, v130, v131\n"
      "v_mfma_f32_16x16x16_bf16 v[178:181], v[108:109], v[186:187], v[178:181]\n"
      "ds_read_b128 v[170:173], v2 offset:27904\n"
      "v_cvt_pk_bf16_f32 v133, v134, v135\n"
      "v_mfma_f32_16x16x16_bf16 v[206:209], v[108:109], v[112:113], v[206:209]\n"
      "v_exp_f32 v116, v116\n"
      "v_cvt_pk_bf16_f32 v88, v128, v129\n"
      "v_mfma_f32_16x16x16_bf16 v[162:165], v[108:109], v[136:137], v[162:165]\n"
      "ds_read_b128 v[128:131], v2 offset:34048\n"
      "v_exp_f32 v158, v158\n"
      "v_mfma_f32_16x16x16_bf16 v[166:169], v[108:109], v[138:139], v[166:169]\n"
      "v_max3_f32 v138, v178, v179, v180\n"
      "v_cvt_pk_bf16_f32 v134, v158, v159\n"
      "v_max3_f32 v138, v138, v208, v209\n"
      "v_cvt_pk_bf16_f32 v135, v160, v161\n"
      "v_max3_f32 v78, v181, v206, v207\n"
      "v_max3_f32 v138, v138, v164, v165\n"
      "v_pk_fma_f32 v[142:143], v[148:149], v[148:149], v[142:143]\n"
      "v_pk_fma_f32 v[142:143], v[150:151], v[150:151], v[142:143]\n"
      "v_max3_f32 v78, v78, v162, v163\n"
      "v_max3_f32 v138, v138, v78, v166\n"
      "v_mov_b32 v113, v138\n"
      "s_waitcnt lgkmcnt(1)\n"
      "v_mfma_f32_16x16x32_bf16 v[210:213], a[228:231], v[170:173], v[210:213]\n"
      "v_permlane16_swap_b32 v138, v113\n"
      "v_mfma_f32_16x16x32_bf16 v[210:213], a[232:235], v[220:223], v[210:213]\n"
      "ds_read_u16 v78, v11\n"
      "s_waitcnt lgkmcnt(1)\n"
      "v_mfma_f32_16x16x32_bf16 v[210:213], a[236:239], v[128:131], v[210:213]\n"
      "v_add_f32 v142, v142, v143\n"
      "v_max_f32 v138, v138, v113\n"
      "v_mfma_f32_16x16x32_bf16 v[122:125], a[252:255], v[170:173], v[122:125]\n"
      "ds_read2_b32 v[192:193], v157 offset0:16 offset1:17\n"
      "v_mov_b32 v113, v138\n"
      "v_mfma_f32_16x16x32_bf16 v[122:125], v[224:227], v[220:223], v[122:125]\n"
      "ds_read_u16 v82, v11 offset:32\n"
      "v_cvt_pk_bf16_f32 v90, v116, 0\n"
      "v_permlane32_swap_b32 v138, v113\n"
      "v_max_f32_e64 v138, v138, v113\n"
      "v_pk_add_f32 v[206:207], v[206:207], v[138:139] op_sel_hi:[1,0] neg_lo:[0,1] neg_hi:[0,1]\n"
      "v_pk_add_f32 v[208:209], v[208:209], v[138:139] op_sel_hi:[1,0] neg_lo:[0,1] neg_hi:[0,1]\n"
      "v_pk_add_f32 v[164:165], v[164:165], v[138:139] op_sel_hi:[1,0] neg_lo:[0,1] neg_hi:[0,1]\n"
      "v_pk_add_f32 v[180:181], v[180:181], v[138:139] op_sel_hi:[1,0] neg_lo:[0,1] neg_hi:[0,1]\n"
      "v_pk_add_f32 v[178:179], v[178:179], v[138:139] op_sel_hi:[1,0] neg_lo:[0,1] neg_hi:[0,1]\n"
      "v_mfma_f32_16x16x32_bf16 v[122:125], v[228:231], v[128:131], v[122:125]\n"
      "v_exp_f32_e64 v180, v180\n"
      "v_mfma_f32_16x16x32_bf16 v[118:121], v[244:247], v[170:173], v[118:121]\n"
      "s_waitcnt lgkmcnt(2)\n"
      "v_exp_f32 v178, v178\n"
      "v_mfma_f32_16x16x32_bf16 v[118:121], v[248:251], v[220:223], v[118:121]\n"
      "ds_read2_b32 v[112:113], v157 offset0:0 offset1:1\n"
      "v_exp_f32 v179, v179\n"
      "v_mfma_f32_16x16x32_bf16 v[118:121], v[252:255], v[128:131], v[118:121]\n"
      "ds_read2_b32 v[220:221], v157 offset0:24 offset1:18\n"
      "v_sub_f32 v166, v166, v138\n"
      "v_mfma_f32_16x16x32_bf16 v[186:189], v[32:35], v[132:135], 0\n"
      "ds_read_u16 v86, v11 offset:64\n"
      "v_cvt_pk_bf16_f32 v178, v178, v179\n"
      "v_mfma_f32_16x16x32_bf16 v[186:189], v[32:35], v[88:91], v[186:189]\n"
      "ds_read2_b32 v[158:159], v157 offset0:2 offset1:10\n"
      "v_exp_f32 v209, v209\n"
      "v_exp_f32 v181, v181\n"
      "v_pk_add_f32 v[162:163], v[162:163], v[138:139] op_sel_hi:[1,0] neg_lo:[0,1] neg_hi:[0,1]\n"
      "v_mfma_f32_16x16x32_bf16 v[170:173], v[194:197], v[132:135], 0\n"
      "ds_read2_b32 v[160:161], v157 offset0:8 offset1:9\n"
      "v_cvt_pk_bf16_f32 v179, v180, v181\n"
      "v_mfma_f32_16x16x32_bf16 v[170:173], v[76:79], v[88:91], v[170:173]\n"
      "v_exp_f32 v208, v208\n"
      "v_mfma_f32_16x16x32_bf16 v[128:131], v[96:99], v[132:135], 0\n"
      "s_waitcnt lgkmcnt(3)\n"
      "v_cvt_pk_bf16_f32 v181, v208, v209\n"
      "v_alignbyte_b32 v220, v220, v220, v92\n"
      "v_mfma_f32_16x16x32_bf16 v[128:131], v[80:83], v[88:91], v[128:131]\n"
      "v_exp_f32_e64 v207, v207\n"
      "v_mfma_f32_16x16x32_bf16 v[114:117], v[104:107], v[132:135], 0\n"
      "s_waitcnt lgkmcnt(1)\n"
      "v_exp_f32_e64 v206, v206\n"
      "v_mfma_f32_16x16x32_bf16 v[114:117], v[84:87], v[88:91], v[114:117]\n"
      "v_cvt_pk_bf16_f32 v180, v206, v207\n"
      "v_alignbyte_b32 v112, v113, v112, v92\n"
      "v_alignbyte_b32 v192, v193, v192, v92\n"
      "v_mfma_f32_16x16x32_bf16 v[132:135], v[182:185], v[100:103], v[64:67]\n"
      "s_waitcnt lgkmcnt(0)\n"
      "v_alignbyte_b32 v160, v161, v160, v92\n"
      "v_alignbyte_b32 v113, v158, v113, v92\n"
      "v_mfma_f32_16x16x32_bf16 v[206:209], v[152:155], v[100:103], v[68:71]\n"
      "v_alignbyte_b32 v193, v221, v193, v92\n"
      "v_alignbyte_b32 v161, v159, v161, v92\n"
      "v_mfma_f32_16x16x32_bf16 v[156:159], v[144:147], v[100:103], v[72:75]\n"
      "v_rcp_f32 v186, v186\n"
      "v_mfma_f32_16x16x32_bf16 v[136:139], v[200:203], v[100:103], v[28:31]\n"
      "v_mul_f32 v172, v186, v172\n"
      "v_mul_f32 v130, v186, v130\n"
      "v_mfma_f32_16x16x16_bf16 v[132:135], v[140:141], v[110:111], v[132:135]\n"
      "v_mul_f32 v170, v186, v170\n"
      "v_exp_f32 v162, v162\n"
      "v_mfma_f32_16x16x16_bf16 v[206:209], v[204:205], v[110:111], v[206:209]\n"
      "v_exp_f32 v166, v166\n"
      "v_mul_f32 v171, v186, v171\n"
      "v_mfma_f32_16x16x16_bf16 v[156:159], v[126:127], v[110:111], v[156:159]\n"
      "v_exp_f32 v165, v165\n"
      "v_mul_f32_e64 v173, v186, v173\n"
      "v_mfma_f32_16x16x16_bf16 v[136:139], v[218:219], v[110:111], v[136:139]\n"
      "v_cvt_pk_bf16_f32 v170, v170, v171\n"
      "v_exp_f32 v164, v164\n"
      "v_mfma_f32_16x16x16_bf16 v[132:135], v[198:199], v[112:113], v[132:135]\n"
      "v_cvt_pk_bf16_f32 v171, v172, v173\n"
      "v_exp_f32 v163, v163\n"
      "v_mfma_f32_16x16x16_bf16 v[206:209], v[198:199], v[160:161], v[206:209]\n"
      "v_mul_f32_e64 v101, v211, v211\n"
      "v_cvt_pk_bf16_f32 v93, v164, v165\n"
      "v_fma_f32 v100, v210, v210, s52\n"
      "v_mfma_f32_16x16x16_bf16 v[156:159], v[198:199], v[192:193], v[156:159]\n"
      "v_cvt_pk_bf16_f32 v92, v162, v163\n"
      "v_mul_f32 v129, v186, v129\n"
      "v_fmac_f32 v101, v213, v213\n"
      "v_mfma_f32_16x16x16_bf16 v[136:139], v[198:199], v[220:221], v[136:139]\n"
      "v_mul_f32 v128, v186, v128\n"
      "v_fmac_f32 v100, v212, v212\n"
      "v_mul_f32_e64 v131, v186, v131\n"
      "v_mfma_f32_16x16x16_bf16 v[132:135], v[108:109], v[112:113], v[132:135]\n"
      "ds_read_b128 v[162:165], v2 offset:25088\n"
      "v_cvt_pk_bf16_f32 v172, v128, v129\n"
      "v_mfma_f32_16x16x16_bf16 v[206:209], v[108:109], v[160:161], v[206:209]\n"
      "ds_read_b128 v[110:113], v2 offset:22016\n"
      "v_fmac_f32_e64 v101, v123, v123\n"
      "v_mfma_f32_16x16x16_bf16 v[156:159], v[108:109], v[192:193], v[156:159]\n"
      "ds_read_b128 v[190:193], v2 offset:18944\n"
      "v_cvt_pk_bf16_f32 v173, v130, v131\n"
      "v_mfma_f32_16x16x16_bf16 v[136:139], v[108:109], v[220:221], v[136:139]\n"
      "v_fmac_f32_e64 v100, v122, v122\n"
      "v_cvt_pk_bf16_f32 v94, v166, 0\n"
      "v_max3_f32 v89, v132, v133, v134\n"
      "v_max3_f32 v89, v89, v208, v209\n"
      "v_max3_f32 v221, v135, v206, v207\n"
      "v_max3_f32 v89, v89, v158, v159\n"
      "v_pk_mul_f32 v[116:117], v[116:117], v[186:187] op_sel_hi:[1,0]\n"
      "v_max3_f32 v221, v221, v156, v157\n"
      "v_max3_f32 v89, v89, v221, v136\n"
      "v_mov_b32 v221, v89\n"
      "v_pk_fma_f32 v[100:101], v[124:125], v[124:125], v[100:101]\n"
      "v_pk_mul_f32 v[114:115], v[114:115], v[186:187] op_sel_hi:[1,0]\n"
      "v_permlane16_swap_b32 v89, v221\n"
      "v_max_f32 v89, v89, v221\n"
      "v_mov_b32 v221, v89\n"
      "v_pk_fma_f32 v[100:101], v[118:119], v[118:119], v[100:101]\n"
      "v_pk_fma_f32 v[100:101], v[120:121], v[120:121], v[100:101]\n"
      "v_permlane32_swap_b32_e64 v89, v221\n"
      "v_max_f32 v89, v89, v221\n"
      "v_pk_add_f32 v[134:135], v[134:135], v[88:89] op_sel:[0,1] neg_lo:[0,1] neg_hi:[0,1]\n"
      "s_waitcnt lgkmcnt(0)\n"
      "v_mfma_f32_16x16x32_bf16 v[220:223], a[216:219], v[190:193], 0\n"
      "ds_write_b128 v3, v[170:173]\n"
      "v_pk_add_f32 v[132:133], v[132:133], v[88:89] op_sel:[0,1] neg_lo:[0,1] neg_hi:[0,1]\n"
      "v_pk_add_f32 v[208:209], v[208:209], v[88:89] op_sel:[0,1] neg_lo:[0,1] neg_hi:[0,1]\n"
      "v_pk_add_f32 v[158:159], v[158:159], v[88:89] op_sel:[0,1] neg_lo:[0,1] neg_hi:[0,1]\n"
      "v_mfma_f32_16x16x32_bf16 v[220:223], a[220:223], v[110:113], v[220:223]\n"
      "v_sub_f32 v136, v136, v89\n"
      "v_cvt_pk_bf16_f32 v114, v114, v115\n"
      "v_mfma_f32_16x16x32_bf16 v[220:223], a[224:227], v[162:165], v[220:223]\n"
      "v_cvt_pk_bf16_f32 v115, v116, v117\n"
      "v_sub_f32 v206, v206, v89\n"
      "v_mfma_f32_16x16x32_bf16 v[166:169], a[240:243], v[190:193], 0\n"
      "ds_read_b128 v[170:173], v10\n"
      "v_sub_f32 v157, v157, v89\n"
      "v_mfma_f32_16x16x32_bf16 v[166:169], a[244:247], v[110:113], v[166:169]\n"
      "v_exp_f32 v159, v159\n"
      "v_mfma_f32_16x16x32_bf16 v[166:169], a[248:251], v[162:165], v[166:169]\n"
      "v_add_f32 v100, v100, v101\n"
      "v_sub_f32 v156, v156, v89\n"
      "v_mfma_f32_16x16x32_bf16 v[186:189], v[232:235], v[190:193], 0\n"
      "v_exp_f32_e64 v209, v209\n"
      "v_mfma_f32_16x16x32_bf16 v[186:189], v[236:239], v[110:113], v[186:189]\n"
      "v_exp_f32 v132, v132\n"
      "v_mfma_f32_16x16x32_bf16 v[186:189], v[240:243], v[162:165], v[186:189]\n"
      "v_exp_f32 v133, v133\n"
      "v_mfma_f32_16x16x32_bf16 v[160:163], v[32:35], v[178:181], 0\n"
      "v_exp_f32 v208, v208\n"
      "v_mfma_f32_16x16x32_bf16 v[160:163], v[32:35], v[92:95], v[160:163]\n"
      "v_exp_f32 v134, v134\n"
      "v_mfma_f32_16x16x32_bf16 v[128:131], v[194:197], v[178:181], 0\n"
      "ds_write_b64 v4, v[114:115]\n"
      "v_mfma_f32_16x16x32_bf16 v[128:131], v[76:79], v[92:95], v[128:131]\n"
      "ds_read_b64 v[114:115], v10 offset:16\n"
      "v_sub_f32 v207, v207, v89\n"
      "v_mfma_f32_16x16x32_bf16 v[110:113], v[96:99], v[178:181], 0\n"
      "s_nop 0\n"
      "v_rcp_f32 v160, v160\n"
      "v_mfma_f32_16x16x32_bf16 v[110:113], v[80:83], v[92:95], v[110:113]\n"
      "v_exp_f32 v158, v158\n"
      "v_mfma_f32_16x16x32_bf16 v[162:165], v[104:107], v[178:181], 0\n"
      "v_exp_f32_e64 v135, v135\n"
      "v_mfma_f32_16x16x32_bf16 v[162:165], v[84:87], v[92:95], v[162:165]\n"
      "s_waitcnt lgkmcnt(2)\n"
      "v_exp_f32 v157, v157\n"
      "v_mfma_f32_16x16x32_bf16 v[178:181], v[170:173], v[182:185], v[30:33]\n"
      "v_exp_f32_e64 v206, v206\n"
      "v_mfma_f32_16x16x32_bf16 v[182:185], v[170:173], v[152:155], v[30:33]\n"
      "ds_read_b128 v[152:155], v2 offset:28160\n"
      "v_cvt_pk_bf16_f32 v89, v158, v159\n"
      "v_mfma_f32_16x16x32_bf16 v[190:193], v[170:173], v[144:147], v[30:33]\n"
      "s_waitcnt lgkmcnt(1)\n"
      "v_exp_f32 v156, v156\n"
      "v_mfma_f32_16x16x32_bf16 v[144:147], v[170:173], v[200:203], v[30:33]\n"
      "ds_read_b128 v[170:173], v2 offset:31232\n"
      "v_cvt_pk_bf16_f32 v132, v132, v133\n"
      "v_mfma_f32_16x16x16_bf16 v[178:181], v[114:115], v[140:141], v[178:181]\n"
      "ds_read_b128 v[138:141], v2 offset:34304\n"
      "v_mul_f32_e64 v162, v160, v162\n"
      "v_mfma_f32_16x16x16_bf16 v[182:185], v[114:115], v[204:205], v[182:185]\n"
      "v_cvt_pk_bf16_f32 v133, v134, v135\n"
      "v_permlane16_swap_b32 v142, v100\n"
      "v_mfma_f32_16x16x16_bf16 v[190:193], v[114:115], v[126:127], v[190:193]\n"
      "v_mul_f32 v110, v160, v110\n"
      "v_cvt_pk_bf16_f32 v135, v208, v209\n"
      "v_add_f32_e64 v142, v142, v100\n"
      "v_mul_f32_e64 v163, v160, v163\n"
      "v_exp_f32_e64 v207, v207\n"
      "v_pk_mul_f32 v[128:129], v[128:129], v[160:161] op_sel_hi:[1,0]\n"
      "v_pk_mul_f32 v[112:113], v[112:113], v[160:161] op_sel_hi:[1,0]\n"
      "v_pk_mul_f32 v[130:131], v[130:131], v[160:161] op_sel_hi:[1,0]\n"
      "v_mfma_f32_16x16x16_bf16 v[144:147], v[114:115], v[218:219], v[144:147]\n"
      "v_exp_f32 v136, v136\n"
      "s_waitcnt lgkmcnt(1)\n"
      "v_mfma_f32_16x16x32_bf16 v[220:223], a[228:231], v[152:155], v[220:223]\n"
      "v_cndmask_b32 v178, v178, v182, s[58:59]\n"
      "v_cvt_pk_bf16_f32 v90, v136, 0\n"
      "v_mfma_f32_16x16x32_bf16 v[220:223], a[232:235], v[170:173], v[220:223]\n"
      "s_waitcnt lgkmcnt(0)\n"
      "v_cvt_pk_bf16_f32 v128, v128, v129\n"
      "v_cndmask_b32 v178, v178, v190, s[60:61]\n"
      "v_mfma_f32_16x16x32_bf16 v[220:223], a[236:239], v[138:141], v[220:223]\n"
      "v_cndmask_b32 v178, v178, v144, s[62:63]\n"
      "v_fmac_f32_e64 v178, s53, v8\n"
      "v_mfma_f32_16x16x32_bf16 v[166:169], a[252:255], v[152:155], v[166:169]\n"
      "s_nop 0\n"
      "v_max_f32_dpp v114, v178, v178 row_ror:8 row_mask:0xf bank_mask:0xf\n"
      "v_cvt_pk_bf16_f32 v88, v156, v157\n"
      "v_mfma_f32_16x16x32_bf16 v[166:169], v[224:227], v[170:173], v[166:169]\n"
      "v_cvt_pk_bf16_f32 v129, v130, v131\n"
      "v_fma_f32 v184, v220, v220, s52\n"
      "v_mfma_f32_16x16x32_bf16 v[166:169], v[228:231], v[138:141], v[166:169]\n"
      "v_mul_f32 v111, v160, v111\n"
      "v_max_f32_dpp v114, v114, v114 row_ror:4 row_mask:0xf bank_mask:0xf\n"
      "v_mfma_f32_16x16x32_bf16 v[186:189], v[244:247], v[152:155], v[186:189]\n"
      "s_nop 0\n"
      "v_max_f32_dpp v114, v114, v114 row_ror:2 row_mask:0xf bank_mask:0xf\n"
      "v_cvt_pk_bf16_f32 v131, v112, v113\n"
      "v_mfma_f32_16x16x32_bf16 v[186:189], v[248:251], v[170:173], v[186:189]\n"
      "v_cvt_pk_bf16_f32 v134, v206, v207\n"
      "v_max_f32_dpp v114, v114, v114 row_ror:1 row_mask:0xf bank_mask:0xf\n"
      "v_mfma_f32_16x16x32_bf16 v[186:189], v[252:255], v[138:141], v[186:189]\n"
      "s_nop 0\n"
      "v_max_f32_dpp v114, v114, v114 row_bcast:15 row_mask:0xa bank_mask:0xf\n"
      "v_cvt_pk_bf16_f32 v130, v110, v111\n"
      "v_mfma_f32_16x16x32_bf16 v[170:173], v[32:35], v[132:135], 0\n"
      "v_mul_f32 v185, v221, v221\n"
      "v_max_f32_dpp v114, v114, v114 row_bcast:31 row_mask:0xc bank_mask:0xf\n"
      "v_mfma_f32_16x16x32_bf16 v[170:173], v[32:35], v[88:91], v[170:173]\n"
      "v_cvt_pk_bf16_f32 v162, v162, v163\n"
      "v_readlane_b32 s72, v114, 63\n"
      "v_mfma_f32_16x16x32_bf16 v[112:115], v[194:197], v[132:135], 0\n"
      "ds_write_b128 v3, v[128:131] offset:256\n"
      "v_pk_mul_f32 v[164:165], v[164:165], v[160:161] op_sel_hi:[1,0]\n"
      "v_pk_fma_f32 v[184:185], v[222:223], v[222:223], v[184:185]\n"
      "v_pk_fma_f32 v[184:185], v[166:167], v[166:167], v[184:185]\n"
      "v_mfma_f32_16x16x32_bf16 v[112:115], v[76:79], v[88:91], v[112:115]\n"
      "v_subrev_f32 v178, s72, v178\n"
      "v_cvt_pk_bf16_f32 v163, v164, v165\n"
      "v_mfma_f32_16x16x32_bf16 v[126:129], v[96:99], v[132:135], 0\n"
      "s_and_b32 s5, s84, s8\n"
      "v_rcp_f32_e64 v170, v170\n"
      "v_mfma_f32_16x16x32_bf16 v[126:129], v[80:83], v[88:91], v[126:129]\n"
      "ds_write_b64 v4, v[162:163] offset:256\n"
      "v_exp_f32_e64 v178, v178\n"
      "v_pk_fma_f32 v[184:185], v[168:169], v[168:169], v[184:185]\n"
      "v_pk_mul_f32 v[114:115], v[114:115], v[170:171] op_sel_hi:[1,0]\n"
      "v_cvt_pk_bf16_f32 v178, v178, v178\n"
      "v_pk_mul_f32 v[112:113], v[112:113], v[170:171] op_sel_hi:[1,0]\n"
      "v_pk_fma_f32 v[184:185], v[186:187], v[186:187], v[184:185]\n"
      "v_pk_fma_f32 v[184:185], v[188:189], v[188:189], v[184:185]\n"
      "v_cvt_pk_bf16_f32 v112, v112, v113\n"
      "v_pk_mul_f32 v[126:127], v[126:127], v[170:171] op_sel_hi:[1,0]\n"
      "v_cvt_pk_bf16_f32 v113, v114, v115\n"
      "v_pk_mul_f32 v[128:129], v[128:129], v[170:171] op_sel_hi:[1,0]\n"
      "v_mfma_f32_16x16x32_bf16 v[160:163], v[104:107], v[132:135], 0\n"
      "ds_write_b16 v13, v178\n"
      "v_cvt_pk_bf16_f32 v114, v126, v127\n"
      "v_mfma_f32_16x16x32_bf16 v[160:163], v[84:87], v[88:91], v[160:163]\n"
      "ds_read_b128 v[178:181], v14\n"
      "v_add_f32 v184, v184, v185\n"
      "v_cvt_pk_bf16_f32 v115, v128, v129\n"
      "v_mov_b32 v100, v184\n"
      "s_add_u32 s42, s42, 1\n"
      "ds_write_b128 v3, v[112:115] offset:512\n"
      "v_permlane16_swap_b32 v184, v100\n"
      "v_add_f32_e64 v184, v184, v100\n"
      "v_pk_mul_f32 v[160:161], v[160:161], v[170:171] op_sel_hi:[1,0]\n"
      "v_pk_mul_f32 v[162:163], v[162:163], v[170:171] op_sel_hi:[1,0]\n"
      "v_permlane32_swap_b32 v142, v184\n"
      "v_add_f32 v142, v142, v184\n"
      "s_add_u32 s43, s43, 1\n"
      "ds_write_b32 v31, v142\n"
      "v_cvt_pk_bf16_f32 v160, v160, v161\n"
      "v_cvt_pk_bf16_f32 v161, v162, v163\n"
      "ds_read_b128 v[100:103], v14 offset:64\n"
      "ds_write_b64 v4, v[160:161] offset:512\n"
      "s_waitcnt lgkmcnt(4)\n"
      "v_mfma_f32_16x16x32_bf16 v[160:163], v[178:181], v[32:35], 0\n"
      "s_waitcnt lgkmcnt(1)\n"
      "v_mfma_f32_16x16x32_bf16 v[160:163], v[100:103], v[32:35], v[160:163]\n"
      "s_cmp_ge_u32 s95, 0x40000000\n"
      "s_cselect_b64 vcc, -1, s[56:57]\n"
      "s_waitcnt lgkmcnt(0)\n"
      "s_barrier\n"
      "s_cbranch_vccz L_sel1_%=\n"
      "s_branch L_disp1_%=\n"
      "L_tail1_%=:\n"
      "s_nop 4\n"
      "ds_read_b128 v[130:133], v9\n"
      "v_mfma_f32_16x16x32_bf16 v[112:115], v[178:181], v[194:197], 0\n"
      "ds_read_b128 v[194:197], v2\n"
      "ds_read_b128 v[136:139], v2 offset:6144\n"
      "v_mfma_f32_16x16x32_bf16 v[112:115], v[100:103], v[76:79], v[112:115]\n"
      "ds_read_b128 v[202:205], v2 offset:6400\n"
      "ds_read_b128 v[182:185], v2 offset:3072\n"
      "v_mfma_f32_16x16x32_bf16 v[126:129], v[178:181], v[96:99], 0\n"
      "ds_read_b128 v[96:99], v7\n"
      "ds_read_b128 v[206:209], v2 offset:12288\n"
      "ds_read_b128 v[198:201], v2 offset:3328\n"
      "v_mfma_f32_16x16x32_bf16 v[126:129], v[100:103], v[80:83], v[126:129]\n"
      "ds_read_b128 v[142:145], v2 offset:9216\n"
      "v_mfma_f32_16x16x32_bf16 v[152:155], v[178:181], v[104:107], 0\n"
      "s_waitcnt lgkmcnt(8)\n"
      "v_add_f32 v132, v132, v133\n"
      "v_add_f32 v130, v130, v131\n"
      "v_add_f32 v130, v130, v132\n"
      "v_rsq_f32 v130, v130\n"
      "v_mfma_f32_16x16x32_bf16 v[152:155], v[100:103], v[84:87], v[152:155]\n"
      "s_waitcnt lgkmcnt(7)\n"
      "v_mul_f32 v130, 0x415db3d7, v130\n"
      "v_mov_b32 v132, v130\n"
      "s_nop 1\n"
      "v_permlane32_swap_b32 v130, v132\n"
      "v_pk_mul_f32 v[186:187], v[186:187], v[132:133] op_sel_hi:[1,0]\n"
      "v_mov_b32 v131, v130\n"
      "v_pk_mul_f32 v[222:223], v[222:223], v[132:133] op_sel_hi:[1,0]\n"
      "v_pk_mul_f32 v[188:189], v[188:189], v[132:133] op_sel_hi:[1,0]\n"
      "v_permlane16_swap_b32_e64 v130, v131\n"
      "v_pk_mul_f32 v[210:211], v[210:211], v[130:131] op_sel:[0,1]\n"
      "v_pk_mul_f32 v[150:151], v[150:151], v[130:131] op_sel_hi:[1,0]\n"
      "v_pk_mul_f32 v[168:169], v[168:169], v[132:133] op_sel_hi:[1,0]\n"
      "v_pk_mul_f32 v[120:121], v[120:121], v[130:131] op_sel:[0,1]\n"
      "v_pk_mul_f32 v[174:175], v[174:175], v[130:131] op_sel_hi:[1,0]\n"
      "v_pk_mul_f32 v[148:149], v[148:149], v[130:131] op_sel_hi:[1,0]\n"
      "v_pk_mul_f32 v[220:221], v[220:221], v[132:133] op_sel_hi:[1,0]\n"
      "v_pk_mul_f32 v[166:167], v[166:167], v[132:133] op_sel_hi:[1,0]\n"
      "v_pk_mul_f32 v[176:177], v[176:177], v[130:131] op_sel_hi:[1,0]\n"
      "v_cvt_pk_bf16_f32 v148, v148, v149\n"
      "v_pk_mul_f32 v[122:123], v[122:123], v[130:131] op_sel:[0,1]\n"
      "v_cvt_pk_bf16_f32 v210, v210, v211\n"
      "v_cvt_pk_bf16_f32 v220, v220, v221\n"
      "v_pk_mul_f32 v[124:125], v[124:125], v[130:131] op_sel:[0,1]\n"
      "v_cvt_pk_bf16_f32 v174, v174, v175\n"
      "v_pk_mul_f32 v[212:213], v[212:213], v[130:131] op_sel:[0,1]\n"
      "v_pk_mul_f32 v[118:119], v[118:119], v[130:131] op_sel:[0,1]\n"
      "v_cvt_pk_bf16_f32 v118, v118, v119\n"
      "v_cvt_pk_bf16_f32 v211, v212, v213\n"
      "v_pk_mul_f32 v[214:215], v[214:215], v[130:131] op_sel_hi:[1,0]\n"
      "v_pk_mul_f32 v[216:217], v[216:217], v[130:131] op_sel_hi:[1,0]\n"
      "v_cvt_pk_bf16_f32 v149, v150, v151\n"
      "v_cvt_pk_bf16_f32 v221, v222, v223\n"
      "v_cvt_pk_bf16_f32 v213, v124, v125\n"
      "v_cvt_pk_bf16_f32 v119, v120, v121\n"
      "v_cvt_pk_bf16_f32 v186, v186, v187\n"
      "v_cvt_pk_bf16_f32 v175, v176, v177\n"
      "v_cvt_pk_bf16_f32 v177, v216, v217\n"
      "v_cvt_pk_bf16_f32 v222, v166, v167\n"
      "v_cvt_pk_bf16_f32 v187, v188, v189\n"
      "v_cvt_pk_bf16_f32 v212, v122, v123\n"
      "v_cvt_pk_bf16_f32 v223, v168, v169\n"
      "v_cvt_pk_bf16_f32 v176, v214, v215\n"
      "s_mul_i32 s92, s40, 3072\n"
      "ds_write_b128 v5, v[220:223] offset:30720\n"
      "ds_write_b128 v5, v[210:213] offset:24576\n"
      "ds_write_b64 v6, v[186:187] offset:30720\n"
      "ds_write_b64 v6, v[148:149] offset:18432\n"
      "ds_write_b128 v5, v[174:177] offset:18432\n"
      "ds_write_b64 v6, v[118:119] offset:24576\n"
      "v_mfma_f32_16x16x32_bf16 v[174:177], a[216:219], v[194:197], 0\n"
      "ds_read_b128 v[210:213], v2 offset:15360\n"
      "s_waitcnt lgkmcnt(6)\n"
      "v_mfma_f32_16x16x32_bf16 v[174:177], a[220:223], v[182:185], v[174:177]\n"
      "ds_read_b128 v[104:107], v2 offset:15872\n"
      "v_mov_b32_dpp v160, v160 quad_perm:[0,0,0,0] row_mask:0xf bank_mask:0xf\n"
      "v_mfma_f32_16x16x32_bf16 v[214:217], a[240:243], v[194:197], 0\n"
      "ds_read_b128 v[220:223], v7 offset:8192\n"
      "v_subrev_u32 v161, 115456, v9\n"
      "v_mfma_f32_16x16x32_bf16 v[214:217], a[244:247], v[182:185], v[214:217]\n"
      "ds_read_b128 v[170:173], v2 offset:12800\n"
      "v_subrev_u32 v102, 115456, v9\n"
      "v_mfma_f32_16x16x32_bf16 v[148:151], v[232:235], v[194:197], 0\n"
      "v_add_u32 v103, s92, v161\n"
      "v_mfma_f32_16x16x32_bf16 v[148:151], v[236:239], v[182:185], v[148:151]\n"
      "buffer_store_dwordx4 v[96:99], v20, s[24:27], s85 offen sc1\n"
      "v_mfma_f32_16x16x32_bf16 v[174:177], a[224:227], v[136:139], v[174:177]\n"
      "ds_read_b128 v[166:169], v2 offset:256\n"
      "v_lshrrev_b32_e64 v161, 4, v103\n"
      "v_mfma_f32_16x16x32_bf16 v[174:177], a[228:231], v[142:145], v[174:177]\n"
      "v_and_b32 v85, 0x3fffffff, v37\n"
      "v_mfma_f32_16x16x32_bf16 v[214:217], a[248:251], v[136:139], v[214:217]\n"
      "ds_read_b128 v[186:189], v2 offset:15616\n"
      "v_mfma_f32_16x16x32_bf16 v[214:217], a[252:255], v[142:145], v[214:217]\n"
      "ds_read_b128 v[96:99], v7 offset:4096\n"
      "v_mfma_f32_16x16x32_bf16 v[148:151], v[240:243], v[136:139], v[148:151]\n"
      "ds_read_b128 v[194:197], v2 offset:12544\n"
      "v_mfma_f32_16x16x32_bf16 v[148:151], v[244:247], v[142:145], v[148:151]\n"
      "ds_read_b128 v[178:181], v2 offset:6656\n"
      "v_mfma_f32_16x16x32_bf16 v[174:177], a[232:235], v[206:209], v[174:177]\n"
      "s_waitcnt lgkmcnt(6)\n"
      "v_add_u32 v100, 128, v161\n"
      "v_add_u32 v86, 64, v161\n"
      "v_mfma_f32_16x16x32_bf16 v[174:177], a[236:239], v[210:213], v[174:177]\n"
      "ds_read_b128 v[128:131], v2 offset:9472\n"
      "v_mfma_f32_16x16x32_bf16 v[214:217], v[224:227], v[206:209], v[214:217]\n"
      "v_ashrrev_i32 v84, 31, v37\n"
      "v_and_b32 v138, 0x300, v102\n"
      "v_mfma_f32_16x16x32_bf16 v[214:217], v[228:231], v[210:213], v[214:217]\n"
      "ds_read_b128 v[162:165], v2 offset:512\n"
      "v_add_u32 v102, v102, v138\n"
      "v_mfma_f32_16x16x32_bf16 v[148:151], v[248:251], v[206:209], v[148:151]\n"
      "v_cndmask_b32 v112, v112, v126, s[58:59]\n"
      "v_mfma_f32_16x16x32_bf16 v[148:151], v[252:255], v[210:213], v[148:151]\n"
      "s_waitcnt lgkmcnt(3)\n"
      "v_add_u32 v102, 150784, v102\n"
      "v_fma_f32 v138, v174, v174, s52\n"
      "v_mfma_f32_16x16x32_bf16 v[210:213], a[216:219], v[166:169], 0\n"
      "v_mul_f32 v139, v175, v175\n"
      "v_mul_u32_u24 v80, 2731, v100\n"
      "v_mfma_f32_16x16x32_bf16 v[210:213], a[220:223], v[198:201], v[210:213]\n"
      "v_subrev_u32 v81, 73728, v6\n"
      "v_fmac_f32_e64 v139, v177, v177\n"
      "v_mfma_f32_16x16x32_bf16 v[122:125], a[240:243], v[166:169], 0\n"
      "ds_read_b128 v[114:117], v2 offset:9728\n"
      "v_fmac_f32 v138, v176, v176\n"
      "v_mfma_f32_16x16x32_bf16 v[122:125], a[244:247], v[198:201], v[122:125]\n"
      "v_fmac_f32 v139, v215, v215\n"
      "v_fmac_f32 v138, v214, v214\n"
      "v_mfma_f32_16x16x32_bf16 v[118:121], v[232:235], v[166:169], 0\n"
      "ds_read_b128 v[154:157], v2 offset:3584\n"
      "v_fmac_f32 v139, v217, v217\n"
      "v_mfma_f32_16x16x32_bf16 v[118:121], v[236:239], v[198:201], v[118:121]\n"
      "s_waitcnt lgkmcnt(2)\n"
      "v_add_u32 v85, 1179648, v85\n"
      "v_lshrrev_b32 v80, 16, v80\n"
      "v_mfma_f32_16x16x32_bf16 v[210:213], a[224:227], v[202:205], v[210:213]\n"
      "ds_read_b128 v[182:185], v102 offset:4096\n"
      "v_mfma_f32_16x16x32_bf16 v[210:213], a[228:231], v[128:131], v[210:213]\n"
      "buffer_store_dwordx4 v[96:99], v21, s[24:27], s85 offen sc1\n"
      "v_mfma_f32_16x16x32_bf16 v[122:125], a[248:251], v[202:205], v[122:125]\n"
      "s_nop 0\n"
      "ds_read_b128 v[96:99], v7 offset:16384\n"
      "v_and_b32 v84, 100270464, v84\n"
      "v_mfma_f32_16x16x32_bf16 v[122:125], a[252:255], v[128:131], v[122:125]\n"
      "v_fmac_f32 v138, v216, v216\n"
      "v_sub_u32_e64 v85, v85, v84\n"
      "v_mfma_f32_16x16x32_bf16 v[118:121], v[240:243], v[202:205], v[118:121]\n"
      "v_rcp_f32 v160, v160\n"
      "v_mfma_f32_16x16x32_bf16 v[118:121], v[244:247], v[128:131], v[118:121]\n"
      "v_mul_u32_u24 v82, 2731, v86\n"
      "v_fmac_f32 v138, v148, v148\n"
      "v_mfma_f32_16x16x32_bf16 v[210:213], a[232:235], v[194:197], v[210:213]\n"
      "v_cndmask_b32 v112, v112, v152, s[60:61]\n"
      "v_fmac_f32 v139, v149, v149\n"
      "v_mfma_f32_16x16x32_bf16 v[210:213], a[236:239], v[186:189], v[210:213]\n"
      "v_add_u32 v78, 0, v161\n"
      "v_mul_u32_u24 v76, 24, v80\n"
      "v_mfma_f32_16x16x32_bf16 v[122:125], v[224:227], v[194:197], v[122:125]\n"
      "v_mul_u32_u24 v77, 2731, v78\n"
      "v_mul_f32 v112, v160, v112\n"
      "v_mfma_f32_16x16x32_bf16 v[122:125], v[228:231], v[186:189], v[122:125]\n"
      "buffer_store_dwordx4 v[220:223], v22, s[24:27], s85 offen sc1\n"
      "v_mfma_f32_16x16x32_bf16 v[118:121], v[248:251], v[194:197], v[118:121]\n"
      "ds_read_b128 v[194:197], v102 offset:6144\n"
      "v_mul_f32_e64 v137, v211, v211\n"
      "v_mfma_f32_16x16x32_bf16 v[118:121], v[252:255], v[186:189], v[118:121]\n"
      "v_fma_f32 v136, v210, v210, s52\n"
      "v_fmac_f32 v137, v213, v213\n"
      "v_mfma_f32_16x16x32_bf16 v[220:223], a[216:219], v[162:165], 0\n"
      "s_waitcnt lgkmcnt(1)\n"
      "v_cvt_pk_bf16_f32 v112, v112, v112\n"
      "v_fmac_f32 v138, v150, v150\n"
      "v_mfma_f32_16x16x32_bf16 v[220:223], a[220:223], v[154:157], v[220:223]\n"
      "v_fmac_f32 v136, v212, v212\n"
      "v_mfma_f32_16x16x32_bf16 v[166:169], a[240:243], v[162:165], 0\n"
      "ds_read_b128 v[126:129], v102 offset:10240\n"
      "v_fmac_f32 v136, v122, v122\n"
      "v_mfma_f32_16x16x32_bf16 v[166:169], a[244:247], v[154:157], v[166:169]\n"
      "ds_read_b128 v[158:161], v102 offset:8192\n"
      "v_lshrrev_b32 v77, 16, v77\n"
      "v_mfma_f32_16x16x32_bf16 v[186:189], v[232:235], v[162:165], 0\n"
      "ds_write_b16 v12, v112\n"
      "v_mfma_f32_16x16x32_bf16 v[186:189], v[236:239], v[154:157], v[186:189]\n"
      "ds_read_b128 v[152:155], v7 offset:12288\n"
      "v_fmac_f32 v137, v123, v123\n"
      "v_mfma_f32_16x16x32_bf16 v[220:223], a[224:227], v[178:181], v[220:223]\n"
      "v_fmac_f32 v137, v125, v125\n"
      "v_fmac_f32 v136, v124, v124\n"
      "v_mfma_f32_16x16x32_bf16 v[220:223], a[228:231], v[114:117], v[220:223]\n"
      "v_fmac_f32 v139, v151, v151\n"
      "v_mfma_f32_16x16x32_bf16 v[166:169], a[248:251], v[178:181], v[166:169]\n"
      "ds_read_b128 v[162:165], v102\n"
      "v_mfma_f32_16x16x32_bf16 v[166:169], a[252:255], v[114:117], v[166:169]\n"
      "ds_read_b128 v[110:113], v102 offset:2048\n"
      "v_add_f32 v138, v138, v139\n"
      "v_mfma_f32_16x16x32_bf16 v[186:189], v[240:243], v[178:181], v[186:189]\n"
      "v_fmac_f32 v136, v118, v118\n"
      "v_fmac_f32 v137, v119, v119\n"
      "v_mfma_f32_16x16x32_bf16 v[186:189], v[244:247], v[114:117], v[186:189]\n"
      "v_fmac_f32 v137, v121, v121\n"
      "v_fmac_f32 v136, v120, v120\n"
      "v_mfma_f32_16x16x32_bf16 v[220:223], a[232:235], v[170:173], v[220:223]\n"
      "s_waitcnt lgkmcnt(2)\n"
      "v_add_f32 v136, v136, v137\n"
      "v_mul_u32_u24 v135, 24, v77\n"
      "v_mfma_f32_16x16x32_bf16 v[220:223], a[236:239], v[104:107], v[220:223]\n"
      "buffer_store_dwordx4 v[152:155], v23, s[24:27], s85 offen sc1\n"
      "v_mfma_f32_16x16x32_bf16 v[166:169], v[224:227], v[170:173], v[166:169]\n"
      "v_permlane16_swap_b32_e64 v138, v136\n"
      "v_mfma_f32_16x16x32_bf16 v[166:169], v[228:231], v[104:107], v[166:169]\n"
      "v_bfe_i32 v84, v37, 30, 1\n"
      "v_add_f32 v138, v138, v136\n"
      "v_mfma_f32_16x16x32_bf16 v[186:189], v[248:251], v[170:173], v[186:189]\n"
      "s_nop 0\n"
      "v_mul_f32_e64 v115, v221, v221\n"
      "v_fma_f32 v114, v220, v220, s52\n"
      "v_mfma_f32_16x16x32_bf16 v[186:189], v[252:255], v[104:107], v[186:189]\n"
      "s_waitcnt lgkmcnt(0)\n"
      "v_fmac_f32 v115, v223, v223\n"
      "v_fmac_f32_e64 v114, v222, v222\n"
      "v_mfma_f32_16x16x32_bf16 v[104:107], a[216:219], v[162:165], 0\n"
      "v_fmac_f32 v115, v167, v167\n"
      "v_fmac_f32 v114, v166, v166\n"
      "v_mfma_f32_16x16x32_bf16 v[104:107], a[220:223], v[110:113], v[104:107]\n"
      "v_fmac_f32 v115, v169, v169\n"
      "v_fmac_f32 v114, v168, v168\n"
      "v_mfma_f32_16x16x32_bf16 v[170:173], a[240:243], v[162:165], 0\n"
      "buffer_store_dwordx4 v[96:99], v25, s[24:27], s85 offen sc1\n"
      "v_pk_fma_f32 v[114:115], v[186:187], v[186:187], v[114:115]\n"
      "v_pk_fma_f32 v[114:115], v[188:189], v[188:189], v[114:115]\n"
      "v_mfma_f32_16x16x32_bf16 v[170:173], a[244:247], v[110:113], v[170:173]\n"
      "v_add_f32 v114, v114, v115\n"
      "v_mov_b32 v136, v114\n"
      "v_mfma_f32_16x16x32_bf16 v[96:99], v[232:235], v[162:165], 0\n"
      "s_nop 0\n"
      "v_permlane16_swap_b32 v114, v136\n"
      "v_mfma_f32_16x16x32_bf16 v[96:99], v[236:239], v[110:113], v[96:99]\n"
      "v_add_f32 v114, v114, v136\n"
      "v_mfma_f32_16x16x32_bf16 v[104:107], a[224:227], v[182:185], v[104:107]\n"
      "s_nop 0\n"
      "v_permlane32_swap_b32 v138, v114\n"
      "v_add_f32 v138, v138, v114\n"
      "v_mfma_f32_16x16x32_bf16 v[104:107], a[228:231], v[194:197], v[104:107]\n"
      "ds_write_b32 v31, v138 offset:1024\n"
      "v_mfma_f32_16x16x32_bf16 v[170:173], a[248:251], v[182:185], v[170:173]\n"
      "v_subrev_u32 v134, 73728, v5\n"
      "v_mfma_f32_16x16x32_bf16 v[170:173], a[252:255], v[194:197], v[170:173]\n"
      "v_sub_u32 v76, v100, v76\n"
      "v_readlane_b32 s85, v37, s42\n"
      "v_mfma_f32_16x16x32_bf16 v[96:99], v[240:243], v[182:185], v[96:99]\n"
      "s_or_b32 s95, s85, s48\n"
      "v_lshrrev_b32 v82, 16, v82\n"
      "v_sub_u32 v135, v78, v135\n"
      "v_and_b32 v100, 7, v80\n"
      "v_mfma_f32_16x16x32_bf16 v[96:99], v[244:247], v[194:197], v[96:99]\n"
      "v_xor_b32 v76, v76, v100\n"
      "v_mfma_f32_16x16x32_bf16 v[104:107], a[232:235], v[158:161], v[104:107]\n"
      "s_cmp_ge_u32 s95, 0x40000000\n"
      "s_cbranch_scc1 L_rare_27_%=\n"
      "L_back_28_%=:\n"
      "v_mfma_f32_16x16x32_bf16 v[104:107], a[236:239], v[126:129], v[104:107]\n"
      "s_waitcnt lgkmcnt(0)\n"
      "s_barrier\n"
      "ds_read_b128 v[194:197], v9 offset:1024\n"
      "v_mfma_f32_16x16x32_bf16 v[170:173], v[224:227], v[158:161], v[170:173]\n"
      "ds_read_b128 v[112:115], v102 offset:256\n"
      "ds_read_b128 v[178:181], v102 offset:2304\n"
      "v_mfma_f32_16x16x32_bf16 v[170:173], v[228:231], v[126:129], v[170:173]\n"
      "ds_read_b128 v[152:155], v7 offset:18432\n"
      "v_fma_f32 v100, v104, v104, s52\n"
      "v_mul_f32_e64 v101, v105, v105\n"
      "v_mfma_f32_16x16x32_bf16 v[96:99], v[248:251], v[158:161], v[96:99]\n"
      "ds_read_b128 v[160:163], v7 offset:22528\n"
      "ds_read_b128 v[136:139], v7 offset:26624\n"
      "ds_read_b128 v[156:159], v102 offset:4352\n"
      "v_pk_fma_f32 v[100:101], v[106:107], v[106:107], v[100:101]\n"
      "v_pk_fma_f32 v[100:101], v[170:171], v[170:171], v[100:101]\n"
      "v_mfma_f32_16x16x32_bf16 v[96:99], v[252:255], v[126:129], v[96:99]\n"
      "ds_read_b128 v[126:129], v7 offset:34816\n"
      "s_waitcnt lgkmcnt(7)\n"
      "v_add_f32 v194, v194, v195\n"
      "v_add_f32 v196, v196, v197\n"
      "v_add_f32 v194, v194, v196\n"
      "v_rsq_f32_e64 v194, v194\n"
      "s_waitcnt lgkmcnt(6)\n"
      "v_mfma_f32_16x16x32_bf16 v[206:209], a[216:219], v[112:115], 0\n"
      "s_waitcnt lgkmcnt(5)\n"
      "v_mul_f32 v194, 0x415db3d7, v194\n"
      "v_mov_b32_e64 v196, v194\n"
      "v_mfma_f32_16x16x32_bf16 v[206:209], a[220:223], v[178:181], v[206:209]\n"
      "ds_read_b128 v[202:205], v102 offset:6400\n"
      "ds_read_b128 v[182:185], v102 offset:8448\n"
      "v_mfma_f32_16x16x32_bf16 v[130:133], a[240:243], v[112:115], 0\n"
      "s_waitcnt lgkmcnt(5)\n"
      "v_permlane32_swap_b32 v194, v196\n"
      "v_mfma_f32_16x16x32_bf16 v[130:133], a[244:247], v[178:181], v[130:133]\n"
      "buffer_store_dwordx4 v[152:155], v20, s[24:27], s85 offen sc1\n"
      "v_mov_b32_e64 v195, v194\n"
      "v_pk_mul_f32 v[220:221], v[220:221], v[196:197] op_sel_hi:[1,0]\n"
      "v_pk_mul_f32 v[222:223], v[222:223], v[196:197] op_sel_hi:[1,0]\n"
      "v_mfma_f32_16x16x32_bf16 v[152:155], v[232:235], v[112:115], 0\n"
      "s_waitcnt lgkmcnt(2)\n"
      "v_permlane16_swap_b32_e64 v194, v195\n"
      "v_mfma_f32_16x16x32_bf16 v[152:155], v[236:239], v[178:181], v[152:155]\n"
      "buffer_store_dwordx4 v[160:163], v21, s[24:27], s85 offen sc1\n"
      "v_pk_fma_f32 v[100:101], v[172:173], v[172:173], v[100:101]\n"
      "v_pk_mul_f32 v[188:189], v[188:189], v[196:197] op_sel_hi:[1,0]\n"
      "v_pk_mul_f32 v[166:167], v[166:167], v[196:197] op_sel_hi:[1,0]\n"
      "v_pk_mul_f32 v[174:175], v[174:175], v[194:195] op_sel_hi:[1,0]\n"
      "v_pk_mul_f32 v[176:177], v[176:177], v[194:195] op_sel_hi:[1,0]\n"
      "v_pk_mul_f32 v[186:187], v[186:187], v[196:197] op_sel_hi:[1,0]\n"
      "v_pk_mul_f32 v[148:149], v[148:149], v[194:195] op_sel_hi:[1,0]\n"
      "v_pk_mul_f32 v[120:121], v[120:121], v[194:195] op_sel:[0,1]\n"
      "v_mfma_f32_16x16x32_bf16 v[206:209], a[224:227], v[156:159], v[206:209]\n"
      "s_waitcnt lgkmcnt(0)\n"
      "v_cvt_pk_bf16_f32 v174, v174, v175\n"
      "v_cvt_pk_bf16_f32 v186, v186, v187\n"
      "v_mfma_f32_16x16x32_bf16 v[206:209], a[228:231], v[202:205], v[206:209]\n"
      "buffer_store_dwordx4 v[136:139], v22, s[24:27], s85 offen sc1\n"
      "v_pk_mul_f32 v[212:213], v[212:213], v[194:195] op_sel:[0,1]\n"
      "v_pk_mul_f32 v[214:215], v[214:215], v[194:195] op_sel_hi:[1,0]\n"
      "v_pk_mul_f32 v[118:119], v[118:119], v[194:195] op_sel:[0,1]\n"
      "v_mfma_f32_16x16x32_bf16 v[130:133], a[248:251], v[156:159], v[130:133]\n"
      "ds_read_b128 v[160:163], v102 offset:10496\n"
      "v_cvt_pk_bf16_f32 v175, v176, v177\n"
      "v_mfma_f32_16x16x32_bf16 v[130:133], a[252:255], v[202:205], v[130:133]\n"
      "v_cvt_pk_bf16_f32 v187, v188, v189\n"
      "v_cvt_pk_bf16_f32 v118, v118, v119\n"
      "v_cvt_pk_bf16_f32 v176, v214, v215\n"
      "v_cvt_pk_bf16_f32 v220, v220, v221\n"
      "v_cvt_pk_bf16_f32 v221, v222, v223\n"
      "v_pk_mul_f32 v[150:151], v[150:151], v[194:195] op_sel_hi:[1,0]\n"
      "v_pk_mul_f32 v[168:169], v[168:169], v[196:197] op_sel_hi:[1,0]\n"
      "v_pk_mul_f32 v[216:217], v[216:217], v[194:195] op_sel_hi:[1,0]\n"
      "v_pk_mul_f32 v[210:211], v[210:211], v[194:195] op_sel:[0,1]\n"
      "v_mfma_f32_16x16x32_bf16 v[152:155], v[240:243], v[156:159], v[152:155]\n"
      "ds_read_b128 v[178:181], v7 offset:30720\n"
      "v_cvt_pk_bf16_f32 v222, v166, v167\n"
      "v_mfma_f32_16x16x32_bf16 v[152:155], v[244:247], v[202:205], v[152:155]\n"
      "ds_write_b64 v6, v[186:187] offset:12288\n"
      "v_cvt_pk_bf16_f32 v223, v168, v169\n"
      "v_mfma_f32_16x16x32_bf16 v[206:209], a[232:235], v[182:185], v[206:209]\n"
      "s_waitcnt lgkmcnt(2)\n"
      "v_cvt_pk_bf16_f32 v148, v148, v149\n"
      "v_cvt_pk_bf16_f32 v177, v216, v217\n"
      "v_mfma_f32_16x16x32_bf16 v[206:209], a[236:239], v[160:163], v[206:209]\n"
      "ds_write_b128 v5, v[174:177]\n"
      "v_mfma_f32_16x16x32_bf16 v[130:133], v[224:227], v[182:185], v[130:133]\n"
      "v_cvt_pk_bf16_f32 v149, v150, v151\n"
      "v_cvt_pk_bf16_f32 v119, v120, v121\n"
      "v_mfma_f32_16x16x32_bf16 v[130:133], v[228:231], v[160:163], v[130:133]\n"
      "ds_write_b64 v6, v[118:119] offset:6144\n"
      "v_mfma_f32_16x16x32_bf16 v[152:155], v[248:251], v[182:185], v[152:155]\n"
      "ds_write_b64 v6, v[148:149]\n"
      "v_mfma_f32_16x16x32_bf16 v[152:155], v[252:255], v[160:163], v[152:155]\n"
      "ds_write_b128 v5, v[220:223] offset:12288\n"
      "v_fma_f32 v220, v206, v206, s52\n"
      "v_mul_f32 v221, v207, v207\n"
      "v_pk_fma_f32 v[220:221], v[208:209], v[208:209], v[220:221]\n"
      "v_pk_fma_f32 v[220:221], v[130:131], v[130:131], v[220:221]\n"
      "v_pk_fma_f32 v[220:221], v[132:133], v[132:133], v[220:221]\n"
      "v_pk_fma_f32 v[100:101], v[96:97], v[96:97], v[100:101]\n"
      "v_pk_fma_f32 v[100:101], v[98:99], v[98:99], v[100:101]\n"
      "v_pk_fma_f32 v[220:221], v[152:153], v[152:153], v[220:221]\n"
      "v_pk_fma_f32 v[220:221], v[154:155], v[154:155], v[220:221]\n"
      "v_add_f32 v100, v100, v101\n"
      "v_add_f32 v220, v220, v221\n"
      "v_cvt_pk_bf16_f32 v210, v210, v211\n"
      "v_pk_mul_f32 v[122:123], v[122:123], v[194:195] op_sel:[0,1]\n"
      "v_cvt_pk_bf16_f32 v211, v212, v213\n"
      "v_permlane16_swap_b32 v100, v220\n"
      "v_add_f32 v100, v100, v220\n"
      "v_mov_b32 v220, v100\n"
      "v_pk_mul_f32 v[124:125], v[124:125], v[194:195] op_sel:[0,1]\n"
      "v_cvt_pk_bf16_f32 v213, v124, v125\n"
      "v_cvt_pk_bf16_f32 v212, v122, v123\n"
      "v_permlane32_swap_b32 v100, v220\n"
      "v_add_f32_e64 v100, v100, v220\n"
      "s_add_u32 s42, s42, 1\n"
      "ds_write_b128 v5, v[210:213] offset:6144\n"
      "ds_write_b32 v31, v100 offset:4608\n"
      "s_waitcnt lgkmcnt(2)\n"
      "buffer_store_dwordx4 v[178:181], v23, s[24:27], s85 offen sc1\n"
      "v_mul_u32_u24 v100, 24, v82\n"
      "v_and_b32 v84, 196224, v84\n"
      "v_and_b32 v78, 7, v77\n"
      "v_sub_u32_e64 v100, v86, v100\n"
      "v_lshl_add_u32 v77, v77, 2, 12\n"
      "v_and_b32_e64 v86, 7, v82\n"
      "buffer_store_dwordx4 v[126:129], v25, s[24:27], s85 offen sc1\n"
      "s_waitcnt lgkmcnt(0)\n"
      "s_barrier\n"
      "ds_read_b128 v[178:181], v9 offset:4608\n"
      "v_lshl_add_u32 v80, v80, 2, 12\n"
      "v_xor_b32_e64 v100, v100, v86\n"
      "v_readlane_b32 s85, v37, s42\n"
      "v_sub_u32 v85, v85, v84\n"
      "s_or_b32 s95, s85, s48\n"
      "ds_read_b128 v[122:125], v7\n"
      "ds_read_b128 v[210:213], v7 offset:8192\n"
      "ds_bpermute_b32 v84, v80, v85\n"
      "v_lshl_add_u32 v82, v82, 2, 12\n"
      "s_cmp_ge_u32 s95, 0x40000000\n"
      "s_cbranch_scc1 L_rare_31_%=\n"
      "L_back_32_%=:\n"
      "ds_read_b128 v[220:223], v7 offset:12288\n"
      "ds_read_b128 v[160:163], v7 offset:4096\n"
      "s_waitcnt lgkmcnt(5)\n"
      "v_add_f32 v180, v180, v181\n"
      "v_add_f32 v178, v178, v179\n"
      "v_add_f32 v178, v178, v180\n"
      "v_rsq_f32 v178, v178\n"
      "s_nop 0\n"
      "v_mul_f32 v178, 0x415db3d7, v178\n"
      "v_mov_b32 v180, v178\n"
      "s_nop 1\n"
      "v_permlane32_swap_b32 v178, v180\n"
      "v_mov_b32_e64 v179, v178\n"
      "s_waitcnt lgkmcnt(2)\n"
      "buffer_store_dwordx4 v[122:125], v20, s[24:27], s85 offen sc1\n"
      "v_permlane16_swap_b32_e64 v178, v179\n"
      "v_pk_mul_f32 v[106:107], v[106:107], v[178:179] op_sel_hi:[1,0]\n"
      "v_pk_mul_f32 v[104:105], v[104:105], v[178:179] op_sel_hi:[1,0]\n"
      "v_pk_mul_f32 v[132:133], v[132:133], v[178:179] op_sel:[0,1]\n"
      "v_pk_mul_f32 v[208:209], v[208:209], v[178:179] op_sel:[0,1]\n"
      "v_pk_mul_f32 v[172:173], v[172:173], v[178:179] op_sel_hi:[1,0]\n"
      "v_cvt_pk_bf16_f32 v104, v104, v105\n"
      "v_pk_mul_f32 v[206:207], v[206:207], v[178:179] op_sel:[0,1]\n"
      "v_pk_mul_f32 v[130:131], v[130:131], v[178:179] op_sel:[0,1]\n"
      "v_cvt_pk_bf16_f32 v105, v106, v107\n"
      "v_cvt_pk_bf16_f32 v206, v206, v207\n"
      "v_pk_mul_f32 v[154:155], v[154:155], v[178:179] op_sel:[0,1]\n"
      "v_pk_mul_f32 v[170:171], v[170:171], v[178:179] op_sel_hi:[1,0]\n"
      "v_cvt_pk_bf16_f32 v107, v172, v173\n"
      "v_pk_mul_f32 v[152:153], v[152:153], v[178:179] op_sel:[0,1]\n"
      "v_pk_mul_f32 v[96:97], v[96:97], v[178:179] op_sel_hi:[1,0]\n"
      "v_cvt_pk_bf16_f32 v207, v208, v209\n"
      "v_cvt_pk_bf16_f32 v106, v170, v171\n"
      "ds_write_b128 v134, v[104:107]\n"
      "v_cvt_pk_bf16_f32 v152, v152, v153\n"
      "v_cvt_pk_bf16_f32 v208, v130, v131\n"
      "v_cvt_pk_bf16_f32 v209, v132, v133\n"
      "v_cvt_pk_bf16_f32 v96, v96, v97\n"
      "v_cvt_pk_bf16_f32 v153, v154, v155\n"
      "ds_write_b128 v134, v[206:209] offset:6144\n"
      "ds_write_b64 v81, v[152:153] offset:6144\n"
      "v_pk_mul_f32 v[98:99], v[98:99], v[178:179] op_sel_hi:[1,0]\n"
      "v_cvt_pk_bf16_f32 v97, v98, v99\n"
      "s_add_u32 s42, s42, 1\n"
      "ds_write_b64 v81, v[96:97]\n"
      "v_xor_b32_e64 v135, v135, v78\n"
      "s_waitcnt lgkmcnt(3)\n"
      "buffer_store_dwordx4 v[160:163], v21, s[24:27], s85 offen sc1\n"
      "s_nop 1\n"
      "ds_read_b128 v[160:163], v7 offset:16384\n"
      "ds_bpermute_b32 v78, v77, v85\n"
      "buffer_store_dwordx4 v[210:213], v22, s[24:27], s85 offen sc1\n"
      "s_waitcnt lgkmcnt(2)\n"
      "s_barrier\n"
      "ds_read_b128 v[210:213], v103\n"
      "buffer_store_dwordx4 v[220:223], v23, s[24:27], s85 offen sc1\n"
      "ds_bpermute_b32 v77, v82, v85\n"
      "s_nop 0\n"
      "ds_read_b128 v[220:223], v103 offset:1024\n"
      "s_waitcnt lgkmcnt(3)\n"
      "buffer_store_dwordx4 v[160:163], v25, s[24:27], s85 offen sc1\n"
      "s_nop 1\n"
      "ds_read_b128 v[160:163], v103 offset:2048\n"
      "v_lshl_add_u32 v78, v135, 4, v78\n"
      "v_lshl_add_u32 v84, v76, 4, v84\n"
      "s_waitcnt lgkmcnt(3)\n"
      "buffer_store_dwordx4 v[210:213], v78, s[24:27], 0 offen sc1\n"
      "s_waitcnt lgkmcnt(2)\n"
      "v_lshl_add_u32 v77, v100, 4, v77\n"
      "s_waitcnt lgkmcnt(1)\n"
      "buffer_store_dwordx4 v[220:223], v77, s[24:27], 0 offen sc1\n"
      "s_waitcnt lgkmcnt(0)\n"
      "buffer_store_dwordx4 v[160:163], v84, s[24:27], 0 offen sc1\n"
      "s_endpgm\n"
      "L_tail0_%=:\n"
      "s_nop 4\n"
      "ds_read_b128 v[134:137], v9 offset:1024\n"
      "v_mfma_f32_16x16x32_bf16 v[112:115], v[178:181], v[194:197], 0\n"
      "ds_read_b128 v[194:197], v2 offset:18432\n"
      "v_subrev_u32 v164, 115456, v9\n"
      "v_mfma_f32_16x16x32_bf16 v[112:115], v[100:103], v[76:79], v[112:115]\n"
      "ds_read_b128 v[130:133], v2 offset:27648\n"
      "ds_read_b128 v[202:205], v2 offset:30720\n"
      "ds_read_b128 v[182:185], v2 offset:24576\n"
      "v_mfma_f32_16x16x32_bf16 v[126:129], v[178:181], v[96:99], 0\n"
      "ds_read_b128 v[206:209], v2 offset:18688\n"
      "v_and_b32 v77, 0x3fffffff, v37\n"
      "v_mfma_f32_16x16x32_bf16 v[126:129], v[100:103], v[80:83], v[126:129]\n"
      "ds_read_b128 v[156:159], v2 offset:21504\n"
      "v_mfma_f32_16x16x32_bf16 v[152:155], v[178:181], v[104:107], 0\n"
      "ds_read_b128 v[104:107], v2 offset:33792\n"
      "s_waitcnt lgkmcnt(7)\n"
      "v_add_f32 v134, v134, v135\n"
      "v_add_f32 v136, v136, v137\n"
      "v_add_f32 v134, v134, v136\n"
      "v_rsq_f32_e64 v134, v134\n"
      "v_cndmask_b32 v112, v112, v126, s[58:59]\n"
      "v_mul_f32 v134, 0x415db3d7, v134\n"
      "v_mov_b32 v136, v134\n"
      "s_mul_i32 s92, s40, 3072\n"
      "v_ashrrev_i32 v78, 31, v37\n"
      "v_permlane32_swap_b32 v134, v136\n"
      "v_mov_b32 v135, v134\n"
      "v_pk_mul_f32 v[220:221], v[220:221], v[136:137] op_sel_hi:[1,0]\n"
      "v_pk_mul_f32 v[166:167], v[166:167], v[136:137] op_sel_hi:[1,0]\n"
      "v_pk_mul_f32 v[168:169], v[168:169], v[136:137] op_sel_hi:[1,0]\n"
      "v_permlane16_swap_b32_e64 v134, v135\n"
      "v_pk_mul_f32 v[212:213], v[212:213], v[134:135] op_sel:[0,1]\n"
      "v_pk_mul_f32 v[124:125], v[124:125], v[134:135] op_sel:[0,1]\n"
      "v_cvt_pk_bf16_f32 v220, v220, v221\n"
      "v_pk_mul_f32 v[210:211], v[210:211], v[134:135] op_sel:[0,1]\n"
      "v_pk_mul_f32 v[186:187], v[186:187], v[136:137] op_sel_hi:[1,0]\n"
      "v_pk_mul_f32 v[214:215], v[214:215], v[134:135] op_sel_hi:[1,0]\n"
      "v_pk_mul_f32 v[176:177], v[176:177], v[134:135] op_sel_hi:[1,0]\n"
      "v_pk_mul_f32 v[118:119], v[118:119], v[134:135] op_sel:[0,1]\n"
      "v_pk_mul_f32 v[174:175], v[174:175], v[134:135] op_sel_hi:[1,0]\n"
      "v_cvt_pk_bf16_f32 v174, v174, v175\n"
      "v_cvt_pk_bf16_f32 v118, v118, v119\n"
      "v_pk_mul_f32 v[148:149], v[148:149], v[134:135] op_sel_hi:[1,0]\n"
      "v_cvt_pk_bf16_f32 v148, v148, v149\n"
      "v_pk_mul_f32 v[222:223], v[222:223], v[136:137] op_sel_hi:[1,0]\n"
      "v_cvt_pk_bf16_f32 v186, v186, v187\n"
      "v_cvt_pk_bf16_f32 v221, v222, v223\n"
      "v_pk_mul_f32 v[122:123], v[122:123], v[134:135] op_sel:[0,1]\n"
      "v_cvt_pk_bf16_f32 v210, v210, v211\n"
      "v_pk_mul_f32 v[120:121], v[120:121], v[134:135] op_sel:[0,1]\n"
      "v_cvt_pk_bf16_f32 v211, v212, v213\n"
      "v_pk_mul_f32 v[150:151], v[150:151], v[134:135] op_sel_hi:[1,0]\n"
      "v_cvt_pk_bf16_f32 v119, v120, v121\n"
      "v_pk_mul_f32 v[216:217], v[216:217], v[134:135] op_sel_hi:[1,0]\n"
      "v_pk_mul_f32 v[188:189], v[188:189], v[136:137] op_sel_hi:[1,0]\n"
      "v_cvt_pk_bf16_f32 v213, v124, v125\n"
      "v_cvt_pk_bf16_f32 v175, v176, v177\n"
      "v_cvt_pk_bf16_f32 v176, v214, v215\n"
      "v_cvt_pk_bf16_f32 v149, v150, v151\n"
      "v_cvt_pk_bf16_f32 v212, v122, v123\n"
      "v_cvt_pk_bf16_f32 v177, v216, v217\n"
      "v_cvt_pk_bf16_f32 v187, v188, v189\n"
      "v_cvt_pk_bf16_f32 v223, v168, v169\n"
      "v_cvt_pk_bf16_f32 v222, v166, v167\n"
      "v_mfma_f32_16x16x32_bf16 v[152:155], v[100:103], v[84:87], v[152:155]\n"
      "ds_write_b64 v6, v[186:187] offset:12288\n"
      "ds_write_b128 v5, v[174:177]\n"
      "ds_write_b64 v6, v[118:119] offset:6144\n"
      "ds_write_b64 v6, v[148:149]\n"
      "ds_write_b128 v5, v[210:213] offset:6144\n"
      "ds_write_b128 v5, v[220:223] offset:12288\n"
      "s_waitcnt lgkmcnt(5)\n"
      "v_mfma_f32_16x16x32_bf16 v[174:177], a[216:219], v[194:197], 0\n"
      "ds_read_b128 v[220:223], v7 offset:18432\n"
      "v_mov_b32_dpp v160, v160 quad_perm:[0,0,0,0] row_mask:0xf bank_mask:0xf\n"
      "v_mfma_f32_16x16x32_bf16 v[174:177], a[220:223], v[156:159], v[174:177]\n"
      "ds_read_b128 v[126:129], v2 offset:18944\n"
      "v_and_b32 v78, 100270464, v78\n"
      "v_mfma_f32_16x16x32_bf16 v[214:217], a[240:243], v[194:197], 0\n"
      "ds_read_b128 v[166:169], v2 offset:27904\n"
      "v_add_u32 v102, s92, v164\n"
      "v_mfma_f32_16x16x32_bf16 v[214:217], a[244:247], v[156:159], v[214:217]\n"
      "v_rcp_f32 v160, v160\n"
      "v_mfma_f32_16x16x32_bf16 v[148:151], v[232:235], v[194:197], 0\n"
      "v_subrev_u32 v113, 115456, v9\n"
      "v_and_b32 v136, 0x300, v113\n"
      "v_mfma_f32_16x16x32_bf16 v[148:151], v[236:239], v[156:159], v[148:151]\n"
      "v_add_u32 v113, v113, v136\n"
      "v_mfma_f32_16x16x32_bf16 v[174:177], a[224:227], v[182:185], v[174:177]\n"
      "ds_read_b128 v[134:137], v2 offset:25088\n"
      "v_mfma_f32_16x16x32_bf16 v[174:177], a[228:231], v[130:133], v[174:177]\n"
      "ds_read_b128 v[156:159], v7 offset:26624\n"
      "v_lshrrev_b32_e64 v164, 4, v102\n"
      "v_mfma_f32_16x16x32_bf16 v[214:217], a[248:251], v[182:185], v[214:217]\n"
      "ds_read_b128 v[186:189], v2 offset:30976\n"
      "v_mfma_f32_16x16x32_bf16 v[214:217], a[252:255], v[130:133], v[214:217]\n"
      "ds_read_b128 v[114:117], v2 offset:21760\n"
      "v_add_u32_e64 v100, 64, v164\n"
      "v_mfma_f32_16x16x32_bf16 v[148:151], v[240:243], v[182:185], v[148:151]\n"
      "v_add_u32 v113, 150784, v113\n"
      "v_mfma_f32_16x16x32_bf16 v[148:151], v[244:247], v[130:133], v[148:151]\n"
      "ds_read_b128 v[180:183], v113 offset:4096\n"
      "v_cndmask_b32 v112, v112, v152, s[60:61]\n"
      "v_mfma_f32_16x16x32_bf16 v[174:177], a[232:235], v[202:205], v[174:177]\n"
      "ds_read_b128 v[194:197], v2 offset:24832\n"
      "v_mfma_f32_16x16x32_bf16 v[174:177], a[236:239], v[104:107], v[174:177]\n"
      "v_mul_u32_u24 v84, 2731, v100\n"
      "v_mfma_f32_16x16x32_bf16 v[214:217], v[224:227], v[202:205], v[214:217]\n"
      "v_add_u32 v85, 128, v164\n"
      "v_mfma_f32_16x16x32_bf16 v[214:217], v[228:231], v[104:107], v[214:217]\n"
      "ds_read_b128 v[130:133], v113 offset:2048\n"
      "v_mfma_f32_16x16x32_bf16 v[148:151], v[248:251], v[202:205], v[148:151]\n"
      "ds_read_b128 v[202:205], v113\n"
      "v_mfma_f32_16x16x32_bf16 v[148:151], v[252:255], v[104:107], v[148:151]\n"
      "ds_read_b128 v[96:99], v2 offset:34048\n"
      "v_mfma_f32_16x16x32_bf16 v[210:213], a[216:219], v[206:209], 0\n"
      "s_waitcnt lgkmcnt(5)\n"
      "v_fma_f32 v184, v174, v174, s52\n"
      "v_mfma_f32_16x16x32_bf16 v[210:213], a[220:223], v[114:117], v[210:213]\n"
      "ds_read_b128 v[170:173], v2 offset:34304\n"
      "v_mfma_f32_16x16x32_bf16 v[122:125], a[240:243], v[206:209], 0\n"
      "v_mul_f32 v185, v175, v175\n"
      "v_mul_f32 v112, v160, v112\n"
      "v_mfma_f32_16x16x32_bf16 v[122:125], a[244:247], v[114:117], v[122:125]\n"
      "v_fmac_f32 v184, v176, v176\n"
      "v_add_u32 v77, 1179648, v77\n"
      "v_mfma_f32_16x16x32_bf16 v[118:121], v[232:235], v[206:209], 0\n"
      "s_waitcnt lgkmcnt(2)\n"
      "v_fmac_f32 v185, v177, v177\n"
      "v_mfma_f32_16x16x32_bf16 v[118:121], v[236:239], v[114:117], v[118:121]\n"
      "ds_read_b128 v[206:209], v7 offset:34816\n"
      "v_add_u32_e64 v86, 0, v164\n"
      "v_mfma_f32_16x16x32_bf16 v[210:213], a[224:227], v[194:197], v[210:213]\n"
      "ds_read_b128 v[114:117], v7 offset:22528\n"
      "v_mul_u32_u24 v101, 2731, v86\n"
      "v_mfma_f32_16x16x32_bf16 v[210:213], a[228:231], v[166:169], v[210:213]\n"
      "v_sub_u32 v77, v77, v78\n"
      "v_lshrrev_b32 v84, 16, v84\n"
      "v_mfma_f32_16x16x32_bf16 v[122:125], a[248:251], v[194:197], v[122:125]\n"
      "buffer_store_dwordx4 v[220:223], v20, s[24:27], s46 offen sc1\n"
      "v_mfma_f32_16x16x32_bf16 v[122:125], a[252:255], v[166:169], v[122:125]\n"
      "ds_read_b128 v[160:163], v2 offset:22016\n"
      "v_fmac_f32 v184, v214, v214\n"
      "v_mfma_f32_16x16x32_bf16 v[118:121], v[240:243], v[194:197], v[118:121]\n"
      "ds_read_b128 v[152:155], v2 offset:28160\n"
      "v_fmac_f32 v185, v215, v215\n"
      "v_mfma_f32_16x16x32_bf16 v[118:121], v[244:247], v[166:169], v[118:121]\n"
      "ds_read_b128 v[104:107], v7 offset:30720\n"
      "v_fmac_f32 v184, v216, v216\n"
      "v_mfma_f32_16x16x32_bf16 v[210:213], a[232:235], v[186:189], v[210:213]\n"
      "s_waitcnt lgkmcnt(3)\n"
      "v_cvt_pk_bf16_f32 v112, v112, v112\n"
      "v_lshrrev_b32_e64 v101, 16, v101\n"
      "v_mfma_f32_16x16x32_bf16 v[210:213], a[236:239], v[96:99], v[210:213]\n"
      "ds_write_b16 v12, v112 offset:16\n"
      "v_mfma_f32_16x16x32_bf16 v[122:125], v[224:227], v[186:189], v[122:125]\n"
      "v_mul_u32_u24 v82, 24, v101\n"
      "v_mfma_f32_16x16x32_bf16 v[122:125], v[228:231], v[96:99], v[122:125]\n"
      "v_fmac_f32 v185, v217, v217\n"
      "v_fmac_f32_e64 v185, v149, v149\n"
      "v_mfma_f32_16x16x32_bf16 v[118:121], v[248:251], v[186:189], v[118:121]\n"
      "s_nop 0\n"
      "v_fma_f32 v178, v210, v210, s52\n"
      "v_mul_u32_u24 v81, 24, v84\n"
      "v_mfma_f32_16x16x32_bf16 v[118:121], v[252:255], v[96:99], v[118:121]\n"
      "s_waitcnt lgkmcnt(1)\n"
      "v_mul_u32_u24 v103, 2731, v85\n"
      "v_mul_f32 v179, v211, v211\n"
      "v_mfma_f32_16x16x32_bf16 v[220:223], a[216:219], v[126:129], 0\n"
      "ds_read_b128 v[96:99], v2 offset:31232\n"
      "v_mfma_f32_16x16x32_bf16 v[220:223], a[220:223], v[160:163], v[220:223]\n"
      "buffer_store_dwordx4 v[114:117], v21, s[24:27], s46 offen sc1\n"
      "v_mfma_f32_16x16x32_bf16 v[166:169], a[240:243], v[126:129], 0\n"
      "v_fmac_f32 v179, v213, v213\n"
      "v_fmac_f32 v184, v148, v148\n"
      "v_mfma_f32_16x16x32_bf16 v[166:169], a[244:247], v[160:163], v[166:169]\n"
      "v_fmac_f32 v178, v212, v212\n"
      "v_fmac_f32 v179, v123, v123\n"
      "v_mfma_f32_16x16x32_bf16 v[186:189], v[232:235], v[126:129], 0\n"
      "v_fmac_f32 v178, v122, v122\n"
      "v_fmac_f32 v185, v151, v151\n"
      "v_mfma_f32_16x16x32_bf16 v[186:189], v[236:239], v[160:163], v[186:189]\n"
      "buffer_store_dwordx4 v[156:159], v22, s[24:27], s46 offen sc1\n"
      "v_mfma_f32_16x16x32_bf16 v[220:223], a[224:227], v[134:137], v[220:223]\n"
      "ds_read_b128 v[126:129], v113 offset:10240\n"
      "v_bfe_i32 v78, v37, 30, 1\n"
      "v_mfma_f32_16x16x32_bf16 v[220:223], a[228:231], v[152:155], v[220:223]\n"
      "v_fmac_f32 v178, v124, v124\n"
      "v_fmac_f32 v179, v125, v125\n"
      "v_mfma_f32_16x16x32_bf16 v[166:169], a[248:251], v[134:137], v[166:169]\n"
      "ds_read_b128 v[114:117], v113 offset:6144\n"
      "v_fmac_f32_e64 v184, v150, v150\n"
      "v_mfma_f32_16x16x32_bf16 v[166:169], a[252:255], v[152:155], v[166:169]\n"
      "buffer_store_dwordx4 v[104:107], v23, s[24:27], s46 offen sc1\n"
      "v_mfma_f32_16x16x32_bf16 v[186:189], v[240:243], v[134:137], v[186:189]\n"
      "ds_read_b128 v[134:137], v113 offset:8192\n"
      "v_fmac_f32 v179, v119, v119\n"
      "v_mfma_f32_16x16x32_bf16 v[186:189], v[244:247], v[152:155], v[186:189]\n"
      "s_waitcnt lgkmcnt(2)\n"
      "v_lshrrev_b32 v103, 16, v103\n"
      "v_fmac_f32 v178, v118, v118\n"
      "v_mfma_f32_16x16x32_bf16 v[220:223], a[232:235], v[96:99], v[220:223]\n"
      "v_add_f32 v184, v184, v185\n"
      "v_mfma_f32_16x16x32_bf16 v[220:223], a[236:239], v[170:173], v[220:223]\n"
      "v_fmac_f32 v179, v121, v121\n"
      "v_fmac_f32 v178, v120, v120\n"
      "v_mfma_f32_16x16x32_bf16 v[166:169], v[224:227], v[96:99], v[166:169]\n"
      "v_sub_u32 v82, v86, v82\n"
      "v_add_f32 v178, v178, v179\n"
      "v_mfma_f32_16x16x32_bf16 v[166:169], v[228:231], v[170:173], v[166:169]\n"
      "s_nop 0\n"
      "v_permlane16_swap_b32 v184, v178\n"
      "v_mfma_f32_16x16x32_bf16 v[186:189], v[248:251], v[96:99], v[186:189]\n"
      "v_mul_f32 v155, v221, v221\n"
      "v_fma_f32 v154, v220, v220, s52\n"
      "v_mfma_f32_16x16x32_bf16 v[186:189], v[252:255], v[170:173], v[186:189]\n"
      "v_fmac_f32 v154, v222, v222\n"
      "v_fmac_f32 v155, v223, v223\n"
      "v_mfma_f32_16x16x32_bf16 v[104:107], a[216:219], v[202:205], 0\n"
      "buffer_store_dwordx4 v[206:209], v25, s[24:27], s46 offen sc1\n"
      "v_pk_fma_f32 v[154:155], v[166:167], v[166:167], v[154:155]\n"
      "v_pk_fma_f32 v[154:155], v[168:169], v[168:169], v[154:155]\n"
      "v_mfma_f32_16x16x32_bf16 v[104:107], a[220:223], v[130:133], v[104:107]\n"
      "s_nop 0\n"
      "v_fmac_f32 v155, v187, v187\n"
      "v_readlane_b32 s46, v37, s42\n"
      "v_mfma_f32_16x16x32_bf16 v[194:197], a[240:243], v[202:205], 0\n"
      "v_fmac_f32 v154, v186, v186\n"
      "v_fmac_f32 v154, v188, v188\n"
      "v_mfma_f32_16x16x32_bf16 v[194:197], a[244:247], v[130:133], v[194:197]\n"
      "v_fmac_f32 v155, v189, v189\n"
      "v_add_f32 v184, v184, v178\n"
      "v_mfma_f32_16x16x32_bf16 v[170:173], v[232:235], v[202:205], 0\n"
      "s_waitcnt lgkmcnt(0)\n"
      "v_add_f32 v154, v154, v155\n"
      "v_mov_b32 v178, v154\n"
      "v_mfma_f32_16x16x32_bf16 v[170:173], v[236:239], v[130:133], v[170:173]\n"
      "s_nop 0\n"
      "v_permlane16_swap_b32 v154, v178\n"
      "v_mfma_f32_16x16x32_bf16 v[104:107], a[224:227], v[180:183], v[104:107]\n"
      "s_or_b32 s95, s46, s48\n"
      "v_add_f32 v154, v154, v178\n"
      "v_mfma_f32_16x16x32_bf16 v[104:107], a[228:231], v[114:117], v[104:107]\n"
      "s_nop 0\n"
      "v_permlane32_swap_b32 v184, v154\n"
      "v_add_f32 v184, v184, v154\n"
      "v_mfma_f32_16x16x32_bf16 v[194:197], a[248:251], v[180:183], v[194:197]\n"
      "ds_write_b32 v31, v184\n"
      "v_mfma_f32_16x16x32_bf16 v[194:197], a[252:255], v[114:117], v[194:197]\n"
      "v_sub_u32 v81, v100, v81\n"
      "v_mfma_f32_16x16x32_bf16 v[170:173], v[240:243], v[180:183], v[170:173]\n"
      "v_and_b32 v100, 7, v84\n"
      "v_lshl_add_u32 v84, v84, 2, 12\n"
      "v_mfma_f32_16x16x32_bf16 v[170:173], v[244:247], v[114:117], v[170:173]\n"
      "v_and_b32 v86, 7, v101\n"
      "v_mfma_f32_16x16x32_bf16 v[104:107], a[232:235], v[134:137], v[104:107]\n"
      "s_cmp_ge_u32 s95, 0x40000000\n"
      "s_cbranch_scc1 L_rare_35_%=\n"
      "L_back_36_%=:\n"
      "v_mfma_f32_16x16x32_bf16 v[104:107], a[236:239], v[126:129], v[104:107]\n"
      "v_mul_u32_u24 v156, 24, v103\n"
      "v_sub_u32_e64 v156, v85, v156\n"
      "v_mfma_f32_16x16x32_bf16 v[194:197], v[224:227], v[134:137], v[194:197]\n"
      "s_waitcnt lgkmcnt(0)\n"
      "s_barrier\n"
      "ds_read_b128 v[130:133], v9\n"
      "ds_read_b128 v[114:117], v7\n"
      "v_mfma_f32_16x16x32_bf16 v[194:197], v[228:231], v[126:129], v[194:197]\n"
      "ds_read_b128 v[204:207], v113 offset:256\n"
      "ds_read_b128 v[178:181], v113 offset:2304\n"
      "ds_read_b128 v[152:155], v113 offset:8448\n"
      "v_fma_f32 v208, v104, v104, s52\n"
      "v_mul_f32_e64 v209, v105, v105\n"
      "v_pk_fma_f32 v[208:209], v[106:107], v[106:107], v[208:209]\n"
      "v_mfma_f32_16x16x32_bf16 v[170:173], v[248:251], v[134:137], v[170:173]\n"
      "ds_read_b128 v[182:185], v113 offset:6400\n"
      "ds_read_b128 v[158:161], v7 offset:4096\n"
      "ds_read_b128 v[96:99], v7 offset:8192\n"
      "ds_read_b128 v[134:137], v113 offset:4352\n"
      "v_pk_fma_f32 v[208:209], v[194:195], v[194:195], v[208:209]\n"
      "v_mfma_f32_16x16x32_bf16 v[170:173], v[252:255], v[126:129], v[170:173]\n"
      "s_waitcnt lgkmcnt(8)\n"
      "v_add_f32 v132, v132, v133\n"
      "v_add_f32 v130, v130, v131\n"
      "v_add_f32 v130, v130, v132\n"
      "v_rsq_f32 v130, v130\n"
      "v_pk_fma_f32 v[208:209], v[196:197], v[196:197], v[208:209]\n"
      "v_mul_f32 v130, 0x415db3d7, v130\n"
      "v_mov_b32 v132, v130\n"
      "v_pk_fma_f32 v[208:209], v[170:171], v[170:171], v[208:209]\n"
      "s_waitcnt lgkmcnt(6)\n"
      "v_mfma_f32_16x16x32_bf16 v[200:203], a[216:219], v[204:207], 0\n"
      "s_waitcnt lgkmcnt(5)\n"
      "v_permlane32_swap_b32_e64 v130, v132\n"
      "v_mfma_f32_16x16x32_bf16 v[200:203], a[220:223], v[178:181], v[200:203]\n"
      "ds_read_b128 v[126:129], v113 offset:10496\n"
      "v_mov_b32_e64 v131, v130\n"
      "v_mfma_f32_16x16x32_bf16 v[162:165], a[240:243], v[204:207], 0\n"
      "buffer_store_dwordx4 v[114:117], v20, s[24:27], s46 offen sc1\n"
      "v_pk_mul_f32 v[166:167], v[166:167], v[132:133] op_sel_hi:[1,0]\n"
      "v_pk_fma_f32 v[208:209], v[172:173], v[172:173], v[208:209]\n"
      "v_pk_mul_f32 v[220:221], v[220:221], v[132:133] op_sel_hi:[1,0]\n"
      "v_pk_mul_f32 v[222:223], v[222:223], v[132:133] op_sel_hi:[1,0]\n"
      "v_mfma_f32_16x16x32_bf16 v[162:165], a[244:247], v[178:181], v[162:165]\n"
      "s_waitcnt lgkmcnt(1)\n"
      "v_permlane16_swap_b32 v130, v131\n"
      "v_mfma_f32_16x16x32_bf16 v[112:115], v[232:235], v[204:207], 0\n"
      "ds_read_b128 v[204:207], v7 offset:12288\n"
      "v_cvt_pk_bf16_f32 v220, v220, v221\n"
      "v_mfma_f32_16x16x32_bf16 v[112:115], v[236:239], v[178:181], v[112:115]\n"
      "ds_read_b128 v[178:181], v7 offset:16384\n"
      "v_cvt_pk_bf16_f32 v221, v222, v223\n"
      "v_mfma_f32_16x16x32_bf16 v[200:203], a[224:227], v[134:137], v[200:203]\n"
      "buffer_store_dwordx4 v[158:161], v21, s[24:27], s46 offen sc1\n"
      "v_pk_mul_f32 v[216:217], v[216:217], v[130:131] op_sel_hi:[1,0]\n"
      "v_pk_mul_f32 v[122:123], v[122:123], v[130:131] op_sel:[0,1]\n"
      "v_pk_mul_f32 v[168:169], v[168:169], v[132:133] op_sel_hi:[1,0]\n"
      "v_pk_mul_f32 v[150:151], v[150:151], v[130:131] op_sel_hi:[1,0]\n"
      "v_pk_mul_f32 v[120:121], v[120:121], v[130:131] op_sel:[0,1]\n"
      "v_pk_mul_f32 v[210:211], v[210:211], v[130:131] op_sel:[0,1]\n"
      "v_pk_mul_f32 v[176:177], v[176:177], v[130:131] op_sel_hi:[1,0]\n"
      "v_mfma_f32_16x16x32_bf16 v[200:203], a[228:231], v[182:185], v[200:203]\n"
      "v_cvt_pk_bf16_f32 v222, v166, v167\n"
      "v_cvt_pk_bf16_f32 v223, v168, v169\n"
      "v_mfma_f32_16x16x32_bf16 v[162:165], a[248:251], v[134:137], v[162:165]\n"
      "buffer_store_dwordx4 v[96:99], v22, s[24:27], s46 offen sc1\n"
      "v_pk_mul_f32 v[148:149], v[148:149], v[130:131] op_sel_hi:[1,0]\n"
      "v_pk_mul_f32 v[174:175], v[174:175], v[130:131] op_sel_hi:[1,0]\n"
      "v_pk_mul_f32 v[212:213], v[212:213], v[130:131] op_sel:[0,1]\n"
      "v_pk_mul_f32 v[118:119], v[118:119], v[130:131] op_sel:[0,1]\n"
      "v_pk_mul_f32 v[214:215], v[214:215], v[130:131] op_sel_hi:[1,0]\n"
      "v_pk_mul_f32 v[124:125], v[124:125], v[130:131] op_sel:[0,1]\n"
      "v_mfma_f32_16x16x32_bf16 v[162:165], a[252:255], v[182:185], v[162:165]\n"
      "v_cvt_pk_bf16_f32 v210, v210, v211\n"
      "v_cvt_pk_bf16_f32 v174, v174, v175\n"
      "v_cvt_pk_bf16_f32 v118, v118, v119\n"
      "v_mfma_f32_16x16x32_bf16 v[112:115], v[240:243], v[134:137], v[112:115]\n"
      "v_cvt_pk_bf16_f32 v211, v212, v213\n"
      "v_cvt_pk_bf16_f32 v175, v176, v177\n"
      "v_cvt_pk_bf16_f32 v119, v120, v121\n"
      "v_mfma_f32_16x16x32_bf16 v[112:115], v[244:247], v[182:185], v[112:115]\n"
      "ds_write_b64 v6, v[118:119] offset:24576\n"
      "v_mfma_f32_16x16x32_bf16 v[200:203], a[232:235], v[152:155], v[200:203]\n"
      "s_waitcnt lgkmcnt(1)\n"
      "v_cvt_pk_bf16_f32 v148, v148, v149\n"
      "v_cvt_pk_bf16_f32 v212, v122, v123\n"
      "v_mfma_f32_16x16x32_bf16 v[200:203], a[236:239], v[126:129], v[200:203]\n"
      "v_cvt_pk_bf16_f32 v213, v124, v125\n"
      "v_cvt_pk_bf16_f32 v177, v216, v217\n"
      "v_cvt_pk_bf16_f32 v149, v150, v151\n"
      "v_mfma_f32_16x16x32_bf16 v[162:165], v[224:227], v[152:155], v[162:165]\n"
      "ds_write_b128 v5, v[210:213] offset:24576\n"
      "v_cvt_pk_bf16_f32 v176, v214, v215\n"
      "v_mfma_f32_16x16x32_bf16 v[162:165], v[228:231], v[126:129], v[162:165]\n"
      "ds_write_b64 v6, v[148:149] offset:18432\n"
      "v_mfma_f32_16x16x32_bf16 v[112:115], v[248:251], v[152:155], v[112:115]\n"
      "ds_write_b128 v5, v[220:223] offset:30720\n"
      "v_mfma_f32_16x16x32_bf16 v[112:115], v[252:255], v[126:129], v[112:115]\n"
      "ds_write_b128 v5, v[174:177] offset:18432\n"
      "v_mul_f32 v177, v201, v201\n"
      "v_fma_f32 v176, v200, v200, s52\n"
      "v_pk_mul_f32 v[186:187], v[186:187], v[132:133] op_sel_hi:[1,0]\n"
      "v_add_f32_e64 v208, v208, v209\n"
      "v_pk_fma_f32 v[176:177], v[202:203], v[202:203], v[176:177]\n"
      "v_pk_fma_f32 v[176:177], v[162:163], v[162:163], v[176:177]\n"
      "v_pk_fma_f32 v[176:177], v[164:165], v[164:165], v[176:177]\n"
      "v_pk_fma_f32 v[176:177], v[112:113], v[112:113], v[176:177]\n"
      "v_pk_fma_f32 v[176:177], v[114:115], v[114:115], v[176:177]\n"
      "v_add_f32 v176, v176, v177\n"
      "v_cvt_pk_bf16_f32 v186, v186, v187\n"
      "v_pk_mul_f32 v[188:189], v[188:189], v[132:133] op_sel_hi:[1,0]\n"
      "v_permlane16_swap_b32 v208, v176\n"
      "v_cvt_pk_bf16_f32 v187, v188, v189\n"
      "v_add_f32 v208, v208, v176\n"
      "v_mov_b32 v176, v208\n"
      "s_add_u32 s42, s42, 1\n"
      "ds_write_b64 v6, v[186:187] offset:30720\n"
      "v_permlane32_swap_b32 v208, v176\n"
      "v_add_f32_e64 v208, v208, v176\n"
      "ds_write_b32 v31, v208 offset:4608\n"
      "buffer_store_dwordx4 v[204:207], v23, s[24:27], s46 offen sc1\n"
      "v_and_b32 v78, 196224, v78\n"
      "v_subrev_u32 v132, 73728, v5\n"
      "v_sub_u32_e64 v77, v77, v78\n"
      "ds_bpermute_b32 v188, v84, v77\n"
      "buffer_store_dwordx4 v[178:181], v25, s[24:27], s46 offen sc1\n"
      "s_waitcnt lgkmcnt(1)\n"
      "s_barrier\n"
      "ds_read_b128 v[176:179], v9 offset:4608\n"
      "v_subrev_u32 v133, 73728, v6\n"
      "ds_read_b128 v[122:125], v7 offset:22528\n"
      "ds_read_b128 v[128:131], v7 offset:18432\n"
      "ds_read_b128 v[210:213], v7 offset:30720\n"
      "v_readlane_b32 s46, v37, s42\n"
      "ds_read_b128 v[214:217], v7 offset:26624\n"
      "v_lshl_add_u32 v101, v101, 2, 12\n"
      "v_add_u32 v12, 32, v12\n"
      "s_or_b32 s95, s46, s48\n"
      "s_cmp_ge_u32 s95, 0x40000000\n"
      "s_cbranch_scc1 L_rare_39_%=\n"
      "L_back_40_%=:\n"
      "v_xor_b32 v82, v82, v86\n"
      "s_waitcnt lgkmcnt(4)\n"
      "v_add_f32 v178, v178, v179\n"
      "v_add_f32 v176, v176, v177\n"
      "v_add_f32 v176, v176, v178\n"
      "v_rsq_f32 v176, v176\n"
      "v_and_b32 v85, 7, v103\n"
      "v_mul_f32 v176, 0x415db3d7, v176\n"
      "v_mov_b32 v178, v176\n"
      "v_lshl_add_u32 v103, v103, 2, 12\n"
      "s_nop 0\n"
      "v_permlane32_swap_b32 v176, v178\n"
      "v_mov_b32 v177, v176\n"
      "s_waitcnt lgkmcnt(1)\n"
      "buffer_store_dwordx4 v[128:131], v20, s[24:27], s46 offen sc1\n"
      "v_permlane16_swap_b32_e64 v176, v177\n"
      "v_pk_mul_f32 v[106:107], v[106:107], v[176:177] op_sel_hi:[1,0]\n"
      "v_pk_mul_f32 v[194:195], v[194:195], v[176:177] op_sel_hi:[1,0]\n"
      "v_pk_mul_f32 v[104:105], v[104:105], v[176:177] op_sel_hi:[1,0]\n"
      "v_pk_mul_f32 v[200:201], v[200:201], v[176:177] op_sel:[0,1]\n"
      "v_pk_mul_f32 v[202:203], v[202:203], v[176:177] op_sel:[0,1]\n"
      "v_pk_mul_f32 v[162:163], v[162:163], v[176:177] op_sel:[0,1]\n"
      "v_pk_mul_f32 v[112:113], v[112:113], v[176:177] op_sel:[0,1]\n"
      "v_cvt_pk_bf16_f32 v200, v200, v201\n"
      "v_pk_mul_f32 v[164:165], v[164:165], v[176:177] op_sel:[0,1]\n"
      "v_pk_mul_f32 v[170:171], v[170:171], v[176:177] op_sel_hi:[1,0]\n"
      "v_cvt_pk_bf16_f32 v201, v202, v203\n"
      "v_cvt_pk_bf16_f32 v112, v112, v113\n"
      "v_cvt_pk_bf16_f32 v202, v162, v163\n"
      "v_cvt_pk_bf16_f32 v104, v104, v105\n"
      "v_pk_mul_f32 v[114:115], v[114:115], v[176:177] op_sel:[0,1]\n"
      "v_pk_mul_f32 v[196:197], v[196:197], v[176:177] op_sel_hi:[1,0]\n"
      "v_cvt_pk_bf16_f32 v203, v164, v165\n"
      "s_add_u32 s42, s42, 1\n"
      "ds_write_b128 v132, v[200:203] offset:6144\n"
      "v_cvt_pk_bf16_f32 v105, v106, v107\n"
      "v_cvt_pk_bf16_f32 v107, v196, v197\n"
      "v_cvt_pk_bf16_f32 v170, v170, v171\n"
      "v_cvt_pk_bf16_f32 v106, v194, v195\n"
      "v_cvt_pk_bf16_f32 v113, v114, v115\n"
      "ds_write_b128 v132, v[104:107]\n"
      "ds_write_b64 v133, v[112:113] offset:6144\n"
      "v_pk_mul_f32 v[172:173], v[172:173], v[176:177] op_sel_hi:[1,0]\n"
      "v_cvt_pk_bf16_f32 v171, v172, v173\n"
      "ds_write_b64 v133, v[170:171]\n"
      "buffer_store_dwordx4 v[122:125], v21, s[24:27], s46 offen sc1\n"
      "ds_bpermute_b32 v179, v103, v77\n"
      "ds_read_b128 v[130:133], v7 offset:34816\n"
      "ds_bpermute_b32 v171, v101, v77\n"
      "s_waitcnt lgkmcnt(5)\n"
      "buffer_store_dwordx4 v[214:217], v22, s[24:27], s46 offen sc1\n"
      "v_xor_b32_e64 v81, v81, v100\n"
      "v_xor_b32_e64 v156, v156, v85\n"
      "v_lshl_add_u32 v188, v81, 4, v188\n"
      "s_waitcnt lgkmcnt(3)\n"
      "s_barrier\n"
      "ds_read_b128 v[194:197], v102\n"
      "buffer_store_dwordx4 v[210:213], v23, s[24:27], s46 offen sc1\n"
      "s_nop 1\n"
      "ds_read_b128 v[212:215], v102 offset:1024\n"
      "s_waitcnt lgkmcnt(2)\n"
      "buffer_store_dwordx4 v[130:133], v25, s[24:27], s46 offen sc1\n"
      "ds_read_b128 v[164:167], v102 offset:2048\n"
      "v_lshl_add_u32 v179, v156, 4, v179\n"
      "v_lshl_add_u32 v171, v82, 4, v171\n"
      "s_waitcnt lgkmcnt(2)\n"
      "buffer_store_dwordx4 v[194:197], v171, s[24:27], 0 offen sc1\n"
      "s_waitcnt lgkmcnt(1)\n"
      "buffer_store_dwordx4 v[212:215], v188, s[24:27], 0 offen sc1\n"
      "s_waitcnt lgkmcnt(0)\n"
      "buffer_store_dwordx4 v[164:167], v179, s[24:27], 0 offen sc1\n"
      "s_endpgm\n"
      "L_rare_1_%=:\n"
      "s_nop 7\n"
      "s_waitcnt lgkmcnt(0)\n"
      "s_add_u32 s72, s42, 1\n"
      "v_readlane_b32 s77, v38, s72\n"
      "v_subrev_u32 v165, 115456, v9\n"
      "s_and_b32 s73, s77, -16\n"
      "s_mov_b32 m0, 110592\n"
      "s_nop 0\n"
      "buffer_load_dwordx4 v165, s[28:31], s73 offen  lds\n"
      "s_add_u32 s73, s73, 1024\n"
      "s_mov_b32 m0, 111616\n"
      "s_nop 0\n"
      "buffer_load_dwordx4 v165, s[28:31], s73 offen  lds\n"
      "s_add_u32 s73, s73, 1024\n"
      "s_mov_b32 m0, 112640\n"
      "s_nop 0\n"
      "buffer_load_dwordx4 v165, s[28:31], s73 offen  lds\n"
      "s_add_u32 s73, s73, 1024\n"
      "s_mov_b32 m0, 113664\n"
      "s_nop 0\n"
      "buffer_load_dwordx4 v165, s[28:31], s73 offen  lds\n"
      "s_add_u32 s73, s73, 1024\n"
      "s_mov_b32 m0, 114688\n"
      "s_mov_b32 exec_hi, 0xffff\n"
      "buffer_load_dwordx4 v165, s[28:31], s73 offen  lds\n"
      "s_mov_b32 exec_hi, -1\n"
      "s_add_u32 s73, s73, 1024\n"
      "s_and_b32 s77, s77, 15\n"
      "s_waitcnt lgkmcnt(0)\n"
      "s_nop 4\n"
      "s_branch L_back_2_%=\n"
      "L_rare_3_%=:\n"
      "s_nop 7\n"
      "s_waitcnt lgkmcnt(0)\n"
      "s_waitcnt vmcnt(0)\n"
      "v_subrev_u32 v138, 115456, v9\n"
      "v_bfe_u32 v163, v138, 4, 4\n"
      "v_mul_u32_u24 v163, 98, v163\n"
      "v_lshrrev_b32 v138, 8, v138\n"
      "v_lshl_add_u32 v163, v138, 3, v163\n"
      "v_add_u32 v163, s77, v163\n"
      "v_and_b32 v139, 2, v163\n"
      "v_and_b32 v163, -4, v163\n"
      "v_add_u32 v163, 110592, v163\n"
      "ds_read2_b32 v[198:199], v163 offset0:0 offset1:1\n"
      "ds_read2_b32 v[200:201], v163 offset0:8 offset1:9\n"
      "ds_read2_b32 v[122:123], v163 offset0:16 offset1:17\n"
      "ds_read2_b32 v[164:165], v163 offset0:2 offset1:10\n"
      "ds_read2_b32 v[146:147], v163 offset0:18 offset1:24\n"
      "s_waitcnt lgkmcnt(4)\n"
      "v_alignbyte_b32 v198, v199, v198, v139\n"
      "s_waitcnt lgkmcnt(1)\n"
      "v_alignbyte_b32 v199, v164, v199, v139\n"
      "v_lshlrev_b32 v138, 16, v198\n"
      "v_fmac_f32 v158, s53, v138\n"
      "v_and_b32 v138, 0xffff0000, v198\n"
      "v_fmac_f32 v159, s53, v138\n"
      "v_lshlrev_b32 v138, 16, v199\n"
      "v_fmac_f32_e64 v160, s53, v138\n"
      "v_and_b32 v138, 0xffff0000, v199\n"
      "v_fmac_f32 v161, s53, v138\n"
      "v_alignbyte_b32 v200, v201, v200, v139\n"
      "v_alignbyte_b32 v201, v165, v201, v139\n"
      "v_lshlrev_b32 v138, 16, v200\n"
      "v_fmac_f32 v126, s53, v138\n"
      "v_and_b32 v138, 0xffff0000, v200\n"
      "v_fmac_f32 v127, s53, v138\n"
      "v_lshlrev_b32 v138, 16, v201\n"
      "v_fmac_f32 v128, s53, v138\n"
      "v_and_b32 v138, 0xffff0000, v201\n"
      "v_fmac_f32 v129, s53, v138\n"
      "v_alignbyte_b32 v122, v123, v122, v139\n"
      "s_waitcnt lgkmcnt(0)\n"
      "v_alignbyte_b32 v123, v146, v123, v139\n"
      "v_lshlrev_b32 v138, 16, v122\n"
      "v_fmac_f32 v176, s53, v138\n"
      "v_and_b32 v138, 0xffff0000, v122\n"
      "v_fmac_f32 v177, s53, v138\n"
      "v_lshlrev_b32 v138, 16, v123\n"
      "v_fmac_f32 v178, s53, v138\n"
      "v_and_b32 v138, 0xffff0000, v123\n"
      "v_fmac_f32 v179, s53, v138\n"
      "v_alignbyte_b32 v138, v147, v147, v139\n"
      "v_lshlrev_b32 v138, 16, v138\n"
      "v_fmac_f32 v172, s53, v138\n"
      "v_max3_f32 v163, v158, v159, v160\n"
      "v_max3_f32 v139, v161, v126, v127\n"
      "v_max3_f32 v163, v163, v128, v129\n"
      "v_max3_f32 v139, v139, v176, v177\n"
      "v_max3_f32 v163, v163, v178, v179\n"
      "v_max3_f32 v163, v163, v139, v172\n"
      "v_mov_b32 v138, v163\n"
      "s_nop 1\n"
      "v_permlane16_swap_b32 v163, v138\n"
      "v_max_f32 v163, v163, v138\n"
      "v_mov_b32 v138, v163\n"
      "s_nop 1\n"
      "v_permlane32_swap_b32 v163, v138\n"
      "v_max_f32 v163, v163, v138\n"
      "v_sub_f32 v158, v158, v163\n"
      "v_sub_f32 v159, v159, v163\n"
      "v_sub_f32 v160, v160, v163\n"
      "v_sub_f32 v161, v161, v163\n"
      "v_sub_f32 v126, v126, v163\n"
      "v_sub_f32 v127, v127, v163\n"
      "v_sub_f32 v128, v128, v163\n"
      "v_sub_f32 v129, v129, v163\n"
      "v_sub_f32 v176, v176, v163\n"
      "v_sub_f32 v177, v177, v163\n"
      "v_sub_f32 v178, v178, v163\n"
      "v_sub_f32 v179, v179, v163\n"
      "v_sub_f32_e64 v172, v172, v163\n"
      "s_waitcnt lgkmcnt(0)\n"
      "s_nop 4\n"
      "s_branch L_back_4_%=\n"
      "L_rare_5_%=:\n"
      "s_nop 7\n"
      "s_waitcnt lgkmcnt(0)\n"
      "s_waitcnt vmcnt(0)\n"
      "v_subrev_u32 v82, 115456, v9\n"
      "v_bfe_u32 v163, v82, 4, 4\n"
      "v_mul_u32_u24 v163, 98, v163\n"
      "v_lshrrev_b32 v82, 8, v82\n"
      "v_lshl_add_u32 v163, v82, 3, v163\n"
      "v_add_u32 v163, s77, v163\n"
      "v_and_b32 v165, 2, v163\n"
      "v_and_b32 v163, -4, v163\n"
      "v_add_u32 v163, 112160, v163\n"
      "ds_read2_b32 v[144:145], v163 offset0:0 offset1:1\n"
      "ds_read2_b32 v[146:147], v163 offset0:8 offset1:9\n"
      "ds_read2_b32 v[148:149], v163 offset0:16 offset1:17\n"
      "ds_read2_b32 v[168:169], v163 offset0:2 offset1:10\n"
      "ds_read2_b32 v[174:175], v163 offset0:18 offset1:24\n"
      "s_waitcnt lgkmcnt(4)\n"
      "v_alignbyte_b32 v144, v145, v144, v165\n"
      "s_waitcnt lgkmcnt(1)\n"
      "v_alignbyte_b32 v145, v168, v145, v165\n"
      "v_lshlrev_b32 v82, 16, v144\n"
      "v_fmac_f32 v140, s53, v82\n"
      "v_and_b32 v82, 0xffff0000, v144\n"
      "v_fmac_f32 v141, s53, v82\n"
      "v_lshlrev_b32 v82, 16, v145\n"
      "v_fmac_f32 v142, s53, v82\n"
      "v_and_b32 v82, 0xffff0000, v145\n"
      "v_fmac_f32 v143, s53, v82\n"
      "v_alignbyte_b32 v146, v147, v146, v165\n"
      "v_alignbyte_b32 v147, v169, v147, v165\n"
      "v_lshlrev_b32 v82, 16, v146\n"
      "v_fmac_f32 v184, s53, v82\n"
      "v_and_b32 v82, 0xffff0000, v146\n"
      "v_fmac_f32 v185, s53, v82\n"
      "v_lshlrev_b32 v82, 16, v147\n"
      "v_fmac_f32_e64 v186, s53, v82\n"
      "v_and_b32 v82, 0xffff0000, v147\n"
      "v_fmac_f32 v187, s53, v82\n"
      "v_alignbyte_b32 v148, v149, v148, v165\n"
      "s_waitcnt lgkmcnt(0)\n"
      "v_alignbyte_b32 v149, v174, v149, v165\n"
      "v_lshlrev_b32 v82, 16, v148\n"
      "v_fmac_f32 v198, s53, v82\n"
      "v_and_b32 v82, 0xffff0000, v148\n"
      "v_fmac_f32 v199, s53, v82\n"
      "v_lshlrev_b32 v82, 16, v149\n"
      "v_fmac_f32_e64 v200, s53, v82\n"
      "v_and_b32 v82, 0xffff0000, v149\n"
      "v_fmac_f32 v201, s53, v82\n"
      "v_alignbyte_b32 v82, v175, v175, v165\n"
      "v_lshlrev_b32 v82, 16, v82\n"
      "v_fmac_f32_e64 v208, s53, v82\n"
      "v_max3_f32 v163, v140, v141, v142\n"
      "v_max3_f32 v165, v143, v184, v185\n"
      "v_max3_f32 v163, v163, v186, v187\n"
      "v_max3_f32 v165, v165, v198, v199\n"
      "v_max3_f32 v163, v163, v200, v201\n"
      "v_max3_f32 v163, v163, v165, v208\n"
      "v_mov_b32 v82, v163\n"
      "s_nop 1\n"
      "v_permlane16_swap_b32 v163, v82\n"
      "v_max_f32 v163, v163, v82\n"
      "v_mov_b32 v82, v163\n"
      "s_nop 1\n"
      "v_permlane32_swap_b32 v163, v82\n"
      "v_max_f32 v163, v163, v82\n"
      "v_sub_f32 v140, v140, v163\n"
      "v_sub_f32 v141, v141, v163\n"
      "v_sub_f32 v142, v142, v163\n"
      "v_sub_f32 v143, v143, v163\n"
      "v_sub_f32 v184, v184, v163\n"
      "v_sub_f32 v185, v185, v163\n"
      "v_sub_f32 v186, v186, v163\n"
      "v_sub_f32 v187, v187, v163\n"
      "v_sub_f32 v198, v198, v163\n"
      "v_sub_f32 v199, v199, v163\n"
      "v_sub_f32 v200, v200, v163\n"
      "v_sub_f32 v201, v201, v163\n"
      "v_sub_f32_e64 v208, v208, v163\n"
      "s_waitcnt lgkmcnt(0)\n"
      "s_nop 4\n"
      "s_branch L_back_6_%=\n"
      "L_rare_7_%=:\n"
      "s_nop 7\n"
      "s_waitcnt lgkmcnt(0)\n"
      "s_waitcnt vmcnt(0)\n"
      "v_subrev_u32 v179, 115456, v9\n"
      "v_bfe_u32 v172, v179, 4, 4\n"
      "v_mul_u32_u24 v172, 98, v172\n"
      "v_lshrrev_b32_e64 v179, 8, v179\n"
      "v_lshl_add_u32 v172, v179, 3, v172\n"
      "v_add_u32 v172, s77, v172\n"
      "v_and_b32 v178, 2, v172\n"
      "v_and_b32_e64 v172, -4, v172\n"
      "v_add_u32 v172, 113728, v172\n"
      "ds_read2_b32 v[170:171], v172 offset0:0 offset1:1\n"
      "ds_read2_b32 v[134:135], v172 offset0:8 offset1:9\n"
      "ds_read2_b32 v[136:137], v172 offset0:16 offset1:17\n"
      "ds_read2_b32 v[102:103], v172 offset0:2 offset1:10\n"
      "ds_read2_b32 v[100:101], v172 offset0:18 offset1:24\n"
      "s_waitcnt lgkmcnt(4)\n"
      "v_alignbyte_b32 v170, v171, v170, v178\n"
      "s_waitcnt lgkmcnt(1)\n"
      "v_alignbyte_b32 v171, v102, v171, v178\n"
      "v_lshlrev_b32 v179, 16, v170\n"
      "v_fmac_f32 v144, s53, v179\n"
      "v_and_b32 v179, 0xffff0000, v170\n"
      "v_fmac_f32 v145, s53, v179\n"
      "v_lshlrev_b32 v179, 16, v171\n"
      "v_fmac_f32_e64 v146, s53, v179\n"
      "v_and_b32 v179, 0xffff0000, v171\n"
      "v_fmac_f32 v147, s53, v179\n"
      "v_alignbyte_b32 v134, v135, v134, v178\n"
      "v_alignbyte_b32 v135, v103, v135, v178\n"
      "v_lshlrev_b32 v179, 16, v134\n"
      "v_fmac_f32 v130, s53, v179\n"
      "v_and_b32 v179, 0xffff0000, v134\n"
      "v_fmac_f32 v131, s53, v179\n"
      "v_lshlrev_b32 v179, 16, v135\n"
      "v_fmac_f32 v132, s53, v179\n"
      "v_and_b32 v179, 0xffff0000, v135\n"
      "v_fmac_f32 v133, s53, v179\n"
      "v_alignbyte_b32 v136, v137, v136, v178\n"
      "s_waitcnt lgkmcnt(0)\n"
      "v_alignbyte_b32 v137, v100, v137, v178\n"
      "v_lshlrev_b32 v179, 16, v136\n"
      "v_fmac_f32 v126, s53, v179\n"
      "v_and_b32 v179, 0xffff0000, v136\n"
      "v_fmac_f32 v127, s53, v179\n"
      "v_lshlrev_b32 v179, 16, v137\n"
      "v_fmac_f32 v128, s53, v179\n"
      "v_and_b32 v179, 0xffff0000, v137\n"
      "v_fmac_f32 v129, s53, v179\n"
      "v_alignbyte_b32 v179, v101, v101, v178\n"
      "v_lshlrev_b32 v179, 16, v179\n"
      "v_fmac_f32 v180, s53, v179\n"
      "v_max3_f32 v172, v144, v145, v146\n"
      "v_max3_f32 v178, v147, v130, v131\n"
      "v_max3_f32 v172, v172, v132, v133\n"
      "v_max3_f32 v178, v178, v126, v127\n"
      "v_max3_f32 v172, v172, v128, v129\n"
      "v_max3_f32 v172, v172, v178, v180\n"
      "v_mov_b32 v179, v172\n"
      "s_nop 1\n"
      "v_permlane16_swap_b32 v172, v179\n"
      "v_max_f32 v172, v172, v179\n"
      "v_mov_b32 v179, v172\n"
      "s_nop 1\n"
      "v_permlane32_swap_b32 v172, v179\n"
      "v_max_f32 v172, v172, v179\n"
      "v_sub_f32 v144, v144, v172\n"
      "v_sub_f32 v145, v145, v172\n"
      "v_sub_f32 v146, v146, v172\n"
      "v_sub_f32 v147, v147, v172\n"
      "v_sub_f32 v130, v130, v172\n"
      "v_sub_f32 v131, v131, v172\n"
      "v_sub_f32 v132, v132, v172\n"
      "v_sub_f32 v133, v133, v172\n"
      "v_sub_f32 v126, v126, v172\n"
      "v_sub_f32 v127, v127, v172\n"
      "v_sub_f32 v128, v128, v172\n"
      "v_sub_f32 v129, v129, v172\n"
      "v_sub_f32 v180, v180, v172\n"
      "s_waitcnt lgkmcnt(0)\n"
      "s_nop 4\n"
      "s_branch L_back_8_%=\n"
      "L_rare_9_%=:\n"
      "s_nop 7\n"
      "s_waitcnt lgkmcnt(0)\n"
      "s_waitcnt vmcnt(0)\n"
      "v_subrev_u32 v129, 115456, v9\n"
      "v_lshrrev_b32 v129, 3, v129\n"
      "v_min_u32 v129, 96, v129\n"
      "v_add_u32 v129, s77, v129\n"
      "v_add_u32 v129, 115296, v129\n"
      "ds_read_u16 v129, v129\n"
      "s_waitcnt lgkmcnt(0)\n"
      "v_lshlrev_b32_e64 v129, 16, v129\n"
      "v_fmac_f32 v148, s53, v129\n"
      "s_nop 1\n"
      "v_max_f32_dpp v128, v148, v148 row_ror:8 row_mask:0xf bank_mask:0xf\n"
      "s_nop 1\n"
      "v_max_f32_dpp v128, v128, v128 row_ror:4 row_mask:0xf bank_mask:0xf\n"
      "s_nop 1\n"
      "v_max_f32_dpp v128, v128, v128 row_ror:2 row_mask:0xf bank_mask:0xf\n"
      "s_nop 1\n"
      "v_max_f32_dpp v128, v128, v128 row_ror:1 row_mask:0xf bank_mask:0xf\n"
      "s_nop 1\n"
      "v_max_f32_dpp v128, v128, v128 row_bcast:15 row_mask:0xa bank_mask:0xf\n"
      "s_nop 1\n"
      "v_max_f32_dpp v128, v128, v128 row_bcast:31 row_mask:0xc bank_mask:0xf\n"
      "s_nop 0\n"
      "v_readlane_b32 s72, v128, 63\n"
      "s_nop 1\n"
      "v_subrev_f32 v148, s72, v148\n"
      "s_waitcnt lgkmcnt(0)\n"
      "s_nop 4\n"
      "s_branch L_back_10_%=\n"
      "L_rare_27_%=:\n"
      "s_nop 7\n"
      "s_waitcnt lgkmcnt(0)\n"
      "s_bitcmp1_b32 s85, 31\n"
      "s_cselect_b32 s75, 100270464, 0\n"
      "s_bitcmp1_b32 s85, 30\n"
      "s_cselect_b32 s76, 196224, 0\n"
      "s_and_b32 s74, s85, 0x3fffffff\n"
      "s_cmp_ge_u32 s85, 0x40000000\n"
      "s_cselect_b32 s74, s74, 0\n"
      "s_cselect_b32 s85, 0, s85\n"
      "s_sub_u32 s74, s74, s69\n"
      "s_add_u32 s69, s69, s74\n"
      "s_sub_u32 s75, s75, s70\n"
      "s_add_u32 s70, s70, s75\n"
      "s_sub_u32 s76, s76, s71\n"
      "s_add_u32 s71, s71, s76\n"
      "v_add_u32 v20, s74, v20\n"
      "v_add_u32 v21, s74, v21\n"
      "v_add_u32 v22, s74, v22\n"
      "v_add_u32 v23, s74, v23\n"
      "v_add_u32 v25, s74, v25\n"
      "s_cmp_eq_u32 s75, 0\n"
      "s_cbranch_scc1 L_ax_25_%=\n"
      "v_bfe_i32 v194, v27, 10, 1\n"
      "v_and_b32 v194, s75, v194\n"
      "v_sub_u32 v20, v20, v194\n"
      "v_bfe_i32 v194, v27, 12, 1\n"
      "v_and_b32 v194, s75, v194\n"
      "v_sub_u32 v21, v21, v194\n"
      "v_bfe_i32 v194, v27, 14, 1\n"
      "v_and_b32 v194, s75, v194\n"
      "v_sub_u32 v22, v22, v194\n"
      "v_bfe_i32 v194, v27, 16, 1\n"
      "v_and_b32 v194, s75, v194\n"
      "v_sub_u32 v23, v23, v194\n"
      "v_bfe_i32 v194, v27, 18, 1\n"
      "v_and_b32 v194, s75, v194\n"
      "v_sub_u32 v25, v25, v194\n"
      "L_ax_25_%=:\n"
      "s_cmp_eq_u32 s76, 0\n"
      "s_cbranch_scc1 L_ax_26_%=\n"
      "v_bfe_i32 v194, v27, 11, 1\n"
      "v_and_b32 v194, s76, v194\n"
      "v_sub_u32_e64 v20, v20, v194\n"
      "v_bfe_i32 v194, v27, 13, 1\n"
      "v_and_b32 v194, s76, v194\n"
      "v_sub_u32 v21, v21, v194\n"
      "v_bfe_i32 v194, v27, 15, 1\n"
      "v_and_b32 v194, s76, v194\n"
      "v_sub_u32 v22, v22, v194\n"
      "v_bfe_i32 v194, v27, 17, 1\n"
      "v_and_b32 v194, s76, v194\n"
      "v_sub_u32 v23, v23, v194\n"
      "v_bfe_i32 v194, v27, 19, 1\n"
      "v_and_b32 v194, s76, v194\n"
      "v_sub_u32 v25, v25, v194\n"
      "L_ax_26_%=:\n"
      "s_or_b32 s48, s66, s69\n"
      "s_cmp_lg_u32 s48, 0\n"
      "s_cselect_b32 s48, 0x40000000, 0\n"
      "s_waitcnt lgkmcnt(0)\n"
      "s_nop 4\n"
      "s_branch L_back_28_%=\n"
      "L_rare_31_%=:\n"
      "s_nop 7\n"
      "s_waitcnt lgkmcnt(0)\n"
      "s_bitcmp1_b32 s85, 31\n"
      "s_cselect_b32 s75, 100270464, 0\n"
      "s_bitcmp1_b32 s85, 30\n"
      "s_cselect_b32 s76, 196224, 0\n"
      "s_and_b32 s74, s85, 0x3fffffff\n"
      "s_cmp_ge_u32 s85, 0x40000000\n"
      "s_cselect_b32 s74, s74, 0\n"
      "s_cselect_b32 s85, 0, s85\n"
      "s_sub_u32 s74, s74, s69\n"
      "s_add_u32 s69, s69, s74\n"
      "s_sub_u32 s75, s75, s70\n"
      "s_add_u32 s70, s70, s75\n"
      "s_sub_u32 s76, s76, s71\n"
      "s_add_u32 s71, s71, s76\n"
      "v_add_u32 v20, s74, v20\n"
      "v_add_u32 v21, s74, v21\n"
      "v_add_u32 v22, s74, v22\n"
      "v_add_u32 v23, s74, v23\n"
      "v_add_u32 v25, s74, v25\n"
      "s_cmp_eq_u32 s75, 0\n"
      "s_cbranch_scc1 L_ax_29_%=\n"
      "v_bfe_i32 v220, v27, 10, 1\n"
      "v_and_b32 v220, s75, v220\n"
      "v_sub_u32 v20, v20, v220\n"
      "v_bfe_i32 v220, v27, 12, 1\n"
      "v_and_b32 v220, s75, v220\n"
      "v_sub_u32 v21, v21, v220\n"
      "v_bfe_i32 v220, v27, 14, 1\n"
      "v_and_b32 v220, s75, v220\n"
      "v_sub_u32 v22, v22, v220\n"
      "v_bfe_i32 v220, v27, 16, 1\n"
      "v_and_b32 v220, s75, v220\n"
      "v_sub_u32 v23, v23, v220\n"
      "v_bfe_i32 v220, v27, 18, 1\n"
      "v_and_b32 v220, s75, v220\n"
      "v_sub_u32 v25, v25, v220\n"
      "L_ax_29_%=:\n"
      "s_cmp_eq_u32 s76, 0\n"
      "s_cbranch_scc1 L_ax_30_%=\n"
      "v_bfe_i32 v220, v27, 11, 1\n"
      "v_and_b32 v220, s76, v220\n"
      "v_sub_u32 v20, v20, v220\n"
      "v_bfe_i32 v220, v27, 13, 1\n"
      "v_and_b32 v220, s76, v220\n"
      "v_sub_u32 v21, v21, v220\n"
      "v_bfe_i32 v220, v27, 15, 1\n"
      "v_and_b32 v220, s76, v220\n"
      "v_sub_u32 v22, v22, v220\n"
      "v_bfe_i32 v220, v27, 17, 1\n"
      "v_and_b32 v220, s76, v220\n"
      "v_sub_u32 v23, v23, v220\n"
      "v_bfe_i32 v220, v27, 19, 1\n"
      "v_and_b32 v220, s76, v220\n"
      "v_sub_u32 v25, v25, v220\n"
      "L_ax_30_%=:\n"
      "s_or_b32 s48, s66, s69\n"
      "s_cmp_lg_u32 s48, 0\n"
      "s_cselect_b32 s48, 0x40000000, 0\n"
      "s_waitcnt lgkmcnt(0)\n"
      "s_nop 4\n"
      "s_branch L_back_32_%=\n"
      "L_rare_35_%=:\n"
      "s_nop 7\n"
      "s_waitcnt lgkmcnt(0)\n"
      "s_bitcmp1_b32 s46, 31\n"
      "s_cselect_b32 s75, 100270464, 0\n"
      "s_bitcmp1_b32 s46, 30\n"
      "s_cselect_b32 s76, 196224, 0\n"
      "s_and_b32 s74, s46, 0x3fffffff\n"
      "s_cmp_ge_u32 s46, 0x40000000\n"
      "s_cselect_b32 s74, s74, 0\n"
      "s_cselect_b32 s46, 0, s46\n"
      "s_sub_u32 s74, s74, s69\n"
      "s_add_u32 s69, s69, s74\n"
      "s_sub_u32 s75, s75, s70\n"
      "s_add_u32 s70, s70, s75\n"
      "s_sub_u32 s76, s76, s71\n"
      "s_add_u32 s71, s71, s76\n"
      "v_add_u32 v20, s74, v20\n"
      "v_add_u32 v21, s74, v21\n"
      "v_add_u32 v22, s74, v22\n"
      "v_add_u32 v23, s74, v23\n"
      "v_add_u32 v25, s74, v25\n"
      "s_cmp_eq_u32 s75, 0\n"
      "s_cbranch_scc1 L_ax_33_%=\n"
      "v_bfe_i32 v116, v27, 10, 1\n"
      "v_and_b32 v116, s75, v116\n"
      "v_sub_u32 v20, v20, v116\n"
      "v_bfe_i32 v116, v27, 12, 1\n"
      "v_and_b32 v116, s75, v116\n"
      "v_sub_u32 v21, v21, v116\n"
      "v_bfe_i32 v116, v27, 14, 1\n"
      "v_and_b32 v116, s75, v116\n"
      "v_sub_u32 v22, v22, v116\n"
      "v_bfe_i32 v116, v27, 16, 1\n"
      "v_and_b32 v116, s75, v116\n"
      "v_sub_u32 v23, v23, v116\n"
      "v_bfe_i32 v116, v27, 18, 1\n"
      "v_and_b32 v116, s75, v116\n"
      "v_sub_u32 v25, v25, v116\n"
      "L_ax_33_%=:\n"
      "s_cmp_eq_u32 s76, 0\n"
      "s_cbranch_scc1 L_ax_34_%=\n"
      "v_bfe_i32 v116, v27, 11, 1\n"
      "v_and_b32 v116, s76, v116\n"
      "v_sub_u32 v20, v20, v116\n"
      "v_bfe_i32 v116, v27, 13, 1\n"
      "v_and_b32 v116, s76, v116\n"
      "v_sub_u32 v21, v21, v116\n"
      "v_bfe_i32 v116, v27, 15, 1\n"
      "v_and_b32 v116, s76, v116\n"
      "v_sub_u32 v22, v22, v116\n"
      "v_bfe_i32 v116, v27, 17, 1\n"
      "v_and_b32 v116, s76, v116\n"
      "v_sub_u32 v23, v23, v116\n"
      "v_bfe_i32 v116, v27, 19, 1\n"
      "v_and_b32 v116, s76, v116\n"
      "v_sub_u32 v25, v25, v116\n"
      "L_ax_34_%=:\n"
      "s_or_b32 s48, s66, s69\n"
      "s_cmp_lg_u32 s48, 0\n"
      "s_cselect_b32 s48, 0x40000000, 0\n"
      "s_waitcnt lgkmcnt(0)\n"
      "s_nop 4\n"
      "s_branch L_back_36_%=\n"
      "L_rare_39_%=:\n"
      "s_nop 7\n"
      "s_waitcnt lgkmcnt(0)\n"
      "s_bitcmp1_b32 s46, 31\n"
      "s_cselect_b32 s75, 100270464, 0\n"
      "s_bitcmp1_b32 s46, 30\n"
      "s_cselect_b32 s76, 196224, 0\n"
      "s_and_b32 s74, s46, 0x3fffffff\n"
      "s_cmp_ge_u32 s46, 0x40000000\n"
      "s_cselect_b32 s74, s74, 0\n"
      "s_cselect_b32 s46, 0, s46\n"
      "s_sub_u32 s74, s74, s69\n"
      "s_add_u32 s69, s69, s74\n"
      "s_sub_u32 s75, s75, s70\n"
      "s_add_u32 s70, s70, s75\n"
      "s_sub_u32 s76, s76, s71\n"
      "s_add_u32 s71, s71, s76\n"
      "v_add_u32 v20, s74, v20\n"
      "v_add_u32 v21, s74, v21\n"
      "v_add_u32 v22, s74, v22\n"
      "v_add_u32 v23, s74, v23\n"
      "v_add_u32 v25, s74, v25\n"
      "s_cmp_eq_u32 s75, 0\n"
      "s_cbranch_scc1 L_ax_37_%=\n"
      "v_bfe_i32 v167, v27, 10, 1\n"
      "v_and_b32 v167, s75, v167\n"
      "v_sub_u32 v20, v20, v167\n"
      "v_bfe_i32 v167, v27, 12, 1\n"
      "v_and_b32 v167, s75, v167\n"
      "v_sub_u32 v21, v21, v167\n"
      "v_bfe_i32 v167, v27, 14, 1\n"
      "v_and_b32 v167, s75, v167\n"
      "v_sub_u32 v22, v22, v167\n"
      "v_bfe_i32 v167, v27, 16, 1\n"
      "v_and_b32 v167, s75, v167\n"
      "v_sub_u32 v23, v23, v167\n"
      "v_bfe_i32 v167, v27, 18, 1\n"
      "v_and_b32 v167, s75, v167\n"
      "v_sub_u32 v25, v25, v167\n"
      "L_ax_37_%=:\n"
      "s_cmp_eq_u32 s76, 0\n"
      "s_cbranch_scc1 L_ax_38_%=\n"
      "v_bfe_i32 v167, v27, 11, 1\n"
      "v_and_b32 v167, s76, v167\n"
      "v_sub_u32 v20, v20, v167\n"
      "v_bfe_i32 v167, v27, 13, 1\n"
      "v_and_b32 v167, s76, v167\n"
      "v_sub_u32 v21, v21, v167\n"
      "v_bfe_i32 v167, v27, 15, 1\n"
      "v_and_b32 v167, s76, v167\n"
      "v_sub_u32 v22, v22, v167\n"
      "v_bfe_i32 v167, v27, 17, 1\n"
      "v_and_b32 v167, s76, v167\n"
      "v_sub_u32 v23, v23, v167\n"
      "v_bfe_i32 v167, v27, 19, 1\n"
      "v_and_b32 v167, s76, v167\n"
      "v_sub_u32 v25, v25, v167\n"
      "L_ax_38_%=:\n"
      "s_or_b32 s48, s66, s69\n"
      "s_cmp_lg_u32 s48, 0\n"
      "s_cselect_b32 s48, 0x40000000, 0\n"
      "s_waitcnt lgkmcnt(0)\n"
      "s_nop 4\n"
      "s_branch L_back_40_%=\n"
      "L_cend_%=:\n"
      :
      : "s"(__builtin_amdgcn_kernarg_segment_ptr()), "s"(wave), "s"(blockIdx.x), "s"(CT_TAB), "s"(lds0)
      : "v0","v1","v2","v3","v4","v5","v6","v7","v8","v9","v10","v11","v12","v13","v14","v15","v16","v17","v18","v19","v20","v21","v22","v23","v24","v25","v26","v27","v28","v29","v30","v31","v32","v33","v34","v35","v36","v37","v38","v39","v40","v41","v42","v43","v44","v45","v46","v47","v48","v49","v50","v51","v52","v53","v54","v55","v56","v57","v58","v59","v60","v61","v62","v63","v64","v65","v66","v67","v68","v69","v70","v71","v72","v73","v74","v75","v76","v77","v78","v79","v80","v81","v82","v83","v84","v85","v86","v87","v88","v89","v90","v91","v92","v93","v94","v95","v96","v97","v98","v99","v100","v101","v102","v103","v104","v105","v106","v107","v108","v109","v110","v111","v112","v113","v114","v115","v116","v117","v118","v119","v120","v121","v122","v123","v124","v125","v126","v127","v128","v129","v130","v131","v132","v133","v134","v135","v136","v137","v138","v139","v140","v141","v142","v143","v144","v145","v146","v147","v148","v149","v150","v151","v152","v153","v154","v155","v156","v157","v158","v159","v160","v161","v162","v163","v164","v165","v166","v167","v168","v169","v170","v171","v172","v173","v174","v175","v176","v177","v178","v179","v180","v181","v182","v183","v184","v185","v186","v187","v188","v189","v190","v191","v192","v193","v194","v195","v196","v197","v198","v199","v200","v201","v202","v203","v204","v205","v206","v207","v208","v209","v210","v211","v212","v213","v214","v215","v216","v217","v218","v219","v220","v221","v222","v223","v224","v225","v226","v227","v228","v229","v230","v231","v232","v233","v234","v235","v236","v237","v238","v239","v240","v241","v242","v243","v244","v245","v246","v247","v248","v249","v250","v251","v252","v253","v254","v255","a0","a1","a2","a3","a4","a5","a6","a7","a8","a9","a10","a11","a12","a13","a14","a15","a16","a17","a18","a19","a20","a21","a22","a23","a24","a25","a26","a27","a28","a29","a30","a31","a32","a33","a34","a35","a36","a37","a38","a39","a40","a41","a42","a43","a44","a45","a46","a47","a48","a49","a50","a51","a52","a53","a54","a55","a56","a57","a58","a59","a60","a61","a62","a63","a64","a65","a66","a67","a68","a69","a70","a71","a72","a73","a74","a75","a76","a77","a78","a79","a80","a81","a82","a83","a84","a85","a86","a87","a88","a89","a90","a91","a92","a93","a94","a95","a96","a97","a98","a99","a100","a101","a102","a103","a104","a105","a106","a107","a108","a109","a110","a111","a112","a113","a114","a115","a116","a117","a118","a119","a120","a121","a122","a123","a124","a125","a126","a127","a128","a129","a130","a131","a132","a133","a134","a135","a136","a137","a138","a139","a140","a141","a142","a143","a144","a145","a146","a147","a148","a149","a150","a151","a152","a153","a154","a155","a156","a157","a158","a159","a160","a161","a162","a163","a164","a165","a166","a167","a168","a169","a170","a171","a172","a173","a174","a175","a176","a177","a178","a179","a180","a181","a182","a183","a184","a185","a186","a187","a188","a189","a190","a191","a192","a193","a194","a195","a196","a197","a198","a199","a200","a201","a202","a203","a204","a205","a206","a207","a208","a209","a210","a211","a212","a213","a214","a215","a216","a217","a218","a219","a220","a221","a222","a223","a224","a225","a226","a227","a228","a229","a230","a231","a232","a233","a234","a235","a236","a237","a238","a239","a240","a241","a242","a243","a244","a245","a246","a247","a248","a249","a250","a251","a252","a253","a254","a255","s16","s17","s18","s19","s20","s21","s22","s23","s24","s25","s26","s27","s28","s29","s30","s31","s33","s34","s35","s36","s37","s38","s39","s40","s41","s42","s43","s44","s45","s46","s47","s48","s49","s50","s51","s52","s53","s54","s55","s56","s57","s58","s59","s60","s61","s62","s63","s64","s65","s66","s67","s68","s69","s70","s71","s72","s73","s74","s75","s76","s77","s78","s79","s80","s81","s82","s83","s84","s85","s86","s87","s88","s89","s90","s91","s92","s93","s94","s95","s96","s97","s98","s99","s100","s101", "vcc", "scc", "memory");
}

}

extern "C" void kernel_launch(void* const* d_in, const int* in_sizes, int n_in, void* d_out, int out_size, void* d_ws,
                              size_t ws_size, hipStream_t stream) {
  const uint16_t* x = (const uint16_t*)d_in[0];
  const uint16_t* wqkv = (const uint16_t*)d_in[1];
  const uint16_t* wo = (const uint16_t*)d_in[2];
  const uint16_t* ls = (const uint16_t*)d_in[3];
  const uint16_t* w1 = (const uint16_t*)d_in[4];
  const uint16_t* w2 = (const uint16_t*)d_in[5];
  const uint16_t* rc = (const uint16_t*)d_in[6];
  const int* ridx = (const int*)d_in[7];
  const uint16_t* mask = (const uint16_t*)d_in[8];
  swin::swin_main<<<256, 256, 0, stream>>>(x, mask, (char*)d_ws, (uint16_t*)d_out, wqkv, wo, ls, w1, w2, rc, ridx);
}
```

```cpp
#include <hip/hip_runtime.h>
#include <hip/hip_bf16.h>
#include <stdint.h>

namespace swin {

constexpr unsigned CT_WORDS = 8192u;
constexpr unsigned LDS_BYTES = 163840u;

__device__ __attribute__((aligned(256))) const unsigned CT_TAB[CT_WORDS] = {0x4800u,0x4820u,0x4840u,0x4800u,0x4808u,0x4828u,0x4848u,0x4810u,0x4810u,0x4830u,0x4850u,0x4820u,0x4818u,0x4838u,0x4858u,0x4830u,0x4880u,0x48a0u,0x48c0u,0x4880u,0x4888u,0x48a8u,0x48c8u,0x4890u,0x4890u,0x48b0u,0x48d0u,0x48a0u,0x4898u,0x48b8u,0x48d8u,0x48b0u,0x4920u,0x4900u,0x4960u,0x4920u,0x4928u,0x4908u,0x4968u,0x4930u,0x4930u,0x4910u,0x4970u,0x4900u,0x4938u,0x4918u,0x4978u,0x4910u,0x49a0u,0x4980u,0x49e0u,0x49a0u,0x49a8u,0x4988u,0x49e8u,0x49b0u,0x49b0u,0x4990u,0x49f0u,0x4980u,0x49b8u,0x4998u,0x49f8u,0x4990u,0x4c40u,0x4c60u,0x4c00u,0x4a00u,0x4c48u,0x4c68u,0x4c08u,0x4a10u,0x4c50u,0x4c70u,0x4c10u,0x4a20u,0x4c58u,0x4c78u,0x4c18u,0x4a30u,0x4cc0u,0x4ce0u,0x4c80u,0x4a80u,0x4cc8u,0x4ce8u,0x4c88u,0x4a90u,0x4cd0u,0x4cf0u,0x4c90u,0x4aa0u,0x4cd8u,0x4cf8u,0x4c98u,0x4ab0u,0x4d60u,0x4d40u,0x4d20u,0x4b20u,0x4d68u,0x4d48u,0x4d28u,0x4b30u,0x4d70u,0x4d50u,0x4d30u,0x4b00u,0x4d78u,0x4d58u,0x4d38u,0x4b10u,0x4de0u,0x4dc0u,0x4da0u,0x4ba0u,0x4de8u,0x4dc8u,0x4da8u,0x4bb0u,0x4df0u,0x4dd0u,0x4db0u,0x4b80u,0x4df8u,0x4dd8u,0x4db8u,0x4b90u,0x5000u,0x5020u,0x5040u,0x4c40u,0x5008u,0x5028u,0x5048u,0x4c50u,0x5010u,0x5030u,0x5050u,0x4c60u,0x5018u,0x5038u,0x5058u,0x4c70u,0x5080u,0x50a0u,0x50c0u,0x4cc0u,0x5088u,0x50a8u,0x50c8u,0x4cd0u,0x5090u,0x50b0u,0x50d0u,0x4ce0u,0x5098u,0x50b8u,0x50d8u,0x4cf0u,0x5120u,0x5100u,0x5160u,0x4d60u,0x5128u,0x5108u,0x5168u,0x4d70u,0x5130u,0x5110u,0x5170u,0x4d40u,0x5138u,0x5118u,0x5178u,0x4d50u,0x51a0u,0x5180u,0x51e0u,0x4de0u,0x51a8u,0x5188u,0x51e8u,0x4df0u,0x51b0u,0x5190u,0x51f0u,0x4dc0u,0x51b8u,0x5198u,0x51f8u,0x4dd0u,0x5440u,0x5460u,0x5400u,0x4e40u,0x5448u,0x5468u,0x5408u,0x4e50u,0x5450u,0x5470u,0x5410u,0x4e60u,0x5458u,0x5478u,0x5418u,0x4e70u,0x54c0u,0x54e0u,0x5480u,0x4ec0u,0x54c8u,0x54e8u,0x5488u,0x4ed0u,0x54d0u,0x54f0u,0x5490u,0x4ee0u,0x54d8u,0x54f8u,0x5498u,0x4ef0u,0x5560u,0x5540u,0x5520u,0x4f60u,0x5568u,0x5548u,0x5528u,0x4f70u,0x5570u,0x5550u,0x5530u,0x4f40u,0x5578u,0x5558u,0x5538u,0x4f50u,0x55e0u,0x55c0u,0x55a0u,0x4fe0u,0x55e8u,0x55c8u,0x55a8u,0x4ff0u,0x55f0u,0x55d0u,0x55b0u,0x4fc0u,0x55f8u,0x55d8u,0x55b8u,0x4fd0u,0x4840u,0x1e800u,0x1eb00u,0x0u,0x4848u,0x1ec90u,0x1ef90u,0x10u,0x4850u,0x1f120u,0x1f420u,0x20u,0x4858u,0x1f5b0u,0x1f8b0u,0x30u,0x48c0u,0x1fa40u,0x1fd40u,0x40u,0x48c8u,0x1fed0u,0x201d0u,0x50u,0x48d0u,0x20360u,0x20660u,0x60u,0x48d8u,0x207f0u,0x20af0u,0x70u,0x4960u,0x20c80u,0x20f80u,0x180u,0x4968u,0x21110u,0x21410u,0x190u,0x4970u,0x215a0u,0x218a0u,0x1a0u,0x4978u,0x21a30u,0x21d30u,0x1b0u,0x49e0u,0x21ec0u,0x221c0u,0x1c0u,0x49e8u,0x22350u,0x22650u,0x1d0u,0x49f0u,0x227e0u,0x22ae0u,0x1e0u,0x49f8u,0x22c70u,0x22f70u,0x1f0u,0x4a40u,0x1e830u,0x1eb08u,0x320u,0x4a48u,0x1ecc0u,0x1ef98u,0x330u,0x4a50u,0x1f150u,0x1f428u,0x300u,0x4a58u,0x1f5e0u,0x1f8b8u,0x310u,0x4ac0u,0x1fa70u,0x1fd48u,0x360u,0x4ac8u,0x1ff00u,0x201d8u,0x370u,0x4ad0u,0x20390u,0x20668u,0x340u,0x4ad8u,0x20820u,0x20af8u,0x350u,0x4b60u,0x20cb0u,0x20f88u,0x4a0u,0x4b68u,0x21140u,0x21418u,0x4b0u,0x4b70u,0x215d0u,0x218a8u,0x480u,0x4b78u,0x21a60u,0x21d38u,0x490u,0x4be0u,0x21ef0u,0x221c8u,0x4e0u,0x4be8u,0x22380u,0x22658u,0x4f0u,0x4bf0u,0x22810u,0x22ae8u,0x4c0u,0x4bf8u,0x22ca0u,0x22f78u,0x4d0u,0x4c00u,0x1e860u,0x1eb10u,0x600u,0x4c08u,0x1ecf0u,0x1efa0u,0x610u,0x4c10u,0x1f180u,0x1f430u,0x620u,0x4c18u,0x1f610u,0x1f8c0u,0x630u,0x4c80u,0x1faa0u,0x1fd50u,0x640u,0x4c88u,0x1ff30u,0x201e0u,0x650u,0x4c90u,0x203c0u,0x20670u,0x660u,0x4c98u,0x20850u,0x20b00u,0x670u,0x4d20u,0x20ce0u,0x20f90u,0x780u,0x4d28u,0x21170u,0x21420u,0x790u,0x4d30u,0x21600u,0x218b0u,0x7a0u,0x4d38u,0x21a90u,0x21d40u,0x7b0u,0x4da0u,0x21f20u,0x221d0u,0x7c0u,0x4da8u,0x223b0u,0x22660u,0x7d0u,0x4db0u,0x22840u,0x22af0u,0x7e0u,0x4db8u,0x22cd0u,0x22f80u,0x7f0u,0x4e00u,0x1e890u,0x1eb18u,0x920u,0x4e08u,0x1ed20u,0x1efa8u,0x930u,0x4e10u,0x1f1b0u,0x1f438u,0x900u,0x4e18u,0x1f640u,0x1f8c8u,0x910u,0x4e80u,0x1fad0u,0x1fd58u,0x960u,0x4e88u,0x1ff60u,0x201e8u,0x970u,0x4e90u,0x203f0u,0x20678u,0x940u,0x4e98u,0x20880u,0x20b08u,0x950u,0x4f20u,0x20d10u,0x20f98u,0xaa0u,0x4f28u,0x211a0u,0x21428u,0xab0u,0x4f30u,0x21630u,0x218b8u,0xa80u,0x4f38u,0x21ac0u,0x21d48u,0xa90u,0x4fa0u,0x21f50u,0x221d8u,0xae0u,0x4fa8u,0x223e0u,0x22668u,0xaf0u,0x4fb0u,0x22870u,0x22af8u,0xac0u,0x4fb8u,0x22d00u,0x22f88u,0xad0u,0x0u,0x40u,0x9000u,0x9000u,0x80u,0xc0u,0x9010u,0x9010u,0x110u,0x150u,0x9020u,0x9020u,0x190u,0x1d0u,0x9030u,0x9030u,0x220u,0x260u,0x9040u,0x9040u,0x2a0u,0x2e0u,0x9050u,0x9050u,0x330u,0x370u,0x9060u,0x9060u,0x3b0u,0x3f0u,0x9070u,0x9070u,0x440u,0x400u,0x9080u,0x9080u,0x4c0u,0x480u,0x9090u,0x9090u,0x550u,0x510u,0x90a0u,0x90a0u,0x5d0u,0x590u,0x90b0u,0x90b0u,0x660u,0x620u,0x90c0u,0x90c0u,0x6e0u,0x6a0u,0x90d0u,0x90d0u,0x770u,0x730u,0x90e0u,0x90e0u,0x7f0u,0x7b0u,0x90f0u,0x90f0u,0x10u,0x50u,0x9300u,0x9300u,0x90u,0xd0u,0x9310u,0x9310u,0x100u,0x140u,0x9320u,0x9320u,0x180u,0x1c0u,0x9330u,0x9330u,0x230u,0x270u,0x9340u,0x9340u,0x2b0u,0x2f0u,0x9350u,0x9350u,0x320u,0x360u,0x9360u,0x9360u,0x3a0u,0x3e0u,0x9370u,0x9370u,0x450u,0x410u,0x9380u,0x9380u,0x4d0u,0x490u,0x9390u,0x9390u,0x540u,0x500u,0x93a0u,0x93a0u,0x5c0u,0x580u,0x93b0u,0x93b0u,0x670u,0x630u,0x93c0u,0x93c0u,0x6f0u,0x6b0u,0x93d0u,0x93d0u,0x760u,0x720u,0x93e0u,0x93e0u,0x7e0u,0x7a0u,0x93f0u,0x93f0u,0x20u,0x60u,0x9600u,0x9600u,0xa0u,0xe0u,0x9610u,0x9610u,0x130u,0x170u,0x9620u,0x9620u,0x1b0u,0x1f0u,0x9630u,0x9630u,0x200u,0x240u,0x9640u,0x9640u,0x280u,0x2c0u,0x9650u,0x9650u,0x310u,0x350u,0x9660u,0x9660u,0x390u,0x3d0u,0x9670u,0x9670u,0x460u,0x420u,0x9680u,0x9680u,0x4e0u,0x4a0u,0x9690u,0x9690u,0x570u,0x530u,0x96a0u,0x96a0u,0x5f0u,0x5b0u,0x96b0u,0x96b0u,0x640u,0x600u,0x96c0u,0x96c0u,0x6c0u,0x680u,0x96d0u,0x96d0u,0x750u,0x710u,0x96e0u,0x96e0u,0x7d0u,0x790u,0x96f0u,0x96f0u,0x30u,0x70u,0x9900u,0x9900u,0xb0u,0xf0u,0x9910u,0x9910u,0x120u,0x160u,0x9920u,0x9920u,0x1a0u,0x1e0u,0x9930u,0x9930u,0x210u,0x250u,0x9940u,0x9940u,0x290u,0x2d0u,0x9950u,0x9950u,0x300u,0x340u,0x9960u,0x9960u,0x380u,0x3c0u,0x9970u,0x9970u,0x470u,0x430u,0x9980u,0x9980u,0x4f0u,0x4b0u,0x9990u,0x9990u,0x560u,0x520u,0x99a0u,0x99a0u,0x5e0u,0x5a0u,0x99b0u,0x99b0u,0x650u,0x610u,0x99c0u,0x99c0u,0x6d0u,0x690u,0x99d0u,0x99d0u,0x740u,0x700u,0x99e0u,0x99e0u,0x7c0u,0x780u,0x99f0u,0x99f0u,0x9c00u,0x12000u,0x12040u,0x12000u,0x9c10u,0x12190u,0x121d0u,0x12010u,0x9c20u,0x12320u,0x12360u,0x12020u,0x9c30u,0x124b0u,0x124f0u,0x12030u,0x9c40u,0x12640u,0x12600u,0x12040u,0x9c50u,0x127d0u,0x12790u,0x12050u,0x9c60u,0x12960u,0x12920u,0x12060u,0x9c70u,0x12af0u,0x12ab0u,0x12070u,0x9c80u,0x12c00u,0x12c40u,0x12080u,0x9c90u,0x12d90u,0x12dd0u,0x12090u,0x9ca0u,0x12f20u,0x12f60u,0x120a0u,0x9cb0u,0x130b0u,0x130f0u,0x120b0u,0x9cc0u,0x13240u,0x13200u,0x120c0u,0x9cd0u,0x133d0u,0x13390u,0x120d0u,0x9ce0u,0x13560u,0x13520u,0x120e0u,0x9cf0u,0x136f0u,0x136b0u,0x120f0u,0x9f00u,0x12010u,0x12048u,0x12100u,0x9f10u,0x12180u,0x121d8u,0x12110u,0x9f20u,0x12330u,0x12368u,0x12120u,0x9f30u,0x124a0u,0x124f8u,0x12130u,0x9f40u,0x12650u,0x12608u,0x12140u,0x9f50u,0x127c0u,0x12798u,0x12150u,0x9f60u,0x12970u,0x12928u,0x12160u,0x9f70u,0x12ae0u,0x12ab8u,0x12170u,0x9f80u,0x12c10u,0x12c48u,0x12180u,0x9f90u,0x12d80u,0x12dd8u,0x12190u,0x9fa0u,0x12f30u,0x12f68u,0x121a0u,0x9fb0u,0x130a0u,0x130f8u,0x121b0u,0x9fc0u,0x13250u,0x13208u,0x121c0u,0x9fd0u,0x133c0u,0x13398u,0x121d0u,0x9fe0u,0x13570u,0x13528u,0x121e0u,0x9ff0u,0x136e0u,0x136b8u,0x121f0u,0xa200u,0x12020u,0x12050u,0x12200u,0xa210u,0x121b0u,0x121c0u,0x12210u,0xa220u,0x12300u,0x12370u,0x12220u,0xa230u,0x12490u,0x124e0u,0x12230u,0xa240u,0x12660u,0x12610u,0x12240u,0xa250u,0x127f0u,0x12780u,0x12250u,0xa260u,0x12940u,0x12930u,0x12260u,0xa270u,0x12ad0u,0x12aa0u,0x12270u,0xa280u,0x12c20u,0x12c50u,0x12280u,0xa290u,0x12db0u,0x12dc0u,0x12290u,0xa2a0u,0x12f00u,0x12f70u,0x122a0u,0xa2b0u,0x13090u,0x130e0u,0x122b0u,0xa2c0u,0x13260u,0x13210u,0x122c0u,0xa2d0u,0x133f0u,0x13380u,0x122d0u,0xa2e0u,0x13540u,0x13530u,0x122e0u,0xa2f0u,0x136d0u,0x136a0u,0x122f0u,0xa500u,0x12030u,0x12058u,0x12300u,0xa510u,0x121a0u,0x121c8u,0x12310u,0xa520u,0x12310u,0x12378u,0x12320u,0xa530u,0x12480u,0x124e8u,0x12330u,0xa540u,0x12670u,0x12618u,0x12340u,0xa550u,0x127e0u,0x12788u,0x12350u,0xa560u,0x12950u,0x12938u,0x12360u,0xa570u,0x12ac0u,0x12aa8u,0x12370u,0xa580u,0x12c30u,0x12c58u,0x12380u,0xa590u,0x12da0u,0x12dc8u,0x12390u,0xa5a0u,0x12f10u,0x12f78u,0x123a0u,0xa5b0u,0x13080u,0x130e8u,0x123b0u,0xa5c0u,0x13270u,0x13218u,0x123c0u,0xa5d0u,0x133e0u,0x13388u,0x123d0u,0xa5e0u,0x13550u,0x13538u,0x123e0u,0xa5f0u,0x136c0u,0x136a8u,0x123f0u,0x4800u,0x1c300u,0x1e800u,0x1eb00u,0x4880u,0x1c310u,0x100000u,0x1eb02u,0x4910u,0x1c320u,0x100000u,0x1eb04u,0x4990u,0x1c330u,0x100000u,0x1eb06u,0x4a20u,0x1c340u,0x1e800u,0x1eb08u,0x4aa0u,0x1c350u,0x100000u,0x1eb0au,0x4b30u,0x1c360u,0x100000u,0x1eb0cu,0x4bb0u,0x1c370u,0x100000u,0x1eb0eu,0x4c40u,0x1c380u,0x1e800u,0x1eb10u,0x4cc0u,0x1c390u,0x100000u,0x1eb12u,0x4d50u,0x1c3a0u,0x100000u,0x1eb14u,0x4dd0u,0x1c3b0u,0x100000u,0x1eb16u,0x4e60u,0x1c3c0u,0x1e800u,0x1eb18u,0x4ee0u,0x1c3d0u,0x100000u,0x1eb1au,0x4f70u,0x1c3e0u,0x100000u,0x1eb1cu,0x4ff0u,0x1c3f0u,0x100000u,0x1eb1eu,0x4810u,0x1c400u,0x1e830u,0x1eb00u,0x4890u,0x1c410u,0x100000u,0x1eb02u,0x4900u,0x1c420u,0x100000u,0x1eb04u,0x4980u,0x1c430u,0x100000u,0x1eb06u,0x4a30u,0x1c440u,0x1e830u,0x1eb08u,0x4ab0u,0x1c450u,0x100000u,0x1eb0au,0x4b20u,0x1c460u,0x100000u,0x1eb0cu,0x4ba0u,0x1c470u,0x100000u,0x1eb0eu,0x4c50u,0x1c480u,0x1e830u,0x1eb10u,0x4cd0u,0x1c490u,0x100000u,0x1eb12u,0x4d40u,0x1c4a0u,0x100000u,0x1eb14u,0x4dc0u,0x1c4b0u,0x100000u,0x1eb16u,0x4e70u,0x1c4c0u,0x1e830u,0x1eb18u,0x4ef0u,0x1c4d0u,0x100000u,0x1eb1au,0x4f60u,0x1c4e0u,0x100000u,0x1eb1cu,0x4fe0u,0x1c4f0u,0x100000u,0x1eb1eu,0x4820u,0x1c500u,0x1e860u,0x1eb00u,0x48a0u,0x1c510u,0x100000u,0x1eb02u,0x4930u,0x1c520u,0x100000u,0x1eb04u,0x49b0u,0x1c530u,0x100000u,0x1eb06u,0x4a00u,0x1c540u,0x1e860u,0x1eb08u,0x4a80u,0x1c550u,0x100000u,0x1eb0au,0x4b10u,0x1c560u,0x100000u,0x1eb0cu,0x4b90u,0x1c570u,0x100000u,0x1eb0eu,0x4c60u,0x1c580u,0x1e860u,0x1eb10u,0x4ce0u,0x1c590u,0x100000u,0x1eb12u,0x4d70u,0x1c5a0u,0x100000u,0x1eb14u,0x4df0u,0x1c5b0u,0x100000u,0x1eb16u,0x4e40u,0x1c5c0u,0x1e860u,0x1eb18u,0x4ec0u,0x1c5d0u,0x100000u,0x1eb1au,0x4f50u,0x1c5e0u,0x100000u,0x1eb1cu,0x4fd0u,0x1c5f0u,0x100000u,0x1eb1eu,0x4830u,0x1c600u,0x1e890u,0x1eb00u,0x48b0u,0x1c610u,0x100000u,0x1eb02u,0x4920u,0x1c620u,0x100000u,0x1eb04u,0x49a0u,0x1c630u,0x100000u,0x1eb06u,0x4a10u,0x1c640u,0x1e890u,0x1eb08u,0x4a90u,0x1c650u,0x100000u,0x1eb0au,0x4b00u,0x1c660u,0x100000u,0x1eb0cu,0x4b80u,0x1c670u,0x100000u,0x1eb0eu,0x4c70u,0x1c680u,0x1e890u,0x1eb10u,0x4cf0u,0x1c690u,0x100000u,0x1eb12u,0x4d60u,0x1c6a0u,0x100000u,0x1eb14u,0x4de0u,0x1c6b0u,0x100000u,0x1eb16u,0x4e50u,0x1c6c0u,0x1e890u,0x1eb18u,0x4ed0u,0x1c6d0u,0x100000u,0x1eb1au,0x4f40u,0x1c6e0u,0x100000u,0x1eb1cu,0x4fc0u,0x1c6f0u,0x100000u,0x1eb1eu,0x24d00u,0x1d300u,0x1d300u,0xc0100u,0x24d02u,0x1d302u,0x100000u,0xc0110u,0x24d04u,0x1d304u,0x100000u,0xc0120u,0x24d06u,0x1d306u,0x100000u,0xc0130u,0x24f00u,0x1d310u,0x1d300u,0xc0140u,0x24f02u,0x1d312u,0x100000u,0xc0150u,0x24f04u,0x1d314u,0x100000u,0xc0160u,0x24f06u,0x1d316u,0x100000u,0xc0170u,0x25100u,0x1d320u,0x1d300u,0xc0280u,0x25102u,0x1d322u,0x100000u,0xc0290u,0x25104u,0x1d324u,0x100000u,0xc02a0u,0x25106u,0x1d326u,0x100000u,0xc02b0u,0x25300u,0x1d330u,0x1d300u,0xc02c0u,0x25302u,0x1d332u,0x100000u,0xc02d0u,0x25304u,0x1d334u,0x100000u,0xc02e0u,0x25306u,0x1d336u,0x100000u,0xc02f0u,0x24d08u,0x1d308u,0x1d310u,0xc0410u,0x24d0au,0x1d30au,0x100000u,0xc0400u,0x24d0cu,0x1d30cu,0x100000u,0xc0430u,0x24d0eu,0x1d30eu,0x100000u,0xc0420u,0x24f08u,0x1d318u,0x1d310u,0xc0450u,0x24f0au,0x1d31au,0x100000u,0xc0440u,0x24f0cu,0x1d31cu,0x100000u,0xc0470u,0x24f0eu,0x1d31eu,0x100000u,0xc0460u,0x25108u,0x1d328u,0x1d310u,0xef990u,0x2510au,0x1d32au,0x100000u,0xef980u,0x2510cu,0x1d32cu,0x100000u,0xef9b0u,0x2510eu,0x1d32eu,0x100000u,0xef9a0u,0x25308u,0x1d338u,0x1d310u,0xef9d0u,0x2530au,0x1d33au,0x100000u,0xef9c0u,0x2530cu,0x1d33cu,0x100000u,0xef9f0u,0x2530eu,0x1d33eu,0x100000u,0xef9e0u,0x25500u,0x1d340u,0x1d320u,0xefb20u,0x25502u,0x1d342u,0x100000u,0xefb30u,0x25504u,0x1d344u,0x100000u,0xefb00u,0x25506u,0x1d346u,0x100000u,0xefb10u,0x25700u,0x1d350u,0x1d320u,0xefb60u,0x25702u,0x1d352u,0x100000u,0xefb70u,0x25704u,0x1d354u,0x100000u,0xefb40u,0x25706u,0x1d356u,0x100000u,0xefb50u,0x25900u,0x1d360u,0x1d320u,0xefca0u,0x25902u,0x1d362u,0x100000u,0xefcb0u,0x25904u,0x1d364u,0x100000u,0xefc80u,0x25906u,0x1d366u,0x100000u,0xefc90u,0x25b00u,0x1d370u,0x1d320u,0xefce0u,0x25b02u,0x1d372u,0x100000u,0xefcf0u,0x25b04u,0x1d374u,0x100000u,0xefcc0u,0x25b06u,0x1d376u,0x100000u,0xefcd0u,0x100000u,0x1d348u,0x1d330u,0xefe30u,0x100000u,0x1d34au,0x100000u,0xefe20u,0x100000u,0x1d34cu,0x100000u,0xefe10u,0x100000u,0x1d34eu,0x100000u,0xefe00u,0x100000u,0x1d358u,0x1d330u,0xefe70u,0x100000u,0x1d35au,0x100000u,0xefe60u,0x100000u,0x1d35cu,0x100000u,0xefe50u,0x100000u,0x1d35eu,0x100000u,0xefe40u,0x100000u,0x1d368u,0x1d330u,0xeffb0u,0x100000u,0x1d36au,0x100000u,0xeffa0u,0x100000u,0x1d36cu,0x100000u,0xeff90u,0x100000u,0x1d36eu,0x100000u,0xeff80u,0x100000u,0x1d378u,0x1d330u,0xefff0u,0x100000u,0x1d37au,0x100000u,0xeffe0u,0x100000u,0x1d37cu,0x100000u,0xeffd0u,0x100000u,0x1d37eu,0x100000u,0xeffc0u,0x0u,0x30420u,0x8fc50u,0xc0000u,0x10u,0x30430u,0x8fc40u,0xc0010u,0x20u,0x30400u,0x8fc70u,0xc0020u,0x30u,0x30410u,0x8fc60u,0xc0030u,0x40u,0x30460u,0x8fc10u,0xc0040u,0x50u,0x30470u,0x8fc00u,0xc0050u,0x60u,0x30440u,0x8fc30u,0xc0060u,0x70u,0x30450u,0x8fc20u,0xc0070u,0x80u,0x304b0u,0x8fcd0u,0xc0080u,0x90u,0x304a0u,0x8fcc0u,0xc0090u,0xa0u,0x30490u,0x8fcf0u,0xc00a0u,0xb0u,0x30480u,0x8fce0u,0xc00b0u,0xc0u,0x304f0u,0x8fc90u,0xc00c0u,0xd0u,0x304e0u,0x8fc80u,0xc00d0u,0xe0u,0x304d0u,0x8fcb0u,0xc00e0u,0xf0u,0x304c0u,0x8fca0u,0xc00f0u,0x100u,0x30530u,0x8fd60u,0xc0100u,0x110u,0x30520u,0x8fd70u,0xc0110u,0x120u,0x30510u,0x8fd40u,0xc0120u,0x130u,0x30500u,0x8fd50u,0xc0130u,0x140u,0x30570u,0x8fd20u,0xc0140u,0x150u,0x30560u,0x8fd30u,0xc0150u,0x160u,0x30550u,0x8fd00u,0xc0160u,0x170u,0x30540u,0x8fd10u,0xc0170u,0x190u,0x305b0u,0x8fde0u,0xc0190u,0x180u,0x305a0u,0x8fdf0u,0xc0180u,0x1b0u,0x30590u,0x8fdc0u,0xc01b0u,0x1a0u,0x30580u,0x8fdd0u,0xc01a0u,0x1d0u,0x305f0u,0x8fda0u,0xc01d0u,0x1c0u,0x305e0u,0x8fdb0u,0xc01c0u,0x1f0u,0x305d0u,0x8fd80u,0xc01f0u,0x1e0u,0x305c0u,0x8fd90u,0xc01e0u,0x210u,0x30640u,0x8fe60u,0xc0210u,0x200u,0x30650u,0x8fe70u,0xc0200u,0x230u,0x30660u,0x8fe40u,0xc0230u,0x220u,0x30670u,0x8fe50u,0xc0220u,0x250u,0x30600u,0x8fe20u,0xc0250u,0x240u,0x30610u,0x8fe30u,0xc0240u,0x270u,0x30620u,0x8fe00u,0xc0270u,0x260u,0x30630u,0x8fe10u,0xc0260u,0x290u,0x306c0u,0x8fef0u,0xc0290u,0x280u,0x306d0u,0x8fee0u,0xc0280u,0x2b0u,0x306e0u,0x8fed0u,0xc02b0u,0x2a0u,0x306f0u,0x8fec0u,0xc02a0u,0x2d0u,0x30680u,0x8feb0u,0xc02d0u,0x2c0u,0x30690u,0x8fea0u,0xc02c0u,0x2f0u,0x306a0u,0x8fe90u,0xc02f0u,0x2e0u,0x306b0u,0x8fe80u,0xc02e0u,0x320u,0x30740u,0x8ff70u,0xc0320u,0x330u,0x30750u,0x8ff60u,0xc0330u,0x300u,0x30760u,0x8ff50u,0xc0300u,0x310u,0x30770u,0x8ff40u,0xc0310u,0x360u,0x30700u,0x8ff30u,0xc0360u,0x370u,0x30710u,0x8ff20u,0xc0370u,0x340u,0x30720u,0x8ff10u,0xc0340u,0x350u,0x30730u,0x8ff00u,0xc0350u,0x3a0u,0x307d0u,0x8fff0u,0xc03a0u,0x3b0u,0x307c0u,0x8ffe0u,0xc03b0u,0x380u,0x307f0u,0x8ffd0u,0xc0380u,0x390u,0x307e0u,0x8ffc0u,0xc0390u,0x3e0u,0x30790u,0x8ffb0u,0xc03e0u,0x3f0u,0x30780u,0x8ffa0u,0xc03f0u,0x3c0u,0x307b0u,0x8ff90u,0xc03c0u,0x3d0u,0x307a0u,0x8ff80u,0xc03d0u,0x0u,0x11f820u,0x0u,0x7030cu,0x0u,0x11f830u,0x0u,0x7030cu,0x0u,0x11f800u,0x0u,0x7030cu,0x0u,0x11f810u,0x0u,0x7030cu,0x0u,0x11f860u,0x0u,0x7030cu,0x0u,0x11f870u,0x0u,0x7030cu,0x0u,0x11f840u,0x0u,0x7030cu,0x0u,0x11f850u,0x0u,0x7030cu,0x0u,0x11f8b0u,0x0u,0x7230cu,0x0u,0x11f8a0u,0x0u,0x7230cu,0x0u,0x11f890u,0x0u,0x7230cu,0x0u,0x11f880u,0x0u,0x7230cu,0x0u,0x11f8f0u,0x0u,0x7230cu,0x0u,0x11f8e0u,0x0u,0x7230cu,0x0u,0x11f8d0u,0x0u,0x7230cu,0x0u,0x11f8c0u,0x0u,0x7230cu,0x0u,0x11f930u,0x0u,0x7232cu,0x0u,0x11f920u,0x0u,0x7232cu,0x0u,0x11f910u,0x0u,0x7232cu,0x0u,0x11f900u,0x0u,0x7232cu,0x0u,0x11f970u,0x0u,0x7232cu,0x0u,0x11f960u,0x0u,0x7232cu,0x0u,0x11f950u,0x0u,0x7232cu,0x0u,0x11f940u,0x0u,0x7232cu,0x0u,0x11f9b0u,0x0u,0x72124u,0x0u,0x11f9a0u,0x0u,0x72124u,0x0u,0x11f990u,0x0u,0x72124u,0x0u,0x11f980u,0x0u,0x72124u,0x0u,0x11f9f0u,0x0u,0x72124u,0x0u,0x11f9e0u,0x0u,0x72124u,0x0u,0x11f9d0u,0x0u,0x72124u,0x0u,0x11f9c0u,0x0u,0x72124u,0x0u,0x11fa40u,0x0u,0x721a6u,0x0u,0x11fa50u,0x0u,0x721a6u,0x0u,0x11fa60u,0x0u,0x721a6u,0x0u,0x11fa70u,0x0u,0x721a6u,0x0u,0x11fa00u,0x0u,0x721a6u,0x0u,0x11fa10u,0x0u,0x721a6u,0x0u,0x11fa20u,0x0u,0x721a6u,0x0u,0x11fa30u,0x0u,0x721a6u,0x0u,0x11fac0u,0x0u,0x72186u,0x0u,0x11fad0u,0x0u,0x72186u,0x0u,0x11fae0u,0x0u,0x72186u,0x0u,0x11faf0u,0x0u,0x72186u,0x0u,0x11fa80u,0x0u,0x72186u,0x0u,0x11fa90u,0x0u,0x72186u,0x0u,0x11faa0u,0x0u,0x72186u,0x0u,0x11fab0u,0x0u,0x72186u,0x0u,0x11fb40u,0x0u,0x72186u,0x0u,0x11fb50u,0x0u,0x72186u,0x0u,0x11fb60u,0x0u,0x72186u,0x0u,0x11fb70u,0x0u,0x72186u,0x0u,0x11fb00u,0x0u,0x72186u,0x0u,0x11fb10u,0x0u,0x72186u,0x0u,0x11fb20u,0x0u,0x72186u,0x0u,0x11fb30u,0x0u,0x72186u,0x0u,0x11fbd0u,0x0u,0x7230cu,0x0u,0x11fbc0u,0x0u,0x7230cu,0x0u,0x11fbf0u,0x0u,0x7230cu,0x0u,0x11fbe0u,0x0u,0x7230cu,0x0u,0x11fb90u,0x0u,0x7230cu,0x0u,0x11fb80u,0x0u,0x7230cu,0x0u,0x11fbb0u,0x0u,0x7230cu,0x0u,0x11fba0u,0x0u,0x7230cu,0x5800u,0x5820u,0x4860u,0x4860u,0x5808u,0x5828u,0x4868u,0x4870u,0x5810u,0x5830u,0x4870u,0x5800u,0x5818u,0x5838u,0x4878u,0x5810u,0x5880u,0x58a0u,0x48e0u,0x48e0u,0x5888u,0x58a8u,0x48e8u,0x48f0u,0x5890u,0x58b0u,0x48f0u,0x5880u,0x5898u,0x58b8u,0x48f8u,0x5890u,0x5920u,0x5900u,0x4940u,0x4940u,0x5928u,0x5908u,0x4948u,0x4950u,0x5930u,0x5910u,0x4950u,0x5920u,0x5938u,0x5918u,0x4958u,0x5930u,0x59a0u,0x5980u,0x49c0u,0x49c0u,0x59a8u,0x5988u,0x49c8u,0x49d0u,0x59b0u,0x5990u,0x49d0u,0x59a0u,0x59b8u,0x5998u,0x49d8u,0x59b0u,0x5c40u,0x5c60u,0x4c20u,0x4a60u,0x5c48u,0x5c68u,0x4c28u,0x4a70u,0x5c50u,0x5c70u,0x4c30u,0x5a00u,0x5c58u,0x5c78u,0x4c38u,0x5a10u,0x5cc0u,0x5ce0u,0x4ca0u,0x4ae0u,0x5cc8u,0x5ce8u,0x4ca8u,0x4af0u,0x5cd0u,0x5cf0u,0x4cb0u,0x5a80u,0x5cd8u,0x5cf8u,0x4cb8u,0x5a90u,0x5d60u,0x5d40u,0x4d00u,0x4b40u,0x5d68u,0x5d48u,0x4d08u,0x4b50u,0x5d70u,0x5d50u,0x4d10u,0x5b20u,0x5d78u,0x5d58u,0x4d18u,0x5b30u,0x5de0u,0x5dc0u,0x4d80u,0x4bc0u,0x5de8u,0x5dc8u,0x4d88u,0x4bd0u,0x5df0u,0x5dd0u,0x4d90u,0x5ba0u,0x5df8u,0x5dd8u,0x4d98u,0x5bb0u,0x6000u,0x6020u,0x5060u,0x4c20u,0x6008u,0x6028u,0x5068u,0x4c30u,0x6010u,0x6030u,0x5070u,0x5c40u,0x6018u,0x6038u,0x5078u,0x5c50u,0x6080u,0x60a0u,0x50e0u,0x4ca0u,0x6088u,0x60a8u,0x50e8u,0x4cb0u,0x6090u,0x60b0u,0x50f0u,0x5cc0u,0x6098u,0x60b8u,0x50f8u,0x5cd0u,0x6120u,0x6100u,0x5140u,0x4d00u,0x6128u,0x6108u,0x5148u,0x4d10u,0x6130u,0x6110u,0x5150u,0x5d60u,0x6138u,0x6118u,0x5158u,0x5d70u,0x61a0u,0x6180u,0x51c0u,0x4d80u,0x61a8u,0x6188u,0x51c8u,0x4d90u,0x61b0u,0x6190u,0x51d0u,0x5de0u,0x61b8u,0x6198u,0x51d8u,0x5df0u,0x6440u,0x6460u,0x5420u,0x4e20u,0x6448u,0x6468u,0x5428u,0x4e30u,0x6450u,0x6470u,0x5430u,0x5e40u,0x6458u,0x6478u,0x5438u,0x5e50u,0x64c0u,0x64e0u,0x54a0u,0x4ea0u,0x64c8u,0x64e8u,0x54a8u,0x4eb0u,0x64d0u,0x64f0u,0x54b0u,0x5ec0u,0x64d8u,0x64f8u,0x54b8u,0x5ed0u,0x6560u,0x6540u,0x5500u,0x4f00u,0x6568u,0x6548u,0x5508u,0x4f10u,0x6570u,0x6550u,0x5510u,0x5f60u,0x6578u,0x6558u,0x5518u,0x5f70u,0x65e0u,0x65c0u,0x5580u,0x4f80u,0x65e8u,0x65c8u,0x5588u,0x4f90u,0x65f0u,0x65d0u,0x5590u,0x5fe0u,0x65f8u,0x65d8u,0x5598u,0x5ff0u,0x5820u,0x1e8c0u,0x1eb60u,0xc40u,0x5828u,0x1ed50u,0x1eff0u,0xc50u,0x5830u,0x1f1e0u,0x1f480u,0xc60u,0x5838u,0x1f670u,0x1f910u,0xc70u,0x58a0u,0x1fb00u,0x1fda0u,0xc00u,0x58a8u,0x1ff90u,0x20230u,0xc10u,0x58b0u,0x20420u,0x206c0u,0xc20u,0x58b8u,0x208b0u,0x20b50u,0xc30u,0x5900u,0x20d40u,0x20fe0u,0xdc0u,0x5908u,0x211d0u,0x21470u,0xdd0u,0x5910u,0x21660u,0x21900u,0xde0u,0x5918u,0x21af0u,0x21d90u,0xdf0u,0x5980u,0x21f80u,0x22220u,0xd80u,0x5988u,0x22410u,0x226b0u,0xd90u,0x5990u,0x228a0u,0x22b40u,0xda0u,0x5998u,0x22d30u,0x22fd0u,0xdb0u,0x5a20u,0x1e8f0u,0x1eb68u,0xf60u,0x5a28u,0x1ed80u,0x1eff8u,0xf70u,0x5a30u,0x1f210u,0x1f488u,0xf40u,0x5a38u,0x1f6a0u,0x1f918u,0xf50u,0x5aa0u,0x1fb30u,0x1fda8u,0xf20u,0x5aa8u,0x1ffc0u,0x20238u,0xf30u,0x5ab0u,0x20450u,0x206c8u,0xf00u,0x5ab8u,0x208e0u,0x20b58u,0xf10u,0x5b00u,0x20d70u,0x20fe8u,0x10e0u,0x5b08u,0x21200u,0x21478u,0x10f0u,0x5b10u,0x21690u,0x21908u,0x10c0u,0x5b18u,0x21b20u,0x21d98u,0x10d0u,0x5b80u,0x21fb0u,0x22228u,0x10a0u,0x5b88u,0x22440u,0x226b8u,0x10b0u,0x5b90u,0x228d0u,0x22b48u,0x1080u,0x5b98u,0x22d60u,0x22fd8u,0x1090u,0x5c60u,0x1e920u,0x1eb70u,0x1240u,0x5c68u,0x1edb0u,0x1f000u,0x1250u,0x5c70u,0x1f240u,0x1f490u,0x1260u,0x5c78u,0x1f6d0u,0x1f920u,0x1270u,0x5ce0u,0x1fb60u,0x1fdb0u,0x1200u,0x5ce8u,0x1fff0u,0x20240u,0x1210u,0x5cf0u,0x20480u,0x206d0u,0x1220u,0x5cf8u,0x20910u,0x20b60u,0x1230u,0x5d40u,0x20da0u,0x20ff0u,0x13c0u,0x5d48u,0x21230u,0x21480u,0x13d0u,0x5d50u,0x216c0u,0x21910u,0x13e0u,0x5d58u,0x21b50u,0x21da0u,0x13f0u,0x5dc0u,0x21fe0u,0x22230u,0x1380u,0x5dc8u,0x22470u,0x226c0u,0x1390u,0x5dd0u,0x22900u,0x22b50u,0x13a0u,0x5dd8u,0x22d90u,0x22fe0u,0x13b0u,0x5e60u,0x1e950u,0x1eb78u,0x1560u,0x5e68u,0x1ede0u,0x1f008u,0x1570u,0x5e70u,0x1f270u,0x1f498u,0x1540u,0x5e78u,0x1f700u,0x1f928u,0x1550u,0x5ee0u,0x1fb90u,0x1fdb8u,0x1520u,0x5ee8u,0x20020u,0x20248u,0x1530u,0x5ef0u,0x204b0u,0x206d8u,0x1500u,0x5ef8u,0x20940u,0x20b68u,0x1510u,0x5f40u,0x20dd0u,0x20ff8u,0x16e0u,0x5f48u,0x21260u,0x21488u,0x16f0u,0x5f50u,0x216f0u,0x21918u,0x16c0u,0x5f58u,0x21b80u,0x21da8u,0x16d0u,0x5fc0u,0x22010u,0x22238u,0x16a0u,0x5fc8u,0x224a0u,0x226c8u,0x16b0u,0x5fd0u,0x22930u,0x22b58u,0x1680u,0x5fd8u,0x22dc0u,0x22fe8u,0x1690u,0x0u,0x40u,0x9000u,0xa800u,0x80u,0xc0u,0x9010u,0xa810u,0x110u,0x150u,0x9020u,0xa820u,0x190u,0x1d0u,0x9030u,0xa830u,0x220u,0x260u,0x9040u,0xa840u,0x2a0u,0x2e0u,0x9050u,0xa850u,0x330u,0x370u,0x9060u,0xa860u,0x3b0u,0x3f0u,0x9070u,0xa870u,0x440u,0x400u,0x9080u,0xa880u,0x4c0u,0x480u,0x9090u,0xa890u,0x550u,0x510u,0x90a0u,0xa8a0u,0x5d0u,0x590u,0x90b0u,0xa8b0u,0x660u,0x620u,0x90c0u,0xa8c0u,0x6e0u,0x6a0u,0x90d0u,0xa8d0u,0x770u,0x730u,0x90e0u,0xa8e0u,0x7f0u,0x7b0u,0x90f0u,0xa8f0u,0x10u,0x50u,0x9300u,0xab00u,0x90u,0xd0u,0x9310u,0xab10u,0x100u,0x140u,0x9320u,0xab20u,0x180u,0x1c0u,0x9330u,0xab30u,0x230u,0x270u,0x9340u,0xab40u,0x2b0u,0x2f0u,0x9350u,0xab50u,0x320u,0x360u,0x9360u,0xab60u,0x3a0u,0x3e0u,0x9370u,0xab70u,0x450u,0x410u,0x9380u,0xab80u,0x4d0u,0x490u,0x9390u,0xab90u,0x540u,0x500u,0x93a0u,0xaba0u,0x5c0u,0x580u,0x93b0u,0xabb0u,0x670u,0x630u,0x93c0u,0xabc0u,0x6f0u,0x6b0u,0x93d0u,0xabd0u,0x760u,0x720u,0x93e0u,0xabe0u,0x7e0u,0x7a0u,0x93f0u,0xabf0u,0x20u,0x60u,0x9600u,0xae00u,0xa0u,0xe0u,0x9610u,0xae10u,0x130u,0x170u,0x9620u,0xae20u,0x1b0u,0x1f0u,0x9630u,0xae30u,0x200u,0x240u,0x9640u,0xae40u,0x280u,0x2c0u,0x9650u,0xae50u,0x310u,0x350u,0x9660u,0xae60u,0x390u,0x3d0u,0x9670u,0xae70u,0x460u,0x420u,0x9680u,0xae80u,0x4e0u,0x4a0u,0x9690u,0xae90u,0x570u,0x530u,0x96a0u,0xaea0u,0x5f0u,0x5b0u,0x96b0u,0xaeb0u,0x640u,0x600u,0x96c0u,0xaec0u,0x6c0u,0x680u,0x96d0u,0xaed0u,0x750u,0x710u,0x96e0u,0xaee0u,0x7d0u,0x790u,0x96f0u,0xaef0u,0x30u,0x70u,0x9900u,0xb100u,0xb0u,0xf0u,0x9910u,0xb110u,0x120u,0x160u,0x9920u,0xb120u,0x1a0u,0x1e0u,0x9930u,0xb130u,0x210u,0x250u,0x9940u,0xb140u,0x290u,0x2d0u,0x9950u,0xb150u,0x300u,0x340u,0x9960u,0xb160u,0x380u,0x3c0u,0x9970u,0xb170u,0x470u,0x430u,0x9980u,0xb180u,0x4f0u,0x4b0u,0x9990u,0xb190u,0x560u,0x520u,0x99a0u,0xb1a0u,0x5e0u,0x5a0u,0x99b0u,0xb1b0u,0x650u,0x610u,0x99c0u,0xb1c0u,0x6d0u,0x690u,0x99d0u,0xb1d0u,0x740u,0x700u,0x99e0u,0xb1e0u,0x7c0u,0x780u,0x99f0u,0xb1f0u,0x9c08u,0x12060u,0x120a0u,0x12400u,0x9c18u,0x121f0u,0x12230u,0x12410u,0x9c28u,0x12340u,0x12380u,0x12420u,0x9c38u,0x124d0u,0x12510u,0x12430u,0x9c48u,0x12620u,0x126e0u,0x12440u,0x9c58u,0x127b0u,0x12870u,0x12450u,0x9c68u,0x12900u,0x129c0u,0x12460u,0x9c78u,0x12a90u,0x12b50u,0x12470u,0x9c88u,0x12c60u,0x12ca0u,0x12480u,0x9c98u,0x12df0u,0x12e30u,0x12490u,0x9ca8u,0x12f40u,0x12f80u,0x124a0u,0x9cb8u,0x130d0u,0x13110u,0x124b0u,0x9cc8u,0x13220u,0x132e0u,0x124c0u,0x9cd8u,0x133b0u,0x13470u,0x124d0u,0x9ce8u,0x13500u,0x135c0u,0x124e0u,0x9cf8u,0x13690u,0x13750u,0x124f0u,0x9f08u,0x12070u,0x120a8u,0x12500u,0x9f18u,0x121e0u,0x12238u,0x12510u,0x9f28u,0x12350u,0x12388u,0x12520u,0x9f38u,0x124c0u,0x12518u,0x12530u,0x9f48u,0x12630u,0x126e8u,0x12540u,0x9f58u,0x127a0u,0x12878u,0x12550u,0x9f68u,0x12910u,0x129c8u,0x12560u,0x9f78u,0x12a80u,0x12b58u,0x12570u,0x9f88u,0x12c70u,0x12ca8u,0x12580u,0x9f98u,0x12de0u,0x12e38u,0x12590u,0x9fa8u,0x12f50u,0x12f88u,0x125a0u,0x9fb8u,0x130c0u,0x13118u,0x125b0u,0x9fc8u,0x13230u,0x132e8u,0x125c0u,0x9fd8u,0x133a0u,0x13478u,0x125d0u,0x9fe8u,0x13510u,0x135c8u,0x125e0u,0x9ff8u,0x13680u,0x13758u,0x125f0u,0xa208u,0x12080u,0x120b0u,0x12600u,0xa218u,0x12210u,0x12220u,0x12610u,0xa228u,0x123a0u,0x12390u,0x12620u,0xa238u,0x12530u,0x12500u,0x12630u,0xa248u,0x126c0u,0x126f0u,0x12640u,0xa258u,0x12850u,0x12860u,0x12650u,0xa268u,0x129e0u,0x129d0u,0x12660u,0xa278u,0x12b70u,0x12b40u,0x12670u,0xa288u,0x12c80u,0x12cb0u,0x12680u,0xa298u,0x12e10u,0x12e20u,0x12690u,0xa2a8u,0x12fa0u,0x12f90u,0x126a0u,0xa2b8u,0x13130u,0x13100u,0x126b0u,0xa2c8u,0x132c0u,0x132f0u,0x126c0u,0xa2d8u,0x13450u,0x13460u,0x126d0u,0xa2e8u,0x135e0u,0x135d0u,0x126e0u,0xa2f8u,0x13770u,0x13740u,0x126f0u,0xa508u,0x12090u,0x120b8u,0x12700u,0xa518u,0x12200u,0x12228u,0x12710u,0xa528u,0x123b0u,0x12398u,0x12720u,0xa538u,0x12520u,0x12508u,0x12730u,0xa548u,0x126d0u,0x126f8u,0x12740u,0xa558u,0x12840u,0x12868u,0x12750u,0xa568u,0x129f0u,0x129d8u,0x12760u,0xa578u,0x12b60u,0x12b48u,0x12770u,0xa588u,0x12c90u,0x12cb8u,0x12780u,0xa598u,0x12e00u,0x12e28u,0x12790u,0xa5a8u,0x12fb0u,0x12f98u,0x127a0u,0xa5b8u,0x13120u,0x13108u,0x127b0u,0xa5c8u,0x132d0u,0x132f8u,0x127c0u,0xa5d8u,0x13440u,0x13468u,0x127d0u,0xa5e8u,0x135f0u,0x135d8u,0x127e0u,0xa5f8u,0x13760u,0x13748u,0x127f0u,0x5000u,0x1c300u,0x1e8c0u,0x1eb60u,0x5080u,0x1c310u,0x100000u,0x1eb62u,0x5110u,0x1c320u,0x100000u,0x1eb64u,0x5190u,0x1c330u,0x100000u,0x1eb66u,0x5220u,0x1c340u,0x1e8c0u,0x1eb68u,0x52a0u,0x1c350u,0x100000u,0x1eb6au,0x5330u,0x1c360u,0x100000u,0x1eb6cu,0x53b0u,0x1c370u,0x100000u,0x1eb6eu,0x5440u,0x1c380u,0x1e8c0u,0x1eb70u,0x54c0u,0x1c390u,0x100000u,0x1eb72u,0x5550u,0x1c3a0u,0x100000u,0x1eb74u,0x55d0u,0x1c3b0u,0x100000u,0x1eb76u,0x5660u,0x1c3c0u,0x1e8c0u,0x1eb78u,0x56e0u,0x1c3d0u,0x100000u,0x1eb7au,0x5770u,0x1c3e0u,0x100000u,0x1eb7cu,0x57f0u,0x1c3f0u,0x100000u,0x1eb7eu,0x5010u,0x1c400u,0x1e8f0u,0x1eb60u,0x5090u,0x1c410u,0x100000u,0x1eb62u,0x5100u,0x1c420u,0x100000u,0x1eb64u,0x5180u,0x1c430u,0x100000u,0x1eb66u,0x5230u,0x1c440u,0x1e8f0u,0x1eb68u,0x52b0u,0x1c450u,0x100000u,0x1eb6au,0x5320u,0x1c460u,0x100000u,0x1eb6cu,0x53a0u,0x1c470u,0x100000u,0x1eb6eu,0x5450u,0x1c480u,0x1e8f0u,0x1eb70u,0x54d0u,0x1c490u,0x100000u,0x1eb72u,0x5540u,0x1c4a0u,0x100000u,0x1eb74u,0x55c0u,0x1c4b0u,0x100000u,0x1eb76u,0x5670u,0x1c4c0u,0x1e8f0u,0x1eb78u,0x56f0u,0x1c4d0u,0x100000u,0x1eb7au,0x5760u,0x1c4e0u,0x100000u,0x1eb7cu,0x57e0u,0x1c4f0u,0x100000u,0x1eb7eu,0x5020u,0x1c500u,0x1e920u,0x1eb60u,0x50a0u,0x1c510u,0x100000u,0x1eb62u,0x5130u,0x1c520u,0x100000u,0x1eb64u,0x51b0u,0x1c530u,0x100000u,0x1eb66u,0x5200u,0x1c540u,0x1e920u,0x1eb68u,0x5280u,0x1c550u,0x100000u,0x1eb6au,0x5310u,0x1c560u,0x100000u,0x1eb6cu,0x5390u,0x1c570u,0x100000u,0x1eb6eu,0x5460u,0x1c580u,0x1e920u,0x1eb70u,0x54e0u,0x1c590u,0x100000u,0x1eb72u,0x5570u,0x1c5a0u,0x100000u,0x1eb74u,0x55f0u,0x1c5b0u,0x100000u,0x1eb76u,0x5640u,0x1c5c0u,0x1e920u,0x1eb78u,0x56c0u,0x1c5d0u,0x100000u,0x1eb7au,0x5750u,0x1c5e0u,0x100000u,0x1eb7cu,0x57d0u,0x1c5f0u,0x100000u,0x1eb7eu,0x5030u,0x1c600u,0x1e950u,0x1eb60u,0x50b0u,0x1c610u,0x100000u,0x1eb62u,0x5120u,0x1c620u,0x100000u,0x1eb64u,0x51a0u,0x1c630u,0x100000u,0x1eb66u,0x5210u,0x1c640u,0x1e950u,0x1eb68u,0x5290u,0x1c650u,0x100000u,0x1eb6au,0x5300u,0x1c660u,0x100000u,0x1eb6cu,0x5380u,0x1c670u,0x100000u,0x1eb6eu,0x5470u,0x1c680u,0x1e950u,0x1eb70u,0x54f0u,0x1c690u,0x100000u,0x1eb72u,0x5560u,0x1c6a0u,0x100000u,0x1eb74u,0x55e0u,0x1c6b0u,0x100000u,0x1eb76u,0x5650u,0x1c6c0u,0x1e950u,0x1eb78u,0x56d0u,0x1c6d0u,0x100000u,0x1eb7au,0x5740u,0x1c6e0u,0x100000u,0x1eb7cu,0x57c0u,0x1c6f0u,0x100000u,0x1eb7eu,0x25d00u,0x1d380u,0x1d380u,0xf0140u,0x25d02u,0x1d382u,0x100000u,0xf0150u,0x25d04u,0x1d384u,0x100000u,0xf0160u,0x25d06u,0x1d386u,0x100000u,0xf0170u,0x25f00u,0x1d390u,0x1d380u,0xf0100u,0x25f02u,0x1d392u,0x100000u,0xf0110u,0x25f04u,0x1d394u,0x100000u,0xf0120u,0x25f06u,0x1d396u,0x100000u,0xf0130u,0x26100u,0x1d3a0u,0x1d380u,0xf02c0u,0x26102u,0x1d3a2u,0x100000u,0xf02d0u,0x26104u,0x1d3a4u,0x100000u,0xf02e0u,0x26106u,0x1d3a6u,0x100000u,0xf02f0u,0x26300u,0x1d3b0u,0x1d380u,0xf0280u,0x26302u,0x1d3b2u,0x100000u,0xf0290u,0x26304u,0x1d3b4u,0x100000u,0xf02a0u,0x26306u,0x1d3b6u,0x100000u,0xf02b0u,0x25d08u,0x1d388u,0x1d390u,0x11f850u,0x25d0au,0x1d38au,0x100000u,0x11f840u,0x25d0cu,0x1d38cu,0x100000u,0x11f870u,0x25d0eu,0x1d38eu,0x100000u,0x11f860u,0x25f08u,0x1d398u,0x1d390u,0x11f810u,0x25f0au,0x1d39au,0x100000u,0x11f800u,0x25f0cu,0x1d39cu,0x100000u,0x11f830u,0x25f0eu,0x1d39eu,0x100000u,0x11f820u,0x26108u,0x1d3a8u,0x1d390u,0x11f9d0u,0x2610au,0x1d3aau,0x100000u,0x11f9c0u,0x2610cu,0x1d3acu,0x100000u,0x11f9f0u,0x2610eu,0x1d3aeu,0x100000u,0x11f9e0u,0x26308u,0x1d3b8u,0x1d390u,0x11f990u,0x2630au,0x1d3bau,0x100000u,0x11f980u,0x2630cu,0x1d3bcu,0x100000u,0x11f9b0u,0x2630eu,0x1d3beu,0x100000u,0x11f9a0u,0x25508u,0x1d3c0u,0x1d3a0u,0x11fb60u,0x2550au,0x1d3c2u,0x100000u,0x11fb70u,0x2550cu,0x1d3c4u,0x100000u,0x11fb40u,0x2550eu,0x1d3c6u,0x100000u,0x11fb50u,0x25708u,0x1d3d0u,0x1d3a0u,0x11fb20u,0x2570au,0x1d3d2u,0x100000u,0x11fb30u,0x2570cu,0x1d3d4u,0x100000u,0x11fb00u,0x2570eu,0x1d3d6u,0x100000u,0x11fb10u,0x25908u,0x1d3e0u,0x1d3a0u,0x11fce0u,0x2590au,0x1d3e2u,0x100000u,0x11fcf0u,0x2590cu,0x1d3e4u,0x100000u,0x11fcc0u,0x2590eu,0x1d3e6u,0x100000u,0x11fcd0u,0x25b08u,0x1d3f0u,0x1d3a0u,0x11fca0u,0x25b0au,0x1d3f2u,0x100000u,0x11fcb0u,0x25b0cu,0x1d3f4u,0x100000u,0x11fc80u,0x25b0eu,0x1d3f6u,0x100000u,0x11fc90u,0x100000u,0x1d3c8u,0x1d3b0u,0x11fe70u,0x100000u,0x1d3cau,0x100000u,0x11fe60u,0x100000u,0x1d3ccu,0x100000u,0x11fe50u,0x100000u,0x1d3ceu,0x100000u,0x11fe40u,0x100000u,0x1d3d8u,0x1d3b0u,0x11fe30u,0x100000u,0x1d3dau,0x100000u,0x11fe20u,0x100000u,0x1d3dcu,0x100000u,0x11fe10u,0x100000u,0x1d3deu,0x100000u,0x11fe00u,0x100000u,0x1d3e8u,0x1d3b0u,0x11fff0u,0x100000u,0x1d3eau,0x100000u,0x11ffe0u,0x100000u,0x1d3ecu,0x100000u,0x11ffd0u,0x100000u,0x1d3eeu,0x100000u,0x11ffc0u,0x100000u,0x1d3f8u,0x1d3b0u,0x11ffb0u,0x100000u,0x1d3fau,0x100000u,0x11ffa0u,0x100000u,0x1d3fcu,0x100000u,0x11ff90u,0x100000u,0x1d3feu,0x100000u,0x11ff80u,0x420u,0x30850u,0x90000u,0xc0420u,0x430u,0x30840u,0x90010u,0xc0430u,0x400u,0x30870u,0x90020u,0xc0400u,0x410u,0x30860u,0x90030u,0xc0410u,0x460u,0x30810u,0x90040u,0xc0460u,0x470u,0x30800u,0x90050u,0xc0470u,0x440u,0x30830u,0x90060u,0xc0440u,0x450u,0x30820u,0x90070u,0xc0450u,0x4b0u,0x308d0u,0x90080u,0xef8b0u,0x4a0u,0x308c0u,0x90090u,0xef8a0u,0x490u,0x308f0u,0x900a0u,0xef890u,0x480u,0x308e0u,0x900b0u,0xef880u,0x4f0u,0x30890u,0x900c0u,0xef8f0u,0x4e0u,0x30880u,0x900d0u,0xef8e0u,0x4d0u,0x308b0u,0x900e0u,0xef8d0u,0x4c0u,0x308a0u,0x900f0u,0xef8c0u,0x530u,0x5fd60u,0x90100u,0xef930u,0x520u,0x5fd70u,0x90110u,0xef920u,0x510u,0x5fd40u,0x90120u,0xef910u,0x500u,0x5fd50u,0x90130u,0xef900u,0x570u,0x5fd20u,0x90140u,0xef970u,0x560u,0x5fd30u,0x90150u,0xef960u,0x550u,0x5fd00u,0x90160u,0xef950u,0x540u,0x5fd10u,0x90170u,0xef940u,0x5b0u,0x5fde0u,0x90190u,0xef9b0u,0x5a0u,0x5fdf0u,0x90180u,0xef9a0u,0x590u,0x5fdc0u,0x901b0u,0xef990u,0x580u,0x5fdd0u,0x901a0u,0xef980u,0x5f0u,0x5fda0u,0x901d0u,0xef9f0u,0x5e0u,0x5fdb0u,0x901c0u,0xef9e0u,0x5d0u,0x5fd80u,0x901f0u,0xef9d0u,0x5c0u,0x5fd90u,0x901e0u,0xef9c0u,0x640u,0x5fe60u,0x90210u,0xefa40u,0x650u,0x5fe70u,0x90200u,0xefa50u,0x660u,0x5fe40u,0x90230u,0xefa60u,0x670u,0x5fe50u,0x90220u,0xefa70u,0x600u,0x5fe20u,0x90250u,0xefa00u,0x610u,0x5fe30u,0x90240u,0xefa10u,0x620u,0x5fe00u,0x90270u,0xefa20u,0x630u,0x5fe10u,0x90260u,0xefa30u,0x6c0u,0x5fef0u,0x90290u,0xefac0u,0x6d0u,0x5fee0u,0x90280u,0xefad0u,0x6e0u,0x5fed0u,0x902b0u,0xefae0u,0x6f0u,0x5fec0u,0x902a0u,0xefaf0u,0x680u,0x5feb0u,0x902d0u,0xefa80u,0x690u,0x5fea0u,0x902c0u,0xefa90u,0x6a0u,0x5fe90u,0x902f0u,0xefaa0u,0x6b0u,0x5fe80u,0x902e0u,0xefab0u,0x740u,0x5ff70u,0x90320u,0xefb40u,0x750u,0x5ff60u,0x90330u,0xefb50u,0x760u,0x5ff50u,0x90300u,0xefb60u,0x770u,0x5ff40u,0x90310u,0xefb70u,0x700u,0x5ff30u,0x90360u,0xefb00u,0x710u,0x5ff20u,0x90370u,0xefb10u,0x720u,0x5ff10u,0x90340u,0xefb20u,0x730u,0x5ff00u,0x90350u,0xefb30u,0x7d0u,0x5fff0u,0x903a0u,0xefbd0u,0x7c0u,0x5ffe0u,0x903b0u,0xefbc0u,0x7f0u,0x5ffd0u,0x90380u,0xefbf0u,0x7e0u,0x5ffc0u,0x90390u,0xefbe0u,0x790u,0x5ffb0u,0x903e0u,0xefb90u,0x780u,0x5ffa0u,0x903f0u,0xefb80u,0x7b0u,0x5ff90u,0x903c0u,0xefbb0u,0x7a0u,0x5ff80u,0x903d0u,0xefba0u,0x0u,0x11fc50u,0x0u,0x7230cu,0x0u,0x11fc40u,0x0u,0x7230cu,0x0u,0x11fc70u,0x0u,0x7230cu,0x0u,0x11fc60u,0x0u,0x7230cu,0x0u,0x11fc10u,0x0u,0x7230cu,0x0u,0x11fc00u,0x0u,0x7230cu,0x0u,0x11fc30u,0x0u,0x7230cu,0x0u,0x11fc20u,0x0u,0x7230cu,0x0u,0x11fcd0u,0x0u,0x5232cu,0x0u,0x11fcc0u,0x0u,0x5232cu,0x0u,0x11fcf0u,0x0u,0x5232cu,0x0u,0x11fce0u,0x0u,0x5232cu,0x0u,0x11fc90u,0x0u,0x5232cu,0x0u,0x11fc80u,0x0u,0x5232cu,0x0u,0x11fcb0u,0x0u,0x5232cu,0x0u,0x11fca0u,0x0u,0x5232cu,0x0u,0x11fd60u,0x0u,0xd0124u,0x0u,0x11fd70u,0x0u,0xd0124u,0x0u,0x11fd40u,0x0u,0xd0124u,0x0u,0x11fd50u,0x0u,0xd0124u,0x0u,0x11fd20u,0x0u,0xd0124u,0x0u,0x11fd30u,0x0u,0xd0124u,0x0u,0x11fd00u,0x0u,0xd0124u,0x0u,0x11fd10u,0x0u,0xd0124u,0x0u,0x11fde0u,0x0u,0xd81a6u,0x0u,0x11fdf0u,0x0u,0xd81a6u,0x0u,0x11fdc0u,0x0u,0xd81a6u,0x0u,0x11fdd0u,0x0u,0xd81a6u,0x0u,0x11fda0u,0x0u,0xd81a6u,0x0u,0x11fdb0u,0x0u,0xd81a6u,0x0u,0x11fd80u,0x0u,0xd81a6u,0x0u,0x11fd90u,0x0u,0xd81a6u,0x0u,0x11fe60u,0x0u,0xd8996u,0x0u,0x11fe70u,0x0u,0xd8996u,0x0u,0x11fe40u,0x0u,0xd8996u,0x0u,0x11fe50u,0x0u,0xd8996u,0x0u,0x11fe20u,0x0u,0xd8996u,0x0u,0x11fe30u,0x0u,0xd8996u,0x0u,0x11fe00u,0x0u,0xd8996u,0x0u,0x11fe10u,0x0u,0xd8996u,0x0u,0x11fef0u,0x0u,0xd8996u,0x0u,0x11fee0u,0x0u,0xd8996u,0x0u,0x11fed0u,0x0u,0xd8996u,0x0u,0x11fec0u,0x0u,0xd8996u,0x0u,0x11feb0u,0x0u,0xd8996u,0x0u,0x11fea0u,0x0u,0xd8996u,0x0u,0x11fe90u,0x0u,0xd8996u,0x0u,0x11fe80u,0x0u,0xd8996u,0x0u,0x11ff70u,0x0u,0xd8b1cu,0x0u,0x11ff60u,0x0u,0xd8b1cu,0x0u,0x11ff50u,0x0u,0xd8b1cu,0x0u,0x11ff40u,0x0u,0xd8b1cu,0x0u,0x11ff30u,0x0u,0xd8b1cu,0x0u,0x11ff20u,0x0u,0xd8b1cu,0x0u,0x11ff10u,0x0u,0xd8b1cu,0x0u,0x11ff00u,0x0u,0xd8b1cu,0x0u,0x11fff0u,0x0u,0xd8b1cu,0x0u,0x11ffe0u,0x0u,0xd8b1cu,0x0u,0x11ffd0u,0x0u,0xd8b1cu,0x0u,0x11ffc0u,0x0u,0xd8b1cu,0x0u,0x11ffb0u,0x0u,0xd8b1cu,0x0u,0x11ffa0u,0x0u,0xd8b1cu,0x0u,0x11ff90u,0x0u,0xd8b1cu,0x0u,0x11ff80u,0x0u,0xd8b1cu,0x5840u,0x5860u,0x6800u,0x5840u,0x5848u,0x5868u,0x6808u,0x5850u,0x5850u,0x5870u,0x6810u,0x5860u,0x5858u,0x5878u,0x6818u,0x5870u,0x58c0u,0x58e0u,0x6880u,0x58c0u,0x58c8u,0x58e8u,0x6888u,0x58d0u,0x58d0u,0x58f0u,0x6890u,0x58e0u,0x58d8u,0x58f8u,0x6898u,0x58f0u,0x5960u,0x5940u,0x6920u,0x5960u,0x5968u,0x5948u,0x6928u,0x5970u,0x5970u,0x5950u,0x6930u,0x5940u,0x5978u,0x5958u,0x6938u,0x5950u,0x59e0u,0x59c0u,0x69a0u,0x59e0u,0x59e8u,0x59c8u,0x69a8u,0x59f0u,0x59f0u,0x59d0u,0x69b0u,0x59c0u,0x59f8u,0x59d8u,0x69b8u,0x59d0u,0x5c00u,0x5c20u,0x6c40u,0x5a40u,0x5c08u,0x5c28u,0x6c48u,0x5a50u,0x5c10u,0x5c30u,0x6c50u,0x5a60u,0x5c18u,0x5c38u,0x6c58u,0x5a70u,0x5c80u,0x5ca0u,0x6cc0u,0x5ac0u,0x5c88u,0x5ca8u,0x6cc8u,0x5ad0u,0x5c90u,0x5cb0u,0x6cd0u,0x5ae0u,0x5c98u,0x5cb8u,0x6cd8u,0x5af0u,0x5d20u,0x5d00u,0x6d60u,0x5b60u,0x5d28u,0x5d08u,0x6d68u,0x5b70u,0x5d30u,0x5d10u,0x6d70u,0x5b40u,0x5d38u,0x5d18u,0x6d78u,0x5b50u,0x5da0u,0x5d80u,0x6de0u,0x5be0u,0x5da8u,0x5d88u,0x6de8u,0x5bf0u,0x5db0u,0x5d90u,0x6df0u,0x5bc0u,0x5db8u,0x5d98u,0x6df8u,0x5bd0u,0x6040u,0x6060u,0x7000u,0x5c00u,0x6048u,0x6068u,0x7008u,0x5c10u,0x6050u,0x6070u,0x7010u,0x5c20u,0x6058u,0x6078u,0x7018u,0x5c30u,0x60c0u,0x60e0u,0x7080u,0x5c80u,0x60c8u,0x60e8u,0x7088u,0x5c90u,0x60d0u,0x60f0u,0x7090u,0x5ca0u,0x60d8u,0x60f8u,0x7098u,0x5cb0u,0x6160u,0x6140u,0x7120u,0x5d20u,0x6168u,0x6148u,0x7128u,0x5d30u,0x6170u,0x6150u,0x7130u,0x5d00u,0x6178u,0x6158u,0x7138u,0x5d10u,0x61e0u,0x61c0u,0x71a0u,0x5da0u,0x61e8u,0x61c8u,0x71a8u,0x5db0u,0x61f0u,0x61d0u,0x71b0u,0x5d80u,0x61f8u,0x61d8u,0x71b8u,0x5d90u,0x6400u,0x6420u,0x7440u,0x5e00u,0x6408u,0x6428u,0x7448u,0x5e10u,0x6410u,0x6430u,0x7450u,0x5e20u,0x6418u,0x6438u,0x7458u,0x5e30u,0x6480u,0x64a0u,0x74c0u,0x5e80u,0x6488u,0x64a8u,0x74c8u,0x5e90u,0x6490u,0x64b0u,0x74d0u,0x5ea0u,0x6498u,0x64b8u,0x74d8u,0x5eb0u,0x6520u,0x6500u,0x7560u,0x5f20u,0x6528u,0x6508u,0x7568u,0x5f30u,0x6530u,0x6510u,0x7570u,0x5f00u,0x6538u,0x6518u,0x7578u,0x5f10u,0x65a0u,0x6580u,0x75e0u,0x5fa0u,0x65a8u,0x6588u,0x75e8u,0x5fb0u,0x65b0u,0x6590u,0x75f0u,0x5f80u,0x65b8u,0x6598u,0x75f8u,0x5f90u,0x6800u,0x1e980u,0x1ebc0u,0x1800u,0x6808u,0x1ee10u,0x1f050u,0x1810u,0x6810u,0x1f2a0u,0x1f4e0u,0x1820u,0x6818u,0x1f730u,0x1f970u,0x1830u,0x6880u,0x1fbc0u,0x1fe00u,0x1840u,0x6888u,0x20050u,0x20290u,0x1850u,0x6890u,0x204e0u,0x20720u,0x1860u,0x6898u,0x20970u,0x20bb0u,0x1870u,0x6920u,0x20e00u,0x21040u,0x1980u,0x6928u,0x21290u,0x214d0u,0x1990u,0x6930u,0x21720u,0x21960u,0x19a0u,0x6938u,0x21bb0u,0x21df0u,0x19b0u,0x69a0u,0x22040u,0x22280u,0x19c0u,0x69a8u,0x224d0u,0x22710u,0x19d0u,0x69b0u,0x22960u,0x22ba0u,0x19e0u,0x69b8u,0x22df0u,0x23030u,0x19f0u,0x6a00u,0x1e9b0u,0x1ebc8u,0x1b20u,0x6a08u,0x1ee40u,0x1f058u,0x1b30u,0x6a10u,0x1f2d0u,0x1f4e8u,0x1b00u,0x6a18u,0x1f760u,0x1f978u,0x1b10u,0x6a80u,0x1fbf0u,0x1fe08u,0x1b60u,0x6a88u,0x20080u,0x20298u,0x1b70u,0x6a90u,0x20510u,0x20728u,0x1b40u,0x6a98u,0x209a0u,0x20bb8u,0x1b50u,0x6b20u,0x20e30u,0x21048u,0x1ca0u,0x6b28u,0x212c0u,0x214d8u,0x1cb0u,0x6b30u,0x21750u,0x21968u,0x1c80u,0x6b38u,0x21be0u,0x21df8u,0x1c90u,0x6ba0u,0x22070u,0x22288u,0x1ce0u,0x6ba8u,0x22500u,0x22718u,0x1cf0u,0x6bb0u,0x22990u,0x22ba8u,0x1cc0u,0x6bb8u,0x22e20u,0x23038u,0x1cd0u,0x6c40u,0x1e9e0u,0x1ebd0u,0x1e00u,0x6c48u,0x1ee70u,0x1f060u,0x1e10u,0x6c50u,0x1f300u,0x1f4f0u,0x1e20u,0x6c58u,0x1f790u,0x1f980u,0x1e30u,0x6cc0u,0x1fc20u,0x1fe10u,0x1e40u,0x6cc8u,0x200b0u,0x202a0u,0x1e50u,0x6cd0u,0x20540u,0x20730u,0x1e60u,0x6cd8u,0x209d0u,0x20bc0u,0x1e70u,0x6d60u,0x20e60u,0x21050u,0x1f80u,0x6d68u,0x212f0u,0x214e0u,0x1f90u,0x6d70u,0x21780u,0x21970u,0x1fa0u,0x6d78u,0x21c10u,0x21e00u,0x1fb0u,0x6de0u,0x220a0u,0x22290u,0x1fc0u,0x6de8u,0x22530u,0x22720u,0x1fd0u,0x6df0u,0x229c0u,0x22bb0u,0x1fe0u,0x6df8u,0x22e50u,0x23040u,0x1ff0u,0x6e40u,0x1ea10u,0x1ebd8u,0x2120u,0x6e48u,0x1eea0u,0x1f068u,0x2130u,0x6e50u,0x1f330u,0x1f4f8u,0x2100u,0x6e58u,0x1f7c0u,0x1f988u,0x2110u,0x6ec0u,0x1fc50u,0x1fe18u,0x2160u,0x6ec8u,0x200e0u,0x202a8u,0x2170u,0x6ed0u,0x20570u,0x20738u,0x2140u,0x6ed8u,0x20a00u,0x20bc8u,0x2150u,0x6f60u,0x20e90u,0x21058u,0x22a0u,0x6f68u,0x21320u,0x214e8u,0x22b0u,0x6f70u,0x217b0u,0x21978u,0x2280u,0x6f78u,0x21c40u,0x21e08u,0x2290u,0x6fe0u,0x220d0u,0x22298u,0x22e0u,0x6fe8u,0x22560u,0x22728u,0x22f0u,0x6ff0u,0x229f0u,0x22bb8u,0x22c0u,0x6ff8u,0x22e80u,0x23048u,0x22d0u,0x0u,0x40u,0x9000u,0xb400u,0x80u,0xc0u,0x9010u,0xb410u,0x110u,0x150u,0x9020u,0xb420u,0x190u,0x1d0u,0x9030u,0xb430u,0x220u,0x260u,0x9040u,0xb440u,0x2a0u,0x2e0u,0x9050u,0xb450u,0x330u,0x370u,0x9060u,0xb460u,0x3b0u,0x3f0u,0x9070u,0xb470u,0x440u,0x400u,0x9080u,0xb480u,0x4c0u,0x480u,0x9090u,0xb490u,0x550u,0x510u,0x90a0u,0xb4a0u,0x5d0u,0x590u,0x90b0u,0xb4b0u,0x660u,0x620u,0x90c0u,0xb4c0u,0x6e0u,0x6a0u,0x90d0u,0xb4d0u,0x770u,0x730u,0x90e0u,0xb4e0u,0x7f0u,0x7b0u,0x90f0u,0xb4f0u,0x10u,0x50u,0x9300u,0xb700u,0x90u,0xd0u,0x9310u,0xb710u,0x100u,0x140u,0x9320u,0xb720u,0x180u,0x1c0u,0x9330u,0xb730u,0x230u,0x270u,0x9340u,0xb740u,0x2b0u,0x2f0u,0x9350u,0xb750u,0x320u,0x360u,0x9360u,0xb760u,0x3a0u,0x3e0u,0x9370u,0xb770u,0x450u,0x410u,0x9380u,0xb780u,0x4d0u,0x490u,0x9390u,0xb790u,0x540u,0x500u,0x93a0u,0xb7a0u,0x5c0u,0x580u,0x93b0u,0xb7b0u,0x670u,0x630u,0x93c0u,0xb7c0u,0x6f0u,0x6b0u,0x93d0u,0xb7d0u,0x760u,0x720u,0x93e0u,0xb7e0u,0x7e0u,0x7a0u,0x93f0u,0xb7f0u,0x20u,0x60u,0x9600u,0xba00u,0xa0u,0xe0u,0x9610u,0xba10u,0x130u,0x170u,0x9620u,0xba20u,0x1b0u,0x1f0u,0x9630u,0xba30u,0x200u,0x240u,0x9640u,0xba40u,0x280u,0x2c0u,0x9650u,0xba50u,0x310u,0x350u,0x9660u,0xba60u,0x390u,0x3d0u,0x9670u,0xba70u,0x460u,0x420u,0x9680u,0xba80u,0x4e0u,0x4a0u,0x9690u,0xba90u,0x570u,0x530u,0x96a0u,0xbaa0u,0x5f0u,0x5b0u,0x96b0u,0xbab0u,0x640u,0x600u,0x96c0u,0xbac0u,0x6c0u,0x680u,0x96d0u,0xbad0u,0x750u,0x710u,0x96e0u,0xbae0u,0x7d0u,0x790u,0x96f0u,0xbaf0u,0x30u,0x70u,0x9900u,0xbd00u,0xb0u,0xf0u,0x9910u,0xbd10u,0x120u,0x160u,0x9920u,0xbd20u,0x1a0u,0x1e0u,0x9930u,0xbd30u,0x210u,0x250u,0x9940u,0xbd40u,0x290u,0x2d0u,0x9950u,0xbd50u,0x300u,0x340u,0x9960u,0xbd60u,0x380u,0x3c0u,0x9970u,0xbd70u,0x470u,0x430u,0x9980u,0xbd80u,0x4f0u,0x4b0u,0x9990u,0xbd90u,0x560u,0x520u,0x99a0u,0xbda0u,0x5e0u,0x5a0u,0x99b0u,0xbdb0u,0x650u,0x610u,0x99c0u,0xbdc0u,0x6d0u,0x690u,0x99d0u,0xbdd0u,0x740u,0x700u,0x99e0u,0xbde0u,0x7c0u,0x780u,0x99f0u,0xbdf0u,0xc000u,0x120c0u,0x12100u,0x12800u,0xc010u,0x12250u,0x12290u,0x12810u,0xc020u,0x123e0u,0x12420u,0x12820u,0xc030u,0x12570u,0x125b0u,0x12830u,0xc040u,0x12680u,0x12740u,0x12840u,0xc050u,0x12810u,0x128d0u,0x12850u,0xc060u,0x129a0u,0x12a60u,0x12860u,0xc070u,0x12b30u,0x12bf0u,0x12870u,0xc080u,0x12cc0u,0x12d00u,0x12880u,0xc090u,0x12e50u,0x12e90u,0x12890u,0xc0a0u,0x12fe0u,0x13020u,0x128a0u,0xc0b0u,0x13170u,0x131b0u,0x128b0u,0xc0c0u,0x13280u,0x13340u,0x128c0u,0xc0d0u,0x13410u,0x134d0u,0x128d0u,0xc0e0u,0x135a0u,0x13660u,0x128e0u,0xc0f0u,0x13730u,0x137f0u,0x128f0u,0xc300u,0x120d0u,0x12108u,0x12900u,0xc310u,0x12240u,0x12298u,0x12910u,0xc320u,0x123f0u,0x12428u,0x12920u,0xc330u,0x12560u,0x125b8u,0x12930u,0xc340u,0x12690u,0x12748u,0x12940u,0xc350u,0x12800u,0x128d8u,0x12950u,0xc360u,0x129b0u,0x12a68u,0x12960u,0xc370u,0x12b20u,0x12bf8u,0x12970u,0xc380u,0x12cd0u,0x12d08u,0x12980u,0xc390u,0x12e40u,0x12e98u,0x12990u,0xc3a0u,0x12ff0u,0x13028u,0x129a0u,0xc3b0u,0x13160u,0x131b8u,0x129b0u,0xc3c0u,0x13290u,0x13348u,0x129c0u,0xc3d0u,0x13400u,0x134d8u,0x129d0u,0xc3e0u,0x135b0u,0x13668u,0x129e0u,0xc3f0u,0x13720u,0x137f8u,0x129f0u,0xc600u,0x120e0u,0x12110u,0x12a00u,0xc610u,0x12270u,0x12280u,0x12a10u,0xc620u,0x123c0u,0x12430u,0x12a20u,0xc630u,0x12550u,0x125a0u,0x12a30u,0xc640u,0x126a0u,0x12750u,0x12a40u,0xc650u,0x12830u,0x128c0u,0x12a50u,0xc660u,0x12980u,0x12a70u,0x12a60u,0xc670u,0x12b10u,0x12be0u,0x12a70u,0xc680u,0x12ce0u,0x12d10u,0x12a80u,0xc690u,0x12e70u,0x12e80u,0x12a90u,0xc6a0u,0x12fc0u,0x13030u,0x12aa0u,0xc6b0u,0x13150u,0x131a0u,0x12ab0u,0xc6c0u,0x132a0u,0x13350u,0x12ac0u,0xc6d0u,0x13430u,0x134c0u,0x12ad0u,0xc6e0u,0x13580u,0x13670u,0x12ae0u,0xc6f0u,0x13710u,0x137e0u,0x12af0u,0xc900u,0x120f0u,0x12118u,0x12b00u,0xc910u,0x12260u,0x12288u,0x12b10u,0xc920u,0x123d0u,0x12438u,0x12b20u,0xc930u,0x12540u,0x125a8u,0x12b30u,0xc940u,0x126b0u,0x12758u,0x12b40u,0xc950u,0x12820u,0x128c8u,0x12b50u,0xc960u,0x12990u,0x12a78u,0x12b60u,0xc970u,0x12b00u,0x12be8u,0x12b70u,0xc980u,0x12cf0u,0x12d18u,0x12b80u,0xc990u,0x12e60u,0x12e88u,0x12b90u,0xc9a0u,0x12fd0u,0x13038u,0x12ba0u,0xc9b0u,0x13140u,0x131a8u,0x12bb0u,0xc9c0u,0x132b0u,0x13358u,0x12bc0u,0xc9d0u,0x13420u,0x134c8u,0x12bd0u,0xc9e0u,0x13590u,0x13678u,0x12be0u,0xc9f0u,0x13700u,0x137e8u,0x12bf0u,0x7800u,0x1c300u,0x1e980u,0x1ebc0u,0x7880u,0x1c310u,0x100000u,0x1ebc2u,0x7910u,0x1c320u,0x100000u,0x1ebc4u,0x7990u,0x1c330u,0x100000u,0x1ebc6u,0x7a20u,0x1c340u,0x1e980u,0x1ebc8u,0x7aa0u,0x1c350u,0x100000u,0x1ebcau,0x7b30u,0x1c360u,0x100000u,0x1ebccu,0x7bb0u,0x1c370u,0x100000u,0x1ebceu,0x7c40u,0x1c380u,0x1e980u,0x1ebd0u,0x7cc0u,0x1c390u,0x100000u,0x1ebd2u,0x7d50u,0x1c3a0u,0x100000u,0x1ebd4u,0x7dd0u,0x1c3b0u,0x100000u,0x1ebd6u,0x7e60u,0x1c3c0u,0x1e980u,0x1ebd8u,0x7ee0u,0x1c3d0u,0x100000u,0x1ebdau,0x7f70u,0x1c3e0u,0x100000u,0x1ebdcu,0x7ff0u,0x1c3f0u,0x100000u,0x1ebdeu,0x7810u,0x1c400u,0x1e9b0u,0x1ebc0u,0x7890u,0x1c410u,0x100000u,0x1ebc2u,0x7900u,0x1c420u,0x100000u,0x1ebc4u,0x7980u,0x1c430u,0x100000u,0x1ebc6u,0x7a30u,0x1c440u,0x1e9b0u,0x1ebc8u,0x7ab0u,0x1c450u,0x100000u,0x1ebcau,0x7b20u,0x1c460u,0x100000u,0x1ebccu,0x7ba0u,0x1c470u,0x100000u,0x1ebceu,0x7c50u,0x1c480u,0x1e9b0u,0x1ebd0u,0x7cd0u,0x1c490u,0x100000u,0x1ebd2u,0x7d40u,0x1c4a0u,0x100000u,0x1ebd4u,0x7dc0u,0x1c4b0u,0x100000u,0x1ebd6u,0x7e70u,0x1c4c0u,0x1e9b0u,0x1ebd8u,0x7ef0u,0x1c4d0u,0x100000u,0x1ebdau,0x7f60u,0x1c4e0u,0x100000u,0x1ebdcu,0x7fe0u,0x1c4f0u,0x100000u,0x1ebdeu,0x7820u,0x1c500u,0x1e9e0u,0x1ebc0u,0x78a0u,0x1c510u,0x100000u,0x1ebc2u,0x7930u,0x1c520u,0x100000u,0x1ebc4u,0x79b0u,0x1c530u,0x100000u,0x1ebc6u,0x7a00u,0x1c540u,0x1e9e0u,0x1ebc8u,0x7a80u,0x1c550u,0x100000u,0x1ebcau,0x7b10u,0x1c560u,0x100000u,0x1ebccu,0x7b90u,0x1c570u,0x100000u,0x1ebceu,0x7c60u,0x1c580u,0x1e9e0u,0x1ebd0u,0x7ce0u,0x1c590u,0x100000u,0x1ebd2u,0x7d70u,0x1c5a0u,0x100000u,0x1ebd4u,0x7df0u,0x1c5b0u,0x100000u,0x1ebd6u,0x7e40u,0x1c5c0u,0x1e9e0u,0x1ebd8u,0x7ec0u,0x1c5d0u,0x100000u,0x1ebdau,0x7f50u,0x1c5e0u,0x100000u,0x1ebdcu,0x7fd0u,0x1c5f0u,0x100000u,0x1ebdeu,0x7830u,0x1c600u,0x1ea10u,0x1ebc0u,0x78b0u,0x1c610u,0x100000u,0x1ebc2u,0x7920u,0x1c620u,0x100000u,0x1ebc4u,0x79a0u,0x1c630u,0x100000u,0x1ebc6u,0x7a10u,0x1c640u,0x1ea10u,0x1ebc8u,0x7a90u,0x1c650u,0x100000u,0x1ebcau,0x7b00u,0x1c660u,0x100000u,0x1ebccu,0x7b80u,0x1c670u,0x100000u,0x1ebceu,0x7c70u,0x1c680u,0x1ea10u,0x1ebd0u,0x7cf0u,0x1c690u,0x100000u,0x1ebd2u,0x7d60u,0x1c6a0u,0x100000u,0x1ebd4u,0x7de0u,0x1c6b0u,0x100000u,0x1ebd6u,0x7e50u,0x1c6c0u,0x1ea10u,0x1ebd8u,0x7ed0u,0x1c6d0u,0x100000u,0x1ebdau,0x7f40u,0x1c6e0u,0x100000u,0x1ebdcu,0x7fc0u,0x1c6f0u,0x100000u,0x1ebdeu,0x26500u,0x1d400u,0x1d400u,0x80000000u,0x26502u,0x1d402u,0x100000u,0x80000000u,0x26504u,0x1d404u,0x100000u,0x80000000u,0x26506u,0x1d406u,0x100000u,0x80000000u,0x26700u,0x1d410u,0x1d400u,0x80000000u,0x26702u,0x1d412u,0x100000u,0x80000000u,0x26704u,0x1d414u,0x100000u,0x80000000u,0x26706u,0x1d416u,0x100000u,0x80000000u,0x26900u,0x1d420u,0x1d400u,0x80000000u,0x26902u,0x1d422u,0x100000u,0x80000000u,0x26904u,0x1d424u,0x100000u,0x80000000u,0x26906u,0x1d426u,0x100000u,0x80000000u,0x26b00u,0x1d430u,0x1d400u,0x80000000u,0x26b02u,0x1d432u,0x100000u,0x80000000u,0x26b04u,0x1d434u,0x100000u,0x80000000u,0x26b06u,0x1d436u,0x100000u,0x80000000u,0x26508u,0x1d408u,0x1d410u,0x80000000u,0x2650au,0x1d40au,0x100000u,0x80000000u,0x2650cu,0x1d40cu,0x100000u,0x80000000u,0x2650eu,0x1d40eu,0x100000u,0x80000000u,0x26708u,0x1d418u,0x1d410u,0x80000000u,0x2670au,0x1d41au,0x100000u,0x80000000u,0x2670cu,0x1d41cu,0x100000u,0x80000000u,0x2670eu,0x1d41eu,0x100000u,0x80000000u,0x26908u,0x1d428u,0x1d410u,0x80000000u,0x2690au,0x1d42au,0x100000u,0x80000000u,0x2690cu,0x1d42cu,0x100000u,0x80000000u,0x2690eu,0x1d42eu,0x100000u,0x80000000u,0x26b08u,0x1d438u,0x1d410u,0x80000000u,0x26b0au,0x1d43au,0x100000u,0x80000000u,0x26b0cu,0x1d43cu,0x100000u,0x80000000u,0x26b0eu,0x1d43eu,0x100000u,0x80000000u,0x26d00u,0x1d440u,0x1d420u,0x80000000u,0x26d02u,0x1d442u,0x100000u,0x80000000u,0x26d04u,0x1d444u,0x100000u,0x80000000u,0x26d06u,0x1d446u,0x100000u,0x80000000u,0x26f00u,0x1d450u,0x1d420u,0x80000000u,0x26f02u,0x1d452u,0x100000u,0x80000000u,0x26f04u,0x1d454u,0x100000u,0x80000000u,0x26f06u,0x1d456u,0x100000u,0x80000000u,0x27100u,0x1d460u,0x1d420u,0x80000000u,0x27102u,0x1d462u,0x100000u,0x80000000u,0x27104u,0x1d464u,0x100000u,0x80000000u,0x27106u,0x1d466u,0x100000u,0x80000000u,0x27300u,0x1d470u,0x1d420u,0x80000000u,0x27302u,0x1d472u,0x100000u,0x80000000u,0x27304u,0x1d474u,0x100000u,0x80000000u,0x27306u,0x1d476u,0x100000u,0x80000000u,0x100000u,0x1d448u,0x1d430u,0x80000000u,0x100000u,0x1d44au,0x100000u,0x80000000u,0x100000u,0x1d44cu,0x100000u,0x80000000u,0x100000u,0x1d44eu,0x100000u,0x80000000u,0x100000u,0x1d458u,0x1d430u,0x80000000u,0x100000u,0x1d45au,0x100000u,0x80000000u,0x100000u,0x1d45cu,0x100000u,0x80000000u,0x100000u,0x1d45eu,0x100000u,0x80000000u,0x100000u,0x1d468u,0x1d430u,0x80000000u,0x100000u,0x1d46au,0x100000u,0x80000000u,0x100000u,0x1d46cu,0x100000u,0x80000000u,0x100000u,0x1d46eu,0x100000u,0x80000000u,0x100000u,0x1d478u,0x1d430u,0x80000000u,0x100000u,0x1d47au,0x100000u,0x80000000u,0x100000u,0x1d47cu,0x100000u,0x80000000u,0x100000u,0x1d47eu,0x100000u,0x80000000u,0x850u,0x60000u,0x90420u,0xefc50u,0x840u,0x60010u,0x90430u,0xefc40u,0x870u,0x60020u,0x90400u,0xefc70u,0x860u,0x60030u,0x90410u,0xefc60u,0x810u,0x60040u,0x90460u,0xefc10u,0x800u,0x60050u,0x90470u,0xefc00u,0x830u,0x60060u,0x90440u,0xefc30u,0x820u,0x60070u,0x90450u,0xefc20u,0x8d0u,0x60080u,0x904b0u,0xefcd0u,0x8c0u,0x60090u,0x904a0u,0xefcc0u,0x8f0u,0x600a0u,0x90490u,0xefcf0u,0x8e0u,0x600b0u,0x90480u,0xefce0u,0x890u,0x600c0u,0x904f0u,0xefc90u,0x880u,0x600d0u,0x904e0u,0xefc80u,0x8b0u,0x600e0u,0x904d0u,0xefcb0u,0x8a0u,0x600f0u,0x904c0u,0xefca0u,0x960u,0x60100u,0x90530u,0xefd60u,0x970u,0x60110u,0x90520u,0xefd70u,0x940u,0x60120u,0x90510u,0xefd40u,0x950u,0x60130u,0x90500u,0xefd50u,0x920u,0x60140u,0x90570u,0xefd20u,0x930u,0x60150u,0x90560u,0xefd30u,0x900u,0x60160u,0x90550u,0xefd00u,0x910u,0x60170u,0x90540u,0xefd10u,0x9e0u,0x60190u,0x905b0u,0xefde0u,0x9f0u,0x60180u,0x905a0u,0xefdf0u,0x9c0u,0x601b0u,0x90590u,0xefdc0u,0x9d0u,0x601a0u,0x90580u,0xefdd0u,0x9a0u,0x601d0u,0x905f0u,0xefda0u,0x9b0u,0x601c0u,0x905e0u,0xefdb0u,0x980u,0x601f0u,0x905d0u,0xefd80u,0x990u,0x601e0u,0x905c0u,0xefd90u,0xa60u,0x60210u,0xbfa40u,0xefe60u,0xa70u,0x60200u,0xbfa50u,0xefe70u,0xa40u,0x60230u,0xbfa60u,0xefe40u,0xa50u,0x60220u,0xbfa70u,0xefe50u,0xa20u,0x60250u,0xbfa00u,0xefe20u,0xa30u,0x60240u,0xbfa10u,0xefe30u,0xa00u,0x60270u,0xbfa20u,0xefe00u,0xa10u,0x60260u,0xbfa30u,0xefe10u,0x2fef0u,0x60290u,0xbfac0u,0xefef0u,0x2fee0u,0x60280u,0xbfad0u,0xefee0u,0x2fed0u,0x602b0u,0xbfae0u,0xefed0u,0x2fec0u,0x602a0u,0xbfaf0u,0xefec0u,0x2feb0u,0x602d0u,0xbfa80u,0xefeb0u,0x2fea0u,0x602c0u,0xbfa90u,0xefea0u,0x2fe90u,0x602f0u,0xbfaa0u,0xefe90u,0x2fe80u,0x602e0u,0xbfab0u,0xefe80u,0x2ff70u,0x60320u,0xbfb40u,0xeff70u,0x2ff60u,0x60330u,0xbfb50u,0xeff60u,0x2ff50u,0x60300u,0xbfb60u,0xeff50u,0x2ff40u,0x60310u,0xbfb70u,0xeff40u,0x2ff30u,0x60360u,0xbfb00u,0xeff30u,0x2ff20u,0x60370u,0xbfb10u,0xeff20u,0x2ff10u,0x60340u,0xbfb20u,0xeff10u,0x2ff00u,0x60350u,0xbfb30u,0xeff00u,0x2fff0u,0x603a0u,0xbfbd0u,0xefff0u,0x2ffe0u,0x603b0u,0xbfbc0u,0xeffe0u,0x2ffd0u,0x60380u,0xbfbf0u,0xeffd0u,0x2ffc0u,0x60390u,0xbfbe0u,0xeffc0u,0x2ffb0u,0x603e0u,0xbfb90u,0xeffb0u,0x2ffa0u,0x603f0u,0xbfb80u,0xeffa0u,0x2ff90u,0x603c0u,0xbfbb0u,0xeff90u,0x2ff80u,0x603d0u,0xbfba0u,0xeff80u,0x0u,0x80000000u,0x0u,0x18830u,0x0u,0x80000000u,0x0u,0x18830u,0x0u,0x80000000u,0x0u,0x18830u,0x0u,0x80000000u,0x0u,0x18830u,0x0u,0x80000000u,0x0u,0x18830u,0x0u,0x80000000u,0x0u,0x18830u,0x0u,0x80000000u,0x0u,0x18830u,0x0u,0x80000000u,0x0u,0x18830u,0x0u,0x80000000u,0x0u,0x18830u,0x0u,0x80000000u,0x0u,0x18830u,0x0u,0x80000000u,0x0u,0x18830u,0x0u,0x80000000u,0x0u,0x18830u,0x0u,0x80000000u,0x0u,0x18830u,0x0u,0x80000000u,0x0u,0x18830u,0x0u,0x80000000u,0x0u,0x18830u,0x0u,0x80000000u,0x0u,0x18830u,0x0u,0x80000000u,0x0u,0x188b2u,0x0u,0x80000000u,0x0u,0x188b2u,0x0u,0x80000000u,0x0u,0x188b2u,0x0u,0x80000000u,0x0u,0x188b2u,0x0u,0x80000000u,0x0u,0x188b2u,0x0u,0x80000000u,0x0u,0x188b2u,0x0u,0x80000000u,0x0u,0x188b2u,0x0u,0x80000000u,0x0u,0x188b2u,0x0u,0x80000000u,0x0u,0x18892u,0x0u,0x80000000u,0x0u,0x18892u,0x0u,0x80000000u,0x0u,0x18892u,0x0u,0x80000000u,0x0u,0x18892u,0x0u,0x80000000u,0x0u,0x18892u,0x0u,0x80000000u,0x0u,0x18892u,0x0u,0x80000000u,0x0u,0x18892u,0x0u,0x80000000u,0x0u,0x18892u,0x0u,0x80000000u,0x0u,0x1489au,0x0u,0x80000000u,0x0u,0x1489au,0x0u,0x80000000u,0x0u,0x1489au,0x0u,0x80000000u,0x0u,0x1489au,0x0u,0x80000000u,0x0u,0x1489au,0x0u,0x80000000u,0x0u,0x1489au,0x0u,0x80000000u,0x0u,0x1489au,0x0u,0x80000000u,0x0u,0x1489au,0x0u,0x80000000u,0x0u,0x34018u,0x0u,0x80000000u,0x0u,0x34018u,0x0u,0x80000000u,0x0u,0x34018u,0x0u,0x80000000u,0x0u,0x34018u,0x0u,0x80000000u,0x0u,0x34018u,0x0u,0x80000000u,0x0u,0x34018u,0x0u,0x80000000u,0x0u,0x34018u,0x0u,0x80000000u,0x0u,0x34018u,0x0u,0x80000000u,0x0u,0x36018u,0x0u,0x80000000u,0x0u,0x36018u,0x0u,0x80000000u,0x0u,0x36018u,0x0u,0x80000000u,0x0u,0x36018u,0x0u,0x80000000u,0x0u,0x36018u,0x0u,0x80000000u,0x0u,0x36018u,0x0u,0x80000000u,0x0u,0x36018u,0x0u,0x80000000u,0x0u,0x36018u,0x0u,0x80000000u,0x0u,0x36030u,0x0u,0x80000000u,0x0u,0x36030u,0x0u,0x80000000u,0x0u,0x36030u,0x0u,0x80000000u,0x0u,0x36030u,0x0u,0x80000000u,0x0u,0x36030u,0x0u,0x80000000u,0x0u,0x36030u,0x0u,0x80000000u,0x0u,0x36030u,0x0u,0x80000000u,0x0u,0x36030u,0x6840u,0x6860u,0x6820u,0x6820u,0x6848u,0x6868u,0x6828u,0x6830u,0x6850u,0x6870u,0x6830u,0x6840u,0x6858u,0x6878u,0x6838u,0x6850u,0x68c0u,0x68e0u,0x68a0u,0x68a0u,0x68c8u,0x68e8u,0x68a8u,0x68b0u,0x68d0u,0x68f0u,0x68b0u,0x68c0u,0x68d8u,0x68f8u,0x68b8u,0x68d0u,0x6960u,0x6940u,0x6900u,0x6900u,0x6968u,0x6948u,0x6908u,0x6910u,0x6970u,0x6950u,0x6910u,0x6960u,0x6978u,0x6958u,0x6918u,0x6970u,0x69e0u,0x69c0u,0x6980u,0x6980u,0x69e8u,0x69c8u,0x6988u,0x6990u,0x69f0u,0x69d0u,0x6990u,0x69e0u,0x69f8u,0x69d8u,0x6998u,0x69f0u,0x6c00u,0x6c20u,0x6c60u,0x6a20u,0x6c08u,0x6c28u,0x6c68u,0x6a30u,0x6c10u,0x6c30u,0x6c70u,0x6a40u,0x6c18u,0x6c38u,0x6c78u,0x6a50u,0x6c80u,0x6ca0u,0x6ce0u,0x6aa0u,0x6c88u,0x6ca8u,0x6ce8u,0x6ab0u,0x6c90u,0x6cb0u,0x6cf0u,0x6ac0u,0x6c98u,0x6cb8u,0x6cf8u,0x6ad0u,0x6d20u,0x6d00u,0x6d40u,0x6b00u,0x6d28u,0x6d08u,0x6d48u,0x6b10u,0x6d30u,0x6d10u,0x6d50u,0x6b60u,0x6d38u,0x6d18u,0x6d58u,0x6b70u,0x6da0u,0x6d80u,0x6dc0u,0x6b80u,0x6da8u,0x6d88u,0x6dc8u,0x6b90u,0x6db0u,0x6d90u,0x6dd0u,0x6be0u,0x6db8u,0x6d98u,0x6dd8u,0x6bf0u,0x7040u,0x7060u,0x7020u,0x6c60u,0x7048u,0x7068u,0x7028u,0x6c70u,0x7050u,0x7070u,0x7030u,0x6c00u,0x7058u,0x7078u,0x7038u,0x6c10u,0x70c0u,0x70e0u,0x70a0u,0x6ce0u,0x70c8u,0x70e8u,0x70a8u,0x6cf0u,0x70d0u,0x70f0u,0x70b0u,0x6c80u,0x70d8u,0x70f8u,0x70b8u,0x6c90u,0x7160u,0x7140u,0x7100u,0x6d40u,0x7168u,0x7148u,0x7108u,0x6d50u,0x7170u,0x7150u,0x7110u,0x6d20u,0x7178u,0x7158u,0x7118u,0x6d30u,0x71e0u,0x71c0u,0x7180u,0x6dc0u,0x71e8u,0x71c8u,0x7188u,0x6dd0u,0x71f0u,0x71d0u,0x7190u,0x6da0u,0x71f8u,0x71d8u,0x7198u,0x6db0u,0x7400u,0x7420u,0x7460u,0x6e60u,0x7408u,0x7428u,0x7468u,0x6e70u,0x7410u,0x7430u,0x7470u,0x6e00u,0x7418u,0x7438u,0x7478u,0x6e10u,0x7480u,0x74a0u,0x74e0u,0x6ee0u,0x7488u,0x74a8u,0x74e8u,0x6ef0u,0x7490u,0x74b0u,0x74f0u,0x6e80u,0x7498u,0x74b8u,0x74f8u,0x6e90u,0x7520u,0x7500u,0x7540u,0x6f40u,0x7528u,0x7508u,0x7548u,0x6f50u,0x7530u,0x7510u,0x7550u,0x6f20u,0x7538u,0x7518u,0x7558u,0x6f30u,0x75a0u,0x7580u,0x75c0u,0x6fc0u,0x75a8u,0x7588u,0x75c8u,0x6fd0u,0x75b0u,0x7590u,0x75d0u,0x6fa0u,0x75b8u,0x7598u,0x75d8u,0x6fb0u,0x6860u,0x1ea40u,0x1ec20u,0x2440u,0x6868u,0x1eed0u,0x1f0b0u,0x2450u,0x6870u,0x1f360u,0x1f540u,0x2460u,0x6878u,0x1f7f0u,0x1f9d0u,0x2470u,0x68e0u,0x1fc80u,0x1fe60u,0x2400u,0x68e8u,0x20110u,0x202f0u,0x2410u,0x68f0u,0x205a0u,0x20780u,0x2420u,0x68f8u,0x20a30u,0x20c10u,0x2430u,0x6940u,0x20ec0u,0x210a0u,0x25c0u,0x6948u,0x21350u,0x21530u,0x25d0u,0x6950u,0x217e0u,0x219c0u,0x25e0u,0x6958u,0x21c70u,0x21e50u,0x25f0u,0x69c0u,0x22100u,0x222e0u,0x2580u,0x69c8u,0x22590u,0x22770u,0x2590u,0x69d0u,0x22a20u,0x22c00u,0x25a0u,0x69d8u,0x22eb0u,0x23090u,0x25b0u,0x6a60u,0x1ea70u,0x1ec28u,0x2760u,0x6a68u,0x1ef00u,0x1f0b8u,0x2770u,0x6a70u,0x1f390u,0x1f548u,0x2740u,0x6a78u,0x1f820u,0x1f9d8u,0x2750u,0x6ae0u,0x1fcb0u,0x1fe68u,0x2720u,0x6ae8u,0x20140u,0x202f8u,0x2730u,0x6af0u,0x205d0u,0x20788u,0x2700u,0x6af8u,0x20a60u,0x20c18u,0x2710u,0x6b40u,0x20ef0u,0x210a8u,0x28e0u,0x6b48u,0x21380u,0x21538u,0x28f0u,0x6b50u,0x21810u,0x219c8u,0x28c0u,0x6b58u,0x21ca0u,0x21e58u,0x28d0u,0x6bc0u,0x22130u,0x222e8u,0x28a0u,0x6bc8u,0x225c0u,0x22778u,0x28b0u,0x6bd0u,0x22a50u,0x22c08u,0x2880u,0x6bd8u,0x22ee0u,0x23098u,0x2890u,0x6c20u,0x1eaa0u,0x1ec30u,0x2a40u,0x6c28u,0x1ef30u,0x1f0c0u,0x2a50u,0x6c30u,0x1f3c0u,0x1f550u,0x2a60u,0x6c38u,0x1f850u,0x1f9e0u,0x2a70u,0x6ca0u,0x1fce0u,0x1fe70u,0x2a00u,0x6ca8u,0x20170u,0x20300u,0x2a10u,0x6cb0u,0x20600u,0x20790u,0x2a20u,0x6cb8u,0x20a90u,0x20c20u,0x2a30u,0x6d00u,0x20f20u,0x210b0u,0x2bc0u,0x6d08u,0x213b0u,0x21540u,0x2bd0u,0x6d10u,0x21840u,0x219d0u,0x2be0u,0x6d18u,0x21cd0u,0x21e60u,0x2bf0u,0x6d80u,0x22160u,0x222f0u,0x2b80u,0x6d88u,0x225f0u,0x22780u,0x2b90u,0x6d90u,0x22a80u,0x22c10u,0x2ba0u,0x6d98u,0x22f10u,0x230a0u,0x2bb0u,0x6e20u,0x1ead0u,0x1ec38u,0x2d60u,0x6e28u,0x1ef60u,0x1f0c8u,0x2d70u,0x6e30u,0x1f3f0u,0x1f558u,0x2d40u,0x6e38u,0x1f880u,0x1f9e8u,0x2d50u,0x6ea0u,0x1fd10u,0x1fe78u,0x2d20u,0x6ea8u,0x201a0u,0x20308u,0x2d30u,0x6eb0u,0x20630u,0x20798u,0x2d00u,0x6eb8u,0x20ac0u,0x20c28u,0x2d10u,0x6f00u,0x20f50u,0x210b8u,0x2ee0u,0x6f08u,0x213e0u,0x21548u,0x2ef0u,0x6f10u,0x21870u,0x219d8u,0x2ec0u,0x6f18u,0x21d00u,0x21e68u,0x2ed0u,0x6f80u,0x22190u,0x222f8u,0x2ea0u,0x6f88u,0x22620u,0x22788u,0x2eb0u,0x6f90u,0x22ab0u,0x22c18u,0x2e80u,0x6f98u,0x22f40u,0x230a8u,0x2e90u,0x0u,0x40u,0x9000u,0xcc00u,0x80u,0xc0u,0x9010u,0xcc10u,0x110u,0x150u,0x9020u,0xcc20u,0x190u,0x1d0u,0x9030u,0xcc30u,0x220u,0x260u,0x9040u,0xcc40u,0x2a0u,0x2e0u,0x9050u,0xcc50u,0x330u,0x370u,0x9060u,0xcc60u,0x3b0u,0x3f0u,0x9070u,0xcc70u,0x440u,0x400u,0x9080u,0xcc80u,0x4c0u,0x480u,0x9090u,0xcc90u,0x550u,0x510u,0x90a0u,0xcca0u,0x5d0u,0x590u,0x90b0u,0xccb0u,0x660u,0x620u,0x90c0u,0xccc0u,0x6e0u,0x6a0u,0x90d0u,0xccd0u,0x770u,0x730u,0x90e0u,0xcce0u,0x7f0u,0x7b0u,0x90f0u,0xccf0u,0x10u,0x50u,0x9300u,0xcf00u,0x90u,0xd0u,0x9310u,0xcf10u,0x100u,0x140u,0x9320u,0xcf20u,0x180u,0x1c0u,0x9330u,0xcf30u,0x230u,0x270u,0x9340u,0xcf40u,0x2b0u,0x2f0u,0x9350u,0xcf50u,0x320u,0x360u,0x9360u,0xcf60u,0x3a0u,0x3e0u,0x9370u,0xcf70u,0x450u,0x410u,0x9380u,0xcf80u,0x4d0u,0x490u,0x9390u,0xcf90u,0x540u,0x500u,0x93a0u,0xcfa0u,0x5c0u,0x580u,0x93b0u,0xcfb0u,0x670u,0x630u,0x93c0u,0xcfc0u,0x6f0u,0x6b0u,0x93d0u,0xcfd0u,0x760u,0x720u,0x93e0u,0xcfe0u,0x7e0u,0x7a0u,0x93f0u,0xcff0u,0x20u,0x60u,0x9600u,0xd200u,0xa0u,0xe0u,0x9610u,0xd210u,0x130u,0x170u,0x9620u,0xd220u,0x1b0u,0x1f0u,0x9630u,0xd230u,0x200u,0x240u,0x9640u,0xd240u,0x280u,0x2c0u,0x9650u,0xd250u,0x310u,0x350u,0x9660u,0xd260u,0x390u,0x3d0u,0x9670u,0xd270u,0x460u,0x420u,0x9680u,0xd280u,0x4e0u,0x4a0u,0x9690u,0xd290u,0x570u,0x530u,0x96a0u,0xd2a0u,0x5f0u,0x5b0u,0x96b0u,0xd2b0u,0x640u,0x600u,0x96c0u,0xd2c0u,0x6c0u,0x680u,0x96d0u,0xd2d0u,0x750u,0x710u,0x96e0u,0xd2e0u,0x7d0u,0x790u,0x96f0u,0xd2f0u,0x30u,0x70u,0x9900u,0xd500u,0xb0u,0xf0u,0x9910u,0xd510u,0x120u,0x160u,0x9920u,0xd520u,0x1a0u,0x1e0u,0x9930u,0xd530u,0x210u,0x250u,0x9940u,0xd540u,0x290u,0x2d0u,0x9950u,0xd550u,0x300u,0x340u,0x9960u,0xd560u,0x380u,0x3c0u,0x9970u,0xd570u,0x470u,0x430u,0x9980u,0xd580u,0x4f0u,0x4b0u,0x9990u,0xd590u,0x560u,0x520u,0x99a0u,0xd5a0u,0x5e0u,0x5a0u,0x99b0u,0xd5b0u,0x650u,0x610u,0x99c0u,0xd5c0u,0x6d0u,0x690u,0x99d0u,0xd5d0u,0x740u,0x700u,0x99e0u,0xd5e0u,0x7c0u,0x780u,0x99f0u,0xd5f0u,0xc008u,0x12120u,0x12160u,0x12c00u,0xc018u,0x122b0u,0x122f0u,0x12c10u,0xc028u,0x12400u,0x12440u,0x12c20u,0xc038u,0x12590u,0x125d0u,0x12c30u,0xc048u,0x12760u,0x12720u,0x12c40u,0xc058u,0x128f0u,0x128b0u,0x12c50u,0xc068u,0x12a40u,0x12a00u,0x12c60u,0xc078u,0x12bd0u,0x12b90u,0x12c70u,0xc088u,0x12d20u,0x12d60u,0x12c80u,0xc098u,0x12eb0u,0x12ef0u,0x12c90u,0xc0a8u,0x13000u,0x13040u,0x12ca0u,0xc0b8u,0x13190u,0x131d0u,0x12cb0u,0xc0c8u,0x13360u,0x13320u,0x12cc0u,0xc0d8u,0x134f0u,0x134b0u,0x12cd0u,0xc0e8u,0x13640u,0x13600u,0x12ce0u,0xc0f8u,0x137d0u,0x13790u,0x12cf0u,0xc308u,0x12130u,0x12168u,0x12d00u,0xc318u,0x122a0u,0x122f8u,0x12d10u,0xc328u,0x12410u,0x12448u,0x12d20u,0xc338u,0x12580u,0x125d8u,0x12d30u,0xc348u,0x12770u,0x12728u,0x12d40u,0xc358u,0x128e0u,0x128b8u,0x12d50u,0xc368u,0x12a50u,0x12a08u,0x12d60u,0xc378u,0x12bc0u,0x12b98u,0x12d70u,0xc388u,0x12d30u,0x12d68u,0x12d80u,0xc398u,0x12ea0u,0x12ef8u,0x12d90u,0xc3a8u,0x13010u,0x13048u,0x12da0u,0xc3b8u,0x13180u,0x131d8u,0x12db0u,0xc3c8u,0x13370u,0x13328u,0x12dc0u,0xc3d8u,0x134e0u,0x134b8u,0x12dd0u,0xc3e8u,0x13650u,0x13608u,0x12de0u,0xc3f8u,0x137c0u,0x13798u,0x12df0u,0xc608u,0x12140u,0x12170u,0x12e00u,0xc618u,0x122d0u,0x122e0u,0x12e10u,0xc628u,0x12460u,0x12450u,0x12e20u,0xc638u,0x125f0u,0x125c0u,0x12e30u,0xc648u,0x12700u,0x12730u,0x12e40u,0xc658u,0x12890u,0x128a0u,0x12e50u,0xc668u,0x12a20u,0x12a10u,0x12e60u,0xc678u,0x12bb0u,0x12b80u,0x12e70u,0xc688u,0x12d40u,0x12d70u,0x12e80u,0xc698u,0x12ed0u,0x12ee0u,0x12e90u,0xc6a8u,0x13060u,0x13050u,0x12ea0u,0xc6b8u,0x131f0u,0x131c0u,0x12eb0u,0xc6c8u,0x13300u,0x13330u,0x12ec0u,0xc6d8u,0x13490u,0x134a0u,0x12ed0u,0xc6e8u,0x13620u,0x13610u,0x12ee0u,0xc6f8u,0x137b0u,0x13780u,0x12ef0u,0xc908u,0x12150u,0x12178u,0x12f00u,0xc918u,0x122c0u,0x122e8u,0x12f10u,0xc928u,0x12470u,0x12458u,0x12f20u,0xc938u,0x125e0u,0x125c8u,0x12f30u,0xc948u,0x12710u,0x12738u,0x12f40u,0xc958u,0x12880u,0x128a8u,0x12f50u,0xc968u,0x12a30u,0x12a18u,0x12f60u,0xc978u,0x12ba0u,0x12b88u,0x12f70u,0xc988u,0x12d50u,0x12d78u,0x12f80u,0xc998u,0x12ec0u,0x12ee8u,0x12f90u,0xc9a8u,0x13070u,0x13058u,0x12fa0u,0xc9b8u,0x131e0u,0x131c8u,0x12fb0u,0xc9c8u,0x13310u,0x13338u,0x12fc0u,0xc9d8u,0x13480u,0x134a8u,0x12fd0u,0xc9e8u,0x13630u,0x13618u,0x12fe0u,0xc9f8u,0x137a0u,0x13788u,0x12ff0u,0x8000u,0x1c300u,0x1ea40u,0x1ec20u,0x8080u,0x1c310u,0x100000u,0x1ec22u,0x8110u,0x1c320u,0x100000u,0x1ec24u,0x8190u,0x1c330u,0x100000u,0x1ec26u,0x8220u,0x1c340u,0x1ea40u,0x1ec28u,0x82a0u,0x1c350u,0x100000u,0x1ec2au,0x8330u,0x1c360u,0x100000u,0x1ec2cu,0x83b0u,0x1c370u,0x100000u,0x1ec2eu,0x8440u,0x1c380u,0x1ea40u,0x1ec30u,0x84c0u,0x1c390u,0x100000u,0x1ec32u,0x8550u,0x1c3a0u,0x100000u,0x1ec34u,0x85d0u,0x1c3b0u,0x100000u,0x1ec36u,0x8660u,0x1c3c0u,0x1ea40u,0x1ec38u,0x86e0u,0x1c3d0u,0x100000u,0x1ec3au,0x8770u,0x1c3e0u,0x100000u,0x1ec3cu,0x87f0u,0x1c3f0u,0x100000u,0x1ec3eu,0x8010u,0x1c400u,0x1ea70u,0x1ec20u,0x8090u,0x1c410u,0x100000u,0x1ec22u,0x8100u,0x1c420u,0x100000u,0x1ec24u,0x8180u,0x1c430u,0x100000u,0x1ec26u,0x8230u,0x1c440u,0x1ea70u,0x1ec28u,0x82b0u,0x1c450u,0x100000u,0x1ec2au,0x8320u,0x1c460u,0x100000u,0x1ec2cu,0x83a0u,0x1c470u,0x100000u,0x1ec2eu,0x8450u,0x1c480u,0x1ea70u,0x1ec30u,0x84d0u,0x1c490u,0x100000u,0x1ec32u,0x8540u,0x1c4a0u,0x100000u,0x1ec34u,0x85c0u,0x1c4b0u,0x100000u,0x1ec36u,0x8670u,0x1c4c0u,0x1ea70u,0x1ec38u,0x86f0u,0x1c4d0u,0x100000u,0x1ec3au,0x8760u,0x1c4e0u,0x100000u,0x1ec3cu,0x87e0u,0x1c4f0u,0x100000u,0x1ec3eu,0x8020u,0x1c500u,0x1eaa0u,0x1ec20u,0x80a0u,0x1c510u,0x100000u,0x1ec22u,0x8130u,0x1c520u,0x100000u,0x1ec24u,0x81b0u,0x1c530u,0x100000u,0x1ec26u,0x8200u,0x1c540u,0x1eaa0u,0x1ec28u,0x8280u,0x1c550u,0x100000u,0x1ec2au,0x8310u,0x1c560u,0x100000u,0x1ec2cu,0x8390u,0x1c570u,0x100000u,0x1ec2eu,0x8460u,0x1c580u,0x1eaa0u,0x1ec30u,0x84e0u,0x1c590u,0x100000u,0x1ec32u,0x8570u,0x1c5a0u,0x100000u,0x1ec34u,0x85f0u,0x1c5b0u,0x100000u,0x1ec36u,0x8640u,0x1c5c0u,0x1eaa0u,0x1ec38u,0x86c0u,0x1c5d0u,0x100000u,0x1ec3au,0x8750u,0x1c5e0u,0x100000u,0x1ec3cu,0x87d0u,0x1c5f0u,0x100000u,0x1ec3eu,0x8030u,0x1c600u,0x1ead0u,0x1ec20u,0x80b0u,0x1c610u,0x100000u,0x1ec22u,0x8120u,0x1c620u,0x100000u,0x1ec24u,0x81a0u,0x1c630u,0x100000u,0x1ec26u,0x8210u,0x1c640u,0x1ead0u,0x1ec28u,0x8290u,0x1c650u,0x100000u,0x1ec2au,0x8300u,0x1c660u,0x100000u,0x1ec2cu,0x8380u,0x1c670u,0x100000u,0x1ec2eu,0x8470u,0x1c680u,0x1ead0u,0x1ec30u,0x84f0u,0x1c690u,0x100000u,0x1ec32u,0x8560u,0x1c6a0u,0x100000u,0x1ec34u,0x85e0u,0x1c6b0u,0x100000u,0x1ec36u,0x8650u,0x1c6c0u,0x1ead0u,0x1ec38u,0x86d0u,0x1c6d0u,0x100000u,0x1ec3au,0x8740u,0x1c6e0u,0x100000u,0x1ec3cu,0x87c0u,0x1c6f0u,0x100000u,0x1ec3eu,0x27500u,0x1d480u,0x1d480u,0x80000000u,0x27502u,0x1d482u,0x100000u,0x80000000u,0x27504u,0x1d484u,0x100000u,0x80000000u,0x27506u,0x1d486u,0x100000u,0x80000000u,0x27700u,0x1d490u,0x1d480u,0x80000000u,0x27702u,0x1d492u,0x100000u,0x80000000u,0x27704u,0x1d494u,0x100000u,0x80000000u,0x27706u,0x1d496u,0x100000u,0x80000000u,0x27900u,0x1d4a0u,0x1d480u,0x80000000u,0x27902u,0x1d4a2u,0x100000u,0x80000000u,0x27904u,0x1d4a4u,0x100000u,0x80000000u,0x27906u,0x1d4a6u,0x100000u,0x80000000u,0x27b00u,0x1d4b0u,0x1d480u,0x80000000u,0x27b02u,0x1d4b2u,0x100000u,0x80000000u,0x27b04u,0x1d4b4u,0x100000u,0x80000000u,0x27b06u,0x1d4b6u,0x100000u,0x80000000u,0x27508u,0x1d488u,0x1d490u,0x80000000u,0x2750au,0x1d48au,0x100000u,0x80000000u,0x2750cu,0x1d48cu,0x100000u,0x80000000u,0x2750eu,0x1d48eu,0x100000u,0x80000000u,0x27708u,0x1d498u,0x1d490u,0x80000000u,0x2770au,0x1d49au,0x100000u,0x80000000u,0x2770cu,0x1d49cu,0x100000u,0x80000000u,0x2770eu,0x1d49eu,0x100000u,0x80000000u,0x27908u,0x1d4a8u,0x1d490u,0x80000000u,0x2790au,0x1d4aau,0x100000u,0x80000000u,0x2790cu,0x1d4acu,0x100000u,0x80000000u,0x2790eu,0x1d4aeu,0x100000u,0x80000000u,0x27b08u,0x1d4b8u,0x1d490u,0x80000000u,0x27b0au,0x1d4bau,0x100000u,0x80000000u,0x27b0cu,0x1d4bcu,0x100000u,0x80000000u,0x27b0eu,0x1d4beu,0x100000u,0x80000000u,0x26d08u,0x1d4c0u,0x1d4a0u,0x80000000u,0x26d0au,0x1d4c2u,0x100000u,0x80000000u,0x26d0cu,0x1d4c4u,0x100000u,0x80000000u,0x26d0eu,0x1d4c6u,0x100000u,0x80000000u,0x26f08u,0x1d4d0u,0x1d4a0u,0x80000000u,0x26f0au,0x1d4d2u,0x100000u,0x80000000u,0x26f0cu,0x1d4d4u,0x100000u,0x80000000u,0x26f0eu,0x1d4d6u,0x100000u,0x80000000u,0x27108u,0x1d4e0u,0x1d4a0u,0x80000000u,0x2710au,0x1d4e2u,0x100000u,0x80000000u,0x2710cu,0x1d4e4u,0x100000u,0x80000000u,0x2710eu,0x1d4e6u,0x100000u,0x80000000u,0x27308u,0x1d4f0u,0x1d4a0u,0x80000000u,0x2730au,0x1d4f2u,0x100000u,0x80000000u,0x2730cu,0x1d4f4u,0x100000u,0x80000000u,0x2730eu,0x1d4f6u,0x100000u,0x80000000u,0x100000u,0x1d4c8u,0x1d4b0u,0x80000000u,0x100000u,0x1d4cau,0x100000u,0x80000000u,0x100000u,0x1d4ccu,0x100000u,0x80000000u,0x100000u,0x1d4ceu,0x100000u,0x80000000u,0x100000u,0x1d4d8u,0x1d4b0u,0x80000000u,0x100000u,0x1d4dau,0x100000u,0x80000000u,0x100000u,0x1d4dcu,0x100000u,0x80000000u,0x100000u,0x1d4deu,0x100000u,0x80000000u,0x100000u,0x1d4e8u,0x1d4b0u,0x80000000u,0x100000u,0x1d4eau,0x100000u,0x80000000u,0x100000u,0x1d4ecu,0x100000u,0x80000000u,0x100000u,0x1d4eeu,0x100000u,0x80000000u,0x100000u,0x1d4f8u,0x1d4b0u,0x80000000u,0x100000u,0x1d4fau,0x100000u,0x80000000u,0x100000u,0x1d4fcu,0x100000u,0x80000000u,0x100000u,0x1d4feu,0x100000u,0x80000000u,0x30000u,0x60420u,0xbfc50u,0xf0000u,0x30010u,0x60430u,0xbfc40u,0xf0010u,0x30020u,0x60400u,0xbfc70u,0xf0020u,0x30030u,0x60410u,0xbfc60u,0xf0030u,0x30040u,0x60460u,0xbfc10u,0xf0040u,0x30050u,0x60470u,0xbfc00u,0xf0050u,0x30060u,0x60440u,0xbfc30u,0xf0060u,0x30070u,0x60450u,0xbfc20u,0xf0070u,0x30080u,0x604b0u,0xbfcd0u,0xf0080u,0x30090u,0x604a0u,0xbfcc0u,0xf0090u,0x300a0u,0x60490u,0xbfcf0u,0xf00a0u,0x300b0u,0x60480u,0xbfce0u,0xf00b0u,0x300c0u,0x604f0u,0xbfc90u,0xf00c0u,0x300d0u,0x604e0u,0xbfc80u,0xf00d0u,0x300e0u,0x604d0u,0xbfcb0u,0xf00e0u,0x300f0u,0x604c0u,0xbfca0u,0xf00f0u,0x30100u,0x60530u,0xbfd60u,0xf0100u,0x30110u,0x60520u,0xbfd70u,0xf0110u,0x30120u,0x60510u,0xbfd40u,0xf0120u,0x30130u,0x60500u,0xbfd50u,0xf0130u,0x30140u,0x60570u,0xbfd20u,0xf0140u,0x30150u,0x60560u,0xbfd30u,0xf0150u,0x30160u,0x60550u,0xbfd00u,0xf0160u,0x30170u,0x60540u,0xbfd10u,0xf0170u,0x30190u,0x605b0u,0xbfde0u,0xf0190u,0x30180u,0x605a0u,0xbfdf0u,0xf0180u,0x301b0u,0x60590u,0xbfdc0u,0xf01b0u,0x301a0u,0x60580u,0xbfdd0u,0xf01a0u,0x301d0u,0x605f0u,0xbfda0u,0xf01d0u,0x301c0u,0x605e0u,0xbfdb0u,0xf01c0u,0x301f0u,0x605d0u,0xbfd80u,0xf01f0u,0x301e0u,0x605c0u,0xbfd90u,0xf01e0u,0x30210u,0x60640u,0xbfe60u,0xf0210u,0x30200u,0x60650u,0xbfe70u,0xf0200u,0x30230u,0x60660u,0xbfe40u,0xf0230u,0x30220u,0x60670u,0xbfe50u,0xf0220u,0x30250u,0x60600u,0xbfe20u,0xf0250u,0x30240u,0x60610u,0xbfe30u,0xf0240u,0x30270u,0x60620u,0xbfe00u,0xf0270u,0x30260u,0x60630u,0xbfe10u,0xf0260u,0x30290u,0x606c0u,0xbfef0u,0xf0290u,0x30280u,0x606d0u,0xbfee0u,0xf0280u,0x302b0u,0x606e0u,0xbfed0u,0xf02b0u,0x302a0u,0x606f0u,0xbfec0u,0xf02a0u,0x302d0u,0x60680u,0xbfeb0u,0xf02d0u,0x302c0u,0x60690u,0xbfea0u,0xf02c0u,0x302f0u,0x606a0u,0xbfe90u,0xf02f0u,0x302e0u,0x606b0u,0xbfe80u,0xf02e0u,0x30320u,0x60740u,0xbff70u,0x11f720u,0x30330u,0x60750u,0xbff60u,0x11f730u,0x30300u,0x60760u,0xbff50u,0x11f700u,0x30310u,0x60770u,0xbff40u,0x11f710u,0x30360u,0x60700u,0xbff30u,0x11f760u,0x30370u,0x60710u,0xbff20u,0x11f770u,0x30340u,0x60720u,0xbff10u,0x11f740u,0x30350u,0x60730u,0xbff00u,0x11f750u,0x303a0u,0x8fbd0u,0xbfff0u,0x11f7a0u,0x303b0u,0x8fbc0u,0xbffe0u,0x11f7b0u,0x30380u,0x8fbf0u,0xbffd0u,0x11f780u,0x30390u,0x8fbe0u,0xbffc0u,0x11f790u,0x303e0u,0x8fb90u,0xbffb0u,0x11f7e0u,0x303f0u,0x8fb80u,0xbffa0u,0x11f7f0u,0x303c0u,0x8fbb0u,0xbff90u,0x11f7c0u,0x303d0u,0x8fba0u,0xbff80u,0x11f7d0u,0x0u,0x80000000u,0x0u,0x36030u,0x0u,0x80000000u,0x0u,0x36030u,0x0u,0x80000000u,0x0u,0x36030u,0x0u,0x80000000u,0x0u,0x36030u,0x0u,0x80000000u,0x0u,0x36030u,0x0u,0x80000000u,0x0u,0x36030u,0x0u,0x80000000u,0x0u,0x36030u,0x0u,0x80000000u,0x0u,0x36030u,0x0u,0x80000000u,0x0u,0x360b2u,0x0u,0x80000000u,0x0u,0x360b2u,0x0u,0x80000000u,0x0u,0x360b2u,0x0u,0x80000000u,0x0u,0x360b2u,0x0u,0x80000000u,0x0u,0x360b2u,0x0u,0x80000000u,0x0u,0x360b2u,0x0u,0x80000000u,0x0u,0x360b2u,0x0u,0x80000000u,0x0u,0x360b2u,0x0u,0x80000000u,0x0u,0x36092u,0x0u,0x80000000u,0x0u,0x36092u,0x0u,0x80000000u,0x0u,0x36092u,0x0u,0x80000000u,0x0u,0x36092u,0x0u,0x80000000u,0x0u,0x36092u,0x0u,0x80000000u,0x0u,0x36092u,0x0u,0x80000000u,0x0u,0x36092u,0x0u,0x80000000u,0x0u,0x36092u,0x0u,0x80000000u,0x0u,0x3609au,0x0u,0x80000000u,0x0u,0x3609au,0x0u,0x80000000u,0x0u,0x3609au,0x0u,0x80000000u,0x0u,0x3609au,0x0u,0x80000000u,0x0u,0x3609au,0x0u,0x80000000u,0x0u,0x3609au,0x0u,0x80000000u,0x0u,0x3609au,0x0u,0x80000000u,0x0u,0x3609au,0x0u,0x80000000u,0x0u,0x36059u,0x0u,0x80000000u,0x0u,0x36059u,0x0u,0x80000000u,0x0u,0x36059u,0x0u,0x80000000u,0x0u,0x36059u,0x0u,0x80000000u,0x0u,0x36059u,0x0u,0x80000000u,0x0u,0x36059u,0x0u,0x80000000u,0x0u,0x36059u,0x0u,0x80000000u,0x0u,0x36059u,0x0u,0x80000000u,0x0u,0x36059u,0x0u,0x80000000u,0x0u,0x36059u,0x0u,0x80000000u,0x0u,0x36059u,0x0u,0x80000000u,0x0u,0x36059u,0x0u,0x80000000u,0x0u,0x36059u,0x0u,0x80000000u,0x0u,0x36059u,0x0u,0x80000000u,0x0u,0x36059u,0x0u,0x80000000u,0x0u,0x36059u,0x0u,0x80000000u,0x0u,0x16071u,0x0u,0x80000000u,0x0u,0x16071u,0x0u,0x80000000u,0x0u,0x16071u,0x0u,0x80000000u,0x0u,0x16071u,0x0u,0x80000000u,0x0u,0x16071u,0x0u,0x80000000u,0x0u,0x16071u,0x0u,0x80000000u,0x0u,0x16071u,0x0u,0x80000000u,0x0u,0x16071u,0x0u,0x80000000u,0x0u,0x14071u,0x0u,0x80000000u,0x0u,0x14071u,0x0u,0x80000000u,0x0u,0x14071u,0x0u,0x80000000u,0x0u,0x14071u,0x0u,0x80000000u,0x0u,0x14071u,0x0u,0x80000000u,0x0u,0x14071u,0x0u,0x80000000u,0x0u,0x14071u,0x0u,0x80000000u,0x0u,0x14071u};

__global__ __launch_bounds__(256, 1) void swin_main(const uint16_t* x, const uint16_t* mask, char* ws, uint16_t* out,
                                                    const uint16_t* wqkv, const uint16_t* wo, const uint16_t* ls,
                                                    const uint16_t* w1, const uint16_t* w2, const uint16_t* rc,
                                                    const int* ridx) {
  __shared__ __attribute__((aligned(16))) char smem[LDS_BYTES];
  const int wave = __builtin_amdgcn_readfirstlane(threadIdx.x >> 6);
  const unsigned lds0 = (unsigned)(size_t)smem;
  asm volatile(
      ".p2align 5\n"
      "s_mov_b64 s[92:93], %0\n"
      "s_mov_b32 s40, %1\n"
      "s_mov_b32 s41, %2\n"
      "s_mov_b64 s[88:89], %3\n"
      "s_load_dwordx2 s[16:17], s[92:93], 0\n"
      "s_load_dwordx2 s[20:21], s[92:93], 32\n"
      "s_load_dwordx2 s[46:47], s[92:93], 40\n"
      "s_load_dwordx8 s[80:87], s[92:93], 48\n"
      "s_load_dwordx2 s[76:77], s[92:93], 80\n"
      "s_mov_b64 s[90:91], s[92:93]\n"
      "s_getpc_b64 s[98:99]\n"
      "L_ipf0_%=:\n"
      "v_mbcnt_lo_u32_b32 v236, -1, 0\n"
      "v_mbcnt_hi_u32_b32 v236, -1, v236\n"
      "v_lshlrev_b32 v8, 2, v236\n"
      "v_lshlrev_b32_e64 v205, 4, v236\n"
      "s_lshr_b32 s68, s41, 3\n"
      "s_mov_b32 s72, 0x3100117e\n"
      "s_mov_b32 s73, 0xe0bec09e\n"
      "s_mov_b32 s70, 0x315e117e\n"
      "s_mov_b32 s71, 0xe04fc09e\n"
      "s_and_b32 s92, s41, 7\n"
      "s_cmp_eq_u32 s92, 6\n"
      "s_cselect_b32 s93, 2, 17\n"
      "s_cmp_lt_u32 s68, s93\n"
      "s_cselect_b64 s[72:73], s[72:73], s[70:71]\n"
      "s_lshl_b32 s92, s92, 3\n"
      "s_lshr_b64 s[72:73], s[72:73], s92\n"
      "s_add_u32 s41, s72, s68\n"
      "s_and_b32 s41, s41, 255\n"
      "s_movk_i32 s96, 4992\n"
      "s_cmp_lt_u32 s41, 192\n"
      "s_cselect_b32 s44, 20, 19\n"
      "s_cselect_b32 s96, 5137, s96\n"
      "s_cmp_lt_u32 s41, 111\n"
      "s_cselect_b32 s96, 5056, s96\n"
      "s_cmp_lt_u32 s41, 17\n"
      "s_cselect_b32 s44, 21, s44\n"
      "s_cselect_b32 s96, 5167, s96\n"
      "s_add_u32 s96, s96, s41\n"
      "s_lshl_b32 s92, s40, 8\n"
      "v_add_u32_e64 v68, s92, v8\n"
      "v_min_u32 v29, 704, v68\n"
      "v_add_u32 v29, 4096, v29\n"
      "v_min_u32 v68, 672, v68\n"
      "v_add_u32 v154, 122112, v68\n"
      "s_lshl_b32 s92, s68, 7\n"
      "v_lshlrev_b32_e64 v237, 7, v236\n"
      "s_mul_i32 s93, s92, 18\n"
      "s_mul_i32 s94, s40, 640\n"
      "v_min_u32 v238, 512, v237\n"
      "v_add_u32 v238, s94, v238\n"
      "v_min_u32 v238, 2176, v238\n"
      "v_add_u32 v238, s93, v238\n"
      "s_cmp_eq_u32 s40, 1\n"
      "s_cselect_b32 s93, 4096, 0\n"
      "s_add_u32 s92, s92, s93\n"
      "s_cmp_eq_u32 s40, 2\n"
      "s_cselect_b32 s93, 9, 8\n"
      "s_cselect_b64 s[72:73], s[98:99], s[88:89]\n"
      "s_cmp_lt_u32 s40, 2\n"
      "s_cselect_b32 s93, 27, s93\n"
      "s_mul_i32 s92, s92, s93\n"
      "s_lshl_b32 s93, s93, 7\n"
      "s_sub_u32 s93, s93, 128\n"
      "v_min_u32 v237, s93, v237\n"
      "v_add_u32 v237, s92, v237\n"
      "s_lshl_b32 s95, s40, 3\n"
      "v_lshrrev_b32 v239, 3, v236\n"
      "v_and_b32 v138, 7, v236\n"
      "v_add_u32_e64 v139, s95, v239\n"
      "v_bfe_u32 v141, v139, 1, 3\n"
      "v_cmp_lt_u32 s[70:71], s44, v139\n"
      "v_min_u32_e64 v139, s44, v139\n"
      "s_mov_b32 s18, 100270464\n"
      "s_mov_b32 s19, 0x20000\n"
      "s_mov_b32 s78, 9604\n"
      "s_mov_b32 s79, 0x20000\n"
      "s_mov_b32 s22, 221184\n"
      "s_mov_b32 s23, 0x20000\n"
      "s_lshl_b32 s64, s40, 10\n"
      "v_add_u32 v36, s64, v205\n"
      "s_add_u32 s75, s64, 18432\n"
      "s_and_b32 s92, s40, 1\n"
      "s_lshl_b32 s92, s92, 2\n"
      "s_mul_i32 s93, s40, 9216\n"
      "v_and_b32_e64 v140, 2, v239\n"
      "v_xor_b32_e64 v140, v140, v138\n"
      "v_xor_b32_e64 v140, s92, v140\n"
      "v_lshl_add_u32 v140, v140, 4, s93\n"
      "v_mul_u32_u24 v97, 1152, v239\n"
      "v_add_u32_e64 v97, v97, v140\n"
      "s_cmp_lt_u32 s40, 2\n"
      "s_cselect_b32 s65, s64, 163840\n"
      "s_waitcnt lgkmcnt(0)\n"
      "s_cselect_b64 s[72:73], s[20:21], s[72:73]\n"
      "global_load_dword v68, v68, s[86:87]\n"
      "global_load_dword a255, v237, s[72:73]\n"
      "global_load_dword a253, v238, s[46:47]\n"
      "s_mul_i32 s92, s40, 8192\n"
      "s_add_u32 s88, s88, s92\n"
      "s_addc_u32 s89, s89, 0\n"
      "s_add_u32 m0, s64, 124928\n"
      "s_movk_i32 s92, 0\n"
      "buffer_load_dwordx4 v36, s[76:79], s92 offen  lds\n"
      "s_add_u32 m0, s64, 129024\n"
      "s_movk_i32 s92, 4096\n"
      "buffer_load_dwordx4 v36, s[76:79], s92 offen  lds\n"
      "s_add_u32 m0, s64, 133120\n"
      "s_cmp_lt_u32 s40, 2\n"
      "s_cselect_b32 m0, m0, s65\n"
      "s_movk_i32 s92, 8192\n"
      "buffer_load_dwordx4 v36, s[76:79], s92 offen  lds\n"
      "s_load_dwordx16 s[0:15], s[82:83], 0\n"
      "s_load_dwordx16 s[48:63], s[84:85], 0\n"
      "s_load_dwordx16 s[24:39], s[84:85], 64\n"
      "s_load_dwordx2 s[66:67], s[80:81], 0\n"
      "s_add_u32 s93, s41, 1216\n"
      "v_lshl_add_u32 v238, v139, 8, s41\n"
      "v_mul_u32_u24 v237, 192, v139\n"
      "v_add_u32 v237, s93, v237\n"
      "v_cmp_lt_u32 vcc, 18, v139\n"
      "v_mov_b32 v140, s96\n"
      "s_nop 0\n"
      "v_cndmask_b32 v238, v238, v237, vcc\n"
      "v_cmp_eq_u32 vcc, s44, v139\n"
      "s_nop 1\n"
      "v_cndmask_b32 v238, v238, v140, vcc\n"
      "v_subrev_u32 v140, 5184, v238\n"
      "v_cmp_le_u32 vcc, 5184, v238\n"
      "v_mul_u32_u24 v234, 58255, v238\n"
      "v_lshrrev_b32_e64 v234, 22, v234\n"
      "v_mul_u32_u24 v69, 72, v234\n"
      "v_sub_u32 v69, v238, v69\n"
      "v_min_u32 v237, 72, v140\n"
      "v_cndmask_b32 v234, v234, v237, vcc\n"
      "v_subrev_u32 v237, 72, v140\n"
      "v_min_u32 v237, 72, v237\n"
      "v_cndmask_b32 v69, v69, v237, vcc\n"
      "v_mad_u32_u24 v237, v234, 7, 9\n"
      "v_subrev_u32 v238, 0x1ff, v237\n"
      "v_min_u32 v237, v237, v238\n"
      "v_mad_u32_u24 v238, v69, 7, 9\n"
      "v_subrev_u32 v140, 0x1ff, v238\n"
      "v_min_u32 v238, v238, v140\n"
      "v_mul_u32_u24 v237, 0x1ff, v237\n"
      "v_add_u32 v237, v237, v238\n"
      "v_mul_u32_u24 v238, 0x180, v237\n"
      "v_bfrev_b32 v139, 1\n"
      "v_cndmask_b32 v238, v238, v139, s[70:71]\n"
      "v_xor_b32 v141, v141, v138\n"
      "v_lshl_add_u32 v238, v141, 4, v238\n"
      "s_add_u32 m0, s64, 150784\n"
      "s_nop 0\n"
      "buffer_load_dwordx4 v238, s[16:19], 0 offen  lds\n"
      "s_add_u32 m0, s64, 154752\n"
      "s_nop 0\n"
      "buffer_load_dwordx4 v238, s[16:19], 0 offen offset:128  lds\n"
      "s_add_u32 m0, s64, 158720\n"
      "s_nop 0\n"
      "buffer_load_dwordx4 v238, s[16:19], 0 offen offset:256  lds\n"
      "s_movk_i32 s95, 504\n"
      "s_and_b32 s92, s40, 1\n"
      "s_lshl_b32 s92, s92, 2\n"
      "v_lshrrev_b32 v238, 1, v239\n"
      "v_xor_b32 v238, v238, v138\n"
      "v_xor_b32 v238, s92, v238\n"
      "v_lshlrev_b32 v238, 4, v238\n"
      "s_lshl_b32 s92, s40, 3\n"
      "s_brev_b32 s43, 1\n"
      "s_cmp_lt_u32 s40, 2\n"
      "s_cselect_b32 s93, 32, -16\n"
      "s_cselect_b32 s94, 0, 128\n"
      "s_cselect_b32 s43, 256, s43\n"
      "v_add_u32 v237, s92, v239\n"
      "v_mul_u32_u24 v239, 37, v237\n"
      "v_lshrrev_b32 v239, 8, v239\n"
      "v_mad_u32_u24 v239, v239, s95, v237\n"
      "v_mul_u32_u24 v239, 384, v239\n"
      "v_add_u32 v16, v239, v238\n"
      "v_add_u32 v239, s93, v237\n"
      "v_mul_u32_u24 v138, 37, v239\n"
      "v_lshrrev_b32 v138, 8, v138\n"
      "v_mad_u32_u24 v138, v138, s95, v239\n"
      "v_mul_u32_u24 v138, 384, v138\n"
      "v_add3_u32 v17, v138, v238, s94\n"
      "v_add_u32 v239, 16, v237\n"
      "v_mul_u32_u24 v237, 37, v239\n"
      "v_lshrrev_b32_e64 v237, 8, v237\n"
      "v_mad_u32_u24 v237, v237, s95, v239\n"
      "v_mul_u32_u24 v237, 384, v237\n"
      "s_movk_i32 s94, 128\n"
      "v_add3_u32 v18, v237, v238, s94\n"
      "v_add_u32 v19, 256, v16\n"
      "v_add_u32 v15, s43, v17\n"
      "s_add_u32 s92, s41, 0\n"
      "s_mul_i32 s93, s92, 58255\n"
      "s_lshr_b32 s93, s93, 22\n"
      "s_mul_i32 s93, s93, 439\n"
      "s_add_u32 s45, s92, s93\n"
      "s_mul_i32 s45, s45, 2688\n"
      "s_add_u32 s45, s45, 589824\n"
      "s_add_u32 m0, s64, 0\n"
      "s_nop 0\n"
      "buffer_load_dwordx4 v16, s[16:19], s45 offen  lds\n"
      "s_add_u32 m0, s64, 4096\n"
      "s_nop 0\n"
      "buffer_load_dwordx4 v17, s[16:19], s45 offen  lds\n"
      "s_add_u32 m0, s64, 8192\n"
      "s_nop 0\n"
      "buffer_load_dwordx4 v18, s[16:19], s45 offen  lds\n"
      "s_add_u32 m0, s64, 12288\n"
      "s_nop 0\n"
      "buffer_load_dwordx4 v19, s[16:19], s45 offen  lds\n"
      "s_add_u32 m0, s65, 16384\n"
      "s_nop 0\n"
      "buffer_load_dwordx4 v15, s[16:19], s45 offen  lds\n"
      "s_add_u32 s92, s41, 0\n"
      "s_mul_i32 s93, s92, 58255\n"
      "s_lshr_b32 s93, s93, 22\n"
      "s_add_u32 s47, s92, s93\n"
      "s_mul_i32 s47, s47, 4802\n"
      "s_and_b32 s47, s47, -4\n"
      "s_add_u32 s92, s41, 256\n"
      "s_mul_i32 s93, s92, 58255\n"
      "s_lshr_b32 s93, s93, 22\n"
      "s_add_u32 s74, s92, s93\n"
      "s_mul_i32 s74, s74, 4802\n"
      "s_and_b32 s74, s74, -4\n"
      "s_mul_i32 s93, s93, 439\n"
      "s_add_u32 s46, s92, s93\n"
      "s_mul_i32 s46, s46, 2688\n"
      "s_add_u32 s46, s46, 589824\n"
      "v_subrev_u32 v237, 2, v236\n"
      "v_cmp_ge_u32 s[72:73], s44, v237\n"
      "v_min_u32 v237, s44, v237\n"
      "s_add_u32 s93, s41, 1216\n"
      "v_lshl_add_u32 v238, v237, 8, s41\n"
      "v_mul_u32_u24 v239, 192, v237\n"
      "v_add_u32 v239, s93, v239\n"
      "v_cmp_lt_u32 vcc, 18, v237\n"
      "v_mov_b32 v138, s96\n"
      "s_nop 0\n"
      "v_cndmask_b32 v238, v238, v239, vcc\n"
      "v_cmp_eq_u32 vcc, s44, v237\n"
      "s_nop 1\n"
      "v_cndmask_b32 v238, v238, v138, vcc\n"
      "v_subrev_u32 v138, 5184, v238\n"
      "v_cmp_le_u32 vcc, 5184, v238\n"
      "v_mul_u32_u24 v141, 58255, v238\n"
      "v_lshrrev_b32 v141, 22, v141\n"
      "v_mul_u32_u24 v139, 72, v141\n"
      "v_sub_u32_e64 v139, v238, v139\n"
      "v_min_u32 v239, 72, v138\n"
      "v_cndmask_b32 v141, v141, v239, vcc\n"
      "v_subrev_u32 v239, 72, v138\n"
      "v_min_u32 v239, 72, v239\n"
      "v_cndmask_b32 v139, v139, v239, vcc\n"
      "v_mul_u32_u24 v38, 73, v141\n"
      "v_add_u32 v38, v38, v139\n"
      "v_mul_u32_u24 v38, 4802, v38\n"
      "v_mul_u32_u24 v237, 0x1ff, v141\n"
      "v_add_u32_e64 v237, v237, v139\n"
      "v_mul_u32_u24 v237, 2688, v237\n"
      "v_add_u32 v237, 589824, v237\n"
      "v_cmp_eq_u32 vcc, 72, v141\n"
      "v_or_b32 v69, 0x80000000, v237\n"
      "s_nop 0\n"
      "v_cndmask_b32 v237, v237, v69, vcc\n"
      "v_cmp_eq_u32 vcc, 72, v139\n"
      "v_or_b32_e64 v69, 0x40000000, v237\n"
      "s_nop 0\n"
      "v_cndmask_b32 v237, v237, v69, vcc\n"
      "v_mov_b32 v239, 0x20000000\n"
      "v_cndmask_b32 v237, v239, v237, s[72:73]\n"
      "v_cndmask_b32 v38, v239, v38, s[72:73]\n"
      "v_mov_b32 v37, v239\n"
      "s_nop 1\n"
      "v_mov_b32_dpp v37, v237 wave_shr:1 row_mask:0xf bank_mask:0xf\n"
      "v_mov_b32 v79, 0\n"
      "v_mov_b32 v83, 0\n"
      "v_mov_b32 v87, 0\n"
      "v_mov_b32 v91, 0\n"
      "v_mov_b32 v95, 0\n"
      "s_lshl_b32 s94, s40, 2\n"
      "s_add_u32 s94, s94, 115456\n"
      "v_add_u32 v31, s94, v205\n"
      "v_and_b32 v69, 3, v236\n"
      "v_cmp_ne_u32 vcc, 0, v69\n"
      "v_mov_b32 v69, 0x3f803f80\n"
      "s_nop 0\n"
      "v_cndmask_b32_e64 v32, v69, 0, vcc\n"
      "v_mov_b32 v33, v32\n"
      "v_mov_b32 v34, v32\n"
      "v_mov_b32 v35, v32\n"
      "v_and_b32 v115, 15, v236\n"
      "v_lshrrev_b32_e64 v66, 4, v236\n"
      "v_mul_u32_u24 v115, 196, v115\n"
      "v_add_u32 v115, 124928, v115\n"
      "v_lshl_add_u32 v66, v66, 4, v115\n"
      "v_min_u32 v190, 192, v8\n"
      "v_add_u32 v190, 124928, v190\n"
      "global_load_dwordx4 v[236:239], v205, s[88:89]\n"
      "s_waitcnt lgkmcnt(0)\n"
      "s_pack_ll_b32_b16 s92, s0, s8\n"
      "s_waitcnt vmcnt(14)\n"
      "v_dot2_f32_bf16 v234, s92, v68, 0\n"
      "s_pack_hh_b32_b16 s93, s0, s8\n"
      "v_dot2_f32_bf16 v157, s93, v68, 0\n"
      "s_pack_ll_b32_b16 s94, s1, s9\n"
      "v_dot2_f32_bf16 v232, s94, v68, 0\n"
      "s_pack_hh_b32_b16 s95, s1, s9\n"
      "v_dot2_f32_bf16 v235, s95, v68, 0\n"
      "s_pack_ll_b32_b16 s92, s2, s10\n"
      "v_dot2_f32_bf16 v233, s92, v68, 0\n"
      "s_pack_hh_b32_b16 s93, s2, s10\n"
      "v_dot2_f32_bf16 v156, s93, v68, 0\n"
      "s_pack_ll_b32_b16 s94, s3, s11\n"
      "v_dot2_f32_bf16 v155, s94, v68, 0\n"
      "s_pack_hh_b32_b16 s95, s3, s11\n"
      "v_dot2_f32_bf16 v132, s95, v68, 0\n"
      "s_pack_ll_b32_b16 s92, s4, s12\n"
      "v_dot2_f32_bf16 v214, s92, v68, 0\n"
      "s_pack_hh_b32_b16 s93, s4, s12\n"
      "v_dot2_f32_bf16 v133, s93, v68, 0\n"
      "s_pack_ll_b32_b16 s94, s5, s13\n"
      "v_dot2_f32_bf16 v213, s94, v68, 0\n"
      "global_load_dwordx4 v[138:141], v205, s[88:89] offset:1024\n"
      "s_pack_hh_b32_b16 s95, s5, s13\n"
      "v_dot2_f32_bf16 v196, s95, v68, 0\n"
      "s_pack_ll_b32_b16 s92, s6, s14\n"
      "v_dot2_f32_bf16 v131, s92, v68, 0\n"
      "s_pack_hh_b32_b16 s93, s6, s14\n"
      "v_dot2_f32_bf16 v59, s93, v68, 0\n"
      "s_pack_ll_b32_b16 s94, s7, s15\n"
      "v_dot2_f32_bf16 v109, s94, v68, 0\n"
      "s_pack_hh_b32_b16 s95, s7, s15\n"
      "v_dot2_f32_bf16 v195, s95, v68, 0\n"
      "s_lshl_b32 s92, s40, 4\n"
      "s_lshr_b64 s[66:67], s[66:67], s92\n"
      "s_lshl_b32 s66, s66, 16\n"
      "v_mov_b32 v68, s66\n"
      "v_max_f32 v68, 0xc2c80000, v68\n"
      "v_min_f32 v68, 0x40930000, v68\n"
      "v_mul_f32 v68, 0x3fb8aa3b, v68\n"
      "v_exp_f32 v68, v68\n"
      "s_nop 0\n"
      "v_cvt_pk_bf16_f32 v68, v68, v68\n"
      "v_lshlrev_b32 v68, 16, v68\n"
      "v_max_f32 v234, 0, v234\n"
      "v_max_f32_e64 v157, 0, v157\n"
      "global_load_dwordx4 v[0:3], v205, s[88:89] offset:2048\n"
      "v_cvt_pk_bf16_f32 v234, v234, v157\n"
      "v_max_f32 v232, 0, v232\n"
      "v_max_f32 v235, 0, v235\n"
      "v_cvt_pk_bf16_f32 v232, v232, v235\n"
      "v_max_f32 v233, 0, v233\n"
      "v_max_f32 v156, 0, v156\n"
      "v_cvt_pk_bf16_f32 v233, v233, v156\n"
      "v_max_f32 v155, 0, v155\n"
      "v_max_f32 v132, 0, v132\n"
      "v_cvt_pk_bf16_f32 v155, v155, v132\n"
      "v_max_f32 v214, 0, v214\n"
      "v_max_f32 v133, 0, v133\n"
      "v_cvt_pk_bf16_f32 v214, v214, v133\n"
      "v_max_f32 v213, 0, v213\n"
      "v_max_f32 v196, 0, v196\n"
      "v_cvt_pk_bf16_f32 v213, v213, v196\n"
      "v_max_f32 v131, 0, v131\n"
      "v_max_f32 v59, 0, v59\n"
      "v_cvt_pk_bf16_f32 v131, v131, v59\n"
      "v_max_f32 v109, 0, v109\n"
      "v_max_f32 v195, 0, v195\n"
      "v_cvt_pk_bf16_f32 v109, v109, v195\n"
      "s_mov_b32 s42, 0\n"
      "s_add_u32 m0, s75, 0\n"
      "s_nop 0\n"
      "buffer_load_dwordx4 v97, s[20:23], s42 offen  lds\n"
      "s_pack_ll_b32_b16 s92, s48, s50\n"
      "v_dot2_f32_bf16 v235, s92, v234, 0\n"
      "s_pack_hh_b32_b16 s93, s48, s50\n"
      "v_dot2_f32_bf16 v156, s93, v234, 0\n"
      "s_pack_ll_b32_b16 s94, s49, s51\n"
      "v_dot2_f32_bf16 v157, s94, v234, 0\n"
      "s_pack_hh_b32_b16 s95, s49, s51\n"
      "v_dot2_f32_bf16 v133, s95, v234, 0\n"
      "s_pack_ll_b32_b16 s92, s52, s54\n"
      "v_dot2_f32_bf16 v235, s92, v232, v235\n"
      "s_pack_hh_b32_b16 s93, s52, s54\n"
      "v_dot2_f32_bf16 v156, s93, v232, v156\n"
      "s_pack_ll_b32_b16 s94, s53, s55\n"
      "v_dot2_f32_bf16 v157, s94, v232, v157\n"
      "s_pack_hh_b32_b16 s95, s53, s55\n"
      "v_dot2_f32_bf16 v133, s95, v232, v133\n"
      "s_pack_ll_b32_b16 s92, s56, s58\n"
      "v_dot2_f32_bf16 v235, s92, v233, v235\n"
      "s_pack_hh_b32_b16 s93, s56, s58\n"
      "v_dot2_f32_bf16 v156, s93, v233, v156\n"
      "s_pack_ll_b32_b16 s94, s57, s59\n"
      "v_dot2_f32_bf16 v157, s94, v233, v157\n"
      "s_add_u32 m0, s75, 3968\n"
      "s_nop 0\n"
      "buffer_load_dwordx4 v97, s[20:23], s42 offen offset:128  lds\n"
      "s_pack_hh_b32_b16 s95, s57, s59\n"
      "v_dot2_f32_bf16 v133, s95, v233, v133\n"
      "s_pack_ll_b32_b16 s92, s60, s62\n"
      "v_dot2_f32_bf16 v235, s92, v155, v235\n"
      "s_pack_hh_b32_b16 s93, s60, s62\n"
      "v_dot2_f32_bf16 v156, s93, v155, v156\n"
      "s_pack_ll_b32_b16 s94, s61, s63\n"
      "v_dot2_f32_bf16 v157, s94, v155, v157\n"
      "s_pack_hh_b32_b16 s95, s61, s63\n"
      "v_dot2_f32_bf16 v133, s95, v155, v133\n"
      "s_pack_ll_b32_b16 s92, s24, s26\n"
      "v_dot2_f32_bf16 v235, s92, v214, v235\n"
      "s_pack_hh_b32_b16 s93, s24, s26\n"
      "v_dot2_f32_bf16 v156, s93, v214, v156\n"
      "s_pack_ll_b32_b16 s94, s25, s27\n"
      "v_dot2_f32_bf16 v157, s94, v214, v157\n"
      "s_pack_hh_b32_b16 s95, s25, s27\n"
      "v_dot2_f32_bf16 v133, s95, v214, v133\n"
      "s_pack_ll_b32_b16 s92, s28, s30\n"
      "v_dot2_f32_bf16 v235, s92, v213, v235\n"
      "s_pack_hh_b32_b16 s93, s28, s30\n"
      "v_dot2_f32_bf16 v156, s93, v213, v156\n"
      "s_pack_ll_b32_b16 s94, s29, s31\n"
      "s_add_u32 m0, s75, 7936\n"
      "s_nop 0\n"
      "buffer_load_dwordx4 v97, s[20:23], s42 offen offset:256  lds\n"
      "v_dot2_f32_bf16 v157, s94, v213, v157\n"
      "s_pack_hh_b32_b16 s95, s29, s31\n"
      "v_dot2_f32_bf16 v133, s95, v213, v133\n"
      "s_pack_ll_b32_b16 s92, s32, s34\n"
      "v_dot2_f32_bf16 v235, s92, v131, v235\n"
      "s_pack_hh_b32_b16 s93, s32, s34\n"
      "v_dot2_f32_bf16 v156, s93, v131, v156\n"
      "s_pack_ll_b32_b16 s94, s33, s35\n"
      "v_dot2_f32_bf16 v157, s94, v131, v157\n"
      "s_pack_hh_b32_b16 s95, s33, s35\n"
      "v_dot2_f32_bf16 v133, s95, v131, v133\n"
      "s_pack_ll_b32_b16 s92, s36, s38\n"
      "v_dot2_f32_bf16 v235, s92, v109, v235\n"
      "s_pack_hh_b32_b16 s93, s36, s38\n"
      "v_dot2_f32_bf16 v156, s93, v109, v156\n"
      "s_pack_ll_b32_b16 s94, s37, s39\n"
      "v_dot2_f32_bf16 v157, s94, v109, v157\n"
      "s_pack_hh_b32_b16 s95, s37, s39\n"
      "v_dot2_f32_bf16 v133, s95, v109, v133\n"
      "s_load_dwordx2 s[28:29], s[90:91], 8\n"
      "s_load_dwordx2 s[34:35], s[90:91], 16\n"
      "s_load_dwordx2 s[24:25], s[90:91], 24\n"
      "s_add_u32 m0, s75, 11904\n"
      "s_nop 0\n"
      "buffer_load_dwordx4 v97, s[20:23], s42 offen offset:384  lds\n"
      "s_load_dwordx2 s[36:37], s[90:91], 40\n"
      "s_mov_b32 s26, 100270464\n"
      "s_mov_b32 s27, 0x20000\n"
      "s_mov_b32 s38, 73728\n"
      "s_mov_b32 s39, 0x20000\n"
      "s_mov_b32 s30, 25589860\n"
      "s_mov_b32 s31, 0x20000\n"
      "s_mov_b32 s53, 0x3fb8aa3b\n"
      "s_mov_b32 s51, 0x2a8cbccc\n"
      "s_mov_b32 s52, 0x38fba882\n"
      "s_mov_b32 s58, 0xffff0000\n"
      "s_mov_b32 s59, 0x0\n"
      "s_mov_b32 s60, 0x0\n"
      "s_mov_b32 s61, 0xffff\n"
      "s_mov_b32 s62, 0x0\n"
      "s_mov_b32 s63, 0xffff0000\n"
      "v_mov_b32 v69, 0x41f00000\n"
      "v_cmp_nle_f32 s[54:55], v68, v69\n"
      "v_mul_f32 v68, s53, v68\n"
      "s_nop 0\n"
      "v_readfirstlane_b32 s50, v68\n"
      "v_cvt_pk_bf16_f32 v235, v235, v235\n"
      "v_lshlrev_b32 v235, 16, v235\n"
      "s_add_u32 m0, s75, 15872\n"
      "s_nop 0\n"
      "buffer_load_dwordx4 v97, s[20:23], s42 offen offset:512  lds\n"
      "v_mul_f32 v235, 0xbfb8aa3b, v235\n"
      "v_exp_f32 v235, v235\n"
      "s_nop 0\n"
      "v_add_f32 v235, 1.0, v235\n"
      "v_rcp_f32 v235, v235\n"
      "s_nop 0\n"
      "v_cvt_pk_bf16_f32 v235, v235, v235\n"
      "v_lshlrev_b32 v235, 16, v235\n"
      "v_add_f32 v235, -0.5, v235\n"
      "v_mul_f32 v235, 0x41b8aa3b, v235\n"
      "ds_write_b32 v154, v235\n"
      "v_cvt_pk_bf16_f32 v156, v156, v156\n"
      "v_lshlrev_b32 v156, 16, v156\n"
      "v_mul_f32 v156, 0xbfb8aa3b, v156\n"
      "v_exp_f32 v156, v156\n"
      "s_nop 0\n"
      "v_add_f32 v156, 1.0, v156\n"
      "v_rcp_f32_e64 v156, v156\n"
      "s_nop 0\n"
      "v_cvt_pk_bf16_f32 v156, v156, v156\n"
      "v_lshlrev_b32 v156, 16, v156\n"
      "v_add_f32 v156, -0.5, v156\n"
      "v_mul_f32 v156, 0x41b8aa3b, v156\n"
      "ds_write_b32 v154, v156 offset:704\n"
      "v_cvt_pk_bf16_f32 v157, v157, v157\n"
      "v_lshlrev_b32 v157, 16, v157\n"
      "s_add_u32 m0, s75, 19840\n"
      "s_nop 0\n"
      "buffer_load_dwordx4 v97, s[20:23], s42 offen offset:640  lds\n"
      "v_mul_f32 v157, 0xbfb8aa3b, v157\n"
      "v_exp_f32 v157, v157\n"
      "s_nop 0\n"
      "v_add_f32 v157, 1.0, v157\n"
      "v_rcp_f32_e64 v157, v157\n"
      "s_nop 0\n"
      "v_cvt_pk_bf16_f32 v157, v157, v157\n"
      "v_lshlrev_b32 v157, 16, v157\n"
      "v_add_f32 v157, -0.5, v157\n"
      "v_mul_f32 v157, 0x41b8aa3b, v157\n"
      "ds_write_b32 v154, v157 offset:1408\n"
      "v_cvt_pk_bf16_f32 v133, v133, v133\n"
      "v_lshlrev_b32 v133, 16, v133\n"
      "v_mul_f32 v133, 0xbfb8aa3b, v133\n"
      "v_exp_f32_e64 v133, v133\n"
      "s_nop 0\n"
      "v_add_f32 v133, 1.0, v133\n"
      "v_rcp_f32_e64 v133, v133\n"
      "s_nop 0\n"
      "v_cvt_pk_bf16_f32 v133, v133, v133\n"
      "v_lshlrev_b32_e64 v133, 16, v133\n"
      "v_add_f32_e64 v133, -0.5, v133\n"
      "v_mul_f32 v133, 0x41b8aa3b, v133\n"
      "ds_write_b32 v154, v133 offset:2112\n"
      "s_mul_i32 s76, s40, 704\n"
      "s_add_u32 s76, s76, 122112\n"
      "s_mov_b32 s43, 36864\n"
      "s_add_u32 m0, s75, 24576\n"
      "s_nop 0\n"
      "buffer_load_dwordx4 v97, s[20:23], s43 offen  lds\n"
      "s_add_u32 m0, s75, 28544\n"
      "s_nop 0\n"
      "buffer_load_dwordx4 v97, s[20:23], s43 offen offset:128  lds\n"
      "s_add_u32 m0, s75, 32512\n"
      "s_nop 0\n"
      "buffer_load_dwordx4 v97, s[20:23], s43 offen offset:256  lds\n"
      "s_add_u32 m0, s75, 36480\n"
      "s_nop 0\n"
      "buffer_load_dwordx4 v97, s[20:23], s43 offen offset:384  lds\n"
      "s_add_u32 m0, s75, 40448\n"
      "s_nop 0\n"
      "buffer_load_dwordx4 v97, s[20:23], s43 offen offset:512  lds\n"
      "s_add_u32 m0, s75, 44416\n"
      "s_nop 0\n"
      "buffer_load_dwordx4 v97, s[20:23], s43 offen offset:640  lds\n"
      "s_mov_b32 s45, s46\n"
      "s_lshl_b32 s9, s68, 1\n"
      "s_add_u32 s9, s9, s40\n"
      "s_mul_i32 s9, s9, 640\n"
      "s_mov_b32 s49, s47\n"
      "s_mov_b32 s66, 0\n"
      "s_mov_b32 s67, 0\n"
      "s_mov_b32 s68, 0\n"
      "s_mov_b32 s69, 0\n"
      "s_mov_b32 s70, 0\n"
      "s_mov_b32 s71, 0\n"
      "s_mov_b32 s48, 0\n"
      "s_mov_b32 s33, 0x7fff7fff\n"
      "s_mov_b32 s42, 1\n"
      "s_mov_b32 s43, 5\n"
      "v_readlane_b32 s47, v38, 4\n"
      "s_nop 0\n"
      "s_and_b32 s47, s47, -4\n"
      "s_waitcnt vmcnt(12)\n"
      "v_add_u32 v135, 150784, v0\n"
      "v_add_u32 v184, 150784, v1\n"
      "v_add_u32 v85, 49152, v236\n"
      "v_add_u32 v84, 49152, v237\n"
      "v_add_u32 v174, 49152, v238\n"
      "v_add_u32 v82, 49152, v239\n"
      "v_add_u32 v78, 49152, v138\n"
      "s_waitcnt vmcnt(6)\n"
      "s_waitcnt lgkmcnt(0)\n"
      "s_barrier\n"
      "s_add_u32 m0, s75, 49152\n"
      "s_mov_b32 s78, 73728\n"
      "buffer_load_dwordx4 v97, s[20:23], s78 offen  lds\n"
      "s_add_u32 m0, s75, 53120\n"
      "ds_read_b128 a[216:219], v135\n"
      "ds_read_b64_tr_b16 a[48:49], v238\n"
      "ds_read_b128 a[220:223], v0\n"
      "ds_read_b64_tr_b16 a[2:3], v236 offset:512\n"
      "ds_read_b64_tr_b16 a[72:73], v236 offset:12288\n"
      "ds_read_b64_tr_b16 a[74:75], v236 offset:12800\n"
      "buffer_load_dwordx4 v97, s[20:23], s78 offen offset:128  lds\n"
      "ds_read_b64_tr_b16 a[0:1], v236\n"
      "ds_read_b64_tr_b16 a[50:51], v238 offset:512\n"
      "ds_read_b64_tr_b16 a[24:25], v237\n"
      "v_add_u32 v178, 6272, v66\n"
      "s_add_u32 m0, s75, 57088\n"
      "ds_read_b128 a[224:227], v0 offset:2048\n"
      "ds_read_b64_tr_b16 a[26:27], v237 offset:512\n"
      "buffer_load_dwordx4 v97, s[20:23], s78 offen offset:256  lds\n"
      "ds_read2_b32 v[68:69], v178 offset0:16 offset1:17\n"
      "ds_read_b32 v223, v115 offset:3328\n"
      "ds_read_b128 a[244:247], v1 offset:4096\n"
      "ds_read_b64_tr_b16 a[120:121], v238 offset:12288\n"
      "s_add_u32 m0, s75, 61056\n"
      "ds_read_b64_tr_b16 a[122:123], v238 offset:12800\n"
      "s_waitcnt lgkmcnt(8)\n"
      "v_mfma_f32_16x16x32_bf16 v[232:235], a[48:51], a[216:219], 0\n"
      "buffer_load_dwordx4 v97, s[20:23], s78 offen offset:384  lds\n"
      "ds_read_b64_tr_b16 a[98:99], v237 offset:12800\n"
      "v_mfma_f32_16x16x32_bf16 v[154:157], a[0:3], a[220:223], 0\n"
      "s_mov_b32 s79, 110592\n"
      "v_add_u32 v74, 3136, v66\n"
      "s_waitcnt lgkmcnt(6)\n"
      "v_mfma_f32_16x16x32_bf16 v[130:133], a[24:27], a[224:227], 0\n"
      "s_add_u32 m0, s75, 65024\n"
      "ds_read_b128 a[228:231], v0 offset:4096\n"
      "ds_read_b64_tr_b16 a[96:97], v237 offset:12288\n"
      "v_mfma_f32_16x16x32_bf16 v[194:197], a[24:27], a[220:223], 0\n"
      "buffer_load_dwordx4 v97, s[20:23], s78 offen offset:512  lds\n"
      "v_mfma_f32_16x16x32_bf16 v[212:215], a[48:51], a[224:227], 0\n"
      "ds_read_b128 a[236:239], v1\n"
      "ds_read2_b32 v[58:59], v74 offset0:18 offset1:19\n"
      "v_mfma_f32_16x16x32_bf16 v[108:111], a[0:3], a[224:227], 0\n"
      "ds_read_b128 a[232:235], v184\n"
      "s_add_u32 m0, s75, 68992\n"
      "ds_read2_b32 v[70:71], v178 offset0:18 offset1:19\n"
      "v_mfma_f32_16x16x32_bf16 v[166:169], a[72:75], a[220:223], 0\n"
      "buffer_load_dwordx4 v97, s[20:23], s78 offen offset:640  lds\n"
      "v_mfma_f32_16x16x32_bf16 v[244:247], a[0:3], a[216:219], 0\n"
      "ds_read_b128 a[252:255], v0 offset:6144\n"
      "s_waitcnt lgkmcnt(5)\n"
      "v_mfma_f32_16x16x32_bf16 v[240:243], a[96:99], a[224:227], 0\n"
      "ds_read2_b32 v[72:73], v178 offset0:32 offset1:33\n"
      "ds_read_b128 a[240:243], v1 offset:2048\n"
      "v_mfma_f32_16x16x32_bf16 v[252:255], a[96:99], a[228:231], 0\n"
      "s_waitcnt vmcnt(6)\n"
      "s_barrier\n"
      "global_load_dwordx4 v[4:7], v205, s[88:89] offset:3072\n"
      "ds_read_b64_tr_b16 a[4:5], v236 offset:24576\n"
      "ds_read_b64_tr_b16 a[28:29], v237 offset:24576\n"
      "s_add_u32 s88, s88, 4096\n"
      "s_addc_u32 s89, s89, 0\n"
      "ds_read_b64_tr_b16 a[54:55], v238 offset:25088\n"
      "ds_read_b64_tr_b16 a[124:125], v238 offset:36864\n"
      "ds_read_b64_tr_b16 a[126:127], v238 offset:37376\n"
      "ds_read_b64_tr_b16 a[100:101], v237 offset:36864\n"
      "v_mfma_f32_16x16x32_bf16 v[180:183], a[72:75], a[224:227], 0\n"
      "ds_read_b64_tr_b16 a[102:103], v237 offset:37376\n"
      "global_load_dwordx4 v[8:11], v205, s[88:89]\n"
      "ds_read_b64_tr_b16 a[52:53], v238 offset:24576\n"
      "ds_read_b64_tr_b16 a[76:77], v236 offset:36864\n"
      "v_mfma_f32_16x16x32_bf16 v[150:153], a[120:123], a[224:227], 0\n"
      "ds_read_b64_tr_b16 a[30:31], v237 offset:25088\n"
      "ds_read_b64_tr_b16 a[78:79], v236 offset:37376\n"
      "ds_read_b64_tr_b16 a[6:7], v236 offset:25088\n"
      "v_mfma_f32_16x16x32_bf16 v[224:227], a[96:99], a[216:219], 0\n"
      "global_load_dwordx4 v[12:15], v205, s[88:89] offset:1024\n"
      "v_mfma_f32_16x16x32_bf16 v[248:251], a[120:123], a[216:219], 0\n"
      "ds_read2_b32 v[50:51], v66 offset0:34 offset1:35\n"
      "ds_read2_b32 v[52:53], v74 offset0:0 offset1:1\n"
      "ds_read_b32 v211, v115 offset:192\n"
      "v_mfma_f32_16x16x32_bf16 v[216:219], a[72:75], a[216:219], 0\n"
      "ds_read_b128 a[224:227], v184 offset:4096\n"
      "v_lshl_add_u32 v223, v223, 2, s76\n"
      "v_mfma_f32_16x16x32_bf16 v[100:103], a[0:3], a[228:231], 0\n"
      "global_load_dwordx4 v[20:23], v205, s[88:89] offset:2048\n"
      "ds_read_b32 v177, v190 offset:9408\n"
      "v_mfma_f32_16x16x32_bf16 v[120:123], a[120:123], a[220:223], 0\n"
      "ds_read2_b32 v[42:43], v66 offset0:2 offset1:3\n"
      "s_waitcnt lgkmcnt(6)\n"
      "v_mfma_f32_16x16x32_bf16 v[120:123], a[124:127], a[236:239], v[120:123]\n"
      "ds_read2_b32 v[62:63], v74 offset0:34 offset1:35\n"
      "ds_read_b128 a[248:251], v135 offset:4096\n"
      "v_mfma_f32_16x16x32_bf16 v[190:193], a[96:99], a[220:223], 0\n"
      "global_load_dwordx4 v[24:27], v205, s[88:89] offset:3072\n"
      "v_mfma_f32_16x16x32_bf16 v[146:149], a[48:51], a[220:223], 0\n"
      "ds_read2_b32 v[44:45], v66 offset0:16 offset1:17\n"
      "s_mov_b32 s82, 0xffff\n"
      "ds_read2_b32 v[46:47], v66 offset0:18 offset1:19\n"
      "v_mfma_f32_16x16x32_bf16 v[146:149], a[52:55], a[236:239], v[146:149]\n"
      "ds_read2_b32 v[60:61], v74 offset0:32 offset1:33\n"
      "v_lshl_add_u32 v73, v73, 2, s76\n"
      "s_add_u32 m0, s75, 73728\n"
      "v_mfma_f32_16x16x32_bf16 v[190:193], a[100:103], a[236:239], v[190:193]\n"
      "buffer_load_dwordx4 v97, s[20:23], s79 offen  lds\n"
      "v_mfma_f32_16x16x32_bf16 v[204:207], a[24:27], a[216:219], 0\n"
      "s_add_u32 m0, s75, 77696\n"
      "ds_read2_b32 v[54:55], v74 offset0:2 offset1:3\n"
      "v_mfma_f32_16x16x32_bf16 v[154:157], a[4:7], a[236:239], v[154:157]\n"
      "ds_read2_b32 v[48:49], v66 offset0:32 offset1:33\n"
      "s_mov_b32 s78, 147456\n"
      "v_mfma_f32_16x16x32_bf16 v[194:197], a[28:31], a[236:239], v[194:197]\n"
      "ds_read2_b32 v[56:57], v74 offset0:16 offset1:17\n"
      "ds_read2_b32 v[74:75], v178 offset0:34 offset1:35\n"
      "v_mfma_f32_16x16x32_bf16 v[232:235], a[52:55], a[232:235], v[232:235]\n"
      "buffer_load_dwordx4 v97, s[20:23], s79 offen offset:128  lds\n"
      "v_mfma_f32_16x16x32_bf16 v[204:207], a[28:31], a[232:235], v[204:207]\n"
      "ds_read_b32 v179, v115 offset:6464\n"
      "v_lshl_add_u32 v70, v70, 2, s76\n"
      "v_mfma_f32_16x16x32_bf16 v[112:115], a[72:75], a[228:231], 0\n"
      "ds_read2_b32 v[64:65], v178 offset0:0 offset1:1\n"
      "s_add_u32 m0, s75, 81664\n"
      "v_mfma_f32_16x16x32_bf16 v[108:111], a[4:7], a[240:243], v[108:111]\n"
      "ds_read_b128 a[216:219], v0 offset:8192\n"
      "s_mov_b32 s91, 0x1ffff\n"
      "v_mfma_f32_16x16x32_bf16 v[244:247], a[4:7], a[232:235], v[244:247]\n"
      "buffer_load_dwordx4 v97, s[20:23], s79 offen offset:256  lds\n"
      "v_mfma_f32_16x16x32_bf16 v[240:243], a[100:103], a[240:243], v[240:243]\n"
      "s_mov_b32 s83, 0x0\n"
      "s_waitcnt lgkmcnt(4)\n"
      "v_lshl_add_u32 v63, v63, 2, s76\n"
      "v_mfma_f32_16x16x32_bf16 v[216:219], a[76:79], a[232:235], v[216:219]\n"
      "s_mov_b32 s90, 0xffffffff\n"
      "v_lshl_add_u32 v177, v177, 2, s76\n"
      "v_lshl_add_u32 v46, v46, 2, s76\n"
      "v_mfma_f32_16x16x32_bf16 v[166:169], a[76:79], a[236:239], v[166:169]\n"
      "ds_read_b128 a[236:239], v1 offset:10240\n"
      "v_lshl_add_u32 v54, v54, 2, s76\n"
      "s_add_u32 m0, s75, 85632\n"
      "v_mfma_f32_16x16x32_bf16 v[116:119], a[120:123], a[228:231], 0\n"
      "buffer_load_dwordx4 v97, s[20:23], s79 offen offset:384  lds\n"
      "s_add_u32 m0, s75, 89600\n"
      "v_mfma_f32_16x16x32_bf16 v[116:119], a[124:127], a[244:247], v[116:119]\n"
      "s_waitcnt vmcnt(9)\n"
      "s_barrier\n"
      "ds_read_b64_tr_b16 a[8:9], v85\n"
      "ds_read_b64_tr_b16 a[104:105], v84 offset:12288\n"
      "ds_read_b64_tr_b16 a[128:129], v174 offset:12288\n"
      "ds_read_b64_tr_b16 a[10:11], v85 offset:512\n"
      "ds_read_b64_tr_b16 a[32:33], v84\n"
      "v_mfma_f32_16x16x32_bf16 v[170:173], a[48:51], a[228:231], 0\n"
      "ds_read_b64_tr_b16 a[56:57], v174\n"
      "ds_read_b64_tr_b16 a[34:35], v84 offset:512\n"
      "buffer_load_dwordx4 v97, s[20:23], s79 offen offset:512  lds\n"
      "ds_read_b64_tr_b16 a[80:81], v85 offset:12288\n"
      "v_mfma_f32_16x16x32_bf16 v[130:133], a[28:31], a[240:243], v[130:133]\n"
      "ds_read_b64_tr_b16 a[130:131], v174 offset:12800\n"
      "ds_read_b64_tr_b16 a[58:59], v174 offset:512\n"
      "s_add_u32 m0, s75, 93568\n"
      "ds_read_b64_tr_b16 a[106:107], v84 offset:12800\n"
      "ds_read_b64_tr_b16 a[82:83], v85 offset:12800\n"
      "buffer_load_dwordx4 v97, s[20:23], s79 offen offset:640  lds\n"
      "v_lshl_add_u32 v49, v49, 2, s76\n"
      "v_lshl_add_u32 v55, v55, 2, s76\n"
      "v_mfma_f32_16x16x32_bf16 v[228:231], a[24:27], a[228:231], 0\n"
      "ds_read_b32 v177, v177\n"
      "v_lshl_add_u32 v50, v50, 2, s76\n"
      "v_mfma_f32_16x16x32_bf16 v[224:227], a[100:103], a[232:235], v[224:227]\n"
      "ds_read2_b32 v[40:41], v66 offset0:0 offset1:1\n"
      "s_add_u32 m0, s75, 0\n"
      "v_lshl_add_u32 v71, v71, 2, s76\n"
      "v_mfma_f32_16x16x32_bf16 v[252:255], a[100:103], a[244:247], v[252:255]\n"
      "buffer_load_dwordx4 v97, s[20:23], s78 offen  lds\n"
      "v_lshl_add_u32 v211, v211, 2, s76\n"
      "v_mfma_f32_16x16x32_bf16 v[180:183], a[76:79], a[240:243], v[180:183]\n"
      "s_add_u32 m0, s75, 3968\n"
      "v_lshl_add_u32 v61, v61, 2, s76\n"
      "v_lshl_add_u32 v58, v58, 2, s76\n"
      "v_mfma_f32_16x16x32_bf16 v[212:215], a[52:55], a[240:243], v[212:215]\n"
      "ds_read_b128 a[220:223], v0 offset:10240\n"
      "s_waitcnt lgkmcnt(3)\n"
      "v_lshl_add_u32 v65, v65, 2, s76\n"
      "v_lshl_add_u32 v72, v72, 2, s76\n"
      "v_mfma_f32_16x16x32_bf16 v[248:251], a[124:127], a[232:235], v[248:251]\n"
      "buffer_load_dwordx4 v97, s[20:23], s78 offen offset:128  lds\n"
      "v_mfma_f32_16x16x32_bf16 v[248:251], a[128:131], a[248:251], v[248:251]\n"
      "ds_read_b128 a[228:231], v1 offset:6144\n"
      "v_lshl_add_u32 v69, v69, 2, s76\n"
      "v_mfma_f32_16x16x32_bf16 v[108:111], a[8:11], a[216:219], v[108:111]\n"
      "ds_read2_b32 v[66:67], v178 offset0:2 offset1:3\n"
      "v_lshl_add_u32 v43, v43, 2, s76\n"
      "v_mfma_f32_16x16x32_bf16 v[150:153], a[124:127], a[240:243], v[150:153]\n"
      "s_add_u32 m0, s75, 7936\n"
      "ds_read_b32 v73, v73\n"
      "v_mfma_f32_16x16x32_bf16 v[100:103], a[4:7], a[244:247], v[100:103]\n"
      "buffer_load_dwordx4 v97, s[20:23], s78 offen offset:256  lds\n"
      "v_mfma_f32_16x16x32_bf16 v[190:193], a[104:107], a[252:255], v[190:193]\n"
      "ds_read_b32 v72, v72\n"
      "s_waitcnt lgkmcnt(4)\n"
      "v_mfma_f32_16x16x32_bf16 v[116:119], a[128:131], a[220:223], v[116:119]\n"
      "ds_read_b128 a[232:235], v1 offset:8192\n"
      "v_lshl_add_u32 v41, v41, 2, s76\n"
      "v_mfma_f32_16x16x32_bf16 v[146:149], a[56:59], a[252:255], v[146:149]\n"
      "s_add_u32 m0, s75, 11904\n"
      "ds_read_b32 v50, v50\n"
      "v_lshl_add_u32 v51, v51, 2, s76\n"
      "v_mfma_f32_16x16x32_bf16 v[112:115], a[76:79], a[244:247], v[112:115]\n"
      "buffer_load_dwordx4 v97, s[20:23], s78 offen offset:384  lds\n"
      "v_mfma_f32_16x16x32_bf16 v[240:243], a[104:107], a[216:219], v[240:243]\n"
      "s_add_u32 m0, s75, 15872\n"
      "ds_read_b32 v71, v71\n"
      "v_mfma_f32_16x16x32_bf16 v[224:227], a[104:107], a[248:251], v[224:227]\n"
      "ds_read_b32 v61, v61\n"
      "v_lshl_add_u32 v47, v47, 2, s76\n"
      "v_mfma_f32_16x16x32_bf16 v[180:183], a[80:83], a[216:219], v[180:183]\n"
      "ds_read_b128 a[240:243], v135 offset:8192\n"
      "v_readlane_b32 s86, v38, s43\n"
      "v_mfma_f32_16x16x32_bf16 v[212:215], a[56:59], a[216:219], v[212:215]\n"
      "buffer_load_dwordx4 v97, s[20:23], s78 offen offset:512  lds\n"
      "v_lshl_add_u32 v42, v42, 2, s76\n"
      "s_mov_b32 s79, 184320\n"
      "v_mfma_f32_16x16x32_bf16 v[204:207], a[32:35], a[248:251], v[204:207]\n"
      "s_add_u32 m0, s75, 19840\n"
      "v_lshl_add_u32 v40, v40, 2, s76\n"
      "v_lshl_add_u32 v75, v75, 2, s76\n"
      "v_mov_b32 v209, 0xf149f2ca\n"
      "v_mfma_f32_16x16x32_bf16 v[112:115], a[80:83], a[220:223], v[112:115]\n"
      "ds_read_b32 v69, v69\n"
      "v_lshl_add_u32 v48, v48, 2, s76\n"
      "v_lshl_add_u32 v44, v44, 2, s76\n"
      "v_mfma_f32_16x16x32_bf16 v[154:157], a[8:11], a[252:255], v[154:157]\n"
      "buffer_load_dwordx4 v97, s[20:23], s78 offen offset:640  lds\n"
      "v_mfma_f32_16x16x32_bf16 v[194:197], a[32:35], a[252:255], v[194:197]\n"
      "s_add_u32 m0, s75, 24576\n"
      "v_lshl_add_u32 v64, v64, 2, s76\n"
      "v_lshl_add_u32 v179, v179, 2, s76\n"
      "v_mfma_f32_16x16x32_bf16 v[232:235], a[56:59], a[248:251], v[232:235]\n"
      "s_waitcnt vmcnt(6)\n"
      "s_barrier\n"
      "ds_read_b64_tr_b16 a[108:109], v84 offset:36864\n"
      "ds_read_b64_tr_b16 a[132:133], v174 offset:36864\n"
      "v_mfma_f32_16x16x32_bf16 v[244:247], a[8:11], a[248:251], v[244:247]\n"
      "ds_read_b64_tr_b16 a[110:111], v84 offset:37376\n"
      "ds_read_b64_tr_b16 a[38:39], v84 offset:25088\n"
      "v_mfma_f32_16x16x32_bf16 v[170:173], a[52:55], a[244:247], v[170:173]\n"
      "ds_read_b64_tr_b16 a[134:135], v174 offset:37376\n"
      "ds_read_b64_tr_b16 a[84:85], v85 offset:36864\n"
      "ds_read_b64_tr_b16 a[12:13], v85 offset:24576\n"
      "ds_read_b64_tr_b16 a[36:37], v84 offset:24576\n"
      "ds_read_b64_tr_b16 a[60:61], v174 offset:24576\n"
      "v_mfma_f32_16x16x32_bf16 v[170:173], a[56:59], a[220:223], v[170:173]\n"
      "ds_read_b64_tr_b16 a[14:15], v85 offset:25088\n"
      "ds_read_b64_tr_b16 a[62:63], v174 offset:25088\n"
      "ds_read_b64_tr_b16 a[86:87], v85 offset:37376\n"
      "v_mfma_f32_16x16x32_bf16 v[216:219], a[80:83], a[248:251], v[216:219]\n"
      "buffer_load_dwordx4 v97, s[20:23], s79 offen  lds\n"
      "v_mfma_f32_16x16x32_bf16 v[228:231], a[28:31], a[244:247], v[228:231]\n"
      "ds_read_b32 v43, v43\n"
      "s_add_u32 m0, s75, 28544\n"
      "v_lshl_add_u32 v45, v45, 2, s76\n"
      "v_mfma_f32_16x16x32_bf16 v[228:231], a[32:35], a[220:223], v[228:231]\n"
      "ds_read_b128 a[244:247], v0 offset:12288\n"
      "v_lshl_add_u32 v56, v56, 2, s76\n"
      "v_mfma_f32_16x16x32_bf16 v[130:133], a[32:35], a[216:219], v[130:133]\n"
      "ds_read_b32 v65, v65\n"
      "buffer_load_dwordx4 v97, s[20:23], s79 offen offset:128  lds\n"
      "s_mov_b32 s78, 0\n"
      "v_mfma_f32_16x16x32_bf16 v[150:153], a[128:131], a[216:219], v[150:153]\n"
      "s_add_u32 m0, s75, 32512\n"
      "v_lshl_add_u32 v52, v52, 2, s76\n"
      "s_waitcnt lgkmcnt(3)\n"
      "v_mfma_f32_16x16x32_bf16 v[212:215], a[60:63], a[232:235], v[212:215]\n"
      "ds_read_b32 v211, v211\n"
      "ds_read_b32 v40, v40\n"
      "v_lshl_add_u32 v60, v60, 2, s76\n"
      "v_mfma_f32_16x16x32_bf16 v[120:123], a[128:131], a[252:255], v[120:123]\n"
      "buffer_load_dwordx4 v97, s[20:23], s79 offen offset:256  lds\n"
      "v_mfma_f32_16x16x32_bf16 v[166:169], a[80:83], a[252:255], v[166:169]\n"
      "ds_read_b32 v44, v44\n"
      "v_lshl_add_u32 v67, v67, 2, s76\n"
      "v_mfma_f32_16x16x32_bf16 v[166:169], a[84:87], a[228:231], v[166:169]\n"
      "ds_read_b128 a[216:219], v184 offset:8192\n"
      "v_lshl_add_u32 v74, v74, 2, s76\n"
      "v_mfma_f32_16x16x32_bf16 v[252:255], a[104:107], a[220:223], v[252:255]\n"
      "ds_read_b32 v48, v48\n"
      "s_add_u32 m0, s75, 36480\n"
      "v_mfma_f32_16x16x32_bf16 v[252:255], a[108:111], a[236:239], v[252:255]\n"
      "buffer_load_dwordx4 v97, s[20:23], s79 offen offset:384  lds\n"
      "v_cndmask_b32 v30, v209, v177, s[90:91]\n"
      "v_mfma_f32_16x16x32_bf16 v[116:119], a[132:135], a[236:239], v[116:119]\n"
      "ds_read_b32 v60, v60\n"
      "v_lshl_add_u32 v66, v66, 2, s76\n"
      "v_mfma_f32_16x16x32_bf16 v[112:115], a[84:87], a[236:239], v[112:115]\n"
      "s_add_u32 m0, s75, 40448\n"
      "ds_read_b32 v75, v75\n"
      "v_mfma_f32_16x16x32_bf16 v[244:247], a[12:15], a[224:227], v[244:247]\n"
      "ds_read_b32 v179, v179\n"
      "v_lshl_add_u32 v57, v57, 2, s76\n"
      "v_mfma_f32_16x16x32_bf16 v[100:103], a[8:11], a[220:223], v[100:103]\n"
      "buffer_load_dwordx4 v97, s[20:23], s79 offen offset:512  lds\n"
      "v_mfma_f32_16x16x32_bf16 v[170:173], a[60:63], a[236:239], v[170:173]\n"
      "ds_read_b128 a[248:251], v0 offset:14336\n"
      "s_add_u32 m0, s75, 44416\n"
      "ds_read_b32 v45, v45\n"
      "v_mfma_f32_16x16x32_bf16 v[240:243], a[108:111], a[232:235], v[240:243]\n"
      "ds_read_b32 v64, v64\n"
      "ds_read_b32 v70, v70\n"
      "v_mfma_f32_16x16x32_bf16 v[204:207], a[36:39], a[224:227], v[204:207]\n"
      "buffer_load_dwordx4 v97, s[20:23], s79 offen offset:640  lds\n"
      "v_mfma_f32_16x16x32_bf16 v[248:251], a[132:135], a[224:227], v[248:251]\n"
      "ds_read_b128 a[252:255], v0 offset:16384\n"
      "s_add_u32 m0, s75, 48384\n"
      "v_mfma_f32_16x16x32_bf16 v[120:123], a[132:135], a[228:231], v[120:123]\n"
      "ds_read_b32 v223, v223\n"
      "v_lshl_add_u32 v53, v53, 2, s76\n"
      "v_mfma_f32_16x16x32_bf16 v[228:231], a[36:39], a[236:239], v[228:231]\n"
      "ds_read_b32 v63, v63\n"
      "ds_read_b32 v53, v53\n"
      "v_mfma_f32_16x16x32_bf16 v[100:103], a[12:15], a[236:239], v[100:103]\n"
      "buffer_load_dwordx4 v97, s[20:23], s78 offen offset:768  lds\n"
      "v_mfma_f32_16x16x32_bf16 v[194:197], a[36:39], a[228:231], v[194:197]\n"
      "s_add_u32 m0, s75, 52352\n"
      "s_waitcnt vmcnt(7)\n"
      "s_barrier\n"
      "ds_read_b64_tr_b16 a[136:137], v238 offset:12288\n"
      "v_mfma_f32_16x16x32_bf16 v[224:227], a[108:111], a[224:227], v[224:227]\n"
      "ds_read_b64_tr_b16 a[88:89], v236 offset:12288\n"
      "ds_read_b64_tr_b16 a[66:67], v238 offset:512\n"
      "ds_read_b64_tr_b16 a[90:91], v236 offset:12800\n"
      "v_mfma_f32_16x16x32_bf16 v[154:157], a[12:15], a[228:231], v[154:157]\n"
      "ds_read_b64_tr_b16 a[16:17], v236\n"
      "ds_read_b64_tr_b16 a[18:19], v236 offset:512\n"
      "v_mfma_f32_16x16x32_bf16 v[232:235], a[60:63], a[224:227], v[232:235]\n"
      "buffer_load_dwordx4 v97, s[20:23], s78 offen offset:896  lds\n"
      "v_mfma_f32_16x16x32_bf16 v[108:111], a[12:15], a[232:235], v[108:111]\n"
      "ds_read_b64_tr_b16 a[138:139], v238 offset:12800\n"
      "ds_read_b64_tr_b16 a[42:43], v237 offset:512\n"
      "ds_read_b64_tr_b16 a[64:65], v238\n"
      "v_mfma_f32_16x16x32_bf16 v[150:153], a[132:135], a[232:235], v[150:153]\n"
      "ds_read_b64_tr_b16 a[112:113], v237 offset:12288\n"
      "ds_read_b64_tr_b16 a[114:115], v237 offset:12800\n"
      "v_mfma_f32_16x16x32_bf16 v[130:133], a[36:39], a[232:235], v[130:133]\n"
      "ds_read_b64_tr_b16 a[40:41], v237\n"
      "s_add_u32 m0, s75, 56320\n"
      "v_mfma_f32_16x16x32_bf16 v[180:183], a[84:87], a[232:235], v[180:183]\n"
      "buffer_load_dwordx4 v97, s[20:23], s78 offen offset:1024  lds\n"
      "v_mfma_f32_16x16x32_bf16 v[216:219], a[84:87], a[224:227], v[216:219]\n"
      "ds_read_b32 v67, v67\n"
      "s_waitcnt lgkmcnt(7)\n"
      "v_mfma_f32_16x16x32_bf16 v[216:219], a[88:91], a[240:243], v[216:219]\n"
      "ds_read_b32 v47, v47\n"
      "v_readlane_b32 s84, v37, s43\n"
      "v_mfma_f32_16x16x32_bf16 v[146:149], a[60:63], a[228:231], v[146:149]\n"
      "s_mov_b32 s78, 36864\n"
      "v_lshl_add_u32 v68, v68, 2, s76\n"
      "s_add_u32 m0, s75, 60672\n"
      "v_mfma_f32_16x16x32_bf16 v[108:111], a[16:19], a[248:251], v[108:111]\n"
      "buffer_load_dwordx4 v97, s[20:23], s78 offen offset:768  lds\n"
      "s_waitcnt lgkmcnt(2)\n"
      "v_mfma_f32_16x16x32_bf16 v[252:255], a[112:115], a[252:255], v[252:255]\n"
      "s_add_u32 m0, s75, 64640\n"
      "ds_read_b128 a[232:235], v135 offset:2048\n"
      "s_mov_b32 s79, 73728\n"
      "v_mfma_f32_16x16x32_bf16 v[120:123], a[136:139], a[244:247], v[120:123]\n"
      "ds_read_b32 v46, v46\n"
      "ds_read_b32 v54, v54\n"
      "v_mfma_f32_16x16x32_bf16 v[232:235], a[64:67], a[240:243], v[232:235]\n"
      "buffer_load_dwordx4 v97, s[20:23], s78 offen offset:896  lds\n"
      "v_mfma_f32_16x16x32_bf16 v[166:169], a[88:91], a[244:247], v[166:169]\n"
      "s_add_u32 m0, s75, 68608\n"
      "ds_read_b32 v68, v68\n"
      "v_mfma_f32_16x16x32_bf16 v[190:193], a[108:111], a[228:231], v[190:193]\n"
      "ds_read_b32 v58, v58\n"
      "ds_read_b32 v41, v41\n"
      "ds_read_b128 a[236:239], v184 offset:2048\n"
      "v_mfma_f32_16x16x32_bf16 v[116:119], a[136:139], a[252:255], v[116:119]\n"
      "buffer_load_dwordx4 v97, s[20:23], s78 offen offset:1024  lds\n"
      "v_mfma_f32_16x16x32_bf16 v[244:247], a[16:19], a[240:243], v[244:247]\n"
      "ds_read_b32 v49, v49\n"
      "v_lshl_add_u32 v62, v62, 2, s76\n"
      "v_mfma_f32_16x16x32_bf16 v[248:251], a[136:139], a[240:243], v[248:251]\n"
      "ds_read_b32 v57, v57\n"
      "v_cndmask_b32 v28, v209, v179, s[82:83]\n"
      "v_mfma_f32_16x16x32_bf16 v[154:157], a[16:19], a[244:247], v[154:157]\n"
      "ds_read_b32 v52, v52\n"
      "s_add_u32 m0, s75, 72960\n"
      "v_mfma_f32_16x16x32_bf16 v[112:115], a[88:91], a[252:255], v[112:115]\n"
      "buffer_load_dwordx4 v97, s[20:23], s79 offen offset:768  lds\n"
      "s_waitcnt lgkmcnt(4)\n"
      "v_mfma_f32_16x16x32_bf16 v[176:179], a[72:75], a[232:235], 0\n"
      "ds_read_b32 v55, v55\n"
      "v_readlane_b32 s85, v37, s42\n"
      "v_mfma_f32_16x16x32_bf16 v[146:149], a[64:67], a[244:247], v[146:149]\n"
      "ds_read_b32 v42, v42\n"
      "ds_read_b32 v74, v74\n"
      "v_mfma_f32_16x16x32_bf16 v[194:197], a[40:43], a[244:247], v[194:197]\n"
      "s_add_u32 m0, s75, 76928\n"
      "ds_read_b32 v56, v56\n"
      "v_mfma_f32_16x16x32_bf16 v[130:133], a[40:43], a[248:251], v[130:133]\n"
      "buffer_load_dwordx4 v97, s[20:23], s79 offen offset:896  lds\n"
      "v_mfma_f32_16x16x32_bf16 v[170:173], a[64:67], a[252:255], v[170:173]\n"
      "ds_read_b128 a[224:227], v1 offset:14336\n"
      "s_add_u32 m0, s75, 80896\n"
      "ds_read_b128 a[228:231], v1 offset:16384\n"
      "v_mfma_f32_16x16x32_bf16 v[228:231], a[40:43], a[252:255], v[228:231]\n"
      "ds_read_b32 v66, v66\n"
      "ds_read_b128 a[220:223], v1 offset:12288\n"
      "v_mfma_f32_16x16x32_bf16 v[100:103], a[16:19], a[252:255], v[100:103]\n"
      "buffer_load_dwordx4 v97, s[20:23], s79 offen offset:1024  lds\n"
      "v_mfma_f32_16x16x32_bf16 v[180:183], a[88:91], a[248:251], v[180:183]\n"
      "s_waitcnt vmcnt(9)\n"
      "s_barrier\n"
      "ds_read_b64_tr_b16 a[94:95], v236 offset:37376\n"
      "v_mfma_f32_16x16x32_bf16 v[190:193], a[112:115], a[244:247], v[190:193]\n"
      "ds_read_b64_tr_b16 a[20:21], v236 offset:24576\n"
      "ds_read_b64_tr_b16 a[92:93], v236 offset:36864\n"
      "v_mfma_f32_16x16x32_bf16 v[142:145], a[0:3], a[232:235], 0\n"
      "ds_read_b64_tr_b16 a[22:23], v236 offset:25088\n"
      "s_mov_b32 s79, 110592\n"
      "v_mfma_f32_16x16x32_bf16 v[224:227], a[112:115], a[240:243], v[224:227]\n"
      "ds_read_b64_tr_b16 a[116:117], v237 offset:36864\n"
      "ds_read_b64_tr_b16 a[68:69], v238 offset:24576\n"
      "v_mfma_f32_16x16x32_bf16 v[212:215], a[64:67], a[248:251], v[212:215]\n"
      "s_add_u32 m0, s75, 85248\n"
      "ds_read_b64_tr_b16 a[44:45], v237 offset:24576\n"
      "v_mfma_f32_16x16x32_bf16 v[204:207], a[40:43], a[240:243], v[204:207]\n"
      "ds_read_b64_tr_b16 a[70:71], v238 offset:25088\n"
      "ds_read_b64_tr_b16 a[140:141], v238 offset:36864\n"
      "v_mfma_f32_16x16x32_bf16 v[162:165], a[48:51], a[232:235], 0\n"
      "buffer_load_dwordx4 v97, s[20:23], s79 offen offset:768  lds\n"
      "v_mfma_f32_16x16x32_bf16 v[150:153], a[136:139], a[248:251], v[150:153]\n"
      "ds_read_b64_tr_b16 a[46:47], v237 offset:25088\n"
      "s_waitcnt lgkmcnt(6)\n"
      "v_mfma_f32_16x16x32_bf16 v[154:157], a[20:23], a[220:223], v[154:157]\n"
      "s_add_u32 m0, s75, 89216\n"
      "ds_read_b64_tr_b16 a[118:119], v237 offset:37376\n"
      "v_mfma_f32_16x16x32_bf16 v[244:247], a[20:23], a[216:219], v[244:247]\n"
      "ds_read_b64_tr_b16 a[142:143], v238 offset:37376\n"
      "v_lshl_add_u32 v59, v59, 2, s76\n"
      "v_mfma_f32_16x16x32_bf16 v[216:219], a[92:95], a[216:219], v[216:219]\n"
      "buffer_load_dwordx4 v97, s[20:23], s79 offen offset:896  lds\n"
      "v_mfma_f32_16x16x32_bf16 v[112:115], a[92:95], a[228:231], v[112:115]\n"
      "s_add_u32 m0, s75, 93184\n"
      "ds_read_b128 a[240:243], v135 offset:6144\n"
      "v_mfma_f32_16x16x32_bf16 v[180:183], a[92:95], a[224:227], v[180:183]\n"
      "ds_read_b128 a[244:247], v184 offset:6144\n"
      "v_mul_f32_e64 v105, v245, v245\n"
      "v_mfma_f32_16x16x32_bf16 v[176:179], a[76:79], a[236:239], v[176:179]\n"
      "s_mov_b32 s78, 147456\n"
      "v_mul_f32_e64 v159, v217, v217\n"
      "v_fma_f32 v104, v244, v244, s51\n"
      "v_mfma_f32_16x16x32_bf16 v[166:169], a[92:95], a[220:223], v[166:169]\n"
      "buffer_load_dwordx4 v97, s[20:23], s79 offen offset:1024  lds\n"
      "v_mfma_f32_16x16x32_bf16 v[100:103], a[20:23], a[228:231], v[100:103]\n"
      "s_waitcnt lgkmcnt(2)\n"
      "v_fma_f32 v158, v216, v216, s51\n"
      "s_add_u32 m0, s75, -768\n"
      "v_mfma_f32_16x16x32_bf16 v[204:207], a[44:47], a[216:219], v[204:207]\n"
      "ds_read_b32 v51, v51\n"
      "v_cndmask_b32 v26, v209, v223, s[82:83]\n"
      "v_mfma_f32_16x16x32_bf16 v[150:153], a[140:143], a[224:227], v[150:153]\n"
      "s_waitcnt lgkmcnt(2)\n"
      "v_cndmask_b32 v24, v209, v211, s[82:83]\n"
      "v_mul_f32_e64 v209, v181, v181\n"
      "v_mfma_f32_16x16x32_bf16 v[108:111], a[20:23], a[224:227], v[108:111]\n"
      "buffer_load_dwordx4 v97, s[20:23], s78 offen offset:768  lds\n"
      "v_mfma_f32_16x16x32_bf16 v[142:145], a[4:7], a[236:239], v[142:145]\n"
      "ds_read_b32 v59, v59\n"
      "v_fma_f32 v208, v180, v180, s51\n"
      "v_mfma_f32_16x16x32_bf16 v[142:145], a[8:11], a[240:243], v[142:145]\n"
      "s_lshl_b32 s92, s40, 6\n"
      "v_fma_f32 v106, v166, v166, s51\n"
      "v_mul_f32 v211, v155, v155\n"
      "v_mfma_f32_16x16x32_bf16 v[130:133], a[44:47], a[224:227], v[130:133]\n"
      "s_add_u32 m0, s75, 3200\n"
      "v_mul_f32 v187, v101, v101\n"
      "v_mul_f32 v107, v167, v167\n"
      "v_mfma_f32_16x16x32_bf16 v[190:193], a[116:119], a[220:223], v[190:193]\n"
      "buffer_load_dwordx4 v97, s[20:23], s78 offen offset:896  lds\n"
      "v_fma_f32 v222, v112, v112, s51\n"
      "v_mul_f32_e64 v223, v113, v113\n"
      "v_pk_fma_f32 v[208:209], v[182:183], v[182:183], v[208:209]\n"
      "v_pk_fma_f32 v[158:159], v[218:219], v[218:219], v[158:159]\n"
      "v_pk_fma_f32 v[104:105], v[246:247], v[246:247], v[104:105]\n"
      "v_pk_fma_f32 v[106:107], v[168:169], v[168:169], v[106:107]\n"
      "v_pk_fma_f32 v[104:105], v[204:205], v[204:205], v[104:105]\n"
      "v_mfma_f32_16x16x32_bf16 v[232:235], a[68:71], a[216:219], v[232:235]\n"
      "s_add_u32 m0, s75, 7168\n"
      "ds_read_b128 a[252:255], v184 offset:10240\n"
      "v_mfma_f32_16x16x32_bf16 v[224:227], a[116:119], a[216:219], v[224:227]\n"
      "buffer_load_dwordx4 v97, s[20:23], s78 offen offset:1024  lds\n"
      "v_mfma_f32_16x16x32_bf16 v[194:197], a[44:47], a[220:223], v[194:197]\n"
      "s_add_u32 s43, s43, 1\n"
      "v_fma_f32 v210, v154, v154, s51\n"
      "s_mov_b32 s78, 184320\n"
      "v_mfma_f32_16x16x32_bf16 v[240:243], a[112:115], a[248:251], v[240:243]\n"
      "s_add_u32 m0, s75, 11520\n"
      "v_fma_f32 v186, v100, v100, s51\n"
      "v_mul_f32 v129, v109, v109\n"
      "v_fma_f32 v128, v108, v108, s51\n"
      "v_mfma_f32_16x16x32_bf16 v[240:243], a[116:119], a[224:227], v[240:243]\n"
      "ds_read_b128 a[248:251], v135 offset:10240\n"
      "buffer_load_dwordx4 v97, s[20:23], s78 offen offset:768  lds\n"
      "v_pk_fma_f32 v[104:105], v[206:207], v[206:207], v[104:105]\n"
      "v_pk_fma_f32 v[158:159], v[224:225], v[224:225], v[158:159]\n"
      "s_add_u32 m0, s75, 15488\n"
      "v_pk_fma_f32 v[106:107], v[190:191], v[190:191], v[106:107]\n"
      "v_pk_fma_f32 v[158:159], v[226:227], v[226:227], v[158:159]\n"
      "v_pk_fma_f32 v[104:105], v[232:233], v[232:233], v[104:105]\n"
      "v_pk_fma_f32 v[186:187], v[102:103], v[102:103], v[186:187]\n"
      "v_pk_fma_f32 v[222:223], v[114:115], v[114:115], v[222:223]\n"
      "v_pk_fma_f32 v[210:211], v[156:157], v[156:157], v[210:211]\n"
      "v_pk_fma_f32 v[104:105], v[234:235], v[234:235], v[104:105]\n"
      "v_pk_fma_f32 v[128:129], v[110:111], v[110:111], v[128:129]\n"
      "v_pk_fma_f32 v[210:211], v[194:195], v[194:195], v[210:211]\n"
      "v_pk_fma_f32 v[208:209], v[240:241], v[240:241], v[208:209]\n"
      "v_mfma_f32_16x16x32_bf16 v[248:251], a[140:143], a[216:219], v[248:251]\n"
      "buffer_load_dwordx4 v97, s[20:23], s78 offen offset:896  lds\n"
      "v_mfma_f32_16x16x32_bf16 v[170:173], a[68:71], a[228:231], v[170:173]\n"
      "s_add_u32 m0, s75, 19456\n"
      "v_add_f32 v104, v104, v105\n"
      "s_waitcnt vmcnt(11)\n"
      "s_barrier\n"
      "v_mfma_f32_16x16x32_bf16 v[116:119], a[140:143], a[228:231], v[116:119]\n"
      "s_mov_b32 s79, 0\n"
      "ds_read_b64_tr_b16 a[146:147], v85 offset:512\n"
      "v_mfma_f32_16x16x32_bf16 v[162:165], a[52:55], a[236:239], v[162:165]\n"
      "ds_read_b64_tr_b16 a[196:197], v174 offset:12288\n"
      "ds_read_b64_tr_b16 a[172:173], v84 offset:12288\n"
      "v_mfma_f32_16x16x32_bf16 v[120:123], a[140:143], a[220:223], v[120:123]\n"
      "buffer_load_dwordx4 v97, s[20:23], s78 offen offset:1024  lds\n"
      "v_mfma_f32_16x16x32_bf16 v[252:255], a[116:119], a[228:231], v[252:255]\n"
      "ds_read_b64_tr_b16 a[144:145], v85\n"
      "ds_read_b64_tr_b16 a[168:169], v84\n"
      "v_mfma_f32_16x16x32_bf16 v[162:165], a[56:59], a[240:243], v[162:165]\n"
      "ds_read_b64_tr_b16 a[174:175], v84 offset:12800\n"
      "s_add_u32 m0, s75, 24576\n"
      "v_mfma_f32_16x16x32_bf16 v[146:149], a[68:71], a[220:223], v[146:149]\n"
      "ds_read_b64_tr_b16 a[170:171], v84 offset:512\n"
      "ds_read_b64_tr_b16 a[198:199], v174 offset:12800\n"
      "v_mfma_f32_16x16x32_bf16 v[228:231], a[44:47], a[228:231], v[228:231]\n"
      "ds_read_b64_tr_b16 a[150:151], v85 offset:12800\n"
      "ds_read_b64_tr_b16 a[194:195], v174 offset:512\n"
      "ds_read_b64_tr_b16 a[148:149], v85 offset:12288\n"
      "v_pk_fma_f32 v[208:209], v[242:243], v[242:243], v[208:209]\n"
      "v_pk_fma_f32 v[210:211], v[196:197], v[196:197], v[210:211]\n"
      "v_pk_fma_f32 v[208:209], v[150:151], v[150:151], v[208:209]\n"
      "v_pk_fma_f32 v[158:159], v[248:249], v[248:249], v[158:159]\n"
      "v_pk_fma_f32 v[128:129], v[130:131], v[130:131], v[128:129]\n"
      "v_mfma_f32_16x16x32_bf16 v[212:215], a[68:71], a[224:227], v[212:215]\n"
      "ds_read_b64_tr_b16 a[192:193], v174\n"
      "buffer_load_dwordx4 v141, s[36:39], s79 offen  lds\n"
      "v_pk_fma_f32 v[222:223], v[252:253], v[252:253], v[222:223]\n"
      "v_pk_fma_f32 v[128:129], v[132:133], v[132:133], v[128:129]\n"
      "v_pk_fma_f32 v[158:159], v[250:251], v[250:251], v[158:159]\n"
      "v_pk_fma_f32 v[208:209], v[152:153], v[152:153], v[208:209]\n"
      "v_add_f32 v158, v158, v159\n"
      "v_add_f32 v208, v208, v209\n"
      "v_pk_fma_f32 v[210:211], v[146:147], v[146:147], v[210:211]\n"
      "v_pk_fma_f32 v[106:107], v[192:193], v[192:193], v[106:107]\n"
      "v_pk_fma_f32 v[222:223], v[254:255], v[254:255], v[222:223]\n"
      "v_permlane16_swap_b32 v104, v158\n"
      "v_pk_fma_f32 v[128:129], v[212:213], v[212:213], v[128:129]\n"
      "v_add_f32 v104, v104, v158\n"
      "v_pk_fma_f32 v[222:223], v[116:117], v[116:117], v[222:223]\n"
      "v_mov_b32 v158, v104\n"
      "v_pk_fma_f32 v[128:129], v[214:215], v[214:215], v[128:129]\n"
      "v_pk_fma_f32 v[106:107], v[120:121], v[120:121], v[106:107]\n"
      "v_pk_fma_f32 v[210:211], v[148:149], v[148:149], v[210:211]\n"
      "v_add_f32 v128, v128, v129\n"
      "v_pk_fma_f32 v[186:187], v[228:229], v[228:229], v[186:187]\n"
      "v_pk_fma_f32 v[106:107], v[122:123], v[122:123], v[106:107]\n"
      "v_permlane16_swap_b32 v128, v208\n"
      "v_add_f32 v128, v128, v208\n"
      "v_mov_b32 v208, v128\n"
      "v_add_f32 v210, v210, v211\n"
      "v_mfma_f32_16x16x32_bf16 v[96:99], a[24:27], a[232:235], 0\n"
      "s_waitcnt lgkmcnt(0)\n"
      "v_add_f32 v106, v106, v107\n"
      "v_permlane32_swap_b32_e64 v104, v158\n"
      "v_mfma_f32_16x16x32_bf16 v[142:145], a[12:15], a[244:247], v[142:145]\n"
      "s_add_u32 m0, s75, 28544\n"
      "v_permlane16_swap_b32 v210, v106\n"
      "v_permlane32_swap_b32 v128, v208\n"
      "v_add_f32 v104, v104, v158\n"
      "v_pk_fma_f32 v[222:223], v[118:119], v[118:119], v[222:223]\n"
      "v_add_f32 v128, v128, v208\n"
      "v_add_f32 v222, v222, v223\n"
      "v_add_f32 v210, v210, v106\n"
      "v_mfma_f32_16x16x32_bf16 v[142:145], a[16:19], a[248:251], v[142:145]\n"
      "buffer_load_dwordx4 v141, s[36:39], s79 offen offset:128  lds\n"
      "v_rsq_f32_e64 v128, v128\n"
      "v_pk_fma_f32 v[186:187], v[230:231], v[230:231], v[186:187]\n"
      "v_mfma_f32_16x16x32_bf16 v[142:145], a[20:23], a[252:255], v[142:145]\n"
      "ds_read_b32 v62, v62\n"
      "v_mov_b32 v106, v210\n"
      "v_rsq_f32 v104, v104\n"
      "v_mov_b32 v208, v128\n"
      "v_mfma_f32_16x16x32_bf16 v[176:179], a[80:83], a[240:243], v[176:179]\n"
      "s_add_u32 m0, s75, 32512\n"
      "v_permlane32_swap_b32 v210, v106\n"
      "v_mfma_f32_16x16x32_bf16 v[176:179], a[84:87], a[244:247], v[176:179]\n"
      "buffer_load_dwordx4 v141, s[36:39], s79 offen offset:256  lds\n"
      "v_add_f32 v210, v210, v106\n"
      "v_permlane16_swap_b32 v128, v208\n"
      "v_mfma_f32_16x16x32_bf16 v[176:179], a[88:91], a[248:251], v[176:179]\n"
      "s_add_u32 m0, s75, 36864\n"
      "v_mov_b32 v158, v104\n"
      "v_mul_f32 v128, s50, v128\n"
      "v_mfma_f32_16x16x32_bf16 v[176:179], a[92:95], a[252:255], v[176:179]\n"
      "s_mov_b32 s79, 12288\n"
      "v_rsq_f32 v210, v210\n"
      "v_permlane16_swap_b32 v104, v158\n"
      "v_mov_b32_e64 v106, v210\n"
      "v_pk_mul_f32 v[108:109], v[108:109], v[128:129] op_sel_hi:[1,0]\n"
      "v_pk_fma_f32 v[186:187], v[170:171], v[170:171], v[186:187]\n"
      "v_pk_mul_f32 v[150:151], v[150:151], v[208:209] op_sel_hi:[1,0]\n"
      "v_pk_mul_f32 v[132:133], v[132:133], v[128:129] op_sel_hi:[1,0]\n"
      "v_pk_mul_f32 v[218:219], v[218:219], v[158:159] op_sel_hi:[1,0]\n"
      "v_mul_f32_e64 v104, s50, v104\n"
      "v_pk_mul_f32 v[216:217], v[216:217], v[158:159] op_sel_hi:[1,0]\n"
      "v_pk_fma_f32 v[186:187], v[172:173], v[172:173], v[186:187]\n"
      "v_pk_mul_f32 v[214:215], v[214:215], v[128:129] op_sel_hi:[1,0]\n"
      "v_mfma_f32_16x16x32_bf16 v[162:165], a[60:63], a[244:247], v[162:165]\n"
      "buffer_load_dwordx4 v141, s[36:39], s79 offen  lds\n"
      "v_add_f32 v186, v186, v187\n"
      "v_permlane16_swap_b32 v210, v106\n"
      "v_pk_mul_f32 v[226:227], v[226:227], v[158:159] op_sel_hi:[1,0]\n"
      "v_pk_mul_f32 v[232:233], v[232:233], v[104:105] op_sel_hi:[1,0]\n"
      "v_pk_mul_f32 v[248:249], v[248:249], v[158:159] op_sel_hi:[1,0]\n"
      "v_pk_mul_f32 v[246:247], v[246:247], v[104:105] op_sel_hi:[1,0]\n"
      "v_pk_mul_f32 v[190:191], v[190:191], v[106:107] op_sel_hi:[1,0]\n"
      "v_mfma_f32_16x16x32_bf16 v[96:99], a[28:31], a[236:239], v[96:99]\n"
      "s_add_u32 m0, s75, 40832\n"
      "v_permlane16_swap_b32 v186, v222\n"
      "v_add_f32 v186, v186, v222\n"
      "v_cvt_pk_bf16_f32 v216, v216, v217\n"
      "v_mov_b32 v222, v186\n"
      "v_mfma_f32_16x16x32_bf16 v[96:99], a[32:35], a[240:243], v[96:99]\n"
      "s_waitcnt vmcnt(10)\n"
      "s_barrier\n"
      "ds_read_b64_tr_b16 a[204:205], v174 offset:36864\n"
      "v_mfma_f32_16x16x32_bf16 v[96:99], a[36:39], a[244:247], v[96:99]\n"
      "buffer_load_dwordx4 v141, s[36:39], s79 offen offset:128  lds\n"
      "v_permlane32_swap_b32 v186, v222\n"
      "v_cvt_pk_bf16_f32 v217, v218, v219\n"
      "v_pk_mul_f32 v[250:251], v[250:251], v[158:159] op_sel_hi:[1,0]\n"
      "v_pk_mul_f32 v[180:181], v[180:181], v[208:209] op_sel_hi:[1,0]\n"
      "v_pk_mul_f32 v[206:207], v[206:207], v[104:105] op_sel_hi:[1,0]\n"
      "v_pk_mul_f32 v[224:225], v[224:225], v[158:159] op_sel_hi:[1,0]\n"
      "v_pk_mul_f32 v[152:153], v[152:153], v[208:209] op_sel_hi:[1,0]\n"
      "v_add_f32_e64 v186, v186, v222\n"
      "v_cvt_pk_bf16_f32 v219, v226, v227\n"
      "v_cvt_pk_bf16_f32 v218, v224, v225\n"
      "v_pk_mul_f32 v[244:245], v[244:245], v[104:105] op_sel_hi:[1,0]\n"
      "v_pk_mul_f32 v[242:243], v[242:243], v[208:209] op_sel_hi:[1,0]\n"
      "v_mfma_f32_16x16x32_bf16 v[200:203], a[120:123], a[232:235], 0\n"
      "ds_read_b64_tr_b16 a[176:177], v84 offset:24576\n"
      "ds_read_b64_tr_b16 a[182:183], v84 offset:37376\n"
      "ds_read_b64_tr_b16 a[154:155], v85 offset:25088\n"
      "v_pk_mul_f32 v[192:193], v[192:193], v[106:107] op_sel_hi:[1,0]\n"
      "v_mul_f32_e64 v175, v177, v177\n"
      "v_cvt_pk_bf16_f32 v248, v248, v249\n"
      "v_pk_mul_f32 v[130:131], v[130:131], v[128:129] op_sel_hi:[1,0]\n"
      "v_mfma_f32_16x16x32_bf16 v[96:99], a[40:43], a[248:251], v[96:99]\n"
      "ds_read_b64_tr_b16 a[152:153], v85 offset:24576\n"
      "ds_read_b64_tr_b16 a[202:203], v174 offset:25088\n"
      "v_cvt_pk_bf16_f32 v249, v250, v251\n"
      "v_mfma_f32_16x16x32_bf16 v[158:161], a[96:99], a[232:235], 0\n"
      "ds_read_b64_tr_b16 a[200:201], v174 offset:24576\n"
      "v_rsq_f32_e64 v186, v186\n"
      "v_mfma_f32_16x16x32_bf16 v[158:161], a[100:103], a[236:239], v[158:161]\n"
      "ds_read_b64_tr_b16 a[206:207], v174 offset:37376\n"
      "ds_read_b64_tr_b16 a[178:179], v84 offset:25088\n"
      "v_mfma_f32_16x16x32_bf16 v[158:161], a[104:107], a[240:243], v[158:161]\n"
      "ds_read_b64_tr_b16 a[158:159], v85 offset:37376\n"
      "v_mul_f32_e64 v210, s50, v210\n"
      "v_mfma_f32_16x16x32_bf16 v[200:203], a[124:127], a[236:239], v[200:203]\n"
      "ds_read_b64_tr_b16 a[156:157], v85 offset:36864\n"
      "s_add_u32 m0, s75, 44800\n"
      "v_mfma_f32_16x16x32_bf16 v[200:203], a[128:131], a[240:243], v[200:203]\n"
      "ds_read_b64_tr_b16 a[180:181], v84 offset:36864\n"
      "v_mul_f32 v85, v143, v143\n"
      "v_mov_b32 v222, v186\n"
      "v_cvt_pk_bf16_f32 v244, v244, v245\n"
      "v_mfma_f32_16x16x32_bf16 v[158:161], a[108:111], a[244:247], v[158:161]\n"
      "s_mov_b32 s78, 24576\n"
      "ds_write_b128 v139, v[216:219] offset:32\n"
      "v_mfma_f32_16x16x32_bf16 v[158:161], a[112:115], a[248:251], v[158:161]\n"
      "ds_read_b128 v[216:219], v184\n"
      "v_cvt_pk_bf16_f32 v245, v246, v247\n"
      "v_mfma_f32_16x16x32_bf16 v[158:161], a[116:119], a[252:255], v[158:161]\n"
      "buffer_load_dwordx4 v141, s[36:39], s79 offen offset:256  lds\n"
      "v_fma_f32 v84, v142, v142, s51\n"
      "v_mfma_f32_16x16x32_bf16 v[96:99], a[44:47], a[252:255], v[96:99]\n"
      "ds_write_b64 v139, v[248:249] offset:24\n"
      "v_permlane16_swap_b32_e64 v186, v222\n"
      "v_cvt_pk_bf16_f32 v232, v232, v233\n"
      "v_pk_mul_f32 v[234:235], v[234:235], v[104:105] op_sel_hi:[1,0]\n"
      "v_pk_mul_f32 v[120:121], v[120:121], v[106:107] op_sel_hi:[1,0]\n"
      "v_mul_f32_e64 v186, s50, v186\n"
      "v_pk_fma_f32 v[84:85], v[144:145], v[144:145], v[84:85]\n"
      "v_pk_mul_f32 v[122:123], v[122:123], v[106:107] op_sel_hi:[1,0]\n"
      "v_pk_mul_f32 v[154:155], v[154:155], v[210:211] op_sel_hi:[1,0]\n"
      "v_pk_mul_f32 v[204:205], v[204:205], v[104:105] op_sel_hi:[1,0]\n"
      "v_cvt_pk_bf16_f32 v247, v206, v207\n"
      "v_pk_mul_f32 v[112:113], v[112:113], v[222:223] op_sel_hi:[1,0]\n"
      "v_mfma_f32_16x16x32_bf16 v[200:203], a[132:135], a[244:247], v[200:203]\n"
      "s_add_u32 m0, s75, 49152\n"
      "ds_read_b128 v[248:251], v184 offset:4096\n"
      "buffer_load_dwordx4 v141, s[36:39], s78 offen  lds\n"
      "v_pk_mul_f32 v[196:197], v[196:197], v[210:211] op_sel_hi:[1,0]\n"
      "v_fma_f32 v174, v176, v176, s51\n"
      "v_pk_mul_f32 v[194:195], v[194:195], v[210:211] op_sel_hi:[1,0]\n"
      "v_pk_fma_f32 v[174:175], v[178:179], v[178:179], v[174:175]\n"
      "v_pk_fma_f32 v[84:85], v[96:97], v[96:97], v[84:85]\n"
      "v_pk_mul_f32 v[166:167], v[166:167], v[106:107] op_sel_hi:[1,0]\n"
      "v_pk_mul_f32 v[254:255], v[254:255], v[222:223] op_sel_hi:[1,0]\n"
      "v_cvt_pk_bf16_f32 v233, v234, v235\n"
      "v_pk_mul_f32 v[212:213], v[212:213], v[128:129] op_sel_hi:[1,0]\n"
      "v_pk_mul_f32 v[240:241], v[240:241], v[208:209] op_sel_hi:[1,0]\n"
      "v_pk_mul_f32 v[182:183], v[182:183], v[208:209] op_sel_hi:[1,0]\n"
      "v_cvt_pk_bf16_f32 v246, v204, v205\n"
      "v_pk_mul_f32 v[148:149], v[148:149], v[210:211] op_sel_hi:[1,0]\n"
      "v_pk_mul_f32 v[116:117], v[116:117], v[222:223] op_sel_hi:[1,0]\n"
      "v_pk_mul_f32 v[156:157], v[156:157], v[210:211] op_sel_hi:[1,0]\n"
      "s_add_u32 m0, s75, 53120\n"
      "v_mfma_f32_16x16x32_bf16 v[200:203], a[136:139], a[248:251], v[200:203]\n"
      "ds_write_b128 v139, v[244:247]\n"
      "v_pk_fma_f32 v[174:175], v[158:159], v[158:159], v[174:175]\n"
      "v_cvt_pk_bf16_f32 v180, v180, v181\n"
      "v_cvt_pk_bf16_f32 v181, v182, v183\n"
      "v_cvt_pk_bf16_f32 v112, v112, v113\n"
      "v_mfma_f32_16x16x32_bf16 v[200:203], a[140:143], a[252:255], v[200:203]\n"
      "ds_read_b128 v[244:247], v135 offset:4096\n"
      "v_cvt_pk_bf16_f32 v212, v212, v213\n"
      "v_mfma_f32_16x16x32_bf16 v[162:165], a[64:67], a[248:251], v[162:165]\n"
      "ds_read_b128 v[204:207], v135\n"
      "buffer_load_dwordx4 v141, s[36:39], s78 offen offset:128  lds\n"
      "v_pk_mul_f32 v[100:101], v[100:101], v[186:187] op_sel_hi:[1,0]\n"
      "v_pk_mul_f32 v[110:111], v[110:111], v[128:129] op_sel_hi:[1,0]\n"
      "v_cvt_pk_bf16_f32 v108, v108, v109\n"
      "v_cvt_pk_bf16_f32 v183, v242, v243\n"
      "s_add_u32 m0, s75, 57088\n"
      "v_pk_mul_f32 v[114:115], v[114:115], v[222:223] op_sel_hi:[1,0]\n"
      "v_pk_fma_f32 v[84:85], v[98:99], v[98:99], v[84:85]\n"
      "v_pk_mul_f32 v[252:253], v[252:253], v[222:223] op_sel_hi:[1,0]\n"
      "v_pk_mul_f32 v[168:169], v[168:169], v[106:107] op_sel_hi:[1,0]\n"
      "v_pk_mul_f32 v[146:147], v[146:147], v[210:211] op_sel_hi:[1,0]\n"
      "v_pk_mul_f32 v[172:173], v[172:173], v[186:187] op_sel_hi:[1,0]\n"
      "v_cvt_pk_bf16_f32 v109, v110, v111\n"
      "v_cvt_pk_bf16_f32 v154, v154, v155\n"
      "v_cvt_pk_bf16_f32 v155, v156, v157\n"
      "v_mfma_f32_16x16x32_bf16 v[162:165], a[68:71], a[252:255], v[162:165]\n"
      "buffer_load_dwordx4 v141, s[36:39], s78 offen offset:256  lds\n"
      "s_mov_b32 s78, 36864\n"
      "v_cvt_pk_bf16_f32 v156, v194, v195\n"
      "s_waitcnt lgkmcnt(0)\n"
      "s_waitcnt vmcnt(9)\n"
      "s_barrier\n"
      "v_pk_mul_f32 v[170:171], v[170:171], v[186:187] op_sel_hi:[1,0]\n"
      "v_cvt_pk_bf16_f32 v157, v196, v197\n"
      "v_pk_fma_f32 v[174:175], v[160:161], v[160:161], v[174:175]\n"
      "s_add_u32 m0, s75, 61440\n"
      "ds_read_b64_tr_b16 a[188:189], v237 offset:12288\n"
      "v_pk_mul_f32 v[230:231], v[230:231], v[186:187] op_sel_hi:[1,0]\n"
      "v_mfma_f32_16x16x32_bf16 v[194:197], a[168:171], v[204:207], 0\n"
      "ds_read_b64_tr_b16 a[212:213], v238 offset:12288\n"
      "ds_read_b64_tr_b16 a[190:191], v237 offset:12800\n"
      "v_mfma_f32_16x16x32_bf16 v[194:197], a[172:175], v[216:219], v[194:197]\n"
      "ds_read_b64_tr_b16 a[186:187], v237 offset:512\n"
      "buffer_load_dwordx4 v141, s[36:39], s78 offen  lds\n"
      "v_mfma_f32_16x16x32_bf16 v[194:197], a[176:179], v[244:247], v[194:197]\n"
      "ds_read_b128 v[104:107], v135 offset:8192\n"
      "v_cvt_pk_bf16_f32 v111, v132, v133\n"
      "v_mfma_f32_16x16x32_bf16 v[194:197], a[180:183], v[248:251], v[194:197]\n"
      "ds_read_b64_tr_b16 a[160:161], v236\n"
      "ds_read_b64_tr_b16 a[214:215], v238 offset:12800\n"
      "v_mfma_f32_16x16x32_bf16 v[208:211], a[192:195], v[204:207], 0\n"
      "ds_read_b64_tr_b16 a[184:185], v237\n"
      "s_add_u32 m0, s75, 65408\n"
      "v_mfma_f32_16x16x32_bf16 v[208:211], a[196:199], v[216:219], v[208:211]\n"
      "ds_read_b64_tr_b16 a[210:211], v238 offset:512\n"
      "v_cvt_pk_bf16_f32 v110, v130, v131\n"
      "v_mfma_f32_16x16x32_bf16 v[208:211], a[200:203], v[244:247], v[208:211]\n"
      "ds_read_b64_tr_b16 a[164:165], v236 offset:12288\n"
      "buffer_load_dwordx4 v141, s[36:39], s78 offen offset:128  lds\n"
      "v_cvt_pk_bf16_f32 v150, v150, v151\n"
      "s_mov_b32 s79, 49152\n"
      "v_pk_fma_f32 v[84:85], v[162:163], v[162:163], v[84:85]\n"
      "v_pk_mul_f32 v[118:119], v[118:119], v[222:223] op_sel_hi:[1,0]\n"
      "v_mfma_f32_16x16x32_bf16 v[208:211], a[204:207], v[248:251], v[208:211]\n"
      "ds_read_b64_tr_b16 a[166:167], v236 offset:12800\n"
      "ds_read_b64_tr_b16 a[208:209], v238\n"
      "v_mfma_f32_16x16x32_bf16 v[222:225], a[144:147], v[204:207], 0\n"
      "ds_read_b128 v[130:133], v184 offset:8192\n"
      "v_cvt_pk_bf16_f32 v120, v120, v121\n"
      "s_add_u32 m0, s75, 69376\n"
      "ds_read_b64_tr_b16 a[162:163], v236 offset:512\n"
      "v_mfma_f32_16x16x32_bf16 v[222:225], a[148:151], v[216:219], v[222:225]\n"
      "buffer_load_dwordx4 v141, s[36:39], s78 offen offset:256  lds\n"
      "ds_read_b128 v[234:237], v184 offset:2048\n"
      "v_pk_mul_f32 v[102:103], v[102:103], v[186:187] op_sel_hi:[1,0]\n"
      "v_pk_fma_f32 v[84:85], v[164:165], v[164:165], v[84:85]\n"
      "v_pk_fma_f32 v[174:175], v[200:201], v[200:201], v[174:175]\n"
      "v_mfma_f32_16x16x32_bf16 v[222:225], a[152:155], v[244:247], v[222:225]\n"
      "ds_read_b128 v[242:245], v135 offset:2048\n"
      "v_cvt_pk_bf16_f32 v113, v114, v115\n"
      "v_cvt_pk_bf16_f32 v166, v166, v167\n"
      "v_mfma_f32_16x16x32_bf16 v[222:225], a[156:159], v[248:251], v[222:225]\n"
      "s_add_u32 m0, s75, 73728\n"
      "ds_read_b128 v[246:249], v135 offset:10240\n"
      "buffer_load_dwordx4 v141, s[36:39], s79 offen  lds\n"
      "v_pk_fma_f32 v[174:175], v[202:203], v[202:203], v[174:175]\n"
      "s_waitcnt lgkmcnt(3)\n"
      "v_mfma_f32_16x16x32_bf16 v[222:225], a[160:163], v[104:107], v[222:225]\n"
      "ds_write_b64 v139, v[232:233] offset:16\n"
      "v_mfma_f32_16x16x32_bf16 v[194:197], a[184:187], v[104:107], v[194:197]\n"
      "s_add_u32 m0, s75, 77696\n"
      "ds_read_b128 v[216:219], v135 offset:6144\n"
      "v_mfma_f32_16x16x32_bf16 v[194:197], a[188:191], v[130:133], v[194:197]\n"
      "buffer_load_dwordx4 v141, s[36:39], s79 offen offset:128  lds\n"
      "v_cvt_pk_bf16_f32 v116, v116, v117\n"
      "v_cvt_pk_bf16_f32 v115, v254, v255\n"
      "v_add_f32 v84, v84, v85\n"
      "v_cvt_pk_bf16_f32 v100, v100, v101\n"
      "v_cvt_pk_bf16_f32 v182, v240, v241\n"
      "v_add_f32 v174, v174, v175\n"
      "v_mfma_f32_16x16x32_bf16 v[208:211], a[208:211], v[104:107], v[208:211]\n"
      "s_add_u32 m0, s75, 81664\n"
      "ds_read_b128 v[204:207], v184 offset:6144\n"
      "v_mfma_f32_16x16x32_bf16 v[208:211], a[212:215], v[130:133], v[208:211]\n"
      "ds_read_b128 v[134:137], v0 offset:10240\n"
      "v_cvt_pk_bf16_f32 v213, v214, v215\n"
      "v_cvt_pk_bf16_f32 v117, v118, v119\n"
      "v_cvt_pk_bf16_f32 v194, v194, v195\n"
      "v_mfma_f32_16x16x32_bf16 v[222:225], a[164:167], v[130:133], v[222:225]\n"
      "buffer_load_dwordx4 v141, s[36:39], s79 offen offset:256  lds\n"
      "s_waitcnt lgkmcnt(2)\n"
      "v_mfma_f32_16x16x32_bf16 v[126:129], a[144:147], v[242:245], 0\n"
      "ds_read_b128 v[104:107], v0 offset:4096\n"
      "v_cvt_pk_bf16_f32 v121, v122, v123\n"
      "v_cvt_pk_bf16_f32 v195, v196, v197\n"
      "v_mfma_f32_16x16x32_bf16 v[196:199], a[192:195], v[242:245], 0\n"
      "s_mov_b32 s79, 61440\n"
      "v_permlane16_swap_b32 v84, v174\n"
      "v_add_f32_e64 v84, v84, v174\n"
      "s_add_u32 m0, s75, 86016\n"
      "v_mfma_f32_16x16x32_bf16 v[196:199], a[196:199], v[234:237], v[196:199]\n"
      "buffer_load_dwordx4 v141, s[36:39], s79 offen  lds\n"
      "v_mfma_f32_16x16x32_bf16 v[130:133], a[168:171], v[242:245], 0\n"
      "ds_read_b128 v[242:245], v184 offset:10240\n"
      "v_mov_b32_e64 v174, v84\n"
      "v_cvt_pk_bf16_f32 v114, v252, v253\n"
      "v_mfma_f32_16x16x32_bf16 v[126:129], a[148:151], v[234:237], v[126:129]\n"
      "s_add_u32 m0, s75, 89984\n"
      "v_permlane32_swap_b32_e64 v84, v174\n"
      "v_mfma_f32_16x16x32_bf16 v[126:129], a[152:155], v[216:219], v[126:129]\n"
      "ds_write_b64 v140, v[194:195] offset:32\n"
      "v_add_f32 v84, v84, v174\n"
      "v_xor_b32 v122, 64, v8\n"
      "v_cvt_pk_bf16_f32 v167, v168, v169\n"
      "v_mfma_f32_16x16x32_bf16 v[196:199], a[200:203], v[216:219], v[196:199]\n"
      "buffer_load_dwordx4 v141, s[36:39], s79 offen offset:128  lds\n"
      "v_mfma_f32_16x16x32_bf16 v[130:133], a[172:175], v[234:237], v[130:133]\n"
      "ds_read_b128 v[250:253], v1 offset:4096\n"
      "v_rsq_f32_e64 v84, v84\n"
      "v_mfma_f32_16x16x32_bf16 v[130:133], a[176:179], v[216:219], v[130:133]\n"
      "s_waitcnt lgkmcnt(2)\n"
      "v_add_u32 v85, 49152, v122\n"
      "v_mov_b32 v174, v84\n"
      "v_mfma_f32_16x16x32_bf16 v[130:133], a[180:183], v[204:207], v[130:133]\n"
      "s_add_u32 m0, s75, 93952\n"
      "v_permlane16_swap_b32_e64 v84, v174\n"
      "v_mfma_f32_16x16x32_bf16 v[126:129], a[156:159], v[204:207], v[126:129]\n"
      "buffer_load_dwordx4 v141, s[36:39], s79 offen offset:256  lds\n"
      "s_waitcnt vmcnt(12)\n"
      "s_barrier\n"
      "ds_read_b64_tr_b16 a[222:223], v239 offset:38912\n"
      "v_pk_mul_f32 v[228:229], v[228:229], v[186:187] op_sel_hi:[1,0]\n"
      "v_mfma_f32_16x16x32_bf16 v[126:129], a[160:163], v[246:249], v[126:129]\n"
      "ds_read_b64_tr_b16 v[232:233], v138 offset:24576\n"
      "ds_read_b64_tr_b16 a[216:217], v239 offset:24576\n"
      "ds_read_b64_tr_b16 a[242:243], v239 offset:26632\n"
      "v_mfma_f32_16x16x32_bf16 v[126:129], a[164:167], v[242:245], v[126:129]\n"
      "buffer_load_dwordx4 v[184:187], v36, s[28:31], s49 offen\n"
      "v_mfma_f32_16x16x32_bf16 v[196:199], a[204:207], v[204:207], v[196:199]\n"
      "ds_read_b128 v[214:217], v1 offset:10240\n"
      "v_cvt_pk_bf16_f32 v146, v146, v147\n"
      "v_mfma_f32_16x16x32_bf16 v[196:199], a[208:211], v[246:249], v[196:199]\n"
      "ds_read_b64_tr_b16 a[246:247], v239 offset:38920\n"
      "ds_read_b64_tr_b16 a[240:241], v239 offset:24584\n"
      "v_mul_f32 v84, s50, v84\n"
      "v_mfma_f32_16x16x32_bf16 v[130:133], a[184:187], v[246:249], v[130:133]\n"
      "ds_read_b64_tr_b16 v[236:237], v138 offset:36864\n"
      "ds_read_b64_tr_b16 a[244:245], v239 offset:36872\n"
      "v_mfma_f32_16x16x32_bf16 v[130:133], a[188:191], v[242:245], v[130:133]\n"
      "ds_read_b64_tr_b16 a[220:221], v239 offset:36864\n"
      "buffer_load_dword v119, v29, s[28:31], s49 offen\n"
      "v_pk_mul_f32 v[162:163], v[162:163], v[84:85] op_sel_hi:[1,0]\n"
      "v_pk_mul_f32 v[96:97], v[96:97], v[84:85] op_sel_hi:[1,0]\n"
      "v_cvt_pk_bf16_f32 v168, v190, v191\n"
      "v_mfma_f32_16x16x32_bf16 v[196:199], a[212:215], v[242:245], v[196:199]\n"
      "ds_read_b64_tr_b16 a[218:219], v239 offset:26624\n"
      "v_cvt_pk_bf16_f32 v101, v102, v103\n"
      "v_mfma_f32_16x16x32_bf16 v[218:221], v[104:107], a[168:171], 0\n"
      "ds_read_b64_tr_b16 v[238:239], v138 offset:38912\n"
      "s_waitcnt lgkmcnt(5)\n"
      "v_mfma_f32_16x16x32_bf16 v[218:221], v[250:253], a[172:175], v[218:221]\n"
      "buffer_load_dwordx4 v[188:191], v36, s[28:31], s74 offen\n"
      "ds_read_b64_tr_b16 v[234:235], v138 offset:26624\n"
      "v_cvt_pk_bf16_f32 v170, v170, v171\n"
      "v_cvt_pk_bf16_f32 v130, v130, v131\n"
      "v_cvt_pk_bf16_f32 v102, v228, v229\n"
      "v_mov_b32 v123, 0xbc80bc80\n"
      "v_pk_mul_f32 v[178:179], v[178:179], v[174:175] op_sel_hi:[1,0]\n"
      "v_cvt_pk_bf16_f32 v171, v172, v173\n"
      "v_mov_b32 v124, 0xbc80bc80\n"
      "v_pk_mul_f32 v[176:177], v[176:177], v[174:175] op_sel_hi:[1,0]\n"
      "v_mov_b32 v125, 0xbc80bc80\n"
      "v_cvt_pk_bf16_f32 v126, v126, v127\n"
      "v_cvt_pk_bf16_f32 v127, v128, v129\n"
      "v_pk_mul_f32 v[142:143], v[142:143], v[84:85] op_sel_hi:[1,0]\n"
      "v_pk_mul_f32 v[144:145], v[144:145], v[84:85] op_sel_hi:[1,0]\n"
      "v_cvt_pk_bf16_f32 v169, v192, v193\n"
      "v_cvt_pk_bf16_f32 v162, v162, v163\n"
      "v_cvt_pk_bf16_f32 v147, v148, v149\n"
      "v_mfma_f32_16x16x32_bf16 v[226:229], v[104:107], a[144:147], 0\n"
      "buffer_load_dword v148, v29, s[28:31], s74 offen\n"
      "v_cvt_pk_bf16_f32 v131, v132, v133\n"
      "s_add_u32 m0, s64, 18432\n"
      "v_pk_mul_f32 v[160:161], v[160:161], v[174:175] op_sel_hi:[1,0]\n"
      "v_pk_mul_f32 v[202:203], v[202:203], v[174:175] op_sel_hi:[1,0]\n"
      "v_pk_mul_f32 v[200:201], v[200:201], v[174:175] op_sel_hi:[1,0]\n"
      "v_cvt_pk_bf16_f32 v103, v230, v231\n"
      "v_pk_mul_f32 v[158:159], v[158:159], v[174:175] op_sel_hi:[1,0]\n"
      "v_cvt_pk_bf16_f32 v196, v196, v197\n"
      "v_pk_mul_f32 v[164:165], v[164:165], v[84:85] op_sel_hi:[1,0]\n"
      "v_cvt_pk_bf16_f32 v163, v164, v165\n"
      "v_pk_mul_f32 v[98:99], v[98:99], v[84:85] op_sel_hi:[1,0]\n"
      "v_cvt_pk_bf16_f32 v208, v208, v209\n"
      "v_cvt_pk_bf16_f32 v197, v198, v199\n"
      "v_cvt_pk_bf16_f32 v200, v200, v201\n"
      "v_mfma_f32_16x16x32_bf16 v[226:229], v[250:253], a[148:151], v[226:229]\n"
      "buffer_load_dwordx4 v16, s[16:19], s45 offen  lds\n"
      "v_subrev_u32 v39, 115456, v9\n"
      "v_cvt_pk_bf16_f32 v176, v176, v177\n"
      "v_mfma_f32_16x16x32_bf16 v[226:229], v[134:137], a[152:155], v[226:229]\n"
      "s_add_u32 m0, s64, 22528\n"
      "v_cvt_pk_bf16_f32 v201, v202, v203\n"
      "v_cvt_pk_bf16_f32 v142, v142, v143\n"
      "v_mfma_f32_16x16x32_bf16 v[226:229], v[214:217], a[156:159], v[226:229]\n"
      "ds_read_b128 v[202:205], v8 offset:24576\n"
      "ds_read_b128 v[240:243], v122 offset:24576\n"
      "v_mfma_f32_16x16x32_bf16 v[218:221], v[134:137], a[176:179], v[218:221]\n"
      "buffer_load_dwordx4 v17, s[16:19], s45 offen  lds\n"
      "v_cvt_pk_bf16_f32 v151, v152, v153\n"
      "v_mfma_f32_16x16x32_bf16 v[172:175], v[104:107], a[192:195], 0\n"
      "s_add_u32 m0, s64, 26624\n"
      "ds_read_b128 v[192:195], v8 offset:28672\n"
      "v_cvt_pk_bf16_f32 v177, v178, v179\n"
      "v_mfma_f32_16x16x32_bf16 v[172:175], v[250:253], a[196:199], v[172:175]\n"
      "ds_read_b128 v[246:249], v122 offset:28672\n"
      "v_lshrrev_b32_e64 v39, 4, v39\n"
      "v_mfma_f32_16x16x32_bf16 v[172:175], v[134:137], a[200:203], v[172:175]\n"
      "ds_read_b128 v[252:255], v0 offset:2048\n"
      "v_cvt_pk_bf16_f32 v143, v144, v145\n"
      "v_mfma_f32_16x16x32_bf16 v[172:175], v[214:217], a[204:207], v[172:175]\n"
      "ds_read_b128 v[134:137], v1 offset:2048\n"
      "buffer_load_dwordx4 v18, s[16:19], s45 offen  lds\n"
      "v_mfma_f32_16x16x32_bf16 v[218:221], v[214:217], a[180:183], v[218:221]\n"
      "s_mov_b32 exec_lo, 0x3f003f\n"
      "s_mov_b32 exec_hi, 0x3f003f\n"
      "ds_write_b64 v140, v[126:127] offset:18688\n"
      "ds_write_b64 v140, v[130:131] offset:18720\n"
      "ds_write_b64 v140, v[196:197] offset:18752\n"
      "s_mov_b64 exec, -1\n"
      "s_add_u32 m0, s64, 30720\n"
      "s_waitcnt lgkmcnt(3)\n"
      "v_mfma_f32_16x16x32_bf16 v[104:107], v[252:255], a[168:171], 0\n"
      "ds_read_b128 v[126:129], v122 offset:32768\n"
      "v_cvt_pk_bf16_f32 v145, v98, v99\n"
      "v_mov_b32 v122, 0xbc80bc80\n"
      "v_cvt_pk_bf16_f32 v178, v158, v159\n"
      "v_mfma_f32_16x16x32_bf16 v[104:107], v[134:137], a[172:175], v[104:107]\n"
      "ds_read_b128 v[214:217], v0 offset:16384\n"
      "v_cvt_pk_bf16_f32 v179, v160, v161\n"
      "v_mfma_f32_16x16x32_bf16 v[158:161], v[202:205], v[122:125], 0\n"
      "buffer_load_dwordx4 v19, s[16:19], s45 offen  lds\n"
      "v_mfma_f32_16x16x32_bf16 v[158:161], v[240:243], v[122:125], v[158:161]\n"
      "s_waitcnt vmcnt(14)\n"
      "s_barrier\n"
      "s_add_u32 m0, s65, 34816\n"
      "v_cvt_pk_bf16_f32 v144, v96, v97\n"
      "v_mfma_f32_16x16x32_bf16 v[158:161], v[192:195], v[122:125], v[158:161]\n"
      "ds_read_b128 v[130:133], v8 offset:32768\n"
      "ds_read_b128 v[202:205], v0 offset:8192\n"
      "v_mfma_f32_16x16x32_bf16 v[158:161], v[246:249], v[122:125], v[158:161]\n"
      "ds_read_b64_tr_b16 a[228:229], v82 offset:12288\n"
      "v_cvt_pk_bf16_f32 v222, v222, v223\n"
      "v_mfma_f32_16x16x32_bf16 v[96:99], v[252:255], a[192:195], 0\n"
      "buffer_load_dwordx4 v15, s[16:19], s45 offen  lds\n"
      "ds_read_b64_tr_b16 a[230:231], v82 offset:14336\n"
      "ds_read_b64_tr_b16 v[242:243], v78 offset:2048\n"
      "v_mfma_f32_16x16x32_bf16 v[194:197], v[252:255], a[144:147], 0\n"
      "ds_read_b64_tr_b16 a[250:251], v82 offset:2056\n"
      "v_cvt_pk_bf16_f32 v223, v224, v225\n"
      "v_mfma_f32_16x16x32_bf16 v[194:197], v[134:137], a[148:151], v[194:197]\n"
      "ds_read_b64_tr_b16 a[254:255], v82 offset:14344\n"
      "ds_read_b64_tr_b16 a[252:253], v82 offset:12296\n"
      "v_mfma_f32_16x16x32_bf16 v[96:99], v[134:137], a[196:199], v[96:99]\n"
      "ds_read_b64_tr_b16 v[246:247], v78 offset:14336\n"
      "ds_read_b64_tr_b16 a[224:225], v82\n"
      "s_waitcnt lgkmcnt(7)\n"
      "v_mfma_f32_16x16x32_bf16 v[96:99], v[202:205], a[200:203], v[96:99]\n"
      "ds_read_b128 v[250:253], v1 offset:8192\n"
      "ds_read_b64_tr_b16 v[240:241], v78\n"
      "v_mfma_f32_16x16x32_bf16 v[158:161], v[130:133], v[122:125], v[158:161]\n"
      "ds_read_b64_tr_b16 v[244:245], v78 offset:12288\n"
      "ds_read_b64_tr_b16 a[226:227], v82 offset:2048\n"
      "v_mfma_f32_16x16x32_bf16 v[158:161], v[126:129], v[122:125], v[158:161]\n"
      "ds_read_b64_tr_b16 a[248:249], v82 offset:8\n"
      "v_cvt_pk_bf16_f32 v209, v210, v211\n"
      "v_mfma_f32_16x16x32_bf16 v[104:107], v[202:205], a[176:179], v[104:107]\n"
      "ds_read_b128 v[126:129], v1 offset:16384\n"
      "v_and_b32 v39, 48, v39\n"
      "v_mfma_f32_16x16x32_bf16 v[218:221], v[214:217], a[184:187], v[218:221]\n"
      "ds_read_b128 v[130:133], v0 offset:14336\n"
      "ds_read_b128 v[134:137], v1 offset:14336\n"
      "v_mfma_f32_16x16x32_bf16 v[226:229], v[214:217], a[160:163], v[226:229]\n"
      "s_mov_b32 exec_lo, 0x3f003f\n"
      "s_mov_b32 exec_hi, 0x3f003f\n"
      "ds_write_b128 v139, v[142:145] offset:18688\n"
      "ds_write_b64 v139, v[162:163] offset:18704\n"
      "ds_write_b64 v139, v[200:201] offset:18712\n"
      "ds_write_b128 v139, v[176:179] offset:18720\n"
      "s_mov_b64 exec, -1\n"
      "v_mfma_f32_16x16x32_bf16 v[172:175], v[214:217], a[208:211], v[172:175]\n"
      "ds_read_b128 v[176:179], v85\n"
      "v_mul_f32 v161, 0x3eaaaaab, v161\n"
      "v_mfma_f32_16x16x32_bf16 v[194:197], v[202:205], a[152:155], v[194:197]\n"
      "s_waitcnt lgkmcnt(5)\n"
      "v_add_u32 v139, 49152, v8\n"
      "v_mul_f32 v160, 0x3eaaaaab, v160\n"
      "v_mfma_f32_16x16x32_bf16 v[194:197], v[250:253], a[156:159], v[194:197]\n"
      "ds_read_b128 v[202:205], v139 offset:4096\n"
      "v_add_u32 v39, 123136, v39\n"
      "v_mfma_f32_16x16x32_bf16 v[104:107], v[250:253], a[180:183], v[104:107]\n"
      "buffer_load_dwordx4 v[214:217], v36, s[28:31], s47 offen\n"
      "v_mfma_f32_16x16x32_bf16 v[226:229], v[126:129], a[164:167], v[226:229]\n"
      "ds_write_b64 v140, v[222:223]\n"
      "v_mfma_f32_16x16x32_bf16 v[96:99], v[250:253], a[204:207], v[96:99]\n"
      "ds_read_b128 v[250:253], v139\n"
      "v_add_u32_e64 v255, s92, v39\n"
      "v_mfma_f32_16x16x32_bf16 v[96:99], v[130:133], a[208:211], v[96:99]\n"
      "ds_read_b128 v[162:165], v139 offset:8192\n"
      "v_mul_f32 v159, 0x3eaaaaab, v159\n"
      "v_mul_f32 v158, 0x3eaaaaab, v158\n"
      "v_mfma_f32_16x16x32_bf16 v[96:99], v[134:137], a[212:215], v[96:99]\n"
      "ds_read_b128 v[198:201], v85 offset:8192\n"
      "v_cvt_pk_bf16_f32 v76, v226, v227\n"
      "v_mfma_f32_16x16x32_bf16 v[218:221], v[126:129], a[188:191], v[218:221]\n"
      "ds_write_b64 v140, v[208:209] offset:64\n"
      "v_mfma_f32_16x16x32_bf16 v[172:175], v[126:129], a[212:215], v[172:175]\n"
      "ds_read_b128 v[224:227], v85 offset:4096\n"
      "v_cvt_pk_bf16_f32 v77, v228, v229\n"
      "v_mfma_f32_16x16x32_bf16 v[126:129], v[180:183], v[154:157], v[44:47]\n"
      "buffer_load_dword v118, v29, s[28:31], s47 offen\n"
      "v_mfma_f32_16x16x32_bf16 v[104:107], v[130:133], a[184:187], v[104:107]\n"
      "ds_read_b128 v[142:145], v1\n"
      "v_cvt_pk_bf16_f32 v99, v98, v99\n"
      "v_mfma_f32_16x16x32_bf16 v[194:197], v[130:133], a[160:163], v[194:197]\n"
      "ds_read_b128 v[130:133], v0\n"
      "v_cvt_pk_bf16_f32 v81, v220, v221\n"
      "v_mfma_f32_16x16x32_bf16 v[194:197], v[134:137], a[164:167], v[194:197]\n"
      "s_waitcnt lgkmcnt(5)\n"
      "v_cvt_pk_bf16_f32 v98, v96, v97\n"
      "v_subrev_u32 v222, 115456, v9\n"
      "v_mfma_f32_16x16x32_bf16 v[104:107], v[134:137], a[188:191], v[104:107]\n"
      "ds_write_b128 v255, v[158:161]\n"
      "v_mfma_f32_16x16x32_bf16 v[208:211], v[250:253], v[122:125], 0\n"
      "s_waitcnt vmcnt(11)\n"
      "s_barrier\n"
      "v_cvt_pk_bf16_f32 v80, v218, v219\n"
      "v_mfma_f32_16x16x32_bf16 v[208:211], v[176:179], v[122:125], v[208:211]\n"
      "ds_read_b128 v[228:231], v139 offset:28672\n"
      "ds_read_b128 v[250:253], v139 offset:24576\n"
      "v_mfma_f32_16x16x32_bf16 v[208:211], v[202:205], v[122:125], v[208:211]\n"
      "ds_read_b128 v[202:205], v85 offset:24576\n"
      "s_waitcnt lgkmcnt(4)\n"
      "v_mfma_f32_16x16x32_bf16 v[208:211], v[224:227], v[122:125], v[208:211]\n"
      "ds_read_b128 v[224:227], v85 offset:32768\n"
      "v_cvt_pk_bf16_f32 v107, v106, v107\n"
      "v_bfe_u32 v96, v222, 8, 2\n"
      "v_mfma_f32_16x16x32_bf16 v[208:211], v[162:165], v[122:125], v[208:211]\n"
      "ds_read_b128 v[218:221], v85 offset:28672\n"
      "v_cvt_pk_bf16_f32 v84, v172, v173\n"
      "v_mfma_f32_16x16x32_bf16 v[208:211], v[198:201], v[122:125], v[208:211]\n"
      "ds_read_b128 v[134:137], v139 offset:32768\n"
      "v_cvt_pk_bf16_f32 v106, v104, v105\n"
      "v_mfma_f32_16x16x16_bf16 v[126:129], v[150:151], v[146:147], v[126:129]\n"
      "v_cvt_pk_bf16_f32 v85, v174, v175\n"
      "v_lshlrev_b32_e64 v96, 2, v96\n"
      "v_cvt_pk_bf16_f32 v197, v196, v197\n"
      "v_mfma_f32_16x16x32_bf16 v[138:141], v[130:133], a[144:147], 0\n"
      "ds_read_b128 v[176:179], v0 offset:6144\n"
      "v_bfe_u32 v222, v222, 4, 4\n"
      "v_mfma_f32_16x16x32_bf16 v[138:141], v[142:145], a[148:151], v[138:141]\n"
      "ds_read_b64_tr_b16 a[238:239], v82 offset:38912\n"
      "s_waitcnt lgkmcnt(5)\n"
      "v_mfma_f32_16x16x32_bf16 v[162:165], v[250:253], v[122:125], 0\n"
      "ds_read_b64_tr_b16 v[252:253], v78 offset:36864\n"
      "s_waitcnt vmcnt(10)\n"
      "v_or3_b32 v184, v184, v185, v186\n"
      "v_mfma_f32_16x16x32_bf16 v[162:165], v[202:205], v[122:125], v[162:165]\n"
      "ds_read_b128 v[202:205], v10 offset:32\n"
      "v_mul_f32 v211, 0x3eaaaaab, v211\n"
      "v_mfma_f32_16x16x32_bf16 v[162:165], v[228:231], v[122:125], v[162:165]\n"
      "ds_read_b128 v[198:201], v1 offset:6144\n"
      "s_waitcnt lgkmcnt(5)\n"
      "v_mfma_f32_16x16x32_bf16 v[162:165], v[218:221], v[122:125], v[162:165]\n"
      "ds_read_b64_tr_b16 a[236:237], v82 offset:36864\n"
      "v_cvt_pk_bf16_f32 v196, v194, v195\n"
      "v_mfma_f32_16x16x32_bf16 v[162:165], v[134:137], v[122:125], v[162:165]\n"
      "ds_read_b64_tr_b16 v[230:231], v82 offset:38920\n"
      "v_sub_u32 v222, v222, v96\n"
      "v_mfma_f32_16x16x32_bf16 v[162:165], v[224:227], v[122:125], v[162:165]\n"
      "ds_read_b64_tr_b16 v[228:229], v82 offset:36872\n"
      "v_mul_f32 v208, 0x3eaaaaab, v208\n"
      "v_mfma_f32_16x16x32_bf16 v[218:221], v[130:133], a[192:195], 0\n"
      "ds_read_b64 v[206:207], v10 offset:24\n"
      "v_mul_f32 v209, 0x3eaaaaab, v209\n"
      "v_mov_b32 v97, 0\n"
      "v_mfma_f32_16x16x32_bf16 v[218:221], v[142:145], a[196:199], v[218:221]\n"
      "s_waitcnt lgkmcnt(5)\n"
      "s_waitcnt vmcnt(9)\n"
      "v_or3_b32 v184, v184, v187, v119\n"
      "v_mul_f32 v162, 0x3eaaaaab, v162\n"
      "v_mfma_f32_16x16x32_bf16 v[122:125], v[130:133], a[168:171], 0\n"
      "ds_read_b128 v[134:137], v1 offset:12288\n"
      "v_mul_f32 v163, 0x3eaaaaab, v163\n"
      "v_mfma_f32_16x16x32_bf16 v[122:125], v[142:145], a[172:175], v[122:125]\n"
      "ds_read_b64_tr_b16 v[250:251], v78 offset:26624\n"
      "v_mul_f32 v164, 0x3eaaaaab, v164\n"
      "v_mfma_f32_16x16x32_bf16 v[122:125], v[176:179], a[176:179], v[122:125]\n"
      "ds_read_b128 v[130:133], v0 offset:12288\n"
      "s_waitcnt vmcnt(8)\n"
      "v_or3_b32 v188, v188, v189, v190\n"
      "v_mfma_f32_16x16x32_bf16 v[158:161], v[166:169], v[154:157], v[40:43]\n"
      "ds_write_b32 v31, v184 offset:6656\n"
      "v_mfma_f32_16x16x32_bf16 v[172:175], v[202:205], v[154:157], v[24:27]\n"
      "ds_read_b64_tr_b16 a[234:235], v82 offset:26624\n"
      "v_mul_f32 v165, 0x3eaaaaab, v165\n"
      "v_mfma_f32_16x16x32_bf16 v[138:141], v[176:179], a[152:155], v[138:141]\n"
      "ds_read_b64_tr_b16 v[224:225], v82 offset:24584\n"
      "v_mul_f32 v210, 0x3eaaaaab, v210\n"
      "v_mfma_f32_16x16x32_bf16 v[218:221], v[176:179], a[200:203], v[218:221]\n"
      "ds_write_b128 v255, v[208:211] offset:256\n"
      "v_mfma_f32_16x16x32_bf16 v[208:211], v[202:205], v[108:111], v[26:29]\n"
      "ds_read_b64_tr_b16 a[232:233], v82 offset:24576\n"
      "s_waitcnt lgkmcnt(5)\n"
      "v_mfma_f32_16x16x32_bf16 v[138:141], v[198:201], a[156:159], v[138:141]\n"
      "ds_read_b64_tr_b16 v[248:249], v78 offset:24576\n"
      "s_waitcnt vmcnt(7)\n"
      "v_or3_b32 v188, v188, v191, v148\n"
      "v_mfma_f32_16x16x32_bf16 v[122:125], v[198:201], a[180:183], v[122:125]\n"
      "ds_write_b128 v255, v[162:165] offset:512\n"
      "v_mfma_f32_16x16x32_bf16 v[122:125], v[130:133], a[184:187], v[122:125]\n"
      "ds_read_b64_tr_b16 v[226:227], v82 offset:26632\n"
      "v_mov_b32 v96, 0x3f80\n"
      "v_mfma_f32_16x16x32_bf16 v[176:179], v[112:115], v[154:157], v[48:51]\n"
      "ds_read_b64_tr_b16 v[254:255], v78 offset:38912\n"
      "v_lshlrev_b32_e64 v222, 4, v222\n"
      "v_mfma_f32_16x16x32_bf16 v[138:141], v[130:133], a[160:163], v[138:141]\n"
      "ds_write_b32 v31, v188 offset:5632\n"
      "v_mfma_f32_16x16x32_bf16 v[138:141], v[134:137], a[164:167], v[138:141]\n"
      "s_waitcnt lgkmcnt(0)\n"
      "s_barrier\n"
      "v_mov_b32_e64 v191, 0\n"
      "v_mfma_f32_16x16x16_bf16 v[158:161], v[120:121], v[146:147], v[158:161]\n"
      "ds_read_b128 v[184:187], v9 offset:6656\n"
      "v_lshlrev_b64 v[96:97], v222, v[96:97]\n"
      "v_mfma_f32_16x16x32_bf16 v[122:125], v[134:137], a[188:191], v[122:125]\n"
      "ds_read_b128 v[162:165], v9 offset:5632\n"
      "v_cmp_gt_u32 s[80:81], 64, v222\n"
      "v_mfma_f32_16x16x32_bf16 v[218:221], v[198:201], a[204:207], v[218:221]\n"
      "ds_read_b128 v[198:201], v39 offset:64\n"
      "v_cndmask_b32_e64 v156, 0, v96, s[80:81]\n"
      "v_mfma_f32_16x16x16_bf16 v[176:179], v[116:117], v[146:147], v[176:179]\n"
      "v_mov_b32_e64 v155, 0\n"
      "v_cndmask_b32_e64 v189, 0, v97, s[80:81]\n"
      "v_cvt_pk_bf16_f32 v194, v138, v139\n"
      "v_mfma_f32_16x16x16_bf16 v[172:175], v[206:207], v[146:147], v[172:175]\n"
      "v_cndmask_b32_e64 v188, 0, v96, s[80:81]\n"
      "v_cvt_pk_bf16_f32 v104, v122, v123\n"
      "v_mov_b32_e64 v154, 0\n"
      "v_mfma_f32_16x16x32_bf16 v[218:221], v[130:133], a[208:211], v[218:221]\n"
      "ds_read_b128 v[130:133], v39\n"
      "v_cndmask_b32_e64 v157, 0, v97, s[80:81]\n"
      "v_mfma_f32_16x16x32_bf16 v[218:221], v[134:137], a[212:215], v[218:221]\n"
      "s_waitcnt lgkmcnt(3)\n"
      "v_or3_b32 v184, v184, v185, v186\n"
      "v_mov_b32 v190, 0\n"
      "v_mfma_f32_16x16x16_bf16 v[208:211], v[206:207], v[212:213], v[208:211]\n"
      "v_cvt_pk_bf16_f32 v195, v140, v141\n"
      "v_bitop3_b32 v184, v184, v187, s33 bitop3:0xa8\n"
      "v_cmp_ne_u32 s[56:57], 0, v184\n"
      "v_mfma_f32_16x16x32_bf16 v[140:143], v[166:169], v[108:111], v[52:55]\n"
      "s_waitcnt lgkmcnt(1)\n"
      "v_cvt_pk_bf16_f32 v97, v220, v221\n"
      "v_or3_b32 v162, v162, v163, v164\n"
      "v_mfma_f32_16x16x32_bf16 v[184:187], v[154:157], v[232:235], v[198:201]\n"
      "s_or_b64 vcc, s[56:57], s[54:55]\n"
      "v_cvt_pk_bf16_f32 v96, v218, v219\n"
      "v_bitop3_b32 v162, v162, v165, s33 bitop3:0xa8\n"
      "v_mfma_f32_16x16x32_bf16 v[218:221], v[154:157], a[216:219], v[198:201]\n"
      "s_cbranch_vccnz L_rare_1_%=\n"
      "L_back_2_%=:\n"
      "v_mfma_f32_16x16x32_bf16 v[134:137], v[154:157], a[240:243], v[198:201]\n"
      "s_nop 2\n"
      "s_cbranch_vccnz L_rare_3_%=\n"
      "L_back_4_%=:\n"
      "v_mfma_f32_16x16x32_bf16 v[198:201], v[112:115], v[108:111], v[60:63]\n"
      "s_waitcnt lgkmcnt(0)\n"
      "v_cvt_pk_bf16_f32 v105, v124, v125\n"
      "v_cmp_ne_u32 s[56:57], 0, v162\n"
      "v_mfma_f32_16x16x32_bf16 v[122:125], v[188:191], v[232:235], v[130:133]\n"
      "v_cvt_pk_bf16_f32 v234, v184, v185\n"
      "v_cvt_pk_bf16_f32 v235, v186, v187\n"
      "v_exp_f32 v159, v159\n"
      "v_cvt_pk_bf16_f32 v134, v134, v135\n"
      "v_mfma_f32_16x16x16_bf16 v[140:143], v[120:121], v[212:213], v[140:143]\n"
      "v_exp_f32 v179, v179\n"
      "v_cvt_pk_bf16_f32 v136, v136, v137\n"
      "v_mfma_f32_16x16x16_bf16 v[198:201], v[116:117], v[212:213], v[198:201]\n"
      "v_cvt_pk_bf16_f32 v233, v124, v125\n"
      "v_exp_f32 v161, v161\n"
      "v_exp_f32 v158, v158\n"
      "v_cvt_pk_bf16_f32 v218, v218, v219\n"
      "v_mfma_f32_16x16x32_bf16 v[184:187], v[180:183], v[108:111], v[56:59]\n"
      "ds_read_b128 v[108:111], v10\n"
      "v_cvt_pk_bf16_f32 v232, v122, v123\n"
      "v_mfma_f32_16x16x32_bf16 v[162:165], v[188:191], a[216:219], v[130:133]\n"
      "v_exp_f32_e64 v129, v129\n"
      "v_mfma_f32_16x16x32_bf16 v[122:125], v[188:191], a[240:243], v[130:133]\n"
      "ds_read_b64 v[222:223], v10 offset:16\n"
      "v_accvgpr_write_b32 a243, v136\n"
      "v_cvt_pk_bf16_f32 v158, v158, v159\n"
      "v_mfma_f32_16x16x16_bf16 v[184:187], v[150:151], v[212:213], v[184:187]\n"
      "v_accvgpr_write_b32 a218, v218\n"
      "v_accvgpr_write_b32 a242, v134\n"
      "v_cvt_pk_bf16_f32 v162, v162, v163\n"
      "v_exp_f32 v128, v128\n"
      "v_exp_f32 v172, v172\n"
      "v_cvt_pk_bf16_f32 v164, v164, v165\n"
      "v_cvt_pk_bf16_f32 v122, v122, v123\n"
      "v_cvt_pk_bf16_f32 v220, v220, v221\n"
      "v_mfma_f32_16x16x32_bf16 v[130:133], v[180:183], v[100:103], v[68:71]\n"
      "s_waitcnt lgkmcnt(1)\n"
      "v_exp_f32 v126, v126\n"
      "v_exp_f32 v160, v160\n"
      "v_accvgpr_write_b32 a240, v122\n"
      "v_mfma_f32_16x16x32_bf16 v[134:137], v[108:111], v[180:183], v[30:33]\n"
      "s_cbranch_vccnz L_rare_5_%=\n"
      "L_back_6_%=:\n"
      "v_accvgpr_write_b32 a217, v164\n"
      "v_exp_f32 v177, v177\n"
      "v_mfma_f32_16x16x16_bf16 v[130:133], v[150:151], v[170:171], v[130:133]\n"
      "v_cvt_pk_bf16_f32 v159, v160, v161\n"
      "v_exp_f32 v187, v187\n"
      "v_exp_f32_e64 v176, v176\n"
      "v_mfma_f32_16x16x32_bf16 v[144:147], v[166:169], v[100:103], v[64:67]\n"
      "s_waitcnt lgkmcnt(0)\n"
      "v_exp_f32 v127, v127\n"
      "v_mfma_f32_16x16x16_bf16 v[134:137], v[222:223], v[150:151], v[134:137]\n"
      "v_exp_f32_e64 v199, v199\n"
      "v_cvt_pk_bf16_f32 v161, v128, v129\n"
      "v_mfma_f32_16x16x32_bf16 v[148:151], v[108:111], v[166:169], v[30:33]\n"
      "ds_read_u16 v82, v11 offset:32\n"
      "v_exp_f32_e64 v208, v208\n"
      "v_mfma_f32_16x16x32_bf16 v[180:183], v[202:205], v[100:103], v[28:31]\n"
      "ds_read_u16 v78, v11\n"
      "v_cvt_pk_bf16_f32 v160, v126, v127\n"
      "v_mfma_f32_16x16x16_bf16 v[144:147], v[120:121], v[170:171], v[144:147]\n"
      "v_exp_f32 v201, v201\n"
      "v_accvgpr_write_b32 a216, v162\n"
      "v_exp_f32 v178, v178\n"
      "v_mfma_f32_16x16x16_bf16 v[180:183], v[206:207], v[170:171], v[180:183]\n"
      "v_exp_f32_e64 v141, v141\n"
      "v_cvt_pk_bf16_f32 v90, v172, 0\n"
      "v_mfma_f32_16x16x32_bf16 v[126:129], v[112:115], v[100:103], v[72:75]\n"
      "ds_read_u16 v86, v11 offset:64\n"
      "v_exp_f32_e64 v200, v200\n"
      "v_mfma_f32_16x16x32_bf16 v[162:165], v[108:111], v[202:205], v[30:33]\n"
      "ds_read_b128 v[202:205], v39 offset:128\n"
      "v_cvt_pk_bf16_f32 v88, v176, v177\n"
      "v_mfma_f32_16x16x16_bf16 v[148:151], v[222:223], v[120:121], v[148:151]\n"
      "v_exp_f32 v143, v143\n"
      "v_exp_f32 v140, v140\n"
      "v_cvt_pk_bf16_f32 v89, v178, v179\n"
      "v_mfma_f32_16x16x32_bf16 v[174:177], v[108:111], v[112:115], v[30:33]\n"
      "ds_read_b128 v[108:111], v39 offset:192\n"
      "v_exp_f32 v185, v185\n"
      "v_mfma_f32_16x16x16_bf16 v[162:165], v[222:223], v[206:207], v[162:165]\n"
      "v_cvt_pk_bf16_f32 v124, v124, v125\n"
      "v_cvt_pk_bf16_f32 v94, v208, 0\n"
      "s_waitcnt vmcnt(1)\n"
      "v_or3_b32 v214, v214, v215, v216\n"
      "v_mfma_f32_16x16x32_bf16 v[206:209], v[194:197], v[158:161], 0\n"
      "s_waitcnt lgkmcnt(2)\n"
      "v_exp_f32 v142, v142\n"
      "v_mfma_f32_16x16x32_bf16 v[206:209], v[76:79], v[88:91], v[206:209]\n"
      "v_exp_f32_e64 v186, v186\n"
      "v_mfma_f32_16x16x16_bf16 v[126:129], v[116:117], v[170:171], v[126:129]\n"
      "v_cvt_pk_bf16_f32 v140, v140, v141\n"
      "v_exp_f32_e64 v184, v184\n"
      "v_mfma_f32_16x16x32_bf16 v[166:169], v[96:99], v[158:161], 0\n"
      "v_cvt_pk_bf16_f32 v93, v200, v201\n"
      "v_cndmask_b32 v148, v148, v134, s[58:59]\n"
      "v_mfma_f32_16x16x32_bf16 v[166:169], v[84:87], v[88:91], v[166:169]\n"
      "s_waitcnt lgkmcnt(0)\n"
      "v_cvt_pk_bf16_f32 v141, v142, v143\n"
      "v_accvgpr_write_b32 a219, v220\n"
      "v_mfma_f32_16x16x32_bf16 v[218:221], v[154:157], v[236:239], v[108:111]\n"
      "s_cbranch_vccnz L_rare_7_%=\n"
      "L_back_8_%=:\n"
      "v_mfma_f32_16x16x32_bf16 v[170:173], v[32:35], v[158:161], 0\n"
      "ds_read_b128 v[134:137], v39 offset:320\n"
      "v_exp_f32_e64 v132, v132\n"
      "v_cvt_pk_bf16_f32 v142, v184, v185\n"
      "v_mfma_f32_16x16x32_bf16 v[170:173], v[32:35], v[88:91], v[170:173]\n"
      "s_or_b32 s95, s84, s85\n"
      "v_exp_f32 v133, v133\n"
      "v_mfma_f32_16x16x16_bf16 v[174:177], v[222:223], v[116:117], v[174:177]\n"
      "v_exp_f32_e64 v198, v198\n"
      "v_cvt_pk_bf16_f32 v143, v186, v187\n"
      "v_mfma_f32_16x16x32_bf16 v[184:187], v[188:191], v[236:239], v[202:205]\n"
      "v_exp_f32 v127, v127\n"
      "v_mfma_f32_16x16x32_bf16 v[100:103], v[188:191], a[220:223], v[202:205]\n"
      "v_rcp_f32 v170, v170\n"
      "v_mfma_f32_16x16x32_bf16 v[120:123], v[104:107], v[158:161], 0\n"
      "ds_read_b128 v[158:161], v39 offset:256\n"
      "v_exp_f32 v144, v144\n"
      "v_mfma_f32_16x16x32_bf16 v[120:123], v[80:83], v[88:91], v[120:123]\n"
      "v_exp_f32 v128, v128\n"
      "v_mfma_f32_16x16x32_bf16 v[176:179], v[188:191], a[244:247], v[202:205]\n"
      "s_waitcnt lgkmcnt(1)\n"
      "v_exp_f32 v126, v126\n"
      "v_mfma_f32_16x16x32_bf16 v[202:205], v[154:157], a[248:251], v[134:137]\n"
      "v_cvt_pk_bf16_f32 v100, v100, v101\n"
      "v_exp_f32 v131, v131\n"
      "v_cvt_pk_bf16_f32 v92, v198, v199\n"
      "v_exp_f32 v145, v145\n"
      "v_pk_mul_f32 v[206:207], v[206:207], v[170:171] op_sel_hi:[1,0]\n"
      "v_exp_f32_e64 v129, v129\n"
      "v_pk_mul_f32 v[122:123], v[122:123], v[170:171] op_sel_hi:[1,0]\n"
      "v_cvt_pk_bf16_f32 v102, v102, v103\n"
      "v_pk_mul_f32 v[208:209], v[208:209], v[170:171] op_sel_hi:[1,0]\n"
      "v_cvt_pk_bf16_f32 v236, v184, v185\n"
      "v_mfma_f32_16x16x32_bf16 v[182:185], v[154:157], a[244:247], v[108:111]\n"
      "s_or_b32 s95, s95, s48\n"
      "v_exp_f32 v147, v147\n"
      "v_mfma_f32_16x16x32_bf16 v[150:153], v[154:157], a[220:223], v[108:111]\n"
      "s_cmp_ge_u32 s95, 0x40000000\n"
      "s_cselect_b32 s94, -1, -2\n"
      "v_cndmask_b32 v148, v148, v174, s[60:61]\n"
      "v_cvt_pk_bf16_f32 v89, v128, v129\n"
      "v_accvgpr_write_b32 a220, v100\n"
      "v_cvt_pk_bf16_f32 v239, v220, v221\n"
      "v_pk_mul_f32 v[166:167], v[166:167], v[170:171] op_sel_hi:[1,0]\n"
      "v_exp_f32 v180, v180\n"
      "v_pk_mul_f32 v[120:121], v[120:121], v[170:171] op_sel_hi:[1,0]\n"
      "v_mfma_f32_16x16x32_bf16 v[108:111], v[154:157], v[240:243], v[134:137]\n"
      "v_cndmask_b32 v148, v148, v162, s[62:63]\n"
      "v_cvt_pk_bf16_f32 v184, v184, v185\n"
      "v_cvt_pk_bf16_f32 v204, v204, v205\n"
      "v_mfma_f32_16x16x32_bf16 v[112:115], v[154:157], a[224:227], v[134:137]\n"
      "s_waitcnt lgkmcnt(0)\n"
      "v_exp_f32 v130, v130\n"
      "v_mfma_f32_16x16x32_bf16 v[162:165], v[188:191], a[224:227], v[158:161]\n"
      "s_waitcnt vmcnt(0)\n"
      "v_or3_b32 v214, v214, v217, v118\n"
      "v_cvt_pk_bf16_f32 v152, v152, v153\n"
      "v_mfma_f32_16x16x32_bf16 v[220:223], v[188:191], v[240:243], v[158:161]\n"
      "v_exp_f32 v146, v146\n"
      "v_cvt_pk_bf16_f32 v237, v186, v187\n"
      "v_cvt_pk_bf16_f32 v144, v144, v145\n"
      "v_mfma_f32_16x16x32_bf16 v[134:137], v[188:191], a[248:251], v[158:161]\n"
      "v_cvt_pk_bf16_f32 v178, v178, v179\n"
      "v_cvt_pk_bf16_f32 v206, v206, v207\n"
      "v_mfma_f32_16x16x32_bf16 v[158:161], v[32:35], v[140:143], 0\n"
      "s_cbranch_vccnz L_rare_9_%=\n"
      "L_back_10_%=:\n"
      "v_cvt_pk_bf16_f32 v240, v220, v221\n"
      "v_mfma_f32_16x16x32_bf16 v[158:161], v[32:35], v[92:95], v[158:161]\n"
      "v_cvt_pk_bf16_f32 v114, v114, v115\n"
      "v_cvt_pk_bf16_f32 v145, v146, v147\n"
      "v_cvt_pk_bf16_f32 v207, v208, v209\n"
      "v_cvt_pk_bf16_f32 v146, v130, v131\n"
      "v_mfma_f32_16x16x32_bf16 v[128:131], v[194:197], v[140:143], 0\n"
      "s_cmp_gt_u32 s42, s44\n"
      "s_cselect_b32 s95, s94, s95\n"
      "v_cvt_pk_bf16_f32 v88, v126, v127\n"
      "v_mfma_f32_16x16x32_bf16 v[128:131], v[76:79], v[92:95], v[128:131]\n"
      "v_exp_f32_e64 v148, v148\n"
      "v_cvt_pk_bf16_f32 v241, v222, v223\n"
      "v_mfma_f32_16x16x32_bf16 v[220:223], v[96:99], v[140:143], 0\n"
      "ds_read_b128 v[116:119], v39 offset:448\n"
      "v_cvt_pk_bf16_f32 v242, v108, v109\n"
      "v_mfma_f32_16x16x32_bf16 v[220:223], v[84:87], v[92:95], v[220:223]\n"
      "s_and_b32 s86, s86, -4\n"
      "v_cvt_pk_bf16_f32 v202, v202, v203\n"
      "v_accvgpr_write_b32 a245, v178\n"
      "v_accvgpr_write_b32 a241, v124\n"
      "v_rcp_f32 v158, v158\n"
      "v_cvt_pk_bf16_f32 v208, v120, v121\n"
      "v_pk_mul_f32 v[128:129], v[128:129], v[158:159] op_sel_hi:[1,0]\n"
      "v_cvt_pk_bf16_f32 v164, v164, v165\n"
      "v_pk_mul_f32 v[130:131], v[130:131], v[158:159] op_sel_hi:[1,0]\n"
      "v_cvt_pk_bf16_f32 v147, v132, v133\n"
      "v_pk_mul_f32 v[222:223], v[222:223], v[158:159] op_sel_hi:[1,0]\n"
      "v_pk_mul_f32 v[220:221], v[220:221], v[158:159] op_sel_hi:[1,0]\n"
      "v_mfma_f32_16x16x32_bf16 v[124:127], v[104:107], v[140:143], 0\n"
      "v_cvt_pk_bf16_f32 v136, v136, v137\n"
      "v_cvt_pk_bf16_f32 v148, v148, v148\n"
      "v_cvt_pk_bf16_f32 v176, v176, v177\n"
      "v_mfma_f32_16x16x32_bf16 v[124:127], v[80:83], v[92:95], v[124:127]\n"
      "v_cvt_pk_bf16_f32 v90, v180, 0\n"
      "v_accvgpr_write_b32 a249, v136\n"
      "v_cvt_pk_bf16_f32 v209, v122, v123\n"
      "v_mfma_f32_16x16x32_bf16 v[140:143], v[32:35], v[144:147], 0\n"
      "ds_read_b128 v[178:181], v39 offset:384\n"
      "v_cvt_pk_bf16_f32 v150, v150, v151\n"
      "v_mfma_f32_16x16x32_bf16 v[140:143], v[32:35], v[88:91], v[140:143]\n"
      "ds_write_b128 v3, v[206:209]\n"
      "v_pk_mul_f32 v[168:169], v[168:169], v[170:171] op_sel_hi:[1,0]\n"
      "v_mfma_f32_16x16x32_bf16 v[206:209], v[104:107], v[144:147], 0\n"
      "ds_read_b128 v[210:213], v39 offset:576\n"
      "v_cvt_pk_bf16_f32 v182, v182, v183\n"
      "v_cvt_pk_bf16_f32 v128, v128, v129\n"
      "v_accvgpr_write_b32 a247, v184\n"
      "v_mfma_f32_16x16x32_bf16 v[206:209], v[80:83], v[88:91], v[206:209]\n"
      "s_waitcnt lgkmcnt(3)\n"
      "v_rcp_f32_e64 v140, v140\n"
      "v_cvt_pk_bf16_f32 v243, v110, v111\n"
      "v_mfma_f32_16x16x32_bf16 v[108:111], v[194:197], v[144:147], 0\n"
      "ds_read_b128 v[170:173], v39 offset:640\n"
      "v_cvt_pk_bf16_f32 v162, v162, v163\n"
      "v_accvgpr_write_b32 a246, v182\n"
      "v_mfma_f32_16x16x32_bf16 v[108:111], v[76:79], v[88:91], v[108:111]\n"
      "ds_write_b16 v13, v148\n"
      "v_mfma_f32_16x16x32_bf16 v[198:201], v[154:157], a[228:231], v[116:119]\n"
      "ds_read_b128 v[136:139], v39 offset:512\n"
      "v_accvgpr_write_b32 a244, v176\n"
      "v_mfma_f32_16x16x32_bf16 v[174:177], v[96:99], v[144:147], 0\n"
      "v_accvgpr_write_b32 a225, v164\n"
      "v_cvt_pk_bf16_f32 v166, v166, v167\n"
      "v_mfma_f32_16x16x32_bf16 v[174:177], v[84:87], v[88:91], v[174:177]\n"
      "v_cvt_pk_bf16_f32 v112, v112, v113\n"
      "v_cvt_pk_bf16_f32 v167, v168, v169\n"
      "v_mfma_f32_16x16x32_bf16 v[120:123], v[154:157], a[252:255], v[116:119]\n"
      "s_waitcnt lgkmcnt(3)\n"
      "v_cvt_pk_bf16_f32 v134, v134, v135\n"
      "v_cvt_pk_bf16_f32 v238, v218, v219\n"
      "v_cvt_pk_bf16_f32 v200, v200, v201\n"
      "v_mfma_f32_16x16x32_bf16 v[184:187], v[188:191], a[228:231], v[178:181]\n"
      "ds_write_b64 v4, v[166:167]\n"
      "v_accvgpr_write_b32 a248, v134\n"
      "v_accvgpr_write_b32 a231, v200\n"
      "v_pk_mul_f32 v[124:125], v[124:125], v[158:159] op_sel_hi:[1,0]\n"
      "v_pk_mul_f32 v[126:127], v[126:127], v[158:159] op_sel_hi:[1,0]\n"
      "v_pk_mul_f32 v[108:109], v[108:109], v[140:141] op_sel_hi:[1,0]\n"
      "v_cvt_pk_bf16_f32 v108, v108, v109\n"
      "v_pk_mul_f32 v[176:177], v[176:177], v[140:141] op_sel_hi:[1,0]\n"
      "v_cvt_pk_bf16_f32 v198, v198, v199\n"
      "v_cvt_pk_bf16_f32 v129, v130, v131\n"
      "v_accvgpr_write_b32 a227, v114\n"
      "v_pk_mul_f32 v[174:175], v[174:175], v[140:141] op_sel_hi:[1,0]\n"
      "v_mfma_f32_16x16x32_bf16 v[216:219], v[188:191], v[244:247], v[178:181]\n"
      "ds_read_b128 v[164:167], v39 offset:704\n"
      "v_cvt_pk_bf16_f32 v186, v186, v187\n"
      "v_cvt_pk_bf16_f32 v122, v122, v123\n"
      "v_mfma_f32_16x16x32_bf16 v[158:161], v[154:157], v[244:247], v[116:119]\n"
      "s_waitcnt lgkmcnt(2)\n"
      "v_accvgpr_write_b32 a221, v102\n"
      "v_cvt_pk_bf16_f32 v130, v124, v125\n"
      "v_mfma_f32_16x16x32_bf16 v[132:135], v[188:191], a[236:239], v[170:173]\n"
      "s_add_u32 s42, s42, 1\n"
      "v_cvt_pk_bf16_f32 v131, v126, v127\n"
      "v_accvgpr_write_b32 a224, v162\n"
      "v_cvt_pk_bf16_f32 v220, v220, v221\n"
      "v_cvt_pk_bf16_f32 v221, v222, v223\n"
      "v_mfma_f32_16x16x32_bf16 v[124:127], v[188:191], a[252:255], v[178:181]\n"
      "s_cmp_ge_u32 s95, 0x40000000\n"
      "s_cselect_b64 vcc, -1, s[56:57]\n"
      "v_accvgpr_write_b32 a226, v112\n"
      "v_cvt_pk_bf16_f32 v134, v134, v135\n"
      "v_mfma_f32_16x16x32_bf16 v[180:183], v[154:157], a[232:235], v[210:213]\n"
      "ds_write_b128 v3, v[128:131] offset:256\n"
      "v_pk_mul_f32 v[110:111], v[110:111], v[140:141] op_sel_hi:[1,0]\n"
      "v_cvt_pk_bf16_f32 v120, v120, v121\n"
      "v_pk_mul_f32 v[208:209], v[208:209], v[140:141] op_sel_hi:[1,0]\n"
      "v_pk_mul_f32 v[206:207], v[206:207], v[140:141] op_sel_hi:[1,0]\n"
      "v_mfma_f32_16x16x32_bf16 v[140:143], v[188:191], v[224:227], v[136:139]\n"
      "ds_read_b128 v[144:147], v14\n"
      "v_cvt_pk_bf16_f32 v124, v124, v125\n"
      "v_cvt_pk_bf16_f32 v184, v184, v185\n"
      "v_cvt_pk_bf16_f32 v174, v174, v175\n"
      "v_mfma_f32_16x16x32_bf16 v[128:131], v[188:191], a[232:235], v[136:139]\n"
      "ds_write_b32 v31, v214 offset:4608\n"
      "v_cvt_pk_bf16_f32 v182, v182, v183\n"
      "v_cvt_pk_bf16_f32 v175, v176, v177\n"
      "v_mfma_f32_16x16x32_bf16 v[114:117], v[188:191], v[252:255], v[170:173]\n"
      "v_cvt_pk_bf16_f32 v180, v180, v181\n"
      "v_cvt_pk_bf16_f32 v247, v160, v161\n"
      "v_cvt_pk_bf16_f32 v109, v110, v111\n"
      "v_accvgpr_write_b32 a228, v184\n"
      "v_mfma_f32_16x16x32_bf16 v[160:163], v[154:157], v[224:227], v[210:213]\n"
      "ds_write_b64 v4, v[174:175] offset:512\n"
      "v_mfma_f32_16x16x32_bf16 v[100:103], v[188:191], v[248:251], v[136:139]\n"
      "s_waitcnt lgkmcnt(3)\n"
      "v_accvgpr_write_b32 a230, v198\n"
      "v_cvt_pk_bf16_f32 v244, v216, v217\n"
      "v_mfma_f32_16x16x32_bf16 v[136:139], v[154:157], a[236:239], v[164:167]\n"
      "ds_read_b128 v[198:201], v14 offset:64\n"
      "v_cvt_pk_bf16_f32 v128, v128, v129\n"
      "v_cvt_pk_bf16_f32 v132, v132, v133\n"
      "v_cvt_pk_bf16_f32 v226, v160, v161\n"
      "v_cvt_pk_bf16_f32 v126, v126, v127\n"
      "v_cvt_pk_bf16_f32 v111, v208, v209\n"
      "v_accvgpr_write_b32 a222, v150\n"
      "v_mfma_f32_16x16x32_bf16 v[148:151], v[154:157], v[228:231], v[164:167]\n"
      "ds_write_b64 v4, v[220:221] offset:256\n"
      "v_mfma_f32_16x16x32_bf16 v[220:223], v[188:191], v[228:231], v[170:173]\n"
      "v_cvt_pk_bf16_f32 v227, v162, v163\n"
      "v_cvt_pk_bf16_f32 v110, v206, v207\n"
      "v_cvt_pk_bf16_f32 v246, v158, v159\n"
      "v_mfma_f32_16x16x32_bf16 v[206:209], v[154:157], v[248:251], v[210:213]\n"
      "ds_write_b128 v3, v[108:111] offset:512\n"
      "v_cvt_pk_bf16_f32 v138, v138, v139\n"
      "v_mfma_f32_16x16x32_bf16 v[188:191], v[154:157], v[252:255], v[164:167]\n"
      "s_waitcnt lgkmcnt(2)\n"
      "v_accvgpr_write_b32 a250, v202\n"
      "v_accvgpr_write_b32 a254, v120\n"
      "v_cvt_pk_bf16_f32 v225, v142, v143\n"
      "v_cvt_pk_bf16_f32 v224, v140, v141\n"
      "v_cvt_pk_bf16_f32 v231, v150, v151\n"
      "v_accvgpr_write_b32 a252, v124\n"
      "v_accvgpr_write_b32 a251, v204\n"
      "v_cvt_pk_bf16_f32 v230, v148, v149\n"
      "v_accvgpr_write_b32 a239, v138\n"
      "v_cvt_pk_bf16_f32 v254, v188, v189\n"
      "v_accvgpr_write_b32 a236, v132\n"
      "v_cvt_pk_bf16_f32 v253, v116, v117\n"
      "v_cvt_pk_bf16_f32 v228, v220, v221\n"
      "v_cvt_pk_bf16_f32 v255, v190, v191\n"
      "v_cvt_pk_bf16_f32 v130, v130, v131\n"
      "v_cvt_pk_bf16_f32 v252, v114, v115\n"
      "v_accvgpr_write_b32 a234, v180\n"
      "v_accvgpr_write_b32 a253, v126\n"
      "v_accvgpr_write_b32 a237, v134\n"
      "v_accvgpr_write_b32 a235, v182\n"
      "v_cvt_pk_bf16_f32 v249, v102, v103\n"
      "v_accvgpr_write_b32 a223, v152\n"
      "v_cvt_pk_bf16_f32 v136, v136, v137\n"
      "v_accvgpr_write_b32 a238, v136\n"
      "v_accvgpr_write_b32 a233, v130\n"
      "v_cvt_pk_bf16_f32 v248, v100, v101\n"
      "v_cvt_pk_bf16_f32 v229, v222, v223\n"
      "v_cvt_pk_bf16_f32 v250, v206, v207\n"
      "v_accvgpr_write_b32 a255, v122\n"
      "v_mfma_f32_16x16x32_bf16 v[162:165], v[144:147], v[32:35], 0\n"
      "v_cvt_pk_bf16_f32 v245, v218, v219\n"
      "v_accvgpr_write_b32 a229, v186\n"
      "v_cvt_pk_bf16_f32 v251, v208, v209\n"
      "v_accvgpr_write_b32 a232, v128\n"
      "v_mfma_f32_16x16x32_bf16 v[162:165], v[198:201], v[32:35], v[162:165]\n"
      "s_add_u32 s10, s98, L_cold_%=-L_ipf0_%=\n"
      "s_addc_u32 s11, s99, 0\n"
      "s_mov_b32 s12, L_cend_%=-L_cold_%=\n"
      "s_sub_u32 s9, s9, 1280\n"
      "v_subrev_u32 v208, 115456, v9\n"
      "s_movk_i32 s13, 80\n"
      "v_cmp_gt_u32 s[14:15], s13, v208\n"
      "v_lshl_add_u32 v208, v208, 3, s9\n"
      "s_cmp_lt_u32 s40, 2\n"
      "s_cselect_b64 s[0:1], s[16:17], s[10:11]\n"
      "s_cselect_b32 s2, s18, s12\n"
      "s_cselect_b32 s8, -1, 0\n"
      "s_cselect_b64 s[14:15], 0, s[14:15]\n"
      "s_mov_b32 s3, s19\n"
      "s_add_u32 s6, s64, 16384\n"
      "s_cmp_lt_u32 s40, 2\n"
      "s_cselect_b32 s6, s6, 122112\n"
      "s_add_u32 s7, s64, 34816\n"
      "s_cmp_lt_u32 s40, 2\n"
      "s_cselect_b32 s7, s7, 122112\n"
      "s_and_b32 s5, s84, s8\n"
      "v_cndmask_b32 v15, v15, v208, s[14:15]\n"
      "s_waitcnt lgkmcnt(0)\n"
      "s_barrier\n"
      "s_or_b64 vcc, vcc, s[54:55]\n"
      "s_cbranch_vccnz L_disp1_%=\n"
      "s_nop 7\n"
      "s_nop 7\n"
      "L_loop1_%=:\n"
      "v_mfma_f32_16x16x32_bf16 v[152:155], v[144:147], v[194:197], 0\n"
      "ds_read_b128 v[186:189], v0 offset:18432\n"
      "v_mfma_f32_16x16x32_bf16 v[152:155], v[198:201], v[76:79], v[152:155]\n"
      "ds_read_b128 v[148:151], v9\n"
      "ds_read_b128 v[114:117], v0 offset:24576\n"
      "v_mfma_f32_16x16x32_bf16 v[210:213], v[144:147], v[104:107], 0\n"
      "ds_read_b128 v[100:103], v1 offset:30720\n"
      "s_add_u32 m0, s64, 0\n"
      "v_mfma_f32_16x16x32_bf16 v[210:213], v[198:201], v[80:83], v[210:213]\n"
      "buffer_load_dwordx4 v16, s[16:19], s84 offen  lds\n"
      "v_mfma_f32_16x16x32_bf16 v[180:183], v[144:147], v[96:99], 0\n"
      "ds_read_b128 v[144:147], v1 offset:18432\n"
      "s_add_u32 m0, s64, 4096\n"
      "v_mfma_f32_16x16x32_bf16 v[180:183], v[198:201], v[84:87], v[180:183]\n"
      "ds_read_b128 v[158:161], v1 offset:24576\n"
      "s_waitcnt lgkmcnt(5)\n"
      "v_mfma_f32_16x16x32_bf16 v[198:201], a[0:3], v[186:189], 0\n"
      "ds_read_b128 v[136:139], v1 offset:26624\n"
      "v_readlane_b32 s46, v37, s42\n"
      "v_mfma_f32_16x16x32_bf16 v[122:125], a[24:27], v[186:189], 0\n"
      "s_waitcnt lgkmcnt(5)\n"
      "v_add_f32 v150, v150, v151\n"
      "v_add_f32 v148, v148, v149\n"
      "v_mfma_f32_16x16x32_bf16 v[182:185], a[48:51], v[186:189], 0\n"
      "ds_read_b128 v[214:217], v0 offset:30720\n"
      "v_add_f32 v148, v148, v150\n"
      "v_mfma_f32_16x16x32_bf16 v[190:193], a[72:75], v[186:189], 0\n"
      "buffer_load_dwordx4 v17, s[16:19], s84 offen  lds\n"
      "v_mfma_f32_16x16x32_bf16 v[202:205], a[96:99], v[186:189], 0\n"
      "s_waitcnt lgkmcnt(3)\n"
      "v_rsq_f32 v148, v148\n"
      "v_mfma_f32_16x16x32_bf16 v[154:157], a[120:123], v[186:189], 0\n"
      "s_add_u32 m0, s64, 8192\n"
      "v_mul_f32 v148, 0x415db3d7, v148\n"
      "v_mov_b32 v150, v148\n"
      "v_mfma_f32_16x16x32_bf16 v[198:201], a[4:7], v[144:147], v[198:201]\n"
      "s_nop 0\n"
      "v_permlane32_swap_b32 v148, v150\n"
      "v_mfma_f32_16x16x32_bf16 v[198:201], a[8:11], v[114:117], v[198:201]\n"
      "ds_read_b128 v[128:131], v9 offset:4608\n"
      "v_cndmask_b32 v152, v152, v210, s[58:59]\n"
      "v_mfma_f32_16x16x32_bf16 v[122:125], a[28:31], v[144:147], v[122:125]\n"
      "v_mov_b32 v149, v148\n"
      "v_mul_f32 v169, v150, v169\n"
      "v_mfma_f32_16x16x32_bf16 v[122:125], a[32:35], v[114:117], v[122:125]\n"
      "ds_read_b128 v[210:213], v1 offset:20480\n"
      "v_readlane_b32 s47, v38, s43\n"
      "v_mfma_f32_16x16x32_bf16 v[182:185], a[52:55], v[144:147], v[182:185]\n"
      "buffer_load_dwordx4 v18, s[16:19], s84 offen  lds\n"
      "v_mfma_f32_16x16x32_bf16 v[182:185], a[56:59], v[114:117], v[182:185]\n"
      "v_permlane16_swap_b32 v148, v149\n"
      "v_mfma_f32_16x16x32_bf16 v[190:193], a[76:79], v[144:147], v[190:193]\n"
      "v_mul_f32 v209, v149, v209\n"
      "v_mul_f32_e64 v178, v149, v178\n"
      "v_mfma_f32_16x16x32_bf16 v[190:193], a[80:83], v[114:117], v[190:193]\n"
      "s_add_u32 m0, s64, 12288\n"
      "v_mul_f32 v143, v148, v143\n"
      "v_mul_f32 v173, v148, v173\n"
      "v_mfma_f32_16x16x32_bf16 v[202:205], a[100:103], v[144:147], v[202:205]\n"
      "v_mul_f32 v206, v149, v206\n"
      "v_mul_f32 v176, v149, v176\n"
      "v_mfma_f32_16x16x32_bf16 v[202:205], a[104:107], v[114:117], v[202:205]\n"
      "buffer_load_dwordx4 v19, s[16:19], s84 offen  lds\n"
      "v_mfma_f32_16x16x32_bf16 v[154:157], a[124:127], v[144:147], v[154:157]\n"
      "v_mul_f32 v207, v149, v207\n"
      "v_mul_f32 v175, v148, v175\n"
      "v_mfma_f32_16x16x32_bf16 v[154:157], a[128:131], v[114:117], v[154:157]\n"
      "s_waitcnt lgkmcnt(0)\n"
      "v_mul_f32 v221, v148, v221\n"
      "v_mul_f32_e64 v220, v148, v220\n"
      "v_mfma_f32_16x16x32_bf16 v[198:201], a[12:15], v[158:161], v[198:201]\n"
      "v_mul_f32 v223, v148, v223\n"
      "v_mul_f32 v177, v149, v177\n"
      "v_mfma_f32_16x16x32_bf16 v[198:201], a[16:19], v[214:217], v[198:201]\n"
      "v_cvt_pk_bf16_f32 v206, v206, v207\n"
      "v_mul_f32 v135, v150, v135\n"
      "v_mfma_f32_16x16x32_bf16 v[198:201], a[20:23], v[100:103], v[198:201]\n"
      "s_mov_b32 m0, s6\n"
      "v_mul_f32_e64 v172, v148, v172\n"
      "v_cndmask_b32 v152, v152, v180, s[60:61]\n"
      "v_mfma_f32_16x16x32_bf16 v[122:125], a[36:39], v[158:161], v[122:125]\n"
      "v_mov_b32_dpp v162, v162 quad_perm:[0,0,0,0] row_mask:0xf bank_mask:0xf\n"
      "v_cvt_pk_bf16_f32 v172, v172, v173\n"
      "v_mfma_f32_16x16x32_bf16 v[122:125], a[40:43], v[214:217], v[122:125]\n"
      "v_mul_f32 v174, v148, v174\n"
      "v_fma_f32 v76, v198, v198, s51\n"
      "v_mfma_f32_16x16x32_bf16 v[122:125], a[44:47], v[100:103], v[122:125]\n"
      "v_cvt_pk_bf16_f32 v173, v174, v175\n"
      "v_mul_f32 v108, v149, v108\n"
      "v_mfma_f32_16x16x32_bf16 v[182:185], a[60:63], v[158:161], v[182:185]\n"
      "buffer_load_dwordx4 v15, s[0:3], s5 offen  lds\n"
      "v_mfma_f32_16x16x32_bf16 v[182:185], a[64:67], v[214:217], v[182:185]\n"
      "v_rcp_f32_e64 v162, v162\n"
      "v_mfma_f32_16x16x32_bf16 v[182:185], a[68:71], v[100:103], v[182:185]\n"
      "v_mul_f32_e64 v222, v148, v222\n"
      "v_cvt_pk_bf16_f32 v174, v220, v221\n"
      "v_mfma_f32_16x16x32_bf16 v[190:193], a[84:87], v[158:161], v[190:193]\n"
      "v_cvt_pk_bf16_f32 v175, v222, v223\n"
      "v_fmac_f32_e64 v76, v199, v199\n"
      "v_mfma_f32_16x16x32_bf16 v[190:193], a[88:91], v[214:217], v[190:193]\n"
      "ds_read_b128 v[218:221], v0 offset:26624\n"
      "v_or3_b32 v128, v128, v129, v130\n"
      "v_mfma_f32_16x16x32_bf16 v[190:193], a[92:95], v[100:103], v[190:193]\n"
      "buffer_load_dwordx4 v[164:167], v36, s[28:31], s86 offen\n"
      "v_mfma_f32_16x16x32_bf16 v[202:205], a[108:111], v[158:161], v[202:205]\n"
      "v_mul_f32 v109, v149, v109\n"
      "v_fmac_f32 v76, v200, v200\n"
      "v_mfma_f32_16x16x32_bf16 v[202:205], a[112:115], v[214:217], v[202:205]\n"
      "ds_write_b128 v5, v[172:175] offset:18432\n"
      "v_mfma_f32_16x16x32_bf16 v[202:205], a[116:119], v[100:103], v[202:205]\n"
      "v_mul_f32 v208, v149, v208\n"
      "v_fmac_f32 v76, v201, v201\n"
      "v_mfma_f32_16x16x32_bf16 v[154:157], a[132:135], v[158:161], v[154:157]\n"
      "v_fma_f32 v130, v190, v190, s51\n"
      "v_mul_f32_e64 v179, v149, v179\n"
      "v_mfma_f32_16x16x32_bf16 v[154:157], a[136:139], v[214:217], v[154:157]\n"
      "v_fmac_f32 v130, v191, v191\n"
      "v_fmac_f32 v76, v122, v122\n"
      "v_mfma_f32_16x16x32_bf16 v[154:157], a[140:143], v[100:103], v[154:157]\n"
      "v_mul_f32 v110, v149, v110\n"
      "v_fmac_f32 v130, v192, v192\n"
      "v_mfma_f32_16x16x32_bf16 v[194:197], v[186:189], a[144:147], 0\n"
      "v_mul_f32_e64 v118, v150, v118\n"
      "v_mfma_f32_16x16x32_bf16 v[194:197], v[144:147], a[148:151], v[194:197]\n"
      "buffer_load_dword v88, v29, s[28:31], s86 offen\n"
      "v_mfma_f32_16x16x32_bf16 v[194:197], v[114:117], a[152:155], v[194:197]\n"
      "ds_read_b128 v[172:175], v0 offset:20480\n"
      "v_mul_f32_e64 v140, v148, v140\n"
      "v_mfma_f32_16x16x32_bf16 v[194:197], v[158:161], a[156:159], v[194:197]\n"
      "v_cvt_pk_bf16_f32 v207, v208, v209\n"
      "v_mul_f32 v111, v149, v111\n"
      "v_mfma_f32_16x16x32_bf16 v[194:197], v[214:217], a[160:163], v[194:197]\n"
      "v_mul_f32 v142, v148, v142\n"
      "v_cvt_pk_bf16_f32 v208, v176, v177\n"
      "v_mfma_f32_16x16x32_bf16 v[194:197], v[100:103], a[164:167], v[194:197]\n"
      "v_mul_f32 v133, v150, v133\n"
      "v_mul_f32 v134, v150, v134\n"
      "v_mfma_f32_16x16x32_bf16 v[104:107], v[186:189], a[168:171], 0\n"
      "v_cvt_pk_bf16_f32 v209, v178, v179\n"
      "v_fmac_f32 v130, v193, v193\n"
      "v_mfma_f32_16x16x32_bf16 v[104:107], v[144:147], a[172:175], v[104:107]\n"
      "v_mul_f32 v121, v150, v121\n"
      "v_fmac_f32_e64 v130, v202, v202\n"
      "v_mfma_f32_16x16x32_bf16 v[104:107], v[114:117], a[176:179], v[104:107]\n"
      "ds_write_b128 v5, v[206:209] offset:24576\n"
      "v_mfma_f32_16x16x32_bf16 v[104:107], v[158:161], a[180:183], v[104:107]\n"
      "v_fmac_f32_e64 v76, v123, v123\n"
      "v_cvt_pk_bf16_f32 v194, v194, v195\n"
      "v_mfma_f32_16x16x32_bf16 v[104:107], v[214:217], a[184:187], v[104:107]\n"
      "v_fmac_f32 v76, v124, v124\n"
      "v_fmac_f32 v130, v203, v203\n"
      "v_mfma_f32_16x16x32_bf16 v[104:107], v[100:103], a[188:191], v[104:107]\n"
      "ds_read_b128 v[206:209], v1 offset:32768\n"
      "v_mul_f32 v141, v148, v141\n"
      "v_mfma_f32_16x16x32_bf16 v[96:99], v[186:189], a[192:195], 0\n"
      "v_cvt_pk_bf16_f32 v108, v108, v109\n"
      "v_fmac_f32 v130, v204, v204\n"
      "v_mfma_f32_16x16x32_bf16 v[96:99], v[144:147], a[196:199], v[96:99]\n"
      "v_readlane_b32 s45, v37, s43\n"
      "v_mul_f32 v170, v150, v170\n"
      "v_mfma_f32_16x16x32_bf16 v[96:99], v[114:117], a[200:203], v[96:99]\n"
      "s_waitcnt lgkmcnt(2)\n"
      "v_bitop3_b32 v128, v128, v131, s33 bitop3:0xa8\n"
      "v_fmac_f32 v130, v205, v205\n"
      "v_mfma_f32_16x16x32_bf16 v[96:99], v[158:161], a[204:207], v[96:99]\n"
      "ds_read_b128 v[158:161], v0 offset:32768\n"
      "v_mul_f32 v132, v150, v132\n"
      "v_mfma_f32_16x16x32_bf16 v[96:99], v[214:217], a[208:211], v[96:99]\n"
      "s_or_b32 s95, s45, s46\n"
      "v_fmac_f32 v130, v154, v154\n"
      "v_fmac_f32 v130, v155, v155\n"
      "v_mfma_f32_16x16x32_bf16 v[96:99], v[100:103], a[212:215], v[96:99]\n"
      "ds_read_b128 v[100:103], v0 offset:28672\n"
      "v_mul_f32 v120, v150, v120\n"
      "v_mfma_f32_16x16x32_bf16 v[112:115], a[0:3], v[172:175], 0\n"
      "v_fmac_f32 v130, v156, v156\n"
      "v_fmac_f32 v130, v157, v157\n"
      "v_mfma_f32_16x16x32_bf16 v[112:115], a[4:7], v[210:213], v[112:115]\n"
      "v_cvt_pk_bf16_f32 v140, v140, v141\n"
      "v_fmac_f32 v76, v125, v125\n"
      "v_mfma_f32_16x16x32_bf16 v[112:115], a[8:11], v[218:221], v[112:115]\n"
      "v_fmac_f32 v76, v182, v182\n"
      "v_cmp_ne_u32 s[56:57], 0, v128\n"
      "v_mfma_f32_16x16x32_bf16 v[112:115], a[12:15], v[136:139], v[112:115]\n"
      "s_waitcnt lgkmcnt(1)\n"
      "v_mul_f32 v168, v150, v168\n"
      "v_fmac_f32 v76, v183, v183\n"
      "v_mfma_f32_16x16x32_bf16 v[112:115], a[16:19], v[158:161], v[112:115]\n"
      "v_mul_f32 v119, v150, v119\n"
      "v_fmac_f32_e64 v76, v184, v184\n"
      "v_mfma_f32_16x16x32_bf16 v[112:115], a[20:23], v[206:209], v[112:115]\n"
      "v_cvt_pk_bf16_f32 v132, v132, v133\n"
      "v_fmac_f32 v76, v185, v185\n"
      "v_mfma_f32_16x16x32_bf16 v[186:189], a[24:27], v[172:175], 0\n"
      "s_or_b32 s95, s95, s48\n"
      "v_mul_f32 v152, v162, v152\n"
      "v_cvt_pk_bf16_f32 v133, v134, v135\n"
      "v_mfma_f32_16x16x32_bf16 v[186:189], a[28:31], v[210:213], v[186:189]\n"
      "v_permlane16_swap_b32 v76, v130\n"
      "v_mfma_f32_16x16x32_bf16 v[186:189], a[32:35], v[218:221], v[186:189]\n"
      "v_fma_f32 v77, v112, v112, s51\n"
      "v_cvt_pk_bf16_f32 v141, v142, v143\n"
      "v_mfma_f32_16x16x32_bf16 v[186:189], a[36:39], v[136:139], v[186:189]\n"
      "ds_write_b64 v6, v[140:141] offset:18432\n"
      "v_mfma_f32_16x16x32_bf16 v[186:189], a[40:43], v[158:161], v[186:189]\n"
      "v_add_f32 v76, v76, v130\n"
      "v_fmac_f32 v77, v113, v113\n"
      "v_mfma_f32_16x16x32_bf16 v[186:189], a[44:47], v[206:209], v[186:189]\n"
      "v_fmac_f32 v77, v114, v114\n"
      "v_fmac_f32 v77, v115, v115\n"
      "v_mfma_f32_16x16x32_bf16 v[214:217], a[48:51], v[172:175], 0\n"
      "v_cvt_pk_bf16_f32 v109, v110, v111\n"
      "v_mov_b32 v130, v76\n"
      "v_mfma_f32_16x16x32_bf16 v[214:217], a[52:55], v[210:213], v[214:217]\n"
      "s_nop 0\n"
      "v_permlane32_swap_b32 v76, v130\n"
      "v_mfma_f32_16x16x32_bf16 v[214:217], a[56:59], v[218:221], v[214:217]\n"
      "v_cvt_pk_bf16_f32 v104, v104, v105\n"
      "v_fmac_f32 v77, v186, v186\n"
      "v_mfma_f32_16x16x32_bf16 v[214:217], a[60:63], v[136:139], v[214:217]\n"
      "ds_write_b64 v6, v[108:109] offset:24576\n"
      "v_mfma_f32_16x16x32_bf16 v[214:217], a[64:67], v[158:161], v[214:217]\n"
      "v_add_f32_e64 v76, v76, v130\n"
      "v_cvt_pk_bf16_f32 v134, v118, v119\n"
      "v_mfma_f32_16x16x32_bf16 v[214:217], a[68:71], v[206:209], v[214:217]\n"
      "v_cvt_pk_bf16_f32 v96, v96, v97\n"
      "v_fmac_f32 v77, v187, v187\n"
      "v_mfma_f32_16x16x32_bf16 v[144:147], a[72:75], v[172:175], 0\n"
      "v_rsq_f32 v76, v76\n"
      "v_mfma_f32_16x16x32_bf16 v[144:147], a[76:79], v[210:213], v[144:147]\n"
      "v_mul_f32 v171, v150, v171\n"
      "v_fmac_f32 v77, v188, v188\n"
      "v_mfma_f32_16x16x32_bf16 v[144:147], a[80:83], v[218:221], v[144:147]\n"
      "v_cvt_pk_bf16_f32 v135, v120, v121\n"
      "v_mov_b32 v130, v76\n"
      "v_mfma_f32_16x16x32_bf16 v[144:147], a[84:87], v[136:139], v[144:147]\n"
      "ds_write_b128 v5, v[132:135] offset:30720\n"
      "v_mfma_f32_16x16x32_bf16 v[144:147], a[88:91], v[158:161], v[144:147]\n"
      "v_permlane16_swap_b32 v76, v130\n"
      "v_mfma_f32_16x16x32_bf16 v[144:147], a[92:95], v[206:209], v[144:147]\n"
      "v_cvt_pk_bf16_f32 v105, v106, v107\n"
      "v_mul_f32 v76, s50, v76\n"
      "v_mfma_f32_16x16x32_bf16 v[116:119], a[96:99], v[172:175], 0\n"
      "v_mul_f32 v201, v76, v201\n"
      "v_mul_f32 v183, v76, v183\n"
      "v_mfma_f32_16x16x32_bf16 v[116:119], a[100:103], v[210:213], v[116:119]\n"
      "v_fmac_f32 v77, v189, v189\n"
      "v_mul_f32_e64 v199, v76, v199\n"
      "v_mfma_f32_16x16x32_bf16 v[116:119], a[104:107], v[218:221], v[116:119]\n"
      "v_cvt_pk_bf16_f32 v168, v168, v169\n"
      "v_fma_f32 v81, v144, v144, s51\n"
      "v_mfma_f32_16x16x32_bf16 v[116:119], a[108:111], v[136:139], v[116:119]\n"
      "v_cvt_pk_bf16_f32 v152, v152, v152\n"
      "v_fmac_f32 v81, v145, v145\n"
      "v_mfma_f32_16x16x32_bf16 v[116:119], a[112:115], v[158:161], v[116:119]\n"
      "v_mul_f32 v157, v130, v157\n"
      "v_cvt_pk_bf16_f32 v195, v196, v197\n"
      "v_mfma_f32_16x16x32_bf16 v[116:119], a[116:119], v[206:209], v[116:119]\n"
      "v_mul_f32 v190, v130, v190\n"
      "v_fmac_f32 v77, v214, v214\n"
      "v_mfma_f32_16x16x32_bf16 v[148:151], a[120:123], v[172:175], 0\n"
      "v_fmac_f32 v81, v146, v146\n"
      "v_mul_f32 v198, v76, v198\n"
      "v_mfma_f32_16x16x32_bf16 v[148:151], a[124:127], v[210:213], v[148:151]\n"
      "v_mul_f32 v192, v130, v192\n"
      "v_cvt_pk_bf16_f32 v198, v198, v199\n"
      "v_mfma_f32_16x16x32_bf16 v[148:151], a[128:131], v[218:221], v[148:151]\n"
      "v_cvt_pk_bf16_f32 v169, v170, v171\n"
      "v_mul_f32 v122, v76, v122\n"
      "v_mfma_f32_16x16x32_bf16 v[148:151], a[132:135], v[136:139], v[148:151]\n"
      "v_mul_f32 v204, v130, v204\n"
      "v_fmac_f32 v81, v147, v147\n"
      "v_mfma_f32_16x16x32_bf16 v[148:151], a[136:139], v[158:161], v[148:151]\n"
      "v_fmac_f32 v77, v215, v215\n"
      "v_fmac_f32 v81, v116, v116\n"
      "v_mfma_f32_16x16x32_bf16 v[148:151], a[140:143], v[206:209], v[148:151]\n"
      "v_fmac_f32 v81, v117, v117\n"
      "v_fmac_f32 v81, v118, v118\n"
      "v_mfma_f32_16x16x32_bf16 v[126:129], v[172:175], a[144:147], 0\n"
      "v_mul_f32 v182, v76, v182\n"
      "v_mul_f32 v184, v76, v184\n"
      "v_mfma_f32_16x16x32_bf16 v[126:129], v[210:213], a[148:151], v[126:129]\n"
      "ds_read_b128 v[108:111], v0 offset:22528\n"
      "v_fmac_f32 v81, v119, v119\n"
      "v_mfma_f32_16x16x32_bf16 v[126:129], v[218:221], a[152:155], v[126:129]\n"
      "v_mul_f32 v203, v130, v203\n"
      "v_fmac_f32 v81, v148, v148\n"
      "v_mfma_f32_16x16x32_bf16 v[126:129], v[136:139], a[156:159], v[126:129]\n"
      "v_mul_f32 v185, v76, v185\n"
      "v_fmac_f32_e64 v77, v216, v216\n"
      "v_mfma_f32_16x16x32_bf16 v[126:129], v[158:161], a[160:163], v[126:129]\n"
      "ds_write_b64 v6, v[168:169] offset:30720\n"
      "v_mfma_f32_16x16x32_bf16 v[126:129], v[206:209], a[164:167], v[126:129]\n"
      "v_mul_f32 v205, v130, v205\n"
      "v_fmac_f32 v81, v149, v149\n"
      "v_mfma_f32_16x16x32_bf16 v[132:135], v[172:175], a[168:171], 0\n"
      "ds_read_b128 v[168:171], v1 offset:22528\n"
      "v_cvt_pk_bf16_f32 v182, v182, v183\n"
      "v_mfma_f32_16x16x32_bf16 v[132:135], v[210:213], a[172:175], v[132:135]\n"
      "v_fmac_f32 v81, v150, v150\n"
      "v_fmac_f32 v81, v151, v151\n"
      "v_mfma_f32_16x16x32_bf16 v[132:135], v[218:221], a[176:179], v[132:135]\n"
      "ds_read_b128 v[140:143], v1 offset:34816\n"
      "v_cvt_pk_bf16_f32 v183, v184, v185\n"
      "v_mfma_f32_16x16x32_bf16 v[132:135], v[136:139], a[180:183], v[132:135]\n"
      "v_fmac_f32 v77, v217, v217\n"
      "v_mul_f32 v155, v130, v155\n"
      "v_mfma_f32_16x16x32_bf16 v[132:135], v[158:161], a[184:187], v[132:135]\n"
      "v_permlane16_swap_b32 v77, v81\n"
      "v_mfma_f32_16x16x32_bf16 v[132:135], v[206:209], a[188:191], v[132:135]\n"
      "v_mul_f32 v191, v130, v191\n"
      "v_mul_f32 v125, v76, v125\n"
      "v_mfma_f32_16x16x32_bf16 v[178:181], v[172:175], a[192:195], 0\n"
      "v_add_f32 v77, v77, v81\n"
      "v_mov_b32_e64 v81, v77\n"
      "v_mfma_f32_16x16x32_bf16 v[178:181], v[210:213], a[196:199], v[178:181]\n"
      "ds_read_b128 v[172:175], v1 offset:28672\n"
      "v_mul_f32 v193, v130, v193\n"
      "v_mfma_f32_16x16x32_bf16 v[178:181], v[218:221], a[200:203], v[178:181]\n"
      "v_permlane32_swap_b32 v77, v81\n"
      "v_mfma_f32_16x16x32_bf16 v[178:181], v[136:139], a[204:207], v[178:181]\n"
      "ds_read_b128 v[220:223], v0 offset:34816\n"
      "v_mul_f32_e64 v200, v76, v200\n"
      "v_mfma_f32_16x16x32_bf16 v[178:181], v[158:161], a[208:211], v[178:181]\n"
      "v_cvt_pk_bf16_f32 v190, v190, v191\n"
      "v_cvt_pk_bf16_f32 v196, v126, v127\n"
      "v_mfma_f32_16x16x32_bf16 v[178:181], v[206:209], a[212:215], v[178:181]\n"
      "s_waitcnt lgkmcnt(2)\n"
      "v_mul_f32 v154, v130, v154\n"
      "v_add_f32_e64 v77, v77, v81\n"
      "v_mfma_f32_16x16x32_bf16 v[158:161], a[0:3], v[108:111], 0\n"
      "s_cmp_ge_u32 s95, 0x40000000\n"
      "s_cselect_b32 s94, -1, -2\n"
      "v_cvt_pk_bf16_f32 v97, v98, v99\n"
      "v_mfma_f32_16x16x32_bf16 v[158:161], a[4:7], v[168:171], v[158:161]\n"
      "v_cvt_pk_bf16_f32 v154, v154, v155\n"
      "v_cvt_pk_bf16_f32 v106, v132, v133\n"
      "v_mfma_f32_16x16x32_bf16 v[158:161], a[8:11], v[100:103], v[158:161]\n"
      "s_waitcnt lgkmcnt(1)\n"
      "v_rsq_f32 v77, v77\n"
      "v_mfma_f32_16x16x32_bf16 v[158:161], a[12:15], v[172:175], v[158:161]\n"
      "s_waitcnt lgkmcnt(0)\n"
      "v_mul_f32 v202, v130, v202\n"
      "v_mov_b32 v81, v77\n"
      "v_mfma_f32_16x16x32_bf16 v[158:161], a[16:19], v[220:223], v[158:161]\n"
      "s_nop 0\n"
      "v_permlane16_swap_b32 v77, v81\n"
      "v_mfma_f32_16x16x32_bf16 v[158:161], a[20:23], v[140:143], v[158:161]\n"
      "v_mul_f32 v77, s50, v77\n"
      "v_mul_f32_e64 v113, v77, v113\n"
      "v_mfma_f32_16x16x32_bf16 v[206:209], a[24:27], v[108:111], 0\n"
      "ds_write_b16 v12, v152\n"
      "v_mfma_f32_16x16x32_bf16 v[206:209], a[28:31], v[168:171], v[206:209]\n"
      "v_mul_f32_e64 v112, v77, v112\n"
      "v_cvt_pk_bf16_f32 v112, v112, v113\n"
      "v_mfma_f32_16x16x32_bf16 v[206:209], a[32:35], v[100:103], v[206:209]\n"
      "v_mul_f32 v217, v77, v217\n"
      "v_mul_f32 v186, v77, v186\n"
      "v_mfma_f32_16x16x32_bf16 v[206:209], a[36:39], v[172:175], v[206:209]\n"
      "v_fma_f32 v176, v158, v158, s51\n"
      "v_mul_f32 v215, v77, v215\n"
      "v_mfma_f32_16x16x32_bf16 v[206:209], a[40:43], v[220:223], v[206:209]\n"
      "v_mul_f32 v189, v77, v189\n"
      "v_fmac_f32 v176, v159, v159\n"
      "v_mfma_f32_16x16x32_bf16 v[206:209], a[44:47], v[140:143], v[206:209]\n"
      "v_mul_f32 v123, v76, v123\n"
      "v_mul_f32 v114, v77, v114\n"
      "v_mfma_f32_16x16x32_bf16 v[136:139], a[48:51], v[108:111], 0\n"
      "v_mul_f32 v214, v77, v214\n"
      "v_mul_f32 v115, v77, v115\n"
      "v_mfma_f32_16x16x32_bf16 v[136:139], a[52:55], v[168:171], v[136:139]\n"
      "v_mul_f32 v144, v81, v144\n"
      "v_fmac_f32 v176, v160, v160\n"
      "v_mfma_f32_16x16x32_bf16 v[136:139], a[56:59], v[100:103], v[136:139]\n"
      "v_cvt_pk_bf16_f32 v191, v192, v193\n"
      "v_mul_f32 v118, v81, v118\n"
      "v_mfma_f32_16x16x32_bf16 v[136:139], a[60:63], v[172:175], v[136:139]\n"
      "v_mul_f32 v187, v77, v187\n"
      "v_mul_f32 v145, v81, v145\n"
      "v_mfma_f32_16x16x32_bf16 v[136:139], a[64:67], v[220:223], v[136:139]\n"
      "v_mul_f32 v188, v77, v188\n"
      "v_mul_f32 v119, v81, v119\n"
      "v_mfma_f32_16x16x32_bf16 v[136:139], a[68:71], v[140:143], v[136:139]\n"
      "v_mul_f32 v156, v130, v156\n"
      "v_cvt_pk_bf16_f32 v113, v114, v115\n"
      "v_mfma_f32_16x16x32_bf16 v[210:213], a[72:75], v[108:111], 0\n"
      "v_cvt_pk_bf16_f32 v199, v200, v201\n"
      "v_fmac_f32 v176, v161, v161\n"
      "v_mfma_f32_16x16x32_bf16 v[210:213], a[76:79], v[168:171], v[210:213]\n"
      "v_fmac_f32 v176, v206, v206\n"
      "v_cvt_pk_bf16_f32 v99, v180, v181\n"
      "v_mfma_f32_16x16x32_bf16 v[210:213], a[80:83], v[100:103], v[210:213]\n"
      "v_mul_f32 v149, v81, v149\n"
      "v_mul_f32_e64 v151, v81, v151\n"
      "v_mfma_f32_16x16x32_bf16 v[210:213], a[84:87], v[172:175], v[210:213]\n"
      "v_cvt_pk_bf16_f32 v197, v128, v129\n"
      "v_cvt_pk_bf16_f32 v193, v204, v205\n"
      "v_mfma_f32_16x16x32_bf16 v[210:213], a[88:91], v[220:223], v[210:213]\n"
      "v_cvt_pk_bf16_f32 v214, v214, v215\n"
      "v_mul_f32_e64 v117, v81, v117\n"
      "v_mfma_f32_16x16x32_bf16 v[210:213], a[92:95], v[140:143], v[210:213]\n"
      "v_cvt_pk_bf16_f32 v200, v122, v123\n"
      "v_mul_f32 v148, v81, v148\n"
      "v_mfma_f32_16x16x32_bf16 v[120:123], a[96:99], v[108:111], 0\n"
      "ds_read_b128 v[130:133], v2\n"
      "v_mul_f32 v124, v76, v124\n"
      "v_mfma_f32_16x16x32_bf16 v[120:123], a[100:103], v[168:171], v[120:123]\n"
      "v_mul_f32_e64 v116, v81, v116\n"
      "v_cvt_pk_bf16_f32 v115, v188, v189\n"
      "v_mfma_f32_16x16x32_bf16 v[120:123], a[104:107], v[100:103], v[120:123]\n"
      "v_cvt_pk_bf16_f32 v155, v156, v157\n"
      "v_fmac_f32 v176, v207, v207\n"
      "v_mfma_f32_16x16x32_bf16 v[120:123], a[108:111], v[172:175], v[120:123]\n"
      "s_and_b32 s47, s47, -4\n"
      "v_mul_f32_e64 v146, v81, v146\n"
      "v_cvt_pk_bf16_f32 v201, v124, v125\n"
      "v_mfma_f32_16x16x32_bf16 v[120:123], a[112:115], v[220:223], v[120:123]\n"
      "v_mul_f32_e64 v147, v81, v147\n"
      "v_cvt_pk_bf16_f32 v144, v144, v145\n"
      "v_mfma_f32_16x16x32_bf16 v[120:123], a[116:119], v[140:143], v[120:123]\n"
      "s_and_b32 s4, s45, s8\n"
      "v_fma_f32 v185, v210, v210, s51\n"
      "v_fmac_f32 v185, v211, v211\n"
      "v_mfma_f32_16x16x32_bf16 v[126:129], a[120:123], v[108:111], 0\n"
      "v_cvt_pk_bf16_f32 v148, v148, v149\n"
      "v_fmac_f32 v176, v208, v208\n"
      "v_mfma_f32_16x16x32_bf16 v[126:129], a[124:127], v[168:171], v[126:129]\n"
      "v_fmac_f32 v185, v212, v212\n"
      "v_cvt_pk_bf16_f32 v107, v134, v135\n"
      "v_mfma_f32_16x16x32_bf16 v[126:129], a[128:131], v[100:103], v[126:129]\n"
      "v_mul_f32 v216, v77, v216\n"
      "v_fmac_f32 v185, v213, v213\n"
      "v_mfma_f32_16x16x32_bf16 v[126:129], a[132:135], v[172:175], v[126:129]\n"
      "v_fmac_f32 v185, v120, v120\n"
      "v_fmac_f32 v176, v209, v209\n"
      "v_mfma_f32_16x16x32_bf16 v[126:129], a[136:139], v[220:223], v[126:129]\n"
      "v_fmac_f32 v176, v136, v136\n"
      "v_cvt_pk_bf16_f32 v192, v202, v203\n"
      "v_mfma_f32_16x16x32_bf16 v[126:129], a[140:143], v[140:143], v[126:129]\n"
      "v_fmac_f32 v176, v137, v137\n"
      "v_fmac_f32 v185, v121, v121\n"
      "v_mfma_f32_16x16x32_bf16 v[202:205], v[108:111], a[144:147], 0\n"
      "s_cmp_gt_u32 s42, s44\n"
      "s_cselect_b32 s95, s94, s95\n"
      "v_fmac_f32 v185, v122, v122\n"
      "v_mfma_f32_16x16x32_bf16 v[202:205], v[168:171], a[148:151], v[202:205]\n"
      "v_fmac_f32 v185, v123, v123\n"
      "v_fmac_f32 v185, v126, v126\n"
      "v_mfma_f32_16x16x32_bf16 v[202:205], v[100:103], a[152:155], v[202:205]\n"
      "v_fmac_f32 v176, v138, v138\n"
      "v_fmac_f32 v176, v139, v139\n"
      "v_mfma_f32_16x16x32_bf16 v[202:205], v[172:175], a[156:159], v[202:205]\n"
      "v_cvt_pk_bf16_f32 v215, v216, v217\n"
      "v_cvt_pk_bf16_f32 v98, v178, v179\n"
      "v_mfma_f32_16x16x32_bf16 v[202:205], v[220:223], a[160:163], v[202:205]\n"
      "v_fmac_f32 v185, v127, v127\n"
      "v_fmac_f32 v185, v128, v128\n"
      "v_mfma_f32_16x16x32_bf16 v[202:205], v[140:143], a[164:167], v[202:205]\n"
      "v_fmac_f32 v185, v129, v129\n"
      "v_cvt_pk_bf16_f32 v145, v146, v147\n"
      "v_mfma_f32_16x16x32_bf16 v[216:219], v[108:111], a[168:171], 0\n"
      "v_permlane16_swap_b32 v176, v185\n"
      "v_mfma_f32_16x16x32_bf16 v[216:219], v[168:171], a[172:175], v[216:219]\n"
      "v_add_f32 v176, v176, v185\n"
      "v_cvt_pk_bf16_f32 v114, v186, v187\n"
      "v_mfma_f32_16x16x32_bf16 v[216:219], v[100:103], a[176:179], v[216:219]\n"
      "v_mov_b32 v185, v176\n"
      "v_cvt_pk_bf16_f32 v147, v118, v119\n"
      "v_mfma_f32_16x16x32_bf16 v[216:219], v[172:175], a[180:183], v[216:219]\n"
      "ds_read_b128 v[186:189], v2 offset:3072\n"
      "v_cvt_pk_bf16_f32 v76, v202, v203\n"
      "v_mfma_f32_16x16x32_bf16 v[216:219], v[220:223], a[184:187], v[216:219]\n"
      "v_permlane32_swap_b32 v176, v185\n"
      "v_mfma_f32_16x16x32_bf16 v[216:219], v[140:143], a[188:191], v[216:219]\n"
      "v_mul_f32 v150, v81, v150\n"
      "v_add_f32_e64 v176, v176, v185\n"
      "v_mfma_f32_16x16x32_bf16 v[178:181], v[108:111], a[192:195], 0\n"
      "ds_read_b128 v[108:111], v2 offset:6144\n"
      "v_cvt_pk_bf16_f32 v146, v116, v117\n"
      "v_mfma_f32_16x16x32_bf16 v[178:181], v[168:171], a[196:199], v[178:181]\n"
      "v_rsq_f32 v176, v176\n"
      "v_mfma_f32_16x16x32_bf16 v[178:181], v[100:103], a[200:203], v[178:181]\n"
      "v_cvt_pk_bf16_f32 v77, v204, v205\n"
      "v_mov_b32 v185, v176\n"
      "v_mfma_f32_16x16x32_bf16 v[178:181], v[172:175], a[204:207], v[178:181]\n"
      "s_waitcnt lgkmcnt(1)\n"
      "v_cvt_pk_bf16_f32 v81, v218, v219\n"
      "v_cvt_pk_bf16_f32 v80, v216, v217\n"
      "v_mfma_f32_16x16x32_bf16 v[178:181], v[220:223], a[208:211], v[178:181]\n"
      "s_add_u32 s42, s42, 1\n"
      "v_permlane16_swap_b32 v176, v185\n"
      "v_mfma_f32_16x16x32_bf16 v[178:181], v[140:143], a[212:215], v[178:181]\n"
      "ds_read_b128 v[116:119], v2 offset:15360\n"
      "v_mul_f32 v176, s50, v176\n"
      "v_mfma_f32_16x16x32_bf16 v[172:175], a[216:219], v[130:133], 0\n"
      "ds_read_b128 v[100:103], v2 offset:12288\n"
      "v_mul_f32 v137, v176, v137\n"
      "v_mfma_f32_16x16x32_bf16 v[172:175], a[220:223], v[186:189], v[172:175]\n"
      "s_waitcnt lgkmcnt(2)\n"
      "v_mul_f32 v120, v185, v120\n"
      "v_mul_f32 v207, v176, v207\n"
      "v_mfma_f32_16x16x32_bf16 v[172:175], a[224:227], v[108:111], v[172:175]\n"
      "ds_read_b128 v[216:219], v2 offset:9216\n"
      "v_mul_f32 v212, v185, v212\n"
      "v_mfma_f32_16x16x32_bf16 v[220:223], a[240:243], v[130:133], 0\n"
      "v_mul_f32 v127, v185, v127\n"
      "v_mul_f32 v210, v185, v210\n"
      "v_mfma_f32_16x16x32_bf16 v[220:223], a[244:247], v[186:189], v[220:223]\n"
      "v_mul_f32 v213, v185, v213\n"
      "v_mul_f32 v160, v176, v160\n"
      "v_mfma_f32_16x16x32_bf16 v[220:223], a[248:251], v[108:111], v[220:223]\n"
      "v_mul_f32 v159, v176, v159\n"
      "v_mul_f32_e64 v211, v185, v211\n"
      "v_mfma_f32_16x16x32_bf16 v[140:143], v[232:235], v[130:133], 0\n"
      "ds_read_b128 v[168:171], v10 offset:1200\n"
      "v_cvt_pk_bf16_f32 v210, v210, v211\n"
      "v_mfma_f32_16x16x32_bf16 v[140:143], v[236:239], v[186:189], v[140:143]\n"
      "ds_read_b64 v[156:157], v10 offset:1192\n"
      "v_mul_f32 v121, v185, v121\n"
      "v_mfma_f32_16x16x32_bf16 v[140:143], v[240:243], v[108:111], v[140:143]\n"
      "s_waitcnt lgkmcnt(2)\n"
      "v_cvt_pk_bf16_f32 v84, v178, v179\n"
      "v_mul_f32 v136, v176, v136\n"
      "v_mfma_f32_16x16x32_bf16 v[172:175], a[228:231], v[216:219], v[172:175]\n"
      "ds_read_b128 v[202:205], v2 offset:256\n"
      "v_mul_f32 v122, v185, v122\n"
      "v_mfma_f32_16x16x32_bf16 v[172:175], a[232:235], v[100:103], v[172:175]\n"
      "ds_read_u16 v86, v11 offset:1232\n"
      "v_mul_f32_e64 v123, v185, v123\n"
      "v_mfma_f32_16x16x32_bf16 v[172:175], a[236:239], v[116:119], v[172:175]\n"
      "v_cvt_pk_bf16_f32 v211, v212, v213\n"
      "v_mul_f32 v126, v185, v126\n"
      "v_mfma_f32_16x16x32_bf16 v[220:223], a[252:255], v[216:219], v[220:223]\n"
      "v_cvt_pk_bf16_f32 v212, v120, v121\n"
      "v_mul_f32 v138, v176, v138\n"
      "v_mfma_f32_16x16x32_bf16 v[220:223], v[224:227], v[100:103], v[220:223]\n"
      "ds_read_b128 v[130:133], v2 offset:6400\n"
      "v_mul_f32 v129, v185, v129\n"
      "v_mfma_f32_16x16x32_bf16 v[220:223], v[228:231], v[116:119], v[220:223]\n"
      "v_mul_f32 v128, v185, v128\n"
      "v_cvt_pk_bf16_f32 v136, v136, v137\n"
      "v_mfma_f32_16x16x32_bf16 v[140:143], v[244:247], v[216:219], v[140:143]\n"
      "ds_read_b128 v[184:187], v2 offset:3328\n"
      "v_cvt_pk_bf16_f32 v126, v126, v127\n"
      "v_mfma_f32_16x16x32_bf16 v[140:143], v[248:251], v[100:103], v[140:143]\n"
      "v_fma_f32 v218, v172, v172, s52\n"
      "v_cvt_pk_bf16_f32 v149, v150, v151\n"
      "v_mfma_f32_16x16x32_bf16 v[140:143], v[252:255], v[116:119], v[140:143]\n"
      "ds_read_u16 v82, v11 offset:1200\n"
      "v_mul_f32_e64 v209, v176, v209\n"
      "v_mfma_f32_16x16x32_bf16 v[100:103], v[190:193], v[198:201], v[40:43]\n"
      "ds_read_u16 v78, v11 offset:1168\n"
      "v_cvt_pk_bf16_f32 v213, v122, v123\n"
      "v_mfma_f32_16x16x32_bf16 v[120:123], v[144:147], v[198:201], v[44:47]\n"
      "v_mul_f32 v208, v176, v208\n"
      "v_mul_f32 v219, v173, v173\n"
      "v_mfma_f32_16x16x32_bf16 v[150:153], v[210:213], v[198:201], v[48:51]\n"
      "s_waitcnt lgkmcnt(3)\n"
      "v_mul_f32 v139, v176, v139\n"
      "v_cvt_pk_bf16_f32 v127, v128, v129\n"
      "v_mfma_f32_16x16x32_bf16 v[116:119], v[168:171], v[198:201], v[24:27]\n"
      "v_cvt_pk_bf16_f32 v85, v180, v181\n"
      "v_mul_f32 v161, v176, v161\n"
      "v_mfma_f32_16x16x16_bf16 v[100:103], v[154:155], v[182:183], v[100:103]\n"
      "v_mul_f32 v158, v176, v158\n"
      "v_cvt_pk_bf16_f32 v158, v158, v159\n"
      "v_mul_f32_e64 v206, v176, v206\n"
      "v_mfma_f32_16x16x16_bf16 v[120:123], v[148:149], v[182:183], v[120:123]\n"
      "v_cvt_pk_bf16_f32 v159, v160, v161\n"
      "v_cvt_pk_bf16_f32 v160, v206, v207\n"
      "v_fmac_f32_e64 v219, v175, v175\n"
      "v_mfma_f32_16x16x16_bf16 v[150:153], v[126:127], v[182:183], v[150:153]\n"
      "v_exp_f32 v101, v101\n"
      "s_waitcnt vmcnt(1)\n"
      "v_or3_b32 v164, v164, v165, v166\n"
      "v_mfma_f32_16x16x16_bf16 v[116:119], v[156:157], v[182:183], v[116:119]\n"
      "v_exp_f32 v103, v103\n"
      "v_cvt_pk_bf16_f32 v161, v208, v209\n"
      "v_mfma_f32_16x16x32_bf16 v[206:209], a[216:219], v[202:205], 0\n"
      "s_waitcnt lgkmcnt(0)\n"
      "v_exp_f32_e64 v100, v100\n"
      "v_mfma_f32_16x16x32_bf16 v[206:209], a[220:223], v[184:187], v[206:209]\n"
      "v_exp_f32 v153, v153\n"
      "v_mfma_f32_16x16x32_bf16 v[206:209], a[224:227], v[130:133], v[206:209]\n"
      "v_cvt_pk_bf16_f32 v100, v100, v101\n"
      "v_fmac_f32 v218, v174, v174\n"
      "v_mfma_f32_16x16x32_bf16 v[176:179], a[240:243], v[202:205], 0\n"
      "v_exp_f32 v152, v152\n"
      "v_mfma_f32_16x16x32_bf16 v[176:179], a[244:247], v[184:187], v[176:179]\n"
      "v_exp_f32 v151, v151\n"
      "v_mfma_f32_16x16x32_bf16 v[176:179], a[248:251], v[130:133], v[176:179]\n"
      "v_exp_f32 v150, v150\n"
      "v_mfma_f32_16x16x32_bf16 v[108:111], v[232:235], v[202:205], 0\n"
      "v_fmac_f32 v219, v221, v221\n"
      "v_cvt_pk_bf16_f32 v137, v138, v139\n"
      "v_mfma_f32_16x16x32_bf16 v[108:111], v[236:239], v[184:187], v[108:111]\n"
      "v_exp_f32 v120, v120\n"
      "v_mfma_f32_16x16x32_bf16 v[108:111], v[240:243], v[130:133], v[108:111]\n"
      "v_exp_f32 v102, v102\n"
      "v_mfma_f32_16x16x32_bf16 v[132:135], v[190:193], v[112:115], v[52:55]\n"
      "v_exp_f32_e64 v116, v116\n"
      "v_mfma_f32_16x16x32_bf16 v[182:185], v[144:147], v[112:115], v[56:59]\n"
      "v_exp_f32_e64 v121, v121\n"
      "v_mfma_f32_16x16x32_bf16 v[202:205], v[210:213], v[112:115], v[60:63]\n"
      "v_exp_f32_e64 v122, v122\n"
      "v_mfma_f32_16x16x32_bf16 v[198:201], v[168:171], v[112:115], v[26:29]\n"
      "v_cvt_pk_bf16_f32 v90, v116, 0\n"
      "v_cvt_pk_bf16_f32 v101, v102, v103\n"
      "v_mfma_f32_16x16x16_bf16 v[132:135], v[154:155], v[214:215], v[132:135]\n"
      "s_waitcnt vmcnt(0)\n"
      "v_or3_b32 v164, v164, v167, v88\n"
      "v_exp_f32 v123, v123\n"
      "v_mfma_f32_16x16x16_bf16 v[182:185], v[148:149], v[214:215], v[182:185]\n"
      "v_cvt_pk_bf16_f32 v103, v122, v123\n"
      "v_cvt_pk_bf16_f32 v102, v120, v121\n"
      "v_fmac_f32 v218, v220, v220\n"
      "v_mfma_f32_16x16x16_bf16 v[202:205], v[126:127], v[214:215], v[202:205]\n"
      "v_cvt_pk_bf16_f32 v88, v150, v151\n"
      "v_exp_f32 v132, v132\n"
      "v_mfma_f32_16x16x16_bf16 v[198:201], v[156:157], v[214:215], v[198:201]\n"
      "v_cvt_pk_bf16_f32 v89, v152, v153\n"
      "v_exp_f32 v182, v182\n"
      "v_mfma_f32_16x16x32_bf16 v[120:123], v[32:35], v[100:103], 0\n"
      "v_exp_f32 v135, v135\n"
      "v_mfma_f32_16x16x32_bf16 v[120:123], v[32:35], v[88:91], v[120:123]\n"
      "v_exp_f32 v185, v185\n"
      "v_mfma_f32_16x16x32_bf16 v[150:153], v[194:197], v[100:103], 0\n"
      "v_exp_f32 v183, v183\n"
      "v_mfma_f32_16x16x32_bf16 v[150:153], v[76:79], v[88:91], v[150:153]\n"
      "v_exp_f32_e64 v133, v133\n"
      "v_mfma_f32_16x16x32_bf16 v[214:217], v[104:107], v[100:103], 0\n"
      "ds_read_b128 v[112:115], v2 offset:9472\n"
      "v_cvt_pk_bf16_f32 v132, v132, v133\n"
      "v_mfma_f32_16x16x32_bf16 v[214:217], v[80:83], v[88:91], v[214:217]\n"
      "v_rcp_f32 v120, v120\n"
      "v_mfma_f32_16x16x32_bf16 v[122:125], v[96:99], v[100:103], 0\n"
      "v_exp_f32 v203, v203\n"
      "v_mfma_f32_16x16x32_bf16 v[122:125], v[84:87], v[88:91], v[122:125]\n"
      "s_add_u32 s43, s43, 1\n"
      "v_exp_f32 v134, v134\n"
      "v_mfma_f32_16x16x32_bf16 v[128:131], v[190:193], v[158:161], v[64:67]\n"
      "v_exp_f32_e64 v205, v205\n"
      "v_mfma_f32_16x16x32_bf16 v[186:189], v[144:147], v[158:161], v[68:71]\n"
      "v_cvt_pk_bf16_f32 v133, v134, v135\n"
      "v_cvt_pk_bf16_f32 v134, v182, v183\n"
      "v_mfma_f32_16x16x32_bf16 v[116:119], v[210:213], v[158:161], v[72:75]\n"
      "s_waitcnt lgkmcnt(0)\n"
      "v_exp_f32 v202, v202\n"
      "v_mfma_f32_16x16x32_bf16 v[180:183], v[168:171], v[158:161], v[28:31]\n"
      "ds_read_b128 v[100:103], v2 offset:12544\n"
      "v_cvt_pk_bf16_f32 v92, v202, v203\n"
      "v_mfma_f32_16x16x16_bf16 v[128:131], v[154:155], v[136:137], v[128:131]\n"
      "v_fmac_f32 v218, v222, v222\n"
      "v_exp_f32 v184, v184\n"
      "v_mfma_f32_16x16x16_bf16 v[186:189], v[148:149], v[136:137], v[186:189]\n"
      "v_cvt_pk_bf16_f32 v135, v184, v185\n"
      "v_exp_f32_e64 v204, v204\n"
      "v_mfma_f32_16x16x16_bf16 v[116:119], v[126:127], v[136:137], v[116:119]\n"
      "ds_read_b128 v[200:203], v2 offset:15616\n"
      "v_fmac_f32_e64 v219, v223, v223\n"
      "v_mfma_f32_16x16x16_bf16 v[180:183], v[156:157], v[136:137], v[180:183]\n"
      "v_cvt_pk_bf16_f32 v93, v204, v205\n"
      "v_exp_f32 v128, v128\n"
      "v_exp_f32 v186, v186\n"
      "v_exp_f32_e64 v129, v129\n"
      "v_pk_mul_f32 v[150:151], v[150:151], v[120:121] op_sel_hi:[1,0]\n"
      "v_pk_mul_f32 v[152:153], v[152:153], v[120:121] op_sel_hi:[1,0]\n"
      "v_pk_mul_f32 v[124:125], v[124:125], v[120:121] op_sel_hi:[1,0]\n"
      "v_pk_mul_f32 v[216:217], v[216:217], v[120:121] op_sel_hi:[1,0]\n"
      "v_pk_mul_f32 v[214:215], v[214:215], v[120:121] op_sel_hi:[1,0]\n"
      "v_cvt_pk_bf16_f32 v150, v150, v151\n"
      "v_pk_mul_f32 v[122:123], v[122:123], v[120:121] op_sel_hi:[1,0]\n"
      "v_pk_fma_f32 v[218:219], v[140:141], v[140:141], v[218:219]\n"
      "v_mfma_f32_16x16x32_bf16 v[206:209], a[228:231], v[112:115], v[206:209]\n"
      "s_waitcnt lgkmcnt(1)\n"
      "v_exp_f32 v198, v198\n"
      "v_mfma_f32_16x16x32_bf16 v[206:209], a[232:235], v[100:103], v[206:209]\n"
      "s_waitcnt lgkmcnt(0)\n"
      "v_exp_f32 v131, v131\n"
      "v_mfma_f32_16x16x32_bf16 v[206:209], a[236:239], v[200:203], v[206:209]\n"
      "v_cvt_pk_bf16_f32 v122, v122, v123\n"
      "v_cvt_pk_bf16_f32 v151, v152, v153\n"
      "v_mfma_f32_16x16x32_bf16 v[176:179], a[252:255], v[112:115], v[176:179]\n"
      "v_cvt_pk_bf16_f32 v94, v198, 0\n"
      "v_cvt_pk_bf16_f32 v153, v216, v217\n"
      "v_mfma_f32_16x16x32_bf16 v[176:179], v[224:227], v[100:103], v[176:179]\n"
      "ds_read_b128 v[136:139], v7 offset:4096\n"
      "v_cvt_pk_bf16_f32 v152, v214, v215\n"
      "v_mfma_f32_16x16x32_bf16 v[176:179], v[228:231], v[200:203], v[176:179]\n"
      "v_exp_f32 v187, v187\n"
      "v_mfma_f32_16x16x32_bf16 v[108:111], v[244:247], v[112:115], v[108:111]\n"
      "v_exp_f32 v118, v118\n"
      "v_mfma_f32_16x16x32_bf16 v[108:111], v[248:251], v[100:103], v[108:111]\n"
      "ds_read_b64 v[120:121], v10 offset:1184\n"
      "v_mul_f32_e64 v159, v207, v207\n"
      "v_mfma_f32_16x16x32_bf16 v[108:111], v[252:255], v[200:203], v[108:111]\n"
      "ds_read_b128 v[182:185], v7\n"
      "v_cvt_pk_bf16_f32 v123, v124, v125\n"
      "v_mfma_f32_16x16x32_bf16 v[100:103], v[32:35], v[132:135], 0\n"
      "v_exp_f32_e64 v119, v119\n"
      "v_mfma_f32_16x16x32_bf16 v[100:103], v[32:35], v[92:95], v[100:103]\n"
      "ds_read_b128 v[214:217], v10 offset:1168\n"
      "v_cvt_pk_bf16_f32 v128, v128, v129\n"
      "v_mfma_f32_16x16x32_bf16 v[202:205], v[194:197], v[132:135], 0\n"
      "v_exp_f32_e64 v117, v117\n"
      "v_mfma_f32_16x16x32_bf16 v[202:205], v[76:79], v[92:95], v[202:205]\n"
      "ds_write_b128 v3, v[150:153] offset:18432\n"
      "v_mfma_f32_16x16x32_bf16 v[112:115], v[104:107], v[132:135], 0\n"
      "s_nop 0\n"
      "v_rcp_f32 v100, v100\n"
      "v_mfma_f32_16x16x32_bf16 v[112:115], v[80:83], v[92:95], v[112:115]\n"
      "v_cvt_pk_bf16_f32 v89, v118, v119\n"
      "v_fma_f32 v158, v206, v206, s52\n"
      "v_mfma_f32_16x16x32_bf16 v[198:201], v[96:99], v[132:135], 0\n"
      "s_waitcnt lgkmcnt(1)\n"
      "v_exp_f32 v130, v130\n"
      "v_mfma_f32_16x16x32_bf16 v[198:201], v[84:87], v[92:95], v[198:201]\n"
      "buffer_store_dwordx4 v[182:185], v20, s[24:27], s85 offen sc1\n"
      "v_pk_mul_f32 v[202:203], v[202:203], v[100:101] op_sel_hi:[1,0]\n"
      "v_pk_mul_f32 v[204:205], v[204:205], v[100:101] op_sel_hi:[1,0]\n"
      "v_pk_mul_f32 v[114:115], v[114:115], v[100:101] op_sel_hi:[1,0]\n"
      "v_mfma_f32_16x16x32_bf16 v[182:185], v[214:217], v[190:193], v[30:33]\n"
      "ds_read_b128 v[160:163], v7 offset:8192\n"
      "v_cvt_pk_bf16_f32 v129, v130, v131\n"
      "v_mfma_f32_16x16x32_bf16 v[150:153], v[214:217], v[144:147], v[30:33]\n"
      "ds_read_b128 v[190:193], v2 offset:512\n"
      "v_cvt_pk_bf16_f32 v202, v202, v203\n"
      "v_mfma_f32_16x16x32_bf16 v[144:147], v[214:217], v[210:213], v[30:33]\n"
      "ds_write_b64 v4, v[122:123] offset:18432\n"
      "v_mfma_f32_16x16x32_bf16 v[122:125], v[214:217], v[168:171], v[30:33]\n"
      "ds_read_b128 v[214:217], v2 offset:3584\n"
      "v_cvt_pk_bf16_f32 v203, v204, v205\n"
      "v_mfma_f32_16x16x16_bf16 v[182:185], v[120:121], v[154:155], v[182:185]\n"
      "ds_read_b128 v[210:213], v2 offset:6656\n"
      "v_exp_f32_e64 v180, v180\n"
      "v_mfma_f32_16x16x16_bf16 v[150:153], v[120:121], v[148:149], v[150:153]\n"
      "v_exp_f32_e64 v189, v189\n"
      "v_cvt_pk_bf16_f32 v205, v114, v115\n"
      "v_mfma_f32_16x16x16_bf16 v[144:147], v[120:121], v[126:127], v[144:147]\n"
      "buffer_store_dwordx4 v[136:139], v21, s[24:27], s85 offen sc1\n"
      "v_pk_fma_f32 v[158:159], v[208:209], v[208:209], v[158:159]\n"
      "v_pk_fma_f32 v[218:219], v[142:143], v[142:143], v[218:219]\n"
      "v_pk_fma_f32 v[158:159], v[176:177], v[176:177], v[158:159]\n"
      "v_pk_mul_f32 v[112:113], v[112:113], v[100:101] op_sel_hi:[1,0]\n"
      "v_pk_fma_f32 v[158:159], v[178:179], v[178:179], v[158:159]\n"
      "v_pk_mul_f32 v[200:201], v[200:201], v[100:101] op_sel_hi:[1,0]\n"
      "v_mfma_f32_16x16x16_bf16 v[122:125], v[120:121], v[156:157], v[122:125]\n"
      "v_cvt_pk_bf16_f32 v90, v180, 0\n"
      "v_cvt_pk_bf16_f32 v204, v112, v113\n"
      "s_waitcnt lgkmcnt(1)\n"
      "v_mfma_f32_16x16x32_bf16 v[132:135], a[216:219], v[190:193], 0\n"
      "ds_write_b128 v3, v[202:205] offset:18688\n"
      "v_pk_fma_f32 v[158:159], v[108:109], v[108:109], v[158:159]\n"
      "v_pk_mul_f32 v[198:199], v[198:199], v[100:101] op_sel_hi:[1,0]\n"
      "v_mfma_f32_16x16x32_bf16 v[132:135], a[220:223], v[214:217], v[132:135]\n"
      "s_waitcnt lgkmcnt(1)\n"
      "v_exp_f32 v188, v188\n"
      "v_mfma_f32_16x16x32_bf16 v[132:135], a[224:227], v[210:213], v[132:135]\n"
      "v_exp_f32 v116, v116\n"
      "v_mfma_f32_16x16x32_bf16 v[118:121], a[240:243], v[190:193], 0\n"
      "buffer_store_dwordx4 v[160:163], v22, s[24:27], s85 offen sc1\n"
      "v_pk_fma_f32 v[158:159], v[110:111], v[110:111], v[158:159]\n"
      "v_mfma_f32_16x16x32_bf16 v[118:121], a[244:247], v[214:217], v[118:121]\n"
      "ds_read_b128 v[202:205], v2 offset:12800\n"
      "v_cvt_pk_bf16_f32 v131, v188, v189\n"
      "v_mfma_f32_16x16x32_bf16 v[118:121], a[248:251], v[210:213], v[118:121]\n"
      "ds_read_b128 v[136:139], v7 offset:16384\n"
      "v_cvt_pk_bf16_f32 v130, v186, v187\n"
      "v_mfma_f32_16x16x32_bf16 v[168:171], v[232:235], v[190:193], 0\n"
      "v_cvt_pk_bf16_f32 v88, v116, v117\n"
      "v_cvt_pk_bf16_f32 v198, v198, v199\n"
      "v_mfma_f32_16x16x32_bf16 v[168:171], v[236:239], v[214:217], v[168:171]\n"
      "ds_read_b128 v[100:103], v7 offset:12288\n"
      "v_cndmask_b32 v182, v182, v150, s[58:59]\n"
      "v_mfma_f32_16x16x32_bf16 v[168:171], v[240:243], v[210:213], v[168:171]\n"
      "ds_read_b128 v[150:153], v2 offset:9728\n"
      "v_add_f32_e64 v218, v218, v219\n"
      "v_mfma_f32_16x16x32_bf16 v[212:215], v[32:35], v[128:131], 0\n"
      "v_cndmask_b32 v182, v182, v144, s[60:61]\n"
      "v_cndmask_b32 v182, v182, v122, s[62:63]\n"
      "v_mfma_f32_16x16x32_bf16 v[212:215], v[32:35], v[88:91], v[212:215]\n"
      "v_exp_f32_e64 v182, v182\n"
      "v_mfma_f32_16x16x32_bf16 v[184:187], v[194:197], v[128:131], 0\n"
      "ds_read_b128 v[114:117], v2 offset:15872\n"
      "v_cvt_pk_bf16_f32 v182, v182, v182\n"
      "v_mfma_f32_16x16x32_bf16 v[184:187], v[76:79], v[88:91], v[184:187]\n"
      "ds_write_b16 v13, v182\n"
      "v_mfma_f32_16x16x32_bf16 v[160:163], v[104:107], v[128:131], 0\n"
      "ds_read_b128 v[144:147], v14\n"
      "v_cvt_pk_bf16_f32 v199, v200, v201\n"
      "v_mfma_f32_16x16x32_bf16 v[160:163], v[80:83], v[88:91], v[160:163]\n"
      "v_rcp_f32_e64 v212, v212\n"
      "v_mfma_f32_16x16x32_bf16 v[214:217], v[96:99], v[128:131], 0\n"
      "ds_write_b64 v4, v[198:199] offset:18688\n"
      "v_mfma_f32_16x16x32_bf16 v[214:217], v[84:87], v[88:91], v[214:217]\n"
      "s_waitcnt lgkmcnt(4)\n"
      "v_mul_f32 v187, v212, v187\n"
      "v_mul_f32_e64 v185, v212, v185\n"
      "v_mfma_f32_16x16x32_bf16 v[132:135], a[228:231], v[150:153], v[132:135]\n"
      "ds_read_b128 v[198:201], v14 offset:64\n"
      "v_mul_f32 v186, v212, v186\n"
      "v_mfma_f32_16x16x32_bf16 v[132:135], a[232:235], v[202:205], v[132:135]\n"
      "s_waitcnt lgkmcnt(4)\n"
      "v_add_f32 v158, v158, v159\n"
      "v_mul_f32 v184, v212, v184\n"
      "v_mfma_f32_16x16x32_bf16 v[132:135], a[236:239], v[114:117], v[132:135]\n"
      "v_mul_f32 v162, v212, v162\n"
      "v_mul_f32 v163, v212, v163\n"
      "v_mfma_f32_16x16x32_bf16 v[118:121], a[252:255], v[150:153], v[118:121]\n"
      "v_cvt_pk_bf16_f32 v184, v184, v185\n"
      "v_cvt_pk_bf16_f32 v185, v186, v187\n"
      "v_mfma_f32_16x16x32_bf16 v[118:121], v[224:227], v[202:205], v[118:121]\n"
      "ds_write_b32 v31, v164 offset:5632\n"
      "v_mfma_f32_16x16x32_bf16 v[118:121], v[228:231], v[114:117], v[118:121]\n"
      "v_fma_f32 v190, v132, v132, s52\n"
      "v_cvt_pk_bf16_f32 v187, v162, v163\n"
      "v_mfma_f32_16x16x32_bf16 v[168:171], v[244:247], v[150:153], v[168:171]\n"
      "buffer_store_dwordx4 v[100:103], v23, s[24:27], s85 offen sc1\n"
      "v_pk_mul_f32 v[160:161], v[160:161], v[212:213] op_sel_hi:[1,0]\n"
      "v_mfma_f32_16x16x32_bf16 v[168:171], v[248:251], v[202:205], v[168:171]\n"
      "s_waitcnt lgkmcnt(1)\n"
      "v_mul_f32 v191, v133, v133\n"
      "v_cvt_pk_bf16_f32 v186, v160, v161\n"
      "v_mfma_f32_16x16x32_bf16 v[168:171], v[252:255], v[114:117], v[168:171]\n"
      "ds_write_b128 v3, v[184:187] offset:18944\n"
      "v_pk_mul_f32 v[214:215], v[214:215], v[212:213] op_sel_hi:[1,0]\n"
      "v_pk_fma_f32 v[190:191], v[134:135], v[134:135], v[190:191]\n"
      "v_pk_mul_f32 v[216:217], v[216:217], v[212:213] op_sel_hi:[1,0]\n"
      "v_pk_fma_f32 v[190:191], v[118:119], v[118:119], v[190:191]\n"
      "v_pk_fma_f32 v[190:191], v[120:121], v[120:121], v[190:191]\n"
      "v_permlane16_swap_b32 v218, v158\n"
      "v_cvt_pk_bf16_f32 v214, v214, v215\n"
      "v_pk_fma_f32 v[190:191], v[168:169], v[168:169], v[190:191]\n"
      "v_add_f32 v218, v218, v158\n"
      "v_pk_fma_f32 v[190:191], v[170:171], v[170:171], v[190:191]\n"
      "v_add_f32 v190, v190, v191\n"
      "v_mov_b32 v158, v190\n"
      "v_cvt_pk_bf16_f32 v215, v216, v217\n"
      "v_mfma_f32_16x16x32_bf16 v[162:165], v[144:147], v[32:35], 0\n"
      "ds_write_b64 v4, v[214:215] offset:18944\n"
      "v_permlane16_swap_b32 v190, v158\n"
      "v_add_f32_e64 v190, v190, v158\n"
      "v_add_u32 v10, 2336, v10\n"
      "s_nop 0\n"
      "v_permlane32_swap_b32_e64 v218, v190\n"
      "v_add_f32 v218, v218, v190\n"
      "v_mfma_f32_16x16x32_bf16 v[162:165], v[198:201], v[32:35], v[162:165]\n"
      "ds_write_b32 v31, v218 offset:1024\n"
      "v_add_u32 v11, 2336, v11\n"
      "s_cmp_ge_u32 s95, 0x40000000\n"
      "s_cselect_b64 vcc, -1, s[56:57]\n"
      "buffer_store_dwordx4 v[136:139], v25, s[24:27], s85 offen sc1\n"
      "s_waitcnt lgkmcnt(0)\n"
      "s_barrier\n"
      "s_cbranch_vccnz L_disp0_%=\n"
      "L_loop0_%=:\n"
      "v_mfma_f32_16x16x32_bf16 v[152:155], v[144:147], v[194:197], 0\n"
      "ds_read_b128 v[190:193], v0\n"
      "ds_read_b128 v[202:205], v9 offset:1024\n"
      "v_mfma_f32_16x16x32_bf16 v[152:155], v[198:201], v[76:79], v[152:155]\n"
      "ds_read_b128 v[100:103], v0 offset:12288\n"
      "v_mfma_f32_16x16x32_bf16 v[210:213], v[144:147], v[104:107], 0\n"
      "s_add_u32 m0, s64, 18432\n"
      "ds_read_b128 v[216:219], v0 offset:6144\n"
      "v_mfma_f32_16x16x32_bf16 v[210:213], v[198:201], v[80:83], v[210:213]\n"
      "buffer_load_dwordx4 v16, s[16:19], s45 offen  lds\n"
      "v_mfma_f32_16x16x32_bf16 v[180:183], v[144:147], v[96:99], 0\n"
      "ds_read_b128 v[128:131], v1\n"
      "s_add_u32 m0, s64, 22528\n"
      "v_mfma_f32_16x16x32_bf16 v[180:183], v[198:201], v[84:87], v[180:183]\n"
      "ds_read_b128 v[114:117], v1 offset:12288\n"
      "s_waitcnt lgkmcnt(5)\n"
      "v_mfma_f32_16x16x32_bf16 v[148:151], a[0:3], v[190:193], 0\n"
      "ds_read_b128 v[212:215], v1 offset:6144\n"
      "v_readlane_b32 s85, v37, s42\n"
      "v_mfma_f32_16x16x32_bf16 v[124:127], a[24:27], v[190:193], 0\n"
      "s_waitcnt lgkmcnt(5)\n"
      "v_add_f32_e64 v204, v204, v205\n"
      "v_cndmask_b32 v152, v152, v210, s[58:59]\n"
      "v_mfma_f32_16x16x32_bf16 v[186:189], a[48:51], v[190:193], 0\n"
      "buffer_load_dwordx4 v17, s[16:19], s45 offen  lds\n"
      "v_mfma_f32_16x16x32_bf16 v[198:201], a[72:75], v[190:193], 0\n"
      "v_add_f32 v202, v202, v203\n"
      "v_add_f32 v202, v202, v204\n"
      "v_mfma_f32_16x16x32_bf16 v[182:185], a[96:99], v[190:193], 0\n"
      "v_rsq_f32 v202, v202\n"
      "v_mfma_f32_16x16x32_bf16 v[144:147], a[120:123], v[190:193], 0\n"
      "s_waitcnt lgkmcnt(1)\n"
      "v_mul_f32 v202, 0x415db3d7, v202\n"
      "v_mov_b32 v204, v202\n"
      "v_mfma_f32_16x16x32_bf16 v[148:151], a[4:7], v[128:131], v[148:151]\n"
      "s_add_u32 m0, s64, 26624\n"
      "v_permlane32_swap_b32 v202, v204\n"
      "v_mfma_f32_16x16x32_bf16 v[148:151], a[8:11], v[216:219], v[148:151]\n"
      "buffer_load_dwordx4 v18, s[16:19], s45 offen  lds\n"
      "v_mfma_f32_16x16x32_bf16 v[124:127], a[28:31], v[128:131], v[124:127]\n"
      "v_mul_f32 v169, v204, v169\n"
      "v_mov_b32 v203, v202\n"
      "v_mfma_f32_16x16x32_bf16 v[124:127], a[32:35], v[216:219], v[124:127]\n"
      "s_add_u32 m0, s64, 30720\n"
      "v_mul_f32 v132, v204, v132\n"
      "v_mul_f32 v133, v204, v133\n"
      "v_mfma_f32_16x16x32_bf16 v[186:189], a[52:55], v[128:131], v[186:189]\n"
      "v_permlane16_swap_b32 v202, v203\n"
      "v_mfma_f32_16x16x32_bf16 v[186:189], a[56:59], v[216:219], v[186:189]\n"
      "ds_read_b128 v[136:139], v1 offset:14336\n"
      "v_mul_f32 v143, v202, v143\n"
      "v_mfma_f32_16x16x32_bf16 v[198:201], a[76:79], v[128:131], v[198:201]\n"
      "v_mul_f32 v172, v202, v172\n"
      "v_mul_f32 v141, v202, v141\n"
      "v_mfma_f32_16x16x32_bf16 v[198:201], a[80:83], v[216:219], v[198:201]\n"
      "buffer_load_dwordx4 v19, s[16:19], s45 offen  lds\n"
      "v_mfma_f32_16x16x32_bf16 v[182:185], a[100:103], v[128:131], v[182:185]\n"
      "v_mul_f32 v223, v202, v223\n"
      "v_mul_f32 v220, v202, v220\n"
      "v_mfma_f32_16x16x32_bf16 v[182:185], a[104:107], v[216:219], v[182:185]\n"
      "v_mul_f32 v168, v204, v168\n"
      "v_mul_f32 v206, v203, v206\n"
      "v_mfma_f32_16x16x32_bf16 v[144:147], a[124:127], v[128:131], v[144:147]\n"
      "s_mov_b32 m0, s7\n"
      "v_mul_f32 v221, v202, v221\n"
      "v_mul_f32 v173, v202, v173\n"
      "v_mfma_f32_16x16x32_bf16 v[144:147], a[128:131], v[216:219], v[144:147]\n"
      "s_waitcnt lgkmcnt(0)\n"
      "v_readlane_b32 s84, v37, s43\n"
      "v_mul_f32 v176, v203, v176\n"
      "v_mfma_f32_16x16x32_bf16 v[148:151], a[12:15], v[212:215], v[148:151]\n"
      "buffer_load_dwordx4 v15, s[0:3], s4 offen  lds\n"
      "v_mfma_f32_16x16x32_bf16 v[148:151], a[16:19], v[100:103], v[148:151]\n"
      "v_mul_f32 v111, v203, v111\n"
      "v_mul_f32 v174, v202, v174\n"
      "v_mfma_f32_16x16x32_bf16 v[148:151], a[20:23], v[114:117], v[148:151]\n"
      "v_mul_f32 v209, v203, v209\n"
      "v_mul_f32 v121, v204, v121\n"
      "v_mfma_f32_16x16x32_bf16 v[124:127], a[36:39], v[212:215], v[124:127]\n"
      "v_mul_f32 v207, v203, v207\n"
      "v_mov_b32_dpp v162, v162 quad_perm:[0,0,0,0] row_mask:0xf bank_mask:0xf\n"
      "v_mfma_f32_16x16x32_bf16 v[124:127], a[40:43], v[100:103], v[124:127]\n"
      "v_mul_f32 v222, v202, v222\n"
      "v_mul_f32 v175, v202, v175\n"
      "v_mfma_f32_16x16x32_bf16 v[124:127], a[44:47], v[114:117], v[124:127]\n"
      "buffer_load_dwordx4 v[156:159], v36, s[28:31], s47 offen\n"
      "v_mfma_f32_16x16x32_bf16 v[186:189], a[60:63], v[212:215], v[186:189]\n"
      "v_cndmask_b32 v152, v152, v180, s[60:61]\n"
      "v_mul_f32 v177, v203, v177\n"
      "v_mfma_f32_16x16x32_bf16 v[186:189], a[64:67], v[100:103], v[186:189]\n"
      "v_cvt_pk_bf16_f32 v172, v172, v173\n"
      "v_cvt_pk_bf16_f32 v173, v174, v175\n"
      "v_mfma_f32_16x16x32_bf16 v[186:189], a[68:71], v[114:117], v[186:189]\n"
      "v_mul_f32 v109, v203, v109\n"
      "v_mul_f32 v140, v202, v140\n"
      "v_mfma_f32_16x16x32_bf16 v[198:201], a[84:87], v[212:215], v[198:201]\n"
      "v_rcp_f32 v162, v162\n"
      "v_mfma_f32_16x16x32_bf16 v[198:201], a[88:91], v[100:103], v[198:201]\n"
      "ds_read_b128 v[164:167], v1 offset:2048\n"
      "v_cvt_pk_bf16_f32 v206, v206, v207\n"
      "v_mfma_f32_16x16x32_bf16 v[198:201], a[92:95], v[114:117], v[198:201]\n"
      "v_mul_f32 v120, v204, v120\n"
      "v_mul_f32 v152, v162, v152\n"
      "v_mfma_f32_16x16x32_bf16 v[182:185], a[108:111], v[212:215], v[182:185]\n"
      "buffer_load_dword v90, v29, s[28:31], s47 offen\n"
      "v_mfma_f32_16x16x32_bf16 v[182:185], a[112:115], v[100:103], v[182:185]\n"
      "v_mul_f32_e64 v171, v204, v171\n"
      "v_fma_f32 v122, v148, v148, s51\n"
      "v_mfma_f32_16x16x32_bf16 v[182:185], a[116:119], v[114:117], v[182:185]\n"
      "v_fma_f32 v81, v198, v198, s51\n"
      "v_mul_f32_e64 v108, v203, v108\n"
      "v_mfma_f32_16x16x32_bf16 v[144:147], a[132:135], v[212:215], v[144:147]\n"
      "ds_read_b128 v[160:163], v0 offset:14336\n"
      "v_cvt_pk_bf16_f32 v174, v220, v221\n"
      "v_mfma_f32_16x16x32_bf16 v[144:147], a[136:139], v[100:103], v[144:147]\n"
      "v_mul_f32 v179, v203, v179\n"
      "v_mul_f32 v178, v203, v178\n"
      "v_mfma_f32_16x16x32_bf16 v[144:147], a[140:143], v[114:117], v[144:147]\n"
      "v_fmac_f32 v122, v149, v149\n"
      "v_cvt_pk_bf16_f32 v175, v222, v223\n"
      "v_mfma_f32_16x16x32_bf16 v[194:197], v[190:193], a[144:147], 0\n"
      "v_fmac_f32 v122, v150, v150\n"
      "v_mfma_f32_16x16x32_bf16 v[194:197], v[128:131], a[148:151], v[194:197]\n"
      "ds_write_b128 v5, v[172:175]\n"
      "v_mfma_f32_16x16x32_bf16 v[194:197], v[216:219], a[152:155], v[194:197]\n"
      "v_fmac_f32 v122, v151, v151\n"
      "v_fmac_f32 v122, v124, v124\n"
      "v_mfma_f32_16x16x32_bf16 v[194:197], v[212:215], a[156:159], v[194:197]\n"
      "v_mul_f32 v208, v203, v208\n"
      "v_cvt_pk_bf16_f32 v168, v168, v169\n"
      "v_mfma_f32_16x16x32_bf16 v[194:197], v[100:103], a[160:163], v[194:197]\n"
      "v_mul_f32 v134, v204, v134\n"
      "v_cvt_pk_bf16_f32 v207, v208, v209\n"
      "v_mfma_f32_16x16x32_bf16 v[194:197], v[114:117], a[164:167], v[194:197]\n"
      "ds_read_b128 v[172:175], v0 offset:2048\n"
      "v_cvt_pk_bf16_f32 v209, v178, v179\n"
      "v_mfma_f32_16x16x32_bf16 v[104:107], v[190:193], a[168:171], 0\n"
      "v_cvt_pk_bf16_f32 v152, v152, v152\n"
      "v_cvt_pk_bf16_f32 v208, v176, v177\n"
      "v_mfma_f32_16x16x32_bf16 v[104:107], v[128:131], a[172:175], v[104:107]\n"
      "ds_read_b128 v[220:223], v1 offset:8192\n"
      "v_cvt_pk_bf16_f32 v108, v108, v109\n"
      "v_mfma_f32_16x16x32_bf16 v[104:107], v[216:219], a[176:179], v[104:107]\n"
      "v_fmac_f32 v81, v199, v199\n"
      "v_fmac_f32 v122, v125, v125\n"
      "v_mfma_f32_16x16x32_bf16 v[104:107], v[212:215], a[180:183], v[104:107]\n"
      "v_mul_f32 v119, v204, v119\n"
      "v_fmac_f32 v122, v126, v126\n"
      "v_mfma_f32_16x16x32_bf16 v[104:107], v[100:103], a[184:187], v[104:107]\n"
      "v_mul_f32 v110, v203, v110\n"
      "v_fmac_f32 v81, v200, v200\n"
      "v_mfma_f32_16x16x32_bf16 v[104:107], v[114:117], a[188:191], v[104:107]\n"
      "ds_write_b16 v12, v152 offset:16\n"
      "v_mfma_f32_16x16x32_bf16 v[96:99], v[190:193], a[192:195], 0\n"
      "v_fmac_f32 v122, v127, v127\n"
      "v_fmac_f32 v81, v201, v201\n"
      "v_mfma_f32_16x16x32_bf16 v[96:99], v[128:131], a[196:199], v[96:99]\n"
      "ds_read_b128 v[152:155], v0 offset:8192\n"
      "v_mul_f32_e64 v170, v204, v170\n"
      "v_mfma_f32_16x16x32_bf16 v[96:99], v[216:219], a[200:203], v[96:99]\n"
      "v_cvt_pk_bf16_f32 v194, v194, v195\n"
      "v_cvt_pk_bf16_f32 v109, v110, v111\n"
      "v_mfma_f32_16x16x32_bf16 v[96:99], v[212:215], a[204:207], v[96:99]\n"
      "ds_write_b128 v5, v[206:209] offset:6144\n"
      "v_mfma_f32_16x16x32_bf16 v[96:99], v[100:103], a[208:211], v[96:99]\n"
      "s_waitcnt lgkmcnt(3)\n"
      "v_fmac_f32 v81, v182, v182\n"
      "v_mul_f32 v142, v202, v142\n"
      "v_mfma_f32_16x16x32_bf16 v[96:99], v[114:117], a[212:215], v[96:99]\n"
      "s_or_b32 s95, s84, s85\n"
      "v_fmac_f32 v122, v186, v186\n"
      "v_fmac_f32 v122, v187, v187\n"
      "v_mfma_f32_16x16x32_bf16 v[190:193], a[0:3], v[172:175], 0\n"
      "s_waitcnt lgkmcnt(1)\n"
      "v_fmac_f32 v81, v183, v183\n"
      "v_cvt_pk_bf16_f32 v140, v140, v141\n"
      "v_mfma_f32_16x16x32_bf16 v[190:193], a[4:7], v[164:167], v[190:193]\n"
      "v_cvt_pk_bf16_f32 v96, v96, v97\n"
      "v_fmac_f32 v81, v184, v184\n"
      "v_mfma_f32_16x16x32_bf16 v[190:193], a[8:11], v[152:155], v[190:193]\n"
      "v_fmac_f32 v122, v188, v188\n"
      "v_fmac_f32 v81, v185, v185\n"
      "v_mfma_f32_16x16x32_bf16 v[190:193], a[12:15], v[220:223], v[190:193]\n"
      "v_mul_f32 v135, v204, v135\n"
      "v_fmac_f32_e64 v81, v144, v144\n"
      "v_mfma_f32_16x16x32_bf16 v[190:193], a[16:19], v[160:163], v[190:193]\n"
      "ds_read_b128 v[100:103], v0 offset:10240\n"
      "v_fmac_f32 v81, v145, v145\n"
      "v_mfma_f32_16x16x32_bf16 v[190:193], a[20:23], v[136:139], v[190:193]\n"
      "ds_read_b128 v[210:213], v9 offset:5632\n"
      "v_fmac_f32 v81, v146, v146\n"
      "v_mfma_f32_16x16x32_bf16 v[176:179], a[24:27], v[172:175], 0\n"
      "ds_read_b128 v[206:209], v1 offset:4096\n"
      "v_cvt_pk_bf16_f32 v132, v132, v133\n"
      "v_mfma_f32_16x16x32_bf16 v[176:179], a[28:31], v[164:167], v[176:179]\n"
      "v_readlane_b32 s86, v38, s43\n"
      "v_cvt_pk_bf16_f32 v141, v142, v143\n"
      "v_mfma_f32_16x16x32_bf16 v[176:179], a[32:35], v[152:155], v[176:179]\n"
      "v_fmac_f32_e64 v122, v189, v189\n"
      "v_cvt_pk_bf16_f32 v169, v170, v171\n"
      "v_mfma_f32_16x16x32_bf16 v[176:179], a[36:39], v[220:223], v[176:179]\n"
      "v_mul_f32 v118, v204, v118\n"
      "v_fmac_f32 v81, v147, v147\n"
      "v_mfma_f32_16x16x32_bf16 v[176:179], a[40:43], v[160:163], v[176:179]\n"
      "v_fma_f32 v84, v190, v190, s51\n"
      "v_fmac_f32_e64 v84, v191, v191\n"
      "v_mfma_f32_16x16x32_bf16 v[176:179], a[44:47], v[136:139], v[176:179]\n"
      "ds_write_b64 v6, v[140:141]\n"
      "v_mfma_f32_16x16x32_bf16 v[202:205], a[48:51], v[172:175], 0\n"
      "s_waitcnt lgkmcnt(1)\n"
      "v_permlane16_swap_b32 v122, v81\n"
      "v_mfma_f32_16x16x32_bf16 v[202:205], a[52:55], v[164:167], v[202:205]\n"
      "v_or3_b32 v210, v210, v211, v212\n"
      "v_add_f32_e64 v122, v122, v81\n"
      "v_mfma_f32_16x16x32_bf16 v[202:205], a[56:59], v[152:155], v[202:205]\n"
      "ds_read_b128 v[216:219], v0 offset:4096\n"
      "v_bitop3_b32 v210, v210, v213, s33 bitop3:0xa8\n"
      "v_mfma_f32_16x16x32_bf16 v[202:205], a[60:63], v[220:223], v[202:205]\n"
      "v_fmac_f32 v84, v192, v192\n"
      "v_mov_b32 v81, v122\n"
      "v_mfma_f32_16x16x32_bf16 v[202:205], a[64:67], v[160:163], v[202:205]\n"
      "s_nop 0\n"
      "v_permlane32_swap_b32 v122, v81\n"
      "v_mfma_f32_16x16x32_bf16 v[202:205], a[68:71], v[136:139], v[202:205]\n"
      "v_add_f32_e64 v122, v122, v81\n"
      "v_cvt_pk_bf16_f32 v195, v196, v197\n"
      "v_mfma_f32_16x16x32_bf16 v[128:131], a[72:75], v[172:175], 0\n"
      "v_cvt_pk_bf16_f32 v133, v134, v135\n"
      "v_fmac_f32_e64 v84, v193, v193\n"
      "v_mfma_f32_16x16x32_bf16 v[128:131], a[76:79], v[164:167], v[128:131]\n"
      "ds_write_b64 v6, v[168:169] offset:12288\n"
      "v_mfma_f32_16x16x32_bf16 v[128:131], a[80:83], v[152:155], v[128:131]\n"
      "v_rsq_f32 v122, v122\n"
      "v_mfma_f32_16x16x32_bf16 v[128:131], a[84:87], v[220:223], v[128:131]\n"
      "v_cvt_pk_bf16_f32 v135, v120, v121\n"
      "v_mov_b32 v81, v122\n"
      "v_mfma_f32_16x16x32_bf16 v[128:131], a[88:91], v[160:163], v[128:131]\n"
      "ds_read_b128 v[212:215], v0 offset:16384\n"
      "v_fmac_f32 v84, v176, v176\n"
      "v_mfma_f32_16x16x32_bf16 v[128:131], a[92:95], v[136:139], v[128:131]\n"
      "v_permlane16_swap_b32 v122, v81\n"
      "v_mfma_f32_16x16x32_bf16 v[168:171], a[96:99], v[172:175], 0\n"
      "v_fmac_f32 v84, v177, v177\n"
      "v_cmp_ne_u32 s[56:57], 0, v210\n"
      "v_mfma_f32_16x16x32_bf16 v[168:171], a[100:103], v[164:167], v[168:171]\n"
      "v_mul_f32 v122, s50, v122\n"
      "v_mul_f32_e64 v149, v122, v149\n"
      "v_mfma_f32_16x16x32_bf16 v[168:171], a[104:107], v[152:155], v[168:171]\n"
      "v_cvt_pk_bf16_f32 v134, v118, v119\n"
      "v_cvt_pk_bf16_f32 v104, v104, v105\n"
      "v_mfma_f32_16x16x32_bf16 v[168:171], a[108:111], v[220:223], v[168:171]\n"
      "v_fma_f32 v210, v128, v128, s51\n"
      "v_fmac_f32_e64 v210, v129, v129\n"
      "v_mfma_f32_16x16x32_bf16 v[168:171], a[112:115], v[160:163], v[168:171]\n"
      "ds_write_b64 v6, v[108:109] offset:6144\n"
      "v_mfma_f32_16x16x32_bf16 v[168:171], a[116:119], v[136:139], v[168:171]\n"
      "v_fmac_f32_e64 v84, v178, v178\n"
      "v_cvt_pk_bf16_f32 v97, v98, v99\n"
      "v_mfma_f32_16x16x32_bf16 v[112:115], a[120:123], v[172:175], 0\n"
      "v_fmac_f32 v210, v130, v130\n"
      "v_mul_f32 v187, v122, v187\n"
      "v_mfma_f32_16x16x32_bf16 v[112:115], a[124:127], v[164:167], v[112:115]\n"
      "v_fmac_f32 v210, v131, v131\n"
      "v_mul_f32 v148, v122, v148\n"
      "v_mfma_f32_16x16x32_bf16 v[112:115], a[128:131], v[152:155], v[112:115]\n"
      "v_mul_f32 v200, v81, v200\n"
      "v_mul_f32 v185, v81, v185\n"
      "v_mfma_f32_16x16x32_bf16 v[112:115], a[132:135], v[220:223], v[112:115]\n"
      "v_mul_f32 v124, v122, v124\n"
      "v_fmac_f32 v210, v168, v168\n"
      "v_mfma_f32_16x16x32_bf16 v[112:115], a[136:139], v[160:163], v[112:115]\n"
      "v_fmac_f32 v210, v169, v169\n"
      "v_fmac_f32 v210, v170, v170\n"
      "v_mfma_f32_16x16x32_bf16 v[112:115], a[140:143], v[136:139], v[112:115]\n"
      "v_fmac_f32 v84, v179, v179\n"
      "v_mul_f32 v150, v122, v150\n"
      "v_mfma_f32_16x16x32_bf16 v[116:119], v[172:175], a[144:147], 0\n"
      "v_fmac_f32 v84, v202, v202\n"
      "v_mul_f32 v147, v81, v147\n"
      "v_mfma_f32_16x16x32_bf16 v[116:119], v[164:167], a[148:151], v[116:119]\n"
      "v_fmac_f32 v84, v203, v203\n"
      "v_fmac_f32 v210, v171, v171\n"
      "v_mfma_f32_16x16x32_bf16 v[116:119], v[152:155], a[152:155], v[116:119]\n"
      "s_or_b32 s95, s95, s48\n"
      "v_mul_f32 v146, v81, v146\n"
      "v_fmac_f32_e64 v84, v204, v204\n"
      "v_mfma_f32_16x16x32_bf16 v[116:119], v[220:223], a[156:159], v[116:119]\n"
      "ds_write_b128 v5, v[132:135] offset:12288\n"
      "v_mfma_f32_16x16x32_bf16 v[116:119], v[160:163], a[160:163], v[116:119]\n"
      "s_cmp_ge_u32 s95, 0x40000000\n"
      "s_cselect_b32 s94, -1, -2\n"
      "v_fmac_f32_e64 v210, v112, v112\n"
      "v_mfma_f32_16x16x32_bf16 v[116:119], v[136:139], a[164:167], v[116:119]\n"
      "v_mul_f32 v198, v81, v198\n"
      "v_fmac_f32 v210, v113, v113\n"
      "v_mfma_f32_16x16x32_bf16 v[108:111], v[172:175], a[168:171], 0\n"
      "v_fmac_f32 v210, v114, v114\n"
      "v_mul_f32 v145, v81, v145\n"
      "v_mfma_f32_16x16x32_bf16 v[108:111], v[164:167], a[172:175], v[108:111]\n"
      "v_mul_f32 v201, v81, v201\n"
      "v_fmac_f32 v84, v205, v205\n"
      "v_mfma_f32_16x16x32_bf16 v[108:111], v[152:155], a[176:179], v[108:111]\n"
      "v_cvt_pk_bf16_f32 v105, v106, v107\n"
      "v_mul_f32 v144, v81, v144\n"
      "v_mfma_f32_16x16x32_bf16 v[108:111], v[220:223], a[180:183], v[108:111]\n"
      "ds_read_b128 v[140:143], v1 offset:10240\n"
      "v_fmac_f32 v210, v115, v115\n"
      "v_mfma_f32_16x16x32_bf16 v[108:111], v[160:163], a[184:187], v[108:111]\n"
      "s_nop 0\n"
      "v_permlane16_swap_b32 v84, v210\n"
      "v_mfma_f32_16x16x32_bf16 v[108:111], v[136:139], a[188:191], v[108:111]\n"
      "v_mul_f32 v151, v122, v151\n"
      "v_add_f32 v84, v84, v210\n"
      "v_mfma_f32_16x16x32_bf16 v[132:135], v[172:175], a[192:195], 0\n"
      "v_mov_b32 v210, v84\n"
      "v_mul_f32 v188, v122, v188\n"
      "v_mfma_f32_16x16x32_bf16 v[132:135], v[164:167], a[196:199], v[132:135]\n"
      "v_mul_f32_e64 v186, v122, v186\n"
      "v_cvt_pk_bf16_f32 v196, v116, v117\n"
      "v_mfma_f32_16x16x32_bf16 v[132:135], v[152:155], a[200:203], v[132:135]\n"
      "v_permlane32_swap_b32 v84, v210\n"
      "v_mfma_f32_16x16x32_bf16 v[132:135], v[220:223], a[204:207], v[132:135]\n"
      "ds_read_b128 v[220:223], v1 offset:16384\n"
      "v_mul_f32 v125, v122, v125\n"
      "v_mfma_f32_16x16x32_bf16 v[132:135], v[160:163], a[208:211], v[132:135]\n"
      "v_mul_f32 v127, v122, v127\n"
      "v_cvt_pk_bf16_f32 v148, v148, v149\n"
      "v_mfma_f32_16x16x32_bf16 v[132:135], v[136:139], a[212:215], v[132:135]\n"
      "s_waitcnt lgkmcnt(1)\n"
      "v_add_f32 v84, v84, v210\n"
      "v_mul_f32 v126, v122, v126\n"
      "v_mfma_f32_16x16x32_bf16 v[136:139], a[0:3], v[216:219], 0\n"
      "v_rsq_f32_e64 v84, v84\n"
      "v_mfma_f32_16x16x32_bf16 v[136:139], a[4:7], v[206:209], v[136:139]\n"
      "v_cvt_pk_bf16_f32 v197, v118, v119\n"
      "v_mov_b32 v210, v84\n"
      "v_mfma_f32_16x16x32_bf16 v[136:139], a[8:11], v[100:103], v[136:139]\n"
      "s_nop 0\n"
      "v_permlane16_swap_b32_e64 v84, v210\n"
      "v_mfma_f32_16x16x32_bf16 v[136:139], a[12:15], v[140:143], v[136:139]\n"
      "ds_read_u16 v82, v11 offset:32\n"
      "v_mul_f32 v189, v122, v189\n"
      "v_mfma_f32_16x16x32_bf16 v[136:139], a[16:19], v[212:215], v[136:139]\n"
      "s_waitcnt lgkmcnt(1)\n"
      "v_mul_f32 v184, v81, v184\n"
      "v_mul_f32 v131, v210, v131\n"
      "v_mfma_f32_16x16x32_bf16 v[136:139], a[20:23], v[220:223], v[136:139]\n"
      "v_mul_f32 v170, v210, v170\n"
      "v_mul_f32 v171, v210, v171\n"
      "v_mfma_f32_16x16x32_bf16 v[152:155], a[24:27], v[216:219], 0\n"
      "v_cvt_pk_bf16_f32 v98, v132, v133\n"
      "v_cvt_pk_bf16_f32 v106, v108, v109\n"
      "v_mfma_f32_16x16x32_bf16 v[152:155], a[28:31], v[206:209], v[152:155]\n"
      "v_mul_f32 v84, s50, v84\n"
      "v_mul_f32 v199, v81, v199\n"
      "v_mfma_f32_16x16x32_bf16 v[152:155], a[32:35], v[100:103], v[152:155]\n"
      "v_fma_f32 v89, v136, v136, s51\n"
      "v_mul_f32_e64 v113, v210, v113\n"
      "v_mfma_f32_16x16x32_bf16 v[152:155], a[36:39], v[140:143], v[152:155]\n"
      "v_cvt_pk_bf16_f32 v198, v198, v199\n"
      "v_mul_f32 v169, v210, v169\n"
      "v_mfma_f32_16x16x32_bf16 v[152:155], a[40:43], v[212:215], v[152:155]\n"
      "v_cvt_pk_bf16_f32 v144, v144, v145\n"
      "v_fmac_f32 v89, v137, v137\n"
      "v_mfma_f32_16x16x32_bf16 v[152:155], a[44:47], v[220:223], v[152:155]\n"
      "v_mul_f32 v205, v84, v205\n"
      "v_mul_f32 v203, v84, v203\n"
      "v_mfma_f32_16x16x32_bf16 v[116:119], a[48:51], v[216:219], 0\n"
      "v_cvt_pk_bf16_f32 v149, v150, v151\n"
      "v_fmac_f32 v89, v138, v138\n"
      "v_mfma_f32_16x16x32_bf16 v[116:119], a[52:55], v[206:209], v[116:119]\n"
      "s_cmp_gt_u32 s42, s44\n"
      "s_cselect_b32 s95, s94, s95\n"
      "v_mul_f32 v192, v84, v192\n"
      "v_mfma_f32_16x16x32_bf16 v[116:119], a[56:59], v[100:103], v[116:119]\n"
      "v_fmac_f32 v89, v139, v139\n"
      "v_cvt_pk_bf16_f32 v150, v124, v125\n"
      "v_mfma_f32_16x16x32_bf16 v[116:119], a[60:63], v[140:143], v[116:119]\n"
      "v_fmac_f32 v89, v152, v152\n"
      "v_mul_f32_e64 v202, v84, v202\n"
      "v_mfma_f32_16x16x32_bf16 v[116:119], a[64:67], v[212:215], v[116:119]\n"
      "v_mul_f32 v182, v81, v182\n"
      "v_fmac_f32 v89, v153, v153\n"
      "v_mfma_f32_16x16x32_bf16 v[116:119], a[68:71], v[220:223], v[116:119]\n"
      "v_fmac_f32 v89, v154, v154\n"
      "v_mul_f32 v191, v84, v191\n"
      "v_mfma_f32_16x16x32_bf16 v[164:167], a[72:75], v[216:219], 0\n"
      "v_fmac_f32 v89, v155, v155\n"
      "v_mul_f32 v129, v210, v129\n"
      "v_mfma_f32_16x16x32_bf16 v[164:167], a[76:79], v[206:209], v[164:167]\n"
      "v_cvt_pk_bf16_f32 v151, v126, v127\n"
      "v_cvt_pk_bf16_f32 v99, v134, v135\n"
      "v_mfma_f32_16x16x32_bf16 v[164:167], a[80:83], v[100:103], v[164:167]\n"
      "v_cvt_pk_bf16_f32 v107, v110, v111\n"
      "v_fmac_f32_e64 v89, v116, v116\n"
      "v_mfma_f32_16x16x32_bf16 v[164:167], a[84:87], v[140:143], v[164:167]\n"
      "v_mul_f32 v130, v210, v130\n"
      "v_mul_f32 v178, v84, v178\n"
      "v_mfma_f32_16x16x32_bf16 v[164:167], a[88:91], v[212:215], v[164:167]\n"
      "v_mul_f32 v112, v210, v112\n"
      "v_mul_f32 v168, v210, v168\n"
      "v_mfma_f32_16x16x32_bf16 v[164:167], a[92:95], v[220:223], v[164:167]\n"
      "v_mul_f32_e64 v128, v210, v128\n"
      "v_cvt_pk_bf16_f32 v112, v112, v113\n"
      "v_mfma_f32_16x16x32_bf16 v[120:123], a[96:99], v[216:219], 0\n"
      "v_cvt_pk_bf16_f32 v186, v186, v187\n"
      "v_cvt_pk_bf16_f32 v202, v202, v203\n"
      "v_mfma_f32_16x16x32_bf16 v[120:123], a[100:103], v[206:209], v[120:123]\n"
      "s_cmp_ge_u32 s95, 0x40000000\n"
      "s_cselect_b64 vcc, -1, s[56:57]\n"
      "v_mul_f32 v183, v81, v183\n"
      "v_mfma_f32_16x16x32_bf16 v[120:123], a[104:107], v[100:103], v[120:123]\n"
      "s_and_b32 s5, s84, s8\n"
      "v_cvt_pk_bf16_f32 v199, v200, v201\n"
      "v_mul_f32_e64 v190, v84, v190\n"
      "v_mfma_f32_16x16x32_bf16 v[120:123], a[108:111], v[140:143], v[120:123]\n"
      "v_cvt_pk_bf16_f32 v201, v184, v185\n"
      "v_fma_f32 v85, v164, v164, s51\n"
      "v_mfma_f32_16x16x32_bf16 v[120:123], a[112:115], v[212:215], v[120:123]\n"
      "v_cvt_pk_bf16_f32 v128, v128, v129\n"
      "v_fmac_f32 v85, v165, v165\n"
      "v_mfma_f32_16x16x32_bf16 v[120:123], a[116:119], v[220:223], v[120:123]\n"
      "v_mul_f32 v193, v84, v193\n"
      "v_fmac_f32_e64 v85, v166, v166\n"
      "v_mfma_f32_16x16x32_bf16 v[132:135], a[120:123], v[216:219], 0\n"
      "v_fmac_f32_e64 v85, v167, v167\n"
      "v_cvt_pk_bf16_f32 v129, v130, v131\n"
      "v_mfma_f32_16x16x32_bf16 v[132:135], a[124:127], v[206:209], v[132:135]\n"
      "v_cvt_pk_bf16_f32 v200, v182, v183\n"
      "v_mul_f32 v115, v210, v115\n"
      "v_mfma_f32_16x16x32_bf16 v[132:135], a[128:131], v[100:103], v[132:135]\n"
      "v_fmac_f32 v89, v117, v117\n"
      "v_mul_f32_e64 v114, v210, v114\n"
      "v_mfma_f32_16x16x32_bf16 v[132:135], a[132:135], v[140:143], v[132:135]\n"
      "v_fmac_f32_e64 v85, v120, v120\n"
      "v_cvt_pk_bf16_f32 v145, v146, v147\n"
      "v_mfma_f32_16x16x32_bf16 v[132:135], a[136:139], v[212:215], v[132:135]\n"
      "ds_read_b128 v[160:163], v2 offset:18432\n"
      "v_fmac_f32 v89, v118, v118\n"
      "v_mfma_f32_16x16x32_bf16 v[132:135], a[140:143], v[220:223], v[132:135]\n"
      "v_cvt_pk_bf16_f32 v131, v170, v171\n"
      "v_fmac_f32 v85, v121, v121\n"
      "v_mfma_f32_16x16x32_bf16 v[182:185], v[216:219], a[144:147], 0\n"
      "v_cvt_pk_bf16_f32 v130, v168, v169\n"
      "v_cvt_pk_bf16_f32 v187, v188, v189\n"
      "v_mfma_f32_16x16x32_bf16 v[182:185], v[206:209], a[148:151], v[182:185]\n"
      "v_fmac_f32 v85, v122, v122\n"
      "v_fmac_f32 v85, v123, v123\n"
      "v_mfma_f32_16x16x32_bf16 v[182:185], v[100:103], a[152:155], v[182:185]\n"
      "v_fmac_f32 v89, v119, v119\n"
      "v_fmac_f32 v85, v132, v132\n"
      "v_mfma_f32_16x16x32_bf16 v[182:185], v[140:143], a[156:159], v[182:185]\n"
      "v_fmac_f32 v85, v133, v133\n"
      "v_fmac_f32 v85, v134, v134\n"
      "v_mfma_f32_16x16x32_bf16 v[182:185], v[212:215], a[160:163], v[182:185]\n"
      "v_fmac_f32 v85, v135, v135\n"
      "v_mul_f32 v176, v84, v176\n"
      "v_mfma_f32_16x16x32_bf16 v[182:185], v[220:223], a[164:167], v[182:185]\n"
      "v_permlane16_swap_b32_e64 v89, v85\n"
      "v_mfma_f32_16x16x32_bf16 v[124:127], v[216:219], a[168:171], 0\n"
      "ds_read_b128 v[168:171], v2 offset:21504\n"
      "v_cvt_pk_bf16_f32 v113, v114, v115\n"
      "v_mfma_f32_16x16x32_bf16 v[124:127], v[206:209], a[172:175], v[124:127]\n"
      "v_add_f32 v89, v89, v85\n"
      "v_mov_b32 v85, v89\n"
      "v_mfma_f32_16x16x32_bf16 v[124:127], v[100:103], a[176:179], v[124:127]\n"
      "s_nop 0\n"
      "v_permlane32_swap_b32 v89, v85\n"
      "v_mfma_f32_16x16x32_bf16 v[124:127], v[140:143], a[180:183], v[124:127]\n"
      "v_add_f32 v89, v89, v85\n"
      "v_cvt_pk_bf16_f32 v190, v190, v191\n"
      "v_mfma_f32_16x16x32_bf16 v[124:127], v[212:215], a[184:187], v[124:127]\n"
      "v_rsq_f32 v89, v89\n"
      "v_mfma_f32_16x16x32_bf16 v[124:127], v[220:223], a[188:191], v[124:127]\n"
      "v_mov_b32 v85, v89\n"
      "v_mul_f32 v177, v84, v177\n"
      "v_mfma_f32_16x16x32_bf16 v[108:111], v[216:219], a[192:195], 0\n"
      "ds_read_b128 v[216:219], v2 offset:24576\n"
      "v_mul_f32 v179, v84, v179\n"
      "v_mfma_f32_16x16x32_bf16 v[108:111], v[206:209], a[196:199], v[108:111]\n"
      "v_permlane16_swap_b32 v89, v85\n"
      "v_mfma_f32_16x16x32_bf16 v[108:111], v[100:103], a[200:203], v[108:111]\n"
      "v_cvt_pk_bf16_f32 v191, v192, v193\n"
      "v_mul_f32 v89, s50, v89\n"
      "v_mfma_f32_16x16x32_bf16 v[108:111], v[140:143], a[204:207], v[108:111]\n"
      "ds_read_b128 v[100:103], v2 offset:27648\n"
      "v_mfma_f32_16x16x32_bf16 v[108:111], v[212:215], a[208:211], v[108:111]\n"
      "s_waitcnt lgkmcnt(2)\n"
      "v_mul_f32 v137, v89, v137\n"
      "v_mul_f32 v122, v85, v122\n"
      "v_mfma_f32_16x16x32_bf16 v[108:111], v[220:223], a[212:215], v[108:111]\n"
      "ds_read_b128 v[212:215], v2 offset:30720\n"
      "v_cvt_pk_bf16_f32 v81, v126, v127\n"
      "v_mfma_f32_16x16x32_bf16 v[172:175], a[216:219], v[160:163], 0\n"
      "v_mul_f32 v164, v85, v164\n"
      "v_mul_f32_e64 v155, v89, v155\n"
      "v_mfma_f32_16x16x32_bf16 v[172:175], a[220:223], v[168:171], v[172:175]\n"
      "s_waitcnt lgkmcnt(2)\n"
      "v_cvt_pk_bf16_f32 v76, v182, v183\n"
      "v_cvt_pk_bf16_f32 v193, v178, v179\n"
      "v_mfma_f32_16x16x32_bf16 v[172:175], a[224:227], v[216:219], v[172:175]\n"
      "v_mul_f32 v154, v89, v154\n"
      "v_mul_f32 v153, v89, v153\n"
      "v_mfma_f32_16x16x32_bf16 v[220:223], a[240:243], v[160:163], 0\n"
      "ds_read_b128 v[206:209], v2 offset:33792\n"
      "v_mul_f32 v116, v89, v116\n"
      "v_mfma_f32_16x16x32_bf16 v[220:223], a[244:247], v[168:171], v[220:223]\n"
      "v_mul_f32 v204, v84, v204\n"
      "v_cvt_pk_bf16_f32 v84, v108, v109\n"
      "v_mfma_f32_16x16x32_bf16 v[220:223], a[248:251], v[216:219], v[220:223]\n"
      "v_mul_f32 v139, v89, v139\n"
      "v_mul_f32 v136, v89, v136\n"
      "v_mfma_f32_16x16x32_bf16 v[140:143], v[232:235], v[160:163], 0\n"
      "ds_read_u16 v78, v11\n"
      "v_mul_f32 v167, v85, v167\n"
      "v_mfma_f32_16x16x32_bf16 v[140:143], v[236:239], v[168:171], v[140:143]\n"
      "s_waitcnt lgkmcnt(2)\n"
      "v_mul_f32 v138, v89, v138\n"
      "v_mul_f32 v132, v85, v132\n"
      "v_mfma_f32_16x16x32_bf16 v[140:143], v[240:243], v[216:219], v[140:143]\n"
      "ds_read_b128 v[168:171], v10 offset:32\n"
      "v_mul_f32 v166, v85, v166\n"
      "v_mfma_f32_16x16x32_bf16 v[172:175], a[228:231], v[100:103], v[172:175]\n"
      "v_mul_f32 v121, v85, v121\n"
      "v_mul_f32 v120, v85, v120\n"
      "v_mfma_f32_16x16x32_bf16 v[172:175], a[232:235], v[212:215], v[172:175]\n"
      "s_waitcnt lgkmcnt(2)\n"
      "v_mul_f32_e64 v165, v85, v165\n"
      "v_cvt_pk_bf16_f32 v164, v164, v165\n"
      "v_mfma_f32_16x16x32_bf16 v[172:175], a[236:239], v[206:209], v[172:175]\n"
      "v_cvt_pk_bf16_f32 v136, v136, v137\n"
      "v_cvt_pk_bf16_f32 v137, v138, v139\n"
      "v_mfma_f32_16x16x32_bf16 v[220:223], a[252:255], v[100:103], v[220:223]\n"
      "v_cvt_pk_bf16_f32 v77, v184, v185\n"
      "v_mul_f32 v123, v85, v123\n"
      "v_mfma_f32_16x16x32_bf16 v[220:223], v[224:227], v[212:215], v[220:223]\n"
      "ds_read_b128 v[180:183], v2 offset:18688\n"
      "v_mul_f32 v133, v85, v133\n"
      "v_mfma_f32_16x16x32_bf16 v[220:223], v[228:231], v[206:209], v[220:223]\n"
      "ds_read_b64 v[114:115], v10 offset:24\n"
      "v_mul_f32 v135, v85, v135\n"
      "v_mfma_f32_16x16x32_bf16 v[140:143], v[244:247], v[100:103], v[140:143]\n"
      "v_cvt_pk_bf16_f32 v80, v124, v125\n"
      "v_mul_f32 v152, v89, v152\n"
      "v_mfma_f32_16x16x32_bf16 v[140:143], v[248:251], v[212:215], v[140:143]\n"
      "ds_read_b128 v[210:213], v2 offset:24832\n"
      "v_cvt_pk_bf16_f32 v165, v166, v167\n"
      "v_mfma_f32_16x16x32_bf16 v[140:143], v[252:255], v[206:209], v[140:143]\n"
      "v_cvt_pk_bf16_f32 v167, v122, v123\n"
      "v_cvt_pk_bf16_f32 v166, v120, v121\n"
      "v_mfma_f32_16x16x32_bf16 v[214:217], v[198:201], v[148:151], v[40:43]\n"
      "v_mul_f32 v134, v85, v134\n"
      "v_cvt_pk_bf16_f32 v85, v110, v111\n"
      "v_mfma_f32_16x16x32_bf16 v[120:123], v[128:131], v[148:151], v[44:47]\n"
      "ds_read_b128 v[100:103], v2 offset:21760\n"
      "v_cvt_pk_bf16_f32 v192, v176, v177\n"
      "v_mfma_f32_16x16x32_bf16 v[160:163], v[164:167], v[148:151], v[48:51]\n"
      "s_waitcnt lgkmcnt(2)\n"
      "v_cvt_pk_bf16_f32 v139, v154, v155\n"
      "v_cvt_pk_bf16_f32 v132, v132, v133\n"
      "v_mfma_f32_16x16x32_bf16 v[124:127], v[168:171], v[148:151], v[24:27]\n"
      "ds_read_u16 v86, v11 offset:64\n"
      "v_cvt_pk_bf16_f32 v133, v134, v135\n"
      "v_mfma_f32_16x16x16_bf16 v[214:217], v[144:145], v[186:187], v[214:217]\n"
      "v_mul_f32 v117, v89, v117\n"
      "v_mul_f32 v118, v89, v118\n"
      "v_fma_f32 v218, v172, v172, s52\n"
      "v_mfma_f32_16x16x16_bf16 v[120:123], v[112:113], v[186:187], v[120:123]\n"
      "v_mul_f32_e64 v119, v89, v119\n"
      "v_cvt_pk_bf16_f32 v203, v204, v205\n"
      "s_waitcnt vmcnt(1)\n"
      "v_or3_b32 v156, v156, v157, v158\n"
      "v_mfma_f32_16x16x16_bf16 v[160:163], v[132:133], v[186:187], v[160:163]\n"
      "v_exp_f32 v216, v216\n"
      "v_cvt_pk_bf16_f32 v116, v116, v117\n"
      "v_mfma_f32_16x16x16_bf16 v[124:127], v[114:115], v[186:187], v[124:127]\n"
      "v_add_u32 v12, 32, v12\n"
      "v_exp_f32 v121, v121\n"
      "v_mfma_f32_16x16x32_bf16 v[206:209], a[216:219], v[180:183], 0\n"
      "s_waitcnt lgkmcnt(1)\n"
      "v_exp_f32 v215, v215\n"
      "v_mfma_f32_16x16x32_bf16 v[206:209], a[220:223], v[100:103], v[206:209]\n"
      "v_exp_f32 v217, v217\n"
      "v_mfma_f32_16x16x32_bf16 v[206:209], a[224:227], v[210:213], v[206:209]\n"
      "v_exp_f32 v122, v122\n"
      "v_mfma_f32_16x16x32_bf16 v[176:179], a[240:243], v[180:183], 0\n"
      "s_add_u32 s43, s43, 1\n"
      "v_exp_f32 v161, v161\n"
      "v_mfma_f32_16x16x32_bf16 v[176:179], a[244:247], v[100:103], v[176:179]\n"
      "v_exp_f32_e64 v214, v214\n"
      "v_mfma_f32_16x16x32_bf16 v[176:179], a[248:251], v[210:213], v[176:179]\n"
      "v_exp_f32 v162, v162\n"
      "v_mfma_f32_16x16x32_bf16 v[108:111], v[232:235], v[180:183], 0\n"
      "v_exp_f32 v123, v123\n"
      "v_mfma_f32_16x16x32_bf16 v[108:111], v[236:239], v[100:103], v[108:111]\n"
      "v_cvt_pk_bf16_f32 v138, v152, v153\n"
      "v_cvt_pk_bf16_f32 v117, v118, v119\n"
      "v_mfma_f32_16x16x32_bf16 v[108:111], v[240:243], v[210:213], v[108:111]\n"
      "v_exp_f32_e64 v163, v163\n"
      "v_mfma_f32_16x16x32_bf16 v[210:213], v[198:201], v[190:193], v[52:55]\n"
      "v_cvt_pk_bf16_f32 v214, v214, v215\n"
      "v_cvt_pk_bf16_f32 v215, v216, v217\n"
      "v_mfma_f32_16x16x32_bf16 v[100:103], v[128:131], v[190:193], v[56:59]\n"
      "v_exp_f32 v124, v124\n"
      "v_mfma_f32_16x16x32_bf16 v[184:187], v[164:167], v[190:193], v[60:63]\n"
      "ds_read_b128 v[146:149], v2 offset:27904\n"
      "s_waitcnt vmcnt(0)\n"
      "v_or3_b32 v156, v156, v159, v90\n"
      "v_mfma_f32_16x16x32_bf16 v[180:183], v[168:171], v[190:193], v[26:29]\n"
      "v_exp_f32 v120, v120\n"
      "v_mfma_f32_16x16x16_bf16 v[210:213], v[144:145], v[202:203], v[210:213]\n"
      "v_exp_f32 v160, v160\n"
      "v_cvt_pk_bf16_f32 v90, v124, 0\n"
      "v_mfma_f32_16x16x16_bf16 v[100:103], v[112:113], v[202:203], v[100:103]\n"
      "v_cvt_pk_bf16_f32 v217, v122, v123\n"
      "v_cvt_pk_bf16_f32 v89, v162, v163\n"
      "v_cvt_pk_bf16_f32 v88, v160, v161\n"
      "v_mfma_f32_16x16x16_bf16 v[184:187], v[132:133], v[202:203], v[184:187]\n"
      "v_cvt_pk_bf16_f32 v216, v120, v121\n"
      "v_exp_f32 v213, v213\n"
      "v_mfma_f32_16x16x16_bf16 v[180:183], v[114:115], v[202:203], v[180:183]\n"
      "v_exp_f32 v212, v212\n"
      "v_exp_f32 v101, v101\n"
      "v_mfma_f32_16x16x32_bf16 v[150:153], v[32:35], v[214:217], 0\n"
      "v_exp_f32 v102, v102\n"
      "v_mfma_f32_16x16x32_bf16 v[150:153], v[32:35], v[88:91], v[150:153]\n"
      "v_exp_f32_e64 v211, v211\n"
      "v_mfma_f32_16x16x32_bf16 v[160:163], v[194:197], v[214:217], 0\n"
      "ds_write_b32 v31, v156 offset:4608\n"
      "v_mfma_f32_16x16x32_bf16 v[160:163], v[76:79], v[88:91], v[160:163]\n"
      "v_exp_f32_e64 v187, v187\n"
      "v_mfma_f32_16x16x32_bf16 v[202:205], v[104:107], v[214:217], 0\n"
      "v_exp_f32 v186, v186\n"
      "v_mfma_f32_16x16x32_bf16 v[202:205], v[80:83], v[88:91], v[202:205]\n"
      "v_exp_f32 v100, v100\n"
      "v_mfma_f32_16x16x32_bf16 v[156:159], v[96:99], v[214:217], 0\n"
      "s_waitcnt lgkmcnt(1)\n"
      "v_exp_f32 v210, v210\n"
      "v_mfma_f32_16x16x32_bf16 v[156:159], v[84:87], v[88:91], v[156:159]\n"
      "v_cvt_pk_bf16_f32 v93, v186, v187\n"
      "v_cvt_pk_bf16_f32 v210, v210, v211\n"
      "v_cvt_pk_bf16_f32 v211, v212, v213\n"
      "v_mfma_f32_16x16x32_bf16 v[190:193], v[198:201], v[136:139], v[64:67]\n"
      "v_exp_f32 v185, v185\n"
      "v_mfma_f32_16x16x32_bf16 v[214:217], v[128:131], v[136:139], v[68:71]\n"
      "v_exp_f32 v103, v103\n"
      "v_mfma_f32_16x16x32_bf16 v[152:155], v[164:167], v[136:139], v[72:75]\n"
      "v_exp_f32_e64 v184, v184\n"
      "v_mfma_f32_16x16x32_bf16 v[186:189], v[168:171], v[136:139], v[28:31]\n"
      "ds_read_b128 v[122:125], v2 offset:30976\n"
      "v_cvt_pk_bf16_f32 v212, v100, v101\n"
      "v_mfma_f32_16x16x16_bf16 v[190:193], v[144:145], v[116:117], v[190:193]\n"
      "v_cvt_pk_bf16_f32 v213, v102, v103\n"
      "v_exp_f32_e64 v180, v180\n"
      "v_mfma_f32_16x16x16_bf16 v[214:217], v[112:113], v[116:117], v[214:217]\n"
      "ds_read_b128 v[136:139], v2 offset:34048\n"
      "v_cvt_pk_bf16_f32 v92, v184, v185\n"
      "v_mfma_f32_16x16x16_bf16 v[152:155], v[132:133], v[116:117], v[152:155]\n"
      "v_cvt_pk_bf16_f32 v94, v180, 0\n"
      "v_rcp_f32 v150, v150\n"
      "v_mfma_f32_16x16x16_bf16 v[186:189], v[114:115], v[116:117], v[186:189]\n"
      "v_exp_f32 v191, v191\n"
      "v_exp_f32_e64 v190, v190\n"
      "v_exp_f32_e64 v192, v192\n"
      "v_pk_mul_f32 v[162:163], v[162:163], v[150:151] op_sel_hi:[1,0]\n"
      "v_pk_mul_f32 v[158:159], v[158:159], v[150:151] op_sel_hi:[1,0]\n"
      "v_pk_mul_f32 v[202:203], v[202:203], v[150:151] op_sel_hi:[1,0]\n"
      "v_pk_mul_f32 v[204:205], v[204:205], v[150:151] op_sel_hi:[1,0]\n"
      "v_pk_mul_f32 v[156:157], v[156:157], v[150:151] op_sel_hi:[1,0]\n"
      "v_pk_mul_f32 v[160:161], v[160:161], v[150:151] op_sel_hi:[1,0]\n"
      "v_mfma_f32_16x16x32_bf16 v[206:209], a[228:231], v[146:149], v[206:209]\n"
      "s_waitcnt lgkmcnt(1)\n"
      "v_cvt_pk_bf16_f32 v156, v156, v157\n"
      "v_cvt_pk_bf16_f32 v157, v158, v159\n"
      "v_mfma_f32_16x16x32_bf16 v[206:209], a[232:235], v[122:125], v[206:209]\n"
      "s_waitcnt lgkmcnt(0)\n"
      "v_exp_f32_e64 v217, v217\n"
      "v_mfma_f32_16x16x32_bf16 v[206:209], a[236:239], v[136:139], v[206:209]\n"
      "ds_read_b128 v[182:185], v7 offset:18432\n"
      "v_cvt_pk_bf16_f32 v160, v160, v161\n"
      "v_mfma_f32_16x16x32_bf16 v[176:179], a[252:255], v[146:149], v[176:179]\n"
      "v_exp_f32_e64 v193, v193\n"
      "v_mfma_f32_16x16x32_bf16 v[176:179], v[224:227], v[122:125], v[176:179]\n"
      "v_exp_f32_e64 v215, v215\n"
      "v_mfma_f32_16x16x32_bf16 v[176:179], v[228:231], v[136:139], v[176:179]\n"
      "v_exp_f32_e64 v214, v214\n"
      "v_mfma_f32_16x16x32_bf16 v[108:111], v[244:247], v[146:149], v[108:111]\n"
      "v_cvt_pk_bf16_f32 v161, v162, v163\n"
      "v_cvt_pk_bf16_f32 v163, v204, v205\n"
      "v_mfma_f32_16x16x32_bf16 v[108:111], v[248:251], v[122:125], v[108:111]\n"
      "ds_read_b128 v[148:151], v7 offset:22528\n"
      "v_cvt_pk_bf16_f32 v190, v190, v191\n"
      "v_mfma_f32_16x16x32_bf16 v[108:111], v[252:255], v[136:139], v[108:111]\n"
      "v_exp_f32_e64 v153, v153\n"
      "v_mfma_f32_16x16x32_bf16 v[116:119], v[32:35], v[210:213], 0\n"
      "ds_read_b64 v[188:189], v10 offset:16\n"
      "v_exp_f32 v216, v216\n"
      "v_mfma_f32_16x16x32_bf16 v[116:119], v[32:35], v[92:95], v[116:119]\n"
      "s_waitcnt lgkmcnt(2)\n"
      "v_cvt_pk_bf16_f32 v191, v192, v193\n"
      "v_cvt_pk_bf16_f32 v193, v216, v217\n"
      "v_mfma_f32_16x16x32_bf16 v[136:139], v[194:197], v[210:213], 0\n"
      "ds_read_b128 v[122:125], v10\n"
      "v_cvt_pk_bf16_f32 v162, v202, v203\n"
      "v_mfma_f32_16x16x32_bf16 v[136:139], v[76:79], v[92:95], v[136:139]\n"
      "buffer_store_dwordx4 v[182:185], v20, s[24:27], s46 offen sc1\n"
      "v_mfma_f32_16x16x32_bf16 v[100:103], v[104:107], v[210:213], 0\n"
      "v_exp_f32_e64 v152, v152\n"
      "v_mfma_f32_16x16x32_bf16 v[100:103], v[80:83], v[92:95], v[100:103]\n"
      "ds_write_b128 v3, v[160:163]\n"
      "v_mfma_f32_16x16x32_bf16 v[202:205], v[96:99], v[210:213], 0\n"
      "v_exp_f32 v186, v186\n"
      "v_mfma_f32_16x16x32_bf16 v[202:205], v[84:87], v[92:95], v[202:205]\n"
      "s_waitcnt lgkmcnt(1)\n"
      "v_rcp_f32 v116, v116\n"
      "v_mfma_f32_16x16x32_bf16 v[210:213], v[122:125], v[198:201], v[30:33]\n"
      "v_exp_f32 v155, v155\n"
      "v_mfma_f32_16x16x32_bf16 v[158:161], v[122:125], v[128:131], v[30:33]\n"
      "ds_read_b128 v[118:121], v7 offset:26624\n"
      "v_cvt_pk_bf16_f32 v88, v152, v153\n"
      "v_mfma_f32_16x16x32_bf16 v[198:201], v[122:125], v[164:167], v[30:33]\n"
      "ds_read_b128 v[162:165], v2 offset:22016\n"
      "v_mul_f32_e64 v219, v173, v173\n"
      "v_mfma_f32_16x16x32_bf16 v[180:183], v[122:125], v[168:171], v[30:33]\n"
      "ds_read_b128 v[122:125], v2 offset:25088\n"
      "v_fma_f32 v152, v206, v206, s52\n"
      "v_mfma_f32_16x16x16_bf16 v[210:213], v[188:189], v[144:145], v[210:213]\n"
      "v_exp_f32 v154, v154\n"
      "v_mul_f32 v153, v207, v207\n"
      "v_mfma_f32_16x16x16_bf16 v[158:161], v[188:189], v[112:113], v[158:161]\n"
      "ds_read_b128 v[128:131], v2 offset:18944\n"
      "v_cvt_pk_bf16_f32 v90, v186, 0\n"
      "v_mfma_f32_16x16x16_bf16 v[198:201], v[188:189], v[132:133], v[198:201]\n"
      "buffer_store_dwordx4 v[148:151], v21, s[24:27], s46 offen sc1\n"
      "v_pk_mul_f32 v[204:205], v[204:205], v[116:117] op_sel_hi:[1,0]\n"
      "v_pk_mul_f32 v[202:203], v[202:203], v[116:117] op_sel_hi:[1,0]\n"
      "v_pk_fma_f32 v[152:153], v[208:209], v[208:209], v[152:153]\n"
      "v_pk_fma_f32 v[218:219], v[174:175], v[174:175], v[218:219]\n"
      "v_pk_fma_f32 v[152:153], v[176:177], v[176:177], v[152:153]\n"
      "v_pk_mul_f32 v[100:101], v[100:101], v[116:117] op_sel_hi:[1,0]\n"
      "v_pk_mul_f32 v[138:139], v[138:139], v[116:117] op_sel_hi:[1,0]\n"
      "v_pk_mul_f32 v[136:137], v[136:137], v[116:117] op_sel_hi:[1,0]\n"
      "v_pk_mul_f32 v[102:103], v[102:103], v[116:117] op_sel_hi:[1,0]\n"
      "v_cvt_pk_bf16_f32 v136, v136, v137\n"
      "v_pk_fma_f32 v[218:219], v[220:221], v[220:221], v[218:219]\n"
      "v_pk_fma_f32 v[218:219], v[222:223], v[222:223], v[218:219]\n"
      "v_mfma_f32_16x16x16_bf16 v[180:183], v[188:189], v[114:115], v[180:183]\n"
      "v_cvt_pk_bf16_f32 v137, v138, v139\n"
      "v_cvt_pk_bf16_f32 v138, v100, v101\n"
      "s_waitcnt lgkmcnt(0)\n"
      "v_mfma_f32_16x16x32_bf16 v[132:135], a[216:219], v[128:131], 0\n"
      "ds_read_b128 v[186:189], v7 offset:34816\n"
      "v_cvt_pk_bf16_f32 v139, v102, v103\n"
      "v_mfma_f32_16x16x32_bf16 v[132:135], a[220:223], v[162:165], v[132:135]\n"
      "ds_write_b128 v3, v[136:139] offset:256\n"
      "v_pk_fma_f32 v[152:153], v[178:179], v[178:179], v[152:153]\n"
      "v_mfma_f32_16x16x32_bf16 v[132:135], a[224:227], v[122:125], v[132:135]\n"
      "buffer_store_dwordx4 v[118:121], v22, s[24:27], s46 offen sc1\n"
      "s_nop 1\n"
      "v_mfma_f32_16x16x32_bf16 v[118:121], a[240:243], v[128:131], 0\n"
      "ds_read_b128 v[182:185], v7 offset:30720\n"
      "v_cndmask_b32 v210, v210, v158, s[58:59]\n"
      "v_mfma_f32_16x16x32_bf16 v[118:121], a[244:247], v[162:165], v[118:121]\n"
      "v_cndmask_b32 v210, v210, v198, s[60:61]\n"
      "v_cvt_pk_bf16_f32 v89, v154, v155\n"
      "v_mfma_f32_16x16x32_bf16 v[118:121], a[248:251], v[122:125], v[118:121]\n"
      "ds_write_b64 v4, v[156:157]\n"
      "v_mfma_f32_16x16x32_bf16 v[168:171], v[232:235], v[128:131], 0\n"
      "v_cvt_pk_bf16_f32 v202, v202, v203\n"
      "v_cvt_pk_bf16_f32 v192, v214, v215\n"
      "v_mfma_f32_16x16x32_bf16 v[168:171], v[236:239], v[162:165], v[168:171]\n"
      "ds_read_b128 v[100:103], v2 offset:34304\n"
      "v_cndmask_b32 v210, v210, v180, s[62:63]\n"
      "v_mfma_f32_16x16x32_bf16 v[168:171], v[240:243], v[122:125], v[168:171]\n"
      "ds_read_b128 v[214:217], v2 offset:28160\n"
      "v_cvt_pk_bf16_f32 v203, v204, v205\n"
      "v_mfma_f32_16x16x32_bf16 v[162:165], v[32:35], v[190:193], 0\n"
      "s_waitcnt lgkmcnt(3)\n"
      "v_exp_f32 v210, v210\n"
      "v_mfma_f32_16x16x32_bf16 v[162:165], v[32:35], v[88:91], v[162:165]\n"
      "ds_write_b64 v4, v[202:203] offset:256\n"
      "v_mfma_f32_16x16x32_bf16 v[156:159], v[194:197], v[190:193], 0\n"
      "ds_read_b128 v[202:205], v2 offset:31232\n"
      "v_cvt_pk_bf16_f32 v210, v210, v210\n"
      "v_mfma_f32_16x16x32_bf16 v[156:159], v[76:79], v[88:91], v[156:159]\n"
      "buffer_store_dwordx4 v[182:185], v23, s[24:27], s46 offen sc1\n"
      "v_pk_fma_f32 v[152:153], v[108:109], v[108:109], v[152:153]\n"
      "v_pk_fma_f32 v[152:153], v[110:111], v[110:111], v[152:153]\n"
      "v_pk_fma_f32 v[218:219], v[140:141], v[140:141], v[218:219]\n"
      "v_mfma_f32_16x16x32_bf16 v[128:131], v[104:107], v[190:193], 0\n"
      "v_rcp_f32 v162, v162\n"
      "v_mfma_f32_16x16x32_bf16 v[128:131], v[80:83], v[88:91], v[128:131]\n"
      "ds_write_b16 v13, v210\n"
      "v_mfma_f32_16x16x32_bf16 v[122:125], v[96:99], v[190:193], 0\n"
      "v_fmac_f32 v218, v142, v142\n"
      "v_add_f32 v152, v152, v153\n"
      "v_mfma_f32_16x16x32_bf16 v[122:125], v[84:87], v[88:91], v[122:125]\n"
      "s_waitcnt lgkmcnt(1)\n"
      "v_fmac_f32 v219, v143, v143\n"
      "v_mul_f32 v157, v162, v157\n"
      "v_mfma_f32_16x16x32_bf16 v[132:135], a[228:231], v[214:217], v[132:135]\n"
      "ds_read_b128 v[144:147], v14\n"
      "v_mul_f32 v156, v162, v156\n"
      "v_mfma_f32_16x16x32_bf16 v[132:135], a[232:235], v[202:205], v[132:135]\n"
      "ds_read_b128 v[198:201], v14 offset:64\n"
      "v_mul_f32 v159, v162, v159\n"
      "v_mfma_f32_16x16x32_bf16 v[132:135], a[236:239], v[100:103], v[132:135]\n"
      "v_mul_f32 v158, v162, v158\n"
      "v_cvt_pk_bf16_f32 v156, v156, v157\n"
      "v_mfma_f32_16x16x32_bf16 v[118:121], a[252:255], v[214:217], v[118:121]\n"
      "v_mul_f32 v128, v162, v128\n"
      "v_cvt_pk_bf16_f32 v157, v158, v159\n"
      "v_mfma_f32_16x16x32_bf16 v[118:121], v[224:227], v[202:205], v[118:121]\n"
      "v_mul_f32 v130, v162, v130\n"
      "v_mul_f32 v131, v162, v131\n"
      "v_mfma_f32_16x16x32_bf16 v[118:121], v[228:231], v[100:103], v[118:121]\n"
      "v_fma_f32 v164, v132, v132, s52\n"
      "v_add_f32_e64 v218, v218, v219\n"
      "v_mfma_f32_16x16x32_bf16 v[168:171], v[244:247], v[214:217], v[168:171]\n"
      "v_mul_f32_e64 v129, v162, v129\n"
      "v_mul_f32 v165, v133, v133\n"
      "v_mfma_f32_16x16x32_bf16 v[168:171], v[248:251], v[202:205], v[168:171]\n"
      "s_waitcnt lgkmcnt(0)\n"
      "v_cvt_pk_bf16_f32 v158, v128, v129\n"
      "v_cvt_pk_bf16_f32 v159, v130, v131\n"
      "v_mfma_f32_16x16x32_bf16 v[168:171], v[252:255], v[100:103], v[168:171]\n"
      "ds_write_b128 v3, v[156:159] offset:512\n"
      "v_pk_fma_f32 v[164:165], v[134:135], v[134:135], v[164:165]\n"
      "v_pk_fma_f32 v[164:165], v[118:119], v[118:119], v[164:165]\n"
      "v_pk_mul_f32 v[122:123], v[122:123], v[162:163] op_sel_hi:[1,0]\n"
      "v_permlane16_swap_b32 v218, v152\n"
      "v_add_f32 v218, v218, v152\n"
      "v_cvt_pk_bf16_f32 v122, v122, v123\n"
      "v_pk_fma_f32 v[164:165], v[120:121], v[120:121], v[164:165]\n"
      "v_pk_fma_f32 v[164:165], v[168:169], v[168:169], v[164:165]\n"
      "v_pk_mul_f32 v[124:125], v[124:125], v[162:163] op_sel_hi:[1,0]\n"
      "v_pk_fma_f32 v[164:165], v[170:171], v[170:171], v[164:165]\n"
      "v_add_f32 v164, v164, v165\n"
      "v_mov_b32 v152, v164\n"
      "v_cvt_pk_bf16_f32 v123, v124, v125\n"
      "s_and_b32 s86, s86, -4\n"
      "ds_write_b64 v4, v[122:123] offset:512\n"
      "v_permlane16_swap_b32 v164, v152\n"
      "v_add_f32 v164, v164, v152\n"
      "s_nop 1\n"
      "v_permlane32_swap_b32 v218, v164\n"
      "v_add_f32 v218, v218, v164\n"
      "v_mfma_f32_16x16x32_bf16 v[162:165], v[144:147], v[32:35], 0\n"
      "ds_write_b32 v31, v218\n"
      "buffer_store_dwordx4 v[186:189], v25, s[24:27], s46 offen sc1\n"
      "v_mfma_f32_16x16x32_bf16 v[162:165], v[198:201], v[32:35], v[162:165]\n"
      "s_add_u32 s42, s42, 1\n"
      "s_waitcnt lgkmcnt(0)\n"
      "s_barrier\n"
      "s_cbranch_vccz L_loop1_%=\n"
      "s_branch L_disp1_%=\n"
      "s_nop 7\n"
      "s_nop 7\n"
      "L_max1_%=:\n"
      "v_mfma_f32_16x16x32_bf16 v[152:155], v[144:147], v[194:197], 0\n"
      "ds_read_b128 v[186:189], v0 offset:18432\n"
      "s_add_u32 m0, s64, 0\n"
      "v_mfma_f32_16x16x32_bf16 v[152:155], v[198:201], v[76:79], v[152:155]\n"
      "ds_read_b128 v[128:131], v9\n"
      "v_mfma_f32_16x16x32_bf16 v[210:213], v[144:147], v[104:107], 0\n"
      "ds_read_b128 v[214:217], v1 offset:30720\n"
      "ds_read_b128 v[202:205], v1 offset:18432\n"
      "v_mfma_f32_16x16x32_bf16 v[210:213], v[198:201], v[80:83], v[210:213]\n"
      "buffer_load_dwordx4 v16, s[16:19], s84 offen  lds\n"
      "v_mfma_f32_16x16x32_bf16 v[180:183], v[144:147], v[96:99], 0\n"
      "ds_read_b128 v[190:193], v1 offset:24576\n"
      "v_readlane_b32 s45, v37, s43\n"
      "v_mfma_f32_16x16x32_bf16 v[180:183], v[198:201], v[84:87], v[180:183]\n"
      "ds_read_b128 v[144:147], v0 offset:24576\n"
      "s_waitcnt lgkmcnt(5)\n"
      "v_mfma_f32_16x16x32_bf16 v[136:139], a[0:3], v[186:189], 0\n"
      "ds_read_b128 v[114:117], v0 offset:26624\n"
      "v_cndmask_b32 v152, v152, v210, s[58:59]\n"
      "v_mfma_f32_16x16x32_bf16 v[122:125], a[24:27], v[186:189], 0\n"
      "s_waitcnt lgkmcnt(5)\n"
      "v_add_f32 v130, v130, v131\n"
      "v_add_f32 v128, v128, v129\n"
      "v_mfma_f32_16x16x32_bf16 v[154:157], a[48:51], v[186:189], 0\n"
      "ds_read_b128 v[100:103], v0 offset:30720\n"
      "v_add_f32_e64 v128, v128, v130\n"
      "v_mfma_f32_16x16x32_bf16 v[182:185], a[72:75], v[186:189], 0\n"
      "s_add_u32 m0, s64, 4096\n"
      "v_rsq_f32 v128, v128\n"
      "v_mfma_f32_16x16x32_bf16 v[198:201], a[96:99], v[186:189], 0\n"
      "s_waitcnt lgkmcnt(3)\n"
      "v_mul_f32 v128, 0x415db3d7, v128\n"
      "v_mov_b32_e64 v130, v128\n"
      "v_mfma_f32_16x16x32_bf16 v[148:151], a[120:123], v[186:189], 0\n"
      "buffer_load_dwordx4 v17, s[16:19], s84 offen  lds\n"
      "v_mfma_f32_16x16x32_bf16 v[136:139], a[4:7], v[202:205], v[136:139]\n"
      "s_waitcnt lgkmcnt(1)\n"
      "v_permlane32_swap_b32 v128, v130\n"
      "v_mfma_f32_16x16x32_bf16 v[136:139], a[8:11], v[144:147], v[136:139]\n"
      "ds_read_b128 v[210:213], v9 offset:4608\n"
      "v_mov_b32 v129, v128\n"
      "v_mfma_f32_16x16x32_bf16 v[122:125], a[28:31], v[202:205], v[122:125]\n"
      "s_nop 0\n"
      "v_permlane16_swap_b32 v128, v129\n"
      "v_mfma_f32_16x16x32_bf16 v[122:125], a[32:35], v[144:147], v[122:125]\n"
      "v_mul_f32 v220, v128, v220\n"
      "v_mul_f32 v223, v128, v223\n"
      "v_mfma_f32_16x16x32_bf16 v[154:157], a[52:55], v[202:205], v[154:157]\n"
      "v_mul_f32 v173, v128, v173\n"
      "v_mul_f32_e64 v221, v128, v221\n"
      "v_mfma_f32_16x16x32_bf16 v[154:157], a[56:59], v[144:147], v[154:157]\n"
      "s_add_u32 m0, s64, 8192\n"
      "v_mul_f32 v172, v128, v172\n"
      "v_mul_f32 v141, v128, v141\n"
      "v_mfma_f32_16x16x32_bf16 v[182:185], a[76:79], v[202:205], v[182:185]\n"
      "v_mul_f32 v177, v129, v177\n"
      "v_mul_f32 v110, v129, v110\n"
      "v_mfma_f32_16x16x32_bf16 v[182:185], a[80:83], v[144:147], v[182:185]\n"
      "buffer_load_dwordx4 v18, s[16:19], s84 offen  lds\n"
      "v_mfma_f32_16x16x32_bf16 v[198:201], a[100:103], v[202:205], v[198:201]\n"
      "v_mul_f32_e64 v176, v129, v176\n"
      "v_cvt_pk_bf16_f32 v172, v172, v173\n"
      "v_mfma_f32_16x16x32_bf16 v[198:201], a[104:107], v[144:147], v[198:201]\n"
      "s_add_u32 m0, s64, 12288\n"
      "v_mul_f32 v175, v128, v175\n"
      "v_mul_f32 v168, v130, v168\n"
      "v_mfma_f32_16x16x32_bf16 v[148:151], a[124:127], v[202:205], v[148:151]\n"
      "s_waitcnt lgkmcnt(0)\n"
      "v_mul_f32 v132, v130, v132\n"
      "v_or3_b32 v210, v210, v211, v212\n"
      "v_mfma_f32_16x16x32_bf16 v[148:151], a[128:131], v[144:147], v[148:151]\n"
      "v_mul_f32 v133, v130, v133\n"
      "v_mul_f32 v222, v128, v222\n"
      "v_mul_f32_e64 v111, v129, v111\n"
      "v_mfma_f32_16x16x32_bf16 v[136:139], a[12:15], v[190:193], v[136:139]\n"
      "buffer_load_dwordx4 v19, s[16:19], s84 offen  lds\n"
      "v_mfma_f32_16x16x32_bf16 v[136:139], a[16:19], v[100:103], v[136:139]\n"
      "v_mul_f32 v170, v130, v170\n"
      "v_mul_f32 v109, v129, v109\n"
      "v_mfma_f32_16x16x32_bf16 v[136:139], a[20:23], v[214:217], v[136:139]\n"
      "s_mov_b32 m0, s6\n"
      "v_mul_f32 v120, v130, v120\n"
      "v_mul_f32 v209, v129, v209\n"
      "v_mfma_f32_16x16x32_bf16 v[122:125], a[36:39], v[190:193], v[122:125]\n"
      "ds_read_b128 v[164:167], v0 offset:32768\n"
      "v_mul_f32 v179, v129, v179\n"
      "v_mfma_f32_16x16x32_bf16 v[122:125], a[40:43], v[100:103], v[122:125]\n"
      "v_mul_f32 v174, v128, v174\n"
      "v_mul_f32 v207, v129, v207\n"
      "v_mfma_f32_16x16x32_bf16 v[122:125], a[44:47], v[214:217], v[122:125]\n"
      "v_mul_f32_e64 v119, v130, v119\n"
      "v_readlane_b32 s46, v37, s42\n"
      "v_mfma_f32_16x16x32_bf16 v[154:157], a[60:63], v[190:193], v[154:157]\n"
      "v_mul_f32 v206, v129, v206\n"
      "v_cvt_pk_bf16_f32 v173, v174, v175\n"
      "v_mfma_f32_16x16x32_bf16 v[154:157], a[64:67], v[100:103], v[154:157]\n"
      "s_or_b32 s95, s45, s46\n"
      "v_mul_f32 v140, v128, v140\n"
      "v_mul_f32 v121, v130, v121\n"
      "v_mfma_f32_16x16x32_bf16 v[154:157], a[68:71], v[214:217], v[154:157]\n"
      "v_mul_f32_e64 v169, v130, v169\n"
      "v_fma_f32 v153, v136, v136, s51\n"
      "v_mfma_f32_16x16x32_bf16 v[182:185], a[84:87], v[190:193], v[182:185]\n"
      "v_cvt_pk_bf16_f32 v174, v220, v221\n"
      "v_mul_f32_e64 v108, v129, v108\n"
      "v_mfma_f32_16x16x32_bf16 v[182:185], a[88:91], v[100:103], v[182:185]\n"
      "buffer_load_dwordx4 v15, s[0:3], s5 offen  lds\n"
      "v_mfma_f32_16x16x32_bf16 v[182:185], a[92:95], v[214:217], v[182:185]\n"
      "v_mul_f32_e64 v208, v129, v208\n"
      "v_mov_b32_dpp v162, v162 quad_perm:[0,0,0,0] row_mask:0xf bank_mask:0xf\n"
      "v_mfma_f32_16x16x32_bf16 v[198:201], a[108:111], v[190:193], v[198:201]\n"
      "v_cvt_pk_bf16_f32 v108, v108, v109\n"
      "v_cvt_pk_bf16_f32 v175, v222, v223\n"
      "v_mfma_f32_16x16x32_bf16 v[198:201], a[112:115], v[100:103], v[198:201]\n"
      "ds_write_b128 v5, v[172:175] offset:18432\n"
      "v_mfma_f32_16x16x32_bf16 v[198:201], a[116:119], v[214:217], v[198:201]\n"
      "ds_read_b128 v[172:175], v1 offset:20480\n"
      "v_fmac_f32_e64 v153, v137, v137\n"
      "v_mfma_f32_16x16x32_bf16 v[148:151], a[132:135], v[190:193], v[148:151]\n"
      "v_fmac_f32 v153, v138, v138\n"
      "v_fma_f32 v163, v182, v182, s51\n"
      "v_mfma_f32_16x16x32_bf16 v[148:151], a[136:139], v[100:103], v[148:151]\n"
      "v_fmac_f32 v153, v139, v139\n"
      "v_cvt_pk_bf16_f32 v140, v140, v141\n"
      "v_mfma_f32_16x16x32_bf16 v[148:151], a[140:143], v[214:217], v[148:151]\n"
      "buffer_load_dwordx4 v[158:161], v36, s[28:31], s86 offen\n"
      "v_mfma_f32_16x16x32_bf16 v[194:197], v[186:189], a[144:147], 0\n"
      "v_cvt_pk_bf16_f32 v206, v206, v207\n"
      "v_mul_f32 v143, v128, v143\n"
      "v_mfma_f32_16x16x32_bf16 v[194:197], v[202:205], a[148:151], v[194:197]\n"
      "v_cvt_pk_bf16_f32 v207, v208, v209\n"
      "v_fmac_f32 v153, v122, v122\n"
      "v_mfma_f32_16x16x32_bf16 v[194:197], v[144:147], a[152:155], v[194:197]\n"
      "ds_read_b128 v[220:223], v0 offset:22528\n"
      "v_mul_f32 v118, v130, v118\n"
      "v_mfma_f32_16x16x32_bf16 v[194:197], v[190:193], a[156:159], v[194:197]\n"
      "v_mul_f32 v178, v129, v178\n"
      "v_mul_f32 v142, v128, v142\n"
      "v_mfma_f32_16x16x32_bf16 v[194:197], v[100:103], a[160:163], v[194:197]\n"
      "v_mul_f32 v171, v130, v171\n"
      "v_fmac_f32 v153, v123, v123\n"
      "v_mfma_f32_16x16x32_bf16 v[194:197], v[214:217], a[164:167], v[194:197]\n"
      "v_fmac_f32 v163, v183, v183\n"
      "v_fmac_f32 v153, v124, v124\n"
      "v_mfma_f32_16x16x32_bf16 v[104:107], v[186:189], a[168:171], 0\n"
      "v_fmac_f32 v163, v184, v184\n"
      "v_cvt_pk_bf16_f32 v168, v168, v169\n"
      "v_mfma_f32_16x16x32_bf16 v[104:107], v[202:205], a[172:175], v[104:107]\n"
      "v_cvt_pk_bf16_f32 v141, v142, v143\n"
      "v_cvt_pk_bf16_f32 v209, v178, v179\n"
      "v_mfma_f32_16x16x32_bf16 v[104:107], v[144:147], a[176:179], v[104:107]\n"
      "v_cvt_pk_bf16_f32 v109, v110, v111\n"
      "v_fmac_f32 v163, v185, v185\n"
      "v_mfma_f32_16x16x32_bf16 v[104:107], v[190:193], a[180:183], v[104:107]\n"
      "ds_read_b128 v[126:129], v1 offset:26624\n"
      "v_fmac_f32 v153, v125, v125\n"
      "v_mfma_f32_16x16x32_bf16 v[104:107], v[100:103], a[184:187], v[104:107]\n"
      "ds_read_b128 v[110:113], v0 offset:20480\n"
      "v_fmac_f32 v163, v198, v198\n"
      "v_mfma_f32_16x16x32_bf16 v[104:107], v[214:217], a[188:191], v[104:107]\n"
      "v_fmac_f32 v153, v154, v154\n"
      "v_cvt_pk_bf16_f32 v194, v194, v195\n"
      "v_mfma_f32_16x16x32_bf16 v[96:99], v[186:189], a[192:195], 0\n"
      "v_cvt_pk_bf16_f32 v169, v170, v171\n"
      "v_fmac_f32_e64 v163, v199, v199\n"
      "v_mfma_f32_16x16x32_bf16 v[96:99], v[202:205], a[196:199], v[96:99]\n"
      "ds_read_b128 v[202:205], v1 offset:32768\n"
      "v_cndmask_b32 v152, v152, v180, s[60:61]\n"
      "v_mfma_f32_16x16x32_bf16 v[96:99], v[144:147], a[200:203], v[96:99]\n"
      "v_rcp_f32_e64 v162, v162\n"
      "v_mfma_f32_16x16x32_bf16 v[96:99], v[190:193], a[204:207], v[96:99]\n"
      "ds_write_b64 v6, v[140:141] offset:18432\n"
      "v_mfma_f32_16x16x32_bf16 v[96:99], v[100:103], a[208:211], v[96:99]\n"
      "s_waitcnt lgkmcnt(2)\n"
      "v_fmac_f32 v163, v200, v200\n"
      "v_cvt_pk_bf16_f32 v132, v132, v133\n"
      "v_mfma_f32_16x16x32_bf16 v[96:99], v[214:217], a[212:215], v[96:99]\n"
      "buffer_load_dword v93, v29, s[28:31], s86 offen\n"
      "v_mfma_f32_16x16x32_bf16 v[216:219], a[0:3], v[110:113], 0\n"
      "v_fmac_f32 v163, v201, v201\n"
      "v_bitop3_b32 v210, v210, v213, s33 bitop3:0xa8\n"
      "v_mfma_f32_16x16x32_bf16 v[216:219], a[4:7], v[172:175], v[216:219]\n"
      "v_mul_f32 v152, v162, v152\n"
      "v_fmac_f32_e64 v163, v148, v148\n"
      "v_mfma_f32_16x16x32_bf16 v[216:219], a[8:11], v[114:117], v[216:219]\n"
      "v_fmac_f32_e64 v163, v149, v149\n"
      "v_cvt_pk_bf16_f32 v208, v176, v177\n"
      "v_mfma_f32_16x16x32_bf16 v[216:219], a[12:15], v[126:129], v[216:219]\n"
      "v_cvt_pk_bf16_f32 v96, v96, v97\n"
      "v_readlane_b32 s47, v38, s43\n"
      "v_mfma_f32_16x16x32_bf16 v[216:219], a[16:19], v[164:167], v[216:219]\n"
      "s_waitcnt lgkmcnt(0)\n"
      "v_fmac_f32 v153, v155, v155\n"
      "v_cvt_pk_bf16_f32 v97, v98, v99\n"
      "v_mfma_f32_16x16x32_bf16 v[216:219], a[20:23], v[202:205], v[216:219]\n"
      "v_fmac_f32_e64 v153, v156, v156\n"
      "v_cvt_pk_bf16_f32 v104, v104, v105\n"
      "v_mfma_f32_16x16x32_bf16 v[190:193], a[24:27], v[110:113], 0\n"
      "ds_read_b128 v[144:147], v1 offset:22528\n"
      "v_mul_f32_e64 v134, v130, v134\n"
      "v_mfma_f32_16x16x32_bf16 v[190:193], a[28:31], v[172:175], v[190:193]\n"
      "ds_read_b128 v[212:215], v1 offset:34816\n"
      "v_mul_f32_e64 v135, v130, v135\n"
      "v_mfma_f32_16x16x32_bf16 v[190:193], a[32:35], v[114:117], v[190:193]\n"
      "ds_write_b128 v5, v[206:209] offset:24576\n"
      "v_mfma_f32_16x16x32_bf16 v[190:193], a[36:39], v[126:129], v[190:193]\n"
      "ds_read_b128 v[186:189], v0 offset:28672\n"
      "v_fma_f32 v180, v216, v216, s51\n"
      "v_mfma_f32_16x16x32_bf16 v[190:193], a[40:43], v[164:167], v[190:193]\n"
      "v_fmac_f32 v163, v150, v150\n"
      "v_fmac_f32 v180, v217, v217\n"
      "v_mfma_f32_16x16x32_bf16 v[190:193], a[44:47], v[202:205], v[190:193]\n"
      "v_fmac_f32 v163, v151, v151\n"
      "v_fmac_f32 v153, v157, v157\n"
      "v_mfma_f32_16x16x32_bf16 v[206:209], a[48:51], v[110:113], 0\n"
      "v_fmac_f32 v180, v218, v218\n"
      "v_cvt_pk_bf16_f32 v152, v152, v152\n"
      "v_mfma_f32_16x16x32_bf16 v[206:209], a[52:55], v[172:175], v[206:209]\n"
      "v_permlane16_swap_b32 v153, v163\n"
      "v_mfma_f32_16x16x32_bf16 v[206:209], a[56:59], v[114:117], v[206:209]\n"
      "ds_write_b16 v12, v152\n"
      "v_mfma_f32_16x16x32_bf16 v[206:209], a[60:63], v[126:129], v[206:209]\n"
      "v_add_f32_e64 v153, v153, v163\n"
      "v_cvt_pk_bf16_f32 v195, v196, v197\n"
      "v_mfma_f32_16x16x32_bf16 v[206:209], a[64:67], v[164:167], v[206:209]\n"
      "ds_read_u16 v86, v11 offset:1232\n"
      "v_cvt_pk_bf16_f32 v133, v134, v135\n"
      "v_mfma_f32_16x16x32_bf16 v[206:209], a[68:71], v[202:205], v[206:209]\n"
      "ds_write_b64 v6, v[108:109] offset:24576\n"
      "v_mfma_f32_16x16x32_bf16 v[100:103], a[72:75], v[110:113], 0\n"
      "v_mov_b32 v163, v153\n"
      "v_fmac_f32 v180, v219, v219\n"
      "v_mfma_f32_16x16x32_bf16 v[100:103], a[76:79], v[172:175], v[100:103]\n"
      "v_permlane32_swap_b32 v153, v163\n"
      "v_mfma_f32_16x16x32_bf16 v[100:103], a[80:83], v[114:117], v[100:103]\n"
      "s_or_b32 s95, s95, s48\n"
      "v_fmac_f32_e64 v180, v190, v190\n"
      "v_cvt_pk_bf16_f32 v105, v106, v107\n"
      "v_mfma_f32_16x16x32_bf16 v[100:103], a[84:87], v[126:129], v[100:103]\n"
      "v_add_f32 v153, v153, v163\n"
      "v_fmac_f32 v180, v191, v191\n"
      "v_mfma_f32_16x16x32_bf16 v[100:103], a[88:91], v[164:167], v[100:103]\n"
      "v_rsq_f32_e64 v153, v153\n"
      "v_mfma_f32_16x16x32_bf16 v[100:103], a[92:95], v[202:205], v[100:103]\n"
      "s_cmp_ge_u32 s95, 0x40000000\n"
      "s_cselect_b32 s94, -1, -2\n"
      "v_mov_b32 v163, v153\n"
      "v_mfma_f32_16x16x32_bf16 v[140:143], a[96:99], v[110:113], 0\n"
      "s_nop 0\n"
      "v_permlane16_swap_b32 v153, v163\n"
      "v_mfma_f32_16x16x32_bf16 v[140:143], a[100:103], v[172:175], v[140:143]\n"
      "v_fmac_f32 v180, v192, v192\n"
      "v_fma_f32 v108, v100, v100, s51\n"
      "v_mfma_f32_16x16x32_bf16 v[140:143], a[104:107], v[114:117], v[140:143]\n"
      "v_fmac_f32 v108, v101, v101\n"
      "v_cvt_pk_bf16_f32 v135, v120, v121\n"
      "v_mfma_f32_16x16x32_bf16 v[140:143], a[108:111], v[126:129], v[140:143]\n"
      "v_mul_f32 v199, v163, v199\n"
      "v_mul_f32_e64 v185, v163, v185\n"
      "v_mfma_f32_16x16x32_bf16 v[140:143], a[112:115], v[164:167], v[140:143]\n"
      "v_cvt_pk_bf16_f32 v134, v118, v119\n"
      "v_mul_f32 v153, s50, v153\n"
      "v_mfma_f32_16x16x32_bf16 v[140:143], a[116:119], v[202:205], v[140:143]\n"
      "v_mul_f32 v155, v153, v155\n"
      "v_fmac_f32 v180, v193, v193\n"
      "v_mfma_f32_16x16x32_bf16 v[118:121], a[120:123], v[110:113], 0\n"
      "v_mul_f32 v137, v153, v137\n"
      "v_fmac_f32 v108, v102, v102\n"
      "v_mfma_f32_16x16x32_bf16 v[118:121], a[124:127], v[172:175], v[118:121]\n"
      "v_fmac_f32 v108, v103, v103\n"
      "v_mul_f32_e64 v154, v153, v154\n"
      "v_mfma_f32_16x16x32_bf16 v[118:121], a[128:131], v[114:117], v[118:121]\n"
      "ds_write_b128 v5, v[132:135] offset:30720\n"
      "v_pk_mul_f32 v[182:183], v[182:183], v[162:163] op_sel:[0,1]\n"
      "v_mfma_f32_16x16x32_bf16 v[118:121], a[132:135], v[126:129], v[118:121]\n"
      "v_fmac_f32 v108, v140, v140\n"
      "v_mul_f32 v148, v163, v148\n"
      "v_mfma_f32_16x16x32_bf16 v[118:121], a[136:139], v[164:167], v[118:121]\n"
      "v_fmac_f32 v108, v141, v141\n"
      "v_fmac_f32 v180, v206, v206\n"
      "v_mfma_f32_16x16x32_bf16 v[118:121], a[140:143], v[202:205], v[118:121]\n"
      "ds_write_b64 v6, v[168:169] offset:30720\n"
      "v_mfma_f32_16x16x32_bf16 v[132:135], v[110:113], a[144:147], 0\n"
      "v_mul_f32 v184, v163, v184\n"
      "v_mul_f32 v157, v153, v157\n"
      "v_mfma_f32_16x16x32_bf16 v[132:135], v[172:175], a[148:151], v[132:135]\n"
      "v_fmac_f32 v108, v142, v142\n"
      "v_mul_f32 v124, v153, v124\n"
      "v_mfma_f32_16x16x32_bf16 v[132:135], v[114:117], a[152:155], v[132:135]\n"
      "v_mul_f32 v151, v163, v151\n"
      "v_mul_f32 v198, v163, v198\n"
      "v_mfma_f32_16x16x32_bf16 v[132:135], v[126:129], a[156:159], v[132:135]\n"
      "v_fmac_f32 v180, v207, v207\n"
      "v_fmac_f32 v108, v143, v143\n"
      "v_mfma_f32_16x16x32_bf16 v[132:135], v[164:167], a[160:163], v[132:135]\n"
      "v_mul_f32 v149, v163, v149\n"
      "v_mul_f32 v139, v153, v139\n"
      "v_mfma_f32_16x16x32_bf16 v[132:135], v[202:205], a[164:167], v[132:135]\n"
      "v_mul_f32 v156, v153, v156\n"
      "v_mul_f32 v136, v153, v136\n"
      "v_mfma_f32_16x16x32_bf16 v[176:179], v[110:113], a[168:171], 0\n"
      "v_fmac_f32_e64 v180, v208, v208\n"
      "v_cvt_pk_bf16_f32 v148, v148, v149\n"
      "v_mfma_f32_16x16x32_bf16 v[176:179], v[172:175], a[172:175], v[176:179]\n"
      "v_mul_f32 v201, v163, v201\n"
      "v_mul_f32 v150, v163, v150\n"
      "v_mfma_f32_16x16x32_bf16 v[176:179], v[114:117], a[176:179], v[176:179]\n"
      "v_cvt_pk_bf16_f32 v196, v132, v133\n"
      "v_fmac_f32 v180, v209, v209\n"
      "v_mfma_f32_16x16x32_bf16 v[176:179], v[126:129], a[180:183], v[176:179]\n"
      "v_fmac_f32 v108, v118, v118\n"
      "v_mul_f32_e64 v138, v153, v138\n"
      "v_mfma_f32_16x16x32_bf16 v[176:179], v[164:167], a[184:187], v[176:179]\n"
      "v_cvt_pk_bf16_f32 v182, v182, v183\n"
      "v_fmac_f32_e64 v108, v119, v119\n"
      "v_mfma_f32_16x16x32_bf16 v[176:179], v[202:205], a[188:191], v[176:179]\n"
      "ds_read_b128 v[130:133], v1 offset:28672\n"
      "v_fmac_f32 v108, v120, v120\n"
      "v_mfma_f32_16x16x32_bf16 v[168:171], v[110:113], a[192:195], 0\n"
      "v_fmac_f32 v108, v121, v121\n"
      "v_cvt_pk_bf16_f32 v197, v134, v135\n"
      "v_mfma_f32_16x16x32_bf16 v[168:171], v[172:175], a[196:199], v[168:171]\n"
      "v_permlane16_swap_b32 v180, v108\n"
      "v_mfma_f32_16x16x32_bf16 v[168:171], v[114:117], a[200:203], v[168:171]\n"
      "ds_read_b128 v[172:175], v0 offset:34816\n"
      "v_mul_f32 v200, v163, v200\n"
      "v_mfma_f32_16x16x32_bf16 v[168:171], v[126:129], a[204:207], v[168:171]\n"
      "v_add_f32 v180, v180, v108\n"
      "v_mov_b32 v108, v180\n"
      "v_mfma_f32_16x16x32_bf16 v[168:171], v[164:167], a[208:211], v[168:171]\n"
      "s_cmp_gt_u32 s42, s44\n"
      "s_cselect_b32 s95, s94, s95\n"
      "v_mul_f32 v123, v153, v123\n"
      "v_mfma_f32_16x16x32_bf16 v[168:171], v[202:205], a[212:215], v[168:171]\n"
      "v_permlane32_swap_b32 v180, v108\n"
      "v_mfma_f32_16x16x32_bf16 v[202:205], a[0:3], v[220:223], 0\n"
      "s_waitcnt lgkmcnt(1)\n"
      "v_add_f32 v180, v180, v108\n"
      "v_cvt_pk_bf16_f32 v154, v154, v155\n"
      "v_mfma_f32_16x16x32_bf16 v[202:205], a[4:7], v[144:147], v[202:205]\n"
      "v_rsq_f32 v180, v180\n"
      "v_mfma_f32_16x16x32_bf16 v[202:205], a[8:11], v[186:189], v[202:205]\n"
      "v_mov_b32 v108, v180\n"
      "v_cvt_pk_bf16_f32 v136, v136, v137\n"
      "v_mfma_f32_16x16x32_bf16 v[202:205], a[12:15], v[130:133], v[202:205]\n"
      "s_waitcnt lgkmcnt(0)\n"
      "v_permlane16_swap_b32 v180, v108\n"
      "v_mfma_f32_16x16x32_bf16 v[202:205], a[16:19], v[172:175], v[202:205]\n"
      "v_mul_f32_e64 v119, v108, v119\n"
      "v_cvt_pk_bf16_f32 v155, v156, v157\n"
      "v_mfma_f32_16x16x32_bf16 v[202:205], a[20:23], v[212:215], v[202:205]\n"
      "v_cvt_pk_bf16_f32 v183, v184, v185\n"
      "v_cvt_pk_bf16_f32 v149, v150, v151\n"
      "v_mfma_f32_16x16x32_bf16 v[162:165], a[24:27], v[220:223], 0\n"
      "v_mul_f32 v125, v153, v125\n"
      "v_mul_f32 v180, s50, v180\n"
      "v_mfma_f32_16x16x32_bf16 v[162:165], a[28:31], v[144:147], v[162:165]\n"
      "v_mul_f32 v100, v108, v100\n"
      "v_mul_f32 v216, v180, v216\n"
      "v_mfma_f32_16x16x32_bf16 v[162:165], a[32:35], v[186:189], v[162:165]\n"
      "v_mul_f32 v207, v180, v207\n"
      "v_mul_f32 v101, v108, v101\n"
      "v_mfma_f32_16x16x32_bf16 v[162:165], a[36:39], v[130:133], v[162:165]\n"
      "v_fma_f32 v157, v202, v202, s51\n"
      "v_mul_f32 v118, v108, v118\n"
      "v_mfma_f32_16x16x32_bf16 v[162:165], a[40:43], v[172:175], v[162:165]\n"
      "v_mul_f32 v217, v180, v217\n"
      "v_mul_f32_e64 v191, v180, v191\n"
      "v_mfma_f32_16x16x32_bf16 v[162:165], a[44:47], v[212:215], v[162:165]\n"
      "v_mul_f32 v219, v180, v219\n"
      "v_fmac_f32 v157, v203, v203\n"
      "v_mfma_f32_16x16x32_bf16 v[126:129], a[48:51], v[220:223], 0\n"
      "v_cvt_pk_bf16_f32 v137, v138, v139\n"
      "v_fmac_f32 v157, v204, v204\n"
      "v_mfma_f32_16x16x32_bf16 v[126:129], a[52:55], v[144:147], v[126:129]\n"
      "v_fmac_f32 v157, v205, v205\n"
      "v_mul_f32 v206, v180, v206\n"
      "v_mfma_f32_16x16x32_bf16 v[126:129], a[56:59], v[186:189], v[126:129]\n"
      "v_mul_f32 v122, v153, v122\n"
      "v_mul_f32 v218, v180, v218\n"
      "v_mfma_f32_16x16x32_bf16 v[126:129], a[60:63], v[130:133], v[126:129]\n"
      "v_cvt_pk_bf16_f32 v98, v168, v169\n"
      "v_mul_f32 v208, v180, v208\n"
      "v_mfma_f32_16x16x32_bf16 v[126:129], a[64:67], v[172:175], v[126:129]\n"
      "v_cvt_pk_bf16_f32 v206, v206, v207\n"
      "v_cvt_pk_bf16_f32 v216, v216, v217\n"
      "v_mfma_f32_16x16x32_bf16 v[126:129], a[68:71], v[212:215], v[126:129]\n"
      "v_mul_f32 v192, v180, v192\n"
      "v_mul_f32 v140, v108, v140\n"
      "v_mfma_f32_16x16x32_bf16 v[112:115], a[72:75], v[220:223], 0\n"
      "v_cvt_pk_bf16_f32 v100, v100, v101\n"
      "v_cvt_pk_bf16_f32 v184, v198, v199\n"
      "v_mfma_f32_16x16x32_bf16 v[112:115], a[76:79], v[144:147], v[112:115]\n"
      "v_fmac_f32 v157, v162, v162\n"
      "v_fmac_f32 v157, v163, v163\n"
      "v_mfma_f32_16x16x32_bf16 v[112:115], a[80:83], v[186:189], v[112:115]\n"
      "v_cvt_pk_bf16_f32 v185, v200, v201\n"
      "v_cmp_ne_u32 s[56:57], 0, v210\n"
      "v_mfma_f32_16x16x32_bf16 v[112:115], a[84:87], v[130:133], v[112:115]\n"
      "v_mul_f32 v102, v108, v102\n"
      "v_mul_f32 v143, v108, v143\n"
      "v_mfma_f32_16x16x32_bf16 v[112:115], a[88:91], v[172:175], v[112:115]\n"
      "v_mul_f32_e64 v120, v108, v120\n"
      "v_cvt_pk_bf16_f32 v217, v218, v219\n"
      "v_mfma_f32_16x16x32_bf16 v[112:115], a[92:95], v[212:215], v[112:115]\n"
      "v_mul_f32 v190, v180, v190\n"
      "v_fmac_f32 v157, v164, v164\n"
      "v_mfma_f32_16x16x32_bf16 v[150:153], a[96:99], v[220:223], 0\n"
      "v_cvt_pk_bf16_f32 v138, v122, v123\n"
      "v_cvt_pk_bf16_f32 v107, v178, v179\n"
      "v_mfma_f32_16x16x32_bf16 v[150:153], a[100:103], v[144:147], v[150:153]\n"
      "v_fmac_f32 v157, v165, v165\n"
      "v_mul_f32 v103, v108, v103\n"
      "v_mfma_f32_16x16x32_bf16 v[150:153], a[104:107], v[186:189], v[150:153]\n"
      "s_and_b32 s47, s47, -4\n"
      "v_fmac_f32 v157, v126, v126\n"
      "v_fma_f32 v210, v112, v112, s51\n"
      "v_mfma_f32_16x16x32_bf16 v[150:153], a[108:111], v[130:133], v[150:153]\n"
      "v_mul_f32 v142, v108, v142\n"
      "v_cvt_pk_bf16_f32 v118, v118, v119\n"
      "v_mfma_f32_16x16x32_bf16 v[150:153], a[112:115], v[172:175], v[150:153]\n"
      "v_mul_f32 v141, v108, v141\n"
      "v_fmac_f32_e64 v210, v113, v113\n"
      "v_mfma_f32_16x16x32_bf16 v[150:153], a[116:119], v[212:215], v[150:153]\n"
      "v_fmac_f32 v210, v114, v114\n"
      "v_cvt_pk_bf16_f32 v101, v102, v103\n"
      "v_mfma_f32_16x16x32_bf16 v[166:169], a[120:123], v[220:223], 0\n"
      "v_fmac_f32 v157, v127, v127\n"
      "v_cvt_pk_bf16_f32 v103, v142, v143\n"
      "v_mfma_f32_16x16x32_bf16 v[166:169], a[124:127], v[144:147], v[166:169]\n"
      "v_cvt_pk_bf16_f32 v102, v140, v141\n"
      "v_fmac_f32_e64 v157, v128, v128\n"
      "v_mfma_f32_16x16x32_bf16 v[166:169], a[128:131], v[186:189], v[166:169]\n"
      "v_cvt_pk_bf16_f32 v139, v124, v125\n"
      "v_fmac_f32_e64 v210, v115, v115\n"
      "v_mfma_f32_16x16x32_bf16 v[166:169], a[132:135], v[130:133], v[166:169]\n"
      "v_cvt_pk_bf16_f32 v106, v176, v177\n"
      "v_fmac_f32 v210, v150, v150\n"
      "v_mfma_f32_16x16x32_bf16 v[166:169], a[136:139], v[172:175], v[166:169]\n"
      "v_cvt_pk_bf16_f32 v99, v170, v171\n"
      "v_fmac_f32 v210, v151, v151\n"
      "v_mfma_f32_16x16x32_bf16 v[166:169], a[140:143], v[212:215], v[166:169]\n"
      "v_fmac_f32 v157, v129, v129\n"
      "v_mul_f32 v121, v108, v121\n"
      "v_mfma_f32_16x16x32_bf16 v[140:143], v[220:223], a[144:147], 0\n"
      "v_fmac_f32 v210, v152, v152\n"
      "v_fmac_f32 v210, v153, v153\n"
      "v_mfma_f32_16x16x32_bf16 v[140:143], v[144:147], a[148:151], v[140:143]\n"
      "v_mul_f32 v193, v180, v193\n"
      "v_mul_f32 v209, v180, v209\n"
      "v_mfma_f32_16x16x32_bf16 v[140:143], v[186:189], a[152:155], v[140:143]\n"
      "ds_read_b128 v[178:181], v2 offset:3072\n"
      "v_cvt_pk_bf16_f32 v219, v192, v193\n"
      "v_mfma_f32_16x16x32_bf16 v[140:143], v[130:133], a[156:159], v[140:143]\n"
      "v_fmac_f32 v210, v166, v166\n"
      "v_fmac_f32 v210, v167, v167\n"
      "v_mfma_f32_16x16x32_bf16 v[140:143], v[172:175], a[160:163], v[140:143]\n"
      "v_cvt_pk_bf16_f32 v218, v190, v191\n"
      "v_fmac_f32 v210, v168, v168\n"
      "v_mfma_f32_16x16x32_bf16 v[140:143], v[212:215], a[164:167], v[140:143]\n"
      "v_cvt_pk_bf16_f32 v119, v120, v121\n"
      "v_fmac_f32 v210, v169, v169\n"
      "v_mfma_f32_16x16x32_bf16 v[198:201], v[220:223], a[168:171], 0\n"
      "s_nop 0\n"
      "v_permlane16_swap_b32 v157, v210\n"
      "v_mfma_f32_16x16x32_bf16 v[198:201], v[144:147], a[172:175], v[198:201]\n"
      "v_add_f32 v157, v157, v210\n"
      "v_mov_b32 v210, v157\n"
      "v_cvt_pk_bf16_f32 v76, v140, v141\n"
      "v_mfma_f32_16x16x32_bf16 v[198:201], v[186:189], a[176:179], v[198:201]\n"
      "v_permlane32_swap_b32_e64 v157, v210\n"
      "v_mfma_f32_16x16x32_bf16 v[198:201], v[130:133], a[180:183], v[198:201]\n"
      "ds_read_b128 v[120:123], v2\n"
      "v_add_f32 v157, v157, v210\n"
      "v_mfma_f32_16x16x32_bf16 v[198:201], v[172:175], a[184:187], v[198:201]\n"
      "v_rsq_f32 v157, v157\n"
      "v_mfma_f32_16x16x32_bf16 v[198:201], v[212:215], a[188:191], v[198:201]\n"
      "ds_read_u16 v82, v11 offset:1200\n"
      "v_mov_b32 v210, v157\n"
      "v_mfma_f32_16x16x32_bf16 v[190:193], v[220:223], a[192:195], 0\n"
      "s_nop 0\n"
      "v_permlane16_swap_b32_e64 v157, v210\n"
      "v_mfma_f32_16x16x32_bf16 v[190:193], v[144:147], a[196:199], v[190:193]\n"
      "ds_read_b128 v[108:111], v2 offset:6144\n"
      "v_mul_f32 v151, v210, v151\n"
      "v_mfma_f32_16x16x32_bf16 v[190:193], v[186:189], a[200:203], v[190:193]\n"
      "v_mul_f32 v112, v210, v112\n"
      "v_mul_f32 v115, v210, v115\n"
      "v_mfma_f32_16x16x32_bf16 v[190:193], v[130:133], a[204:207], v[190:193]\n"
      "ds_read_b128 v[186:189], v2 offset:9216\n"
      "v_mul_f32 v114, v210, v114\n"
      "v_mfma_f32_16x16x32_bf16 v[190:193], v[172:175], a[208:211], v[190:193]\n"
      "s_waitcnt lgkmcnt(3)\n"
      "v_cvt_pk_bf16_f32 v77, v142, v143\n"
      "v_mul_f32 v113, v210, v113\n"
      "v_mfma_f32_16x16x32_bf16 v[190:193], v[212:215], a[212:215], v[190:193]\n"
      "ds_read_u16 v78, v11 offset:1168\n"
      "v_mul_f32 v153, v210, v153\n"
      "v_mfma_f32_16x16x32_bf16 v[172:175], a[216:219], v[120:123], 0\n"
      "ds_read_b64 v[116:117], v10 offset:1192\n"
      "v_mul_f32 v169, v210, v169\n"
      "v_mfma_f32_16x16x32_bf16 v[172:175], a[220:223], v[178:181], v[172:175]\n"
      "s_waitcnt lgkmcnt(3)\n"
      "v_mul_f32 v166, v210, v166\n"
      "v_cvt_pk_bf16_f32 v207, v208, v209\n"
      "v_mfma_f32_16x16x32_bf16 v[172:175], a[224:227], v[108:111], v[172:175]\n"
      "ds_read_b128 v[132:135], v10 offset:1200\n"
      "v_cvt_pk_bf16_f32 v85, v192, v193\n"
      "v_mfma_f32_16x16x32_bf16 v[220:223], a[240:243], v[120:123], 0\n"
      "v_mul_f32 v152, v210, v152\n"
      "v_mul_f32 v167, v210, v167\n"
      "v_mfma_f32_16x16x32_bf16 v[220:223], a[244:247], v[178:181], v[220:223]\n"
      "v_cvt_pk_bf16_f32 v166, v166, v167\n"
      "v_cvt_pk_bf16_f32 v112, v112, v113\n"
      "v_mfma_f32_16x16x32_bf16 v[220:223], a[248:251], v[108:111], v[220:223]\n"
      "v_mul_f32 v150, v210, v150\n"
      "v_mul_f32 v157, s50, v157\n"
      "v_mfma_f32_16x16x32_bf16 v[140:143], v[232:235], v[120:123], 0\n"
      "v_mul_f32_e64 v164, v157, v164\n"
      "v_cvt_pk_bf16_f32 v113, v114, v115\n"
      "v_mfma_f32_16x16x32_bf16 v[140:143], v[236:239], v[178:181], v[140:143]\n"
      "v_cvt_pk_bf16_f32 v80, v198, v199\n"
      "v_mul_f32_e64 v168, v210, v168\n"
      "v_mfma_f32_16x16x32_bf16 v[140:143], v[240:243], v[108:111], v[140:143]\n"
      "v_cvt_pk_bf16_f32 v114, v150, v151\n"
      "v_cvt_pk_bf16_f32 v115, v152, v153\n"
      "v_mfma_f32_16x16x32_bf16 v[150:153], v[182:185], v[136:139], v[40:43]\n"
      "ds_read_b128 v[144:147], v2 offset:12288\n"
      "v_mul_f32_e64 v128, v157, v128\n"
      "v_mfma_f32_16x16x32_bf16 v[210:213], v[100:103], v[136:139], v[44:47]\n"
      "s_waitcnt lgkmcnt(1)\n"
      "v_cvt_pk_bf16_f32 v167, v168, v169\n"
      "v_mul_f32 v126, v157, v126\n"
      "v_mfma_f32_16x16x32_bf16 v[168:171], v[112:115], v[136:139], v[48:51]\n"
      "ds_read_b128 v[178:181], v2 offset:15360\n"
      "v_cvt_pk_bf16_f32 v81, v200, v201\n"
      "v_mfma_f32_16x16x32_bf16 v[120:123], v[132:135], v[136:139], v[24:27]\n"
      "v_mul_f32 v202, v157, v202\n"
      "v_mul_f32 v204, v157, v204\n"
      "v_mfma_f32_16x16x16_bf16 v[150:153], v[148:149], v[154:155], v[150:153]\n"
      "v_mul_f32 v127, v157, v127\n"
      "v_mul_f32 v165, v157, v165\n"
      "v_cvt_pk_bf16_f32 v84, v190, v191\n"
      "v_mfma_f32_16x16x16_bf16 v[210:213], v[118:119], v[154:155], v[210:213]\n"
      "v_mul_f32 v203, v157, v203\n"
      "v_cvt_pk_bf16_f32 v202, v202, v203\n"
      "v_mul_f32 v205, v157, v205\n"
      "v_mfma_f32_16x16x16_bf16 v[168:171], v[166:167], v[154:155], v[168:171]\n"
      "v_cvt_pk_bf16_f32 v203, v204, v205\n"
      "v_cvt_pk_bf16_f32 v126, v126, v127\n"
      "v_max3_f32 v139, v150, v151, v152\n"
      "v_mfma_f32_16x16x16_bf16 v[120:123], v[116:117], v[154:155], v[120:123]\n"
      "v_max3_f32 v201, v153, v210, v211\n"
      "v_mul_f32 v129, v157, v129\n"
      "v_cvt_pk_bf16_f32 v205, v164, v165\n"
      "v_mfma_f32_16x16x32_bf16 v[172:175], a[228:231], v[186:189], v[172:175]\n"
      "s_waitcnt lgkmcnt(0)\n"
      "v_max3_f32 v201, v201, v168, v169\n"
      "v_max3_f32 v139, v139, v212, v213\n"
      "v_mfma_f32_16x16x32_bf16 v[172:175], a[232:235], v[144:147], v[172:175]\n"
      "v_cvt_pk_bf16_f32 v127, v128, v129\n"
      "v_max3_f32 v139, v139, v170, v171\n"
      "v_mfma_f32_16x16x32_bf16 v[172:175], a[236:239], v[178:181], v[172:175]\n"
      "ds_read_b128 v[128:131], v2 offset:256\n"
      "v_max3_f32 v139, v139, v201, v120\n"
      "v_mfma_f32_16x16x32_bf16 v[220:223], a[252:255], v[186:189], v[220:223]\n"
      "ds_read_b128 v[198:201], v2 offset:6400\n"
      "v_mov_b32 v122, v139\n"
      "v_mfma_f32_16x16x32_bf16 v[220:223], v[224:227], v[144:147], v[220:223]\n"
      "s_nop 0\n"
      "v_permlane16_swap_b32_e64 v139, v122\n"
      "v_mfma_f32_16x16x32_bf16 v[220:223], v[228:231], v[178:181], v[220:223]\n"
      "v_max_f32 v139, v139, v122\n"
      "v_mov_b32 v122, v139\n"
      "v_mfma_f32_16x16x32_bf16 v[140:143], v[244:247], v[186:189], v[140:143]\n"
      "ds_read_b128 v[190:193], v2 offset:3328\n"
      "v_permlane32_swap_b32 v139, v122\n"
      "v_max_f32 v139, v139, v122\n"
      "v_sub_f32_e64 v120, v120, v139\n"
      "v_pk_add_f32 v[212:213], v[212:213], v[138:139] op_sel:[0,1] neg_lo:[0,1] neg_hi:[0,1]\n"
      "v_pk_add_f32 v[150:151], v[150:151], v[138:139] op_sel:[0,1] neg_lo:[0,1] neg_hi:[0,1]\n"
      "v_pk_add_f32 v[210:211], v[210:211], v[138:139] op_sel:[0,1] neg_lo:[0,1] neg_hi:[0,1]\n"
      "v_pk_add_f32 v[170:171], v[170:171], v[138:139] op_sel:[0,1] neg_lo:[0,1] neg_hi:[0,1]\n"
      "v_pk_mul_f32 v[162:163], v[162:163], v[156:157] op_sel:[0,1]\n"
      "v_pk_add_f32 v[168:169], v[168:169], v[138:139] op_sel:[0,1] neg_lo:[0,1] neg_hi:[0,1]\n"
      "v_pk_add_f32 v[152:153], v[152:153], v[138:139] op_sel:[0,1] neg_lo:[0,1] neg_hi:[0,1]\n"
      "v_mfma_f32_16x16x32_bf16 v[140:143], v[248:251], v[144:147], v[140:143]\n"
      "v_exp_f32 v168, v168\n"
      "v_mfma_f32_16x16x32_bf16 v[140:143], v[252:255], v[178:181], v[140:143]\n"
      "v_exp_f32 v170, v170\n"
      "v_mfma_f32_16x16x32_bf16 v[144:147], v[182:185], v[216:219], v[52:55]\n"
      "v_exp_f32 v152, v152\n"
      "v_mfma_f32_16x16x32_bf16 v[122:125], v[100:103], v[216:219], v[56:59]\n"
      "v_exp_f32 v169, v169\n"
      "v_mfma_f32_16x16x32_bf16 v[136:139], v[112:115], v[216:219], v[60:63]\n"
      "v_exp_f32 v171, v171\n"
      "v_mfma_f32_16x16x32_bf16 v[154:157], v[132:135], v[216:219], v[26:29]\n"
      "s_waitcnt lgkmcnt(0)\n"
      "v_exp_f32 v212, v212\n"
      "v_mfma_f32_16x16x16_bf16 v[144:147], v[148:149], v[206:207], v[144:147]\n"
      "v_exp_f32 v213, v213\n"
      "v_cvt_pk_bf16_f32 v204, v162, v163\n"
      "v_mfma_f32_16x16x16_bf16 v[122:125], v[118:119], v[206:207], v[122:125]\n"
      "v_exp_f32_e64 v153, v153\n"
      "v_cvt_pk_bf16_f32 v88, v168, v169\n"
      "v_mfma_f32_16x16x16_bf16 v[136:139], v[166:167], v[206:207], v[136:139]\n"
      "v_exp_f32 v151, v151\n"
      "v_exp_f32 v150, v150\n"
      "v_mfma_f32_16x16x16_bf16 v[154:157], v[116:117], v[206:207], v[154:157]\n"
      "v_cvt_pk_bf16_f32 v150, v150, v151\n"
      "v_exp_f32_e64 v210, v210\n"
      "v_mfma_f32_16x16x32_bf16 v[206:209], a[216:219], v[128:131], 0\n"
      "v_max3_f32 v216, v144, v145, v146\n"
      "v_max3_f32 v169, v147, v122, v123\n"
      "v_mfma_f32_16x16x32_bf16 v[206:209], a[220:223], v[190:193], v[206:209]\n"
      "v_max3_f32 v169, v169, v136, v137\n"
      "v_max3_f32 v216, v216, v124, v125\n"
      "v_mfma_f32_16x16x32_bf16 v[206:209], a[224:227], v[198:201], v[206:209]\n"
      "v_exp_f32_e64 v120, v120\n"
      "v_mfma_f32_16x16x32_bf16 v[176:179], a[240:243], v[128:131], 0\n"
      "v_cvt_pk_bf16_f32 v89, v170, v171\n"
      "v_max3_f32 v216, v216, v138, v139\n"
      "v_mfma_f32_16x16x32_bf16 v[176:179], a[244:247], v[190:193], v[176:179]\n"
      "v_cvt_pk_bf16_f32 v151, v152, v153\n"
      "v_cvt_pk_bf16_f32 v153, v212, v213\n"
      "v_mfma_f32_16x16x32_bf16 v[176:179], a[248:251], v[198:201], v[176:179]\n"
      "v_cvt_pk_bf16_f32 v90, v120, 0\n"
      "v_max3_f32 v216, v216, v169, v154\n"
      "v_mfma_f32_16x16x32_bf16 v[108:111], v[232:235], v[128:131], 0\n"
      "v_exp_f32_e64 v211, v211\n"
      "v_mfma_f32_16x16x32_bf16 v[108:111], v[236:239], v[190:193], v[108:111]\n"
      "v_mov_b32 v212, v216\n"
      "v_cvt_pk_bf16_f32 v152, v210, v211\n"
      "v_mfma_f32_16x16x32_bf16 v[108:111], v[240:243], v[198:201], v[108:111]\n"
      "v_permlane16_swap_b32 v216, v212\n"
      "v_mfma_f32_16x16x32_bf16 v[190:193], v[32:35], v[150:153], 0\n"
      "ds_read_b128 v[168:171], v2 offset:9472\n"
      "v_max_f32 v216, v216, v212\n"
      "v_mfma_f32_16x16x32_bf16 v[190:193], v[32:35], v[88:91], v[190:193]\n"
      "v_mov_b32 v212, v216\n"
      "s_waitcnt vmcnt(1)\n"
      "v_or3_b32 v158, v158, v159, v160\n"
      "v_mfma_f32_16x16x32_bf16 v[128:131], v[194:197], v[150:153], 0\n"
      "v_mul_f32 v157, v173, v173\n"
      "v_fma_f32 v156, v172, v172, s52\n"
      "v_mfma_f32_16x16x32_bf16 v[128:131], v[76:79], v[88:91], v[128:131]\n"
      "v_permlane32_swap_b32 v216, v212\n"
      "v_mfma_f32_16x16x32_bf16 v[198:201], v[104:107], v[150:153], 0\n"
      "v_max_f32 v216, v216, v212\n"
      "v_sub_f32 v154, v154, v216\n"
      "s_waitcnt vmcnt(0)\n"
      "v_or3_b32 v158, v158, v161, v93\n"
      "v_rcp_f32_e64 v190, v190\n"
      "v_pk_add_f32 v[138:139], v[138:139], v[216:217] op_sel_hi:[1,0] neg_lo:[0,1] neg_hi:[0,1]\n"
      "v_pk_add_f32 v[144:145], v[144:145], v[216:217] op_sel_hi:[1,0] neg_lo:[0,1] neg_hi:[0,1]\n"
      "v_pk_mul_f32 v[128:129], v[128:129], v[190:191] op_sel_hi:[1,0]\n"
      "v_pk_add_f32 v[122:123], v[122:123], v[216:217] op_sel_hi:[1,0] neg_lo:[0,1] neg_hi:[0,1]\n"
      "v_pk_add_f32 v[146:147], v[146:147], v[216:217] op_sel_hi:[1,0] neg_lo:[0,1] neg_hi:[0,1]\n"
      "v_pk_mul_f32 v[130:131], v[130:131], v[190:191] op_sel_hi:[1,0]\n"
      "v_exp_f32 v122, v122\n"
      "v_mfma_f32_16x16x32_bf16 v[198:201], v[80:83], v[88:91], v[198:201]\n"
      "v_exp_f32 v138, v138\n"
      "v_mfma_f32_16x16x32_bf16 v[210:213], v[96:99], v[150:153], 0\n"
      "v_exp_f32_e64 v139, v139\n"
      "v_mfma_f32_16x16x32_bf16 v[210:213], v[84:87], v[88:91], v[210:213]\n"
      "v_cvt_pk_bf16_f32 v128, v128, v129\n"
      "v_cvt_pk_bf16_f32 v93, v138, v139\n"
      "v_exp_f32_e64 v144, v144\n"
      "v_cvt_pk_bf16_f32 v129, v130, v131\n"
      "v_pk_fma_f32 v[156:157], v[174:175], v[174:175], v[156:157]\n"
      "v_pk_add_f32 v[136:137], v[136:137], v[216:217] op_sel_hi:[1,0] neg_lo:[0,1] neg_hi:[0,1]\n"
      "v_pk_mul_f32 v[198:199], v[198:199], v[190:191] op_sel_hi:[1,0]\n"
      "v_pk_add_f32 v[124:125], v[124:125], v[216:217] op_sel_hi:[1,0] neg_lo:[0,1] neg_hi:[0,1]\n"
      "v_pk_mul_f32 v[212:213], v[212:213], v[190:191] op_sel_hi:[1,0]\n"
      "v_pk_fma_f32 v[156:157], v[220:221], v[220:221], v[156:157]\n"
      "v_pk_mul_f32 v[200:201], v[200:201], v[190:191] op_sel_hi:[1,0]\n"
      "v_pk_mul_f32 v[210:211], v[210:211], v[190:191] op_sel_hi:[1,0]\n"
      "v_cvt_pk_bf16_f32 v130, v198, v199\n"
      "v_cvt_pk_bf16_f32 v131, v200, v201\n"
      "v_mfma_f32_16x16x32_bf16 v[216:219], v[182:185], v[202:205], v[64:67]\n"
      "v_exp_f32 v125, v125\n"
      "v_mfma_f32_16x16x32_bf16 v[186:189], v[100:103], v[202:205], v[68:71]\n"
      "s_waitcnt lgkmcnt(0)\n"
      "v_exp_f32_e64 v146, v146\n"
      "v_mfma_f32_16x16x32_bf16 v[190:193], v[112:115], v[202:205], v[72:75]\n"
      "ds_read_b128 v[160:163], v2 offset:12544\n"
      "v_cvt_pk_bf16_f32 v210, v210, v211\n"
      "v_mfma_f32_16x16x32_bf16 v[150:153], v[132:135], v[202:205], v[28:31]\n"
      "ds_read_b128 v[198:201], v2 offset:15616\n"
      "v_exp_f32 v147, v147\n"
      "v_mfma_f32_16x16x16_bf16 v[216:219], v[148:149], v[126:127], v[216:219]\n"
      "v_cvt_pk_bf16_f32 v211, v212, v213\n"
      "v_exp_f32 v145, v145\n"
      "v_mfma_f32_16x16x16_bf16 v[186:189], v[118:119], v[126:127], v[186:189]\n"
      "v_cvt_pk_bf16_f32 v144, v144, v145\n"
      "v_exp_f32 v123, v123\n"
      "v_mfma_f32_16x16x16_bf16 v[190:193], v[166:167], v[126:127], v[190:193]\n"
      "v_cvt_pk_bf16_f32 v145, v146, v147\n"
      "v_exp_f32 v154, v154\n"
      "v_mfma_f32_16x16x16_bf16 v[150:153], v[116:117], v[126:127], v[150:153]\n"
      "v_cvt_pk_bf16_f32 v146, v122, v123\n"
      "v_exp_f32 v124, v124\n"
      "v_mfma_f32_16x16x32_bf16 v[206:209], a[228:231], v[168:171], v[206:209]\n"
      "s_waitcnt lgkmcnt(1)\n"
      "v_exp_f32 v137, v137\n"
      "v_mfma_f32_16x16x32_bf16 v[206:209], a[232:235], v[160:163], v[206:209]\n"
      "s_waitcnt lgkmcnt(0)\n"
      "v_max3_f32 v90, v216, v217, v218\n"
      "v_cvt_pk_bf16_f32 v94, v154, 0\n"
      "v_mfma_f32_16x16x32_bf16 v[206:209], a[236:239], v[198:201], v[206:209]\n"
      "ds_write_b128 v3, v[128:131] offset:18432\n"
      "v_mfma_f32_16x16x32_bf16 v[176:179], a[252:255], v[168:171], v[176:179]\n"
      "v_cvt_pk_bf16_f32 v147, v124, v125\n"
      "v_max3_f32 v125, v219, v186, v187\n"
      "v_mfma_f32_16x16x32_bf16 v[176:179], v[224:227], v[160:163], v[176:179]\n"
      "ds_read_b64 v[88:89], v10 offset:1184\n"
      "v_max3_f32 v90, v90, v188, v189\n"
      "v_mfma_f32_16x16x32_bf16 v[176:179], v[228:231], v[198:201], v[176:179]\n"
      "v_max3_f32 v125, v125, v190, v191\n"
      "v_max3_f32 v90, v90, v192, v193\n"
      "v_mfma_f32_16x16x32_bf16 v[108:111], v[244:247], v[168:171], v[108:111]\n"
      "v_exp_f32 v136, v136\n"
      "v_mfma_f32_16x16x32_bf16 v[108:111], v[248:251], v[160:163], v[108:111]\n"
      "ds_read_b128 v[152:155], v10 offset:1168\n"
      "v_max3_f32 v90, v90, v125, v150\n"
      "v_mfma_f32_16x16x32_bf16 v[108:111], v[252:255], v[198:201], v[108:111]\n"
      "ds_read_b128 v[198:201], v7\n"
      "v_cvt_pk_bf16_f32 v92, v136, v137\n"
      "v_mfma_f32_16x16x32_bf16 v[160:163], v[32:35], v[144:147], 0\n"
      "v_mul_f32 v125, v207, v207\n"
      "v_mov_b32 v136, v90\n"
      "v_mfma_f32_16x16x32_bf16 v[160:163], v[32:35], v[92:95], v[160:163]\n"
      "s_and_b32 s4, s45, s8\n"
      "v_permlane16_swap_b32 v90, v136\n"
      "v_mfma_f32_16x16x32_bf16 v[212:215], v[194:197], v[144:147], 0\n"
      "s_add_u32 s42, s42, 1\n"
      "v_max_f32 v90, v90, v136\n"
      "v_mov_b32 v136, v90\n"
      "v_mfma_f32_16x16x32_bf16 v[212:215], v[76:79], v[92:95], v[212:215]\n"
      "s_nop 0\n"
      "v_rcp_f32 v160, v160\n"
      "v_mfma_f32_16x16x32_bf16 v[120:123], v[104:107], v[144:147], 0\n"
      "v_permlane32_swap_b32 v90, v136\n"
      "v_mfma_f32_16x16x32_bf16 v[120:123], v[80:83], v[92:95], v[120:123]\n"
      "v_max_f32 v90, v90, v136\n"
      "v_sub_f32 v150, v150, v90\n"
      "v_mfma_f32_16x16x32_bf16 v[136:139], v[96:99], v[144:147], 0\n"
      "s_waitcnt lgkmcnt(0)\n"
      "v_exp_f32_e64 v150, v150\n"
      "v_mfma_f32_16x16x32_bf16 v[136:139], v[84:87], v[92:95], v[136:139]\n"
      "buffer_store_dwordx4 v[198:201], v20, s[24:27], s85 offen sc1\n"
      "v_fma_f32 v124, v206, v206, s52\n"
      "v_pk_mul_f32 v[212:213], v[212:213], v[160:161] op_sel_hi:[1,0]\n"
      "v_pk_add_f32 v[192:193], v[192:193], v[90:91] op_sel_hi:[1,0] neg_lo:[0,1] neg_hi:[0,1]\n"
      "v_pk_add_f32 v[218:219], v[218:219], v[90:91] op_sel_hi:[1,0] neg_lo:[0,1] neg_hi:[0,1]\n"
      "v_pk_fma_f32 v[156:157], v[222:223], v[222:223], v[156:157]\n"
      "v_pk_fma_f32 v[124:125], v[208:209], v[208:209], v[124:125]\n"
      "v_pk_add_f32 v[186:187], v[186:187], v[90:91] op_sel_hi:[1,0] neg_lo:[0,1] neg_hi:[0,1]\n"
      "v_pk_mul_f32 v[214:215], v[214:215], v[160:161] op_sel_hi:[1,0]\n"
      "v_pk_fma_f32 v[124:125], v[176:177], v[176:177], v[124:125]\n"
      "v_pk_mul_f32 v[138:139], v[138:139], v[160:161] op_sel_hi:[1,0]\n"
      "v_pk_add_f32 v[190:191], v[190:191], v[90:91] op_sel_hi:[1,0] neg_lo:[0,1] neg_hi:[0,1]\n"
      "v_pk_mul_f32 v[136:137], v[136:137], v[160:161] op_sel_hi:[1,0]\n"
      "v_pk_mul_f32 v[122:123], v[122:123], v[160:161] op_sel_hi:[1,0]\n"
      "v_pk_add_f32 v[188:189], v[188:189], v[90:91] op_sel_hi:[1,0] neg_lo:[0,1] neg_hi:[0,1]\n"
      "v_pk_add_f32 v[216:217], v[216:217], v[90:91] op_sel_hi:[1,0] neg_lo:[0,1] neg_hi:[0,1]\n"
      "v_cvt_pk_bf16_f32 v212, v212, v213\n"
      "v_pk_mul_f32 v[120:121], v[120:121], v[160:161] op_sel_hi:[1,0]\n"
      "v_mfma_f32_16x16x32_bf16 v[160:163], v[152:155], v[182:185], v[30:33]\n"
      "ds_read_b128 v[144:147], v2 offset:3584\n"
      "v_cvt_pk_bf16_f32 v213, v214, v215\n"
      "v_mfma_f32_16x16x32_bf16 v[182:185], v[152:155], v[100:103], v[30:33]\n"
      "ds_read_b128 v[128:131], v2 offset:6656\n"
      "v_cvt_pk_bf16_f32 v214, v120, v121\n"
      "v_mfma_f32_16x16x32_bf16 v[168:171], v[152:155], v[112:115], v[30:33]\n"
      "ds_read_b128 v[100:103], v2 offset:512\n"
      "v_exp_f32_e64 v189, v189\n"
      "v_mfma_f32_16x16x32_bf16 v[198:201], v[152:155], v[132:135], v[30:33]\n"
      "ds_read_b128 v[152:155], v7 offset:4096\n"
      "v_cvt_pk_bf16_f32 v215, v122, v123\n"
      "v_mfma_f32_16x16x16_bf16 v[160:163], v[88:89], v[148:149], v[160:163]\n"
      "v_exp_f32 v216, v216\n"
      "v_exp_f32 v192, v192\n"
      "v_mfma_f32_16x16x16_bf16 v[182:185], v[88:89], v[118:119], v[182:185]\n"
      "v_exp_f32 v219, v219\n"
      "v_exp_f32 v193, v193\n"
      "v_mfma_f32_16x16x16_bf16 v[168:171], v[88:89], v[166:167], v[168:171]\n"
      "v_exp_f32 v217, v217\n"
      "v_exp_f32 v186, v186\n"
      "v_exp_f32 v218, v218\n"
      "v_pk_fma_f32 v[156:157], v[140:141], v[140:141], v[156:157]\n"
      "v_mfma_f32_16x16x16_bf16 v[198:201], v[88:89], v[116:117], v[198:201]\n"
      "v_cndmask_b32 v160, v160, v182, s[58:59]\n"
      "v_cvt_pk_bf16_f32 v90, v150, 0\n"
      "s_waitcnt lgkmcnt(0)\n"
      "v_mfma_f32_16x16x32_bf16 v[132:135], a[216:219], v[100:103], 0\n"
      "ds_read_b128 v[182:185], v2 offset:12800\n"
      "v_cndmask_b32 v160, v160, v168, s[60:61]\n"
      "v_mfma_f32_16x16x32_bf16 v[132:135], a[220:223], v[144:147], v[132:135]\n"
      "buffer_store_dwordx4 v[152:155], v21, s[24:27], s85 offen sc1\n"
      "v_mfma_f32_16x16x32_bf16 v[132:135], a[224:227], v[128:131], v[132:135]\n"
      "ds_read_b128 v[162:165], v2 offset:9728\n"
      "v_cndmask_b32 v160, v160, v198, s[62:63]\n"
      "v_mfma_f32_16x16x32_bf16 v[118:121], a[240:243], v[100:103], 0\n"
      "ds_read_b128 v[150:153], v2 offset:15872\n"
      "v_exp_f32 v190, v190\n"
      "v_mfma_f32_16x16x32_bf16 v[118:121], a[244:247], v[144:147], v[118:121]\n"
      "ds_read_b128 v[198:201], v7 offset:8192\n"
      "v_exp_f32 v191, v191\n"
      "v_max_f32_dpp v155, v160, v160 row_ror:8 row_mask:0xf bank_mask:0xf\n"
      "v_mfma_f32_16x16x32_bf16 v[118:121], a[248:251], v[128:131], v[118:121]\n"
      "ds_write_b64 v4, v[210:211] offset:18432\n"
      "v_max_f32_dpp v155, v155, v155 row_ror:4 row_mask:0xf bank_mask:0xf\n"
      "v_mfma_f32_16x16x32_bf16 v[168:171], v[232:235], v[100:103], 0\n"
      "ds_read_b128 v[100:103], v7 offset:12288\n"
      "v_cvt_pk_bf16_f32 v89, v192, v193\n"
      "v_mfma_f32_16x16x32_bf16 v[168:171], v[236:239], v[144:147], v[168:171]\n"
      "v_max_f32_dpp v155, v155, v155 row_ror:2 row_mask:0xf bank_mask:0xf\n"
      "v_cvt_pk_bf16_f32 v88, v190, v191\n"
      "v_mfma_f32_16x16x32_bf16 v[168:171], v[240:243], v[128:131], v[168:171]\n"
      "s_waitcnt lgkmcnt(3)\n"
      "v_exp_f32 v187, v187\n"
      "v_mfma_f32_16x16x32_bf16 v[132:135], a[228:231], v[162:165], v[132:135]\n"
      "v_exp_f32 v188, v188\n"
      "v_mfma_f32_16x16x32_bf16 v[132:135], a[232:235], v[182:185], v[132:135]\n"
      "s_waitcnt lgkmcnt(2)\n"
      "v_cvt_pk_bf16_f32 v216, v216, v217\n"
      "v_max_f32_dpp v155, v155, v155 row_ror:1 row_mask:0xf bank_mask:0xf\n"
      "v_mfma_f32_16x16x32_bf16 v[132:135], a[236:239], v[150:153], v[132:135]\n"
      "ds_read_b128 v[190:193], v7 offset:16384\n"
      "v_max_f32_dpp v155, v155, v155 row_bcast:15 row_mask:0xa bank_mask:0xf\n"
      "v_mfma_f32_16x16x32_bf16 v[118:121], a[252:255], v[162:165], v[118:121]\n"
      "s_nop 0\n"
      "v_max_f32_dpp v155, v155, v155 row_bcast:31 row_mask:0xc bank_mask:0xf\n"
      "v_cvt_pk_bf16_f32 v217, v218, v219\n"
      "v_mfma_f32_16x16x32_bf16 v[118:121], v[224:227], v[182:185], v[118:121]\n"
      "buffer_store_dwordx4 v[198:201], v22, s[24:27], s85 offen sc1\n"
      "v_mfma_f32_16x16x32_bf16 v[118:121], v[228:231], v[150:153], v[118:121]\n"
      "v_readlane_b32 s72, v155, 63\n"
      "v_mul_f32 v155, v133, v133\n"
      "v_mfma_f32_16x16x32_bf16 v[168:171], v[244:247], v[162:165], v[168:171]\n"
      "ds_write_b128 v3, v[212:215] offset:18688\n"
      "v_subrev_f32 v160, s72, v160\n"
      "v_fma_f32 v154, v132, v132, s52\n"
      "v_pk_fma_f32 v[124:125], v[178:179], v[178:179], v[124:125]\n"
      "v_pk_fma_f32 v[124:125], v[108:109], v[108:109], v[124:125]\n"
      "v_exp_f32_e64 v160, v160\n"
      "v_pk_fma_f32 v[154:155], v[134:135], v[134:135], v[154:155]\n"
      "v_pk_fma_f32 v[156:157], v[142:143], v[142:143], v[156:157]\n"
      "v_cvt_pk_bf16_f32 v218, v186, v187\n"
      "v_mfma_f32_16x16x32_bf16 v[168:171], v[248:251], v[182:185], v[168:171]\n"
      "s_waitcnt lgkmcnt(1)\n"
      "v_cvt_pk_bf16_f32 v219, v188, v189\n"
      "v_cvt_pk_bf16_f32 v160, v160, v160\n"
      "v_mfma_f32_16x16x32_bf16 v[168:171], v[252:255], v[150:153], v[168:171]\n"
      "ds_write_b16 v13, v160\n"
      "v_mfma_f32_16x16x32_bf16 v[162:165], v[32:35], v[216:219], 0\n"
      "buffer_store_dwordx4 v[100:103], v23, s[24:27], s85 offen sc1\n"
      "v_pk_fma_f32 v[124:125], v[110:111], v[110:111], v[124:125]\n"
      "v_mfma_f32_16x16x32_bf16 v[162:165], v[32:35], v[88:91], v[162:165]\n"
      "v_add_f32 v124, v124, v125\n"
      "v_add_f32 v156, v156, v157\n"
      "v_mfma_f32_16x16x32_bf16 v[186:189], v[194:197], v[216:219], 0\n"
      "ds_read_b128 v[144:147], v14\n"
      "v_cvt_pk_bf16_f32 v136, v136, v137\n"
      "v_mfma_f32_16x16x32_bf16 v[186:189], v[76:79], v[88:91], v[186:189]\n"
      "ds_write_b32 v31, v158 offset:5632\n"
      "v_mfma_f32_16x16x32_bf16 v[150:153], v[104:107], v[216:219], 0\n"
      "v_rcp_f32_e64 v162, v162\n"
      "v_mfma_f32_16x16x32_bf16 v[150:153], v[80:83], v[88:91], v[150:153]\n"
      "ds_read_b128 v[198:201], v14 offset:64\n"
      "v_permlane16_swap_b32 v156, v124\n"
      "v_cvt_pk_bf16_f32 v137, v138, v139\n"
      "v_add_f32 v156, v156, v124\n"
      "v_pk_fma_f32 v[154:155], v[118:119], v[118:119], v[154:155]\n"
      "v_pk_mul_f32 v[186:187], v[186:187], v[162:163] op_sel_hi:[1,0]\n"
      "v_pk_fma_f32 v[154:155], v[120:121], v[120:121], v[154:155]\n"
      "v_pk_fma_f32 v[154:155], v[168:169], v[168:169], v[154:155]\n"
      "v_pk_fma_f32 v[154:155], v[170:171], v[170:171], v[154:155]\n"
      "v_mfma_f32_16x16x32_bf16 v[202:205], v[96:99], v[216:219], 0\n"
      "ds_write_b64 v4, v[136:137] offset:18688\n"
      "v_cvt_pk_bf16_f32 v186, v186, v187\n"
      "v_add_f32_e64 v154, v154, v155\n"
      "v_pk_mul_f32 v[150:151], v[150:151], v[162:163] op_sel_hi:[1,0]\n"
      "v_pk_mul_f32 v[152:153], v[152:153], v[162:163] op_sel_hi:[1,0]\n"
      "v_pk_mul_f32 v[188:189], v[188:189], v[162:163] op_sel_hi:[1,0]\n"
      "v_mfma_f32_16x16x32_bf16 v[202:205], v[84:87], v[88:91], v[202:205]\n"
      "s_waitcnt lgkmcnt(2)\n"
      "v_cvt_pk_bf16_f32 v187, v188, v189\n"
      "v_mov_b32 v124, v154\n"
      "v_cvt_pk_bf16_f32 v189, v152, v153\n"
      "v_cvt_pk_bf16_f32 v188, v150, v151\n"
      "s_add_u32 s43, s43, 1\n"
      "ds_write_b128 v3, v[186:189] offset:18944\n"
      "v_permlane16_swap_b32 v154, v124\n"
      "v_add_f32_e64 v154, v154, v124\n"
      "v_pk_mul_f32 v[202:203], v[202:203], v[162:163] op_sel_hi:[1,0]\n"
      "v_pk_mul_f32 v[204:205], v[204:205], v[162:163] op_sel_hi:[1,0]\n"
      "v_permlane32_swap_b32 v156, v154\n"
      "v_add_f32 v156, v156, v154\n"
      "v_mfma_f32_16x16x32_bf16 v[162:165], v[144:147], v[32:35], 0\n"
      "ds_write_b32 v31, v156 offset:1024\n"
      "v_cvt_pk_bf16_f32 v202, v202, v203\n"
      "v_cvt_pk_bf16_f32 v203, v204, v205\n"
      "s_waitcnt lgkmcnt(2)\n"
      "v_mfma_f32_16x16x32_bf16 v[162:165], v[198:201], v[32:35], v[162:165]\n"
      "ds_write_b64 v4, v[202:203] offset:18944\n"
      "v_add_u32 v10, 2336, v10\n"
      "v_add_u32 v11, 2336, v11\n"
      "buffer_store_dwordx4 v[190:193], v25, s[24:27], s85 offen sc1\n"
      "s_cmp_ge_u32 s95, 0x40000000\n"
      "s_cselect_b64 vcc, -1, s[56:57]\n"
      "s_waitcnt lgkmcnt(0)\n"
      "s_barrier\n"
      "s_cbranch_vccnz L_disp0_%=\n"
      "L_max0_%=:\n"
      "v_mfma_f32_16x16x32_bf16 v[152:155], v[144:147], v[194:197], 0\n"
      "ds_read_b128 v[156:159], v0\n"
      "v_mfma_f32_16x16x32_bf16 v[152:155], v[198:201], v[76:79], v[152:155]\n"
      "ds_read_b128 v[148:151], v9 offset:1024\n"
      "v_readlane_b32 s85, v37, s42\n"
      "v_mfma_f32_16x16x32_bf16 v[210:213], v[144:147], v[104:107], 0\n"
      "s_add_u32 m0, s64, 18432\n"
      "ds_read_b128 v[202:205], v1 offset:12288\n"
      "v_mfma_f32_16x16x32_bf16 v[210:213], v[198:201], v[80:83], v[210:213]\n"
      "buffer_load_dwordx4 v16, s[16:19], s45 offen  lds\n"
      "v_mfma_f32_16x16x32_bf16 v[180:183], v[144:147], v[96:99], 0\n"
      "ds_read_b128 v[112:115], v1\n"
      "ds_read_b128 v[186:189], v0 offset:6144\n"
      "v_mfma_f32_16x16x32_bf16 v[180:183], v[198:201], v[84:87], v[180:183]\n"
      "ds_read_b128 v[144:147], v0 offset:12288\n"
      "s_waitcnt lgkmcnt(5)\n"
      "v_mfma_f32_16x16x32_bf16 v[190:193], a[0:3], v[156:159], 0\n"
      "ds_read_b128 v[128:131], v1 offset:6144\n"
      "s_add_u32 m0, s64, 22528\n"
      "v_mfma_f32_16x16x32_bf16 v[164:167], a[24:27], v[156:159], 0\n"
      "s_waitcnt lgkmcnt(5)\n"
      "v_add_f32 v150, v150, v151\n"
      "v_add_f32 v148, v148, v149\n"
      "v_mfma_f32_16x16x32_bf16 v[100:103], a[48:51], v[156:159], 0\n"
      "buffer_load_dwordx4 v17, s[16:19], s45 offen  lds\n"
      "v_mfma_f32_16x16x32_bf16 v[198:201], a[72:75], v[156:159], 0\n"
      "s_add_u32 m0, s64, 26624\n"
      "v_mov_b32_dpp v162, v162 quad_perm:[0,0,0,0] row_mask:0xf bank_mask:0xf\n"
      "v_add_f32 v148, v148, v150\n"
      "v_mfma_f32_16x16x32_bf16 v[216:219], a[96:99], v[156:159], 0\n"
      "v_rcp_f32 v162, v162\n"
      "v_mfma_f32_16x16x32_bf16 v[122:125], a[120:123], v[156:159], 0\n"
      "s_waitcnt lgkmcnt(2)\n"
      "v_rsq_f32 v148, v148\n"
      "v_mfma_f32_16x16x32_bf16 v[190:193], a[4:7], v[112:115], v[190:193]\n"
      "ds_read_b128 v[182:185], v0 offset:2048\n"
      "v_mul_f32 v148, 0x415db3d7, v148\n"
      "v_mfma_f32_16x16x32_bf16 v[190:193], a[8:11], v[186:189], v[190:193]\n"
      "v_readlane_b32 s84, v37, s43\n"
      "v_mov_b32_e64 v150, v148\n"
      "v_mfma_f32_16x16x32_bf16 v[164:167], a[28:31], v[112:115], v[164:167]\n"
      "buffer_load_dwordx4 v18, s[16:19], s45 offen  lds\n"
      "v_mfma_f32_16x16x32_bf16 v[164:167], a[32:35], v[186:189], v[164:167]\n"
      "v_permlane32_swap_b32 v148, v150\n"
      "v_mfma_f32_16x16x32_bf16 v[100:103], a[52:55], v[112:115], v[100:103]\n"
      "v_cndmask_b32 v152, v152, v210, s[58:59]\n"
      "v_mov_b32 v149, v148\n"
      "v_mfma_f32_16x16x32_bf16 v[100:103], a[56:59], v[186:189], v[100:103]\n"
      "ds_read_b128 v[210:213], v9 offset:5632\n"
      "v_mul_f32_e64 v118, v150, v118\n"
      "v_mfma_f32_16x16x32_bf16 v[198:201], a[76:79], v[112:115], v[198:201]\n"
      "v_cndmask_b32 v152, v152, v180, s[60:61]\n"
      "v_mul_f32 v169, v150, v169\n"
      "v_mfma_f32_16x16x32_bf16 v[198:201], a[80:83], v[186:189], v[198:201]\n"
      "s_add_u32 m0, s64, 30720\n"
      "v_permlane16_swap_b32 v148, v149\n"
      "v_mfma_f32_16x16x32_bf16 v[216:219], a[100:103], v[112:115], v[216:219]\n"
      "v_mul_f32 v120, v150, v120\n"
      "v_mul_f32 v152, v162, v152\n"
      "v_mfma_f32_16x16x32_bf16 v[216:219], a[104:107], v[186:189], v[216:219]\n"
      "v_mul_f32 v143, v148, v143\n"
      "v_mul_f32 v174, v148, v174\n"
      "v_mfma_f32_16x16x32_bf16 v[122:125], a[124:127], v[112:115], v[122:125]\n"
      "s_waitcnt lgkmcnt(1)\n"
      "v_mul_f32 v173, v148, v173\n"
      "v_mul_f32_e64 v175, v148, v175\n"
      "v_mfma_f32_16x16x32_bf16 v[122:125], a[128:131], v[186:189], v[122:125]\n"
      "buffer_load_dwordx4 v19, s[16:19], s45 offen  lds\n"
      "v_mfma_f32_16x16x32_bf16 v[190:193], a[12:15], v[128:131], v[190:193]\n"
      "v_readlane_b32 s86, v38, s43\n"
      "v_mul_f32_e64 v223, v148, v223\n"
      "v_mfma_f32_16x16x32_bf16 v[190:193], a[16:19], v[144:147], v[190:193]\n"
      "v_mul_f32 v221, v148, v221\n"
      "v_mul_f32 v220, v148, v220\n"
      "v_mfma_f32_16x16x32_bf16 v[190:193], a[20:23], v[202:205], v[190:193]\n"
      "v_mul_f32 v140, v148, v140\n"
      "v_mul_f32 v141, v148, v141\n"
      "v_mfma_f32_16x16x32_bf16 v[164:167], a[36:39], v[128:131], v[164:167]\n"
      "s_mov_b32 m0, s7\n"
      "v_mul_f32 v172, v148, v172\n"
      "v_cvt_pk_bf16_f32 v172, v172, v173\n"
      "v_mfma_f32_16x16x32_bf16 v[164:167], a[40:43], v[144:147], v[164:167]\n"
      "s_nop 0\n"
      "v_fma_f32 v180, v190, v190, s51\n"
      "v_mul_f32 v208, v149, v208\n"
      "v_mfma_f32_16x16x32_bf16 v[164:167], a[44:47], v[202:205], v[164:167]\n"
      "ds_read_b128 v[160:163], v0 offset:8192\n"
      "v_cvt_pk_bf16_f32 v173, v174, v175\n"
      "v_mfma_f32_16x16x32_bf16 v[100:103], a[60:63], v[128:131], v[100:103]\n"
      "v_mul_f32 v222, v148, v222\n"
      "v_mul_f32 v142, v148, v142\n"
      "v_mfma_f32_16x16x32_bf16 v[100:103], a[64:67], v[144:147], v[100:103]\n"
      "buffer_load_dwordx4 v15, s[0:3], s4 offen  lds\n"
      "v_mfma_f32_16x16x32_bf16 v[100:103], a[68:71], v[202:205], v[100:103]\n"
      "s_or_b32 s95, s84, s85\n"
      "v_mul_f32 v133, v150, v133\n"
      "v_cvt_pk_bf16_f32 v174, v220, v221\n"
      "v_mfma_f32_16x16x32_bf16 v[198:201], a[84:87], v[128:131], v[198:201]\n"
      "v_cvt_pk_bf16_f32 v175, v222, v223\n"
      "v_mul_f32 v132, v150, v132\n"
      "v_mfma_f32_16x16x32_bf16 v[198:201], a[88:91], v[144:147], v[198:201]\n"
      "v_mul_f32 v108, v149, v108\n"
      "v_fmac_f32_e64 v180, v191, v191\n"
      "v_mfma_f32_16x16x32_bf16 v[198:201], a[92:95], v[202:205], v[198:201]\n"
      "ds_write_b128 v5, v[172:175]\n"
      "v_pk_mul_f32 v[110:111], v[110:111], v[148:149] op_sel:[0,1]\n"
      "v_mfma_f32_16x16x32_bf16 v[216:219], a[108:111], v[128:131], v[216:219]\n"
      "buffer_load_dwordx4 v[136:139], v36, s[28:31], s47 offen\n"
      "v_mfma_f32_16x16x32_bf16 v[216:219], a[112:115], v[144:147], v[216:219]\n"
      "v_mul_f32 v206, v149, v206\n"
      "v_mul_f32 v109, v149, v109\n"
      "v_mfma_f32_16x16x32_bf16 v[216:219], a[116:119], v[202:205], v[216:219]\n"
      "s_waitcnt lgkmcnt(1)\n"
      "v_mul_f32 v176, v149, v176\n"
      "v_or3_b32 v210, v210, v211, v212\n"
      "v_mfma_f32_16x16x32_bf16 v[122:125], a[132:135], v[128:131], v[122:125]\n"
      "v_fma_f32 v211, v198, v198, s51\n"
      "v_mul_f32 v168, v150, v168\n"
      "v_mfma_f32_16x16x32_bf16 v[122:125], a[136:139], v[144:147], v[122:125]\n"
      "v_fmac_f32 v211, v199, v199\n"
      "v_fmac_f32_e64 v180, v192, v192\n"
      "v_mfma_f32_16x16x32_bf16 v[122:125], a[140:143], v[202:205], v[122:125]\n"
      "ds_read_b128 v[172:175], v1 offset:2048\n"
      "v_fmac_f32 v211, v200, v200\n"
      "v_mfma_f32_16x16x32_bf16 v[194:197], v[156:159], a[144:147], 0\n"
      "v_fmac_f32 v211, v201, v201\n"
      "v_mul_f32_e64 v171, v150, v171\n"
      "v_mfma_f32_16x16x32_bf16 v[194:197], v[112:115], a[148:151], v[194:197]\n"
      "v_cvt_pk_bf16_f32 v108, v108, v109\n"
      "v_cvt_pk_bf16_f32 v132, v132, v133\n"
      "v_mfma_f32_16x16x32_bf16 v[194:197], v[186:189], a[152:155], v[194:197]\n"
      "v_fmac_f32 v211, v216, v216\n"
      "v_fmac_f32 v211, v217, v217\n"
      "v_mfma_f32_16x16x32_bf16 v[194:197], v[128:131], a[156:159], v[194:197]\n"
      "ds_read_b128 v[220:223], v1 offset:8192\n"
      "v_bitop3_b32 v210, v210, v213, s33 bitop3:0xa8\n"
      "v_mfma_f32_16x16x32_bf16 v[194:197], v[144:147], a[160:163], v[194:197]\n"
      "v_fmac_f32 v180, v193, v193\n"
      "v_mul_f32 v209, v149, v209\n"
      "v_mfma_f32_16x16x32_bf16 v[194:197], v[202:205], a[164:167], v[194:197]\n"
      "ds_read_b128 v[212:215], v0 offset:14336\n"
      "v_fmac_f32 v211, v218, v218\n"
      "v_mfma_f32_16x16x32_bf16 v[104:107], v[156:159], a[168:171], 0\n"
      "v_cvt_pk_bf16_f32 v140, v140, v141\n"
      "v_mul_f32 v179, v149, v179\n"
      "v_mfma_f32_16x16x32_bf16 v[104:107], v[112:115], a[172:175], v[104:107]\n"
      "v_cvt_pk_bf16_f32 v141, v142, v143\n"
      "v_fmac_f32 v211, v219, v219\n"
      "v_mfma_f32_16x16x32_bf16 v[104:107], v[186:189], a[176:179], v[104:107]\n"
      "v_fmac_f32 v211, v122, v122\n"
      "v_mul_f32 v135, v150, v135\n"
      "v_mfma_f32_16x16x32_bf16 v[104:107], v[128:131], a[180:183], v[104:107]\n"
      "v_mul_f32 v170, v150, v170\n"
      "v_fmac_f32 v180, v164, v164\n"
      "v_mfma_f32_16x16x32_bf16 v[104:107], v[144:147], a[184:187], v[104:107]\n"
      "v_fmac_f32 v211, v123, v123\n"
      "v_mul_f32 v177, v149, v177\n"
      "v_mfma_f32_16x16x32_bf16 v[104:107], v[202:205], a[188:191], v[104:107]\n"
      "v_fmac_f32 v180, v165, v165\n"
      "v_mul_f32_e64 v134, v150, v134\n"
      "v_mfma_f32_16x16x32_bf16 v[96:99], v[156:159], a[192:195], 0\n"
      "ds_read_b128 v[156:159], v1 offset:4096\n"
      "v_fmac_f32 v180, v166, v166\n"
      "v_mfma_f32_16x16x32_bf16 v[96:99], v[112:115], a[196:199], v[96:99]\n"
      "ds_read_b128 v[112:115], v1 offset:14336\n"
      "v_mul_f32 v178, v149, v178\n"
      "v_mfma_f32_16x16x32_bf16 v[96:99], v[186:189], a[200:203], v[96:99]\n"
      "s_or_b32 s95, s95, s48\n"
      "v_fmac_f32 v180, v167, v167\n"
      "v_cvt_pk_bf16_f32 v194, v194, v195\n"
      "v_mfma_f32_16x16x32_bf16 v[96:99], v[128:131], a[204:207], v[96:99]\n"
      "v_fmac_f32_e64 v180, v100, v100\n"
      "v_cvt_pk_bf16_f32 v195, v196, v197\n"
      "v_mfma_f32_16x16x32_bf16 v[96:99], v[144:147], a[208:211], v[96:99]\n"
      "buffer_load_dword v93, v29, s[28:31], s47 offen\n"
      "v_mfma_f32_16x16x32_bf16 v[96:99], v[202:205], a[212:215], v[96:99]\n"
      "v_cvt_pk_bf16_f32 v152, v152, v152\n"
      "v_mul_f32 v207, v149, v207\n"
      "v_mfma_f32_16x16x32_bf16 v[202:205], a[0:3], v[182:185], 0\n"
      "s_waitcnt lgkmcnt(1)\n"
      "v_cvt_pk_bf16_f32 v206, v206, v207\n"
      "v_fmac_f32_e64 v211, v124, v124\n"
      "v_mfma_f32_16x16x32_bf16 v[202:205], a[4:7], v[172:175], v[202:205]\n"
      "ds_write_b16 v12, v152 offset:16\n"
      "v_mfma_f32_16x16x32_bf16 v[202:205], a[8:11], v[160:163], v[202:205]\n"
      "v_cvt_pk_bf16_f32 v168, v168, v169\n"
      "v_fmac_f32_e64 v180, v101, v101\n"
      "v_mfma_f32_16x16x32_bf16 v[202:205], a[12:15], v[220:223], v[202:205]\n"
      "ds_write_b64 v6, v[140:141]\n"
      "v_mfma_f32_16x16x32_bf16 v[202:205], a[16:19], v[212:215], v[202:205]\n"
      "s_waitcnt lgkmcnt(2)\n"
      "v_fmac_f32 v211, v125, v125\n"
      "v_fmac_f32_e64 v180, v102, v102\n"
      "v_mfma_f32_16x16x32_bf16 v[202:205], a[20:23], v[112:115], v[202:205]\n"
      "v_mul_f32 v119, v150, v119\n"
      "v_cvt_pk_bf16_f32 v169, v170, v171\n"
      "v_mfma_f32_16x16x32_bf16 v[126:129], a[24:27], v[182:185], 0\n"
      "v_fmac_f32 v180, v103, v103\n"
      "v_cvt_pk_bf16_f32 v207, v208, v209\n"
      "v_mfma_f32_16x16x32_bf16 v[126:129], a[28:31], v[172:175], v[126:129]\n"
      "v_cvt_pk_bf16_f32 v96, v96, v97\n"
      "v_cvt_pk_bf16_f32 v133, v134, v135\n"
      "v_mfma_f32_16x16x32_bf16 v[126:129], a[32:35], v[160:163], v[126:129]\n"
      "v_permlane16_swap_b32 v180, v211\n"
      "v_mfma_f32_16x16x32_bf16 v[126:129], a[36:39], v[220:223], v[126:129]\n"
      "v_cvt_pk_bf16_f32 v104, v104, v105\n"
      "v_add_f32 v180, v180, v211\n"
      "v_mfma_f32_16x16x32_bf16 v[126:129], a[40:43], v[212:215], v[126:129]\n"
      "v_mov_b32 v211, v180\n"
      "v_cvt_pk_bf16_f32 v208, v176, v177\n"
      "v_mfma_f32_16x16x32_bf16 v[126:129], a[44:47], v[112:115], v[126:129]\n"
      "v_permlane32_swap_b32 v180, v211\n"
      "v_mfma_f32_16x16x32_bf16 v[144:147], a[48:51], v[182:185], 0\n"
      "v_cvt_pk_bf16_f32 v134, v118, v119\n"
      "v_cvt_pk_bf16_f32 v209, v178, v179\n"
      "v_mfma_f32_16x16x32_bf16 v[144:147], a[52:55], v[172:175], v[144:147]\n"
      "v_fma_f32 v131, v202, v202, s51\n"
      "v_add_f32 v180, v180, v211\n"
      "v_mfma_f32_16x16x32_bf16 v[144:147], a[56:59], v[160:163], v[144:147]\n"
      "v_rsq_f32 v180, v180\n"
      "v_mfma_f32_16x16x32_bf16 v[144:147], a[60:63], v[220:223], v[144:147]\n"
      "v_fmac_f32 v131, v203, v203\n"
      "v_mov_b32 v211, v180\n"
      "v_mfma_f32_16x16x32_bf16 v[144:147], a[64:67], v[212:215], v[144:147]\n"
      "v_mul_f32_e64 v121, v150, v121\n"
      "v_cvt_pk_bf16_f32 v105, v106, v107\n"
      "v_mfma_f32_16x16x32_bf16 v[144:147], a[68:71], v[112:115], v[144:147]\n"
      "v_permlane16_swap_b32_e64 v180, v211\n"
      "v_mfma_f32_16x16x32_bf16 v[148:151], a[72:75], v[182:185], 0\n"
      "ds_write_b128 v5, v[206:209] offset:6144\n"
      "v_mfma_f32_16x16x32_bf16 v[148:151], a[76:79], v[172:175], v[148:151]\n"
      "v_fmac_f32 v131, v204, v204\n"
      "v_fmac_f32 v131, v205, v205\n"
      "v_mfma_f32_16x16x32_bf16 v[148:151], a[80:83], v[160:163], v[148:151]\n"
      "ds_read_b128 v[206:209], v1 offset:10240\n"
      "v_mul_f32 v123, v211, v123\n"
      "v_mfma_f32_16x16x32_bf16 v[148:151], a[84:87], v[220:223], v[148:151]\n"
      "v_fmac_f32 v131, v126, v126\n"
      "v_cvt_pk_bf16_f32 v109, v110, v111\n"
      "v_mfma_f32_16x16x32_bf16 v[148:151], a[88:91], v[212:215], v[148:151]\n"
      "ds_read_b64 v[116:117], v10 offset:24\n"
      "v_mul_f32 v180, s50, v180\n"
      "v_mfma_f32_16x16x32_bf16 v[148:151], a[92:95], v[112:115], v[148:151]\n"
      "v_mul_f32 v165, v180, v165\n"
      "v_mul_f32_e64 v218, v211, v218\n"
      "v_mfma_f32_16x16x32_bf16 v[186:189], a[96:99], v[182:185], 0\n"
      "v_mul_f32_e64 v103, v180, v103\n"
      "v_cmp_ne_u32 s[56:57], 0, v210\n"
      "v_mfma_f32_16x16x32_bf16 v[186:189], a[100:103], v[172:175], v[186:189]\n"
      "ds_read_b128 v[140:143], v0 offset:10240\n"
      "v_mul_f32 v192, v180, v192\n"
      "v_mfma_f32_16x16x32_bf16 v[186:189], a[104:107], v[160:163], v[186:189]\n"
      "v_mul_f32 v219, v211, v219\n"
      "v_fma_f32 v130, v148, v148, s51\n"
      "v_mfma_f32_16x16x32_bf16 v[186:189], a[108:111], v[220:223], v[186:189]\n"
      "v_fmac_f32 v131, v127, v127\n"
      "v_fmac_f32 v131, v128, v128\n"
      "v_mfma_f32_16x16x32_bf16 v[186:189], a[112:115], v[212:215], v[186:189]\n"
      "v_fmac_f32 v131, v129, v129\n"
      "v_fmac_f32 v130, v149, v149\n"
      "v_mfma_f32_16x16x32_bf16 v[186:189], a[116:119], v[112:115], v[186:189]\n"
      "v_cvt_pk_bf16_f32 v97, v98, v99\n"
      "v_cvt_pk_bf16_f32 v135, v120, v121\n"
      "v_mfma_f32_16x16x32_bf16 v[152:155], a[120:123], v[182:185], 0\n"
      "ds_read_b128 v[118:121], v0 offset:4096\n"
      "v_mul_f32_e64 v100, v180, v100\n"
      "v_mfma_f32_16x16x32_bf16 v[152:155], a[124:127], v[172:175], v[152:155]\n"
      "ds_write_b64 v6, v[168:169] offset:12288\n"
      "v_mfma_f32_16x16x32_bf16 v[152:155], a[128:131], v[160:163], v[152:155]\n"
      "v_mul_f32 v101, v180, v101\n"
      "v_mul_f32 v124, v211, v124\n"
      "v_mfma_f32_16x16x32_bf16 v[152:155], a[132:135], v[220:223], v[152:155]\n"
      "v_mul_f32 v190, v180, v190\n"
      "v_fmac_f32 v131, v144, v144\n"
      "v_mfma_f32_16x16x32_bf16 v[152:155], a[136:139], v[212:215], v[152:155]\n"
      "v_fmac_f32 v131, v145, v145\n"
      "v_fmac_f32 v130, v150, v150\n"
      "v_mfma_f32_16x16x32_bf16 v[152:155], a[140:143], v[112:115], v[152:155]\n"
      "s_cmp_ge_u32 s95, 0x40000000\n"
      "s_cselect_b32 s94, -1, -2\n"
      "v_mul_f32 v102, v180, v102\n"
      "v_mfma_f32_16x16x32_bf16 v[168:171], v[182:185], a[144:147], 0\n"
      "v_mul_f32 v200, v211, v200\n"
      "v_mul_f32 v216, v211, v216\n"
      "v_mfma_f32_16x16x32_bf16 v[168:171], v[172:175], a[148:151], v[168:171]\n"
      "v_mul_f32 v191, v180, v191\n"
      "v_fmac_f32 v130, v151, v151\n"
      "v_mfma_f32_16x16x32_bf16 v[168:171], v[160:163], a[152:155], v[168:171]\n"
      "v_fmac_f32 v130, v186, v186\n"
      "v_mul_f32_e64 v217, v211, v217\n"
      "v_mfma_f32_16x16x32_bf16 v[168:171], v[220:223], a[156:159], v[168:171]\n"
      "ds_write_b64 v6, v[108:109] offset:6144\n"
      "v_mfma_f32_16x16x32_bf16 v[168:171], v[212:215], a[160:163], v[168:171]\n"
      "v_cvt_pk_bf16_f32 v190, v190, v191\n"
      "v_fmac_f32_e64 v130, v187, v187\n"
      "v_mfma_f32_16x16x32_bf16 v[168:171], v[112:115], a[164:167], v[168:171]\n"
      "v_fmac_f32 v130, v188, v188\n"
      "v_fmac_f32 v130, v189, v189\n"
      "v_mfma_f32_16x16x32_bf16 v[176:179], v[182:185], a[168:171], 0\n"
      "v_mul_f32 v198, v211, v198\n"
      "v_mul_f32 v199, v211, v199\n"
      "v_mfma_f32_16x16x32_bf16 v[176:179], v[172:175], a[172:175], v[176:179]\n"
      "v_mul_f32 v201, v211, v201\n"
      "v_mul_f32 v122, v211, v122\n"
      "v_mfma_f32_16x16x32_bf16 v[176:179], v[160:163], a[176:179], v[176:179]\n"
      "v_fmac_f32 v131, v146, v146\n"
      "v_fmac_f32 v130, v152, v152\n"
      "v_mfma_f32_16x16x32_bf16 v[176:179], v[220:223], a[180:183], v[176:179]\n"
      "v_mul_f32 v166, v180, v166\n"
      "v_fmac_f32 v130, v153, v153\n"
      "v_mfma_f32_16x16x32_bf16 v[176:179], v[212:215], a[184:187], v[176:179]\n"
      "ds_read_b128 v[108:111], v1 offset:16384\n"
      "v_mul_f32_e64 v125, v211, v125\n"
      "v_mfma_f32_16x16x32_bf16 v[176:179], v[112:115], a[188:191], v[176:179]\n"
      "ds_write_b128 v5, v[132:135] offset:12288\n"
      "v_mfma_f32_16x16x32_bf16 v[132:135], v[182:185], a[192:195], 0\n"
      "ds_read_b128 v[182:185], v0 offset:16384\n"
      "v_cvt_pk_bf16_f32 v100, v100, v101\n"
      "v_mfma_f32_16x16x32_bf16 v[132:135], v[172:175], a[196:199], v[132:135]\n"
      "v_fmac_f32 v130, v154, v154\n"
      "v_fmac_f32 v131, v147, v147\n"
      "v_mfma_f32_16x16x32_bf16 v[132:135], v[160:163], a[200:203], v[132:135]\n"
      "v_fmac_f32_e64 v130, v155, v155\n"
      "v_cvt_pk_bf16_f32 v122, v122, v123\n"
      "v_mfma_f32_16x16x32_bf16 v[132:135], v[220:223], a[204:207], v[132:135]\n"
      "v_cvt_pk_bf16_f32 v107, v178, v179\n"
      "v_mul_f32 v193, v180, v193\n"
      "v_mfma_f32_16x16x32_bf16 v[132:135], v[212:215], a[208:211], v[132:135]\n"
      "v_permlane16_swap_b32 v131, v130\n"
      "v_mfma_f32_16x16x32_bf16 v[132:135], v[112:115], a[212:215], v[132:135]\n"
      "s_waitcnt lgkmcnt(2)\n"
      "v_cvt_pk_bf16_f32 v106, v176, v177\n"
      "v_add_f32 v131, v131, v130\n"
      "v_mfma_f32_16x16x32_bf16 v[210:213], a[0:3], v[118:121], 0\n"
      "v_cvt_pk_bf16_f32 v191, v192, v193\n"
      "v_cvt_pk_bf16_f32 v197, v170, v171\n"
      "v_mfma_f32_16x16x32_bf16 v[210:213], a[4:7], v[156:159], v[210:213]\n"
      "v_mul_f32 v167, v180, v167\n"
      "v_mov_b32 v130, v131\n"
      "v_mfma_f32_16x16x32_bf16 v[210:213], a[8:11], v[140:143], v[210:213]\n"
      "v_cvt_pk_bf16_f32 v198, v198, v199\n"
      "v_mul_f32 v164, v180, v164\n"
      "v_mfma_f32_16x16x32_bf16 v[210:213], a[12:15], v[206:209], v[210:213]\n"
      "s_waitcnt lgkmcnt(0)\n"
      "v_permlane32_swap_b32 v131, v130\n"
      "v_add_f32 v131, v131, v130\n"
      "v_mfma_f32_16x16x32_bf16 v[210:213], a[16:19], v[182:185], v[210:213]\n"
      "v_rsq_f32 v131, v131\n"
      "v_mfma_f32_16x16x32_bf16 v[210:213], a[20:23], v[108:111], v[210:213]\n"
      "v_cvt_pk_bf16_f32 v98, v132, v133\n"
      "v_mov_b32 v130, v131\n"
      "v_mfma_f32_16x16x32_bf16 v[178:181], a[24:27], v[118:121], 0\n"
      "s_cmp_gt_u32 s42, s44\n"
      "s_cselect_b32 s95, s94, s95\n"
      "v_cvt_pk_bf16_f32 v192, v164, v165\n"
      "v_mfma_f32_16x16x32_bf16 v[178:181], a[28:31], v[156:159], v[178:181]\n"
      "v_permlane16_swap_b32 v131, v130\n"
      "v_mfma_f32_16x16x32_bf16 v[178:181], a[32:35], v[140:143], v[178:181]\n"
      "v_fma_f32 v214, v210, v210, s51\n"
      "v_fmac_f32 v214, v211, v211\n"
      "v_mfma_f32_16x16x32_bf16 v[178:181], a[36:39], v[206:209], v[178:181]\n"
      "v_fmac_f32 v214, v212, v212\n"
      "v_cvt_pk_bf16_f32 v193, v166, v167\n"
      "v_mfma_f32_16x16x32_bf16 v[178:181], a[40:43], v[182:185], v[178:181]\n"
      "v_mul_f32 v131, s50, v131\n"
      "v_cvt_pk_bf16_f32 v199, v200, v201\n"
      "v_mfma_f32_16x16x32_bf16 v[178:181], a[44:47], v[108:111], v[178:181]\n"
      "v_cvt_pk_bf16_f32 v101, v102, v103\n"
      "v_fmac_f32 v214, v213, v213\n"
      "v_mfma_f32_16x16x32_bf16 v[164:167], a[48:51], v[118:121], 0\n"
      "v_mul_f32 v146, v131, v146\n"
      "v_mul_f32 v151, v130, v151\n"
      "v_mfma_f32_16x16x32_bf16 v[164:167], a[52:55], v[156:159], v[164:167]\n"
      "v_mul_f32 v204, v131, v204\n"
      "v_mul_f32 v205, v131, v205\n"
      "v_mfma_f32_16x16x32_bf16 v[164:167], a[56:59], v[140:143], v[164:167]\n"
      "v_mul_f32 v150, v130, v150\n"
      "v_mul_f32 v148, v130, v148\n"
      "v_mfma_f32_16x16x32_bf16 v[164:167], a[60:63], v[206:209], v[164:167]\n"
      "v_fmac_f32 v214, v178, v178\n"
      "v_mul_f32 v155, v130, v155\n"
      "v_mfma_f32_16x16x32_bf16 v[164:167], a[64:67], v[182:185], v[164:167]\n"
      "v_mul_f32 v145, v131, v145\n"
      "v_mul_f32 v149, v130, v149\n"
      "v_mfma_f32_16x16x32_bf16 v[164:167], a[68:71], v[108:111], v[164:167]\n"
      "v_mul_f32 v154, v130, v154\n"
      "v_fmac_f32_e64 v214, v179, v179\n"
      "v_mfma_f32_16x16x32_bf16 v[112:115], a[72:75], v[118:121], 0\n"
      "v_cvt_pk_bf16_f32 v148, v148, v149\n"
      "v_cvt_pk_bf16_f32 v200, v216, v217\n"
      "v_mfma_f32_16x16x32_bf16 v[112:115], a[76:79], v[156:159], v[112:115]\n"
      "v_mul_f32 v129, v131, v129\n"
      "v_mul_f32 v202, v131, v202\n"
      "v_mfma_f32_16x16x32_bf16 v[112:115], a[80:83], v[140:143], v[112:115]\n"
      "v_mul_f32 v189, v130, v189\n"
      "v_mul_f32 v147, v131, v147\n"
      "v_mfma_f32_16x16x32_bf16 v[112:115], a[84:87], v[206:209], v[112:115]\n"
      "v_mul_f32 v187, v130, v187\n"
      "v_add_u32 v12, 32, v12\n"
      "v_mfma_f32_16x16x32_bf16 v[112:115], a[88:91], v[182:185], v[112:115]\n"
      "v_mul_f32 v153, v130, v153\n"
      "v_fmac_f32 v214, v180, v180\n"
      "v_mfma_f32_16x16x32_bf16 v[112:115], a[92:95], v[108:111], v[112:115]\n"
      "v_cvt_pk_bf16_f32 v149, v150, v151\n"
      "v_cvt_pk_bf16_f32 v201, v218, v219\n"
      "v_mfma_f32_16x16x32_bf16 v[160:163], a[96:99], v[118:121], 0\n"
      "v_mul_f32 v203, v131, v203\n"
      "v_mul_f32 v188, v130, v188\n"
      "v_mfma_f32_16x16x32_bf16 v[160:163], a[100:103], v[156:159], v[160:163]\n"
      "v_mul_f32 v127, v131, v127\n"
      "v_mul_f32 v152, v130, v152\n"
      "v_mfma_f32_16x16x32_bf16 v[160:163], a[104:107], v[140:143], v[160:163]\n"
      "ds_read_u16 v78, v11\n"
      "v_fmac_f32_e64 v214, v181, v181\n"
      "v_mfma_f32_16x16x32_bf16 v[160:163], a[108:111], v[206:209], v[160:163]\n"
      "v_cvt_pk_bf16_f32 v99, v134, v135\n"
      "v_cvt_pk_bf16_f32 v202, v202, v203\n"
      "v_mfma_f32_16x16x32_bf16 v[160:163], a[112:115], v[182:185], v[160:163]\n"
      "v_fma_f32 v103, v112, v112, s51\n"
      "v_fmac_f32 v214, v164, v164\n"
      "v_mfma_f32_16x16x32_bf16 v[160:163], a[116:119], v[108:111], v[160:163]\n"
      "v_mul_f32 v128, v131, v128\n"
      "v_fmac_f32_e64 v214, v165, v165\n"
      "v_mfma_f32_16x16x32_bf16 v[216:219], a[120:123], v[118:121], 0\n"
      "v_fmac_f32 v214, v166, v166\n"
      "v_fmac_f32 v103, v113, v113\n"
      "v_mfma_f32_16x16x32_bf16 v[216:219], a[124:127], v[156:159], v[216:219]\n"
      "v_cvt_pk_bf16_f32 v151, v188, v189\n"
      "v_fmac_f32 v103, v114, v114\n"
      "v_mfma_f32_16x16x32_bf16 v[216:219], a[128:131], v[140:143], v[216:219]\n"
      "v_mul_f32 v126, v131, v126\n"
      "v_mul_f32 v186, v130, v186\n"
      "v_mfma_f32_16x16x32_bf16 v[216:219], a[132:135], v[206:209], v[216:219]\n"
      "s_add_u32 s43, s43, 1\n"
      "v_fmac_f32 v103, v115, v115\n"
      "v_cvt_pk_bf16_f32 v123, v124, v125\n"
      "v_mfma_f32_16x16x32_bf16 v[216:219], a[136:139], v[182:185], v[216:219]\n"
      "v_mul_f32 v144, v131, v144\n"
      "v_cvt_pk_bf16_f32 v152, v152, v153\n"
      "v_mfma_f32_16x16x32_bf16 v[216:219], a[140:143], v[108:111], v[216:219]\n"
      "v_fmac_f32 v103, v160, v160\n"
      "v_cvt_pk_bf16_f32 v203, v204, v205\n"
      "v_mfma_f32_16x16x32_bf16 v[174:177], v[118:121], a[144:147], 0\n"
      "v_fmac_f32 v103, v161, v161\n"
      "v_fmac_f32_e64 v103, v162, v162\n"
      "v_mfma_f32_16x16x32_bf16 v[174:177], v[156:159], a[148:151], v[174:177]\n"
      "v_cvt_pk_bf16_f32 v204, v126, v127\n"
      "v_cvt_pk_bf16_f32 v205, v128, v129\n"
      "v_mfma_f32_16x16x32_bf16 v[174:177], v[140:143], a[152:155], v[174:177]\n"
      "v_fmac_f32 v103, v163, v163\n"
      "v_cvt_pk_bf16_f32 v150, v186, v187\n"
      "v_mfma_f32_16x16x32_bf16 v[174:177], v[206:209], a[156:159], v[174:177]\n"
      "v_fmac_f32 v214, v167, v167\n"
      "v_fmac_f32 v103, v216, v216\n"
      "v_mfma_f32_16x16x32_bf16 v[174:177], v[182:185], a[160:163], v[174:177]\n"
      "v_fmac_f32 v103, v217, v217\n"
      "v_fmac_f32_e64 v103, v218, v218\n"
      "v_mfma_f32_16x16x32_bf16 v[174:177], v[108:111], a[164:167], v[174:177]\n"
      "ds_read_b128 v[186:189], v2 offset:21504\n"
      "v_fmac_f32 v103, v219, v219\n"
      "v_mfma_f32_16x16x32_bf16 v[128:131], v[118:121], a[168:171], 0\n"
      "s_nop 0\n"
      "v_permlane16_swap_b32_e64 v214, v103\n"
      "v_mfma_f32_16x16x32_bf16 v[128:131], v[156:159], a[172:175], v[128:131]\n"
      "v_add_f32 v214, v214, v103\n"
      "v_mov_b32 v103, v214\n"
      "v_mfma_f32_16x16x32_bf16 v[128:131], v[140:143], a[176:179], v[128:131]\n"
      "s_nop 0\n"
      "v_permlane32_swap_b32 v214, v103\n"
      "v_mfma_f32_16x16x32_bf16 v[128:131], v[206:209], a[180:183], v[128:131]\n"
      "v_add_f32 v214, v214, v103\n"
      "v_cvt_pk_bf16_f32 v76, v174, v175\n"
      "v_mfma_f32_16x16x32_bf16 v[128:131], v[182:185], a[184:187], v[128:131]\n"
      "v_rsq_f32 v214, v214\n"
      "v_mfma_f32_16x16x32_bf16 v[128:131], v[108:111], a[188:191], v[128:131]\n"
      "ds_read_b128 v[132:135], v2 offset:18432\n"
      "v_mov_b32_e64 v103, v214\n"
      "v_mfma_f32_16x16x32_bf16 v[124:127], v[118:121], a[192:195], 0\n"
      "ds_read_b128 v[118:121], v2 offset:24576\n"
      "v_cvt_pk_bf16_f32 v144, v144, v145\n"
      "v_mfma_f32_16x16x32_bf16 v[124:127], v[156:159], a[196:199], v[124:127]\n"
      "v_permlane16_swap_b32_e64 v214, v103\n"
      "v_mfma_f32_16x16x32_bf16 v[124:127], v[140:143], a[200:203], v[124:127]\n"
      "v_mul_f32 v162, v103, v162\n"
      "v_mul_f32 v112, v103, v112\n"
      "v_mfma_f32_16x16x32_bf16 v[124:127], v[206:209], a[204:207], v[124:127]\n"
      "v_mul_f32 v216, v103, v216\n"
      "v_mul_f32 v115, v103, v115\n"
      "v_mfma_f32_16x16x32_bf16 v[124:127], v[182:185], a[208:211], v[124:127]\n"
      "v_mul_f32 v217, v103, v217\n"
      "v_mul_f32 v163, v103, v163\n"
      "v_mfma_f32_16x16x32_bf16 v[124:127], v[108:111], a[212:215], v[124:127]\n"
      "s_waitcnt lgkmcnt(1)\n"
      "v_mul_f32 v113, v103, v113\n"
      "v_cvt_pk_bf16_f32 v153, v154, v155\n"
      "v_mfma_f32_16x16x32_bf16 v[172:175], a[216:219], v[132:135], 0\n"
      "ds_read_b128 v[182:185], v10 offset:32\n"
      "v_cvt_pk_bf16_f32 v216, v216, v217\n"
      "v_mfma_f32_16x16x32_bf16 v[172:175], a[220:223], v[186:189], v[172:175]\n"
      "s_waitcnt lgkmcnt(1)\n"
      "v_mul_f32 v161, v103, v161\n"
      "v_mul_f32 v114, v103, v114\n"
      "v_mfma_f32_16x16x32_bf16 v[172:175], a[224:227], v[118:121], v[172:175]\n"
      "v_mul_f32 v214, s50, v214\n"
      "v_mul_f32 v166, v214, v166\n"
      "v_mfma_f32_16x16x32_bf16 v[220:223], a[240:243], v[132:135], 0\n"
      "ds_read_b128 v[206:209], v2 offset:27648\n"
      "v_mul_f32 v219, v103, v219\n"
      "v_mfma_f32_16x16x32_bf16 v[220:223], a[244:247], v[186:189], v[220:223]\n"
      "v_cvt_pk_bf16_f32 v112, v112, v113\n"
      "v_cvt_pk_bf16_f32 v113, v114, v115\n"
      "v_mfma_f32_16x16x32_bf16 v[220:223], a[248:251], v[118:121], v[220:223]\n"
      "v_cvt_pk_bf16_f32 v81, v130, v131\n"
      "v_mul_f32 v164, v214, v164\n"
      "v_mfma_f32_16x16x32_bf16 v[140:143], v[232:235], v[132:135], 0\n"
      "v_mul_f32 v160, v103, v160\n"
      "v_cvt_pk_bf16_f32 v114, v160, v161\n"
      "v_mfma_f32_16x16x32_bf16 v[140:143], v[236:239], v[186:189], v[140:143]\n"
      "ds_read_b128 v[158:161], v2 offset:33792\n"
      "v_cvt_pk_bf16_f32 v84, v124, v125\n"
      "v_mfma_f32_16x16x32_bf16 v[140:143], v[240:243], v[118:121], v[140:143]\n"
      "ds_read_u16 v82, v11 offset:32\n"
      "v_cvt_pk_bf16_f32 v80, v128, v129\n"
      "v_mfma_f32_16x16x32_bf16 v[118:121], v[198:201], v[190:193], v[40:43]\n"
      "v_cvt_pk_bf16_f32 v115, v162, v163\n"
      "v_mul_f32_e64 v218, v103, v218\n"
      "v_mfma_f32_16x16x32_bf16 v[108:111], v[148:151], v[190:193], v[44:47]\n"
      "s_waitcnt lgkmcnt(2)\n"
      "v_mul_f32 v210, v214, v210\n"
      "v_mul_f32_e64 v211, v214, v211\n"
      "v_mfma_f32_16x16x32_bf16 v[128:131], v[112:115], v[190:193], v[48:51]\n"
      "ds_read_b128 v[154:157], v2 offset:30720\n"
      "v_cvt_pk_bf16_f32 v217, v218, v219\n"
      "v_mfma_f32_16x16x32_bf16 v[186:189], v[182:185], v[190:193], v[24:27]\n"
      "v_mul_f32_e64 v165, v214, v165\n"
      "v_cvt_pk_bf16_f32 v77, v176, v177\n"
      "v_mfma_f32_16x16x16_bf16 v[118:121], v[122:123], v[100:101], v[118:121]\n"
      "v_mul_f32 v167, v214, v167\n"
      "v_mul_f32 v212, v214, v212\n"
      "v_mul_f32_e64 v213, v214, v213\n"
      "v_mfma_f32_16x16x16_bf16 v[108:111], v[152:153], v[100:101], v[108:111]\n"
      "v_cvt_pk_bf16_f32 v196, v168, v169\n"
      "v_cvt_pk_bf16_f32 v210, v210, v211\n"
      "v_cvt_pk_bf16_f32 v85, v126, v127\n"
      "v_mfma_f32_16x16x16_bf16 v[128:131], v[216:217], v[100:101], v[128:131]\n"
      "v_cvt_pk_bf16_f32 v164, v164, v165\n"
      "v_cvt_pk_bf16_f32 v145, v146, v147\n"
      "v_max3_f32 v126, v118, v119, v120\n"
      "v_mfma_f32_16x16x16_bf16 v[186:189], v[116:117], v[100:101], v[186:189]\n"
      "v_max3_f32 v126, v126, v110, v111\n"
      "v_cvt_pk_bf16_f32 v211, v212, v213\n"
      "v_max3_f32 v100, v121, v108, v109\n"
      "v_mfma_f32_16x16x32_bf16 v[172:175], a[228:231], v[206:209], v[172:175]\n"
      "s_waitcnt lgkmcnt(0)\n"
      "v_max3_f32 v100, v100, v128, v129\n"
      "v_max3_f32 v126, v126, v130, v131\n"
      "v_mfma_f32_16x16x32_bf16 v[172:175], a[232:235], v[154:157], v[172:175]\n"
      "v_max3_f32 v126, v126, v100, v186\n"
      "v_mov_b32 v100, v126\n"
      "v_mfma_f32_16x16x32_bf16 v[172:175], a[236:239], v[158:161], v[172:175]\n"
      "s_nop 0\n"
      "v_permlane16_swap_b32 v126, v100\n"
      "v_mfma_f32_16x16x32_bf16 v[220:223], a[252:255], v[206:209], v[220:223]\n"
      "v_max_f32 v126, v126, v100\n"
      "v_mov_b32 v100, v126\n"
      "v_mfma_f32_16x16x32_bf16 v[220:223], v[224:227], v[154:157], v[220:223]\n"
      "s_nop 0\n"
      "v_permlane32_swap_b32 v126, v100\n"
      "v_mfma_f32_16x16x32_bf16 v[220:223], v[228:231], v[158:161], v[220:223]\n"
      "v_max_f32 v126, v126, v100\n"
      "v_sub_f32 v119, v119, v126\n"
      "v_mfma_f32_16x16x32_bf16 v[140:143], v[244:247], v[206:209], v[140:143]\n"
      "ds_read_b128 v[100:103], v2 offset:18688\n"
      "v_sub_f32 v118, v118, v126\n"
      "v_mfma_f32_16x16x32_bf16 v[140:143], v[248:251], v[154:157], v[140:143]\n"
      "v_exp_f32 v118, v118\n"
      "v_mfma_f32_16x16x32_bf16 v[140:143], v[252:255], v[158:161], v[140:143]\n"
      "v_exp_f32_e64 v119, v119\n"
      "v_mfma_f32_16x16x32_bf16 v[154:157], v[198:201], v[202:205], v[52:55]\n"
      "ds_read_b128 v[168:171], v2 offset:24832\n"
      "v_sub_f32 v186, v186, v126\n"
      "s_waitcnt vmcnt(1)\n"
      "v_or3_b32 v136, v136, v137, v138\n"
      "v_exp_f32_e64 v186, v186\n"
      "v_pk_mul_f32 v[178:179], v[178:179], v[214:215] op_sel_hi:[1,0]\n"
      "v_pk_add_f32 v[120:121], v[120:121], v[126:127] op_sel_hi:[1,0] neg_lo:[0,1] neg_hi:[0,1]\n"
      "v_pk_add_f32 v[110:111], v[110:111], v[126:127] op_sel_hi:[1,0] neg_lo:[0,1] neg_hi:[0,1]\n"
      "v_pk_mul_f32 v[180:181], v[180:181], v[214:215] op_sel_hi:[1,0]\n"
      "v_pk_add_f32 v[128:129], v[128:129], v[126:127] op_sel_hi:[1,0] neg_lo:[0,1] neg_hi:[0,1]\n"
      "v_pk_add_f32 v[108:109], v[108:109], v[126:127] op_sel_hi:[1,0] neg_lo:[0,1] neg_hi:[0,1]\n"
      "v_pk_add_f32 v[130:131], v[130:131], v[126:127] op_sel_hi:[1,0] neg_lo:[0,1] neg_hi:[0,1]\n"
      "v_mfma_f32_16x16x32_bf16 v[124:127], v[148:151], v[202:205], v[56:59]\n"
      "ds_read_b128 v[158:161], v2 offset:21760\n"
      "v_cvt_pk_bf16_f32 v213, v180, v181\n"
      "v_mfma_f32_16x16x32_bf16 v[188:191], v[112:115], v[202:205], v[60:63]\n"
      "v_exp_f32 v130, v130\n"
      "v_mfma_f32_16x16x32_bf16 v[132:135], v[182:185], v[202:205], v[26:29]\n"
      "s_waitcnt lgkmcnt(2)\n"
      "v_exp_f32_e64 v128, v128\n"
      "v_mfma_f32_16x16x16_bf16 v[154:157], v[122:123], v[144:145], v[154:157]\n"
      "v_cvt_pk_bf16_f32 v90, v186, 0\n"
      "v_exp_f32 v111, v111\n"
      "v_mfma_f32_16x16x16_bf16 v[124:127], v[152:153], v[144:145], v[124:127]\n"
      "v_exp_f32 v120, v120\n"
      "v_cvt_pk_bf16_f32 v165, v166, v167\n"
      "v_mfma_f32_16x16x16_bf16 v[188:191], v[216:217], v[144:145], v[188:191]\n"
      "v_exp_f32_e64 v109, v109\n"
      "v_cvt_pk_bf16_f32 v118, v118, v119\n"
      "v_mfma_f32_16x16x16_bf16 v[132:135], v[116:117], v[144:145], v[132:135]\n"
      "v_cvt_pk_bf16_f32 v212, v178, v179\n"
      "v_exp_f32 v121, v121\n"
      "v_mfma_f32_16x16x32_bf16 v[206:209], a[216:219], v[100:103], 0\n"
      "s_waitcnt lgkmcnt(0)\n"
      "v_cvt_pk_bf16_f32 v119, v120, v121\n"
      "v_max3_f32 v167, v154, v155, v156\n"
      "v_mfma_f32_16x16x32_bf16 v[206:209], a[220:223], v[158:161], v[206:209]\n"
      "v_exp_f32_e64 v108, v108\n"
      "v_mfma_f32_16x16x32_bf16 v[206:209], a[224:227], v[168:171], v[206:209]\n"
      "v_exp_f32_e64 v110, v110\n"
      "v_mfma_f32_16x16x32_bf16 v[176:179], a[240:243], v[100:103], 0\n"
      "v_cvt_pk_bf16_f32 v120, v108, v109\n"
      "v_cvt_pk_bf16_f32 v121, v110, v111\n"
      "v_mfma_f32_16x16x32_bf16 v[176:179], a[244:247], v[158:161], v[176:179]\n"
      "v_exp_f32_e64 v131, v131\n"
      "v_mfma_f32_16x16x32_bf16 v[176:179], a[248:251], v[168:171], v[176:179]\n"
      "v_exp_f32_e64 v129, v129\n"
      "v_mfma_f32_16x16x32_bf16 v[108:111], v[232:235], v[100:103], 0\n"
      "v_max3_f32 v144, v157, v124, v125\n"
      "v_max3_f32 v144, v144, v188, v189\n"
      "v_mfma_f32_16x16x32_bf16 v[108:111], v[236:239], v[158:161], v[108:111]\n"
      "v_cvt_pk_bf16_f32 v88, v128, v129\n"
      "v_max3_f32 v167, v167, v126, v127\n"
      "v_mfma_f32_16x16x32_bf16 v[108:111], v[240:243], v[168:171], v[108:111]\n"
      "v_max3_f32 v167, v167, v190, v191\n"
      "v_cvt_pk_bf16_f32 v89, v130, v131\n"
      "v_mfma_f32_16x16x32_bf16 v[168:171], v[32:35], v[118:121], 0\n"
      "ds_read_u16 v86, v11 offset:64\n"
      "v_fma_f32 v214, v172, v172, s52\n"
      "v_mfma_f32_16x16x32_bf16 v[168:171], v[32:35], v[88:91], v[168:171]\n"
      "v_max3_f32 v167, v167, v144, v132\n"
      "v_mov_b32 v131, v167\n"
      "v_mfma_f32_16x16x32_bf16 v[144:147], v[194:197], v[118:121], 0\n"
      "s_nop 0\n"
      "v_permlane16_swap_b32 v167, v131\n"
      "v_mfma_f32_16x16x32_bf16 v[144:147], v[76:79], v[88:91], v[144:147]\n"
      "ds_read_b128 v[160:163], v2 offset:27904\n"
      "s_waitcnt vmcnt(0)\n"
      "v_or3_b32 v136, v136, v139, v93\n"
      "v_mfma_f32_16x16x32_bf16 v[202:205], v[104:107], v[118:121], 0\n"
      "ds_write_b32 v31, v136 offset:4608\n"
      "v_mfma_f32_16x16x32_bf16 v[202:205], v[80:83], v[88:91], v[202:205]\n"
      "v_max_f32 v167, v167, v131\n"
      "v_mov_b32 v131, v167\n"
      "v_mfma_f32_16x16x32_bf16 v[136:139], v[96:99], v[118:121], 0\n"
      "s_waitcnt lgkmcnt(2)\n"
      "v_rcp_f32 v168, v168\n"
      "v_mfma_f32_16x16x32_bf16 v[136:139], v[84:87], v[88:91], v[136:139]\n"
      "ds_read_b128 v[118:121], v2 offset:30976\n"
      "v_permlane32_swap_b32 v167, v131\n"
      "v_max_f32 v167, v167, v131\n"
      "v_mul_f32_e64 v215, v173, v173\n"
      "v_pk_add_f32 v[154:155], v[154:155], v[166:167] op_sel:[0,1] neg_lo:[0,1] neg_hi:[0,1]\n"
      "v_pk_mul_f32 v[202:203], v[202:203], v[168:169] op_sel_hi:[1,0]\n"
      "v_pk_mul_f32 v[146:147], v[146:147], v[168:169] op_sel_hi:[1,0]\n"
      "v_pk_mul_f32 v[144:145], v[144:145], v[168:169] op_sel_hi:[1,0]\n"
      "v_sub_f32_e64 v132, v132, v167\n"
      "v_pk_mul_f32 v[204:205], v[204:205], v[168:169] op_sel_hi:[1,0]\n"
      "v_cvt_pk_bf16_f32 v144, v144, v145\n"
      "v_pk_mul_f32 v[136:137], v[136:137], v[168:169] op_sel_hi:[1,0]\n"
      "v_pk_fma_f32 v[214:215], v[174:175], v[174:175], v[214:215]\n"
      "v_cvt_pk_bf16_f32 v145, v146, v147\n"
      "v_pk_add_f32 v[124:125], v[124:125], v[166:167] op_sel:[0,1] neg_lo:[0,1] neg_hi:[0,1]\n"
      "v_pk_add_f32 v[156:157], v[156:157], v[166:167] op_sel:[0,1] neg_lo:[0,1] neg_hi:[0,1]\n"
      "v_pk_mul_f32 v[138:139], v[138:139], v[168:169] op_sel_hi:[1,0]\n"
      "v_pk_add_f32 v[188:189], v[188:189], v[166:167] op_sel:[0,1] neg_lo:[0,1] neg_hi:[0,1]\n"
      "v_pk_add_f32 v[126:127], v[126:127], v[166:167] op_sel:[0,1] neg_lo:[0,1] neg_hi:[0,1]\n"
      "v_cvt_pk_bf16_f32 v147, v204, v205\n"
      "v_pk_add_f32 v[190:191], v[190:191], v[166:167] op_sel:[0,1] neg_lo:[0,1] neg_hi:[0,1]\n"
      "v_pk_fma_f32 v[214:215], v[220:221], v[220:221], v[214:215]\n"
      "v_mfma_f32_16x16x32_bf16 v[128:131], v[198:201], v[210:213], v[64:67]\n"
      "v_cvt_pk_bf16_f32 v136, v136, v137\n"
      "v_cvt_pk_bf16_f32 v146, v202, v203\n"
      "v_cvt_pk_bf16_f32 v137, v138, v139\n"
      "v_mfma_f32_16x16x32_bf16 v[202:205], v[148:151], v[210:213], v[68:71]\n"
      "v_exp_f32 v156, v156\n"
      "v_mfma_f32_16x16x32_bf16 v[100:103], v[112:115], v[210:213], v[72:75]\n"
      "s_waitcnt lgkmcnt(0)\n"
      "v_exp_f32_e64 v191, v191\n"
      "v_mfma_f32_16x16x32_bf16 v[166:169], v[182:185], v[210:213], v[28:31]\n"
      "ds_read_b128 v[210:213], v2 offset:34048\n"
      "v_exp_f32 v190, v190\n"
      "v_mfma_f32_16x16x16_bf16 v[128:131], v[122:123], v[164:165], v[128:131]\n"
      "v_exp_f32 v189, v189\n"
      "v_exp_f32 v154, v154\n"
      "v_mfma_f32_16x16x16_bf16 v[202:205], v[152:153], v[164:165], v[202:205]\n"
      "v_exp_f32 v126, v126\n"
      "v_cvt_pk_bf16_f32 v93, v190, v191\n"
      "v_mfma_f32_16x16x16_bf16 v[100:103], v[216:217], v[164:165], v[100:103]\n"
      "v_exp_f32 v155, v155\n"
      "v_exp_f32 v188, v188\n"
      "v_mfma_f32_16x16x16_bf16 v[166:169], v[116:117], v[164:165], v[166:169]\n"
      "v_cvt_pk_bf16_f32 v154, v154, v155\n"
      "v_cvt_pk_bf16_f32 v92, v188, v189\n"
      "v_max3_f32 v181, v128, v129, v130\n"
      "v_mfma_f32_16x16x32_bf16 v[206:209], a[228:231], v[160:163], v[206:209]\n"
      "v_exp_f32 v132, v132\n"
      "v_mfma_f32_16x16x32_bf16 v[206:209], a[232:235], v[118:121], v[206:209]\n"
      "s_waitcnt lgkmcnt(0)\n"
      "v_exp_f32 v127, v127\n"
      "v_mfma_f32_16x16x32_bf16 v[206:209], a[236:239], v[210:213], v[206:209]\n"
      "v_exp_f32 v125, v125\n"
      "v_mfma_f32_16x16x32_bf16 v[176:179], a[252:255], v[160:163], v[176:179]\n"
      "v_exp_f32_e64 v124, v124\n"
      "v_mfma_f32_16x16x32_bf16 v[176:179], v[224:227], v[118:121], v[176:179]\n"
      "v_exp_f32_e64 v157, v157\n"
      "v_mfma_f32_16x16x32_bf16 v[176:179], v[228:231], v[210:213], v[176:179]\n"
      "v_cvt_pk_bf16_f32 v155, v156, v157\n"
      "v_cvt_pk_bf16_f32 v94, v132, 0\n"
      "v_mfma_f32_16x16x32_bf16 v[108:111], v[244:247], v[160:163], v[108:111]\n"
      "v_max3_f32 v181, v181, v204, v205\n"
      "v_cvt_pk_bf16_f32 v156, v124, v125\n"
      "v_mfma_f32_16x16x32_bf16 v[108:111], v[248:251], v[118:121], v[108:111]\n"
      "v_cvt_pk_bf16_f32 v157, v126, v127\n"
      "v_max3_f32 v162, v131, v202, v203\n"
      "v_mfma_f32_16x16x32_bf16 v[108:111], v[252:255], v[210:213], v[108:111]\n"
      "ds_read_b128 v[118:121], v10\n"
      "v_max3_f32 v162, v162, v100, v101\n"
      "v_mfma_f32_16x16x32_bf16 v[168:171], v[32:35], v[154:157], 0\n"
      "ds_read_b128 v[132:135], v7 offset:18432\n"
      "v_max3_f32 v181, v181, v102, v103\n"
      "v_mfma_f32_16x16x32_bf16 v[168:171], v[32:35], v[92:95], v[168:171]\n"
      "v_max3_f32 v181, v181, v162, v166\n"
      "v_mov_b32_e64 v188, v181\n"
      "v_mfma_f32_16x16x32_bf16 v[162:165], v[194:197], v[154:157], 0\n"
      "s_nop 0\n"
      "v_permlane16_swap_b32 v181, v188\n"
      "v_mfma_f32_16x16x32_bf16 v[162:165], v[76:79], v[92:95], v[162:165]\n"
      "ds_read_b64 v[186:187], v10 offset:16\n"
      "v_max_f32_e64 v181, v181, v188\n"
      "v_mfma_f32_16x16x32_bf16 v[210:213], v[104:107], v[154:157], 0\n"
      "ds_write_b128 v3, v[144:147]\n"
      "v_mfma_f32_16x16x32_bf16 v[210:213], v[80:83], v[92:95], v[210:213]\n"
      "ds_read_b128 v[144:147], v2 offset:22016\n"
      "v_mov_b32 v188, v181\n"
      "v_mfma_f32_16x16x32_bf16 v[158:161], v[96:99], v[154:157], 0\n"
      "s_nop 0\n"
      "v_permlane32_swap_b32_e64 v181, v188\n"
      "v_mfma_f32_16x16x32_bf16 v[158:161], v[84:87], v[92:95], v[158:161]\n"
      "s_waitcnt lgkmcnt(3)\n"
      "v_rcp_f32 v168, v168\n"
      "v_mfma_f32_16x16x32_bf16 v[154:157], v[118:121], v[198:201], v[30:33]\n"
      "ds_read_b128 v[198:201], v2 offset:18944\n"
      "v_max_f32_e64 v181, v181, v188\n"
      "v_mfma_f32_16x16x32_bf16 v[188:191], v[118:121], v[148:151], v[30:33]\n"
      "buffer_store_dwordx4 v[132:135], v20, s[24:27], s46 offen sc1\n"
      "v_pk_add_f32 v[100:101], v[100:101], v[180:181] op_sel:[0,1] neg_lo:[0,1] neg_hi:[0,1]\n"
      "v_pk_add_f32 v[130:131], v[130:131], v[180:181] op_sel:[0,1] neg_lo:[0,1] neg_hi:[0,1]\n"
      "v_pk_add_f32 v[202:203], v[202:203], v[180:181] op_sel:[0,1] neg_lo:[0,1] neg_hi:[0,1]\n"
      "v_pk_mul_f32 v[164:165], v[164:165], v[168:169] op_sel_hi:[1,0]\n"
      "v_pk_add_f32 v[128:129], v[128:129], v[180:181] op_sel:[0,1] neg_lo:[0,1] neg_hi:[0,1]\n"
      "v_pk_mul_f32 v[162:163], v[162:163], v[168:169] op_sel_hi:[1,0]\n"
      "v_pk_add_f32 v[102:103], v[102:103], v[180:181] op_sel:[0,1] neg_lo:[0,1] neg_hi:[0,1]\n"
      "v_pk_mul_f32 v[160:161], v[160:161], v[168:169] op_sel_hi:[1,0]\n"
      "v_pk_mul_f32 v[212:213], v[212:213], v[168:169] op_sel_hi:[1,0]\n"
      "v_pk_fma_f32 v[214:215], v[222:223], v[222:223], v[214:215]\n"
      "v_pk_add_f32 v[204:205], v[204:205], v[180:181] op_sel:[0,1] neg_lo:[0,1] neg_hi:[0,1]\n"
      "v_pk_mul_f32 v[210:211], v[210:211], v[168:169] op_sel_hi:[1,0]\n"
      "v_mfma_f32_16x16x32_bf16 v[124:127], v[118:121], v[112:115], v[30:33]\n"
      "s_waitcnt lgkmcnt(0)\n"
      "v_exp_f32 v100, v100\n"
      "v_mfma_f32_16x16x32_bf16 v[112:115], v[118:121], v[182:185], v[30:33]\n"
      "ds_read_b128 v[118:121], v7 offset:22528\n"
      "v_sub_f32_e64 v166, v166, v181\n"
      "v_mfma_f32_16x16x16_bf16 v[154:157], v[186:187], v[122:123], v[154:157]\n"
      "ds_read_b128 v[180:183], v2 offset:25088\n"
      "v_fma_f32 v184, v206, v206, s52\n"
      "v_mfma_f32_16x16x16_bf16 v[188:191], v[186:187], v[152:153], v[188:191]\n"
      "v_exp_f32 v128, v128\n"
      "v_cvt_pk_bf16_f32 v162, v162, v163\n"
      "v_mfma_f32_16x16x16_bf16 v[124:127], v[186:187], v[216:217], v[124:127]\n"
      "v_exp_f32 v101, v101\n"
      "v_exp_f32 v166, v166\n"
      "v_exp_f32 v129, v129\n"
      "v_mfma_f32_16x16x16_bf16 v[112:115], v[186:187], v[116:117], v[112:115]\n"
      "v_mul_f32 v185, v207, v207\n"
      "v_cvt_pk_bf16_f32 v128, v128, v129\n"
      "v_exp_f32 v203, v203\n"
      "v_mfma_f32_16x16x32_bf16 v[132:135], a[216:219], v[198:201], 0\n"
      "ds_read_b128 v[150:153], v2 offset:28160\n"
      "v_cndmask_b32 v154, v154, v188, s[58:59]\n"
      "v_exp_f32 v131, v131\n"
      "v_mfma_f32_16x16x32_bf16 v[132:135], a[220:223], v[144:147], v[132:135]\n"
      "s_waitcnt lgkmcnt(1)\n"
      "v_cndmask_b32 v154, v154, v124, s[60:61]\n"
      "v_cndmask_b32 v154, v154, v112, s[62:63]\n"
      "v_mfma_f32_16x16x32_bf16 v[132:135], a[224:227], v[180:183], v[132:135]\n"
      "buffer_store_dwordx4 v[118:121], v21, s[24:27], s46 offen sc1\n"
      "s_nop 1\n"
      "v_mfma_f32_16x16x32_bf16 v[118:121], a[240:243], v[198:201], 0\n"
      "ds_read_b128 v[186:189], v7 offset:26624\n"
      "v_exp_f32 v204, v204\n"
      "v_mfma_f32_16x16x32_bf16 v[118:121], a[244:247], v[144:147], v[118:121]\n"
      "ds_read_b128 v[124:127], v2 offset:31232\n"
      "v_exp_f32_e64 v102, v102\n"
      "v_max_f32_dpp v155, v154, v154 row_ror:8 row_mask:0xf bank_mask:0xf\n"
      "v_cvt_pk_bf16_f32 v163, v164, v165\n"
      "v_pk_fma_f32 v[214:215], v[140:141], v[140:141], v[214:215]\n"
      "v_pk_fma_f32 v[184:185], v[208:209], v[208:209], v[184:185]\n"
      "v_max_f32_dpp v155, v155, v155 row_ror:4 row_mask:0xf bank_mask:0xf\n"
      "v_cvt_pk_bf16_f32 v165, v212, v213\n"
      "v_pk_fma_f32 v[184:185], v[176:177], v[176:177], v[184:185]\n"
      "v_max_f32_dpp v155, v155, v155 row_ror:2 row_mask:0xf bank_mask:0xf\n"
      "v_pk_fma_f32 v[184:185], v[178:179], v[178:179], v[184:185]\n"
      "v_cvt_pk_bf16_f32 v164, v210, v211\n"
      "v_pk_mul_f32 v[158:159], v[158:159], v[168:169] op_sel_hi:[1,0]\n"
      "v_mfma_f32_16x16x32_bf16 v[118:121], a[248:251], v[180:183], v[118:121]\n"
      "ds_read_b128 v[210:213], v2 offset:34304\n"
      "v_exp_f32_e64 v103, v103\n"
      "v_mfma_f32_16x16x32_bf16 v[168:171], v[232:235], v[198:201], 0\n"
      "v_exp_f32 v130, v130\n"
      "v_mfma_f32_16x16x32_bf16 v[168:171], v[236:239], v[144:147], v[168:171]\n"
      "s_waitcnt lgkmcnt(1)\n"
      "v_max_f32_dpp v155, v155, v155 row_ror:1 row_mask:0xf bank_mask:0xf\n"
      "v_exp_f32 v202, v202\n"
      "v_mfma_f32_16x16x32_bf16 v[168:171], v[240:243], v[180:183], v[168:171]\n"
      "buffer_store_dwordx4 v[186:189], v22, s[24:27], s46 offen sc1\n"
      "v_mfma_f32_16x16x32_bf16 v[132:135], a[228:231], v[150:153], v[132:135]\n"
      "v_max_f32_dpp v155, v155, v155 row_bcast:15 row_mask:0xa bank_mask:0xf\n"
      "v_cvt_pk_bf16_f32 v129, v130, v131\n"
      "v_mfma_f32_16x16x32_bf16 v[132:135], a[232:235], v[124:127], v[132:135]\n"
      "s_waitcnt lgkmcnt(0)\n"
      "v_max_f32_dpp v155, v155, v155 row_bcast:31 row_mask:0xc bank_mask:0xf\n"
      "v_exp_f32 v205, v205\n"
      "v_mfma_f32_16x16x32_bf16 v[132:135], a[236:239], v[210:213], v[132:135]\n"
      "v_readlane_b32 s72, v155, 63\n"
      "v_cvt_pk_bf16_f32 v131, v204, v205\n"
      "v_mfma_f32_16x16x32_bf16 v[118:121], a[252:255], v[150:153], v[118:121]\n"
      "ds_read_b128 v[144:147], v7 offset:30720\n"
      "v_cvt_pk_bf16_f32 v130, v202, v203\n"
      "v_subrev_f32 v154, s72, v154\n"
      "v_mfma_f32_16x16x32_bf16 v[118:121], v[224:227], v[124:127], v[118:121]\n"
      "v_cvt_pk_bf16_f32 v90, v166, 0\n"
      "v_fma_f32 v186, v132, v132, s52\n"
      "v_mfma_f32_16x16x32_bf16 v[118:121], v[228:231], v[210:213], v[118:121]\n"
      "ds_write_b128 v3, v[162:165] offset:256\n"
      "v_pk_fma_f32 v[184:185], v[108:109], v[108:109], v[184:185]\n"
      "v_pk_fma_f32 v[214:215], v[142:143], v[142:143], v[214:215]\n"
      "v_mul_f32_e64 v187, v133, v133\n"
      "v_mfma_f32_16x16x32_bf16 v[168:171], v[244:247], v[150:153], v[168:171]\n"
      "v_exp_f32 v154, v154\n"
      "v_mfma_f32_16x16x32_bf16 v[168:171], v[248:251], v[124:127], v[168:171]\n"
      "v_cvt_pk_bf16_f32 v88, v100, v101\n"
      "v_cvt_pk_bf16_f32 v89, v102, v103\n"
      "v_mfma_f32_16x16x32_bf16 v[168:171], v[252:255], v[210:213], v[168:171]\n"
      "s_waitcnt lgkmcnt(1)\n"
      "v_cvt_pk_bf16_f32 v158, v158, v159\n"
      "v_cvt_pk_bf16_f32 v154, v154, v154\n"
      "v_mfma_f32_16x16x32_bf16 v[162:165], v[32:35], v[128:131], 0\n"
      "ds_write_b16 v13, v154\n"
      "v_mfma_f32_16x16x32_bf16 v[162:165], v[32:35], v[88:91], v[162:165]\n"
      "buffer_store_dwordx4 v[144:147], v23, s[24:27], s46 offen sc1\n"
      "v_pk_fma_f32 v[186:187], v[134:135], v[134:135], v[186:187]\n"
      "v_pk_fma_f32 v[186:187], v[118:119], v[118:119], v[186:187]\n"
      "v_pk_fma_f32 v[184:185], v[110:111], v[110:111], v[184:185]\n"
      "v_mfma_f32_16x16x32_bf16 v[210:213], v[194:197], v[128:131], 0\n"
      "ds_write_b64 v4, v[136:137]\n"
      "v_mfma_f32_16x16x32_bf16 v[210:213], v[76:79], v[88:91], v[210:213]\n"
      "s_nop 0\n"
      "v_rcp_f32 v162, v162\n"
      "v_mfma_f32_16x16x32_bf16 v[152:155], v[104:107], v[128:131], 0\n"
      "ds_read_b128 v[180:183], v7 offset:34816\n"
      "v_add_f32_e64 v214, v214, v215\n"
      "v_mfma_f32_16x16x32_bf16 v[152:155], v[80:83], v[88:91], v[152:155]\n"
      "ds_read_b128 v[144:147], v14\n"
      "v_cvt_pk_bf16_f32 v159, v160, v161\n"
      "v_mfma_f32_16x16x32_bf16 v[202:205], v[96:99], v[128:131], 0\n"
      "ds_read_b128 v[198:201], v14 offset:64\n"
      "v_add_f32_e64 v184, v184, v185\n"
      "v_mfma_f32_16x16x32_bf16 v[202:205], v[84:87], v[88:91], v[202:205]\n"
      "ds_write_b64 v4, v[158:159] offset:256\n"
      "v_permlane16_swap_b32_e64 v214, v184\n"
      "v_pk_mul_f32 v[210:211], v[210:211], v[162:163] op_sel_hi:[1,0]\n"
      "v_pk_fma_f32 v[186:187], v[120:121], v[120:121], v[186:187]\n"
      "v_pk_mul_f32 v[212:213], v[212:213], v[162:163] op_sel_hi:[1,0]\n"
      "v_pk_mul_f32 v[152:153], v[152:153], v[162:163] op_sel_hi:[1,0]\n"
      "v_cvt_pk_bf16_f32 v210, v210, v211\n"
      "v_add_f32_e64 v214, v214, v184\n"
      "v_pk_fma_f32 v[186:187], v[168:169], v[168:169], v[186:187]\n"
      "v_cvt_pk_bf16_f32 v211, v212, v213\n"
      "v_pk_mul_f32 v[154:155], v[154:155], v[162:163] op_sel_hi:[1,0]\n"
      "v_pk_fma_f32 v[186:187], v[170:171], v[170:171], v[186:187]\n"
      "v_cvt_pk_bf16_f32 v212, v152, v153\n"
      "v_add_f32 v186, v186, v187\n"
      "v_cvt_pk_bf16_f32 v213, v154, v155\n"
      "v_mov_b32_e64 v184, v186\n"
      "s_add_u32 s42, s42, 1\n"
      "ds_write_b128 v3, v[210:213] offset:512\n"
      "v_permlane16_swap_b32 v186, v184\n"
      "v_add_f32 v186, v186, v184\n"
      "v_pk_mul_f32 v[202:203], v[202:203], v[162:163] op_sel_hi:[1,0]\n"
      "v_pk_mul_f32 v[204:205], v[204:205], v[162:163] op_sel_hi:[1,0]\n"
      "v_permlane32_swap_b32 v214, v186\n"
      "v_add_f32 v214, v214, v186\n"
      "s_and_b32 s86, s86, -4\n"
      "ds_write_b32 v31, v214\n"
      "v_cvt_pk_bf16_f32 v202, v202, v203\n"
      "v_cvt_pk_bf16_f32 v203, v204, v205\n"
      "s_waitcnt lgkmcnt(2)\n"
      "v_mfma_f32_16x16x32_bf16 v[162:165], v[144:147], v[32:35], 0\n"
      "ds_write_b64 v4, v[202:203] offset:512\n"
      "s_cmp_ge_u32 s95, 0x40000000\n"
      "s_cselect_b64 vcc, -1, s[56:57]\n"
      "s_and_b32 s5, s84, s8\n"
      "v_mfma_f32_16x16x32_bf16 v[162:165], v[198:201], v[32:35], v[162:165]\n"
      "buffer_store_dwordx4 v[180:183], v25, s[24:27], s46 offen sc1\n"
      "s_waitcnt lgkmcnt(0)\n"
      "s_barrier\n"
      "s_cbranch_vccz L_max1_%=\n"
      "L_cold_%=:\n"
      "L_disp1_%=:\n"
      "s_cmp_lt_u32 s95, 0x40000000\n"
      "s_cbranch_scc1 L_nofix_11_%=\n"
      "s_cmp_eq_u32 s95, -2\n"
      "s_cbranch_scc1 L_tail1_%=\n"
      "s_nop 2\n"
      "s_cmp_eq_u32 s84, 0x20000000\n"
      "s_cselect_b32 s66, 0, s66\n"
      "s_cselect_b32 s48, 0, s48\n"
      "s_lshr_b32 s92, s84, 30\n"
      "s_or_b32 s92, s92, s66\n"
      "s_cbranch_scc0 L_nf_12_%=\n"
      "s_bitcmp1_b32 s84, 31\n"
      "s_cselect_b32 s75, 100270464, 0\n"
      "s_bitcmp1_b32 s84, 30\n"
      "s_cselect_b32 s76, 196224, 0\n"
      "s_and_b32 s74, s84, 0x3fffffff\n"
      "s_cmp_ge_u32 s84, 0x40000000\n"
      "s_cselect_b32 s74, s74, 0\n"
      "s_cselect_b32 s84, 0, s84\n"
      "s_sub_u32 s74, s74, s66\n"
      "s_add_u32 s66, s66, s74\n"
      "s_sub_u32 s75, s75, s67\n"
      "s_add_u32 s67, s67, s75\n"
      "s_sub_u32 s76, s76, s68\n"
      "s_add_u32 s68, s68, s76\n"
      "v_add_u32 v16, s74, v16\n"
      "v_add_u32 v17, s74, v17\n"
      "v_add_u32 v18, s74, v18\n"
      "v_add_u32 v19, s74, v19\n"
      "v_add_u32 v15, s74, v15\n"
      "s_cmp_eq_u32 s75, 0\n"
      "s_cbranch_scc1 L_ax_13_%=\n"
      "v_bfe_i32 v155, v27, 0, 1\n"
      "v_and_b32 v155, s75, v155\n"
      "v_sub_u32 v16, v16, v155\n"
      "v_bfe_i32 v155, v27, 2, 1\n"
      "v_and_b32 v155, s75, v155\n"
      "v_sub_u32 v17, v17, v155\n"
      "v_bfe_i32 v155, v27, 4, 1\n"
      "v_and_b32 v155, s75, v155\n"
      "v_sub_u32 v18, v18, v155\n"
      "v_bfe_i32 v155, v27, 6, 1\n"
      "v_and_b32 v155, s75, v155\n"
      "v_sub_u32 v19, v19, v155\n"
      "v_bfe_i32 v155, v27, 8, 1\n"
      "v_and_b32 v155, s75, v155\n"
      "v_sub_u32 v15, v15, v155\n"
      "L_ax_13_%=:\n"
      "s_cmp_eq_u32 s76, 0\n"
      "s_cbranch_scc1 L_ax_14_%=\n"
      "v_bfe_i32 v155, v27, 1, 1\n"
      "v_and_b32 v155, s76, v155\n"
      "v_sub_u32 v16, v16, v155\n"
      "v_bfe_i32 v155, v27, 3, 1\n"
      "v_and_b32 v155, s76, v155\n"
      "v_sub_u32 v17, v17, v155\n"
      "v_bfe_i32 v155, v27, 5, 1\n"
      "v_and_b32 v155, s76, v155\n"
      "v_sub_u32 v18, v18, v155\n"
      "v_bfe_i32 v155, v27, 7, 1\n"
      "v_and_b32 v155, s76, v155\n"
      "v_sub_u32 v19, v19, v155\n"
      "v_bfe_i32 v155, v27, 9, 1\n"
      "v_and_b32 v155, s76, v155\n"
      "v_sub_u32 v15, v15, v155\n"
      "L_ax_14_%=:\n"
      "s_or_b32 s48, s66, s69\n"
      "s_cmp_lg_u32 s48, 0\n"
      "s_cselect_b32 s48, 0x40000000, 0\n"
      "L_nf_12_%=:\n"
      "s_lshr_b32 s92, s85, 30\n"
      "s_or_b32 s92, s92, s69\n"
      "s_cbranch_scc0 L_nf_15_%=\n"
      "s_bitcmp1_b32 s85, 31\n"
      "s_cselect_b32 s75, 100270464, 0\n"
      "s_bitcmp1_b32 s85, 30\n"
      "s_cselect_b32 s76, 196224, 0\n"
      "s_and_b32 s74, s85, 0x3fffffff\n"
      "s_cmp_ge_u32 s85, 0x40000000\n"
      "s_cselect_b32 s74, s74, 0\n"
      "s_cselect_b32 s85, 0, s85\n"
      "s_sub_u32 s74, s74, s69\n"
      "s_add_u32 s69, s69, s74\n"
      "s_sub_u32 s75, s75, s70\n"
      "s_add_u32 s70, s70, s75\n"
      "s_sub_u32 s76, s76, s71\n"
      "s_add_u32 s71, s71, s76\n"
      "v_add_u32 v20, s74, v20\n"
      "v_add_u32 v21, s74, v21\n"
      "v_add_u32 v22, s74, v22\n"
      "v_add_u32 v23, s74, v23\n"
      "v_add_u32 v25, s74, v25\n"
      "s_cmp_eq_u32 s75, 0\n"
      "s_cbranch_scc1 L_ax_16_%=\n"
      "v_bfe_i32 v155, v27, 10, 1\n"
      "v_and_b32 v155, s75, v155\n"
      "v_sub_u32 v20, v20, v155\n"
      "v_bfe_i32 v155, v27, 12, 1\n"
      "v_and_b32 v155, s75, v155\n"
      "v_sub_u32 v21, v21, v155\n"
      "v_bfe_i32 v155, v27, 14, 1\n"
      "v_and_b32 v155, s75, v155\n"
      "v_sub_u32 v22, v22, v155\n"
      "v_bfe_i32 v155, v27, 16, 1\n"
      "v_and_b32 v155, s75, v155\n"
      "v_sub_u32 v23, v23, v155\n"
      "v_bfe_i32 v155, v27, 18, 1\n"
      "v_and_b32 v155, s75, v155\n"
      "v_sub_u32 v25, v25, v155\n"
      "L_ax_16_%=:\n"
      "s_cmp_eq_u32 s76, 0\n"
      "s_cbranch_scc1 L_ax_17_%=\n"
      "v_bfe_i32 v155, v27, 11, 1\n"
      "v_and_b32 v155, s76, v155\n"
      "v_sub_u32 v20, v20, v155\n"
      "v_bfe_i32 v155, v27, 13, 1\n"
      "v_and_b32 v155, s76, v155\n"
      "v_sub_u32 v21, v21, v155\n"
      "v_bfe_i32 v155, v27, 15, 1\n"
      "v_and_b32 v155, s76, v155\n"
      "v_sub_u32 v22, v22, v155\n"
      "v_bfe_i32 v155, v27, 17, 1\n"
      "v_and_b32 v155, s76, v155\n"
      "v_sub_u32 v23, v23, v155\n"
      "v_bfe_i32 v155, v27, 19, 1\n"
      "v_and_b32 v155, s76, v155\n"
      "v_sub_u32 v25, v25, v155\n"
      "L_ax_17_%=:\n"
      "s_or_b32 s48, s66, s69\n"
      "s_cmp_lg_u32 s48, 0\n"
      "s_cselect_b32 s48, 0x40000000, 0\n"
      "L_nf_15_%=:\n"
      "s_and_b32 s5, s84, s8\n"
      "s_sub_u32 s92, s42, 1\n"
      "s_cmp_gt_u32 s92, s44\n"
      "s_cbranch_scc1 L_tail1_%=\n"
      "L_nofix_11_%=:\n"
      "s_cmp_lg_u64 s[56:57], 0\n"
      "s_cbranch_scc1 L_slow1_%=\n"
      "L_sel1_%=:\n"
      "s_cmp_lg_u64 s[54:55], 0\n"
      "s_cbranch_scc1 L_max1_%=\n"
      "s_branch L_loop1_%=\n"
      "L_slow1_%=:\n"
      "s_nop 4\n"
      "v_mfma_f32_16x16x32_bf16 v[152:155], v[144:147], v[194:197], 0\n"
      "ds_read_b128 v[126:129], v0 offset:18432\n"
      "s_add_u32 s72, s42, 1\n"
      "v_mfma_f32_16x16x32_bf16 v[152:155], v[198:201], v[76:79], v[152:155]\n"
      "ds_read_b128 v[158:161], v9\n"
      "v_readlane_b32 s77, v38, s72\n"
      "v_mfma_f32_16x16x32_bf16 v[210:213], v[144:147], v[104:107], 0\n"
      "ds_read_b128 v[184:187], v0 offset:24576\n"
      "s_mov_b32 m0, 110592\n"
      "v_mfma_f32_16x16x32_bf16 v[210:213], v[198:201], v[80:83], v[210:213]\n"
      "ds_read_b128 v[100:103], v1 offset:30720\n"
      "s_and_b32 s73, s77, -16\n"
      "v_mfma_f32_16x16x32_bf16 v[180:183], v[144:147], v[96:99], 0\n"
      "ds_read_b128 v[114:117], v1 offset:18432\n"
      "v_subrev_u32 v196, 115456, v9\n"
      "v_mfma_f32_16x16x32_bf16 v[180:183], v[198:201], v[84:87], v[180:183]\n"
      "buffer_load_dwordx4 v196, s[28:31], s73 offen  lds\n"
      "s_waitcnt lgkmcnt(4)\n"
      "v_mfma_f32_16x16x32_bf16 v[212:215], a[0:3], v[126:129], 0\n"
      "ds_read_b128 v[154:157], v1 offset:24576\n"
      "v_readlane_b32 s45, v37, s43\n"
      "v_mfma_f32_16x16x32_bf16 v[122:125], a[24:27], v[126:129], 0\n"
      "s_waitcnt lgkmcnt(4)\n"
      "v_add_f32 v158, v158, v159\n"
      "v_add_f32_e64 v160, v160, v161\n"
      "v_mfma_f32_16x16x32_bf16 v[164:167], a[48:51], v[126:129], 0\n"
      "s_add_u32 s73, s73, 1024\n"
      "v_add_f32_e64 v158, v158, v160\n"
      "v_mov_b32_dpp v162, v162 quad_perm:[0,0,0,0] row_mask:0xf bank_mask:0xf\n"
      "v_mfma_f32_16x16x32_bf16 v[198:201], a[72:75], v[126:129], 0\n"
      "s_mov_b32 m0, 111616\n"
      "v_rsq_f32 v158, v158\n"
      "v_mfma_f32_16x16x32_bf16 v[202:205], a[96:99], v[126:129], 0\n"
      "s_waitcnt lgkmcnt(1)\n"
      "v_mul_f32 v158, 0x415db3d7, v158\n"
      "v_mov_b32_e64 v160, v158\n"
      "v_mfma_f32_16x16x32_bf16 v[144:147], a[120:123], v[126:129], 0\n"
      "ds_read_b128 v[136:139], v0 offset:30720\n"
      "v_subrev_u32 v77, 115456, v9\n"
      "v_mfma_f32_16x16x32_bf16 v[212:215], a[4:7], v[114:117], v[212:215]\n"
      "buffer_load_dwordx4 v196, s[28:31], s73 offen  lds\n"
      "v_mfma_f32_16x16x32_bf16 v[212:215], a[8:11], v[184:187], v[212:215]\n"
      "s_mov_b32 m0, 112640\n"
      "v_permlane32_swap_b32 v158, v160\n"
      "v_mfma_f32_16x16x32_bf16 v[122:125], a[28:31], v[114:117], v[122:125]\n"
      "v_mul_f32 v133, v160, v133\n"
      "v_bfe_u32 v85, v77, 4, 4\n"
      "v_mfma_f32_16x16x32_bf16 v[122:125], a[32:35], v[184:187], v[122:125]\n"
      "s_add_u32 s73, s73, 1024\n"
      "v_mul_f32 v118, v160, v118\n"
      "v_mov_b32 v159, v158\n"
      "v_mfma_f32_16x16x32_bf16 v[164:167], a[52:55], v[114:117], v[164:167]\n"
      "s_nop 0\n"
      "v_permlane16_swap_b32 v158, v159\n"
      "v_mfma_f32_16x16x32_bf16 v[164:167], a[56:59], v[184:187], v[164:167]\n"
      "v_subrev_u32 v130, 115456, v9\n"
      "v_mul_f32_e64 v175, v158, v175\n"
      "v_mfma_f32_16x16x32_bf16 v[198:201], a[76:79], v[114:117], v[198:201]\n"
      "ds_read_b128 v[188:191], v9 offset:4608\n"
      "v_mul_f32_e64 v221, v158, v221\n"
      "v_mfma_f32_16x16x32_bf16 v[198:201], a[80:83], v[184:187], v[198:201]\n"
      "buffer_load_dwordx4 v196, s[28:31], s73 offen  lds\n"
      "v_mfma_f32_16x16x32_bf16 v[202:205], a[100:103], v[114:117], v[202:205]\n"
      "s_waitcnt lgkmcnt(1)\n"
      "v_mul_f32 v172, v158, v172\n"
      "v_mul_f32_e64 v174, v158, v174\n"
      "v_mfma_f32_16x16x32_bf16 v[202:205], a[104:107], v[184:187], v[202:205]\n"
      "s_add_u32 s73, s73, 1024\n"
      "v_mul_f32 v173, v158, v173\n"
      "v_mul_f32 v108, v159, v108\n"
      "v_mfma_f32_16x16x32_bf16 v[144:147], a[124:127], v[114:117], v[144:147]\n"
      "s_mov_b32 m0, 113664\n"
      "v_mul_f32 v223, v158, v223\n"
      "v_mul_f32 v209, v159, v209\n"
      "v_mfma_f32_16x16x32_bf16 v[144:147], a[128:131], v[184:187], v[144:147]\n"
      "ds_read_b128 v[216:219], v0 offset:32768\n"
      "v_mul_f32_e64 v222, v158, v222\n"
      "v_mfma_f32_16x16x32_bf16 v[212:215], a[12:15], v[154:157], v[212:215]\n"
      "buffer_load_dwordx4 v196, s[28:31], s73 offen  lds\n"
      "v_mfma_f32_16x16x32_bf16 v[212:215], a[16:19], v[136:139], v[212:215]\n"
      "s_add_u32 s73, s73, 1024\n"
      "v_cvt_pk_bf16_f32 v172, v172, v173\n"
      "v_cvt_pk_bf16_f32 v173, v174, v175\n"
      "v_mfma_f32_16x16x32_bf16 v[212:215], a[20:23], v[100:103], v[212:215]\n"
      "s_mov_b32 m0, 114688\n"
      "v_mul_f32 v220, v158, v220\n"
      "v_mul_f32 v179, v159, v179\n"
      "v_mfma_f32_16x16x32_bf16 v[122:125], a[36:39], v[154:157], v[122:125]\n"
      "s_mov_b32 exec_hi, 0xffff\n"
      "buffer_load_dwordx4 v196, s[28:31], s73 offen  lds\n"
      "s_mov_b32 exec_hi, -1\n"
      "v_mfma_f32_16x16x32_bf16 v[122:125], a[40:43], v[136:139], v[122:125]\n"
      "v_cvt_pk_bf16_f32 v174, v220, v221\n"
      "v_cvt_pk_bf16_f32 v175, v222, v223\n"
      "v_mfma_f32_16x16x32_bf16 v[122:125], a[44:47], v[100:103], v[122:125]\n"
      "v_mul_f32 v178, v159, v178\n"
      "v_fma_f32 v76, v212, v212, s51\n"
      "v_mfma_f32_16x16x32_bf16 v[164:167], a[60:63], v[154:157], v[164:167]\n"
      "ds_write_b128 v5, v[172:175] offset:18432\n"
      "v_pk_mul_f32 v[140:141], v[140:141], v[158:159] op_sel_hi:[1,0]\n"
      "v_pk_mul_f32 v[142:143], v[142:143], v[158:159] op_sel_hi:[1,0]\n"
      "v_pk_mul_f32 v[206:207], v[206:207], v[158:159] op_sel:[0,1]\n"
      "v_mfma_f32_16x16x32_bf16 v[164:167], a[64:67], v[136:139], v[164:167]\n"
      "s_add_u32 m0, s64, 0\n"
      "v_cvt_pk_bf16_f32 v140, v140, v141\n"
      "v_mul_f32 v111, v159, v111\n"
      "v_mfma_f32_16x16x32_bf16 v[164:167], a[68:71], v[100:103], v[164:167]\n"
      "buffer_load_dwordx4 v16, s[16:19], s84 offen  lds\n"
      "v_mfma_f32_16x16x32_bf16 v[198:201], a[84:87], v[154:157], v[198:201]\n"
      "ds_read_b128 v[172:175], v1 offset:26624\n"
      "v_mul_f32_e64 v208, v159, v208\n"
      "v_mfma_f32_16x16x32_bf16 v[198:201], a[88:91], v[136:139], v[198:201]\n"
      "ds_read_b128 v[220:223], v1 offset:20480\n"
      "v_cvt_pk_bf16_f32 v141, v142, v143\n"
      "v_mfma_f32_16x16x32_bf16 v[198:201], a[92:95], v[100:103], v[198:201]\n"
      "ds_write_b64 v6, v[140:141] offset:18432\n"
      "v_mfma_f32_16x16x32_bf16 v[202:205], a[108:111], v[154:157], v[202:205]\n"
      "v_fmac_f32 v76, v213, v213\n"
      "v_mul_f32 v177, v159, v177\n"
      "v_mfma_f32_16x16x32_bf16 v[202:205], a[112:115], v[136:139], v[202:205]\n"
      "v_cvt_pk_bf16_f32 v206, v206, v207\n"
      "v_mul_f32 v110, v159, v110\n"
      "v_mfma_f32_16x16x32_bf16 v[202:205], a[116:119], v[100:103], v[202:205]\n"
      "s_add_u32 m0, s64, 4096\n"
      "v_rcp_f32 v162, v162\n"
      "v_mfma_f32_16x16x32_bf16 v[144:147], a[132:135], v[154:157], v[144:147]\n"
      "buffer_load_dwordx4 v17, s[16:19], s84 offen  lds\n"
      "v_mfma_f32_16x16x32_bf16 v[144:147], a[136:139], v[136:139], v[144:147]\n"
      "ds_read_b128 v[140:143], v0 offset:20480\n"
      "v_mul_f32_e64 v109, v159, v109\n"
      "v_mfma_f32_16x16x32_bf16 v[144:147], a[140:143], v[100:103], v[144:147]\n"
      "ds_read_b128 v[148:151], v0 offset:26624\n"
      "v_readlane_b32 s46, v37, s42\n"
      "v_mfma_f32_16x16x32_bf16 v[194:197], v[126:129], a[144:147], 0\n"
      "v_cndmask_b32 v152, v152, v210, s[58:59]\n"
      "v_cvt_pk_bf16_f32 v207, v208, v209\n"
      "v_mfma_f32_16x16x32_bf16 v[194:197], v[114:117], a[148:151], v[194:197]\n"
      "s_add_u32 m0, s64, 8192\n"
      "v_mul_f32_e64 v176, v159, v176\n"
      "v_cvt_pk_bf16_f32 v209, v178, v179\n"
      "v_mfma_f32_16x16x32_bf16 v[194:197], v[184:187], a[152:155], v[194:197]\n"
      "buffer_load_dwordx4 v18, s[16:19], s84 offen  lds\n"
      "v_mfma_f32_16x16x32_bf16 v[194:197], v[154:157], a[156:159], v[194:197]\n"
      "s_add_u32 m0, s64, 12288\n"
      "v_fma_f32 v158, v198, v198, s51\n"
      "v_fmac_f32 v158, v199, v199\n"
      "v_mfma_f32_16x16x32_bf16 v[194:197], v[136:139], a[160:163], v[194:197]\n"
      "v_cvt_pk_bf16_f32 v108, v108, v109\n"
      "v_fmac_f32 v158, v200, v200\n"
      "v_mfma_f32_16x16x32_bf16 v[194:197], v[100:103], a[164:167], v[194:197]\n"
      "v_cndmask_b32 v152, v152, v180, s[60:61]\n"
      "v_mul_u32_u24 v78, 98, v85\n"
      "v_mfma_f32_16x16x32_bf16 v[104:107], v[126:129], a[168:171], 0\n"
      "v_cvt_pk_bf16_f32 v208, v176, v177\n"
      "v_mul_f32 v171, v160, v171\n"
      "v_mfma_f32_16x16x32_bf16 v[104:107], v[114:117], a[172:175], v[104:107]\n"
      "buffer_load_dwordx4 v19, s[16:19], s84 offen  lds\n"
      "v_mfma_f32_16x16x32_bf16 v[104:107], v[184:187], a[176:179], v[104:107]\n"
      "v_fmac_f32 v76, v214, v214\n"
      "v_fmac_f32 v76, v215, v215\n"
      "v_mfma_f32_16x16x32_bf16 v[104:107], v[154:157], a[180:183], v[104:107]\n"
      "v_cvt_pk_bf16_f32 v109, v110, v111\n"
      "v_mul_f32 v168, v160, v168\n"
      "v_mfma_f32_16x16x32_bf16 v[104:107], v[136:139], a[184:187], v[104:107]\n"
      "s_mov_b32 m0, s6\n"
      "v_fmac_f32 v158, v201, v201\n"
      "v_fmac_f32 v158, v202, v202\n"
      "v_mfma_f32_16x16x32_bf16 v[104:107], v[100:103], a[188:191], v[104:107]\n"
      "v_mul_f32 v119, v160, v119\n"
      "v_fmac_f32_e64 v158, v203, v203\n"
      "v_mfma_f32_16x16x32_bf16 v[96:99], v[126:129], a[192:195], 0\n"
      "buffer_load_dwordx4 v15, s[0:3], s5 offen  lds\n"
      "v_mfma_f32_16x16x32_bf16 v[96:99], v[114:117], a[196:199], v[96:99]\n"
      "ds_read_b128 v[176:179], v1 offset:32768\n"
      "v_mul_f32 v132, v160, v132\n"
      "v_mfma_f32_16x16x32_bf16 v[96:99], v[184:187], a[200:203], v[96:99]\n"
      "v_cvt_pk_bf16_f32 v104, v104, v105\n"
      "v_mul_f32 v121, v160, v121\n"
      "v_mfma_f32_16x16x32_bf16 v[96:99], v[154:157], a[204:207], v[96:99]\n"
      "ds_write_b128 v5, v[206:209] offset:24576\n"
      "v_pk_mul_f32 v[134:135], v[134:135], v[160:161] op_sel_hi:[1,0]\n"
      "v_mfma_f32_16x16x32_bf16 v[96:99], v[136:139], a[208:211], v[96:99]\n"
      "buffer_load_dwordx4 v[114:117], v36, s[28:31], s86 offen\n"
      "v_mfma_f32_16x16x32_bf16 v[96:99], v[100:103], a[212:215], v[96:99]\n"
      "s_waitcnt lgkmcnt(2)\n"
      "v_fmac_f32 v158, v204, v204\n"
      "v_mul_f32 v120, v160, v120\n"
      "v_mfma_f32_16x16x32_bf16 v[180:183], a[0:3], v[140:143], 0\n"
      "v_fmac_f32 v76, v122, v122\n"
      "v_cvt_pk_bf16_f32 v194, v194, v195\n"
      "v_mfma_f32_16x16x32_bf16 v[180:183], a[4:7], v[220:223], v[180:183]\n"
      "ds_write_b64 v6, v[108:109] offset:24576\n"
      "v_mfma_f32_16x16x32_bf16 v[180:183], a[8:11], v[148:151], v[180:183]\n"
      "v_fmac_f32 v76, v123, v123\n"
      "v_fmac_f32 v158, v205, v205\n"
      "v_mfma_f32_16x16x32_bf16 v[180:183], a[12:15], v[172:175], v[180:183]\n"
      "s_waitcnt lgkmcnt(1)\n"
      "v_fmac_f32 v76, v124, v124\n"
      "v_mul_f32_e64 v170, v160, v170\n"
      "v_mfma_f32_16x16x32_bf16 v[180:183], a[16:19], v[216:219], v[180:183]\n"
      "buffer_load_dword v86, v29, s[28:31], s86 offen\n"
      "v_mfma_f32_16x16x32_bf16 v[180:183], a[20:23], v[176:179], v[180:183]\n"
      "s_or_b32 s95, s45, s46\n"
      "v_fmac_f32 v158, v144, v144\n"
      "v_cvt_pk_bf16_f32 v96, v96, v97\n"
      "v_mfma_f32_16x16x32_bf16 v[206:209], a[24:27], v[140:143], 0\n"
      "ds_read_b128 v[108:111], v1 offset:22528\n"
      "v_fmac_f32_e64 v76, v125, v125\n"
      "v_mfma_f32_16x16x32_bf16 v[206:209], a[28:31], v[220:223], v[206:209]\n"
      "v_cvt_pk_bf16_f32 v132, v132, v133\n"
      "v_fma_f32 v159, v180, v180, s51\n"
      "v_mfma_f32_16x16x32_bf16 v[206:209], a[32:35], v[148:151], v[206:209]\n"
      "v_mul_f32 v152, v162, v152\n"
      "v_fmac_f32 v76, v164, v164\n"
      "v_mfma_f32_16x16x32_bf16 v[206:209], a[36:39], v[172:175], v[206:209]\n"
      "v_fmac_f32 v76, v165, v165\n"
      "v_fmac_f32 v158, v145, v145\n"
      "v_mfma_f32_16x16x32_bf16 v[206:209], a[40:43], v[216:219], v[206:209]\n"
      "ds_read_b128 v[154:157], v0 offset:22528\n"
      "v_fmac_f32 v158, v146, v146\n"
      "v_mfma_f32_16x16x32_bf16 v[206:209], a[44:47], v[176:179], v[206:209]\n"
      "v_fmac_f32 v158, v147, v147\n"
      "v_fmac_f32_e64 v76, v166, v166\n"
      "v_mfma_f32_16x16x32_bf16 v[100:103], a[48:51], v[140:143], 0\n"
      "v_fmac_f32_e64 v76, v167, v167\n"
      "v_cvt_pk_bf16_f32 v152, v152, v152\n"
      "v_mfma_f32_16x16x32_bf16 v[100:103], a[52:55], v[220:223], v[100:103]\n"
      "v_cvt_pk_bf16_f32 v195, v196, v197\n"
      "v_lshrrev_b32 v130, 3, v130\n"
      "v_mfma_f32_16x16x32_bf16 v[100:103], a[56:59], v[148:151], v[100:103]\n"
      "v_permlane16_swap_b32 v76, v158\n"
      "v_mfma_f32_16x16x32_bf16 v[100:103], a[60:63], v[172:175], v[100:103]\n"
      "v_add_f32 v76, v76, v158\n"
      "v_mov_b32 v158, v76\n"
      "v_mfma_f32_16x16x32_bf16 v[100:103], a[64:67], v[216:219], v[100:103]\n"
      "s_nop 0\n"
      "v_permlane32_swap_b32 v76, v158\n"
      "v_mfma_f32_16x16x32_bf16 v[100:103], a[68:71], v[176:179], v[100:103]\n"
      "v_lshrrev_b32 v77, 8, v77\n"
      "v_min_u32 v130, 96, v130\n"
      "v_mfma_f32_16x16x32_bf16 v[136:139], a[72:75], v[140:143], 0\n"
      "v_fmac_f32 v159, v181, v181\n"
      "v_readlane_b32 s47, v38, s43\n"
      "v_mfma_f32_16x16x32_bf16 v[136:139], a[76:79], v[220:223], v[136:139]\n"
      "v_or3_b32 v188, v188, v189, v190\n"
      "v_add_f32_e64 v76, v76, v158\n"
      "v_mfma_f32_16x16x32_bf16 v[136:139], a[80:83], v[148:151], v[136:139]\n"
      "ds_read_b128 v[126:129], v0 offset:28672\n"
      "v_fmac_f32 v159, v182, v182\n"
      "v_mfma_f32_16x16x32_bf16 v[136:139], a[84:87], v[172:175], v[136:139]\n"
      "s_and_b32 s77, s77, 15\n"
      "v_cvt_pk_bf16_f32 v97, v98, v99\n"
      "v_cvt_pk_bf16_f32 v105, v106, v107\n"
      "v_mfma_f32_16x16x32_bf16 v[136:139], a[88:91], v[216:219], v[136:139]\n"
      "v_rsq_f32 v76, v76\n"
      "v_mfma_f32_16x16x32_bf16 v[136:139], a[92:95], v[176:179], v[136:139]\n"
      "v_cvt_pk_bf16_f32 v133, v134, v135\n"
      "v_cvt_pk_bf16_f32 v134, v118, v119\n"
      "v_mfma_f32_16x16x32_bf16 v[184:187], a[96:99], v[140:143], 0\n"
      "v_cvt_pk_bf16_f32 v135, v120, v121\n"
      "v_fmac_f32 v159, v183, v183\n"
      "v_mfma_f32_16x16x32_bf16 v[184:187], a[100:103], v[220:223], v[184:187]\n"
      "s_or_b32 s95, s95, s48\n"
      "v_mov_b32 v158, v76\n"
      "v_fma_f32 v106, v136, v136, s51\n"
      "v_mfma_f32_16x16x32_bf16 v[184:187], a[104:107], v[148:151], v[184:187]\n"
      "v_permlane16_swap_b32 v76, v158\n"
      "v_mfma_f32_16x16x32_bf16 v[184:187], a[108:111], v[172:175], v[184:187]\n"
      "v_fmac_f32 v106, v137, v137\n"
      "v_mul_f32 v76, s50, v76\n"
      "v_mfma_f32_16x16x32_bf16 v[184:187], a[112:115], v[216:219], v[184:187]\n"
      "v_fmac_f32 v106, v138, v138\n"
      "v_fmac_f32 v159, v206, v206\n"
      "v_mfma_f32_16x16x32_bf16 v[184:187], a[116:119], v[176:179], v[184:187]\n"
      "v_mul_f32 v169, v160, v169\n"
      "v_fmac_f32 v159, v207, v207\n"
      "v_mfma_f32_16x16x32_bf16 v[160:163], a[120:123], v[140:143], 0\n"
      "v_mul_f32 v147, v158, v147\n"
      "v_fmac_f32 v159, v208, v208\n"
      "v_mfma_f32_16x16x32_bf16 v[160:163], a[124:127], v[220:223], v[160:163]\n"
      "ds_write_b128 v5, v[132:135] offset:30720\n"
      "v_mfma_f32_16x16x32_bf16 v[160:163], a[128:131], v[148:151], v[160:163]\n"
      "v_fmac_f32 v106, v139, v139\n"
      "v_mul_f32 v144, v158, v144\n"
      "v_mfma_f32_16x16x32_bf16 v[160:163], a[132:135], v[172:175], v[160:163]\n"
      "v_fmac_f32 v159, v209, v209\n"
      "v_fmac_f32 v106, v184, v184\n"
      "v_mfma_f32_16x16x32_bf16 v[160:163], a[136:139], v[216:219], v[160:163]\n"
      "v_mul_f32 v213, v76, v213\n"
      "v_cvt_pk_bf16_f32 v168, v168, v169\n"
      "v_mfma_f32_16x16x32_bf16 v[160:163], a[140:143], v[176:179], v[160:163]\n"
      "v_lshl_add_u32 v78, v77, 3, v78\n"
      "v_fmac_f32_e64 v106, v185, v185\n"
      "v_mfma_f32_16x16x32_bf16 v[132:135], v[140:143], a[144:147], 0\n"
      "v_mul_f32 v202, v158, v202\n"
      "v_mul_f32 v199, v158, v199\n"
      "v_mfma_f32_16x16x32_bf16 v[132:135], v[220:223], a[148:151], v[132:135]\n"
      "v_cvt_pk_bf16_f32 v169, v170, v171\n"
      "v_fmac_f32 v159, v100, v100\n"
      "v_mfma_f32_16x16x32_bf16 v[132:135], v[148:151], a[152:155], v[132:135]\n"
      "v_mul_f32 v212, v76, v212\n"
      "v_fmac_f32 v106, v186, v186\n"
      "v_mfma_f32_16x16x32_bf16 v[132:135], v[172:175], a[156:159], v[132:135]\n"
      "v_mul_f32 v167, v76, v167\n"
      "v_fmac_f32 v106, v187, v187\n"
      "v_mfma_f32_16x16x32_bf16 v[132:135], v[216:219], a[160:163], v[132:135]\n"
      "ds_write_b16 v12, v152\n"
      "v_mfma_f32_16x16x32_bf16 v[132:135], v[176:179], a[164:167], v[132:135]\n"
      "v_fmac_f32 v159, v101, v101\n"
      "v_lshlrev_b32 v77, 2, v77\n"
      "v_mfma_f32_16x16x32_bf16 v[118:121], v[140:143], a[168:171], 0\n"
      "v_fmac_f32 v106, v160, v160\n"
      "v_fmac_f32_e64 v106, v161, v161\n"
      "v_mfma_f32_16x16x32_bf16 v[118:121], v[220:223], a[172:175], v[118:121]\n"
      "ds_write_b64 v6, v[168:169] offset:30720\n"
      "v_mfma_f32_16x16x32_bf16 v[118:121], v[148:151], a[176:179], v[118:121]\n"
      "v_mul_f32 v145, v158, v145\n"
      "v_fmac_f32 v159, v102, v102\n"
      "v_mfma_f32_16x16x32_bf16 v[118:121], v[172:175], a[180:183], v[118:121]\n"
      "v_bitop3_b32 v188, v188, v191, s33 bitop3:0xa8\n"
      "v_cvt_pk_bf16_f32 v212, v212, v213\n"
      "v_mfma_f32_16x16x32_bf16 v[118:121], v[216:219], a[184:187], v[118:121]\n"
      "ds_read_b128 v[168:171], v1 offset:28672\n"
      "v_fmac_f32 v106, v162, v162\n"
      "v_mfma_f32_16x16x32_bf16 v[118:121], v[176:179], a[188:191], v[118:121]\n"
      "v_fmac_f32 v106, v163, v163\n"
      "v_fmac_f32 v159, v103, v103\n"
      "v_mfma_f32_16x16x32_bf16 v[190:193], v[140:143], a[192:195], 0\n"
      "v_mul_f32 v215, v76, v215\n"
      "v_mul_f32 v198, v158, v198\n"
      "v_mfma_f32_16x16x32_bf16 v[190:193], v[220:223], a[196:199], v[190:193]\n"
      "ds_read_b128 v[220:223], v0 offset:34816\n"
      "v_mul_f32 v165, v76, v165\n"
      "v_mfma_f32_16x16x32_bf16 v[190:193], v[148:151], a[200:203], v[190:193]\n"
      "s_waitcnt lgkmcnt(2)\n"
      "v_permlane16_swap_b32 v159, v106\n"
      "v_mfma_f32_16x16x32_bf16 v[190:193], v[172:175], a[204:207], v[190:193]\n"
      "ds_read_b128 v[172:175], v1 offset:34816\n"
      "v_add_f32_e64 v159, v159, v106\n"
      "v_mfma_f32_16x16x32_bf16 v[190:193], v[216:219], a[208:211], v[190:193]\n"
      "ds_read_b128 v[140:143], v7\n"
      "v_mov_b32 v106, v159\n"
      "v_mfma_f32_16x16x32_bf16 v[190:193], v[176:179], a[212:215], v[190:193]\n"
      "s_nop 0\n"
      "v_permlane32_swap_b32 v159, v106\n"
      "v_mfma_f32_16x16x32_bf16 v[150:153], a[0:3], v[154:157], 0\n"
      "v_add_f32 v159, v159, v106\n"
      "v_add_u32_e64 v130, s77, v130\n"
      "v_mfma_f32_16x16x32_bf16 v[150:153], a[4:7], v[108:111], v[150:153]\n"
      "v_rsq_f32 v159, v159\n"
      "v_mfma_f32_16x16x32_bf16 v[150:153], a[8:11], v[126:129], v[150:153]\n"
      "s_waitcnt lgkmcnt(2)\n"
      "v_cmp_ne_u32 s[56:57], 0, v188\n"
      "v_cvt_pk_bf16_f32 v198, v198, v199\n"
      "v_mfma_f32_16x16x32_bf16 v[150:153], a[12:15], v[168:171], v[150:153]\n"
      "s_waitcnt lgkmcnt(1)\n"
      "v_cvt_pk_bf16_f32 v107, v120, v121\n"
      "v_mov_b32 v106, v159\n"
      "v_mfma_f32_16x16x32_bf16 v[150:153], a[16:19], v[220:223], v[150:153]\n"
      "s_nop 0\n"
      "v_permlane16_swap_b32 v159, v106\n"
      "v_mfma_f32_16x16x32_bf16 v[150:153], a[20:23], v[172:175], v[150:153]\n"
      "v_sub_u32 v85, v85, v77\n"
      "v_mul_f32 v164, v76, v164\n"
      "v_mfma_f32_16x16x32_bf16 v[176:179], a[24:27], v[154:157], 0\n"
      "v_cvt_pk_bf16_f32 v197, v134, v135\n"
      "v_mul_f32 v159, s50, v159\n"
      "v_mfma_f32_16x16x32_bf16 v[176:179], a[28:31], v[108:111], v[176:179]\n"
      "s_and_b32 s47, s47, -4\n"
      "v_mul_f32 v146, v158, v146\n"
      "v_mul_f32 v185, v106, v185\n"
      "v_mfma_f32_16x16x32_bf16 v[176:179], a[32:35], v[126:129], v[176:179]\n"
      "s_waitcnt lgkmcnt(0)\n"
      "v_cvt_pk_bf16_f32 v196, v132, v133\n"
      "v_mul_f32 v214, v76, v214\n"
      "v_mfma_f32_16x16x32_bf16 v[176:179], a[36:39], v[168:171], v[176:179]\n"
      "buffer_store_dwordx4 v[140:143], v20, s[24:27], s85 offen sc1\n"
      "v_pk_mul_f32 v[206:207], v[206:207], v[158:159] op_sel:[0,1]\n"
      "v_pk_mul_f32 v[138:139], v[138:139], v[106:107] op_sel_hi:[1,0]\n"
      "v_pk_mul_f32 v[162:163], v[162:163], v[106:107] op_sel_hi:[1,0]\n"
      "v_pk_mul_f32 v[122:123], v[122:123], v[76:77] op_sel_hi:[1,0]\n"
      "v_pk_mul_f32 v[186:187], v[186:187], v[106:107] op_sel_hi:[1,0]\n"
      "v_pk_mul_f32 v[204:205], v[204:205], v[158:159] op_sel_hi:[1,0]\n"
      "v_pk_mul_f32 v[124:125], v[124:125], v[76:77] op_sel_hi:[1,0]\n"
      "v_pk_mul_f32 v[180:181], v[180:181], v[158:159] op_sel:[0,1]\n"
      "v_mfma_f32_16x16x32_bf16 v[176:179], a[40:43], v[220:223], v[176:179]\n"
      "v_mul_f32 v166, v76, v166\n"
      "v_mul_f32 v201, v158, v201\n"
      "v_mfma_f32_16x16x32_bf16 v[176:179], a[44:47], v[172:175], v[176:179]\n"
      "s_add_u32 s73, s73, 1024\n"
      "v_mul_f32 v137, v106, v137\n"
      "v_fma_f32 v112, v150, v150, s51\n"
      "v_mfma_f32_16x16x32_bf16 v[132:135], a[48:51], v[154:157], 0\n"
      "v_mul_f32 v200, v158, v200\n"
      "v_cvt_pk_bf16_f32 v144, v144, v145\n"
      "v_mfma_f32_16x16x32_bf16 v[132:135], a[52:55], v[108:111], v[132:135]\n"
      "v_mul_f32 v184, v106, v184\n"
      "v_fmac_f32 v112, v151, v151\n"
      "v_mfma_f32_16x16x32_bf16 v[132:135], a[56:59], v[126:129], v[132:135]\n"
      "v_fmac_f32 v112, v152, v152\n"
      "v_mul_f32 v136, v106, v136\n"
      "v_mfma_f32_16x16x32_bf16 v[132:135], a[60:63], v[168:171], v[132:135]\n"
      "v_add_u32_e64 v78, s77, v78\n"
      "v_cvt_pk_bf16_f32 v145, v146, v147\n"
      "v_mfma_f32_16x16x32_bf16 v[132:135], a[64:67], v[220:223], v[132:135]\n"
      "v_lshlrev_b32 v85, 4, v85\n"
      "v_mul_f32 v182, v159, v182\n"
      "v_mfma_f32_16x16x32_bf16 v[132:135], a[68:71], v[172:175], v[132:135]\n"
      "v_cvt_pk_bf16_f32 v213, v214, v215\n"
      "v_cvt_pk_bf16_f32 v180, v180, v181\n"
      "v_mfma_f32_16x16x32_bf16 v[216:219], a[72:75], v[154:157], 0\n"
      "v_mul_f32 v183, v159, v183\n"
      "v_fmac_f32 v112, v153, v153\n"
      "v_mfma_f32_16x16x32_bf16 v[216:219], a[76:79], v[108:111], v[216:219]\n"
      "v_cvt_pk_bf16_f32 v214, v122, v123\n"
      "v_fmac_f32 v112, v176, v176\n"
      "v_mfma_f32_16x16x32_bf16 v[216:219], a[80:83], v[126:129], v[216:219]\n"
      "v_mul_f32 v208, v159, v208\n"
      "v_fmac_f32_e64 v112, v177, v177\n"
      "v_mfma_f32_16x16x32_bf16 v[216:219], a[84:87], v[168:171], v[216:219]\n"
      "v_mul_f32 v161, v106, v161\n"
      "v_mul_f32 v203, v158, v203\n"
      "v_mfma_f32_16x16x32_bf16 v[216:219], a[88:91], v[220:223], v[216:219]\n"
      "v_mul_f32_e64 v160, v106, v160\n"
      "v_cvt_pk_bf16_f32 v136, v136, v137\n"
      "v_mfma_f32_16x16x32_bf16 v[216:219], a[92:95], v[172:175], v[216:219]\n"
      "s_cmp_ge_u32 s95, 0x40000000\n"
      "s_cselect_b32 s94, -1, -2\n"
      "v_fmac_f32_e64 v112, v178, v178\n"
      "v_mfma_f32_16x16x32_bf16 v[146:149], a[96:99], v[154:157], 0\n"
      "v_fmac_f32_e64 v112, v179, v179\n"
      "v_cvt_pk_bf16_f32 v199, v200, v201\n"
      "v_mfma_f32_16x16x32_bf16 v[146:149], a[100:103], v[108:111], v[146:149]\n"
      "v_cvt_pk_bf16_f32 v215, v124, v125\n"
      "v_fma_f32 v113, v216, v216, s51\n"
      "v_mfma_f32_16x16x32_bf16 v[146:149], a[104:107], v[126:129], v[146:149]\n"
      "v_cvt_pk_bf16_f32 v106, v118, v119\n"
      "v_fmac_f32_e64 v113, v217, v217\n"
      "v_mfma_f32_16x16x32_bf16 v[146:149], a[108:111], v[168:171], v[146:149]\n"
      "v_cvt_pk_bf16_f32 v99, v192, v193\n"
      "v_cvt_pk_bf16_f32 v200, v202, v203\n"
      "v_mfma_f32_16x16x32_bf16 v[146:149], a[112:115], v[220:223], v[146:149]\n"
      "v_cvt_pk_bf16_f32 v164, v164, v165\n"
      "v_fmac_f32 v112, v132, v132\n"
      "v_mfma_f32_16x16x32_bf16 v[146:149], a[116:119], v[172:175], v[146:149]\n"
      "v_fmac_f32 v112, v133, v133\n"
      "v_cvt_pk_bf16_f32 v201, v204, v205\n"
      "v_mfma_f32_16x16x32_bf16 v[122:125], a[120:123], v[154:157], 0\n"
      "v_cvt_pk_bf16_f32 v181, v182, v183\n"
      "v_mov_b32 v158, 0x3fb9\n"
      "v_mfma_f32_16x16x32_bf16 v[122:125], a[124:127], v[108:111], v[122:125]\n"
      "v_cvt_pk_bf16_f32 v182, v206, v207\n"
      "v_fmac_f32 v112, v134, v134\n"
      "v_mfma_f32_16x16x32_bf16 v[122:125], a[128:131], v[126:129], v[122:125]\n"
      "v_fmac_f32 v113, v218, v218\n"
      "v_fmac_f32 v113, v219, v219\n"
      "v_mfma_f32_16x16x32_bf16 v[122:125], a[132:135], v[168:171], v[122:125]\n"
      "s_waitcnt vmcnt(8)\n"
      "v_fmac_f32 v113, v146, v146\n"
      "v_mul_f32 v209, v159, v209\n"
      "v_mfma_f32_16x16x32_bf16 v[122:125], a[136:139], v[220:223], v[122:125]\n"
      "v_cvt_pk_bf16_f32 v98, v190, v191\n"
      "v_fmac_f32 v112, v135, v135\n"
      "v_mfma_f32_16x16x32_bf16 v[122:125], a[140:143], v[172:175], v[122:125]\n"
      "v_fmac_f32 v113, v147, v147\n"
      "v_fmac_f32 v113, v148, v148\n"
      "v_fmac_f32 v113, v149, v149\n"
      "v_cvt_pk_bf16_f32 v183, v208, v209\n"
      "v_and_b32_e64 v93, 2, v78\n"
      "v_pk_mul_f32 v[100:101], v[100:101], v[158:159] op_sel:[0,1]\n"
      "v_pk_mul_f32 v[102:103], v[102:103], v[158:159] op_sel:[0,1]\n"
      "v_mfma_f32_16x16x32_bf16 v[140:143], v[154:157], a[144:147], 0\n"
      "v_fmac_f32 v113, v122, v122\n"
      "v_fmac_f32 v113, v123, v123\n"
      "v_fmac_f32 v113, v124, v124\n"
      "v_fmac_f32 v113, v125, v125\n"
      "v_mfma_f32_16x16x32_bf16 v[140:143], v[108:111], a[148:151], v[140:143]\n"
      "s_and_b32 s4, s45, s8\n"
      "v_permlane16_swap_b32 v112, v113\n"
      "v_mfma_f32_16x16x32_bf16 v[140:143], v[126:129], a[152:155], v[140:143]\n"
      "v_add_f32 v112, v112, v113\n"
      "v_mov_b32 v113, v112\n"
      "v_mfma_f32_16x16x32_bf16 v[140:143], v[168:171], a[156:159], v[140:143]\n"
      "s_nop 0\n"
      "v_permlane32_swap_b32 v112, v113\n"
      "v_mfma_f32_16x16x32_bf16 v[140:143], v[220:223], a[160:163], v[140:143]\n"
      "v_add_f32 v112, v112, v113\n"
      "v_rsq_f32 v112, v112\n"
      "v_mfma_f32_16x16x32_bf16 v[140:143], v[172:175], a[164:167], v[140:143]\n"
      "s_cmp_gt_u32 s42, s44\n"
      "s_cselect_b32 s95, s94, s95\n"
      "v_mov_b32 v113, v112\n"
      "v_mfma_f32_16x16x32_bf16 v[206:209], v[154:157], a[168:171], 0\n"
      "v_cvt_pk_bf16_f32 v165, v166, v167\n"
      "v_permlane16_swap_b32 v112, v113\n"
      "v_cvt_pk_bf16_f32 v160, v160, v161\n"
      "v_mul_f32_e64 v112, s50, v112\n"
      "v_pk_mul_f32 v[122:123], v[122:123], v[112:113] op_sel:[0,1]\n"
      "v_pk_mul_f32 v[132:133], v[132:133], v[112:113] op_sel_hi:[1,0]\n"
      "v_pk_mul_f32 v[178:179], v[178:179], v[112:113] op_sel_hi:[1,0]\n"
      "v_pk_mul_f32 v[216:217], v[216:217], v[112:113] op_sel:[0,1]\n"
      "v_pk_mul_f32 v[152:153], v[152:153], v[112:113] op_sel_hi:[1,0]\n"
      "v_pk_mul_f32 v[148:149], v[148:149], v[112:113] op_sel:[0,1]\n"
      "v_pk_mul_f32 v[218:219], v[218:219], v[112:113] op_sel:[0,1]\n"
      "v_pk_mul_f32 v[150:151], v[150:151], v[112:113] op_sel_hi:[1,0]\n"
      "v_mfma_f32_16x16x32_bf16 v[206:209], v[108:111], a[172:175], v[206:209]\n"
      "v_cvt_pk_bf16_f32 v137, v138, v139\n"
      "v_mul_f32_e64 v135, v112, v135\n"
      "v_mfma_f32_16x16x32_bf16 v[206:209], v[126:129], a[176:179], v[206:209]\n"
      "ds_read_b128 v[118:121], v2\n"
      "v_cvt_pk_bf16_f32 v77, v142, v143\n"
      "v_mfma_f32_16x16x32_bf16 v[206:209], v[168:171], a[180:183], v[206:209]\n"
      "v_cvt_pk_bf16_f32 v161, v162, v163\n"
      "v_cvt_pk_bf16_f32 v138, v184, v185\n"
      "v_mfma_f32_16x16x32_bf16 v[206:209], v[220:223], a[184:187], v[206:209]\n"
      "ds_read_b128 v[188:191], v2 offset:6144\n"
      "v_mov_b32_e64 v159, 0\n"
      "v_mfma_f32_16x16x32_bf16 v[206:209], v[172:175], a[188:191], v[206:209]\n"
      "v_cvt_pk_bf16_f32 v76, v140, v141\n"
      "v_and_b32_e64 v78, -4, v78\n"
      "v_mfma_f32_16x16x32_bf16 v[202:205], v[154:157], a[192:195], 0\n"
      "ds_read_b128 v[154:157], v2 offset:3072\n"
      "v_cvt_pk_bf16_f32 v139, v186, v187\n"
      "v_mfma_f32_16x16x32_bf16 v[202:205], v[108:111], a[196:199], v[202:205]\n"
      "ds_read_b128 v[140:143], v7 offset:4096\n"
      "v_mul_f32 v134, v112, v134\n"
      "v_mfma_f32_16x16x32_bf16 v[202:205], v[126:129], a[200:203], v[202:205]\n"
      "ds_read_b128 v[108:111], v2 offset:9216\n"
      "v_cvt_pk_bf16_f32 v216, v216, v217\n"
      "v_mfma_f32_16x16x32_bf16 v[202:205], v[168:171], a[204:207], v[202:205]\n"
      "s_waitcnt lgkmcnt(4)\n"
      "v_cvt_pk_bf16_f32 v100, v100, v101\n"
      "v_cvt_pk_bf16_f32 v81, v208, v209\n"
      "v_mfma_f32_16x16x32_bf16 v[202:205], v[220:223], a[208:211], v[202:205]\n"
      "ds_read_b128 v[168:171], v2 offset:12288\n"
      "v_mov_b32_e64 v167, 0\n"
      "v_mfma_f32_16x16x32_bf16 v[202:205], v[172:175], a[212:215], v[202:205]\n"
      "ds_read_b128 v[208:211], v2 offset:15360\n"
      "v_mul_f32 v146, v113, v146\n"
      "v_mfma_f32_16x16x32_bf16 v[172:175], a[216:219], v[118:121], 0\n"
      "s_waitcnt lgkmcnt(4)\n"
      "v_mul_f32 v147, v113, v147\n"
      "v_add_u32 v187, 110592, v78\n"
      "v_mfma_f32_16x16x32_bf16 v[172:175], a[220:223], v[154:157], v[172:175]\n"
      "s_waitcnt lgkmcnt(3)\n"
      "v_cvt_pk_bf16_f32 v217, v218, v219\n"
      "v_cvt_pk_bf16_f32 v132, v132, v133\n"
      "v_mfma_f32_16x16x32_bf16 v[172:175], a[224:227], v[188:191], v[172:175]\n"
      "buffer_store_dwordx4 v[140:143], v21, s[24:27], s85 offen sc1\n"
      "v_mfma_f32_16x16x32_bf16 v[220:223], a[240:243], v[118:121], 0\n"
      "ds_read2_b32 v[162:163], v187 offset0:2 offset1:10\n"
      "v_cvt_pk_bf16_f32 v84, v202, v203\n"
      "v_mfma_f32_16x16x32_bf16 v[220:223], a[244:247], v[154:157], v[220:223]\n"
      "s_cmp_ge_u32 s95, 0x40000000\n"
      "s_cselect_b64 vcc, -1, s[56:57]\n"
      "v_lshlrev_b64 v[158:159], v85, v[158:159]\n"
      "v_mfma_f32_16x16x32_bf16 v[220:223], a[248:251], v[188:191], v[220:223]\n"
      "s_waitcnt lgkmcnt(1)\n"
      "v_cvt_pk_bf16_f32 v219, v148, v149\n"
      "v_mul_f32 v177, v112, v177\n"
      "v_mfma_f32_16x16x32_bf16 v[140:143], v[232:235], v[118:121], 0\n"
      "ds_read2_b32 v[202:203], v187 offset0:16 offset1:17\n"
      "v_cvt_pk_bf16_f32 v80, v206, v207\n"
      "v_mfma_f32_16x16x32_bf16 v[140:143], v[236:239], v[154:157], v[140:143]\n"
      "ds_read_b128 v[118:121], v7 offset:8192\n"
      "v_cvt_pk_bf16_f32 v150, v150, v151\n"
      "v_mfma_f32_16x16x32_bf16 v[140:143], v[240:243], v[188:191], v[140:143]\n"
      "ds_read2_b32 v[184:185], v187 offset0:0 offset1:1\n"
      "v_cmp_gt_u32 s[72:73], 64, v85\n"
      "v_mfma_f32_16x16x32_bf16 v[172:175], a[228:231], v[108:111], v[172:175]\n"
      "v_cvt_pk_bf16_f32 v218, v146, v147\n"
      "v_cvt_pk_bf16_f32 v151, v152, v153\n"
      "v_mfma_f32_16x16x32_bf16 v[172:175], a[232:235], v[168:171], v[172:175]\n"
      "v_cvt_pk_bf16_f32 v133, v134, v135\n"
      "v_cndmask_b32_e64 v158, 0, v158, s[72:73]\n"
      "v_mfma_f32_16x16x32_bf16 v[172:175], a[236:239], v[208:211], v[172:175]\n"
      "ds_read_b128 v[146:149], v10 offset:1200\n"
      "v_cvt_pk_bf16_f32 v101, v102, v103\n"
      "v_mfma_f32_16x16x32_bf16 v[220:223], a[252:255], v[108:111], v[220:223]\n"
      "ds_read_b64 v[128:129], v10 offset:1192\n"
      "v_mul_f32_e64 v125, v113, v125\n"
      "v_mfma_f32_16x16x32_bf16 v[220:223], v[224:227], v[168:171], v[220:223]\n"
      "ds_read_b128 v[154:157], v7 offset:12288\n"
      "v_mov_b32 v166, 0xbb2b\n"
      "v_mfma_f32_16x16x32_bf16 v[220:223], v[228:231], v[208:211], v[220:223]\n"
      "ds_read2_b32 v[102:103], v187 offset0:8 offset1:9\n"
      "v_mul_f32 v124, v113, v124\n"
      "v_mfma_f32_16x16x32_bf16 v[140:143], v[244:247], v[108:111], v[140:143]\n"
      "s_waitcnt lgkmcnt(4)\n"
      "v_mul_f32 v176, v112, v176\n"
      "v_cvt_pk_bf16_f32 v153, v178, v179\n"
      "v_mfma_f32_16x16x32_bf16 v[140:143], v[248:251], v[168:171], v[140:143]\n"
      "buffer_store_dwordx4 v[118:121], v22, s[24:27], s85 offen sc1\n"
      "v_mfma_f32_16x16x32_bf16 v[140:143], v[252:255], v[208:211], v[140:143]\n"
      "ds_read2_b32 v[178:179], v187 offset0:24 offset1:18\n"
      "v_cndmask_b32_e64 v159, 0, v159, s[72:73]\n"
      "v_mfma_f32_16x16x32_bf16 v[118:121], v[198:201], v[212:215], v[40:43]\n"
      "v_alignbyte_b32 v184, v185, v184, v93\n"
      "v_cmp_gt_u32 s[72:73], 64, v85\n"
      "v_mfma_f32_16x16x32_bf16 v[168:171], v[136:139], v[212:215], v[44:47]\n"
      "s_waitcnt lgkmcnt(1)\n"
      "v_lshlrev_b64 v[166:167], v85, v[166:167]\n"
      "v_cvt_pk_bf16_f32 v122, v122, v123\n"
      "v_mfma_f32_16x16x32_bf16 v[190:193], v[216:219], v[212:215], v[48:51]\n"
      "ds_read_b128 v[186:189], v2 offset:256\n"
      "v_cvt_pk_bf16_f32 v123, v124, v125\n"
      "v_mfma_f32_16x16x32_bf16 v[124:127], v[146:149], v[212:215], v[24:27]\n"
      "ds_read_b128 v[206:209], v7 offset:16384\n"
      "v_alignbyte_b32 v202, v203, v202, v93\n"
      "v_mfma_f32_16x16x16_bf16 v[118:121], v[144:145], v[164:165], v[118:121]\n"
      "v_fma_f32 v112, v172, v172, s52\n"
      "v_alignbyte_b32 v102, v103, v102, v93\n"
      "v_add_u32 v130, 115296, v130\n"
      "v_mfma_f32_16x16x16_bf16 v[168:171], v[160:161], v[164:165], v[168:171]\n"
      "v_cvt_pk_bf16_f32 v152, v176, v177\n"
      "v_cndmask_b32_e64 v166, 0, v166, s[72:73]\n"
      "v_mul_f32_e64 v113, v173, v173\n"
      "v_mfma_f32_16x16x16_bf16 v[190:193], v[122:123], v[164:165], v[190:193]\n"
      "buffer_store_dwordx4 v[154:157], v23, s[24:27], s85 offen sc1\n"
      "v_mfma_f32_16x16x16_bf16 v[124:127], v[128:129], v[164:165], v[124:127]\n"
      "v_alignbyte_b32 v185, v162, v185, v93\n"
      "v_alignbyte_b32 v103, v163, v103, v93\n"
      "v_fmac_f32 v112, v174, v174\n"
      "v_mfma_f32_16x16x16_bf16 v[118:121], v[158:159], v[184:185], v[118:121]\n"
      "s_waitcnt lgkmcnt(0)\n"
      "v_alignbyte_b32 v203, v179, v203, v93\n"
      "v_add_u32 v154, 112160, v78\n"
      "v_mfma_f32_16x16x16_bf16 v[168:171], v[158:159], v[102:103], v[168:171]\n"
      "ds_read_b128 v[162:165], v2 offset:3328\n"
      "v_alignbyte_b32 v178, v178, v178, v93\n"
      "v_mfma_f32_16x16x16_bf16 v[190:193], v[158:159], v[202:203], v[190:193]\n"
      "ds_read2_b32 v[134:135], v154 offset0:8 offset1:9\n"
      "v_cndmask_b32_e64 v167, 0, v167, s[72:73]\n"
      "v_mfma_f32_16x16x16_bf16 v[124:127], v[158:159], v[178:179], v[124:127]\n"
      "ds_read2_b32 v[214:215], v154 offset0:0 offset1:1\n"
      "v_cvt_pk_bf16_f32 v85, v204, v205\n"
      "v_mfma_f32_16x16x16_bf16 v[118:121], v[166:167], v[184:185], v[118:121]\n"
      "ds_read_b128 v[210:213], v2 offset:6400\n"
      "v_fmac_f32_e64 v113, v175, v175\n"
      "v_mfma_f32_16x16x16_bf16 v[168:171], v[166:167], v[102:103], v[168:171]\n"
      "ds_read2_b32 v[108:109], v154 offset0:2 offset1:10\n"
      "v_fmac_f32_e64 v113, v221, v221\n"
      "v_mfma_f32_16x16x16_bf16 v[190:193], v[166:167], v[202:203], v[190:193]\n"
      "buffer_store_dwordx4 v[206:209], v25, s[24:27], s85 offen sc1\n"
      "v_mfma_f32_16x16x16_bf16 v[124:127], v[166:167], v[178:179], v[124:127]\n"
      "v_fmac_f32_e64 v112, v220, v220\n"
      "v_add_u32 v184, 113728, v78\n"
      "v_max3_f32 v203, v118, v119, v120\n"
      "v_mfma_f32_16x16x32_bf16 v[206:209], a[216:219], v[186:189], 0\n"
      "s_waitcnt lgkmcnt(2)\n"
      "v_alignbyte_b32 v134, v135, v134, v93\n"
      "v_max3_f32 v203, v203, v170, v171\n"
      "v_mfma_f32_16x16x32_bf16 v[206:209], a[220:223], v[162:165], v[206:209]\n"
      "s_waitcnt lgkmcnt(1)\n"
      "v_max3_f32 v103, v121, v168, v169\n"
      "v_max3_f32 v203, v203, v192, v193\n"
      "v_mfma_f32_16x16x32_bf16 v[206:209], a[224:227], v[210:213], v[206:209]\n"
      "ds_read_u16 v130, v130\n"
      "v_max3_f32 v103, v103, v190, v191\n"
      "v_mfma_f32_16x16x32_bf16 v[176:179], a[240:243], v[186:189], 0\n"
      "s_waitcnt lgkmcnt(1)\n"
      "v_max3_f32 v203, v203, v103, v124\n"
      "v_alignbyte_b32 v214, v215, v214, v93\n"
      "v_mfma_f32_16x16x32_bf16 v[176:179], a[244:247], v[162:165], v[176:179]\n"
      "ds_read2_b32 v[102:103], v154 offset0:16 offset1:17\n"
      "v_alignbyte_b32 v135, v109, v135, v93\n"
      "v_mfma_f32_16x16x32_bf16 v[176:179], a[248:251], v[210:213], v[176:179]\n"
      "s_waitcnt vmcnt(6)\n"
      "v_or3_b32 v114, v114, v115, v116\n"
      "v_alignbyte_b32 v215, v108, v215, v93\n"
      "v_mfma_f32_16x16x32_bf16 v[108:111], v[232:235], v[186:189], 0\n"
      "ds_read2_b32 v[188:189], v154 offset0:24 offset1:18\n"
      "v_mov_b32_e64 v154, v203\n"
      "v_mfma_f32_16x16x32_bf16 v[108:111], v[236:239], v[162:165], v[108:111]\n"
      "s_nop 0\n"
      "v_permlane16_swap_b32 v203, v154\n"
      "v_mfma_f32_16x16x32_bf16 v[108:111], v[240:243], v[210:213], v[108:111]\n"
      "v_max_f32 v203, v203, v154\n"
      "v_mov_b32 v154, v203\n"
      "v_mfma_f32_16x16x32_bf16 v[162:165], v[198:201], v[180:183], v[52:55]\n"
      "s_nop 0\n"
      "v_permlane32_swap_b32 v203, v154\n"
      "v_mfma_f32_16x16x32_bf16 v[210:213], v[136:139], v[180:183], v[56:59]\n"
      "s_waitcnt lgkmcnt(1)\n"
      "v_max_f32 v203, v203, v154\n"
      "v_sub_f32 v168, v168, v203\n"
      "v_alignbyte_b32 v102, v103, v102, v93\n"
      "v_lshlrev_b32_e64 v130, 16, v130\n"
      "s_waitcnt vmcnt(5)\n"
      "v_or3_b32 v114, v114, v117, v86\n"
      "v_pk_add_f32 v[190:191], v[190:191], v[202:203] op_sel:[0,1] neg_lo:[0,1] neg_hi:[0,1]\n"
      "v_pk_add_f32 v[120:121], v[120:121], v[202:203] op_sel:[0,1] neg_lo:[0,1] neg_hi:[0,1]\n"
      "v_pk_fma_f32 v[112:113], v[222:223], v[222:223], v[112:113]\n"
      "v_pk_add_f32 v[118:119], v[118:119], v[202:203] op_sel:[0,1] neg_lo:[0,1] neg_hi:[0,1]\n"
      "v_pk_add_f32 v[192:193], v[192:193], v[202:203] op_sel:[0,1] neg_lo:[0,1] neg_hi:[0,1]\n"
      "v_pk_add_f32 v[170:171], v[170:171], v[202:203] op_sel:[0,1] neg_lo:[0,1] neg_hi:[0,1]\n"
      "v_mfma_f32_16x16x32_bf16 v[154:157], v[216:219], v[180:183], v[60:63]\n"
      "ds_write_b32 v31, v114 offset:5632\n"
      "v_mfma_f32_16x16x32_bf16 v[114:117], v[146:149], v[180:183], v[26:29]\n"
      "s_waitcnt lgkmcnt(1)\n"
      "v_exp_f32 v192, v192\n"
      "v_mfma_f32_16x16x16_bf16 v[162:165], v[144:145], v[100:101], v[162:165]\n"
      "v_exp_f32_e64 v119, v119\n"
      "v_alignbyte_b32 v103, v189, v103, v93\n"
      "v_mfma_f32_16x16x16_bf16 v[210:213], v[160:161], v[100:101], v[210:213]\n"
      "v_exp_f32 v190, v190\n"
      "v_sub_f32 v124, v124, v203\n"
      "v_mfma_f32_16x16x16_bf16 v[154:157], v[122:123], v[100:101], v[154:157]\n"
      "v_alignbyte_b32 v188, v188, v188, v93\n"
      "v_exp_f32 v170, v170\n"
      "v_mfma_f32_16x16x16_bf16 v[114:117], v[128:129], v[100:101], v[114:117]\n"
      "v_exp_f32 v191, v191\n"
      "v_exp_f32_e64 v118, v118\n"
      "v_mfma_f32_16x16x16_bf16 v[162:165], v[158:159], v[214:215], v[162:165]\n"
      "v_cvt_pk_bf16_f32 v88, v190, v191\n"
      "v_exp_f32_e64 v193, v193\n"
      "v_mfma_f32_16x16x16_bf16 v[210:213], v[158:159], v[134:135], v[210:213]\n"
      "v_cvt_pk_bf16_f32 v118, v118, v119\n"
      "v_exp_f32 v120, v120\n"
      "v_mfma_f32_16x16x16_bf16 v[154:157], v[158:159], v[102:103], v[154:157]\n"
      "v_exp_f32 v121, v121\n"
      "v_cvt_pk_bf16_f32 v89, v192, v193\n"
      "v_mfma_f32_16x16x16_bf16 v[114:117], v[158:159], v[188:189], v[114:117]\n"
      "ds_read_b128 v[180:183], v2 offset:9472\n"
      "v_sub_f32_e64 v169, v169, v203\n"
      "v_mfma_f32_16x16x16_bf16 v[162:165], v[166:167], v[214:215], v[162:165]\n"
      "ds_read_b128 v[202:205], v2 offset:12544\n"
      "v_exp_f32 v169, v169\n"
      "v_mfma_f32_16x16x16_bf16 v[210:213], v[166:167], v[134:135], v[210:213]\n"
      "v_exp_f32 v168, v168\n"
      "v_exp_f32_e64 v171, v171\n"
      "v_mfma_f32_16x16x16_bf16 v[154:157], v[166:167], v[102:103], v[154:157]\n"
      "ds_read_b128 v[100:103], v2 offset:15616\n"
      "v_cvt_pk_bf16_f32 v119, v120, v121\n"
      "v_cvt_pk_bf16_f32 v121, v170, v171\n"
      "v_mfma_f32_16x16x16_bf16 v[114:117], v[166:167], v[188:189], v[114:117]\n"
      "v_exp_f32_e64 v124, v124\n"
      "v_max3_f32 v86, v165, v210, v211\n"
      "v_max3_f32 v134, v162, v163, v164\n"
      "v_max3_f32 v134, v134, v212, v213\n"
      "v_max3_f32 v134, v134, v156, v157\n"
      "v_pk_fma_f32 v[112:113], v[140:141], v[140:141], v[112:113]\n"
      "v_max3_f32 v86, v86, v154, v155\n"
      "v_pk_fma_f32 v[112:113], v[142:143], v[142:143], v[112:113]\n"
      "v_max3_f32 v134, v134, v86, v114\n"
      "v_cvt_pk_bf16_f32 v90, v124, 0\n"
      "v_mov_b32 v117, v134\n"
      "s_waitcnt lgkmcnt(1)\n"
      "v_mfma_f32_16x16x32_bf16 v[206:209], a[228:231], v[180:183], v[206:209]\n"
      "ds_read_u16 v78, v11 offset:1168\n"
      "v_permlane16_swap_b32 v134, v117\n"
      "v_mfma_f32_16x16x32_bf16 v[206:209], a[232:235], v[202:205], v[206:209]\n"
      "s_waitcnt lgkmcnt(1)\n"
      "v_max_f32 v134, v134, v117\n"
      "v_mov_b32 v117, v134\n"
      "v_mfma_f32_16x16x32_bf16 v[206:209], a[236:239], v[100:103], v[206:209]\n"
      "ds_read_u16 v86, v11 offset:1232\n"
      "v_cvt_pk_bf16_f32 v120, v168, v169\n"
      "v_mfma_f32_16x16x32_bf16 v[176:179], a[252:255], v[180:183], v[176:179]\n"
      "ds_read_u16 v82, v11 offset:1200\n"
      "v_permlane32_swap_b32 v134, v117\n"
      "v_max_f32 v134, v134, v117\n"
      "v_sub_f32_e64 v114, v114, v134\n"
      "v_pk_add_f32 v[156:157], v[156:157], v[134:135] op_sel_hi:[1,0] neg_lo:[0,1] neg_hi:[0,1]\n"
      "v_pk_add_f32 v[162:163], v[162:163], v[134:135] op_sel_hi:[1,0] neg_lo:[0,1] neg_hi:[0,1]\n"
      "v_pk_add_f32 v[210:211], v[210:211], v[134:135] op_sel_hi:[1,0] neg_lo:[0,1] neg_hi:[0,1]\n"
      "v_pk_add_f32 v[154:155], v[154:155], v[134:135] op_sel_hi:[1,0] neg_lo:[0,1] neg_hi:[0,1]\n"
      "v_pk_add_f32 v[164:165], v[164:165], v[134:135] op_sel_hi:[1,0] neg_lo:[0,1] neg_hi:[0,1]\n"
      "v_pk_add_f32 v[212:213], v[212:213], v[134:135] op_sel_hi:[1,0] neg_lo:[0,1] neg_hi:[0,1]\n"
      "v_mfma_f32_16x16x32_bf16 v[176:179], v[224:227], v[202:205], v[176:179]\n"
      "ds_read2_b32 v[186:187], v184 offset0:0 offset1:1\n"
      "v_add_f32 v112, v112, v113\n"
      "v_mfma_f32_16x16x32_bf16 v[176:179], v[228:231], v[100:103], v[176:179]\n"
      "v_exp_f32 v162, v162\n"
      "v_mfma_f32_16x16x32_bf16 v[108:111], v[244:247], v[180:183], v[108:111]\n"
      "v_exp_f32_e64 v165, v165\n"
      "v_mfma_f32_16x16x32_bf16 v[108:111], v[248:251], v[202:205], v[108:111]\n"
      "ds_read2_b32 v[134:135], v184 offset0:8 offset1:9\n"
      "v_exp_f32_e64 v210, v210\n"
      "v_mfma_f32_16x16x32_bf16 v[108:111], v[252:255], v[100:103], v[108:111]\n"
      "ds_read2_b32 v[116:117], v184 offset0:2 offset1:10\n"
      "v_exp_f32_e64 v163, v163\n"
      "v_mfma_f32_16x16x32_bf16 v[168:171], v[32:35], v[118:121], 0\n"
      "ds_read2_b32 v[214:215], v184 offset0:16 offset1:17\n"
      "v_cvt_pk_bf16_f32 v162, v162, v163\n"
      "v_mfma_f32_16x16x32_bf16 v[168:171], v[32:35], v[88:91], v[168:171]\n"
      "ds_read2_b32 v[192:193], v184 offset0:24 offset1:18\n"
      "v_exp_f32_e64 v164, v164\n"
      "v_mfma_f32_16x16x32_bf16 v[180:183], v[194:197], v[118:121], 0\n"
      "s_waitcnt lgkmcnt(4)\n"
      "v_exp_f32 v213, v213\n"
      "v_mfma_f32_16x16x32_bf16 v[180:183], v[76:79], v[88:91], v[180:183]\n"
      "v_alignbyte_b32 v186, v187, v186, v93\n"
      "v_cvt_pk_bf16_f32 v163, v164, v165\n"
      "v_mfma_f32_16x16x32_bf16 v[100:103], v[104:107], v[118:121], 0\n"
      "v_exp_f32 v211, v211\n"
      "v_mfma_f32_16x16x32_bf16 v[100:103], v[80:83], v[88:91], v[100:103]\n"
      "s_waitcnt lgkmcnt(2)\n"
      "v_alignbyte_b32 v187, v116, v187, v93\n"
      "v_cvt_pk_bf16_f32 v164, v210, v211\n"
      "v_mfma_f32_16x16x32_bf16 v[202:205], v[96:99], v[118:121], 0\n"
      "s_waitcnt lgkmcnt(1)\n"
      "v_exp_f32 v212, v212\n"
      "v_mfma_f32_16x16x32_bf16 v[202:205], v[84:87], v[88:91], v[202:205]\n"
      "v_alignbyte_b32 v214, v215, v214, v93\n"
      "v_alignbyte_b32 v134, v135, v134, v93\n"
      "v_mfma_f32_16x16x32_bf16 v[188:191], v[198:201], v[150:153], v[64:67]\n"
      "s_waitcnt lgkmcnt(0)\n"
      "v_cvt_pk_bf16_f32 v165, v212, v213\n"
      "v_alignbyte_b32 v192, v192, v192, v93\n"
      "v_mfma_f32_16x16x32_bf16 v[210:213], v[136:139], v[150:153], v[68:71]\n"
      "v_alignbyte_b32 v215, v193, v215, v93\n"
      "v_alignbyte_b32 v135, v117, v135, v93\n"
      "v_mfma_f32_16x16x32_bf16 v[124:127], v[216:219], v[150:153], v[72:75]\n"
      "v_exp_f32 v156, v156\n"
      "v_mfma_f32_16x16x32_bf16 v[116:119], v[146:149], v[150:153], v[28:31]\n"
      "v_rcp_f32 v168, v168\n"
      "v_mfma_f32_16x16x16_bf16 v[188:191], v[144:145], v[132:133], v[188:191]\n"
      "v_exp_f32 v155, v155\n"
      "v_mul_f32 v101, v168, v101\n"
      "v_mfma_f32_16x16x16_bf16 v[210:213], v[160:161], v[132:133], v[210:213]\n"
      "v_exp_f32 v157, v157\n"
      "v_mul_f32 v102, v168, v102\n"
      "v_mfma_f32_16x16x16_bf16 v[124:127], v[122:123], v[132:133], v[124:127]\n"
      "v_mul_f32 v183, v168, v183\n"
      "v_mul_f32 v180, v168, v180\n"
      "v_mul_f32 v100, v168, v100\n"
      "v_mfma_f32_16x16x16_bf16 v[116:119], v[128:129], v[132:133], v[116:119]\n"
      "v_exp_f32 v154, v154\n"
      "v_cvt_pk_bf16_f32 v93, v156, v157\n"
      "v_mfma_f32_16x16x16_bf16 v[188:191], v[158:159], v[186:187], v[188:191]\n"
      "v_mul_f32 v202, v168, v202\n"
      "v_exp_f32_e64 v114, v114\n"
      "v_mfma_f32_16x16x16_bf16 v[210:213], v[158:159], v[134:135], v[210:213]\n"
      "v_mul_f32 v182, v168, v182\n"
      "v_mul_f32 v181, v168, v181\n"
      "v_cvt_pk_bf16_f32 v180, v180, v181\n"
      "v_mfma_f32_16x16x16_bf16 v[124:127], v[158:159], v[214:215], v[124:127]\n"
      "v_cvt_pk_bf16_f32 v181, v182, v183\n"
      "v_mul_f32_e64 v203, v168, v203\n"
      "v_cvt_pk_bf16_f32 v182, v100, v101\n"
      "v_mfma_f32_16x16x16_bf16 v[116:119], v[158:159], v[192:193], v[116:119]\n"
      "ds_read_b128 v[150:153], v2 offset:512\n"
      "v_mul_f32_e64 v103, v168, v103\n"
      "v_mfma_f32_16x16x16_bf16 v[188:191], v[166:167], v[186:187], v[188:191]\n"
      "ds_read_b128 v[184:187], v2 offset:3584\n"
      "v_cvt_pk_bf16_f32 v183, v102, v103\n"
      "v_mfma_f32_16x16x16_bf16 v[210:213], v[166:167], v[134:135], v[210:213]\n"
      "v_cvt_pk_bf16_f32 v94, v114, 0\n"
      "v_fma_f32 v114, v206, v206, s52\n"
      "v_mul_f32_e64 v115, v207, v207\n"
      "v_mfma_f32_16x16x16_bf16 v[124:127], v[166:167], v[214:215], v[124:127]\n"
      "ds_read_b128 v[100:103], v2 offset:6656\n"
      "v_max3_f32 v215, v188, v189, v190\n"
      "v_mfma_f32_16x16x16_bf16 v[116:119], v[166:167], v[192:193], v[116:119]\n"
      "v_fmac_f32 v115, v209, v209\n"
      "v_cvt_pk_bf16_f32 v202, v202, v203\n"
      "v_fmac_f32 v114, v208, v208\n"
      "v_max3_f32 v215, v215, v212, v213\n"
      "v_max3_f32 v135, v191, v210, v211\n"
      "v_max3_f32 v135, v135, v124, v125\n"
      "v_pk_fma_f32 v[114:115], v[176:177], v[176:177], v[114:115]\n"
      "v_max3_f32 v215, v215, v126, v127\n"
      "v_max3_f32 v215, v215, v135, v116\n"
      "v_pk_mul_f32 v[204:205], v[204:205], v[168:169] op_sel_hi:[1,0]\n"
      "v_pk_fma_f32 v[114:115], v[178:179], v[178:179], v[114:115]\n"
      "v_mov_b32 v119, v215\n"
      "s_waitcnt lgkmcnt(1)\n"
      "v_mfma_f32_16x16x32_bf16 v[132:135], a[216:219], v[150:153], 0\n"
      "v_permlane16_swap_b32 v215, v119\n"
      "v_max_f32 v215, v215, v119\n"
      "v_mov_b32_e64 v119, v215\n"
      "v_cvt_pk_bf16_f32 v92, v154, v155\n"
      "v_pk_fma_f32 v[114:115], v[108:109], v[108:109], v[114:115]\n"
      "v_permlane32_swap_b32 v215, v119\n"
      "v_max_f32 v215, v215, v119\n"
      "v_pk_fma_f32 v[114:115], v[110:111], v[110:111], v[114:115]\n"
      "v_pk_add_f32 v[190:191], v[190:191], v[214:215] op_sel:[0,1] neg_lo:[0,1] neg_hi:[0,1]\n"
      "v_pk_add_f32 v[188:189], v[188:189], v[214:215] op_sel:[0,1] neg_lo:[0,1] neg_hi:[0,1]\n"
      "v_mfma_f32_16x16x32_bf16 v[132:135], a[220:223], v[184:187], v[132:135]\n"
      "s_waitcnt lgkmcnt(0)\n"
      "v_exp_f32 v191, v191\n"
      "v_mfma_f32_16x16x32_bf16 v[132:135], a[224:227], v[100:103], v[132:135]\n"
      "ds_write_b128 v3, v[180:183] offset:18432\n"
      "v_pk_add_f32 v[210:211], v[210:211], v[214:215] op_sel:[0,1] neg_lo:[0,1] neg_hi:[0,1]\n"
      "v_pk_add_f32 v[126:127], v[126:127], v[214:215] op_sel:[0,1] neg_lo:[0,1] neg_hi:[0,1]\n"
      "v_pk_add_f32 v[124:125], v[124:125], v[214:215] op_sel:[0,1] neg_lo:[0,1] neg_hi:[0,1]\n"
      "v_pk_add_f32 v[212:213], v[212:213], v[214:215] op_sel:[0,1] neg_lo:[0,1] neg_hi:[0,1]\n"
      "v_mfma_f32_16x16x32_bf16 v[118:121], a[240:243], v[150:153], 0\n"
      "v_exp_f32 v188, v188\n"
      "v_mfma_f32_16x16x32_bf16 v[118:121], a[244:247], v[184:187], v[118:121]\n"
      "v_exp_f32 v125, v125\n"
      "v_mfma_f32_16x16x32_bf16 v[118:121], a[248:251], v[100:103], v[118:121]\n"
      "v_exp_f32 v189, v189\n"
      "v_mfma_f32_16x16x32_bf16 v[168:171], v[232:235], v[150:153], 0\n"
      "v_exp_f32 v190, v190\n"
      "v_mfma_f32_16x16x32_bf16 v[168:171], v[236:239], v[184:187], v[168:171]\n"
      "v_cvt_pk_bf16_f32 v188, v188, v189\n"
      "v_cvt_pk_bf16_f32 v203, v204, v205\n"
      "v_mfma_f32_16x16x32_bf16 v[168:171], v[240:243], v[100:103], v[168:171]\n"
      "ds_read_b128 v[152:155], v10 offset:1168\n"
      "v_sub_f32 v116, v116, v215\n"
      "v_mfma_f32_16x16x32_bf16 v[180:183], v[32:35], v[162:165], 0\n"
      "v_exp_f32 v124, v124\n"
      "v_mfma_f32_16x16x32_bf16 v[180:183], v[32:35], v[92:95], v[180:183]\n"
      "v_exp_f32_e64 v212, v212\n"
      "v_mfma_f32_16x16x32_bf16 v[100:103], v[194:197], v[162:165], 0\n"
      "ds_read_b64 v[166:167], v10 offset:1184\n"
      "v_cvt_pk_bf16_f32 v189, v190, v191\n"
      "v_mfma_f32_16x16x32_bf16 v[100:103], v[76:79], v[92:95], v[100:103]\n"
      "ds_write_b64 v4, v[202:203] offset:18432\n"
      "v_mfma_f32_16x16x32_bf16 v[184:187], v[104:107], v[162:165], 0\n"
      "v_exp_f32 v210, v210\n"
      "v_mfma_f32_16x16x32_bf16 v[184:187], v[80:83], v[92:95], v[184:187]\n"
      "v_add_f32 v114, v114, v115\n"
      "v_cvt_pk_bf16_f32 v88, v124, v125\n"
      "v_mfma_f32_16x16x32_bf16 v[202:205], v[96:99], v[162:165], 0\n"
      "s_waitcnt lgkmcnt(2)\n"
      "v_exp_f32 v213, v213\n"
      "v_mfma_f32_16x16x32_bf16 v[202:205], v[84:87], v[92:95], v[202:205]\n"
      "ds_read_b128 v[162:165], v2 offset:9728\n"
      "v_cvt_pk_bf16_f32 v191, v212, v213\n"
      "v_mfma_f32_16x16x32_bf16 v[212:215], v[152:155], v[198:201], v[30:33]\n"
      "v_rcp_f32_e64 v180, v180\n"
      "v_mfma_f32_16x16x32_bf16 v[198:201], v[152:155], v[136:139], v[30:33]\n"
      "s_waitcnt lgkmcnt(2)\n"
      "v_exp_f32 v211, v211\n"
      "v_mfma_f32_16x16x32_bf16 v[156:159], v[152:155], v[216:219], v[30:33]\n"
      "ds_read_b128 v[136:139], v2 offset:12800\n"
      "v_cvt_pk_bf16_f32 v190, v210, v211\n"
      "v_mfma_f32_16x16x32_bf16 v[216:219], v[152:155], v[146:149], v[30:33]\n"
      "ds_read_b128 v[152:155], v2 offset:15872\n"
      "v_mul_f32_e64 v187, v180, v187\n"
      "v_mfma_f32_16x16x16_bf16 v[212:215], v[166:167], v[144:145], v[212:215]\n"
      "v_mul_f32 v185, v180, v185\n"
      "v_permlane16_swap_b32 v112, v114\n"
      "v_mfma_f32_16x16x16_bf16 v[198:201], v[166:167], v[160:161], v[198:201]\n"
      "v_exp_f32 v127, v127\n"
      "v_mul_f32 v102, v180, v102\n"
      "v_mfma_f32_16x16x16_bf16 v[156:159], v[166:167], v[122:123], v[156:159]\n"
      "v_exp_f32_e64 v116, v116\n"
      "v_mul_f32_e64 v103, v180, v103\n"
      "v_mfma_f32_16x16x16_bf16 v[216:219], v[166:167], v[128:129], v[216:219]\n"
      "v_exp_f32_e64 v126, v126\n"
      "s_waitcnt lgkmcnt(2)\n"
      "v_mfma_f32_16x16x32_bf16 v[132:135], a[228:231], v[162:165], v[132:135]\n"
      "s_waitcnt lgkmcnt(1)\n"
      "v_cndmask_b32 v212, v212, v198, s[58:59]\n"
      "v_cvt_pk_bf16_f32 v90, v116, 0\n"
      "v_mfma_f32_16x16x32_bf16 v[132:135], a[232:235], v[136:139], v[132:135]\n"
      "s_waitcnt lgkmcnt(0)\n"
      "v_mul_f32 v100, v180, v100\n"
      "v_mul_f32_e64 v184, v180, v184\n"
      "v_mfma_f32_16x16x32_bf16 v[132:135], a[236:239], v[152:155], v[132:135]\n"
      "v_cndmask_b32 v212, v212, v156, s[60:61]\n"
      "v_cndmask_b32 v212, v212, v216, s[62:63]\n"
      "v_mfma_f32_16x16x32_bf16 v[118:121], a[252:255], v[162:165], v[118:121]\n"
      "v_mul_f32 v101, v180, v101\n"
      "v_fmac_f32_e64 v212, s53, v130\n"
      "v_mfma_f32_16x16x32_bf16 v[118:121], v[224:227], v[136:139], v[118:121]\n"
      "s_nop 0\n"
      "v_max_f32_dpp v166, v212, v212 row_ror:8 row_mask:0xf bank_mask:0xf\n"
      "v_cvt_pk_bf16_f32 v100, v100, v101\n"
      "v_mfma_f32_16x16x32_bf16 v[118:121], v[228:231], v[152:155], v[118:121]\n"
      "v_max_f32_dpp v166, v166, v166 row_ror:4 row_mask:0xf bank_mask:0xf\n"
      "v_mul_f32_e64 v186, v180, v186\n"
      "v_mfma_f32_16x16x32_bf16 v[168:171], v[244:247], v[162:165], v[168:171]\n"
      "v_cvt_pk_bf16_f32 v101, v102, v103\n"
      "v_max_f32_dpp v166, v166, v166 row_ror:2 row_mask:0xf bank_mask:0xf\n"
      "v_mfma_f32_16x16x32_bf16 v[168:171], v[248:251], v[136:139], v[168:171]\n"
      "s_nop 0\n"
      "v_max_f32_dpp v166, v166, v166 row_ror:1 row_mask:0xf bank_mask:0xf\n"
      "v_cvt_pk_bf16_f32 v89, v126, v127\n"
      "v_mfma_f32_16x16x32_bf16 v[168:171], v[252:255], v[152:155], v[168:171]\n"
      "v_mul_f32 v139, v133, v133\n"
      "v_max_f32_dpp v166, v166, v166 row_bcast:15 row_mask:0xa bank_mask:0xf\n"
      "v_mfma_f32_16x16x32_bf16 v[162:165], v[32:35], v[188:191], 0\n"
      "v_cvt_pk_bf16_f32 v102, v184, v185\n"
      "v_cvt_pk_bf16_f32 v103, v186, v187\n"
      "v_mfma_f32_16x16x32_bf16 v[162:165], v[32:35], v[88:91], v[162:165]\n"
      "v_max_f32_dpp v166, v166, v166 row_bcast:31 row_mask:0xc bank_mask:0xf\n"
      "v_add_f32_e64 v112, v112, v114\n"
      "v_mfma_f32_16x16x32_bf16 v[152:155], v[194:197], v[188:191], 0\n"
      "v_readlane_b32 s72, v166, 63\n"
      "v_fma_f32 v138, v132, v132, s52\n"
      "v_mfma_f32_16x16x32_bf16 v[152:155], v[76:79], v[88:91], v[152:155]\n"
      "ds_write_b128 v3, v[100:103] offset:18688\n"
      "v_pk_fma_f32 v[138:139], v[134:135], v[134:135], v[138:139]\n"
      "v_mfma_f32_16x16x32_bf16 v[164:167], v[104:107], v[188:191], 0\n"
      "v_rcp_f32 v162, v162\n"
      "v_subrev_f32 v212, s72, v212\n"
      "v_exp_f32_e64 v212, v212\n"
      "v_pk_fma_f32 v[138:139], v[118:119], v[118:119], v[138:139]\n"
      "v_pk_fma_f32 v[138:139], v[120:121], v[120:121], v[138:139]\n"
      "v_pk_mul_f32 v[152:153], v[152:153], v[162:163] op_sel_hi:[1,0]\n"
      "v_pk_mul_f32 v[154:155], v[154:155], v[162:163] op_sel_hi:[1,0]\n"
      "v_cvt_pk_bf16_f32 v212, v212, v212\n"
      "v_mfma_f32_16x16x32_bf16 v[164:167], v[80:83], v[88:91], v[164:167]\n"
      "ds_write_b16 v13, v212\n"
      "v_mfma_f32_16x16x32_bf16 v[210:213], v[96:99], v[188:191], 0\n"
      "ds_read_b128 v[144:147], v14\n"
      "ds_read_b128 v[198:201], v14 offset:64\n"
      "v_cvt_pk_bf16_f32 v152, v152, v153\n"
      "v_cvt_pk_bf16_f32 v153, v154, v155\n"
      "v_pk_mul_f32 v[202:203], v[202:203], v[180:181] op_sel_hi:[1,0]\n"
      "v_pk_fma_f32 v[138:139], v[168:169], v[168:169], v[138:139]\n"
      "v_pk_mul_f32 v[164:165], v[164:165], v[162:163] op_sel_hi:[1,0]\n"
      "v_pk_mul_f32 v[166:167], v[166:167], v[162:163] op_sel_hi:[1,0]\n"
      "v_cvt_pk_bf16_f32 v155, v166, v167\n"
      "v_cvt_pk_bf16_f32 v154, v164, v165\n"
      "v_cvt_pk_bf16_f32 v202, v202, v203\n"
      "v_mfma_f32_16x16x32_bf16 v[210:213], v[84:87], v[88:91], v[210:213]\n"
      "ds_write_b128 v3, v[152:155] offset:18944\n"
      "v_pk_fma_f32 v[138:139], v[170:171], v[170:171], v[138:139]\n"
      "v_add_f32 v138, v138, v139\n"
      "v_mov_b32 v114, v138\n"
      "v_pk_mul_f32 v[204:205], v[204:205], v[180:181] op_sel_hi:[1,0]\n"
      "v_cvt_pk_bf16_f32 v203, v204, v205\n"
      "v_permlane16_swap_b32 v138, v114\n"
      "v_add_f32 v138, v138, v114\n"
      "s_add_u32 s42, s42, 1\n"
      "ds_write_b64 v4, v[202:203] offset:18688\n"
      "v_permlane32_swap_b32 v112, v138\n"
      "v_pk_mul_f32 v[210:211], v[210:211], v[162:163] op_sel_hi:[1,0]\n"
      "v_add_f32_e64 v112, v112, v138\n"
      "v_pk_mul_f32 v[212:213], v[212:213], v[162:163] op_sel_hi:[1,0]\n"
      "s_add_u32 s43, s43, 1\n"
      "ds_write_b32 v31, v112 offset:1024\n"
      "v_cvt_pk_bf16_f32 v210, v210, v211\n"
      "v_cvt_pk_bf16_f32 v211, v212, v213\n"
      "s_waitcnt lgkmcnt(2)\n"
      "v_mfma_f32_16x16x32_bf16 v[162:165], v[144:147], v[32:35], 0\n"
      "ds_write_b64 v4, v[210:211] offset:18944\n"
      "v_add_u32 v11, 2336, v11\n"
      "v_mfma_f32_16x16x32_bf16 v[162:165], v[198:201], v[32:35], v[162:165]\n"
      "v_add_u32 v10, 2336, v10\n"
      "s_waitcnt lgkmcnt(0)\n"
      "s_barrier\n"
      "s_cbranch_vccz L_sel0_%=\n"
      "L_disp0_%=:\n"
      "s_cmp_lt_u32 s95, 0x40000000\n"
      "s_cbranch_scc1 L_nofix_18_%=\n"
      "s_cmp_eq_u32 s95, -2\n"
      "s_cbranch_scc1 L_tail0_%=\n"
      "s_nop 2\n"
      "s_cmp_eq_u32 s45, 0x20000000\n"
      "s_cselect_b32 s66, 0, s66\n"
      "s_cselect_b32 s48, 0, s48\n"
      "s_lshr_b32 s92, s45, 30\n"
      "s_or_b32 s92, s92, s66\n"
      "s_cbranch_scc0 L_nf_19_%=\n"
      "s_bitcmp1_b32 s45, 31\n"
      "s_cselect_b32 s75, 100270464, 0\n"
      "s_bitcmp1_b32 s45, 30\n"
      "s_cselect_b32 s76, 196224, 0\n"
      "s_and_b32 s74, s45, 0x3fffffff\n"
      "s_cmp_ge_u32 s45, 0x40000000\n"
      "s_cselect_b32 s74, s74, 0\n"
      "s_cselect_b32 s45, 0, s45\n"
      "s_sub_u32 s74, s74, s66\n"
      "s_add_u32 s66, s66, s74\n"
      "s_sub_u32 s75, s75, s67\n"
      "s_add_u32 s67, s67, s75\n"
      "s_sub_u32 s76, s76, s68\n"
      "s_add_u32 s68, s68, s76\n"
      "v_add_u32 v16, s74, v16\n"
      "v_add_u32 v17, s74, v17\n"
      "v_add_u32 v18, s74, v18\n"
      "v_add_u32 v19, s74, v19\n"
      "v_add_u32_e64 v15, s74, v15\n"
      "s_cmp_eq_u32 s75, 0\n"
      "s_cbranch_scc1 L_ax_20_%=\n"
      "v_bfe_i32 v154, v27, 0, 1\n"
      "v_and_b32 v154, s75, v154\n"
      "v_sub_u32 v16, v16, v154\n"
      "v_bfe_i32 v154, v27, 2, 1\n"
      "v_and_b32 v154, s75, v154\n"
      "v_sub_u32 v17, v17, v154\n"
      "v_bfe_i32 v154, v27, 4, 1\n"
      "v_and_b32 v154, s75, v154\n"
      "v_sub_u32 v18, v18, v154\n"
      "v_bfe_i32 v154, v27, 6, 1\n"
      "v_and_b32 v154, s75, v154\n"
      "v_sub_u32 v19, v19, v154\n"
      "v_bfe_i32 v154, v27, 8, 1\n"
      "v_and_b32 v154, s75, v154\n"
      "v_sub_u32 v15, v15, v154\n"
      "L_ax_20_%=:\n"
      "s_cmp_eq_u32 s76, 0\n"
      "s_cbranch_scc1 L_ax_21_%=\n"
      "v_bfe_i32 v154, v27, 1, 1\n"
      "v_and_b32 v154, s76, v154\n"
      "v_sub_u32 v16, v16, v154\n"
      "v_bfe_i32 v154, v27, 3, 1\n"
      "v_and_b32 v154, s76, v154\n"
      "v_sub_u32 v17, v17, v154\n"
      "v_bfe_i32 v154, v27, 5, 1\n"
      "v_and_b32 v154, s76, v154\n"
      "v_sub_u32 v18, v18, v154\n"
      "v_bfe_i32 v154, v27, 7, 1\n"
      "v_and_b32 v154, s76, v154\n"
      "v_sub_u32 v19, v19, v154\n"
      "v_bfe_i32 v154, v27, 9, 1\n"
      "v_and_b32 v154, s76, v154\n"
      "v_sub_u32 v15, v15, v154\n"
      "L_ax_21_%=:\n"
      "s_or_b32 s48, s66, s69\n"
      "s_cmp_lg_u32 s48, 0\n"
      "s_cselect_b32 s48, 0x40000000, 0\n"
      "L_nf_19_%=:\n"
      "s_lshr_b32 s92, s46, 30\n"
      "s_or_b32 s92, s92, s69\n"
      "s_cbranch_scc0 L_nf_22_%=\n"
      "s_bitcmp1_b32 s46, 31\n"
      "s_cselect_b32 s75, 100270464, 0\n"
      "s_bitcmp1_b32 s46, 30\n"
      "s_cselect_b32 s76, 196224, 0\n"
      "s_and_b32 s74, s46, 0x3fffffff\n"
      "s_cmp_ge_u32 s46, 0x40000000\n"
      "s_cselect_b32 s74, s74, 0\n"
      "s_cselect_b32 s46, 0, s46\n"
      "s_sub_u32 s74, s74, s69\n"
      "s_add_u32 s69, s69, s74\n"
      "s_sub_u32 s75, s75, s70\n"
      "s_add_u32 s70, s70, s75\n"
      "s_sub_u32 s76, s76, s71\n"
      "s_add_u32 s71, s71, s76\n"
      "v_add_u32 v20, s74, v20\n"
      "v_add_u32 v21, s74, v21\n"
      "v_add_u32 v22, s74, v22\n"
      "v_add_u32 v23, s74, v23\n"
      "v_add_u32 v25, s74, v25\n"
      "s_cmp_eq_u32 s75, 0\n"
      "s_cbranch_scc1 L_ax_23_%=\n"
      "v_bfe_i32 v154, v27, 10, 1\n"
      "v_and_b32 v154, s75, v154\n"
      "v_sub_u32 v20, v20, v154\n"
      "v_bfe_i32 v154, v27, 12, 1\n"
      "v_and_b32 v154, s75, v154\n"
      "v_sub_u32 v21, v21, v154\n"
      "v_bfe_i32 v154, v27, 14, 1\n"
      "v_and_b32 v154, s75, v154\n"
      "v_sub_u32 v22, v22, v154\n"
      "v_bfe_i32 v154, v27, 16, 1\n"
      "v_and_b32 v154, s75, v154\n"
      "v_sub_u32 v23, v23, v154\n"
      "v_bfe_i32 v154, v27, 18, 1\n"
      "v_and_b32 v154, s75, v154\n"
      "v_sub_u32 v25, v25, v154\n"
      "L_ax_23_%=:\n"
      "s_cmp_eq_u32 s76, 0\n"
      "s_cbranch_scc1 L_ax_24_%=\n"
      "v_bfe_i32 v154, v27, 11, 1\n"
      "v_and_b32 v154, s76, v154\n"
      "v_sub_u32 v20, v20, v154\n"
      "v_bfe_i32 v154, v27, 13, 1\n"
      "v_and_b32 v154, s76, v154\n"
      "v_sub_u32 v21, v21, v154\n"
      "v_bfe_i32 v154, v27, 15, 1\n"
      "v_and_b32 v154, s76, v154\n"
      "v_sub_u32 v22, v22, v154\n"
      "v_bfe_i32 v154, v27, 17, 1\n"
      "v_and_b32 v154, s76, v154\n"
      "v_sub_u32 v23, v23, v154\n"
      "v_bfe_i32 v154, v27, 19, 1\n"
      "v_and_b32 v154, s76, v154\n"
      "v_sub_u32 v25, v25, v154\n"
      "L_ax_24_%=:\n"
      "s_or_b32 s48, s66, s69\n"
      "s_cmp_lg_u32 s48, 0\n"
      "s_cselect_b32 s48, 0x40000000, 0\n"
      "L_nf_22_%=:\n"
      "s_and_b32 s4, s45, s8\n"
      "s_sub_u32 s92, s42, 1\n"
      "s_cmp_gt_u32 s92, s44\n"
      "s_cbranch_scc1 L_tail0_%=\n"
      "L_nofix_18_%=:\n"
      "s_cmp_lg_u64 s[56:57], 0\n"
      "s_cbranch_scc1 L_slow0_%=\n"
      "L_sel0_%=:\n"
      "s_cmp_lg_u64 s[54:55], 0\n"
      "s_cbranch_scc1 L_max0_%=\n"
      "s_branch L_loop0_%=\n"
      "L_slow0_%=:\n"
      "s_nop 4\n"
      "v_mfma_f32_16x16x32_bf16 v[152:155], v[144:147], v[194:197], 0\n"
      "ds_read_b128 v[124:127], v0\n"
      "v_subrev_u32 v197, 115456, v9\n"
      "v_mfma_f32_16x16x32_bf16 v[152:155], v[198:201], v[76:79], v[152:155]\n"
      "s_mov_b32 m0, 110592\n"
      "ds_read_b128 v[136:139], v9 offset:1024\n"
      "v_mfma_f32_16x16x32_bf16 v[210:213], v[144:147], v[104:107], 0\n"
      "ds_read_b128 v[100:103], v1\n"
      "s_add_u32 s72, s42, 1\n"
      "v_mfma_f32_16x16x32_bf16 v[210:213], v[198:201], v[80:83], v[210:213]\n"
      "ds_read_b128 v[190:193], v0 offset:6144\n"
      "v_readlane_b32 s77, v38, s72\n"
      "v_mfma_f32_16x16x32_bf16 v[180:183], v[144:147], v[96:99], 0\n"
      "ds_read_b128 v[144:147], v1 offset:6144\n"
      "s_and_b32 s73, s77, -16\n"
      "v_mfma_f32_16x16x32_bf16 v[180:183], v[198:201], v[84:87], v[180:183]\n"
      "buffer_load_dwordx4 v197, s[28:31], s73 offen  lds\n"
      "s_waitcnt lgkmcnt(4)\n"
      "v_mfma_f32_16x16x32_bf16 v[198:201], a[0:3], v[124:127], 0\n"
      "ds_read_b128 v[216:219], v1 offset:8192\n"
      "v_subrev_u32 v39, 115456, v9\n"
      "v_mfma_f32_16x16x32_bf16 v[212:215], a[24:27], v[124:127], 0\n"
      "s_waitcnt lgkmcnt(4)\n"
      "v_readlane_b32 s84, v37, s43\n"
      "v_add_f32 v138, v138, v139\n"
      "v_mfma_f32_16x16x32_bf16 v[202:205], a[48:51], v[124:127], 0\n"
      "s_add_u32 s73, s73, 1024\n"
      "v_add_f32_e64 v136, v136, v137\n"
      "v_add_f32_e64 v136, v136, v138\n"
      "v_mfma_f32_16x16x32_bf16 v[182:185], a[72:75], v[124:127], 0\n"
      "s_mov_b32 m0, 111616\n"
      "v_rsq_f32 v136, v136\n"
      "v_mfma_f32_16x16x32_bf16 v[186:189], a[96:99], v[124:127], 0\n"
      "buffer_load_dwordx4 v197, s[28:31], s73 offen  lds\n"
      "v_mfma_f32_16x16x32_bf16 v[112:115], a[120:123], v[124:127], 0\n"
      "s_waitcnt lgkmcnt(1)\n"
      "v_mul_f32 v136, 0x415db3d7, v136\n"
      "v_subrev_u32 v166, 115456, v9\n"
      "v_mfma_f32_16x16x32_bf16 v[198:201], a[4:7], v[100:103], v[198:201]\n"
      "ds_read_b128 v[148:151], v1 offset:12288\n"
      "v_mov_b32 v138, v136\n"
      "v_mfma_f32_16x16x32_bf16 v[198:201], a[8:11], v[190:193], v[198:201]\n"
      "s_mov_b32 m0, 112640\n"
      "v_permlane32_swap_b32 v136, v138\n"
      "v_mfma_f32_16x16x32_bf16 v[212:215], a[28:31], v[100:103], v[212:215]\n"
      "s_add_u32 s73, s73, 1024\n"
      "v_mov_b32 v137, v136\n"
      "v_mul_f32 v120, v138, v120\n"
      "v_mfma_f32_16x16x32_bf16 v[212:215], a[32:35], v[190:193], v[212:215]\n"
      "buffer_load_dwordx4 v197, s[28:31], s73 offen  lds\n"
      "v_mfma_f32_16x16x32_bf16 v[202:205], a[52:55], v[100:103], v[202:205]\n"
      "ds_read_b128 v[158:161], v0 offset:12288\n"
      "v_mul_f32_e64 v133, v138, v133\n"
      "v_mfma_f32_16x16x32_bf16 v[202:205], a[56:59], v[190:193], v[202:205]\n"
      "s_add_u32 s73, s73, 1024\n"
      "v_permlane16_swap_b32 v136, v137\n"
      "v_mfma_f32_16x16x32_bf16 v[182:185], a[76:79], v[100:103], v[182:185]\n"
      "s_mov_b32 m0, 113664\n"
      "v_mul_f32 v109, v137, v109\n"
      "v_mul_f32 v143, v136, v143\n"
      "v_mfma_f32_16x16x32_bf16 v[182:185], a[80:83], v[190:193], v[182:185]\n"
      "v_mul_f32 v175, v136, v175\n"
      "v_mul_f32 v108, v137, v108\n"
      "v_mfma_f32_16x16x32_bf16 v[186:189], a[100:103], v[100:103], v[186:189]\n"
      "v_mul_f32 v220, v136, v220\n"
      "v_mul_f32 v208, v137, v208\n"
      "v_mfma_f32_16x16x32_bf16 v[186:189], a[104:107], v[190:193], v[186:189]\n"
      "ds_read_b128 v[154:157], v0 offset:2048\n"
      "v_mul_f32 v174, v136, v174\n"
      "v_mfma_f32_16x16x32_bf16 v[112:115], a[124:127], v[100:103], v[112:115]\n"
      "buffer_load_dwordx4 v197, s[28:31], s73 offen  lds\n"
      "v_mfma_f32_16x16x32_bf16 v[112:115], a[128:131], v[190:193], v[112:115]\n"
      "s_waitcnt lgkmcnt(1)\n"
      "v_mul_f32 v142, v136, v142\n"
      "v_mul_f32_e64 v132, v138, v132\n"
      "v_mfma_f32_16x16x32_bf16 v[198:201], a[12:15], v[144:147], v[198:201]\n"
      "s_add_u32 s73, s73, 1024\n"
      "v_mul_f32 v169, v138, v169\n"
      "v_mul_f32 v135, v138, v135\n"
      "v_mfma_f32_16x16x32_bf16 v[198:201], a[16:19], v[158:161], v[198:201]\n"
      "ds_read_b128 v[128:131], v9 offset:5632\n"
      "v_mul_f32 v173, v136, v173\n"
      "v_mfma_f32_16x16x32_bf16 v[198:201], a[20:23], v[148:151], v[198:201]\n"
      "s_mov_b32 m0, 114688\n"
      "v_mul_f32 v172, v136, v172\n"
      "v_mul_f32 v221, v136, v221\n"
      "v_mfma_f32_16x16x32_bf16 v[212:215], a[36:39], v[144:147], v[212:215]\n"
      "v_cvt_pk_bf16_f32 v172, v172, v173\n"
      "v_cvt_pk_bf16_f32 v173, v174, v175\n"
      "v_mfma_f32_16x16x32_bf16 v[212:215], a[40:43], v[158:161], v[212:215]\n"
      "s_mov_b32 exec_hi, 0xffff\n"
      "buffer_load_dwordx4 v197, s[28:31], s73 offen  lds\n"
      "s_mov_b32 exec_hi, -1\n"
      "v_pk_mul_f32 v[206:207], v[206:207], v[136:137] op_sel:[0,1]\n"
      "v_pk_mul_f32 v[170:171], v[170:171], v[138:139] op_sel_hi:[1,0]\n"
      "v_pk_mul_f32 v[178:179], v[178:179], v[136:137] op_sel:[0,1]\n"
      "v_pk_mul_f32 v[222:223], v[222:223], v[136:137] op_sel_hi:[1,0]\n"
      "v_pk_mul_f32 v[110:111], v[110:111], v[136:137] op_sel:[0,1]\n"
      "v_mfma_f32_16x16x32_bf16 v[212:215], a[44:47], v[148:151], v[212:215]\n"
      "v_cvt_pk_bf16_f32 v206, v206, v207\n"
      "v_cvt_pk_bf16_f32 v175, v222, v223\n"
      "v_mfma_f32_16x16x32_bf16 v[202:205], a[60:63], v[144:147], v[202:205]\n"
      "s_add_u32 m0, s64, 18432\n"
      "v_lshrrev_b32_e64 v39, 3, v39\n"
      "v_cvt_pk_bf16_f32 v174, v220, v221\n"
      "v_mfma_f32_16x16x32_bf16 v[202:205], a[64:67], v[158:161], v[202:205]\n"
      "ds_write_b128 v5, v[172:175]\n"
      "v_pk_mul_f32 v[176:177], v[176:177], v[136:137] op_sel:[0,1]\n"
      "v_mfma_f32_16x16x32_bf16 v[202:205], a[68:71], v[148:151], v[202:205]\n"
      "buffer_load_dwordx4 v16, s[16:19], s45 offen  lds\n"
      "v_mfma_f32_16x16x32_bf16 v[182:185], a[84:87], v[144:147], v[182:185]\n"
      "s_and_b32 s77, s77, 15\n"
      "v_fma_f32 v181, v198, v198, s51\n"
      "v_mul_f32 v140, v136, v140\n"
      "v_mfma_f32_16x16x32_bf16 v[182:185], a[88:91], v[158:161], v[182:185]\n"
      "s_add_u32 m0, s64, 22528\n"
      "v_mul_f32 v209, v137, v209\n"
      "v_cvt_pk_bf16_f32 v108, v108, v109\n"
      "v_mfma_f32_16x16x32_bf16 v[182:185], a[92:95], v[148:151], v[182:185]\n"
      "v_mul_f32 v168, v138, v168\n"
      "v_cvt_pk_bf16_f32 v207, v208, v209\n"
      "v_mfma_f32_16x16x32_bf16 v[186:189], a[108:111], v[144:147], v[186:189]\n"
      "v_mul_f32 v141, v136, v141\n"
      "v_mul_f32 v121, v138, v121\n"
      "v_mfma_f32_16x16x32_bf16 v[186:189], a[112:115], v[158:161], v[186:189]\n"
      "buffer_load_dwordx4 v17, s[16:19], s45 offen  lds\n"
      "v_mfma_f32_16x16x32_bf16 v[186:189], a[116:119], v[148:151], v[186:189]\n"
      "ds_read_b128 v[172:175], v0 offset:14336\n"
      "v_cvt_pk_bf16_f32 v132, v132, v133\n"
      "v_mfma_f32_16x16x32_bf16 v[112:115], a[132:135], v[144:147], v[112:115]\n"
      "v_mul_f32 v118, v138, v118\n"
      "v_fmac_f32 v181, v199, v199\n"
      "v_mfma_f32_16x16x32_bf16 v[112:115], a[136:139], v[158:161], v[112:115]\n"
      "v_cvt_pk_bf16_f32 v109, v110, v111\n"
      "v_cvt_pk_bf16_f32 v209, v178, v179\n"
      "v_mfma_f32_16x16x32_bf16 v[112:115], a[140:143], v[148:151], v[112:115]\n"
      "s_add_u32 m0, s64, 26624\n"
      "v_fma_f32 v211, v182, v182, s51\n"
      "v_cvt_pk_bf16_f32 v208, v176, v177\n"
      "v_mfma_f32_16x16x32_bf16 v[194:197], v[124:127], a[144:147], 0\n"
      "buffer_load_dwordx4 v18, s[16:19], s45 offen  lds\n"
      "v_mfma_f32_16x16x32_bf16 v[194:197], v[100:103], a[148:151], v[194:197]\n"
      "ds_write_b128 v5, v[206:209] offset:6144\n"
      "v_mfma_f32_16x16x32_bf16 v[194:197], v[190:193], a[152:155], v[194:197]\n"
      "s_add_u32 m0, s64, 30720\n"
      "v_fmac_f32 v211, v183, v183\n"
      "v_fmac_f32 v211, v184, v184\n"
      "v_mfma_f32_16x16x32_bf16 v[194:197], v[144:147], a[156:159], v[194:197]\n"
      "v_min_u32 v39, 96, v39\n"
      "v_fmac_f32_e64 v211, v185, v185\n"
      "v_mfma_f32_16x16x32_bf16 v[194:197], v[158:161], a[160:163], v[194:197]\n"
      "ds_write_b64 v6, v[108:109] offset:6144\n"
      "v_mfma_f32_16x16x32_bf16 v[194:197], v[148:151], a[164:167], v[194:197]\n"
      "buffer_load_dwordx4 v19, s[16:19], s45 offen  lds\n"
      "v_mfma_f32_16x16x32_bf16 v[104:107], v[124:127], a[168:171], 0\n"
      "s_mov_b32 m0, s7\n"
      "v_cndmask_b32 v152, v152, v210, s[58:59]\n"
      "v_fmac_f32 v181, v200, v200\n"
      "v_mfma_f32_16x16x32_bf16 v[104:107], v[100:103], a[172:175], v[104:107]\n"
      "v_cvt_pk_bf16_f32 v140, v140, v141\n"
      "v_fmac_f32 v211, v186, v186\n"
      "v_mfma_f32_16x16x32_bf16 v[104:107], v[190:193], a[176:179], v[104:107]\n"
      "ds_read_b128 v[220:223], v1 offset:2048\n"
      "v_mul_f32 v134, v138, v134\n"
      "v_mfma_f32_16x16x32_bf16 v[104:107], v[144:147], a[180:183], v[104:107]\n"
      "ds_read_b128 v[206:209], v1 offset:14336\n"
      "v_fmac_f32_e64 v211, v187, v187\n"
      "v_mfma_f32_16x16x32_bf16 v[104:107], v[158:161], a[184:187], v[104:107]\n"
      "buffer_load_dwordx4 v15, s[0:3], s4 offen  lds\n"
      "v_mfma_f32_16x16x32_bf16 v[104:107], v[148:151], a[188:191], v[104:107]\n"
      "v_cvt_pk_bf16_f32 v141, v142, v143\n"
      "v_cvt_pk_bf16_f32 v168, v168, v169\n"
      "v_mfma_f32_16x16x32_bf16 v[96:99], v[124:127], a[192:195], 0\n"
      "ds_read_b128 v[176:179], v0 offset:8192\n"
      "v_readlane_b32 s86, v38, s43\n"
      "v_mfma_f32_16x16x32_bf16 v[96:99], v[100:103], a[196:199], v[96:99]\n"
      "v_fmac_f32 v181, v201, v201\n"
      "v_fmac_f32 v181, v212, v212\n"
      "v_mfma_f32_16x16x32_bf16 v[96:99], v[190:193], a[200:203], v[96:99]\n"
      "ds_write_b64 v6, v[140:141]\n"
      "v_mfma_f32_16x16x32_bf16 v[96:99], v[144:147], a[204:207], v[96:99]\n"
      "v_cvt_pk_bf16_f32 v133, v134, v135\n"
      "v_fmac_f32 v211, v188, v188\n"
      "v_mfma_f32_16x16x32_bf16 v[96:99], v[158:161], a[208:211], v[96:99]\n"
      "buffer_load_dwordx4 v[124:127], v36, s[28:31], s47 offen\n"
      "v_mfma_f32_16x16x32_bf16 v[96:99], v[148:151], a[212:215], v[96:99]\n"
      "s_waitcnt lgkmcnt(2)\n"
      "v_fmac_f32 v181, v213, v213\n"
      "v_fmac_f32 v211, v189, v189\n"
      "v_mfma_f32_16x16x32_bf16 v[148:151], a[0:3], v[154:157], 0\n"
      "v_or3_b32 v128, v128, v129, v130\n"
      "v_bfe_u32 v84, v166, 4, 4\n"
      "v_mfma_f32_16x16x32_bf16 v[148:151], a[4:7], v[220:223], v[148:151]\n"
      "s_waitcnt lgkmcnt(1)\n"
      "v_mov_b32_dpp v162, v162 quad_perm:[0,0,0,0] row_mask:0xf bank_mask:0xf\n"
      "v_fmac_f32 v211, v112, v112\n"
      "v_mfma_f32_16x16x32_bf16 v[148:151], a[8:11], v[176:179], v[148:151]\n"
      "v_cvt_pk_bf16_f32 v135, v120, v121\n"
      "v_cndmask_b32 v152, v152, v180, s[60:61]\n"
      "v_mfma_f32_16x16x32_bf16 v[148:151], a[12:15], v[216:219], v[148:151]\n"
      "ds_read_b128 v[144:147], v0 offset:4096\n"
      "v_fmac_f32_e64 v181, v214, v214\n"
      "v_mfma_f32_16x16x32_bf16 v[148:151], a[16:19], v[172:175], v[148:151]\n"
      "buffer_load_dword v78, v29, s[28:31], s47 offen\n"
      "v_mfma_f32_16x16x32_bf16 v[148:151], a[20:23], v[206:209], v[148:151]\n"
      "v_cvt_pk_bf16_f32 v169, v170, v171\n"
      "v_fmac_f32 v181, v215, v215\n"
      "v_mfma_f32_16x16x32_bf16 v[108:111], a[24:27], v[154:157], 0\n"
      "v_fmac_f32 v211, v113, v113\n"
      "v_lshrrev_b32_e64 v166, 8, v166\n"
      "v_mfma_f32_16x16x32_bf16 v[108:111], a[28:31], v[220:223], v[108:111]\n"
      "v_mul_f32_e64 v119, v138, v119\n"
      "v_cvt_pk_bf16_f32 v134, v118, v119\n"
      "v_mfma_f32_16x16x32_bf16 v[108:111], a[32:35], v[176:179], v[108:111]\n"
      "ds_write_b128 v5, v[132:135] offset:12288\n"
      "v_mfma_f32_16x16x32_bf16 v[108:111], a[36:39], v[216:219], v[108:111]\n"
      "v_fmac_f32_e64 v181, v202, v202\n"
      "v_fma_f32 v210, v148, v148, s51\n"
      "v_mfma_f32_16x16x32_bf16 v[108:111], a[40:43], v[172:175], v[108:111]\n"
      "v_cvt_pk_bf16_f32 v194, v194, v195\n"
      "v_fmac_f32_e64 v181, v203, v203\n"
      "v_mfma_f32_16x16x32_bf16 v[108:111], a[44:47], v[206:209], v[108:111]\n"
      "ds_write_b64 v6, v[168:169] offset:12288\n"
      "v_mfma_f32_16x16x32_bf16 v[100:103], a[48:51], v[154:157], 0\n"
      "v_fmac_f32 v211, v114, v114\n"
      "v_fmac_f32 v181, v204, v204\n"
      "v_mfma_f32_16x16x32_bf16 v[100:103], a[52:55], v[220:223], v[100:103]\n"
      "v_fmac_f32 v211, v115, v115\n"
      "v_fmac_f32 v210, v149, v149\n"
      "v_mfma_f32_16x16x32_bf16 v[100:103], a[56:59], v[176:179], v[100:103]\n"
      "v_cvt_pk_bf16_f32 v96, v96, v97\n"
      "v_fmac_f32_e64 v181, v205, v205\n"
      "v_mfma_f32_16x16x32_bf16 v[100:103], a[60:63], v[216:219], v[100:103]\n"
      "s_nop 0\n"
      "v_permlane16_swap_b32 v181, v211\n"
      "v_mfma_f32_16x16x32_bf16 v[100:103], a[64:67], v[172:175], v[100:103]\n"
      "v_fmac_f32 v210, v150, v150\n"
      "v_fmac_f32 v210, v151, v151\n"
      "v_mfma_f32_16x16x32_bf16 v[100:103], a[68:71], v[206:209], v[100:103]\n"
      "v_mul_u32_u24 v82, 98, v84\n"
      "v_add_f32 v181, v181, v211\n"
      "v_mfma_f32_16x16x32_bf16 v[136:139], a[72:75], v[154:157], 0\n"
      "v_mov_b32 v211, v181\n"
      "v_cvt_pk_bf16_f32 v97, v98, v99\n"
      "v_mfma_f32_16x16x32_bf16 v[136:139], a[76:79], v[220:223], v[136:139]\n"
      "v_bitop3_b32 v128, v128, v131, s33 bitop3:0xa8\n"
      "v_cvt_pk_bf16_f32 v195, v196, v197\n"
      "v_mfma_f32_16x16x32_bf16 v[136:139], a[80:83], v[176:179], v[136:139]\n"
      "v_permlane32_swap_b32 v181, v211\n"
      "v_mfma_f32_16x16x32_bf16 v[136:139], a[84:87], v[216:219], v[136:139]\n"
      "v_readlane_b32 s85, v37, s42\n"
      "v_add_f32 v181, v181, v211\n"
      "v_mfma_f32_16x16x32_bf16 v[136:139], a[88:91], v[172:175], v[136:139]\n"
      "v_rsq_f32 v181, v181\n"
      "v_mfma_f32_16x16x32_bf16 v[136:139], a[92:95], v[206:209], v[136:139]\n"
      "v_cvt_pk_bf16_f32 v104, v104, v105\n"
      "v_mov_b32 v211, v181\n"
      "v_mfma_f32_16x16x32_bf16 v[120:123], a[96:99], v[154:157], 0\n"
      "v_fmac_f32 v210, v108, v108\n"
      "v_lshl_add_u32 v82, v166, 3, v82\n"
      "v_mfma_f32_16x16x32_bf16 v[120:123], a[100:103], v[220:223], v[120:123]\n"
      "v_rcp_f32 v162, v162\n"
      "v_mfma_f32_16x16x32_bf16 v[120:123], a[104:107], v[176:179], v[120:123]\n"
      "v_fmac_f32 v210, v109, v109\n"
      "v_fma_f32 v197, v136, v136, s51\n"
      "v_mfma_f32_16x16x32_bf16 v[120:123], a[108:111], v[216:219], v[120:123]\n"
      "v_permlane16_swap_b32 v181, v211\n"
      "v_mfma_f32_16x16x32_bf16 v[120:123], a[112:115], v[172:175], v[120:123]\n"
      "v_fmac_f32 v197, v137, v137\n"
      "v_mul_f32 v112, v211, v112\n"
      "v_mfma_f32_16x16x32_bf16 v[120:123], a[116:119], v[206:209], v[120:123]\n"
      "v_cmp_ne_u32 s[56:57], 0, v128\n"
      "v_fmac_f32 v210, v110, v110\n"
      "v_mfma_f32_16x16x32_bf16 v[128:131], a[120:123], v[154:157], 0\n"
      "v_mul_f32 v183, v211, v183\n"
      "v_lshlrev_b32_e64 v166, 2, v166\n"
      "v_mfma_f32_16x16x32_bf16 v[128:131], a[124:127], v[220:223], v[128:131]\n"
      "ds_read_b128 v[140:143], v1 offset:4096\n"
      "v_mul_f32 v182, v211, v182\n"
      "v_mfma_f32_16x16x32_bf16 v[128:131], a[128:131], v[176:179], v[128:131]\n"
      "v_fmac_f32 v197, v138, v138\n"
      "v_sub_u32_e64 v84, v84, v166\n"
      "v_mfma_f32_16x16x32_bf16 v[128:131], a[132:135], v[216:219], v[128:131]\n"
      "s_or_b32 s95, s84, s85\n"
      "v_cvt_pk_bf16_f32 v105, v106, v107\n"
      "v_add_u32 v82, s77, v82\n"
      "v_mfma_f32_16x16x32_bf16 v[128:131], a[136:139], v[172:175], v[128:131]\n"
      "v_mul_f32 v152, v162, v152\n"
      "v_fmac_f32 v210, v111, v111\n"
      "v_mfma_f32_16x16x32_bf16 v[128:131], a[140:143], v[206:209], v[128:131]\n"
      "v_fmac_f32 v197, v139, v139\n"
      "v_fmac_f32 v197, v120, v120\n"
      "v_mfma_f32_16x16x32_bf16 v[160:163], v[154:157], a[144:147], 0\n"
      "v_fmac_f32 v197, v121, v121\n"
      "v_mfma_f32_16x16x32_bf16 v[160:163], v[220:223], a[148:151], v[160:163]\n"
      "v_cvt_pk_bf16_f32 v152, v152, v152\n"
      "v_fmac_f32 v210, v100, v100\n"
      "v_mfma_f32_16x16x32_bf16 v[160:163], v[176:179], a[152:155], v[160:163]\n"
      "ds_read_b128 v[166:169], v0 offset:16384\n"
      "v_fmac_f32 v197, v122, v122\n"
      "v_mfma_f32_16x16x32_bf16 v[160:163], v[216:219], a[156:159], v[160:163]\n"
      "v_mul_f32 v185, v211, v185\n"
      "v_mul_f32_e64 v181, s50, v181\n"
      "v_mfma_f32_16x16x32_bf16 v[160:163], v[172:175], a[160:163], v[160:163]\n"
      "v_fmac_f32 v210, v101, v101\n"
      "v_fmac_f32 v197, v123, v123\n"
      "v_mfma_f32_16x16x32_bf16 v[160:163], v[206:209], a[164:167], v[160:163]\n"
      "v_fmac_f32 v197, v128, v128\n"
      "v_fmac_f32 v197, v129, v129\n"
      "v_mfma_f32_16x16x32_bf16 v[116:119], v[154:157], a[168:171], 0\n"
      "v_fmac_f32 v210, v102, v102\n"
      "v_mul_f32 v198, v181, v198\n"
      "v_mfma_f32_16x16x32_bf16 v[116:119], v[220:223], a[172:175], v[116:119]\n"
      "v_mul_f32 v199, v181, v199\n"
      "v_mul_f32 v202, v181, v202\n"
      "v_mfma_f32_16x16x32_bf16 v[116:119], v[176:179], a[176:179], v[116:119]\n"
      "v_fmac_f32 v210, v103, v103\n"
      "v_fmac_f32 v197, v130, v130\n"
      "v_mfma_f32_16x16x32_bf16 v[116:119], v[216:219], a[180:183], v[116:119]\n"
      "v_mul_f32 v201, v181, v201\n"
      "v_mul_f32 v113, v211, v113\n"
      "v_mfma_f32_16x16x32_bf16 v[116:119], v[172:175], a[184:187], v[116:119]\n"
      "ds_read_b128 v[132:135], v0 offset:10240\n"
      "v_fmac_f32 v197, v131, v131\n"
      "v_mfma_f32_16x16x32_bf16 v[116:119], v[206:209], a[188:191], v[116:119]\n"
      "s_nop 0\n"
      "v_permlane16_swap_b32 v210, v197\n"
      "v_mfma_f32_16x16x32_bf16 v[190:193], v[154:157], a[192:195], 0\n"
      "ds_read_b128 v[154:157], v1 offset:10240\n"
      "v_lshlrev_b32 v84, 4, v84\n"
      "v_mfma_f32_16x16x32_bf16 v[190:193], v[220:223], a[196:199], v[190:193]\n"
      "v_add_f32 v210, v210, v197\n"
      "v_mov_b32 v180, 0x3fb9\n"
      "v_mfma_f32_16x16x32_bf16 v[190:193], v[176:179], a[200:203], v[190:193]\n"
      "ds_read_b128 v[220:223], v1 offset:16384\n"
      "v_mul_f32 v203, v181, v203\n"
      "v_mfma_f32_16x16x32_bf16 v[190:193], v[216:219], a[204:207], v[190:193]\n"
      "v_mul_f32 v205, v181, v205\n"
      "v_mul_f32 v187, v211, v187\n"
      "v_mfma_f32_16x16x32_bf16 v[190:193], v[172:175], a[208:211], v[190:193]\n"
      "s_waitcnt lgkmcnt(2)\n"
      "v_mul_f32 v184, v211, v184\n"
      "v_mov_b32 v197, v210\n"
      "v_mfma_f32_16x16x32_bf16 v[190:193], v[206:209], a[212:215], v[190:193]\n"
      "s_nop 0\n"
      "v_permlane32_swap_b32 v210, v197\n"
      "v_mfma_f32_16x16x32_bf16 v[216:219], a[0:3], v[144:147], 0\n"
      "v_add_f32 v210, v210, v197\n"
      "v_cvt_pk_bf16_f32 v112, v112, v113\n"
      "v_mfma_f32_16x16x32_bf16 v[216:219], a[4:7], v[140:143], v[216:219]\n"
      "v_cvt_pk_bf16_f32 v182, v182, v183\n"
      "v_mul_f32_e64 v200, v181, v200\n"
      "v_mfma_f32_16x16x32_bf16 v[216:219], a[8:11], v[132:135], v[216:219]\n"
      "s_waitcnt lgkmcnt(0)\n"
      "v_rsq_f32 v210, v210\n"
      "v_mfma_f32_16x16x32_bf16 v[216:219], a[12:15], v[154:157], v[216:219]\n"
      "v_mul_f32 v204, v181, v204\n"
      "v_mul_f32 v212, v181, v212\n"
      "v_mfma_f32_16x16x32_bf16 v[216:219], a[16:19], v[166:169], v[216:219]\n"
      "v_cvt_pk_bf16_f32 v183, v184, v185\n"
      "v_mov_b32 v197, v210\n"
      "v_mfma_f32_16x16x32_bf16 v[216:219], a[20:23], v[220:223], v[216:219]\n"
      "s_nop 0\n"
      "v_permlane16_swap_b32 v210, v197\n"
      "v_mfma_f32_16x16x32_bf16 v[206:209], a[24:27], v[144:147], 0\n"
      "ds_write_b16 v12, v152 offset:16\n"
      "v_mfma_f32_16x16x32_bf16 v[206:209], a[28:31], v[140:143], v[206:209]\n"
      "v_mul_f32 v210, s50, v210\n"
      "v_mul_f32 v186, v211, v186\n"
      "v_mfma_f32_16x16x32_bf16 v[206:209], a[32:35], v[132:135], v[206:209]\n"
      "v_mul_f32 v129, v197, v129\n"
      "v_fma_f32 v152, v216, v216, s51\n"
      "v_mfma_f32_16x16x32_bf16 v[206:209], a[36:39], v[154:157], v[206:209]\n"
      "v_cvt_pk_bf16_f32 v196, v160, v161\n"
      "v_mul_f32 v213, v181, v213\n"
      "v_mfma_f32_16x16x32_bf16 v[206:209], a[40:43], v[166:169], v[206:209]\n"
      "s_or_b32 s95, s95, s48\n"
      "v_fmac_f32 v152, v217, v217\n"
      "v_cvt_pk_bf16_f32 v202, v202, v203\n"
      "v_mfma_f32_16x16x32_bf16 v[206:209], a[44:47], v[220:223], v[206:209]\n"
      "v_cvt_pk_bf16_f32 v184, v186, v187\n"
      "v_mul_f32 v149, v210, v149\n"
      "v_mul_f32 v138, v197, v138\n"
      "v_fmac_f32 v152, v218, v218\n"
      "v_cvt_pk_bf16_f32 v98, v190, v191\n"
      "v_pk_mul_f32 v[120:121], v[120:121], v[196:197] op_sel:[0,1]\n"
      "v_pk_mul_f32 v[114:115], v[114:115], v[210:211] op_sel:[0,1]\n"
      "v_pk_mul_f32 v[214:215], v[214:215], v[180:181] op_sel:[0,1]\n"
      "v_pk_mul_f32 v[188:189], v[188:189], v[210:211] op_sel:[0,1]\n"
      "v_pk_mul_f32 v[122:123], v[122:123], v[196:197] op_sel:[0,1]\n"
      "v_pk_mul_f32 v[136:137], v[136:137], v[196:197] op_sel:[0,1]\n"
      "v_pk_mul_f32 v[100:101], v[100:101], v[210:211] op_sel_hi:[1,0]\n"
      "v_pk_mul_f32 v[110:111], v[110:111], v[210:211] op_sel_hi:[1,0]\n"
      "v_pk_mul_f32 v[108:109], v[108:109], v[210:211] op_sel_hi:[1,0]\n"
      "v_mfma_f32_16x16x32_bf16 v[158:161], a[48:51], v[144:147], 0\n"
      "s_add_u32 s73, s73, 1024\n"
      "v_cvt_pk_bf16_f32 v198, v198, v199\n"
      "v_fmac_f32 v152, v219, v219\n"
      "v_mfma_f32_16x16x32_bf16 v[158:161], a[52:55], v[140:143], v[158:161]\n"
      "v_mul_f32 v103, v210, v103\n"
      "v_cmp_gt_u32 s[72:73], 64, v84\n"
      "v_mfma_f32_16x16x32_bf16 v[158:161], a[56:59], v[132:135], v[158:161]\n"
      "v_mul_f32 v139, v197, v139\n"
      "v_fmac_f32 v152, v206, v206\n"
      "v_mfma_f32_16x16x32_bf16 v[158:161], a[60:63], v[154:157], v[158:161]\n"
      "v_cvt_pk_bf16_f32 v185, v188, v189\n"
      "v_mul_f32 v128, v197, v128\n"
      "v_mfma_f32_16x16x32_bf16 v[158:161], a[64:67], v[166:169], v[158:161]\n"
      "v_cvt_pk_bf16_f32 v199, v200, v201\n"
      "v_mul_f32 v150, v210, v150\n"
      "v_mfma_f32_16x16x32_bf16 v[158:161], a[68:71], v[220:223], v[158:161]\n"
      "v_cvt_pk_bf16_f32 v201, v214, v215\n"
      "v_and_b32 v93, 2, v82\n"
      "v_mfma_f32_16x16x32_bf16 v[186:189], a[72:75], v[144:147], 0\n"
      "v_cvt_pk_bf16_f32 v203, v204, v205\n"
      "v_fmac_f32 v152, v207, v207\n"
      "v_mfma_f32_16x16x32_bf16 v[186:189], a[76:79], v[140:143], v[186:189]\n"
      "v_cvt_pk_bf16_f32 v99, v192, v193\n"
      "v_cvt_pk_bf16_f32 v107, v118, v119\n"
      "v_mfma_f32_16x16x32_bf16 v[186:189], a[80:83], v[132:135], v[186:189]\n"
      "v_mul_f32 v148, v210, v148\n"
      "v_fmac_f32 v152, v208, v208\n"
      "v_mfma_f32_16x16x32_bf16 v[186:189], a[84:87], v[154:157], v[186:189]\n"
      "v_mul_f32 v151, v210, v151\n"
      "v_mul_f32 v130, v197, v130\n"
      "v_mfma_f32_16x16x32_bf16 v[186:189], a[88:91], v[166:169], v[186:189]\n"
      "v_mul_f32 v131, v197, v131\n"
      "v_cvt_pk_bf16_f32 v200, v212, v213\n"
      "v_mfma_f32_16x16x32_bf16 v[186:189], a[92:95], v[220:223], v[186:189]\n"
      "s_and_b32 s86, s86, -4\n"
      "v_add_u32 v39, s77, v39\n"
      "v_fmac_f32 v152, v209, v209\n"
      "v_mfma_f32_16x16x32_bf16 v[176:179], a[96:99], v[144:147], 0\n"
      "v_cvt_pk_bf16_f32 v100, v100, v101\n"
      "v_cvt_pk_bf16_f32 v113, v114, v115\n"
      "v_mfma_f32_16x16x32_bf16 v[176:179], a[100:103], v[140:143], v[176:179]\n"
      "s_cmp_ge_u32 s95, 0x40000000\n"
      "s_cselect_b32 s94, -1, -2\n"
      "v_fma_f32 v171, v186, v186, s51\n"
      "v_mfma_f32_16x16x32_bf16 v[176:179], a[104:107], v[132:135], v[176:179]\n"
      "v_mov_b32 v181, 0\n"
      "v_mul_f32 v102, v210, v102\n"
      "v_mfma_f32_16x16x32_bf16 v[176:179], a[108:111], v[154:157], v[176:179]\n"
      "v_fmac_f32 v171, v187, v187\n"
      "v_fmac_f32 v171, v188, v188\n"
      "v_mfma_f32_16x16x32_bf16 v[176:179], a[112:115], v[166:169], v[176:179]\n"
      "v_fmac_f32_e64 v152, v158, v158\n"
      "v_cvt_pk_bf16_f32 v136, v136, v137\n"
      "v_mfma_f32_16x16x32_bf16 v[176:179], a[116:119], v[220:223], v[176:179]\n"
      "v_cvt_pk_bf16_f32 v106, v116, v117\n"
      "v_and_b32_e64 v82, -4, v82\n"
      "v_mfma_f32_16x16x32_bf16 v[210:213], a[120:123], v[144:147], 0\n"
      "v_fmac_f32_e64 v152, v159, v159\n"
      "v_cvt_pk_bf16_f32 v137, v138, v139\n"
      "v_mfma_f32_16x16x32_bf16 v[210:213], a[124:127], v[140:143], v[210:213]\n"
      "v_cvt_pk_bf16_f32 v148, v148, v149\n"
      "v_cvt_pk_bf16_f32 v138, v120, v121\n"
      "v_mfma_f32_16x16x32_bf16 v[210:213], a[128:131], v[132:135], v[210:213]\n"
      "v_fmac_f32_e64 v171, v189, v189\n"
      "v_cvt_pk_bf16_f32 v197, v162, v163\n"
      "v_mfma_f32_16x16x32_bf16 v[210:213], a[132:135], v[154:157], v[210:213]\n"
      "ds_read_b128 v[172:175], v7 offset:18432\n"
      "v_fmac_f32_e64 v152, v160, v160\n"
      "v_mfma_f32_16x16x32_bf16 v[210:213], a[136:139], v[166:169], v[210:213]\n"
      "v_cvt_pk_bf16_f32 v149, v150, v151\n"
      "v_cvt_pk_bf16_f32 v151, v110, v111\n"
      "v_mfma_f32_16x16x32_bf16 v[210:213], a[140:143], v[220:223], v[210:213]\n"
      "v_cvt_pk_bf16_f32 v139, v122, v123\n"
      "v_fmac_f32 v171, v176, v176\n"
      "v_mfma_f32_16x16x32_bf16 v[118:121], v[144:147], a[144:147], 0\n"
      "s_waitcnt vmcnt(7)\n"
      "v_cvt_pk_bf16_f32 v128, v128, v129\n"
      "v_fmac_f32 v171, v177, v177\n"
      "v_mfma_f32_16x16x32_bf16 v[118:121], v[140:143], a[148:151], v[118:121]\n"
      "v_fmac_f32 v171, v178, v178\n"
      "v_cvt_pk_bf16_f32 v101, v102, v103\n"
      "v_mfma_f32_16x16x32_bf16 v[118:121], v[132:135], a[152:155], v[118:121]\n"
      "s_cmp_gt_u32 s42, s44\n"
      "s_cselect_b32 s95, s94, s95\n"
      "v_fmac_f32_e64 v171, v179, v179\n"
      "v_mfma_f32_16x16x32_bf16 v[118:121], v[154:157], a[156:159], v[118:121]\n"
      "v_lshlrev_b64 v[180:181], v84, v[180:181]\n"
      "v_cvt_pk_bf16_f32 v150, v108, v109\n"
      "v_mfma_f32_16x16x32_bf16 v[118:121], v[166:169], a[160:163], v[118:121]\n"
      "v_add_u32 v39, 115296, v39\n"
      "v_fmac_f32_e64 v171, v210, v210\n"
      "v_mfma_f32_16x16x32_bf16 v[118:121], v[220:223], a[164:167], v[118:121]\n"
      "v_fmac_f32 v171, v211, v211\n"
      "v_fmac_f32 v152, v161, v161\n"
      "v_mfma_f32_16x16x32_bf16 v[108:111], v[144:147], a[168:171], 0\n"
      "v_cvt_pk_bf16_f32 v129, v130, v131\n"
      "v_fmac_f32 v171, v212, v212\n"
      "v_mfma_f32_16x16x32_bf16 v[108:111], v[140:143], a[172:175], v[108:111]\n"
      "ds_read_b128 v[114:117], v2 offset:18432\n"
      "v_fmac_f32 v171, v213, v213\n"
      "v_mfma_f32_16x16x32_bf16 v[108:111], v[132:135], a[176:179], v[108:111]\n"
      "s_nop 0\n"
      "v_permlane16_swap_b32 v152, v171\n"
      "v_mfma_f32_16x16x32_bf16 v[108:111], v[154:157], a[180:183], v[108:111]\n"
      "v_add_f32 v152, v152, v171\n"
      "v_mov_b32 v171, v152\n"
      "v_mfma_f32_16x16x32_bf16 v[108:111], v[166:169], a[184:187], v[108:111]\n"
      "s_nop 0\n"
      "v_permlane32_swap_b32 v152, v171\n"
      "v_mfma_f32_16x16x32_bf16 v[108:111], v[220:223], a[188:191], v[108:111]\n"
      "ds_read_b128 v[190:193], v2 offset:21504\n"
      "v_add_f32_e64 v152, v152, v171\n"
      "v_mfma_f32_16x16x32_bf16 v[162:165], v[144:147], a[192:195], 0\n"
      "s_waitcnt lgkmcnt(2)\n"
      "v_rsq_f32 v152, v152\n"
      "v_mfma_f32_16x16x32_bf16 v[162:165], v[140:143], a[196:199], v[162:165]\n"
      "ds_read_b128 v[144:147], v2 offset:24576\n"
      "v_mov_b32 v171, v152\n"
      "v_mfma_f32_16x16x32_bf16 v[162:165], v[132:135], a[200:203], v[162:165]\n"
      "s_nop 0\n"
      "v_permlane16_swap_b32_e64 v152, v171\n"
      "v_mfma_f32_16x16x32_bf16 v[162:165], v[154:157], a[204:207], v[162:165]\n"
      "ds_read_b128 v[140:143], v7 offset:22528\n"
      "v_mul_f32_e64 v152, s50, v152\n"
      "v_mfma_f32_16x16x32_bf16 v[162:165], v[166:169], a[208:211], v[162:165]\n"
      "buffer_store_dwordx4 v[172:175], v20, s[24:27], s46 offen sc1\n"
      "v_pk_mul_f32 v[186:187], v[186:187], v[170:171] op_sel:[0,1]\n"
      "v_pk_mul_f32 v[178:179], v[178:179], v[170:171] op_sel:[0,1]\n"
      "v_pk_mul_f32 v[212:213], v[212:213], v[170:171] op_sel:[0,1]\n"
      "v_pk_mul_f32 v[176:177], v[176:177], v[170:171] op_sel:[0,1]\n"
      "v_pk_mul_f32 v[210:211], v[210:211], v[170:171] op_sel:[0,1]\n"
      "v_pk_mul_f32 v[158:159], v[158:159], v[152:153] op_sel_hi:[1,0]\n"
      "v_pk_mul_f32 v[216:217], v[216:217], v[152:153] op_sel_hi:[1,0]\n"
      "v_pk_mul_f32 v[188:189], v[188:189], v[170:171] op_sel:[0,1]\n"
      "v_mfma_f32_16x16x32_bf16 v[162:165], v[220:223], a[212:215], v[162:165]\n"
      "s_waitcnt lgkmcnt(1)\n"
      "v_cndmask_b32_e64 v180, 0, v180, s[72:73]\n"
      "v_cvt_pk_bf16_f32 v81, v110, v111\n"
      "v_mfma_f32_16x16x32_bf16 v[172:175], a[216:219], v[114:117], 0\n"
      "ds_read_b128 v[132:135], v2 offset:27648\n"
      "v_add_u32 v167, 110592, v82\n"
      "v_mfma_f32_16x16x32_bf16 v[172:175], a[220:223], v[190:193], v[172:175]\n"
      "s_waitcnt lgkmcnt(1)\n"
      "v_cvt_pk_bf16_f32 v80, v108, v109\n"
      "v_cvt_pk_bf16_f32 v186, v186, v187\n"
      "v_mfma_f32_16x16x32_bf16 v[172:175], a[224:227], v[144:147], v[172:175]\n"
      "ds_read2_b32 v[156:157], v167 offset0:0 offset1:1\n"
      "v_cndmask_b32_e64 v181, 0, v181, s[72:73]\n"
      "v_mfma_f32_16x16x32_bf16 v[220:223], a[240:243], v[114:117], 0\n"
      "ds_read_b128 v[108:111], v2 offset:33792\n"
      "v_cvt_pk_bf16_f32 v85, v164, v165\n"
      "v_mfma_f32_16x16x32_bf16 v[220:223], a[244:247], v[190:193], v[220:223]\n"
      "ds_read_b128 v[168:171], v2 offset:30720\n"
      "v_cvt_pk_bf16_f32 v187, v188, v189\n"
      "v_mfma_f32_16x16x32_bf16 v[220:223], a[248:251], v[144:147], v[220:223]\n"
      "buffer_store_dwordx4 v[140:143], v21, s[24:27], s46 offen sc1\n"
      "v_pk_mul_f32 v[160:161], v[160:161], v[152:153] op_sel_hi:[1,0]\n"
      "v_pk_mul_f32 v[218:219], v[218:219], v[152:153] op_sel_hi:[1,0]\n"
      "v_pk_mul_f32 v[208:209], v[208:209], v[152:153] op_sel_hi:[1,0]\n"
      "v_pk_mul_f32 v[206:207], v[206:207], v[152:153] op_sel_hi:[1,0]\n"
      "v_mfma_f32_16x16x32_bf16 v[140:143], v[232:235], v[114:117], 0\n"
      "ds_read2_b32 v[102:103], v167 offset0:24 offset1:18\n"
      "v_cvt_pk_bf16_f32 v158, v158, v159\n"
      "v_mfma_f32_16x16x32_bf16 v[140:143], v[236:239], v[190:193], v[140:143]\n"
      "ds_read_b128 v[152:155], v7 offset:26624\n"
      "v_cvt_pk_bf16_f32 v216, v216, v217\n"
      "v_mfma_f32_16x16x32_bf16 v[140:143], v[240:243], v[144:147], v[140:143]\n"
      "s_waitcnt lgkmcnt(2)\n"
      "v_alignbyte_b32 v156, v157, v156, v93\n"
      "v_cvt_pk_bf16_f32 v77, v120, v121\n"
      "v_mfma_f32_16x16x32_bf16 v[172:175], a[228:231], v[132:135], v[172:175]\n"
      "ds_read_b64 v[122:123], v10 offset:24\n"
      "v_cvt_pk_bf16_f32 v210, v210, v211\n"
      "v_mfma_f32_16x16x32_bf16 v[172:175], a[232:235], v[168:171], v[172:175]\n"
      "ds_read2_b32 v[164:165], v167 offset0:2 offset1:10\n"
      "v_cvt_pk_bf16_f32 v76, v118, v119\n"
      "v_mfma_f32_16x16x32_bf16 v[172:175], a[236:239], v[108:111], v[172:175]\n"
      "ds_read2_b32 v[190:191], v167 offset0:8 offset1:9\n"
      "v_cvt_pk_bf16_f32 v159, v160, v161\n"
      "v_mfma_f32_16x16x32_bf16 v[220:223], a[252:255], v[132:135], v[220:223]\n"
      "ds_read_b128 v[118:121], v7 offset:30720\n"
      "v_cvt_pk_bf16_f32 v188, v176, v177\n"
      "v_mfma_f32_16x16x32_bf16 v[220:223], v[224:227], v[168:171], v[220:223]\n"
      "s_waitcnt lgkmcnt(4)\n"
      "v_cvt_pk_bf16_f32 v189, v178, v179\n"
      "v_cvt_pk_bf16_f32 v217, v218, v219\n"
      "v_mfma_f32_16x16x32_bf16 v[220:223], v[228:231], v[108:111], v[220:223]\n"
      "ds_read2_b32 v[192:193], v167 offset0:16 offset1:17\n"
      "v_fma_f32 v160, v172, v172, s52\n"
      "v_mfma_f32_16x16x32_bf16 v[140:143], v[244:247], v[132:135], v[140:143]\n"
      "ds_read_b128 v[144:147], v10 offset:32\n"
      "v_add_u32 v115, 112160, v82\n"
      "v_mfma_f32_16x16x32_bf16 v[140:143], v[248:251], v[168:171], v[140:143]\n"
      "buffer_store_dwordx4 v[152:155], v22, s[24:27], s46 offen sc1\n"
      "v_mfma_f32_16x16x32_bf16 v[140:143], v[252:255], v[108:111], v[140:143]\n"
      "ds_read_u16 v39, v39\n"
      "v_mov_b32 v167, 0\n"
      "v_mfma_f32_16x16x32_bf16 v[132:135], v[182:185], v[198:201], v[40:43]\n"
      "s_waitcnt lgkmcnt(3)\n"
      "v_cmp_gt_u32 s[72:73], 64, v84\n"
      "v_alignbyte_b32 v157, v164, v157, v93\n"
      "v_mfma_f32_16x16x32_bf16 v[168:171], v[136:139], v[198:201], v[44:47]\n"
      "s_waitcnt lgkmcnt(2)\n"
      "v_mov_b32 v166, 0xbb2b\n"
      "v_cvt_pk_bf16_f32 v211, v212, v213\n"
      "v_mfma_f32_16x16x32_bf16 v[152:155], v[186:189], v[198:201], v[48:51]\n"
      "s_waitcnt lgkmcnt(1)\n"
      "v_alignbyte_b32 v192, v193, v192, v93\n"
      "v_lshlrev_b64 v[166:167], v84, v[166:167]\n"
      "v_mfma_f32_16x16x32_bf16 v[212:215], v[144:147], v[198:201], v[24:27]\n"
      "ds_read_b128 v[176:179], v7 offset:34816\n"
      "v_alignbyte_b32 v190, v191, v190, v93\n"
      "v_mfma_f32_16x16x16_bf16 v[132:135], v[112:113], v[202:203], v[132:135]\n"
      "v_cvt_pk_bf16_f32 v219, v208, v209\n"
      "v_alignbyte_b32 v193, v103, v193, v93\n"
      "v_mul_f32 v161, v173, v173\n"
      "v_mfma_f32_16x16x16_bf16 v[168:171], v[128:129], v[202:203], v[168:171]\n"
      "ds_read2_b32 v[130:131], v115 offset0:8 offset1:9\n"
      "v_alignbyte_b32 v191, v165, v191, v93\n"
      "v_mfma_f32_16x16x16_bf16 v[152:155], v[210:211], v[202:203], v[152:155]\n"
      "buffer_store_dwordx4 v[118:121], v23, s[24:27], s46 offen sc1\n"
      "v_mfma_f32_16x16x16_bf16 v[212:215], v[122:123], v[202:203], v[212:215]\n"
      "s_nop 0\n"
      "ds_read_b128 v[118:121], v2 offset:18688\n"
      "v_add_u32 v203, 113728, v82\n"
      "v_mfma_f32_16x16x16_bf16 v[132:135], v[180:181], v[156:157], v[132:135]\n"
      "v_cndmask_b32_e64 v166, 0, v166, s[72:73]\n"
      "v_fmac_f32_e64 v161, v175, v175\n"
      "v_alignbyte_b32 v102, v102, v102, v93\n"
      "v_mfma_f32_16x16x16_bf16 v[168:171], v[180:181], v[190:191], v[168:171]\n"
      "ds_read_b128 v[198:201], v2 offset:21760\n"
      "v_cvt_pk_bf16_f32 v218, v206, v207\n"
      "v_mfma_f32_16x16x16_bf16 v[152:155], v[180:181], v[192:193], v[152:155]\n"
      "v_cndmask_b32_e64 v167, 0, v167, s[72:73]\n"
      "v_fmac_f32_e64 v160, v174, v174\n"
      "v_cvt_pk_bf16_f32 v84, v162, v163\n"
      "v_mfma_f32_16x16x16_bf16 v[212:215], v[180:181], v[102:103], v[212:215]\n"
      "ds_read_b128 v[162:165], v2 offset:24832\n"
      "v_add_u32_e64 v12, 32, v12\n"
      "v_mfma_f32_16x16x16_bf16 v[132:135], v[166:167], v[156:157], v[132:135]\n"
      "ds_read2_b32 v[156:157], v115 offset0:2 offset1:10\n"
      "v_fmac_f32 v160, v220, v220\n"
      "v_mfma_f32_16x16x16_bf16 v[168:171], v[166:167], v[190:191], v[168:171]\n"
      "s_waitcnt lgkmcnt(3)\n"
      "v_lshlrev_b32 v39, 16, v39\n"
      "v_fmac_f32 v161, v221, v221\n"
      "v_mfma_f32_16x16x16_bf16 v[152:155], v[166:167], v[192:193], v[152:155]\n"
      "buffer_store_dwordx4 v[176:179], v25, s[24:27], s46 offen sc1\n"
      "v_pk_fma_f32 v[160:161], v[222:223], v[222:223], v[160:161]\n"
      "v_mfma_f32_16x16x16_bf16 v[212:215], v[166:167], v[102:103], v[212:215]\n"
      "ds_read2_b32 v[190:191], v115 offset0:0 offset1:1\n"
      "v_alignbyte_b32 v130, v131, v130, v93\n"
      "v_mfma_f32_16x16x32_bf16 v[206:209], a[216:219], v[118:121], 0\n"
      "s_waitcnt lgkmcnt(2)\n"
      "v_max3_f32 v102, v132, v133, v134\n"
      "v_max3_f32 v102, v102, v170, v171\n"
      "v_mfma_f32_16x16x32_bf16 v[206:209], a[220:223], v[198:201], v[206:209]\n"
      "s_waitcnt lgkmcnt(1)\n"
      "v_max3_f32 v214, v135, v168, v169\n"
      "v_max3_f32 v214, v214, v152, v153\n"
      "v_mfma_f32_16x16x32_bf16 v[206:209], a[224:227], v[162:165], v[206:209]\n"
      "v_alignbyte_b32 v131, v157, v131, v93\n"
      "v_max3_f32 v102, v102, v154, v155\n"
      "v_mfma_f32_16x16x32_bf16 v[176:179], a[240:243], v[118:121], 0\n"
      "v_max3_f32 v102, v102, v214, v212\n"
      "v_mov_b32_e64 v103, v102\n"
      "v_mfma_f32_16x16x32_bf16 v[176:179], a[244:247], v[198:201], v[176:179]\n"
      "s_nop 0\n"
      "v_permlane16_swap_b32 v102, v103\n"
      "v_mfma_f32_16x16x32_bf16 v[176:179], a[248:251], v[162:165], v[176:179]\n"
      "s_waitcnt lgkmcnt(0)\n"
      "v_max_f32 v102, v102, v103\n"
      "s_waitcnt vmcnt(6)\n"
      "v_or3_b32 v124, v124, v125, v126\n"
      "v_mfma_f32_16x16x32_bf16 v[108:111], v[232:235], v[118:121], 0\n"
      "ds_read2_b32 v[214:215], v115 offset0:16 offset1:17\n"
      "v_mov_b32 v103, v102\n"
      "v_mfma_f32_16x16x32_bf16 v[108:111], v[236:239], v[198:201], v[108:111]\n"
      "s_nop 0\n"
      "v_permlane32_swap_b32 v102, v103\n"
      "v_mfma_f32_16x16x32_bf16 v[108:111], v[240:243], v[162:165], v[108:111]\n"
      "ds_read2_b32 v[192:193], v115 offset0:24 offset1:18\n"
      "v_alignbyte_b32 v190, v191, v190, v93\n"
      "v_alignbyte_b32 v191, v156, v191, v93\n"
      "v_max_f32 v102, v102, v103\n"
      "v_sub_f32 v168, v168, v102\n"
      "v_pk_add_f32 v[134:135], v[134:135], v[102:103] op_sel_hi:[1,0] neg_lo:[0,1] neg_hi:[0,1]\n"
      "v_pk_add_f32 v[132:133], v[132:133], v[102:103] op_sel_hi:[1,0] neg_lo:[0,1] neg_hi:[0,1]\n"
      "v_pk_add_f32 v[170:171], v[170:171], v[102:103] op_sel_hi:[1,0] neg_lo:[0,1] neg_hi:[0,1]\n"
      "v_pk_add_f32 v[154:155], v[154:155], v[102:103] op_sel_hi:[1,0] neg_lo:[0,1] neg_hi:[0,1]\n"
      "v_pk_add_f32 v[152:153], v[152:153], v[102:103] op_sel_hi:[1,0] neg_lo:[0,1] neg_hi:[0,1]\n"
      "v_mfma_f32_16x16x32_bf16 v[198:201], v[182:185], v[148:151], v[52:55]\n"
      "v_exp_f32 v133, v133\n"
      "s_waitcnt vmcnt(5)\n"
      "v_or3_b32 v124, v124, v127, v78\n"
      "v_mfma_f32_16x16x32_bf16 v[118:121], v[136:139], v[148:151], v[56:59]\n"
      "ds_write_b32 v31, v124 offset:4608\n"
      "v_mfma_f32_16x16x32_bf16 v[162:165], v[186:189], v[148:151], v[60:63]\n"
      "v_exp_f32 v132, v132\n"
      "v_mfma_f32_16x16x32_bf16 v[124:127], v[144:147], v[148:151], v[26:29]\n"
      "s_waitcnt lgkmcnt(1)\n"
      "v_exp_f32 v152, v152\n"
      "v_mfma_f32_16x16x16_bf16 v[198:201], v[112:113], v[100:101], v[198:201]\n"
      "v_alignbyte_b32 v214, v215, v214, v93\n"
      "v_exp_f32 v134, v134\n"
      "v_mfma_f32_16x16x16_bf16 v[118:121], v[128:129], v[100:101], v[118:121]\n"
      "v_exp_f32 v154, v154\n"
      "v_cvt_pk_bf16_f32 v132, v132, v133\n"
      "v_mfma_f32_16x16x16_bf16 v[162:165], v[210:211], v[100:101], v[162:165]\n"
      "v_sub_f32 v169, v169, v102\n"
      "v_exp_f32 v171, v171\n"
      "v_mfma_f32_16x16x16_bf16 v[124:127], v[122:123], v[100:101], v[124:127]\n"
      "v_exp_f32 v155, v155\n"
      "v_alignbyte_b32 v215, v193, v215, v93\n"
      "v_mfma_f32_16x16x16_bf16 v[198:201], v[180:181], v[190:191], v[198:201]\n"
      "v_alignbyte_b32 v192, v192, v192, v93\n"
      "v_exp_f32 v169, v169\n"
      "v_mfma_f32_16x16x16_bf16 v[118:121], v[180:181], v[130:131], v[118:121]\n"
      "v_exp_f32 v170, v170\n"
      "v_exp_f32 v135, v135\n"
      "v_mfma_f32_16x16x16_bf16 v[162:165], v[180:181], v[214:215], v[162:165]\n"
      "v_sub_f32 v212, v212, v102\n"
      "v_exp_f32_e64 v153, v153\n"
      "v_mfma_f32_16x16x16_bf16 v[124:127], v[180:181], v[192:193], v[124:127]\n"
      "ds_read_b128 v[100:103], v2 offset:30976\n"
      "v_cvt_pk_bf16_f32 v89, v154, v155\n"
      "v_mfma_f32_16x16x16_bf16 v[198:201], v[166:167], v[190:191], v[198:201]\n"
      "ds_read_b128 v[154:157], v2 offset:27904\n"
      "v_cvt_pk_bf16_f32 v133, v134, v135\n"
      "v_mfma_f32_16x16x16_bf16 v[118:121], v[166:167], v[130:131], v[118:121]\n"
      "v_exp_f32 v212, v212\n"
      "v_cvt_pk_bf16_f32 v88, v152, v153\n"
      "v_mfma_f32_16x16x16_bf16 v[162:165], v[166:167], v[214:215], v[162:165]\n"
      "ds_read_b128 v[150:153], v2 offset:34048\n"
      "v_exp_f32 v168, v168\n"
      "v_mfma_f32_16x16x16_bf16 v[124:127], v[166:167], v[192:193], v[124:127]\n"
      "v_max3_f32 v215, v198, v199, v200\n"
      "v_cvt_pk_bf16_f32 v134, v168, v169\n"
      "v_max3_f32 v215, v215, v120, v121\n"
      "v_cvt_pk_bf16_f32 v135, v170, v171\n"
      "v_max3_f32 v78, v201, v118, v119\n"
      "v_max3_f32 v215, v215, v164, v165\n"
      "v_pk_fma_f32 v[160:161], v[140:141], v[140:141], v[160:161]\n"
      "v_pk_fma_f32 v[160:161], v[142:143], v[142:143], v[160:161]\n"
      "v_max3_f32 v78, v78, v162, v163\n"
      "v_max3_f32 v215, v215, v78, v124\n"
      "v_mov_b32 v126, v215\n"
      "s_waitcnt lgkmcnt(1)\n"
      "v_mfma_f32_16x16x32_bf16 v[206:209], a[228:231], v[154:157], v[206:209]\n"
      "v_permlane16_swap_b32 v215, v126\n"
      "v_mfma_f32_16x16x32_bf16 v[206:209], a[232:235], v[100:103], v[206:209]\n"
      "ds_read_u16 v78, v11\n"
      "s_waitcnt lgkmcnt(1)\n"
      "v_mfma_f32_16x16x32_bf16 v[206:209], a[236:239], v[150:153], v[206:209]\n"
      "v_add_f32 v160, v160, v161\n"
      "v_max_f32 v215, v215, v126\n"
      "v_mfma_f32_16x16x32_bf16 v[176:179], a[252:255], v[154:157], v[176:179]\n"
      "ds_read2_b32 v[114:115], v203 offset0:16 offset1:17\n"
      "v_mov_b32 v126, v215\n"
      "v_mfma_f32_16x16x32_bf16 v[176:179], v[224:227], v[100:103], v[176:179]\n"
      "ds_read_u16 v82, v11 offset:32\n"
      "v_cvt_pk_bf16_f32 v90, v212, 0\n"
      "v_permlane32_swap_b32 v215, v126\n"
      "v_max_f32_e64 v215, v215, v126\n"
      "v_pk_add_f32 v[118:119], v[118:119], v[214:215] op_sel:[0,1] neg_lo:[0,1] neg_hi:[0,1]\n"
      "v_pk_add_f32 v[120:121], v[120:121], v[214:215] op_sel:[0,1] neg_lo:[0,1] neg_hi:[0,1]\n"
      "v_pk_add_f32 v[164:165], v[164:165], v[214:215] op_sel:[0,1] neg_lo:[0,1] neg_hi:[0,1]\n"
      "v_pk_add_f32 v[200:201], v[200:201], v[214:215] op_sel:[0,1] neg_lo:[0,1] neg_hi:[0,1]\n"
      "v_pk_add_f32 v[198:199], v[198:199], v[214:215] op_sel:[0,1] neg_lo:[0,1] neg_hi:[0,1]\n"
      "v_mfma_f32_16x16x32_bf16 v[176:179], v[228:231], v[150:153], v[176:179]\n"
      "v_exp_f32_e64 v200, v200\n"
      "v_mfma_f32_16x16x32_bf16 v[108:111], v[244:247], v[154:157], v[108:111]\n"
      "s_waitcnt lgkmcnt(2)\n"
      "v_exp_f32 v198, v198\n"
      "v_mfma_f32_16x16x32_bf16 v[108:111], v[248:251], v[100:103], v[108:111]\n"
      "ds_read2_b32 v[116:117], v203 offset0:0 offset1:1\n"
      "v_exp_f32 v199, v199\n"
      "v_mfma_f32_16x16x32_bf16 v[108:111], v[252:255], v[150:153], v[108:111]\n"
      "ds_read2_b32 v[126:127], v203 offset0:24 offset1:18\n"
      "v_sub_f32 v124, v124, v215\n"
      "v_mfma_f32_16x16x32_bf16 v[168:171], v[32:35], v[132:135], 0\n"
      "ds_read_u16 v86, v11 offset:64\n"
      "v_cvt_pk_bf16_f32 v198, v198, v199\n"
      "v_mfma_f32_16x16x32_bf16 v[168:171], v[32:35], v[88:91], v[168:171]\n"
      "ds_read2_b32 v[150:151], v203 offset0:2 offset1:10\n"
      "v_exp_f32 v121, v121\n"
      "v_exp_f32 v201, v201\n"
      "v_pk_add_f32 v[162:163], v[162:163], v[214:215] op_sel:[0,1] neg_lo:[0,1] neg_hi:[0,1]\n"
      "v_mfma_f32_16x16x32_bf16 v[152:155], v[194:197], v[132:135], 0\n"
      "ds_read2_b32 v[156:157], v203 offset0:8 offset1:9\n"
      "v_cvt_pk_bf16_f32 v199, v200, v201\n"
      "v_mfma_f32_16x16x32_bf16 v[152:155], v[76:79], v[88:91], v[152:155]\n"
      "v_exp_f32 v120, v120\n"
      "v_mfma_f32_16x16x32_bf16 v[212:215], v[104:107], v[132:135], 0\n"
      "s_waitcnt lgkmcnt(3)\n"
      "v_cvt_pk_bf16_f32 v201, v120, v121\n"
      "v_alignbyte_b32 v126, v126, v126, v93\n"
      "v_mfma_f32_16x16x32_bf16 v[212:215], v[80:83], v[88:91], v[212:215]\n"
      "v_exp_f32_e64 v119, v119\n"
      "v_mfma_f32_16x16x32_bf16 v[100:103], v[96:99], v[132:135], 0\n"
      "s_waitcnt lgkmcnt(1)\n"
      "v_exp_f32_e64 v118, v118\n"
      "v_mfma_f32_16x16x32_bf16 v[100:103], v[84:87], v[88:91], v[100:103]\n"
      "v_cvt_pk_bf16_f32 v200, v118, v119\n"
      "v_alignbyte_b32 v116, v117, v116, v93\n"
      "v_alignbyte_b32 v114, v115, v114, v93\n"
      "v_mfma_f32_16x16x32_bf16 v[190:193], v[182:185], v[216:219], v[64:67]\n"
      "s_waitcnt lgkmcnt(0)\n"
      "v_alignbyte_b32 v156, v157, v156, v93\n"
      "v_alignbyte_b32 v117, v150, v117, v93\n"
      "v_mfma_f32_16x16x32_bf16 v[202:205], v[136:139], v[216:219], v[68:71]\n"
      "v_alignbyte_b32 v115, v127, v115, v93\n"
      "v_alignbyte_b32 v157, v151, v157, v93\n"
      "v_mfma_f32_16x16x32_bf16 v[148:151], v[186:189], v[216:219], v[72:75]\n"
      "v_rcp_f32 v168, v168\n"
      "v_mfma_f32_16x16x32_bf16 v[130:133], v[144:147], v[216:219], v[28:31]\n"
      "v_mul_f32 v154, v168, v154\n"
      "v_mul_f32 v214, v168, v214\n"
      "v_mfma_f32_16x16x16_bf16 v[190:193], v[112:113], v[158:159], v[190:193]\n"
      "v_mul_f32 v152, v168, v152\n"
      "v_exp_f32 v162, v162\n"
      "v_mfma_f32_16x16x16_bf16 v[202:205], v[128:129], v[158:159], v[202:205]\n"
      "v_exp_f32 v124, v124\n"
      "v_mul_f32 v153, v168, v153\n"
      "v_mfma_f32_16x16x16_bf16 v[148:151], v[210:211], v[158:159], v[148:151]\n"
      "v_exp_f32 v165, v165\n"
      "v_mul_f32_e64 v155, v168, v155\n"
      "v_mfma_f32_16x16x16_bf16 v[130:133], v[122:123], v[158:159], v[130:133]\n"
      "v_cvt_pk_bf16_f32 v152, v152, v153\n"
      "v_exp_f32 v164, v164\n"
      "v_mfma_f32_16x16x16_bf16 v[190:193], v[180:181], v[116:117], v[190:193]\n"
      "v_cvt_pk_bf16_f32 v153, v154, v155\n"
      "v_exp_f32 v163, v163\n"
      "v_mfma_f32_16x16x16_bf16 v[202:205], v[180:181], v[156:157], v[202:205]\n"
      "v_mul_f32_e64 v159, v207, v207\n"
      "v_cvt_pk_bf16_f32 v93, v164, v165\n"
      "v_fma_f32 v158, v206, v206, s52\n"
      "v_mfma_f32_16x16x16_bf16 v[148:151], v[180:181], v[114:115], v[148:151]\n"
      "v_cvt_pk_bf16_f32 v92, v162, v163\n"
      "v_mul_f32 v213, v168, v213\n"
      "v_fmac_f32 v159, v209, v209\n"
      "v_mfma_f32_16x16x16_bf16 v[130:133], v[180:181], v[126:127], v[130:133]\n"
      "v_mul_f32 v212, v168, v212\n"
      "v_fmac_f32 v158, v208, v208\n"
      "v_mul_f32_e64 v215, v168, v215\n"
      "v_mfma_f32_16x16x16_bf16 v[190:193], v[166:167], v[116:117], v[190:193]\n"
      "ds_read_b128 v[162:165], v2 offset:25088\n"
      "v_cvt_pk_bf16_f32 v154, v212, v213\n"
      "v_mfma_f32_16x16x16_bf16 v[202:205], v[166:167], v[156:157], v[202:205]\n"
      "ds_read_b128 v[216:219], v2 offset:22016\n"
      "v_fmac_f32_e64 v159, v177, v177\n"
      "v_mfma_f32_16x16x16_bf16 v[148:151], v[166:167], v[114:115], v[148:151]\n"
      "ds_read_b128 v[114:117], v2 offset:18944\n"
      "v_cvt_pk_bf16_f32 v155, v214, v215\n"
      "v_mfma_f32_16x16x16_bf16 v[130:133], v[166:167], v[126:127], v[130:133]\n"
      "v_fmac_f32_e64 v158, v176, v176\n"
      "v_cvt_pk_bf16_f32 v94, v124, 0\n"
      "v_max3_f32 v167, v190, v191, v192\n"
      "v_max3_f32 v167, v167, v204, v205\n"
      "v_max3_f32 v134, v193, v202, v203\n"
      "v_max3_f32 v167, v167, v150, v151\n"
      "v_pk_mul_f32 v[102:103], v[102:103], v[168:169] op_sel_hi:[1,0]\n"
      "v_max3_f32 v134, v134, v148, v149\n"
      "v_max3_f32 v167, v167, v134, v130\n"
      "v_mov_b32 v134, v167\n"
      "v_pk_fma_f32 v[158:159], v[178:179], v[178:179], v[158:159]\n"
      "v_pk_mul_f32 v[100:101], v[100:101], v[168:169] op_sel_hi:[1,0]\n"
      "v_permlane16_swap_b32 v167, v134\n"
      "v_max_f32 v167, v167, v134\n"
      "v_mov_b32 v134, v167\n"
      "v_pk_fma_f32 v[158:159], v[108:109], v[108:109], v[158:159]\n"
      "v_pk_fma_f32 v[158:159], v[110:111], v[110:111], v[158:159]\n"
      "v_permlane32_swap_b32_e64 v167, v134\n"
      "v_max_f32 v167, v167, v134\n"
      "v_pk_add_f32 v[192:193], v[192:193], v[166:167] op_sel:[0,1] neg_lo:[0,1] neg_hi:[0,1]\n"
      "s_waitcnt lgkmcnt(0)\n"
      "v_mfma_f32_16x16x32_bf16 v[132:135], a[216:219], v[114:117], 0\n"
      "ds_write_b128 v3, v[152:155]\n"
      "v_pk_add_f32 v[190:191], v[190:191], v[166:167] op_sel:[0,1] neg_lo:[0,1] neg_hi:[0,1]\n"
      "v_pk_add_f32 v[204:205], v[204:205], v[166:167] op_sel:[0,1] neg_lo:[0,1] neg_hi:[0,1]\n"
      "v_pk_add_f32 v[150:151], v[150:151], v[166:167] op_sel:[0,1] neg_lo:[0,1] neg_hi:[0,1]\n"
      "v_mfma_f32_16x16x32_bf16 v[132:135], a[220:223], v[216:219], v[132:135]\n"
      "v_sub_f32 v130, v130, v167\n"
      "v_cvt_pk_bf16_f32 v100, v100, v101\n"
      "v_mfma_f32_16x16x32_bf16 v[132:135], a[224:227], v[162:165], v[132:135]\n"
      "v_cvt_pk_bf16_f32 v101, v102, v103\n"
      "v_sub_f32 v202, v202, v167\n"
      "v_mfma_f32_16x16x32_bf16 v[118:121], a[240:243], v[114:117], 0\n"
      "ds_read_b128 v[152:155], v10\n"
      "v_sub_f32 v149, v149, v167\n"
      "v_mfma_f32_16x16x32_bf16 v[118:121], a[244:247], v[216:219], v[118:121]\n"
      "v_exp_f32 v151, v151\n"
      "v_mfma_f32_16x16x32_bf16 v[118:121], a[248:251], v[162:165], v[118:121]\n"
      "v_add_f32 v158, v158, v159\n"
      "v_sub_f32 v148, v148, v167\n"
      "v_mfma_f32_16x16x32_bf16 v[168:171], v[232:235], v[114:117], 0\n"
      "v_exp_f32_e64 v205, v205\n"
      "v_mfma_f32_16x16x32_bf16 v[168:171], v[236:239], v[216:219], v[168:171]\n"
      "v_exp_f32 v190, v190\n"
      "v_mfma_f32_16x16x32_bf16 v[168:171], v[240:243], v[162:165], v[168:171]\n"
      "v_exp_f32 v191, v191\n"
      "v_mfma_f32_16x16x32_bf16 v[162:165], v[32:35], v[198:201], 0\n"
      "v_exp_f32 v204, v204\n"
      "v_mfma_f32_16x16x32_bf16 v[162:165], v[32:35], v[92:95], v[162:165]\n"
      "v_exp_f32 v192, v192\n"
      "v_mfma_f32_16x16x32_bf16 v[212:215], v[194:197], v[198:201], 0\n"
      "ds_write_b64 v4, v[100:101]\n"
      "v_mfma_f32_16x16x32_bf16 v[212:215], v[76:79], v[92:95], v[212:215]\n"
      "ds_read_b64 v[156:157], v10 offset:16\n"
      "v_sub_f32 v203, v203, v167\n"
      "v_mfma_f32_16x16x32_bf16 v[216:219], v[104:107], v[198:201], 0\n"
      "s_nop 0\n"
      "v_rcp_f32 v162, v162\n"
      "v_mfma_f32_16x16x32_bf16 v[216:219], v[80:83], v[92:95], v[216:219]\n"
      "v_exp_f32 v150, v150\n"
      "v_mfma_f32_16x16x32_bf16 v[164:167], v[96:99], v[198:201], 0\n"
      "v_exp_f32_e64 v193, v193\n"
      "v_mfma_f32_16x16x32_bf16 v[164:167], v[84:87], v[92:95], v[164:167]\n"
      "s_waitcnt lgkmcnt(2)\n"
      "v_exp_f32 v149, v149\n"
      "v_mfma_f32_16x16x32_bf16 v[198:201], v[152:155], v[182:185], v[30:33]\n"
      "v_exp_f32_e64 v202, v202\n"
      "v_mfma_f32_16x16x32_bf16 v[180:183], v[152:155], v[136:139], v[30:33]\n"
      "ds_read_b128 v[124:127], v2 offset:28160\n"
      "v_cvt_pk_bf16_f32 v89, v150, v151\n"
      "v_mfma_f32_16x16x32_bf16 v[136:139], v[152:155], v[186:189], v[30:33]\n"
      "s_waitcnt lgkmcnt(1)\n"
      "v_exp_f32 v148, v148\n"
      "v_mfma_f32_16x16x32_bf16 v[100:103], v[152:155], v[144:147], v[30:33]\n"
      "ds_read_b128 v[144:147], v2 offset:31232\n"
      "v_cvt_pk_bf16_f32 v190, v190, v191\n"
      "v_mfma_f32_16x16x16_bf16 v[198:201], v[156:157], v[112:113], v[198:201]\n"
      "ds_read_b128 v[152:155], v2 offset:34304\n"
      "v_mul_f32_e64 v164, v162, v164\n"
      "v_mfma_f32_16x16x16_bf16 v[180:183], v[156:157], v[128:129], v[180:183]\n"
      "v_cvt_pk_bf16_f32 v191, v192, v193\n"
      "v_permlane16_swap_b32 v160, v158\n"
      "v_mfma_f32_16x16x16_bf16 v[136:139], v[156:157], v[210:211], v[136:139]\n"
      "v_mul_f32 v216, v162, v216\n"
      "v_cvt_pk_bf16_f32 v193, v204, v205\n"
      "v_add_f32_e64 v160, v160, v158\n"
      "v_mul_f32_e64 v165, v162, v165\n"
      "v_exp_f32_e64 v203, v203\n"
      "v_pk_mul_f32 v[212:213], v[212:213], v[162:163] op_sel_hi:[1,0]\n"
      "v_pk_mul_f32 v[218:219], v[218:219], v[162:163] op_sel_hi:[1,0]\n"
      "v_pk_mul_f32 v[214:215], v[214:215], v[162:163] op_sel_hi:[1,0]\n"
      "v_mfma_f32_16x16x16_bf16 v[100:103], v[156:157], v[122:123], v[100:103]\n"
      "v_exp_f32 v130, v130\n"
      "s_waitcnt lgkmcnt(1)\n"
      "v_mfma_f32_16x16x32_bf16 v[132:135], a[228:231], v[124:127], v[132:135]\n"
      "v_cndmask_b32 v198, v198, v180, s[58:59]\n"
      "v_cvt_pk_bf16_f32 v90, v130, 0\n"
      "v_mfma_f32_16x16x32_bf16 v[132:135], a[232:235], v[144:147], v[132:135]\n"
      "s_waitcnt lgkmcnt(0)\n"
      "v_cvt_pk_bf16_f32 v212, v212, v213\n"
      "v_cndmask_b32 v198, v198, v136, s[60:61]\n"
      "v_mfma_f32_16x16x32_bf16 v[132:135], a[236:239], v[152:155], v[132:135]\n"
      "v_cndmask_b32 v198, v198, v100, s[62:63]\n"
      "v_fmac_f32_e64 v198, s53, v39\n"
      "v_mfma_f32_16x16x32_bf16 v[118:121], a[252:255], v[124:127], v[118:121]\n"
      "s_nop 0\n"
      "v_max_f32_dpp v180, v198, v198 row_ror:8 row_mask:0xf bank_mask:0xf\n"
      "v_cvt_pk_bf16_f32 v88, v148, v149\n"
      "v_mfma_f32_16x16x32_bf16 v[118:121], v[224:227], v[144:147], v[118:121]\n"
      "v_cvt_pk_bf16_f32 v213, v214, v215\n"
      "v_fma_f32 v200, v132, v132, s52\n"
      "v_mfma_f32_16x16x32_bf16 v[118:121], v[228:231], v[152:155], v[118:121]\n"
      "v_mul_f32 v217, v162, v217\n"
      "v_max_f32_dpp v180, v180, v180 row_ror:4 row_mask:0xf bank_mask:0xf\n"
      "v_mfma_f32_16x16x32_bf16 v[168:171], v[244:247], v[124:127], v[168:171]\n"
      "s_nop 0\n"
      "v_max_f32_dpp v180, v180, v180 row_ror:2 row_mask:0xf bank_mask:0xf\n"
      "v_cvt_pk_bf16_f32 v215, v218, v219\n"
      "v_mfma_f32_16x16x32_bf16 v[168:171], v[248:251], v[144:147], v[168:171]\n"
      "v_cvt_pk_bf16_f32 v192, v202, v203\n"
      "v_max_f32_dpp v180, v180, v180 row_ror:1 row_mask:0xf bank_mask:0xf\n"
      "v_mfma_f32_16x16x32_bf16 v[168:171], v[252:255], v[152:155], v[168:171]\n"
      "s_nop 0\n"
      "v_max_f32_dpp v180, v180, v180 row_bcast:15 row_mask:0xa bank_mask:0xf\n"
      "v_cvt_pk_bf16_f32 v214, v216, v217\n"
      "v_mfma_f32_16x16x32_bf16 v[152:155], v[32:35], v[190:193], 0\n"
      "v_mul_f32 v201, v133, v133\n"
      "v_max_f32_dpp v180, v180, v180 row_bcast:31 row_mask:0xc bank_mask:0xf\n"
      "v_mfma_f32_16x16x32_bf16 v[152:155], v[32:35], v[88:91], v[152:155]\n"
      "v_cvt_pk_bf16_f32 v164, v164, v165\n"
      "v_readlane_b32 s72, v180, 63\n"
      "v_mfma_f32_16x16x32_bf16 v[180:183], v[194:197], v[190:193], 0\n"
      "ds_write_b128 v3, v[212:215] offset:256\n"
      "v_pk_mul_f32 v[166:167], v[166:167], v[162:163] op_sel_hi:[1,0]\n"
      "v_pk_fma_f32 v[200:201], v[134:135], v[134:135], v[200:201]\n"
      "v_pk_fma_f32 v[200:201], v[118:119], v[118:119], v[200:201]\n"
      "v_mfma_f32_16x16x32_bf16 v[180:183], v[76:79], v[88:91], v[180:183]\n"
      "v_subrev_f32 v198, s72, v198\n"
      "v_cvt_pk_bf16_f32 v165, v166, v167\n"
      "v_mfma_f32_16x16x32_bf16 v[210:213], v[104:107], v[190:193], 0\n"
      "s_and_b32 s5, s84, s8\n"
      "v_rcp_f32_e64 v152, v152\n"
      "v_mfma_f32_16x16x32_bf16 v[210:213], v[80:83], v[88:91], v[210:213]\n"
      "ds_write_b64 v4, v[164:165] offset:256\n"
      "v_exp_f32_e64 v198, v198\n"
      "v_pk_fma_f32 v[200:201], v[120:121], v[120:121], v[200:201]\n"
      "v_pk_mul_f32 v[182:183], v[182:183], v[152:153] op_sel_hi:[1,0]\n"
      "v_cvt_pk_bf16_f32 v198, v198, v198\n"
      "v_pk_mul_f32 v[180:181], v[180:181], v[152:153] op_sel_hi:[1,0]\n"
      "v_pk_fma_f32 v[200:201], v[168:169], v[168:169], v[200:201]\n"
      "v_pk_fma_f32 v[200:201], v[170:171], v[170:171], v[200:201]\n"
      "v_cvt_pk_bf16_f32 v180, v180, v181\n"
      "v_pk_mul_f32 v[210:211], v[210:211], v[152:153] op_sel_hi:[1,0]\n"
      "v_cvt_pk_bf16_f32 v181, v182, v183\n"
      "v_pk_mul_f32 v[212:213], v[212:213], v[152:153] op_sel_hi:[1,0]\n"
      "v_mfma_f32_16x16x32_bf16 v[162:165], v[96:99], v[190:193], 0\n"
      "ds_write_b16 v13, v198\n"
      "v_cvt_pk_bf16_f32 v182, v210, v211\n"
      "v_mfma_f32_16x16x32_bf16 v[162:165], v[84:87], v[88:91], v[162:165]\n"
      "ds_read_b128 v[144:147], v14\n"
      "v_add_f32 v200, v200, v201\n"
      "v_cvt_pk_bf16_f32 v183, v212, v213\n"
      "v_mov_b32 v158, v200\n"
      "s_add_u32 s42, s42, 1\n"
      "ds_write_b128 v3, v[180:183] offset:512\n"
      "v_permlane16_swap_b32 v200, v158\n"
      "v_add_f32_e64 v200, v200, v158\n"
      "v_pk_mul_f32 v[162:163], v[162:163], v[152:153] op_sel_hi:[1,0]\n"
      "v_pk_mul_f32 v[164:165], v[164:165], v[152:153] op_sel_hi:[1,0]\n"
      "v_permlane32_swap_b32 v160, v200\n"
      "v_add_f32 v160, v160, v200\n"
      "s_add_u32 s43, s43, 1\n"
      "ds_write_b32 v31, v160\n"
      "v_cvt_pk_bf16_f32 v162, v162, v163\n"
      "v_cvt_pk_bf16_f32 v163, v164, v165\n"
      "ds_read_b128 v[198:201], v14 offset:64\n"
      "ds_write_b64 v4, v[162:163] offset:512\n"
      "s_waitcnt lgkmcnt(4)\n"
      "v_mfma_f32_16x16x32_bf16 v[162:165], v[144:147], v[32:35], 0\n"
      "s_waitcnt lgkmcnt(1)\n"
      "v_mfma_f32_16x16x32_bf16 v[162:165], v[198:201], v[32:35], v[162:165]\n"
      "s_cmp_ge_u32 s95, 0x40000000\n"
      "s_cselect_b64 vcc, -1, s[56:57]\n"
      "s_waitcnt lgkmcnt(0)\n"
      "s_barrier\n"
      "s_cbranch_vccz L_sel1_%=\n"
      "s_branch L_disp1_%=\n"
      "L_tail1_%=:\n"
      "s_nop 4\n"
      "ds_read_b128 v[128:131], v9\n"
      "v_mfma_f32_16x16x32_bf16 v[152:155], v[144:147], v[194:197], 0\n"
      "ds_read_b128 v[194:197], v2\n"
      "ds_read_b128 v[136:139], v2 offset:6144\n"
      "v_mfma_f32_16x16x32_bf16 v[152:155], v[198:201], v[76:79], v[152:155]\n"
      "ds_read_b128 v[124:127], v2 offset:6400\n"
      "ds_read_b128 v[216:219], v2 offset:3072\n"
      "v_mfma_f32_16x16x32_bf16 v[210:213], v[144:147], v[104:107], 0\n"
      "ds_read_b128 v[104:107], v7\n"
      "ds_read_b128 v[202:205], v2 offset:12288\n"
      "ds_read_b128 v[184:187], v2 offset:3328\n"
      "v_mfma_f32_16x16x32_bf16 v[210:213], v[198:201], v[80:83], v[210:213]\n"
      "ds_read_b128 v[148:151], v2 offset:9216\n"
      "v_mfma_f32_16x16x32_bf16 v[180:183], v[144:147], v[96:99], 0\n"
      "s_waitcnt lgkmcnt(8)\n"
      "v_add_f32 v130, v130, v131\n"
      "v_add_f32 v128, v128, v129\n"
      "v_add_f32 v128, v128, v130\n"
      "v_rsq_f32 v128, v128\n"
      "v_mfma_f32_16x16x32_bf16 v[180:183], v[198:201], v[84:87], v[180:183]\n"
      "s_waitcnt lgkmcnt(7)\n"
      "v_mul_f32 v128, 0x415db3d7, v128\n"
      "v_mov_b32 v130, v128\n"
      "s_nop 1\n"
      "v_permlane32_swap_b32 v128, v130\n"
      "v_pk_mul_f32 v[168:169], v[168:169], v[130:131] op_sel_hi:[1,0]\n"
      "v_mov_b32 v129, v128\n"
      "v_pk_mul_f32 v[134:135], v[134:135], v[130:131] op_sel_hi:[1,0]\n"
      "v_pk_mul_f32 v[170:171], v[170:171], v[130:131] op_sel_hi:[1,0]\n"
      "v_permlane16_swap_b32_e64 v128, v129\n"
      "v_pk_mul_f32 v[206:207], v[206:207], v[128:129] op_sel:[0,1]\n"
      "v_pk_mul_f32 v[142:143], v[142:143], v[128:129] op_sel_hi:[1,0]\n"
      "v_pk_mul_f32 v[120:121], v[120:121], v[130:131] op_sel_hi:[1,0]\n"
      "v_pk_mul_f32 v[110:111], v[110:111], v[128:129] op_sel:[0,1]\n"
      "v_pk_mul_f32 v[172:173], v[172:173], v[128:129] op_sel_hi:[1,0]\n"
      "v_pk_mul_f32 v[140:141], v[140:141], v[128:129] op_sel_hi:[1,0]\n"
      "v_pk_mul_f32 v[132:133], v[132:133], v[130:131] op_sel_hi:[1,0]\n"
      "v_pk_mul_f32 v[118:119], v[118:119], v[130:131] op_sel_hi:[1,0]\n"
      "v_pk_mul_f32 v[174:175], v[174:175], v[128:129] op_sel_hi:[1,0]\n"
      "v_cvt_pk_bf16_f32 v140, v140, v141\n"
      "v_pk_mul_f32 v[176:177], v[176:177], v[128:129] op_sel:[0,1]\n"
      "v_cvt_pk_bf16_f32 v206, v206, v207\n"
      "v_cvt_pk_bf16_f32 v132, v132, v133\n"
      "v_pk_mul_f32 v[178:179], v[178:179], v[128:129] op_sel:[0,1]\n"
      "v_cvt_pk_bf16_f32 v172, v172, v173\n"
      "v_pk_mul_f32 v[208:209], v[208:209], v[128:129] op_sel:[0,1]\n"
      "v_pk_mul_f32 v[108:109], v[108:109], v[128:129] op_sel:[0,1]\n"
      "v_cvt_pk_bf16_f32 v108, v108, v109\n"
      "v_cvt_pk_bf16_f32 v207, v208, v209\n"
      "v_pk_mul_f32 v[220:221], v[220:221], v[128:129] op_sel_hi:[1,0]\n"
      "v_pk_mul_f32 v[222:223], v[222:223], v[128:129] op_sel_hi:[1,0]\n"
      "v_cvt_pk_bf16_f32 v141, v142, v143\n"
      "v_cvt_pk_bf16_f32 v133, v134, v135\n"
      "v_cvt_pk_bf16_f32 v209, v178, v179\n"
      "v_cvt_pk_bf16_f32 v109, v110, v111\n"
      "v_cvt_pk_bf16_f32 v168, v168, v169\n"
      "v_cvt_pk_bf16_f32 v173, v174, v175\n"
      "v_cvt_pk_bf16_f32 v175, v222, v223\n"
      "v_cvt_pk_bf16_f32 v134, v118, v119\n"
      "v_cvt_pk_bf16_f32 v169, v170, v171\n"
      "v_cvt_pk_bf16_f32 v208, v176, v177\n"
      "v_cvt_pk_bf16_f32 v135, v120, v121\n"
      "v_cvt_pk_bf16_f32 v174, v220, v221\n"
      "s_mul_i32 s92, s40, 3072\n"
      "ds_write_b128 v5, v[132:135] offset:30720\n"
      "ds_write_b128 v5, v[206:209] offset:24576\n"
      "ds_write_b64 v6, v[168:169] offset:30720\n"
      "ds_write_b64 v6, v[140:141] offset:18432\n"
      "ds_write_b128 v5, v[172:175] offset:18432\n"
      "ds_write_b64 v6, v[108:109] offset:24576\n"
      "v_mfma_f32_16x16x32_bf16 v[172:175], a[216:219], v[194:197], 0\n"
      "ds_read_b128 v[206:209], v2 offset:15360\n"
      "s_waitcnt lgkmcnt(6)\n"
      "v_mfma_f32_16x16x32_bf16 v[172:175], a[220:223], v[216:219], v[172:175]\n"
      "ds_read_b128 v[144:147], v2 offset:15872\n"
      "v_mov_b32_dpp v162, v162 quad_perm:[0,0,0,0] row_mask:0xf bank_mask:0xf\n"
      "v_mfma_f32_16x16x32_bf16 v[220:223], a[240:243], v[194:197], 0\n"
      "ds_read_b128 v[132:135], v7 offset:8192\n"
      "v_subrev_u32 v163, 115456, v9\n"
      "v_mfma_f32_16x16x32_bf16 v[220:223], a[244:247], v[216:219], v[220:223]\n"
      "ds_read_b128 v[96:99], v2 offset:12800\n"
      "v_subrev_u32 v85, 115456, v9\n"
      "v_mfma_f32_16x16x32_bf16 v[140:143], v[232:235], v[194:197], 0\n"
      "v_add_u32 v86, s92, v163\n"
      "v_mfma_f32_16x16x32_bf16 v[140:143], v[236:239], v[216:219], v[140:143]\n"
      "buffer_store_dwordx4 v[104:107], v20, s[24:27], s85 offen sc1\n"
      "v_mfma_f32_16x16x32_bf16 v[172:175], a[224:227], v[136:139], v[172:175]\n"
      "ds_read_b128 v[118:121], v2 offset:256\n"
      "v_lshrrev_b32_e64 v163, 4, v86\n"
      "v_mfma_f32_16x16x32_bf16 v[172:175], a[228:231], v[148:151], v[172:175]\n"
      "v_and_b32 v84, 0x3fffffff, v37\n"
      "v_mfma_f32_16x16x32_bf16 v[220:223], a[248:251], v[136:139], v[220:223]\n"
      "ds_read_b128 v[168:171], v2 offset:15616\n"
      "v_mfma_f32_16x16x32_bf16 v[220:223], a[252:255], v[148:151], v[220:223]\n"
      "ds_read_b128 v[194:197], v7 offset:4096\n"
      "v_mfma_f32_16x16x32_bf16 v[140:143], v[240:243], v[136:139], v[140:143]\n"
      "ds_read_b128 v[104:107], v2 offset:12544\n"
      "v_mfma_f32_16x16x32_bf16 v[140:143], v[244:247], v[148:151], v[140:143]\n"
      "ds_read_b128 v[198:201], v2 offset:6656\n"
      "v_mfma_f32_16x16x32_bf16 v[172:175], a[232:235], v[202:205], v[172:175]\n"
      "s_waitcnt lgkmcnt(6)\n"
      "v_add_u32 v218, 128, v163\n"
      "v_add_u32 v80, 64, v163\n"
      "v_mfma_f32_16x16x32_bf16 v[172:175], a[236:239], v[206:209], v[172:175]\n"
      "ds_read_b128 v[212:215], v2 offset:9472\n"
      "v_mfma_f32_16x16x32_bf16 v[220:223], v[224:227], v[202:205], v[220:223]\n"
      "v_ashrrev_i32 v81, 31, v37\n"
      "v_and_b32 v148, 0x300, v85\n"
      "v_mfma_f32_16x16x32_bf16 v[220:223], v[228:231], v[206:209], v[220:223]\n"
      "ds_read_b128 v[164:167], v2 offset:512\n"
      "v_add_u32 v85, v85, v148\n"
      "v_mfma_f32_16x16x32_bf16 v[140:143], v[248:251], v[202:205], v[140:143]\n"
      "v_cndmask_b32 v152, v152, v210, s[58:59]\n"
      "v_mfma_f32_16x16x32_bf16 v[140:143], v[252:255], v[206:209], v[140:143]\n"
      "s_waitcnt lgkmcnt(3)\n"
      "v_add_u32 v85, 150784, v85\n"
      "v_fma_f32 v148, v172, v172, s52\n"
      "v_mfma_f32_16x16x32_bf16 v[206:209], a[216:219], v[118:121], 0\n"
      "v_mul_f32 v149, v173, v173\n"
      "v_mul_u32_u24 v76, 2731, v218\n"
      "v_mfma_f32_16x16x32_bf16 v[206:209], a[220:223], v[184:187], v[206:209]\n"
      "v_subrev_u32 v82, 73728, v6\n"
      "v_fmac_f32_e64 v149, v175, v175\n"
      "v_mfma_f32_16x16x32_bf16 v[176:179], a[240:243], v[118:121], 0\n"
      "ds_read_b128 v[154:157], v2 offset:9728\n"
      "v_fmac_f32 v148, v174, v174\n"
      "v_mfma_f32_16x16x32_bf16 v[176:179], a[244:247], v[184:187], v[176:179]\n"
      "v_fmac_f32 v149, v221, v221\n"
      "v_fmac_f32 v148, v220, v220\n"
      "v_mfma_f32_16x16x32_bf16 v[108:111], v[232:235], v[118:121], 0\n"
      "ds_read_b128 v[202:205], v2 offset:3584\n"
      "v_fmac_f32 v149, v223, v223\n"
      "v_mfma_f32_16x16x32_bf16 v[108:111], v[236:239], v[184:187], v[108:111]\n"
      "s_waitcnt lgkmcnt(2)\n"
      "v_add_u32 v84, 1179648, v84\n"
      "v_lshrrev_b32 v76, 16, v76\n"
      "v_mfma_f32_16x16x32_bf16 v[206:209], a[224:227], v[124:127], v[206:209]\n"
      "ds_read_b128 v[182:185], v85 offset:4096\n"
      "v_mfma_f32_16x16x32_bf16 v[206:209], a[228:231], v[212:215], v[206:209]\n"
      "buffer_store_dwordx4 v[194:197], v21, s[24:27], s85 offen sc1\n"
      "v_mfma_f32_16x16x32_bf16 v[176:179], a[248:251], v[124:127], v[176:179]\n"
      "s_nop 0\n"
      "ds_read_b128 v[194:197], v7 offset:16384\n"
      "v_and_b32 v81, 100270464, v81\n"
      "v_mfma_f32_16x16x32_bf16 v[176:179], a[252:255], v[212:215], v[176:179]\n"
      "v_fmac_f32 v148, v222, v222\n"
      "v_sub_u32_e64 v84, v84, v81\n"
      "v_mfma_f32_16x16x32_bf16 v[108:111], v[240:243], v[124:127], v[108:111]\n"
      "v_rcp_f32 v162, v162\n"
      "v_mfma_f32_16x16x32_bf16 v[108:111], v[244:247], v[212:215], v[108:111]\n"
      "v_mul_u32_u24 v77, 2731, v80\n"
      "v_fmac_f32 v148, v140, v140\n"
      "v_mfma_f32_16x16x32_bf16 v[206:209], a[232:235], v[104:107], v[206:209]\n"
      "v_cndmask_b32 v152, v152, v180, s[60:61]\n"
      "v_fmac_f32 v149, v141, v141\n"
      "v_mfma_f32_16x16x32_bf16 v[206:209], a[236:239], v[168:171], v[206:209]\n"
      "v_add_u32 v78, 0, v163\n"
      "v_mul_u32_u24 v190, 24, v76\n"
      "v_mfma_f32_16x16x32_bf16 v[176:179], v[224:227], v[104:107], v[176:179]\n"
      "v_mul_u32_u24 v100, 2731, v78\n"
      "v_mul_f32 v152, v162, v152\n"
      "v_mfma_f32_16x16x32_bf16 v[176:179], v[228:231], v[168:171], v[176:179]\n"
      "buffer_store_dwordx4 v[132:135], v22, s[24:27], s85 offen sc1\n"
      "v_mfma_f32_16x16x32_bf16 v[108:111], v[248:251], v[104:107], v[108:111]\n"
      "ds_read_b128 v[104:107], v85 offset:6144\n"
      "v_mul_f32_e64 v187, v207, v207\n"
      "v_mfma_f32_16x16x32_bf16 v[108:111], v[252:255], v[168:171], v[108:111]\n"
      "v_fma_f32 v186, v206, v206, s52\n"
      "v_fmac_f32 v187, v209, v209\n"
      "v_mfma_f32_16x16x32_bf16 v[132:135], a[216:219], v[164:167], 0\n"
      "s_waitcnt lgkmcnt(1)\n"
      "v_cvt_pk_bf16_f32 v152, v152, v152\n"
      "v_fmac_f32 v148, v142, v142\n"
      "v_mfma_f32_16x16x32_bf16 v[132:135], a[220:223], v[202:205], v[132:135]\n"
      "v_fmac_f32 v186, v208, v208\n"
      "v_mfma_f32_16x16x32_bf16 v[118:121], a[240:243], v[164:167], 0\n"
      "ds_read_b128 v[210:213], v85 offset:10240\n"
      "v_fmac_f32 v186, v176, v176\n"
      "v_mfma_f32_16x16x32_bf16 v[118:121], a[244:247], v[202:205], v[118:121]\n"
      "ds_read_b128 v[160:163], v85 offset:8192\n"
      "v_lshrrev_b32 v100, 16, v100\n"
      "v_mfma_f32_16x16x32_bf16 v[168:171], v[232:235], v[164:167], 0\n"
      "ds_write_b16 v12, v152\n"
      "v_mfma_f32_16x16x32_bf16 v[168:171], v[236:239], v[202:205], v[168:171]\n"
      "ds_read_b128 v[164:167], v7 offset:12288\n"
      "v_fmac_f32 v187, v177, v177\n"
      "v_mfma_f32_16x16x32_bf16 v[132:135], a[224:227], v[198:201], v[132:135]\n"
      "v_fmac_f32 v187, v179, v179\n"
      "v_fmac_f32 v186, v178, v178\n"
      "v_mfma_f32_16x16x32_bf16 v[132:135], a[228:231], v[154:157], v[132:135]\n"
      "v_fmac_f32 v149, v143, v143\n"
      "v_mfma_f32_16x16x32_bf16 v[118:121], a[248:251], v[198:201], v[118:121]\n"
      "ds_read_b128 v[150:153], v85\n"
      "v_mfma_f32_16x16x32_bf16 v[118:121], a[252:255], v[154:157], v[118:121]\n"
      "ds_read_b128 v[124:127], v85 offset:2048\n"
      "v_add_f32 v148, v148, v149\n"
      "v_mfma_f32_16x16x32_bf16 v[168:171], v[240:243], v[198:201], v[168:171]\n"
      "v_fmac_f32 v186, v108, v108\n"
      "v_fmac_f32 v187, v109, v109\n"
      "v_mfma_f32_16x16x32_bf16 v[168:171], v[244:247], v[154:157], v[168:171]\n"
      "v_fmac_f32 v187, v111, v111\n"
      "v_fmac_f32 v186, v110, v110\n"
      "v_mfma_f32_16x16x32_bf16 v[132:135], a[232:235], v[96:99], v[132:135]\n"
      "s_waitcnt lgkmcnt(2)\n"
      "v_add_f32 v186, v186, v187\n"
      "v_mul_u32_u24 v156, 24, v100\n"
      "v_mfma_f32_16x16x32_bf16 v[132:135], a[236:239], v[144:147], v[132:135]\n"
      "buffer_store_dwordx4 v[164:167], v23, s[24:27], s85 offen sc1\n"
      "v_mfma_f32_16x16x32_bf16 v[118:121], v[224:227], v[96:99], v[118:121]\n"
      "v_permlane16_swap_b32_e64 v148, v186\n"
      "v_mfma_f32_16x16x32_bf16 v[118:121], v[228:231], v[144:147], v[118:121]\n"
      "v_bfe_i32 v81, v37, 30, 1\n"
      "v_add_f32 v148, v148, v186\n"
      "v_mfma_f32_16x16x32_bf16 v[168:171], v[248:251], v[96:99], v[168:171]\n"
      "s_nop 0\n"
      "v_mul_f32_e64 v155, v133, v133\n"
      "v_fma_f32 v154, v132, v132, s52\n"
      "v_mfma_f32_16x16x32_bf16 v[168:171], v[252:255], v[144:147], v[168:171]\n"
      "s_waitcnt lgkmcnt(0)\n"
      "v_fmac_f32 v155, v135, v135\n"
      "v_fmac_f32_e64 v154, v134, v134\n"
      "v_mfma_f32_16x16x32_bf16 v[144:147], a[216:219], v[150:153], 0\n"
      "v_fmac_f32 v155, v119, v119\n"
      "v_fmac_f32 v154, v118, v118\n"
      "v_mfma_f32_16x16x32_bf16 v[144:147], a[220:223], v[124:127], v[144:147]\n"
      "v_fmac_f32 v155, v121, v121\n"
      "v_fmac_f32 v154, v120, v120\n"
      "v_mfma_f32_16x16x32_bf16 v[96:99], a[240:243], v[150:153], 0\n"
      "buffer_store_dwordx4 v[194:197], v25, s[24:27], s85 offen sc1\n"
      "v_pk_fma_f32 v[154:155], v[168:169], v[168:169], v[154:155]\n"
      "v_pk_fma_f32 v[154:155], v[170:171], v[170:171], v[154:155]\n"
      "v_mfma_f32_16x16x32_bf16 v[96:99], a[244:247], v[124:127], v[96:99]\n"
      "v_add_f32 v154, v154, v155\n"
      "v_mov_b32 v186, v154\n"
      "v_mfma_f32_16x16x32_bf16 v[194:197], v[232:235], v[150:153], 0\n"
      "s_nop 0\n"
      "v_permlane16_swap_b32 v154, v186\n"
      "v_mfma_f32_16x16x32_bf16 v[194:197], v[236:239], v[124:127], v[194:197]\n"
      "v_add_f32 v154, v154, v186\n"
      "v_mfma_f32_16x16x32_bf16 v[144:147], a[224:227], v[182:185], v[144:147]\n"
      "s_nop 0\n"
      "v_permlane32_swap_b32 v148, v154\n"
      "v_add_f32 v148, v148, v154\n"
      "v_mfma_f32_16x16x32_bf16 v[144:147], a[228:231], v[104:107], v[144:147]\n"
      "ds_write_b32 v31, v148 offset:1024\n"
      "v_mfma_f32_16x16x32_bf16 v[96:99], a[248:251], v[182:185], v[96:99]\n"
      "v_subrev_u32 v157, 73728, v5\n"
      "v_mfma_f32_16x16x32_bf16 v[96:99], a[252:255], v[104:107], v[96:99]\n"
      "v_sub_u32 v190, v218, v190\n"
      "v_readlane_b32 s85, v37, s42\n"
      "v_mfma_f32_16x16x32_bf16 v[194:197], v[240:243], v[182:185], v[194:197]\n"
      "s_or_b32 s95, s85, s48\n"
      "v_lshrrev_b32 v77, 16, v77\n"
      "v_sub_u32 v156, v78, v156\n"
      "v_and_b32 v218, 7, v76\n"
      "v_mfma_f32_16x16x32_bf16 v[194:197], v[244:247], v[104:107], v[194:197]\n"
      "v_xor_b32 v190, v190, v218\n"
      "v_mfma_f32_16x16x32_bf16 v[144:147], a[232:235], v[160:163], v[144:147]\n"
      "s_cmp_ge_u32 s95, 0x40000000\n"
      "s_cbranch_scc1 L_rare_27_%=\n"
      "L_back_28_%=:\n"
      "v_mfma_f32_16x16x32_bf16 v[144:147], a[236:239], v[210:213], v[144:147]\n"
      "s_waitcnt lgkmcnt(0)\n"
      "s_barrier\n"
      "ds_read_b128 v[104:107], v9 offset:1024\n"
      "v_mfma_f32_16x16x32_bf16 v[96:99], v[224:227], v[160:163], v[96:99]\n"
      "ds_read_b128 v[152:155], v85 offset:256\n"
      "ds_read_b128 v[198:201], v85 offset:2304\n"
      "v_mfma_f32_16x16x32_bf16 v[96:99], v[228:231], v[210:213], v[96:99]\n"
      "ds_read_b128 v[180:183], v7 offset:18432\n"
      "v_fma_f32 v218, v144, v144, s52\n"
      "v_mul_f32_e64 v219, v145, v145\n"
      "v_mfma_f32_16x16x32_bf16 v[194:197], v[248:251], v[160:163], v[194:197]\n"
      "ds_read_b128 v[162:165], v7 offset:22528\n"
      "ds_read_b128 v[202:205], v7 offset:26624\n"
      "ds_read_b128 v[184:187], v85 offset:4352\n"
      "v_pk_fma_f32 v[218:219], v[146:147], v[146:147], v[218:219]\n"
      "v_pk_fma_f32 v[218:219], v[96:97], v[96:97], v[218:219]\n"
      "v_mfma_f32_16x16x32_bf16 v[194:197], v[252:255], v[210:213], v[194:197]\n"
      "ds_read_b128 v[210:213], v7 offset:34816\n"
      "s_waitcnt lgkmcnt(7)\n"
      "v_add_f32 v104, v104, v105\n"
      "v_add_f32 v106, v106, v107\n"
      "v_add_f32 v104, v104, v106\n"
      "v_rsq_f32_e64 v104, v104\n"
      "s_waitcnt lgkmcnt(6)\n"
      "v_mfma_f32_16x16x32_bf16 v[148:151], a[216:219], v[152:155], 0\n"
      "s_waitcnt lgkmcnt(5)\n"
      "v_mul_f32 v104, 0x415db3d7, v104\n"
      "v_mov_b32_e64 v106, v104\n"
      "v_mfma_f32_16x16x32_bf16 v[148:151], a[220:223], v[198:201], v[148:151]\n"
      "ds_read_b128 v[214:217], v85 offset:6400\n"
      "ds_read_b128 v[136:139], v85 offset:8448\n"
      "v_mfma_f32_16x16x32_bf16 v[124:127], a[240:243], v[152:155], 0\n"
      "s_waitcnt lgkmcnt(5)\n"
      "v_permlane32_swap_b32 v104, v106\n"
      "v_mfma_f32_16x16x32_bf16 v[124:127], a[244:247], v[198:201], v[124:127]\n"
      "buffer_store_dwordx4 v[180:183], v20, s[24:27], s85 offen sc1\n"
      "v_mov_b32_e64 v105, v104\n"
      "v_pk_mul_f32 v[132:133], v[132:133], v[106:107] op_sel_hi:[1,0]\n"
      "v_pk_mul_f32 v[134:135], v[134:135], v[106:107] op_sel_hi:[1,0]\n"
      "v_mfma_f32_16x16x32_bf16 v[180:183], v[232:235], v[152:155], 0\n"
      "s_waitcnt lgkmcnt(2)\n"
      "v_permlane16_swap_b32_e64 v104, v105\n"
      "v_mfma_f32_16x16x32_bf16 v[180:183], v[236:239], v[198:201], v[180:183]\n"
      "buffer_store_dwordx4 v[162:165], v21, s[24:27], s85 offen sc1\n"
      "v_pk_fma_f32 v[218:219], v[98:99], v[98:99], v[218:219]\n"
      "v_pk_mul_f32 v[170:171], v[170:171], v[106:107] op_sel_hi:[1,0]\n"
      "v_pk_mul_f32 v[118:119], v[118:119], v[106:107] op_sel_hi:[1,0]\n"
      "v_pk_mul_f32 v[172:173], v[172:173], v[104:105] op_sel_hi:[1,0]\n"
      "v_pk_mul_f32 v[174:175], v[174:175], v[104:105] op_sel_hi:[1,0]\n"
      "v_pk_mul_f32 v[168:169], v[168:169], v[106:107] op_sel_hi:[1,0]\n"
      "v_pk_mul_f32 v[140:141], v[140:141], v[104:105] op_sel_hi:[1,0]\n"
      "v_pk_mul_f32 v[110:111], v[110:111], v[104:105] op_sel:[0,1]\n"
      "v_mfma_f32_16x16x32_bf16 v[148:151], a[224:227], v[184:187], v[148:151]\n"
      "s_waitcnt lgkmcnt(0)\n"
      "v_cvt_pk_bf16_f32 v172, v172, v173\n"
      "v_cvt_pk_bf16_f32 v168, v168, v169\n"
      "v_mfma_f32_16x16x32_bf16 v[148:151], a[228:231], v[214:217], v[148:151]\n"
      "buffer_store_dwordx4 v[202:205], v22, s[24:27], s85 offen sc1\n"
      "v_pk_mul_f32 v[208:209], v[208:209], v[104:105] op_sel:[0,1]\n"
      "v_pk_mul_f32 v[220:221], v[220:221], v[104:105] op_sel_hi:[1,0]\n"
      "v_pk_mul_f32 v[108:109], v[108:109], v[104:105] op_sel:[0,1]\n"
      "v_mfma_f32_16x16x32_bf16 v[124:127], a[248:251], v[184:187], v[124:127]\n"
      "ds_read_b128 v[198:201], v85 offset:10496\n"
      "v_cvt_pk_bf16_f32 v173, v174, v175\n"
      "v_mfma_f32_16x16x32_bf16 v[124:127], a[252:255], v[214:217], v[124:127]\n"
      "v_cvt_pk_bf16_f32 v169, v170, v171\n"
      "v_cvt_pk_bf16_f32 v108, v108, v109\n"
      "v_cvt_pk_bf16_f32 v174, v220, v221\n"
      "v_cvt_pk_bf16_f32 v132, v132, v133\n"
      "v_cvt_pk_bf16_f32 v133, v134, v135\n"
      "v_pk_mul_f32 v[142:143], v[142:143], v[104:105] op_sel_hi:[1,0]\n"
      "v_pk_mul_f32 v[120:121], v[120:121], v[106:107] op_sel_hi:[1,0]\n"
      "v_pk_mul_f32 v[222:223], v[222:223], v[104:105] op_sel_hi:[1,0]\n"
      "v_pk_mul_f32 v[206:207], v[206:207], v[104:105] op_sel:[0,1]\n"
      "v_mfma_f32_16x16x32_bf16 v[180:183], v[240:243], v[184:187], v[180:183]\n"
      "ds_read_b128 v[162:165], v7 offset:30720\n"
      "v_cvt_pk_bf16_f32 v134, v118, v119\n"
      "v_mfma_f32_16x16x32_bf16 v[180:183], v[244:247], v[214:217], v[180:183]\n"
      "ds_write_b64 v6, v[168:169] offset:12288\n"
      "v_cvt_pk_bf16_f32 v135, v120, v121\n"
      "v_mfma_f32_16x16x32_bf16 v[148:151], a[232:235], v[136:139], v[148:151]\n"
      "s_waitcnt lgkmcnt(2)\n"
      "v_cvt_pk_bf16_f32 v140, v140, v141\n"
      "v_cvt_pk_bf16_f32 v175, v222, v223\n"
      "v_mfma_f32_16x16x32_bf16 v[148:151], a[236:239], v[198:201], v[148:151]\n"
      "ds_write_b128 v5, v[172:175]\n"
      "v_mfma_f32_16x16x32_bf16 v[124:127], v[224:227], v[136:139], v[124:127]\n"
      "v_cvt_pk_bf16_f32 v141, v142, v143\n"
      "v_cvt_pk_bf16_f32 v109, v110, v111\n"
      "v_mfma_f32_16x16x32_bf16 v[124:127], v[228:231], v[198:201], v[124:127]\n"
      "ds_write_b64 v6, v[108:109] offset:6144\n"
      "v_mfma_f32_16x16x32_bf16 v[180:183], v[248:251], v[136:139], v[180:183]\n"
      "ds_write_b64 v6, v[140:141]\n"
      "v_mfma_f32_16x16x32_bf16 v[180:183], v[252:255], v[198:201], v[180:183]\n"
      "ds_write_b128 v5, v[132:135] offset:12288\n"
      "v_fma_f32 v134, v148, v148, s52\n"
      "v_mul_f32 v135, v149, v149\n"
      "v_pk_fma_f32 v[134:135], v[150:151], v[150:151], v[134:135]\n"
      "v_pk_fma_f32 v[134:135], v[124:125], v[124:125], v[134:135]\n"
      "v_pk_fma_f32 v[134:135], v[126:127], v[126:127], v[134:135]\n"
      "v_pk_fma_f32 v[218:219], v[194:195], v[194:195], v[218:219]\n"
      "v_pk_fma_f32 v[218:219], v[196:197], v[196:197], v[218:219]\n"
      "v_pk_fma_f32 v[134:135], v[180:181], v[180:181], v[134:135]\n"
      "v_pk_fma_f32 v[134:135], v[182:183], v[182:183], v[134:135]\n"
      "v_add_f32 v218, v218, v219\n"
      "v_add_f32 v134, v134, v135\n"
      "v_cvt_pk_bf16_f32 v206, v206, v207\n"
      "v_pk_mul_f32 v[176:177], v[176:177], v[104:105] op_sel:[0,1]\n"
      "v_cvt_pk_bf16_f32 v207, v208, v209\n"
      "v_permlane16_swap_b32 v218, v134\n"
      "v_add_f32 v218, v218, v134\n"
      "v_mov_b32 v134, v218\n"
      "v_pk_mul_f32 v[178:179], v[178:179], v[104:105] op_sel:[0,1]\n"
      "v_cvt_pk_bf16_f32 v209, v178, v179\n"
      "v_cvt_pk_bf16_f32 v208, v176, v177\n"
      "v_permlane32_swap_b32 v218, v134\n"
      "v_add_f32_e64 v218, v218, v134\n"
      "s_add_u32 s42, s42, 1\n"
      "ds_write_b128 v5, v[206:209] offset:6144\n"
      "ds_write_b32 v31, v218 offset:4608\n"
      "s_waitcnt lgkmcnt(2)\n"
      "buffer_store_dwordx4 v[162:165], v23, s[24:27], s85 offen sc1\n"
      "v_mul_u32_u24 v105, 24, v77\n"
      "v_and_b32 v81, 196224, v81\n"
      "v_and_b32 v78, 7, v100\n"
      "v_sub_u32_e64 v105, v80, v105\n"
      "v_lshl_add_u32 v100, v100, 2, 12\n"
      "v_and_b32_e64 v80, 7, v77\n"
      "buffer_store_dwordx4 v[210:213], v25, s[24:27], s85 offen sc1\n"
      "s_waitcnt lgkmcnt(0)\n"
      "s_barrier\n"
      "ds_read_b128 v[206:209], v9 offset:4608\n"
      "v_lshl_add_u32 v76, v76, 2, 12\n"
      "v_xor_b32_e64 v105, v105, v80\n"
      "v_readlane_b32 s85, v37, s42\n"
      "v_sub_u32 v84, v84, v81\n"
      "s_or_b32 s95, s85, s48\n"
      "ds_read_b128 v[162:165], v7\n"
      "ds_read_b128 v[176:179], v7 offset:8192\n"
      "ds_bpermute_b32 v81, v76, v84\n"
      "v_lshl_add_u32 v77, v77, 2, 12\n"
      "s_cmp_ge_u32 s95, 0x40000000\n"
      "s_cbranch_scc1 L_rare_31_%=\n"
      "L_back_32_%=:\n"
      "ds_read_b128 v[132:135], v7 offset:12288\n"
      "ds_read_b128 v[198:201], v7 offset:4096\n"
      "s_waitcnt lgkmcnt(5)\n"
      "v_add_f32 v208, v208, v209\n"
      "v_add_f32 v206, v206, v207\n"
      "v_add_f32 v206, v206, v208\n"
      "v_rsq_f32 v206, v206\n"
      "s_nop 0\n"
      "v_mul_f32 v206, 0x415db3d7, v206\n"
      "v_mov_b32 v208, v206\n"
      "s_nop 1\n"
      "v_permlane32_swap_b32 v206, v208\n"
      "v_mov_b32_e64 v207, v206\n"
      "s_waitcnt lgkmcnt(2)\n"
      "buffer_store_dwordx4 v[162:165], v20, s[24:27], s85 offen sc1\n"
      "v_permlane16_swap_b32_e64 v206, v207\n"
      "v_pk_mul_f32 v[146:147], v[146:147], v[206:207] op_sel_hi:[1,0]\n"
      "v_pk_mul_f32 v[144:145], v[144:145], v[206:207] op_sel_hi:[1,0]\n"
      "v_pk_mul_f32 v[126:127], v[126:127], v[206:207] op_sel:[0,1]\n"
      "v_pk_mul_f32 v[150:151], v[150:151], v[206:207] op_sel:[0,1]\n"
      "v_pk_mul_f32 v[98:99], v[98:99], v[206:207] op_sel_hi:[1,0]\n"
      "v_cvt_pk_bf16_f32 v144, v144, v145\n"
      "v_pk_mul_f32 v[148:149], v[148:149], v[206:207] op_sel:[0,1]\n"
      "v_pk_mul_f32 v[124:125], v[124:125], v[206:207] op_sel:[0,1]\n"
      "v_cvt_pk_bf16_f32 v145, v146, v147\n"
      "v_cvt_pk_bf16_f32 v148, v148, v149\n"
      "v_pk_mul_f32 v[182:183], v[182:183], v[206:207] op_sel:[0,1]\n"
      "v_pk_mul_f32 v[96:97], v[96:97], v[206:207] op_sel_hi:[1,0]\n"
      "v_cvt_pk_bf16_f32 v147, v98, v99\n"
      "v_pk_mul_f32 v[180:181], v[180:181], v[206:207] op_sel:[0,1]\n"
      "v_pk_mul_f32 v[194:195], v[194:195], v[206:207] op_sel_hi:[1,0]\n"
      "v_cvt_pk_bf16_f32 v149, v150, v151\n"
      "v_cvt_pk_bf16_f32 v146, v96, v97\n"
      "ds_write_b128 v157, v[144:147]\n"
      "v_cvt_pk_bf16_f32 v180, v180, v181\n"
      "v_cvt_pk_bf16_f32 v150, v124, v125\n"
      "v_cvt_pk_bf16_f32 v151, v126, v127\n"
      "v_cvt_pk_bf16_f32 v194, v194, v195\n"
      "v_cvt_pk_bf16_f32 v181, v182, v183\n"
      "ds_write_b128 v157, v[148:151] offset:6144\n"
      "ds_write_b64 v82, v[180:181] offset:6144\n"
      "v_pk_mul_f32 v[196:197], v[196:197], v[206:207] op_sel_hi:[1,0]\n"
      "v_cvt_pk_bf16_f32 v195, v196, v197\n"
      "s_add_u32 s42, s42, 1\n"
      "ds_write_b64 v82, v[194:195]\n"
      "v_xor_b32_e64 v156, v156, v78\n"
      "s_waitcnt lgkmcnt(3)\n"
      "buffer_store_dwordx4 v[198:201], v21, s[24:27], s85 offen sc1\n"
      "s_nop 1\n"
      "ds_read_b128 v[198:201], v7 offset:16384\n"
      "ds_bpermute_b32 v78, v100, v84\n"
      "buffer_store_dwordx4 v[176:179], v22, s[24:27], s85 offen sc1\n"
      "s_waitcnt lgkmcnt(2)\n"
      "s_barrier\n"
      "ds_read_b128 v[176:179], v86\n"
      "buffer_store_dwordx4 v[132:135], v23, s[24:27], s85 offen sc1\n"
      "ds_bpermute_b32 v194, v77, v84\n"
      "s_nop 0\n"
      "ds_read_b128 v[132:135], v86 offset:1024\n"
      "s_waitcnt lgkmcnt(3)\n"
      "buffer_store_dwordx4 v[198:201], v25, s[24:27], s85 offen sc1\n"
      "s_nop 1\n"
      "ds_read_b128 v[198:201], v86 offset:2048\n"
      "v_lshl_add_u32 v78, v156, 4, v78\n"
      "v_lshl_add_u32 v81, v190, 4, v81\n"
      "s_waitcnt lgkmcnt(3)\n"
      "buffer_store_dwordx4 v[176:179], v78, s[24:27], 0 offen sc1\n"
      "s_waitcnt lgkmcnt(2)\n"
      "v_lshl_add_u32 v194, v105, 4, v194\n"
      "s_waitcnt lgkmcnt(1)\n"
      "buffer_store_dwordx4 v[132:135], v194, s[24:27], 0 offen sc1\n"
      "s_waitcnt lgkmcnt(0)\n"
      "buffer_store_dwordx4 v[198:201], v81, s[24:27], 0 offen sc1\n"
      "s_endpgm\n"
      "L_tail0_%=:\n"
      "s_nop 4\n"
      "ds_read_b128 v[148:151], v9 offset:1024\n"
      "v_mfma_f32_16x16x32_bf16 v[152:155], v[144:147], v[194:197], 0\n"
      "ds_read_b128 v[194:197], v2 offset:18432\n"
      "v_subrev_u32 v185, 115456, v9\n"
      "v_mfma_f32_16x16x32_bf16 v[152:155], v[198:201], v[76:79], v[152:155]\n"
      "ds_read_b128 v[124:127], v2 offset:27648\n"
      "ds_read_b128 v[216:219], v2 offset:30720\n"
      "ds_read_b128 v[202:205], v2 offset:24576\n"
      "v_mfma_f32_16x16x32_bf16 v[210:213], v[144:147], v[104:107], 0\n"
      "ds_read_b128 v[136:139], v2 offset:18688\n"
      "v_and_b32 v76, 0x3fffffff, v37\n"
      "v_mfma_f32_16x16x32_bf16 v[210:213], v[198:201], v[80:83], v[210:213]\n"
      "ds_read_b128 v[104:107], v2 offset:21504\n"
      "v_mfma_f32_16x16x32_bf16 v[180:183], v[144:147], v[96:99], 0\n"
      "ds_read_b128 v[144:147], v2 offset:33792\n"
      "s_waitcnt lgkmcnt(7)\n"
      "v_add_f32 v148, v148, v149\n"
      "v_add_f32 v150, v150, v151\n"
      "v_add_f32 v148, v148, v150\n"
      "v_rsq_f32_e64 v148, v148\n"
      "v_cndmask_b32 v152, v152, v210, s[58:59]\n"
      "v_mul_f32 v148, 0x415db3d7, v148\n"
      "v_mov_b32 v150, v148\n"
      "s_mul_i32 s92, s40, 3072\n"
      "v_ashrrev_i32 v80, 31, v37\n"
      "v_permlane32_swap_b32 v148, v150\n"
      "v_mov_b32 v149, v148\n"
      "v_pk_mul_f32 v[132:133], v[132:133], v[150:151] op_sel_hi:[1,0]\n"
      "v_pk_mul_f32 v[118:119], v[118:119], v[150:151] op_sel_hi:[1,0]\n"
      "v_pk_mul_f32 v[120:121], v[120:121], v[150:151] op_sel_hi:[1,0]\n"
      "v_permlane16_swap_b32_e64 v148, v149\n"
      "v_pk_mul_f32 v[208:209], v[208:209], v[148:149] op_sel:[0,1]\n"
      "v_pk_mul_f32 v[178:179], v[178:179], v[148:149] op_sel:[0,1]\n"
      "v_cvt_pk_bf16_f32 v132, v132, v133\n"
      "v_pk_mul_f32 v[206:207], v[206:207], v[148:149] op_sel:[0,1]\n"
      "v_pk_mul_f32 v[168:169], v[168:169], v[150:151] op_sel_hi:[1,0]\n"
      "v_pk_mul_f32 v[220:221], v[220:221], v[148:149] op_sel_hi:[1,0]\n"
      "v_pk_mul_f32 v[174:175], v[174:175], v[148:149] op_sel_hi:[1,0]\n"
      "v_pk_mul_f32 v[108:109], v[108:109], v[148:149] op_sel:[0,1]\n"
      "v_pk_mul_f32 v[172:173], v[172:173], v[148:149] op_sel_hi:[1,0]\n"
      "v_cvt_pk_bf16_f32 v172, v172, v173\n"
      "v_cvt_pk_bf16_f32 v108, v108, v109\n"
      "v_pk_mul_f32 v[140:141], v[140:141], v[148:149] op_sel_hi:[1,0]\n"
      "v_cvt_pk_bf16_f32 v140, v140, v141\n"
      "v_pk_mul_f32 v[134:135], v[134:135], v[150:151] op_sel_hi:[1,0]\n"
      "v_cvt_pk_bf16_f32 v168, v168, v169\n"
      "v_cvt_pk_bf16_f32 v133, v134, v135\n"
      "v_pk_mul_f32 v[176:177], v[176:177], v[148:149] op_sel:[0,1]\n"
      "v_cvt_pk_bf16_f32 v206, v206, v207\n"
      "v_pk_mul_f32 v[110:111], v[110:111], v[148:149] op_sel:[0,1]\n"
      "v_cvt_pk_bf16_f32 v207, v208, v209\n"
      "v_pk_mul_f32 v[142:143], v[142:143], v[148:149] op_sel_hi:[1,0]\n"
      "v_cvt_pk_bf16_f32 v109, v110, v111\n"
      "v_pk_mul_f32 v[222:223], v[222:223], v[148:149] op_sel_hi:[1,0]\n"
      "v_pk_mul_f32 v[170:171], v[170:171], v[150:151] op_sel_hi:[1,0]\n"
      "v_cvt_pk_bf16_f32 v209, v178, v179\n"
      "v_cvt_pk_bf16_f32 v173, v174, v175\n"
      "v_cvt_pk_bf16_f32 v174, v220, v221\n"
      "v_cvt_pk_bf16_f32 v141, v142, v143\n"
      "v_cvt_pk_bf16_f32 v208, v176, v177\n"
      "v_cvt_pk_bf16_f32 v175, v222, v223\n"
      "v_cvt_pk_bf16_f32 v169, v170, v171\n"
      "v_cvt_pk_bf16_f32 v135, v120, v121\n"
      "v_cvt_pk_bf16_f32 v134, v118, v119\n"
      "v_mfma_f32_16x16x32_bf16 v[180:183], v[198:201], v[84:87], v[180:183]\n"
      "ds_write_b64 v6, v[168:169] offset:12288\n"
      "ds_write_b128 v5, v[172:175]\n"
      "ds_write_b64 v6, v[108:109] offset:6144\n"
      "ds_write_b64 v6, v[140:141]\n"
      "ds_write_b128 v5, v[206:209] offset:6144\n"
      "ds_write_b128 v5, v[132:135] offset:12288\n"
      "s_waitcnt lgkmcnt(5)\n"
      "v_mfma_f32_16x16x32_bf16 v[172:175], a[216:219], v[194:197], 0\n"
      "ds_read_b128 v[132:135], v7 offset:18432\n"
      "v_mov_b32_dpp v162, v162 quad_perm:[0,0,0,0] row_mask:0xf bank_mask:0xf\n"
      "v_mfma_f32_16x16x32_bf16 v[172:175], a[220:223], v[104:107], v[172:175]\n"
      "ds_read_b128 v[210:213], v2 offset:18944\n"
      "v_and_b32 v80, 100270464, v80\n"
      "v_mfma_f32_16x16x32_bf16 v[220:223], a[240:243], v[194:197], 0\n"
      "ds_read_b128 v[118:121], v2 offset:27904\n"
      "v_add_u32 v86, s92, v185\n"
      "v_mfma_f32_16x16x32_bf16 v[220:223], a[244:247], v[104:107], v[220:223]\n"
      "v_rcp_f32_e64 v162, v162\n"
      "v_mfma_f32_16x16x32_bf16 v[140:143], v[232:235], v[194:197], 0\n"
      "v_subrev_u32 v186, 115456, v9\n"
      "v_and_b32 v194, 0x300, v186\n"
      "v_mfma_f32_16x16x32_bf16 v[140:143], v[236:239], v[104:107], v[140:143]\n"
      "v_add_u32_e64 v186, v186, v194\n"
      "v_mfma_f32_16x16x32_bf16 v[172:175], a[224:227], v[202:205], v[172:175]\n"
      "ds_read_b128 v[104:107], v2 offset:25088\n"
      "v_mfma_f32_16x16x32_bf16 v[172:175], a[228:231], v[124:127], v[172:175]\n"
      "ds_read_b128 v[148:151], v7 offset:26624\n"
      "v_lshrrev_b32_e64 v185, 4, v86\n"
      "v_mfma_f32_16x16x32_bf16 v[220:223], a[248:251], v[202:205], v[220:223]\n"
      "ds_read_b128 v[168:171], v2 offset:30976\n"
      "v_mfma_f32_16x16x32_bf16 v[220:223], a[252:255], v[124:127], v[220:223]\n"
      "ds_read_b128 v[194:197], v2 offset:21760\n"
      "v_add_u32_e64 v78, 64, v185\n"
      "v_mfma_f32_16x16x32_bf16 v[140:143], v[240:243], v[202:205], v[140:143]\n"
      "v_add_u32 v186, 150784, v186\n"
      "v_mfma_f32_16x16x32_bf16 v[140:143], v[244:247], v[124:127], v[140:143]\n"
      "ds_read_b128 v[124:127], v186 offset:4096\n"
      "v_cndmask_b32 v152, v152, v180, s[60:61]\n"
      "v_mfma_f32_16x16x32_bf16 v[172:175], a[232:235], v[216:219], v[172:175]\n"
      "ds_read_b128 v[180:183], v2 offset:24832\n"
      "v_mfma_f32_16x16x32_bf16 v[172:175], a[236:239], v[144:147], v[172:175]\n"
      "v_mul_u32_u24 v81, 2731, v78\n"
      "v_mfma_f32_16x16x32_bf16 v[220:223], v[224:227], v[216:219], v[220:223]\n"
      "v_add_u32 v100, 128, v185\n"
      "v_mfma_f32_16x16x32_bf16 v[220:223], v[228:231], v[144:147], v[220:223]\n"
      "ds_read_b128 v[200:203], v186 offset:2048\n"
      "v_mfma_f32_16x16x32_bf16 v[140:143], v[248:251], v[216:219], v[140:143]\n"
      "ds_read_b128 v[216:219], v186\n"
      "v_mfma_f32_16x16x32_bf16 v[140:143], v[252:255], v[144:147], v[140:143]\n"
      "ds_read_b128 v[144:147], v2 offset:34048\n"
      "v_mfma_f32_16x16x32_bf16 v[206:209], a[216:219], v[136:139], 0\n"
      "s_waitcnt lgkmcnt(5)\n"
      "v_fma_f32 v204, v172, v172, s52\n"
      "v_mfma_f32_16x16x32_bf16 v[206:209], a[220:223], v[194:197], v[206:209]\n"
      "ds_read_b128 v[154:157], v2 offset:34304\n"
      "v_mfma_f32_16x16x32_bf16 v[176:179], a[240:243], v[136:139], 0\n"
      "v_mul_f32 v205, v173, v173\n"
      "v_mul_f32 v152, v162, v152\n"
      "v_mfma_f32_16x16x32_bf16 v[176:179], a[244:247], v[194:197], v[176:179]\n"
      "v_fmac_f32 v204, v174, v174\n"
      "v_add_u32 v76, 1179648, v76\n"
      "v_mfma_f32_16x16x32_bf16 v[108:111], v[232:235], v[136:139], 0\n"
      "s_waitcnt lgkmcnt(2)\n"
      "v_fmac_f32 v205, v175, v175\n"
      "v_mfma_f32_16x16x32_bf16 v[108:111], v[236:239], v[194:197], v[108:111]\n"
      "ds_read_b128 v[164:167], v7 offset:34816\n"
      "v_add_u32_e64 v82, 0, v185\n"
      "v_mfma_f32_16x16x32_bf16 v[206:209], a[224:227], v[180:183], v[206:209]\n"
      "ds_read_b128 v[194:197], v7 offset:22528\n"
      "v_mul_u32_u24 v190, 2731, v82\n"
      "v_mfma_f32_16x16x32_bf16 v[206:209], a[228:231], v[118:121], v[206:209]\n"
      "v_sub_u32 v76, v76, v80\n"
      "v_lshrrev_b32 v81, 16, v81\n"
      "v_mfma_f32_16x16x32_bf16 v[176:179], a[248:251], v[180:183], v[176:179]\n"
      "buffer_store_dwordx4 v[132:135], v20, s[24:27], s46 offen sc1\n"
      "v_mfma_f32_16x16x32_bf16 v[176:179], a[252:255], v[118:121], v[176:179]\n"
      "ds_read_b128 v[96:99], v2 offset:22016\n"
      "v_fmac_f32_e64 v204, v220, v220\n"
      "v_mfma_f32_16x16x32_bf16 v[108:111], v[240:243], v[180:183], v[108:111]\n"
      "ds_read_b128 v[182:185], v2 offset:28160\n"
      "v_fmac_f32 v205, v221, v221\n"
      "v_mfma_f32_16x16x32_bf16 v[108:111], v[244:247], v[118:121], v[108:111]\n"
      "ds_read_b128 v[136:139], v7 offset:30720\n"
      "v_fmac_f32 v204, v222, v222\n"
      "v_mfma_f32_16x16x32_bf16 v[206:209], a[232:235], v[168:171], v[206:209]\n"
      "s_waitcnt lgkmcnt(3)\n"
      "v_cvt_pk_bf16_f32 v152, v152, v152\n"
      "v_lshrrev_b32 v190, 16, v190\n"
      "v_mfma_f32_16x16x32_bf16 v[206:209], a[236:239], v[144:147], v[206:209]\n"
      "ds_write_b16 v12, v152 offset:16\n"
      "v_mfma_f32_16x16x32_bf16 v[176:179], v[224:227], v[168:171], v[176:179]\n"
      "v_mul_u32_u24 v77, 24, v190\n"
      "v_mfma_f32_16x16x32_bf16 v[176:179], v[228:231], v[144:147], v[176:179]\n"
      "v_fmac_f32 v205, v223, v223\n"
      "v_fmac_f32 v205, v141, v141\n"
      "v_mfma_f32_16x16x32_bf16 v[108:111], v[248:251], v[168:171], v[108:111]\n"
      "s_nop 0\n"
      "v_fma_f32 v180, v206, v206, s52\n"
      "v_mul_u32_u24_e64 v193, 24, v81\n"
      "v_mfma_f32_16x16x32_bf16 v[108:111], v[252:255], v[144:147], v[108:111]\n"
      "s_waitcnt lgkmcnt(1)\n"
      "v_mul_u32_u24 v215, 2731, v100\n"
      "v_mul_f32 v181, v207, v207\n"
      "v_mfma_f32_16x16x32_bf16 v[132:135], a[216:219], v[210:213], 0\n"
      "ds_read_b128 v[144:147], v2 offset:31232\n"
      "v_mfma_f32_16x16x32_bf16 v[132:135], a[220:223], v[96:99], v[132:135]\n"
      "buffer_store_dwordx4 v[194:197], v21, s[24:27], s46 offen sc1\n"
      "v_mfma_f32_16x16x32_bf16 v[118:121], a[240:243], v[210:213], 0\n"
      "v_fmac_f32 v181, v209, v209\n"
      "v_fmac_f32 v204, v140, v140\n"
      "v_mfma_f32_16x16x32_bf16 v[118:121], a[244:247], v[96:99], v[118:121]\n"
      "v_fmac_f32 v180, v208, v208\n"
      "v_fmac_f32 v181, v177, v177\n"
      "v_mfma_f32_16x16x32_bf16 v[168:171], v[232:235], v[210:213], 0\n"
      "v_fmac_f32 v180, v176, v176\n"
      "v_fmac_f32 v205, v143, v143\n"
      "v_mfma_f32_16x16x32_bf16 v[168:171], v[236:239], v[96:99], v[168:171]\n"
      "buffer_store_dwordx4 v[148:151], v22, s[24:27], s46 offen sc1\n"
      "v_mfma_f32_16x16x32_bf16 v[132:135], a[224:227], v[104:107], v[132:135]\n"
      "s_nop 0\n"
      "ds_read_b128 v[150:153], v186 offset:10240\n"
      "v_bfe_i32 v80, v37, 30, 1\n"
      "v_mfma_f32_16x16x32_bf16 v[132:135], a[228:231], v[182:185], v[132:135]\n"
      "v_fmac_f32 v180, v178, v178\n"
      "v_fmac_f32 v181, v179, v179\n"
      "v_mfma_f32_16x16x32_bf16 v[118:121], a[248:251], v[104:107], v[118:121]\n"
      "ds_read_b128 v[96:99], v186 offset:6144\n"
      "v_fmac_f32 v204, v142, v142\n"
      "v_mfma_f32_16x16x32_bf16 v[118:121], a[252:255], v[182:185], v[118:121]\n"
      "buffer_store_dwordx4 v[136:139], v23, s[24:27], s46 offen sc1\n"
      "v_mfma_f32_16x16x32_bf16 v[168:171], v[240:243], v[104:107], v[168:171]\n"
      "ds_read_b128 v[104:107], v186 offset:8192\n"
      "v_fmac_f32_e64 v181, v109, v109\n"
      "v_mfma_f32_16x16x32_bf16 v[168:171], v[244:247], v[182:185], v[168:171]\n"
      "s_waitcnt lgkmcnt(2)\n"
      "v_lshrrev_b32 v215, 16, v215\n"
      "v_fmac_f32 v180, v108, v108\n"
      "v_mfma_f32_16x16x32_bf16 v[132:135], a[232:235], v[144:147], v[132:135]\n"
      "v_add_f32 v204, v204, v205\n"
      "v_mfma_f32_16x16x32_bf16 v[132:135], a[236:239], v[154:157], v[132:135]\n"
      "v_fmac_f32 v181, v111, v111\n"
      "v_fmac_f32 v180, v110, v110\n"
      "v_mfma_f32_16x16x32_bf16 v[118:121], v[224:227], v[144:147], v[118:121]\n"
      "v_sub_u32 v77, v82, v77\n"
      "v_add_f32 v180, v180, v181\n"
      "v_mfma_f32_16x16x32_bf16 v[118:121], v[228:231], v[154:157], v[118:121]\n"
      "s_nop 0\n"
      "v_permlane16_swap_b32 v204, v180\n"
      "v_mfma_f32_16x16x32_bf16 v[168:171], v[248:251], v[144:147], v[168:171]\n"
      "v_mul_f32_e64 v183, v133, v133\n"
      "v_fma_f32 v182, v132, v132, s52\n"
      "v_mfma_f32_16x16x32_bf16 v[168:171], v[252:255], v[154:157], v[168:171]\n"
      "v_fmac_f32 v182, v134, v134\n"
      "v_fmac_f32 v183, v135, v135\n"
      "v_mfma_f32_16x16x32_bf16 v[144:147], a[216:219], v[216:219], 0\n"
      "buffer_store_dwordx4 v[164:167], v25, s[24:27], s46 offen sc1\n"
      "v_pk_fma_f32 v[182:183], v[118:119], v[118:119], v[182:183]\n"
      "v_pk_fma_f32 v[182:183], v[120:121], v[120:121], v[182:183]\n"
      "v_mfma_f32_16x16x32_bf16 v[144:147], a[220:223], v[200:203], v[144:147]\n"
      "s_nop 0\n"
      "v_fmac_f32 v183, v169, v169\n"
      "v_readlane_b32 s46, v37, s42\n"
      "v_mfma_f32_16x16x32_bf16 v[164:167], a[240:243], v[216:219], 0\n"
      "v_fmac_f32 v182, v168, v168\n"
      "v_fmac_f32 v182, v170, v170\n"
      "v_mfma_f32_16x16x32_bf16 v[164:167], a[244:247], v[200:203], v[164:167]\n"
      "v_fmac_f32 v183, v171, v171\n"
      "v_add_f32 v204, v204, v180\n"
      "v_mfma_f32_16x16x32_bf16 v[196:199], v[232:235], v[216:219], 0\n"
      "s_waitcnt lgkmcnt(0)\n"
      "v_add_f32 v182, v182, v183\n"
      "v_mov_b32 v180, v182\n"
      "v_mfma_f32_16x16x32_bf16 v[196:199], v[236:239], v[200:203], v[196:199]\n"
      "s_nop 0\n"
      "v_permlane16_swap_b32 v182, v180\n"
      "v_mfma_f32_16x16x32_bf16 v[144:147], a[224:227], v[124:127], v[144:147]\n"
      "s_or_b32 s95, s46, s48\n"
      "v_add_f32 v182, v182, v180\n"
      "v_mfma_f32_16x16x32_bf16 v[144:147], a[228:231], v[96:99], v[144:147]\n"
      "s_nop 0\n"
      "v_permlane32_swap_b32 v204, v182\n"
      "v_add_f32 v204, v204, v182\n"
      "v_mfma_f32_16x16x32_bf16 v[164:167], a[248:251], v[124:127], v[164:167]\n"
      "ds_write_b32 v31, v204\n"
      "v_mfma_f32_16x16x32_bf16 v[164:167], a[252:255], v[96:99], v[164:167]\n"
      "v_sub_u32 v193, v78, v193\n"
      "v_mfma_f32_16x16x32_bf16 v[196:199], v[240:243], v[124:127], v[196:199]\n"
      "v_and_b32 v78, 7, v81\n"
      "v_lshl_add_u32 v81, v81, 2, 12\n"
      "v_mfma_f32_16x16x32_bf16 v[196:199], v[244:247], v[96:99], v[196:199]\n"
      "v_and_b32 v82, 7, v190\n"
      "v_mfma_f32_16x16x32_bf16 v[144:147], a[232:235], v[104:107], v[144:147]\n"
      "s_cmp_ge_u32 s95, 0x40000000\n"
      "s_cbranch_scc1 L_rare_35_%=\n"
      "L_back_36_%=:\n"
      "v_mfma_f32_16x16x32_bf16 v[144:147], a[236:239], v[150:153], v[144:147]\n"
      "v_mul_u32_u24 v194, 24, v215\n"
      "v_sub_u32_e64 v194, v100, v194\n"
      "v_mfma_f32_16x16x32_bf16 v[164:167], v[224:227], v[104:107], v[164:167]\n"
      "s_waitcnt lgkmcnt(0)\n"
      "s_barrier\n"
      "ds_read_b128 v[124:127], v9\n"
      "ds_read_b128 v[200:203], v7\n"
      "v_mfma_f32_16x16x32_bf16 v[164:167], v[228:231], v[150:153], v[164:167]\n"
      "ds_read_b128 v[216:219], v186 offset:256\n"
      "ds_read_b128 v[180:183], v186 offset:2304\n"
      "ds_read_b128 v[210:213], v186 offset:8448\n"
      "v_fma_f32 v84, v144, v144, s52\n"
      "v_mul_f32_e64 v85, v145, v145\n"
      "v_pk_fma_f32 v[84:85], v[146:147], v[146:147], v[84:85]\n"
      "v_mfma_f32_16x16x32_bf16 v[196:199], v[248:251], v[104:107], v[196:199]\n"
      "ds_read_b128 v[154:157], v186 offset:6400\n"
      "ds_read_b128 v[96:99], v7 offset:4096\n"
      "ds_read_b128 v[104:107], v7 offset:8192\n"
      "ds_read_b128 v[136:139], v186 offset:4352\n"
      "v_pk_fma_f32 v[84:85], v[164:165], v[164:165], v[84:85]\n"
      "v_mfma_f32_16x16x32_bf16 v[196:199], v[252:255], v[150:153], v[196:199]\n"
      "s_waitcnt lgkmcnt(8)\n"
      "v_add_f32 v126, v126, v127\n"
      "v_add_f32_e64 v124, v124, v125\n"
      "v_add_f32_e64 v124, v124, v126\n"
      "v_rsq_f32_e64 v124, v124\n"
      "v_pk_fma_f32 v[84:85], v[166:167], v[166:167], v[84:85]\n"
      "v_mul_f32 v124, 0x415db3d7, v124\n"
      "v_mov_b32_e64 v126, v124\n"
      "v_pk_fma_f32 v[84:85], v[196:197], v[196:197], v[84:85]\n"
      "s_waitcnt lgkmcnt(6)\n"
      "v_mfma_f32_16x16x32_bf16 v[148:151], a[216:219], v[216:219], 0\n"
      "s_waitcnt lgkmcnt(5)\n"
      "v_permlane32_swap_b32_e64 v124, v126\n"
      "v_mfma_f32_16x16x32_bf16 v[148:151], a[220:223], v[180:183], v[148:151]\n"
      "ds_read_b128 v[160:163], v186 offset:10496\n"
      "v_mov_b32_e64 v125, v124\n"
      "v_mfma_f32_16x16x32_bf16 v[184:187], a[240:243], v[216:219], 0\n"
      "buffer_store_dwordx4 v[200:203], v20, s[24:27], s46 offen sc1\n"
      "v_pk_mul_f32 v[118:119], v[118:119], v[126:127] op_sel_hi:[1,0]\n"
      "v_pk_fma_f32 v[84:85], v[198:199], v[198:199], v[84:85]\n"
      "v_pk_mul_f32 v[132:133], v[132:133], v[126:127] op_sel_hi:[1,0]\n"
      "v_pk_mul_f32 v[134:135], v[134:135], v[126:127] op_sel_hi:[1,0]\n"
      "v_mfma_f32_16x16x32_bf16 v[184:187], a[244:247], v[180:183], v[184:187]\n"
      "s_waitcnt lgkmcnt(1)\n"
      "v_permlane16_swap_b32 v124, v125\n"
      "v_mfma_f32_16x16x32_bf16 v[202:205], v[232:235], v[216:219], 0\n"
      "ds_read_b128 v[216:219], v7 offset:12288\n"
      "v_cvt_pk_bf16_f32 v132, v132, v133\n"
      "v_mfma_f32_16x16x32_bf16 v[202:205], v[236:239], v[180:183], v[202:205]\n"
      "ds_read_b128 v[180:183], v7 offset:16384\n"
      "v_cvt_pk_bf16_f32 v133, v134, v135\n"
      "v_mfma_f32_16x16x32_bf16 v[148:151], a[224:227], v[136:139], v[148:151]\n"
      "buffer_store_dwordx4 v[96:99], v21, s[24:27], s46 offen sc1\n"
      "v_pk_mul_f32 v[222:223], v[222:223], v[124:125] op_sel_hi:[1,0]\n"
      "v_pk_mul_f32 v[176:177], v[176:177], v[124:125] op_sel:[0,1]\n"
      "v_pk_mul_f32 v[120:121], v[120:121], v[126:127] op_sel_hi:[1,0]\n"
      "v_pk_mul_f32 v[142:143], v[142:143], v[124:125] op_sel_hi:[1,0]\n"
      "v_pk_mul_f32 v[110:111], v[110:111], v[124:125] op_sel:[0,1]\n"
      "v_pk_mul_f32 v[206:207], v[206:207], v[124:125] op_sel:[0,1]\n"
      "v_pk_mul_f32 v[174:175], v[174:175], v[124:125] op_sel_hi:[1,0]\n"
      "v_mfma_f32_16x16x32_bf16 v[148:151], a[228:231], v[154:157], v[148:151]\n"
      "v_cvt_pk_bf16_f32 v134, v118, v119\n"
      "v_cvt_pk_bf16_f32 v135, v120, v121\n"
      "v_mfma_f32_16x16x32_bf16 v[184:187], a[248:251], v[136:139], v[184:187]\n"
      "buffer_store_dwordx4 v[104:107], v22, s[24:27], s46 offen sc1\n"
      "v_pk_mul_f32 v[140:141], v[140:141], v[124:125] op_sel_hi:[1,0]\n"
      "v_pk_mul_f32 v[172:173], v[172:173], v[124:125] op_sel_hi:[1,0]\n"
      "v_pk_mul_f32 v[208:209], v[208:209], v[124:125] op_sel:[0,1]\n"
      "v_pk_mul_f32 v[108:109], v[108:109], v[124:125] op_sel:[0,1]\n"
      "v_pk_mul_f32 v[220:221], v[220:221], v[124:125] op_sel_hi:[1,0]\n"
      "v_pk_mul_f32 v[178:179], v[178:179], v[124:125] op_sel:[0,1]\n"
      "v_mfma_f32_16x16x32_bf16 v[184:187], a[252:255], v[154:157], v[184:187]\n"
      "v_cvt_pk_bf16_f32 v206, v206, v207\n"
      "v_cvt_pk_bf16_f32 v172, v172, v173\n"
      "v_cvt_pk_bf16_f32 v108, v108, v109\n"
      "v_mfma_f32_16x16x32_bf16 v[202:205], v[240:243], v[136:139], v[202:205]\n"
      "v_cvt_pk_bf16_f32 v207, v208, v209\n"
      "v_cvt_pk_bf16_f32 v173, v174, v175\n"
      "v_cvt_pk_bf16_f32 v109, v110, v111\n"
      "v_mfma_f32_16x16x32_bf16 v[202:205], v[244:247], v[154:157], v[202:205]\n"
      "ds_write_b64 v6, v[108:109] offset:24576\n"
      "v_mfma_f32_16x16x32_bf16 v[148:151], a[232:235], v[210:213], v[148:151]\n"
      "s_waitcnt lgkmcnt(1)\n"
      "v_cvt_pk_bf16_f32 v140, v140, v141\n"
      "v_cvt_pk_bf16_f32 v208, v176, v177\n"
      "v_mfma_f32_16x16x32_bf16 v[148:151], a[236:239], v[160:163], v[148:151]\n"
      "v_cvt_pk_bf16_f32 v209, v178, v179\n"
      "v_cvt_pk_bf16_f32 v175, v222, v223\n"
      "v_cvt_pk_bf16_f32 v141, v142, v143\n"
      "v_mfma_f32_16x16x32_bf16 v[184:187], v[224:227], v[210:213], v[184:187]\n"
      "ds_write_b128 v5, v[206:209] offset:24576\n"
      "v_cvt_pk_bf16_f32 v174, v220, v221\n"
      "v_mfma_f32_16x16x32_bf16 v[184:187], v[228:231], v[160:163], v[184:187]\n"
      "ds_write_b64 v6, v[140:141] offset:18432\n"
      "v_mfma_f32_16x16x32_bf16 v[202:205], v[248:251], v[210:213], v[202:205]\n"
      "ds_write_b128 v5, v[132:135] offset:30720\n"
      "v_mfma_f32_16x16x32_bf16 v[202:205], v[252:255], v[160:163], v[202:205]\n"
      "ds_write_b128 v5, v[172:175] offset:18432\n"
      "v_mul_f32 v135, v149, v149\n"
      "v_fma_f32 v134, v148, v148, s52\n"
      "v_pk_mul_f32 v[168:169], v[168:169], v[126:127] op_sel_hi:[1,0]\n"
      "v_add_f32_e64 v84, v84, v85\n"
      "v_pk_fma_f32 v[134:135], v[150:151], v[150:151], v[134:135]\n"
      "v_pk_fma_f32 v[134:135], v[184:185], v[184:185], v[134:135]\n"
      "v_pk_fma_f32 v[134:135], v[186:187], v[186:187], v[134:135]\n"
      "v_pk_fma_f32 v[134:135], v[202:203], v[202:203], v[134:135]\n"
      "v_pk_fma_f32 v[134:135], v[204:205], v[204:205], v[134:135]\n"
      "v_add_f32 v134, v134, v135\n"
      "v_cvt_pk_bf16_f32 v168, v168, v169\n"
      "v_pk_mul_f32 v[170:171], v[170:171], v[126:127] op_sel_hi:[1,0]\n"
      "v_permlane16_swap_b32 v84, v134\n"
      "v_cvt_pk_bf16_f32 v169, v170, v171\n"
      "v_add_f32 v84, v84, v134\n"
      "v_mov_b32 v134, v84\n"
      "s_add_u32 s42, s42, 1\n"
      "ds_write_b64 v6, v[168:169] offset:30720\n"
      "v_permlane32_swap_b32 v84, v134\n"
      "v_add_f32_e64 v84, v84, v134\n"
      "ds_write_b32 v31, v84 offset:4608\n"
      "buffer_store_dwordx4 v[216:219], v23, s[24:27], s46 offen sc1\n"
      "v_and_b32 v80, 196224, v80\n"
      "v_subrev_u32 v210, 73728, v5\n"
      "v_sub_u32_e64 v76, v76, v80\n"
      "ds_bpermute_b32 v111, v81, v76\n"
      "buffer_store_dwordx4 v[180:183], v25, s[24:27], s46 offen sc1\n"
      "s_waitcnt lgkmcnt(1)\n"
      "s_barrier\n"
      "ds_read_b128 v[134:137], v9 offset:4608\n"
      "v_subrev_u32 v211, 73728, v6\n"
      "ds_read_b128 v[180:183], v7 offset:22528\n"
      "ds_read_b128 v[104:107], v7 offset:18432\n"
      "ds_read_b128 v[168:171], v7 offset:30720\n"
      "v_readlane_b32 s46, v37, s42\n"
      "ds_read_b128 v[174:177], v7 offset:26624\n"
      "v_lshl_add_u32 v190, v190, 2, 12\n"
      "v_add_u32 v12, 32, v12\n"
      "s_or_b32 s95, s46, s48\n"
      "s_cmp_ge_u32 s95, 0x40000000\n"
      "s_cbranch_scc1 L_rare_39_%=\n"
      "L_back_40_%=:\n"
      "v_xor_b32 v77, v77, v82\n"
      "s_waitcnt lgkmcnt(4)\n"
      "v_add_f32 v136, v136, v137\n"
      "v_add_f32 v134, v134, v135\n"
      "v_add_f32 v134, v134, v136\n"
      "v_rsq_f32 v134, v134\n"
      "v_and_b32 v100, 7, v215\n"
      "v_mul_f32 v134, 0x415db3d7, v134\n"
      "v_mov_b32 v136, v134\n"
      "v_lshl_add_u32 v215, v215, 2, 12\n"
      "s_nop 0\n"
      "v_permlane32_swap_b32_e64 v134, v136\n"
      "v_mov_b32_e64 v135, v134\n"
      "s_waitcnt lgkmcnt(1)\n"
      "buffer_store_dwordx4 v[104:107], v20, s[24:27], s46 offen sc1\n"
      "v_permlane16_swap_b32_e64 v134, v135\n"
      "v_pk_mul_f32 v[146:147], v[146:147], v[134:135] op_sel_hi:[1,0]\n"
      "v_pk_mul_f32 v[164:165], v[164:165], v[134:135] op_sel_hi:[1,0]\n"
      "v_pk_mul_f32 v[144:145], v[144:145], v[134:135] op_sel_hi:[1,0]\n"
      "v_pk_mul_f32 v[148:149], v[148:149], v[134:135] op_sel:[0,1]\n"
      "v_pk_mul_f32 v[150:151], v[150:151], v[134:135] op_sel:[0,1]\n"
      "v_pk_mul_f32 v[184:185], v[184:185], v[134:135] op_sel:[0,1]\n"
      "v_pk_mul_f32 v[202:203], v[202:203], v[134:135] op_sel:[0,1]\n"
      "v_cvt_pk_bf16_f32 v148, v148, v149\n"
      "v_pk_mul_f32 v[186:187], v[186:187], v[134:135] op_sel:[0,1]\n"
      "v_pk_mul_f32 v[196:197], v[196:197], v[134:135] op_sel_hi:[1,0]\n"
      "v_cvt_pk_bf16_f32 v149, v150, v151\n"
      "v_cvt_pk_bf16_f32 v202, v202, v203\n"
      "v_cvt_pk_bf16_f32 v150, v184, v185\n"
      "v_cvt_pk_bf16_f32 v144, v144, v145\n"
      "v_pk_mul_f32 v[204:205], v[204:205], v[134:135] op_sel:[0,1]\n"
      "v_pk_mul_f32 v[166:167], v[166:167], v[134:135] op_sel_hi:[1,0]\n"
      "v_cvt_pk_bf16_f32 v151, v186, v187\n"
      "s_add_u32 s42, s42, 1\n"
      "ds_write_b128 v210, v[148:151] offset:6144\n"
      "v_cvt_pk_bf16_f32 v145, v146, v147\n"
      "v_cvt_pk_bf16_f32 v147, v166, v167\n"
      "v_cvt_pk_bf16_f32 v196, v196, v197\n"
      "v_cvt_pk_bf16_f32 v146, v164, v165\n"
      "v_cvt_pk_bf16_f32 v203, v204, v205\n"
      "ds_write_b128 v210, v[144:147]\n"
      "ds_write_b64 v211, v[202:203] offset:6144\n"
      "v_pk_mul_f32 v[198:199], v[198:199], v[134:135] op_sel_hi:[1,0]\n"
      "v_cvt_pk_bf16_f32 v197, v198, v199\n"
      "ds_write_b64 v211, v[196:197]\n"
      "buffer_store_dwordx4 v[180:183], v21, s[24:27], s46 offen sc1\n"
      "ds_bpermute_b32 v204, v215, v76\n"
      "ds_read_b128 v[144:147], v7 offset:34816\n"
      "ds_bpermute_b32 v182, v190, v76\n"
      "s_waitcnt lgkmcnt(5)\n"
      "buffer_store_dwordx4 v[174:177], v22, s[24:27], s46 offen sc1\n"
      "v_xor_b32_e64 v193, v193, v78\n"
      "v_xor_b32 v194, v194, v100\n"
      "v_lshl_add_u32 v111, v193, 4, v111\n"
      "s_waitcnt lgkmcnt(3)\n"
      "s_barrier\n"
      "ds_read_b128 v[164:167], v86\n"
      "buffer_store_dwordx4 v[168:171], v23, s[24:27], s46 offen sc1\n"
      "ds_read_b128 v[208:211], v86 offset:1024\n"
      "s_waitcnt lgkmcnt(2)\n"
      "buffer_store_dwordx4 v[144:147], v25, s[24:27], s46 offen sc1\n"
      "ds_read_b128 v[136:139], v86 offset:2048\n"
      "v_lshl_add_u32 v204, v194, 4, v204\n"
      "v_lshl_add_u32 v182, v77, 4, v182\n"
      "s_waitcnt lgkmcnt(2)\n"
      "buffer_store_dwordx4 v[164:167], v182, s[24:27], 0 offen sc1\n"
      "s_waitcnt lgkmcnt(1)\n"
      "buffer_store_dwordx4 v[208:211], v111, s[24:27], 0 offen sc1\n"
      "s_waitcnt lgkmcnt(0)\n"
      "buffer_store_dwordx4 v[136:139], v204, s[24:27], 0 offen sc1\n"
      "s_endpgm\n"
      "L_rare_1_%=:\n"
      "s_nop 7\n"
      "s_waitcnt lgkmcnt(0)\n"
      "s_add_u32 s72, s42, 1\n"
      "v_readlane_b32 s77, v38, s72\n"
      "v_subrev_u32 v165, 115456, v9\n"
      "s_and_b32 s73, s77, -16\n"
      "s_mov_b32 m0, 110592\n"
      "s_nop 0\n"
      "buffer_load_dwordx4 v165, s[28:31], s73 offen  lds\n"
      "s_add_u32 s73, s73, 1024\n"
      "s_mov_b32 m0, 111616\n"
      "s_nop 0\n"
      "buffer_load_dwordx4 v165, s[28:31], s73 offen  lds\n"
      "s_add_u32 s73, s73, 1024\n"
      "s_mov_b32 m0, 112640\n"
      "s_nop 0\n"
      "buffer_load_dwordx4 v165, s[28:31], s73 offen  lds\n"
      "s_add_u32 s73, s73, 1024\n"
      "s_mov_b32 m0, 113664\n"
      "s_nop 0\n"
      "buffer_load_dwordx4 v165, s[28:31], s73 offen  lds\n"
      "s_add_u32 s73, s73, 1024\n"
      "s_mov_b32 m0, 114688\n"
      "s_mov_b32 exec_hi, 0xffff\n"
      "buffer_load_dwordx4 v165, s[28:31], s73 offen  lds\n"
      "s_mov_b32 exec_hi, -1\n"
      "s_add_u32 s73, s73, 1024\n"
      "s_and_b32 s77, s77, 15\n"
      "s_waitcnt lgkmcnt(0)\n"
      "s_nop 4\n"
      "s_branch L_back_2_%=\n"
      "L_rare_3_%=:\n"
      "s_nop 7\n"
      "s_waitcnt lgkmcnt(0)\n"
      "s_waitcnt vmcnt(0)\n"
      "v_subrev_u32 v105, 115456, v9\n"
      "v_bfe_u32 v163, v105, 4, 4\n"
      "v_mul_u32_u24 v163, 98, v163\n"
      "v_lshrrev_b32 v105, 8, v105\n"
      "v_lshl_add_u32 v163, v105, 3, v163\n"
      "v_add_u32 v163, s77, v163\n"
      "v_and_b32 v222, 2, v163\n"
      "v_and_b32 v163, -4, v163\n"
      "v_add_u32 v163, 110592, v163\n"
      "ds_read2_b32 v[198:199], v163 offset0:0 offset1:1\n"
      "ds_read2_b32 v[200:201], v163 offset0:8 offset1:9\n"
      "ds_read2_b32 v[122:123], v163 offset0:16 offset1:17\n"
      "ds_read2_b32 v[164:165], v163 offset0:2 offset1:10\n"
      "ds_read2_b32 v[146:147], v163 offset0:18 offset1:24\n"
      "s_waitcnt lgkmcnt(4)\n"
      "v_alignbyte_b32 v198, v199, v198, v222\n"
      "s_waitcnt lgkmcnt(1)\n"
      "v_alignbyte_b32 v199, v164, v199, v222\n"
      "v_lshlrev_b32 v105, 16, v198\n"
      "v_fmac_f32 v158, s53, v105\n"
      "v_and_b32 v105, 0xffff0000, v198\n"
      "v_fmac_f32 v159, s53, v105\n"
      "v_lshlrev_b32 v105, 16, v199\n"
      "v_fmac_f32_e64 v160, s53, v105\n"
      "v_and_b32 v105, 0xffff0000, v199\n"
      "v_fmac_f32 v161, s53, v105\n"
      "v_alignbyte_b32 v200, v201, v200, v222\n"
      "v_alignbyte_b32 v201, v165, v201, v222\n"
      "v_lshlrev_b32 v105, 16, v200\n"
      "v_fmac_f32 v126, s53, v105\n"
      "v_and_b32 v105, 0xffff0000, v200\n"
      "v_fmac_f32 v127, s53, v105\n"
      "v_lshlrev_b32 v105, 16, v201\n"
      "v_fmac_f32 v128, s53, v105\n"
      "v_and_b32 v105, 0xffff0000, v201\n"
      "v_fmac_f32 v129, s53, v105\n"
      "v_alignbyte_b32 v122, v123, v122, v222\n"
      "s_waitcnt lgkmcnt(0)\n"
      "v_alignbyte_b32 v123, v146, v123, v222\n"
      "v_lshlrev_b32 v105, 16, v122\n"
      "v_fmac_f32 v176, s53, v105\n"
      "v_and_b32 v105, 0xffff0000, v122\n"
      "v_fmac_f32 v177, s53, v105\n"
      "v_lshlrev_b32 v105, 16, v123\n"
      "v_fmac_f32 v178, s53, v105\n"
      "v_and_b32 v105, 0xffff0000, v123\n"
      "v_fmac_f32 v179, s53, v105\n"
      "v_alignbyte_b32 v105, v147, v147, v222\n"
      "v_lshlrev_b32 v105, 16, v105\n"
      "v_fmac_f32 v172, s53, v105\n"
      "v_max3_f32 v163, v158, v159, v160\n"
      "v_max3_f32 v222, v161, v126, v127\n"
      "v_max3_f32 v163, v163, v128, v129\n"
      "v_max3_f32 v222, v222, v176, v177\n"
      "v_max3_f32 v163, v163, v178, v179\n"
      "v_max3_f32 v163, v163, v222, v172\n"
      "v_mov_b32 v105, v163\n"
      "s_nop 1\n"
      "v_permlane16_swap_b32 v163, v105\n"
      "v_max_f32 v163, v163, v105\n"
      "v_mov_b32 v105, v163\n"
      "s_nop 1\n"
      "v_permlane32_swap_b32 v163, v105\n"
      "v_max_f32 v163, v163, v105\n"
      "v_sub_f32 v158, v158, v163\n"
      "v_sub_f32 v159, v159, v163\n"
      "v_sub_f32 v160, v160, v163\n"
      "v_sub_f32 v161, v161, v163\n"
      "v_sub_f32 v126, v126, v163\n"
      "v_sub_f32 v127, v127, v163\n"
      "v_sub_f32 v128, v128, v163\n"
      "v_sub_f32 v129, v129, v163\n"
      "v_sub_f32 v176, v176, v163\n"
      "v_sub_f32 v177, v177, v163\n"
      "v_sub_f32 v178, v178, v163\n"
      "v_sub_f32 v179, v179, v163\n"
      "v_sub_f32_e64 v172, v172, v163\n"
      "s_waitcnt lgkmcnt(0)\n"
      "s_nop 4\n"
      "s_branch L_back_4_%=\n"
      "L_rare_5_%=:\n"
      "s_nop 7\n"
      "s_waitcnt lgkmcnt(0)\n"
      "s_waitcnt vmcnt(0)\n"
      "v_subrev_u32 v148, 115456, v9\n"
      "v_bfe_u32 v165, v148, 4, 4\n"
      "v_mul_u32_u24 v165, 98, v165\n"
      "v_lshrrev_b32 v148, 8, v148\n"
      "v_lshl_add_u32 v165, v148, 3, v165\n"
      "v_add_u32 v165, s77, v165\n"
      "v_and_b32 v163, 2, v165\n"
      "v_and_b32 v165, -4, v165\n"
      "v_add_u32 v165, 112160, v165\n"
      "ds_read2_b32 v[180:181], v165 offset0:0 offset1:1\n"
      "ds_read2_b32 v[182:183], v165 offset0:8 offset1:9\n"
      "ds_read2_b32 v[146:147], v165 offset0:16 offset1:17\n"
      "ds_read2_b32 v[144:145], v165 offset0:2 offset1:10\n"
      "ds_read2_b32 v[174:175], v165 offset0:18 offset1:24\n"
      "s_waitcnt lgkmcnt(4)\n"
      "v_alignbyte_b32 v180, v181, v180, v163\n"
      "s_waitcnt lgkmcnt(1)\n"
      "v_alignbyte_b32 v181, v144, v181, v163\n"
      "v_lshlrev_b32 v148, 16, v180\n"
      "v_fmac_f32 v140, s53, v148\n"
      "v_and_b32 v148, 0xffff0000, v180\n"
      "v_fmac_f32 v141, s53, v148\n"
      "v_lshlrev_b32 v148, 16, v181\n"
      "v_fmac_f32 v142, s53, v148\n"
      "v_and_b32 v148, 0xffff0000, v181\n"
      "v_fmac_f32 v143, s53, v148\n"
      "v_alignbyte_b32 v182, v183, v182, v163\n"
      "v_alignbyte_b32 v183, v145, v183, v163\n"
      "v_lshlrev_b32 v148, 16, v182\n"
      "v_fmac_f32 v184, s53, v148\n"
      "v_and_b32 v148, 0xffff0000, v182\n"
      "v_fmac_f32 v185, s53, v148\n"
      "v_lshlrev_b32 v148, 16, v183\n"
      "v_fmac_f32_e64 v186, s53, v148\n"
      "v_and_b32 v148, 0xffff0000, v183\n"
      "v_fmac_f32 v187, s53, v148\n"
      "v_alignbyte_b32 v146, v147, v146, v163\n"
      "s_waitcnt lgkmcnt(0)\n"
      "v_alignbyte_b32 v147, v174, v147, v163\n"
      "v_lshlrev_b32 v148, 16, v146\n"
      "v_fmac_f32 v198, s53, v148\n"
      "v_and_b32 v148, 0xffff0000, v146\n"
      "v_fmac_f32 v199, s53, v148\n"
      "v_lshlrev_b32 v148, 16, v147\n"
      "v_fmac_f32_e64 v200, s53, v148\n"
      "v_and_b32 v148, 0xffff0000, v147\n"
      "v_fmac_f32 v201, s53, v148\n"
      "v_alignbyte_b32 v148, v175, v175, v163\n"
      "v_lshlrev_b32 v148, 16, v148\n"
      "v_fmac_f32_e64 v208, s53, v148\n"
      "v_max3_f32 v165, v140, v141, v142\n"
      "v_max3_f32 v163, v143, v184, v185\n"
      "v_max3_f32 v165, v165, v186, v187\n"
      "v_max3_f32 v163, v163, v198, v199\n"
      "v_max3_f32 v165, v165, v200, v201\n"
      "v_max3_f32 v165, v165, v163, v208\n"
      "v_mov_b32 v148, v165\n"
      "s_nop 1\n"
      "v_permlane16_swap_b32 v165, v148\n"
      "v_max_f32 v165, v165, v148\n"
      "v_mov_b32 v148, v165\n"
      "s_nop 1\n"
      "v_permlane32_swap_b32 v165, v148\n"
      "v_max_f32 v165, v165, v148\n"
      "v_sub_f32 v140, v140, v165\n"
      "v_sub_f32 v141, v141, v165\n"
      "v_sub_f32 v142, v142, v165\n"
      "v_sub_f32 v143, v143, v165\n"
      "v_sub_f32 v184, v184, v165\n"
      "v_sub_f32 v185, v185, v165\n"
      "v_sub_f32 v186, v186, v165\n"
      "v_sub_f32 v187, v187, v165\n"
      "v_sub_f32 v198, v198, v165\n"
      "v_sub_f32 v199, v199, v165\n"
      "v_sub_f32 v200, v200, v165\n"
      "v_sub_f32 v201, v201, v165\n"
      "v_sub_f32_e64 v208, v208, v165\n"
      "s_waitcnt lgkmcnt(0)\n"
      "s_nop 4\n"
      "s_branch L_back_6_%=\n"
      "L_rare_7_%=:\n"
      "s_nop 7\n"
      "s_waitcnt lgkmcnt(0)\n"
      "s_waitcnt vmcnt(0)\n"
      "v_subrev_u32 v120, 115456, v9\n"
      "v_bfe_u32 v103, v120, 4, 4\n"
      "v_mul_u32_u24 v103, 98, v103\n"
      "v_lshrrev_b32_e64 v120, 8, v120\n"
      "v_lshl_add_u32 v103, v120, 3, v103\n"
      "v_add_u32 v103, s77, v103\n"
      "v_and_b32 v102, 2, v103\n"
      "v_and_b32_e64 v103, -4, v103\n"
      "v_add_u32 v103, 113728, v103\n"
      "ds_read2_b32 v[170:171], v103 offset0:0 offset1:1\n"
      "ds_read2_b32 v[134:135], v103 offset0:8 offset1:9\n"
      "ds_read2_b32 v[136:137], v103 offset0:16 offset1:17\n"
      "ds_read2_b32 v[100:101], v103 offset0:2 offset1:10\n"
      "ds_read2_b32 v[172:173], v103 offset0:18 offset1:24\n"
      "s_waitcnt lgkmcnt(4)\n"
      "v_alignbyte_b32 v170, v171, v170, v102\n"
      "s_waitcnt lgkmcnt(1)\n"
      "v_alignbyte_b32 v171, v100, v171, v102\n"
      "v_lshlrev_b32 v120, 16, v170\n"
      "v_fmac_f32 v144, s53, v120\n"
      "v_and_b32 v120, 0xffff0000, v170\n"
      "v_fmac_f32 v145, s53, v120\n"
      "v_lshlrev_b32 v120, 16, v171\n"
      "v_fmac_f32_e64 v146, s53, v120\n"
      "v_and_b32 v120, 0xffff0000, v171\n"
      "v_fmac_f32 v147, s53, v120\n"
      "v_alignbyte_b32 v134, v135, v134, v102\n"
      "v_alignbyte_b32 v135, v101, v135, v102\n"
      "v_lshlrev_b32 v120, 16, v134\n"
      "v_fmac_f32 v130, s53, v120\n"
      "v_and_b32 v120, 0xffff0000, v134\n"
      "v_fmac_f32 v131, s53, v120\n"
      "v_lshlrev_b32 v120, 16, v135\n"
      "v_fmac_f32 v132, s53, v120\n"
      "v_and_b32 v120, 0xffff0000, v135\n"
      "v_fmac_f32 v133, s53, v120\n"
      "v_alignbyte_b32 v136, v137, v136, v102\n"
      "s_waitcnt lgkmcnt(0)\n"
      "v_alignbyte_b32 v137, v172, v137, v102\n"
      "v_lshlrev_b32 v120, 16, v136\n"
      "v_fmac_f32 v126, s53, v120\n"
      "v_and_b32 v120, 0xffff0000, v136\n"
      "v_fmac_f32 v127, s53, v120\n"
      "v_lshlrev_b32 v120, 16, v137\n"
      "v_fmac_f32 v128, s53, v120\n"
      "v_and_b32 v120, 0xffff0000, v137\n"
      "v_fmac_f32 v129, s53, v120\n"
      "v_alignbyte_b32 v120, v173, v173, v102\n"
      "v_lshlrev_b32 v120, 16, v120\n"
      "v_fmac_f32 v180, s53, v120\n"
      "v_max3_f32 v103, v144, v145, v146\n"
      "v_max3_f32 v102, v147, v130, v131\n"
      "v_max3_f32 v103, v103, v132, v133\n"
      "v_max3_f32 v102, v102, v126, v127\n"
      "v_max3_f32 v103, v103, v128, v129\n"
      "v_max3_f32 v103, v103, v102, v180\n"
      "v_mov_b32 v120, v103\n"
      "s_nop 1\n"
      "v_permlane16_swap_b32 v103, v120\n"
      "v_max_f32 v103, v103, v120\n"
      "v_mov_b32 v120, v103\n"
      "s_nop 1\n"
      "v_permlane32_swap_b32 v103, v120\n"
      "v_max_f32 v103, v103, v120\n"
      "v_sub_f32 v144, v144, v103\n"
      "v_sub_f32 v145, v145, v103\n"
      "v_sub_f32 v146, v146, v103\n"
      "v_sub_f32 v147, v147, v103\n"
      "v_sub_f32 v130, v130, v103\n"
      "v_sub_f32 v131, v131, v103\n"
      "v_sub_f32 v132, v132, v103\n"
      "v_sub_f32 v133, v133, v103\n"
      "v_sub_f32 v126, v126, v103\n"
      "v_sub_f32 v127, v127, v103\n"
      "v_sub_f32 v128, v128, v103\n"
      "v_sub_f32 v129, v129, v103\n"
      "v_sub_f32 v180, v180, v103\n"
      "s_waitcnt lgkmcnt(0)\n"
      "s_nop 4\n"
      "s_branch L_back_8_%=\n"
      "L_rare_9_%=:\n"
      "s_nop 7\n"
      "s_waitcnt lgkmcnt(0)\n"
      "s_waitcnt vmcnt(0)\n"
      "v_subrev_u32 v129, 115456, v9\n"
      "v_lshrrev_b32 v129, 3, v129\n"
      "v_min_u32 v129, 96, v129\n"
      "v_add_u32 v129, s77, v129\n"
      "v_add_u32 v129, 115296, v129\n"
      "ds_read_u16 v129, v129\n"
      "s_waitcnt lgkmcnt(0)\n"
      "v_lshlrev_b32_e64 v129, 16, v129\n"
      "v_fmac_f32 v148, s53, v129\n"
      "s_nop 1\n"
      "v_max_f32_dpp v128, v148, v148 row_ror:8 row_mask:0xf bank_mask:0xf\n"
      "s_nop 1\n"
      "v_max_f32_dpp v128, v128, v128 row_ror:4 row_mask:0xf bank_mask:0xf\n"
      "s_nop 1\n"
      "v_max_f32_dpp v128, v128, v128 row_ror:2 row_mask:0xf bank_mask:0xf\n"
      "s_nop 1\n"
      "v_max_f32_dpp v128, v128, v128 row_ror:1 row_mask:0xf bank_mask:0xf\n"
      "s_nop 1\n"
      "v_max_f32_dpp v128, v128, v128 row_bcast:15 row_mask:0xa bank_mask:0xf\n"
      "s_nop 1\n"
      "v_max_f32_dpp v128, v128, v128 row_bcast:31 row_mask:0xc bank_mask:0xf\n"
      "s_nop 0\n"
      "v_readlane_b32 s72, v128, 63\n"
      "s_nop 1\n"
      "v_subrev_f32 v148, s72, v148\n"
      "s_waitcnt lgkmcnt(0)\n"
      "s_nop 4\n"
      "s_branch L_back_10_%=\n"
      "L_rare_27_%=:\n"
      "s_nop 7\n"
      "s_waitcnt lgkmcnt(0)\n"
      "s_bitcmp1_b32 s85, 31\n"
      "s_cselect_b32 s75, 100270464, 0\n"
      "s_bitcmp1_b32 s85, 30\n"
      "s_cselect_b32 s76, 196224, 0\n"
      "s_and_b32 s74, s85, 0x3fffffff\n"
      "s_cmp_ge_u32 s85, 0x40000000\n"
      "s_cselect_b32 s74, s74, 0\n"
      "s_cselect_b32 s85, 0, s85\n"
      "s_sub_u32 s74, s74, s69\n"
      "s_add_u32 s69, s69, s74\n"
      "s_sub_u32 s75, s75, s70\n"
      "s_add_u32 s70, s70, s75\n"
      "s_sub_u32 s76, s76, s71\n"
      "s_add_u32 s71, s71, s76\n"
      "v_add_u32 v20, s74, v20\n"
      "v_add_u32 v21, s74, v21\n"
      "v_add_u32 v22, s74, v22\n"
      "v_add_u32 v23, s74, v23\n"
      "v_add_u32 v25, s74, v25\n"
      "s_cmp_eq_u32 s75, 0\n"
      "s_cbranch_scc1 L_ax_25_%=\n"
      "v_bfe_i32 v104, v27, 10, 1\n"
      "v_and_b32 v104, s75, v104\n"
      "v_sub_u32 v20, v20, v104\n"
      "v_bfe_i32 v104, v27, 12, 1\n"
      "v_and_b32 v104, s75, v104\n"
      "v_sub_u32 v21, v21, v104\n"
      "v_bfe_i32 v104, v27, 14, 1\n"
      "v_and_b32 v104, s75, v104\n"
      "v_sub_u32 v22, v22, v104\n"
      "v_bfe_i32 v104, v27, 16, 1\n"
      "v_and_b32 v104, s75, v104\n"
      "v_sub_u32 v23, v23, v104\n"
      "v_bfe_i32 v104, v27, 18, 1\n"
      "v_and_b32 v104, s75, v104\n"
      "v_sub_u32 v25, v25, v104\n"
      "L_ax_25_%=:\n"
      "s_cmp_eq_u32 s76, 0\n"
      "s_cbranch_scc1 L_ax_26_%=\n"
      "v_bfe_i32 v104, v27, 11, 1\n"
      "v_and_b32 v104, s76, v104\n"
      "v_sub_u32_e64 v20, v20, v104\n"
      "v_bfe_i32 v104, v27, 13, 1\n"
      "v_and_b32 v104, s76, v104\n"
      "v_sub_u32 v21, v21, v104\n"
      "v_bfe_i32 v104, v27, 15, 1\n"
      "v_and_b32 v104, s76, v104\n"
      "v_sub_u32 v22, v22, v104\n"
      "v_bfe_i32 v104, v27, 17, 1\n"
      "v_and_b32 v104, s76, v104\n"
      "v_sub_u32 v23, v23, v104\n"
      "v_bfe_i32 v104, v27, 19, 1\n"
      "v_and_b32 v104, s76, v104\n"
      "v_sub_u32 v25, v25, v104\n"
      "L_ax_26_%=:\n"
      "s_or_b32 s48, s66, s69\n"
      "s_cmp_lg_u32 s48, 0\n"
      "s_cselect_b32 s48, 0x40000000, 0\n"
      "s_waitcnt lgkmcnt(0)\n"
      "s_nop 4\n"
      "s_branch L_back_28_%=\n"
      "L_rare_31_%=:\n"
      "s_nop 7\n"
      "s_waitcnt lgkmcnt(0)\n"
      "s_bitcmp1_b32 s85, 31\n"
      "s_cselect_b32 s75, 100270464, 0\n"
      "s_bitcmp1_b32 s85, 30\n"
      "s_cselect_b32 s76, 196224, 0\n"
      "s_and_b32 s74, s85, 0x3fffffff\n"
      "s_cmp_ge_u32 s85, 0x40000000\n"
      "s_cselect_b32 s74, s74, 0\n"
      "s_cselect_b32 s85, 0, s85\n"
      "s_sub_u32 s74, s74, s69\n"
      "s_add_u32 s69, s69, s74\n"
      "s_sub_u32 s75, s75, s70\n"
      "s_add_u32 s70, s70, s75\n"
      "s_sub_u32 s76, s76, s71\n"
      "s_add_u32 s71, s71, s76\n"
      "v_add_u32 v20, s74, v20\n"
      "v_add_u32 v21, s74, v21\n"
      "v_add_u32 v22, s74, v22\n"
      "v_add_u32 v23, s74, v23\n"
      "v_add_u32 v25, s74, v25\n"
      "s_cmp_eq_u32 s75, 0\n"
      "s_cbranch_scc1 L_ax_29_%=\n"
      "v_bfe_i32 v134, v27, 10, 1\n"
      "v_and_b32 v134, s75, v134\n"
      "v_sub_u32 v20, v20, v134\n"
      "v_bfe_i32 v134, v27, 12, 1\n"
      "v_and_b32 v134, s75, v134\n"
      "v_sub_u32 v21, v21, v134\n"
      "v_bfe_i32 v134, v27, 14, 1\n"
      "v_and_b32 v134, s75, v134\n"
      "v_sub_u32 v22, v22, v134\n"
      "v_bfe_i32 v134, v27, 16, 1\n"
      "v_and_b32 v134, s75, v134\n"
      "v_sub_u32 v23, v23, v134\n"
      "v_bfe_i32 v134, v27, 18, 1\n"
      "v_and_b32 v134, s75, v134\n"
      "v_sub_u32 v25, v25, v134\n"
      "L_ax_29_%=:\n"
      "s_cmp_eq_u32 s76, 0\n"
      "s_cbranch_scc1 L_ax_30_%=\n"
      "v_bfe_i32 v134, v27, 11, 1\n"
      "v_and_b32 v134, s76, v134\n"
      "v_sub_u32 v20, v20, v134\n"
      "v_bfe_i32 v134, v27, 13, 1\n"
      "v_and_b32 v134, s76, v134\n"
      "v_sub_u32 v21, v21, v134\n"
      "v_bfe_i32 v134, v27, 15, 1\n"
      "v_and_b32 v134, s76, v134\n"
      "v_sub_u32 v22, v22, v134\n"
      "v_bfe_i32 v134, v27, 17, 1\n"
      "v_and_b32 v134, s76, v134\n"
      "v_sub_u32 v23, v23, v134\n"
      "v_bfe_i32 v134, v27, 19, 1\n"
      "v_and_b32 v134, s76, v134\n"
      "v_sub_u32 v25, v25, v134\n"
      "L_ax_30_%=:\n"
      "s_or_b32 s48, s66, s69\n"
      "s_cmp_lg_u32 s48, 0\n"
      "s_cselect_b32 s48, 0x40000000, 0\n"
      "s_waitcnt lgkmcnt(0)\n"
      "s_nop 4\n"
      "s_branch L_back_32_%=\n"
      "L_rare_35_%=:\n"
      "s_nop 7\n"
      "s_waitcnt lgkmcnt(0)\n"
      "s_bitcmp1_b32 s46, 31\n"
      "s_cselect_b32 s75, 100270464, 0\n"
      "s_bitcmp1_b32 s46, 30\n"
      "s_cselect_b32 s76, 196224, 0\n"
      "s_and_b32 s74, s46, 0x3fffffff\n"
      "s_cmp_ge_u32 s46, 0x40000000\n"
      "s_cselect_b32 s74, s74, 0\n"
      "s_cselect_b32 s46, 0, s46\n"
      "s_sub_u32 s74, s74, s69\n"
      "s_add_u32 s69, s69, s74\n"
      "s_sub_u32 s75, s75, s70\n"
      "s_add_u32 s70, s70, s75\n"
      "s_sub_u32 s76, s76, s71\n"
      "s_add_u32 s71, s71, s76\n"
      "v_add_u32 v20, s74, v20\n"
      "v_add_u32 v21, s74, v21\n"
      "v_add_u32 v22, s74, v22\n"
      "v_add_u32 v23, s74, v23\n"
      "v_add_u32 v25, s74, v25\n"
      "s_cmp_eq_u32 s75, 0\n"
      "s_cbranch_scc1 L_ax_33_%=\n"
      "v_bfe_i32 v126, v27, 10, 1\n"
      "v_and_b32 v126, s75, v126\n"
      "v_sub_u32 v20, v20, v126\n"
      "v_bfe_i32 v126, v27, 12, 1\n"
      "v_and_b32 v126, s75, v126\n"
      "v_sub_u32 v21, v21, v126\n"
      "v_bfe_i32 v126, v27, 14, 1\n"
      "v_and_b32 v126, s75, v126\n"
      "v_sub_u32 v22, v22, v126\n"
      "v_bfe_i32 v126, v27, 16, 1\n"
      "v_and_b32 v126, s75, v126\n"
      "v_sub_u32 v23, v23, v126\n"
      "v_bfe_i32 v126, v27, 18, 1\n"
      "v_and_b32 v126, s75, v126\n"
      "v_sub_u32 v25, v25, v126\n"
      "L_ax_33_%=:\n"
      "s_cmp_eq_u32 s76, 0\n"
      "s_cbranch_scc1 L_ax_34_%=\n"
      "v_bfe_i32 v126, v27, 11, 1\n"
      "v_and_b32 v126, s76, v126\n"
      "v_sub_u32 v20, v20, v126\n"
      "v_bfe_i32 v126, v27, 13, 1\n"
      "v_and_b32 v126, s76, v126\n"
      "v_sub_u32 v21, v21, v126\n"
      "v_bfe_i32 v126, v27, 15, 1\n"
      "v_and_b32 v126, s76, v126\n"
      "v_sub_u32 v22, v22, v126\n"
      "v_bfe_i32 v126, v27, 17, 1\n"
      "v_and_b32 v126, s76, v126\n"
      "v_sub_u32 v23, v23, v126\n"
      "v_bfe_i32 v126, v27, 19, 1\n"
      "v_and_b32 v126, s76, v126\n"
      "v_sub_u32 v25, v25, v126\n"
      "L_ax_34_%=:\n"
      "s_or_b32 s48, s66, s69\n"
      "s_cmp_lg_u32 s48, 0\n"
      "s_cselect_b32 s48, 0x40000000, 0\n"
      "s_waitcnt lgkmcnt(0)\n"
      "s_nop 4\n"
      "s_branch L_back_36_%=\n"
      "L_rare_39_%=:\n"
      "s_nop 7\n"
      "s_waitcnt lgkmcnt(0)\n"
      "s_bitcmp1_b32 s46, 31\n"
      "s_cselect_b32 s75, 100270464, 0\n"
      "s_bitcmp1_b32 s46, 30\n"
      "s_cselect_b32 s76, 196224, 0\n"
      "s_and_b32 s74, s46, 0x3fffffff\n"
      "s_cmp_ge_u32 s46, 0x40000000\n"
      "s_cselect_b32 s74, s74, 0\n"
      "s_cselect_b32 s46, 0, s46\n"
      "s_sub_u32 s74, s74, s69\n"
      "s_add_u32 s69, s69, s74\n"
      "s_sub_u32 s75, s75, s70\n"
      "s_add_u32 s70, s70, s75\n"
      "s_sub_u32 s76, s76, s71\n"
      "s_add_u32 s71, s71, s76\n"
      "v_add_u32 v20, s74, v20\n"
      "v_add_u32 v21, s74, v21\n"
      "v_add_u32 v22, s74, v22\n"
      "v_add_u32 v23, s74, v23\n"
      "v_add_u32 v25, s74, v25\n"
      "s_cmp_eq_u32 s75, 0\n"
      "s_cbranch_scc1 L_ax_37_%=\n"
      "v_bfe_i32 v209, v27, 10, 1\n"
      "v_and_b32 v209, s75, v209\n"
      "v_sub_u32 v20, v20, v209\n"
      "v_bfe_i32 v209, v27, 12, 1\n"
      "v_and_b32 v209, s75, v209\n"
      "v_sub_u32 v21, v21, v209\n"
      "v_bfe_i32 v209, v27, 14, 1\n"
      "v_and_b32 v209, s75, v209\n"
      "v_sub_u32 v22, v22, v209\n"
      "v_bfe_i32 v209, v27, 16, 1\n"
      "v_and_b32 v209, s75, v209\n"
      "v_sub_u32 v23, v23, v209\n"
      "v_bfe_i32 v209, v27, 18, 1\n"
      "v_and_b32 v209, s75, v209\n"
      "v_sub_u32 v25, v25, v209\n"
      "L_ax_37_%=:\n"
      "s_cmp_eq_u32 s76, 0\n"
      "s_cbranch_scc1 L_ax_38_%=\n"
      "v_bfe_i32 v209, v27, 11, 1\n"
      "v_and_b32 v209, s76, v209\n"
      "v_sub_u32 v20, v20, v209\n"
      "v_bfe_i32 v209, v27, 13, 1\n"
      "v_and_b32 v209, s76, v209\n"
      "v_sub_u32 v21, v21, v209\n"
      "v_bfe_i32 v209, v27, 15, 1\n"
      "v_and_b32 v209, s76, v209\n"
      "v_sub_u32 v22, v22, v209\n"
      "v_bfe_i32 v209, v27, 17, 1\n"
      "v_and_b32 v209, s76, v209\n"
      "v_sub_u32 v23, v23, v209\n"
      "v_bfe_i32 v209, v27, 19, 1\n"
      "v_and_b32 v209, s76, v209\n"
      "v_sub_u32 v25, v25, v209\n"
      "L_ax_38_%=:\n"
      "s_or_b32 s48, s66, s69\n"
      "s_cmp_lg_u32 s48, 0\n"
      "s_cselect_b32 s48, 0x40000000, 0\n"
      "s_waitcnt lgkmcnt(0)\n"
      "s_nop 4\n"
      "s_branch L_back_40_%=\n"
      "L_cend_%=:\n"
      :
      : "s"(__builtin_amdgcn_kernarg_segment_ptr()), "s"(wave), "s"(blockIdx.x), "s"(CT_TAB), "s"(lds0)
      : "v0","v1","v2","v3","v4","v5","v6","v7","v8","v9","v10","v11","v12","v13","v14","v15","v16","v17","v18","v19","v20","v21","v22","v23","v24","v25","v26","v27","v28","v29","v30","v31","v32","v33","v34","v35","v36","v37","v38","v39","v40","v41","v42","v43","v44","v45","v46","v47","v48","v49","v50","v51","v52","v53","v54","v55","v56","v57","v58","v59","v60","v61","v62","v63","v64","v65","v66","v67","v68","v69","v70","v71","v72","v73","v74","v75","v76","v77","v78","v79","v80","v81","v82","v83","v84","v85","v86","v87","v88","v89","v90","v91","v92","v93","v94","v95","v96","v97","v98","v99","v100","v101","v102","v103","v104","v105","v106","v107","v108","v109","v110","v111","v112","v113","v114","v115","v116","v117","v118","v119","v120","v121","v122","v123","v124","v125","v126","v127","v128","v129","v130","v131","v132","v133","v134","v135","v136","v137","v138","v139","v140","v141","v142","v143","v144","v145","v146","v147","v148","v149","v150","v151","v152","v153","v154","v155","v156","v157","v158","v159","v160","v161","v162","v163","v164","v165","v166","v167","v168","v169","v170","v171","v172","v173","v174","v175","v176","v177","v178","v179","v180","v181","v182","v183","v184","v185","v186","v187","v188","v189","v190","v191","v192","v193","v194","v195","v196","v197","v198","v199","v200","v201","v202","v203","v204","v205","v206","v207","v208","v209","v210","v211","v212","v213","v214","v215","v216","v217","v218","v219","v220","v221","v222","v223","v224","v225","v226","v227","v228","v229","v230","v231","v232","v233","v234","v235","v236","v237","v238","v239","v240","v241","v242","v243","v244","v245","v246","v247","v248","v249","v250","v251","v252","v253","v254","v255","a0","a1","a2","a3","a4","a5","a6","a7","a8","a9","a10","a11","a12","a13","a14","a15","a16","a17","a18","a19","a20","a21","a22","a23","a24","a25","a26","a27","a28","a29","a30","a31","a32","a33","a34","a35","a36","a37","a38","a39","a40","a41","a42","a43","a44","a45","a46","a47","a48","a49","a50","a51","a52","a53","a54","a55","a56","a57","a58","a59","a60","a61","a62","a63","a64","a65","a66","a67","a68","a69","a70","a71","a72","a73","a74","a75","a76","a77","a78","a79","a80","a81","a82","a83","a84","a85","a86","a87","a88","a89","a90","a91","a92","a93","a94","a95","a96","a97","a98","a99","a100","a101","a102","a103","a104","a105","a106","a107","a108","a109","a110","a111","a112","a113","a114","a115","a116","a117","a118","a119","a120","a121","a122","a123","a124","a125","a126","a127","a128","a129","a130","a131","a132","a133","a134","a135","a136","a137","a138","a139","a140","a141","a142","a143","a144","a145","a146","a147","a148","a149","a150","a151","a152","a153","a154","a155","a156","a157","a158","a159","a160","a161","a162","a163","a164","a165","a166","a167","a168","a169","a170","a171","a172","a173","a174","a175","a176","a177","a178","a179","a180","a181","a182","a183","a184","a185","a186","a187","a188","a189","a190","a191","a192","a193","a194","a195","a196","a197","a198","a199","a200","a201","a202","a203","a204","a205","a206","a207","a208","a209","a210","a211","a212","a213","a214","a215","a216","a217","a218","a219","a220","a221","a222","a223","a224","a225","a226","a227","a228","a229","a230","a231","a232","a233","a234","a235","a236","a237","a238","a239","a240","a241","a242","a243","a244","a245","a246","a247","a248","a249","a250","a251","a252","a253","a254","a255","s16","s17","s18","s19","s20","s21","s22","s23","s24","s25","s26","s27","s28","s29","s30","s31","s33","s34","s35","s36","s37","s38","s39","s40","s41","s42","s43","s44","s45","s46","s47","s48","s49","s50","s51","s52","s53","s54","s55","s56","s57","s58","s59","s60","s61","s62","s63","s64","s65","s66","s67","s68","s69","s70","s71","s72","s73","s74","s75","s76","s77","s78","s79","s80","s81","s82","s83","s84","s85","s86","s87","s88","s89","s90","s91","s92","s93","s94","s95","s96","s97","s98","s99","s100","s101", "vcc", "scc", "memory");
}

}

extern "C" void kernel_launch(void* const* d_in, const int* in_sizes, int n_in, void* d_out, int out_size, void* d_ws,
                              size_t ws_size, hipStream_t stream) {
  const uint16_t* x = (const uint16_t*)d_in[0];
  const uint16_t* wqkv = (const uint16_t*)d_in[1];
  const uint16_t* wo = (const uint16_t*)d_in[2];
  const uint16_t* ls = (const uint16_t*)d_in[3];
  const uint16_t* w1 = (const uint16_t*)d_in[4];
  const uint16_t* w2 = (const uint16_t*)d_in[5];
  const uint16_t* rc = (const uint16_t*)d_in[6];
  const int* ridx = (const int*)d_in[7];
  const uint16_t* mask = (const uint16_t*)d_in[8];
  swin::swin_main<<<256, 256, 0, stream>>>(x, mask, (char*)d_ws, (uint16_t*)d_out, wqkv, wo, ls, w1, w2, rc, ridx);
}
```

```cpp
#include <hip/hip_runtime.h>
#include <hip/hip_bf16.h>
#include <stdint.h>

namespace swin {

constexpr unsigned CT_WORDS = 8192u;
constexpr unsigned LDS_BYTES = 163840u;

__device__ __attribute__((aligned(256))) const unsigned CT_TAB[CT_WORDS] = {0x4800u,0x4820u,0x4840u,0x4800u,0x4808u,0x4828u,0x4848u,0x4810u,0x4810u,0x4830u,0x4850u,0x4820u,0x4818u,0x4838u,0x4858u,0x4830u,0x4880u,0x48a0u,0x48c0u,0x4880u,0x4888u,0x48a8u,0x48c8u,0x4890u,0x4890u,0x48b0u,0x48d0u,0x48a0u,0x4898u,0x48b8u,0x48d8u,0x48b0u,0x4920u,0x4900u,0x4960u,0x4920u,0x4928u,0x4908u,0x4968u,0x4930u,0x4930u,0x4910u,0x4970u,0x4900u,0x4938u,0x4918u,0x4978u,0x4910u,0x49a0u,0x4980u,0x49e0u,0x49a0u,0x49a8u,0x4988u,0x49e8u,0x49b0u,0x49b0u,0x4990u,0x49f0u,0x4980u,0x49b8u,0x4998u,0x49f8u,0x4990u,0x4c40u,0x4c60u,0x4c00u,0x4a00u,0x4c48u,0x4c68u,0x4c08u,0x4a10u,0x4c50u,0x4c70u,0x4c10u,0x4a20u,0x4c58u,0x4c78u,0x4c18u,0x4a30u,0x4cc0u,0x4ce0u,0x4c80u,0x4a80u,0x4cc8u,0x4ce8u,0x4c88u,0x4a90u,0x4cd0u,0x4cf0u,0x4c90u,0x4aa0u,0x4cd8u,0x4cf8u,0x4c98u,0x4ab0u,0x4d60u,0x4d40u,0x4d20u,0x4b20u,0x4d68u,0x4d48u,0x4d28u,0x4b30u,0x4d70u,0x4d50u,0x4d30u,0x4b00u,0x4d78u,0x4d58u,0x4d38u,0x4b10u,0x4de0u,0x4dc0u,0x4da0u,0x4ba0u,0x4de8u,0x4dc8u,0x4da8u,0x4bb0u,0x4df0u,0x4dd0u,0x4db0u,0x4b80u,0x4df8u,0x4dd8u,0x4db8u,0x4b90u,0x5000u,0x5020u,0x5040u,0x4c40u,0x5008u,0x5028u,0x5048u,0x4c50u,0x5010u,0x5030u,0x5050u,0x4c60u,0x5018u,0x5038u,0x5058u,0x4c70u,0x5080u,0x50a0u,0x50c0u,0x4cc0u,0x5088u,0x50a8u,0x50c8u,0x4cd0u,0x5090u,0x50b0u,0x50d0u,0x4ce0u,0x5098u,0x50b8u,0x50d8u,0x4cf0u,0x5120u,0x5100u,0x5160u,0x4d60u,0x5128u,0x5108u,0x5168u,0x4d70u,0x5130u,0x5110u,0x5170u,0x4d40u,0x5138u,0x5118u,0x5178u,0x4d50u,0x51a0u,0x5180u,0x51e0u,0x4de0u,0x51a8u,0x5188u,0x51e8u,0x4df0u,0x51b0u,0x5190u,0x51f0u,0x4dc0u,0x51b8u,0x5198u,0x51f8u,0x4dd0u,0x5440u,0x5460u,0x5400u,0x4e40u,0x5448u,0x5468u,0x5408u,0x4e50u,0x5450u,0x5470u,0x5410u,0x4e60u,0x5458u,0x5478u,0x5418u,0x4e70u,0x54c0u,0x54e0u,0x5480u,0x4ec0u,0x54c8u,0x54e8u,0x5488u,0x4ed0u,0x54d0u,0x54f0u,0x5490u,0x4ee0u,0x54d8u,0x54f8u,0x5498u,0x4ef0u,0x5560u,0x5540u,0x5520u,0x4f60u,0x5568u,0x5548u,0x5528u,0x4f70u,0x5570u,0x5550u,0x5530u,0x4f40u,0x5578u,0x5558u,0x5538u,0x4f50u,0x55e0u,0x55c0u,0x55a0u,0x4fe0u,0x55e8u,0x55c8u,0x55a8u,0x4ff0u,0x55f0u,0x55d0u,0x55b0u,0x4fc0u,0x55f8u,0x55d8u,0x55b8u,0x4fd0u,0x4840u,0x1e800u,0x1eb00u,0x0u,0x4848u,0x1ec90u,0x1ef90u,0x10u,0x4850u,0x1f120u,0x1f420u,0x20u,0x4858u,0x1f5b0u,0x1f8b0u,0x30u,0x48c0u,0x1fa40u,0x1fd40u,0x40u,0x48c8u,0x1fed0u,0x201d0u,0x50u,0x48d0u,0x20360u,0x20660u,0x60u,0x48d8u,0x207f0u,0x20af0u,0x70u,0x4960u,0x20c80u,0x20f80u,0x180u,0x4968u,0x21110u,0x21410u,0x190u,0x4970u,0x215a0u,0x218a0u,0x1a0u,0x4978u,0x21a30u,0x21d30u,0x1b0u,0x49e0u,0x21ec0u,0x221c0u,0x1c0u,0x49e8u,0x22350u,0x22650u,0x1d0u,0x49f0u,0x227e0u,0x22ae0u,0x1e0u,0x49f8u,0x22c70u,0x22f70u,0x1f0u,0x4a40u,0x1e830u,0x1eb08u,0x320u,0x4a48u,0x1ecc0u,0x1ef98u,0x330u,0x4a50u,0x1f150u,0x1f428u,0x300u,0x4a58u,0x1f5e0u,0x1f8b8u,0x310u,0x4ac0u,0x1fa70u,0x1fd48u,0x360u,0x4ac8u,0x1ff00u,0x201d8u,0x370u,0x4ad0u,0x20390u,0x20668u,0x340u,0x4ad8u,0x20820u,0x20af8u,0x350u,0x4b60u,0x20cb0u,0x20f88u,0x4a0u,0x4b68u,0x21140u,0x21418u,0x4b0u,0x4b70u,0x215d0u,0x218a8u,0x480u,0x4b78u,0x21a60u,0x21d38u,0x490u,0x4be0u,0x21ef0u,0x221c8u,0x4e0u,0x4be8u,0x22380u,0x22658u,0x4f0u,0x4bf0u,0x22810u,0x22ae8u,0x4c0u,0x4bf8u,0x22ca0u,0x22f78u,0x4d0u,0x4c00u,0x1e860u,0x1eb10u,0x600u,0x4c08u,0x1ecf0u,0x1efa0u,0x610u,0x4c10u,0x1f180u,0x1f430u,0x620u,0x4c18u,0x1f610u,0x1f8c0u,0x630u,0x4c80u,0x1faa0u,0x1fd50u,0x640u,0x4c88u,0x1ff30u,0x201e0u,0x650u,0x4c90u,0x203c0u,0x20670u,0x660u,0x4c98u,0x20850u,0x20b00u,0x670u,0x4d20u,0x20ce0u,0x20f90u,0x780u,0x4d28u,0x21170u,0x21420u,0x790u,0x4d30u,0x21600u,0x218b0u,0x7a0u,0x4d38u,0x21a90u,0x21d40u,0x7b0u,0x4da0u,0x21f20u,0x221d0u,0x7c0u,0x4da8u,0x223b0u,0x22660u,0x7d0u,0x4db0u,0x22840u,0x22af0u,0x7e0u,0x4db8u,0x22cd0u,0x22f80u,0x7f0u,0x4e00u,0x1e890u,0x1eb18u,0x920u,0x4e08u,0x1ed20u,0x1efa8u,0x930u,0x4e10u,0x1f1b0u,0x1f438u,0x900u,0x4e18u,0x1f640u,0x1f8c8u,0x910u,0x4e80u,0x1fad0u,0x1fd58u,0x960u,0x4e88u,0x1ff60u,0x201e8u,0x970u,0x4e90u,0x203f0u,0x20678u,0x940u,0x4e98u,0x20880u,0x20b08u,0x950u,0x4f20u,0x20d10u,0x20f98u,0xaa0u,0x4f28u,0x211a0u,0x21428u,0xab0u,0x4f30u,0x21630u,0x218b8u,0xa80u,0x4f38u,0x21ac0u,0x21d48u,0xa90u,0x4fa0u,0x21f50u,0x221d8u,0xae0u,0x4fa8u,0x223e0u,0x22668u,0xaf0u,0x4fb0u,0x22870u,0x22af8u,0xac0u,0x4fb8u,0x22d00u,0x22f88u,0xad0u,0x0u,0x40u,0x9000u,0x9000u,0x80u,0xc0u,0x9010u,0x9010u,0x110u,0x150u,0x9020u,0x9020u,0x190u,0x1d0u,0x9030u,0x9030u,0x220u,0x260u,0x9040u,0x9040u,0x2a0u,0x2e0u,0x9050u,0x9050u,0x330u,0x370u,0x9060u,0x9060u,0x3b0u,0x3f0u,0x9070u,0x9070u,0x440u,0x400u,0x9080u,0x9080u,0x4c0u,0x480u,0x9090u,0x9090u,0x550u,0x510u,0x90a0u,0x90a0u,0x5d0u,0x590u,0x90b0u,0x90b0u,0x660u,0x620u,0x90c0u,0x90c0u,0x6e0u,0x6a0u,0x90d0u,0x90d0u,0x770u,0x730u,0x90e0u,0x90e0u,0x7f0u,0x7b0u,0x90f0u,0x90f0u,0x10u,0x50u,0x9300u,0x9300u,0x90u,0xd0u,0x9310u,0x9310u,0x100u,0x140u,0x9320u,0x9320u,0x180u,0x1c0u,0x9330u,0x9330u,0x230u,0x270u,0x9340u,0x9340u,0x2b0u,0x2f0u,0x9350u,0x9350u,0x320u,0x360u,0x9360u,0x9360u,0x3a0u,0x3e0u,0x9370u,0x9370u,0x450u,0x410u,0x9380u,0x9380u,0x4d0u,0x490u,0x9390u,0x9390u,0x540u,0x500u,0x93a0u,0x93a0u,0x5c0u,0x580u,0x93b0u,0x93b0u,0x670u,0x630u,0x93c0u,0x93c0u,0x6f0u,0x6b0u,0x93d0u,0x93d0u,0x760u,0x720u,0x93e0u,0x93e0u,0x7e0u,0x7a0u,0x93f0u,0x93f0u,0x20u,0x60u,0x9600u,0x9600u,0xa0u,0xe0u,0x9610u,0x9610u,0x130u,0x170u,0x9620u,0x9620u,0x1b0u,0x1f0u,0x9630u,0x9630u,0x200u,0x240u,0x9640u,0x9640u,0x280u,0x2c0u,0x9650u,0x9650u,0x310u,0x350u,0x9660u,0x9660u,0x390u,0x3d0u,0x9670u,0x9670u,0x460u,0x420u,0x9680u,0x9680u,0x4e0u,0x4a0u,0x9690u,0x9690u,0x570u,0x530u,0x96a0u,0x96a0u,0x5f0u,0x5b0u,0x96b0u,0x96b0u,0x640u,0x600u,0x96c0u,0x96c0u,0x6c0u,0x680u,0x96d0u,0x96d0u,0x750u,0x710u,0x96e0u,0x96e0u,0x7d0u,0x790u,0x96f0u,0x96f0u,0x30u,0x70u,0x9900u,0x9900u,0xb0u,0xf0u,0x9910u,0x9910u,0x120u,0x160u,0x9920u,0x9920u,0x1a0u,0x1e0u,0x9930u,0x9930u,0x210u,0x250u,0x9940u,0x9940u,0x290u,0x2d0u,0x9950u,0x9950u,0x300u,0x340u,0x9960u,0x9960u,0x380u,0x3c0u,0x9970u,0x9970u,0x470u,0x430u,0x9980u,0x9980u,0x4f0u,0x4b0u,0x9990u,0x9990u,0x560u,0x520u,0x99a0u,0x99a0u,0x5e0u,0x5a0u,0x99b0u,0x99b0u,0x650u,0x610u,0x99c0u,0x99c0u,0x6d0u,0x690u,0x99d0u,0x99d0u,0x740u,0x700u,0x99e0u,0x99e0u,0x7c0u,0x780u,0x99f0u,0x99f0u,0x9c00u,0x12000u,0x12040u,0x12000u,0x9c10u,0x12190u,0x121d0u,0x12010u,0x9c20u,0x12320u,0x12360u,0x12020u,0x9c30u,0x124b0u,0x124f0u,0x12030u,0x9c40u,0x12640u,0x12600u,0x12040u,0x9c50u,0x127d0u,0x12790u,0x12050u,0x9c60u,0x12960u,0x12920u,0x12060u,0x9c70u,0x12af0u,0x12ab0u,0x12070u,0x9c80u,0x12c00u,0x12c40u,0x12080u,0x9c90u,0x12d90u,0x12dd0u,0x12090u,0x9ca0u,0x12f20u,0x12f60u,0x120a0u,0x9cb0u,0x130b0u,0x130f0u,0x120b0u,0x9cc0u,0x13240u,0x13200u,0x120c0u,0x9cd0u,0x133d0u,0x13390u,0x120d0u,0x9ce0u,0x13560u,0x13520u,0x120e0u,0x9cf0u,0x136f0u,0x136b0u,0x120f0u,0x9f00u,0x12010u,0x12048u,0x12100u,0x9f10u,0x12180u,0x121d8u,0x12110u,0x9f20u,0x12330u,0x12368u,0x12120u,0x9f30u,0x124a0u,0x124f8u,0x12130u,0x9f40u,0x12650u,0x12608u,0x12140u,0x9f50u,0x127c0u,0x12798u,0x12150u,0x9f60u,0x12970u,0x12928u,0x12160u,0x9f70u,0x12ae0u,0x12ab8u,0x12170u,0x9f80u,0x12c10u,0x12c48u,0x12180u,0x9f90u,0x12d80u,0x12dd8u,0x12190u,0x9fa0u,0x12f30u,0x12f68u,0x121a0u,0x9fb0u,0x130a0u,0x130f8u,0x121b0u,0x9fc0u,0x13250u,0x13208u,0x121c0u,0x9fd0u,0x133c0u,0x13398u,0x121d0u,0x9fe0u,0x13570u,0x13528u,0x121e0u,0x9ff0u,0x136e0u,0x136b8u,0x121f0u,0xa200u,0x12020u,0x12050u,0x12200u,0xa210u,0x121b0u,0x121c0u,0x12210u,0xa220u,0x12300u,0x12370u,0x12220u,0xa230u,0x12490u,0x124e0u,0x12230u,0xa240u,0x12660u,0x12610u,0x12240u,0xa250u,0x127f0u,0x12780u,0x12250u,0xa260u,0x12940u,0x12930u,0x12260u,0xa270u,0x12ad0u,0x12aa0u,0x12270u,0xa280u,0x12c20u,0x12c50u,0x12280u,0xa290u,0x12db0u,0x12dc0u,0x12290u,0xa2a0u,0x12f00u,0x12f70u,0x122a0u,0xa2b0u,0x13090u,0x130e0u,0x122b0u,0xa2c0u,0x13260u,0x13210u,0x122c0u,0xa2d0u,0x133f0u,0x13380u,0x122d0u,0xa2e0u,0x13540u,0x13530u,0x122e0u,0xa2f0u,0x136d0u,0x136a0u,0x122f0u,0xa500u,0x12030u,0x12058u,0x12300u,0xa510u,0x121a0u,0x121c8u,0x12310u,0xa520u,0x12310u,0x12378u,0x12320u,0xa530u,0x12480u,0x124e8u,0x12330u,0xa540u,0x12670u,0x12618u,0x12340u,0xa550u,0x127e0u,0x12788u,0x12350u,0xa560u,0x12950u,0x12938u,0x12360u,0xa570u,0x12ac0u,0x12aa8u,0x12370u,0xa580u,0x12c30u,0x12c58u,0x12380u,0xa590u,0x12da0u,0x12dc8u,0x12390u,0xa5a0u,0x12f10u,0x12f78u,0x123a0u,0xa5b0u,0x13080u,0x130e8u,0x123b0u,0xa5c0u,0x13270u,0x13218u,0x123c0u,0xa5d0u,0x133e0u,0x13388u,0x123d0u,0xa5e0u,0x13550u,0x13538u,0x123e0u,0xa5f0u,0x136c0u,0x136a8u,0x123f0u,0x4800u,0x1c300u,0x1e800u,0x1eb00u,0x4880u,0x1c310u,0x100000u,0x1eb02u,0x4910u,0x1c320u,0x100000u,0x1eb04u,0x4990u,0x1c330u,0x100000u,0x1eb06u,0x4a20u,0x1c340u,0x1e800u,0x1eb08u,0x4aa0u,0x1c350u,0x100000u,0x1eb0au,0x4b30u,0x1c360u,0x100000u,0x1eb0cu,0x4bb0u,0x1c370u,0x100000u,0x1eb0eu,0x4c40u,0x1c380u,0x1e800u,0x1eb10u,0x4cc0u,0x1c390u,0x100000u,0x1eb12u,0x4d50u,0x1c3a0u,0x100000u,0x1eb14u,0x4dd0u,0x1c3b0u,0x100000u,0x1eb16u,0x4e60u,0x1c3c0u,0x1e800u,0x1eb18u,0x4ee0u,0x1c3d0u,0x100000u,0x1eb1au,0x4f70u,0x1c3e0u,0x100000u,0x1eb1cu,0x4ff0u,0x1c3f0u,0x100000u,0x1eb1eu,0x4810u,0x1c400u,0x1e830u,0x1eb00u,0x4890u,0x1c410u,0x100000u,0x1eb02u,0x4900u,0x1c420u,0x100000u,0x1eb04u,0x4980u,0x1c430u,0x100000u,0x1eb06u,0x4a30u,0x1c440u,0x1e830u,0x1eb08u,0x4ab0u,0x1c450u,0x100000u,0x1eb0au,0x4b20u,0x1c460u,0x100000u,0x1eb0cu,0x4ba0u,0x1c470u,0x100000u,0x1eb0eu,0x4c50u,0x1c480u,0x1e830u,0x1eb10u,0x4cd0u,0x1c490u,0x100000u,0x1eb12u,0x4d40u,0x1c4a0u,0x100000u,0x1eb14u,0x4dc0u,0x1c4b0u,0x100000u,0x1eb16u,0x4e70u,0x1c4c0u,0x1e830u,0x1eb18u,0x4ef0u,0x1c4d0u,0x100000u,0x1eb1au,0x4f60u,0x1c4e0u,0x100000u,0x1eb1cu,0x4fe0u,0x1c4f0u,0x100000u,0x1eb1eu,0x4820u,0x1c500u,0x1e860u,0x1eb00u,0x48a0u,0x1c510u,0x100000u,0x1eb02u,0x4930u,0x1c520u,0x100000u,0x1eb04u,0x49b0u,0x1c530u,0x100000u,0x1eb06u,0x4a00u,0x1c540u,0x1e860u,0x1eb08u,0x4a80u,0x1c550u,0x100000u,0x1eb0au,0x4b10u,0x1c560u,0x100000u,0x1eb0cu,0x4b90u,0x1c570u,0x100000u,0x1eb0eu,0x4c60u,0x1c580u,0x1e860u,0x1eb10u,0x4ce0u,0x1c590u,0x100000u,0x1eb12u,0x4d70u,0x1c5a0u,0x100000u,0x1eb14u,0x4df0u,0x1c5b0u,0x100000u,0x1eb16u,0x4e40u,0x1c5c0u,0x1e860u,0x1eb18u,0x4ec0u,0x1c5d0u,0x100000u,0x1eb1au,0x4f50u,0x1c5e0u,0x100000u,0x1eb1cu,0x4fd0u,0x1c5f0u,0x100000u,0x1eb1eu,0x4830u,0x1c600u,0x1e890u,0x1eb00u,0x48b0u,0x1c610u,0x100000u,0x1eb02u,0x4920u,0x1c620u,0x100000u,0x1eb04u,0x49a0u,0x1c630u,0x100000u,0x1eb06u,0x4a10u,0x1c640u,0x1e890u,0x1eb08u,0x4a90u,0x1c650u,0x100000u,0x1eb0au,0x4b00u,0x1c660u,0x100000u,0x1eb0cu,0x4b80u,0x1c670u,0x100000u,0x1eb0eu,0x4c70u,0x1c680u,0x1e890u,0x1eb10u,0x4cf0u,0x1c690u,0x100000u,0x1eb12u,0x4d60u,0x1c6a0u,0x100000u,0x1eb14u,0x4de0u,0x1c6b0u,0x100000u,0x1eb16u,0x4e50u,0x1c6c0u,0x1e890u,0x1eb18u,0x4ed0u,0x1c6d0u,0x100000u,0x1eb1au,0x4f40u,0x1c6e0u,0x100000u,0x1eb1cu,0x4fc0u,0x1c6f0u,0x100000u,0x1eb1eu,0x24d00u,0x1d300u,0x1d300u,0xc0100u,0x24d02u,0x1d302u,0x100000u,0xc0110u,0x24d04u,0x1d304u,0x100000u,0xc0120u,0x24d06u,0x1d306u,0x100000u,0xc0130u,0x24f00u,0x1d310u,0x1d300u,0xc0140u,0x24f02u,0x1d312u,0x100000u,0xc0150u,0x24f04u,0x1d314u,0x100000u,0xc0160u,0x24f06u,0x1d316u,0x100000u,0xc0170u,0x25100u,0x1d320u,0x1d300u,0xc0280u,0x25102u,0x1d322u,0x100000u,0xc0290u,0x25104u,0x1d324u,0x100000u,0xc02a0u,0x25106u,0x1d326u,0x100000u,0xc02b0u,0x25300u,0x1d330u,0x1d300u,0xc02c0u,0x25302u,0x1d332u,0x100000u,0xc02d0u,0x25304u,0x1d334u,0x100000u,0xc02e0u,0x25306u,0x1d336u,0x100000u,0xc02f0u,0x24d08u,0x1d308u,0x1d310u,0xc0410u,0x24d0au,0x1d30au,0x100000u,0xc0400u,0x24d0cu,0x1d30cu,0x100000u,0xc0430u,0x24d0eu,0x1d30eu,0x100000u,0xc0420u,0x24f08u,0x1d318u,0x1d310u,0xc0450u,0x24f0au,0x1d31au,0x100000u,0xc0440u,0x24f0cu,0x1d31cu,0x100000u,0xc0470u,0x24f0eu,0x1d31eu,0x100000u,0xc0460u,0x25108u,0x1d328u,0x1d310u,0xef990u,0x2510au,0x1d32au,0x100000u,0xef980u,0x2510cu,0x1d32cu,0x100000u,0xef9b0u,0x2510eu,0x1d32eu,0x100000u,0xef9a0u,0x25308u,0x1d338u,0x1d310u,0xef9d0u,0x2530au,0x1d33au,0x100000u,0xef9c0u,0x2530cu,0x1d33cu,0x100000u,0xef9f0u,0x2530eu,0x1d33eu,0x100000u,0xef9e0u,0x25500u,0x1d340u,0x1d320u,0xefb20u,0x25502u,0x1d342u,0x100000u,0xefb30u,0x25504u,0x1d344u,0x100000u,0xefb00u,0x25506u,0x1d346u,0x100000u,0xefb10u,0x25700u,0x1d350u,0x1d320u,0xefb60u,0x25702u,0x1d352u,0x100000u,0xefb70u,0x25704u,0x1d354u,0x100000u,0xefb40u,0x25706u,0x1d356u,0x100000u,0xefb50u,0x25900u,0x1d360u,0x1d320u,0xefca0u,0x25902u,0x1d362u,0x100000u,0xefcb0u,0x25904u,0x1d364u,0x100000u,0xefc80u,0x25906u,0x1d366u,0x100000u,0xefc90u,0x25b00u,0x1d370u,0x1d320u,0xefce0u,0x25b02u,0x1d372u,0x100000u,0xefcf0u,0x25b04u,0x1d374u,0x100000u,0xefcc0u,0x25b06u,0x1d376u,0x100000u,0xefcd0u,0x100000u,0x1d348u,0x1d330u,0xefe30u,0x100000u,0x1d34au,0x100000u,0xefe20u,0x100000u,0x1d34cu,0x100000u,0xefe10u,0x100000u,0x1d34eu,0x100000u,0xefe00u,0x100000u,0x1d358u,0x1d330u,0xefe70u,0x100000u,0x1d35au,0x100000u,0xefe60u,0x100000u,0x1d35cu,0x100000u,0xefe50u,0x100000u,0x1d35eu,0x100000u,0xefe40u,0x100000u,0x1d368u,0x1d330u,0xeffb0u,0x100000u,0x1d36au,0x100000u,0xeffa0u,0x100000u,0x1d36cu,0x100000u,0xeff90u,0x100000u,0x1d36eu,0x100000u,0xeff80u,0x100000u,0x1d378u,0x1d330u,0xefff0u,0x100000u,0x1d37au,0x100000u,0xeffe0u,0x100000u,0x1d37cu,0x100000u,0xeffd0u,0x100000u,0x1d37eu,0x100000u,0xeffc0u,0x0u,0x30420u,0x8fc50u,0xc0000u,0x10u,0x30430u,0x8fc40u,0xc0010u,0x20u,0x30400u,0x8fc70u,0xc0020u,0x30u,0x30410u,0x8fc60u,0xc0030u,0x40u,0x30460u,0x8fc10u,0xc0040u,0x50u,0x30470u,0x8fc00u,0xc0050u,0x60u,0x30440u,0x8fc30u,0xc0060u,0x70u,0x30450u,0x8fc20u,0xc0070u,0x80u,0x304b0u,0x8fcd0u,0xc0080u,0x90u,0x304a0u,0x8fcc0u,0xc0090u,0xa0u,0x30490u,0x8fcf0u,0xc00a0u,0xb0u,0x30480u,0x8fce0u,0xc00b0u,0xc0u,0x304f0u,0x8fc90u,0xc00c0u,0xd0u,0x304e0u,0x8fc80u,0xc00d0u,0xe0u,0x304d0u,0x8fcb0u,0xc00e0u,0xf0u,0x304c0u,0x8fca0u,0xc00f0u,0x100u,0x30530u,0x8fd60u,0xc0100u,0x110u,0x30520u,0x8fd70u,0xc0110u,0x120u,0x30510u,0x8fd40u,0xc0120u,0x130u,0x30500u,0x8fd50u,0xc0130u,0x140u,0x30570u,0x8fd20u,0xc0140u,0x150u,0x30560u,0x8fd30u,0xc0150u,0x160u,0x30550u,0x8fd00u,0xc0160u,0x170u,0x30540u,0x8fd10u,0xc0170u,0x190u,0x305b0u,0x8fde0u,0xc0190u,0x180u,0x305a0u,0x8fdf0u,0xc0180u,0x1b0u,0x30590u,0x8fdc0u,0xc01b0u,0x1a0u,0x30580u,0x8fdd0u,0xc01a0u,0x1d0u,0x305f0u,0x8fda0u,0xc01d0u,0x1c0u,0x305e0u,0x8fdb0u,0xc01c0u,0x1f0u,0x305d0u,0x8fd80u,0xc01f0u,0x1e0u,0x305c0u,0x8fd90u,0xc01e0u,0x210u,0x30640u,0x8fe60u,0xc0210u,0x200u,0x30650u,0x8fe70u,0xc0200u,0x230u,0x30660u,0x8fe40u,0xc0230u,0x220u,0x30670u,0x8fe50u,0xc0220u,0x250u,0x30600u,0x8fe20u,0xc0250u,0x240u,0x30610u,0x8fe30u,0xc0240u,0x270u,0x30620u,0x8fe00u,0xc0270u,0x260u,0x30630u,0x8fe10u,0xc0260u,0x290u,0x306c0u,0x8fef0u,0xc0290u,0x280u,0x306d0u,0x8fee0u,0xc0280u,0x2b0u,0x306e0u,0x8fed0u,0xc02b0u,0x2a0u,0x306f0u,0x8fec0u,0xc02a0u,0x2d0u,0x30680u,0x8feb0u,0xc02d0u,0x2c0u,0x30690u,0x8fea0u,0xc02c0u,0x2f0u,0x306a0u,0x8fe90u,0xc02f0u,0x2e0u,0x306b0u,0x8fe80u,0xc02e0u,0x320u,0x30740u,0x8ff70u,0xc0320u,0x330u,0x30750u,0x8ff60u,0xc0330u,0x300u,0x30760u,0x8ff50u,0xc0300u,0x310u,0x30770u,0x8ff40u,0xc0310u,0x360u,0x30700u,0x8ff30u,0xc0360u,0x370u,0x30710u,0x8ff20u,0xc0370u,0x340u,0x30720u,0x8ff10u,0xc0340u,0x350u,0x30730u,0x8ff00u,0xc0350u,0x3a0u,0x307d0u,0x8fff0u,0xc03a0u,0x3b0u,0x307c0u,0x8ffe0u,0xc03b0u,0x380u,0x307f0u,0x8ffd0u,0xc0380u,0x390u,0x307e0u,0x8ffc0u,0xc0390u,0x3e0u,0x30790u,0x8ffb0u,0xc03e0u,0x3f0u,0x30780u,0x8ffa0u,0xc03f0u,0x3c0u,0x307b0u,0x8ff90u,0xc03c0u,0x3d0u,0x307a0u,0x8ff80u,0xc03d0u,0x0u,0x11f820u,0x0u,0x7030cu,0x0u,0x11f830u,0x0u,0x7030cu,0x0u,0x11f800u,0x0u,0x7030cu,0x0u,0x11f810u,0x0u,0x7030cu,0x0u,0x11f860u,0x0u,0x7030cu,0x0u,0x11f870u,0x0u,0x7030cu,0x0u,0x11f840u,0x0u,0x7030cu,0x0u,0x11f850u,0x0u,0x7030cu,0x0u,0x11f8b0u,0x0u,0x7230cu,0x0u,0x11f8a0u,0x0u,0x7230cu,0x0u,0x11f890u,0x0u,0x7230cu,0x0u,0x11f880u,0x0u,0x7230cu,0x0u,0x11f8f0u,0x0u,0x7230cu,0x0u,0x11f8e0u,0x0u,0x7230cu,0x0u,0x11f8d0u,0x0u,0x7230cu,0x0u,0x11f8c0u,0x0u,0x7230cu,0x0u,0x11f930u,0x0u,0x7232cu,0x0u,0x11f920u,0x0u,0x7232cu,0x0u,0x11f910u,0x0u,0x7232cu,0x0u,0x11f900u,0x0u,0x7232cu,0x0u,0x11f970u,0x0u,0x7232cu,0x0u,0x11f960u,0x0u,0x7232cu,0x0u,0x11f950u,0x0u,0x7232cu,0x0u,0x11f940u,0x0u,0x7232cu,0x0u,0x11f9b0u,0x0u,0x72124u,0x0u,0x11f9a0u,0x0u,0x72124u,0x0u,0x11f990u,0x0u,0x72124u,0x0u,0x11f980u,0x0u,0x72124u,0x0u,0x11f9f0u,0x0u,0x72124u,0x0u,0x11f9e0u,0x0u,0x72124u,0x0u,0x11f9d0u,0x0u,0x72124u,0x0u,0x11f9c0u,0x0u,0x72124u,0x0u,0x11fa40u,0x0u,0x721a6u,0x0u,0x11fa50u,0x0u,0x721a6u,0x0u,0x11fa60u,0x0u,0x721a6u,0x0u,0x11fa70u,0x0u,0x721a6u,0x0u,0x11fa00u,0x0u,0x721a6u,0x0u,0x11fa10u,0x0u,0x721a6u,0x0u,0x11fa20u,0x0u,0x721a6u,0x0u,0x11fa30u,0x0u,0x721a6u,0x0u,0x11fac0u,0x0u,0x72186u,0x0u,0x11fad0u,0x0u,0x72186u,0x0u,0x11fae0u,0x0u,0x72186u,0x0u,0x11faf0u,0x0u,0x72186u,0x0u,0x11fa80u,0x0u,0x72186u,0x0u,0x11fa90u,0x0u,0x72186u,0x0u,0x11faa0u,0x0u,0x72186u,0x0u,0x11fab0u,0x0u,0x72186u,0x0u,0x11fb40u,0x0u,0x72186u,0x0u,0x11fb50u,0x0u,0x72186u,0x0u,0x11fb60u,0x0u,0x72186u,0x0u,0x11fb70u,0x0u,0x72186u,0x0u,0x11fb00u,0x0u,0x72186u,0x0u,0x11fb10u,0x0u,0x72186u,0x0u,0x11fb20u,0x0u,0x72186u,0x0u,0x11fb30u,0x0u,0x72186u,0x0u,0x11fbd0u,0x0u,0x7230cu,0x0u,0x11fbc0u,0x0u,0x7230cu,0x0u,0x11fbf0u,0x0u,0x7230cu,0x0u,0x11fbe0u,0x0u,0x7230cu,0x0u,0x11fb90u,0x0u,0x7230cu,0x0u,0x11fb80u,0x0u,0x7230cu,0x0u,0x11fbb0u,0x0u,0x7230cu,0x0u,0x11fba0u,0x0u,0x7230cu,0x5800u,0x5820u,0x4860u,0x4860u,0x5808u,0x5828u,0x4868u,0x4870u,0x5810u,0x5830u,0x4870u,0x5800u,0x5818u,0x5838u,0x4878u,0x5810u,0x5880u,0x58a0u,0x48e0u,0x48e0u,0x5888u,0x58a8u,0x48e8u,0x48f0u,0x5890u,0x58b0u,0x48f0u,0x5880u,0x5898u,0x58b8u,0x48f8u,0x5890u,0x5920u,0x5900u,0x4940u,0x4940u,0x5928u,0x5908u,0x4948u,0x4950u,0x5930u,0x5910u,0x4950u,0x5920u,0x5938u,0x5918u,0x4958u,0x5930u,0x59a0u,0x5980u,0x49c0u,0x49c0u,0x59a8u,0x5988u,0x49c8u,0x49d0u,0x59b0u,0x5990u,0x49d0u,0x59a0u,0x59b8u,0x5998u,0x49d8u,0x59b0u,0x5c40u,0x5c60u,0x4c20u,0x4a60u,0x5c48u,0x5c68u,0x4c28u,0x4a70u,0x5c50u,0x5c70u,0x4c30u,0x5a00u,0x5c58u,0x5c78u,0x4c38u,0x5a10u,0x5cc0u,0x5ce0u,0x4ca0u,0x4ae0u,0x5cc8u,0x5ce8u,0x4ca8u,0x4af0u,0x5cd0u,0x5cf0u,0x4cb0u,0x5a80u,0x5cd8u,0x5cf8u,0x4cb8u,0x5a90u,0x5d60u,0x5d40u,0x4d00u,0x4b40u,0x5d68u,0x5d48u,0x4d08u,0x4b50u,0x5d70u,0x5d50u,0x4d10u,0x5b20u,0x5d78u,0x5d58u,0x4d18u,0x5b30u,0x5de0u,0x5dc0u,0x4d80u,0x4bc0u,0x5de8u,0x5dc8u,0x4d88u,0x4bd0u,0x5df0u,0x5dd0u,0x4d90u,0x5ba0u,0x5df8u,0x5dd8u,0x4d98u,0x5bb0u,0x6000u,0x6020u,0x5060u,0x4c20u,0x6008u,0x6028u,0x5068u,0x4c30u,0x6010u,0x6030u,0x5070u,0x5c40u,0x6018u,0x6038u,0x5078u,0x5c50u,0x6080u,0x60a0u,0x50e0u,0x4ca0u,0x6088u,0x60a8u,0x50e8u,0x4cb0u,0x6090u,0x60b0u,0x50f0u,0x5cc0u,0x6098u,0x60b8u,0x50f8u,0x5cd0u,0x6120u,0x6100u,0x5140u,0x4d00u,0x6128u,0x6108u,0x5148u,0x4d10u,0x6130u,0x6110u,0x5150u,0x5d60u,0x6138u,0x6118u,0x5158u,0x5d70u,0x61a0u,0x6180u,0x51c0u,0x4d80u,0x61a8u,0x6188u,0x51c8u,0x4d90u,0x61b0u,0x6190u,0x51d0u,0x5de0u,0x61b8u,0x6198u,0x51d8u,0x5df0u,0x6440u,0x6460u,0x5420u,0x4e20u,0x6448u,0x6468u,0x5428u,0x4e30u,0x6450u,0x6470u,0x5430u,0x5e40u,0x6458u,0x6478u,0x5438u,0x5e50u,0x64c0u,0x64e0u,0x54a0u,0x4ea0u,0x64c8u,0x64e8u,0x54a8u,0x4eb0u,0x64d0u,0x64f0u,0x54b0u,0x5ec0u,0x64d8u,0x64f8u,0x54b8u,0x5ed0u,0x6560u,0x6540u,0x5500u,0x4f00u,0x6568u,0x6548u,0x5508u,0x4f10u,0x6570u,0x6550u,0x5510u,0x5f60u,0x6578u,0x6558u,0x5518u,0x5f70u,0x65e0u,0x65c0u,0x5580u,0x4f80u,0x65e8u,0x65c8u,0x5588u,0x4f90u,0x65f0u,0x65d0u,0x5590u,0x5fe0u,0x65f8u,0x65d8u,0x5598u,0x5ff0u,0x5820u,0x1e8c0u,0x1eb60u,0xc40u,0x5828u,0x1ed50u,0x1eff0u,0xc50u,0x5830u,0x1f1e0u,0x1f480u,0xc60u,0x5838u,0x1f670u,0x1f910u,0xc70u,0x58a0u,0x1fb00u,0x1fda0u,0xc00u,0x58a8u,0x1ff90u,0x20230u,0xc10u,0x58b0u,0x20420u,0x206c0u,0xc20u,0x58b8u,0x208b0u,0x20b50u,0xc30u,0x5900u,0x20d40u,0x20fe0u,0xdc0u,0x5908u,0x211d0u,0x21470u,0xdd0u,0x5910u,0x21660u,0x21900u,0xde0u,0x5918u,0x21af0u,0x21d90u,0xdf0u,0x5980u,0x21f80u,0x22220u,0xd80u,0x5988u,0x22410u,0x226b0u,0xd90u,0x5990u,0x228a0u,0x22b40u,0xda0u,0x5998u,0x22d30u,0x22fd0u,0xdb0u,0x5a20u,0x1e8f0u,0x1eb68u,0xf60u,0x5a28u,0x1ed80u,0x1eff8u,0xf70u,0x5a30u,0x1f210u,0x1f488u,0xf40u,0x5a38u,0x1f6a0u,0x1f918u,0xf50u,0x5aa0u,0x1fb30u,0x1fda8u,0xf20u,0x5aa8u,0x1ffc0u,0x20238u,0xf30u,0x5ab0u,0x20450u,0x206c8u,0xf00u,0x5ab8u,0x208e0u,0x20b58u,0xf10u,0x5b00u,0x20d70u,0x20fe8u,0x10e0u,0x5b08u,0x21200u,0x21478u,0x10f0u,0x5b10u,0x21690u,0x21908u,0x10c0u,0x5b18u,0x21b20u,0x21d98u,0x10d0u,0x5b80u,0x21fb0u,0x22228u,0x10a0u,0x5b88u,0x22440u,0x226b8u,0x10b0u,0x5b90u,0x228d0u,0x22b48u,0x1080u,0x5b98u,0x22d60u,0x22fd8u,0x1090u,0x5c60u,0x1e920u,0x1eb70u,0x1240u,0x5c68u,0x1edb0u,0x1f000u,0x1250u,0x5c70u,0x1f240u,0x1f490u,0x1260u,0x5c78u,0x1f6d0u,0x1f920u,0x1270u,0x5ce0u,0x1fb60u,0x1fdb0u,0x1200u,0x5ce8u,0x1fff0u,0x20240u,0x1210u,0x5cf0u,0x20480u,0x206d0u,0x1220u,0x5cf8u,0x20910u,0x20b60u,0x1230u,0x5d40u,0x20da0u,0x20ff0u,0x13c0u,0x5d48u,0x21230u,0x21480u,0x13d0u,0x5d50u,0x216c0u,0x21910u,0x13e0u,0x5d58u,0x21b50u,0x21da0u,0x13f0u,0x5dc0u,0x21fe0u,0x22230u,0x1380u,0x5dc8u,0x22470u,0x226c0u,0x1390u,0x5dd0u,0x22900u,0x22b50u,0x13a0u,0x5dd8u,0x22d90u,0x22fe0u,0x13b0u,0x5e60u,0x1e950u,0x1eb78u,0x1560u,0x5e68u,0x1ede0u,0x1f008u,0x1570u,0x5e70u,0x1f270u,0x1f498u,0x1540u,0x5e78u,0x1f700u,0x1f928u,0x1550u,0x5ee0u,0x1fb90u,0x1fdb8u,0x1520u,0x5ee8u,0x20020u,0x20248u,0x1530u,0x5ef0u,0x204b0u,0x206d8u,0x1500u,0x5ef8u,0x20940u,0x20b68u,0x1510u,0x5f40u,0x20dd0u,0x20ff8u,0x16e0u,0x5f48u,0x21260u,0x21488u,0x16f0u,0x5f50u,0x216f0u,0x21918u,0x16c0u,0x5f58u,0x21b80u,0x21da8u,0x16d0u,0x5fc0u,0x22010u,0x22238u,0x16a0u,0x5fc8u,0x224a0u,0x226c8u,0x16b0u,0x5fd0u,0x22930u,0x22b58u,0x1680u,0x5fd8u,0x22dc0u,0x22fe8u,0x1690u,0x0u,0x40u,0x9000u,0xa800u,0x80u,0xc0u,0x9010u,0xa810u,0x110u,0x150u,0x9020u,0xa820u,0x190u,0x1d0u,0x9030u,0xa830u,0x220u,0x260u,0x9040u,0xa840u,0x2a0u,0x2e0u,0x9050u,0xa850u,0x330u,0x370u,0x9060u,0xa860u,0x3b0u,0x3f0u,0x9070u,0xa870u,0x440u,0x400u,0x9080u,0xa880u,0x4c0u,0x480u,0x9090u,0xa890u,0x550u,0x510u,0x90a0u,0xa8a0u,0x5d0u,0x590u,0x90b0u,0xa8b0u,0x660u,0x620u,0x90c0u,0xa8c0u,0x6e0u,0x6a0u,0x90d0u,0xa8d0u,0x770u,0x730u,0x90e0u,0xa8e0u,0x7f0u,0x7b0u,0x90f0u,0xa8f0u,0x10u,0x50u,0x9300u,0xab00u,0x90u,0xd0u,0x9310u,0xab10u,0x100u,0x140u,0x9320u,0xab20u,0x180u,0x1c0u,0x9330u,0xab30u,0x230u,0x270u,0x9340u,0xab40u,0x2b0u,0x2f0u,0x9350u,0xab50u,0x320u,0x360u,0x9360u,0xab60u,0x3a0u,0x3e0u,0x9370u,0xab70u,0x450u,0x410u,0x9380u,0xab80u,0x4d0u,0x490u,0x9390u,0xab90u,0x540u,0x500u,0x93a0u,0xaba0u,0x5c0u,0x580u,0x93b0u,0xabb0u,0x670u,0x630u,0x93c0u,0xabc0u,0x6f0u,0x6b0u,0x93d0u,0xabd0u,0x760u,0x720u,0x93e0u,0xabe0u,0x7e0u,0x7a0u,0x93f0u,0xabf0u,0x20u,0x60u,0x9600u,0xae00u,0xa0u,0xe0u,0x9610u,0xae10u,0x130u,0x170u,0x9620u,0xae20u,0x1b0u,0x1f0u,0x9630u,0xae30u,0x200u,0x240u,0x9640u,0xae40u,0x280u,0x2c0u,0x9650u,0xae50u,0x310u,0x350u,0x9660u,0xae60u,0x390u,0x3d0u,0x9670u,0xae70u,0x460u,0x420u,0x9680u,0xae80u,0x4e0u,0x4a0u,0x9690u,0xae90u,0x570u,0x530u,0x96a0u,0xaea0u,0x5f0u,0x5b0u,0x96b0u,0xaeb0u,0x640u,0x600u,0x96c0u,0xaec0u,0x6c0u,0x680u,0x96d0u,0xaed0u,0x750u,0x710u,0x96e0u,0xaee0u,0x7d0u,0x790u,0x96f0u,0xaef0u,0x30u,0x70u,0x9900u,0xb100u,0xb0u,0xf0u,0x9910u,0xb110u,0x120u,0x160u,0x9920u,0xb120u,0x1a0u,0x1e0u,0x9930u,0xb130u,0x210u,0x250u,0x9940u,0xb140u,0x290u,0x2d0u,0x9950u,0xb150u,0x300u,0x340u,0x9960u,0xb160u,0x380u,0x3c0u,0x9970u,0xb170u,0x470u,0x430u,0x9980u,0xb180u,0x4f0u,0x4b0u,0x9990u,0xb190u,0x560u,0x520u,0x99a0u,0xb1a0u,0x5e0u,0x5a0u,0x99b0u,0xb1b0u,0x650u,0x610u,0x99c0u,0xb1c0u,0x6d0u,0x690u,0x99d0u,0xb1d0u,0x740u,0x700u,0x99e0u,0xb1e0u,0x7c0u,0x780u,0x99f0u,0xb1f0u,0x9c08u,0x12060u,0x120a0u,0x12400u,0x9c18u,0x121f0u,0x12230u,0x12410u,0x9c28u,0x12340u,0x12380u,0x12420u,0x9c38u,0x124d0u,0x12510u,0x12430u,0x9c48u,0x12620u,0x126e0u,0x12440u,0x9c58u,0x127b0u,0x12870u,0x12450u,0x9c68u,0x12900u,0x129c0u,0x12460u,0x9c78u,0x12a90u,0x12b50u,0x12470u,0x9c88u,0x12c60u,0x12ca0u,0x12480u,0x9c98u,0x12df0u,0x12e30u,0x12490u,0x9ca8u,0x12f40u,0x12f80u,0x124a0u,0x9cb8u,0x130d0u,0x13110u,0x124b0u,0x9cc8u,0x13220u,0x132e0u,0x124c0u,0x9cd8u,0x133b0u,0x13470u,0x124d0u,0x9ce8u,0x13500u,0x135c0u,0x124e0u,0x9cf8u,0x13690u,0x13750u,0x124f0u,0x9f08u,0x12070u,0x120a8u,0x12500u,0x9f18u,0x121e0u,0x12238u,0x12510u,0x9f28u,0x12350u,0x12388u,0x12520u,0x9f38u,0x124c0u,0x12518u,0x12530u,0x9f48u,0x12630u,0x126e8u,0x12540u,0x9f58u,0x127a0u,0x12878u,0x12550u,0x9f68u,0x12910u,0x129c8u,0x12560u,0x9f78u,0x12a80u,0x12b58u,0x12570u,0x9f88u,0x12c70u,0x12ca8u,0x12580u,0x9f98u,0x12de0u,0x12e38u,0x12590u,0x9fa8u,0x12f50u,0x12f88u,0x125a0u,0x9fb8u,0x130c0u,0x13118u,0x125b0u,0x9fc8u,0x13230u,0x132e8u,0x125c0u,0x9fd8u,0x133a0u,0x13478u,0x125d0u,0x9fe8u,0x13510u,0x135c8u,0x125e0u,0x9ff8u,0x13680u,0x13758u,0x125f0u,0xa208u,0x12080u,0x120b0u,0x12600u,0xa218u,0x12210u,0x12220u,0x12610u,0xa228u,0x123a0u,0x12390u,0x12620u,0xa238u,0x12530u,0x12500u,0x12630u,0xa248u,0x126c0u,0x126f0u,0x12640u,0xa258u,0x12850u,0x12860u,0x12650u,0xa268u,0x129e0u,0x129d0u,0x12660u,0xa278u,0x12b70u,0x12b40u,0x12670u,0xa288u,0x12c80u,0x12cb0u,0x12680u,0xa298u,0x12e10u,0x12e20u,0x12690u,0xa2a8u,0x12fa0u,0x12f90u,0x126a0u,0xa2b8u,0x13130u,0x13100u,0x126b0u,0xa2c8u,0x132c0u,0x132f0u,0x126c0u,0xa2d8u,0x13450u,0x13460u,0x126d0u,0xa2e8u,0x135e0u,0x135d0u,0x126e0u,0xa2f8u,0x13770u,0x13740u,0x126f0u,0xa508u,0x12090u,0x120b8u,0x12700u,0xa518u,0x12200u,0x12228u,0x12710u,0xa528u,0x123b0u,0x12398u,0x12720u,0xa538u,0x12520u,0x12508u,0x12730u,0xa548u,0x126d0u,0x126f8u,0x12740u,0xa558u,0x12840u,0x12868u,0x12750u,0xa568u,0x129f0u,0x129d8u,0x12760u,0xa578u,0x12b60u,0x12b48u,0x12770u,0xa588u,0x12c90u,0x12cb8u,0x12780u,0xa598u,0x12e00u,0x12e28u,0x12790u,0xa5a8u,0x12fb0u,0x12f98u,0x127a0u,0xa5b8u,0x13120u,0x13108u,0x127b0u,0xa5c8u,0x132d0u,0x132f8u,0x127c0u,0xa5d8u,0x13440u,0x13468u,0x127d0u,0xa5e8u,0x135f0u,0x135d8u,0x127e0u,0xa5f8u,0x13760u,0x13748u,0x127f0u,0x5000u,0x1c300u,0x1e8c0u,0x1eb60u,0x5080u,0x1c310u,0x100000u,0x1eb62u,0x5110u,0x1c320u,0x100000u,0x1eb64u,0x5190u,0x1c330u,0x100000u,0x1eb66u,0x5220u,0x1c340u,0x1e8c0u,0x1eb68u,0x52a0u,0x1c350u,0x100000u,0x1eb6au,0x5330u,0x1c360u,0x100000u,0x1eb6cu,0x53b0u,0x1c370u,0x100000u,0x1eb6eu,0x5440u,0x1c380u,0x1e8c0u,0x1eb70u,0x54c0u,0x1c390u,0x100000u,0x1eb72u,0x5550u,0x1c3a0u,0x100000u,0x1eb74u,0x55d0u,0x1c3b0u,0x100000u,0x1eb76u,0x5660u,0x1c3c0u,0x1e8c0u,0x1eb78u,0x56e0u,0x1c3d0u,0x100000u,0x1eb7au,0x5770u,0x1c3e0u,0x100000u,0x1eb7cu,0x57f0u,0x1c3f0u,0x100000u,0x1eb7eu,0x5010u,0x1c400u,0x1e8f0u,0x1eb60u,0x5090u,0x1c410u,0x100000u,0x1eb62u,0x5100u,0x1c420u,0x100000u,0x1eb64u,0x5180u,0x1c430u,0x100000u,0x1eb66u,0x5230u,0x1c440u,0x1e8f0u,0x1eb68u,0x52b0u,0x1c450u,0x100000u,0x1eb6au,0x5320u,0x1c460u,0x100000u,0x1eb6cu,0x53a0u,0x1c470u,0x100000u,0x1eb6eu,0x5450u,0x1c480u,0x1e8f0u,0x1eb70u,0x54d0u,0x1c490u,0x100000u,0x1eb72u,0x5540u,0x1c4a0u,0x100000u,0x1eb74u,0x55c0u,0x1c4b0u,0x100000u,0x1eb76u,0x5670u,0x1c4c0u,0x1e8f0u,0x1eb78u,0x56f0u,0x1c4d0u,0x100000u,0x1eb7au,0x5760u,0x1c4e0u,0x100000u,0x1eb7cu,0x57e0u,0x1c4f0u,0x100000u,0x1eb7eu,0x5020u,0x1c500u,0x1e920u,0x1eb60u,0x50a0u,0x1c510u,0x100000u,0x1eb62u,0x5130u,0x1c520u,0x100000u,0x1eb64u,0x51b0u,0x1c530u,0x100000u,0x1eb66u,0x5200u,0x1c540u,0x1e920u,0x1eb68u,0x5280u,0x1c550u,0x100000u,0x1eb6au,0x5310u,0x1c560u,0x100000u,0x1eb6cu,0x5390u,0x1c570u,0x100000u,0x1eb6eu,0x5460u,0x1c580u,0x1e920u,0x1eb70u,0x54e0u,0x1c590u,0x100000u,0x1eb72u,0x5570u,0x1c5a0u,0x100000u,0x1eb74u,0x55f0u,0x1c5b0u,0x100000u,0x1eb76u,0x5640u,0x1c5c0u,0x1e920u,0x1eb78u,0x56c0u,0x1c5d0u,0x100000u,0x1eb7au,0x5750u,0x1c5e0u,0x100000u,0x1eb7cu,0x57d0u,0x1c5f0u,0x100000u,0x1eb7eu,0x5030u,0x1c600u,0x1e950u,0x1eb60u,0x50b0u,0x1c610u,0x100000u,0x1eb62u,0x5120u,0x1c620u,0x100000u,0x1eb64u,0x51a0u,0x1c630u,0x100000u,0x1eb66u,0x5210u,0x1c640u,0x1e950u,0x1eb68u,0x5290u,0x1c650u,0x100000u,0x1eb6au,0x5300u,0x1c660u,0x100000u,0x1eb6cu,0x5380u,0x1c670u,0x100000u,0x1eb6eu,0x5470u,0x1c680u,0x1e950u,0x1eb70u,0x54f0u,0x1c690u,0x100000u,0x1eb72u,0x5560u,0x1c6a0u,0x100000u,0x1eb74u,0x55e0u,0x1c6b0u,0x100000u,0x1eb76u,0x5650u,0x1c6c0u,0x1e950u,0x1eb78u,0x56d0u,0x1c6d0u,0x100000u,0x1eb7au,0x5740u,0x1c6e0u,0x100000u,0x1eb7cu,0x57c0u,0x1c6f0u,0x100000u,0x1eb7eu,0x25d00u,0x1d380u,0x1d380u,0xf0140u,0x25d02u,0x1d382u,0x100000u,0xf0150u,0x25d04u,0x1d384u,0x100000u,0xf0160u,0x25d06u,0x1d386u,0x100000u,0xf0170u,0x25f00u,0x1d390u,0x1d380u,0xf0100u,0x25f02u,0x1d392u,0x100000u,0xf0110u,0x25f04u,0x1d394u,0x100000u,0xf0120u,0x25f06u,0x1d396u,0x100000u,0xf0130u,0x26100u,0x1d3a0u,0x1d380u,0xf02c0u,0x26102u,0x1d3a2u,0x100000u,0xf02d0u,0x26104u,0x1d3a4u,0x100000u,0xf02e0u,0x26106u,0x1d3a6u,0x100000u,0xf02f0u,0x26300u,0x1d3b0u,0x1d380u,0xf0280u,0x26302u,0x1d3b2u,0x100000u,0xf0290u,0x26304u,0x1d3b4u,0x100000u,0xf02a0u,0x26306u,0x1d3b6u,0x100000u,0xf02b0u,0x25d08u,0x1d388u,0x1d390u,0x11f850u,0x25d0au,0x1d38au,0x100000u,0x11f840u,0x25d0cu,0x1d38cu,0x100000u,0x11f870u,0x25d0eu,0x1d38eu,0x100000u,0x11f860u,0x25f08u,0x1d398u,0x1d390u,0x11f810u,0x25f0au,0x1d39au,0x100000u,0x11f800u,0x25f0cu,0x1d39cu,0x100000u,0x11f830u,0x25f0eu,0x1d39eu,0x100000u,0x11f820u,0x26108u,0x1d3a8u,0x1d390u,0x11f9d0u,0x2610au,0x1d3aau,0x100000u,0x11f9c0u,0x2610cu,0x1d3acu,0x100000u,0x11f9f0u,0x2610eu,0x1d3aeu,0x100000u,0x11f9e0u,0x26308u,0x1d3b8u,0x1d390u,0x11f990u,0x2630au,0x1d3bau,0x100000u,0x11f980u,0x2630cu,0x1d3bcu,0x100000u,0x11f9b0u,0x2630eu,0x1d3beu,0x100000u,0x11f9a0u,0x25508u,0x1d3c0u,0x1d3a0u,0x11fb60u,0x2550au,0x1d3c2u,0x100000u,0x11fb70u,0x2550cu,0x1d3c4u,0x100000u,0x11fb40u,0x2550eu,0x1d3c6u,0x100000u,0x11fb50u,0x25708u,0x1d3d0u,0x1d3a0u,0x11fb20u,0x2570au,0x1d3d2u,0x100000u,0x11fb30u,0x2570cu,0x1d3d4u,0x100000u,0x11fb00u,0x2570eu,0x1d3d6u,0x100000u,0x11fb10u,0x25908u,0x1d3e0u,0x1d3a0u,0x11fce0u,0x2590au,0x1d3e2u,0x100000u,0x11fcf0u,0x2590cu,0x1d3e4u,0x100000u,0x11fcc0u,0x2590eu,0x1d3e6u,0x100000u,0x11fcd0u,0x25b08u,0x1d3f0u,0x1d3a0u,0x11fca0u,0x25b0au,0x1d3f2u,0x100000u,0x11fcb0u,0x25b0cu,0x1d3f4u,0x100000u,0x11fc80u,0x25b0eu,0x1d3f6u,0x100000u,0x11fc90u,0x100000u,0x1d3c8u,0x1d3b0u,0x11fe70u,0x100000u,0x1d3cau,0x100000u,0x11fe60u,0x100000u,0x1d3ccu,0x100000u,0x11fe50u,0x100000u,0x1d3ceu,0x100000u,0x11fe40u,0x100000u,0x1d3d8u,0x1d3b0u,0x11fe30u,0x100000u,0x1d3dau,0x100000u,0x11fe20u,0x100000u,0x1d3dcu,0x100000u,0x11fe10u,0x100000u,0x1d3deu,0x100000u,0x11fe00u,0x100000u,0x1d3e8u,0x1d3b0u,0x11fff0u,0x100000u,0x1d3eau,0x100000u,0x11ffe0u,0x100000u,0x1d3ecu,0x100000u,0x11ffd0u,0x100000u,0x1d3eeu,0x100000u,0x11ffc0u,0x100000u,0x1d3f8u,0x1d3b0u,0x11ffb0u,0x100000u,0x1d3fau,0x100000u,0x11ffa0u,0x100000u,0x1d3fcu,0x100000u,0x11ff90u,0x100000u,0x1d3feu,0x100000u,0x11ff80u,0x420u,0x30850u,0x90000u,0xc0420u,0x430u,0x30840u,0x90010u,0xc0430u,0x400u,0x30870u,0x90020u,0xc0400u,0x410u,0x30860u,0x90030u,0xc0410u,0x460u,0x30810u,0x90040u,0xc0460u,0x470u,0x30800u,0x90050u,0xc0470u,0x440u,0x30830u,0x90060u,0xc0440u,0x450u,0x30820u,0x90070u,0xc0450u,0x4b0u,0x308d0u,0x90080u,0xef8b0u,0x4a0u,0x308c0u,0x90090u,0xef8a0u,0x490u,0x308f0u,0x900a0u,0xef890u,0x480u,0x308e0u,0x900b0u,0xef880u,0x4f0u,0x30890u,0x900c0u,0xef8f0u,0x4e0u,0x30880u,0x900d0u,0xef8e0u,0x4d0u,0x308b0u,0x900e0u,0xef8d0u,0x4c0u,0x308a0u,0x900f0u,0xef8c0u,0x530u,0x5fd60u,0x90100u,0xef930u,0x520u,0x5fd70u,0x90110u,0xef920u,0x510u,0x5fd40u,0x90120u,0xef910u,0x500u,0x5fd50u,0x90130u,0xef900u,0x570u,0x5fd20u,0x90140u,0xef970u,0x560u,0x5fd30u,0x90150u,0xef960u,0x550u,0x5fd00u,0x90160u,0xef950u,0x540u,0x5fd10u,0x90170u,0xef940u,0x5b0u,0x5fde0u,0x90190u,0xef9b0u,0x5a0u,0x5fdf0u,0x90180u,0xef9a0u,0x590u,0x5fdc0u,0x901b0u,0xef990u,0x580u,0x5fdd0u,0x901a0u,0xef980u,0x5f0u,0x5fda0u,0x901d0u,0xef9f0u,0x5e0u,0x5fdb0u,0x901c0u,0xef9e0u,0x5d0u,0x5fd80u,0x901f0u,0xef9d0u,0x5c0u,0x5fd90u,0x901e0u,0xef9c0u,0x640u,0x5fe60u,0x90210u,0xefa40u,0x650u,0x5fe70u,0x90200u,0xefa50u,0x660u,0x5fe40u,0x90230u,0xefa60u,0x670u,0x5fe50u,0x90220u,0xefa70u,0x600u,0x5fe20u,0x90250u,0xefa00u,0x610u,0x5fe30u,0x90240u,0xefa10u,0x620u,0x5fe00u,0x90270u,0xefa20u,0x630u,0x5fe10u,0x90260u,0xefa30u,0x6c0u,0x5fef0u,0x90290u,0xefac0u,0x6d0u,0x5fee0u,0x90280u,0xefad0u,0x6e0u,0x5fed0u,0x902b0u,0xefae0u,0x6f0u,0x5fec0u,0x902a0u,0xefaf0u,0x680u,0x5feb0u,0x902d0u,0xefa80u,0x690u,0x5fea0u,0x902c0u,0xefa90u,0x6a0u,0x5fe90u,0x902f0u,0xefaa0u,0x6b0u,0x5fe80u,0x902e0u,0xefab0u,0x740u,0x5ff70u,0x90320u,0xefb40u,0x750u,0x5ff60u,0x90330u,0xefb50u,0x760u,0x5ff50u,0x90300u,0xefb60u,0x770u,0x5ff40u,0x90310u,0xefb70u,0x700u,0x5ff30u,0x90360u,0xefb00u,0x710u,0x5ff20u,0x90370u,0xefb10u,0x720u,0x5ff10u,0x90340u,0xefb20u,0x730u,0x5ff00u,0x90350u,0xefb30u,0x7d0u,0x5fff0u,0x903a0u,0xefbd0u,0x7c0u,0x5ffe0u,0x903b0u,0xefbc0u,0x7f0u,0x5ffd0u,0x90380u,0xefbf0u,0x7e0u,0x5ffc0u,0x90390u,0xefbe0u,0x790u,0x5ffb0u,0x903e0u,0xefb90u,0x780u,0x5ffa0u,0x903f0u,0xefb80u,0x7b0u,0x5ff90u,0x903c0u,0xefbb0u,0x7a0u,0x5ff80u,0x903d0u,0xefba0u,0x0u,0x11fc50u,0x0u,0x7230cu,0x0u,0x11fc40u,0x0u,0x7230cu,0x0u,0x11fc70u,0x0u,0x7230cu,0x0u,0x11fc60u,0x0u,0x7230cu,0x0u,0x11fc10u,0x0u,0x7230cu,0x0u,0x11fc00u,0x0u,0x7230cu,0x0u,0x11fc30u,0x0u,0x7230cu,0x0u,0x11fc20u,0x0u,0x7230cu,0x0u,0x11fcd0u,0x0u,0x5232cu,0x0u,0x11fcc0u,0x0u,0x5232cu,0x0u,0x11fcf0u,0x0u,0x5232cu,0x0u,0x11fce0u,0x0u,0x5232cu,0x0u,0x11fc90u,0x0u,0x5232cu,0x0u,0x11fc80u,0x0u,0x5232cu,0x0u,0x11fcb0u,0x0u,0x5232cu,0x0u,0x11fca0u,0x0u,0x5232cu,0x0u,0x11fd60u,0x0u,0xd0124u,0x0u,0x11fd70u,0x0u,0xd0124u,0x0u,0x11fd40u,0x0u,0xd0124u,0x0u,0x11fd50u,0x0u,0xd0124u,0x0u,0x11fd20u,0x0u,0xd0124u,0x0u,0x11fd30u,0x0u,0xd0124u,0x0u,0x11fd00u,0x0u,0xd0124u,0x0u,0x11fd10u,0x0u,0xd0124u,0x0u,0x11fde0u,0x0u,0xd81a6u,0x0u,0x11fdf0u,0x0u,0xd81a6u,0x0u,0x11fdc0u,0x0u,0xd81a6u,0x0u,0x11fdd0u,0x0u,0xd81a6u,0x0u,0x11fda0u,0x0u,0xd81a6u,0x0u,0x11fdb0u,0x0u,0xd81a6u,0x0u,0x11fd80u,0x0u,0xd81a6u,0x0u,0x11fd90u,0x0u,0xd81a6u,0x0u,0x11fe60u,0x0u,0xd8996u,0x0u,0x11fe70u,0x0u,0xd8996u,0x0u,0x11fe40u,0x0u,0xd8996u,0x0u,0x11fe50u,0x0u,0xd8996u,0x0u,0x11fe20u,0x0u,0xd8996u,0x0u,0x11fe30u,0x0u,0xd8996u,0x0u,0x11fe00u,0x0u,0xd8996u,0x0u,0x11fe10u,0x0u,0xd8996u,0x0u,0x11fef0u,0x0u,0xd8996u,0x0u,0x11fee0u,0x0u,0xd8996u,0x0u,0x11fed0u,0x0u,0xd8996u,0x0u,0x11fec0u,0x0u,0xd8996u,0x0u,0x11feb0u,0x0u,0xd8996u,0x0u,0x11fea0u,0x0u,0xd8996u,0x0u,0x11fe90u,0x0u,0xd8996u,0x0u,0x11fe80u,0x0u,0xd8996u,0x0u,0x11ff70u,0x0u,0xd8b1cu,0x0u,0x11ff60u,0x0u,0xd8b1cu,0x0u,0x11ff50u,0x0u,0xd8b1cu,0x0u,0x11ff40u,0x0u,0xd8b1cu,0x0u,0x11ff30u,0x0u,0xd8b1cu,0x0u,0x11ff20u,0x0u,0xd8b1cu,0x0u,0x11ff10u,0x0u,0xd8b1cu,0x0u,0x11ff00u,0x0u,0xd8b1cu,0x0u,0x11fff0u,0x0u,0xd8b1cu,0x0u,0x11ffe0u,0x0u,0xd8b1cu,0x0u,0x11ffd0u,0x0u,0xd8b1cu,0x0u,0x11ffc0u,0x0u,0xd8b1cu,0x0u,0x11ffb0u,0x0u,0xd8b1cu,0x0u,0x11ffa0u,0x0u,0xd8b1cu,0x0u,0x11ff90u,0x0u,0xd8b1cu,0x0u,0x11ff80u,0x0u,0xd8b1cu,0x5840u,0x5860u,0x6800u,0x5840u,0x5848u,0x5868u,0x6808u,0x5850u,0x5850u,0x5870u,0x6810u,0x5860u,0x5858u,0x5878u,0x6818u,0x5870u,0x58c0u,0x58e0u,0x6880u,0x58c0u,0x58c8u,0x58e8u,0x6888u,0x58d0u,0x58d0u,0x58f0u,0x6890u,0x58e0u,0x58d8u,0x58f8u,0x6898u,0x58f0u,0x5960u,0x5940u,0x6920u,0x5960u,0x5968u,0x5948u,0x6928u,0x5970u,0x5970u,0x5950u,0x6930u,0x5940u,0x5978u,0x5958u,0x6938u,0x5950u,0x59e0u,0x59c0u,0x69a0u,0x59e0u,0x59e8u,0x59c8u,0x69a8u,0x59f0u,0x59f0u,0x59d0u,0x69b0u,0x59c0u,0x59f8u,0x59d8u,0x69b8u,0x59d0u,0x5c00u,0x5c20u,0x6c40u,0x5a40u,0x5c08u,0x5c28u,0x6c48u,0x5a50u,0x5c10u,0x5c30u,0x6c50u,0x5a60u,0x5c18u,0x5c38u,0x6c58u,0x5a70u,0x5c80u,0x5ca0u,0x6cc0u,0x5ac0u,0x5c88u,0x5ca8u,0x6cc8u,0x5ad0u,0x5c90u,0x5cb0u,0x6cd0u,0x5ae0u,0x5c98u,0x5cb8u,0x6cd8u,0x5af0u,0x5d20u,0x5d00u,0x6d60u,0x5b60u,0x5d28u,0x5d08u,0x6d68u,0x5b70u,0x5d30u,0x5d10u,0x6d70u,0x5b40u,0x5d38u,0x5d18u,0x6d78u,0x5b50u,0x5da0u,0x5d80u,0x6de0u,0x5be0u,0x5da8u,0x5d88u,0x6de8u,0x5bf0u,0x5db0u,0x5d90u,0x6df0u,0x5bc0u,0x5db8u,0x5d98u,0x6df8u,0x5bd0u,0x6040u,0x6060u,0x7000u,0x5c00u,0x6048u,0x6068u,0x7008u,0x5c10u,0x6050u,0x6070u,0x7010u,0x5c20u,0x6058u,0x6078u,0x7018u,0x5c30u,0x60c0u,0x60e0u,0x7080u,0x5c80u,0x60c8u,0x60e8u,0x7088u,0x5c90u,0x60d0u,0x60f0u,0x7090u,0x5ca0u,0x60d8u,0x60f8u,0x7098u,0x5cb0u,0x6160u,0x6140u,0x7120u,0x5d20u,0x6168u,0x6148u,0x7128u,0x5d30u,0x6170u,0x6150u,0x7130u,0x5d00u,0x6178u,0x6158u,0x7138u,0x5d10u,0x61e0u,0x61c0u,0x71a0u,0x5da0u,0x61e8u,0x61c8u,0x71a8u,0x5db0u,0x61f0u,0x61d0u,0x71b0u,0x5d80u,0x61f8u,0x61d8u,0x71b8u,0x5d90u,0x6400u,0x6420u,0x7440u,0x5e00u,0x6408u,0x6428u,0x7448u,0x5e10u,0x6410u,0x6430u,0x7450u,0x5e20u,0x6418u,0x6438u,0x7458u,0x5e30u,0x6480u,0x64a0u,0x74c0u,0x5e80u,0x6488u,0x64a8u,0x74c8u,0x5e90u,0x6490u,0x64b0u,0x74d0u,0x5ea0u,0x6498u,0x64b8u,0x74d8u,0x5eb0u,0x6520u,0x6500u,0x7560u,0x5f20u,0x6528u,0x6508u,0x7568u,0x5f30u,0x6530u,0x6510u,0x7570u,0x5f00u,0x6538u,0x6518u,0x7578u,0x5f10u,0x65a0u,0x6580u,0x75e0u,0x5fa0u,0x65a8u,0x6588u,0x75e8u,0x5fb0u,0x65b0u,0x6590u,0x75f0u,0x5f80u,0x65b8u,0x6598u,0x75f8u,0x5f90u,0x6800u,0x1e980u,0x1ebc0u,0x1800u,0x6808u,0x1ee10u,0x1f050u,0x1810u,0x6810u,0x1f2a0u,0x1f4e0u,0x1820u,0x6818u,0x1f730u,0x1f970u,0x1830u,0x6880u,0x1fbc0u,0x1fe00u,0x1840u,0x6888u,0x20050u,0x20290u,0x1850u,0x6890u,0x204e0u,0x20720u,0x1860u,0x6898u,0x20970u,0x20bb0u,0x1870u,0x6920u,0x20e00u,0x21040u,0x1980u,0x6928u,0x21290u,0x214d0u,0x1990u,0x6930u,0x21720u,0x21960u,0x19a0u,0x6938u,0x21bb0u,0x21df0u,0x19b0u,0x69a0u,0x22040u,0x22280u,0x19c0u,0x69a8u,0x224d0u,0x22710u,0x19d0u,0x69b0u,0x22960u,0x22ba0u,0x19e0u,0x69b8u,0x22df0u,0x23030u,0x19f0u,0x6a00u,0x1e9b0u,0x1ebc8u,0x1b20u,0x6a08u,0x1ee40u,0x1f058u,0x1b30u,0x6a10u,0x1f2d0u,0x1f4e8u,0x1b00u,0x6a18u,0x1f760u,0x1f978u,0x1b10u,0x6a80u,0x1fbf0u,0x1fe08u,0x1b60u,0x6a88u,0x20080u,0x20298u,0x1b70u,0x6a90u,0x20510u,0x20728u,0x1b40u,0x6a98u,0x209a0u,0x20bb8u,0x1b50u,0x6b20u,0x20e30u,0x21048u,0x1ca0u,0x6b28u,0x212c0u,0x214d8u,0x1cb0u,0x6b30u,0x21750u,0x21968u,0x1c80u,0x6b38u,0x21be0u,0x21df8u,0x1c90u,0x6ba0u,0x22070u,0x22288u,0x1ce0u,0x6ba8u,0x22500u,0x22718u,0x1cf0u,0x6bb0u,0x22990u,0x22ba8u,0x1cc0u,0x6bb8u,0x22e20u,0x23038u,0x1cd0u,0x6c40u,0x1e9e0u,0x1ebd0u,0x1e00u,0x6c48u,0x1ee70u,0x1f060u,0x1e10u,0x6c50u,0x1f300u,0x1f4f0u,0x1e20u,0x6c58u,0x1f790u,0x1f980u,0x1e30u,0x6cc0u,0x1fc20u,0x1fe10u,0x1e40u,0x6cc8u,0x200b0u,0x202a0u,0x1e50u,0x6cd0u,0x20540u,0x20730u,0x1e60u,0x6cd8u,0x209d0u,0x20bc0u,0x1e70u,0x6d60u,0x20e60u,0x21050u,0x1f80u,0x6d68u,0x212f0u,0x214e0u,0x1f90u,0x6d70u,0x21780u,0x21970u,0x1fa0u,0x6d78u,0x21c10u,0x21e00u,0x1fb0u,0x6de0u,0x220a0u,0x22290u,0x1fc0u,0x6de8u,0x22530u,0x22720u,0x1fd0u,0x6df0u,0x229c0u,0x22bb0u,0x1fe0u,0x6df8u,0x22e50u,0x23040u,0x1ff0u,0x6e40u,0x1ea10u,0x1ebd8u,0x2120u,0x6e48u,0x1eea0u,0x1f068u,0x2130u,0x6e50u,0x1f330u,0x1f4f8u,0x2100u,0x6e58u,0x1f7c0u,0x1f988u,0x2110u,0x6ec0u,0x1fc50u,0x1fe18u,0x2160u,0x6ec8u,0x200e0u,0x202a8u,0x2170u,0x6ed0u,0x20570u,0x20738u,0x2140u,0x6ed8u,0x20a00u,0x20bc8u,0x2150u,0x6f60u,0x20e90u,0x21058u,0x22a0u,0x6f68u,0x21320u,0x214e8u,0x22b0u,0x6f70u,0x217b0u,0x21978u,0x2280u,0x6f78u,0x21c40u,0x21e08u,0x2290u,0x6fe0u,0x220d0u,0x22298u,0x22e0u,0x6fe8u,0x22560u,0x22728u,0x22f0u,0x6ff0u,0x229f0u,0x22bb8u,0x22c0u,0x6ff8u,0x22e80u,0x23048u,0x22d0u,0x0u,0x40u,0x9000u,0xb400u,0x80u,0xc0u,0x9010u,0xb410u,0x110u,0x150u,0x9020u,0xb420u,0x190u,0x1d0u,0x9030u,0xb430u,0x220u,0x260u,0x9040u,0xb440u,0x2a0u,0x2e0u,0x9050u,0xb450u,0x330u,0x370u,0x9060u,0xb460u,0x3b0u,0x3f0u,0x9070u,0xb470u,0x440u,0x400u,0x9080u,0xb480u,0x4c0u,0x480u,0x9090u,0xb490u,0x550u,0x510u,0x90a0u,0xb4a0u,0x5d0u,0x590u,0x90b0u,0xb4b0u,0x660u,0x620u,0x90c0u,0xb4c0u,0x6e0u,0x6a0u,0x90d0u,0xb4d0u,0x770u,0x730u,0x90e0u,0xb4e0u,0x7f0u,0x7b0u,0x90f0u,0xb4f0u,0x10u,0x50u,0x9300u,0xb700u,0x90u,0xd0u,0x9310u,0xb710u,0x100u,0x140u,0x9320u,0xb720u,0x180u,0x1c0u,0x9330u,0xb730u,0x230u,0x270u,0x9340u,0xb740u,0x2b0u,0x2f0u,0x9350u,0xb750u,0x320u,0x360u,0x9360u,0xb760u,0x3a0u,0x3e0u,0x9370u,0xb770u,0x450u,0x410u,0x9380u,0xb780u,0x4d0u,0x490u,0x9390u,0xb790u,0x540u,0x500u,0x93a0u,0xb7a0u,0x5c0u,0x580u,0x93b0u,0xb7b0u,0x670u,0x630u,0x93c0u,0xb7c0u,0x6f0u,0x6b0u,0x93d0u,0xb7d0u,0x760u,0x720u,0x93e0u,0xb7e0u,0x7e0u,0x7a0u,0x93f0u,0xb7f0u,0x20u,0x60u,0x9600u,0xba00u,0xa0u,0xe0u,0x9610u,0xba10u,0x130u,0x170u,0x9620u,0xba20u,0x1b0u,0x1f0u,0x9630u,0xba30u,0x200u,0x240u,0x9640u,0xba40u,0x280u,0x2c0u,0x9650u,0xba50u,0x310u,0x350u,0x9660u,0xba60u,0x390u,0x3d0u,0x9670u,0xba70u,0x460u,0x420u,0x9680u,0xba80u,0x4e0u,0x4a0u,0x9690u,0xba90u,0x570u,0x530u,0x96a0u,0xbaa0u,0x5f0u,0x5b0u,0x96b0u,0xbab0u,0x640u,0x600u,0x96c0u,0xbac0u,0x6c0u,0x680u,0x96d0u,0xbad0u,0x750u,0x710u,0x96e0u,0xbae0u,0x7d0u,0x790u,0x96f0u,0xbaf0u,0x30u,0x70u,0x9900u,0xbd00u,0xb0u,0xf0u,0x9910u,0xbd10u,0x120u,0x160u,0x9920u,0xbd20u,0x1a0u,0x1e0u,0x9930u,0xbd30u,0x210u,0x250u,0x9940u,0xbd40u,0x290u,0x2d0u,0x9950u,0xbd50u,0x300u,0x340u,0x9960u,0xbd60u,0x380u,0x3c0u,0x9970u,0xbd70u,0x470u,0x430u,0x9980u,0xbd80u,0x4f0u,0x4b0u,0x9990u,0xbd90u,0x560u,0x520u,0x99a0u,0xbda0u,0x5e0u,0x5a0u,0x99b0u,0xbdb0u,0x650u,0x610u,0x99c0u,0xbdc0u,0x6d0u,0x690u,0x99d0u,0xbdd0u,0x740u,0x700u,0x99e0u,0xbde0u,0x7c0u,0x780u,0x99f0u,0xbdf0u,0xc000u,0x120c0u,0x12100u,0x12800u,0xc010u,0x12250u,0x12290u,0x12810u,0xc020u,0x123e0u,0x12420u,0x12820u,0xc030u,0x12570u,0x125b0u,0x12830u,0xc040u,0x12680u,0x12740u,0x12840u,0xc050u,0x12810u,0x128d0u,0x12850u,0xc060u,0x129a0u,0x12a60u,0x12860u,0xc070u,0x12b30u,0x12bf0u,0x12870u,0xc080u,0x12cc0u,0x12d00u,0x12880u,0xc090u,0x12e50u,0x12e90u,0x12890u,0xc0a0u,0x12fe0u,0x13020u,0x128a0u,0xc0b0u,0x13170u,0x131b0u,0x128b0u,0xc0c0u,0x13280u,0x13340u,0x128c0u,0xc0d0u,0x13410u,0x134d0u,0x128d0u,0xc0e0u,0x135a0u,0x13660u,0x128e0u,0xc0f0u,0x13730u,0x137f0u,0x128f0u,0xc300u,0x120d0u,0x12108u,0x12900u,0xc310u,0x12240u,0x12298u,0x12910u,0xc320u,0x123f0u,0x12428u,0x12920u,0xc330u,0x12560u,0x125b8u,0x12930u,0xc340u,0x12690u,0x12748u,0x12940u,0xc350u,0x12800u,0x128d8u,0x12950u,0xc360u,0x129b0u,0x12a68u,0x12960u,0xc370u,0x12b20u,0x12bf8u,0x12970u,0xc380u,0x12cd0u,0x12d08u,0x12980u,0xc390u,0x12e40u,0x12e98u,0x12990u,0xc3a0u,0x12ff0u,0x13028u,0x129a0u,0xc3b0u,0x13160u,0x131b8u,0x129b0u,0xc3c0u,0x13290u,0x13348u,0x129c0u,0xc3d0u,0x13400u,0x134d8u,0x129d0u,0xc3e0u,0x135b0u,0x13668u,0x129e0u,0xc3f0u,0x13720u,0x137f8u,0x129f0u,0xc600u,0x120e0u,0x12110u,0x12a00u,0xc610u,0x12270u,0x12280u,0x12a10u,0xc620u,0x123c0u,0x12430u,0x12a20u,0xc630u,0x12550u,0x125a0u,0x12a30u,0xc640u,0x126a0u,0x12750u,0x12a40u,0xc650u,0x12830u,0x128c0u,0x12a50u,0xc660u,0x12980u,0x12a70u,0x12a60u,0xc670u,0x12b10u,0x12be0u,0x12a70u,0xc680u,0x12ce0u,0x12d10u,0x12a80u,0xc690u,0x12e70u,0x12e80u,0x12a90u,0xc6a0u,0x12fc0u,0x13030u,0x12aa0u,0xc6b0u,0x13150u,0x131a0u,0x12ab0u,0xc6c0u,0x132a0u,0x13350u,0x12ac0u,0xc6d0u,0x13430u,0x134c0u,0x12ad0u,0xc6e0u,0x13580u,0x13670u,0x12ae0u,0xc6f0u,0x13710u,0x137e0u,0x12af0u,0xc900u,0x120f0u,0x12118u,0x12b00u,0xc910u,0x12260u,0x12288u,0x12b10u,0xc920u,0x123d0u,0x12438u,0x12b20u,0xc930u,0x12540u,0x125a8u,0x12b30u,0xc940u,0x126b0u,0x12758u,0x12b40u,0xc950u,0x12820u,0x128c8u,0x12b50u,0xc960u,0x12990u,0x12a78u,0x12b60u,0xc970u,0x12b00u,0x12be8u,0x12b70u,0xc980u,0x12cf0u,0x12d18u,0x12b80u,0xc990u,0x12e60u,0x12e88u,0x12b90u,0xc9a0u,0x12fd0u,0x13038u,0x12ba0u,0xc9b0u,0x13140u,0x131a8u,0x12bb0u,0xc9c0u,0x132b0u,0x13358u,0x12bc0u,0xc9d0u,0x13420u,0x134c8u,0x12bd0u,0xc9e0u,0x13590u,0x13678u,0x12be0u,0xc9f0u,0x13700u,0x137e8u,0x12bf0u,0x7800u,0x1c300u,0x1e980u,0x1ebc0u,0x7880u,0x1c310u,0x100000u,0x1ebc2u,0x7910u,0x1c320u,0x100000u,0x1ebc4u,0x7990u,0x1c330u,0x100000u,0x1ebc6u,0x7a20u,0x1c340u,0x1e980u,0x1ebc8u,0x7aa0u,0x1c350u,0x100000u,0x1ebcau,0x7b30u,0x1c360u,0x100000u,0x1ebccu,0x7bb0u,0x1c370u,0x100000u,0x1ebceu,0x7c40u,0x1c380u,0x1e980u,0x1ebd0u,0x7cc0u,0x1c390u,0x100000u,0x1ebd2u,0x7d50u,0x1c3a0u,0x100000u,0x1ebd4u,0x7dd0u,0x1c3b0u,0x100000u,0x1ebd6u,0x7e60u,0x1c3c0u,0x1e980u,0x1ebd8u,0x7ee0u,0x1c3d0u,0x100000u,0x1ebdau,0x7f70u,0x1c3e0u,0x100000u,0x1ebdcu,0x7ff0u,0x1c3f0u,0x100000u,0x1ebdeu,0x7810u,0x1c400u,0x1e9b0u,0x1ebc0u,0x7890u,0x1c410u,0x100000u,0x1ebc2u,0x7900u,0x1c420u,0x100000u,0x1ebc4u,0x7980u,0x1c430u,0x100000u,0x1ebc6u,0x7a30u,0x1c440u,0x1e9b0u,0x1ebc8u,0x7ab0u,0x1c450u,0x100000u,0x1ebcau,0x7b20u,0x1c460u,0x100000u,0x1ebccu,0x7ba0u,0x1c470u,0x100000u,0x1ebceu,0x7c50u,0x1c480u,0x1e9b0u,0x1ebd0u,0x7cd0u,0x1c490u,0x100000u,0x1ebd2u,0x7d40u,0x1c4a0u,0x100000u,0x1ebd4u,0x7dc0u,0x1c4b0u,0x100000u,0x1ebd6u,0x7e70u,0x1c4c0u,0x1e9b0u,0x1ebd8u,0x7ef0u,0x1c4d0u,0x100000u,0x1ebdau,0x7f60u,0x1c4e0u,0x100000u,0x1ebdcu,0x7fe0u,0x1c4f0u,0x100000u,0x1ebdeu,0x7820u,0x1c500u,0x1e9e0u,0x1ebc0u,0x78a0u,0x1c510u,0x100000u,0x1ebc2u,0x7930u,0x1c520u,0x100000u,0x1ebc4u,0x79b0u,0x1c530u,0x100000u,0x1ebc6u,0x7a00u,0x1c540u,0x1e9e0u,0x1ebc8u,0x7a80u,0x1c550u,0x100000u,0x1ebcau,0x7b10u,0x1c560u,0x100000u,0x1ebccu,0x7b90u,0x1c570u,0x100000u,0x1ebceu,0x7c60u,0x1c580u,0x1e9e0u,0x1ebd0u,0x7ce0u,0x1c590u,0x100000u,0x1ebd2u,0x7d70u,0x1c5a0u,0x100000u,0x1ebd4u,0x7df0u,0x1c5b0u,0x100000u,0x1ebd6u,0x7e40u,0x1c5c0u,0x1e9e0u,0x1ebd8u,0x7ec0u,0x1c5d0u,0x100000u,0x1ebdau,0x7f50u,0x1c5e0u,0x100000u,0x1ebdcu,0x7fd0u,0x1c5f0u,0x100000u,0x1ebdeu,0x7830u,0x1c600u,0x1ea10u,0x1ebc0u,0x78b0u,0x1c610u,0x100000u,0x1ebc2u,0x7920u,0x1c620u,0x100000u,0x1ebc4u,0x79a0u,0x1c630u,0x100000u,0x1ebc6u,0x7a10u,0x1c640u,0x1ea10u,0x1ebc8u,0x7a90u,0x1c650u,0x100000u,0x1ebcau,0x7b00u,0x1c660u,0x100000u,0x1ebccu,0x7b80u,0x1c670u,0x100000u,0x1ebceu,0x7c70u,0x1c680u,0x1ea10u,0x1ebd0u,0x7cf0u,0x1c690u,0x100000u,0x1ebd2u,0x7d60u,0x1c6a0u,0x100000u,0x1ebd4u,0x7de0u,0x1c6b0u,0x100000u,0x1ebd6u,0x7e50u,0x1c6c0u,0x1ea10u,0x1ebd8u,0x7ed0u,0x1c6d0u,0x100000u,0x1ebdau,0x7f40u,0x1c6e0u,0x100000u,0x1ebdcu,0x7fc0u,0x1c6f0u,0x100000u,0x1ebdeu,0x26500u,0x1d400u,0x1d400u,0x80000000u,0x26502u,0x1d402u,0x100000u,0x80000000u,0x26504u,0x1d404u,0x100000u,0x80000000u,0x26506u,0x1d406u,0x100000u,0x80000000u,0x26700u,0x1d410u,0x1d400u,0x80000000u,0x26702u,0x1d412u,0x100000u,0x80000000u,0x26704u,0x1d414u,0x100000u,0x80000000u,0x26706u,0x1d416u,0x100000u,0x80000000u,0x26900u,0x1d420u,0x1d400u,0x80000000u,0x26902u,0x1d422u,0x100000u,0x80000000u,0x26904u,0x1d424u,0x100000u,0x80000000u,0x26906u,0x1d426u,0x100000u,0x80000000u,0x26b00u,0x1d430u,0x1d400u,0x80000000u,0x26b02u,0x1d432u,0x100000u,0x80000000u,0x26b04u,0x1d434u,0x100000u,0x80000000u,0x26b06u,0x1d436u,0x100000u,0x80000000u,0x26508u,0x1d408u,0x1d410u,0x80000000u,0x2650au,0x1d40au,0x100000u,0x80000000u,0x2650cu,0x1d40cu,0x100000u,0x80000000u,0x2650eu,0x1d40eu,0x100000u,0x80000000u,0x26708u,0x1d418u,0x1d410u,0x80000000u,0x2670au,0x1d41au,0x100000u,0x80000000u,0x2670cu,0x1d41cu,0x100000u,0x80000000u,0x2670eu,0x1d41eu,0x100000u,0x80000000u,0x26908u,0x1d428u,0x1d410u,0x80000000u,0x2690au,0x1d42au,0x100000u,0x80000000u,0x2690cu,0x1d42cu,0x100000u,0x80000000u,0x2690eu,0x1d42eu,0x100000u,0x80000000u,0x26b08u,0x1d438u,0x1d410u,0x80000000u,0x26b0au,0x1d43au,0x100000u,0x80000000u,0x26b0cu,0x1d43cu,0x100000u,0x80000000u,0x26b0eu,0x1d43eu,0x100000u,0x80000000u,0x26d00u,0x1d440u,0x1d420u,0x80000000u,0x26d02u,0x1d442u,0x100000u,0x80000000u,0x26d04u,0x1d444u,0x100000u,0x80000000u,0x26d06u,0x1d446u,0x100000u,0x80000000u,0x26f00u,0x1d450u,0x1d420u,0x80000000u,0x26f02u,0x1d452u,0x100000u,0x80000000u,0x26f04u,0x1d454u,0x100000u,0x80000000u,0x26f06u,0x1d456u,0x100000u,0x80000000u,0x27100u,0x1d460u,0x1d420u,0x80000000u,0x27102u,0x1d462u,0x100000u,0x80000000u,0x27104u,0x1d464u,0x100000u,0x80000000u,0x27106u,0x1d466u,0x100000u,0x80000000u,0x27300u,0x1d470u,0x1d420u,0x80000000u,0x27302u,0x1d472u,0x100000u,0x80000000u,0x27304u,0x1d474u,0x100000u,0x80000000u,0x27306u,0x1d476u,0x100000u,0x80000000u,0x100000u,0x1d448u,0x1d430u,0x80000000u,0x100000u,0x1d44au,0x100000u,0x80000000u,0x100000u,0x1d44cu,0x100000u,0x80000000u,0x100000u,0x1d44eu,0x100000u,0x80000000u,0x100000u,0x1d458u,0x1d430u,0x80000000u,0x100000u,0x1d45au,0x100000u,0x80000000u,0x100000u,0x1d45cu,0x100000u,0x80000000u,0x100000u,0x1d45eu,0x100000u,0x80000000u,0x100000u,0x1d468u,0x1d430u,0x80000000u,0x100000u,0x1d46au,0x100000u,0x80000000u,0x100000u,0x1d46cu,0x100000u,0x80000000u,0x100000u,0x1d46eu,0x100000u,0x80000000u,0x100000u,0x1d478u,0x1d430u,0x80000000u,0x100000u,0x1d47au,0x100000u,0x80000000u,0x100000u,0x1d47cu,0x100000u,0x80000000u,0x100000u,0x1d47eu,0x100000u,0x80000000u,0x850u,0x60000u,0x90420u,0xefc50u,0x840u,0x60010u,0x90430u,0xefc40u,0x870u,0x60020u,0x90400u,0xefc70u,0x860u,0x60030u,0x90410u,0xefc60u,0x810u,0x60040u,0x90460u,0xefc10u,0x800u,0x60050u,0x90470u,0xefc00u,0x830u,0x60060u,0x90440u,0xefc30u,0x820u,0x60070u,0x90450u,0xefc20u,0x8d0u,0x60080u,0x904b0u,0xefcd0u,0x8c0u,0x60090u,0x904a0u,0xefcc0u,0x8f0u,0x600a0u,0x90490u,0xefcf0u,0x8e0u,0x600b0u,0x90480u,0xefce0u,0x890u,0x600c0u,0x904f0u,0xefc90u,0x880u,0x600d0u,0x904e0u,0xefc80u,0x8b0u,0x600e0u,0x904d0u,0xefcb0u,0x8a0u,0x600f0u,0x904c0u,0xefca0u,0x960u,0x60100u,0x90530u,0xefd60u,0x970u,0x60110u,0x90520u,0xefd70u,0x940u,0x60120u,0x90510u,0xefd40u,0x950u,0x60130u,0x90500u,0xefd50u,0x920u,0x60140u,0x90570u,0xefd20u,0x930u,0x60150u,0x90560u,0xefd30u,0x900u,0x60160u,0x90550u,0xefd00u,0x910u,0x60170u,0x90540u,0xefd10u,0x9e0u,0x60190u,0x905b0u,0xefde0u,0x9f0u,0x60180u,0x905a0u,0xefdf0u,0x9c0u,0x601b0u,0x90590u,0xefdc0u,0x9d0u,0x601a0u,0x90580u,0xefdd0u,0x9a0u,0x601d0u,0x905f0u,0xefda0u,0x9b0u,0x601c0u,0x905e0u,0xefdb0u,0x980u,0x601f0u,0x905d0u,0xefd80u,0x990u,0x601e0u,0x905c0u,0xefd90u,0xa60u,0x60210u,0xbfa40u,0xefe60u,0xa70u,0x60200u,0xbfa50u,0xefe70u,0xa40u,0x60230u,0xbfa60u,0xefe40u,0xa50u,0x60220u,0xbfa70u,0xefe50u,0xa20u,0x60250u,0xbfa00u,0xefe20u,0xa30u,0x60240u,0xbfa10u,0xefe30u,0xa00u,0x60270u,0xbfa20u,0xefe00u,0xa10u,0x60260u,0xbfa30u,0xefe10u,0x2fef0u,0x60290u,0xbfac0u,0xefef0u,0x2fee0u,0x60280u,0xbfad0u,0xefee0u,0x2fed0u,0x602b0u,0xbfae0u,0xefed0u,0x2fec0u,0x602a0u,0xbfaf0u,0xefec0u,0x2feb0u,0x602d0u,0xbfa80u,0xefeb0u,0x2fea0u,0x602c0u,0xbfa90u,0xefea0u,0x2fe90u,0x602f0u,0xbfaa0u,0xefe90u,0x2fe80u,0x602e0u,0xbfab0u,0xefe80u,0x2ff70u,0x60320u,0xbfb40u,0xeff70u,0x2ff60u,0x60330u,0xbfb50u,0xeff60u,0x2ff50u,0x60300u,0xbfb60u,0xeff50u,0x2ff40u,0x60310u,0xbfb70u,0xeff40u,0x2ff30u,0x60360u,0xbfb00u,0xeff30u,0x2ff20u,0x60370u,0xbfb10u,0xeff20u,0x2ff10u,0x60340u,0xbfb20u,0xeff10u,0x2ff00u,0x60350u,0xbfb30u,0xeff00u,0x2fff0u,0x603a0u,0xbfbd0u,0xefff0u,0x2ffe0u,0x603b0u,0xbfbc0u,0xeffe0u,0x2ffd0u,0x60380u,0xbfbf0u,0xeffd0u,0x2ffc0u,0x60390u,0xbfbe0u,0xeffc0u,0x2ffb0u,0x603e0u,0xbfb90u,0xeffb0u,0x2ffa0u,0x603f0u,0xbfb80u,0xeffa0u,0x2ff90u,0x603c0u,0xbfbb0u,0xeff90u,0x2ff80u,0x603d0u,0xbfba0u,0xeff80u,0x0u,0x80000000u,0x0u,0x18830u,0x0u,0x80000000u,0x0u,0x18830u,0x0u,0x80000000u,0x0u,0x18830u,0x0u,0x80000000u,0x0u,0x18830u,0x0u,0x80000000u,0x0u,0x18830u,0x0u,0x80000000u,0x0u,0x18830u,0x0u,0x80000000u,0x0u,0x18830u,0x0u,0x80000000u,0x0u,0x18830u,0x0u,0x80000000u,0x0u,0x18830u,0x0u,0x80000000u,0x0u,0x18830u,0x0u,0x80000000u,0x0u,0x18830u,0x0u,0x80000000u,0x0u,0x18830u,0x0u,0x80000000u,0x0u,0x18830u,0x0u,0x80000000u,0x0u,0x18830u,0x0u,0x80000000u,0x0u,0x18830u,0x0u,0x80000000u,0x0u,0x18830u,0x0u,0x80000000u,0x0u,0x188b2u,0x0u,0x80000000u,0x0u,0x188b2u,0x0u,0x80000000u,0x0u,0x188b2u,0x0u,0x80000000u,0x0u,0x188b2u,0x0u,0x80000000u,0x0u,0x188b2u,0x0u,0x80000000u,0x0u,0x188b2u,0x0u,0x80000000u,0x0u,0x188b2u,0x0u,0x80000000u,0x0u,0x188b2u,0x0u,0x80000000u,0x0u,0x18892u,0x0u,0x80000000u,0x0u,0x18892u,0x0u,0x80000000u,0x0u,0x18892u,0x0u,0x80000000u,0x0u,0x18892u,0x0u,0x80000000u,0x0u,0x18892u,0x0u,0x80000000u,0x0u,0x18892u,0x0u,0x80000000u,0x0u,0x18892u,0x0u,0x80000000u,0x0u,0x18892u,0x0u,0x80000000u,0x0u,0x1489au,0x0u,0x80000000u,0x0u,0x1489au,0x0u,0x80000000u,0x0u,0x1489au,0x0u,0x80000000u,0x0u,0x1489au,0x0u,0x80000000u,0x0u,0x1489au,0x0u,0x80000000u,0x0u,0x1489au,0x0u,0x80000000u,0x0u,0x1489au,0x0u,0x80000000u,0x0u,0x1489au,0x0u,0x80000000u,0x0u,0x34018u,0x0u,0x80000000u,0x0u,0x34018u,0x0u,0x80000000u,0x0u,0x34018u,0x0u,0x80000000u,0x0u,0x34018u,0x0u,0x80000000u,0x0u,0x34018u,0x0u,0x80000000u,0x0u,0x34018u,0x0u,0x80000000u,0x0u,0x34018u,0x0u,0x80000000u,0x0u,0x34018u,0x0u,0x80000000u,0x0u,0x36018u,0x0u,0x80000000u,0x0u,0x36018u,0x0u,0x80000000u,0x0u,0x36018u,0x0u,0x80000000u,0x0u,0x36018u,0x0u,0x80000000u,0x0u,0x36018u,0x0u,0x80000000u,0x0u,0x36018u,0x0u,0x80000000u,0x0u,0x36018u,0x0u,0x80000000u,0x0u,0x36018u,0x0u,0x80000000u,0x0u,0x36030u,0x0u,0x80000000u,0x0u,0x36030u,0x0u,0x80000000u,0x0u,0x36030u,0x0u,0x80000000u,0x0u,0x36030u,0x0u,0x80000000u,0x0u,0x36030u,0x0u,0x80000000u,0x0u,0x36030u,0x0u,0x80000000u,0x0u,0x36030u,0x0u,0x80000000u,0x0u,0x36030u,0x6840u,0x6860u,0x6820u,0x6820u,0x6848u,0x6868u,0x6828u,0x6830u,0x6850u,0x6870u,0x6830u,0x6840u,0x6858u,0x6878u,0x6838u,0x6850u,0x68c0u,0x68e0u,0x68a0u,0x68a0u,0x68c8u,0x68e8u,0x68a8u,0x68b0u,0x68d0u,0x68f0u,0x68b0u,0x68c0u,0x68d8u,0x68f8u,0x68b8u,0x68d0u,0x6960u,0x6940u,0x6900u,0x6900u,0x6968u,0x6948u,0x6908u,0x6910u,0x6970u,0x6950u,0x6910u,0x6960u,0x6978u,0x6958u,0x6918u,0x6970u,0x69e0u,0x69c0u,0x6980u,0x6980u,0x69e8u,0x69c8u,0x6988u,0x6990u,0x69f0u,0x69d0u,0x6990u,0x69e0u,0x69f8u,0x69d8u,0x6998u,0x69f0u,0x6c00u,0x6c20u,0x6c60u,0x6a20u,0x6c08u,0x6c28u,0x6c68u,0x6a30u,0x6c10u,0x6c30u,0x6c70u,0x6a40u,0x6c18u,0x6c38u,0x6c78u,0x6a50u,0x6c80u,0x6ca0u,0x6ce0u,0x6aa0u,0x6c88u,0x6ca8u,0x6ce8u,0x6ab0u,0x6c90u,0x6cb0u,0x6cf0u,0x6ac0u,0x6c98u,0x6cb8u,0x6cf8u,0x6ad0u,0x6d20u,0x6d00u,0x6d40u,0x6b00u,0x6d28u,0x6d08u,0x6d48u,0x6b10u,0x6d30u,0x6d10u,0x6d50u,0x6b60u,0x6d38u,0x6d18u,0x6d58u,0x6b70u,0x6da0u,0x6d80u,0x6dc0u,0x6b80u,0x6da8u,0x6d88u,0x6dc8u,0x6b90u,0x6db0u,0x6d90u,0x6dd0u,0x6be0u,0x6db8u,0x6d98u,0x6dd8u,0x6bf0u,0x7040u,0x7060u,0x7020u,0x6c60u,0x7048u,0x7068u,0x7028u,0x6c70u,0x7050u,0x7070u,0x7030u,0x6c00u,0x7058u,0x7078u,0x7038u,0x6c10u,0x70c0u,0x70e0u,0x70a0u,0x6ce0u,0x70c8u,0x70e8u,0x70a8u,0x6cf0u,0x70d0u,0x70f0u,0x70b0u,0x6c80u,0x70d8u,0x70f8u,0x70b8u,0x6c90u,0x7160u,0x7140u,0x7100u,0x6d40u,0x7168u,0x7148u,0x7108u,0x6d50u,0x7170u,0x7150u,0x7110u,0x6d20u,0x7178u,0x7158u,0x7118u,0x6d30u,0x71e0u,0x71c0u,0x7180u,0x6dc0u,0x71e8u,0x71c8u,0x7188u,0x6dd0u,0x71f0u,0x71d0u,0x7190u,0x6da0u,0x71f8u,0x71d8u,0x7198u,0x6db0u,0x7400u,0x7420u,0x7460u,0x6e60u,0x7408u,0x7428u,0x7468u,0x6e70u,0x7410u,0x7430u,0x7470u,0x6e00u,0x7418u,0x7438u,0x7478u,0x6e10u,0x7480u,0x74a0u,0x74e0u,0x6ee0u,0x7488u,0x74a8u,0x74e8u,0x6ef0u,0x7490u,0x74b0u,0x74f0u,0x6e80u,0x7498u,0x74b8u,0x74f8u,0x6e90u,0x7520u,0x7500u,0x7540u,0x6f40u,0x7528u,0x7508u,0x7548u,0x6f50u,0x7530u,0x7510u,0x7550u,0x6f20u,0x7538u,0x7518u,0x7558u,0x6f30u,0x75a0u,0x7580u,0x75c0u,0x6fc0u,0x75a8u,0x7588u,0x75c8u,0x6fd0u,0x75b0u,0x7590u,0x75d0u,0x6fa0u,0x75b8u,0x7598u,0x75d8u,0x6fb0u,0x6860u,0x1ea40u,0x1ec20u,0x2440u,0x6868u,0x1eed0u,0x1f0b0u,0x2450u,0x6870u,0x1f360u,0x1f540u,0x2460u,0x6878u,0x1f7f0u,0x1f9d0u,0x2470u,0x68e0u,0x1fc80u,0x1fe60u,0x2400u,0x68e8u,0x20110u,0x202f0u,0x2410u,0x68f0u,0x205a0u,0x20780u,0x2420u,0x68f8u,0x20a30u,0x20c10u,0x2430u,0x6940u,0x20ec0u,0x210a0u,0x25c0u,0x6948u,0x21350u,0x21530u,0x25d0u,0x6950u,0x217e0u,0x219c0u,0x25e0u,0x6958u,0x21c70u,0x21e50u,0x25f0u,0x69c0u,0x22100u,0x222e0u,0x2580u,0x69c8u,0x22590u,0x22770u,0x2590u,0x69d0u,0x22a20u,0x22c00u,0x25a0u,0x69d8u,0x22eb0u,0x23090u,0x25b0u,0x6a60u,0x1ea70u,0x1ec28u,0x2760u,0x6a68u,0x1ef00u,0x1f0b8u,0x2770u,0x6a70u,0x1f390u,0x1f548u,0x2740u,0x6a78u,0x1f820u,0x1f9d8u,0x2750u,0x6ae0u,0x1fcb0u,0x1fe68u,0x2720u,0x6ae8u,0x20140u,0x202f8u,0x2730u,0x6af0u,0x205d0u,0x20788u,0x2700u,0x6af8u,0x20a60u,0x20c18u,0x2710u,0x6b40u,0x20ef0u,0x210a8u,0x28e0u,0x6b48u,0x21380u,0x21538u,0x28f0u,0x6b50u,0x21810u,0x219c8u,0x28c0u,0x6b58u,0x21ca0u,0x21e58u,0x28d0u,0x6bc0u,0x22130u,0x222e8u,0x28a0u,0x6bc8u,0x225c0u,0x22778u,0x28b0u,0x6bd0u,0x22a50u,0x22c08u,0x2880u,0x6bd8u,0x22ee0u,0x23098u,0x2890u,0x6c20u,0x1eaa0u,0x1ec30u,0x2a40u,0x6c28u,0x1ef30u,0x1f0c0u,0x2a50u,0x6c30u,0x1f3c0u,0x1f550u,0x2a60u,0x6c38u,0x1f850u,0x1f9e0u,0x2a70u,0x6ca0u,0x1fce0u,0x1fe70u,0x2a00u,0x6ca8u,0x20170u,0x20300u,0x2a10u,0x6cb0u,0x20600u,0x20790u,0x2a20u,0x6cb8u,0x20a90u,0x20c20u,0x2a30u,0x6d00u,0x20f20u,0x210b0u,0x2bc0u,0x6d08u,0x213b0u,0x21540u,0x2bd0u,0x6d10u,0x21840u,0x219d0u,0x2be0u,0x6d18u,0x21cd0u,0x21e60u,0x2bf0u,0x6d80u,0x22160u,0x222f0u,0x2b80u,0x6d88u,0x225f0u,0x22780u,0x2b90u,0x6d90u,0x22a80u,0x22c10u,0x2ba0u,0x6d98u,0x22f10u,0x230a0u,0x2bb0u,0x6e20u,0x1ead0u,0x1ec38u,0x2d60u,0x6e28u,0x1ef60u,0x1f0c8u,0x2d70u,0x6e30u,0x1f3f0u,0x1f558u,0x2d40u,0x6e38u,0x1f880u,0x1f9e8u,0x2d50u,0x6ea0u,0x1fd10u,0x1fe78u,0x2d20u,0x6ea8u,0x201a0u,0x20308u,0x2d30u,0x6eb0u,0x20630u,0x20798u,0x2d00u,0x6eb8u,0x20ac0u,0x20c28u,0x2d10u,0x6f00u,0x20f50u,0x210b8u,0x2ee0u,0x6f08u,0x213e0u,0x21548u,0x2ef0u,0x6f10u,0x21870u,0x219d8u,0x2ec0u,0x6f18u,0x21d00u,0x21e68u,0x2ed0u,0x6f80u,0x22190u,0x222f8u,0x2ea0u,0x6f88u,0x22620u,0x22788u,0x2eb0u,0x6f90u,0x22ab0u,0x22c18u,0x2e80u,0x6f98u,0x22f40u,0x230a8u,0x2e90u,0x0u,0x40u,0x9000u,0xcc00u,0x80u,0xc0u,0x9010u,0xcc10u,0x110u,0x150u,0x9020u,0xcc20u,0x190u,0x1d0u,0x9030u,0xcc30u,0x220u,0x260u,0x9040u,0xcc40u,0x2a0u,0x2e0u,0x9050u,0xcc50u,0x330u,0x370u,0x9060u,0xcc60u,0x3b0u,0x3f0u,0x9070u,0xcc70u,0x440u,0x400u,0x9080u,0xcc80u,0x4c0u,0x480u,0x9090u,0xcc90u,0x550u,0x510u,0x90a0u,0xcca0u,0x5d0u,0x590u,0x90b0u,0xccb0u,0x660u,0x620u,0x90c0u,0xccc0u,0x6e0u,0x6a0u,0x90d0u,0xccd0u,0x770u,0x730u,0x90e0u,0xcce0u,0x7f0u,0x7b0u,0x90f0u,0xccf0u,0x10u,0x50u,0x9300u,0xcf00u,0x90u,0xd0u,0x9310u,0xcf10u,0x100u,0x140u,0x9320u,0xcf20u,0x180u,0x1c0u,0x9330u,0xcf30u,0x230u,0x270u,0x9340u,0xcf40u,0x2b0u,0x2f0u,0x9350u,0xcf50u,0x320u,0x360u,0x9360u,0xcf60u,0x3a0u,0x3e0u,0x9370u,0xcf70u,0x450u,0x410u,0x9380u,0xcf80u,0x4d0u,0x490u,0x9390u,0xcf90u,0x540u,0x500u,0x93a0u,0xcfa0u,0x5c0u,0x580u,0x93b0u,0xcfb0u,0x670u,0x630u,0x93c0u,0xcfc0u,0x6f0u,0x6b0u,0x93d0u,0xcfd0u,0x760u,0x720u,0x93e0u,0xcfe0u,0x7e0u,0x7a0u,0x93f0u,0xcff0u,0x20u,0x60u,0x9600u,0xd200u,0xa0u,0xe0u,0x9610u,0xd210u,0x130u,0x170u,0x9620u,0xd220u,0x1b0u,0x1f0u,0x9630u,0xd230u,0x200u,0x240u,0x9640u,0xd240u,0x280u,0x2c0u,0x9650u,0xd250u,0x310u,0x350u,0x9660u,0xd260u,0x390u,0x3d0u,0x9670u,0xd270u,0x460u,0x420u,0x9680u,0xd280u,0x4e0u,0x4a0u,0x9690u,0xd290u,0x570u,0x530u,0x96a0u,0xd2a0u,0x5f0u,0x5b0u,0x96b0u,0xd2b0u,0x640u,0x600u,0x96c0u,0xd2c0u,0x6c0u,0x680u,0x96d0u,0xd2d0u,0x750u,0x710u,0x96e0u,0xd2e0u,0x7d0u,0x790u,0x96f0u,0xd2f0u,0x30u,0x70u,0x9900u,0xd500u,0xb0u,0xf0u,0x9910u,0xd510u,0x120u,0x160u,0x9920u,0xd520u,0x1a0u,0x1e0u,0x9930u,0xd530u,0x210u,0x250u,0x9940u,0xd540u,0x290u,0x2d0u,0x9950u,0xd550u,0x300u,0x340u,0x9960u,0xd560u,0x380u,0x3c0u,0x9970u,0xd570u,0x470u,0x430u,0x9980u,0xd580u,0x4f0u,0x4b0u,0x9990u,0xd590u,0x560u,0x520u,0x99a0u,0xd5a0u,0x5e0u,0x5a0u,0x99b0u,0xd5b0u,0x650u,0x610u,0x99c0u,0xd5c0u,0x6d0u,0x690u,0x99d0u,0xd5d0u,0x740u,0x700u,0x99e0u,0xd5e0u,0x7c0u,0x780u,0x99f0u,0xd5f0u,0xc008u,0x12120u,0x12160u,0x12c00u,0xc018u,0x122b0u,0x122f0u,0x12c10u,0xc028u,0x12400u,0x12440u,0x12c20u,0xc038u,0x12590u,0x125d0u,0x12c30u,0xc048u,0x12760u,0x12720u,0x12c40u,0xc058u,0x128f0u,0x128b0u,0x12c50u,0xc068u,0x12a40u,0x12a00u,0x12c60u,0xc078u,0x12bd0u,0x12b90u,0x12c70u,0xc088u,0x12d20u,0x12d60u,0x12c80u,0xc098u,0x12eb0u,0x12ef0u,0x12c90u,0xc0a8u,0x13000u,0x13040u,0x12ca0u,0xc0b8u,0x13190u,0x131d0u,0x12cb0u,0xc0c8u,0x13360u,0x13320u,0x12cc0u,0xc0d8u,0x134f0u,0x134b0u,0x12cd0u,0xc0e8u,0x13640u,0x13600u,0x12ce0u,0xc0f8u,0x137d0u,0x13790u,0x12cf0u,0xc308u,0x12130u,0x12168u,0x12d00u,0xc318u,0x122a0u,0x122f8u,0x12d10u,0xc328u,0x12410u,0x12448u,0x12d20u,0xc338u,0x12580u,0x125d8u,0x12d30u,0xc348u,0x12770u,0x12728u,0x12d40u,0xc358u,0x128e0u,0x128b8u,0x12d50u,0xc368u,0x12a50u,0x12a08u,0x12d60u,0xc378u,0x12bc0u,0x12b98u,0x12d70u,0xc388u,0x12d30u,0x12d68u,0x12d80u,0xc398u,0x12ea0u,0x12ef8u,0x12d90u,0xc3a8u,0x13010u,0x13048u,0x12da0u,0xc3b8u,0x13180u,0x131d8u,0x12db0u,0xc3c8u,0x13370u,0x13328u,0x12dc0u,0xc3d8u,0x134e0u,0x134b8u,0x12dd0u,0xc3e8u,0x13650u,0x13608u,0x12de0u,0xc3f8u,0x137c0u,0x13798u,0x12df0u,0xc608u,0x12140u,0x12170u,0x12e00u,0xc618u,0x122d0u,0x122e0u,0x12e10u,0xc628u,0x12460u,0x12450u,0x12e20u,0xc638u,0x125f0u,0x125c0u,0x12e30u,0xc648u,0x12700u,0x12730u,0x12e40u,0xc658u,0x12890u,0x128a0u,0x12e50u,0xc668u,0x12a20u,0x12a10u,0x12e60u,0xc678u,0x12bb0u,0x12b80u,0x12e70u,0xc688u,0x12d40u,0x12d70u,0x12e80u,0xc698u,0x12ed0u,0x12ee0u,0x12e90u,0xc6a8u,0x13060u,0x13050u,0x12ea0u,0xc6b8u,0x131f0u,0x131c0u,0x12eb0u,0xc6c8u,0x13300u,0x13330u,0x12ec0u,0xc6d8u,0x13490u,0x134a0u,0x12ed0u,0xc6e8u,0x13620u,0x13610u,0x12ee0u,0xc6f8u,0x137b0u,0x13780u,0x12ef0u,0xc908u,0x12150u,0x12178u,0x12f00u,0xc918u,0x122c0u,0x122e8u,0x12f10u,0xc928u,0x12470u,0x12458u,0x12f20u,0xc938u,0x125e0u,0x125c8u,0x12f30u,0xc948u,0x12710u,0x12738u,0x12f40u,0xc958u,0x12880u,0x128a8u,0x12f50u,0xc968u,0x12a30u,0x12a18u,0x12f60u,0xc978u,0x12ba0u,0x12b88u,0x12f70u,0xc988u,0x12d50u,0x12d78u,0x12f80u,0xc998u,0x12ec0u,0x12ee8u,0x12f90u,0xc9a8u,0x13070u,0x13058u,0x12fa0u,0xc9b8u,0x131e0u,0x131c8u,0x12fb0u,0xc9c8u,0x13310u,0x13338u,0x12fc0u,0xc9d8u,0x13480u,0x134a8u,0x12fd0u,0xc9e8u,0x13630u,0x13618u,0x12fe0u,0xc9f8u,0x137a0u,0x13788u,0x12ff0u,0x8000u,0x1c300u,0x1ea40u,0x1ec20u,0x8080u,0x1c310u,0x100000u,0x1ec22u,0x8110u,0x1c320u,0x100000u,0x1ec24u,0x8190u,0x1c330u,0x100000u,0x1ec26u,0x8220u,0x1c340u,0x1ea40u,0x1ec28u,0x82a0u,0x1c350u,0x100000u,0x1ec2au,0x8330u,0x1c360u,0x100000u,0x1ec2cu,0x83b0u,0x1c370u,0x100000u,0x1ec2eu,0x8440u,0x1c380u,0x1ea40u,0x1ec30u,0x84c0u,0x1c390u,0x100000u,0x1ec32u,0x8550u,0x1c3a0u,0x100000u,0x1ec34u,0x85d0u,0x1c3b0u,0x100000u,0x1ec36u,0x8660u,0x1c3c0u,0x1ea40u,0x1ec38u,0x86e0u,0x1c3d0u,0x100000u,0x1ec3au,0x8770u,0x1c3e0u,0x100000u,0x1ec3cu,0x87f0u,0x1c3f0u,0x100000u,0x1ec3eu,0x8010u,0x1c400u,0x1ea70u,0x1ec20u,0x8090u,0x1c410u,0x100000u,0x1ec22u,0x8100u,0x1c420u,0x100000u,0x1ec24u,0x8180u,0x1c430u,0x100000u,0x1ec26u,0x8230u,0x1c440u,0x1ea70u,0x1ec28u,0x82b0u,0x1c450u,0x100000u,0x1ec2au,0x8320u,0x1c460u,0x100000u,0x1ec2cu,0x83a0u,0x1c470u,0x100000u,0x1ec2eu,0x8450u,0x1c480u,0x1ea70u,0x1ec30u,0x84d0u,0x1c490u,0x100000u,0x1ec32u,0x8540u,0x1c4a0u,0x100000u,0x1ec34u,0x85c0u,0x1c4b0u,0x100000u,0x1ec36u,0x8670u,0x1c4c0u,0x1ea70u,0x1ec38u,0x86f0u,0x1c4d0u,0x100000u,0x1ec3au,0x8760u,0x1c4e0u,0x100000u,0x1ec3cu,0x87e0u,0x1c4f0u,0x100000u,0x1ec3eu,0x8020u,0x1c500u,0x1eaa0u,0x1ec20u,0x80a0u,0x1c510u,0x100000u,0x1ec22u,0x8130u,0x1c520u,0x100000u,0x1ec24u,0x81b0u,0x1c530u,0x100000u,0x1ec26u,0x8200u,0x1c540u,0x1eaa0u,0x1ec28u,0x8280u,0x1c550u,0x100000u,0x1ec2au,0x8310u,0x1c560u,0x100000u,0x1ec2cu,0x8390u,0x1c570u,0x100000u,0x1ec2eu,0x8460u,0x1c580u,0x1eaa0u,0x1ec30u,0x84e0u,0x1c590u,0x100000u,0x1ec32u,0x8570u,0x1c5a0u,0x100000u,0x1ec34u,0x85f0u,0x1c5b0u,0x100000u,0x1ec36u,0x8640u,0x1c5c0u,0x1eaa0u,0x1ec38u,0x86c0u,0x1c5d0u,0x100000u,0x1ec3au,0x8750u,0x1c5e0u,0x100000u,0x1ec3cu,0x87d0u,0x1c5f0u,0x100000u,0x1ec3eu,0x8030u,0x1c600u,0x1ead0u,0x1ec20u,0x80b0u,0x1c610u,0x100000u,0x1ec22u,0x8120u,0x1c620u,0x100000u,0x1ec24u,0x81a0u,0x1c630u,0x100000u,0x1ec26u,0x8210u,0x1c640u,0x1ead0u,0x1ec28u,0x8290u,0x1c650u,0x100000u,0x1ec2au,0x8300u,0x1c660u,0x100000u,0x1ec2cu,0x8380u,0x1c670u,0x100000u,0x1ec2eu,0x8470u,0x1c680u,0x1ead0u,0x1ec30u,0x84f0u,0x1c690u,0x100000u,0x1ec32u,0x8560u,0x1c6a0u,0x100000u,0x1ec34u,0x85e0u,0x1c6b0u,0x100000u,0x1ec36u,0x8650u,0x1c6c0u,0x1ead0u,0x1ec38u,0x86d0u,0x1c6d0u,0x100000u,0x1ec3au,0x8740u,0x1c6e0u,0x100000u,0x1ec3cu,0x87c0u,0x1c6f0u,0x100000u,0x1ec3eu,0x27500u,0x1d480u,0x1d480u,0x80000000u,0x27502u,0x1d482u,0x100000u,0x80000000u,0x27504u,0x1d484u,0x100000u,0x80000000u,0x27506u,0x1d486u,0x100000u,0x80000000u,0x27700u,0x1d490u,0x1d480u,0x80000000u,0x27702u,0x1d492u,0x100000u,0x80000000u,0x27704u,0x1d494u,0x100000u,0x80000000u,0x27706u,0x1d496u,0x100000u,0x80000000u,0x27900u,0x1d4a0u,0x1d480u,0x80000000u,0x27902u,0x1d4a2u,0x100000u,0x80000000u,0x27904u,0x1d4a4u,0x100000u,0x80000000u,0x27906u,0x1d4a6u,0x100000u,0x80000000u,0x27b00u,0x1d4b0u,0x1d480u,0x80000000u,0x27b02u,0x1d4b2u,0x100000u,0x80000000u,0x27b04u,0x1d4b4u,0x100000u,0x80000000u,0x27b06u,0x1d4b6u,0x100000u,0x80000000u,0x27508u,0x1d488u,0x1d490u,0x80000000u,0x2750au,0x1d48au,0x100000u,0x80000000u,0x2750cu,0x1d48cu,0x100000u,0x80000000u,0x2750eu,0x1d48eu,0x100000u,0x80000000u,0x27708u,0x1d498u,0x1d490u,0x80000000u,0x2770au,0x1d49au,0x100000u,0x80000000u,0x2770cu,0x1d49cu,0x100000u,0x80000000u,0x2770eu,0x1d49eu,0x100000u,0x80000000u,0x27908u,0x1d4a8u,0x1d490u,0x80000000u,0x2790au,0x1d4aau,0x100000u,0x80000000u,0x2790cu,0x1d4acu,0x100000u,0x80000000u,0x2790eu,0x1d4aeu,0x100000u,0x80000000u,0x27b08u,0x1d4b8u,0x1d490u,0x80000000u,0x27b0au,0x1d4bau,0x100000u,0x80000000u,0x27b0cu,0x1d4bcu,0x100000u,0x80000000u,0x27b0eu,0x1d4beu,0x100000u,0x80000000u,0x26d08u,0x1d4c0u,0x1d4a0u,0x80000000u,0x26d0au,0x1d4c2u,0x100000u,0x80000000u,0x26d0cu,0x1d4c4u,0x100000u,0x80000000u,0x26d0eu,0x1d4c6u,0x100000u,0x80000000u,0x26f08u,0x1d4d0u,0x1d4a0u,0x80000000u,0x26f0au,0x1d4d2u,0x100000u,0x80000000u,0x26f0cu,0x1d4d4u,0x100000u,0x80000000u,0x26f0eu,0x1d4d6u,0x100000u,0x80000000u,0x27108u,0x1d4e0u,0x1d4a0u,0x80000000u,0x2710au,0x1d4e2u,0x100000u,0x80000000u,0x2710cu,0x1d4e4u,0x100000u,0x80000000u,0x2710eu,0x1d4e6u,0x100000u,0x80000000u,0x27308u,0x1d4f0u,0x1d4a0u,0x80000000u,0x2730au,0x1d4f2u,0x100000u,0x80000000u,0x2730cu,0x1d4f4u,0x100000u,0x80000000u,0x2730eu,0x1d4f6u,0x100000u,0x80000000u,0x100000u,0x1d4c8u,0x1d4b0u,0x80000000u,0x100000u,0x1d4cau,0x100000u,0x80000000u,0x100000u,0x1d4ccu,0x100000u,0x80000000u,0x100000u,0x1d4ceu,0x100000u,0x80000000u,0x100000u,0x1d4d8u,0x1d4b0u,0x80000000u,0x100000u,0x1d4dau,0x100000u,0x80000000u,0x100000u,0x1d4dcu,0x100000u,0x80000000u,0x100000u,0x1d4deu,0x100000u,0x80000000u,0x100000u,0x1d4e8u,0x1d4b0u,0x80000000u,0x100000u,0x1d4eau,0x100000u,0x80000000u,0x100000u,0x1d4ecu,0x100000u,0x80000000u,0x100000u,0x1d4eeu,0x100000u,0x80000000u,0x100000u,0x1d4f8u,0x1d4b0u,0x80000000u,0x100000u,0x1d4fau,0x100000u,0x80000000u,0x100000u,0x1d4fcu,0x100000u,0x80000000u,0x100000u,0x1d4feu,0x100000u,0x80000000u,0x30000u,0x60420u,0xbfc50u,0xf0000u,0x30010u,0x60430u,0xbfc40u,0xf0010u,0x30020u,0x60400u,0xbfc70u,0xf0020u,0x30030u,0x60410u,0xbfc60u,0xf0030u,0x30040u,0x60460u,0xbfc10u,0xf0040u,0x30050u,0x60470u,0xbfc00u,0xf0050u,0x30060u,0x60440u,0xbfc30u,0xf0060u,0x30070u,0x60450u,0xbfc20u,0xf0070u,0x30080u,0x604b0u,0xbfcd0u,0xf0080u,0x30090u,0x604a0u,0xbfcc0u,0xf0090u,0x300a0u,0x60490u,0xbfcf0u,0xf00a0u,0x300b0u,0x60480u,0xbfce0u,0xf00b0u,0x300c0u,0x604f0u,0xbfc90u,0xf00c0u,0x300d0u,0x604e0u,0xbfc80u,0xf00d0u,0x300e0u,0x604d0u,0xbfcb0u,0xf00e0u,0x300f0u,0x604c0u,0xbfca0u,0xf00f0u,0x30100u,0x60530u,0xbfd60u,0xf0100u,0x30110u,0x60520u,0xbfd70u,0xf0110u,0x30120u,0x60510u,0xbfd40u,0xf0120u,0x30130u,0x60500u,0xbfd50u,0xf0130u,0x30140u,0x60570u,0xbfd20u,0xf0140u,0x30150u,0x60560u,0xbfd30u,0xf0150u,0x30160u,0x60550u,0xbfd00u,0xf0160u,0x30170u,0x60540u,0xbfd10u,0xf0170u,0x30190u,0x605b0u,0xbfde0u,0xf0190u,0x30180u,0x605a0u,0xbfdf0u,0xf0180u,0x301b0u,0x60590u,0xbfdc0u,0xf01b0u,0x301a0u,0x60580u,0xbfdd0u,0xf01a0u,0x301d0u,0x605f0u,0xbfda0u,0xf01d0u,0x301c0u,0x605e0u,0xbfdb0u,0xf01c0u,0x301f0u,0x605d0u,0xbfd80u,0xf01f0u,0x301e0u,0x605c0u,0xbfd90u,0xf01e0u,0x30210u,0x60640u,0xbfe60u,0xf0210u,0x30200u,0x60650u,0xbfe70u,0xf0200u,0x30230u,0x60660u,0xbfe40u,0xf0230u,0x30220u,0x60670u,0xbfe50u,0xf0220u,0x30250u,0x60600u,0xbfe20u,0xf0250u,0x30240u,0x60610u,0xbfe30u,0xf0240u,0x30270u,0x60620u,0xbfe00u,0xf0270u,0x30260u,0x60630u,0xbfe10u,0xf0260u,0x30290u,0x606c0u,0xbfef0u,0xf0290u,0x30280u,0x606d0u,0xbfee0u,0xf0280u,0x302b0u,0x606e0u,0xbfed0u,0xf02b0u,0x302a0u,0x606f0u,0xbfec0u,0xf02a0u,0x302d0u,0x60680u,0xbfeb0u,0xf02d0u,0x302c0u,0x60690u,0xbfea0u,0xf02c0u,0x302f0u,0x606a0u,0xbfe90u,0xf02f0u,0x302e0u,0x606b0u,0xbfe80u,0xf02e0u,0x30320u,0x60740u,0xbff70u,0x11f720u,0x30330u,0x60750u,0xbff60u,0x11f730u,0x30300u,0x60760u,0xbff50u,0x11f700u,0x30310u,0x60770u,0xbff40u,0x11f710u,0x30360u,0x60700u,0xbff30u,0x11f760u,0x30370u,0x60710u,0xbff20u,0x11f770u,0x30340u,0x60720u,0xbff10u,0x11f740u,0x30350u,0x60730u,0xbff00u,0x11f750u,0x303a0u,0x8fbd0u,0xbfff0u,0x11f7a0u,0x303b0u,0x8fbc0u,0xbffe0u,0x11f7b0u,0x30380u,0x8fbf0u,0xbffd0u,0x11f780u,0x30390u,0x8fbe0u,0xbffc0u,0x11f790u,0x303e0u,0x8fb90u,0xbffb0u,0x11f7e0u,0x303f0u,0x8fb80u,0xbffa0u,0x11f7f0u,0x303c0u,0x8fbb0u,0xbff90u,0x11f7c0u,0x303d0u,0x8fba0u,0xbff80u,0x11f7d0u,0x0u,0x80000000u,0x0u,0x36030u,0x0u,0x80000000u,0x0u,0x36030u,0x0u,0x80000000u,0x0u,0x36030u,0x0u,0x80000000u,0x0u,0x36030u,0x0u,0x80000000u,0x0u,0x36030u,0x0u,0x80000000u,0x0u,0x36030u,0x0u,0x80000000u,0x0u,0x36030u,0x0u,0x80000000u,0x0u,0x36030u,0x0u,0x80000000u,0x0u,0x360b2u,0x0u,0x80000000u,0x0u,0x360b2u,0x0u,0x80000000u,0x0u,0x360b2u,0x0u,0x80000000u,0x0u,0x360b2u,0x0u,0x80000000u,0x0u,0x360b2u,0x0u,0x80000000u,0x0u,0x360b2u,0x0u,0x80000000u,0x0u,0x360b2u,0x0u,0x80000000u,0x0u,0x360b2u,0x0u,0x80000000u,0x0u,0x36092u,0x0u,0x80000000u,0x0u,0x36092u,0x0u,0x80000000u,0x0u,0x36092u,0x0u,0x80000000u,0x0u,0x36092u,0x0u,0x80000000u,0x0u,0x36092u,0x0u,0x80000000u,0x0u,0x36092u,0x0u,0x80000000u,0x0u,0x36092u,0x0u,0x80000000u,0x0u,0x36092u,0x0u,0x80000000u,0x0u,0x3609au,0x0u,0x80000000u,0x0u,0x3609au,0x0u,0x80000000u,0x0u,0x3609au,0x0u,0x80000000u,0x0u,0x3609au,0x0u,0x80000000u,0x0u,0x3609au,0x0u,0x80000000u,0x0u,0x3609au,0x0u,0x80000000u,0x0u,0x3609au,0x0u,0x80000000u,0x0u,0x3609au,0x0u,0x80000000u,0x0u,0x36059u,0x0u,0x80000000u,0x0u,0x36059u,0x0u,0x80000000u,0x0u,0x36059u,0x0u,0x80000000u,0x0u,0x36059u,0x0u,0x80000000u,0x0u,0x36059u,0x0u,0x80000000u,0x0u,0x36059u,0x0u,0x80000000u,0x0u,0x36059u,0x0u,0x80000000u,0x0u,0x36059u,0x0u,0x80000000u,0x0u,0x36059u,0x0u,0x80000000u,0x0u,0x36059u,0x0u,0x80000000u,0x0u,0x36059u,0x0u,0x80000000u,0x0u,0x36059u,0x0u,0x80000000u,0x0u,0x36059u,0x0u,0x80000000u,0x0u,0x36059u,0x0u,0x80000000u,0x0u,0x36059u,0x0u,0x80000000u,0x0u,0x36059u,0x0u,0x80000000u,0x0u,0x16071u,0x0u,0x80000000u,0x0u,0x16071u,0x0u,0x80000000u,0x0u,0x16071u,0x0u,0x80000000u,0x0u,0x16071u,0x0u,0x80000000u,0x0u,0x16071u,0x0u,0x80000000u,0x0u,0x16071u,0x0u,0x80000000u,0x0u,0x16071u,0x0u,0x80000000u,0x0u,0x16071u,0x0u,0x80000000u,0x0u,0x14071u,0x0u,0x80000000u,0x0u,0x14071u,0x0u,0x80000000u,0x0u,0x14071u,0x0u,0x80000000u,0x0u,0x14071u,0x0u,0x80000000u,0x0u,0x14071u,0x0u,0x80000000u,0x0u,0x14071u,0x0u,0x80000000u,0x0u,0x14071u,0x0u,0x80000000u,0x0u,0x14071u};

__global__ __launch_bounds__(256, 1) void swin_main(const uint16_t* x, const uint16_t* mask, char* ws, uint16_t* out,
                                                    const uint16_t* wqkv, const uint16_t* wo, const uint16_t* ls,
                                                    const uint16_t* w1, const uint16_t* w2, const uint16_t* rc,
                                                    const int* ridx) {
  __shared__ __attribute__((aligned(16))) char smem[LDS_BYTES];
  const int wave = __builtin_amdgcn_readfirstlane(threadIdx.x >> 6);
  const unsigned lds0 = (unsigned)(size_t)smem;
  asm volatile(
      ".p2align 5\n"
      "s_mov_b64 s[92:93], %0\n"
      "s_mov_b32 s40, %1\n"
      "s_mov_b32 s41, %2\n"
      "s_mov_b64 s[88:89], %3\n"
      "s_load_dwordx2 s[16:17], s[92:93], 0\n"
      "s_load_dwordx2 s[20:21], s[92:93], 32\n"
      "s_load_dwordx2 s[46:47], s[92:93], 40\n"
      "s_load_dwordx8 s[80:87], s[92:93], 48\n"
      "s_load_dwordx2 s[76:77], s[92:93], 80\n"
      "s_mov_b64 s[90:91], s[92:93]\n"
      "s_getpc_b64 s[98:99]\n"
      "L_ipf0_%=:\n"
      "v_mbcnt_lo_u32_b32 v236, -1, 0\n"
      "v_mbcnt_hi_u32_b32 v236, -1, v236\n"
      "v_lshlrev_b32 v8, 2, v236\n"
      "v_lshlrev_b32_e64 v205, 4, v236\n"
      "s_lshr_b32 s68, s41, 3\n"
      "s_mov_b32 s72, 0x3100119e\n"
      "s_mov_b32 s73, 0xc0bee051\n"
      "s_mov_b32 s70, 0x317e119e\n"
      "s_mov_b32 s71, 0xc06fe051\n"
      "s_and_b32 s92, s41, 7\n"
      "s_cmp_eq_u32 s92, 6\n"
      "s_cselect_b32 s93, 2, 17\n"
      "s_cmp_lt_u32 s68, s93\n"
      "s_cselect_b64 s[72:73], s[72:73], s[70:71]\n"
      "s_lshl_b32 s92, s92, 3\n"
      "s_lshr_b64 s[72:73], s[72:73], s92\n"
      "s_add_u32 s41, s72, s68\n"
      "s_and_b32 s41, s41, 255\n"
      "s_movk_i32 s96, 5073\n"
      "s_lshr_b32 s92, s41, 5\n"
      "s_cmp_eq_u32 s92, 6\n"
      "s_cselect_b32 s96, 4992, s96\n"
      "s_cmp_lt_u32 s41, 143\n"
      "s_cselect_b32 s96, 5024, s96\n"
      "s_cmp_lt_u32 s41, 192\n"
      "s_cselect_b32 s44, 20, 19\n"
      "s_cmp_lt_u32 s41, 17\n"
      "s_cselect_b32 s44, 21, s44\n"
      "s_cselect_b32 s96, 5167, s96\n"
      "s_add_u32 s96, s96, s41\n"
      "s_lshl_b32 s92, s40, 8\n"
      "v_add_u32_e64 v68, s92, v8\n"
      "v_min_u32 v29, 704, v68\n"
      "v_add_u32 v29, 4096, v29\n"
      "v_min_u32 v68, 672, v68\n"
      "v_add_u32 v154, 122112, v68\n"
      "s_lshl_b32 s92, s68, 7\n"
      "v_lshlrev_b32 v237, 7, v236\n"
      "s_mul_i32 s93, s92, 18\n"
      "s_mul_i32 s94, s40, 640\n"
      "v_min_u32 v238, 512, v237\n"
      "v_add_u32 v238, s94, v238\n"
      "v_min_u32 v238, 2176, v238\n"
      "v_add_u32 v238, s93, v238\n"
      "s_cmp_eq_u32 s40, 1\n"
      "s_cselect_b32 s93, 4096, 0\n"
      "s_add_u32 s92, s92, s93\n"
      "s_cmp_eq_u32 s40, 2\n"
      "s_cselect_b32 s93, 9, 8\n"
      "s_cselect_b64 s[72:73], s[98:99], s[88:89]\n"
      "s_cmp_lt_u32 s40, 2\n"
      "s_cselect_b32 s93, 27, s93\n"
      "s_mul_i32 s92, s92, s93\n"
      "s_lshl_b32 s93, s93, 7\n"
      "s_sub_u32 s93, s93, 128\n"
      "v_min_u32 v237, s93, v237\n"
      "v_add_u32 v237, s92, v237\n"
      "s_lshl_b32 s95, s40, 3\n"
      "v_lshrrev_b32 v239, 3, v236\n"
      "v_and_b32 v138, 7, v236\n"
      "v_add_u32 v139, s95, v239\n"
      "v_bfe_u32 v141, v139, 1, 3\n"
      "v_cmp_lt_u32 s[70:71], s44, v139\n"
      "v_min_u32_e64 v139, s44, v139\n"
      "s_mov_b32 s18, 100270464\n"
      "s_mov_b32 s19, 0x20000\n"
      "s_mov_b32 s78, 9604\n"
      "s_mov_b32 s79, 0x20000\n"
      "s_mov_b32 s22, 221184\n"
      "s_mov_b32 s23, 0x20000\n"
      "s_lshl_b32 s64, s40, 10\n"
      "v_add_u32 v36, s64, v205\n"
      "s_add_u32 s75, s64, 18432\n"
      "s_and_b32 s92, s40, 1\n"
      "s_lshl_b32 s92, s92, 2\n"
      "s_mul_i32 s93, s40, 9216\n"
      "v_and_b32 v140, 2, v239\n"
      "v_xor_b32 v140, v140, v138\n"
      "v_xor_b32 v140, s92, v140\n"
      "v_lshl_add_u32 v140, v140, 4, s93\n"
      "v_mul_u32_u24 v97, 1152, v239\n"
      "v_add_u32_e64 v97, v97, v140\n"
      "s_cmp_lt_u32 s40, 2\n"
      "s_cselect_b32 s65, s64, 163840\n"
      "s_waitcnt lgkmcnt(0)\n"
      "s_cselect_b64 s[72:73], s[20:21], s[72:73]\n"
      "global_load_dword v68, v68, s[86:87]\n"
      "global_load_dword a255, v237, s[72:73]\n"
      "global_load_dword a253, v238, s[46:47]\n"
      "s_mul_i32 s92, s40, 8192\n"
      "s_add_u32 s88, s88, s92\n"
      "s_addc_u32 s89, s89, 0\n"
      "s_add_u32 m0, s64, 124928\n"
      "s_movk_i32 s92, 0\n"
      "buffer_load_dwordx4 v36, s[76:79], s92 offen  lds\n"
      "s_add_u32 m0, s64, 129024\n"
      "s_movk_i32 s92, 4096\n"
      "buffer_load_dwordx4 v36, s[76:79], s92 offen  lds\n"
      "s_add_u32 m0, s64, 133120\n"
      "s_cmp_lt_u32 s40, 2\n"
      "s_cselect_b32 m0, m0, s65\n"
      "s_movk_i32 s92, 8192\n"
      "buffer_load_dwordx4 v36, s[76:79], s92 offen  lds\n"
      "s_load_dwordx16 s[0:15], s[82:83], 0\n"
      "s_load_dwordx16 s[48:63], s[84:85], 0\n"
      "s_load_dwordx16 s[24:39], s[84:85], 64\n"
      "s_load_dwordx2 s[66:67], s[80:81], 0\n"
      "s_movk_i32 s93, 5041\n"
      "s_cmp_lt_u32 s41, 224\n"
      "s_cselect_b32 s92, 192, 0\n"
      "s_cselect_b32 s93, 1184, s93\n"
      "s_cselect_b32 s94, 18, 17\n"
      "s_add_u32 s93, s93, s41\n"
      "v_lshl_add_u32 v238, v139, 8, s41\n"
      "v_mul_u32_u24 v237, s92, v139\n"
      "v_add_u32 v237, s93, v237\n"
      "v_cmp_lt_u32 vcc, s94, v139\n"
      "v_mov_b32_e64 v140, s96\n"
      "s_nop 0\n"
      "v_cndmask_b32 v238, v238, v237, vcc\n"
      "v_cmp_eq_u32 vcc, s44, v139\n"
      "s_nop 1\n"
      "v_cndmask_b32 v238, v238, v140, vcc\n"
      "v_subrev_u32 v140, 5184, v238\n"
      "v_cmp_le_u32 vcc, 5184, v238\n"
      "v_mul_u32_u24 v234, 58255, v238\n"
      "v_lshrrev_b32 v234, 22, v234\n"
      "v_mul_u32_u24 v69, 72, v234\n"
      "v_sub_u32 v69, v238, v69\n"
      "v_min_u32 v237, 72, v140\n"
      "v_cndmask_b32 v234, v234, v237, vcc\n"
      "v_subrev_u32 v237, 72, v140\n"
      "v_min_u32 v237, 72, v237\n"
      "v_cndmask_b32 v69, v69, v237, vcc\n"
      "v_mad_u32_u24 v237, v234, 7, 9\n"
      "v_subrev_u32 v238, 0x1ff, v237\n"
      "v_min_u32 v237, v237, v238\n"
      "v_mad_u32_u24 v238, v69, 7, 9\n"
      "v_subrev_u32 v140, 0x1ff, v238\n"
      "v_min_u32 v238, v238, v140\n"
      "v_mul_u32_u24 v237, 0x1ff, v237\n"
      "v_add_u32 v237, v237, v238\n"
      "v_mul_u32_u24 v238, 0x180, v237\n"
      "v_bfrev_b32 v139, 1\n"
      "v_cndmask_b32 v238, v238, v139, s[70:71]\n"
      "v_xor_b32 v141, v141, v138\n"
      "v_lshl_add_u32 v238, v141, 4, v238\n"
      "s_add_u32 m0, s64, 150784\n"
      "s_nop 0\n"
      "buffer_load_dwordx4 v238, s[16:19], 0 offen  lds\n"
      "s_add_u32 m0, s64, 154752\n"
      "s_nop 0\n"
      "buffer_load_dwordx4 v238, s[16:19], 0 offen offset:128  lds\n"
      "s_add_u32 m0, s64, 158720\n"
      "s_nop 0\n"
      "buffer_load_dwordx4 v238, s[16:19], 0 offen offset:256  lds\n"
      "s_movk_i32 s95, 504\n"
      "s_and_b32 s92, s40, 1\n"
      "s_lshl_b32 s92, s92, 2\n"
      "v_lshrrev_b32 v238, 1, v239\n"
      "v_xor_b32 v238, v238, v138\n"
      "v_xor_b32 v238, s92, v238\n"
      "v_lshlrev_b32_e64 v238, 4, v238\n"
      "s_lshl_b32 s92, s40, 3\n"
      "s_brev_b32 s43, 1\n"
      "s_cmp_lt_u32 s40, 2\n"
      "s_cselect_b32 s93, 32, -16\n"
      "s_cselect_b32 s94, 0, 128\n"
      "s_cselect_b32 s43, 256, s43\n"
      "v_add_u32 v237, s92, v239\n"
      "v_mul_u32_u24 v239, 37, v237\n"
      "v_lshrrev_b32_e64 v239, 8, v239\n"
      "v_mad_u32_u24 v239, v239, s95, v237\n"
      "v_mul_u32_u24 v239, 384, v239\n"
      "v_add_u32 v16, v239, v238\n"
      "v_add_u32 v239, s93, v237\n"
      "v_mul_u32_u24 v138, 37, v239\n"
      "v_lshrrev_b32 v138, 8, v138\n"
      "v_mad_u32_u24 v138, v138, s95, v239\n"
      "v_mul_u32_u24 v138, 384, v138\n"
      "v_add3_u32 v17, v138, v238, s94\n"
      "v_add_u32 v239, 16, v237\n"
      "v_mul_u32_u24 v237, 37, v239\n"
      "v_lshrrev_b32 v237, 8, v237\n"
      "v_mad_u32_u24 v237, v237, s95, v239\n"
      "v_mul_u32_u24 v237, 384, v237\n"
      "s_movk_i32 s94, 128\n"
      "v_add3_u32 v18, v237, v238, s94\n"
      "v_add_u32 v19, 256, v16\n"
      "v_add_u32 v15, s43, v17\n"
      "s_add_u32 s92, s41, 0\n"
      "s_mul_i32 s93, s92, 58255\n"
      "s_lshr_b32 s93, s93, 22\n"
      "s_mul_i32 s93, s93, 439\n"
      "s_add_u32 s45, s92, s93\n"
      "s_mul_i32 s45, s45, 2688\n"
      "s_add_u32 s45, s45, 589824\n"
      "s_add_u32 m0, s64, 0\n"
      "s_nop 0\n"
      "buffer_load_dwordx4 v16, s[16:19], s45 offen  lds\n"
      "s_add_u32 m0, s64, 4096\n"
      "s_nop 0\n"
      "buffer_load_dwordx4 v17, s[16:19], s45 offen  lds\n"
      "s_add_u32 m0, s64, 8192\n"
      "s_nop 0\n"
      "buffer_load_dwordx4 v18, s[16:19], s45 offen  lds\n"
      "s_add_u32 m0, s64, 12288\n"
      "s_nop 0\n"
      "buffer_load_dwordx4 v19, s[16:19], s45 offen  lds\n"
      "s_add_u32 m0, s65, 16384\n"
      "s_nop 0\n"
      "buffer_load_dwordx4 v15, s[16:19], s45 offen  lds\n"
      "s_add_u32 s92, s41, 0\n"
      "s_mul_i32 s93, s92, 58255\n"
      "s_lshr_b32 s93, s93, 22\n"
      "s_add_u32 s47, s92, s93\n"
      "s_mul_i32 s47, s47, 4802\n"
      "s_and_b32 s47, s47, -4\n"
      "s_add_u32 s92, s41, 256\n"
      "s_mul_i32 s93, s92, 58255\n"
      "s_lshr_b32 s93, s93, 22\n"
      "s_add_u32 s74, s92, s93\n"
      "s_mul_i32 s74, s74, 4802\n"
      "s_and_b32 s74, s74, -4\n"
      "s_mul_i32 s93, s93, 439\n"
      "s_add_u32 s46, s92, s93\n"
      "s_mul_i32 s46, s46, 2688\n"
      "s_add_u32 s46, s46, 589824\n"
      "v_subrev_u32 v237, 2, v236\n"
      "v_cmp_ge_u32 s[72:73], s44, v237\n"
      "v_min_u32 v237, s44, v237\n"
      "s_movk_i32 s93, 5041\n"
      "s_cmp_lt_u32 s41, 224\n"
      "s_cselect_b32 s92, 192, 0\n"
      "s_cselect_b32 s93, 1184, s93\n"
      "s_cselect_b32 s94, 18, 17\n"
      "s_add_u32 s93, s93, s41\n"
      "v_lshl_add_u32 v238, v237, 8, s41\n"
      "v_mul_u32_u24 v239, s92, v237\n"
      "v_add_u32 v239, s93, v239\n"
      "v_cmp_lt_u32 vcc, s94, v237\n"
      "v_mov_b32 v138, s96\n"
      "s_nop 0\n"
      "v_cndmask_b32 v238, v238, v239, vcc\n"
      "v_cmp_eq_u32 vcc, s44, v237\n"
      "s_nop 1\n"
      "v_cndmask_b32 v238, v238, v138, vcc\n"
      "v_subrev_u32 v138, 5184, v238\n"
      "v_cmp_le_u32 vcc, 5184, v238\n"
      "v_mul_u32_u24 v141, 58255, v238\n"
      "v_lshrrev_b32 v141, 22, v141\n"
      "v_mul_u32_u24 v139, 72, v141\n"
      "v_sub_u32 v139, v238, v139\n"
      "v_min_u32 v239, 72, v138\n"
      "v_cndmask_b32 v141, v141, v239, vcc\n"
      "v_subrev_u32 v239, 72, v138\n"
      "v_min_u32 v239, 72, v239\n"
      "v_cndmask_b32 v139, v139, v239, vcc\n"
      "v_mul_u32_u24 v38, 73, v141\n"
      "v_add_u32 v38, v38, v139\n"
      "v_mul_u32_u24 v38, 4802, v38\n"
      "v_mul_u32_u24 v237, 0x1ff, v141\n"
      "v_add_u32 v237, v237, v139\n"
      "v_mul_u32_u24 v237, 2688, v237\n"
      "v_add_u32 v237, 589824, v237\n"
      "v_cmp_eq_u32 vcc, 72, v141\n"
      "v_or_b32 v69, 0x80000000, v237\n"
      "s_nop 0\n"
      "v_cndmask_b32 v237, v237, v69, vcc\n"
      "v_cmp_eq_u32 vcc, 72, v139\n"
      "v_or_b32_e64 v69, 0x40000000, v237\n"
      "s_nop 0\n"
      "v_cndmask_b32 v237, v237, v69, vcc\n"
      "v_mov_b32 v239, 0x20000000\n"
      "v_cndmask_b32 v237, v239, v237, s[72:73]\n"
      "v_cndmask_b32 v38, v239, v38, s[72:73]\n"
      "v_mov_b32 v37, v239\n"
      "s_nop 1\n"
      "v_mov_b32_dpp v37, v237 wave_shr:1 row_mask:0xf bank_mask:0xf\n"
      "v_mov_b32 v79, 0\n"
      "v_mov_b32 v83, 0\n"
      "v_mov_b32 v87, 0\n"
      "v_mov_b32 v91, 0\n"
      "v_mov_b32 v95, 0\n"
      "s_lshl_b32 s94, s40, 2\n"
      "s_add_u32 s94, s94, 115456\n"
      "v_add_u32 v31, s94, v205\n"
      "v_and_b32 v69, 3, v236\n"
      "v_cmp_ne_u32 vcc, 0, v69\n"
      "v_mov_b32 v69, 0x3f803f80\n"
      "s_nop 0\n"
      "v_cndmask_b32_e64 v32, v69, 0, vcc\n"
      "v_mov_b32 v33, v32\n"
      "v_mov_b32 v34, v32\n"
      "v_mov_b32 v35, v32\n"
      "v_and_b32 v115, 15, v236\n"
      "v_lshrrev_b32_e64 v66, 4, v236\n"
      "v_mul_u32_u24 v115, 196, v115\n"
      "v_add_u32 v115, 124928, v115\n"
      "v_lshl_add_u32 v66, v66, 4, v115\n"
      "v_min_u32 v190, 192, v8\n"
      "v_add_u32 v190, 124928, v190\n"
      "global_load_dwordx4 v[236:239], v205, s[88:89]\n"
      "s_waitcnt lgkmcnt(0)\n"
      "s_pack_ll_b32_b16 s92, s0, s8\n"
      "s_waitcnt vmcnt(14)\n"
      "v_dot2_f32_bf16 v234, s92, v68, 0\n"
      "s_pack_hh_b32_b16 s93, s0, s8\n"
      "v_dot2_f32_bf16 v157, s93, v68, 0\n"
      "s_pack_ll_b32_b16 s94, s1, s9\n"
      "v_dot2_f32_bf16 v232, s94, v68, 0\n"
      "s_pack_hh_b32_b16 s95, s1, s9\n"
      "v_dot2_f32_bf16 v235, s95, v68, 0\n"
      "s_pack_ll_b32_b16 s92, s2, s10\n"
      "v_dot2_f32_bf16 v233, s92, v68, 0\n"
      "s_pack_hh_b32_b16 s93, s2, s10\n"
      "v_dot2_f32_bf16 v156, s93, v68, 0\n"
      "s_pack_ll_b32_b16 s94, s3, s11\n"
      "v_dot2_f32_bf16 v155, s94, v68, 0\n"
      "s_pack_hh_b32_b16 s95, s3, s11\n"
      "v_dot2_f32_bf16 v132, s95, v68, 0\n"
      "s_pack_ll_b32_b16 s92, s4, s12\n"
      "v_dot2_f32_bf16 v214, s92, v68, 0\n"
      "s_pack_hh_b32_b16 s93, s4, s12\n"
      "v_dot2_f32_bf16 v133, s93, v68, 0\n"
      "s_pack_ll_b32_b16 s94, s5, s13\n"
      "v_dot2_f32_bf16 v213, s94, v68, 0\n"
      "global_load_dwordx4 v[138:141], v205, s[88:89] offset:1024\n"
      "s_pack_hh_b32_b16 s95, s5, s13\n"
      "v_dot2_f32_bf16 v196, s95, v68, 0\n"
      "s_pack_ll_b32_b16 s92, s6, s14\n"
      "v_dot2_f32_bf16 v131, s92, v68, 0\n"
      "s_pack_hh_b32_b16 s93, s6, s14\n"
      "v_dot2_f32_bf16 v59, s93, v68, 0\n"
      "s_pack_ll_b32_b16 s94, s7, s15\n"
      "v_dot2_f32_bf16 v109, s94, v68, 0\n"
      "s_pack_hh_b32_b16 s95, s7, s15\n"
      "v_dot2_f32_bf16 v195, s95, v68, 0\n"
      "s_lshl_b32 s92, s40, 4\n"
      "s_lshr_b64 s[66:67], s[66:67], s92\n"
      "s_lshl_b32 s66, s66, 16\n"
      "v_mov_b32 v68, s66\n"
      "v_max_f32 v68, 0xc2c80000, v68\n"
      "v_min_f32 v68, 0x40930000, v68\n"
      "v_mul_f32 v68, 0x3fb8aa3b, v68\n"
      "v_exp_f32 v68, v68\n"
      "s_nop 0\n"
      "v_cvt_pk_bf16_f32 v68, v68, v68\n"
      "v_lshlrev_b32 v68, 16, v68\n"
      "v_max_f32 v234, 0, v234\n"
      "v_max_f32_e64 v157, 0, v157\n"
      "global_load_dwordx4 v[0:3], v205, s[88:89] offset:2048\n"
      "v_cvt_pk_bf16_f32 v234, v234, v157\n"
      "v_max_f32 v232, 0, v232\n"
      "v_max_f32 v235, 0, v235\n"
      "v_cvt_pk_bf16_f32 v232, v232, v235\n"
      "v_max_f32 v233, 0, v233\n"
      "v_max_f32 v156, 0, v156\n"
      "v_cvt_pk_bf16_f32 v233, v233, v156\n"
      "v_max_f32 v155, 0, v155\n"
      "v_max_f32 v132, 0, v132\n"
      "v_cvt_pk_bf16_f32 v155, v155, v132\n"
      "v_max_f32 v214, 0, v214\n"
      "v_max_f32 v133, 0, v133\n"
      "v_cvt_pk_bf16_f32 v214, v214, v133\n"
      "v_max_f32 v213, 0, v213\n"
      "v_max_f32 v196, 0, v196\n"
      "v_cvt_pk_bf16_f32 v213, v213, v196\n"
      "v_max_f32 v131, 0, v131\n"
      "v_max_f32 v59, 0, v59\n"
      "v_cvt_pk_bf16_f32 v131, v131, v59\n"
      "v_max_f32 v109, 0, v109\n"
      "v_max_f32 v195, 0, v195\n"
      "v_cvt_pk_bf16_f32 v109, v109, v195\n"
      "s_mov_b32 s42, 0\n"
      "s_add_u32 m0, s75, 0\n"
      "s_nop 0\n"
      "buffer_load_dwordx4 v97, s[20:23], s42 offen  lds\n"
      "s_pack_ll_b32_b16 s92, s48, s50\n"
      "v_dot2_f32_bf16 v235, s92, v234, 0\n"
      "s_pack_hh_b32_b16 s93, s48, s50\n"
      "v_dot2_f32_bf16 v156, s93, v234, 0\n"
      "s_pack_ll_b32_b16 s94, s49, s51\n"
      "v_dot2_f32_bf16 v157, s94, v234, 0\n"
      "s_pack_hh_b32_b16 s95, s49, s51\n"
      "v_dot2_f32_bf16 v133, s95, v234, 0\n"
      "s_pack_ll_b32_b16 s92, s52, s54\n"
      "v_dot2_f32_bf16 v235, s92, v232, v235\n"
      "s_pack_hh_b32_b16 s93, s52, s54\n"
      "v_dot2_f32_bf16 v156, s93, v232, v156\n"
      "s_pack_ll_b32_b16 s94, s53, s55\n"
      "v_dot2_f32_bf16 v157, s94, v232, v157\n"
      "s_pack_hh_b32_b16 s95, s53, s55\n"
      "v_dot2_f32_bf16 v133, s95, v232, v133\n"
      "s_pack_ll_b32_b16 s92, s56, s58\n"
      "v_dot2_f32_bf16 v235, s92, v233, v235\n"
      "s_pack_hh_b32_b16 s93, s56, s58\n"
      "v_dot2_f32_bf16 v156, s93, v233, v156\n"
      "s_pack_ll_b32_b16 s94, s57, s59\n"
      "v_dot2_f32_bf16 v157, s94, v233, v157\n"
      "s_add_u32 m0, s75, 3968\n"
      "s_nop 0\n"
      "buffer_load_dwordx4 v97, s[20:23], s42 offen offset:128  lds\n"
      "s_pack_hh_b32_b16 s95, s57, s59\n"
      "v_dot2_f32_bf16 v133, s95, v233, v133\n"
      "s_pack_ll_b32_b16 s92, s60, s62\n"
      "v_dot2_f32_bf16 v235, s92, v155, v235\n"
      "s_pack_hh_b32_b16 s93, s60, s62\n"
      "v_dot2_f32_bf16 v156, s93, v155, v156\n"
      "s_pack_ll_b32_b16 s94, s61, s63\n"
      "v_dot2_f32_bf16 v157, s94, v155, v157\n"
      "s_pack_hh_b32_b16 s95, s61, s63\n"
      "v_dot2_f32_bf16 v133, s95, v155, v133\n"
      "s_pack_ll_b32_b16 s92, s24, s26\n"
      "v_dot2_f32_bf16 v235, s92, v214, v235\n"
      "s_pack_hh_b32_b16 s93, s24, s26\n"
      "v_dot2_f32_bf16 v156, s93, v214, v156\n"
      "s_pack_ll_b32_b16 s94, s25, s27\n"
      "v_dot2_f32_bf16 v157, s94, v214, v157\n"
      "s_pack_hh_b32_b16 s95, s25, s27\n"
      "v_dot2_f32_bf16 v133, s95, v214, v133\n"
      "s_pack_ll_b32_b16 s92, s28, s30\n"
      "v_dot2_f32_bf16 v235, s92, v213, v235\n"
      "s_pack_hh_b32_b16 s93, s28, s30\n"
      "v_dot2_f32_bf16 v156, s93, v213, v156\n"
      "s_pack_ll_b32_b16 s94, s29, s31\n"
      "s_add_u32 m0, s75, 7936\n"
      "s_nop 0\n"
      "buffer_load_dwordx4 v97, s[20:23], s42 offen offset:256  lds\n"
      "v_dot2_f32_bf16 v157, s94, v213, v157\n"
      "s_pack_hh_b32_b16 s95, s29, s31\n"
      "v_dot2_f32_bf16 v133, s95, v213, v133\n"
      "s_pack_ll_b32_b16 s92, s32, s34\n"
      "v_dot2_f32_bf16 v235, s92, v131, v235\n"
      "s_pack_hh_b32_b16 s93, s32, s34\n"
      "v_dot2_f32_bf16 v156, s93, v131, v156\n"
      "s_pack_ll_b32_b16 s94, s33, s35\n"
      "v_dot2_f32_bf16 v157, s94, v131, v157\n"
      "s_pack_hh_b32_b16 s95, s33, s35\n"
      "v_dot2_f32_bf16 v133, s95, v131, v133\n"
      "s_pack_ll_b32_b16 s92, s36, s38\n"
      "v_dot2_f32_bf16 v235, s92, v109, v235\n"
      "s_pack_hh_b32_b16 s93, s36, s38\n"
      "v_dot2_f32_bf16 v156, s93, v109, v156\n"
      "s_pack_ll_b32_b16 s94, s37, s39\n"
      "v_dot2_f32_bf16 v157, s94, v109, v157\n"
      "s_pack_hh_b32_b16 s95, s37, s39\n"
      "v_dot2_f32_bf16 v133, s95, v109, v133\n"
      "s_load_dwordx2 s[28:29], s[90:91], 8\n"
      "s_load_dwordx2 s[34:35], s[90:91], 16\n"
      "s_load_dwordx2 s[24:25], s[90:91], 24\n"
      "s_add_u32 m0, s75, 11904\n"
      "s_nop 0\n"
      "buffer_load_dwordx4 v97, s[20:23], s42 offen offset:384  lds\n"
      "s_load_dwordx2 s[36:37], s[90:91], 40\n"
      "s_mov_b32 s26, 100270464\n"
      "s_mov_b32 s27, 0x20000\n"
      "s_mov_b32 s38, 73728\n"
      "s_mov_b32 s39, 0x20000\n"
      "s_mov_b32 s30, 25589860\n"
      "s_mov_b32 s31, 0x20000\n"
      "s_mov_b32 s53, 0x3fb8aa3b\n"
      "s_mov_b32 s51, 0x2a8cbccc\n"
      "s_mov_b32 s52, 0x38fba882\n"
      "s_mov_b32 s58, 0xffff0000\n"
      "s_mov_b32 s59, 0x0\n"
      "s_mov_b32 s60, 0x0\n"
      "s_mov_b32 s61, 0xffff\n"
      "s_mov_b32 s62, 0x0\n"
      "s_mov_b32 s63, 0xffff0000\n"
      "v_mov_b32 v69, 0x41f00000\n"
      "v_cmp_nle_f32 s[54:55], v68, v69\n"
      "v_mul_f32 v68, s53, v68\n"
      "s_nop 0\n"
      "v_readfirstlane_b32 s50, v68\n"
      "v_cvt_pk_bf16_f32 v235, v235, v235\n"
      "v_lshlrev_b32 v235, 16, v235\n"
      "s_add_u32 m0, s75, 15872\n"
      "s_nop 0\n"
      "buffer_load_dwordx4 v97, s[20:23], s42 offen offset:512  lds\n"
      "v_mul_f32 v235, 0xbfb8aa3b, v235\n"
      "v_exp_f32 v235, v235\n"
      "s_nop 0\n"
      "v_add_f32 v235, 1.0, v235\n"
      "v_rcp_f32 v235, v235\n"
      "s_nop 0\n"
      "v_cvt_pk_bf16_f32 v235, v235, v235\n"
      "v_lshlrev_b32 v235, 16, v235\n"
      "v_add_f32 v235, -0.5, v235\n"
      "v_mul_f32 v235, 0x41b8aa3b, v235\n"
      "ds_write_b32 v154, v235\n"
      "v_cvt_pk_bf16_f32 v156, v156, v156\n"
      "v_lshlrev_b32 v156, 16, v156\n"
      "v_mul_f32 v156, 0xbfb8aa3b, v156\n"
      "v_exp_f32 v156, v156\n"
      "s_nop 0\n"
      "v_add_f32 v156, 1.0, v156\n"
      "v_rcp_f32_e64 v156, v156\n"
      "s_nop 0\n"
      "v_cvt_pk_bf16_f32 v156, v156, v156\n"
      "v_lshlrev_b32 v156, 16, v156\n"
      "v_add_f32 v156, -0.5, v156\n"
      "v_mul_f32 v156, 0x41b8aa3b, v156\n"
      "ds_write_b32 v154, v156 offset:704\n"
      "v_cvt_pk_bf16_f32 v157, v157, v157\n"
      "v_lshlrev_b32 v157, 16, v157\n"
      "s_add_u32 m0, s75, 19840\n"
      "s_nop 0\n"
      "buffer_load_dwordx4 v97, s[20:23], s42 offen offset:640  lds\n"
      "v_mul_f32 v157, 0xbfb8aa3b, v157\n"
      "v_exp_f32 v157, v157\n"
      "s_nop 0\n"
      "v_add_f32 v157, 1.0, v157\n"
      "v_rcp_f32_e64 v157, v157\n"
      "s_nop 0\n"
      "v_cvt_pk_bf16_f32 v157, v157, v157\n"
      "v_lshlrev_b32 v157, 16, v157\n"
      "v_add_f32 v157, -0.5, v157\n"
      "v_mul_f32 v157, 0x41b8aa3b, v157\n"
      "ds_write_b32 v154, v157 offset:1408\n"
      "v_cvt_pk_bf16_f32 v133, v133, v133\n"
      "v_lshlrev_b32 v133, 16, v133\n"
      "v_mul_f32 v133, 0xbfb8aa3b, v133\n"
      "v_exp_f32_e64 v133, v133\n"
      "s_nop 0\n"
      "v_add_f32 v133, 1.0, v133\n"
      "v_rcp_f32_e64 v133, v133\n"
      "s_nop 0\n"
      "v_cvt_pk_bf16_f32 v133, v133, v133\n"
      "v_lshlrev_b32_e64 v133, 16, v133\n"
      "v_add_f32_e64 v133, -0.5, v133\n"
      "v_mul_f32 v133, 0x41b8aa3b, v133\n"
      "ds_write_b32 v154, v133 offset:2112\n"
      "s_mul_i32 s76, s40, 704\n"
      "s_add_u32 s76, s76, 122112\n"
      "s_mov_b32 s43, 36864\n"
      "s_add_u32 m0, s75, 24576\n"
      "s_nop 0\n"
      "buffer_load_dwordx4 v97, s[20:23], s43 offen  lds\n"
      "s_add_u32 m0, s75, 28544\n"
      "s_nop 0\n"
      "buffer_load_dwordx4 v97, s[20:23], s43 offen offset:128  lds\n"
      "s_add_u32 m0, s75, 32512\n"
      "s_nop 0\n"
      "buffer_load_dwordx4 v97, s[20:23], s43 offen offset:256  lds\n"
      "s_add_u32 m0, s75, 36480\n"
      "s_nop 0\n"
      "buffer_load_dwordx4 v97, s[20:23], s43 offen offset:384  lds\n"
      "s_add_u32 m0, s75, 40448\n"
      "s_nop 0\n"
      "buffer_load_dwordx4 v97, s[20:23], s43 offen offset:512  lds\n"
      "s_add_u32 m0, s75, 44416\n"
      "s_nop 0\n"
      "buffer_load_dwordx4 v97, s[20:23], s43 offen offset:640  lds\n"
      "s_mov_b32 s45, s46\n"
      "s_lshl_b32 s9, s68, 1\n"
      "s_add_u32 s9, s9, s40\n"
      "s_mul_i32 s9, s9, 640\n"
      "s_mov_b32 s49, s47\n"
      "s_mov_b32 s66, 0\n"
      "s_mov_b32 s67, 0\n"
      "s_mov_b32 s68, 0\n"
      "s_mov_b32 s69, 0\n"
      "s_mov_b32 s70, 0\n"
      "s_mov_b32 s71, 0\n"
      "s_mov_b32 s48, 0\n"
      "s_mov_b32 s33, 0x7fff7fff\n"
      "s_mov_b32 s42, 1\n"
      "s_mov_b32 s43, 5\n"
      "v_readlane_b32 s47, v38, 4\n"
      "s_nop 0\n"
      "s_and_b32 s47, s47, -4\n"
      "s_waitcnt vmcnt(12)\n"
      "v_add_u32 v135, 150784, v0\n"
      "v_add_u32 v184, 150784, v1\n"
      "v_add_u32 v85, 49152, v236\n"
      "v_add_u32 v84, 49152, v237\n"
      "v_add_u32 v174, 49152, v238\n"
      "v_add_u32 v82, 49152, v239\n"
      "v_add_u32 v78, 49152, v138\n"
      "s_waitcnt vmcnt(6)\n"
      "s_waitcnt lgkmcnt(0)\n"
      "s_barrier\n"
      "s_add_u32 m0, s75, 49152\n"
      "s_mov_b32 s78, 73728\n"
      "buffer_load_dwordx4 v97, s[20:23], s78 offen  lds\n"
      "s_add_u32 m0, s75, 53120\n"
      "ds_read_b128 a[216:219], v135\n"
      "ds_read_b64_tr_b16 a[48:49], v238\n"
      "ds_read_b128 a[220:223], v0\n"
      "ds_read_b64_tr_b16 a[2:3], v236 offset:512\n"
      "ds_read_b64_tr_b16 a[72:73], v236 offset:12288\n"
      "ds_read_b64_tr_b16 a[74:75], v236 offset:12800\n"
      "buffer_load_dwordx4 v97, s[20:23], s78 offen offset:128  lds\n"
      "ds_read_b64_tr_b16 a[0:1], v236\n"
      "ds_read_b64_tr_b16 a[50:51], v238 offset:512\n"
      "ds_read_b64_tr_b16 a[24:25], v237\n"
      "v_add_u32 v178, 6272, v66\n"
      "s_add_u32 m0, s75, 57088\n"
      "ds_read_b128 a[224:227], v0 offset:2048\n"
      "ds_read_b64_tr_b16 a[26:27], v237 offset:512\n"
      "buffer_load_dwordx4 v97, s[20:23], s78 offen offset:256  lds\n"
      "ds_read2_b32 v[68:69], v178 offset0:16 offset1:17\n"
      "ds_read_b32 v223, v115 offset:3328\n"
      "ds_read_b128 a[244:247], v1 offset:4096\n"
      "ds_read_b64_tr_b16 a[120:121], v238 offset:12288\n"
      "s_add_u32 m0, s75, 61056\n"
      "ds_read_b64_tr_b16 a[122:123], v238 offset:12800\n"
      "s_waitcnt lgkmcnt(8)\n"
      "v_mfma_f32_16x16x32_bf16 v[232:235], a[48:51], a[216:219], 0\n"
      "buffer_load_dwordx4 v97, s[20:23], s78 offen offset:384  lds\n"
      "ds_read_b64_tr_b16 a[98:99], v237 offset:12800\n"
      "v_mfma_f32_16x16x32_bf16 v[154:157], a[0:3], a[220:223], 0\n"
      "s_mov_b32 s79, 110592\n"
      "v_add_u32 v74, 3136, v66\n"
      "s_waitcnt lgkmcnt(6)\n"
      "v_mfma_f32_16x16x32_bf16 v[130:133], a[24:27], a[224:227], 0\n"
      "s_add_u32 m0, s75, 65024\n"
      "ds_read_b128 a[228:231], v0 offset:4096\n"
      "ds_read_b64_tr_b16 a[96:97], v237 offset:12288\n"
      "v_mfma_f32_16x16x32_bf16 v[194:197], a[24:27], a[220:223], 0\n"
      "buffer_load_dwordx4 v97, s[20:23], s78 offen offset:512  lds\n"
      "v_mfma_f32_16x16x32_bf16 v[212:215], a[48:51], a[224:227], 0\n"
      "ds_read_b128 a[236:239], v1\n"
      "ds_read2_b32 v[58:59], v74 offset0:18 offset1:19\n"
      "v_mfma_f32_16x16x32_bf16 v[108:111], a[0:3], a[224:227], 0\n"
      "ds_read_b128 a[232:235], v184\n"
      "s_add_u32 m0, s75, 68992\n"
      "ds_read2_b32 v[70:71], v178 offset0:18 offset1:19\n"
      "v_mfma_f32_16x16x32_bf16 v[166:169], a[72:75], a[220:223], 0\n"
      "buffer_load_dwordx4 v97, s[20:23], s78 offen offset:640  lds\n"
      "v_mfma_f32_16x16x32_bf16 v[244:247], a[0:3], a[216:219], 0\n"
      "ds_read_b128 a[252:255], v0 offset:6144\n"
      "s_waitcnt lgkmcnt(5)\n"
      "v_mfma_f32_16x16x32_bf16 v[240:243], a[96:99], a[224:227], 0\n"
      "ds_read2_b32 v[72:73], v178 offset0:32 offset1:33\n"
      "ds_read_b128 a[240:243], v1 offset:2048\n"
      "v_mfma_f32_16x16x32_bf16 v[252:255], a[96:99], a[228:231], 0\n"
      "s_waitcnt vmcnt(6)\n"
      "s_barrier\n"
      "global_load_dwordx4 v[4:7], v205, s[88:89] offset:3072\n"
      "ds_read_b64_tr_b16 a[4:5], v236 offset:24576\n"
      "ds_read_b64_tr_b16 a[28:29], v237 offset:24576\n"
      "s_add_u32 s88, s88, 4096\n"
      "s_addc_u32 s89, s89, 0\n"
      "ds_read_b64_tr_b16 a[54:55], v238 offset:25088\n"
      "ds_read_b64_tr_b16 a[124:125], v238 offset:36864\n"
      "ds_read_b64_tr_b16 a[126:127], v238 offset:37376\n"
      "ds_read_b64_tr_b16 a[100:101], v237 offset:36864\n"
      "v_mfma_f32_16x16x32_bf16 v[180:183], a[72:75], a[224:227], 0\n"
      "ds_read_b64_tr_b16 a[102:103], v237 offset:37376\n"
      "global_load_dwordx4 v[8:11], v205, s[88:89]\n"
      "ds_read_b64_tr_b16 a[52:53], v238 offset:24576\n"
      "ds_read_b64_tr_b16 a[76:77], v236 offset:36864\n"
      "v_mfma_f32_16x16x32_bf16 v[150:153], a[120:123], a[224:227], 0\n"
      "ds_read_b64_tr_b16 a[30:31], v237 offset:25088\n"
      "ds_read_b64_tr_b16 a[78:79], v236 offset:37376\n"
      "ds_read_b64_tr_b16 a[6:7], v236 offset:25088\n"
      "v_mfma_f32_16x16x32_bf16 v[224:227], a[96:99], a[216:219], 0\n"
      "global_load_dwordx4 v[12:15], v205, s[88:89] offset:1024\n"
      "v_mfma_f32_16x16x32_bf16 v[248:251], a[120:123], a[216:219], 0\n"
      "ds_read2_b32 v[50:51], v66 offset0:34 offset1:35\n"
      "ds_read2_b32 v[52:53], v74 offset0:0 offset1:1\n"
      "ds_read_b32 v211, v115 offset:192\n"
      "v_mfma_f32_16x16x32_bf16 v[216:219], a[72:75], a[216:219], 0\n"
      "ds_read_b128 a[224:227], v184 offset:4096\n"
      "v_lshl_add_u32 v223, v223, 2, s76\n"
      "v_mfma_f32_16x16x32_bf16 v[100:103], a[0:3], a[228:231], 0\n"
      "global_load_dwordx4 v[20:23], v205, s[88:89] offset:2048\n"
      "ds_read_b32 v177, v190 offset:9408\n"
      "v_mfma_f32_16x16x32_bf16 v[120:123], a[120:123], a[220:223], 0\n"
      "ds_read2_b32 v[42:43], v66 offset0:2 offset1:3\n"
      "s_waitcnt lgkmcnt(6)\n"
      "v_mfma_f32_16x16x32_bf16 v[120:123], a[124:127], a[236:239], v[120:123]\n"
      "ds_read2_b32 v[62:63], v74 offset0:34 offset1:35\n"
      "ds_read_b128 a[248:251], v135 offset:4096\n"
      "v_mfma_f32_16x16x32_bf16 v[190:193], a[96:99], a[220:223], 0\n"
      "global_load_dwordx4 v[24:27], v205, s[88:89] offset:3072\n"
      "v_mfma_f32_16x16x32_bf16 v[146:149], a[48:51], a[220:223], 0\n"
      "ds_read2_b32 v[44:45], v66 offset0:16 offset1:17\n"
      "s_mov_b32 s82, 0xffff\n"
      "ds_read2_b32 v[46:47], v66 offset0:18 offset1:19\n"
      "v_mfma_f32_16x16x32_bf16 v[146:149], a[52:55], a[236:239], v[146:149]\n"
      "ds_read2_b32 v[60:61], v74 offset0:32 offset1:33\n"
      "v_lshl_add_u32 v73, v73, 2, s76\n"
      "s_add_u32 m0, s75, 73728\n"
      "v_mfma_f32_16x16x32_bf16 v[190:193], a[100:103], a[236:239], v[190:193]\n"
      "buffer_load_dwordx4 v97, s[20:23], s79 offen  lds\n"
      "v_mfma_f32_16x16x32_bf16 v[204:207], a[24:27], a[216:219], 0\n"
      "s_add_u32 m0, s75, 77696\n"
      "ds_read2_b32 v[54:55], v74 offset0:2 offset1:3\n"
      "v_mfma_f32_16x16x32_bf16 v[154:157], a[4:7], a[236:239], v[154:157]\n"
      "ds_read2_b32 v[48:49], v66 offset0:32 offset1:33\n"
      "s_mov_b32 s78, 147456\n"
      "v_mfma_f32_16x16x32_bf16 v[194:197], a[28:31], a[236:239], v[194:197]\n"
      "ds_read2_b32 v[56:57], v74 offset0:16 offset1:17\n"
      "ds_read2_b32 v[74:75], v178 offset0:34 offset1:35\n"
      "v_mfma_f32_16x16x32_bf16 v[232:235], a[52:55], a[232:235], v[232:235]\n"
      "buffer_load_dwordx4 v97, s[20:23], s79 offen offset:128  lds\n"
      "v_mfma_f32_16x16x32_bf16 v[204:207], a[28:31], a[232:235], v[204:207]\n"
      "ds_read_b32 v179, v115 offset:6464\n"
      "v_lshl_add_u32 v70, v70, 2, s76\n"
      "v_mfma_f32_16x16x32_bf16 v[112:115], a[72:75], a[228:231], 0\n"
      "ds_read2_b32 v[64:65], v178 offset0:0 offset1:1\n"
      "s_add_u32 m0, s75, 81664\n"
      "v_mfma_f32_16x16x32_bf16 v[108:111], a[4:7], a[240:243], v[108:111]\n"
      "ds_read_b128 a[216:219], v0 offset:8192\n"
      "s_mov_b32 s91, 0x1ffff\n"
      "v_mfma_f32_16x16x32_bf16 v[244:247], a[4:7], a[232:235], v[244:247]\n"
      "buffer_load_dwordx4 v97, s[20:23], s79 offen offset:256  lds\n"
      "v_mfma_f32_16x16x32_bf16 v[240:243], a[100:103], a[240:243], v[240:243]\n"
      "s_mov_b32 s83, 0x0\n"
      "s_waitcnt lgkmcnt(4)\n"
      "v_lshl_add_u32 v63, v63, 2, s76\n"
      "v_mfma_f32_16x16x32_bf16 v[216:219], a[76:79], a[232:235], v[216:219]\n"
      "s_mov_b32 s90, 0xffffffff\n"
      "v_lshl_add_u32 v177, v177, 2, s76\n"
      "v_lshl_add_u32 v46, v46, 2, s76\n"
      "v_mfma_f32_16x16x32_bf16 v[166:169], a[76:79], a[236:239], v[166:169]\n"
      "ds_read_b128 a[236:239], v1 offset:10240\n"
      "v_lshl_add_u32 v54, v54, 2, s76\n"
      "s_add_u32 m0, s75, 85632\n"
      "v_mfma_f32_16x16x32_bf16 v[116:119], a[120:123], a[228:231], 0\n"
      "buffer_load_dwordx4 v97, s[20:23], s79 offen offset:384  lds\n"
      "s_add_u32 m0, s75, 89600\n"
      "v_mfma_f32_16x16x32_bf16 v[116:119], a[124:127], a[244:247], v[116:119]\n"
      "s_waitcnt vmcnt(9)\n"
      "s_barrier\n"
      "ds_read_b64_tr_b16 a[8:9], v85\n"
      "ds_read_b64_tr_b16 a[104:105], v84 offset:12288\n"
      "ds_read_b64_tr_b16 a[128:129], v174 offset:12288\n"
      "ds_read_b64_tr_b16 a[10:11], v85 offset:512\n"
      "ds_read_b64_tr_b16 a[32:33], v84\n"
      "v_mfma_f32_16x16x32_bf16 v[170:173], a[48:51], a[228:231], 0\n"
      "ds_read_b64_tr_b16 a[56:57], v174\n"
      "ds_read_b64_tr_b16 a[34:35], v84 offset:512\n"
      "buffer_load_dwordx4 v97, s[20:23], s79 offen offset:512  lds\n"
      "ds_read_b64_tr_b16 a[80:81], v85 offset:12288\n"
      "v_mfma_f32_16x16x32_bf16 v[130:133], a[28:31], a[240:243], v[130:133]\n"
      "ds_read_b64_tr_b16 a[130:131], v174 offset:12800\n"
      "ds_read_b64_tr_b16 a[58:59], v174 offset:512\n"
      "s_add_u32 m0, s75, 93568\n"
      "ds_read_b64_tr_b16 a[106:107], v84 offset:12800\n"
      "ds_read_b64_tr_b16 a[82:83], v85 offset:12800\n"
      "buffer_load_dwordx4 v97, s[20:23], s79 offen offset:640  lds\n"
      "v_lshl_add_u32 v49, v49, 2, s76\n"
      "v_lshl_add_u32 v55, v55, 2, s76\n"
      "v_mfma_f32_16x16x32_bf16 v[228:231], a[24:27], a[228:231], 0\n"
      "ds_read_b32 v177, v177\n"
      "v_lshl_add_u32 v50, v50, 2, s76\n"
      "v_mfma_f32_16x16x32_bf16 v[224:227], a[100:103], a[232:235], v[224:227]\n"
      "ds_read2_b32 v[40:41], v66 offset0:0 offset1:1\n"
      "s_add_u32 m0, s75, 0\n"
      "v_lshl_add_u32 v71, v71, 2, s76\n"
      "v_mfma_f32_16x16x32_bf16 v[252:255], a[100:103], a[244:247], v[252:255]\n"
      "buffer_load_dwordx4 v97, s[20:23], s78 offen  lds\n"
      "v_lshl_add_u32 v211, v211, 2, s76\n"
      "v_mfma_f32_16x16x32_bf16 v[180:183], a[76:79], a[240:243], v[180:183]\n"
      "s_add_u32 m0, s75, 3968\n"
      "v_lshl_add_u32 v61, v61, 2, s76\n"
      "v_lshl_add_u32 v58, v58, 2, s76\n"
      "v_mfma_f32_16x16x32_bf16 v[212:215], a[52:55], a[240:243], v[212:215]\n"
      "ds_read_b128 a[220:223], v0 offset:10240\n"
      "s_waitcnt lgkmcnt(3)\n"
      "v_lshl_add_u32 v65, v65, 2, s76\n"
      "v_lshl_add_u32 v72, v72, 2, s76\n"
      "v_mfma_f32_16x16x32_bf16 v[248:251], a[124:127], a[232:235], v[248:251]\n"
      "buffer_load_dwordx4 v97, s[20:23], s78 offen offset:128  lds\n"
      "v_mfma_f32_16x16x32_bf16 v[248:251], a[128:131], a[248:251], v[248:251]\n"
      "ds_read_b128 a[228:231], v1 offset:6144\n"
      "v_lshl_add_u32 v69, v69, 2, s76\n"
      "v_mfma_f32_16x16x32_bf16 v[108:111], a[8:11], a[216:219], v[108:111]\n"
      "ds_read2_b32 v[66:67], v178 offset0:2 offset1:3\n"
      "v_lshl_add_u32 v43, v43, 2, s76\n"
      "v_mfma_f32_16x16x32_bf16 v[150:153], a[124:127], a[240:243], v[150:153]\n"
      "s_add_u32 m0, s75, 7936\n"
      "ds_read_b32 v73, v73\n"
      "v_mfma_f32_16x16x32_bf16 v[100:103], a[4:7], a[244:247], v[100:103]\n"
      "buffer_load_dwordx4 v97, s[20:23], s78 offen offset:256  lds\n"
      "v_mfma_f32_16x16x32_bf16 v[190:193], a[104:107], a[252:255], v[190:193]\n"
      "ds_read_b32 v72, v72\n"
      "s_waitcnt lgkmcnt(4)\n"
      "v_mfma_f32_16x16x32_bf16 v[116:119], a[128:131], a[220:223], v[116:119]\n"
      "ds_read_b128 a[232:235], v1 offset:8192\n"
      "v_lshl_add_u32 v41, v41, 2, s76\n"
      "v_mfma_f32_16x16x32_bf16 v[146:149], a[56:59], a[252:255], v[146:149]\n"
      "s_add_u32 m0, s75, 11904\n"
      "ds_read_b32 v50, v50\n"
      "v_lshl_add_u32 v51, v51, 2, s76\n"
      "v_mfma_f32_16x16x32_bf16 v[112:115], a[76:79], a[244:247], v[112:115]\n"
      "buffer_load_dwordx4 v97, s[20:23], s78 offen offset:384  lds\n"
      "v_mfma_f32_16x16x32_bf16 v[240:243], a[104:107], a[216:219], v[240:243]\n"
      "s_add_u32 m0, s75, 15872\n"
      "ds_read_b32 v71, v71\n"
      "v_mfma_f32_16x16x32_bf16 v[224:227], a[104:107], a[248:251], v[224:227]\n"
      "ds_read_b32 v61, v61\n"
      "v_lshl_add_u32 v47, v47, 2, s76\n"
      "v_mfma_f32_16x16x32_bf16 v[180:183], a[80:83], a[216:219], v[180:183]\n"
      "ds_read_b128 a[240:243], v135 offset:8192\n"
      "v_readlane_b32 s86, v38, s43\n"
      "v_mfma_f32_16x16x32_bf16 v[212:215], a[56:59], a[216:219], v[212:215]\n"
      "buffer_load_dwordx4 v97, s[20:23], s78 offen offset:512  lds\n"
      "v_lshl_add_u32 v42, v42, 2, s76\n"
      "s_mov_b32 s79, 184320\n"
      "v_mfma_f32_16x16x32_bf16 v[204:207], a[32:35], a[248:251], v[204:207]\n"
      "s_add_u32 m0, s75, 19840\n"
      "v_lshl_add_u32 v40, v40, 2, s76\n"
      "v_lshl_add_u32 v75, v75, 2, s76\n"
      "v_mov_b32 v209, 0xf149f2ca\n"
      "v_mfma_f32_16x16x32_bf16 v[112:115], a[80:83], a[220:223], v[112:115]\n"
      "ds_read_b32 v69, v69\n"
      "v_lshl_add_u32 v48, v48, 2, s76\n"
      "v_lshl_add_u32 v44, v44, 2, s76\n"
      "v_mfma_f32_16x16x32_bf16 v[154:157], a[8:11], a[252:255], v[154:157]\n"
      "buffer_load_dwordx4 v97, s[20:23], s78 offen offset:640  lds\n"
      "v_mfma_f32_16x16x32_bf16 v[194:197], a[32:35], a[252:255], v[194:197]\n"
      "s_add_u32 m0, s75, 24576\n"
      "v_lshl_add_u32 v64, v64, 2, s76\n"
      "v_lshl_add_u32 v179, v179, 2, s76\n"
      "v_mfma_f32_16x16x32_bf16 v[232:235], a[56:59], a[248:251], v[232:235]\n"
      "s_waitcnt vmcnt(6)\n"
      "s_barrier\n"
      "ds_read_b64_tr_b16 a[108:109], v84 offset:36864\n"
      "ds_read_b64_tr_b16 a[132:133], v174 offset:36864\n"
      "v_mfma_f32_16x16x32_bf16 v[244:247], a[8:11], a[248:251], v[244:247]\n"
      "ds_read_b64_tr_b16 a[110:111], v84 offset:37376\n"
      "ds_read_b64_tr_b16 a[38:39], v84 offset:25088\n"
      "v_mfma_f32_16x16x32_bf16 v[170:173], a[52:55], a[244:247], v[170:173]\n"
      "ds_read_b64_tr_b16 a[134:135], v174 offset:37376\n"
      "ds_read_b64_tr_b16 a[84:85], v85 offset:36864\n"
      "ds_read_b64_tr_b16 a[12:13], v85 offset:24576\n"
      "ds_read_b64_tr_b16 a[36:37], v84 offset:24576\n"
      "ds_read_b64_tr_b16 a[60:61], v174 offset:24576\n"
      "v_mfma_f32_16x16x32_bf16 v[170:173], a[56:59], a[220:223], v[170:173]\n"
      "ds_read_b64_tr_b16 a[14:15], v85 offset:25088\n"
      "ds_read_b64_tr_b16 a[62:63], v174 offset:25088\n"
      "ds_read_b64_tr_b16 a[86:87], v85 offset:37376\n"
      "v_mfma_f32_16x16x32_bf16 v[216:219], a[80:83], a[248:251], v[216:219]\n"
      "buffer_load_dwordx4 v97, s[20:23], s79 offen  lds\n"
      "v_mfma_f32_16x16x32_bf16 v[228:231], a[28:31], a[244:247], v[228:231]\n"
      "ds_read_b32 v43, v43\n"
      "s_add_u32 m0, s75, 28544\n"
      "v_lshl_add_u32 v45, v45, 2, s76\n"
      "v_mfma_f32_16x16x32_bf16 v[228:231], a[32:35], a[220:223], v[228:231]\n"
      "ds_read_b128 a[244:247], v0 offset:12288\n"
      "v_lshl_add_u32 v56, v56, 2, s76\n"
      "v_mfma_f32_16x16x32_bf16 v[130:133], a[32:35], a[216:219], v[130:133]\n"
      "ds_read_b32 v65, v65\n"
      "buffer_load_dwordx4 v97, s[20:23], s79 offen offset:128  lds\n"
      "s_mov_b32 s78, 0\n"
      "v_mfma_f32_16x16x32_bf16 v[150:153], a[128:131], a[216:219], v[150:153]\n"
      "s_add_u32 m0, s75, 32512\n"
      "v_lshl_add_u32 v52, v52, 2, s76\n"
      "s_waitcnt lgkmcnt(3)\n"
      "v_mfma_f32_16x16x32_bf16 v[212:215], a[60:63], a[232:235], v[212:215]\n"
      "ds_read_b32 v211, v211\n"
      "ds_read_b32 v40, v40\n"
      "v_lshl_add_u32 v60, v60, 2, s76\n"
      "v_mfma_f32_16x16x32_bf16 v[120:123], a[128:131], a[252:255], v[120:123]\n"
      "buffer_load_dwordx4 v97, s[20:23], s79 offen offset:256  lds\n"
      "v_mfma_f32_16x16x32_bf16 v[166:169], a[80:83], a[252:255], v[166:169]\n"
      "ds_read_b32 v44, v44\n"
      "v_lshl_add_u32 v67, v67, 2, s76\n"
      "v_mfma_f32_16x16x32_bf16 v[166:169], a[84:87], a[228:231], v[166:169]\n"
      "ds_read_b128 a[216:219], v184 offset:8192\n"
      "v_lshl_add_u32 v74, v74, 2, s76\n"
      "v_mfma_f32_16x16x32_bf16 v[252:255], a[104:107], a[220:223], v[252:255]\n"
      "ds_read_b32 v48, v48\n"
      "s_add_u32 m0, s75, 36480\n"
      "v_mfma_f32_16x16x32_bf16 v[252:255], a[108:111], a[236:239], v[252:255]\n"
      "buffer_load_dwordx4 v97, s[20:23], s79 offen offset:384  lds\n"
      "v_cndmask_b32 v30, v209, v177, s[90:91]\n"
      "v_mfma_f32_16x16x32_bf16 v[116:119], a[132:135], a[236:239], v[116:119]\n"
      "ds_read_b32 v60, v60\n"
      "v_lshl_add_u32 v66, v66, 2, s76\n"
      "v_mfma_f32_16x16x32_bf16 v[112:115], a[84:87], a[236:239], v[112:115]\n"
      "s_add_u32 m0, s75, 40448\n"
      "ds_read_b32 v75, v75\n"
      "v_mfma_f32_16x16x32_bf16 v[244:247], a[12:15], a[224:227], v[244:247]\n"
      "ds_read_b32 v179, v179\n"
      "v_lshl_add_u32 v57, v57, 2, s76\n"
      "v_mfma_f32_16x16x32_bf16 v[100:103], a[8:11], a[220:223], v[100:103]\n"
      "buffer_load_dwordx4 v97, s[20:23], s79 offen offset:512  lds\n"
      "v_mfma_f32_16x16x32_bf16 v[170:173], a[60:63], a[236:239], v[170:173]\n"
      "ds_read_b128 a[248:251], v0 offset:14336\n"
      "s_add_u32 m0, s75, 44416\n"
      "ds_read_b32 v45, v45\n"
      "v_mfma_f32_16x16x32_bf16 v[240:243], a[108:111], a[232:235], v[240:243]\n"
      "ds_read_b32 v64, v64\n"
      "ds_read_b32 v70, v70\n"
      "v_mfma_f32_16x16x32_bf16 v[204:207], a[36:39], a[224:227], v[204:207]\n"
      "buffer_load_dwordx4 v97, s[20:23], s79 offen offset:640  lds\n"
      "v_mfma_f32_16x16x32_bf16 v[248:251], a[132:135], a[224:227], v[248:251]\n"
      "ds_read_b128 a[252:255], v0 offset:16384\n"
      "s_add_u32 m0, s75, 48384\n"
      "v_mfma_f32_16x16x32_bf16 v[120:123], a[132:135], a[228:231], v[120:123]\n"
      "ds_read_b32 v223, v223\n"
      "v_lshl_add_u32 v53, v53, 2, s76\n"
      "v_mfma_f32_16x16x32_bf16 v[228:231], a[36:39], a[236:239], v[228:231]\n"
      "ds_read_b32 v63, v63\n"
      "ds_read_b32 v53, v53\n"
      "v_mfma_f32_16x16x32_bf16 v[100:103], a[12:15], a[236:239], v[100:103]\n"
      "buffer_load_dwordx4 v97, s[20:23], s78 offen offset:768  lds\n"
      "v_mfma_f32_16x16x32_bf16 v[194:197], a[36:39], a[228:231], v[194:197]\n"
      "s_add_u32 m0, s75, 52352\n"
      "s_waitcnt vmcnt(7)\n"
      "s_barrier\n"
      "ds_read_b64_tr_b16 a[136:137], v238 offset:12288\n"
      "v_mfma_f32_16x16x32_bf16 v[224:227], a[108:111], a[224:227], v[224:227]\n"
      "ds_read_b64_tr_b16 a[88:89], v236 offset:12288\n"
      "ds_read_b64_tr_b16 a[66:67], v238 offset:512\n"
      "ds_read_b64_tr_b16 a[90:91], v236 offset:12800\n"
      "v_mfma_f32_16x16x32_bf16 v[154:157], a[12:15], a[228:231], v[154:157]\n"
      "ds_read_b64_tr_b16 a[16:17], v236\n"
      "ds_read_b64_tr_b16 a[18:19], v236 offset:512\n"
      "v_mfma_f32_16x16x32_bf16 v[232:235], a[60:63], a[224:227], v[232:235]\n"
      "buffer_load_dwordx4 v97, s[20:23], s78 offen offset:896  lds\n"
      "v_mfma_f32_16x16x32_bf16 v[108:111], a[12:15], a[232:235], v[108:111]\n"
      "ds_read_b64_tr_b16 a[138:139], v238 offset:12800\n"
      "ds_read_b64_tr_b16 a[42:43], v237 offset:512\n"
      "ds_read_b64_tr_b16 a[64:65], v238\n"
      "v_mfma_f32_16x16x32_bf16 v[150:153], a[132:135], a[232:235], v[150:153]\n"
      "ds_read_b64_tr_b16 a[112:113], v237 offset:12288\n"
      "ds_read_b64_tr_b16 a[114:115], v237 offset:12800\n"
      "v_mfma_f32_16x16x32_bf16 v[130:133], a[36:39], a[232:235], v[130:133]\n"
      "ds_read_b64_tr_b16 a[40:41], v237\n"
      "s_add_u32 m0, s75, 56320\n"
      "v_mfma_f32_16x16x32_bf16 v[180:183], a[84:87], a[232:235], v[180:183]\n"
      "buffer_load_dwordx4 v97, s[20:23], s78 offen offset:1024  lds\n"
      "v_mfma_f32_16x16x32_bf16 v[216:219], a[84:87], a[224:227], v[216:219]\n"
      "ds_read_b32 v67, v67\n"
      "s_waitcnt lgkmcnt(7)\n"
      "v_mfma_f32_16x16x32_bf16 v[216:219], a[88:91], a[240:243], v[216:219]\n"
      "ds_read_b32 v47, v47\n"
      "v_readlane_b32 s84, v37, s43\n"
      "v_mfma_f32_16x16x32_bf16 v[146:149], a[60:63], a[228:231], v[146:149]\n"
      "s_mov_b32 s78, 36864\n"
      "v_lshl_add_u32 v68, v68, 2, s76\n"
      "s_add_u32 m0, s75, 60672\n"
      "v_mfma_f32_16x16x32_bf16 v[108:111], a[16:19], a[248:251], v[108:111]\n"
      "buffer_load_dwordx4 v97, s[20:23], s78 offen offset:768  lds\n"
      "s_waitcnt lgkmcnt(2)\n"
      "v_mfma_f32_16x16x32_bf16 v[252:255], a[112:115], a[252:255], v[252:255]\n"
      "s_add_u32 m0, s75, 64640\n"
      "ds_read_b128 a[232:235], v135 offset:2048\n"
      "s_mov_b32 s79, 73728\n"
      "v_mfma_f32_16x16x32_bf16 v[120:123], a[136:139], a[244:247], v[120:123]\n"
      "ds_read_b32 v46, v46\n"
      "ds_read_b32 v54, v54\n"
      "v_mfma_f32_16x16x32_bf16 v[232:235], a[64:67], a[240:243], v[232:235]\n"
      "buffer_load_dwordx4 v97, s[20:23], s78 offen offset:896  lds\n"
      "v_mfma_f32_16x16x32_bf16 v[166:169], a[88:91], a[244:247], v[166:169]\n"
      "s_add_u32 m0, s75, 68608\n"
      "ds_read_b32 v68, v68\n"
      "v_mfma_f32_16x16x32_bf16 v[190:193], a[108:111], a[228:231], v[190:193]\n"
      "ds_read_b32 v58, v58\n"
      "ds_read_b32 v41, v41\n"
      "ds_read_b128 a[236:239], v184 offset:2048\n"
      "v_mfma_f32_16x16x32_bf16 v[116:119], a[136:139], a[252:255], v[116:119]\n"
      "buffer_load_dwordx4 v97, s[20:23], s78 offen offset:1024  lds\n"
      "v_mfma_f32_16x16x32_bf16 v[244:247], a[16:19], a[240:243], v[244:247]\n"
      "ds_read_b32 v49, v49\n"
      "v_lshl_add_u32 v62, v62, 2, s76\n"
      "v_mfma_f32_16x16x32_bf16 v[248:251], a[136:139], a[240:243], v[248:251]\n"
      "ds_read_b32 v57, v57\n"
      "v_cndmask_b32 v28, v209, v179, s[82:83]\n"
      "v_mfma_f32_16x16x32_bf16 v[154:157], a[16:19], a[244:247], v[154:157]\n"
      "ds_read_b32 v52, v52\n"
      "s_add_u32 m0, s75, 72960\n"
      "v_mfma_f32_16x16x32_bf16 v[112:115], a[88:91], a[252:255], v[112:115]\n"
      "buffer_load_dwordx4 v97, s[20:23], s79 offen offset:768  lds\n"
      "s_waitcnt lgkmcnt(4)\n"
      "v_mfma_f32_16x16x32_bf16 v[176:179], a[72:75], a[232:235], 0\n"
      "ds_read_b32 v55, v55\n"
      "v_readlane_b32 s85, v37, s42\n"
      "v_mfma_f32_16x16x32_bf16 v[146:149], a[64:67], a[244:247], v[146:149]\n"
      "ds_read_b32 v42, v42\n"
      "ds_read_b32 v74, v74\n"
      "v_mfma_f32_16x16x32_bf16 v[194:197], a[40:43], a[244:247], v[194:197]\n"
      "s_add_u32 m0, s75, 76928\n"
      "ds_read_b32 v56, v56\n"
      "v_mfma_f32_16x16x32_bf16 v[130:133], a[40:43], a[248:251], v[130:133]\n"
      "buffer_load_dwordx4 v97, s[20:23], s79 offen offset:896  lds\n"
      "v_mfma_f32_16x16x32_bf16 v[170:173], a[64:67], a[252:255], v[170:173]\n"
      "ds_read_b128 a[224:227], v1 offset:14336\n"
      "s_add_u32 m0, s75, 80896\n"
      "ds_read_b128 a[228:231], v1 offset:16384\n"
      "v_mfma_f32_16x16x32_bf16 v[228:231], a[40:43], a[252:255], v[228:231]\n"
      "ds_read_b32 v66, v66\n"
      "ds_read_b128 a[220:223], v1 offset:12288\n"
      "v_mfma_f32_16x16x32_bf16 v[100:103], a[16:19], a[252:255], v[100:103]\n"
      "buffer_load_dwordx4 v97, s[20:23], s79 offen offset:1024  lds\n"
      "v_mfma_f32_16x16x32_bf16 v[180:183], a[88:91], a[248:251], v[180:183]\n"
      "s_waitcnt vmcnt(9)\n"
      "s_barrier\n"
      "ds_read_b64_tr_b16 a[94:95], v236 offset:37376\n"
      "v_mfma_f32_16x16x32_bf16 v[190:193], a[112:115], a[244:247], v[190:193]\n"
      "ds_read_b64_tr_b16 a[20:21], v236 offset:24576\n"
      "ds_read_b64_tr_b16 a[92:93], v236 offset:36864\n"
      "v_mfma_f32_16x16x32_bf16 v[142:145], a[0:3], a[232:235], 0\n"
      "ds_read_b64_tr_b16 a[22:23], v236 offset:25088\n"
      "s_mov_b32 s79, 110592\n"
      "v_mfma_f32_16x16x32_bf16 v[224:227], a[112:115], a[240:243], v[224:227]\n"
      "ds_read_b64_tr_b16 a[116:117], v237 offset:36864\n"
      "ds_read_b64_tr_b16 a[68:69], v238 offset:24576\n"
      "v_mfma_f32_16x16x32_bf16 v[212:215], a[64:67], a[248:251], v[212:215]\n"
      "s_add_u32 m0, s75, 85248\n"
      "ds_read_b64_tr_b16 a[44:45], v237 offset:24576\n"
      "v_mfma_f32_16x16x32_bf16 v[204:207], a[40:43], a[240:243], v[204:207]\n"
      "ds_read_b64_tr_b16 a[70:71], v238 offset:25088\n"
      "ds_read_b64_tr_b16 a[140:141], v238 offset:36864\n"
      "v_mfma_f32_16x16x32_bf16 v[162:165], a[48:51], a[232:235], 0\n"
      "buffer_load_dwordx4 v97, s[20:23], s79 offen offset:768  lds\n"
      "v_mfma_f32_16x16x32_bf16 v[150:153], a[136:139], a[248:251], v[150:153]\n"
      "ds_read_b64_tr_b16 a[46:47], v237 offset:25088\n"
      "s_waitcnt lgkmcnt(6)\n"
      "v_mfma_f32_16x16x32_bf16 v[154:157], a[20:23], a[220:223], v[154:157]\n"
      "s_add_u32 m0, s75, 89216\n"
      "ds_read_b64_tr_b16 a[118:119], v237 offset:37376\n"
      "v_mfma_f32_16x16x32_bf16 v[244:247], a[20:23], a[216:219], v[244:247]\n"
      "ds_read_b64_tr_b16 a[142:143], v238 offset:37376\n"
      "v_lshl_add_u32 v59, v59, 2, s76\n"
      "v_mfma_f32_16x16x32_bf16 v[216:219], a[92:95], a[216:219], v[216:219]\n"
      "buffer_load_dwordx4 v97, s[20:23], s79 offen offset:896  lds\n"
      "v_mfma_f32_16x16x32_bf16 v[112:115], a[92:95], a[228:231], v[112:115]\n"
      "s_add_u32 m0, s75, 93184\n"
      "ds_read_b128 a[240:243], v135 offset:6144\n"
      "v_mfma_f32_16x16x32_bf16 v[180:183], a[92:95], a[224:227], v[180:183]\n"
      "ds_read_b128 a[244:247], v184 offset:6144\n"
      "v_mul_f32_e64 v105, v245, v245\n"
      "v_mfma_f32_16x16x32_bf16 v[176:179], a[76:79], a[236:239], v[176:179]\n"
      "s_mov_b32 s78, 147456\n"
      "v_mul_f32_e64 v159, v217, v217\n"
      "v_fma_f32 v104, v244, v244, s51\n"
      "v_mfma_f32_16x16x32_bf16 v[166:169], a[92:95], a[220:223], v[166:169]\n"
      "buffer_load_dwordx4 v97, s[20:23], s79 offen offset:1024  lds\n"
      "v_mfma_f32_16x16x32_bf16 v[100:103], a[20:23], a[228:231], v[100:103]\n"
      "s_waitcnt lgkmcnt(2)\n"
      "v_fma_f32 v158, v216, v216, s51\n"
      "s_add_u32 m0, s75, -768\n"
      "v_mfma_f32_16x16x32_bf16 v[204:207], a[44:47], a[216:219], v[204:207]\n"
      "ds_read_b32 v51, v51\n"
      "v_cndmask_b32 v26, v209, v223, s[82:83]\n"
      "v_mfma_f32_16x16x32_bf16 v[150:153], a[140:143], a[224:227], v[150:153]\n"
      "s_waitcnt lgkmcnt(2)\n"
      "v_cndmask_b32 v24, v209, v211, s[82:83]\n"
      "v_mul_f32_e64 v209, v181, v181\n"
      "v_mfma_f32_16x16x32_bf16 v[108:111], a[20:23], a[224:227], v[108:111]\n"
      "buffer_load_dwordx4 v97, s[20:23], s78 offen offset:768  lds\n"
      "v_mfma_f32_16x16x32_bf16 v[142:145], a[4:7], a[236:239], v[142:145]\n"
      "ds_read_b32 v59, v59\n"
      "v_fma_f32 v208, v180, v180, s51\n"
      "v_mfma_f32_16x16x32_bf16 v[142:145], a[8:11], a[240:243], v[142:145]\n"
      "s_lshl_b32 s92, s40, 6\n"
      "v_fma_f32 v106, v166, v166, s51\n"
      "v_mul_f32 v211, v155, v155\n"
      "v_mfma_f32_16x16x32_bf16 v[130:133], a[44:47], a[224:227], v[130:133]\n"
      "s_add_u32 m0, s75, 3200\n"
      "v_mul_f32 v187, v101, v101\n"
      "v_mul_f32 v107, v167, v167\n"
      "v_mfma_f32_16x16x32_bf16 v[190:193], a[116:119], a[220:223], v[190:193]\n"
      "buffer_load_dwordx4 v97, s[20:23], s78 offen offset:896  lds\n"
      "v_fma_f32 v222, v112, v112, s51\n"
      "v_mul_f32_e64 v223, v113, v113\n"
      "v_pk_fma_f32 v[208:209], v[182:183], v[182:183], v[208:209]\n"
      "v_pk_fma_f32 v[158:159], v[218:219], v[218:219], v[158:159]\n"
      "v_pk_fma_f32 v[104:105], v[246:247], v[246:247], v[104:105]\n"
      "v_pk_fma_f32 v[106:107], v[168:169], v[168:169], v[106:107]\n"
      "v_pk_fma_f32 v[104:105], v[204:205], v[204:205], v[104:105]\n"
      "v_mfma_f32_16x16x32_bf16 v[232:235], a[68:71], a[216:219], v[232:235]\n"
      "s_add_u32 m0, s75, 7168\n"
      "ds_read_b128 a[252:255], v184 offset:10240\n"
      "v_mfma_f32_16x16x32_bf16 v[224:227], a[116:119], a[216:219], v[224:227]\n"
      "buffer_load_dwordx4 v97, s[20:23], s78 offen offset:1024  lds\n"
      "v_mfma_f32_16x16x32_bf16 v[194:197], a[44:47], a[220:223], v[194:197]\n"
      "s_add_u32 s43, s43, 1\n"
      "v_fma_f32 v210, v154, v154, s51\n"
      "s_mov_b32 s78, 184320\n"
      "v_mfma_f32_16x16x32_bf16 v[240:243], a[112:115], a[248:251], v[240:243]\n"
      "s_add_u32 m0, s75, 11520\n"
      "v_fma_f32 v186, v100, v100, s51\n"
      "v_mul_f32 v129, v109, v109\n"
      "v_fma_f32 v128, v108, v108, s51\n"
      "v_mfma_f32_16x16x32_bf16 v[240:243], a[116:119], a[224:227], v[240:243]\n"
      "ds_read_b128 a[248:251], v135 offset:10240\n"
      "buffer_load_dwordx4 v97, s[20:23], s78 offen offset:768  lds\n"
      "v_pk_fma_f32 v[104:105], v[206:207], v[206:207], v[104:105]\n"
      "v_pk_fma_f32 v[158:159], v[224:225], v[224:225], v[158:159]\n"
      "s_add_u32 m0, s75, 15488\n"
      "v_pk_fma_f32 v[106:107], v[190:191], v[190:191], v[106:107]\n"
      "v_pk_fma_f32 v[158:159], v[226:227], v[226:227], v[158:159]\n"
      "v_pk_fma_f32 v[104:105], v[232:233], v[232:233], v[104:105]\n"
      "v_pk_fma_f32 v[186:187], v[102:103], v[102:103], v[186:187]\n"
      "v_pk_fma_f32 v[222:223], v[114:115], v[114:115], v[222:223]\n"
      "v_pk_fma_f32 v[210:211], v[156:157], v[156:157], v[210:211]\n"
      "v_pk_fma_f32 v[104:105], v[234:235], v[234:235], v[104:105]\n"
      "v_pk_fma_f32 v[128:129], v[110:111], v[110:111], v[128:129]\n"
      "v_pk_fma_f32 v[210:211], v[194:195], v[194:195], v[210:211]\n"
      "v_pk_fma_f32 v[208:209], v[240:241], v[240:241], v[208:209]\n"
      "v_mfma_f32_16x16x32_bf16 v[248:251], a[140:143], a[216:219], v[248:251]\n"
      "buffer_load_dwordx4 v97, s[20:23], s78 offen offset:896  lds\n"
      "v_mfma_f32_16x16x32_bf16 v[170:173], a[68:71], a[228:231], v[170:173]\n"
      "s_add_u32 m0, s75, 19456\n"
      "v_add_f32 v104, v104, v105\n"
      "s_waitcnt vmcnt(11)\n"
      "s_barrier\n"
      "v_mfma_f32_16x16x32_bf16 v[116:119], a[140:143], a[228:231], v[116:119]\n"
      "s_mov_b32 s79, 0\n"
      "ds_read_b64_tr_b16 a[146:147], v85 offset:512\n"
      "v_mfma_f32_16x16x32_bf16 v[162:165], a[52:55], a[236:239], v[162:165]\n"
      "ds_read_b64_tr_b16 a[196:197], v174 offset:12288\n"
      "ds_read_b64_tr_b16 a[172:173], v84 offset:12288\n"
      "v_mfma_f32_16x16x32_bf16 v[120:123], a[140:143], a[220:223], v[120:123]\n"
      "buffer_load_dwordx4 v97, s[20:23], s78 offen offset:1024  lds\n"
      "v_mfma_f32_16x16x32_bf16 v[252:255], a[116:119], a[228:231], v[252:255]\n"
      "ds_read_b64_tr_b16 a[144:145], v85\n"
      "ds_read_b64_tr_b16 a[168:169], v84\n"
      "v_mfma_f32_16x16x32_bf16 v[162:165], a[56:59], a[240:243], v[162:165]\n"
      "ds_read_b64_tr_b16 a[174:175], v84 offset:12800\n"
      "s_add_u32 m0, s75, 24576\n"
      "v_mfma_f32_16x16x32_bf16 v[146:149], a[68:71], a[220:223], v[146:149]\n"
      "ds_read_b64_tr_b16 a[170:171], v84 offset:512\n"
      "ds_read_b64_tr_b16 a[198:199], v174 offset:12800\n"
      "v_mfma_f32_16x16x32_bf16 v[228:231], a[44:47], a[228:231], v[228:231]\n"
      "ds_read_b64_tr_b16 a[150:151], v85 offset:12800\n"
      "ds_read_b64_tr_b16 a[194:195], v174 offset:512\n"
      "ds_read_b64_tr_b16 a[148:149], v85 offset:12288\n"
      "v_pk_fma_f32 v[208:209], v[242:243], v[242:243], v[208:209]\n"
      "v_pk_fma_f32 v[210:211], v[196:197], v[196:197], v[210:211]\n"
      "v_pk_fma_f32 v[208:209], v[150:151], v[150:151], v[208:209]\n"
      "v_pk_fma_f32 v[158:159], v[248:249], v[248:249], v[158:159]\n"
      "v_pk_fma_f32 v[128:129], v[130:131], v[130:131], v[128:129]\n"
      "v_mfma_f32_16x16x32_bf16 v[212:215], a[68:71], a[224:227], v[212:215]\n"
      "ds_read_b64_tr_b16 a[192:193], v174\n"
      "buffer_load_dwordx4 v141, s[36:39], s79 offen  lds\n"
      "v_pk_fma_f32 v[222:223], v[252:253], v[252:253], v[222:223]\n"
      "v_pk_fma_f32 v[128:129], v[132:133], v[132:133], v[128:129]\n"
      "v_pk_fma_f32 v[158:159], v[250:251], v[250:251], v[158:159]\n"
      "v_pk_fma_f32 v[208:209], v[152:153], v[152:153], v[208:209]\n"
      "v_add_f32 v158, v158, v159\n"
      "v_add_f32 v208, v208, v209\n"
      "v_pk_fma_f32 v[210:211], v[146:147], v[146:147], v[210:211]\n"
      "v_pk_fma_f32 v[106:107], v[192:193], v[192:193], v[106:107]\n"
      "v_pk_fma_f32 v[222:223], v[254:255], v[254:255], v[222:223]\n"
      "v_permlane16_swap_b32 v104, v158\n"
      "v_pk_fma_f32 v[128:129], v[212:213], v[212:213], v[128:129]\n"
      "v_add_f32 v104, v104, v158\n"
      "v_pk_fma_f32 v[222:223], v[116:117], v[116:117], v[222:223]\n"
      "v_mov_b32 v158, v104\n"
      "v_pk_fma_f32 v[128:129], v[214:215], v[214:215], v[128:129]\n"
      "v_pk_fma_f32 v[106:107], v[120:121], v[120:121], v[106:107]\n"
      "v_pk_fma_f32 v[210:211], v[148:149], v[148:149], v[210:211]\n"
      "v_add_f32 v128, v128, v129\n"
      "v_pk_fma_f32 v[186:187], v[228:229], v[228:229], v[186:187]\n"
      "v_pk_fma_f32 v[106:107], v[122:123], v[122:123], v[106:107]\n"
      "v_permlane16_swap_b32 v128, v208\n"
      "v_add_f32 v128, v128, v208\n"
      "v_mov_b32 v208, v128\n"
      "v_add_f32 v210, v210, v211\n"
      "v_mfma_f32_16x16x32_bf16 v[96:99], a[24:27], a[232:235], 0\n"
      "s_waitcnt lgkmcnt(0)\n"
      "v_add_f32 v106, v106, v107\n"
      "v_permlane32_swap_b32_e64 v104, v158\n"
      "v_mfma_f32_16x16x32_bf16 v[142:145], a[12:15], a[244:247], v[142:145]\n"
      "s_add_u32 m0, s75, 28544\n"
      "v_permlane16_swap_b32 v210, v106\n"
      "v_permlane32_swap_b32 v128, v208\n"
      "v_add_f32 v104, v104, v158\n"
      "v_pk_fma_f32 v[222:223], v[118:119], v[118:119], v[222:223]\n"
      "v_add_f32 v128, v128, v208\n"
      "v_add_f32 v222, v222, v223\n"
      "v_add_f32 v210, v210, v106\n"
      "v_mfma_f32_16x16x32_bf16 v[142:145], a[16:19], a[248:251], v[142:145]\n"
      "buffer_load_dwordx4 v141, s[36:39], s79 offen offset:128  lds\n"
      "v_rsq_f32_e64 v128, v128\n"
      "v_pk_fma_f32 v[186:187], v[230:231], v[230:231], v[186:187]\n"
      "v_mfma_f32_16x16x32_bf16 v[142:145], a[20:23], a[252:255], v[142:145]\n"
      "ds_read_b32 v62, v62\n"
      "v_mov_b32 v106, v210\n"
      "v_rsq_f32 v104, v104\n"
      "v_mov_b32 v208, v128\n"
      "v_mfma_f32_16x16x32_bf16 v[176:179], a[80:83], a[240:243], v[176:179]\n"
      "s_add_u32 m0, s75, 32512\n"
      "v_permlane32_swap_b32 v210, v106\n"
      "v_mfma_f32_16x16x32_bf16 v[176:179], a[84:87], a[244:247], v[176:179]\n"
      "buffer_load_dwordx4 v141, s[36:39], s79 offen offset:256  lds\n"
      "v_add_f32 v210, v210, v106\n"
      "v_permlane16_swap_b32 v128, v208\n"
      "v_mfma_f32_16x16x32_bf16 v[176:179], a[88:91], a[248:251], v[176:179]\n"
      "s_add_u32 m0, s75, 36864\n"
      "v_mov_b32 v158, v104\n"
      "v_mul_f32 v128, s50, v128\n"
      "v_mfma_f32_16x16x32_bf16 v[176:179], a[92:95], a[252:255], v[176:179]\n"
      "s_mov_b32 s79, 12288\n"
      "v_rsq_f32 v210, v210\n"
      "v_permlane16_swap_b32 v104, v158\n"
      "v_mov_b32_e64 v106, v210\n"
      "v_pk_mul_f32 v[108:109], v[108:109], v[128:129] op_sel_hi:[1,0]\n"
      "v_pk_fma_f32 v[186:187], v[170:171], v[170:171], v[186:187]\n"
      "v_pk_mul_f32 v[150:151], v[150:151], v[208:209] op_sel_hi:[1,0]\n"
      "v_pk_mul_f32 v[132:133], v[132:133], v[128:129] op_sel_hi:[1,0]\n"
      "v_pk_mul_f32 v[218:219], v[218:219], v[158:159] op_sel_hi:[1,0]\n"
      "v_mul_f32_e64 v104, s50, v104\n"
      "v_pk_mul_f32 v[216:217], v[216:217], v[158:159] op_sel_hi:[1,0]\n"
      "v_pk_fma_f32 v[186:187], v[172:173], v[172:173], v[186:187]\n"
      "v_pk_mul_f32 v[214:215], v[214:215], v[128:129] op_sel_hi:[1,0]\n"
      "v_mfma_f32_16x16x32_bf16 v[162:165], a[60:63], a[244:247], v[162:165]\n"
      "buffer_load_dwordx4 v141, s[36:39], s79 offen  lds\n"
      "v_add_f32 v186, v186, v187\n"
      "v_permlane16_swap_b32 v210, v106\n"
      "v_pk_mul_f32 v[226:227], v[226:227], v[158:159] op_sel_hi:[1,0]\n"
      "v_pk_mul_f32 v[232:233], v[232:233], v[104:105] op_sel_hi:[1,0]\n"
      "v_pk_mul_f32 v[248:249], v[248:249], v[158:159] op_sel_hi:[1,0]\n"
      "v_pk_mul_f32 v[246:247], v[246:247], v[104:105] op_sel_hi:[1,0]\n"
      "v_pk_mul_f32 v[190:191], v[190:191], v[106:107] op_sel_hi:[1,0]\n"
      "v_mfma_f32_16x16x32_bf16 v[96:99], a[28:31], a[236:239], v[96:99]\n"
      "s_add_u32 m0, s75, 40832\n"
      "v_permlane16_swap_b32 v186, v222\n"
      "v_add_f32 v186, v186, v222\n"
      "v_cvt_pk_bf16_f32 v216, v216, v217\n"
      "v_mov_b32 v222, v186\n"
      "v_mfma_f32_16x16x32_bf16 v[96:99], a[32:35], a[240:243], v[96:99]\n"
      "s_waitcnt vmcnt(10)\n"
      "s_barrier\n"
      "ds_read_b64_tr_b16 a[204:205], v174 offset:36864\n"
      "v_mfma_f32_16x16x32_bf16 v[96:99], a[36:39], a[244:247], v[96:99]\n"
      "buffer_load_dwordx4 v141, s[36:39], s79 offen offset:128  lds\n"
      "v_permlane32_swap_b32 v186, v222\n"
      "v_cvt_pk_bf16_f32 v217, v218, v219\n"
      "v_pk_mul_f32 v[250:251], v[250:251], v[158:159] op_sel_hi:[1,0]\n"
      "v_pk_mul_f32 v[180:181], v[180:181], v[208:209] op_sel_hi:[1,0]\n"
      "v_pk_mul_f32 v[206:207], v[206:207], v[104:105] op_sel_hi:[1,0]\n"
      "v_pk_mul_f32 v[224:225], v[224:225], v[158:159] op_sel_hi:[1,0]\n"
      "v_pk_mul_f32 v[152:153], v[152:153], v[208:209] op_sel_hi:[1,0]\n"
      "v_add_f32_e64 v186, v186, v222\n"
      "v_cvt_pk_bf16_f32 v219, v226, v227\n"
      "v_cvt_pk_bf16_f32 v218, v224, v225\n"
      "v_pk_mul_f32 v[244:245], v[244:245], v[104:105] op_sel_hi:[1,0]\n"
      "v_pk_mul_f32 v[242:243], v[242:243], v[208:209] op_sel_hi:[1,0]\n"
      "v_mfma_f32_16x16x32_bf16 v[200:203], a[120:123], a[232:235], 0\n"
      "ds_read_b64_tr_b16 a[176:177], v84 offset:24576\n"
      "ds_read_b64_tr_b16 a[182:183], v84 offset:37376\n"
      "ds_read_b64_tr_b16 a[154:155], v85 offset:25088\n"
      "v_pk_mul_f32 v[192:193], v[192:193], v[106:107] op_sel_hi:[1,0]\n"
      "v_mul_f32_e64 v175, v177, v177\n"
      "v_cvt_pk_bf16_f32 v248, v248, v249\n"
      "v_pk_mul_f32 v[130:131], v[130:131], v[128:129] op_sel_hi:[1,0]\n"
      "v_mfma_f32_16x16x32_bf16 v[96:99], a[40:43], a[248:251], v[96:99]\n"
      "ds_read_b64_tr_b16 a[152:153], v85 offset:24576\n"
      "ds_read_b64_tr_b16 a[202:203], v174 offset:25088\n"
      "v_cvt_pk_bf16_f32 v249, v250, v251\n"
      "v_mfma_f32_16x16x32_bf16 v[158:161], a[96:99], a[232:235], 0\n"
      "ds_read_b64_tr_b16 a[200:201], v174 offset:24576\n"
      "v_rsq_f32_e64 v186, v186\n"
      "v_mfma_f32_16x16x32_bf16 v[158:161], a[100:103], a[236:239], v[158:161]\n"
      "ds_read_b64_tr_b16 a[206:207], v174 offset:37376\n"
      "ds_read_b64_tr_b16 a[178:179], v84 offset:25088\n"
      "v_mfma_f32_16x16x32_bf16 v[158:161], a[104:107], a[240:243], v[158:161]\n"
      "ds_read_b64_tr_b16 a[158:159], v85 offset:37376\n"
      "v_mul_f32_e64 v210, s50, v210\n"
      "v_mfma_f32_16x16x32_bf16 v[200:203], a[124:127], a[236:239], v[200:203]\n"
      "ds_read_b64_tr_b16 a[156:157], v85 offset:36864\n"
      "s_add_u32 m0, s75, 44800\n"
      "v_mfma_f32_16x16x32_bf16 v[200:203], a[128:131], a[240:243], v[200:203]\n"
      "ds_read_b64_tr_b16 a[180:181], v84 offset:36864\n"
      "v_mul_f32 v85, v143, v143\n"
      "v_mov_b32 v222, v186\n"
      "v_cvt_pk_bf16_f32 v244, v244, v245\n"
      "v_mfma_f32_16x16x32_bf16 v[158:161], a[108:111], a[244:247], v[158:161]\n"
      "s_mov_b32 s78, 24576\n"
      "ds_write_b128 v139, v[216:219] offset:32\n"
      "v_mfma_f32_16x16x32_bf16 v[158:161], a[112:115], a[248:251], v[158:161]\n"
      "ds_read_b128 v[216:219], v184\n"
      "v_cvt_pk_bf16_f32 v245, v246, v247\n"
      "v_mfma_f32_16x16x32_bf16 v[158:161], a[116:119], a[252:255], v[158:161]\n"
      "buffer_load_dwordx4 v141, s[36:39], s79 offen offset:256  lds\n"
      "v_fma_f32 v84, v142, v142, s51\n"
      "v_mfma_f32_16x16x32_bf16 v[96:99], a[44:47], a[252:255], v[96:99]\n"
      "ds_write_b64 v139, v[248:249] offset:24\n"
      "v_permlane16_swap_b32_e64 v186, v222\n"
      "v_cvt_pk_bf16_f32 v232, v232, v233\n"
      "v_pk_mul_f32 v[234:235], v[234:235], v[104:105] op_sel_hi:[1,0]\n"
      "v_pk_mul_f32 v[120:121], v[120:121], v[106:107] op_sel_hi:[1,0]\n"
      "v_mul_f32_e64 v186, s50, v186\n"
      "v_pk_fma_f32 v[84:85], v[144:145], v[144:145], v[84:85]\n"
      "v_pk_mul_f32 v[122:123], v[122:123], v[106:107] op_sel_hi:[1,0]\n"
      "v_pk_mul_f32 v[154:155], v[154:155], v[210:211] op_sel_hi:[1,0]\n"
      "v_pk_mul_f32 v[204:205], v[204:205], v[104:105] op_sel_hi:[1,0]\n"
      "v_cvt_pk_bf16_f32 v247, v206, v207\n"
      "v_pk_mul_f32 v[112:113], v[112:113], v[222:223] op_sel_hi:[1,0]\n"
      "v_mfma_f32_16x16x32_bf16 v[200:203], a[132:135], a[244:247], v[200:203]\n"
      "s_add_u32 m0, s75, 49152\n"
      "ds_read_b128 v[248:251], v184 offset:4096\n"
      "buffer_load_dwordx4 v141, s[36:39], s78 offen  lds\n"
      "v_pk_mul_f32 v[196:197], v[196:197], v[210:211] op_sel_hi:[1,0]\n"
      "v_fma_f32 v174, v176, v176, s51\n"
      "v_pk_mul_f32 v[194:195], v[194:195], v[210:211] op_sel_hi:[1,0]\n"
      "v_pk_fma_f32 v[174:175], v[178:179], v[178:179], v[174:175]\n"
      "v_pk_fma_f32 v[84:85], v[96:97], v[96:97], v[84:85]\n"
      "v_pk_mul_f32 v[166:167], v[166:167], v[106:107] op_sel_hi:[1,0]\n"
      "v_pk_mul_f32 v[254:255], v[254:255], v[222:223] op_sel_hi:[1,0]\n"
      "v_cvt_pk_bf16_f32 v233, v234, v235\n"
      "v_pk_mul_f32 v[212:213], v[212:213], v[128:129] op_sel_hi:[1,0]\n"
      "v_pk_mul_f32 v[240:241], v[240:241], v[208:209] op_sel_hi:[1,0]\n"
      "v_pk_mul_f32 v[182:183], v[182:183], v[208:209] op_sel_hi:[1,0]\n"
      "v_cvt_pk_bf16_f32 v246, v204, v205\n"
      "v_pk_mul_f32 v[148:149], v[148:149], v[210:211] op_sel_hi:[1,0]\n"
      "v_pk_mul_f32 v[116:117], v[116:117], v[222:223] op_sel_hi:[1,0]\n"
      "v_pk_mul_f32 v[156:157], v[156:157], v[210:211] op_sel_hi:[1,0]\n"
      "s_add_u32 m0, s75, 53120\n"
      "v_mfma_f32_16x16x32_bf16 v[200:203], a[136:139], a[248:251], v[200:203]\n"
      "ds_write_b128 v139, v[244:247]\n"
      "v_pk_fma_f32 v[174:175], v[158:159], v[158:159], v[174:175]\n"
      "v_cvt_pk_bf16_f32 v180, v180, v181\n"
      "v_cvt_pk_bf16_f32 v181, v182, v183\n"
      "v_cvt_pk_bf16_f32 v112, v112, v113\n"
      "v_mfma_f32_16x16x32_bf16 v[200:203], a[140:143], a[252:255], v[200:203]\n"
      "ds_read_b128 v[244:247], v135 offset:4096\n"
      "v_cvt_pk_bf16_f32 v212, v212, v213\n"
      "v_mfma_f32_16x16x32_bf16 v[162:165], a[64:67], a[248:251], v[162:165]\n"
      "ds_read_b128 v[204:207], v135\n"
      "buffer_load_dwordx4 v141, s[36:39], s78 offen offset:128  lds\n"
      "v_pk_mul_f32 v[100:101], v[100:101], v[186:187] op_sel_hi:[1,0]\n"
      "v_pk_mul_f32 v[110:111], v[110:111], v[128:129] op_sel_hi:[1,0]\n"
      "v_cvt_pk_bf16_f32 v108, v108, v109\n"
      "v_cvt_pk_bf16_f32 v183, v242, v243\n"
      "s_add_u32 m0, s75, 57088\n"
      "v_pk_mul_f32 v[114:115], v[114:115], v[222:223] op_sel_hi:[1,0]\n"
      "v_pk_fma_f32 v[84:85], v[98:99], v[98:99], v[84:85]\n"
      "v_pk_mul_f32 v[252:253], v[252:253], v[222:223] op_sel_hi:[1,0]\n"
      "v_pk_mul_f32 v[168:169], v[168:169], v[106:107] op_sel_hi:[1,0]\n"
      "v_pk_mul_f32 v[146:147], v[146:147], v[210:211] op_sel_hi:[1,0]\n"
      "v_pk_mul_f32 v[172:173], v[172:173], v[186:187] op_sel_hi:[1,0]\n"
      "v_cvt_pk_bf16_f32 v109, v110, v111\n"
      "v_cvt_pk_bf16_f32 v154, v154, v155\n"
      "v_cvt_pk_bf16_f32 v155, v156, v157\n"
      "v_mfma_f32_16x16x32_bf16 v[162:165], a[68:71], a[252:255], v[162:165]\n"
      "buffer_load_dwordx4 v141, s[36:39], s78 offen offset:256  lds\n"
      "s_mov_b32 s78, 36864\n"
      "v_cvt_pk_bf16_f32 v156, v194, v195\n"
      "s_waitcnt lgkmcnt(0)\n"
      "s_waitcnt vmcnt(9)\n"
      "s_barrier\n"
      "v_pk_mul_f32 v[170:171], v[170:171], v[186:187] op_sel_hi:[1,0]\n"
      "v_cvt_pk_bf16_f32 v157, v196, v197\n"
      "v_pk_fma_f32 v[174:175], v[160:161], v[160:161], v[174:175]\n"
      "s_add_u32 m0, s75, 61440\n"
      "ds_read_b64_tr_b16 a[188:189], v237 offset:12288\n"
      "v_pk_mul_f32 v[230:231], v[230:231], v[186:187] op_sel_hi:[1,0]\n"
      "v_mfma_f32_16x16x32_bf16 v[194:197], a[168:171], v[204:207], 0\n"
      "ds_read_b64_tr_b16 a[212:213], v238 offset:12288\n"
      "ds_read_b64_tr_b16 a[190:191], v237 offset:12800\n"
      "v_mfma_f32_16x16x32_bf16 v[194:197], a[172:175], v[216:219], v[194:197]\n"
      "ds_read_b64_tr_b16 a[186:187], v237 offset:512\n"
      "buffer_load_dwordx4 v141, s[36:39], s78 offen  lds\n"
      "v_mfma_f32_16x16x32_bf16 v[194:197], a[176:179], v[244:247], v[194:197]\n"
      "ds_read_b128 v[104:107], v135 offset:8192\n"
      "v_cvt_pk_bf16_f32 v111, v132, v133\n"
      "v_mfma_f32_16x16x32_bf16 v[194:197], a[180:183], v[248:251], v[194:197]\n"
      "ds_read_b64_tr_b16 a[160:161], v236\n"
      "ds_read_b64_tr_b16 a[214:215], v238 offset:12800\n"
      "v_mfma_f32_16x16x32_bf16 v[208:211], a[192:195], v[204:207], 0\n"
      "ds_read_b64_tr_b16 a[184:185], v237\n"
      "s_add_u32 m0, s75, 65408\n"
      "v_mfma_f32_16x16x32_bf16 v[208:211], a[196:199], v[216:219], v[208:211]\n"
      "ds_read_b64_tr_b16 a[210:211], v238 offset:512\n"
      "v_cvt_pk_bf16_f32 v110, v130, v131\n"
      "v_mfma_f32_16x16x32_bf16 v[208:211], a[200:203], v[244:247], v[208:211]\n"
      "ds_read_b64_tr_b16 a[164:165], v236 offset:12288\n"
      "buffer_load_dwordx4 v141, s[36:39], s78 offen offset:128  lds\n"
      "v_cvt_pk_bf16_f32 v150, v150, v151\n"
      "s_mov_b32 s79, 49152\n"
      "v_pk_fma_f32 v[84:85], v[162:163], v[162:163], v[84:85]\n"
      "v_pk_mul_f32 v[118:119], v[118:119], v[222:223] op_sel_hi:[1,0]\n"
      "v_mfma_f32_16x16x32_bf16 v[208:211], a[204:207], v[248:251], v[208:211]\n"
      "ds_read_b64_tr_b16 a[166:167], v236 offset:12800\n"
      "ds_read_b64_tr_b16 a[208:209], v238\n"
      "v_mfma_f32_16x16x32_bf16 v[222:225], a[144:147], v[204:207], 0\n"
      "ds_read_b128 v[130:133], v184 offset:8192\n"
      "v_cvt_pk_bf16_f32 v120, v120, v121\n"
      "s_add_u32 m0, s75, 69376\n"
      "ds_read_b64_tr_b16 a[162:163], v236 offset:512\n"
      "v_mfma_f32_16x16x32_bf16 v[222:225], a[148:151], v[216:219], v[222:225]\n"
      "buffer_load_dwordx4 v141, s[36:39], s78 offen offset:256  lds\n"
      "ds_read_b128 v[234:237], v184 offset:2048\n"
      "v_pk_mul_f32 v[102:103], v[102:103], v[186:187] op_sel_hi:[1,0]\n"
      "v_pk_fma_f32 v[84:85], v[164:165], v[164:165], v[84:85]\n"
      "v_pk_fma_f32 v[174:175], v[200:201], v[200:201], v[174:175]\n"
      "v_mfma_f32_16x16x32_bf16 v[222:225], a[152:155], v[244:247], v[222:225]\n"
      "ds_read_b128 v[242:245], v135 offset:2048\n"
      "v_cvt_pk_bf16_f32 v113, v114, v115\n"
      "v_cvt_pk_bf16_f32 v166, v166, v167\n"
      "v_mfma_f32_16x16x32_bf16 v[222:225], a[156:159], v[248:251], v[222:225]\n"
      "s_add_u32 m0, s75, 73728\n"
      "ds_read_b128 v[246:249], v135 offset:10240\n"
      "buffer_load_dwordx4 v141, s[36:39], s79 offen  lds\n"
      "v_pk_fma_f32 v[174:175], v[202:203], v[202:203], v[174:175]\n"
      "s_waitcnt lgkmcnt(3)\n"
      "v_mfma_f32_16x16x32_bf16 v[222:225], a[160:163], v[104:107], v[222:225]\n"
      "ds_write_b64 v139, v[232:233] offset:16\n"
      "v_mfma_f32_16x16x32_bf16 v[194:197], a[184:187], v[104:107], v[194:197]\n"
      "s_add_u32 m0, s75, 77696\n"
      "ds_read_b128 v[216:219], v135 offset:6144\n"
      "v_mfma_f32_16x16x32_bf16 v[194:197], a[188:191], v[130:133], v[194:197]\n"
      "buffer_load_dwordx4 v141, s[36:39], s79 offen offset:128  lds\n"
      "v_cvt_pk_bf16_f32 v116, v116, v117\n"
      "v_cvt_pk_bf16_f32 v115, v254, v255\n"
      "v_add_f32 v84, v84, v85\n"
      "v_cvt_pk_bf16_f32 v100, v100, v101\n"
      "v_cvt_pk_bf16_f32 v182, v240, v241\n"
      "v_add_f32 v174, v174, v175\n"
      "v_mfma_f32_16x16x32_bf16 v[208:211], a[208:211], v[104:107], v[208:211]\n"
      "s_add_u32 m0, s75, 81664\n"
      "ds_read_b128 v[204:207], v184 offset:6144\n"
      "v_mfma_f32_16x16x32_bf16 v[208:211], a[212:215], v[130:133], v[208:211]\n"
      "ds_read_b128 v[134:137], v0 offset:10240\n"
      "v_cvt_pk_bf16_f32 v213, v214, v215\n"
      "v_cvt_pk_bf16_f32 v117, v118, v119\n"
      "v_cvt_pk_bf16_f32 v194, v194, v195\n"
      "v_mfma_f32_16x16x32_bf16 v[222:225], a[164:167], v[130:133], v[222:225]\n"
      "buffer_load_dwordx4 v141, s[36:39], s79 offen offset:256  lds\n"
      "s_waitcnt lgkmcnt(2)\n"
      "v_mfma_f32_16x16x32_bf16 v[126:129], a[144:147], v[242:245], 0\n"
      "ds_read_b128 v[104:107], v0 offset:4096\n"
      "v_cvt_pk_bf16_f32 v121, v122, v123\n"
      "v_cvt_pk_bf16_f32 v195, v196, v197\n"
      "v_mfma_f32_16x16x32_bf16 v[196:199], a[192:195], v[242:245], 0\n"
      "s_mov_b32 s79, 61440\n"
      "v_permlane16_swap_b32 v84, v174\n"
      "v_add_f32_e64 v84, v84, v174\n"
      "s_add_u32 m0, s75, 86016\n"
      "v_mfma_f32_16x16x32_bf16 v[196:199], a[196:199], v[234:237], v[196:199]\n"
      "buffer_load_dwordx4 v141, s[36:39], s79 offen  lds\n"
      "v_mfma_f32_16x16x32_bf16 v[130:133], a[168:171], v[242:245], 0\n"
      "ds_read_b128 v[242:245], v184 offset:10240\n"
      "v_mov_b32_e64 v174, v84\n"
      "v_cvt_pk_bf16_f32 v114, v252, v253\n"
      "v_mfma_f32_16x16x32_bf16 v[126:129], a[148:151], v[234:237], v[126:129]\n"
      "s_add_u32 m0, s75, 89984\n"
      "v_permlane32_swap_b32_e64 v84, v174\n"
      "v_mfma_f32_16x16x32_bf16 v[126:129], a[152:155], v[216:219], v[126:129]\n"
      "ds_write_b64 v140, v[194:195] offset:32\n"
      "v_add_f32 v84, v84, v174\n"
      "v_xor_b32 v122, 64, v8\n"
      "v_cvt_pk_bf16_f32 v167, v168, v169\n"
      "v_mfma_f32_16x16x32_bf16 v[196:199], a[200:203], v[216:219], v[196:199]\n"
      "buffer_load_dwordx4 v141, s[36:39], s79 offen offset:128  lds\n"
      "v_mfma_f32_16x16x32_bf16 v[130:133], a[172:175], v[234:237], v[130:133]\n"
      "ds_read_b128 v[250:253], v1 offset:4096\n"
      "v_rsq_f32_e64 v84, v84\n"
      "v_mfma_f32_16x16x32_bf16 v[130:133], a[176:179], v[216:219], v[130:133]\n"
      "s_waitcnt lgkmcnt(2)\n"
      "v_add_u32 v85, 49152, v122\n"
      "v_mov_b32 v174, v84\n"
      "v_mfma_f32_16x16x32_bf16 v[130:133], a[180:183], v[204:207], v[130:133]\n"
      "s_add_u32 m0, s75, 93952\n"
      "v_permlane16_swap_b32_e64 v84, v174\n"
      "v_mfma_f32_16x16x32_bf16 v[126:129], a[156:159], v[204:207], v[126:129]\n"
      "buffer_load_dwordx4 v141, s[36:39], s79 offen offset:256  lds\n"
      "s_waitcnt vmcnt(12)\n"
      "s_barrier\n"
      "ds_read_b64_tr_b16 a[222:223], v239 offset:38912\n"
      "v_pk_mul_f32 v[228:229], v[228:229], v[186:187] op_sel_hi:[1,0]\n"
      "v_mfma_f32_16x16x32_bf16 v[126:129], a[160:163], v[246:249], v[126:129]\n"
      "ds_read_b64_tr_b16 v[232:233], v138 offset:24576\n"
      "ds_read_b64_tr_b16 a[216:217], v239 offset:24576\n"
      "ds_read_b64_tr_b16 a[242:243], v239 offset:26632\n"
      "v_mfma_f32_16x16x32_bf16 v[126:129], a[164:167], v[242:245], v[126:129]\n"
      "buffer_load_dwordx4 v[184:187], v36, s[28:31], s49 offen\n"
      "v_mfma_f32_16x16x32_bf16 v[196:199], a[204:207], v[204:207], v[196:199]\n"
      "ds_read_b128 v[214:217], v1 offset:10240\n"
      "v_cvt_pk_bf16_f32 v146, v146, v147\n"
      "v_mfma_f32_16x16x32_bf16 v[196:199], a[208:211], v[246:249], v[196:199]\n"
      "ds_read_b64_tr_b16 a[246:247], v239 offset:38920\n"
      "ds_read_b64_tr_b16 a[240:241], v239 offset:24584\n"
      "v_mul_f32 v84, s50, v84\n"
      "v_mfma_f32_16x16x32_bf16 v[130:133], a[184:187], v[246:249], v[130:133]\n"
      "ds_read_b64_tr_b16 v[236:237], v138 offset:36864\n"
      "ds_read_b64_tr_b16 a[244:245], v239 offset:36872\n"
      "v_mfma_f32_16x16x32_bf16 v[130:133], a[188:191], v[242:245], v[130:133]\n"
      "ds_read_b64_tr_b16 a[220:221], v239 offset:36864\n"
      "buffer_load_dword v119, v29, s[28:31], s49 offen\n"
      "v_pk_mul_f32 v[162:163], v[162:163], v[84:85] op_sel_hi:[1,0]\n"
      "v_pk_mul_f32 v[96:97], v[96:97], v[84:85] op_sel_hi:[1,0]\n"
      "v_cvt_pk_bf16_f32 v168, v190, v191\n"
      "v_mfma_f32_16x16x32_bf16 v[196:199], a[212:215], v[242:245], v[196:199]\n"
      "ds_read_b64_tr_b16 a[218:219], v239 offset:26624\n"
      "v_cvt_pk_bf16_f32 v101, v102, v103\n"
      "v_mfma_f32_16x16x32_bf16 v[218:221], v[104:107], a[168:171], 0\n"
      "ds_read_b64_tr_b16 v[238:239], v138 offset:38912\n"
      "s_waitcnt lgkmcnt(5)\n"
      "v_mfma_f32_16x16x32_bf16 v[218:221], v[250:253], a[172:175], v[218:221]\n"
      "buffer_load_dwordx4 v[188:191], v36, s[28:31], s74 offen\n"
      "ds_read_b64_tr_b16 v[234:235], v138 offset:26624\n"
      "v_cvt_pk_bf16_f32 v170, v170, v171\n"
      "v_cvt_pk_bf16_f32 v130, v130, v131\n"
      "v_cvt_pk_bf16_f32 v102, v228, v229\n"
      "v_mov_b32 v123, 0xbc80bc80\n"
      "v_pk_mul_f32 v[178:179], v[178:179], v[174:175] op_sel_hi:[1,0]\n"
      "v_cvt_pk_bf16_f32 v171, v172, v173\n"
      "v_mov_b32 v124, 0xbc80bc80\n"
      "v_pk_mul_f32 v[176:177], v[176:177], v[174:175] op_sel_hi:[1,0]\n"
      "v_mov_b32 v125, 0xbc80bc80\n"
      "v_cvt_pk_bf16_f32 v126, v126, v127\n"
      "v_cvt_pk_bf16_f32 v127, v128, v129\n"
      "v_pk_mul_f32 v[142:143], v[142:143], v[84:85] op_sel_hi:[1,0]\n"
      "v_pk_mul_f32 v[144:145], v[144:145], v[84:85] op_sel_hi:[1,0]\n"
      "v_cvt_pk_bf16_f32 v169, v192, v193\n"
      "v_cvt_pk_bf16_f32 v162, v162, v163\n"
      "v_cvt_pk_bf16_f32 v147, v148, v149\n"
      "v_mfma_f32_16x16x32_bf16 v[226:229], v[104:107], a[144:147], 0\n"
      "buffer_load_dword v148, v29, s[28:31], s74 offen\n"
      "v_cvt_pk_bf16_f32 v131, v132, v133\n"
      "s_add_u32 m0, s64, 18432\n"
      "v_pk_mul_f32 v[160:161], v[160:161], v[174:175] op_sel_hi:[1,0]\n"
      "v_pk_mul_f32 v[202:203], v[202:203], v[174:175] op_sel_hi:[1,0]\n"
      "v_pk_mul_f32 v[200:201], v[200:201], v[174:175] op_sel_hi:[1,0]\n"
      "v_cvt_pk_bf16_f32 v103, v230, v231\n"
      "v_pk_mul_f32 v[158:159], v[158:159], v[174:175] op_sel_hi:[1,0]\n"
      "v_cvt_pk_bf16_f32 v196, v196, v197\n"
      "v_pk_mul_f32 v[164:165], v[164:165], v[84:85] op_sel_hi:[1,0]\n"
      "v_cvt_pk_bf16_f32 v163, v164, v165\n"
      "v_pk_mul_f32 v[98:99], v[98:99], v[84:85] op_sel_hi:[1,0]\n"
      "v_cvt_pk_bf16_f32 v208, v208, v209\n"
      "v_cvt_pk_bf16_f32 v197, v198, v199\n"
      "v_cvt_pk_bf16_f32 v200, v200, v201\n"
      "v_mfma_f32_16x16x32_bf16 v[226:229], v[250:253], a[148:151], v[226:229]\n"
      "buffer_load_dwordx4 v16, s[16:19], s45 offen  lds\n"
      "v_subrev_u32 v39, 115456, v9\n"
      "v_cvt_pk_bf16_f32 v176, v176, v177\n"
      "v_mfma_f32_16x16x32_bf16 v[226:229], v[134:137], a[152:155], v[226:229]\n"
      "s_add_u32 m0, s64, 22528\n"
      "v_cvt_pk_bf16_f32 v201, v202, v203\n"
      "v_cvt_pk_bf16_f32 v142, v142, v143\n"
      "v_mfma_f32_16x16x32_bf16 v[226:229], v[214:217], a[156:159], v[226:229]\n"
      "ds_read_b128 v[202:205], v8 offset:24576\n"
      "ds_read_b128 v[240:243], v122 offset:24576\n"
      "v_mfma_f32_16x16x32_bf16 v[218:221], v[134:137], a[176:179], v[218:221]\n"
      "buffer_load_dwordx4 v17, s[16:19], s45 offen  lds\n"
      "v_cvt_pk_bf16_f32 v151, v152, v153\n"
      "v_mfma_f32_16x16x32_bf16 v[172:175], v[104:107], a[192:195], 0\n"
      "s_add_u32 m0, s64, 26624\n"
      "ds_read_b128 v[192:195], v8 offset:28672\n"
      "v_cvt_pk_bf16_f32 v177, v178, v179\n"
      "v_mfma_f32_16x16x32_bf16 v[172:175], v[250:253], a[196:199], v[172:175]\n"
      "ds_read_b128 v[246:249], v122 offset:28672\n"
      "v_lshrrev_b32_e64 v39, 4, v39\n"
      "v_mfma_f32_16x16x32_bf16 v[172:175], v[134:137], a[200:203], v[172:175]\n"
      "ds_read_b128 v[252:255], v0 offset:2048\n"
      "v_cvt_pk_bf16_f32 v143, v144, v145\n"
      "v_mfma_f32_16x16x32_bf16 v[172:175], v[214:217], a[204:207], v[172:175]\n"
      "ds_read_b128 v[134:137], v1 offset:2048\n"
      "buffer_load_dwordx4 v18, s[16:19], s45 offen  lds\n"
      "v_mfma_f32_16x16x32_bf16 v[218:221], v[214:217], a[180:183], v[218:221]\n"
      "s_mov_b32 exec_lo, 0x3f003f\n"
      "s_mov_b32 exec_hi, 0x3f003f\n"
      "ds_write_b64 v140, v[126:127] offset:18688\n"
      "ds_write_b64 v140, v[130:131] offset:18720\n"
      "ds_write_b64 v140, v[196:197] offset:18752\n"
      "s_mov_b64 exec, -1\n"
      "s_add_u32 m0, s64, 30720\n"
      "s_waitcnt lgkmcnt(3)\n"
      "v_mfma_f32_16x16x32_bf16 v[104:107], v[252:255], a[168:171], 0\n"
      "ds_read_b128 v[126:129], v122 offset:32768\n"
      "v_cvt_pk_bf16_f32 v145, v98, v99\n"
      "v_mov_b32 v122, 0xbc80bc80\n"
      "v_cvt_pk_bf16_f32 v178, v158, v159\n"
      "v_mfma_f32_16x16x32_bf16 v[104:107], v[134:137], a[172:175], v[104:107]\n"
      "ds_read_b128 v[214:217], v0 offset:16384\n"
      "v_cvt_pk_bf16_f32 v179, v160, v161\n"
      "v_mfma_f32_16x16x32_bf16 v[158:161], v[202:205], v[122:125], 0\n"
      "buffer_load_dwordx4 v19, s[16:19], s45 offen  lds\n"
      "v_mfma_f32_16x16x32_bf16 v[158:161], v[240:243], v[122:125], v[158:161]\n"
      "s_waitcnt vmcnt(14)\n"
      "s_barrier\n"
      "s_add_u32 m0, s65, 34816\n"
      "v_cvt_pk_bf16_f32 v144, v96, v97\n"
      "v_mfma_f32_16x16x32_bf16 v[158:161], v[192:195], v[122:125], v[158:161]\n"
      "ds_read_b128 v[130:133], v8 offset:32768\n"
      "ds_read_b128 v[202:205], v0 offset:8192\n"
      "v_mfma_f32_16x16x32_bf16 v[158:161], v[246:249], v[122:125], v[158:161]\n"
      "ds_read_b64_tr_b16 a[228:229], v82 offset:12288\n"
      "v_cvt_pk_bf16_f32 v222, v222, v223\n"
      "v_mfma_f32_16x16x32_bf16 v[96:99], v[252:255], a[192:195], 0\n"
      "buffer_load_dwordx4 v15, s[16:19], s45 offen  lds\n"
      "ds_read_b64_tr_b16 a[230:231], v82 offset:14336\n"
      "ds_read_b64_tr_b16 v[242:243], v78 offset:2048\n"
      "v_mfma_f32_16x16x32_bf16 v[194:197], v[252:255], a[144:147], 0\n"
      "ds_read_b64_tr_b16 a[250:251], v82 offset:2056\n"
      "v_cvt_pk_bf16_f32 v223, v224, v225\n"
      "v_mfma_f32_16x16x32_bf16 v[194:197], v[134:137], a[148:151], v[194:197]\n"
      "ds_read_b64_tr_b16 a[254:255], v82 offset:14344\n"
      "ds_read_b64_tr_b16 a[252:253], v82 offset:12296\n"
      "v_mfma_f32_16x16x32_bf16 v[96:99], v[134:137], a[196:199], v[96:99]\n"
      "ds_read_b64_tr_b16 v[246:247], v78 offset:14336\n"
      "ds_read_b64_tr_b16 a[224:225], v82\n"
      "s_waitcnt lgkmcnt(7)\n"
      "v_mfma_f32_16x16x32_bf16 v[96:99], v[202:205], a[200:203], v[96:99]\n"
      "ds_read_b128 v[250:253], v1 offset:8192\n"
      "ds_read_b64_tr_b16 v[240:241], v78\n"
      "v_mfma_f32_16x16x32_bf16 v[158:161], v[130:133], v[122:125], v[158:161]\n"
      "ds_read_b64_tr_b16 v[244:245], v78 offset:12288\n"
      "ds_read_b64_tr_b16 a[226:227], v82 offset:2048\n"
      "v_mfma_f32_16x16x32_bf16 v[158:161], v[126:129], v[122:125], v[158:161]\n"
      "ds_read_b64_tr_b16 a[248:249], v82 offset:8\n"
      "v_cvt_pk_bf16_f32 v209, v210, v211\n"
      "v_mfma_f32_16x16x32_bf16 v[104:107], v[202:205], a[176:179], v[104:107]\n"
      "ds_read_b128 v[126:129], v1 offset:16384\n"
      "v_and_b32 v39, 48, v39\n"
      "v_mfma_f32_16x16x32_bf16 v[218:221], v[214:217], a[184:187], v[218:221]\n"
      "ds_read_b128 v[130:133], v0 offset:14336\n"
      "ds_read_b128 v[134:137], v1 offset:14336\n"
      "v_mfma_f32_16x16x32_bf16 v[226:229], v[214:217], a[160:163], v[226:229]\n"
      "s_mov_b32 exec_lo, 0x3f003f\n"
      "s_mov_b32 exec_hi, 0x3f003f\n"
      "ds_write_b128 v139, v[142:145] offset:18688\n"
      "ds_write_b64 v139, v[162:163] offset:18704\n"
      "ds_write_b64 v139, v[200:201] offset:18712\n"
      "ds_write_b128 v139, v[176:179] offset:18720\n"
      "s_mov_b64 exec, -1\n"
      "v_mfma_f32_16x16x32_bf16 v[172:175], v[214:217], a[208:211], v[172:175]\n"
      "ds_read_b128 v[176:179], v85\n"
      "v_mul_f32 v161, 0x3eaaaaab, v161\n"
      "v_mfma_f32_16x16x32_bf16 v[194:197], v[202:205], a[152:155], v[194:197]\n"
      "s_waitcnt lgkmcnt(5)\n"
      "v_add_u32 v139, 49152, v8\n"
      "v_mul_f32 v160, 0x3eaaaaab, v160\n"
      "v_mfma_f32_16x16x32_bf16 v[194:197], v[250:253], a[156:159], v[194:197]\n"
      "ds_read_b128 v[202:205], v139 offset:4096\n"
      "v_add_u32 v39, 123136, v39\n"
      "v_mfma_f32_16x16x32_bf16 v[104:107], v[250:253], a[180:183], v[104:107]\n"
      "buffer_load_dwordx4 v[214:217], v36, s[28:31], s47 offen\n"
      "v_mfma_f32_16x16x32_bf16 v[226:229], v[126:129], a[164:167], v[226:229]\n"
      "ds_write_b64 v140, v[222:223]\n"
      "v_mfma_f32_16x16x32_bf16 v[96:99], v[250:253], a[204:207], v[96:99]\n"
      "ds_read_b128 v[250:253], v139\n"
      "v_add_u32_e64 v255, s92, v39\n"
      "v_mfma_f32_16x16x32_bf16 v[96:99], v[130:133], a[208:211], v[96:99]\n"
      "ds_read_b128 v[162:165], v139 offset:8192\n"
      "v_mul_f32 v159, 0x3eaaaaab, v159\n"
      "v_mul_f32 v158, 0x3eaaaaab, v158\n"
      "v_mfma_f32_16x16x32_bf16 v[96:99], v[134:137], a[212:215], v[96:99]\n"
      "ds_read_b128 v[198:201], v85 offset:8192\n"
      "v_cvt_pk_bf16_f32 v76, v226, v227\n"
      "v_mfma_f32_16x16x32_bf16 v[218:221], v[126:129], a[188:191], v[218:221]\n"
      "ds_write_b64 v140, v[208:209] offset:64\n"
      "v_mfma_f32_16x16x32_bf16 v[172:175], v[126:129], a[212:215], v[172:175]\n"
      "ds_read_b128 v[224:227], v85 offset:4096\n"
      "v_cvt_pk_bf16_f32 v77, v228, v229\n"
      "v_mfma_f32_16x16x32_bf16 v[126:129], v[180:183], v[154:157], v[44:47]\n"
      "buffer_load_dword v118, v29, s[28:31], s47 offen\n"
      "v_mfma_f32_16x16x32_bf16 v[104:107], v[130:133], a[184:187], v[104:107]\n"
      "ds_read_b128 v[142:145], v1\n"
      "v_cvt_pk_bf16_f32 v99, v98, v99\n"
      "v_mfma_f32_16x16x32_bf16 v[194:197], v[130:133], a[160:163], v[194:197]\n"
      "ds_read_b128 v[130:133], v0\n"
      "v_cvt_pk_bf16_f32 v81, v220, v221\n"
      "v_mfma_f32_16x16x32_bf16 v[194:197], v[134:137], a[164:167], v[194:197]\n"
      "s_waitcnt lgkmcnt(5)\n"
      "v_cvt_pk_bf16_f32 v98, v96, v97\n"
      "v_subrev_u32 v222, 115456, v9\n"
      "v_mfma_f32_16x16x32_bf16 v[104:107], v[134:137], a[188:191], v[104:107]\n"
      "ds_write_b128 v255, v[158:161]\n"
      "v_mfma_f32_16x16x32_bf16 v[208:211], v[250:253], v[122:125], 0\n"
      "s_waitcnt vmcnt(11)\n"
      "s_barrier\n"
      "v_cvt_pk_bf16_f32 v80, v218, v219\n"
      "v_mfma_f32_16x16x32_bf16 v[208:211], v[176:179], v[122:125], v[208:211]\n"
      "ds_read_b128 v[228:231], v139 offset:28672\n"
      "ds_read_b128 v[250:253], v139 offset:24576\n"
      "v_mfma_f32_16x16x32_bf16 v[208:211], v[202:205], v[122:125], v[208:211]\n"
      "ds_read_b128 v[202:205], v85 offset:24576\n"
      "s_waitcnt lgkmcnt(4)\n"
      "v_mfma_f32_16x16x32_bf16 v[208:211], v[224:227], v[122:125], v[208:211]\n"
      "ds_read_b128 v[224:227], v85 offset:32768\n"
      "v_cvt_pk_bf16_f32 v107, v106, v107\n"
      "v_bfe_u32 v96, v222, 8, 2\n"
      "v_mfma_f32_16x16x32_bf16 v[208:211], v[162:165], v[122:125], v[208:211]\n"
      "ds_read_b128 v[218:221], v85 offset:28672\n"
      "v_cvt_pk_bf16_f32 v84, v172, v173\n"
      "v_mfma_f32_16x16x32_bf16 v[208:211], v[198:201], v[122:125], v[208:211]\n"
      "ds_read_b128 v[134:137], v139 offset:32768\n"
      "v_cvt_pk_bf16_f32 v106, v104, v105\n"
      "v_mfma_f32_16x16x16_bf16 v[126:129], v[150:151], v[146:147], v[126:129]\n"
      "v_cvt_pk_bf16_f32 v85, v174, v175\n"
      "v_lshlrev_b32_e64 v96, 2, v96\n"
      "v_cvt_pk_bf16_f32 v197, v196, v197\n"
      "v_mfma_f32_16x16x32_bf16 v[138:141], v[130:133], a[144:147], 0\n"
      "ds_read_b128 v[176:179], v0 offset:6144\n"
      "v_bfe_u32 v222, v222, 4, 4\n"
      "v_mfma_f32_16x16x32_bf16 v[138:141], v[142:145], a[148:151], v[138:141]\n"
      "ds_read_b64_tr_b16 a[238:239], v82 offset:38912\n"
      "s_waitcnt lgkmcnt(5)\n"
      "v_mfma_f32_16x16x32_bf16 v[162:165], v[250:253], v[122:125], 0\n"
      "ds_read_b64_tr_b16 v[252:253], v78 offset:36864\n"
      "s_waitcnt vmcnt(10)\n"
      "v_or3_b32 v184, v184, v185, v186\n"
      "v_mfma_f32_16x16x32_bf16 v[162:165], v[202:205], v[122:125], v[162:165]\n"
      "ds_read_b128 v[202:205], v10 offset:32\n"
      "v_mul_f32 v211, 0x3eaaaaab, v211\n"
      "v_mfma_f32_16x16x32_bf16 v[162:165], v[228:231], v[122:125], v[162:165]\n"
      "ds_read_b128 v[198:201], v1 offset:6144\n"
      "s_waitcnt lgkmcnt(5)\n"
      "v_mfma_f32_16x16x32_bf16 v[162:165], v[218:221], v[122:125], v[162:165]\n"
      "ds_read_b64_tr_b16 a[236:237], v82 offset:36864\n"
      "v_cvt_pk_bf16_f32 v196, v194, v195\n"
      "v_mfma_f32_16x16x32_bf16 v[162:165], v[134:137], v[122:125], v[162:165]\n"
      "ds_read_b64_tr_b16 v[230:231], v82 offset:38920\n"
      "v_sub_u32 v222, v222, v96\n"
      "v_mfma_f32_16x16x32_bf16 v[162:165], v[224:227], v[122:125], v[162:165]\n"
      "ds_read_b64_tr_b16 v[228:229], v82 offset:36872\n"
      "v_mul_f32 v208, 0x3eaaaaab, v208\n"
      "v_mfma_f32_16x16x32_bf16 v[218:221], v[130:133], a[192:195], 0\n"
      "ds_read_b64 v[206:207], v10 offset:24\n"
      "v_mul_f32 v209, 0x3eaaaaab, v209\n"
      "v_mov_b32 v97, 0\n"
      "v_mfma_f32_16x16x32_bf16 v[218:221], v[142:145], a[196:199], v[218:221]\n"
      "s_waitcnt lgkmcnt(5)\n"
      "s_waitcnt vmcnt(9)\n"
      "v_or3_b32 v184, v184, v187, v119\n"
      "v_mul_f32 v162, 0x3eaaaaab, v162\n"
      "v_mfma_f32_16x16x32_bf16 v[122:125], v[130:133], a[168:171], 0\n"
      "ds_read_b128 v[134:137], v1 offset:12288\n"
      "v_mul_f32 v163, 0x3eaaaaab, v163\n"
      "v_mfma_f32_16x16x32_bf16 v[122:125], v[142:145], a[172:175], v[122:125]\n"
      "ds_read_b64_tr_b16 v[250:251], v78 offset:26624\n"
      "v_mul_f32 v164, 0x3eaaaaab, v164\n"
      "v_mfma_f32_16x16x32_bf16 v[122:125], v[176:179], a[176:179], v[122:125]\n"
      "ds_read_b128 v[130:133], v0 offset:12288\n"
      "s_waitcnt vmcnt(8)\n"
      "v_or3_b32 v188, v188, v189, v190\n"
      "v_mfma_f32_16x16x32_bf16 v[158:161], v[166:169], v[154:157], v[40:43]\n"
      "ds_write_b32 v31, v184 offset:6656\n"
      "v_mfma_f32_16x16x32_bf16 v[172:175], v[202:205], v[154:157], v[24:27]\n"
      "ds_read_b64_tr_b16 a[234:235], v82 offset:26624\n"
      "v_mul_f32 v165, 0x3eaaaaab, v165\n"
      "v_mfma_f32_16x16x32_bf16 v[138:141], v[176:179], a[152:155], v[138:141]\n"
      "ds_read_b64_tr_b16 v[224:225], v82 offset:24584\n"
      "v_mul_f32 v210, 0x3eaaaaab, v210\n"
      "v_mfma_f32_16x16x32_bf16 v[218:221], v[176:179], a[200:203], v[218:221]\n"
      "ds_write_b128 v255, v[208:211] offset:256\n"
      "v_mfma_f32_16x16x32_bf16 v[208:211], v[202:205], v[108:111], v[26:29]\n"
      "ds_read_b64_tr_b16 a[232:233], v82 offset:24576\n"
      "s_waitcnt lgkmcnt(5)\n"
      "v_mfma_f32_16x16x32_bf16 v[138:141], v[198:201], a[156:159], v[138:141]\n"
      "ds_read_b64_tr_b16 v[248:249], v78 offset:24576\n"
      "s_waitcnt vmcnt(7)\n"
      "v_or3_b32 v188, v188, v191, v148\n"
      "v_mfma_f32_16x16x32_bf16 v[122:125], v[198:201], a[180:183], v[122:125]\n"
      "ds_write_b128 v255, v[162:165] offset:512\n"
      "v_mfma_f32_16x16x32_bf16 v[122:125], v[130:133], a[184:187], v[122:125]\n"
      "ds_read_b64_tr_b16 v[226:227], v82 offset:26632\n"
      "v_mov_b32 v96, 0x3f80\n"
      "v_mfma_f32_16x16x32_bf16 v[176:179], v[112:115], v[154:157], v[48:51]\n"
      "ds_read_b64_tr_b16 v[254:255], v78 offset:38912\n"
      "v_lshlrev_b32_e64 v222, 4, v222\n"
      "v_mfma_f32_16x16x32_bf16 v[138:141], v[130:133], a[160:163], v[138:141]\n"
      "ds_write_b32 v31, v188 offset:5632\n"
      "v_mfma_f32_16x16x32_bf16 v[138:141], v[134:137], a[164:167], v[138:141]\n"
      "s_waitcnt lgkmcnt(0)\n"
      "s_barrier\n"
      "v_mov_b32_e64 v191, 0\n"
      "v_mfma_f32_16x16x16_bf16 v[158:161], v[120:121], v[146:147], v[158:161]\n"
      "ds_read_b128 v[184:187], v9 offset:6656\n"
      "v_lshlrev_b64 v[96:97], v222, v[96:97]\n"
      "v_mfma_f32_16x16x32_bf16 v[122:125], v[134:137], a[188:191], v[122:125]\n"
      "ds_read_b128 v[162:165], v9 offset:5632\n"
      "v_cmp_gt_u32 s[80:81], 64, v222\n"
      "v_mfma_f32_16x16x32_bf16 v[218:221], v[198:201], a[204:207], v[218:221]\n"
      "ds_read_b128 v[198:201], v39 offset:64\n"
      "v_cndmask_b32_e64 v156, 0, v96, s[80:81]\n"
      "v_mfma_f32_16x16x16_bf16 v[176:179], v[116:117], v[146:147], v[176:179]\n"
      "v_mov_b32_e64 v155, 0\n"
      "v_cndmask_b32_e64 v189, 0, v97, s[80:81]\n"
      "v_cvt_pk_bf16_f32 v194, v138, v139\n"
      "v_mfma_f32_16x16x16_bf16 v[172:175], v[206:207], v[146:147], v[172:175]\n"
      "v_cndmask_b32_e64 v188, 0, v96, s[80:81]\n"
      "v_cvt_pk_bf16_f32 v104, v122, v123\n"
      "v_mov_b32_e64 v154, 0\n"
      "v_mfma_f32_16x16x32_bf16 v[218:221], v[130:133], a[208:211], v[218:221]\n"
      "ds_read_b128 v[130:133], v39\n"
      "v_cndmask_b32_e64 v157, 0, v97, s[80:81]\n"
      "v_mfma_f32_16x16x32_bf16 v[218:221], v[134:137], a[212:215], v[218:221]\n"
      "s_waitcnt lgkmcnt(3)\n"
      "v_or3_b32 v184, v184, v185, v186\n"
      "v_mov_b32 v190, 0\n"
      "v_mfma_f32_16x16x16_bf16 v[208:211], v[206:207], v[212:213], v[208:211]\n"
      "v_cvt_pk_bf16_f32 v195, v140, v141\n"
      "v_bitop3_b32 v184, v184, v187, s33 bitop3:0xa8\n"
      "v_cmp_ne_u32 s[56:57], 0, v184\n"
      "v_mfma_f32_16x16x32_bf16 v[140:143], v[166:169], v[108:111], v[52:55]\n"
      "s_waitcnt lgkmcnt(1)\n"
      "v_cvt_pk_bf16_f32 v97, v220, v221\n"
      "v_or3_b32 v162, v162, v163, v164\n"
      "v_mfma_f32_16x16x32_bf16 v[184:187], v[154:157], v[232:235], v[198:201]\n"
      "s_or_b64 vcc, s[56:57], s[54:55]\n"
      "v_cvt_pk_bf16_f32 v96, v218, v219\n"
      "v_bitop3_b32 v162, v162, v165, s33 bitop3:0xa8\n"
      "v_mfma_f32_16x16x32_bf16 v[218:221], v[154:157], a[216:219], v[198:201]\n"
      "s_cbranch_vccnz L_rare_1_%=\n"
      "L_back_2_%=:\n"
      "v_mfma_f32_16x16x32_bf16 v[134:137], v[154:157], a[240:243], v[198:201]\n"
      "s_nop 2\n"
      "s_cbranch_vccnz L_rare_3_%=\n"
      "L_back_4_%=:\n"
      "v_mfma_f32_16x16x32_bf16 v[198:201], v[112:115], v[108:111], v[60:63]\n"
      "s_waitcnt lgkmcnt(0)\n"
      "v_cvt_pk_bf16_f32 v105, v124, v125\n"
      "v_cmp_ne_u32 s[56:57], 0, v162\n"
      "v_mfma_f32_16x16x32_bf16 v[122:125], v[188:191], v[232:235], v[130:133]\n"
      "v_cvt_pk_bf16_f32 v234, v184, v185\n"
      "v_cvt_pk_bf16_f32 v235, v186, v187\n"
      "v_exp_f32 v159, v159\n"
      "v_cvt_pk_bf16_f32 v134, v134, v135\n"
      "v_mfma_f32_16x16x16_bf16 v[140:143], v[120:121], v[212:213], v[140:143]\n"
      "v_exp_f32 v179, v179\n"
      "v_cvt_pk_bf16_f32 v136, v136, v137\n"
      "v_mfma_f32_16x16x16_bf16 v[198:201], v[116:117], v[212:213], v[198:201]\n"
      "v_cvt_pk_bf16_f32 v233, v124, v125\n"
      "v_exp_f32 v161, v161\n"
      "v_exp_f32 v158, v158\n"
      "v_cvt_pk_bf16_f32 v218, v218, v219\n"
      "v_mfma_f32_16x16x32_bf16 v[184:187], v[180:183], v[108:111], v[56:59]\n"
      "ds_read_b128 v[108:111], v10\n"
      "v_cvt_pk_bf16_f32 v232, v122, v123\n"
      "v_mfma_f32_16x16x32_bf16 v[162:165], v[188:191], a[216:219], v[130:133]\n"
      "v_exp_f32_e64 v129, v129\n"
      "v_mfma_f32_16x16x32_bf16 v[122:125], v[188:191], a[240:243], v[130:133]\n"
      "ds_read_b64 v[222:223], v10 offset:16\n"
      "v_accvgpr_write_b32 a243, v136\n"
      "v_cvt_pk_bf16_f32 v158, v158, v159\n"
      "v_mfma_f32_16x16x16_bf16 v[184:187], v[150:151], v[212:213], v[184:187]\n"
      "v_accvgpr_write_b32 a218, v218\n"
      "v_accvgpr_write_b32 a242, v134\n"
      "v_cvt_pk_bf16_f32 v162, v162, v163\n"
      "v_exp_f32 v128, v128\n"
      "v_exp_f32 v172, v172\n"
      "v_cvt_pk_bf16_f32 v164, v164, v165\n"
      "v_cvt_pk_bf16_f32 v122, v122, v123\n"
      "v_cvt_pk_bf16_f32 v220, v220, v221\n"
      "v_mfma_f32_16x16x32_bf16 v[130:133], v[180:183], v[100:103], v[68:71]\n"
      "s_waitcnt lgkmcnt(1)\n"
      "v_exp_f32 v126, v126\n"
      "v_exp_f32 v160, v160\n"
      "v_accvgpr_write_b32 a240, v122\n"
      "v_mfma_f32_16x16x32_bf16 v[134:137], v[108:111], v[180:183], v[30:33]\n"
      "s_cbranch_vccnz L_rare_5_%=\n"
      "L_back_6_%=:\n"
      "v_accvgpr_write_b32 a217, v164\n"
      "v_exp_f32 v177, v177\n"
      "v_mfma_f32_16x16x16_bf16 v[130:133], v[150:151], v[170:171], v[130:133]\n"
      "v_cvt_pk_bf16_f32 v159, v160, v161\n"
      "v_exp_f32 v187, v187\n"
      "v_exp_f32_e64 v176, v176\n"
      "v_mfma_f32_16x16x32_bf16 v[144:147], v[166:169], v[100:103], v[64:67]\n"
      "s_waitcnt lgkmcnt(0)\n"
      "v_exp_f32 v127, v127\n"
      "v_mfma_f32_16x16x16_bf16 v[134:137], v[222:223], v[150:151], v[134:137]\n"
      "v_exp_f32_e64 v199, v199\n"
      "v_cvt_pk_bf16_f32 v161, v128, v129\n"
      "v_mfma_f32_16x16x32_bf16 v[148:151], v[108:111], v[166:169], v[30:33]\n"
      "ds_read_u16 v82, v11 offset:32\n"
      "v_exp_f32_e64 v208, v208\n"
      "v_mfma_f32_16x16x32_bf16 v[180:183], v[202:205], v[100:103], v[28:31]\n"
      "ds_read_u16 v78, v11\n"
      "v_cvt_pk_bf16_f32 v160, v126, v127\n"
      "v_mfma_f32_16x16x16_bf16 v[144:147], v[120:121], v[170:171], v[144:147]\n"
      "v_exp_f32 v201, v201\n"
      "v_accvgpr_write_b32 a216, v162\n"
      "v_exp_f32 v178, v178\n"
      "v_mfma_f32_16x16x16_bf16 v[180:183], v[206:207], v[170:171], v[180:183]\n"
      "v_exp_f32_e64 v141, v141\n"
      "v_cvt_pk_bf16_f32 v90, v172, 0\n"
      "v_mfma_f32_16x16x32_bf16 v[126:129], v[112:115], v[100:103], v[72:75]\n"
      "ds_read_u16 v86, v11 offset:64\n"
      "v_exp_f32_e64 v200, v200\n"
      "v_mfma_f32_16x16x32_bf16 v[162:165], v[108:111], v[202:205], v[30:33]\n"
      "ds_read_b128 v[202:205], v39 offset:128\n"
      "v_cvt_pk_bf16_f32 v88, v176, v177\n"
      "v_mfma_f32_16x16x16_bf16 v[148:151], v[222:223], v[120:121], v[148:151]\n"
      "v_exp_f32 v143, v143\n"
      "v_exp_f32 v140, v140\n"
      "v_cvt_pk_bf16_f32 v89, v178, v179\n"
      "v_mfma_f32_16x16x32_bf16 v[174:177], v[108:111], v[112:115], v[30:33]\n"
      "ds_read_b128 v[108:111], v39 offset:192\n"
      "v_exp_f32 v185, v185\n"
      "v_mfma_f32_16x16x16_bf16 v[162:165], v[222:223], v[206:207], v[162:165]\n"
      "v_cvt_pk_bf16_f32 v124, v124, v125\n"
      "v_cvt_pk_bf16_f32 v94, v208, 0\n"
      "s_waitcnt vmcnt(1)\n"
      "v_or3_b32 v214, v214, v215, v216\n"
      "v_mfma_f32_16x16x32_bf16 v[206:209], v[194:197], v[158:161], 0\n"
      "s_waitcnt lgkmcnt(2)\n"
      "v_exp_f32 v142, v142\n"
      "v_mfma_f32_16x16x32_bf16 v[206:209], v[76:79], v[88:91], v[206:209]\n"
      "v_exp_f32_e64 v186, v186\n"
      "v_mfma_f32_16x16x16_bf16 v[126:129], v[116:117], v[170:171], v[126:129]\n"
      "v_cvt_pk_bf16_f32 v140, v140, v141\n"
      "v_exp_f32_e64 v184, v184\n"
      "v_mfma_f32_16x16x32_bf16 v[166:169], v[96:99], v[158:161], 0\n"
      "v_cvt_pk_bf16_f32 v93, v200, v201\n"
      "v_cndmask_b32 v148, v148, v134, s[58:59]\n"
      "v_mfma_f32_16x16x32_bf16 v[166:169], v[84:87], v[88:91], v[166:169]\n"
      "s_waitcnt lgkmcnt(0)\n"
      "v_cvt_pk_bf16_f32 v141, v142, v143\n"
      "v_accvgpr_write_b32 a219, v220\n"
      "v_mfma_f32_16x16x32_bf16 v[218:221], v[154:157], v[236:239], v[108:111]\n"
      "s_cbranch_vccnz L_rare_7_%=\n"
      "L_back_8_%=:\n"
      "v_mfma_f32_16x16x32_bf16 v[170:173], v[32:35], v[158:161], 0\n"
      "ds_read_b128 v[134:137], v39 offset:320\n"
      "v_exp_f32_e64 v132, v132\n"
      "v_cvt_pk_bf16_f32 v142, v184, v185\n"
      "v_mfma_f32_16x16x32_bf16 v[170:173], v[32:35], v[88:91], v[170:173]\n"
      "s_or_b32 s95, s84, s85\n"
      "v_exp_f32 v133, v133\n"
      "v_mfma_f32_16x16x16_bf16 v[174:177], v[222:223], v[116:117], v[174:177]\n"
      "v_exp_f32_e64 v198, v198\n"
      "v_cvt_pk_bf16_f32 v143, v186, v187\n"
      "v_mfma_f32_16x16x32_bf16 v[184:187], v[188:191], v[236:239], v[202:205]\n"
      "v_exp_f32 v127, v127\n"
      "v_mfma_f32_16x16x32_bf16 v[100:103], v[188:191], a[220:223], v[202:205]\n"
      "v_rcp_f32 v170, v170\n"
      "v_mfma_f32_16x16x32_bf16 v[120:123], v[104:107], v[158:161], 0\n"
      "ds_read_b128 v[158:161], v39 offset:256\n"
      "v_exp_f32 v144, v144\n"
      "v_mfma_f32_16x16x32_bf16 v[120:123], v[80:83], v[88:91], v[120:123]\n"
      "v_exp_f32 v128, v128\n"
      "v_mfma_f32_16x16x32_bf16 v[176:179], v[188:191], a[244:247], v[202:205]\n"
      "s_waitcnt lgkmcnt(1)\n"
      "v_exp_f32 v126, v126\n"
      "v_mfma_f32_16x16x32_bf16 v[202:205], v[154:157], a[248:251], v[134:137]\n"
      "v_cvt_pk_bf16_f32 v100, v100, v101\n"
      "v_exp_f32 v131, v131\n"
      "v_cvt_pk_bf16_f32 v92, v198, v199\n"
      "v_exp_f32 v145, v145\n"
      "v_pk_mul_f32 v[206:207], v[206:207], v[170:171] op_sel_hi:[1,0]\n"
      "v_exp_f32_e64 v129, v129\n"
      "v_pk_mul_f32 v[122:123], v[122:123], v[170:171] op_sel_hi:[1,0]\n"
      "v_cvt_pk_bf16_f32 v102, v102, v103\n"
      "v_pk_mul_f32 v[208:209], v[208:209], v[170:171] op_sel_hi:[1,0]\n"
      "v_cvt_pk_bf16_f32 v236, v184, v185\n"
      "v_mfma_f32_16x16x32_bf16 v[182:185], v[154:157], a[244:247], v[108:111]\n"
      "s_or_b32 s95, s95, s48\n"
      "v_exp_f32 v147, v147\n"
      "v_mfma_f32_16x16x32_bf16 v[150:153], v[154:157], a[220:223], v[108:111]\n"
      "s_cmp_ge_u32 s95, 0x40000000\n"
      "s_cselect_b32 s94, -1, -2\n"
      "v_cndmask_b32 v148, v148, v174, s[60:61]\n"
      "v_cvt_pk_bf16_f32 v89, v128, v129\n"
      "v_accvgpr_write_b32 a220, v100\n"
      "v_cvt_pk_bf16_f32 v239, v220, v221\n"
      "v_pk_mul_f32 v[166:167], v[166:167], v[170:171] op_sel_hi:[1,0]\n"
      "v_exp_f32 v180, v180\n"
      "v_pk_mul_f32 v[120:121], v[120:121], v[170:171] op_sel_hi:[1,0]\n"
      "v_mfma_f32_16x16x32_bf16 v[108:111], v[154:157], v[240:243], v[134:137]\n"
      "v_cndmask_b32 v148, v148, v162, s[62:63]\n"
      "v_cvt_pk_bf16_f32 v184, v184, v185\n"
      "v_cvt_pk_bf16_f32 v204, v204, v205\n"
      "v_mfma_f32_16x16x32_bf16 v[112:115], v[154:157], a[224:227], v[134:137]\n"
      "s_waitcnt lgkmcnt(0)\n"
      "v_exp_f32 v130, v130\n"
      "v_mfma_f32_16x16x32_bf16 v[162:165], v[188:191], a[224:227], v[158:161]\n"
      "s_waitcnt vmcnt(0)\n"
      "v_or3_b32 v214, v214, v217, v118\n"
      "v_cvt_pk_bf16_f32 v152, v152, v153\n"
      "v_mfma_f32_16x16x32_bf16 v[220:223], v[188:191], v[240:243], v[158:161]\n"
      "v_exp_f32 v146, v146\n"
      "v_cvt_pk_bf16_f32 v237, v186, v187\n"
      "v_cvt_pk_bf16_f32 v144, v144, v145\n"
      "v_mfma_f32_16x16x32_bf16 v[134:137], v[188:191], a[248:251], v[158:161]\n"
      "v_cvt_pk_bf16_f32 v178, v178, v179\n"
      "v_cvt_pk_bf16_f32 v206, v206, v207\n"
      "v_mfma_f32_16x16x32_bf16 v[158:161], v[32:35], v[140:143], 0\n"
      "s_cbranch_vccnz L_rare_9_%=\n"
      "L_back_10_%=:\n"
      "v_cvt_pk_bf16_f32 v240, v220, v221\n"
      "v_mfma_f32_16x16x32_bf16 v[158:161], v[32:35], v[92:95], v[158:161]\n"
      "v_cvt_pk_bf16_f32 v114, v114, v115\n"
      "v_cvt_pk_bf16_f32 v145, v146, v147\n"
      "v_cvt_pk_bf16_f32 v207, v208, v209\n"
      "v_cvt_pk_bf16_f32 v146, v130, v131\n"
      "v_mfma_f32_16x16x32_bf16 v[128:131], v[194:197], v[140:143], 0\n"
      "s_cmp_gt_u32 s42, s44\n"
      "s_cselect_b32 s95, s94, s95\n"
      "v_cvt_pk_bf16_f32 v88, v126, v127\n"
      "v_mfma_f32_16x16x32_bf16 v[128:131], v[76:79], v[92:95], v[128:131]\n"
      "v_exp_f32_e64 v148, v148\n"
      "v_cvt_pk_bf16_f32 v241, v222, v223\n"
      "v_mfma_f32_16x16x32_bf16 v[220:223], v[96:99], v[140:143], 0\n"
      "ds_read_b128 v[116:119], v39 offset:448\n"
      "v_cvt_pk_bf16_f32 v242, v108, v109\n"
      "v_mfma_f32_16x16x32_bf16 v[220:223], v[84:87], v[92:95], v[220:223]\n"
      "s_and_b32 s86, s86, -4\n"
      "v_cvt_pk_bf16_f32 v202, v202, v203\n"
      "v_accvgpr_write_b32 a245, v178\n"
      "v_accvgpr_write_b32 a241, v124\n"
      "v_rcp_f32 v158, v158\n"
      "v_cvt_pk_bf16_f32 v208, v120, v121\n"
      "v_pk_mul_f32 v[128:129], v[128:129], v[158:159] op_sel_hi:[1,0]\n"
      "v_cvt_pk_bf16_f32 v164, v164, v165\n"
      "v_pk_mul_f32 v[130:131], v[130:131], v[158:159] op_sel_hi:[1,0]\n"
      "v_cvt_pk_bf16_f32 v147, v132, v133\n"
      "v_pk_mul_f32 v[222:223], v[222:223], v[158:159] op_sel_hi:[1,0]\n"
      "v_pk_mul_f32 v[220:221], v[220:221], v[158:159] op_sel_hi:[1,0]\n"
      "v_mfma_f32_16x16x32_bf16 v[124:127], v[104:107], v[140:143], 0\n"
      "v_cvt_pk_bf16_f32 v136, v136, v137\n"
      "v_cvt_pk_bf16_f32 v148, v148, v148\n"
      "v_cvt_pk_bf16_f32 v176, v176, v177\n"
      "v_mfma_f32_16x16x32_bf16 v[124:127], v[80:83], v[92:95], v[124:127]\n"
      "v_cvt_pk_bf16_f32 v90, v180, 0\n"
      "v_accvgpr_write_b32 a249, v136\n"
      "v_cvt_pk_bf16_f32 v209, v122, v123\n"
      "v_mfma_f32_16x16x32_bf16 v[140:143], v[32:35], v[144:147], 0\n"
      "ds_read_b128 v[178:181], v39 offset:384\n"
      "v_cvt_pk_bf16_f32 v150, v150, v151\n"
      "v_mfma_f32_16x16x32_bf16 v[140:143], v[32:35], v[88:91], v[140:143]\n"
      "ds_write_b128 v3, v[206:209]\n"
      "v_pk_mul_f32 v[168:169], v[168:169], v[170:171] op_sel_hi:[1,0]\n"
      "v_mfma_f32_16x16x32_bf16 v[206:209], v[104:107], v[144:147], 0\n"
      "ds_read_b128 v[210:213], v39 offset:576\n"
      "v_cvt_pk_bf16_f32 v182, v182, v183\n"
      "v_cvt_pk_bf16_f32 v128, v128, v129\n"
      "v_accvgpr_write_b32 a247, v184\n"
      "v_mfma_f32_16x16x32_bf16 v[206:209], v[80:83], v[88:91], v[206:209]\n"
      "s_waitcnt lgkmcnt(3)\n"
      "v_rcp_f32_e64 v140, v140\n"
      "v_cvt_pk_bf16_f32 v243, v110, v111\n"
      "v_mfma_f32_16x16x32_bf16 v[108:111], v[194:197], v[144:147], 0\n"
      "ds_read_b128 v[170:173], v39 offset:640\n"
      "v_cvt_pk_bf16_f32 v162, v162, v163\n"
      "v_accvgpr_write_b32 a246, v182\n"
      "v_mfma_f32_16x16x32_bf16 v[108:111], v[76:79], v[88:91], v[108:111]\n"
      "ds_write_b16 v13, v148\n"
      "v_mfma_f32_16x16x32_bf16 v[198:201], v[154:157], a[228:231], v[116:119]\n"
      "ds_read_b128 v[136:139], v39 offset:512\n"
      "v_accvgpr_write_b32 a244, v176\n"
      "v_mfma_f32_16x16x32_bf16 v[174:177], v[96:99], v[144:147], 0\n"
      "v_accvgpr_write_b32 a225, v164\n"
      "v_cvt_pk_bf16_f32 v166, v166, v167\n"
      "v_mfma_f32_16x16x32_bf16 v[174:177], v[84:87], v[88:91], v[174:177]\n"
      "v_cvt_pk_bf16_f32 v112, v112, v113\n"
      "v_cvt_pk_bf16_f32 v167, v168, v169\n"
      "v_mfma_f32_16x16x32_bf16 v[120:123], v[154:157], a[252:255], v[116:119]\n"
      "s_waitcnt lgkmcnt(3)\n"
      "v_cvt_pk_bf16_f32 v134, v134, v135\n"
      "v_cvt_pk_bf16_f32 v238, v218, v219\n"
      "v_cvt_pk_bf16_f32 v200, v200, v201\n"
      "v_mfma_f32_16x16x32_bf16 v[184:187], v[188:191], a[228:231], v[178:181]\n"
      "ds_write_b64 v4, v[166:167]\n"
      "v_accvgpr_write_b32 a248, v134\n"
      "v_accvgpr_write_b32 a231, v200\n"
      "v_pk_mul_f32 v[124:125], v[124:125], v[158:159] op_sel_hi:[1,0]\n"
      "v_pk_mul_f32 v[126:127], v[126:127], v[158:159] op_sel_hi:[1,0]\n"
      "v_pk_mul_f32 v[108:109], v[108:109], v[140:141] op_sel_hi:[1,0]\n"
      "v_cvt_pk_bf16_f32 v108, v108, v109\n"
      "v_pk_mul_f32 v[176:177], v[176:177], v[140:141] op_sel_hi:[1,0]\n"
      "v_cvt_pk_bf16_f32 v198, v198, v199\n"
      "v_cvt_pk_bf16_f32 v129, v130, v131\n"
      "v_accvgpr_write_b32 a227, v114\n"
      "v_pk_mul_f32 v[174:175], v[174:175], v[140:141] op_sel_hi:[1,0]\n"
      "v_mfma_f32_16x16x32_bf16 v[216:219], v[188:191], v[244:247], v[178:181]\n"
      "ds_read_b128 v[164:167], v39 offset:704\n"
      "v_cvt_pk_bf16_f32 v186, v186, v187\n"
      "v_cvt_pk_bf16_f32 v122, v122, v123\n"
      "v_mfma_f32_16x16x32_bf16 v[158:161], v[154:157], v[244:247], v[116:119]\n"
      "s_waitcnt lgkmcnt(2)\n"
      "v_accvgpr_write_b32 a221, v102\n"
      "v_cvt_pk_bf16_f32 v130, v124, v125\n"
      "v_mfma_f32_16x16x32_bf16 v[132:135], v[188:191], a[236:239], v[170:173]\n"
      "s_add_u32 s42, s42, 1\n"
      "v_cvt_pk_bf16_f32 v131, v126, v127\n"
      "v_accvgpr_write_b32 a224, v162\n"
      "v_cvt_pk_bf16_f32 v220, v220, v221\n"
      "v_cvt_pk_bf16_f32 v221, v222, v223\n"
      "v_mfma_f32_16x16x32_bf16 v[124:127], v[188:191], a[252:255], v[178:181]\n"
      "s_cmp_ge_u32 s95, 0x40000000\n"
      "s_cselect_b64 vcc, -1, s[56:57]\n"
      "v_accvgpr_write_b32 a226, v112\n"
      "v_cvt_pk_bf16_f32 v134, v134, v135\n"
      "v_mfma_f32_16x16x32_bf16 v[180:183], v[154:157], a[232:235], v[210:213]\n"
      "ds_write_b128 v3, v[128:131] offset:256\n"
      "v_pk_mul_f32 v[110:111], v[110:111], v[140:141] op_sel_hi:[1,0]\n"
      "v_cvt_pk_bf16_f32 v120, v120, v121\n"
      "v_pk_mul_f32 v[208:209], v[208:209], v[140:141] op_sel_hi:[1,0]\n"
      "v_pk_mul_f32 v[206:207], v[206:207], v[140:141] op_sel_hi:[1,0]\n"
      "v_mfma_f32_16x16x32_bf16 v[140:143], v[188:191], v[224:227], v[136:139]\n"
      "ds_read_b128 v[144:147], v14\n"
      "v_cvt_pk_bf16_f32 v124, v124, v125\n"
      "v_cvt_pk_bf16_f32 v184, v184, v185\n"
      "v_cvt_pk_bf16_f32 v174, v174, v175\n"
      "v_mfma_f32_16x16x32_bf16 v[128:131], v[188:191], a[232:235], v[136:139]\n"
      "ds_write_b32 v31, v214 offset:4608\n"
      "v_cvt_pk_bf16_f32 v182, v182, v183\n"
      "v_cvt_pk_bf16_f32 v175, v176, v177\n"
      "v_mfma_f32_16x16x32_bf16 v[114:117], v[188:191], v[252:255], v[170:173]\n"
      "v_cvt_pk_bf16_f32 v180, v180, v181\n"
      "v_cvt_pk_bf16_f32 v247, v160, v161\n"
      "v_cvt_pk_bf16_f32 v109, v110, v111\n"
      "v_accvgpr_write_b32 a228, v184\n"
      "v_mfma_f32_16x16x32_bf16 v[160:163], v[154:157], v[224:227], v[210:213]\n"
      "ds_write_b64 v4, v[174:175] offset:512\n"
      "v_mfma_f32_16x16x32_bf16 v[100:103], v[188:191], v[248:251], v[136:139]\n"
      "s_waitcnt lgkmcnt(3)\n"
      "v_accvgpr_write_b32 a230, v198\n"
      "v_cvt_pk_bf16_f32 v244, v216, v217\n"
      "v_mfma_f32_16x16x32_bf16 v[136:139], v[154:157], a[236:239], v[164:167]\n"
      "ds_read_b128 v[198:201], v14 offset:64\n"
      "v_cvt_pk_bf16_f32 v128, v128, v129\n"
      "v_cvt_pk_bf16_f32 v132, v132, v133\n"
      "v_cvt_pk_bf16_f32 v226, v160, v161\n"
      "v_cvt_pk_bf16_f32 v126, v126, v127\n"
      "v_cvt_pk_bf16_f32 v111, v208, v209\n"
      "v_accvgpr_write_b32 a222, v150\n"
      "v_mfma_f32_16x16x32_bf16 v[148:151], v[154:157], v[228:231], v[164:167]\n"
      "ds_write_b64 v4, v[220:221] offset:256\n"
      "v_mfma_f32_16x16x32_bf16 v[220:223], v[188:191], v[228:231], v[170:173]\n"
      "v_cvt_pk_bf16_f32 v227, v162, v163\n"
      "v_cvt_pk_bf16_f32 v110, v206, v207\n"
      "v_cvt_pk_bf16_f32 v246, v158, v159\n"
      "v_mfma_f32_16x16x32_bf16 v[206:209], v[154:157], v[248:251], v[210:213]\n"
      "ds_write_b128 v3, v[108:111] offset:512\n"
      "v_cvt_pk_bf16_f32 v138, v138, v139\n"
      "v_mfma_f32_16x16x32_bf16 v[188:191], v[154:157], v[252:255], v[164:167]\n"
      "s_waitcnt lgkmcnt(2)\n"
      "v_accvgpr_write_b32 a250, v202\n"
      "v_accvgpr_write_b32 a254, v120\n"
      "v_cvt_pk_bf16_f32 v225, v142, v143\n"
      "v_cvt_pk_bf16_f32 v224, v140, v141\n"
      "v_cvt_pk_bf16_f32 v231, v150, v151\n"
      "v_accvgpr_write_b32 a252, v124\n"
      "v_accvgpr_write_b32 a251, v204\n"
      "v_cvt_pk_bf16_f32 v230, v148, v149\n"
      "v_accvgpr_write_b32 a239, v138\n"
      "v_cvt_pk_bf16_f32 v254, v188, v189\n"
      "v_accvgpr_write_b32 a236, v132\n"
      "v_cvt_pk_bf16_f32 v253, v116, v117\n"
      "v_cvt_pk_bf16_f32 v228, v220, v221\n"
      "v_cvt_pk_bf16_f32 v255, v190, v191\n"
      "v_cvt_pk_bf16_f32 v130, v130, v131\n"
      "v_cvt_pk_bf16_f32 v252, v114, v115\n"
      "v_accvgpr_write_b32 a234, v180\n"
      "v_accvgpr_write_b32 a253, v126\n"
      "v_accvgpr_write_b32 a237, v134\n"
      "v_accvgpr_write_b32 a235, v182\n"
      "v_cvt_pk_bf16_f32 v249, v102, v103\n"
      "v_accvgpr_write_b32 a223, v152\n"
      "v_cvt_pk_bf16_f32 v136, v136, v137\n"
      "v_accvgpr_write_b32 a238, v136\n"
      "v_accvgpr_write_b32 a233, v130\n"
      "v_cvt_pk_bf16_f32 v248, v100, v101\n"
      "v_cvt_pk_bf16_f32 v229, v222, v223\n"
      "v_cvt_pk_bf16_f32 v250, v206, v207\n"
      "v_accvgpr_write_b32 a255, v122\n"
      "v_mfma_f32_16x16x32_bf16 v[162:165], v[144:147], v[32:35], 0\n"
      "v_cvt_pk_bf16_f32 v245, v218, v219\n"
      "v_accvgpr_write_b32 a229, v186\n"
      "v_cvt_pk_bf16_f32 v251, v208, v209\n"
      "v_accvgpr_write_b32 a232, v128\n"
      "v_mfma_f32_16x16x32_bf16 v[162:165], v[198:201], v[32:35], v[162:165]\n"
      "s_add_u32 s10, s98, L_cold_%=-L_ipf0_%=\n"
      "s_addc_u32 s11, s99, 0\n"
      "s_mov_b32 s12, L_cend_%=-L_cold_%=\n"
      "s_sub_u32 s9, s9, 1280\n"
      "v_subrev_u32 v208, 115456, v9\n"
      "s_movk_i32 s13, 80\n"
      "v_cmp_gt_u32 s[14:15], s13, v208\n"
      "v_lshl_add_u32 v208, v208, 3, s9\n"
      "s_cmp_lt_u32 s40, 2\n"
      "s_cselect_b64 s[0:1], s[16:17], s[10:11]\n"
      "s_cselect_b32 s2, s18, s12\n"
      "s_cselect_b32 s8, -1, 0\n"
      "s_cselect_b64 s[14:15], 0, s[14:15]\n"
      "s_mov_b32 s3, s19\n"
      "s_add_u32 s6, s64, 16384\n"
      "s_cmp_lt_u32 s40, 2\n"
      "s_cselect_b32 s6, s6, 122112\n"
      "s_add_u32 s7, s64, 34816\n"
      "s_cmp_lt_u32 s40, 2\n"
      "s_cselect_b32 s7, s7, 122112\n"
      "s_and_b32 s5, s84, s8\n"
      "v_cndmask_b32 v15, v15, v208, s[14:15]\n"
      "s_waitcnt lgkmcnt(0)\n"
      "s_barrier\n"
      "s_or_b64 vcc, vcc, s[54:55]\n"
      "s_cbranch_vccnz L_disp1_%=\n"
      "s_nop 7\n"
      "s_nop 7\n"
      "L_loop1_%=:\n"
      "v_mfma_f32_16x16x32_bf16 v[152:155], v[144:147], v[194:197], 0\n"
      "ds_read_b128 v[186:189], v0 offset:18432\n"
      "v_mfma_f32_16x16x32_bf16 v[152:155], v[198:201], v[76:79], v[152:155]\n"
      "ds_read_b128 v[148:151], v9\n"
      "ds_read_b128 v[114:117], v0 offset:24576\n"
      "v_mfma_f32_16x16x32_bf16 v[210:213], v[144:147], v[104:107], 0\n"
      "ds_read_b128 v[100:103], v1 offset:30720\n"
      "s_add_u32 m0, s64, 0\n"
      "v_mfma_f32_16x16x32_bf16 v[210:213], v[198:201], v[80:83], v[210:213]\n"
      "buffer_load_dwordx4 v16, s[16:19], s84 offen  lds\n"
      "v_mfma_f32_16x16x32_bf16 v[180:183], v[144:147], v[96:99], 0\n"
      "ds_read_b128 v[144:147], v1 offset:18432\n"
      "s_add_u32 m0, s64, 4096\n"
      "v_mfma_f32_16x16x32_bf16 v[180:183], v[198:201], v[84:87], v[180:183]\n"
      "ds_read_b128 v[158:161], v1 offset:24576\n"
      "s_waitcnt lgkmcnt(5)\n"
      "v_mfma_f32_16x16x32_bf16 v[198:201], a[0:3], v[186:189], 0\n"
      "ds_read_b128 v[136:139], v1 offset:26624\n"
      "v_readlane_b32 s46, v37, s42\n"
      "v_mfma_f32_16x16x32_bf16 v[122:125], a[24:27], v[186:189], 0\n"
      "s_waitcnt lgkmcnt(5)\n"
      "v_add_f32 v150, v150, v151\n"
      "v_add_f32 v148, v148, v149\n"
      "v_mfma_f32_16x16x32_bf16 v[182:185], a[48:51], v[186:189], 0\n"
      "ds_read_b128 v[214:217], v0 offset:30720\n"
      "v_add_f32 v148, v148, v150\n"
      "v_mfma_f32_16x16x32_bf16 v[190:193], a[72:75], v[186:189], 0\n"
      "buffer_load_dwordx4 v17, s[16:19], s84 offen  lds\n"
      "v_mfma_f32_16x16x32_bf16 v[202:205], a[96:99], v[186:189], 0\n"
      "s_waitcnt lgkmcnt(3)\n"
      "v_rsq_f32 v148, v148\n"
      "v_mfma_f32_16x16x32_bf16 v[154:157], a[120:123], v[186:189], 0\n"
      "s_add_u32 m0, s64, 8192\n"
      "v_mul_f32 v148, 0x415db3d7, v148\n"
      "v_mov_b32 v150, v148\n"
      "v_mfma_f32_16x16x32_bf16 v[198:201], a[4:7], v[144:147], v[198:201]\n"
      "s_nop 0\n"
      "v_permlane32_swap_b32 v148, v150\n"
      "v_mfma_f32_16x16x32_bf16 v[198:201], a[8:11], v[114:117], v[198:201]\n"
      "ds_read_b128 v[128:131], v9 offset:4608\n"
      "v_cndmask_b32 v152, v152, v210, s[58:59]\n"
      "v_mfma_f32_16x16x32_bf16 v[122:125], a[28:31], v[144:147], v[122:125]\n"
      "v_mov_b32 v149, v148\n"
      "v_mul_f32 v169, v150, v169\n"
      "v_mfma_f32_16x16x32_bf16 v[122:125], a[32:35], v[114:117], v[122:125]\n"
      "ds_read_b128 v[210:213], v1 offset:20480\n"
      "v_readlane_b32 s47, v38, s43\n"
      "v_mfma_f32_16x16x32_bf16 v[182:185], a[52:55], v[144:147], v[182:185]\n"
      "buffer_load_dwordx4 v18, s[16:19], s84 offen  lds\n"
      "v_mfma_f32_16x16x32_bf16 v[182:185], a[56:59], v[114:117], v[182:185]\n"
      "v_permlane16_swap_b32 v148, v149\n"
      "v_mfma_f32_16x16x32_bf16 v[190:193], a[76:79], v[144:147], v[190:193]\n"
      "v_mul_f32 v209, v149, v209\n"
      "v_mul_f32_e64 v178, v149, v178\n"
      "v_mfma_f32_16x16x32_bf16 v[190:193], a[80:83], v[114:117], v[190:193]\n"
      "s_add_u32 m0, s64, 12288\n"
      "v_mul_f32 v143, v148, v143\n"
      "v_mul_f32 v173, v148, v173\n"
      "v_mfma_f32_16x16x32_bf16 v[202:205], a[100:103], v[144:147], v[202:205]\n"
      "v_mul_f32 v206, v149, v206\n"
      "v_mul_f32 v176, v149, v176\n"
      "v_mfma_f32_16x16x32_bf16 v[202:205], a[104:107], v[114:117], v[202:205]\n"
      "buffer_load_dwordx4 v19, s[16:19], s84 offen  lds\n"
      "v_mfma_f32_16x16x32_bf16 v[154:157], a[124:127], v[144:147], v[154:157]\n"
      "v_mul_f32 v207, v149, v207\n"
      "v_mul_f32 v175, v148, v175\n"
      "v_mfma_f32_16x16x32_bf16 v[154:157], a[128:131], v[114:117], v[154:157]\n"
      "s_waitcnt lgkmcnt(0)\n"
      "v_mul_f32 v221, v148, v221\n"
      "v_mul_f32_e64 v220, v148, v220\n"
      "v_mfma_f32_16x16x32_bf16 v[198:201], a[12:15], v[158:161], v[198:201]\n"
      "v_mul_f32 v223, v148, v223\n"
      "v_mul_f32 v177, v149, v177\n"
      "v_mfma_f32_16x16x32_bf16 v[198:201], a[16:19], v[214:217], v[198:201]\n"
      "v_cvt_pk_bf16_f32 v206, v206, v207\n"
      "v_mul_f32 v135, v150, v135\n"
      "v_mfma_f32_16x16x32_bf16 v[198:201], a[20:23], v[100:103], v[198:201]\n"
      "s_mov_b32 m0, s6\n"
      "v_mul_f32_e64 v172, v148, v172\n"
      "v_cndmask_b32 v152, v152, v180, s[60:61]\n"
      "v_mfma_f32_16x16x32_bf16 v[122:125], a[36:39], v[158:161], v[122:125]\n"
      "v_mov_b32_dpp v162, v162 quad_perm:[0,0,0,0] row_mask:0xf bank_mask:0xf\n"
      "v_cvt_pk_bf16_f32 v172, v172, v173\n"
      "v_mfma_f32_16x16x32_bf16 v[122:125], a[40:43], v[214:217], v[122:125]\n"
      "v_mul_f32 v174, v148, v174\n"
      "v_fma_f32 v76, v198, v198, s51\n"
      "v_mfma_f32_16x16x32_bf16 v[122:125], a[44:47], v[100:103], v[122:125]\n"
      "v_cvt_pk_bf16_f32 v173, v174, v175\n"
      "v_mul_f32 v108, v149, v108\n"
      "v_mfma_f32_16x16x32_bf16 v[182:185], a[60:63], v[158:161], v[182:185]\n"
      "buffer_load_dwordx4 v15, s[0:3], s5 offen  lds\n"
      "v_mfma_f32_16x16x32_bf16 v[182:185], a[64:67], v[214:217], v[182:185]\n"
      "v_rcp_f32_e64 v162, v162\n"
      "v_mfma_f32_16x16x32_bf16 v[182:185], a[68:71], v[100:103], v[182:185]\n"
      "v_mul_f32_e64 v222, v148, v222\n"
      "v_cvt_pk_bf16_f32 v174, v220, v221\n"
      "v_mfma_f32_16x16x32_bf16 v[190:193], a[84:87], v[158:161], v[190:193]\n"
      "v_cvt_pk_bf16_f32 v175, v222, v223\n"
      "v_fmac_f32_e64 v76, v199, v199\n"
      "v_mfma_f32_16x16x32_bf16 v[190:193], a[88:91], v[214:217], v[190:193]\n"
      "ds_read_b128 v[218:221], v0 offset:26624\n"
      "v_or3_b32 v128, v128, v129, v130\n"
      "v_mfma_f32_16x16x32_bf16 v[190:193], a[92:95], v[100:103], v[190:193]\n"
      "buffer_load_dwordx4 v[164:167], v36, s[28:31], s86 offen\n"
      "v_mfma_f32_16x16x32_bf16 v[202:205], a[108:111], v[158:161], v[202:205]\n"
      "v_mul_f32 v109, v149, v109\n"
      "v_fmac_f32 v76, v200, v200\n"
      "v_mfma_f32_16x16x32_bf16 v[202:205], a[112:115], v[214:217], v[202:205]\n"
      "ds_write_b128 v5, v[172:175] offset:18432\n"
      "v_mfma_f32_16x16x32_bf16 v[202:205], a[116:119], v[100:103], v[202:205]\n"
      "v_mul_f32 v208, v149, v208\n"
      "v_fmac_f32 v76, v201, v201\n"
      "v_mfma_f32_16x16x32_bf16 v[154:157], a[132:135], v[158:161], v[154:157]\n"
      "v_fma_f32 v130, v190, v190, s51\n"
      "v_mul_f32_e64 v179, v149, v179\n"
      "v_mfma_f32_16x16x32_bf16 v[154:157], a[136:139], v[214:217], v[154:157]\n"
      "v_fmac_f32 v130, v191, v191\n"
      "v_fmac_f32 v76, v122, v122\n"
      "v_mfma_f32_16x16x32_bf16 v[154:157], a[140:143], v[100:103], v[154:157]\n"
      "v_mul_f32 v110, v149, v110\n"
      "v_fmac_f32 v130, v192, v192\n"
      "v_mfma_f32_16x16x32_bf16 v[194:197], v[186:189], a[144:147], 0\n"
      "v_mul_f32_e64 v118, v150, v118\n"
      "v_mfma_f32_16x16x32_bf16 v[194:197], v[144:147], a[148:151], v[194:197]\n"
      "buffer_load_dword v88, v29, s[28:31], s86 offen\n"
      "v_mfma_f32_16x16x32_bf16 v[194:197], v[114:117], a[152:155], v[194:197]\n"
      "ds_read_b128 v[172:175], v0 offset:20480\n"
      "v_mul_f32_e64 v140, v148, v140\n"
      "v_mfma_f32_16x16x32_bf16 v[194:197], v[158:161], a[156:159], v[194:197]\n"
      "v_cvt_pk_bf16_f32 v207, v208, v209\n"
      "v_mul_f32 v111, v149, v111\n"
      "v_mfma_f32_16x16x32_bf16 v[194:197], v[214:217], a[160:163], v[194:197]\n"
      "v_mul_f32 v142, v148, v142\n"
      "v_cvt_pk_bf16_f32 v208, v176, v177\n"
      "v_mfma_f32_16x16x32_bf16 v[194:197], v[100:103], a[164:167], v[194:197]\n"
      "v_mul_f32 v133, v150, v133\n"
      "v_mul_f32 v134, v150, v134\n"
      "v_mfma_f32_16x16x32_bf16 v[104:107], v[186:189], a[168:171], 0\n"
      "v_cvt_pk_bf16_f32 v209, v178, v179\n"
      "v_fmac_f32 v130, v193, v193\n"
      "v_mfma_f32_16x16x32_bf16 v[104:107], v[144:147], a[172:175], v[104:107]\n"
      "v_mul_f32 v121, v150, v121\n"
      "v_fmac_f32_e64 v130, v202, v202\n"
      "v_mfma_f32_16x16x32_bf16 v[104:107], v[114:117], a[176:179], v[104:107]\n"
      "ds_write_b128 v5, v[206:209] offset:24576\n"
      "v_mfma_f32_16x16x32_bf16 v[104:107], v[158:161], a[180:183], v[104:107]\n"
      "v_fmac_f32_e64 v76, v123, v123\n"
      "v_cvt_pk_bf16_f32 v194, v194, v195\n"
      "v_mfma_f32_16x16x32_bf16 v[104:107], v[214:217], a[184:187], v[104:107]\n"
      "v_fmac_f32 v76, v124, v124\n"
      "v_fmac_f32 v130, v203, v203\n"
      "v_mfma_f32_16x16x32_bf16 v[104:107], v[100:103], a[188:191], v[104:107]\n"
      "ds_read_b128 v[206:209], v1 offset:32768\n"
      "v_mul_f32 v141, v148, v141\n"
      "v_mfma_f32_16x16x32_bf16 v[96:99], v[186:189], a[192:195], 0\n"
      "v_cvt_pk_bf16_f32 v108, v108, v109\n"
      "v_fmac_f32 v130, v204, v204\n"
      "v_mfma_f32_16x16x32_bf16 v[96:99], v[144:147], a[196:199], v[96:99]\n"
      "v_readlane_b32 s45, v37, s43\n"
      "v_mul_f32 v170, v150, v170\n"
      "v_mfma_f32_16x16x32_bf16 v[96:99], v[114:117], a[200:203], v[96:99]\n"
      "s_waitcnt lgkmcnt(2)\n"
      "v_bitop3_b32 v128, v128, v131, s33 bitop3:0xa8\n"
      "v_fmac_f32 v130, v205, v205\n"
      "v_mfma_f32_16x16x32_bf16 v[96:99], v[158:161], a[204:207], v[96:99]\n"
      "ds_read_b128 v[158:161], v0 offset:32768\n"
      "v_mul_f32 v132, v150, v132\n"
      "v_mfma_f32_16x16x32_bf16 v[96:99], v[214:217], a[208:211], v[96:99]\n"
      "s_or_b32 s95, s45, s46\n"
      "v_fmac_f32 v130, v154, v154\n"
      "v_fmac_f32 v130, v155, v155\n"
      "v_mfma_f32_16x16x32_bf16 v[96:99], v[100:103], a[212:215], v[96:99]\n"
      "ds_read_b128 v[100:103], v0 offset:28672\n"
      "v_mul_f32 v120, v150, v120\n"
      "v_mfma_f32_16x16x32_bf16 v[112:115], a[0:3], v[172:175], 0\n"
      "v_fmac_f32 v130, v156, v156\n"
      "v_fmac_f32 v130, v157, v157\n"
      "v_mfma_f32_16x16x32_bf16 v[112:115], a[4:7], v[210:213], v[112:115]\n"
      "v_cvt_pk_bf16_f32 v140, v140, v141\n"
      "v_fmac_f32 v76, v125, v125\n"
      "v_mfma_f32_16x16x32_bf16 v[112:115], a[8:11], v[218:221], v[112:115]\n"
      "v_fmac_f32 v76, v182, v182\n"
      "v_cmp_ne_u32 s[56:57], 0, v128\n"
      "v_mfma_f32_16x16x32_bf16 v[112:115], a[12:15], v[136:139], v[112:115]\n"
      "s_waitcnt lgkmcnt(1)\n"
      "v_mul_f32 v168, v150, v168\n"
      "v_fmac_f32 v76, v183, v183\n"
      "v_mfma_f32_16x16x32_bf16 v[112:115], a[16:19], v[158:161], v[112:115]\n"
      "v_mul_f32 v119, v150, v119\n"
      "v_fmac_f32_e64 v76, v184, v184\n"
      "v_mfma_f32_16x16x32_bf16 v[112:115], a[20:23], v[206:209], v[112:115]\n"
      "v_cvt_pk_bf16_f32 v132, v132, v133\n"
      "v_fmac_f32 v76, v185, v185\n"
      "v_mfma_f32_16x16x32_bf16 v[186:189], a[24:27], v[172:175], 0\n"
      "s_or_b32 s95, s95, s48\n"
      "v_mul_f32 v152, v162, v152\n"
      "v_cvt_pk_bf16_f32 v133, v134, v135\n"
      "v_mfma_f32_16x16x32_bf16 v[186:189], a[28:31], v[210:213], v[186:189]\n"
      "v_permlane16_swap_b32 v76, v130\n"
      "v_mfma_f32_16x16x32_bf16 v[186:189], a[32:35], v[218:221], v[186:189]\n"
      "v_fma_f32 v77, v112, v112, s51\n"
      "v_cvt_pk_bf16_f32 v141, v142, v143\n"
      "v_mfma_f32_16x16x32_bf16 v[186:189], a[36:39], v[136:139], v[186:189]\n"
      "ds_write_b64 v6, v[140:141] offset:18432\n"
      "v_mfma_f32_16x16x32_bf16 v[186:189], a[40:43], v[158:161], v[186:189]\n"
      "v_add_f32 v76, v76, v130\n"
      "v_fmac_f32 v77, v113, v113\n"
      "v_mfma_f32_16x16x32_bf16 v[186:189], a[44:47], v[206:209], v[186:189]\n"
      "v_fmac_f32 v77, v114, v114\n"
      "v_fmac_f32 v77, v115, v115\n"
      "v_mfma_f32_16x16x32_bf16 v[214:217], a[48:51], v[172:175], 0\n"
      "v_cvt_pk_bf16_f32 v109, v110, v111\n"
      "v_mov_b32 v130, v76\n"
      "v_mfma_f32_16x16x32_bf16 v[214:217], a[52:55], v[210:213], v[214:217]\n"
      "s_nop 0\n"
      "v_permlane32_swap_b32 v76, v130\n"
      "v_mfma_f32_16x16x32_bf16 v[214:217], a[56:59], v[218:221], v[214:217]\n"
      "v_cvt_pk_bf16_f32 v104, v104, v105\n"
      "v_fmac_f32 v77, v186, v186\n"
      "v_mfma_f32_16x16x32_bf16 v[214:217], a[60:63], v[136:139], v[214:217]\n"
      "ds_write_b64 v6, v[108:109] offset:24576\n"
      "v_mfma_f32_16x16x32_bf16 v[214:217], a[64:67], v[158:161], v[214:217]\n"
      "v_add_f32_e64 v76, v76, v130\n"
      "v_cvt_pk_bf16_f32 v134, v118, v119\n"
      "v_mfma_f32_16x16x32_bf16 v[214:217], a[68:71], v[206:209], v[214:217]\n"
      "v_cvt_pk_bf16_f32 v96, v96, v97\n"
      "v_fmac_f32 v77, v187, v187\n"
      "v_mfma_f32_16x16x32_bf16 v[144:147], a[72:75], v[172:175], 0\n"
      "v_rsq_f32 v76, v76\n"
      "v_mfma_f32_16x16x32_bf16 v[144:147], a[76:79], v[210:213], v[144:147]\n"
      "v_mul_f32 v171, v150, v171\n"
      "v_fmac_f32 v77, v188, v188\n"
      "v_mfma_f32_16x16x32_bf16 v[144:147], a[80:83], v[218:221], v[144:147]\n"
      "v_cvt_pk_bf16_f32 v135, v120, v121\n"
      "v_mov_b32 v130, v76\n"
      "v_mfma_f32_16x16x32_bf16 v[144:147], a[84:87], v[136:139], v[144:147]\n"
      "ds_write_b128 v5, v[132:135] offset:30720\n"
      "v_mfma_f32_16x16x32_bf16 v[144:147], a[88:91], v[158:161], v[144:147]\n"
      "v_permlane16_swap_b32 v76, v130\n"
      "v_mfma_f32_16x16x32_bf16 v[144:147], a[92:95], v[206:209], v[144:147]\n"
      "v_cvt_pk_bf16_f32 v105, v106, v107\n"
      "v_mul_f32 v76, s50, v76\n"
      "v_mfma_f32_16x16x32_bf16 v[116:119], a[96:99], v[172:175], 0\n"
      "v_mul_f32 v201, v76, v201\n"
      "v_mul_f32 v183, v76, v183\n"
      "v_mfma_f32_16x16x32_bf16 v[116:119], a[100:103], v[210:213], v[116:119]\n"
      "v_fmac_f32 v77, v189, v189\n"
      "v_mul_f32_e64 v199, v76, v199\n"
      "v_mfma_f32_16x16x32_bf16 v[116:119], a[104:107], v[218:221], v[116:119]\n"
      "v_cvt_pk_bf16_f32 v168, v168, v169\n"
      "v_fma_f32 v81, v144, v144, s51\n"
      "v_mfma_f32_16x16x32_bf16 v[116:119], a[108:111], v[136:139], v[116:119]\n"
      "v_cvt_pk_bf16_f32 v152, v152, v152\n"
      "v_fmac_f32 v81, v145, v145\n"
      "v_mfma_f32_16x16x32_bf16 v[116:119], a[112:115], v[158:161], v[116:119]\n"
      "v_mul_f32 v157, v130, v157\n"
      "v_cvt_pk_bf16_f32 v195, v196, v197\n"
      "v_mfma_f32_16x16x32_bf16 v[116:119], a[116:119], v[206:209], v[116:119]\n"
      "v_mul_f32 v190, v130, v190\n"
      "v_fmac_f32 v77, v214, v214\n"
      "v_mfma_f32_16x16x32_bf16 v[148:151], a[120:123], v[172:175], 0\n"
      "v_fmac_f32 v81, v146, v146\n"
      "v_mul_f32 v198, v76, v198\n"
      "v_mfma_f32_16x16x32_bf16 v[148:151], a[124:127], v[210:213], v[148:151]\n"
      "v_mul_f32 v192, v130, v192\n"
      "v_cvt_pk_bf16_f32 v198, v198, v199\n"
      "v_mfma_f32_16x16x32_bf16 v[148:151], a[128:131], v[218:221], v[148:151]\n"
      "v_cvt_pk_bf16_f32 v169, v170, v171\n"
      "v_mul_f32 v122, v76, v122\n"
      "v_mfma_f32_16x16x32_bf16 v[148:151], a[132:135], v[136:139], v[148:151]\n"
      "v_mul_f32 v204, v130, v204\n"
      "v_fmac_f32 v81, v147, v147\n"
      "v_mfma_f32_16x16x32_bf16 v[148:151], a[136:139], v[158:161], v[148:151]\n"
      "v_fmac_f32 v77, v215, v215\n"
      "v_fmac_f32 v81, v116, v116\n"
      "v_mfma_f32_16x16x32_bf16 v[148:151], a[140:143], v[206:209], v[148:151]\n"
      "v_fmac_f32 v81, v117, v117\n"
      "v_fmac_f32 v81, v118, v118\n"
      "v_mfma_f32_16x16x32_bf16 v[126:129], v[172:175], a[144:147], 0\n"
      "v_mul_f32 v182, v76, v182\n"
      "v_mul_f32 v184, v76, v184\n"
      "v_mfma_f32_16x16x32_bf16 v[126:129], v[210:213], a[148:151], v[126:129]\n"
      "ds_read_b128 v[108:111], v0 offset:22528\n"
      "v_fmac_f32 v81, v119, v119\n"
      "v_mfma_f32_16x16x32_bf16 v[126:129], v[218:221], a[152:155], v[126:129]\n"
      "v_mul_f32 v203, v130, v203\n"
      "v_fmac_f32 v81, v148, v148\n"
      "v_mfma_f32_16x16x32_bf16 v[126:129], v[136:139], a[156:159], v[126:129]\n"
      "v_mul_f32 v185, v76, v185\n"
      "v_fmac_f32_e64 v77, v216, v216\n"
      "v_mfma_f32_16x16x32_bf16 v[126:129], v[158:161], a[160:163], v[126:129]\n"
      "ds_write_b64 v6, v[168:169] offset:30720\n"
      "v_mfma_f32_16x16x32_bf16 v[126:129], v[206:209], a[164:167], v[126:129]\n"
      "v_mul_f32 v205, v130, v205\n"
      "v_fmac_f32 v81, v149, v149\n"
      "v_mfma_f32_16x16x32_bf16 v[132:135], v[172:175], a[168:171], 0\n"
      "ds_read_b128 v[168:171], v1 offset:22528\n"
      "v_cvt_pk_bf16_f32 v182, v182, v183\n"
      "v_mfma_f32_16x16x32_bf16 v[132:135], v[210:213], a[172:175], v[132:135]\n"
      "v_fmac_f32 v81, v150, v150\n"
      "v_fmac_f32 v81, v151, v151\n"
      "v_mfma_f32_16x16x32_bf16 v[132:135], v[218:221], a[176:179], v[132:135]\n"
      "ds_read_b128 v[140:143], v1 offset:34816\n"
      "v_cvt_pk_bf16_f32 v183, v184, v185\n"
      "v_mfma_f32_16x16x32_bf16 v[132:135], v[136:139], a[180:183], v[132:135]\n"
      "v_fmac_f32 v77, v217, v217\n"
      "v_mul_f32 v155, v130, v155\n"
      "v_mfma_f32_16x16x32_bf16 v[132:135], v[158:161], a[184:187], v[132:135]\n"
      "v_permlane16_swap_b32 v77, v81\n"
      "v_mfma_f32_16x16x32_bf16 v[132:135], v[206:209], a[188:191], v[132:135]\n"
      "v_mul_f32 v191, v130, v191\n"
      "v_mul_f32 v125, v76, v125\n"
      "v_mfma_f32_16x16x32_bf16 v[178:181], v[172:175], a[192:195], 0\n"
      "v_add_f32 v77, v77, v81\n"
      "v_mov_b32_e64 v81, v77\n"
      "v_mfma_f32_16x16x32_bf16 v[178:181], v[210:213], a[196:199], v[178:181]\n"
      "ds_read_b128 v[172:175], v1 offset:28672\n"
      "v_mul_f32 v193, v130, v193\n"
      "v_mfma_f32_16x16x32_bf16 v[178:181], v[218:221], a[200:203], v[178:181]\n"
      "v_permlane32_swap_b32 v77, v81\n"
      "v_mfma_f32_16x16x32_bf16 v[178:181], v[136:139], a[204:207], v[178:181]\n"
      "ds_read_b128 v[220:223], v0 offset:34816\n"
      "v_mul_f32_e64 v200, v76, v200\n"
      "v_mfma_f32_16x16x32_bf16 v[178:181], v[158:161], a[208:211], v[178:181]\n"
      "v_cvt_pk_bf16_f32 v190, v190, v191\n"
      "v_cvt_pk_bf16_f32 v196, v126, v127\n"
      "v_mfma_f32_16x16x32_bf16 v[178:181], v[206:209], a[212:215], v[178:181]\n"
      "s_waitcnt lgkmcnt(2)\n"
      "v_mul_f32 v154, v130, v154\n"
      "v_add_f32_e64 v77, v77, v81\n"
      "v_mfma_f32_16x16x32_bf16 v[158:161], a[0:3], v[108:111], 0\n"
      "s_cmp_ge_u32 s95, 0x40000000\n"
      "s_cselect_b32 s94, -1, -2\n"
      "v_cvt_pk_bf16_f32 v97, v98, v99\n"
      "v_mfma_f32_16x16x32_bf16 v[158:161], a[4:7], v[168:171], v[158:161]\n"
      "v_cvt_pk_bf16_f32 v154, v154, v155\n"
      "v_cvt_pk_bf16_f32 v106, v132, v133\n"
      "v_mfma_f32_16x16x32_bf16 v[158:161], a[8:11], v[100:103], v[158:161]\n"
      "s_waitcnt lgkmcnt(1)\n"
      "v_rsq_f32 v77, v77\n"
      "v_mfma_f32_16x16x32_bf16 v[158:161], a[12:15], v[172:175], v[158:161]\n"
      "s_waitcnt lgkmcnt(0)\n"
      "v_mul_f32 v202, v130, v202\n"
      "v_mov_b32 v81, v77\n"
      "v_mfma_f32_16x16x32_bf16 v[158:161], a[16:19], v[220:223], v[158:161]\n"
      "s_nop 0\n"
      "v_permlane16_swap_b32 v77, v81\n"
      "v_mfma_f32_16x16x32_bf16 v[158:161], a[20:23], v[140:143], v[158:161]\n"
      "v_mul_f32 v77, s50, v77\n"
      "v_mul_f32_e64 v113, v77, v113\n"
      "v_mfma_f32_16x16x32_bf16 v[206:209], a[24:27], v[108:111], 0\n"
      "ds_write_b16 v12, v152\n"
      "v_mfma_f32_16x16x32_bf16 v[206:209], a[28:31], v[168:171], v[206:209]\n"
      "v_mul_f32_e64 v112, v77, v112\n"
      "v_cvt_pk_bf16_f32 v112, v112, v113\n"
      "v_mfma_f32_16x16x32_bf16 v[206:209], a[32:35], v[100:103], v[206:209]\n"
      "v_mul_f32 v217, v77, v217\n"
      "v_mul_f32 v186, v77, v186\n"
      "v_mfma_f32_16x16x32_bf16 v[206:209], a[36:39], v[172:175], v[206:209]\n"
      "v_fma_f32 v176, v158, v158, s51\n"
      "v_mul_f32 v215, v77, v215\n"
      "v_mfma_f32_16x16x32_bf16 v[206:209], a[40:43], v[220:223], v[206:209]\n"
      "v_mul_f32 v189, v77, v189\n"
      "v_fmac_f32 v176, v159, v159\n"
      "v_mfma_f32_16x16x32_bf16 v[206:209], a[44:47], v[140:143], v[206:209]\n"
      "v_mul_f32 v123, v76, v123\n"
      "v_mul_f32 v114, v77, v114\n"
      "v_mfma_f32_16x16x32_bf16 v[136:139], a[48:51], v[108:111], 0\n"
      "v_mul_f32 v214, v77, v214\n"
      "v_mul_f32 v115, v77, v115\n"
      "v_mfma_f32_16x16x32_bf16 v[136:139], a[52:55], v[168:171], v[136:139]\n"
      "v_mul_f32 v144, v81, v144\n"
      "v_fmac_f32 v176, v160, v160\n"
      "v_mfma_f32_16x16x32_bf16 v[136:139], a[56:59], v[100:103], v[136:139]\n"
      "v_cvt_pk_bf16_f32 v191, v192, v193\n"
      "v_mul_f32 v118, v81, v118\n"
      "v_mfma_f32_16x16x32_bf16 v[136:139], a[60:63], v[172:175], v[136:139]\n"
      "v_mul_f32 v187, v77, v187\n"
      "v_mul_f32 v145, v81, v145\n"
      "v_mfma_f32_16x16x32_bf16 v[136:139], a[64:67], v[220:223], v[136:139]\n"
      "v_mul_f32 v188, v77, v188\n"
      "v_mul_f32 v119, v81, v119\n"
      "v_mfma_f32_16x16x32_bf16 v[136:139], a[68:71], v[140:143], v[136:139]\n"
      "v_mul_f32 v156, v130, v156\n"
      "v_cvt_pk_bf16_f32 v113, v114, v115\n"
      "v_mfma_f32_16x16x32_bf16 v[210:213], a[72:75], v[108:111], 0\n"
      "v_cvt_pk_bf16_f32 v199, v200, v201\n"
      "v_fmac_f32 v176, v161, v161\n"
      "v_mfma_f32_16x16x32_bf16 v[210:213], a[76:79], v[168:171], v[210:213]\n"
      "v_fmac_f32 v176, v206, v206\n"
      "v_cvt_pk_bf16_f32 v99, v180, v181\n"
      "v_mfma_f32_16x16x32_bf16 v[210:213], a[80:83], v[100:103], v[210:213]\n"
      "v_mul_f32 v149, v81, v149\n"
      "v_mul_f32_e64 v151, v81, v151\n"
      "v_mfma_f32_16x16x32_bf16 v[210:213], a[84:87], v[172:175], v[210:213]\n"
      "v_cvt_pk_bf16_f32 v197, v128, v129\n"
      "v_cvt_pk_bf16_f32 v193, v204, v205\n"
      "v_mfma_f32_16x16x32_bf16 v[210:213], a[88:91], v[220:223], v[210:213]\n"
      "v_cvt_pk_bf16_f32 v214, v214, v215\n"
      "v_mul_f32_e64 v117, v81, v117\n"
      "v_mfma_f32_16x16x32_bf16 v[210:213], a[92:95], v[140:143], v[210:213]\n"
      "v_cvt_pk_bf16_f32 v200, v122, v123\n"
      "v_mul_f32 v148, v81, v148\n"
      "v_mfma_f32_16x16x32_bf16 v[120:123], a[96:99], v[108:111], 0\n"
      "ds_read_b128 v[130:133], v2\n"
      "v_mul_f32 v124, v76, v124\n"
      "v_mfma_f32_16x16x32_bf16 v[120:123], a[100:103], v[168:171], v[120:123]\n"
      "v_mul_f32_e64 v116, v81, v116\n"
      "v_cvt_pk_bf16_f32 v115, v188, v189\n"
      "v_mfma_f32_16x16x32_bf16 v[120:123], a[104:107], v[100:103], v[120:123]\n"
      "v_cvt_pk_bf16_f32 v155, v156, v157\n"
      "v_fmac_f32 v176, v207, v207\n"
      "v_mfma_f32_16x16x32_bf16 v[120:123], a[108:111], v[172:175], v[120:123]\n"
      "s_and_b32 s47, s47, -4\n"
      "v_mul_f32_e64 v146, v81, v146\n"
      "v_cvt_pk_bf16_f32 v201, v124, v125\n"
      "v_mfma_f32_16x16x32_bf16 v[120:123], a[112:115], v[220:223], v[120:123]\n"
      "v_mul_f32_e64 v147, v81, v147\n"
      "v_cvt_pk_bf16_f32 v144, v144, v145\n"
      "v_mfma_f32_16x16x32_bf16 v[120:123], a[116:119], v[140:143], v[120:123]\n"
      "s_and_b32 s4, s45, s8\n"
      "v_fma_f32 v185, v210, v210, s51\n"
      "v_fmac_f32 v185, v211, v211\n"
      "v_mfma_f32_16x16x32_bf16 v[126:129], a[120:123], v[108:111], 0\n"
      "v_cvt_pk_bf16_f32 v148, v148, v149\n"
      "v_fmac_f32 v176, v208, v208\n"
      "v_mfma_f32_16x16x32_bf16 v[126:129], a[124:127], v[168:171], v[126:129]\n"
      "v_fmac_f32 v185, v212, v212\n"
      "v_cvt_pk_bf16_f32 v107, v134, v135\n"
      "v_mfma_f32_16x16x32_bf16 v[126:129], a[128:131], v[100:103], v[126:129]\n"
      "v_mul_f32 v216, v77, v216\n"
      "v_fmac_f32 v185, v213, v213\n"
      "v_mfma_f32_16x16x32_bf16 v[126:129], a[132:135], v[172:175], v[126:129]\n"
      "v_fmac_f32 v185, v120, v120\n"
      "v_fmac_f32 v176, v209, v209\n"
      "v_mfma_f32_16x16x32_bf16 v[126:129], a[136:139], v[220:223], v[126:129]\n"
      "v_fmac_f32 v176, v136, v136\n"
      "v_cvt_pk_bf16_f32 v192, v202, v203\n"
      "v_mfma_f32_16x16x32_bf16 v[126:129], a[140:143], v[140:143], v[126:129]\n"
      "v_fmac_f32 v176, v137, v137\n"
      "v_fmac_f32 v185, v121, v121\n"
      "v_mfma_f32_16x16x32_bf16 v[202:205], v[108:111], a[144:147], 0\n"
      "s_cmp_gt_u32 s42, s44\n"
      "s_cselect_b32 s95, s94, s95\n"
      "v_fmac_f32 v185, v122, v122\n"
      "v_mfma_f32_16x16x32_bf16 v[202:205], v[168:171], a[148:151], v[202:205]\n"
      "v_fmac_f32 v185, v123, v123\n"
      "v_fmac_f32 v185, v126, v126\n"
      "v_mfma_f32_16x16x32_bf16 v[202:205], v[100:103], a[152:155], v[202:205]\n"
      "v_fmac_f32 v176, v138, v138\n"
      "v_fmac_f32 v176, v139, v139\n"
      "v_mfma_f32_16x16x32_bf16 v[202:205], v[172:175], a[156:159], v[202:205]\n"
      "v_cvt_pk_bf16_f32 v215, v216, v217\n"
      "v_cvt_pk_bf16_f32 v98, v178, v179\n"
      "v_mfma_f32_16x16x32_bf16 v[202:205], v[220:223], a[160:163], v[202:205]\n"
      "v_fmac_f32 v185, v127, v127\n"
      "v_fmac_f32 v185, v128, v128\n"
      "v_mfma_f32_16x16x32_bf16 v[202:205], v[140:143], a[164:167], v[202:205]\n"
      "v_fmac_f32 v185, v129, v129\n"
      "v_cvt_pk_bf16_f32 v145, v146, v147\n"
      "v_mfma_f32_16x16x32_bf16 v[216:219], v[108:111], a[168:171], 0\n"
      "v_permlane16_swap_b32 v176, v185\n"
      "v_mfma_f32_16x16x32_bf16 v[216:219], v[168:171], a[172:175], v[216:219]\n"
      "v_add_f32 v176, v176, v185\n"
      "v_cvt_pk_bf16_f32 v114, v186, v187\n"
      "v_mfma_f32_16x16x32_bf16 v[216:219], v[100:103], a[176:179], v[216:219]\n"
      "v_mov_b32 v185, v176\n"
      "v_cvt_pk_bf16_f32 v147, v118, v119\n"
      "v_mfma_f32_16x16x32_bf16 v[216:219], v[172:175], a[180:183], v[216:219]\n"
      "ds_read_b128 v[186:189], v2 offset:3072\n"
      "v_cvt_pk_bf16_f32 v76, v202, v203\n"
      "v_mfma_f32_16x16x32_bf16 v[216:219], v[220:223], a[184:187], v[216:219]\n"
      "v_permlane32_swap_b32 v176, v185\n"
      "v_mfma_f32_16x16x32_bf16 v[216:219], v[140:143], a[188:191], v[216:219]\n"
      "v_mul_f32 v150, v81, v150\n"
      "v_add_f32_e64 v176, v176, v185\n"
      "v_mfma_f32_16x16x32_bf16 v[178:181], v[108:111], a[192:195], 0\n"
      "ds_read_b128 v[108:111], v2 offset:6144\n"
      "v_cvt_pk_bf16_f32 v146, v116, v117\n"
      "v_mfma_f32_16x16x32_bf16 v[178:181], v[168:171], a[196:199], v[178:181]\n"
      "v_rsq_f32 v176, v176\n"
      "v_mfma_f32_16x16x32_bf16 v[178:181], v[100:103], a[200:203], v[178:181]\n"
      "v_cvt_pk_bf16_f32 v77, v204, v205\n"
      "v_mov_b32 v185, v176\n"
      "v_mfma_f32_16x16x32_bf16 v[178:181], v[172:175], a[204:207], v[178:181]\n"
      "s_waitcnt lgkmcnt(1)\n"
      "v_cvt_pk_bf16_f32 v81, v218, v219\n"
      "v_cvt_pk_bf16_f32 v80, v216, v217\n"
      "v_mfma_f32_16x16x32_bf16 v[178:181], v[220:223], a[208:211], v[178:181]\n"
      "s_add_u32 s42, s42, 1\n"
      "v_permlane16_swap_b32 v176, v185\n"
      "v_mfma_f32_16x16x32_bf16 v[178:181], v[140:143], a[212:215], v[178:181]\n"
      "ds_read_b128 v[116:119], v2 offset:15360\n"
      "v_mul_f32 v176, s50, v176\n"
      "v_mfma_f32_16x16x32_bf16 v[172:175], a[216:219], v[130:133], 0\n"
      "ds_read_b128 v[100:103], v2 offset:12288\n"
      "v_mul_f32 v137, v176, v137\n"
      "v_mfma_f32_16x16x32_bf16 v[172:175], a[220:223], v[186:189], v[172:175]\n"
      "s_waitcnt lgkmcnt(2)\n"
      "v_mul_f32 v120, v185, v120\n"
      "v_mul_f32 v207, v176, v207\n"
      "v_mfma_f32_16x16x32_bf16 v[172:175], a[224:227], v[108:111], v[172:175]\n"
      "ds_read_b128 v[216:219], v2 offset:9216\n"
      "v_mul_f32 v212, v185, v212\n"
      "v_mfma_f32_16x16x32_bf16 v[220:223], a[240:243], v[130:133], 0\n"
      "v_mul_f32 v127, v185, v127\n"
      "v_mul_f32 v210, v185, v210\n"
      "v_mfma_f32_16x16x32_bf16 v[220:223], a[244:247], v[186:189], v[220:223]\n"
      "v_mul_f32 v213, v185, v213\n"
      "v_mul_f32 v160, v176, v160\n"
      "v_mfma_f32_16x16x32_bf16 v[220:223], a[248:251], v[108:111], v[220:223]\n"
      "v_mul_f32 v159, v176, v159\n"
      "v_mul_f32_e64 v211, v185, v211\n"
      "v_mfma_f32_16x16x32_bf16 v[140:143], v[232:235], v[130:133], 0\n"
      "ds_read_b128 v[168:171], v10 offset:1200\n"
      "v_cvt_pk_bf16_f32 v210, v210, v211\n"
      "v_mfma_f32_16x16x32_bf16 v[140:143], v[236:239], v[186:189], v[140:143]\n"
      "ds_read_b64 v[156:157], v10 offset:1192\n"
      "v_mul_f32 v121, v185, v121\n"
      "v_mfma_f32_16x16x32_bf16 v[140:143], v[240:243], v[108:111], v[140:143]\n"
      "s_waitcnt lgkmcnt(2)\n"
      "v_cvt_pk_bf16_f32 v84, v178, v179\n"
      "v_mul_f32 v136, v176, v136\n"
      "v_mfma_f32_16x16x32_bf16 v[172:175], a[228:231], v[216:219], v[172:175]\n"
      "ds_read_b128 v[202:205], v2 offset:256\n"
      "v_mul_f32 v122, v185, v122\n"
      "v_mfma_f32_16x16x32_bf16 v[172:175], a[232:235], v[100:103], v[172:175]\n"
      "ds_read_u16 v86, v11 offset:1232\n"
      "v_mul_f32_e64 v123, v185, v123\n"
      "v_mfma_f32_16x16x32_bf16 v[172:175], a[236:239], v[116:119], v[172:175]\n"
      "v_cvt_pk_bf16_f32 v211, v212, v213\n"
      "v_mul_f32 v126, v185, v126\n"
      "v_mfma_f32_16x16x32_bf16 v[220:223], a[252:255], v[216:219], v[220:223]\n"
      "v_cvt_pk_bf16_f32 v212, v120, v121\n"
      "v_mul_f32 v138, v176, v138\n"
      "v_mfma_f32_16x16x32_bf16 v[220:223], v[224:227], v[100:103], v[220:223]\n"
      "ds_read_b128 v[130:133], v2 offset:6400\n"
      "v_mul_f32 v129, v185, v129\n"
      "v_mfma_f32_16x16x32_bf16 v[220:223], v[228:231], v[116:119], v[220:223]\n"
      "v_mul_f32 v128, v185, v128\n"
      "v_cvt_pk_bf16_f32 v136, v136, v137\n"
      "v_mfma_f32_16x16x32_bf16 v[140:143], v[244:247], v[216:219], v[140:143]\n"
      "ds_read_b128 v[184:187], v2 offset:3328\n"
      "v_cvt_pk_bf16_f32 v126, v126, v127\n"
      "v_mfma_f32_16x16x32_bf16 v[140:143], v[248:251], v[100:103], v[140:143]\n"
      "v_fma_f32 v218, v172, v172, s52\n"
      "v_cvt_pk_bf16_f32 v149, v150, v151\n"
      "v_mfma_f32_16x16x32_bf16 v[140:143], v[252:255], v[116:119], v[140:143]\n"
      "ds_read_u16 v82, v11 offset:1200\n"
      "v_mul_f32_e64 v209, v176, v209\n"
      "v_mfma_f32_16x16x32_bf16 v[100:103], v[190:193], v[198:201], v[40:43]\n"
      "ds_read_u16 v78, v11 offset:1168\n"
      "v_cvt_pk_bf16_f32 v213, v122, v123\n"
      "v_mfma_f32_16x16x32_bf16 v[120:123], v[144:147], v[198:201], v[44:47]\n"
      "v_mul_f32 v208, v176, v208\n"
      "v_mul_f32 v219, v173, v173\n"
      "v_mfma_f32_16x16x32_bf16 v[150:153], v[210:213], v[198:201], v[48:51]\n"
      "s_waitcnt lgkmcnt(3)\n"
      "v_mul_f32 v139, v176, v139\n"
      "v_cvt_pk_bf16_f32 v127, v128, v129\n"
      "v_mfma_f32_16x16x32_bf16 v[116:119], v[168:171], v[198:201], v[24:27]\n"
      "v_cvt_pk_bf16_f32 v85, v180, v181\n"
      "v_mul_f32 v161, v176, v161\n"
      "v_mfma_f32_16x16x16_bf16 v[100:103], v[154:155], v[182:183], v[100:103]\n"
      "v_mul_f32 v158, v176, v158\n"
      "v_cvt_pk_bf16_f32 v158, v158, v159\n"
      "v_mul_f32_e64 v206, v176, v206\n"
      "v_mfma_f32_16x16x16_bf16 v[120:123], v[148:149], v[182:183], v[120:123]\n"
      "v_cvt_pk_bf16_f32 v159, v160, v161\n"
      "v_cvt_pk_bf16_f32 v160, v206, v207\n"
      "v_fmac_f32_e64 v219, v175, v175\n"
      "v_mfma_f32_16x16x16_bf16 v[150:153], v[126:127], v[182:183], v[150:153]\n"
      "v_exp_f32 v101, v101\n"
      "s_waitcnt vmcnt(1)\n"
      "v_or3_b32 v164, v164, v165, v166\n"
      "v_mfma_f32_16x16x16_bf16 v[116:119], v[156:157], v[182:183], v[116:119]\n"
      "v_exp_f32 v103, v103\n"
      "v_cvt_pk_bf16_f32 v161, v208, v209\n"
      "v_mfma_f32_16x16x32_bf16 v[206:209], a[216:219], v[202:205], 0\n"
      "s_waitcnt lgkmcnt(0)\n"
      "v_exp_f32_e64 v100, v100\n"
      "v_mfma_f32_16x16x32_bf16 v[206:209], a[220:223], v[184:187], v[206:209]\n"
      "v_exp_f32 v153, v153\n"
      "v_mfma_f32_16x16x32_bf16 v[206:209], a[224:227], v[130:133], v[206:209]\n"
      "v_cvt_pk_bf16_f32 v100, v100, v101\n"
      "v_fmac_f32 v218, v174, v174\n"
      "v_mfma_f32_16x16x32_bf16 v[176:179], a[240:243], v[202:205], 0\n"
      "v_exp_f32 v152, v152\n"
      "v_mfma_f32_16x16x32_bf16 v[176:179], a[244:247], v[184:187], v[176:179]\n"
      "v_exp_f32 v151, v151\n"
      "v_mfma_f32_16x16x32_bf16 v[176:179], a[248:251], v[130:133], v[176:179]\n"
      "v_exp_f32 v150, v150\n"
      "v_mfma_f32_16x16x32_bf16 v[108:111], v[232:235], v[202:205], 0\n"
      "v_fmac_f32 v219, v221, v221\n"
      "v_cvt_pk_bf16_f32 v137, v138, v139\n"
      "v_mfma_f32_16x16x32_bf16 v[108:111], v[236:239], v[184:187], v[108:111]\n"
      "v_exp_f32 v120, v120\n"
      "v_mfma_f32_16x16x32_bf16 v[108:111], v[240:243], v[130:133], v[108:111]\n"
      "v_exp_f32 v102, v102\n"
      "v_mfma_f32_16x16x32_bf16 v[132:135], v[190:193], v[112:115], v[52:55]\n"
      "v_exp_f32_e64 v116, v116\n"
      "v_mfma_f32_16x16x32_bf16 v[182:185], v[144:147], v[112:115], v[56:59]\n"
      "v_exp_f32_e64 v121, v121\n"
      "v_mfma_f32_16x16x32_bf16 v[202:205], v[210:213], v[112:115], v[60:63]\n"
      "v_exp_f32_e64 v122, v122\n"
      "v_mfma_f32_16x16x32_bf16 v[198:201], v[168:171], v[112:115], v[26:29]\n"
      "v_cvt_pk_bf16_f32 v90, v116, 0\n"
      "v_cvt_pk_bf16_f32 v101, v102, v103\n"
      "v_mfma_f32_16x16x16_bf16 v[132:135], v[154:155], v[214:215], v[132:135]\n"
      "s_waitcnt vmcnt(0)\n"
      "v_or3_b32 v164, v164, v167, v88\n"
      "v_exp_f32 v123, v123\n"
      "v_mfma_f32_16x16x16_bf16 v[182:185], v[148:149], v[214:215], v[182:185]\n"
      "v_cvt_pk_bf16_f32 v103, v122, v123\n"
      "v_cvt_pk_bf16_f32 v102, v120, v121\n"
      "v_fmac_f32 v218, v220, v220\n"
      "v_mfma_f32_16x16x16_bf16 v[202:205], v[126:127], v[214:215], v[202:205]\n"
      "v_cvt_pk_bf16_f32 v88, v150, v151\n"
      "v_exp_f32 v132, v132\n"
      "v_mfma_f32_16x16x16_bf16 v[198:201], v[156:157], v[214:215], v[198:201]\n"
      "v_cvt_pk_bf16_f32 v89, v152, v153\n"
      "v_exp_f32 v182, v182\n"
      "v_mfma_f32_16x16x32_bf16 v[120:123], v[32:35], v[100:103], 0\n"
      "v_exp_f32 v135, v135\n"
      "v_mfma_f32_16x16x32_bf16 v[120:123], v[32:35], v[88:91], v[120:123]\n"
      "v_exp_f32 v185, v185\n"
      "v_mfma_f32_16x16x32_bf16 v[150:153], v[194:197], v[100:103], 0\n"
      "v_exp_f32 v183, v183\n"
      "v_mfma_f32_16x16x32_bf16 v[150:153], v[76:79], v[88:91], v[150:153]\n"
      "v_exp_f32_e64 v133, v133\n"
      "v_mfma_f32_16x16x32_bf16 v[214:217], v[104:107], v[100:103], 0\n"
      "ds_read_b128 v[112:115], v2 offset:9472\n"
      "v_cvt_pk_bf16_f32 v132, v132, v133\n"
      "v_mfma_f32_16x16x32_bf16 v[214:217], v[80:83], v[88:91], v[214:217]\n"
      "v_rcp_f32 v120, v120\n"
      "v_mfma_f32_16x16x32_bf16 v[122:125], v[96:99], v[100:103], 0\n"
      "v_exp_f32 v203, v203\n"
      "v_mfma_f32_16x16x32_bf16 v[122:125], v[84:87], v[88:91], v[122:125]\n"
      "s_add_u32 s43, s43, 1\n"
      "v_exp_f32 v134, v134\n"
      "v_mfma_f32_16x16x32_bf16 v[128:131], v[190:193], v[158:161], v[64:67]\n"
      "v_exp_f32_e64 v205, v205\n"
      "v_mfma_f32_16x16x32_bf16 v[186:189], v[144:147], v[158:161], v[68:71]\n"
      "v_cvt_pk_bf16_f32 v133, v134, v135\n"
      "v_cvt_pk_bf16_f32 v134, v182, v183\n"
      "v_mfma_f32_16x16x32_bf16 v[116:119], v[210:213], v[158:161], v[72:75]\n"
      "s_waitcnt lgkmcnt(0)\n"
      "v_exp_f32 v202, v202\n"
      "v_mfma_f32_16x16x32_bf16 v[180:183], v[168:171], v[158:161], v[28:31]\n"
      "ds_read_b128 v[100:103], v2 offset:12544\n"
      "v_cvt_pk_bf16_f32 v92, v202, v203\n"
      "v_mfma_f32_16x16x16_bf16 v[128:131], v[154:155], v[136:137], v[128:131]\n"
      "v_fmac_f32 v218, v222, v222\n"
      "v_exp_f32 v184, v184\n"
      "v_mfma_f32_16x16x16_bf16 v[186:189], v[148:149], v[136:137], v[186:189]\n"
      "v_cvt_pk_bf16_f32 v135, v184, v185\n"
      "v_exp_f32_e64 v204, v204\n"
      "v_mfma_f32_16x16x16_bf16 v[116:119], v[126:127], v[136:137], v[116:119]\n"
      "ds_read_b128 v[200:203], v2 offset:15616\n"
      "v_fmac_f32_e64 v219, v223, v223\n"
      "v_mfma_f32_16x16x16_bf16 v[180:183], v[156:157], v[136:137], v[180:183]\n"
      "v_cvt_pk_bf16_f32 v93, v204, v205\n"
      "v_exp_f32 v128, v128\n"
      "v_exp_f32 v186, v186\n"
      "v_exp_f32_e64 v129, v129\n"
      "v_pk_mul_f32 v[150:151], v[150:151], v[120:121] op_sel_hi:[1,0]\n"
      "v_pk_mul_f32 v[152:153], v[152:153], v[120:121] op_sel_hi:[1,0]\n"
      "v_pk_mul_f32 v[124:125], v[124:125], v[120:121] op_sel_hi:[1,0]\n"
      "v_pk_mul_f32 v[216:217], v[216:217], v[120:121] op_sel_hi:[1,0]\n"
      "v_pk_mul_f32 v[214:215], v[214:215], v[120:121] op_sel_hi:[1,0]\n"
      "v_cvt_pk_bf16_f32 v150, v150, v151\n"
      "v_pk_mul_f32 v[122:123], v[122:123], v[120:121] op_sel_hi:[1,0]\n"
      "v_pk_fma_f32 v[218:219], v[140:141], v[140:141], v[218:219]\n"
      "v_mfma_f32_16x16x32_bf16 v[206:209], a[228:231], v[112:115], v[206:209]\n"
      "s_waitcnt lgkmcnt(1)\n"
      "v_exp_f32 v198, v198\n"
      "v_mfma_f32_16x16x32_bf16 v[206:209], a[232:235], v[100:103], v[206:209]\n"
      "s_waitcnt lgkmcnt(0)\n"
      "v_exp_f32 v131, v131\n"
      "v_mfma_f32_16x16x32_bf16 v[206:209], a[236:239], v[200:203], v[206:209]\n"
      "v_cvt_pk_bf16_f32 v122, v122, v123\n"
      "v_cvt_pk_bf16_f32 v151, v152, v153\n"
      "v_mfma_f32_16x16x32_bf16 v[176:179], a[252:255], v[112:115], v[176:179]\n"
      "v_cvt_pk_bf16_f32 v94, v198, 0\n"
      "v_cvt_pk_bf16_f32 v153, v216, v217\n"
      "v_mfma_f32_16x16x32_bf16 v[176:179], v[224:227], v[100:103], v[176:179]\n"
      "ds_read_b128 v[136:139], v7 offset:4096\n"
      "v_cvt_pk_bf16_f32 v152, v214, v215\n"
      "v_mfma_f32_16x16x32_bf16 v[176:179], v[228:231], v[200:203], v[176:179]\n"
      "v_exp_f32 v187, v187\n"
      "v_mfma_f32_16x16x32_bf16 v[108:111], v[244:247], v[112:115], v[108:111]\n"
      "v_exp_f32 v118, v118\n"
      "v_mfma_f32_16x16x32_bf16 v[108:111], v[248:251], v[100:103], v[108:111]\n"
      "ds_read_b64 v[120:121], v10 offset:1184\n"
      "v_mul_f32_e64 v159, v207, v207\n"
      "v_mfma_f32_16x16x32_bf16 v[108:111], v[252:255], v[200:203], v[108:111]\n"
      "ds_read_b128 v[182:185], v7\n"
      "v_cvt_pk_bf16_f32 v123, v124, v125\n"
      "v_mfma_f32_16x16x32_bf16 v[100:103], v[32:35], v[132:135], 0\n"
      "v_exp_f32_e64 v119, v119\n"
      "v_mfma_f32_16x16x32_bf16 v[100:103], v[32:35], v[92:95], v[100:103]\n"
      "ds_read_b128 v[214:217], v10 offset:1168\n"
      "v_cvt_pk_bf16_f32 v128, v128, v129\n"
      "v_mfma_f32_16x16x32_bf16 v[202:205], v[194:197], v[132:135], 0\n"
      "v_exp_f32_e64 v117, v117\n"
      "v_mfma_f32_16x16x32_bf16 v[202:205], v[76:79], v[92:95], v[202:205]\n"
      "ds_write_b128 v3, v[150:153] offset:18432\n"
      "v_mfma_f32_16x16x32_bf16 v[112:115], v[104:107], v[132:135], 0\n"
      "s_nop 0\n"
      "v_rcp_f32 v100, v100\n"
      "v_mfma_f32_16x16x32_bf16 v[112:115], v[80:83], v[92:95], v[112:115]\n"
      "v_cvt_pk_bf16_f32 v89, v118, v119\n"
      "v_fma_f32 v158, v206, v206, s52\n"
      "v_mfma_f32_16x16x32_bf16 v[198:201], v[96:99], v[132:135], 0\n"
      "s_waitcnt lgkmcnt(1)\n"
      "v_exp_f32 v130, v130\n"
      "v_mfma_f32_16x16x32_bf16 v[198:201], v[84:87], v[92:95], v[198:201]\n"
      "buffer_store_dwordx4 v[182:185], v20, s[24:27], s85 offen sc1\n"
      "v_pk_mul_f32 v[202:203], v[202:203], v[100:101] op_sel_hi:[1,0]\n"
      "v_pk_mul_f32 v[204:205], v[204:205], v[100:101] op_sel_hi:[1,0]\n"
      "v_pk_mul_f32 v[114:115], v[114:115], v[100:101] op_sel_hi:[1,0]\n"
      "v_mfma_f32_16x16x32_bf16 v[182:185], v[214:217], v[190:193], v[30:33]\n"
      "ds_read_b128 v[160:163], v7 offset:8192\n"
      "v_cvt_pk_bf16_f32 v129, v130, v131\n"
      "v_mfma_f32_16x16x32_bf16 v[150:153], v[214:217], v[144:147], v[30:33]\n"
      "ds_read_b128 v[190:193], v2 offset:512\n"
      "v_cvt_pk_bf16_f32 v202, v202, v203\n"
      "v_mfma_f32_16x16x32_bf16 v[144:147], v[214:217], v[210:213], v[30:33]\n"
      "ds_write_b64 v4, v[122:123] offset:18432\n"
      "v_mfma_f32_16x16x32_bf16 v[122:125], v[214:217], v[168:171], v[30:33]\n"
      "ds_read_b128 v[214:217], v2 offset:3584\n"
      "v_cvt_pk_bf16_f32 v203, v204, v205\n"
      "v_mfma_f32_16x16x16_bf16 v[182:185], v[120:121], v[154:155], v[182:185]\n"
      "ds_read_b128 v[210:213], v2 offset:6656\n"
      "v_exp_f32_e64 v180, v180\n"
      "v_mfma_f32_16x16x16_bf16 v[150:153], v[120:121], v[148:149], v[150:153]\n"
      "v_exp_f32_e64 v189, v189\n"
      "v_cvt_pk_bf16_f32 v205, v114, v115\n"
      "v_mfma_f32_16x16x16_bf16 v[144:147], v[120:121], v[126:127], v[144:147]\n"
      "buffer_store_dwordx4 v[136:139], v21, s[24:27], s85 offen sc1\n"
      "v_pk_fma_f32 v[158:159], v[208:209], v[208:209], v[158:159]\n"
      "v_pk_fma_f32 v[218:219], v[142:143], v[142:143], v[218:219]\n"
      "v_pk_fma_f32 v[158:159], v[176:177], v[176:177], v[158:159]\n"
      "v_pk_mul_f32 v[112:113], v[112:113], v[100:101] op_sel_hi:[1,0]\n"
      "v_pk_fma_f32 v[158:159], v[178:179], v[178:179], v[158:159]\n"
      "v_pk_mul_f32 v[200:201], v[200:201], v[100:101] op_sel_hi:[1,0]\n"
      "v_mfma_f32_16x16x16_bf16 v[122:125], v[120:121], v[156:157], v[122:125]\n"
      "v_cvt_pk_bf16_f32 v90, v180, 0\n"
      "v_cvt_pk_bf16_f32 v204, v112, v113\n"
      "s_waitcnt lgkmcnt(1)\n"
      "v_mfma_f32_16x16x32_bf16 v[132:135], a[216:219], v[190:193], 0\n"
      "ds_write_b128 v3, v[202:205] offset:18688\n"
      "v_pk_fma_f32 v[158:159], v[108:109], v[108:109], v[158:159]\n"
      "v_pk_mul_f32 v[198:199], v[198:199], v[100:101] op_sel_hi:[1,0]\n"
      "v_mfma_f32_16x16x32_bf16 v[132:135], a[220:223], v[214:217], v[132:135]\n"
      "s_waitcnt lgkmcnt(1)\n"
      "v_exp_f32 v188, v188\n"
      "v_mfma_f32_16x16x32_bf16 v[132:135], a[224:227], v[210:213], v[132:135]\n"
      "v_exp_f32 v116, v116\n"
      "v_mfma_f32_16x16x32_bf16 v[118:121], a[240:243], v[190:193], 0\n"
      "buffer_store_dwordx4 v[160:163], v22, s[24:27], s85 offen sc1\n"
      "v_pk_fma_f32 v[158:159], v[110:111], v[110:111], v[158:159]\n"
      "v_mfma_f32_16x16x32_bf16 v[118:121], a[244:247], v[214:217], v[118:121]\n"
      "ds_read_b128 v[202:205], v2 offset:12800\n"
      "v_cvt_pk_bf16_f32 v131, v188, v189\n"
      "v_mfma_f32_16x16x32_bf16 v[118:121], a[248:251], v[210:213], v[118:121]\n"
      "ds_read_b128 v[136:139], v7 offset:16384\n"
      "v_cvt_pk_bf16_f32 v130, v186, v187\n"
      "v_mfma_f32_16x16x32_bf16 v[168:171], v[232:235], v[190:193], 0\n"
      "v_cvt_pk_bf16_f32 v88, v116, v117\n"
      "v_cvt_pk_bf16_f32 v198, v198, v199\n"
      "v_mfma_f32_16x16x32_bf16 v[168:171], v[236:239], v[214:217], v[168:171]\n"
      "ds_read_b128 v[100:103], v7 offset:12288\n"
      "v_cndmask_b32 v182, v182, v150, s[58:59]\n"
      "v_mfma_f32_16x16x32_bf16 v[168:171], v[240:243], v[210:213], v[168:171]\n"
      "ds_read_b128 v[150:153], v2 offset:9728\n"
      "v_add_f32_e64 v218, v218, v219\n"
      "v_mfma_f32_16x16x32_bf16 v[212:215], v[32:35], v[128:131], 0\n"
      "v_cndmask_b32 v182, v182, v144, s[60:61]\n"
      "v_cndmask_b32 v182, v182, v122, s[62:63]\n"
      "v_mfma_f32_16x16x32_bf16 v[212:215], v[32:35], v[88:91], v[212:215]\n"
      "v_exp_f32_e64 v182, v182\n"
      "v_mfma_f32_16x16x32_bf16 v[184:187], v[194:197], v[128:131], 0\n"
      "ds_read_b128 v[114:117], v2 offset:15872\n"
      "v_cvt_pk_bf16_f32 v182, v182, v182\n"
      "v_mfma_f32_16x16x32_bf16 v[184:187], v[76:79], v[88:91], v[184:187]\n"
      "ds_write_b16 v13, v182\n"
      "v_mfma_f32_16x16x32_bf16 v[160:163], v[104:107], v[128:131], 0\n"
      "ds_read_b128 v[144:147], v14\n"
      "v_cvt_pk_bf16_f32 v199, v200, v201\n"
      "v_mfma_f32_16x16x32_bf16 v[160:163], v[80:83], v[88:91], v[160:163]\n"
      "v_rcp_f32_e64 v212, v212\n"
      "v_mfma_f32_16x16x32_bf16 v[214:217], v[96:99], v[128:131], 0\n"
      "ds_write_b64 v4, v[198:199] offset:18688\n"
      "v_mfma_f32_16x16x32_bf16 v[214:217], v[84:87], v[88:91], v[214:217]\n"
      "s_waitcnt lgkmcnt(4)\n"
      "v_mul_f32 v187, v212, v187\n"
      "v_mul_f32_e64 v185, v212, v185\n"
      "v_mfma_f32_16x16x32_bf16 v[132:135], a[228:231], v[150:153], v[132:135]\n"
      "ds_read_b128 v[198:201], v14 offset:64\n"
      "v_mul_f32 v186, v212, v186\n"
      "v_mfma_f32_16x16x32_bf16 v[132:135], a[232:235], v[202:205], v[132:135]\n"
      "s_waitcnt lgkmcnt(4)\n"
      "v_add_f32 v158, v158, v159\n"
      "v_mul_f32 v184, v212, v184\n"
      "v_mfma_f32_16x16x32_bf16 v[132:135], a[236:239], v[114:117], v[132:135]\n"
      "v_mul_f32 v162, v212, v162\n"
      "v_mul_f32 v163, v212, v163\n"
      "v_mfma_f32_16x16x32_bf16 v[118:121], a[252:255], v[150:153], v[118:121]\n"
      "v_cvt_pk_bf16_f32 v184, v184, v185\n"
      "v_cvt_pk_bf16_f32 v185, v186, v187\n"
      "v_mfma_f32_16x16x32_bf16 v[118:121], v[224:227], v[202:205], v[118:121]\n"
      "ds_write_b32 v31, v164 offset:5632\n"
      "v_mfma_f32_16x16x32_bf16 v[118:121], v[228:231], v[114:117], v[118:121]\n"
      "v_fma_f32 v190, v132, v132, s52\n"
      "v_cvt_pk_bf16_f32 v187, v162, v163\n"
      "v_mfma_f32_16x16x32_bf16 v[168:171], v[244:247], v[150:153], v[168:171]\n"
      "buffer_store_dwordx4 v[100:103], v23, s[24:27], s85 offen sc1\n"
      "v_pk_mul_f32 v[160:161], v[160:161], v[212:213] op_sel_hi:[1,0]\n"
      "v_mfma_f32_16x16x32_bf16 v[168:171], v[248:251], v[202:205], v[168:171]\n"
      "s_waitcnt lgkmcnt(1)\n"
      "v_mul_f32 v191, v133, v133\n"
      "v_cvt_pk_bf16_f32 v186, v160, v161\n"
      "v_mfma_f32_16x16x32_bf16 v[168:171], v[252:255], v[114:117], v[168:171]\n"
      "ds_write_b128 v3, v[184:187] offset:18944\n"
      "v_pk_mul_f32 v[214:215], v[214:215], v[212:213] op_sel_hi:[1,0]\n"
      "v_pk_fma_f32 v[190:191], v[134:135], v[134:135], v[190:191]\n"
      "v_pk_mul_f32 v[216:217], v[216:217], v[212:213] op_sel_hi:[1,0]\n"
      "v_pk_fma_f32 v[190:191], v[118:119], v[118:119], v[190:191]\n"
      "v_pk_fma_f32 v[190:191], v[120:121], v[120:121], v[190:191]\n"
      "v_permlane16_swap_b32 v218, v158\n"
      "v_cvt_pk_bf16_f32 v214, v214, v215\n"
      "v_pk_fma_f32 v[190:191], v[168:169], v[168:169], v[190:191]\n"
      "v_add_f32 v218, v218, v158\n"
      "v_pk_fma_f32 v[190:191], v[170:171], v[170:171], v[190:191]\n"
      "v_add_f32 v190, v190, v191\n"
      "v_mov_b32 v158, v190\n"
      "v_cvt_pk_bf16_f32 v215, v216, v217\n"
      "v_mfma_f32_16x16x32_bf16 v[162:165], v[144:147], v[32:35], 0\n"
      "ds_write_b64 v4, v[214:215] offset:18944\n"
      "v_permlane16_swap_b32 v190, v158\n"
      "v_add_f32_e64 v190, v190, v158\n"
      "v_add_u32 v10, 2336, v10\n"
      "s_nop 0\n"
      "v_permlane32_swap_b32_e64 v218, v190\n"
      "v_add_f32 v218, v218, v190\n"
      "v_mfma_f32_16x16x32_bf16 v[162:165], v[198:201], v[32:35], v[162:165]\n"
      "ds_write_b32 v31, v218 offset:1024\n"
      "v_add_u32 v11, 2336, v11\n"
      "s_cmp_ge_u32 s95, 0x40000000\n"
      "s_cselect_b64 vcc, -1, s[56:57]\n"
      "buffer_store_dwordx4 v[136:139], v25, s[24:27], s85 offen sc1\n"
      "s_waitcnt lgkmcnt(0)\n"
      "s_barrier\n"
      "s_cbranch_vccnz L_disp0_%=\n"
      "L_loop0_%=:\n"
      "v_mfma_f32_16x16x32_bf16 v[152:155], v[144:147], v[194:197], 0\n"
      "ds_read_b128 v[190:193], v0\n"
      "ds_read_b128 v[202:205], v9 offset:1024\n"
      "v_mfma_f32_16x16x32_bf16 v[152:155], v[198:201], v[76:79], v[152:155]\n"
      "ds_read_b128 v[100:103], v0 offset:12288\n"
      "v_mfma_f32_16x16x32_bf16 v[210:213], v[144:147], v[104:107], 0\n"
      "s_add_u32 m0, s64, 18432\n"
      "ds_read_b128 v[216:219], v0 offset:6144\n"
      "v_mfma_f32_16x16x32_bf16 v[210:213], v[198:201], v[80:83], v[210:213]\n"
      "buffer_load_dwordx4 v16, s[16:19], s45 offen  lds\n"
      "v_mfma_f32_16x16x32_bf16 v[180:183], v[144:147], v[96:99], 0\n"
      "ds_read_b128 v[128:131], v1\n"
      "s_add_u32 m0, s64, 22528\n"
      "v_mfma_f32_16x16x32_bf16 v[180:183], v[198:201], v[84:87], v[180:183]\n"
      "ds_read_b128 v[114:117], v1 offset:12288\n"
      "s_waitcnt lgkmcnt(5)\n"
      "v_mfma_f32_16x16x32_bf16 v[148:151], a[0:3], v[190:193], 0\n"
      "ds_read_b128 v[212:215], v1 offset:6144\n"
      "v_readlane_b32 s85, v37, s42\n"
      "v_mfma_f32_16x16x32_bf16 v[124:127], a[24:27], v[190:193], 0\n"
      "s_waitcnt lgkmcnt(5)\n"
      "v_add_f32_e64 v204, v204, v205\n"
      "v_cndmask_b32 v152, v152, v210, s[58:59]\n"
      "v_mfma_f32_16x16x32_bf16 v[186:189], a[48:51], v[190:193], 0\n"
      "buffer_load_dwordx4 v17, s[16:19], s45 offen  lds\n"
      "v_mfma_f32_16x16x32_bf16 v[198:201], a[72:75], v[190:193], 0\n"
      "v_add_f32 v202, v202, v203\n"
      "v_add_f32 v202, v202, v204\n"
      "v_mfma_f32_16x16x32_bf16 v[182:185], a[96:99], v[190:193], 0\n"
      "v_rsq_f32 v202, v202\n"
      "v_mfma_f32_16x16x32_bf16 v[144:147], a[120:123], v[190:193], 0\n"
      "s_waitcnt lgkmcnt(1)\n"
      "v_mul_f32 v202, 0x415db3d7, v202\n"
      "v_mov_b32 v204, v202\n"
      "v_mfma_f32_16x16x32_bf16 v[148:151], a[4:7], v[128:131], v[148:151]\n"
      "s_add_u32 m0, s64, 26624\n"
      "v_permlane32_swap_b32 v202, v204\n"
      "v_mfma_f32_16x16x32_bf16 v[148:151], a[8:11], v[216:219], v[148:151]\n"
      "buffer_load_dwordx4 v18, s[16:19], s45 offen  lds\n"
      "v_mfma_f32_16x16x32_bf16 v[124:127], a[28:31], v[128:131], v[124:127]\n"
      "v_mul_f32 v169, v204, v169\n"
      "v_mov_b32 v203, v202\n"
      "v_mfma_f32_16x16x32_bf16 v[124:127], a[32:35], v[216:219], v[124:127]\n"
      "s_add_u32 m0, s64, 30720\n"
      "v_mul_f32 v132, v204, v132\n"
      "v_mul_f32 v133, v204, v133\n"
      "v_mfma_f32_16x16x32_bf16 v[186:189], a[52:55], v[128:131], v[186:189]\n"
      "v_permlane16_swap_b32 v202, v203\n"
      "v_mfma_f32_16x16x32_bf16 v[186:189], a[56:59], v[216:219], v[186:189]\n"
      "ds_read_b128 v[136:139], v1 offset:14336\n"
      "v_mul_f32 v143, v202, v143\n"
      "v_mfma_f32_16x16x32_bf16 v[198:201], a[76:79], v[128:131], v[198:201]\n"
      "v_mul_f32 v172, v202, v172\n"
      "v_mul_f32 v141, v202, v141\n"
      "v_mfma_f32_16x16x32_bf16 v[198:201], a[80:83], v[216:219], v[198:201]\n"
      "buffer_load_dwordx4 v19, s[16:19], s45 offen  lds\n"
      "v_mfma_f32_16x16x32_bf16 v[182:185], a[100:103], v[128:131], v[182:185]\n"
      "v_mul_f32 v223, v202, v223\n"
      "v_mul_f32 v220, v202, v220\n"
      "v_mfma_f32_16x16x32_bf16 v[182:185], a[104:107], v[216:219], v[182:185]\n"
      "v_mul_f32 v168, v204, v168\n"
      "v_mul_f32 v206, v203, v206\n"
      "v_mfma_f32_16x16x32_bf16 v[144:147], a[124:127], v[128:131], v[144:147]\n"
      "s_mov_b32 m0, s7\n"
      "v_mul_f32 v221, v202, v221\n"
      "v_mul_f32 v173, v202, v173\n"
      "v_mfma_f32_16x16x32_bf16 v[144:147], a[128:131], v[216:219], v[144:147]\n"
      "s_waitcnt lgkmcnt(0)\n"
      "v_readlane_b32 s84, v37, s43\n"
      "v_mul_f32 v176, v203, v176\n"
      "v_mfma_f32_16x16x32_bf16 v[148:151], a[12:15], v[212:215], v[148:151]\n"
      "buffer_load_dwordx4 v15, s[0:3], s4 offen  lds\n"
      "v_mfma_f32_16x16x32_bf16 v[148:151], a[16:19], v[100:103], v[148:151]\n"
      "v_mul_f32 v111, v203, v111\n"
      "v_mul_f32 v174, v202, v174\n"
      "v_mfma_f32_16x16x32_bf16 v[148:151], a[20:23], v[114:117], v[148:151]\n"
      "v_mul_f32 v209, v203, v209\n"
      "v_mul_f32 v121, v204, v121\n"
      "v_mfma_f32_16x16x32_bf16 v[124:127], a[36:39], v[212:215], v[124:127]\n"
      "v_mul_f32 v207, v203, v207\n"
      "v_mov_b32_dpp v162, v162 quad_perm:[0,0,0,0] row_mask:0xf bank_mask:0xf\n"
      "v_mfma_f32_16x16x32_bf16 v[124:127], a[40:43], v[100:103], v[124:127]\n"
      "v_mul_f32 v222, v202, v222\n"
      "v_mul_f32 v175, v202, v175\n"
      "v_mfma_f32_16x16x32_bf16 v[124:127], a[44:47], v[114:117], v[124:127]\n"
      "buffer_load_dwordx4 v[156:159], v36, s[28:31], s47 offen\n"
      "v_mfma_f32_16x16x32_bf16 v[186:189], a[60:63], v[212:215], v[186:189]\n"
      "v_cndmask_b32 v152, v152, v180, s[60:61]\n"
      "v_mul_f32 v177, v203, v177\n"
      "v_mfma_f32_16x16x32_bf16 v[186:189], a[64:67], v[100:103], v[186:189]\n"
      "v_cvt_pk_bf16_f32 v172, v172, v173\n"
      "v_cvt_pk_bf16_f32 v173, v174, v175\n"
      "v_mfma_f32_16x16x32_bf16 v[186:189], a[68:71], v[114:117], v[186:189]\n"
      "v_mul_f32 v109, v203, v109\n"
      "v_mul_f32 v140, v202, v140\n"
      "v_mfma_f32_16x16x32_bf16 v[198:201], a[84:87], v[212:215], v[198:201]\n"
      "v_rcp_f32 v162, v162\n"
      "v_mfma_f32_16x16x32_bf16 v[198:201], a[88:91], v[100:103], v[198:201]\n"
      "ds_read_b128 v[164:167], v1 offset:2048\n"
      "v_cvt_pk_bf16_f32 v206, v206, v207\n"
      "v_mfma_f32_16x16x32_bf16 v[198:201], a[92:95], v[114:117], v[198:201]\n"
      "v_mul_f32 v120, v204, v120\n"
      "v_mul_f32 v152, v162, v152\n"
      "v_mfma_f32_16x16x32_bf16 v[182:185], a[108:111], v[212:215], v[182:185]\n"
      "buffer_load_dword v90, v29, s[28:31], s47 offen\n"
      "v_mfma_f32_16x16x32_bf16 v[182:185], a[112:115], v[100:103], v[182:185]\n"
      "v_mul_f32_e64 v171, v204, v171\n"
      "v_fma_f32 v122, v148, v148, s51\n"
      "v_mfma_f32_16x16x32_bf16 v[182:185], a[116:119], v[114:117], v[182:185]\n"
      "v_fma_f32 v81, v198, v198, s51\n"
      "v_mul_f32_e64 v108, v203, v108\n"
      "v_mfma_f32_16x16x32_bf16 v[144:147], a[132:135], v[212:215], v[144:147]\n"
      "ds_read_b128 v[160:163], v0 offset:14336\n"
      "v_cvt_pk_bf16_f32 v174, v220, v221\n"
      "v_mfma_f32_16x16x32_bf16 v[144:147], a[136:139], v[100:103], v[144:147]\n"
      "v_mul_f32 v179, v203, v179\n"
      "v_mul_f32 v178, v203, v178\n"
      "v_mfma_f32_16x16x32_bf16 v[144:147], a[140:143], v[114:117], v[144:147]\n"
      "v_fmac_f32 v122, v149, v149\n"
      "v_cvt_pk_bf16_f32 v175, v222, v223\n"
      "v_mfma_f32_16x16x32_bf16 v[194:197], v[190:193], a[144:147], 0\n"
      "v_fmac_f32 v122, v150, v150\n"
      "v_mfma_f32_16x16x32_bf16 v[194:197], v[128:131], a[148:151], v[194:197]\n"
      "ds_write_b128 v5, v[172:175]\n"
      "v_mfma_f32_16x16x32_bf16 v[194:197], v[216:219], a[152:155], v[194:197]\n"
      "v_fmac_f32 v122, v151, v151\n"
      "v_fmac_f32 v122, v124, v124\n"
      "v_mfma_f32_16x16x32_bf16 v[194:197], v[212:215], a[156:159], v[194:197]\n"
      "v_mul_f32 v208, v203, v208\n"
      "v_cvt_pk_bf16_f32 v168, v168, v169\n"
      "v_mfma_f32_16x16x32_bf16 v[194:197], v[100:103], a[160:163], v[194:197]\n"
      "v_mul_f32 v134, v204, v134\n"
      "v_cvt_pk_bf16_f32 v207, v208, v209\n"
      "v_mfma_f32_16x16x32_bf16 v[194:197], v[114:117], a[164:167], v[194:197]\n"
      "ds_read_b128 v[172:175], v0 offset:2048\n"
      "v_cvt_pk_bf16_f32 v209, v178, v179\n"
      "v_mfma_f32_16x16x32_bf16 v[104:107], v[190:193], a[168:171], 0\n"
      "v_cvt_pk_bf16_f32 v152, v152, v152\n"
      "v_cvt_pk_bf16_f32 v208, v176, v177\n"
      "v_mfma_f32_16x16x32_bf16 v[104:107], v[128:131], a[172:175], v[104:107]\n"
      "ds_read_b128 v[220:223], v1 offset:8192\n"
      "v_cvt_pk_bf16_f32 v108, v108, v109\n"
      "v_mfma_f32_16x16x32_bf16 v[104:107], v[216:219], a[176:179], v[104:107]\n"
      "v_fmac_f32 v81, v199, v199\n"
      "v_fmac_f32 v122, v125, v125\n"
      "v_mfma_f32_16x16x32_bf16 v[104:107], v[212:215], a[180:183], v[104:107]\n"
      "v_mul_f32 v119, v204, v119\n"
      "v_fmac_f32 v122, v126, v126\n"
      "v_mfma_f32_16x16x32_bf16 v[104:107], v[100:103], a[184:187], v[104:107]\n"
      "v_mul_f32 v110, v203, v110\n"
      "v_fmac_f32 v81, v200, v200\n"
      "v_mfma_f32_16x16x32_bf16 v[104:107], v[114:117], a[188:191], v[104:107]\n"
      "ds_write_b16 v12, v152 offset:16\n"
      "v_mfma_f32_16x16x32_bf16 v[96:99], v[190:193], a[192:195], 0\n"
      "v_fmac_f32 v122, v127, v127\n"
      "v_fmac_f32 v81, v201, v201\n"
      "v_mfma_f32_16x16x32_bf16 v[96:99], v[128:131], a[196:199], v[96:99]\n"
      "ds_read_b128 v[152:155], v0 offset:8192\n"
      "v_mul_f32_e64 v170, v204, v170\n"
      "v_mfma_f32_16x16x32_bf16 v[96:99], v[216:219], a[200:203], v[96:99]\n"
      "v_cvt_pk_bf16_f32 v194, v194, v195\n"
      "v_cvt_pk_bf16_f32 v109, v110, v111\n"
      "v_mfma_f32_16x16x32_bf16 v[96:99], v[212:215], a[204:207], v[96:99]\n"
      "ds_write_b128 v5, v[206:209] offset:6144\n"
      "v_mfma_f32_16x16x32_bf16 v[96:99], v[100:103], a[208:211], v[96:99]\n"
      "s_waitcnt lgkmcnt(3)\n"
      "v_fmac_f32 v81, v182, v182\n"
      "v_mul_f32 v142, v202, v142\n"
      "v_mfma_f32_16x16x32_bf16 v[96:99], v[114:117], a[212:215], v[96:99]\n"
      "s_or_b32 s95, s84, s85\n"
      "v_fmac_f32 v122, v186, v186\n"
      "v_fmac_f32 v122, v187, v187\n"
      "v_mfma_f32_16x16x32_bf16 v[190:193], a[0:3], v[172:175], 0\n"
      "s_waitcnt lgkmcnt(1)\n"
      "v_fmac_f32 v81, v183, v183\n"
      "v_cvt_pk_bf16_f32 v140, v140, v141\n"
      "v_mfma_f32_16x16x32_bf16 v[190:193], a[4:7], v[164:167], v[190:193]\n"
      "v_cvt_pk_bf16_f32 v96, v96, v97\n"
      "v_fmac_f32 v81, v184, v184\n"
      "v_mfma_f32_16x16x32_bf16 v[190:193], a[8:11], v[152:155], v[190:193]\n"
      "v_fmac_f32 v122, v188, v188\n"
      "v_fmac_f32 v81, v185, v185\n"
      "v_mfma_f32_16x16x32_bf16 v[190:193], a[12:15], v[220:223], v[190:193]\n"
      "v_mul_f32 v135, v204, v135\n"
      "v_fmac_f32_e64 v81, v144, v144\n"
      "v_mfma_f32_16x16x32_bf16 v[190:193], a[16:19], v[160:163], v[190:193]\n"
      "ds_read_b128 v[100:103], v0 offset:10240\n"
      "v_fmac_f32 v81, v145, v145\n"
      "v_mfma_f32_16x16x32_bf16 v[190:193], a[20:23], v[136:139], v[190:193]\n"
      "ds_read_b128 v[210:213], v9 offset:5632\n"
      "v_fmac_f32 v81, v146, v146\n"
      "v_mfma_f32_16x16x32_bf16 v[176:179], a[24:27], v[172:175], 0\n"
      "ds_read_b128 v[206:209], v1 offset:4096\n"
      "v_cvt_pk_bf16_f32 v132, v132, v133\n"
      "v_mfma_f32_16x16x32_bf16 v[176:179], a[28:31], v[164:167], v[176:179]\n"
      "v_readlane_b32 s86, v38, s43\n"
      "v_cvt_pk_bf16_f32 v141, v142, v143\n"
      "v_mfma_f32_16x16x32_bf16 v[176:179], a[32:35], v[152:155], v[176:179]\n"
      "v_fmac_f32_e64 v122, v189, v189\n"
      "v_cvt_pk_bf16_f32 v169, v170, v171\n"
      "v_mfma_f32_16x16x32_bf16 v[176:179], a[36:39], v[220:223], v[176:179]\n"
      "v_mul_f32 v118, v204, v118\n"
      "v_fmac_f32 v81, v147, v147\n"
      "v_mfma_f32_16x16x32_bf16 v[176:179], a[40:43], v[160:163], v[176:179]\n"
      "v_fma_f32 v84, v190, v190, s51\n"
      "v_fmac_f32_e64 v84, v191, v191\n"
      "v_mfma_f32_16x16x32_bf16 v[176:179], a[44:47], v[136:139], v[176:179]\n"
      "ds_write_b64 v6, v[140:141]\n"
      "v_mfma_f32_16x16x32_bf16 v[202:205], a[48:51], v[172:175], 0\n"
      "s_waitcnt lgkmcnt(1)\n"
      "v_permlane16_swap_b32 v122, v81\n"
      "v_mfma_f32_16x16x32_bf16 v[202:205], a[52:55], v[164:167], v[202:205]\n"
      "v_or3_b32 v210, v210, v211, v212\n"
      "v_add_f32_e64 v122, v122, v81\n"
      "v_mfma_f32_16x16x32_bf16 v[202:205], a[56:59], v[152:155], v[202:205]\n"
      "ds_read_b128 v[216:219], v0 offset:4096\n"
      "v_bitop3_b32 v210, v210, v213, s33 bitop3:0xa8\n"
      "v_mfma_f32_16x16x32_bf16 v[202:205], a[60:63], v[220:223], v[202:205]\n"
      "v_fmac_f32 v84, v192, v192\n"
      "v_mov_b32 v81, v122\n"
      "v_mfma_f32_16x16x32_bf16 v[202:205], a[64:67], v[160:163], v[202:205]\n"
      "s_nop 0\n"
      "v_permlane32_swap_b32 v122, v81\n"
      "v_mfma_f32_16x16x32_bf16 v[202:205], a[68:71], v[136:139], v[202:205]\n"
      "v_add_f32_e64 v122, v122, v81\n"
      "v_cvt_pk_bf16_f32 v195, v196, v197\n"
      "v_mfma_f32_16x16x32_bf16 v[128:131], a[72:75], v[172:175], 0\n"
      "v_cvt_pk_bf16_f32 v133, v134, v135\n"
      "v_fmac_f32_e64 v84, v193, v193\n"
      "v_mfma_f32_16x16x32_bf16 v[128:131], a[76:79], v[164:167], v[128:131]\n"
      "ds_write_b64 v6, v[168:169] offset:12288\n"
      "v_mfma_f32_16x16x32_bf16 v[128:131], a[80:83], v[152:155], v[128:131]\n"
      "v_rsq_f32 v122, v122\n"
      "v_mfma_f32_16x16x32_bf16 v[128:131], a[84:87], v[220:223], v[128:131]\n"
      "v_cvt_pk_bf16_f32 v135, v120, v121\n"
      "v_mov_b32 v81, v122\n"
      "v_mfma_f32_16x16x32_bf16 v[128:131], a[88:91], v[160:163], v[128:131]\n"
      "ds_read_b128 v[212:215], v0 offset:16384\n"
      "v_fmac_f32 v84, v176, v176\n"
      "v_mfma_f32_16x16x32_bf16 v[128:131], a[92:95], v[136:139], v[128:131]\n"
      "v_permlane16_swap_b32 v122, v81\n"
      "v_mfma_f32_16x16x32_bf16 v[168:171], a[96:99], v[172:175], 0\n"
      "v_fmac_f32 v84, v177, v177\n"
      "v_cmp_ne_u32 s[56:57], 0, v210\n"
      "v_mfma_f32_16x16x32_bf16 v[168:171], a[100:103], v[164:167], v[168:171]\n"
      "v_mul_f32 v122, s50, v122\n"
      "v_mul_f32_e64 v149, v122, v149\n"
      "v_mfma_f32_16x16x32_bf16 v[168:171], a[104:107], v[152:155], v[168:171]\n"
      "v_cvt_pk_bf16_f32 v134, v118, v119\n"
      "v_cvt_pk_bf16_f32 v104, v104, v105\n"
      "v_mfma_f32_16x16x32_bf16 v[168:171], a[108:111], v[220:223], v[168:171]\n"
      "v_fma_f32 v210, v128, v128, s51\n"
      "v_fmac_f32_e64 v210, v129, v129\n"
      "v_mfma_f32_16x16x32_bf16 v[168:171], a[112:115], v[160:163], v[168:171]\n"
      "ds_write_b64 v6, v[108:109] offset:6144\n"
      "v_mfma_f32_16x16x32_bf16 v[168:171], a[116:119], v[136:139], v[168:171]\n"
      "v_fmac_f32_e64 v84, v178, v178\n"
      "v_cvt_pk_bf16_f32 v97, v98, v99\n"
      "v_mfma_f32_16x16x32_bf16 v[112:115], a[120:123], v[172:175], 0\n"
      "v_fmac_f32 v210, v130, v130\n"
      "v_mul_f32 v187, v122, v187\n"
      "v_mfma_f32_16x16x32_bf16 v[112:115], a[124:127], v[164:167], v[112:115]\n"
      "v_fmac_f32 v210, v131, v131\n"
      "v_mul_f32 v148, v122, v148\n"
      "v_mfma_f32_16x16x32_bf16 v[112:115], a[128:131], v[152:155], v[112:115]\n"
      "v_mul_f32 v200, v81, v200\n"
      "v_mul_f32 v185, v81, v185\n"
      "v_mfma_f32_16x16x32_bf16 v[112:115], a[132:135], v[220:223], v[112:115]\n"
      "v_mul_f32 v124, v122, v124\n"
      "v_fmac_f32 v210, v168, v168\n"
      "v_mfma_f32_16x16x32_bf16 v[112:115], a[136:139], v[160:163], v[112:115]\n"
      "v_fmac_f32 v210, v169, v169\n"
      "v_fmac_f32 v210, v170, v170\n"
      "v_mfma_f32_16x16x32_bf16 v[112:115], a[140:143], v[136:139], v[112:115]\n"
      "v_fmac_f32 v84, v179, v179\n"
      "v_mul_f32 v150, v122, v150\n"
      "v_mfma_f32_16x16x32_bf16 v[116:119], v[172:175], a[144:147], 0\n"
      "v_fmac_f32 v84, v202, v202\n"
      "v_mul_f32 v147, v81, v147\n"
      "v_mfma_f32_16x16x32_bf16 v[116:119], v[164:167], a[148:151], v[116:119]\n"
      "v_fmac_f32 v84, v203, v203\n"
      "v_fmac_f32 v210, v171, v171\n"
      "v_mfma_f32_16x16x32_bf16 v[116:119], v[152:155], a[152:155], v[116:119]\n"
      "s_or_b32 s95, s95, s48\n"
      "v_mul_f32 v146, v81, v146\n"
      "v_fmac_f32_e64 v84, v204, v204\n"
      "v_mfma_f32_16x16x32_bf16 v[116:119], v[220:223], a[156:159], v[116:119]\n"
      "ds_write_b128 v5, v[132:135] offset:12288\n"
      "v_mfma_f32_16x16x32_bf16 v[116:119], v[160:163], a[160:163], v[116:119]\n"
      "s_cmp_ge_u32 s95, 0x40000000\n"
      "s_cselect_b32 s94, -1, -2\n"
      "v_fmac_f32_e64 v210, v112, v112\n"
      "v_mfma_f32_16x16x32_bf16 v[116:119], v[136:139], a[164:167], v[116:119]\n"
      "v_mul_f32 v198, v81, v198\n"
      "v_fmac_f32 v210, v113, v113\n"
      "v_mfma_f32_16x16x32_bf16 v[108:111], v[172:175], a[168:171], 0\n"
      "v_fmac_f32 v210, v114, v114\n"
      "v_mul_f32 v145, v81, v145\n"
      "v_mfma_f32_16x16x32_bf16 v[108:111], v[164:167], a[172:175], v[108:111]\n"
      "v_mul_f32 v201, v81, v201\n"
      "v_fmac_f32 v84, v205, v205\n"
      "v_mfma_f32_16x16x32_bf16 v[108:111], v[152:155], a[176:179], v[108:111]\n"
      "v_cvt_pk_bf16_f32 v105, v106, v107\n"
      "v_mul_f32 v144, v81, v144\n"
      "v_mfma_f32_16x16x32_bf16 v[108:111], v[220:223], a[180:183], v[108:111]\n"
      "ds_read_b128 v[140:143], v1 offset:10240\n"
      "v_fmac_f32 v210, v115, v115\n"
      "v_mfma_f32_16x16x32_bf16 v[108:111], v[160:163], a[184:187], v[108:111]\n"
      "s_nop 0\n"
      "v_permlane16_swap_b32 v84, v210\n"
      "v_mfma_f32_16x16x32_bf16 v[108:111], v[136:139], a[188:191], v[108:111]\n"
      "v_mul_f32 v151, v122, v151\n"
      "v_add_f32 v84, v84, v210\n"
      "v_mfma_f32_16x16x32_bf16 v[132:135], v[172:175], a[192:195], 0\n"
      "v_mov_b32 v210, v84\n"
      "v_mul_f32 v188, v122, v188\n"
      "v_mfma_f32_16x16x32_bf16 v[132:135], v[164:167], a[196:199], v[132:135]\n"
      "v_mul_f32_e64 v186, v122, v186\n"
      "v_cvt_pk_bf16_f32 v196, v116, v117\n"
      "v_mfma_f32_16x16x32_bf16 v[132:135], v[152:155], a[200:203], v[132:135]\n"
      "v_permlane32_swap_b32 v84, v210\n"
      "v_mfma_f32_16x16x32_bf16 v[132:135], v[220:223], a[204:207], v[132:135]\n"
      "ds_read_b128 v[220:223], v1 offset:16384\n"
      "v_mul_f32 v125, v122, v125\n"
      "v_mfma_f32_16x16x32_bf16 v[132:135], v[160:163], a[208:211], v[132:135]\n"
      "v_mul_f32 v127, v122, v127\n"
      "v_cvt_pk_bf16_f32 v148, v148, v149\n"
      "v_mfma_f32_16x16x32_bf16 v[132:135], v[136:139], a[212:215], v[132:135]\n"
      "s_waitcnt lgkmcnt(1)\n"
      "v_add_f32 v84, v84, v210\n"
      "v_mul_f32 v126, v122, v126\n"
      "v_mfma_f32_16x16x32_bf16 v[136:139], a[0:3], v[216:219], 0\n"
      "v_rsq_f32_e64 v84, v84\n"
      "v_mfma_f32_16x16x32_bf16 v[136:139], a[4:7], v[206:209], v[136:139]\n"
      "v_cvt_pk_bf16_f32 v197, v118, v119\n"
      "v_mov_b32 v210, v84\n"
      "v_mfma_f32_16x16x32_bf16 v[136:139], a[8:11], v[100:103], v[136:139]\n"
      "s_nop 0\n"
      "v_permlane16_swap_b32_e64 v84, v210\n"
      "v_mfma_f32_16x16x32_bf16 v[136:139], a[12:15], v[140:143], v[136:139]\n"
      "ds_read_u16 v82, v11 offset:32\n"
      "v_mul_f32 v189, v122, v189\n"
      "v_mfma_f32_16x16x32_bf16 v[136:139], a[16:19], v[212:215], v[136:139]\n"
      "s_waitcnt lgkmcnt(1)\n"
      "v_mul_f32 v184, v81, v184\n"
      "v_mul_f32 v131, v210, v131\n"
      "v_mfma_f32_16x16x32_bf16 v[136:139], a[20:23], v[220:223], v[136:139]\n"
      "v_mul_f32 v170, v210, v170\n"
      "v_mul_f32 v171, v210, v171\n"
      "v_mfma_f32_16x16x32_bf16 v[152:155], a[24:27], v[216:219], 0\n"
      "v_cvt_pk_bf16_f32 v98, v132, v133\n"
      "v_cvt_pk_bf16_f32 v106, v108, v109\n"
      "v_mfma_f32_16x16x32_bf16 v[152:155], a[28:31], v[206:209], v[152:155]\n"
      "v_mul_f32 v84, s50, v84\n"
      "v_mul_f32 v199, v81, v199\n"
      "v_mfma_f32_16x16x32_bf16 v[152:155], a[32:35], v[100:103], v[152:155]\n"
      "v_fma_f32 v89, v136, v136, s51\n"
      "v_mul_f32_e64 v113, v210, v113\n"
      "v_mfma_f32_16x16x32_bf16 v[152:155], a[36:39], v[140:143], v[152:155]\n"
      "v_cvt_pk_bf16_f32 v198, v198, v199\n"
      "v_mul_f32 v169, v210, v169\n"
      "v_mfma_f32_16x16x32_bf16 v[152:155], a[40:43], v[212:215], v[152:155]\n"
      "v_cvt_pk_bf16_f32 v144, v144, v145\n"
      "v_fmac_f32 v89, v137, v137\n"
      "v_mfma_f32_16x16x32_bf16 v[152:155], a[44:47], v[220:223], v[152:155]\n"
      "v_mul_f32 v205, v84, v205\n"
      "v_mul_f32 v203, v84, v203\n"
      "v_mfma_f32_16x16x32_bf16 v[116:119], a[48:51], v[216:219], 0\n"
      "v_cvt_pk_bf16_f32 v149, v150, v151\n"
      "v_fmac_f32 v89, v138, v138\n"
      "v_mfma_f32_16x16x32_bf16 v[116:119], a[52:55], v[206:209], v[116:119]\n"
      "s_cmp_gt_u32 s42, s44\n"
      "s_cselect_b32 s95, s94, s95\n"
      "v_mul_f32 v192, v84, v192\n"
      "v_mfma_f32_16x16x32_bf16 v[116:119], a[56:59], v[100:103], v[116:119]\n"
      "v_fmac_f32 v89, v139, v139\n"
      "v_cvt_pk_bf16_f32 v150, v124, v125\n"
      "v_mfma_f32_16x16x32_bf16 v[116:119], a[60:63], v[140:143], v[116:119]\n"
      "v_fmac_f32 v89, v152, v152\n"
      "v_mul_f32_e64 v202, v84, v202\n"
      "v_mfma_f32_16x16x32_bf16 v[116:119], a[64:67], v[212:215], v[116:119]\n"
      "v_mul_f32 v182, v81, v182\n"
      "v_fmac_f32 v89, v153, v153\n"
      "v_mfma_f32_16x16x32_bf16 v[116:119], a[68:71], v[220:223], v[116:119]\n"
      "v_fmac_f32 v89, v154, v154\n"
      "v_mul_f32 v191, v84, v191\n"
      "v_mfma_f32_16x16x32_bf16 v[164:167], a[72:75], v[216:219], 0\n"
      "v_fmac_f32 v89, v155, v155\n"
      "v_mul_f32 v129, v210, v129\n"
      "v_mfma_f32_16x16x32_bf16 v[164:167], a[76:79], v[206:209], v[164:167]\n"
      "v_cvt_pk_bf16_f32 v151, v126, v127\n"
      "v_cvt_pk_bf16_f32 v99, v134, v135\n"
      "v_mfma_f32_16x16x32_bf16 v[164:167], a[80:83], v[100:103], v[164:167]\n"
      "v_cvt_pk_bf16_f32 v107, v110, v111\n"
      "v_fmac_f32_e64 v89, v116, v116\n"
      "v_mfma_f32_16x16x32_bf16 v[164:167], a[84:87], v[140:143], v[164:167]\n"
      "v_mul_f32 v130, v210, v130\n"
      "v_mul_f32 v178, v84, v178\n"
      "v_mfma_f32_16x16x32_bf16 v[164:167], a[88:91], v[212:215], v[164:167]\n"
      "v_mul_f32 v112, v210, v112\n"
      "v_mul_f32 v168, v210, v168\n"
      "v_mfma_f32_16x16x32_bf16 v[164:167], a[92:95], v[220:223], v[164:167]\n"
      "v_mul_f32_e64 v128, v210, v128\n"
      "v_cvt_pk_bf16_f32 v112, v112, v113\n"
      "v_mfma_f32_16x16x32_bf16 v[120:123], a[96:99], v[216:219], 0\n"
      "v_cvt_pk_bf16_f32 v186, v186, v187\n"
      "v_cvt_pk_bf16_f32 v202, v202, v203\n"
      "v_mfma_f32_16x16x32_bf16 v[120:123], a[100:103], v[206:209], v[120:123]\n"
      "s_cmp_ge_u32 s95, 0x40000000\n"
      "s_cselect_b64 vcc, -1, s[56:57]\n"
      "v_mul_f32 v183, v81, v183\n"
      "v_mfma_f32_16x16x32_bf16 v[120:123], a[104:107], v[100:103], v[120:123]\n"
      "s_and_b32 s5, s84, s8\n"
      "v_cvt_pk_bf16_f32 v199, v200, v201\n"
      "v_mul_f32_e64 v190, v84, v190\n"
      "v_mfma_f32_16x16x32_bf16 v[120:123], a[108:111], v[140:143], v[120:123]\n"
      "v_cvt_pk_bf16_f32 v201, v184, v185\n"
      "v_fma_f32 v85, v164, v164, s51\n"
      "v_mfma_f32_16x16x32_bf16 v[120:123], a[112:115], v[212:215], v[120:123]\n"
      "v_cvt_pk_bf16_f32 v128, v128, v129\n"
      "v_fmac_f32 v85, v165, v165\n"
      "v_mfma_f32_16x16x32_bf16 v[120:123], a[116:119], v[220:223], v[120:123]\n"
      "v_mul_f32 v193, v84, v193\n"
      "v_fmac_f32_e64 v85, v166, v166\n"
      "v_mfma_f32_16x16x32_bf16 v[132:135], a[120:123], v[216:219], 0\n"
      "v_fmac_f32_e64 v85, v167, v167\n"
      "v_cvt_pk_bf16_f32 v129, v130, v131\n"
      "v_mfma_f32_16x16x32_bf16 v[132:135], a[124:127], v[206:209], v[132:135]\n"
      "v_cvt_pk_bf16_f32 v200, v182, v183\n"
      "v_mul_f32 v115, v210, v115\n"
      "v_mfma_f32_16x16x32_bf16 v[132:135], a[128:131], v[100:103], v[132:135]\n"
      "v_fmac_f32 v89, v117, v117\n"
      "v_mul_f32_e64 v114, v210, v114\n"
      "v_mfma_f32_16x16x32_bf16 v[132:135], a[132:135], v[140:143], v[132:135]\n"
      "v_fmac_f32_e64 v85, v120, v120\n"
      "v_cvt_pk_bf16_f32 v145, v146, v147\n"
      "v_mfma_f32_16x16x32_bf16 v[132:135], a[136:139], v[212:215], v[132:135]\n"
      "ds_read_b128 v[160:163], v2 offset:18432\n"
      "v_fmac_f32 v89, v118, v118\n"
      "v_mfma_f32_16x16x32_bf16 v[132:135], a[140:143], v[220:223], v[132:135]\n"
      "v_cvt_pk_bf16_f32 v131, v170, v171\n"
      "v_fmac_f32 v85, v121, v121\n"
      "v_mfma_f32_16x16x32_bf16 v[182:185], v[216:219], a[144:147], 0\n"
      "v_cvt_pk_bf16_f32 v130, v168, v169\n"
      "v_cvt_pk_bf16_f32 v187, v188, v189\n"
      "v_mfma_f32_16x16x32_bf16 v[182:185], v[206:209], a[148:151], v[182:185]\n"
      "v_fmac_f32 v85, v122, v122\n"
      "v_fmac_f32 v85, v123, v123\n"
      "v_mfma_f32_16x16x32_bf16 v[182:185], v[100:103], a[152:155], v[182:185]\n"
      "v_fmac_f32 v89, v119, v119\n"
      "v_fmac_f32 v85, v132, v132\n"
      "v_mfma_f32_16x16x32_bf16 v[182:185], v[140:143], a[156:159], v[182:185]\n"
      "v_fmac_f32 v85, v133, v133\n"
      "v_fmac_f32 v85, v134, v134\n"
      "v_mfma_f32_16x16x32_bf16 v[182:185], v[212:215], a[160:163], v[182:185]\n"
      "v_fmac_f32 v85, v135, v135\n"
      "v_mul_f32 v176, v84, v176\n"
      "v_mfma_f32_16x16x32_bf16 v[182:185], v[220:223], a[164:167], v[182:185]\n"
      "v_permlane16_swap_b32_e64 v89, v85\n"
      "v_mfma_f32_16x16x32_bf16 v[124:127], v[216:219], a[168:171], 0\n"
      "ds_read_b128 v[168:171], v2 offset:21504\n"
      "v_cvt_pk_bf16_f32 v113, v114, v115\n"
      "v_mfma_f32_16x16x32_bf16 v[124:127], v[206:209], a[172:175], v[124:127]\n"
      "v_add_f32 v89, v89, v85\n"
      "v_mov_b32 v85, v89\n"
      "v_mfma_f32_16x16x32_bf16 v[124:127], v[100:103], a[176:179], v[124:127]\n"
      "s_nop 0\n"
      "v_permlane32_swap_b32 v89, v85\n"
      "v_mfma_f32_16x16x32_bf16 v[124:127], v[140:143], a[180:183], v[124:127]\n"
      "v_add_f32 v89, v89, v85\n"
      "v_cvt_pk_bf16_f32 v190, v190, v191\n"
      "v_mfma_f32_16x16x32_bf16 v[124:127], v[212:215], a[184:187], v[124:127]\n"
      "v_rsq_f32 v89, v89\n"
      "v_mfma_f32_16x16x32_bf16 v[124:127], v[220:223], a[188:191], v[124:127]\n"
      "v_mov_b32 v85, v89\n"
      "v_mul_f32 v177, v84, v177\n"
      "v_mfma_f32_16x16x32_bf16 v[108:111], v[216:219], a[192:195], 0\n"
      "ds_read_b128 v[216:219], v2 offset:24576\n"
      "v_mul_f32 v179, v84, v179\n"
      "v_mfma_f32_16x16x32_bf16 v[108:111], v[206:209], a[196:199], v[108:111]\n"
      "v_permlane16_swap_b32 v89, v85\n"
      "v_mfma_f32_16x16x32_bf16 v[108:111], v[100:103], a[200:203], v[108:111]\n"
      "v_cvt_pk_bf16_f32 v191, v192, v193\n"
      "v_mul_f32 v89, s50, v89\n"
      "v_mfma_f32_16x16x32_bf16 v[108:111], v[140:143], a[204:207], v[108:111]\n"
      "ds_read_b128 v[100:103], v2 offset:27648\n"
      "v_mfma_f32_16x16x32_bf16 v[108:111], v[212:215], a[208:211], v[108:111]\n"
      "s_waitcnt lgkmcnt(2)\n"
      "v_mul_f32 v137, v89, v137\n"
      "v_mul_f32 v122, v85, v122\n"
      "v_mfma_f32_16x16x32_bf16 v[108:111], v[220:223], a[212:215], v[108:111]\n"
      "ds_read_b128 v[212:215], v2 offset:30720\n"
      "v_cvt_pk_bf16_f32 v81, v126, v127\n"
      "v_mfma_f32_16x16x32_bf16 v[172:175], a[216:219], v[160:163], 0\n"
      "v_mul_f32 v164, v85, v164\n"
      "v_mul_f32_e64 v155, v89, v155\n"
      "v_mfma_f32_16x16x32_bf16 v[172:175], a[220:223], v[168:171], v[172:175]\n"
      "s_waitcnt lgkmcnt(2)\n"
      "v_cvt_pk_bf16_f32 v76, v182, v183\n"
      "v_cvt_pk_bf16_f32 v193, v178, v179\n"
      "v_mfma_f32_16x16x32_bf16 v[172:175], a[224:227], v[216:219], v[172:175]\n"
      "v_mul_f32 v154, v89, v154\n"
      "v_mul_f32 v153, v89, v153\n"
      "v_mfma_f32_16x16x32_bf16 v[220:223], a[240:243], v[160:163], 0\n"
      "ds_read_b128 v[206:209], v2 offset:33792\n"
      "v_mul_f32 v116, v89, v116\n"
      "v_mfma_f32_16x16x32_bf16 v[220:223], a[244:247], v[168:171], v[220:223]\n"
      "v_mul_f32 v204, v84, v204\n"
      "v_cvt_pk_bf16_f32 v84, v108, v109\n"
      "v_mfma_f32_16x16x32_bf16 v[220:223], a[248:251], v[216:219], v[220:223]\n"
      "v_mul_f32 v139, v89, v139\n"
      "v_mul_f32 v136, v89, v136\n"
      "v_mfma_f32_16x16x32_bf16 v[140:143], v[232:235], v[160:163], 0\n"
      "ds_read_u16 v78, v11\n"
      "v_mul_f32 v167, v85, v167\n"
      "v_mfma_f32_16x16x32_bf16 v[140:143], v[236:239], v[168:171], v[140:143]\n"
      "s_waitcnt lgkmcnt(2)\n"
      "v_mul_f32 v138, v89, v138\n"
      "v_mul_f32 v132, v85, v132\n"
      "v_mfma_f32_16x16x32_bf16 v[140:143], v[240:243], v[216:219], v[140:143]\n"
      "ds_read_b128 v[168:171], v10 offset:32\n"
      "v_mul_f32 v166, v85, v166\n"
      "v_mfma_f32_16x16x32_bf16 v[172:175], a[228:231], v[100:103], v[172:175]\n"
      "v_mul_f32 v121, v85, v121\n"
      "v_mul_f32 v120, v85, v120\n"
      "v_mfma_f32_16x16x32_bf16 v[172:175], a[232:235], v[212:215], v[172:175]\n"
      "s_waitcnt lgkmcnt(2)\n"
      "v_mul_f32_e64 v165, v85, v165\n"
      "v_cvt_pk_bf16_f32 v164, v164, v165\n"
      "v_mfma_f32_16x16x32_bf16 v[172:175], a[236:239], v[206:209], v[172:175]\n"
      "v_cvt_pk_bf16_f32 v136, v136, v137\n"
      "v_cvt_pk_bf16_f32 v137, v138, v139\n"
      "v_mfma_f32_16x16x32_bf16 v[220:223], a[252:255], v[100:103], v[220:223]\n"
      "v_cvt_pk_bf16_f32 v77, v184, v185\n"
      "v_mul_f32 v123, v85, v123\n"
      "v_mfma_f32_16x16x32_bf16 v[220:223], v[224:227], v[212:215], v[220:223]\n"
      "ds_read_b128 v[180:183], v2 offset:18688\n"
      "v_mul_f32 v133, v85, v133\n"
      "v_mfma_f32_16x16x32_bf16 v[220:223], v[228:231], v[206:209], v[220:223]\n"
      "ds_read_b64 v[114:115], v10 offset:24\n"
      "v_mul_f32 v135, v85, v135\n"
      "v_mfma_f32_16x16x32_bf16 v[140:143], v[244:247], v[100:103], v[140:143]\n"
      "v_cvt_pk_bf16_f32 v80, v124, v125\n"
      "v_mul_f32 v152, v89, v152\n"
      "v_mfma_f32_16x16x32_bf16 v[140:143], v[248:251], v[212:215], v[140:143]\n"
      "ds_read_b128 v[210:213], v2 offset:24832\n"
      "v_cvt_pk_bf16_f32 v165, v166, v167\n"
      "v_mfma_f32_16x16x32_bf16 v[140:143], v[252:255], v[206:209], v[140:143]\n"
      "v_cvt_pk_bf16_f32 v167, v122, v123\n"
      "v_cvt_pk_bf16_f32 v166, v120, v121\n"
      "v_mfma_f32_16x16x32_bf16 v[214:217], v[198:201], v[148:151], v[40:43]\n"
      "v_mul_f32 v134, v85, v134\n"
      "v_cvt_pk_bf16_f32 v85, v110, v111\n"
      "v_mfma_f32_16x16x32_bf16 v[120:123], v[128:131], v[148:151], v[44:47]\n"
      "ds_read_b128 v[100:103], v2 offset:21760\n"
      "v_cvt_pk_bf16_f32 v192, v176, v177\n"
      "v_mfma_f32_16x16x32_bf16 v[160:163], v[164:167], v[148:151], v[48:51]\n"
      "s_waitcnt lgkmcnt(2)\n"
      "v_cvt_pk_bf16_f32 v139, v154, v155\n"
      "v_cvt_pk_bf16_f32 v132, v132, v133\n"
      "v_mfma_f32_16x16x32_bf16 v[124:127], v[168:171], v[148:151], v[24:27]\n"
      "ds_read_u16 v86, v11 offset:64\n"
      "v_cvt_pk_bf16_f32 v133, v134, v135\n"
      "v_mfma_f32_16x16x16_bf16 v[214:217], v[144:145], v[186:187], v[214:217]\n"
      "v_mul_f32 v117, v89, v117\n"
      "v_mul_f32 v118, v89, v118\n"
      "v_fma_f32 v218, v172, v172, s52\n"
      "v_mfma_f32_16x16x16_bf16 v[120:123], v[112:113], v[186:187], v[120:123]\n"
      "v_mul_f32_e64 v119, v89, v119\n"
      "v_cvt_pk_bf16_f32 v203, v204, v205\n"
      "s_waitcnt vmcnt(1)\n"
      "v_or3_b32 v156, v156, v157, v158\n"
      "v_mfma_f32_16x16x16_bf16 v[160:163], v[132:133], v[186:187], v[160:163]\n"
      "v_exp_f32 v216, v216\n"
      "v_cvt_pk_bf16_f32 v116, v116, v117\n"
      "v_mfma_f32_16x16x16_bf16 v[124:127], v[114:115], v[186:187], v[124:127]\n"
      "v_add_u32 v12, 32, v12\n"
      "v_exp_f32 v121, v121\n"
      "v_mfma_f32_16x16x32_bf16 v[206:209], a[216:219], v[180:183], 0\n"
      "s_waitcnt lgkmcnt(1)\n"
      "v_exp_f32 v215, v215\n"
      "v_mfma_f32_16x16x32_bf16 v[206:209], a[220:223], v[100:103], v[206:209]\n"
      "v_exp_f32 v217, v217\n"
      "v_mfma_f32_16x16x32_bf16 v[206:209], a[224:227], v[210:213], v[206:209]\n"
      "v_exp_f32 v122, v122\n"
      "v_mfma_f32_16x16x32_bf16 v[176:179], a[240:243], v[180:183], 0\n"
      "s_add_u32 s43, s43, 1\n"
      "v_exp_f32 v161, v161\n"
      "v_mfma_f32_16x16x32_bf16 v[176:179], a[244:247], v[100:103], v[176:179]\n"
      "v_exp_f32_e64 v214, v214\n"
      "v_mfma_f32_16x16x32_bf16 v[176:179], a[248:251], v[210:213], v[176:179]\n"
      "v_exp_f32 v162, v162\n"
      "v_mfma_f32_16x16x32_bf16 v[108:111], v[232:235], v[180:183], 0\n"
      "v_exp_f32 v123, v123\n"
      "v_mfma_f32_16x16x32_bf16 v[108:111], v[236:239], v[100:103], v[108:111]\n"
      "v_cvt_pk_bf16_f32 v138, v152, v153\n"
      "v_cvt_pk_bf16_f32 v117, v118, v119\n"
      "v_mfma_f32_16x16x32_bf16 v[108:111], v[240:243], v[210:213], v[108:111]\n"
      "v_exp_f32_e64 v163, v163\n"
      "v_mfma_f32_16x16x32_bf16 v[210:213], v[198:201], v[190:193], v[52:55]\n"
      "v_cvt_pk_bf16_f32 v214, v214, v215\n"
      "v_cvt_pk_bf16_f32 v215, v216, v217\n"
      "v_mfma_f32_16x16x32_bf16 v[100:103], v[128:131], v[190:193], v[56:59]\n"
      "v_exp_f32 v124, v124\n"
      "v_mfma_f32_16x16x32_bf16 v[184:187], v[164:167], v[190:193], v[60:63]\n"
      "ds_read_b128 v[146:149], v2 offset:27904\n"
      "s_waitcnt vmcnt(0)\n"
      "v_or3_b32 v156, v156, v159, v90\n"
      "v_mfma_f32_16x16x32_bf16 v[180:183], v[168:171], v[190:193], v[26:29]\n"
      "v_exp_f32 v120, v120\n"
      "v_mfma_f32_16x16x16_bf16 v[210:213], v[144:145], v[202:203], v[210:213]\n"
      "v_exp_f32 v160, v160\n"
      "v_cvt_pk_bf16_f32 v90, v124, 0\n"
      "v_mfma_f32_16x16x16_bf16 v[100:103], v[112:113], v[202:203], v[100:103]\n"
      "v_cvt_pk_bf16_f32 v217, v122, v123\n"
      "v_cvt_pk_bf16_f32 v89, v162, v163\n"
      "v_cvt_pk_bf16_f32 v88, v160, v161\n"
      "v_mfma_f32_16x16x16_bf16 v[184:187], v[132:133], v[202:203], v[184:187]\n"
      "v_cvt_pk_bf16_f32 v216, v120, v121\n"
      "v_exp_f32 v213, v213\n"
      "v_mfma_f32_16x16x16_bf16 v[180:183], v[114:115], v[202:203], v[180:183]\n"
      "v_exp_f32 v212, v212\n"
      "v_exp_f32 v101, v101\n"
      "v_mfma_f32_16x16x32_bf16 v[150:153], v[32:35], v[214:217], 0\n"
      "v_exp_f32 v102, v102\n"
      "v_mfma_f32_16x16x32_bf16 v[150:153], v[32:35], v[88:91], v[150:153]\n"
      "v_exp_f32_e64 v211, v211\n"
      "v_mfma_f32_16x16x32_bf16 v[160:163], v[194:197], v[214:217], 0\n"
      "ds_write_b32 v31, v156 offset:4608\n"
      "v_mfma_f32_16x16x32_bf16 v[160:163], v[76:79], v[88:91], v[160:163]\n"
      "v_exp_f32_e64 v187, v187\n"
      "v_mfma_f32_16x16x32_bf16 v[202:205], v[104:107], v[214:217], 0\n"
      "v_exp_f32 v186, v186\n"
      "v_mfma_f32_16x16x32_bf16 v[202:205], v[80:83], v[88:91], v[202:205]\n"
      "v_exp_f32 v100, v100\n"
      "v_mfma_f32_16x16x32_bf16 v[156:159], v[96:99], v[214:217], 0\n"
      "s_waitcnt lgkmcnt(1)\n"
      "v_exp_f32 v210, v210\n"
      "v_mfma_f32_16x16x32_bf16 v[156:159], v[84:87], v[88:91], v[156:159]\n"
      "v_cvt_pk_bf16_f32 v93, v186, v187\n"
      "v_cvt_pk_bf16_f32 v210, v210, v211\n"
      "v_cvt_pk_bf16_f32 v211, v212, v213\n"
      "v_mfma_f32_16x16x32_bf16 v[190:193], v[198:201], v[136:139], v[64:67]\n"
      "v_exp_f32 v185, v185\n"
      "v_mfma_f32_16x16x32_bf16 v[214:217], v[128:131], v[136:139], v[68:71]\n"
      "v_exp_f32 v103, v103\n"
      "v_mfma_f32_16x16x32_bf16 v[152:155], v[164:167], v[136:139], v[72:75]\n"
      "v_exp_f32_e64 v184, v184\n"
      "v_mfma_f32_16x16x32_bf16 v[186:189], v[168:171], v[136:139], v[28:31]\n"
      "ds_read_b128 v[122:125], v2 offset:30976\n"
      "v_cvt_pk_bf16_f32 v212, v100, v101\n"
      "v_mfma_f32_16x16x16_bf16 v[190:193], v[144:145], v[116:117], v[190:193]\n"
      "v_cvt_pk_bf16_f32 v213, v102, v103\n"
      "v_exp_f32_e64 v180, v180\n"
      "v_mfma_f32_16x16x16_bf16 v[214:217], v[112:113], v[116:117], v[214:217]\n"
      "ds_read_b128 v[136:139], v2 offset:34048\n"
      "v_cvt_pk_bf16_f32 v92, v184, v185\n"
      "v_mfma_f32_16x16x16_bf16 v[152:155], v[132:133], v[116:117], v[152:155]\n"
      "v_cvt_pk_bf16_f32 v94, v180, 0\n"
      "v_rcp_f32 v150, v150\n"
      "v_mfma_f32_16x16x16_bf16 v[186:189], v[114:115], v[116:117], v[186:189]\n"
      "v_exp_f32 v191, v191\n"
      "v_exp_f32_e64 v190, v190\n"
      "v_exp_f32_e64 v192, v192\n"
      "v_pk_mul_f32 v[162:163], v[162:163], v[150:151] op_sel_hi:[1,0]\n"
      "v_pk_mul_f32 v[158:159], v[158:159], v[150:151] op_sel_hi:[1,0]\n"
      "v_pk_mul_f32 v[202:203], v[202:203], v[150:151] op_sel_hi:[1,0]\n"
      "v_pk_mul_f32 v[204:205], v[204:205], v[150:151] op_sel_hi:[1,0]\n"
      "v_pk_mul_f32 v[156:157], v[156:157], v[150:151] op_sel_hi:[1,0]\n"
      "v_pk_mul_f32 v[160:161], v[160:161], v[150:151] op_sel_hi:[1,0]\n"
      "v_mfma_f32_16x16x32_bf16 v[206:209], a[228:231], v[146:149], v[206:209]\n"
      "s_waitcnt lgkmcnt(1)\n"
      "v_cvt_pk_bf16_f32 v156, v156, v157\n"
      "v_cvt_pk_bf16_f32 v157, v158, v159\n"
      "v_mfma_f32_16x16x32_bf16 v[206:209], a[232:235], v[122:125], v[206:209]\n"
      "s_waitcnt lgkmcnt(0)\n"
      "v_exp_f32_e64 v217, v217\n"
      "v_mfma_f32_16x16x32_bf16 v[206:209], a[236:239], v[136:139], v[206:209]\n"
      "ds_read_b128 v[182:185], v7 offset:18432\n"
      "v_cvt_pk_bf16_f32 v160, v160, v161\n"
      "v_mfma_f32_16x16x32_bf16 v[176:179], a[252:255], v[146:149], v[176:179]\n"
      "v_exp_f32_e64 v193, v193\n"
      "v_mfma_f32_16x16x32_bf16 v[176:179], v[224:227], v[122:125], v[176:179]\n"
      "v_exp_f32_e64 v215, v215\n"
      "v_mfma_f32_16x16x32_bf16 v[176:179], v[228:231], v[136:139], v[176:179]\n"
      "v_exp_f32_e64 v214, v214\n"
      "v_mfma_f32_16x16x32_bf16 v[108:111], v[244:247], v[146:149], v[108:111]\n"
      "v_cvt_pk_bf16_f32 v161, v162, v163\n"
      "v_cvt_pk_bf16_f32 v163, v204, v205\n"
      "v_mfma_f32_16x16x32_bf16 v[108:111], v[248:251], v[122:125], v[108:111]\n"
      "ds_read_b128 v[148:151], v7 offset:22528\n"
      "v_cvt_pk_bf16_f32 v190, v190, v191\n"
      "v_mfma_f32_16x16x32_bf16 v[108:111], v[252:255], v[136:139], v[108:111]\n"
      "v_exp_f32_e64 v153, v153\n"
      "v_mfma_f32_16x16x32_bf16 v[116:119], v[32:35], v[210:213], 0\n"
      "ds_read_b64 v[188:189], v10 offset:16\n"
      "v_exp_f32 v216, v216\n"
      "v_mfma_f32_16x16x32_bf16 v[116:119], v[32:35], v[92:95], v[116:119]\n"
      "s_waitcnt lgkmcnt(2)\n"
      "v_cvt_pk_bf16_f32 v191, v192, v193\n"
      "v_cvt_pk_bf16_f32 v193, v216, v217\n"
      "v_mfma_f32_16x16x32_bf16 v[136:139], v[194:197], v[210:213], 0\n"
      "ds_read_b128 v[122:125], v10\n"
      "v_cvt_pk_bf16_f32 v162, v202, v203\n"
      "v_mfma_f32_16x16x32_bf16 v[136:139], v[76:79], v[92:95], v[136:139]\n"
      "buffer_store_dwordx4 v[182:185], v20, s[24:27], s46 offen sc1\n"
      "v_mfma_f32_16x16x32_bf16 v[100:103], v[104:107], v[210:213], 0\n"
      "v_exp_f32_e64 v152, v152\n"
      "v_mfma_f32_16x16x32_bf16 v[100:103], v[80:83], v[92:95], v[100:103]\n"
      "ds_write_b128 v3, v[160:163]\n"
      "v_mfma_f32_16x16x32_bf16 v[202:205], v[96:99], v[210:213], 0\n"
      "v_exp_f32 v186, v186\n"
      "v_mfma_f32_16x16x32_bf16 v[202:205], v[84:87], v[92:95], v[202:205]\n"
      "s_waitcnt lgkmcnt(1)\n"
      "v_rcp_f32 v116, v116\n"
      "v_mfma_f32_16x16x32_bf16 v[210:213], v[122:125], v[198:201], v[30:33]\n"
      "v_exp_f32 v155, v155\n"
      "v_mfma_f32_16x16x32_bf16 v[158:161], v[122:125], v[128:131], v[30:33]\n"
      "ds_read_b128 v[118:121], v7 offset:26624\n"
      "v_cvt_pk_bf16_f32 v88, v152, v153\n"
      "v_mfma_f32_16x16x32_bf16 v[198:201], v[122:125], v[164:167], v[30:33]\n"
      "ds_read_b128 v[162:165], v2 offset:22016\n"
      "v_mul_f32_e64 v219, v173, v173\n"
      "v_mfma_f32_16x16x32_bf16 v[180:183], v[122:125], v[168:171], v[30:33]\n"
      "ds_read_b128 v[122:125], v2 offset:25088\n"
      "v_fma_f32 v152, v206, v206, s52\n"
      "v_mfma_f32_16x16x16_bf16 v[210:213], v[188:189], v[144:145], v[210:213]\n"
      "v_exp_f32 v154, v154\n"
      "v_mul_f32 v153, v207, v207\n"
      "v_mfma_f32_16x16x16_bf16 v[158:161], v[188:189], v[112:113], v[158:161]\n"
      "ds_read_b128 v[128:131], v2 offset:18944\n"
      "v_cvt_pk_bf16_f32 v90, v186, 0\n"
      "v_mfma_f32_16x16x16_bf16 v[198:201], v[188:189], v[132:133], v[198:201]\n"
      "buffer_store_dwordx4 v[148:151], v21, s[24:27], s46 offen sc1\n"
      "v_pk_mul_f32 v[204:205], v[204:205], v[116:117] op_sel_hi:[1,0]\n"
      "v_pk_mul_f32 v[202:203], v[202:203], v[116:117] op_sel_hi:[1,0]\n"
      "v_pk_fma_f32 v[152:153], v[208:209], v[208:209], v[152:153]\n"
      "v_pk_fma_f32 v[218:219], v[174:175], v[174:175], v[218:219]\n"
      "v_pk_fma_f32 v[152:153], v[176:177], v[176:177], v[152:153]\n"
      "v_pk_mul_f32 v[100:101], v[100:101], v[116:117] op_sel_hi:[1,0]\n"
      "v_pk_mul_f32 v[138:139], v[138:139], v[116:117] op_sel_hi:[1,0]\n"
      "v_pk_mul_f32 v[136:137], v[136:137], v[116:117] op_sel_hi:[1,0]\n"
      "v_pk_mul_f32 v[102:103], v[102:103], v[116:117] op_sel_hi:[1,0]\n"
      "v_cvt_pk_bf16_f32 v136, v136, v137\n"
      "v_pk_fma_f32 v[218:219], v[220:221], v[220:221], v[218:219]\n"
      "v_pk_fma_f32 v[218:219], v[222:223], v[222:223], v[218:219]\n"
      "v_mfma_f32_16x16x16_bf16 v[180:183], v[188:189], v[114:115], v[180:183]\n"
      "v_cvt_pk_bf16_f32 v137, v138, v139\n"
      "v_cvt_pk_bf16_f32 v138, v100, v101\n"
      "s_waitcnt lgkmcnt(0)\n"
      "v_mfma_f32_16x16x32_bf16 v[132:135], a[216:219], v[128:131], 0\n"
      "ds_read_b128 v[186:189], v7 offset:34816\n"
      "v_cvt_pk_bf16_f32 v139, v102, v103\n"
      "v_mfma_f32_16x16x32_bf16 v[132:135], a[220:223], v[162:165], v[132:135]\n"
      "ds_write_b128 v3, v[136:139] offset:256\n"
      "v_pk_fma_f32 v[152:153], v[178:179], v[178:179], v[152:153]\n"
      "v_mfma_f32_16x16x32_bf16 v[132:135], a[224:227], v[122:125], v[132:135]\n"
      "buffer_store_dwordx4 v[118:121], v22, s[24:27], s46 offen sc1\n"
      "s_nop 1\n"
      "v_mfma_f32_16x16x32_bf16 v[118:121], a[240:243], v[128:131], 0\n"
      "ds_read_b128 v[182:185], v7 offset:30720\n"
      "v_cndmask_b32 v210, v210, v158, s[58:59]\n"
      "v_mfma_f32_16x16x32_bf16 v[118:121], a[244:247], v[162:165], v[118:121]\n"
      "v_cndmask_b32 v210, v210, v198, s[60:61]\n"
      "v_cvt_pk_bf16_f32 v89, v154, v155\n"
      "v_mfma_f32_16x16x32_bf16 v[118:121], a[248:251], v[122:125], v[118:121]\n"
      "ds_write_b64 v4, v[156:157]\n"
      "v_mfma_f32_16x16x32_bf16 v[168:171], v[232:235], v[128:131], 0\n"
      "v_cvt_pk_bf16_f32 v202, v202, v203\n"
      "v_cvt_pk_bf16_f32 v192, v214, v215\n"
      "v_mfma_f32_16x16x32_bf16 v[168:171], v[236:239], v[162:165], v[168:171]\n"
      "ds_read_b128 v[100:103], v2 offset:34304\n"
      "v_cndmask_b32 v210, v210, v180, s[62:63]\n"
      "v_mfma_f32_16x16x32_bf16 v[168:171], v[240:243], v[122:125], v[168:171]\n"
      "ds_read_b128 v[214:217], v2 offset:28160\n"
      "v_cvt_pk_bf16_f32 v203, v204, v205\n"
      "v_mfma_f32_16x16x32_bf16 v[162:165], v[32:35], v[190:193], 0\n"
      "s_waitcnt lgkmcnt(3)\n"
      "v_exp_f32 v210, v210\n"
      "v_mfma_f32_16x16x32_bf16 v[162:165], v[32:35], v[88:91], v[162:165]\n"
      "ds_write_b64 v4, v[202:203] offset:256\n"
      "v_mfma_f32_16x16x32_bf16 v[156:159], v[194:197], v[190:193], 0\n"
      "ds_read_b128 v[202:205], v2 offset:31232\n"
      "v_cvt_pk_bf16_f32 v210, v210, v210\n"
      "v_mfma_f32_16x16x32_bf16 v[156:159], v[76:79], v[88:91], v[156:159]\n"
      "buffer_store_dwordx4 v[182:185], v23, s[24:27], s46 offen sc1\n"
      "v_pk_fma_f32 v[152:153], v[108:109], v[108:109], v[152:153]\n"
      "v_pk_fma_f32 v[152:153], v[110:111], v[110:111], v[152:153]\n"
      "v_pk_fma_f32 v[218:219], v[140:141], v[140:141], v[218:219]\n"
      "v_mfma_f32_16x16x32_bf16 v[128:131], v[104:107], v[190:193], 0\n"
      "v_rcp_f32 v162, v162\n"
      "v_mfma_f32_16x16x32_bf16 v[128:131], v[80:83], v[88:91], v[128:131]\n"
      "ds_write_b16 v13, v210\n"
      "v_mfma_f32_16x16x32_bf16 v[122:125], v[96:99], v[190:193], 0\n"
      "v_fmac_f32 v218, v142, v142\n"
      "v_add_f32 v152, v152, v153\n"
      "v_mfma_f32_16x16x32_bf16 v[122:125], v[84:87], v[88:91], v[122:125]\n"
      "s_waitcnt lgkmcnt(1)\n"
      "v_fmac_f32 v219, v143, v143\n"
      "v_mul_f32 v157, v162, v157\n"
      "v_mfma_f32_16x16x32_bf16 v[132:135], a[228:231], v[214:217], v[132:135]\n"
      "ds_read_b128 v[144:147], v14\n"
      "v_mul_f32 v156, v162, v156\n"
      "v_mfma_f32_16x16x32_bf16 v[132:135], a[232:235], v[202:205], v[132:135]\n"
      "ds_read_b128 v[198:201], v14 offset:64\n"
      "v_mul_f32 v159, v162, v159\n"
      "v_mfma_f32_16x16x32_bf16 v[132:135], a[236:239], v[100:103], v[132:135]\n"
      "v_mul_f32 v158, v162, v158\n"
      "v_cvt_pk_bf16_f32 v156, v156, v157\n"
      "v_mfma_f32_16x16x32_bf16 v[118:121], a[252:255], v[214:217], v[118:121]\n"
      "v_mul_f32 v128, v162, v128\n"
      "v_cvt_pk_bf16_f32 v157, v158, v159\n"
      "v_mfma_f32_16x16x32_bf16 v[118:121], v[224:227], v[202:205], v[118:121]\n"
      "v_mul_f32 v130, v162, v130\n"
      "v_mul_f32 v131, v162, v131\n"
      "v_mfma_f32_16x16x32_bf16 v[118:121], v[228:231], v[100:103], v[118:121]\n"
      "v_fma_f32 v164, v132, v132, s52\n"
      "v_add_f32_e64 v218, v218, v219\n"
      "v_mfma_f32_16x16x32_bf16 v[168:171], v[244:247], v[214:217], v[168:171]\n"
      "v_mul_f32_e64 v129, v162, v129\n"
      "v_mul_f32 v165, v133, v133\n"
      "v_mfma_f32_16x16x32_bf16 v[168:171], v[248:251], v[202:205], v[168:171]\n"
      "s_waitcnt lgkmcnt(0)\n"
      "v_cvt_pk_bf16_f32 v158, v128, v129\n"
      "v_cvt_pk_bf16_f32 v159, v130, v131\n"
      "v_mfma_f32_16x16x32_bf16 v[168:171], v[252:255], v[100:103], v[168:171]\n"
      "ds_write_b128 v3, v[156:159] offset:512\n"
      "v_pk_fma_f32 v[164:165], v[134:135], v[134:135], v[164:165]\n"
      "v_pk_fma_f32 v[164:165], v[118:119], v[118:119], v[164:165]\n"
      "v_pk_mul_f32 v[122:123], v[122:123], v[162:163] op_sel_hi:[1,0]\n"
      "v_permlane16_swap_b32 v218, v152\n"
      "v_add_f32 v218, v218, v152\n"
      "v_cvt_pk_bf16_f32 v122, v122, v123\n"
      "v_pk_fma_f32 v[164:165], v[120:121], v[120:121], v[164:165]\n"
      "v_pk_fma_f32 v[164:165], v[168:169], v[168:169], v[164:165]\n"
      "v_pk_mul_f32 v[124:125], v[124:125], v[162:163] op_sel_hi:[1,0]\n"
      "v_pk_fma_f32 v[164:165], v[170:171], v[170:171], v[164:165]\n"
      "v_add_f32 v164, v164, v165\n"
      "v_mov_b32 v152, v164\n"
      "v_cvt_pk_bf16_f32 v123, v124, v125\n"
      "s_and_b32 s86, s86, -4\n"
      "ds_write_b64 v4, v[122:123] offset:512\n"
      "v_permlane16_swap_b32 v164, v152\n"
      "v_add_f32 v164, v164, v152\n"
      "s_nop 1\n"
      "v_permlane32_swap_b32 v218, v164\n"
      "v_add_f32 v218, v218, v164\n"
      "v_mfma_f32_16x16x32_bf16 v[162:165], v[144:147], v[32:35], 0\n"
      "ds_write_b32 v31, v218\n"
      "buffer_store_dwordx4 v[186:189], v25, s[24:27], s46 offen sc1\n"
      "v_mfma_f32_16x16x32_bf16 v[162:165], v[198:201], v[32:35], v[162:165]\n"
      "s_add_u32 s42, s42, 1\n"
      "s_waitcnt lgkmcnt(0)\n"
      "s_barrier\n"
      "s_cbranch_vccz L_loop1_%=\n"
      "s_branch L_disp1_%=\n"
      "s_nop 7\n"
      "s_nop 7\n"
      "L_max1_%=:\n"
      "v_mfma_f32_16x16x32_bf16 v[152:155], v[144:147], v[194:197], 0\n"
      "ds_read_b128 v[186:189], v0 offset:18432\n"
      "s_add_u32 m0, s64, 0\n"
      "v_mfma_f32_16x16x32_bf16 v[152:155], v[198:201], v[76:79], v[152:155]\n"
      "ds_read_b128 v[128:131], v9\n"
      "v_mfma_f32_16x16x32_bf16 v[210:213], v[144:147], v[104:107], 0\n"
      "ds_read_b128 v[214:217], v1 offset:30720\n"
      "ds_read_b128 v[202:205], v1 offset:18432\n"
      "v_mfma_f32_16x16x32_bf16 v[210:213], v[198:201], v[80:83], v[210:213]\n"
      "buffer_load_dwordx4 v16, s[16:19], s84 offen  lds\n"
      "v_mfma_f32_16x16x32_bf16 v[180:183], v[144:147], v[96:99], 0\n"
      "ds_read_b128 v[190:193], v1 offset:24576\n"
      "v_readlane_b32 s45, v37, s43\n"
      "v_mfma_f32_16x16x32_bf16 v[180:183], v[198:201], v[84:87], v[180:183]\n"
      "ds_read_b128 v[144:147], v0 offset:24576\n"
      "s_waitcnt lgkmcnt(5)\n"
      "v_mfma_f32_16x16x32_bf16 v[136:139], a[0:3], v[186:189], 0\n"
      "ds_read_b128 v[114:117], v0 offset:26624\n"
      "v_cndmask_b32 v152, v152, v210, s[58:59]\n"
      "v_mfma_f32_16x16x32_bf16 v[122:125], a[24:27], v[186:189], 0\n"
      "s_waitcnt lgkmcnt(5)\n"
      "v_add_f32 v130, v130, v131\n"
      "v_add_f32 v128, v128, v129\n"
      "v_mfma_f32_16x16x32_bf16 v[154:157], a[48:51], v[186:189], 0\n"
      "ds_read_b128 v[100:103], v0 offset:30720\n"
      "v_add_f32_e64 v128, v128, v130\n"
      "v_mfma_f32_16x16x32_bf16 v[182:185], a[72:75], v[186:189], 0\n"
      "s_add_u32 m0, s64, 4096\n"
      "v_rsq_f32 v128, v128\n"
      "v_mfma_f32_16x16x32_bf16 v[198:201], a[96:99], v[186:189], 0\n"
      "s_waitcnt lgkmcnt(3)\n"
      "v_mul_f32 v128, 0x415db3d7, v128\n"
      "v_mov_b32_e64 v130, v128\n"
      "v_mfma_f32_16x16x32_bf16 v[148:151], a[120:123], v[186:189], 0\n"
      "buffer_load_dwordx4 v17, s[16:19], s84 offen  lds\n"
      "v_mfma_f32_16x16x32_bf16 v[136:139], a[4:7], v[202:205], v[136:139]\n"
      "s_waitcnt lgkmcnt(1)\n"
      "v_permlane32_swap_b32 v128, v130\n"
      "v_mfma_f32_16x16x32_bf16 v[136:139], a[8:11], v[144:147], v[136:139]\n"
      "ds_read_b128 v[210:213], v9 offset:4608\n"
      "v_mov_b32 v129, v128\n"
      "v_mfma_f32_16x16x32_bf16 v[122:125], a[28:31], v[202:205], v[122:125]\n"
      "s_nop 0\n"
      "v_permlane16_swap_b32 v128, v129\n"
      "v_mfma_f32_16x16x32_bf16 v[122:125], a[32:35], v[144:147], v[122:125]\n"
      "v_mul_f32 v220, v128, v220\n"
      "v_mul_f32 v223, v128, v223\n"
      "v_mfma_f32_16x16x32_bf16 v[154:157], a[52:55], v[202:205], v[154:157]\n"
      "v_mul_f32 v173, v128, v173\n"
      "v_mul_f32_e64 v221, v128, v221\n"
      "v_mfma_f32_16x16x32_bf16 v[154:157], a[56:59], v[144:147], v[154:157]\n"
      "s_add_u32 m0, s64, 8192\n"
      "v_mul_f32 v172, v128, v172\n"
      "v_mul_f32 v141, v128, v141\n"
      "v_mfma_f32_16x16x32_bf16 v[182:185], a[76:79], v[202:205], v[182:185]\n"
      "v_mul_f32 v177, v129, v177\n"
      "v_mul_f32 v110, v129, v110\n"
      "v_mfma_f32_16x16x32_bf16 v[182:185], a[80:83], v[144:147], v[182:185]\n"
      "buffer_load_dwordx4 v18, s[16:19], s84 offen  lds\n"
      "v_mfma_f32_16x16x32_bf16 v[198:201], a[100:103], v[202:205], v[198:201]\n"
      "v_mul_f32_e64 v176, v129, v176\n"
      "v_cvt_pk_bf16_f32 v172, v172, v173\n"
      "v_mfma_f32_16x16x32_bf16 v[198:201], a[104:107], v[144:147], v[198:201]\n"
      "s_add_u32 m0, s64, 12288\n"
      "v_mul_f32 v175, v128, v175\n"
      "v_mul_f32 v168, v130, v168\n"
      "v_mfma_f32_16x16x32_bf16 v[148:151], a[124:127], v[202:205], v[148:151]\n"
      "s_waitcnt lgkmcnt(0)\n"
      "v_mul_f32 v132, v130, v132\n"
      "v_or3_b32 v210, v210, v211, v212\n"
      "v_mfma_f32_16x16x32_bf16 v[148:151], a[128:131], v[144:147], v[148:151]\n"
      "v_mul_f32 v133, v130, v133\n"
      "v_mul_f32 v222, v128, v222\n"
      "v_mul_f32_e64 v111, v129, v111\n"
      "v_mfma_f32_16x16x32_bf16 v[136:139], a[12:15], v[190:193], v[136:139]\n"
      "buffer_load_dwordx4 v19, s[16:19], s84 offen  lds\n"
      "v_mfma_f32_16x16x32_bf16 v[136:139], a[16:19], v[100:103], v[136:139]\n"
      "v_mul_f32 v170, v130, v170\n"
      "v_mul_f32 v109, v129, v109\n"
      "v_mfma_f32_16x16x32_bf16 v[136:139], a[20:23], v[214:217], v[136:139]\n"
      "s_mov_b32 m0, s6\n"
      "v_mul_f32 v120, v130, v120\n"
      "v_mul_f32 v209, v129, v209\n"
      "v_mfma_f32_16x16x32_bf16 v[122:125], a[36:39], v[190:193], v[122:125]\n"
      "ds_read_b128 v[164:167], v0 offset:32768\n"
      "v_mul_f32 v179, v129, v179\n"
      "v_mfma_f32_16x16x32_bf16 v[122:125], a[40:43], v[100:103], v[122:125]\n"
      "v_mul_f32 v174, v128, v174\n"
      "v_mul_f32 v207, v129, v207\n"
      "v_mfma_f32_16x16x32_bf16 v[122:125], a[44:47], v[214:217], v[122:125]\n"
      "v_mul_f32_e64 v119, v130, v119\n"
      "v_readlane_b32 s46, v37, s42\n"
      "v_mfma_f32_16x16x32_bf16 v[154:157], a[60:63], v[190:193], v[154:157]\n"
      "v_mul_f32 v206, v129, v206\n"
      "v_cvt_pk_bf16_f32 v173, v174, v175\n"
      "v_mfma_f32_16x16x32_bf16 v[154:157], a[64:67], v[100:103], v[154:157]\n"
      "s_or_b32 s95, s45, s46\n"
      "v_mul_f32 v140, v128, v140\n"
      "v_mul_f32 v121, v130, v121\n"
      "v_mfma_f32_16x16x32_bf16 v[154:157], a[68:71], v[214:217], v[154:157]\n"
      "v_mul_f32_e64 v169, v130, v169\n"
      "v_fma_f32 v153, v136, v136, s51\n"
      "v_mfma_f32_16x16x32_bf16 v[182:185], a[84:87], v[190:193], v[182:185]\n"
      "v_cvt_pk_bf16_f32 v174, v220, v221\n"
      "v_mul_f32_e64 v108, v129, v108\n"
      "v_mfma_f32_16x16x32_bf16 v[182:185], a[88:91], v[100:103], v[182:185]\n"
      "buffer_load_dwordx4 v15, s[0:3], s5 offen  lds\n"
      "v_mfma_f32_16x16x32_bf16 v[182:185], a[92:95], v[214:217], v[182:185]\n"
      "v_mul_f32_e64 v208, v129, v208\n"
      "v_mov_b32_dpp v162, v162 quad_perm:[0,0,0,0] row_mask:0xf bank_mask:0xf\n"
      "v_mfma_f32_16x16x32_bf16 v[198:201], a[108:111], v[190:193], v[198:201]\n"
      "v_cvt_pk_bf16_f32 v108, v108, v109\n"
      "v_cvt_pk_bf16_f32 v175, v222, v223\n"
      "v_mfma_f32_16x16x32_bf16 v[198:201], a[112:115], v[100:103], v[198:201]\n"
      "ds_write_b128 v5, v[172:175] offset:18432\n"
      "v_mfma_f32_16x16x32_bf16 v[198:201], a[116:119], v[214:217], v[198:201]\n"
      "ds_read_b128 v[172:175], v1 offset:20480\n"
      "v_fmac_f32_e64 v153, v137, v137\n"
      "v_mfma_f32_16x16x32_bf16 v[148:151], a[132:135], v[190:193], v[148:151]\n"
      "v_fmac_f32 v153, v138, v138\n"
      "v_fma_f32 v163, v182, v182, s51\n"
      "v_mfma_f32_16x16x32_bf16 v[148:151], a[136:139], v[100:103], v[148:151]\n"
      "v_fmac_f32 v153, v139, v139\n"
      "v_cvt_pk_bf16_f32 v140, v140, v141\n"
      "v_mfma_f32_16x16x32_bf16 v[148:151], a[140:143], v[214:217], v[148:151]\n"
      "buffer_load_dwordx4 v[158:161], v36, s[28:31], s86 offen\n"
      "v_mfma_f32_16x16x32_bf16 v[194:197], v[186:189], a[144:147], 0\n"
      "v_cvt_pk_bf16_f32 v206, v206, v207\n"
      "v_mul_f32 v143, v128, v143\n"
      "v_mfma_f32_16x16x32_bf16 v[194:197], v[202:205], a[148:151], v[194:197]\n"
      "v_cvt_pk_bf16_f32 v207, v208, v209\n"
      "v_fmac_f32 v153, v122, v122\n"
      "v_mfma_f32_16x16x32_bf16 v[194:197], v[144:147], a[152:155], v[194:197]\n"
      "ds_read_b128 v[220:223], v0 offset:22528\n"
      "v_mul_f32 v118, v130, v118\n"
      "v_mfma_f32_16x16x32_bf16 v[194:197], v[190:193], a[156:159], v[194:197]\n"
      "v_mul_f32 v178, v129, v178\n"
      "v_mul_f32 v142, v128, v142\n"
      "v_mfma_f32_16x16x32_bf16 v[194:197], v[100:103], a[160:163], v[194:197]\n"
      "v_mul_f32 v171, v130, v171\n"
      "v_fmac_f32 v153, v123, v123\n"
      "v_mfma_f32_16x16x32_bf16 v[194:197], v[214:217], a[164:167], v[194:197]\n"
      "v_fmac_f32 v163, v183, v183\n"
      "v_fmac_f32 v153, v124, v124\n"
      "v_mfma_f32_16x16x32_bf16 v[104:107], v[186:189], a[168:171], 0\n"
      "v_fmac_f32 v163, v184, v184\n"
      "v_cvt_pk_bf16_f32 v168, v168, v169\n"
      "v_mfma_f32_16x16x32_bf16 v[104:107], v[202:205], a[172:175], v[104:107]\n"
      "v_cvt_pk_bf16_f32 v141, v142, v143\n"
      "v_cvt_pk_bf16_f32 v209, v178, v179\n"
      "v_mfma_f32_16x16x32_bf16 v[104:107], v[144:147], a[176:179], v[104:107]\n"
      "v_cvt_pk_bf16_f32 v109, v110, v111\n"
      "v_fmac_f32 v163, v185, v185\n"
      "v_mfma_f32_16x16x32_bf16 v[104:107], v[190:193], a[180:183], v[104:107]\n"
      "ds_read_b128 v[126:129], v1 offset:26624\n"
      "v_fmac_f32 v153, v125, v125\n"
      "v_mfma_f32_16x16x32_bf16 v[104:107], v[100:103], a[184:187], v[104:107]\n"
      "ds_read_b128 v[110:113], v0 offset:20480\n"
      "v_fmac_f32 v163, v198, v198\n"
      "v_mfma_f32_16x16x32_bf16 v[104:107], v[214:217], a[188:191], v[104:107]\n"
      "v_fmac_f32 v153, v154, v154\n"
      "v_cvt_pk_bf16_f32 v194, v194, v195\n"
      "v_mfma_f32_16x16x32_bf16 v[96:99], v[186:189], a[192:195], 0\n"
      "v_cvt_pk_bf16_f32 v169, v170, v171\n"
      "v_fmac_f32_e64 v163, v199, v199\n"
      "v_mfma_f32_16x16x32_bf16 v[96:99], v[202:205], a[196:199], v[96:99]\n"
      "ds_read_b128 v[202:205], v1 offset:32768\n"
      "v_cndmask_b32 v152, v152, v180, s[60:61]\n"
      "v_mfma_f32_16x16x32_bf16 v[96:99], v[144:147], a[200:203], v[96:99]\n"
      "v_rcp_f32_e64 v162, v162\n"
      "v_mfma_f32_16x16x32_bf16 v[96:99], v[190:193], a[204:207], v[96:99]\n"
      "ds_write_b64 v6, v[140:141] offset:18432\n"
      "v_mfma_f32_16x16x32_bf16 v[96:99], v[100:103], a[208:211], v[96:99]\n"
      "s_waitcnt lgkmcnt(2)\n"
      "v_fmac_f32 v163, v200, v200\n"
      "v_cvt_pk_bf16_f32 v132, v132, v133\n"
      "v_mfma_f32_16x16x32_bf16 v[96:99], v[214:217], a[212:215], v[96:99]\n"
      "buffer_load_dword v93, v29, s[28:31], s86 offen\n"
      "v_mfma_f32_16x16x32_bf16 v[216:219], a[0:3], v[110:113], 0\n"
      "v_fmac_f32 v163, v201, v201\n"
      "v_bitop3_b32 v210, v210, v213, s33 bitop3:0xa8\n"
      "v_mfma_f32_16x16x32_bf16 v[216:219], a[4:7], v[172:175], v[216:219]\n"
      "v_mul_f32 v152, v162, v152\n"
      "v_fmac_f32_e64 v163, v148, v148\n"
      "v_mfma_f32_16x16x32_bf16 v[216:219], a[8:11], v[114:117], v[216:219]\n"
      "v_fmac_f32_e64 v163, v149, v149\n"
      "v_cvt_pk_bf16_f32 v208, v176, v177\n"
      "v_mfma_f32_16x16x32_bf16 v[216:219], a[12:15], v[126:129], v[216:219]\n"
      "v_cvt_pk_bf16_f32 v96, v96, v97\n"
      "v_readlane_b32 s47, v38, s43\n"
      "v_mfma_f32_16x16x32_bf16 v[216:219], a[16:19], v[164:167], v[216:219]\n"
      "s_waitcnt lgkmcnt(0)\n"
      "v_fmac_f32 v153, v155, v155\n"
      "v_cvt_pk_bf16_f32 v97, v98, v99\n"
      "v_mfma_f32_16x16x32_bf16 v[216:219], a[20:23], v[202:205], v[216:219]\n"
      "v_fmac_f32_e64 v153, v156, v156\n"
      "v_cvt_pk_bf16_f32 v104, v104, v105\n"
      "v_mfma_f32_16x16x32_bf16 v[190:193], a[24:27], v[110:113], 0\n"
      "ds_read_b128 v[144:147], v1 offset:22528\n"
      "v_mul_f32_e64 v134, v130, v134\n"
      "v_mfma_f32_16x16x32_bf16 v[190:193], a[28:31], v[172:175], v[190:193]\n"
      "ds_read_b128 v[212:215], v1 offset:34816\n"
      "v_mul_f32_e64 v135, v130, v135\n"
      "v_mfma_f32_16x16x32_bf16 v[190:193], a[32:35], v[114:117], v[190:193]\n"
      "ds_write_b128 v5, v[206:209] offset:24576\n"
      "v_mfma_f32_16x16x32_bf16 v[190:193], a[36:39], v[126:129], v[190:193]\n"
      "ds_read_b128 v[186:189], v0 offset:28672\n"
      "v_fma_f32 v180, v216, v216, s51\n"
      "v_mfma_f32_16x16x32_bf16 v[190:193], a[40:43], v[164:167], v[190:193]\n"
      "v_fmac_f32 v163, v150, v150\n"
      "v_fmac_f32 v180, v217, v217\n"
      "v_mfma_f32_16x16x32_bf16 v[190:193], a[44:47], v[202:205], v[190:193]\n"
      "v_fmac_f32 v163, v151, v151\n"
      "v_fmac_f32 v153, v157, v157\n"
      "v_mfma_f32_16x16x32_bf16 v[206:209], a[48:51], v[110:113], 0\n"
      "v_fmac_f32 v180, v218, v218\n"
      "v_cvt_pk_bf16_f32 v152, v152, v152\n"
      "v_mfma_f32_16x16x32_bf16 v[206:209], a[52:55], v[172:175], v[206:209]\n"
      "v_permlane16_swap_b32 v153, v163\n"
      "v_mfma_f32_16x16x32_bf16 v[206:209], a[56:59], v[114:117], v[206:209]\n"
      "ds_write_b16 v12, v152\n"
      "v_mfma_f32_16x16x32_bf16 v[206:209], a[60:63], v[126:129], v[206:209]\n"
      "v_add_f32_e64 v153, v153, v163\n"
      "v_cvt_pk_bf16_f32 v195, v196, v197\n"
      "v_mfma_f32_16x16x32_bf16 v[206:209], a[64:67], v[164:167], v[206:209]\n"
      "ds_read_u16 v86, v11 offset:1232\n"
      "v_cvt_pk_bf16_f32 v133, v134, v135\n"
      "v_mfma_f32_16x16x32_bf16 v[206:209], a[68:71], v[202:205], v[206:209]\n"
      "ds_write_b64 v6, v[108:109] offset:24576\n"
      "v_mfma_f32_16x16x32_bf16 v[100:103], a[72:75], v[110:113], 0\n"
      "v_mov_b32 v163, v153\n"
      "v_fmac_f32 v180, v219, v219\n"
      "v_mfma_f32_16x16x32_bf16 v[100:103], a[76:79], v[172:175], v[100:103]\n"
      "v_permlane32_swap_b32 v153, v163\n"
      "v_mfma_f32_16x16x32_bf16 v[100:103], a[80:83], v[114:117], v[100:103]\n"
      "s_or_b32 s95, s95, s48\n"
      "v_fmac_f32_e64 v180, v190, v190\n"
      "v_cvt_pk_bf16_f32 v105, v106, v107\n"
      "v_mfma_f32_16x16x32_bf16 v[100:103], a[84:87], v[126:129], v[100:103]\n"
      "v_add_f32 v153, v153, v163\n"
      "v_fmac_f32 v180, v191, v191\n"
      "v_mfma_f32_16x16x32_bf16 v[100:103], a[88:91], v[164:167], v[100:103]\n"
      "v_rsq_f32_e64 v153, v153\n"
      "v_mfma_f32_16x16x32_bf16 v[100:103], a[92:95], v[202:205], v[100:103]\n"
      "s_cmp_ge_u32 s95, 0x40000000\n"
      "s_cselect_b32 s94, -1, -2\n"
      "v_mov_b32 v163, v153\n"
      "v_mfma_f32_16x16x32_bf16 v[140:143], a[96:99], v[110:113], 0\n"
      "s_nop 0\n"
      "v_permlane16_swap_b32 v153, v163\n"
      "v_mfma_f32_16x16x32_bf16 v[140:143], a[100:103], v[172:175], v[140:143]\n"
      "v_fmac_f32 v180, v192, v192\n"
      "v_fma_f32 v108, v100, v100, s51\n"
      "v_mfma_f32_16x16x32_bf16 v[140:143], a[104:107], v[114:117], v[140:143]\n"
      "v_fmac_f32 v108, v101, v101\n"
      "v_cvt_pk_bf16_f32 v135, v120, v121\n"
      "v_mfma_f32_16x16x32_bf16 v[140:143], a[108:111], v[126:129], v[140:143]\n"
      "v_mul_f32 v199, v163, v199\n"
      "v_mul_f32_e64 v185, v163, v185\n"
      "v_mfma_f32_16x16x32_bf16 v[140:143], a[112:115], v[164:167], v[140:143]\n"
      "v_cvt_pk_bf16_f32 v134, v118, v119\n"
      "v_mul_f32 v153, s50, v153\n"
      "v_mfma_f32_16x16x32_bf16 v[140:143], a[116:119], v[202:205], v[140:143]\n"
      "v_mul_f32 v155, v153, v155\n"
      "v_fmac_f32 v180, v193, v193\n"
      "v_mfma_f32_16x16x32_bf16 v[118:121], a[120:123], v[110:113], 0\n"
      "v_mul_f32 v137, v153, v137\n"
      "v_fmac_f32 v108, v102, v102\n"
      "v_mfma_f32_16x16x32_bf16 v[118:121], a[124:127], v[172:175], v[118:121]\n"
      "v_fmac_f32 v108, v103, v103\n"
      "v_mul_f32_e64 v154, v153, v154\n"
      "v_mfma_f32_16x16x32_bf16 v[118:121], a[128:131], v[114:117], v[118:121]\n"
      "ds_write_b128 v5, v[132:135] offset:30720\n"
      "v_pk_mul_f32 v[182:183], v[182:183], v[162:163] op_sel:[0,1]\n"
      "v_mfma_f32_16x16x32_bf16 v[118:121], a[132:135], v[126:129], v[118:121]\n"
      "v_fmac_f32 v108, v140, v140\n"
      "v_mul_f32 v148, v163, v148\n"
      "v_mfma_f32_16x16x32_bf16 v[118:121], a[136:139], v[164:167], v[118:121]\n"
      "v_fmac_f32 v108, v141, v141\n"
      "v_fmac_f32 v180, v206, v206\n"
      "v_mfma_f32_16x16x32_bf16 v[118:121], a[140:143], v[202:205], v[118:121]\n"
      "ds_write_b64 v6, v[168:169] offset:30720\n"
      "v_mfma_f32_16x16x32_bf16 v[132:135], v[110:113], a[144:147], 0\n"
      "v_mul_f32 v184, v163, v184\n"
      "v_mul_f32 v157, v153, v157\n"
      "v_mfma_f32_16x16x32_bf16 v[132:135], v[172:175], a[148:151], v[132:135]\n"
      "v_fmac_f32 v108, v142, v142\n"
      "v_mul_f32 v124, v153, v124\n"
      "v_mfma_f32_16x16x32_bf16 v[132:135], v[114:117], a[152:155], v[132:135]\n"
      "v_mul_f32 v151, v163, v151\n"
      "v_mul_f32 v198, v163, v198\n"
      "v_mfma_f32_16x16x32_bf16 v[132:135], v[126:129], a[156:159], v[132:135]\n"
      "v_fmac_f32 v180, v207, v207\n"
      "v_fmac_f32 v108, v143, v143\n"
      "v_mfma_f32_16x16x32_bf16 v[132:135], v[164:167], a[160:163], v[132:135]\n"
      "v_mul_f32 v149, v163, v149\n"
      "v_mul_f32 v139, v153, v139\n"
      "v_mfma_f32_16x16x32_bf16 v[132:135], v[202:205], a[164:167], v[132:135]\n"
      "v_mul_f32 v156, v153, v156\n"
      "v_mul_f32 v136, v153, v136\n"
      "v_mfma_f32_16x16x32_bf16 v[176:179], v[110:113], a[168:171], 0\n"
      "v_fmac_f32_e64 v180, v208, v208\n"
      "v_cvt_pk_bf16_f32 v148, v148, v149\n"
      "v_mfma_f32_16x16x32_bf16 v[176:179], v[172:175], a[172:175], v[176:179]\n"
      "v_mul_f32 v201, v163, v201\n"
      "v_mul_f32 v150, v163, v150\n"
      "v_mfma_f32_16x16x32_bf16 v[176:179], v[114:117], a[176:179], v[176:179]\n"
      "v_cvt_pk_bf16_f32 v196, v132, v133\n"
      "v_fmac_f32 v180, v209, v209\n"
      "v_mfma_f32_16x16x32_bf16 v[176:179], v[126:129], a[180:183], v[176:179]\n"
      "v_fmac_f32 v108, v118, v118\n"
      "v_mul_f32_e64 v138, v153, v138\n"
      "v_mfma_f32_16x16x32_bf16 v[176:179], v[164:167], a[184:187], v[176:179]\n"
      "v_cvt_pk_bf16_f32 v182, v182, v183\n"
      "v_fmac_f32_e64 v108, v119, v119\n"
      "v_mfma_f32_16x16x32_bf16 v[176:179], v[202:205], a[188:191], v[176:179]\n"
      "ds_read_b128 v[130:133], v1 offset:28672\n"
      "v_fmac_f32 v108, v120, v120\n"
      "v_mfma_f32_16x16x32_bf16 v[168:171], v[110:113], a[192:195], 0\n"
      "v_fmac_f32 v108, v121, v121\n"
      "v_cvt_pk_bf16_f32 v197, v134, v135\n"
      "v_mfma_f32_16x16x32_bf16 v[168:171], v[172:175], a[196:199], v[168:171]\n"
      "v_permlane16_swap_b32 v180, v108\n"
      "v_mfma_f32_16x16x32_bf16 v[168:171], v[114:117], a[200:203], v[168:171]\n"
      "ds_read_b128 v[172:175], v0 offset:34816\n"
      "v_mul_f32 v200, v163, v200\n"
      "v_mfma_f32_16x16x32_bf16 v[168:171], v[126:129], a[204:207], v[168:171]\n"
      "v_add_f32 v180, v180, v108\n"
      "v_mov_b32 v108, v180\n"
      "v_mfma_f32_16x16x32_bf16 v[168:171], v[164:167], a[208:211], v[168:171]\n"
      "s_cmp_gt_u32 s42, s44\n"
      "s_cselect_b32 s95, s94, s95\n"
      "v_mul_f32 v123, v153, v123\n"
      "v_mfma_f32_16x16x32_bf16 v[168:171], v[202:205], a[212:215], v[168:171]\n"
      "v_permlane32_swap_b32 v180, v108\n"
      "v_mfma_f32_16x16x32_bf16 v[202:205], a[0:3], v[220:223], 0\n"
      "s_waitcnt lgkmcnt(1)\n"
      "v_add_f32 v180, v180, v108\n"
      "v_cvt_pk_bf16_f32 v154, v154, v155\n"
      "v_mfma_f32_16x16x32_bf16 v[202:205], a[4:7], v[144:147], v[202:205]\n"
      "v_rsq_f32 v180, v180\n"
      "v_mfma_f32_16x16x32_bf16 v[202:205], a[8:11], v[186:189], v[202:205]\n"
      "v_mov_b32 v108, v180\n"
      "v_cvt_pk_bf16_f32 v136, v136, v137\n"
      "v_mfma_f32_16x16x32_bf16 v[202:205], a[12:15], v[130:133], v[202:205]\n"
      "s_waitcnt lgkmcnt(0)\n"
      "v_permlane16_swap_b32 v180, v108\n"
      "v_mfma_f32_16x16x32_bf16 v[202:205], a[16:19], v[172:175], v[202:205]\n"
      "v_mul_f32_e64 v119, v108, v119\n"
      "v_cvt_pk_bf16_f32 v155, v156, v157\n"
      "v_mfma_f32_16x16x32_bf16 v[202:205], a[20:23], v[212:215], v[202:205]\n"
      "v_cvt_pk_bf16_f32 v183, v184, v185\n"
      "v_cvt_pk_bf16_f32 v149, v150, v151\n"
      "v_mfma_f32_16x16x32_bf16 v[162:165], a[24:27], v[220:223], 0\n"
      "v_mul_f32 v125, v153, v125\n"
      "v_mul_f32 v180, s50, v180\n"
      "v_mfma_f32_16x16x32_bf16 v[162:165], a[28:31], v[144:147], v[162:165]\n"
      "v_mul_f32 v100, v108, v100\n"
      "v_mul_f32 v216, v180, v216\n"
      "v_mfma_f32_16x16x32_bf16 v[162:165], a[32:35], v[186:189], v[162:165]\n"
      "v_mul_f32 v207, v180, v207\n"
      "v_mul_f32 v101, v108, v101\n"
      "v_mfma_f32_16x16x32_bf16 v[162:165], a[36:39], v[130:133], v[162:165]\n"
      "v_fma_f32 v157, v202, v202, s51\n"
      "v_mul_f32 v118, v108, v118\n"
      "v_mfma_f32_16x16x32_bf16 v[162:165], a[40:43], v[172:175], v[162:165]\n"
      "v_mul_f32 v217, v180, v217\n"
      "v_mul_f32_e64 v191, v180, v191\n"
      "v_mfma_f32_16x16x32_bf16 v[162:165], a[44:47], v[212:215], v[162:165]\n"
      "v_mul_f32 v219, v180, v219\n"
      "v_fmac_f32 v157, v203, v203\n"
      "v_mfma_f32_16x16x32_bf16 v[126:129], a[48:51], v[220:223], 0\n"
      "v_cvt_pk_bf16_f32 v137, v138, v139\n"
      "v_fmac_f32 v157, v204, v204\n"
      "v_mfma_f32_16x16x32_bf16 v[126:129], a[52:55], v[144:147], v[126:129]\n"
      "v_fmac_f32 v157, v205, v205\n"
      "v_mul_f32 v206, v180, v206\n"
      "v_mfma_f32_16x16x32_bf16 v[126:129], a[56:59], v[186:189], v[126:129]\n"
      "v_mul_f32 v122, v153, v122\n"
      "v_mul_f32 v218, v180, v218\n"
      "v_mfma_f32_16x16x32_bf16 v[126:129], a[60:63], v[130:133], v[126:129]\n"
      "v_cvt_pk_bf16_f32 v98, v168, v169\n"
      "v_mul_f32 v208, v180, v208\n"
      "v_mfma_f32_16x16x32_bf16 v[126:129], a[64:67], v[172:175], v[126:129]\n"
      "v_cvt_pk_bf16_f32 v206, v206, v207\n"
      "v_cvt_pk_bf16_f32 v216, v216, v217\n"
      "v_mfma_f32_16x16x32_bf16 v[126:129], a[68:71], v[212:215], v[126:129]\n"
      "v_mul_f32 v192, v180, v192\n"
      "v_mul_f32 v140, v108, v140\n"
      "v_mfma_f32_16x16x32_bf16 v[112:115], a[72:75], v[220:223], 0\n"
      "v_cvt_pk_bf16_f32 v100, v100, v101\n"
      "v_cvt_pk_bf16_f32 v184, v198, v199\n"
      "v_mfma_f32_16x16x32_bf16 v[112:115], a[76:79], v[144:147], v[112:115]\n"
      "v_fmac_f32 v157, v162, v162\n"
      "v_fmac_f32 v157, v163, v163\n"
      "v_mfma_f32_16x16x32_bf16 v[112:115], a[80:83], v[186:189], v[112:115]\n"
      "v_cvt_pk_bf16_f32 v185, v200, v201\n"
      "v_cmp_ne_u32 s[56:57], 0, v210\n"
      "v_mfma_f32_16x16x32_bf16 v[112:115], a[84:87], v[130:133], v[112:115]\n"
      "v_mul_f32 v102, v108, v102\n"
      "v_mul_f32 v143, v108, v143\n"
      "v_mfma_f32_16x16x32_bf16 v[112:115], a[88:91], v[172:175], v[112:115]\n"
      "v_mul_f32_e64 v120, v108, v120\n"
      "v_cvt_pk_bf16_f32 v217, v218, v219\n"
      "v_mfma_f32_16x16x32_bf16 v[112:115], a[92:95], v[212:215], v[112:115]\n"
      "v_mul_f32 v190, v180, v190\n"
      "v_fmac_f32 v157, v164, v164\n"
      "v_mfma_f32_16x16x32_bf16 v[150:153], a[96:99], v[220:223], 0\n"
      "v_cvt_pk_bf16_f32 v138, v122, v123\n"
      "v_cvt_pk_bf16_f32 v107, v178, v179\n"
      "v_mfma_f32_16x16x32_bf16 v[150:153], a[100:103], v[144:147], v[150:153]\n"
      "v_fmac_f32 v157, v165, v165\n"
      "v_mul_f32 v103, v108, v103\n"
      "v_mfma_f32_16x16x32_bf16 v[150:153], a[104:107], v[186:189], v[150:153]\n"
      "s_and_b32 s47, s47, -4\n"
      "v_fmac_f32 v157, v126, v126\n"
      "v_fma_f32 v210, v112, v112, s51\n"
      "v_mfma_f32_16x16x32_bf16 v[150:153], a[108:111], v[130:133], v[150:153]\n"
      "v_mul_f32 v142, v108, v142\n"
      "v_cvt_pk_bf16_f32 v118, v118, v119\n"
      "v_mfma_f32_16x16x32_bf16 v[150:153], a[112:115], v[172:175], v[150:153]\n"
      "v_mul_f32 v141, v108, v141\n"
      "v_fmac_f32_e64 v210, v113, v113\n"
      "v_mfma_f32_16x16x32_bf16 v[150:153], a[116:119], v[212:215], v[150:153]\n"
      "v_fmac_f32 v210, v114, v114\n"
      "v_cvt_pk_bf16_f32 v101, v102, v103\n"
      "v_mfma_f32_16x16x32_bf16 v[166:169], a[120:123], v[220:223], 0\n"
      "v_fmac_f32 v157, v127, v127\n"
      "v_cvt_pk_bf16_f32 v103, v142, v143\n"
      "v_mfma_f32_16x16x32_bf16 v[166:169], a[124:127], v[144:147], v[166:169]\n"
      "v_cvt_pk_bf16_f32 v102, v140, v141\n"
      "v_fmac_f32_e64 v157, v128, v128\n"
      "v_mfma_f32_16x16x32_bf16 v[166:169], a[128:131], v[186:189], v[166:169]\n"
      "v_cvt_pk_bf16_f32 v139, v124, v125\n"
      "v_fmac_f32_e64 v210, v115, v115\n"
      "v_mfma_f32_16x16x32_bf16 v[166:169], a[132:135], v[130:133], v[166:169]\n"
      "v_cvt_pk_bf16_f32 v106, v176, v177\n"
      "v_fmac_f32 v210, v150, v150\n"
      "v_mfma_f32_16x16x32_bf16 v[166:169], a[136:139], v[172:175], v[166:169]\n"
      "v_cvt_pk_bf16_f32 v99, v170, v171\n"
      "v_fmac_f32 v210, v151, v151\n"
      "v_mfma_f32_16x16x32_bf16 v[166:169], a[140:143], v[212:215], v[166:169]\n"
      "v_fmac_f32 v157, v129, v129\n"
      "v_mul_f32 v121, v108, v121\n"
      "v_mfma_f32_16x16x32_bf16 v[140:143], v[220:223], a[144:147], 0\n"
      "v_fmac_f32 v210, v152, v152\n"
      "v_fmac_f32 v210, v153, v153\n"
      "v_mfma_f32_16x16x32_bf16 v[140:143], v[144:147], a[148:151], v[140:143]\n"
      "v_mul_f32 v193, v180, v193\n"
      "v_mul_f32 v209, v180, v209\n"
      "v_mfma_f32_16x16x32_bf16 v[140:143], v[186:189], a[152:155], v[140:143]\n"
      "ds_read_b128 v[178:181], v2 offset:3072\n"
      "v_cvt_pk_bf16_f32 v219, v192, v193\n"
      "v_mfma_f32_16x16x32_bf16 v[140:143], v[130:133], a[156:159], v[140:143]\n"
      "v_fmac_f32 v210, v166, v166\n"
      "v_fmac_f32 v210, v167, v167\n"
      "v_mfma_f32_16x16x32_bf16 v[140:143], v[172:175], a[160:163], v[140:143]\n"
      "v_cvt_pk_bf16_f32 v218, v190, v191\n"
      "v_fmac_f32 v210, v168, v168\n"
      "v_mfma_f32_16x16x32_bf16 v[140:143], v[212:215], a[164:167], v[140:143]\n"
      "v_cvt_pk_bf16_f32 v119, v120, v121\n"
      "v_fmac_f32 v210, v169, v169\n"
      "v_mfma_f32_16x16x32_bf16 v[198:201], v[220:223], a[168:171], 0\n"
      "s_nop 0\n"
      "v_permlane16_swap_b32 v157, v210\n"
      "v_mfma_f32_16x16x32_bf16 v[198:201], v[144:147], a[172:175], v[198:201]\n"
      "v_add_f32 v157, v157, v210\n"
      "v_mov_b32 v210, v157\n"
      "v_cvt_pk_bf16_f32 v76, v140, v141\n"
      "v_mfma_f32_16x16x32_bf16 v[198:201], v[186:189], a[176:179], v[198:201]\n"
      "v_permlane32_swap_b32_e64 v157, v210\n"
      "v_mfma_f32_16x16x32_bf16 v[198:201], v[130:133], a[180:183], v[198:201]\n"
      "ds_read_b128 v[120:123], v2\n"
      "v_add_f32 v157, v157, v210\n"
      "v_mfma_f32_16x16x32_bf16 v[198:201], v[172:175], a[184:187], v[198:201]\n"
      "v_rsq_f32 v157, v157\n"
      "v_mfma_f32_16x16x32_bf16 v[198:201], v[212:215], a[188:191], v[198:201]\n"
      "ds_read_u16 v82, v11 offset:1200\n"
      "v_mov_b32 v210, v157\n"
      "v_mfma_f32_16x16x32_bf16 v[190:193], v[220:223], a[192:195], 0\n"
      "s_nop 0\n"
      "v_permlane16_swap_b32_e64 v157, v210\n"
      "v_mfma_f32_16x16x32_bf16 v[190:193], v[144:147], a[196:199], v[190:193]\n"
      "ds_read_b128 v[108:111], v2 offset:6144\n"
      "v_mul_f32 v151, v210, v151\n"
      "v_mfma_f32_16x16x32_bf16 v[190:193], v[186:189], a[200:203], v[190:193]\n"
      "v_mul_f32 v112, v210, v112\n"
      "v_mul_f32 v115, v210, v115\n"
      "v_mfma_f32_16x16x32_bf16 v[190:193], v[130:133], a[204:207], v[190:193]\n"
      "ds_read_b128 v[186:189], v2 offset:9216\n"
      "v_mul_f32 v114, v210, v114\n"
      "v_mfma_f32_16x16x32_bf16 v[190:193], v[172:175], a[208:211], v[190:193]\n"
      "s_waitcnt lgkmcnt(3)\n"
      "v_cvt_pk_bf16_f32 v77, v142, v143\n"
      "v_mul_f32 v113, v210, v113\n"
      "v_mfma_f32_16x16x32_bf16 v[190:193], v[212:215], a[212:215], v[190:193]\n"
      "ds_read_u16 v78, v11 offset:1168\n"
      "v_mul_f32 v153, v210, v153\n"
      "v_mfma_f32_16x16x32_bf16 v[172:175], a[216:219], v[120:123], 0\n"
      "ds_read_b64 v[116:117], v10 offset:1192\n"
      "v_mul_f32 v169, v210, v169\n"
      "v_mfma_f32_16x16x32_bf16 v[172:175], a[220:223], v[178:181], v[172:175]\n"
      "s_waitcnt lgkmcnt(3)\n"
      "v_mul_f32 v166, v210, v166\n"
      "v_cvt_pk_bf16_f32 v207, v208, v209\n"
      "v_mfma_f32_16x16x32_bf16 v[172:175], a[224:227], v[108:111], v[172:175]\n"
      "ds_read_b128 v[132:135], v10 offset:1200\n"
      "v_cvt_pk_bf16_f32 v85, v192, v193\n"
      "v_mfma_f32_16x16x32_bf16 v[220:223], a[240:243], v[120:123], 0\n"
      "v_mul_f32 v152, v210, v152\n"
      "v_mul_f32 v167, v210, v167\n"
      "v_mfma_f32_16x16x32_bf16 v[220:223], a[244:247], v[178:181], v[220:223]\n"
      "v_cvt_pk_bf16_f32 v166, v166, v167\n"
      "v_cvt_pk_bf16_f32 v112, v112, v113\n"
      "v_mfma_f32_16x16x32_bf16 v[220:223], a[248:251], v[108:111], v[220:223]\n"
      "v_mul_f32 v150, v210, v150\n"
      "v_mul_f32 v157, s50, v157\n"
      "v_mfma_f32_16x16x32_bf16 v[140:143], v[232:235], v[120:123], 0\n"
      "v_mul_f32_e64 v164, v157, v164\n"
      "v_cvt_pk_bf16_f32 v113, v114, v115\n"
      "v_mfma_f32_16x16x32_bf16 v[140:143], v[236:239], v[178:181], v[140:143]\n"
      "v_cvt_pk_bf16_f32 v80, v198, v199\n"
      "v_mul_f32_e64 v168, v210, v168\n"
      "v_mfma_f32_16x16x32_bf16 v[140:143], v[240:243], v[108:111], v[140:143]\n"
      "v_cvt_pk_bf16_f32 v114, v150, v151\n"
      "v_cvt_pk_bf16_f32 v115, v152, v153\n"
      "v_mfma_f32_16x16x32_bf16 v[150:153], v[182:185], v[136:139], v[40:43]\n"
      "ds_read_b128 v[144:147], v2 offset:12288\n"
      "v_mul_f32_e64 v128, v157, v128\n"
      "v_mfma_f32_16x16x32_bf16 v[210:213], v[100:103], v[136:139], v[44:47]\n"
      "s_waitcnt lgkmcnt(1)\n"
      "v_cvt_pk_bf16_f32 v167, v168, v169\n"
      "v_mul_f32 v126, v157, v126\n"
      "v_mfma_f32_16x16x32_bf16 v[168:171], v[112:115], v[136:139], v[48:51]\n"
      "ds_read_b128 v[178:181], v2 offset:15360\n"
      "v_cvt_pk_bf16_f32 v81, v200, v201\n"
      "v_mfma_f32_16x16x32_bf16 v[120:123], v[132:135], v[136:139], v[24:27]\n"
      "v_mul_f32 v202, v157, v202\n"
      "v_mul_f32 v204, v157, v204\n"
      "v_mfma_f32_16x16x16_bf16 v[150:153], v[148:149], v[154:155], v[150:153]\n"
      "v_mul_f32 v127, v157, v127\n"
      "v_mul_f32 v165, v157, v165\n"
      "v_cvt_pk_bf16_f32 v84, v190, v191\n"
      "v_mfma_f32_16x16x16_bf16 v[210:213], v[118:119], v[154:155], v[210:213]\n"
      "v_mul_f32 v203, v157, v203\n"
      "v_cvt_pk_bf16_f32 v202, v202, v203\n"
      "v_mul_f32 v205, v157, v205\n"
      "v_mfma_f32_16x16x16_bf16 v[168:171], v[166:167], v[154:155], v[168:171]\n"
      "v_cvt_pk_bf16_f32 v203, v204, v205\n"
      "v_cvt_pk_bf16_f32 v126, v126, v127\n"
      "v_max3_f32 v139, v150, v151, v152\n"
      "v_mfma_f32_16x16x16_bf16 v[120:123], v[116:117], v[154:155], v[120:123]\n"
      "v_max3_f32 v201, v153, v210, v211\n"
      "v_mul_f32 v129, v157, v129\n"
      "v_cvt_pk_bf16_f32 v205, v164, v165\n"
      "v_mfma_f32_16x16x32_bf16 v[172:175], a[228:231], v[186:189], v[172:175]\n"
      "s_waitcnt lgkmcnt(0)\n"
      "v_max3_f32 v201, v201, v168, v169\n"
      "v_max3_f32 v139, v139, v212, v213\n"
      "v_mfma_f32_16x16x32_bf16 v[172:175], a[232:235], v[144:147], v[172:175]\n"
      "v_cvt_pk_bf16_f32 v127, v128, v129\n"
      "v_max3_f32 v139, v139, v170, v171\n"
      "v_mfma_f32_16x16x32_bf16 v[172:175], a[236:239], v[178:181], v[172:175]\n"
      "ds_read_b128 v[128:131], v2 offset:256\n"
      "v_max3_f32 v139, v139, v201, v120\n"
      "v_mfma_f32_16x16x32_bf16 v[220:223], a[252:255], v[186:189], v[220:223]\n"
      "ds_read_b128 v[198:201], v2 offset:6400\n"
      "v_mov_b32 v122, v139\n"
      "v_mfma_f32_16x16x32_bf16 v[220:223], v[224:227], v[144:147], v[220:223]\n"
      "s_nop 0\n"
      "v_permlane16_swap_b32_e64 v139, v122\n"
      "v_mfma_f32_16x16x32_bf16 v[220:223], v[228:231], v[178:181], v[220:223]\n"
      "v_max_f32 v139, v139, v122\n"
      "v_mov_b32 v122, v139\n"
      "v_mfma_f32_16x16x32_bf16 v[140:143], v[244:247], v[186:189], v[140:143]\n"
      "ds_read_b128 v[190:193], v2 offset:3328\n"
      "v_permlane32_swap_b32 v139, v122\n"
      "v_max_f32 v139, v139, v122\n"
      "v_sub_f32_e64 v120, v120, v139\n"
      "v_pk_add_f32 v[212:213], v[212:213], v[138:139] op_sel:[0,1] neg_lo:[0,1] neg_hi:[0,1]\n"
      "v_pk_add_f32 v[150:151], v[150:151], v[138:139] op_sel:[0,1] neg_lo:[0,1] neg_hi:[0,1]\n"
      "v_pk_add_f32 v[210:211], v[210:211], v[138:139] op_sel:[0,1] neg_lo:[0,1] neg_hi:[0,1]\n"
      "v_pk_add_f32 v[170:171], v[170:171], v[138:139] op_sel:[0,1] neg_lo:[0,1] neg_hi:[0,1]\n"
      "v_pk_mul_f32 v[162:163], v[162:163], v[156:157] op_sel:[0,1]\n"
      "v_pk_add_f32 v[168:169], v[168:169], v[138:139] op_sel:[0,1] neg_lo:[0,1] neg_hi:[0,1]\n"
      "v_pk_add_f32 v[152:153], v[152:153], v[138:139] op_sel:[0,1] neg_lo:[0,1] neg_hi:[0,1]\n"
      "v_mfma_f32_16x16x32_bf16 v[140:143], v[248:251], v[144:147], v[140:143]\n"
      "v_exp_f32 v168, v168\n"
      "v_mfma_f32_16x16x32_bf16 v[140:143], v[252:255], v[178:181], v[140:143]\n"
      "v_exp_f32 v170, v170\n"
      "v_mfma_f32_16x16x32_bf16 v[144:147], v[182:185], v[216:219], v[52:55]\n"
      "v_exp_f32 v152, v152\n"
      "v_mfma_f32_16x16x32_bf16 v[122:125], v[100:103], v[216:219], v[56:59]\n"
      "v_exp_f32 v169, v169\n"
      "v_mfma_f32_16x16x32_bf16 v[136:139], v[112:115], v[216:219], v[60:63]\n"
      "v_exp_f32 v171, v171\n"
      "v_mfma_f32_16x16x32_bf16 v[154:157], v[132:135], v[216:219], v[26:29]\n"
      "s_waitcnt lgkmcnt(0)\n"
      "v_exp_f32 v212, v212\n"
      "v_mfma_f32_16x16x16_bf16 v[144:147], v[148:149], v[206:207], v[144:147]\n"
      "v_exp_f32 v213, v213\n"
      "v_cvt_pk_bf16_f32 v204, v162, v163\n"
      "v_mfma_f32_16x16x16_bf16 v[122:125], v[118:119], v[206:207], v[122:125]\n"
      "v_exp_f32_e64 v153, v153\n"
      "v_cvt_pk_bf16_f32 v88, v168, v169\n"
      "v_mfma_f32_16x16x16_bf16 v[136:139], v[166:167], v[206:207], v[136:139]\n"
      "v_exp_f32 v151, v151\n"
      "v_exp_f32 v150, v150\n"
      "v_mfma_f32_16x16x16_bf16 v[154:157], v[116:117], v[206:207], v[154:157]\n"
      "v_cvt_pk_bf16_f32 v150, v150, v151\n"
      "v_exp_f32_e64 v210, v210\n"
      "v_mfma_f32_16x16x32_bf16 v[206:209], a[216:219], v[128:131], 0\n"
      "v_max3_f32 v216, v144, v145, v146\n"
      "v_max3_f32 v169, v147, v122, v123\n"
      "v_mfma_f32_16x16x32_bf16 v[206:209], a[220:223], v[190:193], v[206:209]\n"
      "v_max3_f32 v169, v169, v136, v137\n"
      "v_max3_f32 v216, v216, v124, v125\n"
      "v_mfma_f32_16x16x32_bf16 v[206:209], a[224:227], v[198:201], v[206:209]\n"
      "v_exp_f32_e64 v120, v120\n"
      "v_mfma_f32_16x16x32_bf16 v[176:179], a[240:243], v[128:131], 0\n"
      "v_cvt_pk_bf16_f32 v89, v170, v171\n"
      "v_max3_f32 v216, v216, v138, v139\n"
      "v_mfma_f32_16x16x32_bf16 v[176:179], a[244:247], v[190:193], v[176:179]\n"
      "v_cvt_pk_bf16_f32 v151, v152, v153\n"
      "v_cvt_pk_bf16_f32 v153, v212, v213\n"
      "v_mfma_f32_16x16x32_bf16 v[176:179], a[248:251], v[198:201], v[176:179]\n"
      "v_cvt_pk_bf16_f32 v90, v120, 0\n"
      "v_max3_f32 v216, v216, v169, v154\n"
      "v_mfma_f32_16x16x32_bf16 v[108:111], v[232:235], v[128:131], 0\n"
      "v_exp_f32_e64 v211, v211\n"
      "v_mfma_f32_16x16x32_bf16 v[108:111], v[236:239], v[190:193], v[108:111]\n"
      "v_mov_b32 v212, v216\n"
      "v_cvt_pk_bf16_f32 v152, v210, v211\n"
      "v_mfma_f32_16x16x32_bf16 v[108:111], v[240:243], v[198:201], v[108:111]\n"
      "v_permlane16_swap_b32 v216, v212\n"
      "v_mfma_f32_16x16x32_bf16 v[190:193], v[32:35], v[150:153], 0\n"
      "ds_read_b128 v[168:171], v2 offset:9472\n"
      "v_max_f32 v216, v216, v212\n"
      "v_mfma_f32_16x16x32_bf16 v[190:193], v[32:35], v[88:91], v[190:193]\n"
      "v_mov_b32 v212, v216\n"
      "s_waitcnt vmcnt(1)\n"
      "v_or3_b32 v158, v158, v159, v160\n"
      "v_mfma_f32_16x16x32_bf16 v[128:131], v[194:197], v[150:153], 0\n"
      "v_mul_f32 v157, v173, v173\n"
      "v_fma_f32 v156, v172, v172, s52\n"
      "v_mfma_f32_16x16x32_bf16 v[128:131], v[76:79], v[88:91], v[128:131]\n"
      "v_permlane32_swap_b32 v216, v212\n"
      "v_mfma_f32_16x16x32_bf16 v[198:201], v[104:107], v[150:153], 0\n"
      "v_max_f32 v216, v216, v212\n"
      "v_sub_f32 v154, v154, v216\n"
      "s_waitcnt vmcnt(0)\n"
      "v_or3_b32 v158, v158, v161, v93\n"
      "v_rcp_f32_e64 v190, v190\n"
      "v_pk_add_f32 v[138:139], v[138:139], v[216:217] op_sel_hi:[1,0] neg_lo:[0,1] neg_hi:[0,1]\n"
      "v_pk_add_f32 v[144:145], v[144:145], v[216:217] op_sel_hi:[1,0] neg_lo:[0,1] neg_hi:[0,1]\n"
      "v_pk_mul_f32 v[128:129], v[128:129], v[190:191] op_sel_hi:[1,0]\n"
      "v_pk_add_f32 v[122:123], v[122:123], v[216:217] op_sel_hi:[1,0] neg_lo:[0,1] neg_hi:[0,1]\n"
      "v_pk_add_f32 v[146:147], v[146:147], v[216:217] op_sel_hi:[1,0] neg_lo:[0,1] neg_hi:[0,1]\n"
      "v_pk_mul_f32 v[130:131], v[130:131], v[190:191] op_sel_hi:[1,0]\n"
      "v_exp_f32 v122, v122\n"
      "v_mfma_f32_16x16x32_bf16 v[198:201], v[80:83], v[88:91], v[198:201]\n"
      "v_exp_f32 v138, v138\n"
      "v_mfma_f32_16x16x32_bf16 v[210:213], v[96:99], v[150:153], 0\n"
      "v_exp_f32_e64 v139, v139\n"
      "v_mfma_f32_16x16x32_bf16 v[210:213], v[84:87], v[88:91], v[210:213]\n"
      "v_cvt_pk_bf16_f32 v128, v128, v129\n"
      "v_cvt_pk_bf16_f32 v93, v138, v139\n"
      "v_exp_f32_e64 v144, v144\n"
      "v_cvt_pk_bf16_f32 v129, v130, v131\n"
      "v_pk_fma_f32 v[156:157], v[174:175], v[174:175], v[156:157]\n"
      "v_pk_add_f32 v[136:137], v[136:137], v[216:217] op_sel_hi:[1,0] neg_lo:[0,1] neg_hi:[0,1]\n"
      "v_pk_mul_f32 v[198:199], v[198:199], v[190:191] op_sel_hi:[1,0]\n"
      "v_pk_add_f32 v[124:125], v[124:125], v[216:217] op_sel_hi:[1,0] neg_lo:[0,1] neg_hi:[0,1]\n"
      "v_pk_mul_f32 v[212:213], v[212:213], v[190:191] op_sel_hi:[1,0]\n"
      "v_pk_fma_f32 v[156:157], v[220:221], v[220:221], v[156:157]\n"
      "v_pk_mul_f32 v[200:201], v[200:201], v[190:191] op_sel_hi:[1,0]\n"
      "v_pk_mul_f32 v[210:211], v[210:211], v[190:191] op_sel_hi:[1,0]\n"
      "v_cvt_pk_bf16_f32 v130, v198, v199\n"
      "v_cvt_pk_bf16_f32 v131, v200, v201\n"
      "v_mfma_f32_16x16x32_bf16 v[216:219], v[182:185], v[202:205], v[64:67]\n"
      "v_exp_f32 v125, v125\n"
      "v_mfma_f32_16x16x32_bf16 v[186:189], v[100:103], v[202:205], v[68:71]\n"
      "s_waitcnt lgkmcnt(0)\n"
      "v_exp_f32_e64 v146, v146\n"
      "v_mfma_f32_16x16x32_bf16 v[190:193], v[112:115], v[202:205], v[72:75]\n"
      "ds_read_b128 v[160:163], v2 offset:12544\n"
      "v_cvt_pk_bf16_f32 v210, v210, v211\n"
      "v_mfma_f32_16x16x32_bf16 v[150:153], v[132:135], v[202:205], v[28:31]\n"
      "ds_read_b128 v[198:201], v2 offset:15616\n"
      "v_exp_f32 v147, v147\n"
      "v_mfma_f32_16x16x16_bf16 v[216:219], v[148:149], v[126:127], v[216:219]\n"
      "v_cvt_pk_bf16_f32 v211, v212, v213\n"
      "v_exp_f32 v145, v145\n"
      "v_mfma_f32_16x16x16_bf16 v[186:189], v[118:119], v[126:127], v[186:189]\n"
      "v_cvt_pk_bf16_f32 v144, v144, v145\n"
      "v_exp_f32 v123, v123\n"
      "v_mfma_f32_16x16x16_bf16 v[190:193], v[166:167], v[126:127], v[190:193]\n"
      "v_cvt_pk_bf16_f32 v145, v146, v147\n"
      "v_exp_f32 v154, v154\n"
      "v_mfma_f32_16x16x16_bf16 v[150:153], v[116:117], v[126:127], v[150:153]\n"
      "v_cvt_pk_bf16_f32 v146, v122, v123\n"
      "v_exp_f32 v124, v124\n"
      "v_mfma_f32_16x16x32_bf16 v[206:209], a[228:231], v[168:171], v[206:209]\n"
      "s_waitcnt lgkmcnt(1)\n"
      "v_exp_f32 v137, v137\n"
      "v_mfma_f32_16x16x32_bf16 v[206:209], a[232:235], v[160:163], v[206:209]\n"
      "s_waitcnt lgkmcnt(0)\n"
      "v_max3_f32 v90, v216, v217, v218\n"
      "v_cvt_pk_bf16_f32 v94, v154, 0\n"
      "v_mfma_f32_16x16x32_bf16 v[206:209], a[236:239], v[198:201], v[206:209]\n"
      "ds_write_b128 v3, v[128:131] offset:18432\n"
      "v_mfma_f32_16x16x32_bf16 v[176:179], a[252:255], v[168:171], v[176:179]\n"
      "v_cvt_pk_bf16_f32 v147, v124, v125\n"
      "v_max3_f32 v125, v219, v186, v187\n"
      "v_mfma_f32_16x16x32_bf16 v[176:179], v[224:227], v[160:163], v[176:179]\n"
      "ds_read_b64 v[88:89], v10 offset:1184\n"
      "v_max3_f32 v90, v90, v188, v189\n"
      "v_mfma_f32_16x16x32_bf16 v[176:179], v[228:231], v[198:201], v[176:179]\n"
      "v_max3_f32 v125, v125, v190, v191\n"
      "v_max3_f32 v90, v90, v192, v193\n"
      "v_mfma_f32_16x16x32_bf16 v[108:111], v[244:247], v[168:171], v[108:111]\n"
      "v_exp_f32 v136, v136\n"
      "v_mfma_f32_16x16x32_bf16 v[108:111], v[248:251], v[160:163], v[108:111]\n"
      "ds_read_b128 v[152:155], v10 offset:1168\n"
      "v_max3_f32 v90, v90, v125, v150\n"
      "v_mfma_f32_16x16x32_bf16 v[108:111], v[252:255], v[198:201], v[108:111]\n"
      "ds_read_b128 v[198:201], v7\n"
      "v_cvt_pk_bf16_f32 v92, v136, v137\n"
      "v_mfma_f32_16x16x32_bf16 v[160:163], v[32:35], v[144:147], 0\n"
      "v_mul_f32 v125, v207, v207\n"
      "v_mov_b32 v136, v90\n"
      "v_mfma_f32_16x16x32_bf16 v[160:163], v[32:35], v[92:95], v[160:163]\n"
      "s_and_b32 s4, s45, s8\n"
      "v_permlane16_swap_b32 v90, v136\n"
      "v_mfma_f32_16x16x32_bf16 v[212:215], v[194:197], v[144:147], 0\n"
      "s_add_u32 s42, s42, 1\n"
      "v_max_f32 v90, v90, v136\n"
      "v_mov_b32 v136, v90\n"
      "v_mfma_f32_16x16x32_bf16 v[212:215], v[76:79], v[92:95], v[212:215]\n"
      "s_nop 0\n"
      "v_rcp_f32 v160, v160\n"
      "v_mfma_f32_16x16x32_bf16 v[120:123], v[104:107], v[144:147], 0\n"
      "v_permlane32_swap_b32 v90, v136\n"
      "v_mfma_f32_16x16x32_bf16 v[120:123], v[80:83], v[92:95], v[120:123]\n"
      "v_max_f32 v90, v90, v136\n"
      "v_sub_f32 v150, v150, v90\n"
      "v_mfma_f32_16x16x32_bf16 v[136:139], v[96:99], v[144:147], 0\n"
      "s_waitcnt lgkmcnt(0)\n"
      "v_exp_f32_e64 v150, v150\n"
      "v_mfma_f32_16x16x32_bf16 v[136:139], v[84:87], v[92:95], v[136:139]\n"
      "buffer_store_dwordx4 v[198:201], v20, s[24:27], s85 offen sc1\n"
      "v_fma_f32 v124, v206, v206, s52\n"
      "v_pk_mul_f32 v[212:213], v[212:213], v[160:161] op_sel_hi:[1,0]\n"
      "v_pk_add_f32 v[192:193], v[192:193], v[90:91] op_sel_hi:[1,0] neg_lo:[0,1] neg_hi:[0,1]\n"
      "v_pk_add_f32 v[218:219], v[218:219], v[90:91] op_sel_hi:[1,0] neg_lo:[0,1] neg_hi:[0,1]\n"
      "v_pk_fma_f32 v[156:157], v[222:223], v[222:223], v[156:157]\n"
      "v_pk_fma_f32 v[124:125], v[208:209], v[208:209], v[124:125]\n"
      "v_pk_add_f32 v[186:187], v[186:187], v[90:91] op_sel_hi:[1,0] neg_lo:[0,1] neg_hi:[0,1]\n"
      "v_pk_mul_f32 v[214:215], v[214:215], v[160:161] op_sel_hi:[1,0]\n"
      "v_pk_fma_f32 v[124:125], v[176:177], v[176:177], v[124:125]\n"
      "v_pk_mul_f32 v[138:139], v[138:139], v[160:161] op_sel_hi:[1,0]\n"
      "v_pk_add_f32 v[190:191], v[190:191], v[90:91] op_sel_hi:[1,0] neg_lo:[0,1] neg_hi:[0,1]\n"
      "v_pk_mul_f32 v[136:137], v[136:137], v[160:161] op_sel_hi:[1,0]\n"
      "v_pk_mul_f32 v[122:123], v[122:123], v[160:161] op_sel_hi:[1,0]\n"
      "v_pk_add_f32 v[188:189], v[188:189], v[90:91] op_sel_hi:[1,0] neg_lo:[0,1] neg_hi:[0,1]\n"
      "v_pk_add_f32 v[216:217], v[216:217], v[90:91] op_sel_hi:[1,0] neg_lo:[0,1] neg_hi:[0,1]\n"
      "v_cvt_pk_bf16_f32 v212, v212, v213\n"
      "v_pk_mul_f32 v[120:121], v[120:121], v[160:161] op_sel_hi:[1,0]\n"
      "v_mfma_f32_16x16x32_bf16 v[160:163], v[152:155], v[182:185], v[30:33]\n"
      "ds_read_b128 v[144:147], v2 offset:3584\n"
      "v_cvt_pk_bf16_f32 v213, v214, v215\n"
      "v_mfma_f32_16x16x32_bf16 v[182:185], v[152:155], v[100:103], v[30:33]\n"
      "ds_read_b128 v[128:131], v2 offset:6656\n"
      "v_cvt_pk_bf16_f32 v214, v120, v121\n"
      "v_mfma_f32_16x16x32_bf16 v[168:171], v[152:155], v[112:115], v[30:33]\n"
      "ds_read_b128 v[100:103], v2 offset:512\n"
      "v_exp_f32_e64 v189, v189\n"
      "v_mfma_f32_16x16x32_bf16 v[198:201], v[152:155], v[132:135], v[30:33]\n"
      "ds_read_b128 v[152:155], v7 offset:4096\n"
      "v_cvt_pk_bf16_f32 v215, v122, v123\n"
      "v_mfma_f32_16x16x16_bf16 v[160:163], v[88:89], v[148:149], v[160:163]\n"
      "v_exp_f32 v216, v216\n"
      "v_exp_f32 v192, v192\n"
      "v_mfma_f32_16x16x16_bf16 v[182:185], v[88:89], v[118:119], v[182:185]\n"
      "v_exp_f32 v219, v219\n"
      "v_exp_f32 v193, v193\n"
      "v_mfma_f32_16x16x16_bf16 v[168:171], v[88:89], v[166:167], v[168:171]\n"
      "v_exp_f32 v217, v217\n"
      "v_exp_f32 v186, v186\n"
      "v_exp_f32 v218, v218\n"
      "v_pk_fma_f32 v[156:157], v[140:141], v[140:141], v[156:157]\n"
      "v_mfma_f32_16x16x16_bf16 v[198:201], v[88:89], v[116:117], v[198:201]\n"
      "v_cndmask_b32 v160, v160, v182, s[58:59]\n"
      "v_cvt_pk_bf16_f32 v90, v150, 0\n"
      "s_waitcnt lgkmcnt(0)\n"
      "v_mfma_f32_16x16x32_bf16 v[132:135], a[216:219], v[100:103], 0\n"
      "ds_read_b128 v[182:185], v2 offset:12800\n"
      "v_cndmask_b32 v160, v160, v168, s[60:61]\n"
      "v_mfma_f32_16x16x32_bf16 v[132:135], a[220:223], v[144:147], v[132:135]\n"
      "buffer_store_dwordx4 v[152:155], v21, s[24:27], s85 offen sc1\n"
      "v_mfma_f32_16x16x32_bf16 v[132:135], a[224:227], v[128:131], v[132:135]\n"
      "ds_read_b128 v[162:165], v2 offset:9728\n"
      "v_cndmask_b32 v160, v160, v198, s[62:63]\n"
      "v_mfma_f32_16x16x32_bf16 v[118:121], a[240:243], v[100:103], 0\n"
      "ds_read_b128 v[150:153], v2 offset:15872\n"
      "v_exp_f32 v190, v190\n"
      "v_mfma_f32_16x16x32_bf16 v[118:121], a[244:247], v[144:147], v[118:121]\n"
      "ds_read_b128 v[198:201], v7 offset:8192\n"
      "v_exp_f32 v191, v191\n"
      "v_max_f32_dpp v155, v160, v160 row_ror:8 row_mask:0xf bank_mask:0xf\n"
      "v_mfma_f32_16x16x32_bf16 v[118:121], a[248:251], v[128:131], v[118:121]\n"
      "ds_write_b64 v4, v[210:211] offset:18432\n"
      "v_max_f32_dpp v155, v155, v155 row_ror:4 row_mask:0xf bank_mask:0xf\n"
      "v_mfma_f32_16x16x32_bf16 v[168:171], v[232:235], v[100:103], 0\n"
      "ds_read_b128 v[100:103], v7 offset:12288\n"
      "v_cvt_pk_bf16_f32 v89, v192, v193\n"
      "v_mfma_f32_16x16x32_bf16 v[168:171], v[236:239], v[144:147], v[168:171]\n"
      "v_max_f32_dpp v155, v155, v155 row_ror:2 row_mask:0xf bank_mask:0xf\n"
      "v_cvt_pk_bf16_f32 v88, v190, v191\n"
      "v_mfma_f32_16x16x32_bf16 v[168:171], v[240:243], v[128:131], v[168:171]\n"
      "s_waitcnt lgkmcnt(3)\n"
      "v_exp_f32 v187, v187\n"
      "v_mfma_f32_16x16x32_bf16 v[132:135], a[228:231], v[162:165], v[132:135]\n"
      "v_exp_f32 v188, v188\n"
      "v_mfma_f32_16x16x32_bf16 v[132:135], a[232:235], v[182:185], v[132:135]\n"
      "s_waitcnt lgkmcnt(2)\n"
      "v_cvt_pk_bf16_f32 v216, v216, v217\n"
      "v_max_f32_dpp v155, v155, v155 row_ror:1 row_mask:0xf bank_mask:0xf\n"
      "v_mfma_f32_16x16x32_bf16 v[132:135], a[236:239], v[150:153], v[132:135]\n"
      "ds_read_b128 v[190:193], v7 offset:16384\n"
      "v_max_f32_dpp v155, v155, v155 row_bcast:15 row_mask:0xa bank_mask:0xf\n"
      "v_mfma_f32_16x16x32_bf16 v[118:121], a[252:255], v[162:165], v[118:121]\n"
      "s_nop 0\n"
      "v_max_f32_dpp v155, v155, v155 row_bcast:31 row_mask:0xc bank_mask:0xf\n"
      "v_cvt_pk_bf16_f32 v217, v218, v219\n"
      "v_mfma_f32_16x16x32_bf16 v[118:121], v[224:227], v[182:185], v[118:121]\n"
      "buffer_store_dwordx4 v[198:201], v22, s[24:27], s85 offen sc1\n"
      "v_mfma_f32_16x16x32_bf16 v[118:121], v[228:231], v[150:153], v[118:121]\n"
      "v_readlane_b32 s72, v155, 63\n"
      "v_mul_f32 v155, v133, v133\n"
      "v_mfma_f32_16x16x32_bf16 v[168:171], v[244:247], v[162:165], v[168:171]\n"
      "ds_write_b128 v3, v[212:215] offset:18688\n"
      "v_subrev_f32 v160, s72, v160\n"
      "v_fma_f32 v154, v132, v132, s52\n"
      "v_pk_fma_f32 v[124:125], v[178:179], v[178:179], v[124:125]\n"
      "v_pk_fma_f32 v[124:125], v[108:109], v[108:109], v[124:125]\n"
      "v_exp_f32_e64 v160, v160\n"
      "v_pk_fma_f32 v[154:155], v[134:135], v[134:135], v[154:155]\n"
      "v_pk_fma_f32 v[156:157], v[142:143], v[142:143], v[156:157]\n"
      "v_cvt_pk_bf16_f32 v218, v186, v187\n"
      "v_mfma_f32_16x16x32_bf16 v[168:171], v[248:251], v[182:185], v[168:171]\n"
      "s_waitcnt lgkmcnt(1)\n"
      "v_cvt_pk_bf16_f32 v219, v188, v189\n"
      "v_cvt_pk_bf16_f32 v160, v160, v160\n"
      "v_mfma_f32_16x16x32_bf16 v[168:171], v[252:255], v[150:153], v[168:171]\n"
      "ds_write_b16 v13, v160\n"
      "v_mfma_f32_16x16x32_bf16 v[162:165], v[32:35], v[216:219], 0\n"
      "buffer_store_dwordx4 v[100:103], v23, s[24:27], s85 offen sc1\n"
      "v_pk_fma_f32 v[124:125], v[110:111], v[110:111], v[124:125]\n"
      "v_mfma_f32_16x16x32_bf16 v[162:165], v[32:35], v[88:91], v[162:165]\n"
      "v_add_f32 v124, v124, v125\n"
      "v_add_f32 v156, v156, v157\n"
      "v_mfma_f32_16x16x32_bf16 v[186:189], v[194:197], v[216:219], 0\n"
      "ds_read_b128 v[144:147], v14\n"
      "v_cvt_pk_bf16_f32 v136, v136, v137\n"
      "v_mfma_f32_16x16x32_bf16 v[186:189], v[76:79], v[88:91], v[186:189]\n"
      "ds_write_b32 v31, v158 offset:5632\n"
      "v_mfma_f32_16x16x32_bf16 v[150:153], v[104:107], v[216:219], 0\n"
      "v_rcp_f32_e64 v162, v162\n"
      "v_mfma_f32_16x16x32_bf16 v[150:153], v[80:83], v[88:91], v[150:153]\n"
      "ds_read_b128 v[198:201], v14 offset:64\n"
      "v_permlane16_swap_b32 v156, v124\n"
      "v_cvt_pk_bf16_f32 v137, v138, v139\n"
      "v_add_f32 v156, v156, v124\n"
      "v_pk_fma_f32 v[154:155], v[118:119], v[118:119], v[154:155]\n"
      "v_pk_mul_f32 v[186:187], v[186:187], v[162:163] op_sel_hi:[1,0]\n"
      "v_pk_fma_f32 v[154:155], v[120:121], v[120:121], v[154:155]\n"
      "v_pk_fma_f32 v[154:155], v[168:169], v[168:169], v[154:155]\n"
      "v_pk_fma_f32 v[154:155], v[170:171], v[170:171], v[154:155]\n"
      "v_mfma_f32_16x16x32_bf16 v[202:205], v[96:99], v[216:219], 0\n"
      "ds_write_b64 v4, v[136:137] offset:18688\n"
      "v_cvt_pk_bf16_f32 v186, v186, v187\n"
      "v_add_f32_e64 v154, v154, v155\n"
      "v_pk_mul_f32 v[150:151], v[150:151], v[162:163] op_sel_hi:[1,0]\n"
      "v_pk_mul_f32 v[152:153], v[152:153], v[162:163] op_sel_hi:[1,0]\n"
      "v_pk_mul_f32 v[188:189], v[188:189], v[162:163] op_sel_hi:[1,0]\n"
      "v_mfma_f32_16x16x32_bf16 v[202:205], v[84:87], v[88:91], v[202:205]\n"
      "s_waitcnt lgkmcnt(2)\n"
      "v_cvt_pk_bf16_f32 v187, v188, v189\n"
      "v_mov_b32 v124, v154\n"
      "v_cvt_pk_bf16_f32 v189, v152, v153\n"
      "v_cvt_pk_bf16_f32 v188, v150, v151\n"
      "s_add_u32 s43, s43, 1\n"
      "ds_write_b128 v3, v[186:189] offset:18944\n"
      "v_permlane16_swap_b32 v154, v124\n"
      "v_add_f32_e64 v154, v154, v124\n"
      "v_pk_mul_f32 v[202:203], v[202:203], v[162:163] op_sel_hi:[1,0]\n"
      "v_pk_mul_f32 v[204:205], v[204:205], v[162:163] op_sel_hi:[1,0]\n"
      "v_permlane32_swap_b32 v156, v154\n"
      "v_add_f32 v156, v156, v154\n"
      "v_mfma_f32_16x16x32_bf16 v[162:165], v[144:147], v[32:35], 0\n"
      "ds_write_b32 v31, v156 offset:1024\n"
      "v_cvt_pk_bf16_f32 v202, v202, v203\n"
      "v_cvt_pk_bf16_f32 v203, v204, v205\n"
      "s_waitcnt lgkmcnt(2)\n"
      "v_mfma_f32_16x16x32_bf16 v[162:165], v[198:201], v[32:35], v[162:165]\n"
      "ds_write_b64 v4, v[202:203] offset:18944\n"
      "v_add_u32 v10, 2336, v10\n"
      "v_add_u32 v11, 2336, v11\n"
      "buffer_store_dwordx4 v[190:193], v25, s[24:27], s85 offen sc1\n"
      "s_cmp_ge_u32 s95, 0x40000000\n"
      "s_cselect_b64 vcc, -1, s[56:57]\n"
      "s_waitcnt lgkmcnt(0)\n"
      "s_barrier\n"
      "s_cbranch_vccnz L_disp0_%=\n"
      "L_max0_%=:\n"
      "v_mfma_f32_16x16x32_bf16 v[152:155], v[144:147], v[194:197], 0\n"
      "ds_read_b128 v[156:159], v0\n"
      "v_mfma_f32_16x16x32_bf16 v[152:155], v[198:201], v[76:79], v[152:155]\n"
      "ds_read_b128 v[148:151], v9 offset:1024\n"
      "v_readlane_b32 s85, v37, s42\n"
      "v_mfma_f32_16x16x32_bf16 v[210:213], v[144:147], v[104:107], 0\n"
      "s_add_u32 m0, s64, 18432\n"
      "ds_read_b128 v[202:205], v1 offset:12288\n"
      "v_mfma_f32_16x16x32_bf16 v[210:213], v[198:201], v[80:83], v[210:213]\n"
      "buffer_load_dwordx4 v16, s[16:19], s45 offen  lds\n"
      "v_mfma_f32_16x16x32_bf16 v[180:183], v[144:147], v[96:99], 0\n"
      "ds_read_b128 v[112:115], v1\n"
      "ds_read_b128 v[186:189], v0 offset:6144\n"
      "v_mfma_f32_16x16x32_bf16 v[180:183], v[198:201], v[84:87], v[180:183]\n"
      "ds_read_b128 v[144:147], v0 offset:12288\n"
      "s_waitcnt lgkmcnt(5)\n"
      "v_mfma_f32_16x16x32_bf16 v[190:193], a[0:3], v[156:159], 0\n"
      "ds_read_b128 v[128:131], v1 offset:6144\n"
      "s_add_u32 m0, s64, 22528\n"
      "v_mfma_f32_16x16x32_bf16 v[164:167], a[24:27], v[156:159], 0\n"
      "s_waitcnt lgkmcnt(5)\n"
      "v_add_f32 v150, v150, v151\n"
      "v_add_f32 v148, v148, v149\n"
      "v_mfma_f32_16x16x32_bf16 v[100:103], a[48:51], v[156:159], 0\n"
      "buffer_load_dwordx4 v17, s[16:19], s45 offen  lds\n"
      "v_mfma_f32_16x16x32_bf16 v[198:201], a[72:75], v[156:159], 0\n"
      "s_add_u32 m0, s64, 26624\n"
      "v_mov_b32_dpp v162, v162 quad_perm:[0,0,0,0] row_mask:0xf bank_mask:0xf\n"
      "v_add_f32 v148, v148, v150\n"
      "v_mfma_f32_16x16x32_bf16 v[216:219], a[96:99], v[156:159], 0\n"
      "v_rcp_f32 v162, v162\n"
      "v_mfma_f32_16x16x32_bf16 v[122:125], a[120:123], v[156:159], 0\n"
      "s_waitcnt lgkmcnt(2)\n"
      "v_rsq_f32 v148, v148\n"
      "v_mfma_f32_16x16x32_bf16 v[190:193], a[4:7], v[112:115], v[190:193]\n"
      "ds_read_b128 v[182:185], v0 offset:2048\n"
      "v_mul_f32 v148, 0x415db3d7, v148\n"
      "v_mfma_f32_16x16x32_bf16 v[190:193], a[8:11], v[186:189], v[190:193]\n"
      "v_readlane_b32 s84, v37, s43\n"
      "v_mov_b32_e64 v150, v148\n"
      "v_mfma_f32_16x16x32_bf16 v[164:167], a[28:31], v[112:115], v[164:167]\n"
      "buffer_load_dwordx4 v18, s[16:19], s45 offen  lds\n"
      "v_mfma_f32_16x16x32_bf16 v[164:167], a[32:35], v[186:189], v[164:167]\n"
      "v_permlane32_swap_b32 v148, v150\n"
      "v_mfma_f32_16x16x32_bf16 v[100:103], a[52:55], v[112:115], v[100:103]\n"
      "v_cndmask_b32 v152, v152, v210, s[58:59]\n"
      "v_mov_b32 v149, v148\n"
      "v_mfma_f32_16x16x32_bf16 v[100:103], a[56:59], v[186:189], v[100:103]\n"
      "ds_read_b128 v[210:213], v9 offset:5632\n"
      "v_mul_f32_e64 v118, v150, v118\n"
      "v_mfma_f32_16x16x32_bf16 v[198:201], a[76:79], v[112:115], v[198:201]\n"
      "v_cndmask_b32 v152, v152, v180, s[60:61]\n"
      "v_mul_f32 v169, v150, v169\n"
      "v_mfma_f32_16x16x32_bf16 v[198:201], a[80:83], v[186:189], v[198:201]\n"
      "s_add_u32 m0, s64, 30720\n"
      "v_permlane16_swap_b32 v148, v149\n"
      "v_mfma_f32_16x16x32_bf16 v[216:219], a[100:103], v[112:115], v[216:219]\n"
      "v_mul_f32 v120, v150, v120\n"
      "v_mul_f32 v152, v162, v152\n"
      "v_mfma_f32_16x16x32_bf16 v[216:219], a[104:107], v[186:189], v[216:219]\n"
      "v_mul_f32 v143, v148, v143\n"
      "v_mul_f32 v174, v148, v174\n"
      "v_mfma_f32_16x16x32_bf16 v[122:125], a[124:127], v[112:115], v[122:125]\n"
      "s_waitcnt lgkmcnt(1)\n"
      "v_mul_f32 v173, v148, v173\n"
      "v_mul_f32_e64 v175, v148, v175\n"
      "v_mfma_f32_16x16x32_bf16 v[122:125], a[128:131], v[186:189], v[122:125]\n"
      "buffer_load_dwordx4 v19, s[16:19], s45 offen  lds\n"
      "v_mfma_f32_16x16x32_bf16 v[190:193], a[12:15], v[128:131], v[190:193]\n"
      "v_readlane_b32 s86, v38, s43\n"
      "v_mul_f32_e64 v223, v148, v223\n"
      "v_mfma_f32_16x16x32_bf16 v[190:193], a[16:19], v[144:147], v[190:193]\n"
      "v_mul_f32 v221, v148, v221\n"
      "v_mul_f32 v220, v148, v220\n"
      "v_mfma_f32_16x16x32_bf16 v[190:193], a[20:23], v[202:205], v[190:193]\n"
      "v_mul_f32 v140, v148, v140\n"
      "v_mul_f32 v141, v148, v141\n"
      "v_mfma_f32_16x16x32_bf16 v[164:167], a[36:39], v[128:131], v[164:167]\n"
      "s_mov_b32 m0, s7\n"
      "v_mul_f32 v172, v148, v172\n"
      "v_cvt_pk_bf16_f32 v172, v172, v173\n"
      "v_mfma_f32_16x16x32_bf16 v[164:167], a[40:43], v[144:147], v[164:167]\n"
      "s_nop 0\n"
      "v_fma_f32 v180, v190, v190, s51\n"
      "v_mul_f32 v208, v149, v208\n"
      "v_mfma_f32_16x16x32_bf16 v[164:167], a[44:47], v[202:205], v[164:167]\n"
      "ds_read_b128 v[160:163], v0 offset:8192\n"
      "v_cvt_pk_bf16_f32 v173, v174, v175\n"
      "v_mfma_f32_16x16x32_bf16 v[100:103], a[60:63], v[128:131], v[100:103]\n"
      "v_mul_f32 v222, v148, v222\n"
      "v_mul_f32 v142, v148, v142\n"
      "v_mfma_f32_16x16x32_bf16 v[100:103], a[64:67], v[144:147], v[100:103]\n"
      "buffer_load_dwordx4 v15, s[0:3], s4 offen  lds\n"
      "v_mfma_f32_16x16x32_bf16 v[100:103], a[68:71], v[202:205], v[100:103]\n"
      "s_or_b32 s95, s84, s85\n"
      "v_mul_f32 v133, v150, v133\n"
      "v_cvt_pk_bf16_f32 v174, v220, v221\n"
      "v_mfma_f32_16x16x32_bf16 v[198:201], a[84:87], v[128:131], v[198:201]\n"
      "v_cvt_pk_bf16_f32 v175, v222, v223\n"
      "v_mul_f32 v132, v150, v132\n"
      "v_mfma_f32_16x16x32_bf16 v[198:201], a[88:91], v[144:147], v[198:201]\n"
      "v_mul_f32 v108, v149, v108\n"
      "v_fmac_f32_e64 v180, v191, v191\n"
      "v_mfma_f32_16x16x32_bf16 v[198:201], a[92:95], v[202:205], v[198:201]\n"
      "ds_write_b128 v5, v[172:175]\n"
      "v_pk_mul_f32 v[110:111], v[110:111], v[148:149] op_sel:[0,1]\n"
      "v_mfma_f32_16x16x32_bf16 v[216:219], a[108:111], v[128:131], v[216:219]\n"
      "buffer_load_dwordx4 v[136:139], v36, s[28:31], s47 offen\n"
      "v_mfma_f32_16x16x32_bf16 v[216:219], a[112:115], v[144:147], v[216:219]\n"
      "v_mul_f32 v206, v149, v206\n"
      "v_mul_f32 v109, v149, v109\n"
      "v_mfma_f32_16x16x32_bf16 v[216:219], a[116:119], v[202:205], v[216:219]\n"
      "s_waitcnt lgkmcnt(1)\n"
      "v_mul_f32 v176, v149, v176\n"
      "v_or3_b32 v210, v210, v211, v212\n"
      "v_mfma_f32_16x16x32_bf16 v[122:125], a[132:135], v[128:131], v[122:125]\n"
      "v_fma_f32 v211, v198, v198, s51\n"
      "v_mul_f32 v168, v150, v168\n"
      "v_mfma_f32_16x16x32_bf16 v[122:125], a[136:139], v[144:147], v[122:125]\n"
      "v_fmac_f32 v211, v199, v199\n"
      "v_fmac_f32_e64 v180, v192, v192\n"
      "v_mfma_f32_16x16x32_bf16 v[122:125], a[140:143], v[202:205], v[122:125]\n"
      "ds_read_b128 v[172:175], v1 offset:2048\n"
      "v_fmac_f32 v211, v200, v200\n"
      "v_mfma_f32_16x16x32_bf16 v[194:197], v[156:159], a[144:147], 0\n"
      "v_fmac_f32 v211, v201, v201\n"
      "v_mul_f32_e64 v171, v150, v171\n"
      "v_mfma_f32_16x16x32_bf16 v[194:197], v[112:115], a[148:151], v[194:197]\n"
      "v_cvt_pk_bf16_f32 v108, v108, v109\n"
      "v_cvt_pk_bf16_f32 v132, v132, v133\n"
      "v_mfma_f32_16x16x32_bf16 v[194:197], v[186:189], a[152:155], v[194:197]\n"
      "v_fmac_f32 v211, v216, v216\n"
      "v_fmac_f32 v211, v217, v217\n"
      "v_mfma_f32_16x16x32_bf16 v[194:197], v[128:131], a[156:159], v[194:197]\n"
      "ds_read_b128 v[220:223], v1 offset:8192\n"
      "v_bitop3_b32 v210, v210, v213, s33 bitop3:0xa8\n"
      "v_mfma_f32_16x16x32_bf16 v[194:197], v[144:147], a[160:163], v[194:197]\n"
      "v_fmac_f32 v180, v193, v193\n"
      "v_mul_f32 v209, v149, v209\n"
      "v_mfma_f32_16x16x32_bf16 v[194:197], v[202:205], a[164:167], v[194:197]\n"
      "ds_read_b128 v[212:215], v0 offset:14336\n"
      "v_fmac_f32 v211, v218, v218\n"
      "v_mfma_f32_16x16x32_bf16 v[104:107], v[156:159], a[168:171], 0\n"
      "v_cvt_pk_bf16_f32 v140, v140, v141\n"
      "v_mul_f32 v179, v149, v179\n"
      "v_mfma_f32_16x16x32_bf16 v[104:107], v[112:115], a[172:175], v[104:107]\n"
      "v_cvt_pk_bf16_f32 v141, v142, v143\n"
      "v_fmac_f32 v211, v219, v219\n"
      "v_mfma_f32_16x16x32_bf16 v[104:107], v[186:189], a[176:179], v[104:107]\n"
      "v_fmac_f32 v211, v122, v122\n"
      "v_mul_f32 v135, v150, v135\n"
      "v_mfma_f32_16x16x32_bf16 v[104:107], v[128:131], a[180:183], v[104:107]\n"
      "v_mul_f32 v170, v150, v170\n"
      "v_fmac_f32 v180, v164, v164\n"
      "v_mfma_f32_16x16x32_bf16 v[104:107], v[144:147], a[184:187], v[104:107]\n"
      "v_fmac_f32 v211, v123, v123\n"
      "v_mul_f32 v177, v149, v177\n"
      "v_mfma_f32_16x16x32_bf16 v[104:107], v[202:205], a[188:191], v[104:107]\n"
      "v_fmac_f32 v180, v165, v165\n"
      "v_mul_f32_e64 v134, v150, v134\n"
      "v_mfma_f32_16x16x32_bf16 v[96:99], v[156:159], a[192:195], 0\n"
      "ds_read_b128 v[156:159], v1 offset:4096\n"
      "v_fmac_f32 v180, v166, v166\n"
      "v_mfma_f32_16x16x32_bf16 v[96:99], v[112:115], a[196:199], v[96:99]\n"
      "ds_read_b128 v[112:115], v1 offset:14336\n"
      "v_mul_f32 v178, v149, v178\n"
      "v_mfma_f32_16x16x32_bf16 v[96:99], v[186:189], a[200:203], v[96:99]\n"
      "s_or_b32 s95, s95, s48\n"
      "v_fmac_f32 v180, v167, v167\n"
      "v_cvt_pk_bf16_f32 v194, v194, v195\n"
      "v_mfma_f32_16x16x32_bf16 v[96:99], v[128:131], a[204:207], v[96:99]\n"
      "v_fmac_f32_e64 v180, v100, v100\n"
      "v_cvt_pk_bf16_f32 v195, v196, v197\n"
      "v_mfma_f32_16x16x32_bf16 v[96:99], v[144:147], a[208:211], v[96:99]\n"
      "buffer_load_dword v93, v29, s[28:31], s47 offen\n"
      "v_mfma_f32_16x16x32_bf16 v[96:99], v[202:205], a[212:215], v[96:99]\n"
      "v_cvt_pk_bf16_f32 v152, v152, v152\n"
      "v_mul_f32 v207, v149, v207\n"
      "v_mfma_f32_16x16x32_bf16 v[202:205], a[0:3], v[182:185], 0\n"
      "s_waitcnt lgkmcnt(1)\n"
      "v_cvt_pk_bf16_f32 v206, v206, v207\n"
      "v_fmac_f32_e64 v211, v124, v124\n"
      "v_mfma_f32_16x16x32_bf16 v[202:205], a[4:7], v[172:175], v[202:205]\n"
      "ds_write_b16 v12, v152 offset:16\n"
      "v_mfma_f32_16x16x32_bf16 v[202:205], a[8:11], v[160:163], v[202:205]\n"
      "v_cvt_pk_bf16_f32 v168, v168, v169\n"
      "v_fmac_f32_e64 v180, v101, v101\n"
      "v_mfma_f32_16x16x32_bf16 v[202:205], a[12:15], v[220:223], v[202:205]\n"
      "ds_write_b64 v6, v[140:141]\n"
      "v_mfma_f32_16x16x32_bf16 v[202:205], a[16:19], v[212:215], v[202:205]\n"
      "s_waitcnt lgkmcnt(2)\n"
      "v_fmac_f32 v211, v125, v125\n"
      "v_fmac_f32_e64 v180, v102, v102\n"
      "v_mfma_f32_16x16x32_bf16 v[202:205], a[20:23], v[112:115], v[202:205]\n"
      "v_mul_f32 v119, v150, v119\n"
      "v_cvt_pk_bf16_f32 v169, v170, v171\n"
      "v_mfma_f32_16x16x32_bf16 v[126:129], a[24:27], v[182:185], 0\n"
      "v_fmac_f32 v180, v103, v103\n"
      "v_cvt_pk_bf16_f32 v207, v208, v209\n"
      "v_mfma_f32_16x16x32_bf16 v[126:129], a[28:31], v[172:175], v[126:129]\n"
      "v_cvt_pk_bf16_f32 v96, v96, v97\n"
      "v_cvt_pk_bf16_f32 v133, v134, v135\n"
      "v_mfma_f32_16x16x32_bf16 v[126:129], a[32:35], v[160:163], v[126:129]\n"
      "v_permlane16_swap_b32 v180, v211\n"
      "v_mfma_f32_16x16x32_bf16 v[126:129], a[36:39], v[220:223], v[126:129]\n"
      "v_cvt_pk_bf16_f32 v104, v104, v105\n"
      "v_add_f32 v180, v180, v211\n"
      "v_mfma_f32_16x16x32_bf16 v[126:129], a[40:43], v[212:215], v[126:129]\n"
      "v_mov_b32 v211, v180\n"
      "v_cvt_pk_bf16_f32 v208, v176, v177\n"
      "v_mfma_f32_16x16x32_bf16 v[126:129], a[44:47], v[112:115], v[126:129]\n"
      "v_permlane32_swap_b32 v180, v211\n"
      "v_mfma_f32_16x16x32_bf16 v[144:147], a[48:51], v[182:185], 0\n"
      "v_cvt_pk_bf16_f32 v134, v118, v119\n"
      "v_cvt_pk_bf16_f32 v209, v178, v179\n"
      "v_mfma_f32_16x16x32_bf16 v[144:147], a[52:55], v[172:175], v[144:147]\n"
      "v_fma_f32 v131, v202, v202, s51\n"
      "v_add_f32 v180, v180, v211\n"
      "v_mfma_f32_16x16x32_bf16 v[144:147], a[56:59], v[160:163], v[144:147]\n"
      "v_rsq_f32 v180, v180\n"
      "v_mfma_f32_16x16x32_bf16 v[144:147], a[60:63], v[220:223], v[144:147]\n"
      "v_fmac_f32 v131, v203, v203\n"
      "v_mov_b32 v211, v180\n"
      "v_mfma_f32_16x16x32_bf16 v[144:147], a[64:67], v[212:215], v[144:147]\n"
      "v_mul_f32_e64 v121, v150, v121\n"
      "v_cvt_pk_bf16_f32 v105, v106, v107\n"
      "v_mfma_f32_16x16x32_bf16 v[144:147], a[68:71], v[112:115], v[144:147]\n"
      "v_permlane16_swap_b32_e64 v180, v211\n"
      "v_mfma_f32_16x16x32_bf16 v[148:151], a[72:75], v[182:185], 0\n"
      "ds_write_b128 v5, v[206:209] offset:6144\n"
      "v_mfma_f32_16x16x32_bf16 v[148:151], a[76:79], v[172:175], v[148:151]\n"
      "v_fmac_f32 v131, v204, v204\n"
      "v_fmac_f32 v131, v205, v205\n"
      "v_mfma_f32_16x16x32_bf16 v[148:151], a[80:83], v[160:163], v[148:151]\n"
      "ds_read_b128 v[206:209], v1 offset:10240\n"
      "v_mul_f32 v123, v211, v123\n"
      "v_mfma_f32_16x16x32_bf16 v[148:151], a[84:87], v[220:223], v[148:151]\n"
      "v_fmac_f32 v131, v126, v126\n"
      "v_cvt_pk_bf16_f32 v109, v110, v111\n"
      "v_mfma_f32_16x16x32_bf16 v[148:151], a[88:91], v[212:215], v[148:151]\n"
      "ds_read_b64 v[116:117], v10 offset:24\n"
      "v_mul_f32 v180, s50, v180\n"
      "v_mfma_f32_16x16x32_bf16 v[148:151], a[92:95], v[112:115], v[148:151]\n"
      "v_mul_f32 v165, v180, v165\n"
      "v_mul_f32_e64 v218, v211, v218\n"
      "v_mfma_f32_16x16x32_bf16 v[186:189], a[96:99], v[182:185], 0\n"
      "v_mul_f32_e64 v103, v180, v103\n"
      "v_cmp_ne_u32 s[56:57], 0, v210\n"
      "v_mfma_f32_16x16x32_bf16 v[186:189], a[100:103], v[172:175], v[186:189]\n"
      "ds_read_b128 v[140:143], v0 offset:10240\n"
      "v_mul_f32 v192, v180, v192\n"
      "v_mfma_f32_16x16x32_bf16 v[186:189], a[104:107], v[160:163], v[186:189]\n"
      "v_mul_f32 v219, v211, v219\n"
      "v_fma_f32 v130, v148, v148, s51\n"
      "v_mfma_f32_16x16x32_bf16 v[186:189], a[108:111], v[220:223], v[186:189]\n"
      "v_fmac_f32 v131, v127, v127\n"
      "v_fmac_f32 v131, v128, v128\n"
      "v_mfma_f32_16x16x32_bf16 v[186:189], a[112:115], v[212:215], v[186:189]\n"
      "v_fmac_f32 v131, v129, v129\n"
      "v_fmac_f32 v130, v149, v149\n"
      "v_mfma_f32_16x16x32_bf16 v[186:189], a[116:119], v[112:115], v[186:189]\n"
      "v_cvt_pk_bf16_f32 v97, v98, v99\n"
      "v_cvt_pk_bf16_f32 v135, v120, v121\n"
      "v_mfma_f32_16x16x32_bf16 v[152:155], a[120:123], v[182:185], 0\n"
      "ds_read_b128 v[118:121], v0 offset:4096\n"
      "v_mul_f32_e64 v100, v180, v100\n"
      "v_mfma_f32_16x16x32_bf16 v[152:155], a[124:127], v[172:175], v[152:155]\n"
      "ds_write_b64 v6, v[168:169] offset:12288\n"
      "v_mfma_f32_16x16x32_bf16 v[152:155], a[128:131], v[160:163], v[152:155]\n"
      "v_mul_f32 v101, v180, v101\n"
      "v_mul_f32 v124, v211, v124\n"
      "v_mfma_f32_16x16x32_bf16 v[152:155], a[132:135], v[220:223], v[152:155]\n"
      "v_mul_f32 v190, v180, v190\n"
      "v_fmac_f32 v131, v144, v144\n"
      "v_mfma_f32_16x16x32_bf16 v[152:155], a[136:139], v[212:215], v[152:155]\n"
      "v_fmac_f32 v131, v145, v145\n"
      "v_fmac_f32 v130, v150, v150\n"
      "v_mfma_f32_16x16x32_bf16 v[152:155], a[140:143], v[112:115], v[152:155]\n"
      "s_cmp_ge_u32 s95, 0x40000000\n"
      "s_cselect_b32 s94, -1, -2\n"
      "v_mul_f32 v102, v180, v102\n"
      "v_mfma_f32_16x16x32_bf16 v[168:171], v[182:185], a[144:147], 0\n"
      "v_mul_f32 v200, v211, v200\n"
      "v_mul_f32 v216, v211, v216\n"
      "v_mfma_f32_16x16x32_bf16 v[168:171], v[172:175], a[148:151], v[168:171]\n"
      "v_mul_f32 v191, v180, v191\n"
      "v_fmac_f32 v130, v151, v151\n"
      "v_mfma_f32_16x16x32_bf16 v[168:171], v[160:163], a[152:155], v[168:171]\n"
      "v_fmac_f32 v130, v186, v186\n"
      "v_mul_f32_e64 v217, v211, v217\n"
      "v_mfma_f32_16x16x32_bf16 v[168:171], v[220:223], a[156:159], v[168:171]\n"
      "ds_write_b64 v6, v[108:109] offset:6144\n"
      "v_mfma_f32_16x16x32_bf16 v[168:171], v[212:215], a[160:163], v[168:171]\n"
      "v_cvt_pk_bf16_f32 v190, v190, v191\n"
      "v_fmac_f32_e64 v130, v187, v187\n"
      "v_mfma_f32_16x16x32_bf16 v[168:171], v[112:115], a[164:167], v[168:171]\n"
      "v_fmac_f32 v130, v188, v188\n"
      "v_fmac_f32 v130, v189, v189\n"
      "v_mfma_f32_16x16x32_bf16 v[176:179], v[182:185], a[168:171], 0\n"
      "v_mul_f32 v198, v211, v198\n"
      "v_mul_f32 v199, v211, v199\n"
      "v_mfma_f32_16x16x32_bf16 v[176:179], v[172:175], a[172:175], v[176:179]\n"
      "v_mul_f32 v201, v211, v201\n"
      "v_mul_f32 v122, v211, v122\n"
      "v_mfma_f32_16x16x32_bf16 v[176:179], v[160:163], a[176:179], v[176:179]\n"
      "v_fmac_f32 v131, v146, v146\n"
      "v_fmac_f32 v130, v152, v152\n"
      "v_mfma_f32_16x16x32_bf16 v[176:179], v[220:223], a[180:183], v[176:179]\n"
      "v_mul_f32 v166, v180, v166\n"
      "v_fmac_f32 v130, v153, v153\n"
      "v_mfma_f32_16x16x32_bf16 v[176:179], v[212:215], a[184:187], v[176:179]\n"
      "ds_read_b128 v[108:111], v1 offset:16384\n"
      "v_mul_f32_e64 v125, v211, v125\n"
      "v_mfma_f32_16x16x32_bf16 v[176:179], v[112:115], a[188:191], v[176:179]\n"
      "ds_write_b128 v5, v[132:135] offset:12288\n"
      "v_mfma_f32_16x16x32_bf16 v[132:135], v[182:185], a[192:195], 0\n"
      "ds_read_b128 v[182:185], v0 offset:16384\n"
      "v_cvt_pk_bf16_f32 v100, v100, v101\n"
      "v_mfma_f32_16x16x32_bf16 v[132:135], v[172:175], a[196:199], v[132:135]\n"
      "v_fmac_f32 v130, v154, v154\n"
      "v_fmac_f32 v131, v147, v147\n"
      "v_mfma_f32_16x16x32_bf16 v[132:135], v[160:163], a[200:203], v[132:135]\n"
      "v_fmac_f32_e64 v130, v155, v155\n"
      "v_cvt_pk_bf16_f32 v122, v122, v123\n"
      "v_mfma_f32_16x16x32_bf16 v[132:135], v[220:223], a[204:207], v[132:135]\n"
      "v_cvt_pk_bf16_f32 v107, v178, v179\n"
      "v_mul_f32 v193, v180, v193\n"
      "v_mfma_f32_16x16x32_bf16 v[132:135], v[212:215], a[208:211], v[132:135]\n"
      "v_permlane16_swap_b32 v131, v130\n"
      "v_mfma_f32_16x16x32_bf16 v[132:135], v[112:115], a[212:215], v[132:135]\n"
      "s_waitcnt lgkmcnt(2)\n"
      "v_cvt_pk_bf16_f32 v106, v176, v177\n"
      "v_add_f32 v131, v131, v130\n"
      "v_mfma_f32_16x16x32_bf16 v[210:213], a[0:3], v[118:121], 0\n"
      "v_cvt_pk_bf16_f32 v191, v192, v193\n"
      "v_cvt_pk_bf16_f32 v197, v170, v171\n"
      "v_mfma_f32_16x16x32_bf16 v[210:213], a[4:7], v[156:159], v[210:213]\n"
      "v_mul_f32 v167, v180, v167\n"
      "v_mov_b32 v130, v131\n"
      "v_mfma_f32_16x16x32_bf16 v[210:213], a[8:11], v[140:143], v[210:213]\n"
      "v_cvt_pk_bf16_f32 v198, v198, v199\n"
      "v_mul_f32 v164, v180, v164\n"
      "v_mfma_f32_16x16x32_bf16 v[210:213], a[12:15], v[206:209], v[210:213]\n"
      "s_waitcnt lgkmcnt(0)\n"
      "v_permlane32_swap_b32 v131, v130\n"
      "v_add_f32 v131, v131, v130\n"
      "v_mfma_f32_16x16x32_bf16 v[210:213], a[16:19], v[182:185], v[210:213]\n"
      "v_rsq_f32 v131, v131\n"
      "v_mfma_f32_16x16x32_bf16 v[210:213], a[20:23], v[108:111], v[210:213]\n"
      "v_cvt_pk_bf16_f32 v98, v132, v133\n"
      "v_mov_b32 v130, v131\n"
      "v_mfma_f32_16x16x32_bf16 v[178:181], a[24:27], v[118:121], 0\n"
      "s_cmp_gt_u32 s42, s44\n"
      "s_cselect_b32 s95, s94, s95\n"
      "v_cvt_pk_bf16_f32 v192, v164, v165\n"
      "v_mfma_f32_16x16x32_bf16 v[178:181], a[28:31], v[156:159], v[178:181]\n"
      "v_permlane16_swap_b32 v131, v130\n"
      "v_mfma_f32_16x16x32_bf16 v[178:181], a[32:35], v[140:143], v[178:181]\n"
      "v_fma_f32 v214, v210, v210, s51\n"
      "v_fmac_f32 v214, v211, v211\n"
      "v_mfma_f32_16x16x32_bf16 v[178:181], a[36:39], v[206:209], v[178:181]\n"
      "v_fmac_f32 v214, v212, v212\n"
      "v_cvt_pk_bf16_f32 v193, v166, v167\n"
      "v_mfma_f32_16x16x32_bf16 v[178:181], a[40:43], v[182:185], v[178:181]\n"
      "v_mul_f32 v131, s50, v131\n"
      "v_cvt_pk_bf16_f32 v199, v200, v201\n"
      "v_mfma_f32_16x16x32_bf16 v[178:181], a[44:47], v[108:111], v[178:181]\n"
      "v_cvt_pk_bf16_f32 v101, v102, v103\n"
      "v_fmac_f32 v214, v213, v213\n"
      "v_mfma_f32_16x16x32_bf16 v[164:167], a[48:51], v[118:121], 0\n"
      "v_mul_f32 v146, v131, v146\n"
      "v_mul_f32 v151, v130, v151\n"
      "v_mfma_f32_16x16x32_bf16 v[164:167], a[52:55], v[156:159], v[164:167]\n"
      "v_mul_f32 v204, v131, v204\n"
      "v_mul_f32 v205, v131, v205\n"
      "v_mfma_f32_16x16x32_bf16 v[164:167], a[56:59], v[140:143], v[164:167]\n"
      "v_mul_f32 v150, v130, v150\n"
      "v_mul_f32 v148, v130, v148\n"
      "v_mfma_f32_16x16x32_bf16 v[164:167], a[60:63], v[206:209], v[164:167]\n"
      "v_fmac_f32 v214, v178, v178\n"
      "v_mul_f32 v155, v130, v155\n"
      "v_mfma_f32_16x16x32_bf16 v[164:167], a[64:67], v[182:185], v[164:167]\n"
      "v_mul_f32 v145, v131, v145\n"
      "v_mul_f32 v149, v130, v149\n"
      "v_mfma_f32_16x16x32_bf16 v[164:167], a[68:71], v[108:111], v[164:167]\n"
      "v_mul_f32 v154, v130, v154\n"
      "v_fmac_f32_e64 v214, v179, v179\n"
      "v_mfma_f32_16x16x32_bf16 v[112:115], a[72:75], v[118:121], 0\n"
      "v_cvt_pk_bf16_f32 v148, v148, v149\n"
      "v_cvt_pk_bf16_f32 v200, v216, v217\n"
      "v_mfma_f32_16x16x32_bf16 v[112:115], a[76:79], v[156:159], v[112:115]\n"
      "v_mul_f32 v129, v131, v129\n"
      "v_mul_f32 v202, v131, v202\n"
      "v_mfma_f32_16x16x32_bf16 v[112:115], a[80:83], v[140:143], v[112:115]\n"
      "v_mul_f32 v189, v130, v189\n"
      "v_mul_f32 v147, v131, v147\n"
      "v_mfma_f32_16x16x32_bf16 v[112:115], a[84:87], v[206:209], v[112:115]\n"
      "v_mul_f32 v187, v130, v187\n"
      "v_add_u32 v12, 32, v12\n"
      "v_mfma_f32_16x16x32_bf16 v[112:115], a[88:91], v[182:185], v[112:115]\n"
      "v_mul_f32 v153, v130, v153\n"
      "v_fmac_f32 v214, v180, v180\n"
      "v_mfma_f32_16x16x32_bf16 v[112:115], a[92:95], v[108:111], v[112:115]\n"
      "v_cvt_pk_bf16_f32 v149, v150, v151\n"
      "v_cvt_pk_bf16_f32 v201, v218, v219\n"
      "v_mfma_f32_16x16x32_bf16 v[160:163], a[96:99], v[118:121], 0\n"
      "v_mul_f32 v203, v131, v203\n"
      "v_mul_f32 v188, v130, v188\n"
      "v_mfma_f32_16x16x32_bf16 v[160:163], a[100:103], v[156:159], v[160:163]\n"
      "v_mul_f32 v127, v131, v127\n"
      "v_mul_f32 v152, v130, v152\n"
      "v_mfma_f32_16x16x32_bf16 v[160:163], a[104:107], v[140:143], v[160:163]\n"
      "ds_read_u16 v78, v11\n"
      "v_fmac_f32_e64 v214, v181, v181\n"
      "v_mfma_f32_16x16x32_bf16 v[160:163], a[108:111], v[206:209], v[160:163]\n"
      "v_cvt_pk_bf16_f32 v99, v134, v135\n"
      "v_cvt_pk_bf16_f32 v202, v202, v203\n"
      "v_mfma_f32_16x16x32_bf16 v[160:163], a[112:115], v[182:185], v[160:163]\n"
      "v_fma_f32 v103, v112, v112, s51\n"
      "v_fmac_f32 v214, v164, v164\n"
      "v_mfma_f32_16x16x32_bf16 v[160:163], a[116:119], v[108:111], v[160:163]\n"
      "v_mul_f32 v128, v131, v128\n"
      "v_fmac_f32_e64 v214, v165, v165\n"
      "v_mfma_f32_16x16x32_bf16 v[216:219], a[120:123], v[118:121], 0\n"
      "v_fmac_f32 v214, v166, v166\n"
      "v_fmac_f32 v103, v113, v113\n"
      "v_mfma_f32_16x16x32_bf16 v[216:219], a[124:127], v[156:159], v[216:219]\n"
      "v_cvt_pk_bf16_f32 v151, v188, v189\n"
      "v_fmac_f32 v103, v114, v114\n"
      "v_mfma_f32_16x16x32_bf16 v[216:219], a[128:131], v[140:143], v[216:219]\n"
      "v_mul_f32 v126, v131, v126\n"
      "v_mul_f32 v186, v130, v186\n"
      "v_mfma_f32_16x16x32_bf16 v[216:219], a[132:135], v[206:209], v[216:219]\n"
      "s_add_u32 s43, s43, 1\n"
      "v_fmac_f32 v103, v115, v115\n"
      "v_cvt_pk_bf16_f32 v123, v124, v125\n"
      "v_mfma_f32_16x16x32_bf16 v[216:219], a[136:139], v[182:185], v[216:219]\n"
      "v_mul_f32 v144, v131, v144\n"
      "v_cvt_pk_bf16_f32 v152, v152, v153\n"
      "v_mfma_f32_16x16x32_bf16 v[216:219], a[140:143], v[108:111], v[216:219]\n"
      "v_fmac_f32 v103, v160, v160\n"
      "v_cvt_pk_bf16_f32 v203, v204, v205\n"
      "v_mfma_f32_16x16x32_bf16 v[174:177], v[118:121], a[144:147], 0\n"
      "v_fmac_f32 v103, v161, v161\n"
      "v_fmac_f32_e64 v103, v162, v162\n"
      "v_mfma_f32_16x16x32_bf16 v[174:177], v[156:159], a[148:151], v[174:177]\n"
      "v_cvt_pk_bf16_f32 v204, v126, v127\n"
      "v_cvt_pk_bf16_f32 v205, v128, v129\n"
      "v_mfma_f32_16x16x32_bf16 v[174:177], v[140:143], a[152:155], v[174:177]\n"
      "v_fmac_f32 v103, v163, v163\n"
      "v_cvt_pk_bf16_f32 v150, v186, v187\n"
      "v_mfma_f32_16x16x32_bf16 v[174:177], v[206:209], a[156:159], v[174:177]\n"
      "v_fmac_f32 v214, v167, v167\n"
      "v_fmac_f32 v103, v216, v216\n"
      "v_mfma_f32_16x16x32_bf16 v[174:177], v[182:185], a[160:163], v[174:177]\n"
      "v_fmac_f32 v103, v217, v217\n"
      "v_fmac_f32_e64 v103, v218, v218\n"
      "v_mfma_f32_16x16x32_bf16 v[174:177], v[108:111], a[164:167], v[174:177]\n"
      "ds_read_b128 v[186:189], v2 offset:21504\n"
      "v_fmac_f32 v103, v219, v219\n"
      "v_mfma_f32_16x16x32_bf16 v[128:131], v[118:121], a[168:171], 0\n"
      "s_nop 0\n"
      "v_permlane16_swap_b32_e64 v214, v103\n"
      "v_mfma_f32_16x16x32_bf16 v[128:131], v[156:159], a[172:175], v[128:131]\n"
      "v_add_f32 v214, v214, v103\n"
      "v_mov_b32 v103, v214\n"
      "v_mfma_f32_16x16x32_bf16 v[128:131], v[140:143], a[176:179], v[128:131]\n"
      "s_nop 0\n"
      "v_permlane32_swap_b32 v214, v103\n"
      "v_mfma_f32_16x16x32_bf16 v[128:131], v[206:209], a[180:183], v[128:131]\n"
      "v_add_f32 v214, v214, v103\n"
      "v_cvt_pk_bf16_f32 v76, v174, v175\n"
      "v_mfma_f32_16x16x32_bf16 v[128:131], v[182:185], a[184:187], v[128:131]\n"
      "v_rsq_f32 v214, v214\n"
      "v_mfma_f32_16x16x32_bf16 v[128:131], v[108:111], a[188:191], v[128:131]\n"
      "ds_read_b128 v[132:135], v2 offset:18432\n"
      "v_mov_b32_e64 v103, v214\n"
      "v_mfma_f32_16x16x32_bf16 v[124:127], v[118:121], a[192:195], 0\n"
      "ds_read_b128 v[118:121], v2 offset:24576\n"
      "v_cvt_pk_bf16_f32 v144, v144, v145\n"
      "v_mfma_f32_16x16x32_bf16 v[124:127], v[156:159], a[196:199], v[124:127]\n"
      "v_permlane16_swap_b32_e64 v214, v103\n"
      "v_mfma_f32_16x16x32_bf16 v[124:127], v[140:143], a[200:203], v[124:127]\n"
      "v_mul_f32 v162, v103, v162\n"
      "v_mul_f32 v112, v103, v112\n"
      "v_mfma_f32_16x16x32_bf16 v[124:127], v[206:209], a[204:207], v[124:127]\n"
      "v_mul_f32 v216, v103, v216\n"
      "v_mul_f32 v115, v103, v115\n"
      "v_mfma_f32_16x16x32_bf16 v[124:127], v[182:185], a[208:211], v[124:127]\n"
      "v_mul_f32 v217, v103, v217\n"
      "v_mul_f32 v163, v103, v163\n"
      "v_mfma_f32_16x16x32_bf16 v[124:127], v[108:111], a[212:215], v[124:127]\n"
      "s_waitcnt lgkmcnt(1)\n"
      "v_mul_f32 v113, v103, v113\n"
      "v_cvt_pk_bf16_f32 v153, v154, v155\n"
      "v_mfma_f32_16x16x32_bf16 v[172:175], a[216:219], v[132:135], 0\n"
      "ds_read_b128 v[182:185], v10 offset:32\n"
      "v_cvt_pk_bf16_f32 v216, v216, v217\n"
      "v_mfma_f32_16x16x32_bf16 v[172:175], a[220:223], v[186:189], v[172:175]\n"
      "s_waitcnt lgkmcnt(1)\n"
      "v_mul_f32 v161, v103, v161\n"
      "v_mul_f32 v114, v103, v114\n"
      "v_mfma_f32_16x16x32_bf16 v[172:175], a[224:227], v[118:121], v[172:175]\n"
      "v_mul_f32 v214, s50, v214\n"
      "v_mul_f32 v166, v214, v166\n"
      "v_mfma_f32_16x16x32_bf16 v[220:223], a[240:243], v[132:135], 0\n"
      "ds_read_b128 v[206:209], v2 offset:27648\n"
      "v_mul_f32 v219, v103, v219\n"
      "v_mfma_f32_16x16x32_bf16 v[220:223], a[244:247], v[186:189], v[220:223]\n"
      "v_cvt_pk_bf16_f32 v112, v112, v113\n"
      "v_cvt_pk_bf16_f32 v113, v114, v115\n"
      "v_mfma_f32_16x16x32_bf16 v[220:223], a[248:251], v[118:121], v[220:223]\n"
      "v_cvt_pk_bf16_f32 v81, v130, v131\n"
      "v_mul_f32 v164, v214, v164\n"
      "v_mfma_f32_16x16x32_bf16 v[140:143], v[232:235], v[132:135], 0\n"
      "v_mul_f32 v160, v103, v160\n"
      "v_cvt_pk_bf16_f32 v114, v160, v161\n"
      "v_mfma_f32_16x16x32_bf16 v[140:143], v[236:239], v[186:189], v[140:143]\n"
      "ds_read_b128 v[158:161], v2 offset:33792\n"
      "v_cvt_pk_bf16_f32 v84, v124, v125\n"
      "v_mfma_f32_16x16x32_bf16 v[140:143], v[240:243], v[118:121], v[140:143]\n"
      "ds_read_u16 v82, v11 offset:32\n"
      "v_cvt_pk_bf16_f32 v80, v128, v129\n"
      "v_mfma_f32_16x16x32_bf16 v[118:121], v[198:201], v[190:193], v[40:43]\n"
      "v_cvt_pk_bf16_f32 v115, v162, v163\n"
      "v_mul_f32_e64 v218, v103, v218\n"
      "v_mfma_f32_16x16x32_bf16 v[108:111], v[148:151], v[190:193], v[44:47]\n"
      "s_waitcnt lgkmcnt(2)\n"
      "v_mul_f32 v210, v214, v210\n"
      "v_mul_f32_e64 v211, v214, v211\n"
      "v_mfma_f32_16x16x32_bf16 v[128:131], v[112:115], v[190:193], v[48:51]\n"
      "ds_read_b128 v[154:157], v2 offset:30720\n"
      "v_cvt_pk_bf16_f32 v217, v218, v219\n"
      "v_mfma_f32_16x16x32_bf16 v[186:189], v[182:185], v[190:193], v[24:27]\n"
      "v_mul_f32_e64 v165, v214, v165\n"
      "v_cvt_pk_bf16_f32 v77, v176, v177\n"
      "v_mfma_f32_16x16x16_bf16 v[118:121], v[122:123], v[100:101], v[118:121]\n"
      "v_mul_f32 v167, v214, v167\n"
      "v_mul_f32 v212, v214, v212\n"
      "v_mul_f32_e64 v213, v214, v213\n"
      "v_mfma_f32_16x16x16_bf16 v[108:111], v[152:153], v[100:101], v[108:111]\n"
      "v_cvt_pk_bf16_f32 v196, v168, v169\n"
      "v_cvt_pk_bf16_f32 v210, v210, v211\n"
      "v_cvt_pk_bf16_f32 v85, v126, v127\n"
      "v_mfma_f32_16x16x16_bf16 v[128:131], v[216:217], v[100:101], v[128:131]\n"
      "v_cvt_pk_bf16_f32 v164, v164, v165\n"
      "v_cvt_pk_bf16_f32 v145, v146, v147\n"
      "v_max3_f32 v126, v118, v119, v120\n"
      "v_mfma_f32_16x16x16_bf16 v[186:189], v[116:117], v[100:101], v[186:189]\n"
      "v_max3_f32 v126, v126, v110, v111\n"
      "v_cvt_pk_bf16_f32 v211, v212, v213\n"
      "v_max3_f32 v100, v121, v108, v109\n"
      "v_mfma_f32_16x16x32_bf16 v[172:175], a[228:231], v[206:209], v[172:175]\n"
      "s_waitcnt lgkmcnt(0)\n"
      "v_max3_f32 v100, v100, v128, v129\n"
      "v_max3_f32 v126, v126, v130, v131\n"
      "v_mfma_f32_16x16x32_bf16 v[172:175], a[232:235], v[154:157], v[172:175]\n"
      "v_max3_f32 v126, v126, v100, v186\n"
      "v_mov_b32 v100, v126\n"
      "v_mfma_f32_16x16x32_bf16 v[172:175], a[236:239], v[158:161], v[172:175]\n"
      "s_nop 0\n"
      "v_permlane16_swap_b32 v126, v100\n"
      "v_mfma_f32_16x16x32_bf16 v[220:223], a[252:255], v[206:209], v[220:223]\n"
      "v_max_f32 v126, v126, v100\n"
      "v_mov_b32 v100, v126\n"
      "v_mfma_f32_16x16x32_bf16 v[220:223], v[224:227], v[154:157], v[220:223]\n"
      "s_nop 0\n"
      "v_permlane32_swap_b32 v126, v100\n"
      "v_mfma_f32_16x16x32_bf16 v[220:223], v[228:231], v[158:161], v[220:223]\n"
      "v_max_f32 v126, v126, v100\n"
      "v_sub_f32 v119, v119, v126\n"
      "v_mfma_f32_16x16x32_bf16 v[140:143], v[244:247], v[206:209], v[140:143]\n"
      "ds_read_b128 v[100:103], v2 offset:18688\n"
      "v_sub_f32 v118, v118, v126\n"
      "v_mfma_f32_16x16x32_bf16 v[140:143], v[248:251], v[154:157], v[140:143]\n"
      "v_exp_f32 v118, v118\n"
      "v_mfma_f32_16x16x32_bf16 v[140:143], v[252:255], v[158:161], v[140:143]\n"
      "v_exp_f32_e64 v119, v119\n"
      "v_mfma_f32_16x16x32_bf16 v[154:157], v[198:201], v[202:205], v[52:55]\n"
      "ds_read_b128 v[168:171], v2 offset:24832\n"
      "v_sub_f32 v186, v186, v126\n"
      "s_waitcnt vmcnt(1)\n"
      "v_or3_b32 v136, v136, v137, v138\n"
      "v_exp_f32_e64 v186, v186\n"
      "v_pk_mul_f32 v[178:179], v[178:179], v[214:215] op_sel_hi:[1,0]\n"
      "v_pk_add_f32 v[120:121], v[120:121], v[126:127] op_sel_hi:[1,0] neg_lo:[0,1] neg_hi:[0,1]\n"
      "v_pk_add_f32 v[110:111], v[110:111], v[126:127] op_sel_hi:[1,0] neg_lo:[0,1] neg_hi:[0,1]\n"
      "v_pk_mul_f32 v[180:181], v[180:181], v[214:215] op_sel_hi:[1,0]\n"
      "v_pk_add_f32 v[128:129], v[128:129], v[126:127] op_sel_hi:[1,0] neg_lo:[0,1] neg_hi:[0,1]\n"
      "v_pk_add_f32 v[108:109], v[108:109], v[126:127] op_sel_hi:[1,0] neg_lo:[0,1] neg_hi:[0,1]\n"
      "v_pk_add_f32 v[130:131], v[130:131], v[126:127] op_sel_hi:[1,0] neg_lo:[0,1] neg_hi:[0,1]\n"
      "v_mfma_f32_16x16x32_bf16 v[124:127], v[148:151], v[202:205], v[56:59]\n"
      "ds_read_b128 v[158:161], v2 offset:21760\n"
      "v_cvt_pk_bf16_f32 v213, v180, v181\n"
      "v_mfma_f32_16x16x32_bf16 v[188:191], v[112:115], v[202:205], v[60:63]\n"
      "v_exp_f32 v130, v130\n"
      "v_mfma_f32_16x16x32_bf16 v[132:135], v[182:185], v[202:205], v[26:29]\n"
      "s_waitcnt lgkmcnt(2)\n"
      "v_exp_f32_e64 v128, v128\n"
      "v_mfma_f32_16x16x16_bf16 v[154:157], v[122:123], v[144:145], v[154:157]\n"
      "v_cvt_pk_bf16_f32 v90, v186, 0\n"
      "v_exp_f32 v111, v111\n"
      "v_mfma_f32_16x16x16_bf16 v[124:127], v[152:153], v[144:145], v[124:127]\n"
      "v_exp_f32 v120, v120\n"
      "v_cvt_pk_bf16_f32 v165, v166, v167\n"
      "v_mfma_f32_16x16x16_bf16 v[188:191], v[216:217], v[144:145], v[188:191]\n"
      "v_exp_f32_e64 v109, v109\n"
      "v_cvt_pk_bf16_f32 v118, v118, v119\n"
      "v_mfma_f32_16x16x16_bf16 v[132:135], v[116:117], v[144:145], v[132:135]\n"
      "v_cvt_pk_bf16_f32 v212, v178, v179\n"
      "v_exp_f32 v121, v121\n"
      "v_mfma_f32_16x16x32_bf16 v[206:209], a[216:219], v[100:103], 0\n"
      "s_waitcnt lgkmcnt(0)\n"
      "v_cvt_pk_bf16_f32 v119, v120, v121\n"
      "v_max3_f32 v167, v154, v155, v156\n"
      "v_mfma_f32_16x16x32_bf16 v[206:209], a[220:223], v[158:161], v[206:209]\n"
      "v_exp_f32_e64 v108, v108\n"
      "v_mfma_f32_16x16x32_bf16 v[206:209], a[224:227], v[168:171], v[206:209]\n"
      "v_exp_f32_e64 v110, v110\n"
      "v_mfma_f32_16x16x32_bf16 v[176:179], a[240:243], v[100:103], 0\n"
      "v_cvt_pk_bf16_f32 v120, v108, v109\n"
      "v_cvt_pk_bf16_f32 v121, v110, v111\n"
      "v_mfma_f32_16x16x32_bf16 v[176:179], a[244:247], v[158:161], v[176:179]\n"
      "v_exp_f32_e64 v131, v131\n"
      "v_mfma_f32_16x16x32_bf16 v[176:179], a[248:251], v[168:171], v[176:179]\n"
      "v_exp_f32_e64 v129, v129\n"
      "v_mfma_f32_16x16x32_bf16 v[108:111], v[232:235], v[100:103], 0\n"
      "v_max3_f32 v144, v157, v124, v125\n"
      "v_max3_f32 v144, v144, v188, v189\n"
      "v_mfma_f32_16x16x32_bf16 v[108:111], v[236:239], v[158:161], v[108:111]\n"
      "v_cvt_pk_bf16_f32 v88, v128, v129\n"
      "v_max3_f32 v167, v167, v126, v127\n"
      "v_mfma_f32_16x16x32_bf16 v[108:111], v[240:243], v[168:171], v[108:111]\n"
      "v_max3_f32 v167, v167, v190, v191\n"
      "v_cvt_pk_bf16_f32 v89, v130, v131\n"
      "v_mfma_f32_16x16x32_bf16 v[168:171], v[32:35], v[118:121], 0\n"
      "ds_read_u16 v86, v11 offset:64\n"
      "v_fma_f32 v214, v172, v172, s52\n"
      "v_mfma_f32_16x16x32_bf16 v[168:171], v[32:35], v[88:91], v[168:171]\n"
      "v_max3_f32 v167, v167, v144, v132\n"
      "v_mov_b32 v131, v167\n"
      "v_mfma_f32_16x16x32_bf16 v[144:147], v[194:197], v[118:121], 0\n"
      "s_nop 0\n"
      "v_permlane16_swap_b32 v167, v131\n"
      "v_mfma_f32_16x16x32_bf16 v[144:147], v[76:79], v[88:91], v[144:147]\n"
      "ds_read_b128 v[160:163], v2 offset:27904\n"
      "s_waitcnt vmcnt(0)\n"
      "v_or3_b32 v136, v136, v139, v93\n"
      "v_mfma_f32_16x16x32_bf16 v[202:205], v[104:107], v[118:121], 0\n"
      "ds_write_b32 v31, v136 offset:4608\n"
      "v_mfma_f32_16x16x32_bf16 v[202:205], v[80:83], v[88:91], v[202:205]\n"
      "v_max_f32 v167, v167, v131\n"
      "v_mov_b32 v131, v167\n"
      "v_mfma_f32_16x16x32_bf16 v[136:139], v[96:99], v[118:121], 0\n"
      "s_waitcnt lgkmcnt(2)\n"
      "v_rcp_f32 v168, v168\n"
      "v_mfma_f32_16x16x32_bf16 v[136:139], v[84:87], v[88:91], v[136:139]\n"
      "ds_read_b128 v[118:121], v2 offset:30976\n"
      "v_permlane32_swap_b32 v167, v131\n"
      "v_max_f32 v167, v167, v131\n"
      "v_mul_f32_e64 v215, v173, v173\n"
      "v_pk_add_f32 v[154:155], v[154:155], v[166:167] op_sel:[0,1] neg_lo:[0,1] neg_hi:[0,1]\n"
      "v_pk_mul_f32 v[202:203], v[202:203], v[168:169] op_sel_hi:[1,0]\n"
      "v_pk_mul_f32 v[146:147], v[146:147], v[168:169] op_sel_hi:[1,0]\n"
      "v_pk_mul_f32 v[144:145], v[144:145], v[168:169] op_sel_hi:[1,0]\n"
      "v_sub_f32_e64 v132, v132, v167\n"
      "v_pk_mul_f32 v[204:205], v[204:205], v[168:169] op_sel_hi:[1,0]\n"
      "v_cvt_pk_bf16_f32 v144, v144, v145\n"
      "v_pk_mul_f32 v[136:137], v[136:137], v[168:169] op_sel_hi:[1,0]\n"
      "v_pk_fma_f32 v[214:215], v[174:175], v[174:175], v[214:215]\n"
      "v_cvt_pk_bf16_f32 v145, v146, v147\n"
      "v_pk_add_f32 v[124:125], v[124:125], v[166:167] op_sel:[0,1] neg_lo:[0,1] neg_hi:[0,1]\n"
      "v_pk_add_f32 v[156:157], v[156:157], v[166:167] op_sel:[0,1] neg_lo:[0,1] neg_hi:[0,1]\n"
      "v_pk_mul_f32 v[138:139], v[138:139], v[168:169] op_sel_hi:[1,0]\n"
      "v_pk_add_f32 v[188:189], v[188:189], v[166:167] op_sel:[0,1] neg_lo:[0,1] neg_hi:[0,1]\n"
      "v_pk_add_f32 v[126:127], v[126:127], v[166:167] op_sel:[0,1] neg_lo:[0,1] neg_hi:[0,1]\n"
      "v_cvt_pk_bf16_f32 v147, v204, v205\n"
      "v_pk_add_f32 v[190:191], v[190:191], v[166:167] op_sel:[0,1] neg_lo:[0,1] neg_hi:[0,1]\n"
      "v_pk_fma_f32 v[214:215], v[220:221], v[220:221], v[214:215]\n"
      "v_mfma_f32_16x16x32_bf16 v[128:131], v[198:201], v[210:213], v[64:67]\n"
      "v_cvt_pk_bf16_f32 v136, v136, v137\n"
      "v_cvt_pk_bf16_f32 v146, v202, v203\n"
      "v_cvt_pk_bf16_f32 v137, v138, v139\n"
      "v_mfma_f32_16x16x32_bf16 v[202:205], v[148:151], v[210:213], v[68:71]\n"
      "v_exp_f32 v156, v156\n"
      "v_mfma_f32_16x16x32_bf16 v[100:103], v[112:115], v[210:213], v[72:75]\n"
      "s_waitcnt lgkmcnt(0)\n"
      "v_exp_f32_e64 v191, v191\n"
      "v_mfma_f32_16x16x32_bf16 v[166:169], v[182:185], v[210:213], v[28:31]\n"
      "ds_read_b128 v[210:213], v2 offset:34048\n"
      "v_exp_f32 v190, v190\n"
      "v_mfma_f32_16x16x16_bf16 v[128:131], v[122:123], v[164:165], v[128:131]\n"
      "v_exp_f32 v189, v189\n"
      "v_exp_f32 v154, v154\n"
      "v_mfma_f32_16x16x16_bf16 v[202:205], v[152:153], v[164:165], v[202:205]\n"
      "v_exp_f32 v126, v126\n"
      "v_cvt_pk_bf16_f32 v93, v190, v191\n"
      "v_mfma_f32_16x16x16_bf16 v[100:103], v[216:217], v[164:165], v[100:103]\n"
      "v_exp_f32 v155, v155\n"
      "v_exp_f32 v188, v188\n"
      "v_mfma_f32_16x16x16_bf16 v[166:169], v[116:117], v[164:165], v[166:169]\n"
      "v_cvt_pk_bf16_f32 v154, v154, v155\n"
      "v_cvt_pk_bf16_f32 v92, v188, v189\n"
      "v_max3_f32 v181, v128, v129, v130\n"
      "v_mfma_f32_16x16x32_bf16 v[206:209], a[228:231], v[160:163], v[206:209]\n"
      "v_exp_f32 v132, v132\n"
      "v_mfma_f32_16x16x32_bf16 v[206:209], a[232:235], v[118:121], v[206:209]\n"
      "s_waitcnt lgkmcnt(0)\n"
      "v_exp_f32 v127, v127\n"
      "v_mfma_f32_16x16x32_bf16 v[206:209], a[236:239], v[210:213], v[206:209]\n"
      "v_exp_f32 v125, v125\n"
      "v_mfma_f32_16x16x32_bf16 v[176:179], a[252:255], v[160:163], v[176:179]\n"
      "v_exp_f32_e64 v124, v124\n"
      "v_mfma_f32_16x16x32_bf16 v[176:179], v[224:227], v[118:121], v[176:179]\n"
      "v_exp_f32_e64 v157, v157\n"
      "v_mfma_f32_16x16x32_bf16 v[176:179], v[228:231], v[210:213], v[176:179]\n"
      "v_cvt_pk_bf16_f32 v155, v156, v157\n"
      "v_cvt_pk_bf16_f32 v94, v132, 0\n"
      "v_mfma_f32_16x16x32_bf16 v[108:111], v[244:247], v[160:163], v[108:111]\n"
      "v_max3_f32 v181, v181, v204, v205\n"
      "v_cvt_pk_bf16_f32 v156, v124, v125\n"
      "v_mfma_f32_16x16x32_bf16 v[108:111], v[248:251], v[118:121], v[108:111]\n"
      "v_cvt_pk_bf16_f32 v157, v126, v127\n"
      "v_max3_f32 v162, v131, v202, v203\n"
      "v_mfma_f32_16x16x32_bf16 v[108:111], v[252:255], v[210:213], v[108:111]\n"
      "ds_read_b128 v[118:121], v10\n"
      "v_max3_f32 v162, v162, v100, v101\n"
      "v_mfma_f32_16x16x32_bf16 v[168:171], v[32:35], v[154:157], 0\n"
      "ds_read_b128 v[132:135], v7 offset:18432\n"
      "v_max3_f32 v181, v181, v102, v103\n"
      "v_mfma_f32_16x16x32_bf16 v[168:171], v[32:35], v[92:95], v[168:171]\n"
      "v_max3_f32 v181, v181, v162, v166\n"
      "v_mov_b32_e64 v188, v181\n"
      "v_mfma_f32_16x16x32_bf16 v[162:165], v[194:197], v[154:157], 0\n"
      "s_nop 0\n"
      "v_permlane16_swap_b32 v181, v188\n"
      "v_mfma_f32_16x16x32_bf16 v[162:165], v[76:79], v[92:95], v[162:165]\n"
      "ds_read_b64 v[186:187], v10 offset:16\n"
      "v_max_f32_e64 v181, v181, v188\n"
      "v_mfma_f32_16x16x32_bf16 v[210:213], v[104:107], v[154:157], 0\n"
      "ds_write_b128 v3, v[144:147]\n"
      "v_mfma_f32_16x16x32_bf16 v[210:213], v[80:83], v[92:95], v[210:213]\n"
      "ds_read_b128 v[144:147], v2 offset:22016\n"
      "v_mov_b32 v188, v181\n"
      "v_mfma_f32_16x16x32_bf16 v[158:161], v[96:99], v[154:157], 0\n"
      "s_nop 0\n"
      "v_permlane32_swap_b32_e64 v181, v188\n"
      "v_mfma_f32_16x16x32_bf16 v[158:161], v[84:87], v[92:95], v[158:161]\n"
      "s_waitcnt lgkmcnt(3)\n"
      "v_rcp_f32 v168, v168\n"
      "v_mfma_f32_16x16x32_bf16 v[154:157], v[118:121], v[198:201], v[30:33]\n"
      "ds_read_b128 v[198:201], v2 offset:18944\n"
      "v_max_f32_e64 v181, v181, v188\n"
      "v_mfma_f32_16x16x32_bf16 v[188:191], v[118:121], v[148:151], v[30:33]\n"
      "buffer_store_dwordx4 v[132:135], v20, s[24:27], s46 offen sc1\n"
      "v_pk_add_f32 v[100:101], v[100:101], v[180:181] op_sel:[0,1] neg_lo:[0,1] neg_hi:[0,1]\n"
      "v_pk_add_f32 v[130:131], v[130:131], v[180:181] op_sel:[0,1] neg_lo:[0,1] neg_hi:[0,1]\n"
      "v_pk_add_f32 v[202:203], v[202:203], v[180:181] op_sel:[0,1] neg_lo:[0,1] neg_hi:[0,1]\n"
      "v_pk_mul_f32 v[164:165], v[164:165], v[168:169] op_sel_hi:[1,0]\n"
      "v_pk_add_f32 v[128:129], v[128:129], v[180:181] op_sel:[0,1] neg_lo:[0,1] neg_hi:[0,1]\n"
      "v_pk_mul_f32 v[162:163], v[162:163], v[168:169] op_sel_hi:[1,0]\n"
      "v_pk_add_f32 v[102:103], v[102:103], v[180:181] op_sel:[0,1] neg_lo:[0,1] neg_hi:[0,1]\n"
      "v_pk_mul_f32 v[160:161], v[160:161], v[168:169] op_sel_hi:[1,0]\n"
      "v_pk_mul_f32 v[212:213], v[212:213], v[168:169] op_sel_hi:[1,0]\n"
      "v_pk_fma_f32 v[214:215], v[222:223], v[222:223], v[214:215]\n"
      "v_pk_add_f32 v[204:205], v[204:205], v[180:181] op_sel:[0,1] neg_lo:[0,1] neg_hi:[0,1]\n"
      "v_pk_mul_f32 v[210:211], v[210:211], v[168:169] op_sel_hi:[1,0]\n"
      "v_mfma_f32_16x16x32_bf16 v[124:127], v[118:121], v[112:115], v[30:33]\n"
      "s_waitcnt lgkmcnt(0)\n"
      "v_exp_f32 v100, v100\n"
      "v_mfma_f32_16x16x32_bf16 v[112:115], v[118:121], v[182:185], v[30:33]\n"
      "ds_read_b128 v[118:121], v7 offset:22528\n"
      "v_sub_f32_e64 v166, v166, v181\n"
      "v_mfma_f32_16x16x16_bf16 v[154:157], v[186:187], v[122:123], v[154:157]\n"
      "ds_read_b128 v[180:183], v2 offset:25088\n"
      "v_fma_f32 v184, v206, v206, s52\n"
      "v_mfma_f32_16x16x16_bf16 v[188:191], v[186:187], v[152:153], v[188:191]\n"
      "v_exp_f32 v128, v128\n"
      "v_cvt_pk_bf16_f32 v162, v162, v163\n"
      "v_mfma_f32_16x16x16_bf16 v[124:127], v[186:187], v[216:217], v[124:127]\n"
      "v_exp_f32 v101, v101\n"
      "v_exp_f32 v166, v166\n"
      "v_exp_f32 v129, v129\n"
      "v_mfma_f32_16x16x16_bf16 v[112:115], v[186:187], v[116:117], v[112:115]\n"
      "v_mul_f32 v185, v207, v207\n"
      "v_cvt_pk_bf16_f32 v128, v128, v129\n"
      "v_exp_f32 v203, v203\n"
      "v_mfma_f32_16x16x32_bf16 v[132:135], a[216:219], v[198:201], 0\n"
      "ds_read_b128 v[150:153], v2 offset:28160\n"
      "v_cndmask_b32 v154, v154, v188, s[58:59]\n"
      "v_exp_f32 v131, v131\n"
      "v_mfma_f32_16x16x32_bf16 v[132:135], a[220:223], v[144:147], v[132:135]\n"
      "s_waitcnt lgkmcnt(1)\n"
      "v_cndmask_b32 v154, v154, v124, s[60:61]\n"
      "v_cndmask_b32 v154, v154, v112, s[62:63]\n"
      "v_mfma_f32_16x16x32_bf16 v[132:135], a[224:227], v[180:183], v[132:135]\n"
      "buffer_store_dwordx4 v[118:121], v21, s[24:27], s46 offen sc1\n"
      "s_nop 1\n"
      "v_mfma_f32_16x16x32_bf16 v[118:121], a[240:243], v[198:201], 0\n"
      "ds_read_b128 v[186:189], v7 offset:26624\n"
      "v_exp_f32 v204, v204\n"
      "v_mfma_f32_16x16x32_bf16 v[118:121], a[244:247], v[144:147], v[118:121]\n"
      "ds_read_b128 v[124:127], v2 offset:31232\n"
      "v_exp_f32_e64 v102, v102\n"
      "v_max_f32_dpp v155, v154, v154 row_ror:8 row_mask:0xf bank_mask:0xf\n"
      "v_cvt_pk_bf16_f32 v163, v164, v165\n"
      "v_pk_fma_f32 v[214:215], v[140:141], v[140:141], v[214:215]\n"
      "v_pk_fma_f32 v[184:185], v[208:209], v[208:209], v[184:185]\n"
      "v_max_f32_dpp v155, v155, v155 row_ror:4 row_mask:0xf bank_mask:0xf\n"
      "v_cvt_pk_bf16_f32 v165, v212, v213\n"
      "v_pk_fma_f32 v[184:185], v[176:177], v[176:177], v[184:185]\n"
      "v_max_f32_dpp v155, v155, v155 row_ror:2 row_mask:0xf bank_mask:0xf\n"
      "v_pk_fma_f32 v[184:185], v[178:179], v[178:179], v[184:185]\n"
      "v_cvt_pk_bf16_f32 v164, v210, v211\n"
      "v_pk_mul_f32 v[158:159], v[158:159], v[168:169] op_sel_hi:[1,0]\n"
      "v_mfma_f32_16x16x32_bf16 v[118:121], a[248:251], v[180:183], v[118:121]\n"
      "ds_read_b128 v[210:213], v2 offset:34304\n"
      "v_exp_f32_e64 v103, v103\n"
      "v_mfma_f32_16x16x32_bf16 v[168:171], v[232:235], v[198:201], 0\n"
      "v_exp_f32 v130, v130\n"
      "v_mfma_f32_16x16x32_bf16 v[168:171], v[236:239], v[144:147], v[168:171]\n"
      "s_waitcnt lgkmcnt(1)\n"
      "v_max_f32_dpp v155, v155, v155 row_ror:1 row_mask:0xf bank_mask:0xf\n"
      "v_exp_f32 v202, v202\n"
      "v_mfma_f32_16x16x32_bf16 v[168:171], v[240:243], v[180:183], v[168:171]\n"
      "buffer_store_dwordx4 v[186:189], v22, s[24:27], s46 offen sc1\n"
      "v_mfma_f32_16x16x32_bf16 v[132:135], a[228:231], v[150:153], v[132:135]\n"
      "v_max_f32_dpp v155, v155, v155 row_bcast:15 row_mask:0xa bank_mask:0xf\n"
      "v_cvt_pk_bf16_f32 v129, v130, v131\n"
      "v_mfma_f32_16x16x32_bf16 v[132:135], a[232:235], v[124:127], v[132:135]\n"
      "s_waitcnt lgkmcnt(0)\n"
      "v_max_f32_dpp v155, v155, v155 row_bcast:31 row_mask:0xc bank_mask:0xf\n"
      "v_exp_f32 v205, v205\n"
      "v_mfma_f32_16x16x32_bf16 v[132:135], a[236:239], v[210:213], v[132:135]\n"
      "v_readlane_b32 s72, v155, 63\n"
      "v_cvt_pk_bf16_f32 v131, v204, v205\n"
      "v_mfma_f32_16x16x32_bf16 v[118:121], a[252:255], v[150:153], v[118:121]\n"
      "ds_read_b128 v[144:147], v7 offset:30720\n"
      "v_cvt_pk_bf16_f32 v130, v202, v203\n"
      "v_subrev_f32 v154, s72, v154\n"
      "v_mfma_f32_16x16x32_bf16 v[118:121], v[224:227], v[124:127], v[118:121]\n"
      "v_cvt_pk_bf16_f32 v90, v166, 0\n"
      "v_fma_f32 v186, v132, v132, s52\n"
      "v_mfma_f32_16x16x32_bf16 v[118:121], v[228:231], v[210:213], v[118:121]\n"
      "ds_write_b128 v3, v[162:165] offset:256\n"
      "v_pk_fma_f32 v[184:185], v[108:109], v[108:109], v[184:185]\n"
      "v_pk_fma_f32 v[214:215], v[142:143], v[142:143], v[214:215]\n"
      "v_mul_f32_e64 v187, v133, v133\n"
      "v_mfma_f32_16x16x32_bf16 v[168:171], v[244:247], v[150:153], v[168:171]\n"
      "v_exp_f32 v154, v154\n"
      "v_mfma_f32_16x16x32_bf16 v[168:171], v[248:251], v[124:127], v[168:171]\n"
      "v_cvt_pk_bf16_f32 v88, v100, v101\n"
      "v_cvt_pk_bf16_f32 v89, v102, v103\n"
      "v_mfma_f32_16x16x32_bf16 v[168:171], v[252:255], v[210:213], v[168:171]\n"
      "s_waitcnt lgkmcnt(1)\n"
      "v_cvt_pk_bf16_f32 v158, v158, v159\n"
      "v_cvt_pk_bf16_f32 v154, v154, v154\n"
      "v_mfma_f32_16x16x32_bf16 v[162:165], v[32:35], v[128:131], 0\n"
      "ds_write_b16 v13, v154\n"
      "v_mfma_f32_16x16x32_bf16 v[162:165], v[32:35], v[88:91], v[162:165]\n"
      "buffer_store_dwordx4 v[144:147], v23, s[24:27], s46 offen sc1\n"
      "v_pk_fma_f32 v[186:187], v[134:135], v[134:135], v[186:187]\n"
      "v_pk_fma_f32 v[186:187], v[118:119], v[118:119], v[186:187]\n"
      "v_pk_fma_f32 v[184:185], v[110:111], v[110:111], v[184:185]\n"
      "v_mfma_f32_16x16x32_bf16 v[210:213], v[194:197], v[128:131], 0\n"
      "ds_write_b64 v4, v[136:137]\n"
      "v_mfma_f32_16x16x32_bf16 v[210:213], v[76:79], v[88:91], v[210:213]\n"
      "s_nop 0\n"
      "v_rcp_f32 v162, v162\n"
      "v_mfma_f32_16x16x32_bf16 v[152:155], v[104:107], v[128:131], 0\n"
      "ds_read_b128 v[180:183], v7 offset:34816\n"
      "v_add_f32_e64 v214, v214, v215\n"
      "v_mfma_f32_16x16x32_bf16 v[152:155], v[80:83], v[88:91], v[152:155]\n"
      "ds_read_b128 v[144:147], v14\n"
      "v_cvt_pk_bf16_f32 v159, v160, v161\n"
      "v_mfma_f32_16x16x32_bf16 v[202:205], v[96:99], v[128:131], 0\n"
      "ds_read_b128 v[198:201], v14 offset:64\n"
      "v_add_f32_e64 v184, v184, v185\n"
      "v_mfma_f32_16x16x32_bf16 v[202:205], v[84:87], v[88:91], v[202:205]\n"
      "ds_write_b64 v4, v[158:159] offset:256\n"
      "v_permlane16_swap_b32_e64 v214, v184\n"
      "v_pk_mul_f32 v[210:211], v[210:211], v[162:163] op_sel_hi:[1,0]\n"
      "v_pk_fma_f32 v[186:187], v[120:121], v[120:121], v[186:187]\n"
      "v_pk_mul_f32 v[212:213], v[212:213], v[162:163] op_sel_hi:[1,0]\n"
      "v_pk_mul_f32 v[152:153], v[152:153], v[162:163] op_sel_hi:[1,0]\n"
      "v_cvt_pk_bf16_f32 v210, v210, v211\n"
      "v_add_f32_e64 v214, v214, v184\n"
      "v_pk_fma_f32 v[186:187], v[168:169], v[168:169], v[186:187]\n"
      "v_cvt_pk_bf16_f32 v211, v212, v213\n"
      "v_pk_mul_f32 v[154:155], v[154:155], v[162:163] op_sel_hi:[1,0]\n"
      "v_pk_fma_f32 v[186:187], v[170:171], v[170:171], v[186:187]\n"
      "v_cvt_pk_bf16_f32 v212, v152, v153\n"
      "v_add_f32 v186, v186, v187\n"
      "v_cvt_pk_bf16_f32 v213, v154, v155\n"
      "v_mov_b32_e64 v184, v186\n"
      "s_add_u32 s42, s42, 1\n"
      "ds_write_b128 v3, v[210:213] offset:512\n"
      "v_permlane16_swap_b32 v186, v184\n"
      "v_add_f32 v186, v186, v184\n"
      "v_pk_mul_f32 v[202:203], v[202:203], v[162:163] op_sel_hi:[1,0]\n"
      "v_pk_mul_f32 v[204:205], v[204:205], v[162:163] op_sel_hi:[1,0]\n"
      "v_permlane32_swap_b32 v214, v186\n"
      "v_add_f32 v214, v214, v186\n"
      "s_and_b32 s86, s86, -4\n"
      "ds_write_b32 v31, v214\n"
      "v_cvt_pk_bf16_f32 v202, v202, v203\n"
      "v_cvt_pk_bf16_f32 v203, v204, v205\n"
      "s_waitcnt lgkmcnt(2)\n"
      "v_mfma_f32_16x16x32_bf16 v[162:165], v[144:147], v[32:35], 0\n"
      "ds_write_b64 v4, v[202:203] offset:512\n"
      "s_cmp_ge_u32 s95, 0x40000000\n"
      "s_cselect_b64 vcc, -1, s[56:57]\n"
      "s_and_b32 s5, s84, s8\n"
      "v_mfma_f32_16x16x32_bf16 v[162:165], v[198:201], v[32:35], v[162:165]\n"
      "buffer_store_dwordx4 v[180:183], v25, s[24:27], s46 offen sc1\n"
      "s_waitcnt lgkmcnt(0)\n"
      "s_barrier\n"
      "s_cbranch_vccz L_max1_%=\n"
      "L_cold_%=:\n"
      "L_disp1_%=:\n"
      "s_cmp_lt_u32 s95, 0x40000000\n"
      "s_cbranch_scc1 L_nofix_11_%=\n"
      "s_cmp_eq_u32 s95, -2\n"
      "s_cbranch_scc1 L_tail1_%=\n"
      "s_nop 2\n"
      "s_cmp_eq_u32 s84, 0x20000000\n"
      "s_cselect_b32 s66, 0, s66\n"
      "s_cselect_b32 s48, 0, s48\n"
      "s_lshr_b32 s92, s84, 30\n"
      "s_or_b32 s92, s92, s66\n"
      "s_cbranch_scc0 L_nf_12_%=\n"
      "s_bitcmp1_b32 s84, 31\n"
      "s_cselect_b32 s75, 100270464, 0\n"
      "s_bitcmp1_b32 s84, 30\n"
      "s_cselect_b32 s76, 196224, 0\n"
      "s_and_b32 s74, s84, 0x3fffffff\n"
      "s_cmp_ge_u32 s84, 0x40000000\n"
      "s_cselect_b32 s74, s74, 0\n"
      "s_cselect_b32 s84, 0, s84\n"
      "s_sub_u32 s74, s74, s66\n"
      "s_add_u32 s66, s66, s74\n"
      "s_sub_u32 s75, s75, s67\n"
      "s_add_u32 s67, s67, s75\n"
      "s_sub_u32 s76, s76, s68\n"
      "s_add_u32 s68, s68, s76\n"
      "v_add_u32 v16, s74, v16\n"
      "v_add_u32 v17, s74, v17\n"
      "v_add_u32 v18, s74, v18\n"
      "v_add_u32 v19, s74, v19\n"
      "v_add_u32 v15, s74, v15\n"
      "s_cmp_eq_u32 s75, 0\n"
      "s_cbranch_scc1 L_ax_13_%=\n"
      "v_bfe_i32 v155, v27, 0, 1\n"
      "v_and_b32 v155, s75, v155\n"
      "v_sub_u32 v16, v16, v155\n"
      "v_bfe_i32 v155, v27, 2, 1\n"
      "v_and_b32 v155, s75, v155\n"
      "v_sub_u32 v17, v17, v155\n"
      "v_bfe_i32 v155, v27, 4, 1\n"
      "v_and_b32 v155, s75, v155\n"
      "v_sub_u32 v18, v18, v155\n"
      "v_bfe_i32 v155, v27, 6, 1\n"
      "v_and_b32 v155, s75, v155\n"
      "v_sub_u32 v19, v19, v155\n"
      "v_bfe_i32 v155, v27, 8, 1\n"
      "v_and_b32 v155, s75, v155\n"
      "v_sub_u32 v15, v15, v155\n"
      "L_ax_13_%=:\n"
      "s_cmp_eq_u32 s76, 0\n"
      "s_cbranch_scc1 L_ax_14_%=\n"
      "v_bfe_i32 v155, v27, 1, 1\n"
      "v_and_b32 v155, s76, v155\n"
      "v_sub_u32 v16, v16, v155\n"
      "v_bfe_i32 v155, v27, 3, 1\n"
      "v_and_b32 v155, s76, v155\n"
      "v_sub_u32 v17, v17, v155\n"
      "v_bfe_i32 v155, v27, 5, 1\n"
      "v_and_b32 v155, s76, v155\n"
      "v_sub_u32 v18, v18, v155\n"
      "v_bfe_i32 v155, v27, 7, 1\n"
      "v_and_b32 v155, s76, v155\n"
      "v_sub_u32 v19, v19, v155\n"
      "v_bfe_i32 v155, v27, 9, 1\n"
      "v_and_b32 v155, s76, v155\n"
      "v_sub_u32 v15, v15, v155\n"
      "L_ax_14_%=:\n"
      "s_or_b32 s48, s66, s69\n"
      "s_cmp_lg_u32 s48, 0\n"
      "s_cselect_b32 s48, 0x40000000, 0\n"
      "L_nf_12_%=:\n"
      "s_lshr_b32 s92, s85, 30\n"
      "s_or_b32 s92, s92, s69\n"
      "s_cbranch_scc0 L_nf_15_%=\n"
      "s_bitcmp1_b32 s85, 31\n"
      "s_cselect_b32 s75, 100270464, 0\n"
      "s_bitcmp1_b32 s85, 30\n"
      "s_cselect_b32 s76, 196224, 0\n"
      "s_and_b32 s74, s85, 0x3fffffff\n"
      "s_cmp_ge_u32 s85, 0x40000000\n"
      "s_cselect_b32 s74, s74, 0\n"
      "s_cselect_b32 s85, 0, s85\n"
      "s_sub_u32 s74, s74, s69\n"
      "s_add_u32 s69, s69, s74\n"
      "s_sub_u32 s75, s75, s70\n"
      "s_add_u32 s70, s70, s75\n"
      "s_sub_u32 s76, s76, s71\n"
      "s_add_u32 s71, s71, s76\n"
      "v_add_u32 v20, s74, v20\n"
      "v_add_u32 v21, s74, v21\n"
      "v_add_u32 v22, s74, v22\n"
      "v_add_u32 v23, s74, v23\n"
      "v_add_u32 v25, s74, v25\n"
      "s_cmp_eq_u32 s75, 0\n"
      "s_cbranch_scc1 L_ax_16_%=\n"
      "v_bfe_i32 v155, v27, 10, 1\n"
      "v_and_b32 v155, s75, v155\n"
      "v_sub_u32 v20, v20, v155\n"
      "v_bfe_i32 v155, v27, 12, 1\n"
      "v_and_b32 v155, s75, v155\n"
      "v_sub_u32 v21, v21, v155\n"
      "v_bfe_i32 v155, v27, 14, 1\n"
      "v_and_b32 v155, s75, v155\n"
      "v_sub_u32 v22, v22, v155\n"
      "v_bfe_i32 v155, v27, 16, 1\n"
      "v_and_b32 v155, s75, v155\n"
      "v_sub_u32 v23, v23, v155\n"
      "v_bfe_i32 v155, v27, 18, 1\n"
      "v_and_b32 v155, s75, v155\n"
      "v_sub_u32 v25, v25, v155\n"
      "L_ax_16_%=:\n"
      "s_cmp_eq_u32 s76, 0\n"
      "s_cbranch_scc1 L_ax_17_%=\n"
      "v_bfe_i32 v155, v27, 11, 1\n"
      "v_and_b32 v155, s76, v155\n"
      "v_sub_u32 v20, v20, v155\n"
      "v_bfe_i32 v155, v27, 13, 1\n"
      "v_and_b32 v155, s76, v155\n"
      "v_sub_u32 v21, v21, v155\n"
      "v_bfe_i32 v155, v27, 15, 1\n"
      "v_and_b32 v155, s76, v155\n"
      "v_sub_u32 v22, v22, v155\n"
      "v_bfe_i32 v155, v27, 17, 1\n"
      "v_and_b32 v155, s76, v155\n"
      "v_sub_u32 v23, v23, v155\n"
      "v_bfe_i32 v155, v27, 19, 1\n"
      "v_and_b32 v155, s76, v155\n"
      "v_sub_u32 v25, v25, v155\n"
      "L_ax_17_%=:\n"
      "s_or_b32 s48, s66, s69\n"
      "s_cmp_lg_u32 s48, 0\n"
      "s_cselect_b32 s48, 0x40000000, 0\n"
      "L_nf_15_%=:\n"
      "s_and_b32 s5, s84, s8\n"
      "s_sub_u32 s92, s42, 1\n"
      "s_cmp_gt_u32 s92, s44\n"
      "s_cbranch_scc1 L_tail1_%=\n"
      "L_nofix_11_%=:\n"
      "s_cmp_lg_u64 s[56:57], 0\n"
      "s_cbranch_scc1 L_slow1_%=\n"
      "L_sel1_%=:\n"
      "s_cmp_lg_u64 s[54:55], 0\n"
      "s_cbranch_scc1 L_max1_%=\n"
      "s_branch L_loop1_%=\n"
      "L_slow1_%=:\n"
      "s_nop 4\n"
      "v_mfma_f32_16x16x32_bf16 v[152:155], v[144:147], v[194:197], 0\n"
      "ds_read_b128 v[126:129], v0 offset:18432\n"
      "s_add_u32 s72, s42, 1\n"
      "v_mfma_f32_16x16x32_bf16 v[152:155], v[198:201], v[76:79], v[152:155]\n"
      "ds_read_b128 v[158:161], v9\n"
      "v_readlane_b32 s77, v38, s72\n"
      "v_mfma_f32_16x16x32_bf16 v[210:213], v[144:147], v[104:107], 0\n"
      "ds_read_b128 v[184:187], v0 offset:24576\n"
      "s_mov_b32 m0, 110592\n"
      "v_mfma_f32_16x16x32_bf16 v[210:213], v[198:201], v[80:83], v[210:213]\n"
      "ds_read_b128 v[100:103], v1 offset:30720\n"
      "s_and_b32 s73, s77, -16\n"
      "v_mfma_f32_16x16x32_bf16 v[180:183], v[144:147], v[96:99], 0\n"
      "ds_read_b128 v[114:117], v1 offset:18432\n"
      "v_subrev_u32 v196, 115456, v9\n"
      "v_mfma_f32_16x16x32_bf16 v[180:183], v[198:201], v[84:87], v[180:183]\n"
      "buffer_load_dwordx4 v196, s[28:31], s73 offen  lds\n"
      "s_waitcnt lgkmcnt(4)\n"
      "v_mfma_f32_16x16x32_bf16 v[212:215], a[0:3], v[126:129], 0\n"
      "ds_read_b128 v[154:157], v1 offset:24576\n"
      "v_readlane_b32 s45, v37, s43\n"
      "v_mfma_f32_16x16x32_bf16 v[122:125], a[24:27], v[126:129], 0\n"
      "s_waitcnt lgkmcnt(4)\n"
      "v_add_f32 v158, v158, v159\n"
      "v_add_f32_e64 v160, v160, v161\n"
      "v_mfma_f32_16x16x32_bf16 v[164:167], a[48:51], v[126:129], 0\n"
      "s_add_u32 s73, s73, 1024\n"
      "v_add_f32_e64 v158, v158, v160\n"
      "v_mov_b32_dpp v162, v162 quad_perm:[0,0,0,0] row_mask:0xf bank_mask:0xf\n"
      "v_mfma_f32_16x16x32_bf16 v[198:201], a[72:75], v[126:129], 0\n"
      "s_mov_b32 m0, 111616\n"
      "v_rsq_f32 v158, v158\n"
      "v_mfma_f32_16x16x32_bf16 v[202:205], a[96:99], v[126:129], 0\n"
      "s_waitcnt lgkmcnt(1)\n"
      "v_mul_f32 v158, 0x415db3d7, v158\n"
      "v_mov_b32_e64 v160, v158\n"
      "v_mfma_f32_16x16x32_bf16 v[144:147], a[120:123], v[126:129], 0\n"
      "ds_read_b128 v[136:139], v0 offset:30720\n"
      "v_subrev_u32 v77, 115456, v9\n"
      "v_mfma_f32_16x16x32_bf16 v[212:215], a[4:7], v[114:117], v[212:215]\n"
      "buffer_load_dwordx4 v196, s[28:31], s73 offen  lds\n"
      "v_mfma_f32_16x16x32_bf16 v[212:215], a[8:11], v[184:187], v[212:215]\n"
      "s_mov_b32 m0, 112640\n"
      "v_permlane32_swap_b32 v158, v160\n"
      "v_mfma_f32_16x16x32_bf16 v[122:125], a[28:31], v[114:117], v[122:125]\n"
      "v_mul_f32 v133, v160, v133\n"
      "v_bfe_u32 v85, v77, 4, 4\n"
      "v_mfma_f32_16x16x32_bf16 v[122:125], a[32:35], v[184:187], v[122:125]\n"
      "s_add_u32 s73, s73, 1024\n"
      "v_mul_f32 v118, v160, v118\n"
      "v_mov_b32 v159, v158\n"
      "v_mfma_f32_16x16x32_bf16 v[164:167], a[52:55], v[114:117], v[164:167]\n"
      "s_nop 0\n"
      "v_permlane16_swap_b32 v158, v159\n"
      "v_mfma_f32_16x16x32_bf16 v[164:167], a[56:59], v[184:187], v[164:167]\n"
      "v_subrev_u32 v130, 115456, v9\n"
      "v_mul_f32_e64 v175, v158, v175\n"
      "v_mfma_f32_16x16x32_bf16 v[198:201], a[76:79], v[114:117], v[198:201]\n"
      "ds_read_b128 v[188:191], v9 offset:4608\n"
      "v_mul_f32_e64 v221, v158, v221\n"
      "v_mfma_f32_16x16x32_bf16 v[198:201], a[80:83], v[184:187], v[198:201]\n"
      "buffer_load_dwordx4 v196, s[28:31], s73 offen  lds\n"
      "v_mfma_f32_16x16x32_bf16 v[202:205], a[100:103], v[114:117], v[202:205]\n"
      "s_waitcnt lgkmcnt(1)\n"
      "v_mul_f32 v172, v158, v172\n"
      "v_mul_f32_e64 v174, v158, v174\n"
      "v_mfma_f32_16x16x32_bf16 v[202:205], a[104:107], v[184:187], v[202:205]\n"
      "s_add_u32 s73, s73, 1024\n"
      "v_mul_f32 v173, v158, v173\n"
      "v_mul_f32 v108, v159, v108\n"
      "v_mfma_f32_16x16x32_bf16 v[144:147], a[124:127], v[114:117], v[144:147]\n"
      "s_mov_b32 m0, 113664\n"
      "v_mul_f32 v223, v158, v223\n"
      "v_mul_f32 v209, v159, v209\n"
      "v_mfma_f32_16x16x32_bf16 v[144:147], a[128:131], v[184:187], v[144:147]\n"
      "ds_read_b128 v[216:219], v0 offset:32768\n"
      "v_mul_f32_e64 v222, v158, v222\n"
      "v_mfma_f32_16x16x32_bf16 v[212:215], a[12:15], v[154:157], v[212:215]\n"
      "buffer_load_dwordx4 v196, s[28:31], s73 offen  lds\n"
      "v_mfma_f32_16x16x32_bf16 v[212:215], a[16:19], v[136:139], v[212:215]\n"
      "s_add_u32 s73, s73, 1024\n"
      "v_cvt_pk_bf16_f32 v172, v172, v173\n"
      "v_cvt_pk_bf16_f32 v173, v174, v175\n"
      "v_mfma_f32_16x16x32_bf16 v[212:215], a[20:23], v[100:103], v[212:215]\n"
      "s_mov_b32 m0, 114688\n"
      "v_mul_f32 v220, v158, v220\n"
      "v_mul_f32 v179, v159, v179\n"
      "v_mfma_f32_16x16x32_bf16 v[122:125], a[36:39], v[154:157], v[122:125]\n"
      "s_mov_b32 exec_hi, 0xffff\n"
      "buffer_load_dwordx4 v196, s[28:31], s73 offen  lds\n"
      "s_mov_b32 exec_hi, -1\n"
      "v_mfma_f32_16x16x32_bf16 v[122:125], a[40:43], v[136:139], v[122:125]\n"
      "v_cvt_pk_bf16_f32 v174, v220, v221\n"
      "v_cvt_pk_bf16_f32 v175, v222, v223\n"
      "v_mfma_f32_16x16x32_bf16 v[122:125], a[44:47], v[100:103], v[122:125]\n"
      "v_mul_f32 v178, v159, v178\n"
      "v_fma_f32 v76, v212, v212, s51\n"
      "v_mfma_f32_16x16x32_bf16 v[164:167], a[60:63], v[154:157], v[164:167]\n"
      "ds_write_b128 v5, v[172:175] offset:18432\n"
      "v_pk_mul_f32 v[140:141], v[140:141], v[158:159] op_sel_hi:[1,0]\n"
      "v_pk_mul_f32 v[142:143], v[142:143], v[158:159] op_sel_hi:[1,0]\n"
      "v_pk_mul_f32 v[206:207], v[206:207], v[158:159] op_sel:[0,1]\n"
      "v_mfma_f32_16x16x32_bf16 v[164:167], a[64:67], v[136:139], v[164:167]\n"
      "s_add_u32 m0, s64, 0\n"
      "v_cvt_pk_bf16_f32 v140, v140, v141\n"
      "v_mul_f32 v111, v159, v111\n"
      "v_mfma_f32_16x16x32_bf16 v[164:167], a[68:71], v[100:103], v[164:167]\n"
      "buffer_load_dwordx4 v16, s[16:19], s84 offen  lds\n"
      "v_mfma_f32_16x16x32_bf16 v[198:201], a[84:87], v[154:157], v[198:201]\n"
      "ds_read_b128 v[172:175], v1 offset:26624\n"
      "v_mul_f32_e64 v208, v159, v208\n"
      "v_mfma_f32_16x16x32_bf16 v[198:201], a[88:91], v[136:139], v[198:201]\n"
      "ds_read_b128 v[220:223], v1 offset:20480\n"
      "v_cvt_pk_bf16_f32 v141, v142, v143\n"
      "v_mfma_f32_16x16x32_bf16 v[198:201], a[92:95], v[100:103], v[198:201]\n"
      "ds_write_b64 v6, v[140:141] offset:18432\n"
      "v_mfma_f32_16x16x32_bf16 v[202:205], a[108:111], v[154:157], v[202:205]\n"
      "v_fmac_f32 v76, v213, v213\n"
      "v_mul_f32 v177, v159, v177\n"
      "v_mfma_f32_16x16x32_bf16 v[202:205], a[112:115], v[136:139], v[202:205]\n"
      "v_cvt_pk_bf16_f32 v206, v206, v207\n"
      "v_mul_f32 v110, v159, v110\n"
      "v_mfma_f32_16x16x32_bf16 v[202:205], a[116:119], v[100:103], v[202:205]\n"
      "s_add_u32 m0, s64, 4096\n"
      "v_rcp_f32 v162, v162\n"
      "v_mfma_f32_16x16x32_bf16 v[144:147], a[132:135], v[154:157], v[144:147]\n"
      "buffer_load_dwordx4 v17, s[16:19], s84 offen  lds\n"
      "v_mfma_f32_16x16x32_bf16 v[144:147], a[136:139], v[136:139], v[144:147]\n"
      "ds_read_b128 v[140:143], v0 offset:20480\n"
      "v_mul_f32_e64 v109, v159, v109\n"
      "v_mfma_f32_16x16x32_bf16 v[144:147], a[140:143], v[100:103], v[144:147]\n"
      "ds_read_b128 v[148:151], v0 offset:26624\n"
      "v_readlane_b32 s46, v37, s42\n"
      "v_mfma_f32_16x16x32_bf16 v[194:197], v[126:129], a[144:147], 0\n"
      "v_cndmask_b32 v152, v152, v210, s[58:59]\n"
      "v_cvt_pk_bf16_f32 v207, v208, v209\n"
      "v_mfma_f32_16x16x32_bf16 v[194:197], v[114:117], a[148:151], v[194:197]\n"
      "s_add_u32 m0, s64, 8192\n"
      "v_mul_f32_e64 v176, v159, v176\n"
      "v_cvt_pk_bf16_f32 v209, v178, v179\n"
      "v_mfma_f32_16x16x32_bf16 v[194:197], v[184:187], a[152:155], v[194:197]\n"
      "buffer_load_dwordx4 v18, s[16:19], s84 offen  lds\n"
      "v_mfma_f32_16x16x32_bf16 v[194:197], v[154:157], a[156:159], v[194:197]\n"
      "s_add_u32 m0, s64, 12288\n"
      "v_fma_f32 v158, v198, v198, s51\n"
      "v_fmac_f32 v158, v199, v199\n"
      "v_mfma_f32_16x16x32_bf16 v[194:197], v[136:139], a[160:163], v[194:197]\n"
      "v_cvt_pk_bf16_f32 v108, v108, v109\n"
      "v_fmac_f32 v158, v200, v200\n"
      "v_mfma_f32_16x16x32_bf16 v[194:197], v[100:103], a[164:167], v[194:197]\n"
      "v_cndmask_b32 v152, v152, v180, s[60:61]\n"
      "v_mul_u32_u24 v78, 98, v85\n"
      "v_mfma_f32_16x16x32_bf16 v[104:107], v[126:129], a[168:171], 0\n"
      "v_cvt_pk_bf16_f32 v208, v176, v177\n"
      "v_mul_f32 v171, v160, v171\n"
      "v_mfma_f32_16x16x32_bf16 v[104:107], v[114:117], a[172:175], v[104:107]\n"
      "buffer_load_dwordx4 v19, s[16:19], s84 offen  lds\n"
      "v_mfma_f32_16x16x32_bf16 v[104:107], v[184:187], a[176:179], v[104:107]\n"
      "v_fmac_f32 v76, v214, v214\n"
      "v_fmac_f32 v76, v215, v215\n"
      "v_mfma_f32_16x16x32_bf16 v[104:107], v[154:157], a[180:183], v[104:107]\n"
      "v_cvt_pk_bf16_f32 v109, v110, v111\n"
      "v_mul_f32 v168, v160, v168\n"
      "v_mfma_f32_16x16x32_bf16 v[104:107], v[136:139], a[184:187], v[104:107]\n"
      "s_mov_b32 m0, s6\n"
      "v_fmac_f32 v158, v201, v201\n"
      "v_fmac_f32 v158, v202, v202\n"
      "v_mfma_f32_16x16x32_bf16 v[104:107], v[100:103], a[188:191], v[104:107]\n"
      "v_mul_f32 v119, v160, v119\n"
      "v_fmac_f32_e64 v158, v203, v203\n"
      "v_mfma_f32_16x16x32_bf16 v[96:99], v[126:129], a[192:195], 0\n"
      "buffer_load_dwordx4 v15, s[0:3], s5 offen  lds\n"
      "v_mfma_f32_16x16x32_bf16 v[96:99], v[114:117], a[196:199], v[96:99]\n"
      "ds_read_b128 v[176:179], v1 offset:32768\n"
      "v_mul_f32 v132, v160, v132\n"
      "v_mfma_f32_16x16x32_bf16 v[96:99], v[184:187], a[200:203], v[96:99]\n"
      "v_cvt_pk_bf16_f32 v104, v104, v105\n"
      "v_mul_f32 v121, v160, v121\n"
      "v_mfma_f32_16x16x32_bf16 v[96:99], v[154:157], a[204:207], v[96:99]\n"
      "ds_write_b128 v5, v[206:209] offset:24576\n"
      "v_pk_mul_f32 v[134:135], v[134:135], v[160:161] op_sel_hi:[1,0]\n"
      "v_mfma_f32_16x16x32_bf16 v[96:99], v[136:139], a[208:211], v[96:99]\n"
      "buffer_load_dwordx4 v[114:117], v36, s[28:31], s86 offen\n"
      "v_mfma_f32_16x16x32_bf16 v[96:99], v[100:103], a[212:215], v[96:99]\n"
      "s_waitcnt lgkmcnt(2)\n"
      "v_fmac_f32 v158, v204, v204\n"
      "v_mul_f32 v120, v160, v120\n"
      "v_mfma_f32_16x16x32_bf16 v[180:183], a[0:3], v[140:143], 0\n"
      "v_fmac_f32 v76, v122, v122\n"
      "v_cvt_pk_bf16_f32 v194, v194, v195\n"
      "v_mfma_f32_16x16x32_bf16 v[180:183], a[4:7], v[220:223], v[180:183]\n"
      "ds_write_b64 v6, v[108:109] offset:24576\n"
      "v_mfma_f32_16x16x32_bf16 v[180:183], a[8:11], v[148:151], v[180:183]\n"
      "v_fmac_f32 v76, v123, v123\n"
      "v_fmac_f32 v158, v205, v205\n"
      "v_mfma_f32_16x16x32_bf16 v[180:183], a[12:15], v[172:175], v[180:183]\n"
      "s_waitcnt lgkmcnt(1)\n"
      "v_fmac_f32 v76, v124, v124\n"
      "v_mul_f32_e64 v170, v160, v170\n"
      "v_mfma_f32_16x16x32_bf16 v[180:183], a[16:19], v[216:219], v[180:183]\n"
      "buffer_load_dword v86, v29, s[28:31], s86 offen\n"
      "v_mfma_f32_16x16x32_bf16 v[180:183], a[20:23], v[176:179], v[180:183]\n"
      "s_or_b32 s95, s45, s46\n"
      "v_fmac_f32 v158, v144, v144\n"
      "v_cvt_pk_bf16_f32 v96, v96, v97\n"
      "v_mfma_f32_16x16x32_bf16 v[206:209], a[24:27], v[140:143], 0\n"
      "ds_read_b128 v[108:111], v1 offset:22528\n"
      "v_fmac_f32_e64 v76, v125, v125\n"
      "v_mfma_f32_16x16x32_bf16 v[206:209], a[28:31], v[220:223], v[206:209]\n"
      "v_cvt_pk_bf16_f32 v132, v132, v133\n"
      "v_fma_f32 v159, v180, v180, s51\n"
      "v_mfma_f32_16x16x32_bf16 v[206:209], a[32:35], v[148:151], v[206:209]\n"
      "v_mul_f32 v152, v162, v152\n"
      "v_fmac_f32 v76, v164, v164\n"
      "v_mfma_f32_16x16x32_bf16 v[206:209], a[36:39], v[172:175], v[206:209]\n"
      "v_fmac_f32 v76, v165, v165\n"
      "v_fmac_f32 v158, v145, v145\n"
      "v_mfma_f32_16x16x32_bf16 v[206:209], a[40:43], v[216:219], v[206:209]\n"
      "ds_read_b128 v[154:157], v0 offset:22528\n"
      "v_fmac_f32 v158, v146, v146\n"
      "v_mfma_f32_16x16x32_bf16 v[206:209], a[44:47], v[176:179], v[206:209]\n"
      "v_fmac_f32 v158, v147, v147\n"
      "v_fmac_f32_e64 v76, v166, v166\n"
      "v_mfma_f32_16x16x32_bf16 v[100:103], a[48:51], v[140:143], 0\n"
      "v_fmac_f32_e64 v76, v167, v167\n"
      "v_cvt_pk_bf16_f32 v152, v152, v152\n"
      "v_mfma_f32_16x16x32_bf16 v[100:103], a[52:55], v[220:223], v[100:103]\n"
      "v_cvt_pk_bf16_f32 v195, v196, v197\n"
      "v_lshrrev_b32 v130, 3, v130\n"
      "v_mfma_f32_16x16x32_bf16 v[100:103], a[56:59], v[148:151], v[100:103]\n"
      "v_permlane16_swap_b32 v76, v158\n"
      "v_mfma_f32_16x16x32_bf16 v[100:103], a[60:63], v[172:175], v[100:103]\n"
      "v_add_f32 v76, v76, v158\n"
      "v_mov_b32 v158, v76\n"
      "v_mfma_f32_16x16x32_bf16 v[100:103], a[64:67], v[216:219], v[100:103]\n"
      "s_nop 0\n"
      "v_permlane32_swap_b32 v76, v158\n"
      "v_mfma_f32_16x16x32_bf16 v[100:103], a[68:71], v[176:179], v[100:103]\n"
      "v_lshrrev_b32 v77, 8, v77\n"
      "v_min_u32 v130, 96, v130\n"
      "v_mfma_f32_16x16x32_bf16 v[136:139], a[72:75], v[140:143], 0\n"
      "v_fmac_f32 v159, v181, v181\n"
      "v_readlane_b32 s47, v38, s43\n"
      "v_mfma_f32_16x16x32_bf16 v[136:139], a[76:79], v[220:223], v[136:139]\n"
      "v_or3_b32 v188, v188, v189, v190\n"
      "v_add_f32_e64 v76, v76, v158\n"
      "v_mfma_f32_16x16x32_bf16 v[136:139], a[80:83], v[148:151], v[136:139]\n"
      "ds_read_b128 v[126:129], v0 offset:28672\n"
      "v_fmac_f32 v159, v182, v182\n"
      "v_mfma_f32_16x16x32_bf16 v[136:139], a[84:87], v[172:175], v[136:139]\n"
      "s_and_b32 s77, s77, 15\n"
      "v_cvt_pk_bf16_f32 v97, v98, v99\n"
      "v_cvt_pk_bf16_f32 v105, v106, v107\n"
      "v_mfma_f32_16x16x32_bf16 v[136:139], a[88:91], v[216:219], v[136:139]\n"
      "v_rsq_f32 v76, v76\n"
      "v_mfma_f32_16x16x32_bf16 v[136:139], a[92:95], v[176:179], v[136:139]\n"
      "v_cvt_pk_bf16_f32 v133, v134, v135\n"
      "v_cvt_pk_bf16_f32 v134, v118, v119\n"
      "v_mfma_f32_16x16x32_bf16 v[184:187], a[96:99], v[140:143], 0\n"
      "v_cvt_pk_bf16_f32 v135, v120, v121\n"
      "v_fmac_f32 v159, v183, v183\n"
      "v_mfma_f32_16x16x32_bf16 v[184:187], a[100:103], v[220:223], v[184:187]\n"
      "s_or_b32 s95, s95, s48\n"
      "v_mov_b32 v158, v76\n"
      "v_fma_f32 v106, v136, v136, s51\n"
      "v_mfma_f32_16x16x32_bf16 v[184:187], a[104:107], v[148:151], v[184:187]\n"
      "v_permlane16_swap_b32 v76, v158\n"
      "v_mfma_f32_16x16x32_bf16 v[184:187], a[108:111], v[172:175], v[184:187]\n"
      "v_fmac_f32 v106, v137, v137\n"
      "v_mul_f32 v76, s50, v76\n"
      "v_mfma_f32_16x16x32_bf16 v[184:187], a[112:115], v[216:219], v[184:187]\n"
      "v_fmac_f32 v106, v138, v138\n"
      "v_fmac_f32 v159, v206, v206\n"
      "v_mfma_f32_16x16x32_bf16 v[184:187], a[116:119], v[176:179], v[184:187]\n"
      "v_mul_f32 v169, v160, v169\n"
      "v_fmac_f32 v159, v207, v207\n"
      "v_mfma_f32_16x16x32_bf16 v[160:163], a[120:123], v[140:143], 0\n"
      "v_mul_f32 v147, v158, v147\n"
      "v_fmac_f32 v159, v208, v208\n"
      "v_mfma_f32_16x16x32_bf16 v[160:163], a[124:127], v[220:223], v[160:163]\n"
      "ds_write_b128 v5, v[132:135] offset:30720\n"
      "v_mfma_f32_16x16x32_bf16 v[160:163], a[128:131], v[148:151], v[160:163]\n"
      "v_fmac_f32 v106, v139, v139\n"
      "v_mul_f32 v144, v158, v144\n"
      "v_mfma_f32_16x16x32_bf16 v[160:163], a[132:135], v[172:175], v[160:163]\n"
      "v_fmac_f32 v159, v209, v209\n"
      "v_fmac_f32 v106, v184, v184\n"
      "v_mfma_f32_16x16x32_bf16 v[160:163], a[136:139], v[216:219], v[160:163]\n"
      "v_mul_f32 v213, v76, v213\n"
      "v_cvt_pk_bf16_f32 v168, v168, v169\n"
      "v_mfma_f32_16x16x32_bf16 v[160:163], a[140:143], v[176:179], v[160:163]\n"
      "v_lshl_add_u32 v78, v77, 3, v78\n"
      "v_fmac_f32_e64 v106, v185, v185\n"
      "v_mfma_f32_16x16x32_bf16 v[132:135], v[140:143], a[144:147], 0\n"
      "v_mul_f32 v202, v158, v202\n"
      "v_mul_f32 v199, v158, v199\n"
      "v_mfma_f32_16x16x32_bf16 v[132:135], v[220:223], a[148:151], v[132:135]\n"
      "v_cvt_pk_bf16_f32 v169, v170, v171\n"
      "v_fmac_f32 v159, v100, v100\n"
      "v_mfma_f32_16x16x32_bf16 v[132:135], v[148:151], a[152:155], v[132:135]\n"
      "v_mul_f32 v212, v76, v212\n"
      "v_fmac_f32 v106, v186, v186\n"
      "v_mfma_f32_16x16x32_bf16 v[132:135], v[172:175], a[156:159], v[132:135]\n"
      "v_mul_f32 v167, v76, v167\n"
      "v_fmac_f32 v106, v187, v187\n"
      "v_mfma_f32_16x16x32_bf16 v[132:135], v[216:219], a[160:163], v[132:135]\n"
      "ds_write_b16 v12, v152\n"
      "v_mfma_f32_16x16x32_bf16 v[132:135], v[176:179], a[164:167], v[132:135]\n"
      "v_fmac_f32 v159, v101, v101\n"
      "v_lshlrev_b32 v77, 2, v77\n"
      "v_mfma_f32_16x16x32_bf16 v[118:121], v[140:143], a[168:171], 0\n"
      "v_fmac_f32 v106, v160, v160\n"
      "v_fmac_f32_e64 v106, v161, v161\n"
      "v_mfma_f32_16x16x32_bf16 v[118:121], v[220:223], a[172:175], v[118:121]\n"
      "ds_write_b64 v6, v[168:169] offset:30720\n"
      "v_mfma_f32_16x16x32_bf16 v[118:121], v[148:151], a[176:179], v[118:121]\n"
      "v_mul_f32 v145, v158, v145\n"
      "v_fmac_f32 v159, v102, v102\n"
      "v_mfma_f32_16x16x32_bf16 v[118:121], v[172:175], a[180:183], v[118:121]\n"
      "v_bitop3_b32 v188, v188, v191, s33 bitop3:0xa8\n"
      "v_cvt_pk_bf16_f32 v212, v212, v213\n"
      "v_mfma_f32_16x16x32_bf16 v[118:121], v[216:219], a[184:187], v[118:121]\n"
      "ds_read_b128 v[168:171], v1 offset:28672\n"
      "v_fmac_f32 v106, v162, v162\n"
      "v_mfma_f32_16x16x32_bf16 v[118:121], v[176:179], a[188:191], v[118:121]\n"
      "v_fmac_f32 v106, v163, v163\n"
      "v_fmac_f32 v159, v103, v103\n"
      "v_mfma_f32_16x16x32_bf16 v[190:193], v[140:143], a[192:195], 0\n"
      "v_mul_f32 v215, v76, v215\n"
      "v_mul_f32 v198, v158, v198\n"
      "v_mfma_f32_16x16x32_bf16 v[190:193], v[220:223], a[196:199], v[190:193]\n"
      "ds_read_b128 v[220:223], v0 offset:34816\n"
      "v_mul_f32 v165, v76, v165\n"
      "v_mfma_f32_16x16x32_bf16 v[190:193], v[148:151], a[200:203], v[190:193]\n"
      "s_waitcnt lgkmcnt(2)\n"
      "v_permlane16_swap_b32 v159, v106\n"
      "v_mfma_f32_16x16x32_bf16 v[190:193], v[172:175], a[204:207], v[190:193]\n"
      "ds_read_b128 v[172:175], v1 offset:34816\n"
      "v_add_f32_e64 v159, v159, v106\n"
      "v_mfma_f32_16x16x32_bf16 v[190:193], v[216:219], a[208:211], v[190:193]\n"
      "ds_read_b128 v[140:143], v7\n"
      "v_mov_b32 v106, v159\n"
      "v_mfma_f32_16x16x32_bf16 v[190:193], v[176:179], a[212:215], v[190:193]\n"
      "s_nop 0\n"
      "v_permlane32_swap_b32 v159, v106\n"
      "v_mfma_f32_16x16x32_bf16 v[150:153], a[0:3], v[154:157], 0\n"
      "v_add_f32 v159, v159, v106\n"
      "v_add_u32_e64 v130, s77, v130\n"
      "v_mfma_f32_16x16x32_bf16 v[150:153], a[4:7], v[108:111], v[150:153]\n"
      "v_rsq_f32 v159, v159\n"
      "v_mfma_f32_16x16x32_bf16 v[150:153], a[8:11], v[126:129], v[150:153]\n"
      "s_waitcnt lgkmcnt(2)\n"
      "v_cmp_ne_u32 s[56:57], 0, v188\n"
      "v_cvt_pk_bf16_f32 v198, v198, v199\n"
      "v_mfma_f32_16x16x32_bf16 v[150:153], a[12:15], v[168:171], v[150:153]\n"
      "s_waitcnt lgkmcnt(1)\n"
      "v_cvt_pk_bf16_f32 v107, v120, v121\n"
      "v_mov_b32 v106, v159\n"
      "v_mfma_f32_16x16x32_bf16 v[150:153], a[16:19], v[220:223], v[150:153]\n"
      "s_nop 0\n"
      "v_permlane16_swap_b32 v159, v106\n"
      "v_mfma_f32_16x16x32_bf16 v[150:153], a[20:23], v[172:175], v[150:153]\n"
      "v_sub_u32 v85, v85, v77\n"
      "v_mul_f32 v164, v76, v164\n"
      "v_mfma_f32_16x16x32_bf16 v[176:179], a[24:27], v[154:157], 0\n"
      "v_cvt_pk_bf16_f32 v197, v134, v135\n"
      "v_mul_f32 v159, s50, v159\n"
      "v_mfma_f32_16x16x32_bf16 v[176:179], a[28:31], v[108:111], v[176:179]\n"
      "s_and_b32 s47, s47, -4\n"
      "v_mul_f32 v146, v158, v146\n"
      "v_mul_f32 v185, v106, v185\n"
      "v_mfma_f32_16x16x32_bf16 v[176:179], a[32:35], v[126:129], v[176:179]\n"
      "s_waitcnt lgkmcnt(0)\n"
      "v_cvt_pk_bf16_f32 v196, v132, v133\n"
      "v_mul_f32 v214, v76, v214\n"
      "v_mfma_f32_16x16x32_bf16 v[176:179], a[36:39], v[168:171], v[176:179]\n"
      "buffer_store_dwordx4 v[140:143], v20, s[24:27], s85 offen sc1\n"
      "v_pk_mul_f32 v[206:207], v[206:207], v[158:159] op_sel:[0,1]\n"
      "v_pk_mul_f32 v[138:139], v[138:139], v[106:107] op_sel_hi:[1,0]\n"
      "v_pk_mul_f32 v[162:163], v[162:163], v[106:107] op_sel_hi:[1,0]\n"
      "v_pk_mul_f32 v[122:123], v[122:123], v[76:77] op_sel_hi:[1,0]\n"
      "v_pk_mul_f32 v[186:187], v[186:187], v[106:107] op_sel_hi:[1,0]\n"
      "v_pk_mul_f32 v[204:205], v[204:205], v[158:159] op_sel_hi:[1,0]\n"
      "v_pk_mul_f32 v[124:125], v[124:125], v[76:77] op_sel_hi:[1,0]\n"
      "v_pk_mul_f32 v[180:181], v[180:181], v[158:159] op_sel:[0,1]\n"
      "v_mfma_f32_16x16x32_bf16 v[176:179], a[40:43], v[220:223], v[176:179]\n"
      "v_mul_f32 v166, v76, v166\n"
      "v_mul_f32 v201, v158, v201\n"
      "v_mfma_f32_16x16x32_bf16 v[176:179], a[44:47], v[172:175], v[176:179]\n"
      "s_add_u32 s73, s73, 1024\n"
      "v_mul_f32 v137, v106, v137\n"
      "v_fma_f32 v112, v150, v150, s51\n"
      "v_mfma_f32_16x16x32_bf16 v[132:135], a[48:51], v[154:157], 0\n"
      "v_mul_f32 v200, v158, v200\n"
      "v_cvt_pk_bf16_f32 v144, v144, v145\n"
      "v_mfma_f32_16x16x32_bf16 v[132:135], a[52:55], v[108:111], v[132:135]\n"
      "v_mul_f32 v184, v106, v184\n"
      "v_fmac_f32 v112, v151, v151\n"
      "v_mfma_f32_16x16x32_bf16 v[132:135], a[56:59], v[126:129], v[132:135]\n"
      "v_fmac_f32 v112, v152, v152\n"
      "v_mul_f32 v136, v106, v136\n"
      "v_mfma_f32_16x16x32_bf16 v[132:135], a[60:63], v[168:171], v[132:135]\n"
      "v_add_u32_e64 v78, s77, v78\n"
      "v_cvt_pk_bf16_f32 v145, v146, v147\n"
      "v_mfma_f32_16x16x32_bf16 v[132:135], a[64:67], v[220:223], v[132:135]\n"
      "v_lshlrev_b32 v85, 4, v85\n"
      "v_mul_f32 v182, v159, v182\n"
      "v_mfma_f32_16x16x32_bf16 v[132:135], a[68:71], v[172:175], v[132:135]\n"
      "v_cvt_pk_bf16_f32 v213, v214, v215\n"
      "v_cvt_pk_bf16_f32 v180, v180, v181\n"
      "v_mfma_f32_16x16x32_bf16 v[216:219], a[72:75], v[154:157], 0\n"
      "v_mul_f32 v183, v159, v183\n"
      "v_fmac_f32 v112, v153, v153\n"
      "v_mfma_f32_16x16x32_bf16 v[216:219], a[76:79], v[108:111], v[216:219]\n"
      "v_cvt_pk_bf16_f32 v214, v122, v123\n"
      "v_fmac_f32 v112, v176, v176\n"
      "v_mfma_f32_16x16x32_bf16 v[216:219], a[80:83], v[126:129], v[216:219]\n"
      "v_mul_f32 v208, v159, v208\n"
      "v_fmac_f32_e64 v112, v177, v177\n"
      "v_mfma_f32_16x16x32_bf16 v[216:219], a[84:87], v[168:171], v[216:219]\n"
      "v_mul_f32 v161, v106, v161\n"
      "v_mul_f32 v203, v158, v203\n"
      "v_mfma_f32_16x16x32_bf16 v[216:219], a[88:91], v[220:223], v[216:219]\n"
      "v_mul_f32_e64 v160, v106, v160\n"
      "v_cvt_pk_bf16_f32 v136, v136, v137\n"
      "v_mfma_f32_16x16x32_bf16 v[216:219], a[92:95], v[172:175], v[216:219]\n"
      "s_cmp_ge_u32 s95, 0x40000000\n"
      "s_cselect_b32 s94, -1, -2\n"
      "v_fmac_f32_e64 v112, v178, v178\n"
      "v_mfma_f32_16x16x32_bf16 v[146:149], a[96:99], v[154:157], 0\n"
      "v_fmac_f32_e64 v112, v179, v179\n"
      "v_cvt_pk_bf16_f32 v199, v200, v201\n"
      "v_mfma_f32_16x16x32_bf16 v[146:149], a[100:103], v[108:111], v[146:149]\n"
      "v_cvt_pk_bf16_f32 v215, v124, v125\n"
      "v_fma_f32 v113, v216, v216, s51\n"
      "v_mfma_f32_16x16x32_bf16 v[146:149], a[104:107], v[126:129], v[146:149]\n"
      "v_cvt_pk_bf16_f32 v106, v118, v119\n"
      "v_fmac_f32_e64 v113, v217, v217\n"
      "v_mfma_f32_16x16x32_bf16 v[146:149], a[108:111], v[168:171], v[146:149]\n"
      "v_cvt_pk_bf16_f32 v99, v192, v193\n"
      "v_cvt_pk_bf16_f32 v200, v202, v203\n"
      "v_mfma_f32_16x16x32_bf16 v[146:149], a[112:115], v[220:223], v[146:149]\n"
      "v_cvt_pk_bf16_f32 v164, v164, v165\n"
      "v_fmac_f32 v112, v132, v132\n"
      "v_mfma_f32_16x16x32_bf16 v[146:149], a[116:119], v[172:175], v[146:149]\n"
      "v_fmac_f32 v112, v133, v133\n"
      "v_cvt_pk_bf16_f32 v201, v204, v205\n"
      "v_mfma_f32_16x16x32_bf16 v[122:125], a[120:123], v[154:157], 0\n"
      "v_cvt_pk_bf16_f32 v181, v182, v183\n"
      "v_mov_b32 v158, 0x3fb9\n"
      "v_mfma_f32_16x16x32_bf16 v[122:125], a[124:127], v[108:111], v[122:125]\n"
      "v_cvt_pk_bf16_f32 v182, v206, v207\n"
      "v_fmac_f32 v112, v134, v134\n"
      "v_mfma_f32_16x16x32_bf16 v[122:125], a[128:131], v[126:129], v[122:125]\n"
      "v_fmac_f32 v113, v218, v218\n"
      "v_fmac_f32 v113, v219, v219\n"
      "v_mfma_f32_16x16x32_bf16 v[122:125], a[132:135], v[168:171], v[122:125]\n"
      "s_waitcnt vmcnt(8)\n"
      "v_fmac_f32 v113, v146, v146\n"
      "v_mul_f32 v209, v159, v209\n"
      "v_mfma_f32_16x16x32_bf16 v[122:125], a[136:139], v[220:223], v[122:125]\n"
      "v_cvt_pk_bf16_f32 v98, v190, v191\n"
      "v_fmac_f32 v112, v135, v135\n"
      "v_mfma_f32_16x16x32_bf16 v[122:125], a[140:143], v[172:175], v[122:125]\n"
      "v_fmac_f32 v113, v147, v147\n"
      "v_fmac_f32 v113, v148, v148\n"
      "v_fmac_f32 v113, v149, v149\n"
      "v_cvt_pk_bf16_f32 v183, v208, v209\n"
      "v_and_b32_e64 v93, 2, v78\n"
      "v_pk_mul_f32 v[100:101], v[100:101], v[158:159] op_sel:[0,1]\n"
      "v_pk_mul_f32 v[102:103], v[102:103], v[158:159] op_sel:[0,1]\n"
      "v_mfma_f32_16x16x32_bf16 v[140:143], v[154:157], a[144:147], 0\n"
      "v_fmac_f32 v113, v122, v122\n"
      "v_fmac_f32 v113, v123, v123\n"
      "v_fmac_f32 v113, v124, v124\n"
      "v_fmac_f32 v113, v125, v125\n"
      "v_mfma_f32_16x16x32_bf16 v[140:143], v[108:111], a[148:151], v[140:143]\n"
      "s_and_b32 s4, s45, s8\n"
      "v_permlane16_swap_b32 v112, v113\n"
      "v_mfma_f32_16x16x32_bf16 v[140:143], v[126:129], a[152:155], v[140:143]\n"
      "v_add_f32 v112, v112, v113\n"
      "v_mov_b32 v113, v112\n"
      "v_mfma_f32_16x16x32_bf16 v[140:143], v[168:171], a[156:159], v[140:143]\n"
      "s_nop 0\n"
      "v_permlane32_swap_b32 v112, v113\n"
      "v_mfma_f32_16x16x32_bf16 v[140:143], v[220:223], a[160:163], v[140:143]\n"
      "v_add_f32 v112, v112, v113\n"
      "v_rsq_f32 v112, v112\n"
      "v_mfma_f32_16x16x32_bf16 v[140:143], v[172:175], a[164:167], v[140:143]\n"
      "s_cmp_gt_u32 s42, s44\n"
      "s_cselect_b32 s95, s94, s95\n"
      "v_mov_b32 v113, v112\n"
      "v_mfma_f32_16x16x32_bf16 v[206:209], v[154:157], a[168:171], 0\n"
      "v_cvt_pk_bf16_f32 v165, v166, v167\n"
      "v_permlane16_swap_b32 v112, v113\n"
      "v_cvt_pk_bf16_f32 v160, v160, v161\n"
      "v_mul_f32_e64 v112, s50, v112\n"
      "v_pk_mul_f32 v[122:123], v[122:123], v[112:113] op_sel:[0,1]\n"
      "v_pk_mul_f32 v[132:133], v[132:133], v[112:113] op_sel_hi:[1,0]\n"
      "v_pk_mul_f32 v[178:179], v[178:179], v[112:113] op_sel_hi:[1,0]\n"
      "v_pk_mul_f32 v[216:217], v[216:217], v[112:113] op_sel:[0,1]\n"
      "v_pk_mul_f32 v[152:153], v[152:153], v[112:113] op_sel_hi:[1,0]\n"
      "v_pk_mul_f32 v[148:149], v[148:149], v[112:113] op_sel:[0,1]\n"
      "v_pk_mul_f32 v[218:219], v[218:219], v[112:113] op_sel:[0,1]\n"
      "v_pk_mul_f32 v[150:151], v[150:151], v[112:113] op_sel_hi:[1,0]\n"
      "v_mfma_f32_16x16x32_bf16 v[206:209], v[108:111], a[172:175], v[206:209]\n"
      "v_cvt_pk_bf16_f32 v137, v138, v139\n"
      "v_mul_f32_e64 v135, v112, v135\n"
      "v_mfma_f32_16x16x32_bf16 v[206:209], v[126:129], a[176:179], v[206:209]\n"
      "ds_read_b128 v[118:121], v2\n"
      "v_cvt_pk_bf16_f32 v77, v142, v143\n"
      "v_mfma_f32_16x16x32_bf16 v[206:209], v[168:171], a[180:183], v[206:209]\n"
      "v_cvt_pk_bf16_f32 v161, v162, v163\n"
      "v_cvt_pk_bf16_f32 v138, v184, v185\n"
      "v_mfma_f32_16x16x32_bf16 v[206:209], v[220:223], a[184:187], v[206:209]\n"
      "ds_read_b128 v[188:191], v2 offset:6144\n"
      "v_mov_b32_e64 v159, 0\n"
      "v_mfma_f32_16x16x32_bf16 v[206:209], v[172:175], a[188:191], v[206:209]\n"
      "v_cvt_pk_bf16_f32 v76, v140, v141\n"
      "v_and_b32_e64 v78, -4, v78\n"
      "v_mfma_f32_16x16x32_bf16 v[202:205], v[154:157], a[192:195], 0\n"
      "ds_read_b128 v[154:157], v2 offset:3072\n"
      "v_cvt_pk_bf16_f32 v139, v186, v187\n"
      "v_mfma_f32_16x16x32_bf16 v[202:205], v[108:111], a[196:199], v[202:205]\n"
      "ds_read_b128 v[140:143], v7 offset:4096\n"
      "v_mul_f32 v134, v112, v134\n"
      "v_mfma_f32_16x16x32_bf16 v[202:205], v[126:129], a[200:203], v[202:205]\n"
      "ds_read_b128 v[108:111], v2 offset:9216\n"
      "v_cvt_pk_bf16_f32 v216, v216, v217\n"
      "v_mfma_f32_16x16x32_bf16 v[202:205], v[168:171], a[204:207], v[202:205]\n"
      "s_waitcnt lgkmcnt(4)\n"
      "v_cvt_pk_bf16_f32 v100, v100, v101\n"
      "v_cvt_pk_bf16_f32 v81, v208, v209\n"
      "v_mfma_f32_16x16x32_bf16 v[202:205], v[220:223], a[208:211], v[202:205]\n"
      "ds_read_b128 v[168:171], v2 offset:12288\n"
      "v_mov_b32_e64 v167, 0\n"
      "v_mfma_f32_16x16x32_bf16 v[202:205], v[172:175], a[212:215], v[202:205]\n"
      "ds_read_b128 v[208:211], v2 offset:15360\n"
      "v_mul_f32 v146, v113, v146\n"
      "v_mfma_f32_16x16x32_bf16 v[172:175], a[216:219], v[118:121], 0\n"
      "s_waitcnt lgkmcnt(4)\n"
      "v_mul_f32 v147, v113, v147\n"
      "v_add_u32 v187, 110592, v78\n"
      "v_mfma_f32_16x16x32_bf16 v[172:175], a[220:223], v[154:157], v[172:175]\n"
      "s_waitcnt lgkmcnt(3)\n"
      "v_cvt_pk_bf16_f32 v217, v218, v219\n"
      "v_cvt_pk_bf16_f32 v132, v132, v133\n"
      "v_mfma_f32_16x16x32_bf16 v[172:175], a[224:227], v[188:191], v[172:175]\n"
      "buffer_store_dwordx4 v[140:143], v21, s[24:27], s85 offen sc1\n"
      "v_mfma_f32_16x16x32_bf16 v[220:223], a[240:243], v[118:121], 0\n"
      "ds_read2_b32 v[162:163], v187 offset0:2 offset1:10\n"
      "v_cvt_pk_bf16_f32 v84, v202, v203\n"
      "v_mfma_f32_16x16x32_bf16 v[220:223], a[244:247], v[154:157], v[220:223]\n"
      "s_cmp_ge_u32 s95, 0x40000000\n"
      "s_cselect_b64 vcc, -1, s[56:57]\n"
      "v_lshlrev_b64 v[158:159], v85, v[158:159]\n"
      "v_mfma_f32_16x16x32_bf16 v[220:223], a[248:251], v[188:191], v[220:223]\n"
      "s_waitcnt lgkmcnt(1)\n"
      "v_cvt_pk_bf16_f32 v219, v148, v149\n"
      "v_mul_f32 v177, v112, v177\n"
      "v_mfma_f32_16x16x32_bf16 v[140:143], v[232:235], v[118:121], 0\n"
      "ds_read2_b32 v[202:203], v187 offset0:16 offset1:17\n"
      "v_cvt_pk_bf16_f32 v80, v206, v207\n"
      "v_mfma_f32_16x16x32_bf16 v[140:143], v[236:239], v[154:157], v[140:143]\n"
      "ds_read_b128 v[118:121], v7 offset:8192\n"
      "v_cvt_pk_bf16_f32 v150, v150, v151\n"
      "v_mfma_f32_16x16x32_bf16 v[140:143], v[240:243], v[188:191], v[140:143]\n"
      "ds_read2_b32 v[184:185], v187 offset0:0 offset1:1\n"
      "v_cmp_gt_u32 s[72:73], 64, v85\n"
      "v_mfma_f32_16x16x32_bf16 v[172:175], a[228:231], v[108:111], v[172:175]\n"
      "v_cvt_pk_bf16_f32 v218, v146, v147\n"
      "v_cvt_pk_bf16_f32 v151, v152, v153\n"
      "v_mfma_f32_16x16x32_bf16 v[172:175], a[232:235], v[168:171], v[172:175]\n"
      "v_cvt_pk_bf16_f32 v133, v134, v135\n"
      "v_cndmask_b32_e64 v158, 0, v158, s[72:73]\n"
      "v_mfma_f32_16x16x32_bf16 v[172:175], a[236:239], v[208:211], v[172:175]\n"
      "ds_read_b128 v[146:149], v10 offset:1200\n"
      "v_cvt_pk_bf16_f32 v101, v102, v103\n"
      "v_mfma_f32_16x16x32_bf16 v[220:223], a[252:255], v[108:111], v[220:223]\n"
      "ds_read_b64 v[128:129], v10 offset:1192\n"
      "v_mul_f32_e64 v125, v113, v125\n"
      "v_mfma_f32_16x16x32_bf16 v[220:223], v[224:227], v[168:171], v[220:223]\n"
      "ds_read_b128 v[154:157], v7 offset:12288\n"
      "v_mov_b32 v166, 0xbb2b\n"
      "v_mfma_f32_16x16x32_bf16 v[220:223], v[228:231], v[208:211], v[220:223]\n"
      "ds_read2_b32 v[102:103], v187 offset0:8 offset1:9\n"
      "v_mul_f32 v124, v113, v124\n"
      "v_mfma_f32_16x16x32_bf16 v[140:143], v[244:247], v[108:111], v[140:143]\n"
      "s_waitcnt lgkmcnt(4)\n"
      "v_mul_f32 v176, v112, v176\n"
      "v_cvt_pk_bf16_f32 v153, v178, v179\n"
      "v_mfma_f32_16x16x32_bf16 v[140:143], v[248:251], v[168:171], v[140:143]\n"
      "buffer_store_dwordx4 v[118:121], v22, s[24:27], s85 offen sc1\n"
      "v_mfma_f32_16x16x32_bf16 v[140:143], v[252:255], v[208:211], v[140:143]\n"
      "ds_read2_b32 v[178:179], v187 offset0:24 offset1:18\n"
      "v_cndmask_b32_e64 v159, 0, v159, s[72:73]\n"
      "v_mfma_f32_16x16x32_bf16 v[118:121], v[198:201], v[212:215], v[40:43]\n"
      "v_alignbyte_b32 v184, v185, v184, v93\n"
      "v_cmp_gt_u32 s[72:73], 64, v85\n"
      "v_mfma_f32_16x16x32_bf16 v[168:171], v[136:139], v[212:215], v[44:47]\n"
      "s_waitcnt lgkmcnt(1)\n"
      "v_lshlrev_b64 v[166:167], v85, v[166:167]\n"
      "v_cvt_pk_bf16_f32 v122, v122, v123\n"
      "v_mfma_f32_16x16x32_bf16 v[190:193], v[216:219], v[212:215], v[48:51]\n"
      "ds_read_b128 v[186:189], v2 offset:256\n"
      "v_cvt_pk_bf16_f32 v123, v124, v125\n"
      "v_mfma_f32_16x16x32_bf16 v[124:127], v[146:149], v[212:215], v[24:27]\n"
      "ds_read_b128 v[206:209], v7 offset:16384\n"
      "v_alignbyte_b32 v202, v203, v202, v93\n"
      "v_mfma_f32_16x16x16_bf16 v[118:121], v[144:145], v[164:165], v[118:121]\n"
      "v_fma_f32 v112, v172, v172, s52\n"
      "v_alignbyte_b32 v102, v103, v102, v93\n"
      "v_add_u32 v130, 115296, v130\n"
      "v_mfma_f32_16x16x16_bf16 v[168:171], v[160:161], v[164:165], v[168:171]\n"
      "v_cvt_pk_bf16_f32 v152, v176, v177\n"
      "v_cndmask_b32_e64 v166, 0, v166, s[72:73]\n"
      "v_mul_f32_e64 v113, v173, v173\n"
      "v_mfma_f32_16x16x16_bf16 v[190:193], v[122:123], v[164:165], v[190:193]\n"
      "buffer_store_dwordx4 v[154:157], v23, s[24:27], s85 offen sc1\n"
      "v_mfma_f32_16x16x16_bf16 v[124:127], v[128:129], v[164:165], v[124:127]\n"
      "v_alignbyte_b32 v185, v162, v185, v93\n"
      "v_alignbyte_b32 v103, v163, v103, v93\n"
      "v_fmac_f32 v112, v174, v174\n"
      "v_mfma_f32_16x16x16_bf16 v[118:121], v[158:159], v[184:185], v[118:121]\n"
      "s_waitcnt lgkmcnt(0)\n"
      "v_alignbyte_b32 v203, v179, v203, v93\n"
      "v_add_u32 v154, 112160, v78\n"
      "v_mfma_f32_16x16x16_bf16 v[168:171], v[158:159], v[102:103], v[168:171]\n"
      "ds_read_b128 v[162:165], v2 offset:3328\n"
      "v_alignbyte_b32 v178, v178, v178, v93\n"
      "v_mfma_f32_16x16x16_bf16 v[190:193], v[158:159], v[202:203], v[190:193]\n"
      "ds_read2_b32 v[134:135], v154 offset0:8 offset1:9\n"
      "v_cndmask_b32_e64 v167, 0, v167, s[72:73]\n"
      "v_mfma_f32_16x16x16_bf16 v[124:127], v[158:159], v[178:179], v[124:127]\n"
      "ds_read2_b32 v[214:215], v154 offset0:0 offset1:1\n"
      "v_cvt_pk_bf16_f32 v85, v204, v205\n"
      "v_mfma_f32_16x16x16_bf16 v[118:121], v[166:167], v[184:185], v[118:121]\n"
      "ds_read_b128 v[210:213], v2 offset:6400\n"
      "v_fmac_f32_e64 v113, v175, v175\n"
      "v_mfma_f32_16x16x16_bf16 v[168:171], v[166:167], v[102:103], v[168:171]\n"
      "ds_read2_b32 v[108:109], v154 offset0:2 offset1:10\n"
      "v_fmac_f32_e64 v113, v221, v221\n"
      "v_mfma_f32_16x16x16_bf16 v[190:193], v[166:167], v[202:203], v[190:193]\n"
      "buffer_store_dwordx4 v[206:209], v25, s[24:27], s85 offen sc1\n"
      "v_mfma_f32_16x16x16_bf16 v[124:127], v[166:167], v[178:179], v[124:127]\n"
      "v_fmac_f32_e64 v112, v220, v220\n"
      "v_add_u32 v184, 113728, v78\n"
      "v_max3_f32 v203, v118, v119, v120\n"
      "v_mfma_f32_16x16x32_bf16 v[206:209], a[216:219], v[186:189], 0\n"
      "s_waitcnt lgkmcnt(2)\n"
      "v_alignbyte_b32 v134, v135, v134, v93\n"
      "v_max3_f32 v203, v203, v170, v171\n"
      "v_mfma_f32_16x16x32_bf16 v[206:209], a[220:223], v[162:165], v[206:209]\n"
      "s_waitcnt lgkmcnt(1)\n"
      "v_max3_f32 v103, v121, v168, v169\n"
      "v_max3_f32 v203, v203, v192, v193\n"
      "v_mfma_f32_16x16x32_bf16 v[206:209], a[224:227], v[210:213], v[206:209]\n"
      "ds_read_u16 v130, v130\n"
      "v_max3_f32 v103, v103, v190, v191\n"
      "v_mfma_f32_16x16x32_bf16 v[176:179], a[240:243], v[186:189], 0\n"
      "s_waitcnt lgkmcnt(1)\n"
      "v_max3_f32 v203, v203, v103, v124\n"
      "v_alignbyte_b32 v214, v215, v214, v93\n"
      "v_mfma_f32_16x16x32_bf16 v[176:179], a[244:247], v[162:165], v[176:179]\n"
      "ds_read2_b32 v[102:103], v154 offset0:16 offset1:17\n"
      "v_alignbyte_b32 v135, v109, v135, v93\n"
      "v_mfma_f32_16x16x32_bf16 v[176:179], a[248:251], v[210:213], v[176:179]\n"
      "s_waitcnt vmcnt(6)\n"
      "v_or3_b32 v114, v114, v115, v116\n"
      "v_alignbyte_b32 v215, v108, v215, v93\n"
      "v_mfma_f32_16x16x32_bf16 v[108:111], v[232:235], v[186:189], 0\n"
      "ds_read2_b32 v[188:189], v154 offset0:24 offset1:18\n"
      "v_mov_b32_e64 v154, v203\n"
      "v_mfma_f32_16x16x32_bf16 v[108:111], v[236:239], v[162:165], v[108:111]\n"
      "s_nop 0\n"
      "v_permlane16_swap_b32 v203, v154\n"
      "v_mfma_f32_16x16x32_bf16 v[108:111], v[240:243], v[210:213], v[108:111]\n"
      "v_max_f32 v203, v203, v154\n"
      "v_mov_b32 v154, v203\n"
      "v_mfma_f32_16x16x32_bf16 v[162:165], v[198:201], v[180:183], v[52:55]\n"
      "s_nop 0\n"
      "v_permlane32_swap_b32 v203, v154\n"
      "v_mfma_f32_16x16x32_bf16 v[210:213], v[136:139], v[180:183], v[56:59]\n"
      "s_waitcnt lgkmcnt(1)\n"
      "v_max_f32 v203, v203, v154\n"
      "v_sub_f32 v168, v168, v203\n"
      "v_alignbyte_b32 v102, v103, v102, v93\n"
      "v_lshlrev_b32_e64 v130, 16, v130\n"
      "s_waitcnt vmcnt(5)\n"
      "v_or3_b32 v114, v114, v117, v86\n"
      "v_pk_add_f32 v[190:191], v[190:191], v[202:203] op_sel:[0,1] neg_lo:[0,1] neg_hi:[0,1]\n"
      "v_pk_add_f32 v[120:121], v[120:121], v[202:203] op_sel:[0,1] neg_lo:[0,1] neg_hi:[0,1]\n"
      "v_pk_fma_f32 v[112:113], v[222:223], v[222:223], v[112:113]\n"
      "v_pk_add_f32 v[118:119], v[118:119], v[202:203] op_sel:[0,1] neg_lo:[0,1] neg_hi:[0,1]\n"
      "v_pk_add_f32 v[192:193], v[192:193], v[202:203] op_sel:[0,1] neg_lo:[0,1] neg_hi:[0,1]\n"
      "v_pk_add_f32 v[170:171], v[170:171], v[202:203] op_sel:[0,1] neg_lo:[0,1] neg_hi:[0,1]\n"
      "v_mfma_f32_16x16x32_bf16 v[154:157], v[216:219], v[180:183], v[60:63]\n"
      "ds_write_b32 v31, v114 offset:5632\n"
      "v_mfma_f32_16x16x32_bf16 v[114:117], v[146:149], v[180:183], v[26:29]\n"
      "s_waitcnt lgkmcnt(1)\n"
      "v_exp_f32 v192, v192\n"
      "v_mfma_f32_16x16x16_bf16 v[162:165], v[144:145], v[100:101], v[162:165]\n"
      "v_exp_f32_e64 v119, v119\n"
      "v_alignbyte_b32 v103, v189, v103, v93\n"
      "v_mfma_f32_16x16x16_bf16 v[210:213], v[160:161], v[100:101], v[210:213]\n"
      "v_exp_f32 v190, v190\n"
      "v_sub_f32 v124, v124, v203\n"
      "v_mfma_f32_16x16x16_bf16 v[154:157], v[122:123], v[100:101], v[154:157]\n"
      "v_alignbyte_b32 v188, v188, v188, v93\n"
      "v_exp_f32 v170, v170\n"
      "v_mfma_f32_16x16x16_bf16 v[114:117], v[128:129], v[100:101], v[114:117]\n"
      "v_exp_f32 v191, v191\n"
      "v_exp_f32_e64 v118, v118\n"
      "v_mfma_f32_16x16x16_bf16 v[162:165], v[158:159], v[214:215], v[162:165]\n"
      "v_cvt_pk_bf16_f32 v88, v190, v191\n"
      "v_exp_f32_e64 v193, v193\n"
      "v_mfma_f32_16x16x16_bf16 v[210:213], v[158:159], v[134:135], v[210:213]\n"
      "v_cvt_pk_bf16_f32 v118, v118, v119\n"
      "v_exp_f32 v120, v120\n"
      "v_mfma_f32_16x16x16_bf16 v[154:157], v[158:159], v[102:103], v[154:157]\n"
      "v_exp_f32 v121, v121\n"
      "v_cvt_pk_bf16_f32 v89, v192, v193\n"
      "v_mfma_f32_16x16x16_bf16 v[114:117], v[158:159], v[188:189], v[114:117]\n"
      "ds_read_b128 v[180:183], v2 offset:9472\n"
      "v_sub_f32_e64 v169, v169, v203\n"
      "v_mfma_f32_16x16x16_bf16 v[162:165], v[166:167], v[214:215], v[162:165]\n"
      "ds_read_b128 v[202:205], v2 offset:12544\n"
      "v_exp_f32 v169, v169\n"
      "v_mfma_f32_16x16x16_bf16 v[210:213], v[166:167], v[134:135], v[210:213]\n"
      "v_exp_f32 v168, v168\n"
      "v_exp_f32_e64 v171, v171\n"
      "v_mfma_f32_16x16x16_bf16 v[154:157], v[166:167], v[102:103], v[154:157]\n"
      "ds_read_b128 v[100:103], v2 offset:15616\n"
      "v_cvt_pk_bf16_f32 v119, v120, v121\n"
      "v_cvt_pk_bf16_f32 v121, v170, v171\n"
      "v_mfma_f32_16x16x16_bf16 v[114:117], v[166:167], v[188:189], v[114:117]\n"
      "v_exp_f32_e64 v124, v124\n"
      "v_max3_f32 v86, v165, v210, v211\n"
      "v_max3_f32 v134, v162, v163, v164\n"
      "v_max3_f32 v134, v134, v212, v213\n"
      "v_max3_f32 v134, v134, v156, v157\n"
      "v_pk_fma_f32 v[112:113], v[140:141], v[140:141], v[112:113]\n"
      "v_max3_f32 v86, v86, v154, v155\n"
      "v_pk_fma_f32 v[112:113], v[142:143], v[142:143], v[112:113]\n"
      "v_max3_f32 v134, v134, v86, v114\n"
      "v_cvt_pk_bf16_f32 v90, v124, 0\n"
      "v_mov_b32 v117, v134\n"
      "s_waitcnt lgkmcnt(1)\n"
      "v_mfma_f32_16x16x32_bf16 v[206:209], a[228:231], v[180:183], v[206:209]\n"
      "ds_read_u16 v78, v11 offset:1168\n"
      "v_permlane16_swap_b32 v134, v117\n"
      "v_mfma_f32_16x16x32_bf16 v[206:209], a[232:235], v[202:205], v[206:209]\n"
      "s_waitcnt lgkmcnt(1)\n"
      "v_max_f32 v134, v134, v117\n"
      "v_mov_b32 v117, v134\n"
      "v_mfma_f32_16x16x32_bf16 v[206:209], a[236:239], v[100:103], v[206:209]\n"
      "ds_read_u16 v86, v11 offset:1232\n"
      "v_cvt_pk_bf16_f32 v120, v168, v169\n"
      "v_mfma_f32_16x16x32_bf16 v[176:179], a[252:255], v[180:183], v[176:179]\n"
      "ds_read_u16 v82, v11 offset:1200\n"
      "v_permlane32_swap_b32 v134, v117\n"
      "v_max_f32 v134, v134, v117\n"
      "v_sub_f32_e64 v114, v114, v134\n"
      "v_pk_add_f32 v[156:157], v[156:157], v[134:135] op_sel_hi:[1,0] neg_lo:[0,1] neg_hi:[0,1]\n"
      "v_pk_add_f32 v[162:163], v[162:163], v[134:135] op_sel_hi:[1,0] neg_lo:[0,1] neg_hi:[0,1]\n"
      "v_pk_add_f32 v[210:211], v[210:211], v[134:135] op_sel_hi:[1,0] neg_lo:[0,1] neg_hi:[0,1]\n"
      "v_pk_add_f32 v[154:155], v[154:155], v[134:135] op_sel_hi:[1,0] neg_lo:[0,1] neg_hi:[0,1]\n"
      "v_pk_add_f32 v[164:165], v[164:165], v[134:135] op_sel_hi:[1,0] neg_lo:[0,1] neg_hi:[0,1]\n"
      "v_pk_add_f32 v[212:213], v[212:213], v[134:135] op_sel_hi:[1,0] neg_lo:[0,1] neg_hi:[0,1]\n"
      "v_mfma_f32_16x16x32_bf16 v[176:179], v[224:227], v[202:205], v[176:179]\n"
      "ds_read2_b32 v[186:187], v184 offset0:0 offset1:1\n"
      "v_add_f32 v112, v112, v113\n"
      "v_mfma_f32_16x16x32_bf16 v[176:179], v[228:231], v[100:103], v[176:179]\n"
      "v_exp_f32 v162, v162\n"
      "v_mfma_f32_16x16x32_bf16 v[108:111], v[244:247], v[180:183], v[108:111]\n"
      "v_exp_f32_e64 v165, v165\n"
      "v_mfma_f32_16x16x32_bf16 v[108:111], v[248:251], v[202:205], v[108:111]\n"
      "ds_read2_b32 v[134:135], v184 offset0:8 offset1:9\n"
      "v_exp_f32_e64 v210, v210\n"
      "v_mfma_f32_16x16x32_bf16 v[108:111], v[252:255], v[100:103], v[108:111]\n"
      "ds_read2_b32 v[116:117], v184 offset0:2 offset1:10\n"
      "v_exp_f32_e64 v163, v163\n"
      "v_mfma_f32_16x16x32_bf16 v[168:171], v[32:35], v[118:121], 0\n"
      "ds_read2_b32 v[214:215], v184 offset0:16 offset1:17\n"
      "v_cvt_pk_bf16_f32 v162, v162, v163\n"
      "v_mfma_f32_16x16x32_bf16 v[168:171], v[32:35], v[88:91], v[168:171]\n"
      "ds_read2_b32 v[192:193], v184 offset0:24 offset1:18\n"
      "v_exp_f32_e64 v164, v164\n"
      "v_mfma_f32_16x16x32_bf16 v[180:183], v[194:197], v[118:121], 0\n"
      "s_waitcnt lgkmcnt(4)\n"
      "v_exp_f32 v213, v213\n"
      "v_mfma_f32_16x16x32_bf16 v[180:183], v[76:79], v[88:91], v[180:183]\n"
      "v_alignbyte_b32 v186, v187, v186, v93\n"
      "v_cvt_pk_bf16_f32 v163, v164, v165\n"
      "v_mfma_f32_16x16x32_bf16 v[100:103], v[104:107], v[118:121], 0\n"
      "v_exp_f32 v211, v211\n"
      "v_mfma_f32_16x16x32_bf16 v[100:103], v[80:83], v[88:91], v[100:103]\n"
      "s_waitcnt lgkmcnt(2)\n"
      "v_alignbyte_b32 v187, v116, v187, v93\n"
      "v_cvt_pk_bf16_f32 v164, v210, v211\n"
      "v_mfma_f32_16x16x32_bf16 v[202:205], v[96:99], v[118:121], 0\n"
      "s_waitcnt lgkmcnt(1)\n"
      "v_exp_f32 v212, v212\n"
      "v_mfma_f32_16x16x32_bf16 v[202:205], v[84:87], v[88:91], v[202:205]\n"
      "v_alignbyte_b32 v214, v215, v214, v93\n"
      "v_alignbyte_b32 v134, v135, v134, v93\n"
      "v_mfma_f32_16x16x32_bf16 v[188:191], v[198:201], v[150:153], v[64:67]\n"
      "s_waitcnt lgkmcnt(0)\n"
      "v_cvt_pk_bf16_f32 v165, v212, v213\n"
      "v_alignbyte_b32 v192, v192, v192, v93\n"
      "v_mfma_f32_16x16x32_bf16 v[210:213], v[136:139], v[150:153], v[68:71]\n"
      "v_alignbyte_b32 v215, v193, v215, v93\n"
      "v_alignbyte_b32 v135, v117, v135, v93\n"
      "v_mfma_f32_16x16x32_bf16 v[124:127], v[216:219], v[150:153], v[72:75]\n"
      "v_exp_f32 v156, v156\n"
      "v_mfma_f32_16x16x32_bf16 v[116:119], v[146:149], v[150:153], v[28:31]\n"
      "v_rcp_f32 v168, v168\n"
      "v_mfma_f32_16x16x16_bf16 v[188:191], v[144:145], v[132:133], v[188:191]\n"
      "v_exp_f32 v155, v155\n"
      "v_mul_f32 v101, v168, v101\n"
      "v_mfma_f32_16x16x16_bf16 v[210:213], v[160:161], v[132:133], v[210:213]\n"
      "v_exp_f32 v157, v157\n"
      "v_mul_f32 v102, v168, v102\n"
      "v_mfma_f32_16x16x16_bf16 v[124:127], v[122:123], v[132:133], v[124:127]\n"
      "v_mul_f32 v183, v168, v183\n"
      "v_mul_f32 v180, v168, v180\n"
      "v_mul_f32 v100, v168, v100\n"
      "v_mfma_f32_16x16x16_bf16 v[116:119], v[128:129], v[132:133], v[116:119]\n"
      "v_exp_f32 v154, v154\n"
      "v_cvt_pk_bf16_f32 v93, v156, v157\n"
      "v_mfma_f32_16x16x16_bf16 v[188:191], v[158:159], v[186:187], v[188:191]\n"
      "v_mul_f32 v202, v168, v202\n"
      "v_exp_f32_e64 v114, v114\n"
      "v_mfma_f32_16x16x16_bf16 v[210:213], v[158:159], v[134:135], v[210:213]\n"
      "v_mul_f32 v182, v168, v182\n"
      "v_mul_f32 v181, v168, v181\n"
      "v_cvt_pk_bf16_f32 v180, v180, v181\n"
      "v_mfma_f32_16x16x16_bf16 v[124:127], v[158:159], v[214:215], v[124:127]\n"
      "v_cvt_pk_bf16_f32 v181, v182, v183\n"
      "v_mul_f32_e64 v203, v168, v203\n"
      "v_cvt_pk_bf16_f32 v182, v100, v101\n"
      "v_mfma_f32_16x16x16_bf16 v[116:119], v[158:159], v[192:193], v[116:119]\n"
      "ds_read_b128 v[150:153], v2 offset:512\n"
      "v_mul_f32_e64 v103, v168, v103\n"
      "v_mfma_f32_16x16x16_bf16 v[188:191], v[166:167], v[186:187], v[188:191]\n"
      "ds_read_b128 v[184:187], v2 offset:3584\n"
      "v_cvt_pk_bf16_f32 v183, v102, v103\n"
      "v_mfma_f32_16x16x16_bf16 v[210:213], v[166:167], v[134:135], v[210:213]\n"
      "v_cvt_pk_bf16_f32 v94, v114, 0\n"
      "v_fma_f32 v114, v206, v206, s52\n"
      "v_mul_f32_e64 v115, v207, v207\n"
      "v_mfma_f32_16x16x16_bf16 v[124:127], v[166:167], v[214:215], v[124:127]\n"
      "ds_read_b128 v[100:103], v2 offset:6656\n"
      "v_max3_f32 v215, v188, v189, v190\n"
      "v_mfma_f32_16x16x16_bf16 v[116:119], v[166:167], v[192:193], v[116:119]\n"
      "v_fmac_f32 v115, v209, v209\n"
      "v_cvt_pk_bf16_f32 v202, v202, v203\n"
      "v_fmac_f32 v114, v208, v208\n"
      "v_max3_f32 v215, v215, v212, v213\n"
      "v_max3_f32 v135, v191, v210, v211\n"
      "v_max3_f32 v135, v135, v124, v125\n"
      "v_pk_fma_f32 v[114:115], v[176:177], v[176:177], v[114:115]\n"
      "v_max3_f32 v215, v215, v126, v127\n"
      "v_max3_f32 v215, v215, v135, v116\n"
      "v_pk_mul_f32 v[204:205], v[204:205], v[168:169] op_sel_hi:[1,0]\n"
      "v_pk_fma_f32 v[114:115], v[178:179], v[178:179], v[114:115]\n"
      "v_mov_b32 v119, v215\n"
      "s_waitcnt lgkmcnt(1)\n"
      "v_mfma_f32_16x16x32_bf16 v[132:135], a[216:219], v[150:153], 0\n"
      "v_permlane16_swap_b32 v215, v119\n"
      "v_max_f32 v215, v215, v119\n"
      "v_mov_b32_e64 v119, v215\n"
      "v_cvt_pk_bf16_f32 v92, v154, v155\n"
      "v_pk_fma_f32 v[114:115], v[108:109], v[108:109], v[114:115]\n"
      "v_permlane32_swap_b32 v215, v119\n"
      "v_max_f32 v215, v215, v119\n"
      "v_pk_fma_f32 v[114:115], v[110:111], v[110:111], v[114:115]\n"
      "v_pk_add_f32 v[190:191], v[190:191], v[214:215] op_sel:[0,1] neg_lo:[0,1] neg_hi:[0,1]\n"
      "v_pk_add_f32 v[188:189], v[188:189], v[214:215] op_sel:[0,1] neg_lo:[0,1] neg_hi:[0,1]\n"
      "v_mfma_f32_16x16x32_bf16 v[132:135], a[220:223], v[184:187], v[132:135]\n"
      "s_waitcnt lgkmcnt(0)\n"
      "v_exp_f32 v191, v191\n"
      "v_mfma_f32_16x16x32_bf16 v[132:135], a[224:227], v[100:103], v[132:135]\n"
      "ds_write_b128 v3, v[180:183] offset:18432\n"
      "v_pk_add_f32 v[210:211], v[210:211], v[214:215] op_sel:[0,1] neg_lo:[0,1] neg_hi:[0,1]\n"
      "v_pk_add_f32 v[126:127], v[126:127], v[214:215] op_sel:[0,1] neg_lo:[0,1] neg_hi:[0,1]\n"
      "v_pk_add_f32 v[124:125], v[124:125], v[214:215] op_sel:[0,1] neg_lo:[0,1] neg_hi:[0,1]\n"
      "v_pk_add_f32 v[212:213], v[212:213], v[214:215] op_sel:[0,1] neg_lo:[0,1] neg_hi:[0,1]\n"
      "v_mfma_f32_16x16x32_bf16 v[118:121], a[240:243], v[150:153], 0\n"
      "v_exp_f32 v188, v188\n"
      "v_mfma_f32_16x16x32_bf16 v[118:121], a[244:247], v[184:187], v[118:121]\n"
      "v_exp_f32 v125, v125\n"
      "v_mfma_f32_16x16x32_bf16 v[118:121], a[248:251], v[100:103], v[118:121]\n"
      "v_exp_f32 v189, v189\n"
      "v_mfma_f32_16x16x32_bf16 v[168:171], v[232:235], v[150:153], 0\n"
      "v_exp_f32 v190, v190\n"
      "v_mfma_f32_16x16x32_bf16 v[168:171], v[236:239], v[184:187], v[168:171]\n"
      "v_cvt_pk_bf16_f32 v188, v188, v189\n"
      "v_cvt_pk_bf16_f32 v203, v204, v205\n"
      "v_mfma_f32_16x16x32_bf16 v[168:171], v[240:243], v[100:103], v[168:171]\n"
      "ds_read_b128 v[152:155], v10 offset:1168\n"
      "v_sub_f32 v116, v116, v215\n"
      "v_mfma_f32_16x16x32_bf16 v[180:183], v[32:35], v[162:165], 0\n"
      "v_exp_f32 v124, v124\n"
      "v_mfma_f32_16x16x32_bf16 v[180:183], v[32:35], v[92:95], v[180:183]\n"
      "v_exp_f32_e64 v212, v212\n"
      "v_mfma_f32_16x16x32_bf16 v[100:103], v[194:197], v[162:165], 0\n"
      "ds_read_b64 v[166:167], v10 offset:1184\n"
      "v_cvt_pk_bf16_f32 v189, v190, v191\n"
      "v_mfma_f32_16x16x32_bf16 v[100:103], v[76:79], v[92:95], v[100:103]\n"
      "ds_write_b64 v4, v[202:203] offset:18432\n"
      "v_mfma_f32_16x16x32_bf16 v[184:187], v[104:107], v[162:165], 0\n"
      "v_exp_f32 v210, v210\n"
      "v_mfma_f32_16x16x32_bf16 v[184:187], v[80:83], v[92:95], v[184:187]\n"
      "v_add_f32 v114, v114, v115\n"
      "v_cvt_pk_bf16_f32 v88, v124, v125\n"
      "v_mfma_f32_16x16x32_bf16 v[202:205], v[96:99], v[162:165], 0\n"
      "s_waitcnt lgkmcnt(2)\n"
      "v_exp_f32 v213, v213\n"
      "v_mfma_f32_16x16x32_bf16 v[202:205], v[84:87], v[92:95], v[202:205]\n"
      "ds_read_b128 v[162:165], v2 offset:9728\n"
      "v_cvt_pk_bf16_f32 v191, v212, v213\n"
      "v_mfma_f32_16x16x32_bf16 v[212:215], v[152:155], v[198:201], v[30:33]\n"
      "v_rcp_f32_e64 v180, v180\n"
      "v_mfma_f32_16x16x32_bf16 v[198:201], v[152:155], v[136:139], v[30:33]\n"
      "s_waitcnt lgkmcnt(2)\n"
      "v_exp_f32 v211, v211\n"
      "v_mfma_f32_16x16x32_bf16 v[156:159], v[152:155], v[216:219], v[30:33]\n"
      "ds_read_b128 v[136:139], v2 offset:12800\n"
      "v_cvt_pk_bf16_f32 v190, v210, v211\n"
      "v_mfma_f32_16x16x32_bf16 v[216:219], v[152:155], v[146:149], v[30:33]\n"
      "ds_read_b128 v[152:155], v2 offset:15872\n"
      "v_mul_f32_e64 v187, v180, v187\n"
      "v_mfma_f32_16x16x16_bf16 v[212:215], v[166:167], v[144:145], v[212:215]\n"
      "v_mul_f32 v185, v180, v185\n"
      "v_permlane16_swap_b32 v112, v114\n"
      "v_mfma_f32_16x16x16_bf16 v[198:201], v[166:167], v[160:161], v[198:201]\n"
      "v_exp_f32 v127, v127\n"
      "v_mul_f32 v102, v180, v102\n"
      "v_mfma_f32_16x16x16_bf16 v[156:159], v[166:167], v[122:123], v[156:159]\n"
      "v_exp_f32_e64 v116, v116\n"
      "v_mul_f32_e64 v103, v180, v103\n"
      "v_mfma_f32_16x16x16_bf16 v[216:219], v[166:167], v[128:129], v[216:219]\n"
      "v_exp_f32_e64 v126, v126\n"
      "s_waitcnt lgkmcnt(2)\n"
      "v_mfma_f32_16x16x32_bf16 v[132:135], a[228:231], v[162:165], v[132:135]\n"
      "s_waitcnt lgkmcnt(1)\n"
      "v_cndmask_b32 v212, v212, v198, s[58:59]\n"
      "v_cvt_pk_bf16_f32 v90, v116, 0\n"
      "v_mfma_f32_16x16x32_bf16 v[132:135], a[232:235], v[136:139], v[132:135]\n"
      "s_waitcnt lgkmcnt(0)\n"
      "v_mul_f32 v100, v180, v100\n"
      "v_mul_f32_e64 v184, v180, v184\n"
      "v_mfma_f32_16x16x32_bf16 v[132:135], a[236:239], v[152:155], v[132:135]\n"
      "v_cndmask_b32 v212, v212, v156, s[60:61]\n"
      "v_cndmask_b32 v212, v212, v216, s[62:63]\n"
      "v_mfma_f32_16x16x32_bf16 v[118:121], a[252:255], v[162:165], v[118:121]\n"
      "v_mul_f32 v101, v180, v101\n"
      "v_fmac_f32_e64 v212, s53, v130\n"
      "v_mfma_f32_16x16x32_bf16 v[118:121], v[224:227], v[136:139], v[118:121]\n"
      "s_nop 0\n"
      "v_max_f32_dpp v166, v212, v212 row_ror:8 row_mask:0xf bank_mask:0xf\n"
      "v_cvt_pk_bf16_f32 v100, v100, v101\n"
      "v_mfma_f32_16x16x32_bf16 v[118:121], v[228:231], v[152:155], v[118:121]\n"
      "v_max_f32_dpp v166, v166, v166 row_ror:4 row_mask:0xf bank_mask:0xf\n"
      "v_mul_f32_e64 v186, v180, v186\n"
      "v_mfma_f32_16x16x32_bf16 v[168:171], v[244:247], v[162:165], v[168:171]\n"
      "v_cvt_pk_bf16_f32 v101, v102, v103\n"
      "v_max_f32_dpp v166, v166, v166 row_ror:2 row_mask:0xf bank_mask:0xf\n"
      "v_mfma_f32_16x16x32_bf16 v[168:171], v[248:251], v[136:139], v[168:171]\n"
      "s_nop 0\n"
      "v_max_f32_dpp v166, v166, v166 row_ror:1 row_mask:0xf bank_mask:0xf\n"
      "v_cvt_pk_bf16_f32 v89, v126, v127\n"
      "v_mfma_f32_16x16x32_bf16 v[168:171], v[252:255], v[152:155], v[168:171]\n"
      "v_mul_f32 v139, v133, v133\n"
      "v_max_f32_dpp v166, v166, v166 row_bcast:15 row_mask:0xa bank_mask:0xf\n"
      "v_mfma_f32_16x16x32_bf16 v[162:165], v[32:35], v[188:191], 0\n"
      "v_cvt_pk_bf16_f32 v102, v184, v185\n"
      "v_cvt_pk_bf16_f32 v103, v186, v187\n"
      "v_mfma_f32_16x16x32_bf16 v[162:165], v[32:35], v[88:91], v[162:165]\n"
      "v_max_f32_dpp v166, v166, v166 row_bcast:31 row_mask:0xc bank_mask:0xf\n"
      "v_add_f32_e64 v112, v112, v114\n"
      "v_mfma_f32_16x16x32_bf16 v[152:155], v[194:197], v[188:191], 0\n"
      "v_readlane_b32 s72, v166, 63\n"
      "v_fma_f32 v138, v132, v132, s52\n"
      "v_mfma_f32_16x16x32_bf16 v[152:155], v[76:79], v[88:91], v[152:155]\n"
      "ds_write_b128 v3, v[100:103] offset:18688\n"
      "v_pk_fma_f32 v[138:139], v[134:135], v[134:135], v[138:139]\n"
      "v_mfma_f32_16x16x32_bf16 v[164:167], v[104:107], v[188:191], 0\n"
      "v_rcp_f32 v162, v162\n"
      "v_subrev_f32 v212, s72, v212\n"
      "v_exp_f32_e64 v212, v212\n"
      "v_pk_fma_f32 v[138:139], v[118:119], v[118:119], v[138:139]\n"
      "v_pk_fma_f32 v[138:139], v[120:121], v[120:121], v[138:139]\n"
      "v_pk_mul_f32 v[152:153], v[152:153], v[162:163] op_sel_hi:[1,0]\n"
      "v_pk_mul_f32 v[154:155], v[154:155], v[162:163] op_sel_hi:[1,0]\n"
      "v_cvt_pk_bf16_f32 v212, v212, v212\n"
      "v_mfma_f32_16x16x32_bf16 v[164:167], v[80:83], v[88:91], v[164:167]\n"
      "ds_write_b16 v13, v212\n"
      "v_mfma_f32_16x16x32_bf16 v[210:213], v[96:99], v[188:191], 0\n"
      "ds_read_b128 v[144:147], v14\n"
      "ds_read_b128 v[198:201], v14 offset:64\n"
      "v_cvt_pk_bf16_f32 v152, v152, v153\n"
      "v_cvt_pk_bf16_f32 v153, v154, v155\n"
      "v_pk_mul_f32 v[202:203], v[202:203], v[180:181] op_sel_hi:[1,0]\n"
      "v_pk_fma_f32 v[138:139], v[168:169], v[168:169], v[138:139]\n"
      "v_pk_mul_f32 v[164:165], v[164:165], v[162:163] op_sel_hi:[1,0]\n"
      "v_pk_mul_f32 v[166:167], v[166:167], v[162:163] op_sel_hi:[1,0]\n"
      "v_cvt_pk_bf16_f32 v155, v166, v167\n"
      "v_cvt_pk_bf16_f32 v154, v164, v165\n"
      "v_cvt_pk_bf16_f32 v202, v202, v203\n"
      "v_mfma_f32_16x16x32_bf16 v[210:213], v[84:87], v[88:91], v[210:213]\n"
      "ds_write_b128 v3, v[152:155] offset:18944\n"
      "v_pk_fma_f32 v[138:139], v[170:171], v[170:171], v[138:139]\n"
      "v_add_f32 v138, v138, v139\n"
      "v_mov_b32 v114, v138\n"
      "v_pk_mul_f32 v[204:205], v[204:205], v[180:181] op_sel_hi:[1,0]\n"
      "v_cvt_pk_bf16_f32 v203, v204, v205\n"
      "v_permlane16_swap_b32 v138, v114\n"
      "v_add_f32 v138, v138, v114\n"
      "s_add_u32 s42, s42, 1\n"
      "ds_write_b64 v4, v[202:203] offset:18688\n"
      "v_permlane32_swap_b32 v112, v138\n"
      "v_pk_mul_f32 v[210:211], v[210:211], v[162:163] op_sel_hi:[1,0]\n"
      "v_add_f32_e64 v112, v112, v138\n"
      "v_pk_mul_f32 v[212:213], v[212:213], v[162:163] op_sel_hi:[1,0]\n"
      "s_add_u32 s43, s43, 1\n"
      "ds_write_b32 v31, v112 offset:1024\n"
      "v_cvt_pk_bf16_f32 v210, v210, v211\n"
      "v_cvt_pk_bf16_f32 v211, v212, v213\n"
      "s_waitcnt lgkmcnt(2)\n"
      "v_mfma_f32_16x16x32_bf16 v[162:165], v[144:147], v[32:35], 0\n"
      "ds_write_b64 v4, v[210:211] offset:18944\n"
      "v_add_u32 v11, 2336, v11\n"
      "v_mfma_f32_16x16x32_bf16 v[162:165], v[198:201], v[32:35], v[162:165]\n"
      "v_add_u32 v10, 2336, v10\n"
      "s_waitcnt lgkmcnt(0)\n"
      "s_barrier\n"
      "s_cbranch_vccz L_sel0_%=\n"
      "L_disp0_%=:\n"
      "s_cmp_lt_u32 s95, 0x40000000\n"
      "s_cbranch_scc1 L_nofix_18_%=\n"
      "s_cmp_eq_u32 s95, -2\n"
      "s_cbranch_scc1 L_tail0_%=\n"
      "s_nop 2\n"
      "s_cmp_eq_u32 s45, 0x20000000\n"
      "s_cselect_b32 s66, 0, s66\n"
      "s_cselect_b32 s48, 0, s48\n"
      "s_lshr_b32 s92, s45, 30\n"
      "s_or_b32 s92, s92, s66\n"
      "s_cbranch_scc0 L_nf_19_%=\n"
      "s_bitcmp1_b32 s45, 31\n"
      "s_cselect_b32 s75, 100270464, 0\n"
      "s_bitcmp1_b32 s45, 30\n"
      "s_cselect_b32 s76, 196224, 0\n"
      "s_and_b32 s74, s45, 0x3fffffff\n"
      "s_cmp_ge_u32 s45, 0x40000000\n"
      "s_cselect_b32 s74, s74, 0\n"
      "s_cselect_b32 s45, 0, s45\n"
      "s_sub_u32 s74, s74, s66\n"
      "s_add_u32 s66, s66, s74\n"
      "s_sub_u32 s75, s75, s67\n"
      "s_add_u32 s67, s67, s75\n"
      "s_sub_u32 s76, s76, s68\n"
      "s_add_u32 s68, s68, s76\n"
      "v_add_u32 v16, s74, v16\n"
      "v_add_u32 v17, s74, v17\n"
      "v_add_u32 v18, s74, v18\n"
      "v_add_u32 v19, s74, v19\n"
      "v_add_u32_e64 v15, s74, v15\n"
      "s_cmp_eq_u32 s75, 0\n"
      "s_cbranch_scc1 L_ax_20_%=\n"
      "v_bfe_i32 v154, v27, 0, 1\n"
      "v_and_b32 v154, s75, v154\n"
      "v_sub_u32 v16, v16, v154\n"
      "v_bfe_i32 v154, v27, 2, 1\n"
      "v_and_b32 v154, s75, v154\n"
      "v_sub_u32 v17, v17, v154\n"
      "v_bfe_i32 v154, v27, 4, 1\n"
      "v_and_b32 v154, s75, v154\n"
      "v_sub_u32 v18, v18, v154\n"
      "v_bfe_i32 v154, v27, 6, 1\n"
      "v_and_b32 v154, s75, v154\n"
      "v_sub_u32 v19, v19, v154\n"
      "v_bfe_i32 v154, v27, 8, 1\n"
      "v_and_b32 v154, s75, v154\n"
      "v_sub_u32 v15, v15, v154\n"
      "L_ax_20_%=:\n"
      "s_cmp_eq_u32 s76, 0\n"
      "s_cbranch_scc1 L_ax_21_%=\n"
      "v_bfe_i32 v154, v27, 1, 1\n"
      "v_and_b32 v154, s76, v154\n"
      "v_sub_u32 v16, v16, v154\n"
      "v_bfe_i32 v154, v27, 3, 1\n"
      "v_and_b32 v154, s76, v154\n"
      "v_sub_u32 v17, v17, v154\n"
      "v_bfe_i32 v154, v27, 5, 1\n"
      "v_and_b32 v154, s76, v154\n"
      "v_sub_u32 v18, v18, v154\n"
      "v_bfe_i32 v154, v27, 7, 1\n"
      "v_and_b32 v154, s76, v154\n"
      "v_sub_u32 v19, v19, v154\n"
      "v_bfe_i32 v154, v27, 9, 1\n"
      "v_and_b32 v154, s76, v154\n"
      "v_sub_u32 v15, v15, v154\n"
      "L_ax_21_%=:\n"
      "s_or_b32 s48, s66, s69\n"
      "s_cmp_lg_u32 s48, 0\n"
      "s_cselect_b32 s48, 0x40000000, 0\n"
      "L_nf_19_%=:\n"
      "s_lshr_b32 s92, s46, 30\n"
      "s_or_b32 s92, s92, s69\n"
      "s_cbranch_scc0 L_nf_22_%=\n"
      "s_bitcmp1_b32 s46, 31\n"
      "s_cselect_b32 s75, 100270464, 0\n"
      "s_bitcmp1_b32 s46, 30\n"
      "s_cselect_b32 s76, 196224, 0\n"
      "s_and_b32 s74, s46, 0x3fffffff\n"
      "s_cmp_ge_u32 s46, 0x40000000\n"
      "s_cselect_b32 s74, s74, 0\n"
      "s_cselect_b32 s46, 0, s46\n"
      "s_sub_u32 s74, s74, s69\n"
      "s_add_u32 s69, s69, s74\n"
      "s_sub_u32 s75, s75, s70\n"
      "s_add_u32 s70, s70, s75\n"
      "s_sub_u32 s76, s76, s71\n"
      "s_add_u32 s71, s71, s76\n"
      "v_add_u32 v20, s74, v20\n"
      "v_add_u32 v21, s74, v21\n"
      "v_add_u32 v22, s74, v22\n"
      "v_add_u32 v23, s74, v23\n"
      "v_add_u32 v25, s74, v25\n"
      "s_cmp_eq_u32 s75, 0\n"
      "s_cbranch_scc1 L_ax_23_%=\n"
      "v_bfe_i32 v154, v27, 10, 1\n"
      "v_and_b32 v154, s75, v154\n"
      "v_sub_u32 v20, v20, v154\n"
      "v_bfe_i32 v154, v27, 12, 1\n"
      "v_and_b32 v154, s75, v154\n"
      "v_sub_u32 v21, v21, v154\n"
      "v_bfe_i32 v154, v27, 14, 1\n"
      "v_and_b32 v154, s75, v154\n"
      "v_sub_u32 v22, v22, v154\n"
      "v_bfe_i32 v154, v27, 16, 1\n"
      "v_and_b32 v154, s75, v154\n"
      "v_sub_u32 v23, v23, v154\n"
      "v_bfe_i32 v154, v27, 18, 1\n"
      "v_and_b32 v154, s75, v154\n"
      "v_sub_u32 v25, v25, v154\n"
      "L_ax_23_%=:\n"
      "s_cmp_eq_u32 s76, 0\n"
      "s_cbranch_scc1 L_ax_24_%=\n"
      "v_bfe_i32 v154, v27, 11, 1\n"
      "v_and_b32 v154, s76, v154\n"
      "v_sub_u32 v20, v20, v154\n"
      "v_bfe_i32 v154, v27, 13, 1\n"
      "v_and_b32 v154, s76, v154\n"
      "v_sub_u32 v21, v21, v154\n"
      "v_bfe_i32 v154, v27, 15, 1\n"
      "v_and_b32 v154, s76, v154\n"
      "v_sub_u32 v22, v22, v154\n"
      "v_bfe_i32 v154, v27, 17, 1\n"
      "v_and_b32 v154, s76, v154\n"
      "v_sub_u32 v23, v23, v154\n"
      "v_bfe_i32 v154, v27, 19, 1\n"
      "v_and_b32 v154, s76, v154\n"
      "v_sub_u32 v25, v25, v154\n"
      "L_ax_24_%=:\n"
      "s_or_b32 s48, s66, s69\n"
      "s_cmp_lg_u32 s48, 0\n"
      "s_cselect_b32 s48, 0x40000000, 0\n"
      "L_nf_22_%=:\n"
      "s_and_b32 s4, s45, s8\n"
      "s_sub_u32 s92, s42, 1\n"
      "s_cmp_gt_u32 s92, s44\n"
      "s_cbranch_scc1 L_tail0_%=\n"
      "L_nofix_18_%=:\n"
      "s_cmp_lg_u64 s[56:57], 0\n"
      "s_cbranch_scc1 L_slow0_%=\n"
      "L_sel0_%=:\n"
      "s_cmp_lg_u64 s[54:55], 0\n"
      "s_cbranch_scc1 L_max0_%=\n"
      "s_branch L_loop0_%=\n"
      "L_slow0_%=:\n"
      "s_nop 4\n"
      "v_mfma_f32_16x16x32_bf16 v[152:155], v[144:147], v[194:197], 0\n"
      "ds_read_b128 v[124:127], v0\n"
      "v_subrev_u32 v197, 115456, v9\n"
      "v_mfma_f32_16x16x32_bf16 v[152:155], v[198:201], v[76:79], v[152:155]\n"
      "s_mov_b32 m0, 110592\n"
      "ds_read_b128 v[136:139], v9 offset:1024\n"
      "v_mfma_f32_16x16x32_bf16 v[210:213], v[144:147], v[104:107], 0\n"
      "ds_read_b128 v[100:103], v1\n"
      "s_add_u32 s72, s42, 1\n"
      "v_mfma_f32_16x16x32_bf16 v[210:213], v[198:201], v[80:83], v[210:213]\n"
      "ds_read_b128 v[190:193], v0 offset:6144\n"
      "v_readlane_b32 s77, v38, s72\n"
      "v_mfma_f32_16x16x32_bf16 v[180:183], v[144:147], v[96:99], 0\n"
      "ds_read_b128 v[144:147], v1 offset:6144\n"
      "s_and_b32 s73, s77, -16\n"
      "v_mfma_f32_16x16x32_bf16 v[180:183], v[198:201], v[84:87], v[180:183]\n"
      "buffer_load_dwordx4 v197, s[28:31], s73 offen  lds\n"
      "s_waitcnt lgkmcnt(4)\n"
      "v_mfma_f32_16x16x32_bf16 v[198:201], a[0:3], v[124:127], 0\n"
      "ds_read_b128 v[216:219], v1 offset:8192\n"
      "v_subrev_u32 v39, 115456, v9\n"
      "v_mfma_f32_16x16x32_bf16 v[212:215], a[24:27], v[124:127], 0\n"
      "s_waitcnt lgkmcnt(4)\n"
      "v_readlane_b32 s84, v37, s43\n"
      "v_add_f32 v138, v138, v139\n"
      "v_mfma_f32_16x16x32_bf16 v[202:205], a[48:51], v[124:127], 0\n"
      "s_add_u32 s73, s73, 1024\n"
      "v_add_f32_e64 v136, v136, v137\n"
      "v_add_f32_e64 v136, v136, v138\n"
      "v_mfma_f32_16x16x32_bf16 v[182:185], a[72:75], v[124:127], 0\n"
      "s_mov_b32 m0, 111616\n"
      "v_rsq_f32 v136, v136\n"
      "v_mfma_f32_16x16x32_bf16 v[186:189], a[96:99], v[124:127], 0\n"
      "buffer_load_dwordx4 v197, s[28:31], s73 offen  lds\n"
      "v_mfma_f32_16x16x32_bf16 v[112:115], a[120:123], v[124:127], 0\n"
      "s_waitcnt lgkmcnt(1)\n"
      "v_mul_f32 v136, 0x415db3d7, v136\n"
      "v_subrev_u32 v166, 115456, v9\n"
      "v_mfma_f32_16x16x32_bf16 v[198:201], a[4:7], v[100:103], v[198:201]\n"
      "ds_read_b128 v[148:151], v1 offset:12288\n"
      "v_mov_b32 v138, v136\n"
      "v_mfma_f32_16x16x32_bf16 v[198:201], a[8:11], v[190:193], v[198:201]\n"
      "s_mov_b32 m0, 112640\n"
      "v_permlane32_swap_b32 v136, v138\n"
      "v_mfma_f32_16x16x32_bf16 v[212:215], a[28:31], v[100:103], v[212:215]\n"
      "s_add_u32 s73, s73, 1024\n"
      "v_mov_b32 v137, v136\n"
      "v_mul_f32 v120, v138, v120\n"
      "v_mfma_f32_16x16x32_bf16 v[212:215], a[32:35], v[190:193], v[212:215]\n"
      "buffer_load_dwordx4 v197, s[28:31], s73 offen  lds\n"
      "v_mfma_f32_16x16x32_bf16 v[202:205], a[52:55], v[100:103], v[202:205]\n"
      "ds_read_b128 v[158:161], v0 offset:12288\n"
      "v_mul_f32_e64 v133, v138, v133\n"
      "v_mfma_f32_16x16x32_bf16 v[202:205], a[56:59], v[190:193], v[202:205]\n"
      "s_add_u32 s73, s73, 1024\n"
      "v_permlane16_swap_b32 v136, v137\n"
      "v_mfma_f32_16x16x32_bf16 v[182:185], a[76:79], v[100:103], v[182:185]\n"
      "s_mov_b32 m0, 113664\n"
      "v_mul_f32 v109, v137, v109\n"
      "v_mul_f32 v143, v136, v143\n"
      "v_mfma_f32_16x16x32_bf16 v[182:185], a[80:83], v[190:193], v[182:185]\n"
      "v_mul_f32 v175, v136, v175\n"
      "v_mul_f32 v108, v137, v108\n"
      "v_mfma_f32_16x16x32_bf16 v[186:189], a[100:103], v[100:103], v[186:189]\n"
      "v_mul_f32 v220, v136, v220\n"
      "v_mul_f32 v208, v137, v208\n"
      "v_mfma_f32_16x16x32_bf16 v[186:189], a[104:107], v[190:193], v[186:189]\n"
      "ds_read_b128 v[154:157], v0 offset:2048\n"
      "v_mul_f32 v174, v136, v174\n"
      "v_mfma_f32_16x16x32_bf16 v[112:115], a[124:127], v[100:103], v[112:115]\n"
      "buffer_load_dwordx4 v197, s[28:31], s73 offen  lds\n"
      "v_mfma_f32_16x16x32_bf16 v[112:115], a[128:131], v[190:193], v[112:115]\n"
      "s_waitcnt lgkmcnt(1)\n"
      "v_mul_f32 v142, v136, v142\n"
      "v_mul_f32_e64 v132, v138, v132\n"
      "v_mfma_f32_16x16x32_bf16 v[198:201], a[12:15], v[144:147], v[198:201]\n"
      "s_add_u32 s73, s73, 1024\n"
      "v_mul_f32 v169, v138, v169\n"
      "v_mul_f32 v135, v138, v135\n"
      "v_mfma_f32_16x16x32_bf16 v[198:201], a[16:19], v[158:161], v[198:201]\n"
      "ds_read_b128 v[128:131], v9 offset:5632\n"
      "v_mul_f32 v173, v136, v173\n"
      "v_mfma_f32_16x16x32_bf16 v[198:201], a[20:23], v[148:151], v[198:201]\n"
      "s_mov_b32 m0, 114688\n"
      "v_mul_f32 v172, v136, v172\n"
      "v_mul_f32 v221, v136, v221\n"
      "v_mfma_f32_16x16x32_bf16 v[212:215], a[36:39], v[144:147], v[212:215]\n"
      "v_cvt_pk_bf16_f32 v172, v172, v173\n"
      "v_cvt_pk_bf16_f32 v173, v174, v175\n"
      "v_mfma_f32_16x16x32_bf16 v[212:215], a[40:43], v[158:161], v[212:215]\n"
      "s_mov_b32 exec_hi, 0xffff\n"
      "buffer_load_dwordx4 v197, s[28:31], s73 offen  lds\n"
      "s_mov_b32 exec_hi, -1\n"
      "v_pk_mul_f32 v[206:207], v[206:207], v[136:137] op_sel:[0,1]\n"
      "v_pk_mul_f32 v[170:171], v[170:171], v[138:139] op_sel_hi:[1,0]\n"
      "v_pk_mul_f32 v[178:179], v[178:179], v[136:137] op_sel:[0,1]\n"
      "v_pk_mul_f32 v[222:223], v[222:223], v[136:137] op_sel_hi:[1,0]\n"
      "v_pk_mul_f32 v[110:111], v[110:111], v[136:137] op_sel:[0,1]\n"
      "v_mfma_f32_16x16x32_bf16 v[212:215], a[44:47], v[148:151], v[212:215]\n"
      "v_cvt_pk_bf16_f32 v206, v206, v207\n"
      "v_cvt_pk_bf16_f32 v175, v222, v223\n"
      "v_mfma_f32_16x16x32_bf16 v[202:205], a[60:63], v[144:147], v[202:205]\n"
      "s_add_u32 m0, s64, 18432\n"
      "v_lshrrev_b32_e64 v39, 3, v39\n"
      "v_cvt_pk_bf16_f32 v174, v220, v221\n"
      "v_mfma_f32_16x16x32_bf16 v[202:205], a[64:67], v[158:161], v[202:205]\n"
      "ds_write_b128 v5, v[172:175]\n"
      "v_pk_mul_f32 v[176:177], v[176:177], v[136:137] op_sel:[0,1]\n"
      "v_mfma_f32_16x16x32_bf16 v[202:205], a[68:71], v[148:151], v[202:205]\n"
      "buffer_load_dwordx4 v16, s[16:19], s45 offen  lds\n"
      "v_mfma_f32_16x16x32_bf16 v[182:185], a[84:87], v[144:147], v[182:185]\n"
      "s_and_b32 s77, s77, 15\n"
      "v_fma_f32 v181, v198, v198, s51\n"
      "v_mul_f32 v140, v136, v140\n"
      "v_mfma_f32_16x16x32_bf16 v[182:185], a[88:91], v[158:161], v[182:185]\n"
      "s_add_u32 m0, s64, 22528\n"
      "v_mul_f32 v209, v137, v209\n"
      "v_cvt_pk_bf16_f32 v108, v108, v109\n"
      "v_mfma_f32_16x16x32_bf16 v[182:185], a[92:95], v[148:151], v[182:185]\n"
      "v_mul_f32 v168, v138, v168\n"
      "v_cvt_pk_bf16_f32 v207, v208, v209\n"
      "v_mfma_f32_16x16x32_bf16 v[186:189], a[108:111], v[144:147], v[186:189]\n"
      "v_mul_f32 v141, v136, v141\n"
      "v_mul_f32 v121, v138, v121\n"
      "v_mfma_f32_16x16x32_bf16 v[186:189], a[112:115], v[158:161], v[186:189]\n"
      "buffer_load_dwordx4 v17, s[16:19], s45 offen  lds\n"
      "v_mfma_f32_16x16x32_bf16 v[186:189], a[116:119], v[148:151], v[186:189]\n"
      "ds_read_b128 v[172:175], v0 offset:14336\n"
      "v_cvt_pk_bf16_f32 v132, v132, v133\n"
      "v_mfma_f32_16x16x32_bf16 v[112:115], a[132:135], v[144:147], v[112:115]\n"
      "v_mul_f32 v118, v138, v118\n"
      "v_fmac_f32 v181, v199, v199\n"
      "v_mfma_f32_16x16x32_bf16 v[112:115], a[136:139], v[158:161], v[112:115]\n"
      "v_cvt_pk_bf16_f32 v109, v110, v111\n"
      "v_cvt_pk_bf16_f32 v209, v178, v179\n"
      "v_mfma_f32_16x16x32_bf16 v[112:115], a[140:143], v[148:151], v[112:115]\n"
      "s_add_u32 m0, s64, 26624\n"
      "v_fma_f32 v211, v182, v182, s51\n"
      "v_cvt_pk_bf16_f32 v208, v176, v177\n"
      "v_mfma_f32_16x16x32_bf16 v[194:197], v[124:127], a[144:147], 0\n"
      "buffer_load_dwordx4 v18, s[16:19], s45 offen  lds\n"
      "v_mfma_f32_16x16x32_bf16 v[194:197], v[100:103], a[148:151], v[194:197]\n"
      "ds_write_b128 v5, v[206:209] offset:6144\n"
      "v_mfma_f32_16x16x32_bf16 v[194:197], v[190:193], a[152:155], v[194:197]\n"
      "s_add_u32 m0, s64, 30720\n"
      "v_fmac_f32 v211, v183, v183\n"
      "v_fmac_f32 v211, v184, v184\n"
      "v_mfma_f32_16x16x32_bf16 v[194:197], v[144:147], a[156:159], v[194:197]\n"
      "v_min_u32 v39, 96, v39\n"
      "v_fmac_f32_e64 v211, v185, v185\n"
      "v_mfma_f32_16x16x32_bf16 v[194:197], v[158:161], a[160:163], v[194:197]\n"
      "ds_write_b64 v6, v[108:109] offset:6144\n"
      "v_mfma_f32_16x16x32_bf16 v[194:197], v[148:151], a[164:167], v[194:197]\n"
      "buffer_load_dwordx4 v19, s[16:19], s45 offen  lds\n"
      "v_mfma_f32_16x16x32_bf16 v[104:107], v[124:127], a[168:171], 0\n"
      "s_mov_b32 m0, s7\n"
      "v_cndmask_b32 v152, v152, v210, s[58:59]\n"
      "v_fmac_f32 v181, v200, v200\n"
      "v_mfma_f32_16x16x32_bf16 v[104:107], v[100:103], a[172:175], v[104:107]\n"
      "v_cvt_pk_bf16_f32 v140, v140, v141\n"
      "v_fmac_f32 v211, v186, v186\n"
      "v_mfma_f32_16x16x32_bf16 v[104:107], v[190:193], a[176:179], v[104:107]\n"
      "ds_read_b128 v[220:223], v1 offset:2048\n"
      "v_mul_f32 v134, v138, v134\n"
      "v_mfma_f32_16x16x32_bf16 v[104:107], v[144:147], a[180:183], v[104:107]\n"
      "ds_read_b128 v[206:209], v1 offset:14336\n"
      "v_fmac_f32_e64 v211, v187, v187\n"
      "v_mfma_f32_16x16x32_bf16 v[104:107], v[158:161], a[184:187], v[104:107]\n"
      "buffer_load_dwordx4 v15, s[0:3], s4 offen  lds\n"
      "v_mfma_f32_16x16x32_bf16 v[104:107], v[148:151], a[188:191], v[104:107]\n"
      "v_cvt_pk_bf16_f32 v141, v142, v143\n"
      "v_cvt_pk_bf16_f32 v168, v168, v169\n"
      "v_mfma_f32_16x16x32_bf16 v[96:99], v[124:127], a[192:195], 0\n"
      "ds_read_b128 v[176:179], v0 offset:8192\n"
      "v_readlane_b32 s86, v38, s43\n"
      "v_mfma_f32_16x16x32_bf16 v[96:99], v[100:103], a[196:199], v[96:99]\n"
      "v_fmac_f32 v181, v201, v201\n"
      "v_fmac_f32 v181, v212, v212\n"
      "v_mfma_f32_16x16x32_bf16 v[96:99], v[190:193], a[200:203], v[96:99]\n"
      "ds_write_b64 v6, v[140:141]\n"
      "v_mfma_f32_16x16x32_bf16 v[96:99], v[144:147], a[204:207], v[96:99]\n"
      "v_cvt_pk_bf16_f32 v133, v134, v135\n"
      "v_fmac_f32 v211, v188, v188\n"
      "v_mfma_f32_16x16x32_bf16 v[96:99], v[158:161], a[208:211], v[96:99]\n"
      "buffer_load_dwordx4 v[124:127], v36, s[28:31], s47 offen\n"
      "v_mfma_f32_16x16x32_bf16 v[96:99], v[148:151], a[212:215], v[96:99]\n"
      "s_waitcnt lgkmcnt(2)\n"
      "v_fmac_f32 v181, v213, v213\n"
      "v_fmac_f32 v211, v189, v189\n"
      "v_mfma_f32_16x16x32_bf16 v[148:151], a[0:3], v[154:157], 0\n"
      "v_or3_b32 v128, v128, v129, v130\n"
      "v_bfe_u32 v84, v166, 4, 4\n"
      "v_mfma_f32_16x16x32_bf16 v[148:151], a[4:7], v[220:223], v[148:151]\n"
      "s_waitcnt lgkmcnt(1)\n"
      "v_mov_b32_dpp v162, v162 quad_perm:[0,0,0,0] row_mask:0xf bank_mask:0xf\n"
      "v_fmac_f32 v211, v112, v112\n"
      "v_mfma_f32_16x16x32_bf16 v[148:151], a[8:11], v[176:179], v[148:151]\n"
      "v_cvt_pk_bf16_f32 v135, v120, v121\n"
      "v_cndmask_b32 v152, v152, v180, s[60:61]\n"
      "v_mfma_f32_16x16x32_bf16 v[148:151], a[12:15], v[216:219], v[148:151]\n"
      "ds_read_b128 v[144:147], v0 offset:4096\n"
      "v_fmac_f32_e64 v181, v214, v214\n"
      "v_mfma_f32_16x16x32_bf16 v[148:151], a[16:19], v[172:175], v[148:151]\n"
      "buffer_load_dword v78, v29, s[28:31], s47 offen\n"
      "v_mfma_f32_16x16x32_bf16 v[148:151], a[20:23], v[206:209], v[148:151]\n"
      "v_cvt_pk_bf16_f32 v169, v170, v171\n"
      "v_fmac_f32 v181, v215, v215\n"
      "v_mfma_f32_16x16x32_bf16 v[108:111], a[24:27], v[154:157], 0\n"
      "v_fmac_f32 v211, v113, v113\n"
      "v_lshrrev_b32_e64 v166, 8, v166\n"
      "v_mfma_f32_16x16x32_bf16 v[108:111], a[28:31], v[220:223], v[108:111]\n"
      "v_mul_f32_e64 v119, v138, v119\n"
      "v_cvt_pk_bf16_f32 v134, v118, v119\n"
      "v_mfma_f32_16x16x32_bf16 v[108:111], a[32:35], v[176:179], v[108:111]\n"
      "ds_write_b128 v5, v[132:135] offset:12288\n"
      "v_mfma_f32_16x16x32_bf16 v[108:111], a[36:39], v[216:219], v[108:111]\n"
      "v_fmac_f32_e64 v181, v202, v202\n"
      "v_fma_f32 v210, v148, v148, s51\n"
      "v_mfma_f32_16x16x32_bf16 v[108:111], a[40:43], v[172:175], v[108:111]\n"
      "v_cvt_pk_bf16_f32 v194, v194, v195\n"
      "v_fmac_f32_e64 v181, v203, v203\n"
      "v_mfma_f32_16x16x32_bf16 v[108:111], a[44:47], v[206:209], v[108:111]\n"
      "ds_write_b64 v6, v[168:169] offset:12288\n"
      "v_mfma_f32_16x16x32_bf16 v[100:103], a[48:51], v[154:157], 0\n"
      "v_fmac_f32 v211, v114, v114\n"
      "v_fmac_f32 v181, v204, v204\n"
      "v_mfma_f32_16x16x32_bf16 v[100:103], a[52:55], v[220:223], v[100:103]\n"
      "v_fmac_f32 v211, v115, v115\n"
      "v_fmac_f32 v210, v149, v149\n"
      "v_mfma_f32_16x16x32_bf16 v[100:103], a[56:59], v[176:179], v[100:103]\n"
      "v_cvt_pk_bf16_f32 v96, v96, v97\n"
      "v_fmac_f32_e64 v181, v205, v205\n"
      "v_mfma_f32_16x16x32_bf16 v[100:103], a[60:63], v[216:219], v[100:103]\n"
      "s_nop 0\n"
      "v_permlane16_swap_b32 v181, v211\n"
      "v_mfma_f32_16x16x32_bf16 v[100:103], a[64:67], v[172:175], v[100:103]\n"
      "v_fmac_f32 v210, v150, v150\n"
      "v_fmac_f32 v210, v151, v151\n"
      "v_mfma_f32_16x16x32_bf16 v[100:103], a[68:71], v[206:209], v[100:103]\n"
      "v_mul_u32_u24 v82, 98, v84\n"
      "v_add_f32 v181, v181, v211\n"
      "v_mfma_f32_16x16x32_bf16 v[136:139], a[72:75], v[154:157], 0\n"
      "v_mov_b32 v211, v181\n"
      "v_cvt_pk_bf16_f32 v97, v98, v99\n"
      "v_mfma_f32_16x16x32_bf16 v[136:139], a[76:79], v[220:223], v[136:139]\n"
      "v_bitop3_b32 v128, v128, v131, s33 bitop3:0xa8\n"
      "v_cvt_pk_bf16_f32 v195, v196, v197\n"
      "v_mfma_f32_16x16x32_bf16 v[136:139], a[80:83], v[176:179], v[136:139]\n"
      "v_permlane32_swap_b32 v181, v211\n"
      "v_mfma_f32_16x16x32_bf16 v[136:139], a[84:87], v[216:219], v[136:139]\n"
      "v_readlane_b32 s85, v37, s42\n"
      "v_add_f32 v181, v181, v211\n"
      "v_mfma_f32_16x16x32_bf16 v[136:139], a[88:91], v[172:175], v[136:139]\n"
      "v_rsq_f32 v181, v181\n"
      "v_mfma_f32_16x16x32_bf16 v[136:139], a[92:95], v[206:209], v[136:139]\n"
      "v_cvt_pk_bf16_f32 v104, v104, v105\n"
      "v_mov_b32 v211, v181\n"
      "v_mfma_f32_16x16x32_bf16 v[120:123], a[96:99], v[154:157], 0\n"
      "v_fmac_f32 v210, v108, v108\n"
      "v_lshl_add_u32 v82, v166, 3, v82\n"
      "v_mfma_f32_16x16x32_bf16 v[120:123], a[100:103], v[220:223], v[120:123]\n"
      "v_rcp_f32 v162, v162\n"
      "v_mfma_f32_16x16x32_bf16 v[120:123], a[104:107], v[176:179], v[120:123]\n"
      "v_fmac_f32 v210, v109, v109\n"
      "v_fma_f32 v197, v136, v136, s51\n"
      "v_mfma_f32_16x16x32_bf16 v[120:123], a[108:111], v[216:219], v[120:123]\n"
      "v_permlane16_swap_b32 v181, v211\n"
      "v_mfma_f32_16x16x32_bf16 v[120:123], a[112:115], v[172:175], v[120:123]\n"
      "v_fmac_f32 v197, v137, v137\n"
      "v_mul_f32 v112, v211, v112\n"
      "v_mfma_f32_16x16x32_bf16 v[120:123], a[116:119], v[206:209], v[120:123]\n"
      "v_cmp_ne_u32 s[56:57], 0, v128\n"
      "v_fmac_f32 v210, v110, v110\n"
      "v_mfma_f32_16x16x32_bf16 v[128:131], a[120:123], v[154:157], 0\n"
      "v_mul_f32 v183, v211, v183\n"
      "v_lshlrev_b32_e64 v166, 2, v166\n"
      "v_mfma_f32_16x16x32_bf16 v[128:131], a[124:127], v[220:223], v[128:131]\n"
      "ds_read_b128 v[140:143], v1 offset:4096\n"
      "v_mul_f32 v182, v211, v182\n"
      "v_mfma_f32_16x16x32_bf16 v[128:131], a[128:131], v[176:179], v[128:131]\n"
      "v_fmac_f32 v197, v138, v138\n"
      "v_sub_u32_e64 v84, v84, v166\n"
      "v_mfma_f32_16x16x32_bf16 v[128:131], a[132:135], v[216:219], v[128:131]\n"
      "s_or_b32 s95, s84, s85\n"
      "v_cvt_pk_bf16_f32 v105, v106, v107\n"
      "v_add_u32 v82, s77, v82\n"
      "v_mfma_f32_16x16x32_bf16 v[128:131], a[136:139], v[172:175], v[128:131]\n"
      "v_mul_f32 v152, v162, v152\n"
      "v_fmac_f32 v210, v111, v111\n"
      "v_mfma_f32_16x16x32_bf16 v[128:131], a[140:143], v[206:209], v[128:131]\n"
      "v_fmac_f32 v197, v139, v139\n"
      "v_fmac_f32 v197, v120, v120\n"
      "v_mfma_f32_16x16x32_bf16 v[160:163], v[154:157], a[144:147], 0\n"
      "v_fmac_f32 v197, v121, v121\n"
      "v_mfma_f32_16x16x32_bf16 v[160:163], v[220:223], a[148:151], v[160:163]\n"
      "v_cvt_pk_bf16_f32 v152, v152, v152\n"
      "v_fmac_f32 v210, v100, v100\n"
      "v_mfma_f32_16x16x32_bf16 v[160:163], v[176:179], a[152:155], v[160:163]\n"
      "ds_read_b128 v[166:169], v0 offset:16384\n"
      "v_fmac_f32 v197, v122, v122\n"
      "v_mfma_f32_16x16x32_bf16 v[160:163], v[216:219], a[156:159], v[160:163]\n"
      "v_mul_f32 v185, v211, v185\n"
      "v_mul_f32_e64 v181, s50, v181\n"
      "v_mfma_f32_16x16x32_bf16 v[160:163], v[172:175], a[160:163], v[160:163]\n"
      "v_fmac_f32 v210, v101, v101\n"
      "v_fmac_f32 v197, v123, v123\n"
      "v_mfma_f32_16x16x32_bf16 v[160:163], v[206:209], a[164:167], v[160:163]\n"
      "v_fmac_f32 v197, v128, v128\n"
      "v_fmac_f32 v197, v129, v129\n"
      "v_mfma_f32_16x16x32_bf16 v[116:119], v[154:157], a[168:171], 0\n"
      "v_fmac_f32 v210, v102, v102\n"
      "v_mul_f32 v198, v181, v198\n"
      "v_mfma_f32_16x16x32_bf16 v[116:119], v[220:223], a[172:175], v[116:119]\n"
      "v_mul_f32 v199, v181, v199\n"
      "v_mul_f32 v202, v181, v202\n"
      "v_mfma_f32_16x16x32_bf16 v[116:119], v[176:179], a[176:179], v[116:119]\n"
      "v_fmac_f32 v210, v103, v103\n"
      "v_fmac_f32 v197, v130, v130\n"
      "v_mfma_f32_16x16x32_bf16 v[116:119], v[216:219], a[180:183], v[116:119]\n"
      "v_mul_f32 v201, v181, v201\n"
      "v_mul_f32 v113, v211, v113\n"
      "v_mfma_f32_16x16x32_bf16 v[116:119], v[172:175], a[184:187], v[116:119]\n"
      "ds_read_b128 v[132:135], v0 offset:10240\n"
      "v_fmac_f32 v197, v131, v131\n"
      "v_mfma_f32_16x16x32_bf16 v[116:119], v[206:209], a[188:191], v[116:119]\n"
      "s_nop 0\n"
      "v_permlane16_swap_b32 v210, v197\n"
      "v_mfma_f32_16x16x32_bf16 v[190:193], v[154:157], a[192:195], 0\n"
      "ds_read_b128 v[154:157], v1 offset:10240\n"
      "v_lshlrev_b32 v84, 4, v84\n"
      "v_mfma_f32_16x16x32_bf16 v[190:193], v[220:223], a[196:199], v[190:193]\n"
      "v_add_f32 v210, v210, v197\n"
      "v_mov_b32 v180, 0x3fb9\n"
      "v_mfma_f32_16x16x32_bf16 v[190:193], v[176:179], a[200:203], v[190:193]\n"
      "ds_read_b128 v[220:223], v1 offset:16384\n"
      "v_mul_f32 v203, v181, v203\n"
      "v_mfma_f32_16x16x32_bf16 v[190:193], v[216:219], a[204:207], v[190:193]\n"
      "v_mul_f32 v205, v181, v205\n"
      "v_mul_f32 v187, v211, v187\n"
      "v_mfma_f32_16x16x32_bf16 v[190:193], v[172:175], a[208:211], v[190:193]\n"
      "s_waitcnt lgkmcnt(2)\n"
      "v_mul_f32 v184, v211, v184\n"
      "v_mov_b32 v197, v210\n"
      "v_mfma_f32_16x16x32_bf16 v[190:193], v[206:209], a[212:215], v[190:193]\n"
      "s_nop 0\n"
      "v_permlane32_swap_b32 v210, v197\n"
      "v_mfma_f32_16x16x32_bf16 v[216:219], a[0:3], v[144:147], 0\n"
      "v_add_f32 v210, v210, v197\n"
      "v_cvt_pk_bf16_f32 v112, v112, v113\n"
      "v_mfma_f32_16x16x32_bf16 v[216:219], a[4:7], v[140:143], v[216:219]\n"
      "v_cvt_pk_bf16_f32 v182, v182, v183\n"
      "v_mul_f32_e64 v200, v181, v200\n"
      "v_mfma_f32_16x16x32_bf16 v[216:219], a[8:11], v[132:135], v[216:219]\n"
      "s_waitcnt lgkmcnt(0)\n"
      "v_rsq_f32 v210, v210\n"
      "v_mfma_f32_16x16x32_bf16 v[216:219], a[12:15], v[154:157], v[216:219]\n"
      "v_mul_f32 v204, v181, v204\n"
      "v_mul_f32 v212, v181, v212\n"
      "v_mfma_f32_16x16x32_bf16 v[216:219], a[16:19], v[166:169], v[216:219]\n"
      "v_cvt_pk_bf16_f32 v183, v184, v185\n"
      "v_mov_b32 v197, v210\n"
      "v_mfma_f32_16x16x32_bf16 v[216:219], a[20:23], v[220:223], v[216:219]\n"
      "s_nop 0\n"
      "v_permlane16_swap_b32 v210, v197\n"
      "v_mfma_f32_16x16x32_bf16 v[206:209], a[24:27], v[144:147], 0\n"
      "ds_write_b16 v12, v152 offset:16\n"
      "v_mfma_f32_16x16x32_bf16 v[206:209], a[28:31], v[140:143], v[206:209]\n"
      "v_mul_f32 v210, s50, v210\n"
      "v_mul_f32 v186, v211, v186\n"
      "v_mfma_f32_16x16x32_bf16 v[206:209], a[32:35], v[132:135], v[206:209]\n"
      "v_mul_f32 v129, v197, v129\n"
      "v_fma_f32 v152, v216, v216, s51\n"
      "v_mfma_f32_16x16x32_bf16 v[206:209], a[36:39], v[154:157], v[206:209]\n"
      "v_cvt_pk_bf16_f32 v196, v160, v161\n"
      "v_mul_f32 v213, v181, v213\n"
      "v_mfma_f32_16x16x32_bf16 v[206:209], a[40:43], v[166:169], v[206:209]\n"
      "s_or_b32 s95, s95, s48\n"
      "v_fmac_f32 v152, v217, v217\n"
      "v_cvt_pk_bf16_f32 v202, v202, v203\n"
      "v_mfma_f32_16x16x32_bf16 v[206:209], a[44:47], v[220:223], v[206:209]\n"
      "v_cvt_pk_bf16_f32 v184, v186, v187\n"
      "v_mul_f32 v149, v210, v149\n"
      "v_mul_f32 v138, v197, v138\n"
      "v_fmac_f32 v152, v218, v218\n"
      "v_cvt_pk_bf16_f32 v98, v190, v191\n"
      "v_pk_mul_f32 v[120:121], v[120:121], v[196:197] op_sel:[0,1]\n"
      "v_pk_mul_f32 v[114:115], v[114:115], v[210:211] op_sel:[0,1]\n"
      "v_pk_mul_f32 v[214:215], v[214:215], v[180:181] op_sel:[0,1]\n"
      "v_pk_mul_f32 v[188:189], v[188:189], v[210:211] op_sel:[0,1]\n"
      "v_pk_mul_f32 v[122:123], v[122:123], v[196:197] op_sel:[0,1]\n"
      "v_pk_mul_f32 v[136:137], v[136:137], v[196:197] op_sel:[0,1]\n"
      "v_pk_mul_f32 v[100:101], v[100:101], v[210:211] op_sel_hi:[1,0]\n"
      "v_pk_mul_f32 v[110:111], v[110:111], v[210:211] op_sel_hi:[1,0]\n"
      "v_pk_mul_f32 v[108:109], v[108:109], v[210:211] op_sel_hi:[1,0]\n"
      "v_mfma_f32_16x16x32_bf16 v[158:161], a[48:51], v[144:147], 0\n"
      "s_add_u32 s73, s73, 1024\n"
      "v_cvt_pk_bf16_f32 v198, v198, v199\n"
      "v_fmac_f32 v152, v219, v219\n"
      "v_mfma_f32_16x16x32_bf16 v[158:161], a[52:55], v[140:143], v[158:161]\n"
      "v_mul_f32 v103, v210, v103\n"
      "v_cmp_gt_u32 s[72:73], 64, v84\n"
      "v_mfma_f32_16x16x32_bf16 v[158:161], a[56:59], v[132:135], v[158:161]\n"
      "v_mul_f32 v139, v197, v139\n"
      "v_fmac_f32 v152, v206, v206\n"
      "v_mfma_f32_16x16x32_bf16 v[158:161], a[60:63], v[154:157], v[158:161]\n"
      "v_cvt_pk_bf16_f32 v185, v188, v189\n"
      "v_mul_f32 v128, v197, v128\n"
      "v_mfma_f32_16x16x32_bf16 v[158:161], a[64:67], v[166:169], v[158:161]\n"
      "v_cvt_pk_bf16_f32 v199, v200, v201\n"
      "v_mul_f32 v150, v210, v150\n"
      "v_mfma_f32_16x16x32_bf16 v[158:161], a[68:71], v[220:223], v[158:161]\n"
      "v_cvt_pk_bf16_f32 v201, v214, v215\n"
      "v_and_b32 v93, 2, v82\n"
      "v_mfma_f32_16x16x32_bf16 v[186:189], a[72:75], v[144:147], 0\n"
      "v_cvt_pk_bf16_f32 v203, v204, v205\n"
      "v_fmac_f32 v152, v207, v207\n"
      "v_mfma_f32_16x16x32_bf16 v[186:189], a[76:79], v[140:143], v[186:189]\n"
      "v_cvt_pk_bf16_f32 v99, v192, v193\n"
      "v_cvt_pk_bf16_f32 v107, v118, v119\n"
      "v_mfma_f32_16x16x32_bf16 v[186:189], a[80:83], v[132:135], v[186:189]\n"
      "v_mul_f32 v148, v210, v148\n"
      "v_fmac_f32 v152, v208, v208\n"
      "v_mfma_f32_16x16x32_bf16 v[186:189], a[84:87], v[154:157], v[186:189]\n"
      "v_mul_f32 v151, v210, v151\n"
      "v_mul_f32 v130, v197, v130\n"
      "v_mfma_f32_16x16x32_bf16 v[186:189], a[88:91], v[166:169], v[186:189]\n"
      "v_mul_f32 v131, v197, v131\n"
      "v_cvt_pk_bf16_f32 v200, v212, v213\n"
      "v_mfma_f32_16x16x32_bf16 v[186:189], a[92:95], v[220:223], v[186:189]\n"
      "s_and_b32 s86, s86, -4\n"
      "v_add_u32 v39, s77, v39\n"
      "v_fmac_f32 v152, v209, v209\n"
      "v_mfma_f32_16x16x32_bf16 v[176:179], a[96:99], v[144:147], 0\n"
      "v_cvt_pk_bf16_f32 v100, v100, v101\n"
      "v_cvt_pk_bf16_f32 v113, v114, v115\n"
      "v_mfma_f32_16x16x32_bf16 v[176:179], a[100:103], v[140:143], v[176:179]\n"
      "s_cmp_ge_u32 s95, 0x40000000\n"
      "s_cselect_b32 s94, -1, -2\n"
      "v_fma_f32 v171, v186, v186, s51\n"
      "v_mfma_f32_16x16x32_bf16 v[176:179], a[104:107], v[132:135], v[176:179]\n"
      "v_mov_b32 v181, 0\n"
      "v_mul_f32 v102, v210, v102\n"
      "v_mfma_f32_16x16x32_bf16 v[176:179], a[108:111], v[154:157], v[176:179]\n"
      "v_fmac_f32 v171, v187, v187\n"
      "v_fmac_f32 v171, v188, v188\n"
      "v_mfma_f32_16x16x32_bf16 v[176:179], a[112:115], v[166:169], v[176:179]\n"
      "v_fmac_f32_e64 v152, v158, v158\n"
      "v_cvt_pk_bf16_f32 v136, v136, v137\n"
      "v_mfma_f32_16x16x32_bf16 v[176:179], a[116:119], v[220:223], v[176:179]\n"
      "v_cvt_pk_bf16_f32 v106, v116, v117\n"
      "v_and_b32_e64 v82, -4, v82\n"
      "v_mfma_f32_16x16x32_bf16 v[210:213], a[120:123], v[144:147], 0\n"
      "v_fmac_f32_e64 v152, v159, v159\n"
      "v_cvt_pk_bf16_f32 v137, v138, v139\n"
      "v_mfma_f32_16x16x32_bf16 v[210:213], a[124:127], v[140:143], v[210:213]\n"
      "v_cvt_pk_bf16_f32 v148, v148, v149\n"
      "v_cvt_pk_bf16_f32 v138, v120, v121\n"
      "v_mfma_f32_16x16x32_bf16 v[210:213], a[128:131], v[132:135], v[210:213]\n"
      "v_fmac_f32_e64 v171, v189, v189\n"
      "v_cvt_pk_bf16_f32 v197, v162, v163\n"
      "v_mfma_f32_16x16x32_bf16 v[210:213], a[132:135], v[154:157], v[210:213]\n"
      "ds_read_b128 v[172:175], v7 offset:18432\n"
      "v_fmac_f32_e64 v152, v160, v160\n"
      "v_mfma_f32_16x16x32_bf16 v[210:213], a[136:139], v[166:169], v[210:213]\n"
      "v_cvt_pk_bf16_f32 v149, v150, v151\n"
      "v_cvt_pk_bf16_f32 v151, v110, v111\n"
      "v_mfma_f32_16x16x32_bf16 v[210:213], a[140:143], v[220:223], v[210:213]\n"
      "v_cvt_pk_bf16_f32 v139, v122, v123\n"
      "v_fmac_f32 v171, v176, v176\n"
      "v_mfma_f32_16x16x32_bf16 v[118:121], v[144:147], a[144:147], 0\n"
      "s_waitcnt vmcnt(7)\n"
      "v_cvt_pk_bf16_f32 v128, v128, v129\n"
      "v_fmac_f32 v171, v177, v177\n"
      "v_mfma_f32_16x16x32_bf16 v[118:121], v[140:143], a[148:151], v[118:121]\n"
      "v_fmac_f32 v171, v178, v178\n"
      "v_cvt_pk_bf16_f32 v101, v102, v103\n"
      "v_mfma_f32_16x16x32_bf16 v[118:121], v[132:135], a[152:155], v[118:121]\n"
      "s_cmp_gt_u32 s42, s44\n"
      "s_cselect_b32 s95, s94, s95\n"
      "v_fmac_f32_e64 v171, v179, v179\n"
      "v_mfma_f32_16x16x32_bf16 v[118:121], v[154:157], a[156:159], v[118:121]\n"
      "v_lshlrev_b64 v[180:181], v84, v[180:181]\n"
      "v_cvt_pk_bf16_f32 v150, v108, v109\n"
      "v_mfma_f32_16x16x32_bf16 v[118:121], v[166:169], a[160:163], v[118:121]\n"
      "v_add_u32 v39, 115296, v39\n"
      "v_fmac_f32_e64 v171, v210, v210\n"
      "v_mfma_f32_16x16x32_bf16 v[118:121], v[220:223], a[164:167], v[118:121]\n"
      "v_fmac_f32 v171, v211, v211\n"
      "v_fmac_f32 v152, v161, v161\n"
      "v_mfma_f32_16x16x32_bf16 v[108:111], v[144:147], a[168:171], 0\n"
      "v_cvt_pk_bf16_f32 v129, v130, v131\n"
      "v_fmac_f32 v171, v212, v212\n"
      "v_mfma_f32_16x16x32_bf16 v[108:111], v[140:143], a[172:175], v[108:111]\n"
      "ds_read_b128 v[114:117], v2 offset:18432\n"
      "v_fmac_f32 v171, v213, v213\n"
      "v_mfma_f32_16x16x32_bf16 v[108:111], v[132:135], a[176:179], v[108:111]\n"
      "s_nop 0\n"
      "v_permlane16_swap_b32 v152, v171\n"
      "v_mfma_f32_16x16x32_bf16 v[108:111], v[154:157], a[180:183], v[108:111]\n"
      "v_add_f32 v152, v152, v171\n"
      "v_mov_b32 v171, v152\n"
      "v_mfma_f32_16x16x32_bf16 v[108:111], v[166:169], a[184:187], v[108:111]\n"
      "s_nop 0\n"
      "v_permlane32_swap_b32 v152, v171\n"
      "v_mfma_f32_16x16x32_bf16 v[108:111], v[220:223], a[188:191], v[108:111]\n"
      "ds_read_b128 v[190:193], v2 offset:21504\n"
      "v_add_f32_e64 v152, v152, v171\n"
      "v_mfma_f32_16x16x32_bf16 v[162:165], v[144:147], a[192:195], 0\n"
      "s_waitcnt lgkmcnt(2)\n"
      "v_rsq_f32 v152, v152\n"
      "v_mfma_f32_16x16x32_bf16 v[162:165], v[140:143], a[196:199], v[162:165]\n"
      "ds_read_b128 v[144:147], v2 offset:24576\n"
      "v_mov_b32 v171, v152\n"
      "v_mfma_f32_16x16x32_bf16 v[162:165], v[132:135], a[200:203], v[162:165]\n"
      "s_nop 0\n"
      "v_permlane16_swap_b32_e64 v152, v171\n"
      "v_mfma_f32_16x16x32_bf16 v[162:165], v[154:157], a[204:207], v[162:165]\n"
      "ds_read_b128 v[140:143], v7 offset:22528\n"
      "v_mul_f32_e64 v152, s50, v152\n"
      "v_mfma_f32_16x16x32_bf16 v[162:165], v[166:169], a[208:211], v[162:165]\n"
      "buffer_store_dwordx4 v[172:175], v20, s[24:27], s46 offen sc1\n"
      "v_pk_mul_f32 v[186:187], v[186:187], v[170:171] op_sel:[0,1]\n"
      "v_pk_mul_f32 v[178:179], v[178:179], v[170:171] op_sel:[0,1]\n"
      "v_pk_mul_f32 v[212:213], v[212:213], v[170:171] op_sel:[0,1]\n"
      "v_pk_mul_f32 v[176:177], v[176:177], v[170:171] op_sel:[0,1]\n"
      "v_pk_mul_f32 v[210:211], v[210:211], v[170:171] op_sel:[0,1]\n"
      "v_pk_mul_f32 v[158:159], v[158:159], v[152:153] op_sel_hi:[1,0]\n"
      "v_pk_mul_f32 v[216:217], v[216:217], v[152:153] op_sel_hi:[1,0]\n"
      "v_pk_mul_f32 v[188:189], v[188:189], v[170:171] op_sel:[0,1]\n"
      "v_mfma_f32_16x16x32_bf16 v[162:165], v[220:223], a[212:215], v[162:165]\n"
      "s_waitcnt lgkmcnt(1)\n"
      "v_cndmask_b32_e64 v180, 0, v180, s[72:73]\n"
      "v_cvt_pk_bf16_f32 v81, v110, v111\n"
      "v_mfma_f32_16x16x32_bf16 v[172:175], a[216:219], v[114:117], 0\n"
      "ds_read_b128 v[132:135], v2 offset:27648\n"
      "v_add_u32 v167, 110592, v82\n"
      "v_mfma_f32_16x16x32_bf16 v[172:175], a[220:223], v[190:193], v[172:175]\n"
      "s_waitcnt lgkmcnt(1)\n"
      "v_cvt_pk_bf16_f32 v80, v108, v109\n"
      "v_cvt_pk_bf16_f32 v186, v186, v187\n"
      "v_mfma_f32_16x16x32_bf16 v[172:175], a[224:227], v[144:147], v[172:175]\n"
      "ds_read2_b32 v[156:157], v167 offset0:0 offset1:1\n"
      "v_cndmask_b32_e64 v181, 0, v181, s[72:73]\n"
      "v_mfma_f32_16x16x32_bf16 v[220:223], a[240:243], v[114:117], 0\n"
      "ds_read_b128 v[108:111], v2 offset:33792\n"
      "v_cvt_pk_bf16_f32 v85, v164, v165\n"
      "v_mfma_f32_16x16x32_bf16 v[220:223], a[244:247], v[190:193], v[220:223]\n"
      "ds_read_b128 v[168:171], v2 offset:30720\n"
      "v_cvt_pk_bf16_f32 v187, v188, v189\n"
      "v_mfma_f32_16x16x32_bf16 v[220:223], a[248:251], v[144:147], v[220:223]\n"
      "buffer_store_dwordx4 v[140:143], v21, s[24:27], s46 offen sc1\n"
      "v_pk_mul_f32 v[160:161], v[160:161], v[152:153] op_sel_hi:[1,0]\n"
      "v_pk_mul_f32 v[218:219], v[218:219], v[152:153] op_sel_hi:[1,0]\n"
      "v_pk_mul_f32 v[208:209], v[208:209], v[152:153] op_sel_hi:[1,0]\n"
      "v_pk_mul_f32 v[206:207], v[206:207], v[152:153] op_sel_hi:[1,0]\n"
      "v_mfma_f32_16x16x32_bf16 v[140:143], v[232:235], v[114:117], 0\n"
      "ds_read2_b32 v[102:103], v167 offset0:24 offset1:18\n"
      "v_cvt_pk_bf16_f32 v158, v158, v159\n"
      "v_mfma_f32_16x16x32_bf16 v[140:143], v[236:239], v[190:193], v[140:143]\n"
      "ds_read_b128 v[152:155], v7 offset:26624\n"
      "v_cvt_pk_bf16_f32 v216, v216, v217\n"
      "v_mfma_f32_16x16x32_bf16 v[140:143], v[240:243], v[144:147], v[140:143]\n"
      "s_waitcnt lgkmcnt(2)\n"
      "v_alignbyte_b32 v156, v157, v156, v93\n"
      "v_cvt_pk_bf16_f32 v77, v120, v121\n"
      "v_mfma_f32_16x16x32_bf16 v[172:175], a[228:231], v[132:135], v[172:175]\n"
      "ds_read_b64 v[122:123], v10 offset:24\n"
      "v_cvt_pk_bf16_f32 v210, v210, v211\n"
      "v_mfma_f32_16x16x32_bf16 v[172:175], a[232:235], v[168:171], v[172:175]\n"
      "ds_read2_b32 v[164:165], v167 offset0:2 offset1:10\n"
      "v_cvt_pk_bf16_f32 v76, v118, v119\n"
      "v_mfma_f32_16x16x32_bf16 v[172:175], a[236:239], v[108:111], v[172:175]\n"
      "ds_read2_b32 v[190:191], v167 offset0:8 offset1:9\n"
      "v_cvt_pk_bf16_f32 v159, v160, v161\n"
      "v_mfma_f32_16x16x32_bf16 v[220:223], a[252:255], v[132:135], v[220:223]\n"
      "ds_read_b128 v[118:121], v7 offset:30720\n"
      "v_cvt_pk_bf16_f32 v188, v176, v177\n"
      "v_mfma_f32_16x16x32_bf16 v[220:223], v[224:227], v[168:171], v[220:223]\n"
      "s_waitcnt lgkmcnt(4)\n"
      "v_cvt_pk_bf16_f32 v189, v178, v179\n"
      "v_cvt_pk_bf16_f32 v217, v218, v219\n"
      "v_mfma_f32_16x16x32_bf16 v[220:223], v[228:231], v[108:111], v[220:223]\n"
      "ds_read2_b32 v[192:193], v167 offset0:16 offset1:17\n"
      "v_fma_f32 v160, v172, v172, s52\n"
      "v_mfma_f32_16x16x32_bf16 v[140:143], v[244:247], v[132:135], v[140:143]\n"
      "ds_read_b128 v[144:147], v10 offset:32\n"
      "v_add_u32 v115, 112160, v82\n"
      "v_mfma_f32_16x16x32_bf16 v[140:143], v[248:251], v[168:171], v[140:143]\n"
      "buffer_store_dwordx4 v[152:155], v22, s[24:27], s46 offen sc1\n"
      "v_mfma_f32_16x16x32_bf16 v[140:143], v[252:255], v[108:111], v[140:143]\n"
      "ds_read_u16 v39, v39\n"
      "v_mov_b32 v167, 0\n"
      "v_mfma_f32_16x16x32_bf16 v[132:135], v[182:185], v[198:201], v[40:43]\n"
      "s_waitcnt lgkmcnt(3)\n"
      "v_cmp_gt_u32 s[72:73], 64, v84\n"
      "v_alignbyte_b32 v157, v164, v157, v93\n"
      "v_mfma_f32_16x16x32_bf16 v[168:171], v[136:139], v[198:201], v[44:47]\n"
      "s_waitcnt lgkmcnt(2)\n"
      "v_mov_b32 v166, 0xbb2b\n"
      "v_cvt_pk_bf16_f32 v211, v212, v213\n"
      "v_mfma_f32_16x16x32_bf16 v[152:155], v[186:189], v[198:201], v[48:51]\n"
      "s_waitcnt lgkmcnt(1)\n"
      "v_alignbyte_b32 v192, v193, v192, v93\n"
      "v_lshlrev_b64 v[166:167], v84, v[166:167]\n"
      "v_mfma_f32_16x16x32_bf16 v[212:215], v[144:147], v[198:201], v[24:27]\n"
      "ds_read_b128 v[176:179], v7 offset:34816\n"
      "v_alignbyte_b32 v190, v191, v190, v93\n"
      "v_mfma_f32_16x16x16_bf16 v[132:135], v[112:113], v[202:203], v[132:135]\n"
      "v_cvt_pk_bf16_f32 v219, v208, v209\n"
      "v_alignbyte_b32 v193, v103, v193, v93\n"
      "v_mul_f32 v161, v173, v173\n"
      "v_mfma_f32_16x16x16_bf16 v[168:171], v[128:129], v[202:203], v[168:171]\n"
      "ds_read2_b32 v[130:131], v115 offset0:8 offset1:9\n"
      "v_alignbyte_b32 v191, v165, v191, v93\n"
      "v_mfma_f32_16x16x16_bf16 v[152:155], v[210:211], v[202:203], v[152:155]\n"
      "buffer_store_dwordx4 v[118:121], v23, s[24:27], s46 offen sc1\n"
      "v_mfma_f32_16x16x16_bf16 v[212:215], v[122:123], v[202:203], v[212:215]\n"
      "s_nop 0\n"
      "ds_read_b128 v[118:121], v2 offset:18688\n"
      "v_add_u32 v203, 113728, v82\n"
      "v_mfma_f32_16x16x16_bf16 v[132:135], v[180:181], v[156:157], v[132:135]\n"
      "v_cndmask_b32_e64 v166, 0, v166, s[72:73]\n"
      "v_fmac_f32_e64 v161, v175, v175\n"
      "v_alignbyte_b32 v102, v102, v102, v93\n"
      "v_mfma_f32_16x16x16_bf16 v[168:171], v[180:181], v[190:191], v[168:171]\n"
      "ds_read_b128 v[198:201], v2 offset:21760\n"
      "v_cvt_pk_bf16_f32 v218, v206, v207\n"
      "v_mfma_f32_16x16x16_bf16 v[152:155], v[180:181], v[192:193], v[152:155]\n"
      "v_cndmask_b32_e64 v167, 0, v167, s[72:73]\n"
      "v_fmac_f32_e64 v160, v174, v174\n"
      "v_cvt_pk_bf16_f32 v84, v162, v163\n"
      "v_mfma_f32_16x16x16_bf16 v[212:215], v[180:181], v[102:103], v[212:215]\n"
      "ds_read_b128 v[162:165], v2 offset:24832\n"
      "v_add_u32_e64 v12, 32, v12\n"
      "v_mfma_f32_16x16x16_bf16 v[132:135], v[166:167], v[156:157], v[132:135]\n"
      "ds_read2_b32 v[156:157], v115 offset0:2 offset1:10\n"
      "v_fmac_f32 v160, v220, v220\n"
      "v_mfma_f32_16x16x16_bf16 v[168:171], v[166:167], v[190:191], v[168:171]\n"
      "s_waitcnt lgkmcnt(3)\n"
      "v_lshlrev_b32 v39, 16, v39\n"
      "v_fmac_f32 v161, v221, v221\n"
      "v_mfma_f32_16x16x16_bf16 v[152:155], v[166:167], v[192:193], v[152:155]\n"
      "buffer_store_dwordx4 v[176:179], v25, s[24:27], s46 offen sc1\n"
      "v_pk_fma_f32 v[160:161], v[222:223], v[222:223], v[160:161]\n"
      "v_mfma_f32_16x16x16_bf16 v[212:215], v[166:167], v[102:103], v[212:215]\n"
      "ds_read2_b32 v[190:191], v115 offset0:0 offset1:1\n"
      "v_alignbyte_b32 v130, v131, v130, v93\n"
      "v_mfma_f32_16x16x32_bf16 v[206:209], a[216:219], v[118:121], 0\n"
      "s_waitcnt lgkmcnt(2)\n"
      "v_max3_f32 v102, v132, v133, v134\n"
      "v_max3_f32 v102, v102, v170, v171\n"
      "v_mfma_f32_16x16x32_bf16 v[206:209], a[220:223], v[198:201], v[206:209]\n"
      "s_waitcnt lgkmcnt(1)\n"
      "v_max3_f32 v214, v135, v168, v169\n"
      "v_max3_f32 v214, v214, v152, v153\n"
      "v_mfma_f32_16x16x32_bf16 v[206:209], a[224:227], v[162:165], v[206:209]\n"
      "v_alignbyte_b32 v131, v157, v131, v93\n"
      "v_max3_f32 v102, v102, v154, v155\n"
      "v_mfma_f32_16x16x32_bf16 v[176:179], a[240:243], v[118:121], 0\n"
      "v_max3_f32 v102, v102, v214, v212\n"
      "v_mov_b32_e64 v103, v102\n"
      "v_mfma_f32_16x16x32_bf16 v[176:179], a[244:247], v[198:201], v[176:179]\n"
      "s_nop 0\n"
      "v_permlane16_swap_b32 v102, v103\n"
      "v_mfma_f32_16x16x32_bf16 v[176:179], a[248:251], v[162:165], v[176:179]\n"
      "s_waitcnt lgkmcnt(0)\n"
      "v_max_f32 v102, v102, v103\n"
      "s_waitcnt vmcnt(6)\n"
      "v_or3_b32 v124, v124, v125, v126\n"
      "v_mfma_f32_16x16x32_bf16 v[108:111], v[232:235], v[118:121], 0\n"
      "ds_read2_b32 v[214:215], v115 offset0:16 offset1:17\n"
      "v_mov_b32 v103, v102\n"
      "v_mfma_f32_16x16x32_bf16 v[108:111], v[236:239], v[198:201], v[108:111]\n"
      "s_nop 0\n"
      "v_permlane32_swap_b32 v102, v103\n"
      "v_mfma_f32_16x16x32_bf16 v[108:111], v[240:243], v[162:165], v[108:111]\n"
      "ds_read2_b32 v[192:193], v115 offset0:24 offset1:18\n"
      "v_alignbyte_b32 v190, v191, v190, v93\n"
      "v_alignbyte_b32 v191, v156, v191, v93\n"
      "v_max_f32 v102, v102, v103\n"
      "v_sub_f32 v168, v168, v102\n"
      "v_pk_add_f32 v[134:135], v[134:135], v[102:103] op_sel_hi:[1,0] neg_lo:[0,1] neg_hi:[0,1]\n"
      "v_pk_add_f32 v[132:133], v[132:133], v[102:103] op_sel_hi:[1,0] neg_lo:[0,1] neg_hi:[0,1]\n"
      "v_pk_add_f32 v[170:171], v[170:171], v[102:103] op_sel_hi:[1,0] neg_lo:[0,1] neg_hi:[0,1]\n"
      "v_pk_add_f32 v[154:155], v[154:155], v[102:103] op_sel_hi:[1,0] neg_lo:[0,1] neg_hi:[0,1]\n"
      "v_pk_add_f32 v[152:153], v[152:153], v[102:103] op_sel_hi:[1,0] neg_lo:[0,1] neg_hi:[0,1]\n"
      "v_mfma_f32_16x16x32_bf16 v[198:201], v[182:185], v[148:151], v[52:55]\n"
      "v_exp_f32 v133, v133\n"
      "s_waitcnt vmcnt(5)\n"
      "v_or3_b32 v124, v124, v127, v78\n"
      "v_mfma_f32_16x16x32_bf16 v[118:121], v[136:139], v[148:151], v[56:59]\n"
      "ds_write_b32 v31, v124 offset:4608\n"
      "v_mfma_f32_16x16x32_bf16 v[162:165], v[186:189], v[148:151], v[60:63]\n"
      "v_exp_f32 v132, v132\n"
      "v_mfma_f32_16x16x32_bf16 v[124:127], v[144:147], v[148:151], v[26:29]\n"
      "s_waitcnt lgkmcnt(1)\n"
      "v_exp_f32 v152, v152\n"
      "v_mfma_f32_16x16x16_bf16 v[198:201], v[112:113], v[100:101], v[198:201]\n"
      "v_alignbyte_b32 v214, v215, v214, v93\n"
      "v_exp_f32 v134, v134\n"
      "v_mfma_f32_16x16x16_bf16 v[118:121], v[128:129], v[100:101], v[118:121]\n"
      "v_exp_f32 v154, v154\n"
      "v_cvt_pk_bf16_f32 v132, v132, v133\n"
      "v_mfma_f32_16x16x16_bf16 v[162:165], v[210:211], v[100:101], v[162:165]\n"
      "v_sub_f32 v169, v169, v102\n"
      "v_exp_f32 v171, v171\n"
      "v_mfma_f32_16x16x16_bf16 v[124:127], v[122:123], v[100:101], v[124:127]\n"
      "v_exp_f32 v155, v155\n"
      "v_alignbyte_b32 v215, v193, v215, v93\n"
      "v_mfma_f32_16x16x16_bf16 v[198:201], v[180:181], v[190:191], v[198:201]\n"
      "v_alignbyte_b32 v192, v192, v192, v93\n"
      "v_exp_f32 v169, v169\n"
      "v_mfma_f32_16x16x16_bf16 v[118:121], v[180:181], v[130:131], v[118:121]\n"
      "v_exp_f32 v170, v170\n"
      "v_exp_f32 v135, v135\n"
      "v_mfma_f32_16x16x16_bf16 v[162:165], v[180:181], v[214:215], v[162:165]\n"
      "v_sub_f32 v212, v212, v102\n"
      "v_exp_f32_e64 v153, v153\n"
      "v_mfma_f32_16x16x16_bf16 v[124:127], v[180:181], v[192:193], v[124:127]\n"
      "ds_read_b128 v[100:103], v2 offset:30976\n"
      "v_cvt_pk_bf16_f32 v89, v154, v155\n"
      "v_mfma_f32_16x16x16_bf16 v[198:201], v[166:167], v[190:191], v[198:201]\n"
      "ds_read_b128 v[154:157], v2 offset:27904\n"
      "v_cvt_pk_bf16_f32 v133, v134, v135\n"
      "v_mfma_f32_16x16x16_bf16 v[118:121], v[166:167], v[130:131], v[118:121]\n"
      "v_exp_f32 v212, v212\n"
      "v_cvt_pk_bf16_f32 v88, v152, v153\n"
      "v_mfma_f32_16x16x16_bf16 v[162:165], v[166:167], v[214:215], v[162:165]\n"
      "ds_read_b128 v[150:153], v2 offset:34048\n"
      "v_exp_f32 v168, v168\n"
      "v_mfma_f32_16x16x16_bf16 v[124:127], v[166:167], v[192:193], v[124:127]\n"
      "v_max3_f32 v215, v198, v199, v200\n"
      "v_cvt_pk_bf16_f32 v134, v168, v169\n"
      "v_max3_f32 v215, v215, v120, v121\n"
      "v_cvt_pk_bf16_f32 v135, v170, v171\n"
      "v_max3_f32 v78, v201, v118, v119\n"
      "v_max3_f32 v215, v215, v164, v165\n"
      "v_pk_fma_f32 v[160:161], v[140:141], v[140:141], v[160:161]\n"
      "v_pk_fma_f32 v[160:161], v[142:143], v[142:143], v[160:161]\n"
      "v_max3_f32 v78, v78, v162, v163\n"
      "v_max3_f32 v215, v215, v78, v124\n"
      "v_mov_b32 v126, v215\n"
      "s_waitcnt lgkmcnt(1)\n"
      "v_mfma_f32_16x16x32_bf16 v[206:209], a[228:231], v[154:157], v[206:209]\n"
      "v_permlane16_swap_b32 v215, v126\n"
      "v_mfma_f32_16x16x32_bf16 v[206:209], a[232:235], v[100:103], v[206:209]\n"
      "ds_read_u16 v78, v11\n"
      "s_waitcnt lgkmcnt(1)\n"
      "v_mfma_f32_16x16x32_bf16 v[206:209], a[236:239], v[150:153], v[206:209]\n"
      "v_add_f32 v160, v160, v161\n"
      "v_max_f32 v215, v215, v126\n"
      "v_mfma_f32_16x16x32_bf16 v[176:179], a[252:255], v[154:157], v[176:179]\n"
      "ds_read2_b32 v[114:115], v203 offset0:16 offset1:17\n"
      "v_mov_b32 v126, v215\n"
      "v_mfma_f32_16x16x32_bf16 v[176:179], v[224:227], v[100:103], v[176:179]\n"
      "ds_read_u16 v82, v11 offset:32\n"
      "v_cvt_pk_bf16_f32 v90, v212, 0\n"
      "v_permlane32_swap_b32 v215, v126\n"
      "v_max_f32_e64 v215, v215, v126\n"
      "v_pk_add_f32 v[118:119], v[118:119], v[214:215] op_sel:[0,1] neg_lo:[0,1] neg_hi:[0,1]\n"
      "v_pk_add_f32 v[120:121], v[120:121], v[214:215] op_sel:[0,1] neg_lo:[0,1] neg_hi:[0,1]\n"
      "v_pk_add_f32 v[164:165], v[164:165], v[214:215] op_sel:[0,1] neg_lo:[0,1] neg_hi:[0,1]\n"
      "v_pk_add_f32 v[200:201], v[200:201], v[214:215] op_sel:[0,1] neg_lo:[0,1] neg_hi:[0,1]\n"
      "v_pk_add_f32 v[198:199], v[198:199], v[214:215] op_sel:[0,1] neg_lo:[0,1] neg_hi:[0,1]\n"
      "v_mfma_f32_16x16x32_bf16 v[176:179], v[228:231], v[150:153], v[176:179]\n"
      "v_exp_f32_e64 v200, v200\n"
      "v_mfma_f32_16x16x32_bf16 v[108:111], v[244:247], v[154:157], v[108:111]\n"
      "s_waitcnt lgkmcnt(2)\n"
      "v_exp_f32 v198, v198\n"
      "v_mfma_f32_16x16x32_bf16 v[108:111], v[248:251], v[100:103], v[108:111]\n"
      "ds_read2_b32 v[116:117], v203 offset0:0 offset1:1\n"
      "v_exp_f32 v199, v199\n"
      "v_mfma_f32_16x16x32_bf16 v[108:111], v[252:255], v[150:153], v[108:111]\n"
      "ds_read2_b32 v[126:127], v203 offset0:24 offset1:18\n"
      "v_sub_f32 v124, v124, v215\n"
      "v_mfma_f32_16x16x32_bf16 v[168:171], v[32:35], v[132:135], 0\n"
      "ds_read_u16 v86, v11 offset:64\n"
      "v_cvt_pk_bf16_f32 v198, v198, v199\n"
      "v_mfma_f32_16x16x32_bf16 v[168:171], v[32:35], v[88:91], v[168:171]\n"
      "ds_read2_b32 v[150:151], v203 offset0:2 offset1:10\n"
      "v_exp_f32 v121, v121\n"
      "v_exp_f32 v201, v201\n"
      "v_pk_add_f32 v[162:163], v[162:163], v[214:215] op_sel:[0,1] neg_lo:[0,1] neg_hi:[0,1]\n"
      "v_mfma_f32_16x16x32_bf16 v[152:155], v[194:197], v[132:135], 0\n"
      "ds_read2_b32 v[156:157], v203 offset0:8 offset1:9\n"
      "v_cvt_pk_bf16_f32 v199, v200, v201\n"
      "v_mfma_f32_16x16x32_bf16 v[152:155], v[76:79], v[88:91], v[152:155]\n"
      "v_exp_f32 v120, v120\n"
      "v_mfma_f32_16x16x32_bf16 v[212:215], v[104:107], v[132:135], 0\n"
      "s_waitcnt lgkmcnt(3)\n"
      "v_cvt_pk_bf16_f32 v201, v120, v121\n"
      "v_alignbyte_b32 v126, v126, v126, v93\n"
      "v_mfma_f32_16x16x32_bf16 v[212:215], v[80:83], v[88:91], v[212:215]\n"
      "v_exp_f32_e64 v119, v119\n"
      "v_mfma_f32_16x16x32_bf16 v[100:103], v[96:99], v[132:135], 0\n"
      "s_waitcnt lgkmcnt(1)\n"
      "v_exp_f32_e64 v118, v118\n"
      "v_mfma_f32_16x16x32_bf16 v[100:103], v[84:87], v[88:91], v[100:103]\n"
      "v_cvt_pk_bf16_f32 v200, v118, v119\n"
      "v_alignbyte_b32 v116, v117, v116, v93\n"
      "v_alignbyte_b32 v114, v115, v114, v93\n"
      "v_mfma_f32_16x16x32_bf16 v[190:193], v[182:185], v[216:219], v[64:67]\n"
      "s_waitcnt lgkmcnt(0)\n"
      "v_alignbyte_b32 v156, v157, v156, v93\n"
      "v_alignbyte_b32 v117, v150, v117, v93\n"
      "v_mfma_f32_16x16x32_bf16 v[202:205], v[136:139], v[216:219], v[68:71]\n"
      "v_alignbyte_b32 v115, v127, v115, v93\n"
      "v_alignbyte_b32 v157, v151, v157, v93\n"
      "v_mfma_f32_16x16x32_bf16 v[148:151], v[186:189], v[216:219], v[72:75]\n"
      "v_rcp_f32 v168, v168\n"
      "v_mfma_f32_16x16x32_bf16 v[130:133], v[144:147], v[216:219], v[28:31]\n"
      "v_mul_f32 v154, v168, v154\n"
      "v_mul_f32 v214, v168, v214\n"
      "v_mfma_f32_16x16x16_bf16 v[190:193], v[112:113], v[158:159], v[190:193]\n"
      "v_mul_f32 v152, v168, v152\n"
      "v_exp_f32 v162, v162\n"
      "v_mfma_f32_16x16x16_bf16 v[202:205], v[128:129], v[158:159], v[202:205]\n"
      "v_exp_f32 v124, v124\n"
      "v_mul_f32 v153, v168, v153\n"
      "v_mfma_f32_16x16x16_bf16 v[148:151], v[210:211], v[158:159], v[148:151]\n"
      "v_exp_f32 v165, v165\n"
      "v_mul_f32_e64 v155, v168, v155\n"
      "v_mfma_f32_16x16x16_bf16 v[130:133], v[122:123], v[158:159], v[130:133]\n"
      "v_cvt_pk_bf16_f32 v152, v152, v153\n"
      "v_exp_f32 v164, v164\n"
      "v_mfma_f32_16x16x16_bf16 v[190:193], v[180:181], v[116:117], v[190:193]\n"
      "v_cvt_pk_bf16_f32 v153, v154, v155\n"
      "v_exp_f32 v163, v163\n"
      "v_mfma_f32_16x16x16_bf16 v[202:205], v[180:181], v[156:157], v[202:205]\n"
      "v_mul_f32_e64 v159, v207, v207\n"
      "v_cvt_pk_bf16_f32 v93, v164, v165\n"
      "v_fma_f32 v158, v206, v206, s52\n"
      "v_mfma_f32_16x16x16_bf16 v[148:151], v[180:181], v[114:115], v[148:151]\n"
      "v_cvt_pk_bf16_f32 v92, v162, v163\n"
      "v_mul_f32 v213, v168, v213\n"
      "v_fmac_f32 v159, v209, v209\n"
      "v_mfma_f32_16x16x16_bf16 v[130:133], v[180:181], v[126:127], v[130:133]\n"
      "v_mul_f32 v212, v168, v212\n"
      "v_fmac_f32 v158, v208, v208\n"
      "v_mul_f32_e64 v215, v168, v215\n"
      "v_mfma_f32_16x16x16_bf16 v[190:193], v[166:167], v[116:117], v[190:193]\n"
      "ds_read_b128 v[162:165], v2 offset:25088\n"
      "v_cvt_pk_bf16_f32 v154, v212, v213\n"
      "v_mfma_f32_16x16x16_bf16 v[202:205], v[166:167], v[156:157], v[202:205]\n"
      "ds_read_b128 v[216:219], v2 offset:22016\n"
      "v_fmac_f32_e64 v159, v177, v177\n"
      "v_mfma_f32_16x16x16_bf16 v[148:151], v[166:167], v[114:115], v[148:151]\n"
      "ds_read_b128 v[114:117], v2 offset:18944\n"
      "v_cvt_pk_bf16_f32 v155, v214, v215\n"
      "v_mfma_f32_16x16x16_bf16 v[130:133], v[166:167], v[126:127], v[130:133]\n"
      "v_fmac_f32_e64 v158, v176, v176\n"
      "v_cvt_pk_bf16_f32 v94, v124, 0\n"
      "v_max3_f32 v167, v190, v191, v192\n"
      "v_max3_f32 v167, v167, v204, v205\n"
      "v_max3_f32 v134, v193, v202, v203\n"
      "v_max3_f32 v167, v167, v150, v151\n"
      "v_pk_mul_f32 v[102:103], v[102:103], v[168:169] op_sel_hi:[1,0]\n"
      "v_max3_f32 v134, v134, v148, v149\n"
      "v_max3_f32 v167, v167, v134, v130\n"
      "v_mov_b32 v134, v167\n"
      "v_pk_fma_f32 v[158:159], v[178:179], v[178:179], v[158:159]\n"
      "v_pk_mul_f32 v[100:101], v[100:101], v[168:169] op_sel_hi:[1,0]\n"
      "v_permlane16_swap_b32 v167, v134\n"
      "v_max_f32 v167, v167, v134\n"
      "v_mov_b32 v134, v167\n"
      "v_pk_fma_f32 v[158:159], v[108:109], v[108:109], v[158:159]\n"
      "v_pk_fma_f32 v[158:159], v[110:111], v[110:111], v[158:159]\n"
      "v_permlane32_swap_b32_e64 v167, v134\n"
      "v_max_f32 v167, v167, v134\n"
      "v_pk_add_f32 v[192:193], v[192:193], v[166:167] op_sel:[0,1] neg_lo:[0,1] neg_hi:[0,1]\n"
      "s_waitcnt lgkmcnt(0)\n"
      "v_mfma_f32_16x16x32_bf16 v[132:135], a[216:219], v[114:117], 0\n"
      "ds_write_b128 v3, v[152:155]\n"
      "v_pk_add_f32 v[190:191], v[190:191], v[166:167] op_sel:[0,1] neg_lo:[0,1] neg_hi:[0,1]\n"
      "v_pk_add_f32 v[204:205], v[204:205], v[166:167] op_sel:[0,1] neg_lo:[0,1] neg_hi:[0,1]\n"
      "v_pk_add_f32 v[150:151], v[150:151], v[166:167] op_sel:[0,1] neg_lo:[0,1] neg_hi:[0,1]\n"
      "v_mfma_f32_16x16x32_bf16 v[132:135], a[220:223], v[216:219], v[132:135]\n"
      "v_sub_f32 v130, v130, v167\n"
      "v_cvt_pk_bf16_f32 v100, v100, v101\n"
      "v_mfma_f32_16x16x32_bf16 v[132:135], a[224:227], v[162:165], v[132:135]\n"
      "v_cvt_pk_bf16_f32 v101, v102, v103\n"
      "v_sub_f32 v202, v202, v167\n"
      "v_mfma_f32_16x16x32_bf16 v[118:121], a[240:243], v[114:117], 0\n"
      "ds_read_b128 v[152:155], v10\n"
      "v_sub_f32 v149, v149, v167\n"
      "v_mfma_f32_16x16x32_bf16 v[118:121], a[244:247], v[216:219], v[118:121]\n"
      "v_exp_f32 v151, v151\n"
      "v_mfma_f32_16x16x32_bf16 v[118:121], a[248:251], v[162:165], v[118:121]\n"
      "v_add_f32 v158, v158, v159\n"
      "v_sub_f32 v148, v148, v167\n"
      "v_mfma_f32_16x16x32_bf16 v[168:171], v[232:235], v[114:117], 0\n"
      "v_exp_f32_e64 v205, v205\n"
      "v_mfma_f32_16x16x32_bf16 v[168:171], v[236:239], v[216:219], v[168:171]\n"
      "v_exp_f32 v190, v190\n"
      "v_mfma_f32_16x16x32_bf16 v[168:171], v[240:243], v[162:165], v[168:171]\n"
      "v_exp_f32 v191, v191\n"
      "v_mfma_f32_16x16x32_bf16 v[162:165], v[32:35], v[198:201], 0\n"
      "v_exp_f32 v204, v204\n"
      "v_mfma_f32_16x16x32_bf16 v[162:165], v[32:35], v[92:95], v[162:165]\n"
      "v_exp_f32 v192, v192\n"
      "v_mfma_f32_16x16x32_bf16 v[212:215], v[194:197], v[198:201], 0\n"
      "ds_write_b64 v4, v[100:101]\n"
      "v_mfma_f32_16x16x32_bf16 v[212:215], v[76:79], v[92:95], v[212:215]\n"
      "ds_read_b64 v[156:157], v10 offset:16\n"
      "v_sub_f32 v203, v203, v167\n"
      "v_mfma_f32_16x16x32_bf16 v[216:219], v[104:107], v[198:201], 0\n"
      "s_nop 0\n"
      "v_rcp_f32 v162, v162\n"
      "v_mfma_f32_16x16x32_bf16 v[216:219], v[80:83], v[92:95], v[216:219]\n"
      "v_exp_f32 v150, v150\n"
      "v_mfma_f32_16x16x32_bf16 v[164:167], v[96:99], v[198:201], 0\n"
      "v_exp_f32_e64 v193, v193\n"
      "v_mfma_f32_16x16x32_bf16 v[164:167], v[84:87], v[92:95], v[164:167]\n"
      "s_waitcnt lgkmcnt(2)\n"
      "v_exp_f32 v149, v149\n"
      "v_mfma_f32_16x16x32_bf16 v[198:201], v[152:155], v[182:185], v[30:33]\n"
      "v_exp_f32_e64 v202, v202\n"
      "v_mfma_f32_16x16x32_bf16 v[180:183], v[152:155], v[136:139], v[30:33]\n"
      "ds_read_b128 v[124:127], v2 offset:28160\n"
      "v_cvt_pk_bf16_f32 v89, v150, v151\n"
      "v_mfma_f32_16x16x32_bf16 v[136:139], v[152:155], v[186:189], v[30:33]\n"
      "s_waitcnt lgkmcnt(1)\n"
      "v_exp_f32 v148, v148\n"
      "v_mfma_f32_16x16x32_bf16 v[100:103], v[152:155], v[144:147], v[30:33]\n"
      "ds_read_b128 v[144:147], v2 offset:31232\n"
      "v_cvt_pk_bf16_f32 v190, v190, v191\n"
      "v_mfma_f32_16x16x16_bf16 v[198:201], v[156:157], v[112:113], v[198:201]\n"
      "ds_read_b128 v[152:155], v2 offset:34304\n"
      "v_mul_f32_e64 v164, v162, v164\n"
      "v_mfma_f32_16x16x16_bf16 v[180:183], v[156:157], v[128:129], v[180:183]\n"
      "v_cvt_pk_bf16_f32 v191, v192, v193\n"
      "v_permlane16_swap_b32 v160, v158\n"
      "v_mfma_f32_16x16x16_bf16 v[136:139], v[156:157], v[210:211], v[136:139]\n"
      "v_mul_f32 v216, v162, v216\n"
      "v_cvt_pk_bf16_f32 v193, v204, v205\n"
      "v_add_f32_e64 v160, v160, v158\n"
      "v_mul_f32_e64 v165, v162, v165\n"
      "v_exp_f32_e64 v203, v203\n"
      "v_pk_mul_f32 v[212:213], v[212:213], v[162:163] op_sel_hi:[1,0]\n"
      "v_pk_mul_f32 v[218:219], v[218:219], v[162:163] op_sel_hi:[1,0]\n"
      "v_pk_mul_f32 v[214:215], v[214:215], v[162:163] op_sel_hi:[1,0]\n"
      "v_mfma_f32_16x16x16_bf16 v[100:103], v[156:157], v[122:123], v[100:103]\n"
      "v_exp_f32 v130, v130\n"
      "s_waitcnt lgkmcnt(1)\n"
      "v_mfma_f32_16x16x32_bf16 v[132:135], a[228:231], v[124:127], v[132:135]\n"
      "v_cndmask_b32 v198, v198, v180, s[58:59]\n"
      "v_cvt_pk_bf16_f32 v90, v130, 0\n"
      "v_mfma_f32_16x16x32_bf16 v[132:135], a[232:235], v[144:147], v[132:135]\n"
      "s_waitcnt lgkmcnt(0)\n"
      "v_cvt_pk_bf16_f32 v212, v212, v213\n"
      "v_cndmask_b32 v198, v198, v136, s[60:61]\n"
      "v_mfma_f32_16x16x32_bf16 v[132:135], a[236:239], v[152:155], v[132:135]\n"
      "v_cndmask_b32 v198, v198, v100, s[62:63]\n"
      "v_fmac_f32_e64 v198, s53, v39\n"
      "v_mfma_f32_16x16x32_bf16 v[118:121], a[252:255], v[124:127], v[118:121]\n"
      "s_nop 0\n"
      "v_max_f32_dpp v180, v198, v198 row_ror:8 row_mask:0xf bank_mask:0xf\n"
      "v_cvt_pk_bf16_f32 v88, v148, v149\n"
      "v_mfma_f32_16x16x32_bf16 v[118:121], v[224:227], v[144:147], v[118:121]\n"
      "v_cvt_pk_bf16_f32 v213, v214, v215\n"
      "v_fma_f32 v200, v132, v132, s52\n"
      "v_mfma_f32_16x16x32_bf16 v[118:121], v[228:231], v[152:155], v[118:121]\n"
      "v_mul_f32 v217, v162, v217\n"
      "v_max_f32_dpp v180, v180, v180 row_ror:4 row_mask:0xf bank_mask:0xf\n"
      "v_mfma_f32_16x16x32_bf16 v[168:171], v[244:247], v[124:127], v[168:171]\n"
      "s_nop 0\n"
      "v_max_f32_dpp v180, v180, v180 row_ror:2 row_mask:0xf bank_mask:0xf\n"
      "v_cvt_pk_bf16_f32 v215, v218, v219\n"
      "v_mfma_f32_16x16x32_bf16 v[168:171], v[248:251], v[144:147], v[168:171]\n"
      "v_cvt_pk_bf16_f32 v192, v202, v203\n"
      "v_max_f32_dpp v180, v180, v180 row_ror:1 row_mask:0xf bank_mask:0xf\n"
      "v_mfma_f32_16x16x32_bf16 v[168:171], v[252:255], v[152:155], v[168:171]\n"
      "s_nop 0\n"
      "v_max_f32_dpp v180, v180, v180 row_bcast:15 row_mask:0xa bank_mask:0xf\n"
      "v_cvt_pk_bf16_f32 v214, v216, v217\n"
      "v_mfma_f32_16x16x32_bf16 v[152:155], v[32:35], v[190:193], 0\n"
      "v_mul_f32 v201, v133, v133\n"
      "v_max_f32_dpp v180, v180, v180 row_bcast:31 row_mask:0xc bank_mask:0xf\n"
      "v_mfma_f32_16x16x32_bf16 v[152:155], v[32:35], v[88:91], v[152:155]\n"
      "v_cvt_pk_bf16_f32 v164, v164, v165\n"
      "v_readlane_b32 s72, v180, 63\n"
      "v_mfma_f32_16x16x32_bf16 v[180:183], v[194:197], v[190:193], 0\n"
      "ds_write_b128 v3, v[212:215] offset:256\n"
      "v_pk_mul_f32 v[166:167], v[166:167], v[162:163] op_sel_hi:[1,0]\n"
      "v_pk_fma_f32 v[200:201], v[134:135], v[134:135], v[200:201]\n"
      "v_pk_fma_f32 v[200:201], v[118:119], v[118:119], v[200:201]\n"
      "v_mfma_f32_16x16x32_bf16 v[180:183], v[76:79], v[88:91], v[180:183]\n"
      "v_subrev_f32 v198, s72, v198\n"
      "v_cvt_pk_bf16_f32 v165, v166, v167\n"
      "v_mfma_f32_16x16x32_bf16 v[210:213], v[104:107], v[190:193], 0\n"
      "s_and_b32 s5, s84, s8\n"
      "v_rcp_f32_e64 v152, v152\n"
      "v_mfma_f32_16x16x32_bf16 v[210:213], v[80:83], v[88:91], v[210:213]\n"
      "ds_write_b64 v4, v[164:165] offset:256\n"
      "v_exp_f32_e64 v198, v198\n"
      "v_pk_fma_f32 v[200:201], v[120:121], v[120:121], v[200:201]\n"
      "v_pk_mul_f32 v[182:183], v[182:183], v[152:153] op_sel_hi:[1,0]\n"
      "v_cvt_pk_bf16_f32 v198, v198, v198\n"
      "v_pk_mul_f32 v[180:181], v[180:181], v[152:153] op_sel_hi:[1,0]\n"
      "v_pk_fma_f32 v[200:201], v[168:169], v[168:169], v[200:201]\n"
      "v_pk_fma_f32 v[200:201], v[170:171], v[170:171], v[200:201]\n"
      "v_cvt_pk_bf16_f32 v180, v180, v181\n"
      "v_pk_mul_f32 v[210:211], v[210:211], v[152:153] op_sel_hi:[1,0]\n"
      "v_cvt_pk_bf16_f32 v181, v182, v183\n"
      "v_pk_mul_f32 v[212:213], v[212:213], v[152:153] op_sel_hi:[1,0]\n"
      "v_mfma_f32_16x16x32_bf16 v[162:165], v[96:99], v[190:193], 0\n"
      "ds_write_b16 v13, v198\n"
      "v_cvt_pk_bf16_f32 v182, v210, v211\n"
      "v_mfma_f32_16x16x32_bf16 v[162:165], v[84:87], v[88:91], v[162:165]\n"
      "ds_read_b128 v[144:147], v14\n"
      "v_add_f32 v200, v200, v201\n"
      "v_cvt_pk_bf16_f32 v183, v212, v213\n"
      "v_mov_b32 v158, v200\n"
      "s_add_u32 s42, s42, 1\n"
      "ds_write_b128 v3, v[180:183] offset:512\n"
      "v_permlane16_swap_b32 v200, v158\n"
      "v_add_f32_e64 v200, v200, v158\n"
      "v_pk_mul_f32 v[162:163], v[162:163], v[152:153] op_sel_hi:[1,0]\n"
      "v_pk_mul_f32 v[164:165], v[164:165], v[152:153] op_sel_hi:[1,0]\n"
      "v_permlane32_swap_b32 v160, v200\n"
      "v_add_f32 v160, v160, v200\n"
      "s_add_u32 s43, s43, 1\n"
      "ds_write_b32 v31, v160\n"
      "v_cvt_pk_bf16_f32 v162, v162, v163\n"
      "v_cvt_pk_bf16_f32 v163, v164, v165\n"
      "ds_read_b128 v[198:201], v14 offset:64\n"
      "ds_write_b64 v4, v[162:163] offset:512\n"
      "s_waitcnt lgkmcnt(4)\n"
      "v_mfma_f32_16x16x32_bf16 v[162:165], v[144:147], v[32:35], 0\n"
      "s_waitcnt lgkmcnt(1)\n"
      "v_mfma_f32_16x16x32_bf16 v[162:165], v[198:201], v[32:35], v[162:165]\n"
      "s_cmp_ge_u32 s95, 0x40000000\n"
      "s_cselect_b64 vcc, -1, s[56:57]\n"
      "s_waitcnt lgkmcnt(0)\n"
      "s_barrier\n"
      "s_cbranch_vccz L_sel1_%=\n"
      "s_branch L_disp1_%=\n"
      "L_tail1_%=:\n"
      "s_nop 4\n"
      "ds_read_b128 v[128:131], v9\n"
      "v_mfma_f32_16x16x32_bf16 v[152:155], v[144:147], v[194:197], 0\n"
      "ds_read_b128 v[194:197], v2\n"
      "ds_read_b128 v[136:139], v2 offset:6144\n"
      "v_mfma_f32_16x16x32_bf16 v[152:155], v[198:201], v[76:79], v[152:155]\n"
      "ds_read_b128 v[124:127], v2 offset:6400\n"
      "ds_read_b128 v[216:219], v2 offset:3072\n"
      "v_mfma_f32_16x16x32_bf16 v[210:213], v[144:147], v[104:107], 0\n"
      "ds_read_b128 v[104:107], v7\n"
      "ds_read_b128 v[202:205], v2 offset:12288\n"
      "ds_read_b128 v[184:187], v2 offset:3328\n"
      "v_mfma_f32_16x16x32_bf16 v[210:213], v[198:201], v[80:83], v[210:213]\n"
      "ds_read_b128 v[148:151], v2 offset:9216\n"
      "v_mfma_f32_16x16x32_bf16 v[180:183], v[144:147], v[96:99], 0\n"
      "s_waitcnt lgkmcnt(8)\n"
      "v_add_f32 v130, v130, v131\n"
      "v_add_f32 v128, v128, v129\n"
      "v_add_f32 v128, v128, v130\n"
      "v_rsq_f32 v128, v128\n"
      "v_mfma_f32_16x16x32_bf16 v[180:183], v[198:201], v[84:87], v[180:183]\n"
      "s_waitcnt lgkmcnt(7)\n"
      "v_mul_f32 v128, 0x415db3d7, v128\n"
      "v_mov_b32 v130, v128\n"
      "s_nop 1\n"
      "v_permlane32_swap_b32 v128, v130\n"
      "v_pk_mul_f32 v[168:169], v[168:169], v[130:131] op_sel_hi:[1,0]\n"
      "v_mov_b32 v129, v128\n"
      "v_pk_mul_f32 v[134:135], v[134:135], v[130:131] op_sel_hi:[1,0]\n"
      "v_pk_mul_f32 v[170:171], v[170:171], v[130:131] op_sel_hi:[1,0]\n"
      "v_permlane16_swap_b32_e64 v128, v129\n"
      "v_pk_mul_f32 v[206:207], v[206:207], v[128:129] op_sel:[0,1]\n"
      "v_pk_mul_f32 v[142:143], v[142:143], v[128:129] op_sel_hi:[1,0]\n"
      "v_pk_mul_f32 v[120:121], v[120:121], v[130:131] op_sel_hi:[1,0]\n"
      "v_pk_mul_f32 v[110:111], v[110:111], v[128:129] op_sel:[0,1]\n"
      "v_pk_mul_f32 v[172:173], v[172:173], v[128:129] op_sel_hi:[1,0]\n"
      "v_pk_mul_f32 v[140:141], v[140:141], v[128:129] op_sel_hi:[1,0]\n"
      "v_pk_mul_f32 v[132:133], v[132:133], v[130:131] op_sel_hi:[1,0]\n"
      "v_pk_mul_f32 v[118:119], v[118:119], v[130:131] op_sel_hi:[1,0]\n"
      "v_pk_mul_f32 v[174:175], v[174:175], v[128:129] op_sel_hi:[1,0]\n"
      "v_cvt_pk_bf16_f32 v140, v140, v141\n"
      "v_pk_mul_f32 v[176:177], v[176:177], v[128:129] op_sel:[0,1]\n"
      "v_cvt_pk_bf16_f32 v206, v206, v207\n"
      "v_cvt_pk_bf16_f32 v132, v132, v133\n"
      "v_pk_mul_f32 v[178:179], v[178:179], v[128:129] op_sel:[0,1]\n"
      "v_cvt_pk_bf16_f32 v172, v172, v173\n"
      "v_pk_mul_f32 v[208:209], v[208:209], v[128:129] op_sel:[0,1]\n"
      "v_pk_mul_f32 v[108:109], v[108:109], v[128:129] op_sel:[0,1]\n"
      "v_cvt_pk_bf16_f32 v108, v108, v109\n"
      "v_cvt_pk_bf16_f32 v207, v208, v209\n"
      "v_pk_mul_f32 v[220:221], v[220:221], v[128:129] op_sel_hi:[1,0]\n"
      "v_pk_mul_f32 v[222:223], v[222:223], v[128:129] op_sel_hi:[1,0]\n"
      "v_cvt_pk_bf16_f32 v141, v142, v143\n"
      "v_cvt_pk_bf16_f32 v133, v134, v135\n"
      "v_cvt_pk_bf16_f32 v209, v178, v179\n"
      "v_cvt_pk_bf16_f32 v109, v110, v111\n"
      "v_cvt_pk_bf16_f32 v168, v168, v169\n"
      "v_cvt_pk_bf16_f32 v173, v174, v175\n"
      "v_cvt_pk_bf16_f32 v175, v222, v223\n"
      "v_cvt_pk_bf16_f32 v134, v118, v119\n"
      "v_cvt_pk_bf16_f32 v169, v170, v171\n"
      "v_cvt_pk_bf16_f32 v208, v176, v177\n"
      "v_cvt_pk_bf16_f32 v135, v120, v121\n"
      "v_cvt_pk_bf16_f32 v174, v220, v221\n"
      "s_mul_i32 s92, s40, 3072\n"
      "ds_write_b128 v5, v[132:135] offset:30720\n"
      "ds_write_b128 v5, v[206:209] offset:24576\n"
      "ds_write_b64 v6, v[168:169] offset:30720\n"
      "ds_write_b64 v6, v[140:141] offset:18432\n"
      "ds_write_b128 v5, v[172:175] offset:18432\n"
      "ds_write_b64 v6, v[108:109] offset:24576\n"
      "v_mfma_f32_16x16x32_bf16 v[172:175], a[216:219], v[194:197], 0\n"
      "ds_read_b128 v[206:209], v2 offset:15360\n"
      "s_waitcnt lgkmcnt(6)\n"
      "v_mfma_f32_16x16x32_bf16 v[172:175], a[220:223], v[216:219], v[172:175]\n"
      "ds_read_b128 v[144:147], v2 offset:15872\n"
      "v_mov_b32_dpp v162, v162 quad_perm:[0,0,0,0] row_mask:0xf bank_mask:0xf\n"
      "v_mfma_f32_16x16x32_bf16 v[220:223], a[240:243], v[194:197], 0\n"
      "ds_read_b128 v[132:135], v7 offset:8192\n"
      "v_subrev_u32 v163, 115456, v9\n"
      "v_mfma_f32_16x16x32_bf16 v[220:223], a[244:247], v[216:219], v[220:223]\n"
      "ds_read_b128 v[96:99], v2 offset:12800\n"
      "v_subrev_u32 v85, 115456, v9\n"
      "v_mfma_f32_16x16x32_bf16 v[140:143], v[232:235], v[194:197], 0\n"
      "v_add_u32 v86, s92, v163\n"
      "v_mfma_f32_16x16x32_bf16 v[140:143], v[236:239], v[216:219], v[140:143]\n"
      "buffer_store_dwordx4 v[104:107], v20, s[24:27], s85 offen sc1\n"
      "v_mfma_f32_16x16x32_bf16 v[172:175], a[224:227], v[136:139], v[172:175]\n"
      "ds_read_b128 v[118:121], v2 offset:256\n"
      "v_lshrrev_b32_e64 v163, 4, v86\n"
      "v_mfma_f32_16x16x32_bf16 v[172:175], a[228:231], v[148:151], v[172:175]\n"
      "v_and_b32 v84, 0x3fffffff, v37\n"
      "v_mfma_f32_16x16x32_bf16 v[220:223], a[248:251], v[136:139], v[220:223]\n"
      "ds_read_b128 v[168:171], v2 offset:15616\n"
      "v_mfma_f32_16x16x32_bf16 v[220:223], a[252:255], v[148:151], v[220:223]\n"
      "ds_read_b128 v[194:197], v7 offset:4096\n"
      "v_mfma_f32_16x16x32_bf16 v[140:143], v[240:243], v[136:139], v[140:143]\n"
      "ds_read_b128 v[104:107], v2 offset:12544\n"
      "v_mfma_f32_16x16x32_bf16 v[140:143], v[244:247], v[148:151], v[140:143]\n"
      "ds_read_b128 v[198:201], v2 offset:6656\n"
      "v_mfma_f32_16x16x32_bf16 v[172:175], a[232:235], v[202:205], v[172:175]\n"
      "s_waitcnt lgkmcnt(6)\n"
      "v_add_u32 v218, 128, v163\n"
      "v_add_u32 v80, 64, v163\n"
      "v_mfma_f32_16x16x32_bf16 v[172:175], a[236:239], v[206:209], v[172:175]\n"
      "ds_read_b128 v[212:215], v2 offset:9472\n"
      "v_mfma_f32_16x16x32_bf16 v[220:223], v[224:227], v[202:205], v[220:223]\n"
      "v_ashrrev_i32 v81, 31, v37\n"
      "v_and_b32 v148, 0x300, v85\n"
      "v_mfma_f32_16x16x32_bf16 v[220:223], v[228:231], v[206:209], v[220:223]\n"
      "ds_read_b128 v[164:167], v2 offset:512\n"
      "v_add_u32 v85, v85, v148\n"
      "v_mfma_f32_16x16x32_bf16 v[140:143], v[248:251], v[202:205], v[140:143]\n"
      "v_cndmask_b32 v152, v152, v210, s[58:59]\n"
      "v_mfma_f32_16x16x32_bf16 v[140:143], v[252:255], v[206:209], v[140:143]\n"
      "s_waitcnt lgkmcnt(3)\n"
      "v_add_u32 v85, 150784, v85\n"
      "v_fma_f32 v148, v172, v172, s52\n"
      "v_mfma_f32_16x16x32_bf16 v[206:209], a[216:219], v[118:121], 0\n"
      "v_mul_f32 v149, v173, v173\n"
      "v_mul_u32_u24 v76, 2731, v218\n"
      "v_mfma_f32_16x16x32_bf16 v[206:209], a[220:223], v[184:187], v[206:209]\n"
      "v_subrev_u32 v82, 73728, v6\n"
      "v_fmac_f32_e64 v149, v175, v175\n"
      "v_mfma_f32_16x16x32_bf16 v[176:179], a[240:243], v[118:121], 0\n"
      "ds_read_b128 v[154:157], v2 offset:9728\n"
      "v_fmac_f32 v148, v174, v174\n"
      "v_mfma_f32_16x16x32_bf16 v[176:179], a[244:247], v[184:187], v[176:179]\n"
      "v_fmac_f32 v149, v221, v221\n"
      "v_fmac_f32 v148, v220, v220\n"
      "v_mfma_f32_16x16x32_bf16 v[108:111], v[232:235], v[118:121], 0\n"
      "ds_read_b128 v[202:205], v2 offset:3584\n"
      "v_fmac_f32 v149, v223, v223\n"
      "v_mfma_f32_16x16x32_bf16 v[108:111], v[236:239], v[184:187], v[108:111]\n"
      "s_waitcnt lgkmcnt(2)\n"
      "v_add_u32 v84, 1179648, v84\n"
      "v_lshrrev_b32 v76, 16, v76\n"
      "v_mfma_f32_16x16x32_bf16 v[206:209], a[224:227], v[124:127], v[206:209]\n"
      "ds_read_b128 v[182:185], v85 offset:4096\n"
      "v_mfma_f32_16x16x32_bf16 v[206:209], a[228:231], v[212:215], v[206:209]\n"
      "buffer_store_dwordx4 v[194:197], v21, s[24:27], s85 offen sc1\n"
      "v_mfma_f32_16x16x32_bf16 v[176:179], a[248:251], v[124:127], v[176:179]\n"
      "s_nop 0\n"
      "ds_read_b128 v[194:197], v7 offset:16384\n"
      "v_and_b32 v81, 100270464, v81\n"
      "v_mfma_f32_16x16x32_bf16 v[176:179], a[252:255], v[212:215], v[176:179]\n"
      "v_fmac_f32 v148, v222, v222\n"
      "v_sub_u32_e64 v84, v84, v81\n"
      "v_mfma_f32_16x16x32_bf16 v[108:111], v[240:243], v[124:127], v[108:111]\n"
      "v_rcp_f32 v162, v162\n"
      "v_mfma_f32_16x16x32_bf16 v[108:111], v[244:247], v[212:215], v[108:111]\n"
      "v_mul_u32_u24 v77, 2731, v80\n"
      "v_fmac_f32 v148, v140, v140\n"
      "v_mfma_f32_16x16x32_bf16 v[206:209], a[232:235], v[104:107], v[206:209]\n"
      "v_cndmask_b32 v152, v152, v180, s[60:61]\n"
      "v_fmac_f32 v149, v141, v141\n"
      "v_mfma_f32_16x16x32_bf16 v[206:209], a[236:239], v[168:171], v[206:209]\n"
      "v_add_u32 v78, 0, v163\n"
      "v_mul_u32_u24 v190, 24, v76\n"
      "v_mfma_f32_16x16x32_bf16 v[176:179], v[224:227], v[104:107], v[176:179]\n"
      "v_mul_u32_u24 v100, 2731, v78\n"
      "v_mul_f32 v152, v162, v152\n"
      "v_mfma_f32_16x16x32_bf16 v[176:179], v[228:231], v[168:171], v[176:179]\n"
      "buffer_store_dwordx4 v[132:135], v22, s[24:27], s85 offen sc1\n"
      "v_mfma_f32_16x16x32_bf16 v[108:111], v[248:251], v[104:107], v[108:111]\n"
      "ds_read_b128 v[104:107], v85 offset:6144\n"
      "v_mul_f32_e64 v187, v207, v207\n"
      "v_mfma_f32_16x16x32_bf16 v[108:111], v[252:255], v[168:171], v[108:111]\n"
      "v_fma_f32 v186, v206, v206, s52\n"
      "v_fmac_f32 v187, v209, v209\n"
      "v_mfma_f32_16x16x32_bf16 v[132:135], a[216:219], v[164:167], 0\n"
      "s_waitcnt lgkmcnt(1)\n"
      "v_cvt_pk_bf16_f32 v152, v152, v152\n"
      "v_fmac_f32 v148, v142, v142\n"
      "v_mfma_f32_16x16x32_bf16 v[132:135], a[220:223], v[202:205], v[132:135]\n"
      "v_fmac_f32 v186, v208, v208\n"
      "v_mfma_f32_16x16x32_bf16 v[118:121], a[240:243], v[164:167], 0\n"
      "ds_read_b128 v[210:213], v85 offset:10240\n"
      "v_fmac_f32 v186, v176, v176\n"
      "v_mfma_f32_16x16x32_bf16 v[118:121], a[244:247], v[202:205], v[118:121]\n"
      "ds_read_b128 v[160:163], v85 offset:8192\n"
      "v_lshrrev_b32 v100, 16, v100\n"
      "v_mfma_f32_16x16x32_bf16 v[168:171], v[232:235], v[164:167], 0\n"
      "ds_write_b16 v12, v152\n"
      "v_mfma_f32_16x16x32_bf16 v[168:171], v[236:239], v[202:205], v[168:171]\n"
      "ds_read_b128 v[164:167], v7 offset:12288\n"
      "v_fmac_f32 v187, v177, v177\n"
      "v_mfma_f32_16x16x32_bf16 v[132:135], a[224:227], v[198:201], v[132:135]\n"
      "v_fmac_f32 v187, v179, v179\n"
      "v_fmac_f32 v186, v178, v178\n"
      "v_mfma_f32_16x16x32_bf16 v[132:135], a[228:231], v[154:157], v[132:135]\n"
      "v_fmac_f32 v149, v143, v143\n"
      "v_mfma_f32_16x16x32_bf16 v[118:121], a[248:251], v[198:201], v[118:121]\n"
      "ds_read_b128 v[150:153], v85\n"
      "v_mfma_f32_16x16x32_bf16 v[118:121], a[252:255], v[154:157], v[118:121]\n"
      "ds_read_b128 v[124:127], v85 offset:2048\n"
      "v_add_f32 v148, v148, v149\n"
      "v_mfma_f32_16x16x32_bf16 v[168:171], v[240:243], v[198:201], v[168:171]\n"
      "v_fmac_f32 v186, v108, v108\n"
      "v_fmac_f32 v187, v109, v109\n"
      "v_mfma_f32_16x16x32_bf16 v[168:171], v[244:247], v[154:157], v[168:171]\n"
      "v_fmac_f32 v187, v111, v111\n"
      "v_fmac_f32 v186, v110, v110\n"
      "v_mfma_f32_16x16x32_bf16 v[132:135], a[232:235], v[96:99], v[132:135]\n"
      "s_waitcnt lgkmcnt(2)\n"
      "v_add_f32 v186, v186, v187\n"
      "v_mul_u32_u24 v156, 24, v100\n"
      "v_mfma_f32_16x16x32_bf16 v[132:135], a[236:239], v[144:147], v[132:135]\n"
      "buffer_store_dwordx4 v[164:167], v23, s[24:27], s85 offen sc1\n"
      "v_mfma_f32_16x16x32_bf16 v[118:121], v[224:227], v[96:99], v[118:121]\n"
      "v_permlane16_swap_b32_e64 v148, v186\n"
      "v_mfma_f32_16x16x32_bf16 v[118:121], v[228:231], v[144:147], v[118:121]\n"
      "v_bfe_i32 v81, v37, 30, 1\n"
      "v_add_f32 v148, v148, v186\n"
      "v_mfma_f32_16x16x32_bf16 v[168:171], v[248:251], v[96:99], v[168:171]\n"
      "s_nop 0\n"
      "v_mul_f32_e64 v155, v133, v133\n"
      "v_fma_f32 v154, v132, v132, s52\n"
      "v_mfma_f32_16x16x32_bf16 v[168:171], v[252:255], v[144:147], v[168:171]\n"
      "s_waitcnt lgkmcnt(0)\n"
      "v_fmac_f32 v155, v135, v135\n"
      "v_fmac_f32_e64 v154, v134, v134\n"
      "v_mfma_f32_16x16x32_bf16 v[144:147], a[216:219], v[150:153], 0\n"
      "v_fmac_f32 v155, v119, v119\n"
      "v_fmac_f32 v154, v118, v118\n"
      "v_mfma_f32_16x16x32_bf16 v[144:147], a[220:223], v[124:127], v[144:147]\n"
      "v_fmac_f32 v155, v121, v121\n"
      "v_fmac_f32 v154, v120, v120\n"
      "v_mfma_f32_16x16x32_bf16 v[96:99], a[240:243], v[150:153], 0\n"
      "buffer_store_dwordx4 v[194:197], v25, s[24:27], s85 offen sc1\n"
      "v_pk_fma_f32 v[154:155], v[168:169], v[168:169], v[154:155]\n"
      "v_pk_fma_f32 v[154:155], v[170:171], v[170:171], v[154:155]\n"
      "v_mfma_f32_16x16x32_bf16 v[96:99], a[244:247], v[124:127], v[96:99]\n"
      "v_add_f32 v154, v154, v155\n"
      "v_mov_b32 v186, v154\n"
      "v_mfma_f32_16x16x32_bf16 v[194:197], v[232:235], v[150:153], 0\n"
      "s_nop 0\n"
      "v_permlane16_swap_b32 v154, v186\n"
      "v_mfma_f32_16x16x32_bf16 v[194:197], v[236:239], v[124:127], v[194:197]\n"
      "v_add_f32 v154, v154, v186\n"
      "v_mfma_f32_16x16x32_bf16 v[144:147], a[224:227], v[182:185], v[144:147]\n"
      "s_nop 0\n"
      "v_permlane32_swap_b32 v148, v154\n"
      "v_add_f32 v148, v148, v154\n"
      "v_mfma_f32_16x16x32_bf16 v[144:147], a[228:231], v[104:107], v[144:147]\n"
      "ds_write_b32 v31, v148 offset:1024\n"
      "v_mfma_f32_16x16x32_bf16 v[96:99], a[248:251], v[182:185], v[96:99]\n"
      "v_subrev_u32 v157, 73728, v5\n"
      "v_mfma_f32_16x16x32_bf16 v[96:99], a[252:255], v[104:107], v[96:99]\n"
      "v_sub_u32 v190, v218, v190\n"
      "v_readlane_b32 s85, v37, s42\n"
      "v_mfma_f32_16x16x32_bf16 v[194:197], v[240:243], v[182:185], v[194:197]\n"
      "s_or_b32 s95, s85, s48\n"
      "v_lshrrev_b32 v77, 16, v77\n"
      "v_sub_u32 v156, v78, v156\n"
      "v_and_b32 v218, 7, v76\n"
      "v_mfma_f32_16x16x32_bf16 v[194:197], v[244:247], v[104:107], v[194:197]\n"
      "v_xor_b32 v190, v190, v218\n"
      "v_mfma_f32_16x16x32_bf16 v[144:147], a[232:235], v[160:163], v[144:147]\n"
      "s_cmp_ge_u32 s95, 0x40000000\n"
      "s_cbranch_scc1 L_rare_27_%=\n"
      "L_back_28_%=:\n"
      "v_mfma_f32_16x16x32_bf16 v[144:147], a[236:239], v[210:213], v[144:147]\n"
      "s_waitcnt lgkmcnt(0)\n"
      "s_barrier\n"
      "ds_read_b128 v[104:107], v9 offset:1024\n"
      "v_mfma_f32_16x16x32_bf16 v[96:99], v[224:227], v[160:163], v[96:99]\n"
      "ds_read_b128 v[152:155], v85 offset:256\n"
      "ds_read_b128 v[198:201], v85 offset:2304\n"
      "v_mfma_f32_16x16x32_bf16 v[96:99], v[228:231], v[210:213], v[96:99]\n"
      "ds_read_b128 v[180:183], v7 offset:18432\n"
      "v_fma_f32 v218, v144, v144, s52\n"
      "v_mul_f32_e64 v219, v145, v145\n"
      "v_mfma_f32_16x16x32_bf16 v[194:197], v[248:251], v[160:163], v[194:197]\n"
      "ds_read_b128 v[162:165], v7 offset:22528\n"
      "ds_read_b128 v[202:205], v7 offset:26624\n"
      "ds_read_b128 v[184:187], v85 offset:4352\n"
      "v_pk_fma_f32 v[218:219], v[146:147], v[146:147], v[218:219]\n"
      "v_pk_fma_f32 v[218:219], v[96:97], v[96:97], v[218:219]\n"
      "v_mfma_f32_16x16x32_bf16 v[194:197], v[252:255], v[210:213], v[194:197]\n"
      "ds_read_b128 v[210:213], v7 offset:34816\n"
      "s_waitcnt lgkmcnt(7)\n"
      "v_add_f32 v104, v104, v105\n"
      "v_add_f32 v106, v106, v107\n"
      "v_add_f32 v104, v104, v106\n"
      "v_rsq_f32_e64 v104, v104\n"
      "s_waitcnt lgkmcnt(6)\n"
      "v_mfma_f32_16x16x32_bf16 v[148:151], a[216:219], v[152:155], 0\n"
      "s_waitcnt lgkmcnt(5)\n"
      "v_mul_f32 v104, 0x415db3d7, v104\n"
      "v_mov_b32_e64 v106, v104\n"
      "v_mfma_f32_16x16x32_bf16 v[148:151], a[220:223], v[198:201], v[148:151]\n"
      "ds_read_b128 v[214:217], v85 offset:6400\n"
      "ds_read_b128 v[136:139], v85 offset:8448\n"
      "v_mfma_f32_16x16x32_bf16 v[124:127], a[240:243], v[152:155], 0\n"
      "s_waitcnt lgkmcnt(5)\n"
      "v_permlane32_swap_b32 v104, v106\n"
      "v_mfma_f32_16x16x32_bf16 v[124:127], a[244:247], v[198:201], v[124:127]\n"
      "buffer_store_dwordx4 v[180:183], v20, s[24:27], s85 offen sc1\n"
      "v_mov_b32_e64 v105, v104\n"
      "v_pk_mul_f32 v[132:133], v[132:133], v[106:107] op_sel_hi:[1,0]\n"
      "v_pk_mul_f32 v[134:135], v[134:135], v[106:107] op_sel_hi:[1,0]\n"
      "v_mfma_f32_16x16x32_bf16 v[180:183], v[232:235], v[152:155], 0\n"
      "s_waitcnt lgkmcnt(2)\n"
      "v_permlane16_swap_b32_e64 v104, v105\n"
      "v_mfma_f32_16x16x32_bf16 v[180:183], v[236:239], v[198:201], v[180:183]\n"
      "buffer_store_dwordx4 v[162:165], v21, s[24:27], s85 offen sc1\n"
      "v_pk_fma_f32 v[218:219], v[98:99], v[98:99], v[218:219]\n"
      "v_pk_mul_f32 v[170:171], v[170:171], v[106:107] op_sel_hi:[1,0]\n"
      "v_pk_mul_f32 v[118:119], v[118:119], v[106:107] op_sel_hi:[1,0]\n"
      "v_pk_mul_f32 v[172:173], v[172:173], v[104:105] op_sel_hi:[1,0]\n"
      "v_pk_mul_f32 v[174:175], v[174:175], v[104:105] op_sel_hi:[1,0]\n"
      "v_pk_mul_f32 v[168:169], v[168:169], v[106:107] op_sel_hi:[1,0]\n"
      "v_pk_mul_f32 v[140:141], v[140:141], v[104:105] op_sel_hi:[1,0]\n"
      "v_pk_mul_f32 v[110:111], v[110:111], v[104:105] op_sel:[0,1]\n"
      "v_mfma_f32_16x16x32_bf16 v[148:151], a[224:227], v[184:187], v[148:151]\n"
      "s_waitcnt lgkmcnt(0)\n"
      "v_cvt_pk_bf16_f32 v172, v172, v173\n"
      "v_cvt_pk_bf16_f32 v168, v168, v169\n"
      "v_mfma_f32_16x16x32_bf16 v[148:151], a[228:231], v[214:217], v[148:151]\n"
      "buffer_store_dwordx4 v[202:205], v22, s[24:27], s85 offen sc1\n"
      "v_pk_mul_f32 v[208:209], v[208:209], v[104:105] op_sel:[0,1]\n"
      "v_pk_mul_f32 v[220:221], v[220:221], v[104:105] op_sel_hi:[1,0]\n"
      "v_pk_mul_f32 v[108:109], v[108:109], v[104:105] op_sel:[0,1]\n"
      "v_mfma_f32_16x16x32_bf16 v[124:127], a[248:251], v[184:187], v[124:127]\n"
      "ds_read_b128 v[198:201], v85 offset:10496\n"
      "v_cvt_pk_bf16_f32 v173, v174, v175\n"
      "v_mfma_f32_16x16x32_bf16 v[124:127], a[252:255], v[214:217], v[124:127]\n"
      "v_cvt_pk_bf16_f32 v169, v170, v171\n"
      "v_cvt_pk_bf16_f32 v108, v108, v109\n"
      "v_cvt_pk_bf16_f32 v174, v220, v221\n"
      "v_cvt_pk_bf16_f32 v132, v132, v133\n"
      "v_cvt_pk_bf16_f32 v133, v134, v135\n"
      "v_pk_mul_f32 v[142:143], v[142:143], v[104:105] op_sel_hi:[1,0]\n"
      "v_pk_mul_f32 v[120:121], v[120:121], v[106:107] op_sel_hi:[1,0]\n"
      "v_pk_mul_f32 v[222:223], v[222:223], v[104:105] op_sel_hi:[1,0]\n"
      "v_pk_mul_f32 v[206:207], v[206:207], v[104:105] op_sel:[0,1]\n"
      "v_mfma_f32_16x16x32_bf16 v[180:183], v[240:243], v[184:187], v[180:183]\n"
      "ds_read_b128 v[162:165], v7 offset:30720\n"
      "v_cvt_pk_bf16_f32 v134, v118, v119\n"
      "v_mfma_f32_16x16x32_bf16 v[180:183], v[244:247], v[214:217], v[180:183]\n"
      "ds_write_b64 v6, v[168:169] offset:12288\n"
      "v_cvt_pk_bf16_f32 v135, v120, v121\n"
      "v_mfma_f32_16x16x32_bf16 v[148:151], a[232:235], v[136:139], v[148:151]\n"
      "s_waitcnt lgkmcnt(2)\n"
      "v_cvt_pk_bf16_f32 v140, v140, v141\n"
      "v_cvt_pk_bf16_f32 v175, v222, v223\n"
      "v_mfma_f32_16x16x32_bf16 v[148:151], a[236:239], v[198:201], v[148:151]\n"
      "ds_write_b128 v5, v[172:175]\n"
      "v_mfma_f32_16x16x32_bf16 v[124:127], v[224:227], v[136:139], v[124:127]\n"
      "v_cvt_pk_bf16_f32 v141, v142, v143\n"
      "v_cvt_pk_bf16_f32 v109, v110, v111\n"
      "v_mfma_f32_16x16x32_bf16 v[124:127], v[228:231], v[198:201], v[124:127]\n"
      "ds_write_b64 v6, v[108:109] offset:6144\n"
      "v_mfma_f32_16x16x32_bf16 v[180:183], v[248:251], v[136:139], v[180:183]\n"
      "ds_write_b64 v6, v[140:141]\n"
      "v_mfma_f32_16x16x32_bf16 v[180:183], v[252:255], v[198:201], v[180:183]\n"
      "ds_write_b128 v5, v[132:135] offset:12288\n"
      "v_fma_f32 v134, v148, v148, s52\n"
      "v_mul_f32 v135, v149, v149\n"
      "v_pk_fma_f32 v[134:135], v[150:151], v[150:151], v[134:135]\n"
      "v_pk_fma_f32 v[134:135], v[124:125], v[124:125], v[134:135]\n"
      "v_pk_fma_f32 v[134:135], v[126:127], v[126:127], v[134:135]\n"
      "v_pk_fma_f32 v[218:219], v[194:195], v[194:195], v[218:219]\n"
      "v_pk_fma_f32 v[218:219], v[196:197], v[196:197], v[218:219]\n"
      "v_pk_fma_f32 v[134:135], v[180:181], v[180:181], v[134:135]\n"
      "v_pk_fma_f32 v[134:135], v[182:183], v[182:183], v[134:135]\n"
      "v_add_f32 v218, v218, v219\n"
      "v_add_f32 v134, v134, v135\n"
      "v_cvt_pk_bf16_f32 v206, v206, v207\n"
      "v_pk_mul_f32 v[176:177], v[176:177], v[104:105] op_sel:[0,1]\n"
      "v_cvt_pk_bf16_f32 v207, v208, v209\n"
      "v_permlane16_swap_b32 v218, v134\n"
      "v_add_f32 v218, v218, v134\n"
      "v_mov_b32 v134, v218\n"
      "v_pk_mul_f32 v[178:179], v[178:179], v[104:105] op_sel:[0,1]\n"
      "v_cvt_pk_bf16_f32 v209, v178, v179\n"
      "v_cvt_pk_bf16_f32 v208, v176, v177\n"
      "v_permlane32_swap_b32 v218, v134\n"
      "v_add_f32_e64 v218, v218, v134\n"
      "s_add_u32 s42, s42, 1\n"
      "ds_write_b128 v5, v[206:209] offset:6144\n"
      "ds_write_b32 v31, v218 offset:4608\n"
      "s_waitcnt lgkmcnt(2)\n"
      "buffer_store_dwordx4 v[162:165], v23, s[24:27], s85 offen sc1\n"
      "v_mul_u32_u24 v105, 24, v77\n"
      "v_and_b32 v81, 196224, v81\n"
      "v_and_b32 v78, 7, v100\n"
      "v_sub_u32_e64 v105, v80, v105\n"
      "v_lshl_add_u32 v100, v100, 2, 12\n"
      "v_and_b32_e64 v80, 7, v77\n"
      "buffer_store_dwordx4 v[210:213], v25, s[24:27], s85 offen sc1\n"
      "s_waitcnt lgkmcnt(0)\n"
      "s_barrier\n"
      "ds_read_b128 v[206:209], v9 offset:4608\n"
      "v_lshl_add_u32 v76, v76, 2, 12\n"
      "v_xor_b32_e64 v105, v105, v80\n"
      "v_readlane_b32 s85, v37, s42\n"
      "v_sub_u32 v84, v84, v81\n"
      "s_or_b32 s95, s85, s48\n"
      "ds_read_b128 v[162:165], v7\n"
      "ds_read_b128 v[176:179], v7 offset:8192\n"
      "ds_bpermute_b32 v81, v76, v84\n"
      "v_lshl_add_u32 v77, v77, 2, 12\n"
      "s_cmp_ge_u32 s95, 0x40000000\n"
      "s_cbranch_scc1 L_rare_31_%=\n"
      "L_back_32_%=:\n"
      "ds_read_b128 v[132:135], v7 offset:12288\n"
      "ds_read_b128 v[198:201], v7 offset:4096\n"
      "s_waitcnt lgkmcnt(5)\n"
      "v_add_f32 v208, v208, v209\n"
      "v_add_f32 v206, v206, v207\n"
      "v_add_f32 v206, v206, v208\n"
      "v_rsq_f32 v206, v206\n"
      "s_nop 0\n"
      "v_mul_f32 v206, 0x415db3d7, v206\n"
      "v_mov_b32 v208, v206\n"
      "s_nop 1\n"
      "v_permlane32_swap_b32 v206, v208\n"
      "v_mov_b32_e64 v207, v206\n"
      "s_waitcnt lgkmcnt(2)\n"
      "buffer_store_dwordx4 v[162:165], v20, s[24:27], s85 offen sc1\n"
      "v_permlane16_swap_b32_e64 v206, v207\n"
      "v_pk_mul_f32 v[146:147], v[146:147], v[206:207] op_sel_hi:[1,0]\n"
      "v_pk_mul_f32 v[144:145], v[144:145], v[206:207] op_sel_hi:[1,0]\n"
      "v_pk_mul_f32 v[126:127], v[126:127], v[206:207] op_sel:[0,1]\n"
      "v_pk_mul_f32 v[150:151], v[150:151], v[206:207] op_sel:[0,1]\n"
      "v_pk_mul_f32 v[98:99], v[98:99], v[206:207] op_sel_hi:[1,0]\n"
      "v_cvt_pk_bf16_f32 v144, v144, v145\n"
      "v_pk_mul_f32 v[148:149], v[148:149], v[206:207] op_sel:[0,1]\n"
      "v_pk_mul_f32 v[124:125], v[124:125], v[206:207] op_sel:[0,1]\n"
      "v_cvt_pk_bf16_f32 v145, v146, v147\n"
      "v_cvt_pk_bf16_f32 v148, v148, v149\n"
      "v_pk_mul_f32 v[182:183], v[182:183], v[206:207] op_sel:[0,1]\n"
      "v_pk_mul_f32 v[96:97], v[96:97], v[206:207] op_sel_hi:[1,0]\n"
      "v_cvt_pk_bf16_f32 v147, v98, v99\n"
      "v_pk_mul_f32 v[180:181], v[180:181], v[206:207] op_sel:[0,1]\n"
      "v_pk_mul_f32 v[194:195], v[194:195], v[206:207] op_sel_hi:[1,0]\n"
      "v_cvt_pk_bf16_f32 v149, v150, v151\n"
      "v_cvt_pk_bf16_f32 v146, v96, v97\n"
      "ds_write_b128 v157, v[144:147]\n"
      "v_cvt_pk_bf16_f32 v180, v180, v181\n"
      "v_cvt_pk_bf16_f32 v150, v124, v125\n"
      "v_cvt_pk_bf16_f32 v151, v126, v127\n"
      "v_cvt_pk_bf16_f32 v194, v194, v195\n"
      "v_cvt_pk_bf16_f32 v181, v182, v183\n"
      "ds_write_b128 v157, v[148:151] offset:6144\n"
      "ds_write_b64 v82, v[180:181] offset:6144\n"
      "v_pk_mul_f32 v[196:197], v[196:197], v[206:207] op_sel_hi:[1,0]\n"
      "v_cvt_pk_bf16_f32 v195, v196, v197\n"
      "s_add_u32 s42, s42, 1\n"
      "ds_write_b64 v82, v[194:195]\n"
      "v_xor_b32_e64 v156, v156, v78\n"
      "s_waitcnt lgkmcnt(3)\n"
      "buffer_store_dwordx4 v[198:201], v21, s[24:27], s85 offen sc1\n"
      "s_nop 1\n"
      "ds_read_b128 v[198:201], v7 offset:16384\n"
      "ds_bpermute_b32 v78, v100, v84\n"
      "buffer_store_dwordx4 v[176:179], v22, s[24:27], s85 offen sc1\n"
      "s_waitcnt lgkmcnt(2)\n"
      "s_barrier\n"
      "ds_read_b128 v[176:179], v86\n"
      "buffer_store_dwordx4 v[132:135], v23, s[24:27], s85 offen sc1\n"
      "ds_bpermute_b32 v194, v77, v84\n"
      "s_nop 0\n"
      "ds_read_b128 v[132:135], v86 offset:1024\n"
      "s_waitcnt lgkmcnt(3)\n"
      "buffer_store_dwordx4 v[198:201], v25, s[24:27], s85 offen sc1\n"
      "s_nop 1\n"
      "ds_read_b128 v[198:201], v86 offset:2048\n"
      "v_lshl_add_u32 v78, v156, 4, v78\n"
      "v_lshl_add_u32 v81, v190, 4, v81\n"
      "s_waitcnt lgkmcnt(3)\n"
      "buffer_store_dwordx4 v[176:179], v78, s[24:27], 0 offen sc1\n"
      "s_waitcnt lgkmcnt(2)\n"
      "v_lshl_add_u32 v194, v105, 4, v194\n"
      "s_waitcnt lgkmcnt(1)\n"
      "buffer_store_dwordx4 v[132:135], v194, s[24:27], 0 offen sc1\n"
      "s_waitcnt lgkmcnt(0)\n"
      "buffer_store_dwordx4 v[198:201], v81, s[24:27], 0 offen sc1\n"
      "s_endpgm\n"
      "L_tail0_%=:\n"
      "s_nop 4\n"
      "ds_read_b128 v[148:151], v9 offset:1024\n"
      "v_mfma_f32_16x16x32_bf16 v[152:155], v[144:147], v[194:197], 0\n"
      "ds_read_b128 v[194:197], v2 offset:18432\n"
      "v_subrev_u32 v185, 115456, v9\n"
      "v_mfma_f32_16x16x32_bf16 v[152:155], v[198:201], v[76:79], v[152:155]\n"
      "ds_read_b128 v[124:127], v2 offset:27648\n"
      "ds_read_b128 v[216:219], v2 offset:30720\n"
      "ds_read_b128 v[202:205], v2 offset:24576\n"
      "v_mfma_f32_16x16x32_bf16 v[210:213], v[144:147], v[104:107], 0\n"
      "ds_read_b128 v[136:139], v2 offset:18688\n"
      "v_and_b32 v76, 0x3fffffff, v37\n"
      "v_mfma_f32_16x16x32_bf16 v[210:213], v[198:201], v[80:83], v[210:213]\n"
      "ds_read_b128 v[104:107], v2 offset:21504\n"
      "v_mfma_f32_16x16x32_bf16 v[180:183], v[144:147], v[96:99], 0\n"
      "ds_read_b128 v[144:147], v2 offset:33792\n"
      "s_waitcnt lgkmcnt(7)\n"
      "v_add_f32 v148, v148, v149\n"
      "v_add_f32 v150, v150, v151\n"
      "v_add_f32 v148, v148, v150\n"
      "v_rsq_f32_e64 v148, v148\n"
      "v_cndmask_b32 v152, v152, v210, s[58:59]\n"
      "v_mul_f32 v148, 0x415db3d7, v148\n"
      "v_mov_b32 v150, v148\n"
      "s_mul_i32 s92, s40, 3072\n"
      "v_ashrrev_i32 v80, 31, v37\n"
      "v_permlane32_swap_b32 v148, v150\n"
      "v_mov_b32 v149, v148\n"
      "v_pk_mul_f32 v[132:133], v[132:133], v[150:151] op_sel_hi:[1,0]\n"
      "v_pk_mul_f32 v[118:119], v[118:119], v[150:151] op_sel_hi:[1,0]\n"
      "v_pk_mul_f32 v[120:121], v[120:121], v[150:151] op_sel_hi:[1,0]\n"
      "v_permlane16_swap_b32_e64 v148, v149\n"
      "v_pk_mul_f32 v[208:209], v[208:209], v[148:149] op_sel:[0,1]\n"
      "v_pk_mul_f32 v[178:179], v[178:179], v[148:149] op_sel:[0,1]\n"
      "v_cvt_pk_bf16_f32 v132, v132, v133\n"
      "v_pk_mul_f32 v[206:207], v[206:207], v[148:149] op_sel:[0,1]\n"
      "v_pk_mul_f32 v[168:169], v[168:169], v[150:151] op_sel_hi:[1,0]\n"
      "v_pk_mul_f32 v[220:221], v[220:221], v[148:149] op_sel_hi:[1,0]\n"
      "v_pk_mul_f32 v[174:175], v[174:175], v[148:149] op_sel_hi:[1,0]\n"
      "v_pk_mul_f32 v[108:109], v[108:109], v[148:149] op_sel:[0,1]\n"
      "v_pk_mul_f32 v[172:173], v[172:173], v[148:149] op_sel_hi:[1,0]\n"
      "v_cvt_pk_bf16_f32 v172, v172, v173\n"
      "v_cvt_pk_bf16_f32 v108, v108, v109\n"
      "v_pk_mul_f32 v[140:141], v[140:141], v[148:149] op_sel_hi:[1,0]\n"
      "v_cvt_pk_bf16_f32 v140, v140, v141\n"
      "v_pk_mul_f32 v[134:135], v[134:135], v[150:151] op_sel_hi:[1,0]\n"
      "v_cvt_pk_bf16_f32 v168, v168, v169\n"
      "v_cvt_pk_bf16_f32 v133, v134, v135\n"
      "v_pk_mul_f32 v[176:177], v[176:177], v[148:149] op_sel:[0,1]\n"
      "v_cvt_pk_bf16_f32 v206, v206, v207\n"
      "v_pk_mul_f32 v[110:111], v[110:111], v[148:149] op_sel:[0,1]\n"
      "v_cvt_pk_bf16_f32 v207, v208, v209\n"
      "v_pk_mul_f32 v[142:143], v[142:143], v[148:149] op_sel_hi:[1,0]\n"
      "v_cvt_pk_bf16_f32 v109, v110, v111\n"
      "v_pk_mul_f32 v[222:223], v[222:223], v[148:149] op_sel_hi:[1,0]\n"
      "v_pk_mul_f32 v[170:171], v[170:171], v[150:151] op_sel_hi:[1,0]\n"
      "v_cvt_pk_bf16_f32 v209, v178, v179\n"
      "v_cvt_pk_bf16_f32 v173, v174, v175\n"
      "v_cvt_pk_bf16_f32 v174, v220, v221\n"
      "v_cvt_pk_bf16_f32 v141, v142, v143\n"
      "v_cvt_pk_bf16_f32 v208, v176, v177\n"
      "v_cvt_pk_bf16_f32 v175, v222, v223\n"
      "v_cvt_pk_bf16_f32 v169, v170, v171\n"
      "v_cvt_pk_bf16_f32 v135, v120, v121\n"
      "v_cvt_pk_bf16_f32 v134, v118, v119\n"
      "v_mfma_f32_16x16x32_bf16 v[180:183], v[198:201], v[84:87], v[180:183]\n"
      "ds_write_b64 v6, v[168:169] offset:12288\n"
      "ds_write_b128 v5, v[172:175]\n"
      "ds_write_b64 v6, v[108:109] offset:6144\n"
      "ds_write_b64 v6, v[140:141]\n"
      "ds_write_b128 v5, v[206:209] offset:6144\n"
      "ds_write_b128 v5, v[132:135] offset:12288\n"
      "s_waitcnt lgkmcnt(5)\n"
      "v_mfma_f32_16x16x32_bf16 v[172:175], a[216:219], v[194:197], 0\n"
      "ds_read_b128 v[132:135], v7 offset:18432\n"
      "v_mov_b32_dpp v162, v162 quad_perm:[0,0,0,0] row_mask:0xf bank_mask:0xf\n"
      "v_mfma_f32_16x16x32_bf16 v[172:175], a[220:223], v[104:107], v[172:175]\n"
      "ds_read_b128 v[210:213], v2 offset:18944\n"
      "v_and_b32 v80, 100270464, v80\n"
      "v_mfma_f32_16x16x32_bf16 v[220:223], a[240:243], v[194:197], 0\n"
      "ds_read_b128 v[118:121], v2 offset:27904\n"
      "v_add_u32 v86, s92, v185\n"
      "v_mfma_f32_16x16x32_bf16 v[220:223], a[244:247], v[104:107], v[220:223]\n"
      "v_rcp_f32_e64 v162, v162\n"
      "v_mfma_f32_16x16x32_bf16 v[140:143], v[232:235], v[194:197], 0\n"
      "v_subrev_u32 v186, 115456, v9\n"
      "v_and_b32 v194, 0x300, v186\n"
      "v_mfma_f32_16x16x32_bf16 v[140:143], v[236:239], v[104:107], v[140:143]\n"
      "v_add_u32_e64 v186, v186, v194\n"
      "v_mfma_f32_16x16x32_bf16 v[172:175], a[224:227], v[202:205], v[172:175]\n"
      "ds_read_b128 v[104:107], v2 offset:25088\n"
      "v_mfma_f32_16x16x32_bf16 v[172:175], a[228:231], v[124:127], v[172:175]\n"
      "ds_read_b128 v[148:151], v7 offset:26624\n"
      "v_lshrrev_b32_e64 v185, 4, v86\n"
      "v_mfma_f32_16x16x32_bf16 v[220:223], a[248:251], v[202:205], v[220:223]\n"
      "ds_read_b128 v[168:171], v2 offset:30976\n"
      "v_mfma_f32_16x16x32_bf16 v[220:223], a[252:255], v[124:127], v[220:223]\n"
      "ds_read_b128 v[194:197], v2 offset:21760\n"
      "v_add_u32_e64 v78, 64, v185\n"
      "v_mfma_f32_16x16x32_bf16 v[140:143], v[240:243], v[202:205], v[140:143]\n"
      "v_add_u32 v186, 150784, v186\n"
      "v_mfma_f32_16x16x32_bf16 v[140:143], v[244:247], v[124:127], v[140:143]\n"
      "ds_read_b128 v[124:127], v186 offset:4096\n"
      "v_cndmask_b32 v152, v152, v180, s[60:61]\n"
      "v_mfma_f32_16x16x32_bf16 v[172:175], a[232:235], v[216:219], v[172:175]\n"
      "ds_read_b128 v[180:183], v2 offset:24832\n"
      "v_mfma_f32_16x16x32_bf16 v[172:175], a[236:239], v[144:147], v[172:175]\n"
      "v_mul_u32_u24 v81, 2731, v78\n"
      "v_mfma_f32_16x16x32_bf16 v[220:223], v[224:227], v[216:219], v[220:223]\n"
      "v_add_u32 v100, 128, v185\n"
      "v_mfma_f32_16x16x32_bf16 v[220:223], v[228:231], v[144:147], v[220:223]\n"
      "ds_read_b128 v[200:203], v186 offset:2048\n"
      "v_mfma_f32_16x16x32_bf16 v[140:143], v[248:251], v[216:219], v[140:143]\n"
      "ds_read_b128 v[216:219], v186\n"
      "v_mfma_f32_16x16x32_bf16 v[140:143], v[252:255], v[144:147], v[140:143]\n"
      "ds_read_b128 v[144:147], v2 offset:34048\n"
      "v_mfma_f32_16x16x32_bf16 v[206:209], a[216:219], v[136:139], 0\n"
      "s_waitcnt lgkmcnt(5)\n"
      "v_fma_f32 v204, v172, v172, s52\n"
      "v_mfma_f32_16x16x32_bf16 v[206:209], a[220:223], v[194:197], v[206:209]\n"
      "ds_read_b128 v[154:157], v2 offset:34304\n"
      "v_mfma_f32_16x16x32_bf16 v[176:179], a[240:243], v[136:139], 0\n"
      "v_mul_f32 v205, v173, v173\n"
      "v_mul_f32 v152, v162, v152\n"
      "v_mfma_f32_16x16x32_bf16 v[176:179], a[244:247], v[194:197], v[176:179]\n"
      "v_fmac_f32 v204, v174, v174\n"
      "v_add_u32 v76, 1179648, v76\n"
      "v_mfma_f32_16x16x32_bf16 v[108:111], v[232:235], v[136:139], 0\n"
      "s_waitcnt lgkmcnt(2)\n"
      "v_fmac_f32 v205, v175, v175\n"
      "v_mfma_f32_16x16x32_bf16 v[108:111], v[236:239], v[194:197], v[108:111]\n"
      "ds_read_b128 v[164:167], v7 offset:34816\n"
      "v_add_u32_e64 v82, 0, v185\n"
      "v_mfma_f32_16x16x32_bf16 v[206:209], a[224:227], v[180:183], v[206:209]\n"
      "ds_read_b128 v[194:197], v7 offset:22528\n"
      "v_mul_u32_u24 v190, 2731, v82\n"
      "v_mfma_f32_16x16x32_bf16 v[206:209], a[228:231], v[118:121], v[206:209]\n"
      "v_sub_u32 v76, v76, v80\n"
      "v_lshrrev_b32 v81, 16, v81\n"
      "v_mfma_f32_16x16x32_bf16 v[176:179], a[248:251], v[180:183], v[176:179]\n"
      "buffer_store_dwordx4 v[132:135], v20, s[24:27], s46 offen sc1\n"
      "v_mfma_f32_16x16x32_bf16 v[176:179], a[252:255], v[118:121], v[176:179]\n"
      "ds_read_b128 v[96:99], v2 offset:22016\n"
      "v_fmac_f32_e64 v204, v220, v220\n"
      "v_mfma_f32_16x16x32_bf16 v[108:111], v[240:243], v[180:183], v[108:111]\n"
      "ds_read_b128 v[182:185], v2 offset:28160\n"
      "v_fmac_f32 v205, v221, v221\n"
      "v_mfma_f32_16x16x32_bf16 v[108:111], v[244:247], v[118:121], v[108:111]\n"
      "ds_read_b128 v[136:139], v7 offset:30720\n"
      "v_fmac_f32 v204, v222, v222\n"
      "v_mfma_f32_16x16x32_bf16 v[206:209], a[232:235], v[168:171], v[206:209]\n"
      "s_waitcnt lgkmcnt(3)\n"
      "v_cvt_pk_bf16_f32 v152, v152, v152\n"
      "v_lshrrev_b32 v190, 16, v190\n"
      "v_mfma_f32_16x16x32_bf16 v[206:209], a[236:239], v[144:147], v[206:209]\n"
      "ds_write_b16 v12, v152 offset:16\n"
      "v_mfma_f32_16x16x32_bf16 v[176:179], v[224:227], v[168:171], v[176:179]\n"
      "v_mul_u32_u24 v77, 24, v190\n"
      "v_mfma_f32_16x16x32_bf16 v[176:179], v[228:231], v[144:147], v[176:179]\n"
      "v_fmac_f32 v205, v223, v223\n"
      "v_fmac_f32 v205, v141, v141\n"
      "v_mfma_f32_16x16x32_bf16 v[108:111], v[248:251], v[168:171], v[108:111]\n"
      "s_nop 0\n"
      "v_fma_f32 v180, v206, v206, s52\n"
      "v_mul_u32_u24_e64 v193, 24, v81\n"
      "v_mfma_f32_16x16x32_bf16 v[108:111], v[252:255], v[144:147], v[108:111]\n"
      "s_waitcnt lgkmcnt(1)\n"
      "v_mul_u32_u24 v215, 2731, v100\n"
      "v_mul_f32 v181, v207, v207\n"
      "v_mfma_f32_16x16x32_bf16 v[132:135], a[216:219], v[210:213], 0\n"
      "ds_read_b128 v[144:147], v2 offset:31232\n"
      "v_mfma_f32_16x16x32_bf16 v[132:135], a[220:223], v[96:99], v[132:135]\n"
      "buffer_store_dwordx4 v[194:197], v21, s[24:27], s46 offen sc1\n"
      "v_mfma_f32_16x16x32_bf16 v[118:121], a[240:243], v[210:213], 0\n"
      "v_fmac_f32 v181, v209, v209\n"
      "v_fmac_f32 v204, v140, v140\n"
      "v_mfma_f32_16x16x32_bf16 v[118:121], a[244:247], v[96:99], v[118:121]\n"
      "v_fmac_f32 v180, v208, v208\n"
      "v_fmac_f32 v181, v177, v177\n"
      "v_mfma_f32_16x16x32_bf16 v[168:171], v[232:235], v[210:213], 0\n"
      "v_fmac_f32 v180, v176, v176\n"
      "v_fmac_f32 v205, v143, v143\n"
      "v_mfma_f32_16x16x32_bf16 v[168:171], v[236:239], v[96:99], v[168:171]\n"
      "buffer_store_dwordx4 v[148:151], v22, s[24:27], s46 offen sc1\n"
      "v_mfma_f32_16x16x32_bf16 v[132:135], a[224:227], v[104:107], v[132:135]\n"
      "s_nop 0\n"
      "ds_read_b128 v[150:153], v186 offset:10240\n"
      "v_bfe_i32 v80, v37, 30, 1\n"
      "v_mfma_f32_16x16x32_bf16 v[132:135], a[228:231], v[182:185], v[132:135]\n"
      "v_fmac_f32 v180, v178, v178\n"
      "v_fmac_f32 v181, v179, v179\n"
      "v_mfma_f32_16x16x32_bf16 v[118:121], a[248:251], v[104:107], v[118:121]\n"
      "ds_read_b128 v[96:99], v186 offset:6144\n"
      "v_fmac_f32 v204, v142, v142\n"
      "v_mfma_f32_16x16x32_bf16 v[118:121], a[252:255], v[182:185], v[118:121]\n"
      "buffer_store_dwordx4 v[136:139], v23, s[24:27], s46 offen sc1\n"
      "v_mfma_f32_16x16x32_bf16 v[168:171], v[240:243], v[104:107], v[168:171]\n"
      "ds_read_b128 v[104:107], v186 offset:8192\n"
      "v_fmac_f32_e64 v181, v109, v109\n"
      "v_mfma_f32_16x16x32_bf16 v[168:171], v[244:247], v[182:185], v[168:171]\n"
      "s_waitcnt lgkmcnt(2)\n"
      "v_lshrrev_b32 v215, 16, v215\n"
      "v_fmac_f32 v180, v108, v108\n"
      "v_mfma_f32_16x16x32_bf16 v[132:135], a[232:235], v[144:147], v[132:135]\n"
      "v_add_f32 v204, v204, v205\n"
      "v_mfma_f32_16x16x32_bf16 v[132:135], a[236:239], v[154:157], v[132:135]\n"
      "v_fmac_f32 v181, v111, v111\n"
      "v_fmac_f32 v180, v110, v110\n"
      "v_mfma_f32_16x16x32_bf16 v[118:121], v[224:227], v[144:147], v[118:121]\n"
      "v_sub_u32 v77, v82, v77\n"
      "v_add_f32 v180, v180, v181\n"
      "v_mfma_f32_16x16x32_bf16 v[118:121], v[228:231], v[154:157], v[118:121]\n"
      "s_nop 0\n"
      "v_permlane16_swap_b32 v204, v180\n"
      "v_mfma_f32_16x16x32_bf16 v[168:171], v[248:251], v[144:147], v[168:171]\n"
      "v_mul_f32_e64 v183, v133, v133\n"
      "v_fma_f32 v182, v132, v132, s52\n"
      "v_mfma_f32_16x16x32_bf16 v[168:171], v[252:255], v[154:157], v[168:171]\n"
      "v_fmac_f32 v182, v134, v134\n"
      "v_fmac_f32 v183, v135, v135\n"
      "v_mfma_f32_16x16x32_bf16 v[144:147], a[216:219], v[216:219], 0\n"
      "buffer_store_dwordx4 v[164:167], v25, s[24:27], s46 offen sc1\n"
      "v_pk_fma_f32 v[182:183], v[118:119], v[118:119], v[182:183]\n"
      "v_pk_fma_f32 v[182:183], v[120:121], v[120:121], v[182:183]\n"
      "v_mfma_f32_16x16x32_bf16 v[144:147], a[220:223], v[200:203], v[144:147]\n"
      "s_nop 0\n"
      "v_fmac_f32 v183, v169, v169\n"
      "v_readlane_b32 s46, v37, s42\n"
      "v_mfma_f32_16x16x32_bf16 v[164:167], a[240:243], v[216:219], 0\n"
      "v_fmac_f32 v182, v168, v168\n"
      "v_fmac_f32 v182, v170, v170\n"
      "v_mfma_f32_16x16x32_bf16 v[164:167], a[244:247], v[200:203], v[164:167]\n"
      "v_fmac_f32 v183, v171, v171\n"
      "v_add_f32 v204, v204, v180\n"
      "v_mfma_f32_16x16x32_bf16 v[196:199], v[232:235], v[216:219], 0\n"
      "s_waitcnt lgkmcnt(0)\n"
      "v_add_f32 v182, v182, v183\n"
      "v_mov_b32 v180, v182\n"
      "v_mfma_f32_16x16x32_bf16 v[196:199], v[236:239], v[200:203], v[196:199]\n"
      "s_nop 0\n"
      "v_permlane16_swap_b32 v182, v180\n"
      "v_mfma_f32_16x16x32_bf16 v[144:147], a[224:227], v[124:127], v[144:147]\n"
      "s_or_b32 s95, s46, s48\n"
      "v_add_f32 v182, v182, v180\n"
      "v_mfma_f32_16x16x32_bf16 v[144:147], a[228:231], v[96:99], v[144:147]\n"
      "s_nop 0\n"
      "v_permlane32_swap_b32 v204, v182\n"
      "v_add_f32 v204, v204, v182\n"
      "v_mfma_f32_16x16x32_bf16 v[164:167], a[248:251], v[124:127], v[164:167]\n"
      "ds_write_b32 v31, v204\n"
      "v_mfma_f32_16x16x32_bf16 v[164:167], a[252:255], v[96:99], v[164:167]\n"
      "v_sub_u32 v193, v78, v193\n"
      "v_mfma_f32_16x16x32_bf16 v[196:199], v[240:243], v[124:127], v[196:199]\n"
      "v_and_b32 v78, 7, v81\n"
      "v_lshl_add_u32 v81, v81, 2, 12\n"
      "v_mfma_f32_16x16x32_bf16 v[196:199], v[244:247], v[96:99], v[196:199]\n"
      "v_and_b32 v82, 7, v190\n"
      "v_mfma_f32_16x16x32_bf16 v[144:147], a[232:235], v[104:107], v[144:147]\n"
      "s_cmp_ge_u32 s95, 0x40000000\n"
      "s_cbranch_scc1 L_rare_35_%=\n"
      "L_back_36_%=:\n"
      "v_mfma_f32_16x16x32_bf16 v[144:147], a[236:239], v[150:153], v[144:147]\n"
      "v_mul_u32_u24 v194, 24, v215\n"
      "v_sub_u32_e64 v194, v100, v194\n"
      "v_mfma_f32_16x16x32_bf16 v[164:167], v[224:227], v[104:107], v[164:167]\n"
      "s_waitcnt lgkmcnt(0)\n"
      "s_barrier\n"
      "ds_read_b128 v[124:127], v9\n"
      "ds_read_b128 v[200:203], v7\n"
      "v_mfma_f32_16x16x32_bf16 v[164:167], v[228:231], v[150:153], v[164:167]\n"
      "ds_read_b128 v[216:219], v186 offset:256\n"
      "ds_read_b128 v[180:183], v186 offset:2304\n"
      "ds_read_b128 v[210:213], v186 offset:8448\n"
      "v_fma_f32 v84, v144, v144, s52\n"
      "v_mul_f32_e64 v85, v145, v145\n"
      "v_pk_fma_f32 v[84:85], v[146:147], v[146:147], v[84:85]\n"
      "v_mfma_f32_16x16x32_bf16 v[196:199], v[248:251], v[104:107], v[196:199]\n"
      "ds_read_b128 v[154:157], v186 offset:6400\n"
      "ds_read_b128 v[96:99], v7 offset:4096\n"
      "ds_read_b128 v[104:107], v7 offset:8192\n"
      "ds_read_b128 v[136:139], v186 offset:4352\n"
      "v_pk_fma_f32 v[84:85], v[164:165], v[164:165], v[84:85]\n"
      "v_mfma_f32_16x16x32_bf16 v[196:199], v[252:255], v[150:153], v[196:199]\n"
      "s_waitcnt lgkmcnt(8)\n"
      "v_add_f32 v126, v126, v127\n"
      "v_add_f32_e64 v124, v124, v125\n"
      "v_add_f32_e64 v124, v124, v126\n"
      "v_rsq_f32_e64 v124, v124\n"
      "v_pk_fma_f32 v[84:85], v[166:167], v[166:167], v[84:85]\n"
      "v_mul_f32 v124, 0x415db3d7, v124\n"
      "v_mov_b32_e64 v126, v124\n"
      "v_pk_fma_f32 v[84:85], v[196:197], v[196:197], v[84:85]\n"
      "s_waitcnt lgkmcnt(6)\n"
      "v_mfma_f32_16x16x32_bf16 v[148:151], a[216:219], v[216:219], 0\n"
      "s_waitcnt lgkmcnt(5)\n"
      "v_permlane32_swap_b32_e64 v124, v126\n"
      "v_mfma_f32_16x16x32_bf16 v[148:151], a[220:223], v[180:183], v[148:151]\n"
      "ds_read_b128 v[160:163], v186 offset:10496\n"
      "v_mov_b32_e64 v125, v124\n"
      "v_mfma_f32_16x16x32_bf16 v[184:187], a[240:243], v[216:219], 0\n"
      "buffer_store_dwordx4 v[200:203], v20, s[24:27], s46 offen sc1\n"
      "v_pk_mul_f32 v[118:119], v[118:119], v[126:127] op_sel_hi:[1,0]\n"
      "v_pk_fma_f32 v[84:85], v[198:199], v[198:199], v[84:85]\n"
      "v_pk_mul_f32 v[132:133], v[132:133], v[126:127] op_sel_hi:[1,0]\n"
      "v_pk_mul_f32 v[134:135], v[134:135], v[126:127] op_sel_hi:[1,0]\n"
      "v_mfma_f32_16x16x32_bf16 v[184:187], a[244:247], v[180:183], v[184:187]\n"
      "s_waitcnt lgkmcnt(1)\n"
      "v_permlane16_swap_b32 v124, v125\n"
      "v_mfma_f32_16x16x32_bf16 v[202:205], v[232:235], v[216:219], 0\n"
      "ds_read_b128 v[216:219], v7 offset:12288\n"
      "v_cvt_pk_bf16_f32 v132, v132, v133\n"
      "v_mfma_f32_16x16x32_bf16 v[202:205], v[236:239], v[180:183], v[202:205]\n"
      "ds_read_b128 v[180:183], v7 offset:16384\n"
      "v_cvt_pk_bf16_f32 v133, v134, v135\n"
      "v_mfma_f32_16x16x32_bf16 v[148:151], a[224:227], v[136:139], v[148:151]\n"
      "buffer_store_dwordx4 v[96:99], v21, s[24:27], s46 offen sc1\n"
      "v_pk_mul_f32 v[222:223], v[222:223], v[124:125] op_sel_hi:[1,0]\n"
      "v_pk_mul_f32 v[176:177], v[176:177], v[124:125] op_sel:[0,1]\n"
      "v_pk_mul_f32 v[120:121], v[120:121], v[126:127] op_sel_hi:[1,0]\n"
      "v_pk_mul_f32 v[142:143], v[142:143], v[124:125] op_sel_hi:[1,0]\n"
      "v_pk_mul_f32 v[110:111], v[110:111], v[124:125] op_sel:[0,1]\n"
      "v_pk_mul_f32 v[206:207], v[206:207], v[124:125] op_sel:[0,1]\n"
      "v_pk_mul_f32 v[174:175], v[174:175], v[124:125] op_sel_hi:[1,0]\n"
      "v_mfma_f32_16x16x32_bf16 v[148:151], a[228:231], v[154:157], v[148:151]\n"
      "v_cvt_pk_bf16_f32 v134, v118, v119\n"
      "v_cvt_pk_bf16_f32 v135, v120, v121\n"
      "v_mfma_f32_16x16x32_bf16 v[184:187], a[248:251], v[136:139], v[184:187]\n"
      "buffer_store_dwordx4 v[104:107], v22, s[24:27], s46 offen sc1\n"
      "v_pk_mul_f32 v[140:141], v[140:141], v[124:125] op_sel_hi:[1,0]\n"
      "v_pk_mul_f32 v[172:173], v[172:173], v[124:125] op_sel_hi:[1,0]\n"
      "v_pk_mul_f32 v[208:209], v[208:209], v[124:125] op_sel:[0,1]\n"
      "v_pk_mul_f32 v[108:109], v[108:109], v[124:125] op_sel:[0,1]\n"
      "v_pk_mul_f32 v[220:221], v[220:221], v[124:125] op_sel_hi:[1,0]\n"
      "v_pk_mul_f32 v[178:179], v[178:179], v[124:125] op_sel:[0,1]\n"
      "v_mfma_f32_16x16x32_bf16 v[184:187], a[252:255], v[154:157], v[184:187]\n"
      "v_cvt_pk_bf16_f32 v206, v206, v207\n"
      "v_cvt_pk_bf16_f32 v172, v172, v173\n"
      "v_cvt_pk_bf16_f32 v108, v108, v109\n"
      "v_mfma_f32_16x16x32_bf16 v[202:205], v[240:243], v[136:139], v[202:205]\n"
      "v_cvt_pk_bf16_f32 v207, v208, v209\n"
      "v_cvt_pk_bf16_f32 v173, v174, v175\n"
      "v_cvt_pk_bf16_f32 v109, v110, v111\n"
      "v_mfma_f32_16x16x32_bf16 v[202:205], v[244:247], v[154:157], v[202:205]\n"
      "ds_write_b64 v6, v[108:109] offset:24576\n"
      "v_mfma_f32_16x16x32_bf16 v[148:151], a[232:235], v[210:213], v[148:151]\n"
      "s_waitcnt lgkmcnt(1)\n"
      "v_cvt_pk_bf16_f32 v140, v140, v141\n"
      "v_cvt_pk_bf16_f32 v208, v176, v177\n"
      "v_mfma_f32_16x16x32_bf16 v[148:151], a[236:239], v[160:163], v[148:151]\n"
      "v_cvt_pk_bf16_f32 v209, v178, v179\n"
      "v_cvt_pk_bf16_f32 v175, v222, v223\n"
      "v_cvt_pk_bf16_f32 v141, v142, v143\n"
      "v_mfma_f32_16x16x32_bf16 v[184:187], v[224:227], v[210:213], v[184:187]\n"
      "ds_write_b128 v5, v[206:209] offset:24576\n"
      "v_cvt_pk_bf16_f32 v174, v220, v221\n"
      "v_mfma_f32_16x16x32_bf16 v[184:187], v[228:231], v[160:163], v[184:187]\n"
      "ds_write_b64 v6, v[140:141] offset:18432\n"
      "v_mfma_f32_16x16x32_bf16 v[202:205], v[248:251], v[210:213], v[202:205]\n"
      "ds_write_b128 v5, v[132:135] offset:30720\n"
      "v_mfma_f32_16x16x32_bf16 v[202:205], v[252:255], v[160:163], v[202:205]\n"
      "ds_write_b128 v5, v[172:175] offset:18432\n"
      "v_mul_f32 v135, v149, v149\n"
      "v_fma_f32 v134, v148, v148, s52\n"
      "v_pk_mul_f32 v[168:169], v[168:169], v[126:127] op_sel_hi:[1,0]\n"
      "v_add_f32_e64 v84, v84, v85\n"
      "v_pk_fma_f32 v[134:135], v[150:151], v[150:151], v[134:135]\n"
      "v_pk_fma_f32 v[134:135], v[184:185], v[184:185], v[134:135]\n"
      "v_pk_fma_f32 v[134:135], v[186:187], v[186:187], v[134:135]\n"
      "v_pk_fma_f32 v[134:135], v[202:203], v[202:203], v[134:135]\n"
      "v_pk_fma_f32 v[134:135], v[204:205], v[204:205], v[134:135]\n"
      "v_add_f32 v134, v134, v135\n"
      "v_cvt_pk_bf16_f32 v168, v168, v169\n"
      "v_pk_mul_f32 v[170:171], v[170:171], v[126:127] op_sel_hi:[1,0]\n"
      "v_permlane16_swap_b32 v84, v134\n"
      "v_cvt_pk_bf16_f32 v169, v170, v171\n"
      "v_add_f32 v84, v84, v134\n"
      "v_mov_b32 v134, v84\n"
      "s_add_u32 s42, s42, 1\n"
      "ds_write_b64 v6, v[168:169] offset:30720\n"
      "v_permlane32_swap_b32 v84, v134\n"
      "v_add_f32_e64 v84, v84, v134\n"
      "ds_write_b32 v31, v84 offset:4608\n"
      "buffer_store_dwordx4 v[216:219], v23, s[24:27], s46 offen sc1\n"
      "v_and_b32 v80, 196224, v80\n"
      "v_subrev_u32 v210, 73728, v5\n"
      "v_sub_u32_e64 v76, v76, v80\n"
      "ds_bpermute_b32 v111, v81, v76\n"
      "buffer_store_dwordx4 v[180:183], v25, s[24:27], s46 offen sc1\n"
      "s_waitcnt lgkmcnt(1)\n"
      "s_barrier\n"
      "ds_read_b128 v[134:137], v9 offset:4608\n"
      "v_subrev_u32 v211, 73728, v6\n"
      "ds_read_b128 v[180:183], v7 offset:22528\n"
      "ds_read_b128 v[104:107], v7 offset:18432\n"
      "ds_read_b128 v[168:171], v7 offset:30720\n"
      "v_readlane_b32 s46, v37, s42\n"
      "ds_read_b128 v[174:177], v7 offset:26624\n"
      "v_lshl_add_u32 v190, v190, 2, 12\n"
      "v_add_u32 v12, 32, v12\n"
      "s_or_b32 s95, s46, s48\n"
      "s_cmp_ge_u32 s95, 0x40000000\n"
      "s_cbranch_scc1 L_rare_39_%=\n"
      "L_back_40_%=:\n"
      "v_xor_b32 v77, v77, v82\n"
      "s_waitcnt lgkmcnt(4)\n"
      "v_add_f32 v136, v136, v137\n"
      "v_add_f32 v134, v134, v135\n"
      "v_add_f32 v134, v134, v136\n"
      "v_rsq_f32 v134, v134\n"
      "v_and_b32 v100, 7, v215\n"
      "v_mul_f32 v134, 0x415db3d7, v134\n"
      "v_mov_b32 v136, v134\n"
      "v_lshl_add_u32 v215, v215, 2, 12\n"
      "s_nop 0\n"
      "v_permlane32_swap_b32_e64 v134, v136\n"
      "v_mov_b32_e64 v135, v134\n"
      "s_waitcnt lgkmcnt(1)\n"
      "buffer_store_dwordx4 v[104:107], v20, s[24:27], s46 offen sc1\n"
      "v_permlane16_swap_b32_e64 v134, v135\n"
      "v_pk_mul_f32 v[146:147], v[146:147], v[134:135] op_sel_hi:[1,0]\n"
      "v_pk_mul_f32 v[164:165], v[164:165], v[134:135] op_sel_hi:[1,0]\n"
      "v_pk_mul_f32 v[144:145], v[144:145], v[134:135] op_sel_hi:[1,0]\n"
      "v_pk_mul_f32 v[148:149], v[148:149], v[134:135] op_sel:[0,1]\n"
      "v_pk_mul_f32 v[150:151], v[150:151], v[134:135] op_sel:[0,1]\n"
      "v_pk_mul_f32 v[184:185], v[184:185], v[134:135] op_sel:[0,1]\n"
      "v_pk_mul_f32 v[202:203], v[202:203], v[134:135] op_sel:[0,1]\n"
      "v_cvt_pk_bf16_f32 v148, v148, v149\n"
      "v_pk_mul_f32 v[186:187], v[186:187], v[134:135] op_sel:[0,1]\n"
      "v_pk_mul_f32 v[196:197], v[196:197], v[134:135] op_sel_hi:[1,0]\n"
      "v_cvt_pk_bf16_f32 v149, v150, v151\n"
      "v_cvt_pk_bf16_f32 v202, v202, v203\n"
      "v_cvt_pk_bf16_f32 v150, v184, v185\n"
      "v_cvt_pk_bf16_f32 v144, v144, v145\n"
      "v_pk_mul_f32 v[204:205], v[204:205], v[134:135] op_sel:[0,1]\n"
      "v_pk_mul_f32 v[166:167], v[166:167], v[134:135] op_sel_hi:[1,0]\n"
      "v_cvt_pk_bf16_f32 v151, v186, v187\n"
      "s_add_u32 s42, s42, 1\n"
      "ds_write_b128 v210, v[148:151] offset:6144\n"
      "v_cvt_pk_bf16_f32 v145, v146, v147\n"
      "v_cvt_pk_bf16_f32 v147, v166, v167\n"
      "v_cvt_pk_bf16_f32 v196, v196, v197\n"
      "v_cvt_pk_bf16_f32 v146, v164, v165\n"
      "v_cvt_pk_bf16_f32 v203, v204, v205\n"
      "ds_write_b128 v210, v[144:147]\n"
      "ds_write_b64 v211, v[202:203] offset:6144\n"
      "v_pk_mul_f32 v[198:199], v[198:199], v[134:135] op_sel_hi:[1,0]\n"
      "v_cvt_pk_bf16_f32 v197, v198, v199\n"
      "ds_write_b64 v211, v[196:197]\n"
      "buffer_store_dwordx4 v[180:183], v21, s[24:27], s46 offen sc1\n"
      "ds_bpermute_b32 v204, v215, v76\n"
      "ds_read_b128 v[144:147], v7 offset:34816\n"
      "ds_bpermute_b32 v182, v190, v76\n"
      "s_waitcnt lgkmcnt(5)\n"
      "buffer_store_dwordx4 v[174:177], v22, s[24:27], s46 offen sc1\n"
      "v_xor_b32_e64 v193, v193, v78\n"
      "v_xor_b32 v194, v194, v100\n"
      "v_lshl_add_u32 v111, v193, 4, v111\n"
      "s_waitcnt lgkmcnt(3)\n"
      "s_barrier\n"
      "ds_read_b128 v[164:167], v86\n"
      "buffer_store_dwordx4 v[168:171], v23, s[24:27], s46 offen sc1\n"
      "ds_read_b128 v[208:211], v86 offset:1024\n"
      "s_waitcnt lgkmcnt(2)\n"
      "buffer_store_dwordx4 v[144:147], v25, s[24:27], s46 offen sc1\n"
      "ds_read_b128 v[136:139], v86 offset:2048\n"
      "v_lshl_add_u32 v204, v194, 4, v204\n"
      "v_lshl_add_u32 v182, v77, 4, v182\n"
      "s_waitcnt lgkmcnt(2)\n"
      "buffer_store_dwordx4 v[164:167], v182, s[24:27], 0 offen sc1\n"
      "s_waitcnt lgkmcnt(1)\n"
      "buffer_store_dwordx4 v[208:211], v111, s[24:27], 0 offen sc1\n"
      "s_waitcnt lgkmcnt(0)\n"
      "buffer_store_dwordx4 v[136:139], v204, s[24:27], 0 offen sc1\n"
      "s_endpgm\n"
      "L_rare_1_%=:\n"
      "s_nop 7\n"
      "s_waitcnt lgkmcnt(0)\n"
      "s_add_u32 s72, s42, 1\n"
      "v_readlane_b32 s77, v38, s72\n"
      "v_subrev_u32 v165, 115456, v9\n"
      "s_and_b32 s73, s77, -16\n"
      "s_mov_b32 m0, 110592\n"
      "s_nop 0\n"
      "buffer_load_dwordx4 v165, s[28:31], s73 offen  lds\n"
      "s_add_u32 s73, s73, 1024\n"
      "s_mov_b32 m0, 111616\n"
      "s_nop 0\n"
      "buffer_load_dwordx4 v165, s[28:31], s73 offen  lds\n"
      "s_add_u32 s73, s73, 1024\n"
      "s_mov_b32 m0, 112640\n"
      "s_nop 0\n"
      "buffer_load_dwordx4 v165, s[28:31], s73 offen  lds\n"
      "s_add_u32 s73, s73, 1024\n"
      "s_mov_b32 m0, 113664\n"
      "s_nop 0\n"
      "buffer_load_dwordx4 v165, s[28:31], s73 offen  lds\n"
      "s_add_u32 s73, s73, 1024\n"
      "s_mov_b32 m0, 114688\n"
      "s_mov_b32 exec_hi, 0xffff\n"
      "buffer_load_dwordx4 v165, s[28:31], s73 offen  lds\n"
      "s_mov_b32 exec_hi, -1\n"
      "s_add_u32 s73, s73, 1024\n"
      "s_and_b32 s77, s77, 15\n"
      "s_waitcnt lgkmcnt(0)\n"
      "s_nop 4\n"
      "s_branch L_back_2_%=\n"
      "L_rare_3_%=:\n"
      "s_nop 7\n"
      "s_waitcnt lgkmcnt(0)\n"
      "s_waitcnt vmcnt(0)\n"
      "v_subrev_u32 v105, 115456, v9\n"
      "v_bfe_u32 v163, v105, 4, 4\n"
      "v_mul_u32_u24 v163, 98, v163\n"
      "v_lshrrev_b32 v105, 8, v105\n"
      "v_lshl_add_u32 v163, v105, 3, v163\n"
      "v_add_u32 v163, s77, v163\n"
      "v_and_b32 v222, 2, v163\n"
      "v_and_b32 v163, -4, v163\n"
      "v_add_u32 v163, 110592, v163\n"
      "ds_read2_b32 v[198:199], v163 offset0:0 offset1:1\n"
      "ds_read2_b32 v[200:201], v163 offset0:8 offset1:9\n"
      "ds_read2_b32 v[122:123], v163 offset0:16 offset1:17\n"
      "ds_read2_b32 v[164:165], v163 offset0:2 offset1:10\n"
      "ds_read2_b32 v[146:147], v163 offset0:18 offset1:24\n"
      "s_waitcnt lgkmcnt(4)\n"
      "v_alignbyte_b32 v198, v199, v198, v222\n"
      "s_waitcnt lgkmcnt(1)\n"
      "v_alignbyte_b32 v199, v164, v199, v222\n"
      "v_lshlrev_b32 v105, 16, v198\n"
      "v_fmac_f32 v158, s53, v105\n"
      "v_and_b32 v105, 0xffff0000, v198\n"
      "v_fmac_f32 v159, s53, v105\n"
      "v_lshlrev_b32 v105, 16, v199\n"
      "v_fmac_f32_e64 v160, s53, v105\n"
      "v_and_b32 v105, 0xffff0000, v199\n"
      "v_fmac_f32 v161, s53, v105\n"
      "v_alignbyte_b32 v200, v201, v200, v222\n"
      "v_alignbyte_b32 v201, v165, v201, v222\n"
      "v_lshlrev_b32 v105, 16, v200\n"
      "v_fmac_f32 v126, s53, v105\n"
      "v_and_b32 v105, 0xffff0000, v200\n"
      "v_fmac_f32 v127, s53, v105\n"
      "v_lshlrev_b32 v105, 16, v201\n"
      "v_fmac_f32 v128, s53, v105\n"
      "v_and_b32 v105, 0xffff0000, v201\n"
      "v_fmac_f32 v129, s53, v105\n"
      "v_alignbyte_b32 v122, v123, v122, v222\n"
      "s_waitcnt lgkmcnt(0)\n"
      "v_alignbyte_b32 v123, v146, v123, v222\n"
      "v_lshlrev_b32 v105, 16, v122\n"
      "v_fmac_f32 v176, s53, v105\n"
      "v_and_b32 v105, 0xffff0000, v122\n"
      "v_fmac_f32 v177, s53, v105\n"
      "v_lshlrev_b32 v105, 16, v123\n"
      "v_fmac_f32 v178, s53, v105\n"
      "v_and_b32 v105, 0xffff0000, v123\n"
      "v_fmac_f32 v179, s53, v105\n"
      "v_alignbyte_b32 v105, v147, v147, v222\n"
      "v_lshlrev_b32 v105, 16, v105\n"
      "v_fmac_f32 v172, s53, v105\n"
      "v_max3_f32 v163, v158, v159, v160\n"
      "v_max3_f32 v222, v161, v126, v127\n"
      "v_max3_f32 v163, v163, v128, v129\n"
      "v_max3_f32 v222, v222, v176, v177\n"
      "v_max3_f32 v163, v163, v178, v179\n"
      "v_max3_f32 v163, v163, v222, v172\n"
      "v_mov_b32 v105, v163\n"
      "s_nop 1\n"
      "v_permlane16_swap_b32 v163, v105\n"
      "v_max_f32 v163, v163, v105\n"
      "v_mov_b32 v105, v163\n"
      "s_nop 1\n"
      "v_permlane32_swap_b32 v163, v105\n"
      "v_max_f32 v163, v163, v105\n"
      "v_sub_f32 v158, v158, v163\n"
      "v_sub_f32 v159, v159, v163\n"
      "v_sub_f32 v160, v160, v163\n"
      "v_sub_f32 v161, v161, v163\n"
      "v_sub_f32 v126, v126, v163\n"
      "v_sub_f32 v127, v127, v163\n"
      "v_sub_f32 v128, v128, v163\n"
      "v_sub_f32 v129, v129, v163\n"
      "v_sub_f32 v176, v176, v163\n"
      "v_sub_f32 v177, v177, v163\n"
      "v_sub_f32 v178, v178, v163\n"
      "v_sub_f32 v179, v179, v163\n"
      "v_sub_f32_e64 v172, v172, v163\n"
      "s_waitcnt lgkmcnt(0)\n"
      "s_nop 4\n"
      "s_branch L_back_4_%=\n"
      "L_rare_5_%=:\n"
      "s_nop 7\n"
      "s_waitcnt lgkmcnt(0)\n"
      "s_waitcnt vmcnt(0)\n"
      "v_subrev_u32 v148, 115456, v9\n"
      "v_bfe_u32 v165, v148, 4, 4\n"
      "v_mul_u32_u24 v165, 98, v165\n"
      "v_lshrrev_b32 v148, 8, v148\n"
      "v_lshl_add_u32 v165, v148, 3, v165\n"
      "v_add_u32 v165, s77, v165\n"
      "v_and_b32 v163, 2, v165\n"
      "v_and_b32 v165, -4, v165\n"
      "v_add_u32 v165, 112160, v165\n"
      "ds_read2_b32 v[180:181], v165 offset0:0 offset1:1\n"
      "ds_read2_b32 v[182:183], v165 offset0:8 offset1:9\n"
      "ds_read2_b32 v[146:147], v165 offset0:16 offset1:17\n"
      "ds_read2_b32 v[144:145], v165 offset0:2 offset1:10\n"
      "ds_read2_b32 v[174:175], v165 offset0:18 offset1:24\n"
      "s_waitcnt lgkmcnt(4)\n"
      "v_alignbyte_b32 v180, v181, v180, v163\n"
      "s_waitcnt lgkmcnt(1)\n"
      "v_alignbyte_b32 v181, v144, v181, v163\n"
      "v_lshlrev_b32 v148, 16, v180\n"
      "v_fmac_f32 v140, s53, v148\n"
      "v_and_b32 v148, 0xffff0000, v180\n"
      "v_fmac_f32 v141, s53, v148\n"
      "v_lshlrev_b32 v148, 16, v181\n"
      "v_fmac_f32 v142, s53, v148\n"
      "v_and_b32 v148, 0xffff0000, v181\n"
      "v_fmac_f32 v143, s53, v148\n"
      "v_alignbyte_b32 v182, v183, v182, v163\n"
      "v_alignbyte_b32 v183, v145, v183, v163\n"
      "v_lshlrev_b32 v148, 16, v182\n"
      "v_fmac_f32 v184, s53, v148\n"
      "v_and_b32 v148, 0xffff0000, v182\n"
      "v_fmac_f32 v185, s53, v148\n"
      "v_lshlrev_b32 v148, 16, v183\n"
      "v_fmac_f32_e64 v186, s53, v148\n"
      "v_and_b32 v148, 0xffff0000, v183\n"
      "v_fmac_f32 v187, s53, v148\n"
      "v_alignbyte_b32 v146, v147, v146, v163\n"
      "s_waitcnt lgkmcnt(0)\n"
      "v_alignbyte_b32 v147, v174, v147, v163\n"
      "v_lshlrev_b32 v148, 16, v146\n"
      "v_fmac_f32 v198, s53, v148\n"
      "v_and_b32 v148, 0xffff0000, v146\n"
      "v_fmac_f32 v199, s53, v148\n"
      "v_lshlrev_b32 v148, 16, v147\n"
      "v_fmac_f32_e64 v200, s53, v148\n"
      "v_and_b32 v148, 0xffff0000, v147\n"
      "v_fmac_f32 v201, s53, v148\n"
      "v_alignbyte_b32 v148, v175, v175, v163\n"
      "v_lshlrev_b32 v148, 16, v148\n"
      "v_fmac_f32_e64 v208, s53, v148\n"
      "v_max3_f32 v165, v140, v141, v142\n"
      "v_max3_f32 v163, v143, v184, v185\n"
      "v_max3_f32 v165, v165, v186, v187\n"
      "v_max3_f32 v163, v163, v198, v199\n"
      "v_max3_f32 v165, v165, v200, v201\n"
      "v_max3_f32 v165, v165, v163, v208\n"
      "v_mov_b32 v148, v165\n"
      "s_nop 1\n"
      "v_permlane16_swap_b32 v165, v148\n"
      "v_max_f32 v165, v165, v148\n"
      "v_mov_b32 v148, v165\n"
      "s_nop 1\n"
      "v_permlane32_swap_b32 v165, v148\n"
      "v_max_f32 v165, v165, v148\n"
      "v_sub_f32 v140, v140, v165\n"
      "v_sub_f32 v141, v141, v165\n"
      "v_sub_f32 v142, v142, v165\n"
      "v_sub_f32 v143, v143, v165\n"
      "v_sub_f32 v184, v184, v165\n"
      "v_sub_f32 v185, v185, v165\n"
      "v_sub_f32 v186, v186, v165\n"
      "v_sub_f32 v187, v187, v165\n"
      "v_sub_f32 v198, v198, v165\n"
      "v_sub_f32 v199, v199, v165\n"
      "v_sub_f32 v200, v200, v165\n"
      "v_sub_f32 v201, v201, v165\n"
      "v_sub_f32_e64 v208, v208, v165\n"
      "s_waitcnt lgkmcnt(0)\n"
      "s_nop 4\n"
      "s_branch L_back_6_%=\n"
      "L_rare_7_%=:\n"
      "s_nop 7\n"
      "s_waitcnt lgkmcnt(0)\n"
      "s_waitcnt vmcnt(0)\n"
      "v_subrev_u32 v120, 115456, v9\n"
      "v_bfe_u32 v103, v120, 4, 4\n"
      "v_mul_u32_u24 v103, 98, v103\n"
      "v_lshrrev_b32_e64 v120, 8, v120\n"
      "v_lshl_add_u32 v103, v120, 3, v103\n"
      "v_add_u32 v103, s77, v103\n"
      "v_and_b32 v102, 2, v103\n"
      "v_and_b32_e64 v103, -4, v103\n"
      "v_add_u32 v103, 113728, v103\n"
      "ds_read2_b32 v[170:171], v103 offset0:0 offset1:1\n"
      "ds_read2_b32 v[134:135], v103 offset0:8 offset1:9\n"
      "ds_read2_b32 v[136:137], v103 offset0:16 offset1:17\n"
      "ds_read2_b32 v[100:101], v103 offset0:2 offset1:10\n"
      "ds_read2_b32 v[172:173], v103 offset0:18 offset1:24\n"
      "s_waitcnt lgkmcnt(4)\n"
      "v_alignbyte_b32 v170, v171, v170, v102\n"
      "s_waitcnt lgkmcnt(1)\n"
      "v_alignbyte_b32 v171, v100, v171, v102\n"
      "v_lshlrev_b32 v120, 16, v170\n"
      "v_fmac_f32 v144, s53, v120\n"
      "v_and_b32 v120, 0xffff0000, v170\n"
      "v_fmac_f32 v145, s53, v120\n"
      "v_lshlrev_b32 v120, 16, v171\n"
      "v_fmac_f32_e64 v146, s53, v120\n"
      "v_and_b32 v120, 0xffff0000, v171\n"
      "v_fmac_f32 v147, s53, v120\n"
      "v_alignbyte_b32 v134, v135, v134, v102\n"
      "v_alignbyte_b32 v135, v101, v135, v102\n"
      "v_lshlrev_b32 v120, 16, v134\n"
      "v_fmac_f32 v130, s53, v120\n"
      "v_and_b32 v120, 0xffff0000, v134\n"
      "v_fmac_f32 v131, s53, v120\n"
      "v_lshlrev_b32 v120, 16, v135\n"
      "v_fmac_f32 v132, s53, v120\n"
      "v_and_b32 v120, 0xffff0000, v135\n"
      "v_fmac_f32 v133, s53, v120\n"
      "v_alignbyte_b32 v136, v137, v136, v102\n"
      "s_waitcnt lgkmcnt(0)\n"
      "v_alignbyte_b32 v137, v172, v137, v102\n"
      "v_lshlrev_b32 v120, 16, v136\n"
      "v_fmac_f32 v126, s53, v120\n"
      "v_and_b32 v120, 0xffff0000, v136\n"
      "v_fmac_f32 v127, s53, v120\n"
      "v_lshlrev_b32 v120, 16, v137\n"
      "v_fmac_f32 v128, s53, v120\n"
      "v_and_b32 v120, 0xffff0000, v137\n"
      "v_fmac_f32 v129, s53, v120\n"
      "v_alignbyte_b32 v120, v173, v173, v102\n"
      "v_lshlrev_b32 v120, 16, v120\n"
      "v_fmac_f32 v180, s53, v120\n"
      "v_max3_f32 v103, v144, v145, v146\n"
      "v_max3_f32 v102, v147, v130, v131\n"
      "v_max3_f32 v103, v103, v132, v133\n"
      "v_max3_f32 v102, v102, v126, v127\n"
      "v_max3_f32 v103, v103, v128, v129\n"
      "v_max3_f32 v103, v103, v102, v180\n"
      "v_mov_b32 v120, v103\n"
      "s_nop 1\n"
      "v_permlane16_swap_b32 v103, v120\n"
      "v_max_f32 v103, v103, v120\n"
      "v_mov_b32 v120, v103\n"
      "s_nop 1\n"
      "v_permlane32_swap_b32 v103, v120\n"
      "v_max_f32 v103, v103, v120\n"
      "v_sub_f32 v144, v144, v103\n"
      "v_sub_f32 v145, v145, v103\n"
      "v_sub_f32 v146, v146, v103\n"
      "v_sub_f32 v147, v147, v103\n"
      "v_sub_f32 v130, v130, v103\n"
      "v_sub_f32 v131, v131, v103\n"
      "v_sub_f32 v132, v132, v103\n"
      "v_sub_f32 v133, v133, v103\n"
      "v_sub_f32 v126, v126, v103\n"
      "v_sub_f32 v127, v127, v103\n"
      "v_sub_f32 v128, v128, v103\n"
      "v_sub_f32 v129, v129, v103\n"
      "v_sub_f32 v180, v180, v103\n"
      "s_waitcnt lgkmcnt(0)\n"
      "s_nop 4\n"
      "s_branch L_back_8_%=\n"
      "L_rare_9_%=:\n"
      "s_nop 7\n"
      "s_waitcnt lgkmcnt(0)\n"
      "s_waitcnt vmcnt(0)\n"
      "v_subrev_u32 v129, 115456, v9\n"
      "v_lshrrev_b32 v129, 3, v129\n"
      "v_min_u32 v129, 96, v129\n"
      "v_add_u32 v129, s77, v129\n"
      "v_add_u32 v129, 115296, v129\n"
      "ds_read_u16 v129, v129\n"
      "s_waitcnt lgkmcnt(0)\n"
      "v_lshlrev_b32_e64 v129, 16, v129\n"
      "v_fmac_f32 v148, s53, v129\n"
      "s_nop 1\n"
      "v_max_f32_dpp v128, v148, v148 row_ror:8 row_mask:0xf bank_mask:0xf\n"
      "s_nop 1\n"
      "v_max_f32_dpp v128, v128, v128 row_ror:4 row_mask:0xf bank_mask:0xf\n"
      "s_nop 1\n"
      "v_max_f32_dpp v128, v128, v128 row_ror:2 row_mask:0xf bank_mask:0xf\n"
      "s_nop 1\n"
      "v_max_f32_dpp v128, v128, v128 row_ror:1 row_mask:0xf bank_mask:0xf\n"
      "s_nop 1\n"
      "v_max_f32_dpp v128, v128, v128 row_bcast:15 row_mask:0xa bank_mask:0xf\n"
      "s_nop 1\n"
      "v_max_f32_dpp v128, v128, v128 row_bcast:31 row_mask:0xc bank_mask:0xf\n"
      "s_nop 0\n"
      "v_readlane_b32 s72, v128, 63\n"
      "s_nop 1\n"
      "v_subrev_f32 v148, s72, v148\n"
      "s_waitcnt lgkmcnt(0)\n"
      "s_nop 4\n"
      "s_branch L_back_10_%=\n"
      "L_rare_27_%=:\n"
      "s_nop 7\n"
      "s_waitcnt lgkmcnt(0)\n"
      "s_bitcmp1_b32 s85, 31\n"
      "s_cselect_b32 s75, 100270464, 0\n"
      "s_bitcmp1_b32 s85, 30\n"
      "s_cselect_b32 s76, 196224, 0\n"
      "s_and_b32 s74, s85, 0x3fffffff\n"
      "s_cmp_ge_u32 s85, 0x40000000\n"
      "s_cselect_b32 s74, s74, 0\n"
      "s_cselect_b32 s85, 0, s85\n"
      "s_sub_u32 s74, s74, s69\n"
      "s_add_u32 s69, s69, s74\n"
      "s_sub_u32 s75, s75, s70\n"
      "s_add_u32 s70, s70, s75\n"
      "s_sub_u32 s76, s76, s71\n"
      "s_add_u32 s71, s71, s76\n"
      "v_add_u32 v20, s74, v20\n"
      "v_add_u32 v21, s74, v21\n"
      "v_add_u32 v22, s74, v22\n"
      "v_add_u32 v23, s74, v23\n"
      "v_add_u32 v25, s74, v25\n"
      "s_cmp_eq_u32 s75, 0\n"
      "s_cbranch_scc1 L_ax_25_%=\n"
      "v_bfe_i32 v104, v27, 10, 1\n"
      "v_and_b32 v104, s75, v104\n"
      "v_sub_u32 v20, v20, v104\n"
      "v_bfe_i32 v104, v27, 12, 1\n"
      "v_and_b32 v104, s75, v104\n"
      "v_sub_u32 v21, v21, v104\n"
      "v_bfe_i32 v104, v27, 14, 1\n"
      "v_and_b32 v104, s75, v104\n"
      "v_sub_u32 v22, v22, v104\n"
      "v_bfe_i32 v104, v27, 16, 1\n"
      "v_and_b32 v104, s75, v104\n"
      "v_sub_u32 v23, v23, v104\n"
      "v_bfe_i32 v104, v27, 18, 1\n"
      "v_and_b32 v104, s75, v104\n"
      "v_sub_u32 v25, v25, v104\n"
      "L_ax_25_%=:\n"
      "s_cmp_eq_u32 s76, 0\n"
      "s_cbranch_scc1 L_ax_26_%=\n"
      "v_bfe_i32 v104, v27, 11, 1\n"
      "v_and_b32 v104, s76, v104\n"
      "v_sub_u32_e64 v20, v20, v104\n"
      "v_bfe_i32 v104, v27, 13, 1\n"
      "v_and_b32 v104, s76, v104\n"
      "v_sub_u32 v21, v21, v104\n"
      "v_bfe_i32 v104, v27, 15, 1\n"
      "v_and_b32 v104, s76, v104\n"
      "v_sub_u32 v22, v22, v104\n"
      "v_bfe_i32 v104, v27, 17, 1\n"
      "v_and_b32 v104, s76, v104\n"
      "v_sub_u32 v23, v23, v104\n"
      "v_bfe_i32 v104, v27, 19, 1\n"
      "v_and_b32 v104, s76, v104\n"
      "v_sub_u32 v25, v25, v104\n"
      "L_ax_26_%=:\n"
      "s_or_b32 s48, s66, s69\n"
      "s_cmp_lg_u32 s48, 0\n"
      "s_cselect_b32 s48, 0x40000000, 0\n"
      "s_waitcnt lgkmcnt(0)\n"
      "s_nop 4\n"
      "s_branch L_back_28_%=\n"
      "L_rare_31_%=:\n"
      "s_nop 7\n"
      "s_waitcnt lgkmcnt(0)\n"
      "s_bitcmp1_b32 s85, 31\n"
      "s_cselect_b32 s75, 100270464, 0\n"
      "s_bitcmp1_b32 s85, 30\n"
      "s_cselect_b32 s76, 196224, 0\n"
      "s_and_b32 s74, s85, 0x3fffffff\n"
      "s_cmp_ge_u32 s85, 0x40000000\n"
      "s_cselect_b32 s74, s74, 0\n"
      "s_cselect_b32 s85, 0, s85\n"
      "s_sub_u32 s74, s74, s69\n"
      "s_add_u32 s69, s69, s74\n"
      "s_sub_u32 s75, s75, s70\n"
      "s_add_u32 s70, s70, s75\n"
      "s_sub_u32 s76, s76, s71\n"
      "s_add_u32 s71, s71, s76\n"
      "v_add_u32 v20, s74, v20\n"
      "v_add_u32 v21, s74, v21\n"
      "v_add_u32 v22, s74, v22\n"
      "v_add_u32 v23, s74, v23\n"
      "v_add_u32 v25, s74, v25\n"
      "s_cmp_eq_u32 s75, 0\n"
      "s_cbranch_scc1 L_ax_29_%=\n"
      "v_bfe_i32 v134, v27, 10, 1\n"
      "v_and_b32 v134, s75, v134\n"
      "v_sub_u32 v20, v20, v134\n"
      "v_bfe_i32 v134, v27, 12, 1\n"
      "v_and_b32 v134, s75, v134\n"
      "v_sub_u32 v21, v21, v134\n"
      "v_bfe_i32 v134, v27, 14, 1\n"
      "v_and_b32 v134, s75, v134\n"
      "v_sub_u32 v22, v22, v134\n"
      "v_bfe_i32 v134, v27, 16, 1\n"
      "v_and_b32 v134, s75, v134\n"
      "v_sub_u32 v23, v23, v134\n"
      "v_bfe_i32 v134, v27, 18, 1\n"
      "v_and_b32 v134, s75, v134\n"
      "v_sub_u32 v25, v25, v134\n"
      "L_ax_29_%=:\n"
      "s_cmp_eq_u32 s76, 0\n"
      "s_cbranch_scc1 L_ax_30_%=\n"
      "v_bfe_i32 v134, v27, 11, 1\n"
      "v_and_b32 v134, s76, v134\n"
      "v_sub_u32 v20, v20, v134\n"
      "v_bfe_i32 v134, v27, 13, 1\n"
      "v_and_b32 v134, s76, v134\n"
      "v_sub_u32 v21, v21, v134\n"
      "v_bfe_i32 v134, v27, 15, 1\n"
      "v_and_b32 v134, s76, v134\n"
      "v_sub_u32 v22, v22, v134\n"
      "v_bfe_i32 v134, v27, 17, 1\n"
      "v_and_b32 v134, s76, v134\n"
      "v_sub_u32 v23, v23, v134\n"
      "v_bfe_i32 v134, v27, 19, 1\n"
      "v_and_b32 v134, s76, v134\n"
      "v_sub_u32 v25, v25, v134\n"
      "L_ax_30_%=:\n"
      "s_or_b32 s48, s66, s69\n"
      "s_cmp_lg_u32 s48, 0\n"
      "s_cselect_b32 s48, 0x40000000, 0\n"
      "s_waitcnt lgkmcnt(0)\n"
      "s_nop 4\n"
      "s_branch L_back_32_%=\n"
      "L_rare_35_%=:\n"
      "s_nop 7\n"
      "s_waitcnt lgkmcnt(0)\n"
      "s_bitcmp1_b32 s46, 31\n"
      "s_cselect_b32 s75, 100270464, 0\n"
      "s_bitcmp1_b32 s46, 30\n"
      "s_cselect_b32 s76, 196224, 0\n"
      "s_and_b32 s74, s46, 0x3fffffff\n"
      "s_cmp_ge_u32 s46, 0x40000000\n"
      "s_cselect_b32 s74, s74, 0\n"
      "s_cselect_b32 s46, 0, s46\n"
      "s_sub_u32 s74, s74, s69\n"
      "s_add_u32 s69, s69, s74\n"
      "s_sub_u32 s75, s75, s70\n"
      "s_add_u32 s70, s70, s75\n"
      "s_sub_u32 s76, s76, s71\n"
      "s_add_u32 s71, s71, s76\n"
      "v_add_u32 v20, s74, v20\n"
      "v_add_u32 v21, s74, v21\n"
      "v_add_u32 v22, s74, v22\n"
      "v_add_u32 v23, s74, v23\n"
      "v_add_u32 v25, s74, v25\n"
      "s_cmp_eq_u32 s75, 0\n"
      "s_cbranch_scc1 L_ax_33_%=\n"
      "v_bfe_i32 v126, v27, 10, 1\n"
      "v_and_b32 v126, s75, v126\n"
      "v_sub_u32 v20, v20, v126\n"
      "v_bfe_i32 v126, v27, 12, 1\n"
      "v_and_b32 v126, s75, v126\n"
      "v_sub_u32 v21, v21, v126\n"
      "v_bfe_i32 v126, v27, 14, 1\n"
      "v_and_b32 v126, s75, v126\n"
      "v_sub_u32 v22, v22, v126\n"
      "v_bfe_i32 v126, v27, 16, 1\n"
      "v_and_b32 v126, s75, v126\n"
      "v_sub_u32 v23, v23, v126\n"
      "v_bfe_i32 v126, v27, 18, 1\n"
      "v_and_b32 v126, s75, v126\n"
      "v_sub_u32 v25, v25, v126\n"
      "L_ax_33_%=:\n"
      "s_cmp_eq_u32 s76, 0\n"
      "s_cbranch_scc1 L_ax_34_%=\n"
      "v_bfe_i32 v126, v27, 11, 1\n"
      "v_and_b32 v126, s76, v126\n"
      "v_sub_u32 v20, v20, v126\n"
      "v_bfe_i32 v126, v27, 13, 1\n"
      "v_and_b32 v126, s76, v126\n"
      "v_sub_u32 v21, v21, v126\n"
      "v_bfe_i32 v126, v27, 15, 1\n"
      "v_and_b32 v126, s76, v126\n"
      "v_sub_u32 v22, v22, v126\n"
      "v_bfe_i32 v126, v27, 17, 1\n"
      "v_and_b32 v126, s76, v126\n"
      "v_sub_u32 v23, v23, v126\n"
      "v_bfe_i32 v126, v27, 19, 1\n"
      "v_and_b32 v126, s76, v126\n"
      "v_sub_u32 v25, v25, v126\n"
      "L_ax_34_%=:\n"
      "s_or_b32 s48, s66, s69\n"
      "s_cmp_lg_u32 s48, 0\n"
      "s_cselect_b32 s48, 0x40000000, 0\n"
      "s_waitcnt lgkmcnt(0)\n"
      "s_nop 4\n"
      "s_branch L_back_36_%=\n"
      "L_rare_39_%=:\n"
      "s_nop 7\n"
      "s_waitcnt lgkmcnt(0)\n"
      "s_bitcmp1_b32 s46, 31\n"
      "s_cselect_b32 s75, 100270464, 0\n"
      "s_bitcmp1_b32 s46, 30\n"
      "s_cselect_b32 s76, 196224, 0\n"
      "s_and_b32 s74, s46, 0x3fffffff\n"
      "s_cmp_ge_u32 s46, 0x40000000\n"
      "s_cselect_b32 s74, s74, 0\n"
      "s_cselect_b32 s46, 0, s46\n"
      "s_sub_u32 s74, s74, s69\n"
      "s_add_u32 s69, s69, s74\n"
      "s_sub_u32 s75, s75, s70\n"
      "s_add_u32 s70, s70, s75\n"
      "s_sub_u32 s76, s76, s71\n"
      "s_add_u32 s71, s71, s76\n"
      "v_add_u32 v20, s74, v20\n"
      "v_add_u32 v21, s74, v21\n"
      "v_add_u32 v22, s74, v22\n"
      "v_add_u32 v23, s74, v23\n"
      "v_add_u32 v25, s74, v25\n"
      "s_cmp_eq_u32 s75, 0\n"
      "s_cbranch_scc1 L_ax_37_%=\n"
      "v_bfe_i32 v209, v27, 10, 1\n"
      "v_and_b32 v209, s75, v209\n"
      "v_sub_u32 v20, v20, v209\n"
      "v_bfe_i32 v209, v27, 12, 1\n"
      "v_and_b32 v209, s75, v209\n"
      "v_sub_u32 v21, v21, v209\n"
      "v_bfe_i32 v209, v27, 14, 1\n"
      "v_and_b32 v209, s75, v209\n"
      "v_sub_u32 v22, v22, v209\n"
      "v_bfe_i32 v209, v27, 16, 1\n"
      "v_and_b32 v209, s75, v209\n"
      "v_sub_u32 v23, v23, v209\n"
      "v_bfe_i32 v209, v27, 18, 1\n"
      "v_and_b32 v209, s75, v209\n"
      "v_sub_u32 v25, v25, v209\n"
      "L_ax_37_%=:\n"
      "s_cmp_eq_u32 s76, 0\n"
      "s_cbranch_scc1 L_ax_38_%=\n"
      "v_bfe_i32 v209, v27, 11, 1\n"
      "v_and_b32 v209, s76, v209\n"
      "v_sub_u32 v20, v20, v209\n"
      "v_bfe_i32 v209, v27, 13, 1\n"
      "v_and_b32 v209, s76, v209\n"
      "v_sub_u32 v21, v21, v209\n"
      "v_bfe_i32 v209, v27, 15, 1\n"
      "v_and_b32 v209, s76, v209\n"
      "v_sub_u32 v22, v22, v209\n"
      "v_bfe_i32 v209, v27, 17, 1\n"
      "v_and_b32 v209, s76, v209\n"
      "v_sub_u32 v23, v23, v209\n"
      "v_bfe_i32 v209, v27, 19, 1\n"
      "v_and_b32 v209, s76, v209\n"
      "v_sub_u32 v25, v25, v209\n"
      "L_ax_38_%=:\n"
      "s_or_b32 s48, s66, s69\n"
      "s_cmp_lg_u32 s48, 0\n"
      "s_cselect_b32 s48, 0x40000000, 0\n"
      "s_waitcnt lgkmcnt(0)\n"
      "s_nop 4\n"
      "s_branch L_back_40_%=\n"
      "L_cend_%=:\n"
      :
      : "s"(__builtin_amdgcn_kernarg_segment_ptr()), "s"(wave), "s"(blockIdx.x), "s"(CT_TAB), "s"(lds0)
      : "v0","v1","v2","v3","v4","v5","v6","v7","v8","v9","v10","v11","v12","v13","v14","v15","v16","v17","v18","v19","v20","v21","v22","v23","v24","v25","v26","v27","v28","v29","v30","v31","v32","v33","v34","v35","v36","v37","v38","v39","v40","v41","v42","v43","v44","v45","v46","v47","v48","v49","v50","v51","v52","v53","v54","v55","v56","v57","v58","v59","v60","v61","v62","v63","v64","v65","v66","v67","v68","v69","v70","v71","v72","v73","v74","v75","v76","v77","v78","v79","v80","v81","v82","v83","v84","v85","v86","v87","v88","v89","v90","v91","v92","v93","v94","v95","v96","v97","v98","v99","v100","v101","v102","v103","v104","v105","v106","v107","v108","v109","v110","v111","v112","v113","v114","v115","v116","v117","v118","v119","v120","v121","v122","v123","v124","v125","v126","v127","v128","v129","v130","v131","v132","v133","v134","v135","v136","v137","v138","v139","v140","v141","v142","v143","v144","v145","v146","v147","v148","v149","v150","v151","v152","v153","v154","v155","v156","v157","v158","v159","v160","v161","v162","v163","v164","v165","v166","v167","v168","v169","v170","v171","v172","v173","v174","v175","v176","v177","v178","v179","v180","v181","v182","v183","v184","v185","v186","v187","v188","v189","v190","v191","v192","v193","v194","v195","v196","v197","v198","v199","v200","v201","v202","v203","v204","v205","v206","v207","v208","v209","v210","v211","v212","v213","v214","v215","v216","v217","v218","v219","v220","v221","v222","v223","v224","v225","v226","v227","v228","v229","v230","v231","v232","v233","v234","v235","v236","v237","v238","v239","v240","v241","v242","v243","v244","v245","v246","v247","v248","v249","v250","v251","v252","v253","v254","v255","a0","a1","a2","a3","a4","a5","a6","a7","a8","a9","a10","a11","a12","a13","a14","a15","a16","a17","a18","a19","a20","a21","a22","a23","a24","a25","a26","a27","a28","a29","a30","a31","a32","a33","a34","a35","a36","a37","a38","a39","a40","a41","a42","a43","a44","a45","a46","a47","a48","a49","a50","a51","a52","a53","a54","a55","a56","a57","a58","a59","a60","a61","a62","a63","a64","a65","a66","a67","a68","a69","a70","a71","a72","a73","a74","a75","a76","a77","a78","a79","a80","a81","a82","a83","a84","a85","a86","a87","a88","a89","a90","a91","a92","a93","a94","a95","a96","a97","a98","a99","a100","a101","a102","a103","a104","a105","a106","a107","a108","a109","a110","a111","a112","a113","a114","a115","a116","a117","a118","a119","a120","a121","a122","a123","a124","a125","a126","a127","a128","a129","a130","a131","a132","a133","a134","a135","a136","a137","a138","a139","a140","a141","a142","a143","a144","a145","a146","a147","a148","a149","a150","a151","a152","a153","a154","a155","a156","a157","a158","a159","a160","a161","a162","a163","a164","a165","a166","a167","a168","a169","a170","a171","a172","a173","a174","a175","a176","a177","a178","a179","a180","a181","a182","a183","a184","a185","a186","a187","a188","a189","a190","a191","a192","a193","a194","a195","a196","a197","a198","a199","a200","a201","a202","a203","a204","a205","a206","a207","a208","a209","a210","a211","a212","a213","a214","a215","a216","a217","a218","a219","a220","a221","a222","a223","a224","a225","a226","a227","a228","a229","a230","a231","a232","a233","a234","a235","a236","a237","a238","a239","a240","a241","a242","a243","a244","a245","a246","a247","a248","a249","a250","a251","a252","a253","a254","a255","s16","s17","s18","s19","s20","s21","s22","s23","s24","s25","s26","s27","s28","s29","s30","s31","s33","s34","s35","s36","s37","s38","s39","s40","s41","s42","s43","s44","s45","s46","s47","s48","s49","s50","s51","s52","s53","s54","s55","s56","s57","s58","s59","s60","s61","s62","s63","s64","s65","s66","s67","s68","s69","s70","s71","s72","s73","s74","s75","s76","s77","s78","s79","s80","s81","s82","s83","s84","s85","s86","s87","s88","s89","s90","s91","s92","s93","s94","s95","s96","s97","s98","s99","s100","s101", "vcc", "scc", "memory");
}

}

extern "C" void kernel_launch(void* const* d_in, const int* in_sizes, int n_in, void* d_out, int out_size, void* d_ws,
                              size_t ws_size, hipStream_t stream) {
  const uint16_t* x = (const uint16_t*)d_in[0];
  const uint16_t* wqkv = (const uint16_t*)d_in[1];
  const uint16_t* wo = (const uint16_t*)d_in[2];
  const uint16_t* ls = (const uint16_t*)d_in[3];
  const uint16_t* w1 = (const uint16_t*)d_in[4];
  const uint16_t* w2 = (const uint16_t*)d_in[5];
  const uint16_t* rc = (const uint16_t*)d_in[6];
  const int* ridx = (const int*)d_in[7];
  const uint16_t* mask = (const uint16_t*)d_in[8];
  swin::swin_main<<<256, 256, 0, stream>>>(x, mask, (char*)d_ws, (uint16_t*)d_out, wqkv, wo, ls, w1, w2, rc, ridx);
}
```

```cpp
#include <hip/hip_runtime.h>
#include <hip/hip_bf16.h>
#include <stdint.h>

namespace swin {

constexpr unsigned CT_WORDS = 8192u;
constexpr unsigned LDS_BYTES = 163840u;

__device__ __attribute__((aligned(256))) const unsigned CT_TAB[CT_WORDS] = {0x4800u,0x4820u,0x4840u,0x4800u,0x4808u,0x4828u,0x4848u,0x4810u,0x4810u,0x4830u,0x4850u,0x4820u,0x4818u,0x4838u,0x4858u,0x4830u,0x4880u,0x48a0u,0x48c0u,0x4880u,0x4888u,0x48a8u,0x48c8u,0x4890u,0x4890u,0x48b0u,0x48d0u,0x48a0u,0x4898u,0x48b8u,0x48d8u,0x48b0u,0x4920u,0x4900u,0x4960u,0x4920u,0x4928u,0x4908u,0x4968u,0x4930u,0x4930u,0x4910u,0x4970u,0x4900u,0x4938u,0x4918u,0x4978u,0x4910u,0x49a0u,0x4980u,0x49e0u,0x49a0u,0x49a8u,0x4988u,0x49e8u,0x49b0u,0x49b0u,0x4990u,0x49f0u,0x4980u,0x49b8u,0x4998u,0x49f8u,0x4990u,0x4c40u,0x4c60u,0x4c00u,0x4a00u,0x4c48u,0x4c68u,0x4c08u,0x4a10u,0x4c50u,0x4c70u,0x4c10u,0x4a20u,0x4c58u,0x4c78u,0x4c18u,0x4a30u,0x4cc0u,0x4ce0u,0x4c80u,0x4a80u,0x4cc8u,0x4ce8u,0x4c88u,0x4a90u,0x4cd0u,0x4cf0u,0x4c90u,0x4aa0u,0x4cd8u,0x4cf8u,0x4c98u,0x4ab0u,0x4d60u,0x4d40u,0x4d20u,0x4b20u,0x4d68u,0x4d48u,0x4d28u,0x4b30u,0x4d70u,0x4d50u,0x4d30u,0x4b00u,0x4d78u,0x4d58u,0x4d38u,0x4b10u,0x4de0u,0x4dc0u,0x4da0u,0x4ba0u,0x4de8u,0x4dc8u,0x4da8u,0x4bb0u,0x4df0u,0x4dd0u,0x4db0u,0x4b80u,0x4df8u,0x4dd8u,0x4db8u,0x4b90u,0x5000u,0x5020u,0x5040u,0x4c40u,0x5008u,0x5028u,0x5048u,0x4c50u,0x5010u,0x5030u,0x5050u,0x4c60u,0x5018u,0x5038u,0x5058u,0x4c70u,0x5080u,0x50a0u,0x50c0u,0x4cc0u,0x5088u,0x50a8u,0x50c8u,0x4cd0u,0x5090u,0x50b0u,0x50d0u,0x4ce0u,0x5098u,0x50b8u,0x50d8u,0x4cf0u,0x5120u,0x5100u,0x5160u,0x4d60u,0x5128u,0x5108u,0x5168u,0x4d70u,0x5130u,0x5110u,0x5170u,0x4d40u,0x5138u,0x5118u,0x5178u,0x4d50u,0x51a0u,0x5180u,0x51e0u,0x4de0u,0x51a8u,0x5188u,0x51e8u,0x4df0u,0x51b0u,0x5190u,0x51f0u,0x4dc0u,0x51b8u,0x5198u,0x51f8u,0x4dd0u,0x5440u,0x5460u,0x5400u,0x4e40u,0x5448u,0x5468u,0x5408u,0x4e50u,0x5450u,0x5470u,0x5410u,0x4e60u,0x5458u,0x5478u,0x5418u,0x4e70u,0x54c0u,0x54e0u,0x5480u,0x4ec0u,0x54c8u,0x54e8u,0x5488u,0x4ed0u,0x54d0u,0x54f0u,0x5490u,0x4ee0u,0x54d8u,0x54f8u,0x5498u,0x4ef0u,0x5560u,0x5540u,0x5520u,0x4f60u,0x5568u,0x5548u,0x5528u,0x4f70u,0x5570u,0x5550u,0x5530u,0x4f40u,0x5578u,0x5558u,0x5538u,0x4f50u,0x55e0u,0x55c0u,0x55a0u,0x4fe0u,0x55e8u,0x55c8u,0x55a8u,0x4ff0u,0x55f0u,0x55d0u,0x55b0u,0x4fc0u,0x55f8u,0x55d8u,0x55b8u,0x4fd0u,0x4840u,0x1e800u,0x1eb00u,0x0u,0x4848u,0x1ec90u,0x1ef90u,0x10u,0x4850u,0x1f120u,0x1f420u,0x20u,0x4858u,0x1f5b0u,0x1f8b0u,0x30u,0x48c0u,0x1fa40u,0x1fd40u,0x40u,0x48c8u,0x1fed0u,0x201d0u,0x50u,0x48d0u,0x20360u,0x20660u,0x60u,0x48d8u,0x207f0u,0x20af0u,0x70u,0x4960u,0x20c80u,0x20f80u,0x180u,0x4968u,0x21110u,0x21410u,0x190u,0x4970u,0x215a0u,0x218a0u,0x1a0u,0x4978u,0x21a30u,0x21d30u,0x1b0u,0x49e0u,0x21ec0u,0x221c0u,0x1c0u,0x49e8u,0x22350u,0x22650u,0x1d0u,0x49f0u,0x227e0u,0x22ae0u,0x1e0u,0x49f8u,0x22c70u,0x22f70u,0x1f0u,0x4a40u,0x1e830u,0x1eb08u,0x320u,0x4a48u,0x1ecc0u,0x1ef98u,0x330u,0x4a50u,0x1f150u,0x1f428u,0x300u,0x4a58u,0x1f5e0u,0x1f8b8u,0x310u,0x4ac0u,0x1fa70u,0x1fd48u,0x360u,0x4ac8u,0x1ff00u,0x201d8u,0x370u,0x4ad0u,0x20390u,0x20668u,0x340u,0x4ad8u,0x20820u,0x20af8u,0x350u,0x4b60u,0x20cb0u,0x20f88u,0x4a0u,0x4b68u,0x21140u,0x21418u,0x4b0u,0x4b70u,0x215d0u,0x218a8u,0x480u,0x4b78u,0x21a60u,0x21d38u,0x490u,0x4be0u,0x21ef0u,0x221c8u,0x4e0u,0x4be8u,0x22380u,0x22658u,0x4f0u,0x4bf0u,0x22810u,0x22ae8u,0x4c0u,0x4bf8u,0x22ca0u,0x22f78u,0x4d0u,0x4c00u,0x1e860u,0x1eb10u,0x600u,0x4c08u,0x1ecf0u,0x1efa0u,0x610u,0x4c10u,0x1f180u,0x1f430u,0x620u,0x4c18u,0x1f610u,0x1f8c0u,0x630u,0x4c80u,0x1faa0u,0x1fd50u,0x640u,0x4c88u,0x1ff30u,0x201e0u,0x650u,0x4c90u,0x203c0u,0x20670u,0x660u,0x4c98u,0x20850u,0x20b00u,0x670u,0x4d20u,0x20ce0u,0x20f90u,0x780u,0x4d28u,0x21170u,0x21420u,0x790u,0x4d30u,0x21600u,0x218b0u,0x7a0u,0x4d38u,0x21a90u,0x21d40u,0x7b0u,0x4da0u,0x21f20u,0x221d0u,0x7c0u,0x4da8u,0x223b0u,0x22660u,0x7d0u,0x4db0u,0x22840u,0x22af0u,0x7e0u,0x4db8u,0x22cd0u,0x22f80u,0x7f0u,0x4e00u,0x1e890u,0x1eb18u,0x920u,0x4e08u,0x1ed20u,0x1efa8u,0x930u,0x4e10u,0x1f1b0u,0x1f438u,0x900u,0x4e18u,0x1f640u,0x1f8c8u,0x910u,0x4e80u,0x1fad0u,0x1fd58u,0x960u,0x4e88u,0x1ff60u,0x201e8u,0x970u,0x4e90u,0x203f0u,0x20678u,0x940u,0x4e98u,0x20880u,0x20b08u,0x950u,0x4f20u,0x20d10u,0x20f98u,0xaa0u,0x4f28u,0x211a0u,0x21428u,0xab0u,0x4f30u,0x21630u,0x218b8u,0xa80u,0x4f38u,0x21ac0u,0x21d48u,0xa90u,0x4fa0u,0x21f50u,0x221d8u,0xae0u,0x4fa8u,0x223e0u,0x22668u,0xaf0u,0x4fb0u,0x22870u,0x22af8u,0xac0u,0x4fb8u,0x22d00u,0x22f88u,0xad0u,0x0u,0x40u,0x9000u,0x9000u,0x80u,0xc0u,0x9010u,0x9010u,0x110u,0x150u,0x9020u,0x9020u,0x190u,0x1d0u,0x9030u,0x9030u,0x220u,0x260u,0x9040u,0x9040u,0x2a0u,0x2e0u,0x9050u,0x9050u,0x330u,0x370u,0x9060u,0x9060u,0x3b0u,0x3f0u,0x9070u,0x9070u,0x440u,0x400u,0x9080u,0x9080u,0x4c0u,0x480u,0x9090u,0x9090u,0x550u,0x510u,0x90a0u,0x90a0u,0x5d0u,0x590u,0x90b0u,0x90b0u,0x660u,0x620u,0x90c0u,0x90c0u,0x6e0u,0x6a0u,0x90d0u,0x90d0u,0x770u,0x730u,0x90e0u,0x90e0u,0x7f0u,0x7b0u,0x90f0u,0x90f0u,0x10u,0x50u,0x9300u,0x9300u,0x90u,0xd0u,0x9310u,0x9310u,0x100u,0x140u,0x9320u,0x9320u,0x180u,0x1c0u,0x9330u,0x9330u,0x230u,0x270u,0x9340u,0x9340u,0x2b0u,0x2f0u,0x9350u,0x9350u,0x320u,0x360u,0x9360u,0x9360u,0x3a0u,0x3e0u,0x9370u,0x9370u,0x450u,0x410u,0x9380u,0x9380u,0x4d0u,0x490u,0x9390u,0x9390u,0x540u,0x500u,0x93a0u,0x93a0u,0x5c0u,0x580u,0x93b0u,0x93b0u,0x670u,0x630u,0x93c0u,0x93c0u,0x6f0u,0x6b0u,0x93d0u,0x93d0u,0x760u,0x720u,0x93e0u,0x93e0u,0x7e0u,0x7a0u,0x93f0u,0x93f0u,0x20u,0x60u,0x9600u,0x9600u,0xa0u,0xe0u,0x9610u,0x9610u,0x130u,0x170u,0x9620u,0x9620u,0x1b0u,0x1f0u,0x9630u,0x9630u,0x200u,0x240u,0x9640u,0x9640u,0x280u,0x2c0u,0x9650u,0x9650u,0x310u,0x350u,0x9660u,0x9660u,0x390u,0x3d0u,0x9670u,0x9670u,0x460u,0x420u,0x9680u,0x9680u,0x4e0u,0x4a0u,0x9690u,0x9690u,0x570u,0x530u,0x96a0u,0x96a0u,0x5f0u,0x5b0u,0x96b0u,0x96b0u,0x640u,0x600u,0x96c0u,0x96c0u,0x6c0u,0x680u,0x96d0u,0x96d0u,0x750u,0x710u,0x96e0u,0x96e0u,0x7d0u,0x790u,0x96f0u,0x96f0u,0x30u,0x70u,0x9900u,0x9900u,0xb0u,0xf0u,0x9910u,0x9910u,0x120u,0x160u,0x9920u,0x9920u,0x1a0u,0x1e0u,0x9930u,0x9930u,0x210u,0x250u,0x9940u,0x9940u,0x290u,0x2d0u,0x9950u,0x9950u,0x300u,0x340u,0x9960u,0x9960u,0x380u,0x3c0u,0x9970u,0x9970u,0x470u,0x430u,0x9980u,0x9980u,0x4f0u,0x4b0u,0x9990u,0x9990u,0x560u,0x520u,0x99a0u,0x99a0u,0x5e0u,0x5a0u,0x99b0u,0x99b0u,0x650u,0x610u,0x99c0u,0x99c0u,0x6d0u,0x690u,0x99d0u,0x99d0u,0x740u,0x700u,0x99e0u,0x99e0u,0x7c0u,0x780u,0x99f0u,0x99f0u,0x9c00u,0x12000u,0x12040u,0x12000u,0x9c10u,0x12190u,0x121d0u,0x12010u,0x9c20u,0x12320u,0x12360u,0x12020u,0x9c30u,0x124b0u,0x124f0u,0x12030u,0x9c40u,0x12640u,0x12600u,0x12040u,0x9c50u,0x127d0u,0x12790u,0x12050u,0x9c60u,0x12960u,0x12920u,0x12060u,0x9c70u,0x12af0u,0x12ab0u,0x12070u,0x9c80u,0x12c00u,0x12c40u,0x12080u,0x9c90u,0x12d90u,0x12dd0u,0x12090u,0x9ca0u,0x12f20u,0x12f60u,0x120a0u,0x9cb0u,0x130b0u,0x130f0u,0x120b0u,0x9cc0u,0x13240u,0x13200u,0x120c0u,0x9cd0u,0x133d0u,0x13390u,0x120d0u,0x9ce0u,0x13560u,0x13520u,0x120e0u,0x9cf0u,0x136f0u,0x136b0u,0x120f0u,0x9f00u,0x12010u,0x12048u,0x12100u,0x9f10u,0x12180u,0x121d8u,0x12110u,0x9f20u,0x12330u,0x12368u,0x12120u,0x9f30u,0x124a0u,0x124f8u,0x12130u,0x9f40u,0x12650u,0x12608u,0x12140u,0x9f50u,0x127c0u,0x12798u,0x12150u,0x9f60u,0x12970u,0x12928u,0x12160u,0x9f70u,0x12ae0u,0x12ab8u,0x12170u,0x9f80u,0x12c10u,0x12c48u,0x12180u,0x9f90u,0x12d80u,0x12dd8u,0x12190u,0x9fa0u,0x12f30u,0x12f68u,0x121a0u,0x9fb0u,0x130a0u,0x130f8u,0x121b0u,0x9fc0u,0x13250u,0x13208u,0x121c0u,0x9fd0u,0x133c0u,0x13398u,0x121d0u,0x9fe0u,0x13570u,0x13528u,0x121e0u,0x9ff0u,0x136e0u,0x136b8u,0x121f0u,0xa200u,0x12020u,0x12050u,0x12200u,0xa210u,0x121b0u,0x121c0u,0x12210u,0xa220u,0x12300u,0x12370u,0x12220u,0xa230u,0x12490u,0x124e0u,0x12230u,0xa240u,0x12660u,0x12610u,0x12240u,0xa250u,0x127f0u,0x12780u,0x12250u,0xa260u,0x12940u,0x12930u,0x12260u,0xa270u,0x12ad0u,0x12aa0u,0x12270u,0xa280u,0x12c20u,0x12c50u,0x12280u,0xa290u,0x12db0u,0x12dc0u,0x12290u,0xa2a0u,0x12f00u,0x12f70u,0x122a0u,0xa2b0u,0x13090u,0x130e0u,0x122b0u,0xa2c0u,0x13260u,0x13210u,0x122c0u,0xa2d0u,0x133f0u,0x13380u,0x122d0u,0xa2e0u,0x13540u,0x13530u,0x122e0u,0xa2f0u,0x136d0u,0x136a0u,0x122f0u,0xa500u,0x12030u,0x12058u,0x12300u,0xa510u,0x121a0u,0x121c8u,0x12310u,0xa520u,0x12310u,0x12378u,0x12320u,0xa530u,0x12480u,0x124e8u,0x12330u,0xa540u,0x12670u,0x12618u,0x12340u,0xa550u,0x127e0u,0x12788u,0x12350u,0xa560u,0x12950u,0x12938u,0x12360u,0xa570u,0x12ac0u,0x12aa8u,0x12370u,0xa580u,0x12c30u,0x12c58u,0x12380u,0xa590u,0x12da0u,0x12dc8u,0x12390u,0xa5a0u,0x12f10u,0x12f78u,0x123a0u,0xa5b0u,0x13080u,0x130e8u,0x123b0u,0xa5c0u,0x13270u,0x13218u,0x123c0u,0xa5d0u,0x133e0u,0x13388u,0x123d0u,0xa5e0u,0x13550u,0x13538u,0x123e0u,0xa5f0u,0x136c0u,0x136a8u,0x123f0u,0x4800u,0x1c300u,0x1e800u,0x1eb00u,0x4880u,0x1c310u,0x100000u,0x1eb02u,0x4910u,0x1c320u,0x100000u,0x1eb04u,0x4990u,0x1c330u,0x100000u,0x1eb06u,0x4a20u,0x1c340u,0x1e800u,0x1eb08u,0x4aa0u,0x1c350u,0x100000u,0x1eb0au,0x4b30u,0x1c360u,0x100000u,0x1eb0cu,0x4bb0u,0x1c370u,0x100000u,0x1eb0eu,0x4c40u,0x1c380u,0x1e800u,0x1eb10u,0x4cc0u,0x1c390u,0x100000u,0x1eb12u,0x4d50u,0x1c3a0u,0x100000u,0x1eb14u,0x4dd0u,0x1c3b0u,0x100000u,0x1eb16u,0x4e60u,0x1c3c0u,0x1e800u,0x1eb18u,0x4ee0u,0x1c3d0u,0x100000u,0x1eb1au,0x4f70u,0x1c3e0u,0x100000u,0x1eb1cu,0x4ff0u,0x1c3f0u,0x100000u,0x1eb1eu,0x4810u,0x1c400u,0x1e830u,0x1eb00u,0x4890u,0x1c410u,0x100000u,0x1eb02u,0x4900u,0x1c420u,0x100000u,0x1eb04u,0x4980u,0x1c430u,0x100000u,0x1eb06u,0x4a30u,0x1c440u,0x1e830u,0x1eb08u,0x4ab0u,0x1c450u,0x100000u,0x1eb0au,0x4b20u,0x1c460u,0x100000u,0x1eb0cu,0x4ba0u,0x1c470u,0x100000u,0x1eb0eu,0x4c50u,0x1c480u,0x1e830u,0x1eb10u,0x4cd0u,0x1c490u,0x100000u,0x1eb12u,0x4d40u,0x1c4a0u,0x100000u,0x1eb14u,0x4dc0u,0x1c4b0u,0x100000u,0x1eb16u,0x4e70u,0x1c4c0u,0x1e830u,0x1eb18u,0x4ef0u,0x1c4d0u,0x100000u,0x1eb1au,0x4f60u,0x1c4e0u,0x100000u,0x1eb1cu,0x4fe0u,0x1c4f0u,0x100000u,0x1eb1eu,0x4820u,0x1c500u,0x1e860u,0x1eb00u,0x48a0u,0x1c510u,0x100000u,0x1eb02u,0x4930u,0x1c520u,0x100000u,0x1eb04u,0x49b0u,0x1c530u,0x100000u,0x1eb06u,0x4a00u,0x1c540u,0x1e860u,0x1eb08u,0x4a80u,0x1c550u,0x100000u,0x1eb0au,0x4b10u,0x1c560u,0x100000u,0x1eb0cu,0x4b90u,0x1c570u,0x100000u,0x1eb0eu,0x4c60u,0x1c580u,0x1e860u,0x1eb10u,0x4ce0u,0x1c590u,0x100000u,0x1eb12u,0x4d70u,0x1c5a0u,0x100000u,0x1eb14u,0x4df0u,0x1c5b0u,0x100000u,0x1eb16u,0x4e40u,0x1c5c0u,0x1e860u,0x1eb18u,0x4ec0u,0x1c5d0u,0x100000u,0x1eb1au,0x4f50u,0x1c5e0u,0x100000u,0x1eb1cu,0x4fd0u,0x1c5f0u,0x100000u,0x1eb1eu,0x4830u,0x1c600u,0x1e890u,0x1eb00u,0x48b0u,0x1c610u,0x100000u,0x1eb02u,0x4920u,0x1c620u,0x100000u,0x1eb04u,0x49a0u,0x1c630u,0x100000u,0x1eb06u,0x4a10u,0x1c640u,0x1e890u,0x1eb08u,0x4a90u,0x1c650u,0x100000u,0x1eb0au,0x4b00u,0x1c660u,0x100000u,0x1eb0cu,0x4b80u,0x1c670u,0x100000u,0x1eb0eu,0x4c70u,0x1c680u,0x1e890u,0x1eb10u,0x4cf0u,0x1c690u,0x100000u,0x1eb12u,0x4d60u,0x1c6a0u,0x100000u,0x1eb14u,0x4de0u,0x1c6b0u,0x100000u,0x1eb16u,0x4e50u,0x1c6c0u,0x1e890u,0x1eb18u,0x4ed0u,0x1c6d0u,0x100000u,0x1eb1au,0x4f40u,0x1c6e0u,0x100000u,0x1eb1cu,0x4fc0u,0x1c6f0u,0x100000u,0x1eb1eu,0x24d00u,0x1d300u,0x1d300u,0xc0100u,0x24d02u,0x1d302u,0x100000u,0xc0110u,0x24d04u,0x1d304u,0x100000u,0xc0120u,0x24d06u,0x1d306u,0x100000u,0xc0130u,0x24f00u,0x1d310u,0x1d300u,0xc0140u,0x24f02u,0x1d312u,0x100000u,0xc0150u,0x24f04u,0x1d314u,0x100000u,0xc0160u,0x24f06u,0x1d316u,0x100000u,0xc0170u,0x25100u,0x1d320u,0x1d300u,0xc0280u,0x25102u,0x1d322u,0x100000u,0xc0290u,0x25104u,0x1d324u,0x100000u,0xc02a0u,0x25106u,0x1d326u,0x100000u,0xc02b0u,0x25300u,0x1d330u,0x1d300u,0xc02c0u,0x25302u,0x1d332u,0x100000u,0xc02d0u,0x25304u,0x1d334u,0x100000u,0xc02e0u,0x25306u,0x1d336u,0x100000u,0xc02f0u,0x24d08u,0x1d308u,0x1d310u,0xc0410u,0x24d0au,0x1d30au,0x100000u,0xc0400u,0x24d0cu,0x1d30cu,0x100000u,0xc0430u,0x24d0eu,0x1d30eu,0x100000u,0xc0420u,0x24f08u,0x1d318u,0x1d310u,0xc0450u,0x24f0au,0x1d31au,0x100000u,0xc0440u,0x24f0cu,0x1d31cu,0x100000u,0xc0470u,0x24f0eu,0x1d31eu,0x100000u,0xc0460u,0x25108u,0x1d328u,0x1d310u,0xef990u,0x2510au,0x1d32au,0x100000u,0xef980u,0x2510cu,0x1d32cu,0x100000u,0xef9b0u,0x2510eu,0x1d32eu,0x100000u,0xef9a0u,0x25308u,0x1d338u,0x1d310u,0xef9d0u,0x2530au,0x1d33au,0x100000u,0xef9c0u,0x2530cu,0x1d33cu,0x100000u,0xef9f0u,0x2530eu,0x1d33eu,0x100000u,0xef9e0u,0x25500u,0x1d340u,0x1d320u,0xefb20u,0x25502u,0x1d342u,0x100000u,0xefb30u,0x25504u,0x1d344u,0x100000u,0xefb00u,0x25506u,0x1d346u,0x100000u,0xefb10u,0x25700u,0x1d350u,0x1d320u,0xefb60u,0x25702u,0x1d352u,0x100000u,0xefb70u,0x25704u,0x1d354u,0x100000u,0xefb40u,0x25706u,0x1d356u,0x100000u,0xefb50u,0x25900u,0x1d360u,0x1d320u,0xefca0u,0x25902u,0x1d362u,0x100000u,0xefcb0u,0x25904u,0x1d364u,0x100000u,0xefc80u,0x25906u,0x1d366u,0x100000u,0xefc90u,0x25b00u,0x1d370u,0x1d320u,0xefce0u,0x25b02u,0x1d372u,0x100000u,0xefcf0u,0x25b04u,0x1d374u,0x100000u,0xefcc0u,0x25b06u,0x1d376u,0x100000u,0xefcd0u,0x100000u,0x1d348u,0x1d330u,0xefe30u,0x100000u,0x1d34au,0x100000u,0xefe20u,0x100000u,0x1d34cu,0x100000u,0xefe10u,0x100000u,0x1d34eu,0x100000u,0xefe00u,0x100000u,0x1d358u,0x1d330u,0xefe70u,0x100000u,0x1d35au,0x100000u,0xefe60u,0x100000u,0x1d35cu,0x100000u,0xefe50u,0x100000u,0x1d35eu,0x100000u,0xefe40u,0x100000u,0x1d368u,0x1d330u,0xeffb0u,0x100000u,0x1d36au,0x100000u,0xeffa0u,0x100000u,0x1d36cu,0x100000u,0xeff90u,0x100000u,0x1d36eu,0x100000u,0xeff80u,0x100000u,0x1d378u,0x1d330u,0xefff0u,0x100000u,0x1d37au,0x100000u,0xeffe0u,0x100000u,0x1d37cu,0x100000u,0xeffd0u,0x100000u,0x1d37eu,0x100000u,0xeffc0u,0x0u,0x30420u,0x8fc50u,0xc0000u,0x10u,0x30430u,0x8fc40u,0xc0010u,0x20u,0x30400u,0x8fc70u,0xc0020u,0x30u,0x30410u,0x8fc60u,0xc0030u,0x40u,0x30460u,0x8fc10u,0xc0040u,0x50u,0x30470u,0x8fc00u,0xc0050u,0x60u,0x30440u,0x8fc30u,0xc0060u,0x70u,0x30450u,0x8fc20u,0xc0070u,0x80u,0x304b0u,0x8fcd0u,0xc0080u,0x90u,0x304a0u,0x8fcc0u,0xc0090u,0xa0u,0x30490u,0x8fcf0u,0xc00a0u,0xb0u,0x30480u,0x8fce0u,0xc00b0u,0xc0u,0x304f0u,0x8fc90u,0xc00c0u,0xd0u,0x304e0u,0x8fc80u,0xc00d0u,0xe0u,0x304d0u,0x8fcb0u,0xc00e0u,0xf0u,0x304c0u,0x8fca0u,0xc00f0u,0x100u,0x30530u,0x8fd60u,0xc0100u,0x110u,0x30520u,0x8fd70u,0xc0110u,0x120u,0x30510u,0x8fd40u,0xc0120u,0x130u,0x30500u,0x8fd50u,0xc0130u,0x140u,0x30570u,0x8fd20u,0xc0140u,0x150u,0x30560u,0x8fd30u,0xc0150u,0x160u,0x30550u,0x8fd00u,0xc0160u,0x170u,0x30540u,0x8fd10u,0xc0170u,0x190u,0x305b0u,0x8fde0u,0xc0190u,0x180u,0x305a0u,0x8fdf0u,0xc0180u,0x1b0u,0x30590u,0x8fdc0u,0xc01b0u,0x1a0u,0x30580u,0x8fdd0u,0xc01a0u,0x1d0u,0x305f0u,0x8fda0u,0xc01d0u,0x1c0u,0x305e0u,0x8fdb0u,0xc01c0u,0x1f0u,0x305d0u,0x8fd80u,0xc01f0u,0x1e0u,0x305c0u,0x8fd90u,0xc01e0u,0x210u,0x30640u,0x8fe60u,0xc0210u,0x200u,0x30650u,0x8fe70u,0xc0200u,0x230u,0x30660u,0x8fe40u,0xc0230u,0x220u,0x30670u,0x8fe50u,0xc0220u,0x250u,0x30600u,0x8fe20u,0xc0250u,0x240u,0x30610u,0x8fe30u,0xc0240u,0x270u,0x30620u,0x8fe00u,0xc0270u,0x260u,0x30630u,0x8fe10u,0xc0260u,0x290u,0x306c0u,0x8fef0u,0xc0290u,0x280u,0x306d0u,0x8fee0u,0xc0280u,0x2b0u,0x306e0u,0x8fed0u,0xc02b0u,0x2a0u,0x306f0u,0x8fec0u,0xc02a0u,0x2d0u,0x30680u,0x8feb0u,0xc02d0u,0x2c0u,0x30690u,0x8fea0u,0xc02c0u,0x2f0u,0x306a0u,0x8fe90u,0xc02f0u,0x2e0u,0x306b0u,0x8fe80u,0xc02e0u,0x320u,0x30740u,0x8ff70u,0xc0320u,0x330u,0x30750u,0x8ff60u,0xc0330u,0x300u,0x30760u,0x8ff50u,0xc0300u,0x310u,0x30770u,0x8ff40u,0xc0310u,0x360u,0x30700u,0x8ff30u,0xc0360u,0x370u,0x30710u,0x8ff20u,0xc0370u,0x340u,0x30720u,0x8ff10u,0xc0340u,0x350u,0x30730u,0x8ff00u,0xc0350u,0x3a0u,0x307d0u,0x8fff0u,0xc03a0u,0x3b0u,0x307c0u,0x8ffe0u,0xc03b0u,0x380u,0x307f0u,0x8ffd0u,0xc0380u,0x390u,0x307e0u,0x8ffc0u,0xc0390u,0x3e0u,0x30790u,0x8ffb0u,0xc03e0u,0x3f0u,0x30780u,0x8ffa0u,0xc03f0u,0x3c0u,0x307b0u,0x8ff90u,0xc03c0u,0x3d0u,0x307a0u,0x8ff80u,0xc03d0u,0x0u,0x11f820u,0x0u,0x7030cu,0x0u,0x11f830u,0x0u,0x7030cu,0x0u,0x11f800u,0x0u,0x7030cu,0x0u,0x11f810u,0x0u,0x7030cu,0x0u,0x11f860u,0x0u,0x7030cu,0x0u,0x11f870u,0x0u,0x7030cu,0x0u,0x11f840u,0x0u,0x7030cu,0x0u,0x11f850u,0x0u,0x7030cu,0x0u,0x11f8b0u,0x0u,0x7230cu,0x0u,0x11f8a0u,0x0u,0x7230cu,0x0u,0x11f890u,0x0u,0x7230cu,0x0u,0x11f880u,0x0u,0x7230cu,0x0u,0x11f8f0u,0x0u,0x7230cu,0x0u,0x11f8e0u,0x0u,0x7230cu,0x0u,0x11f8d0u,0x0u,0x7230cu,0x0u,0x11f8c0u,0x0u,0x7230cu,0x0u,0x11f930u,0x0u,0x7232cu,0x0u,0x11f920u,0x0u,0x7232cu,0x0u,0x11f910u,0x0u,0x7232cu,0x0u,0x11f900u,0x0u,0x7232cu,0x0u,0x11f970u,0x0u,0x7232cu,0x0u,0x11f960u,0x0u,0x7232cu,0x0u,0x11f950u,0x0u,0x7232cu,0x0u,0x11f940u,0x0u,0x7232cu,0x0u,0x11f9b0u,0x0u,0x72124u,0x0u,0x11f9a0u,0x0u,0x72124u,0x0u,0x11f990u,0x0u,0x72124u,0x0u,0x11f980u,0x0u,0x72124u,0x0u,0x11f9f0u,0x0u,0x72124u,0x0u,0x11f9e0u,0x0u,0x72124u,0x0u,0x11f9d0u,0x0u,0x72124u,0x0u,0x11f9c0u,0x0u,0x72124u,0x0u,0x11fa40u,0x0u,0x721a6u,0x0u,0x11fa50u,0x0u,0x721a6u,0x0u,0x11fa60u,0x0u,0x721a6u,0x0u,0x11fa70u,0x0u,0x721a6u,0x0u,0x11fa00u,0x0u,0x721a6u,0x0u,0x11fa10u,0x0u,0x721a6u,0x0u,0x11fa20u,0x0u,0x721a6u,0x0u,0x11fa30u,0x0u,0x721a6u,0x0u,0x11fac0u,0x0u,0x72186u,0x0u,0x11fad0u,0x0u,0x72186u,0x0u,0x11fae0u,0x0u,0x72186u,0x0u,0x11faf0u,0x0u,0x72186u,0x0u,0x11fa80u,0x0u,0x72186u,0x0u,0x11fa90u,0x0u,0x72186u,0x0u,0x11faa0u,0x0u,0x72186u,0x0u,0x11fab0u,0x0u,0x72186u,0x0u,0x11fb40u,0x0u,0x72186u,0x0u,0x11fb50u,0x0u,0x72186u,0x0u,0x11fb60u,0x0u,0x72186u,0x0u,0x11fb70u,0x0u,0x72186u,0x0u,0x11fb00u,0x0u,0x72186u,0x0u,0x11fb10u,0x0u,0x72186u,0x0u,0x11fb20u,0x0u,0x72186u,0x0u,0x11fb30u,0x0u,0x72186u,0x0u,0x11fbd0u,0x0u,0x7230cu,0x0u,0x11fbc0u,0x0u,0x7230cu,0x0u,0x11fbf0u,0x0u,0x7230cu,0x0u,0x11fbe0u,0x0u,0x7230cu,0x0u,0x11fb90u,0x0u,0x7230cu,0x0u,0x11fb80u,0x0u,0x7230cu,0x0u,0x11fbb0u,0x0u,0x7230cu,0x0u,0x11fba0u,0x0u,0x7230cu,0x5800u,0x5820u,0x4860u,0x4860u,0x5808u,0x5828u,0x4868u,0x4870u,0x5810u,0x5830u,0x4870u,0x5800u,0x5818u,0x5838u,0x4878u,0x5810u,0x5880u,0x58a0u,0x48e0u,0x48e0u,0x5888u,0x58a8u,0x48e8u,0x48f0u,0x5890u,0x58b0u,0x48f0u,0x5880u,0x5898u,0x58b8u,0x48f8u,0x5890u,0x5920u,0x5900u,0x4940u,0x4940u,0x5928u,0x5908u,0x4948u,0x4950u,0x5930u,0x5910u,0x4950u,0x5920u,0x5938u,0x5918u,0x4958u,0x5930u,0x59a0u,0x5980u,0x49c0u,0x49c0u,0x59a8u,0x5988u,0x49c8u,0x49d0u,0x59b0u,0x5990u,0x49d0u,0x59a0u,0x59b8u,0x5998u,0x49d8u,0x59b0u,0x5c40u,0x5c60u,0x4c20u,0x4a60u,0x5c48u,0x5c68u,0x4c28u,0x4a70u,0x5c50u,0x5c70u,0x4c30u,0x5a00u,0x5c58u,0x5c78u,0x4c38u,0x5a10u,0x5cc0u,0x5ce0u,0x4ca0u,0x4ae0u,0x5cc8u,0x5ce8u,0x4ca8u,0x4af0u,0x5cd0u,0x5cf0u,0x4cb0u,0x5a80u,0x5cd8u,0x5cf8u,0x4cb8u,0x5a90u,0x5d60u,0x5d40u,0x4d00u,0x4b40u,0x5d68u,0x5d48u,0x4d08u,0x4b50u,0x5d70u,0x5d50u,0x4d10u,0x5b20u,0x5d78u,0x5d58u,0x4d18u,0x5b30u,0x5de0u,0x5dc0u,0x4d80u,0x4bc0u,0x5de8u,0x5dc8u,0x4d88u,0x4bd0u,0x5df0u,0x5dd0u,0x4d90u,0x5ba0u,0x5df8u,0x5dd8u,0x4d98u,0x5bb0u,0x6000u,0x6020u,0x5060u,0x4c20u,0x6008u,0x6028u,0x5068u,0x4c30u,0x6010u,0x6030u,0x5070u,0x5c40u,0x6018u,0x6038u,0x5078u,0x5c50u,0x6080u,0x60a0u,0x50e0u,0x4ca0u,0x6088u,0x60a8u,0x50e8u,0x4cb0u,0x6090u,0x60b0u,0x50f0u,0x5cc0u,0x6098u,0x60b8u,0x50f8u,0x5cd0u,0x6120u,0x6100u,0x5140u,0x4d00u,0x6128u,0x6108u,0x5148u,0x4d10u,0x6130u,0x6110u,0x5150u,0x5d60u,0x6138u,0x6118u,0x5158u,0x5d70u,0x61a0u,0x6180u,0x51c0u,0x4d80u,0x61a8u,0x6188u,0x51c8u,0x4d90u,0x61b0u,0x6190u,0x51d0u,0x5de0u,0x61b8u,0x6198u,0x51d8u,0x5df0u,0x6440u,0x6460u,0x5420u,0x4e20u,0x6448u,0x6468u,0x5428u,0x4e30u,0x6450u,0x6470u,0x5430u,0x5e40u,0x6458u,0x6478u,0x5438u,0x5e50u,0x64c0u,0x64e0u,0x54a0u,0x4ea0u,0x64c8u,0x64e8u,0x54a8u,0x4eb0u,0x64d0u,0x64f0u,0x54b0u,0x5ec0u,0x64d8u,0x64f8u,0x54b8u,0x5ed0u,0x6560u,0x6540u,0x5500u,0x4f00u,0x6568u,0x6548u,0x5508u,0x4f10u,0x6570u,0x6550u,0x5510u,0x5f60u,0x6578u,0x6558u,0x5518u,0x5f70u,0x65e0u,0x65c0u,0x5580u,0x4f80u,0x65e8u,0x65c8u,0x5588u,0x4f90u,0x65f0u,0x65d0u,0x5590u,0x5fe0u,0x65f8u,0x65d8u,0x5598u,0x5ff0u,0x5820u,0x1e8c0u,0x1eb60u,0xc40u,0x5828u,0x1ed50u,0x1eff0u,0xc50u,0x5830u,0x1f1e0u,0x1f480u,0xc60u,0x5838u,0x1f670u,0x1f910u,0xc70u,0x58a0u,0x1fb00u,0x1fda0u,0xc00u,0x58a8u,0x1ff90u,0x20230u,0xc10u,0x58b0u,0x20420u,0x206c0u,0xc20u,0x58b8u,0x208b0u,0x20b50u,0xc30u,0x5900u,0x20d40u,0x20fe0u,0xdc0u,0x5908u,0x211d0u,0x21470u,0xdd0u,0x5910u,0x21660u,0x21900u,0xde0u,0x5918u,0x21af0u,0x21d90u,0xdf0u,0x5980u,0x21f80u,0x22220u,0xd80u,0x5988u,0x22410u,0x226b0u,0xd90u,0x5990u,0x228a0u,0x22b40u,0xda0u,0x5998u,0x22d30u,0x22fd0u,0xdb0u,0x5a20u,0x1e8f0u,0x1eb68u,0xf60u,0x5a28u,0x1ed80u,0x1eff8u,0xf70u,0x5a30u,0x1f210u,0x1f488u,0xf40u,0x5a38u,0x1f6a0u,0x1f918u,0xf50u,0x5aa0u,0x1fb30u,0x1fda8u,0xf20u,0x5aa8u,0x1ffc0u,0x20238u,0xf30u,0x5ab0u,0x20450u,0x206c8u,0xf00u,0x5ab8u,0x208e0u,0x20b58u,0xf10u,0x5b00u,0x20d70u,0x20fe8u,0x10e0u,0x5b08u,0x21200u,0x21478u,0x10f0u,0x5b10u,0x21690u,0x21908u,0x10c0u,0x5b18u,0x21b20u,0x21d98u,0x10d0u,0x5b80u,0x21fb0u,0x22228u,0x10a0u,0x5b88u,0x22440u,0x226b8u,0x10b0u,0x5b90u,0x228d0u,0x22b48u,0x1080u,0x5b98u,0x22d60u,0x22fd8u,0x1090u,0x5c60u,0x1e920u,0x1eb70u,0x1240u,0x5c68u,0x1edb0u,0x1f000u,0x1250u,0x5c70u,0x1f240u,0x1f490u,0x1260u,0x5c78u,0x1f6d0u,0x1f920u,0x1270u,0x5ce0u,0x1fb60u,0x1fdb0u,0x1200u,0x5ce8u,0x1fff0u,0x20240u,0x1210u,0x5cf0u,0x20480u,0x206d0u,0x1220u,0x5cf8u,0x20910u,0x20b60u,0x1230u,0x5d40u,0x20da0u,0x20ff0u,0x13c0u,0x5d48u,0x21230u,0x21480u,0x13d0u,0x5d50u,0x216c0u,0x21910u,0x13e0u,0x5d58u,0x21b50u,0x21da0u,0x13f0u,0x5dc0u,0x21fe0u,0x22230u,0x1380u,0x5dc8u,0x22470u,0x226c0u,0x1390u,0x5dd0u,0x22900u,0x22b50u,0x13a0u,0x5dd8u,0x22d90u,0x22fe0u,0x13b0u,0x5e60u,0x1e950u,0x1eb78u,0x1560u,0x5e68u,0x1ede0u,0x1f008u,0x1570u,0x5e70u,0x1f270u,0x1f498u,0x1540u,0x5e78u,0x1f700u,0x1f928u,0x1550u,0x5ee0u,0x1fb90u,0x1fdb8u,0x1520u,0x5ee8u,0x20020u,0x20248u,0x1530u,0x5ef0u,0x204b0u,0x206d8u,0x1500u,0x5ef8u,0x20940u,0x20b68u,0x1510u,0x5f40u,0x20dd0u,0x20ff8u,0x16e0u,0x5f48u,0x21260u,0x21488u,0x16f0u,0x5f50u,0x216f0u,0x21918u,0x16c0u,0x5f58u,0x21b80u,0x21da8u,0x16d0u,0x5fc0u,0x22010u,0x22238u,0x16a0u,0x5fc8u,0x224a0u,0x226c8u,0x16b0u,0x5fd0u,0x22930u,0x22b58u,0x1680u,0x5fd8u,0x22dc0u,0x22fe8u,0x1690u,0x0u,0x40u,0x9000u,0xa800u,0x80u,0xc0u,0x9010u,0xa810u,0x110u,0x150u,0x9020u,0xa820u,0x190u,0x1d0u,0x9030u,0xa830u,0x220u,0x260u,0x9040u,0xa840u,0x2a0u,0x2e0u,0x9050u,0xa850u,0x330u,0x370u,0x9060u,0xa860u,0x3b0u,0x3f0u,0x9070u,0xa870u,0x440u,0x400u,0x9080u,0xa880u,0x4c0u,0x480u,0x9090u,0xa890u,0x550u,0x510u,0x90a0u,0xa8a0u,0x5d0u,0x590u,0x90b0u,0xa8b0u,0x660u,0x620u,0x90c0u,0xa8c0u,0x6e0u,0x6a0u,0x90d0u,0xa8d0u,0x770u,0x730u,0x90e0u,0xa8e0u,0x7f0u,0x7b0u,0x90f0u,0xa8f0u,0x10u,0x50u,0x9300u,0xab00u,0x90u,0xd0u,0x9310u,0xab10u,0x100u,0x140u,0x9320u,0xab20u,0x180u,0x1c0u,0x9330u,0xab30u,0x230u,0x270u,0x9340u,0xab40u,0x2b0u,0x2f0u,0x9350u,0xab50u,0x320u,0x360u,0x9360u,0xab60u,0x3a0u,0x3e0u,0x9370u,0xab70u,0x450u,0x410u,0x9380u,0xab80u,0x4d0u,0x490u,0x9390u,0xab90u,0x540u,0x500u,0x93a0u,0xaba0u,0x5c0u,0x580u,0x93b0u,0xabb0u,0x670u,0x630u,0x93c0u,0xabc0u,0x6f0u,0x6b0u,0x93d0u,0xabd0u,0x760u,0x720u,0x93e0u,0xabe0u,0x7e0u,0x7a0u,0x93f0u,0xabf0u,0x20u,0x60u,0x9600u,0xae00u,0xa0u,0xe0u,0x9610u,0xae10u,0x130u,0x170u,0x9620u,0xae20u,0x1b0u,0x1f0u,0x9630u,0xae30u,0x200u,0x240u,0x9640u,0xae40u,0x280u,0x2c0u,0x9650u,0xae50u,0x310u,0x350u,0x9660u,0xae60u,0x390u,0x3d0u,0x9670u,0xae70u,0x460u,0x420u,0x9680u,0xae80u,0x4e0u,0x4a0u,0x9690u,0xae90u,0x570u,0x530u,0x96a0u,0xaea0u,0x5f0u,0x5b0u,0x96b0u,0xaeb0u,0x640u,0x600u,0x96c0u,0xaec0u,0x6c0u,0x680u,0x96d0u,0xaed0u,0x750u,0x710u,0x96e0u,0xaee0u,0x7d0u,0x790u,0x96f0u,0xaef0u,0x30u,0x70u,0x9900u,0xb100u,0xb0u,0xf0u,0x9910u,0xb110u,0x120u,0x160u,0x9920u,0xb120u,0x1a0u,0x1e0u,0x9930u,0xb130u,0x210u,0x250u,0x9940u,0xb140u,0x290u,0x2d0u,0x9950u,0xb150u,0x300u,0x340u,0x9960u,0xb160u,0x380u,0x3c0u,0x9970u,0xb170u,0x470u,0x430u,0x9980u,0xb180u,0x4f0u,0x4b0u,0x9990u,0xb190u,0x560u,0x520u,0x99a0u,0xb1a0u,0x5e0u,0x5a0u,0x99b0u,0xb1b0u,0x650u,0x610u,0x99c0u,0xb1c0u,0x6d0u,0x690u,0x99d0u,0xb1d0u,0x740u,0x700u,0x99e0u,0xb1e0u,0x7c0u,0x780u,0x99f0u,0xb1f0u,0x9c08u,0x12060u,0x120a0u,0x12400u,0x9c18u,0x121f0u,0x12230u,0x12410u,0x9c28u,0x12340u,0x12380u,0x12420u,0x9c38u,0x124d0u,0x12510u,0x12430u,0x9c48u,0x12620u,0x126e0u,0x12440u,0x9c58u,0x127b0u,0x12870u,0x12450u,0x9c68u,0x12900u,0x129c0u,0x12460u,0x9c78u,0x12a90u,0x12b50u,0x12470u,0x9c88u,0x12c60u,0x12ca0u,0x12480u,0x9c98u,0x12df0u,0x12e30u,0x12490u,0x9ca8u,0x12f40u,0x12f80u,0x124a0u,0x9cb8u,0x130d0u,0x13110u,0x124b0u,0x9cc8u,0x13220u,0x132e0u,0x124c0u,0x9cd8u,0x133b0u,0x13470u,0x124d0u,0x9ce8u,0x13500u,0x135c0u,0x124e0u,0x9cf8u,0x13690u,0x13750u,0x124f0u,0x9f08u,0x12070u,0x120a8u,0x12500u,0x9f18u,0x121e0u,0x12238u,0x12510u,0x9f28u,0x12350u,0x12388u,0x12520u,0x9f38u,0x124c0u,0x12518u,0x12530u,0x9f48u,0x12630u,0x126e8u,0x12540u,0x9f58u,0x127a0u,0x12878u,0x12550u,0x9f68u,0x12910u,0x129c8u,0x12560u,0x9f78u,0x12a80u,0x12b58u,0x12570u,0x9f88u,0x12c70u,0x12ca8u,0x12580u,0x9f98u,0x12de0u,0x12e38u,0x12590u,0x9fa8u,0x12f50u,0x12f88u,0x125a0u,0x9fb8u,0x130c0u,0x13118u,0x125b0u,0x9fc8u,0x13230u,0x132e8u,0x125c0u,0x9fd8u,0x133a0u,0x13478u,0x125d0u,0x9fe8u,0x13510u,0x135c8u,0x125e0u,0x9ff8u,0x13680u,0x13758u,0x125f0u,0xa208u,0x12080u,0x120b0u,0x12600u,0xa218u,0x12210u,0x12220u,0x12610u,0xa228u,0x123a0u,0x12390u,0x12620u,0xa238u,0x12530u,0x12500u,0x12630u,0xa248u,0x126c0u,0x126f0u,0x12640u,0xa258u,0x12850u,0x12860u,0x12650u,0xa268u,0x129e0u,0x129d0u,0x12660u,0xa278u,0x12b70u,0x12b40u,0x12670u,0xa288u,0x12c80u,0x12cb0u,0x12680u,0xa298u,0x12e10u,0x12e20u,0x12690u,0xa2a8u,0x12fa0u,0x12f90u,0x126a0u,0xa2b8u,0x13130u,0x13100u,0x126b0u,0xa2c8u,0x132c0u,0x132f0u,0x126c0u,0xa2d8u,0x13450u,0x13460u,0x126d0u,0xa2e8u,0x135e0u,0x135d0u,0x126e0u,0xa2f8u,0x13770u,0x13740u,0x126f0u,0xa508u,0x12090u,0x120b8u,0x12700u,0xa518u,0x12200u,0x12228u,0x12710u,0xa528u,0x123b0u,0x12398u,0x12720u,0xa538u,0x12520u,0x12508u,0x12730u,0xa548u,0x126d0u,0x126f8u,0x12740u,0xa558u,0x12840u,0x12868u,0x12750u,0xa568u,0x129f0u,0x129d8u,0x12760u,0xa578u,0x12b60u,0x12b48u,0x12770u,0xa588u,0x12c90u,0x12cb8u,0x12780u,0xa598u,0x12e00u,0x12e28u,0x12790u,0xa5a8u,0x12fb0u,0x12f98u,0x127a0u,0xa5b8u,0x13120u,0x13108u,0x127b0u,0xa5c8u,0x132d0u,0x132f8u,0x127c0u,0xa5d8u,0x13440u,0x13468u,0x127d0u,0xa5e8u,0x135f0u,0x135d8u,0x127e0u,0xa5f8u,0x13760u,0x13748u,0x127f0u,0x5000u,0x1c300u,0x1e8c0u,0x1eb60u,0x5080u,0x1c310u,0x100000u,0x1eb62u,0x5110u,0x1c320u,0x100000u,0x1eb64u,0x5190u,0x1c330u,0x100000u,0x1eb66u,0x5220u,0x1c340u,0x1e8c0u,0x1eb68u,0x52a0u,0x1c350u,0x100000u,0x1eb6au,0x5330u,0x1c360u,0x100000u,0x1eb6cu,0x53b0u,0x1c370u,0x100000u,0x1eb6eu,0x5440u,0x1c380u,0x1e8c0u,0x1eb70u,0x54c0u,0x1c390u,0x100000u,0x1eb72u,0x5550u,0x1c3a0u,0x100000u,0x1eb74u,0x55d0u,0x1c3b0u,0x100000u,0x1eb76u,0x5660u,0x1c3c0u,0x1e8c0u,0x1eb78u,0x56e0u,0x1c3d0u,0x100000u,0x1eb7au,0x5770u,0x1c3e0u,0x100000u,0x1eb7cu,0x57f0u,0x1c3f0u,0x100000u,0x1eb7eu,0x5010u,0x1c400u,0x1e8f0u,0x1eb60u,0x5090u,0x1c410u,0x100000u,0x1eb62u,0x5100u,0x1c420u,0x100000u,0x1eb64u,0x5180u,0x1c430u,0x100000u,0x1eb66u,0x5230u,0x1c440u,0x1e8f0u,0x1eb68u,0x52b0u,0x1c450u,0x100000u,0x1eb6au,0x5320u,0x1c460u,0x100000u,0x1eb6cu,0x53a0u,0x1c470u,0x100000u,0x1eb6eu,0x5450u,0x1c480u,0x1e8f0u,0x1eb70u,0x54d0u,0x1c490u,0x100000u,0x1eb72u,0x5540u,0x1c4a0u,0x100000u,0x1eb74u,0x55c0u,0x1c4b0u,0x100000u,0x1eb76u,0x5670u,0x1c4c0u,0x1e8f0u,0x1eb78u,0x56f0u,0x1c4d0u,0x100000u,0x1eb7au,0x5760u,0x1c4e0u,0x100000u,0x1eb7cu,0x57e0u,0x1c4f0u,0x100000u,0x1eb7eu,0x5020u,0x1c500u,0x1e920u,0x1eb60u,0x50a0u,0x1c510u,0x100000u,0x1eb62u,0x5130u,0x1c520u,0x100000u,0x1eb64u,0x51b0u,0x1c530u,0x100000u,0x1eb66u,0x5200u,0x1c540u,0x1e920u,0x1eb68u,0x5280u,0x1c550u,0x100000u,0x1eb6au,0x5310u,0x1c560u,0x100000u,0x1eb6cu,0x5390u,0x1c570u,0x100000u,0x1eb6eu,0x5460u,0x1c580u,0x1e920u,0x1eb70u,0x54e0u,0x1c590u,0x100000u,0x1eb72u,0x5570u,0x1c5a0u,0x100000u,0x1eb74u,0x55f0u,0x1c5b0u,0x100000u,0x1eb76u,0x5640u,0x1c5c0u,0x1e920u,0x1eb78u,0x56c0u,0x1c5d0u,0x100000u,0x1eb7au,0x5750u,0x1c5e0u,0x100000u,0x1eb7cu,0x57d0u,0x1c5f0u,0x100000u,0x1eb7eu,0x5030u,0x1c600u,0x1e950u,0x1eb60u,0x50b0u,0x1c610u,0x100000u,0x1eb62u,0x5120u,0x1c620u,0x100000u,0x1eb64u,0x51a0u,0x1c630u,0x100000u,0x1eb66u,0x5210u,0x1c640u,0x1e950u,0x1eb68u,0x5290u,0x1c650u,0x100000u,0x1eb6au,0x5300u,0x1c660u,0x100000u,0x1eb6cu,0x5380u,0x1c670u,0x100000u,0x1eb6eu,0x5470u,0x1c680u,0x1e950u,0x1eb70u,0x54f0u,0x1c690u,0x100000u,0x1eb72u,0x5560u,0x1c6a0u,0x100000u,0x1eb74u,0x55e0u,0x1c6b0u,0x100000u,0x1eb76u,0x5650u,0x1c6c0u,0x1e950u,0x1eb78u,0x56d0u,0x1c6d0u,0x100000u,0x1eb7au,0x5740u,0x1c6e0u,0x100000u,0x1eb7cu,0x57c0u,0x1c6f0u,0x100000u,0x1eb7eu,0x25d00u,0x1d380u,0x1d380u,0xf0140u,0x25d02u,0x1d382u,0x100000u,0xf0150u,0x25d04u,0x1d384u,0x100000u,0xf0160u,0x25d06u,0x1d386u,0x100000u,0xf0170u,0x25f00u,0x1d390u,0x1d380u,0xf0100u,0x25f02u,0x1d392u,0x100000u,0xf0110u,0x25f04u,0x1d394u,0x100000u,0xf0120u,0x25f06u,0x1d396u,0x100000u,0xf0130u,0x26100u,0x1d3a0u,0x1d380u,0xf02c0u,0x26102u,0x1d3a2u,0x100000u,0xf02d0u,0x26104u,0x1d3a4u,0x100000u,0xf02e0u,0x26106u,0x1d3a6u,0x100000u,0xf02f0u,0x26300u,0x1d3b0u,0x1d380u,0xf0280u,0x26302u,0x1d3b2u,0x100000u,0xf0290u,0x26304u,0x1d3b4u,0x100000u,0xf02a0u,0x26306u,0x1d3b6u,0x100000u,0xf02b0u,0x25d08u,0x1d388u,0x1d390u,0x11f850u,0x25d0au,0x1d38au,0x100000u,0x11f840u,0x25d0cu,0x1d38cu,0x100000u,0x11f870u,0x25d0eu,0x1d38eu,0x100000u,0x11f860u,0x25f08u,0x1d398u,0x1d390u,0x11f810u,0x25f0au,0x1d39au,0x100000u,0x11f800u,0x25f0cu,0x1d39cu,0x100000u,0x11f830u,0x25f0eu,0x1d39eu,0x100000u,0x11f820u,0x26108u,0x1d3a8u,0x1d390u,0x11f9d0u,0x2610au,0x1d3aau,0x100000u,0x11f9c0u,0x2610cu,0x1d3acu,0x100000u,0x11f9f0u,0x2610eu,0x1d3aeu,0x100000u,0x11f9e0u,0x26308u,0x1d3b8u,0x1d390u,0x11f990u,0x2630au,0x1d3bau,0x100000u,0x11f980u,0x2630cu,0x1d3bcu,0x100000u,0x11f9b0u,0x2630eu,0x1d3beu,0x100000u,0x11f9a0u,0x25508u,0x1d3c0u,0x1d3a0u,0x11fb60u,0x2550au,0x1d3c2u,0x100000u,0x11fb70u,0x2550cu,0x1d3c4u,0x100000u,0x11fb40u,0x2550eu,0x1d3c6u,0x100000u,0x11fb50u,0x25708u,0x1d3d0u,0x1d3a0u,0x11fb20u,0x2570au,0x1d3d2u,0x100000u,0x11fb30u,0x2570cu,0x1d3d4u,0x100000u,0x11fb00u,0x2570eu,0x1d3d6u,0x100000u,0x11fb10u,0x25908u,0x1d3e0u,0x1d3a0u,0x11fce0u,0x2590au,0x1d3e2u,0x100000u,0x11fcf0u,0x2590cu,0x1d3e4u,0x100000u,0x11fcc0u,0x2590eu,0x1d3e6u,0x100000u,0x11fcd0u,0x25b08u,0x1d3f0u,0x1d3a0u,0x11fca0u,0x25b0au,0x1d3f2u,0x100000u,0x11fcb0u,0x25b0cu,0x1d3f4u,0x100000u,0x11fc80u,0x25b0eu,0x1d3f6u,0x100000u,0x11fc90u,0x100000u,0x1d3c8u,0x1d3b0u,0x11fe70u,0x100000u,0x1d3cau,0x100000u,0x11fe60u,0x100000u,0x1d3ccu,0x100000u,0x11fe50u,0x100000u,0x1d3ceu,0x100000u,0x11fe40u,0x100000u,0x1d3d8u,0x1d3b0u,0x11fe30u,0x100000u,0x1d3dau,0x100000u,0x11fe20u,0x100000u,0x1d3dcu,0x100000u,0x11fe10u,0x100000u,0x1d3deu,0x100000u,0x11fe00u,0x100000u,0x1d3e8u,0x1d3b0u,0x11fff0u,0x100000u,0x1d3eau,0x100000u,0x11ffe0u,0x100000u,0x1d3ecu,0x100000u,0x11ffd0u,0x100000u,0x1d3eeu,0x100000u,0x11ffc0u,0x100000u,0x1d3f8u,0x1d3b0u,0x11ffb0u,0x100000u,0x1d3fau,0x100000u,0x11ffa0u,0x100000u,0x1d3fcu,0x100000u,0x11ff90u,0x100000u,0x1d3feu,0x100000u,0x11ff80u,0x420u,0x30850u,0x90000u,0xc0420u,0x430u,0x30840u,0x90010u,0xc0430u,0x400u,0x30870u,0x90020u,0xc0400u,0x410u,0x30860u,0x90030u,0xc0410u,0x460u,0x30810u,0x90040u,0xc0460u,0x470u,0x30800u,0x90050u,0xc0470u,0x440u,0x30830u,0x90060u,0xc0440u,0x450u,0x30820u,0x90070u,0xc0450u,0x4b0u,0x308d0u,0x90080u,0xef8b0u,0x4a0u,0x308c0u,0x90090u,0xef8a0u,0x490u,0x308f0u,0x900a0u,0xef890u,0x480u,0x308e0u,0x900b0u,0xef880u,0x4f0u,0x30890u,0x900c0u,0xef8f0u,0x4e0u,0x30880u,0x900d0u,0xef8e0u,0x4d0u,0x308b0u,0x900e0u,0xef8d0u,0x4c0u,0x308a0u,0x900f0u,0xef8c0u,0x530u,0x5fd60u,0x90100u,0xef930u,0x520u,0x5fd70u,0x90110u,0xef920u,0x510u,0x5fd40u,0x90120u,0xef910u,0x500u,0x5fd50u,0x90130u,0xef900u,0x570u,0x5fd20u,0x90140u,0xef970u,0x560u,0x5fd30u,0x90150u,0xef960u,0x550u,0x5fd00u,0x90160u,0xef950u,0x540u,0x5fd10u,0x90170u,0xef940u,0x5b0u,0x5fde0u,0x90190u,0xef9b0u,0x5a0u,0x5fdf0u,0x90180u,0xef9a0u,0x590u,0x5fdc0u,0x901b0u,0xef990u,0x580u,0x5fdd0u,0x901a0u,0xef980u,0x5f0u,0x5fda0u,0x901d0u,0xef9f0u,0x5e0u,0x5fdb0u,0x901c0u,0xef9e0u,0x5d0u,0x5fd80u,0x901f0u,0xef9d0u,0x5c0u,0x5fd90u,0x901e0u,0xef9c0u,0x640u,0x5fe60u,0x90210u,0xefa40u,0x650u,0x5fe70u,0x90200u,0xefa50u,0x660u,0x5fe40u,0x90230u,0xefa60u,0x670u,0x5fe50u,0x90220u,0xefa70u,0x600u,0x5fe20u,0x90250u,0xefa00u,0x610u,0x5fe30u,0x90240u,0xefa10u,0x620u,0x5fe00u,0x90270u,0xefa20u,0x630u,0x5fe10u,0x90260u,0xefa30u,0x6c0u,0x5fef0u,0x90290u,0xefac0u,0x6d0u,0x5fee0u,0x90280u,0xefad0u,0x6e0u,0x5fed0u,0x902b0u,0xefae0u,0x6f0u,0x5fec0u,0x902a0u,0xefaf0u,0x680u,0x5feb0u,0x902d0u,0xefa80u,0x690u,0x5fea0u,0x902c0u,0xefa90u,0x6a0u,0x5fe90u,0x902f0u,0xefaa0u,0x6b0u,0x5fe80u,0x902e0u,0xefab0u,0x740u,0x5ff70u,0x90320u,0xefb40u,0x750u,0x5ff60u,0x90330u,0xefb50u,0x760u,0x5ff50u,0x90300u,0xefb60u,0x770u,0x5ff40u,0x90310u,0xefb70u,0x700u,0x5ff30u,0x90360u,0xefb00u,0x710u,0x5ff20u,0x90370u,0xefb10u,0x720u,0x5ff10u,0x90340u,0xefb20u,0x730u,0x5ff00u,0x90350u,0xefb30u,0x7d0u,0x5fff0u,0x903a0u,0xefbd0u,0x7c0u,0x5ffe0u,0x903b0u,0xefbc0u,0x7f0u,0x5ffd0u,0x90380u,0xefbf0u,0x7e0u,0x5ffc0u,0x90390u,0xefbe0u,0x790u,0x5ffb0u,0x903e0u,0xefb90u,0x780u,0x5ffa0u,0x903f0u,0xefb80u,0x7b0u,0x5ff90u,0x903c0u,0xefbb0u,0x7a0u,0x5ff80u,0x903d0u,0xefba0u,0x0u,0x11fc50u,0x0u,0x7230cu,0x0u,0x11fc40u,0x0u,0x7230cu,0x0u,0x11fc70u,0x0u,0x7230cu,0x0u,0x11fc60u,0x0u,0x7230cu,0x0u,0x11fc10u,0x0u,0x7230cu,0x0u,0x11fc00u,0x0u,0x7230cu,0x0u,0x11fc30u,0x0u,0x7230cu,0x0u,0x11fc20u,0x0u,0x7230cu,0x0u,0x11fcd0u,0x0u,0x5232cu,0x0u,0x11fcc0u,0x0u,0x5232cu,0x0u,0x11fcf0u,0x0u,0x5232cu,0x0u,0x11fce0u,0x0u,0x5232cu,0x0u,0x11fc90u,0x0u,0x5232cu,0x0u,0x11fc80u,0x0u,0x5232cu,0x0u,0x11fcb0u,0x0u,0x5232cu,0x0u,0x11fca0u,0x0u,0x5232cu,0x0u,0x11fd60u,0x0u,0xd0124u,0x0u,0x11fd70u,0x0u,0xd0124u,0x0u,0x11fd40u,0x0u,0xd0124u,0x0u,0x11fd50u,0x0u,0xd0124u,0x0u,0x11fd20u,0x0u,0xd0124u,0x0u,0x11fd30u,0x0u,0xd0124u,0x0u,0x11fd00u,0x0u,0xd0124u,0x0u,0x11fd10u,0x0u,0xd0124u,0x0u,0x11fde0u,0x0u,0xd81a6u,0x0u,0x11fdf0u,0x0u,0xd81a6u,0x0u,0x11fdc0u,0x0u,0xd81a6u,0x0u,0x11fdd0u,0x0u,0xd81a6u,0x0u,0x11fda0u,0x0u,0xd81a6u,0x0u,0x11fdb0u,0x0u,0xd81a6u,0x0u,0x11fd80u,0x0u,0xd81a6u,0x0u,0x11fd90u,0x0u,0xd81a6u,0x0u,0x11fe60u,0x0u,0xd8996u,0x0u,0x11fe70u,0x0u,0xd8996u,0x0u,0x11fe40u,0x0u,0xd8996u,0x0u,0x11fe50u,0x0u,0xd8996u,0x0u,0x11fe20u,0x0u,0xd8996u,0x0u,0x11fe30u,0x0u,0xd8996u,0x0u,0x11fe00u,0x0u,0xd8996u,0x0u,0x11fe10u,0x0u,0xd8996u,0x0u,0x11fef0u,0x0u,0xd8996u,0x0u,0x11fee0u,0x0u,0xd8996u,0x0u,0x11fed0u,0x0u,0xd8996u,0x0u,0x11fec0u,0x0u,0xd8996u,0x0u,0x11feb0u,0x0u,0xd8996u,0x0u,0x11fea0u,0x0u,0xd8996u,0x0u,0x11fe90u,0x0u,0xd8996u,0x0u,0x11fe80u,0x0u,0xd8996u,0x0u,0x11ff70u,0x0u,0xd8b1cu,0x0u,0x11ff60u,0x0u,0xd8b1cu,0x0u,0x11ff50u,0x0u,0xd8b1cu,0x0u,0x11ff40u,0x0u,0xd8b1cu,0x0u,0x11ff30u,0x0u,0xd8b1cu,0x0u,0x11ff20u,0x0u,0xd8b1cu,0x0u,0x11ff10u,0x0u,0xd8b1cu,0x0u,0x11ff00u,0x0u,0xd8b1cu,0x0u,0x11fff0u,0x0u,0xd8b1cu,0x0u,0x11ffe0u,0x0u,0xd8b1cu,0x0u,0x11ffd0u,0x0u,0xd8b1cu,0x0u,0x11ffc0u,0x0u,0xd8b1cu,0x0u,0x11ffb0u,0x0u,0xd8b1cu,0x0u,0x11ffa0u,0x0u,0xd8b1cu,0x0u,0x11ff90u,0x0u,0xd8b1cu,0x0u,0x11ff80u,0x0u,0xd8b1cu,0x5840u,0x5860u,0x6800u,0x5840u,0x5848u,0x5868u,0x6808u,0x5850u,0x5850u,0x5870u,0x6810u,0x5860u,0x5858u,0x5878u,0x6818u,0x5870u,0x58c0u,0x58e0u,0x6880u,0x58c0u,0x58c8u,0x58e8u,0x6888u,0x58d0u,0x58d0u,0x58f0u,0x6890u,0x58e0u,0x58d8u,0x58f8u,0x6898u,0x58f0u,0x5960u,0x5940u,0x6920u,0x5960u,0x5968u,0x5948u,0x6928u,0x5970u,0x5970u,0x5950u,0x6930u,0x5940u,0x5978u,0x5958u,0x6938u,0x5950u,0x59e0u,0x59c0u,0x69a0u,0x59e0u,0x59e8u,0x59c8u,0x69a8u,0x59f0u,0x59f0u,0x59d0u,0x69b0u,0x59c0u,0x59f8u,0x59d8u,0x69b8u,0x59d0u,0x5c00u,0x5c20u,0x6c40u,0x5a40u,0x5c08u,0x5c28u,0x6c48u,0x5a50u,0x5c10u,0x5c30u,0x6c50u,0x5a60u,0x5c18u,0x5c38u,0x6c58u,0x5a70u,0x5c80u,0x5ca0u,0x6cc0u,0x5ac0u,0x5c88u,0x5ca8u,0x6cc8u,0x5ad0u,0x5c90u,0x5cb0u,0x6cd0u,0x5ae0u,0x5c98u,0x5cb8u,0x6cd8u,0x5af0u,0x5d20u,0x5d00u,0x6d60u,0x5b60u,0x5d28u,0x5d08u,0x6d68u,0x5b70u,0x5d30u,0x5d10u,0x6d70u,0x5b40u,0x5d38u,0x5d18u,0x6d78u,0x5b50u,0x5da0u,0x5d80u,0x6de0u,0x5be0u,0x5da8u,0x5d88u,0x6de8u,0x5bf0u,0x5db0u,0x5d90u,0x6df0u,0x5bc0u,0x5db8u,0x5d98u,0x6df8u,0x5bd0u,0x6040u,0x6060u,0x7000u,0x5c00u,0x6048u,0x6068u,0x7008u,0x5c10u,0x6050u,0x6070u,0x7010u,0x5c20u,0x6058u,0x6078u,0x7018u,0x5c30u,0x60c0u,0x60e0u,0x7080u,0x5c80u,0x60c8u,0x60e8u,0x7088u,0x5c90u,0x60d0u,0x60f0u,0x7090u,0x5ca0u,0x60d8u,0x60f8u,0x7098u,0x5cb0u,0x6160u,0x6140u,0x7120u,0x5d20u,0x6168u,0x6148u,0x7128u,0x5d30u,0x6170u,0x6150u,0x7130u,0x5d00u,0x6178u,0x6158u,0x7138u,0x5d10u,0x61e0u,0x61c0u,0x71a0u,0x5da0u,0x61e8u,0x61c8u,0x71a8u,0x5db0u,0x61f0u,0x61d0u,0x71b0u,0x5d80u,0x61f8u,0x61d8u,0x71b8u,0x5d90u,0x6400u,0x6420u,0x7440u,0x5e00u,0x6408u,0x6428u,0x7448u,0x5e10u,0x6410u,0x6430u,0x7450u,0x5e20u,0x6418u,0x6438u,0x7458u,0x5e30u,0x6480u,0x64a0u,0x74c0u,0x5e80u,0x6488u,0x64a8u,0x74c8u,0x5e90u,0x6490u,0x64b0u,0x74d0u,0x5ea0u,0x6498u,0x64b8u,0x74d8u,0x5eb0u,0x6520u,0x6500u,0x7560u,0x5f20u,0x6528u,0x6508u,0x7568u,0x5f30u,0x6530u,0x6510u,0x7570u,0x5f00u,0x6538u,0x6518u,0x7578u,0x5f10u,0x65a0u,0x6580u,0x75e0u,0x5fa0u,0x65a8u,0x6588u,0x75e8u,0x5fb0u,0x65b0u,0x6590u,0x75f0u,0x5f80u,0x65b8u,0x6598u,0x75f8u,0x5f90u,0x6800u,0x1e980u,0x1ebc0u,0x1800u,0x6808u,0x1ee10u,0x1f050u,0x1810u,0x6810u,0x1f2a0u,0x1f4e0u,0x1820u,0x6818u,0x1f730u,0x1f970u,0x1830u,0x6880u,0x1fbc0u,0x1fe00u,0x1840u,0x6888u,0x20050u,0x20290u,0x1850u,0x6890u,0x204e0u,0x20720u,0x1860u,0x6898u,0x20970u,0x20bb0u,0x1870u,0x6920u,0x20e00u,0x21040u,0x1980u,0x6928u,0x21290u,0x214d0u,0x1990u,0x6930u,0x21720u,0x21960u,0x19a0u,0x6938u,0x21bb0u,0x21df0u,0x19b0u,0x69a0u,0x22040u,0x22280u,0x19c0u,0x69a8u,0x224d0u,0x22710u,0x19d0u,0x69b0u,0x22960u,0x22ba0u,0x19e0u,0x69b8u,0x22df0u,0x23030u,0x19f0u,0x6a00u,0x1e9b0u,0x1ebc8u,0x1b20u,0x6a08u,0x1ee40u,0x1f058u,0x1b30u,0x6a10u,0x1f2d0u,0x1f4e8u,0x1b00u,0x6a18u,0x1f760u,0x1f978u,0x1b10u,0x6a80u,0x1fbf0u,0x1fe08u,0x1b60u,0x6a88u,0x20080u,0x20298u,0x1b70u,0x6a90u,0x20510u,0x20728u,0x1b40u,0x6a98u,0x209a0u,0x20bb8u,0x1b50u,0x6b20u,0x20e30u,0x21048u,0x1ca0u,0x6b28u,0x212c0u,0x214d8u,0x1cb0u,0x6b30u,0x21750u,0x21968u,0x1c80u,0x6b38u,0x21be0u,0x21df8u,0x1c90u,0x6ba0u,0x22070u,0x22288u,0x1ce0u,0x6ba8u,0x22500u,0x22718u,0x1cf0u,0x6bb0u,0x22990u,0x22ba8u,0x1cc0u,0x6bb8u,0x22e20u,0x23038u,0x1cd0u,0x6c40u,0x1e9e0u,0x1ebd0u,0x1e00u,0x6c48u,0x1ee70u,0x1f060u,0x1e10u,0x6c50u,0x1f300u,0x1f4f0u,0x1e20u,0x6c58u,0x1f790u,0x1f980u,0x1e30u,0x6cc0u,0x1fc20u,0x1fe10u,0x1e40u,0x6cc8u,0x200b0u,0x202a0u,0x1e50u,0x6cd0u,0x20540u,0x20730u,0x1e60u,0x6cd8u,0x209d0u,0x20bc0u,0x1e70u,0x6d60u,0x20e60u,0x21050u,0x1f80u,0x6d68u,0x212f0u,0x214e0u,0x1f90u,0x6d70u,0x21780u,0x21970u,0x1fa0u,0x6d78u,0x21c10u,0x21e00u,0x1fb0u,0x6de0u,0x220a0u,0x22290u,0x1fc0u,0x6de8u,0x22530u,0x22720u,0x1fd0u,0x6df0u,0x229c0u,0x22bb0u,0x1fe0u,0x6df8u,0x22e50u,0x23040u,0x1ff0u,0x6e40u,0x1ea10u,0x1ebd8u,0x2120u,0x6e48u,0x1eea0u,0x1f068u,0x2130u,0x6e50u,0x1f330u,0x1f4f8u,0x2100u,0x6e58u,0x1f7c0u,0x1f988u,0x2110u,0x6ec0u,0x1fc50u,0x1fe18u,0x2160u,0x6ec8u,0x200e0u,0x202a8u,0x2170u,0x6ed0u,0x20570u,0x20738u,0x2140u,0x6ed8u,0x20a00u,0x20bc8u,0x2150u,0x6f60u,0x20e90u,0x21058u,0x22a0u,0x6f68u,0x21320u,0x214e8u,0x22b0u,0x6f70u,0x217b0u,0x21978u,0x2280u,0x6f78u,0x21c40u,0x21e08u,0x2290u,0x6fe0u,0x220d0u,0x22298u,0x22e0u,0x6fe8u,0x22560u,0x22728u,0x22f0u,0x6ff0u,0x229f0u,0x22bb8u,0x22c0u,0x6ff8u,0x22e80u,0x23048u,0x22d0u,0x0u,0x40u,0x9000u,0xb400u,0x80u,0xc0u,0x9010u,0xb410u,0x110u,0x150u,0x9020u,0xb420u,0x190u,0x1d0u,0x9030u,0xb430u,0x220u,0x260u,0x9040u,0xb440u,0x2a0u,0x2e0u,0x9050u,0xb450u,0x330u,0x370u,0x9060u,0xb460u,0x3b0u,0x3f0u,0x9070u,0xb470u,0x440u,0x400u,0x9080u,0xb480u,0x4c0u,0x480u,0x9090u,0xb490u,0x550u,0x510u,0x90a0u,0xb4a0u,0x5d0u,0x590u,0x90b0u,0xb4b0u,0x660u,0x620u,0x90c0u,0xb4c0u,0x6e0u,0x6a0u,0x90d0u,0xb4d0u,0x770u,0x730u,0x90e0u,0xb4e0u,0x7f0u,0x7b0u,0x90f0u,0xb4f0u,0x10u,0x50u,0x9300u,0xb700u,0x90u,0xd0u,0x9310u,0xb710u,0x100u,0x140u,0x9320u,0xb720u,0x180u,0x1c0u,0x9330u,0xb730u,0x230u,0x270u,0x9340u,0xb740u,0x2b0u,0x2f0u,0x9350u,0xb750u,0x320u,0x360u,0x9360u,0xb760u,0x3a0u,0x3e0u,0x9370u,0xb770u,0x450u,0x410u,0x9380u,0xb780u,0x4d0u,0x490u,0x9390u,0xb790u,0x540u,0x500u,0x93a0u,0xb7a0u,0x5c0u,0x580u,0x93b0u,0xb7b0u,0x670u,0x630u,0x93c0u,0xb7c0u,0x6f0u,0x6b0u,0x93d0u,0xb7d0u,0x760u,0x720u,0x93e0u,0xb7e0u,0x7e0u,0x7a0u,0x93f0u,0xb7f0u,0x20u,0x60u,0x9600u,0xba00u,0xa0u,0xe0u,0x9610u,0xba10u,0x130u,0x170u,0x9620u,0xba20u,0x1b0u,0x1f0u,0x9630u,0xba30u,0x200u,0x240u,0x9640u,0xba40u,0x280u,0x2c0u,0x9650u,0xba50u,0x310u,0x350u,0x9660u,0xba60u,0x390u,0x3d0u,0x9670u,0xba70u,0x460u,0x420u,0x9680u,0xba80u,0x4e0u,0x4a0u,0x9690u,0xba90u,0x570u,0x530u,0x96a0u,0xbaa0u,0x5f0u,0x5b0u,0x96b0u,0xbab0u,0x640u,0x600u,0x96c0u,0xbac0u,0x6c0u,0x680u,0x96d0u,0xbad0u,0x750u,0x710u,0x96e0u,0xbae0u,0x7d0u,0x790u,0x96f0u,0xbaf0u,0x30u,0x70u,0x9900u,0xbd00u,0xb0u,0xf0u,0x9910u,0xbd10u,0x120u,0x160u,0x9920u,0xbd20u,0x1a0u,0x1e0u,0x9930u,0xbd30u,0x210u,0x250u,0x9940u,0xbd40u,0x290u,0x2d0u,0x9950u,0xbd50u,0x300u,0x340u,0x9960u,0xbd60u,0x380u,0x3c0u,0x9970u,0xbd70u,0x470u,0x430u,0x9980u,0xbd80u,0x4f0u,0x4b0u,0x9990u,0xbd90u,0x560u,0x520u,0x99a0u,0xbda0u,0x5e0u,0x5a0u,0x99b0u,0xbdb0u,0x650u,0x610u,0x99c0u,0xbdc0u,0x6d0u,0x690u,0x99d0u,0xbdd0u,0x740u,0x700u,0x99e0u,0xbde0u,0x7c0u,0x780u,0x99f0u,0xbdf0u,0xc000u,0x120c0u,0x12100u,0x12800u,0xc010u,0x12250u,0x12290u,0x12810u,0xc020u,0x123e0u,0x12420u,0x12820u,0xc030u,0x12570u,0x125b0u,0x12830u,0xc040u,0x12680u,0x12740u,0x12840u,0xc050u,0x12810u,0x128d0u,0x12850u,0xc060u,0x129a0u,0x12a60u,0x12860u,0xc070u,0x12b30u,0x12bf0u,0x12870u,0xc080u,0x12cc0u,0x12d00u,0x12880u,0xc090u,0x12e50u,0x12e90u,0x12890u,0xc0a0u,0x12fe0u,0x13020u,0x128a0u,0xc0b0u,0x13170u,0x131b0u,0x128b0u,0xc0c0u,0x13280u,0x13340u,0x128c0u,0xc0d0u,0x13410u,0x134d0u,0x128d0u,0xc0e0u,0x135a0u,0x13660u,0x128e0u,0xc0f0u,0x13730u,0x137f0u,0x128f0u,0xc300u,0x120d0u,0x12108u,0x12900u,0xc310u,0x12240u,0x12298u,0x12910u,0xc320u,0x123f0u,0x12428u,0x12920u,0xc330u,0x12560u,0x125b8u,0x12930u,0xc340u,0x12690u,0x12748u,0x12940u,0xc350u,0x12800u,0x128d8u,0x12950u,0xc360u,0x129b0u,0x12a68u,0x12960u,0xc370u,0x12b20u,0x12bf8u,0x12970u,0xc380u,0x12cd0u,0x12d08u,0x12980u,0xc390u,0x12e40u,0x12e98u,0x12990u,0xc3a0u,0x12ff0u,0x13028u,0x129a0u,0xc3b0u,0x13160u,0x131b8u,0x129b0u,0xc3c0u,0x13290u,0x13348u,0x129c0u,0xc3d0u,0x13400u,0x134d8u,0x129d0u,0xc3e0u,0x135b0u,0x13668u,0x129e0u,0xc3f0u,0x13720u,0x137f8u,0x129f0u,0xc600u,0x120e0u,0x12110u,0x12a00u,0xc610u,0x12270u,0x12280u,0x12a10u,0xc620u,0x123c0u,0x12430u,0x12a20u,0xc630u,0x12550u,0x125a0u,0x12a30u,0xc640u,0x126a0u,0x12750u,0x12a40u,0xc650u,0x12830u,0x128c0u,0x12a50u,0xc660u,0x12980u,0x12a70u,0x12a60u,0xc670u,0x12b10u,0x12be0u,0x12a70u,0xc680u,0x12ce0u,0x12d10u,0x12a80u,0xc690u,0x12e70u,0x12e80u,0x12a90u,0xc6a0u,0x12fc0u,0x13030u,0x12aa0u,0xc6b0u,0x13150u,0x131a0u,0x12ab0u,0xc6c0u,0x132a0u,0x13350u,0x12ac0u,0xc6d0u,0x13430u,0x134c0u,0x12ad0u,0xc6e0u,0x13580u,0x13670u,0x12ae0u,0xc6f0u,0x13710u,0x137e0u,0x12af0u,0xc900u,0x120f0u,0x12118u,0x12b00u,0xc910u,0x12260u,0x12288u,0x12b10u,0xc920u,0x123d0u,0x12438u,0x12b20u,0xc930u,0x12540u,0x125a8u,0x12b30u,0xc940u,0x126b0u,0x12758u,0x12b40u,0xc950u,0x12820u,0x128c8u,0x12b50u,0xc960u,0x12990u,0x12a78u,0x12b60u,0xc970u,0x12b00u,0x12be8u,0x12b70u,0xc980u,0x12cf0u,0x12d18u,0x12b80u,0xc990u,0x12e60u,0x12e88u,0x12b90u,0xc9a0u,0x12fd0u,0x13038u,0x12ba0u,0xc9b0u,0x13140u,0x131a8u,0x12bb0u,0xc9c0u,0x132b0u,0x13358u,0x12bc0u,0xc9d0u,0x13420u,0x134c8u,0x12bd0u,0xc9e0u,0x13590u,0x13678u,0x12be0u,0xc9f0u,0x13700u,0x137e8u,0x12bf0u,0x7800u,0x1c300u,0x1e980u,0x1ebc0u,0x7880u,0x1c310u,0x100000u,0x1ebc2u,0x7910u,0x1c320u,0x100000u,0x1ebc4u,0x7990u,0x1c330u,0x100000u,0x1ebc6u,0x7a20u,0x1c340u,0x1e980u,0x1ebc8u,0x7aa0u,0x1c350u,0x100000u,0x1ebcau,0x7b30u,0x1c360u,0x100000u,0x1ebccu,0x7bb0u,0x1c370u,0x100000u,0x1ebceu,0x7c40u,0x1c380u,0x1e980u,0x1ebd0u,0x7cc0u,0x1c390u,0x100000u,0x1ebd2u,0x7d50u,0x1c3a0u,0x100000u,0x1ebd4u,0x7dd0u,0x1c3b0u,0x100000u,0x1ebd6u,0x7e60u,0x1c3c0u,0x1e980u,0x1ebd8u,0x7ee0u,0x1c3d0u,0x100000u,0x1ebdau,0x7f70u,0x1c3e0u,0x100000u,0x1ebdcu,0x7ff0u,0x1c3f0u,0x100000u,0x1ebdeu,0x7810u,0x1c400u,0x1e9b0u,0x1ebc0u,0x7890u,0x1c410u,0x100000u,0x1ebc2u,0x7900u,0x1c420u,0x100000u,0x1ebc4u,0x7980u,0x1c430u,0x100000u,0x1ebc6u,0x7a30u,0x1c440u,0x1e9b0u,0x1ebc8u,0x7ab0u,0x1c450u,0x100000u,0x1ebcau,0x7b20u,0x1c460u,0x100000u,0x1ebccu,0x7ba0u,0x1c470u,0x100000u,0x1ebceu,0x7c50u,0x1c480u,0x1e9b0u,0x1ebd0u,0x7cd0u,0x1c490u,0x100000u,0x1ebd2u,0x7d40u,0x1c4a0u,0x100000u,0x1ebd4u,0x7dc0u,0x1c4b0u,0x100000u,0x1ebd6u,0x7e70u,0x1c4c0u,0x1e9b0u,0x1ebd8u,0x7ef0u,0x1c4d0u,0x100000u,0x1ebdau,0x7f60u,0x1c4e0u,0x100000u,0x1ebdcu,0x7fe0u,0x1c4f0u,0x100000u,0x1ebdeu,0x7820u,0x1c500u,0x1e9e0u,0x1ebc0u,0x78a0u,0x1c510u,0x100000u,0x1ebc2u,0x7930u,0x1c520u,0x100000u,0x1ebc4u,0x79b0u,0x1c530u,0x100000u,0x1ebc6u,0x7a00u,0x1c540u,0x1e9e0u,0x1ebc8u,0x7a80u,0x1c550u,0x100000u,0x1ebcau,0x7b10u,0x1c560u,0x100000u,0x1ebccu,0x7b90u,0x1c570u,0x100000u,0x1ebceu,0x7c60u,0x1c580u,0x1e9e0u,0x1ebd0u,0x7ce0u,0x1c590u,0x100000u,0x1ebd2u,0x7d70u,0x1c5a0u,0x100000u,0x1ebd4u,0x7df0u,0x1c5b0u,0x100000u,0x1ebd6u,0x7e40u,0x1c5c0u,0x1e9e0u,0x1ebd8u,0x7ec0u,0x1c5d0u,0x100000u,0x1ebdau,0x7f50u,0x1c5e0u,0x100000u,0x1ebdcu,0x7fd0u,0x1c5f0u,0x100000u,0x1ebdeu,0x7830u,0x1c600u,0x1ea10u,0x1ebc0u,0x78b0u,0x1c610u,0x100000u,0x1ebc2u,0x7920u,0x1c620u,0x100000u,0x1ebc4u,0x79a0u,0x1c630u,0x100000u,0x1ebc6u,0x7a10u,0x1c640u,0x1ea10u,0x1ebc8u,0x7a90u,0x1c650u,0x100000u,0x1ebcau,0x7b00u,0x1c660u,0x100000u,0x1ebccu,0x7b80u,0x1c670u,0x100000u,0x1ebceu,0x7c70u,0x1c680u,0x1ea10u,0x1ebd0u,0x7cf0u,0x1c690u,0x100000u,0x1ebd2u,0x7d60u,0x1c6a0u,0x100000u,0x1ebd4u,0x7de0u,0x1c6b0u,0x100000u,0x1ebd6u,0x7e50u,0x1c6c0u,0x1ea10u,0x1ebd8u,0x7ed0u,0x1c6d0u,0x100000u,0x1ebdau,0x7f40u,0x1c6e0u,0x100000u,0x1ebdcu,0x7fc0u,0x1c6f0u,0x100000u,0x1ebdeu,0x26500u,0x1d400u,0x1d400u,0x80000000u,0x26502u,0x1d402u,0x100000u,0x80000000u,0x26504u,0x1d404u,0x100000u,0x80000000u,0x26506u,0x1d406u,0x100000u,0x80000000u,0x26700u,0x1d410u,0x1d400u,0x80000000u,0x26702u,0x1d412u,0x100000u,0x80000000u,0x26704u,0x1d414u,0x100000u,0x80000000u,0x26706u,0x1d416u,0x100000u,0x80000000u,0x26900u,0x1d420u,0x1d400u,0x80000000u,0x26902u,0x1d422u,0x100000u,0x80000000u,0x26904u,0x1d424u,0x100000u,0x80000000u,0x26906u,0x1d426u,0x100000u,0x80000000u,0x26b00u,0x1d430u,0x1d400u,0x80000000u,0x26b02u,0x1d432u,0x100000u,0x80000000u,0x26b04u,0x1d434u,0x100000u,0x80000000u,0x26b06u,0x1d436u,0x100000u,0x80000000u,0x26508u,0x1d408u,0x1d410u,0x80000000u,0x2650au,0x1d40au,0x100000u,0x80000000u,0x2650cu,0x1d40cu,0x100000u,0x80000000u,0x2650eu,0x1d40eu,0x100000u,0x80000000u,0x26708u,0x1d418u,0x1d410u,0x80000000u,0x2670au,0x1d41au,0x100000u,0x80000000u,0x2670cu,0x1d41cu,0x100000u,0x80000000u,0x2670eu,0x1d41eu,0x100000u,0x80000000u,0x26908u,0x1d428u,0x1d410u,0x80000000u,0x2690au,0x1d42au,0x100000u,0x80000000u,0x2690cu,0x1d42cu,0x100000u,0x80000000u,0x2690eu,0x1d42eu,0x100000u,0x80000000u,0x26b08u,0x1d438u,0x1d410u,0x80000000u,0x26b0au,0x1d43au,0x100000u,0x80000000u,0x26b0cu,0x1d43cu,0x100000u,0x80000000u,0x26b0eu,0x1d43eu,0x100000u,0x80000000u,0x26d00u,0x1d440u,0x1d420u,0x80000000u,0x26d02u,0x1d442u,0x100000u,0x80000000u,0x26d04u,0x1d444u,0x100000u,0x80000000u,0x26d06u,0x1d446u,0x100000u,0x80000000u,0x26f00u,0x1d450u,0x1d420u,0x80000000u,0x26f02u,0x1d452u,0x100000u,0x80000000u,0x26f04u,0x1d454u,0x100000u,0x80000000u,0x26f06u,0x1d456u,0x100000u,0x80000000u,0x27100u,0x1d460u,0x1d420u,0x80000000u,0x27102u,0x1d462u,0x100000u,0x80000000u,0x27104u,0x1d464u,0x100000u,0x80000000u,0x27106u,0x1d466u,0x100000u,0x80000000u,0x27300u,0x1d470u,0x1d420u,0x80000000u,0x27302u,0x1d472u,0x100000u,0x80000000u,0x27304u,0x1d474u,0x100000u,0x80000000u,0x27306u,0x1d476u,0x100000u,0x80000000u,0x100000u,0x1d448u,0x1d430u,0x80000000u,0x100000u,0x1d44au,0x100000u,0x80000000u,0x100000u,0x1d44cu,0x100000u,0x80000000u,0x100000u,0x1d44eu,0x100000u,0x80000000u,0x100000u,0x1d458u,0x1d430u,0x80000000u,0x100000u,0x1d45au,0x100000u,0x80000000u,0x100000u,0x1d45cu,0x100000u,0x80000000u,0x100000u,0x1d45eu,0x100000u,0x80000000u,0x100000u,0x1d468u,0x1d430u,0x80000000u,0x100000u,0x1d46au,0x100000u,0x80000000u,0x100000u,0x1d46cu,0x100000u,0x80000000u,0x100000u,0x1d46eu,0x100000u,0x80000000u,0x100000u,0x1d478u,0x1d430u,0x80000000u,0x100000u,0x1d47au,0x100000u,0x80000000u,0x100000u,0x1d47cu,0x100000u,0x80000000u,0x100000u,0x1d47eu,0x100000u,0x80000000u,0x850u,0x60000u,0x90420u,0xefc50u,0x840u,0x60010u,0x90430u,0xefc40u,0x870u,0x60020u,0x90400u,0xefc70u,0x860u,0x60030u,0x90410u,0xefc60u,0x810u,0x60040u,0x90460u,0xefc10u,0x800u,0x60050u,0x90470u,0xefc00u,0x830u,0x60060u,0x90440u,0xefc30u,0x820u,0x60070u,0x90450u,0xefc20u,0x8d0u,0x60080u,0x904b0u,0xefcd0u,0x8c0u,0x60090u,0x904a0u,0xefcc0u,0x8f0u,0x600a0u,0x90490u,0xefcf0u,0x8e0u,0x600b0u,0x90480u,0xefce0u,0x890u,0x600c0u,0x904f0u,0xefc90u,0x880u,0x600d0u,0x904e0u,0xefc80u,0x8b0u,0x600e0u,0x904d0u,0xefcb0u,0x8a0u,0x600f0u,0x904c0u,0xefca0u,0x960u,0x60100u,0x90530u,0xefd60u,0x970u,0x60110u,0x90520u,0xefd70u,0x940u,0x60120u,0x90510u,0xefd40u,0x950u,0x60130u,0x90500u,0xefd50u,0x920u,0x60140u,0x90570u,0xefd20u,0x930u,0x60150u,0x90560u,0xefd30u,0x900u,0x60160u,0x90550u,0xefd00u,0x910u,0x60170u,0x90540u,0xefd10u,0x9e0u,0x60190u,0x905b0u,0xefde0u,0x9f0u,0x60180u,0x905a0u,0xefdf0u,0x9c0u,0x601b0u,0x90590u,0xefdc0u,0x9d0u,0x601a0u,0x90580u,0xefdd0u,0x9a0u,0x601d0u,0x905f0u,0xefda0u,0x9b0u,0x601c0u,0x905e0u,0xefdb0u,0x980u,0x601f0u,0x905d0u,0xefd80u,0x990u,0x601e0u,0x905c0u,0xefd90u,0xa60u,0x60210u,0xbfa40u,0xefe60u,0xa70u,0x60200u,0xbfa50u,0xefe70u,0xa40u,0x60230u,0xbfa60u,0xefe40u,0xa50u,0x60220u,0xbfa70u,0xefe50u,0xa20u,0x60250u,0xbfa00u,0xefe20u,0xa30u,0x60240u,0xbfa10u,0xefe30u,0xa00u,0x60270u,0xbfa20u,0xefe00u,0xa10u,0x60260u,0xbfa30u,0xefe10u,0x2fef0u,0x60290u,0xbfac0u,0xefef0u,0x2fee0u,0x60280u,0xbfad0u,0xefee0u,0x2fed0u,0x602b0u,0xbfae0u,0xefed0u,0x2fec0u,0x602a0u,0xbfaf0u,0xefec0u,0x2feb0u,0x602d0u,0xbfa80u,0xefeb0u,0x2fea0u,0x602c0u,0xbfa90u,0xefea0u,0x2fe90u,0x602f0u,0xbfaa0u,0xefe90u,0x2fe80u,0x602e0u,0xbfab0u,0xefe80u,0x2ff70u,0x60320u,0xbfb40u,0xeff70u,0x2ff60u,0x60330u,0xbfb50u,0xeff60u,0x2ff50u,0x60300u,0xbfb60u,0xeff50u,0x2ff40u,0x60310u,0xbfb70u,0xeff40u,0x2ff30u,0x60360u,0xbfb00u,0xeff30u,0x2ff20u,0x60370u,0xbfb10u,0xeff20u,0x2ff10u,0x60340u,0xbfb20u,0xeff10u,0x2ff00u,0x60350u,0xbfb30u,0xeff00u,0x2fff0u,0x603a0u,0xbfbd0u,0xefff0u,0x2ffe0u,0x603b0u,0xbfbc0u,0xeffe0u,0x2ffd0u,0x60380u,0xbfbf0u,0xeffd0u,0x2ffc0u,0x60390u,0xbfbe0u,0xeffc0u,0x2ffb0u,0x603e0u,0xbfb90u,0xeffb0u,0x2ffa0u,0x603f0u,0xbfb80u,0xeffa0u,0x2ff90u,0x603c0u,0xbfbb0u,0xeff90u,0x2ff80u,0x603d0u,0xbfba0u,0xeff80u,0x0u,0x80000000u,0x0u,0x18830u,0x0u,0x80000000u,0x0u,0x18830u,0x0u,0x80000000u,0x0u,0x18830u,0x0u,0x80000000u,0x0u,0x18830u,0x0u,0x80000000u,0x0u,0x18830u,0x0u,0x80000000u,0x0u,0x18830u,0x0u,0x80000000u,0x0u,0x18830u,0x0u,0x80000000u,0x0u,0x18830u,0x0u,0x80000000u,0x0u,0x18830u,0x0u,0x80000000u,0x0u,0x18830u,0x0u,0x80000000u,0x0u,0x18830u,0x0u,0x80000000u,0x0u,0x18830u,0x0u,0x80000000u,0x0u,0x18830u,0x0u,0x80000000u,0x0u,0x18830u,0x0u,0x80000000u,0x0u,0x18830u,0x0u,0x80000000u,0x0u,0x18830u,0x0u,0x80000000u,0x0u,0x188b2u,0x0u,0x80000000u,0x0u,0x188b2u,0x0u,0x80000000u,0x0u,0x188b2u,0x0u,0x80000000u,0x0u,0x188b2u,0x0u,0x80000000u,0x0u,0x188b2u,0x0u,0x80000000u,0x0u,0x188b2u,0x0u,0x80000000u,0x0u,0x188b2u,0x0u,0x80000000u,0x0u,0x188b2u,0x0u,0x80000000u,0x0u,0x18892u,0x0u,0x80000000u,0x0u,0x18892u,0x0u,0x80000000u,0x0u,0x18892u,0x0u,0x80000000u,0x0u,0x18892u,0x0u,0x80000000u,0x0u,0x18892u,0x0u,0x80000000u,0x0u,0x18892u,0x0u,0x80000000u,0x0u,0x18892u,0x0u,0x80000000u,0x0u,0x18892u,0x0u,0x80000000u,0x0u,0x1489au,0x0u,0x80000000u,0x0u,0x1489au,0x0u,0x80000000u,0x0u,0x1489au,0x0u,0x80000000u,0x0u,0x1489au,0x0u,0x80000000u,0x0u,0x1489au,0x0u,0x80000000u,0x0u,0x1489au,0x0u,0x80000000u,0x0u,0x1489au,0x0u,0x80000000u,0x0u,0x1489au,0x0u,0x80000000u,0x0u,0x34018u,0x0u,0x80000000u,0x0u,0x34018u,0x0u,0x80000000u,0x0u,0x34018u,0x0u,0x80000000u,0x0u,0x34018u,0x0u,0x80000000u,0x0u,0x34018u,0x0u,0x80000000u,0x0u,0x34018u,0x0u,0x80000000u,0x0u,0x34018u,0x0u,0x80000000u,0x0u,0x34018u,0x0u,0x80000000u,0x0u,0x36018u,0x0u,0x80000000u,0x0u,0x36018u,0x0u,0x80000000u,0x0u,0x36018u,0x0u,0x80000000u,0x0u,0x36018u,0x0u,0x80000000u,0x0u,0x36018u,0x0u,0x80000000u,0x0u,0x36018u,0x0u,0x80000000u,0x0u,0x36018u,0x0u,0x80000000u,0x0u,0x36018u,0x0u,0x80000000u,0x0u,0x36030u,0x0u,0x80000000u,0x0u,0x36030u,0x0u,0x80000000u,0x0u,0x36030u,0x0u,0x80000000u,0x0u,0x36030u,0x0u,0x80000000u,0x0u,0x36030u,0x0u,0x80000000u,0x0u,0x36030u,0x0u,0x80000000u,0x0u,0x36030u,0x0u,0x80000000u,0x0u,0x36030u,0x6840u,0x6860u,0x6820u,0x6820u,0x6848u,0x6868u,0x6828u,0x6830u,0x6850u,0x6870u,0x6830u,0x6840u,0x6858u,0x6878u,0x6838u,0x6850u,0x68c0u,0x68e0u,0x68a0u,0x68a0u,0x68c8u,0x68e8u,0x68a8u,0x68b0u,0x68d0u,0x68f0u,0x68b0u,0x68c0u,0x68d8u,0x68f8u,0x68b8u,0x68d0u,0x6960u,0x6940u,0x6900u,0x6900u,0x6968u,0x6948u,0x6908u,0x6910u,0x6970u,0x6950u,0x6910u,0x6960u,0x6978u,0x6958u,0x6918u,0x6970u,0x69e0u,0x69c0u,0x6980u,0x6980u,0x69e8u,0x69c8u,0x6988u,0x6990u,0x69f0u,0x69d0u,0x6990u,0x69e0u,0x69f8u,0x69d8u,0x6998u,0x69f0u,0x6c00u,0x6c20u,0x6c60u,0x6a20u,0x6c08u,0x6c28u,0x6c68u,0x6a30u,0x6c10u,0x6c30u,0x6c70u,0x6a40u,0x6c18u,0x6c38u,0x6c78u,0x6a50u,0x6c80u,0x6ca0u,0x6ce0u,0x6aa0u,0x6c88u,0x6ca8u,0x6ce8u,0x6ab0u,0x6c90u,0x6cb0u,0x6cf0u,0x6ac0u,0x6c98u,0x6cb8u,0x6cf8u,0x6ad0u,0x6d20u,0x6d00u,0x6d40u,0x6b00u,0x6d28u,0x6d08u,0x6d48u,0x6b10u,0x6d30u,0x6d10u,0x6d50u,0x6b60u,0x6d38u,0x6d18u,0x6d58u,0x6b70u,0x6da0u,0x6d80u,0x6dc0u,0x6b80u,0x6da8u,0x6d88u,0x6dc8u,0x6b90u,0x6db0u,0x6d90u,0x6dd0u,0x6be0u,0x6db8u,0x6d98u,0x6dd8u,0x6bf0u,0x7040u,0x7060u,0x7020u,0x6c60u,0x7048u,0x7068u,0x7028u,0x6c70u,0x7050u,0x7070u,0x7030u,0x6c00u,0x7058u,0x7078u,0x7038u,0x6c10u,0x70c0u,0x70e0u,0x70a0u,0x6ce0u,0x70c8u,0x70e8u,0x70a8u,0x6cf0u,0x70d0u,0x70f0u,0x70b0u,0x6c80u,0x70d8u,0x70f8u,0x70b8u,0x6c90u,0x7160u,0x7140u,0x7100u,0x6d40u,0x7168u,0x7148u,0x7108u,0x6d50u,0x7170u,0x7150u,0x7110u,0x6d20u,0x7178u,0x7158u,0x7118u,0x6d30u,0x71e0u,0x71c0u,0x7180u,0x6dc0u,0x71e8u,0x71c8u,0x7188u,0x6dd0u,0x71f0u,0x71d0u,0x7190u,0x6da0u,0x71f8u,0x71d8u,0x7198u,0x6db0u,0x7400u,0x7420u,0x7460u,0x6e60u,0x7408u,0x7428u,0x7468u,0x6e70u,0x7410u,0x7430u,0x7470u,0x6e00u,0x7418u,0x7438u,0x7478u,0x6e10u,0x7480u,0x74a0u,0x74e0u,0x6ee0u,0x7488u,0x74a8u,0x74e8u,0x6ef0u,0x7490u,0x74b0u,0x74f0u,0x6e80u,0x7498u,0x74b8u,0x74f8u,0x6e90u,0x7520u,0x7500u,0x7540u,0x6f40u,0x7528u,0x7508u,0x7548u,0x6f50u,0x7530u,0x7510u,0x7550u,0x6f20u,0x7538u,0x7518u,0x7558u,0x6f30u,0x75a0u,0x7580u,0x75c0u,0x6fc0u,0x75a8u,0x7588u,0x75c8u,0x6fd0u,0x75b0u,0x7590u,0x75d0u,0x6fa0u,0x75b8u,0x7598u,0x75d8u,0x6fb0u,0x6860u,0x1ea40u,0x1ec20u,0x2440u,0x6868u,0x1eed0u,0x1f0b0u,0x2450u,0x6870u,0x1f360u,0x1f540u,0x2460u,0x6878u,0x1f7f0u,0x1f9d0u,0x2470u,0x68e0u,0x1fc80u,0x1fe60u,0x2400u,0x68e8u,0x20110u,0x202f0u,0x2410u,0x68f0u,0x205a0u,0x20780u,0x2420u,0x68f8u,0x20a30u,0x20c10u,0x2430u,0x6940u,0x20ec0u,0x210a0u,0x25c0u,0x6948u,0x21350u,0x21530u,0x25d0u,0x6950u,0x217e0u,0x219c0u,0x25e0u,0x6958u,0x21c70u,0x21e50u,0x25f0u,0x69c0u,0x22100u,0x222e0u,0x2580u,0x69c8u,0x22590u,0x22770u,0x2590u,0x69d0u,0x22a20u,0x22c00u,0x25a0u,0x69d8u,0x22eb0u,0x23090u,0x25b0u,0x6a60u,0x1ea70u,0x1ec28u,0x2760u,0x6a68u,0x1ef00u,0x1f0b8u,0x2770u,0x6a70u,0x1f390u,0x1f548u,0x2740u,0x6a78u,0x1f820u,0x1f9d8u,0x2750u,0x6ae0u,0x1fcb0u,0x1fe68u,0x2720u,0x6ae8u,0x20140u,0x202f8u,0x2730u,0x6af0u,0x205d0u,0x20788u,0x2700u,0x6af8u,0x20a60u,0x20c18u,0x2710u,0x6b40u,0x20ef0u,0x210a8u,0x28e0u,0x6b48u,0x21380u,0x21538u,0x28f0u,0x6b50u,0x21810u,0x219c8u,0x28c0u,0x6b58u,0x21ca0u,0x21e58u,0x28d0u,0x6bc0u,0x22130u,0x222e8u,0x28a0u,0x6bc8u,0x225c0u,0x22778u,0x28b0u,0x6bd0u,0x22a50u,0x22c08u,0x2880u,0x6bd8u,0x22ee0u,0x23098u,0x2890u,0x6c20u,0x1eaa0u,0x1ec30u,0x2a40u,0x6c28u,0x1ef30u,0x1f0c0u,0x2a50u,0x6c30u,0x1f3c0u,0x1f550u,0x2a60u,0x6c38u,0x1f850u,0x1f9e0u,0x2a70u,0x6ca0u,0x1fce0u,0x1fe70u,0x2a00u,0x6ca8u,0x20170u,0x20300u,0x2a10u,0x6cb0u,0x20600u,0x20790u,0x2a20u,0x6cb8u,0x20a90u,0x20c20u,0x2a30u,0x6d00u,0x20f20u,0x210b0u,0x2bc0u,0x6d08u,0x213b0u,0x21540u,0x2bd0u,0x6d10u,0x21840u,0x219d0u,0x2be0u,0x6d18u,0x21cd0u,0x21e60u,0x2bf0u,0x6d80u,0x22160u,0x222f0u,0x2b80u,0x6d88u,0x225f0u,0x22780u,0x2b90u,0x6d90u,0x22a80u,0x22c10u,0x2ba0u,0x6d98u,0x22f10u,0x230a0u,0x2bb0u,0x6e20u,0x1ead0u,0x1ec38u,0x2d60u,0x6e28u,0x1ef60u,0x1f0c8u,0x2d70u,0x6e30u,0x1f3f0u,0x1f558u,0x2d40u,0x6e38u,0x1f880u,0x1f9e8u,0x2d50u,0x6ea0u,0x1fd10u,0x1fe78u,0x2d20u,0x6ea8u,0x201a0u,0x20308u,0x2d30u,0x6eb0u,0x20630u,0x20798u,0x2d00u,0x6eb8u,0x20ac0u,0x20c28u,0x2d10u,0x6f00u,0x20f50u,0x210b8u,0x2ee0u,0x6f08u,0x213e0u,0x21548u,0x2ef0u,0x6f10u,0x21870u,0x219d8u,0x2ec0u,0x6f18u,0x21d00u,0x21e68u,0x2ed0u,0x6f80u,0x22190u,0x222f8u,0x2ea0u,0x6f88u,0x22620u,0x22788u,0x2eb0u,0x6f90u,0x22ab0u,0x22c18u,0x2e80u,0x6f98u,0x22f40u,0x230a8u,0x2e90u,0x0u,0x40u,0x9000u,0xcc00u,0x80u,0xc0u,0x9010u,0xcc10u,0x110u,0x150u,0x9020u,0xcc20u,0x190u,0x1d0u,0x9030u,0xcc30u,0x220u,0x260u,0x9040u,0xcc40u,0x2a0u,0x2e0u,0x9050u,0xcc50u,0x330u,0x370u,0x9060u,0xcc60u,0x3b0u,0x3f0u,0x9070u,0xcc70u,0x440u,0x400u,0x9080u,0xcc80u,0x4c0u,0x480u,0x9090u,0xcc90u,0x550u,0x510u,0x90a0u,0xcca0u,0x5d0u,0x590u,0x90b0u,0xccb0u,0x660u,0x620u,0x90c0u,0xccc0u,0x6e0u,0x6a0u,0x90d0u,0xccd0u,0x770u,0x730u,0x90e0u,0xcce0u,0x7f0u,0x7b0u,0x90f0u,0xccf0u,0x10u,0x50u,0x9300u,0xcf00u,0x90u,0xd0u,0x9310u,0xcf10u,0x100u,0x140u,0x9320u,0xcf20u,0x180u,0x1c0u,0x9330u,0xcf30u,0x230u,0x270u,0x9340u,0xcf40u,0x2b0u,0x2f0u,0x9350u,0xcf50u,0x320u,0x360u,0x9360u,0xcf60u,0x3a0u,0x3e0u,0x9370u,0xcf70u,0x450u,0x410u,0x9380u,0xcf80u,0x4d0u,0x490u,0x9390u,0xcf90u,0x540u,0x500u,0x93a0u,0xcfa0u,0x5c0u,0x580u,0x93b0u,0xcfb0u,0x670u,0x630u,0x93c0u,0xcfc0u,0x6f0u,0x6b0u,0x93d0u,0xcfd0u,0x760u,0x720u,0x93e0u,0xcfe0u,0x7e0u,0x7a0u,0x93f0u,0xcff0u,0x20u,0x60u,0x9600u,0xd200u,0xa0u,0xe0u,0x9610u,0xd210u,0x130u,0x170u,0x9620u,0xd220u,0x1b0u,0x1f0u,0x9630u,0xd230u,0x200u,0x240u,0x9640u,0xd240u,0x280u,0x2c0u,0x9650u,0xd250u,0x310u,0x350u,0x9660u,0xd260u,0x390u,0x3d0u,0x9670u,0xd270u,0x460u,0x420u,0x9680u,0xd280u,0x4e0u,0x4a0u,0x9690u,0xd290u,0x570u,0x530u,0x96a0u,0xd2a0u,0x5f0u,0x5b0u,0x96b0u,0xd2b0u,0x640u,0x600u,0x96c0u,0xd2c0u,0x6c0u,0x680u,0x96d0u,0xd2d0u,0x750u,0x710u,0x96e0u,0xd2e0u,0x7d0u,0x790u,0x96f0u,0xd2f0u,0x30u,0x70u,0x9900u,0xd500u,0xb0u,0xf0u,0x9910u,0xd510u,0x120u,0x160u,0x9920u,0xd520u,0x1a0u,0x1e0u,0x9930u,0xd530u,0x210u,0x250u,0x9940u,0xd540u,0x290u,0x2d0u,0x9950u,0xd550u,0x300u,0x340u,0x9960u,0xd560u,0x380u,0x3c0u,0x9970u,0xd570u,0x470u,0x430u,0x9980u,0xd580u,0x4f0u,0x4b0u,0x9990u,0xd590u,0x560u,0x520u,0x99a0u,0xd5a0u,0x5e0u,0x5a0u,0x99b0u,0xd5b0u,0x650u,0x610u,0x99c0u,0xd5c0u,0x6d0u,0x690u,0x99d0u,0xd5d0u,0x740u,0x700u,0x99e0u,0xd5e0u,0x7c0u,0x780u,0x99f0u,0xd5f0u,0xc008u,0x12120u,0x12160u,0x12c00u,0xc018u,0x122b0u,0x122f0u,0x12c10u,0xc028u,0x12400u,0x12440u,0x12c20u,0xc038u,0x12590u,0x125d0u,0x12c30u,0xc048u,0x12760u,0x12720u,0x12c40u,0xc058u,0x128f0u,0x128b0u,0x12c50u,0xc068u,0x12a40u,0x12a00u,0x12c60u,0xc078u,0x12bd0u,0x12b90u,0x12c70u,0xc088u,0x12d20u,0x12d60u,0x12c80u,0xc098u,0x12eb0u,0x12ef0u,0x12c90u,0xc0a8u,0x13000u,0x13040u,0x12ca0u,0xc0b8u,0x13190u,0x131d0u,0x12cb0u,0xc0c8u,0x13360u,0x13320u,0x12cc0u,0xc0d8u,0x134f0u,0x134b0u,0x12cd0u,0xc0e8u,0x13640u,0x13600u,0x12ce0u,0xc0f8u,0x137d0u,0x13790u,0x12cf0u,0xc308u,0x12130u,0x12168u,0x12d00u,0xc318u,0x122a0u,0x122f8u,0x12d10u,0xc328u,0x12410u,0x12448u,0x12d20u,0xc338u,0x12580u,0x125d8u,0x12d30u,0xc348u,0x12770u,0x12728u,0x12d40u,0xc358u,0x128e0u,0x128b8u,0x12d50u,0xc368u,0x12a50u,0x12a08u,0x12d60u,0xc378u,0x12bc0u,0x12b98u,0x12d70u,0xc388u,0x12d30u,0x12d68u,0x12d80u,0xc398u,0x12ea0u,0x12ef8u,0x12d90u,0xc3a8u,0x13010u,0x13048u,0x12da0u,0xc3b8u,0x13180u,0x131d8u,0x12db0u,0xc3c8u,0x13370u,0x13328u,0x12dc0u,0xc3d8u,0x134e0u,0x134b8u,0x12dd0u,0xc3e8u,0x13650u,0x13608u,0x12de0u,0xc3f8u,0x137c0u,0x13798u,0x12df0u,0xc608u,0x12140u,0x12170u,0x12e00u,0xc618u,0x122d0u,0x122e0u,0x12e10u,0xc628u,0x12460u,0x12450u,0x12e20u,0xc638u,0x125f0u,0x125c0u,0x12e30u,0xc648u,0x12700u,0x12730u,0x12e40u,0xc658u,0x12890u,0x128a0u,0x12e50u,0xc668u,0x12a20u,0x12a10u,0x12e60u,0xc678u,0x12bb0u,0x12b80u,0x12e70u,0xc688u,0x12d40u,0x12d70u,0x12e80u,0xc698u,0x12ed0u,0x12ee0u,0x12e90u,0xc6a8u,0x13060u,0x13050u,0x12ea0u,0xc6b8u,0x131f0u,0x131c0u,0x12eb0u,0xc6c8u,0x13300u,0x13330u,0x12ec0u,0xc6d8u,0x13490u,0x134a0u,0x12ed0u,0xc6e8u,0x13620u,0x13610u,0x12ee0u,0xc6f8u,0x137b0u,0x13780u,0x12ef0u,0xc908u,0x12150u,0x12178u,0x12f00u,0xc918u,0x122c0u,0x122e8u,0x12f10u,0xc928u,0x12470u,0x12458u,0x12f20u,0xc938u,0x125e0u,0x125c8u,0x12f30u,0xc948u,0x12710u,0x12738u,0x12f40u,0xc958u,0x12880u,0x128a8u,0x12f50u,0xc968u,0x12a30u,0x12a18u,0x12f60u,0xc978u,0x12ba0u,0x12b88u,0x12f70u,0xc988u,0x12d50u,0x12d78u,0x12f80u,0xc998u,0x12ec0u,0x12ee8u,0x12f90u,0xc9a8u,0x13070u,0x13058u,0x12fa0u,0xc9b8u,0x131e0u,0x131c8u,0x12fb0u,0xc9c8u,0x13310u,0x13338u,0x12fc0u,0xc9d8u,0x13480u,0x134a8u,0x12fd0u,0xc9e8u,0x13630u,0x13618u,0x12fe0u,0xc9f8u,0x137a0u,0x13788u,0x12ff0u,0x8000u,0x1c300u,0x1ea40u,0x1ec20u,0x8080u,0x1c310u,0x100000u,0x1ec22u,0x8110u,0x1c320u,0x100000u,0x1ec24u,0x8190u,0x1c330u,0x100000u,0x1ec26u,0x8220u,0x1c340u,0x1ea40u,0x1ec28u,0x82a0u,0x1c350u,0x100000u,0x1ec2au,0x8330u,0x1c360u,0x100000u,0x1ec2cu,0x83b0u,0x1c370u,0x100000u,0x1ec2eu,0x8440u,0x1c380u,0x1ea40u,0x1ec30u,0x84c0u,0x1c390u,0x100000u,0x1ec32u,0x8550u,0x1c3a0u,0x100000u,0x1ec34u,0x85d0u,0x1c3b0u,0x100000u,0x1ec36u,0x8660u,0x1c3c0u,0x1ea40u,0x1ec38u,0x86e0u,0x1c3d0u,0x100000u,0x1ec3au,0x8770u,0x1c3e0u,0x100000u,0x1ec3cu,0x87f0u,0x1c3f0u,0x100000u,0x1ec3eu,0x8010u,0x1c400u,0x1ea70u,0x1ec20u,0x8090u,0x1c410u,0x100000u,0x1ec22u,0x8100u,0x1c420u,0x100000u,0x1ec24u,0x8180u,0x1c430u,0x100000u,0x1ec26u,0x8230u,0x1c440u,0x1ea70u,0x1ec28u,0x82b0u,0x1c450u,0x100000u,0x1ec2au,0x8320u,0x1c460u,0x100000u,0x1ec2cu,0x83a0u,0x1c470u,0x100000u,0x1ec2eu,0x8450u,0x1c480u,0x1ea70u,0x1ec30u,0x84d0u,0x1c490u,0x100000u,0x1ec32u,0x8540u,0x1c4a0u,0x100000u,0x1ec34u,0x85c0u,0x1c4b0u,0x100000u,0x1ec36u,0x8670u,0x1c4c0u,0x1ea70u,0x1ec38u,0x86f0u,0x1c4d0u,0x100000u,0x1ec3au,0x8760u,0x1c4e0u,0x100000u,0x1ec3cu,0x87e0u,0x1c4f0u,0x100000u,0x1ec3eu,0x8020u,0x1c500u,0x1eaa0u,0x1ec20u,0x80a0u,0x1c510u,0x100000u,0x1ec22u,0x8130u,0x1c520u,0x100000u,0x1ec24u,0x81b0u,0x1c530u,0x100000u,0x1ec26u,0x8200u,0x1c540u,0x1eaa0u,0x1ec28u,0x8280u,0x1c550u,0x100000u,0x1ec2au,0x8310u,0x1c560u,0x100000u,0x1ec2cu,0x8390u,0x1c570u,0x100000u,0x1ec2eu,0x8460u,0x1c580u,0x1eaa0u,0x1ec30u,0x84e0u,0x1c590u,0x100000u,0x1ec32u,0x8570u,0x1c5a0u,0x100000u,0x1ec34u,0x85f0u,0x1c5b0u,0x100000u,0x1ec36u,0x8640u,0x1c5c0u,0x1eaa0u,0x1ec38u,0x86c0u,0x1c5d0u,0x100000u,0x1ec3au,0x8750u,0x1c5e0u,0x100000u,0x1ec3cu,0x87d0u,0x1c5f0u,0x100000u,0x1ec3eu,0x8030u,0x1c600u,0x1ead0u,0x1ec20u,0x80b0u,0x1c610u,0x100000u,0x1ec22u,0x8120u,0x1c620u,0x100000u,0x1ec24u,0x81a0u,0x1c630u,0x100000u,0x1ec26u,0x8210u,0x1c640u,0x1ead0u,0x1ec28u,0x8290u,0x1c650u,0x100000u,0x1ec2au,0x8300u,0x1c660u,0x100000u,0x1ec2cu,0x8380u,0x1c670u,0x100000u,0x1ec2eu,0x8470u,0x1c680u,0x1ead0u,0x1ec30u,0x84f0u,0x1c690u,0x100000u,0x1ec32u,0x8560u,0x1c6a0u,0x100000u,0x1ec34u,0x85e0u,0x1c6b0u,0x100000u,0x1ec36u,0x8650u,0x1c6c0u,0x1ead0u,0x1ec38u,0x86d0u,0x1c6d0u,0x100000u,0x1ec3au,0x8740u,0x1c6e0u,0x100000u,0x1ec3cu,0x87c0u,0x1c6f0u,0x100000u,0x1ec3eu,0x27500u,0x1d480u,0x1d480u,0x80000000u,0x27502u,0x1d482u,0x100000u,0x80000000u,0x27504u,0x1d484u,0x100000u,0x80000000u,0x27506u,0x1d486u,0x100000u,0x80000000u,0x27700u,0x1d490u,0x1d480u,0x80000000u,0x27702u,0x1d492u,0x100000u,0x80000000u,0x27704u,0x1d494u,0x100000u,0x80000000u,0x27706u,0x1d496u,0x100000u,0x80000000u,0x27900u,0x1d4a0u,0x1d480u,0x80000000u,0x27902u,0x1d4a2u,0x100000u,0x80000000u,0x27904u,0x1d4a4u,0x100000u,0x80000000u,0x27906u,0x1d4a6u,0x100000u,0x80000000u,0x27b00u,0x1d4b0u,0x1d480u,0x80000000u,0x27b02u,0x1d4b2u,0x100000u,0x80000000u,0x27b04u,0x1d4b4u,0x100000u,0x80000000u,0x27b06u,0x1d4b6u,0x100000u,0x80000000u,0x27508u,0x1d488u,0x1d490u,0x80000000u,0x2750au,0x1d48au,0x100000u,0x80000000u,0x2750cu,0x1d48cu,0x100000u,0x80000000u,0x2750eu,0x1d48eu,0x100000u,0x80000000u,0x27708u,0x1d498u,0x1d490u,0x80000000u,0x2770au,0x1d49au,0x100000u,0x80000000u,0x2770cu,0x1d49cu,0x100000u,0x80000000u,0x2770eu,0x1d49eu,0x100000u,0x80000000u,0x27908u,0x1d4a8u,0x1d490u,0x80000000u,0x2790au,0x1d4aau,0x100000u,0x80000000u,0x2790cu,0x1d4acu,0x100000u,0x80000000u,0x2790eu,0x1d4aeu,0x100000u,0x80000000u,0x27b08u,0x1d4b8u,0x1d490u,0x80000000u,0x27b0au,0x1d4bau,0x100000u,0x80000000u,0x27b0cu,0x1d4bcu,0x100000u,0x80000000u,0x27b0eu,0x1d4beu,0x100000u,0x80000000u,0x26d08u,0x1d4c0u,0x1d4a0u,0x80000000u,0x26d0au,0x1d4c2u,0x100000u,0x80000000u,0x26d0cu,0x1d4c4u,0x100000u,0x80000000u,0x26d0eu,0x1d4c6u,0x100000u,0x80000000u,0x26f08u,0x1d4d0u,0x1d4a0u,0x80000000u,0x26f0au,0x1d4d2u,0x100000u,0x80000000u,0x26f0cu,0x1d4d4u,0x100000u,0x80000000u,0x26f0eu,0x1d4d6u,0x100000u,0x80000000u,0x27108u,0x1d4e0u,0x1d4a0u,0x80000000u,0x2710au,0x1d4e2u,0x100000u,0x80000000u,0x2710cu,0x1d4e4u,0x100000u,0x80000000u,0x2710eu,0x1d4e6u,0x100000u,0x80000000u,0x27308u,0x1d4f0u,0x1d4a0u,0x80000000u,0x2730au,0x1d4f2u,0x100000u,0x80000000u,0x2730cu,0x1d4f4u,0x100000u,0x80000000u,0x2730eu,0x1d4f6u,0x100000u,0x80000000u,0x100000u,0x1d4c8u,0x1d4b0u,0x80000000u,0x100000u,0x1d4cau,0x100000u,0x80000000u,0x100000u,0x1d4ccu,0x100000u,0x80000000u,0x100000u,0x1d4ceu,0x100000u,0x80000000u,0x100000u,0x1d4d8u,0x1d4b0u,0x80000000u,0x100000u,0x1d4dau,0x100000u,0x80000000u,0x100000u,0x1d4dcu,0x100000u,0x80000000u,0x100000u,0x1d4deu,0x100000u,0x80000000u,0x100000u,0x1d4e8u,0x1d4b0u,0x80000000u,0x100000u,0x1d4eau,0x100000u,0x80000000u,0x100000u,0x1d4ecu,0x100000u,0x80000000u,0x100000u,0x1d4eeu,0x100000u,0x80000000u,0x100000u,0x1d4f8u,0x1d4b0u,0x80000000u,0x100000u,0x1d4fau,0x100000u,0x80000000u,0x100000u,0x1d4fcu,0x100000u,0x80000000u,0x100000u,0x1d4feu,0x100000u,0x80000000u,0x30000u,0x60420u,0xbfc50u,0xf0000u,0x30010u,0x60430u,0xbfc40u,0xf0010u,0x30020u,0x60400u,0xbfc70u,0xf0020u,0x30030u,0x60410u,0xbfc60u,0xf0030u,0x30040u,0x60460u,0xbfc10u,0xf0040u,0x30050u,0x60470u,0xbfc00u,0xf0050u,0x30060u,0x60440u,0xbfc30u,0xf0060u,0x30070u,0x60450u,0xbfc20u,0xf0070u,0x30080u,0x604b0u,0xbfcd0u,0xf0080u,0x30090u,0x604a0u,0xbfcc0u,0xf0090u,0x300a0u,0x60490u,0xbfcf0u,0xf00a0u,0x300b0u,0x60480u,0xbfce0u,0xf00b0u,0x300c0u,0x604f0u,0xbfc90u,0xf00c0u,0x300d0u,0x604e0u,0xbfc80u,0xf00d0u,0x300e0u,0x604d0u,0xbfcb0u,0xf00e0u,0x300f0u,0x604c0u,0xbfca0u,0xf00f0u,0x30100u,0x60530u,0xbfd60u,0xf0100u,0x30110u,0x60520u,0xbfd70u,0xf0110u,0x30120u,0x60510u,0xbfd40u,0xf0120u,0x30130u,0x60500u,0xbfd50u,0xf0130u,0x30140u,0x60570u,0xbfd20u,0xf0140u,0x30150u,0x60560u,0xbfd30u,0xf0150u,0x30160u,0x60550u,0xbfd00u,0xf0160u,0x30170u,0x60540u,0xbfd10u,0xf0170u,0x30190u,0x605b0u,0xbfde0u,0xf0190u,0x30180u,0x605a0u,0xbfdf0u,0xf0180u,0x301b0u,0x60590u,0xbfdc0u,0xf01b0u,0x301a0u,0x60580u,0xbfdd0u,0xf01a0u,0x301d0u,0x605f0u,0xbfda0u,0xf01d0u,0x301c0u,0x605e0u,0xbfdb0u,0xf01c0u,0x301f0u,0x605d0u,0xbfd80u,0xf01f0u,0x301e0u,0x605c0u,0xbfd90u,0xf01e0u,0x30210u,0x60640u,0xbfe60u,0xf0210u,0x30200u,0x60650u,0xbfe70u,0xf0200u,0x30230u,0x60660u,0xbfe40u,0xf0230u,0x30220u,0x60670u,0xbfe50u,0xf0220u,0x30250u,0x60600u,0xbfe20u,0xf0250u,0x30240u,0x60610u,0xbfe30u,0xf0240u,0x30270u,0x60620u,0xbfe00u,0xf0270u,0x30260u,0x60630u,0xbfe10u,0xf0260u,0x30290u,0x606c0u,0xbfef0u,0xf0290u,0x30280u,0x606d0u,0xbfee0u,0xf0280u,0x302b0u,0x606e0u,0xbfed0u,0xf02b0u,0x302a0u,0x606f0u,0xbfec0u,0xf02a0u,0x302d0u,0x60680u,0xbfeb0u,0xf02d0u,0x302c0u,0x60690u,0xbfea0u,0xf02c0u,0x302f0u,0x606a0u,0xbfe90u,0xf02f0u,0x302e0u,0x606b0u,0xbfe80u,0xf02e0u,0x30320u,0x60740u,0xbff70u,0x11f720u,0x30330u,0x60750u,0xbff60u,0x11f730u,0x30300u,0x60760u,0xbff50u,0x11f700u,0x30310u,0x60770u,0xbff40u,0x11f710u,0x30360u,0x60700u,0xbff30u,0x11f760u,0x30370u,0x60710u,0xbff20u,0x11f770u,0x30340u,0x60720u,0xbff10u,0x11f740u,0x30350u,0x60730u,0xbff00u,0x11f750u,0x303a0u,0x8fbd0u,0xbfff0u,0x11f7a0u,0x303b0u,0x8fbc0u,0xbffe0u,0x11f7b0u,0x30380u,0x8fbf0u,0xbffd0u,0x11f780u,0x30390u,0x8fbe0u,0xbffc0u,0x11f790u,0x303e0u,0x8fb90u,0xbffb0u,0x11f7e0u,0x303f0u,0x8fb80u,0xbffa0u,0x11f7f0u,0x303c0u,0x8fbb0u,0xbff90u,0x11f7c0u,0x303d0u,0x8fba0u,0xbff80u,0x11f7d0u,0x0u,0x80000000u,0x0u,0x36030u,0x0u,0x80000000u,0x0u,0x36030u,0x0u,0x80000000u,0x0u,0x36030u,0x0u,0x80000000u,0x0u,0x36030u,0x0u,0x80000000u,0x0u,0x36030u,0x0u,0x80000000u,0x0u,0x36030u,0x0u,0x80000000u,0x0u,0x36030u,0x0u,0x80000000u,0x0u,0x36030u,0x0u,0x80000000u,0x0u,0x360b2u,0x0u,0x80000000u,0x0u,0x360b2u,0x0u,0x80000000u,0x0u,0x360b2u,0x0u,0x80000000u,0x0u,0x360b2u,0x0u,0x80000000u,0x0u,0x360b2u,0x0u,0x80000000u,0x0u,0x360b2u,0x0u,0x80000000u,0x0u,0x360b2u,0x0u,0x80000000u,0x0u,0x360b2u,0x0u,0x80000000u,0x0u,0x36092u,0x0u,0x80000000u,0x0u,0x36092u,0x0u,0x80000000u,0x0u,0x36092u,0x0u,0x80000000u,0x0u,0x36092u,0x0u,0x80000000u,0x0u,0x36092u,0x0u,0x80000000u,0x0u,0x36092u,0x0u,0x80000000u,0x0u,0x36092u,0x0u,0x80000000u,0x0u,0x36092u,0x0u,0x80000000u,0x0u,0x3609au,0x0u,0x80000000u,0x0u,0x3609au,0x0u,0x80000000u,0x0u,0x3609au,0x0u,0x80000000u,0x0u,0x3609au,0x0u,0x80000000u,0x0u,0x3609au,0x0u,0x80000000u,0x0u,0x3609au,0x0u,0x80000000u,0x0u,0x3609au,0x0u,0x80000000u,0x0u,0x3609au,0x0u,0x80000000u,0x0u,0x36059u,0x0u,0x80000000u,0x0u,0x36059u,0x0u,0x80000000u,0x0u,0x36059u,0x0u,0x80000000u,0x0u,0x36059u,0x0u,0x80000000u,0x0u,0x36059u,0x0u,0x80000000u,0x0u,0x36059u,0x0u,0x80000000u,0x0u,0x36059u,0x0u,0x80000000u,0x0u,0x36059u,0x0u,0x80000000u,0x0u,0x36059u,0x0u,0x80000000u,0x0u,0x36059u,0x0u,0x80000000u,0x0u,0x36059u,0x0u,0x80000000u,0x0u,0x36059u,0x0u,0x80000000u,0x0u,0x36059u,0x0u,0x80000000u,0x0u,0x36059u,0x0u,0x80000000u,0x0u,0x36059u,0x0u,0x80000000u,0x0u,0x36059u,0x0u,0x80000000u,0x0u,0x16071u,0x0u,0x80000000u,0x0u,0x16071u,0x0u,0x80000000u,0x0u,0x16071u,0x0u,0x80000000u,0x0u,0x16071u,0x0u,0x80000000u,0x0u,0x16071u,0x0u,0x80000000u,0x0u,0x16071u,0x0u,0x80000000u,0x0u,0x16071u,0x0u,0x80000000u,0x0u,0x16071u,0x0u,0x80000000u,0x0u,0x14071u,0x0u,0x80000000u,0x0u,0x14071u,0x0u,0x80000000u,0x0u,0x14071u,0x0u,0x80000000u,0x0u,0x14071u,0x0u,0x80000000u,0x0u,0x14071u,0x0u,0x80000000u,0x0u,0x14071u,0x0u,0x80000000u,0x0u,0x14071u,0x0u,0x80000000u,0x0u,0x14071u};

__global__ __launch_bounds__(256, 1) void swin_main(const uint16_t* x, const uint16_t* mask, char* ws, uint16_t* out,
                                                    const uint16_t* wqkv, const uint16_t* wo, const uint16_t* ls,
                                                    const uint16_t* w1, const uint16_t* w2, const uint16_t* rc,
                                                    const int* ridx) {
  __shared__ __attribute__((aligned(16))) char smem[LDS_BYTES];
  const int wave = __builtin_amdgcn_readfirstlane(threadIdx.x >> 6);
  const unsigned lds0 = (unsigned)(size_t)smem;
  asm volatile(
      ".p2align 5\n"
      "s_mov_b64 s[92:93], %0\n"
      "s_mov_b32 s40, %1\n"
      "s_mov_b32 s41, %2\n"
      "s_mov_b64 s[88:89], %3\n"
      "s_load_dwordx2 s[16:17], s[92:93], 0\n"
      "s_load_dwordx2 s[20:21], s[92:93], 32\n"
      "s_load_dwordx2 s[46:47], s[92:93], 40\n"
      "s_load_dwordx8 s[80:87], s[92:93], 48\n"
      "s_load_dwordx2 s[76:77], s[92:93], 80\n"
      "s_mov_b64 s[90:91], s[92:93]\n"
      "s_getpc_b64 s[98:99]\n"
      "L_ipf0_%=:\n"
      "v_mbcnt_lo_u32_b32 v8, -1, 0\n"
      "v_mbcnt_hi_u32_b32 v8, -1, v8\n"
      "v_lshlrev_b32 v240, 2, v8\n"
      "v_lshlrev_b32_e64 v175, 4, v8\n"
      "s_lshr_b32 s68, s41, 3\n"
      "s_mov_b32 s72, 0x3100119e\n"
      "s_mov_b32 s73, 0xc0bee051\n"
      "s_mov_b32 s70, 0x317e119e\n"
      "s_mov_b32 s71, 0xc06fe051\n"
      "s_and_b32 s92, s41, 7\n"
      "s_cmp_eq_u32 s92, 6\n"
      "s_cselect_b32 s93, 2, 17\n"
      "s_cmp_lt_u32 s68, s93\n"
      "s_cselect_b64 s[72:73], s[72:73], s[70:71]\n"
      "s_lshl_b32 s92, s92, 3\n"
      "s_lshr_b64 s[72:73], s[72:73], s92\n"
      "s_add_u32 s41, s72, s68\n"
      "s_and_b32 s41, s41, 255\n"
      "s_movk_i32 s96, 5073\n"
      "s_lshr_b32 s92, s41, 5\n"
      "s_cmp_eq_u32 s92, 6\n"
      "s_cselect_b32 s96, 4992, s96\n"
      "s_cmp_lt_u32 s41, 143\n"
      "s_cselect_b32 s96, 5024, s96\n"
      "s_cmp_lt_u32 s41, 192\n"
      "s_cselect_b32 s44, 20, 19\n"
      "s_cmp_lt_u32 s41, 17\n"
      "s_cselect_b32 s44, 21, s44\n"
      "s_cselect_b32 s96, 5167, s96\n"
      "s_add_u32 s96, s96, s41\n"
      "s_lshl_b32 s92, s40, 8\n"
      "v_add_u32_e64 v43, s92, v240\n"
      "v_min_u32 v29, 704, v43\n"
      "v_add_u32 v29, 4096, v29\n"
      "v_min_u32 v43, 672, v43\n"
      "v_add_u32 v58, 122112, v43\n"
      "s_lshl_b32 s92, s68, 7\n"
      "v_lshlrev_b32 v238, 7, v8\n"
      "s_mul_i32 s93, s92, 18\n"
      "s_mul_i32 s94, s40, 640\n"
      "v_min_u32 v241, 512, v238\n"
      "v_add_u32 v241, s94, v241\n"
      "v_min_u32 v241, 2176, v241\n"
      "v_add_u32 v241, s93, v241\n"
      "s_cmp_eq_u32 s40, 1\n"
      "s_cselect_b32 s93, 4096, 0\n"
      "s_add_u32 s92, s92, s93\n"
      "s_cmp_eq_u32 s40, 2\n"
      "s_cselect_b32 s93, 9, 8\n"
      "s_cselect_b64 s[72:73], s[98:99], s[88:89]\n"
      "s_cmp_lt_u32 s40, 2\n"
      "s_cselect_b32 s93, 27, s93\n"
      "s_mul_i32 s92, s92, s93\n"
      "s_lshl_b32 s93, s93, 7\n"
      "s_sub_u32 s93, s93, 128\n"
      "v_min_u32 v238, s93, v238\n"
      "v_add_u32 v238, s92, v238\n"
      "s_lshl_b32 s95, s40, 3\n"
      "v_lshrrev_b32 v239, 3, v8\n"
      "v_and_b32 v255, 7, v8\n"
      "v_add_u32 v254, s95, v239\n"
      "v_bfe_u32 v253, v254, 1, 3\n"
      "v_cmp_lt_u32 s[70:71], s44, v254\n"
      "v_min_u32_e64 v254, s44, v254\n"
      "s_mov_b32 s18, 100270464\n"
      "s_mov_b32 s19, 0x20000\n"
      "s_mov_b32 s78, 9604\n"
      "s_mov_b32 s79, 0x20000\n"
      "s_mov_b32 s22, 221184\n"
      "s_mov_b32 s23, 0x20000\n"
      "s_lshl_b32 s64, s40, 10\n"
      "v_add_u32 v36, s64, v175\n"
      "s_add_u32 s75, s64, 18432\n"
      "s_and_b32 s92, s40, 1\n"
      "s_lshl_b32 s92, s92, 2\n"
      "s_mul_i32 s93, s40, 9216\n"
      "v_and_b32 v252, 2, v239\n"
      "v_xor_b32 v252, v252, v255\n"
      "v_xor_b32 v252, s92, v252\n"
      "v_lshl_add_u32 v252, v252, 4, s93\n"
      "v_mul_u32_u24 v136, 1152, v239\n"
      "v_add_u32_e64 v136, v136, v252\n"
      "s_cmp_lt_u32 s40, 2\n"
      "s_cselect_b32 s65, s64, 163840\n"
      "s_waitcnt lgkmcnt(0)\n"
      "s_cselect_b64 s[72:73], s[20:21], s[72:73]\n"
      "global_load_dword v43, v43, s[86:87]\n"
      "global_load_dword a255, v238, s[72:73]\n"
      "global_load_dword a253, v241, s[46:47]\n"
      "s_mul_i32 s92, s40, 8192\n"
      "s_add_u32 s88, s88, s92\n"
      "s_addc_u32 s89, s89, 0\n"
      "s_add_u32 m0, s64, 124928\n"
      "s_movk_i32 s92, 0\n"
      "buffer_load_dwordx4 v36, s[76:79], s92 offen  lds\n"
      "s_add_u32 m0, s64, 129024\n"
      "s_movk_i32 s92, 4096\n"
      "buffer_load_dwordx4 v36, s[76:79], s92 offen  lds\n"
      "s_add_u32 m0, s64, 133120\n"
      "s_cmp_lt_u32 s40, 2\n"
      "s_cselect_b32 m0, m0, s65\n"
      "s_movk_i32 s92, 8192\n"
      "buffer_load_dwordx4 v36, s[76:79], s92 offen  lds\n"
      "s_load_dwordx16 s[0:15], s[82:83], 0\n"
      "s_load_dwordx16 s[48:63], s[84:85], 0\n"
      "s_load_dwordx16 s[24:39], s[84:85], 64\n"
      "s_load_dwordx2 s[66:67], s[80:81], 0\n"
      "s_movk_i32 s93, 5041\n"
      "s_cmp_lt_u32 s41, 224\n"
      "s_cselect_b32 s92, 192, 0\n"
      "s_cselect_b32 s93, 1184, s93\n"
      "s_cselect_b32 s94, 18, 17\n"
      "s_add_u32 s93, s93, s41\n"
      "v_lshl_add_u32 v238, v254, 8, s41\n"
      "v_mul_u32_u24 v241, s92, v254\n"
      "v_add_u32 v241, s93, v241\n"
      "v_cmp_lt_u32 vcc, s94, v254\n"
      "v_mov_b32_e64 v252, s96\n"
      "s_nop 0\n"
      "v_cndmask_b32 v238, v238, v241, vcc\n"
      "v_cmp_eq_u32 vcc, s44, v254\n"
      "s_nop 1\n"
      "v_cndmask_b32 v238, v238, v252, vcc\n"
      "v_subrev_u32 v252, 5184, v238\n"
      "v_cmp_le_u32 vcc, 5184, v238\n"
      "v_mul_u32_u24 v215, 58255, v238\n"
      "v_lshrrev_b32 v215, 22, v215\n"
      "v_mul_u32_u24 v107, 72, v215\n"
      "v_sub_u32 v107, v238, v107\n"
      "v_min_u32 v241, 72, v252\n"
      "v_cndmask_b32 v215, v215, v241, vcc\n"
      "v_subrev_u32 v241, 72, v252\n"
      "v_min_u32 v241, 72, v241\n"
      "v_cndmask_b32 v107, v107, v241, vcc\n"
      "v_mad_u32_u24 v238, v215, 7, 9\n"
      "v_subrev_u32 v241, 0x1ff, v238\n"
      "v_min_u32 v238, v238, v241\n"
      "v_mad_u32_u24 v241, v107, 7, 9\n"
      "v_subrev_u32 v252, 0x1ff, v241\n"
      "v_min_u32 v241, v241, v252\n"
      "v_mul_u32_u24 v238, 0x1ff, v238\n"
      "v_add_u32 v238, v238, v241\n"
      "v_mul_u32_u24 v241, 0x180, v238\n"
      "v_bfrev_b32 v254, 1\n"
      "v_cndmask_b32 v241, v241, v254, s[70:71]\n"
      "v_xor_b32 v253, v253, v255\n"
      "v_lshl_add_u32 v241, v253, 4, v241\n"
      "s_add_u32 m0, s64, 150784\n"
      "s_nop 0\n"
      "buffer_load_dwordx4 v241, s[16:19], 0 offen  lds\n"
      "s_add_u32 m0, s64, 154752\n"
      "s_nop 0\n"
      "buffer_load_dwordx4 v241, s[16:19], 0 offen offset:128  lds\n"
      "s_add_u32 m0, s64, 158720\n"
      "s_nop 0\n"
      "buffer_load_dwordx4 v241, s[16:19], 0 offen offset:256  lds\n"
      "s_movk_i32 s95, 504\n"
      "s_and_b32 s92, s40, 1\n"
      "s_lshl_b32 s92, s92, 2\n"
      "v_lshrrev_b32 v241, 1, v239\n"
      "v_xor_b32 v241, v241, v255\n"
      "v_xor_b32 v241, s92, v241\n"
      "v_lshlrev_b32_e64 v241, 4, v241\n"
      "s_lshl_b32 s92, s40, 3\n"
      "s_brev_b32 s43, 1\n"
      "s_cmp_lt_u32 s40, 2\n"
      "s_cselect_b32 s93, 32, -16\n"
      "s_cselect_b32 s94, 0, 128\n"
      "s_cselect_b32 s43, 256, s43\n"
      "v_add_u32 v238, s92, v239\n"
      "v_mul_u32_u24 v239, 37, v238\n"
      "v_lshrrev_b32_e64 v239, 8, v239\n"
      "v_mad_u32_u24 v239, v239, s95, v238\n"
      "v_mul_u32_u24 v239, 384, v239\n"
      "v_add_u32 v16, v239, v241\n"
      "v_add_u32 v239, s93, v238\n"
      "v_mul_u32_u24 v255, 37, v239\n"
      "v_lshrrev_b32 v255, 8, v255\n"
      "v_mad_u32_u24 v255, v255, s95, v239\n"
      "v_mul_u32_u24 v255, 384, v255\n"
      "v_add3_u32 v17, v255, v241, s94\n"
      "v_add_u32 v239, 16, v238\n"
      "v_mul_u32_u24 v238, 37, v239\n"
      "v_lshrrev_b32 v238, 8, v238\n"
      "v_mad_u32_u24 v238, v238, s95, v239\n"
      "v_mul_u32_u24 v238, 384, v238\n"
      "s_movk_i32 s94, 128\n"
      "v_add3_u32 v18, v238, v241, s94\n"
      "v_add_u32 v19, 256, v16\n"
      "v_add_u32 v15, s43, v17\n"
      "s_add_u32 s92, s41, 0\n"
      "s_mul_i32 s93, s92, 58255\n"
      "s_lshr_b32 s93, s93, 22\n"
      "s_mul_i32 s93, s93, 439\n"
      "s_add_u32 s45, s92, s93\n"
      "s_mul_i32 s45, s45, 2688\n"
      "s_add_u32 s45, s45, 589824\n"
      "s_add_u32 m0, s64, 0\n"
      "s_nop 0\n"
      "buffer_load_dwordx4 v16, s[16:19], s45 offen  lds\n"
      "s_add_u32 m0, s64, 4096\n"
      "s_nop 0\n"
      "buffer_load_dwordx4 v17, s[16:19], s45 offen  lds\n"
      "s_add_u32 m0, s64, 8192\n"
      "s_nop 0\n"
      "buffer_load_dwordx4 v18, s[16:19], s45 offen  lds\n"
      "s_add_u32 m0, s64, 12288\n"
      "s_nop 0\n"
      "buffer_load_dwordx4 v19, s[16:19], s45 offen  lds\n"
      "s_add_u32 m0, s65, 16384\n"
      "s_nop 0\n"
      "buffer_load_dwordx4 v15, s[16:19], s45 offen  lds\n"
      "s_add_u32 s92, s41, 0\n"
      "s_mul_i32 s93, s92, 58255\n"
      "s_lshr_b32 s93, s93, 22\n"
      "s_add_u32 s47, s92, s93\n"
      "s_mul_i32 s47, s47, 4802\n"
      "s_and_b32 s47, s47, -4\n"
      "s_add_u32 s92, s41, 256\n"
      "s_mul_i32 s93, s92, 58255\n"
      "s_lshr_b32 s93, s93, 22\n"
      "s_add_u32 s74, s92, s93\n"
      "s_mul_i32 s74, s74, 4802\n"
      "s_and_b32 s74, s74, -4\n"
      "s_mul_i32 s93, s93, 439\n"
      "s_add_u32 s46, s92, s93\n"
      "s_mul_i32 s46, s46, 2688\n"
      "s_add_u32 s46, s46, 589824\n"
      "v_subrev_u32 v238, 2, v8\n"
      "v_cmp_ge_u32 s[72:73], s44, v238\n"
      "v_min_u32 v238, s44, v238\n"
      "s_movk_i32 s93, 5041\n"
      "s_cmp_lt_u32 s41, 224\n"
      "s_cselect_b32 s92, 192, 0\n"
      "s_cselect_b32 s93, 1184, s93\n"
      "s_cselect_b32 s94, 18, 17\n"
      "s_add_u32 s93, s93, s41\n"
      "v_lshl_add_u32 v239, v238, 8, s41\n"
      "v_mul_u32_u24 v241, s92, v238\n"
      "v_add_u32 v241, s93, v241\n"
      "v_cmp_lt_u32 vcc, s94, v238\n"
      "v_mov_b32 v255, s96\n"
      "s_nop 0\n"
      "v_cndmask_b32 v239, v239, v241, vcc\n"
      "v_cmp_eq_u32 vcc, s44, v238\n"
      "s_nop 1\n"
      "v_cndmask_b32 v239, v239, v255, vcc\n"
      "v_subrev_u32 v255, 5184, v239\n"
      "v_cmp_le_u32 vcc, 5184, v239\n"
      "v_mul_u32_u24 v253, 58255, v239\n"
      "v_lshrrev_b32 v253, 22, v253\n"
      "v_mul_u32_u24 v254, 72, v253\n"
      "v_sub_u32 v254, v239, v254\n"
      "v_min_u32 v241, 72, v255\n"
      "v_cndmask_b32 v253, v253, v241, vcc\n"
      "v_subrev_u32 v241, 72, v255\n"
      "v_min_u32 v241, 72, v241\n"
      "v_cndmask_b32 v254, v254, v241, vcc\n"
      "v_mul_u32_u24 v38, 73, v253\n"
      "v_add_u32 v38, v38, v254\n"
      "v_mul_u32_u24 v38, 4802, v38\n"
      "v_mul_u32_u24 v238, 0x1ff, v253\n"
      "v_add_u32 v238, v238, v254\n"
      "v_mul_u32_u24 v238, 2688, v238\n"
      "v_add_u32 v238, 589824, v238\n"
      "v_cmp_eq_u32 vcc, 72, v253\n"
      "v_or_b32 v215, 0x80000000, v238\n"
      "s_nop 0\n"
      "v_cndmask_b32 v238, v238, v215, vcc\n"
      "v_cmp_eq_u32 vcc, 72, v254\n"
      "v_or_b32_e64 v215, 0x40000000, v238\n"
      "s_nop 0\n"
      "v_cndmask_b32 v238, v238, v215, vcc\n"
      "v_mov_b32 v241, 0x20000000\n"
      "v_cndmask_b32 v238, v241, v238, s[72:73]\n"
      "v_cndmask_b32 v38, v241, v38, s[72:73]\n"
      "v_mov_b32 v37, v241\n"
      "s_nop 1\n"
      "v_mov_b32_dpp v37, v238 wave_shr:1 row_mask:0xf bank_mask:0xf\n"
      "v_mov_b32 v79, 0\n"
      "v_mov_b32 v83, 0\n"
      "v_mov_b32 v87, 0\n"
      "v_mov_b32 v91, 0\n"
      "v_mov_b32 v95, 0\n"
      "s_lshl_b32 s94, s40, 2\n"
      "s_add_u32 s94, s94, 115456\n"
      "v_add_u32 v31, s94, v175\n"
      "v_and_b32 v215, 3, v8\n"
      "v_cmp_ne_u32 vcc, 0, v215\n"
      "v_mov_b32 v215, 0x3f803f80\n"
      "s_nop 0\n"
      "v_cndmask_b32_e64 v32, v215, 0, vcc\n"
      "v_mov_b32 v33, v32\n"
      "v_mov_b32 v34, v32\n"
      "v_mov_b32 v35, v32\n"
      "v_and_b32 v51, 15, v8\n"
      "v_lshrrev_b32_e64 v216, 4, v8\n"
      "v_mul_u32_u24 v51, 196, v51\n"
      "v_add_u32 v51, 124928, v51\n"
      "v_lshl_add_u32 v216, v216, 4, v51\n"
      "v_min_u32 v66, 192, v240\n"
      "v_add_u32 v66, 124928, v66\n"
      "global_load_dwordx4 v[238:241], v175, s[88:89]\n"
      "s_waitcnt lgkmcnt(0)\n"
      "s_pack_ll_b32_b16 s92, s0, s8\n"
      "s_waitcnt vmcnt(14)\n"
      "v_dot2_f32_bf16 v107, s92, v43, 0\n"
      "s_pack_hh_b32_b16 s93, s0, s8\n"
      "v_dot2_f32_bf16 v42, s93, v43, 0\n"
      "s_pack_ll_b32_b16 s94, s1, s9\n"
      "v_dot2_f32_bf16 v75, s94, v43, 0\n"
      "s_pack_hh_b32_b16 s95, s1, s9\n"
      "v_dot2_f32_bf16 v106, s95, v43, 0\n"
      "s_pack_ll_b32_b16 s92, s2, s10\n"
      "v_dot2_f32_bf16 v214, s92, v43, 0\n"
      "s_pack_hh_b32_b16 s93, s2, s10\n"
      "v_dot2_f32_bf16 v212, s93, v43, 0\n"
      "s_pack_ll_b32_b16 s94, s3, s11\n"
      "v_dot2_f32_bf16 v213, s94, v43, 0\n"
      "s_pack_hh_b32_b16 s95, s3, s11\n"
      "v_dot2_f32_bf16 v74, s95, v43, 0\n"
      "s_pack_ll_b32_b16 s92, s4, s12\n"
      "v_dot2_f32_bf16 v235, s92, v43, 0\n"
      "s_pack_hh_b32_b16 s93, s4, s12\n"
      "v_dot2_f32_bf16 v104, s93, v43, 0\n"
      "s_pack_ll_b32_b16 s94, s5, s13\n"
      "v_dot2_f32_bf16 v234, s94, v43, 0\n"
      "global_load_dwordx4 v[252:255], v175, s[88:89] offset:1024\n"
      "s_pack_hh_b32_b16 s95, s5, s13\n"
      "v_dot2_f32_bf16 v237, s95, v43, 0\n"
      "s_pack_ll_b32_b16 s92, s6, s14\n"
      "v_dot2_f32_bf16 v60, s92, v43, 0\n"
      "s_pack_hh_b32_b16 s93, s6, s14\n"
      "v_dot2_f32_bf16 v210, s93, v43, 0\n"
      "s_pack_ll_b32_b16 s94, s7, s15\n"
      "v_dot2_f32_bf16 v211, s94, v43, 0\n"
      "s_pack_hh_b32_b16 s95, s7, s15\n"
      "v_dot2_f32_bf16 v61, s95, v43, 0\n"
      "s_lshl_b32 s92, s40, 4\n"
      "s_lshr_b64 s[66:67], s[66:67], s92\n"
      "s_lshl_b32 s66, s66, 16\n"
      "v_mov_b32 v43, s66\n"
      "v_max_f32 v43, 0xc2c80000, v43\n"
      "v_min_f32 v43, 0x40930000, v43\n"
      "v_mul_f32 v43, 0x3fb8aa3b, v43\n"
      "v_exp_f32 v43, v43\n"
      "s_nop 0\n"
      "v_cvt_pk_bf16_f32 v43, v43, v43\n"
      "v_lshlrev_b32 v43, 16, v43\n"
      "v_max_f32 v107, 0, v107\n"
      "v_max_f32_e64 v42, 0, v42\n"
      "global_load_dwordx4 v[0:3], v175, s[88:89] offset:2048\n"
      "v_cvt_pk_bf16_f32 v107, v107, v42\n"
      "v_max_f32 v75, 0, v75\n"
      "v_max_f32 v106, 0, v106\n"
      "v_cvt_pk_bf16_f32 v75, v75, v106\n"
      "v_max_f32 v214, 0, v214\n"
      "v_max_f32 v212, 0, v212\n"
      "v_cvt_pk_bf16_f32 v214, v214, v212\n"
      "v_max_f32 v213, 0, v213\n"
      "v_max_f32 v74, 0, v74\n"
      "v_cvt_pk_bf16_f32 v213, v213, v74\n"
      "v_max_f32 v235, 0, v235\n"
      "v_max_f32 v104, 0, v104\n"
      "v_cvt_pk_bf16_f32 v235, v235, v104\n"
      "v_max_f32 v234, 0, v234\n"
      "v_max_f32 v237, 0, v237\n"
      "v_cvt_pk_bf16_f32 v234, v234, v237\n"
      "v_max_f32 v60, 0, v60\n"
      "v_max_f32 v210, 0, v210\n"
      "v_cvt_pk_bf16_f32 v60, v60, v210\n"
      "v_max_f32 v211, 0, v211\n"
      "v_max_f32 v61, 0, v61\n"
      "v_cvt_pk_bf16_f32 v211, v211, v61\n"
      "s_mov_b32 s42, 0\n"
      "s_add_u32 m0, s75, 0\n"
      "s_nop 0\n"
      "buffer_load_dwordx4 v136, s[20:23], s42 offen  lds\n"
      "s_pack_ll_b32_b16 s92, s48, s50\n"
      "v_dot2_f32_bf16 v42, s92, v107, 0\n"
      "s_pack_hh_b32_b16 s93, s48, s50\n"
      "v_dot2_f32_bf16 v74, s93, v107, 0\n"
      "s_pack_ll_b32_b16 s94, s49, s51\n"
      "v_dot2_f32_bf16 v212, s94, v107, 0\n"
      "s_pack_hh_b32_b16 s95, s49, s51\n"
      "v_dot2_f32_bf16 v104, s95, v107, 0\n"
      "s_pack_ll_b32_b16 s92, s52, s54\n"
      "v_dot2_f32_bf16 v42, s92, v75, v42\n"
      "s_pack_hh_b32_b16 s93, s52, s54\n"
      "v_dot2_f32_bf16 v74, s93, v75, v74\n"
      "s_pack_ll_b32_b16 s94, s53, s55\n"
      "v_dot2_f32_bf16 v212, s94, v75, v212\n"
      "s_pack_hh_b32_b16 s95, s53, s55\n"
      "v_dot2_f32_bf16 v104, s95, v75, v104\n"
      "s_pack_ll_b32_b16 s92, s56, s58\n"
      "v_dot2_f32_bf16 v42, s92, v214, v42\n"
      "s_pack_hh_b32_b16 s93, s56, s58\n"
      "v_dot2_f32_bf16 v74, s93, v214, v74\n"
      "s_pack_ll_b32_b16 s94, s57, s59\n"
      "v_dot2_f32_bf16 v212, s94, v214, v212\n"
      "s_add_u32 m0, s75, 3968\n"
      "s_nop 0\n"
      "buffer_load_dwordx4 v136, s[20:23], s42 offen offset:128  lds\n"
      "s_pack_hh_b32_b16 s95, s57, s59\n"
      "v_dot2_f32_bf16 v104, s95, v214, v104\n"
      "s_pack_ll_b32_b16 s92, s60, s62\n"
      "v_dot2_f32_bf16 v42, s92, v213, v42\n"
      "s_pack_hh_b32_b16 s93, s60, s62\n"
      "v_dot2_f32_bf16 v74, s93, v213, v74\n"
      "s_pack_ll_b32_b16 s94, s61, s63\n"
      "v_dot2_f32_bf16 v212, s94, v213, v212\n"
      "s_pack_hh_b32_b16 s95, s61, s63\n"
      "v_dot2_f32_bf16 v104, s95, v213, v104\n"
      "s_pack_ll_b32_b16 s92, s24, s26\n"
      "v_dot2_f32_bf16 v42, s92, v235, v42\n"
      "s_pack_hh_b32_b16 s93, s24, s26\n"
      "v_dot2_f32_bf16 v74, s93, v235, v74\n"
      "s_pack_ll_b32_b16 s94, s25, s27\n"
      "v_dot2_f32_bf16 v212, s94, v235, v212\n"
      "s_pack_hh_b32_b16 s95, s25, s27\n"
      "v_dot2_f32_bf16 v104, s95, v235, v104\n"
      "s_pack_ll_b32_b16 s92, s28, s30\n"
      "v_dot2_f32_bf16 v42, s92, v234, v42\n"
      "s_pack_hh_b32_b16 s93, s28, s30\n"
      "v_dot2_f32_bf16 v74, s93, v234, v74\n"
      "s_pack_ll_b32_b16 s94, s29, s31\n"
      "s_add_u32 m0, s75, 7936\n"
      "s_nop 0\n"
      "buffer_load_dwordx4 v136, s[20:23], s42 offen offset:256  lds\n"
      "v_dot2_f32_bf16 v212, s94, v234, v212\n"
      "s_pack_hh_b32_b16 s95, s29, s31\n"
      "v_dot2_f32_bf16 v104, s95, v234, v104\n"
      "s_pack_ll_b32_b16 s92, s32, s34\n"
      "v_dot2_f32_bf16 v42, s92, v60, v42\n"
      "s_pack_hh_b32_b16 s93, s32, s34\n"
      "v_dot2_f32_bf16 v74, s93, v60, v74\n"
      "s_pack_ll_b32_b16 s94, s33, s35\n"
      "v_dot2_f32_bf16 v212, s94, v60, v212\n"
      "s_pack_hh_b32_b16 s95, s33, s35\n"
      "v_dot2_f32_bf16 v104, s95, v60, v104\n"
      "s_pack_ll_b32_b16 s92, s36, s38\n"
      "v_dot2_f32_bf16 v42, s92, v211, v42\n"
      "s_pack_hh_b32_b16 s93, s36, s38\n"
      "v_dot2_f32_bf16 v74, s93, v211, v74\n"
      "s_pack_ll_b32_b16 s94, s37, s39\n"
      "v_dot2_f32_bf16 v212, s94, v211, v212\n"
      "s_pack_hh_b32_b16 s95, s37, s39\n"
      "v_dot2_f32_bf16 v104, s95, v211, v104\n"
      "s_load_dwordx2 s[28:29], s[90:91], 8\n"
      "s_load_dwordx2 s[34:35], s[90:91], 16\n"
      "s_load_dwordx2 s[24:25], s[90:91], 24\n"
      "s_add_u32 m0, s75, 11904\n"
      "s_nop 0\n"
      "buffer_load_dwordx4 v136, s[20:23], s42 offen offset:384  lds\n"
      "s_load_dwordx2 s[36:37], s[90:91], 40\n"
      "s_mov_b32 s26, 100270464\n"
      "s_mov_b32 s27, 0x20000\n"
      "s_mov_b32 s38, 73728\n"
      "s_mov_b32 s39, 0x20000\n"
      "s_mov_b32 s30, 25589860\n"
      "s_mov_b32 s31, 0x20000\n"
      "s_mov_b32 s53, 0x3fb8aa3b\n"
      "s_mov_b32 s51, 0x2a8cbccc\n"
      "s_mov_b32 s52, 0x38fba882\n"
      "s_mov_b32 s58, 0xffff0000\n"
      "s_mov_b32 s59, 0x0\n"
      "s_mov_b32 s60, 0x0\n"
      "s_mov_b32 s61, 0xffff\n"
      "s_mov_b32 s62, 0x0\n"
      "s_mov_b32 s63, 0xffff0000\n"
      "v_mov_b32 v215, 0x41f00000\n"
      "v_cmp_nle_f32 s[54:55], v43, v215\n"
      "v_mul_f32 v43, s53, v43\n"
      "s_nop 0\n"
      "v_readfirstlane_b32 s50, v43\n"
      "v_cvt_pk_bf16_f32 v42, v42, v42\n"
      "v_lshlrev_b32 v42, 16, v42\n"
      "s_add_u32 m0, s75, 15872\n"
      "s_nop 0\n"
      "buffer_load_dwordx4 v136, s[20:23], s42 offen offset:512  lds\n"
      "v_mul_f32 v42, 0xbfb8aa3b, v42\n"
      "v_exp_f32 v42, v42\n"
      "s_nop 0\n"
      "v_add_f32 v42, 1.0, v42\n"
      "v_rcp_f32 v42, v42\n"
      "s_nop 0\n"
      "v_cvt_pk_bf16_f32 v42, v42, v42\n"
      "v_lshlrev_b32 v42, 16, v42\n"
      "v_add_f32 v42, -0.5, v42\n"
      "v_mul_f32 v42, 0x41b8aa3b, v42\n"
      "ds_write_b32 v58, v42\n"
      "v_cvt_pk_bf16_f32 v74, v74, v74\n"
      "v_lshlrev_b32 v74, 16, v74\n"
      "v_mul_f32 v74, 0xbfb8aa3b, v74\n"
      "v_exp_f32 v74, v74\n"
      "s_nop 0\n"
      "v_add_f32 v74, 1.0, v74\n"
      "v_rcp_f32_e64 v74, v74\n"
      "s_nop 0\n"
      "v_cvt_pk_bf16_f32 v74, v74, v74\n"
      "v_lshlrev_b32 v74, 16, v74\n"
      "v_add_f32 v74, -0.5, v74\n"
      "v_mul_f32 v74, 0x41b8aa3b, v74\n"
      "ds_write_b32 v58, v74 offset:704\n"
      "v_cvt_pk_bf16_f32 v212, v212, v212\n"
      "v_lshlrev_b32 v212, 16, v212\n"
      "s_add_u32 m0, s75, 19840\n"
      "s_nop 0\n"
      "buffer_load_dwordx4 v136, s[20:23], s42 offen offset:640  lds\n"
      "v_mul_f32 v212, 0xbfb8aa3b, v212\n"
      "v_exp_f32 v212, v212\n"
      "s_nop 0\n"
      "v_add_f32 v212, 1.0, v212\n"
      "v_rcp_f32_e64 v212, v212\n"
      "s_nop 0\n"
      "v_cvt_pk_bf16_f32 v212, v212, v212\n"
      "v_lshlrev_b32 v212, 16, v212\n"
      "v_add_f32 v212, -0.5, v212\n"
      "v_mul_f32 v212, 0x41b8aa3b, v212\n"
      "ds_write_b32 v58, v212 offset:1408\n"
      "v_cvt_pk_bf16_f32 v104, v104, v104\n"
      "v_lshlrev_b32 v104, 16, v104\n"
      "v_mul_f32 v104, 0xbfb8aa3b, v104\n"
      "v_exp_f32 v104, v104\n"
      "s_nop 0\n"
      "v_add_f32 v104, 1.0, v104\n"
      "v_rcp_f32 v104, v104\n"
      "s_nop 0\n"
      "v_cvt_pk_bf16_f32 v104, v104, v104\n"
      "v_lshlrev_b32_e64 v104, 16, v104\n"
      "v_add_f32_e64 v104, -0.5, v104\n"
      "v_mul_f32 v104, 0x41b8aa3b, v104\n"
      "ds_write_b32 v58, v104 offset:2112\n"
      "s_mul_i32 s76, s40, 704\n"
      "s_add_u32 s76, s76, 122112\n"
      "s_mov_b32 s43, 36864\n"
      "s_add_u32 m0, s75, 24576\n"
      "s_nop 0\n"
      "buffer_load_dwordx4 v136, s[20:23], s43 offen  lds\n"
      "s_add_u32 m0, s75, 28544\n"
      "s_nop 0\n"
      "buffer_load_dwordx4 v136, s[20:23], s43 offen offset:128  lds\n"
      "s_add_u32 m0, s75, 32512\n"
      "s_nop 0\n"
      "buffer_load_dwordx4 v136, s[20:23], s43 offen offset:256  lds\n"
      "s_add_u32 m0, s75, 36480\n"
      "s_nop 0\n"
      "buffer_load_dwordx4 v136, s[20:23], s43 offen offset:384  lds\n"
      "s_add_u32 m0, s75, 40448\n"
      "s_nop 0\n"
      "buffer_load_dwordx4 v136, s[20:23], s43 offen offset:512  lds\n"
      "s_add_u32 m0, s75, 44416\n"
      "s_nop 0\n"
      "buffer_load_dwordx4 v136, s[20:23], s43 offen offset:640  lds\n"
      "s_mov_b32 s45, s46\n"
      "s_lshl_b32 s9, s68, 1\n"
      "s_add_u32 s9, s9, s40\n"
      "s_mul_i32 s9, s9, 640\n"
      "s_mov_b32 s49, s47\n"
      "s_mov_b32 s66, 0\n"
      "s_mov_b32 s67, 0\n"
      "s_mov_b32 s68, 0\n"
      "s_mov_b32 s69, 0\n"
      "s_mov_b32 s70, 0\n"
      "s_mov_b32 s71, 0\n"
      "s_mov_b32 s48, 0\n"
      "s_mov_b32 s33, 0x7fff7fff\n"
      "s_mov_b32 s42, 1\n"
      "s_mov_b32 s43, 5\n"
      "v_readlane_b32 s47, v38, 4\n"
      "s_nop 0\n"
      "s_and_b32 s47, s47, -4\n"
      "s_waitcnt vmcnt(12)\n"
      "v_add_u32 v243, 150784, v0\n"
      "v_add_u32 v173, 150784, v1\n"
      "v_add_u32 v202, 49152, v238\n"
      "v_add_u32 v203, 49152, v239\n"
      "v_add_u32 v204, 49152, v240\n"
      "v_add_u32 v78, 49152, v241\n"
      "v_add_u32 v186, 49152, v252\n"
      "s_waitcnt vmcnt(6)\n"
      "s_waitcnt lgkmcnt(0)\n"
      "s_barrier\n"
      "s_add_u32 m0, s75, 49152\n"
      "s_mov_b32 s78, 73728\n"
      "buffer_load_dwordx4 v136, s[20:23], s78 offen  lds\n"
      "s_add_u32 m0, s75, 53120\n"
      "ds_read_b128 a[228:231], v0 offset:4096\n"
      "ds_read_b64_tr_b16 a[122:123], v240 offset:12800\n"
      "ds_read_b128 a[216:219], v243\n"
      "ds_read_b128 a[224:227], v0 offset:2048\n"
      "ds_read_b128 a[220:223], v0\n"
      "buffer_load_dwordx4 v136, s[20:23], s78 offen offset:128  lds\n"
      "ds_read_b64_tr_b16 a[2:3], v238 offset:512\n"
      "ds_read_b64_tr_b16 a[26:27], v239 offset:512\n"
      "ds_read_b64_tr_b16 a[120:121], v240 offset:12288\n"
      "ds_read_b64_tr_b16 a[24:25], v239\n"
      "v_add_u32 v50, 6272, v216\n"
      "s_add_u32 m0, s75, 57088\n"
      "ds_read_b64_tr_b16 a[74:75], v238 offset:12800\n"
      "buffer_load_dwordx4 v136, s[20:23], s78 offen offset:256  lds\n"
      "ds_read_b64_tr_b16 a[72:73], v238 offset:12288\n"
      "ds_read_b64_tr_b16 a[48:49], v240\n"
      "ds_read_b64_tr_b16 a[0:1], v238\n"
      "s_add_u32 m0, s75, 61056\n"
      "ds_read_b64_tr_b16 a[96:97], v239 offset:12288\n"
      "ds_read_b128 a[236:239], v1\n"
      "s_mov_b32 s79, 110592\n"
      "buffer_load_dwordx4 v136, s[20:23], s78 offen offset:384  lds\n"
      "ds_read_b64_tr_b16 a[50:51], v240 offset:512\n"
      "s_add_u32 m0, s75, 65024\n"
      "ds_read2_b32 v[42:43], v216 offset0:2 offset1:3\n"
      "ds_read2_b32 v[74:75], v50 offset0:34 offset1:35\n"
      "ds_read_b64_tr_b16 a[98:99], v239 offset:12800\n"
      "v_add_u32 v64, 3136, v216\n"
      "s_waitcnt lgkmcnt(8)\n"
      "v_mfma_f32_16x16x32_bf16 v[212:215], a[120:123], a[220:223], 0\n"
      "buffer_load_dwordx4 v136, s[20:23], s78 offen offset:512  lds\n"
      "ds_read2_b32 v[58:59], v64 offset0:18 offset1:19\n"
      "v_mfma_f32_16x16x32_bf16 v[104:107], a[72:75], a[228:231], 0\n"
      "ds_read_b32 v192, v51 offset:6464\n"
      "s_waitcnt lgkmcnt(6)\n"
      "v_mfma_f32_16x16x32_bf16 v[208:211], a[0:3], a[220:223], 0\n"
      "ds_read_b128 a[240:243], v1 offset:2048\n"
      "s_add_u32 m0, s75, 68992\n"
      "v_mfma_f32_16x16x32_bf16 v[234:237], a[24:27], a[216:219], 0\n"
      "s_mov_b32 s83, 0x0\n"
      "buffer_load_dwordx4 v136, s[20:23], s78 offen offset:640  lds\n"
      "v_mfma_f32_16x16x32_bf16 v[142:145], a[72:75], a[224:227], 0\n"
      "ds_read2_b32 v[60:61], v64 offset0:32 offset1:33\n"
      "s_waitcnt lgkmcnt(4)\n"
      "v_mfma_f32_16x16x32_bf16 v[228:231], a[96:99], a[224:227], 0\n"
      "ds_read_b128 a[232:235], v173\n"
      "ds_read2_b32 v[44:45], v216 offset0:16 offset1:17\n"
      "v_mfma_f32_16x16x32_bf16 v[130:133], a[120:123], a[216:219], 0\n"
      "ds_read2_b32 v[40:41], v216 offset0:0 offset1:1\n"
      "global_load_dwordx4 v[4:7], v175, s[88:89] offset:3072\n"
      "s_waitcnt vmcnt(7)\n"
      "s_barrier\n"
      "ds_read_b64_tr_b16 a[100:101], v239 offset:36864\n"
      "v_mfma_f32_16x16x32_bf16 v[194:197], a[48:51], a[216:219], 0\n"
      "ds_read_b64_tr_b16 a[28:29], v239 offset:24576\n"
      "ds_read_b64_tr_b16 a[76:77], v238 offset:36864\n"
      "s_add_u32 s88, s88, 4096\n"
      "s_addc_u32 s89, s89, 0\n"
      "ds_read_b64_tr_b16 a[52:53], v240 offset:24576\n"
      "global_load_dwordx4 v[8:11], v175, s[88:89]\n"
      "ds_read_b64_tr_b16 a[54:55], v240 offset:25088\n"
      "ds_read_b64_tr_b16 a[4:5], v238 offset:24576\n"
      "ds_read_b64_tr_b16 a[126:127], v240 offset:37376\n"
      "ds_read_b64_tr_b16 a[124:125], v240 offset:36864\n"
      "ds_read_b64_tr_b16 a[6:7], v238 offset:25088\n"
      "ds_read_b64_tr_b16 a[30:31], v239 offset:25088\n"
      "ds_read_b64_tr_b16 a[78:79], v238 offset:37376\n"
      "ds_read_b64_tr_b16 a[102:103], v239 offset:37376\n"
      "v_mfma_f32_16x16x32_bf16 v[188:191], a[48:51], a[220:223], 0\n"
      "global_load_dwordx4 v[12:15], v175, s[88:89] offset:1024\n"
      "ds_read_b128 a[248:251], v243 offset:4096\n"
      "v_mfma_f32_16x16x32_bf16 v[244:247], a[24:27], a[228:231], 0\n"
      "ds_read2_b32 v[54:55], v64 offset0:2 offset1:3\n"
      "ds_read2_b32 v[72:73], v50 offset0:32 offset1:33\n"
      "v_mfma_f32_16x16x32_bf16 v[124:127], a[48:51], a[224:227], 0\n"
      "ds_read2_b32 v[56:57], v64 offset0:16 offset1:17\n"
      "v_lshl_add_u32 v42, v42, 2, s76\n"
      "v_mfma_f32_16x16x32_bf16 v[166:169], a[0:3], a[224:227], 0\n"
      "global_load_dwordx4 v[20:23], v175, s[88:89] offset:2048\n"
      "v_mfma_f32_16x16x32_bf16 v[150:153], a[120:123], a[228:231], 0\n"
      "ds_read_b128 a[244:247], v1 offset:4096\n"
      "s_add_u32 m0, s75, 73728\n"
      "ds_read2_b32 v[68:69], v50 offset0:16 offset1:17\n"
      "v_mfma_f32_16x16x32_bf16 v[248:251], a[96:99], a[220:223], 0\n"
      "s_waitcnt lgkmcnt(6)\n"
      "v_lshl_add_u32 v61, v61, 2, s76\n"
      "v_lshl_add_u32 v41, v41, 2, s76\n"
      "v_mfma_f32_16x16x32_bf16 v[224:227], a[96:99], a[228:231], 0\n"
      "ds_read_b32 v114, v51 offset:192\n"
      "global_load_dwordx4 v[24:27], v175, s[88:89] offset:3072\n"
      "ds_read_b128 a[252:255], v0 offset:6144\n"
      "v_mfma_f32_16x16x32_bf16 v[174:177], a[72:75], a[220:223], 0\n"
      "ds_read_b32 v222, v66 offset:9408\n"
      "ds_read2_b32 v[48:49], v216 offset0:32 offset1:33\n"
      "v_mfma_f32_16x16x32_bf16 v[130:133], a[124:127], a[232:235], v[130:133]\n"
      "v_lshl_add_u32 v75, v75, 2, s76\n"
      "v_lshl_add_u32 v58, v58, 2, s76\n"
      "v_mfma_f32_16x16x32_bf16 v[182:185], a[0:3], a[228:231], 0\n"
      "buffer_load_dwordx4 v136, s[20:23], s79 offen  lds\n"
      "s_mov_b32 s78, 147456\n"
      "s_add_u32 m0, s75, 77696\n"
      "v_mfma_f32_16x16x32_bf16 v[248:251], a[100:103], a[236:239], v[248:251]\n"
      "ds_read2_b32 v[66:67], v50 offset0:2 offset1:3\n"
      "v_lshl_add_u32 v45, v45, 2, s76\n"
      "v_lshl_add_u32 v40, v40, 2, s76\n"
      "v_mfma_f32_16x16x32_bf16 v[218:221], a[120:123], a[224:227], 0\n"
      "s_waitcnt lgkmcnt(3)\n"
      "v_readlane_b32 s84, v37, s43\n"
      "v_mov_b32 v155, 0xf149f2ca\n"
      "v_mfma_f32_16x16x32_bf16 v[208:211], a[4:7], a[236:239], v[208:211]\n"
      "buffer_load_dwordx4 v136, s[20:23], s79 offen offset:128  lds\n"
      "v_lshl_add_u32 v73, v73, 2, s76\n"
      "v_lshl_add_u32 v59, v59, 2, s76\n"
      "v_lshl_add_u32 v74, v74, 2, s76\n"
      "v_mfma_f32_16x16x32_bf16 v[108:111], a[24:27], a[220:223], 0\n"
      "ds_read2_b32 v[70:71], v50 offset0:18 offset1:19\n"
      "v_lshl_add_u32 v57, v57, 2, s76\n"
      "v_mfma_f32_16x16x32_bf16 v[108:111], a[28:31], a[236:239], v[108:111]\n"
      "s_add_u32 m0, s75, 81664\n"
      "ds_read_b32 v45, v45\n"
      "v_mfma_f32_16x16x32_bf16 v[104:107], a[76:79], a[244:247], v[104:107]\n"
      "buffer_load_dwordx4 v136, s[20:23], s79 offen offset:256  lds\n"
      "v_mfma_f32_16x16x32_bf16 v[178:181], a[72:75], a[216:219], 0\n"
      "ds_read2_b32 v[62:63], v64 offset0:34 offset1:35\n"
      "s_add_u32 m0, s75, 85632\n"
      "v_mfma_f32_16x16x32_bf16 v[146:149], a[0:3], a[216:219], 0\n"
      "ds_read_b128 a[220:223], v0 offset:10240\n"
      "v_lshl_add_u32 v68, v68, 2, s76\n"
      "v_mfma_f32_16x16x32_bf16 v[166:169], a[4:7], a[240:243], v[166:169]\n"
      "ds_read_b128 v[138:141], v173\n"
      "s_mov_b32 s91, 0x1ffff\n"
      "v_mfma_f32_16x16x32_bf16 v[194:197], a[52:55], a[232:235], v[194:197]\n"
      "buffer_load_dwordx4 v136, s[20:23], s79 offen offset:384  lds\n"
      "v_mfma_f32_16x16x32_bf16 v[120:123], a[96:99], a[216:219], 0\n"
      "ds_read_b128 a[216:219], v0 offset:8192\n"
      "s_add_u32 m0, s75, 89600\n"
      "v_mfma_f32_16x16x32_bf16 v[124:127], a[52:55], a[240:243], v[124:127]\n"
      "s_waitcnt vmcnt(9)\n"
      "s_barrier\n"
      "ds_read_b64_tr_b16 a[80:81], v202 offset:12288\n"
      "ds_read_b64_tr_b16 a[106:107], v203 offset:12800\n"
      "v_mfma_f32_16x16x32_bf16 v[160:163], a[48:51], a[228:231], 0\n"
      "ds_read_b64_tr_b16 a[10:11], v202 offset:512\n"
      "buffer_load_dwordx4 v136, s[20:23], s79 offen offset:512  lds\n"
      "ds_read_b64_tr_b16 a[32:33], v203\n"
      "ds_read_b64_tr_b16 a[58:59], v204 offset:512\n"
      "ds_read_b64_tr_b16 a[34:35], v203 offset:512\n"
      "s_add_u32 m0, s75, 93568\n"
      "ds_read_b64_tr_b16 a[56:57], v204\n"
      "v_mfma_f32_16x16x32_bf16 v[146:149], a[4:7], a[232:235], v[146:149]\n"
      "ds_read_b64_tr_b16 a[82:83], v202 offset:12800\n"
      "buffer_load_dwordx4 v136, s[20:23], s79 offen offset:640  lds\n"
      "ds_read_b64_tr_b16 a[130:131], v204 offset:12800\n"
      "v_mfma_f32_16x16x32_bf16 v[182:185], a[4:7], a[244:247], v[182:185]\n"
      "ds_read_b64_tr_b16 a[128:129], v204 offset:12288\n"
      "ds_read_b64_tr_b16 a[8:9], v202\n"
      "v_mfma_f32_16x16x32_bf16 v[100:103], a[24:27], a[224:227], 0\n"
      "ds_read_b64_tr_b16 a[104:105], v203 offset:12288\n"
      "s_add_u32 m0, s75, 0\n"
      "v_mfma_f32_16x16x32_bf16 v[142:145], a[76:79], a[240:243], v[142:145]\n"
      "buffer_load_dwordx4 v136, s[20:23], s78 offen  lds\n"
      "v_mfma_f32_16x16x32_bf16 v[178:181], a[76:79], a[232:235], v[178:181]\n"
      "s_add_u32 m0, s75, 3968\n"
      "ds_read2_b32 v[52:53], v64 offset0:0 offset1:1\n"
      "v_mfma_f32_16x16x32_bf16 v[228:231], a[100:103], a[240:243], v[228:231]\n"
      "ds_read2_b32 v[64:65], v50 offset0:0 offset1:1\n"
      "v_lshl_add_u32 v114, v114, 2, s76\n"
      "v_mfma_f32_16x16x32_bf16 v[212:215], a[124:127], a[236:239], v[212:215]\n"
      "ds_read_b32 v61, v61\n"
      "s_waitcnt lgkmcnt(5)\n"
      "v_mfma_f32_16x16x32_bf16 v[130:133], a[128:131], a[248:251], v[130:133]\n"
      "buffer_load_dwordx4 v136, s[20:23], s78 offen offset:128  lds\n"
      "v_readlane_b32 s85, v37, s42\n"
      "v_lshl_add_u32 v55, v55, 2, s76\n"
      "v_mfma_f32_16x16x32_bf16 v[218:221], a[124:127], a[240:243], v[218:221]\n"
      "ds_read_b128 a[228:231], v1 offset:6144\n"
      "s_mov_b32 s79, 184320\n"
      "s_add_u32 m0, s75, 7936\n"
      "v_mfma_f32_16x16x32_bf16 v[120:123], a[100:103], a[232:235], v[120:123]\n"
      "ds_read_b32 v154, v51 offset:3328\n"
      "v_lshl_add_u32 v43, v43, 2, s76\n"
      "v_mfma_f32_16x16x32_bf16 v[174:177], a[76:79], a[236:239], v[174:177]\n"
      "buffer_load_dwordx4 v136, s[20:23], s78 offen offset:256  lds\n"
      "v_mfma_f32_16x16x32_bf16 v[160:163], a[52:55], a[244:247], v[160:163]\n"
      "ds_read2_b32 v[50:51], v216 offset0:34 offset1:35\n"
      "v_lshl_add_u32 v63, v63, 2, s76\n"
      "v_mfma_f32_16x16x32_bf16 v[150:153], a[124:127], a[244:247], v[150:153]\n"
      "ds_read_b128 a[224:227], v173 offset:4096\n"
      "v_lshl_add_u32 v56, v56, 2, s76\n"
      "v_mfma_f32_16x16x32_bf16 v[244:247], a[28:31], a[244:247], v[244:247]\n"
      "s_add_u32 m0, s75, 11904\n"
      "v_lshl_add_u32 v48, v48, 2, s76\n"
      "v_lshl_add_u32 v192, v192, 2, s76\n"
      "v_mfma_f32_16x16x32_bf16 v[188:191], a[52:55], a[236:239], v[188:191]\n"
      "buffer_load_dwordx4 v136, s[20:23], s78 offen offset:384  lds\n"
      "v_mfma_f32_16x16x32_bf16 v[188:191], a[56:59], a[252:255], v[188:191]\n"
      "s_add_u32 m0, s75, 15872\n"
      "ds_read_b32 v192, v192\n"
      "v_mfma_f32_16x16x32_bf16 v[174:177], a[80:83], a[252:255], v[174:177]\n"
      "s_waitcnt lgkmcnt(3)\n"
      "v_lshl_add_u32 v154, v154, 2, s76\n"
      "v_lshl_add_u32 v54, v54, 2, s76\n"
      "v_mfma_f32_16x16x32_bf16 v[224:227], a[100:103], a[244:247], v[224:227]\n"
      "ds_read_b128 a[236:239], v1 offset:10240\n"
      "v_lshl_add_u32 v53, v53, 2, s76\n"
      "v_mfma_f32_16x16x32_bf16 v[244:247], a[32:35], a[220:223], v[244:247]\n"
      "buffer_load_dwordx4 v136, s[20:23], s78 offen offset:512  lds\n"
      "v_mfma_f32_16x16x32_bf16 v[178:181], a[80:83], a[248:251], v[178:181]\n"
      "s_add_u32 m0, s75, 19840\n"
      "v_lshl_add_u32 v70, v70, 2, s76\n"
      "v_lshl_add_u32 v69, v69, 2, s76\n"
      "v_mfma_f32_16x16x32_bf16 v[234:237], a[28:31], a[232:235], v[234:237]\n"
      "ds_read2_b32 v[46:47], v216 offset0:18 offset1:19\n"
      "v_lshl_add_u32 v52, v52, 2, s76\n"
      "v_mfma_f32_16x16x32_bf16 v[194:197], a[56:59], a[248:251], v[194:197]\n"
      "ds_read_b128 a[232:235], v1 offset:8192\n"
      "v_lshl_add_u32 v222, v222, 2, s76\n"
      "v_mfma_f32_16x16x32_bf16 v[124:127], a[56:59], a[216:219], v[124:127]\n"
      "buffer_load_dwordx4 v136, s[20:23], s78 offen offset:640  lds\n"
      "v_lshl_add_u32 v44, v44, 2, s76\n"
      "s_mov_b32 s82, 0xffff\n"
      "v_mfma_f32_16x16x32_bf16 v[150:153], a[128:131], a[220:223], v[150:153]\n"
      "ds_read_b32 v44, v44\n"
      "v_lshl_add_u32 v72, v72, 2, s76\n"
      "v_lshl_add_u32 v71, v71, 2, s76\n"
      "v_mfma_f32_16x16x32_bf16 v[224:227], a[104:107], a[220:223], v[224:227]\n"
      "s_add_u32 m0, s75, 24576\n"
      "ds_read_b128 a[244:247], v0 offset:12288\n"
      "v_mfma_f32_16x16x32_bf16 v[120:123], a[104:107], a[248:251], v[120:123]\n"
      "buffer_load_dwordx4 v136, s[20:23], s79 offen  lds\n"
      "s_add_u32 m0, s75, 28544\n"
      "v_mfma_f32_16x16x32_bf16 v[212:215], a[128:131], a[252:255], v[212:215]\n"
      "s_waitcnt vmcnt(7)\n"
      "s_barrier\n"
      "ds_read_b64_tr_b16 a[60:61], v204 offset:24576\n"
      "ds_read_b64_tr_b16 a[38:39], v203 offset:25088\n"
      "ds_read_b64_tr_b16 a[134:135], v204 offset:37376\n"
      "ds_read_b64_tr_b16 a[86:87], v202 offset:37376\n"
      "v_mfma_f32_16x16x32_bf16 v[160:163], a[56:59], a[220:223], v[160:163]\n"
      "buffer_load_dwordx4 v136, s[20:23], s79 offen offset:128  lds\n"
      "ds_read_b64_tr_b16 a[110:111], v203 offset:37376\n"
      "v_mfma_f32_16x16x32_bf16 v[234:237], a[32:35], a[248:251], v[234:237]\n"
      "ds_read_b64_tr_b16 a[132:133], v204 offset:36864\n"
      "s_add_u32 m0, s75, 32512\n"
      "ds_read_b64_tr_b16 a[108:109], v203 offset:36864\n"
      "ds_read_b64_tr_b16 a[84:85], v202 offset:36864\n"
      "ds_read_b64_tr_b16 a[12:13], v202 offset:24576\n"
      "ds_read_b64_tr_b16 a[36:37], v203 offset:24576\n"
      "v_mfma_f32_16x16x32_bf16 v[248:251], a[104:107], a[252:255], v[248:251]\n"
      "ds_read_b64_tr_b16 a[62:63], v204 offset:25088\n"
      "ds_read_b64_tr_b16 a[14:15], v202 offset:25088\n"
      "v_mfma_f32_16x16x32_bf16 v[208:211], a[8:11], a[252:255], v[208:211]\n"
      "buffer_load_dwordx4 v136, s[20:23], s79 offen offset:256  lds\n"
      "v_mfma_f32_16x16x32_bf16 v[182:185], a[8:11], a[220:223], v[182:185]\n"
      "s_mov_b32 s90, 0xffffffff\n"
      "s_add_u32 m0, s75, 36480\n"
      "v_lshl_add_u32 v62, v62, 2, s76\n"
      "v_mfma_f32_16x16x32_bf16 v[108:111], a[32:35], a[252:255], v[108:111]\n"
      "ds_read_b32 v40, v40\n"
      "v_lshl_add_u32 v60, v60, 2, s76\n"
      "v_lshl_add_u32 v65, v65, 2, s76\n"
      "v_mfma_f32_16x16x32_bf16 v[142:145], a[80:83], a[216:219], v[142:145]\n"
      "ds_read_b32 v57, v57\n"
      "v_lshl_add_u32 v64, v64, 2, s76\n"
      "v_mfma_f32_16x16x32_bf16 v[100:103], a[28:31], a[240:243], v[100:103]\n"
      "buffer_load_dwordx4 v136, s[20:23], s79 offen offset:384  lds\n"
      "v_mfma_f32_16x16x32_bf16 v[100:103], a[32:35], a[216:219], v[100:103]\n"
      "ds_read_b32 v73, v73\n"
      "s_waitcnt lgkmcnt(3)\n"
      "v_mfma_f32_16x16x32_bf16 v[100:103], a[36:39], a[232:235], v[100:103]\n"
      "ds_read_b32 v59, v59\n"
      "s_add_u32 m0, s75, 40448\n"
      "v_mfma_f32_16x16x32_bf16 v[108:111], a[36:39], a[228:231], v[108:111]\n"
      "ds_read_b32 v55, v55\n"
      "v_lshl_add_u32 v66, v66, 2, s76\n"
      "v_mfma_f32_16x16x32_bf16 v[208:211], a[12:15], a[228:231], v[208:211]\n"
      "buffer_load_dwordx4 v136, s[20:23], s79 offen offset:512  lds\n"
      "v_mfma_f32_16x16x32_bf16 v[104:107], a[80:83], a[220:223], v[104:107]\n"
      "ds_read_b32 v54, v54\n"
      "ds_read_b128 a[252:255], v0 offset:16384\n"
      "v_mfma_f32_16x16x32_bf16 v[188:191], a[60:63], a[228:231], v[188:191]\n"
      "ds_read_b32 v75, v75\n"
      "ds_read_b32 v62, v62\n"
      "s_add_u32 m0, s75, 44416\n"
      "v_mfma_f32_16x16x32_bf16 v[218:221], a[128:131], a[216:219], v[218:221]\n"
      "buffer_load_dwordx4 v136, s[20:23], s79 offen offset:640  lds\n"
      "v_mfma_f32_16x16x32_bf16 v[146:149], a[8:11], a[248:251], v[146:149]\n"
      "ds_read_b128 a[220:223], v1 offset:12288\n"
      "s_mov_b32 s78, 0\n"
      "v_mfma_f32_16x16x32_bf16 v[146:149], a[12:15], a[224:227], v[146:149]\n"
      "ds_read_b128 a[240:243], v243 offset:8192\n"
      "s_add_u32 m0, s75, 48384\n"
      "v_mfma_f32_16x16x32_bf16 v[244:247], a[36:39], a[236:239], v[244:247]\n"
      "ds_read_b32 v53, v53\n"
      "ds_read_b32 v48, v48\n"
      "v_mfma_f32_16x16x32_bf16 v[194:197], a[60:63], a[224:227], v[194:197]\n"
      "buffer_load_dwordx4 v136, s[20:23], s78 offen offset:768  lds\n"
      "v_mfma_f32_16x16x32_bf16 v[224:227], a[108:111], a[236:239], v[224:227]\n"
      "ds_read_b128 a[248:251], v0 offset:14336\n"
      "v_lshl_add_u32 v67, v67, 2, s76\n"
      "v_mfma_f32_16x16x32_bf16 v[178:181], a[84:87], a[224:227], v[178:181]\n"
      "ds_read_b32 v114, v114\n"
      "s_add_u32 m0, s75, 52352\n"
      "v_mfma_f32_16x16x32_bf16 v[218:221], a[132:135], a[232:235], v[218:221]\n"
      "ds_read_b32 v222, v222\n"
      "v_lshl_add_u32 v51, v51, 2, s76\n"
      "v_mfma_f32_16x16x32_bf16 v[142:145], a[84:87], a[232:235], v[142:145]\n"
      "buffer_load_dwordx4 v136, s[20:23], s78 offen offset:896  lds\n"
      "s_add_u32 m0, s75, 56320\n"
      "v_mfma_f32_16x16x32_bf16 v[166:169], a[8:11], a[216:219], v[166:169]\n"
      "ds_read_b32 v63, v63\n"
      "ds_read_b32 v67, v67\n"
      "v_mfma_f32_16x16x32_bf16 v[166:169], a[12:15], a[232:235], v[166:169]\n"
      "ds_read_b32 v72, v72\n"
      "ds_read_b32 v41, v41\n"
      "v_mfma_f32_16x16x32_bf16 v[228:231], a[104:107], a[216:219], v[228:231]\n"
      "buffer_load_dwordx4 v136, s[20:23], s78 offen offset:1024  lds\n"
      "v_mfma_f32_16x16x32_bf16 v[228:231], a[108:111], a[232:235], v[228:231]\n"
      "s_waitcnt vmcnt(9)\n"
      "s_barrier\n"
      "ds_read_b64_tr_b16 a[16:17], v238\n"
      "ds_read_b64_tr_b16 a[64:65], v240\n"
      "v_mfma_f32_16x16x32_bf16 v[174:177], a[84:87], a[228:231], v[174:177]\n"
      "ds_read_b64_tr_b16 a[42:43], v239 offset:512\n"
      "ds_read_b64_tr_b16 a[114:115], v239 offset:12800\n"
      "v_mfma_f32_16x16x32_bf16 v[248:251], a[108:111], a[228:231], v[248:251]\n"
      "ds_read_b64_tr_b16 a[66:67], v240 offset:512\n"
      "ds_read_b64_tr_b16 a[18:19], v238 offset:512\n"
      "v_mfma_f32_16x16x32_bf16 v[124:127], a[60:63], a[232:235], v[124:127]\n"
      "ds_read_b64_tr_b16 a[88:89], v238 offset:12288\n"
      "ds_read_b64_tr_b16 a[112:113], v239 offset:12288\n"
      "ds_read_b64_tr_b16 a[90:91], v238 offset:12800\n"
      "v_mfma_f32_16x16x32_bf16 v[234:237], a[36:39], a[224:227], v[234:237]\n"
      "ds_read_b64_tr_b16 a[138:139], v240 offset:12800\n"
      "ds_read_b64_tr_b16 a[136:137], v240 offset:12288\n"
      "v_mfma_f32_16x16x32_bf16 v[120:123], a[108:111], a[224:227], v[120:123]\n"
      "ds_read_b64_tr_b16 a[40:41], v239\n"
      "s_add_u32 m0, s75, 60672\n"
      "v_mfma_f32_16x16x32_bf16 v[150:153], a[132:135], a[236:239], v[150:153]\n"
      "s_mov_b32 s78, 36864\n"
      "ds_read_b32 v42, v42\n"
      "v_mfma_f32_16x16x32_bf16 v[182:185], a[12:15], a[236:239], v[182:185]\n"
      "buffer_load_dwordx4 v136, s[20:23], s78 offen offset:768  lds\n"
      "v_lshl_add_u32 v47, v47, 2, s76\n"
      "v_mfma_f32_16x16x32_bf16 v[104:107], a[84:87], a[236:239], v[104:107]\n"
      "ds_read_b32 v66, v66\n"
      "ds_read_b128 a[232:235], v243 offset:2048\n"
      "v_mfma_f32_16x16x32_bf16 v[212:215], a[132:135], a[228:231], v[212:215]\n"
      "s_add_u32 m0, s75, 64640\n"
      "ds_read_b128 a[216:219], v173 offset:8192\n"
      "v_mfma_f32_16x16x32_bf16 v[130:133], a[132:135], a[224:227], v[130:133]\n"
      "ds_read_b32 v60, v60\n"
      "s_waitcnt lgkmcnt(5)\n"
      "v_mfma_f32_16x16x32_bf16 v[182:185], a[16:19], a[252:255], v[182:185]\n"
      "buffer_load_dwordx4 v136, s[20:23], s78 offen offset:896  lds\n"
      "v_mfma_f32_16x16x32_bf16 v[248:251], a[112:115], a[244:247], v[248:251]\n"
      "ds_read_b32 v47, v47\n"
      "ds_read_b32 v70, v70\n"
      "v_mfma_f32_16x16x32_bf16 v[166:169], a[16:19], a[248:251], v[166:169]\n"
      "ds_read_b32 v68, v68\n"
      "v_lshl_add_u32 v49, v49, 2, s76\n"
      "v_mfma_f32_16x16x32_bf16 v[146:149], a[16:19], a[240:243], v[146:149]\n"
      "ds_read_b32 v65, v65\n"
      "s_add_u32 m0, s75, 68608\n"
      "v_mfma_f32_16x16x32_bf16 v[234:237], a[40:43], a[240:243], v[234:237]\n"
      "buffer_load_dwordx4 v136, s[20:23], s78 offen offset:1024  lds\n"
      "v_mfma_f32_16x16x32_bf16 v[104:107], a[88:91], a[252:255], v[104:107]\n"
      "ds_read_b32 v71, v71\n"
      "v_cndmask_b32 v30, v155, v222, s[90:91]\n"
      "v_mfma_f32_16x16x32_bf16 v[194:197], a[64:67], a[240:243], v[194:197]\n"
      "s_mov_b32 s79, 73728\n"
      "s_add_u32 m0, s75, 72960\n"
      "v_lshl_add_u32 v50, v50, 2, s76\n"
      "v_mfma_f32_16x16x32_bf16 v[208:211], a[16:19], a[244:247], v[208:211]\n"
      "ds_read_b32 v56, v56\n"
      "ds_read_b128 a[228:231], v1 offset:16384\n"
      "v_mfma_f32_16x16x32_bf16 v[142:145], a[88:91], a[248:251], v[142:145]\n"
      "buffer_load_dwordx4 v136, s[20:23], s79 offen offset:768  lds\n"
      "v_mfma_f32_16x16x32_bf16 v[178:181], a[88:91], a[240:243], v[178:181]\n"
      "ds_read_b32 v69, v69\n"
      "s_add_u32 m0, s75, 76928\n"
      "v_mfma_f32_16x16x32_bf16 v[212:215], a[136:139], a[244:247], v[212:215]\n"
      "ds_read_b32 v74, v74\n"
      "ds_read_b32 v154, v154\n"
      "ds_read_b32 v50, v50\n"
      "v_mfma_f32_16x16x32_bf16 v[188:191], a[64:67], a[244:247], v[188:191]\n"
      "buffer_load_dwordx4 v136, s[20:23], s79 offen offset:896  lds\n"
      "v_mfma_f32_16x16x32_bf16 v[124:127], a[64:67], a[248:251], v[124:127]\n"
      "ds_read_b32 v49, v49\n"
      "ds_read_b32 v58, v58\n"
      "v_mfma_f32_16x16x32_bf16 v[174:177], a[88:91], a[244:247], v[174:177]\n"
      "ds_read_b32 v43, v43\n"
      "v_cndmask_b32 v28, v155, v192, s[82:83]\n"
      "v_mfma_f32_16x16x32_bf16 v[108:111], a[40:43], a[244:247], v[108:111]\n"
      "s_add_u32 m0, s75, 80896\n"
      "ds_read_b128 a[224:227], v1 offset:14336\n"
      "v_mfma_f32_16x16x32_bf16 v[130:133], a[136:139], a[240:243], v[130:133]\n"
      "s_or_b32 s95, s84, s85\n"
      "ds_read_b32 v52, v52\n"
      "v_mfma_f32_16x16x32_bf16 v[120:123], a[112:115], a[240:243], v[120:123]\n"
      "buffer_load_dwordx4 v136, s[20:23], s79 offen offset:1024  lds\n"
      "v_mfma_f32_16x16x32_bf16 v[228:231], a[112:115], a[248:251], v[228:231]\n"
      "s_mov_b32 s79, 110592\n"
      "s_add_u32 m0, s75, 85248\n"
      "v_lshl_add_u32 v46, v46, 2, s76\n"
      "v_mfma_f32_16x16x32_bf16 v[244:247], a[40:43], a[252:255], v[244:247]\n"
      "s_waitcnt vmcnt(9)\n"
      "s_barrier\n"
      "ds_read_b64_tr_b16 a[20:21], v238 offset:24576\n"
      "ds_read_b64_tr_b16 a[22:23], v238 offset:25088\n"
      "v_mfma_f32_16x16x32_bf16 v[160:163], a[60:63], a[236:239], v[160:163]\n"
      "ds_read_b64_tr_b16 a[46:47], v239 offset:25088\n"
      "ds_read_b64_tr_b16 a[94:95], v238 offset:37376\n"
      "v_mfma_f32_16x16x32_bf16 v[160:163], a[64:67], a[252:255], v[160:163]\n"
      "s_mov_b32 s78, 147456\n"
      "ds_read_b64_tr_b16 a[44:45], v239 offset:24576\n"
      "ds_read_b64_tr_b16 a[92:93], v238 offset:36864\n"
      "v_mfma_f32_16x16x32_bf16 v[150:153], a[136:139], a[252:255], v[150:153]\n"
      "buffer_load_dwordx4 v136, s[20:23], s79 offen offset:768  lds\n"
      "v_mfma_f32_16x16x32_bf16 v[224:227], a[112:115], a[252:255], v[224:227]\n"
      "s_add_u32 m0, s75, 89216\n"
      "ds_read_b64_tr_b16 a[70:71], v240 offset:25088\n"
      "v_mfma_f32_16x16x32_bf16 v[100:103], a[40:43], a[248:251], v[100:103]\n"
      "ds_read_b64_tr_b16 a[118:119], v239 offset:37376\n"
      "s_waitcnt lgkmcnt(6)\n"
      "v_mfma_f32_16x16x32_bf16 v[166:169], a[20:23], a[224:227], v[166:169]\n"
      "ds_read_b64_tr_b16 a[116:117], v239 offset:36864\n"
      "v_cndmask_b32 v26, v155, v154, s[82:83]\n"
      "v_mfma_f32_16x16x32_bf16 v[182:185], a[20:23], a[228:231], v[182:185]\n"
      "buffer_load_dwordx4 v136, s[20:23], s79 offen offset:896  lds\n"
      "v_cndmask_b32 v24, v155, v114, s[82:83]\n"
      "v_mfma_f32_16x16x32_bf16 v[208:211], a[20:23], a[220:223], v[208:211]\n"
      "ds_read_b64_tr_b16 a[140:141], v240 offset:36864\n"
      "s_waitcnt lgkmcnt(4)\n"
      "v_mfma_f32_16x16x32_bf16 v[174:177], a[92:95], a[220:223], v[174:177]\n"
      "ds_read_b64_tr_b16 a[142:143], v240 offset:37376\n"
      "s_add_u32 m0, s75, 93184\n"
      "v_mfma_f32_16x16x32_bf16 v[178:181], a[92:95], a[216:219], v[178:181]\n"
      "ds_read_b64_tr_b16 a[68:69], v240 offset:24576\n"
      "v_fma_f32 v222, v182, v182, s51\n"
      "v_mfma_f32_16x16x32_bf16 v[146:149], a[20:23], a[216:219], v[146:149]\n"
      "buffer_load_dwordx4 v136, s[20:23], s79 offen offset:1024  lds\n"
      "v_mfma_f32_16x16x32_bf16 v[108:111], a[44:47], a[220:223], v[108:111]\n"
      "ds_read_b128 a[240:243], v243 offset:6144\n"
      "v_fma_f32 v216, v208, v208, s51\n"
      "v_mfma_f32_16x16x32_bf16 v[104:107], a[92:95], a[228:231], v[104:107]\n"
      "s_add_u32 m0, s75, -768\n"
      "v_fma_f32 v154, v166, v166, s51\n"
      "v_fma_f32 v192, v178, v178, s51\n"
      "v_mfma_f32_16x16x32_bf16 v[234:237], a[44:47], a[216:219], v[234:237]\n"
      "ds_read_b128 a[252:255], v173 offset:10240\n"
      "v_mul_f32 v199, v175, v175\n"
      "v_mfma_f32_16x16x32_bf16 v[112:115], a[0:3], a[232:235], 0\n"
      "buffer_load_dwordx4 v136, s[20:23], s78 offen offset:768  lds\n"
      "v_mul_f32 v155, v167, v167\n"
      "v_mfma_f32_16x16x32_bf16 v[218:221], a[136:139], a[248:251], v[218:221]\n"
      "s_add_u32 m0, s75, 3200\n"
      "v_mul_f32 v207, v147, v147\n"
      "s_waitcnt lgkmcnt(2)\n"
      "v_mfma_f32_16x16x32_bf16 v[130:133], a[140:143], a[216:219], v[130:133]\n"
      "ds_read_b32 v64, v64\n"
      "v_fma_f32 v206, v146, v146, s51\n"
      "v_mfma_f32_16x16x32_bf16 v[248:251], a[116:119], a[220:223], v[248:251]\n"
      "ds_read_b32 v46, v46\n"
      "v_mul_f32 v193, v179, v179\n"
      "v_mfma_f32_16x16x32_bf16 v[142:145], a[92:95], a[224:227], v[142:145]\n"
      "buffer_load_dwordx4 v136, s[20:23], s78 offen offset:896  lds\n"
      "v_mul_f32_e64 v217, v209, v209\n"
      "v_fma_f32 v198, v174, v174, s51\n"
      "v_pk_fma_f32 v[198:199], v[176:177], v[176:177], v[198:199]\n"
      "v_pk_fma_f32 v[192:193], v[180:181], v[180:181], v[192:193]\n"
      "v_mfma_f32_16x16x32_bf16 v[120:123], a[116:119], a[216:219], v[120:123]\n"
      "s_lshl_b32 s92, s40, 6\n"
      "ds_read_b32 v51, v51\n"
      "v_mfma_f32_16x16x32_bf16 v[150:153], a[140:143], a[228:231], v[150:153]\n"
      "ds_read_b128 a[244:247], v173 offset:6144\n"
      "s_add_u32 m0, s75, 7168\n"
      "v_mfma_f32_16x16x32_bf16 v[224:227], a[116:119], a[228:231], v[224:227]\n"
      "buffer_load_dwordx4 v136, s[20:23], s78 offen offset:1024  lds\n"
      "s_add_u32 m0, s75, 11520\n"
      "v_mul_f32_e64 v223, v183, v183\n"
      "v_mfma_f32_16x16x32_bf16 v[194:197], a[68:71], a[216:219], v[194:197]\n"
      "s_mov_b32 s78, 184320\n"
      "v_fma_f32 v84, v104, v104, s51\n"
      "v_fma_f32 v76, v142, v142, s51\n"
      "v_mfma_f32_16x16x32_bf16 v[188:191], a[68:71], a[220:223], v[188:191]\n"
      "ds_read_b128 a[236:239], v173 offset:2048\n"
      "v_mul_f32_e64 v77, v143, v143\n"
      "v_mfma_f32_16x16x32_bf16 v[212:215], a[140:143], a[220:223], v[212:215]\n"
      "ds_read_b128 a[248:251], v243 offset:10240\n"
      "buffer_load_dwordx4 v136, s[20:23], s78 offen offset:768  lds\n"
      "v_pk_fma_f32 v[192:193], v[120:121], v[120:121], v[192:193]\n"
      "v_pk_fma_f32 v[206:207], v[148:149], v[148:149], v[206:207]\n"
      "v_mul_f32_e64 v85, v105, v105\n"
      "v_pk_fma_f32 v[206:207], v[234:235], v[234:235], v[206:207]\n"
      "v_pk_fma_f32 v[216:217], v[210:211], v[210:211], v[216:217]\n"
      "v_pk_fma_f32 v[198:199], v[248:249], v[248:249], v[198:199]\n"
      "v_pk_fma_f32 v[206:207], v[236:237], v[236:237], v[206:207]\n"
      "v_pk_fma_f32 v[198:199], v[250:251], v[250:251], v[198:199]\n"
      "v_pk_fma_f32 v[192:193], v[122:123], v[122:123], v[192:193]\n"
      "v_pk_fma_f32 v[192:193], v[130:131], v[130:131], v[192:193]\n"
      "v_pk_fma_f32 v[216:217], v[108:109], v[108:109], v[216:217]\n"
      "v_pk_fma_f32 v[198:199], v[212:213], v[212:213], v[198:199]\n"
      "v_pk_fma_f32 v[198:199], v[214:215], v[214:215], v[198:199]\n"
      "v_pk_fma_f32 v[76:77], v[144:145], v[144:145], v[76:77]\n"
      "s_add_u32 m0, s75, 15488\n"
      "v_pk_fma_f32 v[84:85], v[106:107], v[106:107], v[84:85]\n"
      "v_pk_fma_f32 v[216:217], v[110:111], v[110:111], v[216:217]\n"
      "v_pk_fma_f32 v[154:155], v[168:169], v[168:169], v[154:155]\n"
      "v_pk_fma_f32 v[222:223], v[184:185], v[184:185], v[222:223]\n"
      "v_pk_fma_f32 v[216:217], v[188:189], v[188:189], v[216:217]\n"
      "v_pk_fma_f32 v[216:217], v[190:191], v[190:191], v[216:217]\n"
      "v_pk_fma_f32 v[192:193], v[132:133], v[132:133], v[192:193]\n"
      "v_pk_fma_f32 v[206:207], v[194:195], v[194:195], v[206:207]\n"
      "v_pk_fma_f32 v[84:85], v[224:225], v[224:225], v[84:85]\n"
      "v_pk_fma_f32 v[206:207], v[196:197], v[196:197], v[206:207]\n"
      "v_mfma_f32_16x16x32_bf16 v[228:231], a[116:119], a[224:227], v[228:231]\n"
      "buffer_load_dwordx4 v136, s[20:23], s78 offen offset:896  lds\n"
      "v_add_f32 v198, v198, v199\n"
      "s_waitcnt lgkmcnt(0)\n"
      "v_mfma_f32_16x16x32_bf16 v[112:115], a[4:7], a[236:239], v[112:115]\n"
      "s_add_u32 m0, s75, 19456\n"
      "v_add_f32 v192, v192, v193\n"
      "v_add_f32 v206, v206, v207\n"
      "v_mfma_f32_16x16x32_bf16 v[160:163], a[68:71], a[228:231], v[160:163]\n"
      "s_waitcnt vmcnt(11)\n"
      "s_barrier\n"
      "ds_read_b64_tr_b16 a[174:175], v203 offset:12800\n"
      "ds_read_b64_tr_b16 a[172:173], v203 offset:12288\n"
      "v_permlane16_swap_b32 v206, v192\n"
      "v_add_f32 v216, v216, v217\n"
      "v_add_f32_e64 v206, v206, v192\n"
      "v_mfma_f32_16x16x32_bf16 v[96:99], a[72:75], a[232:235], 0\n"
      "ds_read_b64_tr_b16 a[198:199], v204 offset:12800\n"
      "v_mov_b32 v192, v206\n"
      "v_mfma_f32_16x16x32_bf16 v[96:99], a[76:79], a[236:239], v[96:99]\n"
      "ds_read_b64_tr_b16 a[170:171], v203 offset:512\n"
      "v_permlane32_swap_b32 v206, v192\n"
      "v_mfma_f32_16x16x32_bf16 v[96:99], a[80:83], a[240:243], v[96:99]\n"
      "buffer_load_dwordx4 v136, s[20:23], s78 offen offset:1024  lds\n"
      "s_add_u32 m0, s75, 24576\n"
      "v_permlane16_swap_b32_e64 v216, v198\n"
      "v_pk_fma_f32 v[76:77], v[228:229], v[228:229], v[76:77]\n"
      "v_pk_fma_f32 v[84:85], v[226:227], v[226:227], v[84:85]\n"
      "v_mfma_f32_16x16x32_bf16 v[96:99], a[84:87], a[244:247], v[96:99]\n"
      "ds_read_b64_tr_b16 a[150:151], v202 offset:12800\n"
      "v_add_f32_e64 v216, v216, v198\n"
      "v_mov_b32_e64 v198, v216\n"
      "v_mfma_f32_16x16x32_bf16 v[218:221], a[140:143], a[224:227], v[218:221]\n"
      "ds_read_b64_tr_b16 a[168:169], v203\n"
      "ds_read_b64_tr_b16 a[146:147], v202 offset:512\n"
      "v_add_f32_e64 v206, v206, v192\n"
      "v_mfma_f32_16x16x32_bf16 v[124:127], a[68:71], a[224:227], v[124:127]\n"
      "ds_read_b64_tr_b16 a[144:145], v202\n"
      "v_permlane32_swap_b32 v216, v198\n"
      "v_mfma_f32_16x16x32_bf16 v[100:103], a[44:47], a[224:227], v[100:103]\n"
      "ds_read_b64_tr_b16 a[196:197], v204 offset:12288\n"
      "s_mov_b32 s79, 0\n"
      "v_mfma_f32_16x16x32_bf16 v[244:247], a[44:47], a[228:231], v[244:247]\n"
      "ds_read_b64_tr_b16 a[194:195], v204 offset:512\n"
      "ds_read_b64_tr_b16 a[148:149], v202 offset:12288\n"
      "ds_read_b64_tr_b16 a[192:193], v204\n"
      "v_mfma_f32_16x16x32_bf16 v[134:137], a[48:51], a[232:235], 0\n"
      "buffer_load_dwordx4 v255, s[36:39], s79 offen  lds\n"
      "v_rsq_f32_e64 v206, v206\n"
      "v_pk_fma_f32 v[154:155], v[100:101], v[100:101], v[154:155]\n"
      "v_pk_fma_f32 v[84:85], v[150:151], v[150:151], v[84:85]\n"
      "v_pk_fma_f32 v[84:85], v[152:153], v[152:153], v[84:85]\n"
      "v_pk_fma_f32 v[154:155], v[102:103], v[102:103], v[154:155]\n"
      "v_pk_fma_f32 v[222:223], v[244:245], v[244:245], v[222:223]\n"
      "s_add_u32 m0, s75, 28544\n"
      "v_mov_b32 v192, v206\n"
      "v_pk_fma_f32 v[76:77], v[230:231], v[230:231], v[76:77]\n"
      "v_pk_fma_f32 v[222:223], v[246:247], v[246:247], v[222:223]\n"
      "v_add_f32 v84, v84, v85\n"
      "v_mfma_f32_16x16x32_bf16 v[112:115], a[8:11], a[240:243], v[112:115]\n"
      "buffer_load_dwordx4 v255, s[36:39], s79 offen offset:128  lds\n"
      "v_permlane16_swap_b32 v206, v192\n"
      "v_pk_fma_f32 v[76:77], v[218:219], v[218:219], v[76:77]\n"
      "v_mul_f32 v206, s50, v206\n"
      "v_pk_fma_f32 v[76:77], v[220:221], v[220:221], v[76:77]\n"
      "v_pk_mul_f32 v[194:195], v[194:195], v[206:207] op_sel_hi:[1,0]\n"
      "v_pk_mul_f32 v[178:179], v[178:179], v[192:193] op_sel_hi:[1,0]\n"
      "v_pk_fma_f32 v[154:155], v[124:125], v[124:125], v[154:155]\n"
      "v_add_f32_e64 v76, v76, v77\n"
      "v_pk_mul_f32 v[146:147], v[146:147], v[206:207] op_sel_hi:[1,0]\n"
      "v_pk_fma_f32 v[222:223], v[160:161], v[160:161], v[222:223]\n"
      "v_pk_fma_f32 v[154:155], v[126:127], v[126:127], v[154:155]\n"
      "v_add_f32_e64 v216, v216, v198\n"
      "v_mfma_f32_16x16x32_bf16 v[112:115], a[12:15], a[244:247], v[112:115]\n"
      "s_add_u32 m0, s75, 32512\n"
      "v_rsq_f32 v216, v216\n"
      "v_mfma_f32_16x16x32_bf16 v[156:159], a[24:27], a[232:235], 0\n"
      "s_waitcnt lgkmcnt(0)\n"
      "v_add_f32 v154, v154, v155\n"
      "v_mov_b32 v198, v216\n"
      "v_mfma_f32_16x16x32_bf16 v[156:159], a[28:31], a[236:239], v[156:159]\n"
      "buffer_load_dwordx4 v255, s[36:39], s79 offen offset:256  lds\n"
      "v_permlane16_swap_b32_e64 v216, v198\n"
      "v_pk_fma_f32 v[222:223], v[162:163], v[162:163], v[222:223]\n"
      "v_add_f32 v222, v222, v223\n"
      "v_pk_mul_f32 v[214:215], v[214:215], v[198:199] op_sel_hi:[1,0]\n"
      "v_permlane16_swap_b32 v154, v76\n"
      "v_pk_mul_f32 v[120:121], v[120:121], v[192:193] op_sel_hi:[1,0]\n"
      "s_add_u32 m0, s75, 36864\n"
      "v_pk_mul_f32 v[196:197], v[196:197], v[206:207] op_sel_hi:[1,0]\n"
      "s_mov_b32 s79, 12288\n"
      "v_mfma_f32_16x16x32_bf16 v[96:99], a[88:91], a[248:251], v[96:99]\n"
      "buffer_load_dwordx4 v255, s[36:39], s79 offen  lds\n"
      "v_mul_f32_e64 v216, s50, v216\n"
      "v_mfma_f32_16x16x32_bf16 v[96:99], a[92:95], a[252:255], v[96:99]\n"
      "s_add_u32 m0, s75, 40832\n"
      "v_cvt_pk_bf16_f32 v194, v194, v195\n"
      "v_cvt_pk_bf16_f32 v195, v196, v197\n"
      "v_mfma_f32_16x16x32_bf16 v[156:159], a[32:35], a[240:243], v[156:159]\n"
      "ds_write_b64 v253, v[194:195] offset:16\n"
      "v_add_f32_e64 v154, v154, v76\n"
      "v_cvt_pk_bf16_f32 v178, v178, v179\n"
      "v_pk_mul_f32 v[180:181], v[180:181], v[192:193] op_sel_hi:[1,0]\n"
      "v_pk_mul_f32 v[122:123], v[122:123], v[192:193] op_sel_hi:[1,0]\n"
      "v_pk_mul_f32 v[130:131], v[130:131], v[192:193] op_sel_hi:[1,0]\n"
      "v_permlane16_swap_b32 v222, v84\n"
      "v_add_f32 v222, v222, v84\n"
      "v_pk_mul_f32 v[234:235], v[234:235], v[206:207] op_sel_hi:[1,0]\n"
      "v_pk_mul_f32 v[148:149], v[148:149], v[206:207] op_sel_hi:[1,0]\n"
      "v_mov_b32 v76, v154\n"
      "v_cvt_pk_bf16_f32 v146, v146, v147\n"
      "v_pk_mul_f32 v[188:189], v[188:189], v[216:217] op_sel_hi:[1,0]\n"
      "v_mov_b32 v84, v222\n"
      "v_pk_mul_f32 v[132:133], v[132:133], v[192:193] op_sel_hi:[1,0]\n"
      "v_pk_mul_f32 v[108:109], v[108:109], v[216:217] op_sel_hi:[1,0]\n"
      "v_mul_f32_e64 v81, v97, v97\n"
      "v_pk_mul_f32 v[174:175], v[174:175], v[198:199] op_sel_hi:[1,0]\n"
      "v_pk_mul_f32 v[212:213], v[212:213], v[198:199] op_sel_hi:[1,0]\n"
      "v_pk_mul_f32 v[248:249], v[248:249], v[198:199] op_sel_hi:[1,0]\n"
      "v_pk_mul_f32 v[208:209], v[208:209], v[216:217] op_sel_hi:[1,0]\n"
      "v_pk_mul_f32 v[176:177], v[176:177], v[198:199] op_sel_hi:[1,0]\n"
      "v_pk_mul_f32 v[250:251], v[250:251], v[198:199] op_sel_hi:[1,0]\n"
      "v_mfma_f32_16x16x32_bf16 v[156:159], a[36:39], a[244:247], v[156:159]\n"
      "buffer_load_dwordx4 v255, s[36:39], s79 offen offset:128  lds\n"
      "v_cvt_pk_bf16_f32 v130, v130, v131\n"
      "v_mfma_f32_16x16x32_bf16 v[134:137], a[52:55], a[236:239], v[134:137]\n"
      "s_add_u32 m0, s75, 44800\n"
      "v_cvt_pk_bf16_f32 v147, v148, v149\n"
      "v_cvt_pk_bf16_f32 v131, v132, v133\n"
      "v_mfma_f32_16x16x32_bf16 v[134:137], a[56:59], a[240:243], v[134:137]\n"
      "s_waitcnt vmcnt(11)\n"
      "s_barrier\n"
      "v_cvt_pk_bf16_f32 v212, v212, v213\n"
      "v_mfma_f32_16x16x32_bf16 v[134:137], a[60:63], a[244:247], v[134:137]\n"
      "ds_read_b64_tr_b16 a[154:155], v202 offset:25088\n"
      "buffer_load_dwordx4 v255, s[36:39], s79 offen offset:256  lds\n"
      "v_pk_mul_f32 v[210:211], v[210:211], v[216:217] op_sel_hi:[1,0]\n"
      "v_mfma_f32_16x16x32_bf16 v[112:115], a[16:19], a[248:251], v[112:115]\n"
      "ds_read_b64_tr_b16 a[180:181], v203 offset:36864\n"
      "v_cvt_pk_bf16_f32 v174, v174, v175\n"
      "v_cvt_pk_bf16_f32 v208, v208, v209\n"
      "v_mfma_f32_16x16x32_bf16 v[112:115], a[20:23], a[252:255], v[112:115]\n"
      "ds_read_b64_tr_b16 a[176:177], v203 offset:24576\n"
      "ds_read_b64_tr_b16 a[202:203], v204 offset:25088\n"
      "v_mfma_f32_16x16x32_bf16 v[134:137], a[64:67], a[248:251], v[134:137]\n"
      "s_mov_b32 s78, 24576\n"
      "ds_read_b64_tr_b16 a[156:157], v202 offset:36864\n"
      "v_cvt_pk_bf16_f32 v213, v214, v215\n"
      "v_mfma_f32_16x16x32_bf16 v[192:195], a[120:123], a[232:235], 0\n"
      "s_add_u32 m0, s75, 49152\n"
      "ds_read_b64_tr_b16 a[182:183], v203 offset:37376\n"
      "ds_read_b64_tr_b16 a[200:201], v204 offset:24576\n"
      "ds_read_b64_tr_b16 a[206:207], v204 offset:37376\n"
      "v_fma_f32 v80, v96, v96, s51\n"
      "v_pk_mul_f32 v[236:237], v[236:237], v[206:207] op_sel_hi:[1,0]\n"
      "v_pk_mul_f32 v[110:111], v[110:111], v[216:217] op_sel_hi:[1,0]\n"
      "v_mfma_f32_16x16x32_bf16 v[192:195], a[124:127], a[236:239], v[192:195]\n"
      "ds_read_b64_tr_b16 a[204:205], v204 offset:36864\n"
      "ds_read_b64_tr_b16 a[158:159], v202 offset:37376\n"
      "ds_read_b64_tr_b16 a[152:153], v202 offset:24576\n"
      "v_mfma_f32_16x16x32_bf16 v[192:195], a[128:131], a[240:243], v[192:195]\n"
      "ds_read_b64_tr_b16 a[178:179], v203 offset:25088\n"
      "v_cvt_pk_bf16_f32 v148, v234, v235\n"
      "v_fma_f32 v202, v112, v112, s51\n"
      "v_cvt_pk_bf16_f32 v149, v236, v237\n"
      "v_cvt_pk_bf16_f32 v175, v176, v177\n"
      "v_mfma_f32_16x16x32_bf16 v[156:159], a[40:43], a[248:251], v[156:159]\n"
      "buffer_load_dwordx4 v255, s[36:39], s78 offen  lds\n"
      "v_cvt_pk_bf16_f32 v179, v180, v181\n"
      "v_mfma_f32_16x16x32_bf16 v[156:159], a[44:47], a[252:255], v[156:159]\n"
      "ds_read_b128 v[234:237], v243 offset:4096\n"
      "v_cvt_pk_bf16_f32 v176, v248, v249\n"
      "v_mfma_f32_16x16x32_bf16 v[192:195], a[132:135], a[244:247], v[192:195]\n"
      "ds_write_b128 v253, v[146:149]\n"
      "v_permlane32_swap_b32_e64 v154, v76\n"
      "v_pk_mul_f32 v[190:191], v[190:191], v[216:217] op_sel_hi:[1,0]\n"
      "s_add_u32 m0, s75, 53120\n"
      "v_mfma_f32_16x16x32_bf16 v[146:149], a[96:99], a[232:235], 0\n"
      "buffer_load_dwordx4 v255, s[36:39], s78 offen offset:128  lds\n"
      "v_mfma_f32_16x16x32_bf16 v[146:149], a[100:103], a[236:239], v[146:149]\n"
      "ds_read_b128 v[214:217], v173 offset:4096\n"
      "v_add_f32_e64 v154, v154, v76\n"
      "v_mfma_f32_16x16x32_bf16 v[146:149], a[104:107], a[240:243], v[146:149]\n"
      "ds_read_b128 v[196:199], v243\n"
      "v_cvt_pk_bf16_f32 v177, v250, v251\n"
      "v_mfma_f32_16x16x32_bf16 v[192:195], a[136:139], a[248:251], v[192:195]\n"
      "s_add_u32 m0, s75, 57088\n"
      "v_permlane32_swap_b32 v222, v84\n"
      "v_mul_f32 v203, v113, v113\n"
      "v_mfma_f32_16x16x32_bf16 v[192:195], a[140:143], a[252:255], v[192:195]\n"
      "ds_write_b64 v253, v[130:131] offset:24\n"
      "v_cvt_pk_bf16_f32 v188, v188, v189\n"
      "v_mfma_f32_16x16x32_bf16 v[146:149], a[108:111], a[244:247], v[146:149]\n"
      "buffer_load_dwordx4 v255, s[36:39], s78 offen offset:256  lds\n"
      "v_cvt_pk_bf16_f32 v180, v120, v121\n"
      "v_add_f32 v222, v222, v84\n"
      "v_pk_fma_f32 v[202:203], v[114:115], v[114:115], v[202:203]\n"
      "v_mfma_f32_16x16x32_bf16 v[146:149], a[112:115], a[248:251], v[146:149]\n"
      "s_mov_b32 s78, 36864\n"
      "v_rsq_f32 v222, v222\n"
      "v_mfma_f32_16x16x32_bf16 v[146:149], a[116:119], a[252:255], v[146:149]\n"
      "s_add_u32 m0, s75, 61440\n"
      "v_mov_b32 v84, v222\n"
      "s_waitcnt lgkmcnt(1)\n"
      "v_mfma_f32_16x16x32_bf16 v[130:133], a[168:171], v[196:199], 0\n"
      "buffer_load_dwordx4 v255, s[36:39], s78 offen  lds\n"
      "v_permlane16_swap_b32 v222, v84\n"
      "v_pk_fma_f32 v[80:81], v[98:99], v[98:99], v[80:81]\n"
      "v_mfma_f32_16x16x32_bf16 v[130:133], a[172:175], v[138:141], v[130:133]\n"
      "s_add_u32 m0, s75, 65408\n"
      "v_mul_f32 v222, s50, v222\n"
      "v_cvt_pk_bf16_f32 v181, v122, v123\n"
      "v_rsq_f32 v154, v154\n"
      "v_cvt_pk_bf16_f32 v189, v190, v191\n"
      "v_mov_b32 v76, v154\n"
      "v_mfma_f32_16x16x32_bf16 v[248:251], a[144:147], v[196:199], 0\n"
      "buffer_load_dwordx4 v255, s[36:39], s78 offen offset:128  lds\n"
      "v_mfma_f32_16x16x32_bf16 v[248:251], a[148:151], v[138:141], v[248:251]\n"
      "ds_write_b128 v253, v[178:181] offset:32\n"
      "v_pk_mul_f32 v[244:245], v[244:245], v[222:223] op_sel_hi:[1,0]\n"
      "v_pk_mul_f32 v[152:153], v[152:153], v[84:85] op_sel_hi:[1,0]\n"
      "v_permlane16_swap_b32_e64 v154, v76\n"
      "s_add_u32 m0, s75, 69376\n"
      "v_pk_mul_f32 v[184:185], v[184:185], v[222:223] op_sel_hi:[1,0]\n"
      "v_pk_fma_f32 v[202:203], v[156:157], v[156:157], v[202:203]\n"
      "v_pk_mul_f32 v[150:151], v[150:151], v[84:85] op_sel_hi:[1,0]\n"
      "v_mfma_f32_16x16x32_bf16 v[248:251], a[152:155], v[234:237], v[248:251]\n"
      "buffer_load_dwordx4 v255, s[36:39], s78 offen offset:256  lds\n"
      "v_mfma_f32_16x16x32_bf16 v[248:251], a[156:159], v[214:217], v[248:251]\n"
      "s_waitcnt vmcnt(12)\n"
      "s_barrier\n"
      "s_add_u32 m0, s75, 73728\n"
      "v_mul_f32_e64 v154, s50, v154\n"
      "v_mfma_f32_16x16x32_bf16 v[134:137], a[68:71], a[252:255], v[134:137]\n"
      "ds_read_b64_tr_b16 a[186:187], v239 offset:512\n"
      "ds_read_b64_tr_b16 a[166:167], v238 offset:12800\n"
      "ds_read_b64_tr_b16 a[184:185], v239\n"
      "s_mov_b32 s79, 49152\n"
      "ds_read_b64_tr_b16 a[214:215], v240 offset:12800\n"
      "ds_read_b64_tr_b16 a[160:161], v238\n"
      "ds_read_b64_tr_b16 a[190:191], v239 offset:12800\n"
      "v_pk_mul_f32 v[162:163], v[162:163], v[222:223] op_sel_hi:[1,0]\n"
      "v_pk_mul_f32 v[246:247], v[246:247], v[222:223] op_sel_hi:[1,0]\n"
      "v_mfma_f32_16x16x32_bf16 v[130:133], a[176:179], v[234:237], v[130:133]\n"
      "ds_read_b64_tr_b16 a[188:189], v239 offset:12288\n"
      "ds_read_b64_tr_b16 a[208:209], v240\n"
      "v_mfma_f32_16x16x32_bf16 v[204:207], a[192:195], v[196:199], 0\n"
      "ds_read_b64_tr_b16 a[212:213], v240 offset:12288\n"
      "ds_read_b64_tr_b16 a[164:165], v238 offset:12288\n"
      "ds_read_b64_tr_b16 a[162:163], v238 offset:512\n"
      "ds_read_b64_tr_b16 a[210:211], v240 offset:512\n"
      "v_pk_mul_f32 v[160:161], v[160:161], v[222:223] op_sel_hi:[1,0]\n"
      "v_pk_mul_f32 v[166:167], v[166:167], v[154:155] op_sel_hi:[1,0]\n"
      "v_mfma_f32_16x16x32_bf16 v[204:207], a[196:199], v[138:141], v[204:207]\n"
      "ds_read_b128 v[196:199], v173 offset:8192\n"
      "ds_read_b128 v[138:141], v243 offset:2048\n"
      "v_mfma_f32_16x16x32_bf16 v[204:207], a[200:203], v[234:237], v[204:207]\n"
      "buffer_load_dwordx4 v255, s[36:39], s79 offen  lds\n"
      "ds_read_b128 v[234:237], v243 offset:8192\n"
      "v_pk_fma_f32 v[202:203], v[158:159], v[158:159], v[202:203]\n"
      "s_add_u32 m0, s75, 77696\n"
      "v_pk_fma_f32 v[202:203], v[134:135], v[134:135], v[202:203]\n"
      "v_pk_mul_f32 v[100:101], v[100:101], v[154:155] op_sel_hi:[1,0]\n"
      "v_pk_mul_f32 v[126:127], v[126:127], v[154:155] op_sel_hi:[1,0]\n"
      "v_pk_fma_f32 v[80:81], v[146:147], v[146:147], v[80:81]\n"
      "v_cvt_pk_bf16_f32 v209, v210, v211\n"
      "v_pk_mul_f32 v[224:225], v[224:225], v[84:85] op_sel_hi:[1,0]\n"
      "v_pk_mul_f32 v[220:221], v[220:221], v[76:77] op_sel_hi:[1,0]\n"
      "v_pk_mul_f32 v[182:183], v[182:183], v[222:223] op_sel_hi:[1,0]\n"
      "v_pk_fma_f32 v[80:81], v[148:149], v[148:149], v[80:81]\n"
      "v_cvt_pk_bf16_f32 v211, v110, v111\n"
      "v_pk_fma_f32 v[80:81], v[192:193], v[192:193], v[80:81]\n"
      "v_pk_fma_f32 v[202:203], v[136:137], v[136:137], v[202:203]\n"
      "v_pk_mul_f32 v[218:219], v[218:219], v[76:77] op_sel_hi:[1,0]\n"
      "v_cvt_pk_bf16_f32 v182, v182, v183\n"
      "v_cvt_pk_bf16_f32 v183, v184, v185\n"
      "v_pk_fma_f32 v[80:81], v[194:195], v[194:195], v[80:81]\n"
      "v_mfma_f32_16x16x32_bf16 v[204:207], a[204:207], v[214:217], v[204:207]\n"
      "ds_read_b128 v[120:123], v173 offset:2048\n"
      "v_cvt_pk_bf16_f32 v218, v218, v219\n"
      "v_add_f32_e64 v202, v202, v203\n"
      "v_mfma_f32_16x16x32_bf16 v[130:133], a[180:183], v[214:217], v[130:133]\n"
      "buffer_load_dwordx4 v255, s[36:39], s79 offen offset:128  lds\n"
      "s_add_u32 m0, s75, 81664\n"
      "v_add_f32 v80, v80, v81\n"
      "s_waitcnt lgkmcnt(1)\n"
      "v_mfma_f32_16x16x32_bf16 v[130:133], a[184:187], v[234:237], v[130:133]\n"
      "ds_read_b128 v[214:217], v243 offset:6144\n"
      "v_cvt_pk_bf16_f32 v210, v108, v109\n"
      "v_mfma_f32_16x16x32_bf16 v[130:133], a[188:191], v[196:199], v[130:133]\n"
      "ds_read_b128 v[108:111], v243 offset:10240\n"
      "v_permlane16_swap_b32 v202, v80\n"
      "v_add_f32 v202, v202, v80\n"
      "v_mfma_f32_16x16x32_bf16 v[204:207], a[208:211], v[234:237], v[204:207]\n"
      "buffer_load_dwordx4 v255, s[36:39], s79 offen offset:256  lds\n"
      "v_cvt_pk_bf16_f32 v160, v160, v161\n"
      "v_cvt_pk_bf16_f32 v150, v150, v151\n"
      "v_mfma_f32_16x16x32_bf16 v[204:207], a[212:215], v[196:199], v[204:207]\n"
      "s_mov_b32 s79, 61440\n"
      "v_cvt_pk_bf16_f32 v184, v244, v245\n"
      "v_cvt_pk_bf16_f32 v219, v220, v221\n"
      "v_cvt_pk_bf16_f32 v130, v130, v131\n"
      "v_cvt_pk_bf16_f32 v131, v132, v133\n"
      "v_mfma_f32_16x16x32_bf16 v[242:245], a[168:171], v[138:141], 0\n"
      "ds_read_b128 v[220:223], v173 offset:10240\n"
      "v_mov_b32_e64 v80, v202\n"
      "v_mfma_f32_16x16x32_bf16 v[248:251], a[160:163], v[234:237], v[248:251]\n"
      "s_add_u32 m0, s75, 86016\n"
      "ds_read_b128 v[116:119], v173 offset:6144\n"
      "v_permlane32_swap_b32 v202, v80\n"
      "v_add_f32 v202, v202, v80\n"
      "v_pk_mul_f32 v[144:145], v[144:145], v[76:77] op_sel_hi:[1,0]\n"
      "v_mfma_f32_16x16x32_bf16 v[248:251], a[164:167], v[196:199], v[248:251]\n"
      "buffer_load_dwordx4 v255, s[36:39], s79 offen  lds\n"
      "v_rsq_f32_e64 v202, v202\n"
      "v_pk_mul_f32 v[230:231], v[230:231], v[76:77] op_sel_hi:[1,0]\n"
      "s_add_u32 m0, s75, 89984\n"
      "v_mov_b32 v198, 0xbc80bc80\n"
      "v_mov_b32_e64 v80, v202\n"
      "v_cvt_pk_bf16_f32 v204, v204, v205\n"
      "v_mfma_f32_16x16x32_bf16 v[170:173], a[192:195], v[138:141], 0\n"
      "ds_write_b64 v254, v[130:131] offset:32\n"
      "v_permlane16_swap_b32 v202, v80\n"
      "v_mul_f32_e64 v202, s50, v202\n"
      "v_pk_mul_f32 v[102:103], v[102:103], v[154:155] op_sel_hi:[1,0]\n"
      "v_pk_mul_f32 v[104:105], v[104:105], v[84:85] op_sel_hi:[1,0]\n"
      "v_cvt_pk_bf16_f32 v248, v248, v249\n"
      "v_pk_mul_f32 v[192:193], v[192:193], v[80:81] op_sel_hi:[1,0]\n"
      "s_waitcnt lgkmcnt(1)\n"
      "v_mfma_f32_16x16x32_bf16 v[170:173], a[196:199], v[120:123], v[170:173]\n"
      "buffer_load_dwordx4 v255, s[36:39], s79 offen offset:128  lds\n"
      "ds_read_b128 v[130:133], v1 offset:10240\n"
      "v_pk_mul_f32 v[124:125], v[124:125], v[154:155] op_sel_hi:[1,0]\n"
      "v_mfma_f32_16x16x32_bf16 v[178:181], a[144:147], v[138:141], 0\n"
      "s_waitcnt vmcnt(11)\n"
      "s_barrier\n"
      "v_cvt_pk_bf16_f32 v205, v206, v207\n"
      "v_mfma_f32_16x16x32_bf16 v[178:181], a[148:151], v[120:123], v[178:181]\n"
      "ds_read_b64_tr_b16 a[244:245], v241 offset:36872\n"
      "ds_read_b64_tr_b16 a[216:217], v241 offset:24576\n"
      "v_mfma_f32_16x16x32_bf16 v[178:181], a[152:155], v[214:217], v[178:181]\n"
      "s_add_u32 m0, s75, 93952\n"
      "v_cvt_pk_bf16_f32 v124, v124, v125\n"
      "v_cvt_pk_bf16_f32 v185, v246, v247\n"
      "v_mfma_f32_16x16x32_bf16 v[178:181], a[156:159], v[116:119], v[178:181]\n"
      "buffer_load_dwordx4 v255, s[36:39], s79 offen offset:256  lds\n"
      "v_mfma_f32_16x16x32_bf16 v[242:245], a[172:175], v[120:123], v[242:245]\n"
      "ds_read_b64_tr_b16 a[242:243], v241 offset:26632\n"
      "v_cvt_pk_bf16_f32 v125, v126, v127\n"
      "v_cvt_pk_bf16_f32 v249, v250, v251\n"
      "v_mfma_f32_16x16x32_bf16 v[170:173], a[200:203], v[214:217], v[170:173]\n"
      "ds_read_b64_tr_b16 a[222:223], v241 offset:38912\n"
      "ds_read_b64_tr_b16 a[220:221], v241 offset:36864\n"
      "v_mfma_f32_16x16x32_bf16 v[170:173], a[204:207], v[116:119], v[170:173]\n"
      "ds_read_b64_tr_b16 a[246:247], v241 offset:38920\n"
      "v_mov_b32 v200, 0xbc80bc80\n"
      "v_mfma_f32_16x16x32_bf16 v[170:173], a[208:211], v[108:111], v[170:173]\n"
      "ds_read_b64_tr_b16 v[234:235], v252 offset:26624\n"
      "v_cvt_pk_bf16_f32 v192, v192, v193\n"
      "v_mfma_f32_16x16x32_bf16 v[170:173], a[212:215], v[220:223], v[170:173]\n"
      "buffer_load_dwordx4 v[126:129], v36, s[28:31], s49 offen\n"
      "v_mfma_f32_16x16x32_bf16 v[242:245], a[176:179], v[214:217], v[242:245]\n"
      "ds_read_b64_tr_b16 a[240:241], v241 offset:24584\n"
      "ds_read_b64_tr_b16 v[236:237], v252 offset:36864\n"
      "ds_read_b64_tr_b16 v[238:239], v252 offset:38912\n"
      "v_pk_mul_f32 v[96:97], v[96:97], v[80:81] op_sel_hi:[1,0]\n"
      "v_mfma_f32_16x16x32_bf16 v[242:245], a[180:183], v[116:119], v[242:245]\n"
      "ds_read_b64_tr_b16 a[218:219], v241 offset:26624\n"
      "s_add_u32 m0, s64, 18432\n"
      "v_cvt_pk_bf16_f32 v170, v170, v171\n"
      "v_mfma_f32_16x16x32_bf16 v[242:245], a[184:187], v[108:111], v[242:245]\n"
      "buffer_load_dword v203, v29, s[28:31], s49 offen\n"
      "v_mfma_f32_16x16x32_bf16 v[242:245], a[188:191], v[220:223], v[242:245]\n"
      "ds_read_b128 v[214:217], v1 offset:4096\n"
      "ds_read_b64_tr_b16 v[232:233], v252 offset:24576\n"
      "v_mfma_f32_16x16x32_bf16 v[178:181], a[160:163], v[108:111], v[178:181]\n"
      "ds_read_b128 v[108:111], v0 offset:10240\n"
      "v_cvt_pk_bf16_f32 v171, v172, v173\n"
      "v_mfma_f32_16x16x32_bf16 v[178:181], a[164:167], v[220:223], v[178:181]\n"
      "ds_read_b128 v[138:141], v0 offset:4096\n"
      "buffer_load_dwordx4 v[220:223], v36, s[28:31], s74 offen\n"
      "v_pk_mul_f32 v[194:195], v[194:195], v[80:81] op_sel_hi:[1,0]\n"
      "v_pk_mul_f32 v[148:149], v[148:149], v[80:81] op_sel_hi:[1,0]\n"
      "v_pk_mul_f32 v[158:159], v[158:159], v[202:203] op_sel_hi:[1,0]\n"
      "v_pk_mul_f32 v[134:135], v[134:135], v[202:203] op_sel_hi:[1,0]\n"
      "v_mov_b32 v201, 0xbc80bc80\n"
      "v_cvt_pk_bf16_f32 v242, v242, v243\n"
      "v_pk_mul_f32 v[168:169], v[168:169], v[154:155] op_sel_hi:[1,0]\n"
      "v_pk_mul_f32 v[112:113], v[112:113], v[202:203] op_sel_hi:[1,0]\n"
      "v_pk_mul_f32 v[156:157], v[156:157], v[202:203] op_sel_hi:[1,0]\n"
      "v_cvt_pk_bf16_f32 v161, v162, v163\n"
      "v_cvt_pk_bf16_f32 v166, v166, v167\n"
      "v_cvt_pk_bf16_f32 v151, v152, v153\n"
      "v_cvt_pk_bf16_f32 v178, v178, v179\n"
      "v_pk_mul_f32 v[226:227], v[226:227], v[84:85] op_sel_hi:[1,0]\n"
      "v_cvt_pk_bf16_f32 v112, v112, v113\n"
      "v_cvt_pk_bf16_f32 v243, v244, v245\n"
      "v_pk_mul_f32 v[136:137], v[136:137], v[202:203] op_sel_hi:[1,0]\n"
      "v_mov_b32 v199, 0xbc80bc80\n"
      "v_pk_mul_f32 v[114:115], v[114:115], v[202:203] op_sel_hi:[1,0]\n"
      "v_pk_mul_f32 v[146:147], v[146:147], v[80:81] op_sel_hi:[1,0]\n"
      "v_pk_mul_f32 v[98:99], v[98:99], v[80:81] op_sel_hi:[1,0]\n"
      "v_cvt_pk_bf16_f32 v179, v180, v181\n"
      "s_waitcnt lgkmcnt(0)\n"
      "v_mfma_f32_16x16x32_bf16 v[120:123], v[138:141], a[144:147], 0\n"
      "buffer_load_dword v163, v29, s[28:31], s74 offen\n"
      "v_cvt_pk_bf16_f32 v167, v168, v169\n"
      "v_cvt_pk_bf16_f32 v169, v102, v103\n"
      "v_mfma_f32_16x16x32_bf16 v[116:119], v[138:141], a[168:171], 0\n"
      "ds_write_b64 v254, v[204:205] offset:64\n"
      "v_mfma_f32_16x16x32_bf16 v[116:119], v[214:217], a[172:175], v[116:119]\n"
      "ds_read_b128 v[204:207], v1 offset:16384\n"
      "v_cvt_pk_bf16_f32 v113, v114, v115\n"
      "v_cvt_pk_bf16_f32 v168, v100, v101\n"
      "v_mfma_f32_16x16x32_bf16 v[152:155], v[138:141], a[192:195], 0\n"
      "buffer_load_dwordx4 v16, s[16:19], s45 offen  lds\n"
      "v_mfma_f32_16x16x32_bf16 v[152:155], v[214:217], a[196:199], v[152:155]\n"
      "ds_read_b128 v[244:247], v0 offset:16384\n"
      "v_xor_b32 v80, 64, v8\n"
      "s_mov_b32 exec_lo, 0x3f003f\n"
      "s_mov_b32 exec_hi, 0x3f003f\n"
      "ds_write_b64 v254, v[178:179] offset:18688\n"
      "ds_write_b64 v254, v[242:243] offset:18720\n"
      "ds_write_b64 v254, v[170:171] offset:18752\n"
      "s_mov_b64 exec, -1\n"
      "v_pk_mul_f32 v[142:143], v[142:143], v[76:77] op_sel_hi:[1,0]\n"
      "v_cvt_pk_bf16_f32 v114, v156, v157\n"
      "v_pk_mul_f32 v[106:107], v[106:107], v[84:85] op_sel_hi:[1,0]\n"
      "v_mfma_f32_16x16x32_bf16 v[120:123], v[214:217], a[148:151], v[120:123]\n"
      "ds_read_b128 v[138:141], v1 offset:2048\n"
      "v_cvt_pk_bf16_f32 v104, v104, v105\n"
      "v_mfma_f32_16x16x32_bf16 v[120:123], v[108:111], a[152:155], v[120:123]\n"
      "s_add_u32 m0, s64, 22528\n"
      "ds_read_b128 v[178:181], v0 offset:2048\n"
      "v_mfma_f32_16x16x32_bf16 v[120:123], v[130:133], a[156:159], v[120:123]\n"
      "buffer_load_dwordx4 v17, s[16:19], s45 offen  lds\n"
      "v_mfma_f32_16x16x32_bf16 v[152:155], v[108:111], a[200:203], v[152:155]\n"
      "ds_write_b64 v254, v[248:249]\n"
      "v_mfma_f32_16x16x32_bf16 v[152:155], v[130:133], a[204:207], v[152:155]\n"
      "ds_read_b128 v[240:243], v8 offset:24576\n"
      "v_cvt_pk_bf16_f32 v134, v134, v135\n"
      "v_mfma_f32_16x16x32_bf16 v[116:119], v[108:111], a[176:179], v[116:119]\n"
      "s_add_u32 m0, s64, 26624\n"
      "ds_read_b128 v[214:217], v8 offset:28672\n"
      "v_mfma_f32_16x16x32_bf16 v[116:119], v[130:133], a[180:183], v[116:119]\n"
      "buffer_load_dwordx4 v18, s[16:19], s45 offen  lds\n"
      "ds_read_b128 v[130:133], v80 offset:24576\n"
      "s_waitcnt lgkmcnt(4)\n"
      "v_mfma_f32_16x16x32_bf16 v[116:119], v[244:247], a[184:187], v[116:119]\n"
      "s_waitcnt vmcnt(13)\n"
      "s_barrier\n"
      "ds_read_b64_tr_b16 a[224:225], v78\n"
      "v_mfma_f32_16x16x32_bf16 v[116:119], v[204:207], a[188:191], v[116:119]\n"
      "ds_read_b64_tr_b16 a[248:249], v78 offset:8\n"
      "ds_read_b64_tr_b16 a[226:227], v78 offset:2048\n"
      "v_cvt_pk_bf16_f32 v96, v96, v97\n"
      "v_cvt_pk_bf16_f32 v115, v158, v159\n"
      "v_pk_mul_f32 v[228:229], v[228:229], v[76:77] op_sel_hi:[1,0]\n"
      "v_mfma_f32_16x16x32_bf16 v[120:123], v[244:247], a[160:163], v[120:123]\n"
      "ds_read_b64_tr_b16 a[254:255], v78 offset:14344\n"
      "s_add_u32 m0, s64, 30720\n"
      "v_mfma_f32_16x16x32_bf16 v[120:123], v[204:207], a[164:167], v[120:123]\n"
      "buffer_load_dwordx4 v19, s[16:19], s45 offen  lds\n"
      "s_waitcnt lgkmcnt(4)\n"
      "v_mfma_f32_16x16x32_bf16 v[248:251], v[240:243], v[198:201], 0\n"
      "ds_read_b64_tr_b16 a[230:231], v78 offset:14336\n"
      "v_cvt_pk_bf16_f32 v105, v106, v107\n"
      "v_mfma_f32_16x16x32_bf16 v[248:251], v[130:133], v[198:201], v[248:251]\n"
      "ds_read_b128 v[108:111], v0 offset:8192\n"
      "v_cvt_pk_bf16_f32 v193, v194, v195\n"
      "v_mfma_f32_16x16x32_bf16 v[130:133], v[178:181], a[168:171], 0\n"
      "s_add_u32 m0, s65, 34816\n"
      "ds_read_b64_tr_b16 v[242:243], v186 offset:2048\n"
      "buffer_load_dwordx4 v15, s[16:19], s45 offen  lds\n"
      "v_mfma_f32_16x16x32_bf16 v[130:133], v[138:141], a[172:175], v[130:133]\n"
      "ds_read_b128 v[156:159], v80 offset:28672\n"
      "ds_read_b64_tr_b16 a[252:253], v78 offset:12296\n"
      "v_mfma_f32_16x16x32_bf16 v[152:155], v[244:247], a[208:211], v[152:155]\n"
      "ds_read_b64_tr_b16 v[246:247], v186 offset:14336\n"
      "ds_read_b64_tr_b16 a[250:251], v78 offset:2056\n"
      "v_mfma_f32_16x16x32_bf16 v[152:155], v[204:207], a[212:215], v[152:155]\n"
      "ds_read_b64_tr_b16 v[240:241], v186\n"
      "ds_read_b64_tr_b16 a[228:229], v78 offset:12288\n"
      "v_mfma_f32_16x16x32_bf16 v[194:197], v[178:181], a[192:195], 0\n"
      "ds_read_b64_tr_b16 v[244:245], v186 offset:12288\n"
      "v_cvt_pk_bf16_f32 v135, v136, v137\n"
      "v_cvt_pk_bf16_f32 v106, v224, v225\n"
      "v_mfma_f32_16x16x32_bf16 v[194:197], v[138:141], a[196:199], v[194:197]\n"
      "ds_read_b128 v[204:207], v1 offset:8192\n"
      "v_cvt_pk_bf16_f32 v107, v226, v227\n"
      "v_mfma_f32_16x16x32_bf16 v[248:251], v[214:217], v[198:201], v[248:251]\n"
      "s_waitcnt lgkmcnt(7)\n"
      "v_cvt_pk_bf16_f32 v97, v98, v99\n"
      "v_add_u32 v137, 49152, v8\n"
      "v_cvt_pk_bf16_f32 v98, v146, v147\n"
      "v_mfma_f32_16x16x32_bf16 v[248:251], v[156:159], v[198:201], v[248:251]\n"
      "ds_read_b128 v[224:227], v137\n"
      "v_cvt_pk_bf16_f32 v85, v154, v155\n"
      "v_mfma_f32_16x16x32_bf16 v[214:217], v[178:181], a[144:147], 0\n"
      "ds_read_b128 v[178:181], v1 offset:14336\n"
      "v_cvt_pk_bf16_f32 v99, v148, v149\n"
      "v_mfma_f32_16x16x32_bf16 v[214:217], v[138:141], a[148:151], v[214:217]\n"
      "ds_read_b128 v[138:141], v0 offset:14336\n"
      "v_cvt_pk_bf16_f32 v142, v142, v143\n"
      "v_mfma_f32_16x16x32_bf16 v[214:217], v[108:111], a[152:155], v[214:217]\n"
      "ds_read_b128 v[146:149], v8 offset:32768\n"
      "v_subrev_u32 v187, 115456, v9\n"
      "v_mfma_f32_16x16x32_bf16 v[130:133], v[108:111], a[176:179], v[130:133]\n"
      "s_mov_b32 exec_lo, 0x3f003f\n"
      "s_mov_b32 exec_hi, 0x3f003f\n"
      "ds_write_b128 v253, v[112:115] offset:18688\n"
      "ds_write_b64 v253, v[134:135] offset:18704\n"
      "ds_write_b64 v253, v[192:193] offset:18712\n"
      "ds_write_b128 v253, v[96:99] offset:18720\n"
      "s_mov_b64 exec, -1\n"
      "v_mfma_f32_16x16x32_bf16 v[194:197], v[108:111], a[200:203], v[194:197]\n"
      "s_waitcnt lgkmcnt(4)\n"
      "v_cvt_pk_bf16_f32 v143, v144, v145\n"
      "v_cvt_pk_bf16_f32 v144, v228, v229\n"
      "v_add_u32 v134, 49152, v80\n"
      "v_mfma_f32_16x16x32_bf16 v[248:251], v[146:149], v[198:201], v[248:251]\n"
      "ds_read_b128 v[252:255], v134 offset:4096\n"
      "v_cvt_pk_bf16_f32 v145, v230, v231\n"
      "v_mfma_f32_16x16x32_bf16 v[214:217], v[204:207], a[156:159], v[214:217]\n"
      "buffer_load_dwordx4 v[100:103], v36, s[28:31], s47 offen\n"
      "v_mfma_f32_16x16x32_bf16 v[214:217], v[138:141], a[160:163], v[214:217]\n"
      "ds_read_b128 v[228:231], v80 offset:32768\n"
      "v_cvt_pk_bf16_f32 v84, v152, v153\n"
      "v_mfma_f32_16x16x32_bf16 v[146:149], v[224:227], v[198:201], 0\n"
      "ds_read_b128 v[224:227], v134\n"
      "v_cvt_pk_bf16_f32 v76, v120, v121\n"
      "v_mfma_f32_16x16x32_bf16 v[194:197], v[204:207], a[204:207], v[194:197]\n"
      "ds_read_b128 v[190:193], v137 offset:4096\n"
      "v_cvt_pk_bf16_f32 v80, v116, v117\n"
      "v_mfma_f32_16x16x32_bf16 v[194:197], v[138:141], a[208:211], v[194:197]\n"
      "buffer_load_dword v8, v29, s[28:31], s47 offen\n"
      "v_subrev_u32 v39, 115456, v9\n"
      "v_mfma_f32_16x16x32_bf16 v[130:133], v[204:207], a[180:183], v[130:133]\n"
      "ds_read_b128 v[204:207], v134 offset:8192\n"
      "v_lshrrev_b32_e64 v39, 4, v39\n"
      "v_mfma_f32_16x16x32_bf16 v[130:133], v[138:141], a[184:187], v[130:133]\n"
      "ds_read_b128 v[138:141], v137 offset:8192\n"
      "v_cvt_pk_bf16_f32 v81, v118, v119\n"
      "v_mfma_f32_16x16x32_bf16 v[130:133], v[178:181], a[188:191], v[130:133]\n"
      "ds_read_b128 v[156:159], v1\n"
      "v_cvt_pk_bf16_f32 v77, v122, v123\n"
      "v_mfma_f32_16x16x32_bf16 v[214:217], v[178:181], a[164:167], v[214:217]\n"
      "ds_read_b128 v[114:117], v0\n"
      "v_readlane_b32 s86, v38, s43\n"
      "v_mfma_f32_16x16x32_bf16 v[194:197], v[178:181], a[212:215], v[194:197]\n"
      "s_waitcnt lgkmcnt(4)\n"
      "v_bfe_u32 v108, v187, 8, 2\n"
      "v_and_b32_e64 v39, 48, v39\n"
      "v_cvt_pk_bf16_f32 v133, v132, v133\n"
      "v_mfma_f32_16x16x32_bf16 v[248:251], v[228:231], v[198:201], v[248:251]\n"
      "s_waitcnt vmcnt(11)\n"
      "s_barrier\n"
      "v_lshlrev_b32_e64 v108, 2, v108\n"
      "v_mfma_f32_16x16x32_bf16 v[146:149], v[224:227], v[198:201], v[146:149]\n"
      "ds_read_b128 v[228:231], v137 offset:24576\n"
      "v_cvt_pk_bf16_f32 v132, v130, v131\n"
      "v_bfe_u32 v187, v187, 4, 4\n"
      "v_mfma_f32_16x16x32_bf16 v[146:149], v[190:193], v[198:201], v[146:149]\n"
      "ds_read_b64_tr_b16 a[238:239], v78 offset:38912\n"
      "v_cvt_pk_bf16_f32 v197, v196, v197\n"
      "v_mfma_f32_16x16x32_bf16 v[146:149], v[252:255], v[198:201], v[146:149]\n"
      "ds_read_b128 v[252:255], v137 offset:28672\n"
      "s_waitcnt lgkmcnt(3)\n"
      "v_mfma_f32_16x16x32_bf16 v[146:149], v[138:141], v[198:201], v[146:149]\n"
      "ds_read_b128 v[224:227], v134 offset:24576\n"
      "v_cvt_pk_bf16_f32 v217, v216, v217\n"
      "v_mfma_f32_16x16x32_bf16 v[146:149], v[204:207], v[198:201], v[146:149]\n"
      "ds_read_b128 v[204:207], v134 offset:28672\n"
      "v_mul_f32 v251, 0x3eaaaaab, v251\n"
      "v_mfma_f32_16x16x32_bf16 v[190:193], v[114:117], a[168:171], 0\n"
      "ds_read_b128 v[178:181], v134 offset:32768\n"
      "v_cvt_pk_bf16_f32 v216, v214, v215\n"
      "v_mfma_f32_16x16x32_bf16 v[138:141], v[114:117], a[144:147], 0\n"
      "ds_read_b128 v[120:123], v137 offset:32768\n"
      "v_mul_f32 v250, 0x3eaaaaab, v250\n"
      "v_mfma_f32_16x16x32_bf16 v[138:141], v[156:159], a[148:151], v[138:141]\n"
      "ds_read_b64_tr_b16 a[232:233], v78 offset:24576\n"
      "v_mul_f32 v249, 0x3eaaaaab, v249\n"
      "v_mfma_f32_16x16x32_bf16 v[134:137], v[114:117], a[192:195], 0\n"
      "ds_read_b64_tr_b16 a[234:235], v78 offset:26624\n"
      "v_mul_f32 v248, 0x3eaaaaab, v248\n"
      "v_mfma_f32_16x16x32_bf16 v[134:137], v[156:159], a[196:199], v[134:137]\n"
      "ds_read_b128 v[96:99], v1 offset:12288\n"
      "s_waitcnt lgkmcnt(6)\n"
      "v_mfma_f32_16x16x32_bf16 v[112:115], v[228:231], v[198:201], 0\n"
      "ds_read_b128 v[152:155], v0 offset:6144\n"
      "s_waitcnt vmcnt(10)\n"
      "v_or3_b32 v126, v126, v127, v128\n"
      "v_mfma_f32_16x16x32_bf16 v[112:115], v[224:227], v[198:201], v[112:115]\n"
      "ds_read_b128 v[116:119], v1 offset:6144\n"
      "v_sub_u32 v187, v187, v108\n"
      "v_mfma_f32_16x16x32_bf16 v[112:115], v[252:255], v[198:201], v[112:115]\n"
      "s_waitcnt lgkmcnt(5)\n"
      "v_add_u32 v39, 123136, v39\n"
      "v_add_u32_e64 v227, s92, v39\n"
      "v_mfma_f32_16x16x32_bf16 v[170:173], v[142:145], v[208:211], v[44:47]\n"
      "ds_write_b128 v227, v[248:251]\n"
      "v_mfma_f32_16x16x32_bf16 v[190:193], v[156:159], a[172:175], v[190:193]\n"
      "ds_read_b64_tr_b16 v[224:225], v78 offset:24584\n"
      "v_mul_f32 v148, 0x3eaaaaab, v148\n"
      "v_mfma_f32_16x16x32_bf16 v[112:115], v[204:207], v[198:201], v[112:115]\n"
      "ds_read_b128 v[204:207], v10\n"
      "s_waitcnt vmcnt(9)\n"
      "v_or3_b32 v126, v126, v129, v203\n"
      "v_mfma_f32_16x16x32_bf16 v[112:115], v[120:123], v[198:201], v[112:115]\n"
      "ds_read_b64_tr_b16 v[254:255], v186 offset:38912\n"
      "s_waitcnt lgkmcnt(4)\n"
      "v_mfma_f32_16x16x32_bf16 v[138:141], v[152:155], a[152:155], v[138:141]\n"
      "ds_read_b128 v[120:123], v0 offset:12288\n"
      "v_mul_f32 v149, 0x3eaaaaab, v149\n"
      "v_mfma_f32_16x16x32_bf16 v[156:159], v[174:177], v[208:211], v[40:43]\n"
      "ds_read_b64_tr_b16 v[228:229], v78 offset:36872\n"
      "s_waitcnt vmcnt(8)\n"
      "v_or3_b32 v220, v220, v221, v222\n"
      "v_mfma_f32_16x16x32_bf16 v[112:115], v[178:181], v[198:201], v[112:115]\n"
      "ds_read_b128 v[178:181], v10 offset:32\n"
      "v_mov_b32_e64 v129, 0\n"
      "v_mfma_f32_16x16x32_bf16 v[190:193], v[152:155], a[176:179], v[190:193]\n"
      "ds_read_b64_tr_b16 a[236:237], v78 offset:36864\n"
      "v_mul_f32 v146, 0x3eaaaaab, v146\n"
      "v_mfma_f32_16x16x32_bf16 v[138:141], v[116:119], a[156:159], v[138:141]\n"
      "ds_write_b32 v31, v126 offset:6656\n"
      "v_mfma_f32_16x16x32_bf16 v[200:203], v[104:107], v[208:211], v[48:51]\n"
      "s_or_b32 s95, s95, s48\n"
      "v_mul_f32 v147, 0x3eaaaaab, v147\n"
      "v_mul_f32 v113, 0x3eaaaaab, v113\n"
      "v_mfma_f32_16x16x32_bf16 v[134:137], v[152:155], a[200:203], v[134:137]\n"
      "ds_write_b128 v227, v[146:149] offset:256\n"
      "v_mfma_f32_16x16x16_bf16 v[156:159], v[212:213], v[188:189], v[156:159]\n"
      "ds_read_b64_tr_b16 v[230:231], v78 offset:38920\n"
      "v_cvt_pk_bf16_f32 v196, v194, v195\n"
      "v_mfma_f32_16x16x32_bf16 v[146:149], v[142:145], v[166:169], v[56:59]\n"
      "s_waitcnt lgkmcnt(3)\n"
      "s_waitcnt vmcnt(7)\n"
      "v_or3_b32 v220, v220, v223, v163\n"
      "v_mul_f32 v114, 0x3eaaaaab, v114\n"
      "v_mfma_f32_16x16x32_bf16 v[190:193], v[116:119], a[180:183], v[190:193]\n"
      "ds_write_b32 v31, v220 offset:5632\n"
      "v_mfma_f32_16x16x32_bf16 v[190:193], v[120:123], a[184:187], v[190:193]\n"
      "ds_read_b64_tr_b16 v[252:253], v186 offset:36864\n"
      "v_mov_b32_e64 v109, 0\n"
      "v_mfma_f32_16x16x32_bf16 v[190:193], v[96:99], a[188:191], v[190:193]\n"
      "ds_read_b64 v[220:221], v10 offset:24\n"
      "v_mul_f32 v115, 0x3eaaaaab, v115\n"
      "v_mfma_f32_16x16x32_bf16 v[138:141], v[120:123], a[160:163], v[138:141]\n"
      "ds_read_b64_tr_b16 v[250:251], v186 offset:26624\n"
      "v_mul_f32 v112, 0x3eaaaaab, v112\n"
      "v_mfma_f32_16x16x32_bf16 v[138:141], v[96:99], a[164:167], v[138:141]\n"
      "ds_write_b128 v227, v[112:115] offset:512\n"
      "v_mfma_f32_16x16x32_bf16 v[162:165], v[178:181], v[166:169], v[26:29]\n"
      "ds_read_b64_tr_b16 v[248:249], v186 offset:24576\n"
      "ds_read_b64 v[222:223], v10 offset:16\n"
      "v_mfma_f32_16x16x32_bf16 v[134:137], v[116:119], a[204:207], v[134:137]\n"
      "ds_read_b64_tr_b16 v[226:227], v78 offset:26632\n"
      "v_cvt_pk_bf16_f32 v131, v192, v193\n"
      "v_mfma_f32_16x16x16_bf16 v[200:203], v[150:151], v[188:189], v[200:203]\n"
      "v_cvt_pk_bf16_f32 v214, v138, v139\n"
      "s_waitcnt lgkmcnt(0)\n"
      "s_barrier\n"
      "v_mov_b32 v108, 0x3f80\n"
      "v_mfma_f32_16x16x32_bf16 v[116:119], v[178:181], v[208:211], v[24:27]\n"
      "ds_read_b128 v[208:211], v9 offset:6656\n"
      "v_lshlrev_b32 v187, 4, v187\n"
      "v_mfma_f32_16x16x32_bf16 v[134:137], v[120:123], a[208:211], v[134:137]\n"
      "s_waitcnt lgkmcnt(5)\n"
      "v_cmp_gt_u32 s[80:81], 64, v187\n"
      "v_cvt_pk_bf16_f32 v215, v140, v141\n"
      "v_mfma_f32_16x16x32_bf16 v[134:137], v[96:99], a[212:215], v[134:137]\n"
      "ds_read_u16 v78, v11\n"
      "v_lshlrev_b64 v[108:109], v187, v[108:109]\n"
      "v_mfma_f32_16x16x32_bf16 v[112:115], v[104:107], v[166:169], v[60:63]\n"
      "ds_read_b128 v[120:123], v9 offset:5632\n"
      "v_cndmask_b32_e64 v186, 0, v108, s[80:81]\n"
      "v_mfma_f32_16x16x16_bf16 v[146:149], v[218:219], v[124:125], v[146:149]\n"
      "v_cndmask_b32_e64 v187, 0, v109, s[80:81]\n"
      "v_cndmask_b32_e64 v127, 0, v109, s[80:81]\n"
      "v_cvt_pk_bf16_f32 v195, v136, v137\n"
      "v_mfma_f32_16x16x16_bf16 v[116:119], v[220:221], v[188:189], v[116:119]\n"
      "s_cmp_ge_u32 s95, 0x40000000\n"
      "s_cselect_b32 s94, -1, -2\n"
      "v_cvt_pk_bf16_f32 v130, v190, v191\n"
      "v_mfma_f32_16x16x16_bf16 v[162:165], v[220:221], v[124:125], v[162:165]\n"
      "s_waitcnt lgkmcnt(2)\n"
      "v_or3_b32 v208, v208, v209, v210\n"
      "v_bitop3_b32 v208, v208, v211, s33 bitop3:0xa8\n"
      "v_mfma_f32_16x16x16_bf16 v[170:173], v[218:219], v[188:189], v[170:173]\n"
      "v_cmp_ne_u32 s[56:57], 0, v208\n"
      "v_cndmask_b32_e64 v126, 0, v108, s[80:81]\n"
      "s_or_b64 vcc, s[56:57], s[54:55]\n"
      "v_mfma_f32_16x16x32_bf16 v[152:155], v[174:177], v[166:169], v[52:55]\n"
      "s_cbranch_vccnz L_rare_1_%=\n"
      "L_back_2_%=:\n"
      "v_mfma_f32_16x16x32_bf16 v[188:191], v[178:181], v[182:185], v[28:31]\n"
      "ds_read_u16 v86, v11 offset:64\n"
      "v_cvt_pk_bf16_f32 v194, v134, v135\n"
      "v_mfma_f32_16x16x32_bf16 v[166:169], v[204:207], v[178:181], v[30:33]\n"
      "s_cbranch_vccnz L_rare_3_%=\n"
      "L_back_4_%=:\n"
      "v_mfma_f32_16x16x16_bf16 v[112:115], v[150:151], v[124:125], v[112:115]\n"
      "v_exp_f32 v157, v157\n"
      "v_exp_f32 v158, v158\n"
      "v_mfma_f32_16x16x16_bf16 v[152:155], v[212:213], v[124:125], v[152:155]\n"
      "v_exp_f32 v156, v156\n"
      "v_exp_f32_e64 v171, v171\n"
      "v_mfma_f32_16x16x32_bf16 v[134:137], v[142:145], v[182:185], v[68:71]\n"
      "s_waitcnt lgkmcnt(1)\n"
      "v_or3_b32 v120, v120, v121, v122\n"
      "v_exp_f32 v173, v173\n"
      "v_bitop3_b32 v120, v120, v123, s33 bitop3:0xa8\n"
      "v_mfma_f32_16x16x16_bf16 v[188:191], v[220:221], v[160:161], v[188:191]\n"
      "v_exp_f32 v170, v170\n"
      "v_exp_f32 v159, v159\n"
      "v_mfma_f32_16x16x32_bf16 v[122:125], v[204:207], v[142:145], v[30:33]\n"
      "s_cbranch_vccnz L_rare_5_%=\n"
      "L_back_6_%=:\n"
      "v_mfma_f32_16x16x16_bf16 v[166:169], v[222:223], v[220:221], v[166:169]\n"
      "v_exp_f32 v116, v116\n"
      "v_exp_f32 v147, v147\n"
      "v_exp_f32_e64 v203, v203\n"
      "v_cvt_pk_bf16_f32 v156, v156, v157\n"
      "v_mfma_f32_16x16x32_bf16 v[138:141], v[204:207], v[174:177], v[30:33]\n"
      "ds_read_b128 v[96:99], v39\n"
      "v_cmp_ne_u32 s[56:57], 0, v120\n"
      "v_mfma_f32_16x16x16_bf16 v[122:125], v[222:223], v[218:219], v[122:125]\n"
      "v_exp_f32 v114, v114\n"
      "v_exp_f32 v146, v146\n"
      "v_mfma_f32_16x16x32_bf16 v[190:193], v[174:177], v[182:185], v[64:67]\n"
      "ds_read_b128 v[142:145], v39 offset:64\n"
      "v_exp_f32 v155, v155\n"
      "v_mfma_f32_16x16x16_bf16 v[134:137], v[218:219], v[160:161], v[134:137]\n"
      "v_exp_f32 v112, v112\n"
      "v_exp_f32 v113, v113\n"
      "v_exp_f32 v162, v162\n"
      "v_cvt_pk_bf16_f32 v157, v158, v159\n"
      "v_mfma_f32_16x16x32_bf16 v[178:181], v[104:107], v[182:185], v[72:75]\n"
      "ds_read_b128 v[118:121], v39 offset:192\n"
      "v_exp_f32_e64 v154, v154\n"
      "v_mfma_f32_16x16x16_bf16 v[138:141], v[222:223], v[212:213], v[138:141]\n"
      "v_mov_b32 v184, 0\n"
      "v_exp_f32 v172, v172\n"
      "v_cvt_pk_bf16_f32 v158, v170, v171\n"
      "v_mov_b32_e64 v185, 0\n"
      "v_mfma_f32_16x16x32_bf16 v[168:171], v[204:207], v[104:107], v[30:33]\n"
      "s_waitcnt lgkmcnt(1)\n"
      "v_exp_f32 v152, v152\n"
      "v_mfma_f32_16x16x32_bf16 v[106:109], v[184:187], a[240:243], v[142:145]\n"
      "v_exp_f32_e64 v153, v153\n"
      "v_cndmask_b32 v138, v138, v122, s[58:59]\n"
      "v_mfma_f32_16x16x32_bf16 v[174:177], v[184:187], a[216:219], v[142:145]\n"
      "v_mov_b32 v128, 0\n"
      "v_cvt_pk_bf16_f32 v152, v152, v153\n"
      "v_exp_f32 v202, v202\n"
      "v_mfma_f32_16x16x16_bf16 v[168:171], v[222:223], v[150:151], v[168:171]\n"
      "v_exp_f32_e64 v148, v148\n"
      "v_cvt_pk_bf16_f32 v153, v154, v155\n"
      "v_mfma_f32_16x16x16_bf16 v[178:181], v[150:151], v[160:161], v[178:181]\n"
      "v_cvt_pk_bf16_f32 v159, v172, v173\n"
      "v_exp_f32_e64 v201, v201\n"
      "v_mfma_f32_16x16x32_bf16 v[122:125], v[184:187], v[232:235], v[142:145]\n"
      "v_cvt_pk_bf16_f32 v154, v146, v147\n"
      "v_cvt_pk_bf16_f32 v89, v202, v203\n"
      "v_mfma_f32_16x16x32_bf16 v[204:207], v[126:129], a[240:243], v[96:99]\n"
      "ds_read_u16 v82, v11 offset:32\n"
      "v_exp_f32_e64 v200, v200\n"
      "v_mfma_f32_16x16x32_bf16 v[220:223], v[126:129], a[216:219], v[96:99]\n"
      "s_waitcnt lgkmcnt(1)\n"
      "v_exp_f32 v149, v149\n"
      "v_cvt_pk_bf16_f32 v92, v112, v113\n"
      "v_mfma_f32_16x16x32_bf16 v[208:211], v[184:187], a[244:247], v[118:121]\n"
      "v_cvt_pk_bf16_f32 v155, v148, v149\n"
      "v_cvt_pk_bf16_f32 v106, v106, v107\n"
      "v_mfma_f32_16x16x32_bf16 v[110:113], v[126:129], v[232:235], v[96:99]\n"
      "v_cvt_pk_bf16_f32 v90, v116, 0\n"
      "v_cvt_pk_bf16_f32 v94, v162, 0\n"
      "v_mfma_f32_16x16x16_bf16 v[190:193], v[212:213], v[160:161], v[190:193]\n"
      "v_exp_f32 v115, v115\n"
      "s_waitcnt vmcnt(1)\n"
      "v_or3_b32 v100, v100, v101, v102\n"
      "v_accvgpr_write_b32 a242, v106\n"
      "v_cvt_pk_bf16_f32 v210, v210, v211\n"
      "v_mfma_f32_16x16x32_bf16 v[144:147], v[184:187], a[220:223], v[118:121]\n"
      "ds_read_b128 v[162:165], v39 offset:128\n"
      "v_cvt_pk_bf16_f32 v88, v200, v201\n"
      "v_cndmask_b32 v138, v138, v168, s[60:61]\n"
      "v_mfma_f32_16x16x32_bf16 v[200:203], v[32:35], v[156:159], 0\n"
      "v_cvt_pk_bf16_f32 v220, v220, v221\n"
      "v_cvt_pk_bf16_f32 v93, v114, v115\n"
      "v_mfma_f32_16x16x32_bf16 v[200:203], v[32:35], v[88:91], v[200:203]\n"
      "s_cbranch_vccnz L_rare_7_%=\n"
      "L_back_8_%=:\n"
      "v_mfma_f32_16x16x32_bf16 v[140:143], v[32:35], v[152:155], 0\n"
      "v_cvt_pk_bf16_f32 v233, v112, v113\n"
      "v_cndmask_b32 v138, v138, v166, s[62:63]\n"
      "v_mfma_f32_16x16x32_bf16 v[140:143], v[32:35], v[92:95], v[140:143]\n"
      "s_waitcnt lgkmcnt(1)\n"
      "v_exp_f32_e64 v178, v178\n"
      "v_cvt_pk_bf16_f32 v234, v122, v123\n"
      "s_waitcnt vmcnt(0)\n"
      "v_or3_b32 v100, v100, v103, v8\n"
      "v_mfma_f32_16x16x32_bf16 v[96:99], v[130:133], v[156:159], 0\n"
      "s_cbranch_vccnz L_rare_9_%=\n"
      "L_back_10_%=:\n"
      "v_mfma_f32_16x16x32_bf16 v[96:99], v[80:83], v[88:91], v[96:99]\n"
      "v_cvt_pk_bf16_f32 v208, v208, v209\n"
      "v_cvt_pk_bf16_f32 v108, v108, v109\n"
      "v_mfma_f32_16x16x32_bf16 v[166:169], v[130:133], v[152:155], 0\n"
      "v_cvt_pk_bf16_f32 v176, v176, v177\n"
      "v_cvt_pk_bf16_f32 v204, v204, v205\n"
      "v_mfma_f32_16x16x32_bf16 v[166:169], v[80:83], v[92:95], v[166:169]\n"
      "ds_read_b128 v[114:117], v39 offset:320\n"
      "v_rcp_f32 v200, v200\n"
      "v_mfma_f32_16x16x32_bf16 v[104:107], v[194:197], v[152:155], 0\n"
      "v_rcp_f32 v140, v140\n"
      "v_mfma_f32_16x16x32_bf16 v[104:107], v[84:87], v[92:95], v[104:107]\n"
      "s_cmp_gt_u32 s42, s44\n"
      "s_cselect_b32 s95, s94, s95\n"
      "v_exp_f32 v188, v188\n"
      "v_mfma_f32_16x16x32_bf16 v[148:151], v[214:217], v[152:155], 0\n"
      "v_exp_f32 v179, v179\n"
      "v_mfma_f32_16x16x32_bf16 v[148:151], v[76:79], v[92:95], v[148:151]\n"
      "v_cvt_pk_bf16_f32 v144, v144, v145\n"
      "v_cvt_pk_bf16_f32 v174, v174, v175\n"
      "v_mfma_f32_16x16x32_bf16 v[170:173], v[194:197], v[156:159], 0\n"
      "v_cvt_pk_bf16_f32 v222, v222, v223\n"
      "v_exp_f32_e64 v134, v134\n"
      "v_mfma_f32_16x16x32_bf16 v[170:173], v[84:87], v[88:91], v[170:173]\n"
      "s_waitcnt lgkmcnt(0)\n"
      "v_cvt_pk_bf16_f32 v235, v124, v125\n"
      "v_cvt_pk_bf16_f32 v146, v146, v147\n"
      "v_exp_f32 v193, v193\n"
      "v_mfma_f32_16x16x32_bf16 v[122:125], v[214:217], v[156:159], 0\n"
      "ds_write_b32 v31, v100 offset:4608\n"
      "v_mfma_f32_16x16x32_bf16 v[122:125], v[76:79], v[88:91], v[122:125]\n"
      "ds_read_b128 v[156:159], v39 offset:256\n"
      "v_exp_f32 v191, v191\n"
      "v_exp_f32 v138, v138\n"
      "v_cvt_pk_bf16_f32 v232, v110, v111\n"
      "v_pk_mul_f32 v[170:171], v[170:171], v[200:201] op_sel_hi:[1,0]\n"
      "v_mfma_f32_16x16x32_bf16 v[152:155], v[126:129], a[244:247], v[162:165]\n"
      "v_exp_f32_e64 v180, v180\n"
      "v_exp_f32_e64 v190, v190\n"
      "v_mfma_f32_16x16x32_bf16 v[110:113], v[184:187], a[224:227], v[114:117]\n"
      "v_exp_f32_e64 v181, v181\n"
      "v_exp_f32_e64 v192, v192\n"
      "v_accvgpr_write_b32 a243, v108\n"
      "v_pk_mul_f32 v[148:149], v[148:149], v[140:141] op_sel_hi:[1,0]\n"
      "v_pk_mul_f32 v[98:99], v[98:99], v[200:201] op_sel_hi:[1,0]\n"
      "v_cvt_pk_bf16_f32 v190, v190, v191\n"
      "v_exp_f32_e64 v137, v137\n"
      "v_exp_f32_e64 v136, v136\n"
      "v_cvt_pk_bf16_f32 v89, v180, v181\n"
      "v_pk_mul_f32 v[124:125], v[124:125], v[200:201] op_sel_hi:[1,0]\n"
      "v_exp_f32_e64 v135, v135\n"
      "v_pk_mul_f32 v[104:105], v[104:105], v[140:141] op_sel_hi:[1,0]\n"
      "v_pk_mul_f32 v[122:123], v[122:123], v[200:201] op_sel_hi:[1,0]\n"
      "v_cvt_pk_bf16_f32 v191, v192, v193\n"
      "v_cvt_pk_bf16_f32 v122, v122, v123\n"
      "v_accvgpr_write_b32 a219, v176\n"
      "v_pk_mul_f32 v[96:97], v[96:97], v[200:201] op_sel_hi:[1,0]\n"
      "v_cvt_pk_bf16_f32 v138, v138, v138\n"
      "v_cvt_pk_bf16_f32 v170, v170, v171\n"
      "v_cvt_pk_bf16_f32 v148, v148, v149\n"
      "v_cvt_pk_bf16_f32 v110, v110, v111\n"
      "v_pk_mul_f32 v[106:107], v[106:107], v[140:141] op_sel_hi:[1,0]\n"
      "v_pk_mul_f32 v[168:169], v[168:169], v[140:141] op_sel_hi:[1,0]\n"
      "v_pk_mul_f32 v[166:167], v[166:167], v[140:141] op_sel_hi:[1,0]\n"
      "v_pk_mul_f32 v[172:173], v[172:173], v[200:201] op_sel_hi:[1,0]\n"
      "v_cvt_pk_bf16_f32 v152, v152, v153\n"
      "v_pk_mul_f32 v[150:151], v[150:151], v[140:141] op_sel_hi:[1,0]\n"
      "v_accvgpr_write_b32 a218, v174\n"
      "v_mfma_f32_16x16x32_bf16 v[174:177], v[184:187], a[248:251], v[114:117]\n"
      "s_waitcnt lgkmcnt(0)\n"
      "v_cvt_pk_bf16_f32 v123, v124, v125\n"
      "v_cvt_pk_bf16_f32 v192, v134, v135\n"
      "v_cvt_pk_bf16_f32 v104, v104, v105\n"
      "v_mfma_f32_16x16x32_bf16 v[180:183], v[126:129], v[240:243], v[156:159]\n"
      "v_cvt_pk_bf16_f32 v124, v96, v97\n"
      "v_cvt_pk_bf16_f32 v105, v106, v107\n"
      "v_mfma_f32_16x16x32_bf16 v[140:143], v[184:187], v[240:243], v[114:117]\n"
      "ds_write_b64 v4, v[104:105] offset:256\n"
      "v_accvgpr_write_b32 a247, v210\n"
      "v_mfma_f32_16x16x32_bf16 v[106:109], v[126:129], a[248:251], v[156:159]\n"
      "v_cvt_pk_bf16_f32 v88, v178, v179\n"
      "v_cvt_pk_bf16_f32 v176, v176, v177\n"
      "v_mfma_f32_16x16x32_bf16 v[200:203], v[184:187], v[236:239], v[118:121]\n"
      "s_add_u32 s43, s43, 1\n"
      "v_cvt_pk_bf16_f32 v193, v136, v137\n"
      "v_cvt_pk_bf16_f32 v125, v98, v99\n"
      "v_cvt_pk_bf16_f32 v242, v140, v141\n"
      "v_mfma_f32_16x16x32_bf16 v[134:137], v[126:129], a[220:223], v[162:165]\n"
      "v_cvt_pk_bf16_f32 v154, v154, v155\n"
      "v_cvt_pk_bf16_f32 v90, v188, 0\n"
      "v_mfma_f32_16x16x32_bf16 v[102:105], v[126:129], v[236:239], v[162:165]\n"
      "ds_read_b128 v[210:213], v39 offset:384\n"
      "v_accvgpr_write_b32 a244, v152\n"
      "v_mfma_f32_16x16x32_bf16 v[118:121], v[126:129], a[224:227], v[156:159]\n"
      "ds_read_b128 v[96:99], v39 offset:448\n"
      "v_cvt_pk_bf16_f32 v240, v180, v181\n"
      "v_accvgpr_write_b32 a222, v144\n"
      "v_mfma_f32_16x16x32_bf16 v[156:159], v[32:35], v[190:193], 0\n"
      "ds_write_b128 v3, v[122:125]\n"
      "v_mfma_f32_16x16x32_bf16 v[156:159], v[32:35], v[88:91], v[156:159]\n"
      "v_cvt_pk_bf16_f32 v174, v174, v175\n"
      "v_cvt_pk_bf16_f32 v238, v200, v201\n"
      "v_mfma_f32_16x16x32_bf16 v[122:125], v[130:133], v[190:193], 0\n"
      "s_and_b32 s86, s86, -4\n"
      "v_accvgpr_write_b32 a240, v204\n"
      "v_cvt_pk_bf16_f32 v108, v108, v109\n"
      "v_cvt_pk_bf16_f32 v149, v150, v151\n"
      "v_cvt_pk_bf16_f32 v239, v202, v203\n"
      "v_cvt_pk_bf16_f32 v236, v102, v103\n"
      "v_mfma_f32_16x16x32_bf16 v[122:125], v[80:83], v[88:91], v[122:125]\n"
      "s_waitcnt lgkmcnt(1)\n"
      "v_rcp_f32_e64 v156, v156\n"
      "v_mfma_f32_16x16x32_bf16 v[202:205], v[126:129], a[228:231], v[210:213]\n"
      "ds_write_b16 v13, v138\n"
      "v_mfma_f32_16x16x32_bf16 v[114:117], v[126:129], a[252:255], v[210:213]\n"
      "v_cvt_pk_bf16_f32 v243, v142, v143\n"
      "v_accvgpr_write_b32 a251, v176\n"
      "v_mfma_f32_16x16x32_bf16 v[142:145], v[214:217], v[190:193], 0\n"
      "ds_read_b128 v[100:103], v39 offset:640\n"
      "v_cvt_pk_bf16_f32 v171, v172, v173\n"
      "v_mfma_f32_16x16x32_bf16 v[142:145], v[76:79], v[88:91], v[142:145]\n"
      "ds_read_b128 v[158:161], v39 offset:576\n"
      "v_cvt_pk_bf16_f32 v204, v204, v205\n"
      "v_cvt_pk_bf16_f32 v151, v168, v169\n"
      "v_mfma_f32_16x16x32_bf16 v[138:141], v[194:197], v[190:193], 0\n"
      "ds_read_b128 v[188:191], v39 offset:704\n"
      "v_cvt_pk_bf16_f32 v150, v166, v167\n"
      "v_cvt_pk_bf16_f32 v206, v206, v207\n"
      "v_mfma_f32_16x16x32_bf16 v[138:141], v[84:87], v[88:91], v[138:141]\n"
      "v_cvt_pk_bf16_f32 v114, v114, v115\n"
      "v_cvt_pk_bf16_f32 v116, v116, v117\n"
      "v_accvgpr_write_b32 a226, v110\n"
      "v_cvt_pk_bf16_f32 v134, v134, v135\n"
      "v_mfma_f32_16x16x32_bf16 v[176:179], v[184:187], a[252:255], v[96:99]\n"
      "v_cvt_pk_bf16_f32 v136, v136, v137\n"
      "v_accvgpr_write_b32 a250, v174\n"
      "v_cvt_pk_bf16_f32 v202, v202, v203\n"
      "v_accvgpr_write_b32 a252, v114\n"
      "v_mfma_f32_16x16x32_bf16 v[172:175], v[184:187], v[244:247], v[96:99]\n"
      "ds_write_b64 v4, v[170:171]\n"
      "v_mfma_f32_16x16x32_bf16 v[164:167], v[184:187], a[228:231], v[96:99]\n"
      "s_waitcnt lgkmcnt(2)\n"
      "v_accvgpr_write_b32 a223, v146\n"
      "v_accvgpr_write_b32 a216, v220\n"
      "v_mfma_f32_16x16x32_bf16 v[218:221], v[184:187], a[232:235], v[158:161]\n"
      "ds_read_b128 v[168:171], v39 offset:512\n"
      "v_cvt_pk_bf16_f32 v120, v120, v121\n"
      "v_mfma_f32_16x16x32_bf16 v[198:201], v[126:129], v[244:247], v[210:213]\n"
      "s_waitcnt lgkmcnt(2)\n"
      "v_cvt_pk_bf16_f32 v246, v172, v173\n"
      "v_accvgpr_write_b32 a245, v154\n"
      "v_cvt_pk_bf16_f32 v241, v182, v183\n"
      "v_cvt_pk_bf16_f32 v166, v166, v167\n"
      "v_mfma_f32_16x16x32_bf16 v[180:183], v[184:187], v[252:255], v[188:191]\n"
      "ds_read_b128 v[210:213], v14\n"
      "v_cvt_pk_bf16_f32 v176, v176, v177\n"
      "v_accvgpr_write_b32 a246, v208\n"
      "v_cvt_pk_bf16_f32 v178, v178, v179\n"
      "v_cvt_pk_bf16_f32 v245, v200, v201\n"
      "v_accvgpr_write_b32 a217, v222\n"
      "v_mfma_f32_16x16x32_bf16 v[96:99], v[126:129], a[236:239], v[100:103]\n"
      "v_cvt_pk_bf16_f32 v220, v220, v221\n"
      "v_cvt_pk_bf16_f32 v118, v118, v119\n"
      "v_cvt_pk_bf16_f32 v164, v164, v165\n"
      "v_accvgpr_write_b32 a224, v118\n"
      "v_accvgpr_write_b32 a254, v176\n"
      "v_pk_mul_f32 v[138:139], v[138:139], v[156:157] op_sel_hi:[1,0]\n"
      "v_pk_mul_f32 v[140:141], v[140:141], v[156:157] op_sel_hi:[1,0]\n"
      "v_accvgpr_write_b32 a225, v120\n"
      "v_pk_mul_f32 v[142:143], v[142:143], v[156:157] op_sel_hi:[1,0]\n"
      "v_mfma_f32_16x16x32_bf16 v[118:121], v[184:187], a[236:239], v[188:191]\n"
      "s_waitcnt lgkmcnt(1)\n"
      "v_accvgpr_write_b32 a228, v202\n"
      "v_cvt_pk_bf16_f32 v112, v112, v113\n"
      "v_cvt_pk_bf16_f32 v106, v106, v107\n"
      "v_accvgpr_write_b32 a249, v108\n"
      "v_cvt_pk_bf16_f32 v142, v142, v143\n"
      "v_cvt_pk_bf16_f32 v247, v174, v175\n"
      "v_pk_mul_f32 v[144:145], v[144:145], v[156:157] op_sel_hi:[1,0]\n"
      "v_pk_mul_f32 v[124:125], v[124:125], v[156:157] op_sel_hi:[1,0]\n"
      "v_pk_mul_f32 v[122:123], v[122:123], v[156:157] op_sel_hi:[1,0]\n"
      "v_mfma_f32_16x16x32_bf16 v[108:111], v[184:187], v[228:231], v[188:191]\n"
      "ds_write_b128 v3, v[148:151] offset:256\n"
      "v_mfma_f32_16x16x32_bf16 v[188:191], v[126:129], v[252:255], v[100:103]\n"
      "s_cmp_ge_u32 s95, 0x40000000\n"
      "s_cselect_b64 vcc, -1, s[56:57]\n"
      "v_cvt_pk_bf16_f32 v255, v182, v183\n"
      "v_mfma_f32_16x16x32_bf16 v[146:149], v[126:129], a[232:235], v[168:171]\n"
      "ds_read_b128 v[174:177], v14 offset:64\n"
      "v_accvgpr_write_b32 a255, v178\n"
      "v_accvgpr_write_b32 a231, v166\n"
      "v_mfma_f32_16x16x32_bf16 v[200:203], v[126:129], v[228:231], v[100:103]\n"
      "v_accvgpr_write_b32 a221, v136\n"
      "v_cvt_pk_bf16_f32 v143, v144, v145\n"
      "v_accvgpr_write_b32 a253, v116\n"
      "v_accvgpr_write_b32 a235, v220\n"
      "v_cvt_pk_bf16_f32 v252, v188, v189\n"
      "v_cvt_pk_bf16_f32 v96, v96, v97\n"
      "v_accvgpr_write_b32 a227, v112\n"
      "v_cvt_pk_bf16_f32 v237, v104, v105\n"
      "v_cvt_pk_bf16_f32 v138, v138, v139\n"
      "v_cvt_pk_bf16_f32 v145, v124, v125\n"
      "v_cvt_pk_bf16_f32 v118, v118, v119\n"
      "v_cvt_pk_bf16_f32 v139, v140, v141\n"
      "v_accvgpr_write_b32 a220, v134\n"
      "v_cvt_pk_bf16_f32 v228, v200, v201\n"
      "v_cvt_pk_bf16_f32 v98, v98, v99\n"
      "v_accvgpr_write_b32 a237, v98\n"
      "v_cvt_pk_bf16_f32 v218, v218, v219\n"
      "v_mfma_f32_16x16x32_bf16 v[220:223], v[184:187], v[248:251], v[158:161]\n"
      "ds_write_b64 v4, v[138:139] offset:512\n"
      "v_mfma_f32_16x16x32_bf16 v[138:141], v[126:129], v[224:227], v[168:171]\n"
      "v_cvt_pk_bf16_f32 v148, v148, v149\n"
      "v_cvt_pk_bf16_f32 v144, v122, v123\n"
      "v_cvt_pk_bf16_f32 v244, v198, v199\n"
      "v_mfma_f32_16x16x32_bf16 v[122:125], v[184:187], v[224:227], v[158:161]\n"
      "s_waitcnt lgkmcnt(1)\n"
      "v_accvgpr_write_b32 a234, v218\n"
      "v_cvt_pk_bf16_f32 v146, v146, v147\n"
      "v_accvgpr_write_b32 a230, v164\n"
      "v_mfma_f32_16x16x32_bf16 v[186:189], v[126:129], v[248:251], v[168:171]\n"
      "ds_write_b128 v3, v[142:145] offset:512\n"
      "v_cvt_pk_bf16_f32 v253, v190, v191\n"
      "v_cvt_pk_bf16_f32 v120, v120, v121\n"
      "v_cvt_pk_bf16_f32 v230, v108, v109\n"
      "v_accvgpr_write_b32 a233, v148\n"
      "v_cvt_pk_bf16_f32 v225, v140, v141\n"
      "v_accvgpr_write_b32 a238, v118\n"
      "v_cvt_pk_bf16_f32 v226, v122, v123\n"
      "v_accvgpr_write_b32 a241, v206\n"
      "v_cvt_pk_bf16_f32 v251, v222, v223\n"
      "v_accvgpr_write_b32 a232, v146\n"
      "v_cvt_pk_bf16_f32 v231, v110, v111\n"
      "v_cvt_pk_bf16_f32 v250, v220, v221\n"
      "v_cvt_pk_bf16_f32 v224, v138, v139\n"
      "v_mfma_f32_16x16x32_bf16 v[150:153], v[210:213], v[32:35], 0\n"
      "s_add_u32 s42, s42, 1\n"
      "v_cvt_pk_bf16_f32 v229, v202, v203\n"
      "v_accvgpr_write_b32 a248, v106\n"
      "v_cvt_pk_bf16_f32 v249, v188, v189\n"
      "v_accvgpr_write_b32 a239, v120\n"
      "v_cvt_pk_bf16_f32 v248, v186, v187\n"
      "v_cvt_pk_bf16_f32 v227, v124, v125\n"
      "v_accvgpr_write_b32 a229, v204\n"
      "v_cvt_pk_bf16_f32 v254, v180, v181\n"
      "v_accvgpr_write_b32 a236, v96\n"
      "v_mfma_f32_16x16x32_bf16 v[150:153], v[174:177], v[32:35], v[150:153]\n"
      "s_add_u32 s10, s98, L_cold_%=-L_ipf0_%=\n"
      "s_addc_u32 s11, s99, 0\n"
      "s_mov_b32 s12, L_cend_%=-L_cold_%=\n"
      "s_sub_u32 s9, s9, 1280\n"
      "v_subrev_u32 v124, 115456, v9\n"
      "s_movk_i32 s13, 80\n"
      "v_cmp_gt_u32 s[14:15], s13, v124\n"
      "v_lshl_add_u32 v124, v124, 3, s9\n"
      "s_cmp_lt_u32 s40, 2\n"
      "s_cselect_b64 s[0:1], s[16:17], s[10:11]\n"
      "s_cselect_b32 s2, s18, s12\n"
      "s_cselect_b32 s8, -1, 0\n"
      "s_cselect_b64 s[14:15], 0, s[14:15]\n"
      "s_mov_b32 s3, s19\n"
      "s_add_u32 s6, s64, 16384\n"
      "s_cmp_lt_u32 s40, 2\n"
      "s_cselect_b32 s6, s6, 122112\n"
      "s_add_u32 s7, s64, 34816\n"
      "s_cmp_lt_u32 s40, 2\n"
      "s_cselect_b32 s7, s7, 122112\n"
      "s_and_b32 s5, s84, s8\n"
      "v_cndmask_b32 v15, v15, v124, s[14:15]\n"
      "s_waitcnt lgkmcnt(0)\n"
      "s_barrier\n"
      "s_or_b64 vcc, vcc, s[54:55]\n"
      "s_cbranch_vccnz L_disp1_%=\n"
      "s_nop 7\n"
      "s_nop 7\n"
      "L_loop1_%=:\n"
      "v_mfma_f32_16x16x32_bf16 v[164:167], v[210:213], v[214:217], 0\n"
      "ds_read_b128 v[160:163], v0 offset:18432\n"
      "v_mfma_f32_16x16x32_bf16 v[164:167], v[174:177], v[76:79], v[164:167]\n"
      "ds_read_b128 v[100:103], v9\n"
      "ds_read_b128 v[134:137], v0 offset:24576\n"
      "v_mfma_f32_16x16x32_bf16 v[170:173], v[210:213], v[130:133], 0\n"
      "ds_read_b128 v[116:119], v1 offset:30720\n"
      "s_add_u32 m0, s64, 0\n"
      "v_mfma_f32_16x16x32_bf16 v[170:173], v[174:177], v[80:83], v[170:173]\n"
      "buffer_load_dwordx4 v16, s[16:19], s84 offen  lds\n"
      "v_mfma_f32_16x16x32_bf16 v[128:131], v[210:213], v[194:197], 0\n"
      "ds_read_b128 v[210:213], v1 offset:18432\n"
      "s_add_u32 m0, s64, 4096\n"
      "v_mfma_f32_16x16x32_bf16 v[128:131], v[174:177], v[84:87], v[128:131]\n"
      "ds_read_b128 v[96:99], v1 offset:24576\n"
      "s_waitcnt lgkmcnt(5)\n"
      "v_mfma_f32_16x16x32_bf16 v[166:169], a[0:3], v[160:163], 0\n"
      "ds_read_b128 v[198:201], v1 offset:26624\n"
      "v_readlane_b32 s46, v37, s42\n"
      "v_mfma_f32_16x16x32_bf16 v[190:193], a[24:27], v[160:163], 0\n"
      "s_waitcnt lgkmcnt(5)\n"
      "v_add_f32 v102, v102, v103\n"
      "v_add_f32 v100, v100, v101\n"
      "v_mfma_f32_16x16x32_bf16 v[178:181], a[48:51], v[160:163], 0\n"
      "ds_read_b128 v[112:115], v0 offset:30720\n"
      "v_add_f32 v100, v100, v102\n"
      "v_mfma_f32_16x16x32_bf16 v[108:111], a[72:75], v[160:163], 0\n"
      "buffer_load_dwordx4 v17, s[16:19], s84 offen  lds\n"
      "v_mfma_f32_16x16x32_bf16 v[146:149], a[96:99], v[160:163], 0\n"
      "s_waitcnt lgkmcnt(3)\n"
      "v_rsq_f32 v100, v100\n"
      "v_mfma_f32_16x16x32_bf16 v[202:205], a[120:123], v[160:163], 0\n"
      "s_add_u32 m0, s64, 8192\n"
      "v_mul_f32 v100, 0x415db3d7, v100\n"
      "v_mov_b32 v102, v100\n"
      "v_mfma_f32_16x16x32_bf16 v[166:169], a[4:7], v[210:213], v[166:169]\n"
      "s_nop 0\n"
      "v_permlane32_swap_b32_e64 v100, v102\n"
      "v_mfma_f32_16x16x32_bf16 v[166:169], a[8:11], v[134:137], v[166:169]\n"
      "ds_read_b128 v[174:177], v9 offset:4608\n"
      "v_cndmask_b32 v164, v164, v170, s[58:59]\n"
      "v_mfma_f32_16x16x32_bf16 v[190:193], a[28:31], v[210:213], v[190:193]\n"
      "v_mov_b32 v101, v100\n"
      "v_mul_f32 v183, v102, v183\n"
      "v_mfma_f32_16x16x32_bf16 v[190:193], a[32:35], v[134:137], v[190:193]\n"
      "ds_read_b128 v[138:141], v1 offset:20480\n"
      "v_readlane_b32 s47, v38, s43\n"
      "v_mfma_f32_16x16x32_bf16 v[178:181], a[52:55], v[210:213], v[178:181]\n"
      "buffer_load_dwordx4 v18, s[16:19], s84 offen  lds\n"
      "v_mfma_f32_16x16x32_bf16 v[178:181], a[56:59], v[134:137], v[178:181]\n"
      "v_permlane16_swap_b32 v100, v101\n"
      "v_mfma_f32_16x16x32_bf16 v[108:111], a[76:79], v[210:213], v[108:111]\n"
      "v_mul_f32 v123, v101, v123\n"
      "v_mul_f32_e64 v220, v101, v220\n"
      "v_mfma_f32_16x16x32_bf16 v[108:111], a[80:83], v[134:137], v[108:111]\n"
      "s_add_u32 m0, s64, 12288\n"
      "v_mul_f32 v159, v100, v159\n"
      "v_mul_f32 v187, v100, v187\n"
      "v_mfma_f32_16x16x32_bf16 v[146:149], a[100:103], v[210:213], v[146:149]\n"
      "v_mul_f32 v120, v101, v120\n"
      "v_mul_f32 v218, v101, v218\n"
      "v_mfma_f32_16x16x32_bf16 v[146:149], a[104:107], v[134:137], v[146:149]\n"
      "buffer_load_dwordx4 v19, s[16:19], s84 offen  lds\n"
      "v_mfma_f32_16x16x32_bf16 v[202:205], a[124:127], v[210:213], v[202:205]\n"
      "v_mul_f32 v121, v101, v121\n"
      "v_mul_f32 v189, v100, v189\n"
      "v_mfma_f32_16x16x32_bf16 v[202:205], a[128:131], v[134:137], v[202:205]\n"
      "s_waitcnt lgkmcnt(0)\n"
      "v_mul_f32 v143, v100, v143\n"
      "v_mul_f32 v142, v100, v142\n"
      "v_mfma_f32_16x16x32_bf16 v[166:169], a[12:15], v[96:99], v[166:169]\n"
      "v_mul_f32 v145, v100, v145\n"
      "v_mul_f32 v219, v101, v219\n"
      "v_mfma_f32_16x16x32_bf16 v[166:169], a[16:19], v[112:115], v[166:169]\n"
      "v_cvt_pk_bf16_f32 v120, v120, v121\n"
      "v_mul_f32 v127, v102, v127\n"
      "v_mfma_f32_16x16x32_bf16 v[166:169], a[20:23], v[116:119], v[166:169]\n"
      "s_mov_b32 m0, s6\n"
      "v_mul_f32 v186, v100, v186\n"
      "v_cndmask_b32 v164, v164, v128, s[60:61]\n"
      "v_mfma_f32_16x16x32_bf16 v[190:193], a[36:39], v[96:99], v[190:193]\n"
      "v_mov_b32_dpp v150, v150 quad_perm:[0,0,0,0] row_mask:0xf bank_mask:0xf\n"
      "v_cvt_pk_bf16_f32 v186, v186, v187\n"
      "v_mfma_f32_16x16x32_bf16 v[190:193], a[40:43], v[112:115], v[190:193]\n"
      "v_mul_f32 v188, v100, v188\n"
      "v_fma_f32 v76, v166, v166, s51\n"
      "v_mfma_f32_16x16x32_bf16 v[190:193], a[44:47], v[116:119], v[190:193]\n"
      "v_cvt_pk_bf16_f32 v187, v188, v189\n"
      "v_mul_f32 v104, v101, v104\n"
      "v_mfma_f32_16x16x32_bf16 v[178:181], a[60:63], v[96:99], v[178:181]\n"
      "buffer_load_dwordx4 v15, s[0:3], s5 offen  lds\n"
      "v_mfma_f32_16x16x32_bf16 v[178:181], a[64:67], v[112:115], v[178:181]\n"
      "v_rcp_f32 v150, v150\n"
      "v_mfma_f32_16x16x32_bf16 v[178:181], a[68:71], v[116:119], v[178:181]\n"
      "v_mul_f32 v144, v100, v144\n"
      "v_cvt_pk_bf16_f32 v188, v142, v143\n"
      "v_mfma_f32_16x16x32_bf16 v[108:111], a[84:87], v[96:99], v[108:111]\n"
      "v_cvt_pk_bf16_f32 v189, v144, v145\n"
      "v_fmac_f32_e64 v76, v167, v167\n"
      "v_mfma_f32_16x16x32_bf16 v[108:111], a[88:91], v[112:115], v[108:111]\n"
      "ds_read_b128 v[142:145], v0 offset:26624\n"
      "v_or3_b32 v174, v174, v175, v176\n"
      "v_mfma_f32_16x16x32_bf16 v[108:111], a[92:95], v[116:119], v[108:111]\n"
      "buffer_load_dwordx4 v[170:173], v36, s[28:31], s86 offen\n"
      "v_mfma_f32_16x16x32_bf16 v[146:149], a[108:111], v[96:99], v[146:149]\n"
      "v_mul_f32 v105, v101, v105\n"
      "v_fmac_f32 v76, v168, v168\n"
      "v_mfma_f32_16x16x32_bf16 v[146:149], a[112:115], v[112:115], v[146:149]\n"
      "ds_write_b128 v5, v[186:189] offset:18432\n"
      "v_mfma_f32_16x16x32_bf16 v[146:149], a[116:119], v[116:119], v[146:149]\n"
      "v_mul_f32 v122, v101, v122\n"
      "v_fmac_f32 v76, v169, v169\n"
      "v_mfma_f32_16x16x32_bf16 v[202:205], a[132:135], v[96:99], v[202:205]\n"
      "v_fma_f32 v151, v108, v108, s51\n"
      "v_mul_f32 v221, v101, v221\n"
      "v_mfma_f32_16x16x32_bf16 v[202:205], a[136:139], v[112:115], v[202:205]\n"
      "v_fmac_f32 v151, v109, v109\n"
      "v_fmac_f32 v76, v190, v190\n"
      "v_mfma_f32_16x16x32_bf16 v[202:205], a[140:143], v[116:119], v[202:205]\n"
      "v_mul_f32 v106, v101, v106\n"
      "v_fmac_f32 v151, v110, v110\n"
      "v_mfma_f32_16x16x32_bf16 v[214:217], v[160:163], a[144:147], 0\n"
      "v_mul_f32 v206, v102, v206\n"
      "v_mfma_f32_16x16x32_bf16 v[214:217], v[210:213], a[148:151], v[214:217]\n"
      "buffer_load_dword v88, v29, s[28:31], s86 offen\n"
      "v_mfma_f32_16x16x32_bf16 v[214:217], v[134:137], a[152:155], v[214:217]\n"
      "ds_read_b128 v[186:189], v0 offset:20480\n"
      "v_mul_f32_e64 v156, v100, v156\n"
      "v_mfma_f32_16x16x32_bf16 v[214:217], v[96:99], a[156:159], v[214:217]\n"
      "v_cvt_pk_bf16_f32 v121, v122, v123\n"
      "v_mul_f32 v107, v101, v107\n"
      "v_mfma_f32_16x16x32_bf16 v[214:217], v[112:115], a[160:163], v[214:217]\n"
      "v_mul_f32 v158, v100, v158\n"
      "v_cvt_pk_bf16_f32 v122, v218, v219\n"
      "v_mfma_f32_16x16x32_bf16 v[214:217], v[116:119], a[164:167], v[214:217]\n"
      "v_mul_f32 v125, v102, v125\n"
      "v_mul_f32 v126, v102, v126\n"
      "v_mfma_f32_16x16x32_bf16 v[130:133], v[160:163], a[168:171], 0\n"
      "v_cvt_pk_bf16_f32 v123, v220, v221\n"
      "v_fmac_f32 v151, v111, v111\n"
      "v_mfma_f32_16x16x32_bf16 v[130:133], v[210:213], a[172:175], v[130:133]\n"
      "v_mul_f32 v209, v102, v209\n"
      "v_fmac_f32_e64 v151, v146, v146\n"
      "v_mfma_f32_16x16x32_bf16 v[130:133], v[134:137], a[176:179], v[130:133]\n"
      "ds_write_b128 v5, v[120:123] offset:24576\n"
      "v_mfma_f32_16x16x32_bf16 v[130:133], v[96:99], a[180:183], v[130:133]\n"
      "v_fmac_f32_e64 v76, v191, v191\n"
      "v_cvt_pk_bf16_f32 v214, v214, v215\n"
      "v_mfma_f32_16x16x32_bf16 v[130:133], v[112:115], a[184:187], v[130:133]\n"
      "v_fmac_f32 v76, v192, v192\n"
      "v_fmac_f32 v151, v147, v147\n"
      "v_mfma_f32_16x16x32_bf16 v[130:133], v[116:119], a[188:191], v[130:133]\n"
      "ds_read_b128 v[218:221], v1 offset:32768\n"
      "v_mul_f32 v157, v100, v157\n"
      "v_mfma_f32_16x16x32_bf16 v[194:197], v[160:163], a[192:195], 0\n"
      "v_cvt_pk_bf16_f32 v104, v104, v105\n"
      "v_fmac_f32 v151, v148, v148\n"
      "v_mfma_f32_16x16x32_bf16 v[194:197], v[210:213], a[196:199], v[194:197]\n"
      "v_readlane_b32 s45, v37, s43\n"
      "v_mul_f32 v184, v102, v184\n"
      "v_mfma_f32_16x16x32_bf16 v[194:197], v[134:137], a[200:203], v[194:197]\n"
      "s_waitcnt lgkmcnt(2)\n"
      "v_bitop3_b32 v174, v174, v177, s33 bitop3:0xa8\n"
      "v_fmac_f32 v151, v149, v149\n"
      "v_mfma_f32_16x16x32_bf16 v[194:197], v[96:99], a[204:207], v[194:197]\n"
      "ds_read_b128 v[134:137], v0 offset:32768\n"
      "v_mul_f32 v124, v102, v124\n"
      "v_mfma_f32_16x16x32_bf16 v[194:197], v[112:115], a[208:211], v[194:197]\n"
      "s_or_b32 s95, s45, s46\n"
      "v_fmac_f32 v151, v202, v202\n"
      "v_fmac_f32 v151, v203, v203\n"
      "v_mfma_f32_16x16x32_bf16 v[194:197], v[116:119], a[212:215], v[194:197]\n"
      "ds_read_b128 v[118:121], v0 offset:28672\n"
      "v_mul_f32 v208, v102, v208\n"
      "v_mfma_f32_16x16x32_bf16 v[210:213], a[0:3], v[186:189], 0\n"
      "v_fmac_f32 v151, v204, v204\n"
      "v_fmac_f32 v151, v205, v205\n"
      "v_mfma_f32_16x16x32_bf16 v[210:213], a[4:7], v[138:141], v[210:213]\n"
      "v_cvt_pk_bf16_f32 v156, v156, v157\n"
      "v_fmac_f32 v76, v193, v193\n"
      "v_mfma_f32_16x16x32_bf16 v[210:213], a[8:11], v[142:145], v[210:213]\n"
      "v_fmac_f32 v76, v178, v178\n"
      "v_cmp_ne_u32 s[56:57], 0, v174\n"
      "v_mfma_f32_16x16x32_bf16 v[210:213], a[12:15], v[198:201], v[210:213]\n"
      "s_waitcnt lgkmcnt(1)\n"
      "v_mul_f32 v182, v102, v182\n"
      "v_fmac_f32 v76, v179, v179\n"
      "v_mfma_f32_16x16x32_bf16 v[210:213], a[16:19], v[134:137], v[210:213]\n"
      "v_mul_f32 v207, v102, v207\n"
      "v_fmac_f32_e64 v76, v180, v180\n"
      "v_mfma_f32_16x16x32_bf16 v[210:213], a[20:23], v[218:221], v[210:213]\n"
      "v_cvt_pk_bf16_f32 v124, v124, v125\n"
      "v_fmac_f32 v76, v181, v181\n"
      "v_mfma_f32_16x16x32_bf16 v[174:177], a[24:27], v[186:189], 0\n"
      "s_or_b32 s95, s95, s48\n"
      "v_mul_f32 v164, v150, v164\n"
      "v_cvt_pk_bf16_f32 v125, v126, v127\n"
      "v_mfma_f32_16x16x32_bf16 v[174:177], a[28:31], v[138:141], v[174:177]\n"
      "v_permlane16_swap_b32 v76, v151\n"
      "v_mfma_f32_16x16x32_bf16 v[174:177], a[32:35], v[142:145], v[174:177]\n"
      "v_fma_f32 v116, v210, v210, s51\n"
      "v_cvt_pk_bf16_f32 v157, v158, v159\n"
      "v_mfma_f32_16x16x32_bf16 v[174:177], a[36:39], v[198:201], v[174:177]\n"
      "ds_write_b64 v6, v[156:157] offset:18432\n"
      "v_mfma_f32_16x16x32_bf16 v[174:177], a[40:43], v[134:137], v[174:177]\n"
      "v_add_f32 v76, v76, v151\n"
      "v_fmac_f32 v116, v211, v211\n"
      "v_mfma_f32_16x16x32_bf16 v[174:177], a[44:47], v[218:221], v[174:177]\n"
      "v_fmac_f32 v116, v212, v212\n"
      "v_fmac_f32 v116, v213, v213\n"
      "v_mfma_f32_16x16x32_bf16 v[112:115], a[48:51], v[186:189], 0\n"
      "v_cvt_pk_bf16_f32 v105, v106, v107\n"
      "v_mov_b32 v151, v76\n"
      "v_mfma_f32_16x16x32_bf16 v[112:115], a[52:55], v[138:141], v[112:115]\n"
      "s_nop 0\n"
      "v_permlane32_swap_b32 v76, v151\n"
      "v_mfma_f32_16x16x32_bf16 v[112:115], a[56:59], v[142:145], v[112:115]\n"
      "v_cvt_pk_bf16_f32 v130, v130, v131\n"
      "v_fmac_f32 v116, v174, v174\n"
      "v_mfma_f32_16x16x32_bf16 v[112:115], a[60:63], v[198:201], v[112:115]\n"
      "ds_write_b64 v6, v[104:105] offset:24576\n"
      "v_mfma_f32_16x16x32_bf16 v[112:115], a[64:67], v[134:137], v[112:115]\n"
      "v_add_f32_e64 v76, v76, v151\n"
      "v_cvt_pk_bf16_f32 v126, v206, v207\n"
      "v_mfma_f32_16x16x32_bf16 v[112:115], a[68:71], v[218:221], v[112:115]\n"
      "v_cvt_pk_bf16_f32 v194, v194, v195\n"
      "v_fmac_f32 v116, v175, v175\n"
      "v_mfma_f32_16x16x32_bf16 v[96:99], a[72:75], v[186:189], 0\n"
      "v_rsq_f32 v76, v76\n"
      "v_mfma_f32_16x16x32_bf16 v[96:99], a[76:79], v[138:141], v[96:99]\n"
      "v_mul_f32 v185, v102, v185\n"
      "v_fmac_f32 v116, v176, v176\n"
      "v_mfma_f32_16x16x32_bf16 v[96:99], a[80:83], v[142:145], v[96:99]\n"
      "v_cvt_pk_bf16_f32 v127, v208, v209\n"
      "v_mov_b32 v151, v76\n"
      "v_mfma_f32_16x16x32_bf16 v[96:99], a[84:87], v[198:201], v[96:99]\n"
      "ds_write_b128 v5, v[124:127] offset:30720\n"
      "v_mfma_f32_16x16x32_bf16 v[96:99], a[88:91], v[134:137], v[96:99]\n"
      "v_permlane16_swap_b32 v76, v151\n"
      "v_mfma_f32_16x16x32_bf16 v[96:99], a[92:95], v[218:221], v[96:99]\n"
      "v_cvt_pk_bf16_f32 v131, v132, v133\n"
      "v_mul_f32 v76, s50, v76\n"
      "v_mfma_f32_16x16x32_bf16 v[156:159], a[96:99], v[186:189], 0\n"
      "v_mul_f32 v169, v76, v169\n"
      "v_mul_f32 v179, v76, v179\n"
      "v_mfma_f32_16x16x32_bf16 v[156:159], a[100:103], v[138:141], v[156:159]\n"
      "v_fmac_f32 v116, v177, v177\n"
      "v_mul_f32_e64 v167, v76, v167\n"
      "v_mfma_f32_16x16x32_bf16 v[156:159], a[104:107], v[142:145], v[156:159]\n"
      "v_cvt_pk_bf16_f32 v182, v182, v183\n"
      "v_fma_f32 v117, v96, v96, s51\n"
      "v_mfma_f32_16x16x32_bf16 v[156:159], a[108:111], v[198:201], v[156:159]\n"
      "v_cvt_pk_bf16_f32 v164, v164, v164\n"
      "v_fmac_f32 v117, v97, v97\n"
      "v_mfma_f32_16x16x32_bf16 v[156:159], a[112:115], v[134:137], v[156:159]\n"
      "v_mul_f32 v205, v151, v205\n"
      "v_cvt_pk_bf16_f32 v215, v216, v217\n"
      "v_mfma_f32_16x16x32_bf16 v[156:159], a[116:119], v[218:221], v[156:159]\n"
      "v_mul_f32 v108, v151, v108\n"
      "v_fmac_f32 v116, v112, v112\n"
      "v_mfma_f32_16x16x32_bf16 v[152:155], a[120:123], v[186:189], 0\n"
      "v_fmac_f32 v117, v98, v98\n"
      "v_mul_f32 v166, v76, v166\n"
      "v_mfma_f32_16x16x32_bf16 v[152:155], a[124:127], v[138:141], v[152:155]\n"
      "v_mul_f32 v110, v151, v110\n"
      "v_cvt_pk_bf16_f32 v166, v166, v167\n"
      "v_mfma_f32_16x16x32_bf16 v[152:155], a[128:131], v[142:145], v[152:155]\n"
      "v_cvt_pk_bf16_f32 v183, v184, v185\n"
      "v_mul_f32 v190, v76, v190\n"
      "v_mfma_f32_16x16x32_bf16 v[152:155], a[132:135], v[198:201], v[152:155]\n"
      "v_mul_f32 v148, v151, v148\n"
      "v_fmac_f32 v117, v99, v99\n"
      "v_mfma_f32_16x16x32_bf16 v[152:155], a[136:139], v[134:137], v[152:155]\n"
      "v_fmac_f32 v116, v113, v113\n"
      "v_fmac_f32 v117, v156, v156\n"
      "v_mfma_f32_16x16x32_bf16 v[152:155], a[140:143], v[218:221], v[152:155]\n"
      "v_fmac_f32 v117, v157, v157\n"
      "v_fmac_f32 v117, v158, v158\n"
      "v_mfma_f32_16x16x32_bf16 v[126:129], v[186:189], a[144:147], 0\n"
      "v_mul_f32 v178, v76, v178\n"
      "v_mul_f32 v180, v76, v180\n"
      "v_mfma_f32_16x16x32_bf16 v[126:129], v[138:141], a[148:151], v[126:129]\n"
      "ds_read_b128 v[104:107], v0 offset:22528\n"
      "v_fmac_f32 v117, v159, v159\n"
      "v_mfma_f32_16x16x32_bf16 v[126:129], v[142:145], a[152:155], v[126:129]\n"
      "v_mul_f32 v147, v151, v147\n"
      "v_fmac_f32 v117, v152, v152\n"
      "v_mfma_f32_16x16x32_bf16 v[126:129], v[198:201], a[156:159], v[126:129]\n"
      "v_mul_f32 v181, v76, v181\n"
      "v_fmac_f32_e64 v116, v114, v114\n"
      "v_mfma_f32_16x16x32_bf16 v[126:129], v[134:137], a[160:163], v[126:129]\n"
      "ds_write_b64 v6, v[182:183] offset:30720\n"
      "v_mfma_f32_16x16x32_bf16 v[126:129], v[218:221], a[164:167], v[126:129]\n"
      "v_mul_f32 v149, v151, v149\n"
      "v_fmac_f32 v117, v153, v153\n"
      "v_mfma_f32_16x16x32_bf16 v[206:209], v[186:189], a[168:171], 0\n"
      "ds_read_b128 v[100:103], v1 offset:22528\n"
      "v_cvt_pk_bf16_f32 v178, v178, v179\n"
      "v_mfma_f32_16x16x32_bf16 v[206:209], v[138:141], a[172:175], v[206:209]\n"
      "v_fmac_f32 v117, v154, v154\n"
      "v_fmac_f32 v117, v155, v155\n"
      "v_mfma_f32_16x16x32_bf16 v[206:209], v[142:145], a[176:179], v[206:209]\n"
      "ds_read_b128 v[160:163], v1 offset:34816\n"
      "v_cvt_pk_bf16_f32 v179, v180, v181\n"
      "v_mfma_f32_16x16x32_bf16 v[206:209], v[198:201], a[180:183], v[206:209]\n"
      "v_fmac_f32 v116, v115, v115\n"
      "v_mul_f32 v203, v151, v203\n"
      "v_mfma_f32_16x16x32_bf16 v[206:209], v[134:137], a[184:187], v[206:209]\n"
      "v_permlane16_swap_b32 v116, v117\n"
      "v_mfma_f32_16x16x32_bf16 v[206:209], v[218:221], a[188:191], v[206:209]\n"
      "v_mul_f32 v109, v151, v109\n"
      "v_mul_f32 v193, v76, v193\n"
      "v_mfma_f32_16x16x32_bf16 v[122:125], v[186:189], a[192:195], 0\n"
      "v_add_f32 v116, v116, v117\n"
      "v_mov_b32_e64 v117, v116\n"
      "v_mfma_f32_16x16x32_bf16 v[122:125], v[138:141], a[196:199], v[122:125]\n"
      "ds_read_b128 v[182:185], v1 offset:28672\n"
      "v_mul_f32 v111, v151, v111\n"
      "v_mfma_f32_16x16x32_bf16 v[122:125], v[142:145], a[200:203], v[122:125]\n"
      "v_permlane32_swap_b32 v116, v117\n"
      "v_mfma_f32_16x16x32_bf16 v[122:125], v[198:201], a[204:207], v[122:125]\n"
      "ds_read_b128 v[142:145], v0 offset:34816\n"
      "v_mul_f32_e64 v168, v76, v168\n"
      "v_mfma_f32_16x16x32_bf16 v[122:125], v[134:137], a[208:211], v[122:125]\n"
      "v_cvt_pk_bf16_f32 v108, v108, v109\n"
      "v_cvt_pk_bf16_f32 v216, v126, v127\n"
      "v_mfma_f32_16x16x32_bf16 v[122:125], v[218:221], a[212:215], v[122:125]\n"
      "s_waitcnt lgkmcnt(2)\n"
      "v_mul_f32 v202, v151, v202\n"
      "v_add_f32_e64 v116, v116, v117\n"
      "v_mfma_f32_16x16x32_bf16 v[134:137], a[0:3], v[104:107], 0\n"
      "s_cmp_ge_u32 s95, 0x40000000\n"
      "s_cselect_b32 s94, -1, -2\n"
      "v_cvt_pk_bf16_f32 v195, v196, v197\n"
      "v_mfma_f32_16x16x32_bf16 v[134:137], a[4:7], v[100:103], v[134:137]\n"
      "v_cvt_pk_bf16_f32 v202, v202, v203\n"
      "v_cvt_pk_bf16_f32 v132, v206, v207\n"
      "v_mfma_f32_16x16x32_bf16 v[134:137], a[8:11], v[118:121], v[134:137]\n"
      "s_waitcnt lgkmcnt(1)\n"
      "v_rsq_f32 v116, v116\n"
      "v_mfma_f32_16x16x32_bf16 v[134:137], a[12:15], v[182:185], v[134:137]\n"
      "s_waitcnt lgkmcnt(0)\n"
      "v_mul_f32 v146, v151, v146\n"
      "v_mov_b32 v117, v116\n"
      "v_mfma_f32_16x16x32_bf16 v[134:137], a[16:19], v[142:145], v[134:137]\n"
      "s_nop 0\n"
      "v_permlane16_swap_b32 v116, v117\n"
      "v_mfma_f32_16x16x32_bf16 v[134:137], a[20:23], v[160:163], v[134:137]\n"
      "v_mul_f32 v116, s50, v116\n"
      "v_mul_f32_e64 v211, v116, v211\n"
      "v_mfma_f32_16x16x32_bf16 v[218:221], a[24:27], v[104:107], 0\n"
      "ds_write_b16 v12, v164\n"
      "v_mfma_f32_16x16x32_bf16 v[218:221], a[28:31], v[100:103], v[218:221]\n"
      "v_mul_f32_e64 v210, v116, v210\n"
      "v_cvt_pk_bf16_f32 v210, v210, v211\n"
      "v_mfma_f32_16x16x32_bf16 v[218:221], a[32:35], v[118:121], v[218:221]\n"
      "v_mul_f32 v115, v116, v115\n"
      "v_mul_f32 v174, v116, v174\n"
      "v_mfma_f32_16x16x32_bf16 v[218:221], a[36:39], v[182:185], v[218:221]\n"
      "v_fma_f32 v181, v134, v134, s51\n"
      "v_mul_f32 v113, v116, v113\n"
      "v_mfma_f32_16x16x32_bf16 v[218:221], a[40:43], v[142:145], v[218:221]\n"
      "v_mul_f32 v177, v116, v177\n"
      "v_fmac_f32 v181, v135, v135\n"
      "v_mfma_f32_16x16x32_bf16 v[218:221], a[44:47], v[160:163], v[218:221]\n"
      "v_mul_f32 v191, v76, v191\n"
      "v_mul_f32 v212, v116, v212\n"
      "v_mfma_f32_16x16x32_bf16 v[198:201], a[48:51], v[104:107], 0\n"
      "v_mul_f32 v112, v116, v112\n"
      "v_mul_f32 v213, v116, v213\n"
      "v_mfma_f32_16x16x32_bf16 v[198:201], a[52:55], v[100:103], v[198:201]\n"
      "v_mul_f32 v96, v117, v96\n"
      "v_fmac_f32 v181, v136, v136\n"
      "v_mfma_f32_16x16x32_bf16 v[198:201], a[56:59], v[118:121], v[198:201]\n"
      "v_cvt_pk_bf16_f32 v109, v110, v111\n"
      "v_mul_f32 v158, v117, v158\n"
      "v_mfma_f32_16x16x32_bf16 v[198:201], a[60:63], v[182:185], v[198:201]\n"
      "v_mul_f32 v175, v116, v175\n"
      "v_mul_f32 v97, v117, v97\n"
      "v_mfma_f32_16x16x32_bf16 v[198:201], a[64:67], v[142:145], v[198:201]\n"
      "v_mul_f32 v176, v116, v176\n"
      "v_mul_f32 v159, v117, v159\n"
      "v_mfma_f32_16x16x32_bf16 v[198:201], a[68:71], v[160:163], v[198:201]\n"
      "v_mul_f32 v204, v151, v204\n"
      "v_cvt_pk_bf16_f32 v211, v212, v213\n"
      "v_mfma_f32_16x16x32_bf16 v[138:141], a[72:75], v[104:107], 0\n"
      "v_cvt_pk_bf16_f32 v167, v168, v169\n"
      "v_fmac_f32 v181, v137, v137\n"
      "v_mfma_f32_16x16x32_bf16 v[138:141], a[76:79], v[100:103], v[138:141]\n"
      "v_fmac_f32 v181, v218, v218\n"
      "v_cvt_pk_bf16_f32 v197, v124, v125\n"
      "v_mfma_f32_16x16x32_bf16 v[138:141], a[80:83], v[118:121], v[138:141]\n"
      "v_mul_f32 v153, v117, v153\n"
      "v_mul_f32_e64 v155, v117, v155\n"
      "v_mfma_f32_16x16x32_bf16 v[138:141], a[84:87], v[182:185], v[138:141]\n"
      "v_cvt_pk_bf16_f32 v217, v128, v129\n"
      "v_cvt_pk_bf16_f32 v111, v148, v149\n"
      "v_mfma_f32_16x16x32_bf16 v[138:141], a[88:91], v[142:145], v[138:141]\n"
      "v_cvt_pk_bf16_f32 v112, v112, v113\n"
      "v_mul_f32_e64 v157, v117, v157\n"
      "v_mfma_f32_16x16x32_bf16 v[138:141], a[92:95], v[160:163], v[138:141]\n"
      "v_cvt_pk_bf16_f32 v168, v190, v191\n"
      "v_mul_f32 v152, v117, v152\n"
      "v_mfma_f32_16x16x32_bf16 v[126:129], a[96:99], v[104:107], 0\n"
      "ds_read_b128 v[148:151], v2\n"
      "v_mul_f32 v192, v76, v192\n"
      "v_mfma_f32_16x16x32_bf16 v[126:129], a[100:103], v[100:103], v[126:129]\n"
      "v_mul_f32_e64 v156, v117, v156\n"
      "v_cvt_pk_bf16_f32 v213, v176, v177\n"
      "v_mfma_f32_16x16x32_bf16 v[126:129], a[104:107], v[118:121], v[126:129]\n"
      "v_cvt_pk_bf16_f32 v203, v204, v205\n"
      "v_fmac_f32 v181, v219, v219\n"
      "v_mfma_f32_16x16x32_bf16 v[126:129], a[108:111], v[182:185], v[126:129]\n"
      "s_and_b32 s47, s47, -4\n"
      "v_mul_f32_e64 v98, v117, v98\n"
      "v_cvt_pk_bf16_f32 v169, v192, v193\n"
      "v_mfma_f32_16x16x32_bf16 v[126:129], a[112:115], v[142:145], v[126:129]\n"
      "v_mul_f32_e64 v99, v117, v99\n"
      "v_cvt_pk_bf16_f32 v96, v96, v97\n"
      "v_mfma_f32_16x16x32_bf16 v[126:129], a[116:119], v[160:163], v[126:129]\n"
      "s_and_b32 s4, s45, s8\n"
      "v_fma_f32 v164, v138, v138, s51\n"
      "v_fmac_f32 v164, v139, v139\n"
      "v_mfma_f32_16x16x32_bf16 v[204:207], a[120:123], v[104:107], 0\n"
      "v_cvt_pk_bf16_f32 v152, v152, v153\n"
      "v_fmac_f32 v181, v220, v220\n"
      "v_mfma_f32_16x16x32_bf16 v[204:207], a[124:127], v[100:103], v[204:207]\n"
      "v_fmac_f32 v164, v140, v140\n"
      "v_cvt_pk_bf16_f32 v133, v208, v209\n"
      "v_mfma_f32_16x16x32_bf16 v[204:207], a[128:131], v[118:121], v[204:207]\n"
      "v_mul_f32 v114, v116, v114\n"
      "v_fmac_f32 v164, v141, v141\n"
      "v_mfma_f32_16x16x32_bf16 v[204:207], a[132:135], v[182:185], v[204:207]\n"
      "v_fmac_f32 v164, v126, v126\n"
      "v_fmac_f32 v181, v221, v221\n"
      "v_mfma_f32_16x16x32_bf16 v[204:207], a[136:139], v[142:145], v[204:207]\n"
      "v_fmac_f32 v181, v198, v198\n"
      "v_cvt_pk_bf16_f32 v110, v146, v147\n"
      "v_mfma_f32_16x16x32_bf16 v[204:207], a[140:143], v[160:163], v[204:207]\n"
      "v_fmac_f32 v181, v199, v199\n"
      "v_fmac_f32 v164, v127, v127\n"
      "v_mfma_f32_16x16x32_bf16 v[186:189], v[104:107], a[144:147], 0\n"
      "s_cmp_gt_u32 s42, s44\n"
      "s_cselect_b32 s95, s94, s95\n"
      "v_fmac_f32 v164, v128, v128\n"
      "v_mfma_f32_16x16x32_bf16 v[186:189], v[100:103], a[148:151], v[186:189]\n"
      "v_fmac_f32 v164, v129, v129\n"
      "v_fmac_f32 v164, v204, v204\n"
      "v_mfma_f32_16x16x32_bf16 v[186:189], v[118:121], a[152:155], v[186:189]\n"
      "v_fmac_f32 v181, v200, v200\n"
      "v_fmac_f32 v181, v201, v201\n"
      "v_mfma_f32_16x16x32_bf16 v[186:189], v[182:185], a[156:159], v[186:189]\n"
      "v_cvt_pk_bf16_f32 v113, v114, v115\n"
      "v_cvt_pk_bf16_f32 v196, v122, v123\n"
      "v_mfma_f32_16x16x32_bf16 v[186:189], v[142:145], a[160:163], v[186:189]\n"
      "v_fmac_f32 v164, v205, v205\n"
      "v_fmac_f32 v164, v206, v206\n"
      "v_mfma_f32_16x16x32_bf16 v[186:189], v[160:163], a[164:167], v[186:189]\n"
      "v_fmac_f32 v164, v207, v207\n"
      "v_cvt_pk_bf16_f32 v97, v98, v99\n"
      "v_mfma_f32_16x16x32_bf16 v[122:125], v[104:107], a[168:171], 0\n"
      "v_permlane16_swap_b32 v181, v164\n"
      "v_mfma_f32_16x16x32_bf16 v[122:125], v[100:103], a[172:175], v[122:125]\n"
      "v_add_f32 v181, v181, v164\n"
      "v_cvt_pk_bf16_f32 v212, v174, v175\n"
      "v_mfma_f32_16x16x32_bf16 v[122:125], v[118:121], a[176:179], v[122:125]\n"
      "v_mov_b32 v164, v181\n"
      "v_cvt_pk_bf16_f32 v99, v158, v159\n"
      "v_mfma_f32_16x16x32_bf16 v[122:125], v[182:185], a[180:183], v[122:125]\n"
      "ds_read_b128 v[190:193], v2 offset:3072\n"
      "v_cvt_pk_bf16_f32 v76, v186, v187\n"
      "v_mfma_f32_16x16x32_bf16 v[122:125], v[142:145], a[184:187], v[122:125]\n"
      "v_permlane32_swap_b32 v181, v164\n"
      "v_mfma_f32_16x16x32_bf16 v[122:125], v[160:163], a[188:191], v[122:125]\n"
      "v_mul_f32 v154, v117, v154\n"
      "v_add_f32_e64 v181, v181, v164\n"
      "v_mfma_f32_16x16x32_bf16 v[174:177], v[104:107], a[192:195], 0\n"
      "ds_read_b128 v[114:117], v2 offset:6144\n"
      "v_cvt_pk_bf16_f32 v98, v156, v157\n"
      "v_mfma_f32_16x16x32_bf16 v[174:177], v[100:103], a[196:199], v[174:177]\n"
      "v_rsq_f32 v181, v181\n"
      "v_mfma_f32_16x16x32_bf16 v[174:177], v[118:121], a[200:203], v[174:177]\n"
      "v_cvt_pk_bf16_f32 v77, v188, v189\n"
      "v_mov_b32 v164, v181\n"
      "v_mfma_f32_16x16x32_bf16 v[174:177], v[182:185], a[204:207], v[174:177]\n"
      "s_waitcnt lgkmcnt(1)\n"
      "v_cvt_pk_bf16_f32 v81, v124, v125\n"
      "v_cvt_pk_bf16_f32 v80, v122, v123\n"
      "v_mfma_f32_16x16x32_bf16 v[174:177], v[142:145], a[208:211], v[174:177]\n"
      "s_add_u32 s42, s42, 1\n"
      "v_permlane16_swap_b32 v181, v164\n"
      "v_mfma_f32_16x16x32_bf16 v[174:177], v[160:163], a[212:215], v[174:177]\n"
      "ds_read_b128 v[160:163], v2 offset:15360\n"
      "v_mul_f32 v181, s50, v181\n"
      "v_mfma_f32_16x16x32_bf16 v[186:189], a[216:219], v[148:151], 0\n"
      "ds_read_b128 v[120:123], v2 offset:12288\n"
      "v_mul_f32 v199, v181, v199\n"
      "v_mfma_f32_16x16x32_bf16 v[186:189], a[220:223], v[190:193], v[186:189]\n"
      "s_waitcnt lgkmcnt(2)\n"
      "v_mul_f32 v126, v164, v126\n"
      "v_mul_f32 v219, v181, v219\n"
      "v_mfma_f32_16x16x32_bf16 v[186:189], a[224:227], v[114:117], v[186:189]\n"
      "ds_read_b128 v[104:107], v2 offset:9216\n"
      "v_mul_f32 v140, v164, v140\n"
      "v_mfma_f32_16x16x32_bf16 v[142:145], a[240:243], v[148:151], 0\n"
      "v_mul_f32 v205, v164, v205\n"
      "v_mul_f32 v138, v164, v138\n"
      "v_mfma_f32_16x16x32_bf16 v[142:145], a[244:247], v[190:193], v[142:145]\n"
      "v_mul_f32 v141, v164, v141\n"
      "v_mul_f32 v136, v181, v136\n"
      "v_mfma_f32_16x16x32_bf16 v[142:145], a[248:251], v[114:117], v[142:145]\n"
      "v_mul_f32 v135, v181, v135\n"
      "v_mul_f32_e64 v139, v164, v139\n"
      "v_mfma_f32_16x16x32_bf16 v[156:159], v[232:235], v[148:151], 0\n"
      "ds_read_b128 v[100:103], v10 offset:1200\n"
      "v_cvt_pk_bf16_f32 v138, v138, v139\n"
      "v_mfma_f32_16x16x32_bf16 v[156:159], v[236:239], v[190:193], v[156:159]\n"
      "ds_read_b64 v[150:151], v10 offset:1192\n"
      "v_mul_f32 v127, v164, v127\n"
      "v_mfma_f32_16x16x32_bf16 v[156:159], v[240:243], v[114:117], v[156:159]\n"
      "s_waitcnt lgkmcnt(2)\n"
      "v_cvt_pk_bf16_f32 v84, v174, v175\n"
      "v_mul_f32 v198, v181, v198\n"
      "v_mfma_f32_16x16x32_bf16 v[186:189], a[228:231], v[104:107], v[186:189]\n"
      "ds_read_b128 v[182:185], v2 offset:256\n"
      "v_mul_f32 v128, v164, v128\n"
      "v_mfma_f32_16x16x32_bf16 v[186:189], a[232:235], v[120:123], v[186:189]\n"
      "ds_read_u16 v86, v11 offset:1232\n"
      "v_mul_f32_e64 v129, v164, v129\n"
      "v_mfma_f32_16x16x32_bf16 v[186:189], a[236:239], v[160:163], v[186:189]\n"
      "v_cvt_pk_bf16_f32 v139, v140, v141\n"
      "v_mul_f32 v204, v164, v204\n"
      "v_mfma_f32_16x16x32_bf16 v[142:145], a[252:255], v[104:107], v[142:145]\n"
      "v_cvt_pk_bf16_f32 v140, v126, v127\n"
      "v_mul_f32 v200, v181, v200\n"
      "v_mfma_f32_16x16x32_bf16 v[142:145], v[224:227], v[120:123], v[142:145]\n"
      "ds_read_b128 v[190:193], v2 offset:6400\n"
      "v_mul_f32 v207, v164, v207\n"
      "v_mfma_f32_16x16x32_bf16 v[142:145], v[228:231], v[160:163], v[142:145]\n"
      "v_mul_f32 v206, v164, v206\n"
      "v_cvt_pk_bf16_f32 v198, v198, v199\n"
      "v_mfma_f32_16x16x32_bf16 v[156:159], v[244:247], v[104:107], v[156:159]\n"
      "ds_read_b128 v[124:127], v2 offset:3328\n"
      "v_cvt_pk_bf16_f32 v204, v204, v205\n"
      "v_mfma_f32_16x16x32_bf16 v[156:159], v[248:251], v[120:123], v[156:159]\n"
      "v_fma_f32 v222, v186, v186, s52\n"
      "v_cvt_pk_bf16_f32 v153, v154, v155\n"
      "v_mfma_f32_16x16x32_bf16 v[156:159], v[252:255], v[160:163], v[156:159]\n"
      "ds_read_u16 v82, v11 offset:1200\n"
      "v_mul_f32_e64 v221, v181, v221\n"
      "v_mfma_f32_16x16x32_bf16 v[116:119], v[108:111], v[166:169], v[40:43]\n"
      "ds_read_u16 v78, v11 offset:1168\n"
      "v_cvt_pk_bf16_f32 v141, v128, v129\n"
      "v_mfma_f32_16x16x32_bf16 v[146:149], v[96:99], v[166:169], v[44:47]\n"
      "v_mul_f32 v220, v181, v220\n"
      "v_mul_f32 v223, v187, v187\n"
      "v_mfma_f32_16x16x32_bf16 v[162:165], v[138:141], v[166:169], v[48:51]\n"
      "s_waitcnt lgkmcnt(3)\n"
      "v_mul_f32 v201, v181, v201\n"
      "v_cvt_pk_bf16_f32 v205, v206, v207\n"
      "v_mfma_f32_16x16x32_bf16 v[206:209], v[100:103], v[166:169], v[24:27]\n"
      "v_cvt_pk_bf16_f32 v85, v176, v177\n"
      "v_mul_f32 v137, v181, v137\n"
      "v_mfma_f32_16x16x16_bf16 v[116:119], v[202:203], v[178:179], v[116:119]\n"
      "v_mul_f32 v134, v181, v134\n"
      "v_cvt_pk_bf16_f32 v134, v134, v135\n"
      "v_mul_f32_e64 v218, v181, v218\n"
      "v_mfma_f32_16x16x16_bf16 v[146:149], v[152:153], v[178:179], v[146:149]\n"
      "v_cvt_pk_bf16_f32 v135, v136, v137\n"
      "v_cvt_pk_bf16_f32 v136, v218, v219\n"
      "v_fmac_f32_e64 v223, v189, v189\n"
      "v_mfma_f32_16x16x16_bf16 v[162:165], v[204:205], v[178:179], v[162:165]\n"
      "v_exp_f32 v117, v117\n"
      "s_waitcnt vmcnt(1)\n"
      "v_or3_b32 v170, v170, v171, v172\n"
      "v_mfma_f32_16x16x16_bf16 v[206:209], v[150:151], v[178:179], v[206:209]\n"
      "v_exp_f32 v119, v119\n"
      "v_cvt_pk_bf16_f32 v137, v220, v221\n"
      "v_mfma_f32_16x16x32_bf16 v[120:123], a[216:219], v[182:185], 0\n"
      "s_waitcnt lgkmcnt(0)\n"
      "v_exp_f32_e64 v116, v116\n"
      "v_mfma_f32_16x16x32_bf16 v[120:123], a[220:223], v[124:127], v[120:123]\n"
      "v_exp_f32 v165, v165\n"
      "v_mfma_f32_16x16x32_bf16 v[120:123], a[224:227], v[190:193], v[120:123]\n"
      "v_cvt_pk_bf16_f32 v116, v116, v117\n"
      "v_fmac_f32 v222, v188, v188\n"
      "v_mfma_f32_16x16x32_bf16 v[218:221], a[240:243], v[182:185], 0\n"
      "v_exp_f32 v164, v164\n"
      "v_mfma_f32_16x16x32_bf16 v[218:221], a[244:247], v[124:127], v[218:221]\n"
      "v_exp_f32 v163, v163\n"
      "v_mfma_f32_16x16x32_bf16 v[218:221], a[248:251], v[190:193], v[218:221]\n"
      "v_exp_f32 v162, v162\n"
      "v_mfma_f32_16x16x32_bf16 v[104:107], v[232:235], v[182:185], 0\n"
      "v_fmac_f32 v223, v143, v143\n"
      "v_cvt_pk_bf16_f32 v199, v200, v201\n"
      "v_mfma_f32_16x16x32_bf16 v[104:107], v[236:239], v[124:127], v[104:107]\n"
      "v_exp_f32 v146, v146\n"
      "v_mfma_f32_16x16x32_bf16 v[104:107], v[240:243], v[190:193], v[104:107]\n"
      "v_exp_f32 v118, v118\n"
      "v_mfma_f32_16x16x32_bf16 v[182:185], v[108:111], v[210:213], v[52:55]\n"
      "v_exp_f32_e64 v206, v206\n"
      "v_mfma_f32_16x16x32_bf16 v[174:177], v[96:99], v[210:213], v[56:59]\n"
      "v_exp_f32_e64 v147, v147\n"
      "v_mfma_f32_16x16x32_bf16 v[126:129], v[138:141], v[210:213], v[60:63]\n"
      "v_exp_f32_e64 v148, v148\n"
      "v_mfma_f32_16x16x32_bf16 v[178:181], v[100:103], v[210:213], v[26:29]\n"
      "v_cvt_pk_bf16_f32 v90, v206, 0\n"
      "v_cvt_pk_bf16_f32 v117, v118, v119\n"
      "v_mfma_f32_16x16x16_bf16 v[182:185], v[202:203], v[112:113], v[182:185]\n"
      "s_waitcnt vmcnt(0)\n"
      "v_or3_b32 v170, v170, v173, v88\n"
      "v_exp_f32 v149, v149\n"
      "v_mfma_f32_16x16x16_bf16 v[174:177], v[152:153], v[112:113], v[174:177]\n"
      "v_cvt_pk_bf16_f32 v119, v148, v149\n"
      "v_cvt_pk_bf16_f32 v118, v146, v147\n"
      "v_fmac_f32 v222, v142, v142\n"
      "v_mfma_f32_16x16x16_bf16 v[126:129], v[204:205], v[112:113], v[126:129]\n"
      "v_cvt_pk_bf16_f32 v88, v162, v163\n"
      "v_exp_f32 v182, v182\n"
      "v_mfma_f32_16x16x16_bf16 v[178:181], v[150:151], v[112:113], v[178:181]\n"
      "v_cvt_pk_bf16_f32 v89, v164, v165\n"
      "v_exp_f32 v174, v174\n"
      "v_mfma_f32_16x16x32_bf16 v[164:167], v[32:35], v[116:119], 0\n"
      "v_exp_f32 v185, v185\n"
      "v_mfma_f32_16x16x32_bf16 v[164:167], v[32:35], v[88:91], v[164:167]\n"
      "v_exp_f32 v177, v177\n"
      "v_mfma_f32_16x16x32_bf16 v[190:193], v[214:217], v[116:119], 0\n"
      "v_exp_f32 v175, v175\n"
      "v_mfma_f32_16x16x32_bf16 v[190:193], v[76:79], v[88:91], v[190:193]\n"
      "v_exp_f32_e64 v183, v183\n"
      "v_mfma_f32_16x16x32_bf16 v[206:209], v[130:133], v[116:119], 0\n"
      "ds_read_b128 v[160:163], v2 offset:9472\n"
      "v_cvt_pk_bf16_f32 v182, v182, v183\n"
      "v_mfma_f32_16x16x32_bf16 v[206:209], v[80:83], v[88:91], v[206:209]\n"
      "v_rcp_f32 v164, v164\n"
      "v_mfma_f32_16x16x32_bf16 v[166:169], v[194:197], v[116:119], 0\n"
      "v_exp_f32 v127, v127\n"
      "v_mfma_f32_16x16x32_bf16 v[166:169], v[84:87], v[88:91], v[166:169]\n"
      "s_add_u32 s43, s43, 1\n"
      "v_exp_f32 v184, v184\n"
      "v_mfma_f32_16x16x32_bf16 v[146:149], v[108:111], v[134:137], v[64:67]\n"
      "v_exp_f32_e64 v129, v129\n"
      "v_mfma_f32_16x16x32_bf16 v[210:213], v[96:99], v[134:137], v[68:71]\n"
      "v_cvt_pk_bf16_f32 v183, v184, v185\n"
      "v_cvt_pk_bf16_f32 v184, v174, v175\n"
      "v_mfma_f32_16x16x32_bf16 v[172:175], v[138:141], v[134:137], v[72:75]\n"
      "s_waitcnt lgkmcnt(0)\n"
      "v_exp_f32 v126, v126\n"
      "v_mfma_f32_16x16x32_bf16 v[112:115], v[100:103], v[134:137], v[28:31]\n"
      "ds_read_b128 v[134:137], v2 offset:12544\n"
      "v_cvt_pk_bf16_f32 v92, v126, v127\n"
      "v_mfma_f32_16x16x16_bf16 v[146:149], v[202:203], v[198:199], v[146:149]\n"
      "v_fmac_f32 v222, v144, v144\n"
      "v_exp_f32 v176, v176\n"
      "v_mfma_f32_16x16x16_bf16 v[210:213], v[152:153], v[198:199], v[210:213]\n"
      "v_cvt_pk_bf16_f32 v185, v176, v177\n"
      "v_exp_f32_e64 v128, v128\n"
      "v_mfma_f32_16x16x16_bf16 v[172:175], v[204:205], v[198:199], v[172:175]\n"
      "ds_read_b128 v[124:127], v2 offset:15616\n"
      "v_fmac_f32_e64 v223, v145, v145\n"
      "v_mfma_f32_16x16x16_bf16 v[112:115], v[150:151], v[198:199], v[112:115]\n"
      "v_cvt_pk_bf16_f32 v93, v128, v129\n"
      "v_exp_f32 v146, v146\n"
      "v_exp_f32 v210, v210\n"
      "v_exp_f32_e64 v147, v147\n"
      "v_pk_mul_f32 v[190:191], v[190:191], v[164:165] op_sel_hi:[1,0]\n"
      "v_pk_mul_f32 v[192:193], v[192:193], v[164:165] op_sel_hi:[1,0]\n"
      "v_pk_mul_f32 v[168:169], v[168:169], v[164:165] op_sel_hi:[1,0]\n"
      "v_pk_mul_f32 v[208:209], v[208:209], v[164:165] op_sel_hi:[1,0]\n"
      "v_pk_mul_f32 v[206:207], v[206:207], v[164:165] op_sel_hi:[1,0]\n"
      "v_cvt_pk_bf16_f32 v190, v190, v191\n"
      "v_pk_mul_f32 v[166:167], v[166:167], v[164:165] op_sel_hi:[1,0]\n"
      "v_pk_fma_f32 v[222:223], v[156:157], v[156:157], v[222:223]\n"
      "v_mfma_f32_16x16x32_bf16 v[120:123], a[228:231], v[160:163], v[120:123]\n"
      "s_waitcnt lgkmcnt(1)\n"
      "v_exp_f32 v178, v178\n"
      "v_mfma_f32_16x16x32_bf16 v[120:123], a[232:235], v[134:137], v[120:123]\n"
      "s_waitcnt lgkmcnt(0)\n"
      "v_exp_f32 v149, v149\n"
      "v_mfma_f32_16x16x32_bf16 v[120:123], a[236:239], v[124:127], v[120:123]\n"
      "v_cvt_pk_bf16_f32 v166, v166, v167\n"
      "v_cvt_pk_bf16_f32 v191, v192, v193\n"
      "v_mfma_f32_16x16x32_bf16 v[218:221], a[252:255], v[160:163], v[218:221]\n"
      "v_cvt_pk_bf16_f32 v94, v178, 0\n"
      "v_cvt_pk_bf16_f32 v193, v208, v209\n"
      "v_mfma_f32_16x16x32_bf16 v[218:221], v[224:227], v[134:137], v[218:221]\n"
      "ds_read_b128 v[114:117], v7 offset:4096\n"
      "v_cvt_pk_bf16_f32 v192, v206, v207\n"
      "v_mfma_f32_16x16x32_bf16 v[218:221], v[228:231], v[124:127], v[218:221]\n"
      "v_exp_f32 v211, v211\n"
      "v_mfma_f32_16x16x32_bf16 v[104:107], v[244:247], v[160:163], v[104:107]\n"
      "v_exp_f32 v174, v174\n"
      "v_mfma_f32_16x16x32_bf16 v[104:107], v[248:251], v[134:137], v[104:107]\n"
      "ds_read_b64 v[118:119], v10 offset:1184\n"
      "v_mul_f32_e64 v161, v121, v121\n"
      "v_mfma_f32_16x16x32_bf16 v[104:107], v[252:255], v[124:127], v[104:107]\n"
      "ds_read_b128 v[162:165], v7\n"
      "v_cvt_pk_bf16_f32 v167, v168, v169\n"
      "v_mfma_f32_16x16x32_bf16 v[176:179], v[32:35], v[182:185], 0\n"
      "v_exp_f32_e64 v175, v175\n"
      "v_mfma_f32_16x16x32_bf16 v[176:179], v[32:35], v[92:95], v[176:179]\n"
      "ds_read_b128 v[124:127], v10 offset:1168\n"
      "v_cvt_pk_bf16_f32 v146, v146, v147\n"
      "v_mfma_f32_16x16x32_bf16 v[206:209], v[214:217], v[182:185], 0\n"
      "v_exp_f32_e64 v173, v173\n"
      "v_mfma_f32_16x16x32_bf16 v[206:209], v[76:79], v[92:95], v[206:209]\n"
      "ds_write_b128 v3, v[190:193] offset:18432\n"
      "v_mfma_f32_16x16x32_bf16 v[198:201], v[130:133], v[182:185], 0\n"
      "s_nop 0\n"
      "v_rcp_f32 v176, v176\n"
      "v_mfma_f32_16x16x32_bf16 v[198:201], v[80:83], v[92:95], v[198:201]\n"
      "v_cvt_pk_bf16_f32 v89, v174, v175\n"
      "v_fma_f32 v160, v120, v120, s52\n"
      "v_mfma_f32_16x16x32_bf16 v[190:193], v[194:197], v[182:185], 0\n"
      "s_waitcnt lgkmcnt(1)\n"
      "v_exp_f32 v148, v148\n"
      "v_mfma_f32_16x16x32_bf16 v[190:193], v[84:87], v[92:95], v[190:193]\n"
      "buffer_store_dwordx4 v[162:165], v20, s[24:27], s85 offen sc1\n"
      "v_pk_mul_f32 v[206:207], v[206:207], v[176:177] op_sel_hi:[1,0]\n"
      "v_pk_mul_f32 v[208:209], v[208:209], v[176:177] op_sel_hi:[1,0]\n"
      "v_pk_mul_f32 v[200:201], v[200:201], v[176:177] op_sel_hi:[1,0]\n"
      "v_mfma_f32_16x16x32_bf16 v[178:181], v[124:127], v[108:111], v[30:33]\n"
      "ds_read_b128 v[182:185], v7 offset:8192\n"
      "v_cvt_pk_bf16_f32 v147, v148, v149\n"
      "v_mfma_f32_16x16x32_bf16 v[108:111], v[124:127], v[96:99], v[30:33]\n"
      "ds_read_b128 v[134:137], v2 offset:512\n"
      "v_cvt_pk_bf16_f32 v206, v206, v207\n"
      "v_mfma_f32_16x16x32_bf16 v[162:165], v[124:127], v[138:141], v[30:33]\n"
      "ds_write_b64 v4, v[166:167] offset:18432\n"
      "v_mfma_f32_16x16x32_bf16 v[166:169], v[124:127], v[100:103], v[30:33]\n"
      "ds_read_b128 v[96:99], v2 offset:3584\n"
      "v_cvt_pk_bf16_f32 v207, v208, v209\n"
      "v_mfma_f32_16x16x16_bf16 v[178:181], v[118:119], v[202:203], v[178:181]\n"
      "ds_read_b128 v[138:141], v2 offset:6656\n"
      "v_exp_f32_e64 v112, v112\n"
      "v_mfma_f32_16x16x16_bf16 v[108:111], v[118:119], v[152:153], v[108:111]\n"
      "v_exp_f32_e64 v213, v213\n"
      "v_cvt_pk_bf16_f32 v209, v200, v201\n"
      "v_mfma_f32_16x16x16_bf16 v[162:165], v[118:119], v[204:205], v[162:165]\n"
      "buffer_store_dwordx4 v[114:117], v21, s[24:27], s85 offen sc1\n"
      "v_pk_fma_f32 v[160:161], v[122:123], v[122:123], v[160:161]\n"
      "v_pk_fma_f32 v[222:223], v[158:159], v[158:159], v[222:223]\n"
      "v_pk_fma_f32 v[160:161], v[218:219], v[218:219], v[160:161]\n"
      "v_pk_mul_f32 v[198:199], v[198:199], v[176:177] op_sel_hi:[1,0]\n"
      "v_pk_fma_f32 v[160:161], v[220:221], v[220:221], v[160:161]\n"
      "v_pk_mul_f32 v[192:193], v[192:193], v[176:177] op_sel_hi:[1,0]\n"
      "v_mfma_f32_16x16x16_bf16 v[166:169], v[118:119], v[150:151], v[166:169]\n"
      "v_cvt_pk_bf16_f32 v90, v112, 0\n"
      "v_cvt_pk_bf16_f32 v208, v198, v199\n"
      "s_waitcnt lgkmcnt(1)\n"
      "v_mfma_f32_16x16x32_bf16 v[124:127], a[216:219], v[134:137], 0\n"
      "ds_write_b128 v3, v[206:209] offset:18688\n"
      "v_pk_fma_f32 v[160:161], v[104:105], v[104:105], v[160:161]\n"
      "v_pk_mul_f32 v[190:191], v[190:191], v[176:177] op_sel_hi:[1,0]\n"
      "v_mfma_f32_16x16x32_bf16 v[124:127], a[220:223], v[96:99], v[124:127]\n"
      "s_waitcnt lgkmcnt(1)\n"
      "v_exp_f32 v212, v212\n"
      "v_mfma_f32_16x16x32_bf16 v[124:127], a[224:227], v[138:141], v[124:127]\n"
      "v_exp_f32 v172, v172\n"
      "v_mfma_f32_16x16x32_bf16 v[206:209], a[240:243], v[134:137], 0\n"
      "buffer_store_dwordx4 v[182:185], v22, s[24:27], s85 offen sc1\n"
      "v_pk_fma_f32 v[160:161], v[106:107], v[106:107], v[160:161]\n"
      "v_mfma_f32_16x16x32_bf16 v[206:209], a[244:247], v[96:99], v[206:209]\n"
      "ds_read_b128 v[100:103], v2 offset:12800\n"
      "v_cvt_pk_bf16_f32 v149, v212, v213\n"
      "v_mfma_f32_16x16x32_bf16 v[206:209], a[248:251], v[138:141], v[206:209]\n"
      "ds_read_b128 v[116:119], v7 offset:16384\n"
      "v_cvt_pk_bf16_f32 v148, v210, v211\n"
      "v_mfma_f32_16x16x32_bf16 v[182:185], v[232:235], v[134:137], 0\n"
      "v_cvt_pk_bf16_f32 v88, v172, v173\n"
      "v_cvt_pk_bf16_f32 v190, v190, v191\n"
      "v_mfma_f32_16x16x32_bf16 v[182:185], v[236:239], v[96:99], v[182:185]\n"
      "ds_read_b128 v[200:203], v7 offset:12288\n"
      "v_cndmask_b32 v178, v178, v108, s[58:59]\n"
      "v_mfma_f32_16x16x32_bf16 v[182:185], v[240:243], v[138:141], v[182:185]\n"
      "ds_read_b128 v[150:153], v2 offset:9728\n"
      "v_add_f32_e64 v222, v222, v223\n"
      "v_mfma_f32_16x16x32_bf16 v[172:175], v[32:35], v[146:149], 0\n"
      "v_cndmask_b32 v178, v178, v162, s[60:61]\n"
      "v_cndmask_b32 v178, v178, v166, s[62:63]\n"
      "v_mfma_f32_16x16x32_bf16 v[172:175], v[32:35], v[88:91], v[172:175]\n"
      "v_exp_f32_e64 v178, v178\n"
      "v_mfma_f32_16x16x32_bf16 v[138:141], v[214:217], v[146:149], 0\n"
      "ds_read_b128 v[110:113], v2 offset:15872\n"
      "v_cvt_pk_bf16_f32 v178, v178, v178\n"
      "v_mfma_f32_16x16x32_bf16 v[138:141], v[76:79], v[88:91], v[138:141]\n"
      "ds_write_b16 v13, v178\n"
      "v_mfma_f32_16x16x32_bf16 v[162:165], v[130:133], v[146:149], 0\n"
      "ds_read_b128 v[210:213], v14\n"
      "v_cvt_pk_bf16_f32 v191, v192, v193\n"
      "v_mfma_f32_16x16x32_bf16 v[162:165], v[80:83], v[88:91], v[162:165]\n"
      "v_rcp_f32_e64 v172, v172\n"
      "v_mfma_f32_16x16x32_bf16 v[178:181], v[194:197], v[146:149], 0\n"
      "ds_write_b64 v4, v[190:191] offset:18688\n"
      "v_mfma_f32_16x16x32_bf16 v[178:181], v[84:87], v[88:91], v[178:181]\n"
      "s_waitcnt lgkmcnt(4)\n"
      "v_mul_f32 v141, v172, v141\n"
      "v_mul_f32_e64 v139, v172, v139\n"
      "v_mfma_f32_16x16x32_bf16 v[124:127], a[228:231], v[150:153], v[124:127]\n"
      "ds_read_b128 v[174:177], v14 offset:64\n"
      "v_mul_f32 v140, v172, v140\n"
      "v_mfma_f32_16x16x32_bf16 v[124:127], a[232:235], v[100:103], v[124:127]\n"
      "s_waitcnt lgkmcnt(4)\n"
      "v_add_f32 v160, v160, v161\n"
      "v_mul_f32 v138, v172, v138\n"
      "v_mfma_f32_16x16x32_bf16 v[124:127], a[236:239], v[110:113], v[124:127]\n"
      "v_mul_f32 v164, v172, v164\n"
      "v_mul_f32 v165, v172, v165\n"
      "v_mfma_f32_16x16x32_bf16 v[206:209], a[252:255], v[150:153], v[206:209]\n"
      "v_cvt_pk_bf16_f32 v138, v138, v139\n"
      "v_cvt_pk_bf16_f32 v139, v140, v141\n"
      "v_mfma_f32_16x16x32_bf16 v[206:209], v[224:227], v[100:103], v[206:209]\n"
      "ds_write_b32 v31, v170 offset:5632\n"
      "v_mfma_f32_16x16x32_bf16 v[206:209], v[228:231], v[110:113], v[206:209]\n"
      "v_fma_f32 v190, v124, v124, s52\n"
      "v_cvt_pk_bf16_f32 v141, v164, v165\n"
      "v_mfma_f32_16x16x32_bf16 v[182:185], v[244:247], v[150:153], v[182:185]\n"
      "buffer_store_dwordx4 v[200:203], v23, s[24:27], s85 offen sc1\n"
      "v_pk_mul_f32 v[162:163], v[162:163], v[172:173] op_sel_hi:[1,0]\n"
      "v_mfma_f32_16x16x32_bf16 v[182:185], v[248:251], v[100:103], v[182:185]\n"
      "s_waitcnt lgkmcnt(1)\n"
      "v_mul_f32 v191, v125, v125\n"
      "v_cvt_pk_bf16_f32 v140, v162, v163\n"
      "v_mfma_f32_16x16x32_bf16 v[182:185], v[252:255], v[110:113], v[182:185]\n"
      "ds_write_b128 v3, v[138:141] offset:18944\n"
      "v_pk_mul_f32 v[178:179], v[178:179], v[172:173] op_sel_hi:[1,0]\n"
      "v_pk_fma_f32 v[190:191], v[126:127], v[126:127], v[190:191]\n"
      "v_pk_mul_f32 v[180:181], v[180:181], v[172:173] op_sel_hi:[1,0]\n"
      "v_pk_fma_f32 v[190:191], v[206:207], v[206:207], v[190:191]\n"
      "v_pk_fma_f32 v[190:191], v[208:209], v[208:209], v[190:191]\n"
      "v_permlane16_swap_b32 v222, v160\n"
      "v_cvt_pk_bf16_f32 v178, v178, v179\n"
      "v_pk_fma_f32 v[190:191], v[182:183], v[182:183], v[190:191]\n"
      "v_add_f32 v222, v222, v160\n"
      "v_pk_fma_f32 v[190:191], v[184:185], v[184:185], v[190:191]\n"
      "v_add_f32 v190, v190, v191\n"
      "v_mov_b32 v160, v190\n"
      "v_cvt_pk_bf16_f32 v179, v180, v181\n"
      "v_mfma_f32_16x16x32_bf16 v[150:153], v[210:213], v[32:35], 0\n"
      "ds_write_b64 v4, v[178:179] offset:18944\n"
      "v_permlane16_swap_b32 v190, v160\n"
      "v_add_f32_e64 v190, v190, v160\n"
      "v_add_u32 v10, 2336, v10\n"
      "s_nop 0\n"
      "v_permlane32_swap_b32_e64 v222, v190\n"
      "v_add_f32 v222, v222, v190\n"
      "v_mfma_f32_16x16x32_bf16 v[150:153], v[174:177], v[32:35], v[150:153]\n"
      "ds_write_b32 v31, v222 offset:1024\n"
      "v_add_u32 v11, 2336, v11\n"
      "s_cmp_ge_u32 s95, 0x40000000\n"
      "s_cselect_b64 vcc, -1, s[56:57]\n"
      "buffer_store_dwordx4 v[116:119], v25, s[24:27], s85 offen sc1\n"
      "s_waitcnt lgkmcnt(0)\n"
      "s_barrier\n"
      "s_cbranch_vccnz L_disp0_%=\n"
      "L_loop0_%=:\n"
      "v_mfma_f32_16x16x32_bf16 v[164:167], v[210:213], v[214:217], 0\n"
      "ds_read_b128 v[178:181], v0\n"
      "ds_read_b128 v[134:137], v9 offset:1024\n"
      "v_mfma_f32_16x16x32_bf16 v[164:167], v[174:177], v[76:79], v[164:167]\n"
      "ds_read_b128 v[190:193], v0 offset:12288\n"
      "v_mfma_f32_16x16x32_bf16 v[170:173], v[210:213], v[130:133], 0\n"
      "s_add_u32 m0, s64, 18432\n"
      "ds_read_b128 v[100:103], v0 offset:6144\n"
      "v_mfma_f32_16x16x32_bf16 v[170:173], v[174:177], v[80:83], v[170:173]\n"
      "buffer_load_dwordx4 v16, s[16:19], s45 offen  lds\n"
      "v_mfma_f32_16x16x32_bf16 v[128:131], v[210:213], v[194:197], 0\n"
      "ds_read_b128 v[138:141], v1\n"
      "s_add_u32 m0, s64, 22528\n"
      "v_mfma_f32_16x16x32_bf16 v[128:131], v[174:177], v[84:87], v[128:131]\n"
      "ds_read_b128 v[112:115], v1 offset:12288\n"
      "s_waitcnt lgkmcnt(5)\n"
      "v_mfma_f32_16x16x32_bf16 v[166:169], a[0:3], v[178:181], 0\n"
      "ds_read_b128 v[160:163], v1 offset:6144\n"
      "v_readlane_b32 s85, v37, s42\n"
      "v_mfma_f32_16x16x32_bf16 v[108:111], a[24:27], v[178:181], 0\n"
      "s_waitcnt lgkmcnt(5)\n"
      "v_add_f32_e64 v136, v136, v137\n"
      "v_cndmask_b32 v164, v164, v170, s[58:59]\n"
      "v_mfma_f32_16x16x32_bf16 v[210:213], a[48:51], v[178:181], 0\n"
      "buffer_load_dwordx4 v17, s[16:19], s45 offen  lds\n"
      "v_mfma_f32_16x16x32_bf16 v[170:173], a[72:75], v[178:181], 0\n"
      "v_add_f32 v134, v134, v135\n"
      "v_add_f32 v134, v134, v136\n"
      "v_mfma_f32_16x16x32_bf16 v[198:201], a[96:99], v[178:181], 0\n"
      "v_rsq_f32 v134, v134\n"
      "v_mfma_f32_16x16x32_bf16 v[146:149], a[120:123], v[178:181], 0\n"
      "s_waitcnt lgkmcnt(1)\n"
      "v_mul_f32 v134, 0x415db3d7, v134\n"
      "v_mov_b32 v136, v134\n"
      "v_mfma_f32_16x16x32_bf16 v[166:169], a[4:7], v[138:141], v[166:169]\n"
      "s_add_u32 m0, s64, 26624\n"
      "v_permlane32_swap_b32 v134, v136\n"
      "v_mfma_f32_16x16x32_bf16 v[166:169], a[8:11], v[100:103], v[166:169]\n"
      "buffer_load_dwordx4 v18, s[16:19], s45 offen  lds\n"
      "v_mfma_f32_16x16x32_bf16 v[108:111], a[28:31], v[138:141], v[108:111]\n"
      "v_mul_f32 v183, v136, v183\n"
      "v_mov_b32 v135, v134\n"
      "v_mfma_f32_16x16x32_bf16 v[108:111], a[32:35], v[100:103], v[108:111]\n"
      "s_add_u32 m0, s64, 30720\n"
      "v_mul_f32 v124, v136, v124\n"
      "v_mul_f32 v125, v136, v125\n"
      "v_mfma_f32_16x16x32_bf16 v[210:213], a[52:55], v[138:141], v[210:213]\n"
      "v_permlane16_swap_b32 v134, v135\n"
      "v_mfma_f32_16x16x32_bf16 v[210:213], a[56:59], v[100:103], v[210:213]\n"
      "ds_read_b128 v[152:155], v1 offset:14336\n"
      "v_mul_f32 v159, v134, v159\n"
      "v_mfma_f32_16x16x32_bf16 v[170:173], a[76:79], v[138:141], v[170:173]\n"
      "v_mul_f32 v186, v134, v186\n"
      "v_mul_f32 v157, v134, v157\n"
      "v_mfma_f32_16x16x32_bf16 v[170:173], a[80:83], v[100:103], v[170:173]\n"
      "buffer_load_dwordx4 v19, s[16:19], s45 offen  lds\n"
      "v_mfma_f32_16x16x32_bf16 v[198:201], a[100:103], v[138:141], v[198:201]\n"
      "v_mul_f32 v145, v134, v145\n"
      "v_mul_f32 v142, v134, v142\n"
      "v_mfma_f32_16x16x32_bf16 v[198:201], a[104:107], v[100:103], v[198:201]\n"
      "v_mul_f32 v182, v136, v182\n"
      "v_mul_f32 v120, v135, v120\n"
      "v_mfma_f32_16x16x32_bf16 v[146:149], a[124:127], v[138:141], v[146:149]\n"
      "s_mov_b32 m0, s7\n"
      "v_mul_f32 v143, v134, v143\n"
      "v_mul_f32 v187, v134, v187\n"
      "v_mfma_f32_16x16x32_bf16 v[146:149], a[128:131], v[100:103], v[146:149]\n"
      "s_waitcnt lgkmcnt(0)\n"
      "v_readlane_b32 s84, v37, s43\n"
      "v_mul_f32 v218, v135, v218\n"
      "v_mfma_f32_16x16x32_bf16 v[166:169], a[12:15], v[160:163], v[166:169]\n"
      "buffer_load_dwordx4 v15, s[0:3], s4 offen  lds\n"
      "v_mfma_f32_16x16x32_bf16 v[166:169], a[16:19], v[190:193], v[166:169]\n"
      "v_mul_f32 v107, v135, v107\n"
      "v_mul_f32 v188, v134, v188\n"
      "v_mfma_f32_16x16x32_bf16 v[166:169], a[20:23], v[112:115], v[166:169]\n"
      "v_mul_f32 v123, v135, v123\n"
      "v_mul_f32 v209, v136, v209\n"
      "v_mfma_f32_16x16x32_bf16 v[108:111], a[36:39], v[160:163], v[108:111]\n"
      "v_mul_f32 v121, v135, v121\n"
      "v_mov_b32_dpp v150, v150 quad_perm:[0,0,0,0] row_mask:0xf bank_mask:0xf\n"
      "v_mfma_f32_16x16x32_bf16 v[108:111], a[40:43], v[190:193], v[108:111]\n"
      "v_mul_f32 v144, v134, v144\n"
      "v_mul_f32 v189, v134, v189\n"
      "v_mfma_f32_16x16x32_bf16 v[108:111], a[44:47], v[112:115], v[108:111]\n"
      "buffer_load_dwordx4 v[174:177], v36, s[28:31], s47 offen\n"
      "v_mfma_f32_16x16x32_bf16 v[210:213], a[60:63], v[160:163], v[210:213]\n"
      "v_cndmask_b32 v164, v164, v128, s[60:61]\n"
      "v_mul_f32 v219, v135, v219\n"
      "v_mfma_f32_16x16x32_bf16 v[210:213], a[64:67], v[190:193], v[210:213]\n"
      "v_cvt_pk_bf16_f32 v186, v186, v187\n"
      "v_cvt_pk_bf16_f32 v187, v188, v189\n"
      "v_mfma_f32_16x16x32_bf16 v[210:213], a[68:71], v[112:115], v[210:213]\n"
      "v_mul_f32 v105, v135, v105\n"
      "v_mul_f32 v156, v134, v156\n"
      "v_mfma_f32_16x16x32_bf16 v[170:173], a[84:87], v[160:163], v[170:173]\n"
      "v_rcp_f32 v150, v150\n"
      "v_mfma_f32_16x16x32_bf16 v[170:173], a[88:91], v[190:193], v[170:173]\n"
      "ds_read_b128 v[116:119], v1 offset:2048\n"
      "v_cvt_pk_bf16_f32 v120, v120, v121\n"
      "v_mfma_f32_16x16x32_bf16 v[170:173], a[92:95], v[112:115], v[170:173]\n"
      "v_mul_f32 v208, v136, v208\n"
      "v_mul_f32 v164, v150, v164\n"
      "v_mfma_f32_16x16x32_bf16 v[198:201], a[108:111], v[160:163], v[198:201]\n"
      "buffer_load_dword v90, v29, s[28:31], s47 offen\n"
      "v_mfma_f32_16x16x32_bf16 v[198:201], a[112:115], v[190:193], v[198:201]\n"
      "v_mul_f32_e64 v185, v136, v185\n"
      "v_fma_f32 v150, v166, v166, s51\n"
      "v_mfma_f32_16x16x32_bf16 v[198:201], a[116:119], v[112:115], v[198:201]\n"
      "v_fma_f32 v151, v170, v170, s51\n"
      "v_mul_f32_e64 v104, v135, v104\n"
      "v_mfma_f32_16x16x32_bf16 v[146:149], a[132:135], v[160:163], v[146:149]\n"
      "ds_read_b128 v[202:205], v0 offset:14336\n"
      "v_cvt_pk_bf16_f32 v188, v142, v143\n"
      "v_mfma_f32_16x16x32_bf16 v[146:149], a[136:139], v[190:193], v[146:149]\n"
      "v_mul_f32 v221, v135, v221\n"
      "v_mul_f32 v220, v135, v220\n"
      "v_mfma_f32_16x16x32_bf16 v[146:149], a[140:143], v[112:115], v[146:149]\n"
      "v_fmac_f32 v150, v167, v167\n"
      "v_cvt_pk_bf16_f32 v189, v144, v145\n"
      "v_mfma_f32_16x16x32_bf16 v[214:217], v[178:181], a[144:147], 0\n"
      "v_fmac_f32 v150, v168, v168\n"
      "v_mfma_f32_16x16x32_bf16 v[214:217], v[138:141], a[148:151], v[214:217]\n"
      "ds_write_b128 v5, v[186:189]\n"
      "v_mfma_f32_16x16x32_bf16 v[214:217], v[100:103], a[152:155], v[214:217]\n"
      "v_fmac_f32 v150, v169, v169\n"
      "v_fmac_f32 v150, v108, v108\n"
      "v_mfma_f32_16x16x32_bf16 v[214:217], v[160:163], a[156:159], v[214:217]\n"
      "v_mul_f32 v122, v135, v122\n"
      "v_cvt_pk_bf16_f32 v182, v182, v183\n"
      "v_mfma_f32_16x16x32_bf16 v[214:217], v[190:193], a[160:163], v[214:217]\n"
      "v_mul_f32 v126, v136, v126\n"
      "v_cvt_pk_bf16_f32 v121, v122, v123\n"
      "v_mfma_f32_16x16x32_bf16 v[214:217], v[112:115], a[164:167], v[214:217]\n"
      "ds_read_b128 v[96:99], v0 offset:2048\n"
      "v_cvt_pk_bf16_f32 v123, v220, v221\n"
      "v_mfma_f32_16x16x32_bf16 v[130:133], v[178:181], a[168:171], 0\n"
      "v_cvt_pk_bf16_f32 v164, v164, v164\n"
      "v_cvt_pk_bf16_f32 v122, v218, v219\n"
      "v_mfma_f32_16x16x32_bf16 v[130:133], v[138:141], a[172:175], v[130:133]\n"
      "ds_read_b128 v[142:145], v1 offset:8192\n"
      "v_cvt_pk_bf16_f32 v104, v104, v105\n"
      "v_mfma_f32_16x16x32_bf16 v[130:133], v[100:103], a[176:179], v[130:133]\n"
      "v_fmac_f32 v151, v171, v171\n"
      "v_fmac_f32 v150, v109, v109\n"
      "v_mfma_f32_16x16x32_bf16 v[130:133], v[160:163], a[180:183], v[130:133]\n"
      "v_mul_f32 v207, v136, v207\n"
      "v_fmac_f32 v150, v110, v110\n"
      "v_mfma_f32_16x16x32_bf16 v[130:133], v[190:193], a[184:187], v[130:133]\n"
      "v_mul_f32 v106, v135, v106\n"
      "v_fmac_f32 v151, v172, v172\n"
      "v_mfma_f32_16x16x32_bf16 v[130:133], v[112:115], a[188:191], v[130:133]\n"
      "ds_write_b16 v12, v164 offset:16\n"
      "v_mfma_f32_16x16x32_bf16 v[194:197], v[178:181], a[192:195], 0\n"
      "v_fmac_f32 v150, v111, v111\n"
      "v_fmac_f32 v151, v173, v173\n"
      "v_mfma_f32_16x16x32_bf16 v[194:197], v[138:141], a[196:199], v[194:197]\n"
      "ds_read_b128 v[178:181], v0 offset:8192\n"
      "v_mul_f32_e64 v184, v136, v184\n"
      "v_mfma_f32_16x16x32_bf16 v[194:197], v[100:103], a[200:203], v[194:197]\n"
      "v_cvt_pk_bf16_f32 v214, v214, v215\n"
      "v_cvt_pk_bf16_f32 v105, v106, v107\n"
      "v_mfma_f32_16x16x32_bf16 v[194:197], v[160:163], a[204:207], v[194:197]\n"
      "ds_write_b128 v5, v[120:123] offset:6144\n"
      "v_mfma_f32_16x16x32_bf16 v[194:197], v[190:193], a[208:211], v[194:197]\n"
      "s_waitcnt lgkmcnt(3)\n"
      "v_fmac_f32 v151, v198, v198\n"
      "v_mul_f32 v158, v134, v158\n"
      "v_mfma_f32_16x16x32_bf16 v[194:197], v[112:115], a[212:215], v[194:197]\n"
      "s_or_b32 s95, s84, s85\n"
      "v_fmac_f32 v150, v210, v210\n"
      "v_fmac_f32 v150, v211, v211\n"
      "v_mfma_f32_16x16x32_bf16 v[112:115], a[0:3], v[96:99], 0\n"
      "s_waitcnt lgkmcnt(1)\n"
      "v_fmac_f32 v151, v199, v199\n"
      "v_cvt_pk_bf16_f32 v156, v156, v157\n"
      "v_mfma_f32_16x16x32_bf16 v[112:115], a[4:7], v[116:119], v[112:115]\n"
      "v_cvt_pk_bf16_f32 v194, v194, v195\n"
      "v_fmac_f32 v151, v200, v200\n"
      "v_mfma_f32_16x16x32_bf16 v[112:115], a[8:11], v[178:181], v[112:115]\n"
      "v_fmac_f32 v150, v212, v212\n"
      "v_fmac_f32 v151, v201, v201\n"
      "v_mfma_f32_16x16x32_bf16 v[112:115], a[12:15], v[142:145], v[112:115]\n"
      "v_mul_f32 v127, v136, v127\n"
      "v_fmac_f32_e64 v151, v146, v146\n"
      "v_mfma_f32_16x16x32_bf16 v[112:115], a[16:19], v[202:205], v[112:115]\n"
      "ds_read_b128 v[186:189], v0 offset:10240\n"
      "v_fmac_f32 v151, v147, v147\n"
      "v_mfma_f32_16x16x32_bf16 v[112:115], a[20:23], v[152:155], v[112:115]\n"
      "ds_read_b128 v[100:103], v9 offset:5632\n"
      "v_fmac_f32 v151, v148, v148\n"
      "v_mfma_f32_16x16x32_bf16 v[190:193], a[24:27], v[96:99], 0\n"
      "ds_read_b128 v[120:123], v1 offset:4096\n"
      "v_cvt_pk_bf16_f32 v124, v124, v125\n"
      "v_mfma_f32_16x16x32_bf16 v[190:193], a[28:31], v[116:119], v[190:193]\n"
      "v_readlane_b32 s86, v38, s43\n"
      "v_cvt_pk_bf16_f32 v157, v158, v159\n"
      "v_mfma_f32_16x16x32_bf16 v[190:193], a[32:35], v[178:181], v[190:193]\n"
      "v_fmac_f32_e64 v150, v213, v213\n"
      "v_cvt_pk_bf16_f32 v183, v184, v185\n"
      "v_mfma_f32_16x16x32_bf16 v[190:193], a[36:39], v[142:145], v[190:193]\n"
      "v_mul_f32 v206, v136, v206\n"
      "v_fmac_f32 v151, v149, v149\n"
      "v_mfma_f32_16x16x32_bf16 v[190:193], a[40:43], v[202:205], v[190:193]\n"
      "v_fma_f32 v84, v112, v112, s51\n"
      "v_fmac_f32_e64 v84, v113, v113\n"
      "v_mfma_f32_16x16x32_bf16 v[190:193], a[44:47], v[152:155], v[190:193]\n"
      "ds_write_b64 v6, v[156:157]\n"
      "v_mfma_f32_16x16x32_bf16 v[160:163], a[48:51], v[96:99], 0\n"
      "s_waitcnt lgkmcnt(1)\n"
      "v_permlane16_swap_b32 v150, v151\n"
      "v_mfma_f32_16x16x32_bf16 v[160:163], a[52:55], v[116:119], v[160:163]\n"
      "v_or3_b32 v100, v100, v101, v102\n"
      "v_add_f32_e64 v150, v150, v151\n"
      "v_mfma_f32_16x16x32_bf16 v[160:163], a[56:59], v[178:181], v[160:163]\n"
      "ds_read_b128 v[134:137], v0 offset:4096\n"
      "v_bitop3_b32 v100, v100, v103, s33 bitop3:0xa8\n"
      "v_mfma_f32_16x16x32_bf16 v[160:163], a[60:63], v[142:145], v[160:163]\n"
      "v_fmac_f32 v84, v114, v114\n"
      "v_mov_b32 v151, v150\n"
      "v_mfma_f32_16x16x32_bf16 v[160:163], a[64:67], v[202:205], v[160:163]\n"
      "s_nop 0\n"
      "v_permlane32_swap_b32 v150, v151\n"
      "v_mfma_f32_16x16x32_bf16 v[160:163], a[68:71], v[152:155], v[160:163]\n"
      "v_add_f32_e64 v150, v150, v151\n"
      "v_cvt_pk_bf16_f32 v215, v216, v217\n"
      "v_mfma_f32_16x16x32_bf16 v[138:141], a[72:75], v[96:99], 0\n"
      "v_cvt_pk_bf16_f32 v125, v126, v127\n"
      "v_fmac_f32_e64 v84, v115, v115\n"
      "v_mfma_f32_16x16x32_bf16 v[138:141], a[76:79], v[116:119], v[138:141]\n"
      "ds_write_b64 v6, v[182:183] offset:12288\n"
      "v_mfma_f32_16x16x32_bf16 v[138:141], a[80:83], v[178:181], v[138:141]\n"
      "v_rsq_f32 v150, v150\n"
      "v_mfma_f32_16x16x32_bf16 v[138:141], a[84:87], v[142:145], v[138:141]\n"
      "v_cvt_pk_bf16_f32 v127, v208, v209\n"
      "v_mov_b32 v151, v150\n"
      "v_mfma_f32_16x16x32_bf16 v[138:141], a[88:91], v[202:205], v[138:141]\n"
      "ds_read_b128 v[220:223], v0 offset:16384\n"
      "v_fmac_f32 v84, v190, v190\n"
      "v_mfma_f32_16x16x32_bf16 v[138:141], a[92:95], v[152:155], v[138:141]\n"
      "v_permlane16_swap_b32 v150, v151\n"
      "v_mfma_f32_16x16x32_bf16 v[156:159], a[96:99], v[96:99], 0\n"
      "v_fmac_f32 v84, v191, v191\n"
      "v_cmp_ne_u32 s[56:57], 0, v100\n"
      "v_mfma_f32_16x16x32_bf16 v[156:159], a[100:103], v[116:119], v[156:159]\n"
      "v_mul_f32 v150, s50, v150\n"
      "v_mul_f32_e64 v167, v150, v167\n"
      "v_mfma_f32_16x16x32_bf16 v[156:159], a[104:107], v[178:181], v[156:159]\n"
      "v_cvt_pk_bf16_f32 v126, v206, v207\n"
      "v_cvt_pk_bf16_f32 v130, v130, v131\n"
      "v_mfma_f32_16x16x32_bf16 v[156:159], a[108:111], v[142:145], v[156:159]\n"
      "v_fma_f32 v219, v138, v138, s51\n"
      "v_fmac_f32_e64 v219, v139, v139\n"
      "v_mfma_f32_16x16x32_bf16 v[156:159], a[112:115], v[202:205], v[156:159]\n"
      "ds_write_b64 v6, v[104:105] offset:6144\n"
      "v_mfma_f32_16x16x32_bf16 v[156:159], a[116:119], v[152:155], v[156:159]\n"
      "v_fmac_f32_e64 v84, v192, v192\n"
      "v_cvt_pk_bf16_f32 v195, v196, v197\n"
      "v_mfma_f32_16x16x32_bf16 v[182:185], a[120:123], v[96:99], 0\n"
      "v_fmac_f32 v219, v140, v140\n"
      "v_mul_f32 v211, v150, v211\n"
      "v_mfma_f32_16x16x32_bf16 v[182:185], a[124:127], v[116:119], v[182:185]\n"
      "v_fmac_f32 v219, v141, v141\n"
      "v_mul_f32 v166, v150, v166\n"
      "v_mfma_f32_16x16x32_bf16 v[182:185], a[128:131], v[178:181], v[182:185]\n"
      "v_mul_f32 v172, v151, v172\n"
      "v_mul_f32 v201, v151, v201\n"
      "v_mfma_f32_16x16x32_bf16 v[182:185], a[132:135], v[142:145], v[182:185]\n"
      "v_mul_f32 v108, v150, v108\n"
      "v_fmac_f32 v219, v156, v156\n"
      "v_mfma_f32_16x16x32_bf16 v[182:185], a[136:139], v[202:205], v[182:185]\n"
      "v_fmac_f32 v219, v157, v157\n"
      "v_fmac_f32 v219, v158, v158\n"
      "v_mfma_f32_16x16x32_bf16 v[182:185], a[140:143], v[152:155], v[182:185]\n"
      "v_fmac_f32 v84, v193, v193\n"
      "v_mul_f32 v168, v150, v168\n"
      "v_mfma_f32_16x16x32_bf16 v[104:107], v[96:99], a[144:147], 0\n"
      "v_fmac_f32 v84, v160, v160\n"
      "v_mul_f32 v149, v151, v149\n"
      "v_mfma_f32_16x16x32_bf16 v[104:107], v[116:119], a[148:151], v[104:107]\n"
      "v_fmac_f32 v84, v161, v161\n"
      "v_fmac_f32 v219, v159, v159\n"
      "v_mfma_f32_16x16x32_bf16 v[104:107], v[178:181], a[152:155], v[104:107]\n"
      "s_or_b32 s95, s95, s48\n"
      "v_mul_f32 v148, v151, v148\n"
      "v_fmac_f32_e64 v84, v162, v162\n"
      "v_mfma_f32_16x16x32_bf16 v[104:107], v[142:145], a[156:159], v[104:107]\n"
      "ds_write_b128 v5, v[124:127] offset:12288\n"
      "v_mfma_f32_16x16x32_bf16 v[104:107], v[202:205], a[160:163], v[104:107]\n"
      "s_cmp_ge_u32 s95, 0x40000000\n"
      "s_cselect_b32 s94, -1, -2\n"
      "v_fmac_f32_e64 v219, v182, v182\n"
      "v_mfma_f32_16x16x32_bf16 v[104:107], v[152:155], a[164:167], v[104:107]\n"
      "v_mul_f32 v170, v151, v170\n"
      "v_fmac_f32 v219, v183, v183\n"
      "v_mfma_f32_16x16x32_bf16 v[124:127], v[96:99], a[168:171], 0\n"
      "v_fmac_f32 v219, v184, v184\n"
      "v_mul_f32 v147, v151, v147\n"
      "v_mfma_f32_16x16x32_bf16 v[124:127], v[116:119], a[172:175], v[124:127]\n"
      "v_mul_f32 v173, v151, v173\n"
      "v_fmac_f32 v84, v163, v163\n"
      "v_mfma_f32_16x16x32_bf16 v[124:127], v[178:181], a[176:179], v[124:127]\n"
      "v_cvt_pk_bf16_f32 v131, v132, v133\n"
      "v_mul_f32 v146, v151, v146\n"
      "v_mfma_f32_16x16x32_bf16 v[124:127], v[142:145], a[180:183], v[124:127]\n"
      "ds_read_b128 v[206:209], v1 offset:10240\n"
      "v_fmac_f32 v219, v185, v185\n"
      "v_mfma_f32_16x16x32_bf16 v[124:127], v[202:205], a[184:187], v[124:127]\n"
      "s_nop 0\n"
      "v_permlane16_swap_b32 v84, v219\n"
      "v_mfma_f32_16x16x32_bf16 v[124:127], v[152:155], a[188:191], v[124:127]\n"
      "v_mul_f32 v169, v150, v169\n"
      "v_add_f32 v84, v84, v219\n"
      "v_mfma_f32_16x16x32_bf16 v[100:103], v[96:99], a[192:195], 0\n"
      "v_mov_b32 v219, v84\n"
      "v_mul_f32 v212, v150, v212\n"
      "v_mfma_f32_16x16x32_bf16 v[100:103], v[116:119], a[196:199], v[100:103]\n"
      "v_mul_f32_e64 v210, v150, v210\n"
      "v_cvt_pk_bf16_f32 v216, v104, v105\n"
      "v_mfma_f32_16x16x32_bf16 v[100:103], v[178:181], a[200:203], v[100:103]\n"
      "v_permlane32_swap_b32 v84, v219\n"
      "v_mfma_f32_16x16x32_bf16 v[100:103], v[142:145], a[204:207], v[100:103]\n"
      "ds_read_b128 v[142:145], v1 offset:16384\n"
      "v_mul_f32 v109, v150, v109\n"
      "v_mfma_f32_16x16x32_bf16 v[100:103], v[202:205], a[208:211], v[100:103]\n"
      "v_mul_f32 v111, v150, v111\n"
      "v_cvt_pk_bf16_f32 v166, v166, v167\n"
      "v_mfma_f32_16x16x32_bf16 v[100:103], v[152:155], a[212:215], v[100:103]\n"
      "s_waitcnt lgkmcnt(1)\n"
      "v_add_f32 v84, v84, v219\n"
      "v_mul_f32 v110, v150, v110\n"
      "v_mfma_f32_16x16x32_bf16 v[202:205], a[0:3], v[134:137], 0\n"
      "v_rsq_f32_e64 v84, v84\n"
      "v_mfma_f32_16x16x32_bf16 v[202:205], a[4:7], v[120:123], v[202:205]\n"
      "v_cvt_pk_bf16_f32 v217, v106, v107\n"
      "v_mov_b32 v219, v84\n"
      "v_mfma_f32_16x16x32_bf16 v[202:205], a[8:11], v[186:189], v[202:205]\n"
      "s_nop 0\n"
      "v_permlane16_swap_b32_e64 v84, v219\n"
      "v_mfma_f32_16x16x32_bf16 v[202:205], a[12:15], v[206:209], v[202:205]\n"
      "ds_read_u16 v82, v11 offset:32\n"
      "v_mul_f32 v213, v150, v213\n"
      "v_mfma_f32_16x16x32_bf16 v[202:205], a[16:19], v[220:223], v[202:205]\n"
      "s_waitcnt lgkmcnt(1)\n"
      "v_mul_f32 v200, v151, v200\n"
      "v_mul_f32 v141, v219, v141\n"
      "v_mfma_f32_16x16x32_bf16 v[202:205], a[20:23], v[142:145], v[202:205]\n"
      "v_mul_f32 v158, v219, v158\n"
      "v_mul_f32 v159, v219, v159\n"
      "v_mfma_f32_16x16x32_bf16 v[152:155], a[24:27], v[134:137], 0\n"
      "v_cvt_pk_bf16_f32 v196, v100, v101\n"
      "v_cvt_pk_bf16_f32 v132, v124, v125\n"
      "v_mfma_f32_16x16x32_bf16 v[152:155], a[28:31], v[120:123], v[152:155]\n"
      "v_mul_f32 v84, s50, v84\n"
      "v_mul_f32 v171, v151, v171\n"
      "v_mfma_f32_16x16x32_bf16 v[152:155], a[32:35], v[186:189], v[152:155]\n"
      "v_fma_f32 v128, v202, v202, s51\n"
      "v_mul_f32_e64 v183, v219, v183\n"
      "v_mfma_f32_16x16x32_bf16 v[152:155], a[36:39], v[206:209], v[152:155]\n"
      "v_cvt_pk_bf16_f32 v170, v170, v171\n"
      "v_mul_f32 v157, v219, v157\n"
      "v_mfma_f32_16x16x32_bf16 v[152:155], a[40:43], v[220:223], v[152:155]\n"
      "v_cvt_pk_bf16_f32 v146, v146, v147\n"
      "v_fmac_f32 v128, v203, v203\n"
      "v_mfma_f32_16x16x32_bf16 v[152:155], a[44:47], v[142:145], v[152:155]\n"
      "v_mul_f32 v163, v84, v163\n"
      "v_mul_f32 v161, v84, v161\n"
      "v_mfma_f32_16x16x32_bf16 v[178:181], a[48:51], v[134:137], 0\n"
      "v_cvt_pk_bf16_f32 v167, v168, v169\n"
      "v_fmac_f32 v128, v204, v204\n"
      "v_mfma_f32_16x16x32_bf16 v[178:181], a[52:55], v[120:123], v[178:181]\n"
      "s_cmp_gt_u32 s42, s44\n"
      "s_cselect_b32 s95, s94, s95\n"
      "v_mul_f32 v114, v84, v114\n"
      "v_mfma_f32_16x16x32_bf16 v[178:181], a[56:59], v[186:189], v[178:181]\n"
      "v_fmac_f32 v128, v205, v205\n"
      "v_cvt_pk_bf16_f32 v168, v108, v109\n"
      "v_mfma_f32_16x16x32_bf16 v[178:181], a[60:63], v[206:209], v[178:181]\n"
      "v_fmac_f32 v128, v152, v152\n"
      "v_mul_f32_e64 v160, v84, v160\n"
      "v_mfma_f32_16x16x32_bf16 v[178:181], a[64:67], v[220:223], v[178:181]\n"
      "v_mul_f32 v198, v151, v198\n"
      "v_fmac_f32 v128, v153, v153\n"
      "v_mfma_f32_16x16x32_bf16 v[178:181], a[68:71], v[142:145], v[178:181]\n"
      "v_fmac_f32 v128, v154, v154\n"
      "v_mul_f32 v113, v84, v113\n"
      "v_mfma_f32_16x16x32_bf16 v[116:119], a[72:75], v[134:137], 0\n"
      "v_fmac_f32 v128, v155, v155\n"
      "v_mul_f32 v139, v219, v139\n"
      "v_mfma_f32_16x16x32_bf16 v[116:119], a[76:79], v[120:123], v[116:119]\n"
      "v_cvt_pk_bf16_f32 v169, v110, v111\n"
      "v_cvt_pk_bf16_f32 v197, v102, v103\n"
      "v_mfma_f32_16x16x32_bf16 v[116:119], a[80:83], v[186:189], v[116:119]\n"
      "v_cvt_pk_bf16_f32 v133, v126, v127\n"
      "v_fmac_f32_e64 v128, v178, v178\n"
      "v_mfma_f32_16x16x32_bf16 v[116:119], a[84:87], v[206:209], v[116:119]\n"
      "v_mul_f32 v140, v219, v140\n"
      "v_mul_f32 v192, v84, v192\n"
      "v_mfma_f32_16x16x32_bf16 v[116:119], a[88:91], v[220:223], v[116:119]\n"
      "v_mul_f32 v182, v219, v182\n"
      "v_mul_f32 v156, v219, v156\n"
      "v_mfma_f32_16x16x32_bf16 v[116:119], a[92:95], v[142:145], v[116:119]\n"
      "v_mul_f32_e64 v138, v219, v138\n"
      "v_cvt_pk_bf16_f32 v182, v182, v183\n"
      "v_mfma_f32_16x16x32_bf16 v[104:107], a[96:99], v[134:137], 0\n"
      "v_cvt_pk_bf16_f32 v210, v210, v211\n"
      "v_cvt_pk_bf16_f32 v160, v160, v161\n"
      "v_mfma_f32_16x16x32_bf16 v[104:107], a[100:103], v[120:123], v[104:107]\n"
      "s_cmp_ge_u32 s95, 0x40000000\n"
      "s_cselect_b64 vcc, -1, s[56:57]\n"
      "v_mul_f32 v199, v151, v199\n"
      "v_mfma_f32_16x16x32_bf16 v[104:107], a[104:107], v[186:189], v[104:107]\n"
      "s_and_b32 s5, s84, s8\n"
      "v_cvt_pk_bf16_f32 v171, v172, v173\n"
      "v_mul_f32_e64 v112, v84, v112\n"
      "v_mfma_f32_16x16x32_bf16 v[104:107], a[108:111], v[206:209], v[104:107]\n"
      "v_cvt_pk_bf16_f32 v173, v200, v201\n"
      "v_fma_f32 v164, v116, v116, s51\n"
      "v_mfma_f32_16x16x32_bf16 v[104:107], a[112:115], v[220:223], v[104:107]\n"
      "v_cvt_pk_bf16_f32 v138, v138, v139\n"
      "v_fmac_f32 v164, v117, v117\n"
      "v_mfma_f32_16x16x32_bf16 v[104:107], a[116:119], v[142:145], v[104:107]\n"
      "v_mul_f32 v115, v84, v115\n"
      "v_fmac_f32_e64 v164, v118, v118\n"
      "v_mfma_f32_16x16x32_bf16 v[124:127], a[120:123], v[134:137], 0\n"
      "v_fmac_f32_e64 v164, v119, v119\n"
      "v_cvt_pk_bf16_f32 v139, v140, v141\n"
      "v_mfma_f32_16x16x32_bf16 v[124:127], a[124:127], v[120:123], v[124:127]\n"
      "v_cvt_pk_bf16_f32 v172, v198, v199\n"
      "v_mul_f32 v185, v219, v185\n"
      "v_mfma_f32_16x16x32_bf16 v[124:127], a[128:131], v[186:189], v[124:127]\n"
      "v_fmac_f32 v128, v179, v179\n"
      "v_mul_f32_e64 v184, v219, v184\n"
      "v_mfma_f32_16x16x32_bf16 v[124:127], a[132:135], v[206:209], v[124:127]\n"
      "v_fmac_f32_e64 v164, v104, v104\n"
      "v_cvt_pk_bf16_f32 v147, v148, v149\n"
      "v_mfma_f32_16x16x32_bf16 v[124:127], a[136:139], v[220:223], v[124:127]\n"
      "ds_read_b128 v[148:151], v2 offset:18432\n"
      "v_fmac_f32 v128, v180, v180\n"
      "v_mfma_f32_16x16x32_bf16 v[124:127], a[140:143], v[142:145], v[124:127]\n"
      "v_cvt_pk_bf16_f32 v141, v158, v159\n"
      "v_fmac_f32 v164, v105, v105\n"
      "v_mfma_f32_16x16x32_bf16 v[96:99], v[134:137], a[144:147], 0\n"
      "v_cvt_pk_bf16_f32 v140, v156, v157\n"
      "v_cvt_pk_bf16_f32 v211, v212, v213\n"
      "v_mfma_f32_16x16x32_bf16 v[96:99], v[120:123], a[148:151], v[96:99]\n"
      "v_fmac_f32 v164, v106, v106\n"
      "v_fmac_f32 v164, v107, v107\n"
      "v_mfma_f32_16x16x32_bf16 v[96:99], v[186:189], a[152:155], v[96:99]\n"
      "v_fmac_f32 v128, v181, v181\n"
      "v_fmac_f32 v164, v124, v124\n"
      "v_mfma_f32_16x16x32_bf16 v[96:99], v[206:209], a[156:159], v[96:99]\n"
      "v_fmac_f32 v164, v125, v125\n"
      "v_fmac_f32 v164, v126, v126\n"
      "v_mfma_f32_16x16x32_bf16 v[96:99], v[220:223], a[160:163], v[96:99]\n"
      "v_fmac_f32 v164, v127, v127\n"
      "v_mul_f32 v190, v84, v190\n"
      "v_mfma_f32_16x16x32_bf16 v[96:99], v[142:145], a[164:167], v[96:99]\n"
      "v_permlane16_swap_b32_e64 v128, v164\n"
      "v_mfma_f32_16x16x32_bf16 v[198:201], v[134:137], a[168:171], 0\n"
      "ds_read_b128 v[100:103], v2 offset:21504\n"
      "v_cvt_pk_bf16_f32 v183, v184, v185\n"
      "v_mfma_f32_16x16x32_bf16 v[198:201], v[120:123], a[172:175], v[198:201]\n"
      "v_add_f32 v128, v128, v164\n"
      "v_mov_b32 v164, v128\n"
      "v_mfma_f32_16x16x32_bf16 v[198:201], v[186:189], a[176:179], v[198:201]\n"
      "s_nop 0\n"
      "v_permlane32_swap_b32 v128, v164\n"
      "v_mfma_f32_16x16x32_bf16 v[198:201], v[206:209], a[180:183], v[198:201]\n"
      "v_add_f32 v128, v128, v164\n"
      "v_cvt_pk_bf16_f32 v112, v112, v113\n"
      "v_mfma_f32_16x16x32_bf16 v[198:201], v[220:223], a[184:187], v[198:201]\n"
      "v_rsq_f32 v128, v128\n"
      "v_mfma_f32_16x16x32_bf16 v[198:201], v[142:145], a[188:191], v[198:201]\n"
      "v_mov_b32 v164, v128\n"
      "v_mul_f32 v191, v84, v191\n"
      "v_mfma_f32_16x16x32_bf16 v[108:111], v[134:137], a[192:195], 0\n"
      "ds_read_b128 v[134:137], v2 offset:24576\n"
      "v_mul_f32 v193, v84, v193\n"
      "v_mfma_f32_16x16x32_bf16 v[108:111], v[120:123], a[196:199], v[108:111]\n"
      "v_permlane16_swap_b32 v128, v164\n"
      "v_mfma_f32_16x16x32_bf16 v[108:111], v[186:189], a[200:203], v[108:111]\n"
      "v_cvt_pk_bf16_f32 v113, v114, v115\n"
      "v_mul_f32 v128, s50, v128\n"
      "v_mfma_f32_16x16x32_bf16 v[108:111], v[206:209], a[204:207], v[108:111]\n"
      "ds_read_b128 v[120:123], v2 offset:27648\n"
      "v_mfma_f32_16x16x32_bf16 v[108:111], v[220:223], a[208:211], v[108:111]\n"
      "s_waitcnt lgkmcnt(2)\n"
      "v_mul_f32 v203, v128, v203\n"
      "v_mul_f32 v106, v164, v106\n"
      "v_mfma_f32_16x16x32_bf16 v[108:111], v[142:145], a[212:215], v[108:111]\n"
      "ds_read_b128 v[206:209], v2 offset:30720\n"
      "v_cvt_pk_bf16_f32 v81, v200, v201\n"
      "v_mfma_f32_16x16x32_bf16 v[186:189], a[216:219], v[148:151], 0\n"
      "v_mul_f32 v116, v164, v116\n"
      "v_mul_f32_e64 v155, v128, v155\n"
      "v_mfma_f32_16x16x32_bf16 v[186:189], a[220:223], v[100:103], v[186:189]\n"
      "s_waitcnt lgkmcnt(2)\n"
      "v_cvt_pk_bf16_f32 v76, v96, v97\n"
      "v_cvt_pk_bf16_f32 v115, v192, v193\n"
      "v_mfma_f32_16x16x32_bf16 v[186:189], a[224:227], v[134:137], v[186:189]\n"
      "v_mul_f32 v154, v128, v154\n"
      "v_mul_f32 v153, v128, v153\n"
      "v_mfma_f32_16x16x32_bf16 v[142:145], a[240:243], v[148:151], 0\n"
      "ds_read_b128 v[218:221], v2 offset:33792\n"
      "v_mul_f32 v178, v128, v178\n"
      "v_mfma_f32_16x16x32_bf16 v[142:145], a[244:247], v[100:103], v[142:145]\n"
      "v_mul_f32 v162, v84, v162\n"
      "v_cvt_pk_bf16_f32 v84, v108, v109\n"
      "v_mfma_f32_16x16x32_bf16 v[142:145], a[248:251], v[134:137], v[142:145]\n"
      "v_mul_f32 v205, v128, v205\n"
      "v_mul_f32 v202, v128, v202\n"
      "v_mfma_f32_16x16x32_bf16 v[156:159], v[232:235], v[148:151], 0\n"
      "ds_read_u16 v78, v11\n"
      "v_mul_f32 v119, v164, v119\n"
      "v_mfma_f32_16x16x32_bf16 v[156:159], v[236:239], v[100:103], v[156:159]\n"
      "s_waitcnt lgkmcnt(2)\n"
      "v_mul_f32 v204, v128, v204\n"
      "v_mul_f32 v124, v164, v124\n"
      "v_mfma_f32_16x16x32_bf16 v[156:159], v[240:243], v[134:137], v[156:159]\n"
      "ds_read_b128 v[134:137], v10 offset:32\n"
      "v_mul_f32 v118, v164, v118\n"
      "v_mfma_f32_16x16x32_bf16 v[186:189], a[228:231], v[120:123], v[186:189]\n"
      "v_mul_f32 v105, v164, v105\n"
      "v_mul_f32 v104, v164, v104\n"
      "v_mfma_f32_16x16x32_bf16 v[186:189], a[232:235], v[206:209], v[186:189]\n"
      "s_waitcnt lgkmcnt(2)\n"
      "v_mul_f32_e64 v117, v164, v117\n"
      "v_cvt_pk_bf16_f32 v116, v116, v117\n"
      "v_mfma_f32_16x16x32_bf16 v[186:189], a[236:239], v[218:221], v[186:189]\n"
      "v_cvt_pk_bf16_f32 v202, v202, v203\n"
      "v_cvt_pk_bf16_f32 v203, v204, v205\n"
      "v_mfma_f32_16x16x32_bf16 v[142:145], a[252:255], v[120:123], v[142:145]\n"
      "v_cvt_pk_bf16_f32 v77, v98, v99\n"
      "v_mul_f32 v107, v164, v107\n"
      "v_mfma_f32_16x16x32_bf16 v[142:145], v[224:227], v[206:209], v[142:145]\n"
      "ds_read_b128 v[96:99], v2 offset:18688\n"
      "v_mul_f32 v125, v164, v125\n"
      "v_mfma_f32_16x16x32_bf16 v[142:145], v[228:231], v[218:221], v[142:145]\n"
      "ds_read_b64 v[184:185], v10 offset:24\n"
      "v_mul_f32 v127, v164, v127\n"
      "v_mfma_f32_16x16x32_bf16 v[156:159], v[244:247], v[120:123], v[156:159]\n"
      "v_cvt_pk_bf16_f32 v80, v198, v199\n"
      "v_mul_f32 v152, v128, v152\n"
      "v_mfma_f32_16x16x32_bf16 v[156:159], v[248:251], v[206:209], v[156:159]\n"
      "ds_read_b128 v[206:209], v2 offset:24832\n"
      "v_cvt_pk_bf16_f32 v117, v118, v119\n"
      "v_mfma_f32_16x16x32_bf16 v[156:159], v[252:255], v[218:221], v[156:159]\n"
      "v_cvt_pk_bf16_f32 v119, v106, v107\n"
      "v_cvt_pk_bf16_f32 v118, v104, v105\n"
      "v_mfma_f32_16x16x32_bf16 v[198:201], v[170:173], v[166:169], v[40:43]\n"
      "v_mul_f32 v126, v164, v126\n"
      "v_cvt_pk_bf16_f32 v85, v110, v111\n"
      "v_mfma_f32_16x16x32_bf16 v[108:111], v[138:141], v[166:169], v[44:47]\n"
      "ds_read_b128 v[100:103], v2 offset:21760\n"
      "v_cvt_pk_bf16_f32 v114, v190, v191\n"
      "v_mfma_f32_16x16x32_bf16 v[190:193], v[116:119], v[166:169], v[48:51]\n"
      "s_waitcnt lgkmcnt(2)\n"
      "v_cvt_pk_bf16_f32 v205, v154, v155\n"
      "v_cvt_pk_bf16_f32 v124, v124, v125\n"
      "v_mfma_f32_16x16x32_bf16 v[148:151], v[134:137], v[166:169], v[24:27]\n"
      "ds_read_u16 v86, v11 offset:64\n"
      "v_cvt_pk_bf16_f32 v125, v126, v127\n"
      "v_mfma_f32_16x16x16_bf16 v[198:201], v[146:147], v[210:211], v[198:201]\n"
      "v_mul_f32 v179, v128, v179\n"
      "v_mul_f32 v180, v128, v180\n"
      "v_fma_f32 v164, v186, v186, s52\n"
      "v_mfma_f32_16x16x16_bf16 v[108:111], v[182:183], v[210:211], v[108:111]\n"
      "v_mul_f32_e64 v181, v128, v181\n"
      "v_cvt_pk_bf16_f32 v161, v162, v163\n"
      "s_waitcnt vmcnt(1)\n"
      "v_or3_b32 v174, v174, v175, v176\n"
      "v_mfma_f32_16x16x16_bf16 v[190:193], v[124:125], v[210:211], v[190:193]\n"
      "v_exp_f32 v200, v200\n"
      "v_cvt_pk_bf16_f32 v178, v178, v179\n"
      "v_mfma_f32_16x16x16_bf16 v[148:151], v[184:185], v[210:211], v[148:151]\n"
      "v_add_u32 v12, 32, v12\n"
      "v_exp_f32 v109, v109\n"
      "v_mfma_f32_16x16x32_bf16 v[120:123], a[216:219], v[96:99], 0\n"
      "s_waitcnt lgkmcnt(1)\n"
      "v_exp_f32 v199, v199\n"
      "v_mfma_f32_16x16x32_bf16 v[120:123], a[220:223], v[100:103], v[120:123]\n"
      "v_exp_f32 v201, v201\n"
      "v_mfma_f32_16x16x32_bf16 v[120:123], a[224:227], v[206:209], v[120:123]\n"
      "v_exp_f32 v110, v110\n"
      "v_mfma_f32_16x16x32_bf16 v[218:221], a[240:243], v[96:99], 0\n"
      "s_add_u32 s43, s43, 1\n"
      "v_exp_f32 v191, v191\n"
      "v_mfma_f32_16x16x32_bf16 v[218:221], a[244:247], v[100:103], v[218:221]\n"
      "v_exp_f32_e64 v198, v198\n"
      "v_mfma_f32_16x16x32_bf16 v[218:221], a[248:251], v[206:209], v[218:221]\n"
      "v_exp_f32 v192, v192\n"
      "v_mfma_f32_16x16x32_bf16 v[104:107], v[232:235], v[96:99], 0\n"
      "v_exp_f32 v111, v111\n"
      "v_mfma_f32_16x16x32_bf16 v[104:107], v[236:239], v[100:103], v[104:107]\n"
      "v_cvt_pk_bf16_f32 v204, v152, v153\n"
      "v_cvt_pk_bf16_f32 v179, v180, v181\n"
      "v_mfma_f32_16x16x32_bf16 v[104:107], v[240:243], v[206:209], v[104:107]\n"
      "v_exp_f32_e64 v193, v193\n"
      "v_mfma_f32_16x16x32_bf16 v[206:209], v[170:173], v[112:115], v[52:55]\n"
      "v_cvt_pk_bf16_f32 v198, v198, v199\n"
      "v_cvt_pk_bf16_f32 v199, v200, v201\n"
      "v_mfma_f32_16x16x32_bf16 v[210:213], v[138:141], v[112:115], v[56:59]\n"
      "v_exp_f32 v148, v148\n"
      "v_mfma_f32_16x16x32_bf16 v[150:153], v[116:119], v[112:115], v[60:63]\n"
      "ds_read_b128 v[126:129], v2 offset:27904\n"
      "s_waitcnt vmcnt(0)\n"
      "v_or3_b32 v174, v174, v177, v90\n"
      "v_mfma_f32_16x16x32_bf16 v[96:99], v[134:137], v[112:115], v[26:29]\n"
      "v_exp_f32 v108, v108\n"
      "v_mfma_f32_16x16x16_bf16 v[206:209], v[146:147], v[160:161], v[206:209]\n"
      "v_exp_f32 v190, v190\n"
      "v_cvt_pk_bf16_f32 v90, v148, 0\n"
      "v_mfma_f32_16x16x16_bf16 v[210:213], v[182:183], v[160:161], v[210:213]\n"
      "v_cvt_pk_bf16_f32 v201, v110, v111\n"
      "v_cvt_pk_bf16_f32 v89, v192, v193\n"
      "v_cvt_pk_bf16_f32 v88, v190, v191\n"
      "v_mfma_f32_16x16x16_bf16 v[150:153], v[124:125], v[160:161], v[150:153]\n"
      "v_cvt_pk_bf16_f32 v200, v108, v109\n"
      "v_exp_f32 v209, v209\n"
      "v_mfma_f32_16x16x16_bf16 v[96:99], v[184:185], v[160:161], v[96:99]\n"
      "v_exp_f32 v208, v208\n"
      "v_exp_f32 v211, v211\n"
      "v_mfma_f32_16x16x32_bf16 v[166:169], v[32:35], v[198:201], 0\n"
      "v_exp_f32 v212, v212\n"
      "v_mfma_f32_16x16x32_bf16 v[166:169], v[32:35], v[88:91], v[166:169]\n"
      "v_exp_f32_e64 v207, v207\n"
      "v_mfma_f32_16x16x32_bf16 v[190:193], v[214:217], v[198:201], 0\n"
      "ds_write_b32 v31, v174 offset:4608\n"
      "v_mfma_f32_16x16x32_bf16 v[190:193], v[76:79], v[88:91], v[190:193]\n"
      "v_exp_f32_e64 v153, v153\n"
      "v_mfma_f32_16x16x32_bf16 v[174:177], v[130:133], v[198:201], 0\n"
      "v_exp_f32 v152, v152\n"
      "v_mfma_f32_16x16x32_bf16 v[174:177], v[80:83], v[88:91], v[174:177]\n"
      "v_exp_f32 v210, v210\n"
      "v_mfma_f32_16x16x32_bf16 v[112:115], v[194:197], v[198:201], 0\n"
      "s_waitcnt lgkmcnt(1)\n"
      "v_exp_f32 v206, v206\n"
      "v_mfma_f32_16x16x32_bf16 v[112:115], v[84:87], v[88:91], v[112:115]\n"
      "v_cvt_pk_bf16_f32 v93, v152, v153\n"
      "v_cvt_pk_bf16_f32 v206, v206, v207\n"
      "v_cvt_pk_bf16_f32 v207, v208, v209\n"
      "v_mfma_f32_16x16x32_bf16 v[198:201], v[170:173], v[202:205], v[64:67]\n"
      "v_exp_f32 v151, v151\n"
      "v_mfma_f32_16x16x32_bf16 v[152:155], v[138:141], v[202:205], v[68:71]\n"
      "v_exp_f32 v213, v213\n"
      "v_mfma_f32_16x16x32_bf16 v[108:111], v[116:119], v[202:205], v[72:75]\n"
      "v_exp_f32_e64 v150, v150\n"
      "v_mfma_f32_16x16x32_bf16 v[98:101], v[134:137], v[202:205], v[28:31]\n"
      "ds_read_b128 v[202:205], v2 offset:30976\n"
      "v_cvt_pk_bf16_f32 v208, v210, v211\n"
      "v_mfma_f32_16x16x16_bf16 v[198:201], v[146:147], v[178:179], v[198:201]\n"
      "v_cvt_pk_bf16_f32 v209, v212, v213\n"
      "v_exp_f32_e64 v96, v96\n"
      "v_mfma_f32_16x16x16_bf16 v[152:155], v[182:183], v[178:179], v[152:155]\n"
      "ds_read_b128 v[210:213], v2 offset:34048\n"
      "v_cvt_pk_bf16_f32 v92, v150, v151\n"
      "v_mfma_f32_16x16x16_bf16 v[108:111], v[124:125], v[178:179], v[108:111]\n"
      "v_cvt_pk_bf16_f32 v94, v96, 0\n"
      "v_rcp_f32 v166, v166\n"
      "v_mfma_f32_16x16x16_bf16 v[98:101], v[184:185], v[178:179], v[98:101]\n"
      "v_exp_f32 v199, v199\n"
      "v_exp_f32_e64 v198, v198\n"
      "v_exp_f32_e64 v200, v200\n"
      "v_pk_mul_f32 v[192:193], v[192:193], v[166:167] op_sel_hi:[1,0]\n"
      "v_pk_mul_f32 v[114:115], v[114:115], v[166:167] op_sel_hi:[1,0]\n"
      "v_pk_mul_f32 v[174:175], v[174:175], v[166:167] op_sel_hi:[1,0]\n"
      "v_pk_mul_f32 v[176:177], v[176:177], v[166:167] op_sel_hi:[1,0]\n"
      "v_pk_mul_f32 v[112:113], v[112:113], v[166:167] op_sel_hi:[1,0]\n"
      "v_pk_mul_f32 v[190:191], v[190:191], v[166:167] op_sel_hi:[1,0]\n"
      "v_mfma_f32_16x16x32_bf16 v[120:123], a[228:231], v[126:129], v[120:123]\n"
      "s_waitcnt lgkmcnt(1)\n"
      "v_cvt_pk_bf16_f32 v112, v112, v113\n"
      "v_cvt_pk_bf16_f32 v113, v114, v115\n"
      "v_mfma_f32_16x16x32_bf16 v[120:123], a[232:235], v[202:205], v[120:123]\n"
      "s_waitcnt lgkmcnt(0)\n"
      "v_exp_f32_e64 v155, v155\n"
      "v_mfma_f32_16x16x32_bf16 v[120:123], a[236:239], v[210:213], v[120:123]\n"
      "ds_read_b128 v[148:151], v7 offset:18432\n"
      "v_cvt_pk_bf16_f32 v190, v190, v191\n"
      "v_mfma_f32_16x16x32_bf16 v[218:221], a[252:255], v[126:129], v[218:221]\n"
      "v_exp_f32_e64 v201, v201\n"
      "v_mfma_f32_16x16x32_bf16 v[218:221], v[224:227], v[202:205], v[218:221]\n"
      "v_exp_f32_e64 v153, v153\n"
      "v_mfma_f32_16x16x32_bf16 v[218:221], v[228:231], v[210:213], v[218:221]\n"
      "v_exp_f32_e64 v152, v152\n"
      "v_mfma_f32_16x16x32_bf16 v[104:107], v[244:247], v[126:129], v[104:107]\n"
      "v_cvt_pk_bf16_f32 v191, v192, v193\n"
      "v_cvt_pk_bf16_f32 v193, v176, v177\n"
      "v_mfma_f32_16x16x32_bf16 v[104:107], v[248:251], v[202:205], v[104:107]\n"
      "ds_read_b128 v[126:129], v7 offset:22528\n"
      "v_cvt_pk_bf16_f32 v198, v198, v199\n"
      "v_mfma_f32_16x16x32_bf16 v[104:107], v[252:255], v[210:213], v[104:107]\n"
      "v_exp_f32_e64 v109, v109\n"
      "v_mfma_f32_16x16x32_bf16 v[210:213], v[32:35], v[206:209], 0\n"
      "ds_read_b64 v[114:115], v10 offset:16\n"
      "v_exp_f32 v154, v154\n"
      "v_mfma_f32_16x16x32_bf16 v[210:213], v[32:35], v[92:95], v[210:213]\n"
      "s_waitcnt lgkmcnt(2)\n"
      "v_cvt_pk_bf16_f32 v199, v200, v201\n"
      "v_cvt_pk_bf16_f32 v201, v154, v155\n"
      "v_mfma_f32_16x16x32_bf16 v[202:205], v[214:217], v[206:209], 0\n"
      "ds_read_b128 v[176:179], v10\n"
      "v_cvt_pk_bf16_f32 v192, v174, v175\n"
      "v_mfma_f32_16x16x32_bf16 v[202:205], v[76:79], v[92:95], v[202:205]\n"
      "buffer_store_dwordx4 v[148:151], v20, s[24:27], s46 offen sc1\n"
      "s_nop 1\n"
      "v_mfma_f32_16x16x32_bf16 v[148:151], v[130:133], v[206:209], 0\n"
      "v_exp_f32 v108, v108\n"
      "v_mfma_f32_16x16x32_bf16 v[148:151], v[80:83], v[92:95], v[148:151]\n"
      "ds_write_b128 v3, v[190:193]\n"
      "v_mfma_f32_16x16x32_bf16 v[190:193], v[194:197], v[206:209], 0\n"
      "v_exp_f32 v98, v98\n"
      "v_mfma_f32_16x16x32_bf16 v[190:193], v[84:87], v[92:95], v[190:193]\n"
      "s_waitcnt lgkmcnt(1)\n"
      "v_rcp_f32 v210, v210\n"
      "v_mfma_f32_16x16x32_bf16 v[166:169], v[176:179], v[170:173], v[30:33]\n"
      "v_exp_f32 v111, v111\n"
      "v_mfma_f32_16x16x32_bf16 v[100:103], v[176:179], v[138:141], v[30:33]\n"
      "ds_read_b128 v[206:209], v7 offset:26624\n"
      "v_cvt_pk_bf16_f32 v88, v108, v109\n"
      "v_mfma_f32_16x16x32_bf16 v[172:175], v[176:179], v[116:119], v[30:33]\n"
      "ds_read_b128 v[160:163], v2 offset:22016\n"
      "v_mul_f32_e64 v165, v187, v187\n"
      "v_mfma_f32_16x16x32_bf16 v[116:119], v[176:179], v[134:137], v[30:33]\n"
      "ds_read_b128 v[176:179], v2 offset:25088\n"
      "v_fma_f32 v170, v120, v120, s52\n"
      "v_mfma_f32_16x16x16_bf16 v[166:169], v[114:115], v[146:147], v[166:169]\n"
      "v_exp_f32 v110, v110\n"
      "v_mul_f32 v171, v121, v121\n"
      "v_mfma_f32_16x16x16_bf16 v[100:103], v[114:115], v[182:183], v[100:103]\n"
      "ds_read_b128 v[134:137], v2 offset:18944\n"
      "v_cvt_pk_bf16_f32 v90, v98, 0\n"
      "v_mfma_f32_16x16x16_bf16 v[172:175], v[114:115], v[124:125], v[172:175]\n"
      "buffer_store_dwordx4 v[126:129], v21, s[24:27], s46 offen sc1\n"
      "v_pk_mul_f32 v[192:193], v[192:193], v[210:211] op_sel_hi:[1,0]\n"
      "v_pk_mul_f32 v[190:191], v[190:191], v[210:211] op_sel_hi:[1,0]\n"
      "v_pk_fma_f32 v[170:171], v[122:123], v[122:123], v[170:171]\n"
      "v_pk_fma_f32 v[164:165], v[188:189], v[188:189], v[164:165]\n"
      "v_pk_fma_f32 v[170:171], v[218:219], v[218:219], v[170:171]\n"
      "v_pk_mul_f32 v[148:149], v[148:149], v[210:211] op_sel_hi:[1,0]\n"
      "v_pk_mul_f32 v[204:205], v[204:205], v[210:211] op_sel_hi:[1,0]\n"
      "v_pk_mul_f32 v[202:203], v[202:203], v[210:211] op_sel_hi:[1,0]\n"
      "v_pk_mul_f32 v[150:151], v[150:151], v[210:211] op_sel_hi:[1,0]\n"
      "v_cvt_pk_bf16_f32 v202, v202, v203\n"
      "v_pk_fma_f32 v[164:165], v[142:143], v[142:143], v[164:165]\n"
      "v_pk_fma_f32 v[164:165], v[144:145], v[144:145], v[164:165]\n"
      "v_mfma_f32_16x16x16_bf16 v[116:119], v[114:115], v[184:185], v[116:119]\n"
      "v_cvt_pk_bf16_f32 v203, v204, v205\n"
      "v_cvt_pk_bf16_f32 v204, v148, v149\n"
      "s_waitcnt lgkmcnt(0)\n"
      "v_mfma_f32_16x16x32_bf16 v[124:127], a[216:219], v[134:137], 0\n"
      "ds_read_b128 v[146:149], v7 offset:34816\n"
      "v_cvt_pk_bf16_f32 v205, v150, v151\n"
      "v_mfma_f32_16x16x32_bf16 v[124:127], a[220:223], v[160:163], v[124:127]\n"
      "ds_write_b128 v3, v[202:205] offset:256\n"
      "v_pk_fma_f32 v[170:171], v[220:221], v[220:221], v[170:171]\n"
      "v_mfma_f32_16x16x32_bf16 v[124:127], a[224:227], v[176:179], v[124:127]\n"
      "buffer_store_dwordx4 v[206:209], v22, s[24:27], s46 offen sc1\n"
      "s_nop 1\n"
      "v_mfma_f32_16x16x32_bf16 v[206:209], a[240:243], v[134:137], 0\n"
      "ds_read_b128 v[210:213], v7 offset:30720\n"
      "v_cndmask_b32 v166, v166, v100, s[58:59]\n"
      "v_mfma_f32_16x16x32_bf16 v[206:209], a[244:247], v[160:163], v[206:209]\n"
      "v_cndmask_b32 v166, v166, v172, s[60:61]\n"
      "v_cvt_pk_bf16_f32 v89, v110, v111\n"
      "v_mfma_f32_16x16x32_bf16 v[206:209], a[248:251], v[176:179], v[206:209]\n"
      "ds_write_b64 v4, v[112:113]\n"
      "v_mfma_f32_16x16x32_bf16 v[182:185], v[232:235], v[134:137], 0\n"
      "v_cvt_pk_bf16_f32 v190, v190, v191\n"
      "v_cvt_pk_bf16_f32 v200, v152, v153\n"
      "v_mfma_f32_16x16x32_bf16 v[182:185], v[236:239], v[160:163], v[182:185]\n"
      "ds_read_b128 v[150:153], v2 offset:34304\n"
      "v_cndmask_b32 v166, v166, v116, s[62:63]\n"
      "v_mfma_f32_16x16x32_bf16 v[182:185], v[240:243], v[176:179], v[182:185]\n"
      "ds_read_b128 v[202:205], v2 offset:28160\n"
      "v_cvt_pk_bf16_f32 v191, v192, v193\n"
      "v_mfma_f32_16x16x32_bf16 v[172:175], v[32:35], v[198:201], 0\n"
      "s_waitcnt lgkmcnt(3)\n"
      "v_exp_f32 v166, v166\n"
      "v_mfma_f32_16x16x32_bf16 v[172:175], v[32:35], v[88:91], v[172:175]\n"
      "ds_write_b64 v4, v[190:191] offset:256\n"
      "v_mfma_f32_16x16x32_bf16 v[98:101], v[214:217], v[198:201], 0\n"
      "ds_read_b128 v[112:115], v2 offset:31232\n"
      "v_cvt_pk_bf16_f32 v166, v166, v166\n"
      "v_mfma_f32_16x16x32_bf16 v[98:101], v[76:79], v[88:91], v[98:101]\n"
      "buffer_store_dwordx4 v[210:213], v23, s[24:27], s46 offen sc1\n"
      "v_pk_fma_f32 v[170:171], v[104:105], v[104:105], v[170:171]\n"
      "v_pk_fma_f32 v[170:171], v[106:107], v[106:107], v[170:171]\n"
      "v_pk_fma_f32 v[164:165], v[156:157], v[156:157], v[164:165]\n"
      "v_mfma_f32_16x16x32_bf16 v[116:119], v[130:133], v[198:201], 0\n"
      "v_rcp_f32 v172, v172\n"
      "v_mfma_f32_16x16x32_bf16 v[116:119], v[80:83], v[88:91], v[116:119]\n"
      "ds_write_b16 v13, v166\n"
      "v_mfma_f32_16x16x32_bf16 v[134:137], v[194:197], v[198:201], 0\n"
      "v_fmac_f32 v164, v158, v158\n"
      "v_add_f32 v170, v170, v171\n"
      "v_mfma_f32_16x16x32_bf16 v[134:137], v[84:87], v[88:91], v[134:137]\n"
      "s_waitcnt lgkmcnt(1)\n"
      "v_fmac_f32 v165, v159, v159\n"
      "v_mul_f32 v99, v172, v99\n"
      "v_mfma_f32_16x16x32_bf16 v[124:127], a[228:231], v[202:205], v[124:127]\n"
      "ds_read_b128 v[210:213], v14\n"
      "v_mul_f32 v98, v172, v98\n"
      "v_mfma_f32_16x16x32_bf16 v[124:127], a[232:235], v[112:115], v[124:127]\n"
      "ds_read_b128 v[174:177], v14 offset:64\n"
      "v_mul_f32 v101, v172, v101\n"
      "v_mfma_f32_16x16x32_bf16 v[124:127], a[236:239], v[150:153], v[124:127]\n"
      "v_mul_f32 v100, v172, v100\n"
      "v_cvt_pk_bf16_f32 v98, v98, v99\n"
      "v_mfma_f32_16x16x32_bf16 v[206:209], a[252:255], v[202:205], v[206:209]\n"
      "v_mul_f32 v116, v172, v116\n"
      "v_cvt_pk_bf16_f32 v99, v100, v101\n"
      "v_mfma_f32_16x16x32_bf16 v[206:209], v[224:227], v[112:115], v[206:209]\n"
      "v_mul_f32 v118, v172, v118\n"
      "v_mul_f32 v119, v172, v119\n"
      "v_mfma_f32_16x16x32_bf16 v[206:209], v[228:231], v[150:153], v[206:209]\n"
      "v_fma_f32 v198, v124, v124, s52\n"
      "v_add_f32_e64 v164, v164, v165\n"
      "v_mfma_f32_16x16x32_bf16 v[182:185], v[244:247], v[202:205], v[182:185]\n"
      "v_mul_f32_e64 v117, v172, v117\n"
      "v_mul_f32 v199, v125, v125\n"
      "v_mfma_f32_16x16x32_bf16 v[182:185], v[248:251], v[112:115], v[182:185]\n"
      "s_waitcnt lgkmcnt(0)\n"
      "v_cvt_pk_bf16_f32 v100, v116, v117\n"
      "v_cvt_pk_bf16_f32 v101, v118, v119\n"
      "v_mfma_f32_16x16x32_bf16 v[182:185], v[252:255], v[150:153], v[182:185]\n"
      "ds_write_b128 v3, v[98:101] offset:512\n"
      "v_pk_fma_f32 v[198:199], v[126:127], v[126:127], v[198:199]\n"
      "v_pk_fma_f32 v[198:199], v[206:207], v[206:207], v[198:199]\n"
      "v_pk_mul_f32 v[134:135], v[134:135], v[172:173] op_sel_hi:[1,0]\n"
      "v_permlane16_swap_b32 v164, v170\n"
      "v_add_f32 v164, v164, v170\n"
      "v_cvt_pk_bf16_f32 v134, v134, v135\n"
      "v_pk_fma_f32 v[198:199], v[208:209], v[208:209], v[198:199]\n"
      "v_pk_fma_f32 v[198:199], v[182:183], v[182:183], v[198:199]\n"
      "v_pk_mul_f32 v[136:137], v[136:137], v[172:173] op_sel_hi:[1,0]\n"
      "v_pk_fma_f32 v[198:199], v[184:185], v[184:185], v[198:199]\n"
      "v_add_f32 v198, v198, v199\n"
      "v_mov_b32 v170, v198\n"
      "v_cvt_pk_bf16_f32 v135, v136, v137\n"
      "s_and_b32 s86, s86, -4\n"
      "ds_write_b64 v4, v[134:135] offset:512\n"
      "v_permlane16_swap_b32 v198, v170\n"
      "v_add_f32 v198, v198, v170\n"
      "s_nop 1\n"
      "v_permlane32_swap_b32 v164, v198\n"
      "v_add_f32 v164, v164, v198\n"
      "v_mfma_f32_16x16x32_bf16 v[150:153], v[210:213], v[32:35], 0\n"
      "ds_write_b32 v31, v164\n"
      "buffer_store_dwordx4 v[146:149], v25, s[24:27], s46 offen sc1\n"
      "v_mfma_f32_16x16x32_bf16 v[150:153], v[174:177], v[32:35], v[150:153]\n"
      "s_add_u32 s42, s42, 1\n"
      "s_waitcnt lgkmcnt(0)\n"
      "s_barrier\n"
      "s_cbranch_vccz L_loop1_%=\n"
      "s_branch L_disp1_%=\n"
      "s_nop 7\n"
      "s_nop 7\n"
      "L_max1_%=:\n"
      "v_mfma_f32_16x16x32_bf16 v[164:167], v[210:213], v[214:217], 0\n"
      "ds_read_b128 v[100:103], v0 offset:18432\n"
      "s_add_u32 m0, s64, 0\n"
      "v_mfma_f32_16x16x32_bf16 v[164:167], v[174:177], v[76:79], v[164:167]\n"
      "ds_read_b128 v[134:137], v9\n"
      "v_mfma_f32_16x16x32_bf16 v[170:173], v[210:213], v[130:133], 0\n"
      "ds_read_b128 v[198:201], v1 offset:30720\n"
      "ds_read_b128 v[112:115], v1 offset:18432\n"
      "v_mfma_f32_16x16x32_bf16 v[170:173], v[174:177], v[80:83], v[170:173]\n"
      "buffer_load_dwordx4 v16, s[16:19], s84 offen  lds\n"
      "v_mfma_f32_16x16x32_bf16 v[128:131], v[210:213], v[194:197], 0\n"
      "ds_read_b128 v[210:213], v1 offset:24576\n"
      "v_readlane_b32 s45, v37, s43\n"
      "v_mfma_f32_16x16x32_bf16 v[128:131], v[174:177], v[84:87], v[128:131]\n"
      "ds_read_b128 v[96:99], v0 offset:24576\n"
      "s_waitcnt lgkmcnt(5)\n"
      "v_mfma_f32_16x16x32_bf16 v[190:193], a[0:3], v[100:103], 0\n"
      "ds_read_b128 v[166:169], v0 offset:26624\n"
      "v_cndmask_b32 v164, v164, v170, s[58:59]\n"
      "v_mfma_f32_16x16x32_bf16 v[174:177], a[24:27], v[100:103], 0\n"
      "s_waitcnt lgkmcnt(5)\n"
      "v_add_f32 v136, v136, v137\n"
      "v_add_f32 v134, v134, v135\n"
      "v_mfma_f32_16x16x32_bf16 v[170:173], a[48:51], v[100:103], 0\n"
      "ds_read_b128 v[160:163], v0 offset:30720\n"
      "v_add_f32_e64 v134, v134, v136\n"
      "v_mfma_f32_16x16x32_bf16 v[178:181], a[72:75], v[100:103], 0\n"
      "s_add_u32 m0, s64, 4096\n"
      "v_rsq_f32 v134, v134\n"
      "v_mfma_f32_16x16x32_bf16 v[138:141], a[96:99], v[100:103], 0\n"
      "s_waitcnt lgkmcnt(3)\n"
      "v_mul_f32 v134, 0x415db3d7, v134\n"
      "v_mov_b32_e64 v136, v134\n"
      "v_mfma_f32_16x16x32_bf16 v[116:119], a[120:123], v[100:103], 0\n"
      "buffer_load_dwordx4 v17, s[16:19], s84 offen  lds\n"
      "v_mfma_f32_16x16x32_bf16 v[190:193], a[4:7], v[112:115], v[190:193]\n"
      "s_waitcnt lgkmcnt(1)\n"
      "v_permlane32_swap_b32 v134, v136\n"
      "v_mfma_f32_16x16x32_bf16 v[190:193], a[8:11], v[96:99], v[190:193]\n"
      "ds_read_b128 v[108:111], v9 offset:4608\n"
      "v_mov_b32 v135, v134\n"
      "v_mfma_f32_16x16x32_bf16 v[174:177], a[28:31], v[112:115], v[174:177]\n"
      "s_nop 0\n"
      "v_permlane16_swap_b32 v134, v135\n"
      "v_mfma_f32_16x16x32_bf16 v[174:177], a[32:35], v[96:99], v[174:177]\n"
      "v_mul_f32 v142, v134, v142\n"
      "v_mul_f32 v145, v134, v145\n"
      "v_mfma_f32_16x16x32_bf16 v[170:173], a[52:55], v[112:115], v[170:173]\n"
      "v_mul_f32 v187, v134, v187\n"
      "v_mul_f32_e64 v143, v134, v143\n"
      "v_mfma_f32_16x16x32_bf16 v[170:173], a[56:59], v[96:99], v[170:173]\n"
      "s_add_u32 m0, s64, 8192\n"
      "v_mul_f32 v186, v134, v186\n"
      "v_mul_f32 v157, v134, v157\n"
      "v_mfma_f32_16x16x32_bf16 v[178:181], a[76:79], v[112:115], v[178:181]\n"
      "v_mul_f32 v219, v135, v219\n"
      "v_mul_f32 v106, v135, v106\n"
      "v_mfma_f32_16x16x32_bf16 v[178:181], a[80:83], v[96:99], v[178:181]\n"
      "buffer_load_dwordx4 v18, s[16:19], s84 offen  lds\n"
      "v_mfma_f32_16x16x32_bf16 v[138:141], a[100:103], v[112:115], v[138:141]\n"
      "v_mul_f32_e64 v218, v135, v218\n"
      "v_cvt_pk_bf16_f32 v186, v186, v187\n"
      "v_mfma_f32_16x16x32_bf16 v[138:141], a[104:107], v[96:99], v[138:141]\n"
      "s_add_u32 m0, s64, 12288\n"
      "v_mul_f32 v189, v134, v189\n"
      "v_mul_f32 v182, v136, v182\n"
      "v_mfma_f32_16x16x32_bf16 v[116:119], a[124:127], v[112:115], v[116:119]\n"
      "s_waitcnt lgkmcnt(0)\n"
      "v_mul_f32 v124, v136, v124\n"
      "v_or3_b32 v108, v108, v109, v110\n"
      "v_mfma_f32_16x16x32_bf16 v[116:119], a[128:131], v[96:99], v[116:119]\n"
      "v_mul_f32 v125, v136, v125\n"
      "v_mul_f32 v144, v134, v144\n"
      "v_mul_f32_e64 v107, v135, v107\n"
      "v_mfma_f32_16x16x32_bf16 v[190:193], a[12:15], v[210:213], v[190:193]\n"
      "buffer_load_dwordx4 v19, s[16:19], s84 offen  lds\n"
      "v_mfma_f32_16x16x32_bf16 v[190:193], a[16:19], v[160:163], v[190:193]\n"
      "v_mul_f32 v184, v136, v184\n"
      "v_mul_f32 v105, v135, v105\n"
      "v_mfma_f32_16x16x32_bf16 v[190:193], a[20:23], v[198:201], v[190:193]\n"
      "s_mov_b32 m0, s6\n"
      "v_mul_f32 v208, v136, v208\n"
      "v_mul_f32 v123, v135, v123\n"
      "v_mfma_f32_16x16x32_bf16 v[174:177], a[36:39], v[210:213], v[174:177]\n"
      "ds_read_b128 v[202:205], v0 offset:32768\n"
      "v_mul_f32 v221, v135, v221\n"
      "v_mfma_f32_16x16x32_bf16 v[174:177], a[40:43], v[160:163], v[174:177]\n"
      "v_mul_f32 v188, v134, v188\n"
      "v_mul_f32 v121, v135, v121\n"
      "v_mfma_f32_16x16x32_bf16 v[174:177], a[44:47], v[198:201], v[174:177]\n"
      "v_mul_f32_e64 v207, v136, v207\n"
      "v_readlane_b32 s46, v37, s42\n"
      "v_mfma_f32_16x16x32_bf16 v[170:173], a[60:63], v[210:213], v[170:173]\n"
      "v_mul_f32 v120, v135, v120\n"
      "v_cvt_pk_bf16_f32 v187, v188, v189\n"
      "v_mfma_f32_16x16x32_bf16 v[170:173], a[64:67], v[160:163], v[170:173]\n"
      "s_or_b32 s95, s45, s46\n"
      "v_mul_f32 v156, v134, v156\n"
      "v_mul_f32 v209, v136, v209\n"
      "v_mfma_f32_16x16x32_bf16 v[170:173], a[68:71], v[198:201], v[170:173]\n"
      "v_mul_f32_e64 v183, v136, v183\n"
      "v_fma_f32 v76, v190, v190, s51\n"
      "v_mfma_f32_16x16x32_bf16 v[178:181], a[84:87], v[210:213], v[178:181]\n"
      "v_cvt_pk_bf16_f32 v188, v142, v143\n"
      "v_mul_f32_e64 v104, v135, v104\n"
      "v_mfma_f32_16x16x32_bf16 v[178:181], a[88:91], v[160:163], v[178:181]\n"
      "buffer_load_dwordx4 v15, s[0:3], s5 offen  lds\n"
      "v_mfma_f32_16x16x32_bf16 v[178:181], a[92:95], v[198:201], v[178:181]\n"
      "v_mul_f32_e64 v122, v135, v122\n"
      "v_mov_b32_dpp v150, v150 quad_perm:[0,0,0,0] row_mask:0xf bank_mask:0xf\n"
      "v_mfma_f32_16x16x32_bf16 v[138:141], a[108:111], v[210:213], v[138:141]\n"
      "v_cvt_pk_bf16_f32 v104, v104, v105\n"
      "v_cvt_pk_bf16_f32 v189, v144, v145\n"
      "v_mfma_f32_16x16x32_bf16 v[138:141], a[112:115], v[160:163], v[138:141]\n"
      "ds_write_b128 v5, v[186:189] offset:18432\n"
      "v_mfma_f32_16x16x32_bf16 v[138:141], a[116:119], v[198:201], v[138:141]\n"
      "ds_read_b128 v[142:145], v1 offset:20480\n"
      "v_fmac_f32_e64 v76, v191, v191\n"
      "v_mfma_f32_16x16x32_bf16 v[116:119], a[132:135], v[210:213], v[116:119]\n"
      "v_fmac_f32 v76, v192, v192\n"
      "v_fma_f32 v165, v178, v178, s51\n"
      "v_mfma_f32_16x16x32_bf16 v[116:119], a[136:139], v[160:163], v[116:119]\n"
      "v_fmac_f32 v76, v193, v193\n"
      "v_cvt_pk_bf16_f32 v156, v156, v157\n"
      "v_mfma_f32_16x16x32_bf16 v[116:119], a[140:143], v[198:201], v[116:119]\n"
      "buffer_load_dwordx4 v[152:155], v36, s[28:31], s86 offen\n"
      "v_mfma_f32_16x16x32_bf16 v[214:217], v[100:103], a[144:147], 0\n"
      "v_cvt_pk_bf16_f32 v120, v120, v121\n"
      "v_mul_f32 v159, v134, v159\n"
      "v_mfma_f32_16x16x32_bf16 v[214:217], v[112:115], a[148:151], v[214:217]\n"
      "v_cvt_pk_bf16_f32 v121, v122, v123\n"
      "v_fmac_f32 v76, v174, v174\n"
      "v_mfma_f32_16x16x32_bf16 v[214:217], v[96:99], a[152:155], v[214:217]\n"
      "ds_read_b128 v[146:149], v0 offset:22528\n"
      "v_mul_f32 v206, v136, v206\n"
      "v_mfma_f32_16x16x32_bf16 v[214:217], v[210:213], a[156:159], v[214:217]\n"
      "v_mul_f32 v220, v135, v220\n"
      "v_mul_f32 v158, v134, v158\n"
      "v_mfma_f32_16x16x32_bf16 v[214:217], v[160:163], a[160:163], v[214:217]\n"
      "v_mul_f32 v185, v136, v185\n"
      "v_fmac_f32 v76, v175, v175\n"
      "v_mfma_f32_16x16x32_bf16 v[214:217], v[198:201], a[164:167], v[214:217]\n"
      "v_fmac_f32 v165, v179, v179\n"
      "v_fmac_f32 v76, v176, v176\n"
      "v_mfma_f32_16x16x32_bf16 v[130:133], v[100:103], a[168:171], 0\n"
      "v_fmac_f32 v165, v180, v180\n"
      "v_cvt_pk_bf16_f32 v182, v182, v183\n"
      "v_mfma_f32_16x16x32_bf16 v[130:133], v[112:115], a[172:175], v[130:133]\n"
      "v_cvt_pk_bf16_f32 v157, v158, v159\n"
      "v_cvt_pk_bf16_f32 v123, v220, v221\n"
      "v_mfma_f32_16x16x32_bf16 v[130:133], v[96:99], a[176:179], v[130:133]\n"
      "v_cvt_pk_bf16_f32 v105, v106, v107\n"
      "v_fmac_f32 v165, v181, v181\n"
      "v_mfma_f32_16x16x32_bf16 v[130:133], v[210:213], a[180:183], v[130:133]\n"
      "ds_read_b128 v[186:189], v1 offset:26624\n"
      "v_fmac_f32 v76, v177, v177\n"
      "v_mfma_f32_16x16x32_bf16 v[130:133], v[160:163], a[184:187], v[130:133]\n"
      "ds_read_b128 v[220:223], v0 offset:20480\n"
      "v_fmac_f32 v165, v138, v138\n"
      "v_mfma_f32_16x16x32_bf16 v[130:133], v[198:201], a[188:191], v[130:133]\n"
      "v_fmac_f32 v76, v170, v170\n"
      "v_cvt_pk_bf16_f32 v214, v214, v215\n"
      "v_mfma_f32_16x16x32_bf16 v[194:197], v[100:103], a[192:195], 0\n"
      "v_cvt_pk_bf16_f32 v183, v184, v185\n"
      "v_fmac_f32_e64 v165, v139, v139\n"
      "v_mfma_f32_16x16x32_bf16 v[194:197], v[112:115], a[196:199], v[194:197]\n"
      "ds_read_b128 v[100:103], v1 offset:32768\n"
      "v_cndmask_b32 v164, v164, v128, s[60:61]\n"
      "v_mfma_f32_16x16x32_bf16 v[194:197], v[96:99], a[200:203], v[194:197]\n"
      "v_rcp_f32_e64 v150, v150\n"
      "v_mfma_f32_16x16x32_bf16 v[194:197], v[210:213], a[204:207], v[194:197]\n"
      "ds_write_b64 v6, v[156:157] offset:18432\n"
      "v_mfma_f32_16x16x32_bf16 v[194:197], v[160:163], a[208:211], v[194:197]\n"
      "s_waitcnt lgkmcnt(2)\n"
      "v_fmac_f32 v165, v140, v140\n"
      "v_cvt_pk_bf16_f32 v124, v124, v125\n"
      "v_mfma_f32_16x16x32_bf16 v[194:197], v[198:201], a[212:215], v[194:197]\n"
      "buffer_load_dword v93, v29, s[28:31], s86 offen\n"
      "v_mfma_f32_16x16x32_bf16 v[198:201], a[0:3], v[220:223], 0\n"
      "v_fmac_f32 v165, v141, v141\n"
      "v_bitop3_b32 v108, v108, v111, s33 bitop3:0xa8\n"
      "v_mfma_f32_16x16x32_bf16 v[198:201], a[4:7], v[142:145], v[198:201]\n"
      "v_mul_f32 v164, v150, v164\n"
      "v_fmac_f32_e64 v165, v116, v116\n"
      "v_mfma_f32_16x16x32_bf16 v[198:201], a[8:11], v[166:169], v[198:201]\n"
      "v_fmac_f32_e64 v165, v117, v117\n"
      "v_cvt_pk_bf16_f32 v122, v218, v219\n"
      "v_mfma_f32_16x16x32_bf16 v[198:201], a[12:15], v[186:189], v[198:201]\n"
      "v_cvt_pk_bf16_f32 v194, v194, v195\n"
      "v_readlane_b32 s47, v38, s43\n"
      "v_mfma_f32_16x16x32_bf16 v[198:201], a[16:19], v[202:205], v[198:201]\n"
      "s_waitcnt lgkmcnt(0)\n"
      "v_fmac_f32 v76, v171, v171\n"
      "v_cvt_pk_bf16_f32 v195, v196, v197\n"
      "v_mfma_f32_16x16x32_bf16 v[198:201], a[20:23], v[100:103], v[198:201]\n"
      "v_fmac_f32_e64 v76, v172, v172\n"
      "v_cvt_pk_bf16_f32 v130, v130, v131\n"
      "v_mfma_f32_16x16x32_bf16 v[210:213], a[24:27], v[220:223], 0\n"
      "ds_read_b128 v[156:159], v1 offset:22528\n"
      "v_mul_f32_e64 v126, v136, v126\n"
      "v_mfma_f32_16x16x32_bf16 v[210:213], a[28:31], v[142:145], v[210:213]\n"
      "ds_read_b128 v[160:163], v1 offset:34816\n"
      "v_mul_f32_e64 v127, v136, v127\n"
      "v_mfma_f32_16x16x32_bf16 v[210:213], a[32:35], v[166:169], v[210:213]\n"
      "ds_write_b128 v5, v[120:123] offset:24576\n"
      "v_mfma_f32_16x16x32_bf16 v[210:213], a[36:39], v[186:189], v[210:213]\n"
      "ds_read_b128 v[96:99], v0 offset:28672\n"
      "v_fma_f32 v150, v198, v198, s51\n"
      "v_mfma_f32_16x16x32_bf16 v[210:213], a[40:43], v[202:205], v[210:213]\n"
      "v_fmac_f32 v165, v118, v118\n"
      "v_fmac_f32 v150, v199, v199\n"
      "v_mfma_f32_16x16x32_bf16 v[210:213], a[44:47], v[100:103], v[210:213]\n"
      "v_fmac_f32 v165, v119, v119\n"
      "v_fmac_f32 v76, v173, v173\n"
      "v_mfma_f32_16x16x32_bf16 v[134:137], a[48:51], v[220:223], 0\n"
      "v_fmac_f32 v150, v200, v200\n"
      "v_cvt_pk_bf16_f32 v164, v164, v164\n"
      "v_mfma_f32_16x16x32_bf16 v[134:137], a[52:55], v[142:145], v[134:137]\n"
      "v_permlane16_swap_b32 v76, v165\n"
      "v_mfma_f32_16x16x32_bf16 v[134:137], a[56:59], v[166:169], v[134:137]\n"
      "ds_write_b16 v12, v164\n"
      "v_mfma_f32_16x16x32_bf16 v[134:137], a[60:63], v[186:189], v[134:137]\n"
      "v_add_f32_e64 v76, v76, v165\n"
      "v_cvt_pk_bf16_f32 v215, v216, v217\n"
      "v_mfma_f32_16x16x32_bf16 v[134:137], a[64:67], v[202:205], v[134:137]\n"
      "ds_read_u16 v86, v11 offset:1232\n"
      "v_cvt_pk_bf16_f32 v125, v126, v127\n"
      "v_mfma_f32_16x16x32_bf16 v[134:137], a[68:71], v[100:103], v[134:137]\n"
      "ds_write_b64 v6, v[104:105] offset:24576\n"
      "v_mfma_f32_16x16x32_bf16 v[112:115], a[72:75], v[220:223], 0\n"
      "v_mov_b32 v165, v76\n"
      "v_fmac_f32 v150, v201, v201\n"
      "v_mfma_f32_16x16x32_bf16 v[112:115], a[76:79], v[142:145], v[112:115]\n"
      "v_permlane32_swap_b32 v76, v165\n"
      "v_mfma_f32_16x16x32_bf16 v[112:115], a[80:83], v[166:169], v[112:115]\n"
      "s_or_b32 s95, s95, s48\n"
      "v_fmac_f32_e64 v150, v210, v210\n"
      "v_cvt_pk_bf16_f32 v131, v132, v133\n"
      "v_mfma_f32_16x16x32_bf16 v[112:115], a[84:87], v[186:189], v[112:115]\n"
      "v_add_f32 v76, v76, v165\n"
      "v_fmac_f32 v150, v211, v211\n"
      "v_mfma_f32_16x16x32_bf16 v[112:115], a[88:91], v[202:205], v[112:115]\n"
      "v_rsq_f32_e64 v76, v76\n"
      "v_mfma_f32_16x16x32_bf16 v[112:115], a[92:95], v[100:103], v[112:115]\n"
      "s_cmp_ge_u32 s95, 0x40000000\n"
      "s_cselect_b32 s94, -1, -2\n"
      "v_mov_b32 v165, v76\n"
      "v_mfma_f32_16x16x32_bf16 v[104:107], a[96:99], v[220:223], 0\n"
      "s_nop 0\n"
      "v_permlane16_swap_b32 v76, v165\n"
      "v_mfma_f32_16x16x32_bf16 v[104:107], a[100:103], v[142:145], v[104:107]\n"
      "v_fmac_f32 v150, v212, v212\n"
      "v_fma_f32 v82, v112, v112, s51\n"
      "v_mfma_f32_16x16x32_bf16 v[104:107], a[104:107], v[166:169], v[104:107]\n"
      "v_fmac_f32 v82, v113, v113\n"
      "v_cvt_pk_bf16_f32 v127, v208, v209\n"
      "v_mfma_f32_16x16x32_bf16 v[104:107], a[108:111], v[186:189], v[104:107]\n"
      "v_mul_f32 v139, v165, v139\n"
      "v_mul_f32_e64 v181, v165, v181\n"
      "v_mfma_f32_16x16x32_bf16 v[104:107], a[112:115], v[202:205], v[104:107]\n"
      "v_cvt_pk_bf16_f32 v126, v206, v207\n"
      "v_mul_f32 v76, s50, v76\n"
      "v_mfma_f32_16x16x32_bf16 v[104:107], a[116:119], v[100:103], v[104:107]\n"
      "v_mul_f32 v171, v76, v171\n"
      "v_fmac_f32 v150, v213, v213\n"
      "v_mfma_f32_16x16x32_bf16 v[206:209], a[120:123], v[220:223], 0\n"
      "v_mul_f32 v191, v76, v191\n"
      "v_fmac_f32 v82, v114, v114\n"
      "v_mfma_f32_16x16x32_bf16 v[206:209], a[124:127], v[142:145], v[206:209]\n"
      "v_fmac_f32 v82, v115, v115\n"
      "v_mul_f32_e64 v170, v76, v170\n"
      "v_mfma_f32_16x16x32_bf16 v[206:209], a[128:131], v[166:169], v[206:209]\n"
      "ds_write_b128 v5, v[124:127] offset:30720\n"
      "v_pk_mul_f32 v[178:179], v[178:179], v[164:165] op_sel:[0,1]\n"
      "v_mfma_f32_16x16x32_bf16 v[206:209], a[132:135], v[186:189], v[206:209]\n"
      "v_fmac_f32 v82, v104, v104\n"
      "v_mul_f32 v116, v165, v116\n"
      "v_mfma_f32_16x16x32_bf16 v[206:209], a[136:139], v[202:205], v[206:209]\n"
      "v_fmac_f32 v82, v105, v105\n"
      "v_fmac_f32 v150, v134, v134\n"
      "v_mfma_f32_16x16x32_bf16 v[206:209], a[140:143], v[100:103], v[206:209]\n"
      "ds_write_b64 v6, v[182:183] offset:30720\n"
      "v_mfma_f32_16x16x32_bf16 v[122:125], v[220:223], a[144:147], 0\n"
      "v_mul_f32 v180, v165, v180\n"
      "v_mul_f32 v173, v76, v173\n"
      "v_mfma_f32_16x16x32_bf16 v[122:125], v[142:145], a[148:151], v[122:125]\n"
      "v_fmac_f32 v82, v106, v106\n"
      "v_mul_f32 v176, v76, v176\n"
      "v_mfma_f32_16x16x32_bf16 v[122:125], v[166:169], a[152:155], v[122:125]\n"
      "v_mul_f32 v119, v165, v119\n"
      "v_mul_f32 v138, v165, v138\n"
      "v_mfma_f32_16x16x32_bf16 v[122:125], v[186:189], a[156:159], v[122:125]\n"
      "v_fmac_f32 v150, v135, v135\n"
      "v_fmac_f32 v82, v107, v107\n"
      "v_mfma_f32_16x16x32_bf16 v[122:125], v[202:205], a[160:163], v[122:125]\n"
      "v_mul_f32 v117, v165, v117\n"
      "v_mul_f32 v193, v76, v193\n"
      "v_mfma_f32_16x16x32_bf16 v[122:125], v[100:103], a[164:167], v[122:125]\n"
      "v_mul_f32 v172, v76, v172\n"
      "v_mul_f32 v190, v76, v190\n"
      "v_mfma_f32_16x16x32_bf16 v[182:185], v[220:223], a[168:171], 0\n"
      "v_fmac_f32_e64 v150, v136, v136\n"
      "v_cvt_pk_bf16_f32 v116, v116, v117\n"
      "v_mfma_f32_16x16x32_bf16 v[182:185], v[142:145], a[172:175], v[182:185]\n"
      "v_mul_f32 v141, v165, v141\n"
      "v_mul_f32 v118, v165, v118\n"
      "v_mfma_f32_16x16x32_bf16 v[182:185], v[166:169], a[176:179], v[182:185]\n"
      "v_cvt_pk_bf16_f32 v216, v122, v123\n"
      "v_fmac_f32 v150, v137, v137\n"
      "v_mfma_f32_16x16x32_bf16 v[182:185], v[186:189], a[180:183], v[182:185]\n"
      "v_fmac_f32 v82, v206, v206\n"
      "v_mul_f32_e64 v192, v76, v192\n"
      "v_mfma_f32_16x16x32_bf16 v[182:185], v[202:205], a[184:187], v[182:185]\n"
      "v_cvt_pk_bf16_f32 v178, v178, v179\n"
      "v_fmac_f32_e64 v82, v207, v207\n"
      "v_mfma_f32_16x16x32_bf16 v[182:185], v[100:103], a[188:191], v[182:185]\n"
      "ds_read_b128 v[126:129], v1 offset:28672\n"
      "v_fmac_f32 v82, v208, v208\n"
      "v_mfma_f32_16x16x32_bf16 v[120:123], v[220:223], a[192:195], 0\n"
      "v_fmac_f32 v82, v209, v209\n"
      "v_cvt_pk_bf16_f32 v217, v124, v125\n"
      "v_mfma_f32_16x16x32_bf16 v[120:123], v[142:145], a[196:199], v[120:123]\n"
      "v_permlane16_swap_b32 v150, v82\n"
      "v_mfma_f32_16x16x32_bf16 v[120:123], v[166:169], a[200:203], v[120:123]\n"
      "ds_read_b128 v[142:145], v0 offset:34816\n"
      "v_mul_f32 v140, v165, v140\n"
      "v_mfma_f32_16x16x32_bf16 v[120:123], v[186:189], a[204:207], v[120:123]\n"
      "v_add_f32 v150, v150, v82\n"
      "v_mov_b32 v82, v150\n"
      "v_mfma_f32_16x16x32_bf16 v[120:123], v[202:205], a[208:211], v[120:123]\n"
      "s_cmp_gt_u32 s42, s44\n"
      "s_cselect_b32 s95, s94, s95\n"
      "v_mul_f32 v175, v76, v175\n"
      "v_mfma_f32_16x16x32_bf16 v[120:123], v[100:103], a[212:215], v[120:123]\n"
      "v_permlane32_swap_b32 v150, v82\n"
      "v_mfma_f32_16x16x32_bf16 v[164:167], a[0:3], v[146:149], 0\n"
      "s_waitcnt lgkmcnt(1)\n"
      "v_add_f32 v150, v150, v82\n"
      "v_cvt_pk_bf16_f32 v170, v170, v171\n"
      "v_mfma_f32_16x16x32_bf16 v[164:167], a[4:7], v[156:159], v[164:167]\n"
      "v_rsq_f32 v150, v150\n"
      "v_mfma_f32_16x16x32_bf16 v[164:167], a[8:11], v[96:99], v[164:167]\n"
      "v_mov_b32 v82, v150\n"
      "v_cvt_pk_bf16_f32 v190, v190, v191\n"
      "v_mfma_f32_16x16x32_bf16 v[164:167], a[12:15], v[126:129], v[164:167]\n"
      "s_waitcnt lgkmcnt(0)\n"
      "v_permlane16_swap_b32 v150, v82\n"
      "v_mfma_f32_16x16x32_bf16 v[164:167], a[16:19], v[142:145], v[164:167]\n"
      "v_mul_f32_e64 v207, v82, v207\n"
      "v_cvt_pk_bf16_f32 v171, v172, v173\n"
      "v_mfma_f32_16x16x32_bf16 v[164:167], a[20:23], v[160:163], v[164:167]\n"
      "v_cvt_pk_bf16_f32 v179, v180, v181\n"
      "v_cvt_pk_bf16_f32 v117, v118, v119\n"
      "v_mfma_f32_16x16x32_bf16 v[218:221], a[24:27], v[146:149], 0\n"
      "v_mul_f32 v177, v76, v177\n"
      "v_mul_f32 v150, s50, v150\n"
      "v_mfma_f32_16x16x32_bf16 v[218:221], a[28:31], v[156:159], v[218:221]\n"
      "v_mul_f32 v112, v82, v112\n"
      "v_mul_f32 v198, v150, v198\n"
      "v_mfma_f32_16x16x32_bf16 v[218:221], a[32:35], v[96:99], v[218:221]\n"
      "v_mul_f32 v135, v150, v135\n"
      "v_mul_f32 v113, v82, v113\n"
      "v_mfma_f32_16x16x32_bf16 v[218:221], a[36:39], v[126:129], v[218:221]\n"
      "v_fma_f32 v111, v164, v164, s51\n"
      "v_mul_f32 v206, v82, v206\n"
      "v_mfma_f32_16x16x32_bf16 v[218:221], a[40:43], v[142:145], v[218:221]\n"
      "v_mul_f32 v199, v150, v199\n"
      "v_mul_f32_e64 v211, v150, v211\n"
      "v_mfma_f32_16x16x32_bf16 v[218:221], a[44:47], v[160:163], v[218:221]\n"
      "v_mul_f32 v201, v150, v201\n"
      "v_fmac_f32 v111, v165, v165\n"
      "v_mfma_f32_16x16x32_bf16 v[202:205], a[48:51], v[146:149], 0\n"
      "v_cvt_pk_bf16_f32 v191, v192, v193\n"
      "v_fmac_f32 v111, v166, v166\n"
      "v_mfma_f32_16x16x32_bf16 v[202:205], a[52:55], v[156:159], v[202:205]\n"
      "v_fmac_f32 v111, v167, v167\n"
      "v_mul_f32 v134, v150, v134\n"
      "v_mfma_f32_16x16x32_bf16 v[202:205], a[56:59], v[96:99], v[202:205]\n"
      "v_mul_f32 v174, v76, v174\n"
      "v_mul_f32 v200, v150, v200\n"
      "v_mfma_f32_16x16x32_bf16 v[202:205], a[60:63], v[126:129], v[202:205]\n"
      "v_cvt_pk_bf16_f32 v196, v120, v121\n"
      "v_mul_f32 v136, v150, v136\n"
      "v_mfma_f32_16x16x32_bf16 v[202:205], a[64:67], v[142:145], v[202:205]\n"
      "v_cvt_pk_bf16_f32 v134, v134, v135\n"
      "v_cvt_pk_bf16_f32 v198, v198, v199\n"
      "v_mfma_f32_16x16x32_bf16 v[202:205], a[68:71], v[160:163], v[202:205]\n"
      "v_mul_f32 v212, v150, v212\n"
      "v_mul_f32 v104, v82, v104\n"
      "v_mfma_f32_16x16x32_bf16 v[100:103], a[72:75], v[146:149], 0\n"
      "v_cvt_pk_bf16_f32 v112, v112, v113\n"
      "v_cvt_pk_bf16_f32 v180, v138, v139\n"
      "v_mfma_f32_16x16x32_bf16 v[100:103], a[76:79], v[156:159], v[100:103]\n"
      "v_fmac_f32 v111, v218, v218\n"
      "v_fmac_f32 v111, v219, v219\n"
      "v_mfma_f32_16x16x32_bf16 v[100:103], a[80:83], v[96:99], v[100:103]\n"
      "v_cvt_pk_bf16_f32 v181, v140, v141\n"
      "v_cmp_ne_u32 s[56:57], 0, v108\n"
      "v_mfma_f32_16x16x32_bf16 v[100:103], a[84:87], v[126:129], v[100:103]\n"
      "v_mul_f32 v114, v82, v114\n"
      "v_mul_f32 v107, v82, v107\n"
      "v_mfma_f32_16x16x32_bf16 v[100:103], a[88:91], v[142:145], v[100:103]\n"
      "v_mul_f32_e64 v208, v82, v208\n"
      "v_cvt_pk_bf16_f32 v199, v200, v201\n"
      "v_mfma_f32_16x16x32_bf16 v[100:103], a[92:95], v[160:163], v[100:103]\n"
      "v_mul_f32 v210, v150, v210\n"
      "v_fmac_f32 v111, v220, v220\n"
      "v_mfma_f32_16x16x32_bf16 v[118:121], a[96:99], v[146:149], 0\n"
      "v_cvt_pk_bf16_f32 v192, v174, v175\n"
      "v_cvt_pk_bf16_f32 v133, v184, v185\n"
      "v_mfma_f32_16x16x32_bf16 v[118:121], a[100:103], v[156:159], v[118:121]\n"
      "v_fmac_f32 v111, v221, v221\n"
      "v_mul_f32 v115, v82, v115\n"
      "v_mfma_f32_16x16x32_bf16 v[118:121], a[104:107], v[96:99], v[118:121]\n"
      "s_and_b32 s47, s47, -4\n"
      "v_fmac_f32 v111, v202, v202\n"
      "v_fma_f32 v168, v100, v100, s51\n"
      "v_mfma_f32_16x16x32_bf16 v[118:121], a[108:111], v[126:129], v[118:121]\n"
      "v_mul_f32 v106, v82, v106\n"
      "v_cvt_pk_bf16_f32 v206, v206, v207\n"
      "v_mfma_f32_16x16x32_bf16 v[118:121], a[112:115], v[142:145], v[118:121]\n"
      "v_mul_f32 v105, v82, v105\n"
      "v_fmac_f32_e64 v168, v101, v101\n"
      "v_mfma_f32_16x16x32_bf16 v[118:121], a[116:119], v[160:163], v[118:121]\n"
      "v_fmac_f32 v168, v102, v102\n"
      "v_cvt_pk_bf16_f32 v113, v114, v115\n"
      "v_mfma_f32_16x16x32_bf16 v[172:175], a[120:123], v[146:149], 0\n"
      "v_fmac_f32 v111, v203, v203\n"
      "v_cvt_pk_bf16_f32 v115, v106, v107\n"
      "v_mfma_f32_16x16x32_bf16 v[172:175], a[124:127], v[156:159], v[172:175]\n"
      "v_cvt_pk_bf16_f32 v114, v104, v105\n"
      "v_fmac_f32_e64 v111, v204, v204\n"
      "v_mfma_f32_16x16x32_bf16 v[172:175], a[128:131], v[96:99], v[172:175]\n"
      "v_cvt_pk_bf16_f32 v193, v176, v177\n"
      "v_fmac_f32_e64 v168, v103, v103\n"
      "v_mfma_f32_16x16x32_bf16 v[172:175], a[132:135], v[126:129], v[172:175]\n"
      "v_cvt_pk_bf16_f32 v132, v182, v183\n"
      "v_fmac_f32 v168, v118, v118\n"
      "v_mfma_f32_16x16x32_bf16 v[172:175], a[136:139], v[142:145], v[172:175]\n"
      "v_cvt_pk_bf16_f32 v197, v122, v123\n"
      "v_fmac_f32 v168, v119, v119\n"
      "v_mfma_f32_16x16x32_bf16 v[172:175], a[140:143], v[160:163], v[172:175]\n"
      "v_fmac_f32 v111, v205, v205\n"
      "v_mul_f32 v209, v82, v209\n"
      "v_mfma_f32_16x16x32_bf16 v[186:189], v[146:149], a[144:147], 0\n"
      "v_fmac_f32 v168, v120, v120\n"
      "v_fmac_f32 v168, v121, v121\n"
      "v_mfma_f32_16x16x32_bf16 v[186:189], v[156:159], a[148:151], v[186:189]\n"
      "v_mul_f32 v213, v150, v213\n"
      "v_mul_f32 v137, v150, v137\n"
      "v_mfma_f32_16x16x32_bf16 v[186:189], v[96:99], a[152:155], v[186:189]\n"
      "ds_read_b128 v[104:107], v2 offset:3072\n"
      "v_cvt_pk_bf16_f32 v201, v212, v213\n"
      "v_mfma_f32_16x16x32_bf16 v[186:189], v[126:129], a[156:159], v[186:189]\n"
      "v_fmac_f32 v168, v172, v172\n"
      "v_fmac_f32 v168, v173, v173\n"
      "v_mfma_f32_16x16x32_bf16 v[186:189], v[142:145], a[160:163], v[186:189]\n"
      "v_cvt_pk_bf16_f32 v200, v210, v211\n"
      "v_fmac_f32 v168, v174, v174\n"
      "v_mfma_f32_16x16x32_bf16 v[186:189], v[160:163], a[164:167], v[186:189]\n"
      "v_cvt_pk_bf16_f32 v207, v208, v209\n"
      "v_fmac_f32 v168, v175, v175\n"
      "v_mfma_f32_16x16x32_bf16 v[122:125], v[146:149], a[168:171], 0\n"
      "s_nop 0\n"
      "v_permlane16_swap_b32 v111, v168\n"
      "v_mfma_f32_16x16x32_bf16 v[122:125], v[156:159], a[172:175], v[122:125]\n"
      "v_add_f32 v111, v111, v168\n"
      "v_mov_b32 v168, v111\n"
      "v_cvt_pk_bf16_f32 v76, v186, v187\n"
      "v_mfma_f32_16x16x32_bf16 v[122:125], v[96:99], a[176:179], v[122:125]\n"
      "v_permlane32_swap_b32_e64 v111, v168\n"
      "v_mfma_f32_16x16x32_bf16 v[122:125], v[126:129], a[180:183], v[122:125]\n"
      "ds_read_b128 v[182:185], v2\n"
      "v_add_f32 v111, v111, v168\n"
      "v_mfma_f32_16x16x32_bf16 v[122:125], v[142:145], a[184:187], v[122:125]\n"
      "v_rsq_f32 v111, v111\n"
      "v_mfma_f32_16x16x32_bf16 v[122:125], v[160:163], a[188:191], v[122:125]\n"
      "ds_read_u16 v82, v11 offset:1200\n"
      "v_mov_b32 v168, v111\n"
      "v_mfma_f32_16x16x32_bf16 v[208:211], v[146:149], a[192:195], 0\n"
      "s_nop 0\n"
      "v_permlane16_swap_b32_e64 v111, v168\n"
      "v_mfma_f32_16x16x32_bf16 v[208:211], v[156:159], a[196:199], v[208:211]\n"
      "ds_read_b128 v[148:151], v2 offset:6144\n"
      "v_mul_f32 v119, v168, v119\n"
      "v_mfma_f32_16x16x32_bf16 v[208:211], v[96:99], a[200:203], v[208:211]\n"
      "v_mul_f32 v100, v168, v100\n"
      "v_mul_f32 v103, v168, v103\n"
      "v_mfma_f32_16x16x32_bf16 v[208:211], v[126:129], a[204:207], v[208:211]\n"
      "ds_read_b128 v[126:129], v2 offset:9216\n"
      "v_mul_f32 v102, v168, v102\n"
      "v_mfma_f32_16x16x32_bf16 v[208:211], v[142:145], a[208:211], v[208:211]\n"
      "s_waitcnt lgkmcnt(3)\n"
      "v_cvt_pk_bf16_f32 v77, v188, v189\n"
      "v_mul_f32 v101, v168, v101\n"
      "v_mfma_f32_16x16x32_bf16 v[208:211], v[160:163], a[212:215], v[208:211]\n"
      "ds_read_u16 v78, v11 offset:1168\n"
      "v_mul_f32 v121, v168, v121\n"
      "v_mfma_f32_16x16x32_bf16 v[186:189], a[216:219], v[182:185], 0\n"
      "ds_read_b64 v[146:147], v10 offset:1192\n"
      "v_mul_f32 v175, v168, v175\n"
      "v_mfma_f32_16x16x32_bf16 v[186:189], a[220:223], v[104:107], v[186:189]\n"
      "s_waitcnt lgkmcnt(3)\n"
      "v_mul_f32 v172, v168, v172\n"
      "v_cvt_pk_bf16_f32 v135, v136, v137\n"
      "v_mfma_f32_16x16x32_bf16 v[186:189], a[224:227], v[148:151], v[186:189]\n"
      "ds_read_b128 v[160:163], v10 offset:1200\n"
      "v_cvt_pk_bf16_f32 v85, v210, v211\n"
      "v_mfma_f32_16x16x32_bf16 v[142:145], a[240:243], v[182:185], 0\n"
      "v_mul_f32 v120, v168, v120\n"
      "v_mul_f32 v173, v168, v173\n"
      "v_mfma_f32_16x16x32_bf16 v[142:145], a[244:247], v[104:107], v[142:145]\n"
      "v_cvt_pk_bf16_f32 v172, v172, v173\n"
      "v_cvt_pk_bf16_f32 v100, v100, v101\n"
      "v_mfma_f32_16x16x32_bf16 v[142:145], a[248:251], v[148:151], v[142:145]\n"
      "v_mul_f32 v118, v168, v118\n"
      "v_mul_f32 v111, s50, v111\n"
      "v_mfma_f32_16x16x32_bf16 v[156:159], v[232:235], v[182:185], 0\n"
      "v_mul_f32_e64 v220, v111, v220\n"
      "v_cvt_pk_bf16_f32 v101, v102, v103\n"
      "v_mfma_f32_16x16x32_bf16 v[156:159], v[236:239], v[104:107], v[156:159]\n"
      "v_cvt_pk_bf16_f32 v80, v122, v123\n"
      "v_mul_f32_e64 v174, v168, v174\n"
      "v_mfma_f32_16x16x32_bf16 v[156:159], v[240:243], v[148:151], v[156:159]\n"
      "v_cvt_pk_bf16_f32 v102, v118, v119\n"
      "v_cvt_pk_bf16_f32 v103, v120, v121\n"
      "v_mfma_f32_16x16x32_bf16 v[182:185], v[178:181], v[190:193], v[40:43]\n"
      "ds_read_b128 v[120:123], v2 offset:12288\n"
      "v_mul_f32_e64 v204, v111, v204\n"
      "v_mfma_f32_16x16x32_bf16 v[210:213], v[112:115], v[190:193], v[44:47]\n"
      "s_waitcnt lgkmcnt(1)\n"
      "v_cvt_pk_bf16_f32 v173, v174, v175\n"
      "v_mul_f32 v202, v111, v202\n"
      "v_mfma_f32_16x16x32_bf16 v[104:107], v[100:103], v[190:193], v[48:51]\n"
      "ds_read_b128 v[174:177], v2 offset:15360\n"
      "v_cvt_pk_bf16_f32 v81, v124, v125\n"
      "v_mfma_f32_16x16x32_bf16 v[148:151], v[160:163], v[190:193], v[24:27]\n"
      "v_mul_f32 v164, v111, v164\n"
      "v_mul_f32 v166, v111, v166\n"
      "v_mfma_f32_16x16x16_bf16 v[182:185], v[116:117], v[170:171], v[182:185]\n"
      "v_mul_f32 v203, v111, v203\n"
      "v_mul_f32 v221, v111, v221\n"
      "v_cvt_pk_bf16_f32 v84, v208, v209\n"
      "v_mfma_f32_16x16x16_bf16 v[210:213], v[206:207], v[170:171], v[210:213]\n"
      "v_mul_f32 v165, v111, v165\n"
      "v_cvt_pk_bf16_f32 v164, v164, v165\n"
      "v_mul_f32 v167, v111, v167\n"
      "v_mfma_f32_16x16x16_bf16 v[104:107], v[172:173], v[170:171], v[104:107]\n"
      "v_cvt_pk_bf16_f32 v165, v166, v167\n"
      "v_cvt_pk_bf16_f32 v202, v202, v203\n"
      "v_max3_f32 v136, v182, v183, v184\n"
      "v_mfma_f32_16x16x16_bf16 v[148:151], v[146:147], v[170:171], v[148:151]\n"
      "v_max3_f32 v193, v185, v210, v211\n"
      "v_mul_f32 v205, v111, v205\n"
      "v_cvt_pk_bf16_f32 v167, v220, v221\n"
      "v_mfma_f32_16x16x32_bf16 v[186:189], a[228:231], v[126:129], v[186:189]\n"
      "s_waitcnt lgkmcnt(0)\n"
      "v_max3_f32 v193, v193, v104, v105\n"
      "v_max3_f32 v136, v136, v212, v213\n"
      "v_mfma_f32_16x16x32_bf16 v[186:189], a[232:235], v[120:123], v[186:189]\n"
      "v_cvt_pk_bf16_f32 v203, v204, v205\n"
      "v_max3_f32 v136, v136, v106, v107\n"
      "v_mfma_f32_16x16x32_bf16 v[186:189], a[236:239], v[174:177], v[186:189]\n"
      "ds_read_b128 v[168:171], v2 offset:256\n"
      "v_max3_f32 v136, v136, v193, v148\n"
      "v_mfma_f32_16x16x32_bf16 v[142:145], a[252:255], v[126:129], v[142:145]\n"
      "ds_read_b128 v[190:193], v2 offset:6400\n"
      "v_mov_b32 v137, v136\n"
      "v_mfma_f32_16x16x32_bf16 v[142:145], v[224:227], v[120:123], v[142:145]\n"
      "s_nop 0\n"
      "v_permlane16_swap_b32_e64 v136, v137\n"
      "v_mfma_f32_16x16x32_bf16 v[142:145], v[228:231], v[174:177], v[142:145]\n"
      "v_max_f32 v136, v136, v137\n"
      "v_mov_b32 v137, v136\n"
      "v_mfma_f32_16x16x32_bf16 v[156:159], v[244:247], v[126:129], v[156:159]\n"
      "ds_read_b128 v[124:127], v2 offset:3328\n"
      "v_permlane32_swap_b32 v136, v137\n"
      "v_max_f32 v136, v136, v137\n"
      "v_sub_f32_e64 v148, v148, v136\n"
      "v_pk_add_f32 v[212:213], v[212:213], v[136:137] op_sel_hi:[1,0] neg_lo:[0,1] neg_hi:[0,1]\n"
      "v_pk_add_f32 v[182:183], v[182:183], v[136:137] op_sel_hi:[1,0] neg_lo:[0,1] neg_hi:[0,1]\n"
      "v_pk_add_f32 v[210:211], v[210:211], v[136:137] op_sel_hi:[1,0] neg_lo:[0,1] neg_hi:[0,1]\n"
      "v_pk_add_f32 v[106:107], v[106:107], v[136:137] op_sel_hi:[1,0] neg_lo:[0,1] neg_hi:[0,1]\n"
      "v_pk_mul_f32 v[218:219], v[218:219], v[110:111] op_sel:[0,1]\n"
      "v_pk_add_f32 v[104:105], v[104:105], v[136:137] op_sel_hi:[1,0] neg_lo:[0,1] neg_hi:[0,1]\n"
      "v_pk_add_f32 v[184:185], v[184:185], v[136:137] op_sel_hi:[1,0] neg_lo:[0,1] neg_hi:[0,1]\n"
      "v_mfma_f32_16x16x32_bf16 v[156:159], v[248:251], v[120:123], v[156:159]\n"
      "v_exp_f32 v104, v104\n"
      "v_mfma_f32_16x16x32_bf16 v[156:159], v[252:255], v[174:177], v[156:159]\n"
      "v_exp_f32 v106, v106\n"
      "v_mfma_f32_16x16x32_bf16 v[174:177], v[178:181], v[198:201], v[52:55]\n"
      "v_exp_f32 v184, v184\n"
      "v_mfma_f32_16x16x32_bf16 v[96:99], v[112:115], v[198:201], v[56:59]\n"
      "v_exp_f32 v105, v105\n"
      "v_mfma_f32_16x16x32_bf16 v[136:139], v[100:103], v[198:201], v[60:63]\n"
      "v_exp_f32 v107, v107\n"
      "v_mfma_f32_16x16x32_bf16 v[108:111], v[160:163], v[198:201], v[26:29]\n"
      "s_waitcnt lgkmcnt(0)\n"
      "v_exp_f32 v212, v212\n"
      "v_mfma_f32_16x16x16_bf16 v[174:177], v[116:117], v[134:135], v[174:177]\n"
      "v_exp_f32 v213, v213\n"
      "v_cvt_pk_bf16_f32 v166, v218, v219\n"
      "v_mfma_f32_16x16x16_bf16 v[96:99], v[206:207], v[134:135], v[96:99]\n"
      "v_exp_f32_e64 v185, v185\n"
      "v_cvt_pk_bf16_f32 v88, v104, v105\n"
      "v_mfma_f32_16x16x16_bf16 v[136:139], v[172:173], v[134:135], v[136:139]\n"
      "v_exp_f32 v183, v183\n"
      "v_exp_f32 v182, v182\n"
      "v_mfma_f32_16x16x16_bf16 v[108:111], v[146:147], v[134:135], v[108:111]\n"
      "v_cvt_pk_bf16_f32 v182, v182, v183\n"
      "v_exp_f32_e64 v210, v210\n"
      "v_mfma_f32_16x16x32_bf16 v[120:123], a[216:219], v[168:171], 0\n"
      "v_max3_f32 v201, v174, v175, v176\n"
      "v_max3_f32 v105, v177, v96, v97\n"
      "v_mfma_f32_16x16x32_bf16 v[120:123], a[220:223], v[124:127], v[120:123]\n"
      "v_max3_f32 v105, v105, v136, v137\n"
      "v_max3_f32 v201, v201, v98, v99\n"
      "v_mfma_f32_16x16x32_bf16 v[120:123], a[224:227], v[190:193], v[120:123]\n"
      "v_exp_f32_e64 v148, v148\n"
      "v_mfma_f32_16x16x32_bf16 v[218:221], a[240:243], v[168:171], 0\n"
      "v_cvt_pk_bf16_f32 v89, v106, v107\n"
      "v_max3_f32 v201, v201, v138, v139\n"
      "v_mfma_f32_16x16x32_bf16 v[218:221], a[244:247], v[124:127], v[218:221]\n"
      "v_cvt_pk_bf16_f32 v183, v184, v185\n"
      "v_cvt_pk_bf16_f32 v185, v212, v213\n"
      "v_mfma_f32_16x16x32_bf16 v[218:221], a[248:251], v[190:193], v[218:221]\n"
      "v_cvt_pk_bf16_f32 v90, v148, 0\n"
      "v_max3_f32 v201, v201, v105, v108\n"
      "v_mfma_f32_16x16x32_bf16 v[104:107], v[232:235], v[168:171], 0\n"
      "v_exp_f32_e64 v211, v211\n"
      "v_mfma_f32_16x16x32_bf16 v[104:107], v[236:239], v[124:127], v[104:107]\n"
      "v_mov_b32 v200, v201\n"
      "v_cvt_pk_bf16_f32 v184, v210, v211\n"
      "v_mfma_f32_16x16x32_bf16 v[104:107], v[240:243], v[190:193], v[104:107]\n"
      "v_permlane16_swap_b32 v201, v200\n"
      "v_mfma_f32_16x16x32_bf16 v[124:127], v[32:35], v[182:185], 0\n"
      "ds_read_b128 v[208:211], v2 offset:9472\n"
      "v_max_f32 v201, v201, v200\n"
      "v_mfma_f32_16x16x32_bf16 v[124:127], v[32:35], v[88:91], v[124:127]\n"
      "v_mov_b32 v200, v201\n"
      "s_waitcnt vmcnt(1)\n"
      "v_or3_b32 v152, v152, v153, v154\n"
      "v_mfma_f32_16x16x32_bf16 v[148:151], v[214:217], v[182:185], 0\n"
      "v_mul_f32 v129, v187, v187\n"
      "v_fma_f32 v128, v186, v186, s52\n"
      "v_mfma_f32_16x16x32_bf16 v[148:151], v[76:79], v[88:91], v[148:151]\n"
      "v_permlane32_swap_b32 v201, v200\n"
      "v_mfma_f32_16x16x32_bf16 v[168:171], v[130:133], v[182:185], 0\n"
      "v_max_f32 v201, v201, v200\n"
      "v_sub_f32 v108, v108, v201\n"
      "s_waitcnt vmcnt(0)\n"
      "v_or3_b32 v152, v152, v155, v93\n"
      "v_rcp_f32_e64 v124, v124\n"
      "v_pk_add_f32 v[138:139], v[138:139], v[200:201] op_sel:[0,1] neg_lo:[0,1] neg_hi:[0,1]\n"
      "v_pk_add_f32 v[174:175], v[174:175], v[200:201] op_sel:[0,1] neg_lo:[0,1] neg_hi:[0,1]\n"
      "v_pk_mul_f32 v[148:149], v[148:149], v[124:125] op_sel_hi:[1,0]\n"
      "v_pk_add_f32 v[96:97], v[96:97], v[200:201] op_sel:[0,1] neg_lo:[0,1] neg_hi:[0,1]\n"
      "v_pk_add_f32 v[176:177], v[176:177], v[200:201] op_sel:[0,1] neg_lo:[0,1] neg_hi:[0,1]\n"
      "v_pk_mul_f32 v[150:151], v[150:151], v[124:125] op_sel_hi:[1,0]\n"
      "v_exp_f32 v96, v96\n"
      "v_mfma_f32_16x16x32_bf16 v[168:171], v[80:83], v[88:91], v[168:171]\n"
      "v_exp_f32 v138, v138\n"
      "v_mfma_f32_16x16x32_bf16 v[190:193], v[194:197], v[182:185], 0\n"
      "v_exp_f32_e64 v139, v139\n"
      "v_mfma_f32_16x16x32_bf16 v[190:193], v[84:87], v[88:91], v[190:193]\n"
      "v_cvt_pk_bf16_f32 v148, v148, v149\n"
      "v_cvt_pk_bf16_f32 v93, v138, v139\n"
      "v_exp_f32_e64 v174, v174\n"
      "v_cvt_pk_bf16_f32 v149, v150, v151\n"
      "v_pk_fma_f32 v[128:129], v[188:189], v[188:189], v[128:129]\n"
      "v_pk_add_f32 v[136:137], v[136:137], v[200:201] op_sel:[0,1] neg_lo:[0,1] neg_hi:[0,1]\n"
      "v_pk_mul_f32 v[168:169], v[168:169], v[124:125] op_sel_hi:[1,0]\n"
      "v_pk_add_f32 v[98:99], v[98:99], v[200:201] op_sel:[0,1] neg_lo:[0,1] neg_hi:[0,1]\n"
      "v_pk_mul_f32 v[192:193], v[192:193], v[124:125] op_sel_hi:[1,0]\n"
      "v_pk_fma_f32 v[128:129], v[142:143], v[142:143], v[128:129]\n"
      "v_pk_mul_f32 v[170:171], v[170:171], v[124:125] op_sel_hi:[1,0]\n"
      "v_pk_mul_f32 v[190:191], v[190:191], v[124:125] op_sel_hi:[1,0]\n"
      "v_cvt_pk_bf16_f32 v150, v168, v169\n"
      "v_cvt_pk_bf16_f32 v151, v170, v171\n"
      "v_mfma_f32_16x16x32_bf16 v[168:171], v[178:181], v[164:167], v[64:67]\n"
      "v_exp_f32 v99, v99\n"
      "v_mfma_f32_16x16x32_bf16 v[198:201], v[112:115], v[164:167], v[68:71]\n"
      "s_waitcnt lgkmcnt(0)\n"
      "v_exp_f32_e64 v176, v176\n"
      "v_mfma_f32_16x16x32_bf16 v[138:141], v[100:103], v[164:167], v[72:75]\n"
      "ds_read_b128 v[124:127], v2 offset:12544\n"
      "v_cvt_pk_bf16_f32 v190, v190, v191\n"
      "v_mfma_f32_16x16x32_bf16 v[182:185], v[160:163], v[164:167], v[28:31]\n"
      "ds_read_b128 v[164:167], v2 offset:15616\n"
      "v_exp_f32 v177, v177\n"
      "v_mfma_f32_16x16x16_bf16 v[168:171], v[116:117], v[202:203], v[168:171]\n"
      "v_cvt_pk_bf16_f32 v191, v192, v193\n"
      "v_exp_f32 v175, v175\n"
      "v_mfma_f32_16x16x16_bf16 v[198:201], v[206:207], v[202:203], v[198:201]\n"
      "v_cvt_pk_bf16_f32 v174, v174, v175\n"
      "v_exp_f32 v97, v97\n"
      "v_mfma_f32_16x16x16_bf16 v[138:141], v[172:173], v[202:203], v[138:141]\n"
      "v_cvt_pk_bf16_f32 v175, v176, v177\n"
      "v_exp_f32 v108, v108\n"
      "v_mfma_f32_16x16x16_bf16 v[182:185], v[146:147], v[202:203], v[182:185]\n"
      "v_cvt_pk_bf16_f32 v176, v96, v97\n"
      "v_exp_f32 v98, v98\n"
      "v_mfma_f32_16x16x32_bf16 v[120:123], a[228:231], v[208:211], v[120:123]\n"
      "s_waitcnt lgkmcnt(1)\n"
      "v_exp_f32 v137, v137\n"
      "v_mfma_f32_16x16x32_bf16 v[120:123], a[232:235], v[124:127], v[120:123]\n"
      "s_waitcnt lgkmcnt(0)\n"
      "v_max3_f32 v212, v168, v169, v170\n"
      "v_cvt_pk_bf16_f32 v94, v108, 0\n"
      "v_mfma_f32_16x16x32_bf16 v[120:123], a[236:239], v[164:167], v[120:123]\n"
      "ds_write_b128 v3, v[148:151] offset:18432\n"
      "v_mfma_f32_16x16x32_bf16 v[218:221], a[252:255], v[208:211], v[218:221]\n"
      "v_cvt_pk_bf16_f32 v177, v98, v99\n"
      "v_max3_f32 v203, v171, v198, v199\n"
      "v_mfma_f32_16x16x32_bf16 v[218:221], v[224:227], v[124:127], v[218:221]\n"
      "ds_read_b64 v[184:185], v10 offset:1184\n"
      "v_max3_f32 v212, v212, v200, v201\n"
      "v_mfma_f32_16x16x32_bf16 v[218:221], v[228:231], v[164:167], v[218:221]\n"
      "v_max3_f32 v203, v203, v138, v139\n"
      "v_max3_f32 v212, v212, v140, v141\n"
      "v_mfma_f32_16x16x32_bf16 v[104:107], v[244:247], v[208:211], v[104:107]\n"
      "v_exp_f32_e64 v136, v136\n"
      "v_mfma_f32_16x16x32_bf16 v[104:107], v[248:251], v[124:127], v[104:107]\n"
      "ds_read_b128 v[124:127], v10 offset:1168\n"
      "v_max3_f32 v212, v212, v203, v182\n"
      "v_mfma_f32_16x16x32_bf16 v[104:107], v[252:255], v[164:167], v[104:107]\n"
      "ds_read_b128 v[208:211], v7\n"
      "v_cvt_pk_bf16_f32 v92, v136, v137\n"
      "v_mfma_f32_16x16x32_bf16 v[164:167], v[32:35], v[174:177], 0\n"
      "v_mul_f32 v203, v121, v121\n"
      "v_mov_b32 v108, v212\n"
      "v_mfma_f32_16x16x32_bf16 v[164:167], v[32:35], v[92:95], v[164:167]\n"
      "s_and_b32 s4, s45, s8\n"
      "v_permlane16_swap_b32 v212, v108\n"
      "v_mfma_f32_16x16x32_bf16 v[148:151], v[214:217], v[174:177], 0\n"
      "s_add_u32 s42, s42, 1\n"
      "v_max_f32 v212, v212, v108\n"
      "v_mov_b32 v108, v212\n"
      "v_mfma_f32_16x16x32_bf16 v[148:151], v[76:79], v[92:95], v[148:151]\n"
      "s_nop 0\n"
      "v_rcp_f32_e64 v164, v164\n"
      "v_mfma_f32_16x16x32_bf16 v[96:99], v[130:133], v[174:177], 0\n"
      "v_permlane32_swap_b32 v212, v108\n"
      "v_mfma_f32_16x16x32_bf16 v[96:99], v[80:83], v[92:95], v[96:99]\n"
      "v_max_f32 v212, v212, v108\n"
      "v_sub_f32_e64 v182, v182, v212\n"
      "v_mfma_f32_16x16x32_bf16 v[108:111], v[194:197], v[174:177], 0\n"
      "s_waitcnt lgkmcnt(0)\n"
      "v_exp_f32 v182, v182\n"
      "v_mfma_f32_16x16x32_bf16 v[108:111], v[84:87], v[92:95], v[108:111]\n"
      "buffer_store_dwordx4 v[208:211], v20, s[24:27], s85 offen sc1\n"
      "v_fma_f32 v202, v120, v120, s52\n"
      "v_pk_mul_f32 v[148:149], v[148:149], v[164:165] op_sel_hi:[1,0]\n"
      "v_pk_add_f32 v[140:141], v[140:141], v[212:213] op_sel_hi:[1,0] neg_lo:[0,1] neg_hi:[0,1]\n"
      "v_pk_add_f32 v[170:171], v[170:171], v[212:213] op_sel_hi:[1,0] neg_lo:[0,1] neg_hi:[0,1]\n"
      "v_pk_fma_f32 v[128:129], v[144:145], v[144:145], v[128:129]\n"
      "v_pk_fma_f32 v[202:203], v[122:123], v[122:123], v[202:203]\n"
      "v_pk_add_f32 v[198:199], v[198:199], v[212:213] op_sel_hi:[1,0] neg_lo:[0,1] neg_hi:[0,1]\n"
      "v_pk_mul_f32 v[150:151], v[150:151], v[164:165] op_sel_hi:[1,0]\n"
      "v_pk_fma_f32 v[202:203], v[218:219], v[218:219], v[202:203]\n"
      "v_pk_mul_f32 v[110:111], v[110:111], v[164:165] op_sel_hi:[1,0]\n"
      "v_pk_add_f32 v[138:139], v[138:139], v[212:213] op_sel_hi:[1,0] neg_lo:[0,1] neg_hi:[0,1]\n"
      "v_pk_mul_f32 v[108:109], v[108:109], v[164:165] op_sel_hi:[1,0]\n"
      "v_pk_mul_f32 v[98:99], v[98:99], v[164:165] op_sel_hi:[1,0]\n"
      "v_pk_add_f32 v[200:201], v[200:201], v[212:213] op_sel_hi:[1,0] neg_lo:[0,1] neg_hi:[0,1]\n"
      "v_pk_add_f32 v[168:169], v[168:169], v[212:213] op_sel_hi:[1,0] neg_lo:[0,1] neg_hi:[0,1]\n"
      "v_cvt_pk_bf16_f32 v148, v148, v149\n"
      "v_pk_mul_f32 v[96:97], v[96:97], v[164:165] op_sel_hi:[1,0]\n"
      "v_mfma_f32_16x16x32_bf16 v[174:177], v[124:127], v[178:181], v[30:33]\n"
      "ds_read_b128 v[210:213], v2 offset:3584\n"
      "v_cvt_pk_bf16_f32 v149, v150, v151\n"
      "v_mfma_f32_16x16x32_bf16 v[164:167], v[124:127], v[112:115], v[30:33]\n"
      "ds_read_b128 v[178:181], v2 offset:6656\n"
      "v_cvt_pk_bf16_f32 v150, v96, v97\n"
      "v_mfma_f32_16x16x32_bf16 v[112:115], v[124:127], v[100:103], v[30:33]\n"
      "ds_read_b128 v[100:103], v2 offset:512\n"
      "v_exp_f32_e64 v201, v201\n"
      "v_mfma_f32_16x16x32_bf16 v[134:137], v[124:127], v[160:163], v[30:33]\n"
      "ds_read_b128 v[160:163], v7 offset:4096\n"
      "v_cvt_pk_bf16_f32 v151, v98, v99\n"
      "v_mfma_f32_16x16x16_bf16 v[174:177], v[184:185], v[116:117], v[174:177]\n"
      "v_exp_f32 v168, v168\n"
      "v_exp_f32 v140, v140\n"
      "v_mfma_f32_16x16x16_bf16 v[164:167], v[184:185], v[206:207], v[164:167]\n"
      "v_exp_f32 v171, v171\n"
      "v_exp_f32 v141, v141\n"
      "v_mfma_f32_16x16x16_bf16 v[112:115], v[184:185], v[172:173], v[112:115]\n"
      "v_exp_f32 v169, v169\n"
      "v_exp_f32 v198, v198\n"
      "v_exp_f32_e64 v170, v170\n"
      "v_pk_fma_f32 v[128:129], v[156:157], v[156:157], v[128:129]\n"
      "v_mfma_f32_16x16x16_bf16 v[134:137], v[184:185], v[146:147], v[134:137]\n"
      "v_cndmask_b32 v174, v174, v164, s[58:59]\n"
      "v_cvt_pk_bf16_f32 v90, v182, 0\n"
      "s_waitcnt lgkmcnt(0)\n"
      "v_mfma_f32_16x16x32_bf16 v[124:127], a[216:219], v[100:103], 0\n"
      "ds_read_b128 v[164:167], v2 offset:12800\n"
      "v_cndmask_b32 v174, v174, v112, s[60:61]\n"
      "v_mfma_f32_16x16x32_bf16 v[124:127], a[220:223], v[210:213], v[124:127]\n"
      "buffer_store_dwordx4 v[160:163], v21, s[24:27], s85 offen sc1\n"
      "v_mfma_f32_16x16x32_bf16 v[124:127], a[224:227], v[178:181], v[124:127]\n"
      "s_nop 0\n"
      "ds_read_b128 v[160:163], v2 offset:9728\n"
      "v_cndmask_b32 v174, v174, v134, s[62:63]\n"
      "v_mfma_f32_16x16x32_bf16 v[206:209], a[240:243], v[100:103], 0\n"
      "ds_read_b128 v[134:137], v2 offset:15872\n"
      "v_exp_f32_e64 v138, v138\n"
      "v_mfma_f32_16x16x32_bf16 v[206:209], a[244:247], v[210:213], v[206:209]\n"
      "ds_read_b128 v[96:99], v7 offset:8192\n"
      "v_exp_f32_e64 v139, v139\n"
      "v_max_f32_dpp v175, v174, v174 row_ror:8 row_mask:0xf bank_mask:0xf\n"
      "v_mfma_f32_16x16x32_bf16 v[206:209], a[248:251], v[178:181], v[206:209]\n"
      "ds_write_b64 v4, v[190:191] offset:18432\n"
      "v_max_f32_dpp v175, v175, v175 row_ror:4 row_mask:0xf bank_mask:0xf\n"
      "v_mfma_f32_16x16x32_bf16 v[182:185], v[232:235], v[100:103], 0\n"
      "ds_read_b128 v[190:193], v7 offset:12288\n"
      "v_cvt_pk_bf16_f32 v89, v140, v141\n"
      "v_mfma_f32_16x16x32_bf16 v[182:185], v[236:239], v[210:213], v[182:185]\n"
      "v_max_f32_dpp v175, v175, v175 row_ror:2 row_mask:0xf bank_mask:0xf\n"
      "v_cvt_pk_bf16_f32 v88, v138, v139\n"
      "v_mfma_f32_16x16x32_bf16 v[182:185], v[240:243], v[178:181], v[182:185]\n"
      "s_waitcnt lgkmcnt(3)\n"
      "v_exp_f32 v199, v199\n"
      "v_mfma_f32_16x16x32_bf16 v[124:127], a[228:231], v[160:163], v[124:127]\n"
      "v_exp_f32 v200, v200\n"
      "v_mfma_f32_16x16x32_bf16 v[124:127], a[232:235], v[164:167], v[124:127]\n"
      "s_waitcnt lgkmcnt(2)\n"
      "v_cvt_pk_bf16_f32 v168, v168, v169\n"
      "v_max_f32_dpp v175, v175, v175 row_ror:1 row_mask:0xf bank_mask:0xf\n"
      "v_mfma_f32_16x16x32_bf16 v[124:127], a[236:239], v[134:137], v[124:127]\n"
      "ds_read_b128 v[100:103], v7 offset:16384\n"
      "v_max_f32_dpp v175, v175, v175 row_bcast:15 row_mask:0xa bank_mask:0xf\n"
      "v_mfma_f32_16x16x32_bf16 v[206:209], a[252:255], v[160:163], v[206:209]\n"
      "s_nop 0\n"
      "v_max_f32_dpp v175, v175, v175 row_bcast:31 row_mask:0xc bank_mask:0xf\n"
      "v_cvt_pk_bf16_f32 v169, v170, v171\n"
      "v_mfma_f32_16x16x32_bf16 v[206:209], v[224:227], v[164:167], v[206:209]\n"
      "buffer_store_dwordx4 v[96:99], v22, s[24:27], s85 offen sc1\n"
      "v_mfma_f32_16x16x32_bf16 v[206:209], v[228:231], v[134:137], v[206:209]\n"
      "v_readlane_b32 s72, v175, 63\n"
      "v_mul_f32 v97, v125, v125\n"
      "v_mfma_f32_16x16x32_bf16 v[182:185], v[244:247], v[160:163], v[182:185]\n"
      "ds_write_b128 v3, v[148:151] offset:18688\n"
      "v_subrev_f32 v174, s72, v174\n"
      "v_fma_f32 v96, v124, v124, s52\n"
      "v_pk_fma_f32 v[202:203], v[220:221], v[220:221], v[202:203]\n"
      "v_pk_fma_f32 v[202:203], v[104:105], v[104:105], v[202:203]\n"
      "v_exp_f32_e64 v174, v174\n"
      "v_pk_fma_f32 v[96:97], v[126:127], v[126:127], v[96:97]\n"
      "v_pk_fma_f32 v[128:129], v[158:159], v[158:159], v[128:129]\n"
      "v_cvt_pk_bf16_f32 v170, v198, v199\n"
      "v_mfma_f32_16x16x32_bf16 v[182:185], v[248:251], v[164:167], v[182:185]\n"
      "s_waitcnt lgkmcnt(1)\n"
      "v_cvt_pk_bf16_f32 v171, v200, v201\n"
      "v_cvt_pk_bf16_f32 v174, v174, v174\n"
      "v_mfma_f32_16x16x32_bf16 v[182:185], v[252:255], v[134:137], v[182:185]\n"
      "ds_write_b16 v13, v174\n"
      "v_mfma_f32_16x16x32_bf16 v[172:175], v[32:35], v[168:171], 0\n"
      "buffer_store_dwordx4 v[190:193], v23, s[24:27], s85 offen sc1\n"
      "v_pk_fma_f32 v[202:203], v[106:107], v[106:107], v[202:203]\n"
      "v_mfma_f32_16x16x32_bf16 v[172:175], v[32:35], v[88:91], v[172:175]\n"
      "v_add_f32 v202, v202, v203\n"
      "v_add_f32 v128, v128, v129\n"
      "v_mfma_f32_16x16x32_bf16 v[164:167], v[214:217], v[168:171], 0\n"
      "ds_read_b128 v[210:213], v14\n"
      "v_cvt_pk_bf16_f32 v108, v108, v109\n"
      "v_mfma_f32_16x16x32_bf16 v[164:167], v[76:79], v[88:91], v[164:167]\n"
      "ds_write_b32 v31, v152 offset:5632\n"
      "v_mfma_f32_16x16x32_bf16 v[150:153], v[130:133], v[168:171], 0\n"
      "v_rcp_f32_e64 v172, v172\n"
      "v_mfma_f32_16x16x32_bf16 v[150:153], v[80:83], v[88:91], v[150:153]\n"
      "ds_read_b128 v[174:177], v14 offset:64\n"
      "v_permlane16_swap_b32_e64 v128, v202\n"
      "v_cvt_pk_bf16_f32 v109, v110, v111\n"
      "v_add_f32_e64 v128, v128, v202\n"
      "v_pk_fma_f32 v[96:97], v[206:207], v[206:207], v[96:97]\n"
      "v_pk_mul_f32 v[164:165], v[164:165], v[172:173] op_sel_hi:[1,0]\n"
      "v_pk_fma_f32 v[96:97], v[208:209], v[208:209], v[96:97]\n"
      "v_pk_fma_f32 v[96:97], v[182:183], v[182:183], v[96:97]\n"
      "v_pk_fma_f32 v[96:97], v[184:185], v[184:185], v[96:97]\n"
      "v_mfma_f32_16x16x32_bf16 v[112:115], v[194:197], v[168:171], 0\n"
      "ds_write_b64 v4, v[108:109] offset:18688\n"
      "v_cvt_pk_bf16_f32 v164, v164, v165\n"
      "v_add_f32_e64 v96, v96, v97\n"
      "v_pk_mul_f32 v[150:151], v[150:151], v[172:173] op_sel_hi:[1,0]\n"
      "v_pk_mul_f32 v[152:153], v[152:153], v[172:173] op_sel_hi:[1,0]\n"
      "v_pk_mul_f32 v[166:167], v[166:167], v[172:173] op_sel_hi:[1,0]\n"
      "v_mfma_f32_16x16x32_bf16 v[112:115], v[84:87], v[88:91], v[112:115]\n"
      "s_waitcnt lgkmcnt(2)\n"
      "v_cvt_pk_bf16_f32 v165, v166, v167\n"
      "v_mov_b32 v202, v96\n"
      "v_cvt_pk_bf16_f32 v167, v152, v153\n"
      "v_cvt_pk_bf16_f32 v166, v150, v151\n"
      "s_add_u32 s43, s43, 1\n"
      "ds_write_b128 v3, v[164:167] offset:18944\n"
      "v_permlane16_swap_b32 v96, v202\n"
      "v_add_f32_e64 v96, v96, v202\n"
      "v_pk_mul_f32 v[112:113], v[112:113], v[172:173] op_sel_hi:[1,0]\n"
      "v_pk_mul_f32 v[114:115], v[114:115], v[172:173] op_sel_hi:[1,0]\n"
      "v_permlane32_swap_b32 v128, v96\n"
      "v_add_f32 v128, v128, v96\n"
      "v_mfma_f32_16x16x32_bf16 v[150:153], v[210:213], v[32:35], 0\n"
      "ds_write_b32 v31, v128 offset:1024\n"
      "v_cvt_pk_bf16_f32 v112, v112, v113\n"
      "v_cvt_pk_bf16_f32 v113, v114, v115\n"
      "s_waitcnt lgkmcnt(2)\n"
      "v_mfma_f32_16x16x32_bf16 v[150:153], v[174:177], v[32:35], v[150:153]\n"
      "ds_write_b64 v4, v[112:113] offset:18944\n"
      "v_add_u32 v10, 2336, v10\n"
      "v_add_u32 v11, 2336, v11\n"
      "buffer_store_dwordx4 v[100:103], v25, s[24:27], s85 offen sc1\n"
      "s_cmp_ge_u32 s95, 0x40000000\n"
      "s_cselect_b64 vcc, -1, s[56:57]\n"
      "s_waitcnt lgkmcnt(0)\n"
      "s_barrier\n"
      "s_cbranch_vccnz L_disp0_%=\n"
      "L_max0_%=:\n"
      "v_mfma_f32_16x16x32_bf16 v[164:167], v[210:213], v[214:217], 0\n"
      "ds_read_b128 v[100:103], v0\n"
      "v_mfma_f32_16x16x32_bf16 v[164:167], v[174:177], v[76:79], v[164:167]\n"
      "ds_read_b128 v[190:193], v9 offset:1024\n"
      "v_readlane_b32 s85, v37, s42\n"
      "v_mfma_f32_16x16x32_bf16 v[170:173], v[210:213], v[130:133], 0\n"
      "s_add_u32 m0, s64, 18432\n"
      "ds_read_b128 v[160:163], v1 offset:12288\n"
      "v_mfma_f32_16x16x32_bf16 v[170:173], v[174:177], v[80:83], v[170:173]\n"
      "buffer_load_dwordx4 v16, s[16:19], s45 offen  lds\n"
      "v_mfma_f32_16x16x32_bf16 v[128:131], v[210:213], v[194:197], 0\n"
      "ds_read_b128 v[112:115], v1\n"
      "ds_read_b128 v[202:205], v0 offset:6144\n"
      "v_mfma_f32_16x16x32_bf16 v[128:131], v[174:177], v[84:87], v[128:131]\n"
      "ds_read_b128 v[96:99], v0 offset:12288\n"
      "s_waitcnt lgkmcnt(5)\n"
      "v_mfma_f32_16x16x32_bf16 v[116:119], a[0:3], v[100:103], 0\n"
      "ds_read_b128 v[134:137], v1 offset:6144\n"
      "s_add_u32 m0, s64, 22528\n"
      "v_mfma_f32_16x16x32_bf16 v[178:181], a[24:27], v[100:103], 0\n"
      "s_waitcnt lgkmcnt(5)\n"
      "v_add_f32 v192, v192, v193\n"
      "v_add_f32 v190, v190, v191\n"
      "v_mfma_f32_16x16x32_bf16 v[152:155], a[48:51], v[100:103], 0\n"
      "buffer_load_dwordx4 v17, s[16:19], s45 offen  lds\n"
      "v_mfma_f32_16x16x32_bf16 v[198:201], a[72:75], v[100:103], 0\n"
      "s_add_u32 m0, s64, 26624\n"
      "v_mov_b32_dpp v150, v150 quad_perm:[0,0,0,0] row_mask:0xf bank_mask:0xf\n"
      "v_add_f32 v190, v190, v192\n"
      "v_mfma_f32_16x16x32_bf16 v[166:169], a[96:99], v[100:103], 0\n"
      "v_rcp_f32 v150, v150\n"
      "v_mfma_f32_16x16x32_bf16 v[146:149], a[120:123], v[100:103], 0\n"
      "s_waitcnt lgkmcnt(2)\n"
      "v_rsq_f32 v190, v190\n"
      "v_mfma_f32_16x16x32_bf16 v[116:119], a[4:7], v[112:115], v[116:119]\n"
      "ds_read_b128 v[108:111], v0 offset:2048\n"
      "v_mul_f32 v190, 0x415db3d7, v190\n"
      "v_mfma_f32_16x16x32_bf16 v[116:119], a[8:11], v[202:205], v[116:119]\n"
      "v_readlane_b32 s84, v37, s43\n"
      "v_mov_b32_e64 v192, v190\n"
      "v_mfma_f32_16x16x32_bf16 v[178:181], a[28:31], v[112:115], v[178:181]\n"
      "buffer_load_dwordx4 v18, s[16:19], s45 offen  lds\n"
      "v_mfma_f32_16x16x32_bf16 v[178:181], a[32:35], v[202:205], v[178:181]\n"
      "v_permlane32_swap_b32 v190, v192\n"
      "v_mfma_f32_16x16x32_bf16 v[152:155], a[52:55], v[112:115], v[152:155]\n"
      "v_cndmask_b32 v164, v164, v170, s[58:59]\n"
      "v_mov_b32 v191, v190\n"
      "v_mfma_f32_16x16x32_bf16 v[152:155], a[56:59], v[202:205], v[152:155]\n"
      "ds_read_b128 v[138:141], v9 offset:5632\n"
      "v_mul_f32_e64 v206, v192, v206\n"
      "v_mfma_f32_16x16x32_bf16 v[198:201], a[76:79], v[112:115], v[198:201]\n"
      "v_cndmask_b32 v164, v164, v128, s[60:61]\n"
      "v_mul_f32 v183, v192, v183\n"
      "v_mfma_f32_16x16x32_bf16 v[198:201], a[80:83], v[202:205], v[198:201]\n"
      "s_add_u32 m0, s64, 30720\n"
      "v_permlane16_swap_b32 v190, v191\n"
      "v_mfma_f32_16x16x32_bf16 v[166:169], a[100:103], v[112:115], v[166:169]\n"
      "v_mul_f32 v208, v192, v208\n"
      "v_mul_f32 v164, v150, v164\n"
      "v_mfma_f32_16x16x32_bf16 v[166:169], a[104:107], v[202:205], v[166:169]\n"
      "v_mul_f32 v159, v190, v159\n"
      "v_mul_f32 v188, v190, v188\n"
      "v_mfma_f32_16x16x32_bf16 v[146:149], a[124:127], v[112:115], v[146:149]\n"
      "s_waitcnt lgkmcnt(1)\n"
      "v_mul_f32 v187, v190, v187\n"
      "v_mul_f32_e64 v189, v190, v189\n"
      "v_mfma_f32_16x16x32_bf16 v[146:149], a[128:131], v[202:205], v[146:149]\n"
      "buffer_load_dwordx4 v19, s[16:19], s45 offen  lds\n"
      "v_mfma_f32_16x16x32_bf16 v[116:119], a[12:15], v[134:137], v[116:119]\n"
      "v_readlane_b32 s86, v38, s43\n"
      "v_mul_f32_e64 v145, v190, v145\n"
      "v_mfma_f32_16x16x32_bf16 v[116:119], a[16:19], v[96:99], v[116:119]\n"
      "v_mul_f32 v143, v190, v143\n"
      "v_mul_f32 v142, v190, v142\n"
      "v_mfma_f32_16x16x32_bf16 v[116:119], a[20:23], v[160:163], v[116:119]\n"
      "v_mul_f32 v156, v190, v156\n"
      "v_mul_f32 v157, v190, v157\n"
      "v_mfma_f32_16x16x32_bf16 v[178:181], a[36:39], v[134:137], v[178:181]\n"
      "s_mov_b32 m0, s7\n"
      "v_mul_f32 v186, v190, v186\n"
      "v_cvt_pk_bf16_f32 v186, v186, v187\n"
      "v_mfma_f32_16x16x32_bf16 v[178:181], a[40:43], v[96:99], v[178:181]\n"
      "s_nop 0\n"
      "v_fma_f32 v78, v116, v116, s51\n"
      "v_mul_f32 v122, v191, v122\n"
      "v_mfma_f32_16x16x32_bf16 v[178:181], a[44:47], v[160:163], v[178:181]\n"
      "ds_read_b128 v[174:177], v0 offset:8192\n"
      "v_cvt_pk_bf16_f32 v187, v188, v189\n"
      "v_mfma_f32_16x16x32_bf16 v[152:155], a[60:63], v[134:137], v[152:155]\n"
      "v_mul_f32 v144, v190, v144\n"
      "v_mul_f32 v158, v190, v158\n"
      "v_mfma_f32_16x16x32_bf16 v[152:155], a[64:67], v[96:99], v[152:155]\n"
      "buffer_load_dwordx4 v15, s[0:3], s4 offen  lds\n"
      "v_mfma_f32_16x16x32_bf16 v[152:155], a[68:71], v[160:163], v[152:155]\n"
      "s_or_b32 s95, s84, s85\n"
      "v_mul_f32 v125, v192, v125\n"
      "v_cvt_pk_bf16_f32 v188, v142, v143\n"
      "v_mfma_f32_16x16x32_bf16 v[198:201], a[84:87], v[134:137], v[198:201]\n"
      "v_cvt_pk_bf16_f32 v189, v144, v145\n"
      "v_mul_f32 v124, v192, v124\n"
      "v_mfma_f32_16x16x32_bf16 v[198:201], a[88:91], v[96:99], v[198:201]\n"
      "v_mul_f32 v104, v191, v104\n"
      "v_fmac_f32_e64 v78, v117, v117\n"
      "v_mfma_f32_16x16x32_bf16 v[198:201], a[92:95], v[160:163], v[198:201]\n"
      "ds_write_b128 v5, v[186:189]\n"
      "v_pk_mul_f32 v[106:107], v[106:107], v[190:191] op_sel:[0,1]\n"
      "v_mfma_f32_16x16x32_bf16 v[166:169], a[108:111], v[134:137], v[166:169]\n"
      "buffer_load_dwordx4 v[210:213], v36, s[28:31], s47 offen\n"
      "v_mfma_f32_16x16x32_bf16 v[166:169], a[112:115], v[96:99], v[166:169]\n"
      "v_mul_f32 v120, v191, v120\n"
      "v_mul_f32 v105, v191, v105\n"
      "v_mfma_f32_16x16x32_bf16 v[166:169], a[116:119], v[160:163], v[166:169]\n"
      "s_waitcnt lgkmcnt(1)\n"
      "v_mul_f32 v218, v191, v218\n"
      "v_or3_b32 v138, v138, v139, v140\n"
      "v_mfma_f32_16x16x32_bf16 v[146:149], a[132:135], v[134:137], v[146:149]\n"
      "v_fma_f32 v76, v198, v198, s51\n"
      "v_mul_f32 v182, v192, v182\n"
      "v_mfma_f32_16x16x32_bf16 v[146:149], a[136:139], v[96:99], v[146:149]\n"
      "v_fmac_f32 v76, v199, v199\n"
      "v_fmac_f32_e64 v78, v118, v118\n"
      "v_mfma_f32_16x16x32_bf16 v[146:149], a[140:143], v[160:163], v[146:149]\n"
      "ds_read_b128 v[142:145], v1 offset:2048\n"
      "v_fmac_f32 v76, v200, v200\n"
      "v_mfma_f32_16x16x32_bf16 v[214:217], v[100:103], a[144:147], 0\n"
      "v_fmac_f32 v76, v201, v201\n"
      "v_mul_f32_e64 v185, v192, v185\n"
      "v_mfma_f32_16x16x32_bf16 v[214:217], v[112:115], a[148:151], v[214:217]\n"
      "v_cvt_pk_bf16_f32 v104, v104, v105\n"
      "v_cvt_pk_bf16_f32 v124, v124, v125\n"
      "v_mfma_f32_16x16x32_bf16 v[214:217], v[202:205], a[152:155], v[214:217]\n"
      "v_fmac_f32 v76, v166, v166\n"
      "v_fmac_f32 v76, v167, v167\n"
      "v_mfma_f32_16x16x32_bf16 v[214:217], v[134:137], a[156:159], v[214:217]\n"
      "ds_read_b128 v[186:189], v1 offset:8192\n"
      "v_bitop3_b32 v138, v138, v141, s33 bitop3:0xa8\n"
      "v_mfma_f32_16x16x32_bf16 v[214:217], v[96:99], a[160:163], v[214:217]\n"
      "v_fmac_f32 v78, v119, v119\n"
      "v_mul_f32 v123, v191, v123\n"
      "v_mfma_f32_16x16x32_bf16 v[214:217], v[160:163], a[164:167], v[214:217]\n"
      "ds_read_b128 v[170:173], v0 offset:14336\n"
      "v_fmac_f32 v76, v168, v168\n"
      "v_mfma_f32_16x16x32_bf16 v[130:133], v[100:103], a[168:171], 0\n"
      "v_cvt_pk_bf16_f32 v156, v156, v157\n"
      "v_mul_f32 v221, v191, v221\n"
      "v_mfma_f32_16x16x32_bf16 v[130:133], v[112:115], a[172:175], v[130:133]\n"
      "v_cvt_pk_bf16_f32 v157, v158, v159\n"
      "v_fmac_f32 v76, v169, v169\n"
      "v_mfma_f32_16x16x32_bf16 v[130:133], v[202:205], a[176:179], v[130:133]\n"
      "v_fmac_f32 v76, v146, v146\n"
      "v_mul_f32 v127, v192, v127\n"
      "v_mfma_f32_16x16x32_bf16 v[130:133], v[134:137], a[180:183], v[130:133]\n"
      "v_mul_f32 v184, v192, v184\n"
      "v_fmac_f32 v78, v178, v178\n"
      "v_mfma_f32_16x16x32_bf16 v[130:133], v[96:99], a[184:187], v[130:133]\n"
      "v_fmac_f32 v76, v147, v147\n"
      "v_mul_f32 v219, v191, v219\n"
      "v_mfma_f32_16x16x32_bf16 v[130:133], v[160:163], a[188:191], v[130:133]\n"
      "v_fmac_f32 v78, v179, v179\n"
      "v_mul_f32_e64 v126, v192, v126\n"
      "v_mfma_f32_16x16x32_bf16 v[194:197], v[100:103], a[192:195], 0\n"
      "ds_read_b128 v[100:103], v1 offset:4096\n"
      "v_fmac_f32 v78, v180, v180\n"
      "v_mfma_f32_16x16x32_bf16 v[194:197], v[112:115], a[196:199], v[194:197]\n"
      "ds_read_b128 v[112:115], v1 offset:14336\n"
      "v_mul_f32 v220, v191, v220\n"
      "v_mfma_f32_16x16x32_bf16 v[194:197], v[202:205], a[200:203], v[194:197]\n"
      "s_or_b32 s95, s95, s48\n"
      "v_fmac_f32 v78, v181, v181\n"
      "v_cvt_pk_bf16_f32 v214, v214, v215\n"
      "v_mfma_f32_16x16x32_bf16 v[194:197], v[134:137], a[204:207], v[194:197]\n"
      "v_fmac_f32_e64 v78, v152, v152\n"
      "v_cvt_pk_bf16_f32 v215, v216, v217\n"
      "v_mfma_f32_16x16x32_bf16 v[194:197], v[96:99], a[208:211], v[194:197]\n"
      "buffer_load_dword v93, v29, s[28:31], s47 offen\n"
      "v_mfma_f32_16x16x32_bf16 v[194:197], v[160:163], a[212:215], v[194:197]\n"
      "v_cvt_pk_bf16_f32 v164, v164, v164\n"
      "v_mul_f32 v121, v191, v121\n"
      "v_mfma_f32_16x16x32_bf16 v[202:205], a[0:3], v[108:111], 0\n"
      "s_waitcnt lgkmcnt(1)\n"
      "v_cvt_pk_bf16_f32 v120, v120, v121\n"
      "v_fmac_f32_e64 v76, v148, v148\n"
      "v_mfma_f32_16x16x32_bf16 v[202:205], a[4:7], v[142:145], v[202:205]\n"
      "ds_write_b16 v12, v164 offset:16\n"
      "v_mfma_f32_16x16x32_bf16 v[202:205], a[8:11], v[174:177], v[202:205]\n"
      "v_cvt_pk_bf16_f32 v182, v182, v183\n"
      "v_fmac_f32_e64 v78, v153, v153\n"
      "v_mfma_f32_16x16x32_bf16 v[202:205], a[12:15], v[186:189], v[202:205]\n"
      "ds_write_b64 v6, v[156:157]\n"
      "v_mfma_f32_16x16x32_bf16 v[202:205], a[16:19], v[170:173], v[202:205]\n"
      "s_waitcnt lgkmcnt(2)\n"
      "v_fmac_f32 v76, v149, v149\n"
      "v_fmac_f32_e64 v78, v154, v154\n"
      "v_mfma_f32_16x16x32_bf16 v[202:205], a[20:23], v[112:115], v[202:205]\n"
      "v_mul_f32 v207, v192, v207\n"
      "v_cvt_pk_bf16_f32 v183, v184, v185\n"
      "v_mfma_f32_16x16x32_bf16 v[134:137], a[24:27], v[108:111], 0\n"
      "v_fmac_f32 v78, v155, v155\n"
      "v_cvt_pk_bf16_f32 v121, v122, v123\n"
      "v_mfma_f32_16x16x32_bf16 v[134:137], a[28:31], v[142:145], v[134:137]\n"
      "v_cvt_pk_bf16_f32 v194, v194, v195\n"
      "v_cvt_pk_bf16_f32 v125, v126, v127\n"
      "v_mfma_f32_16x16x32_bf16 v[134:137], a[32:35], v[174:177], v[134:137]\n"
      "v_permlane16_swap_b32 v78, v76\n"
      "v_mfma_f32_16x16x32_bf16 v[134:137], a[36:39], v[186:189], v[134:137]\n"
      "v_cvt_pk_bf16_f32 v130, v130, v131\n"
      "v_add_f32 v78, v78, v76\n"
      "v_mfma_f32_16x16x32_bf16 v[134:137], a[40:43], v[170:173], v[134:137]\n"
      "v_mov_b32 v76, v78\n"
      "v_cvt_pk_bf16_f32 v122, v218, v219\n"
      "v_mfma_f32_16x16x32_bf16 v[134:137], a[44:47], v[112:115], v[134:137]\n"
      "v_permlane32_swap_b32 v78, v76\n"
      "v_mfma_f32_16x16x32_bf16 v[96:99], a[48:51], v[108:111], 0\n"
      "v_cvt_pk_bf16_f32 v126, v206, v207\n"
      "v_cvt_pk_bf16_f32 v123, v220, v221\n"
      "v_mfma_f32_16x16x32_bf16 v[96:99], a[52:55], v[142:145], v[96:99]\n"
      "v_fma_f32 v156, v202, v202, s51\n"
      "v_add_f32 v78, v78, v76\n"
      "v_mfma_f32_16x16x32_bf16 v[96:99], a[56:59], v[174:177], v[96:99]\n"
      "v_rsq_f32 v78, v78\n"
      "v_mfma_f32_16x16x32_bf16 v[96:99], a[60:63], v[186:189], v[96:99]\n"
      "v_fmac_f32 v156, v203, v203\n"
      "v_mov_b32 v76, v78\n"
      "v_mfma_f32_16x16x32_bf16 v[96:99], a[64:67], v[170:173], v[96:99]\n"
      "v_mul_f32_e64 v209, v192, v209\n"
      "v_cvt_pk_bf16_f32 v131, v132, v133\n"
      "v_mfma_f32_16x16x32_bf16 v[96:99], a[68:71], v[112:115], v[96:99]\n"
      "v_permlane16_swap_b32_e64 v78, v76\n"
      "v_mfma_f32_16x16x32_bf16 v[162:165], a[72:75], v[108:111], 0\n"
      "ds_write_b128 v5, v[120:123] offset:6144\n"
      "v_mfma_f32_16x16x32_bf16 v[162:165], a[76:79], v[142:145], v[162:165]\n"
      "v_fmac_f32 v156, v204, v204\n"
      "v_fmac_f32 v156, v205, v205\n"
      "v_mfma_f32_16x16x32_bf16 v[162:165], a[80:83], v[174:177], v[162:165]\n"
      "ds_read_b128 v[158:161], v1 offset:10240\n"
      "v_mul_f32 v147, v76, v147\n"
      "v_mfma_f32_16x16x32_bf16 v[162:165], a[84:87], v[186:189], v[162:165]\n"
      "v_fmac_f32 v156, v134, v134\n"
      "v_cvt_pk_bf16_f32 v105, v106, v107\n"
      "v_mfma_f32_16x16x32_bf16 v[162:165], a[88:91], v[170:173], v[162:165]\n"
      "ds_read_b64 v[128:129], v10 offset:24\n"
      "v_mul_f32 v78, s50, v78\n"
      "v_mfma_f32_16x16x32_bf16 v[162:165], a[92:95], v[112:115], v[162:165]\n"
      "v_mul_f32 v179, v78, v179\n"
      "v_mul_f32_e64 v168, v76, v168\n"
      "v_mfma_f32_16x16x32_bf16 v[218:221], a[96:99], v[108:111], 0\n"
      "v_mul_f32_e64 v155, v78, v155\n"
      "v_cmp_ne_u32 s[56:57], 0, v138\n"
      "v_mfma_f32_16x16x32_bf16 v[218:221], a[100:103], v[142:145], v[218:221]\n"
      "ds_read_b128 v[120:123], v0 offset:10240\n"
      "v_mul_f32 v118, v78, v118\n"
      "v_mfma_f32_16x16x32_bf16 v[218:221], a[104:107], v[174:177], v[218:221]\n"
      "v_mul_f32 v169, v76, v169\n"
      "v_fma_f32 v157, v162, v162, s51\n"
      "v_mfma_f32_16x16x32_bf16 v[218:221], a[108:111], v[186:189], v[218:221]\n"
      "v_fmac_f32 v156, v135, v135\n"
      "v_fmac_f32 v156, v136, v136\n"
      "v_mfma_f32_16x16x32_bf16 v[218:221], a[112:115], v[170:173], v[218:221]\n"
      "v_fmac_f32 v156, v137, v137\n"
      "v_fmac_f32 v157, v163, v163\n"
      "v_mfma_f32_16x16x32_bf16 v[218:221], a[116:119], v[112:115], v[218:221]\n"
      "v_cvt_pk_bf16_f32 v195, v196, v197\n"
      "v_cvt_pk_bf16_f32 v127, v208, v209\n"
      "v_mfma_f32_16x16x32_bf16 v[138:141], a[120:123], v[108:111], 0\n"
      "ds_read_b128 v[190:193], v0 offset:4096\n"
      "v_mul_f32_e64 v152, v78, v152\n"
      "v_mfma_f32_16x16x32_bf16 v[138:141], a[124:127], v[142:145], v[138:141]\n"
      "ds_write_b64 v6, v[182:183] offset:12288\n"
      "v_mfma_f32_16x16x32_bf16 v[138:141], a[128:131], v[174:177], v[138:141]\n"
      "v_mul_f32 v153, v78, v153\n"
      "v_mul_f32 v148, v76, v148\n"
      "v_mfma_f32_16x16x32_bf16 v[138:141], a[132:135], v[186:189], v[138:141]\n"
      "v_mul_f32 v116, v78, v116\n"
      "v_fmac_f32 v156, v96, v96\n"
      "v_mfma_f32_16x16x32_bf16 v[138:141], a[136:139], v[170:173], v[138:141]\n"
      "v_fmac_f32 v156, v97, v97\n"
      "v_fmac_f32 v157, v164, v164\n"
      "v_mfma_f32_16x16x32_bf16 v[138:141], a[140:143], v[112:115], v[138:141]\n"
      "s_cmp_ge_u32 s95, 0x40000000\n"
      "s_cselect_b32 s94, -1, -2\n"
      "v_mul_f32 v154, v78, v154\n"
      "v_mfma_f32_16x16x32_bf16 v[182:185], v[108:111], a[144:147], 0\n"
      "v_mul_f32 v200, v76, v200\n"
      "v_mul_f32 v166, v76, v166\n"
      "v_mfma_f32_16x16x32_bf16 v[182:185], v[142:145], a[148:151], v[182:185]\n"
      "v_mul_f32 v117, v78, v117\n"
      "v_fmac_f32 v157, v165, v165\n"
      "v_mfma_f32_16x16x32_bf16 v[182:185], v[174:177], a[152:155], v[182:185]\n"
      "v_fmac_f32 v157, v218, v218\n"
      "v_mul_f32_e64 v167, v76, v167\n"
      "v_mfma_f32_16x16x32_bf16 v[182:185], v[186:189], a[156:159], v[182:185]\n"
      "ds_write_b64 v6, v[104:105] offset:6144\n"
      "v_mfma_f32_16x16x32_bf16 v[182:185], v[170:173], a[160:163], v[182:185]\n"
      "v_cvt_pk_bf16_f32 v116, v116, v117\n"
      "v_fmac_f32_e64 v157, v219, v219\n"
      "v_mfma_f32_16x16x32_bf16 v[182:185], v[112:115], a[164:167], v[182:185]\n"
      "v_fmac_f32 v157, v220, v220\n"
      "v_fmac_f32 v157, v221, v221\n"
      "v_mfma_f32_16x16x32_bf16 v[206:209], v[108:111], a[168:171], 0\n"
      "v_mul_f32 v198, v76, v198\n"
      "v_mul_f32 v199, v76, v199\n"
      "v_mfma_f32_16x16x32_bf16 v[206:209], v[142:145], a[172:175], v[206:209]\n"
      "v_mul_f32 v201, v76, v201\n"
      "v_mul_f32 v146, v76, v146\n"
      "v_mfma_f32_16x16x32_bf16 v[206:209], v[174:177], a[176:179], v[206:209]\n"
      "v_fmac_f32 v156, v98, v98\n"
      "v_fmac_f32 v157, v138, v138\n"
      "v_mfma_f32_16x16x32_bf16 v[206:209], v[186:189], a[180:183], v[206:209]\n"
      "v_mul_f32 v180, v78, v180\n"
      "v_fmac_f32 v157, v139, v139\n"
      "v_mfma_f32_16x16x32_bf16 v[206:209], v[170:173], a[184:187], v[206:209]\n"
      "ds_read_b128 v[104:107], v1 offset:16384\n"
      "v_mul_f32_e64 v149, v76, v149\n"
      "v_mfma_f32_16x16x32_bf16 v[206:209], v[112:115], a[188:191], v[206:209]\n"
      "ds_write_b128 v5, v[124:127] offset:12288\n"
      "v_mfma_f32_16x16x32_bf16 v[124:127], v[108:111], a[192:195], 0\n"
      "ds_read_b128 v[108:111], v0 offset:16384\n"
      "v_cvt_pk_bf16_f32 v152, v152, v153\n"
      "v_mfma_f32_16x16x32_bf16 v[124:127], v[142:145], a[196:199], v[124:127]\n"
      "v_fmac_f32 v157, v140, v140\n"
      "v_fmac_f32 v156, v99, v99\n"
      "v_mfma_f32_16x16x32_bf16 v[124:127], v[174:177], a[200:203], v[124:127]\n"
      "v_fmac_f32_e64 v157, v141, v141\n"
      "v_cvt_pk_bf16_f32 v146, v146, v147\n"
      "v_mfma_f32_16x16x32_bf16 v[124:127], v[186:189], a[204:207], v[124:127]\n"
      "v_cvt_pk_bf16_f32 v133, v208, v209\n"
      "v_mul_f32 v119, v78, v119\n"
      "v_mfma_f32_16x16x32_bf16 v[124:127], v[170:173], a[208:211], v[124:127]\n"
      "v_permlane16_swap_b32 v156, v157\n"
      "v_mfma_f32_16x16x32_bf16 v[124:127], v[112:115], a[212:215], v[124:127]\n"
      "s_waitcnt lgkmcnt(2)\n"
      "v_cvt_pk_bf16_f32 v132, v206, v207\n"
      "v_add_f32 v156, v156, v157\n"
      "v_mfma_f32_16x16x32_bf16 v[174:177], a[0:3], v[190:193], 0\n"
      "v_cvt_pk_bf16_f32 v117, v118, v119\n"
      "v_cvt_pk_bf16_f32 v217, v184, v185\n"
      "v_mfma_f32_16x16x32_bf16 v[174:177], a[4:7], v[100:103], v[174:177]\n"
      "v_mul_f32 v181, v78, v181\n"
      "v_mov_b32 v157, v156\n"
      "v_mfma_f32_16x16x32_bf16 v[174:177], a[8:11], v[120:123], v[174:177]\n"
      "v_cvt_pk_bf16_f32 v198, v198, v199\n"
      "v_mul_f32 v178, v78, v178\n"
      "v_mfma_f32_16x16x32_bf16 v[174:177], a[12:15], v[158:161], v[174:177]\n"
      "s_waitcnt lgkmcnt(0)\n"
      "v_permlane32_swap_b32 v156, v157\n"
      "v_add_f32 v156, v156, v157\n"
      "v_mfma_f32_16x16x32_bf16 v[174:177], a[16:19], v[108:111], v[174:177]\n"
      "v_rsq_f32 v156, v156\n"
      "v_mfma_f32_16x16x32_bf16 v[174:177], a[20:23], v[104:107], v[174:177]\n"
      "v_cvt_pk_bf16_f32 v196, v124, v125\n"
      "v_mov_b32 v157, v156\n"
      "v_mfma_f32_16x16x32_bf16 v[112:115], a[24:27], v[190:193], 0\n"
      "s_cmp_gt_u32 s42, s44\n"
      "s_cselect_b32 s95, s94, s95\n"
      "v_cvt_pk_bf16_f32 v118, v178, v179\n"
      "v_mfma_f32_16x16x32_bf16 v[112:115], a[28:31], v[100:103], v[112:115]\n"
      "v_permlane16_swap_b32 v156, v157\n"
      "v_mfma_f32_16x16x32_bf16 v[112:115], a[32:35], v[120:123], v[112:115]\n"
      "v_fma_f32 v90, v174, v174, s51\n"
      "v_fmac_f32 v90, v175, v175\n"
      "v_mfma_f32_16x16x32_bf16 v[112:115], a[36:39], v[158:161], v[112:115]\n"
      "v_fmac_f32 v90, v176, v176\n"
      "v_cvt_pk_bf16_f32 v119, v180, v181\n"
      "v_mfma_f32_16x16x32_bf16 v[112:115], a[40:43], v[108:111], v[112:115]\n"
      "v_mul_f32 v156, s50, v156\n"
      "v_cvt_pk_bf16_f32 v199, v200, v201\n"
      "v_mfma_f32_16x16x32_bf16 v[112:115], a[44:47], v[104:107], v[112:115]\n"
      "v_cvt_pk_bf16_f32 v153, v154, v155\n"
      "v_fmac_f32 v90, v177, v177\n"
      "v_mfma_f32_16x16x32_bf16 v[206:209], a[48:51], v[190:193], 0\n"
      "v_mul_f32 v98, v156, v98\n"
      "v_mul_f32 v165, v157, v165\n"
      "v_mfma_f32_16x16x32_bf16 v[206:209], a[52:55], v[100:103], v[206:209]\n"
      "v_mul_f32 v204, v156, v204\n"
      "v_mul_f32 v205, v156, v205\n"
      "v_mfma_f32_16x16x32_bf16 v[206:209], a[56:59], v[120:123], v[206:209]\n"
      "v_mul_f32 v164, v157, v164\n"
      "v_mul_f32 v162, v157, v162\n"
      "v_mfma_f32_16x16x32_bf16 v[206:209], a[60:63], v[158:161], v[206:209]\n"
      "v_fmac_f32 v90, v112, v112\n"
      "v_mul_f32 v141, v157, v141\n"
      "v_mfma_f32_16x16x32_bf16 v[206:209], a[64:67], v[108:111], v[206:209]\n"
      "v_mul_f32 v97, v156, v97\n"
      "v_mul_f32 v163, v157, v163\n"
      "v_mfma_f32_16x16x32_bf16 v[206:209], a[68:71], v[104:107], v[206:209]\n"
      "v_mul_f32 v140, v157, v140\n"
      "v_fmac_f32_e64 v90, v113, v113\n"
      "v_mfma_f32_16x16x32_bf16 v[170:173], a[72:75], v[190:193], 0\n"
      "v_cvt_pk_bf16_f32 v162, v162, v163\n"
      "v_cvt_pk_bf16_f32 v200, v166, v167\n"
      "v_mfma_f32_16x16x32_bf16 v[170:173], a[76:79], v[100:103], v[170:173]\n"
      "v_mul_f32 v137, v156, v137\n"
      "v_mul_f32 v202, v156, v202\n"
      "v_mfma_f32_16x16x32_bf16 v[170:173], a[80:83], v[120:123], v[170:173]\n"
      "v_mul_f32 v221, v157, v221\n"
      "v_mul_f32 v99, v156, v99\n"
      "v_mfma_f32_16x16x32_bf16 v[170:173], a[84:87], v[158:161], v[170:173]\n"
      "v_mul_f32 v219, v157, v219\n"
      "v_add_u32 v12, 32, v12\n"
      "v_mfma_f32_16x16x32_bf16 v[170:173], a[88:91], v[108:111], v[170:173]\n"
      "v_mul_f32 v139, v157, v139\n"
      "v_fmac_f32 v90, v114, v114\n"
      "v_mfma_f32_16x16x32_bf16 v[170:173], a[92:95], v[104:107], v[170:173]\n"
      "v_cvt_pk_bf16_f32 v163, v164, v165\n"
      "v_cvt_pk_bf16_f32 v201, v168, v169\n"
      "v_mfma_f32_16x16x32_bf16 v[166:169], a[96:99], v[190:193], 0\n"
      "v_mul_f32 v203, v156, v203\n"
      "v_mul_f32 v220, v157, v220\n"
      "v_mfma_f32_16x16x32_bf16 v[166:169], a[100:103], v[100:103], v[166:169]\n"
      "v_mul_f32 v135, v156, v135\n"
      "v_mul_f32 v138, v157, v138\n"
      "v_mfma_f32_16x16x32_bf16 v[166:169], a[104:107], v[120:123], v[166:169]\n"
      "ds_read_u16 v78, v11\n"
      "v_fmac_f32_e64 v90, v115, v115\n"
      "v_mfma_f32_16x16x32_bf16 v[166:169], a[108:111], v[158:161], v[166:169]\n"
      "v_cvt_pk_bf16_f32 v197, v126, v127\n"
      "v_cvt_pk_bf16_f32 v202, v202, v203\n"
      "v_mfma_f32_16x16x32_bf16 v[166:169], a[112:115], v[108:111], v[166:169]\n"
      "v_fma_f32 v185, v170, v170, s51\n"
      "v_fmac_f32 v90, v206, v206\n"
      "v_mfma_f32_16x16x32_bf16 v[166:169], a[116:119], v[104:107], v[166:169]\n"
      "v_mul_f32 v136, v156, v136\n"
      "v_fmac_f32_e64 v90, v207, v207\n"
      "v_mfma_f32_16x16x32_bf16 v[124:127], a[120:123], v[190:193], 0\n"
      "v_fmac_f32 v90, v208, v208\n"
      "v_fmac_f32 v185, v171, v171\n"
      "v_mfma_f32_16x16x32_bf16 v[124:127], a[124:127], v[100:103], v[124:127]\n"
      "v_cvt_pk_bf16_f32 v165, v220, v221\n"
      "v_fmac_f32 v185, v172, v172\n"
      "v_mfma_f32_16x16x32_bf16 v[124:127], a[128:131], v[120:123], v[124:127]\n"
      "v_mul_f32 v134, v156, v134\n"
      "v_mul_f32 v218, v157, v218\n"
      "v_mfma_f32_16x16x32_bf16 v[124:127], a[132:135], v[158:161], v[124:127]\n"
      "s_add_u32 s43, s43, 1\n"
      "v_fmac_f32 v185, v173, v173\n"
      "v_cvt_pk_bf16_f32 v147, v148, v149\n"
      "v_mfma_f32_16x16x32_bf16 v[124:127], a[136:139], v[108:111], v[124:127]\n"
      "v_mul_f32 v96, v156, v96\n"
      "v_cvt_pk_bf16_f32 v138, v138, v139\n"
      "v_mfma_f32_16x16x32_bf16 v[124:127], a[140:143], v[104:107], v[124:127]\n"
      "v_fmac_f32 v185, v166, v166\n"
      "v_cvt_pk_bf16_f32 v203, v204, v205\n"
      "v_mfma_f32_16x16x32_bf16 v[220:223], v[190:193], a[144:147], 0\n"
      "v_fmac_f32 v185, v167, v167\n"
      "v_fmac_f32_e64 v185, v168, v168\n"
      "v_mfma_f32_16x16x32_bf16 v[220:223], v[100:103], a[148:151], v[220:223]\n"
      "v_cvt_pk_bf16_f32 v204, v134, v135\n"
      "v_cvt_pk_bf16_f32 v205, v136, v137\n"
      "v_mfma_f32_16x16x32_bf16 v[220:223], v[120:123], a[152:155], v[220:223]\n"
      "v_fmac_f32 v185, v169, v169\n"
      "v_cvt_pk_bf16_f32 v164, v218, v219\n"
      "v_mfma_f32_16x16x32_bf16 v[220:223], v[158:161], a[156:159], v[220:223]\n"
      "v_fmac_f32 v90, v209, v209\n"
      "v_fmac_f32 v185, v124, v124\n"
      "v_mfma_f32_16x16x32_bf16 v[220:223], v[108:111], a[160:163], v[220:223]\n"
      "v_fmac_f32 v185, v125, v125\n"
      "v_fmac_f32_e64 v185, v126, v126\n"
      "v_mfma_f32_16x16x32_bf16 v[220:223], v[104:107], a[164:167], v[220:223]\n"
      "ds_read_b128 v[178:181], v2 offset:21504\n"
      "v_fmac_f32 v185, v127, v127\n"
      "v_mfma_f32_16x16x32_bf16 v[154:157], v[190:193], a[168:171], 0\n"
      "s_nop 0\n"
      "v_permlane16_swap_b32_e64 v90, v185\n"
      "v_mfma_f32_16x16x32_bf16 v[154:157], v[100:103], a[172:175], v[154:157]\n"
      "v_add_f32 v90, v90, v185\n"
      "v_mov_b32 v185, v90\n"
      "v_mfma_f32_16x16x32_bf16 v[154:157], v[120:123], a[176:179], v[154:157]\n"
      "s_nop 0\n"
      "v_permlane32_swap_b32 v90, v185\n"
      "v_mfma_f32_16x16x32_bf16 v[154:157], v[158:161], a[180:183], v[154:157]\n"
      "v_add_f32 v90, v90, v185\n"
      "v_cvt_pk_bf16_f32 v76, v220, v221\n"
      "v_mfma_f32_16x16x32_bf16 v[154:157], v[108:111], a[184:187], v[154:157]\n"
      "v_rsq_f32 v90, v90\n"
      "v_mfma_f32_16x16x32_bf16 v[154:157], v[104:107], a[188:191], v[154:157]\n"
      "ds_read_b128 v[218:221], v2 offset:18432\n"
      "v_mov_b32_e64 v185, v90\n"
      "v_mfma_f32_16x16x32_bf16 v[134:137], v[190:193], a[192:195], 0\n"
      "ds_read_b128 v[190:193], v2 offset:24576\n"
      "v_cvt_pk_bf16_f32 v96, v96, v97\n"
      "v_mfma_f32_16x16x32_bf16 v[134:137], v[100:103], a[196:199], v[134:137]\n"
      "v_permlane16_swap_b32_e64 v90, v185\n"
      "v_mfma_f32_16x16x32_bf16 v[134:137], v[120:123], a[200:203], v[134:137]\n"
      "v_mul_f32 v168, v185, v168\n"
      "v_mul_f32 v170, v185, v170\n"
      "v_mfma_f32_16x16x32_bf16 v[134:137], v[158:161], a[204:207], v[134:137]\n"
      "v_mul_f32 v124, v185, v124\n"
      "v_mul_f32 v173, v185, v173\n"
      "v_mfma_f32_16x16x32_bf16 v[134:137], v[108:111], a[208:211], v[134:137]\n"
      "v_mul_f32 v125, v185, v125\n"
      "v_mul_f32 v169, v185, v169\n"
      "v_mfma_f32_16x16x32_bf16 v[134:137], v[104:107], a[212:215], v[134:137]\n"
      "s_waitcnt lgkmcnt(1)\n"
      "v_mul_f32 v171, v185, v171\n"
      "v_cvt_pk_bf16_f32 v139, v140, v141\n"
      "v_mfma_f32_16x16x32_bf16 v[186:189], a[216:219], v[218:221], 0\n"
      "ds_read_b128 v[100:103], v10 offset:32\n"
      "v_cvt_pk_bf16_f32 v124, v124, v125\n"
      "v_mfma_f32_16x16x32_bf16 v[186:189], a[220:223], v[178:181], v[186:189]\n"
      "s_waitcnt lgkmcnt(1)\n"
      "v_mul_f32 v167, v185, v167\n"
      "v_mul_f32 v172, v185, v172\n"
      "v_mfma_f32_16x16x32_bf16 v[186:189], a[224:227], v[190:193], v[186:189]\n"
      "v_mul_f32 v90, s50, v90\n"
      "v_mul_f32 v208, v90, v208\n"
      "v_mfma_f32_16x16x32_bf16 v[142:145], a[240:243], v[218:221], 0\n"
      "ds_read_b128 v[148:151], v2 offset:27648\n"
      "v_mul_f32 v127, v185, v127\n"
      "v_mfma_f32_16x16x32_bf16 v[142:145], a[244:247], v[178:181], v[142:145]\n"
      "v_cvt_pk_bf16_f32 v170, v170, v171\n"
      "v_cvt_pk_bf16_f32 v171, v172, v173\n"
      "v_mfma_f32_16x16x32_bf16 v[142:145], a[248:251], v[190:193], v[142:145]\n"
      "v_cvt_pk_bf16_f32 v81, v156, v157\n"
      "v_mul_f32 v206, v90, v206\n"
      "v_mfma_f32_16x16x32_bf16 v[156:159], v[232:235], v[218:221], 0\n"
      "v_mul_f32 v166, v185, v166\n"
      "v_cvt_pk_bf16_f32 v172, v166, v167\n"
      "v_mfma_f32_16x16x32_bf16 v[156:159], v[236:239], v[178:181], v[156:159]\n"
      "ds_read_b128 v[218:221], v2 offset:33792\n"
      "v_cvt_pk_bf16_f32 v84, v134, v135\n"
      "v_mfma_f32_16x16x32_bf16 v[156:159], v[240:243], v[190:193], v[156:159]\n"
      "ds_read_u16 v82, v11 offset:32\n"
      "v_cvt_pk_bf16_f32 v80, v154, v155\n"
      "v_mfma_f32_16x16x32_bf16 v[190:193], v[198:201], v[116:119], v[40:43]\n"
      "v_cvt_pk_bf16_f32 v173, v168, v169\n"
      "v_mul_f32_e64 v126, v185, v126\n"
      "v_mfma_f32_16x16x32_bf16 v[104:107], v[162:165], v[116:119], v[44:47]\n"
      "s_waitcnt lgkmcnt(2)\n"
      "v_mul_f32 v174, v90, v174\n"
      "v_mul_f32_e64 v175, v90, v175\n"
      "v_mfma_f32_16x16x32_bf16 v[166:169], v[170:173], v[116:119], v[48:51]\n"
      "ds_read_b128 v[108:111], v2 offset:30720\n"
      "v_cvt_pk_bf16_f32 v125, v126, v127\n"
      "v_mfma_f32_16x16x32_bf16 v[120:123], v[100:103], v[116:119], v[24:27]\n"
      "v_mul_f32_e64 v207, v90, v207\n"
      "v_cvt_pk_bf16_f32 v77, v222, v223\n"
      "v_mfma_f32_16x16x16_bf16 v[190:193], v[146:147], v[152:153], v[190:193]\n"
      "v_mul_f32 v209, v90, v209\n"
      "v_mul_f32 v176, v90, v176\n"
      "v_mul_f32_e64 v177, v90, v177\n"
      "v_mfma_f32_16x16x16_bf16 v[104:107], v[138:139], v[152:153], v[104:107]\n"
      "v_cvt_pk_bf16_f32 v216, v182, v183\n"
      "v_cvt_pk_bf16_f32 v174, v174, v175\n"
      "v_cvt_pk_bf16_f32 v85, v136, v137\n"
      "v_mfma_f32_16x16x16_bf16 v[166:169], v[124:125], v[152:153], v[166:169]\n"
      "v_cvt_pk_bf16_f32 v206, v206, v207\n"
      "v_cvt_pk_bf16_f32 v97, v98, v99\n"
      "v_max3_f32 v119, v190, v191, v192\n"
      "v_mfma_f32_16x16x16_bf16 v[120:123], v[128:129], v[152:153], v[120:123]\n"
      "v_max3_f32 v119, v119, v106, v107\n"
      "v_cvt_pk_bf16_f32 v175, v176, v177\n"
      "v_max3_f32 v183, v193, v104, v105\n"
      "v_mfma_f32_16x16x32_bf16 v[186:189], a[228:231], v[148:151], v[186:189]\n"
      "s_waitcnt lgkmcnt(0)\n"
      "v_max3_f32 v183, v183, v166, v167\n"
      "v_max3_f32 v119, v119, v168, v169\n"
      "v_mfma_f32_16x16x32_bf16 v[186:189], a[232:235], v[108:111], v[186:189]\n"
      "v_max3_f32 v119, v119, v183, v120\n"
      "v_mov_b32 v183, v119\n"
      "v_mfma_f32_16x16x32_bf16 v[186:189], a[236:239], v[218:221], v[186:189]\n"
      "s_nop 0\n"
      "v_permlane16_swap_b32 v119, v183\n"
      "v_mfma_f32_16x16x32_bf16 v[142:145], a[252:255], v[148:151], v[142:145]\n"
      "v_max_f32 v119, v119, v183\n"
      "v_mov_b32 v183, v119\n"
      "v_mfma_f32_16x16x32_bf16 v[142:145], v[224:227], v[108:111], v[142:145]\n"
      "s_nop 0\n"
      "v_permlane32_swap_b32 v119, v183\n"
      "v_mfma_f32_16x16x32_bf16 v[142:145], v[228:231], v[218:221], v[142:145]\n"
      "v_max_f32 v119, v119, v183\n"
      "v_sub_f32 v191, v191, v119\n"
      "v_mfma_f32_16x16x32_bf16 v[156:159], v[244:247], v[148:151], v[156:159]\n"
      "ds_read_b128 v[182:185], v2 offset:18688\n"
      "v_sub_f32 v190, v190, v119\n"
      "v_mfma_f32_16x16x32_bf16 v[156:159], v[248:251], v[108:111], v[156:159]\n"
      "v_exp_f32 v190, v190\n"
      "v_mfma_f32_16x16x32_bf16 v[156:159], v[252:255], v[218:221], v[156:159]\n"
      "v_exp_f32_e64 v191, v191\n"
      "v_mfma_f32_16x16x32_bf16 v[134:137], v[198:201], v[202:205], v[52:55]\n"
      "ds_read_b128 v[108:111], v2 offset:24832\n"
      "v_sub_f32 v120, v120, v119\n"
      "s_waitcnt vmcnt(1)\n"
      "v_or3_b32 v210, v210, v211, v212\n"
      "v_exp_f32_e64 v120, v120\n"
      "v_pk_mul_f32 v[112:113], v[112:113], v[90:91] op_sel_hi:[1,0]\n"
      "v_pk_add_f32 v[192:193], v[192:193], v[118:119] op_sel:[0,1] neg_lo:[0,1] neg_hi:[0,1]\n"
      "v_pk_add_f32 v[106:107], v[106:107], v[118:119] op_sel:[0,1] neg_lo:[0,1] neg_hi:[0,1]\n"
      "v_pk_mul_f32 v[114:115], v[114:115], v[90:91] op_sel_hi:[1,0]\n"
      "v_pk_add_f32 v[166:167], v[166:167], v[118:119] op_sel:[0,1] neg_lo:[0,1] neg_hi:[0,1]\n"
      "v_pk_add_f32 v[104:105], v[104:105], v[118:119] op_sel:[0,1] neg_lo:[0,1] neg_hi:[0,1]\n"
      "v_pk_add_f32 v[168:169], v[168:169], v[118:119] op_sel:[0,1] neg_lo:[0,1] neg_hi:[0,1]\n"
      "v_mfma_f32_16x16x32_bf16 v[148:151], v[162:165], v[202:205], v[56:59]\n"
      "ds_read_b128 v[116:119], v2 offset:21760\n"
      "v_cvt_pk_bf16_f32 v177, v114, v115\n"
      "v_mfma_f32_16x16x32_bf16 v[152:155], v[170:173], v[202:205], v[60:63]\n"
      "v_exp_f32 v168, v168\n"
      "v_mfma_f32_16x16x32_bf16 v[178:181], v[100:103], v[202:205], v[26:29]\n"
      "s_waitcnt lgkmcnt(2)\n"
      "v_exp_f32_e64 v166, v166\n"
      "v_mfma_f32_16x16x16_bf16 v[134:137], v[146:147], v[96:97], v[134:137]\n"
      "v_cvt_pk_bf16_f32 v90, v120, 0\n"
      "v_exp_f32 v107, v107\n"
      "v_mfma_f32_16x16x16_bf16 v[148:151], v[138:139], v[96:97], v[148:151]\n"
      "v_exp_f32 v192, v192\n"
      "v_cvt_pk_bf16_f32 v207, v208, v209\n"
      "v_mfma_f32_16x16x16_bf16 v[152:155], v[124:125], v[96:97], v[152:155]\n"
      "v_exp_f32_e64 v105, v105\n"
      "v_cvt_pk_bf16_f32 v190, v190, v191\n"
      "v_mfma_f32_16x16x16_bf16 v[178:181], v[128:129], v[96:97], v[178:181]\n"
      "v_cvt_pk_bf16_f32 v176, v112, v113\n"
      "v_exp_f32 v193, v193\n"
      "v_mfma_f32_16x16x32_bf16 v[120:123], a[216:219], v[182:185], 0\n"
      "s_waitcnt lgkmcnt(0)\n"
      "v_cvt_pk_bf16_f32 v191, v192, v193\n"
      "v_max3_f32 v203, v134, v135, v136\n"
      "v_mfma_f32_16x16x32_bf16 v[120:123], a[220:223], v[116:119], v[120:123]\n"
      "v_exp_f32_e64 v104, v104\n"
      "v_mfma_f32_16x16x32_bf16 v[120:123], a[224:227], v[108:111], v[120:123]\n"
      "v_exp_f32_e64 v106, v106\n"
      "v_mfma_f32_16x16x32_bf16 v[218:221], a[240:243], v[182:185], 0\n"
      "v_cvt_pk_bf16_f32 v192, v104, v105\n"
      "v_cvt_pk_bf16_f32 v193, v106, v107\n"
      "v_mfma_f32_16x16x32_bf16 v[218:221], a[244:247], v[116:119], v[218:221]\n"
      "v_exp_f32_e64 v169, v169\n"
      "v_mfma_f32_16x16x32_bf16 v[218:221], a[248:251], v[108:111], v[218:221]\n"
      "v_exp_f32_e64 v167, v167\n"
      "v_mfma_f32_16x16x32_bf16 v[104:107], v[232:235], v[182:185], 0\n"
      "v_max3_f32 v112, v137, v148, v149\n"
      "v_max3_f32 v112, v112, v152, v153\n"
      "v_mfma_f32_16x16x32_bf16 v[104:107], v[236:239], v[116:119], v[104:107]\n"
      "v_cvt_pk_bf16_f32 v88, v166, v167\n"
      "v_max3_f32 v203, v203, v150, v151\n"
      "v_mfma_f32_16x16x32_bf16 v[104:107], v[240:243], v[108:111], v[104:107]\n"
      "v_max3_f32 v203, v203, v154, v155\n"
      "v_cvt_pk_bf16_f32 v89, v168, v169\n"
      "v_mfma_f32_16x16x32_bf16 v[182:185], v[32:35], v[190:193], 0\n"
      "ds_read_u16 v86, v11 offset:64\n"
      "v_fma_f32 v140, v186, v186, s52\n"
      "v_mfma_f32_16x16x32_bf16 v[182:185], v[32:35], v[88:91], v[182:185]\n"
      "v_max3_f32 v203, v203, v112, v178\n"
      "v_mov_b32 v108, v203\n"
      "v_mfma_f32_16x16x32_bf16 v[112:115], v[214:217], v[190:193], 0\n"
      "s_nop 0\n"
      "v_permlane16_swap_b32 v203, v108\n"
      "v_mfma_f32_16x16x32_bf16 v[112:115], v[76:79], v[88:91], v[112:115]\n"
      "ds_read_b128 v[116:119], v2 offset:27904\n"
      "s_waitcnt vmcnt(0)\n"
      "v_or3_b32 v210, v210, v213, v93\n"
      "v_mfma_f32_16x16x32_bf16 v[96:99], v[130:133], v[190:193], 0\n"
      "ds_write_b32 v31, v210 offset:4608\n"
      "v_mfma_f32_16x16x32_bf16 v[96:99], v[80:83], v[88:91], v[96:99]\n"
      "v_max_f32 v203, v203, v108\n"
      "v_mov_b32 v108, v203\n"
      "v_mfma_f32_16x16x32_bf16 v[210:213], v[194:197], v[190:193], 0\n"
      "s_waitcnt lgkmcnt(2)\n"
      "v_rcp_f32 v182, v182\n"
      "v_mfma_f32_16x16x32_bf16 v[210:213], v[84:87], v[88:91], v[210:213]\n"
      "ds_read_b128 v[190:193], v2 offset:30976\n"
      "v_permlane32_swap_b32 v203, v108\n"
      "v_max_f32 v203, v203, v108\n"
      "v_mul_f32_e64 v141, v187, v187\n"
      "v_pk_add_f32 v[134:135], v[134:135], v[202:203] op_sel:[0,1] neg_lo:[0,1] neg_hi:[0,1]\n"
      "v_pk_mul_f32 v[96:97], v[96:97], v[182:183] op_sel_hi:[1,0]\n"
      "v_pk_mul_f32 v[114:115], v[114:115], v[182:183] op_sel_hi:[1,0]\n"
      "v_pk_mul_f32 v[112:113], v[112:113], v[182:183] op_sel_hi:[1,0]\n"
      "v_sub_f32_e64 v178, v178, v203\n"
      "v_pk_mul_f32 v[98:99], v[98:99], v[182:183] op_sel_hi:[1,0]\n"
      "v_cvt_pk_bf16_f32 v112, v112, v113\n"
      "v_pk_mul_f32 v[210:211], v[210:211], v[182:183] op_sel_hi:[1,0]\n"
      "v_pk_fma_f32 v[140:141], v[188:189], v[188:189], v[140:141]\n"
      "v_cvt_pk_bf16_f32 v113, v114, v115\n"
      "v_pk_add_f32 v[148:149], v[148:149], v[202:203] op_sel:[0,1] neg_lo:[0,1] neg_hi:[0,1]\n"
      "v_pk_add_f32 v[136:137], v[136:137], v[202:203] op_sel:[0,1] neg_lo:[0,1] neg_hi:[0,1]\n"
      "v_pk_mul_f32 v[212:213], v[212:213], v[182:183] op_sel_hi:[1,0]\n"
      "v_pk_add_f32 v[152:153], v[152:153], v[202:203] op_sel:[0,1] neg_lo:[0,1] neg_hi:[0,1]\n"
      "v_pk_add_f32 v[150:151], v[150:151], v[202:203] op_sel:[0,1] neg_lo:[0,1] neg_hi:[0,1]\n"
      "v_cvt_pk_bf16_f32 v115, v98, v99\n"
      "v_pk_add_f32 v[154:155], v[154:155], v[202:203] op_sel:[0,1] neg_lo:[0,1] neg_hi:[0,1]\n"
      "v_pk_fma_f32 v[140:141], v[142:143], v[142:143], v[140:141]\n"
      "v_mfma_f32_16x16x32_bf16 v[202:205], v[198:201], v[174:177], v[64:67]\n"
      "v_cvt_pk_bf16_f32 v210, v210, v211\n"
      "v_cvt_pk_bf16_f32 v114, v96, v97\n"
      "v_cvt_pk_bf16_f32 v211, v212, v213\n"
      "v_mfma_f32_16x16x32_bf16 v[108:111], v[162:165], v[174:177], v[68:71]\n"
      "v_exp_f32 v136, v136\n"
      "v_mfma_f32_16x16x32_bf16 v[96:99], v[170:173], v[174:177], v[72:75]\n"
      "s_waitcnt lgkmcnt(0)\n"
      "v_exp_f32_e64 v155, v155\n"
      "v_mfma_f32_16x16x32_bf16 v[180:183], v[100:103], v[174:177], v[28:31]\n"
      "ds_read_b128 v[174:177], v2 offset:34048\n"
      "v_exp_f32 v154, v154\n"
      "v_mfma_f32_16x16x16_bf16 v[202:205], v[146:147], v[206:207], v[202:205]\n"
      "v_exp_f32 v153, v153\n"
      "v_exp_f32 v134, v134\n"
      "v_mfma_f32_16x16x16_bf16 v[108:111], v[138:139], v[206:207], v[108:111]\n"
      "v_exp_f32 v150, v150\n"
      "v_cvt_pk_bf16_f32 v93, v154, v155\n"
      "v_mfma_f32_16x16x16_bf16 v[96:99], v[124:125], v[206:207], v[96:99]\n"
      "v_exp_f32 v135, v135\n"
      "v_exp_f32 v152, v152\n"
      "v_mfma_f32_16x16x16_bf16 v[180:183], v[128:129], v[206:207], v[180:183]\n"
      "v_cvt_pk_bf16_f32 v134, v134, v135\n"
      "v_cvt_pk_bf16_f32 v92, v152, v153\n"
      "v_max3_f32 v155, v202, v203, v204\n"
      "v_mfma_f32_16x16x32_bf16 v[120:123], a[228:231], v[116:119], v[120:123]\n"
      "v_exp_f32 v178, v178\n"
      "v_mfma_f32_16x16x32_bf16 v[120:123], a[232:235], v[190:193], v[120:123]\n"
      "s_waitcnt lgkmcnt(0)\n"
      "v_exp_f32 v151, v151\n"
      "v_mfma_f32_16x16x32_bf16 v[120:123], a[236:239], v[174:177], v[120:123]\n"
      "v_exp_f32 v149, v149\n"
      "v_mfma_f32_16x16x32_bf16 v[218:221], a[252:255], v[116:119], v[218:221]\n"
      "v_exp_f32_e64 v148, v148\n"
      "v_mfma_f32_16x16x32_bf16 v[218:221], v[224:227], v[190:193], v[218:221]\n"
      "v_exp_f32_e64 v137, v137\n"
      "v_mfma_f32_16x16x32_bf16 v[218:221], v[228:231], v[174:177], v[218:221]\n"
      "v_cvt_pk_bf16_f32 v135, v136, v137\n"
      "v_cvt_pk_bf16_f32 v94, v178, 0\n"
      "v_mfma_f32_16x16x32_bf16 v[104:107], v[244:247], v[116:119], v[104:107]\n"
      "v_max3_f32 v155, v155, v110, v111\n"
      "v_cvt_pk_bf16_f32 v136, v148, v149\n"
      "v_mfma_f32_16x16x32_bf16 v[104:107], v[248:251], v[190:193], v[104:107]\n"
      "v_cvt_pk_bf16_f32 v137, v150, v151\n"
      "v_max3_f32 v178, v205, v108, v109\n"
      "v_mfma_f32_16x16x32_bf16 v[104:107], v[252:255], v[174:177], v[104:107]\n"
      "ds_read_b128 v[206:209], v10\n"
      "v_max3_f32 v178, v178, v96, v97\n"
      "v_mfma_f32_16x16x32_bf16 v[182:185], v[32:35], v[134:137], 0\n"
      "ds_read_b128 v[174:177], v7 offset:18432\n"
      "v_max3_f32 v155, v155, v98, v99\n"
      "v_mfma_f32_16x16x32_bf16 v[182:185], v[32:35], v[92:95], v[182:185]\n"
      "v_max3_f32 v155, v155, v178, v180\n"
      "v_mov_b32_e64 v154, v155\n"
      "v_mfma_f32_16x16x32_bf16 v[166:169], v[214:217], v[134:137], 0\n"
      "s_nop 0\n"
      "v_permlane16_swap_b32 v155, v154\n"
      "v_mfma_f32_16x16x32_bf16 v[166:169], v[76:79], v[92:95], v[166:169]\n"
      "ds_read_b64 v[178:179], v10 offset:16\n"
      "v_max_f32_e64 v155, v155, v154\n"
      "v_mfma_f32_16x16x32_bf16 v[190:193], v[130:133], v[134:137], 0\n"
      "ds_write_b128 v3, v[112:115]\n"
      "v_mfma_f32_16x16x32_bf16 v[190:193], v[80:83], v[92:95], v[190:193]\n"
      "ds_read_b128 v[116:119], v2 offset:22016\n"
      "v_mov_b32 v154, v155\n"
      "v_mfma_f32_16x16x32_bf16 v[112:115], v[194:197], v[134:137], 0\n"
      "s_nop 0\n"
      "v_permlane32_swap_b32_e64 v155, v154\n"
      "v_mfma_f32_16x16x32_bf16 v[112:115], v[84:87], v[92:95], v[112:115]\n"
      "s_waitcnt lgkmcnt(3)\n"
      "v_rcp_f32 v182, v182\n"
      "v_mfma_f32_16x16x32_bf16 v[150:153], v[206:209], v[198:201], v[30:33]\n"
      "ds_read_b128 v[198:201], v2 offset:18944\n"
      "v_max_f32_e64 v155, v155, v154\n"
      "v_mfma_f32_16x16x32_bf16 v[134:137], v[206:209], v[162:165], v[30:33]\n"
      "buffer_store_dwordx4 v[174:177], v20, s[24:27], s46 offen sc1\n"
      "v_pk_add_f32 v[96:97], v[96:97], v[154:155] op_sel:[0,1] neg_lo:[0,1] neg_hi:[0,1]\n"
      "v_pk_add_f32 v[204:205], v[204:205], v[154:155] op_sel:[0,1] neg_lo:[0,1] neg_hi:[0,1]\n"
      "v_pk_add_f32 v[108:109], v[108:109], v[154:155] op_sel:[0,1] neg_lo:[0,1] neg_hi:[0,1]\n"
      "v_pk_mul_f32 v[168:169], v[168:169], v[182:183] op_sel_hi:[1,0]\n"
      "v_pk_add_f32 v[202:203], v[202:203], v[154:155] op_sel:[0,1] neg_lo:[0,1] neg_hi:[0,1]\n"
      "v_pk_mul_f32 v[166:167], v[166:167], v[182:183] op_sel_hi:[1,0]\n"
      "v_pk_add_f32 v[98:99], v[98:99], v[154:155] op_sel:[0,1] neg_lo:[0,1] neg_hi:[0,1]\n"
      "v_pk_mul_f32 v[114:115], v[114:115], v[182:183] op_sel_hi:[1,0]\n"
      "v_pk_mul_f32 v[192:193], v[192:193], v[182:183] op_sel_hi:[1,0]\n"
      "v_pk_fma_f32 v[140:141], v[144:145], v[144:145], v[140:141]\n"
      "v_pk_add_f32 v[110:111], v[110:111], v[154:155] op_sel:[0,1] neg_lo:[0,1] neg_hi:[0,1]\n"
      "v_pk_mul_f32 v[190:191], v[190:191], v[182:183] op_sel_hi:[1,0]\n"
      "v_mfma_f32_16x16x32_bf16 v[174:177], v[206:209], v[170:173], v[30:33]\n"
      "s_waitcnt lgkmcnt(0)\n"
      "v_exp_f32 v96, v96\n"
      "v_mfma_f32_16x16x32_bf16 v[170:173], v[206:209], v[100:103], v[30:33]\n"
      "ds_read_b128 v[206:209], v7 offset:22528\n"
      "v_sub_f32_e64 v180, v180, v155\n"
      "v_mfma_f32_16x16x16_bf16 v[150:153], v[178:179], v[146:147], v[150:153]\n"
      "ds_read_b128 v[162:165], v2 offset:25088\n"
      "v_fma_f32 v146, v120, v120, s52\n"
      "v_mfma_f32_16x16x16_bf16 v[134:137], v[178:179], v[138:139], v[134:137]\n"
      "v_exp_f32 v202, v202\n"
      "v_cvt_pk_bf16_f32 v166, v166, v167\n"
      "v_mfma_f32_16x16x16_bf16 v[174:177], v[178:179], v[124:125], v[174:177]\n"
      "v_exp_f32 v97, v97\n"
      "v_exp_f32 v180, v180\n"
      "v_exp_f32 v203, v203\n"
      "v_mfma_f32_16x16x16_bf16 v[170:173], v[178:179], v[128:129], v[170:173]\n"
      "v_mul_f32 v147, v121, v121\n"
      "v_cvt_pk_bf16_f32 v202, v202, v203\n"
      "v_exp_f32 v109, v109\n"
      "v_mfma_f32_16x16x32_bf16 v[124:127], a[216:219], v[198:201], 0\n"
      "ds_read_b128 v[176:179], v2 offset:28160\n"
      "v_cndmask_b32 v150, v150, v134, s[58:59]\n"
      "v_exp_f32 v205, v205\n"
      "v_mfma_f32_16x16x32_bf16 v[124:127], a[220:223], v[116:119], v[124:127]\n"
      "s_waitcnt lgkmcnt(1)\n"
      "v_cndmask_b32 v150, v150, v174, s[60:61]\n"
      "v_cndmask_b32 v150, v150, v170, s[62:63]\n"
      "v_mfma_f32_16x16x32_bf16 v[124:127], a[224:227], v[162:165], v[124:127]\n"
      "buffer_store_dwordx4 v[206:209], v21, s[24:27], s46 offen sc1\n"
      "s_nop 1\n"
      "v_mfma_f32_16x16x32_bf16 v[206:209], a[240:243], v[198:201], 0\n"
      "ds_read_b128 v[172:175], v7 offset:26624\n"
      "v_exp_f32 v110, v110\n"
      "v_mfma_f32_16x16x32_bf16 v[206:209], a[244:247], v[116:119], v[206:209]\n"
      "ds_read_b128 v[152:155], v2 offset:31232\n"
      "v_exp_f32_e64 v98, v98\n"
      "v_max_f32_dpp v151, v150, v150 row_ror:8 row_mask:0xf bank_mask:0xf\n"
      "v_cvt_pk_bf16_f32 v167, v168, v169\n"
      "v_pk_fma_f32 v[140:141], v[156:157], v[156:157], v[140:141]\n"
      "v_pk_fma_f32 v[146:147], v[122:123], v[122:123], v[146:147]\n"
      "v_max_f32_dpp v151, v151, v151 row_ror:4 row_mask:0xf bank_mask:0xf\n"
      "v_cvt_pk_bf16_f32 v169, v192, v193\n"
      "v_pk_fma_f32 v[146:147], v[218:219], v[218:219], v[146:147]\n"
      "v_max_f32_dpp v151, v151, v151 row_ror:2 row_mask:0xf bank_mask:0xf\n"
      "v_pk_fma_f32 v[146:147], v[220:221], v[220:221], v[146:147]\n"
      "v_cvt_pk_bf16_f32 v168, v190, v191\n"
      "v_pk_mul_f32 v[112:113], v[112:113], v[182:183] op_sel_hi:[1,0]\n"
      "v_mfma_f32_16x16x32_bf16 v[206:209], a[248:251], v[162:165], v[206:209]\n"
      "ds_read_b128 v[100:103], v2 offset:34304\n"
      "v_exp_f32_e64 v99, v99\n"
      "v_mfma_f32_16x16x32_bf16 v[182:185], v[232:235], v[198:201], 0\n"
      "v_exp_f32 v204, v204\n"
      "v_mfma_f32_16x16x32_bf16 v[182:185], v[236:239], v[116:119], v[182:185]\n"
      "s_waitcnt lgkmcnt(1)\n"
      "v_max_f32_dpp v151, v151, v151 row_ror:1 row_mask:0xf bank_mask:0xf\n"
      "v_exp_f32 v108, v108\n"
      "v_mfma_f32_16x16x32_bf16 v[182:185], v[240:243], v[162:165], v[182:185]\n"
      "buffer_store_dwordx4 v[172:175], v22, s[24:27], s46 offen sc1\n"
      "v_mfma_f32_16x16x32_bf16 v[124:127], a[228:231], v[176:179], v[124:127]\n"
      "v_max_f32_dpp v151, v151, v151 row_bcast:15 row_mask:0xa bank_mask:0xf\n"
      "v_cvt_pk_bf16_f32 v203, v204, v205\n"
      "v_mfma_f32_16x16x32_bf16 v[124:127], a[232:235], v[152:155], v[124:127]\n"
      "s_waitcnt lgkmcnt(0)\n"
      "v_max_f32_dpp v151, v151, v151 row_bcast:31 row_mask:0xc bank_mask:0xf\n"
      "v_exp_f32 v111, v111\n"
      "v_mfma_f32_16x16x32_bf16 v[124:127], a[236:239], v[100:103], v[124:127]\n"
      "v_readlane_b32 s72, v151, 63\n"
      "v_cvt_pk_bf16_f32 v205, v110, v111\n"
      "v_mfma_f32_16x16x32_bf16 v[206:209], a[252:255], v[176:179], v[206:209]\n"
      "ds_read_b128 v[172:175], v7 offset:30720\n"
      "v_cvt_pk_bf16_f32 v204, v108, v109\n"
      "v_subrev_f32 v150, s72, v150\n"
      "v_mfma_f32_16x16x32_bf16 v[206:209], v[224:227], v[152:155], v[206:209]\n"
      "v_cvt_pk_bf16_f32 v90, v180, 0\n"
      "v_fma_f32 v110, v124, v124, s52\n"
      "v_mfma_f32_16x16x32_bf16 v[206:209], v[228:231], v[100:103], v[206:209]\n"
      "ds_write_b128 v3, v[166:169] offset:256\n"
      "v_pk_fma_f32 v[146:147], v[104:105], v[104:105], v[146:147]\n"
      "v_pk_fma_f32 v[140:141], v[158:159], v[158:159], v[140:141]\n"
      "v_mul_f32_e64 v111, v125, v125\n"
      "v_mfma_f32_16x16x32_bf16 v[182:185], v[244:247], v[176:179], v[182:185]\n"
      "v_exp_f32 v150, v150\n"
      "v_mfma_f32_16x16x32_bf16 v[182:185], v[248:251], v[152:155], v[182:185]\n"
      "v_cvt_pk_bf16_f32 v88, v96, v97\n"
      "v_cvt_pk_bf16_f32 v89, v98, v99\n"
      "v_mfma_f32_16x16x32_bf16 v[182:185], v[252:255], v[100:103], v[182:185]\n"
      "s_waitcnt lgkmcnt(1)\n"
      "v_cvt_pk_bf16_f32 v112, v112, v113\n"
      "v_cvt_pk_bf16_f32 v150, v150, v150\n"
      "v_mfma_f32_16x16x32_bf16 v[164:167], v[32:35], v[202:205], 0\n"
      "ds_write_b16 v13, v150\n"
      "v_mfma_f32_16x16x32_bf16 v[164:167], v[32:35], v[88:91], v[164:167]\n"
      "buffer_store_dwordx4 v[172:175], v23, s[24:27], s46 offen sc1\n"
      "v_pk_fma_f32 v[110:111], v[126:127], v[126:127], v[110:111]\n"
      "v_pk_fma_f32 v[110:111], v[206:207], v[206:207], v[110:111]\n"
      "v_pk_fma_f32 v[146:147], v[106:107], v[106:107], v[146:147]\n"
      "v_mfma_f32_16x16x32_bf16 v[150:153], v[214:217], v[202:205], 0\n"
      "ds_write_b64 v4, v[210:211]\n"
      "v_mfma_f32_16x16x32_bf16 v[150:153], v[76:79], v[88:91], v[150:153]\n"
      "s_nop 0\n"
      "v_rcp_f32 v164, v164\n"
      "v_mfma_f32_16x16x32_bf16 v[170:173], v[130:133], v[202:205], 0\n"
      "ds_read_b128 v[166:169], v7 offset:34816\n"
      "v_add_f32_e64 v140, v140, v141\n"
      "v_mfma_f32_16x16x32_bf16 v[170:173], v[80:83], v[88:91], v[170:173]\n"
      "ds_read_b128 v[210:213], v14\n"
      "v_cvt_pk_bf16_f32 v113, v114, v115\n"
      "v_mfma_f32_16x16x32_bf16 v[96:99], v[194:197], v[202:205], 0\n"
      "ds_read_b128 v[174:177], v14 offset:64\n"
      "v_add_f32_e64 v146, v146, v147\n"
      "v_mfma_f32_16x16x32_bf16 v[96:99], v[84:87], v[88:91], v[96:99]\n"
      "ds_write_b64 v4, v[112:113] offset:256\n"
      "v_permlane16_swap_b32_e64 v140, v146\n"
      "v_pk_mul_f32 v[150:151], v[150:151], v[164:165] op_sel_hi:[1,0]\n"
      "v_pk_fma_f32 v[110:111], v[208:209], v[208:209], v[110:111]\n"
      "v_pk_mul_f32 v[152:153], v[152:153], v[164:165] op_sel_hi:[1,0]\n"
      "v_pk_mul_f32 v[170:171], v[170:171], v[164:165] op_sel_hi:[1,0]\n"
      "v_cvt_pk_bf16_f32 v150, v150, v151\n"
      "v_add_f32_e64 v140, v140, v146\n"
      "v_pk_fma_f32 v[110:111], v[182:183], v[182:183], v[110:111]\n"
      "v_cvt_pk_bf16_f32 v151, v152, v153\n"
      "v_pk_mul_f32 v[172:173], v[172:173], v[164:165] op_sel_hi:[1,0]\n"
      "v_pk_fma_f32 v[110:111], v[184:185], v[184:185], v[110:111]\n"
      "v_cvt_pk_bf16_f32 v152, v170, v171\n"
      "v_add_f32 v110, v110, v111\n"
      "v_cvt_pk_bf16_f32 v153, v172, v173\n"
      "v_mov_b32_e64 v146, v110\n"
      "s_add_u32 s42, s42, 1\n"
      "ds_write_b128 v3, v[150:153] offset:512\n"
      "v_permlane16_swap_b32 v110, v146\n"
      "v_add_f32 v110, v110, v146\n"
      "v_pk_mul_f32 v[96:97], v[96:97], v[164:165] op_sel_hi:[1,0]\n"
      "v_pk_mul_f32 v[98:99], v[98:99], v[164:165] op_sel_hi:[1,0]\n"
      "v_permlane32_swap_b32 v140, v110\n"
      "v_add_f32 v140, v140, v110\n"
      "s_and_b32 s86, s86, -4\n"
      "ds_write_b32 v31, v140\n"
      "v_cvt_pk_bf16_f32 v96, v96, v97\n"
      "v_cvt_pk_bf16_f32 v97, v98, v99\n"
      "s_waitcnt lgkmcnt(2)\n"
      "v_mfma_f32_16x16x32_bf16 v[150:153], v[210:213], v[32:35], 0\n"
      "ds_write_b64 v4, v[96:97] offset:512\n"
      "s_cmp_ge_u32 s95, 0x40000000\n"
      "s_cselect_b64 vcc, -1, s[56:57]\n"
      "s_and_b32 s5, s84, s8\n"
      "v_mfma_f32_16x16x32_bf16 v[150:153], v[174:177], v[32:35], v[150:153]\n"
      "buffer_store_dwordx4 v[166:169], v25, s[24:27], s46 offen sc1\n"
      "s_waitcnt lgkmcnt(0)\n"
      "s_barrier\n"
      "s_cbranch_vccz L_max1_%=\n"
      "L_cold_%=:\n"
      "L_disp1_%=:\n"
      "s_cmp_lt_u32 s95, 0x40000000\n"
      "s_cbranch_scc1 L_nofix_11_%=\n"
      "s_cmp_eq_u32 s95, -2\n"
      "s_cbranch_scc1 L_tail1_%=\n"
      "s_nop 2\n"
      "s_cmp_eq_u32 s84, 0x20000000\n"
      "s_cselect_b32 s66, 0, s66\n"
      "s_cselect_b32 s48, 0, s48\n"
      "s_lshr_b32 s92, s84, 30\n"
      "s_or_b32 s92, s92, s66\n"
      "s_cbranch_scc0 L_nf_12_%=\n"
      "s_bitcmp1_b32 s84, 31\n"
      "s_cselect_b32 s75, 100270464, 0\n"
      "s_bitcmp1_b32 s84, 30\n"
      "s_cselect_b32 s76, 196224, 0\n"
      "s_and_b32 s74, s84, 0x3fffffff\n"
      "s_cmp_ge_u32 s84, 0x40000000\n"
      "s_cselect_b32 s74, s74, 0\n"
      "s_cselect_b32 s84, 0, s84\n"
      "s_sub_u32 s74, s74, s66\n"
      "s_add_u32 s66, s66, s74\n"
      "s_sub_u32 s75, s75, s67\n"
      "s_add_u32 s67, s67, s75\n"
      "s_sub_u32 s76, s76, s68\n"
      "s_add_u32 s68, s68, s76\n"
      "v_add_u32 v16, s74, v16\n"
      "v_add_u32 v17, s74, v17\n"
      "v_add_u32 v18, s74, v18\n"
      "v_add_u32 v19, s74, v19\n"
      "v_add_u32 v15, s74, v15\n"
      "s_cmp_eq_u32 s75, 0\n"
      "s_cbranch_scc1 L_ax_13_%=\n"
      "v_bfe_i32 v167, v27, 0, 1\n"
      "v_and_b32 v167, s75, v167\n"
      "v_sub_u32 v16, v16, v167\n"
      "v_bfe_i32 v167, v27, 2, 1\n"
      "v_and_b32 v167, s75, v167\n"
      "v_sub_u32 v17, v17, v167\n"
      "v_bfe_i32 v167, v27, 4, 1\n"
      "v_and_b32 v167, s75, v167\n"
      "v_sub_u32 v18, v18, v167\n"
      "v_bfe_i32 v167, v27, 6, 1\n"
      "v_and_b32 v167, s75, v167\n"
      "v_sub_u32 v19, v19, v167\n"
      "v_bfe_i32 v167, v27, 8, 1\n"
      "v_and_b32 v167, s75, v167\n"
      "v_sub_u32 v15, v15, v167\n"
      "L_ax_13_%=:\n"
      "s_cmp_eq_u32 s76, 0\n"
      "s_cbranch_scc1 L_ax_14_%=\n"
      "v_bfe_i32 v167, v27, 1, 1\n"
      "v_and_b32 v167, s76, v167\n"
      "v_sub_u32 v16, v16, v167\n"
      "v_bfe_i32 v167, v27, 3, 1\n"
      "v_and_b32 v167, s76, v167\n"
      "v_sub_u32 v17, v17, v167\n"
      "v_bfe_i32 v167, v27, 5, 1\n"
      "v_and_b32 v167, s76, v167\n"
      "v_sub_u32 v18, v18, v167\n"
      "v_bfe_i32 v167, v27, 7, 1\n"
      "v_and_b32 v167, s76, v167\n"
      "v_sub_u32 v19, v19, v167\n"
      "v_bfe_i32 v167, v27, 9, 1\n"
      "v_and_b32 v167, s76, v167\n"
      "v_sub_u32 v15, v15, v167\n"
      "L_ax_14_%=:\n"
      "s_or_b32 s48, s66, s69\n"
      "s_cmp_lg_u32 s48, 0\n"
      "s_cselect_b32 s48, 0x40000000, 0\n"
      "L_nf_12_%=:\n"
      "s_lshr_b32 s92, s85, 30\n"
      "s_or_b32 s92, s92, s69\n"
      "s_cbranch_scc0 L_nf_15_%=\n"
      "s_bitcmp1_b32 s85, 31\n"
      "s_cselect_b32 s75, 100270464, 0\n"
      "s_bitcmp1_b32 s85, 30\n"
      "s_cselect_b32 s76, 196224, 0\n"
      "s_and_b32 s74, s85, 0x3fffffff\n"
      "s_cmp_ge_u32 s85, 0x40000000\n"
      "s_cselect_b32 s74, s74, 0\n"
      "s_cselect_b32 s85, 0, s85\n"
      "s_sub_u32 s74, s74, s69\n"
      "s_add_u32 s69, s69, s74\n"
      "s_sub_u32 s75, s75, s70\n"
      "s_add_u32 s70, s70, s75\n"
      "s_sub_u32 s76, s76, s71\n"
      "s_add_u32 s71, s71, s76\n"
      "v_add_u32 v20, s74, v20\n"
      "v_add_u32 v21, s74, v21\n"
      "v_add_u32 v22, s74, v22\n"
      "v_add_u32 v23, s74, v23\n"
      "v_add_u32 v25, s74, v25\n"
      "s_cmp_eq_u32 s75, 0\n"
      "s_cbranch_scc1 L_ax_16_%=\n"
      "v_bfe_i32 v167, v27, 10, 1\n"
      "v_and_b32 v167, s75, v167\n"
      "v_sub_u32 v20, v20, v167\n"
      "v_bfe_i32 v167, v27, 12, 1\n"
      "v_and_b32 v167, s75, v167\n"
      "v_sub_u32 v21, v21, v167\n"
      "v_bfe_i32 v167, v27, 14, 1\n"
      "v_and_b32 v167, s75, v167\n"
      "v_sub_u32 v22, v22, v167\n"
      "v_bfe_i32 v167, v27, 16, 1\n"
      "v_and_b32 v167, s75, v167\n"
      "v_sub_u32 v23, v23, v167\n"
      "v_bfe_i32 v167, v27, 18, 1\n"
      "v_and_b32 v167, s75, v167\n"
      "v_sub_u32 v25, v25, v167\n"
      "L_ax_16_%=:\n"
      "s_cmp_eq_u32 s76, 0\n"
      "s_cbranch_scc1 L_ax_17_%=\n"
      "v_bfe_i32 v167, v27, 11, 1\n"
      "v_and_b32 v167, s76, v167\n"
      "v_sub_u32 v20, v20, v167\n"
      "v_bfe_i32 v167, v27, 13, 1\n"
      "v_and_b32 v167, s76, v167\n"
      "v_sub_u32 v21, v21, v167\n"
      "v_bfe_i32 v167, v27, 15, 1\n"
      "v_and_b32 v167, s76, v167\n"
      "v_sub_u32 v22, v22, v167\n"
      "v_bfe_i32 v167, v27, 17, 1\n"
      "v_and_b32 v167, s76, v167\n"
      "v_sub_u32 v23, v23, v167\n"
      "v_bfe_i32 v167, v27, 19, 1\n"
      "v_and_b32 v167, s76, v167\n"
      "v_sub_u32 v25, v25, v167\n"
      "L_ax_17_%=:\n"
      "s_or_b32 s48, s66, s69\n"
      "s_cmp_lg_u32 s48, 0\n"
      "s_cselect_b32 s48, 0x40000000, 0\n"
      "L_nf_15_%=:\n"
      "s_and_b32 s5, s84, s8\n"
      "s_sub_u32 s92, s42, 1\n"
      "s_cmp_gt_u32 s92, s44\n"
      "s_cbranch_scc1 L_tail1_%=\n"
      "L_nofix_11_%=:\n"
      "s_cmp_lg_u64 s[56:57], 0\n"
      "s_cbranch_scc1 L_slow1_%=\n"
      "L_sel1_%=:\n"
      "s_cmp_lg_u64 s[54:55], 0\n"
      "s_cbranch_scc1 L_max1_%=\n"
      "s_branch L_loop1_%=\n"
      "L_slow1_%=:\n"
      "s_nop 4\n"
      "v_mfma_f32_16x16x32_bf16 v[164:167], v[210:213], v[214:217], 0\n"
      "ds_read_b128 v[202:205], v0 offset:18432\n"
      "s_add_u32 s72, s42, 1\n"
      "v_mfma_f32_16x16x32_bf16 v[164:167], v[174:177], v[76:79], v[164:167]\n"
      "ds_read_b128 v[108:111], v9\n"
      "v_readlane_b32 s77, v38, s72\n"
      "v_mfma_f32_16x16x32_bf16 v[170:173], v[210:213], v[130:133], 0\n"
      "ds_read_b128 v[100:103], v0 offset:24576\n"
      "s_mov_b32 m0, 110592\n"
      "v_mfma_f32_16x16x32_bf16 v[170:173], v[174:177], v[80:83], v[170:173]\n"
      "ds_read_b128 v[198:201], v1 offset:30720\n"
      "s_and_b32 s73, s77, -16\n"
      "v_mfma_f32_16x16x32_bf16 v[128:131], v[210:213], v[194:197], 0\n"
      "ds_read_b128 v[210:213], v1 offset:18432\n"
      "v_subrev_u32 v217, 115456, v9\n"
      "v_mfma_f32_16x16x32_bf16 v[128:131], v[174:177], v[84:87], v[128:131]\n"
      "buffer_load_dwordx4 v217, s[28:31], s73 offen  lds\n"
      "s_waitcnt lgkmcnt(4)\n"
      "v_mfma_f32_16x16x32_bf16 v[138:141], a[0:3], v[202:205], 0\n"
      "ds_read_b128 v[134:137], v1 offset:24576\n"
      "v_readlane_b32 s45, v37, s43\n"
      "v_mfma_f32_16x16x32_bf16 v[160:163], a[24:27], v[202:205], 0\n"
      "s_waitcnt lgkmcnt(4)\n"
      "v_add_f32 v108, v108, v109\n"
      "v_add_f32_e64 v110, v110, v111\n"
      "v_mfma_f32_16x16x32_bf16 v[96:99], a[48:51], v[202:205], 0\n"
      "s_add_u32 s73, s73, 1024\n"
      "v_add_f32_e64 v108, v108, v110\n"
      "v_mov_b32_dpp v150, v150 quad_perm:[0,0,0,0] row_mask:0xf bank_mask:0xf\n"
      "v_mfma_f32_16x16x32_bf16 v[166:169], a[72:75], v[202:205], 0\n"
      "s_mov_b32 m0, 111616\n"
      "v_rsq_f32 v108, v108\n"
      "v_mfma_f32_16x16x32_bf16 v[116:119], a[96:99], v[202:205], 0\n"
      "s_waitcnt lgkmcnt(1)\n"
      "v_mul_f32 v108, 0x415db3d7, v108\n"
      "v_mov_b32_e64 v110, v108\n"
      "v_mfma_f32_16x16x32_bf16 v[146:149], a[120:123], v[202:205], 0\n"
      "ds_read_b128 v[112:115], v0 offset:30720\n"
      "v_subrev_u32 v165, 115456, v9\n"
      "v_mfma_f32_16x16x32_bf16 v[138:141], a[4:7], v[210:213], v[138:141]\n"
      "buffer_load_dwordx4 v217, s[28:31], s73 offen  lds\n"
      "v_mfma_f32_16x16x32_bf16 v[138:141], a[8:11], v[100:103], v[138:141]\n"
      "s_mov_b32 m0, 112640\n"
      "v_permlane32_swap_b32 v108, v110\n"
      "v_mfma_f32_16x16x32_bf16 v[160:163], a[28:31], v[210:213], v[160:163]\n"
      "v_mul_f32 v125, v110, v125\n"
      "v_bfe_u32 v85, v165, 4, 4\n"
      "v_mfma_f32_16x16x32_bf16 v[160:163], a[32:35], v[100:103], v[160:163]\n"
      "s_add_u32 s73, s73, 1024\n"
      "v_mul_f32 v206, v110, v206\n"
      "v_mov_b32 v109, v108\n"
      "v_mfma_f32_16x16x32_bf16 v[96:99], a[52:55], v[210:213], v[96:99]\n"
      "s_nop 0\n"
      "v_permlane16_swap_b32 v108, v109\n"
      "v_mfma_f32_16x16x32_bf16 v[96:99], a[56:59], v[100:103], v[96:99]\n"
      "v_subrev_u32 v39, 115456, v9\n"
      "v_mul_f32_e64 v189, v108, v189\n"
      "v_mfma_f32_16x16x32_bf16 v[166:169], a[76:79], v[210:213], v[166:169]\n"
      "ds_read_b128 v[190:193], v9 offset:4608\n"
      "v_mul_f32_e64 v143, v108, v143\n"
      "v_mfma_f32_16x16x32_bf16 v[166:169], a[80:83], v[100:103], v[166:169]\n"
      "buffer_load_dwordx4 v217, s[28:31], s73 offen  lds\n"
      "v_mfma_f32_16x16x32_bf16 v[116:119], a[100:103], v[210:213], v[116:119]\n"
      "s_waitcnt lgkmcnt(1)\n"
      "v_mul_f32 v186, v108, v186\n"
      "v_mul_f32_e64 v188, v108, v188\n"
      "v_mfma_f32_16x16x32_bf16 v[116:119], a[104:107], v[100:103], v[116:119]\n"
      "s_add_u32 s73, s73, 1024\n"
      "v_mul_f32 v187, v108, v187\n"
      "v_mul_f32 v104, v109, v104\n"
      "v_mfma_f32_16x16x32_bf16 v[146:149], a[124:127], v[210:213], v[146:149]\n"
      "s_mov_b32 m0, 113664\n"
      "v_mul_f32 v145, v108, v145\n"
      "v_mul_f32 v123, v109, v123\n"
      "v_mfma_f32_16x16x32_bf16 v[146:149], a[128:131], v[100:103], v[146:149]\n"
      "ds_read_b128 v[178:181], v0 offset:32768\n"
      "v_mul_f32_e64 v144, v108, v144\n"
      "v_mfma_f32_16x16x32_bf16 v[138:141], a[12:15], v[134:137], v[138:141]\n"
      "buffer_load_dwordx4 v217, s[28:31], s73 offen  lds\n"
      "v_mfma_f32_16x16x32_bf16 v[138:141], a[16:19], v[112:115], v[138:141]\n"
      "s_add_u32 s73, s73, 1024\n"
      "v_cvt_pk_bf16_f32 v186, v186, v187\n"
      "v_cvt_pk_bf16_f32 v187, v188, v189\n"
      "v_mfma_f32_16x16x32_bf16 v[138:141], a[20:23], v[198:201], v[138:141]\n"
      "s_mov_b32 m0, 114688\n"
      "v_mul_f32 v142, v108, v142\n"
      "v_mul_f32 v221, v109, v221\n"
      "v_mfma_f32_16x16x32_bf16 v[160:163], a[36:39], v[134:137], v[160:163]\n"
      "s_mov_b32 exec_hi, 0xffff\n"
      "buffer_load_dwordx4 v217, s[28:31], s73 offen  lds\n"
      "s_mov_b32 exec_hi, -1\n"
      "v_mfma_f32_16x16x32_bf16 v[160:163], a[40:43], v[112:115], v[160:163]\n"
      "v_cvt_pk_bf16_f32 v188, v142, v143\n"
      "v_cvt_pk_bf16_f32 v189, v144, v145\n"
      "v_mfma_f32_16x16x32_bf16 v[160:163], a[44:47], v[198:201], v[160:163]\n"
      "v_mul_f32 v220, v109, v220\n"
      "v_fma_f32 v77, v138, v138, s51\n"
      "v_mfma_f32_16x16x32_bf16 v[96:99], a[60:63], v[134:137], v[96:99]\n"
      "ds_write_b128 v5, v[186:189] offset:18432\n"
      "v_pk_mul_f32 v[156:157], v[156:157], v[108:109] op_sel_hi:[1,0]\n"
      "v_pk_mul_f32 v[158:159], v[158:159], v[108:109] op_sel_hi:[1,0]\n"
      "v_pk_mul_f32 v[120:121], v[120:121], v[108:109] op_sel:[0,1]\n"
      "v_mfma_f32_16x16x32_bf16 v[96:99], a[64:67], v[112:115], v[96:99]\n"
      "s_add_u32 m0, s64, 0\n"
      "v_cvt_pk_bf16_f32 v156, v156, v157\n"
      "v_mul_f32 v107, v109, v107\n"
      "v_mfma_f32_16x16x32_bf16 v[96:99], a[68:71], v[198:201], v[96:99]\n"
      "buffer_load_dwordx4 v16, s[16:19], s84 offen  lds\n"
      "v_mfma_f32_16x16x32_bf16 v[166:169], a[84:87], v[134:137], v[166:169]\n"
      "ds_read_b128 v[142:145], v1 offset:26624\n"
      "v_mul_f32_e64 v122, v109, v122\n"
      "v_mfma_f32_16x16x32_bf16 v[166:169], a[88:91], v[112:115], v[166:169]\n"
      "ds_read_b128 v[174:177], v1 offset:20480\n"
      "v_cvt_pk_bf16_f32 v157, v158, v159\n"
      "v_mfma_f32_16x16x32_bf16 v[166:169], a[92:95], v[198:201], v[166:169]\n"
      "ds_write_b64 v6, v[156:157] offset:18432\n"
      "v_mfma_f32_16x16x32_bf16 v[116:119], a[108:111], v[134:137], v[116:119]\n"
      "v_fmac_f32 v77, v139, v139\n"
      "v_mul_f32 v219, v109, v219\n"
      "v_mfma_f32_16x16x32_bf16 v[116:119], a[112:115], v[112:115], v[116:119]\n"
      "v_cvt_pk_bf16_f32 v120, v120, v121\n"
      "v_mul_f32 v106, v109, v106\n"
      "v_mfma_f32_16x16x32_bf16 v[116:119], a[116:119], v[198:201], v[116:119]\n"
      "s_add_u32 m0, s64, 4096\n"
      "v_rcp_f32 v150, v150\n"
      "v_mfma_f32_16x16x32_bf16 v[146:149], a[132:135], v[134:137], v[146:149]\n"
      "buffer_load_dwordx4 v17, s[16:19], s84 offen  lds\n"
      "v_mfma_f32_16x16x32_bf16 v[146:149], a[136:139], v[112:115], v[146:149]\n"
      "ds_read_b128 v[154:157], v0 offset:20480\n"
      "v_mul_f32_e64 v105, v109, v105\n"
      "v_mfma_f32_16x16x32_bf16 v[146:149], a[140:143], v[198:201], v[146:149]\n"
      "ds_read_b128 v[186:189], v0 offset:26624\n"
      "v_readlane_b32 s46, v37, s42\n"
      "v_mfma_f32_16x16x32_bf16 v[214:217], v[202:205], a[144:147], 0\n"
      "v_cndmask_b32 v164, v164, v170, s[58:59]\n"
      "v_cvt_pk_bf16_f32 v121, v122, v123\n"
      "v_mfma_f32_16x16x32_bf16 v[214:217], v[210:213], a[148:151], v[214:217]\n"
      "s_add_u32 m0, s64, 8192\n"
      "v_mul_f32_e64 v218, v109, v218\n"
      "v_cvt_pk_bf16_f32 v123, v220, v221\n"
      "v_mfma_f32_16x16x32_bf16 v[214:217], v[100:103], a[152:155], v[214:217]\n"
      "buffer_load_dwordx4 v18, s[16:19], s84 offen  lds\n"
      "v_mfma_f32_16x16x32_bf16 v[214:217], v[134:137], a[156:159], v[214:217]\n"
      "s_add_u32 m0, s64, 12288\n"
      "v_fma_f32 v158, v166, v166, s51\n"
      "v_fmac_f32 v158, v167, v167\n"
      "v_mfma_f32_16x16x32_bf16 v[214:217], v[112:115], a[160:163], v[214:217]\n"
      "v_cvt_pk_bf16_f32 v104, v104, v105\n"
      "v_fmac_f32 v158, v168, v168\n"
      "v_mfma_f32_16x16x32_bf16 v[214:217], v[198:201], a[164:167], v[214:217]\n"
      "v_cndmask_b32 v164, v164, v128, s[60:61]\n"
      "v_mul_u32_u24 v78, 98, v85\n"
      "v_mfma_f32_16x16x32_bf16 v[130:133], v[202:205], a[168:171], 0\n"
      "v_cvt_pk_bf16_f32 v122, v218, v219\n"
      "v_mul_f32 v185, v110, v185\n"
      "v_mfma_f32_16x16x32_bf16 v[130:133], v[210:213], a[172:175], v[130:133]\n"
      "buffer_load_dwordx4 v19, s[16:19], s84 offen  lds\n"
      "v_mfma_f32_16x16x32_bf16 v[130:133], v[100:103], a[176:179], v[130:133]\n"
      "v_fmac_f32 v77, v140, v140\n"
      "v_fmac_f32 v77, v141, v141\n"
      "v_mfma_f32_16x16x32_bf16 v[130:133], v[134:137], a[180:183], v[130:133]\n"
      "v_cvt_pk_bf16_f32 v105, v106, v107\n"
      "v_mul_f32 v182, v110, v182\n"
      "v_mfma_f32_16x16x32_bf16 v[130:133], v[112:115], a[184:187], v[130:133]\n"
      "s_mov_b32 m0, s6\n"
      "v_fmac_f32 v158, v169, v169\n"
      "v_fmac_f32 v158, v116, v116\n"
      "v_mfma_f32_16x16x32_bf16 v[130:133], v[198:201], a[188:191], v[130:133]\n"
      "v_mul_f32 v207, v110, v207\n"
      "v_fmac_f32_e64 v158, v117, v117\n"
      "v_mfma_f32_16x16x32_bf16 v[194:197], v[202:205], a[192:195], 0\n"
      "buffer_load_dwordx4 v15, s[0:3], s5 offen  lds\n"
      "v_mfma_f32_16x16x32_bf16 v[194:197], v[210:213], a[196:199], v[194:197]\n"
      "ds_read_b128 v[170:173], v1 offset:32768\n"
      "v_mul_f32 v124, v110, v124\n"
      "v_mfma_f32_16x16x32_bf16 v[194:197], v[100:103], a[200:203], v[194:197]\n"
      "v_cvt_pk_bf16_f32 v130, v130, v131\n"
      "v_mul_f32 v209, v110, v209\n"
      "v_mfma_f32_16x16x32_bf16 v[194:197], v[134:137], a[204:207], v[194:197]\n"
      "ds_write_b128 v5, v[120:123] offset:24576\n"
      "v_pk_mul_f32 v[126:127], v[126:127], v[110:111] op_sel_hi:[1,0]\n"
      "v_mfma_f32_16x16x32_bf16 v[194:197], v[112:115], a[208:211], v[194:197]\n"
      "buffer_load_dwordx4 v[112:115], v36, s[28:31], s86 offen\n"
      "v_mfma_f32_16x16x32_bf16 v[194:197], v[198:201], a[212:215], v[194:197]\n"
      "s_waitcnt lgkmcnt(2)\n"
      "v_fmac_f32 v158, v118, v118\n"
      "v_mul_f32 v208, v110, v208\n"
      "v_mfma_f32_16x16x32_bf16 v[210:213], a[0:3], v[154:157], 0\n"
      "v_fmac_f32 v77, v160, v160\n"
      "v_cvt_pk_bf16_f32 v214, v214, v215\n"
      "v_mfma_f32_16x16x32_bf16 v[210:213], a[4:7], v[174:177], v[210:213]\n"
      "ds_write_b64 v6, v[104:105] offset:24576\n"
      "v_mfma_f32_16x16x32_bf16 v[210:213], a[8:11], v[186:189], v[210:213]\n"
      "v_fmac_f32 v77, v161, v161\n"
      "v_fmac_f32 v158, v119, v119\n"
      "v_mfma_f32_16x16x32_bf16 v[210:213], a[12:15], v[142:145], v[210:213]\n"
      "s_waitcnt lgkmcnt(1)\n"
      "v_fmac_f32 v77, v162, v162\n"
      "v_mul_f32_e64 v184, v110, v184\n"
      "v_mfma_f32_16x16x32_bf16 v[210:213], a[16:19], v[178:181], v[210:213]\n"
      "buffer_load_dword v86, v29, s[28:31], s86 offen\n"
      "v_mfma_f32_16x16x32_bf16 v[210:213], a[20:23], v[170:173], v[210:213]\n"
      "s_or_b32 s95, s45, s46\n"
      "v_fmac_f32 v158, v146, v146\n"
      "v_cvt_pk_bf16_f32 v194, v194, v195\n"
      "v_mfma_f32_16x16x32_bf16 v[218:221], a[24:27], v[154:157], 0\n"
      "ds_read_b128 v[104:107], v1 offset:22528\n"
      "v_fmac_f32_e64 v77, v163, v163\n"
      "v_mfma_f32_16x16x32_bf16 v[218:221], a[28:31], v[174:177], v[218:221]\n"
      "v_cvt_pk_bf16_f32 v124, v124, v125\n"
      "v_fma_f32 v159, v210, v210, s51\n"
      "v_mfma_f32_16x16x32_bf16 v[218:221], a[32:35], v[186:189], v[218:221]\n"
      "v_mul_f32 v164, v150, v164\n"
      "v_fmac_f32 v77, v96, v96\n"
      "v_mfma_f32_16x16x32_bf16 v[218:221], a[36:39], v[142:145], v[218:221]\n"
      "v_fmac_f32 v77, v97, v97\n"
      "v_fmac_f32 v158, v147, v147\n"
      "v_mfma_f32_16x16x32_bf16 v[218:221], a[40:43], v[178:181], v[218:221]\n"
      "ds_read_b128 v[134:137], v0 offset:22528\n"
      "v_fmac_f32 v158, v148, v148\n"
      "v_mfma_f32_16x16x32_bf16 v[218:221], a[44:47], v[170:173], v[218:221]\n"
      "v_fmac_f32 v158, v149, v149\n"
      "v_fmac_f32_e64 v77, v98, v98\n"
      "v_mfma_f32_16x16x32_bf16 v[150:153], a[48:51], v[154:157], 0\n"
      "v_fmac_f32_e64 v77, v99, v99\n"
      "v_cvt_pk_bf16_f32 v164, v164, v164\n"
      "v_mfma_f32_16x16x32_bf16 v[150:153], a[52:55], v[174:177], v[150:153]\n"
      "v_cvt_pk_bf16_f32 v215, v216, v217\n"
      "v_lshrrev_b32 v39, 3, v39\n"
      "v_mfma_f32_16x16x32_bf16 v[150:153], a[56:59], v[186:189], v[150:153]\n"
      "v_permlane16_swap_b32 v77, v158\n"
      "v_mfma_f32_16x16x32_bf16 v[150:153], a[60:63], v[142:145], v[150:153]\n"
      "v_add_f32 v77, v77, v158\n"
      "v_mov_b32 v158, v77\n"
      "v_mfma_f32_16x16x32_bf16 v[150:153], a[64:67], v[178:181], v[150:153]\n"
      "s_nop 0\n"
      "v_permlane32_swap_b32 v77, v158\n"
      "v_mfma_f32_16x16x32_bf16 v[150:153], a[68:71], v[170:173], v[150:153]\n"
      "v_lshrrev_b32 v165, 8, v165\n"
      "v_min_u32 v39, 96, v39\n"
      "v_mfma_f32_16x16x32_bf16 v[100:103], a[72:75], v[154:157], 0\n"
      "v_fmac_f32 v159, v211, v211\n"
      "v_readlane_b32 s47, v38, s43\n"
      "v_mfma_f32_16x16x32_bf16 v[100:103], a[76:79], v[174:177], v[100:103]\n"
      "v_or3_b32 v190, v190, v191, v192\n"
      "v_add_f32_e64 v77, v77, v158\n"
      "v_mfma_f32_16x16x32_bf16 v[100:103], a[80:83], v[186:189], v[100:103]\n"
      "ds_read_b128 v[198:201], v0 offset:28672\n"
      "v_fmac_f32 v159, v212, v212\n"
      "v_mfma_f32_16x16x32_bf16 v[100:103], a[84:87], v[142:145], v[100:103]\n"
      "s_and_b32 s77, s77, 15\n"
      "v_cvt_pk_bf16_f32 v195, v196, v197\n"
      "v_cvt_pk_bf16_f32 v131, v132, v133\n"
      "v_mfma_f32_16x16x32_bf16 v[100:103], a[88:91], v[178:181], v[100:103]\n"
      "v_rsq_f32 v77, v77\n"
      "v_mfma_f32_16x16x32_bf16 v[100:103], a[92:95], v[170:173], v[100:103]\n"
      "v_cvt_pk_bf16_f32 v125, v126, v127\n"
      "v_cvt_pk_bf16_f32 v126, v206, v207\n"
      "v_mfma_f32_16x16x32_bf16 v[120:123], a[96:99], v[154:157], 0\n"
      "v_cvt_pk_bf16_f32 v127, v208, v209\n"
      "v_fmac_f32 v159, v213, v213\n"
      "v_mfma_f32_16x16x32_bf16 v[120:123], a[100:103], v[174:177], v[120:123]\n"
      "s_or_b32 s95, s95, s48\n"
      "v_mov_b32 v158, v77\n"
      "v_fma_f32 v192, v100, v100, s51\n"
      "v_mfma_f32_16x16x32_bf16 v[120:123], a[104:107], v[186:189], v[120:123]\n"
      "v_permlane16_swap_b32 v77, v158\n"
      "v_mfma_f32_16x16x32_bf16 v[120:123], a[108:111], v[142:145], v[120:123]\n"
      "v_fmac_f32 v192, v101, v101\n"
      "v_mul_f32 v77, s50, v77\n"
      "v_mfma_f32_16x16x32_bf16 v[120:123], a[112:115], v[178:181], v[120:123]\n"
      "v_fmac_f32 v192, v102, v102\n"
      "v_fmac_f32 v159, v218, v218\n"
      "v_mfma_f32_16x16x32_bf16 v[120:123], a[116:119], v[170:173], v[120:123]\n"
      "v_mul_f32 v183, v110, v183\n"
      "v_fmac_f32 v159, v219, v219\n"
      "v_mfma_f32_16x16x32_bf16 v[202:205], a[120:123], v[154:157], 0\n"
      "v_mul_f32 v149, v158, v149\n"
      "v_fmac_f32 v159, v220, v220\n"
      "v_mfma_f32_16x16x32_bf16 v[202:205], a[124:127], v[174:177], v[202:205]\n"
      "ds_write_b128 v5, v[124:127] offset:30720\n"
      "v_mfma_f32_16x16x32_bf16 v[202:205], a[128:131], v[186:189], v[202:205]\n"
      "v_fmac_f32 v192, v103, v103\n"
      "v_mul_f32 v146, v158, v146\n"
      "v_mfma_f32_16x16x32_bf16 v[202:205], a[132:135], v[142:145], v[202:205]\n"
      "v_fmac_f32 v159, v221, v221\n"
      "v_fmac_f32 v192, v120, v120\n"
      "v_mfma_f32_16x16x32_bf16 v[202:205], a[136:139], v[178:181], v[202:205]\n"
      "v_mul_f32 v139, v77, v139\n"
      "v_cvt_pk_bf16_f32 v182, v182, v183\n"
      "v_mfma_f32_16x16x32_bf16 v[202:205], a[140:143], v[170:173], v[202:205]\n"
      "v_lshl_add_u32 v78, v165, 3, v78\n"
      "v_fmac_f32_e64 v192, v121, v121\n"
      "v_mfma_f32_16x16x32_bf16 v[108:111], v[154:157], a[144:147], 0\n"
      "v_mul_f32 v116, v158, v116\n"
      "v_mul_f32 v167, v158, v167\n"
      "v_mfma_f32_16x16x32_bf16 v[108:111], v[174:177], a[148:151], v[108:111]\n"
      "v_cvt_pk_bf16_f32 v183, v184, v185\n"
      "v_fmac_f32 v159, v150, v150\n"
      "v_mfma_f32_16x16x32_bf16 v[108:111], v[186:189], a[152:155], v[108:111]\n"
      "v_mul_f32 v138, v77, v138\n"
      "v_fmac_f32 v192, v122, v122\n"
      "v_mfma_f32_16x16x32_bf16 v[108:111], v[142:145], a[156:159], v[108:111]\n"
      "v_mul_f32 v99, v77, v99\n"
      "v_fmac_f32 v192, v123, v123\n"
      "v_mfma_f32_16x16x32_bf16 v[108:111], v[178:181], a[160:163], v[108:111]\n"
      "ds_write_b16 v12, v164\n"
      "v_mfma_f32_16x16x32_bf16 v[108:111], v[170:173], a[164:167], v[108:111]\n"
      "v_fmac_f32 v159, v151, v151\n"
      "v_lshlrev_b32 v165, 2, v165\n"
      "v_mfma_f32_16x16x32_bf16 v[126:129], v[154:157], a[168:171], 0\n"
      "v_fmac_f32 v192, v202, v202\n"
      "v_fmac_f32_e64 v192, v203, v203\n"
      "v_mfma_f32_16x16x32_bf16 v[126:129], v[174:177], a[172:175], v[126:129]\n"
      "ds_write_b64 v6, v[182:183] offset:30720\n"
      "v_mfma_f32_16x16x32_bf16 v[126:129], v[186:189], a[176:179], v[126:129]\n"
      "v_mul_f32 v147, v158, v147\n"
      "v_fmac_f32 v159, v152, v152\n"
      "v_mfma_f32_16x16x32_bf16 v[126:129], v[142:145], a[180:183], v[126:129]\n"
      "v_bitop3_b32 v190, v190, v193, s33 bitop3:0xa8\n"
      "v_cvt_pk_bf16_f32 v138, v138, v139\n"
      "v_mfma_f32_16x16x32_bf16 v[126:129], v[178:181], a[184:187], v[126:129]\n"
      "ds_read_b128 v[206:209], v1 offset:28672\n"
      "v_fmac_f32 v192, v204, v204\n"
      "v_mfma_f32_16x16x32_bf16 v[126:129], v[170:173], a[188:191], v[126:129]\n"
      "v_fmac_f32 v192, v205, v205\n"
      "v_fmac_f32 v159, v153, v153\n"
      "v_mfma_f32_16x16x32_bf16 v[182:185], v[154:157], a[192:195], 0\n"
      "v_mul_f32 v141, v77, v141\n"
      "v_mul_f32 v166, v158, v166\n"
      "v_mfma_f32_16x16x32_bf16 v[182:185], v[174:177], a[196:199], v[182:185]\n"
      "ds_read_b128 v[154:157], v0 offset:34816\n"
      "v_mul_f32 v97, v77, v97\n"
      "v_mfma_f32_16x16x32_bf16 v[182:185], v[186:189], a[200:203], v[182:185]\n"
      "s_waitcnt lgkmcnt(2)\n"
      "v_permlane16_swap_b32 v159, v192\n"
      "v_mfma_f32_16x16x32_bf16 v[182:185], v[142:145], a[204:207], v[182:185]\n"
      "ds_read_b128 v[186:189], v1 offset:34816\n"
      "v_add_f32_e64 v159, v159, v192\n"
      "v_mfma_f32_16x16x32_bf16 v[182:185], v[178:181], a[208:211], v[182:185]\n"
      "ds_read_b128 v[142:145], v7\n"
      "v_mov_b32 v192, v159\n"
      "v_mfma_f32_16x16x32_bf16 v[182:185], v[170:173], a[212:215], v[182:185]\n"
      "s_nop 0\n"
      "v_permlane32_swap_b32 v159, v192\n"
      "v_mfma_f32_16x16x32_bf16 v[178:181], a[0:3], v[134:137], 0\n"
      "v_add_f32 v159, v159, v192\n"
      "v_add_u32_e64 v39, s77, v39\n"
      "v_mfma_f32_16x16x32_bf16 v[178:181], a[4:7], v[104:107], v[178:181]\n"
      "v_rsq_f32 v159, v159\n"
      "v_mfma_f32_16x16x32_bf16 v[178:181], a[8:11], v[198:201], v[178:181]\n"
      "s_waitcnt lgkmcnt(2)\n"
      "v_cmp_ne_u32 s[56:57], 0, v190\n"
      "v_cvt_pk_bf16_f32 v166, v166, v167\n"
      "v_mfma_f32_16x16x32_bf16 v[178:181], a[12:15], v[206:209], v[178:181]\n"
      "s_waitcnt lgkmcnt(1)\n"
      "v_cvt_pk_bf16_f32 v133, v128, v129\n"
      "v_mov_b32 v192, v159\n"
      "v_mfma_f32_16x16x32_bf16 v[178:181], a[16:19], v[154:157], v[178:181]\n"
      "s_nop 0\n"
      "v_permlane16_swap_b32 v159, v192\n"
      "v_mfma_f32_16x16x32_bf16 v[178:181], a[20:23], v[186:189], v[178:181]\n"
      "v_sub_u32 v85, v85, v165\n"
      "v_mul_f32 v96, v77, v96\n"
      "v_mfma_f32_16x16x32_bf16 v[170:173], a[24:27], v[134:137], 0\n"
      "v_cvt_pk_bf16_f32 v217, v110, v111\n"
      "v_mul_f32 v159, s50, v159\n"
      "v_mfma_f32_16x16x32_bf16 v[170:173], a[28:31], v[104:107], v[170:173]\n"
      "s_and_b32 s47, s47, -4\n"
      "v_mul_f32 v148, v158, v148\n"
      "v_mul_f32 v121, v192, v121\n"
      "v_mfma_f32_16x16x32_bf16 v[170:173], a[32:35], v[198:201], v[170:173]\n"
      "s_waitcnt lgkmcnt(0)\n"
      "v_cvt_pk_bf16_f32 v216, v108, v109\n"
      "v_mul_f32 v140, v77, v140\n"
      "v_mfma_f32_16x16x32_bf16 v[170:173], a[36:39], v[206:209], v[170:173]\n"
      "buffer_store_dwordx4 v[142:145], v20, s[24:27], s85 offen sc1\n"
      "v_pk_mul_f32 v[218:219], v[218:219], v[158:159] op_sel:[0,1]\n"
      "v_pk_mul_f32 v[102:103], v[102:103], v[192:193] op_sel_hi:[1,0]\n"
      "v_pk_mul_f32 v[204:205], v[204:205], v[192:193] op_sel_hi:[1,0]\n"
      "v_pk_mul_f32 v[160:161], v[160:161], v[76:77] op_sel:[0,1]\n"
      "v_pk_mul_f32 v[122:123], v[122:123], v[192:193] op_sel_hi:[1,0]\n"
      "v_pk_mul_f32 v[118:119], v[118:119], v[158:159] op_sel_hi:[1,0]\n"
      "v_pk_mul_f32 v[162:163], v[162:163], v[76:77] op_sel:[0,1]\n"
      "v_pk_mul_f32 v[210:211], v[210:211], v[158:159] op_sel:[0,1]\n"
      "v_mfma_f32_16x16x32_bf16 v[170:173], a[40:43], v[154:157], v[170:173]\n"
      "v_mul_f32 v98, v77, v98\n"
      "v_mul_f32 v169, v158, v169\n"
      "v_mfma_f32_16x16x32_bf16 v[170:173], a[44:47], v[186:189], v[170:173]\n"
      "s_add_u32 s73, s73, 1024\n"
      "v_mul_f32 v101, v192, v101\n"
      "v_fma_f32 v82, v178, v178, s51\n"
      "v_mfma_f32_16x16x32_bf16 v[108:111], a[48:51], v[134:137], 0\n"
      "v_mul_f32 v168, v158, v168\n"
      "v_cvt_pk_bf16_f32 v146, v146, v147\n"
      "v_mfma_f32_16x16x32_bf16 v[108:111], a[52:55], v[104:107], v[108:111]\n"
      "v_mul_f32 v120, v192, v120\n"
      "v_fmac_f32 v82, v179, v179\n"
      "v_mfma_f32_16x16x32_bf16 v[108:111], a[56:59], v[198:201], v[108:111]\n"
      "v_fmac_f32 v82, v180, v180\n"
      "v_mul_f32 v100, v192, v100\n"
      "v_mfma_f32_16x16x32_bf16 v[108:111], a[60:63], v[206:209], v[108:111]\n"
      "v_add_u32_e64 v78, s77, v78\n"
      "v_cvt_pk_bf16_f32 v147, v148, v149\n"
      "v_mfma_f32_16x16x32_bf16 v[108:111], a[64:67], v[154:157], v[108:111]\n"
      "v_lshlrev_b32 v85, 4, v85\n"
      "v_mul_f32 v212, v159, v212\n"
      "v_mfma_f32_16x16x32_bf16 v[108:111], a[68:71], v[186:189], v[108:111]\n"
      "v_cvt_pk_bf16_f32 v139, v140, v141\n"
      "v_cvt_pk_bf16_f32 v210, v210, v211\n"
      "v_mfma_f32_16x16x32_bf16 v[174:177], a[72:75], v[134:137], 0\n"
      "v_mul_f32 v213, v159, v213\n"
      "v_fmac_f32 v82, v181, v181\n"
      "v_mfma_f32_16x16x32_bf16 v[174:177], a[76:79], v[104:107], v[174:177]\n"
      "v_cvt_pk_bf16_f32 v140, v160, v161\n"
      "v_fmac_f32 v82, v170, v170\n"
      "v_mfma_f32_16x16x32_bf16 v[174:177], a[80:83], v[198:201], v[174:177]\n"
      "v_mul_f32 v220, v159, v220\n"
      "v_fmac_f32_e64 v82, v171, v171\n"
      "v_mfma_f32_16x16x32_bf16 v[174:177], a[84:87], v[206:209], v[174:177]\n"
      "v_mul_f32 v203, v192, v203\n"
      "v_mul_f32 v117, v158, v117\n"
      "v_mfma_f32_16x16x32_bf16 v[174:177], a[88:91], v[154:157], v[174:177]\n"
      "v_mul_f32_e64 v202, v192, v202\n"
      "v_cvt_pk_bf16_f32 v100, v100, v101\n"
      "v_mfma_f32_16x16x32_bf16 v[174:177], a[92:95], v[186:189], v[174:177]\n"
      "s_cmp_ge_u32 s95, 0x40000000\n"
      "s_cselect_b32 s94, -1, -2\n"
      "v_fmac_f32_e64 v82, v172, v172\n"
      "v_mfma_f32_16x16x32_bf16 v[190:193], a[96:99], v[134:137], 0\n"
      "v_fmac_f32_e64 v82, v173, v173\n"
      "v_cvt_pk_bf16_f32 v167, v168, v169\n"
      "v_mfma_f32_16x16x32_bf16 v[190:193], a[100:103], v[104:107], v[190:193]\n"
      "v_cvt_pk_bf16_f32 v141, v162, v163\n"
      "v_fma_f32 v88, v174, v174, s51\n"
      "v_mfma_f32_16x16x32_bf16 v[190:193], a[104:107], v[198:201], v[190:193]\n"
      "v_cvt_pk_bf16_f32 v132, v126, v127\n"
      "v_fmac_f32_e64 v88, v175, v175\n"
      "v_mfma_f32_16x16x32_bf16 v[190:193], a[108:111], v[206:209], v[190:193]\n"
      "v_cvt_pk_bf16_f32 v197, v184, v185\n"
      "v_cvt_pk_bf16_f32 v168, v116, v117\n"
      "v_mfma_f32_16x16x32_bf16 v[190:193], a[112:115], v[154:157], v[190:193]\n"
      "v_cvt_pk_bf16_f32 v96, v96, v97\n"
      "v_fmac_f32 v82, v108, v108\n"
      "v_mfma_f32_16x16x32_bf16 v[190:193], a[116:119], v[186:189], v[190:193]\n"
      "v_fmac_f32 v82, v109, v109\n"
      "v_cvt_pk_bf16_f32 v169, v118, v119\n"
      "v_mfma_f32_16x16x32_bf16 v[162:165], a[120:123], v[134:137], 0\n"
      "v_cvt_pk_bf16_f32 v211, v212, v213\n"
      "v_mov_b32 v148, 0x3fb9\n"
      "v_mfma_f32_16x16x32_bf16 v[162:165], a[124:127], v[104:107], v[162:165]\n"
      "v_cvt_pk_bf16_f32 v212, v218, v219\n"
      "v_fmac_f32 v82, v110, v110\n"
      "v_mfma_f32_16x16x32_bf16 v[162:165], a[128:131], v[198:201], v[162:165]\n"
      "v_fmac_f32 v88, v176, v176\n"
      "v_fmac_f32 v88, v177, v177\n"
      "v_mfma_f32_16x16x32_bf16 v[162:165], a[132:135], v[206:209], v[162:165]\n"
      "s_waitcnt vmcnt(8)\n"
      "v_fmac_f32 v88, v190, v190\n"
      "v_mul_f32 v221, v159, v221\n"
      "v_mfma_f32_16x16x32_bf16 v[162:165], a[136:139], v[154:157], v[162:165]\n"
      "v_cvt_pk_bf16_f32 v196, v182, v183\n"
      "v_fmac_f32 v82, v111, v111\n"
      "v_mfma_f32_16x16x32_bf16 v[162:165], a[140:143], v[186:189], v[162:165]\n"
      "v_fmac_f32 v88, v191, v191\n"
      "v_fmac_f32 v88, v192, v192\n"
      "v_fmac_f32 v88, v193, v193\n"
      "v_cvt_pk_bf16_f32 v213, v220, v221\n"
      "v_and_b32_e64 v93, 2, v78\n"
      "v_pk_mul_f32 v[150:151], v[150:151], v[158:159] op_sel:[0,1]\n"
      "v_pk_mul_f32 v[152:153], v[152:153], v[158:159] op_sel:[0,1]\n"
      "v_mfma_f32_16x16x32_bf16 v[142:145], v[134:137], a[144:147], 0\n"
      "v_fmac_f32 v88, v162, v162\n"
      "v_fmac_f32 v88, v163, v163\n"
      "v_fmac_f32 v88, v164, v164\n"
      "v_fmac_f32 v88, v165, v165\n"
      "v_mfma_f32_16x16x32_bf16 v[142:145], v[104:107], a[148:151], v[142:145]\n"
      "s_and_b32 s4, s45, s8\n"
      "v_permlane16_swap_b32 v82, v88\n"
      "v_mfma_f32_16x16x32_bf16 v[142:145], v[198:201], a[152:155], v[142:145]\n"
      "v_add_f32 v82, v82, v88\n"
      "v_mov_b32 v88, v82\n"
      "v_mfma_f32_16x16x32_bf16 v[142:145], v[206:209], a[156:159], v[142:145]\n"
      "s_nop 0\n"
      "v_permlane32_swap_b32 v82, v88\n"
      "v_mfma_f32_16x16x32_bf16 v[142:145], v[154:157], a[160:163], v[142:145]\n"
      "v_add_f32 v82, v82, v88\n"
      "v_rsq_f32 v82, v82\n"
      "v_mfma_f32_16x16x32_bf16 v[142:145], v[186:189], a[164:167], v[142:145]\n"
      "s_cmp_gt_u32 s42, s44\n"
      "s_cselect_b32 s95, s94, s95\n"
      "v_mov_b32 v88, v82\n"
      "v_mfma_f32_16x16x32_bf16 v[116:119], v[134:137], a[168:171], 0\n"
      "v_cvt_pk_bf16_f32 v97, v98, v99\n"
      "v_permlane16_swap_b32 v82, v88\n"
      "v_cvt_pk_bf16_f32 v202, v202, v203\n"
      "v_mul_f32_e64 v82, s50, v82\n"
      "v_pk_mul_f32 v[162:163], v[162:163], v[88:89] op_sel_hi:[1,0]\n"
      "v_pk_mul_f32 v[108:109], v[108:109], v[82:83] op_sel_hi:[1,0]\n"
      "v_pk_mul_f32 v[172:173], v[172:173], v[82:83] op_sel_hi:[1,0]\n"
      "v_pk_mul_f32 v[174:175], v[174:175], v[88:89] op_sel_hi:[1,0]\n"
      "v_pk_mul_f32 v[180:181], v[180:181], v[82:83] op_sel_hi:[1,0]\n"
      "v_pk_mul_f32 v[192:193], v[192:193], v[88:89] op_sel_hi:[1,0]\n"
      "v_pk_mul_f32 v[176:177], v[176:177], v[88:89] op_sel_hi:[1,0]\n"
      "v_pk_mul_f32 v[178:179], v[178:179], v[82:83] op_sel_hi:[1,0]\n"
      "v_mfma_f32_16x16x32_bf16 v[116:119], v[104:107], a[172:175], v[116:119]\n"
      "v_cvt_pk_bf16_f32 v101, v102, v103\n"
      "v_mul_f32_e64 v111, v82, v111\n"
      "v_mfma_f32_16x16x32_bf16 v[116:119], v[198:201], a[176:179], v[116:119]\n"
      "ds_read_b128 v[182:185], v2\n"
      "v_cvt_pk_bf16_f32 v77, v144, v145\n"
      "v_mfma_f32_16x16x32_bf16 v[116:119], v[206:209], a[180:183], v[116:119]\n"
      "v_cvt_pk_bf16_f32 v203, v204, v205\n"
      "v_cvt_pk_bf16_f32 v102, v120, v121\n"
      "v_mfma_f32_16x16x32_bf16 v[116:119], v[154:157], a[184:187], v[116:119]\n"
      "ds_read_b128 v[126:129], v2 offset:6144\n"
      "v_mov_b32 v149, 0\n"
      "v_mfma_f32_16x16x32_bf16 v[116:119], v[186:189], a[188:191], v[116:119]\n"
      "v_cvt_pk_bf16_f32 v76, v142, v143\n"
      "v_and_b32 v78, -4, v78\n"
      "v_mfma_f32_16x16x32_bf16 v[158:161], v[134:137], a[192:195], 0\n"
      "ds_read_b128 v[220:223], v2 offset:3072\n"
      "v_cvt_pk_bf16_f32 v103, v122, v123\n"
      "v_mfma_f32_16x16x32_bf16 v[158:161], v[104:107], a[196:199], v[158:161]\n"
      "ds_read_b128 v[142:145], v7 offset:4096\n"
      "v_mul_f32 v110, v82, v110\n"
      "v_mfma_f32_16x16x32_bf16 v[158:161], v[198:201], a[200:203], v[158:161]\n"
      "ds_read_b128 v[120:123], v2 offset:9216\n"
      "v_cvt_pk_bf16_f32 v174, v174, v175\n"
      "v_mfma_f32_16x16x32_bf16 v[158:161], v[206:209], a[204:207], v[158:161]\n"
      "s_waitcnt lgkmcnt(4)\n"
      "v_cvt_pk_bf16_f32 v150, v150, v151\n"
      "v_cvt_pk_bf16_f32 v81, v118, v119\n"
      "v_mfma_f32_16x16x32_bf16 v[158:161], v[154:157], a[208:211], v[158:161]\n"
      "ds_read_b128 v[104:107], v2 offset:12288\n"
      "v_mov_b32_e64 v125, 0\n"
      "v_mfma_f32_16x16x32_bf16 v[158:161], v[186:189], a[212:215], v[158:161]\n"
      "ds_read_b128 v[134:137], v2 offset:15360\n"
      "v_mul_f32 v190, v88, v190\n"
      "v_mfma_f32_16x16x32_bf16 v[186:189], a[216:219], v[182:185], 0\n"
      "s_waitcnt lgkmcnt(4)\n"
      "v_mul_f32_e64 v191, v88, v191\n"
      "v_add_u32 v155, 110592, v78\n"
      "v_mfma_f32_16x16x32_bf16 v[186:189], a[220:223], v[220:223], v[186:189]\n"
      "s_waitcnt lgkmcnt(3)\n"
      "v_cvt_pk_bf16_f32 v175, v176, v177\n"
      "v_cvt_pk_bf16_f32 v108, v108, v109\n"
      "v_mfma_f32_16x16x32_bf16 v[186:189], a[224:227], v[126:129], v[186:189]\n"
      "buffer_store_dwordx4 v[142:145], v21, s[24:27], s85 offen sc1\n"
      "s_nop 1\n"
      "v_mfma_f32_16x16x32_bf16 v[142:145], a[240:243], v[182:185], 0\n"
      "ds_read2_b32 v[118:119], v155 offset0:2 offset1:10\n"
      "v_cvt_pk_bf16_f32 v84, v158, v159\n"
      "v_mfma_f32_16x16x32_bf16 v[142:145], a[244:247], v[220:223], v[142:145]\n"
      "s_cmp_ge_u32 s95, 0x40000000\n"
      "s_cselect_b64 vcc, -1, s[56:57]\n"
      "v_lshlrev_b64 v[148:149], v85, v[148:149]\n"
      "v_mfma_f32_16x16x32_bf16 v[142:145], a[248:251], v[126:129], v[142:145]\n"
      "s_waitcnt lgkmcnt(1)\n"
      "v_cvt_pk_bf16_f32 v177, v192, v193\n"
      "v_mul_f32 v171, v82, v171\n"
      "v_mfma_f32_16x16x32_bf16 v[156:159], v[232:235], v[182:185], 0\n"
      "ds_read2_b32 v[98:99], v155 offset0:16 offset1:17\n"
      "v_cvt_pk_bf16_f32 v80, v116, v117\n"
      "v_mfma_f32_16x16x32_bf16 v[156:159], v[236:239], v[220:223], v[156:159]\n"
      "ds_read_b128 v[218:221], v7 offset:8192\n"
      "v_cvt_pk_bf16_f32 v178, v178, v179\n"
      "v_mfma_f32_16x16x32_bf16 v[156:159], v[240:243], v[126:129], v[156:159]\n"
      "ds_read2_b32 v[116:117], v155 offset0:0 offset1:1\n"
      "v_cmp_gt_u32 s[72:73], 64, v85\n"
      "v_mfma_f32_16x16x32_bf16 v[186:189], a[228:231], v[120:123], v[186:189]\n"
      "v_cvt_pk_bf16_f32 v176, v190, v191\n"
      "v_cvt_pk_bf16_f32 v179, v180, v181\n"
      "v_mfma_f32_16x16x32_bf16 v[186:189], a[232:235], v[104:107], v[186:189]\n"
      "v_cvt_pk_bf16_f32 v109, v110, v111\n"
      "v_cndmask_b32_e64 v148, 0, v148, s[72:73]\n"
      "v_mfma_f32_16x16x32_bf16 v[186:189], a[236:239], v[134:137], v[186:189]\n"
      "ds_read_b128 v[198:201], v10 offset:1200\n"
      "v_cvt_pk_bf16_f32 v151, v152, v153\n"
      "v_mfma_f32_16x16x32_bf16 v[142:145], a[252:255], v[120:123], v[142:145]\n"
      "ds_read_b64 v[222:223], v10 offset:1192\n"
      "v_mul_f32_e64 v165, v88, v165\n"
      "v_mfma_f32_16x16x32_bf16 v[142:145], v[224:227], v[104:107], v[142:145]\n"
      "ds_read_b128 v[206:209], v7 offset:12288\n"
      "v_mov_b32 v124, 0xbb2b\n"
      "v_mfma_f32_16x16x32_bf16 v[142:145], v[228:231], v[134:137], v[142:145]\n"
      "ds_read2_b32 v[110:111], v155 offset0:8 offset1:9\n"
      "v_mul_f32 v164, v88, v164\n"
      "v_mfma_f32_16x16x32_bf16 v[156:159], v[244:247], v[120:123], v[156:159]\n"
      "s_waitcnt lgkmcnt(4)\n"
      "v_mul_f32_e64 v170, v82, v170\n"
      "v_cvt_pk_bf16_f32 v181, v172, v173\n"
      "v_mfma_f32_16x16x32_bf16 v[156:159], v[248:251], v[104:107], v[156:159]\n"
      "buffer_store_dwordx4 v[218:221], v22, s[24:27], s85 offen sc1\n"
      "v_mfma_f32_16x16x32_bf16 v[156:159], v[252:255], v[134:137], v[156:159]\n"
      "s_nop 0\n"
      "ds_read2_b32 v[220:221], v155 offset0:24 offset1:18\n"
      "v_cndmask_b32_e64 v149, 0, v149, s[72:73]\n"
      "v_mfma_f32_16x16x32_bf16 v[152:155], v[166:169], v[138:141], v[40:43]\n"
      "v_alignbyte_b32 v116, v117, v116, v93\n"
      "v_cmp_gt_u32 s[72:73], 64, v85\n"
      "v_mfma_f32_16x16x32_bf16 v[190:193], v[100:103], v[138:141], v[44:47]\n"
      "s_waitcnt lgkmcnt(1)\n"
      "v_lshlrev_b64 v[124:125], v85, v[124:125]\n"
      "v_cvt_pk_bf16_f32 v162, v162, v163\n"
      "v_mfma_f32_16x16x32_bf16 v[126:129], v[174:177], v[138:141], v[48:51]\n"
      "ds_read_b128 v[134:137], v2 offset:256\n"
      "v_cvt_pk_bf16_f32 v163, v164, v165\n"
      "v_mfma_f32_16x16x32_bf16 v[182:185], v[198:201], v[138:141], v[24:27]\n"
      "ds_read_b128 v[120:123], v7 offset:16384\n"
      "v_alignbyte_b32 v98, v99, v98, v93\n"
      "v_mfma_f32_16x16x16_bf16 v[152:155], v[146:147], v[96:97], v[152:155]\n"
      "v_fma_f32 v204, v186, v186, s52\n"
      "v_alignbyte_b32 v110, v111, v110, v93\n"
      "v_add_u32 v39, 115296, v39\n"
      "v_mfma_f32_16x16x16_bf16 v[190:193], v[202:203], v[96:97], v[190:193]\n"
      "v_cvt_pk_bf16_f32 v180, v170, v171\n"
      "v_cndmask_b32_e64 v124, 0, v124, s[72:73]\n"
      "v_mul_f32_e64 v205, v187, v187\n"
      "v_mfma_f32_16x16x16_bf16 v[126:129], v[162:163], v[96:97], v[126:129]\n"
      "buffer_store_dwordx4 v[206:209], v23, s[24:27], s85 offen sc1\n"
      "v_mfma_f32_16x16x16_bf16 v[182:185], v[222:223], v[96:97], v[182:185]\n"
      "v_alignbyte_b32 v117, v118, v117, v93\n"
      "v_alignbyte_b32 v111, v119, v111, v93\n"
      "v_fmac_f32 v204, v188, v188\n"
      "v_mfma_f32_16x16x16_bf16 v[152:155], v[148:149], v[116:117], v[152:155]\n"
      "s_waitcnt lgkmcnt(0)\n"
      "v_alignbyte_b32 v99, v221, v99, v93\n"
      "v_add_u32 v119, 112160, v78\n"
      "v_mfma_f32_16x16x16_bf16 v[190:193], v[148:149], v[110:111], v[190:193]\n"
      "ds_read_b128 v[206:209], v2 offset:3328\n"
      "v_alignbyte_b32 v220, v220, v220, v93\n"
      "v_mfma_f32_16x16x16_bf16 v[126:129], v[148:149], v[98:99], v[126:129]\n"
      "ds_read2_b32 v[96:97], v119 offset0:8 offset1:9\n"
      "v_cndmask_b32_e64 v125, 0, v125, s[72:73]\n"
      "v_mfma_f32_16x16x16_bf16 v[182:185], v[148:149], v[220:221], v[182:185]\n"
      "ds_read2_b32 v[164:165], v119 offset0:0 offset1:1\n"
      "v_cvt_pk_bf16_f32 v85, v160, v161\n"
      "v_mfma_f32_16x16x16_bf16 v[152:155], v[124:125], v[116:117], v[152:155]\n"
      "ds_read_b128 v[170:173], v2 offset:6400\n"
      "v_fmac_f32 v205, v189, v189\n"
      "v_mfma_f32_16x16x16_bf16 v[190:193], v[124:125], v[110:111], v[190:193]\n"
      "ds_read2_b32 v[116:117], v119 offset0:2 offset1:10\n"
      "v_fmac_f32 v205, v143, v143\n"
      "v_mfma_f32_16x16x16_bf16 v[126:129], v[124:125], v[98:99], v[126:129]\n"
      "buffer_store_dwordx4 v[120:123], v25, s[24:27], s85 offen sc1\n"
      "v_mfma_f32_16x16x16_bf16 v[182:185], v[124:125], v[220:221], v[182:185]\n"
      "v_fmac_f32_e64 v204, v142, v142\n"
      "v_add_u32 v111, 113728, v78\n"
      "v_max3_f32 v78, v152, v153, v154\n"
      "v_mfma_f32_16x16x32_bf16 v[120:123], a[216:219], v[134:137], 0\n"
      "s_waitcnt lgkmcnt(2)\n"
      "v_alignbyte_b32 v96, v97, v96, v93\n"
      "v_max3_f32 v78, v78, v192, v193\n"
      "v_mfma_f32_16x16x32_bf16 v[120:123], a[220:223], v[206:209], v[120:123]\n"
      "s_waitcnt lgkmcnt(1)\n"
      "v_max3_f32 v184, v155, v190, v191\n"
      "v_max3_f32 v78, v78, v128, v129\n"
      "v_mfma_f32_16x16x32_bf16 v[120:123], a[224:227], v[170:173], v[120:123]\n"
      "ds_read_u16 v39, v39\n"
      "v_max3_f32 v184, v184, v126, v127\n"
      "v_mfma_f32_16x16x32_bf16 v[218:221], a[240:243], v[134:137], 0\n"
      "s_waitcnt lgkmcnt(1)\n"
      "v_max3_f32 v78, v78, v184, v182\n"
      "v_alignbyte_b32 v164, v165, v164, v93\n"
      "v_mfma_f32_16x16x32_bf16 v[218:221], a[244:247], v[206:209], v[218:221]\n"
      "ds_read2_b32 v[184:185], v119 offset0:16 offset1:17\n"
      "v_alignbyte_b32 v97, v117, v97, v93\n"
      "v_mfma_f32_16x16x32_bf16 v[218:221], a[248:251], v[170:173], v[218:221]\n"
      "s_waitcnt vmcnt(6)\n"
      "v_or3_b32 v112, v112, v113, v114\n"
      "v_alignbyte_b32 v165, v116, v165, v93\n"
      "v_mfma_f32_16x16x32_bf16 v[104:107], v[232:235], v[134:137], 0\n"
      "ds_read2_b32 v[98:99], v119 offset0:24 offset1:18\n"
      "v_mov_b32_e64 v113, v78\n"
      "v_mfma_f32_16x16x32_bf16 v[104:107], v[236:239], v[206:209], v[104:107]\n"
      "s_nop 0\n"
      "v_permlane16_swap_b32 v78, v113\n"
      "v_mfma_f32_16x16x32_bf16 v[104:107], v[240:243], v[170:173], v[104:107]\n"
      "v_max_f32 v78, v78, v113\n"
      "v_mov_b32 v113, v78\n"
      "v_mfma_f32_16x16x32_bf16 v[116:119], v[166:169], v[210:213], v[52:55]\n"
      "s_nop 0\n"
      "v_permlane32_swap_b32 v78, v113\n"
      "v_mfma_f32_16x16x32_bf16 v[138:141], v[100:103], v[210:213], v[56:59]\n"
      "s_waitcnt lgkmcnt(1)\n"
      "v_max_f32 v78, v78, v113\n"
      "v_sub_f32 v190, v190, v78\n"
      "v_alignbyte_b32 v184, v185, v184, v93\n"
      "v_lshlrev_b32_e64 v39, 16, v39\n"
      "s_waitcnt vmcnt(5)\n"
      "v_or3_b32 v112, v112, v115, v86\n"
      "v_pk_add_f32 v[126:127], v[126:127], v[78:79] op_sel_hi:[1,0] neg_lo:[0,1] neg_hi:[0,1]\n"
      "v_pk_add_f32 v[154:155], v[154:155], v[78:79] op_sel_hi:[1,0] neg_lo:[0,1] neg_hi:[0,1]\n"
      "v_pk_fma_f32 v[204:205], v[144:145], v[144:145], v[204:205]\n"
      "v_pk_add_f32 v[152:153], v[152:153], v[78:79] op_sel_hi:[1,0] neg_lo:[0,1] neg_hi:[0,1]\n"
      "v_pk_add_f32 v[128:129], v[128:129], v[78:79] op_sel_hi:[1,0] neg_lo:[0,1] neg_hi:[0,1]\n"
      "v_pk_add_f32 v[192:193], v[192:193], v[78:79] op_sel_hi:[1,0] neg_lo:[0,1] neg_hi:[0,1]\n"
      "v_mfma_f32_16x16x32_bf16 v[170:173], v[174:177], v[210:213], v[60:63]\n"
      "ds_write_b32 v31, v112 offset:5632\n"
      "v_mfma_f32_16x16x32_bf16 v[112:115], v[198:201], v[210:213], v[26:29]\n"
      "s_waitcnt lgkmcnt(1)\n"
      "v_exp_f32 v128, v128\n"
      "v_mfma_f32_16x16x16_bf16 v[116:119], v[146:147], v[150:151], v[116:119]\n"
      "v_exp_f32_e64 v153, v153\n"
      "v_alignbyte_b32 v185, v99, v185, v93\n"
      "v_mfma_f32_16x16x16_bf16 v[138:141], v[202:203], v[150:151], v[138:141]\n"
      "v_exp_f32 v126, v126\n"
      "v_sub_f32 v182, v182, v78\n"
      "v_mfma_f32_16x16x16_bf16 v[170:173], v[162:163], v[150:151], v[170:173]\n"
      "v_alignbyte_b32 v98, v98, v98, v93\n"
      "v_exp_f32 v192, v192\n"
      "v_mfma_f32_16x16x16_bf16 v[112:115], v[222:223], v[150:151], v[112:115]\n"
      "v_exp_f32 v127, v127\n"
      "v_exp_f32_e64 v152, v152\n"
      "v_mfma_f32_16x16x16_bf16 v[116:119], v[148:149], v[164:165], v[116:119]\n"
      "v_cvt_pk_bf16_f32 v88, v126, v127\n"
      "v_exp_f32_e64 v129, v129\n"
      "v_mfma_f32_16x16x16_bf16 v[138:141], v[148:149], v[96:97], v[138:141]\n"
      "v_cvt_pk_bf16_f32 v152, v152, v153\n"
      "v_exp_f32 v154, v154\n"
      "v_mfma_f32_16x16x16_bf16 v[170:173], v[148:149], v[184:185], v[170:173]\n"
      "v_exp_f32 v155, v155\n"
      "v_cvt_pk_bf16_f32 v89, v128, v129\n"
      "v_mfma_f32_16x16x16_bf16 v[112:115], v[148:149], v[98:99], v[112:115]\n"
      "ds_read_b128 v[210:213], v2 offset:9472\n"
      "v_sub_f32_e64 v191, v191, v78\n"
      "v_mfma_f32_16x16x16_bf16 v[116:119], v[124:125], v[164:165], v[116:119]\n"
      "ds_read_b128 v[126:129], v2 offset:12544\n"
      "v_exp_f32 v191, v191\n"
      "v_mfma_f32_16x16x16_bf16 v[138:141], v[124:125], v[96:97], v[138:141]\n"
      "v_exp_f32 v190, v190\n"
      "v_exp_f32_e64 v193, v193\n"
      "v_mfma_f32_16x16x16_bf16 v[170:173], v[124:125], v[184:185], v[170:173]\n"
      "ds_read_b128 v[134:137], v2 offset:15616\n"
      "v_cvt_pk_bf16_f32 v153, v154, v155\n"
      "v_cvt_pk_bf16_f32 v155, v192, v193\n"
      "v_mfma_f32_16x16x16_bf16 v[112:115], v[124:125], v[98:99], v[112:115]\n"
      "v_exp_f32_e64 v182, v182\n"
      "v_max3_f32 v78, v119, v138, v139\n"
      "v_max3_f32 v165, v116, v117, v118\n"
      "v_max3_f32 v165, v165, v140, v141\n"
      "v_max3_f32 v165, v165, v172, v173\n"
      "v_pk_fma_f32 v[204:205], v[156:157], v[156:157], v[204:205]\n"
      "v_max3_f32 v78, v78, v170, v171\n"
      "v_pk_fma_f32 v[204:205], v[158:159], v[158:159], v[204:205]\n"
      "v_max3_f32 v165, v165, v78, v112\n"
      "v_cvt_pk_bf16_f32 v90, v182, 0\n"
      "v_mov_b32 v164, v165\n"
      "s_waitcnt lgkmcnt(1)\n"
      "v_mfma_f32_16x16x32_bf16 v[120:123], a[228:231], v[210:213], v[120:123]\n"
      "ds_read_u16 v78, v11 offset:1168\n"
      "v_permlane16_swap_b32 v165, v164\n"
      "v_mfma_f32_16x16x32_bf16 v[120:123], a[232:235], v[126:129], v[120:123]\n"
      "s_waitcnt lgkmcnt(1)\n"
      "v_max_f32 v165, v165, v164\n"
      "v_mov_b32 v164, v165\n"
      "v_mfma_f32_16x16x32_bf16 v[120:123], a[236:239], v[134:137], v[120:123]\n"
      "ds_read_u16 v86, v11 offset:1232\n"
      "v_cvt_pk_bf16_f32 v154, v190, v191\n"
      "v_mfma_f32_16x16x32_bf16 v[218:221], a[252:255], v[210:213], v[218:221]\n"
      "ds_read_u16 v82, v11 offset:1200\n"
      "v_permlane32_swap_b32 v165, v164\n"
      "v_max_f32 v165, v165, v164\n"
      "v_sub_f32_e64 v112, v112, v165\n"
      "v_pk_add_f32 v[172:173], v[172:173], v[164:165] op_sel:[0,1] neg_lo:[0,1] neg_hi:[0,1]\n"
      "v_pk_add_f32 v[116:117], v[116:117], v[164:165] op_sel:[0,1] neg_lo:[0,1] neg_hi:[0,1]\n"
      "v_pk_add_f32 v[138:139], v[138:139], v[164:165] op_sel:[0,1] neg_lo:[0,1] neg_hi:[0,1]\n"
      "v_pk_add_f32 v[170:171], v[170:171], v[164:165] op_sel:[0,1] neg_lo:[0,1] neg_hi:[0,1]\n"
      "v_pk_add_f32 v[118:119], v[118:119], v[164:165] op_sel:[0,1] neg_lo:[0,1] neg_hi:[0,1]\n"
      "v_pk_add_f32 v[140:141], v[140:141], v[164:165] op_sel:[0,1] neg_lo:[0,1] neg_hi:[0,1]\n"
      "v_mfma_f32_16x16x32_bf16 v[218:221], v[224:227], v[126:129], v[218:221]\n"
      "ds_read2_b32 v[164:165], v111 offset0:0 offset1:1\n"
      "v_add_f32 v204, v204, v205\n"
      "v_mfma_f32_16x16x32_bf16 v[218:221], v[228:231], v[134:137], v[218:221]\n"
      "v_exp_f32 v116, v116\n"
      "v_mfma_f32_16x16x32_bf16 v[104:107], v[244:247], v[210:213], v[104:107]\n"
      "v_exp_f32_e64 v119, v119\n"
      "v_mfma_f32_16x16x32_bf16 v[104:107], v[248:251], v[126:129], v[104:107]\n"
      "ds_read2_b32 v[212:213], v111 offset0:8 offset1:9\n"
      "v_exp_f32_e64 v138, v138\n"
      "v_mfma_f32_16x16x32_bf16 v[104:107], v[252:255], v[134:137], v[104:107]\n"
      "ds_read2_b32 v[190:191], v111 offset0:2 offset1:10\n"
      "v_exp_f32_e64 v117, v117\n"
      "v_mfma_f32_16x16x32_bf16 v[206:209], v[32:35], v[152:155], 0\n"
      "ds_read2_b32 v[114:115], v111 offset0:16 offset1:17\n"
      "v_cvt_pk_bf16_f32 v116, v116, v117\n"
      "v_mfma_f32_16x16x32_bf16 v[206:209], v[32:35], v[88:91], v[206:209]\n"
      "ds_read2_b32 v[160:161], v111 offset0:24 offset1:18\n"
      "v_exp_f32_e64 v118, v118\n"
      "v_mfma_f32_16x16x32_bf16 v[182:185], v[214:217], v[152:155], 0\n"
      "s_waitcnt lgkmcnt(4)\n"
      "v_exp_f32 v141, v141\n"
      "v_mfma_f32_16x16x32_bf16 v[182:185], v[76:79], v[88:91], v[182:185]\n"
      "v_alignbyte_b32 v164, v165, v164, v93\n"
      "v_cvt_pk_bf16_f32 v117, v118, v119\n"
      "v_mfma_f32_16x16x32_bf16 v[126:129], v[130:133], v[152:155], 0\n"
      "v_exp_f32 v139, v139\n"
      "v_mfma_f32_16x16x32_bf16 v[126:129], v[80:83], v[88:91], v[126:129]\n"
      "s_waitcnt lgkmcnt(2)\n"
      "v_alignbyte_b32 v165, v190, v165, v93\n"
      "v_cvt_pk_bf16_f32 v118, v138, v139\n"
      "v_mfma_f32_16x16x32_bf16 v[134:137], v[194:197], v[152:155], 0\n"
      "s_waitcnt lgkmcnt(1)\n"
      "v_exp_f32 v140, v140\n"
      "v_mfma_f32_16x16x32_bf16 v[134:137], v[84:87], v[88:91], v[134:137]\n"
      "v_alignbyte_b32 v114, v115, v114, v93\n"
      "v_alignbyte_b32 v212, v213, v212, v93\n"
      "v_mfma_f32_16x16x32_bf16 v[96:99], v[166:169], v[178:181], v[64:67]\n"
      "s_waitcnt lgkmcnt(0)\n"
      "v_cvt_pk_bf16_f32 v119, v140, v141\n"
      "v_alignbyte_b32 v160, v160, v160, v93\n"
      "v_mfma_f32_16x16x32_bf16 v[138:141], v[100:103], v[178:181], v[68:71]\n"
      "v_alignbyte_b32 v115, v161, v115, v93\n"
      "v_alignbyte_b32 v213, v191, v213, v93\n"
      "v_mfma_f32_16x16x32_bf16 v[190:193], v[174:177], v[178:181], v[72:75]\n"
      "v_exp_f32 v172, v172\n"
      "v_mfma_f32_16x16x32_bf16 v[150:153], v[198:201], v[178:181], v[28:31]\n"
      "v_rcp_f32 v206, v206\n"
      "v_mfma_f32_16x16x16_bf16 v[96:99], v[146:147], v[108:109], v[96:99]\n"
      "v_exp_f32 v171, v171\n"
      "v_mul_f32 v127, v206, v127\n"
      "v_mfma_f32_16x16x16_bf16 v[138:141], v[202:203], v[108:109], v[138:141]\n"
      "v_exp_f32 v173, v173\n"
      "v_mul_f32 v128, v206, v128\n"
      "v_mfma_f32_16x16x16_bf16 v[190:193], v[162:163], v[108:109], v[190:193]\n"
      "v_mul_f32 v185, v206, v185\n"
      "v_mul_f32 v182, v206, v182\n"
      "v_mul_f32 v126, v206, v126\n"
      "v_mfma_f32_16x16x16_bf16 v[150:153], v[222:223], v[108:109], v[150:153]\n"
      "v_exp_f32 v170, v170\n"
      "v_cvt_pk_bf16_f32 v93, v172, v173\n"
      "v_mfma_f32_16x16x16_bf16 v[96:99], v[148:149], v[164:165], v[96:99]\n"
      "v_mul_f32 v134, v206, v134\n"
      "v_exp_f32_e64 v112, v112\n"
      "v_mfma_f32_16x16x16_bf16 v[138:141], v[148:149], v[212:213], v[138:141]\n"
      "v_mul_f32 v184, v206, v184\n"
      "v_mul_f32 v183, v206, v183\n"
      "v_cvt_pk_bf16_f32 v182, v182, v183\n"
      "v_mfma_f32_16x16x16_bf16 v[190:193], v[148:149], v[114:115], v[190:193]\n"
      "v_cvt_pk_bf16_f32 v183, v184, v185\n"
      "v_mul_f32_e64 v135, v206, v135\n"
      "v_cvt_pk_bf16_f32 v184, v126, v127\n"
      "v_mfma_f32_16x16x16_bf16 v[150:153], v[148:149], v[160:161], v[150:153]\n"
      "ds_read_b128 v[108:111], v2 offset:512\n"
      "v_mul_f32_e64 v129, v206, v129\n"
      "v_mfma_f32_16x16x16_bf16 v[96:99], v[124:125], v[164:165], v[96:99]\n"
      "ds_read_b128 v[178:181], v2 offset:3584\n"
      "v_cvt_pk_bf16_f32 v185, v128, v129\n"
      "v_mfma_f32_16x16x16_bf16 v[138:141], v[124:125], v[212:213], v[138:141]\n"
      "v_cvt_pk_bf16_f32 v94, v112, 0\n"
      "v_fma_f32 v128, v120, v120, s52\n"
      "v_mul_f32_e64 v129, v121, v121\n"
      "v_mfma_f32_16x16x16_bf16 v[190:193], v[124:125], v[114:115], v[190:193]\n"
      "ds_read_b128 v[112:115], v2 offset:6656\n"
      "v_max3_f32 v149, v96, v97, v98\n"
      "v_mfma_f32_16x16x16_bf16 v[150:153], v[124:125], v[160:161], v[150:153]\n"
      "v_fmac_f32 v129, v123, v123\n"
      "v_cvt_pk_bf16_f32 v134, v134, v135\n"
      "v_fmac_f32 v128, v122, v122\n"
      "v_max3_f32 v149, v149, v140, v141\n"
      "v_max3_f32 v124, v99, v138, v139\n"
      "v_max3_f32 v124, v124, v190, v191\n"
      "v_pk_fma_f32 v[128:129], v[218:219], v[218:219], v[128:129]\n"
      "v_max3_f32 v149, v149, v192, v193\n"
      "v_max3_f32 v149, v149, v124, v150\n"
      "v_pk_mul_f32 v[136:137], v[136:137], v[206:207] op_sel_hi:[1,0]\n"
      "v_pk_fma_f32 v[128:129], v[220:221], v[220:221], v[128:129]\n"
      "v_mov_b32 v206, v149\n"
      "s_waitcnt lgkmcnt(1)\n"
      "v_mfma_f32_16x16x32_bf16 v[124:127], a[216:219], v[108:111], 0\n"
      "v_permlane16_swap_b32 v149, v206\n"
      "v_max_f32 v149, v149, v206\n"
      "v_mov_b32_e64 v206, v149\n"
      "v_cvt_pk_bf16_f32 v92, v170, v171\n"
      "v_pk_fma_f32 v[128:129], v[104:105], v[104:105], v[128:129]\n"
      "v_permlane32_swap_b32 v149, v206\n"
      "v_max_f32 v149, v149, v206\n"
      "v_pk_fma_f32 v[128:129], v[106:107], v[106:107], v[128:129]\n"
      "v_pk_add_f32 v[98:99], v[98:99], v[148:149] op_sel:[0,1] neg_lo:[0,1] neg_hi:[0,1]\n"
      "v_pk_add_f32 v[96:97], v[96:97], v[148:149] op_sel:[0,1] neg_lo:[0,1] neg_hi:[0,1]\n"
      "v_mfma_f32_16x16x32_bf16 v[124:127], a[220:223], v[178:181], v[124:127]\n"
      "s_waitcnt lgkmcnt(0)\n"
      "v_exp_f32 v99, v99\n"
      "v_mfma_f32_16x16x32_bf16 v[124:127], a[224:227], v[112:115], v[124:127]\n"
      "ds_write_b128 v3, v[182:185] offset:18432\n"
      "v_pk_add_f32 v[138:139], v[138:139], v[148:149] op_sel:[0,1] neg_lo:[0,1] neg_hi:[0,1]\n"
      "v_pk_add_f32 v[192:193], v[192:193], v[148:149] op_sel:[0,1] neg_lo:[0,1] neg_hi:[0,1]\n"
      "v_pk_add_f32 v[190:191], v[190:191], v[148:149] op_sel:[0,1] neg_lo:[0,1] neg_hi:[0,1]\n"
      "v_pk_add_f32 v[140:141], v[140:141], v[148:149] op_sel:[0,1] neg_lo:[0,1] neg_hi:[0,1]\n"
      "v_mfma_f32_16x16x32_bf16 v[206:209], a[240:243], v[108:111], 0\n"
      "v_exp_f32 v96, v96\n"
      "v_mfma_f32_16x16x32_bf16 v[206:209], a[244:247], v[178:181], v[206:209]\n"
      "v_exp_f32 v191, v191\n"
      "v_mfma_f32_16x16x32_bf16 v[206:209], a[248:251], v[112:115], v[206:209]\n"
      "v_exp_f32 v97, v97\n"
      "v_mfma_f32_16x16x32_bf16 v[182:185], v[232:235], v[108:111], 0\n"
      "v_exp_f32 v98, v98\n"
      "v_mfma_f32_16x16x32_bf16 v[182:185], v[236:239], v[178:181], v[182:185]\n"
      "v_cvt_pk_bf16_f32 v96, v96, v97\n"
      "v_cvt_pk_bf16_f32 v135, v136, v137\n"
      "v_mfma_f32_16x16x32_bf16 v[182:185], v[240:243], v[112:115], v[182:185]\n"
      "ds_read_b128 v[210:213], v10 offset:1168\n"
      "v_sub_f32 v150, v150, v149\n"
      "v_mfma_f32_16x16x32_bf16 v[112:115], v[32:35], v[116:119], 0\n"
      "v_exp_f32 v190, v190\n"
      "v_mfma_f32_16x16x32_bf16 v[112:115], v[32:35], v[92:95], v[112:115]\n"
      "v_exp_f32_e64 v140, v140\n"
      "v_mfma_f32_16x16x32_bf16 v[170:173], v[214:217], v[116:119], 0\n"
      "ds_read_b64 v[148:149], v10 offset:1184\n"
      "v_cvt_pk_bf16_f32 v97, v98, v99\n"
      "v_mfma_f32_16x16x32_bf16 v[170:173], v[76:79], v[92:95], v[170:173]\n"
      "ds_write_b64 v4, v[134:135] offset:18432\n"
      "v_mfma_f32_16x16x32_bf16 v[178:181], v[130:133], v[116:119], 0\n"
      "v_exp_f32 v138, v138\n"
      "v_mfma_f32_16x16x32_bf16 v[178:181], v[80:83], v[92:95], v[178:181]\n"
      "v_add_f32 v128, v128, v129\n"
      "v_cvt_pk_bf16_f32 v88, v190, v191\n"
      "v_mfma_f32_16x16x32_bf16 v[152:155], v[194:197], v[116:119], 0\n"
      "s_waitcnt lgkmcnt(2)\n"
      "v_exp_f32 v141, v141\n"
      "v_mfma_f32_16x16x32_bf16 v[152:155], v[84:87], v[92:95], v[152:155]\n"
      "ds_read_b128 v[108:111], v2 offset:9728\n"
      "v_cvt_pk_bf16_f32 v99, v140, v141\n"
      "v_mfma_f32_16x16x32_bf16 v[134:137], v[210:213], v[166:169], v[30:33]\n"
      "v_rcp_f32_e64 v112, v112\n"
      "v_mfma_f32_16x16x32_bf16 v[164:167], v[210:213], v[100:103], v[30:33]\n"
      "s_waitcnt lgkmcnt(2)\n"
      "v_exp_f32 v139, v139\n"
      "v_mfma_f32_16x16x32_bf16 v[116:119], v[210:213], v[174:177], v[30:33]\n"
      "ds_read_b128 v[174:177], v2 offset:12800\n"
      "v_cvt_pk_bf16_f32 v98, v138, v139\n"
      "v_mfma_f32_16x16x32_bf16 v[100:103], v[210:213], v[198:201], v[30:33]\n"
      "ds_read_b128 v[210:213], v2 offset:15872\n"
      "v_mul_f32_e64 v181, v112, v181\n"
      "v_mfma_f32_16x16x16_bf16 v[134:137], v[148:149], v[146:147], v[134:137]\n"
      "v_mul_f32 v179, v112, v179\n"
      "v_permlane16_swap_b32 v204, v128\n"
      "v_mfma_f32_16x16x16_bf16 v[164:167], v[148:149], v[202:203], v[164:167]\n"
      "v_exp_f32 v193, v193\n"
      "v_mul_f32 v172, v112, v172\n"
      "v_mfma_f32_16x16x16_bf16 v[116:119], v[148:149], v[162:163], v[116:119]\n"
      "v_exp_f32_e64 v150, v150\n"
      "v_mul_f32_e64 v173, v112, v173\n"
      "v_mfma_f32_16x16x16_bf16 v[100:103], v[148:149], v[222:223], v[100:103]\n"
      "v_exp_f32_e64 v192, v192\n"
      "s_waitcnt lgkmcnt(2)\n"
      "v_mfma_f32_16x16x32_bf16 v[124:127], a[228:231], v[108:111], v[124:127]\n"
      "s_waitcnt lgkmcnt(1)\n"
      "v_cndmask_b32 v134, v134, v164, s[58:59]\n"
      "v_cvt_pk_bf16_f32 v90, v150, 0\n"
      "v_mfma_f32_16x16x32_bf16 v[124:127], a[232:235], v[174:177], v[124:127]\n"
      "s_waitcnt lgkmcnt(0)\n"
      "v_mul_f32 v170, v112, v170\n"
      "v_mul_f32_e64 v178, v112, v178\n"
      "v_mfma_f32_16x16x32_bf16 v[124:127], a[236:239], v[210:213], v[124:127]\n"
      "v_cndmask_b32 v134, v134, v116, s[60:61]\n"
      "v_cndmask_b32 v134, v134, v100, s[62:63]\n"
      "v_mfma_f32_16x16x32_bf16 v[206:209], a[252:255], v[108:111], v[206:209]\n"
      "v_mul_f32 v171, v112, v171\n"
      "v_fmac_f32_e64 v134, s53, v39\n"
      "v_mfma_f32_16x16x32_bf16 v[206:209], v[224:227], v[174:177], v[206:209]\n"
      "s_nop 0\n"
      "v_max_f32_dpp v150, v134, v134 row_ror:8 row_mask:0xf bank_mask:0xf\n"
      "v_cvt_pk_bf16_f32 v170, v170, v171\n"
      "v_mfma_f32_16x16x32_bf16 v[206:209], v[228:231], v[210:213], v[206:209]\n"
      "v_max_f32_dpp v150, v150, v150 row_ror:4 row_mask:0xf bank_mask:0xf\n"
      "v_mul_f32_e64 v180, v112, v180\n"
      "v_mfma_f32_16x16x32_bf16 v[182:185], v[244:247], v[108:111], v[182:185]\n"
      "v_cvt_pk_bf16_f32 v171, v172, v173\n"
      "v_max_f32_dpp v150, v150, v150 row_ror:2 row_mask:0xf bank_mask:0xf\n"
      "v_mfma_f32_16x16x32_bf16 v[182:185], v[248:251], v[174:177], v[182:185]\n"
      "s_nop 0\n"
      "v_max_f32_dpp v150, v150, v150 row_ror:1 row_mask:0xf bank_mask:0xf\n"
      "v_cvt_pk_bf16_f32 v89, v192, v193\n"
      "v_mfma_f32_16x16x32_bf16 v[182:185], v[252:255], v[210:213], v[182:185]\n"
      "v_mul_f32 v151, v125, v125\n"
      "v_max_f32_dpp v150, v150, v150 row_bcast:15 row_mask:0xa bank_mask:0xf\n"
      "v_mfma_f32_16x16x32_bf16 v[164:167], v[32:35], v[96:99], 0\n"
      "v_cvt_pk_bf16_f32 v172, v178, v179\n"
      "v_cvt_pk_bf16_f32 v173, v180, v181\n"
      "v_mfma_f32_16x16x32_bf16 v[164:167], v[32:35], v[88:91], v[164:167]\n"
      "v_max_f32_dpp v150, v150, v150 row_bcast:31 row_mask:0xc bank_mask:0xf\n"
      "v_add_f32_e64 v204, v204, v128\n"
      "v_mfma_f32_16x16x32_bf16 v[178:181], v[214:217], v[96:99], 0\n"
      "v_readlane_b32 s72, v150, 63\n"
      "v_fma_f32 v150, v124, v124, s52\n"
      "v_mfma_f32_16x16x32_bf16 v[178:181], v[76:79], v[88:91], v[178:181]\n"
      "ds_write_b128 v3, v[170:173] offset:18688\n"
      "v_pk_fma_f32 v[150:151], v[126:127], v[126:127], v[150:151]\n"
      "v_mfma_f32_16x16x32_bf16 v[170:173], v[130:133], v[96:99], 0\n"
      "v_rcp_f32 v164, v164\n"
      "v_subrev_f32 v134, s72, v134\n"
      "v_exp_f32_e64 v134, v134\n"
      "v_pk_fma_f32 v[150:151], v[206:207], v[206:207], v[150:151]\n"
      "v_pk_fma_f32 v[150:151], v[208:209], v[208:209], v[150:151]\n"
      "v_pk_mul_f32 v[178:179], v[178:179], v[164:165] op_sel_hi:[1,0]\n"
      "v_pk_mul_f32 v[180:181], v[180:181], v[164:165] op_sel_hi:[1,0]\n"
      "v_cvt_pk_bf16_f32 v134, v134, v134\n"
      "v_mfma_f32_16x16x32_bf16 v[170:173], v[80:83], v[88:91], v[170:173]\n"
      "ds_write_b16 v13, v134\n"
      "v_mfma_f32_16x16x32_bf16 v[166:169], v[194:197], v[96:99], 0\n"
      "ds_read_b128 v[210:213], v14\n"
      "ds_read_b128 v[174:177], v14 offset:64\n"
      "v_cvt_pk_bf16_f32 v178, v178, v179\n"
      "v_cvt_pk_bf16_f32 v179, v180, v181\n"
      "v_pk_mul_f32 v[152:153], v[152:153], v[112:113] op_sel_hi:[1,0]\n"
      "v_pk_fma_f32 v[150:151], v[182:183], v[182:183], v[150:151]\n"
      "v_pk_mul_f32 v[170:171], v[170:171], v[164:165] op_sel_hi:[1,0]\n"
      "v_pk_mul_f32 v[172:173], v[172:173], v[164:165] op_sel_hi:[1,0]\n"
      "v_cvt_pk_bf16_f32 v181, v172, v173\n"
      "v_cvt_pk_bf16_f32 v180, v170, v171\n"
      "v_cvt_pk_bf16_f32 v152, v152, v153\n"
      "v_mfma_f32_16x16x32_bf16 v[166:169], v[84:87], v[88:91], v[166:169]\n"
      "ds_write_b128 v3, v[178:181] offset:18944\n"
      "v_pk_fma_f32 v[150:151], v[184:185], v[184:185], v[150:151]\n"
      "v_add_f32 v150, v150, v151\n"
      "v_mov_b32 v128, v150\n"
      "v_pk_mul_f32 v[154:155], v[154:155], v[112:113] op_sel_hi:[1,0]\n"
      "v_cvt_pk_bf16_f32 v153, v154, v155\n"
      "v_permlane16_swap_b32 v150, v128\n"
      "v_add_f32 v150, v150, v128\n"
      "s_add_u32 s42, s42, 1\n"
      "ds_write_b64 v4, v[152:153] offset:18688\n"
      "v_permlane32_swap_b32 v204, v150\n"
      "v_pk_mul_f32 v[166:167], v[166:167], v[164:165] op_sel_hi:[1,0]\n"
      "v_add_f32_e64 v204, v204, v150\n"
      "v_pk_mul_f32 v[168:169], v[168:169], v[164:165] op_sel_hi:[1,0]\n"
      "s_add_u32 s43, s43, 1\n"
      "ds_write_b32 v31, v204 offset:1024\n"
      "v_cvt_pk_bf16_f32 v166, v166, v167\n"
      "v_cvt_pk_bf16_f32 v167, v168, v169\n"
      "s_waitcnt lgkmcnt(2)\n"
      "v_mfma_f32_16x16x32_bf16 v[150:153], v[210:213], v[32:35], 0\n"
      "ds_write_b64 v4, v[166:167] offset:18944\n"
      "v_add_u32 v11, 2336, v11\n"
      "v_mfma_f32_16x16x32_bf16 v[150:153], v[174:177], v[32:35], v[150:153]\n"
      "v_add_u32 v10, 2336, v10\n"
      "s_waitcnt lgkmcnt(0)\n"
      "s_barrier\n"
      "s_cbranch_vccz L_sel0_%=\n"
      "L_disp0_%=:\n"
      "s_cmp_lt_u32 s95, 0x40000000\n"
      "s_cbranch_scc1 L_nofix_18_%=\n"
      "s_cmp_eq_u32 s95, -2\n"
      "s_cbranch_scc1 L_tail0_%=\n"
      "s_nop 2\n"
      "s_cmp_eq_u32 s45, 0x20000000\n"
      "s_cselect_b32 s66, 0, s66\n"
      "s_cselect_b32 s48, 0, s48\n"
      "s_lshr_b32 s92, s45, 30\n"
      "s_or_b32 s92, s92, s66\n"
      "s_cbranch_scc0 L_nf_19_%=\n"
      "s_bitcmp1_b32 s45, 31\n"
      "s_cselect_b32 s75, 100270464, 0\n"
      "s_bitcmp1_b32 s45, 30\n"
      "s_cselect_b32 s76, 196224, 0\n"
      "s_and_b32 s74, s45, 0x3fffffff\n"
      "s_cmp_ge_u32 s45, 0x40000000\n"
      "s_cselect_b32 s74, s74, 0\n"
      "s_cselect_b32 s45, 0, s45\n"
      "s_sub_u32 s74, s74, s66\n"
      "s_add_u32 s66, s66, s74\n"
      "s_sub_u32 s75, s75, s67\n"
      "s_add_u32 s67, s67, s75\n"
      "s_sub_u32 s76, s76, s68\n"
      "s_add_u32 s68, s68, s76\n"
      "v_add_u32 v16, s74, v16\n"
      "v_add_u32 v17, s74, v17\n"
      "v_add_u32 v18, s74, v18\n"
      "v_add_u32 v19, s74, v19\n"
      "v_add_u32_e64 v15, s74, v15\n"
      "s_cmp_eq_u32 s75, 0\n"
      "s_cbranch_scc1 L_ax_20_%=\n"
      "v_bfe_i32 v166, v27, 0, 1\n"
      "v_and_b32 v166, s75, v166\n"
      "v_sub_u32 v16, v16, v166\n"
      "v_bfe_i32 v166, v27, 2, 1\n"
      "v_and_b32 v166, s75, v166\n"
      "v_sub_u32 v17, v17, v166\n"
      "v_bfe_i32 v166, v27, 4, 1\n"
      "v_and_b32 v166, s75, v166\n"
      "v_sub_u32 v18, v18, v166\n"
      "v_bfe_i32 v166, v27, 6, 1\n"
      "v_and_b32 v166, s75, v166\n"
      "v_sub_u32 v19, v19, v166\n"
      "v_bfe_i32 v166, v27, 8, 1\n"
      "v_and_b32 v166, s75, v166\n"
      "v_sub_u32 v15, v15, v166\n"
      "L_ax_20_%=:\n"
      "s_cmp_eq_u32 s76, 0\n"
      "s_cbranch_scc1 L_ax_21_%=\n"
      "v_bfe_i32 v166, v27, 1, 1\n"
      "v_and_b32 v166, s76, v166\n"
      "v_sub_u32 v16, v16, v166\n"
      "v_bfe_i32 v166, v27, 3, 1\n"
      "v_and_b32 v166, s76, v166\n"
      "v_sub_u32 v17, v17, v166\n"
      "v_bfe_i32 v166, v27, 5, 1\n"
      "v_and_b32 v166, s76, v166\n"
      "v_sub_u32 v18, v18, v166\n"
      "v_bfe_i32 v166, v27, 7, 1\n"
      "v_and_b32 v166, s76, v166\n"
      "v_sub_u32 v19, v19, v166\n"
      "v_bfe_i32 v166, v27, 9, 1\n"
      "v_and_b32 v166, s76, v166\n"
      "v_sub_u32 v15, v15, v166\n"
      "L_ax_21_%=:\n"
      "s_or_b32 s48, s66, s69\n"
      "s_cmp_lg_u32 s48, 0\n"
      "s_cselect_b32 s48, 0x40000000, 0\n"
      "L_nf_19_%=:\n"
      "s_lshr_b32 s92, s46, 30\n"
      "s_or_b32 s92, s92, s69\n"
      "s_cbranch_scc0 L_nf_22_%=\n"
      "s_bitcmp1_b32 s46, 31\n"
      "s_cselect_b32 s75, 100270464, 0\n"
      "s_bitcmp1_b32 s46, 30\n"
      "s_cselect_b32 s76, 196224, 0\n"
      "s_and_b32 s74, s46, 0x3fffffff\n"
      "s_cmp_ge_u32 s46, 0x40000000\n"
      "s_cselect_b32 s74, s74, 0\n"
      "s_cselect_b32 s46, 0, s46\n"
      "s_sub_u32 s74, s74, s69\n"
      "s_add_u32 s69, s69, s74\n"
      "s_sub_u32 s75, s75, s70\n"
      "s_add_u32 s70, s70, s75\n"
      "s_sub_u32 s76, s76, s71\n"
      "s_add_u32 s71, s71, s76\n"
      "v_add_u32 v20, s74, v20\n"
      "v_add_u32 v21, s74, v21\n"
      "v_add_u32 v22, s74, v22\n"
      "v_add_u32 v23, s74, v23\n"
      "v_add_u32 v25, s74, v25\n"
      "s_cmp_eq_u32 s75, 0\n"
      "s_cbranch_scc1 L_ax_23_%=\n"
      "v_bfe_i32 v166, v27, 10, 1\n"
      "v_and_b32 v166, s75, v166\n"
      "v_sub_u32 v20, v20, v166\n"
      "v_bfe_i32 v166, v27, 12, 1\n"
      "v_and_b32 v166, s75, v166\n"
      "v_sub_u32 v21, v21, v166\n"
      "v_bfe_i32 v166, v27, 14, 1\n"
      "v_and_b32 v166, s75, v166\n"
      "v_sub_u32 v22, v22, v166\n"
      "v_bfe_i32 v166, v27, 16, 1\n"
      "v_and_b32 v166, s75, v166\n"
      "v_sub_u32 v23, v23, v166\n"
      "v_bfe_i32 v166, v27, 18, 1\n"
      "v_and_b32 v166, s75, v166\n"
      "v_sub_u32 v25, v25, v166\n"
      "L_ax_23_%=:\n"
      "s_cmp_eq_u32 s76, 0\n"
      "s_cbranch_scc1 L_ax_24_%=\n"
      "v_bfe_i32 v166, v27, 11, 1\n"
      "v_and_b32 v166, s76, v166\n"
      "v_sub_u32 v20, v20, v166\n"
      "v_bfe_i32 v166, v27, 13, 1\n"
      "v_and_b32 v166, s76, v166\n"
      "v_sub_u32 v21, v21, v166\n"
      "v_bfe_i32 v166, v27, 15, 1\n"
      "v_and_b32 v166, s76, v166\n"
      "v_sub_u32 v22, v22, v166\n"
      "v_bfe_i32 v166, v27, 17, 1\n"
      "v_and_b32 v166, s76, v166\n"
      "v_sub_u32 v23, v23, v166\n"
      "v_bfe_i32 v166, v27, 19, 1\n"
      "v_and_b32 v166, s76, v166\n"
      "v_sub_u32 v25, v25, v166\n"
      "L_ax_24_%=:\n"
      "s_or_b32 s48, s66, s69\n"
      "s_cmp_lg_u32 s48, 0\n"
      "s_cselect_b32 s48, 0x40000000, 0\n"
      "L_nf_22_%=:\n"
      "s_and_b32 s4, s45, s8\n"
      "s_sub_u32 s92, s42, 1\n"
      "s_cmp_gt_u32 s92, s44\n"
      "s_cbranch_scc1 L_tail0_%=\n"
      "L_nofix_18_%=:\n"
      "s_cmp_lg_u64 s[56:57], 0\n"
      "s_cbranch_scc1 L_slow0_%=\n"
      "L_sel0_%=:\n"
      "s_cmp_lg_u64 s[54:55], 0\n"
      "s_cbranch_scc1 L_max0_%=\n"
      "s_branch L_loop0_%=\n"
      "L_slow0_%=:\n"
      "s_nop 4\n"
      "v_mfma_f32_16x16x32_bf16 v[164:167], v[210:213], v[214:217], 0\n"
      "ds_read_b128 v[202:205], v0\n"
      "v_subrev_u32 v214, 115456, v9\n"
      "v_mfma_f32_16x16x32_bf16 v[164:167], v[174:177], v[76:79], v[164:167]\n"
      "s_mov_b32 m0, 110592\n"
      "ds_read_b128 v[108:111], v9 offset:1024\n"
      "v_mfma_f32_16x16x32_bf16 v[170:173], v[210:213], v[130:133], 0\n"
      "ds_read_b128 v[116:119], v1\n"
      "s_add_u32 s72, s42, 1\n"
      "v_mfma_f32_16x16x32_bf16 v[170:173], v[174:177], v[80:83], v[170:173]\n"
      "ds_read_b128 v[100:103], v0 offset:6144\n"
      "v_readlane_b32 s77, v38, s72\n"
      "v_mfma_f32_16x16x32_bf16 v[128:131], v[210:213], v[194:197], 0\n"
      "ds_read_b128 v[210:213], v1 offset:6144\n"
      "s_and_b32 s73, s77, -16\n"
      "v_mfma_f32_16x16x32_bf16 v[128:131], v[174:177], v[84:87], v[128:131]\n"
      "buffer_load_dwordx4 v214, s[28:31], s73 offen  lds\n"
      "s_waitcnt lgkmcnt(4)\n"
      "v_mfma_f32_16x16x32_bf16 v[174:177], a[0:3], v[202:205], 0\n"
      "ds_read_b128 v[166:169], v1 offset:8192\n"
      "v_subrev_u32 v39, 115456, v9\n"
      "v_mfma_f32_16x16x32_bf16 v[198:201], a[24:27], v[202:205], 0\n"
      "s_waitcnt lgkmcnt(4)\n"
      "v_readlane_b32 s84, v37, s43\n"
      "v_add_f32 v110, v110, v111\n"
      "v_mfma_f32_16x16x32_bf16 v[190:193], a[48:51], v[202:205], 0\n"
      "s_add_u32 s73, s73, 1024\n"
      "v_add_f32_e64 v108, v108, v109\n"
      "v_add_f32_e64 v108, v108, v110\n"
      "v_mfma_f32_16x16x32_bf16 v[138:141], a[72:75], v[202:205], 0\n"
      "s_mov_b32 m0, 111616\n"
      "v_rsq_f32 v108, v108\n"
      "v_mfma_f32_16x16x32_bf16 v[178:181], a[96:99], v[202:205], 0\n"
      "buffer_load_dwordx4 v214, s[28:31], s73 offen  lds\n"
      "v_mfma_f32_16x16x32_bf16 v[112:115], a[120:123], v[202:205], 0\n"
      "s_waitcnt lgkmcnt(1)\n"
      "v_mul_f32 v108, 0x415db3d7, v108\n"
      "v_subrev_u32 v111, 115456, v9\n"
      "v_mfma_f32_16x16x32_bf16 v[174:177], a[4:7], v[116:119], v[174:177]\n"
      "ds_read_b128 v[146:149], v1 offset:12288\n"
      "v_mov_b32 v110, v108\n"
      "v_mfma_f32_16x16x32_bf16 v[174:177], a[8:11], v[100:103], v[174:177]\n"
      "s_mov_b32 m0, 112640\n"
      "v_permlane32_swap_b32 v108, v110\n"
      "v_mfma_f32_16x16x32_bf16 v[198:201], a[28:31], v[116:119], v[198:201]\n"
      "s_add_u32 s73, s73, 1024\n"
      "v_mov_b32 v109, v108\n"
      "v_mul_f32 v208, v110, v208\n"
      "v_mfma_f32_16x16x32_bf16 v[198:201], a[32:35], v[100:103], v[198:201]\n"
      "buffer_load_dwordx4 v214, s[28:31], s73 offen  lds\n"
      "v_mfma_f32_16x16x32_bf16 v[190:193], a[52:55], v[116:119], v[190:193]\n"
      "ds_read_b128 v[134:137], v0 offset:12288\n"
      "v_mul_f32_e64 v125, v110, v125\n"
      "v_mfma_f32_16x16x32_bf16 v[190:193], a[56:59], v[100:103], v[190:193]\n"
      "s_add_u32 s73, s73, 1024\n"
      "v_permlane16_swap_b32 v108, v109\n"
      "v_mfma_f32_16x16x32_bf16 v[138:141], a[76:79], v[116:119], v[138:141]\n"
      "s_mov_b32 m0, 113664\n"
      "v_mul_f32 v105, v109, v105\n"
      "v_mul_f32 v159, v108, v159\n"
      "v_mfma_f32_16x16x32_bf16 v[138:141], a[80:83], v[100:103], v[138:141]\n"
      "v_mul_f32 v189, v108, v189\n"
      "v_mul_f32 v104, v109, v104\n"
      "v_mfma_f32_16x16x32_bf16 v[178:181], a[100:103], v[116:119], v[178:181]\n"
      "v_mul_f32 v142, v108, v142\n"
      "v_mul_f32 v122, v109, v122\n"
      "v_mfma_f32_16x16x32_bf16 v[178:181], a[104:107], v[100:103], v[178:181]\n"
      "ds_read_b128 v[96:99], v0 offset:2048\n"
      "v_mul_f32 v188, v108, v188\n"
      "v_mfma_f32_16x16x32_bf16 v[112:115], a[124:127], v[116:119], v[112:115]\n"
      "buffer_load_dwordx4 v214, s[28:31], s73 offen  lds\n"
      "v_mfma_f32_16x16x32_bf16 v[112:115], a[128:131], v[100:103], v[112:115]\n"
      "s_waitcnt lgkmcnt(1)\n"
      "v_mul_f32 v158, v108, v158\n"
      "v_mul_f32_e64 v124, v110, v124\n"
      "v_mfma_f32_16x16x32_bf16 v[174:177], a[12:15], v[210:213], v[174:177]\n"
      "s_add_u32 s73, s73, 1024\n"
      "v_mul_f32 v183, v110, v183\n"
      "v_mul_f32 v127, v110, v127\n"
      "v_mfma_f32_16x16x32_bf16 v[174:177], a[16:19], v[134:137], v[174:177]\n"
      "ds_read_b128 v[160:163], v9 offset:5632\n"
      "v_mul_f32 v187, v108, v187\n"
      "v_mfma_f32_16x16x32_bf16 v[174:177], a[20:23], v[146:149], v[174:177]\n"
      "s_mov_b32 m0, 114688\n"
      "v_mul_f32 v186, v108, v186\n"
      "v_mul_f32 v143, v108, v143\n"
      "v_mfma_f32_16x16x32_bf16 v[198:201], a[36:39], v[210:213], v[198:201]\n"
      "v_cvt_pk_bf16_f32 v186, v186, v187\n"
      "v_cvt_pk_bf16_f32 v187, v188, v189\n"
      "v_mfma_f32_16x16x32_bf16 v[198:201], a[40:43], v[134:137], v[198:201]\n"
      "s_mov_b32 exec_hi, 0xffff\n"
      "buffer_load_dwordx4 v214, s[28:31], s73 offen  lds\n"
      "s_mov_b32 exec_hi, -1\n"
      "v_pk_mul_f32 v[120:121], v[120:121], v[108:109] op_sel:[0,1]\n"
      "v_pk_mul_f32 v[184:185], v[184:185], v[110:111] op_sel_hi:[1,0]\n"
      "v_pk_mul_f32 v[220:221], v[220:221], v[108:109] op_sel:[0,1]\n"
      "v_pk_mul_f32 v[144:145], v[144:145], v[108:109] op_sel_hi:[1,0]\n"
      "v_pk_mul_f32 v[106:107], v[106:107], v[108:109] op_sel:[0,1]\n"
      "v_mfma_f32_16x16x32_bf16 v[198:201], a[44:47], v[146:149], v[198:201]\n"
      "v_cvt_pk_bf16_f32 v120, v120, v121\n"
      "v_cvt_pk_bf16_f32 v189, v144, v145\n"
      "v_mfma_f32_16x16x32_bf16 v[190:193], a[60:63], v[210:213], v[190:193]\n"
      "s_add_u32 m0, s64, 18432\n"
      "v_lshrrev_b32_e64 v39, 3, v39\n"
      "v_cvt_pk_bf16_f32 v188, v142, v143\n"
      "v_mfma_f32_16x16x32_bf16 v[190:193], a[64:67], v[134:137], v[190:193]\n"
      "ds_write_b128 v5, v[186:189]\n"
      "v_pk_mul_f32 v[218:219], v[218:219], v[108:109] op_sel:[0,1]\n"
      "v_mfma_f32_16x16x32_bf16 v[190:193], a[68:71], v[146:149], v[190:193]\n"
      "buffer_load_dwordx4 v16, s[16:19], s45 offen  lds\n"
      "v_mfma_f32_16x16x32_bf16 v[138:141], a[84:87], v[210:213], v[138:141]\n"
      "s_and_b32 s77, s77, 15\n"
      "v_fma_f32 v223, v174, v174, s51\n"
      "v_mul_f32 v156, v108, v156\n"
      "v_mfma_f32_16x16x32_bf16 v[138:141], a[88:91], v[134:137], v[138:141]\n"
      "s_add_u32 m0, s64, 22528\n"
      "v_mul_f32 v123, v109, v123\n"
      "v_cvt_pk_bf16_f32 v104, v104, v105\n"
      "v_mfma_f32_16x16x32_bf16 v[138:141], a[92:95], v[146:149], v[138:141]\n"
      "v_mul_f32 v182, v110, v182\n"
      "v_cvt_pk_bf16_f32 v121, v122, v123\n"
      "v_mfma_f32_16x16x32_bf16 v[178:181], a[108:111], v[210:213], v[178:181]\n"
      "v_mul_f32 v157, v108, v157\n"
      "v_mul_f32 v209, v110, v209\n"
      "v_mfma_f32_16x16x32_bf16 v[178:181], a[112:115], v[134:137], v[178:181]\n"
      "buffer_load_dwordx4 v17, s[16:19], s45 offen  lds\n"
      "v_mfma_f32_16x16x32_bf16 v[178:181], a[116:119], v[146:149], v[178:181]\n"
      "ds_read_b128 v[186:189], v0 offset:14336\n"
      "v_cvt_pk_bf16_f32 v124, v124, v125\n"
      "v_mfma_f32_16x16x32_bf16 v[112:115], a[132:135], v[210:213], v[112:115]\n"
      "v_mul_f32 v206, v110, v206\n"
      "v_fmac_f32 v223, v175, v175\n"
      "v_mfma_f32_16x16x32_bf16 v[112:115], a[136:139], v[134:137], v[112:115]\n"
      "v_cvt_pk_bf16_f32 v105, v106, v107\n"
      "v_cvt_pk_bf16_f32 v123, v220, v221\n"
      "v_mfma_f32_16x16x32_bf16 v[112:115], a[140:143], v[146:149], v[112:115]\n"
      "s_add_u32 m0, s64, 26624\n"
      "v_fma_f32 v81, v138, v138, s51\n"
      "v_cvt_pk_bf16_f32 v122, v218, v219\n"
      "v_mfma_f32_16x16x32_bf16 v[214:217], v[202:205], a[144:147], 0\n"
      "buffer_load_dwordx4 v18, s[16:19], s45 offen  lds\n"
      "v_mfma_f32_16x16x32_bf16 v[214:217], v[116:119], a[148:151], v[214:217]\n"
      "ds_write_b128 v5, v[120:123] offset:6144\n"
      "v_mfma_f32_16x16x32_bf16 v[214:217], v[100:103], a[152:155], v[214:217]\n"
      "s_add_u32 m0, s64, 30720\n"
      "v_fmac_f32 v81, v139, v139\n"
      "v_fmac_f32 v81, v140, v140\n"
      "v_mfma_f32_16x16x32_bf16 v[214:217], v[210:213], a[156:159], v[214:217]\n"
      "v_min_u32 v39, 96, v39\n"
      "v_fmac_f32_e64 v81, v141, v141\n"
      "v_mfma_f32_16x16x32_bf16 v[214:217], v[134:137], a[160:163], v[214:217]\n"
      "ds_write_b64 v6, v[104:105] offset:6144\n"
      "v_mfma_f32_16x16x32_bf16 v[214:217], v[146:149], a[164:167], v[214:217]\n"
      "buffer_load_dwordx4 v19, s[16:19], s45 offen  lds\n"
      "v_mfma_f32_16x16x32_bf16 v[130:133], v[202:205], a[168:171], 0\n"
      "s_mov_b32 m0, s7\n"
      "v_cndmask_b32 v164, v164, v170, s[58:59]\n"
      "v_fmac_f32 v223, v176, v176\n"
      "v_mfma_f32_16x16x32_bf16 v[130:133], v[116:119], a[172:175], v[130:133]\n"
      "v_cvt_pk_bf16_f32 v156, v156, v157\n"
      "v_fmac_f32 v81, v178, v178\n"
      "v_mfma_f32_16x16x32_bf16 v[130:133], v[100:103], a[176:179], v[130:133]\n"
      "ds_read_b128 v[142:145], v1 offset:2048\n"
      "v_mul_f32 v126, v110, v126\n"
      "v_mfma_f32_16x16x32_bf16 v[130:133], v[210:213], a[180:183], v[130:133]\n"
      "ds_read_b128 v[120:123], v1 offset:14336\n"
      "v_fmac_f32_e64 v81, v179, v179\n"
      "v_mfma_f32_16x16x32_bf16 v[130:133], v[134:137], a[184:187], v[130:133]\n"
      "buffer_load_dwordx4 v15, s[0:3], s4 offen  lds\n"
      "v_mfma_f32_16x16x32_bf16 v[130:133], v[146:149], a[188:191], v[130:133]\n"
      "v_cvt_pk_bf16_f32 v157, v158, v159\n"
      "v_cvt_pk_bf16_f32 v182, v182, v183\n"
      "v_mfma_f32_16x16x32_bf16 v[194:197], v[202:205], a[192:195], 0\n"
      "ds_read_b128 v[218:221], v0 offset:8192\n"
      "v_readlane_b32 s86, v38, s43\n"
      "v_mfma_f32_16x16x32_bf16 v[194:197], v[116:119], a[196:199], v[194:197]\n"
      "v_fmac_f32 v223, v177, v177\n"
      "v_fmac_f32 v223, v198, v198\n"
      "v_mfma_f32_16x16x32_bf16 v[194:197], v[100:103], a[200:203], v[194:197]\n"
      "ds_write_b64 v6, v[156:157]\n"
      "v_mfma_f32_16x16x32_bf16 v[194:197], v[210:213], a[204:207], v[194:197]\n"
      "v_cvt_pk_bf16_f32 v125, v126, v127\n"
      "v_fmac_f32 v81, v180, v180\n"
      "v_mfma_f32_16x16x32_bf16 v[194:197], v[134:137], a[208:211], v[194:197]\n"
      "buffer_load_dwordx4 v[210:213], v36, s[28:31], s47 offen\n"
      "v_mfma_f32_16x16x32_bf16 v[194:197], v[146:149], a[212:215], v[194:197]\n"
      "s_waitcnt lgkmcnt(2)\n"
      "v_fmac_f32 v223, v199, v199\n"
      "v_fmac_f32 v81, v181, v181\n"
      "v_mfma_f32_16x16x32_bf16 v[170:173], a[0:3], v[96:99], 0\n"
      "v_or3_b32 v160, v160, v161, v162\n"
      "v_bfe_u32 v84, v111, 4, 4\n"
      "v_mfma_f32_16x16x32_bf16 v[170:173], a[4:7], v[142:145], v[170:173]\n"
      "s_waitcnt lgkmcnt(1)\n"
      "v_mov_b32_dpp v150, v150 quad_perm:[0,0,0,0] row_mask:0xf bank_mask:0xf\n"
      "v_fmac_f32 v81, v112, v112\n"
      "v_mfma_f32_16x16x32_bf16 v[170:173], a[8:11], v[218:221], v[170:173]\n"
      "v_cvt_pk_bf16_f32 v127, v208, v209\n"
      "v_cndmask_b32 v164, v164, v128, s[60:61]\n"
      "v_mfma_f32_16x16x32_bf16 v[170:173], a[12:15], v[166:169], v[170:173]\n"
      "ds_read_b128 v[156:159], v0 offset:4096\n"
      "v_fmac_f32_e64 v223, v200, v200\n"
      "v_mfma_f32_16x16x32_bf16 v[170:173], a[16:19], v[186:189], v[170:173]\n"
      "buffer_load_dword v78, v29, s[28:31], s47 offen\n"
      "v_mfma_f32_16x16x32_bf16 v[170:173], a[20:23], v[120:123], v[170:173]\n"
      "v_cvt_pk_bf16_f32 v183, v184, v185\n"
      "v_fmac_f32 v223, v201, v201\n"
      "v_mfma_f32_16x16x32_bf16 v[104:107], a[24:27], v[96:99], 0\n"
      "v_fmac_f32 v81, v113, v113\n"
      "v_lshrrev_b32_e64 v111, 8, v111\n"
      "v_mfma_f32_16x16x32_bf16 v[104:107], a[28:31], v[142:145], v[104:107]\n"
      "v_mul_f32_e64 v207, v110, v207\n"
      "v_cvt_pk_bf16_f32 v126, v206, v207\n"
      "v_mfma_f32_16x16x32_bf16 v[104:107], a[32:35], v[218:221], v[104:107]\n"
      "ds_write_b128 v5, v[124:127] offset:12288\n"
      "v_mfma_f32_16x16x32_bf16 v[104:107], a[36:39], v[166:169], v[104:107]\n"
      "v_fmac_f32_e64 v223, v190, v190\n"
      "v_fma_f32 v137, v170, v170, s51\n"
      "v_mfma_f32_16x16x32_bf16 v[104:107], a[40:43], v[186:189], v[104:107]\n"
      "v_cvt_pk_bf16_f32 v214, v214, v215\n"
      "v_fmac_f32_e64 v223, v191, v191\n"
      "v_mfma_f32_16x16x32_bf16 v[104:107], a[44:47], v[120:123], v[104:107]\n"
      "ds_write_b64 v6, v[182:183] offset:12288\n"
      "v_mfma_f32_16x16x32_bf16 v[182:185], a[48:51], v[96:99], 0\n"
      "v_fmac_f32 v81, v114, v114\n"
      "v_fmac_f32 v223, v192, v192\n"
      "v_mfma_f32_16x16x32_bf16 v[182:185], a[52:55], v[142:145], v[182:185]\n"
      "v_fmac_f32 v81, v115, v115\n"
      "v_fmac_f32 v137, v171, v171\n"
      "v_mfma_f32_16x16x32_bf16 v[182:185], a[56:59], v[218:221], v[182:185]\n"
      "v_cvt_pk_bf16_f32 v194, v194, v195\n"
      "v_fmac_f32_e64 v223, v193, v193\n"
      "v_mfma_f32_16x16x32_bf16 v[182:185], a[60:63], v[166:169], v[182:185]\n"
      "s_nop 0\n"
      "v_permlane16_swap_b32 v223, v81\n"
      "v_mfma_f32_16x16x32_bf16 v[182:185], a[64:67], v[186:189], v[182:185]\n"
      "v_fmac_f32 v137, v172, v172\n"
      "v_fmac_f32 v137, v173, v173\n"
      "v_mfma_f32_16x16x32_bf16 v[182:185], a[68:71], v[120:123], v[182:185]\n"
      "v_mul_u32_u24 v82, 98, v84\n"
      "v_add_f32 v223, v223, v81\n"
      "v_mfma_f32_16x16x32_bf16 v[152:155], a[72:75], v[96:99], 0\n"
      "v_mov_b32 v81, v223\n"
      "v_cvt_pk_bf16_f32 v195, v196, v197\n"
      "v_mfma_f32_16x16x32_bf16 v[152:155], a[76:79], v[142:145], v[152:155]\n"
      "v_bitop3_b32 v160, v160, v163, s33 bitop3:0xa8\n"
      "v_cvt_pk_bf16_f32 v215, v216, v217\n"
      "v_mfma_f32_16x16x32_bf16 v[152:155], a[80:83], v[218:221], v[152:155]\n"
      "v_permlane32_swap_b32 v223, v81\n"
      "v_mfma_f32_16x16x32_bf16 v[152:155], a[84:87], v[166:169], v[152:155]\n"
      "v_readlane_b32 s85, v37, s42\n"
      "v_add_f32 v223, v223, v81\n"
      "v_mfma_f32_16x16x32_bf16 v[152:155], a[88:91], v[186:189], v[152:155]\n"
      "v_rsq_f32 v223, v223\n"
      "v_mfma_f32_16x16x32_bf16 v[152:155], a[92:95], v[120:123], v[152:155]\n"
      "v_cvt_pk_bf16_f32 v130, v130, v131\n"
      "v_mov_b32 v81, v223\n"
      "v_mfma_f32_16x16x32_bf16 v[206:209], a[96:99], v[96:99], 0\n"
      "v_fmac_f32 v137, v104, v104\n"
      "v_lshl_add_u32 v82, v111, 3, v82\n"
      "v_mfma_f32_16x16x32_bf16 v[206:209], a[100:103], v[142:145], v[206:209]\n"
      "v_rcp_f32 v150, v150\n"
      "v_mfma_f32_16x16x32_bf16 v[206:209], a[104:107], v[218:221], v[206:209]\n"
      "v_fmac_f32 v137, v105, v105\n"
      "v_fma_f32 v217, v152, v152, s51\n"
      "v_mfma_f32_16x16x32_bf16 v[206:209], a[108:111], v[166:169], v[206:209]\n"
      "v_permlane16_swap_b32 v223, v81\n"
      "v_mfma_f32_16x16x32_bf16 v[206:209], a[112:115], v[186:189], v[206:209]\n"
      "v_fmac_f32 v217, v153, v153\n"
      "v_mul_f32 v112, v81, v112\n"
      "v_mfma_f32_16x16x32_bf16 v[206:209], a[116:119], v[120:123], v[206:209]\n"
      "v_cmp_ne_u32 s[56:57], 0, v160\n"
      "v_fmac_f32 v137, v106, v106\n"
      "v_mfma_f32_16x16x32_bf16 v[146:149], a[120:123], v[96:99], 0\n"
      "v_mul_f32 v139, v81, v139\n"
      "v_lshlrev_b32_e64 v111, 2, v111\n"
      "v_mfma_f32_16x16x32_bf16 v[146:149], a[124:127], v[142:145], v[146:149]\n"
      "ds_read_b128 v[202:205], v1 offset:4096\n"
      "v_mul_f32 v138, v81, v138\n"
      "v_mfma_f32_16x16x32_bf16 v[146:149], a[128:131], v[218:221], v[146:149]\n"
      "v_fmac_f32 v217, v154, v154\n"
      "v_sub_u32_e64 v84, v84, v111\n"
      "v_mfma_f32_16x16x32_bf16 v[146:149], a[132:135], v[166:169], v[146:149]\n"
      "s_or_b32 s95, s84, s85\n"
      "v_cvt_pk_bf16_f32 v131, v132, v133\n"
      "v_add_u32 v82, s77, v82\n"
      "v_mfma_f32_16x16x32_bf16 v[146:149], a[136:139], v[186:189], v[146:149]\n"
      "v_mul_f32 v164, v150, v164\n"
      "v_fmac_f32 v137, v107, v107\n"
      "v_mfma_f32_16x16x32_bf16 v[146:149], a[140:143], v[120:123], v[146:149]\n"
      "v_fmac_f32 v217, v155, v155\n"
      "v_fmac_f32 v217, v206, v206\n"
      "v_mfma_f32_16x16x32_bf16 v[132:135], v[96:99], a[144:147], 0\n"
      "v_fmac_f32 v217, v207, v207\n"
      "v_mfma_f32_16x16x32_bf16 v[132:135], v[142:145], a[148:151], v[132:135]\n"
      "v_cvt_pk_bf16_f32 v164, v164, v164\n"
      "v_fmac_f32 v137, v182, v182\n"
      "v_mfma_f32_16x16x32_bf16 v[132:135], v[218:221], a[152:155], v[132:135]\n"
      "ds_read_b128 v[160:163], v0 offset:16384\n"
      "v_fmac_f32 v217, v208, v208\n"
      "v_mfma_f32_16x16x32_bf16 v[132:135], v[166:169], a[156:159], v[132:135]\n"
      "v_mul_f32 v141, v81, v141\n"
      "v_mul_f32_e64 v223, s50, v223\n"
      "v_mfma_f32_16x16x32_bf16 v[132:135], v[186:189], a[160:163], v[132:135]\n"
      "v_fmac_f32 v137, v183, v183\n"
      "v_fmac_f32 v217, v209, v209\n"
      "v_mfma_f32_16x16x32_bf16 v[132:135], v[120:123], a[164:167], v[132:135]\n"
      "v_fmac_f32 v217, v146, v146\n"
      "v_fmac_f32 v217, v147, v147\n"
      "v_mfma_f32_16x16x32_bf16 v[100:103], v[96:99], a[168:171], 0\n"
      "v_fmac_f32 v137, v184, v184\n"
      "v_mul_f32 v174, v223, v174\n"
      "v_mfma_f32_16x16x32_bf16 v[100:103], v[142:145], a[172:175], v[100:103]\n"
      "v_mul_f32 v175, v223, v175\n"
      "v_mul_f32 v190, v223, v190\n"
      "v_mfma_f32_16x16x32_bf16 v[100:103], v[218:221], a[176:179], v[100:103]\n"
      "v_fmac_f32 v137, v185, v185\n"
      "v_fmac_f32 v217, v148, v148\n"
      "v_mfma_f32_16x16x32_bf16 v[100:103], v[166:169], a[180:183], v[100:103]\n"
      "v_mul_f32 v177, v223, v177\n"
      "v_mul_f32 v113, v81, v113\n"
      "v_mfma_f32_16x16x32_bf16 v[100:103], v[186:189], a[184:187], v[100:103]\n"
      "ds_read_b128 v[116:119], v0 offset:10240\n"
      "v_fmac_f32 v217, v149, v149\n"
      "v_mfma_f32_16x16x32_bf16 v[100:103], v[120:123], a[188:191], v[100:103]\n"
      "s_nop 0\n"
      "v_permlane16_swap_b32 v137, v217\n"
      "v_mfma_f32_16x16x32_bf16 v[108:111], v[96:99], a[192:195], 0\n"
      "ds_read_b128 v[96:99], v1 offset:10240\n"
      "v_lshlrev_b32 v84, 4, v84\n"
      "v_mfma_f32_16x16x32_bf16 v[108:111], v[142:145], a[196:199], v[108:111]\n"
      "v_add_f32 v137, v137, v217\n"
      "v_mov_b32 v222, 0x3fb9\n"
      "v_mfma_f32_16x16x32_bf16 v[108:111], v[218:221], a[200:203], v[108:111]\n"
      "ds_read_b128 v[142:145], v1 offset:16384\n"
      "v_mul_f32 v191, v223, v191\n"
      "v_mfma_f32_16x16x32_bf16 v[108:111], v[166:169], a[204:207], v[108:111]\n"
      "v_mul_f32 v193, v223, v193\n"
      "v_mul_f32 v179, v81, v179\n"
      "v_mfma_f32_16x16x32_bf16 v[108:111], v[186:189], a[208:211], v[108:111]\n"
      "s_waitcnt lgkmcnt(2)\n"
      "v_mul_f32 v140, v81, v140\n"
      "v_mov_b32 v217, v137\n"
      "v_mfma_f32_16x16x32_bf16 v[108:111], v[120:123], a[212:215], v[108:111]\n"
      "s_nop 0\n"
      "v_permlane32_swap_b32 v137, v217\n"
      "v_mfma_f32_16x16x32_bf16 v[166:169], a[0:3], v[156:159], 0\n"
      "v_add_f32 v137, v137, v217\n"
      "v_cvt_pk_bf16_f32 v112, v112, v113\n"
      "v_mfma_f32_16x16x32_bf16 v[166:169], a[4:7], v[202:205], v[166:169]\n"
      "v_cvt_pk_bf16_f32 v138, v138, v139\n"
      "v_mul_f32_e64 v176, v223, v176\n"
      "v_mfma_f32_16x16x32_bf16 v[166:169], a[8:11], v[116:119], v[166:169]\n"
      "s_waitcnt lgkmcnt(0)\n"
      "v_rsq_f32 v137, v137\n"
      "v_mfma_f32_16x16x32_bf16 v[166:169], a[12:15], v[96:99], v[166:169]\n"
      "v_mul_f32 v192, v223, v192\n"
      "v_mul_f32 v198, v223, v198\n"
      "v_mfma_f32_16x16x32_bf16 v[166:169], a[16:19], v[160:163], v[166:169]\n"
      "v_cvt_pk_bf16_f32 v139, v140, v141\n"
      "v_mov_b32 v217, v137\n"
      "v_mfma_f32_16x16x32_bf16 v[166:169], a[20:23], v[142:145], v[166:169]\n"
      "s_nop 0\n"
      "v_permlane16_swap_b32 v137, v217\n"
      "v_mfma_f32_16x16x32_bf16 v[120:123], a[24:27], v[156:159], 0\n"
      "ds_write_b16 v12, v164 offset:16\n"
      "v_mfma_f32_16x16x32_bf16 v[120:123], a[28:31], v[202:205], v[120:123]\n"
      "v_mul_f32 v137, s50, v137\n"
      "v_mul_f32 v178, v81, v178\n"
      "v_mfma_f32_16x16x32_bf16 v[120:123], a[32:35], v[116:119], v[120:123]\n"
      "v_mul_f32 v147, v217, v147\n"
      "v_fma_f32 v128, v166, v166, s51\n"
      "v_mfma_f32_16x16x32_bf16 v[120:123], a[36:39], v[96:99], v[120:123]\n"
      "v_cvt_pk_bf16_f32 v216, v132, v133\n"
      "v_mul_f32 v199, v223, v199\n"
      "v_mfma_f32_16x16x32_bf16 v[120:123], a[40:43], v[160:163], v[120:123]\n"
      "s_or_b32 s95, s95, s48\n"
      "v_fmac_f32 v128, v167, v167\n"
      "v_cvt_pk_bf16_f32 v190, v190, v191\n"
      "v_mfma_f32_16x16x32_bf16 v[120:123], a[44:47], v[142:145], v[120:123]\n"
      "v_cvt_pk_bf16_f32 v140, v178, v179\n"
      "v_mul_f32 v171, v137, v171\n"
      "v_mul_f32 v154, v217, v154\n"
      "v_fmac_f32 v128, v168, v168\n"
      "v_cvt_pk_bf16_f32 v196, v108, v109\n"
      "v_pk_mul_f32 v[206:207], v[206:207], v[216:217] op_sel:[0,1]\n"
      "v_pk_mul_f32 v[114:115], v[114:115], v[80:81] op_sel:[0,1]\n"
      "v_pk_mul_f32 v[200:201], v[200:201], v[222:223] op_sel:[0,1]\n"
      "v_pk_mul_f32 v[180:181], v[180:181], v[80:81] op_sel:[0,1]\n"
      "v_pk_mul_f32 v[208:209], v[208:209], v[216:217] op_sel:[0,1]\n"
      "v_pk_mul_f32 v[152:153], v[152:153], v[216:217] op_sel:[0,1]\n"
      "v_pk_mul_f32 v[182:183], v[182:183], v[136:137] op_sel:[0,1]\n"
      "v_pk_mul_f32 v[106:107], v[106:107], v[136:137] op_sel:[0,1]\n"
      "v_pk_mul_f32 v[104:105], v[104:105], v[136:137] op_sel:[0,1]\n"
      "v_mfma_f32_16x16x32_bf16 v[124:127], a[48:51], v[156:159], 0\n"
      "s_add_u32 s73, s73, 1024\n"
      "v_cvt_pk_bf16_f32 v174, v174, v175\n"
      "v_fmac_f32 v128, v169, v169\n"
      "v_mfma_f32_16x16x32_bf16 v[124:127], a[52:55], v[202:205], v[124:127]\n"
      "v_mul_f32 v185, v137, v185\n"
      "v_cmp_gt_u32 s[72:73], 64, v84\n"
      "v_mfma_f32_16x16x32_bf16 v[124:127], a[56:59], v[116:119], v[124:127]\n"
      "v_mul_f32 v155, v217, v155\n"
      "v_fmac_f32 v128, v120, v120\n"
      "v_mfma_f32_16x16x32_bf16 v[124:127], a[60:63], v[96:99], v[124:127]\n"
      "v_cvt_pk_bf16_f32 v141, v180, v181\n"
      "v_mul_f32 v146, v217, v146\n"
      "v_mfma_f32_16x16x32_bf16 v[124:127], a[64:67], v[160:163], v[124:127]\n"
      "v_cvt_pk_bf16_f32 v175, v176, v177\n"
      "v_mul_f32 v172, v137, v172\n"
      "v_mfma_f32_16x16x32_bf16 v[124:127], a[68:71], v[142:145], v[124:127]\n"
      "v_cvt_pk_bf16_f32 v177, v200, v201\n"
      "v_and_b32 v93, 2, v82\n"
      "v_mfma_f32_16x16x32_bf16 v[178:181], a[72:75], v[156:159], 0\n"
      "v_cvt_pk_bf16_f32 v191, v192, v193\n"
      "v_fmac_f32 v128, v121, v121\n"
      "v_mfma_f32_16x16x32_bf16 v[178:181], a[76:79], v[202:205], v[178:181]\n"
      "v_cvt_pk_bf16_f32 v197, v110, v111\n"
      "v_cvt_pk_bf16_f32 v133, v102, v103\n"
      "v_mfma_f32_16x16x32_bf16 v[178:181], a[80:83], v[116:119], v[178:181]\n"
      "v_mul_f32 v170, v137, v170\n"
      "v_fmac_f32 v128, v122, v122\n"
      "v_mfma_f32_16x16x32_bf16 v[178:181], a[84:87], v[96:99], v[178:181]\n"
      "v_mul_f32 v173, v137, v173\n"
      "v_mul_f32 v148, v217, v148\n"
      "v_mfma_f32_16x16x32_bf16 v[178:181], a[88:91], v[160:163], v[178:181]\n"
      "v_mul_f32 v149, v217, v149\n"
      "v_cvt_pk_bf16_f32 v176, v198, v199\n"
      "v_mfma_f32_16x16x32_bf16 v[178:181], a[92:95], v[142:145], v[178:181]\n"
      "s_and_b32 s86, s86, -4\n"
      "v_add_u32 v39, s77, v39\n"
      "v_fmac_f32 v128, v123, v123\n"
      "v_mfma_f32_16x16x32_bf16 v[218:221], a[96:99], v[156:159], 0\n"
      "v_cvt_pk_bf16_f32 v182, v182, v183\n"
      "v_cvt_pk_bf16_f32 v113, v114, v115\n"
      "v_mfma_f32_16x16x32_bf16 v[218:221], a[100:103], v[202:205], v[218:221]\n"
      "s_cmp_ge_u32 s95, 0x40000000\n"
      "s_cselect_b32 s94, -1, -2\n"
      "v_fma_f32 v81, v178, v178, s51\n"
      "v_mfma_f32_16x16x32_bf16 v[218:221], a[104:107], v[116:119], v[218:221]\n"
      "v_mov_b32 v223, 0\n"
      "v_mul_f32 v184, v137, v184\n"
      "v_mfma_f32_16x16x32_bf16 v[218:221], a[108:111], v[96:99], v[218:221]\n"
      "v_fmac_f32 v81, v179, v179\n"
      "v_fmac_f32 v81, v180, v180\n"
      "v_mfma_f32_16x16x32_bf16 v[218:221], a[112:115], v[160:163], v[218:221]\n"
      "v_fmac_f32_e64 v128, v124, v124\n"
      "v_cvt_pk_bf16_f32 v152, v152, v153\n"
      "v_mfma_f32_16x16x32_bf16 v[218:221], a[116:119], v[142:145], v[218:221]\n"
      "v_cvt_pk_bf16_f32 v132, v100, v101\n"
      "v_and_b32_e64 v82, -4, v82\n"
      "v_mfma_f32_16x16x32_bf16 v[108:111], a[120:123], v[156:159], 0\n"
      "v_fmac_f32_e64 v128, v125, v125\n"
      "v_cvt_pk_bf16_f32 v153, v154, v155\n"
      "v_mfma_f32_16x16x32_bf16 v[108:111], a[124:127], v[202:205], v[108:111]\n"
      "v_cvt_pk_bf16_f32 v170, v170, v171\n"
      "v_cvt_pk_bf16_f32 v154, v206, v207\n"
      "v_mfma_f32_16x16x32_bf16 v[108:111], a[128:131], v[116:119], v[108:111]\n"
      "v_fmac_f32_e64 v81, v181, v181\n"
      "v_cvt_pk_bf16_f32 v217, v134, v135\n"
      "v_mfma_f32_16x16x32_bf16 v[108:111], a[132:135], v[96:99], v[108:111]\n"
      "ds_read_b128 v[186:189], v7 offset:18432\n"
      "v_fmac_f32_e64 v128, v126, v126\n"
      "v_mfma_f32_16x16x32_bf16 v[108:111], a[136:139], v[160:163], v[108:111]\n"
      "v_cvt_pk_bf16_f32 v171, v172, v173\n"
      "v_cvt_pk_bf16_f32 v173, v106, v107\n"
      "v_mfma_f32_16x16x32_bf16 v[108:111], a[140:143], v[142:145], v[108:111]\n"
      "v_cvt_pk_bf16_f32 v155, v208, v209\n"
      "v_fmac_f32 v81, v218, v218\n"
      "v_mfma_f32_16x16x32_bf16 v[206:209], v[156:159], a[144:147], 0\n"
      "s_waitcnt vmcnt(7)\n"
      "v_cvt_pk_bf16_f32 v146, v146, v147\n"
      "v_fmac_f32 v81, v219, v219\n"
      "v_mfma_f32_16x16x32_bf16 v[206:209], v[202:205], a[148:151], v[206:209]\n"
      "v_fmac_f32 v81, v220, v220\n"
      "v_cvt_pk_bf16_f32 v183, v184, v185\n"
      "v_mfma_f32_16x16x32_bf16 v[206:209], v[116:119], a[152:155], v[206:209]\n"
      "s_cmp_gt_u32 s42, s44\n"
      "s_cselect_b32 s95, s94, s95\n"
      "v_fmac_f32_e64 v81, v221, v221\n"
      "v_mfma_f32_16x16x32_bf16 v[206:209], v[96:99], a[156:159], v[206:209]\n"
      "v_lshlrev_b64 v[222:223], v84, v[222:223]\n"
      "v_cvt_pk_bf16_f32 v172, v104, v105\n"
      "v_mfma_f32_16x16x32_bf16 v[206:209], v[160:163], a[160:163], v[206:209]\n"
      "v_add_u32 v39, 115296, v39\n"
      "v_fmac_f32_e64 v81, v108, v108\n"
      "v_mfma_f32_16x16x32_bf16 v[206:209], v[142:145], a[164:167], v[206:209]\n"
      "v_fmac_f32 v81, v109, v109\n"
      "v_fmac_f32 v128, v127, v127\n"
      "v_mfma_f32_16x16x32_bf16 v[104:107], v[156:159], a[168:171], 0\n"
      "v_cvt_pk_bf16_f32 v147, v148, v149\n"
      "v_fmac_f32 v81, v110, v110\n"
      "v_mfma_f32_16x16x32_bf16 v[104:107], v[202:205], a[172:175], v[104:107]\n"
      "ds_read_b128 v[100:103], v2 offset:18432\n"
      "v_fmac_f32 v81, v111, v111\n"
      "v_mfma_f32_16x16x32_bf16 v[104:107], v[116:119], a[176:179], v[104:107]\n"
      "s_nop 0\n"
      "v_permlane16_swap_b32 v128, v81\n"
      "v_mfma_f32_16x16x32_bf16 v[104:107], v[96:99], a[180:183], v[104:107]\n"
      "v_add_f32 v128, v128, v81\n"
      "v_mov_b32 v81, v128\n"
      "v_mfma_f32_16x16x32_bf16 v[104:107], v[160:163], a[184:187], v[104:107]\n"
      "s_nop 0\n"
      "v_permlane32_swap_b32 v128, v81\n"
      "v_mfma_f32_16x16x32_bf16 v[104:107], v[142:145], a[188:191], v[104:107]\n"
      "ds_read_b128 v[198:201], v2 offset:21504\n"
      "v_add_f32_e64 v128, v128, v81\n"
      "v_mfma_f32_16x16x32_bf16 v[134:137], v[156:159], a[192:195], 0\n"
      "s_waitcnt lgkmcnt(2)\n"
      "v_rsq_f32 v128, v128\n"
      "v_mfma_f32_16x16x32_bf16 v[134:137], v[202:205], a[196:199], v[134:137]\n"
      "ds_read_b128 v[202:205], v2 offset:24576\n"
      "v_mov_b32 v81, v128\n"
      "v_mfma_f32_16x16x32_bf16 v[134:137], v[116:119], a[200:203], v[134:137]\n"
      "s_nop 0\n"
      "v_permlane16_swap_b32_e64 v128, v81\n"
      "v_mfma_f32_16x16x32_bf16 v[134:137], v[96:99], a[204:207], v[134:137]\n"
      "ds_read_b128 v[156:159], v7 offset:22528\n"
      "v_mul_f32_e64 v128, s50, v128\n"
      "v_mfma_f32_16x16x32_bf16 v[134:137], v[160:163], a[208:211], v[134:137]\n"
      "buffer_store_dwordx4 v[186:189], v20, s[24:27], s46 offen sc1\n"
      "v_pk_mul_f32 v[178:179], v[178:179], v[80:81] op_sel:[0,1]\n"
      "v_pk_mul_f32 v[220:221], v[220:221], v[80:81] op_sel:[0,1]\n"
      "v_pk_mul_f32 v[110:111], v[110:111], v[80:81] op_sel:[0,1]\n"
      "v_pk_mul_f32 v[218:219], v[218:219], v[80:81] op_sel:[0,1]\n"
      "v_pk_mul_f32 v[108:109], v[108:109], v[80:81] op_sel:[0,1]\n"
      "v_pk_mul_f32 v[124:125], v[124:125], v[128:129] op_sel_hi:[1,0]\n"
      "v_pk_mul_f32 v[166:167], v[166:167], v[128:129] op_sel_hi:[1,0]\n"
      "v_pk_mul_f32 v[180:181], v[180:181], v[80:81] op_sel:[0,1]\n"
      "v_mfma_f32_16x16x32_bf16 v[134:137], v[142:145], a[212:215], v[134:137]\n"
      "s_waitcnt lgkmcnt(1)\n"
      "v_cndmask_b32_e64 v222, 0, v222, s[72:73]\n"
      "v_cvt_pk_bf16_f32 v81, v106, v107\n"
      "v_mfma_f32_16x16x32_bf16 v[186:189], a[216:219], v[100:103], 0\n"
      "ds_read_b128 v[162:165], v2 offset:27648\n"
      "v_add_u32 v160, 110592, v82\n"
      "v_mfma_f32_16x16x32_bf16 v[186:189], a[220:223], v[198:201], v[186:189]\n"
      "s_waitcnt lgkmcnt(1)\n"
      "v_cvt_pk_bf16_f32 v80, v104, v105\n"
      "v_cvt_pk_bf16_f32 v178, v178, v179\n"
      "v_mfma_f32_16x16x32_bf16 v[186:189], a[224:227], v[202:205], v[186:189]\n"
      "ds_read2_b32 v[184:185], v160 offset0:0 offset1:1\n"
      "v_cndmask_b32_e64 v223, 0, v223, s[72:73]\n"
      "v_mfma_f32_16x16x32_bf16 v[142:145], a[240:243], v[100:103], 0\n"
      "ds_read_b128 v[104:107], v2 offset:33792\n"
      "v_cvt_pk_bf16_f32 v85, v136, v137\n"
      "v_mfma_f32_16x16x32_bf16 v[142:145], a[244:247], v[198:201], v[142:145]\n"
      "ds_read_b128 v[114:117], v2 offset:30720\n"
      "v_cvt_pk_bf16_f32 v179, v180, v181\n"
      "v_mfma_f32_16x16x32_bf16 v[142:145], a[248:251], v[202:205], v[142:145]\n"
      "buffer_store_dwordx4 v[156:159], v21, s[24:27], s46 offen sc1\n"
      "v_pk_mul_f32 v[126:127], v[126:127], v[128:129] op_sel_hi:[1,0]\n"
      "v_pk_mul_f32 v[168:169], v[168:169], v[128:129] op_sel_hi:[1,0]\n"
      "v_pk_mul_f32 v[122:123], v[122:123], v[128:129] op_sel_hi:[1,0]\n"
      "v_pk_mul_f32 v[120:121], v[120:121], v[128:129] op_sel_hi:[1,0]\n"
      "v_mfma_f32_16x16x32_bf16 v[156:159], v[232:235], v[100:103], 0\n"
      "ds_read2_b32 v[136:137], v160 offset0:24 offset1:18\n"
      "v_cvt_pk_bf16_f32 v124, v124, v125\n"
      "v_mfma_f32_16x16x32_bf16 v[156:159], v[236:239], v[198:201], v[156:159]\n"
      "ds_read_b128 v[100:103], v7 offset:26624\n"
      "v_cvt_pk_bf16_f32 v166, v166, v167\n"
      "v_mfma_f32_16x16x32_bf16 v[156:159], v[240:243], v[202:205], v[156:159]\n"
      "s_waitcnt lgkmcnt(2)\n"
      "v_alignbyte_b32 v184, v185, v184, v93\n"
      "v_cvt_pk_bf16_f32 v77, v208, v209\n"
      "v_mfma_f32_16x16x32_bf16 v[186:189], a[228:231], v[162:165], v[186:189]\n"
      "ds_read_b64 v[128:129], v10 offset:24\n"
      "v_cvt_pk_bf16_f32 v108, v108, v109\n"
      "v_mfma_f32_16x16x32_bf16 v[186:189], a[232:235], v[114:117], v[186:189]\n"
      "ds_read2_b32 v[98:99], v160 offset0:2 offset1:10\n"
      "v_cvt_pk_bf16_f32 v76, v206, v207\n"
      "v_mfma_f32_16x16x32_bf16 v[186:189], a[236:239], v[104:107], v[186:189]\n"
      "ds_read2_b32 v[202:203], v160 offset0:8 offset1:9\n"
      "v_cvt_pk_bf16_f32 v125, v126, v127\n"
      "v_mfma_f32_16x16x32_bf16 v[142:145], a[252:255], v[162:165], v[142:145]\n"
      "ds_read_b128 v[206:209], v7 offset:30720\n"
      "v_cvt_pk_bf16_f32 v180, v218, v219\n"
      "v_mfma_f32_16x16x32_bf16 v[142:145], v[224:227], v[114:117], v[142:145]\n"
      "s_waitcnt lgkmcnt(4)\n"
      "v_cvt_pk_bf16_f32 v181, v220, v221\n"
      "v_cvt_pk_bf16_f32 v167, v168, v169\n"
      "v_mfma_f32_16x16x32_bf16 v[142:145], v[228:231], v[104:107], v[142:145]\n"
      "ds_read2_b32 v[204:205], v160 offset0:16 offset1:17\n"
      "v_fma_f32 v160, v186, v186, s52\n"
      "v_mfma_f32_16x16x32_bf16 v[156:159], v[244:247], v[162:165], v[156:159]\n"
      "ds_read_b128 v[162:165], v10 offset:32\n"
      "v_add_u32 v89, 112160, v82\n"
      "v_mfma_f32_16x16x32_bf16 v[156:159], v[248:251], v[114:117], v[156:159]\n"
      "buffer_store_dwordx4 v[100:103], v22, s[24:27], s46 offen sc1\n"
      "v_mfma_f32_16x16x32_bf16 v[156:159], v[252:255], v[104:107], v[156:159]\n"
      "ds_read_u16 v39, v39\n"
      "v_mov_b32 v127, 0\n"
      "v_mfma_f32_16x16x32_bf16 v[148:151], v[138:141], v[174:177], v[40:43]\n"
      "s_waitcnt lgkmcnt(3)\n"
      "v_cmp_gt_u32 s[72:73], 64, v84\n"
      "v_alignbyte_b32 v185, v98, v185, v93\n"
      "v_mfma_f32_16x16x32_bf16 v[100:103], v[152:155], v[174:177], v[44:47]\n"
      "s_waitcnt lgkmcnt(2)\n"
      "v_mov_b32 v126, 0xbb2b\n"
      "v_cvt_pk_bf16_f32 v109, v110, v111\n"
      "v_mfma_f32_16x16x32_bf16 v[114:117], v[178:181], v[174:177], v[48:51]\n"
      "s_waitcnt lgkmcnt(1)\n"
      "v_alignbyte_b32 v204, v205, v204, v93\n"
      "v_lshlrev_b64 v[126:127], v84, v[126:127]\n"
      "v_mfma_f32_16x16x32_bf16 v[198:201], v[162:165], v[174:177], v[24:27]\n"
      "ds_read_b128 v[218:221], v7 offset:34816\n"
      "v_alignbyte_b32 v202, v203, v202, v93\n"
      "v_mfma_f32_16x16x16_bf16 v[148:151], v[112:113], v[190:191], v[148:151]\n"
      "v_cvt_pk_bf16_f32 v169, v122, v123\n"
      "v_alignbyte_b32 v205, v137, v205, v93\n"
      "v_mul_f32 v161, v187, v187\n"
      "v_mfma_f32_16x16x16_bf16 v[100:103], v[146:147], v[190:191], v[100:103]\n"
      "ds_read2_b32 v[192:193], v89 offset0:8 offset1:9\n"
      "v_alignbyte_b32 v203, v99, v203, v93\n"
      "v_mfma_f32_16x16x16_bf16 v[114:117], v[108:109], v[190:191], v[114:117]\n"
      "buffer_store_dwordx4 v[206:209], v23, s[24:27], s46 offen sc1\n"
      "v_mfma_f32_16x16x16_bf16 v[198:201], v[128:129], v[190:191], v[198:201]\n"
      "s_nop 0\n"
      "ds_read_b128 v[206:209], v2 offset:18688\n"
      "v_add_u32 v190, 113728, v82\n"
      "v_mfma_f32_16x16x16_bf16 v[148:151], v[222:223], v[184:185], v[148:151]\n"
      "v_cndmask_b32_e64 v126, 0, v126, s[72:73]\n"
      "v_fmac_f32_e64 v161, v189, v189\n"
      "v_alignbyte_b32 v136, v136, v136, v93\n"
      "v_mfma_f32_16x16x16_bf16 v[100:103], v[222:223], v[202:203], v[100:103]\n"
      "ds_read_b128 v[174:177], v2 offset:21760\n"
      "v_cvt_pk_bf16_f32 v168, v120, v121\n"
      "v_mfma_f32_16x16x16_bf16 v[114:117], v[222:223], v[204:205], v[114:117]\n"
      "v_cndmask_b32_e64 v127, 0, v127, s[72:73]\n"
      "v_fmac_f32_e64 v160, v188, v188\n"
      "v_cvt_pk_bf16_f32 v84, v134, v135\n"
      "v_mfma_f32_16x16x16_bf16 v[198:201], v[222:223], v[136:137], v[198:201]\n"
      "ds_read_b128 v[96:99], v2 offset:24832\n"
      "v_add_u32_e64 v12, 32, v12\n"
      "v_mfma_f32_16x16x16_bf16 v[148:151], v[126:127], v[184:185], v[148:151]\n"
      "ds_read2_b32 v[134:135], v89 offset0:2 offset1:10\n"
      "v_fmac_f32 v160, v142, v142\n"
      "v_mfma_f32_16x16x16_bf16 v[100:103], v[126:127], v[202:203], v[100:103]\n"
      "s_waitcnt lgkmcnt(3)\n"
      "v_lshlrev_b32 v39, 16, v39\n"
      "v_fmac_f32 v161, v143, v143\n"
      "v_mfma_f32_16x16x16_bf16 v[114:117], v[126:127], v[204:205], v[114:117]\n"
      "buffer_store_dwordx4 v[218:221], v25, s[24:27], s46 offen sc1\n"
      "v_pk_fma_f32 v[160:161], v[144:145], v[144:145], v[160:161]\n"
      "v_mfma_f32_16x16x16_bf16 v[198:201], v[126:127], v[136:137], v[198:201]\n"
      "ds_read2_b32 v[202:203], v89 offset0:0 offset1:1\n"
      "v_alignbyte_b32 v192, v193, v192, v93\n"
      "v_mfma_f32_16x16x32_bf16 v[120:123], a[216:219], v[206:209], 0\n"
      "s_waitcnt lgkmcnt(2)\n"
      "v_max3_f32 v185, v148, v149, v150\n"
      "v_max3_f32 v185, v185, v102, v103\n"
      "v_mfma_f32_16x16x32_bf16 v[120:123], a[220:223], v[174:177], v[120:123]\n"
      "s_waitcnt lgkmcnt(1)\n"
      "v_max3_f32 v200, v151, v100, v101\n"
      "v_max3_f32 v200, v200, v114, v115\n"
      "v_mfma_f32_16x16x32_bf16 v[120:123], a[224:227], v[96:99], v[120:123]\n"
      "v_alignbyte_b32 v193, v135, v193, v93\n"
      "v_max3_f32 v185, v185, v116, v117\n"
      "v_mfma_f32_16x16x32_bf16 v[218:221], a[240:243], v[206:209], 0\n"
      "v_max3_f32 v185, v185, v200, v198\n"
      "v_mov_b32_e64 v135, v185\n"
      "v_mfma_f32_16x16x32_bf16 v[218:221], a[244:247], v[174:177], v[218:221]\n"
      "s_nop 0\n"
      "v_permlane16_swap_b32 v185, v135\n"
      "v_mfma_f32_16x16x32_bf16 v[218:221], a[248:251], v[96:99], v[218:221]\n"
      "s_waitcnt lgkmcnt(0)\n"
      "v_max_f32 v185, v185, v135\n"
      "s_waitcnt vmcnt(6)\n"
      "v_or3_b32 v210, v210, v211, v212\n"
      "v_mfma_f32_16x16x32_bf16 v[104:107], v[232:235], v[206:209], 0\n"
      "ds_read2_b32 v[200:201], v89 offset0:16 offset1:17\n"
      "v_mov_b32 v135, v185\n"
      "v_mfma_f32_16x16x32_bf16 v[104:107], v[236:239], v[174:177], v[104:107]\n"
      "s_nop 0\n"
      "v_permlane32_swap_b32 v185, v135\n"
      "v_mfma_f32_16x16x32_bf16 v[104:107], v[240:243], v[96:99], v[104:107]\n"
      "ds_read2_b32 v[98:99], v89 offset0:24 offset1:18\n"
      "v_alignbyte_b32 v202, v203, v202, v93\n"
      "v_alignbyte_b32 v203, v134, v203, v93\n"
      "v_max_f32 v185, v185, v135\n"
      "v_sub_f32 v100, v100, v185\n"
      "v_pk_add_f32 v[150:151], v[150:151], v[184:185] op_sel:[0,1] neg_lo:[0,1] neg_hi:[0,1]\n"
      "v_pk_add_f32 v[148:149], v[148:149], v[184:185] op_sel:[0,1] neg_lo:[0,1] neg_hi:[0,1]\n"
      "v_pk_add_f32 v[102:103], v[102:103], v[184:185] op_sel:[0,1] neg_lo:[0,1] neg_hi:[0,1]\n"
      "v_pk_add_f32 v[116:117], v[116:117], v[184:185] op_sel:[0,1] neg_lo:[0,1] neg_hi:[0,1]\n"
      "v_pk_add_f32 v[114:115], v[114:115], v[184:185] op_sel:[0,1] neg_lo:[0,1] neg_hi:[0,1]\n"
      "v_mfma_f32_16x16x32_bf16 v[174:177], v[138:141], v[170:173], v[52:55]\n"
      "v_exp_f32 v149, v149\n"
      "s_waitcnt vmcnt(5)\n"
      "v_or3_b32 v210, v210, v213, v78\n"
      "v_mfma_f32_16x16x32_bf16 v[206:209], v[152:155], v[170:173], v[56:59]\n"
      "ds_write_b32 v31, v210 offset:4608\n"
      "v_mfma_f32_16x16x32_bf16 v[210:213], v[178:181], v[170:173], v[60:63]\n"
      "v_exp_f32 v148, v148\n"
      "v_mfma_f32_16x16x32_bf16 v[134:137], v[162:165], v[170:173], v[26:29]\n"
      "s_waitcnt lgkmcnt(1)\n"
      "v_exp_f32 v114, v114\n"
      "v_mfma_f32_16x16x16_bf16 v[174:177], v[112:113], v[182:183], v[174:177]\n"
      "v_alignbyte_b32 v200, v201, v200, v93\n"
      "v_exp_f32 v150, v150\n"
      "v_mfma_f32_16x16x16_bf16 v[206:209], v[146:147], v[182:183], v[206:209]\n"
      "v_exp_f32 v116, v116\n"
      "v_cvt_pk_bf16_f32 v148, v148, v149\n"
      "v_mfma_f32_16x16x16_bf16 v[210:213], v[108:109], v[182:183], v[210:213]\n"
      "v_sub_f32 v101, v101, v185\n"
      "v_exp_f32 v103, v103\n"
      "v_mfma_f32_16x16x16_bf16 v[134:137], v[128:129], v[182:183], v[134:137]\n"
      "v_exp_f32 v117, v117\n"
      "v_alignbyte_b32 v201, v99, v201, v93\n"
      "v_mfma_f32_16x16x16_bf16 v[174:177], v[222:223], v[202:203], v[174:177]\n"
      "v_alignbyte_b32 v98, v98, v98, v93\n"
      "v_exp_f32 v101, v101\n"
      "v_mfma_f32_16x16x16_bf16 v[206:209], v[222:223], v[192:193], v[206:209]\n"
      "v_exp_f32 v102, v102\n"
      "v_exp_f32 v151, v151\n"
      "v_mfma_f32_16x16x16_bf16 v[210:213], v[222:223], v[200:201], v[210:213]\n"
      "v_sub_f32 v198, v198, v185\n"
      "v_exp_f32_e64 v115, v115\n"
      "v_mfma_f32_16x16x16_bf16 v[134:137], v[222:223], v[98:99], v[134:137]\n"
      "ds_read_b128 v[182:185], v2 offset:30976\n"
      "v_cvt_pk_bf16_f32 v89, v116, v117\n"
      "v_mfma_f32_16x16x16_bf16 v[174:177], v[126:127], v[202:203], v[174:177]\n"
      "ds_read_b128 v[170:173], v2 offset:27904\n"
      "v_cvt_pk_bf16_f32 v149, v150, v151\n"
      "v_mfma_f32_16x16x16_bf16 v[206:209], v[126:127], v[192:193], v[206:209]\n"
      "v_exp_f32 v198, v198\n"
      "v_cvt_pk_bf16_f32 v88, v114, v115\n"
      "v_mfma_f32_16x16x16_bf16 v[210:213], v[126:127], v[200:201], v[210:213]\n"
      "ds_read_b128 v[200:203], v2 offset:34048\n"
      "v_exp_f32 v100, v100\n"
      "v_mfma_f32_16x16x16_bf16 v[134:137], v[126:127], v[98:99], v[134:137]\n"
      "v_max3_f32 v115, v174, v175, v176\n"
      "v_cvt_pk_bf16_f32 v150, v100, v101\n"
      "v_max3_f32 v115, v115, v208, v209\n"
      "v_cvt_pk_bf16_f32 v151, v102, v103\n"
      "v_max3_f32 v103, v177, v206, v207\n"
      "v_max3_f32 v115, v115, v212, v213\n"
      "v_pk_fma_f32 v[160:161], v[156:157], v[156:157], v[160:161]\n"
      "v_pk_fma_f32 v[160:161], v[158:159], v[158:159], v[160:161]\n"
      "v_max3_f32 v103, v103, v210, v211\n"
      "v_max3_f32 v115, v115, v103, v134\n"
      "v_mov_b32 v101, v115\n"
      "s_waitcnt lgkmcnt(1)\n"
      "v_mfma_f32_16x16x32_bf16 v[120:123], a[228:231], v[170:173], v[120:123]\n"
      "v_permlane16_swap_b32 v115, v101\n"
      "v_mfma_f32_16x16x32_bf16 v[120:123], a[232:235], v[182:185], v[120:123]\n"
      "ds_read_u16 v78, v11\n"
      "s_waitcnt lgkmcnt(1)\n"
      "v_mfma_f32_16x16x32_bf16 v[120:123], a[236:239], v[200:203], v[120:123]\n"
      "v_add_f32 v160, v160, v161\n"
      "v_max_f32 v115, v115, v101\n"
      "v_mfma_f32_16x16x32_bf16 v[218:221], a[252:255], v[170:173], v[218:221]\n"
      "ds_read2_b32 v[102:103], v190 offset0:16 offset1:17\n"
      "v_mov_b32 v101, v115\n"
      "v_mfma_f32_16x16x32_bf16 v[218:221], v[224:227], v[182:185], v[218:221]\n"
      "ds_read_u16 v82, v11 offset:32\n"
      "v_cvt_pk_bf16_f32 v90, v198, 0\n"
      "v_permlane32_swap_b32 v115, v101\n"
      "v_max_f32_e64 v115, v115, v101\n"
      "v_pk_add_f32 v[206:207], v[206:207], v[114:115] op_sel:[0,1] neg_lo:[0,1] neg_hi:[0,1]\n"
      "v_pk_add_f32 v[208:209], v[208:209], v[114:115] op_sel:[0,1] neg_lo:[0,1] neg_hi:[0,1]\n"
      "v_pk_add_f32 v[212:213], v[212:213], v[114:115] op_sel:[0,1] neg_lo:[0,1] neg_hi:[0,1]\n"
      "v_pk_add_f32 v[176:177], v[176:177], v[114:115] op_sel:[0,1] neg_lo:[0,1] neg_hi:[0,1]\n"
      "v_pk_add_f32 v[174:175], v[174:175], v[114:115] op_sel:[0,1] neg_lo:[0,1] neg_hi:[0,1]\n"
      "v_mfma_f32_16x16x32_bf16 v[218:221], v[228:231], v[200:203], v[218:221]\n"
      "v_exp_f32_e64 v176, v176\n"
      "v_mfma_f32_16x16x32_bf16 v[104:107], v[244:247], v[170:173], v[104:107]\n"
      "s_waitcnt lgkmcnt(2)\n"
      "v_exp_f32 v174, v174\n"
      "v_mfma_f32_16x16x32_bf16 v[104:107], v[248:251], v[182:185], v[104:107]\n"
      "ds_read2_b32 v[100:101], v190 offset0:0 offset1:1\n"
      "v_exp_f32 v175, v175\n"
      "v_mfma_f32_16x16x32_bf16 v[104:107], v[252:255], v[200:203], v[104:107]\n"
      "ds_read2_b32 v[136:137], v190 offset0:24 offset1:18\n"
      "v_sub_f32 v134, v134, v115\n"
      "v_mfma_f32_16x16x32_bf16 v[182:185], v[32:35], v[148:151], 0\n"
      "ds_read_u16 v86, v11 offset:64\n"
      "v_cvt_pk_bf16_f32 v174, v174, v175\n"
      "v_mfma_f32_16x16x32_bf16 v[182:185], v[32:35], v[88:91], v[182:185]\n"
      "ds_read2_b32 v[192:193], v190 offset0:2 offset1:10\n"
      "v_exp_f32 v209, v209\n"
      "v_exp_f32 v177, v177\n"
      "v_pk_add_f32 v[210:211], v[210:211], v[114:115] op_sel:[0,1] neg_lo:[0,1] neg_hi:[0,1]\n"
      "v_mfma_f32_16x16x32_bf16 v[170:173], v[214:217], v[148:151], 0\n"
      "ds_read2_b32 v[198:199], v190 offset0:8 offset1:9\n"
      "v_cvt_pk_bf16_f32 v175, v176, v177\n"
      "v_mfma_f32_16x16x32_bf16 v[170:173], v[76:79], v[88:91], v[170:173]\n"
      "v_exp_f32 v208, v208\n"
      "v_mfma_f32_16x16x32_bf16 v[114:117], v[130:133], v[148:151], 0\n"
      "s_waitcnt lgkmcnt(3)\n"
      "v_cvt_pk_bf16_f32 v177, v208, v209\n"
      "v_alignbyte_b32 v136, v136, v136, v93\n"
      "v_mfma_f32_16x16x32_bf16 v[114:117], v[80:83], v[88:91], v[114:117]\n"
      "v_exp_f32_e64 v207, v207\n"
      "v_mfma_f32_16x16x32_bf16 v[200:203], v[194:197], v[148:151], 0\n"
      "s_waitcnt lgkmcnt(1)\n"
      "v_exp_f32_e64 v206, v206\n"
      "v_mfma_f32_16x16x32_bf16 v[200:203], v[84:87], v[88:91], v[200:203]\n"
      "v_cvt_pk_bf16_f32 v176, v206, v207\n"
      "v_alignbyte_b32 v100, v101, v100, v93\n"
      "v_alignbyte_b32 v102, v103, v102, v93\n"
      "v_mfma_f32_16x16x32_bf16 v[96:99], v[138:141], v[166:169], v[64:67]\n"
      "s_waitcnt lgkmcnt(0)\n"
      "v_alignbyte_b32 v198, v199, v198, v93\n"
      "v_alignbyte_b32 v101, v192, v101, v93\n"
      "v_mfma_f32_16x16x32_bf16 v[148:151], v[152:155], v[166:169], v[68:71]\n"
      "v_alignbyte_b32 v103, v137, v103, v93\n"
      "v_alignbyte_b32 v199, v193, v199, v93\n"
      "v_mfma_f32_16x16x32_bf16 v[190:193], v[178:181], v[166:169], v[72:75]\n"
      "v_rcp_f32 v182, v182\n"
      "v_mfma_f32_16x16x32_bf16 v[204:207], v[162:165], v[166:169], v[28:31]\n"
      "v_mul_f32 v172, v182, v172\n"
      "v_mul_f32 v116, v182, v116\n"
      "v_mfma_f32_16x16x16_bf16 v[96:99], v[112:113], v[124:125], v[96:99]\n"
      "v_mul_f32 v170, v182, v170\n"
      "v_exp_f32 v210, v210\n"
      "v_mfma_f32_16x16x16_bf16 v[148:151], v[146:147], v[124:125], v[148:151]\n"
      "v_exp_f32 v134, v134\n"
      "v_mul_f32 v171, v182, v171\n"
      "v_mfma_f32_16x16x16_bf16 v[190:193], v[108:109], v[124:125], v[190:193]\n"
      "v_exp_f32 v213, v213\n"
      "v_mul_f32_e64 v173, v182, v173\n"
      "v_mfma_f32_16x16x16_bf16 v[204:207], v[128:129], v[124:125], v[204:207]\n"
      "v_cvt_pk_bf16_f32 v170, v170, v171\n"
      "v_exp_f32 v212, v212\n"
      "v_mfma_f32_16x16x16_bf16 v[96:99], v[222:223], v[100:101], v[96:99]\n"
      "v_cvt_pk_bf16_f32 v171, v172, v173\n"
      "v_exp_f32 v211, v211\n"
      "v_mfma_f32_16x16x16_bf16 v[148:151], v[222:223], v[198:199], v[148:151]\n"
      "v_mul_f32_e64 v111, v121, v121\n"
      "v_cvt_pk_bf16_f32 v93, v212, v213\n"
      "v_fma_f32 v110, v120, v120, s52\n"
      "v_mfma_f32_16x16x16_bf16 v[190:193], v[222:223], v[102:103], v[190:193]\n"
      "v_cvt_pk_bf16_f32 v92, v210, v211\n"
      "v_mul_f32 v115, v182, v115\n"
      "v_fmac_f32 v111, v123, v123\n"
      "v_mfma_f32_16x16x16_bf16 v[204:207], v[222:223], v[136:137], v[204:207]\n"
      "v_mul_f32 v114, v182, v114\n"
      "v_fmac_f32 v110, v122, v122\n"
      "v_mul_f32_e64 v117, v182, v117\n"
      "v_mfma_f32_16x16x16_bf16 v[96:99], v[126:127], v[100:101], v[96:99]\n"
      "ds_read_b128 v[210:213], v2 offset:25088\n"
      "v_cvt_pk_bf16_f32 v172, v114, v115\n"
      "v_mfma_f32_16x16x16_bf16 v[148:151], v[126:127], v[198:199], v[148:151]\n"
      "ds_read_b128 v[166:169], v2 offset:22016\n"
      "v_fmac_f32_e64 v111, v219, v219\n"
      "v_mfma_f32_16x16x16_bf16 v[190:193], v[126:127], v[102:103], v[190:193]\n"
      "ds_read_b128 v[100:103], v2 offset:18944\n"
      "v_cvt_pk_bf16_f32 v173, v116, v117\n"
      "v_mfma_f32_16x16x16_bf16 v[204:207], v[126:127], v[136:137], v[204:207]\n"
      "v_fmac_f32_e64 v110, v218, v218\n"
      "v_cvt_pk_bf16_f32 v94, v134, 0\n"
      "v_max3_f32 v198, v96, v97, v98\n"
      "v_max3_f32 v198, v198, v150, v151\n"
      "v_max3_f32 v127, v99, v148, v149\n"
      "v_max3_f32 v198, v198, v192, v193\n"
      "v_pk_mul_f32 v[202:203], v[202:203], v[182:183] op_sel_hi:[1,0]\n"
      "v_max3_f32 v127, v127, v190, v191\n"
      "v_max3_f32 v198, v198, v127, v204\n"
      "v_mov_b32 v127, v198\n"
      "v_pk_fma_f32 v[110:111], v[220:221], v[220:221], v[110:111]\n"
      "v_pk_mul_f32 v[200:201], v[200:201], v[182:183] op_sel_hi:[1,0]\n"
      "v_permlane16_swap_b32 v198, v127\n"
      "v_max_f32 v198, v198, v127\n"
      "v_mov_b32 v127, v198\n"
      "v_pk_fma_f32 v[110:111], v[104:105], v[104:105], v[110:111]\n"
      "v_pk_fma_f32 v[110:111], v[106:107], v[106:107], v[110:111]\n"
      "v_permlane32_swap_b32_e64 v198, v127\n"
      "v_max_f32 v198, v198, v127\n"
      "v_pk_add_f32 v[98:99], v[98:99], v[198:199] op_sel_hi:[1,0] neg_lo:[0,1] neg_hi:[0,1]\n"
      "s_waitcnt lgkmcnt(0)\n"
      "v_mfma_f32_16x16x32_bf16 v[124:127], a[216:219], v[100:103], 0\n"
      "ds_write_b128 v3, v[170:173]\n"
      "v_pk_add_f32 v[96:97], v[96:97], v[198:199] op_sel_hi:[1,0] neg_lo:[0,1] neg_hi:[0,1]\n"
      "v_pk_add_f32 v[150:151], v[150:151], v[198:199] op_sel_hi:[1,0] neg_lo:[0,1] neg_hi:[0,1]\n"
      "v_pk_add_f32 v[192:193], v[192:193], v[198:199] op_sel_hi:[1,0] neg_lo:[0,1] neg_hi:[0,1]\n"
      "v_mfma_f32_16x16x32_bf16 v[124:127], a[220:223], v[166:169], v[124:127]\n"
      "v_sub_f32 v204, v204, v198\n"
      "v_cvt_pk_bf16_f32 v200, v200, v201\n"
      "v_mfma_f32_16x16x32_bf16 v[124:127], a[224:227], v[210:213], v[124:127]\n"
      "v_cvt_pk_bf16_f32 v201, v202, v203\n"
      "v_sub_f32 v148, v148, v198\n"
      "v_mfma_f32_16x16x32_bf16 v[206:209], a[240:243], v[100:103], 0\n"
      "ds_read_b128 v[170:173], v10\n"
      "v_sub_f32 v191, v191, v198\n"
      "v_mfma_f32_16x16x32_bf16 v[206:209], a[244:247], v[166:169], v[206:209]\n"
      "v_exp_f32 v193, v193\n"
      "v_mfma_f32_16x16x32_bf16 v[206:209], a[248:251], v[210:213], v[206:209]\n"
      "v_add_f32 v110, v110, v111\n"
      "v_sub_f32 v190, v190, v198\n"
      "v_mfma_f32_16x16x32_bf16 v[182:185], v[232:235], v[100:103], 0\n"
      "v_exp_f32_e64 v151, v151\n"
      "v_mfma_f32_16x16x32_bf16 v[182:185], v[236:239], v[166:169], v[182:185]\n"
      "v_exp_f32 v96, v96\n"
      "v_mfma_f32_16x16x32_bf16 v[182:185], v[240:243], v[210:213], v[182:185]\n"
      "v_exp_f32 v97, v97\n"
      "v_mfma_f32_16x16x32_bf16 v[210:213], v[32:35], v[174:177], 0\n"
      "v_exp_f32 v150, v150\n"
      "v_mfma_f32_16x16x32_bf16 v[210:213], v[32:35], v[92:95], v[210:213]\n"
      "v_exp_f32 v98, v98\n"
      "v_mfma_f32_16x16x32_bf16 v[166:169], v[214:217], v[174:177], 0\n"
      "ds_write_b64 v4, v[200:201]\n"
      "v_mfma_f32_16x16x32_bf16 v[166:169], v[76:79], v[92:95], v[166:169]\n"
      "ds_read_b64 v[202:203], v10 offset:16\n"
      "v_sub_f32 v149, v149, v198\n"
      "v_mfma_f32_16x16x32_bf16 v[198:201], v[130:133], v[174:177], 0\n"
      "s_nop 0\n"
      "v_rcp_f32 v210, v210\n"
      "v_mfma_f32_16x16x32_bf16 v[198:201], v[80:83], v[92:95], v[198:201]\n"
      "v_exp_f32 v192, v192\n"
      "v_mfma_f32_16x16x32_bf16 v[134:137], v[194:197], v[174:177], 0\n"
      "v_exp_f32_e64 v99, v99\n"
      "v_mfma_f32_16x16x32_bf16 v[134:137], v[84:87], v[92:95], v[134:137]\n"
      "s_waitcnt lgkmcnt(2)\n"
      "v_exp_f32 v191, v191\n"
      "v_mfma_f32_16x16x32_bf16 v[174:177], v[170:173], v[138:141], v[30:33]\n"
      "v_exp_f32_e64 v148, v148\n"
      "v_mfma_f32_16x16x32_bf16 v[116:119], v[170:173], v[152:155], v[30:33]\n"
      "ds_read_b128 v[152:155], v2 offset:28160\n"
      "v_cvt_pk_bf16_f32 v89, v192, v193\n"
      "v_mfma_f32_16x16x32_bf16 v[138:141], v[170:173], v[178:181], v[30:33]\n"
      "s_waitcnt lgkmcnt(1)\n"
      "v_exp_f32 v190, v190\n"
      "v_mfma_f32_16x16x32_bf16 v[100:103], v[170:173], v[162:165], v[30:33]\n"
      "ds_read_b128 v[170:173], v2 offset:31232\n"
      "v_cvt_pk_bf16_f32 v96, v96, v97\n"
      "v_mfma_f32_16x16x16_bf16 v[174:177], v[202:203], v[112:113], v[174:177]\n"
      "ds_read_b128 v[162:165], v2 offset:34304\n"
      "v_mul_f32_e64 v134, v210, v134\n"
      "v_mfma_f32_16x16x16_bf16 v[116:119], v[202:203], v[146:147], v[116:119]\n"
      "v_cvt_pk_bf16_f32 v97, v98, v99\n"
      "v_permlane16_swap_b32 v160, v110\n"
      "v_mfma_f32_16x16x16_bf16 v[138:141], v[202:203], v[108:109], v[138:141]\n"
      "v_mul_f32 v198, v210, v198\n"
      "v_cvt_pk_bf16_f32 v99, v150, v151\n"
      "v_add_f32_e64 v160, v160, v110\n"
      "v_mul_f32_e64 v135, v210, v135\n"
      "v_exp_f32_e64 v149, v149\n"
      "v_pk_mul_f32 v[166:167], v[166:167], v[210:211] op_sel_hi:[1,0]\n"
      "v_pk_mul_f32 v[200:201], v[200:201], v[210:211] op_sel_hi:[1,0]\n"
      "v_pk_mul_f32 v[168:169], v[168:169], v[210:211] op_sel_hi:[1,0]\n"
      "v_mfma_f32_16x16x16_bf16 v[100:103], v[202:203], v[128:129], v[100:103]\n"
      "v_exp_f32 v204, v204\n"
      "s_waitcnt lgkmcnt(1)\n"
      "v_mfma_f32_16x16x32_bf16 v[124:127], a[228:231], v[152:155], v[124:127]\n"
      "v_cndmask_b32 v174, v174, v116, s[58:59]\n"
      "v_cvt_pk_bf16_f32 v90, v204, 0\n"
      "v_mfma_f32_16x16x32_bf16 v[124:127], a[232:235], v[170:173], v[124:127]\n"
      "s_waitcnt lgkmcnt(0)\n"
      "v_cvt_pk_bf16_f32 v166, v166, v167\n"
      "v_cndmask_b32 v174, v174, v138, s[60:61]\n"
      "v_mfma_f32_16x16x32_bf16 v[124:127], a[236:239], v[162:165], v[124:127]\n"
      "v_cndmask_b32 v174, v174, v100, s[62:63]\n"
      "v_fmac_f32_e64 v174, s53, v39\n"
      "v_mfma_f32_16x16x32_bf16 v[206:209], a[252:255], v[152:155], v[206:209]\n"
      "s_nop 0\n"
      "v_max_f32_dpp v212, v174, v174 row_ror:8 row_mask:0xf bank_mask:0xf\n"
      "v_cvt_pk_bf16_f32 v88, v190, v191\n"
      "v_mfma_f32_16x16x32_bf16 v[206:209], v[224:227], v[170:173], v[206:209]\n"
      "v_cvt_pk_bf16_f32 v167, v168, v169\n"
      "v_fma_f32 v176, v124, v124, s52\n"
      "v_mfma_f32_16x16x32_bf16 v[206:209], v[228:231], v[162:165], v[206:209]\n"
      "v_mul_f32 v199, v210, v199\n"
      "v_max_f32_dpp v212, v212, v212 row_ror:4 row_mask:0xf bank_mask:0xf\n"
      "v_mfma_f32_16x16x32_bf16 v[182:185], v[244:247], v[152:155], v[182:185]\n"
      "s_nop 0\n"
      "v_max_f32_dpp v212, v212, v212 row_ror:2 row_mask:0xf bank_mask:0xf\n"
      "v_cvt_pk_bf16_f32 v169, v200, v201\n"
      "v_mfma_f32_16x16x32_bf16 v[182:185], v[248:251], v[170:173], v[182:185]\n"
      "v_cvt_pk_bf16_f32 v98, v148, v149\n"
      "v_max_f32_dpp v212, v212, v212 row_ror:1 row_mask:0xf bank_mask:0xf\n"
      "v_mfma_f32_16x16x32_bf16 v[182:185], v[252:255], v[162:165], v[182:185]\n"
      "s_nop 0\n"
      "v_max_f32_dpp v212, v212, v212 row_bcast:15 row_mask:0xa bank_mask:0xf\n"
      "v_cvt_pk_bf16_f32 v168, v198, v199\n"
      "v_mfma_f32_16x16x32_bf16 v[150:153], v[32:35], v[96:99], 0\n"
      "v_mul_f32 v177, v125, v125\n"
      "v_max_f32_dpp v212, v212, v212 row_bcast:31 row_mask:0xc bank_mask:0xf\n"
      "v_mfma_f32_16x16x32_bf16 v[150:153], v[32:35], v[88:91], v[150:153]\n"
      "v_cvt_pk_bf16_f32 v134, v134, v135\n"
      "v_readlane_b32 s72, v212, 63\n"
      "v_mfma_f32_16x16x32_bf16 v[170:173], v[214:217], v[96:99], 0\n"
      "ds_write_b128 v3, v[166:169] offset:256\n"
      "v_pk_mul_f32 v[136:137], v[136:137], v[210:211] op_sel_hi:[1,0]\n"
      "v_pk_fma_f32 v[176:177], v[126:127], v[126:127], v[176:177]\n"
      "v_pk_fma_f32 v[176:177], v[206:207], v[206:207], v[176:177]\n"
      "v_mfma_f32_16x16x32_bf16 v[170:173], v[76:79], v[88:91], v[170:173]\n"
      "v_subrev_f32 v174, s72, v174\n"
      "v_cvt_pk_bf16_f32 v135, v136, v137\n"
      "v_mfma_f32_16x16x32_bf16 v[164:167], v[130:133], v[96:99], 0\n"
      "s_and_b32 s5, s84, s8\n"
      "v_rcp_f32_e64 v150, v150\n"
      "v_mfma_f32_16x16x32_bf16 v[164:167], v[80:83], v[88:91], v[164:167]\n"
      "ds_write_b64 v4, v[134:135] offset:256\n"
      "v_exp_f32_e64 v174, v174\n"
      "v_pk_fma_f32 v[176:177], v[208:209], v[208:209], v[176:177]\n"
      "v_pk_mul_f32 v[172:173], v[172:173], v[150:151] op_sel_hi:[1,0]\n"
      "v_cvt_pk_bf16_f32 v174, v174, v174\n"
      "v_pk_mul_f32 v[170:171], v[170:171], v[150:151] op_sel_hi:[1,0]\n"
      "v_pk_fma_f32 v[176:177], v[182:183], v[182:183], v[176:177]\n"
      "v_pk_fma_f32 v[176:177], v[184:185], v[184:185], v[176:177]\n"
      "v_cvt_pk_bf16_f32 v170, v170, v171\n"
      "v_pk_mul_f32 v[164:165], v[164:165], v[150:151] op_sel_hi:[1,0]\n"
      "v_cvt_pk_bf16_f32 v171, v172, v173\n"
      "v_pk_mul_f32 v[166:167], v[166:167], v[150:151] op_sel_hi:[1,0]\n"
      "v_mfma_f32_16x16x32_bf16 v[152:155], v[194:197], v[96:99], 0\n"
      "ds_write_b16 v13, v174\n"
      "v_cvt_pk_bf16_f32 v172, v164, v165\n"
      "v_mfma_f32_16x16x32_bf16 v[152:155], v[84:87], v[88:91], v[152:155]\n"
      "ds_read_b128 v[210:213], v14\n"
      "v_add_f32 v176, v176, v177\n"
      "v_cvt_pk_bf16_f32 v173, v166, v167\n"
      "v_mov_b32 v110, v176\n"
      "s_add_u32 s42, s42, 1\n"
      "ds_write_b128 v3, v[170:173] offset:512\n"
      "v_permlane16_swap_b32 v176, v110\n"
      "v_add_f32_e64 v176, v176, v110\n"
      "v_pk_mul_f32 v[152:153], v[152:153], v[150:151] op_sel_hi:[1,0]\n"
      "v_pk_mul_f32 v[154:155], v[154:155], v[150:151] op_sel_hi:[1,0]\n"
      "v_permlane32_swap_b32 v160, v176\n"
      "v_add_f32_e64 v160, v160, v176\n"
      "s_add_u32 s43, s43, 1\n"
      "ds_write_b32 v31, v160\n"
      "v_cvt_pk_bf16_f32 v152, v152, v153\n"
      "v_cvt_pk_bf16_f32 v153, v154, v155\n"
      "ds_read_b128 v[174:177], v14 offset:64\n"
      "ds_write_b64 v4, v[152:153] offset:512\n"
      "s_waitcnt lgkmcnt(4)\n"
      "v_mfma_f32_16x16x32_bf16 v[150:153], v[210:213], v[32:35], 0\n"
      "s_waitcnt lgkmcnt(1)\n"
      "v_mfma_f32_16x16x32_bf16 v[150:153], v[174:177], v[32:35], v[150:153]\n"
      "s_cmp_ge_u32 s95, 0x40000000\n"
      "s_cselect_b64 vcc, -1, s[56:57]\n"
      "s_waitcnt lgkmcnt(0)\n"
      "s_barrier\n"
      "s_cbranch_vccz L_sel1_%=\n"
      "s_branch L_disp1_%=\n"
      "L_tail1_%=:\n"
      "s_nop 4\n"
      "ds_read_b128 v[134:137], v9\n"
      "v_mfma_f32_16x16x32_bf16 v[164:167], v[210:213], v[214:217], 0\n"
      "ds_read_b128 v[214:217], v2\n"
      "ds_read_b128 v[116:119], v2 offset:15360\n"
      "v_mfma_f32_16x16x32_bf16 v[164:167], v[174:177], v[76:79], v[164:167]\n"
      "ds_read_b128 v[138:141], v2 offset:256\n"
      "ds_read_b128 v[100:103], v2 offset:6144\n"
      "v_mfma_f32_16x16x32_bf16 v[170:173], v[210:213], v[130:133], 0\n"
      "ds_read_b128 v[200:203], v2 offset:3072\n"
      "v_ashrrev_i32 v96, 31, v37\n"
      "v_mfma_f32_16x16x32_bf16 v[170:173], v[174:177], v[80:83], v[170:173]\n"
      "ds_read_b128 v[112:115], v2 offset:12288\n"
      "ds_read_b128 v[178:181], v7\n"
      "v_mfma_f32_16x16x32_bf16 v[128:131], v[210:213], v[194:197], 0\n"
      "ds_read_b128 v[210:213], v2 offset:9216\n"
      "s_waitcnt lgkmcnt(8)\n"
      "v_add_f32 v136, v136, v137\n"
      "v_add_f32 v134, v134, v135\n"
      "v_add_f32 v134, v134, v136\n"
      "v_rsq_f32_e64 v134, v134\n"
      "v_mfma_f32_16x16x32_bf16 v[128:131], v[174:177], v[84:87], v[128:131]\n"
      "s_waitcnt lgkmcnt(7)\n"
      "v_mul_f32 v134, 0x415db3d7, v134\n"
      "v_mov_b32 v136, v134\n"
      "s_nop 1\n"
      "v_permlane32_swap_b32 v134, v136\n"
      "v_mov_b32 v135, v134\n"
      "v_pk_mul_f32 v[124:125], v[124:125], v[136:137] op_sel_hi:[1,0]\n"
      "v_pk_mul_f32 v[208:209], v[208:209], v[136:137] op_sel_hi:[1,0]\n"
      "v_permlane16_swap_b32 v134, v135\n"
      "v_pk_mul_f32 v[206:207], v[206:207], v[136:137] op_sel_hi:[1,0]\n"
      "v_pk_mul_f32 v[186:187], v[186:187], v[134:135] op_sel_hi:[1,0]\n"
      "v_pk_mul_f32 v[156:157], v[156:157], v[134:135] op_sel_hi:[1,0]\n"
      "v_pk_mul_f32 v[126:127], v[126:127], v[136:137] op_sel_hi:[1,0]\n"
      "v_pk_mul_f32 v[122:123], v[122:123], v[134:135] op_sel:[0,1]\n"
      "v_pk_mul_f32 v[220:221], v[220:221], v[134:135] op_sel:[0,1]\n"
      "v_pk_mul_f32 v[188:189], v[188:189], v[134:135] op_sel_hi:[1,0]\n"
      "v_pk_mul_f32 v[144:145], v[144:145], v[134:135] op_sel_hi:[1,0]\n"
      "v_cvt_pk_bf16_f32 v156, v156, v157\n"
      "v_cvt_pk_bf16_f32 v124, v124, v125\n"
      "v_cvt_pk_bf16_f32 v186, v186, v187\n"
      "v_pk_mul_f32 v[120:121], v[120:121], v[134:135] op_sel:[0,1]\n"
      "v_pk_mul_f32 v[184:185], v[184:185], v[136:137] op_sel_hi:[1,0]\n"
      "v_pk_mul_f32 v[104:105], v[104:105], v[134:135] op_sel:[0,1]\n"
      "v_cvt_pk_bf16_f32 v125, v126, v127\n"
      "v_cvt_pk_bf16_f32 v104, v104, v105\n"
      "v_pk_mul_f32 v[142:143], v[142:143], v[134:135] op_sel_hi:[1,0]\n"
      "v_cvt_pk_bf16_f32 v187, v188, v189\n"
      "v_cvt_pk_bf16_f32 v120, v120, v121\n"
      "v_cvt_pk_bf16_f32 v121, v122, v123\n"
      "v_cvt_pk_bf16_f32 v123, v220, v221\n"
      "v_pk_mul_f32 v[106:107], v[106:107], v[134:135] op_sel:[0,1]\n"
      "v_pk_mul_f32 v[158:159], v[158:159], v[134:135] op_sel_hi:[1,0]\n"
      "v_cvt_pk_bf16_f32 v105, v106, v107\n"
      "v_cvt_pk_bf16_f32 v127, v208, v209\n"
      "v_pk_mul_f32 v[218:219], v[218:219], v[134:135] op_sel:[0,1]\n"
      "v_cvt_pk_bf16_f32 v126, v206, v207\n"
      "v_pk_mul_f32 v[182:183], v[182:183], v[136:137] op_sel_hi:[1,0]\n"
      "v_cvt_pk_bf16_f32 v182, v182, v183\n"
      "v_cvt_pk_bf16_f32 v183, v184, v185\n"
      "v_cvt_pk_bf16_f32 v188, v142, v143\n"
      "v_cvt_pk_bf16_f32 v122, v218, v219\n"
      "v_cvt_pk_bf16_f32 v157, v158, v159\n"
      "v_cvt_pk_bf16_f32 v189, v144, v145\n"
      "s_mul_i32 s92, s40, 3072\n"
      "ds_write_b128 v5, v[120:123] offset:24576\n"
      "ds_write_b64 v6, v[182:183] offset:30720\n"
      "ds_write_b128 v5, v[124:127] offset:30720\n"
      "ds_write_b128 v5, v[186:189] offset:18432\n"
      "ds_write_b64 v6, v[104:105] offset:24576\n"
      "ds_write_b64 v6, v[156:157] offset:18432\n"
      "v_mfma_f32_16x16x32_bf16 v[186:189], a[216:219], v[214:217], 0\n"
      "ds_read_b128 v[130:133], v2 offset:12800\n"
      "s_waitcnt lgkmcnt(5)\n"
      "v_mfma_f32_16x16x32_bf16 v[186:189], a[220:223], v[200:203], v[186:189]\n"
      "buffer_store_dwordx4 v[178:181], v20, s[24:27], s85 offen sc1\n"
      "v_mfma_f32_16x16x32_bf16 v[142:145], a[240:243], v[214:217], 0\n"
      "v_subrev_u32 v84, 115456, v9\n"
      "v_mfma_f32_16x16x32_bf16 v[142:145], a[244:247], v[200:203], v[142:145]\n"
      "ds_read_b128 v[124:127], v2 offset:6400\n"
      "v_and_b32 v85, 0x3fffffff, v37\n"
      "v_mfma_f32_16x16x32_bf16 v[156:159], v[232:235], v[214:217], 0\n"
      "ds_read_b128 v[206:209], v2 offset:3328\n"
      "v_mfma_f32_16x16x32_bf16 v[156:159], v[236:239], v[200:203], v[156:159]\n"
      "ds_read_b128 v[182:185], v7 offset:8192\n"
      "v_cndmask_b32 v164, v164, v170, s[58:59]\n"
      "v_mfma_f32_16x16x32_bf16 v[186:189], a[224:227], v[100:103], v[186:189]\n"
      "ds_read_b128 v[214:217], v7 offset:12288\n"
      "v_add_u32 v85, 1179648, v85\n"
      "v_mfma_f32_16x16x32_bf16 v[186:189], a[228:231], v[210:213], v[186:189]\n"
      "ds_read_b128 v[194:197], v2 offset:3584\n"
      "v_subrev_u32 v165, 115456, v9\n"
      "v_mfma_f32_16x16x32_bf16 v[142:145], a[248:251], v[100:103], v[142:145]\n"
      "v_cndmask_b32 v164, v164, v128, s[60:61]\n"
      "v_and_b32 v96, 100270464, v96\n"
      "v_mfma_f32_16x16x32_bf16 v[142:145], a[252:255], v[210:213], v[142:145]\n"
      "v_and_b32 v104, 0x300, v84\n"
      "v_add_u32 v128, s92, v165\n"
      "v_mfma_f32_16x16x32_bf16 v[156:159], v[240:243], v[100:103], v[156:159]\n"
      "v_add_u32_e64 v84, v84, v104\n"
      "v_mfma_f32_16x16x32_bf16 v[156:159], v[244:247], v[210:213], v[156:159]\n"
      "ds_read_b128 v[104:107], v7 offset:4096\n"
      "v_mfma_f32_16x16x32_bf16 v[186:189], a[232:235], v[112:115], v[186:189]\n"
      "v_lshrrev_b32_e64 v165, 4, v128\n"
      "v_add_u32 v84, 150784, v84\n"
      "v_mfma_f32_16x16x32_bf16 v[186:189], a[236:239], v[116:119], v[186:189]\n"
      "ds_read_b128 v[210:213], v2 offset:15616\n"
      "v_add_u32_e64 v86, 64, v165\n"
      "v_mfma_f32_16x16x32_bf16 v[142:145], v[224:227], v[112:115], v[142:145]\n"
      "v_mfma_f32_16x16x32_bf16 v[142:145], v[228:231], v[116:119], v[142:145]\n"
      "ds_read_b128 v[174:177], v2 offset:9472\n"
      "v_mov_b32_dpp v150, v150 quad_perm:[0,0,0,0] row_mask:0xf bank_mask:0xf\n"
      "v_mfma_f32_16x16x32_bf16 v[156:159], v[248:251], v[112:115], v[156:159]\n"
      "ds_read_b128 v[170:173], v2 offset:12544\n"
      "v_fma_f32 v114, v186, v186, s52\n"
      "v_mfma_f32_16x16x32_bf16 v[156:159], v[252:255], v[116:119], v[156:159]\n"
      "ds_read_b128 v[152:155], v84 offset:2048\n"
      "v_add_u32 v80, 128, v165\n"
      "v_mfma_f32_16x16x32_bf16 v[120:123], a[216:219], v[138:141], 0\n"
      "s_waitcnt lgkmcnt(4)\n"
      "v_mul_f32 v115, v187, v187\n"
      "v_mul_u32_u24 v82, 2731, v86\n"
      "v_mfma_f32_16x16x32_bf16 v[120:123], a[220:223], v[206:209], v[120:123]\n"
      "v_rcp_f32_e64 v150, v150\n"
      "v_mfma_f32_16x16x32_bf16 v[218:221], a[240:243], v[138:141], 0\n"
      "buffer_store_dwordx4 v[104:107], v21, s[24:27], s85 offen sc1\n"
      "v_mfma_f32_16x16x32_bf16 v[218:221], a[244:247], v[206:209], v[218:221]\n"
      "v_mul_f32_e64 v164, v150, v164\n"
      "v_mfma_f32_16x16x32_bf16 v[104:107], v[232:235], v[138:141], 0\n"
      "ds_read_b128 v[148:151], v2 offset:6656\n"
      "v_mfma_f32_16x16x32_bf16 v[104:107], v[236:239], v[206:209], v[104:107]\n"
      "s_waitcnt lgkmcnt(1)\n"
      "v_fmac_f32 v115, v189, v189\n"
      "v_fmac_f32_e64 v114, v188, v188\n"
      "v_mfma_f32_16x16x32_bf16 v[120:123], a[224:227], v[124:127], v[120:123]\n"
      "ds_read_b128 v[166:169], v2 offset:512\n"
      "v_mfma_f32_16x16x32_bf16 v[120:123], a[228:231], v[174:177], v[120:123]\n"
      "v_mul_u32_u24 v76, 2731, v80\n"
      "v_mfma_f32_16x16x32_bf16 v[218:221], a[248:251], v[124:127], v[218:221]\n"
      "ds_read_b128 v[98:101], v2 offset:15872\n"
      "v_mfma_f32_16x16x32_bf16 v[218:221], a[252:255], v[174:177], v[218:221]\n"
      "ds_read_b128 v[134:137], v84 offset:8192\n"
      "v_mfma_f32_16x16x32_bf16 v[104:107], v[240:243], v[124:127], v[104:107]\n"
      "v_cvt_pk_bf16_f32 v164, v164, v164\n"
      "v_lshrrev_b32 v82, 16, v82\n"
      "v_mfma_f32_16x16x32_bf16 v[104:107], v[244:247], v[174:177], v[104:107]\n"
      "v_fmac_f32 v114, v142, v142\n"
      "v_fmac_f32_e64 v115, v143, v143\n"
      "v_mfma_f32_16x16x32_bf16 v[120:123], a[232:235], v[170:173], v[120:123]\n"
      "ds_read_b128 v[174:177], v84\n"
      "v_mfma_f32_16x16x32_bf16 v[120:123], a[236:239], v[210:213], v[120:123]\n"
      "buffer_store_dwordx4 v[182:185], v22, s[24:27], s85 offen sc1\n"
      "v_mfma_f32_16x16x32_bf16 v[218:221], v[224:227], v[170:173], v[218:221]\n"
      "v_sub_u32 v85, v85, v96\n"
      "v_fmac_f32 v114, v144, v144\n"
      "v_mfma_f32_16x16x32_bf16 v[218:221], v[228:231], v[210:213], v[218:221]\n"
      "ds_write_b16 v12, v164\n"
      "v_mfma_f32_16x16x32_bf16 v[104:107], v[248:251], v[170:173], v[104:107]\n"
      "s_nop 0\n"
      "v_mul_f32 v139, v121, v121\n"
      "v_fma_f32 v138, v120, v120, s52\n"
      "v_mfma_f32_16x16x32_bf16 v[104:107], v[252:255], v[210:213], v[104:107]\n"
      "s_waitcnt lgkmcnt(1)\n"
      "v_fmac_f32 v138, v122, v122\n"
      "v_fmac_f32_e64 v139, v123, v123\n"
      "v_mfma_f32_16x16x32_bf16 v[124:127], a[216:219], v[166:169], 0\n"
      "ds_read_b128 v[210:213], v2 offset:9728\n"
      "v_fmac_f32 v138, v218, v218\n"
      "v_mfma_f32_16x16x32_bf16 v[124:127], a[220:223], v[194:197], v[124:127]\n"
      "ds_read_b128 v[170:173], v84 offset:4096\n"
      "v_fmac_f32 v139, v219, v219\n"
      "v_mfma_f32_16x16x32_bf16 v[206:209], a[240:243], v[166:169], 0\n"
      "v_mfma_f32_16x16x32_bf16 v[206:209], a[244:247], v[194:197], v[206:209]\n"
      "buffer_store_dwordx4 v[214:217], v23, s[24:27], s85 offen sc1\n"
      "v_mfma_f32_16x16x32_bf16 v[182:185], v[232:235], v[166:169], 0\n"
      "v_fmac_f32 v115, v145, v145\n"
      "v_fmac_f32 v114, v156, v156\n"
      "v_mfma_f32_16x16x32_bf16 v[182:185], v[236:239], v[194:197], v[182:185]\n"
      "v_fmac_f32 v139, v221, v221\n"
      "v_fmac_f32 v115, v157, v157\n"
      "v_mfma_f32_16x16x32_bf16 v[124:127], a[224:227], v[148:151], v[124:127]\n"
      "s_waitcnt lgkmcnt(0)\n"
      "v_fmac_f32 v138, v220, v220\n"
      "v_fmac_f32 v114, v158, v158\n"
      "v_mfma_f32_16x16x32_bf16 v[124:127], a[228:231], v[210:213], v[124:127]\n"
      "ds_read_b128 v[194:197], v84 offset:10240\n"
      "v_mul_u32_u24 v81, 24, v82\n"
      "v_mfma_f32_16x16x32_bf16 v[206:209], a[248:251], v[148:151], v[206:209]\n"
      "v_fmac_f32 v115, v159, v159\n"
      "v_fmac_f32 v139, v105, v105\n"
      "v_mfma_f32_16x16x32_bf16 v[206:209], a[252:255], v[210:213], v[206:209]\n"
      "v_add_u32_e64 v78, 0, v165\n"
      "v_mfma_f32_16x16x32_bf16 v[182:185], v[240:243], v[148:151], v[182:185]\n"
      "v_add_f32 v114, v114, v115\n"
      "v_mfma_f32_16x16x32_bf16 v[182:185], v[244:247], v[210:213], v[182:185]\n"
      "v_fmac_f32 v138, v104, v104\n"
      "v_lshrrev_b32_e64 v76, 16, v76\n"
      "v_mfma_f32_16x16x32_bf16 v[124:127], a[232:235], v[130:133], v[124:127]\n"
      "v_fmac_f32 v139, v107, v107\n"
      "v_mfma_f32_16x16x32_bf16 v[124:127], a[236:239], v[98:101], v[124:127]\n"
      "ds_read_b128 v[210:213], v84 offset:6144\n"
      "v_fmac_f32 v138, v106, v106\n"
      "v_mfma_f32_16x16x32_bf16 v[206:209], v[224:227], v[130:133], v[206:209]\n"
      "v_mul_u32_u24 v77, 2731, v78\n"
      "v_add_f32 v138, v138, v139\n"
      "v_mfma_f32_16x16x32_bf16 v[206:209], v[228:231], v[98:101], v[206:209]\n"
      "s_nop 0\n"
      "v_permlane16_swap_b32 v114, v138\n"
      "v_mfma_f32_16x16x32_bf16 v[182:185], v[248:251], v[130:133], v[182:185]\n"
      "v_mul_f32 v117, v125, v125\n"
      "v_fma_f32 v116, v124, v124, s52\n"
      "v_mfma_f32_16x16x32_bf16 v[182:185], v[252:255], v[98:101], v[182:185]\n"
      "v_fmac_f32 v116, v126, v126\n"
      "v_fmac_f32 v117, v127, v127\n"
      "v_mfma_f32_16x16x32_bf16 v[130:133], a[216:219], v[174:177], 0\n"
      "ds_read_b128 v[214:217], v7 offset:16384\n"
      "v_fmac_f32_e64 v117, v207, v207\n"
      "v_mfma_f32_16x16x32_bf16 v[130:133], a[220:223], v[152:155], v[130:133]\n"
      "v_fmac_f32 v116, v206, v206\n"
      "v_add_f32 v114, v114, v138\n"
      "v_mfma_f32_16x16x32_bf16 v[164:167], a[240:243], v[174:177], 0\n"
      "v_fmac_f32 v117, v209, v209\n"
      "v_fmac_f32 v116, v208, v208\n"
      "v_mfma_f32_16x16x32_bf16 v[164:167], a[244:247], v[152:155], v[164:167]\n"
      "v_fmac_f32 v117, v183, v183\n"
      "v_fmac_f32 v116, v182, v182\n"
      "v_mfma_f32_16x16x32_bf16 v[148:151], v[232:235], v[174:177], 0\n"
      "v_fmac_f32 v117, v185, v185\n"
      "v_fmac_f32 v116, v184, v184\n"
      "v_mfma_f32_16x16x32_bf16 v[148:151], v[236:239], v[152:155], v[148:151]\n"
      "s_waitcnt lgkmcnt(1)\n"
      "v_add_f32 v116, v116, v117\n"
      "v_mov_b32 v138, v116\n"
      "v_mfma_f32_16x16x32_bf16 v[130:133], a[224:227], v[170:173], v[130:133]\n"
      "s_nop 0\n"
      "v_permlane16_swap_b32 v116, v138\n"
      "v_mfma_f32_16x16x32_bf16 v[130:133], a[228:231], v[210:213], v[130:133]\n"
      "v_bfe_i32 v96, v37, 30, 1\n"
      "v_add_f32 v116, v116, v138\n"
      "v_mfma_f32_16x16x32_bf16 v[164:167], a[248:251], v[170:173], v[164:167]\n"
      "s_nop 0\n"
      "v_permlane32_swap_b32 v114, v116\n"
      "v_mfma_f32_16x16x32_bf16 v[164:167], a[252:255], v[210:213], v[164:167]\n"
      "v_and_b32 v96, 196224, v96\n"
      "v_add_f32_e64 v114, v114, v116\n"
      "v_mfma_f32_16x16x32_bf16 v[148:151], v[240:243], v[170:173], v[148:151]\n"
      "ds_write_b32 v31, v114 offset:1024\n"
      "v_mfma_f32_16x16x32_bf16 v[148:151], v[244:247], v[210:213], v[148:151]\n"
      "s_waitcnt lgkmcnt(1)\n"
      "v_sub_u32 v85, v85, v96\n"
      "v_lshrrev_b32 v77, 16, v77\n"
      "v_mfma_f32_16x16x32_bf16 v[130:133], a[232:235], v[134:137], v[130:133]\n"
      "buffer_store_dwordx4 v[214:217], v25, s[24:27], s85 offen sc1\n"
      "v_mfma_f32_16x16x32_bf16 v[130:133], a[236:239], v[194:197], v[130:133]\n"
      "v_readlane_b32 s85, v37, s42\n"
      "v_mfma_f32_16x16x32_bf16 v[164:167], v[224:227], v[134:137], v[164:167]\n"
      "s_or_b32 s95, s85, s48\n"
      "s_cmp_ge_u32 s95, 0x40000000\n"
      "s_cbranch_scc1 L_rare_27_%=\n"
      "L_back_28_%=:\n"
      "v_mfma_f32_16x16x32_bf16 v[164:167], v[228:231], v[194:197], v[164:167]\n"
      "s_waitcnt lgkmcnt(0)\n"
      "s_barrier\n"
      "ds_read_b128 v[210:213], v84 offset:256\n"
      "ds_read_b128 v[174:177], v9 offset:1024\n"
      "ds_read_b128 v[214:217], v84 offset:2304\n"
      "ds_read_b128 v[170:173], v7 offset:18432\n"
      "v_fma_f32 v112, v130, v130, s52\n"
      "v_mul_f32_e64 v113, v131, v131\n"
      "v_pk_fma_f32 v[112:113], v[132:133], v[132:133], v[112:113]\n"
      "v_pk_fma_f32 v[112:113], v[164:165], v[164:165], v[112:113]\n"
      "v_mfma_f32_16x16x32_bf16 v[148:151], v[248:251], v[134:137], v[148:151]\n"
      "ds_read_b128 v[152:155], v7 offset:34816\n"
      "ds_read_b128 v[114:117], v84 offset:4352\n"
      "ds_read_b128 v[136:139], v7 offset:30720\n"
      "ds_read_b128 v[100:103], v7 offset:22528\n"
      "v_mfma_f32_16x16x32_bf16 v[148:151], v[252:255], v[194:197], v[148:151]\n"
      "ds_read_b128 v[194:197], v84 offset:6400\n"
      "ds_read_b128 v[200:203], v7 offset:26624\n"
      "s_waitcnt lgkmcnt(9)\n"
      "v_mfma_f32_16x16x32_bf16 v[96:99], a[216:219], v[210:213], 0\n"
      "s_waitcnt lgkmcnt(8)\n"
      "v_add_f32 v174, v174, v175\n"
      "v_add_f32 v176, v176, v177\n"
      "v_add_f32 v174, v174, v176\n"
      "v_rsq_f32 v174, v174\n"
      "s_waitcnt lgkmcnt(7)\n"
      "v_mfma_f32_16x16x32_bf16 v[96:99], a[220:223], v[214:217], v[96:99]\n"
      "s_waitcnt lgkmcnt(6)\n"
      "v_mul_f32 v174, 0x415db3d7, v174\n"
      "v_mov_b32 v176, v174\n"
      "v_mfma_f32_16x16x32_bf16 v[178:181], a[240:243], v[210:213], 0\n"
      "ds_read_b128 v[108:111], v84 offset:10496\n"
      "v_permlane32_swap_b32 v174, v176\n"
      "v_mov_b32_e64 v175, v174\n"
      "v_mfma_f32_16x16x32_bf16 v[178:181], a[244:247], v[214:217], v[178:181]\n"
      "buffer_store_dwordx4 v[170:173], v20, s[24:27], s85 offen sc1\n"
      "s_nop 1\n"
      "v_mfma_f32_16x16x32_bf16 v[170:173], v[232:235], v[210:213], 0\n"
      "s_waitcnt lgkmcnt(3)\n"
      "v_permlane16_swap_b32_e64 v174, v175\n"
      "v_mfma_f32_16x16x32_bf16 v[170:173], v[236:239], v[214:217], v[170:173]\n"
      "buffer_store_dwordx4 v[100:103], v21, s[24:27], s85 offen sc1\n"
      "v_pk_mul_f32 v[144:145], v[144:145], v[174:175] op_sel_hi:[1,0]\n"
      "v_pk_fma_f32 v[112:113], v[166:167], v[166:167], v[112:113]\n"
      "v_pk_mul_f32 v[104:105], v[104:105], v[174:175] op_sel:[0,1]\n"
      "v_pk_mul_f32 v[182:183], v[182:183], v[176:177] op_sel_hi:[1,0]\n"
      "v_pk_mul_f32 v[220:221], v[220:221], v[174:175] op_sel:[0,1]\n"
      "v_pk_mul_f32 v[184:185], v[184:185], v[176:177] op_sel_hi:[1,0]\n"
      "v_pk_mul_f32 v[186:187], v[186:187], v[174:175] op_sel_hi:[1,0]\n"
      "v_pk_mul_f32 v[218:219], v[218:219], v[174:175] op_sel:[0,1]\n"
      "v_pk_mul_f32 v[142:143], v[142:143], v[174:175] op_sel_hi:[1,0]\n"
      "v_pk_mul_f32 v[122:123], v[122:123], v[174:175] op_sel:[0,1]\n"
      "v_pk_mul_f32 v[120:121], v[120:121], v[174:175] op_sel:[0,1]\n"
      "v_pk_mul_f32 v[106:107], v[106:107], v[174:175] op_sel:[0,1]\n"
      "v_mfma_f32_16x16x32_bf16 v[96:99], a[224:227], v[114:117], v[96:99]\n"
      "s_waitcnt lgkmcnt(0)\n"
      "v_cvt_pk_bf16_f32 v186, v186, v187\n"
      "v_cvt_pk_bf16_f32 v182, v182, v183\n"
      "v_mfma_f32_16x16x32_bf16 v[96:99], a[228:231], v[194:197], v[96:99]\n"
      "buffer_store_dwordx4 v[200:203], v22, s[24:27], s85 offen sc1\n"
      "v_mfma_f32_16x16x32_bf16 v[178:181], a[248:251], v[114:117], v[178:181]\n"
      "ds_read_b128 v[212:215], v84 offset:8448\n"
      "v_cvt_pk_bf16_f32 v120, v120, v121\n"
      "v_mfma_f32_16x16x32_bf16 v[178:181], a[252:255], v[194:197], v[178:181]\n"
      "buffer_store_dwordx4 v[136:139], v23, s[24:27], s85 offen sc1\n"
      "v_pk_mul_f32 v[208:209], v[208:209], v[176:177] op_sel_hi:[1,0]\n"
      "v_pk_mul_f32 v[158:159], v[158:159], v[174:175] op_sel_hi:[1,0]\n"
      "v_pk_fma_f32 v[112:113], v[148:149], v[148:149], v[112:113]\n"
      "v_pk_mul_f32 v[124:125], v[124:125], v[176:177] op_sel_hi:[1,0]\n"
      "v_cvt_pk_bf16_f32 v104, v104, v105\n"
      "v_pk_mul_f32 v[188:189], v[188:189], v[174:175] op_sel_hi:[1,0]\n"
      "v_pk_mul_f32 v[156:157], v[156:157], v[174:175] op_sel_hi:[1,0]\n"
      "v_cvt_pk_bf16_f32 v121, v122, v123\n"
      "v_cvt_pk_bf16_f32 v122, v218, v219\n"
      "v_pk_fma_f32 v[112:113], v[150:151], v[150:151], v[112:113]\n"
      "v_mfma_f32_16x16x32_bf16 v[170:173], v[240:243], v[114:117], v[170:173]\n"
      "v_cvt_pk_bf16_f32 v187, v188, v189\n"
      "v_cvt_pk_bf16_f32 v105, v106, v107\n"
      "v_mfma_f32_16x16x32_bf16 v[170:173], v[244:247], v[194:197], v[170:173]\n"
      "s_waitcnt lgkmcnt(0)\n"
      "v_cvt_pk_bf16_f32 v123, v220, v221\n"
      "v_cvt_pk_bf16_f32 v183, v184, v185\n"
      "v_mfma_f32_16x16x32_bf16 v[96:99], a[232:235], v[212:215], v[96:99]\n"
      "ds_write_b128 v5, v[120:123] offset:6144\n"
      "v_mfma_f32_16x16x32_bf16 v[96:99], a[236:239], v[108:111], v[96:99]\n"
      "ds_write_b64 v6, v[104:105] offset:6144\n"
      "v_cvt_pk_bf16_f32 v156, v156, v157\n"
      "v_cvt_pk_bf16_f32 v188, v142, v143\n"
      "v_mfma_f32_16x16x32_bf16 v[178:181], v[224:227], v[212:215], v[178:181]\n"
      "v_cvt_pk_bf16_f32 v189, v144, v145\n"
      "v_cvt_pk_bf16_f32 v157, v158, v159\n"
      "v_mfma_f32_16x16x32_bf16 v[178:181], v[228:231], v[108:111], v[178:181]\n"
      "ds_write_b64 v6, v[182:183] offset:12288\n"
      "v_mul_f32_e64 v159, v97, v97\n"
      "v_mfma_f32_16x16x32_bf16 v[170:173], v[248:251], v[212:215], v[170:173]\n"
      "ds_write_b64 v6, v[156:157]\n"
      "v_mfma_f32_16x16x32_bf16 v[170:173], v[252:255], v[108:111], v[170:173]\n"
      "ds_write_b128 v5, v[186:189]\n"
      "v_pk_mul_f32 v[126:127], v[126:127], v[176:177] op_sel_hi:[1,0]\n"
      "v_fma_f32 v158, v96, v96, s52\n"
      "v_pk_fma_f32 v[158:159], v[98:99], v[98:99], v[158:159]\n"
      "v_pk_fma_f32 v[158:159], v[178:179], v[178:179], v[158:159]\n"
      "v_pk_fma_f32 v[158:159], v[180:181], v[180:181], v[158:159]\n"
      "v_cvt_pk_bf16_f32 v124, v124, v125\n"
      "v_add_f32 v112, v112, v113\n"
      "v_pk_fma_f32 v[158:159], v[170:171], v[170:171], v[158:159]\n"
      "v_pk_fma_f32 v[158:159], v[172:173], v[172:173], v[158:159]\n"
      "v_add_f32 v158, v158, v159\n"
      "v_cvt_pk_bf16_f32 v125, v126, v127\n"
      "v_pk_mul_f32 v[206:207], v[206:207], v[176:177] op_sel_hi:[1,0]\n"
      "v_permlane16_swap_b32 v112, v158\n"
      "v_add_f32 v112, v112, v158\n"
      "v_mov_b32 v158, v112\n"
      "v_cvt_pk_bf16_f32 v126, v206, v207\n"
      "v_cvt_pk_bf16_f32 v127, v208, v209\n"
      "v_permlane32_swap_b32 v112, v158\n"
      "v_add_f32 v112, v112, v158\n"
      "s_add_u32 s42, s42, 1\n"
      "ds_write_b128 v5, v[124:127] offset:12288\n"
      "ds_write_b32 v31, v112 offset:4608\n"
      "buffer_store_dwordx4 v[152:155], v25, s[24:27], s85 offen sc1\n"
      "s_nop 1\n"
      "v_mul_u32_u24 v153, 24, v76\n"
      "v_sub_u32 v153, v80, v153\n"
      "v_mul_u32_u24 v152, 24, v77\n"
      "v_sub_u32_e64 v81, v86, v81\n"
      "v_readlane_b32 s85, v37, s42\n"
      "v_and_b32 v86, 7, v82\n"
      "v_lshl_add_u32 v82, v82, 2, 12\n"
      "v_and_b32 v80, 7, v76\n"
      "ds_bpermute_b32 v176, v82, v85\n"
      "v_subrev_u32 v82, 73728, v5\n"
      "v_lshl_add_u32 v76, v76, 2, 12\n"
      "s_or_b32 s95, s85, s48\n"
      "v_sub_u32 v152, v78, v152\n"
      "s_waitcnt lgkmcnt(1)\n"
      "s_barrier\n"
      "ds_read_b128 v[124:127], v9 offset:4608\n"
      "ds_read_b128 v[206:209], v7 offset:4096\n"
      "ds_read_b128 v[186:189], v7\n"
      "v_subrev_u32 v183, 73728, v6\n"
      "v_and_b32 v78, 7, v77\n"
      "ds_read_b128 v[156:159], v7 offset:8192\n"
      "ds_bpermute_b32 v182, v76, v85\n"
      "v_xor_b32 v81, v81, v86\n"
      "v_lshl_add_u32 v77, v77, 2, 12\n"
      "s_cmp_ge_u32 s95, 0x40000000\n"
      "s_cbranch_scc1 L_rare_31_%=\n"
      "L_back_32_%=:\n"
      "ds_read_b128 v[212:215], v7 offset:12288\n"
      "s_waitcnt lgkmcnt(5)\n"
      "v_add_f32 v124, v124, v125\n"
      "v_add_f32 v126, v126, v127\n"
      "v_add_f32 v124, v124, v126\n"
      "v_rsq_f32 v124, v124\n"
      "s_nop 0\n"
      "v_mul_f32 v124, 0x415db3d7, v124\n"
      "v_mov_b32 v126, v124\n"
      "s_waitcnt lgkmcnt(3)\n"
      "buffer_store_dwordx4 v[186:189], v20, s[24:27], s85 offen sc1\n"
      "v_permlane32_swap_b32 v124, v126\n"
      "v_mov_b32 v125, v124\n"
      "s_nop 1\n"
      "v_permlane16_swap_b32 v124, v125\n"
      "v_pk_mul_f32 v[96:97], v[96:97], v[124:125] op_sel:[0,1]\n"
      "v_pk_mul_f32 v[164:165], v[164:165], v[124:125] op_sel_hi:[1,0]\n"
      "v_pk_mul_f32 v[150:151], v[150:151], v[124:125] op_sel_hi:[1,0]\n"
      "v_pk_mul_f32 v[130:131], v[130:131], v[124:125] op_sel_hi:[1,0]\n"
      "v_pk_mul_f32 v[132:133], v[132:133], v[124:125] op_sel_hi:[1,0]\n"
      "v_pk_mul_f32 v[148:149], v[148:149], v[124:125] op_sel_hi:[1,0]\n"
      "v_cvt_pk_bf16_f32 v130, v130, v131\n"
      "v_pk_mul_f32 v[180:181], v[180:181], v[124:125] op_sel:[0,1]\n"
      "v_pk_mul_f32 v[172:173], v[172:173], v[124:125] op_sel:[0,1]\n"
      "v_cvt_pk_bf16_f32 v131, v132, v133\n"
      "v_pk_mul_f32 v[166:167], v[166:167], v[124:125] op_sel_hi:[1,0]\n"
      "v_pk_mul_f32 v[178:179], v[178:179], v[124:125] op_sel:[0,1]\n"
      "v_cvt_pk_bf16_f32 v132, v164, v165\n"
      "v_pk_mul_f32 v[98:99], v[98:99], v[124:125] op_sel:[0,1]\n"
      "v_cvt_pk_bf16_f32 v148, v148, v149\n"
      "v_cvt_pk_bf16_f32 v133, v166, v167\n"
      "ds_write_b128 v82, v[130:133]\n"
      "v_cvt_pk_bf16_f32 v149, v150, v151\n"
      "v_cvt_pk_bf16_f32 v96, v96, v97\n"
      "v_pk_mul_f32 v[170:171], v[170:171], v[124:125] op_sel:[0,1]\n"
      "v_cvt_pk_bf16_f32 v97, v98, v99\n"
      "v_cvt_pk_bf16_f32 v98, v178, v179\n"
      "v_cvt_pk_bf16_f32 v99, v180, v181\n"
      "s_add_u32 s42, s42, 1\n"
      "ds_write_b128 v82, v[96:99] offset:6144\n"
      "ds_write_b64 v183, v[148:149]\n"
      "v_cvt_pk_bf16_f32 v170, v170, v171\n"
      "v_cvt_pk_bf16_f32 v171, v172, v173\n"
      "ds_write_b64 v183, v[170:171] offset:6144\n"
      "ds_read_b128 v[130:133], v7 offset:16384\n"
      "v_xor_b32_e64 v153, v153, v80\n"
      "buffer_store_dwordx4 v[206:209], v21, s[24:27], s85 offen sc1\n"
      "s_waitcnt lgkmcnt(4)\n"
      "v_lshl_add_u32 v182, v153, 4, v182\n"
      "ds_bpermute_b32 v153, v77, v85\n"
      "buffer_store_dwordx4 v[156:159], v22, s[24:27], s85 offen sc1\n"
      "s_waitcnt lgkmcnt(2)\n"
      "s_barrier\n"
      "ds_read_b128 v[156:159], v128\n"
      "buffer_store_dwordx4 v[212:215], v23, s[24:27], s85 offen sc1\n"
      "s_nop 1\n"
      "ds_read_b128 v[212:215], v128 offset:1024\n"
      "v_xor_b32_e64 v152, v152, v78\n"
      "s_waitcnt lgkmcnt(2)\n"
      "buffer_store_dwordx4 v[130:133], v25, s[24:27], s85 offen sc1\n"
      "s_nop 1\n"
      "ds_read_b128 v[130:133], v128 offset:2048\n"
      "v_lshl_add_u32 v176, v81, 4, v176\n"
      "v_lshl_add_u32 v153, v152, 4, v153\n"
      "s_waitcnt lgkmcnt(2)\n"
      "buffer_store_dwordx4 v[156:159], v153, s[24:27], 0 offen sc1\n"
      "s_waitcnt lgkmcnt(1)\n"
      "buffer_store_dwordx4 v[212:215], v176, s[24:27], 0 offen sc1\n"
      "s_waitcnt lgkmcnt(0)\n"
      "buffer_store_dwordx4 v[130:133], v182, s[24:27], 0 offen sc1\n"
      "s_endpgm\n"
      "L_tail0_%=:\n"
      "s_nop 4\n"
      "ds_read_b128 v[96:99], v9 offset:1024\n"
      "v_mfma_f32_16x16x32_bf16 v[164:167], v[210:213], v[214:217], 0\n"
      "ds_read_b128 v[214:217], v2 offset:18432\n"
      "ds_read_b128 v[178:181], v2 offset:27648\n"
      "v_mfma_f32_16x16x32_bf16 v[164:167], v[174:177], v[76:79], v[164:167]\n"
      "ds_read_b128 v[200:203], v7 offset:18432\n"
      "v_mfma_f32_16x16x32_bf16 v[170:173], v[210:213], v[130:133], 0\n"
      "ds_read_b128 v[100:103], v2 offset:24576\n"
      "ds_read_b128 v[110:113], v7 offset:34816\n"
      "v_mfma_f32_16x16x32_bf16 v[170:173], v[174:177], v[80:83], v[170:173]\n"
      "ds_read_b128 v[134:137], v2 offset:30720\n"
      "v_mfma_f32_16x16x32_bf16 v[128:131], v[210:213], v[194:197], 0\n"
      "ds_read_b128 v[194:197], v2 offset:21504\n"
      "v_subrev_u32 v149, 115456, v9\n"
      "s_waitcnt lgkmcnt(7)\n"
      "v_add_f32 v96, v96, v97\n"
      "v_add_f32 v98, v98, v99\n"
      "v_add_f32 v96, v96, v98\n"
      "v_rsq_f32 v96, v96\n"
      "v_mfma_f32_16x16x32_bf16 v[128:131], v[174:177], v[84:87], v[128:131]\n"
      "s_waitcnt lgkmcnt(6)\n"
      "v_mul_f32 v96, 0x415db3d7, v96\n"
      "v_mov_b32 v98, v96\n"
      "v_and_b32 v84, 0x3fffffff, v37\n"
      "s_nop 0\n"
      "v_permlane32_swap_b32 v96, v98\n"
      "v_mov_b32 v97, v96\n"
      "v_pk_mul_f32 v[182:183], v[182:183], v[98:99] op_sel_hi:[1,0]\n"
      "v_pk_mul_f32 v[126:127], v[126:127], v[98:99] op_sel_hi:[1,0]\n"
      "v_pk_mul_f32 v[206:207], v[206:207], v[98:99] op_sel_hi:[1,0]\n"
      "v_permlane16_swap_b32_e64 v96, v97\n"
      "v_pk_mul_f32 v[188:189], v[188:189], v[96:97] op_sel_hi:[1,0]\n"
      "v_pk_mul_f32 v[142:143], v[142:143], v[96:97] op_sel_hi:[1,0]\n"
      "v_pk_mul_f32 v[104:105], v[104:105], v[96:97] op_sel:[0,1]\n"
      "v_pk_mul_f32 v[218:219], v[218:219], v[96:97] op_sel:[0,1]\n"
      "v_pk_mul_f32 v[120:121], v[120:121], v[96:97] op_sel:[0,1]\n"
      "v_pk_mul_f32 v[208:209], v[208:209], v[98:99] op_sel_hi:[1,0]\n"
      "v_pk_mul_f32 v[124:125], v[124:125], v[98:99] op_sel_hi:[1,0]\n"
      "v_pk_mul_f32 v[184:185], v[184:185], v[98:99] op_sel_hi:[1,0]\n"
      "v_pk_mul_f32 v[106:107], v[106:107], v[96:97] op_sel:[0,1]\n"
      "v_pk_mul_f32 v[186:187], v[186:187], v[96:97] op_sel_hi:[1,0]\n"
      "v_cvt_pk_bf16_f32 v120, v120, v121\n"
      "v_pk_mul_f32 v[122:123], v[122:123], v[96:97] op_sel:[0,1]\n"
      "v_cvt_pk_bf16_f32 v186, v186, v187\n"
      "v_pk_mul_f32 v[144:145], v[144:145], v[96:97] op_sel_hi:[1,0]\n"
      "v_cvt_pk_bf16_f32 v182, v182, v183\n"
      "v_pk_mul_f32 v[158:159], v[158:159], v[96:97] op_sel_hi:[1,0]\n"
      "v_cvt_pk_bf16_f32 v124, v124, v125\n"
      "v_pk_mul_f32 v[156:157], v[156:157], v[96:97] op_sel_hi:[1,0]\n"
      "v_cvt_pk_bf16_f32 v187, v188, v189\n"
      "v_cvt_pk_bf16_f32 v125, v126, v127\n"
      "v_cvt_pk_bf16_f32 v121, v122, v123\n"
      "v_cvt_pk_bf16_f32 v104, v104, v105\n"
      "v_cvt_pk_bf16_f32 v188, v142, v143\n"
      "v_cvt_pk_bf16_f32 v126, v206, v207\n"
      "v_cvt_pk_bf16_f32 v156, v156, v157\n"
      "v_cvt_pk_bf16_f32 v122, v218, v219\n"
      "v_cvt_pk_bf16_f32 v157, v158, v159\n"
      "v_pk_mul_f32 v[220:221], v[220:221], v[96:97] op_sel:[0,1]\n"
      "v_cvt_pk_bf16_f32 v105, v106, v107\n"
      "v_cvt_pk_bf16_f32 v123, v220, v221\n"
      "v_cvt_pk_bf16_f32 v189, v144, v145\n"
      "v_cvt_pk_bf16_f32 v127, v208, v209\n"
      "v_cvt_pk_bf16_f32 v183, v184, v185\n"
      "s_mul_i32 s92, s40, 3072\n"
      "ds_write_b128 v5, v[124:127] offset:12288\n"
      "ds_write_b128 v5, v[186:189]\n"
      "ds_write_b64 v6, v[156:157]\n"
      "ds_write_b128 v5, v[120:123] offset:6144\n"
      "ds_write_b64 v6, v[104:105] offset:6144\n"
      "ds_write_b64 v6, v[182:183] offset:12288\n"
      "v_mfma_f32_16x16x32_bf16 v[186:189], a[216:219], v[214:217], 0\n"
      "s_waitcnt lgkmcnt(5)\n"
      "v_mfma_f32_16x16x32_bf16 v[186:189], a[220:223], v[194:197], v[186:189]\n"
      "ds_read_b128 v[124:127], v2 offset:18688\n"
      "v_mfma_f32_16x16x32_bf16 v[142:145], a[240:243], v[214:217], 0\n"
      "buffer_store_dwordx4 v[200:203], v20, s[24:27], s46 offen sc1\n"
      "v_mfma_f32_16x16x32_bf16 v[142:145], a[244:247], v[194:197], v[142:145]\n"
      "v_add_u32 v82, s92, v149\n"
      "v_cndmask_b32 v164, v164, v170, s[58:59]\n"
      "v_mfma_f32_16x16x32_bf16 v[156:159], v[232:235], v[214:217], 0\n"
      "ds_read_b128 v[120:123], v7 offset:22528\n"
      "v_mfma_f32_16x16x32_bf16 v[156:159], v[236:239], v[194:197], v[156:159]\n"
      "ds_read_b128 v[218:221], v2 offset:33792\n"
      "v_lshrrev_b32_e64 v149, 4, v82\n"
      "v_mfma_f32_16x16x32_bf16 v[186:189], a[224:227], v[100:103], v[186:189]\n"
      "ds_read_b128 v[206:209], v2 offset:34048\n"
      "v_mfma_f32_16x16x32_bf16 v[186:189], a[228:231], v[178:181], v[186:189]\n"
      "ds_read_b128 v[212:215], v2 offset:18944\n"
      "v_subrev_u32 v76, 115456, v9\n"
      "v_mfma_f32_16x16x32_bf16 v[142:145], a[248:251], v[100:103], v[142:145]\n"
      "ds_read_b128 v[194:197], v2 offset:22016\n"
      "v_mfma_f32_16x16x32_bf16 v[142:145], a[252:255], v[178:181], v[142:145]\n"
      "ds_read_b128 v[182:185], v2 offset:21760\n"
      "v_add_u32 v85, 64, v149\n"
      "v_mfma_f32_16x16x32_bf16 v[156:159], v[240:243], v[100:103], v[156:159]\n"
      "ds_read_b128 v[174:177], v2 offset:30976\n"
      "v_add_u32 v217, 0, v149\n"
      "v_mfma_f32_16x16x32_bf16 v[156:159], v[244:247], v[178:181], v[156:159]\n"
      "v_add_u32 v80, 128, v149\n"
      "v_ashrrev_i32 v77, 31, v37\n"
      "v_mfma_f32_16x16x32_bf16 v[186:189], a[232:235], v[134:137], v[186:189]\n"
      "s_waitcnt lgkmcnt(5)\n"
      "v_mov_b32_dpp v150, v150 quad_perm:[0,0,0,0] row_mask:0xf bank_mask:0xf\n"
      "v_cndmask_b32 v164, v164, v128, s[60:61]\n"
      "v_mfma_f32_16x16x32_bf16 v[186:189], a[236:239], v[218:221], v[186:189]\n"
      "ds_read_b128 v[98:101], v2 offset:27904\n"
      "v_and_b32 v201, 0x300, v76\n"
      "v_mfma_f32_16x16x32_bf16 v[142:145], v[224:227], v[134:137], v[142:145]\n"
      "ds_read_b128 v[178:181], v2 offset:24832\n"
      "v_add_u32_e64 v76, v76, v201\n"
      "v_mfma_f32_16x16x32_bf16 v[142:145], v[228:231], v[218:221], v[142:145]\n"
      "ds_read_b128 v[200:203], v7 offset:26624\n"
      "v_mul_u32_u24 v86, 2731, v80\n"
      "v_mfma_f32_16x16x32_bf16 v[156:159], v[248:251], v[134:137], v[156:159]\n"
      "v_rcp_f32 v150, v150\n"
      "v_mfma_f32_16x16x32_bf16 v[156:159], v[252:255], v[218:221], v[156:159]\n"
      "buffer_store_dwordx4 v[120:123], v21, s[24:27], s46 offen sc1\n"
      "s_nop 1\n"
      "v_mfma_f32_16x16x32_bf16 v[120:123], a[216:219], v[124:127], 0\n"
      "s_waitcnt lgkmcnt(2)\n"
      "v_add_u32 v76, 150784, v76\n"
      "v_mfma_f32_16x16x32_bf16 v[120:123], a[220:223], v[182:185], v[120:123]\n"
      "v_lshrrev_b32 v86, 16, v86\n"
      "v_fma_f32 v152, v186, v186, s52\n"
      "v_mfma_f32_16x16x32_bf16 v[218:221], a[240:243], v[124:127], 0\n"
      "v_mul_f32 v164, v150, v164\n"
      "v_mul_f32 v153, v187, v187\n"
      "v_mfma_f32_16x16x32_bf16 v[218:221], a[244:247], v[182:185], v[218:221]\n"
      "ds_read_b128 v[134:137], v76 offset:2048\n"
      "v_mul_u32_u24 v216, 2731, v217\n"
      "v_mfma_f32_16x16x32_bf16 v[104:107], v[232:235], v[124:127], 0\n"
      "s_waitcnt lgkmcnt(1)\n"
      "v_add_u32 v84, 1179648, v84\n"
      "v_mfma_f32_16x16x32_bf16 v[104:107], v[236:239], v[182:185], v[104:107]\n"
      "v_mfma_f32_16x16x32_bf16 v[120:123], a[224:227], v[178:181], v[120:123]\n"
      "v_cvt_pk_bf16_f32 v164, v164, v164\n"
      "v_fmac_f32 v153, v189, v189\n"
      "v_mfma_f32_16x16x32_bf16 v[120:123], a[228:231], v[98:101], v[120:123]\n"
      "v_mfma_f32_16x16x32_bf16 v[218:221], a[248:251], v[178:181], v[218:221]\n"
      "ds_read_b128 v[148:151], v76 offset:10240\n"
      "v_fmac_f32_e64 v152, v188, v188\n"
      "v_mfma_f32_16x16x32_bf16 v[218:221], a[252:255], v[98:101], v[218:221]\n"
      "ds_read_b128 v[128:131], v2 offset:34304\n"
      "v_mfma_f32_16x16x32_bf16 v[104:107], v[240:243], v[178:181], v[104:107]\n"
      "ds_read_b128 v[178:181], v76 offset:4096\n"
      "v_fmac_f32_e64 v153, v143, v143\n"
      "v_mfma_f32_16x16x32_bf16 v[104:107], v[244:247], v[98:101], v[104:107]\n"
      "ds_read_b128 v[100:103], v2 offset:28160\n"
      "v_and_b32 v77, 100270464, v77\n"
      "v_mfma_f32_16x16x32_bf16 v[120:123], a[232:235], v[174:177], v[120:123]\n"
      "v_lshrrev_b32 v216, 16, v216\n"
      "v_mul_u32_u24 v78, 2731, v85\n"
      "v_mfma_f32_16x16x32_bf16 v[120:123], a[236:239], v[206:209], v[120:123]\n"
      "ds_read_b128 v[96:99], v76 offset:8192\n"
      "v_fmac_f32 v152, v142, v142\n"
      "v_mfma_f32_16x16x32_bf16 v[218:221], v[224:227], v[174:177], v[218:221]\n"
      "ds_read_b128 v[114:117], v2 offset:25088\n"
      "v_mfma_f32_16x16x32_bf16 v[218:221], v[228:231], v[206:209], v[218:221]\n"
      "ds_read_b128 v[138:141], v2 offset:31232\n"
      "v_mul_u32_u24 v81, 24, v86\n"
      "v_mfma_f32_16x16x32_bf16 v[104:107], v[248:251], v[174:177], v[104:107]\n"
      "v_sub_u32 v84, v84, v77\n"
      "v_mfma_f32_16x16x32_bf16 v[104:107], v[252:255], v[206:209], v[104:107]\n"
      "v_fma_f32 v176, v120, v120, s52\n"
      "v_mul_f32 v177, v121, v121\n"
      "v_mfma_f32_16x16x32_bf16 v[124:127], a[216:219], v[212:215], 0\n"
      "v_fmac_f32 v152, v144, v144\n"
      "v_fmac_f32 v177, v123, v123\n"
      "v_mfma_f32_16x16x32_bf16 v[124:127], a[220:223], v[194:197], v[124:127]\n"
      "v_fmac_f32 v176, v122, v122\n"
      "v_fmac_f32 v176, v218, v218\n"
      "v_mfma_f32_16x16x32_bf16 v[206:209], a[240:243], v[212:215], 0\n"
      "ds_read_b128 v[172:175], v7 offset:30720\n"
      "v_fmac_f32 v177, v219, v219\n"
      "v_mfma_f32_16x16x32_bf16 v[206:209], a[244:247], v[194:197], v[206:209]\n"
      "s_waitcnt lgkmcnt(1)\n"
      "v_fmac_f32 v176, v220, v220\n"
      "v_fmac_f32 v177, v221, v221\n"
      "v_mfma_f32_16x16x32_bf16 v[182:185], v[232:235], v[212:215], 0\n"
      "ds_read_b128 v[210:213], v76 offset:6144\n"
      "v_fmac_f32 v153, v145, v145\n"
      "v_mfma_f32_16x16x32_bf16 v[182:185], v[236:239], v[194:197], v[182:185]\n"
      "buffer_store_dwordx4 v[200:203], v22, s[24:27], s46 offen sc1\n"
      "v_mfma_f32_16x16x32_bf16 v[124:127], a[224:227], v[114:117], v[124:127]\n"
      "v_fmac_f32 v177, v105, v105\n"
      "v_fmac_f32 v176, v104, v104\n"
      "v_mfma_f32_16x16x32_bf16 v[124:127], a[228:231], v[100:103], v[124:127]\n"
      "v_fmac_f32_e64 v153, v157, v157\n"
      "v_mfma_f32_16x16x32_bf16 v[206:209], a[248:251], v[114:117], v[206:209]\n"
      "ds_write_b16 v12, v164 offset:16\n"
      "v_mfma_f32_16x16x32_bf16 v[206:209], a[252:255], v[100:103], v[206:209]\n"
      "ds_read_b128 v[194:197], v76\n"
      "v_fmac_f32 v152, v156, v156\n"
      "v_mfma_f32_16x16x32_bf16 v[182:185], v[240:243], v[114:117], v[182:185]\n"
      "v_fmac_f32 v177, v107, v107\n"
      "v_fmac_f32 v176, v106, v106\n"
      "v_mfma_f32_16x16x32_bf16 v[182:185], v[244:247], v[100:103], v[182:185]\n"
      "v_fmac_f32 v152, v158, v158\n"
      "v_mfma_f32_16x16x32_bf16 v[124:127], a[232:235], v[138:141], v[124:127]\n"
      "v_fmac_f32 v153, v159, v159\n"
      "v_add_f32 v152, v152, v153\n"
      "v_mfma_f32_16x16x32_bf16 v[124:127], a[236:239], v[128:131], v[124:127]\n"
      "s_waitcnt lgkmcnt(2)\n"
      "v_lshrrev_b32 v78, 16, v78\n"
      "v_add_f32_e64 v176, v176, v177\n"
      "v_mfma_f32_16x16x32_bf16 v[206:209], v[224:227], v[138:141], v[206:209]\n"
      "buffer_store_dwordx4 v[172:175], v23, s[24:27], s46 offen sc1\n"
      "v_mfma_f32_16x16x32_bf16 v[206:209], v[228:231], v[128:131], v[206:209]\n"
      "v_permlane16_swap_b32_e64 v152, v176\n"
      "v_mfma_f32_16x16x32_bf16 v[182:185], v[248:251], v[138:141], v[182:185]\n"
      "v_fma_f32 v138, v124, v124, s52\n"
      "v_mul_f32 v139, v125, v125\n"
      "v_mfma_f32_16x16x32_bf16 v[182:185], v[252:255], v[128:131], v[182:185]\n"
      "s_waitcnt lgkmcnt(0)\n"
      "v_fmac_f32 v139, v127, v127\n"
      "v_add_f32 v152, v152, v176\n"
      "v_mfma_f32_16x16x32_bf16 v[172:175], a[216:219], v[194:197], 0\n"
      "v_fmac_f32 v138, v126, v126\n"
      "v_fmac_f32 v139, v207, v207\n"
      "v_mfma_f32_16x16x32_bf16 v[172:175], a[220:223], v[134:137], v[172:175]\n"
      "v_bfe_i32 v77, v37, 30, 1\n"
      "v_fmac_f32 v138, v206, v206\n"
      "v_mfma_f32_16x16x32_bf16 v[164:167], a[240:243], v[194:197], 0\n"
      "v_fmac_f32 v138, v208, v208\n"
      "v_fmac_f32 v139, v209, v209\n"
      "v_mfma_f32_16x16x32_bf16 v[164:167], a[244:247], v[134:137], v[164:167]\n"
      "v_fmac_f32 v139, v183, v183\n"
      "v_fmac_f32 v138, v182, v182\n"
      "v_mfma_f32_16x16x32_bf16 v[130:133], v[232:235], v[194:197], 0\n"
      "v_fmac_f32 v138, v184, v184\n"
      "v_fmac_f32 v139, v185, v185\n"
      "v_mfma_f32_16x16x32_bf16 v[130:133], v[236:239], v[134:137], v[130:133]\n"
      "v_add_f32 v138, v138, v139\n"
      "v_mov_b32 v176, v138\n"
      "v_mfma_f32_16x16x32_bf16 v[172:175], a[224:227], v[178:181], v[172:175]\n"
      "s_nop 0\n"
      "v_permlane16_swap_b32 v138, v176\n"
      "v_add_f32 v138, v138, v176\n"
      "v_mfma_f32_16x16x32_bf16 v[172:175], a[228:231], v[210:213], v[172:175]\n"
      "s_nop 0\n"
      "v_permlane32_swap_b32 v152, v138\n"
      "v_add_f32_e64 v152, v152, v138\n"
      "v_mfma_f32_16x16x32_bf16 v[164:167], a[248:251], v[178:181], v[164:167]\n"
      "ds_write_b32 v31, v152\n"
      "v_mfma_f32_16x16x32_bf16 v[164:167], a[252:255], v[210:213], v[164:167]\n"
      "buffer_store_dwordx4 v[110:113], v25, s[24:27], s46 offen sc1\n"
      "v_mfma_f32_16x16x32_bf16 v[130:133], v[240:243], v[178:181], v[130:133]\n"
      "v_mul_u32_u24_e64 v128, 24, v216\n"
      "v_readlane_b32 s46, v37, s42\n"
      "v_mfma_f32_16x16x32_bf16 v[130:133], v[244:247], v[210:213], v[130:133]\n"
      "v_sub_u32 v81, v80, v81\n"
      "s_or_b32 s95, s46, s48\n"
      "v_mfma_f32_16x16x32_bf16 v[172:175], a[232:235], v[96:99], v[172:175]\n"
      "s_cmp_ge_u32 s95, 0x40000000\n"
      "s_cbranch_scc1 L_rare_35_%=\n"
      "L_back_36_%=:\n"
      "v_and_b32_e64 v80, 7, v86\n"
      "v_mfma_f32_16x16x32_bf16 v[172:175], a[236:239], v[148:151], v[172:175]\n"
      "s_waitcnt lgkmcnt(0)\n"
      "s_barrier\n"
      "ds_read_b128 v[136:139], v9\n"
      "ds_read_b128 v[178:181], v76 offset:256\n"
      "v_mfma_f32_16x16x32_bf16 v[164:167], v[224:227], v[96:99], v[164:167]\n"
      "ds_read_b128 v[212:215], v76 offset:2304\n"
      "ds_read_b128 v[110:113], v7\n"
      "ds_read_b128 v[200:203], v76 offset:4352\n"
      "v_mfma_f32_16x16x32_bf16 v[164:167], v[228:231], v[148:151], v[164:167]\n"
      "ds_read_b128 v[194:197], v7 offset:8192\n"
      "v_fma_f32 v134, v172, v172, s52\n"
      "v_mul_f32_e64 v135, v173, v173\n"
      "v_mfma_f32_16x16x32_bf16 v[130:133], v[248:251], v[96:99], v[130:133]\n"
      "ds_read_b128 v[100:103], v7 offset:4096\n"
      "ds_read_b128 v[96:99], v76 offset:6400\n"
      "ds_read_b128 v[116:119], v76 offset:10496\n"
      "v_pk_fma_f32 v[134:135], v[174:175], v[174:175], v[134:135]\n"
      "v_pk_fma_f32 v[134:135], v[164:165], v[164:165], v[134:135]\n"
      "v_mfma_f32_16x16x32_bf16 v[130:133], v[252:255], v[148:151], v[130:133]\n"
      "ds_read_b128 v[152:155], v7 offset:12288\n"
      "s_waitcnt lgkmcnt(8)\n"
      "v_add_f32 v138, v138, v139\n"
      "v_add_f32 v136, v136, v137\n"
      "v_add_f32 v136, v136, v138\n"
      "v_pk_fma_f32 v[134:135], v[166:167], v[166:167], v[134:135]\n"
      "v_rsq_f32 v136, v136\n"
      "v_mfma_f32_16x16x32_bf16 v[168:171], a[216:219], v[178:181], 0\n"
      "s_waitcnt lgkmcnt(6)\n"
      "v_mul_f32 v136, 0x415db3d7, v136\n"
      "v_mov_b32 v138, v136\n"
      "v_mfma_f32_16x16x32_bf16 v[168:171], a[220:223], v[212:215], v[168:171]\n"
      "buffer_store_dwordx4 v[110:113], v20, s[24:27], s46 offen sc1\n"
      "s_nop 1\n"
      "v_mfma_f32_16x16x32_bf16 v[112:115], a[240:243], v[178:181], 0\n"
      "s_waitcnt lgkmcnt(3)\n"
      "v_permlane32_swap_b32 v136, v138\n"
      "v_mfma_f32_16x16x32_bf16 v[112:115], a[244:247], v[212:215], v[112:115]\n"
      "ds_read_b128 v[148:151], v7 offset:16384\n"
      "v_mov_b32_e64 v137, v136\n"
      "v_mfma_f32_16x16x32_bf16 v[108:111], v[232:235], v[178:181], 0\n"
      "ds_read_b128 v[176:179], v76 offset:8448\n"
      "v_permlane16_swap_b32 v136, v137\n"
      "v_mfma_f32_16x16x32_bf16 v[108:111], v[236:239], v[212:215], v[108:111]\n"
      "buffer_store_dwordx4 v[100:103], v21, s[24:27], s46 offen sc1\n"
      "v_pk_mul_f32 v[104:105], v[104:105], v[136:137] op_sel:[0,1]\n"
      "v_pk_fma_f32 v[134:135], v[130:131], v[130:131], v[134:135]\n"
      "v_pk_mul_f32 v[106:107], v[106:107], v[136:137] op_sel:[0,1]\n"
      "v_pk_mul_f32 v[208:209], v[208:209], v[138:139] op_sel_hi:[1,0]\n"
      "v_pk_mul_f32 v[124:125], v[124:125], v[138:139] op_sel_hi:[1,0]\n"
      "v_pk_mul_f32 v[206:207], v[206:207], v[138:139] op_sel_hi:[1,0]\n"
      "v_pk_mul_f32 v[120:121], v[120:121], v[136:137] op_sel:[0,1]\n"
      "v_pk_mul_f32 v[218:219], v[218:219], v[136:137] op_sel:[0,1]\n"
      "v_pk_mul_f32 v[158:159], v[158:159], v[136:137] op_sel_hi:[1,0]\n"
      "v_pk_mul_f32 v[188:189], v[188:189], v[136:137] op_sel_hi:[1,0]\n"
      "v_pk_mul_f32 v[184:185], v[184:185], v[138:139] op_sel_hi:[1,0]\n"
      "v_pk_mul_f32 v[144:145], v[144:145], v[136:137] op_sel_hi:[1,0]\n"
      "v_pk_mul_f32 v[186:187], v[186:187], v[136:137] op_sel_hi:[1,0]\n"
      "v_pk_mul_f32 v[182:183], v[182:183], v[138:139] op_sel_hi:[1,0]\n"
      "v_pk_mul_f32 v[126:127], v[126:127], v[138:139] op_sel_hi:[1,0]\n"
      "v_pk_mul_f32 v[156:157], v[156:157], v[136:137] op_sel_hi:[1,0]\n"
      "v_mfma_f32_16x16x32_bf16 v[168:171], a[224:227], v[200:203], v[168:171]\n"
      "s_waitcnt lgkmcnt(0)\n"
      "v_cvt_pk_bf16_f32 v120, v120, v121\n"
      "v_cvt_pk_bf16_f32 v104, v104, v105\n"
      "v_mfma_f32_16x16x32_bf16 v[168:171], a[228:231], v[96:99], v[168:171]\n"
      "v_cvt_pk_bf16_f32 v124, v124, v125\n"
      "v_cvt_pk_bf16_f32 v125, v126, v127\n"
      "v_mfma_f32_16x16x32_bf16 v[112:115], a[248:251], v[200:203], v[112:115]\n"
      "v_cvt_pk_bf16_f32 v156, v156, v157\n"
      "v_cvt_pk_bf16_f32 v182, v182, v183\n"
      "v_mfma_f32_16x16x32_bf16 v[112:115], a[252:255], v[96:99], v[112:115]\n"
      "buffer_store_dwordx4 v[194:197], v22, s[24:27], s46 offen sc1\n"
      "v_pk_fma_f32 v[134:135], v[132:133], v[132:133], v[134:135]\n"
      "v_cvt_pk_bf16_f32 v105, v106, v107\n"
      "v_pk_mul_f32 v[220:221], v[220:221], v[136:137] op_sel:[0,1]\n"
      "v_pk_mul_f32 v[122:123], v[122:123], v[136:137] op_sel:[0,1]\n"
      "v_mfma_f32_16x16x32_bf16 v[108:111], v[240:243], v[200:203], v[108:111]\n"
      "ds_write_b64 v6, v[104:105] offset:24576\n"
      "v_cvt_pk_bf16_f32 v127, v208, v209\n"
      "v_mfma_f32_16x16x32_bf16 v[108:111], v[244:247], v[96:99], v[108:111]\n"
      "v_cvt_pk_bf16_f32 v183, v184, v185\n"
      "v_cvt_pk_bf16_f32 v126, v206, v207\n"
      "v_mfma_f32_16x16x32_bf16 v[168:171], a[232:235], v[176:179], v[168:171]\n"
      "v_cvt_pk_bf16_f32 v121, v122, v123\n"
      "v_cvt_pk_bf16_f32 v123, v220, v221\n"
      "v_mfma_f32_16x16x32_bf16 v[168:171], a[236:239], v[116:119], v[168:171]\n"
      "v_cvt_pk_bf16_f32 v122, v218, v219\n"
      "v_cvt_pk_bf16_f32 v157, v158, v159\n"
      "v_mfma_f32_16x16x32_bf16 v[112:115], v[224:227], v[176:179], v[112:115]\n"
      "ds_write_b64 v6, v[182:183] offset:30720\n"
      "v_mfma_f32_16x16x32_bf16 v[112:115], v[228:231], v[116:119], v[112:115]\n"
      "ds_write_b64 v6, v[156:157] offset:18432\n"
      "v_mfma_f32_16x16x32_bf16 v[108:111], v[248:251], v[176:179], v[108:111]\n"
      "ds_write_b128 v5, v[120:123] offset:24576\n"
      "v_mfma_f32_16x16x32_bf16 v[108:111], v[252:255], v[116:119], v[108:111]\n"
      "ds_write_b128 v5, v[124:127] offset:30720\n"
      "v_mul_f32_e64 v183, v169, v169\n"
      "v_fma_f32 v182, v168, v168, s52\n"
      "v_pk_fma_f32 v[182:183], v[170:171], v[170:171], v[182:183]\n"
      "v_pk_fma_f32 v[182:183], v[112:113], v[112:113], v[182:183]\n"
      "v_cvt_pk_bf16_f32 v186, v186, v187\n"
      "v_pk_fma_f32 v[182:183], v[114:115], v[114:115], v[182:183]\n"
      "v_add_f32_e64 v134, v134, v135\n"
      "v_pk_fma_f32 v[182:183], v[108:109], v[108:109], v[182:183]\n"
      "v_pk_fma_f32 v[182:183], v[110:111], v[110:111], v[182:183]\n"
      "v_add_f32 v182, v182, v183\n"
      "v_pk_mul_f32 v[142:143], v[142:143], v[136:137] op_sel_hi:[1,0]\n"
      "v_cvt_pk_bf16_f32 v187, v188, v189\n"
      "v_permlane16_swap_b32 v134, v182\n"
      "v_add_f32 v134, v134, v182\n"
      "v_mov_b32 v182, v134\n"
      "v_cvt_pk_bf16_f32 v189, v144, v145\n"
      "v_cvt_pk_bf16_f32 v188, v142, v143\n"
      "v_permlane32_swap_b32_e64 v134, v182\n"
      "v_add_f32 v134, v134, v182\n"
      "s_add_u32 s42, s42, 1\n"
      "ds_write_b128 v5, v[186:189] offset:18432\n"
      "ds_write_b32 v31, v134 offset:4608\n"
      "v_and_b32 v77, 196224, v77\n"
      "buffer_store_dwordx4 v[152:155], v23, s[24:27], s46 offen sc1\n"
      "v_sub_u32 v128, v217, v128\n"
      "v_mul_u32_u24 v127, 24, v78\n"
      "v_sub_u32 v127, v85, v127\n"
      "v_and_b32 v85, 7, v78\n"
      "v_lshl_add_u32 v86, v86, 2, 12\n"
      "v_sub_u32_e64 v84, v84, v77\n"
      "buffer_store_dwordx4 v[148:151], v25, s[24:27], s46 offen sc1\n"
      "v_subrev_u32 v135, 73728, v5\n"
      "s_waitcnt lgkmcnt(0)\n"
      "s_barrier\n"
      "ds_read_b128 v[152:155], v9 offset:4608\n"
      "v_lshl_add_u32 v78, v78, 2, 12\n"
      "v_readlane_b32 s46, v37, s42\n"
      "s_nop 0\n"
      "s_or_b32 s95, s46, s48\n"
      "ds_read_b128 v[186:189], v7 offset:26624\n"
      "ds_read_b128 v[182:185], v7 offset:30720\n"
      "ds_read_b128 v[142:145], v7 offset:18432\n"
      "v_subrev_u32 v151, 73728, v6\n"
      "s_cmp_ge_u32 s95, 0x40000000\n"
      "s_cbranch_scc1 L_rare_39_%=\n"
      "L_back_40_%=:\n"
      "ds_bpermute_b32 v106, v86, v84\n"
      "ds_read_b128 v[218:221], v7 offset:22528\n"
      "v_add_u32 v12, 32, v12\n"
      "s_waitcnt lgkmcnt(5)\n"
      "v_add_f32 v154, v154, v155\n"
      "v_add_f32 v152, v152, v153\n"
      "v_add_f32 v152, v152, v154\n"
      "v_rsq_f32 v152, v152\n"
      "v_and_b32 v217, 7, v216\n"
      "v_mul_f32 v152, 0x415db3d7, v152\n"
      "v_mov_b32 v154, v152\n"
      "v_xor_b32 v128, v128, v217\n"
      "v_lshl_add_u32 v216, v216, 2, 12\n"
      "v_permlane32_swap_b32 v152, v154\n"
      "v_mov_b32 v153, v152\n"
      "v_xor_b32 v81, v81, v80\n"
      "s_nop 0\n"
      "v_permlane16_swap_b32 v152, v153\n"
      "v_pk_mul_f32 v[170:171], v[170:171], v[152:153] op_sel:[0,1]\n"
      "v_pk_mul_f32 v[168:169], v[168:169], v[152:153] op_sel:[0,1]\n"
      "v_pk_mul_f32 v[112:113], v[112:113], v[152:153] op_sel:[0,1]\n"
      "v_pk_mul_f32 v[132:133], v[132:133], v[152:153] op_sel_hi:[1,0]\n"
      "v_pk_mul_f32 v[164:165], v[164:165], v[152:153] op_sel_hi:[1,0]\n"
      "v_pk_mul_f32 v[172:173], v[172:173], v[152:153] op_sel_hi:[1,0]\n"
      "v_pk_mul_f32 v[174:175], v[174:175], v[152:153] op_sel_hi:[1,0]\n"
      "v_pk_mul_f32 v[166:167], v[166:167], v[152:153] op_sel_hi:[1,0]\n"
      "v_cvt_pk_bf16_f32 v168, v168, v169\n"
      "v_cvt_pk_bf16_f32 v172, v172, v173\n"
      "v_cvt_pk_bf16_f32 v169, v170, v171\n"
      "v_pk_mul_f32 v[114:115], v[114:115], v[152:153] op_sel:[0,1]\n"
      "v_cvt_pk_bf16_f32 v173, v174, v175\n"
      "v_cvt_pk_bf16_f32 v175, v166, v167\n"
      "v_pk_mul_f32 v[130:131], v[130:131], v[152:153] op_sel_hi:[1,0]\n"
      "v_pk_mul_f32 v[108:109], v[108:109], v[152:153] op_sel:[0,1]\n"
      "v_cvt_pk_bf16_f32 v130, v130, v131\n"
      "v_pk_mul_f32 v[110:111], v[110:111], v[152:153] op_sel:[0,1]\n"
      "v_cvt_pk_bf16_f32 v131, v132, v133\n"
      "v_cvt_pk_bf16_f32 v170, v112, v113\n"
      "v_cvt_pk_bf16_f32 v108, v108, v109\n"
      "v_cvt_pk_bf16_f32 v171, v114, v115\n"
      "v_cvt_pk_bf16_f32 v174, v164, v165\n"
      "s_add_u32 s42, s42, 1\n"
      "ds_write_b128 v135, v[168:171] offset:6144\n"
      "ds_write_b128 v135, v[172:175]\n"
      "ds_write_b64 v151, v[130:131]\n"
      "v_cvt_pk_bf16_f32 v109, v110, v111\n"
      "s_waitcnt lgkmcnt(3)\n"
      "buffer_store_dwordx4 v[142:145], v20, s[24:27], s46 offen sc1\n"
      "ds_write_b64 v151, v[108:109] offset:6144\n"
      "s_nop 0\n"
      "ds_bpermute_b32 v142, v78, v84\n"
      "ds_read_b128 v[152:155], v7 offset:34816\n"
      "buffer_store_dwordx4 v[218:221], v21, s[24:27], s46 offen sc1\n"
      "v_xor_b32 v127, v127, v85\n"
      "ds_bpermute_b32 v166, v216, v84\n"
      "buffer_store_dwordx4 v[186:189], v22, s[24:27], s46 offen sc1\n"
      "s_waitcnt lgkmcnt(3)\n"
      "s_barrier\n"
      "ds_read_b128 v[130:133], v82\n"
      "buffer_store_dwordx4 v[182:185], v23, s[24:27], s46 offen sc1\n"
      "ds_read_b128 v[110:113], v82 offset:1024\n"
      "s_nop 0\n"
      "ds_read_b128 v[184:187], v82 offset:2048\n"
      "s_waitcnt lgkmcnt(3)\n"
      "buffer_store_dwordx4 v[152:155], v25, s[24:27], s46 offen sc1\n"
      "v_lshl_add_u32 v166, v128, 4, v166\n"
      "v_lshl_add_u32 v106, v81, 4, v106\n"
      "s_waitcnt lgkmcnt(2)\n"
      "buffer_store_dwordx4 v[130:133], v166, s[24:27], 0 offen sc1\n"
      "v_lshl_add_u32 v142, v127, 4, v142\n"
      "s_waitcnt lgkmcnt(1)\n"
      "buffer_store_dwordx4 v[110:113], v142, s[24:27], 0 offen sc1\n"
      "s_waitcnt lgkmcnt(0)\n"
      "buffer_store_dwordx4 v[184:187], v106, s[24:27], 0 offen sc1\n"
      "s_endpgm\n"
      "L_rare_1_%=:\n"
      "s_nop 7\n"
      "s_waitcnt lgkmcnt(0)\n"
      "s_add_u32 s72, s42, 1\n"
      "v_readlane_b32 s77, v38, s72\n"
      "v_subrev_u32 v189, 115456, v9\n"
      "s_and_b32 s73, s77, -16\n"
      "s_mov_b32 m0, 110592\n"
      "s_nop 0\n"
      "buffer_load_dwordx4 v189, s[28:31], s73 offen  lds\n"
      "s_add_u32 s73, s73, 1024\n"
      "s_mov_b32 m0, 111616\n"
      "s_nop 0\n"
      "buffer_load_dwordx4 v189, s[28:31], s73 offen  lds\n"
      "s_add_u32 s73, s73, 1024\n"
      "s_mov_b32 m0, 112640\n"
      "s_nop 0\n"
      "buffer_load_dwordx4 v189, s[28:31], s73 offen  lds\n"
      "s_add_u32 s73, s73, 1024\n"
      "s_mov_b32 m0, 113664\n"
      "s_nop 0\n"
      "buffer_load_dwordx4 v189, s[28:31], s73 offen  lds\n"
      "s_add_u32 s73, s73, 1024\n"
      "s_mov_b32 m0, 114688\n"
      "s_mov_b32 exec_hi, 0xffff\n"
      "buffer_load_dwordx4 v189, s[28:31], s73 offen  lds\n"
      "s_mov_b32 exec_hi, -1\n"
      "s_add_u32 s73, s73, 1024\n"
      "s_and_b32 s77, s77, 15\n"
      "s_waitcnt lgkmcnt(0)\n"
      "s_nop 4\n"
      "s_branch L_back_2_%=\n"
      "L_rare_3_%=:\n"
      "s_nop 7\n"
      "s_waitcnt lgkmcnt(0)\n"
      "s_waitcnt vmcnt(0)\n"
      "v_subrev_u32 v179, 115456, v9\n"
      "v_bfe_u32 v138, v179, 4, 4\n"
      "v_mul_u32_u24 v138, 98, v138\n"
      "v_lshrrev_b32 v179, 8, v179\n"
      "v_lshl_add_u32 v138, v179, 3, v138\n"
      "v_add_u32 v138, s77, v138\n"
      "v_and_b32 v139, 2, v138\n"
      "v_and_b32 v138, -4, v138\n"
      "v_add_u32 v138, 110592, v138\n"
      "ds_read2_b32 v[134:135], v138 offset0:0 offset1:1\n"
      "ds_read2_b32 v[136:137], v138 offset0:8 offset1:9\n"
      "ds_read2_b32 v[96:97], v138 offset0:16 offset1:17\n"
      "ds_read2_b32 v[140:141], v138 offset0:2 offset1:10\n"
      "ds_read2_b32 v[98:99], v138 offset0:18 offset1:24\n"
      "s_waitcnt lgkmcnt(4)\n"
      "v_alignbyte_b32 v134, v135, v134, v139\n"
      "s_waitcnt lgkmcnt(1)\n"
      "v_alignbyte_b32 v135, v140, v135, v139\n"
      "v_lshlrev_b32 v179, 16, v134\n"
      "v_fmac_f32 v156, s53, v179\n"
      "v_and_b32 v179, 0xffff0000, v134\n"
      "v_fmac_f32 v157, s53, v179\n"
      "v_lshlrev_b32 v179, 16, v135\n"
      "v_fmac_f32_e64 v158, s53, v179\n"
      "v_and_b32 v179, 0xffff0000, v135\n"
      "v_fmac_f32 v159, s53, v179\n"
      "v_alignbyte_b32 v136, v137, v136, v139\n"
      "v_alignbyte_b32 v137, v141, v137, v139\n"
      "v_lshlrev_b32 v179, 16, v136\n"
      "v_fmac_f32 v170, s53, v179\n"
      "v_and_b32 v179, 0xffff0000, v136\n"
      "v_fmac_f32 v171, s53, v179\n"
      "v_lshlrev_b32 v179, 16, v137\n"
      "v_fmac_f32 v172, s53, v179\n"
      "v_and_b32 v179, 0xffff0000, v137\n"
      "v_fmac_f32 v173, s53, v179\n"
      "v_alignbyte_b32 v96, v97, v96, v139\n"
      "s_waitcnt lgkmcnt(0)\n"
      "v_alignbyte_b32 v97, v98, v97, v139\n"
      "v_lshlrev_b32 v179, 16, v96\n"
      "v_fmac_f32 v200, s53, v179\n"
      "v_and_b32 v179, 0xffff0000, v96\n"
      "v_fmac_f32 v201, s53, v179\n"
      "v_lshlrev_b32 v179, 16, v97\n"
      "v_fmac_f32 v202, s53, v179\n"
      "v_and_b32 v179, 0xffff0000, v97\n"
      "v_fmac_f32 v203, s53, v179\n"
      "v_alignbyte_b32 v179, v99, v99, v139\n"
      "v_lshlrev_b32 v179, 16, v179\n"
      "v_fmac_f32 v116, s53, v179\n"
      "v_max3_f32 v138, v156, v157, v158\n"
      "v_max3_f32 v139, v159, v170, v171\n"
      "v_max3_f32 v138, v138, v172, v173\n"
      "v_max3_f32 v139, v139, v200, v201\n"
      "v_max3_f32 v138, v138, v202, v203\n"
      "v_max3_f32 v138, v138, v139, v116\n"
      "v_mov_b32 v179, v138\n"
      "s_nop 1\n"
      "v_permlane16_swap_b32 v138, v179\n"
      "v_max_f32 v138, v138, v179\n"
      "v_mov_b32 v179, v138\n"
      "s_nop 1\n"
      "v_permlane32_swap_b32 v138, v179\n"
      "v_max_f32 v138, v138, v179\n"
      "v_sub_f32 v156, v156, v138\n"
      "v_sub_f32 v157, v157, v138\n"
      "v_sub_f32 v158, v158, v138\n"
      "v_sub_f32 v159, v159, v138\n"
      "v_sub_f32 v170, v170, v138\n"
      "v_sub_f32 v171, v171, v138\n"
      "v_sub_f32 v172, v172, v138\n"
      "v_sub_f32 v173, v173, v138\n"
      "v_sub_f32 v200, v200, v138\n"
      "v_sub_f32 v201, v201, v138\n"
      "v_sub_f32 v202, v202, v138\n"
      "v_sub_f32 v203, v203, v138\n"
      "v_sub_f32_e64 v116, v116, v138\n"
      "s_waitcnt lgkmcnt(0)\n"
      "s_nop 4\n"
      "s_branch L_back_4_%=\n"
      "L_rare_5_%=:\n"
      "s_nop 7\n"
      "s_waitcnt lgkmcnt(0)\n"
      "s_waitcnt vmcnt(0)\n"
      "v_subrev_u32 v121, 115456, v9\n"
      "v_bfe_u32 v138, v121, 4, 4\n"
      "v_mul_u32_u24 v138, 98, v138\n"
      "v_lshrrev_b32 v121, 8, v121\n"
      "v_lshl_add_u32 v138, v121, 3, v138\n"
      "v_add_u32 v138, s77, v138\n"
      "v_and_b32 v139, 2, v138\n"
      "v_and_b32 v138, -4, v138\n"
      "v_add_u32 v138, 112160, v138\n"
      "ds_read2_b32 v[140:141], v138 offset0:0 offset1:1\n"
      "ds_read2_b32 v[98:99], v138 offset0:8 offset1:9\n"
      "ds_read2_b32 v[96:97], v138 offset0:16 offset1:17\n"
      "ds_read2_b32 v[144:145], v138 offset0:2 offset1:10\n"
      "ds_read2_b32 v[142:143], v138 offset0:18 offset1:24\n"
      "s_waitcnt lgkmcnt(4)\n"
      "v_alignbyte_b32 v140, v141, v140, v139\n"
      "s_waitcnt lgkmcnt(1)\n"
      "v_alignbyte_b32 v141, v144, v141, v139\n"
      "v_lshlrev_b32 v121, 16, v140\n"
      "v_fmac_f32 v152, s53, v121\n"
      "v_and_b32 v121, 0xffff0000, v140\n"
      "v_fmac_f32 v153, s53, v121\n"
      "v_lshlrev_b32 v121, 16, v141\n"
      "v_fmac_f32 v154, s53, v121\n"
      "v_and_b32 v121, 0xffff0000, v141\n"
      "v_fmac_f32 v155, s53, v121\n"
      "v_alignbyte_b32 v98, v99, v98, v139\n"
      "v_alignbyte_b32 v99, v145, v99, v139\n"
      "v_lshlrev_b32 v121, 16, v98\n"
      "v_fmac_f32 v146, s53, v121\n"
      "v_and_b32 v121, 0xffff0000, v98\n"
      "v_fmac_f32 v147, s53, v121\n"
      "v_lshlrev_b32 v121, 16, v99\n"
      "v_fmac_f32_e64 v148, s53, v121\n"
      "v_and_b32 v121, 0xffff0000, v99\n"
      "v_fmac_f32 v149, s53, v121\n"
      "v_alignbyte_b32 v96, v97, v96, v139\n"
      "s_waitcnt lgkmcnt(0)\n"
      "v_alignbyte_b32 v97, v142, v97, v139\n"
      "v_lshlrev_b32 v121, 16, v96\n"
      "v_fmac_f32 v112, s53, v121\n"
      "v_and_b32 v121, 0xffff0000, v96\n"
      "v_fmac_f32 v113, s53, v121\n"
      "v_lshlrev_b32 v121, 16, v97\n"
      "v_fmac_f32_e64 v114, s53, v121\n"
      "v_and_b32 v121, 0xffff0000, v97\n"
      "v_fmac_f32 v115, s53, v121\n"
      "v_alignbyte_b32 v121, v143, v143, v139\n"
      "v_lshlrev_b32 v121, 16, v121\n"
      "v_fmac_f32_e64 v162, s53, v121\n"
      "v_max3_f32 v138, v152, v153, v154\n"
      "v_max3_f32 v139, v155, v146, v147\n"
      "v_max3_f32 v138, v138, v148, v149\n"
      "v_max3_f32 v139, v139, v112, v113\n"
      "v_max3_f32 v138, v138, v114, v115\n"
      "v_max3_f32 v138, v138, v139, v162\n"
      "v_mov_b32 v121, v138\n"
      "s_nop 1\n"
      "v_permlane16_swap_b32 v138, v121\n"
      "v_max_f32 v138, v138, v121\n"
      "v_mov_b32 v121, v138\n"
      "s_nop 1\n"
      "v_permlane32_swap_b32 v138, v121\n"
      "v_max_f32 v138, v138, v121\n"
      "v_sub_f32 v152, v152, v138\n"
      "v_sub_f32 v153, v153, v138\n"
      "v_sub_f32 v154, v154, v138\n"
      "v_sub_f32 v155, v155, v138\n"
      "v_sub_f32 v146, v146, v138\n"
      "v_sub_f32 v147, v147, v138\n"
      "v_sub_f32 v148, v148, v138\n"
      "v_sub_f32 v149, v149, v138\n"
      "v_sub_f32 v112, v112, v138\n"
      "v_sub_f32 v113, v113, v138\n"
      "v_sub_f32 v114, v114, v138\n"
      "v_sub_f32 v115, v115, v138\n"
      "v_sub_f32_e64 v162, v162, v138\n"
      "s_waitcnt lgkmcnt(0)\n"
      "s_nop 4\n"
      "s_branch L_back_6_%=\n"
      "L_rare_7_%=:\n"
      "s_nop 7\n"
      "s_waitcnt lgkmcnt(0)\n"
      "s_waitcnt vmcnt(0)\n"
      "v_subrev_u32 v116, 115456, v9\n"
      "v_bfe_u32 v115, v116, 4, 4\n"
      "v_mul_u32_u24 v115, 98, v115\n"
      "v_lshrrev_b32_e64 v116, 8, v116\n"
      "v_lshl_add_u32 v115, v116, 3, v115\n"
      "v_add_u32 v115, s77, v115\n"
      "v_and_b32 v114, 2, v115\n"
      "v_and_b32_e64 v115, -4, v115\n"
      "v_add_u32 v115, 113728, v115\n"
      "ds_read2_b32 v[142:143], v115 offset0:0 offset1:1\n"
      "ds_read2_b32 v[140:141], v115 offset0:8 offset1:9\n"
      "ds_read2_b32 v[96:97], v115 offset0:16 offset1:17\n"
      "ds_read2_b32 v[98:99], v115 offset0:2 offset1:10\n"
      "ds_read2_b32 v[168:169], v115 offset0:18 offset1:24\n"
      "s_waitcnt lgkmcnt(4)\n"
      "v_alignbyte_b32 v142, v143, v142, v114\n"
      "s_waitcnt lgkmcnt(1)\n"
      "v_alignbyte_b32 v143, v98, v143, v114\n"
      "v_lshlrev_b32 v116, 16, v142\n"
      "v_fmac_f32 v190, s53, v116\n"
      "v_and_b32 v116, 0xffff0000, v142\n"
      "v_fmac_f32 v191, s53, v116\n"
      "v_lshlrev_b32 v116, 16, v143\n"
      "v_fmac_f32_e64 v192, s53, v116\n"
      "v_and_b32 v116, 0xffff0000, v143\n"
      "v_fmac_f32 v193, s53, v116\n"
      "v_alignbyte_b32 v140, v141, v140, v114\n"
      "v_alignbyte_b32 v141, v99, v141, v114\n"
      "v_lshlrev_b32 v116, 16, v140\n"
      "v_fmac_f32 v134, s53, v116\n"
      "v_and_b32 v116, 0xffff0000, v140\n"
      "v_fmac_f32 v135, s53, v116\n"
      "v_lshlrev_b32 v116, 16, v141\n"
      "v_fmac_f32 v136, s53, v116\n"
      "v_and_b32 v116, 0xffff0000, v141\n"
      "v_fmac_f32 v137, s53, v116\n"
      "v_alignbyte_b32 v96, v97, v96, v114\n"
      "s_waitcnt lgkmcnt(0)\n"
      "v_alignbyte_b32 v97, v168, v97, v114\n"
      "v_lshlrev_b32 v116, 16, v96\n"
      "v_fmac_f32 v178, s53, v116\n"
      "v_and_b32 v116, 0xffff0000, v96\n"
      "v_fmac_f32 v179, s53, v116\n"
      "v_lshlrev_b32 v116, 16, v97\n"
      "v_fmac_f32 v180, s53, v116\n"
      "v_and_b32 v116, 0xffff0000, v97\n"
      "v_fmac_f32 v181, s53, v116\n"
      "v_alignbyte_b32 v116, v169, v169, v114\n"
      "v_lshlrev_b32 v116, 16, v116\n"
      "v_fmac_f32 v188, s53, v116\n"
      "v_max3_f32 v115, v190, v191, v192\n"
      "v_max3_f32 v114, v193, v134, v135\n"
      "v_max3_f32 v115, v115, v136, v137\n"
      "v_max3_f32 v114, v114, v178, v179\n"
      "v_max3_f32 v115, v115, v180, v181\n"
      "v_max3_f32 v115, v115, v114, v188\n"
      "v_mov_b32 v116, v115\n"
      "s_nop 1\n"
      "v_permlane16_swap_b32 v115, v116\n"
      "v_max_f32 v115, v115, v116\n"
      "v_mov_b32 v116, v115\n"
      "s_nop 1\n"
      "v_permlane32_swap_b32 v115, v116\n"
      "v_max_f32 v115, v115, v116\n"
      "v_sub_f32 v190, v190, v115\n"
      "v_sub_f32 v191, v191, v115\n"
      "v_sub_f32 v192, v192, v115\n"
      "v_sub_f32 v193, v193, v115\n"
      "v_sub_f32 v134, v134, v115\n"
      "v_sub_f32 v135, v135, v115\n"
      "v_sub_f32 v136, v136, v115\n"
      "v_sub_f32 v137, v137, v115\n"
      "v_sub_f32 v178, v178, v115\n"
      "v_sub_f32 v179, v179, v115\n"
      "v_sub_f32 v180, v180, v115\n"
      "v_sub_f32 v181, v181, v115\n"
      "v_sub_f32 v188, v188, v115\n"
      "s_waitcnt lgkmcnt(0)\n"
      "s_nop 4\n"
      "s_branch L_back_8_%=\n"
      "L_rare_9_%=:\n"
      "s_nop 7\n"
      "s_waitcnt lgkmcnt(0)\n"
      "s_waitcnt vmcnt(0)\n"
      "v_subrev_u32 v166, 115456, v9\n"
      "v_lshrrev_b32 v166, 3, v166\n"
      "v_min_u32 v166, 96, v166\n"
      "v_add_u32 v166, s77, v166\n"
      "v_add_u32 v166, 115296, v166\n"
      "ds_read_u16 v166, v166\n"
      "s_waitcnt lgkmcnt(0)\n"
      "v_lshlrev_b32_e64 v166, 16, v166\n"
      "v_fmac_f32 v138, s53, v166\n"
      "s_nop 1\n"
      "v_max_f32_dpp v168, v138, v138 row_ror:8 row_mask:0xf bank_mask:0xf\n"
      "s_nop 1\n"
      "v_max_f32_dpp v168, v168, v168 row_ror:4 row_mask:0xf bank_mask:0xf\n"
      "s_nop 1\n"
      "v_max_f32_dpp v168, v168, v168 row_ror:2 row_mask:0xf bank_mask:0xf\n"
      "s_nop 1\n"
      "v_max_f32_dpp v168, v168, v168 row_ror:1 row_mask:0xf bank_mask:0xf\n"
      "s_nop 1\n"
      "v_max_f32_dpp v168, v168, v168 row_bcast:15 row_mask:0xa bank_mask:0xf\n"
      "s_nop 1\n"
      "v_max_f32_dpp v168, v168, v168 row_bcast:31 row_mask:0xc bank_mask:0xf\n"
      "s_nop 0\n"
      "v_readlane_b32 s72, v168, 63\n"
      "s_nop 1\n"
      "v_subrev_f32 v138, s72, v138\n"
      "s_waitcnt lgkmcnt(0)\n"
      "s_nop 4\n"
      "s_branch L_back_10_%=\n"
      "L_rare_27_%=:\n"
      "s_nop 7\n"
      "s_waitcnt lgkmcnt(0)\n"
      "s_bitcmp1_b32 s85, 31\n"
      "s_cselect_b32 s75, 100270464, 0\n"
      "s_bitcmp1_b32 s85, 30\n"
      "s_cselect_b32 s76, 196224, 0\n"
      "s_and_b32 s74, s85, 0x3fffffff\n"
      "s_cmp_ge_u32 s85, 0x40000000\n"
      "s_cselect_b32 s74, s74, 0\n"
      "s_cselect_b32 s85, 0, s85\n"
      "s_sub_u32 s74, s74, s69\n"
      "s_add_u32 s69, s69, s74\n"
      "s_sub_u32 s75, s75, s70\n"
      "s_add_u32 s70, s70, s75\n"
      "s_sub_u32 s76, s76, s71\n"
      "s_add_u32 s71, s71, s76\n"
      "v_add_u32 v20, s74, v20\n"
      "v_add_u32 v21, s74, v21\n"
      "v_add_u32 v22, s74, v22\n"
      "v_add_u32 v23, s74, v23\n"
      "v_add_u32 v25, s74, v25\n"
      "s_cmp_eq_u32 s75, 0\n"
      "s_cbranch_scc1 L_ax_25_%=\n"
      "v_bfe_i32 v210, v27, 10, 1\n"
      "v_and_b32 v210, s75, v210\n"
      "v_sub_u32 v20, v20, v210\n"
      "v_bfe_i32 v210, v27, 12, 1\n"
      "v_and_b32 v210, s75, v210\n"
      "v_sub_u32 v21, v21, v210\n"
      "v_bfe_i32 v210, v27, 14, 1\n"
      "v_and_b32 v210, s75, v210\n"
      "v_sub_u32 v22, v22, v210\n"
      "v_bfe_i32 v210, v27, 16, 1\n"
      "v_and_b32 v210, s75, v210\n"
      "v_sub_u32 v23, v23, v210\n"
      "v_bfe_i32 v210, v27, 18, 1\n"
      "v_and_b32 v210, s75, v210\n"
      "v_sub_u32 v25, v25, v210\n"
      "L_ax_25_%=:\n"
      "s_cmp_eq_u32 s76, 0\n"
      "s_cbranch_scc1 L_ax_26_%=\n"
      "v_bfe_i32 v210, v27, 11, 1\n"
      "v_and_b32 v210, s76, v210\n"
      "v_sub_u32_e64 v20, v20, v210\n"
      "v_bfe_i32 v210, v27, 13, 1\n"
      "v_and_b32 v210, s76, v210\n"
      "v_sub_u32 v21, v21, v210\n"
      "v_bfe_i32 v210, v27, 15, 1\n"
      "v_and_b32 v210, s76, v210\n"
      "v_sub_u32 v22, v22, v210\n"
      "v_bfe_i32 v210, v27, 17, 1\n"
      "v_and_b32 v210, s76, v210\n"
      "v_sub_u32 v23, v23, v210\n"
      "v_bfe_i32 v210, v27, 19, 1\n"
      "v_and_b32 v210, s76, v210\n"
      "v_sub_u32 v25, v25, v210\n"
      "L_ax_26_%=:\n"
      "s_or_b32 s48, s66, s69\n"
      "s_cmp_lg_u32 s48, 0\n"
      "s_cselect_b32 s48, 0x40000000, 0\n"
      "s_waitcnt lgkmcnt(0)\n"
      "s_nop 4\n"
      "s_branch L_back_28_%=\n"
      "L_rare_31_%=:\n"
      "s_nop 7\n"
      "s_waitcnt lgkmcnt(0)\n"
      "s_bitcmp1_b32 s85, 31\n"
      "s_cselect_b32 s75, 100270464, 0\n"
      "s_bitcmp1_b32 s85, 30\n"
      "s_cselect_b32 s76, 196224, 0\n"
      "s_and_b32 s74, s85, 0x3fffffff\n"
      "s_cmp_ge_u32 s85, 0x40000000\n"
      "s_cselect_b32 s74, s74, 0\n"
      "s_cselect_b32 s85, 0, s85\n"
      "s_sub_u32 s74, s74, s69\n"
      "s_add_u32 s69, s69, s74\n"
      "s_sub_u32 s75, s75, s70\n"
      "s_add_u32 s70, s70, s75\n"
      "s_sub_u32 s76, s76, s71\n"
      "s_add_u32 s71, s71, s76\n"
      "v_add_u32 v20, s74, v20\n"
      "v_add_u32 v21, s74, v21\n"
      "v_add_u32 v22, s74, v22\n"
      "v_add_u32 v23, s74, v23\n"
      "v_add_u32 v25, s74, v25\n"
      "s_cmp_eq_u32 s75, 0\n"
      "s_cbranch_scc1 L_ax_29_%=\n"
      "v_bfe_i32 v214, v27, 10, 1\n"
      "v_and_b32 v214, s75, v214\n"
      "v_sub_u32 v20, v20, v214\n"
      "v_bfe_i32 v214, v27, 12, 1\n"
      "v_and_b32 v214, s75, v214\n"
      "v_sub_u32 v21, v21, v214\n"
      "v_bfe_i32 v214, v27, 14, 1\n"
      "v_and_b32 v214, s75, v214\n"
      "v_sub_u32 v22, v22, v214\n"
      "v_bfe_i32 v214, v27, 16, 1\n"
      "v_and_b32 v214, s75, v214\n"
      "v_sub_u32 v23, v23, v214\n"
      "v_bfe_i32 v214, v27, 18, 1\n"
      "v_and_b32 v214, s75, v214\n"
      "v_sub_u32 v25, v25, v214\n"
      "L_ax_29_%=:\n"
      "s_cmp_eq_u32 s76, 0\n"
      "s_cbranch_scc1 L_ax_30_%=\n"
      "v_bfe_i32 v214, v27, 11, 1\n"
      "v_and_b32 v214, s76, v214\n"
      "v_sub_u32 v20, v20, v214\n"
      "v_bfe_i32 v214, v27, 13, 1\n"
      "v_and_b32 v214, s76, v214\n"
      "v_sub_u32 v21, v21, v214\n"
      "v_bfe_i32 v214, v27, 15, 1\n"
      "v_and_b32 v214, s76, v214\n"
      "v_sub_u32 v22, v22, v214\n"
      "v_bfe_i32 v214, v27, 17, 1\n"
      "v_and_b32 v214, s76, v214\n"
      "v_sub_u32 v23, v23, v214\n"
      "v_bfe_i32 v214, v27, 19, 1\n"
      "v_and_b32 v214, s76, v214\n"
      "v_sub_u32 v25, v25, v214\n"
      "L_ax_30_%=:\n"
      "s_or_b32 s48, s66, s69\n"
      "s_cmp_lg_u32 s48, 0\n"
      "s_cselect_b32 s48, 0x40000000, 0\n"
      "s_waitcnt lgkmcnt(0)\n"
      "s_nop 4\n"
      "s_branch L_back_32_%=\n"
      "L_rare_35_%=:\n"
      "s_nop 7\n"
      "s_waitcnt lgkmcnt(0)\n"
      "s_bitcmp1_b32 s46, 31\n"
      "s_cselect_b32 s75, 100270464, 0\n"
      "s_bitcmp1_b32 s46, 30\n"
      "s_cselect_b32 s76, 196224, 0\n"
      "s_and_b32 s74, s46, 0x3fffffff\n"
      "s_cmp_ge_u32 s46, 0x40000000\n"
      "s_cselect_b32 s74, s74, 0\n"
      "s_cselect_b32 s46, 0, s46\n"
      "s_sub_u32 s74, s74, s69\n"
      "s_add_u32 s69, s69, s74\n"
      "s_sub_u32 s75, s75, s70\n"
      "s_add_u32 s70, s70, s75\n"
      "s_sub_u32 s76, s76, s71\n"
      "s_add_u32 s71, s71, s76\n"
      "v_add_u32 v20, s74, v20\n"
      "v_add_u32 v21, s74, v21\n"
      "v_add_u32 v22, s74, v22\n"
      "v_add_u32 v23, s74, v23\n"
      "v_add_u32 v25, s74, v25\n"
      "s_cmp_eq_u32 s75, 0\n"
      "s_cbranch_scc1 L_ax_33_%=\n"
      "v_bfe_i32 v179, v27, 10, 1\n"
      "v_and_b32 v179, s75, v179\n"
      "v_sub_u32 v20, v20, v179\n"
      "v_bfe_i32 v179, v27, 12, 1\n"
      "v_and_b32 v179, s75, v179\n"
      "v_sub_u32 v21, v21, v179\n"
      "v_bfe_i32 v179, v27, 14, 1\n"
      "v_and_b32 v179, s75, v179\n"
      "v_sub_u32 v22, v22, v179\n"
      "v_bfe_i32 v179, v27, 16, 1\n"
      "v_and_b32 v179, s75, v179\n"
      "v_sub_u32 v23, v23, v179\n"
      "v_bfe_i32 v179, v27, 18, 1\n"
      "v_and_b32 v179, s75, v179\n"
      "v_sub_u32 v25, v25, v179\n"
      "L_ax_33_%=:\n"
      "s_cmp_eq_u32 s76, 0\n"
      "s_cbranch_scc1 L_ax_34_%=\n"
      "v_bfe_i32 v179, v27, 11, 1\n"
      "v_and_b32 v179, s76, v179\n"
      "v_sub_u32 v20, v20, v179\n"
      "v_bfe_i32 v179, v27, 13, 1\n"
      "v_and_b32 v179, s76, v179\n"
      "v_sub_u32 v21, v21, v179\n"
      "v_bfe_i32 v179, v27, 15, 1\n"
      "v_and_b32 v179, s76, v179\n"
      "v_sub_u32 v22, v22, v179\n"
      "v_bfe_i32 v179, v27, 17, 1\n"
      "v_and_b32 v179, s76, v179\n"
      "v_sub_u32 v23, v23, v179\n"
      "v_bfe_i32 v179, v27, 19, 1\n"
      "v_and_b32 v179, s76, v179\n"
      "v_sub_u32 v25, v25, v179\n"
      "L_ax_34_%=:\n"
      "s_or_b32 s48, s66, s69\n"
      "s_cmp_lg_u32 s48, 0\n"
      "s_cselect_b32 s48, 0x40000000, 0\n"
      "s_waitcnt lgkmcnt(0)\n"
      "s_nop 4\n"
      "s_branch L_back_36_%=\n"
      "L_rare_39_%=:\n"
      "s_nop 7\n"
      "s_waitcnt lgkmcnt(0)\n"
      "s_bitcmp1_b32 s46, 31\n"
      "s_cselect_b32 s75, 100270464, 0\n"
      "s_bitcmp1_b32 s46, 30\n"
      "s_cselect_b32 s76, 196224, 0\n"
      "s_and_b32 s74, s46, 0x3fffffff\n"
      "s_cmp_ge_u32 s46, 0x40000000\n"
      "s_cselect_b32 s74, s74, 0\n"
      "s_cselect_b32 s46, 0, s46\n"
      "s_sub_u32 s74, s74, s69\n"
      "s_add_u32 s69, s69, s74\n"
      "s_sub_u32 s75, s75, s70\n"
      "s_add_u32 s70, s70, s75\n"
      "s_sub_u32 s76, s76, s71\n"
      "s_add_u32 s71, s71, s76\n"
      "v_add_u32 v20, s74, v20\n"
      "v_add_u32 v21, s74, v21\n"
      "v_add_u32 v22, s74, v22\n"
      "v_add_u32 v23, s74, v23\n"
      "v_add_u32 v25, s74, v25\n"
      "s_cmp_eq_u32 s75, 0\n"
      "s_cbranch_scc1 L_ax_37_%=\n"
      "v_bfe_i32 v219, v27, 10, 1\n"
      "v_and_b32 v219, s75, v219\n"
      "v_sub_u32 v20, v20, v219\n"
      "v_bfe_i32 v219, v27, 12, 1\n"
      "v_and_b32 v219, s75, v219\n"
      "v_sub_u32 v21, v21, v219\n"
      "v_bfe_i32 v219, v27, 14, 1\n"
      "v_and_b32 v219, s75, v219\n"
      "v_sub_u32 v22, v22, v219\n"
      "v_bfe_i32 v219, v27, 16, 1\n"
      "v_and_b32 v219, s75, v219\n"
      "v_sub_u32 v23, v23, v219\n"
      "v_bfe_i32 v219, v27, 18, 1\n"
      "v_and_b32 v219, s75, v219\n"
      "v_sub_u32 v25, v25, v219\n"
      "L_ax_37_%=:\n"
      "s_cmp_eq_u32 s76, 0\n"
      "s_cbranch_scc1 L_ax_38_%=\n"
      "v_bfe_i32 v219, v27, 11, 1\n"
      "v_and_b32 v219, s76, v219\n"
      "v_sub_u32 v20, v20, v219\n"
      "v_bfe_i32 v219, v27, 13, 1\n"
      "v_and_b32 v219, s76, v219\n"
      "v_sub_u32 v21, v21, v219\n"
      "v_bfe_i32 v219, v27, 15, 1\n"
      "v_and_b32 v219, s76, v219\n"
      "v_sub_u32 v22, v22, v219\n"
      "v_bfe_i32 v219, v27, 17, 1\n"
      "v_and_b32 v219, s76, v219\n"
      "v_sub_u32 v23, v23, v219\n"
      "v_bfe_i32 v219, v27, 19, 1\n"
      "v_and_b32 v219, s76, v219\n"
      "v_sub_u32 v25, v25, v219\n"
      "L_ax_38_%=:\n"
      "s_or_b32 s48, s66, s69\n"
      "s_cmp_lg_u32 s48, 0\n"
      "s_cselect_b32 s48, 0x40000000, 0\n"
      "s_waitcnt lgkmcnt(0)\n"
      "s_nop 4\n"
      "s_branch L_back_40_%=\n"
      "L_cend_%=:\n"
      :
      : "s"(__builtin_amdgcn_kernarg_segment_ptr()), "s"(wave), "s"(blockIdx.x), "s"(CT_TAB), "s"(lds0)
      : "v0","v1","v2","v3","v4","v5","v6","v7","v8","v9","v10","v11","v12","v13","v14","v15","v16","v17","v18","v19","v20","v21","v22","v23","v24","v25","v26","v27","v28","v29","v30","v31","v32","v33","v34","v35","v36","v37","v38","v39","v40","v41","v42","v43","v44","v45","v46","v47","v48","v49","v50","v51","v52","v53","v54","v55","v56","v57","v58","v59","v60","v61","v62","v63","v64","v65","v66","v67","v68","v69","v70","v71","v72","v73","v74","v75","v76","v77","v78","v79","v80","v81","v82","v83","v84","v85","v86","v87","v88","v89","v90","v91","v92","v93","v94","v95","v96","v97","v98","v99","v100","v101","v102","v103","v104","v105","v106","v107","v108","v109","v110","v111","v112","v113","v114","v115","v116","v117","v118","v119","v120","v121","v122","v123","v124","v125","v126","v127","v128","v129","v130","v131","v132","v133","v134","v135","v136","v137","v138","v139","v140","v141","v142","v143","v144","v145","v146","v147","v148","v149","v150","v151","v152","v153","v154","v155","v156","v157","v158","v159","v160","v161","v162","v163","v164","v165","v166","v167","v168","v169","v170","v171","v172","v173","v174","v175","v176","v177","v178","v179","v180","v181","v182","v183","v184","v185","v186","v187","v188","v189","v190","v191","v192","v193","v194","v195","v196","v197","v198","v199","v200","v201","v202","v203","v204","v205","v206","v207","v208","v209","v210","v211","v212","v213","v214","v215","v216","v217","v218","v219","v220","v221","v222","v223","v224","v225","v226","v227","v228","v229","v230","v231","v232","v233","v234","v235","v236","v237","v238","v239","v240","v241","v242","v243","v244","v245","v246","v247","v248","v249","v250","v251","v252","v253","v254","v255","a0","a1","a2","a3","a4","a5","a6","a7","a8","a9","a10","a11","a12","a13","a14","a15","a16","a17","a18","a19","a20","a21","a22","a23","a24","a25","a26","a27","a28","a29","a30","a31","a32","a33","a34","a35","a36","a37","a38","a39","a40","a41","a42","a43","a44","a45","a46","a47","a48","a49","a50","a51","a52","a53","a54","a55","a56","a57","a58","a59","a60","a61","a62","a63","a64","a65","a66","a67","a68","a69","a70","a71","a72","a73","a74","a75","a76","a77","a78","a79","a80","a81","a82","a83","a84","a85","a86","a87","a88","a89","a90","a91","a92","a93","a94","a95","a96","a97","a98","a99","a100","a101","a102","a103","a104","a105","a106","a107","a108","a109","a110","a111","a112","a113","a114","a115","a116","a117","a118","a119","a120","a121","a122","a123","a124","a125","a126","a127","a128","a129","a130","a131","a132","a133","a134","a135","a136","a137","a138","a139","a140","a141","a142","a143","a144","a145","a146","a147","a148","a149","a150","a151","a152","a153","a154","a155","a156","a157","a158","a159","a160","a161","a162","a163","a164","a165","a166","a167","a168","a169","a170","a171","a172","a173","a174","a175","a176","a177","a178","a179","a180","a181","a182","a183","a184","a185","a186","a187","a188","a189","a190","a191","a192","a193","a194","a195","a196","a197","a198","a199","a200","a201","a202","a203","a204","a205","a206","a207","a208","a209","a210","a211","a212","a213","a214","a215","a216","a217","a218","a219","a220","a221","a222","a223","a224","a225","a226","a227","a228","a229","a230","a231","a232","a233","a234","a235","a236","a237","a238","a239","a240","a241","a242","a243","a244","a245","a246","a247","a248","a249","a250","a251","a252","a253","a254","a255","s16","s17","s18","s19","s20","s21","s22","s23","s24","s25","s26","s27","s28","s29","s30","s31","s33","s34","s35","s36","s37","s38","s39","s40","s41","s42","s43","s44","s45","s46","s47","s48","s49","s50","s51","s52","s53","s54","s55","s56","s57","s58","s59","s60","s61","s62","s63","s64","s65","s66","s67","s68","s69","s70","s71","s72","s73","s74","s75","s76","s77","s78","s79","s80","s81","s82","s83","s84","s85","s86","s87","s88","s89","s90","s91","s92","s93","s94","s95","s96","s97","s98","s99","s100","s101", "vcc", "scc", "memory");
}

}

extern "C" void kernel_launch(void* const* d_in, const int* in_sizes, int n_in, void* d_out, int out_size, void* d_ws,
                              size_t ws_size, hipStream_t stream) {
  const uint16_t* x = (const uint16_t*)d_in[0];
  const uint16_t* wqkv = (const uint16_t*)d_in[1];
  const uint16_t* wo = (const uint16_t*)d_in[2];
  const uint16_t* ls = (const uint16_t*)d_in[3];
  const uint16_t* w1 = (const uint16_t*)d_in[4];
  const uint16_t* w2 = (const uint16_t*)d_in[5];
  const uint16_t* rc = (const uint16_t*)d_in[6];
  const int* ridx = (const int*)d_in[7];
  const uint16_t* mask = (const uint16_t*)d_in[8];
  swin::swin_main<<<256, 256, 0, stream>>>(x, mask, (char*)d_ws, (uint16_t*)d_out, wqkv, wo, ls, w1, w2, rc, ridx);
}
```

```cpp
#include <hip/hip_runtime.h>
#include <hip/hip_bf16.h>
#include <stdint.h>

namespace swin {

constexpr unsigned CT_WORDS = 8192u;
constexpr unsigned LDS_BYTES = 163840u;

__device__ __attribute__((aligned(256))) const unsigned CT_TAB[CT_WORDS] = {0x4800u,0x4820u,0x4840u,0x4800u,0x4808u,0x4828u,0x4848u,0x4810u,0x4810u,0x4830u,0x4850u,0x4820u,0x4818u,0x4838u,0x4858u,0x4830u,0x4880u,0x48a0u,0x48c0u,0x4880u,0x4888u,0x48a8u,0x48c8u,0x4890u,0x4890u,0x48b0u,0x48d0u,0x48a0u,0x4898u,0x48b8u,0x48d8u,0x48b0u,0x4920u,0x4900u,0x4960u,0x4920u,0x4928u,0x4908u,0x4968u,0x4930u,0x4930u,0x4910u,0x4970u,0x4900u,0x4938u,0x4918u,0x4978u,0x4910u,0x49a0u,0x4980u,0x49e0u,0x49a0u,0x49a8u,0x4988u,0x49e8u,0x49b0u,0x49b0u,0x4990u,0x49f0u,0x4980u,0x49b8u,0x4998u,0x49f8u,0x4990u,0x4c40u,0x4c60u,0x4c00u,0x4a00u,0x4c48u,0x4c68u,0x4c08u,0x4a10u,0x4c50u,0x4c70u,0x4c10u,0x4a20u,0x4c58u,0x4c78u,0x4c18u,0x4a30u,0x4cc0u,0x4ce0u,0x4c80u,0x4a80u,0x4cc8u,0x4ce8u,0x4c88u,0x4a90u,0x4cd0u,0x4cf0u,0x4c90u,0x4aa0u,0x4cd8u,0x4cf8u,0x4c98u,0x4ab0u,0x4d60u,0x4d40u,0x4d20u,0x4b20u,0x4d68u,0x4d48u,0x4d28u,0x4b30u,0x4d70u,0x4d50u,0x4d30u,0x4b00u,0x4d78u,0x4d58u,0x4d38u,0x4b10u,0x4de0u,0x4dc0u,0x4da0u,0x4ba0u,0x4de8u,0x4dc8u,0x4da8u,0x4bb0u,0x4df0u,0x4dd0u,0x4db0u,0x4b80u,0x4df8u,0x4dd8u,0x4db8u,0x4b90u,0x5000u,0x5020u,0x5040u,0x4c40u,0x5008u,0x5028u,0x5048u,0x4c50u,0x5010u,0x5030u,0x5050u,0x4c60u,0x5018u,0x5038u,0x5058u,0x4c70u,0x5080u,0x50a0u,0x50c0u,0x4cc0u,0x5088u,0x50a8u,0x50c8u,0x4cd0u,0x5090u,0x50b0u,0x50d0u,0x4ce0u,0x5098u,0x50b8u,0x50d8u,0x4cf0u,0x5120u,0x5100u,0x5160u,0x4d60u,0x5128u,0x5108u,0x5168u,0x4d70u,0x5130u,0x5110u,0x5170u,0x4d40u,0x5138u,0x5118u,0x5178u,0x4d50u,0x51a0u,0x5180u,0x51e0u,0x4de0u,0x51a8u,0x5188u,0x51e8u,0x4df0u,0x51b0u,0x5190u,0x51f0u,0x4dc0u,0x51b8u,0x5198u,0x51f8u,0x4dd0u,0x5440u,0x5460u,0x5400u,0x4e40u,0x5448u,0x5468u,0x5408u,0x4e50u,0x5450u,0x5470u,0x5410u,0x4e60u,0x5458u,0x5478u,0x5418u,0x4e70u,0x54c0u,0x54e0u,0x5480u,0x4ec0u,0x54c8u,0x54e8u,0x5488u,0x4ed0u,0x54d0u,0x54f0u,0x5490u,0x4ee0u,0x54d8u,0x54f8u,0x5498u,0x4ef0u,0x5560u,0x5540u,0x5520u,0x4f60u,0x5568u,0x5548u,0x5528u,0x4f70u,0x5570u,0x5550u,0x5530u,0x4f40u,0x5578u,0x5558u,0x5538u,0x4f50u,0x55e0u,0x55c0u,0x55a0u,0x4fe0u,0x55e8u,0x55c8u,0x55a8u,0x4ff0u,0x55f0u,0x55d0u,0x55b0u,0x4fc0u,0x55f8u,0x55d8u,0x55b8u,0x4fd0u,0x4840u,0x1e800u,0x1eb00u,0x0u,0x4848u,0x1ec90u,0x1ef90u,0x10u,0x4850u,0x1f120u,0x1f420u,0x20u,0x4858u,0x1f5b0u,0x1f8b0u,0x30u,0x48c0u,0x1fa40u,0x1fd40u,0x40u,0x48c8u,0x1fed0u,0x201d0u,0x50u,0x48d0u,0x20360u,0x20660u,0x60u,0x48d8u,0x207f0u,0x20af0u,0x70u,0x4960u,0x20c80u,0x20f80u,0x180u,0x4968u,0x21110u,0x21410u,0x190u,0x4970u,0x215a0u,0x218a0u,0x1a0u,0x4978u,0x21a30u,0x21d30u,0x1b0u,0x49e0u,0x21ec0u,0x221c0u,0x1c0u,0x49e8u,0x22350u,0x22650u,0x1d0u,0x49f0u,0x227e0u,0x22ae0u,0x1e0u,0x49f8u,0x22c70u,0x22f70u,0x1f0u,0x4a40u,0x1e830u,0x1eb08u,0x320u,0x4a48u,0x1ecc0u,0x1ef98u,0x330u,0x4a50u,0x1f150u,0x1f428u,0x300u,0x4a58u,0x1f5e0u,0x1f8b8u,0x310u,0x4ac0u,0x1fa70u,0x1fd48u,0x360u,0x4ac8u,0x1ff00u,0x201d8u,0x370u,0x4ad0u,0x20390u,0x20668u,0x340u,0x4ad8u,0x20820u,0x20af8u,0x350u,0x4b60u,0x20cb0u,0x20f88u,0x4a0u,0x4b68u,0x21140u,0x21418u,0x4b0u,0x4b70u,0x215d0u,0x218a8u,0x480u,0x4b78u,0x21a60u,0x21d38u,0x490u,0x4be0u,0x21ef0u,0x221c8u,0x4e0u,0x4be8u,0x22380u,0x22658u,0x4f0u,0x4bf0u,0x22810u,0x22ae8u,0x4c0u,0x4bf8u,0x22ca0u,0x22f78u,0x4d0u,0x4c00u,0x1e860u,0x1eb10u,0x600u,0x4c08u,0x1ecf0u,0x1efa0u,0x610u,0x4c10u,0x1f180u,0x1f430u,0x620u,0x4c18u,0x1f610u,0x1f8c0u,0x630u,0x4c80u,0x1faa0u,0x1fd50u,0x640u,0x4c88u,0x1ff30u,0x201e0u,0x650u,0x4c90u,0x203c0u,0x20670u,0x660u,0x4c98u,0x20850u,0x20b00u,0x670u,0x4d20u,0x20ce0u,0x20f90u,0x780u,0x4d28u,0x21170u,0x21420u,0x790u,0x4d30u,0x21600u,0x218b0u,0x7a0u,0x4d38u,0x21a90u,0x21d40u,0x7b0u,0x4da0u,0x21f20u,0x221d0u,0x7c0u,0x4da8u,0x223b0u,0x22660u,0x7d0u,0x4db0u,0x22840u,0x22af0u,0x7e0u,0x4db8u,0x22cd0u,0x22f80u,0x7f0u,0x4e00u,0x1e890u,0x1eb18u,0x920u,0x4e08u,0x1ed20u,0x1efa8u,0x930u,0x4e10u,0x1f1b0u,0x1f438u,0x900u,0x4e18u,0x1f640u,0x1f8c8u,0x910u,0x4e80u,0x1fad0u,0x1fd58u,0x960u,0x4e88u,0x1ff60u,0x201e8u,0x970u,0x4e90u,0x203f0u,0x20678u,0x940u,0x4e98u,0x20880u,0x20b08u,0x950u,0x4f20u,0x20d10u,0x20f98u,0xaa0u,0x4f28u,0x211a0u,0x21428u,0xab0u,0x4f30u,0x21630u,0x218b8u,0xa80u,0x4f38u,0x21ac0u,0x21d48u,0xa90u,0x4fa0u,0x21f50u,0x221d8u,0xae0u,0x4fa8u,0x223e0u,0x22668u,0xaf0u,0x4fb0u,0x22870u,0x22af8u,0xac0u,0x4fb8u,0x22d00u,0x22f88u,0xad0u,0x0u,0x40u,0x9000u,0x9000u,0x80u,0xc0u,0x9010u,0x9010u,0x110u,0x150u,0x9020u,0x9020u,0x190u,0x1d0u,0x9030u,0x9030u,0x220u,0x260u,0x9040u,0x9040u,0x2a0u,0x2e0u,0x9050u,0x9050u,0x330u,0x370u,0x9060u,0x9060u,0x3b0u,0x3f0u,0x9070u,0x9070u,0x440u,0x400u,0x9080u,0x9080u,0x4c0u,0x480u,0x9090u,0x9090u,0x550u,0x510u,0x90a0u,0x90a0u,0x5d0u,0x590u,0x90b0u,0x90b0u,0x660u,0x620u,0x90c0u,0x90c0u,0x6e0u,0x6a0u,0x90d0u,0x90d0u,0x770u,0x730u,0x90e0u,0x90e0u,0x7f0u,0x7b0u,0x90f0u,0x90f0u,0x10u,0x50u,0x9300u,0x9300u,0x90u,0xd0u,0x9310u,0x9310u,0x100u,0x140u,0x9320u,0x9320u,0x180u,0x1c0u,0x9330u,0x9330u,0x230u,0x270u,0x9340u,0x9340u,0x2b0u,0x2f0u,0x9350u,0x9350u,0x320u,0x360u,0x9360u,0x9360u,0x3a0u,0x3e0u,0x9370u,0x9370u,0x450u,0x410u,0x9380u,0x9380u,0x4d0u,0x490u,0x9390u,0x9390u,0x540u,0x500u,0x93a0u,0x93a0u,0x5c0u,0x580u,0x93b0u,0x93b0u,0x670u,0x630u,0x93c0u,0x93c0u,0x6f0u,0x6b0u,0x93d0u,0x93d0u,0x760u,0x720u,0x93e0u,0x93e0u,0x7e0u,0x7a0u,0x93f0u,0x93f0u,0x20u,0x60u,0x9600u,0x9600u,0xa0u,0xe0u,0x9610u,0x9610u,0x130u,0x170u,0x9620u,0x9620u,0x1b0u,0x1f0u,0x9630u,0x9630u,0x200u,0x240u,0x9640u,0x9640u,0x280u,0x2c0u,0x9650u,0x9650u,0x310u,0x350u,0x9660u,0x9660u,0x390u,0x3d0u,0x9670u,0x9670u,0x460u,0x420u,0x9680u,0x9680u,0x4e0u,0x4a0u,0x9690u,0x9690u,0x570u,0x530u,0x96a0u,0x96a0u,0x5f0u,0x5b0u,0x96b0u,0x96b0u,0x640u,0x600u,0x96c0u,0x96c0u,0x6c0u,0x680u,0x96d0u,0x96d0u,0x750u,0x710u,0x96e0u,0x96e0u,0x7d0u,0x790u,0x96f0u,0x96f0u,0x30u,0x70u,0x9900u,0x9900u,0xb0u,0xf0u,0x9910u,0x9910u,0x120u,0x160u,0x9920u,0x9920u,0x1a0u,0x1e0u,0x9930u,0x9930u,0x210u,0x250u,0x9940u,0x9940u,0x290u,0x2d0u,0x9950u,0x9950u,0x300u,0x340u,0x9960u,0x9960u,0x380u,0x3c0u,0x9970u,0x9970u,0x470u,0x430u,0x9980u,0x9980u,0x4f0u,0x4b0u,0x9990u,0x9990u,0x560u,0x520u,0x99a0u,0x99a0u,0x5e0u,0x5a0u,0x99b0u,0x99b0u,0x650u,0x610u,0x99c0u,0x99c0u,0x6d0u,0x690u,0x99d0u,0x99d0u,0x740u,0x700u,0x99e0u,0x99e0u,0x7c0u,0x780u,0x99f0u,0x99f0u,0x9c00u,0x12000u,0x12040u,0x12000u,0x9c10u,0x12190u,0x121d0u,0x12010u,0x9c20u,0x12320u,0x12360u,0x12020u,0x9c30u,0x124b0u,0x124f0u,0x12030u,0x9c40u,0x12640u,0x12600u,0x12040u,0x9c50u,0x127d0u,0x12790u,0x12050u,0x9c60u,0x12960u,0x12920u,0x12060u,0x9c70u,0x12af0u,0x12ab0u,0x12070u,0x9c80u,0x12c00u,0x12c40u,0x12080u,0x9c90u,0x12d90u,0x12dd0u,0x12090u,0x9ca0u,0x12f20u,0x12f60u,0x120a0u,0x9cb0u,0x130b0u,0x130f0u,0x120b0u,0x9cc0u,0x13240u,0x13200u,0x120c0u,0x9cd0u,0x133d0u,0x13390u,0x120d0u,0x9ce0u,0x13560u,0x13520u,0x120e0u,0x9cf0u,0x136f0u,0x136b0u,0x120f0u,0x9f00u,0x12010u,0x12048u,0x12100u,0x9f10u,0x12180u,0x121d8u,0x12110u,0x9f20u,0x12330u,0x12368u,0x12120u,0x9f30u,0x124a0u,0x124f8u,0x12130u,0x9f40u,0x12650u,0x12608u,0x12140u,0x9f50u,0x127c0u,0x12798u,0x12150u,0x9f60u,0x12970u,0x12928u,0x12160u,0x9f70u,0x12ae0u,0x12ab8u,0x12170u,0x9f80u,0x12c10u,0x12c48u,0x12180u,0x9f90u,0x12d80u,0x12dd8u,0x12190u,0x9fa0u,0x12f30u,0x12f68u,0x121a0u,0x9fb0u,0x130a0u,0x130f8u,0x121b0u,0x9fc0u,0x13250u,0x13208u,0x121c0u,0x9fd0u,0x133c0u,0x13398u,0x121d0u,0x9fe0u,0x13570u,0x13528u,0x121e0u,0x9ff0u,0x136e0u,0x136b8u,0x121f0u,0xa200u,0x12020u,0x12050u,0x12200u,0xa210u,0x121b0u,0x121c0u,0x12210u,0xa220u,0x12300u,0x12370u,0x12220u,0xa230u,0x12490u,0x124e0u,0x12230u,0xa240u,0x12660u,0x12610u,0x12240u,0xa250u,0x127f0u,0x12780u,0x12250u,0xa260u,0x12940u,0x12930u,0x12260u,0xa270u,0x12ad0u,0x12aa0u,0x12270u,0xa280u,0x12c20u,0x12c50u,0x12280u,0xa290u,0x12db0u,0x12dc0u,0x12290u,0xa2a0u,0x12f00u,0x12f70u,0x122a0u,0xa2b0u,0x13090u,0x130e0u,0x122b0u,0xa2c0u,0x13260u,0x13210u,0x122c0u,0xa2d0u,0x133f0u,0x13380u,0x122d0u,0xa2e0u,0x13540u,0x13530u,0x122e0u,0xa2f0u,0x136d0u,0x136a0u,0x122f0u,0xa500u,0x12030u,0x12058u,0x12300u,0xa510u,0x121a0u,0x121c8u,0x12310u,0xa520u,0x12310u,0x12378u,0x12320u,0xa530u,0x12480u,0x124e8u,0x12330u,0xa540u,0x12670u,0x12618u,0x12340u,0xa550u,0x127e0u,0x12788u,0x12350u,0xa560u,0x12950u,0x12938u,0x12360u,0xa570u,0x12ac0u,0x12aa8u,0x12370u,0xa580u,0x12c30u,0x12c58u,0x12380u,0xa590u,0x12da0u,0x12dc8u,0x12390u,0xa5a0u,0x12f10u,0x12f78u,0x123a0u,0xa5b0u,0x13080u,0x130e8u,0x123b0u,0xa5c0u,0x13270u,0x13218u,0x123c0u,0xa5d0u,0x133e0u,0x13388u,0x123d0u,0xa5e0u,0x13550u,0x13538u,0x123e0u,0xa5f0u,0x136c0u,0x136a8u,0x123f0u,0x4800u,0x1c300u,0x1e800u,0x1eb00u,0x4880u,0x1c310u,0x100000u,0x1eb02u,0x4910u,0x1c320u,0x100000u,0x1eb04u,0x4990u,0x1c330u,0x100000u,0x1eb06u,0x4a20u,0x1c340u,0x1e800u,0x1eb08u,0x4aa0u,0x1c350u,0x100000u,0x1eb0au,0x4b30u,0x1c360u,0x100000u,0x1eb0cu,0x4bb0u,0x1c370u,0x100000u,0x1eb0eu,0x4c40u,0x1c380u,0x1e800u,0x1eb10u,0x4cc0u,0x1c390u,0x100000u,0x1eb12u,0x4d50u,0x1c3a0u,0x100000u,0x1eb14u,0x4dd0u,0x1c3b0u,0x100000u,0x1eb16u,0x4e60u,0x1c3c0u,0x1e800u,0x1eb18u,0x4ee0u,0x1c3d0u,0x100000u,0x1eb1au,0x4f70u,0x1c3e0u,0x100000u,0x1eb1cu,0x4ff0u,0x1c3f0u,0x100000u,0x1eb1eu,0x4810u,0x1c400u,0x1e830u,0x1eb00u,0x4890u,0x1c410u,0x100000u,0x1eb02u,0x4900u,0x1c420u,0x100000u,0x1eb04u,0x4980u,0x1c430u,0x100000u,0x1eb06u,0x4a30u,0x1c440u,0x1e830u,0x1eb08u,0x4ab0u,0x1c450u,0x100000u,0x1eb0au,0x4b20u,0x1c460u,0x100000u,0x1eb0cu,0x4ba0u,0x1c470u,0x100000u,0x1eb0eu,0x4c50u,0x1c480u,0x1e830u,0x1eb10u,0x4cd0u,0x1c490u,0x100000u,0x1eb12u,0x4d40u,0x1c4a0u,0x100000u,0x1eb14u,0x4dc0u,0x1c4b0u,0x100000u,0x1eb16u,0x4e70u,0x1c4c0u,0x1e830u,0x1eb18u,0x4ef0u,0x1c4d0u,0x100000u,0x1eb1au,0x4f60u,0x1c4e0u,0x100000u,0x1eb1cu,0x4fe0u,0x1c4f0u,0x100000u,0x1eb1eu,0x4820u,0x1c500u,0x1e860u,0x1eb00u,0x48a0u,0x1c510u,0x100000u,0x1eb02u,0x4930u,0x1c520u,0x100000u,0x1eb04u,0x49b0u,0x1c530u,0x100000u,0x1eb06u,0x4a00u,0x1c540u,0x1e860u,0x1eb08u,0x4a80u,0x1c550u,0x100000u,0x1eb0au,0x4b10u,0x1c560u,0x100000u,0x1eb0cu,0x4b90u,0x1c570u,0x100000u,0x1eb0eu,0x4c60u,0x1c580u,0x1e860u,0x1eb10u,0x4ce0u,0x1c590u,0x100000u,0x1eb12u,0x4d70u,0x1c5a0u,0x100000u,0x1eb14u,0x4df0u,0x1c5b0u,0x100000u,0x1eb16u,0x4e40u,0x1c5c0u,0x1e860u,0x1eb18u,0x4ec0u,0x1c5d0u,0x100000u,0x1eb1au,0x4f50u,0x1c5e0u,0x100000u,0x1eb1cu,0x4fd0u,0x1c5f0u,0x100000u,0x1eb1eu,0x4830u,0x1c600u,0x1e890u,0x1eb00u,0x48b0u,0x1c610u,0x100000u,0x1eb02u,0x4920u,0x1c620u,0x100000u,0x1eb04u,0x49a0u,0x1c630u,0x100000u,0x1eb06u,0x4a10u,0x1c640u,0x1e890u,0x1eb08u,0x4a90u,0x1c650u,0x100000u,0x1eb0au,0x4b00u,0x1c660u,0x100000u,0x1eb0cu,0x4b80u,0x1c670u,0x100000u,0x1eb0eu,0x4c70u,0x1c680u,0x1e890u,0x1eb10u,0x4cf0u,0x1c690u,0x100000u,0x1eb12u,0x4d60u,0x1c6a0u,0x100000u,0x1eb14u,0x4de0u,0x1c6b0u,0x100000u,0x1eb16u,0x4e50u,0x1c6c0u,0x1e890u,0x1eb18u,0x4ed0u,0x1c6d0u,0x100000u,0x1eb1au,0x4f40u,0x1c6e0u,0x100000u,0x1eb1cu,0x4fc0u,0x1c6f0u,0x100000u,0x1eb1eu,0x24d00u,0x1d300u,0x1d300u,0xc0100u,0x24d02u,0x1d302u,0x100000u,0xc0110u,0x24d04u,0x1d304u,0x100000u,0xc0120u,0x24d06u,0x1d306u,0x100000u,0xc0130u,0x24f00u,0x1d310u,0x1d300u,0xc0140u,0x24f02u,0x1d312u,0x100000u,0xc0150u,0x24f04u,0x1d314u,0x100000u,0xc0160u,0x24f06u,0x1d316u,0x100000u,0xc0170u,0x25100u,0x1d320u,0x1d300u,0xc0280u,0x25102u,0x1d322u,0x100000u,0xc0290u,0x25104u,0x1d324u,0x100000u,0xc02a0u,0x25106u,0x1d326u,0x100000u,0xc02b0u,0x25300u,0x1d330u,0x1d300u,0xc02c0u,0x25302u,0x1d332u,0x100000u,0xc02d0u,0x25304u,0x1d334u,0x100000u,0xc02e0u,0x25306u,0x1d336u,0x100000u,0xc02f0u,0x24d08u,0x1d308u,0x1d310u,0xc0410u,0x24d0au,0x1d30au,0x100000u,0xc0400u,0x24d0cu,0x1d30cu,0x100000u,0xc0430u,0x24d0eu,0x1d30eu,0x100000u,0xc0420u,0x24f08u,0x1d318u,0x1d310u,0xc0450u,0x24f0au,0x1d31au,0x100000u,0xc0440u,0x24f0cu,0x1d31cu,0x100000u,0xc0470u,0x24f0eu,0x1d31eu,0x100000u,0xc0460u,0x25108u,0x1d328u,0x1d310u,0xef990u,0x2510au,0x1d32au,0x100000u,0xef980u,0x2510cu,0x1d32cu,0x100000u,0xef9b0u,0x2510eu,0x1d32eu,0x100000u,0xef9a0u,0x25308u,0x1d338u,0x1d310u,0xef9d0u,0x2530au,0x1d33au,0x100000u,0xef9c0u,0x2530cu,0x1d33cu,0x100000u,0xef9f0u,0x2530eu,0x1d33eu,0x100000u,0xef9e0u,0x25500u,0x1d340u,0x1d320u,0xefb20u,0x25502u,0x1d342u,0x100000u,0xefb30u,0x25504u,0x1d344u,0x100000u,0xefb00u,0x25506u,0x1d346u,0x100000u,0xefb10u,0x25700u,0x1d350u,0x1d320u,0xefb60u,0x25702u,0x1d352u,0x100000u,0xefb70u,0x25704u,0x1d354u,0x100000u,0xefb40u,0x25706u,0x1d356u,0x100000u,0xefb50u,0x25900u,0x1d360u,0x1d320u,0xefca0u,0x25902u,0x1d362u,0x100000u,0xefcb0u,0x25904u,0x1d364u,0x100000u,0xefc80u,0x25906u,0x1d366u,0x100000u,0xefc90u,0x25b00u,0x1d370u,0x1d320u,0xefce0u,0x25b02u,0x1d372u,0x100000u,0xefcf0u,0x25b04u,0x1d374u,0x100000u,0xefcc0u,0x25b06u,0x1d376u,0x100000u,0xefcd0u,0x100000u,0x1d348u,0x1d330u,0xefe30u,0x100000u,0x1d34au,0x100000u,0xefe20u,0x100000u,0x1d34cu,0x100000u,0xefe10u,0x100000u,0x1d34eu,0x100000u,0xefe00u,0x100000u,0x1d358u,0x1d330u,0xefe70u,0x100000u,0x1d35au,0x100000u,0xefe60u,0x100000u,0x1d35cu,0x100000u,0xefe50u,0x100000u,0x1d35eu,0x100000u,0xefe40u,0x100000u,0x1d368u,0x1d330u,0xeffb0u,0x100000u,0x1d36au,0x100000u,0xeffa0u,0x100000u,0x1d36cu,0x100000u,0xeff90u,0x100000u,0x1d36eu,0x100000u,0xeff80u,0x100000u,0x1d378u,0x1d330u,0xefff0u,0x100000u,0x1d37au,0x100000u,0xeffe0u,0x100000u,0x1d37cu,0x100000u,0xeffd0u,0x100000u,0x1d37eu,0x100000u,0xeffc0u,0x0u,0x30420u,0x8fc50u,0xc0000u,0x10u,0x30430u,0x8fc40u,0xc0010u,0x20u,0x30400u,0x8fc70u,0xc0020u,0x30u,0x30410u,0x8fc60u,0xc0030u,0x40u,0x30460u,0x8fc10u,0xc0040u,0x50u,0x30470u,0x8fc00u,0xc0050u,0x60u,0x30440u,0x8fc30u,0xc0060u,0x70u,0x30450u,0x8fc20u,0xc0070u,0x80u,0x304b0u,0x8fcd0u,0xc0080u,0x90u,0x304a0u,0x8fcc0u,0xc0090u,0xa0u,0x30490u,0x8fcf0u,0xc00a0u,0xb0u,0x30480u,0x8fce0u,0xc00b0u,0xc0u,0x304f0u,0x8fc90u,0xc00c0u,0xd0u,0x304e0u,0x8fc80u,0xc00d0u,0xe0u,0x304d0u,0x8fcb0u,0xc00e0u,0xf0u,0x304c0u,0x8fca0u,0xc00f0u,0x100u,0x30530u,0x8fd60u,0xc0100u,0x110u,0x30520u,0x8fd70u,0xc0110u,0x120u,0x30510u,0x8fd40u,0xc0120u,0x130u,0x30500u,0x8fd50u,0xc0130u,0x140u,0x30570u,0x8fd20u,0xc0140u,0x150u,0x30560u,0x8fd30u,0xc0150u,0x160u,0x30550u,0x8fd00u,0xc0160u,0x170u,0x30540u,0x8fd10u,0xc0170u,0x190u,0x305b0u,0x8fde0u,0xc0190u,0x180u,0x305a0u,0x8fdf0u,0xc0180u,0x1b0u,0x30590u,0x8fdc0u,0xc01b0u,0x1a0u,0x30580u,0x8fdd0u,0xc01a0u,0x1d0u,0x305f0u,0x8fda0u,0xc01d0u,0x1c0u,0x305e0u,0x8fdb0u,0xc01c0u,0x1f0u,0x305d0u,0x8fd80u,0xc01f0u,0x1e0u,0x305c0u,0x8fd90u,0xc01e0u,0x210u,0x30640u,0x8fe60u,0xc0210u,0x200u,0x30650u,0x8fe70u,0xc0200u,0x230u,0x30660u,0x8fe40u,0xc0230u,0x220u,0x30670u,0x8fe50u,0xc0220u,0x250u,0x30600u,0x8fe20u,0xc0250u,0x240u,0x30610u,0x8fe30u,0xc0240u,0x270u,0x30620u,0x8fe00u,0xc0270u,0x260u,0x30630u,0x8fe10u,0xc0260u,0x290u,0x306c0u,0x8fef0u,0xc0290u,0x280u,0x306d0u,0x8fee0u,0xc0280u,0x2b0u,0x306e0u,0x8fed0u,0xc02b0u,0x2a0u,0x306f0u,0x8fec0u,0xc02a0u,0x2d0u,0x30680u,0x8feb0u,0xc02d0u,0x2c0u,0x30690u,0x8fea0u,0xc02c0u,0x2f0u,0x306a0u,0x8fe90u,0xc02f0u,0x2e0u,0x306b0u,0x8fe80u,0xc02e0u,0x320u,0x30740u,0x8ff70u,0xc0320u,0x330u,0x30750u,0x8ff60u,0xc0330u,0x300u,0x30760u,0x8ff50u,0xc0300u,0x310u,0x30770u,0x8ff40u,0xc0310u,0x360u,0x30700u,0x8ff30u,0xc0360u,0x370u,0x30710u,0x8ff20u,0xc0370u,0x340u,0x30720u,0x8ff10u,0xc0340u,0x350u,0x30730u,0x8ff00u,0xc0350u,0x3a0u,0x307d0u,0x8fff0u,0xc03a0u,0x3b0u,0x307c0u,0x8ffe0u,0xc03b0u,0x380u,0x307f0u,0x8ffd0u,0xc0380u,0x390u,0x307e0u,0x8ffc0u,0xc0390u,0x3e0u,0x30790u,0x8ffb0u,0xc03e0u,0x3f0u,0x30780u,0x8ffa0u,0xc03f0u,0x3c0u,0x307b0u,0x8ff90u,0xc03c0u,0x3d0u,0x307a0u,0x8ff80u,0xc03d0u,0x0u,0x11f820u,0x0u,0x7030cu,0x0u,0x11f830u,0x0u,0x7030cu,0x0u,0x11f800u,0x0u,0x7030cu,0x0u,0x11f810u,0x0u,0x7030cu,0x0u,0x11f860u,0x0u,0x7030cu,0x0u,0x11f870u,0x0u,0x7030cu,0x0u,0x11f840u,0x0u,0x7030cu,0x0u,0x11f850u,0x0u,0x7030cu,0x0u,0x11f8b0u,0x0u,0x7230cu,0x0u,0x11f8a0u,0x0u,0x7230cu,0x0u,0x11f890u,0x0u,0x7230cu,0x0u,0x11f880u,0x0u,0x7230cu,0x0u,0x11f8f0u,0x0u,0x7230cu,0x0u,0x11f8e0u,0x0u,0x7230cu,0x0u,0x11f8d0u,0x0u,0x7230cu,0x0u,0x11f8c0u,0x0u,0x7230cu,0x0u,0x11f930u,0x0u,0x7232cu,0x0u,0x11f920u,0x0u,0x7232cu,0x0u,0x11f910u,0x0u,0x7232cu,0x0u,0x11f900u,0x0u,0x7232cu,0x0u,0x11f970u,0x0u,0x7232cu,0x0u,0x11f960u,0x0u,0x7232cu,0x0u,0x11f950u,0x0u,0x7232cu,0x0u,0x11f940u,0x0u,0x7232cu,0x0u,0x11f9b0u,0x0u,0x72124u,0x0u,0x11f9a0u,0x0u,0x72124u,0x0u,0x11f990u,0x0u,0x72124u,0x0u,0x11f980u,0x0u,0x72124u,0x0u,0x11f9f0u,0x0u,0x72124u,0x0u,0x11f9e0u,0x0u,0x72124u,0x0u,0x11f9d0u,0x0u,0x72124u,0x0u,0x11f9c0u,0x0u,0x72124u,0x0u,0x11fa40u,0x0u,0x721a6u,0x0u,0x11fa50u,0x0u,0x721a6u,0x0u,0x11fa60u,0x0u,0x721a6u,0x0u,0x11fa70u,0x0u,0x721a6u,0x0u,0x11fa00u,0x0u,0x721a6u,0x0u,0x11fa10u,0x0u,0x721a6u,0x0u,0x11fa20u,0x0u,0x721a6u,0x0u,0x11fa30u,0x0u,0x721a6u,0x0u,0x11fac0u,0x0u,0x72186u,0x0u,0x11fad0u,0x0u,0x72186u,0x0u,0x11fae0u,0x0u,0x72186u,0x0u,0x11faf0u,0x0u,0x72186u,0x0u,0x11fa80u,0x0u,0x72186u,0x0u,0x11fa90u,0x0u,0x72186u,0x0u,0x11faa0u,0x0u,0x72186u,0x0u,0x11fab0u,0x0u,0x72186u,0x0u,0x11fb40u,0x0u,0x72186u,0x0u,0x11fb50u,0x0u,0x72186u,0x0u,0x11fb60u,0x0u,0x72186u,0x0u,0x11fb70u,0x0u,0x72186u,0x0u,0x11fb00u,0x0u,0x72186u,0x0u,0x11fb10u,0x0u,0x72186u,0x0u,0x11fb20u,0x0u,0x72186u,0x0u,0x11fb30u,0x0u,0x72186u,0x0u,0x11fbd0u,0x0u,0x7230cu,0x0u,0x11fbc0u,0x0u,0x7230cu,0x0u,0x11fbf0u,0x0u,0x7230cu,0x0u,0x11fbe0u,0x0u,0x7230cu,0x0u,0x11fb90u,0x0u,0x7230cu,0x0u,0x11fb80u,0x0u,0x7230cu,0x0u,0x11fbb0u,0x0u,0x7230cu,0x0u,0x11fba0u,0x0u,0x7230cu,0x5800u,0x5820u,0x4860u,0x4860u,0x5808u,0x5828u,0x4868u,0x4870u,0x5810u,0x5830u,0x4870u,0x5800u,0x5818u,0x5838u,0x4878u,0x5810u,0x5880u,0x58a0u,0x48e0u,0x48e0u,0x5888u,0x58a8u,0x48e8u,0x48f0u,0x5890u,0x58b0u,0x48f0u,0x5880u,0x5898u,0x58b8u,0x48f8u,0x5890u,0x5920u,0x5900u,0x4940u,0x4940u,0x5928u,0x5908u,0x4948u,0x4950u,0x5930u,0x5910u,0x4950u,0x5920u,0x5938u,0x5918u,0x4958u,0x5930u,0x59a0u,0x5980u,0x49c0u,0x49c0u,0x59a8u,0x5988u,0x49c8u,0x49d0u,0x59b0u,0x5990u,0x49d0u,0x59a0u,0x59b8u,0x5998u,0x49d8u,0x59b0u,0x5c40u,0x5c60u,0x4c20u,0x4a60u,0x5c48u,0x5c68u,0x4c28u,0x4a70u,0x5c50u,0x5c70u,0x4c30u,0x5a00u,0x5c58u,0x5c78u,0x4c38u,0x5a10u,0x5cc0u,0x5ce0u,0x4ca0u,0x4ae0u,0x5cc8u,0x5ce8u,0x4ca8u,0x4af0u,0x5cd0u,0x5cf0u,0x4cb0u,0x5a80u,0x5cd8u,0x5cf8u,0x4cb8u,0x5a90u,0x5d60u,0x5d40u,0x4d00u,0x4b40u,0x5d68u,0x5d48u,0x4d08u,0x4b50u,0x5d70u,0x5d50u,0x4d10u,0x5b20u,0x5d78u,0x5d58u,0x4d18u,0x5b30u,0x5de0u,0x5dc0u,0x4d80u,0x4bc0u,0x5de8u,0x5dc8u,0x4d88u,0x4bd0u,0x5df0u,0x5dd0u,0x4d90u,0x5ba0u,0x5df8u,0x5dd8u,0x4d98u,0x5bb0u,0x6000u,0x6020u,0x5060u,0x4c20u,0x6008u,0x6028u,0x5068u,0x4c30u,0x6010u,0x6030u,0x5070u,0x5c40u,0x6018u,0x6038u,0x5078u,0x5c50u,0x6080u,0x60a0u,0x50e0u,0x4ca0u,0x6088u,0x60a8u,0x50e8u,0x4cb0u,0x6090u,0x60b0u,0x50f0u,0x5cc0u,0x6098u,0x60b8u,0x50f8u,0x5cd0u,0x6120u,0x6100u,0x5140u,0x4d00u,0x6128u,0x6108u,0x5148u,0x4d10u,0x6130u,0x6110u,0x5150u,0x5d60u,0x6138u,0x6118u,0x5158u,0x5d70u,0x61a0u,0x6180u,0x51c0u,0x4d80u,0x61a8u,0x6188u,0x51c8u,0x4d90u,0x61b0u,0x6190u,0x51d0u,0x5de0u,0x61b8u,0x6198u,0x51d8u,0x5df0u,0x6440u,0x6460u,0x5420u,0x4e20u,0x6448u,0x6468u,0x5428u,0x4e30u,0x6450u,0x6470u,0x5430u,0x5e40u,0x6458u,0x6478u,0x5438u,0x5e50u,0x64c0u,0x64e0u,0x54a0u,0x4ea0u,0x64c8u,0x64e8u,0x54a8u,0x4eb0u,0x64d0u,0x64f0u,0x54b0u,0x5ec0u,0x64d8u,0x64f8u,0x54b8u,0x5ed0u,0x6560u,0x6540u,0x5500u,0x4f00u,0x6568u,0x6548u,0x5508u,0x4f10u,0x6570u,0x6550u,0x5510u,0x5f60u,0x6578u,0x6558u,0x5518u,0x5f70u,0x65e0u,0x65c0u,0x5580u,0x4f80u,0x65e8u,0x65c8u,0x5588u,0x4f90u,0x65f0u,0x65d0u,0x5590u,0x5fe0u,0x65f8u,0x65d8u,0x5598u,0x5ff0u,0x5820u,0x1e8c0u,0x1eb60u,0xc40u,0x5828u,0x1ed50u,0x1eff0u,0xc50u,0x5830u,0x1f1e0u,0x1f480u,0xc60u,0x5838u,0x1f670u,0x1f910u,0xc70u,0x58a0u,0x1fb00u,0x1fda0u,0xc00u,0x58a8u,0x1ff90u,0x20230u,0xc10u,0x58b0u,0x20420u,0x206c0u,0xc20u,0x58b8u,0x208b0u,0x20b50u,0xc30u,0x5900u,0x20d40u,0x20fe0u,0xdc0u,0x5908u,0x211d0u,0x21470u,0xdd0u,0x5910u,0x21660u,0x21900u,0xde0u,0x5918u,0x21af0u,0x21d90u,0xdf0u,0x5980u,0x21f80u,0x22220u,0xd80u,0x5988u,0x22410u,0x226b0u,0xd90u,0x5990u,0x228a0u,0x22b40u,0xda0u,0x5998u,0x22d30u,0x22fd0u,0xdb0u,0x5a20u,0x1e8f0u,0x1eb68u,0xf60u,0x5a28u,0x1ed80u,0x1eff8u,0xf70u,0x5a30u,0x1f210u,0x1f488u,0xf40u,0x5a38u,0x1f6a0u,0x1f918u,0xf50u,0x5aa0u,0x1fb30u,0x1fda8u,0xf20u,0x5aa8u,0x1ffc0u,0x20238u,0xf30u,0x5ab0u,0x20450u,0x206c8u,0xf00u,0x5ab8u,0x208e0u,0x20b58u,0xf10u,0x5b00u,0x20d70u,0x20fe8u,0x10e0u,0x5b08u,0x21200u,0x21478u,0x10f0u,0x5b10u,0x21690u,0x21908u,0x10c0u,0x5b18u,0x21b20u,0x21d98u,0x10d0u,0x5b80u,0x21fb0u,0x22228u,0x10a0u,0x5b88u,0x22440u,0x226b8u,0x10b0u,0x5b90u,0x228d0u,0x22b48u,0x1080u,0x5b98u,0x22d60u,0x22fd8u,0x1090u,0x5c60u,0x1e920u,0x1eb70u,0x1240u,0x5c68u,0x1edb0u,0x1f000u,0x1250u,0x5c70u,0x1f240u,0x1f490u,0x1260u,0x5c78u,0x1f6d0u,0x1f920u,0x1270u,0x5ce0u,0x1fb60u,0x1fdb0u,0x1200u,0x5ce8u,0x1fff0u,0x20240u,0x1210u,0x5cf0u,0x20480u,0x206d0u,0x1220u,0x5cf8u,0x20910u,0x20b60u,0x1230u,0x5d40u,0x20da0u,0x20ff0u,0x13c0u,0x5d48u,0x21230u,0x21480u,0x13d0u,0x5d50u,0x216c0u,0x21910u,0x13e0u,0x5d58u,0x21b50u,0x21da0u,0x13f0u,0x5dc0u,0x21fe0u,0x22230u,0x1380u,0x5dc8u,0x22470u,0x226c0u,0x1390u,0x5dd0u,0x22900u,0x22b50u,0x13a0u,0x5dd8u,0x22d90u,0x22fe0u,0x13b0u,0x5e60u,0x1e950u,0x1eb78u,0x1560u,0x5e68u,0x1ede0u,0x1f008u,0x1570u,0x5e70u,0x1f270u,0x1f498u,0x1540u,0x5e78u,0x1f700u,0x1f928u,0x1550u,0x5ee0u,0x1fb90u,0x1fdb8u,0x1520u,0x5ee8u,0x20020u,0x20248u,0x1530u,0x5ef0u,0x204b0u,0x206d8u,0x1500u,0x5ef8u,0x20940u,0x20b68u,0x1510u,0x5f40u,0x20dd0u,0x20ff8u,0x16e0u,0x5f48u,0x21260u,0x21488u,0x16f0u,0x5f50u,0x216f0u,0x21918u,0x16c0u,0x5f58u,0x21b80u,0x21da8u,0x16d0u,0x5fc0u,0x22010u,0x22238u,0x16a0u,0x5fc8u,0x224a0u,0x226c8u,0x16b0u,0x5fd0u,0x22930u,0x22b58u,0x1680u,0x5fd8u,0x22dc0u,0x22fe8u,0x1690u,0x0u,0x40u,0x9000u,0xa800u,0x80u,0xc0u,0x9010u,0xa810u,0x110u,0x150u,0x9020u,0xa820u,0x190u,0x1d0u,0x9030u,0xa830u,0x220u,0x260u,0x9040u,0xa840u,0x2a0u,0x2e0u,0x9050u,0xa850u,0x330u,0x370u,0x9060u,0xa860u,0x3b0u,0x3f0u,0x9070u,0xa870u,0x440u,0x400u,0x9080u,0xa880u,0x4c0u,0x480u,0x9090u,0xa890u,0x550u,0x510u,0x90a0u,0xa8a0u,0x5d0u,0x590u,0x90b0u,0xa8b0u,0x660u,0x620u,0x90c0u,0xa8c0u,0x6e0u,0x6a0u,0x90d0u,0xa8d0u,0x770u,0x730u,0x90e0u,0xa8e0u,0x7f0u,0x7b0u,0x90f0u,0xa8f0u,0x10u,0x50u,0x9300u,0xab00u,0x90u,0xd0u,0x9310u,0xab10u,0x100u,0x140u,0x9320u,0xab20u,0x180u,0x1c0u,0x9330u,0xab30u,0x230u,0x270u,0x9340u,0xab40u,0x2b0u,0x2f0u,0x9350u,0xab50u,0x320u,0x360u,0x9360u,0xab60u,0x3a0u,0x3e0u,0x9370u,0xab70u,0x450u,0x410u,0x9380u,0xab80u,0x4d0u,0x490u,0x9390u,0xab90u,0x540u,0x500u,0x93a0u,0xaba0u,0x5c0u,0x580u,0x93b0u,0xabb0u,0x670u,0x630u,0x93c0u,0xabc0u,0x6f0u,0x6b0u,0x93d0u,0xabd0u,0x760u,0x720u,0x93e0u,0xabe0u,0x7e0u,0x7a0u,0x93f0u,0xabf0u,0x20u,0x60u,0x9600u,0xae00u,0xa0u,0xe0u,0x9610u,0xae10u,0x130u,0x170u,0x9620u,0xae20u,0x1b0u,0x1f0u,0x9630u,0xae30u,0x200u,0x240u,0x9640u,0xae40u,0x280u,0x2c0u,0x9650u,0xae50u,0x310u,0x350u,0x9660u,0xae60u,0x390u,0x3d0u,0x9670u,0xae70u,0x460u,0x420u,0x9680u,0xae80u,0x4e0u,0x4a0u,0x9690u,0xae90u,0x570u,0x530u,0x96a0u,0xaea0u,0x5f0u,0x5b0u,0x96b0u,0xaeb0u,0x640u,0x600u,0x96c0u,0xaec0u,0x6c0u,0x680u,0x96d0u,0xaed0u,0x750u,0x710u,0x96e0u,0xaee0u,0x7d0u,0x790u,0x96f0u,0xaef0u,0x30u,0x70u,0x9900u,0xb100u,0xb0u,0xf0u,0x9910u,0xb110u,0x120u,0x160u,0x9920u,0xb120u,0x1a0u,0x1e0u,0x9930u,0xb130u,0x210u,0x250u,0x9940u,0xb140u,0x290u,0x2d0u,0x9950u,0xb150u,0x300u,0x340u,0x9960u,0xb160u,0x380u,0x3c0u,0x9970u,0xb170u,0x470u,0x430u,0x9980u,0xb180u,0x4f0u,0x4b0u,0x9990u,0xb190u,0x560u,0x520u,0x99a0u,0xb1a0u,0x5e0u,0x5a0u,0x99b0u,0xb1b0u,0x650u,0x610u,0x99c0u,0xb1c0u,0x6d0u,0x690u,0x99d0u,0xb1d0u,0x740u,0x700u,0x99e0u,0xb1e0u,0x7c0u,0x780u,0x99f0u,0xb1f0u,0x9c08u,0x12060u,0x120a0u,0x12400u,0x9c18u,0x121f0u,0x12230u,0x12410u,0x9c28u,0x12340u,0x12380u,0x12420u,0x9c38u,0x124d0u,0x12510u,0x12430u,0x9c48u,0x12620u,0x126e0u,0x12440u,0x9c58u,0x127b0u,0x12870u,0x12450u,0x9c68u,0x12900u,0x129c0u,0x12460u,0x9c78u,0x12a90u,0x12b50u,0x12470u,0x9c88u,0x12c60u,0x12ca0u,0x12480u,0x9c98u,0x12df0u,0x12e30u,0x12490u,0x9ca8u,0x12f40u,0x12f80u,0x124a0u,0x9cb8u,0x130d0u,0x13110u,0x124b0u,0x9cc8u,0x13220u,0x132e0u,0x124c0u,0x9cd8u,0x133b0u,0x13470u,0x124d0u,0x9ce8u,0x13500u,0x135c0u,0x124e0u,0x9cf8u,0x13690u,0x13750u,0x124f0u,0x9f08u,0x12070u,0x120a8u,0x12500u,0x9f18u,0x121e0u,0x12238u,0x12510u,0x9f28u,0x12350u,0x12388u,0x12520u,0x9f38u,0x124c0u,0x12518u,0x12530u,0x9f48u,0x12630u,0x126e8u,0x12540u,0x9f58u,0x127a0u,0x12878u,0x12550u,0x9f68u,0x12910u,0x129c8u,0x12560u,0x9f78u,0x12a80u,0x12b58u,0x12570u,0x9f88u,0x12c70u,0x12ca8u,0x12580u,0x9f98u,0x12de0u,0x12e38u,0x12590u,0x9fa8u,0x12f50u,0x12f88u,0x125a0u,0x9fb8u,0x130c0u,0x13118u,0x125b0u,0x9fc8u,0x13230u,0x132e8u,0x125c0u,0x9fd8u,0x133a0u,0x13478u,0x125d0u,0x9fe8u,0x13510u,0x135c8u,0x125e0u,0x9ff8u,0x13680u,0x13758u,0x125f0u,0xa208u,0x12080u,0x120b0u,0x12600u,0xa218u,0x12210u,0x12220u,0x12610u,0xa228u,0x123a0u,0x12390u,0x12620u,0xa238u,0x12530u,0x12500u,0x12630u,0xa248u,0x126c0u,0x126f0u,0x12640u,0xa258u,0x12850u,0x12860u,0x12650u,0xa268u,0x129e0u,0x129d0u,0x12660u,0xa278u,0x12b70u,0x12b40u,0x12670u,0xa288u,0x12c80u,0x12cb0u,0x12680u,0xa298u,0x12e10u,0x12e20u,0x12690u,0xa2a8u,0x12fa0u,0x12f90u,0x126a0u,0xa2b8u,0x13130u,0x13100u,0x126b0u,0xa2c8u,0x132c0u,0x132f0u,0x126c0u,0xa2d8u,0x13450u,0x13460u,0x126d0u,0xa2e8u,0x135e0u,0x135d0u,0x126e0u,0xa2f8u,0x13770u,0x13740u,0x126f0u,0xa508u,0x12090u,0x120b8u,0x12700u,0xa518u,0x12200u,0x12228u,0x12710u,0xa528u,0x123b0u,0x12398u,0x12720u,0xa538u,0x12520u,0x12508u,0x12730u,0xa548u,0x126d0u,0x126f8u,0x12740u,0xa558u,0x12840u,0x12868u,0x12750u,0xa568u,0x129f0u,0x129d8u,0x12760u,0xa578u,0x12b60u,0x12b48u,0x12770u,0xa588u,0x12c90u,0x12cb8u,0x12780u,0xa598u,0x12e00u,0x12e28u,0x12790u,0xa5a8u,0x12fb0u,0x12f98u,0x127a0u,0xa5b8u,0x13120u,0x13108u,0x127b0u,0xa5c8u,0x132d0u,0x132f8u,0x127c0u,0xa5d8u,0x13440u,0x13468u,0x127d0u,0xa5e8u,0x135f0u,0x135d8u,0x127e0u,0xa5f8u,0x13760u,0x13748u,0x127f0u,0x5000u,0x1c300u,0x1e8c0u,0x1eb60u,0x5080u,0x1c310u,0x100000u,0x1eb62u,0x5110u,0x1c320u,0x100000u,0x1eb64u,0x5190u,0x1c330u,0x100000u,0x1eb66u,0x5220u,0x1c340u,0x1e8c0u,0x1eb68u,0x52a0u,0x1c350u,0x100000u,0x1eb6au,0x5330u,0x1c360u,0x100000u,0x1eb6cu,0x53b0u,0x1c370u,0x100000u,0x1eb6eu,0x5440u,0x1c380u,0x1e8c0u,0x1eb70u,0x54c0u,0x1c390u,0x100000u,0x1eb72u,0x5550u,0x1c3a0u,0x100000u,0x1eb74u,0x55d0u,0x1c3b0u,0x100000u,0x1eb76u,0x5660u,0x1c3c0u,0x1e8c0u,0x1eb78u,0x56e0u,0x1c3d0u,0x100000u,0x1eb7au,0x5770u,0x1c3e0u,0x100000u,0x1eb7cu,0x57f0u,0x1c3f0u,0x100000u,0x1eb7eu,0x5010u,0x1c400u,0x1e8f0u,0x1eb60u,0x5090u,0x1c410u,0x100000u,0x1eb62u,0x5100u,0x1c420u,0x100000u,0x1eb64u,0x5180u,0x1c430u,0x100000u,0x1eb66u,0x5230u,0x1c440u,0x1e8f0u,0x1eb68u,0x52b0u,0x1c450u,0x100000u,0x1eb6au,0x5320u,0x1c460u,0x100000u,0x1eb6cu,0x53a0u,0x1c470u,0x100000u,0x1eb6eu,0x5450u,0x1c480u,0x1e8f0u,0x1eb70u,0x54d0u,0x1c490u,0x100000u,0x1eb72u,0x5540u,0x1c4a0u,0x100000u,0x1eb74u,0x55c0u,0x1c4b0u,0x100000u,0x1eb76u,0x5670u,0x1c4c0u,0x1e8f0u,0x1eb78u,0x56f0u,0x1c4d0u,0x100000u,0x1eb7au,0x5760u,0x1c4e0u,0x100000u,0x1eb7cu,0x57e0u,0x1c4f0u,0x100000u,0x1eb7eu,0x5020u,0x1c500u,0x1e920u,0x1eb60u,0x50a0u,0x1c510u,0x100000u,0x1eb62u,0x5130u,0x1c520u,0x100000u,0x1eb64u,0x51b0u,0x1c530u,0x100000u,0x1eb66u,0x5200u,0x1c540u,0x1e920u,0x1eb68u,0x5280u,0x1c550u,0x100000u,0x1eb6au,0x5310u,0x1c560u,0x100000u,0x1eb6cu,0x5390u,0x1c570u,0x100000u,0x1eb6eu,0x5460u,0x1c580u,0x1e920u,0x1eb70u,0x54e0u,0x1c590u,0x100000u,0x1eb72u,0x5570u,0x1c5a0u,0x100000u,0x1eb74u,0x55f0u,0x1c5b0u,0x100000u,0x1eb76u,0x5640u,0x1c5c0u,0x1e920u,0x1eb78u,0x56c0u,0x1c5d0u,0x100000u,0x1eb7au,0x5750u,0x1c5e0u,0x100000u,0x1eb7cu,0x57d0u,0x1c5f0u,0x100000u,0x1eb7eu,0x5030u,0x1c600u,0x1e950u,0x1eb60u,0x50b0u,0x1c610u,0x100000u,0x1eb62u,0x5120u,0x1c620u,0x100000u,0x1eb64u,0x51a0u,0x1c630u,0x100000u,0x1eb66u,0x5210u,0x1c640u,0x1e950u,0x1eb68u,0x5290u,0x1c650u,0x100000u,0x1eb6au,0x5300u,0x1c660u,0x100000u,0x1eb6cu,0x5380u,0x1c670u,0x100000u,0x1eb6eu,0x5470u,0x1c680u,0x1e950u,0x1eb70u,0x54f0u,0x1c690u,0x100000u,0x1eb72u,0x5560u,0x1c6a0u,0x100000u,0x1eb74u,0x55e0u,0x1c6b0u,0x100000u,0x1eb76u,0x5650u,0x1c6c0u,0x1e950u,0x1eb78u,0x56d0u,0x1c6d0u,0x100000u,0x1eb7au,0x5740u,0x1c6e0u,0x100000u,0x1eb7cu,0x57c0u,0x1c6f0u,0x100000u,0x1eb7eu,0x25d00u,0x1d380u,0x1d380u,0xf0140u,0x25d02u,0x1d382u,0x100000u,0xf0150u,0x25d04u,0x1d384u,0x100000u,0xf0160u,0x25d06u,0x1d386u,0x100000u,0xf0170u,0x25f00u,0x1d390u,0x1d380u,0xf0100u,0x25f02u,0x1d392u,0x100000u,0xf0110u,0x25f04u,0x1d394u,0x100000u,0xf0120u,0x25f06u,0x1d396u,0x100000u,0xf0130u,0x26100u,0x1d3a0u,0x1d380u,0xf02c0u,0x26102u,0x1d3a2u,0x100000u,0xf02d0u,0x26104u,0x1d3a4u,0x100000u,0xf02e0u,0x26106u,0x1d3a6u,0x100000u,0xf02f0u,0x26300u,0x1d3b0u,0x1d380u,0xf0280u,0x26302u,0x1d3b2u,0x100000u,0xf0290u,0x26304u,0x1d3b4u,0x100000u,0xf02a0u,0x26306u,0x1d3b6u,0x100000u,0xf02b0u,0x25d08u,0x1d388u,0x1d390u,0x11f850u,0x25d0au,0x1d38au,0x100000u,0x11f840u,0x25d0cu,0x1d38cu,0x100000u,0x11f870u,0x25d0eu,0x1d38eu,0x100000u,0x11f860u,0x25f08u,0x1d398u,0x1d390u,0x11f810u,0x25f0au,0x1d39au,0x100000u,0x11f800u,0x25f0cu,0x1d39cu,0x100000u,0x11f830u,0x25f0eu,0x1d39eu,0x100000u,0x11f820u,0x26108u,0x1d3a8u,0x1d390u,0x11f9d0u,0x2610au,0x1d3aau,0x100000u,0x11f9c0u,0x2610cu,0x1d3acu,0x100000u,0x11f9f0u,0x2610eu,0x1d3aeu,0x100000u,0x11f9e0u,0x26308u,0x1d3b8u,0x1d390u,0x11f990u,0x2630au,0x1d3bau,0x100000u,0x11f980u,0x2630cu,0x1d3bcu,0x100000u,0x11f9b0u,0x2630eu,0x1d3beu,0x100000u,0x11f9a0u,0x25508u,0x1d3c0u,0x1d3a0u,0x11fb60u,0x2550au,0x1d3c2u,0x100000u,0x11fb70u,0x2550cu,0x1d3c4u,0x100000u,0x11fb40u,0x2550eu,0x1d3c6u,0x100000u,0x11fb50u,0x25708u,0x1d3d0u,0x1d3a0u,0x11fb20u,0x2570au,0x1d3d2u,0x100000u,0x11fb30u,0x2570cu,0x1d3d4u,0x100000u,0x11fb00u,0x2570eu,0x1d3d6u,0x100000u,0x11fb10u,0x25908u,0x1d3e0u,0x1d3a0u,0x11fce0u,0x2590au,0x1d3e2u,0x100000u,0x11fcf0u,0x2590cu,0x1d3e4u,0x100000u,0x11fcc0u,0x2590eu,0x1d3e6u,0x100000u,0x11fcd0u,0x25b08u,0x1d3f0u,0x1d3a0u,0x11fca0u,0x25b0au,0x1d3f2u,0x100000u,0x11fcb0u,0x25b0cu,0x1d3f4u,0x100000u,0x11fc80u,0x25b0eu,0x1d3f6u,0x100000u,0x11fc90u,0x100000u,0x1d3c8u,0x1d3b0u,0x11fe70u,0x100000u,0x1d3cau,0x100000u,0x11fe60u,0x100000u,0x1d3ccu,0x100000u,0x11fe50u,0x100000u,0x1d3ceu,0x100000u,0x11fe40u,0x100000u,0x1d3d8u,0x1d3b0u,0x11fe30u,0x100000u,0x1d3dau,0x100000u,0x11fe20u,0x100000u,0x1d3dcu,0x100000u,0x11fe10u,0x100000u,0x1d3deu,0x100000u,0x11fe00u,0x100000u,0x1d3e8u,0x1d3b0u,0x11fff0u,0x100000u,0x1d3eau,0x100000u,0x11ffe0u,0x100000u,0x1d3ecu,0x100000u,0x11ffd0u,0x100000u,0x1d3eeu,0x100000u,0x11ffc0u,0x100000u,0x1d3f8u,0x1d3b0u,0x11ffb0u,0x100000u,0x1d3fau,0x100000u,0x11ffa0u,0x100000u,0x1d3fcu,0x100000u,0x11ff90u,0x100000u,0x1d3feu,0x100000u,0x11ff80u,0x420u,0x30850u,0x90000u,0xc0420u,0x430u,0x30840u,0x90010u,0xc0430u,0x400u,0x30870u,0x90020u,0xc0400u,0x410u,0x30860u,0x90030u,0xc0410u,0x460u,0x30810u,0x90040u,0xc0460u,0x470u,0x30800u,0x90050u,0xc0470u,0x440u,0x30830u,0x90060u,0xc0440u,0x450u,0x30820u,0x90070u,0xc0450u,0x4b0u,0x308d0u,0x90080u,0xef8b0u,0x4a0u,0x308c0u,0x90090u,0xef8a0u,0x490u,0x308f0u,0x900a0u,0xef890u,0x480u,0x308e0u,0x900b0u,0xef880u,0x4f0u,0x30890u,0x900c0u,0xef8f0u,0x4e0u,0x30880u,0x900d0u,0xef8e0u,0x4d0u,0x308b0u,0x900e0u,0xef8d0u,0x4c0u,0x308a0u,0x900f0u,0xef8c0u,0x530u,0x5fd60u,0x90100u,0xef930u,0x520u,0x5fd70u,0x90110u,0xef920u,0x510u,0x5fd40u,0x90120u,0xef910u,0x500u,0x5fd50u,0x90130u,0xef900u,0x570u,0x5fd20u,0x90140u,0xef970u,0x560u,0x5fd30u,0x90150u,0xef960u,0x550u,0x5fd00u,0x90160u,0xef950u,0x540u,0x5fd10u,0x90170u,0xef940u,0x5b0u,0x5fde0u,0x90190u,0xef9b0u,0x5a0u,0x5fdf0u,0x90180u,0xef9a0u,0x590u,0x5fdc0u,0x901b0u,0xef990u,0x580u,0x5fdd0u,0x901a0u,0xef980u,0x5f0u,0x5fda0u,0x901d0u,0xef9f0u,0x5e0u,0x5fdb0u,0x901c0u,0xef9e0u,0x5d0u,0x5fd80u,0x901f0u,0xef9d0u,0x5c0u,0x5fd90u,0x901e0u,0xef9c0u,0x640u,0x5fe60u,0x90210u,0xefa40u,0x650u,0x5fe70u,0x90200u,0xefa50u,0x660u,0x5fe40u,0x90230u,0xefa60u,0x670u,0x5fe50u,0x90220u,0xefa70u,0x600u,0x5fe20u,0x90250u,0xefa00u,0x610u,0x5fe30u,0x90240u,0xefa10u,0x620u,0x5fe00u,0x90270u,0xefa20u,0x630u,0x5fe10u,0x90260u,0xefa30u,0x6c0u,0x5fef0u,0x90290u,0xefac0u,0x6d0u,0x5fee0u,0x90280u,0xefad0u,0x6e0u,0x5fed0u,0x902b0u,0xefae0u,0x6f0u,0x5fec0u,0x902a0u,0xefaf0u,0x680u,0x5feb0u,0x902d0u,0xefa80u,0x690u,0x5fea0u,0x902c0u,0xefa90u,0x6a0u,0x5fe90u,0x902f0u,0xefaa0u,0x6b0u,0x5fe80u,0x902e0u,0xefab0u,0x740u,0x5ff70u,0x90320u,0xefb40u,0x750u,0x5ff60u,0x90330u,0xefb50u,0x760u,0x5ff50u,0x90300u,0xefb60u,0x770u,0x5ff40u,0x90310u,0xefb70u,0x700u,0x5ff30u,0x90360u,0xefb00u,0x710u,0x5ff20u,0x90370u,0xefb10u,0x720u,0x5ff10u,0x90340u,0xefb20u,0x730u,0x5ff00u,0x90350u,0xefb30u,0x7d0u,0x5fff0u,0x903a0u,0xefbd0u,0x7c0u,0x5ffe0u,0x903b0u,0xefbc0u,0x7f0u,0x5ffd0u,0x90380u,0xefbf0u,0x7e0u,0x5ffc0u,0x90390u,0xefbe0u,0x790u,0x5ffb0u,0x903e0u,0xefb90u,0x780u,0x5ffa0u,0x903f0u,0xefb80u,0x7b0u,0x5ff90u,0x903c0u,0xefbb0u,0x7a0u,0x5ff80u,0x903d0u,0xefba0u,0x0u,0x11fc50u,0x0u,0x7230cu,0x0u,0x11fc40u,0x0u,0x7230cu,0x0u,0x11fc70u,0x0u,0x7230cu,0x0u,0x11fc60u,0x0u,0x7230cu,0x0u,0x11fc10u,0x0u,0x7230cu,0x0u,0x11fc00u,0x0u,0x7230cu,0x0u,0x11fc30u,0x0u,0x7230cu,0x0u,0x11fc20u,0x0u,0x7230cu,0x0u,0x11fcd0u,0x0u,0x5232cu,0x0u,0x11fcc0u,0x0u,0x5232cu,0x0u,0x11fcf0u,0x0u,0x5232cu,0x0u,0x11fce0u,0x0u,0x5232cu,0x0u,0x11fc90u,0x0u,0x5232cu,0x0u,0x11fc80u,0x0u,0x5232cu,0x0u,0x11fcb0u,0x0u,0x5232cu,0x0u,0x11fca0u,0x0u,0x5232cu,0x0u,0x11fd60u,0x0u,0xd0124u,0x0u,0x11fd70u,0x0u,0xd0124u,0x0u,0x11fd40u,0x0u,0xd0124u,0x0u,0x11fd50u,0x0u,0xd0124u,0x0u,0x11fd20u,0x0u,0xd0124u,0x0u,0x11fd30u,0x0u,0xd0124u,0x0u,0x11fd00u,0x0u,0xd0124u,0x0u,0x11fd10u,0x0u,0xd0124u,0x0u,0x11fde0u,0x0u,0xd81a6u,0x0u,0x11fdf0u,0x0u,0xd81a6u,0x0u,0x11fdc0u,0x0u,0xd81a6u,0x0u,0x11fdd0u,0x0u,0xd81a6u,0x0u,0x11fda0u,0x0u,0xd81a6u,0x0u,0x11fdb0u,0x0u,0xd81a6u,0x0u,0x11fd80u,0x0u,0xd81a6u,0x0u,0x11fd90u,0x0u,0xd81a6u,0x0u,0x11fe60u,0x0u,0xd8996u,0x0u,0x11fe70u,0x0u,0xd8996u,0x0u,0x11fe40u,0x0u,0xd8996u,0x0u,0x11fe50u,0x0u,0xd8996u,0x0u,0x11fe20u,0x0u,0xd8996u,0x0u,0x11fe30u,0x0u,0xd8996u,0x0u,0x11fe00u,0x0u,0xd8996u,0x0u,0x11fe10u,0x0u,0xd8996u,0x0u,0x11fef0u,0x0u,0xd8996u,0x0u,0x11fee0u,0x0u,0xd8996u,0x0u,0x11fed0u,0x0u,0xd8996u,0x0u,0x11fec0u,0x0u,0xd8996u,0x0u,0x11feb0u,0x0u,0xd8996u,0x0u,0x11fea0u,0x0u,0xd8996u,0x0u,0x11fe90u,0x0u,0xd8996u,0x0u,0x11fe80u,0x0u,0xd8996u,0x0u,0x11ff70u,0x0u,0xd8b1cu,0x0u,0x11ff60u,0x0u,0xd8b1cu,0x0u,0x11ff50u,0x0u,0xd8b1cu,0x0u,0x11ff40u,0x0u,0xd8b1cu,0x0u,0x11ff30u,0x0u,0xd8b1cu,0x0u,0x11ff20u,0x0u,0xd8b1cu,0x0u,0x11ff10u,0x0u,0xd8b1cu,0x0u,0x11ff00u,0x0u,0xd8b1cu,0x0u,0x11fff0u,0x0u,0xd8b1cu,0x0u,0x11ffe0u,0x0u,0xd8b1cu,0x0u,0x11ffd0u,0x0u,0xd8b1cu,0x0u,0x11ffc0u,0x0u,0xd8b1cu,0x0u,0x11ffb0u,0x0u,0xd8b1cu,0x0u,0x11ffa0u,0x0u,0xd8b1cu,0x0u,0x11ff90u,0x0u,0xd8b1cu,0x0u,0x11ff80u,0x0u,0xd8b1cu,0x5840u,0x5860u,0x6800u,0x5840u,0x5848u,0x5868u,0x6808u,0x5850u,0x5850u,0x5870u,0x6810u,0x5860u,0x5858u,0x5878u,0x6818u,0x5870u,0x58c0u,0x58e0u,0x6880u,0x58c0u,0x58c8u,0x58e8u,0x6888u,0x58d0u,0x58d0u,0x58f0u,0x6890u,0x58e0u,0x58d8u,0x58f8u,0x6898u,0x58f0u,0x5960u,0x5940u,0x6920u,0x5960u,0x5968u,0x5948u,0x6928u,0x5970u,0x5970u,0x5950u,0x6930u,0x5940u,0x5978u,0x5958u,0x6938u,0x5950u,0x59e0u,0x59c0u,0x69a0u,0x59e0u,0x59e8u,0x59c8u,0x69a8u,0x59f0u,0x59f0u,0x59d0u,0x69b0u,0x59c0u,0x59f8u,0x59d8u,0x69b8u,0x59d0u,0x5c00u,0x5c20u,0x6c40u,0x5a40u,0x5c08u,0x5c28u,0x6c48u,0x5a50u,0x5c10u,0x5c30u,0x6c50u,0x5a60u,0x5c18u,0x5c38u,0x6c58u,0x5a70u,0x5c80u,0x5ca0u,0x6cc0u,0x5ac0u,0x5c88u,0x5ca8u,0x6cc8u,0x5ad0u,0x5c90u,0x5cb0u,0x6cd0u,0x5ae0u,0x5c98u,0x5cb8u,0x6cd8u,0x5af0u,0x5d20u,0x5d00u,0x6d60u,0x5b60u,0x5d28u,0x5d08u,0x6d68u,0x5b70u,0x5d30u,0x5d10u,0x6d70u,0x5b40u,0x5d38u,0x5d18u,0x6d78u,0x5b50u,0x5da0u,0x5d80u,0x6de0u,0x5be0u,0x5da8u,0x5d88u,0x6de8u,0x5bf0u,0x5db0u,0x5d90u,0x6df0u,0x5bc0u,0x5db8u,0x5d98u,0x6df8u,0x5bd0u,0x6040u,0x6060u,0x7000u,0x5c00u,0x6048u,0x6068u,0x7008u,0x5c10u,0x6050u,0x6070u,0x7010u,0x5c20u,0x6058u,0x6078u,0x7018u,0x5c30u,0x60c0u,0x60e0u,0x7080u,0x5c80u,0x60c8u,0x60e8u,0x7088u,0x5c90u,0x60d0u,0x60f0u,0x7090u,0x5ca0u,0x60d8u,0x60f8u,0x7098u,0x5cb0u,0x6160u,0x6140u,0x7120u,0x5d20u,0x6168u,0x6148u,0x7128u,0x5d30u,0x6170u,0x6150u,0x7130u,0x5d00u,0x6178u,0x6158u,0x7138u,0x5d10u,0x61e0u,0x61c0u,0x71a0u,0x5da0u,0x61e8u,0x61c8u,0x71a8u,0x5db0u,0x61f0u,0x61d0u,0x71b0u,0x5d80u,0x61f8u,0x61d8u,0x71b8u,0x5d90u,0x6400u,0x6420u,0x7440u,0x5e00u,0x6408u,0x6428u,0x7448u,0x5e10u,0x6410u,0x6430u,0x7450u,0x5e20u,0x6418u,0x6438u,0x7458u,0x5e30u,0x6480u,0x64a0u,0x74c0u,0x5e80u,0x6488u,0x64a8u,0x74c8u,0x5e90u,0x6490u,0x64b0u,0x74d0u,0x5ea0u,0x6498u,0x64b8u,0x74d8u,0x5eb0u,0x6520u,0x6500u,0x7560u,0x5f20u,0x6528u,0x6508u,0x7568u,0x5f30u,0x6530u,0x6510u,0x7570u,0x5f00u,0x6538u,0x6518u,0x7578u,0x5f10u,0x65a0u,0x6580u,0x75e0u,0x5fa0u,0x65a8u,0x6588u,0x75e8u,0x5fb0u,0x65b0u,0x6590u,0x75f0u,0x5f80u,0x65b8u,0x6598u,0x75f8u,0x5f90u,0x6800u,0x1e980u,0x1ebc0u,0x1800u,0x6808u,0x1ee10u,0x1f050u,0x1810u,0x6810u,0x1f2a0u,0x1f4e0u,0x1820u,0x6818u,0x1f730u,0x1f970u,0x1830u,0x6880u,0x1fbc0u,0x1fe00u,0x1840u,0x6888u,0x20050u,0x20290u,0x1850u,0x6890u,0x204e0u,0x20720u,0x1860u,0x6898u,0x20970u,0x20bb0u,0x1870u,0x6920u,0x20e00u,0x21040u,0x1980u,0x6928u,0x21290u,0x214d0u,0x1990u,0x6930u,0x21720u,0x21960u,0x19a0u,0x6938u,0x21bb0u,0x21df0u,0x19b0u,0x69a0u,0x22040u,0x22280u,0x19c0u,0x69a8u,0x224d0u,0x22710u,0x19d0u,0x69b0u,0x22960u,0x22ba0u,0x19e0u,0x69b8u,0x22df0u,0x23030u,0x19f0u,0x6a00u,0x1e9b0u,0x1ebc8u,0x1b20u,0x6a08u,0x1ee40u,0x1f058u,0x1b30u,0x6a10u,0x1f2d0u,0x1f4e8u,0x1b00u,0x6a18u,0x1f760u,0x1f978u,0x1b10u,0x6a80u,0x1fbf0u,0x1fe08u,0x1b60u,0x6a88u,0x20080u,0x20298u,0x1b70u,0x6a90u,0x20510u,0x20728u,0x1b40u,0x6a98u,0x209a0u,0x20bb8u,0x1b50u,0x6b20u,0x20e30u,0x21048u,0x1ca0u,0x6b28u,0x212c0u,0x214d8u,0x1cb0u,0x6b30u,0x21750u,0x21968u,0x1c80u,0x6b38u,0x21be0u,0x21df8u,0x1c90u,0x6ba0u,0x22070u,0x22288u,0x1ce0u,0x6ba8u,0x22500u,0x22718u,0x1cf0u,0x6bb0u,0x22990u,0x22ba8u,0x1cc0u,0x6bb8u,0x22e20u,0x23038u,0x1cd0u,0x6c40u,0x1e9e0u,0x1ebd0u,0x1e00u,0x6c48u,0x1ee70u,0x1f060u,0x1e10u,0x6c50u,0x1f300u,0x1f4f0u,0x1e20u,0x6c58u,0x1f790u,0x1f980u,0x1e30u,0x6cc0u,0x1fc20u,0x1fe10u,0x1e40u,0x6cc8u,0x200b0u,0x202a0u,0x1e50u,0x6cd0u,0x20540u,0x20730u,0x1e60u,0x6cd8u,0x209d0u,0x20bc0u,0x1e70u,0x6d60u,0x20e60u,0x21050u,0x1f80u,0x6d68u,0x212f0u,0x214e0u,0x1f90u,0x6d70u,0x21780u,0x21970u,0x1fa0u,0x6d78u,0x21c10u,0x21e00u,0x1fb0u,0x6de0u,0x220a0u,0x22290u,0x1fc0u,0x6de8u,0x22530u,0x22720u,0x1fd0u,0x6df0u,0x229c0u,0x22bb0u,0x1fe0u,0x6df8u,0x22e50u,0x23040u,0x1ff0u,0x6e40u,0x1ea10u,0x1ebd8u,0x2120u,0x6e48u,0x1eea0u,0x1f068u,0x2130u,0x6e50u,0x1f330u,0x1f4f8u,0x2100u,0x6e58u,0x1f7c0u,0x1f988u,0x2110u,0x6ec0u,0x1fc50u,0x1fe18u,0x2160u,0x6ec8u,0x200e0u,0x202a8u,0x2170u,0x6ed0u,0x20570u,0x20738u,0x2140u,0x6ed8u,0x20a00u,0x20bc8u,0x2150u,0x6f60u,0x20e90u,0x21058u,0x22a0u,0x6f68u,0x21320u,0x214e8u,0x22b0u,0x6f70u,0x217b0u,0x21978u,0x2280u,0x6f78u,0x21c40u,0x21e08u,0x2290u,0x6fe0u,0x220d0u,0x22298u,0x22e0u,0x6fe8u,0x22560u,0x22728u,0x22f0u,0x6ff0u,0x229f0u,0x22bb8u,0x22c0u,0x6ff8u,0x22e80u,0x23048u,0x22d0u,0x0u,0x40u,0x9000u,0xb400u,0x80u,0xc0u,0x9010u,0xb410u,0x110u,0x150u,0x9020u,0xb420u,0x190u,0x1d0u,0x9030u,0xb430u,0x220u,0x260u,0x9040u,0xb440u,0x2a0u,0x2e0u,0x9050u,0xb450u,0x330u,0x370u,0x9060u,0xb460u,0x3b0u,0x3f0u,0x9070u,0xb470u,0x440u,0x400u,0x9080u,0xb480u,0x4c0u,0x480u,0x9090u,0xb490u,0x550u,0x510u,0x90a0u,0xb4a0u,0x5d0u,0x590u,0x90b0u,0xb4b0u,0x660u,0x620u,0x90c0u,0xb4c0u,0x6e0u,0x6a0u,0x90d0u,0xb4d0u,0x770u,0x730u,0x90e0u,0xb4e0u,0x7f0u,0x7b0u,0x90f0u,0xb4f0u,0x10u,0x50u,0x9300u,0xb700u,0x90u,0xd0u,0x9310u,0xb710u,0x100u,0x140u,0x9320u,0xb720u,0x180u,0x1c0u,0x9330u,0xb730u,0x230u,0x270u,0x9340u,0xb740u,0x2b0u,0x2f0u,0x9350u,0xb750u,0x320u,0x360u,0x9360u,0xb760u,0x3a0u,0x3e0u,0x9370u,0xb770u,0x450u,0x410u,0x9380u,0xb780u,0x4d0u,0x490u,0x9390u,0xb790u,0x540u,0x500u,0x93a0u,0xb7a0u,0x5c0u,0x580u,0x93b0u,0xb7b0u,0x670u,0x630u,0x93c0u,0xb7c0u,0x6f0u,0x6b0u,0x93d0u,0xb7d0u,0x760u,0x720u,0x93e0u,0xb7e0u,0x7e0u,0x7a0u,0x93f0u,0xb7f0u,0x20u,0x60u,0x9600u,0xba00u,0xa0u,0xe0u,0x9610u,0xba10u,0x130u,0x170u,0x9620u,0xba20u,0x1b0u,0x1f0u,0x9630u,0xba30u,0x200u,0x240u,0x9640u,0xba40u,0x280u,0x2c0u,0x9650u,0xba50u,0x310u,0x350u,0x9660u,0xba60u,0x390u,0x3d0u,0x9670u,0xba70u,0x460u,0x420u,0x9680u,0xba80u,0x4e0u,0x4a0u,0x9690u,0xba90u,0x570u,0x530u,0x96a0u,0xbaa0u,0x5f0u,0x5b0u,0x96b0u,0xbab0u,0x640u,0x600u,0x96c0u,0xbac0u,0x6c0u,0x680u,0x96d0u,0xbad0u,0x750u,0x710u,0x96e0u,0xbae0u,0x7d0u,0x790u,0x96f0u,0xbaf0u,0x30u,0x70u,0x9900u,0xbd00u,0xb0u,0xf0u,0x9910u,0xbd10u,0x120u,0x160u,0x9920u,0xbd20u,0x1a0u,0x1e0u,0x9930u,0xbd30u,0x210u,0x250u,0x9940u,0xbd40u,0x290u,0x2d0u,0x9950u,0xbd50u,0x300u,0x340u,0x9960u,0xbd60u,0x380u,0x3c0u,0x9970u,0xbd70u,0x470u,0x430u,0x9980u,0xbd80u,0x4f0u,0x4b0u,0x9990u,0xbd90u,0x560u,0x520u,0x99a0u,0xbda0u,0x5e0u,0x5a0u,0x99b0u,0xbdb0u,0x650u,0x610u,0x99c0u,0xbdc0u,0x6d0u,0x690u,0x99d0u,0xbdd0u,0x740u,0x700u,0x99e0u,0xbde0u,0x7c0u,0x780u,0x99f0u,0xbdf0u,0xc000u,0x120c0u,0x12100u,0x12800u,0xc010u,0x12250u,0x12290u,0x12810u,0xc020u,0x123e0u,0x12420u,0x12820u,0xc030u,0x12570u,0x125b0u,0x12830u,0xc040u,0x12680u,0x12740u,0x12840u,0xc050u,0x12810u,0x128d0u,0x12850u,0xc060u,0x129a0u,0x12a60u,0x12860u,0xc070u,0x12b30u,0x12bf0u,0x12870u,0xc080u,0x12cc0u,0x12d00u,0x12880u,0xc090u,0x12e50u,0x12e90u,0x12890u,0xc0a0u,0x12fe0u,0x13020u,0x128a0u,0xc0b0u,0x13170u,0x131b0u,0x128b0u,0xc0c0u,0x13280u,0x13340u,0x128c0u,0xc0d0u,0x13410u,0x134d0u,0x128d0u,0xc0e0u,0x135a0u,0x13660u,0x128e0u,0xc0f0u,0x13730u,0x137f0u,0x128f0u,0xc300u,0x120d0u,0x12108u,0x12900u,0xc310u,0x12240u,0x12298u,0x12910u,0xc320u,0x123f0u,0x12428u,0x12920u,0xc330u,0x12560u,0x125b8u,0x12930u,0xc340u,0x12690u,0x12748u,0x12940u,0xc350u,0x12800u,0x128d8u,0x12950u,0xc360u,0x129b0u,0x12a68u,0x12960u,0xc370u,0x12b20u,0x12bf8u,0x12970u,0xc380u,0x12cd0u,0x12d08u,0x12980u,0xc390u,0x12e40u,0x12e98u,0x12990u,0xc3a0u,0x12ff0u,0x13028u,0x129a0u,0xc3b0u,0x13160u,0x131b8u,0x129b0u,0xc3c0u,0x13290u,0x13348u,0x129c0u,0xc3d0u,0x13400u,0x134d8u,0x129d0u,0xc3e0u,0x135b0u,0x13668u,0x129e0u,0xc3f0u,0x13720u,0x137f8u,0x129f0u,0xc600u,0x120e0u,0x12110u,0x12a00u,0xc610u,0x12270u,0x12280u,0x12a10u,0xc620u,0x123c0u,0x12430u,0x12a20u,0xc630u,0x12550u,0x125a0u,0x12a30u,0xc640u,0x126a0u,0x12750u,0x12a40u,0xc650u,0x12830u,0x128c0u,0x12a50u,0xc660u,0x12980u,0x12a70u,0x12a60u,0xc670u,0x12b10u,0x12be0u,0x12a70u,0xc680u,0x12ce0u,0x12d10u,0x12a80u,0xc690u,0x12e70u,0x12e80u,0x12a90u,0xc6a0u,0x12fc0u,0x13030u,0x12aa0u,0xc6b0u,0x13150u,0x131a0u,0x12ab0u,0xc6c0u,0x132a0u,0x13350u,0x12ac0u,0xc6d0u,0x13430u,0x134c0u,0x12ad0u,0xc6e0u,0x13580u,0x13670u,0x12ae0u,0xc6f0u,0x13710u,0x137e0u,0x12af0u,0xc900u,0x120f0u,0x12118u,0x12b00u,0xc910u,0x12260u,0x12288u,0x12b10u,0xc920u,0x123d0u,0x12438u,0x12b20u,0xc930u,0x12540u,0x125a8u,0x12b30u,0xc940u,0x126b0u,0x12758u,0x12b40u,0xc950u,0x12820u,0x128c8u,0x12b50u,0xc960u,0x12990u,0x12a78u,0x12b60u,0xc970u,0x12b00u,0x12be8u,0x12b70u,0xc980u,0x12cf0u,0x12d18u,0x12b80u,0xc990u,0x12e60u,0x12e88u,0x12b90u,0xc9a0u,0x12fd0u,0x13038u,0x12ba0u,0xc9b0u,0x13140u,0x131a8u,0x12bb0u,0xc9c0u,0x132b0u,0x13358u,0x12bc0u,0xc9d0u,0x13420u,0x134c8u,0x12bd0u,0xc9e0u,0x13590u,0x13678u,0x12be0u,0xc9f0u,0x13700u,0x137e8u,0x12bf0u,0x7800u,0x1c300u,0x1e980u,0x1ebc0u,0x7880u,0x1c310u,0x100000u,0x1ebc2u,0x7910u,0x1c320u,0x100000u,0x1ebc4u,0x7990u,0x1c330u,0x100000u,0x1ebc6u,0x7a20u,0x1c340u,0x1e980u,0x1ebc8u,0x7aa0u,0x1c350u,0x100000u,0x1ebcau,0x7b30u,0x1c360u,0x100000u,0x1ebccu,0x7bb0u,0x1c370u,0x100000u,0x1ebceu,0x7c40u,0x1c380u,0x1e980u,0x1ebd0u,0x7cc0u,0x1c390u,0x100000u,0x1ebd2u,0x7d50u,0x1c3a0u,0x100000u,0x1ebd4u,0x7dd0u,0x1c3b0u,0x100000u,0x1ebd6u,0x7e60u,0x1c3c0u,0x1e980u,0x1ebd8u,0x7ee0u,0x1c3d0u,0x100000u,0x1ebdau,0x7f70u,0x1c3e0u,0x100000u,0x1ebdcu,0x7ff0u,0x1c3f0u,0x100000u,0x1ebdeu,0x7810u,0x1c400u,0x1e9b0u,0x1ebc0u,0x7890u,0x1c410u,0x100000u,0x1ebc2u,0x7900u,0x1c420u,0x100000u,0x1ebc4u,0x7980u,0x1c430u,0x100000u,0x1ebc6u,0x7a30u,0x1c440u,0x1e9b0u,0x1ebc8u,0x7ab0u,0x1c450u,0x100000u,0x1ebcau,0x7b20u,0x1c460u,0x100000u,0x1ebccu,0x7ba0u,0x1c470u,0x100000u,0x1ebceu,0x7c50u,0x1c480u,0x1e9b0u,0x1ebd0u,0x7cd0u,0x1c490u,0x100000u,0x1ebd2u,0x7d40u,0x1c4a0u,0x100000u,0x1ebd4u,0x7dc0u,0x1c4b0u,0x100000u,0x1ebd6u,0x7e70u,0x1c4c0u,0x1e9b0u,0x1ebd8u,0x7ef0u,0x1c4d0u,0x100000u,0x1ebdau,0x7f60u,0x1c4e0u,0x100000u,0x1ebdcu,0x7fe0u,0x1c4f0u,0x100000u,0x1ebdeu,0x7820u,0x1c500u,0x1e9e0u,0x1ebc0u,0x78a0u,0x1c510u,0x100000u,0x1ebc2u,0x7930u,0x1c520u,0x100000u,0x1ebc4u,0x79b0u,0x1c530u,0x100000u,0x1ebc6u,0x7a00u,0x1c540u,0x1e9e0u,0x1ebc8u,0x7a80u,0x1c550u,0x100000u,0x1ebcau,0x7b10u,0x1c560u,0x100000u,0x1ebccu,0x7b90u,0x1c570u,0x100000u,0x1ebceu,0x7c60u,0x1c580u,0x1e9e0u,0x1ebd0u,0x7ce0u,0x1c590u,0x100000u,0x1ebd2u,0x7d70u,0x1c5a0u,0x100000u,0x1ebd4u,0x7df0u,0x1c5b0u,0x100000u,0x1ebd6u,0x7e40u,0x1c5c0u,0x1e9e0u,0x1ebd8u,0x7ec0u,0x1c5d0u,0x100000u,0x1ebdau,0x7f50u,0x1c5e0u,0x100000u,0x1ebdcu,0x7fd0u,0x1c5f0u,0x100000u,0x1ebdeu,0x7830u,0x1c600u,0x1ea10u,0x1ebc0u,0x78b0u,0x1c610u,0x100000u,0x1ebc2u,0x7920u,0x1c620u,0x100000u,0x1ebc4u,0x79a0u,0x1c630u,0x100000u,0x1ebc6u,0x7a10u,0x1c640u,0x1ea10u,0x1ebc8u,0x7a90u,0x1c650u,0x100000u,0x1ebcau,0x7b00u,0x1c660u,0x100000u,0x1ebccu,0x7b80u,0x1c670u,0x100000u,0x1ebceu,0x7c70u,0x1c680u,0x1ea10u,0x1ebd0u,0x7cf0u,0x1c690u,0x100000u,0x1ebd2u,0x7d60u,0x1c6a0u,0x100000u,0x1ebd4u,0x7de0u,0x1c6b0u,0x100000u,0x1ebd6u,0x7e50u,0x1c6c0u,0x1ea10u,0x1ebd8u,0x7ed0u,0x1c6d0u,0x100000u,0x1ebdau,0x7f40u,0x1c6e0u,0x100000u,0x1ebdcu,0x7fc0u,0x1c6f0u,0x100000u,0x1ebdeu,0x26500u,0x1d400u,0x1d400u,0x80000000u,0x26502u,0x1d402u,0x100000u,0x80000000u,0x26504u,0x1d404u,0x100000u,0x80000000u,0x26506u,0x1d406u,0x100000u,0x80000000u,0x26700u,0x1d410u,0x1d400u,0x80000000u,0x26702u,0x1d412u,0x100000u,0x80000000u,0x26704u,0x1d414u,0x100000u,0x80000000u,0x26706u,0x1d416u,0x100000u,0x80000000u,0x26900u,0x1d420u,0x1d400u,0x80000000u,0x26902u,0x1d422u,0x100000u,0x80000000u,0x26904u,0x1d424u,0x100000u,0x80000000u,0x26906u,0x1d426u,0x100000u,0x80000000u,0x26b00u,0x1d430u,0x1d400u,0x80000000u,0x26b02u,0x1d432u,0x100000u,0x80000000u,0x26b04u,0x1d434u,0x100000u,0x80000000u,0x26b06u,0x1d436u,0x100000u,0x80000000u,0x26508u,0x1d408u,0x1d410u,0x80000000u,0x2650au,0x1d40au,0x100000u,0x80000000u,0x2650cu,0x1d40cu,0x100000u,0x80000000u,0x2650eu,0x1d40eu,0x100000u,0x80000000u,0x26708u,0x1d418u,0x1d410u,0x80000000u,0x2670au,0x1d41au,0x100000u,0x80000000u,0x2670cu,0x1d41cu,0x100000u,0x80000000u,0x2670eu,0x1d41eu,0x100000u,0x80000000u,0x26908u,0x1d428u,0x1d410u,0x80000000u,0x2690au,0x1d42au,0x100000u,0x80000000u,0x2690cu,0x1d42cu,0x100000u,0x80000000u,0x2690eu,0x1d42eu,0x100000u,0x80000000u,0x26b08u,0x1d438u,0x1d410u,0x80000000u,0x26b0au,0x1d43au,0x100000u,0x80000000u,0x26b0cu,0x1d43cu,0x100000u,0x80000000u,0x26b0eu,0x1d43eu,0x100000u,0x80000000u,0x26d00u,0x1d440u,0x1d420u,0x80000000u,0x26d02u,0x1d442u,0x100000u,0x80000000u,0x26d04u,0x1d444u,0x100000u,0x80000000u,0x26d06u,0x1d446u,0x100000u,0x80000000u,0x26f00u,0x1d450u,0x1d420u,0x80000000u,0x26f02u,0x1d452u,0x100000u,0x80000000u,0x26f04u,0x1d454u,0x100000u,0x80000000u,0x26f06u,0x1d456u,0x100000u,0x80000000u,0x27100u,0x1d460u,0x1d420u,0x80000000u,0x27102u,0x1d462u,0x100000u,0x80000000u,0x27104u,0x1d464u,0x100000u,0x80000000u,0x27106u,0x1d466u,0x100000u,0x80000000u,0x27300u,0x1d470u,0x1d420u,0x80000000u,0x27302u,0x1d472u,0x100000u,0x80000000u,0x27304u,0x1d474u,0x100000u,0x80000000u,0x27306u,0x1d476u,0x100000u,0x80000000u,0x100000u,0x1d448u,0x1d430u,0x80000000u,0x100000u,0x1d44au,0x100000u,0x80000000u,0x100000u,0x1d44cu,0x100000u,0x80000000u,0x100000u,0x1d44eu,0x100000u,0x80000000u,0x100000u,0x1d458u,0x1d430u,0x80000000u,0x100000u,0x1d45au,0x100000u,0x80000000u,0x100000u,0x1d45cu,0x100000u,0x80000000u,0x100000u,0x1d45eu,0x100000u,0x80000000u,0x100000u,0x1d468u,0x1d430u,0x80000000u,0x100000u,0x1d46au,0x100000u,0x80000000u,0x100000u,0x1d46cu,0x100000u,0x80000000u,0x100000u,0x1d46eu,0x100000u,0x80000000u,0x100000u,0x1d478u,0x1d430u,0x80000000u,0x100000u,0x1d47au,0x100000u,0x80000000u,0x100000u,0x1d47cu,0x100000u,0x80000000u,0x100000u,0x1d47eu,0x100000u,0x80000000u,0x850u,0x60000u,0x90420u,0xefc50u,0x840u,0x60010u,0x90430u,0xefc40u,0x870u,0x60020u,0x90400u,0xefc70u,0x860u,0x60030u,0x90410u,0xefc60u,0x810u,0x60040u,0x90460u,0xefc10u,0x800u,0x60050u,0x90470u,0xefc00u,0x830u,0x60060u,0x90440u,0xefc30u,0x820u,0x60070u,0x90450u,0xefc20u,0x8d0u,0x60080u,0x904b0u,0xefcd0u,0x8c0u,0x60090u,0x904a0u,0xefcc0u,0x8f0u,0x600a0u,0x90490u,0xefcf0u,0x8e0u,0x600b0u,0x90480u,0xefce0u,0x890u,0x600c0u,0x904f0u,0xefc90u,0x880u,0x600d0u,0x904e0u,0xefc80u,0x8b0u,0x600e0u,0x904d0u,0xefcb0u,0x8a0u,0x600f0u,0x904c0u,0xefca0u,0x960u,0x60100u,0x90530u,0xefd60u,0x970u,0x60110u,0x90520u,0xefd70u,0x940u,0x60120u,0x90510u,0xefd40u,0x950u,0x60130u,0x90500u,0xefd50u,0x920u,0x60140u,0x90570u,0xefd20u,0x930u,0x60150u,0x90560u,0xefd30u,0x900u,0x60160u,0x90550u,0xefd00u,0x910u,0x60170u,0x90540u,0xefd10u,0x9e0u,0x60190u,0x905b0u,0xefde0u,0x9f0u,0x60180u,0x905a0u,0xefdf0u,0x9c0u,0x601b0u,0x90590u,0xefdc0u,0x9d0u,0x601a0u,0x90580u,0xefdd0u,0x9a0u,0x601d0u,0x905f0u,0xefda0u,0x9b0u,0x601c0u,0x905e0u,0xefdb0u,0x980u,0x601f0u,0x905d0u,0xefd80u,0x990u,0x601e0u,0x905c0u,0xefd90u,0xa60u,0x60210u,0xbfa40u,0xefe60u,0xa70u,0x60200u,0xbfa50u,0xefe70u,0xa40u,0x60230u,0xbfa60u,0xefe40u,0xa50u,0x60220u,0xbfa70u,0xefe50u,0xa20u,0x60250u,0xbfa00u,0xefe20u,0xa30u,0x60240u,0xbfa10u,0xefe30u,0xa00u,0x60270u,0xbfa20u,0xefe00u,0xa10u,0x60260u,0xbfa30u,0xefe10u,0x2fef0u,0x60290u,0xbfac0u,0xefef0u,0x2fee0u,0x60280u,0xbfad0u,0xefee0u,0x2fed0u,0x602b0u,0xbfae0u,0xefed0u,0x2fec0u,0x602a0u,0xbfaf0u,0xefec0u,0x2feb0u,0x602d0u,0xbfa80u,0xefeb0u,0x2fea0u,0x602c0u,0xbfa90u,0xefea0u,0x2fe90u,0x602f0u,0xbfaa0u,0xefe90u,0x2fe80u,0x602e0u,0xbfab0u,0xefe80u,0x2ff70u,0x60320u,0xbfb40u,0xeff70u,0x2ff60u,0x60330u,0xbfb50u,0xeff60u,0x2ff50u,0x60300u,0xbfb60u,0xeff50u,0x2ff40u,0x60310u,0xbfb70u,0xeff40u,0x2ff30u,0x60360u,0xbfb00u,0xeff30u,0x2ff20u,0x60370u,0xbfb10u,0xeff20u,0x2ff10u,0x60340u,0xbfb20u,0xeff10u,0x2ff00u,0x60350u,0xbfb30u,0xeff00u,0x2fff0u,0x603a0u,0xbfbd0u,0xefff0u,0x2ffe0u,0x603b0u,0xbfbc0u,0xeffe0u,0x2ffd0u,0x60380u,0xbfbf0u,0xeffd0u,0x2ffc0u,0x60390u,0xbfbe0u,0xeffc0u,0x2ffb0u,0x603e0u,0xbfb90u,0xeffb0u,0x2ffa0u,0x603f0u,0xbfb80u,0xeffa0u,0x2ff90u,0x603c0u,0xbfbb0u,0xeff90u,0x2ff80u,0x603d0u,0xbfba0u,0xeff80u,0x0u,0x80000000u,0x0u,0x18830u,0x0u,0x80000000u,0x0u,0x18830u,0x0u,0x80000000u,0x0u,0x18830u,0x0u,0x80000000u,0x0u,0x18830u,0x0u,0x80000000u,0x0u,0x18830u,0x0u,0x80000000u,0x0u,0x18830u,0x0u,0x80000000u,0x0u,0x18830u,0x0u,0x80000000u,0x0u,0x18830u,0x0u,0x80000000u,0x0u,0x18830u,0x0u,0x80000000u,0x0u,0x18830u,0x0u,0x80000000u,0x0u,0x18830u,0x0u,0x80000000u,0x0u,0x18830u,0x0u,0x80000000u,0x0u,0x18830u,0x0u,0x80000000u,0x0u,0x18830u,0x0u,0x80000000u,0x0u,0x18830u,0x0u,0x80000000u,0x0u,0x18830u,0x0u,0x80000000u,0x0u,0x188b2u,0x0u,0x80000000u,0x0u,0x188b2u,0x0u,0x80000000u,0x0u,0x188b2u,0x0u,0x80000000u,0x0u,0x188b2u,0x0u,0x80000000u,0x0u,0x188b2u,0x0u,0x80000000u,0x0u,0x188b2u,0x0u,0x80000000u,0x0u,0x188b2u,0x0u,0x80000000u,0x0u,0x188b2u,0x0u,0x80000000u,0x0u,0x18892u,0x0u,0x80000000u,0x0u,0x18892u,0x0u,0x80000000u,0x0u,0x18892u,0x0u,0x80000000u,0x0u,0x18892u,0x0u,0x80000000u,0x0u,0x18892u,0x0u,0x80000000u,0x0u,0x18892u,0x0u,0x80000000u,0x0u,0x18892u,0x0u,0x80000000u,0x0u,0x18892u,0x0u,0x80000000u,0x0u,0x1489au,0x0u,0x80000000u,0x0u,0x1489au,0x0u,0x80000000u,0x0u,0x1489au,0x0u,0x80000000u,0x0u,0x1489au,0x0u,0x80000000u,0x0u,0x1489au,0x0u,0x80000000u,0x0u,0x1489au,0x0u,0x80000000u,0x0u,0x1489au,0x0u,0x80000000u,0x0u,0x1489au,0x0u,0x80000000u,0x0u,0x34018u,0x0u,0x80000000u,0x0u,0x34018u,0x0u,0x80000000u,0x0u,0x34018u,0x0u,0x80000000u,0x0u,0x34018u,0x0u,0x80000000u,0x0u,0x34018u,0x0u,0x80000000u,0x0u,0x34018u,0x0u,0x80000000u,0x0u,0x34018u,0x0u,0x80000000u,0x0u,0x34018u,0x0u,0x80000000u,0x0u,0x36018u,0x0u,0x80000000u,0x0u,0x36018u,0x0u,0x80000000u,0x0u,0x36018u,0x0u,0x80000000u,0x0u,0x36018u,0x0u,0x80000000u,0x0u,0x36018u,0x0u,0x80000000u,0x0u,0x36018u,0x0u,0x80000000u,0x0u,0x36018u,0x0u,0x80000000u,0x0u,0x36018u,0x0u,0x80000000u,0x0u,0x36030u,0x0u,0x80000000u,0x0u,0x36030u,0x0u,0x80000000u,0x0u,0x36030u,0x0u,0x80000000u,0x0u,0x36030u,0x0u,0x80000000u,0x0u,0x36030u,0x0u,0x80000000u,0x0u,0x36030u,0x0u,0x80000000u,0x0u,0x36030u,0x0u,0x80000000u,0x0u,0x36030u,0x6840u,0x6860u,0x6820u,0x6820u,0x6848u,0x6868u,0x6828u,0x6830u,0x6850u,0x6870u,0x6830u,0x6840u,0x6858u,0x6878u,0x6838u,0x6850u,0x68c0u,0x68e0u,0x68a0u,0x68a0u,0x68c8u,0x68e8u,0x68a8u,0x68b0u,0x68d0u,0x68f0u,0x68b0u,0x68c0u,0x68d8u,0x68f8u,0x68b8u,0x68d0u,0x6960u,0x6940u,0x6900u,0x6900u,0x6968u,0x6948u,0x6908u,0x6910u,0x6970u,0x6950u,0x6910u,0x6960u,0x6978u,0x6958u,0x6918u,0x6970u,0x69e0u,0x69c0u,0x6980u,0x6980u,0x69e8u,0x69c8u,0x6988u,0x6990u,0x69f0u,0x69d0u,0x6990u,0x69e0u,0x69f8u,0x69d8u,0x6998u,0x69f0u,0x6c00u,0x6c20u,0x6c60u,0x6a20u,0x6c08u,0x6c28u,0x6c68u,0x6a30u,0x6c10u,0x6c30u,0x6c70u,0x6a40u,0x6c18u,0x6c38u,0x6c78u,0x6a50u,0x6c80u,0x6ca0u,0x6ce0u,0x6aa0u,0x6c88u,0x6ca8u,0x6ce8u,0x6ab0u,0x6c90u,0x6cb0u,0x6cf0u,0x6ac0u,0x6c98u,0x6cb8u,0x6cf8u,0x6ad0u,0x6d20u,0x6d00u,0x6d40u,0x6b00u,0x6d28u,0x6d08u,0x6d48u,0x6b10u,0x6d30u,0x6d10u,0x6d50u,0x6b60u,0x6d38u,0x6d18u,0x6d58u,0x6b70u,0x6da0u,0x6d80u,0x6dc0u,0x6b80u,0x6da8u,0x6d88u,0x6dc8u,0x6b90u,0x6db0u,0x6d90u,0x6dd0u,0x6be0u,0x6db8u,0x6d98u,0x6dd8u,0x6bf0u,0x7040u,0x7060u,0x7020u,0x6c60u,0x7048u,0x7068u,0x7028u,0x6c70u,0x7050u,0x7070u,0x7030u,0x6c00u,0x7058u,0x7078u,0x7038u,0x6c10u,0x70c0u,0x70e0u,0x70a0u,0x6ce0u,0x70c8u,0x70e8u,0x70a8u,0x6cf0u,0x70d0u,0x70f0u,0x70b0u,0x6c80u,0x70d8u,0x70f8u,0x70b8u,0x6c90u,0x7160u,0x7140u,0x7100u,0x6d40u,0x7168u,0x7148u,0x7108u,0x6d50u,0x7170u,0x7150u,0x7110u,0x6d20u,0x7178u,0x7158u,0x7118u,0x6d30u,0x71e0u,0x71c0u,0x7180u,0x6dc0u,0x71e8u,0x71c8u,0x7188u,0x6dd0u,0x71f0u,0x71d0u,0x7190u,0x6da0u,0x71f8u,0x71d8u,0x7198u,0x6db0u,0x7400u,0x7420u,0x7460u,0x6e60u,0x7408u,0x7428u,0x7468u,0x6e70u,0x7410u,0x7430u,0x7470u,0x6e00u,0x7418u,0x7438u,0x7478u,0x6e10u,0x7480u,0x74a0u,0x74e0u,0x6ee0u,0x7488u,0x74a8u,0x74e8u,0x6ef0u,0x7490u,0x74b0u,0x74f0u,0x6e80u,0x7498u,0x74b8u,0x74f8u,0x6e90u,0x7520u,0x7500u,0x7540u,0x6f40u,0x7528u,0x7508u,0x7548u,0x6f50u,0x7530u,0x7510u,0x7550u,0x6f20u,0x7538u,0x7518u,0x7558u,0x6f30u,0x75a0u,0x7580u,0x75c0u,0x6fc0u,0x75a8u,0x7588u,0x75c8u,0x6fd0u,0x75b0u,0x7590u,0x75d0u,0x6fa0u,0x75b8u,0x7598u,0x75d8u,0x6fb0u,0x6860u,0x1ea40u,0x1ec20u,0x2440u,0x6868u,0x1eed0u,0x1f0b0u,0x2450u,0x6870u,0x1f360u,0x1f540u,0x2460u,0x6878u,0x1f7f0u,0x1f9d0u,0x2470u,0x68e0u,0x1fc80u,0x1fe60u,0x2400u,0x68e8u,0x20110u,0x202f0u,0x2410u,0x68f0u,0x205a0u,0x20780u,0x2420u,0x68f8u,0x20a30u,0x20c10u,0x2430u,0x6940u,0x20ec0u,0x210a0u,0x25c0u,0x6948u,0x21350u,0x21530u,0x25d0u,0x6950u,0x217e0u,0x219c0u,0x25e0u,0x6958u,0x21c70u,0x21e50u,0x25f0u,0x69c0u,0x22100u,0x222e0u,0x2580u,0x69c8u,0x22590u,0x22770u,0x2590u,0x69d0u,0x22a20u,0x22c00u,0x25a0u,0x69d8u,0x22eb0u,0x23090u,0x25b0u,0x6a60u,0x1ea70u,0x1ec28u,0x2760u,0x6a68u,0x1ef00u,0x1f0b8u,0x2770u,0x6a70u,0x1f390u,0x1f548u,0x2740u,0x6a78u,0x1f820u,0x1f9d8u,0x2750u,0x6ae0u,0x1fcb0u,0x1fe68u,0x2720u,0x6ae8u,0x20140u,0x202f8u,0x2730u,0x6af0u,0x205d0u,0x20788u,0x2700u,0x6af8u,0x20a60u,0x20c18u,0x2710u,0x6b40u,0x20ef0u,0x210a8u,0x28e0u,0x6b48u,0x21380u,0x21538u,0x28f0u,0x6b50u,0x21810u,0x219c8u,0x28c0u,0x6b58u,0x21ca0u,0x21e58u,0x28d0u,0x6bc0u,0x22130u,0x222e8u,0x28a0u,0x6bc8u,0x225c0u,0x22778u,0x28b0u,0x6bd0u,0x22a50u,0x22c08u,0x2880u,0x6bd8u,0x22ee0u,0x23098u,0x2890u,0x6c20u,0x1eaa0u,0x1ec30u,0x2a40u,0x6c28u,0x1ef30u,0x1f0c0u,0x2a50u,0x6c30u,0x1f3c0u,0x1f550u,0x2a60u,0x6c38u,0x1f850u,0x1f9e0u,0x2a70u,0x6ca0u,0x1fce0u,0x1fe70u,0x2a00u,0x6ca8u,0x20170u,0x20300u,0x2a10u,0x6cb0u,0x20600u,0x20790u,0x2a20u,0x6cb8u,0x20a90u,0x20c20u,0x2a30u,0x6d00u,0x20f20u,0x210b0u,0x2bc0u,0x6d08u,0x213b0u,0x21540u,0x2bd0u,0x6d10u,0x21840u,0x219d0u,0x2be0u,0x6d18u,0x21cd0u,0x21e60u,0x2bf0u,0x6d80u,0x22160u,0x222f0u,0x2b80u,0x6d88u,0x225f0u,0x22780u,0x2b90u,0x6d90u,0x22a80u,0x22c10u,0x2ba0u,0x6d98u,0x22f10u,0x230a0u,0x2bb0u,0x6e20u,0x1ead0u,0x1ec38u,0x2d60u,0x6e28u,0x1ef60u,0x1f0c8u,0x2d70u,0x6e30u,0x1f3f0u,0x1f558u,0x2d40u,0x6e38u,0x1f880u,0x1f9e8u,0x2d50u,0x6ea0u,0x1fd10u,0x1fe78u,0x2d20u,0x6ea8u,0x201a0u,0x20308u,0x2d30u,0x6eb0u,0x20630u,0x20798u,0x2d00u,0x6eb8u,0x20ac0u,0x20c28u,0x2d10u,0x6f00u,0x20f50u,0x210b8u,0x2ee0u,0x6f08u,0x213e0u,0x21548u,0x2ef0u,0x6f10u,0x21870u,0x219d8u,0x2ec0u,0x6f18u,0x21d00u,0x21e68u,0x2ed0u,0x6f80u,0x22190u,0x222f8u,0x2ea0u,0x6f88u,0x22620u,0x22788u,0x2eb0u,0x6f90u,0x22ab0u,0x22c18u,0x2e80u,0x6f98u,0x22f40u,0x230a8u,0x2e90u,0x0u,0x40u,0x9000u,0xcc00u,0x80u,0xc0u,0x9010u,0xcc10u,0x110u,0x150u,0x9020u,0xcc20u,0x190u,0x1d0u,0x9030u,0xcc30u,0x220u,0x260u,0x9040u,0xcc40u,0x2a0u,0x2e0u,0x9050u,0xcc50u,0x330u,0x370u,0x9060u,0xcc60u,0x3b0u,0x3f0u,0x9070u,0xcc70u,0x440u,0x400u,0x9080u,0xcc80u,0x4c0u,0x480u,0x9090u,0xcc90u,0x550u,0x510u,0x90a0u,0xcca0u,0x5d0u,0x590u,0x90b0u,0xccb0u,0x660u,0x620u,0x90c0u,0xccc0u,0x6e0u,0x6a0u,0x90d0u,0xccd0u,0x770u,0x730u,0x90e0u,0xcce0u,0x7f0u,0x7b0u,0x90f0u,0xccf0u,0x10u,0x50u,0x9300u,0xcf00u,0x90u,0xd0u,0x9310u,0xcf10u,0x100u,0x140u,0x9320u,0xcf20u,0x180u,0x1c0u,0x9330u,0xcf30u,0x230u,0x270u,0x9340u,0xcf40u,0x2b0u,0x2f0u,0x9350u,0xcf50u,0x320u,0x360u,0x9360u,0xcf60u,0x3a0u,0x3e0u,0x9370u,0xcf70u,0x450u,0x410u,0x9380u,0xcf80u,0x4d0u,0x490u,0x9390u,0xcf90u,0x540u,0x500u,0x93a0u,0xcfa0u,0x5c0u,0x580u,0x93b0u,0xcfb0u,0x670u,0x630u,0x93c0u,0xcfc0u,0x6f0u,0x6b0u,0x93d0u,0xcfd0u,0x760u,0x720u,0x93e0u,0xcfe0u,0x7e0u,0x7a0u,0x93f0u,0xcff0u,0x20u,0x60u,0x9600u,0xd200u,0xa0u,0xe0u,0x9610u,0xd210u,0x130u,0x170u,0x9620u,0xd220u,0x1b0u,0x1f0u,0x9630u,0xd230u,0x200u,0x240u,0x9640u,0xd240u,0x280u,0x2c0u,0x9650u,0xd250u,0x310u,0x350u,0x9660u,0xd260u,0x390u,0x3d0u,0x9670u,0xd270u,0x460u,0x420u,0x9680u,0xd280u,0x4e0u,0x4a0u,0x9690u,0xd290u,0x570u,0x530u,0x96a0u,0xd2a0u,0x5f0u,0x5b0u,0x96b0u,0xd2b0u,0x640u,0x600u,0x96c0u,0xd2c0u,0x6c0u,0x680u,0x96d0u,0xd2d0u,0x750u,0x710u,0x96e0u,0xd2e0u,0x7d0u,0x790u,0x96f0u,0xd2f0u,0x30u,0x70u,0x9900u,0xd500u,0xb0u,0xf0u,0x9910u,0xd510u,0x120u,0x160u,0x9920u,0xd520u,0x1a0u,0x1e0u,0x9930u,0xd530u,0x210u,0x250u,0x9940u,0xd540u,0x290u,0x2d0u,0x9950u,0xd550u,0x300u,0x340u,0x9960u,0xd560u,0x380u,0x3c0u,0x9970u,0xd570u,0x470u,0x430u,0x9980u,0xd580u,0x4f0u,0x4b0u,0x9990u,0xd590u,0x560u,0x520u,0x99a0u,0xd5a0u,0x5e0u,0x5a0u,0x99b0u,0xd5b0u,0x650u,0x610u,0x99c0u,0xd5c0u,0x6d0u,0x690u,0x99d0u,0xd5d0u,0x740u,0x700u,0x99e0u,0xd5e0u,0x7c0u,0x780u,0x99f0u,0xd5f0u,0xc008u,0x12120u,0x12160u,0x12c00u,0xc018u,0x122b0u,0x122f0u,0x12c10u,0xc028u,0x12400u,0x12440u,0x12c20u,0xc038u,0x12590u,0x125d0u,0x12c30u,0xc048u,0x12760u,0x12720u,0x12c40u,0xc058u,0x128f0u,0x128b0u,0x12c50u,0xc068u,0x12a40u,0x12a00u,0x12c60u,0xc078u,0x12bd0u,0x12b90u,0x12c70u,0xc088u,0x12d20u,0x12d60u,0x12c80u,0xc098u,0x12eb0u,0x12ef0u,0x12c90u,0xc0a8u,0x13000u,0x13040u,0x12ca0u,0xc0b8u,0x13190u,0x131d0u,0x12cb0u,0xc0c8u,0x13360u,0x13320u,0x12cc0u,0xc0d8u,0x134f0u,0x134b0u,0x12cd0u,0xc0e8u,0x13640u,0x13600u,0x12ce0u,0xc0f8u,0x137d0u,0x13790u,0x12cf0u,0xc308u,0x12130u,0x12168u,0x12d00u,0xc318u,0x122a0u,0x122f8u,0x12d10u,0xc328u,0x12410u,0x12448u,0x12d20u,0xc338u,0x12580u,0x125d8u,0x12d30u,0xc348u,0x12770u,0x12728u,0x12d40u,0xc358u,0x128e0u,0x128b8u,0x12d50u,0xc368u,0x12a50u,0x12a08u,0x12d60u,0xc378u,0x12bc0u,0x12b98u,0x12d70u,0xc388u,0x12d30u,0x12d68u,0x12d80u,0xc398u,0x12ea0u,0x12ef8u,0x12d90u,0xc3a8u,0x13010u,0x13048u,0x12da0u,0xc3b8u,0x13180u,0x131d8u,0x12db0u,0xc3c8u,0x13370u,0x13328u,0x12dc0u,0xc3d8u,0x134e0u,0x134b8u,0x12dd0u,0xc3e8u,0x13650u,0x13608u,0x12de0u,0xc3f8u,0x137c0u,0x13798u,0x12df0u,0xc608u,0x12140u,0x12170u,0x12e00u,0xc618u,0x122d0u,0x122e0u,0x12e10u,0xc628u,0x12460u,0x12450u,0x12e20u,0xc638u,0x125f0u,0x125c0u,0x12e30u,0xc648u,0x12700u,0x12730u,0x12e40u,0xc658u,0x12890u,0x128a0u,0x12e50u,0xc668u,0x12a20u,0x12a10u,0x12e60u,0xc678u,0x12bb0u,0x12b80u,0x12e70u,0xc688u,0x12d40u,0x12d70u,0x12e80u,0xc698u,0x12ed0u,0x12ee0u,0x12e90u,0xc6a8u,0x13060u,0x13050u,0x12ea0u,0xc6b8u,0x131f0u,0x131c0u,0x12eb0u,0xc6c8u,0x13300u,0x13330u,0x12ec0u,0xc6d8u,0x13490u,0x134a0u,0x12ed0u,0xc6e8u,0x13620u,0x13610u,0x12ee0u,0xc6f8u,0x137b0u,0x13780u,0x12ef0u,0xc908u,0x12150u,0x12178u,0x12f00u,0xc918u,0x122c0u,0x122e8u,0x12f10u,0xc928u,0x12470u,0x12458u,0x12f20u,0xc938u,0x125e0u,0x125c8u,0x12f30u,0xc948u,0x12710u,0x12738u,0x12f40u,0xc958u,0x12880u,0x128a8u,0x12f50u,0xc968u,0x12a30u,0x12a18u,0x12f60u,0xc978u,0x12ba0u,0x12b88u,0x12f70u,0xc988u,0x12d50u,0x12d78u,0x12f80u,0xc998u,0x12ec0u,0x12ee8u,0x12f90u,0xc9a8u,0x13070u,0x13058u,0x12fa0u,0xc9b8u,0x131e0u,0x131c8u,0x12fb0u,0xc9c8u,0x13310u,0x13338u,0x12fc0u,0xc9d8u,0x13480u,0x134a8u,0x12fd0u,0xc9e8u,0x13630u,0x13618u,0x12fe0u,0xc9f8u,0x137a0u,0x13788u,0x12ff0u,0x8000u,0x1c300u,0x1ea40u,0x1ec20u,0x8080u,0x1c310u,0x100000u,0x1ec22u,0x8110u,0x1c320u,0x100000u,0x1ec24u,0x8190u,0x1c330u,0x100000u,0x1ec26u,0x8220u,0x1c340u,0x1ea40u,0x1ec28u,0x82a0u,0x1c350u,0x100000u,0x1ec2au,0x8330u,0x1c360u,0x100000u,0x1ec2cu,0x83b0u,0x1c370u,0x100000u,0x1ec2eu,0x8440u,0x1c380u,0x1ea40u,0x1ec30u,0x84c0u,0x1c390u,0x100000u,0x1ec32u,0x8550u,0x1c3a0u,0x100000u,0x1ec34u,0x85d0u,0x1c3b0u,0x100000u,0x1ec36u,0x8660u,0x1c3c0u,0x1ea40u,0x1ec38u,0x86e0u,0x1c3d0u,0x100000u,0x1ec3au,0x8770u,0x1c3e0u,0x100000u,0x1ec3cu,0x87f0u,0x1c3f0u,0x100000u,0x1ec3eu,0x8010u,0x1c400u,0x1ea70u,0x1ec20u,0x8090u,0x1c410u,0x100000u,0x1ec22u,0x8100u,0x1c420u,0x100000u,0x1ec24u,0x8180u,0x1c430u,0x100000u,0x1ec26u,0x8230u,0x1c440u,0x1ea70u,0x1ec28u,0x82b0u,0x1c450u,0x100000u,0x1ec2au,0x8320u,0x1c460u,0x100000u,0x1ec2cu,0x83a0u,0x1c470u,0x100000u,0x1ec2eu,0x8450u,0x1c480u,0x1ea70u,0x1ec30u,0x84d0u,0x1c490u,0x100000u,0x1ec32u,0x8540u,0x1c4a0u,0x100000u,0x1ec34u,0x85c0u,0x1c4b0u,0x100000u,0x1ec36u,0x8670u,0x1c4c0u,0x1ea70u,0x1ec38u,0x86f0u,0x1c4d0u,0x100000u,0x1ec3au,0x8760u,0x1c4e0u,0x100000u,0x1ec3cu,0x87e0u,0x1c4f0u,0x100000u,0x1ec3eu,0x8020u,0x1c500u,0x1eaa0u,0x1ec20u,0x80a0u,0x1c510u,0x100000u,0x1ec22u,0x8130u,0x1c520u,0x100000u,0x1ec24u,0x81b0u,0x1c530u,0x100000u,0x1ec26u,0x8200u,0x1c540u,0x1eaa0u,0x1ec28u,0x8280u,0x1c550u,0x100000u,0x1ec2au,0x8310u,0x1c560u,0x100000u,0x1ec2cu,0x8390u,0x1c570u,0x100000u,0x1ec2eu,0x8460u,0x1c580u,0x1eaa0u,0x1ec30u,0x84e0u,0x1c590u,0x100000u,0x1ec32u,0x8570u,0x1c5a0u,0x100000u,0x1ec34u,0x85f0u,0x1c5b0u,0x100000u,0x1ec36u,0x8640u,0x1c5c0u,0x1eaa0u,0x1ec38u,0x86c0u,0x1c5d0u,0x100000u,0x1ec3au,0x8750u,0x1c5e0u,0x100000u,0x1ec3cu,0x87d0u,0x1c5f0u,0x100000u,0x1ec3eu,0x8030u,0x1c600u,0x1ead0u,0x1ec20u,0x80b0u,0x1c610u,0x100000u,0x1ec22u,0x8120u,0x1c620u,0x100000u,0x1ec24u,0x81a0u,0x1c630u,0x100000u,0x1ec26u,0x8210u,0x1c640u,0x1ead0u,0x1ec28u,0x8290u,0x1c650u,0x100000u,0x1ec2au,0x8300u,0x1c660u,0x100000u,0x1ec2cu,0x8380u,0x1c670u,0x100000u,0x1ec2eu,0x8470u,0x1c680u,0x1ead0u,0x1ec30u,0x84f0u,0x1c690u,0x100000u,0x1ec32u,0x8560u,0x1c6a0u,0x100000u,0x1ec34u,0x85e0u,0x1c6b0u,0x100000u,0x1ec36u,0x8650u,0x1c6c0u,0x1ead0u,0x1ec38u,0x86d0u,0x1c6d0u,0x100000u,0x1ec3au,0x8740u,0x1c6e0u,0x100000u,0x1ec3cu,0x87c0u,0x1c6f0u,0x100000u,0x1ec3eu,0x27500u,0x1d480u,0x1d480u,0x80000000u,0x27502u,0x1d482u,0x100000u,0x80000000u,0x27504u,0x1d484u,0x100000u,0x80000000u,0x27506u,0x1d486u,0x100000u,0x80000000u,0x27700u,0x1d490u,0x1d480u,0x80000000u,0x27702u,0x1d492u,0x100000u,0x80000000u,0x27704u,0x1d494u,0x100000u,0x80000000u,0x27706u,0x1d496u,0x100000u,0x80000000u,0x27900u,0x1d4a0u,0x1d480u,0x80000000u,0x27902u,0x1d4a2u,0x100000u,0x80000000u,0x27904u,0x1d4a4u,0x100000u,0x80000000u,0x27906u,0x1d4a6u,0x100000u,0x80000000u,0x27b00u,0x1d4b0u,0x1d480u,0x80000000u,0x27b02u,0x1d4b2u,0x100000u,0x80000000u,0x27b04u,0x1d4b4u,0x100000u,0x80000000u,0x27b06u,0x1d4b6u,0x100000u,0x80000000u,0x27508u,0x1d488u,0x1d490u,0x80000000u,0x2750au,0x1d48au,0x100000u,0x80000000u,0x2750cu,0x1d48cu,0x100000u,0x80000000u,0x2750eu,0x1d48eu,0x100000u,0x80000000u,0x27708u,0x1d498u,0x1d490u,0x80000000u,0x2770au,0x1d49au,0x100000u,0x80000000u,0x2770cu,0x1d49cu,0x100000u,0x80000000u,0x2770eu,0x1d49eu,0x100000u,0x80000000u,0x27908u,0x1d4a8u,0x1d490u,0x80000000u,0x2790au,0x1d4aau,0x100000u,0x80000000u,0x2790cu,0x1d4acu,0x100000u,0x80000000u,0x2790eu,0x1d4aeu,0x100000u,0x80000000u,0x27b08u,0x1d4b8u,0x1d490u,0x80000000u,0x27b0au,0x1d4bau,0x100000u,0x80000000u,0x27b0cu,0x1d4bcu,0x100000u,0x80000000u,0x27b0eu,0x1d4beu,0x100000u,0x80000000u,0x26d08u,0x1d4c0u,0x1d4a0u,0x80000000u,0x26d0au,0x1d4c2u,0x100000u,0x80000000u,0x26d0cu,0x1d4c4u,0x100000u,0x80000000u,0x26d0eu,0x1d4c6u,0x100000u,0x80000000u,0x26f08u,0x1d4d0u,0x1d4a0u,0x80000000u,0x26f0au,0x1d4d2u,0x100000u,0x80000000u,0x26f0cu,0x1d4d4u,0x100000u,0x80000000u,0x26f0eu,0x1d4d6u,0x100000u,0x80000000u,0x27108u,0x1d4e0u,0x1d4a0u,0x80000000u,0x2710au,0x1d4e2u,0x100000u,0x80000000u,0x2710cu,0x1d4e4u,0x100000u,0x80000000u,0x2710eu,0x1d4e6u,0x100000u,0x80000000u,0x27308u,0x1d4f0u,0x1d4a0u,0x80000000u,0x2730au,0x1d4f2u,0x100000u,0x80000000u,0x2730cu,0x1d4f4u,0x100000u,0x80000000u,0x2730eu,0x1d4f6u,0x100000u,0x80000000u,0x100000u,0x1d4c8u,0x1d4b0u,0x80000000u,0x100000u,0x1d4cau,0x100000u,0x80000000u,0x100000u,0x1d4ccu,0x100000u,0x80000000u,0x100000u,0x1d4ceu,0x100000u,0x80000000u,0x100000u,0x1d4d8u,0x1d4b0u,0x80000000u,0x100000u,0x1d4dau,0x100000u,0x80000000u,0x100000u,0x1d4dcu,0x100000u,0x80000000u,0x100000u,0x1d4deu,0x100000u,0x80000000u,0x100000u,0x1d4e8u,0x1d4b0u,0x80000000u,0x100000u,0x1d4eau,0x100000u,0x80000000u,0x100000u,0x1d4ecu,0x100000u,0x80000000u,0x100000u,0x1d4eeu,0x100000u,0x80000000u,0x100000u,0x1d4f8u,0x1d4b0u,0x80000000u,0x100000u,0x1d4fau,0x100000u,0x80000000u,0x100000u,0x1d4fcu,0x100000u,0x80000000u,0x100000u,0x1d4feu,0x100000u,0x80000000u,0x30000u,0x60420u,0xbfc50u,0xf0000u,0x30010u,0x60430u,0xbfc40u,0xf0010u,0x30020u,0x60400u,0xbfc70u,0xf0020u,0x30030u,0x60410u,0xbfc60u,0xf0030u,0x30040u,0x60460u,0xbfc10u,0xf0040u,0x30050u,0x60470u,0xbfc00u,0xf0050u,0x30060u,0x60440u,0xbfc30u,0xf0060u,0x30070u,0x60450u,0xbfc20u,0xf0070u,0x30080u,0x604b0u,0xbfcd0u,0xf0080u,0x30090u,0x604a0u,0xbfcc0u,0xf0090u,0x300a0u,0x60490u,0xbfcf0u,0xf00a0u,0x300b0u,0x60480u,0xbfce0u,0xf00b0u,0x300c0u,0x604f0u,0xbfc90u,0xf00c0u,0x300d0u,0x604e0u,0xbfc80u,0xf00d0u,0x300e0u,0x604d0u,0xbfcb0u,0xf00e0u,0x300f0u,0x604c0u,0xbfca0u,0xf00f0u,0x30100u,0x60530u,0xbfd60u,0xf0100u,0x30110u,0x60520u,0xbfd70u,0xf0110u,0x30120u,0x60510u,0xbfd40u,0xf0120u,0x30130u,0x60500u,0xbfd50u,0xf0130u,0x30140u,0x60570u,0xbfd20u,0xf0140u,0x30150u,0x60560u,0xbfd30u,0xf0150u,0x30160u,0x60550u,0xbfd00u,0xf0160u,0x30170u,0x60540u,0xbfd10u,0xf0170u,0x30190u,0x605b0u,0xbfde0u,0xf0190u,0x30180u,0x605a0u,0xbfdf0u,0xf0180u,0x301b0u,0x60590u,0xbfdc0u,0xf01b0u,0x301a0u,0x60580u,0xbfdd0u,0xf01a0u,0x301d0u,0x605f0u,0xbfda0u,0xf01d0u,0x301c0u,0x605e0u,0xbfdb0u,0xf01c0u,0x301f0u,0x605d0u,0xbfd80u,0xf01f0u,0x301e0u,0x605c0u,0xbfd90u,0xf01e0u,0x30210u,0x60640u,0xbfe60u,0xf0210u,0x30200u,0x60650u,0xbfe70u,0xf0200u,0x30230u,0x60660u,0xbfe40u,0xf0230u,0x30220u,0x60670u,0xbfe50u,0xf0220u,0x30250u,0x60600u,0xbfe20u,0xf0250u,0x30240u,0x60610u,0xbfe30u,0xf0240u,0x30270u,0x60620u,0xbfe00u,0xf0270u,0x30260u,0x60630u,0xbfe10u,0xf0260u,0x30290u,0x606c0u,0xbfef0u,0xf0290u,0x30280u,0x606d0u,0xbfee0u,0xf0280u,0x302b0u,0x606e0u,0xbfed0u,0xf02b0u,0x302a0u,0x606f0u,0xbfec0u,0xf02a0u,0x302d0u,0x60680u,0xbfeb0u,0xf02d0u,0x302c0u,0x60690u,0xbfea0u,0xf02c0u,0x302f0u,0x606a0u,0xbfe90u,0xf02f0u,0x302e0u,0x606b0u,0xbfe80u,0xf02e0u,0x30320u,0x60740u,0xbff70u,0x11f720u,0x30330u,0x60750u,0xbff60u,0x11f730u,0x30300u,0x60760u,0xbff50u,0x11f700u,0x30310u,0x60770u,0xbff40u,0x11f710u,0x30360u,0x60700u,0xbff30u,0x11f760u,0x30370u,0x60710u,0xbff20u,0x11f770u,0x30340u,0x60720u,0xbff10u,0x11f740u,0x30350u,0x60730u,0xbff00u,0x11f750u,0x303a0u,0x8fbd0u,0xbfff0u,0x11f7a0u,0x303b0u,0x8fbc0u,0xbffe0u,0x11f7b0u,0x30380u,0x8fbf0u,0xbffd0u,0x11f780u,0x30390u,0x8fbe0u,0xbffc0u,0x11f790u,0x303e0u,0x8fb90u,0xbffb0u,0x11f7e0u,0x303f0u,0x8fb80u,0xbffa0u,0x11f7f0u,0x303c0u,0x8fbb0u,0xbff90u,0x11f7c0u,0x303d0u,0x8fba0u,0xbff80u,0x11f7d0u,0x0u,0x80000000u,0x0u,0x36030u,0x0u,0x80000000u,0x0u,0x36030u,0x0u,0x80000000u,0x0u,0x36030u,0x0u,0x80000000u,0x0u,0x36030u,0x0u,0x80000000u,0x0u,0x36030u,0x0u,0x80000000u,0x0u,0x36030u,0x0u,0x80000000u,0x0u,0x36030u,0x0u,0x80000000u,0x0u,0x36030u,0x0u,0x80000000u,0x0u,0x360b2u,0x0u,0x80000000u,0x0u,0x360b2u,0x0u,0x80000000u,0x0u,0x360b2u,0x0u,0x80000000u,0x0u,0x360b2u,0x0u,0x80000000u,0x0u,0x360b2u,0x0u,0x80000000u,0x0u,0x360b2u,0x0u,0x80000000u,0x0u,0x360b2u,0x0u,0x80000000u,0x0u,0x360b2u,0x0u,0x80000000u,0x0u,0x36092u,0x0u,0x80000000u,0x0u,0x36092u,0x0u,0x80000000u,0x0u,0x36092u,0x0u,0x80000000u,0x0u,0x36092u,0x0u,0x80000000u,0x0u,0x36092u,0x0u,0x80000000u,0x0u,0x36092u,0x0u,0x80000000u,0x0u,0x36092u,0x0u,0x80000000u,0x0u,0x36092u,0x0u,0x80000000u,0x0u,0x3609au,0x0u,0x80000000u,0x0u,0x3609au,0x0u,0x80000000u,0x0u,0x3609au,0x0u,0x80000000u,0x0u,0x3609au,0x0u,0x80000000u,0x0u,0x3609au,0x0u,0x80000000u,0x0u,0x3609au,0x0u,0x80000000u,0x0u,0x3609au,0x0u,0x80000000u,0x0u,0x3609au,0x0u,0x80000000u,0x0u,0x36059u,0x0u,0x80000000u,0x0u,0x36059u,0x0u,0x80000000u,0x0u,0x36059u,0x0u,0x80000000u,0x0u,0x36059u,0x0u,0x80000000u,0x0u,0x36059u,0x0u,0x80000000u,0x0u,0x36059u,0x0u,0x80000000u,0x0u,0x36059u,0x0u,0x80000000u,0x0u,0x36059u,0x0u,0x80000000u,0x0u,0x36059u,0x0u,0x80000000u,0x0u,0x36059u,0x0u,0x80000000u,0x0u,0x36059u,0x0u,0x80000000u,0x0u,0x36059u,0x0u,0x80000000u,0x0u,0x36059u,0x0u,0x80000000u,0x0u,0x36059u,0x0u,0x80000000u,0x0u,0x36059u,0x0u,0x80000000u,0x0u,0x36059u,0x0u,0x80000000u,0x0u,0x16071u,0x0u,0x80000000u,0x0u,0x16071u,0x0u,0x80000000u,0x0u,0x16071u,0x0u,0x80000000u,0x0u,0x16071u,0x0u,0x80000000u,0x0u,0x16071u,0x0u,0x80000000u,0x0u,0x16071u,0x0u,0x80000000u,0x0u,0x16071u,0x0u,0x80000000u,0x0u,0x16071u,0x0u,0x80000000u,0x0u,0x14071u,0x0u,0x80000000u,0x0u,0x14071u,0x0u,0x80000000u,0x0u,0x14071u,0x0u,0x80000000u,0x0u,0x14071u,0x0u,0x80000000u,0x0u,0x14071u,0x0u,0x80000000u,0x0u,0x14071u,0x0u,0x80000000u,0x0u,0x14071u,0x0u,0x80000000u,0x0u,0x14071u};

__global__ __launch_bounds__(256, 1) void swin_main(const uint16_t* x, const uint16_t* mask, char* ws, uint16_t* out,
                                                    const uint16_t* wqkv, const uint16_t* wo, const uint16_t* ls,
                                                    const uint16_t* w1, const uint16_t* w2, const uint16_t* rc,
                                                    const int* ridx) {
  __shared__ __attribute__((aligned(16))) char smem[LDS_BYTES];
  const int wave = __builtin_amdgcn_readfirstlane(threadIdx.x >> 6);
  const unsigned lds0 = (unsigned)(size_t)smem;
  asm volatile(
      ".p2align 5\n"
      "s_mov_b64 s[92:93], %0\n"
      "s_mov_b32 s40, %1\n"
      "s_mov_b32 s41, %2\n"
      "s_mov_b64 s[88:89], %3\n"
      "s_load_dwordx2 s[16:17], s[92:93], 0\n"
      "s_load_dwordx2 s[20:21], s[92:93], 32\n"
      "s_load_dwordx2 s[46:47], s[92:93], 40\n"
      "s_load_dwordx8 s[80:87], s[92:93], 48\n"
      "s_load_dwordx2 s[76:77], s[92:93], 80\n"
      "s_mov_b64 s[90:91], s[92:93]\n"
      "s_getpc_b64 s[98:99]\n"
      "L_ipf0_%=:\n"
      "v_mbcnt_lo_u32_b32 v8, -1, 0\n"
      "v_mbcnt_hi_u32_b32 v8, -1, v8\n"
      "v_lshlrev_b32 v241, 2, v8\n"
      "v_lshlrev_b32_e64 v144, 4, v8\n"
      "s_lshr_b32 s68, s41, 3\n"
      "s_mov_b32 s72, 0x3100119e\n"
      "s_mov_b32 s73, 0xc0bee051\n"
      "s_mov_b32 s70, 0x317e119e\n"
      "s_mov_b32 s71, 0xc06fe051\n"
      "s_and_b32 s92, s41, 7\n"
      "s_cmp_eq_u32 s92, 6\n"
      "s_cselect_b32 s93, 2, 17\n"
      "s_cmp_lt_u32 s68, s93\n"
      "s_cselect_b64 s[72:73], s[72:73], s[70:71]\n"
      "s_lshl_b32 s92, s92, 3\n"
      "s_lshr_b64 s[72:73], s[72:73], s92\n"
      "s_add_u32 s41, s72, s68\n"
      "s_and_b32 s41, s41, 255\n"
      "s_movk_i32 s96, 5073\n"
      "s_lshr_b32 s92, s41, 5\n"
      "s_cmp_eq_u32 s92, 6\n"
      "s_cselect_b32 s96, 4992, s96\n"
      "s_cmp_lt_u32 s41, 143\n"
      "s_cselect_b32 s96, 5024, s96\n"
      "s_cmp_lt_u32 s41, 192\n"
      "s_cselect_b32 s44, 20, 19\n"
      "s_cmp_lt_u32 s41, 17\n"
      "s_cselect_b32 s44, 21, s44\n"
      "s_cselect_b32 s96, 5167, s96\n"
      "s_add_u32 s96, s96, s41\n"
      "s_lshl_b32 s92, s40, 8\n"
      "v_add_u32_e64 v58, s92, v241\n"
      "v_min_u32 v29, 704, v58\n"
      "v_add_u32 v29, 4096, v29\n"
      "v_min_u32 v58, 672, v58\n"
      "v_add_u32 v214, 122112, v58\n"
      "s_lshl_b32 s92, s68, 7\n"
      "v_lshlrev_b32 v238, 7, v8\n"
      "s_mul_i32 s93, s92, 18\n"
      "s_mul_i32 s94, s40, 640\n"
      "v_min_u32 v239, 512, v238\n"
      "v_add_u32 v239, s94, v239\n"
      "v_min_u32 v239, 2176, v239\n"
      "v_add_u32 v239, s93, v239\n"
      "s_cmp_eq_u32 s40, 1\n"
      "s_cselect_b32 s93, 4096, 0\n"
      "s_add_u32 s92, s92, s93\n"
      "s_cmp_eq_u32 s40, 2\n"
      "s_cselect_b32 s93, 9, 8\n"
      "s_cselect_b64 s[72:73], s[98:99], s[88:89]\n"
      "s_cmp_lt_u32 s40, 2\n"
      "s_cselect_b32 s93, 27, s93\n"
      "s_mul_i32 s92, s92, s93\n"
      "s_lshl_b32 s93, s93, 7\n"
      "s_sub_u32 s93, s93, 128\n"
      "v_min_u32 v238, s93, v238\n"
      "v_add_u32 v238, s92, v238\n"
      "s_lshl_b32 s95, s40, 3\n"
      "v_lshrrev_b32 v253, 3, v8\n"
      "v_and_b32 v240, 7, v8\n"
      "v_add_u32 v252, s95, v253\n"
      "v_bfe_u32 v255, v252, 1, 3\n"
      "v_cmp_lt_u32 s[70:71], s44, v252\n"
      "v_min_u32_e64 v252, s44, v252\n"
      "s_mov_b32 s18, 100270464\n"
      "s_mov_b32 s19, 0x20000\n"
      "s_mov_b32 s78, 9604\n"
      "s_mov_b32 s79, 0x20000\n"
      "s_mov_b32 s22, 221184\n"
      "s_mov_b32 s23, 0x20000\n"
      "s_lshl_b32 s64, s40, 10\n"
      "v_add_u32 v36, s64, v144\n"
      "s_add_u32 s75, s64, 18432\n"
      "s_and_b32 s92, s40, 1\n"
      "s_lshl_b32 s92, s92, 2\n"
      "s_mul_i32 s93, s40, 9216\n"
      "v_and_b32 v254, 2, v253\n"
      "v_xor_b32 v254, v254, v240\n"
      "v_xor_b32 v254, s92, v254\n"
      "v_lshl_add_u32 v254, v254, 4, s93\n"
      "v_mul_u32_u24 v135, 1152, v253\n"
      "v_add_u32_e64 v135, v135, v254\n"
      "s_cmp_lt_u32 s40, 2\n"
      "s_cselect_b32 s65, s64, 163840\n"
      "s_waitcnt lgkmcnt(0)\n"
      "s_cselect_b64 s[72:73], s[20:21], s[72:73]\n"
      "global_load_dword v58, v58, s[86:87]\n"
      "global_load_dword a255, v238, s[72:73]\n"
      "global_load_dword a253, v239, s[46:47]\n"
      "s_mul_i32 s92, s40, 8192\n"
      "s_add_u32 s88, s88, s92\n"
      "s_addc_u32 s89, s89, 0\n"
      "s_add_u32 m0, s64, 124928\n"
      "s_movk_i32 s92, 0\n"
      "buffer_load_dwordx4 v36, s[76:79], s92 offen  lds\n"
      "s_add_u32 m0, s64, 129024\n"
      "s_movk_i32 s92, 4096\n"
      "buffer_load_dwordx4 v36, s[76:79], s92 offen  lds\n"
      "s_add_u32 m0, s64, 133120\n"
      "s_cmp_lt_u32 s40, 2\n"
      "s_cselect_b32 m0, m0, s65\n"
      "s_movk_i32 s92, 8192\n"
      "buffer_load_dwordx4 v36, s[76:79], s92 offen  lds\n"
      "s_load_dwordx16 s[0:15], s[82:83], 0\n"
      "s_load_dwordx16 s[48:63], s[84:85], 0\n"
      "s_load_dwordx16 s[24:39], s[84:85], 64\n"
      "s_load_dwordx2 s[66:67], s[80:81], 0\n"
      "s_movk_i32 s93, 5041\n"
      "s_cmp_lt_u32 s41, 224\n"
      "s_cselect_b32 s92, 192, 0\n"
      "s_cselect_b32 s93, 1184, s93\n"
      "s_cselect_b32 s94, 18, 17\n"
      "s_add_u32 s93, s93, s41\n"
      "v_lshl_add_u32 v238, v252, 8, s41\n"
      "v_mul_u32_u24 v239, s92, v252\n"
      "v_add_u32 v239, s93, v239\n"
      "v_cmp_lt_u32 vcc, s94, v252\n"
      "v_mov_b32_e64 v254, s96\n"
      "s_nop 0\n"
      "v_cndmask_b32 v238, v238, v239, vcc\n"
      "v_cmp_eq_u32 vcc, s44, v252\n"
      "s_nop 1\n"
      "v_cndmask_b32 v238, v238, v254, vcc\n"
      "v_subrev_u32 v254, 5184, v238\n"
      "v_cmp_le_u32 vcc, 5184, v238\n"
      "v_mul_u32_u24 v213, 58255, v238\n"
      "v_lshrrev_b32 v213, 22, v213\n"
      "v_mul_u32_u24 v43, 72, v213\n"
      "v_sub_u32 v43, v238, v43\n"
      "v_min_u32 v239, 72, v254\n"
      "v_cndmask_b32 v213, v213, v239, vcc\n"
      "v_subrev_u32 v239, 72, v254\n"
      "v_min_u32 v239, 72, v239\n"
      "v_cndmask_b32 v43, v43, v239, vcc\n"
      "v_mad_u32_u24 v238, v213, 7, 9\n"
      "v_subrev_u32 v239, 0x1ff, v238\n"
      "v_min_u32 v238, v238, v239\n"
      "v_mad_u32_u24 v239, v43, 7, 9\n"
      "v_subrev_u32 v254, 0x1ff, v239\n"
      "v_min_u32 v239, v239, v254\n"
      "v_mul_u32_u24 v238, 0x1ff, v238\n"
      "v_add_u32 v238, v238, v239\n"
      "v_mul_u32_u24 v239, 0x180, v238\n"
      "v_bfrev_b32 v252, 1\n"
      "v_cndmask_b32 v239, v239, v252, s[70:71]\n"
      "v_xor_b32 v255, v255, v240\n"
      "v_lshl_add_u32 v239, v255, 4, v239\n"
      "s_add_u32 m0, s64, 150784\n"
      "s_nop 0\n"
      "buffer_load_dwordx4 v239, s[16:19], 0 offen  lds\n"
      "s_add_u32 m0, s64, 154752\n"
      "s_nop 0\n"
      "buffer_load_dwordx4 v239, s[16:19], 0 offen offset:128  lds\n"
      "s_add_u32 m0, s64, 158720\n"
      "s_nop 0\n"
      "buffer_load_dwordx4 v239, s[16:19], 0 offen offset:256  lds\n"
      "s_movk_i32 s95, 504\n"
      "s_and_b32 s92, s40, 1\n"
      "s_lshl_b32 s92, s92, 2\n"
      "v_lshrrev_b32 v239, 1, v253\n"
      "v_xor_b32 v239, v239, v240\n"
      "v_xor_b32 v239, s92, v239\n"
      "v_lshlrev_b32_e64 v239, 4, v239\n"
      "s_lshl_b32 s92, s40, 3\n"
      "s_brev_b32 s43, 1\n"
      "s_cmp_lt_u32 s40, 2\n"
      "s_cselect_b32 s93, 32, -16\n"
      "s_cselect_b32 s94, 0, 128\n"
      "s_cselect_b32 s43, 256, s43\n"
      "v_add_u32 v240, s92, v253\n"
      "v_mul_u32_u24 v238, 37, v240\n"
      "v_lshrrev_b32_e64 v238, 8, v238\n"
      "v_mad_u32_u24 v238, v238, s95, v240\n"
      "v_mul_u32_u24 v238, 384, v238\n"
      "v_add_u32 v16, v238, v239\n"
      "v_add_u32 v238, s93, v240\n"
      "v_mul_u32_u24 v253, 37, v238\n"
      "v_lshrrev_b32 v253, 8, v253\n"
      "v_mad_u32_u24 v253, v253, s95, v238\n"
      "v_mul_u32_u24 v253, 384, v253\n"
      "v_add3_u32 v17, v253, v239, s94\n"
      "v_add_u32 v238, 16, v240\n"
      "v_mul_u32_u24 v240, 37, v238\n"
      "v_lshrrev_b32 v240, 8, v240\n"
      "v_mad_u32_u24 v240, v240, s95, v238\n"
      "v_mul_u32_u24 v240, 384, v240\n"
      "s_movk_i32 s94, 128\n"
      "v_add3_u32 v18, v240, v239, s94\n"
      "v_add_u32 v19, 256, v16\n"
      "v_add_u32 v15, s43, v17\n"
      "s_add_u32 s92, s41, 0\n"
      "s_mul_i32 s93, s92, 58255\n"
      "s_lshr_b32 s93, s93, 22\n"
      "s_mul_i32 s93, s93, 439\n"
      "s_add_u32 s45, s92, s93\n"
      "s_mul_i32 s45, s45, 2688\n"
      "s_add_u32 s45, s45, 589824\n"
      "s_add_u32 m0, s64, 0\n"
      "s_nop 0\n"
      "buffer_load_dwordx4 v16, s[16:19], s45 offen  lds\n"
      "s_add_u32 m0, s64, 4096\n"
      "s_nop 0\n"
      "buffer_load_dwordx4 v17, s[16:19], s45 offen  lds\n"
      "s_add_u32 m0, s64, 8192\n"
      "s_nop 0\n"
      "buffer_load_dwordx4 v18, s[16:19], s45 offen  lds\n"
      "s_add_u32 m0, s64, 12288\n"
      "s_nop 0\n"
      "buffer_load_dwordx4 v19, s[16:19], s45 offen  lds\n"
      "s_add_u32 m0, s65, 16384\n"
      "s_nop 0\n"
      "buffer_load_dwordx4 v15, s[16:19], s45 offen  lds\n"
      "s_add_u32 s92, s41, 0\n"
      "s_mul_i32 s93, s92, 58255\n"
      "s_lshr_b32 s93, s93, 22\n"
      "s_add_u32 s47, s92, s93\n"
      "s_mul_i32 s47, s47, 4802\n"
      "s_and_b32 s47, s47, -4\n"
      "s_add_u32 s92, s41, 256\n"
      "s_mul_i32 s93, s92, 58255\n"
      "s_lshr_b32 s93, s93, 22\n"
      "s_add_u32 s74, s92, s93\n"
      "s_mul_i32 s74, s74, 4802\n"
      "s_and_b32 s74, s74, -4\n"
      "s_mul_i32 s93, s93, 439\n"
      "s_add_u32 s46, s92, s93\n"
      "s_mul_i32 s46, s46, 2688\n"
      "s_add_u32 s46, s46, 589824\n"
      "v_subrev_u32 v239, 2, v8\n"
      "v_cmp_ge_u32 s[72:73], s44, v239\n"
      "v_min_u32 v239, s44, v239\n"
      "s_movk_i32 s93, 5041\n"
      "s_cmp_lt_u32 s41, 224\n"
      "s_cselect_b32 s92, 192, 0\n"
      "s_cselect_b32 s93, 1184, s93\n"
      "s_cselect_b32 s94, 18, 17\n"
      "s_add_u32 s93, s93, s41\n"
      "v_lshl_add_u32 v240, v239, 8, s41\n"
      "v_mul_u32_u24 v238, s92, v239\n"
      "v_add_u32 v238, s93, v238\n"
      "v_cmp_lt_u32 vcc, s94, v239\n"
      "v_mov_b32 v253, s96\n"
      "s_nop 0\n"
      "v_cndmask_b32 v240, v240, v238, vcc\n"
      "v_cmp_eq_u32 vcc, s44, v239\n"
      "s_nop 1\n"
      "v_cndmask_b32 v240, v240, v253, vcc\n"
      "v_subrev_u32 v253, 5184, v240\n"
      "v_cmp_le_u32 vcc, 5184, v240\n"
      "v_mul_u32_u24 v255, 58255, v240\n"
      "v_lshrrev_b32 v255, 22, v255\n"
      "v_mul_u32_u24 v252, 72, v255\n"
      "v_sub_u32 v252, v240, v252\n"
      "v_min_u32 v238, 72, v253\n"
      "v_cndmask_b32 v255, v255, v238, vcc\n"
      "v_subrev_u32 v238, 72, v253\n"
      "v_min_u32 v238, 72, v238\n"
      "v_cndmask_b32 v252, v252, v238, vcc\n"
      "v_mul_u32_u24 v38, 73, v255\n"
      "v_add_u32 v38, v38, v252\n"
      "v_mul_u32_u24 v38, 4802, v38\n"
      "v_mul_u32_u24 v239, 0x1ff, v255\n"
      "v_add_u32 v239, v239, v252\n"
      "v_mul_u32_u24 v239, 2688, v239\n"
      "v_add_u32 v239, 589824, v239\n"
      "v_cmp_eq_u32 vcc, 72, v255\n"
      "v_or_b32 v43, 0x80000000, v239\n"
      "s_nop 0\n"
      "v_cndmask_b32 v239, v239, v43, vcc\n"
      "v_cmp_eq_u32 vcc, 72, v252\n"
      "v_or_b32_e64 v43, 0x40000000, v239\n"
      "s_nop 0\n"
      "v_cndmask_b32 v239, v239, v43, vcc\n"
      "v_mov_b32 v238, 0x20000000\n"
      "v_cndmask_b32 v239, v238, v239, s[72:73]\n"
      "v_cndmask_b32 v38, v238, v38, s[72:73]\n"
      "v_mov_b32 v37, v238\n"
      "s_nop 1\n"
      "v_mov_b32_dpp v37, v239 wave_shr:1 row_mask:0xf bank_mask:0xf\n"
      "v_mov_b32 v79, 0\n"
      "v_mov_b32 v83, 0\n"
      "v_mov_b32 v87, 0\n"
      "v_mov_b32 v91, 0\n"
      "v_mov_b32 v95, 0\n"
      "s_lshl_b32 s94, s40, 2\n"
      "s_add_u32 s94, s94, 115456\n"
      "v_add_u32 v31, s94, v144\n"
      "v_and_b32 v43, 3, v8\n"
      "v_cmp_ne_u32 vcc, 0, v43\n"
      "v_mov_b32 v43, 0x3f803f80\n"
      "s_nop 0\n"
      "v_cndmask_b32_e64 v32, v43, 0, vcc\n"
      "v_mov_b32 v33, v32\n"
      "v_mov_b32 v34, v32\n"
      "v_mov_b32 v35, v32\n"
      "v_and_b32 v51, 15, v8\n"
      "v_lshrrev_b32_e64 v192, 4, v8\n"
      "v_mul_u32_u24 v51, 196, v51\n"
      "v_add_u32 v51, 124928, v51\n"
      "v_lshl_add_u32 v192, v192, 4, v51\n"
      "v_min_u32 v48, 192, v241\n"
      "v_add_u32 v48, 124928, v48\n"
      "global_load_dwordx4 v[238:241], v144, s[88:89]\n"
      "s_waitcnt lgkmcnt(0)\n"
      "s_pack_ll_b32_b16 s92, s0, s8\n"
      "s_waitcnt vmcnt(14)\n"
      "v_dot2_f32_bf16 v213, s92, v58, 0\n"
      "s_pack_hh_b32_b16 s93, s0, s8\n"
      "v_dot2_f32_bf16 v42, s93, v58, 0\n"
      "s_pack_ll_b32_b16 s94, s1, s9\n"
      "v_dot2_f32_bf16 v74, s94, v58, 0\n"
      "s_pack_hh_b32_b16 s95, s1, s9\n"
      "v_dot2_f32_bf16 v212, s95, v58, 0\n"
      "s_pack_ll_b32_b16 s92, s2, s10\n"
      "v_dot2_f32_bf16 v75, s92, v58, 0\n"
      "s_pack_hh_b32_b16 s93, s2, s10\n"
      "v_dot2_f32_bf16 v211, s93, v58, 0\n"
      "s_pack_ll_b32_b16 s94, s3, s11\n"
      "v_dot2_f32_bf16 v209, s94, v58, 0\n"
      "s_pack_hh_b32_b16 s95, s3, s11\n"
      "v_dot2_f32_bf16 v228, s95, v58, 0\n"
      "s_pack_ll_b32_b16 s92, s4, s12\n"
      "v_dot2_f32_bf16 v210, s92, v58, 0\n"
      "s_pack_hh_b32_b16 s93, s4, s12\n"
      "v_dot2_f32_bf16 v133, s93, v58, 0\n"
      "s_pack_ll_b32_b16 s94, s5, s13\n"
      "v_dot2_f32_bf16 v215, s94, v58, 0\n"
      "global_load_dwordx4 v[252:255], v144, s[88:89] offset:1024\n"
      "s_pack_hh_b32_b16 s95, s5, s13\n"
      "v_dot2_f32_bf16 v175, s95, v58, 0\n"
      "s_pack_ll_b32_b16 s92, s6, s14\n"
      "v_dot2_f32_bf16 v59, s92, v58, 0\n"
      "s_pack_hh_b32_b16 s93, s6, s14\n"
      "v_dot2_f32_bf16 v208, s93, v58, 0\n"
      "s_pack_ll_b32_b16 s94, s7, s15\n"
      "v_dot2_f32_bf16 v177, s94, v58, 0\n"
      "s_pack_hh_b32_b16 s95, s7, s15\n"
      "v_dot2_f32_bf16 v174, s95, v58, 0\n"
      "s_lshl_b32 s92, s40, 4\n"
      "s_lshr_b64 s[66:67], s[66:67], s92\n"
      "s_lshl_b32 s66, s66, 16\n"
      "v_mov_b32 v58, s66\n"
      "v_max_f32 v58, 0xc2c80000, v58\n"
      "v_min_f32 v58, 0x40930000, v58\n"
      "v_mul_f32 v58, 0x3fb8aa3b, v58\n"
      "v_exp_f32 v58, v58\n"
      "s_nop 0\n"
      "v_cvt_pk_bf16_f32 v58, v58, v58\n"
      "v_lshlrev_b32 v58, 16, v58\n"
      "v_max_f32 v213, 0, v213\n"
      "v_max_f32_e64 v42, 0, v42\n"
      "global_load_dwordx4 v[0:3], v144, s[88:89] offset:2048\n"
      "v_cvt_pk_bf16_f32 v213, v213, v42\n"
      "v_max_f32 v74, 0, v74\n"
      "v_max_f32 v212, 0, v212\n"
      "v_cvt_pk_bf16_f32 v74, v74, v212\n"
      "v_max_f32 v75, 0, v75\n"
      "v_max_f32 v211, 0, v211\n"
      "v_cvt_pk_bf16_f32 v75, v75, v211\n"
      "v_max_f32 v209, 0, v209\n"
      "v_max_f32 v228, 0, v228\n"
      "v_cvt_pk_bf16_f32 v209, v209, v228\n"
      "v_max_f32 v210, 0, v210\n"
      "v_max_f32 v133, 0, v133\n"
      "v_cvt_pk_bf16_f32 v210, v210, v133\n"
      "v_max_f32 v215, 0, v215\n"
      "v_max_f32 v175, 0, v175\n"
      "v_cvt_pk_bf16_f32 v215, v215, v175\n"
      "v_max_f32_e64 v59, 0, v59\n"
      "v_max_f32_e64 v208, 0, v208\n"
      "v_cvt_pk_bf16_f32 v59, v59, v208\n"
      "v_max_f32_e64 v177, 0, v177\n"
      "v_max_f32_e64 v174, 0, v174\n"
      "v_cvt_pk_bf16_f32 v177, v177, v174\n"
      "s_mov_b32 s42, 0\n"
      "s_add_u32 m0, s75, 0\n"
      "s_nop 0\n"
      "buffer_load_dwordx4 v135, s[20:23], s42 offen  lds\n"
      "s_pack_ll_b32_b16 s92, s48, s50\n"
      "v_dot2_f32_bf16 v174, s92, v213, 0\n"
      "s_pack_hh_b32_b16 s93, s48, s50\n"
      "v_dot2_f32_bf16 v42, s93, v213, 0\n"
      "s_pack_ll_b32_b16 s94, s49, s51\n"
      "v_dot2_f32_bf16 v212, s94, v213, 0\n"
      "s_pack_hh_b32_b16 s95, s49, s51\n"
      "v_dot2_f32_bf16 v175, s95, v213, 0\n"
      "s_pack_ll_b32_b16 s92, s52, s54\n"
      "v_dot2_f32_bf16 v174, s92, v74, v174\n"
      "s_pack_hh_b32_b16 s93, s52, s54\n"
      "v_dot2_f32_bf16 v42, s93, v74, v42\n"
      "s_pack_ll_b32_b16 s94, s53, s55\n"
      "v_dot2_f32_bf16 v212, s94, v74, v212\n"
      "s_pack_hh_b32_b16 s95, s53, s55\n"
      "v_dot2_f32_bf16 v175, s95, v74, v175\n"
      "s_pack_ll_b32_b16 s92, s56, s58\n"
      "v_dot2_f32_bf16 v174, s92, v75, v174\n"
      "s_pack_hh_b32_b16 s93, s56, s58\n"
      "v_dot2_f32_bf16 v42, s93, v75, v42\n"
      "s_pack_ll_b32_b16 s94, s57, s59\n"
      "v_dot2_f32_bf16 v212, s94, v75, v212\n"
      "s_add_u32 m0, s75, 3968\n"
      "s_nop 0\n"
      "buffer_load_dwordx4 v135, s[20:23], s42 offen offset:128  lds\n"
      "s_pack_hh_b32_b16 s95, s57, s59\n"
      "v_dot2_f32_bf16 v175, s95, v75, v175\n"
      "s_pack_ll_b32_b16 s92, s60, s62\n"
      "v_dot2_f32_bf16 v174, s92, v209, v174\n"
      "s_pack_hh_b32_b16 s93, s60, s62\n"
      "v_dot2_f32_bf16 v42, s93, v209, v42\n"
      "s_pack_ll_b32_b16 s94, s61, s63\n"
      "v_dot2_f32_bf16 v212, s94, v209, v212\n"
      "s_pack_hh_b32_b16 s95, s61, s63\n"
      "v_dot2_f32_bf16 v175, s95, v209, v175\n"
      "s_pack_ll_b32_b16 s92, s24, s26\n"
      "v_dot2_f32_bf16 v174, s92, v210, v174\n"
      "s_pack_hh_b32_b16 s93, s24, s26\n"
      "v_dot2_f32_bf16 v42, s93, v210, v42\n"
      "s_pack_ll_b32_b16 s94, s25, s27\n"
      "v_dot2_f32_bf16 v212, s94, v210, v212\n"
      "s_pack_hh_b32_b16 s95, s25, s27\n"
      "v_dot2_f32_bf16 v175, s95, v210, v175\n"
      "s_pack_ll_b32_b16 s92, s28, s30\n"
      "v_dot2_f32_bf16 v174, s92, v215, v174\n"
      "s_pack_hh_b32_b16 s93, s28, s30\n"
      "v_dot2_f32_bf16 v42, s93, v215, v42\n"
      "s_pack_ll_b32_b16 s94, s29, s31\n"
      "s_add_u32 m0, s75, 7936\n"
      "s_nop 0\n"
      "buffer_load_dwordx4 v135, s[20:23], s42 offen offset:256  lds\n"
      "v_dot2_f32_bf16 v212, s94, v215, v212\n"
      "s_pack_hh_b32_b16 s95, s29, s31\n"
      "v_dot2_f32_bf16 v175, s95, v215, v175\n"
      "s_pack_ll_b32_b16 s92, s32, s34\n"
      "v_dot2_f32_bf16 v174, s92, v59, v174\n"
      "s_pack_hh_b32_b16 s93, s32, s34\n"
      "v_dot2_f32_bf16 v42, s93, v59, v42\n"
      "s_pack_ll_b32_b16 s94, s33, s35\n"
      "v_dot2_f32_bf16 v212, s94, v59, v212\n"
      "s_pack_hh_b32_b16 s95, s33, s35\n"
      "v_dot2_f32_bf16 v175, s95, v59, v175\n"
      "s_pack_ll_b32_b16 s92, s36, s38\n"
      "v_dot2_f32_bf16 v174, s92, v177, v174\n"
      "s_pack_hh_b32_b16 s93, s36, s38\n"
      "v_dot2_f32_bf16 v42, s93, v177, v42\n"
      "s_pack_ll_b32_b16 s94, s37, s39\n"
      "v_dot2_f32_bf16 v212, s94, v177, v212\n"
      "s_pack_hh_b32_b16 s95, s37, s39\n"
      "v_dot2_f32_bf16 v175, s95, v177, v175\n"
      "s_load_dwordx2 s[28:29], s[90:91], 8\n"
      "s_load_dwordx2 s[34:35], s[90:91], 16\n"
      "s_load_dwordx2 s[24:25], s[90:91], 24\n"
      "s_add_u32 m0, s75, 11904\n"
      "s_nop 0\n"
      "buffer_load_dwordx4 v135, s[20:23], s42 offen offset:384  lds\n"
      "s_load_dwordx2 s[36:37], s[90:91], 40\n"
      "s_mov_b32 s26, 100270464\n"
      "s_mov_b32 s27, 0x20000\n"
      "s_mov_b32 s38, 73728\n"
      "s_mov_b32 s39, 0x20000\n"
      "s_mov_b32 s30, 25589860\n"
      "s_mov_b32 s31, 0x20000\n"
      "s_mov_b32 s53, 0x3fb8aa3b\n"
      "s_mov_b32 s51, 0x2a8cbccc\n"
      "s_mov_b32 s52, 0x38fba882\n"
      "s_mov_b32 s58, 0xffff0000\n"
      "s_mov_b32 s59, 0x0\n"
      "s_mov_b32 s60, 0x0\n"
      "s_mov_b32 s61, 0xffff\n"
      "s_mov_b32 s62, 0x0\n"
      "s_mov_b32 s63, 0xffff0000\n"
      "v_mov_b32 v43, 0x41f00000\n"
      "v_cmp_nle_f32 s[54:55], v58, v43\n"
      "v_mul_f32 v58, s53, v58\n"
      "s_nop 0\n"
      "v_readfirstlane_b32 s50, v58\n"
      "v_cvt_pk_bf16_f32 v174, v174, v174\n"
      "v_lshlrev_b32 v174, 16, v174\n"
      "s_add_u32 m0, s75, 15872\n"
      "s_nop 0\n"
      "buffer_load_dwordx4 v135, s[20:23], s42 offen offset:512  lds\n"
      "v_mul_f32 v174, 0xbfb8aa3b, v174\n"
      "v_exp_f32 v174, v174\n"
      "s_nop 0\n"
      "v_add_f32 v174, 1.0, v174\n"
      "v_rcp_f32 v174, v174\n"
      "s_nop 0\n"
      "v_cvt_pk_bf16_f32 v174, v174, v174\n"
      "v_lshlrev_b32 v174, 16, v174\n"
      "v_add_f32_e64 v174, -0.5, v174\n"
      "v_mul_f32 v174, 0x41b8aa3b, v174\n"
      "ds_write_b32 v214, v174\n"
      "v_cvt_pk_bf16_f32 v42, v42, v42\n"
      "v_lshlrev_b32 v42, 16, v42\n"
      "v_mul_f32 v42, 0xbfb8aa3b, v42\n"
      "v_exp_f32 v42, v42\n"
      "s_nop 0\n"
      "v_add_f32 v42, 1.0, v42\n"
      "v_rcp_f32 v42, v42\n"
      "s_nop 0\n"
      "v_cvt_pk_bf16_f32 v42, v42, v42\n"
      "v_lshlrev_b32 v42, 16, v42\n"
      "v_add_f32 v42, -0.5, v42\n"
      "v_mul_f32 v42, 0x41b8aa3b, v42\n"
      "ds_write_b32 v214, v42 offset:704\n"
      "v_cvt_pk_bf16_f32 v212, v212, v212\n"
      "v_lshlrev_b32_e64 v212, 16, v212\n"
      "s_add_u32 m0, s75, 19840\n"
      "s_nop 0\n"
      "buffer_load_dwordx4 v135, s[20:23], s42 offen offset:640  lds\n"
      "v_mul_f32 v212, 0xbfb8aa3b, v212\n"
      "v_exp_f32 v212, v212\n"
      "s_nop 0\n"
      "v_add_f32 v212, 1.0, v212\n"
      "v_rcp_f32 v212, v212\n"
      "s_nop 0\n"
      "v_cvt_pk_bf16_f32 v212, v212, v212\n"
      "v_lshlrev_b32 v212, 16, v212\n"
      "v_add_f32 v212, -0.5, v212\n"
      "v_mul_f32 v212, 0x41b8aa3b, v212\n"
      "ds_write_b32 v214, v212 offset:1408\n"
      "v_cvt_pk_bf16_f32 v175, v175, v175\n"
      "v_lshlrev_b32_e64 v175, 16, v175\n"
      "v_mul_f32 v175, 0xbfb8aa3b, v175\n"
      "v_exp_f32 v175, v175\n"
      "s_nop 0\n"
      "v_add_f32 v175, 1.0, v175\n"
      "v_rcp_f32_e64 v175, v175\n"
      "s_nop 0\n"
      "v_cvt_pk_bf16_f32 v175, v175, v175\n"
      "v_lshlrev_b32 v175, 16, v175\n"
      "v_add_f32 v175, -0.5, v175\n"
      "v_mul_f32 v175, 0x41b8aa3b, v175\n"
      "ds_write_b32 v214, v175 offset:2112\n"
      "s_mul_i32 s76, s40, 704\n"
      "s_add_u32 s76, s76, 122112\n"
      "s_mov_b32 s43, 36864\n"
      "s_add_u32 m0, s75, 24576\n"
      "s_nop 0\n"
      "buffer_load_dwordx4 v135, s[20:23], s43 offen  lds\n"
      "s_add_u32 m0, s75, 28544\n"
      "s_nop 0\n"
      "buffer_load_dwordx4 v135, s[20:23], s43 offen offset:128  lds\n"
      "s_add_u32 m0, s75, 32512\n"
      "s_nop 0\n"
      "buffer_load_dwordx4 v135, s[20:23], s43 offen offset:256  lds\n"
      "s_add_u32 m0, s75, 36480\n"
      "s_nop 0\n"
      "buffer_load_dwordx4 v135, s[20:23], s43 offen offset:384  lds\n"
      "s_add_u32 m0, s75, 40448\n"
      "s_nop 0\n"
      "buffer_load_dwordx4 v135, s[20:23], s43 offen offset:512  lds\n"
      "s_add_u32 m0, s75, 44416\n"
      "s_nop 0\n"
      "buffer_load_dwordx4 v135, s[20:23], s43 offen offset:640  lds\n"
      "s_mov_b32 s45, s46\n"
      "s_lshl_b32 s9, s68, 1\n"
      "s_add_u32 s9, s9, s40\n"
      "s_mul_i32 s9, s9, 640\n"
      "s_add_u32 s10, s98, L_cold_%=-L_ipf0_%=\n"
      "s_addc_u32 s11, s99, 0\n"
      "s_mov_b32 s12, L_cend_%=-L_cold_%=\n"
      "s_sub_u32 s9, s9, 1280\n"
      "v_mov_b32_e64 v39, v144\n"
      "s_movk_i32 s13, 80\n"
      "v_cmp_gt_u32 s[14:15], s13, v39\n"
      "v_lshl_add_u32 v39, v39, 3, s9\n"
      "s_cmp_lt_u32 s40, 2\n"
      "s_cselect_b64 s[0:1], s[16:17], s[10:11]\n"
      "s_cselect_b32 s2, s18, s12\n"
      "s_cselect_b32 s8, -1, 0\n"
      "s_cselect_b64 s[14:15], 0, s[14:15]\n"
      "s_mov_b32 s3, s19\n"
      "s_add_u32 s6, s64, 16384\n"
      "s_cmp_lt_u32 s40, 2\n"
      "s_cselect_b32 s6, s6, 122112\n"
      "s_add_u32 s7, s64, 34816\n"
      "s_cmp_lt_u32 s40, 2\n"
      "s_cselect_b32 s7, s7, 122112\n"
      "s_mov_b32 s49, s47\n"
      "s_mov_b32 s66, 0\n"
      "s_mov_b32 s67, 0\n"
      "s_mov_b32 s68, 0\n"
      "s_mov_b32 s69, 0\n"
      "s_mov_b32 s70, 0\n"
      "s_mov_b32 s71, 0\n"
      "s_mov_b32 s48, 0\n"
      "s_mov_b32 s33, 0x7fff7fff\n"
      "s_mov_b32 s42, 1\n"
      "s_mov_b32 s43, 5\n"
      "v_readlane_b32 s47, v38, 4\n"
      "s_nop 0\n"
      "s_and_b32 s47, s47, -4\n"
      "s_waitcnt vmcnt(12)\n"
      "v_add_u32 v172, 150784, v0\n"
      "v_add_u32 v171, 150784, v1\n"
      "v_add_u32 v187, 49152, v238\n"
      "v_add_u32 v186, 49152, v239\n"
      "v_add_u32 v243, 49152, v240\n"
      "v_add_u32 v78, 49152, v241\n"
      "v_add_u32 v93, 49152, v252\n"
      "s_waitcnt vmcnt(6)\n"
      "s_waitcnt lgkmcnt(0)\n"
      "s_barrier\n"
      "s_add_u32 m0, s75, 49152\n"
      "s_mov_b32 s78, 73728\n"
      "buffer_load_dwordx4 v135, s[20:23], s78 offen  lds\n"
      "s_add_u32 m0, s75, 53120\n"
      "ds_read_b128 a[228:231], v0 offset:4096\n"
      "ds_read_b64_tr_b16 a[122:123], v240 offset:12800\n"
      "ds_read_b128 a[216:219], v172\n"
      "ds_read_b128 a[224:227], v0 offset:2048\n"
      "ds_read_b128 a[220:223], v0\n"
      "buffer_load_dwordx4 v135, s[20:23], s78 offen offset:128  lds\n"
      "ds_read_b64_tr_b16 a[2:3], v238 offset:512\n"
      "ds_read_b64_tr_b16 a[26:27], v239 offset:512\n"
      "ds_read_b64_tr_b16 a[120:121], v240 offset:12288\n"
      "ds_read_b64_tr_b16 a[24:25], v239\n"
      "v_add_u32 v50, 6272, v192\n"
      "s_add_u32 m0, s75, 57088\n"
      "ds_read_b64_tr_b16 a[74:75], v238 offset:12800\n"
      "buffer_load_dwordx4 v135, s[20:23], s78 offen offset:256  lds\n"
      "ds_read_b64_tr_b16 a[72:73], v238 offset:12288\n"
      "ds_read_b64_tr_b16 a[48:49], v240\n"
      "ds_read_b64_tr_b16 a[0:1], v238\n"
      "s_add_u32 m0, s75, 61056\n"
      "ds_read_b64_tr_b16 a[96:97], v239 offset:12288\n"
      "ds_read_b128 a[236:239], v1\n"
      "s_mov_b32 s79, 110592\n"
      "buffer_load_dwordx4 v135, s[20:23], s78 offen offset:384  lds\n"
      "ds_read_b64_tr_b16 a[50:51], v240 offset:512\n"
      "s_add_u32 m0, s75, 65024\n"
      "ds_read2_b32 v[42:43], v192 offset0:2 offset1:3\n"
      "ds_read2_b32 v[74:75], v50 offset0:34 offset1:35\n"
      "ds_read_b64_tr_b16 a[98:99], v239 offset:12800\n"
      "v_add_u32 v64, 3136, v192\n"
      "s_waitcnt lgkmcnt(8)\n"
      "v_mfma_f32_16x16x32_bf16 v[212:215], a[120:123], a[220:223], 0\n"
      "buffer_load_dwordx4 v135, s[20:23], s78 offen offset:512  lds\n"
      "ds_read2_b32 v[58:59], v64 offset0:18 offset1:19\n"
      "v_mfma_f32_16x16x32_bf16 v[174:177], a[72:75], a[228:231], 0\n"
      "ds_read_b32 v222, v51 offset:6464\n"
      "s_waitcnt lgkmcnt(6)\n"
      "v_mfma_f32_16x16x32_bf16 v[208:211], a[0:3], a[220:223], 0\n"
      "ds_read_b128 a[240:243], v1 offset:2048\n"
      "s_add_u32 m0, s75, 68992\n"
      "v_mfma_f32_16x16x32_bf16 v[234:237], a[24:27], a[216:219], 0\n"
      "s_mov_b32 s83, 0x0\n"
      "buffer_load_dwordx4 v135, s[20:23], s78 offen offset:640  lds\n"
      "v_mfma_f32_16x16x32_bf16 v[182:185], a[72:75], a[224:227], 0\n"
      "ds_read2_b32 v[60:61], v64 offset0:32 offset1:33\n"
      "s_waitcnt lgkmcnt(4)\n"
      "v_mfma_f32_16x16x32_bf16 v[228:231], a[96:99], a[224:227], 0\n"
      "ds_read_b128 a[232:235], v171\n"
      "ds_read2_b32 v[44:45], v192 offset0:16 offset1:17\n"
      "v_mfma_f32_16x16x32_bf16 v[130:133], a[120:123], a[216:219], 0\n"
      "ds_read2_b32 v[40:41], v192 offset0:0 offset1:1\n"
      "global_load_dwordx4 v[4:7], v144, s[88:89] offset:3072\n"
      "s_waitcnt vmcnt(7)\n"
      "s_barrier\n"
      "ds_read_b64_tr_b16 a[100:101], v239 offset:36864\n"
      "v_mfma_f32_16x16x32_bf16 v[194:197], a[48:51], a[216:219], 0\n"
      "ds_read_b64_tr_b16 a[28:29], v239 offset:24576\n"
      "ds_read_b64_tr_b16 a[76:77], v238 offset:36864\n"
      "s_add_u32 s88, s88, 4096\n"
      "s_addc_u32 s89, s89, 0\n"
      "ds_read_b64_tr_b16 a[52:53], v240 offset:24576\n"
      "global_load_dwordx4 v[8:11], v144, s[88:89]\n"
      "ds_read_b64_tr_b16 a[54:55], v240 offset:25088\n"
      "ds_read_b64_tr_b16 a[4:5], v238 offset:24576\n"
      "ds_read_b64_tr_b16 a[126:127], v240 offset:37376\n"
      "ds_read_b64_tr_b16 a[124:125], v240 offset:36864\n"
      "ds_read_b64_tr_b16 a[6:7], v238 offset:25088\n"
      "ds_read_b64_tr_b16 a[30:31], v239 offset:25088\n"
      "ds_read_b64_tr_b16 a[78:79], v238 offset:37376\n"
      "ds_read_b64_tr_b16 a[102:103], v239 offset:37376\n"
      "v_mfma_f32_16x16x32_bf16 v[124:127], a[48:51], a[220:223], 0\n"
      "global_load_dwordx4 v[12:15], v144, s[88:89] offset:1024\n"
      "ds_read_b128 a[248:251], v172 offset:4096\n"
      "v_mfma_f32_16x16x32_bf16 v[244:247], a[24:27], a[228:231], 0\n"
      "ds_read2_b32 v[54:55], v64 offset0:2 offset1:3\n"
      "ds_read2_b32 v[72:73], v50 offset0:32 offset1:33\n"
      "v_mfma_f32_16x16x32_bf16 v[188:191], a[48:51], a[224:227], 0\n"
      "ds_read2_b32 v[56:57], v64 offset0:16 offset1:17\n"
      "v_lshl_add_u32 v42, v42, 2, s76\n"
      "v_mfma_f32_16x16x32_bf16 v[166:169], a[0:3], a[224:227], 0\n"
      "global_load_dwordx4 v[20:23], v144, s[88:89] offset:2048\n"
      "v_mfma_f32_16x16x32_bf16 v[202:205], a[120:123], a[228:231], 0\n"
      "ds_read_b128 a[244:247], v1 offset:4096\n"
      "s_add_u32 m0, s75, 73728\n"
      "ds_read2_b32 v[68:69], v50 offset0:16 offset1:17\n"
      "v_mfma_f32_16x16x32_bf16 v[248:251], a[96:99], a[220:223], 0\n"
      "s_waitcnt lgkmcnt(6)\n"
      "v_lshl_add_u32 v61, v61, 2, s76\n"
      "v_lshl_add_u32 v41, v41, 2, s76\n"
      "v_mfma_f32_16x16x32_bf16 v[224:227], a[96:99], a[228:231], 0\n"
      "ds_read_b32 v216, v51 offset:192\n"
      "global_load_dwordx4 v[24:27], v144, s[88:89] offset:3072\n"
      "ds_read_b128 a[252:255], v0 offset:6144\n"
      "v_mfma_f32_16x16x32_bf16 v[142:145], a[72:75], a[220:223], 0\n"
      "ds_read_b32 v199, v48 offset:9408\n"
      "ds_read2_b32 v[48:49], v192 offset0:32 offset1:33\n"
      "v_mfma_f32_16x16x32_bf16 v[130:133], a[124:127], a[232:235], v[130:133]\n"
      "v_lshl_add_u32 v75, v75, 2, s76\n"
      "v_lshl_add_u32 v58, v58, 2, s76\n"
      "v_mfma_f32_16x16x32_bf16 v[160:163], a[0:3], a[228:231], 0\n"
      "buffer_load_dwordx4 v135, s[20:23], s79 offen  lds\n"
      "s_mov_b32 s78, 147456\n"
      "s_add_u32 m0, s75, 77696\n"
      "v_mfma_f32_16x16x32_bf16 v[248:251], a[100:103], a[236:239], v[248:251]\n"
      "ds_read2_b32 v[66:67], v50 offset0:2 offset1:3\n"
      "v_lshl_add_u32 v45, v45, 2, s76\n"
      "v_lshl_add_u32 v40, v40, 2, s76\n"
      "v_mfma_f32_16x16x32_bf16 v[218:221], a[120:123], a[224:227], 0\n"
      "s_waitcnt lgkmcnt(3)\n"
      "v_readlane_b32 s84, v37, s43\n"
      "v_mov_b32 v114, 0xf149f2ca\n"
      "v_mfma_f32_16x16x32_bf16 v[208:211], a[4:7], a[236:239], v[208:211]\n"
      "buffer_load_dwordx4 v135, s[20:23], s79 offen offset:128  lds\n"
      "v_lshl_add_u32 v73, v73, 2, s76\n"
      "v_lshl_add_u32 v59, v59, 2, s76\n"
      "v_lshl_add_u32 v74, v74, 2, s76\n"
      "v_mfma_f32_16x16x32_bf16 v[108:111], a[24:27], a[220:223], 0\n"
      "ds_read2_b32 v[70:71], v50 offset0:18 offset1:19\n"
      "v_lshl_add_u32 v57, v57, 2, s76\n"
      "v_mfma_f32_16x16x32_bf16 v[108:111], a[28:31], a[236:239], v[108:111]\n"
      "s_add_u32 m0, s75, 81664\n"
      "ds_read_b32 v45, v45\n"
      "v_mfma_f32_16x16x32_bf16 v[174:177], a[76:79], a[244:247], v[174:177]\n"
      "buffer_load_dwordx4 v135, s[20:23], s79 offen offset:256  lds\n"
      "v_mfma_f32_16x16x32_bf16 v[178:181], a[72:75], a[216:219], 0\n"
      "ds_read2_b32 v[62:63], v64 offset0:34 offset1:35\n"
      "s_add_u32 m0, s75, 85632\n"
      "v_mfma_f32_16x16x32_bf16 v[146:149], a[0:3], a[216:219], 0\n"
      "ds_read_b128 a[220:223], v0 offset:10240\n"
      "v_lshl_add_u32 v68, v68, 2, s76\n"
      "v_mfma_f32_16x16x32_bf16 v[166:169], a[4:7], a[240:243], v[166:169]\n"
      "ds_read_b128 v[138:141], v171\n"
      "s_mov_b32 s91, 0x1ffff\n"
      "v_mfma_f32_16x16x32_bf16 v[194:197], a[52:55], a[232:235], v[194:197]\n"
      "buffer_load_dwordx4 v135, s[20:23], s79 offen offset:384  lds\n"
      "v_mfma_f32_16x16x32_bf16 v[120:123], a[96:99], a[216:219], 0\n"
      "ds_read_b128 a[216:219], v0 offset:8192\n"
      "s_add_u32 m0, s75, 89600\n"
      "v_mfma_f32_16x16x32_bf16 v[188:191], a[52:55], a[240:243], v[188:191]\n"
      "s_waitcnt vmcnt(9)\n"
      "s_barrier\n"
      "ds_read_b64_tr_b16 a[80:81], v187 offset:12288\n"
      "ds_read_b64_tr_b16 a[106:107], v186 offset:12800\n"
      "v_mfma_f32_16x16x32_bf16 v[102:105], a[48:51], a[228:231], 0\n"
      "ds_read_b64_tr_b16 a[10:11], v187 offset:512\n"
      "buffer_load_dwordx4 v135, s[20:23], s79 offen offset:512  lds\n"
      "ds_read_b64_tr_b16 a[32:33], v186\n"
      "ds_read_b64_tr_b16 a[58:59], v243 offset:512\n"
      "ds_read_b64_tr_b16 a[34:35], v186 offset:512\n"
      "s_add_u32 m0, s75, 93568\n"
      "ds_read_b64_tr_b16 a[56:57], v243\n"
      "v_mfma_f32_16x16x32_bf16 v[146:149], a[4:7], a[232:235], v[146:149]\n"
      "ds_read_b64_tr_b16 a[82:83], v187 offset:12800\n"
      "buffer_load_dwordx4 v135, s[20:23], s79 offen offset:640  lds\n"
      "ds_read_b64_tr_b16 a[130:131], v243 offset:12800\n"
      "v_mfma_f32_16x16x32_bf16 v[160:163], a[4:7], a[244:247], v[160:163]\n"
      "ds_read_b64_tr_b16 a[128:129], v243 offset:12288\n"
      "ds_read_b64_tr_b16 a[8:9], v187\n"
      "v_mfma_f32_16x16x32_bf16 v[152:155], a[24:27], a[224:227], 0\n"
      "ds_read_b64_tr_b16 a[104:105], v186 offset:12288\n"
      "s_add_u32 m0, s75, 0\n"
      "v_mfma_f32_16x16x32_bf16 v[182:185], a[76:79], a[240:243], v[182:185]\n"
      "buffer_load_dwordx4 v135, s[20:23], s78 offen  lds\n"
      "v_mfma_f32_16x16x32_bf16 v[178:181], a[76:79], a[232:235], v[178:181]\n"
      "s_add_u32 m0, s75, 3968\n"
      "ds_read2_b32 v[52:53], v64 offset0:0 offset1:1\n"
      "v_mfma_f32_16x16x32_bf16 v[228:231], a[100:103], a[240:243], v[228:231]\n"
      "ds_read2_b32 v[64:65], v50 offset0:0 offset1:1\n"
      "v_lshl_add_u32 v216, v216, 2, s76\n"
      "v_mfma_f32_16x16x32_bf16 v[212:215], a[124:127], a[236:239], v[212:215]\n"
      "ds_read_b32 v61, v61\n"
      "s_waitcnt lgkmcnt(5)\n"
      "v_mfma_f32_16x16x32_bf16 v[130:133], a[128:131], a[248:251], v[130:133]\n"
      "buffer_load_dwordx4 v135, s[20:23], s78 offen offset:128  lds\n"
      "v_readlane_b32 s85, v37, s42\n"
      "v_lshl_add_u32 v55, v55, 2, s76\n"
      "v_mfma_f32_16x16x32_bf16 v[218:221], a[124:127], a[240:243], v[218:221]\n"
      "ds_read_b128 a[228:231], v1 offset:6144\n"
      "s_mov_b32 s79, 184320\n"
      "s_add_u32 m0, s75, 7936\n"
      "v_mfma_f32_16x16x32_bf16 v[120:123], a[100:103], a[232:235], v[120:123]\n"
      "ds_read_b32 v206, v51 offset:3328\n"
      "v_lshl_add_u32 v43, v43, 2, s76\n"
      "v_mfma_f32_16x16x32_bf16 v[142:145], a[76:79], a[236:239], v[142:145]\n"
      "buffer_load_dwordx4 v135, s[20:23], s78 offen offset:256  lds\n"
      "v_mfma_f32_16x16x32_bf16 v[102:105], a[52:55], a[244:247], v[102:105]\n"
      "ds_read2_b32 v[50:51], v192 offset0:34 offset1:35\n"
      "v_lshl_add_u32 v63, v63, 2, s76\n"
      "v_mfma_f32_16x16x32_bf16 v[202:205], a[124:127], a[244:247], v[202:205]\n"
      "ds_read_b128 a[224:227], v171 offset:4096\n"
      "v_lshl_add_u32 v56, v56, 2, s76\n"
      "v_mfma_f32_16x16x32_bf16 v[244:247], a[28:31], a[244:247], v[244:247]\n"
      "s_add_u32 m0, s75, 11904\n"
      "v_lshl_add_u32 v48, v48, 2, s76\n"
      "v_lshl_add_u32 v222, v222, 2, s76\n"
      "v_mfma_f32_16x16x32_bf16 v[124:127], a[52:55], a[236:239], v[124:127]\n"
      "buffer_load_dwordx4 v135, s[20:23], s78 offen offset:384  lds\n"
      "v_mfma_f32_16x16x32_bf16 v[124:127], a[56:59], a[252:255], v[124:127]\n"
      "s_add_u32 m0, s75, 15872\n"
      "ds_read_b32 v222, v222\n"
      "v_mfma_f32_16x16x32_bf16 v[142:145], a[80:83], a[252:255], v[142:145]\n"
      "s_waitcnt lgkmcnt(3)\n"
      "v_lshl_add_u32 v206, v206, 2, s76\n"
      "v_lshl_add_u32 v54, v54, 2, s76\n"
      "v_mfma_f32_16x16x32_bf16 v[224:227], a[100:103], a[244:247], v[224:227]\n"
      "ds_read_b128 a[236:239], v1 offset:10240\n"
      "v_lshl_add_u32 v53, v53, 2, s76\n"
      "v_mfma_f32_16x16x32_bf16 v[244:247], a[32:35], a[220:223], v[244:247]\n"
      "buffer_load_dwordx4 v135, s[20:23], s78 offen offset:512  lds\n"
      "v_mfma_f32_16x16x32_bf16 v[178:181], a[80:83], a[248:251], v[178:181]\n"
      "s_add_u32 m0, s75, 19840\n"
      "v_lshl_add_u32 v70, v70, 2, s76\n"
      "v_lshl_add_u32 v69, v69, 2, s76\n"
      "v_mfma_f32_16x16x32_bf16 v[234:237], a[28:31], a[232:235], v[234:237]\n"
      "ds_read2_b32 v[46:47], v192 offset0:18 offset1:19\n"
      "v_lshl_add_u32 v52, v52, 2, s76\n"
      "v_mfma_f32_16x16x32_bf16 v[194:197], a[56:59], a[248:251], v[194:197]\n"
      "ds_read_b128 a[232:235], v1 offset:8192\n"
      "v_lshl_add_u32 v199, v199, 2, s76\n"
      "v_mfma_f32_16x16x32_bf16 v[188:191], a[56:59], a[216:219], v[188:191]\n"
      "buffer_load_dwordx4 v135, s[20:23], s78 offen offset:640  lds\n"
      "v_lshl_add_u32 v44, v44, 2, s76\n"
      "s_mov_b32 s82, 0xffff\n"
      "v_mfma_f32_16x16x32_bf16 v[202:205], a[128:131], a[220:223], v[202:205]\n"
      "ds_read_b32 v44, v44\n"
      "v_lshl_add_u32 v72, v72, 2, s76\n"
      "v_lshl_add_u32 v71, v71, 2, s76\n"
      "v_mfma_f32_16x16x32_bf16 v[224:227], a[104:107], a[220:223], v[224:227]\n"
      "s_add_u32 m0, s75, 24576\n"
      "ds_read_b128 a[244:247], v0 offset:12288\n"
      "v_mfma_f32_16x16x32_bf16 v[120:123], a[104:107], a[248:251], v[120:123]\n"
      "buffer_load_dwordx4 v135, s[20:23], s79 offen  lds\n"
      "s_add_u32 m0, s75, 28544\n"
      "v_mfma_f32_16x16x32_bf16 v[212:215], a[128:131], a[252:255], v[212:215]\n"
      "s_waitcnt vmcnt(7)\n"
      "s_barrier\n"
      "ds_read_b64_tr_b16 a[60:61], v243 offset:24576\n"
      "ds_read_b64_tr_b16 a[38:39], v186 offset:25088\n"
      "ds_read_b64_tr_b16 a[134:135], v243 offset:37376\n"
      "ds_read_b64_tr_b16 a[86:87], v187 offset:37376\n"
      "v_mfma_f32_16x16x32_bf16 v[102:105], a[56:59], a[220:223], v[102:105]\n"
      "buffer_load_dwordx4 v135, s[20:23], s79 offen offset:128  lds\n"
      "ds_read_b64_tr_b16 a[110:111], v186 offset:37376\n"
      "v_mfma_f32_16x16x32_bf16 v[234:237], a[32:35], a[248:251], v[234:237]\n"
      "ds_read_b64_tr_b16 a[132:133], v243 offset:36864\n"
      "s_add_u32 m0, s75, 32512\n"
      "ds_read_b64_tr_b16 a[108:109], v186 offset:36864\n"
      "ds_read_b64_tr_b16 a[84:85], v187 offset:36864\n"
      "ds_read_b64_tr_b16 a[12:13], v187 offset:24576\n"
      "ds_read_b64_tr_b16 a[36:37], v186 offset:24576\n"
      "v_mfma_f32_16x16x32_bf16 v[248:251], a[104:107], a[252:255], v[248:251]\n"
      "ds_read_b64_tr_b16 a[62:63], v243 offset:25088\n"
      "ds_read_b64_tr_b16 a[14:15], v187 offset:25088\n"
      "v_mfma_f32_16x16x32_bf16 v[208:211], a[8:11], a[252:255], v[208:211]\n"
      "buffer_load_dwordx4 v135, s[20:23], s79 offen offset:256  lds\n"
      "v_mfma_f32_16x16x32_bf16 v[160:163], a[8:11], a[220:223], v[160:163]\n"
      "s_mov_b32 s90, 0xffffffff\n"
      "s_add_u32 m0, s75, 36480\n"
      "v_lshl_add_u32 v62, v62, 2, s76\n"
      "v_mfma_f32_16x16x32_bf16 v[108:111], a[32:35], a[252:255], v[108:111]\n"
      "ds_read_b32 v40, v40\n"
      "v_lshl_add_u32 v60, v60, 2, s76\n"
      "v_lshl_add_u32 v65, v65, 2, s76\n"
      "v_mfma_f32_16x16x32_bf16 v[182:185], a[80:83], a[216:219], v[182:185]\n"
      "ds_read_b32 v57, v57\n"
      "v_lshl_add_u32 v64, v64, 2, s76\n"
      "v_mfma_f32_16x16x32_bf16 v[152:155], a[28:31], a[240:243], v[152:155]\n"
      "buffer_load_dwordx4 v135, s[20:23], s79 offen offset:384  lds\n"
      "v_mfma_f32_16x16x32_bf16 v[152:155], a[32:35], a[216:219], v[152:155]\n"
      "ds_read_b32 v73, v73\n"
      "s_waitcnt lgkmcnt(3)\n"
      "v_mfma_f32_16x16x32_bf16 v[152:155], a[36:39], a[232:235], v[152:155]\n"
      "ds_read_b32 v59, v59\n"
      "s_add_u32 m0, s75, 40448\n"
      "v_mfma_f32_16x16x32_bf16 v[108:111], a[36:39], a[228:231], v[108:111]\n"
      "ds_read_b32 v55, v55\n"
      "v_lshl_add_u32 v66, v66, 2, s76\n"
      "v_mfma_f32_16x16x32_bf16 v[208:211], a[12:15], a[228:231], v[208:211]\n"
      "buffer_load_dwordx4 v135, s[20:23], s79 offen offset:512  lds\n"
      "v_mfma_f32_16x16x32_bf16 v[174:177], a[80:83], a[220:223], v[174:177]\n"
      "ds_read_b32 v54, v54\n"
      "ds_read_b128 a[252:255], v0 offset:16384\n"
      "v_mfma_f32_16x16x32_bf16 v[124:127], a[60:63], a[228:231], v[124:127]\n"
      "ds_read_b32 v75, v75\n"
      "ds_read_b32 v62, v62\n"
      "s_add_u32 m0, s75, 44416\n"
      "v_mfma_f32_16x16x32_bf16 v[218:221], a[128:131], a[216:219], v[218:221]\n"
      "buffer_load_dwordx4 v135, s[20:23], s79 offen offset:640  lds\n"
      "v_mfma_f32_16x16x32_bf16 v[146:149], a[8:11], a[248:251], v[146:149]\n"
      "ds_read_b128 a[220:223], v1 offset:12288\n"
      "s_mov_b32 s78, 0\n"
      "v_mfma_f32_16x16x32_bf16 v[146:149], a[12:15], a[224:227], v[146:149]\n"
      "ds_read_b128 a[240:243], v172 offset:8192\n"
      "s_add_u32 m0, s75, 48384\n"
      "v_mfma_f32_16x16x32_bf16 v[244:247], a[36:39], a[236:239], v[244:247]\n"
      "ds_read_b32 v53, v53\n"
      "ds_read_b32 v48, v48\n"
      "v_mfma_f32_16x16x32_bf16 v[194:197], a[60:63], a[224:227], v[194:197]\n"
      "buffer_load_dwordx4 v135, s[20:23], s78 offen offset:768  lds\n"
      "v_mfma_f32_16x16x32_bf16 v[224:227], a[108:111], a[236:239], v[224:227]\n"
      "ds_read_b128 a[248:251], v0 offset:14336\n"
      "v_lshl_add_u32 v67, v67, 2, s76\n"
      "v_mfma_f32_16x16x32_bf16 v[178:181], a[84:87], a[224:227], v[178:181]\n"
      "ds_read_b32 v216, v216\n"
      "s_add_u32 m0, s75, 52352\n"
      "v_mfma_f32_16x16x32_bf16 v[218:221], a[132:135], a[232:235], v[218:221]\n"
      "ds_read_b32 v199, v199\n"
      "v_lshl_add_u32 v51, v51, 2, s76\n"
      "v_mfma_f32_16x16x32_bf16 v[182:185], a[84:87], a[232:235], v[182:185]\n"
      "buffer_load_dwordx4 v135, s[20:23], s78 offen offset:896  lds\n"
      "s_add_u32 m0, s75, 56320\n"
      "v_mfma_f32_16x16x32_bf16 v[166:169], a[8:11], a[216:219], v[166:169]\n"
      "ds_read_b32 v63, v63\n"
      "ds_read_b32 v67, v67\n"
      "v_mfma_f32_16x16x32_bf16 v[166:169], a[12:15], a[232:235], v[166:169]\n"
      "ds_read_b32 v72, v72\n"
      "ds_read_b32 v41, v41\n"
      "v_mfma_f32_16x16x32_bf16 v[228:231], a[104:107], a[216:219], v[228:231]\n"
      "buffer_load_dwordx4 v135, s[20:23], s78 offen offset:1024  lds\n"
      "v_mfma_f32_16x16x32_bf16 v[228:231], a[108:111], a[232:235], v[228:231]\n"
      "s_waitcnt vmcnt(9)\n"
      "s_barrier\n"
      "ds_read_b64_tr_b16 a[16:17], v238\n"
      "ds_read_b64_tr_b16 a[64:65], v240\n"
      "v_mfma_f32_16x16x32_bf16 v[142:145], a[84:87], a[228:231], v[142:145]\n"
      "ds_read_b64_tr_b16 a[42:43], v239 offset:512\n"
      "ds_read_b64_tr_b16 a[114:115], v239 offset:12800\n"
      "v_mfma_f32_16x16x32_bf16 v[248:251], a[108:111], a[228:231], v[248:251]\n"
      "ds_read_b64_tr_b16 a[66:67], v240 offset:512\n"
      "ds_read_b64_tr_b16 a[18:19], v238 offset:512\n"
      "v_mfma_f32_16x16x32_bf16 v[188:191], a[60:63], a[232:235], v[188:191]\n"
      "ds_read_b64_tr_b16 a[88:89], v238 offset:12288\n"
      "ds_read_b64_tr_b16 a[112:113], v239 offset:12288\n"
      "ds_read_b64_tr_b16 a[90:91], v238 offset:12800\n"
      "v_mfma_f32_16x16x32_bf16 v[234:237], a[36:39], a[224:227], v[234:237]\n"
      "ds_read_b64_tr_b16 a[138:139], v240 offset:12800\n"
      "ds_read_b64_tr_b16 a[136:137], v240 offset:12288\n"
      "v_mfma_f32_16x16x32_bf16 v[120:123], a[108:111], a[224:227], v[120:123]\n"
      "ds_read_b64_tr_b16 a[40:41], v239\n"
      "s_add_u32 m0, s75, 60672\n"
      "v_mfma_f32_16x16x32_bf16 v[202:205], a[132:135], a[236:239], v[202:205]\n"
      "s_mov_b32 s78, 36864\n"
      "ds_read_b32 v42, v42\n"
      "v_mfma_f32_16x16x32_bf16 v[160:163], a[12:15], a[236:239], v[160:163]\n"
      "buffer_load_dwordx4 v135, s[20:23], s78 offen offset:768  lds\n"
      "v_lshl_add_u32 v47, v47, 2, s76\n"
      "v_mfma_f32_16x16x32_bf16 v[174:177], a[84:87], a[236:239], v[174:177]\n"
      "ds_read_b32 v66, v66\n"
      "ds_read_b128 a[232:235], v172 offset:2048\n"
      "v_mfma_f32_16x16x32_bf16 v[212:215], a[132:135], a[228:231], v[212:215]\n"
      "s_add_u32 m0, s75, 64640\n"
      "ds_read_b128 a[216:219], v171 offset:8192\n"
      "v_mfma_f32_16x16x32_bf16 v[130:133], a[132:135], a[224:227], v[130:133]\n"
      "ds_read_b32 v60, v60\n"
      "s_waitcnt lgkmcnt(5)\n"
      "v_mfma_f32_16x16x32_bf16 v[160:163], a[16:19], a[252:255], v[160:163]\n"
      "buffer_load_dwordx4 v135, s[20:23], s78 offen offset:896  lds\n"
      "v_mfma_f32_16x16x32_bf16 v[248:251], a[112:115], a[244:247], v[248:251]\n"
      "ds_read_b32 v47, v47\n"
      "ds_read_b32 v70, v70\n"
      "v_mfma_f32_16x16x32_bf16 v[166:169], a[16:19], a[248:251], v[166:169]\n"
      "ds_read_b32 v68, v68\n"
      "v_lshl_add_u32 v49, v49, 2, s76\n"
      "v_mfma_f32_16x16x32_bf16 v[146:149], a[16:19], a[240:243], v[146:149]\n"
      "ds_read_b32 v65, v65\n"
      "s_add_u32 m0, s75, 68608\n"
      "v_mfma_f32_16x16x32_bf16 v[234:237], a[40:43], a[240:243], v[234:237]\n"
      "buffer_load_dwordx4 v135, s[20:23], s78 offen offset:1024  lds\n"
      "v_mfma_f32_16x16x32_bf16 v[174:177], a[88:91], a[252:255], v[174:177]\n"
      "ds_read_b32 v71, v71\n"
      "v_cndmask_b32 v30, v114, v199, s[90:91]\n"
      "v_mfma_f32_16x16x32_bf16 v[194:197], a[64:67], a[240:243], v[194:197]\n"
      "s_mov_b32 s79, 73728\n"
      "s_add_u32 m0, s75, 72960\n"
      "v_lshl_add_u32 v50, v50, 2, s76\n"
      "v_mfma_f32_16x16x32_bf16 v[208:211], a[16:19], a[244:247], v[208:211]\n"
      "ds_read_b32 v56, v56\n"
      "ds_read_b128 a[228:231], v1 offset:16384\n"
      "v_mfma_f32_16x16x32_bf16 v[182:185], a[88:91], a[248:251], v[182:185]\n"
      "buffer_load_dwordx4 v135, s[20:23], s79 offen offset:768  lds\n"
      "v_mfma_f32_16x16x32_bf16 v[178:181], a[88:91], a[240:243], v[178:181]\n"
      "ds_read_b32 v69, v69\n"
      "s_add_u32 m0, s75, 76928\n"
      "v_mfma_f32_16x16x32_bf16 v[212:215], a[136:139], a[244:247], v[212:215]\n"
      "ds_read_b32 v74, v74\n"
      "ds_read_b32 v206, v206\n"
      "ds_read_b32 v50, v50\n"
      "v_mfma_f32_16x16x32_bf16 v[124:127], a[64:67], a[244:247], v[124:127]\n"
      "buffer_load_dwordx4 v135, s[20:23], s79 offen offset:896  lds\n"
      "v_mfma_f32_16x16x32_bf16 v[188:191], a[64:67], a[248:251], v[188:191]\n"
      "ds_read_b32 v49, v49\n"
      "ds_read_b32 v58, v58\n"
      "v_mfma_f32_16x16x32_bf16 v[142:145], a[88:91], a[244:247], v[142:145]\n"
      "ds_read_b32 v43, v43\n"
      "v_cndmask_b32 v28, v114, v222, s[82:83]\n"
      "v_mfma_f32_16x16x32_bf16 v[108:111], a[40:43], a[244:247], v[108:111]\n"
      "s_add_u32 m0, s75, 80896\n"
      "ds_read_b128 a[224:227], v1 offset:14336\n"
      "v_mfma_f32_16x16x32_bf16 v[130:133], a[136:139], a[240:243], v[130:133]\n"
      "s_or_b32 s95, s84, s85\n"
      "ds_read_b32 v52, v52\n"
      "v_mfma_f32_16x16x32_bf16 v[120:123], a[112:115], a[240:243], v[120:123]\n"
      "buffer_load_dwordx4 v135, s[20:23], s79 offen offset:1024  lds\n"
      "v_mfma_f32_16x16x32_bf16 v[228:231], a[112:115], a[248:251], v[228:231]\n"
      "s_mov_b32 s79, 110592\n"
      "s_add_u32 m0, s75, 85248\n"
      "v_lshl_add_u32 v46, v46, 2, s76\n"
      "v_mfma_f32_16x16x32_bf16 v[244:247], a[40:43], a[252:255], v[244:247]\n"
      "s_waitcnt vmcnt(9)\n"
      "s_barrier\n"
      "ds_read_b64_tr_b16 a[20:21], v238 offset:24576\n"
      "ds_read_b64_tr_b16 a[22:23], v238 offset:25088\n"
      "v_mfma_f32_16x16x32_bf16 v[102:105], a[60:63], a[236:239], v[102:105]\n"
      "ds_read_b64_tr_b16 a[46:47], v239 offset:25088\n"
      "ds_read_b64_tr_b16 a[94:95], v238 offset:37376\n"
      "v_mfma_f32_16x16x32_bf16 v[102:105], a[64:67], a[252:255], v[102:105]\n"
      "s_mov_b32 s78, 147456\n"
      "ds_read_b64_tr_b16 a[44:45], v239 offset:24576\n"
      "ds_read_b64_tr_b16 a[92:93], v238 offset:36864\n"
      "v_mfma_f32_16x16x32_bf16 v[202:205], a[136:139], a[252:255], v[202:205]\n"
      "buffer_load_dwordx4 v135, s[20:23], s79 offen offset:768  lds\n"
      "v_mfma_f32_16x16x32_bf16 v[224:227], a[112:115], a[252:255], v[224:227]\n"
      "s_add_u32 m0, s75, 89216\n"
      "ds_read_b64_tr_b16 a[70:71], v240 offset:25088\n"
      "v_mfma_f32_16x16x32_bf16 v[152:155], a[40:43], a[248:251], v[152:155]\n"
      "ds_read_b64_tr_b16 a[118:119], v239 offset:37376\n"
      "s_waitcnt lgkmcnt(6)\n"
      "v_mfma_f32_16x16x32_bf16 v[166:169], a[20:23], a[224:227], v[166:169]\n"
      "ds_read_b64_tr_b16 a[116:117], v239 offset:36864\n"
      "v_cndmask_b32 v26, v114, v206, s[82:83]\n"
      "v_mfma_f32_16x16x32_bf16 v[160:163], a[20:23], a[228:231], v[160:163]\n"
      "buffer_load_dwordx4 v135, s[20:23], s79 offen offset:896  lds\n"
      "v_cndmask_b32 v24, v114, v216, s[82:83]\n"
      "v_mfma_f32_16x16x32_bf16 v[208:211], a[20:23], a[220:223], v[208:211]\n"
      "ds_read_b64_tr_b16 a[140:141], v240 offset:36864\n"
      "s_waitcnt lgkmcnt(4)\n"
      "v_mfma_f32_16x16x32_bf16 v[142:145], a[92:95], a[220:223], v[142:145]\n"
      "ds_read_b64_tr_b16 a[142:143], v240 offset:37376\n"
      "s_add_u32 m0, s75, 93184\n"
      "v_mfma_f32_16x16x32_bf16 v[178:181], a[92:95], a[216:219], v[178:181]\n"
      "ds_read_b64_tr_b16 a[68:69], v240 offset:24576\n"
      "v_fma_f32 v222, v160, v160, s51\n"
      "v_mfma_f32_16x16x32_bf16 v[146:149], a[20:23], a[216:219], v[146:149]\n"
      "buffer_load_dwordx4 v135, s[20:23], s79 offen offset:1024  lds\n"
      "v_mfma_f32_16x16x32_bf16 v[108:111], a[44:47], a[220:223], v[108:111]\n"
      "ds_read_b128 a[240:243], v172 offset:6144\n"
      "v_fma_f32 v216, v208, v208, s51\n"
      "v_mfma_f32_16x16x32_bf16 v[174:177], a[92:95], a[228:231], v[174:177]\n"
      "s_add_u32 m0, s75, -768\n"
      "v_fma_f32 v206, v166, v166, s51\n"
      "v_fma_f32 v192, v178, v178, s51\n"
      "v_mfma_f32_16x16x32_bf16 v[234:237], a[44:47], a[216:219], v[234:237]\n"
      "ds_read_b128 a[252:255], v171 offset:10240\n"
      "v_mul_f32_e64 v199, v143, v143\n"
      "v_mfma_f32_16x16x32_bf16 v[112:115], a[0:3], a[232:235], 0\n"
      "buffer_load_dwordx4 v135, s[20:23], s78 offen offset:768  lds\n"
      "v_mul_f32_e64 v207, v167, v167\n"
      "v_mfma_f32_16x16x32_bf16 v[218:221], a[136:139], a[248:251], v[218:221]\n"
      "s_add_u32 m0, s75, 3200\n"
      "v_mul_f32 v129, v147, v147\n"
      "s_waitcnt lgkmcnt(2)\n"
      "v_mfma_f32_16x16x32_bf16 v[130:133], a[140:143], a[216:219], v[130:133]\n"
      "ds_read_b32 v64, v64\n"
      "v_fma_f32 v128, v146, v146, s51\n"
      "v_mfma_f32_16x16x32_bf16 v[248:251], a[116:119], a[220:223], v[248:251]\n"
      "ds_read_b32 v46, v46\n"
      "v_mul_f32_e64 v193, v179, v179\n"
      "v_mfma_f32_16x16x32_bf16 v[182:185], a[92:95], a[224:227], v[182:185]\n"
      "buffer_load_dwordx4 v135, s[20:23], s78 offen offset:896  lds\n"
      "v_mul_f32 v217, v209, v209\n"
      "v_fma_f32 v198, v142, v142, s51\n"
      "v_pk_fma_f32 v[198:199], v[144:145], v[144:145], v[198:199]\n"
      "v_pk_fma_f32 v[192:193], v[180:181], v[180:181], v[192:193]\n"
      "v_mfma_f32_16x16x32_bf16 v[120:123], a[116:119], a[216:219], v[120:123]\n"
      "s_lshl_b32 s92, s40, 6\n"
      "ds_read_b32 v51, v51\n"
      "v_mfma_f32_16x16x32_bf16 v[202:205], a[140:143], a[228:231], v[202:205]\n"
      "ds_read_b128 a[244:247], v171 offset:6144\n"
      "s_add_u32 m0, s75, 7168\n"
      "v_mfma_f32_16x16x32_bf16 v[224:227], a[116:119], a[228:231], v[224:227]\n"
      "buffer_load_dwordx4 v135, s[20:23], s78 offen offset:1024  lds\n"
      "s_add_u32 m0, s75, 11520\n"
      "v_mul_f32_e64 v223, v161, v161\n"
      "v_mfma_f32_16x16x32_bf16 v[194:197], a[68:71], a[216:219], v[194:197]\n"
      "s_mov_b32 s78, 184320\n"
      "v_fma_f32 v76, v174, v174, s51\n"
      "v_fma_f32 v84, v182, v182, s51\n"
      "v_mfma_f32_16x16x32_bf16 v[124:127], a[68:71], a[220:223], v[124:127]\n"
      "ds_read_b128 a[236:239], v171 offset:2048\n"
      "v_mul_f32_e64 v85, v183, v183\n"
      "v_mfma_f32_16x16x32_bf16 v[212:215], a[140:143], a[220:223], v[212:215]\n"
      "ds_read_b128 a[248:251], v172 offset:10240\n"
      "buffer_load_dwordx4 v135, s[20:23], s78 offen offset:768  lds\n"
      "v_pk_fma_f32 v[192:193], v[120:121], v[120:121], v[192:193]\n"
      "v_pk_fma_f32 v[128:129], v[148:149], v[148:149], v[128:129]\n"
      "v_mul_f32_e64 v77, v175, v175\n"
      "v_pk_fma_f32 v[128:129], v[234:235], v[234:235], v[128:129]\n"
      "v_pk_fma_f32 v[216:217], v[210:211], v[210:211], v[216:217]\n"
      "v_pk_fma_f32 v[198:199], v[248:249], v[248:249], v[198:199]\n"
      "v_pk_fma_f32 v[128:129], v[236:237], v[236:237], v[128:129]\n"
      "v_pk_fma_f32 v[198:199], v[250:251], v[250:251], v[198:199]\n"
      "v_pk_fma_f32 v[192:193], v[122:123], v[122:123], v[192:193]\n"
      "v_pk_fma_f32 v[192:193], v[130:131], v[130:131], v[192:193]\n"
      "v_pk_fma_f32 v[216:217], v[108:109], v[108:109], v[216:217]\n"
      "v_pk_fma_f32 v[198:199], v[212:213], v[212:213], v[198:199]\n"
      "v_pk_fma_f32 v[198:199], v[214:215], v[214:215], v[198:199]\n"
      "v_pk_fma_f32 v[84:85], v[184:185], v[184:185], v[84:85]\n"
      "s_add_u32 m0, s75, 15488\n"
      "v_pk_fma_f32 v[76:77], v[176:177], v[176:177], v[76:77]\n"
      "v_pk_fma_f32 v[216:217], v[110:111], v[110:111], v[216:217]\n"
      "v_pk_fma_f32 v[206:207], v[168:169], v[168:169], v[206:207]\n"
      "v_pk_fma_f32 v[222:223], v[162:163], v[162:163], v[222:223]\n"
      "v_pk_fma_f32 v[216:217], v[124:125], v[124:125], v[216:217]\n"
      "v_pk_fma_f32 v[216:217], v[126:127], v[126:127], v[216:217]\n"
      "v_pk_fma_f32 v[192:193], v[132:133], v[132:133], v[192:193]\n"
      "v_pk_fma_f32 v[128:129], v[194:195], v[194:195], v[128:129]\n"
      "v_pk_fma_f32 v[76:77], v[224:225], v[224:225], v[76:77]\n"
      "v_pk_fma_f32 v[128:129], v[196:197], v[196:197], v[128:129]\n"
      "v_mfma_f32_16x16x32_bf16 v[228:231], a[116:119], a[224:227], v[228:231]\n"
      "buffer_load_dwordx4 v135, s[20:23], s78 offen offset:896  lds\n"
      "v_add_f32 v198, v198, v199\n"
      "s_waitcnt lgkmcnt(0)\n"
      "v_mfma_f32_16x16x32_bf16 v[112:115], a[4:7], a[236:239], v[112:115]\n"
      "s_add_u32 m0, s75, 19456\n"
      "v_add_f32 v192, v192, v193\n"
      "v_add_f32 v128, v128, v129\n"
      "v_mfma_f32_16x16x32_bf16 v[102:105], a[68:71], a[228:231], v[102:105]\n"
      "s_waitcnt vmcnt(11)\n"
      "s_barrier\n"
      "ds_read_b64_tr_b16 a[174:175], v186 offset:12800\n"
      "ds_read_b64_tr_b16 a[172:173], v186 offset:12288\n"
      "v_permlane16_swap_b32 v128, v192\n"
      "v_add_f32 v216, v216, v217\n"
      "v_add_f32 v128, v128, v192\n"
      "v_mfma_f32_16x16x32_bf16 v[96:99], a[72:75], a[232:235], 0\n"
      "ds_read_b64_tr_b16 a[198:199], v243 offset:12800\n"
      "v_mov_b32 v192, v128\n"
      "v_mfma_f32_16x16x32_bf16 v[96:99], a[76:79], a[236:239], v[96:99]\n"
      "ds_read_b64_tr_b16 a[170:171], v186 offset:512\n"
      "v_permlane32_swap_b32_e64 v128, v192\n"
      "v_mfma_f32_16x16x32_bf16 v[96:99], a[80:83], a[240:243], v[96:99]\n"
      "buffer_load_dwordx4 v135, s[20:23], s78 offen offset:1024  lds\n"
      "s_add_u32 m0, s75, 24576\n"
      "v_permlane16_swap_b32_e64 v216, v198\n"
      "v_pk_fma_f32 v[84:85], v[228:229], v[228:229], v[84:85]\n"
      "v_pk_fma_f32 v[76:77], v[226:227], v[226:227], v[76:77]\n"
      "v_mfma_f32_16x16x32_bf16 v[96:99], a[84:87], a[244:247], v[96:99]\n"
      "ds_read_b64_tr_b16 a[150:151], v187 offset:12800\n"
      "v_add_f32 v216, v216, v198\n"
      "v_mov_b32 v198, v216\n"
      "v_mfma_f32_16x16x32_bf16 v[218:221], a[140:143], a[224:227], v[218:221]\n"
      "ds_read_b64_tr_b16 a[168:169], v186\n"
      "ds_read_b64_tr_b16 a[146:147], v187 offset:512\n"
      "v_add_f32_e64 v128, v128, v192\n"
      "v_mfma_f32_16x16x32_bf16 v[188:191], a[68:71], a[224:227], v[188:191]\n"
      "ds_read_b64_tr_b16 a[144:145], v187\n"
      "v_permlane32_swap_b32 v216, v198\n"
      "v_mfma_f32_16x16x32_bf16 v[152:155], a[44:47], a[224:227], v[152:155]\n"
      "ds_read_b64_tr_b16 a[196:197], v243 offset:12288\n"
      "s_mov_b32 s79, 0\n"
      "v_mfma_f32_16x16x32_bf16 v[244:247], a[44:47], a[228:231], v[244:247]\n"
      "ds_read_b64_tr_b16 a[194:195], v243 offset:512\n"
      "ds_read_b64_tr_b16 a[148:149], v187 offset:12288\n"
      "ds_read_b64_tr_b16 a[192:193], v243\n"
      "v_mfma_f32_16x16x32_bf16 v[134:137], a[48:51], a[232:235], 0\n"
      "buffer_load_dwordx4 v255, s[36:39], s79 offen  lds\n"
      "v_rsq_f32_e64 v128, v128\n"
      "v_pk_fma_f32 v[206:207], v[152:153], v[152:153], v[206:207]\n"
      "v_pk_fma_f32 v[76:77], v[202:203], v[202:203], v[76:77]\n"
      "v_pk_fma_f32 v[76:77], v[204:205], v[204:205], v[76:77]\n"
      "v_pk_fma_f32 v[206:207], v[154:155], v[154:155], v[206:207]\n"
      "v_pk_fma_f32 v[222:223], v[244:245], v[244:245], v[222:223]\n"
      "s_add_u32 m0, s75, 28544\n"
      "v_mov_b32 v192, v128\n"
      "v_pk_fma_f32 v[84:85], v[230:231], v[230:231], v[84:85]\n"
      "v_pk_fma_f32 v[222:223], v[246:247], v[246:247], v[222:223]\n"
      "v_add_f32 v76, v76, v77\n"
      "v_mfma_f32_16x16x32_bf16 v[112:115], a[8:11], a[240:243], v[112:115]\n"
      "buffer_load_dwordx4 v255, s[36:39], s79 offen offset:128  lds\n"
      "v_permlane16_swap_b32 v128, v192\n"
      "v_pk_fma_f32 v[84:85], v[218:219], v[218:219], v[84:85]\n"
      "v_mul_f32 v128, s50, v128\n"
      "v_pk_fma_f32 v[84:85], v[220:221], v[220:221], v[84:85]\n"
      "v_pk_mul_f32 v[194:195], v[194:195], v[128:129] op_sel_hi:[1,0]\n"
      "v_pk_mul_f32 v[178:179], v[178:179], v[192:193] op_sel_hi:[1,0]\n"
      "v_pk_fma_f32 v[206:207], v[188:189], v[188:189], v[206:207]\n"
      "v_add_f32 v84, v84, v85\n"
      "v_pk_mul_f32 v[146:147], v[146:147], v[128:129] op_sel_hi:[1,0]\n"
      "v_pk_fma_f32 v[222:223], v[102:103], v[102:103], v[222:223]\n"
      "v_pk_fma_f32 v[206:207], v[190:191], v[190:191], v[206:207]\n"
      "v_add_f32 v216, v216, v198\n"
      "v_mfma_f32_16x16x32_bf16 v[112:115], a[12:15], a[244:247], v[112:115]\n"
      "s_add_u32 m0, s75, 32512\n"
      "v_rsq_f32 v216, v216\n"
      "v_mfma_f32_16x16x32_bf16 v[156:159], a[24:27], a[232:235], 0\n"
      "s_waitcnt lgkmcnt(0)\n"
      "v_add_f32 v206, v206, v207\n"
      "v_mov_b32 v198, v216\n"
      "v_mfma_f32_16x16x32_bf16 v[156:159], a[28:31], a[236:239], v[156:159]\n"
      "buffer_load_dwordx4 v255, s[36:39], s79 offen offset:256  lds\n"
      "v_permlane16_swap_b32_e64 v216, v198\n"
      "v_pk_fma_f32 v[222:223], v[104:105], v[104:105], v[222:223]\n"
      "v_add_f32 v222, v222, v223\n"
      "v_pk_mul_f32 v[214:215], v[214:215], v[198:199] op_sel_hi:[1,0]\n"
      "v_permlane16_swap_b32 v206, v84\n"
      "v_pk_mul_f32 v[120:121], v[120:121], v[192:193] op_sel_hi:[1,0]\n"
      "s_add_u32 m0, s75, 36864\n"
      "v_pk_mul_f32 v[196:197], v[196:197], v[128:129] op_sel_hi:[1,0]\n"
      "s_mov_b32 s79, 12288\n"
      "v_mfma_f32_16x16x32_bf16 v[96:99], a[88:91], a[248:251], v[96:99]\n"
      "buffer_load_dwordx4 v255, s[36:39], s79 offen  lds\n"
      "v_mul_f32_e64 v216, s50, v216\n"
      "v_mfma_f32_16x16x32_bf16 v[96:99], a[92:95], a[252:255], v[96:99]\n"
      "s_add_u32 m0, s75, 40832\n"
      "v_cvt_pk_bf16_f32 v194, v194, v195\n"
      "v_cvt_pk_bf16_f32 v195, v196, v197\n"
      "v_mfma_f32_16x16x32_bf16 v[156:159], a[32:35], a[240:243], v[156:159]\n"
      "ds_write_b64 v253, v[194:195] offset:16\n"
      "v_add_f32_e64 v206, v206, v84\n"
      "v_cvt_pk_bf16_f32 v178, v178, v179\n"
      "v_pk_mul_f32 v[180:181], v[180:181], v[192:193] op_sel_hi:[1,0]\n"
      "v_pk_mul_f32 v[122:123], v[122:123], v[192:193] op_sel_hi:[1,0]\n"
      "v_pk_mul_f32 v[130:131], v[130:131], v[192:193] op_sel_hi:[1,0]\n"
      "v_permlane16_swap_b32 v222, v76\n"
      "v_add_f32 v222, v222, v76\n"
      "v_pk_mul_f32 v[234:235], v[234:235], v[128:129] op_sel_hi:[1,0]\n"
      "v_pk_mul_f32 v[148:149], v[148:149], v[128:129] op_sel_hi:[1,0]\n"
      "v_mov_b32 v84, v206\n"
      "v_cvt_pk_bf16_f32 v146, v146, v147\n"
      "v_pk_mul_f32 v[124:125], v[124:125], v[216:217] op_sel_hi:[1,0]\n"
      "v_mov_b32 v76, v222\n"
      "v_pk_mul_f32 v[132:133], v[132:133], v[192:193] op_sel_hi:[1,0]\n"
      "v_pk_mul_f32 v[108:109], v[108:109], v[216:217] op_sel_hi:[1,0]\n"
      "v_mul_f32_e64 v81, v97, v97\n"
      "v_pk_mul_f32 v[142:143], v[142:143], v[198:199] op_sel_hi:[1,0]\n"
      "v_pk_mul_f32 v[212:213], v[212:213], v[198:199] op_sel_hi:[1,0]\n"
      "v_pk_mul_f32 v[248:249], v[248:249], v[198:199] op_sel_hi:[1,0]\n"
      "v_pk_mul_f32 v[208:209], v[208:209], v[216:217] op_sel_hi:[1,0]\n"
      "v_pk_mul_f32 v[144:145], v[144:145], v[198:199] op_sel_hi:[1,0]\n"
      "v_pk_mul_f32 v[250:251], v[250:251], v[198:199] op_sel_hi:[1,0]\n"
      "v_mfma_f32_16x16x32_bf16 v[156:159], a[36:39], a[244:247], v[156:159]\n"
      "buffer_load_dwordx4 v255, s[36:39], s79 offen offset:128  lds\n"
      "v_cvt_pk_bf16_f32 v130, v130, v131\n"
      "v_mfma_f32_16x16x32_bf16 v[134:137], a[52:55], a[236:239], v[134:137]\n"
      "s_add_u32 m0, s75, 44800\n"
      "v_cvt_pk_bf16_f32 v147, v148, v149\n"
      "v_cvt_pk_bf16_f32 v131, v132, v133\n"
      "v_mfma_f32_16x16x32_bf16 v[134:137], a[56:59], a[240:243], v[134:137]\n"
      "s_waitcnt vmcnt(11)\n"
      "s_barrier\n"
      "v_cvt_pk_bf16_f32 v212, v212, v213\n"
      "v_mfma_f32_16x16x32_bf16 v[134:137], a[60:63], a[244:247], v[134:137]\n"
      "ds_read_b64_tr_b16 a[154:155], v187 offset:25088\n"
      "buffer_load_dwordx4 v255, s[36:39], s79 offen offset:256  lds\n"
      "v_pk_mul_f32 v[210:211], v[210:211], v[216:217] op_sel_hi:[1,0]\n"
      "v_mfma_f32_16x16x32_bf16 v[112:115], a[16:19], a[248:251], v[112:115]\n"
      "ds_read_b64_tr_b16 a[180:181], v186 offset:36864\n"
      "v_cvt_pk_bf16_f32 v142, v142, v143\n"
      "v_cvt_pk_bf16_f32 v208, v208, v209\n"
      "v_mfma_f32_16x16x32_bf16 v[112:115], a[20:23], a[252:255], v[112:115]\n"
      "ds_read_b64_tr_b16 a[176:177], v186 offset:24576\n"
      "ds_read_b64_tr_b16 a[202:203], v243 offset:25088\n"
      "v_mfma_f32_16x16x32_bf16 v[134:137], a[64:67], a[248:251], v[134:137]\n"
      "s_mov_b32 s78, 24576\n"
      "ds_read_b64_tr_b16 a[156:157], v187 offset:36864\n"
      "v_cvt_pk_bf16_f32 v213, v214, v215\n"
      "v_mfma_f32_16x16x32_bf16 v[192:195], a[120:123], a[232:235], 0\n"
      "s_add_u32 m0, s75, 49152\n"
      "ds_read_b64_tr_b16 a[182:183], v186 offset:37376\n"
      "ds_read_b64_tr_b16 a[200:201], v243 offset:24576\n"
      "ds_read_b64_tr_b16 a[206:207], v243 offset:37376\n"
      "v_fma_f32 v80, v96, v96, s51\n"
      "v_pk_mul_f32 v[236:237], v[236:237], v[128:129] op_sel_hi:[1,0]\n"
      "v_pk_mul_f32 v[110:111], v[110:111], v[216:217] op_sel_hi:[1,0]\n"
      "v_mfma_f32_16x16x32_bf16 v[192:195], a[124:127], a[236:239], v[192:195]\n"
      "ds_read_b64_tr_b16 a[204:205], v243 offset:36864\n"
      "ds_read_b64_tr_b16 a[158:159], v187 offset:37376\n"
      "ds_read_b64_tr_b16 a[152:153], v187 offset:24576\n"
      "v_mfma_f32_16x16x32_bf16 v[192:195], a[128:131], a[240:243], v[192:195]\n"
      "ds_read_b64_tr_b16 a[178:179], v186 offset:25088\n"
      "v_cvt_pk_bf16_f32 v148, v234, v235\n"
      "v_fma_f32 v186, v112, v112, s51\n"
      "v_cvt_pk_bf16_f32 v149, v236, v237\n"
      "v_cvt_pk_bf16_f32 v143, v144, v145\n"
      "v_mfma_f32_16x16x32_bf16 v[156:159], a[40:43], a[248:251], v[156:159]\n"
      "buffer_load_dwordx4 v255, s[36:39], s78 offen  lds\n"
      "v_cvt_pk_bf16_f32 v179, v180, v181\n"
      "v_mfma_f32_16x16x32_bf16 v[156:159], a[44:47], a[252:255], v[156:159]\n"
      "ds_read_b128 v[234:237], v172 offset:4096\n"
      "v_cvt_pk_bf16_f32 v144, v248, v249\n"
      "v_mfma_f32_16x16x32_bf16 v[192:195], a[132:135], a[244:247], v[192:195]\n"
      "ds_write_b128 v253, v[146:149]\n"
      "v_permlane32_swap_b32_e64 v206, v84\n"
      "v_pk_mul_f32 v[126:127], v[126:127], v[216:217] op_sel_hi:[1,0]\n"
      "s_add_u32 m0, s75, 53120\n"
      "v_mfma_f32_16x16x32_bf16 v[146:149], a[96:99], a[232:235], 0\n"
      "buffer_load_dwordx4 v255, s[36:39], s78 offen offset:128  lds\n"
      "v_mfma_f32_16x16x32_bf16 v[146:149], a[100:103], a[236:239], v[146:149]\n"
      "ds_read_b128 v[214:217], v171 offset:4096\n"
      "v_add_f32_e64 v206, v206, v84\n"
      "v_mfma_f32_16x16x32_bf16 v[146:149], a[104:107], a[240:243], v[146:149]\n"
      "ds_read_b128 v[196:199], v172\n"
      "v_cvt_pk_bf16_f32 v145, v250, v251\n"
      "v_mfma_f32_16x16x32_bf16 v[192:195], a[136:139], a[248:251], v[192:195]\n"
      "s_add_u32 m0, s75, 57088\n"
      "v_permlane32_swap_b32 v222, v76\n"
      "v_mul_f32 v187, v113, v113\n"
      "v_mfma_f32_16x16x32_bf16 v[192:195], a[140:143], a[252:255], v[192:195]\n"
      "ds_write_b64 v253, v[130:131] offset:24\n"
      "v_cvt_pk_bf16_f32 v124, v124, v125\n"
      "v_mfma_f32_16x16x32_bf16 v[146:149], a[108:111], a[244:247], v[146:149]\n"
      "buffer_load_dwordx4 v255, s[36:39], s78 offen offset:256  lds\n"
      "v_cvt_pk_bf16_f32 v180, v120, v121\n"
      "v_add_f32 v222, v222, v76\n"
      "v_pk_fma_f32 v[186:187], v[114:115], v[114:115], v[186:187]\n"
      "v_mfma_f32_16x16x32_bf16 v[146:149], a[112:115], a[248:251], v[146:149]\n"
      "s_mov_b32 s78, 36864\n"
      "v_rsq_f32 v222, v222\n"
      "v_mfma_f32_16x16x32_bf16 v[146:149], a[116:119], a[252:255], v[146:149]\n"
      "s_add_u32 m0, s75, 61440\n"
      "v_mov_b32 v76, v222\n"
      "s_waitcnt lgkmcnt(1)\n"
      "v_mfma_f32_16x16x32_bf16 v[130:133], a[168:171], v[196:199], 0\n"
      "buffer_load_dwordx4 v255, s[36:39], s78 offen  lds\n"
      "v_permlane16_swap_b32 v222, v76\n"
      "v_pk_fma_f32 v[80:81], v[98:99], v[98:99], v[80:81]\n"
      "v_mfma_f32_16x16x32_bf16 v[130:133], a[172:175], v[138:141], v[130:133]\n"
      "s_add_u32 m0, s75, 65408\n"
      "v_mul_f32 v222, s50, v222\n"
      "v_cvt_pk_bf16_f32 v181, v122, v123\n"
      "v_rsq_f32 v206, v206\n"
      "v_cvt_pk_bf16_f32 v125, v126, v127\n"
      "v_mov_b32 v84, v206\n"
      "v_mfma_f32_16x16x32_bf16 v[248:251], a[144:147], v[196:199], 0\n"
      "buffer_load_dwordx4 v255, s[36:39], s78 offen offset:128  lds\n"
      "v_mfma_f32_16x16x32_bf16 v[248:251], a[148:151], v[138:141], v[248:251]\n"
      "ds_write_b128 v253, v[178:181] offset:32\n"
      "v_pk_mul_f32 v[244:245], v[244:245], v[222:223] op_sel_hi:[1,0]\n"
      "v_pk_mul_f32 v[204:205], v[204:205], v[76:77] op_sel_hi:[1,0]\n"
      "v_permlane16_swap_b32_e64 v206, v84\n"
      "s_add_u32 m0, s75, 69376\n"
      "v_pk_mul_f32 v[162:163], v[162:163], v[222:223] op_sel_hi:[1,0]\n"
      "v_pk_fma_f32 v[186:187], v[156:157], v[156:157], v[186:187]\n"
      "v_pk_mul_f32 v[202:203], v[202:203], v[76:77] op_sel_hi:[1,0]\n"
      "v_mfma_f32_16x16x32_bf16 v[248:251], a[152:155], v[234:237], v[248:251]\n"
      "buffer_load_dwordx4 v255, s[36:39], s78 offen offset:256  lds\n"
      "v_mfma_f32_16x16x32_bf16 v[248:251], a[156:159], v[214:217], v[248:251]\n"
      "s_waitcnt vmcnt(12)\n"
      "s_barrier\n"
      "s_add_u32 m0, s75, 73728\n"
      "v_mul_f32_e64 v206, s50, v206\n"
      "v_mfma_f32_16x16x32_bf16 v[134:137], a[68:71], a[252:255], v[134:137]\n"
      "ds_read_b64_tr_b16 a[186:187], v239 offset:512\n"
      "ds_read_b64_tr_b16 a[166:167], v238 offset:12800\n"
      "ds_read_b64_tr_b16 a[184:185], v239\n"
      "s_mov_b32 s79, 49152\n"
      "ds_read_b64_tr_b16 a[214:215], v240 offset:12800\n"
      "ds_read_b64_tr_b16 a[160:161], v238\n"
      "ds_read_b64_tr_b16 a[190:191], v239 offset:12800\n"
      "v_pk_mul_f32 v[104:105], v[104:105], v[222:223] op_sel_hi:[1,0]\n"
      "v_pk_mul_f32 v[246:247], v[246:247], v[222:223] op_sel_hi:[1,0]\n"
      "v_mfma_f32_16x16x32_bf16 v[130:133], a[176:179], v[234:237], v[130:133]\n"
      "ds_read_b64_tr_b16 a[188:189], v239 offset:12288\n"
      "ds_read_b64_tr_b16 a[208:209], v240\n"
      "v_mfma_f32_16x16x32_bf16 v[126:129], a[192:195], v[196:199], 0\n"
      "ds_read_b64_tr_b16 a[212:213], v240 offset:12288\n"
      "ds_read_b64_tr_b16 a[164:165], v238 offset:12288\n"
      "ds_read_b64_tr_b16 a[162:163], v238 offset:512\n"
      "ds_read_b64_tr_b16 a[210:211], v240 offset:512\n"
      "v_pk_mul_f32 v[102:103], v[102:103], v[222:223] op_sel_hi:[1,0]\n"
      "v_pk_mul_f32 v[166:167], v[166:167], v[206:207] op_sel_hi:[1,0]\n"
      "v_mfma_f32_16x16x32_bf16 v[126:129], a[196:199], v[138:141], v[126:129]\n"
      "ds_read_b128 v[196:199], v171 offset:8192\n"
      "ds_read_b128 v[138:141], v172 offset:2048\n"
      "v_mfma_f32_16x16x32_bf16 v[126:129], a[200:203], v[234:237], v[126:129]\n"
      "buffer_load_dwordx4 v255, s[36:39], s79 offen  lds\n"
      "ds_read_b128 v[234:237], v172 offset:8192\n"
      "v_pk_fma_f32 v[186:187], v[158:159], v[158:159], v[186:187]\n"
      "s_add_u32 m0, s75, 77696\n"
      "v_pk_fma_f32 v[186:187], v[134:135], v[134:135], v[186:187]\n"
      "v_pk_mul_f32 v[152:153], v[152:153], v[206:207] op_sel_hi:[1,0]\n"
      "v_pk_mul_f32 v[190:191], v[190:191], v[206:207] op_sel_hi:[1,0]\n"
      "v_pk_fma_f32 v[80:81], v[146:147], v[146:147], v[80:81]\n"
      "v_cvt_pk_bf16_f32 v209, v210, v211\n"
      "v_pk_mul_f32 v[224:225], v[224:225], v[76:77] op_sel_hi:[1,0]\n"
      "v_pk_mul_f32 v[220:221], v[220:221], v[84:85] op_sel_hi:[1,0]\n"
      "v_pk_mul_f32 v[160:161], v[160:161], v[222:223] op_sel_hi:[1,0]\n"
      "v_pk_fma_f32 v[80:81], v[148:149], v[148:149], v[80:81]\n"
      "v_cvt_pk_bf16_f32 v211, v110, v111\n"
      "v_pk_fma_f32 v[80:81], v[192:193], v[192:193], v[80:81]\n"
      "v_pk_fma_f32 v[186:187], v[136:137], v[136:137], v[186:187]\n"
      "v_pk_mul_f32 v[218:219], v[218:219], v[84:85] op_sel_hi:[1,0]\n"
      "v_cvt_pk_bf16_f32 v160, v160, v161\n"
      "v_cvt_pk_bf16_f32 v161, v162, v163\n"
      "v_pk_fma_f32 v[80:81], v[194:195], v[194:195], v[80:81]\n"
      "v_mfma_f32_16x16x32_bf16 v[126:129], a[204:207], v[214:217], v[126:129]\n"
      "ds_read_b128 v[178:181], v171 offset:2048\n"
      "v_cvt_pk_bf16_f32 v218, v218, v219\n"
      "v_add_f32_e64 v186, v186, v187\n"
      "v_mfma_f32_16x16x32_bf16 v[130:133], a[180:183], v[214:217], v[130:133]\n"
      "buffer_load_dwordx4 v255, s[36:39], s79 offen offset:128  lds\n"
      "s_add_u32 m0, s75, 81664\n"
      "v_add_f32 v80, v80, v81\n"
      "s_waitcnt lgkmcnt(1)\n"
      "v_mfma_f32_16x16x32_bf16 v[130:133], a[184:187], v[234:237], v[130:133]\n"
      "ds_read_b128 v[214:217], v172 offset:6144\n"
      "v_cvt_pk_bf16_f32 v210, v108, v109\n"
      "v_mfma_f32_16x16x32_bf16 v[130:133], a[188:191], v[196:199], v[130:133]\n"
      "ds_read_b128 v[108:111], v172 offset:10240\n"
      "v_permlane16_swap_b32 v186, v80\n"
      "v_add_f32 v186, v186, v80\n"
      "v_mfma_f32_16x16x32_bf16 v[126:129], a[208:211], v[234:237], v[126:129]\n"
      "buffer_load_dwordx4 v255, s[36:39], s79 offen offset:256  lds\n"
      "v_cvt_pk_bf16_f32 v102, v102, v103\n"
      "v_cvt_pk_bf16_f32 v202, v202, v203\n"
      "v_mfma_f32_16x16x32_bf16 v[126:129], a[212:215], v[196:199], v[126:129]\n"
      "s_mov_b32 s79, 61440\n"
      "v_cvt_pk_bf16_f32 v162, v244, v245\n"
      "v_cvt_pk_bf16_f32 v219, v220, v221\n"
      "v_cvt_pk_bf16_f32 v130, v130, v131\n"
      "v_cvt_pk_bf16_f32 v131, v132, v133\n"
      "v_mfma_f32_16x16x32_bf16 v[242:245], a[168:171], v[138:141], 0\n"
      "ds_read_b128 v[220:223], v171 offset:10240\n"
      "v_mov_b32_e64 v80, v186\n"
      "v_mfma_f32_16x16x32_bf16 v[248:251], a[160:163], v[234:237], v[248:251]\n"
      "s_add_u32 m0, s75, 86016\n"
      "ds_read_b128 v[120:123], v171 offset:6144\n"
      "v_permlane32_swap_b32 v186, v80\n"
      "v_add_f32 v186, v186, v80\n"
      "v_pk_mul_f32 v[184:185], v[184:185], v[84:85] op_sel_hi:[1,0]\n"
      "v_mfma_f32_16x16x32_bf16 v[248:251], a[164:167], v[196:199], v[248:251]\n"
      "buffer_load_dwordx4 v255, s[36:39], s79 offen  lds\n"
      "v_rsq_f32_e64 v186, v186\n"
      "v_pk_mul_f32 v[230:231], v[230:231], v[84:85] op_sel_hi:[1,0]\n"
      "s_add_u32 m0, s75, 89984\n"
      "v_mov_b32 v198, 0xbc80bc80\n"
      "v_mov_b32_e64 v80, v186\n"
      "v_cvt_pk_bf16_f32 v126, v126, v127\n"
      "v_mfma_f32_16x16x32_bf16 v[170:173], a[192:195], v[138:141], 0\n"
      "ds_write_b64 v254, v[130:131] offset:32\n"
      "v_permlane16_swap_b32 v186, v80\n"
      "v_mul_f32_e64 v186, s50, v186\n"
      "v_pk_mul_f32 v[154:155], v[154:155], v[206:207] op_sel_hi:[1,0]\n"
      "v_pk_mul_f32 v[174:175], v[174:175], v[76:77] op_sel_hi:[1,0]\n"
      "v_cvt_pk_bf16_f32 v248, v248, v249\n"
      "v_pk_mul_f32 v[192:193], v[192:193], v[80:81] op_sel_hi:[1,0]\n"
      "s_waitcnt lgkmcnt(1)\n"
      "v_mfma_f32_16x16x32_bf16 v[170:173], a[196:199], v[178:181], v[170:173]\n"
      "buffer_load_dwordx4 v255, s[36:39], s79 offen offset:128  lds\n"
      "ds_read_b128 v[130:133], v1 offset:10240\n"
      "v_pk_mul_f32 v[188:189], v[188:189], v[206:207] op_sel_hi:[1,0]\n"
      "v_mfma_f32_16x16x32_bf16 v[116:119], a[144:147], v[138:141], 0\n"
      "s_waitcnt vmcnt(11)\n"
      "s_barrier\n"
      "v_cvt_pk_bf16_f32 v127, v128, v129\n"
      "v_mfma_f32_16x16x32_bf16 v[116:119], a[148:151], v[178:181], v[116:119]\n"
      "ds_read_b64_tr_b16 a[244:245], v241 offset:36872\n"
      "ds_read_b64_tr_b16 a[216:217], v241 offset:24576\n"
      "v_mfma_f32_16x16x32_bf16 v[116:119], a[152:155], v[214:217], v[116:119]\n"
      "s_add_u32 m0, s75, 93952\n"
      "v_cvt_pk_bf16_f32 v188, v188, v189\n"
      "v_cvt_pk_bf16_f32 v163, v246, v247\n"
      "v_mfma_f32_16x16x32_bf16 v[116:119], a[156:159], v[120:123], v[116:119]\n"
      "buffer_load_dwordx4 v255, s[36:39], s79 offen offset:256  lds\n"
      "v_mfma_f32_16x16x32_bf16 v[242:245], a[172:175], v[178:181], v[242:245]\n"
      "ds_read_b64_tr_b16 a[242:243], v241 offset:26632\n"
      "v_cvt_pk_bf16_f32 v189, v190, v191\n"
      "v_cvt_pk_bf16_f32 v249, v250, v251\n"
      "v_mfma_f32_16x16x32_bf16 v[170:173], a[200:203], v[214:217], v[170:173]\n"
      "ds_read_b64_tr_b16 a[222:223], v241 offset:38912\n"
      "ds_read_b64_tr_b16 a[220:221], v241 offset:36864\n"
      "v_mfma_f32_16x16x32_bf16 v[170:173], a[204:207], v[120:123], v[170:173]\n"
      "ds_read_b64_tr_b16 a[246:247], v241 offset:38920\n"
      "v_mov_b32 v200, 0xbc80bc80\n"
      "v_mfma_f32_16x16x32_bf16 v[170:173], a[208:211], v[108:111], v[170:173]\n"
      "ds_read_b64_tr_b16 v[234:235], v252 offset:26624\n"
      "v_cvt_pk_bf16_f32 v192, v192, v193\n"
      "v_mfma_f32_16x16x32_bf16 v[170:173], a[212:215], v[220:223], v[170:173]\n"
      "buffer_load_dwordx4 v[138:141], v36, s[28:31], s49 offen\n"
      "v_mfma_f32_16x16x32_bf16 v[242:245], a[176:179], v[214:217], v[242:245]\n"
      "ds_read_b64_tr_b16 a[240:241], v241 offset:24584\n"
      "ds_read_b64_tr_b16 v[236:237], v252 offset:36864\n"
      "ds_read_b64_tr_b16 v[238:239], v252 offset:38912\n"
      "v_pk_mul_f32 v[96:97], v[96:97], v[80:81] op_sel_hi:[1,0]\n"
      "v_mfma_f32_16x16x32_bf16 v[242:245], a[180:183], v[120:123], v[242:245]\n"
      "ds_read_b64_tr_b16 a[218:219], v241 offset:26624\n"
      "s_add_u32 m0, s64, 18432\n"
      "v_cvt_pk_bf16_f32 v170, v170, v171\n"
      "v_mfma_f32_16x16x32_bf16 v[242:245], a[184:187], v[108:111], v[242:245]\n"
      "buffer_load_dword v187, v29, s[28:31], s49 offen\n"
      "v_mfma_f32_16x16x32_bf16 v[242:245], a[188:191], v[220:223], v[242:245]\n"
      "ds_read_b128 v[214:217], v1 offset:4096\n"
      "ds_read_b64_tr_b16 v[232:233], v252 offset:24576\n"
      "v_mfma_f32_16x16x32_bf16 v[116:119], a[160:163], v[108:111], v[116:119]\n"
      "ds_read_b128 v[108:111], v0 offset:10240\n"
      "v_cvt_pk_bf16_f32 v171, v172, v173\n"
      "v_mfma_f32_16x16x32_bf16 v[116:119], a[164:167], v[220:223], v[116:119]\n"
      "ds_read_b128 v[178:181], v0 offset:4096\n"
      "buffer_load_dwordx4 v[220:223], v36, s[28:31], s74 offen\n"
      "v_pk_mul_f32 v[194:195], v[194:195], v[80:81] op_sel_hi:[1,0]\n"
      "v_pk_mul_f32 v[148:149], v[148:149], v[80:81] op_sel_hi:[1,0]\n"
      "v_pk_mul_f32 v[158:159], v[158:159], v[186:187] op_sel_hi:[1,0]\n"
      "v_pk_mul_f32 v[134:135], v[134:135], v[186:187] op_sel_hi:[1,0]\n"
      "v_mov_b32 v201, 0xbc80bc80\n"
      "v_cvt_pk_bf16_f32 v242, v242, v243\n"
      "v_pk_mul_f32 v[168:169], v[168:169], v[206:207] op_sel_hi:[1,0]\n"
      "v_pk_mul_f32 v[112:113], v[112:113], v[186:187] op_sel_hi:[1,0]\n"
      "v_pk_mul_f32 v[156:157], v[156:157], v[186:187] op_sel_hi:[1,0]\n"
      "v_cvt_pk_bf16_f32 v103, v104, v105\n"
      "v_cvt_pk_bf16_f32 v166, v166, v167\n"
      "v_cvt_pk_bf16_f32 v203, v204, v205\n"
      "v_cvt_pk_bf16_f32 v116, v116, v117\n"
      "v_pk_mul_f32 v[226:227], v[226:227], v[76:77] op_sel_hi:[1,0]\n"
      "v_cvt_pk_bf16_f32 v112, v112, v113\n"
      "v_cvt_pk_bf16_f32 v243, v244, v245\n"
      "v_pk_mul_f32 v[136:137], v[136:137], v[186:187] op_sel_hi:[1,0]\n"
      "v_mov_b32 v199, 0xbc80bc80\n"
      "v_pk_mul_f32 v[114:115], v[114:115], v[186:187] op_sel_hi:[1,0]\n"
      "v_pk_mul_f32 v[146:147], v[146:147], v[80:81] op_sel_hi:[1,0]\n"
      "v_pk_mul_f32 v[98:99], v[98:99], v[80:81] op_sel_hi:[1,0]\n"
      "v_cvt_pk_bf16_f32 v117, v118, v119\n"
      "s_waitcnt lgkmcnt(0)\n"
      "v_mfma_f32_16x16x32_bf16 v[204:207], v[178:181], a[144:147], 0\n"
      "buffer_load_dword v105, v29, s[28:31], s74 offen\n"
      "v_cvt_pk_bf16_f32 v167, v168, v169\n"
      "v_cvt_pk_bf16_f32 v169, v154, v155\n"
      "v_mfma_f32_16x16x32_bf16 v[118:121], v[178:181], a[168:171], 0\n"
      "ds_write_b64 v254, v[126:127] offset:64\n"
      "v_mfma_f32_16x16x32_bf16 v[118:121], v[214:217], a[172:175], v[118:121]\n"
      "ds_read_b128 v[126:129], v1 offset:16384\n"
      "v_cvt_pk_bf16_f32 v113, v114, v115\n"
      "v_cvt_pk_bf16_f32 v168, v152, v153\n"
      "v_mfma_f32_16x16x32_bf16 v[152:155], v[178:181], a[192:195], 0\n"
      "buffer_load_dwordx4 v16, s[16:19], s45 offen  lds\n"
      "v_mfma_f32_16x16x32_bf16 v[152:155], v[214:217], a[196:199], v[152:155]\n"
      "ds_read_b128 v[244:247], v0 offset:16384\n"
      "v_xor_b32 v80, 64, v8\n"
      "s_mov_b32 exec_lo, 0x3f003f\n"
      "s_mov_b32 exec_hi, 0x3f003f\n"
      "ds_write_b64 v254, v[116:117] offset:18688\n"
      "ds_write_b64 v254, v[242:243] offset:18720\n"
      "ds_write_b64 v254, v[170:171] offset:18752\n"
      "s_mov_b64 exec, -1\n"
      "v_pk_mul_f32 v[182:183], v[182:183], v[84:85] op_sel_hi:[1,0]\n"
      "v_cvt_pk_bf16_f32 v114, v156, v157\n"
      "v_pk_mul_f32 v[176:177], v[176:177], v[76:77] op_sel_hi:[1,0]\n"
      "v_mfma_f32_16x16x32_bf16 v[204:207], v[214:217], a[148:151], v[204:207]\n"
      "ds_read_b128 v[178:181], v1 offset:2048\n"
      "v_cvt_pk_bf16_f32 v174, v174, v175\n"
      "v_mfma_f32_16x16x32_bf16 v[204:207], v[108:111], a[152:155], v[204:207]\n"
      "s_add_u32 m0, s64, 22528\n"
      "ds_read_b128 v[170:173], v0 offset:2048\n"
      "v_mfma_f32_16x16x32_bf16 v[204:207], v[130:133], a[156:159], v[204:207]\n"
      "buffer_load_dwordx4 v17, s[16:19], s45 offen  lds\n"
      "v_mfma_f32_16x16x32_bf16 v[152:155], v[108:111], a[200:203], v[152:155]\n"
      "ds_write_b64 v254, v[248:249]\n"
      "v_mfma_f32_16x16x32_bf16 v[152:155], v[130:133], a[204:207], v[152:155]\n"
      "ds_read_b128 v[240:243], v8 offset:24576\n"
      "v_cvt_pk_bf16_f32 v134, v134, v135\n"
      "v_mfma_f32_16x16x32_bf16 v[118:121], v[108:111], a[176:179], v[118:121]\n"
      "s_add_u32 m0, s64, 26624\n"
      "ds_read_b128 v[214:217], v8 offset:28672\n"
      "v_mfma_f32_16x16x32_bf16 v[118:121], v[130:133], a[180:183], v[118:121]\n"
      "buffer_load_dwordx4 v18, s[16:19], s45 offen  lds\n"
      "ds_read_b128 v[130:133], v80 offset:24576\n"
      "s_waitcnt lgkmcnt(4)\n"
      "v_mfma_f32_16x16x32_bf16 v[118:121], v[244:247], a[184:187], v[118:121]\n"
      "s_waitcnt vmcnt(13)\n"
      "s_barrier\n"
      "ds_read_b64_tr_b16 a[224:225], v78\n"
      "v_mfma_f32_16x16x32_bf16 v[118:121], v[126:129], a[188:191], v[118:121]\n"
      "ds_read_b64_tr_b16 a[248:249], v78 offset:8\n"
      "ds_read_b64_tr_b16 a[226:227], v78 offset:2048\n"
      "v_cvt_pk_bf16_f32 v96, v96, v97\n"
      "v_cvt_pk_bf16_f32 v115, v158, v159\n"
      "v_pk_mul_f32 v[228:229], v[228:229], v[84:85] op_sel_hi:[1,0]\n"
      "v_mfma_f32_16x16x32_bf16 v[204:207], v[244:247], a[160:163], v[204:207]\n"
      "ds_read_b64_tr_b16 a[254:255], v78 offset:14344\n"
      "s_add_u32 m0, s64, 30720\n"
      "v_mfma_f32_16x16x32_bf16 v[204:207], v[126:129], a[164:167], v[204:207]\n"
      "buffer_load_dwordx4 v19, s[16:19], s45 offen  lds\n"
      "s_waitcnt lgkmcnt(4)\n"
      "v_mfma_f32_16x16x32_bf16 v[248:251], v[240:243], v[198:201], 0\n"
      "ds_read_b64_tr_b16 a[230:231], v78 offset:14336\n"
      "v_cvt_pk_bf16_f32 v175, v176, v177\n"
      "v_mfma_f32_16x16x32_bf16 v[248:251], v[130:133], v[198:201], v[248:251]\n"
      "ds_read_b128 v[108:111], v0 offset:8192\n"
      "v_cvt_pk_bf16_f32 v193, v194, v195\n"
      "v_mfma_f32_16x16x32_bf16 v[130:133], v[170:173], a[168:171], 0\n"
      "s_add_u32 m0, s65, 34816\n"
      "ds_read_b64_tr_b16 v[242:243], v93 offset:2048\n"
      "buffer_load_dwordx4 v15, s[16:19], s45 offen  lds\n"
      "v_mfma_f32_16x16x32_bf16 v[130:133], v[178:181], a[172:175], v[130:133]\n"
      "ds_read_b128 v[156:159], v80 offset:28672\n"
      "ds_read_b64_tr_b16 a[252:253], v78 offset:12296\n"
      "v_mfma_f32_16x16x32_bf16 v[152:155], v[244:247], a[208:211], v[152:155]\n"
      "ds_read_b64_tr_b16 v[246:247], v93 offset:14336\n"
      "ds_read_b64_tr_b16 a[250:251], v78 offset:2056\n"
      "v_mfma_f32_16x16x32_bf16 v[152:155], v[126:129], a[212:215], v[152:155]\n"
      "ds_read_b64_tr_b16 v[240:241], v93\n"
      "ds_read_b64_tr_b16 a[228:229], v78 offset:12288\n"
      "v_mfma_f32_16x16x32_bf16 v[194:197], v[170:173], a[192:195], 0\n"
      "ds_read_b64_tr_b16 v[244:245], v93 offset:12288\n"
      "v_cvt_pk_bf16_f32 v135, v136, v137\n"
      "v_cvt_pk_bf16_f32 v176, v224, v225\n"
      "v_mfma_f32_16x16x32_bf16 v[194:197], v[178:181], a[196:199], v[194:197]\n"
      "ds_read_b128 v[126:129], v1 offset:8192\n"
      "v_cvt_pk_bf16_f32 v177, v226, v227\n"
      "v_mfma_f32_16x16x32_bf16 v[248:251], v[214:217], v[198:201], v[248:251]\n"
      "s_waitcnt lgkmcnt(7)\n"
      "v_cvt_pk_bf16_f32 v97, v98, v99\n"
      "v_add_u32 v123, 49152, v8\n"
      "v_cvt_pk_bf16_f32 v98, v146, v147\n"
      "v_mfma_f32_16x16x32_bf16 v[248:251], v[156:159], v[198:201], v[248:251]\n"
      "ds_read_b128 v[224:227], v123\n"
      "v_cvt_pk_bf16_f32 v85, v154, v155\n"
      "v_mfma_f32_16x16x32_bf16 v[214:217], v[170:173], a[144:147], 0\n"
      "ds_read_b128 v[170:173], v1 offset:14336\n"
      "v_cvt_pk_bf16_f32 v99, v148, v149\n"
      "v_mfma_f32_16x16x32_bf16 v[214:217], v[178:181], a[148:151], v[214:217]\n"
      "ds_read_b128 v[178:181], v0 offset:14336\n"
      "v_cvt_pk_bf16_f32 v182, v182, v183\n"
      "v_mfma_f32_16x16x32_bf16 v[214:217], v[108:111], a[152:155], v[214:217]\n"
      "ds_read_b128 v[146:149], v8 offset:32768\n"
      "v_subrev_u32 v186, 115456, v9\n"
      "v_mfma_f32_16x16x32_bf16 v[130:133], v[108:111], a[176:179], v[130:133]\n"
      "s_mov_b32 exec_lo, 0x3f003f\n"
      "s_mov_b32 exec_hi, 0x3f003f\n"
      "ds_write_b128 v253, v[112:115] offset:18688\n"
      "ds_write_b64 v253, v[134:135] offset:18704\n"
      "ds_write_b64 v253, v[192:193] offset:18712\n"
      "ds_write_b128 v253, v[96:99] offset:18720\n"
      "s_mov_b64 exec, -1\n"
      "v_mfma_f32_16x16x32_bf16 v[194:197], v[108:111], a[200:203], v[194:197]\n"
      "s_waitcnt lgkmcnt(4)\n"
      "v_cvt_pk_bf16_f32 v183, v184, v185\n"
      "v_cvt_pk_bf16_f32 v184, v228, v229\n"
      "v_add_u32 v96, 49152, v80\n"
      "v_mfma_f32_16x16x32_bf16 v[248:251], v[146:149], v[198:201], v[248:251]\n"
      "ds_read_b128 v[252:255], v96 offset:4096\n"
      "v_cvt_pk_bf16_f32 v185, v230, v231\n"
      "v_mfma_f32_16x16x32_bf16 v[214:217], v[126:129], a[156:159], v[214:217]\n"
      "buffer_load_dwordx4 v[108:111], v36, s[28:31], s47 offen\n"
      "v_mfma_f32_16x16x32_bf16 v[214:217], v[178:181], a[160:163], v[214:217]\n"
      "ds_read_b128 v[228:231], v80 offset:32768\n"
      "v_cvt_pk_bf16_f32 v84, v152, v153\n"
      "v_mfma_f32_16x16x32_bf16 v[146:149], v[224:227], v[198:201], 0\n"
      "ds_read_b128 v[224:227], v96\n"
      "v_cvt_pk_bf16_f32 v76, v204, v205\n"
      "v_mfma_f32_16x16x32_bf16 v[194:197], v[126:129], a[204:207], v[194:197]\n"
      "ds_read_b128 v[190:193], v123 offset:4096\n"
      "v_cvt_pk_bf16_f32 v80, v118, v119\n"
      "v_mfma_f32_16x16x32_bf16 v[194:197], v[178:181], a[208:211], v[194:197]\n"
      "buffer_load_dword v150, v29, s[28:31], s47 offen\n"
      "v_subrev_u32 v8, 115456, v9\n"
      "v_mfma_f32_16x16x32_bf16 v[130:133], v[126:129], a[180:183], v[130:133]\n"
      "ds_read_b128 v[126:129], v96 offset:8192\n"
      "v_lshrrev_b32_e64 v8, 4, v8\n"
      "v_mfma_f32_16x16x32_bf16 v[130:133], v[178:181], a[184:187], v[130:133]\n"
      "ds_read_b128 v[178:181], v123 offset:8192\n"
      "v_cvt_pk_bf16_f32 v81, v120, v121\n"
      "v_mfma_f32_16x16x32_bf16 v[130:133], v[170:173], a[188:191], v[130:133]\n"
      "ds_read_b128 v[134:137], v1\n"
      "v_cvt_pk_bf16_f32 v77, v206, v207\n"
      "v_mfma_f32_16x16x32_bf16 v[214:217], v[170:173], a[164:167], v[214:217]\n"
      "ds_read_b128 v[204:207], v0\n"
      "v_readlane_b32 s86, v38, s43\n"
      "v_mfma_f32_16x16x32_bf16 v[194:197], v[170:173], a[212:215], v[194:197]\n"
      "s_waitcnt lgkmcnt(4)\n"
      "v_bfe_u32 v104, v186, 8, 2\n"
      "v_and_b32_e64 v8, 48, v8\n"
      "v_cvt_pk_bf16_f32 v133, v132, v133\n"
      "v_mfma_f32_16x16x32_bf16 v[248:251], v[228:231], v[198:201], v[248:251]\n"
      "s_waitcnt vmcnt(11)\n"
      "s_barrier\n"
      "v_lshlrev_b32_e64 v104, 2, v104\n"
      "v_mfma_f32_16x16x32_bf16 v[146:149], v[224:227], v[198:201], v[146:149]\n"
      "ds_read_b128 v[228:231], v123 offset:24576\n"
      "v_cvt_pk_bf16_f32 v132, v130, v131\n"
      "v_bfe_u32 v186, v186, 4, 4\n"
      "v_mfma_f32_16x16x32_bf16 v[146:149], v[190:193], v[198:201], v[146:149]\n"
      "ds_read_b64_tr_b16 a[238:239], v78 offset:38912\n"
      "v_cvt_pk_bf16_f32 v197, v196, v197\n"
      "v_mfma_f32_16x16x32_bf16 v[146:149], v[252:255], v[198:201], v[146:149]\n"
      "ds_read_b128 v[252:255], v123 offset:28672\n"
      "s_waitcnt lgkmcnt(3)\n"
      "v_mfma_f32_16x16x32_bf16 v[146:149], v[178:181], v[198:201], v[146:149]\n"
      "ds_read_b128 v[224:227], v96 offset:24576\n"
      "v_cvt_pk_bf16_f32 v217, v216, v217\n"
      "v_mfma_f32_16x16x32_bf16 v[146:149], v[126:129], v[198:201], v[146:149]\n"
      "ds_read_b128 v[126:129], v96 offset:28672\n"
      "v_mul_f32 v251, 0x3eaaaaab, v251\n"
      "v_mfma_f32_16x16x32_bf16 v[190:193], v[204:207], a[168:171], 0\n"
      "ds_read_b128 v[178:181], v96 offset:32768\n"
      "v_cvt_pk_bf16_f32 v216, v214, v215\n"
      "v_mfma_f32_16x16x32_bf16 v[170:173], v[204:207], a[144:147], 0\n"
      "ds_read_b128 v[116:119], v123 offset:32768\n"
      "v_mul_f32 v250, 0x3eaaaaab, v250\n"
      "v_mfma_f32_16x16x32_bf16 v[170:173], v[134:137], a[148:151], v[170:173]\n"
      "ds_read_b64_tr_b16 a[232:233], v78 offset:24576\n"
      "v_mul_f32 v249, 0x3eaaaaab, v249\n"
      "v_mfma_f32_16x16x32_bf16 v[120:123], v[204:207], a[192:195], 0\n"
      "ds_read_b64_tr_b16 a[234:235], v78 offset:26624\n"
      "v_mul_f32 v248, 0x3eaaaaab, v248\n"
      "v_mfma_f32_16x16x32_bf16 v[120:123], v[134:137], a[196:199], v[120:123]\n"
      "ds_read_b128 v[204:207], v1 offset:12288\n"
      "s_waitcnt lgkmcnt(6)\n"
      "v_mfma_f32_16x16x32_bf16 v[96:99], v[228:231], v[198:201], 0\n"
      "ds_read_b128 v[156:159], v0 offset:6144\n"
      "s_waitcnt vmcnt(10)\n"
      "v_or3_b32 v138, v138, v139, v140\n"
      "v_mfma_f32_16x16x32_bf16 v[96:99], v[224:227], v[198:201], v[96:99]\n"
      "ds_read_b128 v[112:115], v1 offset:6144\n"
      "v_sub_u32 v186, v186, v104\n"
      "v_mfma_f32_16x16x32_bf16 v[96:99], v[252:255], v[198:201], v[96:99]\n"
      "s_waitcnt lgkmcnt(5)\n"
      "v_add_u32 v8, 123136, v8\n"
      "v_add_u32_e64 v226, s92, v8\n"
      "v_mfma_f32_16x16x32_bf16 v[152:155], v[182:185], v[208:211], v[44:47]\n"
      "ds_write_b128 v226, v[248:251]\n"
      "v_mfma_f32_16x16x32_bf16 v[190:193], v[134:137], a[172:175], v[190:193]\n"
      "ds_read_b64_tr_b16 v[224:225], v78 offset:24584\n"
      "v_mul_f32 v148, 0x3eaaaaab, v148\n"
      "v_mfma_f32_16x16x32_bf16 v[96:99], v[126:129], v[198:201], v[96:99]\n"
      "ds_read_b128 v[126:129], v10\n"
      "s_waitcnt vmcnt(9)\n"
      "v_or3_b32 v138, v138, v141, v187\n"
      "v_mfma_f32_16x16x32_bf16 v[96:99], v[116:119], v[198:201], v[96:99]\n"
      "ds_read_b64_tr_b16 v[254:255], v93 offset:38912\n"
      "s_waitcnt lgkmcnt(4)\n"
      "v_mfma_f32_16x16x32_bf16 v[170:173], v[156:159], a[152:155], v[170:173]\n"
      "ds_read_b128 v[116:119], v0 offset:12288\n"
      "v_mul_f32 v149, 0x3eaaaaab, v149\n"
      "v_mfma_f32_16x16x32_bf16 v[134:137], v[142:145], v[208:211], v[40:43]\n"
      "ds_read_b64_tr_b16 v[228:229], v78 offset:36872\n"
      "s_waitcnt vmcnt(8)\n"
      "v_or3_b32 v220, v220, v221, v222\n"
      "v_mfma_f32_16x16x32_bf16 v[96:99], v[178:181], v[198:201], v[96:99]\n"
      "ds_read_b128 v[178:181], v10 offset:32\n"
      "v_mov_b32_e64 v141, 0\n"
      "v_mfma_f32_16x16x32_bf16 v[190:193], v[156:159], a[176:179], v[190:193]\n"
      "ds_read_b64_tr_b16 a[236:237], v78 offset:36864\n"
      "v_mul_f32 v146, 0x3eaaaaab, v146\n"
      "v_mfma_f32_16x16x32_bf16 v[170:173], v[112:115], a[156:159], v[170:173]\n"
      "ds_write_b32 v31, v138 offset:6656\n"
      "v_mfma_f32_16x16x32_bf16 v[198:201], v[174:177], v[208:211], v[48:51]\n"
      "s_or_b32 s95, s95, s48\n"
      "v_mul_f32 v147, 0x3eaaaaab, v147\n"
      "v_mul_f32 v97, 0x3eaaaaab, v97\n"
      "v_mfma_f32_16x16x32_bf16 v[120:123], v[156:159], a[200:203], v[120:123]\n"
      "ds_write_b128 v226, v[146:149] offset:256\n"
      "v_mfma_f32_16x16x16_bf16 v[134:137], v[212:213], v[124:125], v[134:137]\n"
      "ds_read_b64_tr_b16 v[230:231], v78 offset:38920\n"
      "v_cvt_pk_bf16_f32 v196, v194, v195\n"
      "v_mfma_f32_16x16x32_bf16 v[146:149], v[182:185], v[166:169], v[56:59]\n"
      "s_waitcnt lgkmcnt(3)\n"
      "s_waitcnt vmcnt(7)\n"
      "v_or3_b32 v220, v220, v223, v105\n"
      "v_mul_f32 v98, 0x3eaaaaab, v98\n"
      "v_mfma_f32_16x16x32_bf16 v[190:193], v[112:115], a[180:183], v[190:193]\n"
      "ds_write_b32 v31, v220 offset:5632\n"
      "v_mfma_f32_16x16x32_bf16 v[190:193], v[116:119], a[184:187], v[190:193]\n"
      "ds_read_b64_tr_b16 v[252:253], v93 offset:36864\n"
      "v_mov_b32_e64 v105, 0\n"
      "v_mfma_f32_16x16x32_bf16 v[190:193], v[204:207], a[188:191], v[190:193]\n"
      "ds_read_b64 v[106:107], v10 offset:24\n"
      "v_mul_f32 v99, 0x3eaaaaab, v99\n"
      "v_mfma_f32_16x16x32_bf16 v[170:173], v[116:119], a[160:163], v[170:173]\n"
      "ds_read_b64_tr_b16 v[250:251], v93 offset:26624\n"
      "v_mul_f32 v96, 0x3eaaaaab, v96\n"
      "v_mfma_f32_16x16x32_bf16 v[170:173], v[204:207], a[164:167], v[170:173]\n"
      "ds_write_b128 v226, v[96:99] offset:512\n"
      "v_mfma_f32_16x16x32_bf16 v[220:223], v[178:181], v[166:169], v[26:29]\n"
      "ds_read_b64_tr_b16 v[248:249], v93 offset:24576\n"
      "ds_read_b64 v[92:93], v10 offset:16\n"
      "v_mfma_f32_16x16x32_bf16 v[120:123], v[112:115], a[204:207], v[120:123]\n"
      "ds_read_b64_tr_b16 v[226:227], v78 offset:26632\n"
      "v_cvt_pk_bf16_f32 v131, v192, v193\n"
      "v_mfma_f32_16x16x16_bf16 v[198:201], v[202:203], v[124:125], v[198:201]\n"
      "v_cvt_pk_bf16_f32 v214, v170, v171\n"
      "s_waitcnt lgkmcnt(0)\n"
      "s_barrier\n"
      "v_mov_b32 v104, 0x3f80\n"
      "v_mfma_f32_16x16x32_bf16 v[156:159], v[178:181], v[208:211], v[24:27]\n"
      "ds_read_b128 v[208:211], v9 offset:6656\n"
      "v_lshlrev_b32 v186, 4, v186\n"
      "v_mfma_f32_16x16x32_bf16 v[120:123], v[116:119], a[208:211], v[120:123]\n"
      "s_waitcnt lgkmcnt(5)\n"
      "v_cmp_gt_u32 s[80:81], 64, v186\n"
      "v_cvt_pk_bf16_f32 v215, v172, v173\n"
      "v_mfma_f32_16x16x32_bf16 v[120:123], v[204:207], a[212:215], v[120:123]\n"
      "ds_read_u16 v78, v11\n"
      "v_lshlrev_b64 v[104:105], v186, v[104:105]\n"
      "v_mfma_f32_16x16x32_bf16 v[204:207], v[174:177], v[166:169], v[60:63]\n"
      "ds_read_b128 v[96:99], v9 offset:5632\n"
      "v_cndmask_b32_e64 v186, 0, v104, s[80:81]\n"
      "v_mfma_f32_16x16x16_bf16 v[146:149], v[218:219], v[188:189], v[146:149]\n"
      "v_cndmask_b32_e64 v187, 0, v105, s[80:81]\n"
      "v_cndmask_b32_e64 v139, 0, v105, s[80:81]\n"
      "v_cvt_pk_bf16_f32 v195, v122, v123\n"
      "v_mfma_f32_16x16x16_bf16 v[156:159], v[106:107], v[124:125], v[156:159]\n"
      "s_cmp_ge_u32 s95, 0x40000000\n"
      "s_cselect_b32 s94, -1, -2\n"
      "v_cvt_pk_bf16_f32 v130, v190, v191\n"
      "v_mfma_f32_16x16x16_bf16 v[220:223], v[106:107], v[188:189], v[220:223]\n"
      "s_waitcnt lgkmcnt(2)\n"
      "v_or3_b32 v208, v208, v209, v210\n"
      "v_bitop3_b32 v208, v208, v211, s33 bitop3:0xa8\n"
      "v_mfma_f32_16x16x16_bf16 v[152:155], v[218:219], v[124:125], v[152:155]\n"
      "v_cmp_ne_u32 s[56:57], 0, v208\n"
      "v_cndmask_b32_e64 v138, 0, v104, s[80:81]\n"
      "s_or_b64 vcc, s[56:57], s[54:55]\n"
      "v_mfma_f32_16x16x32_bf16 v[208:211], v[142:145], v[166:169], v[52:55]\n"
      "s_cbranch_vccnz L_rare_1_%=\n"
      "L_back_2_%=:\n"
      "v_mfma_f32_16x16x32_bf16 v[112:115], v[178:181], v[160:163], v[28:31]\n"
      "ds_read_u16 v86, v11 offset:64\n"
      "v_cvt_pk_bf16_f32 v194, v120, v121\n"
      "v_mfma_f32_16x16x32_bf16 v[190:193], v[126:129], v[178:181], v[30:33]\n"
      "s_cbranch_vccnz L_rare_3_%=\n"
      "L_back_4_%=:\n"
      "v_mfma_f32_16x16x16_bf16 v[204:207], v[202:203], v[188:189], v[204:207]\n"
      "v_exp_f32 v135, v135\n"
      "v_exp_f32 v136, v136\n"
      "v_mfma_f32_16x16x16_bf16 v[208:211], v[212:213], v[188:189], v[208:211]\n"
      "v_exp_f32 v134, v134\n"
      "v_exp_f32_e64 v153, v153\n"
      "v_mfma_f32_16x16x32_bf16 v[164:167], v[182:185], v[160:163], v[68:71]\n"
      "s_waitcnt lgkmcnt(1)\n"
      "v_or3_b32 v96, v96, v97, v98\n"
      "v_exp_f32 v155, v155\n"
      "v_bitop3_b32 v96, v96, v99, s33 bitop3:0xa8\n"
      "v_mfma_f32_16x16x16_bf16 v[112:115], v[106:107], v[102:103], v[112:115]\n"
      "v_exp_f32 v152, v152\n"
      "v_exp_f32 v137, v137\n"
      "v_mfma_f32_16x16x32_bf16 v[178:181], v[126:129], v[182:185], v[30:33]\n"
      "s_cbranch_vccnz L_rare_5_%=\n"
      "L_back_6_%=:\n"
      "v_mfma_f32_16x16x16_bf16 v[190:193], v[92:93], v[106:107], v[190:193]\n"
      "v_exp_f32 v156, v156\n"
      "v_exp_f32 v147, v147\n"
      "v_exp_f32_e64 v201, v201\n"
      "v_cvt_pk_bf16_f32 v134, v134, v135\n"
      "v_mfma_f32_16x16x32_bf16 v[168:171], v[126:129], v[142:145], v[30:33]\n"
      "ds_read_b128 v[118:121], v8\n"
      "v_cmp_ne_u32 s[56:57], 0, v96\n"
      "v_mfma_f32_16x16x16_bf16 v[178:181], v[92:93], v[218:219], v[178:181]\n"
      "v_exp_f32 v206, v206\n"
      "v_exp_f32 v146, v146\n"
      "v_mfma_f32_16x16x32_bf16 v[114:117], v[142:145], v[160:163], v[64:67]\n"
      "ds_read_b128 v[142:145], v8 offset:64\n"
      "v_exp_f32 v211, v211\n"
      "v_mfma_f32_16x16x16_bf16 v[164:167], v[218:219], v[102:103], v[164:167]\n"
      "v_exp_f32 v204, v204\n"
      "v_exp_f32 v205, v205\n"
      "v_exp_f32 v220, v220\n"
      "v_cvt_pk_bf16_f32 v135, v136, v137\n"
      "v_mfma_f32_16x16x32_bf16 v[122:125], v[174:177], v[160:163], v[72:75]\n"
      "ds_read_b128 v[160:163], v8 offset:192\n"
      "v_exp_f32_e64 v210, v210\n"
      "v_mfma_f32_16x16x16_bf16 v[168:171], v[92:93], v[212:213], v[168:171]\n"
      "v_mov_b32 v184, 0\n"
      "v_exp_f32 v154, v154\n"
      "v_cvt_pk_bf16_f32 v136, v152, v153\n"
      "v_mov_b32_e64 v185, 0\n"
      "v_mfma_f32_16x16x32_bf16 v[96:99], v[126:129], v[174:177], v[30:33]\n"
      "s_waitcnt lgkmcnt(1)\n"
      "v_exp_f32 v208, v208\n"
      "v_mfma_f32_16x16x32_bf16 v[174:177], v[184:187], a[240:243], v[142:145]\n"
      "v_exp_f32_e64 v209, v209\n"
      "v_cndmask_b32 v168, v168, v178, s[58:59]\n"
      "v_mfma_f32_16x16x32_bf16 v[126:129], v[184:187], a[216:219], v[142:145]\n"
      "v_mov_b32 v140, 0\n"
      "v_cvt_pk_bf16_f32 v208, v208, v209\n"
      "v_exp_f32 v200, v200\n"
      "v_mfma_f32_16x16x16_bf16 v[96:99], v[92:93], v[202:203], v[96:99]\n"
      "v_exp_f32_e64 v148, v148\n"
      "v_cvt_pk_bf16_f32 v209, v210, v211\n"
      "v_mfma_f32_16x16x16_bf16 v[122:125], v[202:203], v[102:103], v[122:125]\n"
      "v_cvt_pk_bf16_f32 v137, v154, v155\n"
      "v_exp_f32_e64 v199, v199\n"
      "v_mfma_f32_16x16x32_bf16 v[104:107], v[184:187], v[232:235], v[142:145]\n"
      "v_cvt_pk_bf16_f32 v210, v146, v147\n"
      "v_cvt_pk_bf16_f32 v89, v200, v201\n"
      "v_mfma_f32_16x16x32_bf16 v[200:203], v[138:141], a[240:243], v[118:121]\n"
      "ds_read_u16 v82, v11 offset:32\n"
      "v_exp_f32_e64 v198, v198\n"
      "v_mfma_f32_16x16x32_bf16 v[170:173], v[138:141], a[216:219], v[118:121]\n"
      "s_waitcnt lgkmcnt(1)\n"
      "v_exp_f32 v149, v149\n"
      "v_cvt_pk_bf16_f32 v92, v204, v205\n"
      "v_mfma_f32_16x16x32_bf16 v[178:181], v[184:187], a[244:247], v[160:163]\n"
      "v_cvt_pk_bf16_f32 v211, v148, v149\n"
      "v_cvt_pk_bf16_f32 v174, v174, v175\n"
      "v_mfma_f32_16x16x32_bf16 v[146:149], v[138:141], v[232:235], v[118:121]\n"
      "v_cvt_pk_bf16_f32 v90, v156, 0\n"
      "v_cvt_pk_bf16_f32 v94, v220, 0\n"
      "v_mfma_f32_16x16x16_bf16 v[114:117], v[212:213], v[102:103], v[114:117]\n"
      "v_exp_f32 v207, v207\n"
      "s_waitcnt vmcnt(1)\n"
      "v_or3_b32 v108, v108, v109, v110\n"
      "v_accvgpr_write_b32 a242, v174\n"
      "v_cvt_pk_bf16_f32 v180, v180, v181\n"
      "v_mfma_f32_16x16x32_bf16 v[220:223], v[184:187], a[220:223], v[160:163]\n"
      "ds_read_b128 v[156:159], v8 offset:128\n"
      "v_cvt_pk_bf16_f32 v88, v198, v199\n"
      "v_cndmask_b32 v168, v168, v96, s[60:61]\n"
      "v_mfma_f32_16x16x32_bf16 v[118:121], v[32:35], v[134:137], 0\n"
      "v_cvt_pk_bf16_f32 v170, v170, v171\n"
      "v_cvt_pk_bf16_f32 v93, v206, v207\n"
      "v_mfma_f32_16x16x32_bf16 v[118:121], v[32:35], v[88:91], v[118:121]\n"
      "s_cbranch_vccnz L_rare_7_%=\n"
      "L_back_8_%=:\n"
      "v_mfma_f32_16x16x32_bf16 v[204:207], v[32:35], v[208:211], 0\n"
      "v_cvt_pk_bf16_f32 v233, v148, v149\n"
      "v_cndmask_b32 v168, v168, v190, s[62:63]\n"
      "v_mfma_f32_16x16x32_bf16 v[204:207], v[32:35], v[92:95], v[204:207]\n"
      "s_waitcnt lgkmcnt(1)\n"
      "v_exp_f32_e64 v122, v122\n"
      "v_cvt_pk_bf16_f32 v234, v104, v105\n"
      "s_waitcnt vmcnt(0)\n"
      "v_or3_b32 v108, v108, v111, v150\n"
      "v_mfma_f32_16x16x32_bf16 v[188:191], v[130:133], v[134:137], 0\n"
      "s_cbranch_vccnz L_rare_9_%=\n"
      "L_back_10_%=:\n"
      "v_mfma_f32_16x16x32_bf16 v[188:191], v[80:83], v[88:91], v[188:191]\n"
      "v_cvt_pk_bf16_f32 v178, v178, v179\n"
      "v_cvt_pk_bf16_f32 v176, v176, v177\n"
      "v_mfma_f32_16x16x32_bf16 v[142:145], v[130:133], v[208:211], 0\n"
      "v_cvt_pk_bf16_f32 v128, v128, v129\n"
      "v_cvt_pk_bf16_f32 v200, v200, v201\n"
      "v_mfma_f32_16x16x32_bf16 v[142:145], v[80:83], v[92:95], v[142:145]\n"
      "ds_read_b128 v[102:105], v8 offset:320\n"
      "v_rcp_f32 v118, v118\n"
      "v_mfma_f32_16x16x32_bf16 v[152:155], v[194:197], v[208:211], 0\n"
      "v_rcp_f32 v204, v204\n"
      "v_mfma_f32_16x16x32_bf16 v[152:155], v[84:87], v[92:95], v[152:155]\n"
      "s_cmp_gt_u32 s42, s44\n"
      "s_cselect_b32 s95, s94, s95\n"
      "v_exp_f32 v112, v112\n"
      "v_mfma_f32_16x16x32_bf16 v[148:151], v[214:217], v[208:211], 0\n"
      "v_exp_f32 v123, v123\n"
      "v_mfma_f32_16x16x32_bf16 v[148:151], v[76:79], v[92:95], v[148:151]\n"
      "v_cvt_pk_bf16_f32 v220, v220, v221\n"
      "v_cvt_pk_bf16_f32 v126, v126, v127\n"
      "v_mfma_f32_16x16x32_bf16 v[96:99], v[194:197], v[134:137], 0\n"
      "v_cvt_pk_bf16_f32 v172, v172, v173\n"
      "v_exp_f32_e64 v164, v164\n"
      "v_mfma_f32_16x16x32_bf16 v[96:99], v[84:87], v[88:91], v[96:99]\n"
      "s_waitcnt lgkmcnt(0)\n"
      "v_cvt_pk_bf16_f32 v235, v106, v107\n"
      "v_cvt_pk_bf16_f32 v222, v222, v223\n"
      "v_exp_f32 v117, v117\n"
      "v_mfma_f32_16x16x32_bf16 v[206:209], v[214:217], v[134:137], 0\n"
      "ds_write_b32 v31, v108 offset:4608\n"
      "v_mfma_f32_16x16x32_bf16 v[206:209], v[76:79], v[88:91], v[206:209]\n"
      "ds_read_b128 v[106:109], v8 offset:256\n"
      "v_exp_f32 v115, v115\n"
      "v_exp_f32 v168, v168\n"
      "v_cvt_pk_bf16_f32 v232, v146, v147\n"
      "v_pk_mul_f32 v[96:97], v[96:97], v[118:119] op_sel_hi:[1,0]\n"
      "v_mfma_f32_16x16x32_bf16 v[210:213], v[138:141], a[244:247], v[156:159]\n"
      "v_exp_f32 v124, v124\n"
      "v_exp_f32 v114, v114\n"
      "v_mfma_f32_16x16x32_bf16 v[134:137], v[184:187], a[224:227], v[102:105]\n"
      "v_exp_f32 v125, v125\n"
      "v_exp_f32 v116, v116\n"
      "v_accvgpr_write_b32 a243, v176\n"
      "v_pk_mul_f32 v[148:149], v[148:149], v[204:205] op_sel_hi:[1,0]\n"
      "v_pk_mul_f32 v[190:191], v[190:191], v[118:119] op_sel_hi:[1,0]\n"
      "v_cvt_pk_bf16_f32 v114, v114, v115\n"
      "v_exp_f32 v167, v167\n"
      "v_exp_f32 v166, v166\n"
      "v_cvt_pk_bf16_f32 v89, v124, v125\n"
      "v_pk_mul_f32 v[208:209], v[208:209], v[118:119] op_sel_hi:[1,0]\n"
      "v_exp_f32_e64 v165, v165\n"
      "v_pk_mul_f32 v[152:153], v[152:153], v[204:205] op_sel_hi:[1,0]\n"
      "v_pk_mul_f32 v[206:207], v[206:207], v[118:119] op_sel_hi:[1,0]\n"
      "v_cvt_pk_bf16_f32 v115, v116, v117\n"
      "v_cvt_pk_bf16_f32 v206, v206, v207\n"
      "v_accvgpr_write_b32 a219, v128\n"
      "v_pk_mul_f32 v[188:189], v[188:189], v[118:119] op_sel_hi:[1,0]\n"
      "v_cvt_pk_bf16_f32 v168, v168, v168\n"
      "v_cvt_pk_bf16_f32 v96, v96, v97\n"
      "v_cvt_pk_bf16_f32 v148, v148, v149\n"
      "v_cvt_pk_bf16_f32 v134, v134, v135\n"
      "v_pk_mul_f32 v[154:155], v[154:155], v[204:205] op_sel_hi:[1,0]\n"
      "v_pk_mul_f32 v[144:145], v[144:145], v[204:205] op_sel_hi:[1,0]\n"
      "v_pk_mul_f32 v[142:143], v[142:143], v[204:205] op_sel_hi:[1,0]\n"
      "v_pk_mul_f32 v[98:99], v[98:99], v[118:119] op_sel_hi:[1,0]\n"
      "v_cvt_pk_bf16_f32 v210, v210, v211\n"
      "v_pk_mul_f32 v[150:151], v[150:151], v[204:205] op_sel_hi:[1,0]\n"
      "v_accvgpr_write_b32 a218, v126\n"
      "v_mfma_f32_16x16x32_bf16 v[174:177], v[184:187], a[248:251], v[102:105]\n"
      "s_waitcnt lgkmcnt(0)\n"
      "v_cvt_pk_bf16_f32 v207, v208, v209\n"
      "v_cvt_pk_bf16_f32 v116, v164, v165\n"
      "v_cvt_pk_bf16_f32 v152, v152, v153\n"
      "v_mfma_f32_16x16x32_bf16 v[126:129], v[138:141], v[240:243], v[106:109]\n"
      "v_cvt_pk_bf16_f32 v208, v188, v189\n"
      "v_cvt_pk_bf16_f32 v153, v154, v155\n"
      "v_mfma_f32_16x16x32_bf16 v[118:121], v[184:187], v[240:243], v[102:105]\n"
      "ds_write_b64 v4, v[152:153] offset:256\n"
      "v_accvgpr_write_b32 a247, v180\n"
      "v_mfma_f32_16x16x32_bf16 v[102:105], v[138:141], a[248:251], v[106:109]\n"
      "v_cvt_pk_bf16_f32 v88, v122, v123\n"
      "v_cvt_pk_bf16_f32 v176, v176, v177\n"
      "v_mfma_f32_16x16x32_bf16 v[180:183], v[184:187], v[236:239], v[160:163]\n"
      "s_add_u32 s43, s43, 1\n"
      "v_cvt_pk_bf16_f32 v117, v166, v167\n"
      "v_cvt_pk_bf16_f32 v209, v190, v191\n"
      "v_cvt_pk_bf16_f32 v242, v118, v119\n"
      "v_mfma_f32_16x16x32_bf16 v[188:191], v[138:141], a[220:223], v[156:159]\n"
      "v_cvt_pk_bf16_f32 v212, v212, v213\n"
      "v_cvt_pk_bf16_f32 v90, v112, 0\n"
      "v_mfma_f32_16x16x32_bf16 v[160:163], v[138:141], v[236:239], v[156:159]\n"
      "ds_read_b128 v[122:125], v8 offset:384\n"
      "v_accvgpr_write_b32 a244, v210\n"
      "v_mfma_f32_16x16x32_bf16 v[164:167], v[138:141], a[224:227], v[106:109]\n"
      "s_nop 2\n"
      "ds_read_b128 v[106:109], v8 offset:448\n"
      "v_cvt_pk_bf16_f32 v240, v126, v127\n"
      "v_accvgpr_write_b32 a222, v220\n"
      "v_mfma_f32_16x16x32_bf16 v[156:159], v[32:35], v[114:117], 0\n"
      "ds_write_b128 v3, v[206:209]\n"
      "v_mfma_f32_16x16x32_bf16 v[156:159], v[32:35], v[88:91], v[156:159]\n"
      "v_cvt_pk_bf16_f32 v174, v174, v175\n"
      "v_cvt_pk_bf16_f32 v238, v180, v181\n"
      "v_mfma_f32_16x16x32_bf16 v[218:221], v[130:133], v[114:117], 0\n"
      "s_and_b32 s86, s86, -4\n"
      "v_accvgpr_write_b32 a240, v200\n"
      "v_cvt_pk_bf16_f32 v104, v104, v105\n"
      "v_cvt_pk_bf16_f32 v149, v150, v151\n"
      "v_cvt_pk_bf16_f32 v239, v182, v183\n"
      "v_cvt_pk_bf16_f32 v236, v160, v161\n"
      "v_mfma_f32_16x16x32_bf16 v[218:221], v[80:83], v[88:91], v[218:221]\n"
      "s_waitcnt lgkmcnt(1)\n"
      "v_rcp_f32 v156, v156\n"
      "v_mfma_f32_16x16x32_bf16 v[180:183], v[138:141], a[228:231], v[122:125]\n"
      "ds_write_b16 v13, v168\n"
      "v_mfma_f32_16x16x32_bf16 v[206:209], v[138:141], a[252:255], v[122:125]\n"
      "v_cvt_pk_bf16_f32 v243, v120, v121\n"
      "v_accvgpr_write_b32 a251, v176\n"
      "v_mfma_f32_16x16x32_bf16 v[118:121], v[214:217], v[114:117], 0\n"
      "ds_read_b128 v[198:201], v8 offset:640\n"
      "v_cvt_pk_bf16_f32 v97, v98, v99\n"
      "v_mfma_f32_16x16x32_bf16 v[118:121], v[76:79], v[88:91], v[118:121]\n"
      "ds_read_b128 v[110:113], v8 offset:576\n"
      "v_cvt_pk_bf16_f32 v182, v182, v183\n"
      "v_cvt_pk_bf16_f32 v151, v144, v145\n"
      "v_mfma_f32_16x16x32_bf16 v[144:147], v[194:197], v[114:117], 0\n"
      "ds_read_b128 v[152:155], v8 offset:704\n"
      "v_cvt_pk_bf16_f32 v150, v142, v143\n"
      "v_cvt_pk_bf16_f32 v202, v202, v203\n"
      "v_mfma_f32_16x16x32_bf16 v[144:147], v[84:87], v[88:91], v[144:147]\n"
      "v_cvt_pk_bf16_f32 v206, v206, v207\n"
      "v_cvt_pk_bf16_f32 v208, v208, v209\n"
      "v_accvgpr_write_b32 a226, v134\n"
      "v_cvt_pk_bf16_f32 v188, v188, v189\n"
      "v_mfma_f32_16x16x32_bf16 v[98:101], v[184:187], a[252:255], v[106:109]\n"
      "v_cvt_pk_bf16_f32 v190, v190, v191\n"
      "v_accvgpr_write_b32 a250, v174\n"
      "v_cvt_pk_bf16_f32 v180, v180, v181\n"
      "v_accvgpr_write_b32 a252, v206\n"
      "v_mfma_f32_16x16x32_bf16 v[174:177], v[184:187], v[244:247], v[106:109]\n"
      "ds_write_b64 v4, v[96:97]\n"
      "v_mfma_f32_16x16x32_bf16 v[158:161], v[184:187], a[228:231], v[106:109]\n"
      "s_waitcnt lgkmcnt(2)\n"
      "v_accvgpr_write_b32 a223, v222\n"
      "v_accvgpr_write_b32 a216, v170\n"
      "v_mfma_f32_16x16x32_bf16 v[204:207], v[184:187], a[232:235], v[110:113]\n"
      "ds_read_b128 v[106:109], v8 offset:512\n"
      "v_cvt_pk_bf16_f32 v166, v166, v167\n"
      "v_mfma_f32_16x16x32_bf16 v[168:171], v[138:141], v[244:247], v[122:125]\n"
      "s_waitcnt lgkmcnt(2)\n"
      "v_cvt_pk_bf16_f32 v246, v174, v175\n"
      "v_accvgpr_write_b32 a245, v212\n"
      "v_cvt_pk_bf16_f32 v241, v128, v129\n"
      "v_cvt_pk_bf16_f32 v160, v160, v161\n"
      "v_mfma_f32_16x16x32_bf16 v[124:127], v[184:187], v[252:255], v[152:155]\n"
      "ds_read_b128 v[210:213], v14\n"
      "v_cvt_pk_bf16_f32 v98, v98, v99\n"
      "v_accvgpr_write_b32 a246, v178\n"
      "v_cvt_pk_bf16_f32 v100, v100, v101\n"
      "v_cvt_pk_bf16_f32 v245, v170, v171\n"
      "v_accvgpr_write_b32 a217, v172\n"
      "v_mfma_f32_16x16x32_bf16 v[114:117], v[138:141], a[236:239], v[198:201]\n"
      "v_cvt_pk_bf16_f32 v206, v206, v207\n"
      "v_cvt_pk_bf16_f32 v164, v164, v165\n"
      "v_cvt_pk_bf16_f32 v158, v158, v159\n"
      "v_accvgpr_write_b32 a224, v164\n"
      "v_accvgpr_write_b32 a254, v98\n"
      "v_pk_mul_f32 v[144:145], v[144:145], v[156:157] op_sel_hi:[1,0]\n"
      "v_pk_mul_f32 v[146:147], v[146:147], v[156:157] op_sel_hi:[1,0]\n"
      "v_accvgpr_write_b32 a225, v166\n"
      "v_pk_mul_f32 v[118:119], v[118:119], v[156:157] op_sel_hi:[1,0]\n"
      "v_mfma_f32_16x16x32_bf16 v[164:167], v[184:187], a[236:239], v[152:155]\n"
      "s_waitcnt lgkmcnt(1)\n"
      "v_accvgpr_write_b32 a228, v180\n"
      "v_cvt_pk_bf16_f32 v136, v136, v137\n"
      "v_cvt_pk_bf16_f32 v102, v102, v103\n"
      "v_accvgpr_write_b32 a249, v104\n"
      "v_cvt_pk_bf16_f32 v118, v118, v119\n"
      "v_cvt_pk_bf16_f32 v247, v176, v177\n"
      "v_pk_mul_f32 v[120:121], v[120:121], v[156:157] op_sel_hi:[1,0]\n"
      "v_pk_mul_f32 v[220:221], v[220:221], v[156:157] op_sel_hi:[1,0]\n"
      "v_pk_mul_f32 v[218:219], v[218:219], v[156:157] op_sel_hi:[1,0]\n"
      "v_mfma_f32_16x16x32_bf16 v[178:181], v[184:187], v[228:231], v[152:155]\n"
      "ds_write_b128 v3, v[148:151] offset:256\n"
      "v_mfma_f32_16x16x32_bf16 v[152:155], v[138:141], v[252:255], v[198:201]\n"
      "s_cmp_ge_u32 s95, 0x40000000\n"
      "s_cselect_b64 vcc, -1, s[56:57]\n"
      "v_cvt_pk_bf16_f32 v255, v126, v127\n"
      "v_mfma_f32_16x16x32_bf16 v[126:129], v[138:141], a[232:235], v[106:109]\n"
      "ds_read_b128 v[172:175], v14 offset:64\n"
      "v_accvgpr_write_b32 a255, v100\n"
      "v_accvgpr_write_b32 a231, v160\n"
      "v_mfma_f32_16x16x32_bf16 v[148:151], v[138:141], v[228:231], v[198:201]\n"
      "v_accvgpr_write_b32 a221, v190\n"
      "v_cvt_pk_bf16_f32 v119, v120, v121\n"
      "v_accvgpr_write_b32 a253, v208\n"
      "v_accvgpr_write_b32 a235, v206\n"
      "v_cvt_pk_bf16_f32 v252, v152, v153\n"
      "v_cvt_pk_bf16_f32 v114, v114, v115\n"
      "v_accvgpr_write_b32 a227, v136\n"
      "v_cvt_pk_bf16_f32 v237, v162, v163\n"
      "v_cvt_pk_bf16_f32 v144, v144, v145\n"
      "v_cvt_pk_bf16_f32 v121, v220, v221\n"
      "v_cvt_pk_bf16_f32 v164, v164, v165\n"
      "v_cvt_pk_bf16_f32 v145, v146, v147\n"
      "v_accvgpr_write_b32 a220, v188\n"
      "v_cvt_pk_bf16_f32 v228, v148, v149\n"
      "v_cvt_pk_bf16_f32 v116, v116, v117\n"
      "v_accvgpr_write_b32 a237, v116\n"
      "v_cvt_pk_bf16_f32 v204, v204, v205\n"
      "v_mfma_f32_16x16x32_bf16 v[188:191], v[184:187], v[248:251], v[110:113]\n"
      "ds_write_b64 v4, v[144:145] offset:512\n"
      "v_mfma_f32_16x16x32_bf16 v[144:147], v[138:141], v[224:227], v[106:109]\n"
      "v_cvt_pk_bf16_f32 v128, v128, v129\n"
      "v_cvt_pk_bf16_f32 v120, v218, v219\n"
      "v_cvt_pk_bf16_f32 v244, v168, v169\n"
      "v_mfma_f32_16x16x32_bf16 v[218:221], v[184:187], v[224:227], v[110:113]\n"
      "s_waitcnt lgkmcnt(1)\n"
      "v_accvgpr_write_b32 a234, v204\n"
      "v_cvt_pk_bf16_f32 v126, v126, v127\n"
      "v_accvgpr_write_b32 a230, v158\n"
      "v_mfma_f32_16x16x32_bf16 v[184:187], v[138:141], v[248:251], v[106:109]\n"
      "ds_write_b128 v3, v[118:121] offset:512\n"
      "v_cvt_pk_bf16_f32 v253, v154, v155\n"
      "v_cvt_pk_bf16_f32 v166, v166, v167\n"
      "v_cvt_pk_bf16_f32 v230, v178, v179\n"
      "v_accvgpr_write_b32 a233, v128\n"
      "v_cvt_pk_bf16_f32 v225, v146, v147\n"
      "v_accvgpr_write_b32 a238, v164\n"
      "v_cvt_pk_bf16_f32 v226, v218, v219\n"
      "v_accvgpr_write_b32 a241, v202\n"
      "v_cvt_pk_bf16_f32 v251, v190, v191\n"
      "v_accvgpr_write_b32 a232, v126\n"
      "v_cvt_pk_bf16_f32 v231, v180, v181\n"
      "v_cvt_pk_bf16_f32 v250, v188, v189\n"
      "v_cvt_pk_bf16_f32 v224, v144, v145\n"
      "v_mfma_f32_16x16x32_bf16 v[188:191], v[210:213], v[32:35], 0\n"
      "s_add_u32 s42, s42, 1\n"
      "v_cvt_pk_bf16_f32 v229, v150, v151\n"
      "v_accvgpr_write_b32 a248, v102\n"
      "v_cvt_pk_bf16_f32 v249, v186, v187\n"
      "v_accvgpr_write_b32 a239, v166\n"
      "v_cvt_pk_bf16_f32 v248, v184, v185\n"
      "v_cvt_pk_bf16_f32 v227, v220, v221\n"
      "v_accvgpr_write_b32 a229, v182\n"
      "v_cvt_pk_bf16_f32 v254, v124, v125\n"
      "v_accvgpr_write_b32 a236, v114\n"
      "v_mfma_f32_16x16x32_bf16 v[188:191], v[172:175], v[32:35], v[188:191]\n"
      "s_and_b32 s5, s84, s8\n"
      "v_cndmask_b32 v15, v15, v39, s[14:15]\n"
      "s_waitcnt lgkmcnt(0)\n"
      "s_barrier\n"
      "s_or_b64 vcc, vcc, s[54:55]\n"
      "s_cbranch_vccnz L_disp1_%=\n"
      "L_loop1_%=:\n"
      "v_mfma_f32_16x16x32_bf16 v[120:123], v[210:213], v[214:217], 0\n"
      "ds_read_b128 v[202:205], v0 offset:18432\n"
      "v_mfma_f32_16x16x32_bf16 v[120:123], v[172:175], v[76:79], v[120:123]\n"
      "ds_read_b128 v[138:141], v9\n"
      "ds_read_b128 v[142:145], v0 offset:24576\n"
      "v_mfma_f32_16x16x32_bf16 v[148:151], v[210:213], v[130:133], 0\n"
      "ds_read_b128 v[206:209], v1 offset:30720\n"
      "s_add_u32 m0, s64, 0\n"
      "v_mfma_f32_16x16x32_bf16 v[148:151], v[172:175], v[80:83], v[148:151]\n"
      "buffer_load_dwordx4 v16, s[16:19], s84 offen  lds\n"
      "v_mfma_f32_16x16x32_bf16 v[178:181], v[210:213], v[194:197], 0\n"
      "ds_read_b128 v[126:129], v1 offset:18432\n"
      "s_add_u32 m0, s64, 4096\n"
      "v_mfma_f32_16x16x32_bf16 v[178:181], v[172:175], v[84:87], v[178:181]\n"
      "ds_read_b128 v[156:159], v1 offset:24576\n"
      "s_waitcnt lgkmcnt(5)\n"
      "v_mfma_f32_16x16x32_bf16 v[112:115], a[0:3], v[202:205], 0\n"
      "ds_read_b128 v[172:175], v1 offset:26624\n"
      "v_readlane_b32 s46, v37, s42\n"
      "v_mfma_f32_16x16x32_bf16 v[100:103], a[24:27], v[202:205], 0\n"
      "s_waitcnt lgkmcnt(5)\n"
      "v_add_f32 v140, v140, v141\n"
      "v_add_f32 v138, v138, v139\n"
      "v_mfma_f32_16x16x32_bf16 v[198:201], a[48:51], v[202:205], 0\n"
      "ds_read_b128 v[104:107], v0 offset:30720\n"
      "v_add_f32 v138, v138, v140\n"
      "v_mfma_f32_16x16x32_bf16 v[180:183], a[72:75], v[202:205], 0\n"
      "buffer_load_dwordx4 v17, s[16:19], s84 offen  lds\n"
      "v_mfma_f32_16x16x32_bf16 v[210:213], a[96:99], v[202:205], 0\n"
      "s_waitcnt lgkmcnt(3)\n"
      "v_rsq_f32 v138, v138\n"
      "v_mfma_f32_16x16x32_bf16 v[218:221], a[120:123], v[202:205], 0\n"
      "s_add_u32 m0, s64, 8192\n"
      "v_mul_f32 v138, 0x415db3d7, v138\n"
      "v_mov_b32_e64 v140, v138\n"
      "v_mfma_f32_16x16x32_bf16 v[112:115], a[4:7], v[126:129], v[112:115]\n"
      "s_nop 0\n"
      "v_permlane32_swap_b32 v138, v140\n"
      "v_mfma_f32_16x16x32_bf16 v[112:115], a[8:11], v[142:145], v[112:115]\n"
      "ds_read_b128 v[122:125], v9 offset:4608\n"
      "v_cndmask_b32 v120, v120, v148, s[58:59]\n"
      "v_mfma_f32_16x16x32_bf16 v[100:103], a[28:31], v[126:129], v[100:103]\n"
      "v_mov_b32 v139, v138\n"
      "v_mul_f32 v97, v140, v97\n"
      "v_mfma_f32_16x16x32_bf16 v[100:103], a[32:35], v[142:145], v[100:103]\n"
      "ds_read_b128 v[146:149], v1 offset:20480\n"
      "v_readlane_b32 s47, v38, s43\n"
      "v_mfma_f32_16x16x32_bf16 v[198:201], a[52:55], v[126:129], v[198:201]\n"
      "buffer_load_dwordx4 v18, s[16:19], s84 offen  lds\n"
      "v_mfma_f32_16x16x32_bf16 v[198:201], a[56:59], v[142:145], v[198:201]\n"
      "v_permlane16_swap_b32 v138, v139\n"
      "v_mfma_f32_16x16x32_bf16 v[180:183], a[76:79], v[126:129], v[180:183]\n"
      "v_mul_f32 v163, v139, v163\n"
      "v_mul_f32_e64 v166, v139, v166\n"
      "v_mfma_f32_16x16x32_bf16 v[180:183], a[80:83], v[142:145], v[180:183]\n"
      "s_add_u32 m0, s64, 12288\n"
      "v_mul_f32 v171, v138, v171\n"
      "v_mul_f32 v185, v138, v185\n"
      "v_mfma_f32_16x16x32_bf16 v[210:213], a[100:103], v[126:129], v[210:213]\n"
      "v_mul_f32 v160, v139, v160\n"
      "v_mul_f32 v164, v139, v164\n"
      "v_mfma_f32_16x16x32_bf16 v[210:213], a[104:107], v[142:145], v[210:213]\n"
      "buffer_load_dwordx4 v19, s[16:19], s84 offen  lds\n"
      "v_mfma_f32_16x16x32_bf16 v[218:221], a[124:127], v[126:129], v[218:221]\n"
      "v_mul_f32 v161, v139, v161\n"
      "v_mul_f32 v187, v138, v187\n"
      "v_mfma_f32_16x16x32_bf16 v[218:221], a[128:131], v[142:145], v[218:221]\n"
      "s_waitcnt lgkmcnt(0)\n"
      "v_mul_f32 v153, v138, v153\n"
      "v_mul_f32_e64 v152, v138, v152\n"
      "v_mfma_f32_16x16x32_bf16 v[112:115], a[12:15], v[156:159], v[112:115]\n"
      "v_mul_f32 v155, v138, v155\n"
      "v_mul_f32 v165, v139, v165\n"
      "v_mfma_f32_16x16x32_bf16 v[112:115], a[16:19], v[104:107], v[112:115]\n"
      "v_cvt_pk_bf16_f32 v160, v160, v161\n"
      "v_mul_f32 v111, v140, v111\n"
      "v_mfma_f32_16x16x32_bf16 v[112:115], a[20:23], v[206:209], v[112:115]\n"
      "s_mov_b32 m0, s6\n"
      "v_mul_f32_e64 v184, v138, v184\n"
      "v_cndmask_b32 v120, v120, v178, s[60:61]\n"
      "v_mfma_f32_16x16x32_bf16 v[100:103], a[36:39], v[156:159], v[100:103]\n"
      "v_mov_b32_dpp v188, v188 quad_perm:[0,0,0,0] row_mask:0xf bank_mask:0xf\n"
      "v_cvt_pk_bf16_f32 v184, v184, v185\n"
      "v_mfma_f32_16x16x32_bf16 v[100:103], a[40:43], v[104:107], v[100:103]\n"
      "v_mul_f32 v186, v138, v186\n"
      "v_fma_f32 v141, v112, v112, s51\n"
      "v_mfma_f32_16x16x32_bf16 v[100:103], a[44:47], v[206:209], v[100:103]\n"
      "v_cvt_pk_bf16_f32 v185, v186, v187\n"
      "v_mul_f32 v134, v139, v134\n"
      "v_mfma_f32_16x16x32_bf16 v[198:201], a[60:63], v[156:159], v[198:201]\n"
      "buffer_load_dwordx4 v15, s[0:3], s5 offen  lds\n"
      "v_mfma_f32_16x16x32_bf16 v[198:201], a[64:67], v[104:107], v[198:201]\n"
      "v_rcp_f32_e64 v188, v188\n"
      "v_mfma_f32_16x16x32_bf16 v[198:201], a[68:71], v[206:209], v[198:201]\n"
      "v_mul_f32_e64 v154, v138, v154\n"
      "v_cvt_pk_bf16_f32 v186, v152, v153\n"
      "v_mfma_f32_16x16x32_bf16 v[180:183], a[84:87], v[156:159], v[180:183]\n"
      "v_cvt_pk_bf16_f32 v187, v154, v155\n"
      "v_fmac_f32_e64 v141, v113, v113\n"
      "v_mfma_f32_16x16x32_bf16 v[180:183], a[88:91], v[104:107], v[180:183]\n"
      "ds_read_b128 v[150:153], v0 offset:26624\n"
      "v_or3_b32 v122, v122, v123, v124\n"
      "v_mfma_f32_16x16x32_bf16 v[180:183], a[92:95], v[206:209], v[180:183]\n"
      "buffer_load_dwordx4 v[176:179], v36, s[28:31], s86 offen\n"
      "v_mfma_f32_16x16x32_bf16 v[210:213], a[108:111], v[156:159], v[210:213]\n"
      "v_mul_f32 v135, v139, v135\n"
      "v_fmac_f32 v141, v114, v114\n"
      "v_mfma_f32_16x16x32_bf16 v[210:213], a[112:115], v[104:107], v[210:213]\n"
      "ds_write_b128 v5, v[184:187] offset:18432\n"
      "v_mfma_f32_16x16x32_bf16 v[210:213], a[116:119], v[206:209], v[210:213]\n"
      "v_mul_f32 v162, v139, v162\n"
      "v_fmac_f32 v141, v115, v115\n"
      "v_mfma_f32_16x16x32_bf16 v[218:221], a[132:135], v[156:159], v[218:221]\n"
      "v_fma_f32 v76, v180, v180, s51\n"
      "v_mul_f32_e64 v167, v139, v167\n"
      "v_mfma_f32_16x16x32_bf16 v[218:221], a[136:139], v[104:107], v[218:221]\n"
      "v_fmac_f32 v76, v181, v181\n"
      "v_fmac_f32 v141, v100, v100\n"
      "v_mfma_f32_16x16x32_bf16 v[218:221], a[140:143], v[206:209], v[218:221]\n"
      "v_mul_f32 v136, v139, v136\n"
      "v_fmac_f32 v76, v182, v182\n"
      "v_mfma_f32_16x16x32_bf16 v[214:217], v[202:205], a[144:147], 0\n"
      "v_mul_f32_e64 v116, v140, v116\n"
      "v_mfma_f32_16x16x32_bf16 v[214:217], v[126:129], a[148:151], v[214:217]\n"
      "buffer_load_dword v189, v29, s[28:31], s86 offen\n"
      "v_mfma_f32_16x16x32_bf16 v[214:217], v[142:145], a[152:155], v[214:217]\n"
      "ds_read_b128 v[184:187], v0 offset:20480\n"
      "v_mul_f32_e64 v168, v138, v168\n"
      "v_mfma_f32_16x16x32_bf16 v[214:217], v[156:159], a[156:159], v[214:217]\n"
      "v_cvt_pk_bf16_f32 v161, v162, v163\n"
      "v_mul_f32 v137, v139, v137\n"
      "v_mfma_f32_16x16x32_bf16 v[214:217], v[104:107], a[160:163], v[214:217]\n"
      "v_mul_f32 v170, v138, v170\n"
      "v_cvt_pk_bf16_f32 v162, v164, v165\n"
      "v_mfma_f32_16x16x32_bf16 v[214:217], v[206:209], a[164:167], v[214:217]\n"
      "v_mul_f32 v109, v140, v109\n"
      "v_mul_f32 v110, v140, v110\n"
      "v_mfma_f32_16x16x32_bf16 v[130:133], v[202:205], a[168:171], 0\n"
      "v_cvt_pk_bf16_f32 v163, v166, v167\n"
      "v_fmac_f32 v76, v183, v183\n"
      "v_mfma_f32_16x16x32_bf16 v[130:133], v[126:129], a[172:175], v[130:133]\n"
      "v_mul_f32 v119, v140, v119\n"
      "v_fmac_f32_e64 v76, v210, v210\n"
      "v_mfma_f32_16x16x32_bf16 v[130:133], v[142:145], a[176:179], v[130:133]\n"
      "ds_write_b128 v5, v[160:163] offset:24576\n"
      "v_mfma_f32_16x16x32_bf16 v[130:133], v[156:159], a[180:183], v[130:133]\n"
      "v_fmac_f32_e64 v141, v101, v101\n"
      "v_cvt_pk_bf16_f32 v214, v214, v215\n"
      "v_mfma_f32_16x16x32_bf16 v[130:133], v[104:107], a[184:187], v[130:133]\n"
      "v_fmac_f32 v141, v102, v102\n"
      "v_fmac_f32 v76, v211, v211\n"
      "v_mfma_f32_16x16x32_bf16 v[130:133], v[206:209], a[188:191], v[130:133]\n"
      "ds_read_b128 v[160:163], v1 offset:32768\n"
      "v_mul_f32 v169, v138, v169\n"
      "v_mfma_f32_16x16x32_bf16 v[194:197], v[202:205], a[192:195], 0\n"
      "v_cvt_pk_bf16_f32 v134, v134, v135\n"
      "v_fmac_f32 v76, v212, v212\n"
      "v_mfma_f32_16x16x32_bf16 v[194:197], v[126:129], a[196:199], v[194:197]\n"
      "v_readlane_b32 s45, v37, s43\n"
      "v_mul_f32 v98, v140, v98\n"
      "v_mfma_f32_16x16x32_bf16 v[194:197], v[142:145], a[200:203], v[194:197]\n"
      "s_waitcnt lgkmcnt(2)\n"
      "v_bitop3_b32 v122, v122, v125, s33 bitop3:0xa8\n"
      "v_fmac_f32 v76, v213, v213\n"
      "v_mfma_f32_16x16x32_bf16 v[194:197], v[156:159], a[204:207], v[194:197]\n"
      "ds_read_b128 v[202:205], v0 offset:32768\n"
      "v_mul_f32 v108, v140, v108\n"
      "v_mfma_f32_16x16x32_bf16 v[194:197], v[104:107], a[208:211], v[194:197]\n"
      "s_or_b32 s95, s45, s46\n"
      "v_fmac_f32 v76, v218, v218\n"
      "v_fmac_f32 v76, v219, v219\n"
      "v_mfma_f32_16x16x32_bf16 v[194:197], v[206:209], a[212:215], v[194:197]\n"
      "ds_read_b128 v[164:167], v0 offset:28672\n"
      "v_mul_f32 v118, v140, v118\n"
      "v_mfma_f32_16x16x32_bf16 v[126:129], a[0:3], v[184:187], 0\n"
      "v_fmac_f32 v76, v220, v220\n"
      "v_fmac_f32 v76, v221, v221\n"
      "v_mfma_f32_16x16x32_bf16 v[126:129], a[4:7], v[146:149], v[126:129]\n"
      "v_cvt_pk_bf16_f32 v168, v168, v169\n"
      "v_fmac_f32 v141, v103, v103\n"
      "v_mfma_f32_16x16x32_bf16 v[126:129], a[8:11], v[150:153], v[126:129]\n"
      "v_fmac_f32 v141, v198, v198\n"
      "v_cmp_ne_u32 s[56:57], 0, v122\n"
      "v_mfma_f32_16x16x32_bf16 v[126:129], a[12:15], v[172:175], v[126:129]\n"
      "s_waitcnt lgkmcnt(1)\n"
      "v_mul_f32 v96, v140, v96\n"
      "v_fmac_f32 v141, v199, v199\n"
      "v_mfma_f32_16x16x32_bf16 v[126:129], a[16:19], v[202:205], v[126:129]\n"
      "v_mul_f32 v117, v140, v117\n"
      "v_fmac_f32_e64 v141, v200, v200\n"
      "v_mfma_f32_16x16x32_bf16 v[126:129], a[20:23], v[160:163], v[126:129]\n"
      "v_cvt_pk_bf16_f32 v108, v108, v109\n"
      "v_fmac_f32 v141, v201, v201\n"
      "v_mfma_f32_16x16x32_bf16 v[142:145], a[24:27], v[184:187], 0\n"
      "s_or_b32 s95, s95, s48\n"
      "v_mul_f32 v120, v188, v120\n"
      "v_cvt_pk_bf16_f32 v109, v110, v111\n"
      "v_mfma_f32_16x16x32_bf16 v[142:145], a[28:31], v[146:149], v[142:145]\n"
      "v_permlane16_swap_b32 v141, v76\n"
      "v_mfma_f32_16x16x32_bf16 v[142:145], a[32:35], v[150:153], v[142:145]\n"
      "v_fma_f32 v158, v126, v126, s51\n"
      "v_cvt_pk_bf16_f32 v169, v170, v171\n"
      "v_mfma_f32_16x16x32_bf16 v[142:145], a[36:39], v[172:175], v[142:145]\n"
      "ds_write_b64 v6, v[168:169] offset:18432\n"
      "v_mfma_f32_16x16x32_bf16 v[142:145], a[40:43], v[202:205], v[142:145]\n"
      "v_add_f32 v141, v141, v76\n"
      "v_fmac_f32 v158, v127, v127\n"
      "v_mfma_f32_16x16x32_bf16 v[142:145], a[44:47], v[160:163], v[142:145]\n"
      "v_fmac_f32 v158, v128, v128\n"
      "v_fmac_f32 v158, v129, v129\n"
      "v_mfma_f32_16x16x32_bf16 v[190:193], a[48:51], v[184:187], 0\n"
      "v_cvt_pk_bf16_f32 v135, v136, v137\n"
      "v_mov_b32 v76, v141\n"
      "v_mfma_f32_16x16x32_bf16 v[190:193], a[52:55], v[146:149], v[190:193]\n"
      "s_nop 0\n"
      "v_permlane32_swap_b32 v141, v76\n"
      "v_mfma_f32_16x16x32_bf16 v[190:193], a[56:59], v[150:153], v[190:193]\n"
      "v_cvt_pk_bf16_f32 v130, v130, v131\n"
      "v_fmac_f32 v158, v142, v142\n"
      "v_mfma_f32_16x16x32_bf16 v[190:193], a[60:63], v[172:175], v[190:193]\n"
      "ds_write_b64 v6, v[134:135] offset:24576\n"
      "v_mfma_f32_16x16x32_bf16 v[190:193], a[64:67], v[202:205], v[190:193]\n"
      "v_add_f32_e64 v141, v141, v76\n"
      "v_cvt_pk_bf16_f32 v110, v116, v117\n"
      "v_mfma_f32_16x16x32_bf16 v[190:193], a[68:71], v[160:163], v[190:193]\n"
      "v_cvt_pk_bf16_f32 v194, v194, v195\n"
      "v_fmac_f32 v158, v143, v143\n"
      "v_mfma_f32_16x16x32_bf16 v[122:125], a[72:75], v[184:187], 0\n"
      "v_rsq_f32 v141, v141\n"
      "v_mfma_f32_16x16x32_bf16 v[122:125], a[76:79], v[146:149], v[122:125]\n"
      "v_mul_f32 v99, v140, v99\n"
      "v_fmac_f32 v158, v144, v144\n"
      "v_mfma_f32_16x16x32_bf16 v[122:125], a[80:83], v[150:153], v[122:125]\n"
      "v_cvt_pk_bf16_f32 v111, v118, v119\n"
      "v_mov_b32 v76, v141\n"
      "v_mfma_f32_16x16x32_bf16 v[122:125], a[84:87], v[172:175], v[122:125]\n"
      "ds_write_b128 v5, v[108:111] offset:30720\n"
      "v_mfma_f32_16x16x32_bf16 v[122:125], a[88:91], v[202:205], v[122:125]\n"
      "v_permlane16_swap_b32 v141, v76\n"
      "v_mfma_f32_16x16x32_bf16 v[122:125], a[92:95], v[160:163], v[122:125]\n"
      "v_cvt_pk_bf16_f32 v131, v132, v133\n"
      "v_mul_f32 v141, s50, v141\n"
      "v_mfma_f32_16x16x32_bf16 v[206:209], a[96:99], v[184:187], 0\n"
      "v_mul_f32 v115, v141, v115\n"
      "v_mul_f32 v199, v141, v199\n"
      "v_mfma_f32_16x16x32_bf16 v[206:209], a[100:103], v[146:149], v[206:209]\n"
      "v_fmac_f32 v158, v145, v145\n"
      "v_mul_f32_e64 v113, v141, v113\n"
      "v_mfma_f32_16x16x32_bf16 v[206:209], a[104:107], v[150:153], v[206:209]\n"
      "v_cvt_pk_bf16_f32 v96, v96, v97\n"
      "v_fma_f32 v159, v122, v122, s51\n"
      "v_mfma_f32_16x16x32_bf16 v[206:209], a[108:111], v[172:175], v[206:209]\n"
      "v_cvt_pk_bf16_f32 v120, v120, v120\n"
      "v_fmac_f32 v159, v123, v123\n"
      "v_mfma_f32_16x16x32_bf16 v[206:209], a[112:115], v[202:205], v[206:209]\n"
      "v_mul_f32 v221, v76, v221\n"
      "v_cvt_pk_bf16_f32 v215, v216, v217\n"
      "v_mfma_f32_16x16x32_bf16 v[206:209], a[116:119], v[160:163], v[206:209]\n"
      "v_mul_f32 v180, v76, v180\n"
      "v_fmac_f32 v158, v190, v190\n"
      "v_mfma_f32_16x16x32_bf16 v[116:119], a[120:123], v[184:187], 0\n"
      "v_fmac_f32 v159, v124, v124\n"
      "v_mul_f32 v112, v141, v112\n"
      "v_mfma_f32_16x16x32_bf16 v[116:119], a[124:127], v[146:149], v[116:119]\n"
      "v_mul_f32 v182, v76, v182\n"
      "v_cvt_pk_bf16_f32 v112, v112, v113\n"
      "v_mfma_f32_16x16x32_bf16 v[116:119], a[128:131], v[150:153], v[116:119]\n"
      "v_cvt_pk_bf16_f32 v97, v98, v99\n"
      "v_mul_f32 v100, v141, v100\n"
      "v_mfma_f32_16x16x32_bf16 v[116:119], a[132:135], v[172:175], v[116:119]\n"
      "v_mul_f32 v212, v76, v212\n"
      "v_fmac_f32 v159, v125, v125\n"
      "v_mfma_f32_16x16x32_bf16 v[116:119], a[136:139], v[202:205], v[116:119]\n"
      "v_fmac_f32 v158, v191, v191\n"
      "v_fmac_f32 v159, v206, v206\n"
      "v_mfma_f32_16x16x32_bf16 v[116:119], a[140:143], v[160:163], v[116:119]\n"
      "v_fmac_f32 v159, v207, v207\n"
      "v_fmac_f32 v159, v208, v208\n"
      "v_mfma_f32_16x16x32_bf16 v[132:135], v[184:187], a[144:147], 0\n"
      "v_mul_f32 v198, v141, v198\n"
      "v_mul_f32 v200, v141, v200\n"
      "v_mfma_f32_16x16x32_bf16 v[132:135], v[146:149], a[148:151], v[132:135]\n"
      "ds_read_b128 v[168:171], v0 offset:22528\n"
      "v_fmac_f32 v159, v209, v209\n"
      "v_mfma_f32_16x16x32_bf16 v[132:135], v[150:153], a[152:155], v[132:135]\n"
      "v_mul_f32 v211, v76, v211\n"
      "v_fmac_f32 v159, v116, v116\n"
      "v_mfma_f32_16x16x32_bf16 v[132:135], v[172:175], a[156:159], v[132:135]\n"
      "v_mul_f32 v201, v141, v201\n"
      "v_fmac_f32_e64 v158, v192, v192\n"
      "v_mfma_f32_16x16x32_bf16 v[132:135], v[202:205], a[160:163], v[132:135]\n"
      "ds_write_b64 v6, v[96:97] offset:30720\n"
      "v_mfma_f32_16x16x32_bf16 v[132:135], v[160:163], a[164:167], v[132:135]\n"
      "v_mul_f32 v213, v76, v213\n"
      "v_fmac_f32 v159, v117, v117\n"
      "v_mfma_f32_16x16x32_bf16 v[136:139], v[184:187], a[168:171], 0\n"
      "ds_read_b128 v[154:157], v1 offset:22528\n"
      "v_cvt_pk_bf16_f32 v198, v198, v199\n"
      "v_mfma_f32_16x16x32_bf16 v[136:139], v[146:149], a[172:175], v[136:139]\n"
      "v_fmac_f32 v159, v118, v118\n"
      "v_fmac_f32 v159, v119, v119\n"
      "v_mfma_f32_16x16x32_bf16 v[136:139], v[150:153], a[176:179], v[136:139]\n"
      "ds_read_b128 v[96:99], v1 offset:34816\n"
      "v_cvt_pk_bf16_f32 v199, v200, v201\n"
      "v_mfma_f32_16x16x32_bf16 v[136:139], v[172:175], a[180:183], v[136:139]\n"
      "v_fmac_f32 v158, v193, v193\n"
      "v_mul_f32 v219, v76, v219\n"
      "v_mfma_f32_16x16x32_bf16 v[136:139], v[202:205], a[184:187], v[136:139]\n"
      "v_permlane16_swap_b32 v158, v159\n"
      "v_mfma_f32_16x16x32_bf16 v[136:139], v[160:163], a[188:191], v[136:139]\n"
      "v_mul_f32 v181, v76, v181\n"
      "v_mul_f32 v103, v141, v103\n"
      "v_mfma_f32_16x16x32_bf16 v[104:107], v[184:187], a[192:195], 0\n"
      "v_add_f32 v158, v158, v159\n"
      "v_mov_b32_e64 v159, v158\n"
      "v_mfma_f32_16x16x32_bf16 v[104:107], v[146:149], a[196:199], v[104:107]\n"
      "ds_read_b128 v[184:187], v1 offset:28672\n"
      "v_mul_f32 v183, v76, v183\n"
      "v_mfma_f32_16x16x32_bf16 v[104:107], v[150:153], a[200:203], v[104:107]\n"
      "v_permlane32_swap_b32 v158, v159\n"
      "v_mfma_f32_16x16x32_bf16 v[104:107], v[172:175], a[204:207], v[104:107]\n"
      "ds_read_b128 v[150:153], v0 offset:34816\n"
      "v_mul_f32_e64 v114, v141, v114\n"
      "v_mfma_f32_16x16x32_bf16 v[104:107], v[202:205], a[208:211], v[104:107]\n"
      "v_cvt_pk_bf16_f32 v180, v180, v181\n"
      "v_cvt_pk_bf16_f32 v216, v132, v133\n"
      "v_mfma_f32_16x16x32_bf16 v[104:107], v[160:163], a[212:215], v[104:107]\n"
      "s_waitcnt lgkmcnt(2)\n"
      "v_mul_f32 v218, v76, v218\n"
      "v_add_f32_e64 v158, v158, v159\n"
      "v_mfma_f32_16x16x32_bf16 v[146:149], a[0:3], v[168:171], 0\n"
      "s_cmp_ge_u32 s95, 0x40000000\n"
      "s_cselect_b32 s94, -1, -2\n"
      "v_cvt_pk_bf16_f32 v195, v196, v197\n"
      "v_mfma_f32_16x16x32_bf16 v[146:149], a[4:7], v[154:157], v[146:149]\n"
      "v_cvt_pk_bf16_f32 v218, v218, v219\n"
      "v_cvt_pk_bf16_f32 v132, v136, v137\n"
      "v_mfma_f32_16x16x32_bf16 v[146:149], a[8:11], v[164:167], v[146:149]\n"
      "s_waitcnt lgkmcnt(1)\n"
      "v_rsq_f32 v158, v158\n"
      "v_mfma_f32_16x16x32_bf16 v[146:149], a[12:15], v[184:187], v[146:149]\n"
      "s_waitcnt lgkmcnt(0)\n"
      "v_mul_f32 v210, v76, v210\n"
      "v_mov_b32 v159, v158\n"
      "v_mfma_f32_16x16x32_bf16 v[146:149], a[16:19], v[150:153], v[146:149]\n"
      "s_nop 0\n"
      "v_permlane16_swap_b32 v158, v159\n"
      "v_mfma_f32_16x16x32_bf16 v[146:149], a[20:23], v[96:99], v[146:149]\n"
      "v_mul_f32 v158, s50, v158\n"
      "v_mul_f32_e64 v127, v158, v127\n"
      "v_mfma_f32_16x16x32_bf16 v[160:163], a[24:27], v[168:171], 0\n"
      "ds_write_b16 v12, v120\n"
      "v_mfma_f32_16x16x32_bf16 v[160:163], a[28:31], v[154:157], v[160:163]\n"
      "v_mul_f32_e64 v126, v158, v126\n"
      "v_cvt_pk_bf16_f32 v126, v126, v127\n"
      "v_mfma_f32_16x16x32_bf16 v[160:163], a[32:35], v[164:167], v[160:163]\n"
      "v_mul_f32 v193, v158, v193\n"
      "v_mul_f32 v142, v158, v142\n"
      "v_mfma_f32_16x16x32_bf16 v[160:163], a[36:39], v[184:187], v[160:163]\n"
      "v_fma_f32 v201, v146, v146, s51\n"
      "v_mul_f32 v191, v158, v191\n"
      "v_mfma_f32_16x16x32_bf16 v[160:163], a[40:43], v[150:153], v[160:163]\n"
      "v_mul_f32 v145, v158, v145\n"
      "v_fmac_f32 v201, v147, v147\n"
      "v_mfma_f32_16x16x32_bf16 v[160:163], a[44:47], v[96:99], v[160:163]\n"
      "v_mul_f32 v101, v141, v101\n"
      "v_mul_f32 v128, v158, v128\n"
      "v_mfma_f32_16x16x32_bf16 v[108:111], a[48:51], v[168:171], 0\n"
      "v_mul_f32 v190, v158, v190\n"
      "v_mul_f32 v129, v158, v129\n"
      "v_mfma_f32_16x16x32_bf16 v[108:111], a[52:55], v[154:157], v[108:111]\n"
      "v_mul_f32 v122, v159, v122\n"
      "v_fmac_f32 v201, v148, v148\n"
      "v_mfma_f32_16x16x32_bf16 v[108:111], a[56:59], v[164:167], v[108:111]\n"
      "v_cvt_pk_bf16_f32 v181, v182, v183\n"
      "v_mul_f32 v208, v159, v208\n"
      "v_mfma_f32_16x16x32_bf16 v[108:111], a[60:63], v[184:187], v[108:111]\n"
      "v_mul_f32 v143, v158, v143\n"
      "v_mul_f32 v123, v159, v123\n"
      "v_mfma_f32_16x16x32_bf16 v[108:111], a[64:67], v[150:153], v[108:111]\n"
      "v_mul_f32 v144, v158, v144\n"
      "v_mul_f32 v209, v159, v209\n"
      "v_mfma_f32_16x16x32_bf16 v[108:111], a[68:71], v[96:99], v[108:111]\n"
      "v_mul_f32 v220, v76, v220\n"
      "v_cvt_pk_bf16_f32 v127, v128, v129\n"
      "v_mfma_f32_16x16x32_bf16 v[202:205], a[72:75], v[168:171], 0\n"
      "v_cvt_pk_bf16_f32 v113, v114, v115\n"
      "v_fmac_f32 v201, v149, v149\n"
      "v_mfma_f32_16x16x32_bf16 v[202:205], a[76:79], v[154:157], v[202:205]\n"
      "v_fmac_f32 v201, v160, v160\n"
      "v_cvt_pk_bf16_f32 v197, v106, v107\n"
      "v_mfma_f32_16x16x32_bf16 v[202:205], a[80:83], v[164:167], v[202:205]\n"
      "v_mul_f32 v117, v159, v117\n"
      "v_mul_f32_e64 v119, v159, v119\n"
      "v_mfma_f32_16x16x32_bf16 v[202:205], a[84:87], v[184:187], v[202:205]\n"
      "v_cvt_pk_bf16_f32 v217, v134, v135\n"
      "v_cvt_pk_bf16_f32 v183, v212, v213\n"
      "v_mfma_f32_16x16x32_bf16 v[202:205], a[88:91], v[150:153], v[202:205]\n"
      "v_cvt_pk_bf16_f32 v190, v190, v191\n"
      "v_mul_f32_e64 v207, v159, v207\n"
      "v_mfma_f32_16x16x32_bf16 v[202:205], a[92:95], v[96:99], v[202:205]\n"
      "v_cvt_pk_bf16_f32 v114, v100, v101\n"
      "v_mul_f32 v116, v159, v116\n"
      "v_mfma_f32_16x16x32_bf16 v[134:137], a[96:99], v[168:171], 0\n"
      "ds_read_b128 v[172:175], v2\n"
      "v_mul_f32 v102, v141, v102\n"
      "v_mfma_f32_16x16x32_bf16 v[134:137], a[100:103], v[154:157], v[134:137]\n"
      "v_mul_f32_e64 v206, v159, v206\n"
      "v_cvt_pk_bf16_f32 v129, v144, v145\n"
      "v_mfma_f32_16x16x32_bf16 v[134:137], a[104:107], v[164:167], v[134:137]\n"
      "v_cvt_pk_bf16_f32 v219, v220, v221\n"
      "v_fmac_f32 v201, v161, v161\n"
      "v_mfma_f32_16x16x32_bf16 v[134:137], a[108:111], v[184:187], v[134:137]\n"
      "s_and_b32 s47, s47, -4\n"
      "v_mul_f32_e64 v124, v159, v124\n"
      "v_cvt_pk_bf16_f32 v115, v102, v103\n"
      "v_mfma_f32_16x16x32_bf16 v[134:137], a[112:115], v[150:153], v[134:137]\n"
      "v_mul_f32_e64 v125, v159, v125\n"
      "v_cvt_pk_bf16_f32 v122, v122, v123\n"
      "v_mfma_f32_16x16x32_bf16 v[134:137], a[116:119], v[96:99], v[134:137]\n"
      "s_and_b32 s4, s45, s8\n"
      "v_fma_f32 v120, v202, v202, s51\n"
      "v_fmac_f32 v120, v203, v203\n"
      "v_mfma_f32_16x16x32_bf16 v[100:103], a[120:123], v[168:171], 0\n"
      "v_cvt_pk_bf16_f32 v116, v116, v117\n"
      "v_fmac_f32 v201, v162, v162\n"
      "v_mfma_f32_16x16x32_bf16 v[100:103], a[124:127], v[154:157], v[100:103]\n"
      "v_fmac_f32 v120, v204, v204\n"
      "v_cvt_pk_bf16_f32 v133, v138, v139\n"
      "v_mfma_f32_16x16x32_bf16 v[100:103], a[128:131], v[164:167], v[100:103]\n"
      "v_mul_f32 v192, v158, v192\n"
      "v_fmac_f32 v120, v205, v205\n"
      "v_mfma_f32_16x16x32_bf16 v[100:103], a[132:135], v[184:187], v[100:103]\n"
      "v_fmac_f32 v120, v134, v134\n"
      "v_fmac_f32 v201, v163, v163\n"
      "v_mfma_f32_16x16x32_bf16 v[100:103], a[136:139], v[150:153], v[100:103]\n"
      "v_fmac_f32 v201, v108, v108\n"
      "v_cvt_pk_bf16_f32 v182, v210, v211\n"
      "v_mfma_f32_16x16x32_bf16 v[100:103], a[140:143], v[96:99], v[100:103]\n"
      "v_fmac_f32 v201, v109, v109\n"
      "v_fmac_f32 v120, v135, v135\n"
      "v_mfma_f32_16x16x32_bf16 v[210:213], v[168:171], a[144:147], 0\n"
      "s_cmp_gt_u32 s42, s44\n"
      "s_cselect_b32 s95, s94, s95\n"
      "v_fmac_f32 v120, v136, v136\n"
      "v_mfma_f32_16x16x32_bf16 v[210:213], v[154:157], a[148:151], v[210:213]\n"
      "v_fmac_f32 v120, v137, v137\n"
      "v_fmac_f32 v120, v100, v100\n"
      "v_mfma_f32_16x16x32_bf16 v[210:213], v[164:167], a[152:155], v[210:213]\n"
      "v_fmac_f32 v201, v110, v110\n"
      "v_fmac_f32 v201, v111, v111\n"
      "v_mfma_f32_16x16x32_bf16 v[210:213], v[184:187], a[156:159], v[210:213]\n"
      "v_cvt_pk_bf16_f32 v191, v192, v193\n"
      "v_cvt_pk_bf16_f32 v196, v104, v105\n"
      "v_mfma_f32_16x16x32_bf16 v[210:213], v[150:153], a[160:163], v[210:213]\n"
      "v_fmac_f32 v120, v101, v101\n"
      "v_fmac_f32 v120, v102, v102\n"
      "v_mfma_f32_16x16x32_bf16 v[210:213], v[96:99], a[164:167], v[210:213]\n"
      "v_fmac_f32 v120, v103, v103\n"
      "v_cvt_pk_bf16_f32 v123, v124, v125\n"
      "v_mfma_f32_16x16x32_bf16 v[220:223], v[168:171], a[168:171], 0\n"
      "v_permlane16_swap_b32 v201, v120\n"
      "v_mfma_f32_16x16x32_bf16 v[220:223], v[154:157], a[172:175], v[220:223]\n"
      "v_add_f32 v201, v201, v120\n"
      "v_cvt_pk_bf16_f32 v128, v142, v143\n"
      "v_mfma_f32_16x16x32_bf16 v[220:223], v[164:167], a[176:179], v[220:223]\n"
      "v_mov_b32 v120, v201\n"
      "v_cvt_pk_bf16_f32 v125, v208, v209\n"
      "v_mfma_f32_16x16x32_bf16 v[220:223], v[184:187], a[180:183], v[220:223]\n"
      "ds_read_b128 v[138:141], v2 offset:3072\n"
      "v_cvt_pk_bf16_f32 v76, v210, v211\n"
      "v_mfma_f32_16x16x32_bf16 v[220:223], v[150:153], a[184:187], v[220:223]\n"
      "v_permlane32_swap_b32 v201, v120\n"
      "v_mfma_f32_16x16x32_bf16 v[220:223], v[96:99], a[188:191], v[220:223]\n"
      "v_mul_f32 v118, v159, v118\n"
      "v_add_f32_e64 v201, v201, v120\n"
      "v_mfma_f32_16x16x32_bf16 v[208:211], v[168:171], a[192:195], 0\n"
      "ds_read_b128 v[142:145], v2 offset:6144\n"
      "v_cvt_pk_bf16_f32 v124, v206, v207\n"
      "v_mfma_f32_16x16x32_bf16 v[208:211], v[154:157], a[196:199], v[208:211]\n"
      "v_rsq_f32 v201, v201\n"
      "v_mfma_f32_16x16x32_bf16 v[208:211], v[164:167], a[200:203], v[208:211]\n"
      "v_cvt_pk_bf16_f32 v77, v212, v213\n"
      "v_mov_b32 v120, v201\n"
      "v_mfma_f32_16x16x32_bf16 v[208:211], v[184:187], a[204:207], v[208:211]\n"
      "s_waitcnt lgkmcnt(1)\n"
      "v_cvt_pk_bf16_f32 v81, v222, v223\n"
      "v_cvt_pk_bf16_f32 v80, v220, v221\n"
      "v_mfma_f32_16x16x32_bf16 v[208:211], v[150:153], a[208:211], v[208:211]\n"
      "s_add_u32 s42, s42, 1\n"
      "v_permlane16_swap_b32 v201, v120\n"
      "v_mfma_f32_16x16x32_bf16 v[208:211], v[96:99], a[212:215], v[208:211]\n"
      "ds_read_b128 v[220:223], v2 offset:15360\n"
      "v_mul_f32 v201, s50, v201\n"
      "v_mfma_f32_16x16x32_bf16 v[184:187], a[216:219], v[172:175], 0\n"
      "ds_read_b128 v[96:99], v2 offset:12288\n"
      "v_mul_f32 v109, v201, v109\n"
      "v_mfma_f32_16x16x32_bf16 v[184:187], a[220:223], v[138:141], v[184:187]\n"
      "s_waitcnt lgkmcnt(2)\n"
      "v_mul_f32 v134, v120, v134\n"
      "v_mul_f32 v161, v201, v161\n"
      "v_mfma_f32_16x16x32_bf16 v[184:187], a[224:227], v[142:145], v[184:187]\n"
      "ds_read_b128 v[104:107], v2 offset:9216\n"
      "v_mul_f32 v204, v120, v204\n"
      "v_mfma_f32_16x16x32_bf16 v[152:155], a[240:243], v[172:175], 0\n"
      "v_mul_f32 v101, v120, v101\n"
      "v_mul_f32 v202, v120, v202\n"
      "v_mfma_f32_16x16x32_bf16 v[152:155], a[244:247], v[138:141], v[152:155]\n"
      "v_mul_f32 v205, v120, v205\n"
      "v_mul_f32 v148, v201, v148\n"
      "v_mfma_f32_16x16x32_bf16 v[152:155], a[248:251], v[142:145], v[152:155]\n"
      "v_mul_f32 v147, v201, v147\n"
      "v_mul_f32_e64 v203, v120, v203\n"
      "v_mfma_f32_16x16x32_bf16 v[168:171], v[232:235], v[172:175], 0\n"
      "ds_read_b128 v[156:159], v10 offset:1200\n"
      "v_cvt_pk_bf16_f32 v202, v202, v203\n"
      "v_mfma_f32_16x16x32_bf16 v[168:171], v[236:239], v[138:141], v[168:171]\n"
      "ds_read_b64 v[192:193], v10 offset:1192\n"
      "v_mul_f32 v135, v120, v135\n"
      "v_mfma_f32_16x16x32_bf16 v[168:171], v[240:243], v[142:145], v[168:171]\n"
      "s_waitcnt lgkmcnt(2)\n"
      "v_cvt_pk_bf16_f32 v84, v208, v209\n"
      "v_mul_f32 v108, v201, v108\n"
      "v_mfma_f32_16x16x32_bf16 v[184:187], a[228:231], v[104:107], v[184:187]\n"
      "ds_read_b128 v[142:145], v2 offset:256\n"
      "v_mul_f32 v136, v120, v136\n"
      "v_mfma_f32_16x16x32_bf16 v[184:187], a[232:235], v[96:99], v[184:187]\n"
      "ds_read_u16 v86, v11 offset:1232\n"
      "v_mul_f32_e64 v137, v120, v137\n"
      "v_mfma_f32_16x16x32_bf16 v[184:187], a[236:239], v[220:223], v[184:187]\n"
      "v_cvt_pk_bf16_f32 v203, v204, v205\n"
      "v_mul_f32 v100, v120, v100\n"
      "v_mfma_f32_16x16x32_bf16 v[152:155], a[252:255], v[104:107], v[152:155]\n"
      "v_cvt_pk_bf16_f32 v204, v134, v135\n"
      "v_mul_f32 v110, v201, v110\n"
      "v_mfma_f32_16x16x32_bf16 v[152:155], v[224:227], v[96:99], v[152:155]\n"
      "ds_read_b128 v[172:175], v2 offset:6400\n"
      "v_mul_f32 v103, v120, v103\n"
      "v_mfma_f32_16x16x32_bf16 v[152:155], v[228:231], v[220:223], v[152:155]\n"
      "v_mul_f32 v102, v120, v102\n"
      "v_cvt_pk_bf16_f32 v108, v108, v109\n"
      "v_mfma_f32_16x16x32_bf16 v[168:171], v[244:247], v[104:107], v[168:171]\n"
      "ds_read_b128 v[138:141], v2 offset:3328\n"
      "v_cvt_pk_bf16_f32 v100, v100, v101\n"
      "v_mfma_f32_16x16x32_bf16 v[168:171], v[248:251], v[96:99], v[168:171]\n"
      "v_fma_f32 v150, v184, v184, s52\n"
      "v_cvt_pk_bf16_f32 v117, v118, v119\n"
      "v_mfma_f32_16x16x32_bf16 v[168:171], v[252:255], v[220:223], v[168:171]\n"
      "ds_read_u16 v82, v11 offset:1200\n"
      "v_mul_f32_e64 v163, v201, v163\n"
      "v_mfma_f32_16x16x32_bf16 v[118:121], v[180:183], v[112:115], v[40:43]\n"
      "ds_read_u16 v78, v11 offset:1168\n"
      "v_cvt_pk_bf16_f32 v205, v136, v137\n"
      "v_mfma_f32_16x16x32_bf16 v[104:107], v[122:125], v[112:115], v[44:47]\n"
      "v_mul_f32 v162, v201, v162\n"
      "v_mul_f32 v151, v185, v185\n"
      "v_mfma_f32_16x16x32_bf16 v[206:209], v[202:205], v[112:115], v[48:51]\n"
      "s_waitcnt lgkmcnt(3)\n"
      "v_mul_f32 v111, v201, v111\n"
      "v_cvt_pk_bf16_f32 v101, v102, v103\n"
      "v_mfma_f32_16x16x32_bf16 v[220:223], v[156:159], v[112:115], v[24:27]\n"
      "v_cvt_pk_bf16_f32 v85, v210, v211\n"
      "v_mul_f32 v149, v201, v149\n"
      "v_mfma_f32_16x16x16_bf16 v[118:121], v[218:219], v[198:199], v[118:121]\n"
      "v_mul_f32 v146, v201, v146\n"
      "v_cvt_pk_bf16_f32 v146, v146, v147\n"
      "v_mul_f32_e64 v160, v201, v160\n"
      "v_mfma_f32_16x16x16_bf16 v[104:107], v[116:117], v[198:199], v[104:107]\n"
      "v_cvt_pk_bf16_f32 v147, v148, v149\n"
      "v_cvt_pk_bf16_f32 v148, v160, v161\n"
      "v_fmac_f32_e64 v151, v187, v187\n"
      "v_mfma_f32_16x16x16_bf16 v[206:209], v[100:101], v[198:199], v[206:209]\n"
      "v_exp_f32 v119, v119\n"
      "s_waitcnt vmcnt(1)\n"
      "v_or3_b32 v176, v176, v177, v178\n"
      "v_mfma_f32_16x16x16_bf16 v[220:223], v[192:193], v[198:199], v[220:223]\n"
      "v_exp_f32 v121, v121\n"
      "v_cvt_pk_bf16_f32 v149, v162, v163\n"
      "v_mfma_f32_16x16x32_bf16 v[160:163], a[216:219], v[142:145], 0\n"
      "s_waitcnt lgkmcnt(0)\n"
      "v_exp_f32_e64 v118, v118\n"
      "v_mfma_f32_16x16x32_bf16 v[160:163], a[220:223], v[138:141], v[160:163]\n"
      "v_exp_f32 v209, v209\n"
      "v_mfma_f32_16x16x32_bf16 v[160:163], a[224:227], v[172:175], v[160:163]\n"
      "v_cvt_pk_bf16_f32 v118, v118, v119\n"
      "v_fmac_f32 v150, v186, v186\n"
      "v_mfma_f32_16x16x32_bf16 v[164:167], a[240:243], v[142:145], 0\n"
      "v_exp_f32 v208, v208\n"
      "v_mfma_f32_16x16x32_bf16 v[164:167], a[244:247], v[138:141], v[164:167]\n"
      "v_exp_f32 v207, v207\n"
      "v_mfma_f32_16x16x32_bf16 v[164:167], a[248:251], v[172:175], v[164:167]\n"
      "v_exp_f32 v206, v206\n"
      "v_mfma_f32_16x16x32_bf16 v[134:137], v[232:235], v[142:145], 0\n"
      "v_fmac_f32 v151, v153, v153\n"
      "v_cvt_pk_bf16_f32 v109, v110, v111\n"
      "v_mfma_f32_16x16x32_bf16 v[134:137], v[236:239], v[138:141], v[134:137]\n"
      "v_exp_f32 v104, v104\n"
      "v_mfma_f32_16x16x32_bf16 v[134:137], v[240:243], v[172:175], v[134:137]\n"
      "v_exp_f32 v120, v120\n"
      "v_mfma_f32_16x16x32_bf16 v[138:141], v[180:183], v[126:129], v[52:55]\n"
      "v_exp_f32_e64 v220, v220\n"
      "v_mfma_f32_16x16x32_bf16 v[172:175], v[122:125], v[126:129], v[56:59]\n"
      "v_exp_f32_e64 v105, v105\n"
      "v_mfma_f32_16x16x32_bf16 v[112:115], v[202:205], v[126:129], v[60:63]\n"
      "v_exp_f32_e64 v106, v106\n"
      "v_mfma_f32_16x16x32_bf16 v[96:99], v[156:159], v[126:129], v[26:29]\n"
      "v_cvt_pk_bf16_f32 v90, v220, 0\n"
      "v_cvt_pk_bf16_f32 v119, v120, v121\n"
      "v_mfma_f32_16x16x16_bf16 v[138:141], v[218:219], v[190:191], v[138:141]\n"
      "s_waitcnt vmcnt(0)\n"
      "v_or3_b32 v176, v176, v179, v189\n"
      "v_exp_f32 v107, v107\n"
      "v_mfma_f32_16x16x16_bf16 v[172:175], v[116:117], v[190:191], v[172:175]\n"
      "v_cvt_pk_bf16_f32 v121, v106, v107\n"
      "v_cvt_pk_bf16_f32 v120, v104, v105\n"
      "v_fmac_f32 v150, v152, v152\n"
      "v_mfma_f32_16x16x16_bf16 v[112:115], v[100:101], v[190:191], v[112:115]\n"
      "v_cvt_pk_bf16_f32 v88, v206, v207\n"
      "v_exp_f32 v138, v138\n"
      "v_mfma_f32_16x16x16_bf16 v[96:99], v[192:193], v[190:191], v[96:99]\n"
      "v_cvt_pk_bf16_f32 v89, v208, v209\n"
      "v_exp_f32 v172, v172\n"
      "v_mfma_f32_16x16x32_bf16 v[126:129], v[32:35], v[118:121], 0\n"
      "v_exp_f32 v141, v141\n"
      "v_mfma_f32_16x16x32_bf16 v[126:129], v[32:35], v[88:91], v[126:129]\n"
      "v_exp_f32 v175, v175\n"
      "v_mfma_f32_16x16x32_bf16 v[188:191], v[214:217], v[118:121], 0\n"
      "v_exp_f32 v173, v173\n"
      "v_mfma_f32_16x16x32_bf16 v[188:191], v[76:79], v[88:91], v[188:191]\n"
      "v_exp_f32_e64 v139, v139\n"
      "v_mfma_f32_16x16x32_bf16 v[206:209], v[130:133], v[118:121], 0\n"
      "ds_read_b128 v[198:201], v2 offset:9472\n"
      "v_cvt_pk_bf16_f32 v138, v138, v139\n"
      "v_mfma_f32_16x16x32_bf16 v[206:209], v[80:83], v[88:91], v[206:209]\n"
      "v_rcp_f32 v126, v126\n"
      "v_mfma_f32_16x16x32_bf16 v[210:213], v[194:197], v[118:121], 0\n"
      "v_exp_f32 v113, v113\n"
      "v_mfma_f32_16x16x32_bf16 v[210:213], v[84:87], v[88:91], v[210:213]\n"
      "s_add_u32 s43, s43, 1\n"
      "v_exp_f32 v140, v140\n"
      "v_mfma_f32_16x16x32_bf16 v[220:223], v[180:183], v[146:149], v[64:67]\n"
      "v_exp_f32_e64 v115, v115\n"
      "v_mfma_f32_16x16x32_bf16 v[142:145], v[122:125], v[146:149], v[68:71]\n"
      "v_cvt_pk_bf16_f32 v139, v140, v141\n"
      "v_cvt_pk_bf16_f32 v140, v172, v173\n"
      "v_mfma_f32_16x16x32_bf16 v[102:105], v[202:205], v[146:149], v[72:75]\n"
      "s_waitcnt lgkmcnt(0)\n"
      "v_exp_f32 v112, v112\n"
      "v_mfma_f32_16x16x32_bf16 v[118:121], v[156:159], v[146:149], v[28:31]\n"
      "ds_read_b128 v[146:149], v2 offset:12544\n"
      "v_cvt_pk_bf16_f32 v92, v112, v113\n"
      "v_mfma_f32_16x16x16_bf16 v[220:223], v[218:219], v[108:109], v[220:223]\n"
      "v_fmac_f32 v150, v154, v154\n"
      "v_exp_f32 v174, v174\n"
      "v_mfma_f32_16x16x16_bf16 v[142:145], v[116:117], v[108:109], v[142:145]\n"
      "v_cvt_pk_bf16_f32 v141, v174, v175\n"
      "v_exp_f32_e64 v114, v114\n"
      "v_mfma_f32_16x16x16_bf16 v[102:105], v[100:101], v[108:109], v[102:105]\n"
      "ds_read_b128 v[172:175], v2 offset:15616\n"
      "v_fmac_f32_e64 v151, v155, v155\n"
      "v_mfma_f32_16x16x16_bf16 v[118:121], v[192:193], v[108:109], v[118:121]\n"
      "v_cvt_pk_bf16_f32 v93, v114, v115\n"
      "v_exp_f32 v220, v220\n"
      "v_exp_f32 v142, v142\n"
      "v_exp_f32_e64 v221, v221\n"
      "v_pk_mul_f32 v[188:189], v[188:189], v[126:127] op_sel_hi:[1,0]\n"
      "v_pk_mul_f32 v[190:191], v[190:191], v[126:127] op_sel_hi:[1,0]\n"
      "v_pk_mul_f32 v[212:213], v[212:213], v[126:127] op_sel_hi:[1,0]\n"
      "v_pk_mul_f32 v[208:209], v[208:209], v[126:127] op_sel_hi:[1,0]\n"
      "v_pk_mul_f32 v[206:207], v[206:207], v[126:127] op_sel_hi:[1,0]\n"
      "v_cvt_pk_bf16_f32 v188, v188, v189\n"
      "v_pk_mul_f32 v[210:211], v[210:211], v[126:127] op_sel_hi:[1,0]\n"
      "v_pk_fma_f32 v[150:151], v[168:169], v[168:169], v[150:151]\n"
      "v_mfma_f32_16x16x32_bf16 v[160:163], a[228:231], v[198:201], v[160:163]\n"
      "s_waitcnt lgkmcnt(1)\n"
      "v_exp_f32 v96, v96\n"
      "v_mfma_f32_16x16x32_bf16 v[160:163], a[232:235], v[146:149], v[160:163]\n"
      "s_waitcnt lgkmcnt(0)\n"
      "v_exp_f32 v223, v223\n"
      "v_mfma_f32_16x16x32_bf16 v[160:163], a[236:239], v[172:175], v[160:163]\n"
      "v_cvt_pk_bf16_f32 v210, v210, v211\n"
      "v_cvt_pk_bf16_f32 v189, v190, v191\n"
      "v_mfma_f32_16x16x32_bf16 v[164:167], a[252:255], v[198:201], v[164:167]\n"
      "v_cvt_pk_bf16_f32 v94, v96, 0\n"
      "v_cvt_pk_bf16_f32 v191, v208, v209\n"
      "v_mfma_f32_16x16x32_bf16 v[164:167], v[224:227], v[146:149], v[164:167]\n"
      "ds_read_b128 v[112:115], v7 offset:4096\n"
      "v_cvt_pk_bf16_f32 v190, v206, v207\n"
      "v_mfma_f32_16x16x32_bf16 v[164:167], v[228:231], v[172:175], v[164:167]\n"
      "v_exp_f32 v143, v143\n"
      "v_mfma_f32_16x16x32_bf16 v[134:137], v[244:247], v[198:201], v[134:137]\n"
      "v_exp_f32 v104, v104\n"
      "v_mfma_f32_16x16x32_bf16 v[134:137], v[248:251], v[146:149], v[134:137]\n"
      "ds_read_b64 v[178:179], v10 offset:1184\n"
      "v_mul_f32_e64 v149, v161, v161\n"
      "v_mfma_f32_16x16x32_bf16 v[134:137], v[252:255], v[172:175], v[134:137]\n"
      "ds_read_b128 v[206:209], v7\n"
      "v_cvt_pk_bf16_f32 v211, v212, v213\n"
      "v_mfma_f32_16x16x32_bf16 v[96:99], v[32:35], v[138:141], 0\n"
      "v_exp_f32_e64 v105, v105\n"
      "v_mfma_f32_16x16x32_bf16 v[96:99], v[32:35], v[92:95], v[96:99]\n"
      "ds_read_b128 v[108:111], v10 offset:1168\n"
      "v_cvt_pk_bf16_f32 v220, v220, v221\n"
      "v_mfma_f32_16x16x32_bf16 v[198:201], v[214:217], v[138:141], 0\n"
      "v_exp_f32_e64 v103, v103\n"
      "v_mfma_f32_16x16x32_bf16 v[198:201], v[76:79], v[92:95], v[198:201]\n"
      "ds_write_b128 v3, v[188:191] offset:18432\n"
      "v_mfma_f32_16x16x32_bf16 v[126:129], v[130:133], v[138:141], 0\n"
      "s_nop 0\n"
      "v_rcp_f32 v96, v96\n"
      "v_mfma_f32_16x16x32_bf16 v[126:129], v[80:83], v[92:95], v[126:129]\n"
      "v_cvt_pk_bf16_f32 v89, v104, v105\n"
      "v_fma_f32 v148, v160, v160, s52\n"
      "v_mfma_f32_16x16x32_bf16 v[172:175], v[194:197], v[138:141], 0\n"
      "s_waitcnt lgkmcnt(1)\n"
      "v_exp_f32 v222, v222\n"
      "v_mfma_f32_16x16x32_bf16 v[172:175], v[84:87], v[92:95], v[172:175]\n"
      "buffer_store_dwordx4 v[206:209], v20, s[24:27], s85 offen sc1\n"
      "v_pk_mul_f32 v[198:199], v[198:199], v[96:97] op_sel_hi:[1,0]\n"
      "v_pk_mul_f32 v[200:201], v[200:201], v[96:97] op_sel_hi:[1,0]\n"
      "v_pk_mul_f32 v[128:129], v[128:129], v[96:97] op_sel_hi:[1,0]\n"
      "v_mfma_f32_16x16x32_bf16 v[188:191], v[108:111], v[180:183], v[30:33]\n"
      "ds_read_b128 v[206:209], v7 offset:8192\n"
      "v_cvt_pk_bf16_f32 v221, v222, v223\n"
      "v_mfma_f32_16x16x32_bf16 v[138:141], v[108:111], v[122:125], v[30:33]\n"
      "ds_read_b128 v[104:107], v2 offset:512\n"
      "v_cvt_pk_bf16_f32 v198, v198, v199\n"
      "v_mfma_f32_16x16x32_bf16 v[122:125], v[108:111], v[202:205], v[30:33]\n"
      "ds_write_b64 v4, v[210:211] offset:18432\n"
      "v_mfma_f32_16x16x32_bf16 v[210:213], v[108:111], v[156:159], v[30:33]\n"
      "ds_read_b128 v[202:205], v2 offset:3584\n"
      "v_cvt_pk_bf16_f32 v199, v200, v201\n"
      "v_mfma_f32_16x16x16_bf16 v[188:191], v[178:179], v[218:219], v[188:191]\n"
      "ds_read_b128 v[156:159], v2 offset:6656\n"
      "v_exp_f32_e64 v118, v118\n"
      "v_mfma_f32_16x16x16_bf16 v[138:141], v[178:179], v[116:117], v[138:141]\n"
      "v_exp_f32_e64 v145, v145\n"
      "v_cvt_pk_bf16_f32 v201, v128, v129\n"
      "v_mfma_f32_16x16x16_bf16 v[122:125], v[178:179], v[100:101], v[122:125]\n"
      "buffer_store_dwordx4 v[112:115], v21, s[24:27], s85 offen sc1\n"
      "v_pk_fma_f32 v[148:149], v[162:163], v[162:163], v[148:149]\n"
      "v_pk_fma_f32 v[150:151], v[170:171], v[170:171], v[150:151]\n"
      "v_pk_fma_f32 v[148:149], v[164:165], v[164:165], v[148:149]\n"
      "v_pk_mul_f32 v[126:127], v[126:127], v[96:97] op_sel_hi:[1,0]\n"
      "v_pk_fma_f32 v[148:149], v[166:167], v[166:167], v[148:149]\n"
      "v_pk_mul_f32 v[174:175], v[174:175], v[96:97] op_sel_hi:[1,0]\n"
      "v_mfma_f32_16x16x16_bf16 v[210:213], v[178:179], v[192:193], v[210:213]\n"
      "v_cvt_pk_bf16_f32 v90, v118, 0\n"
      "v_cvt_pk_bf16_f32 v200, v126, v127\n"
      "s_waitcnt lgkmcnt(1)\n"
      "v_mfma_f32_16x16x32_bf16 v[108:111], a[216:219], v[104:107], 0\n"
      "ds_write_b128 v3, v[198:201] offset:18688\n"
      "v_pk_fma_f32 v[148:149], v[134:135], v[134:135], v[148:149]\n"
      "v_pk_mul_f32 v[172:173], v[172:173], v[96:97] op_sel_hi:[1,0]\n"
      "v_mfma_f32_16x16x32_bf16 v[108:111], a[220:223], v[202:205], v[108:111]\n"
      "s_waitcnt lgkmcnt(1)\n"
      "v_exp_f32 v144, v144\n"
      "v_mfma_f32_16x16x32_bf16 v[108:111], a[224:227], v[156:159], v[108:111]\n"
      "v_exp_f32 v102, v102\n"
      "v_mfma_f32_16x16x32_bf16 v[116:119], a[240:243], v[104:107], 0\n"
      "buffer_store_dwordx4 v[206:209], v22, s[24:27], s85 offen sc1\n"
      "v_pk_fma_f32 v[148:149], v[136:137], v[136:137], v[148:149]\n"
      "v_mfma_f32_16x16x32_bf16 v[116:119], a[244:247], v[202:205], v[116:119]\n"
      "ds_read_b128 v[178:181], v2 offset:12800\n"
      "v_cvt_pk_bf16_f32 v223, v144, v145\n"
      "v_mfma_f32_16x16x32_bf16 v[116:119], a[248:251], v[156:159], v[116:119]\n"
      "ds_read_b128 v[126:129], v7 offset:16384\n"
      "v_cvt_pk_bf16_f32 v222, v142, v143\n"
      "v_mfma_f32_16x16x32_bf16 v[96:99], v[232:235], v[104:107], 0\n"
      "v_cvt_pk_bf16_f32 v88, v102, v103\n"
      "v_cvt_pk_bf16_f32 v172, v172, v173\n"
      "v_mfma_f32_16x16x32_bf16 v[96:99], v[236:239], v[202:205], v[96:99]\n"
      "ds_read_b128 v[112:115], v7 offset:12288\n"
      "v_cndmask_b32 v188, v188, v138, s[58:59]\n"
      "v_mfma_f32_16x16x32_bf16 v[96:99], v[240:243], v[156:159], v[96:99]\n"
      "ds_read_b128 v[138:141], v2 offset:9728\n"
      "v_add_f32_e64 v150, v150, v151\n"
      "v_mfma_f32_16x16x32_bf16 v[204:207], v[32:35], v[220:223], 0\n"
      "v_cndmask_b32 v188, v188, v122, s[60:61]\n"
      "v_cndmask_b32 v188, v188, v210, s[62:63]\n"
      "v_mfma_f32_16x16x32_bf16 v[204:207], v[32:35], v[88:91], v[204:207]\n"
      "v_exp_f32_e64 v188, v188\n"
      "v_mfma_f32_16x16x32_bf16 v[142:145], v[214:217], v[220:223], 0\n"
      "ds_read_b128 v[120:123], v2 offset:15872\n"
      "v_cvt_pk_bf16_f32 v188, v188, v188\n"
      "v_mfma_f32_16x16x32_bf16 v[142:145], v[76:79], v[88:91], v[142:145]\n"
      "ds_write_b16 v13, v188\n"
      "v_mfma_f32_16x16x32_bf16 v[104:107], v[130:133], v[220:223], 0\n"
      "ds_read_b128 v[210:213], v14\n"
      "v_cvt_pk_bf16_f32 v173, v174, v175\n"
      "v_mfma_f32_16x16x32_bf16 v[104:107], v[80:83], v[88:91], v[104:107]\n"
      "v_rcp_f32_e64 v204, v204\n"
      "v_mfma_f32_16x16x32_bf16 v[200:203], v[194:197], v[220:223], 0\n"
      "ds_write_b64 v4, v[172:173] offset:18688\n"
      "v_mfma_f32_16x16x32_bf16 v[200:203], v[84:87], v[88:91], v[200:203]\n"
      "s_waitcnt lgkmcnt(4)\n"
      "v_mul_f32 v145, v204, v145\n"
      "v_mul_f32_e64 v143, v204, v143\n"
      "v_mfma_f32_16x16x32_bf16 v[108:111], a[228:231], v[138:141], v[108:111]\n"
      "ds_read_b128 v[172:175], v14 offset:64\n"
      "v_mul_f32 v144, v204, v144\n"
      "v_mfma_f32_16x16x32_bf16 v[108:111], a[232:235], v[178:181], v[108:111]\n"
      "s_waitcnt lgkmcnt(4)\n"
      "v_add_f32 v148, v148, v149\n"
      "v_mul_f32 v142, v204, v142\n"
      "v_mfma_f32_16x16x32_bf16 v[108:111], a[236:239], v[120:123], v[108:111]\n"
      "v_mul_f32 v106, v204, v106\n"
      "v_mul_f32 v107, v204, v107\n"
      "v_mfma_f32_16x16x32_bf16 v[116:119], a[252:255], v[138:141], v[116:119]\n"
      "v_cvt_pk_bf16_f32 v142, v142, v143\n"
      "v_cvt_pk_bf16_f32 v143, v144, v145\n"
      "v_mfma_f32_16x16x32_bf16 v[116:119], v[224:227], v[178:181], v[116:119]\n"
      "ds_write_b32 v31, v176 offset:5632\n"
      "v_mfma_f32_16x16x32_bf16 v[116:119], v[228:231], v[120:123], v[116:119]\n"
      "v_fma_f32 v206, v108, v108, s52\n"
      "v_cvt_pk_bf16_f32 v145, v106, v107\n"
      "v_mfma_f32_16x16x32_bf16 v[96:99], v[244:247], v[138:141], v[96:99]\n"
      "buffer_store_dwordx4 v[112:115], v23, s[24:27], s85 offen sc1\n"
      "v_pk_mul_f32 v[104:105], v[104:105], v[204:205] op_sel_hi:[1,0]\n"
      "v_mfma_f32_16x16x32_bf16 v[96:99], v[248:251], v[178:181], v[96:99]\n"
      "s_waitcnt lgkmcnt(1)\n"
      "v_mul_f32 v207, v109, v109\n"
      "v_cvt_pk_bf16_f32 v144, v104, v105\n"
      "v_mfma_f32_16x16x32_bf16 v[96:99], v[252:255], v[120:123], v[96:99]\n"
      "ds_write_b128 v3, v[142:145] offset:18944\n"
      "v_pk_mul_f32 v[200:201], v[200:201], v[204:205] op_sel_hi:[1,0]\n"
      "v_pk_fma_f32 v[206:207], v[110:111], v[110:111], v[206:207]\n"
      "v_pk_mul_f32 v[202:203], v[202:203], v[204:205] op_sel_hi:[1,0]\n"
      "v_pk_fma_f32 v[206:207], v[116:117], v[116:117], v[206:207]\n"
      "v_pk_fma_f32 v[206:207], v[118:119], v[118:119], v[206:207]\n"
      "v_permlane16_swap_b32 v150, v148\n"
      "v_cvt_pk_bf16_f32 v200, v200, v201\n"
      "v_pk_fma_f32 v[206:207], v[96:97], v[96:97], v[206:207]\n"
      "v_add_f32 v150, v150, v148\n"
      "v_pk_fma_f32 v[206:207], v[98:99], v[98:99], v[206:207]\n"
      "v_add_f32 v206, v206, v207\n"
      "v_mov_b32 v148, v206\n"
      "v_cvt_pk_bf16_f32 v201, v202, v203\n"
      "v_mfma_f32_16x16x32_bf16 v[188:191], v[210:213], v[32:35], 0\n"
      "ds_write_b64 v4, v[200:201] offset:18944\n"
      "v_permlane16_swap_b32 v206, v148\n"
      "v_add_f32_e64 v206, v206, v148\n"
      "v_add_u32 v10, 2336, v10\n"
      "s_nop 0\n"
      "v_permlane32_swap_b32_e64 v150, v206\n"
      "v_add_f32 v150, v150, v206\n"
      "v_mfma_f32_16x16x32_bf16 v[188:191], v[172:175], v[32:35], v[188:191]\n"
      "ds_write_b32 v31, v150 offset:1024\n"
      "v_add_u32 v11, 2336, v11\n"
      "s_cmp_ge_u32 s95, 0x40000000\n"
      "s_cselect_b64 vcc, -1, s[56:57]\n"
      "buffer_store_dwordx4 v[126:129], v25, s[24:27], s85 offen sc1\n"
      "s_waitcnt lgkmcnt(0)\n"
      "s_barrier\n"
      "s_cbranch_vccnz L_disp0_%=\n"
      "L_loop0_%=:\n"
      "v_mfma_f32_16x16x32_bf16 v[120:123], v[210:213], v[214:217], 0\n"
      "ds_read_b128 v[202:205], v0\n"
      "ds_read_b128 v[126:129], v9 offset:1024\n"
      "v_mfma_f32_16x16x32_bf16 v[120:123], v[172:175], v[76:79], v[120:123]\n"
      "ds_read_b128 v[112:115], v0 offset:12288\n"
      "v_mfma_f32_16x16x32_bf16 v[148:151], v[210:213], v[130:133], 0\n"
      "s_add_u32 m0, s64, 18432\n"
      "ds_read_b128 v[142:145], v0 offset:6144\n"
      "v_mfma_f32_16x16x32_bf16 v[148:151], v[172:175], v[80:83], v[148:151]\n"
      "buffer_load_dwordx4 v16, s[16:19], s45 offen  lds\n"
      "v_mfma_f32_16x16x32_bf16 v[178:181], v[210:213], v[194:197], 0\n"
      "ds_read_b128 v[210:213], v1\n"
      "s_add_u32 m0, s64, 22528\n"
      "v_mfma_f32_16x16x32_bf16 v[178:181], v[172:175], v[84:87], v[178:181]\n"
      "ds_read_b128 v[156:159], v1 offset:12288\n"
      "s_waitcnt lgkmcnt(5)\n"
      "v_mfma_f32_16x16x32_bf16 v[206:209], a[0:3], v[202:205], 0\n"
      "ds_read_b128 v[122:125], v1 offset:6144\n"
      "v_readlane_b32 s85, v37, s42\n"
      "v_mfma_f32_16x16x32_bf16 v[100:103], a[24:27], v[202:205], 0\n"
      "s_waitcnt lgkmcnt(5)\n"
      "v_add_f32_e64 v128, v128, v129\n"
      "v_cndmask_b32 v120, v120, v148, s[58:59]\n"
      "v_mfma_f32_16x16x32_bf16 v[218:221], a[48:51], v[202:205], 0\n"
      "buffer_load_dwordx4 v17, s[16:19], s45 offen  lds\n"
      "v_mfma_f32_16x16x32_bf16 v[174:177], a[72:75], v[202:205], 0\n"
      "v_add_f32 v126, v126, v127\n"
      "v_add_f32 v126, v126, v128\n"
      "v_mfma_f32_16x16x32_bf16 v[104:107], a[96:99], v[202:205], 0\n"
      "v_rsq_f32 v126, v126\n"
      "v_mfma_f32_16x16x32_bf16 v[146:149], a[120:123], v[202:205], 0\n"
      "s_waitcnt lgkmcnt(1)\n"
      "v_mul_f32 v126, 0x415db3d7, v126\n"
      "v_mov_b32 v128, v126\n"
      "v_mfma_f32_16x16x32_bf16 v[206:209], a[4:7], v[210:213], v[206:209]\n"
      "s_add_u32 m0, s64, 26624\n"
      "v_permlane32_swap_b32 v126, v128\n"
      "v_mfma_f32_16x16x32_bf16 v[206:209], a[8:11], v[142:145], v[206:209]\n"
      "buffer_load_dwordx4 v18, s[16:19], s45 offen  lds\n"
      "v_mfma_f32_16x16x32_bf16 v[100:103], a[28:31], v[210:213], v[100:103]\n"
      "v_mul_f32 v97, v128, v97\n"
      "v_mov_b32 v127, v126\n"
      "v_mfma_f32_16x16x32_bf16 v[100:103], a[32:35], v[142:145], v[100:103]\n"
      "s_add_u32 m0, s64, 30720\n"
      "v_mul_f32 v108, v128, v108\n"
      "v_mul_f32 v109, v128, v109\n"
      "v_mfma_f32_16x16x32_bf16 v[218:221], a[52:55], v[210:213], v[218:221]\n"
      "v_permlane16_swap_b32 v126, v127\n"
      "v_mfma_f32_16x16x32_bf16 v[218:221], a[56:59], v[142:145], v[218:221]\n"
      "ds_read_b128 v[198:201], v1 offset:14336\n"
      "v_mul_f32 v171, v126, v171\n"
      "v_mfma_f32_16x16x32_bf16 v[174:177], a[76:79], v[210:213], v[174:177]\n"
      "v_mul_f32 v184, v126, v184\n"
      "v_mul_f32 v169, v126, v169\n"
      "v_mfma_f32_16x16x32_bf16 v[174:177], a[80:83], v[142:145], v[174:177]\n"
      "buffer_load_dwordx4 v19, s[16:19], s45 offen  lds\n"
      "v_mfma_f32_16x16x32_bf16 v[104:107], a[100:103], v[210:213], v[104:107]\n"
      "v_mul_f32 v155, v126, v155\n"
      "v_mul_f32 v152, v126, v152\n"
      "v_mfma_f32_16x16x32_bf16 v[104:107], a[104:107], v[142:145], v[104:107]\n"
      "v_mul_f32 v96, v128, v96\n"
      "v_mul_f32 v160, v127, v160\n"
      "v_mfma_f32_16x16x32_bf16 v[146:149], a[124:127], v[210:213], v[146:149]\n"
      "s_mov_b32 m0, s7\n"
      "v_mul_f32 v153, v126, v153\n"
      "v_mul_f32 v185, v126, v185\n"
      "v_mfma_f32_16x16x32_bf16 v[146:149], a[128:131], v[142:145], v[146:149]\n"
      "s_waitcnt lgkmcnt(0)\n"
      "v_readlane_b32 s84, v37, s43\n"
      "v_mul_f32 v164, v127, v164\n"
      "v_mfma_f32_16x16x32_bf16 v[206:209], a[12:15], v[122:125], v[206:209]\n"
      "buffer_load_dwordx4 v15, s[0:3], s4 offen  lds\n"
      "v_mfma_f32_16x16x32_bf16 v[206:209], a[16:19], v[112:115], v[206:209]\n"
      "v_mul_f32 v137, v127, v137\n"
      "v_mul_f32 v186, v126, v186\n"
      "v_mfma_f32_16x16x32_bf16 v[206:209], a[20:23], v[156:159], v[206:209]\n"
      "v_mul_f32 v163, v127, v163\n"
      "v_mul_f32 v119, v128, v119\n"
      "v_mfma_f32_16x16x32_bf16 v[100:103], a[36:39], v[122:125], v[100:103]\n"
      "v_mul_f32 v161, v127, v161\n"
      "v_mov_b32_dpp v188, v188 quad_perm:[0,0,0,0] row_mask:0xf bank_mask:0xf\n"
      "v_mfma_f32_16x16x32_bf16 v[100:103], a[40:43], v[112:115], v[100:103]\n"
      "v_mul_f32 v154, v126, v154\n"
      "v_mul_f32 v187, v126, v187\n"
      "v_mfma_f32_16x16x32_bf16 v[100:103], a[44:47], v[156:159], v[100:103]\n"
      "buffer_load_dwordx4 v[138:141], v36, s[28:31], s47 offen\n"
      "v_mfma_f32_16x16x32_bf16 v[218:221], a[60:63], v[122:125], v[218:221]\n"
      "v_cndmask_b32 v120, v120, v178, s[60:61]\n"
      "v_mul_f32 v165, v127, v165\n"
      "v_mfma_f32_16x16x32_bf16 v[218:221], a[64:67], v[112:115], v[218:221]\n"
      "v_cvt_pk_bf16_f32 v184, v184, v185\n"
      "v_cvt_pk_bf16_f32 v185, v186, v187\n"
      "v_mfma_f32_16x16x32_bf16 v[218:221], a[68:71], v[156:159], v[218:221]\n"
      "v_mul_f32 v135, v127, v135\n"
      "v_mul_f32 v168, v126, v168\n"
      "v_mfma_f32_16x16x32_bf16 v[174:177], a[84:87], v[122:125], v[174:177]\n"
      "v_rcp_f32 v188, v188\n"
      "v_mfma_f32_16x16x32_bf16 v[174:177], a[88:91], v[112:115], v[174:177]\n"
      "ds_read_b128 v[190:193], v1 offset:2048\n"
      "v_cvt_pk_bf16_f32 v160, v160, v161\n"
      "v_mfma_f32_16x16x32_bf16 v[174:177], a[92:95], v[156:159], v[174:177]\n"
      "v_mul_f32 v118, v128, v118\n"
      "v_mul_f32 v120, v188, v120\n"
      "v_mfma_f32_16x16x32_bf16 v[104:107], a[108:111], v[122:125], v[104:107]\n"
      "buffer_load_dword v90, v29, s[28:31], s47 offen\n"
      "v_mfma_f32_16x16x32_bf16 v[104:107], a[112:115], v[112:115], v[104:107]\n"
      "v_mul_f32_e64 v99, v128, v99\n"
      "v_fma_f32 v222, v206, v206, s51\n"
      "v_mfma_f32_16x16x32_bf16 v[104:107], a[116:119], v[156:159], v[104:107]\n"
      "v_fma_f32 v223, v174, v174, s51\n"
      "v_mul_f32_e64 v134, v127, v134\n"
      "v_mfma_f32_16x16x32_bf16 v[146:149], a[132:135], v[122:125], v[146:149]\n"
      "ds_read_b128 v[178:181], v0 offset:14336\n"
      "v_cvt_pk_bf16_f32 v186, v152, v153\n"
      "v_mfma_f32_16x16x32_bf16 v[146:149], a[136:139], v[112:115], v[146:149]\n"
      "v_mul_f32 v167, v127, v167\n"
      "v_mul_f32 v166, v127, v166\n"
      "v_mfma_f32_16x16x32_bf16 v[146:149], a[140:143], v[156:159], v[146:149]\n"
      "v_fmac_f32 v222, v207, v207\n"
      "v_cvt_pk_bf16_f32 v187, v154, v155\n"
      "v_mfma_f32_16x16x32_bf16 v[214:217], v[202:205], a[144:147], 0\n"
      "v_fmac_f32 v222, v208, v208\n"
      "v_mfma_f32_16x16x32_bf16 v[214:217], v[210:213], a[148:151], v[214:217]\n"
      "ds_write_b128 v5, v[184:187]\n"
      "v_mfma_f32_16x16x32_bf16 v[214:217], v[142:145], a[152:155], v[214:217]\n"
      "v_fmac_f32 v222, v209, v209\n"
      "v_fmac_f32 v222, v100, v100\n"
      "v_mfma_f32_16x16x32_bf16 v[214:217], v[122:125], a[156:159], v[214:217]\n"
      "v_mul_f32 v162, v127, v162\n"
      "v_cvt_pk_bf16_f32 v96, v96, v97\n"
      "v_mfma_f32_16x16x32_bf16 v[214:217], v[112:115], a[160:163], v[214:217]\n"
      "v_mul_f32 v110, v128, v110\n"
      "v_cvt_pk_bf16_f32 v161, v162, v163\n"
      "v_mfma_f32_16x16x32_bf16 v[214:217], v[156:159], a[164:167], v[214:217]\n"
      "ds_read_b128 v[182:185], v0 offset:2048\n"
      "v_cvt_pk_bf16_f32 v163, v166, v167\n"
      "v_mfma_f32_16x16x32_bf16 v[130:133], v[202:205], a[168:171], 0\n"
      "v_cvt_pk_bf16_f32 v120, v120, v120\n"
      "v_cvt_pk_bf16_f32 v162, v164, v165\n"
      "v_mfma_f32_16x16x32_bf16 v[130:133], v[210:213], a[172:175], v[130:133]\n"
      "ds_read_b128 v[150:153], v1 offset:8192\n"
      "v_cvt_pk_bf16_f32 v134, v134, v135\n"
      "v_mfma_f32_16x16x32_bf16 v[130:133], v[142:145], a[176:179], v[130:133]\n"
      "v_fmac_f32 v223, v175, v175\n"
      "v_fmac_f32 v222, v101, v101\n"
      "v_mfma_f32_16x16x32_bf16 v[130:133], v[122:125], a[180:183], v[130:133]\n"
      "v_mul_f32 v117, v128, v117\n"
      "v_fmac_f32 v222, v102, v102\n"
      "v_mfma_f32_16x16x32_bf16 v[130:133], v[112:115], a[184:187], v[130:133]\n"
      "v_mul_f32 v136, v127, v136\n"
      "v_fmac_f32 v223, v176, v176\n"
      "v_mfma_f32_16x16x32_bf16 v[130:133], v[156:159], a[188:191], v[130:133]\n"
      "ds_write_b16 v12, v120 offset:16\n"
      "v_mfma_f32_16x16x32_bf16 v[194:197], v[202:205], a[192:195], 0\n"
      "v_fmac_f32 v222, v103, v103\n"
      "v_fmac_f32 v223, v177, v177\n"
      "v_mfma_f32_16x16x32_bf16 v[194:197], v[210:213], a[196:199], v[194:197]\n"
      "ds_read_b128 v[202:205], v0 offset:8192\n"
      "v_mul_f32_e64 v98, v128, v98\n"
      "v_mfma_f32_16x16x32_bf16 v[194:197], v[142:145], a[200:203], v[194:197]\n"
      "v_cvt_pk_bf16_f32 v214, v214, v215\n"
      "v_cvt_pk_bf16_f32 v135, v136, v137\n"
      "v_mfma_f32_16x16x32_bf16 v[194:197], v[122:125], a[204:207], v[194:197]\n"
      "ds_write_b128 v5, v[160:163] offset:6144\n"
      "v_mfma_f32_16x16x32_bf16 v[194:197], v[112:115], a[208:211], v[194:197]\n"
      "s_waitcnt lgkmcnt(3)\n"
      "v_fmac_f32 v223, v104, v104\n"
      "v_mul_f32 v170, v126, v170\n"
      "v_mfma_f32_16x16x32_bf16 v[194:197], v[156:159], a[212:215], v[194:197]\n"
      "s_or_b32 s95, s84, s85\n"
      "v_fmac_f32 v222, v218, v218\n"
      "v_fmac_f32 v222, v219, v219\n"
      "v_mfma_f32_16x16x32_bf16 v[112:115], a[0:3], v[182:185], 0\n"
      "s_waitcnt lgkmcnt(1)\n"
      "v_fmac_f32 v223, v105, v105\n"
      "v_cvt_pk_bf16_f32 v168, v168, v169\n"
      "v_mfma_f32_16x16x32_bf16 v[112:115], a[4:7], v[190:193], v[112:115]\n"
      "v_cvt_pk_bf16_f32 v194, v194, v195\n"
      "v_fmac_f32 v223, v106, v106\n"
      "v_mfma_f32_16x16x32_bf16 v[112:115], a[8:11], v[202:205], v[112:115]\n"
      "v_fmac_f32 v222, v220, v220\n"
      "v_fmac_f32 v223, v107, v107\n"
      "v_mfma_f32_16x16x32_bf16 v[112:115], a[12:15], v[150:153], v[112:115]\n"
      "v_mul_f32 v111, v128, v111\n"
      "v_fmac_f32_e64 v223, v146, v146\n"
      "v_mfma_f32_16x16x32_bf16 v[112:115], a[16:19], v[178:181], v[112:115]\n"
      "ds_read_b128 v[186:189], v0 offset:10240\n"
      "v_fmac_f32 v223, v147, v147\n"
      "v_mfma_f32_16x16x32_bf16 v[112:115], a[20:23], v[198:201], v[112:115]\n"
      "ds_read_b128 v[120:123], v9 offset:5632\n"
      "v_fmac_f32 v223, v148, v148\n"
      "v_mfma_f32_16x16x32_bf16 v[210:213], a[24:27], v[182:185], 0\n"
      "ds_read_b128 v[124:127], v1 offset:4096\n"
      "v_cvt_pk_bf16_f32 v108, v108, v109\n"
      "v_mfma_f32_16x16x32_bf16 v[210:213], a[28:31], v[190:193], v[210:213]\n"
      "v_readlane_b32 s86, v38, s43\n"
      "v_cvt_pk_bf16_f32 v169, v170, v171\n"
      "v_mfma_f32_16x16x32_bf16 v[210:213], a[32:35], v[202:205], v[210:213]\n"
      "v_fmac_f32_e64 v222, v221, v221\n"
      "v_cvt_pk_bf16_f32 v97, v98, v99\n"
      "v_mfma_f32_16x16x32_bf16 v[210:213], a[36:39], v[150:153], v[210:213]\n"
      "v_mul_f32 v116, v128, v116\n"
      "v_fmac_f32 v223, v149, v149\n"
      "v_mfma_f32_16x16x32_bf16 v[210:213], a[40:43], v[178:181], v[210:213]\n"
      "v_fma_f32 v84, v112, v112, s51\n"
      "v_fmac_f32_e64 v84, v113, v113\n"
      "v_mfma_f32_16x16x32_bf16 v[210:213], a[44:47], v[198:201], v[210:213]\n"
      "ds_write_b64 v6, v[168:169]\n"
      "v_mfma_f32_16x16x32_bf16 v[158:161], a[48:51], v[182:185], 0\n"
      "s_waitcnt lgkmcnt(1)\n"
      "v_permlane16_swap_b32 v222, v223\n"
      "v_mfma_f32_16x16x32_bf16 v[158:161], a[52:55], v[190:193], v[158:161]\n"
      "v_or3_b32 v120, v120, v121, v122\n"
      "v_add_f32_e64 v222, v222, v223\n"
      "v_mfma_f32_16x16x32_bf16 v[158:161], a[56:59], v[202:205], v[158:161]\n"
      "ds_read_b128 v[162:165], v0 offset:4096\n"
      "v_bitop3_b32 v120, v120, v123, s33 bitop3:0xa8\n"
      "v_mfma_f32_16x16x32_bf16 v[158:161], a[60:63], v[150:153], v[158:161]\n"
      "v_fmac_f32 v84, v114, v114\n"
      "v_mov_b32 v223, v222\n"
      "v_mfma_f32_16x16x32_bf16 v[158:161], a[64:67], v[178:181], v[158:161]\n"
      "s_nop 0\n"
      "v_permlane32_swap_b32 v222, v223\n"
      "v_mfma_f32_16x16x32_bf16 v[158:161], a[68:71], v[198:201], v[158:161]\n"
      "v_add_f32_e64 v222, v222, v223\n"
      "v_cvt_pk_bf16_f32 v215, v216, v217\n"
      "v_mfma_f32_16x16x32_bf16 v[142:145], a[72:75], v[182:185], 0\n"
      "v_cvt_pk_bf16_f32 v109, v110, v111\n"
      "v_fmac_f32_e64 v84, v115, v115\n"
      "v_mfma_f32_16x16x32_bf16 v[142:145], a[76:79], v[190:193], v[142:145]\n"
      "ds_write_b64 v6, v[96:97] offset:12288\n"
      "v_mfma_f32_16x16x32_bf16 v[142:145], a[80:83], v[202:205], v[142:145]\n"
      "v_rsq_f32 v222, v222\n"
      "v_mfma_f32_16x16x32_bf16 v[142:145], a[84:87], v[150:153], v[142:145]\n"
      "v_cvt_pk_bf16_f32 v111, v118, v119\n"
      "v_mov_b32 v223, v222\n"
      "v_mfma_f32_16x16x32_bf16 v[142:145], a[88:91], v[178:181], v[142:145]\n"
      "ds_read_b128 v[154:157], v0 offset:16384\n"
      "v_fmac_f32 v84, v210, v210\n"
      "v_mfma_f32_16x16x32_bf16 v[142:145], a[92:95], v[198:201], v[142:145]\n"
      "v_permlane16_swap_b32 v222, v223\n"
      "v_mfma_f32_16x16x32_bf16 v[170:173], a[96:99], v[182:185], 0\n"
      "v_fmac_f32 v84, v211, v211\n"
      "v_cmp_ne_u32 s[56:57], 0, v120\n"
      "v_mfma_f32_16x16x32_bf16 v[170:173], a[100:103], v[190:193], v[170:173]\n"
      "v_mul_f32 v222, s50, v222\n"
      "v_mul_f32_e64 v207, v222, v207\n"
      "v_mfma_f32_16x16x32_bf16 v[170:173], a[104:107], v[202:205], v[170:173]\n"
      "v_cvt_pk_bf16_f32 v110, v116, v117\n"
      "v_cvt_pk_bf16_f32 v130, v130, v131\n"
      "v_mfma_f32_16x16x32_bf16 v[170:173], a[108:111], v[150:153], v[170:173]\n"
      "v_fma_f32 v128, v142, v142, s51\n"
      "v_fmac_f32_e64 v128, v143, v143\n"
      "v_mfma_f32_16x16x32_bf16 v[170:173], a[112:115], v[178:181], v[170:173]\n"
      "ds_write_b64 v6, v[134:135] offset:6144\n"
      "v_mfma_f32_16x16x32_bf16 v[170:173], a[116:119], v[198:201], v[170:173]\n"
      "v_fmac_f32_e64 v84, v212, v212\n"
      "v_cvt_pk_bf16_f32 v195, v196, v197\n"
      "v_mfma_f32_16x16x32_bf16 v[120:123], a[120:123], v[182:185], 0\n"
      "v_fmac_f32 v128, v144, v144\n"
      "v_mul_f32 v219, v222, v219\n"
      "v_mfma_f32_16x16x32_bf16 v[120:123], a[124:127], v[190:193], v[120:123]\n"
      "v_fmac_f32 v128, v145, v145\n"
      "v_mul_f32 v206, v222, v206\n"
      "v_mfma_f32_16x16x32_bf16 v[120:123], a[128:131], v[202:205], v[120:123]\n"
      "v_mul_f32 v176, v223, v176\n"
      "v_mul_f32 v107, v223, v107\n"
      "v_mfma_f32_16x16x32_bf16 v[120:123], a[132:135], v[150:153], v[120:123]\n"
      "v_mul_f32 v100, v222, v100\n"
      "v_fmac_f32 v128, v170, v170\n"
      "v_mfma_f32_16x16x32_bf16 v[120:123], a[136:139], v[178:181], v[120:123]\n"
      "v_fmac_f32 v128, v171, v171\n"
      "v_fmac_f32 v128, v172, v172\n"
      "v_mfma_f32_16x16x32_bf16 v[120:123], a[140:143], v[198:201], v[120:123]\n"
      "v_fmac_f32 v84, v213, v213\n"
      "v_mul_f32 v208, v222, v208\n"
      "v_mfma_f32_16x16x32_bf16 v[96:99], v[182:185], a[144:147], 0\n"
      "v_fmac_f32 v84, v158, v158\n"
      "v_mul_f32 v149, v223, v149\n"
      "v_mfma_f32_16x16x32_bf16 v[96:99], v[190:193], a[148:151], v[96:99]\n"
      "v_fmac_f32 v84, v159, v159\n"
      "v_fmac_f32 v128, v173, v173\n"
      "v_mfma_f32_16x16x32_bf16 v[96:99], v[202:205], a[152:155], v[96:99]\n"
      "s_or_b32 s95, s95, s48\n"
      "v_mul_f32 v148, v223, v148\n"
      "v_fmac_f32_e64 v84, v160, v160\n"
      "v_mfma_f32_16x16x32_bf16 v[96:99], v[150:153], a[156:159], v[96:99]\n"
      "ds_write_b128 v5, v[108:111] offset:12288\n"
      "v_mfma_f32_16x16x32_bf16 v[96:99], v[178:181], a[160:163], v[96:99]\n"
      "s_cmp_ge_u32 s95, 0x40000000\n"
      "s_cselect_b32 s94, -1, -2\n"
      "v_fmac_f32_e64 v128, v120, v120\n"
      "v_mfma_f32_16x16x32_bf16 v[96:99], v[198:201], a[164:167], v[96:99]\n"
      "v_mul_f32 v174, v223, v174\n"
      "v_fmac_f32 v128, v121, v121\n"
      "v_mfma_f32_16x16x32_bf16 v[134:137], v[182:185], a[168:171], 0\n"
      "v_fmac_f32 v128, v122, v122\n"
      "v_mul_f32 v147, v223, v147\n"
      "v_mfma_f32_16x16x32_bf16 v[134:137], v[190:193], a[172:175], v[134:137]\n"
      "v_mul_f32 v177, v223, v177\n"
      "v_fmac_f32 v84, v161, v161\n"
      "v_mfma_f32_16x16x32_bf16 v[134:137], v[202:205], a[176:179], v[134:137]\n"
      "v_cvt_pk_bf16_f32 v131, v132, v133\n"
      "v_mul_f32 v146, v223, v146\n"
      "v_mfma_f32_16x16x32_bf16 v[134:137], v[150:153], a[180:183], v[134:137]\n"
      "ds_read_b128 v[166:169], v1 offset:10240\n"
      "v_fmac_f32 v128, v123, v123\n"
      "v_mfma_f32_16x16x32_bf16 v[134:137], v[178:181], a[184:187], v[134:137]\n"
      "s_nop 0\n"
      "v_permlane16_swap_b32 v84, v128\n"
      "v_mfma_f32_16x16x32_bf16 v[134:137], v[198:201], a[188:191], v[134:137]\n"
      "v_mul_f32 v209, v222, v209\n"
      "v_add_f32 v84, v84, v128\n"
      "v_mfma_f32_16x16x32_bf16 v[116:119], v[182:185], a[192:195], 0\n"
      "v_mov_b32 v128, v84\n"
      "v_mul_f32 v220, v222, v220\n"
      "v_mfma_f32_16x16x32_bf16 v[116:119], v[190:193], a[196:199], v[116:119]\n"
      "v_mul_f32_e64 v218, v222, v218\n"
      "v_cvt_pk_bf16_f32 v216, v96, v97\n"
      "v_mfma_f32_16x16x32_bf16 v[116:119], v[202:205], a[200:203], v[116:119]\n"
      "v_permlane32_swap_b32 v84, v128\n"
      "v_mfma_f32_16x16x32_bf16 v[116:119], v[150:153], a[204:207], v[116:119]\n"
      "ds_read_b128 v[150:153], v1 offset:16384\n"
      "v_mul_f32 v101, v222, v101\n"
      "v_mfma_f32_16x16x32_bf16 v[116:119], v[178:181], a[208:211], v[116:119]\n"
      "v_mul_f32 v103, v222, v103\n"
      "v_cvt_pk_bf16_f32 v206, v206, v207\n"
      "v_mfma_f32_16x16x32_bf16 v[116:119], v[198:201], a[212:215], v[116:119]\n"
      "s_waitcnt lgkmcnt(1)\n"
      "v_add_f32 v84, v84, v128\n"
      "v_mul_f32 v102, v222, v102\n"
      "v_mfma_f32_16x16x32_bf16 v[108:111], a[0:3], v[162:165], 0\n"
      "v_rsq_f32_e64 v84, v84\n"
      "v_mfma_f32_16x16x32_bf16 v[108:111], a[4:7], v[124:127], v[108:111]\n"
      "v_cvt_pk_bf16_f32 v217, v98, v99\n"
      "v_mov_b32 v128, v84\n"
      "v_mfma_f32_16x16x32_bf16 v[108:111], a[8:11], v[186:189], v[108:111]\n"
      "s_nop 0\n"
      "v_permlane16_swap_b32_e64 v84, v128\n"
      "v_mfma_f32_16x16x32_bf16 v[108:111], a[12:15], v[166:169], v[108:111]\n"
      "ds_read_u16 v82, v11 offset:32\n"
      "v_mul_f32 v221, v222, v221\n"
      "v_mfma_f32_16x16x32_bf16 v[108:111], a[16:19], v[154:157], v[108:111]\n"
      "s_waitcnt lgkmcnt(1)\n"
      "v_mul_f32 v106, v223, v106\n"
      "v_mul_f32 v145, v128, v145\n"
      "v_mfma_f32_16x16x32_bf16 v[108:111], a[20:23], v[150:153], v[108:111]\n"
      "v_mul_f32 v172, v128, v172\n"
      "v_mul_f32 v173, v128, v173\n"
      "v_mfma_f32_16x16x32_bf16 v[180:183], a[24:27], v[162:165], 0\n"
      "v_cvt_pk_bf16_f32 v196, v116, v117\n"
      "v_cvt_pk_bf16_f32 v132, v134, v135\n"
      "v_mfma_f32_16x16x32_bf16 v[180:183], a[28:31], v[124:127], v[180:183]\n"
      "v_mul_f32 v84, s50, v84\n"
      "v_mul_f32 v175, v223, v175\n"
      "v_mfma_f32_16x16x32_bf16 v[180:183], a[32:35], v[186:189], v[180:183]\n"
      "v_fma_f32 v178, v108, v108, s51\n"
      "v_mul_f32_e64 v121, v128, v121\n"
      "v_mfma_f32_16x16x32_bf16 v[180:183], a[36:39], v[166:169], v[180:183]\n"
      "v_cvt_pk_bf16_f32 v174, v174, v175\n"
      "v_mul_f32 v171, v128, v171\n"
      "v_mfma_f32_16x16x32_bf16 v[180:183], a[40:43], v[154:157], v[180:183]\n"
      "v_cvt_pk_bf16_f32 v146, v146, v147\n"
      "v_fmac_f32 v178, v109, v109\n"
      "v_mfma_f32_16x16x32_bf16 v[180:183], a[44:47], v[150:153], v[180:183]\n"
      "v_mul_f32 v161, v84, v161\n"
      "v_mul_f32 v159, v84, v159\n"
      "v_mfma_f32_16x16x32_bf16 v[190:193], a[48:51], v[162:165], 0\n"
      "v_cvt_pk_bf16_f32 v207, v208, v209\n"
      "v_fmac_f32 v178, v110, v110\n"
      "v_mfma_f32_16x16x32_bf16 v[190:193], a[52:55], v[124:127], v[190:193]\n"
      "s_cmp_gt_u32 s42, s44\n"
      "s_cselect_b32 s95, s94, s95\n"
      "v_mul_f32 v114, v84, v114\n"
      "v_mfma_f32_16x16x32_bf16 v[190:193], a[56:59], v[186:189], v[190:193]\n"
      "v_fmac_f32 v178, v111, v111\n"
      "v_cvt_pk_bf16_f32 v208, v100, v101\n"
      "v_mfma_f32_16x16x32_bf16 v[190:193], a[60:63], v[166:169], v[190:193]\n"
      "v_fmac_f32 v178, v180, v180\n"
      "v_mul_f32_e64 v158, v84, v158\n"
      "v_mfma_f32_16x16x32_bf16 v[190:193], a[64:67], v[154:157], v[190:193]\n"
      "v_mul_f32 v104, v223, v104\n"
      "v_fmac_f32 v178, v181, v181\n"
      "v_mfma_f32_16x16x32_bf16 v[190:193], a[68:71], v[150:153], v[190:193]\n"
      "v_fmac_f32 v178, v182, v182\n"
      "v_mul_f32 v113, v84, v113\n"
      "v_mfma_f32_16x16x32_bf16 v[198:201], a[72:75], v[162:165], 0\n"
      "v_fmac_f32 v178, v183, v183\n"
      "v_mul_f32 v143, v128, v143\n"
      "v_mfma_f32_16x16x32_bf16 v[198:201], a[76:79], v[124:127], v[198:201]\n"
      "v_cvt_pk_bf16_f32 v209, v102, v103\n"
      "v_cvt_pk_bf16_f32 v197, v118, v119\n"
      "v_mfma_f32_16x16x32_bf16 v[198:201], a[80:83], v[186:189], v[198:201]\n"
      "v_cvt_pk_bf16_f32 v133, v136, v137\n"
      "v_fmac_f32_e64 v178, v190, v190\n"
      "v_mfma_f32_16x16x32_bf16 v[198:201], a[84:87], v[166:169], v[198:201]\n"
      "v_mul_f32 v144, v128, v144\n"
      "v_mul_f32 v212, v84, v212\n"
      "v_mfma_f32_16x16x32_bf16 v[198:201], a[88:91], v[154:157], v[198:201]\n"
      "v_mul_f32 v120, v128, v120\n"
      "v_mul_f32 v170, v128, v170\n"
      "v_mfma_f32_16x16x32_bf16 v[198:201], a[92:95], v[150:153], v[198:201]\n"
      "v_mul_f32_e64 v142, v128, v142\n"
      "v_cvt_pk_bf16_f32 v120, v120, v121\n"
      "v_mfma_f32_16x16x32_bf16 v[96:99], a[96:99], v[162:165], 0\n"
      "v_cvt_pk_bf16_f32 v218, v218, v219\n"
      "v_cvt_pk_bf16_f32 v158, v158, v159\n"
      "v_mfma_f32_16x16x32_bf16 v[96:99], a[100:103], v[124:127], v[96:99]\n"
      "s_cmp_ge_u32 s95, 0x40000000\n"
      "s_cselect_b64 vcc, -1, s[56:57]\n"
      "v_mul_f32 v105, v223, v105\n"
      "v_mfma_f32_16x16x32_bf16 v[96:99], a[104:107], v[186:189], v[96:99]\n"
      "s_and_b32 s5, s84, s8\n"
      "v_cvt_pk_bf16_f32 v175, v176, v177\n"
      "v_mul_f32_e64 v112, v84, v112\n"
      "v_mfma_f32_16x16x32_bf16 v[96:99], a[108:111], v[166:169], v[96:99]\n"
      "v_cvt_pk_bf16_f32 v177, v106, v107\n"
      "v_fma_f32 v85, v198, v198, s51\n"
      "v_mfma_f32_16x16x32_bf16 v[96:99], a[112:115], v[154:157], v[96:99]\n"
      "v_cvt_pk_bf16_f32 v142, v142, v143\n"
      "v_fmac_f32 v85, v199, v199\n"
      "v_mfma_f32_16x16x32_bf16 v[96:99], a[116:119], v[150:153], v[96:99]\n"
      "v_mul_f32 v115, v84, v115\n"
      "v_fmac_f32_e64 v85, v200, v200\n"
      "v_mfma_f32_16x16x32_bf16 v[202:205], a[120:123], v[162:165], 0\n"
      "v_fmac_f32_e64 v85, v201, v201\n"
      "v_cvt_pk_bf16_f32 v143, v144, v145\n"
      "v_mfma_f32_16x16x32_bf16 v[202:205], a[124:127], v[124:127], v[202:205]\n"
      "v_cvt_pk_bf16_f32 v176, v104, v105\n"
      "v_mul_f32 v123, v128, v123\n"
      "v_mfma_f32_16x16x32_bf16 v[202:205], a[128:131], v[186:189], v[202:205]\n"
      "v_fmac_f32 v178, v191, v191\n"
      "v_mul_f32_e64 v122, v128, v122\n"
      "v_mfma_f32_16x16x32_bf16 v[202:205], a[132:135], v[166:169], v[202:205]\n"
      "v_fmac_f32_e64 v85, v96, v96\n"
      "v_cvt_pk_bf16_f32 v147, v148, v149\n"
      "v_mfma_f32_16x16x32_bf16 v[202:205], a[136:139], v[154:157], v[202:205]\n"
      "ds_read_b128 v[102:105], v2 offset:18432\n"
      "v_fmac_f32 v178, v192, v192\n"
      "v_mfma_f32_16x16x32_bf16 v[202:205], a[140:143], v[150:153], v[202:205]\n"
      "v_cvt_pk_bf16_f32 v145, v172, v173\n"
      "v_fmac_f32 v85, v97, v97\n"
      "v_mfma_f32_16x16x32_bf16 v[134:137], v[162:165], a[144:147], 0\n"
      "v_cvt_pk_bf16_f32 v144, v170, v171\n"
      "v_cvt_pk_bf16_f32 v219, v220, v221\n"
      "v_mfma_f32_16x16x32_bf16 v[134:137], v[124:127], a[148:151], v[134:137]\n"
      "v_fmac_f32 v85, v98, v98\n"
      "v_fmac_f32 v85, v99, v99\n"
      "v_mfma_f32_16x16x32_bf16 v[134:137], v[186:189], a[152:155], v[134:137]\n"
      "v_fmac_f32 v178, v193, v193\n"
      "v_fmac_f32 v85, v202, v202\n"
      "v_mfma_f32_16x16x32_bf16 v[134:137], v[166:169], a[156:159], v[134:137]\n"
      "v_fmac_f32 v85, v203, v203\n"
      "v_fmac_f32 v85, v204, v204\n"
      "v_mfma_f32_16x16x32_bf16 v[134:137], v[154:157], a[160:163], v[134:137]\n"
      "v_fmac_f32 v85, v205, v205\n"
      "v_mul_f32 v210, v84, v210\n"
      "v_mfma_f32_16x16x32_bf16 v[134:137], v[150:153], a[164:167], v[134:137]\n"
      "v_permlane16_swap_b32_e64 v178, v85\n"
      "v_mfma_f32_16x16x32_bf16 v[220:223], v[162:165], a[168:171], 0\n"
      "ds_read_b128 v[116:119], v2 offset:21504\n"
      "v_cvt_pk_bf16_f32 v121, v122, v123\n"
      "v_mfma_f32_16x16x32_bf16 v[220:223], v[124:127], a[172:175], v[220:223]\n"
      "v_add_f32 v178, v178, v85\n"
      "v_mov_b32 v85, v178\n"
      "v_mfma_f32_16x16x32_bf16 v[220:223], v[186:189], a[176:179], v[220:223]\n"
      "s_nop 0\n"
      "v_permlane32_swap_b32 v178, v85\n"
      "v_mfma_f32_16x16x32_bf16 v[220:223], v[166:169], a[180:183], v[220:223]\n"
      "v_add_f32 v178, v178, v85\n"
      "v_cvt_pk_bf16_f32 v112, v112, v113\n"
      "v_mfma_f32_16x16x32_bf16 v[220:223], v[154:157], a[184:187], v[220:223]\n"
      "v_rsq_f32 v178, v178\n"
      "v_mfma_f32_16x16x32_bf16 v[220:223], v[150:153], a[188:191], v[220:223]\n"
      "v_mov_b32 v85, v178\n"
      "v_mul_f32 v211, v84, v211\n"
      "v_mfma_f32_16x16x32_bf16 v[170:173], v[162:165], a[192:195], 0\n"
      "ds_read_b128 v[162:165], v2 offset:24576\n"
      "v_mul_f32 v213, v84, v213\n"
      "v_mfma_f32_16x16x32_bf16 v[170:173], v[124:127], a[196:199], v[170:173]\n"
      "v_permlane16_swap_b32 v178, v85\n"
      "v_mfma_f32_16x16x32_bf16 v[170:173], v[186:189], a[200:203], v[170:173]\n"
      "v_cvt_pk_bf16_f32 v113, v114, v115\n"
      "v_mul_f32 v178, s50, v178\n"
      "v_mfma_f32_16x16x32_bf16 v[170:173], v[166:169], a[204:207], v[170:173]\n"
      "ds_read_b128 v[122:125], v2 offset:27648\n"
      "v_mfma_f32_16x16x32_bf16 v[170:173], v[154:157], a[208:211], v[170:173]\n"
      "s_waitcnt lgkmcnt(2)\n"
      "v_mul_f32 v109, v178, v109\n"
      "v_mul_f32 v98, v85, v98\n"
      "v_mfma_f32_16x16x32_bf16 v[170:173], v[150:153], a[212:215], v[170:173]\n"
      "ds_read_b128 v[148:151], v2 offset:30720\n"
      "v_cvt_pk_bf16_f32 v81, v222, v223\n"
      "v_mfma_f32_16x16x32_bf16 v[184:187], a[216:219], v[102:105], 0\n"
      "v_mul_f32 v198, v85, v198\n"
      "v_mul_f32_e64 v183, v178, v183\n"
      "v_mfma_f32_16x16x32_bf16 v[184:187], a[220:223], v[116:119], v[184:187]\n"
      "s_waitcnt lgkmcnt(2)\n"
      "v_cvt_pk_bf16_f32 v76, v134, v135\n"
      "v_cvt_pk_bf16_f32 v115, v212, v213\n"
      "v_mfma_f32_16x16x32_bf16 v[184:187], a[224:227], v[162:165], v[184:187]\n"
      "v_mul_f32 v182, v178, v182\n"
      "v_mul_f32 v181, v178, v181\n"
      "v_mfma_f32_16x16x32_bf16 v[152:155], a[240:243], v[102:105], 0\n"
      "ds_read_b128 v[126:129], v2 offset:33792\n"
      "v_mul_f32 v190, v178, v190\n"
      "v_mfma_f32_16x16x32_bf16 v[152:155], a[244:247], v[116:119], v[152:155]\n"
      "v_mul_f32 v160, v84, v160\n"
      "v_cvt_pk_bf16_f32 v84, v170, v171\n"
      "v_mfma_f32_16x16x32_bf16 v[152:155], a[248:251], v[162:165], v[152:155]\n"
      "v_mul_f32 v111, v178, v111\n"
      "v_mul_f32 v108, v178, v108\n"
      "v_mfma_f32_16x16x32_bf16 v[168:171], v[232:235], v[102:105], 0\n"
      "ds_read_u16 v78, v11\n"
      "v_mul_f32 v201, v85, v201\n"
      "v_mfma_f32_16x16x32_bf16 v[168:171], v[236:239], v[116:119], v[168:171]\n"
      "s_waitcnt lgkmcnt(2)\n"
      "v_mul_f32 v110, v178, v110\n"
      "v_mul_f32 v202, v85, v202\n"
      "v_mfma_f32_16x16x32_bf16 v[168:171], v[240:243], v[162:165], v[168:171]\n"
      "ds_read_b128 v[100:103], v10 offset:32\n"
      "v_mul_f32 v200, v85, v200\n"
      "v_mfma_f32_16x16x32_bf16 v[184:187], a[228:231], v[122:125], v[184:187]\n"
      "v_mul_f32 v97, v85, v97\n"
      "v_mul_f32 v96, v85, v96\n"
      "v_mfma_f32_16x16x32_bf16 v[184:187], a[232:235], v[148:151], v[184:187]\n"
      "s_waitcnt lgkmcnt(2)\n"
      "v_mul_f32_e64 v199, v85, v199\n"
      "v_cvt_pk_bf16_f32 v198, v198, v199\n"
      "v_mfma_f32_16x16x32_bf16 v[184:187], a[236:239], v[126:129], v[184:187]\n"
      "v_cvt_pk_bf16_f32 v108, v108, v109\n"
      "v_cvt_pk_bf16_f32 v109, v110, v111\n"
      "v_mfma_f32_16x16x32_bf16 v[152:155], a[252:255], v[122:125], v[152:155]\n"
      "v_cvt_pk_bf16_f32 v77, v136, v137\n"
      "v_mul_f32 v99, v85, v99\n"
      "v_mfma_f32_16x16x32_bf16 v[152:155], v[224:227], v[148:151], v[152:155]\n"
      "ds_read_b128 v[104:107], v2 offset:18688\n"
      "v_mul_f32 v203, v85, v203\n"
      "v_mfma_f32_16x16x32_bf16 v[152:155], v[228:231], v[126:129], v[152:155]\n"
      "ds_read_b64 v[188:189], v10 offset:24\n"
      "v_mul_f32 v205, v85, v205\n"
      "v_mfma_f32_16x16x32_bf16 v[168:171], v[244:247], v[122:125], v[168:171]\n"
      "v_cvt_pk_bf16_f32 v80, v220, v221\n"
      "v_mul_f32 v180, v178, v180\n"
      "v_mfma_f32_16x16x32_bf16 v[168:171], v[248:251], v[148:151], v[168:171]\n"
      "ds_read_b128 v[220:223], v2 offset:24832\n"
      "v_cvt_pk_bf16_f32 v199, v200, v201\n"
      "v_mfma_f32_16x16x32_bf16 v[168:171], v[252:255], v[126:129], v[168:171]\n"
      "v_cvt_pk_bf16_f32 v201, v98, v99\n"
      "v_cvt_pk_bf16_f32 v200, v96, v97\n"
      "v_mfma_f32_16x16x32_bf16 v[96:99], v[174:177], v[206:209], v[40:43]\n"
      "v_mul_f32 v204, v85, v204\n"
      "v_cvt_pk_bf16_f32 v85, v172, v173\n"
      "v_mfma_f32_16x16x32_bf16 v[122:125], v[142:145], v[206:209], v[44:47]\n"
      "ds_read_b128 v[126:129], v2 offset:21760\n"
      "v_cvt_pk_bf16_f32 v114, v210, v211\n"
      "v_mfma_f32_16x16x32_bf16 v[116:119], v[198:201], v[206:209], v[48:51]\n"
      "s_waitcnt lgkmcnt(2)\n"
      "v_cvt_pk_bf16_f32 v111, v182, v183\n"
      "v_cvt_pk_bf16_f32 v202, v202, v203\n"
      "v_mfma_f32_16x16x32_bf16 v[210:213], v[100:103], v[206:209], v[24:27]\n"
      "ds_read_u16 v86, v11 offset:64\n"
      "v_cvt_pk_bf16_f32 v203, v204, v205\n"
      "v_mfma_f32_16x16x16_bf16 v[96:99], v[146:147], v[218:219], v[96:99]\n"
      "v_mul_f32 v191, v178, v191\n"
      "v_mul_f32 v192, v178, v192\n"
      "v_fma_f32 v204, v184, v184, s52\n"
      "v_mfma_f32_16x16x16_bf16 v[122:125], v[120:121], v[218:219], v[122:125]\n"
      "v_mul_f32_e64 v193, v178, v193\n"
      "v_cvt_pk_bf16_f32 v159, v160, v161\n"
      "s_waitcnt vmcnt(1)\n"
      "v_or3_b32 v138, v138, v139, v140\n"
      "v_mfma_f32_16x16x16_bf16 v[116:119], v[202:203], v[218:219], v[116:119]\n"
      "v_exp_f32 v98, v98\n"
      "v_cvt_pk_bf16_f32 v190, v190, v191\n"
      "v_mfma_f32_16x16x16_bf16 v[210:213], v[188:189], v[218:219], v[210:213]\n"
      "v_add_u32 v12, 32, v12\n"
      "v_exp_f32 v123, v123\n"
      "v_mfma_f32_16x16x32_bf16 v[160:163], a[216:219], v[104:107], 0\n"
      "s_waitcnt lgkmcnt(1)\n"
      "v_exp_f32 v97, v97\n"
      "v_mfma_f32_16x16x32_bf16 v[160:163], a[220:223], v[126:129], v[160:163]\n"
      "v_exp_f32 v99, v99\n"
      "v_mfma_f32_16x16x32_bf16 v[160:163], a[224:227], v[220:223], v[160:163]\n"
      "v_exp_f32 v124, v124\n"
      "v_mfma_f32_16x16x32_bf16 v[164:167], a[240:243], v[104:107], 0\n"
      "s_add_u32 s43, s43, 1\n"
      "v_exp_f32 v117, v117\n"
      "v_mfma_f32_16x16x32_bf16 v[164:167], a[244:247], v[126:129], v[164:167]\n"
      "v_exp_f32_e64 v96, v96\n"
      "v_mfma_f32_16x16x32_bf16 v[164:167], a[248:251], v[220:223], v[164:167]\n"
      "v_exp_f32 v118, v118\n"
      "v_mfma_f32_16x16x32_bf16 v[134:137], v[232:235], v[104:107], 0\n"
      "v_exp_f32 v125, v125\n"
      "v_mfma_f32_16x16x32_bf16 v[134:137], v[236:239], v[126:129], v[134:137]\n"
      "v_cvt_pk_bf16_f32 v110, v180, v181\n"
      "v_cvt_pk_bf16_f32 v191, v192, v193\n"
      "v_mfma_f32_16x16x32_bf16 v[134:137], v[240:243], v[220:223], v[134:137]\n"
      "v_exp_f32_e64 v119, v119\n"
      "v_mfma_f32_16x16x32_bf16 v[148:151], v[174:177], v[112:115], v[52:55]\n"
      "v_cvt_pk_bf16_f32 v96, v96, v97\n"
      "v_cvt_pk_bf16_f32 v97, v98, v99\n"
      "v_mfma_f32_16x16x32_bf16 v[126:129], v[142:145], v[112:115], v[56:59]\n"
      "v_exp_f32 v210, v210\n"
      "v_mfma_f32_16x16x32_bf16 v[220:223], v[198:201], v[112:115], v[60:63]\n"
      "ds_read_b128 v[104:107], v2 offset:27904\n"
      "s_waitcnt vmcnt(0)\n"
      "v_or3_b32 v138, v138, v141, v90\n"
      "v_mfma_f32_16x16x32_bf16 v[206:209], v[100:103], v[112:115], v[26:29]\n"
      "v_exp_f32 v122, v122\n"
      "v_mfma_f32_16x16x16_bf16 v[148:151], v[146:147], v[158:159], v[148:151]\n"
      "v_exp_f32 v116, v116\n"
      "v_cvt_pk_bf16_f32 v90, v210, 0\n"
      "v_mfma_f32_16x16x16_bf16 v[126:129], v[120:121], v[158:159], v[126:129]\n"
      "v_cvt_pk_bf16_f32 v99, v124, v125\n"
      "v_cvt_pk_bf16_f32 v89, v118, v119\n"
      "v_cvt_pk_bf16_f32 v88, v116, v117\n"
      "v_mfma_f32_16x16x16_bf16 v[220:223], v[202:203], v[158:159], v[220:223]\n"
      "v_cvt_pk_bf16_f32 v98, v122, v123\n"
      "v_exp_f32 v151, v151\n"
      "v_mfma_f32_16x16x16_bf16 v[206:209], v[188:189], v[158:159], v[206:209]\n"
      "v_exp_f32 v150, v150\n"
      "v_exp_f32 v127, v127\n"
      "v_mfma_f32_16x16x32_bf16 v[116:119], v[32:35], v[96:99], 0\n"
      "v_exp_f32 v128, v128\n"
      "v_mfma_f32_16x16x32_bf16 v[116:119], v[32:35], v[88:91], v[116:119]\n"
      "v_exp_f32_e64 v149, v149\n"
      "v_mfma_f32_16x16x32_bf16 v[156:159], v[214:217], v[96:99], 0\n"
      "ds_write_b32 v31, v138 offset:4608\n"
      "v_mfma_f32_16x16x32_bf16 v[156:159], v[76:79], v[88:91], v[156:159]\n"
      "v_exp_f32_e64 v223, v223\n"
      "v_mfma_f32_16x16x32_bf16 v[208:211], v[130:133], v[96:99], 0\n"
      "v_exp_f32 v222, v222\n"
      "v_mfma_f32_16x16x32_bf16 v[208:211], v[80:83], v[88:91], v[208:211]\n"
      "v_exp_f32 v126, v126\n"
      "v_mfma_f32_16x16x32_bf16 v[112:115], v[194:197], v[96:99], 0\n"
      "s_waitcnt lgkmcnt(1)\n"
      "v_exp_f32 v148, v148\n"
      "v_mfma_f32_16x16x32_bf16 v[112:115], v[84:87], v[88:91], v[112:115]\n"
      "v_cvt_pk_bf16_f32 v93, v222, v223\n"
      "v_cvt_pk_bf16_f32 v148, v148, v149\n"
      "v_cvt_pk_bf16_f32 v149, v150, v151\n"
      "v_mfma_f32_16x16x32_bf16 v[138:141], v[174:177], v[108:111], v[64:67]\n"
      "v_exp_f32 v221, v221\n"
      "v_mfma_f32_16x16x32_bf16 v[178:181], v[142:145], v[108:111], v[68:71]\n"
      "v_exp_f32 v129, v129\n"
      "v_mfma_f32_16x16x32_bf16 v[122:125], v[198:201], v[108:111], v[72:75]\n"
      "v_exp_f32_e64 v220, v220\n"
      "v_mfma_f32_16x16x32_bf16 v[96:99], v[100:103], v[108:111], v[28:31]\n"
      "ds_read_b128 v[108:111], v2 offset:30976\n"
      "v_cvt_pk_bf16_f32 v150, v126, v127\n"
      "v_mfma_f32_16x16x16_bf16 v[138:141], v[146:147], v[190:191], v[138:141]\n"
      "v_cvt_pk_bf16_f32 v151, v128, v129\n"
      "v_exp_f32_e64 v206, v206\n"
      "v_mfma_f32_16x16x16_bf16 v[178:181], v[120:121], v[190:191], v[178:181]\n"
      "ds_read_b128 v[126:129], v2 offset:34048\n"
      "v_cvt_pk_bf16_f32 v92, v220, v221\n"
      "v_mfma_f32_16x16x16_bf16 v[122:125], v[202:203], v[190:191], v[122:125]\n"
      "v_cvt_pk_bf16_f32 v94, v206, 0\n"
      "v_rcp_f32 v116, v116\n"
      "v_mfma_f32_16x16x16_bf16 v[96:99], v[188:189], v[190:191], v[96:99]\n"
      "v_exp_f32 v139, v139\n"
      "v_exp_f32_e64 v138, v138\n"
      "v_exp_f32_e64 v140, v140\n"
      "v_pk_mul_f32 v[158:159], v[158:159], v[116:117] op_sel_hi:[1,0]\n"
      "v_pk_mul_f32 v[114:115], v[114:115], v[116:117] op_sel_hi:[1,0]\n"
      "v_pk_mul_f32 v[208:209], v[208:209], v[116:117] op_sel_hi:[1,0]\n"
      "v_pk_mul_f32 v[210:211], v[210:211], v[116:117] op_sel_hi:[1,0]\n"
      "v_pk_mul_f32 v[112:113], v[112:113], v[116:117] op_sel_hi:[1,0]\n"
      "v_pk_mul_f32 v[156:157], v[156:157], v[116:117] op_sel_hi:[1,0]\n"
      "v_mfma_f32_16x16x32_bf16 v[160:163], a[228:231], v[104:107], v[160:163]\n"
      "s_waitcnt lgkmcnt(1)\n"
      "v_cvt_pk_bf16_f32 v112, v112, v113\n"
      "v_cvt_pk_bf16_f32 v113, v114, v115\n"
      "v_mfma_f32_16x16x32_bf16 v[160:163], a[232:235], v[108:111], v[160:163]\n"
      "s_waitcnt lgkmcnt(0)\n"
      "v_exp_f32_e64 v181, v181\n"
      "v_mfma_f32_16x16x32_bf16 v[160:163], a[236:239], v[126:129], v[160:163]\n"
      "ds_read_b128 v[116:119], v7 offset:18432\n"
      "v_cvt_pk_bf16_f32 v156, v156, v157\n"
      "v_mfma_f32_16x16x32_bf16 v[164:167], a[252:255], v[104:107], v[164:167]\n"
      "v_exp_f32_e64 v141, v141\n"
      "v_mfma_f32_16x16x32_bf16 v[164:167], v[224:227], v[108:111], v[164:167]\n"
      "v_exp_f32_e64 v179, v179\n"
      "v_mfma_f32_16x16x32_bf16 v[164:167], v[228:231], v[126:129], v[164:167]\n"
      "v_exp_f32_e64 v178, v178\n"
      "v_mfma_f32_16x16x32_bf16 v[134:137], v[244:247], v[104:107], v[134:137]\n"
      "v_cvt_pk_bf16_f32 v157, v158, v159\n"
      "v_cvt_pk_bf16_f32 v159, v210, v211\n"
      "v_mfma_f32_16x16x32_bf16 v[134:137], v[248:251], v[108:111], v[134:137]\n"
      "ds_read_b128 v[108:111], v7 offset:22528\n"
      "v_cvt_pk_bf16_f32 v138, v138, v139\n"
      "v_mfma_f32_16x16x32_bf16 v[134:137], v[252:255], v[126:129], v[134:137]\n"
      "v_exp_f32_e64 v123, v123\n"
      "v_mfma_f32_16x16x32_bf16 v[190:193], v[32:35], v[148:151], 0\n"
      "ds_read_b64 v[98:99], v10 offset:16\n"
      "v_exp_f32 v180, v180\n"
      "v_mfma_f32_16x16x32_bf16 v[190:193], v[32:35], v[92:95], v[190:193]\n"
      "s_waitcnt lgkmcnt(2)\n"
      "v_cvt_pk_bf16_f32 v139, v140, v141\n"
      "v_cvt_pk_bf16_f32 v141, v180, v181\n"
      "v_mfma_f32_16x16x32_bf16 v[126:129], v[214:217], v[148:151], 0\n"
      "ds_read_b128 v[220:223], v10\n"
      "v_cvt_pk_bf16_f32 v158, v208, v209\n"
      "v_mfma_f32_16x16x32_bf16 v[126:129], v[76:79], v[92:95], v[126:129]\n"
      "buffer_store_dwordx4 v[116:119], v20, s[24:27], s46 offen sc1\n"
      "v_mfma_f32_16x16x32_bf16 v[206:209], v[130:133], v[148:151], 0\n"
      "v_exp_f32_e64 v122, v122\n"
      "v_mfma_f32_16x16x32_bf16 v[206:209], v[80:83], v[92:95], v[206:209]\n"
      "ds_write_b128 v3, v[156:159]\n"
      "v_mfma_f32_16x16x32_bf16 v[210:213], v[194:197], v[148:151], 0\n"
      "v_exp_f32 v96, v96\n"
      "v_mfma_f32_16x16x32_bf16 v[210:213], v[84:87], v[92:95], v[210:213]\n"
      "s_waitcnt lgkmcnt(1)\n"
      "v_rcp_f32 v190, v190\n"
      "v_mfma_f32_16x16x32_bf16 v[114:117], v[220:223], v[174:177], v[30:33]\n"
      "v_exp_f32 v125, v125\n"
      "v_mfma_f32_16x16x32_bf16 v[174:177], v[220:223], v[142:145], v[30:33]\n"
      "ds_read_b128 v[180:183], v7 offset:26624\n"
      "v_cvt_pk_bf16_f32 v88, v122, v123\n"
      "v_mfma_f32_16x16x32_bf16 v[148:151], v[220:223], v[198:201], v[30:33]\n"
      "ds_read_b128 v[142:145], v2 offset:22016\n"
      "v_mul_f32_e64 v205, v185, v185\n"
      "v_mfma_f32_16x16x32_bf16 v[104:107], v[220:223], v[100:103], v[30:33]\n"
      "ds_read_b128 v[156:159], v2 offset:25088\n"
      "v_fma_f32 v198, v160, v160, s52\n"
      "v_mfma_f32_16x16x16_bf16 v[114:117], v[98:99], v[146:147], v[114:117]\n"
      "v_exp_f32 v124, v124\n"
      "v_mul_f32 v199, v161, v161\n"
      "v_mfma_f32_16x16x16_bf16 v[174:177], v[98:99], v[120:121], v[174:177]\n"
      "ds_read_b128 v[120:123], v2 offset:18944\n"
      "v_cvt_pk_bf16_f32 v90, v96, 0\n"
      "v_mfma_f32_16x16x16_bf16 v[148:151], v[98:99], v[202:203], v[148:151]\n"
      "buffer_store_dwordx4 v[108:111], v21, s[24:27], s46 offen sc1\n"
      "v_pk_mul_f32 v[212:213], v[212:213], v[190:191] op_sel_hi:[1,0]\n"
      "v_pk_mul_f32 v[210:211], v[210:211], v[190:191] op_sel_hi:[1,0]\n"
      "v_pk_fma_f32 v[198:199], v[162:163], v[162:163], v[198:199]\n"
      "v_pk_fma_f32 v[204:205], v[186:187], v[186:187], v[204:205]\n"
      "v_pk_fma_f32 v[198:199], v[164:165], v[164:165], v[198:199]\n"
      "v_pk_mul_f32 v[206:207], v[206:207], v[190:191] op_sel_hi:[1,0]\n"
      "v_pk_mul_f32 v[128:129], v[128:129], v[190:191] op_sel_hi:[1,0]\n"
      "v_pk_mul_f32 v[126:127], v[126:127], v[190:191] op_sel_hi:[1,0]\n"
      "v_pk_mul_f32 v[208:209], v[208:209], v[190:191] op_sel_hi:[1,0]\n"
      "v_cvt_pk_bf16_f32 v126, v126, v127\n"
      "v_pk_fma_f32 v[204:205], v[152:153], v[152:153], v[204:205]\n"
      "v_pk_fma_f32 v[204:205], v[154:155], v[154:155], v[204:205]\n"
      "v_mfma_f32_16x16x16_bf16 v[104:107], v[98:99], v[188:189], v[104:107]\n"
      "v_cvt_pk_bf16_f32 v127, v128, v129\n"
      "v_cvt_pk_bf16_f32 v128, v206, v207\n"
      "s_waitcnt lgkmcnt(0)\n"
      "v_mfma_f32_16x16x32_bf16 v[108:111], a[216:219], v[120:123], 0\n"
      "ds_read_b128 v[200:203], v7 offset:34816\n"
      "v_cvt_pk_bf16_f32 v129, v208, v209\n"
      "v_mfma_f32_16x16x32_bf16 v[108:111], a[220:223], v[142:145], v[108:111]\n"
      "ds_write_b128 v3, v[126:129] offset:256\n"
      "v_pk_fma_f32 v[198:199], v[166:167], v[166:167], v[198:199]\n"
      "v_mfma_f32_16x16x32_bf16 v[108:111], a[224:227], v[156:159], v[108:111]\n"
      "buffer_store_dwordx4 v[180:183], v22, s[24:27], s46 offen sc1\n"
      "v_mfma_f32_16x16x32_bf16 v[116:119], a[240:243], v[120:123], 0\n"
      "s_nop 0\n"
      "ds_read_b128 v[180:183], v7 offset:30720\n"
      "v_cndmask_b32 v114, v114, v174, s[58:59]\n"
      "v_mfma_f32_16x16x32_bf16 v[116:119], a[244:247], v[142:145], v[116:119]\n"
      "v_cndmask_b32 v114, v114, v148, s[60:61]\n"
      "v_cvt_pk_bf16_f32 v89, v124, v125\n"
      "v_mfma_f32_16x16x32_bf16 v[116:119], a[248:251], v[156:159], v[116:119]\n"
      "ds_write_b64 v4, v[112:113]\n"
      "v_mfma_f32_16x16x32_bf16 v[96:99], v[232:235], v[120:123], 0\n"
      "v_cvt_pk_bf16_f32 v210, v210, v211\n"
      "v_cvt_pk_bf16_f32 v140, v178, v179\n"
      "v_mfma_f32_16x16x32_bf16 v[96:99], v[236:239], v[142:145], v[96:99]\n"
      "ds_read_b128 v[126:129], v2 offset:34304\n"
      "v_cndmask_b32 v114, v114, v104, s[62:63]\n"
      "v_mfma_f32_16x16x32_bf16 v[96:99], v[240:243], v[156:159], v[96:99]\n"
      "ds_read_b128 v[188:191], v2 offset:28160\n"
      "v_cvt_pk_bf16_f32 v211, v212, v213\n"
      "v_mfma_f32_16x16x32_bf16 v[156:159], v[32:35], v[138:141], 0\n"
      "s_waitcnt lgkmcnt(3)\n"
      "v_exp_f32 v114, v114\n"
      "v_mfma_f32_16x16x32_bf16 v[156:159], v[32:35], v[88:91], v[156:159]\n"
      "ds_write_b64 v4, v[210:211] offset:256\n"
      "v_mfma_f32_16x16x32_bf16 v[142:145], v[214:217], v[138:141], 0\n"
      "ds_read_b128 v[120:123], v2 offset:31232\n"
      "v_cvt_pk_bf16_f32 v114, v114, v114\n"
      "v_mfma_f32_16x16x32_bf16 v[142:145], v[76:79], v[88:91], v[142:145]\n"
      "buffer_store_dwordx4 v[180:183], v23, s[24:27], s46 offen sc1\n"
      "v_pk_fma_f32 v[198:199], v[134:135], v[134:135], v[198:199]\n"
      "v_pk_fma_f32 v[198:199], v[136:137], v[136:137], v[198:199]\n"
      "v_pk_fma_f32 v[204:205], v[168:169], v[168:169], v[204:205]\n"
      "v_mfma_f32_16x16x32_bf16 v[180:183], v[130:133], v[138:141], 0\n"
      "v_rcp_f32 v156, v156\n"
      "v_mfma_f32_16x16x32_bf16 v[180:183], v[80:83], v[88:91], v[180:183]\n"
      "ds_write_b16 v13, v114\n"
      "v_mfma_f32_16x16x32_bf16 v[112:115], v[194:197], v[138:141], 0\n"
      "v_fmac_f32 v204, v170, v170\n"
      "v_add_f32 v198, v198, v199\n"
      "v_mfma_f32_16x16x32_bf16 v[112:115], v[84:87], v[88:91], v[112:115]\n"
      "s_waitcnt lgkmcnt(1)\n"
      "v_fmac_f32 v205, v171, v171\n"
      "v_mul_f32 v143, v156, v143\n"
      "v_mfma_f32_16x16x32_bf16 v[108:111], a[228:231], v[188:191], v[108:111]\n"
      "ds_read_b128 v[210:213], v14\n"
      "v_mul_f32 v142, v156, v142\n"
      "v_mfma_f32_16x16x32_bf16 v[108:111], a[232:235], v[120:123], v[108:111]\n"
      "ds_read_b128 v[172:175], v14 offset:64\n"
      "v_mul_f32 v145, v156, v145\n"
      "v_mfma_f32_16x16x32_bf16 v[108:111], a[236:239], v[126:129], v[108:111]\n"
      "v_mul_f32 v144, v156, v144\n"
      "v_cvt_pk_bf16_f32 v142, v142, v143\n"
      "v_mfma_f32_16x16x32_bf16 v[116:119], a[252:255], v[188:191], v[116:119]\n"
      "v_mul_f32 v180, v156, v180\n"
      "v_cvt_pk_bf16_f32 v143, v144, v145\n"
      "v_mfma_f32_16x16x32_bf16 v[116:119], v[224:227], v[120:123], v[116:119]\n"
      "v_mul_f32 v182, v156, v182\n"
      "v_mul_f32 v183, v156, v183\n"
      "v_mfma_f32_16x16x32_bf16 v[116:119], v[228:231], v[126:129], v[116:119]\n"
      "v_fma_f32 v158, v108, v108, s52\n"
      "v_add_f32_e64 v204, v204, v205\n"
      "v_mfma_f32_16x16x32_bf16 v[96:99], v[244:247], v[188:191], v[96:99]\n"
      "v_mul_f32_e64 v181, v156, v181\n"
      "v_mul_f32 v159, v109, v109\n"
      "v_mfma_f32_16x16x32_bf16 v[96:99], v[248:251], v[120:123], v[96:99]\n"
      "s_waitcnt lgkmcnt(0)\n"
      "v_cvt_pk_bf16_f32 v144, v180, v181\n"
      "v_cvt_pk_bf16_f32 v145, v182, v183\n"
      "v_mfma_f32_16x16x32_bf16 v[96:99], v[252:255], v[126:129], v[96:99]\n"
      "ds_write_b128 v3, v[142:145] offset:512\n"
      "v_pk_fma_f32 v[158:159], v[110:111], v[110:111], v[158:159]\n"
      "v_pk_fma_f32 v[158:159], v[116:117], v[116:117], v[158:159]\n"
      "v_pk_mul_f32 v[112:113], v[112:113], v[156:157] op_sel_hi:[1,0]\n"
      "v_permlane16_swap_b32 v204, v198\n"
      "v_add_f32 v204, v204, v198\n"
      "v_cvt_pk_bf16_f32 v112, v112, v113\n"
      "v_pk_fma_f32 v[158:159], v[118:119], v[118:119], v[158:159]\n"
      "v_pk_fma_f32 v[158:159], v[96:97], v[96:97], v[158:159]\n"
      "v_pk_mul_f32 v[114:115], v[114:115], v[156:157] op_sel_hi:[1,0]\n"
      "v_pk_fma_f32 v[158:159], v[98:99], v[98:99], v[158:159]\n"
      "v_add_f32 v158, v158, v159\n"
      "v_mov_b32 v198, v158\n"
      "v_cvt_pk_bf16_f32 v113, v114, v115\n"
      "s_and_b32 s86, s86, -4\n"
      "ds_write_b64 v4, v[112:113] offset:512\n"
      "v_permlane16_swap_b32 v158, v198\n"
      "v_add_f32 v158, v158, v198\n"
      "s_nop 1\n"
      "v_permlane32_swap_b32 v204, v158\n"
      "v_add_f32 v204, v204, v158\n"
      "v_mfma_f32_16x16x32_bf16 v[188:191], v[210:213], v[32:35], 0\n"
      "ds_write_b32 v31, v204\n"
      "buffer_store_dwordx4 v[200:203], v25, s[24:27], s46 offen sc1\n"
      "v_mfma_f32_16x16x32_bf16 v[188:191], v[172:175], v[32:35], v[188:191]\n"
      "s_add_u32 s42, s42, 1\n"
      "s_waitcnt lgkmcnt(0)\n"
      "s_barrier\n"
      "s_cbranch_vccz L_loop1_%=\n"
      "s_branch L_disp1_%=\n"
      "s_nop 7\n"
      "s_nop 7\n"
      "L_max1_%=:\n"
      "v_mfma_f32_16x16x32_bf16 v[120:123], v[210:213], v[214:217], 0\n"
      "ds_read_b128 v[156:159], v0 offset:18432\n"
      "s_add_u32 m0, s64, 0\n"
      "v_mfma_f32_16x16x32_bf16 v[120:123], v[172:175], v[76:79], v[120:123]\n"
      "ds_read_b128 v[140:143], v9\n"
      "v_mfma_f32_16x16x32_bf16 v[148:151], v[210:213], v[130:133], 0\n"
      "ds_read_b128 v[144:147], v1 offset:30720\n"
      "ds_read_b128 v[198:201], v1 offset:18432\n"
      "v_mfma_f32_16x16x32_bf16 v[148:151], v[172:175], v[80:83], v[148:151]\n"
      "buffer_load_dwordx4 v16, s[16:19], s84 offen  lds\n"
      "v_mfma_f32_16x16x32_bf16 v[178:181], v[210:213], v[194:197], 0\n"
      "ds_read_b128 v[104:107], v1 offset:24576\n"
      "v_readlane_b32 s45, v37, s43\n"
      "v_mfma_f32_16x16x32_bf16 v[178:181], v[172:175], v[84:87], v[178:181]\n"
      "ds_read_b128 v[210:213], v0 offset:24576\n"
      "s_waitcnt lgkmcnt(5)\n"
      "v_mfma_f32_16x16x32_bf16 v[202:205], a[0:3], v[156:159], 0\n"
      "ds_read_b128 v[122:125], v0 offset:26624\n"
      "v_cndmask_b32 v120, v120, v148, s[58:59]\n"
      "v_mfma_f32_16x16x32_bf16 v[218:221], a[24:27], v[156:159], 0\n"
      "s_waitcnt lgkmcnt(5)\n"
      "v_add_f32 v142, v142, v143\n"
      "v_add_f32 v140, v140, v141\n"
      "v_mfma_f32_16x16x32_bf16 v[180:183], a[48:51], v[156:159], 0\n"
      "ds_read_b128 v[100:103], v0 offset:30720\n"
      "v_add_f32_e64 v140, v140, v142\n"
      "v_mfma_f32_16x16x32_bf16 v[126:129], a[72:75], v[156:159], 0\n"
      "s_add_u32 m0, s64, 4096\n"
      "v_rsq_f32 v140, v140\n"
      "v_mfma_f32_16x16x32_bf16 v[190:193], a[96:99], v[156:159], 0\n"
      "s_waitcnt lgkmcnt(3)\n"
      "v_mul_f32 v140, 0x415db3d7, v140\n"
      "v_mov_b32_e64 v142, v140\n"
      "v_mfma_f32_16x16x32_bf16 v[174:177], a[120:123], v[156:159], 0\n"
      "buffer_load_dwordx4 v17, s[16:19], s84 offen  lds\n"
      "v_mfma_f32_16x16x32_bf16 v[202:205], a[4:7], v[198:201], v[202:205]\n"
      "s_waitcnt lgkmcnt(1)\n"
      "v_permlane32_swap_b32 v140, v142\n"
      "v_mfma_f32_16x16x32_bf16 v[202:205], a[8:11], v[210:213], v[202:205]\n"
      "ds_read_b128 v[112:115], v9 offset:4608\n"
      "v_mov_b32 v141, v140\n"
      "v_mfma_f32_16x16x32_bf16 v[218:221], a[28:31], v[198:201], v[218:221]\n"
      "s_nop 0\n"
      "v_permlane16_swap_b32 v140, v141\n"
      "v_mfma_f32_16x16x32_bf16 v[218:221], a[32:35], v[210:213], v[218:221]\n"
      "v_mul_f32 v152, v140, v152\n"
      "v_mul_f32 v155, v140, v155\n"
      "v_mfma_f32_16x16x32_bf16 v[180:183], a[52:55], v[198:201], v[180:183]\n"
      "v_mul_f32 v185, v140, v185\n"
      "v_mul_f32_e64 v153, v140, v153\n"
      "v_mfma_f32_16x16x32_bf16 v[180:183], a[56:59], v[210:213], v[180:183]\n"
      "s_add_u32 m0, s64, 8192\n"
      "v_mul_f32 v184, v140, v184\n"
      "v_mul_f32 v169, v140, v169\n"
      "v_mfma_f32_16x16x32_bf16 v[126:129], a[76:79], v[198:201], v[126:129]\n"
      "v_mul_f32 v165, v141, v165\n"
      "v_mul_f32 v136, v141, v136\n"
      "v_mfma_f32_16x16x32_bf16 v[126:129], a[80:83], v[210:213], v[126:129]\n"
      "buffer_load_dwordx4 v18, s[16:19], s84 offen  lds\n"
      "v_mfma_f32_16x16x32_bf16 v[190:193], a[100:103], v[198:201], v[190:193]\n"
      "v_mul_f32_e64 v164, v141, v164\n"
      "v_cvt_pk_bf16_f32 v184, v184, v185\n"
      "v_mfma_f32_16x16x32_bf16 v[190:193], a[104:107], v[210:213], v[190:193]\n"
      "s_add_u32 m0, s64, 12288\n"
      "v_mul_f32 v187, v140, v187\n"
      "v_mul_f32 v96, v142, v96\n"
      "v_mfma_f32_16x16x32_bf16 v[174:177], a[124:127], v[198:201], v[174:177]\n"
      "s_waitcnt lgkmcnt(0)\n"
      "v_mul_f32 v108, v142, v108\n"
      "v_or3_b32 v112, v112, v113, v114\n"
      "v_mfma_f32_16x16x32_bf16 v[174:177], a[128:131], v[210:213], v[174:177]\n"
      "v_mul_f32 v109, v142, v109\n"
      "v_mul_f32 v154, v140, v154\n"
      "v_mul_f32_e64 v137, v141, v137\n"
      "v_mfma_f32_16x16x32_bf16 v[202:205], a[12:15], v[104:107], v[202:205]\n"
      "buffer_load_dwordx4 v19, s[16:19], s84 offen  lds\n"
      "v_mfma_f32_16x16x32_bf16 v[202:205], a[16:19], v[100:103], v[202:205]\n"
      "v_mul_f32 v98, v142, v98\n"
      "v_mul_f32 v135, v141, v135\n"
      "v_mfma_f32_16x16x32_bf16 v[202:205], a[20:23], v[144:147], v[202:205]\n"
      "s_mov_b32 m0, s6\n"
      "v_mul_f32 v118, v142, v118\n"
      "v_mul_f32 v163, v141, v163\n"
      "v_mfma_f32_16x16x32_bf16 v[218:221], a[36:39], v[104:107], v[218:221]\n"
      "ds_read_b128 v[148:151], v0 offset:32768\n"
      "v_mul_f32 v167, v141, v167\n"
      "v_mfma_f32_16x16x32_bf16 v[218:221], a[40:43], v[100:103], v[218:221]\n"
      "v_mul_f32 v186, v140, v186\n"
      "v_mul_f32 v161, v141, v161\n"
      "v_mfma_f32_16x16x32_bf16 v[218:221], a[44:47], v[144:147], v[218:221]\n"
      "v_mul_f32_e64 v117, v142, v117\n"
      "v_readlane_b32 s46, v37, s42\n"
      "v_mfma_f32_16x16x32_bf16 v[180:183], a[60:63], v[104:107], v[180:183]\n"
      "v_mul_f32 v160, v141, v160\n"
      "v_cvt_pk_bf16_f32 v185, v186, v187\n"
      "v_mfma_f32_16x16x32_bf16 v[180:183], a[64:67], v[100:103], v[180:183]\n"
      "s_or_b32 s95, s45, s46\n"
      "v_mul_f32 v168, v140, v168\n"
      "v_mul_f32 v119, v142, v119\n"
      "v_mfma_f32_16x16x32_bf16 v[180:183], a[68:71], v[144:147], v[180:183]\n"
      "v_mul_f32_e64 v97, v142, v97\n"
      "v_fma_f32 v189, v202, v202, s51\n"
      "v_mfma_f32_16x16x32_bf16 v[126:129], a[84:87], v[104:107], v[126:129]\n"
      "v_cvt_pk_bf16_f32 v186, v152, v153\n"
      "v_mul_f32_e64 v134, v141, v134\n"
      "v_mfma_f32_16x16x32_bf16 v[126:129], a[88:91], v[100:103], v[126:129]\n"
      "buffer_load_dwordx4 v15, s[0:3], s5 offen  lds\n"
      "v_mfma_f32_16x16x32_bf16 v[126:129], a[92:95], v[144:147], v[126:129]\n"
      "v_mul_f32_e64 v162, v141, v162\n"
      "v_mov_b32_dpp v188, v188 quad_perm:[0,0,0,0] row_mask:0xf bank_mask:0xf\n"
      "v_mfma_f32_16x16x32_bf16 v[190:193], a[108:111], v[104:107], v[190:193]\n"
      "v_cvt_pk_bf16_f32 v134, v134, v135\n"
      "v_cvt_pk_bf16_f32 v187, v154, v155\n"
      "v_mfma_f32_16x16x32_bf16 v[190:193], a[112:115], v[100:103], v[190:193]\n"
      "ds_write_b128 v5, v[184:187] offset:18432\n"
      "v_mfma_f32_16x16x32_bf16 v[190:193], a[116:119], v[144:147], v[190:193]\n"
      "ds_read_b128 v[152:155], v1 offset:20480\n"
      "v_fmac_f32_e64 v189, v203, v203\n"
      "v_mfma_f32_16x16x32_bf16 v[174:177], a[132:135], v[104:107], v[174:177]\n"
      "v_fmac_f32 v189, v204, v204\n"
      "v_fma_f32 v179, v126, v126, s51\n"
      "v_mfma_f32_16x16x32_bf16 v[174:177], a[136:139], v[100:103], v[174:177]\n"
      "v_fmac_f32 v189, v205, v205\n"
      "v_cvt_pk_bf16_f32 v168, v168, v169\n"
      "v_mfma_f32_16x16x32_bf16 v[174:177], a[140:143], v[144:147], v[174:177]\n"
      "buffer_load_dwordx4 v[206:209], v36, s[28:31], s86 offen\n"
      "v_mfma_f32_16x16x32_bf16 v[214:217], v[156:159], a[144:147], 0\n"
      "v_cvt_pk_bf16_f32 v160, v160, v161\n"
      "v_mul_f32 v171, v140, v171\n"
      "v_mfma_f32_16x16x32_bf16 v[214:217], v[198:201], a[148:151], v[214:217]\n"
      "v_cvt_pk_bf16_f32 v161, v162, v163\n"
      "v_fmac_f32 v189, v218, v218\n"
      "v_mfma_f32_16x16x32_bf16 v[214:217], v[210:213], a[152:155], v[214:217]\n"
      "ds_read_b128 v[184:187], v0 offset:22528\n"
      "v_mul_f32 v116, v142, v116\n"
      "v_mfma_f32_16x16x32_bf16 v[214:217], v[104:107], a[156:159], v[214:217]\n"
      "v_mul_f32 v166, v141, v166\n"
      "v_mul_f32 v170, v140, v170\n"
      "v_mfma_f32_16x16x32_bf16 v[214:217], v[100:103], a[160:163], v[214:217]\n"
      "v_mul_f32 v99, v142, v99\n"
      "v_fmac_f32 v189, v219, v219\n"
      "v_mfma_f32_16x16x32_bf16 v[214:217], v[144:147], a[164:167], v[214:217]\n"
      "v_fmac_f32 v179, v127, v127\n"
      "v_fmac_f32 v189, v220, v220\n"
      "v_mfma_f32_16x16x32_bf16 v[130:133], v[156:159], a[168:171], 0\n"
      "v_fmac_f32 v179, v128, v128\n"
      "v_cvt_pk_bf16_f32 v96, v96, v97\n"
      "v_mfma_f32_16x16x32_bf16 v[130:133], v[198:201], a[172:175], v[130:133]\n"
      "v_cvt_pk_bf16_f32 v169, v170, v171\n"
      "v_cvt_pk_bf16_f32 v163, v166, v167\n"
      "v_mfma_f32_16x16x32_bf16 v[130:133], v[210:213], a[176:179], v[130:133]\n"
      "v_cvt_pk_bf16_f32 v135, v136, v137\n"
      "v_fmac_f32 v179, v129, v129\n"
      "v_mfma_f32_16x16x32_bf16 v[130:133], v[104:107], a[180:183], v[130:133]\n"
      "ds_read_b128 v[136:139], v1 offset:26624\n"
      "v_fmac_f32 v189, v221, v221\n"
      "v_mfma_f32_16x16x32_bf16 v[130:133], v[100:103], a[184:187], v[130:133]\n"
      "ds_read_b128 v[170:173], v0 offset:20480\n"
      "v_fmac_f32 v179, v190, v190\n"
      "v_mfma_f32_16x16x32_bf16 v[130:133], v[144:147], a[188:191], v[130:133]\n"
      "v_fmac_f32 v189, v180, v180\n"
      "v_cvt_pk_bf16_f32 v214, v214, v215\n"
      "v_mfma_f32_16x16x32_bf16 v[194:197], v[156:159], a[192:195], 0\n"
      "v_cvt_pk_bf16_f32 v97, v98, v99\n"
      "v_fmac_f32_e64 v179, v191, v191\n"
      "v_mfma_f32_16x16x32_bf16 v[194:197], v[198:201], a[196:199], v[194:197]\n"
      "ds_read_b128 v[198:201], v1 offset:32768\n"
      "v_cndmask_b32 v120, v120, v178, s[60:61]\n"
      "v_mfma_f32_16x16x32_bf16 v[194:197], v[210:213], a[200:203], v[194:197]\n"
      "v_rcp_f32_e64 v188, v188\n"
      "v_mfma_f32_16x16x32_bf16 v[194:197], v[104:107], a[204:207], v[194:197]\n"
      "ds_write_b64 v6, v[168:169] offset:18432\n"
      "v_mfma_f32_16x16x32_bf16 v[194:197], v[100:103], a[208:211], v[194:197]\n"
      "s_waitcnt lgkmcnt(2)\n"
      "v_fmac_f32 v179, v192, v192\n"
      "v_cvt_pk_bf16_f32 v108, v108, v109\n"
      "v_mfma_f32_16x16x32_bf16 v[194:197], v[144:147], a[212:215], v[194:197]\n"
      "buffer_load_dword v93, v29, s[28:31], s86 offen\n"
      "v_mfma_f32_16x16x32_bf16 v[210:213], a[0:3], v[170:173], 0\n"
      "v_fmac_f32 v179, v193, v193\n"
      "v_bitop3_b32 v112, v112, v115, s33 bitop3:0xa8\n"
      "v_mfma_f32_16x16x32_bf16 v[210:213], a[4:7], v[152:155], v[210:213]\n"
      "v_mul_f32 v120, v188, v120\n"
      "v_fmac_f32_e64 v179, v174, v174\n"
      "v_mfma_f32_16x16x32_bf16 v[210:213], a[8:11], v[122:125], v[210:213]\n"
      "v_fmac_f32_e64 v179, v175, v175\n"
      "v_cvt_pk_bf16_f32 v162, v164, v165\n"
      "v_mfma_f32_16x16x32_bf16 v[210:213], a[12:15], v[136:139], v[210:213]\n"
      "v_cvt_pk_bf16_f32 v194, v194, v195\n"
      "v_readlane_b32 s47, v38, s43\n"
      "v_mfma_f32_16x16x32_bf16 v[210:213], a[16:19], v[148:151], v[210:213]\n"
      "s_waitcnt lgkmcnt(0)\n"
      "v_fmac_f32 v189, v181, v181\n"
      "v_cvt_pk_bf16_f32 v195, v196, v197\n"
      "v_mfma_f32_16x16x32_bf16 v[210:213], a[20:23], v[198:201], v[210:213]\n"
      "v_fmac_f32_e64 v189, v182, v182\n"
      "v_cvt_pk_bf16_f32 v130, v130, v131\n"
      "v_mfma_f32_16x16x32_bf16 v[100:103], a[24:27], v[170:173], 0\n"
      "ds_read_b128 v[156:159], v1 offset:22528\n"
      "v_mul_f32_e64 v110, v142, v110\n"
      "v_mfma_f32_16x16x32_bf16 v[100:103], a[28:31], v[152:155], v[100:103]\n"
      "ds_read_b128 v[166:169], v1 offset:34816\n"
      "v_mul_f32_e64 v111, v142, v111\n"
      "v_mfma_f32_16x16x32_bf16 v[100:103], a[32:35], v[122:125], v[100:103]\n"
      "ds_write_b128 v5, v[160:163] offset:24576\n"
      "v_mfma_f32_16x16x32_bf16 v[100:103], a[36:39], v[136:139], v[100:103]\n"
      "ds_read_b128 v[162:165], v0 offset:28672\n"
      "v_fma_f32 v160, v210, v210, s51\n"
      "v_mfma_f32_16x16x32_bf16 v[100:103], a[40:43], v[148:151], v[100:103]\n"
      "v_fmac_f32 v179, v176, v176\n"
      "v_fmac_f32 v160, v211, v211\n"
      "v_mfma_f32_16x16x32_bf16 v[100:103], a[44:47], v[198:201], v[100:103]\n"
      "v_fmac_f32 v179, v177, v177\n"
      "v_fmac_f32 v189, v183, v183\n"
      "v_mfma_f32_16x16x32_bf16 v[144:147], a[48:51], v[170:173], 0\n"
      "v_fmac_f32 v160, v212, v212\n"
      "v_cvt_pk_bf16_f32 v120, v120, v120\n"
      "v_mfma_f32_16x16x32_bf16 v[144:147], a[52:55], v[152:155], v[144:147]\n"
      "v_permlane16_swap_b32 v189, v179\n"
      "v_mfma_f32_16x16x32_bf16 v[144:147], a[56:59], v[122:125], v[144:147]\n"
      "ds_write_b16 v12, v120\n"
      "v_mfma_f32_16x16x32_bf16 v[144:147], a[60:63], v[136:139], v[144:147]\n"
      "v_add_f32_e64 v189, v189, v179\n"
      "v_cvt_pk_bf16_f32 v215, v216, v217\n"
      "v_mfma_f32_16x16x32_bf16 v[144:147], a[64:67], v[148:151], v[144:147]\n"
      "ds_read_u16 v86, v11 offset:1232\n"
      "v_cvt_pk_bf16_f32 v109, v110, v111\n"
      "v_mfma_f32_16x16x32_bf16 v[144:147], a[68:71], v[198:201], v[144:147]\n"
      "ds_write_b64 v6, v[134:135] offset:24576\n"
      "v_mfma_f32_16x16x32_bf16 v[104:107], a[72:75], v[170:173], 0\n"
      "v_mov_b32 v179, v189\n"
      "v_fmac_f32 v160, v213, v213\n"
      "v_mfma_f32_16x16x32_bf16 v[104:107], a[76:79], v[152:155], v[104:107]\n"
      "v_permlane32_swap_b32 v189, v179\n"
      "v_mfma_f32_16x16x32_bf16 v[104:107], a[80:83], v[122:125], v[104:107]\n"
      "s_or_b32 s95, s95, s48\n"
      "v_fmac_f32_e64 v160, v100, v100\n"
      "v_cvt_pk_bf16_f32 v131, v132, v133\n"
      "v_mfma_f32_16x16x32_bf16 v[104:107], a[84:87], v[136:139], v[104:107]\n"
      "v_add_f32 v189, v189, v179\n"
      "v_fmac_f32 v160, v101, v101\n"
      "v_mfma_f32_16x16x32_bf16 v[104:107], a[88:91], v[148:151], v[104:107]\n"
      "v_rsq_f32_e64 v189, v189\n"
      "v_mfma_f32_16x16x32_bf16 v[104:107], a[92:95], v[198:201], v[104:107]\n"
      "s_cmp_ge_u32 s95, 0x40000000\n"
      "s_cselect_b32 s94, -1, -2\n"
      "v_mov_b32 v179, v189\n"
      "v_mfma_f32_16x16x32_bf16 v[140:143], a[96:99], v[170:173], 0\n"
      "s_nop 0\n"
      "v_permlane16_swap_b32 v189, v179\n"
      "v_mfma_f32_16x16x32_bf16 v[140:143], a[100:103], v[152:155], v[140:143]\n"
      "v_fmac_f32 v160, v102, v102\n"
      "v_fma_f32 v161, v104, v104, s51\n"
      "v_mfma_f32_16x16x32_bf16 v[140:143], a[104:107], v[122:125], v[140:143]\n"
      "v_fmac_f32 v161, v105, v105\n"
      "v_cvt_pk_bf16_f32 v111, v118, v119\n"
      "v_mfma_f32_16x16x32_bf16 v[140:143], a[108:111], v[136:139], v[140:143]\n"
      "v_mul_f32 v191, v179, v191\n"
      "v_mul_f32_e64 v129, v179, v129\n"
      "v_mfma_f32_16x16x32_bf16 v[140:143], a[112:115], v[148:151], v[140:143]\n"
      "v_cvt_pk_bf16_f32 v110, v116, v117\n"
      "v_mul_f32 v189, s50, v189\n"
      "v_mfma_f32_16x16x32_bf16 v[140:143], a[116:119], v[198:201], v[140:143]\n"
      "v_mul_f32 v181, v189, v181\n"
      "v_fmac_f32 v160, v103, v103\n"
      "v_mfma_f32_16x16x32_bf16 v[114:117], a[120:123], v[170:173], 0\n"
      "v_mul_f32 v203, v189, v203\n"
      "v_fmac_f32 v161, v106, v106\n"
      "v_mfma_f32_16x16x32_bf16 v[114:117], a[124:127], v[152:155], v[114:117]\n"
      "v_fmac_f32 v161, v107, v107\n"
      "v_mul_f32_e64 v180, v189, v180\n"
      "v_mfma_f32_16x16x32_bf16 v[114:117], a[128:131], v[122:125], v[114:117]\n"
      "ds_write_b128 v5, v[108:111] offset:30720\n"
      "v_pk_mul_f32 v[126:127], v[126:127], v[178:179] op_sel:[0,1]\n"
      "v_mfma_f32_16x16x32_bf16 v[114:117], a[132:135], v[136:139], v[114:117]\n"
      "v_fmac_f32 v161, v140, v140\n"
      "v_mul_f32 v174, v179, v174\n"
      "v_mfma_f32_16x16x32_bf16 v[114:117], a[136:139], v[148:151], v[114:117]\n"
      "v_fmac_f32 v161, v141, v141\n"
      "v_fmac_f32 v160, v144, v144\n"
      "v_mfma_f32_16x16x32_bf16 v[114:117], a[140:143], v[198:201], v[114:117]\n"
      "ds_write_b64 v6, v[96:97] offset:30720\n"
      "v_mfma_f32_16x16x32_bf16 v[132:135], v[170:173], a[144:147], 0\n"
      "v_mul_f32 v128, v179, v128\n"
      "v_mul_f32 v183, v189, v183\n"
      "v_mfma_f32_16x16x32_bf16 v[132:135], v[152:155], a[148:151], v[132:135]\n"
      "v_fmac_f32 v161, v142, v142\n"
      "v_mul_f32 v220, v189, v220\n"
      "v_mfma_f32_16x16x32_bf16 v[132:135], v[122:125], a[152:155], v[132:135]\n"
      "v_mul_f32 v177, v179, v177\n"
      "v_mul_f32 v190, v179, v190\n"
      "v_mfma_f32_16x16x32_bf16 v[132:135], v[136:139], a[156:159], v[132:135]\n"
      "v_fmac_f32 v160, v145, v145\n"
      "v_fmac_f32 v161, v143, v143\n"
      "v_mfma_f32_16x16x32_bf16 v[132:135], v[148:151], a[160:163], v[132:135]\n"
      "v_mul_f32 v175, v179, v175\n"
      "v_mul_f32 v205, v189, v205\n"
      "v_mfma_f32_16x16x32_bf16 v[132:135], v[198:201], a[164:167], v[132:135]\n"
      "v_mul_f32 v182, v189, v182\n"
      "v_mul_f32 v202, v189, v202\n"
      "v_mfma_f32_16x16x32_bf16 v[96:99], v[170:173], a[168:171], 0\n"
      "v_fmac_f32_e64 v160, v146, v146\n"
      "v_cvt_pk_bf16_f32 v174, v174, v175\n"
      "v_mfma_f32_16x16x32_bf16 v[96:99], v[152:155], a[172:175], v[96:99]\n"
      "v_mul_f32 v193, v179, v193\n"
      "v_mul_f32 v176, v179, v176\n"
      "v_mfma_f32_16x16x32_bf16 v[96:99], v[122:125], a[176:179], v[96:99]\n"
      "v_cvt_pk_bf16_f32 v216, v132, v133\n"
      "v_fmac_f32 v160, v147, v147\n"
      "v_mfma_f32_16x16x32_bf16 v[96:99], v[136:139], a[180:183], v[96:99]\n"
      "v_fmac_f32 v161, v114, v114\n"
      "v_mul_f32_e64 v204, v189, v204\n"
      "v_mfma_f32_16x16x32_bf16 v[96:99], v[148:151], a[184:187], v[96:99]\n"
      "v_cvt_pk_bf16_f32 v126, v126, v127\n"
      "v_fmac_f32_e64 v161, v115, v115\n"
      "v_mfma_f32_16x16x32_bf16 v[96:99], v[198:201], a[188:191], v[96:99]\n"
      "ds_read_b128 v[108:111], v1 offset:28672\n"
      "v_fmac_f32 v161, v116, v116\n"
      "v_mfma_f32_16x16x32_bf16 v[118:121], v[170:173], a[192:195], 0\n"
      "v_fmac_f32 v161, v117, v117\n"
      "v_cvt_pk_bf16_f32 v217, v134, v135\n"
      "v_mfma_f32_16x16x32_bf16 v[118:121], v[152:155], a[196:199], v[118:121]\n"
      "v_permlane16_swap_b32 v160, v161\n"
      "v_mfma_f32_16x16x32_bf16 v[118:121], v[122:125], a[200:203], v[118:121]\n"
      "ds_read_b128 v[152:155], v0 offset:34816\n"
      "v_mul_f32 v192, v179, v192\n"
      "v_mfma_f32_16x16x32_bf16 v[118:121], v[136:139], a[204:207], v[118:121]\n"
      "v_add_f32 v160, v160, v161\n"
      "v_mov_b32 v161, v160\n"
      "v_mfma_f32_16x16x32_bf16 v[118:121], v[148:151], a[208:211], v[118:121]\n"
      "s_cmp_gt_u32 s42, s44\n"
      "s_cselect_b32 s95, s94, s95\n"
      "v_mul_f32 v219, v189, v219\n"
      "v_mfma_f32_16x16x32_bf16 v[118:121], v[198:201], a[212:215], v[118:121]\n"
      "v_permlane32_swap_b32 v160, v161\n"
      "v_mfma_f32_16x16x32_bf16 v[122:125], a[0:3], v[184:187], 0\n"
      "s_waitcnt lgkmcnt(1)\n"
      "v_add_f32 v160, v160, v161\n"
      "v_cvt_pk_bf16_f32 v180, v180, v181\n"
      "v_mfma_f32_16x16x32_bf16 v[122:125], a[4:7], v[156:159], v[122:125]\n"
      "v_rsq_f32 v160, v160\n"
      "v_mfma_f32_16x16x32_bf16 v[122:125], a[8:11], v[162:165], v[122:125]\n"
      "v_mov_b32 v161, v160\n"
      "v_cvt_pk_bf16_f32 v202, v202, v203\n"
      "v_mfma_f32_16x16x32_bf16 v[122:125], a[12:15], v[108:111], v[122:125]\n"
      "s_waitcnt lgkmcnt(0)\n"
      "v_permlane16_swap_b32 v160, v161\n"
      "v_mfma_f32_16x16x32_bf16 v[122:125], a[16:19], v[152:155], v[122:125]\n"
      "v_mul_f32_e64 v115, v161, v115\n"
      "v_cvt_pk_bf16_f32 v181, v182, v183\n"
      "v_mfma_f32_16x16x32_bf16 v[122:125], a[20:23], v[166:169], v[122:125]\n"
      "v_cvt_pk_bf16_f32 v127, v128, v129\n"
      "v_cvt_pk_bf16_f32 v175, v176, v177\n"
      "v_mfma_f32_16x16x32_bf16 v[176:179], a[24:27], v[184:187], 0\n"
      "v_mul_f32 v221, v189, v221\n"
      "v_mul_f32 v160, s50, v160\n"
      "v_mfma_f32_16x16x32_bf16 v[176:179], a[28:31], v[156:159], v[176:179]\n"
      "v_mul_f32 v104, v161, v104\n"
      "v_mul_f32 v210, v160, v210\n"
      "v_mfma_f32_16x16x32_bf16 v[176:179], a[32:35], v[162:165], v[176:179]\n"
      "v_mul_f32 v145, v160, v145\n"
      "v_mul_f32 v105, v161, v105\n"
      "v_mfma_f32_16x16x32_bf16 v[176:179], a[36:39], v[108:111], v[176:179]\n"
      "v_fma_f32 v88, v122, v122, s51\n"
      "v_mul_f32 v114, v161, v114\n"
      "v_mfma_f32_16x16x32_bf16 v[176:179], a[40:43], v[152:155], v[176:179]\n"
      "v_mul_f32 v211, v160, v211\n"
      "v_mul_f32_e64 v101, v160, v101\n"
      "v_mfma_f32_16x16x32_bf16 v[176:179], a[44:47], v[166:169], v[176:179]\n"
      "v_mul_f32 v213, v160, v213\n"
      "v_fmac_f32 v88, v123, v123\n"
      "v_mfma_f32_16x16x32_bf16 v[198:201], a[48:51], v[184:187], 0\n"
      "v_cvt_pk_bf16_f32 v203, v204, v205\n"
      "v_fmac_f32 v88, v124, v124\n"
      "v_mfma_f32_16x16x32_bf16 v[198:201], a[52:55], v[156:159], v[198:201]\n"
      "v_fmac_f32 v88, v125, v125\n"
      "v_mul_f32 v144, v160, v144\n"
      "v_mfma_f32_16x16x32_bf16 v[198:201], a[56:59], v[162:165], v[198:201]\n"
      "v_mul_f32 v218, v189, v218\n"
      "v_mul_f32 v212, v160, v212\n"
      "v_mfma_f32_16x16x32_bf16 v[198:201], a[60:63], v[108:111], v[198:201]\n"
      "v_cvt_pk_bf16_f32 v196, v118, v119\n"
      "v_mul_f32 v146, v160, v146\n"
      "v_mfma_f32_16x16x32_bf16 v[198:201], a[64:67], v[152:155], v[198:201]\n"
      "v_cvt_pk_bf16_f32 v144, v144, v145\n"
      "v_cvt_pk_bf16_f32 v210, v210, v211\n"
      "v_mfma_f32_16x16x32_bf16 v[198:201], a[68:71], v[166:169], v[198:201]\n"
      "v_mul_f32 v102, v160, v102\n"
      "v_mul_f32 v140, v161, v140\n"
      "v_mfma_f32_16x16x32_bf16 v[148:151], a[72:75], v[184:187], 0\n"
      "v_cvt_pk_bf16_f32 v104, v104, v105\n"
      "v_cvt_pk_bf16_f32 v128, v190, v191\n"
      "v_mfma_f32_16x16x32_bf16 v[148:151], a[76:79], v[156:159], v[148:151]\n"
      "v_fmac_f32 v88, v176, v176\n"
      "v_fmac_f32 v88, v177, v177\n"
      "v_mfma_f32_16x16x32_bf16 v[148:151], a[80:83], v[162:165], v[148:151]\n"
      "v_cvt_pk_bf16_f32 v129, v192, v193\n"
      "v_cmp_ne_u32 s[56:57], 0, v112\n"
      "v_mfma_f32_16x16x32_bf16 v[148:151], a[84:87], v[108:111], v[148:151]\n"
      "v_mul_f32 v106, v161, v106\n"
      "v_mul_f32 v143, v161, v143\n"
      "v_mfma_f32_16x16x32_bf16 v[148:151], a[88:91], v[152:155], v[148:151]\n"
      "v_mul_f32_e64 v116, v161, v116\n"
      "v_cvt_pk_bf16_f32 v211, v212, v213\n"
      "v_mfma_f32_16x16x32_bf16 v[148:151], a[92:95], v[166:169], v[148:151]\n"
      "v_mul_f32 v100, v160, v100\n"
      "v_fmac_f32 v88, v178, v178\n"
      "v_mfma_f32_16x16x32_bf16 v[134:137], a[96:99], v[184:187], 0\n"
      "v_cvt_pk_bf16_f32 v204, v218, v219\n"
      "v_cvt_pk_bf16_f32 v133, v98, v99\n"
      "v_mfma_f32_16x16x32_bf16 v[134:137], a[100:103], v[156:159], v[134:137]\n"
      "v_fmac_f32 v88, v179, v179\n"
      "v_mul_f32 v107, v161, v107\n"
      "v_mfma_f32_16x16x32_bf16 v[134:137], a[104:107], v[162:165], v[134:137]\n"
      "s_and_b32 s47, s47, -4\n"
      "v_fmac_f32 v88, v198, v198\n"
      "v_fma_f32 v192, v148, v148, s51\n"
      "v_mfma_f32_16x16x32_bf16 v[134:137], a[108:111], v[108:111], v[134:137]\n"
      "v_mul_f32 v142, v161, v142\n"
      "v_cvt_pk_bf16_f32 v114, v114, v115\n"
      "v_mfma_f32_16x16x32_bf16 v[134:137], a[112:115], v[152:155], v[134:137]\n"
      "v_mul_f32 v141, v161, v141\n"
      "v_fmac_f32_e64 v192, v149, v149\n"
      "v_mfma_f32_16x16x32_bf16 v[134:137], a[116:119], v[166:169], v[134:137]\n"
      "v_fmac_f32 v192, v150, v150\n"
      "v_cvt_pk_bf16_f32 v105, v106, v107\n"
      "v_mfma_f32_16x16x32_bf16 v[188:191], a[120:123], v[184:187], 0\n"
      "v_fmac_f32 v88, v199, v199\n"
      "v_cvt_pk_bf16_f32 v107, v142, v143\n"
      "v_mfma_f32_16x16x32_bf16 v[188:191], a[124:127], v[156:159], v[188:191]\n"
      "v_cvt_pk_bf16_f32 v106, v140, v141\n"
      "v_fmac_f32_e64 v88, v200, v200\n"
      "v_mfma_f32_16x16x32_bf16 v[188:191], a[128:131], v[162:165], v[188:191]\n"
      "v_cvt_pk_bf16_f32 v205, v220, v221\n"
      "v_fmac_f32_e64 v192, v151, v151\n"
      "v_mfma_f32_16x16x32_bf16 v[188:191], a[132:135], v[108:111], v[188:191]\n"
      "v_cvt_pk_bf16_f32 v132, v96, v97\n"
      "v_fmac_f32 v192, v134, v134\n"
      "v_mfma_f32_16x16x32_bf16 v[188:191], a[136:139], v[152:155], v[188:191]\n"
      "v_cvt_pk_bf16_f32 v197, v120, v121\n"
      "v_fmac_f32 v192, v135, v135\n"
      "v_mfma_f32_16x16x32_bf16 v[188:191], a[140:143], v[166:169], v[188:191]\n"
      "v_fmac_f32 v88, v201, v201\n"
      "v_mul_f32 v117, v161, v117\n"
      "v_mfma_f32_16x16x32_bf16 v[96:99], v[184:187], a[144:147], 0\n"
      "v_fmac_f32 v192, v136, v136\n"
      "v_fmac_f32 v192, v137, v137\n"
      "v_mfma_f32_16x16x32_bf16 v[96:99], v[156:159], a[148:151], v[96:99]\n"
      "v_mul_f32 v103, v160, v103\n"
      "v_mul_f32 v147, v160, v147\n"
      "v_mfma_f32_16x16x32_bf16 v[96:99], v[162:165], a[152:155], v[96:99]\n"
      "ds_read_b128 v[118:121], v2 offset:3072\n"
      "v_cvt_pk_bf16_f32 v213, v102, v103\n"
      "v_mfma_f32_16x16x32_bf16 v[96:99], v[108:111], a[156:159], v[96:99]\n"
      "v_fmac_f32 v192, v188, v188\n"
      "v_fmac_f32 v192, v189, v189\n"
      "v_mfma_f32_16x16x32_bf16 v[96:99], v[152:155], a[160:163], v[96:99]\n"
      "v_cvt_pk_bf16_f32 v212, v100, v101\n"
      "v_fmac_f32 v192, v190, v190\n"
      "v_mfma_f32_16x16x32_bf16 v[96:99], v[166:169], a[164:167], v[96:99]\n"
      "v_cvt_pk_bf16_f32 v115, v116, v117\n"
      "v_fmac_f32 v192, v191, v191\n"
      "v_mfma_f32_16x16x32_bf16 v[100:103], v[184:187], a[168:171], 0\n"
      "s_nop 0\n"
      "v_permlane16_swap_b32 v88, v192\n"
      "v_mfma_f32_16x16x32_bf16 v[100:103], v[156:159], a[172:175], v[100:103]\n"
      "v_add_f32 v88, v88, v192\n"
      "v_mov_b32 v192, v88\n"
      "v_cvt_pk_bf16_f32 v76, v96, v97\n"
      "v_mfma_f32_16x16x32_bf16 v[100:103], v[162:165], a[176:179], v[100:103]\n"
      "v_permlane32_swap_b32_e64 v88, v192\n"
      "v_mfma_f32_16x16x32_bf16 v[100:103], v[108:111], a[180:183], v[100:103]\n"
      "ds_read_b128 v[138:141], v2\n"
      "v_add_f32 v88, v88, v192\n"
      "v_mfma_f32_16x16x32_bf16 v[100:103], v[152:155], a[184:187], v[100:103]\n"
      "v_rsq_f32 v88, v88\n"
      "v_mfma_f32_16x16x32_bf16 v[100:103], v[166:169], a[188:191], v[100:103]\n"
      "ds_read_u16 v82, v11 offset:1200\n"
      "v_mov_b32 v192, v88\n"
      "v_mfma_f32_16x16x32_bf16 v[218:221], v[184:187], a[192:195], 0\n"
      "s_nop 0\n"
      "v_permlane16_swap_b32_e64 v88, v192\n"
      "v_mfma_f32_16x16x32_bf16 v[218:221], v[156:159], a[196:199], v[218:221]\n"
      "ds_read_b128 v[158:161], v2 offset:6144\n"
      "v_mul_f32 v135, v192, v135\n"
      "v_mfma_f32_16x16x32_bf16 v[218:221], v[162:165], a[200:203], v[218:221]\n"
      "v_mul_f32 v148, v192, v148\n"
      "v_mul_f32 v151, v192, v151\n"
      "v_mfma_f32_16x16x32_bf16 v[218:221], v[108:111], a[204:207], v[218:221]\n"
      "ds_read_b128 v[162:165], v2 offset:9216\n"
      "v_mul_f32 v150, v192, v150\n"
      "v_mfma_f32_16x16x32_bf16 v[218:221], v[152:155], a[208:211], v[218:221]\n"
      "s_waitcnt lgkmcnt(3)\n"
      "v_cvt_pk_bf16_f32 v77, v98, v99\n"
      "v_mul_f32 v149, v192, v149\n"
      "v_mfma_f32_16x16x32_bf16 v[218:221], v[166:169], a[212:215], v[218:221]\n"
      "ds_read_u16 v78, v11 offset:1168\n"
      "v_mul_f32 v137, v192, v137\n"
      "v_mfma_f32_16x16x32_bf16 v[184:187], a[216:219], v[138:141], 0\n"
      "ds_read_b64 v[108:109], v10 offset:1192\n"
      "v_mul_f32 v191, v192, v191\n"
      "v_mfma_f32_16x16x32_bf16 v[184:187], a[220:223], v[118:121], v[184:187]\n"
      "s_waitcnt lgkmcnt(3)\n"
      "v_mul_f32 v188, v192, v188\n"
      "v_cvt_pk_bf16_f32 v145, v146, v147\n"
      "v_mfma_f32_16x16x32_bf16 v[184:187], a[224:227], v[158:161], v[184:187]\n"
      "ds_read_b128 v[110:113], v10 offset:1200\n"
      "v_cvt_pk_bf16_f32 v85, v220, v221\n"
      "v_mfma_f32_16x16x32_bf16 v[152:155], a[240:243], v[138:141], 0\n"
      "v_mul_f32 v136, v192, v136\n"
      "v_mul_f32 v189, v192, v189\n"
      "v_mfma_f32_16x16x32_bf16 v[152:155], a[244:247], v[118:121], v[152:155]\n"
      "v_cvt_pk_bf16_f32 v188, v188, v189\n"
      "v_cvt_pk_bf16_f32 v148, v148, v149\n"
      "v_mfma_f32_16x16x32_bf16 v[152:155], a[248:251], v[158:161], v[152:155]\n"
      "v_mul_f32 v134, v192, v134\n"
      "v_mul_f32 v88, s50, v88\n"
      "v_mfma_f32_16x16x32_bf16 v[168:171], v[232:235], v[138:141], 0\n"
      "v_mul_f32_e64 v178, v88, v178\n"
      "v_cvt_pk_bf16_f32 v149, v150, v151\n"
      "v_mfma_f32_16x16x32_bf16 v[168:171], v[236:239], v[118:121], v[168:171]\n"
      "v_cvt_pk_bf16_f32 v80, v100, v101\n"
      "v_mul_f32_e64 v190, v192, v190\n"
      "v_mfma_f32_16x16x32_bf16 v[168:171], v[240:243], v[158:161], v[168:171]\n"
      "v_cvt_pk_bf16_f32 v150, v134, v135\n"
      "v_cvt_pk_bf16_f32 v151, v136, v137\n"
      "v_mfma_f32_16x16x32_bf16 v[220:223], v[126:129], v[202:205], v[40:43]\n"
      "ds_read_b128 v[96:99], v2 offset:12288\n"
      "v_mul_f32_e64 v200, v88, v200\n"
      "v_mfma_f32_16x16x32_bf16 v[156:159], v[104:107], v[202:205], v[44:47]\n"
      "s_waitcnt lgkmcnt(1)\n"
      "v_cvt_pk_bf16_f32 v189, v190, v191\n"
      "v_mul_f32 v198, v88, v198\n"
      "v_mfma_f32_16x16x32_bf16 v[134:137], v[148:151], v[202:205], v[48:51]\n"
      "ds_read_b128 v[190:193], v2 offset:15360\n"
      "v_cvt_pk_bf16_f32 v81, v102, v103\n"
      "v_mfma_f32_16x16x32_bf16 v[138:141], v[110:113], v[202:205], v[24:27]\n"
      "v_mul_f32 v122, v88, v122\n"
      "v_mul_f32 v124, v88, v124\n"
      "v_mfma_f32_16x16x16_bf16 v[220:223], v[174:175], v[180:181], v[220:223]\n"
      "v_mul_f32 v199, v88, v199\n"
      "v_mul_f32 v179, v88, v179\n"
      "v_cvt_pk_bf16_f32 v84, v218, v219\n"
      "v_mfma_f32_16x16x16_bf16 v[156:159], v[114:115], v[180:181], v[156:159]\n"
      "v_mul_f32 v123, v88, v123\n"
      "v_cvt_pk_bf16_f32 v122, v122, v123\n"
      "v_mul_f32 v125, v88, v125\n"
      "v_mfma_f32_16x16x16_bf16 v[134:137], v[188:189], v[180:181], v[134:137]\n"
      "v_cvt_pk_bf16_f32 v123, v124, v125\n"
      "v_cvt_pk_bf16_f32 v198, v198, v199\n"
      "v_max3_f32 v103, v220, v221, v222\n"
      "v_mfma_f32_16x16x16_bf16 v[138:141], v[108:109], v[180:181], v[138:141]\n"
      "v_max3_f32 v180, v223, v156, v157\n"
      "v_mul_f32 v201, v88, v201\n"
      "v_cvt_pk_bf16_f32 v125, v178, v179\n"
      "v_mfma_f32_16x16x32_bf16 v[184:187], a[228:231], v[162:165], v[184:187]\n"
      "s_waitcnt lgkmcnt(0)\n"
      "v_max3_f32 v180, v180, v134, v135\n"
      "v_max3_f32 v103, v103, v158, v159\n"
      "v_mfma_f32_16x16x32_bf16 v[184:187], a[232:235], v[96:99], v[184:187]\n"
      "v_cvt_pk_bf16_f32 v199, v200, v201\n"
      "v_max3_f32 v103, v103, v136, v137\n"
      "v_mfma_f32_16x16x32_bf16 v[184:187], a[236:239], v[190:193], v[184:187]\n"
      "ds_read_b128 v[116:119], v2 offset:256\n"
      "v_max3_f32 v103, v103, v180, v138\n"
      "v_mfma_f32_16x16x32_bf16 v[152:155], a[252:255], v[162:165], v[152:155]\n"
      "ds_read_b128 v[180:183], v2 offset:6400\n"
      "v_mov_b32 v140, v103\n"
      "v_mfma_f32_16x16x32_bf16 v[152:155], v[224:227], v[96:99], v[152:155]\n"
      "s_nop 0\n"
      "v_permlane16_swap_b32_e64 v103, v140\n"
      "v_mfma_f32_16x16x32_bf16 v[152:155], v[228:231], v[190:193], v[152:155]\n"
      "v_max_f32 v103, v103, v140\n"
      "v_mov_b32 v140, v103\n"
      "v_mfma_f32_16x16x32_bf16 v[168:171], v[244:247], v[162:165], v[168:171]\n"
      "ds_read_b128 v[200:203], v2 offset:3328\n"
      "v_permlane32_swap_b32 v103, v140\n"
      "v_max_f32 v103, v103, v140\n"
      "v_sub_f32_e64 v138, v138, v103\n"
      "v_pk_add_f32 v[158:159], v[158:159], v[102:103] op_sel:[0,1] neg_lo:[0,1] neg_hi:[0,1]\n"
      "v_pk_add_f32 v[220:221], v[220:221], v[102:103] op_sel:[0,1] neg_lo:[0,1] neg_hi:[0,1]\n"
      "v_pk_add_f32 v[156:157], v[156:157], v[102:103] op_sel:[0,1] neg_lo:[0,1] neg_hi:[0,1]\n"
      "v_pk_add_f32 v[136:137], v[136:137], v[102:103] op_sel:[0,1] neg_lo:[0,1] neg_hi:[0,1]\n"
      "v_pk_mul_f32 v[176:177], v[176:177], v[88:89] op_sel_hi:[1,0]\n"
      "v_pk_add_f32 v[134:135], v[134:135], v[102:103] op_sel:[0,1] neg_lo:[0,1] neg_hi:[0,1]\n"
      "v_pk_add_f32 v[222:223], v[222:223], v[102:103] op_sel:[0,1] neg_lo:[0,1] neg_hi:[0,1]\n"
      "v_mfma_f32_16x16x32_bf16 v[168:171], v[248:251], v[96:99], v[168:171]\n"
      "v_exp_f32 v134, v134\n"
      "v_mfma_f32_16x16x32_bf16 v[168:171], v[252:255], v[190:193], v[168:171]\n"
      "v_exp_f32 v136, v136\n"
      "v_mfma_f32_16x16x32_bf16 v[100:103], v[126:129], v[210:213], v[52:55]\n"
      "v_exp_f32 v222, v222\n"
      "v_mfma_f32_16x16x32_bf16 v[190:193], v[104:107], v[210:213], v[56:59]\n"
      "v_exp_f32 v135, v135\n"
      "v_mfma_f32_16x16x32_bf16 v[140:143], v[148:151], v[210:213], v[60:63]\n"
      "v_exp_f32 v137, v137\n"
      "v_mfma_f32_16x16x32_bf16 v[96:99], v[110:113], v[210:213], v[26:29]\n"
      "s_waitcnt lgkmcnt(0)\n"
      "v_exp_f32 v158, v158\n"
      "v_mfma_f32_16x16x16_bf16 v[100:103], v[174:175], v[144:145], v[100:103]\n"
      "v_exp_f32 v159, v159\n"
      "v_cvt_pk_bf16_f32 v124, v176, v177\n"
      "v_mfma_f32_16x16x16_bf16 v[190:193], v[114:115], v[144:145], v[190:193]\n"
      "v_exp_f32_e64 v223, v223\n"
      "v_cvt_pk_bf16_f32 v88, v134, v135\n"
      "v_mfma_f32_16x16x16_bf16 v[140:143], v[188:189], v[144:145], v[140:143]\n"
      "v_exp_f32 v221, v221\n"
      "v_exp_f32 v220, v220\n"
      "v_mfma_f32_16x16x16_bf16 v[96:99], v[108:109], v[144:145], v[96:99]\n"
      "v_cvt_pk_bf16_f32 v220, v220, v221\n"
      "v_exp_f32_e64 v156, v156\n"
      "v_mfma_f32_16x16x32_bf16 v[160:163], a[216:219], v[116:119], 0\n"
      "v_max3_f32 v177, v100, v101, v102\n"
      "v_max3_f32 v135, v103, v190, v191\n"
      "v_mfma_f32_16x16x32_bf16 v[160:163], a[220:223], v[200:203], v[160:163]\n"
      "v_max3_f32 v135, v135, v140, v141\n"
      "v_max3_f32 v177, v177, v192, v193\n"
      "v_mfma_f32_16x16x32_bf16 v[160:163], a[224:227], v[180:183], v[160:163]\n"
      "v_exp_f32_e64 v138, v138\n"
      "v_mfma_f32_16x16x32_bf16 v[164:167], a[240:243], v[116:119], 0\n"
      "v_cvt_pk_bf16_f32 v89, v136, v137\n"
      "v_max3_f32 v177, v177, v142, v143\n"
      "v_mfma_f32_16x16x32_bf16 v[164:167], a[244:247], v[200:203], v[164:167]\n"
      "v_cvt_pk_bf16_f32 v221, v222, v223\n"
      "v_cvt_pk_bf16_f32 v223, v158, v159\n"
      "v_mfma_f32_16x16x32_bf16 v[164:167], a[248:251], v[180:183], v[164:167]\n"
      "v_cvt_pk_bf16_f32 v90, v138, 0\n"
      "v_max3_f32 v177, v177, v135, v96\n"
      "v_mfma_f32_16x16x32_bf16 v[134:137], v[232:235], v[116:119], 0\n"
      "v_exp_f32_e64 v157, v157\n"
      "v_mfma_f32_16x16x32_bf16 v[134:137], v[236:239], v[200:203], v[134:137]\n"
      "v_mov_b32 v176, v177\n"
      "v_cvt_pk_bf16_f32 v222, v156, v157\n"
      "v_mfma_f32_16x16x32_bf16 v[134:137], v[240:243], v[180:183], v[134:137]\n"
      "v_permlane16_swap_b32 v177, v176\n"
      "v_mfma_f32_16x16x32_bf16 v[116:119], v[32:35], v[220:223], 0\n"
      "ds_read_b128 v[156:159], v2 offset:9472\n"
      "v_max_f32 v177, v177, v176\n"
      "v_mfma_f32_16x16x32_bf16 v[116:119], v[32:35], v[88:91], v[116:119]\n"
      "v_mov_b32 v176, v177\n"
      "s_waitcnt vmcnt(1)\n"
      "v_or3_b32 v206, v206, v207, v208\n"
      "v_mfma_f32_16x16x32_bf16 v[144:147], v[214:217], v[220:223], 0\n"
      "v_mul_f32 v139, v185, v185\n"
      "v_fma_f32 v138, v184, v184, s52\n"
      "v_mfma_f32_16x16x32_bf16 v[144:147], v[76:79], v[88:91], v[144:147]\n"
      "v_permlane32_swap_b32 v177, v176\n"
      "v_mfma_f32_16x16x32_bf16 v[202:205], v[130:133], v[220:223], 0\n"
      "v_max_f32 v177, v177, v176\n"
      "v_sub_f32 v96, v96, v177\n"
      "s_waitcnt vmcnt(0)\n"
      "v_or3_b32 v206, v206, v209, v93\n"
      "v_rcp_f32_e64 v116, v116\n"
      "v_pk_add_f32 v[142:143], v[142:143], v[176:177] op_sel:[0,1] neg_lo:[0,1] neg_hi:[0,1]\n"
      "v_pk_add_f32 v[100:101], v[100:101], v[176:177] op_sel:[0,1] neg_lo:[0,1] neg_hi:[0,1]\n"
      "v_pk_mul_f32 v[144:145], v[144:145], v[116:117] op_sel_hi:[1,0]\n"
      "v_pk_add_f32 v[190:191], v[190:191], v[176:177] op_sel:[0,1] neg_lo:[0,1] neg_hi:[0,1]\n"
      "v_pk_add_f32 v[102:103], v[102:103], v[176:177] op_sel:[0,1] neg_lo:[0,1] neg_hi:[0,1]\n"
      "v_pk_mul_f32 v[146:147], v[146:147], v[116:117] op_sel_hi:[1,0]\n"
      "v_exp_f32 v190, v190\n"
      "v_mfma_f32_16x16x32_bf16 v[202:205], v[80:83], v[88:91], v[202:205]\n"
      "v_exp_f32 v142, v142\n"
      "v_mfma_f32_16x16x32_bf16 v[180:183], v[194:197], v[220:223], 0\n"
      "v_exp_f32_e64 v143, v143\n"
      "v_mfma_f32_16x16x32_bf16 v[180:183], v[84:87], v[88:91], v[180:183]\n"
      "v_cvt_pk_bf16_f32 v144, v144, v145\n"
      "v_cvt_pk_bf16_f32 v93, v142, v143\n"
      "v_exp_f32_e64 v100, v100\n"
      "v_cvt_pk_bf16_f32 v145, v146, v147\n"
      "v_pk_fma_f32 v[138:139], v[186:187], v[186:187], v[138:139]\n"
      "v_pk_add_f32 v[140:141], v[140:141], v[176:177] op_sel:[0,1] neg_lo:[0,1] neg_hi:[0,1]\n"
      "v_pk_mul_f32 v[202:203], v[202:203], v[116:117] op_sel_hi:[1,0]\n"
      "v_pk_add_f32 v[192:193], v[192:193], v[176:177] op_sel:[0,1] neg_lo:[0,1] neg_hi:[0,1]\n"
      "v_pk_mul_f32 v[182:183], v[182:183], v[116:117] op_sel_hi:[1,0]\n"
      "v_pk_fma_f32 v[138:139], v[152:153], v[152:153], v[138:139]\n"
      "v_pk_mul_f32 v[204:205], v[204:205], v[116:117] op_sel_hi:[1,0]\n"
      "v_pk_mul_f32 v[180:181], v[180:181], v[116:117] op_sel_hi:[1,0]\n"
      "v_cvt_pk_bf16_f32 v146, v202, v203\n"
      "v_cvt_pk_bf16_f32 v147, v204, v205\n"
      "v_mfma_f32_16x16x32_bf16 v[218:221], v[126:129], v[122:125], v[64:67]\n"
      "v_exp_f32 v193, v193\n"
      "v_mfma_f32_16x16x32_bf16 v[176:179], v[104:107], v[122:125], v[68:71]\n"
      "s_waitcnt lgkmcnt(0)\n"
      "v_exp_f32_e64 v102, v102\n"
      "v_mfma_f32_16x16x32_bf16 v[208:211], v[148:151], v[122:125], v[72:75]\n"
      "ds_read_b128 v[116:119], v2 offset:12544\n"
      "v_cvt_pk_bf16_f32 v180, v180, v181\n"
      "v_mfma_f32_16x16x32_bf16 v[202:205], v[110:113], v[122:125], v[28:31]\n"
      "ds_read_b128 v[120:123], v2 offset:15616\n"
      "v_exp_f32 v103, v103\n"
      "v_mfma_f32_16x16x16_bf16 v[218:221], v[174:175], v[198:199], v[218:221]\n"
      "v_cvt_pk_bf16_f32 v181, v182, v183\n"
      "v_exp_f32 v101, v101\n"
      "v_mfma_f32_16x16x16_bf16 v[176:179], v[114:115], v[198:199], v[176:179]\n"
      "v_cvt_pk_bf16_f32 v100, v100, v101\n"
      "v_exp_f32 v191, v191\n"
      "v_mfma_f32_16x16x16_bf16 v[208:211], v[188:189], v[198:199], v[208:211]\n"
      "v_cvt_pk_bf16_f32 v101, v102, v103\n"
      "v_exp_f32 v96, v96\n"
      "v_mfma_f32_16x16x16_bf16 v[202:205], v[108:109], v[198:199], v[202:205]\n"
      "v_cvt_pk_bf16_f32 v102, v190, v191\n"
      "v_exp_f32 v192, v192\n"
      "v_mfma_f32_16x16x32_bf16 v[160:163], a[228:231], v[156:159], v[160:163]\n"
      "s_waitcnt lgkmcnt(1)\n"
      "v_exp_f32 v141, v141\n"
      "v_mfma_f32_16x16x32_bf16 v[160:163], a[232:235], v[116:119], v[160:163]\n"
      "s_waitcnt lgkmcnt(0)\n"
      "v_max3_f32 v199, v218, v219, v220\n"
      "v_cvt_pk_bf16_f32 v94, v96, 0\n"
      "v_mfma_f32_16x16x32_bf16 v[160:163], a[236:239], v[120:123], v[160:163]\n"
      "ds_write_b128 v3, v[144:147] offset:18432\n"
      "v_mfma_f32_16x16x32_bf16 v[164:167], a[252:255], v[156:159], v[164:167]\n"
      "v_cvt_pk_bf16_f32 v103, v192, v193\n"
      "v_max3_f32 v96, v221, v176, v177\n"
      "v_mfma_f32_16x16x32_bf16 v[164:167], v[224:227], v[116:119], v[164:167]\n"
      "ds_read_b64 v[88:89], v10 offset:1184\n"
      "v_max3_f32 v199, v199, v178, v179\n"
      "v_mfma_f32_16x16x32_bf16 v[164:167], v[228:231], v[120:123], v[164:167]\n"
      "v_max3_f32 v96, v96, v208, v209\n"
      "v_max3_f32 v199, v199, v210, v211\n"
      "v_mfma_f32_16x16x32_bf16 v[134:137], v[244:247], v[156:159], v[134:137]\n"
      "v_exp_f32 v140, v140\n"
      "v_mfma_f32_16x16x32_bf16 v[134:137], v[248:251], v[116:119], v[134:137]\n"
      "ds_read_b128 v[116:119], v10 offset:1168\n"
      "v_max3_f32 v199, v199, v96, v202\n"
      "v_mfma_f32_16x16x32_bf16 v[134:137], v[252:255], v[120:123], v[134:137]\n"
      "ds_read_b128 v[96:99], v7\n"
      "v_cvt_pk_bf16_f32 v92, v140, v141\n"
      "v_mfma_f32_16x16x32_bf16 v[120:123], v[32:35], v[100:103], 0\n"
      "v_mul_f32 v159, v161, v161\n"
      "v_mov_b32 v146, v199\n"
      "v_mfma_f32_16x16x32_bf16 v[120:123], v[32:35], v[92:95], v[120:123]\n"
      "s_and_b32 s4, s45, s8\n"
      "v_permlane16_swap_b32 v199, v146\n"
      "v_mfma_f32_16x16x32_bf16 v[190:193], v[214:217], v[100:103], 0\n"
      "s_add_u32 s42, s42, 1\n"
      "v_max_f32 v199, v199, v146\n"
      "v_mov_b32 v146, v199\n"
      "v_mfma_f32_16x16x32_bf16 v[190:193], v[76:79], v[92:95], v[190:193]\n"
      "s_nop 0\n"
      "v_rcp_f32 v120, v120\n"
      "v_mfma_f32_16x16x32_bf16 v[122:125], v[130:133], v[100:103], 0\n"
      "v_permlane32_swap_b32 v199, v146\n"
      "v_mfma_f32_16x16x32_bf16 v[122:125], v[80:83], v[92:95], v[122:125]\n"
      "v_max_f32 v199, v199, v146\n"
      "v_sub_f32 v202, v202, v199\n"
      "v_mfma_f32_16x16x32_bf16 v[144:147], v[194:197], v[100:103], 0\n"
      "s_waitcnt lgkmcnt(0)\n"
      "v_exp_f32_e64 v202, v202\n"
      "v_mfma_f32_16x16x32_bf16 v[144:147], v[84:87], v[92:95], v[144:147]\n"
      "buffer_store_dwordx4 v[96:99], v20, s[24:27], s85 offen sc1\n"
      "v_fma_f32 v158, v160, v160, s52\n"
      "v_pk_mul_f32 v[190:191], v[190:191], v[120:121] op_sel_hi:[1,0]\n"
      "v_pk_add_f32 v[210:211], v[210:211], v[198:199] op_sel:[0,1] neg_lo:[0,1] neg_hi:[0,1]\n"
      "v_pk_add_f32 v[220:221], v[220:221], v[198:199] op_sel:[0,1] neg_lo:[0,1] neg_hi:[0,1]\n"
      "v_pk_fma_f32 v[138:139], v[154:155], v[154:155], v[138:139]\n"
      "v_pk_fma_f32 v[158:159], v[162:163], v[162:163], v[158:159]\n"
      "v_pk_add_f32 v[176:177], v[176:177], v[198:199] op_sel:[0,1] neg_lo:[0,1] neg_hi:[0,1]\n"
      "v_pk_mul_f32 v[192:193], v[192:193], v[120:121] op_sel_hi:[1,0]\n"
      "v_pk_fma_f32 v[158:159], v[164:165], v[164:165], v[158:159]\n"
      "v_pk_mul_f32 v[146:147], v[146:147], v[120:121] op_sel_hi:[1,0]\n"
      "v_pk_add_f32 v[208:209], v[208:209], v[198:199] op_sel:[0,1] neg_lo:[0,1] neg_hi:[0,1]\n"
      "v_pk_mul_f32 v[144:145], v[144:145], v[120:121] op_sel_hi:[1,0]\n"
      "v_pk_mul_f32 v[124:125], v[124:125], v[120:121] op_sel_hi:[1,0]\n"
      "v_pk_add_f32 v[178:179], v[178:179], v[198:199] op_sel:[0,1] neg_lo:[0,1] neg_hi:[0,1]\n"
      "v_pk_add_f32 v[218:219], v[218:219], v[198:199] op_sel:[0,1] neg_lo:[0,1] neg_hi:[0,1]\n"
      "v_cvt_pk_bf16_f32 v190, v190, v191\n"
      "v_pk_mul_f32 v[122:123], v[122:123], v[120:121] op_sel_hi:[1,0]\n"
      "v_mfma_f32_16x16x32_bf16 v[100:103], v[116:119], v[126:129], v[30:33]\n"
      "ds_read_b128 v[198:201], v2 offset:3584\n"
      "v_cvt_pk_bf16_f32 v191, v192, v193\n"
      "v_mfma_f32_16x16x32_bf16 v[96:99], v[116:119], v[104:107], v[30:33]\n"
      "ds_read_b128 v[126:129], v2 offset:6656\n"
      "v_cvt_pk_bf16_f32 v192, v122, v123\n"
      "v_mfma_f32_16x16x32_bf16 v[120:123], v[116:119], v[148:151], v[30:33]\n"
      "ds_read_b128 v[148:151], v2 offset:512\n"
      "v_exp_f32_e64 v179, v179\n"
      "v_mfma_f32_16x16x32_bf16 v[140:143], v[116:119], v[110:113], v[30:33]\n"
      "ds_read_b128 v[116:119], v7 offset:4096\n"
      "v_cvt_pk_bf16_f32 v193, v124, v125\n"
      "v_mfma_f32_16x16x16_bf16 v[100:103], v[88:89], v[174:175], v[100:103]\n"
      "v_exp_f32 v218, v218\n"
      "v_exp_f32 v210, v210\n"
      "v_mfma_f32_16x16x16_bf16 v[96:99], v[88:89], v[114:115], v[96:99]\n"
      "v_exp_f32 v221, v221\n"
      "v_exp_f32 v211, v211\n"
      "v_mfma_f32_16x16x16_bf16 v[120:123], v[88:89], v[188:189], v[120:123]\n"
      "v_exp_f32 v219, v219\n"
      "v_exp_f32 v176, v176\n"
      "v_exp_f32 v220, v220\n"
      "v_pk_fma_f32 v[138:139], v[168:169], v[168:169], v[138:139]\n"
      "v_mfma_f32_16x16x16_bf16 v[140:143], v[88:89], v[108:109], v[140:143]\n"
      "v_cndmask_b32 v100, v100, v96, s[58:59]\n"
      "v_cvt_pk_bf16_f32 v90, v202, 0\n"
      "s_waitcnt lgkmcnt(0)\n"
      "v_mfma_f32_16x16x32_bf16 v[108:111], a[216:219], v[148:151], 0\n"
      "ds_read_b128 v[122:125], v2 offset:12800\n"
      "v_cndmask_b32 v100, v100, v120, s[60:61]\n"
      "v_mfma_f32_16x16x32_bf16 v[108:111], a[220:223], v[198:201], v[108:111]\n"
      "buffer_store_dwordx4 v[116:119], v21, s[24:27], s85 offen sc1\n"
      "v_mfma_f32_16x16x32_bf16 v[108:111], a[224:227], v[126:129], v[108:111]\n"
      "ds_read_b128 v[102:105], v2 offset:9728\n"
      "v_cndmask_b32 v100, v100, v140, s[62:63]\n"
      "v_mfma_f32_16x16x32_bf16 v[116:119], a[240:243], v[148:151], 0\n"
      "ds_read_b128 v[140:143], v2 offset:15872\n"
      "v_exp_f32 v208, v208\n"
      "v_mfma_f32_16x16x32_bf16 v[116:119], a[244:247], v[198:201], v[116:119]\n"
      "ds_read_b128 v[172:175], v7 offset:8192\n"
      "v_exp_f32 v209, v209\n"
      "v_max_f32_dpp v188, v100, v100 row_ror:8 row_mask:0xf bank_mask:0xf\n"
      "v_mfma_f32_16x16x32_bf16 v[116:119], a[248:251], v[126:129], v[116:119]\n"
      "ds_write_b64 v4, v[180:181] offset:18432\n"
      "v_max_f32_dpp v188, v188, v188 row_ror:4 row_mask:0xf bank_mask:0xf\n"
      "v_mfma_f32_16x16x32_bf16 v[96:99], v[232:235], v[148:151], 0\n"
      "ds_read_b128 v[148:151], v7 offset:12288\n"
      "v_cvt_pk_bf16_f32 v89, v210, v211\n"
      "v_mfma_f32_16x16x32_bf16 v[96:99], v[236:239], v[198:201], v[96:99]\n"
      "v_max_f32_dpp v188, v188, v188 row_ror:2 row_mask:0xf bank_mask:0xf\n"
      "v_cvt_pk_bf16_f32 v88, v208, v209\n"
      "v_mfma_f32_16x16x32_bf16 v[96:99], v[240:243], v[126:129], v[96:99]\n"
      "s_waitcnt lgkmcnt(3)\n"
      "v_exp_f32 v177, v177\n"
      "v_mfma_f32_16x16x32_bf16 v[108:111], a[228:231], v[102:105], v[108:111]\n"
      "v_exp_f32 v178, v178\n"
      "v_mfma_f32_16x16x32_bf16 v[108:111], a[232:235], v[122:125], v[108:111]\n"
      "s_waitcnt lgkmcnt(2)\n"
      "v_cvt_pk_bf16_f32 v218, v218, v219\n"
      "v_max_f32_dpp v188, v188, v188 row_ror:1 row_mask:0xf bank_mask:0xf\n"
      "v_mfma_f32_16x16x32_bf16 v[108:111], a[236:239], v[140:143], v[108:111]\n"
      "ds_read_b128 v[180:183], v7 offset:16384\n"
      "v_max_f32_dpp v188, v188, v188 row_bcast:15 row_mask:0xa bank_mask:0xf\n"
      "v_mfma_f32_16x16x32_bf16 v[116:119], a[252:255], v[102:105], v[116:119]\n"
      "s_nop 0\n"
      "v_max_f32_dpp v188, v188, v188 row_bcast:31 row_mask:0xc bank_mask:0xf\n"
      "v_cvt_pk_bf16_f32 v219, v220, v221\n"
      "v_mfma_f32_16x16x32_bf16 v[116:119], v[224:227], v[122:125], v[116:119]\n"
      "buffer_store_dwordx4 v[172:175], v22, s[24:27], s85 offen sc1\n"
      "v_mfma_f32_16x16x32_bf16 v[116:119], v[228:231], v[140:143], v[116:119]\n"
      "v_readlane_b32 s72, v188, 63\n"
      "v_mul_f32 v209, v109, v109\n"
      "v_mfma_f32_16x16x32_bf16 v[96:99], v[244:247], v[102:105], v[96:99]\n"
      "ds_write_b128 v3, v[190:193] offset:18688\n"
      "v_subrev_f32 v100, s72, v100\n"
      "v_fma_f32 v208, v108, v108, s52\n"
      "v_pk_fma_f32 v[158:159], v[166:167], v[166:167], v[158:159]\n"
      "v_pk_fma_f32 v[158:159], v[134:135], v[134:135], v[158:159]\n"
      "v_exp_f32_e64 v100, v100\n"
      "v_pk_fma_f32 v[208:209], v[110:111], v[110:111], v[208:209]\n"
      "v_pk_fma_f32 v[138:139], v[170:171], v[170:171], v[138:139]\n"
      "v_cvt_pk_bf16_f32 v220, v176, v177\n"
      "v_mfma_f32_16x16x32_bf16 v[96:99], v[248:251], v[122:125], v[96:99]\n"
      "s_waitcnt lgkmcnt(1)\n"
      "v_cvt_pk_bf16_f32 v221, v178, v179\n"
      "v_cvt_pk_bf16_f32 v100, v100, v100\n"
      "v_mfma_f32_16x16x32_bf16 v[96:99], v[252:255], v[140:143], v[96:99]\n"
      "ds_write_b16 v13, v100\n"
      "v_mfma_f32_16x16x32_bf16 v[188:191], v[32:35], v[218:221], 0\n"
      "buffer_store_dwordx4 v[148:151], v23, s[24:27], s85 offen sc1\n"
      "v_pk_fma_f32 v[158:159], v[136:137], v[136:137], v[158:159]\n"
      "v_mfma_f32_16x16x32_bf16 v[188:191], v[32:35], v[88:91], v[188:191]\n"
      "v_add_f32 v158, v158, v159\n"
      "v_add_f32 v138, v138, v139\n"
      "v_mfma_f32_16x16x32_bf16 v[148:151], v[214:217], v[218:221], 0\n"
      "ds_read_b128 v[210:213], v14\n"
      "v_cvt_pk_bf16_f32 v144, v144, v145\n"
      "v_mfma_f32_16x16x32_bf16 v[148:151], v[76:79], v[88:91], v[148:151]\n"
      "ds_write_b32 v31, v206 offset:5632\n"
      "v_mfma_f32_16x16x32_bf16 v[120:123], v[130:133], v[218:221], 0\n"
      "v_rcp_f32_e64 v188, v188\n"
      "v_mfma_f32_16x16x32_bf16 v[120:123], v[80:83], v[88:91], v[120:123]\n"
      "ds_read_b128 v[172:175], v14 offset:64\n"
      "v_permlane16_swap_b32 v138, v158\n"
      "v_cvt_pk_bf16_f32 v145, v146, v147\n"
      "v_add_f32 v138, v138, v158\n"
      "v_pk_fma_f32 v[208:209], v[116:117], v[116:117], v[208:209]\n"
      "v_pk_mul_f32 v[148:149], v[148:149], v[188:189] op_sel_hi:[1,0]\n"
      "v_pk_fma_f32 v[208:209], v[118:119], v[118:119], v[208:209]\n"
      "v_pk_fma_f32 v[208:209], v[96:97], v[96:97], v[208:209]\n"
      "v_pk_fma_f32 v[208:209], v[98:99], v[98:99], v[208:209]\n"
      "v_mfma_f32_16x16x32_bf16 v[176:179], v[194:197], v[218:221], 0\n"
      "ds_write_b64 v4, v[144:145] offset:18688\n"
      "v_cvt_pk_bf16_f32 v148, v148, v149\n"
      "v_add_f32_e64 v208, v208, v209\n"
      "v_pk_mul_f32 v[120:121], v[120:121], v[188:189] op_sel_hi:[1,0]\n"
      "v_pk_mul_f32 v[122:123], v[122:123], v[188:189] op_sel_hi:[1,0]\n"
      "v_pk_mul_f32 v[150:151], v[150:151], v[188:189] op_sel_hi:[1,0]\n"
      "v_mfma_f32_16x16x32_bf16 v[176:179], v[84:87], v[88:91], v[176:179]\n"
      "s_waitcnt lgkmcnt(2)\n"
      "v_cvt_pk_bf16_f32 v149, v150, v151\n"
      "v_mov_b32 v158, v208\n"
      "v_cvt_pk_bf16_f32 v151, v122, v123\n"
      "v_cvt_pk_bf16_f32 v150, v120, v121\n"
      "s_add_u32 s43, s43, 1\n"
      "ds_write_b128 v3, v[148:151] offset:18944\n"
      "v_permlane16_swap_b32 v208, v158\n"
      "v_add_f32_e64 v208, v208, v158\n"
      "v_pk_mul_f32 v[176:177], v[176:177], v[188:189] op_sel_hi:[1,0]\n"
      "v_pk_mul_f32 v[178:179], v[178:179], v[188:189] op_sel_hi:[1,0]\n"
      "v_permlane32_swap_b32 v138, v208\n"
      "v_add_f32 v138, v138, v208\n"
      "v_mfma_f32_16x16x32_bf16 v[188:191], v[210:213], v[32:35], 0\n"
      "ds_write_b32 v31, v138 offset:1024\n"
      "v_cvt_pk_bf16_f32 v176, v176, v177\n"
      "v_cvt_pk_bf16_f32 v177, v178, v179\n"
      "s_waitcnt lgkmcnt(2)\n"
      "v_mfma_f32_16x16x32_bf16 v[188:191], v[172:175], v[32:35], v[188:191]\n"
      "ds_write_b64 v4, v[176:177] offset:18944\n"
      "v_add_u32 v10, 2336, v10\n"
      "v_add_u32 v11, 2336, v11\n"
      "buffer_store_dwordx4 v[180:183], v25, s[24:27], s85 offen sc1\n"
      "s_cmp_ge_u32 s95, 0x40000000\n"
      "s_cselect_b64 vcc, -1, s[56:57]\n"
      "s_waitcnt lgkmcnt(0)\n"
      "s_barrier\n"
      "s_cbranch_vccnz L_disp0_%=\n"
      "L_max0_%=:\n"
      "v_mfma_f32_16x16x32_bf16 v[120:123], v[210:213], v[214:217], 0\n"
      "ds_read_b128 v[206:209], v0\n"
      "v_mfma_f32_16x16x32_bf16 v[120:123], v[172:175], v[76:79], v[120:123]\n"
      "ds_read_b128 v[156:159], v9 offset:1024\n"
      "v_readlane_b32 s85, v37, s42\n"
      "v_mfma_f32_16x16x32_bf16 v[148:151], v[210:213], v[130:133], 0\n"
      "s_add_u32 m0, s64, 18432\n"
      "ds_read_b128 v[144:147], v1 offset:12288\n"
      "v_mfma_f32_16x16x32_bf16 v[148:151], v[172:175], v[80:83], v[148:151]\n"
      "buffer_load_dwordx4 v16, s[16:19], s45 offen  lds\n"
      "v_mfma_f32_16x16x32_bf16 v[178:181], v[210:213], v[194:197], 0\n"
      "ds_read_b128 v[100:103], v1\n"
      "ds_read_b128 v[122:125], v0 offset:6144\n"
      "v_mfma_f32_16x16x32_bf16 v[178:181], v[172:175], v[84:87], v[178:181]\n"
      "ds_read_b128 v[218:221], v0 offset:12288\n"
      "s_waitcnt lgkmcnt(5)\n"
      "v_mfma_f32_16x16x32_bf16 v[198:201], a[0:3], v[206:209], 0\n"
      "ds_read_b128 v[104:107], v1 offset:6144\n"
      "s_add_u32 m0, s64, 22528\n"
      "v_mfma_f32_16x16x32_bf16 v[172:175], a[24:27], v[206:209], 0\n"
      "s_waitcnt lgkmcnt(5)\n"
      "v_add_f32 v158, v158, v159\n"
      "v_add_f32 v156, v156, v157\n"
      "v_mfma_f32_16x16x32_bf16 v[126:129], a[48:51], v[206:209], 0\n"
      "buffer_load_dwordx4 v17, s[16:19], s45 offen  lds\n"
      "v_mfma_f32_16x16x32_bf16 v[180:183], a[72:75], v[206:209], 0\n"
      "s_add_u32 m0, s64, 26624\n"
      "v_mov_b32_dpp v188, v188 quad_perm:[0,0,0,0] row_mask:0xf bank_mask:0xf\n"
      "v_add_f32 v156, v156, v158\n"
      "v_mfma_f32_16x16x32_bf16 v[190:193], a[96:99], v[206:209], 0\n"
      "v_rcp_f32 v188, v188\n"
      "v_mfma_f32_16x16x32_bf16 v[210:213], a[120:123], v[206:209], 0\n"
      "s_waitcnt lgkmcnt(2)\n"
      "v_rsq_f32 v156, v156\n"
      "v_mfma_f32_16x16x32_bf16 v[198:201], a[4:7], v[100:103], v[198:201]\n"
      "ds_read_b128 v[202:205], v0 offset:2048\n"
      "v_mul_f32 v156, 0x415db3d7, v156\n"
      "v_mfma_f32_16x16x32_bf16 v[198:201], a[8:11], v[122:125], v[198:201]\n"
      "v_readlane_b32 s84, v37, s43\n"
      "v_mov_b32_e64 v158, v156\n"
      "v_mfma_f32_16x16x32_bf16 v[172:175], a[28:31], v[100:103], v[172:175]\n"
      "buffer_load_dwordx4 v18, s[16:19], s45 offen  lds\n"
      "v_mfma_f32_16x16x32_bf16 v[172:175], a[32:35], v[122:125], v[172:175]\n"
      "v_permlane32_swap_b32 v156, v158\n"
      "v_mfma_f32_16x16x32_bf16 v[126:129], a[52:55], v[100:103], v[126:129]\n"
      "v_cndmask_b32 v120, v120, v148, s[58:59]\n"
      "v_mov_b32 v157, v156\n"
      "v_mfma_f32_16x16x32_bf16 v[126:129], a[56:59], v[122:125], v[126:129]\n"
      "ds_read_b128 v[138:141], v9 offset:5632\n"
      "v_mul_f32_e64 v116, v158, v116\n"
      "v_mfma_f32_16x16x32_bf16 v[180:183], a[76:79], v[100:103], v[180:183]\n"
      "v_cndmask_b32 v120, v120, v178, s[60:61]\n"
      "v_mul_f32 v97, v158, v97\n"
      "v_mfma_f32_16x16x32_bf16 v[180:183], a[80:83], v[122:125], v[180:183]\n"
      "s_add_u32 m0, s64, 30720\n"
      "v_permlane16_swap_b32 v156, v157\n"
      "v_mfma_f32_16x16x32_bf16 v[190:193], a[100:103], v[100:103], v[190:193]\n"
      "v_mul_f32 v118, v158, v118\n"
      "v_mul_f32 v120, v188, v120\n"
      "v_mfma_f32_16x16x32_bf16 v[190:193], a[104:107], v[122:125], v[190:193]\n"
      "v_mul_f32 v171, v156, v171\n"
      "v_mul_f32 v186, v156, v186\n"
      "v_mfma_f32_16x16x32_bf16 v[210:213], a[124:127], v[100:103], v[210:213]\n"
      "s_waitcnt lgkmcnt(1)\n"
      "v_mul_f32 v185, v156, v185\n"
      "v_mul_f32_e64 v187, v156, v187\n"
      "v_mfma_f32_16x16x32_bf16 v[210:213], a[128:131], v[122:125], v[210:213]\n"
      "buffer_load_dwordx4 v19, s[16:19], s45 offen  lds\n"
      "v_mfma_f32_16x16x32_bf16 v[198:201], a[12:15], v[104:107], v[198:201]\n"
      "v_readlane_b32 s86, v38, s43\n"
      "v_mul_f32_e64 v155, v156, v155\n"
      "v_mfma_f32_16x16x32_bf16 v[198:201], a[16:19], v[218:221], v[198:201]\n"
      "v_mul_f32 v153, v156, v153\n"
      "v_mul_f32 v152, v156, v152\n"
      "v_mfma_f32_16x16x32_bf16 v[198:201], a[20:23], v[144:147], v[198:201]\n"
      "v_mul_f32 v168, v156, v168\n"
      "v_mul_f32 v169, v156, v169\n"
      "v_mfma_f32_16x16x32_bf16 v[172:175], a[36:39], v[104:107], v[172:175]\n"
      "s_mov_b32 m0, s7\n"
      "v_mul_f32 v184, v156, v184\n"
      "v_cvt_pk_bf16_f32 v184, v184, v185\n"
      "v_mfma_f32_16x16x32_bf16 v[172:175], a[40:43], v[218:221], v[172:175]\n"
      "s_nop 0\n"
      "v_fma_f32 v78, v198, v198, s51\n"
      "v_mul_f32 v162, v157, v162\n"
      "v_mfma_f32_16x16x32_bf16 v[172:175], a[44:47], v[144:147], v[172:175]\n"
      "ds_read_b128 v[176:179], v0 offset:8192\n"
      "v_cvt_pk_bf16_f32 v185, v186, v187\n"
      "v_mfma_f32_16x16x32_bf16 v[126:129], a[60:63], v[104:107], v[126:129]\n"
      "v_mul_f32 v154, v156, v154\n"
      "v_mul_f32 v170, v156, v170\n"
      "v_mfma_f32_16x16x32_bf16 v[126:129], a[64:67], v[218:221], v[126:129]\n"
      "buffer_load_dwordx4 v15, s[0:3], s4 offen  lds\n"
      "v_mfma_f32_16x16x32_bf16 v[126:129], a[68:71], v[144:147], v[126:129]\n"
      "s_or_b32 s95, s84, s85\n"
      "v_mul_f32 v109, v158, v109\n"
      "v_cvt_pk_bf16_f32 v186, v152, v153\n"
      "v_mfma_f32_16x16x32_bf16 v[180:183], a[84:87], v[104:107], v[180:183]\n"
      "v_cvt_pk_bf16_f32 v187, v154, v155\n"
      "v_mul_f32 v108, v158, v108\n"
      "v_mfma_f32_16x16x32_bf16 v[180:183], a[88:91], v[218:221], v[180:183]\n"
      "v_mul_f32 v134, v157, v134\n"
      "v_fmac_f32_e64 v78, v199, v199\n"
      "v_mfma_f32_16x16x32_bf16 v[180:183], a[92:95], v[144:147], v[180:183]\n"
      "ds_write_b128 v5, v[184:187]\n"
      "v_pk_mul_f32 v[136:137], v[136:137], v[156:157] op_sel:[0,1]\n"
      "v_mfma_f32_16x16x32_bf16 v[190:193], a[108:111], v[104:107], v[190:193]\n"
      "buffer_load_dwordx4 v[148:151], v36, s[28:31], s47 offen\n"
      "v_mfma_f32_16x16x32_bf16 v[190:193], a[112:115], v[218:221], v[190:193]\n"
      "v_mul_f32 v160, v157, v160\n"
      "v_mul_f32 v135, v157, v135\n"
      "v_mfma_f32_16x16x32_bf16 v[190:193], a[116:119], v[144:147], v[190:193]\n"
      "s_waitcnt lgkmcnt(1)\n"
      "v_mul_f32 v164, v157, v164\n"
      "v_or3_b32 v138, v138, v139, v140\n"
      "v_mfma_f32_16x16x32_bf16 v[210:213], a[132:135], v[104:107], v[210:213]\n"
      "v_fma_f32 v223, v180, v180, s51\n"
      "v_mul_f32 v96, v158, v96\n"
      "v_mfma_f32_16x16x32_bf16 v[210:213], a[136:139], v[218:221], v[210:213]\n"
      "v_fmac_f32 v223, v181, v181\n"
      "v_fmac_f32_e64 v78, v200, v200\n"
      "v_mfma_f32_16x16x32_bf16 v[210:213], a[140:143], v[144:147], v[210:213]\n"
      "ds_read_b128 v[184:187], v1 offset:2048\n"
      "v_fmac_f32 v223, v182, v182\n"
      "v_mfma_f32_16x16x32_bf16 v[214:217], v[206:209], a[144:147], 0\n"
      "v_fmac_f32 v223, v183, v183\n"
      "v_mul_f32_e64 v99, v158, v99\n"
      "v_mfma_f32_16x16x32_bf16 v[214:217], v[100:103], a[148:151], v[214:217]\n"
      "v_cvt_pk_bf16_f32 v134, v134, v135\n"
      "v_cvt_pk_bf16_f32 v108, v108, v109\n"
      "v_mfma_f32_16x16x32_bf16 v[214:217], v[122:125], a[152:155], v[214:217]\n"
      "v_fmac_f32 v223, v190, v190\n"
      "v_fmac_f32 v223, v191, v191\n"
      "v_mfma_f32_16x16x32_bf16 v[214:217], v[104:107], a[156:159], v[214:217]\n"
      "ds_read_b128 v[112:115], v1 offset:8192\n"
      "v_bitop3_b32 v138, v138, v141, s33 bitop3:0xa8\n"
      "v_mfma_f32_16x16x32_bf16 v[214:217], v[218:221], a[160:163], v[214:217]\n"
      "v_fmac_f32 v78, v201, v201\n"
      "v_mul_f32 v163, v157, v163\n"
      "v_mfma_f32_16x16x32_bf16 v[214:217], v[144:147], a[164:167], v[214:217]\n"
      "ds_read_b128 v[140:143], v0 offset:14336\n"
      "v_fmac_f32 v223, v192, v192\n"
      "v_mfma_f32_16x16x32_bf16 v[130:133], v[206:209], a[168:171], 0\n"
      "v_cvt_pk_bf16_f32 v168, v168, v169\n"
      "v_mul_f32 v167, v157, v167\n"
      "v_mfma_f32_16x16x32_bf16 v[130:133], v[100:103], a[172:175], v[130:133]\n"
      "v_cvt_pk_bf16_f32 v169, v170, v171\n"
      "v_fmac_f32 v223, v193, v193\n"
      "v_mfma_f32_16x16x32_bf16 v[130:133], v[122:125], a[176:179], v[130:133]\n"
      "v_fmac_f32 v223, v210, v210\n"
      "v_mul_f32 v111, v158, v111\n"
      "v_mfma_f32_16x16x32_bf16 v[130:133], v[104:107], a[180:183], v[130:133]\n"
      "v_mul_f32 v98, v158, v98\n"
      "v_fmac_f32 v78, v172, v172\n"
      "v_mfma_f32_16x16x32_bf16 v[130:133], v[218:221], a[184:187], v[130:133]\n"
      "v_fmac_f32 v223, v211, v211\n"
      "v_mul_f32 v165, v157, v165\n"
      "v_mfma_f32_16x16x32_bf16 v[130:133], v[144:147], a[188:191], v[130:133]\n"
      "v_fmac_f32 v78, v173, v173\n"
      "v_mul_f32_e64 v110, v158, v110\n"
      "v_mfma_f32_16x16x32_bf16 v[194:197], v[206:209], a[192:195], 0\n"
      "ds_read_b128 v[152:155], v1 offset:4096\n"
      "v_fmac_f32 v78, v174, v174\n"
      "v_mfma_f32_16x16x32_bf16 v[194:197], v[100:103], a[196:199], v[194:197]\n"
      "ds_read_b128 v[100:103], v1 offset:14336\n"
      "v_mul_f32 v166, v157, v166\n"
      "v_mfma_f32_16x16x32_bf16 v[194:197], v[122:125], a[200:203], v[194:197]\n"
      "s_or_b32 s95, s95, s48\n"
      "v_fmac_f32 v78, v175, v175\n"
      "v_cvt_pk_bf16_f32 v214, v214, v215\n"
      "v_mfma_f32_16x16x32_bf16 v[194:197], v[104:107], a[204:207], v[194:197]\n"
      "v_fmac_f32_e64 v78, v126, v126\n"
      "v_cvt_pk_bf16_f32 v215, v216, v217\n"
      "v_mfma_f32_16x16x32_bf16 v[194:197], v[218:221], a[208:211], v[194:197]\n"
      "buffer_load_dword v93, v29, s[28:31], s47 offen\n"
      "v_mfma_f32_16x16x32_bf16 v[194:197], v[144:147], a[212:215], v[194:197]\n"
      "v_cvt_pk_bf16_f32 v120, v120, v120\n"
      "v_mul_f32 v161, v157, v161\n"
      "v_mfma_f32_16x16x32_bf16 v[206:209], a[0:3], v[202:205], 0\n"
      "s_waitcnt lgkmcnt(1)\n"
      "v_cvt_pk_bf16_f32 v160, v160, v161\n"
      "v_fmac_f32_e64 v223, v212, v212\n"
      "v_mfma_f32_16x16x32_bf16 v[206:209], a[4:7], v[184:187], v[206:209]\n"
      "ds_write_b16 v12, v120 offset:16\n"
      "v_mfma_f32_16x16x32_bf16 v[206:209], a[8:11], v[176:179], v[206:209]\n"
      "v_cvt_pk_bf16_f32 v96, v96, v97\n"
      "v_fmac_f32_e64 v78, v127, v127\n"
      "v_mfma_f32_16x16x32_bf16 v[206:209], a[12:15], v[112:115], v[206:209]\n"
      "ds_write_b64 v6, v[168:169]\n"
      "v_mfma_f32_16x16x32_bf16 v[206:209], a[16:19], v[140:143], v[206:209]\n"
      "s_waitcnt lgkmcnt(2)\n"
      "v_fmac_f32 v223, v213, v213\n"
      "v_fmac_f32_e64 v78, v128, v128\n"
      "v_mfma_f32_16x16x32_bf16 v[206:209], a[20:23], v[100:103], v[206:209]\n"
      "v_mul_f32 v117, v158, v117\n"
      "v_cvt_pk_bf16_f32 v97, v98, v99\n"
      "v_mfma_f32_16x16x32_bf16 v[122:125], a[24:27], v[202:205], 0\n"
      "v_fmac_f32 v78, v129, v129\n"
      "v_cvt_pk_bf16_f32 v161, v162, v163\n"
      "v_mfma_f32_16x16x32_bf16 v[122:125], a[28:31], v[184:187], v[122:125]\n"
      "v_cvt_pk_bf16_f32 v194, v194, v195\n"
      "v_cvt_pk_bf16_f32 v109, v110, v111\n"
      "v_mfma_f32_16x16x32_bf16 v[122:125], a[32:35], v[176:179], v[122:125]\n"
      "v_permlane16_swap_b32 v78, v223\n"
      "v_mfma_f32_16x16x32_bf16 v[122:125], a[36:39], v[112:115], v[122:125]\n"
      "v_cvt_pk_bf16_f32 v130, v130, v131\n"
      "v_add_f32 v78, v78, v223\n"
      "v_mfma_f32_16x16x32_bf16 v[122:125], a[40:43], v[140:143], v[122:125]\n"
      "v_mov_b32 v223, v78\n"
      "v_cvt_pk_bf16_f32 v162, v164, v165\n"
      "v_mfma_f32_16x16x32_bf16 v[122:125], a[44:47], v[100:103], v[122:125]\n"
      "v_permlane32_swap_b32 v78, v223\n"
      "v_mfma_f32_16x16x32_bf16 v[144:147], a[48:51], v[202:205], 0\n"
      "v_cvt_pk_bf16_f32 v110, v116, v117\n"
      "v_cvt_pk_bf16_f32 v163, v166, v167\n"
      "v_mfma_f32_16x16x32_bf16 v[144:147], a[52:55], v[184:187], v[144:147]\n"
      "v_fma_f32 v76, v206, v206, s51\n"
      "v_add_f32 v78, v78, v223\n"
      "v_mfma_f32_16x16x32_bf16 v[144:147], a[56:59], v[176:179], v[144:147]\n"
      "v_rsq_f32 v78, v78\n"
      "v_mfma_f32_16x16x32_bf16 v[144:147], a[60:63], v[112:115], v[144:147]\n"
      "v_fmac_f32 v76, v207, v207\n"
      "v_mov_b32 v223, v78\n"
      "v_mfma_f32_16x16x32_bf16 v[144:147], a[64:67], v[140:143], v[144:147]\n"
      "v_mul_f32_e64 v119, v158, v119\n"
      "v_cvt_pk_bf16_f32 v131, v132, v133\n"
      "v_mfma_f32_16x16x32_bf16 v[144:147], a[68:71], v[100:103], v[144:147]\n"
      "v_permlane16_swap_b32_e64 v78, v223\n"
      "v_mfma_f32_16x16x32_bf16 v[156:159], a[72:75], v[202:205], 0\n"
      "ds_write_b128 v5, v[160:163] offset:6144\n"
      "v_mfma_f32_16x16x32_bf16 v[156:159], a[76:79], v[184:187], v[156:159]\n"
      "v_fmac_f32 v76, v208, v208\n"
      "v_fmac_f32 v76, v209, v209\n"
      "v_mfma_f32_16x16x32_bf16 v[156:159], a[80:83], v[176:179], v[156:159]\n"
      "ds_read_b128 v[168:171], v1 offset:10240\n"
      "v_mul_f32 v211, v223, v211\n"
      "v_mfma_f32_16x16x32_bf16 v[156:159], a[84:87], v[112:115], v[156:159]\n"
      "v_fmac_f32 v76, v122, v122\n"
      "v_cvt_pk_bf16_f32 v135, v136, v137\n"
      "v_mfma_f32_16x16x32_bf16 v[156:159], a[88:91], v[140:143], v[156:159]\n"
      "ds_read_b64 v[188:189], v10 offset:24\n"
      "v_mul_f32 v78, s50, v78\n"
      "v_mfma_f32_16x16x32_bf16 v[156:159], a[92:95], v[100:103], v[156:159]\n"
      "v_mul_f32 v173, v78, v173\n"
      "v_mul_f32_e64 v192, v223, v192\n"
      "v_mfma_f32_16x16x32_bf16 v[164:167], a[96:99], v[202:205], 0\n"
      "v_mul_f32_e64 v129, v78, v129\n"
      "v_cmp_ne_u32 s[56:57], 0, v138\n"
      "v_mfma_f32_16x16x32_bf16 v[164:167], a[100:103], v[184:187], v[164:167]\n"
      "ds_read_b128 v[136:139], v0 offset:10240\n"
      "v_mul_f32 v200, v78, v200\n"
      "v_mfma_f32_16x16x32_bf16 v[164:167], a[104:107], v[176:179], v[164:167]\n"
      "v_mul_f32 v193, v223, v193\n"
      "v_fma_f32 v81, v156, v156, s51\n"
      "v_mfma_f32_16x16x32_bf16 v[164:167], a[108:111], v[112:115], v[164:167]\n"
      "v_fmac_f32 v76, v123, v123\n"
      "v_fmac_f32 v76, v124, v124\n"
      "v_mfma_f32_16x16x32_bf16 v[164:167], a[112:115], v[140:143], v[164:167]\n"
      "v_fmac_f32 v76, v125, v125\n"
      "v_fmac_f32 v81, v157, v157\n"
      "v_mfma_f32_16x16x32_bf16 v[164:167], a[116:119], v[100:103], v[164:167]\n"
      "v_cvt_pk_bf16_f32 v195, v196, v197\n"
      "v_cvt_pk_bf16_f32 v111, v118, v119\n"
      "v_mfma_f32_16x16x32_bf16 v[118:121], a[120:123], v[202:205], 0\n"
      "ds_read_b128 v[160:163], v0 offset:4096\n"
      "v_mul_f32_e64 v126, v78, v126\n"
      "v_mfma_f32_16x16x32_bf16 v[118:121], a[124:127], v[184:187], v[118:121]\n"
      "ds_write_b64 v6, v[96:97] offset:12288\n"
      "v_mfma_f32_16x16x32_bf16 v[118:121], a[128:131], v[176:179], v[118:121]\n"
      "v_mul_f32 v127, v78, v127\n"
      "v_mul_f32 v212, v223, v212\n"
      "v_mfma_f32_16x16x32_bf16 v[118:121], a[132:135], v[112:115], v[118:121]\n"
      "v_mul_f32 v198, v78, v198\n"
      "v_fmac_f32 v76, v144, v144\n"
      "v_mfma_f32_16x16x32_bf16 v[118:121], a[136:139], v[140:143], v[118:121]\n"
      "v_fmac_f32 v76, v145, v145\n"
      "v_fmac_f32 v81, v158, v158\n"
      "v_mfma_f32_16x16x32_bf16 v[118:121], a[140:143], v[100:103], v[118:121]\n"
      "s_cmp_ge_u32 s95, 0x40000000\n"
      "s_cselect_b32 s94, -1, -2\n"
      "v_mul_f32 v128, v78, v128\n"
      "v_mfma_f32_16x16x32_bf16 v[104:107], v[202:205], a[144:147], 0\n"
      "v_mul_f32 v182, v223, v182\n"
      "v_mul_f32 v190, v223, v190\n"
      "v_mfma_f32_16x16x32_bf16 v[104:107], v[184:187], a[148:151], v[104:107]\n"
      "v_mul_f32 v199, v78, v199\n"
      "v_fmac_f32 v81, v159, v159\n"
      "v_mfma_f32_16x16x32_bf16 v[104:107], v[176:179], a[152:155], v[104:107]\n"
      "v_fmac_f32 v81, v164, v164\n"
      "v_mul_f32_e64 v191, v223, v191\n"
      "v_mfma_f32_16x16x32_bf16 v[104:107], v[112:115], a[156:159], v[104:107]\n"
      "ds_write_b64 v6, v[134:135] offset:6144\n"
      "v_mfma_f32_16x16x32_bf16 v[104:107], v[140:143], a[160:163], v[104:107]\n"
      "v_cvt_pk_bf16_f32 v198, v198, v199\n"
      "v_fmac_f32_e64 v81, v165, v165\n"
      "v_mfma_f32_16x16x32_bf16 v[104:107], v[100:103], a[164:167], v[104:107]\n"
      "v_fmac_f32 v81, v166, v166\n"
      "v_fmac_f32 v81, v167, v167\n"
      "v_mfma_f32_16x16x32_bf16 v[216:219], v[202:205], a[168:171], 0\n"
      "v_mul_f32 v180, v223, v180\n"
      "v_mul_f32 v181, v223, v181\n"
      "v_mfma_f32_16x16x32_bf16 v[216:219], v[184:187], a[172:175], v[216:219]\n"
      "v_mul_f32 v183, v223, v183\n"
      "v_mul_f32 v210, v223, v210\n"
      "v_mfma_f32_16x16x32_bf16 v[216:219], v[176:179], a[176:179], v[216:219]\n"
      "v_fmac_f32 v76, v146, v146\n"
      "v_fmac_f32 v81, v118, v118\n"
      "v_mfma_f32_16x16x32_bf16 v[216:219], v[112:115], a[180:183], v[216:219]\n"
      "v_mul_f32 v174, v78, v174\n"
      "v_fmac_f32 v81, v119, v119\n"
      "v_mfma_f32_16x16x32_bf16 v[216:219], v[140:143], a[184:187], v[216:219]\n"
      "ds_read_b128 v[96:99], v1 offset:16384\n"
      "v_mul_f32_e64 v213, v223, v213\n"
      "v_mfma_f32_16x16x32_bf16 v[216:219], v[100:103], a[188:191], v[216:219]\n"
      "ds_write_b128 v5, v[108:111] offset:12288\n"
      "v_mfma_f32_16x16x32_bf16 v[220:223], v[202:205], a[192:195], 0\n"
      "ds_read_b128 v[202:205], v0 offset:16384\n"
      "v_cvt_pk_bf16_f32 v126, v126, v127\n"
      "v_mfma_f32_16x16x32_bf16 v[220:223], v[184:187], a[196:199], v[220:223]\n"
      "v_fmac_f32 v81, v120, v120\n"
      "v_fmac_f32 v76, v147, v147\n"
      "v_mfma_f32_16x16x32_bf16 v[220:223], v[176:179], a[200:203], v[220:223]\n"
      "v_fmac_f32_e64 v81, v121, v121\n"
      "v_cvt_pk_bf16_f32 v210, v210, v211\n"
      "v_mfma_f32_16x16x32_bf16 v[220:223], v[112:115], a[204:207], v[220:223]\n"
      "v_cvt_pk_bf16_f32 v133, v218, v219\n"
      "v_mul_f32 v201, v78, v201\n"
      "v_mfma_f32_16x16x32_bf16 v[220:223], v[140:143], a[208:211], v[220:223]\n"
      "v_permlane16_swap_b32 v76, v81\n"
      "v_mfma_f32_16x16x32_bf16 v[220:223], v[100:103], a[212:215], v[220:223]\n"
      "s_waitcnt lgkmcnt(2)\n"
      "v_cvt_pk_bf16_f32 v132, v216, v217\n"
      "v_add_f32 v76, v76, v81\n"
      "v_mfma_f32_16x16x32_bf16 v[100:103], a[0:3], v[160:163], 0\n"
      "v_cvt_pk_bf16_f32 v199, v200, v201\n"
      "v_cvt_pk_bf16_f32 v217, v106, v107\n"
      "v_mfma_f32_16x16x32_bf16 v[100:103], a[4:7], v[152:155], v[100:103]\n"
      "v_mul_f32 v175, v78, v175\n"
      "v_mov_b32 v81, v76\n"
      "v_mfma_f32_16x16x32_bf16 v[100:103], a[8:11], v[136:139], v[100:103]\n"
      "v_cvt_pk_bf16_f32 v180, v180, v181\n"
      "v_mul_f32 v172, v78, v172\n"
      "v_mfma_f32_16x16x32_bf16 v[100:103], a[12:15], v[168:171], v[100:103]\n"
      "s_waitcnt lgkmcnt(0)\n"
      "v_permlane32_swap_b32 v76, v81\n"
      "v_add_f32 v76, v76, v81\n"
      "v_mfma_f32_16x16x32_bf16 v[100:103], a[16:19], v[202:205], v[100:103]\n"
      "v_rsq_f32 v76, v76\n"
      "v_mfma_f32_16x16x32_bf16 v[100:103], a[20:23], v[96:99], v[100:103]\n"
      "v_cvt_pk_bf16_f32 v196, v220, v221\n"
      "v_mov_b32 v81, v76\n"
      "v_mfma_f32_16x16x32_bf16 v[140:143], a[24:27], v[160:163], 0\n"
      "s_cmp_gt_u32 s42, s44\n"
      "s_cselect_b32 s95, s94, s95\n"
      "v_cvt_pk_bf16_f32 v200, v172, v173\n"
      "v_mfma_f32_16x16x32_bf16 v[140:143], a[28:31], v[152:155], v[140:143]\n"
      "v_permlane16_swap_b32 v76, v81\n"
      "v_mfma_f32_16x16x32_bf16 v[140:143], a[32:35], v[136:139], v[140:143]\n"
      "v_fma_f32 v116, v100, v100, s51\n"
      "v_fmac_f32 v116, v101, v101\n"
      "v_mfma_f32_16x16x32_bf16 v[140:143], a[36:39], v[168:171], v[140:143]\n"
      "v_fmac_f32 v116, v102, v102\n"
      "v_cvt_pk_bf16_f32 v201, v174, v175\n"
      "v_mfma_f32_16x16x32_bf16 v[140:143], a[40:43], v[202:205], v[140:143]\n"
      "v_mul_f32 v76, s50, v76\n"
      "v_cvt_pk_bf16_f32 v181, v182, v183\n"
      "v_mfma_f32_16x16x32_bf16 v[140:143], a[44:47], v[96:99], v[140:143]\n"
      "v_cvt_pk_bf16_f32 v127, v128, v129\n"
      "v_fmac_f32 v116, v103, v103\n"
      "v_mfma_f32_16x16x32_bf16 v[106:109], a[48:51], v[160:163], 0\n"
      "v_mul_f32 v146, v76, v146\n"
      "v_mul_f32 v159, v81, v159\n"
      "v_mfma_f32_16x16x32_bf16 v[106:109], a[52:55], v[152:155], v[106:109]\n"
      "v_mul_f32 v208, v76, v208\n"
      "v_mul_f32 v209, v76, v209\n"
      "v_mfma_f32_16x16x32_bf16 v[106:109], a[56:59], v[136:139], v[106:109]\n"
      "v_mul_f32 v158, v81, v158\n"
      "v_mul_f32 v156, v81, v156\n"
      "v_mfma_f32_16x16x32_bf16 v[106:109], a[60:63], v[168:171], v[106:109]\n"
      "v_fmac_f32 v116, v140, v140\n"
      "v_mul_f32 v121, v81, v121\n"
      "v_mfma_f32_16x16x32_bf16 v[106:109], a[64:67], v[202:205], v[106:109]\n"
      "v_mul_f32 v145, v76, v145\n"
      "v_mul_f32 v157, v81, v157\n"
      "v_mfma_f32_16x16x32_bf16 v[106:109], a[68:71], v[96:99], v[106:109]\n"
      "v_mul_f32 v120, v81, v120\n"
      "v_fmac_f32_e64 v116, v141, v141\n"
      "v_mfma_f32_16x16x32_bf16 v[176:179], a[72:75], v[160:163], 0\n"
      "v_cvt_pk_bf16_f32 v156, v156, v157\n"
      "v_cvt_pk_bf16_f32 v182, v190, v191\n"
      "v_mfma_f32_16x16x32_bf16 v[176:179], a[76:79], v[152:155], v[176:179]\n"
      "v_mul_f32 v125, v76, v125\n"
      "v_mul_f32 v206, v76, v206\n"
      "v_mfma_f32_16x16x32_bf16 v[176:179], a[80:83], v[136:139], v[176:179]\n"
      "v_mul_f32 v167, v81, v167\n"
      "v_mul_f32 v147, v76, v147\n"
      "v_mfma_f32_16x16x32_bf16 v[176:179], a[84:87], v[168:171], v[176:179]\n"
      "v_mul_f32 v165, v81, v165\n"
      "v_add_u32 v12, 32, v12\n"
      "v_mfma_f32_16x16x32_bf16 v[176:179], a[88:91], v[202:205], v[176:179]\n"
      "v_mul_f32 v119, v81, v119\n"
      "v_fmac_f32 v116, v142, v142\n"
      "v_mfma_f32_16x16x32_bf16 v[176:179], a[92:95], v[96:99], v[176:179]\n"
      "v_cvt_pk_bf16_f32 v157, v158, v159\n"
      "v_cvt_pk_bf16_f32 v183, v192, v193\n"
      "v_mfma_f32_16x16x32_bf16 v[172:175], a[96:99], v[160:163], 0\n"
      "v_mul_f32 v207, v76, v207\n"
      "v_mul_f32 v166, v81, v166\n"
      "v_mfma_f32_16x16x32_bf16 v[172:175], a[100:103], v[152:155], v[172:175]\n"
      "v_mul_f32 v123, v76, v123\n"
      "v_mul_f32 v118, v81, v118\n"
      "v_mfma_f32_16x16x32_bf16 v[172:175], a[104:107], v[136:139], v[172:175]\n"
      "ds_read_u16 v78, v11\n"
      "v_fmac_f32_e64 v116, v143, v143\n"
      "v_mfma_f32_16x16x32_bf16 v[172:175], a[108:111], v[168:171], v[172:175]\n"
      "v_cvt_pk_bf16_f32 v197, v222, v223\n"
      "v_cvt_pk_bf16_f32 v206, v206, v207\n"
      "v_mfma_f32_16x16x32_bf16 v[172:175], a[112:115], v[202:205], v[172:175]\n"
      "v_fma_f32 v216, v176, v176, s51\n"
      "v_fmac_f32 v116, v106, v106\n"
      "v_mfma_f32_16x16x32_bf16 v[172:175], a[116:119], v[96:99], v[172:175]\n"
      "v_mul_f32 v124, v76, v124\n"
      "v_fmac_f32_e64 v116, v107, v107\n"
      "v_mfma_f32_16x16x32_bf16 v[218:221], a[120:123], v[160:163], 0\n"
      "v_fmac_f32 v116, v108, v108\n"
      "v_fmac_f32 v216, v177, v177\n"
      "v_mfma_f32_16x16x32_bf16 v[218:221], a[124:127], v[152:155], v[218:221]\n"
      "v_cvt_pk_bf16_f32 v159, v166, v167\n"
      "v_fmac_f32 v216, v178, v178\n"
      "v_mfma_f32_16x16x32_bf16 v[218:221], a[128:131], v[136:139], v[218:221]\n"
      "v_mul_f32 v122, v76, v122\n"
      "v_mul_f32 v164, v81, v164\n"
      "v_mfma_f32_16x16x32_bf16 v[218:221], a[132:135], v[168:171], v[218:221]\n"
      "s_add_u32 s43, s43, 1\n"
      "v_fmac_f32 v216, v179, v179\n"
      "v_cvt_pk_bf16_f32 v211, v212, v213\n"
      "v_mfma_f32_16x16x32_bf16 v[218:221], a[136:139], v[202:205], v[218:221]\n"
      "v_mul_f32 v144, v76, v144\n"
      "v_cvt_pk_bf16_f32 v118, v118, v119\n"
      "v_mfma_f32_16x16x32_bf16 v[218:221], a[140:143], v[96:99], v[218:221]\n"
      "v_fmac_f32 v216, v172, v172\n"
      "v_cvt_pk_bf16_f32 v207, v208, v209\n"
      "v_mfma_f32_16x16x32_bf16 v[112:115], v[160:163], a[144:147], 0\n"
      "v_fmac_f32 v216, v173, v173\n"
      "v_fmac_f32_e64 v216, v174, v174\n"
      "v_mfma_f32_16x16x32_bf16 v[112:115], v[152:155], a[148:151], v[112:115]\n"
      "v_cvt_pk_bf16_f32 v208, v122, v123\n"
      "v_cvt_pk_bf16_f32 v209, v124, v125\n"
      "v_mfma_f32_16x16x32_bf16 v[112:115], v[136:139], a[152:155], v[112:115]\n"
      "v_fmac_f32 v216, v175, v175\n"
      "v_cvt_pk_bf16_f32 v158, v164, v165\n"
      "v_mfma_f32_16x16x32_bf16 v[112:115], v[168:171], a[156:159], v[112:115]\n"
      "v_fmac_f32 v116, v109, v109\n"
      "v_fmac_f32 v216, v218, v218\n"
      "v_mfma_f32_16x16x32_bf16 v[112:115], v[202:205], a[160:163], v[112:115]\n"
      "v_fmac_f32 v216, v219, v219\n"
      "v_fmac_f32_e64 v216, v220, v220\n"
      "v_mfma_f32_16x16x32_bf16 v[112:115], v[96:99], a[164:167], v[112:115]\n"
      "ds_read_b128 v[164:167], v2 offset:21504\n"
      "v_fmac_f32 v216, v221, v221\n"
      "v_mfma_f32_16x16x32_bf16 v[190:193], v[160:163], a[168:171], 0\n"
      "s_nop 0\n"
      "v_permlane16_swap_b32_e64 v116, v216\n"
      "v_mfma_f32_16x16x32_bf16 v[190:193], v[152:155], a[172:175], v[190:193]\n"
      "v_add_f32 v116, v116, v216\n"
      "v_mov_b32 v216, v116\n"
      "v_mfma_f32_16x16x32_bf16 v[190:193], v[136:139], a[176:179], v[190:193]\n"
      "s_nop 0\n"
      "v_permlane32_swap_b32 v116, v216\n"
      "v_mfma_f32_16x16x32_bf16 v[190:193], v[168:171], a[180:183], v[190:193]\n"
      "v_add_f32 v116, v116, v216\n"
      "v_cvt_pk_bf16_f32 v76, v112, v113\n"
      "v_mfma_f32_16x16x32_bf16 v[190:193], v[202:205], a[184:187], v[190:193]\n"
      "v_rsq_f32 v116, v116\n"
      "v_mfma_f32_16x16x32_bf16 v[190:193], v[96:99], a[188:191], v[190:193]\n"
      "ds_read_b128 v[122:125], v2 offset:18432\n"
      "v_mov_b32_e64 v216, v116\n"
      "v_mfma_f32_16x16x32_bf16 v[110:113], v[160:163], a[192:195], 0\n"
      "ds_read_b128 v[160:163], v2 offset:24576\n"
      "v_cvt_pk_bf16_f32 v144, v144, v145\n"
      "v_mfma_f32_16x16x32_bf16 v[110:113], v[152:155], a[196:199], v[110:113]\n"
      "v_permlane16_swap_b32_e64 v116, v216\n"
      "v_mfma_f32_16x16x32_bf16 v[110:113], v[136:139], a[200:203], v[110:113]\n"
      "v_mul_f32 v174, v216, v174\n"
      "v_mul_f32 v176, v216, v176\n"
      "v_mfma_f32_16x16x32_bf16 v[110:113], v[168:171], a[204:207], v[110:113]\n"
      "v_mul_f32 v218, v216, v218\n"
      "v_mul_f32 v179, v216, v179\n"
      "v_mfma_f32_16x16x32_bf16 v[110:113], v[202:205], a[208:211], v[110:113]\n"
      "v_mul_f32 v219, v216, v219\n"
      "v_mul_f32 v175, v216, v175\n"
      "v_mfma_f32_16x16x32_bf16 v[110:113], v[96:99], a[212:215], v[110:113]\n"
      "s_waitcnt lgkmcnt(1)\n"
      "v_mul_f32 v177, v216, v177\n"
      "v_cvt_pk_bf16_f32 v119, v120, v121\n"
      "v_mfma_f32_16x16x32_bf16 v[184:187], a[216:219], v[122:125], 0\n"
      "ds_read_b128 v[202:205], v10 offset:32\n"
      "v_cvt_pk_bf16_f32 v218, v218, v219\n"
      "v_mfma_f32_16x16x32_bf16 v[184:187], a[220:223], v[164:167], v[184:187]\n"
      "s_waitcnt lgkmcnt(1)\n"
      "v_mul_f32 v173, v216, v173\n"
      "v_mul_f32 v178, v216, v178\n"
      "v_mfma_f32_16x16x32_bf16 v[184:187], a[224:227], v[160:163], v[184:187]\n"
      "v_mul_f32 v116, s50, v116\n"
      "v_mul_f32 v108, v116, v108\n"
      "v_mfma_f32_16x16x32_bf16 v[152:155], a[240:243], v[122:125], 0\n"
      "ds_read_b128 v[96:99], v2 offset:27648\n"
      "v_mul_f32 v221, v216, v221\n"
      "v_mfma_f32_16x16x32_bf16 v[152:155], a[244:247], v[164:167], v[152:155]\n"
      "v_cvt_pk_bf16_f32 v176, v176, v177\n"
      "v_cvt_pk_bf16_f32 v177, v178, v179\n"
      "v_mfma_f32_16x16x32_bf16 v[152:155], a[248:251], v[160:163], v[152:155]\n"
      "v_cvt_pk_bf16_f32 v81, v192, v193\n"
      "v_mul_f32 v106, v116, v106\n"
      "v_mfma_f32_16x16x32_bf16 v[168:171], v[232:235], v[122:125], 0\n"
      "v_mul_f32 v172, v216, v172\n"
      "v_cvt_pk_bf16_f32 v178, v172, v173\n"
      "v_mfma_f32_16x16x32_bf16 v[168:171], v[236:239], v[164:167], v[168:171]\n"
      "ds_read_b128 v[164:167], v2 offset:33792\n"
      "v_cvt_pk_bf16_f32 v84, v110, v111\n"
      "v_mfma_f32_16x16x32_bf16 v[168:171], v[240:243], v[160:163], v[168:171]\n"
      "ds_read_u16 v82, v11 offset:32\n"
      "v_cvt_pk_bf16_f32 v80, v190, v191\n"
      "v_mfma_f32_16x16x32_bf16 v[190:193], v[180:183], v[198:201], v[40:43]\n"
      "v_cvt_pk_bf16_f32 v179, v174, v175\n"
      "v_mul_f32_e64 v220, v216, v220\n"
      "v_mfma_f32_16x16x32_bf16 v[136:139], v[156:159], v[198:201], v[44:47]\n"
      "s_waitcnt lgkmcnt(2)\n"
      "v_mul_f32 v100, v116, v100\n"
      "v_mul_f32_e64 v101, v116, v101\n"
      "v_mfma_f32_16x16x32_bf16 v[172:175], v[176:179], v[198:201], v[48:51]\n"
      "ds_read_b128 v[122:125], v2 offset:30720\n"
      "v_cvt_pk_bf16_f32 v219, v220, v221\n"
      "v_mfma_f32_16x16x32_bf16 v[160:163], v[202:205], v[198:201], v[24:27]\n"
      "v_mul_f32_e64 v107, v116, v107\n"
      "v_cvt_pk_bf16_f32 v77, v114, v115\n"
      "v_mfma_f32_16x16x16_bf16 v[190:193], v[210:211], v[126:127], v[190:193]\n"
      "v_mul_f32 v109, v116, v109\n"
      "v_mul_f32 v102, v116, v102\n"
      "v_mul_f32_e64 v103, v116, v103\n"
      "v_mfma_f32_16x16x16_bf16 v[136:139], v[118:119], v[126:127], v[136:139]\n"
      "v_cvt_pk_bf16_f32 v216, v104, v105\n"
      "v_cvt_pk_bf16_f32 v100, v100, v101\n"
      "v_cvt_pk_bf16_f32 v85, v112, v113\n"
      "v_mfma_f32_16x16x16_bf16 v[172:175], v[218:219], v[126:127], v[172:175]\n"
      "v_cvt_pk_bf16_f32 v106, v106, v107\n"
      "v_cvt_pk_bf16_f32 v145, v146, v147\n"
      "v_max3_f32 v199, v190, v191, v192\n"
      "v_mfma_f32_16x16x16_bf16 v[160:163], v[188:189], v[126:127], v[160:163]\n"
      "v_max3_f32 v199, v199, v138, v139\n"
      "v_cvt_pk_bf16_f32 v101, v102, v103\n"
      "v_max3_f32 v112, v193, v136, v137\n"
      "v_mfma_f32_16x16x32_bf16 v[184:187], a[228:231], v[96:99], v[184:187]\n"
      "s_waitcnt lgkmcnt(0)\n"
      "v_max3_f32 v112, v112, v172, v173\n"
      "v_max3_f32 v199, v199, v174, v175\n"
      "v_mfma_f32_16x16x32_bf16 v[184:187], a[232:235], v[122:125], v[184:187]\n"
      "v_max3_f32 v199, v199, v112, v160\n"
      "v_mov_b32 v112, v199\n"
      "v_mfma_f32_16x16x32_bf16 v[184:187], a[236:239], v[164:167], v[184:187]\n"
      "s_nop 0\n"
      "v_permlane16_swap_b32 v199, v112\n"
      "v_mfma_f32_16x16x32_bf16 v[152:155], a[252:255], v[96:99], v[152:155]\n"
      "v_max_f32 v199, v199, v112\n"
      "v_mov_b32 v112, v199\n"
      "v_mfma_f32_16x16x32_bf16 v[152:155], v[224:227], v[122:125], v[152:155]\n"
      "s_nop 0\n"
      "v_permlane32_swap_b32 v199, v112\n"
      "v_mfma_f32_16x16x32_bf16 v[152:155], v[228:231], v[164:167], v[152:155]\n"
      "v_max_f32 v199, v199, v112\n"
      "v_sub_f32 v191, v191, v199\n"
      "v_mfma_f32_16x16x32_bf16 v[168:171], v[244:247], v[96:99], v[168:171]\n"
      "ds_read_b128 v[110:113], v2 offset:18688\n"
      "v_sub_f32 v190, v190, v199\n"
      "v_mfma_f32_16x16x32_bf16 v[168:171], v[248:251], v[122:125], v[168:171]\n"
      "v_exp_f32 v190, v190\n"
      "v_mfma_f32_16x16x32_bf16 v[168:171], v[252:255], v[164:167], v[168:171]\n"
      "v_exp_f32_e64 v191, v191\n"
      "v_mfma_f32_16x16x32_bf16 v[220:223], v[180:183], v[206:209], v[52:55]\n"
      "ds_read_b128 v[120:123], v2 offset:24832\n"
      "v_sub_f32 v160, v160, v199\n"
      "s_waitcnt vmcnt(1)\n"
      "v_or3_b32 v148, v148, v149, v150\n"
      "v_exp_f32_e64 v160, v160\n"
      "v_pk_mul_f32 v[140:141], v[140:141], v[116:117] op_sel_hi:[1,0]\n"
      "v_pk_add_f32 v[192:193], v[192:193], v[198:199] op_sel:[0,1] neg_lo:[0,1] neg_hi:[0,1]\n"
      "v_pk_add_f32 v[138:139], v[138:139], v[198:199] op_sel:[0,1] neg_lo:[0,1] neg_hi:[0,1]\n"
      "v_pk_mul_f32 v[142:143], v[142:143], v[116:117] op_sel_hi:[1,0]\n"
      "v_pk_add_f32 v[172:173], v[172:173], v[198:199] op_sel:[0,1] neg_lo:[0,1] neg_hi:[0,1]\n"
      "v_pk_add_f32 v[136:137], v[136:137], v[198:199] op_sel:[0,1] neg_lo:[0,1] neg_hi:[0,1]\n"
      "v_pk_add_f32 v[174:175], v[174:175], v[198:199] op_sel:[0,1] neg_lo:[0,1] neg_hi:[0,1]\n"
      "v_mfma_f32_16x16x32_bf16 v[198:201], v[156:159], v[206:209], v[56:59]\n"
      "ds_read_b128 v[124:127], v2 offset:21760\n"
      "v_cvt_pk_bf16_f32 v103, v142, v143\n"
      "v_mfma_f32_16x16x32_bf16 v[114:117], v[176:179], v[206:209], v[60:63]\n"
      "v_exp_f32 v174, v174\n"
      "v_mfma_f32_16x16x32_bf16 v[96:99], v[202:205], v[206:209], v[26:29]\n"
      "s_waitcnt lgkmcnt(2)\n"
      "v_exp_f32_e64 v172, v172\n"
      "v_mfma_f32_16x16x16_bf16 v[220:223], v[210:211], v[144:145], v[220:223]\n"
      "v_cvt_pk_bf16_f32 v90, v160, 0\n"
      "v_exp_f32 v139, v139\n"
      "v_mfma_f32_16x16x16_bf16 v[198:201], v[118:119], v[144:145], v[198:201]\n"
      "v_exp_f32 v192, v192\n"
      "v_cvt_pk_bf16_f32 v107, v108, v109\n"
      "v_mfma_f32_16x16x16_bf16 v[114:117], v[218:219], v[144:145], v[114:117]\n"
      "v_exp_f32_e64 v137, v137\n"
      "v_cvt_pk_bf16_f32 v190, v190, v191\n"
      "v_mfma_f32_16x16x16_bf16 v[96:99], v[188:189], v[144:145], v[96:99]\n"
      "v_cvt_pk_bf16_f32 v102, v140, v141\n"
      "v_exp_f32 v193, v193\n"
      "v_mfma_f32_16x16x32_bf16 v[160:163], a[216:219], v[110:113], 0\n"
      "s_waitcnt lgkmcnt(0)\n"
      "v_cvt_pk_bf16_f32 v191, v192, v193\n"
      "v_max3_f32 v141, v220, v221, v222\n"
      "v_mfma_f32_16x16x32_bf16 v[160:163], a[220:223], v[124:127], v[160:163]\n"
      "v_exp_f32 v136, v136\n"
      "v_mfma_f32_16x16x32_bf16 v[160:163], a[224:227], v[120:123], v[160:163]\n"
      "v_exp_f32 v138, v138\n"
      "v_mfma_f32_16x16x32_bf16 v[164:167], a[240:243], v[110:113], 0\n"
      "v_cvt_pk_bf16_f32 v192, v136, v137\n"
      "v_cvt_pk_bf16_f32 v193, v138, v139\n"
      "v_mfma_f32_16x16x32_bf16 v[164:167], a[244:247], v[124:127], v[164:167]\n"
      "v_exp_f32 v175, v175\n"
      "v_mfma_f32_16x16x32_bf16 v[164:167], a[248:251], v[120:123], v[164:167]\n"
      "v_exp_f32 v173, v173\n"
      "v_mfma_f32_16x16x32_bf16 v[134:137], v[232:235], v[110:113], 0\n"
      "v_max3_f32 v145, v223, v198, v199\n"
      "v_max3_f32 v145, v145, v114, v115\n"
      "v_mfma_f32_16x16x32_bf16 v[134:137], v[236:239], v[124:127], v[134:137]\n"
      "v_cvt_pk_bf16_f32 v88, v172, v173\n"
      "v_max3_f32 v141, v141, v200, v201\n"
      "v_mfma_f32_16x16x32_bf16 v[134:137], v[240:243], v[120:123], v[134:137]\n"
      "v_max3_f32 v141, v141, v116, v117\n"
      "v_cvt_pk_bf16_f32 v89, v174, v175\n"
      "v_mfma_f32_16x16x32_bf16 v[108:111], v[32:35], v[190:193], 0\n"
      "ds_read_u16 v86, v11 offset:64\n"
      "v_fma_f32 v128, v184, v184, s52\n"
      "v_mfma_f32_16x16x32_bf16 v[108:111], v[32:35], v[88:91], v[108:111]\n"
      "v_max3_f32 v141, v141, v145, v96\n"
      "v_mov_b32 v139, v141\n"
      "v_mfma_f32_16x16x32_bf16 v[172:175], v[214:217], v[190:193], 0\n"
      "s_nop 0\n"
      "v_permlane16_swap_b32 v141, v139\n"
      "v_mfma_f32_16x16x32_bf16 v[172:175], v[76:79], v[88:91], v[172:175]\n"
      "ds_read_b128 v[142:145], v2 offset:27904\n"
      "s_waitcnt vmcnt(0)\n"
      "v_or3_b32 v148, v148, v151, v93\n"
      "v_mfma_f32_16x16x32_bf16 v[206:209], v[130:133], v[190:193], 0\n"
      "ds_write_b32 v31, v148 offset:4608\n"
      "v_mfma_f32_16x16x32_bf16 v[206:209], v[80:83], v[88:91], v[206:209]\n"
      "v_max_f32 v141, v141, v139\n"
      "v_mov_b32 v139, v141\n"
      "v_mfma_f32_16x16x32_bf16 v[146:149], v[194:197], v[190:193], 0\n"
      "s_waitcnt lgkmcnt(2)\n"
      "v_rcp_f32 v108, v108\n"
      "v_mfma_f32_16x16x32_bf16 v[146:149], v[84:87], v[88:91], v[146:149]\n"
      "ds_read_b128 v[120:123], v2 offset:30976\n"
      "v_permlane32_swap_b32 v141, v139\n"
      "v_max_f32 v141, v141, v139\n"
      "v_mul_f32_e64 v129, v185, v185\n"
      "v_pk_add_f32 v[220:221], v[220:221], v[140:141] op_sel:[0,1] neg_lo:[0,1] neg_hi:[0,1]\n"
      "v_pk_mul_f32 v[206:207], v[206:207], v[108:109] op_sel_hi:[1,0]\n"
      "v_pk_mul_f32 v[174:175], v[174:175], v[108:109] op_sel_hi:[1,0]\n"
      "v_pk_mul_f32 v[172:173], v[172:173], v[108:109] op_sel_hi:[1,0]\n"
      "v_sub_f32_e64 v96, v96, v141\n"
      "v_pk_mul_f32 v[208:209], v[208:209], v[108:109] op_sel_hi:[1,0]\n"
      "v_cvt_pk_bf16_f32 v172, v172, v173\n"
      "v_pk_mul_f32 v[146:147], v[146:147], v[108:109] op_sel_hi:[1,0]\n"
      "v_pk_fma_f32 v[128:129], v[186:187], v[186:187], v[128:129]\n"
      "v_cvt_pk_bf16_f32 v173, v174, v175\n"
      "v_pk_add_f32 v[198:199], v[198:199], v[140:141] op_sel:[0,1] neg_lo:[0,1] neg_hi:[0,1]\n"
      "v_pk_add_f32 v[222:223], v[222:223], v[140:141] op_sel:[0,1] neg_lo:[0,1] neg_hi:[0,1]\n"
      "v_pk_mul_f32 v[148:149], v[148:149], v[108:109] op_sel_hi:[1,0]\n"
      "v_pk_add_f32 v[114:115], v[114:115], v[140:141] op_sel:[0,1] neg_lo:[0,1] neg_hi:[0,1]\n"
      "v_pk_add_f32 v[200:201], v[200:201], v[140:141] op_sel:[0,1] neg_lo:[0,1] neg_hi:[0,1]\n"
      "v_cvt_pk_bf16_f32 v175, v208, v209\n"
      "v_pk_add_f32 v[116:117], v[116:117], v[140:141] op_sel:[0,1] neg_lo:[0,1] neg_hi:[0,1]\n"
      "v_pk_fma_f32 v[128:129], v[152:153], v[152:153], v[128:129]\n"
      "v_mfma_f32_16x16x32_bf16 v[138:141], v[180:183], v[100:103], v[64:67]\n"
      "v_cvt_pk_bf16_f32 v146, v146, v147\n"
      "v_cvt_pk_bf16_f32 v174, v206, v207\n"
      "v_cvt_pk_bf16_f32 v147, v148, v149\n"
      "v_mfma_f32_16x16x32_bf16 v[206:209], v[156:159], v[100:103], v[68:71]\n"
      "v_exp_f32_e64 v222, v222\n"
      "v_mfma_f32_16x16x32_bf16 v[148:151], v[176:179], v[100:103], v[72:75]\n"
      "s_waitcnt lgkmcnt(0)\n"
      "v_exp_f32 v117, v117\n"
      "v_mfma_f32_16x16x32_bf16 v[190:193], v[202:205], v[100:103], v[28:31]\n"
      "ds_read_b128 v[108:111], v2 offset:34048\n"
      "v_exp_f32 v116, v116\n"
      "v_mfma_f32_16x16x16_bf16 v[138:141], v[210:211], v[106:107], v[138:141]\n"
      "v_exp_f32 v115, v115\n"
      "v_exp_f32 v220, v220\n"
      "v_mfma_f32_16x16x16_bf16 v[206:209], v[118:119], v[106:107], v[206:209]\n"
      "v_exp_f32 v200, v200\n"
      "v_cvt_pk_bf16_f32 v93, v116, v117\n"
      "v_mfma_f32_16x16x16_bf16 v[148:151], v[218:219], v[106:107], v[148:151]\n"
      "v_exp_f32 v221, v221\n"
      "v_exp_f32 v114, v114\n"
      "v_mfma_f32_16x16x16_bf16 v[190:193], v[188:189], v[106:107], v[190:193]\n"
      "v_cvt_pk_bf16_f32 v220, v220, v221\n"
      "v_cvt_pk_bf16_f32 v92, v114, v115\n"
      "v_max3_f32 v213, v138, v139, v140\n"
      "v_mfma_f32_16x16x32_bf16 v[160:163], a[228:231], v[142:145], v[160:163]\n"
      "v_exp_f32 v96, v96\n"
      "v_mfma_f32_16x16x32_bf16 v[160:163], a[232:235], v[120:123], v[160:163]\n"
      "s_waitcnt lgkmcnt(0)\n"
      "v_exp_f32 v201, v201\n"
      "v_mfma_f32_16x16x32_bf16 v[160:163], a[236:239], v[108:111], v[160:163]\n"
      "v_exp_f32 v199, v199\n"
      "v_mfma_f32_16x16x32_bf16 v[164:167], a[252:255], v[142:145], v[164:167]\n"
      "v_exp_f32 v198, v198\n"
      "v_mfma_f32_16x16x32_bf16 v[164:167], v[224:227], v[120:123], v[164:167]\n"
      "v_exp_f32 v223, v223\n"
      "v_mfma_f32_16x16x32_bf16 v[164:167], v[228:231], v[108:111], v[164:167]\n"
      "v_cvt_pk_bf16_f32 v221, v222, v223\n"
      "v_cvt_pk_bf16_f32 v94, v96, 0\n"
      "v_mfma_f32_16x16x32_bf16 v[134:137], v[244:247], v[142:145], v[134:137]\n"
      "v_max3_f32 v213, v213, v208, v209\n"
      "v_cvt_pk_bf16_f32 v222, v198, v199\n"
      "v_mfma_f32_16x16x32_bf16 v[134:137], v[248:251], v[120:123], v[134:137]\n"
      "v_cvt_pk_bf16_f32 v223, v200, v201\n"
      "v_max3_f32 v198, v141, v206, v207\n"
      "v_mfma_f32_16x16x32_bf16 v[134:137], v[252:255], v[108:111], v[134:137]\n"
      "ds_read_b128 v[108:111], v10\n"
      "v_max3_f32 v198, v198, v148, v149\n"
      "v_mfma_f32_16x16x32_bf16 v[112:115], v[32:35], v[220:223], 0\n"
      "ds_read_b128 v[120:123], v7 offset:18432\n"
      "v_max3_f32 v213, v213, v150, v151\n"
      "v_mfma_f32_16x16x32_bf16 v[112:115], v[32:35], v[92:95], v[112:115]\n"
      "v_max3_f32 v213, v213, v198, v190\n"
      "v_mov_b32 v96, v213\n"
      "v_mfma_f32_16x16x32_bf16 v[124:127], v[214:217], v[220:223], 0\n"
      "s_nop 0\n"
      "v_permlane16_swap_b32_e64 v213, v96\n"
      "v_mfma_f32_16x16x32_bf16 v[124:127], v[76:79], v[92:95], v[124:127]\n"
      "ds_read_b64 v[198:199], v10 offset:16\n"
      "v_max_f32_e64 v213, v213, v96\n"
      "v_mfma_f32_16x16x32_bf16 v[104:107], v[130:133], v[220:223], 0\n"
      "ds_write_b128 v3, v[172:175]\n"
      "v_mfma_f32_16x16x32_bf16 v[104:107], v[80:83], v[92:95], v[104:107]\n"
      "ds_read_b128 v[100:103], v2 offset:22016\n"
      "v_mov_b32 v96, v213\n"
      "v_mfma_f32_16x16x32_bf16 v[142:145], v[194:197], v[220:223], 0\n"
      "s_nop 0\n"
      "v_permlane32_swap_b32 v213, v96\n"
      "v_mfma_f32_16x16x32_bf16 v[142:145], v[84:87], v[92:95], v[142:145]\n"
      "s_waitcnt lgkmcnt(3)\n"
      "v_rcp_f32_e64 v112, v112\n"
      "v_mfma_f32_16x16x32_bf16 v[172:175], v[108:111], v[180:183], v[30:33]\n"
      "ds_read_b128 v[180:183], v2 offset:18944\n"
      "v_max_f32_e64 v213, v213, v96\n"
      "v_mfma_f32_16x16x32_bf16 v[96:99], v[108:111], v[156:159], v[30:33]\n"
      "buffer_store_dwordx4 v[120:123], v20, s[24:27], s46 offen sc1\n"
      "v_pk_add_f32 v[148:149], v[148:149], v[212:213] op_sel:[0,1] neg_lo:[0,1] neg_hi:[0,1]\n"
      "v_pk_add_f32 v[140:141], v[140:141], v[212:213] op_sel:[0,1] neg_lo:[0,1] neg_hi:[0,1]\n"
      "v_pk_add_f32 v[206:207], v[206:207], v[212:213] op_sel:[0,1] neg_lo:[0,1] neg_hi:[0,1]\n"
      "v_pk_mul_f32 v[126:127], v[126:127], v[112:113] op_sel_hi:[1,0]\n"
      "v_pk_add_f32 v[138:139], v[138:139], v[212:213] op_sel:[0,1] neg_lo:[0,1] neg_hi:[0,1]\n"
      "v_pk_mul_f32 v[124:125], v[124:125], v[112:113] op_sel_hi:[1,0]\n"
      "v_pk_add_f32 v[150:151], v[150:151], v[212:213] op_sel:[0,1] neg_lo:[0,1] neg_hi:[0,1]\n"
      "v_pk_mul_f32 v[144:145], v[144:145], v[112:113] op_sel_hi:[1,0]\n"
      "v_pk_mul_f32 v[106:107], v[106:107], v[112:113] op_sel_hi:[1,0]\n"
      "v_pk_fma_f32 v[128:129], v[154:155], v[154:155], v[128:129]\n"
      "v_pk_add_f32 v[208:209], v[208:209], v[212:213] op_sel:[0,1] neg_lo:[0,1] neg_hi:[0,1]\n"
      "v_pk_mul_f32 v[104:105], v[104:105], v[112:113] op_sel_hi:[1,0]\n"
      "v_mfma_f32_16x16x32_bf16 v[120:123], v[108:111], v[176:179], v[30:33]\n"
      "s_waitcnt lgkmcnt(0)\n"
      "v_exp_f32 v148, v148\n"
      "v_mfma_f32_16x16x32_bf16 v[114:117], v[108:111], v[202:205], v[30:33]\n"
      "ds_read_b128 v[176:179], v7 offset:22528\n"
      "v_sub_f32_e64 v190, v190, v213\n"
      "v_mfma_f32_16x16x16_bf16 v[172:175], v[198:199], v[210:211], v[172:175]\n"
      "ds_read_b128 v[210:213], v2 offset:25088\n"
      "v_fma_f32 v222, v160, v160, s52\n"
      "v_mfma_f32_16x16x16_bf16 v[96:99], v[198:199], v[118:119], v[96:99]\n"
      "v_exp_f32 v138, v138\n"
      "v_cvt_pk_bf16_f32 v124, v124, v125\n"
      "v_mfma_f32_16x16x16_bf16 v[120:123], v[198:199], v[218:219], v[120:123]\n"
      "v_exp_f32 v149, v149\n"
      "v_exp_f32 v190, v190\n"
      "v_exp_f32 v139, v139\n"
      "v_mfma_f32_16x16x16_bf16 v[114:117], v[198:199], v[188:189], v[114:117]\n"
      "v_mul_f32_e64 v223, v161, v161\n"
      "v_cvt_pk_bf16_f32 v138, v138, v139\n"
      "v_exp_f32_e64 v207, v207\n"
      "v_mfma_f32_16x16x32_bf16 v[108:111], a[216:219], v[180:183], 0\n"
      "ds_read_b128 v[202:205], v2 offset:28160\n"
      "v_cndmask_b32 v172, v172, v96, s[58:59]\n"
      "v_exp_f32 v141, v141\n"
      "v_mfma_f32_16x16x32_bf16 v[108:111], a[220:223], v[100:103], v[108:111]\n"
      "s_waitcnt lgkmcnt(1)\n"
      "v_cndmask_b32 v172, v172, v120, s[60:61]\n"
      "v_cndmask_b32 v172, v172, v114, s[62:63]\n"
      "v_mfma_f32_16x16x32_bf16 v[108:111], a[224:227], v[210:213], v[108:111]\n"
      "buffer_store_dwordx4 v[176:179], v21, s[24:27], s46 offen sc1\n"
      "v_mfma_f32_16x16x32_bf16 v[116:119], a[240:243], v[180:183], 0\n"
      "ds_read_b128 v[120:123], v7 offset:26624\n"
      "v_exp_f32 v208, v208\n"
      "v_mfma_f32_16x16x32_bf16 v[116:119], a[244:247], v[100:103], v[116:119]\n"
      "ds_read_b128 v[174:177], v2 offset:31232\n"
      "v_exp_f32 v150, v150\n"
      "v_max_f32_dpp v220, v172, v172 row_ror:8 row_mask:0xf bank_mask:0xf\n"
      "v_cvt_pk_bf16_f32 v125, v126, v127\n"
      "v_pk_fma_f32 v[128:129], v[168:169], v[168:169], v[128:129]\n"
      "v_pk_fma_f32 v[222:223], v[162:163], v[162:163], v[222:223]\n"
      "v_max_f32_dpp v220, v220, v220 row_ror:4 row_mask:0xf bank_mask:0xf\n"
      "v_cvt_pk_bf16_f32 v127, v106, v107\n"
      "v_pk_fma_f32 v[222:223], v[164:165], v[164:165], v[222:223]\n"
      "v_max_f32_dpp v220, v220, v220 row_ror:2 row_mask:0xf bank_mask:0xf\n"
      "v_pk_fma_f32 v[222:223], v[166:167], v[166:167], v[222:223]\n"
      "v_cvt_pk_bf16_f32 v126, v104, v105\n"
      "v_pk_mul_f32 v[142:143], v[142:143], v[112:113] op_sel_hi:[1,0]\n"
      "v_mfma_f32_16x16x32_bf16 v[116:119], a[248:251], v[210:213], v[116:119]\n"
      "ds_read_b128 v[198:201], v2 offset:34304\n"
      "v_exp_f32 v151, v151\n"
      "v_mfma_f32_16x16x32_bf16 v[96:99], v[232:235], v[180:183], 0\n"
      "v_exp_f32_e64 v140, v140\n"
      "v_mfma_f32_16x16x32_bf16 v[96:99], v[236:239], v[100:103], v[96:99]\n"
      "s_waitcnt lgkmcnt(1)\n"
      "v_max_f32_dpp v220, v220, v220 row_ror:1 row_mask:0xf bank_mask:0xf\n"
      "v_exp_f32_e64 v206, v206\n"
      "v_mfma_f32_16x16x32_bf16 v[96:99], v[240:243], v[210:213], v[96:99]\n"
      "buffer_store_dwordx4 v[120:123], v22, s[24:27], s46 offen sc1\n"
      "v_mfma_f32_16x16x32_bf16 v[108:111], a[228:231], v[202:205], v[108:111]\n"
      "v_max_f32_dpp v220, v220, v220 row_bcast:15 row_mask:0xa bank_mask:0xf\n"
      "v_cvt_pk_bf16_f32 v139, v140, v141\n"
      "v_mfma_f32_16x16x32_bf16 v[108:111], a[232:235], v[174:177], v[108:111]\n"
      "s_waitcnt lgkmcnt(0)\n"
      "v_max_f32_dpp v220, v220, v220 row_bcast:31 row_mask:0xc bank_mask:0xf\n"
      "v_exp_f32 v209, v209\n"
      "v_mfma_f32_16x16x32_bf16 v[108:111], a[236:239], v[198:201], v[108:111]\n"
      "v_readlane_b32 s72, v220, 63\n"
      "v_cvt_pk_bf16_f32 v141, v208, v209\n"
      "v_mfma_f32_16x16x32_bf16 v[116:119], a[252:255], v[202:205], v[116:119]\n"
      "ds_read_b128 v[210:213], v7 offset:30720\n"
      "v_cvt_pk_bf16_f32 v140, v206, v207\n"
      "v_subrev_f32 v172, s72, v172\n"
      "v_mfma_f32_16x16x32_bf16 v[116:119], v[224:227], v[174:177], v[116:119]\n"
      "v_cvt_pk_bf16_f32 v90, v190, 0\n"
      "v_fma_f32 v220, v108, v108, s52\n"
      "v_mfma_f32_16x16x32_bf16 v[116:119], v[228:231], v[198:201], v[116:119]\n"
      "ds_write_b128 v3, v[124:127] offset:256\n"
      "v_pk_fma_f32 v[222:223], v[134:135], v[134:135], v[222:223]\n"
      "v_pk_fma_f32 v[128:129], v[170:171], v[170:171], v[128:129]\n"
      "v_mul_f32 v221, v109, v109\n"
      "v_mfma_f32_16x16x32_bf16 v[96:99], v[244:247], v[202:205], v[96:99]\n"
      "v_exp_f32 v172, v172\n"
      "v_mfma_f32_16x16x32_bf16 v[96:99], v[248:251], v[174:177], v[96:99]\n"
      "v_cvt_pk_bf16_f32 v88, v148, v149\n"
      "v_cvt_pk_bf16_f32 v89, v150, v151\n"
      "v_mfma_f32_16x16x32_bf16 v[96:99], v[252:255], v[198:201], v[96:99]\n"
      "s_waitcnt lgkmcnt(1)\n"
      "v_cvt_pk_bf16_f32 v142, v142, v143\n"
      "v_cvt_pk_bf16_f32 v172, v172, v172\n"
      "v_mfma_f32_16x16x32_bf16 v[188:191], v[32:35], v[138:141], 0\n"
      "ds_write_b16 v13, v172\n"
      "v_mfma_f32_16x16x32_bf16 v[188:191], v[32:35], v[88:91], v[188:191]\n"
      "buffer_store_dwordx4 v[210:213], v23, s[24:27], s46 offen sc1\n"
      "v_pk_fma_f32 v[220:221], v[110:111], v[110:111], v[220:221]\n"
      "v_pk_fma_f32 v[220:221], v[116:117], v[116:117], v[220:221]\n"
      "v_pk_fma_f32 v[222:223], v[136:137], v[136:137], v[222:223]\n"
      "v_mfma_f32_16x16x32_bf16 v[120:123], v[214:217], v[138:141], 0\n"
      "ds_write_b64 v4, v[146:147]\n"
      "v_mfma_f32_16x16x32_bf16 v[120:123], v[76:79], v[88:91], v[120:123]\n"
      "s_nop 0\n"
      "v_rcp_f32 v188, v188\n"
      "v_mfma_f32_16x16x32_bf16 v[178:181], v[130:133], v[138:141], 0\n"
      "ds_read_b128 v[148:151], v7 offset:34816\n"
      "v_add_f32 v128, v128, v129\n"
      "v_mfma_f32_16x16x32_bf16 v[178:181], v[80:83], v[88:91], v[178:181]\n"
      "ds_read_b128 v[210:213], v14\n"
      "v_cvt_pk_bf16_f32 v143, v144, v145\n"
      "v_mfma_f32_16x16x32_bf16 v[206:209], v[194:197], v[138:141], 0\n"
      "ds_read_b128 v[172:175], v14 offset:64\n"
      "v_add_f32 v222, v222, v223\n"
      "v_mfma_f32_16x16x32_bf16 v[206:209], v[84:87], v[88:91], v[206:209]\n"
      "ds_write_b64 v4, v[142:143] offset:256\n"
      "v_permlane16_swap_b32_e64 v128, v222\n"
      "v_pk_mul_f32 v[120:121], v[120:121], v[188:189] op_sel_hi:[1,0]\n"
      "v_pk_fma_f32 v[220:221], v[118:119], v[118:119], v[220:221]\n"
      "v_pk_mul_f32 v[122:123], v[122:123], v[188:189] op_sel_hi:[1,0]\n"
      "v_pk_mul_f32 v[178:179], v[178:179], v[188:189] op_sel_hi:[1,0]\n"
      "v_cvt_pk_bf16_f32 v120, v120, v121\n"
      "v_add_f32_e64 v128, v128, v222\n"
      "v_pk_fma_f32 v[220:221], v[96:97], v[96:97], v[220:221]\n"
      "v_cvt_pk_bf16_f32 v121, v122, v123\n"
      "v_pk_mul_f32 v[180:181], v[180:181], v[188:189] op_sel_hi:[1,0]\n"
      "v_pk_fma_f32 v[220:221], v[98:99], v[98:99], v[220:221]\n"
      "v_cvt_pk_bf16_f32 v122, v178, v179\n"
      "v_add_f32 v220, v220, v221\n"
      "v_cvt_pk_bf16_f32 v123, v180, v181\n"
      "v_mov_b32_e64 v222, v220\n"
      "s_add_u32 s42, s42, 1\n"
      "ds_write_b128 v3, v[120:123] offset:512\n"
      "v_permlane16_swap_b32 v220, v222\n"
      "v_add_f32 v220, v220, v222\n"
      "v_pk_mul_f32 v[206:207], v[206:207], v[188:189] op_sel_hi:[1,0]\n"
      "v_pk_mul_f32 v[208:209], v[208:209], v[188:189] op_sel_hi:[1,0]\n"
      "v_permlane32_swap_b32 v128, v220\n"
      "v_add_f32 v128, v128, v220\n"
      "s_and_b32 s86, s86, -4\n"
      "ds_write_b32 v31, v128\n"
      "v_cvt_pk_bf16_f32 v206, v206, v207\n"
      "v_cvt_pk_bf16_f32 v207, v208, v209\n"
      "s_waitcnt lgkmcnt(2)\n"
      "v_mfma_f32_16x16x32_bf16 v[188:191], v[210:213], v[32:35], 0\n"
      "ds_write_b64 v4, v[206:207] offset:512\n"
      "s_cmp_ge_u32 s95, 0x40000000\n"
      "s_cselect_b64 vcc, -1, s[56:57]\n"
      "s_and_b32 s5, s84, s8\n"
      "v_mfma_f32_16x16x32_bf16 v[188:191], v[172:175], v[32:35], v[188:191]\n"
      "buffer_store_dwordx4 v[148:151], v25, s[24:27], s46 offen sc1\n"
      "s_waitcnt lgkmcnt(0)\n"
      "s_barrier\n"
      "s_cbranch_vccz L_max1_%=\n"
      "L_cold_%=:\n"
      "L_disp1_%=:\n"
      "s_cmp_lt_u32 s95, 0x40000000\n"
      "s_cbranch_scc1 L_nofix_11_%=\n"
      "s_cmp_eq_u32 s95, -2\n"
      "s_cbranch_scc1 L_tail1_%=\n"
      "s_nop 2\n"
      "s_cmp_eq_u32 s84, 0x20000000\n"
      "s_cselect_b32 s66, 0, s66\n"
      "s_cselect_b32 s48, 0, s48\n"
      "s_lshr_b32 s92, s84, 30\n"
      "s_or_b32 s92, s92, s66\n"
      "s_cbranch_scc0 L_nf_12_%=\n"
      "s_bitcmp1_b32 s84, 31\n"
      "s_cselect_b32 s75, 100270464, 0\n"
      "s_bitcmp1_b32 s84, 30\n"
      "s_cselect_b32 s76, 196224, 0\n"
      "s_and_b32 s74, s84, 0x3fffffff\n"
      "s_cmp_ge_u32 s84, 0x40000000\n"
      "s_cselect_b32 s74, s74, 0\n"
      "s_cselect_b32 s84, 0, s84\n"
      "s_sub_u32 s74, s74, s66\n"
      "s_add_u32 s66, s66, s74\n"
      "s_sub_u32 s75, s75, s67\n"
      "s_add_u32 s67, s67, s75\n"
      "s_sub_u32 s76, s76, s68\n"
      "s_add_u32 s68, s68, s76\n"
      "v_add_u32 v16, s74, v16\n"
      "v_add_u32 v17, s74, v17\n"
      "v_add_u32 v18, s74, v18\n"
      "v_add_u32 v19, s74, v19\n"
      "v_add_u32 v15, s74, v15\n"
      "s_cmp_eq_u32 s75, 0\n"
      "s_cbranch_scc1 L_ax_13_%=\n"
      "v_bfe_i32 v151, v27, 0, 1\n"
      "v_and_b32 v151, s75, v151\n"
      "v_sub_u32 v16, v16, v151\n"
      "v_bfe_i32 v151, v27, 2, 1\n"
      "v_and_b32 v151, s75, v151\n"
      "v_sub_u32 v17, v17, v151\n"
      "v_bfe_i32 v151, v27, 4, 1\n"
      "v_and_b32 v151, s75, v151\n"
      "v_sub_u32 v18, v18, v151\n"
      "v_bfe_i32 v151, v27, 6, 1\n"
      "v_and_b32 v151, s75, v151\n"
      "v_sub_u32 v19, v19, v151\n"
      "v_bfe_i32 v151, v27, 8, 1\n"
      "v_and_b32 v151, s75, v151\n"
      "v_sub_u32 v15, v15, v151\n"
      "L_ax_13_%=:\n"
      "s_cmp_eq_u32 s76, 0\n"
      "s_cbranch_scc1 L_ax_14_%=\n"
      "v_bfe_i32 v151, v27, 1, 1\n"
      "v_and_b32 v151, s76, v151\n"
      "v_sub_u32 v16, v16, v151\n"
      "v_bfe_i32 v151, v27, 3, 1\n"
      "v_and_b32 v151, s76, v151\n"
      "v_sub_u32 v17, v17, v151\n"
      "v_bfe_i32 v151, v27, 5, 1\n"
      "v_and_b32 v151, s76, v151\n"
      "v_sub_u32 v18, v18, v151\n"
      "v_bfe_i32 v151, v27, 7, 1\n"
      "v_and_b32 v151, s76, v151\n"
      "v_sub_u32 v19, v19, v151\n"
      "v_bfe_i32 v151, v27, 9, 1\n"
      "v_and_b32 v151, s76, v151\n"
      "v_sub_u32 v15, v15, v151\n"
      "L_ax_14_%=:\n"
      "s_or_b32 s48, s66, s69\n"
      "s_cmp_lg_u32 s48, 0\n"
      "s_cselect_b32 s48, 0x40000000, 0\n"
      "L_nf_12_%=:\n"
      "s_lshr_b32 s92, s85, 30\n"
      "s_or_b32 s92, s92, s69\n"
      "s_cbranch_scc0 L_nf_15_%=\n"
      "s_bitcmp1_b32 s85, 31\n"
      "s_cselect_b32 s75, 100270464, 0\n"
      "s_bitcmp1_b32 s85, 30\n"
      "s_cselect_b32 s76, 196224, 0\n"
      "s_and_b32 s74, s85, 0x3fffffff\n"
      "s_cmp_ge_u32 s85, 0x40000000\n"
      "s_cselect_b32 s74, s74, 0\n"
      "s_cselect_b32 s85, 0, s85\n"
      "s_sub_u32 s74, s74, s69\n"
      "s_add_u32 s69, s69, s74\n"
      "s_sub_u32 s75, s75, s70\n"
      "s_add_u32 s70, s70, s75\n"
      "s_sub_u32 s76, s76, s71\n"
      "s_add_u32 s71, s71, s76\n"
      "v_add_u32 v20, s74, v20\n"
      "v_add_u32 v21, s74, v21\n"
      "v_add_u32 v22, s74, v22\n"
      "v_add_u32 v23, s74, v23\n"
      "v_add_u32 v25, s74, v25\n"
      "s_cmp_eq_u32 s75, 0\n"
      "s_cbranch_scc1 L_ax_16_%=\n"
      "v_bfe_i32 v151, v27, 10, 1\n"
      "v_and_b32 v151, s75, v151\n"
      "v_sub_u32 v20, v20, v151\n"
      "v_bfe_i32 v151, v27, 12, 1\n"
      "v_and_b32 v151, s75, v151\n"
      "v_sub_u32 v21, v21, v151\n"
      "v_bfe_i32 v151, v27, 14, 1\n"
      "v_and_b32 v151, s75, v151\n"
      "v_sub_u32 v22, v22, v151\n"
      "v_bfe_i32 v151, v27, 16, 1\n"
      "v_and_b32 v151, s75, v151\n"
      "v_sub_u32 v23, v23, v151\n"
      "v_bfe_i32 v151, v27, 18, 1\n"
      "v_and_b32 v151, s75, v151\n"
      "v_sub_u32 v25, v25, v151\n"
      "L_ax_16_%=:\n"
      "s_cmp_eq_u32 s76, 0\n"
      "s_cbranch_scc1 L_ax_17_%=\n"
      "v_bfe_i32 v151, v27, 11, 1\n"
      "v_and_b32 v151, s76, v151\n"
      "v_sub_u32 v20, v20, v151\n"
      "v_bfe_i32 v151, v27, 13, 1\n"
      "v_and_b32 v151, s76, v151\n"
      "v_sub_u32 v21, v21, v151\n"
      "v_bfe_i32 v151, v27, 15, 1\n"
      "v_and_b32 v151, s76, v151\n"
      "v_sub_u32 v22, v22, v151\n"
      "v_bfe_i32 v151, v27, 17, 1\n"
      "v_and_b32 v151, s76, v151\n"
      "v_sub_u32 v23, v23, v151\n"
      "v_bfe_i32 v151, v27, 19, 1\n"
      "v_and_b32 v151, s76, v151\n"
      "v_sub_u32 v25, v25, v151\n"
      "L_ax_17_%=:\n"
      "s_or_b32 s48, s66, s69\n"
      "s_cmp_lg_u32 s48, 0\n"
      "s_cselect_b32 s48, 0x40000000, 0\n"
      "L_nf_15_%=:\n"
      "s_and_b32 s5, s84, s8\n"
      "s_sub_u32 s92, s42, 1\n"
      "s_cmp_gt_u32 s92, s44\n"
      "s_cbranch_scc1 L_tail1_%=\n"
      "L_nofix_11_%=:\n"
      "s_cmp_lg_u64 s[56:57], 0\n"
      "s_cbranch_scc1 L_slow1_%=\n"
      "L_sel1_%=:\n"
      "s_cmp_lg_u64 s[54:55], 0\n"
      "s_cbranch_scc1 L_max1_%=\n"
      "s_branch L_loop1_%=\n"
      "L_slow1_%=:\n"
      "s_nop 4\n"
      "v_mfma_f32_16x16x32_bf16 v[120:123], v[210:213], v[214:217], 0\n"
      "ds_read_b128 v[142:145], v0 offset:18432\n"
      "s_add_u32 s72, s42, 1\n"
      "v_mfma_f32_16x16x32_bf16 v[120:123], v[172:175], v[76:79], v[120:123]\n"
      "ds_read_b128 v[138:141], v9\n"
      "v_readlane_b32 s77, v38, s72\n"
      "v_mfma_f32_16x16x32_bf16 v[148:151], v[210:213], v[130:133], 0\n"
      "ds_read_b128 v[220:223], v0 offset:24576\n"
      "s_mov_b32 m0, 110592\n"
      "v_mfma_f32_16x16x32_bf16 v[148:151], v[172:175], v[80:83], v[148:151]\n"
      "ds_read_b128 v[206:209], v1 offset:30720\n"
      "s_and_b32 s73, s77, -16\n"
      "v_mfma_f32_16x16x32_bf16 v[178:181], v[210:213], v[194:197], 0\n"
      "ds_read_b128 v[210:213], v1 offset:18432\n"
      "v_subrev_u32 v215, 115456, v9\n"
      "v_mfma_f32_16x16x32_bf16 v[178:181], v[172:175], v[84:87], v[178:181]\n"
      "buffer_load_dwordx4 v215, s[28:31], s73 offen  lds\n"
      "s_waitcnt lgkmcnt(4)\n"
      "v_mfma_f32_16x16x32_bf16 v[172:175], a[0:3], v[142:145], 0\n"
      "ds_read_b128 v[198:201], v1 offset:24576\n"
      "v_readlane_b32 s45, v37, s43\n"
      "v_mfma_f32_16x16x32_bf16 v[104:107], a[24:27], v[142:145], 0\n"
      "s_waitcnt lgkmcnt(4)\n"
      "v_add_f32 v138, v138, v139\n"
      "v_add_f32_e64 v140, v140, v141\n"
      "v_mfma_f32_16x16x32_bf16 v[112:115], a[48:51], v[142:145], 0\n"
      "s_add_u32 s73, s73, 1024\n"
      "v_add_f32_e64 v138, v138, v140\n"
      "v_mov_b32_dpp v188, v188 quad_perm:[0,0,0,0] row_mask:0xf bank_mask:0xf\n"
      "v_mfma_f32_16x16x32_bf16 v[126:129], a[72:75], v[142:145], 0\n"
      "s_mov_b32 m0, 111616\n"
      "v_rsq_f32 v138, v138\n"
      "v_mfma_f32_16x16x32_bf16 v[190:193], a[96:99], v[142:145], 0\n"
      "s_waitcnt lgkmcnt(1)\n"
      "v_mul_f32 v138, 0x415db3d7, v138\n"
      "v_mov_b32_e64 v140, v138\n"
      "v_mfma_f32_16x16x32_bf16 v[122:125], a[120:123], v[142:145], 0\n"
      "ds_read_b128 v[100:103], v0 offset:30720\n"
      "v_subrev_u32 v189, 115456, v9\n"
      "v_mfma_f32_16x16x32_bf16 v[172:175], a[4:7], v[210:213], v[172:175]\n"
      "buffer_load_dwordx4 v215, s[28:31], s73 offen  lds\n"
      "v_mfma_f32_16x16x32_bf16 v[172:175], a[8:11], v[220:223], v[172:175]\n"
      "s_mov_b32 m0, 112640\n"
      "v_permlane32_swap_b32 v138, v140\n"
      "v_mfma_f32_16x16x32_bf16 v[104:107], a[28:31], v[210:213], v[104:107]\n"
      "v_mul_f32 v109, v140, v109\n"
      "v_bfe_u32 v85, v189, 4, 4\n"
      "v_mfma_f32_16x16x32_bf16 v[104:107], a[32:35], v[220:223], v[104:107]\n"
      "s_add_u32 s73, s73, 1024\n"
      "v_mul_f32 v116, v140, v116\n"
      "v_mov_b32 v139, v138\n"
      "v_mfma_f32_16x16x32_bf16 v[112:115], a[52:55], v[210:213], v[112:115]\n"
      "s_nop 0\n"
      "v_permlane16_swap_b32 v138, v139\n"
      "v_mfma_f32_16x16x32_bf16 v[112:115], a[56:59], v[220:223], v[112:115]\n"
      "v_subrev_u32 v39, 115456, v9\n"
      "v_mul_f32_e64 v187, v138, v187\n"
      "v_mfma_f32_16x16x32_bf16 v[126:129], a[76:79], v[210:213], v[126:129]\n"
      "ds_read_b128 v[202:205], v9 offset:4608\n"
      "v_mul_f32_e64 v153, v138, v153\n"
      "v_mfma_f32_16x16x32_bf16 v[126:129], a[80:83], v[220:223], v[126:129]\n"
      "buffer_load_dwordx4 v215, s[28:31], s73 offen  lds\n"
      "v_mfma_f32_16x16x32_bf16 v[190:193], a[100:103], v[210:213], v[190:193]\n"
      "s_waitcnt lgkmcnt(1)\n"
      "v_mul_f32 v184, v138, v184\n"
      "v_mul_f32_e64 v186, v138, v186\n"
      "v_mfma_f32_16x16x32_bf16 v[190:193], a[104:107], v[220:223], v[190:193]\n"
      "s_add_u32 s73, s73, 1024\n"
      "v_mul_f32 v185, v138, v185\n"
      "v_mul_f32 v134, v139, v134\n"
      "v_mfma_f32_16x16x32_bf16 v[122:125], a[124:127], v[210:213], v[122:125]\n"
      "s_mov_b32 m0, 113664\n"
      "v_mul_f32 v155, v138, v155\n"
      "v_mul_f32 v163, v139, v163\n"
      "v_mfma_f32_16x16x32_bf16 v[122:125], a[128:131], v[220:223], v[122:125]\n"
      "ds_read_b128 v[156:159], v0 offset:32768\n"
      "v_mul_f32_e64 v154, v138, v154\n"
      "v_mfma_f32_16x16x32_bf16 v[172:175], a[12:15], v[198:201], v[172:175]\n"
      "buffer_load_dwordx4 v215, s[28:31], s73 offen  lds\n"
      "v_mfma_f32_16x16x32_bf16 v[172:175], a[16:19], v[100:103], v[172:175]\n"
      "s_add_u32 s73, s73, 1024\n"
      "v_cvt_pk_bf16_f32 v184, v184, v185\n"
      "v_cvt_pk_bf16_f32 v185, v186, v187\n"
      "v_mfma_f32_16x16x32_bf16 v[172:175], a[20:23], v[206:209], v[172:175]\n"
      "s_mov_b32 m0, 114688\n"
      "v_mul_f32 v152, v138, v152\n"
      "v_mul_f32 v167, v139, v167\n"
      "v_mfma_f32_16x16x32_bf16 v[104:107], a[36:39], v[198:201], v[104:107]\n"
      "s_mov_b32 exec_hi, 0xffff\n"
      "buffer_load_dwordx4 v215, s[28:31], s73 offen  lds\n"
      "s_mov_b32 exec_hi, -1\n"
      "v_mfma_f32_16x16x32_bf16 v[104:107], a[40:43], v[100:103], v[104:107]\n"
      "v_cvt_pk_bf16_f32 v186, v152, v153\n"
      "v_cvt_pk_bf16_f32 v187, v154, v155\n"
      "v_mfma_f32_16x16x32_bf16 v[104:107], a[44:47], v[206:209], v[104:107]\n"
      "v_mul_f32 v166, v139, v166\n"
      "v_fma_f32 v77, v172, v172, s51\n"
      "v_mfma_f32_16x16x32_bf16 v[112:115], a[60:63], v[198:201], v[112:115]\n"
      "ds_write_b128 v5, v[184:187] offset:18432\n"
      "v_pk_mul_f32 v[168:169], v[168:169], v[138:139] op_sel_hi:[1,0]\n"
      "v_pk_mul_f32 v[170:171], v[170:171], v[138:139] op_sel_hi:[1,0]\n"
      "v_pk_mul_f32 v[160:161], v[160:161], v[138:139] op_sel:[0,1]\n"
      "v_mfma_f32_16x16x32_bf16 v[112:115], a[64:67], v[100:103], v[112:115]\n"
      "s_add_u32 m0, s64, 0\n"
      "v_cvt_pk_bf16_f32 v168, v168, v169\n"
      "v_mul_f32 v137, v139, v137\n"
      "v_mfma_f32_16x16x32_bf16 v[112:115], a[68:71], v[206:209], v[112:115]\n"
      "buffer_load_dwordx4 v16, s[16:19], s84 offen  lds\n"
      "v_mfma_f32_16x16x32_bf16 v[126:129], a[84:87], v[198:201], v[126:129]\n"
      "ds_read_b128 v[180:183], v1 offset:26624\n"
      "v_mul_f32_e64 v162, v139, v162\n"
      "v_mfma_f32_16x16x32_bf16 v[126:129], a[88:91], v[100:103], v[126:129]\n"
      "ds_read_b128 v[152:155], v1 offset:20480\n"
      "v_cvt_pk_bf16_f32 v169, v170, v171\n"
      "v_mfma_f32_16x16x32_bf16 v[126:129], a[92:95], v[206:209], v[126:129]\n"
      "ds_write_b64 v6, v[168:169] offset:18432\n"
      "v_mfma_f32_16x16x32_bf16 v[190:193], a[108:111], v[198:201], v[190:193]\n"
      "v_fmac_f32 v77, v173, v173\n"
      "v_mul_f32 v165, v139, v165\n"
      "v_mfma_f32_16x16x32_bf16 v[190:193], a[112:115], v[100:103], v[190:193]\n"
      "v_cvt_pk_bf16_f32 v160, v160, v161\n"
      "v_mul_f32 v136, v139, v136\n"
      "v_mfma_f32_16x16x32_bf16 v[190:193], a[116:119], v[206:209], v[190:193]\n"
      "s_add_u32 m0, s64, 4096\n"
      "v_rcp_f32 v188, v188\n"
      "v_mfma_f32_16x16x32_bf16 v[122:125], a[132:135], v[198:201], v[122:125]\n"
      "buffer_load_dwordx4 v17, s[16:19], s84 offen  lds\n"
      "v_mfma_f32_16x16x32_bf16 v[122:125], a[136:139], v[100:103], v[122:125]\n"
      "ds_read_b128 v[168:171], v0 offset:20480\n"
      "v_mul_f32_e64 v135, v139, v135\n"
      "v_mfma_f32_16x16x32_bf16 v[122:125], a[140:143], v[206:209], v[122:125]\n"
      "ds_read_b128 v[184:187], v0 offset:26624\n"
      "v_readlane_b32 s46, v37, s42\n"
      "v_mfma_f32_16x16x32_bf16 v[214:217], v[142:145], a[144:147], 0\n"
      "v_cndmask_b32 v120, v120, v148, s[58:59]\n"
      "v_cvt_pk_bf16_f32 v161, v162, v163\n"
      "v_mfma_f32_16x16x32_bf16 v[214:217], v[210:213], a[148:151], v[214:217]\n"
      "s_add_u32 m0, s64, 8192\n"
      "v_mul_f32_e64 v164, v139, v164\n"
      "v_cvt_pk_bf16_f32 v163, v166, v167\n"
      "v_mfma_f32_16x16x32_bf16 v[214:217], v[220:223], a[152:155], v[214:217]\n"
      "buffer_load_dwordx4 v18, s[16:19], s84 offen  lds\n"
      "v_mfma_f32_16x16x32_bf16 v[214:217], v[198:201], a[156:159], v[214:217]\n"
      "s_add_u32 m0, s64, 12288\n"
      "v_fma_f32 v76, v126, v126, s51\n"
      "v_fmac_f32 v76, v127, v127\n"
      "v_mfma_f32_16x16x32_bf16 v[214:217], v[100:103], a[160:163], v[214:217]\n"
      "v_cvt_pk_bf16_f32 v134, v134, v135\n"
      "v_fmac_f32 v76, v128, v128\n"
      "v_mfma_f32_16x16x32_bf16 v[214:217], v[206:209], a[164:167], v[214:217]\n"
      "v_cndmask_b32 v120, v120, v178, s[60:61]\n"
      "v_mul_u32_u24 v78, 98, v85\n"
      "v_mfma_f32_16x16x32_bf16 v[130:133], v[142:145], a[168:171], 0\n"
      "v_cvt_pk_bf16_f32 v162, v164, v165\n"
      "v_mul_f32 v99, v140, v99\n"
      "v_mfma_f32_16x16x32_bf16 v[130:133], v[210:213], a[172:175], v[130:133]\n"
      "buffer_load_dwordx4 v19, s[16:19], s84 offen  lds\n"
      "v_mfma_f32_16x16x32_bf16 v[130:133], v[220:223], a[176:179], v[130:133]\n"
      "v_fmac_f32 v77, v174, v174\n"
      "v_fmac_f32 v77, v175, v175\n"
      "v_mfma_f32_16x16x32_bf16 v[130:133], v[198:201], a[180:183], v[130:133]\n"
      "v_cvt_pk_bf16_f32 v135, v136, v137\n"
      "v_mul_f32 v96, v140, v96\n"
      "v_mfma_f32_16x16x32_bf16 v[130:133], v[100:103], a[184:187], v[130:133]\n"
      "s_mov_b32 m0, s6\n"
      "v_fmac_f32 v76, v129, v129\n"
      "v_fmac_f32 v76, v190, v190\n"
      "v_mfma_f32_16x16x32_bf16 v[130:133], v[206:209], a[188:191], v[130:133]\n"
      "v_mul_f32 v117, v140, v117\n"
      "v_fmac_f32_e64 v76, v191, v191\n"
      "v_mfma_f32_16x16x32_bf16 v[194:197], v[142:145], a[192:195], 0\n"
      "buffer_load_dwordx4 v15, s[0:3], s5 offen  lds\n"
      "v_mfma_f32_16x16x32_bf16 v[194:197], v[210:213], a[196:199], v[194:197]\n"
      "ds_read_b128 v[210:213], v1 offset:32768\n"
      "v_mul_f32 v108, v140, v108\n"
      "v_mfma_f32_16x16x32_bf16 v[194:197], v[220:223], a[200:203], v[194:197]\n"
      "v_cvt_pk_bf16_f32 v130, v130, v131\n"
      "v_mul_f32 v119, v140, v119\n"
      "v_mfma_f32_16x16x32_bf16 v[194:197], v[198:201], a[204:207], v[194:197]\n"
      "ds_write_b128 v5, v[160:163] offset:24576\n"
      "v_pk_mul_f32 v[110:111], v[110:111], v[140:141] op_sel_hi:[1,0]\n"
      "v_mfma_f32_16x16x32_bf16 v[194:197], v[100:103], a[208:211], v[194:197]\n"
      "buffer_load_dwordx4 v[198:201], v36, s[28:31], s86 offen\n"
      "v_mfma_f32_16x16x32_bf16 v[194:197], v[206:209], a[212:215], v[194:197]\n"
      "s_waitcnt lgkmcnt(2)\n"
      "v_fmac_f32 v76, v192, v192\n"
      "v_mul_f32 v118, v140, v118\n"
      "v_mfma_f32_16x16x32_bf16 v[100:103], a[0:3], v[168:171], 0\n"
      "v_fmac_f32 v77, v104, v104\n"
      "v_cvt_pk_bf16_f32 v214, v214, v215\n"
      "v_mfma_f32_16x16x32_bf16 v[100:103], a[4:7], v[152:155], v[100:103]\n"
      "ds_write_b64 v6, v[134:135] offset:24576\n"
      "v_mfma_f32_16x16x32_bf16 v[100:103], a[8:11], v[184:187], v[100:103]\n"
      "v_fmac_f32 v77, v105, v105\n"
      "v_fmac_f32 v76, v193, v193\n"
      "v_mfma_f32_16x16x32_bf16 v[100:103], a[12:15], v[180:183], v[100:103]\n"
      "s_waitcnt lgkmcnt(1)\n"
      "v_fmac_f32 v77, v106, v106\n"
      "v_mul_f32_e64 v98, v140, v98\n"
      "v_mfma_f32_16x16x32_bf16 v[100:103], a[16:19], v[156:159], v[100:103]\n"
      "buffer_load_dword v86, v29, s[28:31], s86 offen\n"
      "v_mfma_f32_16x16x32_bf16 v[100:103], a[20:23], v[210:213], v[100:103]\n"
      "s_or_b32 s95, s45, s46\n"
      "v_fmac_f32 v76, v122, v122\n"
      "v_cvt_pk_bf16_f32 v194, v194, v195\n"
      "v_mfma_f32_16x16x32_bf16 v[160:163], a[24:27], v[168:171], 0\n"
      "ds_read_b128 v[218:221], v1 offset:22528\n"
      "v_fmac_f32_e64 v77, v107, v107\n"
      "v_mfma_f32_16x16x32_bf16 v[160:163], a[28:31], v[152:155], v[160:163]\n"
      "v_cvt_pk_bf16_f32 v108, v108, v109\n"
      "v_fma_f32 v222, v100, v100, s51\n"
      "v_mfma_f32_16x16x32_bf16 v[160:163], a[32:35], v[184:187], v[160:163]\n"
      "v_mul_f32 v120, v188, v120\n"
      "v_fmac_f32 v77, v112, v112\n"
      "v_mfma_f32_16x16x32_bf16 v[160:163], a[36:39], v[180:183], v[160:163]\n"
      "v_fmac_f32 v77, v113, v113\n"
      "v_fmac_f32 v76, v123, v123\n"
      "v_mfma_f32_16x16x32_bf16 v[160:163], a[40:43], v[156:159], v[160:163]\n"
      "ds_read_b128 v[176:179], v0 offset:22528\n"
      "v_fmac_f32 v76, v124, v124\n"
      "v_mfma_f32_16x16x32_bf16 v[160:163], a[44:47], v[210:213], v[160:163]\n"
      "v_fmac_f32 v76, v125, v125\n"
      "v_fmac_f32_e64 v77, v114, v114\n"
      "v_mfma_f32_16x16x32_bf16 v[148:151], a[48:51], v[168:171], 0\n"
      "v_fmac_f32_e64 v77, v115, v115\n"
      "v_cvt_pk_bf16_f32 v120, v120, v120\n"
      "v_mfma_f32_16x16x32_bf16 v[148:151], a[52:55], v[152:155], v[148:151]\n"
      "v_cvt_pk_bf16_f32 v215, v216, v217\n"
      "v_lshrrev_b32 v39, 3, v39\n"
      "v_mfma_f32_16x16x32_bf16 v[148:151], a[56:59], v[184:187], v[148:151]\n"
      "v_permlane16_swap_b32 v77, v76\n"
      "v_mfma_f32_16x16x32_bf16 v[148:151], a[60:63], v[180:183], v[148:151]\n"
      "v_add_f32 v77, v77, v76\n"
      "v_mov_b32 v76, v77\n"
      "v_mfma_f32_16x16x32_bf16 v[148:151], a[64:67], v[156:159], v[148:151]\n"
      "s_nop 0\n"
      "v_permlane32_swap_b32 v77, v76\n"
      "v_mfma_f32_16x16x32_bf16 v[148:151], a[68:71], v[210:213], v[148:151]\n"
      "v_lshrrev_b32 v189, 8, v189\n"
      "v_min_u32 v39, 96, v39\n"
      "v_mfma_f32_16x16x32_bf16 v[206:209], a[72:75], v[168:171], 0\n"
      "v_fmac_f32 v222, v101, v101\n"
      "v_readlane_b32 s47, v38, s43\n"
      "v_mfma_f32_16x16x32_bf16 v[206:209], a[76:79], v[152:155], v[206:209]\n"
      "v_or3_b32 v202, v202, v203, v204\n"
      "v_add_f32_e64 v77, v77, v76\n"
      "v_mfma_f32_16x16x32_bf16 v[206:209], a[80:83], v[184:187], v[206:209]\n"
      "ds_read_b128 v[134:137], v0 offset:28672\n"
      "v_fmac_f32 v222, v102, v102\n"
      "v_mfma_f32_16x16x32_bf16 v[206:209], a[84:87], v[180:183], v[206:209]\n"
      "s_and_b32 s77, s77, 15\n"
      "v_cvt_pk_bf16_f32 v195, v196, v197\n"
      "v_cvt_pk_bf16_f32 v131, v132, v133\n"
      "v_mfma_f32_16x16x32_bf16 v[206:209], a[88:91], v[156:159], v[206:209]\n"
      "v_rsq_f32 v77, v77\n"
      "v_mfma_f32_16x16x32_bf16 v[206:209], a[92:95], v[210:213], v[206:209]\n"
      "v_cvt_pk_bf16_f32 v109, v110, v111\n"
      "v_cvt_pk_bf16_f32 v110, v116, v117\n"
      "v_mfma_f32_16x16x32_bf16 v[164:167], a[96:99], v[168:171], 0\n"
      "v_cvt_pk_bf16_f32 v111, v118, v119\n"
      "v_fmac_f32 v222, v103, v103\n"
      "v_mfma_f32_16x16x32_bf16 v[164:167], a[100:103], v[152:155], v[164:167]\n"
      "s_or_b32 s95, s95, s48\n"
      "v_mov_b32 v76, v77\n"
      "v_fma_f32 v132, v206, v206, s51\n"
      "v_mfma_f32_16x16x32_bf16 v[164:167], a[104:107], v[184:187], v[164:167]\n"
      "v_permlane16_swap_b32 v77, v76\n"
      "v_mfma_f32_16x16x32_bf16 v[164:167], a[108:111], v[180:183], v[164:167]\n"
      "v_fmac_f32 v132, v207, v207\n"
      "v_mul_f32 v77, s50, v77\n"
      "v_mfma_f32_16x16x32_bf16 v[164:167], a[112:115], v[156:159], v[164:167]\n"
      "v_fmac_f32 v132, v208, v208\n"
      "v_fmac_f32 v222, v160, v160\n"
      "v_mfma_f32_16x16x32_bf16 v[164:167], a[116:119], v[210:213], v[164:167]\n"
      "v_mul_f32 v97, v140, v97\n"
      "v_fmac_f32 v222, v161, v161\n"
      "v_mfma_f32_16x16x32_bf16 v[142:145], a[120:123], v[168:171], 0\n"
      "v_mul_f32 v125, v76, v125\n"
      "v_fmac_f32 v222, v162, v162\n"
      "v_mfma_f32_16x16x32_bf16 v[142:145], a[124:127], v[152:155], v[142:145]\n"
      "ds_write_b128 v5, v[108:111] offset:30720\n"
      "v_mfma_f32_16x16x32_bf16 v[142:145], a[128:131], v[184:187], v[142:145]\n"
      "v_fmac_f32 v132, v209, v209\n"
      "v_mul_f32 v122, v76, v122\n"
      "v_mfma_f32_16x16x32_bf16 v[142:145], a[132:135], v[180:183], v[142:145]\n"
      "v_fmac_f32 v222, v163, v163\n"
      "v_fmac_f32 v132, v164, v164\n"
      "v_mfma_f32_16x16x32_bf16 v[142:145], a[136:139], v[156:159], v[142:145]\n"
      "v_mul_f32 v173, v77, v173\n"
      "v_cvt_pk_bf16_f32 v96, v96, v97\n"
      "v_mfma_f32_16x16x32_bf16 v[142:145], a[140:143], v[210:213], v[142:145]\n"
      "v_lshl_add_u32 v78, v189, 3, v78\n"
      "v_fmac_f32_e64 v132, v165, v165\n"
      "v_mfma_f32_16x16x32_bf16 v[116:119], v[168:171], a[144:147], 0\n"
      "v_mul_f32 v190, v76, v190\n"
      "v_mul_f32 v127, v76, v127\n"
      "v_mfma_f32_16x16x32_bf16 v[116:119], v[152:155], a[148:151], v[116:119]\n"
      "v_cvt_pk_bf16_f32 v97, v98, v99\n"
      "v_fmac_f32 v222, v148, v148\n"
      "v_mfma_f32_16x16x32_bf16 v[116:119], v[184:187], a[152:155], v[116:119]\n"
      "v_mul_f32 v172, v77, v172\n"
      "v_fmac_f32 v132, v166, v166\n"
      "v_mfma_f32_16x16x32_bf16 v[116:119], v[180:183], a[156:159], v[116:119]\n"
      "v_mul_f32 v115, v77, v115\n"
      "v_fmac_f32 v132, v167, v167\n"
      "v_mfma_f32_16x16x32_bf16 v[116:119], v[156:159], a[160:163], v[116:119]\n"
      "ds_write_b16 v12, v120\n"
      "v_mfma_f32_16x16x32_bf16 v[116:119], v[210:213], a[164:167], v[116:119]\n"
      "v_fmac_f32 v222, v149, v149\n"
      "v_lshlrev_b32 v189, 2, v189\n"
      "v_mfma_f32_16x16x32_bf16 v[138:141], v[168:171], a[168:171], 0\n"
      "v_fmac_f32 v132, v142, v142\n"
      "v_fmac_f32_e64 v132, v143, v143\n"
      "v_mfma_f32_16x16x32_bf16 v[138:141], v[152:155], a[172:175], v[138:141]\n"
      "ds_write_b64 v6, v[96:97] offset:30720\n"
      "v_mfma_f32_16x16x32_bf16 v[138:141], v[184:187], a[176:179], v[138:141]\n"
      "v_mul_f32 v123, v76, v123\n"
      "v_fmac_f32 v222, v150, v150\n"
      "v_mfma_f32_16x16x32_bf16 v[138:141], v[180:183], a[180:183], v[138:141]\n"
      "v_bitop3_b32 v202, v202, v205, s33 bitop3:0xa8\n"
      "v_cvt_pk_bf16_f32 v172, v172, v173\n"
      "v_mfma_f32_16x16x32_bf16 v[138:141], v[156:159], a[184:187], v[138:141]\n"
      "ds_read_b128 v[108:111], v1 offset:28672\n"
      "v_fmac_f32 v132, v144, v144\n"
      "v_mfma_f32_16x16x32_bf16 v[138:141], v[210:213], a[188:191], v[138:141]\n"
      "v_fmac_f32 v132, v145, v145\n"
      "v_fmac_f32 v222, v151, v151\n"
      "v_mfma_f32_16x16x32_bf16 v[96:99], v[168:171], a[192:195], 0\n"
      "v_mul_f32 v175, v77, v175\n"
      "v_mul_f32 v126, v76, v126\n"
      "v_mfma_f32_16x16x32_bf16 v[96:99], v[152:155], a[196:199], v[96:99]\n"
      "ds_read_b128 v[152:155], v0 offset:34816\n"
      "v_mul_f32 v113, v77, v113\n"
      "v_mfma_f32_16x16x32_bf16 v[96:99], v[184:187], a[200:203], v[96:99]\n"
      "s_waitcnt lgkmcnt(2)\n"
      "v_permlane16_swap_b32 v222, v132\n"
      "v_mfma_f32_16x16x32_bf16 v[96:99], v[180:183], a[204:207], v[96:99]\n"
      "ds_read_b128 v[184:187], v1 offset:34816\n"
      "v_add_f32_e64 v222, v222, v132\n"
      "v_mfma_f32_16x16x32_bf16 v[96:99], v[156:159], a[208:211], v[96:99]\n"
      "ds_read_b128 v[168:171], v7\n"
      "v_mov_b32 v132, v222\n"
      "v_mfma_f32_16x16x32_bf16 v[96:99], v[210:213], a[212:215], v[96:99]\n"
      "s_nop 0\n"
      "v_permlane32_swap_b32 v222, v132\n"
      "v_mfma_f32_16x16x32_bf16 v[210:213], a[0:3], v[176:179], 0\n"
      "v_add_f32 v222, v222, v132\n"
      "v_add_u32_e64 v39, s77, v39\n"
      "v_mfma_f32_16x16x32_bf16 v[210:213], a[4:7], v[218:221], v[210:213]\n"
      "v_rsq_f32 v222, v222\n"
      "v_mfma_f32_16x16x32_bf16 v[210:213], a[8:11], v[134:137], v[210:213]\n"
      "s_waitcnt lgkmcnt(2)\n"
      "v_cmp_ne_u32 s[56:57], 0, v202\n"
      "v_cvt_pk_bf16_f32 v126, v126, v127\n"
      "v_mfma_f32_16x16x32_bf16 v[210:213], a[12:15], v[108:111], v[210:213]\n"
      "s_waitcnt lgkmcnt(1)\n"
      "v_cvt_pk_bf16_f32 v133, v140, v141\n"
      "v_mov_b32 v132, v222\n"
      "v_mfma_f32_16x16x32_bf16 v[210:213], a[16:19], v[152:155], v[210:213]\n"
      "s_nop 0\n"
      "v_permlane16_swap_b32 v222, v132\n"
      "v_mfma_f32_16x16x32_bf16 v[210:213], a[20:23], v[184:187], v[210:213]\n"
      "v_sub_u32 v85, v85, v189\n"
      "v_mul_f32 v112, v77, v112\n"
      "v_mfma_f32_16x16x32_bf16 v[156:159], a[24:27], v[176:179], 0\n"
      "v_cvt_pk_bf16_f32 v217, v118, v119\n"
      "v_mul_f32 v222, s50, v222\n"
      "v_mfma_f32_16x16x32_bf16 v[156:159], a[28:31], v[218:221], v[156:159]\n"
      "s_and_b32 s47, s47, -4\n"
      "v_mul_f32 v124, v76, v124\n"
      "v_mul_f32 v165, v132, v165\n"
      "v_mfma_f32_16x16x32_bf16 v[156:159], a[32:35], v[134:137], v[156:159]\n"
      "s_waitcnt lgkmcnt(0)\n"
      "v_cvt_pk_bf16_f32 v216, v116, v117\n"
      "v_mul_f32 v174, v77, v174\n"
      "v_mfma_f32_16x16x32_bf16 v[156:159], a[36:39], v[108:111], v[156:159]\n"
      "buffer_store_dwordx4 v[168:171], v20, s[24:27], s85 offen sc1\n"
      "v_pk_mul_f32 v[160:161], v[160:161], v[222:223] op_sel_hi:[1,0]\n"
      "v_pk_mul_f32 v[208:209], v[208:209], v[132:133] op_sel_hi:[1,0]\n"
      "v_pk_mul_f32 v[144:145], v[144:145], v[132:133] op_sel_hi:[1,0]\n"
      "v_pk_mul_f32 v[104:105], v[104:105], v[76:77] op_sel:[0,1]\n"
      "v_pk_mul_f32 v[166:167], v[166:167], v[132:133] op_sel_hi:[1,0]\n"
      "v_pk_mul_f32 v[192:193], v[192:193], v[76:77] op_sel_hi:[1,0]\n"
      "v_pk_mul_f32 v[106:107], v[106:107], v[76:77] op_sel:[0,1]\n"
      "v_pk_mul_f32 v[100:101], v[100:101], v[222:223] op_sel_hi:[1,0]\n"
      "v_mfma_f32_16x16x32_bf16 v[156:159], a[40:43], v[152:155], v[156:159]\n"
      "v_mul_f32 v114, v77, v114\n"
      "v_mul_f32 v129, v76, v129\n"
      "v_mfma_f32_16x16x32_bf16 v[156:159], a[44:47], v[184:187], v[156:159]\n"
      "s_add_u32 s73, s73, 1024\n"
      "v_mul_f32 v207, v132, v207\n"
      "v_fma_f32 v146, v210, v210, s51\n"
      "v_mfma_f32_16x16x32_bf16 v[116:119], a[48:51], v[176:179], 0\n"
      "v_mul_f32 v128, v76, v128\n"
      "v_cvt_pk_bf16_f32 v122, v122, v123\n"
      "v_mfma_f32_16x16x32_bf16 v[116:119], a[52:55], v[218:221], v[116:119]\n"
      "v_mul_f32 v164, v132, v164\n"
      "v_fmac_f32 v146, v211, v211\n"
      "v_mfma_f32_16x16x32_bf16 v[116:119], a[56:59], v[134:137], v[116:119]\n"
      "v_fmac_f32 v146, v212, v212\n"
      "v_mul_f32 v206, v132, v206\n"
      "v_mfma_f32_16x16x32_bf16 v[116:119], a[60:63], v[108:111], v[116:119]\n"
      "v_add_u32_e64 v78, s77, v78\n"
      "v_cvt_pk_bf16_f32 v123, v124, v125\n"
      "v_mfma_f32_16x16x32_bf16 v[116:119], a[64:67], v[152:155], v[116:119]\n"
      "v_lshlrev_b32 v85, 4, v85\n"
      "v_mul_f32 v102, v222, v102\n"
      "v_mfma_f32_16x16x32_bf16 v[116:119], a[68:71], v[184:187], v[116:119]\n"
      "v_cvt_pk_bf16_f32 v173, v174, v175\n"
      "v_cvt_pk_bf16_f32 v100, v100, v101\n"
      "v_mfma_f32_16x16x32_bf16 v[202:205], a[72:75], v[176:179], 0\n"
      "v_mul_f32 v103, v222, v103\n"
      "v_fmac_f32 v146, v213, v213\n"
      "v_mfma_f32_16x16x32_bf16 v[202:205], a[76:79], v[218:221], v[202:205]\n"
      "v_cvt_pk_bf16_f32 v174, v104, v105\n"
      "v_fmac_f32 v146, v156, v156\n"
      "v_mfma_f32_16x16x32_bf16 v[202:205], a[80:83], v[134:137], v[202:205]\n"
      "v_mul_f32 v162, v222, v162\n"
      "v_fmac_f32_e64 v146, v157, v157\n"
      "v_mfma_f32_16x16x32_bf16 v[202:205], a[84:87], v[108:111], v[202:205]\n"
      "v_mul_f32 v143, v132, v143\n"
      "v_mul_f32 v191, v76, v191\n"
      "v_mfma_f32_16x16x32_bf16 v[202:205], a[88:91], v[152:155], v[202:205]\n"
      "v_mul_f32_e64 v142, v132, v142\n"
      "v_cvt_pk_bf16_f32 v206, v206, v207\n"
      "v_mfma_f32_16x16x32_bf16 v[202:205], a[92:95], v[184:187], v[202:205]\n"
      "s_cmp_ge_u32 s95, 0x40000000\n"
      "s_cselect_b32 s94, -1, -2\n"
      "v_fmac_f32_e64 v146, v158, v158\n"
      "v_mfma_f32_16x16x32_bf16 v[180:183], a[96:99], v[176:179], 0\n"
      "v_fmac_f32_e64 v146, v159, v159\n"
      "v_cvt_pk_bf16_f32 v127, v128, v129\n"
      "v_mfma_f32_16x16x32_bf16 v[180:183], a[100:103], v[218:221], v[180:183]\n"
      "v_cvt_pk_bf16_f32 v175, v106, v107\n"
      "v_fma_f32 v147, v202, v202, s51\n"
      "v_mfma_f32_16x16x32_bf16 v[180:183], a[104:107], v[134:137], v[180:183]\n"
      "v_cvt_pk_bf16_f32 v132, v138, v139\n"
      "v_fmac_f32_e64 v147, v203, v203\n"
      "v_mfma_f32_16x16x32_bf16 v[180:183], a[108:111], v[108:111], v[180:183]\n"
      "v_cvt_pk_bf16_f32 v197, v98, v99\n"
      "v_cvt_pk_bf16_f32 v128, v190, v191\n"
      "v_mfma_f32_16x16x32_bf16 v[180:183], a[112:115], v[152:155], v[180:183]\n"
      "v_cvt_pk_bf16_f32 v112, v112, v113\n"
      "v_fmac_f32 v146, v116, v116\n"
      "v_mfma_f32_16x16x32_bf16 v[180:183], a[116:119], v[184:187], v[180:183]\n"
      "v_fmac_f32 v146, v117, v117\n"
      "v_cvt_pk_bf16_f32 v129, v192, v193\n"
      "v_mfma_f32_16x16x32_bf16 v[188:191], a[120:123], v[176:179], 0\n"
      "v_cvt_pk_bf16_f32 v101, v102, v103\n"
      "v_mov_b32 v192, 0x3fb9\n"
      "v_mfma_f32_16x16x32_bf16 v[188:191], a[124:127], v[218:221], v[188:191]\n"
      "v_cvt_pk_bf16_f32 v102, v160, v161\n"
      "v_fmac_f32 v146, v118, v118\n"
      "v_mfma_f32_16x16x32_bf16 v[188:191], a[128:131], v[134:137], v[188:191]\n"
      "v_fmac_f32 v147, v204, v204\n"
      "v_fmac_f32 v147, v205, v205\n"
      "v_mfma_f32_16x16x32_bf16 v[188:191], a[132:135], v[108:111], v[188:191]\n"
      "s_waitcnt vmcnt(8)\n"
      "v_fmac_f32 v147, v180, v180\n"
      "v_mul_f32 v163, v222, v163\n"
      "v_mfma_f32_16x16x32_bf16 v[188:191], a[136:139], v[152:155], v[188:191]\n"
      "v_cvt_pk_bf16_f32 v196, v96, v97\n"
      "v_fmac_f32 v146, v119, v119\n"
      "v_mfma_f32_16x16x32_bf16 v[188:191], a[140:143], v[184:187], v[188:191]\n"
      "v_fmac_f32 v147, v181, v181\n"
      "v_fmac_f32 v147, v182, v182\n"
      "v_fmac_f32 v147, v183, v183\n"
      "v_cvt_pk_bf16_f32 v103, v162, v163\n"
      "v_and_b32_e64 v93, 2, v78\n"
      "v_pk_mul_f32 v[148:149], v[148:149], v[222:223] op_sel_hi:[1,0]\n"
      "v_pk_mul_f32 v[150:151], v[150:151], v[222:223] op_sel_hi:[1,0]\n"
      "v_mfma_f32_16x16x32_bf16 v[168:171], v[176:179], a[144:147], 0\n"
      "v_fmac_f32 v147, v188, v188\n"
      "v_fmac_f32 v147, v189, v189\n"
      "v_fmac_f32 v147, v190, v190\n"
      "v_fmac_f32 v147, v191, v191\n"
      "v_mfma_f32_16x16x32_bf16 v[168:171], v[218:221], a[148:151], v[168:171]\n"
      "s_and_b32 s4, s45, s8\n"
      "v_permlane16_swap_b32 v146, v147\n"
      "v_mfma_f32_16x16x32_bf16 v[168:171], v[134:137], a[152:155], v[168:171]\n"
      "v_add_f32 v146, v146, v147\n"
      "v_mov_b32 v147, v146\n"
      "v_mfma_f32_16x16x32_bf16 v[168:171], v[108:111], a[156:159], v[168:171]\n"
      "s_nop 0\n"
      "v_permlane32_swap_b32 v146, v147\n"
      "v_mfma_f32_16x16x32_bf16 v[168:171], v[152:155], a[160:163], v[168:171]\n"
      "v_add_f32 v146, v146, v147\n"
      "v_rsq_f32 v146, v146\n"
      "v_mfma_f32_16x16x32_bf16 v[168:171], v[184:187], a[164:167], v[168:171]\n"
      "s_cmp_gt_u32 s42, s44\n"
      "s_cselect_b32 s95, s94, s95\n"
      "v_mov_b32 v147, v146\n"
      "v_mfma_f32_16x16x32_bf16 v[96:99], v[176:179], a[168:171], 0\n"
      "v_cvt_pk_bf16_f32 v113, v114, v115\n"
      "v_permlane16_swap_b32 v146, v147\n"
      "v_cvt_pk_bf16_f32 v142, v142, v143\n"
      "v_mul_f32_e64 v146, s50, v146\n"
      "v_pk_mul_f32 v[188:189], v[188:189], v[146:147] op_sel:[0,1]\n"
      "v_pk_mul_f32 v[116:117], v[116:117], v[146:147] op_sel_hi:[1,0]\n"
      "v_pk_mul_f32 v[158:159], v[158:159], v[146:147] op_sel_hi:[1,0]\n"
      "v_pk_mul_f32 v[202:203], v[202:203], v[146:147] op_sel:[0,1]\n"
      "v_pk_mul_f32 v[212:213], v[212:213], v[146:147] op_sel_hi:[1,0]\n"
      "v_pk_mul_f32 v[182:183], v[182:183], v[146:147] op_sel:[0,1]\n"
      "v_pk_mul_f32 v[204:205], v[204:205], v[146:147] op_sel:[0,1]\n"
      "v_pk_mul_f32 v[210:211], v[210:211], v[146:147] op_sel_hi:[1,0]\n"
      "v_mfma_f32_16x16x32_bf16 v[96:99], v[218:221], a[172:175], v[96:99]\n"
      "v_cvt_pk_bf16_f32 v207, v208, v209\n"
      "v_mul_f32_e64 v119, v146, v119\n"
      "v_mfma_f32_16x16x32_bf16 v[96:99], v[134:137], a[176:179], v[96:99]\n"
      "ds_read_b128 v[160:163], v2\n"
      "v_cvt_pk_bf16_f32 v77, v170, v171\n"
      "v_mfma_f32_16x16x32_bf16 v[96:99], v[108:111], a[180:183], v[96:99]\n"
      "v_cvt_pk_bf16_f32 v143, v144, v145\n"
      "v_cvt_pk_bf16_f32 v208, v164, v165\n"
      "v_mfma_f32_16x16x32_bf16 v[96:99], v[152:155], a[184:187], v[96:99]\n"
      "ds_read_b128 v[104:107], v2 offset:6144\n"
      "v_mov_b32_e64 v193, 0\n"
      "v_mfma_f32_16x16x32_bf16 v[96:99], v[184:187], a[188:191], v[96:99]\n"
      "v_cvt_pk_bf16_f32 v76, v168, v169\n"
      "v_and_b32_e64 v78, -4, v78\n"
      "v_mfma_f32_16x16x32_bf16 v[138:141], v[176:179], a[192:195], 0\n"
      "ds_read_b128 v[176:179], v2 offset:3072\n"
      "v_cvt_pk_bf16_f32 v209, v166, v167\n"
      "v_mfma_f32_16x16x32_bf16 v[138:141], v[218:221], a[196:199], v[138:141]\n"
      "ds_read_b128 v[168:171], v7 offset:4096\n"
      "v_mul_f32 v118, v146, v118\n"
      "v_mfma_f32_16x16x32_bf16 v[138:141], v[134:137], a[200:203], v[138:141]\n"
      "ds_read_b128 v[134:137], v2 offset:9216\n"
      "v_cvt_pk_bf16_f32 v202, v202, v203\n"
      "v_mfma_f32_16x16x32_bf16 v[138:141], v[108:111], a[204:207], v[138:141]\n"
      "s_waitcnt lgkmcnt(4)\n"
      "v_cvt_pk_bf16_f32 v148, v148, v149\n"
      "v_cvt_pk_bf16_f32 v81, v98, v99\n"
      "v_mfma_f32_16x16x32_bf16 v[138:141], v[152:155], a[208:211], v[138:141]\n"
      "ds_read_b128 v[108:111], v2 offset:12288\n"
      "v_mov_b32_e64 v125, 0\n"
      "v_mfma_f32_16x16x32_bf16 v[138:141], v[184:187], a[212:215], v[138:141]\n"
      "ds_read_b128 v[220:223], v2 offset:15360\n"
      "v_mul_f32 v180, v147, v180\n"
      "v_mfma_f32_16x16x32_bf16 v[184:187], a[216:219], v[160:163], 0\n"
      "s_waitcnt lgkmcnt(4)\n"
      "v_mul_f32 v181, v147, v181\n"
      "v_add_u32 v98, 110592, v78\n"
      "v_mfma_f32_16x16x32_bf16 v[184:187], a[220:223], v[176:179], v[184:187]\n"
      "s_waitcnt lgkmcnt(3)\n"
      "v_cvt_pk_bf16_f32 v203, v204, v205\n"
      "v_cvt_pk_bf16_f32 v116, v116, v117\n"
      "v_mfma_f32_16x16x32_bf16 v[184:187], a[224:227], v[104:107], v[184:187]\n"
      "buffer_store_dwordx4 v[168:171], v21, s[24:27], s85 offen sc1\n"
      "v_mfma_f32_16x16x32_bf16 v[152:155], a[240:243], v[160:163], 0\n"
      "ds_read2_b32 v[164:165], v98 offset0:2 offset1:10\n"
      "v_cvt_pk_bf16_f32 v84, v138, v139\n"
      "v_mfma_f32_16x16x32_bf16 v[152:155], a[244:247], v[176:179], v[152:155]\n"
      "s_cmp_ge_u32 s95, 0x40000000\n"
      "s_cselect_b64 vcc, -1, s[56:57]\n"
      "v_lshlrev_b64 v[192:193], v85, v[192:193]\n"
      "v_mfma_f32_16x16x32_bf16 v[152:155], a[248:251], v[104:107], v[152:155]\n"
      "s_waitcnt lgkmcnt(1)\n"
      "v_cvt_pk_bf16_f32 v205, v182, v183\n"
      "v_mul_f32 v157, v146, v157\n"
      "v_mfma_f32_16x16x32_bf16 v[168:171], v[232:235], v[160:163], 0\n"
      "ds_read2_b32 v[138:139], v98 offset0:16 offset1:17\n"
      "v_cvt_pk_bf16_f32 v80, v96, v97\n"
      "v_mfma_f32_16x16x32_bf16 v[168:171], v[236:239], v[176:179], v[168:171]\n"
      "ds_read_b128 v[160:163], v7 offset:8192\n"
      "v_cvt_pk_bf16_f32 v210, v210, v211\n"
      "v_mfma_f32_16x16x32_bf16 v[168:171], v[240:243], v[104:107], v[168:171]\n"
      "ds_read2_b32 v[176:177], v98 offset0:0 offset1:1\n"
      "v_cmp_gt_u32 s[72:73], 64, v85\n"
      "v_mfma_f32_16x16x32_bf16 v[184:187], a[228:231], v[134:137], v[184:187]\n"
      "v_cvt_pk_bf16_f32 v204, v180, v181\n"
      "v_cvt_pk_bf16_f32 v211, v212, v213\n"
      "v_mfma_f32_16x16x32_bf16 v[184:187], a[232:235], v[108:111], v[184:187]\n"
      "v_cvt_pk_bf16_f32 v117, v118, v119\n"
      "v_cndmask_b32_e64 v192, 0, v192, s[72:73]\n"
      "v_mfma_f32_16x16x32_bf16 v[184:187], a[236:239], v[220:223], v[184:187]\n"
      "ds_read_b128 v[104:107], v10 offset:1200\n"
      "v_cvt_pk_bf16_f32 v149, v150, v151\n"
      "v_mfma_f32_16x16x32_bf16 v[152:155], a[252:255], v[134:137], v[152:155]\n"
      "ds_read_b64 v[218:219], v10 offset:1192\n"
      "v_mul_f32_e64 v191, v147, v191\n"
      "v_mfma_f32_16x16x32_bf16 v[152:155], v[224:227], v[108:111], v[152:155]\n"
      "ds_read_b128 v[118:121], v7 offset:12288\n"
      "v_mov_b32 v124, 0xbb2b\n"
      "v_mfma_f32_16x16x32_bf16 v[152:155], v[228:231], v[220:223], v[152:155]\n"
      "ds_read2_b32 v[150:151], v98 offset0:8 offset1:9\n"
      "v_mul_f32 v190, v147, v190\n"
      "v_mfma_f32_16x16x32_bf16 v[168:171], v[244:247], v[134:137], v[168:171]\n"
      "s_waitcnt lgkmcnt(4)\n"
      "v_mul_f32 v156, v146, v156\n"
      "v_cvt_pk_bf16_f32 v213, v158, v159\n"
      "v_mfma_f32_16x16x32_bf16 v[168:171], v[248:251], v[108:111], v[168:171]\n"
      "buffer_store_dwordx4 v[160:163], v22, s[24:27], s85 offen sc1\n"
      "v_mfma_f32_16x16x32_bf16 v[168:171], v[252:255], v[220:223], v[168:171]\n"
      "ds_read2_b32 v[166:167], v98 offset0:24 offset1:18\n"
      "v_cndmask_b32_e64 v193, 0, v193, s[72:73]\n"
      "v_mfma_f32_16x16x32_bf16 v[96:99], v[126:129], v[172:175], v[40:43]\n"
      "v_alignbyte_b32 v176, v177, v176, v93\n"
      "v_cmp_gt_u32 s[72:73], 64, v85\n"
      "v_mfma_f32_16x16x32_bf16 v[108:111], v[206:209], v[172:175], v[44:47]\n"
      "s_waitcnt lgkmcnt(1)\n"
      "v_lshlrev_b64 v[124:125], v85, v[124:125]\n"
      "v_cvt_pk_bf16_f32 v188, v188, v189\n"
      "v_mfma_f32_16x16x32_bf16 v[220:223], v[202:205], v[172:175], v[48:51]\n"
      "ds_read_b128 v[144:147], v2 offset:256\n"
      "v_cvt_pk_bf16_f32 v189, v190, v191\n"
      "v_mfma_f32_16x16x32_bf16 v[178:181], v[104:107], v[172:175], v[24:27]\n"
      "ds_read_b128 v[160:163], v7 offset:16384\n"
      "v_alignbyte_b32 v138, v139, v138, v93\n"
      "v_mfma_f32_16x16x16_bf16 v[96:99], v[122:123], v[112:113], v[96:99]\n"
      "v_fma_f32 v114, v184, v184, s52\n"
      "v_alignbyte_b32 v150, v151, v150, v93\n"
      "v_add_u32 v39, 115296, v39\n"
      "v_mfma_f32_16x16x16_bf16 v[108:111], v[142:143], v[112:113], v[108:111]\n"
      "v_cvt_pk_bf16_f32 v212, v156, v157\n"
      "v_cndmask_b32_e64 v124, 0, v124, s[72:73]\n"
      "v_mul_f32_e64 v115, v185, v185\n"
      "v_mfma_f32_16x16x16_bf16 v[220:223], v[188:189], v[112:113], v[220:223]\n"
      "buffer_store_dwordx4 v[118:121], v23, s[24:27], s85 offen sc1\n"
      "v_mfma_f32_16x16x16_bf16 v[178:181], v[218:219], v[112:113], v[178:181]\n"
      "v_alignbyte_b32 v177, v164, v177, v93\n"
      "v_alignbyte_b32 v151, v165, v151, v93\n"
      "v_fmac_f32 v114, v186, v186\n"
      "v_mfma_f32_16x16x16_bf16 v[96:99], v[192:193], v[176:177], v[96:99]\n"
      "s_waitcnt lgkmcnt(0)\n"
      "v_alignbyte_b32 v139, v167, v139, v93\n"
      "v_add_u32 v112, 112160, v78\n"
      "v_mfma_f32_16x16x16_bf16 v[108:111], v[192:193], v[150:151], v[108:111]\n"
      "ds_read_b128 v[118:121], v2 offset:3328\n"
      "v_alignbyte_b32 v166, v166, v166, v93\n"
      "v_mfma_f32_16x16x16_bf16 v[220:223], v[192:193], v[138:139], v[220:223]\n"
      "ds_read2_b32 v[156:157], v112 offset0:8 offset1:9\n"
      "v_cndmask_b32_e64 v125, 0, v125, s[72:73]\n"
      "v_mfma_f32_16x16x16_bf16 v[178:181], v[192:193], v[166:167], v[178:181]\n"
      "ds_read2_b32 v[182:183], v112 offset0:0 offset1:1\n"
      "v_cvt_pk_bf16_f32 v85, v140, v141\n"
      "v_mfma_f32_16x16x16_bf16 v[96:99], v[124:125], v[176:177], v[96:99]\n"
      "ds_read_b128 v[172:175], v2 offset:6400\n"
      "v_fmac_f32_e64 v115, v187, v187\n"
      "v_mfma_f32_16x16x16_bf16 v[108:111], v[124:125], v[150:151], v[108:111]\n"
      "ds_read2_b32 v[140:141], v112 offset0:2 offset1:10\n"
      "v_fmac_f32_e64 v115, v153, v153\n"
      "v_mfma_f32_16x16x16_bf16 v[220:223], v[124:125], v[138:139], v[220:223]\n"
      "buffer_store_dwordx4 v[160:163], v25, s[24:27], s85 offen sc1\n"
      "v_mfma_f32_16x16x16_bf16 v[178:181], v[124:125], v[166:167], v[178:181]\n"
      "v_fmac_f32_e64 v114, v152, v152\n"
      "v_add_u32 v190, 113728, v78\n"
      "v_max3_f32 v78, v96, v97, v98\n"
      "v_mfma_f32_16x16x32_bf16 v[160:163], a[216:219], v[144:147], 0\n"
      "s_waitcnt lgkmcnt(2)\n"
      "v_alignbyte_b32 v156, v157, v156, v93\n"
      "v_max3_f32 v78, v78, v110, v111\n"
      "v_mfma_f32_16x16x32_bf16 v[160:163], a[220:223], v[118:121], v[160:163]\n"
      "s_waitcnt lgkmcnt(1)\n"
      "v_max3_f32 v180, v99, v108, v109\n"
      "v_max3_f32 v78, v78, v222, v223\n"
      "v_mfma_f32_16x16x32_bf16 v[160:163], a[224:227], v[172:175], v[160:163]\n"
      "ds_read_u16 v39, v39\n"
      "v_max3_f32 v180, v180, v220, v221\n"
      "v_mfma_f32_16x16x32_bf16 v[164:167], a[240:243], v[144:147], 0\n"
      "s_waitcnt lgkmcnt(1)\n"
      "v_max3_f32 v78, v78, v180, v178\n"
      "v_alignbyte_b32 v182, v183, v182, v93\n"
      "v_mfma_f32_16x16x32_bf16 v[164:167], a[244:247], v[118:121], v[164:167]\n"
      "ds_read2_b32 v[180:181], v112 offset0:16 offset1:17\n"
      "v_alignbyte_b32 v157, v141, v157, v93\n"
      "v_mfma_f32_16x16x32_bf16 v[164:167], a[248:251], v[172:175], v[164:167]\n"
      "s_waitcnt vmcnt(6)\n"
      "v_or3_b32 v198, v198, v199, v200\n"
      "v_alignbyte_b32 v183, v140, v183, v93\n"
      "v_mfma_f32_16x16x32_bf16 v[134:137], v[232:235], v[144:147], 0\n"
      "ds_read2_b32 v[158:159], v112 offset0:24 offset1:18\n"
      "v_mov_b32_e64 v177, v78\n"
      "v_mfma_f32_16x16x32_bf16 v[134:137], v[236:239], v[118:121], v[134:137]\n"
      "s_nop 0\n"
      "v_permlane16_swap_b32 v78, v177\n"
      "v_mfma_f32_16x16x32_bf16 v[134:137], v[240:243], v[172:175], v[134:137]\n"
      "v_max_f32 v78, v78, v177\n"
      "v_mov_b32 v177, v78\n"
      "v_mfma_f32_16x16x32_bf16 v[144:147], v[126:129], v[100:103], v[52:55]\n"
      "s_nop 0\n"
      "v_permlane32_swap_b32 v78, v177\n"
      "v_mfma_f32_16x16x32_bf16 v[138:141], v[206:209], v[100:103], v[56:59]\n"
      "s_waitcnt lgkmcnt(1)\n"
      "v_max_f32 v78, v78, v177\n"
      "v_sub_f32 v108, v108, v78\n"
      "v_alignbyte_b32 v180, v181, v180, v93\n"
      "v_lshlrev_b32_e64 v39, 16, v39\n"
      "s_waitcnt vmcnt(5)\n"
      "v_or3_b32 v198, v198, v201, v86\n"
      "v_pk_add_f32 v[220:221], v[220:221], v[78:79] op_sel_hi:[1,0] neg_lo:[0,1] neg_hi:[0,1]\n"
      "v_pk_add_f32 v[98:99], v[98:99], v[78:79] op_sel_hi:[1,0] neg_lo:[0,1] neg_hi:[0,1]\n"
      "v_pk_fma_f32 v[114:115], v[154:155], v[154:155], v[114:115]\n"
      "v_pk_add_f32 v[96:97], v[96:97], v[78:79] op_sel_hi:[1,0] neg_lo:[0,1] neg_hi:[0,1]\n"
      "v_pk_add_f32 v[222:223], v[222:223], v[78:79] op_sel_hi:[1,0] neg_lo:[0,1] neg_hi:[0,1]\n"
      "v_pk_add_f32 v[110:111], v[110:111], v[78:79] op_sel_hi:[1,0] neg_lo:[0,1] neg_hi:[0,1]\n"
      "v_mfma_f32_16x16x32_bf16 v[118:121], v[202:205], v[100:103], v[60:63]\n"
      "ds_write_b32 v31, v198 offset:5632\n"
      "v_mfma_f32_16x16x32_bf16 v[174:177], v[104:107], v[100:103], v[26:29]\n"
      "s_waitcnt lgkmcnt(1)\n"
      "v_exp_f32 v222, v222\n"
      "v_mfma_f32_16x16x16_bf16 v[144:147], v[122:123], v[148:149], v[144:147]\n"
      "v_exp_f32_e64 v97, v97\n"
      "v_alignbyte_b32 v181, v159, v181, v93\n"
      "v_mfma_f32_16x16x16_bf16 v[138:141], v[142:143], v[148:149], v[138:141]\n"
      "v_exp_f32 v220, v220\n"
      "v_sub_f32 v178, v178, v78\n"
      "v_mfma_f32_16x16x16_bf16 v[118:121], v[188:189], v[148:149], v[118:121]\n"
      "v_alignbyte_b32 v158, v158, v158, v93\n"
      "v_exp_f32 v110, v110\n"
      "v_mfma_f32_16x16x16_bf16 v[174:177], v[218:219], v[148:149], v[174:177]\n"
      "v_exp_f32 v221, v221\n"
      "v_exp_f32_e64 v96, v96\n"
      "v_mfma_f32_16x16x16_bf16 v[144:147], v[192:193], v[182:183], v[144:147]\n"
      "v_cvt_pk_bf16_f32 v88, v220, v221\n"
      "v_exp_f32_e64 v223, v223\n"
      "v_mfma_f32_16x16x16_bf16 v[138:141], v[192:193], v[156:157], v[138:141]\n"
      "v_cvt_pk_bf16_f32 v96, v96, v97\n"
      "v_exp_f32 v98, v98\n"
      "v_mfma_f32_16x16x16_bf16 v[118:121], v[192:193], v[180:181], v[118:121]\n"
      "v_exp_f32 v99, v99\n"
      "v_cvt_pk_bf16_f32 v89, v222, v223\n"
      "v_mfma_f32_16x16x16_bf16 v[174:177], v[192:193], v[158:159], v[174:177]\n"
      "ds_read_b128 v[148:151], v2 offset:9472\n"
      "v_sub_f32_e64 v109, v109, v78\n"
      "v_mfma_f32_16x16x16_bf16 v[144:147], v[124:125], v[182:183], v[144:147]\n"
      "ds_read_b128 v[220:223], v2 offset:12544\n"
      "v_exp_f32 v109, v109\n"
      "v_mfma_f32_16x16x16_bf16 v[138:141], v[124:125], v[156:157], v[138:141]\n"
      "v_exp_f32 v108, v108\n"
      "v_exp_f32_e64 v111, v111\n"
      "v_mfma_f32_16x16x16_bf16 v[118:121], v[124:125], v[180:181], v[118:121]\n"
      "ds_read_b128 v[100:103], v2 offset:15616\n"
      "v_cvt_pk_bf16_f32 v97, v98, v99\n"
      "v_cvt_pk_bf16_f32 v99, v110, v111\n"
      "v_mfma_f32_16x16x16_bf16 v[174:177], v[124:125], v[158:159], v[174:177]\n"
      "v_exp_f32_e64 v178, v178\n"
      "v_max3_f32 v78, v147, v138, v139\n"
      "v_max3_f32 v111, v144, v145, v146\n"
      "v_max3_f32 v111, v111, v140, v141\n"
      "v_max3_f32 v111, v111, v120, v121\n"
      "v_pk_fma_f32 v[114:115], v[168:169], v[168:169], v[114:115]\n"
      "v_max3_f32 v78, v78, v118, v119\n"
      "v_pk_fma_f32 v[114:115], v[170:171], v[170:171], v[114:115]\n"
      "v_max3_f32 v111, v111, v78, v174\n"
      "v_cvt_pk_bf16_f32 v90, v178, 0\n"
      "v_mov_b32 v110, v111\n"
      "s_waitcnt lgkmcnt(1)\n"
      "v_mfma_f32_16x16x32_bf16 v[160:163], a[228:231], v[148:151], v[160:163]\n"
      "ds_read_u16 v78, v11 offset:1168\n"
      "v_permlane16_swap_b32 v111, v110\n"
      "v_mfma_f32_16x16x32_bf16 v[160:163], a[232:235], v[220:223], v[160:163]\n"
      "s_waitcnt lgkmcnt(1)\n"
      "v_max_f32 v111, v111, v110\n"
      "v_mov_b32 v110, v111\n"
      "v_mfma_f32_16x16x32_bf16 v[160:163], a[236:239], v[100:103], v[160:163]\n"
      "ds_read_u16 v86, v11 offset:1232\n"
      "v_cvt_pk_bf16_f32 v98, v108, v109\n"
      "v_mfma_f32_16x16x32_bf16 v[164:167], a[252:255], v[148:151], v[164:167]\n"
      "ds_read_u16 v82, v11 offset:1200\n"
      "v_permlane32_swap_b32 v111, v110\n"
      "v_max_f32 v111, v111, v110\n"
      "v_sub_f32_e64 v174, v174, v111\n"
      "v_pk_add_f32 v[120:121], v[120:121], v[110:111] op_sel:[0,1] neg_lo:[0,1] neg_hi:[0,1]\n"
      "v_pk_add_f32 v[144:145], v[144:145], v[110:111] op_sel:[0,1] neg_lo:[0,1] neg_hi:[0,1]\n"
      "v_pk_add_f32 v[138:139], v[138:139], v[110:111] op_sel:[0,1] neg_lo:[0,1] neg_hi:[0,1]\n"
      "v_pk_add_f32 v[118:119], v[118:119], v[110:111] op_sel:[0,1] neg_lo:[0,1] neg_hi:[0,1]\n"
      "v_pk_add_f32 v[146:147], v[146:147], v[110:111] op_sel:[0,1] neg_lo:[0,1] neg_hi:[0,1]\n"
      "v_pk_add_f32 v[140:141], v[140:141], v[110:111] op_sel:[0,1] neg_lo:[0,1] neg_hi:[0,1]\n"
      "v_mfma_f32_16x16x32_bf16 v[164:167], v[224:227], v[220:223], v[164:167]\n"
      "ds_read2_b32 v[108:109], v190 offset0:0 offset1:1\n"
      "v_add_f32 v114, v114, v115\n"
      "v_mfma_f32_16x16x32_bf16 v[164:167], v[228:231], v[100:103], v[164:167]\n"
      "v_exp_f32 v144, v144\n"
      "v_mfma_f32_16x16x32_bf16 v[134:137], v[244:247], v[148:151], v[134:137]\n"
      "v_exp_f32_e64 v147, v147\n"
      "v_mfma_f32_16x16x32_bf16 v[134:137], v[248:251], v[220:223], v[134:137]\n"
      "ds_read2_b32 v[112:113], v190 offset0:8 offset1:9\n"
      "v_exp_f32_e64 v138, v138\n"
      "v_mfma_f32_16x16x32_bf16 v[134:137], v[252:255], v[100:103], v[134:137]\n"
      "ds_read2_b32 v[222:223], v190 offset0:2 offset1:10\n"
      "v_exp_f32_e64 v145, v145\n"
      "v_mfma_f32_16x16x32_bf16 v[148:151], v[32:35], v[96:99], 0\n"
      "ds_read2_b32 v[172:173], v190 offset0:16 offset1:17\n"
      "v_cvt_pk_bf16_f32 v144, v144, v145\n"
      "v_mfma_f32_16x16x32_bf16 v[148:151], v[32:35], v[88:91], v[148:151]\n"
      "ds_read2_b32 v[110:111], v190 offset0:24 offset1:18\n"
      "v_exp_f32_e64 v146, v146\n"
      "v_mfma_f32_16x16x32_bf16 v[180:183], v[214:217], v[96:99], 0\n"
      "s_waitcnt lgkmcnt(4)\n"
      "v_exp_f32 v141, v141\n"
      "v_mfma_f32_16x16x32_bf16 v[180:183], v[76:79], v[88:91], v[180:183]\n"
      "v_alignbyte_b32 v108, v109, v108, v93\n"
      "v_cvt_pk_bf16_f32 v145, v146, v147\n"
      "v_mfma_f32_16x16x32_bf16 v[198:201], v[130:133], v[96:99], 0\n"
      "v_exp_f32 v139, v139\n"
      "v_mfma_f32_16x16x32_bf16 v[198:201], v[80:83], v[88:91], v[198:201]\n"
      "s_waitcnt lgkmcnt(2)\n"
      "v_alignbyte_b32 v109, v222, v109, v93\n"
      "v_cvt_pk_bf16_f32 v146, v138, v139\n"
      "v_mfma_f32_16x16x32_bf16 v[156:159], v[194:197], v[96:99], 0\n"
      "s_waitcnt lgkmcnt(1)\n"
      "v_exp_f32 v140, v140\n"
      "v_mfma_f32_16x16x32_bf16 v[156:159], v[84:87], v[88:91], v[156:159]\n"
      "v_alignbyte_b32 v172, v173, v172, v93\n"
      "v_alignbyte_b32 v112, v113, v112, v93\n"
      "v_mfma_f32_16x16x32_bf16 v[100:103], v[126:129], v[210:213], v[64:67]\n"
      "s_waitcnt lgkmcnt(0)\n"
      "v_cvt_pk_bf16_f32 v147, v140, v141\n"
      "v_alignbyte_b32 v110, v110, v110, v93\n"
      "v_mfma_f32_16x16x32_bf16 v[138:141], v[206:209], v[210:213], v[68:71]\n"
      "v_alignbyte_b32 v173, v111, v173, v93\n"
      "v_alignbyte_b32 v113, v223, v113, v93\n"
      "v_mfma_f32_16x16x32_bf16 v[220:223], v[202:205], v[210:213], v[72:75]\n"
      "v_exp_f32 v120, v120\n"
      "v_mfma_f32_16x16x32_bf16 v[176:179], v[104:107], v[210:213], v[28:31]\n"
      "v_rcp_f32 v148, v148\n"
      "v_mfma_f32_16x16x16_bf16 v[100:103], v[122:123], v[116:117], v[100:103]\n"
      "v_exp_f32 v119, v119\n"
      "v_mul_f32 v199, v148, v199\n"
      "v_mfma_f32_16x16x16_bf16 v[138:141], v[142:143], v[116:117], v[138:141]\n"
      "v_exp_f32 v121, v121\n"
      "v_mul_f32 v200, v148, v200\n"
      "v_mfma_f32_16x16x16_bf16 v[220:223], v[188:189], v[116:117], v[220:223]\n"
      "v_mul_f32 v183, v148, v183\n"
      "v_mul_f32 v180, v148, v180\n"
      "v_mul_f32 v198, v148, v198\n"
      "v_mfma_f32_16x16x16_bf16 v[176:179], v[218:219], v[116:117], v[176:179]\n"
      "v_exp_f32 v118, v118\n"
      "v_cvt_pk_bf16_f32 v93, v120, v121\n"
      "v_mfma_f32_16x16x16_bf16 v[100:103], v[192:193], v[108:109], v[100:103]\n"
      "v_mul_f32 v156, v148, v156\n"
      "v_exp_f32_e64 v174, v174\n"
      "v_mfma_f32_16x16x16_bf16 v[138:141], v[192:193], v[112:113], v[138:141]\n"
      "v_mul_f32 v182, v148, v182\n"
      "v_mul_f32 v181, v148, v181\n"
      "v_cvt_pk_bf16_f32 v180, v180, v181\n"
      "v_mfma_f32_16x16x16_bf16 v[220:223], v[192:193], v[172:173], v[220:223]\n"
      "v_cvt_pk_bf16_f32 v181, v182, v183\n"
      "v_mul_f32_e64 v157, v148, v157\n"
      "v_cvt_pk_bf16_f32 v182, v198, v199\n"
      "v_mfma_f32_16x16x16_bf16 v[176:179], v[192:193], v[110:111], v[176:179]\n"
      "ds_read_b128 v[190:193], v2 offset:512\n"
      "v_mul_f32_e64 v201, v148, v201\n"
      "v_mfma_f32_16x16x16_bf16 v[100:103], v[124:125], v[108:109], v[100:103]\n"
      "ds_read_b128 v[210:213], v2 offset:3584\n"
      "v_cvt_pk_bf16_f32 v183, v200, v201\n"
      "v_mfma_f32_16x16x16_bf16 v[138:141], v[124:125], v[112:113], v[138:141]\n"
      "v_cvt_pk_bf16_f32 v94, v174, 0\n"
      "v_fma_f32 v112, v160, v160, s52\n"
      "v_mul_f32_e64 v113, v161, v161\n"
      "v_mfma_f32_16x16x16_bf16 v[220:223], v[124:125], v[172:173], v[220:223]\n"
      "ds_read_b128 v[172:175], v2 offset:6656\n"
      "v_max3_f32 v121, v100, v101, v102\n"
      "v_mfma_f32_16x16x16_bf16 v[176:179], v[124:125], v[110:111], v[176:179]\n"
      "v_fmac_f32 v113, v163, v163\n"
      "v_cvt_pk_bf16_f32 v156, v156, v157\n"
      "v_fmac_f32 v112, v162, v162\n"
      "v_max3_f32 v121, v121, v140, v141\n"
      "v_max3_f32 v111, v103, v138, v139\n"
      "v_max3_f32 v111, v111, v220, v221\n"
      "v_pk_fma_f32 v[112:113], v[164:165], v[164:165], v[112:113]\n"
      "v_max3_f32 v121, v121, v222, v223\n"
      "v_max3_f32 v121, v121, v111, v176\n"
      "v_pk_mul_f32 v[158:159], v[158:159], v[148:149] op_sel_hi:[1,0]\n"
      "v_pk_fma_f32 v[112:113], v[166:167], v[166:167], v[112:113]\n"
      "v_mov_b32 v116, v121\n"
      "s_waitcnt lgkmcnt(1)\n"
      "v_mfma_f32_16x16x32_bf16 v[108:111], a[216:219], v[190:193], 0\n"
      "v_permlane16_swap_b32 v121, v116\n"
      "v_max_f32 v121, v121, v116\n"
      "v_mov_b32_e64 v116, v121\n"
      "v_cvt_pk_bf16_f32 v92, v118, v119\n"
      "v_pk_fma_f32 v[112:113], v[134:135], v[134:135], v[112:113]\n"
      "v_permlane32_swap_b32 v121, v116\n"
      "v_max_f32 v121, v121, v116\n"
      "v_pk_fma_f32 v[112:113], v[136:137], v[136:137], v[112:113]\n"
      "v_pk_add_f32 v[102:103], v[102:103], v[120:121] op_sel:[0,1] neg_lo:[0,1] neg_hi:[0,1]\n"
      "v_pk_add_f32 v[100:101], v[100:101], v[120:121] op_sel:[0,1] neg_lo:[0,1] neg_hi:[0,1]\n"
      "v_mfma_f32_16x16x32_bf16 v[108:111], a[220:223], v[210:213], v[108:111]\n"
      "s_waitcnt lgkmcnt(0)\n"
      "v_exp_f32 v103, v103\n"
      "v_mfma_f32_16x16x32_bf16 v[108:111], a[224:227], v[172:175], v[108:111]\n"
      "ds_write_b128 v3, v[180:183] offset:18432\n"
      "v_pk_add_f32 v[138:139], v[138:139], v[120:121] op_sel:[0,1] neg_lo:[0,1] neg_hi:[0,1]\n"
      "v_pk_add_f32 v[222:223], v[222:223], v[120:121] op_sel:[0,1] neg_lo:[0,1] neg_hi:[0,1]\n"
      "v_pk_add_f32 v[220:221], v[220:221], v[120:121] op_sel:[0,1] neg_lo:[0,1] neg_hi:[0,1]\n"
      "v_pk_add_f32 v[140:141], v[140:141], v[120:121] op_sel:[0,1] neg_lo:[0,1] neg_hi:[0,1]\n"
      "v_mfma_f32_16x16x32_bf16 v[116:119], a[240:243], v[190:193], 0\n"
      "v_exp_f32 v100, v100\n"
      "v_mfma_f32_16x16x32_bf16 v[116:119], a[244:247], v[210:213], v[116:119]\n"
      "v_exp_f32 v221, v221\n"
      "v_mfma_f32_16x16x32_bf16 v[116:119], a[248:251], v[172:175], v[116:119]\n"
      "v_exp_f32 v101, v101\n"
      "v_mfma_f32_16x16x32_bf16 v[96:99], v[232:235], v[190:193], 0\n"
      "v_exp_f32 v102, v102\n"
      "v_mfma_f32_16x16x32_bf16 v[96:99], v[236:239], v[210:213], v[96:99]\n"
      "v_cvt_pk_bf16_f32 v100, v100, v101\n"
      "v_cvt_pk_bf16_f32 v157, v158, v159\n"
      "v_mfma_f32_16x16x32_bf16 v[96:99], v[240:243], v[172:175], v[96:99]\n"
      "ds_read_b128 v[210:213], v10 offset:1168\n"
      "v_sub_f32 v176, v176, v121\n"
      "v_mfma_f32_16x16x32_bf16 v[178:181], v[32:35], v[144:147], 0\n"
      "v_exp_f32 v220, v220\n"
      "v_mfma_f32_16x16x32_bf16 v[178:181], v[32:35], v[92:95], v[178:181]\n"
      "v_exp_f32_e64 v140, v140\n"
      "v_mfma_f32_16x16x32_bf16 v[172:175], v[214:217], v[144:147], 0\n"
      "ds_read_b64 v[120:121], v10 offset:1184\n"
      "v_cvt_pk_bf16_f32 v101, v102, v103\n"
      "v_mfma_f32_16x16x32_bf16 v[172:175], v[76:79], v[92:95], v[172:175]\n"
      "ds_write_b64 v4, v[156:157] offset:18432\n"
      "v_mfma_f32_16x16x32_bf16 v[148:151], v[130:133], v[144:147], 0\n"
      "v_exp_f32 v138, v138\n"
      "v_mfma_f32_16x16x32_bf16 v[148:151], v[80:83], v[92:95], v[148:151]\n"
      "v_add_f32 v112, v112, v113\n"
      "v_cvt_pk_bf16_f32 v88, v220, v221\n"
      "v_mfma_f32_16x16x32_bf16 v[180:183], v[194:197], v[144:147], 0\n"
      "s_waitcnt lgkmcnt(2)\n"
      "v_exp_f32 v141, v141\n"
      "v_mfma_f32_16x16x32_bf16 v[180:183], v[84:87], v[92:95], v[180:183]\n"
      "ds_read_b128 v[190:193], v2 offset:9728\n"
      "v_cvt_pk_bf16_f32 v103, v140, v141\n"
      "v_mfma_f32_16x16x32_bf16 v[198:201], v[210:213], v[126:129], v[30:33]\n"
      "v_rcp_f32_e64 v178, v178\n"
      "v_mfma_f32_16x16x32_bf16 v[144:147], v[210:213], v[206:209], v[30:33]\n"
      "s_waitcnt lgkmcnt(2)\n"
      "v_exp_f32 v139, v139\n"
      "v_mfma_f32_16x16x32_bf16 v[156:159], v[210:213], v[202:205], v[30:33]\n"
      "ds_read_b128 v[206:209], v2 offset:12800\n"
      "v_cvt_pk_bf16_f32 v102, v138, v139\n"
      "v_mfma_f32_16x16x32_bf16 v[124:127], v[210:213], v[104:107], v[30:33]\n"
      "ds_read_b128 v[210:213], v2 offset:15872\n"
      "v_mul_f32_e64 v151, v178, v151\n"
      "v_mfma_f32_16x16x16_bf16 v[198:201], v[120:121], v[122:123], v[198:201]\n"
      "v_mul_f32 v149, v178, v149\n"
      "v_permlane16_swap_b32 v114, v112\n"
      "v_mfma_f32_16x16x16_bf16 v[144:147], v[120:121], v[142:143], v[144:147]\n"
      "v_exp_f32 v223, v223\n"
      "v_mul_f32 v174, v178, v174\n"
      "v_mfma_f32_16x16x16_bf16 v[156:159], v[120:121], v[188:189], v[156:159]\n"
      "v_exp_f32_e64 v176, v176\n"
      "v_mul_f32_e64 v175, v178, v175\n"
      "v_mfma_f32_16x16x16_bf16 v[124:127], v[120:121], v[218:219], v[124:127]\n"
      "v_exp_f32_e64 v222, v222\n"
      "s_waitcnt lgkmcnt(2)\n"
      "v_mfma_f32_16x16x32_bf16 v[108:111], a[228:231], v[190:193], v[108:111]\n"
      "s_waitcnt lgkmcnt(1)\n"
      "v_cndmask_b32 v198, v198, v144, s[58:59]\n"
      "v_cvt_pk_bf16_f32 v90, v176, 0\n"
      "v_mfma_f32_16x16x32_bf16 v[108:111], a[232:235], v[206:209], v[108:111]\n"
      "s_waitcnt lgkmcnt(0)\n"
      "v_mul_f32 v172, v178, v172\n"
      "v_mul_f32_e64 v148, v178, v148\n"
      "v_mfma_f32_16x16x32_bf16 v[108:111], a[236:239], v[210:213], v[108:111]\n"
      "v_cndmask_b32 v198, v198, v156, s[60:61]\n"
      "v_cndmask_b32 v198, v198, v124, s[62:63]\n"
      "v_mfma_f32_16x16x32_bf16 v[116:119], a[252:255], v[190:193], v[116:119]\n"
      "v_mul_f32 v173, v178, v173\n"
      "v_fmac_f32_e64 v198, s53, v39\n"
      "v_mfma_f32_16x16x32_bf16 v[116:119], v[224:227], v[206:209], v[116:119]\n"
      "s_nop 0\n"
      "v_max_f32_dpp v205, v198, v198 row_ror:8 row_mask:0xf bank_mask:0xf\n"
      "v_cvt_pk_bf16_f32 v172, v172, v173\n"
      "v_mfma_f32_16x16x32_bf16 v[116:119], v[228:231], v[210:213], v[116:119]\n"
      "v_max_f32_dpp v205, v205, v205 row_ror:4 row_mask:0xf bank_mask:0xf\n"
      "v_mul_f32_e64 v150, v178, v150\n"
      "v_mfma_f32_16x16x32_bf16 v[96:99], v[244:247], v[190:193], v[96:99]\n"
      "v_cvt_pk_bf16_f32 v173, v174, v175\n"
      "v_max_f32_dpp v205, v205, v205 row_ror:2 row_mask:0xf bank_mask:0xf\n"
      "v_mfma_f32_16x16x32_bf16 v[96:99], v[248:251], v[206:209], v[96:99]\n"
      "s_nop 0\n"
      "v_max_f32_dpp v205, v205, v205 row_ror:1 row_mask:0xf bank_mask:0xf\n"
      "v_cvt_pk_bf16_f32 v89, v222, v223\n"
      "v_mfma_f32_16x16x32_bf16 v[96:99], v[252:255], v[210:213], v[96:99]\n"
      "v_mul_f32 v223, v109, v109\n"
      "v_max_f32_dpp v205, v205, v205 row_bcast:15 row_mask:0xa bank_mask:0xf\n"
      "v_mfma_f32_16x16x32_bf16 v[188:191], v[32:35], v[100:103], 0\n"
      "v_cvt_pk_bf16_f32 v174, v148, v149\n"
      "v_cvt_pk_bf16_f32 v175, v150, v151\n"
      "v_mfma_f32_16x16x32_bf16 v[188:191], v[32:35], v[88:91], v[188:191]\n"
      "v_max_f32_dpp v205, v205, v205 row_bcast:31 row_mask:0xc bank_mask:0xf\n"
      "v_add_f32_e64 v114, v114, v112\n"
      "v_mfma_f32_16x16x32_bf16 v[120:123], v[214:217], v[100:103], 0\n"
      "v_readlane_b32 s72, v205, 63\n"
      "v_fma_f32 v222, v108, v108, s52\n"
      "v_mfma_f32_16x16x32_bf16 v[120:123], v[76:79], v[88:91], v[120:123]\n"
      "ds_write_b128 v3, v[172:175] offset:18688\n"
      "v_pk_fma_f32 v[222:223], v[110:111], v[110:111], v[222:223]\n"
      "v_mfma_f32_16x16x32_bf16 v[148:151], v[130:133], v[100:103], 0\n"
      "v_rcp_f32 v188, v188\n"
      "v_subrev_f32 v198, s72, v198\n"
      "v_exp_f32_e64 v198, v198\n"
      "v_pk_fma_f32 v[222:223], v[116:117], v[116:117], v[222:223]\n"
      "v_pk_fma_f32 v[222:223], v[118:119], v[118:119], v[222:223]\n"
      "v_pk_mul_f32 v[120:121], v[120:121], v[188:189] op_sel_hi:[1,0]\n"
      "v_pk_mul_f32 v[122:123], v[122:123], v[188:189] op_sel_hi:[1,0]\n"
      "v_cvt_pk_bf16_f32 v198, v198, v198\n"
      "v_mfma_f32_16x16x32_bf16 v[148:151], v[80:83], v[88:91], v[148:151]\n"
      "ds_write_b16 v13, v198\n"
      "v_mfma_f32_16x16x32_bf16 v[202:205], v[194:197], v[100:103], 0\n"
      "ds_read_b128 v[210:213], v14\n"
      "ds_read_b128 v[172:175], v14 offset:64\n"
      "v_cvt_pk_bf16_f32 v120, v120, v121\n"
      "v_cvt_pk_bf16_f32 v121, v122, v123\n"
      "v_pk_mul_f32 v[180:181], v[180:181], v[178:179] op_sel_hi:[1,0]\n"
      "v_pk_fma_f32 v[222:223], v[96:97], v[96:97], v[222:223]\n"
      "v_pk_mul_f32 v[148:149], v[148:149], v[188:189] op_sel_hi:[1,0]\n"
      "v_pk_mul_f32 v[150:151], v[150:151], v[188:189] op_sel_hi:[1,0]\n"
      "v_cvt_pk_bf16_f32 v123, v150, v151\n"
      "v_cvt_pk_bf16_f32 v122, v148, v149\n"
      "v_cvt_pk_bf16_f32 v180, v180, v181\n"
      "v_mfma_f32_16x16x32_bf16 v[202:205], v[84:87], v[88:91], v[202:205]\n"
      "ds_write_b128 v3, v[120:123] offset:18944\n"
      "v_pk_fma_f32 v[222:223], v[98:99], v[98:99], v[222:223]\n"
      "v_add_f32 v222, v222, v223\n"
      "v_mov_b32 v112, v222\n"
      "v_pk_mul_f32 v[182:183], v[182:183], v[178:179] op_sel_hi:[1,0]\n"
      "v_cvt_pk_bf16_f32 v181, v182, v183\n"
      "v_permlane16_swap_b32 v222, v112\n"
      "v_add_f32 v222, v222, v112\n"
      "s_add_u32 s42, s42, 1\n"
      "ds_write_b64 v4, v[180:181] offset:18688\n"
      "v_permlane32_swap_b32 v114, v222\n"
      "v_pk_mul_f32 v[202:203], v[202:203], v[188:189] op_sel_hi:[1,0]\n"
      "v_add_f32_e64 v114, v114, v222\n"
      "v_pk_mul_f32 v[204:205], v[204:205], v[188:189] op_sel_hi:[1,0]\n"
      "s_add_u32 s43, s43, 1\n"
      "ds_write_b32 v31, v114 offset:1024\n"
      "v_cvt_pk_bf16_f32 v202, v202, v203\n"
      "v_cvt_pk_bf16_f32 v203, v204, v205\n"
      "s_waitcnt lgkmcnt(2)\n"
      "v_mfma_f32_16x16x32_bf16 v[188:191], v[210:213], v[32:35], 0\n"
      "ds_write_b64 v4, v[202:203] offset:18944\n"
      "v_add_u32 v11, 2336, v11\n"
      "v_mfma_f32_16x16x32_bf16 v[188:191], v[172:175], v[32:35], v[188:191]\n"
      "v_add_u32 v10, 2336, v10\n"
      "s_waitcnt lgkmcnt(0)\n"
      "s_barrier\n"
      "s_cbranch_vccz L_sel0_%=\n"
      "L_disp0_%=:\n"
      "s_cmp_lt_u32 s95, 0x40000000\n"
      "s_cbranch_scc1 L_nofix_18_%=\n"
      "s_cmp_eq_u32 s95, -2\n"
      "s_cbranch_scc1 L_tail0_%=\n"
      "s_nop 2\n"
      "s_cmp_eq_u32 s45, 0x20000000\n"
      "s_cselect_b32 s66, 0, s66\n"
      "s_cselect_b32 s48, 0, s48\n"
      "s_lshr_b32 s92, s45, 30\n"
      "s_or_b32 s92, s92, s66\n"
      "s_cbranch_scc0 L_nf_19_%=\n"
      "s_bitcmp1_b32 s45, 31\n"
      "s_cselect_b32 s75, 100270464, 0\n"
      "s_bitcmp1_b32 s45, 30\n"
      "s_cselect_b32 s76, 196224, 0\n"
      "s_and_b32 s74, s45, 0x3fffffff\n"
      "s_cmp_ge_u32 s45, 0x40000000\n"
      "s_cselect_b32 s74, s74, 0\n"
      "s_cselect_b32 s45, 0, s45\n"
      "s_sub_u32 s74, s74, s66\n"
      "s_add_u32 s66, s66, s74\n"
      "s_sub_u32 s75, s75, s67\n"
      "s_add_u32 s67, s67, s75\n"
      "s_sub_u32 s76, s76, s68\n"
      "s_add_u32 s68, s68, s76\n"
      "v_add_u32 v16, s74, v16\n"
      "v_add_u32 v17, s74, v17\n"
      "v_add_u32 v18, s74, v18\n"
      "v_add_u32 v19, s74, v19\n"
      "v_add_u32_e64 v15, s74, v15\n"
      "s_cmp_eq_u32 s75, 0\n"
      "s_cbranch_scc1 L_ax_20_%=\n"
      "v_bfe_i32 v203, v27, 0, 1\n"
      "v_and_b32 v203, s75, v203\n"
      "v_sub_u32 v16, v16, v203\n"
      "v_bfe_i32 v203, v27, 2, 1\n"
      "v_and_b32 v203, s75, v203\n"
      "v_sub_u32 v17, v17, v203\n"
      "v_bfe_i32 v203, v27, 4, 1\n"
      "v_and_b32 v203, s75, v203\n"
      "v_sub_u32 v18, v18, v203\n"
      "v_bfe_i32 v203, v27, 6, 1\n"
      "v_and_b32 v203, s75, v203\n"
      "v_sub_u32 v19, v19, v203\n"
      "v_bfe_i32 v203, v27, 8, 1\n"
      "v_and_b32 v203, s75, v203\n"
      "v_sub_u32 v15, v15, v203\n"
      "L_ax_20_%=:\n"
      "s_cmp_eq_u32 s76, 0\n"
      "s_cbranch_scc1 L_ax_21_%=\n"
      "v_bfe_i32 v203, v27, 1, 1\n"
      "v_and_b32 v203, s76, v203\n"
      "v_sub_u32 v16, v16, v203\n"
      "v_bfe_i32 v203, v27, 3, 1\n"
      "v_and_b32 v203, s76, v203\n"
      "v_sub_u32 v17, v17, v203\n"
      "v_bfe_i32 v203, v27, 5, 1\n"
      "v_and_b32 v203, s76, v203\n"
      "v_sub_u32 v18, v18, v203\n"
      "v_bfe_i32 v203, v27, 7, 1\n"
      "v_and_b32 v203, s76, v203\n"
      "v_sub_u32 v19, v19, v203\n"
      "v_bfe_i32 v203, v27, 9, 1\n"
      "v_and_b32 v203, s76, v203\n"
      "v_sub_u32 v15, v15, v203\n"
      "L_ax_21_%=:\n"
      "s_or_b32 s48, s66, s69\n"
      "s_cmp_lg_u32 s48, 0\n"
      "s_cselect_b32 s48, 0x40000000, 0\n"
      "L_nf_19_%=:\n"
      "s_lshr_b32 s92, s46, 30\n"
      "s_or_b32 s92, s92, s69\n"
      "s_cbranch_scc0 L_nf_22_%=\n"
      "s_bitcmp1_b32 s46, 31\n"
      "s_cselect_b32 s75, 100270464, 0\n"
      "s_bitcmp1_b32 s46, 30\n"
      "s_cselect_b32 s76, 196224, 0\n"
      "s_and_b32 s74, s46, 0x3fffffff\n"
      "s_cmp_ge_u32 s46, 0x40000000\n"
      "s_cselect_b32 s74, s74, 0\n"
      "s_cselect_b32 s46, 0, s46\n"
      "s_sub_u32 s74, s74, s69\n"
      "s_add_u32 s69, s69, s74\n"
      "s_sub_u32 s75, s75, s70\n"
      "s_add_u32 s70, s70, s75\n"
      "s_sub_u32 s76, s76, s71\n"
      "s_add_u32 s71, s71, s76\n"
      "v_add_u32 v20, s74, v20\n"
      "v_add_u32 v21, s74, v21\n"
      "v_add_u32 v22, s74, v22\n"
      "v_add_u32 v23, s74, v23\n"
      "v_add_u32 v25, s74, v25\n"
      "s_cmp_eq_u32 s75, 0\n"
      "s_cbranch_scc1 L_ax_23_%=\n"
      "v_bfe_i32 v203, v27, 10, 1\n"
      "v_and_b32 v203, s75, v203\n"
      "v_sub_u32 v20, v20, v203\n"
      "v_bfe_i32 v203, v27, 12, 1\n"
      "v_and_b32 v203, s75, v203\n"
      "v_sub_u32 v21, v21, v203\n"
      "v_bfe_i32 v203, v27, 14, 1\n"
      "v_and_b32 v203, s75, v203\n"
      "v_sub_u32 v22, v22, v203\n"
      "v_bfe_i32 v203, v27, 16, 1\n"
      "v_and_b32 v203, s75, v203\n"
      "v_sub_u32 v23, v23, v203\n"
      "v_bfe_i32 v203, v27, 18, 1\n"
      "v_and_b32 v203, s75, v203\n"
      "v_sub_u32 v25, v25, v203\n"
      "L_ax_23_%=:\n"
      "s_cmp_eq_u32 s76, 0\n"
      "s_cbranch_scc1 L_ax_24_%=\n"
      "v_bfe_i32 v203, v27, 11, 1\n"
      "v_and_b32 v203, s76, v203\n"
      "v_sub_u32 v20, v20, v203\n"
      "v_bfe_i32 v203, v27, 13, 1\n"
      "v_and_b32 v203, s76, v203\n"
      "v_sub_u32 v21, v21, v203\n"
      "v_bfe_i32 v203, v27, 15, 1\n"
      "v_and_b32 v203, s76, v203\n"
      "v_sub_u32 v22, v22, v203\n"
      "v_bfe_i32 v203, v27, 17, 1\n"
      "v_and_b32 v203, s76, v203\n"
      "v_sub_u32 v23, v23, v203\n"
      "v_bfe_i32 v203, v27, 19, 1\n"
      "v_and_b32 v203, s76, v203\n"
      "v_sub_u32 v25, v25, v203\n"
      "L_ax_24_%=:\n"
      "s_or_b32 s48, s66, s69\n"
      "s_cmp_lg_u32 s48, 0\n"
      "s_cselect_b32 s48, 0x40000000, 0\n"
      "L_nf_22_%=:\n"
      "s_and_b32 s4, s45, s8\n"
      "s_sub_u32 s92, s42, 1\n"
      "s_cmp_gt_u32 s92, s44\n"
      "s_cbranch_scc1 L_tail0_%=\n"
      "L_nofix_18_%=:\n"
      "s_cmp_lg_u64 s[56:57], 0\n"
      "s_cbranch_scc1 L_slow0_%=\n"
      "L_sel0_%=:\n"
      "s_cmp_lg_u64 s[54:55], 0\n"
      "s_cbranch_scc1 L_max0_%=\n"
      "s_branch L_loop0_%=\n"
      "L_slow0_%=:\n"
      "s_nop 4\n"
      "v_mfma_f32_16x16x32_bf16 v[120:123], v[210:213], v[214:217], 0\n"
      "ds_read_b128 v[202:205], v0\n"
      "v_subrev_u32 v214, 115456, v9\n"
      "v_mfma_f32_16x16x32_bf16 v[120:123], v[172:175], v[76:79], v[120:123]\n"
      "s_mov_b32 m0, 110592\n"
      "ds_read_b128 v[216:219], v9 offset:1024\n"
      "v_mfma_f32_16x16x32_bf16 v[148:151], v[210:213], v[130:133], 0\n"
      "ds_read_b128 v[220:223], v1\n"
      "s_add_u32 s72, s42, 1\n"
      "v_mfma_f32_16x16x32_bf16 v[148:151], v[172:175], v[80:83], v[148:151]\n"
      "ds_read_b128 v[104:107], v0 offset:6144\n"
      "v_readlane_b32 s77, v38, s72\n"
      "v_mfma_f32_16x16x32_bf16 v[178:181], v[210:213], v[194:197], 0\n"
      "ds_read_b128 v[210:213], v1 offset:6144\n"
      "s_and_b32 s73, s77, -16\n"
      "v_mfma_f32_16x16x32_bf16 v[178:181], v[172:175], v[84:87], v[178:181]\n"
      "buffer_load_dwordx4 v214, s[28:31], s73 offen  lds\n"
      "s_waitcnt lgkmcnt(4)\n"
      "v_mfma_f32_16x16x32_bf16 v[172:175], a[0:3], v[202:205], 0\n"
      "ds_read_b128 v[122:125], v1 offset:8192\n"
      "v_subrev_u32 v142, 115456, v9\n"
      "v_mfma_f32_16x16x32_bf16 v[112:115], a[24:27], v[202:205], 0\n"
      "s_waitcnt lgkmcnt(4)\n"
      "v_readlane_b32 s84, v37, s43\n"
      "v_add_f32 v218, v218, v219\n"
      "v_mfma_f32_16x16x32_bf16 v[180:183], a[48:51], v[202:205], 0\n"
      "s_add_u32 s73, s73, 1024\n"
      "v_add_f32_e64 v216, v216, v217\n"
      "v_add_f32_e64 v216, v216, v218\n"
      "v_mfma_f32_16x16x32_bf16 v[100:103], a[72:75], v[202:205], 0\n"
      "s_mov_b32 m0, 111616\n"
      "v_rsq_f32 v216, v216\n"
      "v_mfma_f32_16x16x32_bf16 v[206:209], a[96:99], v[202:205], 0\n"
      "buffer_load_dwordx4 v214, s[28:31], s73 offen  lds\n"
      "v_mfma_f32_16x16x32_bf16 v[144:147], a[120:123], v[202:205], 0\n"
      "s_waitcnt lgkmcnt(1)\n"
      "v_mul_f32 v216, 0x415db3d7, v216\n"
      "v_subrev_u32 v219, 115456, v9\n"
      "v_mfma_f32_16x16x32_bf16 v[172:175], a[4:7], v[220:223], v[172:175]\n"
      "ds_read_b128 v[126:129], v1 offset:12288\n"
      "v_mov_b32 v218, v216\n"
      "v_mfma_f32_16x16x32_bf16 v[172:175], a[8:11], v[104:107], v[172:175]\n"
      "s_mov_b32 m0, 112640\n"
      "v_permlane32_swap_b32 v216, v218\n"
      "v_mfma_f32_16x16x32_bf16 v[112:115], a[28:31], v[220:223], v[112:115]\n"
      "s_add_u32 s73, s73, 1024\n"
      "v_mov_b32 v217, v216\n"
      "v_mul_f32 v118, v218, v118\n"
      "v_mfma_f32_16x16x32_bf16 v[112:115], a[32:35], v[104:107], v[112:115]\n"
      "buffer_load_dwordx4 v214, s[28:31], s73 offen  lds\n"
      "v_mfma_f32_16x16x32_bf16 v[180:183], a[52:55], v[220:223], v[180:183]\n"
      "ds_read_b128 v[198:201], v0 offset:12288\n"
      "v_mul_f32_e64 v109, v218, v109\n"
      "v_mfma_f32_16x16x32_bf16 v[180:183], a[56:59], v[104:107], v[180:183]\n"
      "s_add_u32 s73, s73, 1024\n"
      "v_permlane16_swap_b32 v216, v217\n"
      "v_mfma_f32_16x16x32_bf16 v[100:103], a[76:79], v[220:223], v[100:103]\n"
      "s_mov_b32 m0, 113664\n"
      "v_mul_f32 v135, v217, v135\n"
      "v_mul_f32 v171, v216, v171\n"
      "v_mfma_f32_16x16x32_bf16 v[100:103], a[80:83], v[104:107], v[100:103]\n"
      "v_mul_f32 v187, v216, v187\n"
      "v_mul_f32 v134, v217, v134\n"
      "v_mfma_f32_16x16x32_bf16 v[206:209], a[100:103], v[220:223], v[206:209]\n"
      "v_mul_f32 v152, v216, v152\n"
      "v_mul_f32 v162, v217, v162\n"
      "v_mfma_f32_16x16x32_bf16 v[206:209], a[104:107], v[104:107], v[206:209]\n"
      "ds_read_b128 v[138:141], v0 offset:2048\n"
      "v_mul_f32 v186, v216, v186\n"
      "v_mfma_f32_16x16x32_bf16 v[144:147], a[124:127], v[220:223], v[144:147]\n"
      "buffer_load_dwordx4 v214, s[28:31], s73 offen  lds\n"
      "v_mfma_f32_16x16x32_bf16 v[144:147], a[128:131], v[104:107], v[144:147]\n"
      "s_waitcnt lgkmcnt(1)\n"
      "v_mul_f32 v170, v216, v170\n"
      "v_mul_f32_e64 v108, v218, v108\n"
      "v_mfma_f32_16x16x32_bf16 v[172:175], a[12:15], v[210:213], v[172:175]\n"
      "s_add_u32 s73, s73, 1024\n"
      "v_mul_f32 v97, v218, v97\n"
      "v_mul_f32 v111, v218, v111\n"
      "v_mfma_f32_16x16x32_bf16 v[172:175], a[16:19], v[198:201], v[172:175]\n"
      "ds_read_b128 v[156:159], v9 offset:5632\n"
      "v_mul_f32 v185, v216, v185\n"
      "v_mfma_f32_16x16x32_bf16 v[172:175], a[20:23], v[126:129], v[172:175]\n"
      "s_mov_b32 m0, 114688\n"
      "v_mul_f32 v184, v216, v184\n"
      "v_mul_f32 v153, v216, v153\n"
      "v_mfma_f32_16x16x32_bf16 v[112:115], a[36:39], v[210:213], v[112:115]\n"
      "v_cvt_pk_bf16_f32 v184, v184, v185\n"
      "v_cvt_pk_bf16_f32 v185, v186, v187\n"
      "v_mfma_f32_16x16x32_bf16 v[112:115], a[40:43], v[198:201], v[112:115]\n"
      "s_mov_b32 exec_hi, 0xffff\n"
      "buffer_load_dwordx4 v214, s[28:31], s73 offen  lds\n"
      "s_mov_b32 exec_hi, -1\n"
      "v_pk_mul_f32 v[160:161], v[160:161], v[216:217] op_sel:[0,1]\n"
      "v_pk_mul_f32 v[98:99], v[98:99], v[218:219] op_sel_hi:[1,0]\n"
      "v_pk_mul_f32 v[166:167], v[166:167], v[216:217] op_sel:[0,1]\n"
      "v_pk_mul_f32 v[154:155], v[154:155], v[216:217] op_sel_hi:[1,0]\n"
      "v_pk_mul_f32 v[136:137], v[136:137], v[216:217] op_sel:[0,1]\n"
      "v_mfma_f32_16x16x32_bf16 v[112:115], a[44:47], v[126:129], v[112:115]\n"
      "v_cvt_pk_bf16_f32 v160, v160, v161\n"
      "v_cvt_pk_bf16_f32 v187, v154, v155\n"
      "v_mfma_f32_16x16x32_bf16 v[180:183], a[60:63], v[210:213], v[180:183]\n"
      "s_add_u32 m0, s64, 18432\n"
      "v_lshrrev_b32_e64 v142, 3, v142\n"
      "v_cvt_pk_bf16_f32 v186, v152, v153\n"
      "v_mfma_f32_16x16x32_bf16 v[180:183], a[64:67], v[198:201], v[180:183]\n"
      "ds_write_b128 v5, v[184:187]\n"
      "v_pk_mul_f32 v[164:165], v[164:165], v[216:217] op_sel:[0,1]\n"
      "v_mfma_f32_16x16x32_bf16 v[180:183], a[68:71], v[126:129], v[180:183]\n"
      "buffer_load_dwordx4 v16, s[16:19], s45 offen  lds\n"
      "v_mfma_f32_16x16x32_bf16 v[100:103], a[84:87], v[210:213], v[100:103]\n"
      "s_and_b32 s77, s77, 15\n"
      "v_fma_f32 v81, v172, v172, s51\n"
      "v_mul_f32 v168, v216, v168\n"
      "v_mfma_f32_16x16x32_bf16 v[100:103], a[88:91], v[198:201], v[100:103]\n"
      "s_add_u32 m0, s64, 22528\n"
      "v_mul_f32 v163, v217, v163\n"
      "v_cvt_pk_bf16_f32 v134, v134, v135\n"
      "v_mfma_f32_16x16x32_bf16 v[100:103], a[92:95], v[126:129], v[100:103]\n"
      "v_mul_f32 v96, v218, v96\n"
      "v_cvt_pk_bf16_f32 v161, v162, v163\n"
      "v_mfma_f32_16x16x32_bf16 v[206:209], a[108:111], v[210:213], v[206:209]\n"
      "v_mul_f32 v169, v216, v169\n"
      "v_mul_f32 v119, v218, v119\n"
      "v_mfma_f32_16x16x32_bf16 v[206:209], a[112:115], v[198:201], v[206:209]\n"
      "buffer_load_dwordx4 v17, s[16:19], s45 offen  lds\n"
      "v_mfma_f32_16x16x32_bf16 v[206:209], a[116:119], v[126:129], v[206:209]\n"
      "ds_read_b128 v[184:187], v0 offset:14336\n"
      "v_cvt_pk_bf16_f32 v108, v108, v109\n"
      "v_mfma_f32_16x16x32_bf16 v[144:147], a[132:135], v[210:213], v[144:147]\n"
      "v_mul_f32 v116, v218, v116\n"
      "v_fmac_f32 v81, v173, v173\n"
      "v_mfma_f32_16x16x32_bf16 v[144:147], a[136:139], v[198:201], v[144:147]\n"
      "v_cvt_pk_bf16_f32 v135, v136, v137\n"
      "v_cvt_pk_bf16_f32 v163, v166, v167\n"
      "v_mfma_f32_16x16x32_bf16 v[144:147], a[140:143], v[126:129], v[144:147]\n"
      "s_add_u32 m0, s64, 26624\n"
      "v_fma_f32 v80, v100, v100, s51\n"
      "v_cvt_pk_bf16_f32 v162, v164, v165\n"
      "v_mfma_f32_16x16x32_bf16 v[214:217], v[202:205], a[144:147], 0\n"
      "buffer_load_dwordx4 v18, s[16:19], s45 offen  lds\n"
      "v_mfma_f32_16x16x32_bf16 v[214:217], v[220:223], a[148:151], v[214:217]\n"
      "ds_write_b128 v5, v[160:163] offset:6144\n"
      "v_mfma_f32_16x16x32_bf16 v[214:217], v[104:107], a[152:155], v[214:217]\n"
      "s_add_u32 m0, s64, 30720\n"
      "v_fmac_f32 v80, v101, v101\n"
      "v_fmac_f32 v80, v102, v102\n"
      "v_mfma_f32_16x16x32_bf16 v[214:217], v[210:213], a[156:159], v[214:217]\n"
      "v_min_u32 v142, 96, v142\n"
      "v_fmac_f32_e64 v80, v103, v103\n"
      "v_mfma_f32_16x16x32_bf16 v[214:217], v[198:201], a[160:163], v[214:217]\n"
      "ds_write_b64 v6, v[134:135] offset:6144\n"
      "v_mfma_f32_16x16x32_bf16 v[214:217], v[126:129], a[164:167], v[214:217]\n"
      "buffer_load_dwordx4 v19, s[16:19], s45 offen  lds\n"
      "v_mfma_f32_16x16x32_bf16 v[130:133], v[202:205], a[168:171], 0\n"
      "s_mov_b32 m0, s7\n"
      "v_cndmask_b32 v120, v120, v148, s[58:59]\n"
      "v_fmac_f32 v81, v174, v174\n"
      "v_mfma_f32_16x16x32_bf16 v[130:133], v[220:223], a[172:175], v[130:133]\n"
      "v_cvt_pk_bf16_f32 v168, v168, v169\n"
      "v_fmac_f32 v80, v206, v206\n"
      "v_mfma_f32_16x16x32_bf16 v[130:133], v[104:107], a[176:179], v[130:133]\n"
      "ds_read_b128 v[152:155], v1 offset:2048\n"
      "v_mul_f32 v110, v218, v110\n"
      "v_mfma_f32_16x16x32_bf16 v[130:133], v[210:213], a[180:183], v[130:133]\n"
      "ds_read_b128 v[160:163], v1 offset:14336\n"
      "v_fmac_f32_e64 v80, v207, v207\n"
      "v_mfma_f32_16x16x32_bf16 v[130:133], v[198:201], a[184:187], v[130:133]\n"
      "buffer_load_dwordx4 v15, s[0:3], s4 offen  lds\n"
      "v_mfma_f32_16x16x32_bf16 v[130:133], v[126:129], a[188:191], v[130:133]\n"
      "v_cvt_pk_bf16_f32 v169, v170, v171\n"
      "v_cvt_pk_bf16_f32 v96, v96, v97\n"
      "v_mfma_f32_16x16x32_bf16 v[194:197], v[202:205], a[192:195], 0\n"
      "ds_read_b128 v[164:167], v0 offset:8192\n"
      "v_readlane_b32 s86, v38, s43\n"
      "v_mfma_f32_16x16x32_bf16 v[194:197], v[220:223], a[196:199], v[194:197]\n"
      "v_fmac_f32 v81, v175, v175\n"
      "v_fmac_f32 v81, v112, v112\n"
      "v_mfma_f32_16x16x32_bf16 v[194:197], v[104:107], a[200:203], v[194:197]\n"
      "ds_write_b64 v6, v[168:169]\n"
      "v_mfma_f32_16x16x32_bf16 v[194:197], v[210:213], a[204:207], v[194:197]\n"
      "v_cvt_pk_bf16_f32 v109, v110, v111\n"
      "v_fmac_f32 v80, v208, v208\n"
      "v_mfma_f32_16x16x32_bf16 v[194:197], v[198:201], a[208:211], v[194:197]\n"
      "buffer_load_dwordx4 v[210:213], v36, s[28:31], s47 offen\n"
      "v_mfma_f32_16x16x32_bf16 v[194:197], v[126:129], a[212:215], v[194:197]\n"
      "s_waitcnt lgkmcnt(2)\n"
      "v_fmac_f32 v81, v113, v113\n"
      "v_fmac_f32 v80, v209, v209\n"
      "v_mfma_f32_16x16x32_bf16 v[148:151], a[0:3], v[138:141], 0\n"
      "v_or3_b32 v156, v156, v157, v158\n"
      "v_bfe_u32 v223, v219, 4, 4\n"
      "v_mfma_f32_16x16x32_bf16 v[148:151], a[4:7], v[152:155], v[148:151]\n"
      "s_waitcnt lgkmcnt(1)\n"
      "v_mov_b32_dpp v188, v188 quad_perm:[0,0,0,0] row_mask:0xf bank_mask:0xf\n"
      "v_fmac_f32 v80, v144, v144\n"
      "v_mfma_f32_16x16x32_bf16 v[148:151], a[8:11], v[164:167], v[148:151]\n"
      "v_cvt_pk_bf16_f32 v111, v118, v119\n"
      "v_cndmask_b32 v120, v120, v178, s[60:61]\n"
      "v_mfma_f32_16x16x32_bf16 v[148:151], a[12:15], v[122:125], v[148:151]\n"
      "ds_read_b128 v[168:171], v0 offset:4096\n"
      "v_fmac_f32_e64 v81, v114, v114\n"
      "v_mfma_f32_16x16x32_bf16 v[148:151], a[16:19], v[184:187], v[148:151]\n"
      "buffer_load_dword v78, v29, s[28:31], s47 offen\n"
      "v_mfma_f32_16x16x32_bf16 v[148:151], a[20:23], v[160:163], v[148:151]\n"
      "v_cvt_pk_bf16_f32 v97, v98, v99\n"
      "v_fmac_f32 v81, v115, v115\n"
      "v_mfma_f32_16x16x32_bf16 v[134:137], a[24:27], v[138:141], 0\n"
      "v_fmac_f32 v80, v145, v145\n"
      "v_lshrrev_b32_e64 v219, 8, v219\n"
      "v_mfma_f32_16x16x32_bf16 v[134:137], a[28:31], v[152:155], v[134:137]\n"
      "v_mul_f32_e64 v117, v218, v117\n"
      "v_cvt_pk_bf16_f32 v110, v116, v117\n"
      "v_mfma_f32_16x16x32_bf16 v[134:137], a[32:35], v[164:167], v[134:137]\n"
      "ds_write_b128 v5, v[108:111] offset:12288\n"
      "v_mfma_f32_16x16x32_bf16 v[134:137], a[36:39], v[122:125], v[134:137]\n"
      "v_fmac_f32_e64 v81, v180, v180\n"
      "v_fma_f32 v201, v148, v148, s51\n"
      "v_mfma_f32_16x16x32_bf16 v[134:137], a[40:43], v[184:187], v[134:137]\n"
      "v_cvt_pk_bf16_f32 v214, v214, v215\n"
      "v_fmac_f32_e64 v81, v181, v181\n"
      "v_mfma_f32_16x16x32_bf16 v[134:137], a[44:47], v[160:163], v[134:137]\n"
      "ds_write_b64 v6, v[96:97] offset:12288\n"
      "v_mfma_f32_16x16x32_bf16 v[108:111], a[48:51], v[138:141], 0\n"
      "v_fmac_f32 v80, v146, v146\n"
      "v_fmac_f32 v81, v182, v182\n"
      "v_mfma_f32_16x16x32_bf16 v[108:111], a[52:55], v[152:155], v[108:111]\n"
      "v_fmac_f32 v80, v147, v147\n"
      "v_fmac_f32 v201, v149, v149\n"
      "v_mfma_f32_16x16x32_bf16 v[108:111], a[56:59], v[164:167], v[108:111]\n"
      "v_cvt_pk_bf16_f32 v194, v194, v195\n"
      "v_fmac_f32_e64 v81, v183, v183\n"
      "v_mfma_f32_16x16x32_bf16 v[108:111], a[60:63], v[122:125], v[108:111]\n"
      "s_nop 0\n"
      "v_permlane16_swap_b32 v81, v80\n"
      "v_mfma_f32_16x16x32_bf16 v[108:111], a[64:67], v[184:187], v[108:111]\n"
      "v_fmac_f32 v201, v150, v150\n"
      "v_fmac_f32 v201, v151, v151\n"
      "v_mfma_f32_16x16x32_bf16 v[108:111], a[68:71], v[160:163], v[108:111]\n"
      "v_mul_u32_u24 v84, 98, v223\n"
      "v_add_f32 v81, v81, v80\n"
      "v_mfma_f32_16x16x32_bf16 v[176:179], a[72:75], v[138:141], 0\n"
      "v_mov_b32 v80, v81\n"
      "v_cvt_pk_bf16_f32 v195, v196, v197\n"
      "v_mfma_f32_16x16x32_bf16 v[176:179], a[76:79], v[152:155], v[176:179]\n"
      "v_bitop3_b32 v156, v156, v159, s33 bitop3:0xa8\n"
      "v_cvt_pk_bf16_f32 v215, v216, v217\n"
      "v_mfma_f32_16x16x32_bf16 v[176:179], a[80:83], v[164:167], v[176:179]\n"
      "v_permlane32_swap_b32 v81, v80\n"
      "v_mfma_f32_16x16x32_bf16 v[176:179], a[84:87], v[122:125], v[176:179]\n"
      "v_readlane_b32 s85, v37, s42\n"
      "v_add_f32 v81, v81, v80\n"
      "v_mfma_f32_16x16x32_bf16 v[176:179], a[88:91], v[184:187], v[176:179]\n"
      "v_rsq_f32 v81, v81\n"
      "v_mfma_f32_16x16x32_bf16 v[176:179], a[92:95], v[160:163], v[176:179]\n"
      "v_cvt_pk_bf16_f32 v130, v130, v131\n"
      "v_mov_b32 v80, v81\n"
      "v_mfma_f32_16x16x32_bf16 v[116:119], a[96:99], v[138:141], 0\n"
      "v_fmac_f32 v201, v134, v134\n"
      "v_lshl_add_u32 v84, v219, 3, v84\n"
      "v_mfma_f32_16x16x32_bf16 v[116:119], a[100:103], v[152:155], v[116:119]\n"
      "v_rcp_f32 v188, v188\n"
      "v_mfma_f32_16x16x32_bf16 v[116:119], a[104:107], v[164:167], v[116:119]\n"
      "v_fmac_f32 v201, v135, v135\n"
      "v_fma_f32 v189, v176, v176, s51\n"
      "v_mfma_f32_16x16x32_bf16 v[116:119], a[108:111], v[122:125], v[116:119]\n"
      "v_permlane16_swap_b32 v81, v80\n"
      "v_mfma_f32_16x16x32_bf16 v[116:119], a[112:115], v[184:187], v[116:119]\n"
      "v_fmac_f32 v189, v177, v177\n"
      "v_mul_f32 v144, v80, v144\n"
      "v_mfma_f32_16x16x32_bf16 v[116:119], a[116:119], v[160:163], v[116:119]\n"
      "v_cmp_ne_u32 s[56:57], 0, v156\n"
      "v_fmac_f32 v201, v136, v136\n"
      "v_mfma_f32_16x16x32_bf16 v[190:193], a[120:123], v[138:141], 0\n"
      "v_mul_f32 v101, v80, v101\n"
      "v_lshlrev_b32_e64 v219, 2, v219\n"
      "v_mfma_f32_16x16x32_bf16 v[190:193], a[124:127], v[152:155], v[190:193]\n"
      "ds_read_b128 v[96:99], v1 offset:4096\n"
      "v_mul_f32 v100, v80, v100\n"
      "v_mfma_f32_16x16x32_bf16 v[190:193], a[128:131], v[164:167], v[190:193]\n"
      "v_fmac_f32 v189, v178, v178\n"
      "v_sub_u32_e64 v223, v223, v219\n"
      "v_mfma_f32_16x16x32_bf16 v[190:193], a[132:135], v[122:125], v[190:193]\n"
      "s_or_b32 s95, s84, s85\n"
      "v_cvt_pk_bf16_f32 v131, v132, v133\n"
      "v_add_u32 v84, s77, v84\n"
      "v_mfma_f32_16x16x32_bf16 v[190:193], a[136:139], v[184:187], v[190:193]\n"
      "v_mul_f32 v120, v188, v120\n"
      "v_fmac_f32 v201, v137, v137\n"
      "v_mfma_f32_16x16x32_bf16 v[190:193], a[140:143], v[160:163], v[190:193]\n"
      "v_fmac_f32 v189, v179, v179\n"
      "v_fmac_f32 v189, v116, v116\n"
      "v_mfma_f32_16x16x32_bf16 v[196:199], v[138:141], a[144:147], 0\n"
      "v_fmac_f32 v189, v117, v117\n"
      "v_mfma_f32_16x16x32_bf16 v[196:199], v[152:155], a[148:151], v[196:199]\n"
      "v_cvt_pk_bf16_f32 v120, v120, v120\n"
      "v_fmac_f32 v201, v108, v108\n"
      "v_mfma_f32_16x16x32_bf16 v[196:199], v[164:167], a[152:155], v[196:199]\n"
      "ds_read_b128 v[156:159], v0 offset:16384\n"
      "v_fmac_f32 v189, v118, v118\n"
      "v_mfma_f32_16x16x32_bf16 v[196:199], v[122:125], a[156:159], v[196:199]\n"
      "v_mul_f32 v103, v80, v103\n"
      "v_mul_f32_e64 v81, s50, v81\n"
      "v_mfma_f32_16x16x32_bf16 v[196:199], v[184:187], a[160:163], v[196:199]\n"
      "v_fmac_f32 v201, v109, v109\n"
      "v_fmac_f32 v189, v119, v119\n"
      "v_mfma_f32_16x16x32_bf16 v[196:199], v[160:163], a[164:167], v[196:199]\n"
      "v_fmac_f32 v189, v190, v190\n"
      "v_fmac_f32 v189, v191, v191\n"
      "v_mfma_f32_16x16x32_bf16 v[126:129], v[138:141], a[168:171], 0\n"
      "v_fmac_f32 v201, v110, v110\n"
      "v_mul_f32 v172, v81, v172\n"
      "v_mfma_f32_16x16x32_bf16 v[126:129], v[152:155], a[172:175], v[126:129]\n"
      "v_mul_f32 v173, v81, v173\n"
      "v_mul_f32 v180, v81, v180\n"
      "v_mfma_f32_16x16x32_bf16 v[126:129], v[164:167], a[176:179], v[126:129]\n"
      "v_fmac_f32 v201, v111, v111\n"
      "v_fmac_f32 v189, v192, v192\n"
      "v_mfma_f32_16x16x32_bf16 v[126:129], v[122:125], a[180:183], v[126:129]\n"
      "v_mul_f32 v175, v81, v175\n"
      "v_mul_f32 v145, v80, v145\n"
      "v_mfma_f32_16x16x32_bf16 v[126:129], v[184:187], a[184:187], v[126:129]\n"
      "ds_read_b128 v[202:205], v0 offset:10240\n"
      "v_fmac_f32 v189, v193, v193\n"
      "v_mfma_f32_16x16x32_bf16 v[126:129], v[160:163], a[188:191], v[126:129]\n"
      "s_nop 0\n"
      "v_permlane16_swap_b32 v201, v189\n"
      "v_mfma_f32_16x16x32_bf16 v[218:221], v[138:141], a[192:195], 0\n"
      "ds_read_b128 v[104:107], v1 offset:10240\n"
      "v_lshlrev_b32 v223, 4, v223\n"
      "v_mfma_f32_16x16x32_bf16 v[218:221], v[152:155], a[196:199], v[218:221]\n"
      "v_add_f32 v201, v201, v189\n"
      "v_mov_b32 v188, 0x3fb9\n"
      "v_mfma_f32_16x16x32_bf16 v[218:221], v[164:167], a[200:203], v[218:221]\n"
      "ds_read_b128 v[152:155], v1 offset:16384\n"
      "v_mul_f32 v181, v81, v181\n"
      "v_mfma_f32_16x16x32_bf16 v[218:221], v[122:125], a[204:207], v[218:221]\n"
      "v_mul_f32 v183, v81, v183\n"
      "v_mul_f32 v207, v80, v207\n"
      "v_mfma_f32_16x16x32_bf16 v[218:221], v[184:187], a[208:211], v[218:221]\n"
      "s_waitcnt lgkmcnt(2)\n"
      "v_mul_f32 v102, v80, v102\n"
      "v_mov_b32 v189, v201\n"
      "v_mfma_f32_16x16x32_bf16 v[218:221], v[160:163], a[212:215], v[218:221]\n"
      "s_nop 0\n"
      "v_permlane32_swap_b32 v201, v189\n"
      "v_mfma_f32_16x16x32_bf16 v[122:125], a[0:3], v[168:171], 0\n"
      "v_add_f32 v201, v201, v189\n"
      "v_cvt_pk_bf16_f32 v144, v144, v145\n"
      "v_mfma_f32_16x16x32_bf16 v[122:125], a[4:7], v[96:99], v[122:125]\n"
      "v_cvt_pk_bf16_f32 v100, v100, v101\n"
      "v_mul_f32_e64 v174, v81, v174\n"
      "v_mfma_f32_16x16x32_bf16 v[122:125], a[8:11], v[202:205], v[122:125]\n"
      "s_waitcnt lgkmcnt(0)\n"
      "v_rsq_f32 v201, v201\n"
      "v_mfma_f32_16x16x32_bf16 v[122:125], a[12:15], v[104:107], v[122:125]\n"
      "v_mul_f32 v182, v81, v182\n"
      "v_mul_f32 v112, v81, v112\n"
      "v_mfma_f32_16x16x32_bf16 v[122:125], a[16:19], v[156:159], v[122:125]\n"
      "v_cvt_pk_bf16_f32 v101, v102, v103\n"
      "v_mov_b32 v189, v201\n"
      "v_mfma_f32_16x16x32_bf16 v[122:125], a[20:23], v[152:155], v[122:125]\n"
      "s_nop 0\n"
      "v_permlane16_swap_b32 v201, v189\n"
      "v_mfma_f32_16x16x32_bf16 v[160:163], a[24:27], v[168:171], 0\n"
      "ds_write_b16 v12, v120 offset:16\n"
      "v_mfma_f32_16x16x32_bf16 v[160:163], a[28:31], v[96:99], v[160:163]\n"
      "v_mul_f32 v201, s50, v201\n"
      "v_mul_f32 v206, v80, v206\n"
      "v_mfma_f32_16x16x32_bf16 v[160:163], a[32:35], v[202:205], v[160:163]\n"
      "v_mul_f32 v191, v189, v191\n"
      "v_fma_f32 v120, v122, v122, s51\n"
      "v_mfma_f32_16x16x32_bf16 v[160:163], a[36:39], v[104:107], v[160:163]\n"
      "v_cvt_pk_bf16_f32 v216, v196, v197\n"
      "v_mul_f32 v113, v81, v113\n"
      "v_mfma_f32_16x16x32_bf16 v[160:163], a[40:43], v[156:159], v[160:163]\n"
      "s_or_b32 s95, s95, s48\n"
      "v_fmac_f32 v120, v123, v123\n"
      "v_cvt_pk_bf16_f32 v180, v180, v181\n"
      "v_mfma_f32_16x16x32_bf16 v[160:163], a[44:47], v[152:155], v[160:163]\n"
      "v_cvt_pk_bf16_f32 v102, v206, v207\n"
      "v_mul_f32 v149, v201, v149\n"
      "v_mul_f32 v178, v189, v178\n"
      "v_fmac_f32 v120, v124, v124\n"
      "v_cvt_pk_bf16_f32 v196, v218, v219\n"
      "v_pk_mul_f32 v[116:117], v[116:117], v[188:189] op_sel:[0,1]\n"
      "v_pk_mul_f32 v[146:147], v[146:147], v[80:81] op_sel_hi:[1,0]\n"
      "v_pk_mul_f32 v[114:115], v[114:115], v[80:81] op_sel:[0,1]\n"
      "v_pk_mul_f32 v[208:209], v[208:209], v[80:81] op_sel_hi:[1,0]\n"
      "v_pk_mul_f32 v[118:119], v[118:119], v[188:189] op_sel:[0,1]\n"
      "v_pk_mul_f32 v[176:177], v[176:177], v[188:189] op_sel:[0,1]\n"
      "v_pk_mul_f32 v[108:109], v[108:109], v[200:201] op_sel:[0,1]\n"
      "v_pk_mul_f32 v[136:137], v[136:137], v[200:201] op_sel:[0,1]\n"
      "v_pk_mul_f32 v[134:135], v[134:135], v[200:201] op_sel:[0,1]\n"
      "v_mfma_f32_16x16x32_bf16 v[138:141], a[48:51], v[168:171], 0\n"
      "s_add_u32 s73, s73, 1024\n"
      "v_cvt_pk_bf16_f32 v172, v172, v173\n"
      "v_fmac_f32 v120, v125, v125\n"
      "v_mfma_f32_16x16x32_bf16 v[138:141], a[52:55], v[96:99], v[138:141]\n"
      "v_mul_f32 v111, v201, v111\n"
      "v_cmp_gt_u32 s[72:73], 64, v223\n"
      "v_mfma_f32_16x16x32_bf16 v[138:141], a[56:59], v[202:205], v[138:141]\n"
      "v_mul_f32 v179, v189, v179\n"
      "v_fmac_f32 v120, v160, v160\n"
      "v_mfma_f32_16x16x32_bf16 v[138:141], a[60:63], v[104:107], v[138:141]\n"
      "v_cvt_pk_bf16_f32 v103, v208, v209\n"
      "v_mul_f32 v190, v189, v190\n"
      "v_mfma_f32_16x16x32_bf16 v[138:141], a[64:67], v[156:159], v[138:141]\n"
      "v_cvt_pk_bf16_f32 v173, v174, v175\n"
      "v_mul_f32 v150, v201, v150\n"
      "v_mfma_f32_16x16x32_bf16 v[138:141], a[68:71], v[152:155], v[138:141]\n"
      "v_cvt_pk_bf16_f32 v175, v114, v115\n"
      "v_and_b32 v93, 2, v84\n"
      "v_mfma_f32_16x16x32_bf16 v[206:209], a[72:75], v[168:171], 0\n"
      "v_cvt_pk_bf16_f32 v181, v182, v183\n"
      "v_fmac_f32 v120, v161, v161\n"
      "v_mfma_f32_16x16x32_bf16 v[206:209], a[76:79], v[96:99], v[206:209]\n"
      "v_cvt_pk_bf16_f32 v197, v220, v221\n"
      "v_cvt_pk_bf16_f32 v133, v128, v129\n"
      "v_mfma_f32_16x16x32_bf16 v[206:209], a[80:83], v[202:205], v[206:209]\n"
      "v_mul_f32 v148, v201, v148\n"
      "v_fmac_f32 v120, v162, v162\n"
      "v_mfma_f32_16x16x32_bf16 v[206:209], a[84:87], v[104:107], v[206:209]\n"
      "v_mul_f32 v151, v201, v151\n"
      "v_mul_f32 v192, v189, v192\n"
      "v_mfma_f32_16x16x32_bf16 v[206:209], a[88:91], v[156:159], v[206:209]\n"
      "v_mul_f32 v193, v189, v193\n"
      "v_cvt_pk_bf16_f32 v174, v112, v113\n"
      "v_mfma_f32_16x16x32_bf16 v[206:209], a[92:95], v[152:155], v[206:209]\n"
      "s_and_b32 s86, s86, -4\n"
      "v_add_u32 v142, s77, v142\n"
      "v_fmac_f32 v120, v163, v163\n"
      "v_mfma_f32_16x16x32_bf16 v[164:167], a[96:99], v[168:171], 0\n"
      "v_cvt_pk_bf16_f32 v108, v108, v109\n"
      "v_cvt_pk_bf16_f32 v145, v146, v147\n"
      "v_mfma_f32_16x16x32_bf16 v[164:167], a[100:103], v[96:99], v[164:167]\n"
      "s_cmp_ge_u32 s95, 0x40000000\n"
      "s_cselect_b32 s94, -1, -2\n"
      "v_fma_f32 v81, v206, v206, s51\n"
      "v_mfma_f32_16x16x32_bf16 v[164:167], a[104:107], v[202:205], v[164:167]\n"
      "v_mov_b32 v189, 0\n"
      "v_mul_f32 v110, v201, v110\n"
      "v_mfma_f32_16x16x32_bf16 v[164:167], a[108:111], v[104:107], v[164:167]\n"
      "v_fmac_f32 v81, v207, v207\n"
      "v_fmac_f32 v81, v208, v208\n"
      "v_mfma_f32_16x16x32_bf16 v[164:167], a[112:115], v[156:159], v[164:167]\n"
      "v_fmac_f32_e64 v120, v138, v138\n"
      "v_cvt_pk_bf16_f32 v176, v176, v177\n"
      "v_mfma_f32_16x16x32_bf16 v[164:167], a[116:119], v[152:155], v[164:167]\n"
      "v_cvt_pk_bf16_f32 v132, v126, v127\n"
      "v_and_b32_e64 v84, -4, v84\n"
      "v_mfma_f32_16x16x32_bf16 v[218:221], a[120:123], v[168:171], 0\n"
      "v_fmac_f32_e64 v120, v139, v139\n"
      "v_cvt_pk_bf16_f32 v177, v178, v179\n"
      "v_mfma_f32_16x16x32_bf16 v[218:221], a[124:127], v[96:99], v[218:221]\n"
      "v_cvt_pk_bf16_f32 v148, v148, v149\n"
      "v_cvt_pk_bf16_f32 v178, v116, v117\n"
      "v_mfma_f32_16x16x32_bf16 v[218:221], a[128:131], v[202:205], v[218:221]\n"
      "v_fmac_f32_e64 v81, v209, v209\n"
      "v_cvt_pk_bf16_f32 v217, v198, v199\n"
      "v_mfma_f32_16x16x32_bf16 v[218:221], a[132:135], v[104:107], v[218:221]\n"
      "ds_read_b128 v[184:187], v7 offset:18432\n"
      "v_fmac_f32_e64 v120, v140, v140\n"
      "v_mfma_f32_16x16x32_bf16 v[218:221], a[136:139], v[156:159], v[218:221]\n"
      "v_cvt_pk_bf16_f32 v149, v150, v151\n"
      "v_cvt_pk_bf16_f32 v151, v136, v137\n"
      "v_mfma_f32_16x16x32_bf16 v[218:221], a[140:143], v[152:155], v[218:221]\n"
      "v_cvt_pk_bf16_f32 v179, v118, v119\n"
      "v_fmac_f32 v81, v164, v164\n"
      "v_mfma_f32_16x16x32_bf16 v[116:119], v[168:171], a[144:147], 0\n"
      "s_waitcnt vmcnt(7)\n"
      "v_cvt_pk_bf16_f32 v190, v190, v191\n"
      "v_fmac_f32 v81, v165, v165\n"
      "v_mfma_f32_16x16x32_bf16 v[116:119], v[96:99], a[148:151], v[116:119]\n"
      "v_fmac_f32 v81, v166, v166\n"
      "v_cvt_pk_bf16_f32 v109, v110, v111\n"
      "v_mfma_f32_16x16x32_bf16 v[116:119], v[202:205], a[152:155], v[116:119]\n"
      "s_cmp_gt_u32 s42, s44\n"
      "s_cselect_b32 s95, s94, s95\n"
      "v_fmac_f32_e64 v81, v167, v167\n"
      "v_mfma_f32_16x16x32_bf16 v[116:119], v[104:107], a[156:159], v[116:119]\n"
      "v_lshlrev_b64 v[188:189], v223, v[188:189]\n"
      "v_cvt_pk_bf16_f32 v150, v134, v135\n"
      "v_mfma_f32_16x16x32_bf16 v[116:119], v[156:159], a[160:163], v[116:119]\n"
      "v_add_u32 v142, 115296, v142\n"
      "v_fmac_f32_e64 v81, v218, v218\n"
      "v_mfma_f32_16x16x32_bf16 v[116:119], v[152:155], a[164:167], v[116:119]\n"
      "v_fmac_f32 v81, v219, v219\n"
      "v_fmac_f32 v120, v141, v141\n"
      "v_mfma_f32_16x16x32_bf16 v[134:137], v[168:171], a[168:171], 0\n"
      "v_cvt_pk_bf16_f32 v191, v192, v193\n"
      "v_fmac_f32 v81, v220, v220\n"
      "v_mfma_f32_16x16x32_bf16 v[134:137], v[96:99], a[172:175], v[134:137]\n"
      "ds_read_b128 v[110:113], v2 offset:18432\n"
      "v_fmac_f32 v81, v221, v221\n"
      "v_mfma_f32_16x16x32_bf16 v[134:137], v[202:205], a[176:179], v[134:137]\n"
      "s_nop 0\n"
      "v_permlane16_swap_b32 v120, v81\n"
      "v_mfma_f32_16x16x32_bf16 v[134:137], v[104:107], a[180:183], v[134:137]\n"
      "v_add_f32 v120, v120, v81\n"
      "v_mov_b32 v81, v120\n"
      "v_mfma_f32_16x16x32_bf16 v[134:137], v[156:159], a[184:187], v[134:137]\n"
      "s_nop 0\n"
      "v_permlane32_swap_b32 v120, v81\n"
      "v_mfma_f32_16x16x32_bf16 v[134:137], v[152:155], a[188:191], v[134:137]\n"
      "ds_read_b128 v[126:129], v2 offset:21504\n"
      "v_add_f32_e64 v120, v120, v81\n"
      "v_mfma_f32_16x16x32_bf16 v[198:201], v[168:171], a[192:195], 0\n"
      "s_waitcnt lgkmcnt(2)\n"
      "v_rsq_f32 v120, v120\n"
      "v_mfma_f32_16x16x32_bf16 v[198:201], v[96:99], a[196:199], v[198:201]\n"
      "ds_read_b128 v[96:99], v2 offset:24576\n"
      "v_mov_b32 v81, v120\n"
      "v_mfma_f32_16x16x32_bf16 v[198:201], v[202:205], a[200:203], v[198:201]\n"
      "s_nop 0\n"
      "v_permlane16_swap_b32_e64 v120, v81\n"
      "v_mfma_f32_16x16x32_bf16 v[198:201], v[104:107], a[204:207], v[198:201]\n"
      "ds_read_b128 v[168:171], v7 offset:22528\n"
      "v_mul_f32_e64 v120, s50, v120\n"
      "v_mfma_f32_16x16x32_bf16 v[198:201], v[156:159], a[208:211], v[198:201]\n"
      "buffer_store_dwordx4 v[184:187], v20, s[24:27], s46 offen sc1\n"
      "v_pk_mul_f32 v[206:207], v[206:207], v[80:81] op_sel:[0,1]\n"
      "v_pk_mul_f32 v[166:167], v[166:167], v[80:81] op_sel:[0,1]\n"
      "v_pk_mul_f32 v[220:221], v[220:221], v[80:81] op_sel:[0,1]\n"
      "v_pk_mul_f32 v[164:165], v[164:165], v[80:81] op_sel:[0,1]\n"
      "v_pk_mul_f32 v[218:219], v[218:219], v[80:81] op_sel:[0,1]\n"
      "v_pk_mul_f32 v[138:139], v[138:139], v[120:121] op_sel_hi:[1,0]\n"
      "v_pk_mul_f32 v[122:123], v[122:123], v[120:121] op_sel_hi:[1,0]\n"
      "v_pk_mul_f32 v[208:209], v[208:209], v[80:81] op_sel:[0,1]\n"
      "v_mfma_f32_16x16x32_bf16 v[198:201], v[152:155], a[212:215], v[198:201]\n"
      "s_waitcnt lgkmcnt(1)\n"
      "v_cndmask_b32_e64 v188, 0, v188, s[72:73]\n"
      "v_cvt_pk_bf16_f32 v81, v136, v137\n"
      "v_mfma_f32_16x16x32_bf16 v[184:187], a[216:219], v[110:113], 0\n"
      "ds_read_b128 v[202:205], v2 offset:27648\n"
      "v_add_u32 v156, 110592, v84\n"
      "v_mfma_f32_16x16x32_bf16 v[184:187], a[220:223], v[126:129], v[184:187]\n"
      "s_waitcnt lgkmcnt(1)\n"
      "v_cvt_pk_bf16_f32 v80, v134, v135\n"
      "v_cvt_pk_bf16_f32 v206, v206, v207\n"
      "v_mfma_f32_16x16x32_bf16 v[184:187], a[224:227], v[96:99], v[184:187]\n"
      "ds_read2_b32 v[114:115], v156 offset0:0 offset1:1\n"
      "v_cndmask_b32_e64 v189, 0, v189, s[72:73]\n"
      "v_mfma_f32_16x16x32_bf16 v[152:155], a[240:243], v[110:113], 0\n"
      "ds_read_b128 v[134:137], v2 offset:33792\n"
      "v_cvt_pk_bf16_f32 v85, v200, v201\n"
      "v_mfma_f32_16x16x32_bf16 v[152:155], a[244:247], v[126:129], v[152:155]\n"
      "ds_read_b128 v[104:107], v2 offset:30720\n"
      "v_cvt_pk_bf16_f32 v207, v208, v209\n"
      "v_mfma_f32_16x16x32_bf16 v[152:155], a[248:251], v[96:99], v[152:155]\n"
      "buffer_store_dwordx4 v[168:171], v21, s[24:27], s46 offen sc1\n"
      "v_pk_mul_f32 v[140:141], v[140:141], v[120:121] op_sel_hi:[1,0]\n"
      "v_pk_mul_f32 v[124:125], v[124:125], v[120:121] op_sel_hi:[1,0]\n"
      "v_pk_mul_f32 v[162:163], v[162:163], v[120:121] op_sel_hi:[1,0]\n"
      "v_pk_mul_f32 v[160:161], v[160:161], v[120:121] op_sel_hi:[1,0]\n"
      "v_mfma_f32_16x16x32_bf16 v[168:171], v[232:235], v[110:113], 0\n"
      "ds_read2_b32 v[88:89], v156 offset0:24 offset1:18\n"
      "v_cvt_pk_bf16_f32 v138, v138, v139\n"
      "v_mfma_f32_16x16x32_bf16 v[168:171], v[236:239], v[126:129], v[168:171]\n"
      "ds_read_b128 v[110:113], v7 offset:26624\n"
      "v_cvt_pk_bf16_f32 v122, v122, v123\n"
      "v_mfma_f32_16x16x32_bf16 v[168:171], v[240:243], v[96:99], v[168:171]\n"
      "s_waitcnt lgkmcnt(2)\n"
      "v_alignbyte_b32 v114, v115, v114, v93\n"
      "v_cvt_pk_bf16_f32 v77, v118, v119\n"
      "v_mfma_f32_16x16x32_bf16 v[184:187], a[228:231], v[202:205], v[184:187]\n"
      "ds_read_b64 v[192:193], v10 offset:24\n"
      "v_cvt_pk_bf16_f32 v218, v218, v219\n"
      "v_mfma_f32_16x16x32_bf16 v[184:187], a[232:235], v[104:107], v[184:187]\n"
      "ds_read2_b32 v[182:183], v156 offset0:2 offset1:10\n"
      "v_cvt_pk_bf16_f32 v76, v116, v117\n"
      "v_mfma_f32_16x16x32_bf16 v[184:187], a[236:239], v[134:137], v[184:187]\n"
      "ds_read2_b32 v[120:121], v156 offset0:8 offset1:9\n"
      "v_cvt_pk_bf16_f32 v139, v140, v141\n"
      "v_mfma_f32_16x16x32_bf16 v[152:155], a[252:255], v[202:205], v[152:155]\n"
      "ds_read_b128 v[116:119], v7 offset:30720\n"
      "v_cvt_pk_bf16_f32 v208, v164, v165\n"
      "v_mfma_f32_16x16x32_bf16 v[152:155], v[224:227], v[104:107], v[152:155]\n"
      "s_waitcnt lgkmcnt(4)\n"
      "v_cvt_pk_bf16_f32 v209, v166, v167\n"
      "v_cvt_pk_bf16_f32 v123, v124, v125\n"
      "v_mfma_f32_16x16x32_bf16 v[152:155], v[228:231], v[134:137], v[152:155]\n"
      "ds_read2_b32 v[140:141], v156 offset0:16 offset1:17\n"
      "v_fma_f32 v222, v184, v184, s52\n"
      "v_mfma_f32_16x16x32_bf16 v[168:171], v[244:247], v[202:205], v[168:171]\n"
      "ds_read_b128 v[156:159], v10 offset:32\n"
      "v_add_u32 v204, 112160, v84\n"
      "v_mfma_f32_16x16x32_bf16 v[168:171], v[248:251], v[104:107], v[168:171]\n"
      "buffer_store_dwordx4 v[110:113], v22, s[24:27], s46 offen sc1\n"
      "v_mfma_f32_16x16x32_bf16 v[168:171], v[252:255], v[134:137], v[168:171]\n"
      "ds_read_u16 v142, v142\n"
      "v_mov_b32 v147, 0\n"
      "v_mfma_f32_16x16x32_bf16 v[96:99], v[100:103], v[172:175], v[40:43]\n"
      "s_waitcnt lgkmcnt(3)\n"
      "v_cmp_gt_u32 s[72:73], 64, v223\n"
      "v_alignbyte_b32 v115, v182, v115, v93\n"
      "v_mfma_f32_16x16x32_bf16 v[110:113], v[176:179], v[172:175], v[44:47]\n"
      "s_waitcnt lgkmcnt(2)\n"
      "v_mov_b32 v146, 0xbb2b\n"
      "v_cvt_pk_bf16_f32 v219, v220, v221\n"
      "v_mfma_f32_16x16x32_bf16 v[126:129], v[206:209], v[172:175], v[48:51]\n"
      "s_waitcnt lgkmcnt(1)\n"
      "v_alignbyte_b32 v140, v141, v140, v93\n"
      "v_lshlrev_b64 v[146:147], v223, v[146:147]\n"
      "v_mfma_f32_16x16x32_bf16 v[200:203], v[156:159], v[172:175], v[24:27]\n"
      "ds_read_b128 v[164:167], v7 offset:34816\n"
      "v_alignbyte_b32 v120, v121, v120, v93\n"
      "v_mfma_f32_16x16x16_bf16 v[96:99], v[144:145], v[180:181], v[96:99]\n"
      "v_cvt_pk_bf16_f32 v125, v162, v163\n"
      "v_alignbyte_b32 v141, v89, v141, v93\n"
      "v_mul_f32 v223, v185, v185\n"
      "v_mfma_f32_16x16x16_bf16 v[110:113], v[190:191], v[180:181], v[110:113]\n"
      "ds_read2_b32 v[104:105], v204 offset0:8 offset1:9\n"
      "v_alignbyte_b32 v121, v183, v121, v93\n"
      "v_mfma_f32_16x16x16_bf16 v[126:129], v[218:219], v[180:181], v[126:129]\n"
      "buffer_store_dwordx4 v[116:119], v23, s[24:27], s46 offen sc1\n"
      "v_mfma_f32_16x16x16_bf16 v[200:203], v[192:193], v[180:181], v[200:203]\n"
      "s_nop 0\n"
      "ds_read_b128 v[116:119], v2 offset:18688\n"
      "v_add_u32 v205, 113728, v84\n"
      "v_mfma_f32_16x16x16_bf16 v[96:99], v[188:189], v[114:115], v[96:99]\n"
      "v_cndmask_b32_e64 v146, 0, v146, s[72:73]\n"
      "v_fmac_f32_e64 v223, v187, v187\n"
      "v_alignbyte_b32 v88, v88, v88, v93\n"
      "v_mfma_f32_16x16x16_bf16 v[110:113], v[188:189], v[120:121], v[110:113]\n"
      "ds_read_b128 v[172:175], v2 offset:21760\n"
      "v_cvt_pk_bf16_f32 v124, v160, v161\n"
      "v_mfma_f32_16x16x16_bf16 v[126:129], v[188:189], v[140:141], v[126:129]\n"
      "v_cndmask_b32_e64 v147, 0, v147, s[72:73]\n"
      "v_fmac_f32_e64 v222, v186, v186\n"
      "v_cvt_pk_bf16_f32 v84, v198, v199\n"
      "v_mfma_f32_16x16x16_bf16 v[200:203], v[188:189], v[88:89], v[200:203]\n"
      "ds_read_b128 v[180:183], v2 offset:24832\n"
      "v_add_u32_e64 v12, 32, v12\n"
      "v_mfma_f32_16x16x16_bf16 v[96:99], v[146:147], v[114:115], v[96:99]\n"
      "ds_read2_b32 v[106:107], v204 offset0:2 offset1:10\n"
      "v_fmac_f32 v222, v152, v152\n"
      "v_mfma_f32_16x16x16_bf16 v[110:113], v[146:147], v[120:121], v[110:113]\n"
      "s_waitcnt lgkmcnt(3)\n"
      "v_lshlrev_b32 v142, 16, v142\n"
      "v_fmac_f32 v223, v153, v153\n"
      "v_mfma_f32_16x16x16_bf16 v[126:129], v[146:147], v[140:141], v[126:129]\n"
      "buffer_store_dwordx4 v[164:167], v25, s[24:27], s46 offen sc1\n"
      "v_pk_fma_f32 v[222:223], v[154:155], v[154:155], v[222:223]\n"
      "v_mfma_f32_16x16x16_bf16 v[200:203], v[146:147], v[88:89], v[200:203]\n"
      "ds_read2_b32 v[198:199], v204 offset0:0 offset1:1\n"
      "v_alignbyte_b32 v104, v105, v104, v93\n"
      "v_mfma_f32_16x16x32_bf16 v[160:163], a[216:219], v[116:119], 0\n"
      "s_waitcnt lgkmcnt(2)\n"
      "v_max3_f32 v89, v96, v97, v98\n"
      "v_max3_f32 v89, v89, v112, v113\n"
      "v_mfma_f32_16x16x32_bf16 v[160:163], a[220:223], v[172:175], v[160:163]\n"
      "s_waitcnt lgkmcnt(1)\n"
      "v_max3_f32 v203, v99, v110, v111\n"
      "v_max3_f32 v203, v203, v126, v127\n"
      "v_mfma_f32_16x16x32_bf16 v[160:163], a[224:227], v[180:183], v[160:163]\n"
      "v_alignbyte_b32 v105, v107, v105, v93\n"
      "v_max3_f32 v89, v89, v128, v129\n"
      "v_mfma_f32_16x16x32_bf16 v[164:167], a[240:243], v[116:119], 0\n"
      "v_max3_f32 v89, v89, v203, v200\n"
      "v_mov_b32_e64 v107, v89\n"
      "v_mfma_f32_16x16x32_bf16 v[164:167], a[244:247], v[172:175], v[164:167]\n"
      "s_nop 0\n"
      "v_permlane16_swap_b32 v89, v107\n"
      "v_mfma_f32_16x16x32_bf16 v[164:167], a[248:251], v[180:183], v[164:167]\n"
      "s_waitcnt lgkmcnt(0)\n"
      "v_max_f32 v89, v89, v107\n"
      "s_waitcnt vmcnt(6)\n"
      "v_or3_b32 v210, v210, v211, v212\n"
      "v_mfma_f32_16x16x32_bf16 v[134:137], v[232:235], v[116:119], 0\n"
      "ds_read2_b32 v[202:203], v204 offset0:16 offset1:17\n"
      "v_mov_b32 v107, v89\n"
      "v_mfma_f32_16x16x32_bf16 v[134:137], v[236:239], v[172:175], v[134:137]\n"
      "s_nop 0\n"
      "v_permlane32_swap_b32 v89, v107\n"
      "v_mfma_f32_16x16x32_bf16 v[134:137], v[240:243], v[180:183], v[134:137]\n"
      "ds_read2_b32 v[114:115], v204 offset0:24 offset1:18\n"
      "v_alignbyte_b32 v198, v199, v198, v93\n"
      "v_alignbyte_b32 v199, v106, v199, v93\n"
      "v_max_f32 v89, v89, v107\n"
      "v_sub_f32 v110, v110, v89\n"
      "v_pk_add_f32 v[98:99], v[98:99], v[88:89] op_sel:[0,1] neg_lo:[0,1] neg_hi:[0,1]\n"
      "v_pk_add_f32 v[96:97], v[96:97], v[88:89] op_sel:[0,1] neg_lo:[0,1] neg_hi:[0,1]\n"
      "v_pk_add_f32 v[112:113], v[112:113], v[88:89] op_sel:[0,1] neg_lo:[0,1] neg_hi:[0,1]\n"
      "v_pk_add_f32 v[128:129], v[128:129], v[88:89] op_sel:[0,1] neg_lo:[0,1] neg_hi:[0,1]\n"
      "v_pk_add_f32 v[126:127], v[126:127], v[88:89] op_sel:[0,1] neg_lo:[0,1] neg_hi:[0,1]\n"
      "v_mfma_f32_16x16x32_bf16 v[172:175], v[100:103], v[148:151], v[52:55]\n"
      "v_exp_f32 v97, v97\n"
      "s_waitcnt vmcnt(5)\n"
      "v_or3_b32 v210, v210, v213, v78\n"
      "v_mfma_f32_16x16x32_bf16 v[116:119], v[176:179], v[148:151], v[56:59]\n"
      "ds_write_b32 v31, v210 offset:4608\n"
      "v_mfma_f32_16x16x32_bf16 v[210:213], v[206:209], v[148:151], v[60:63]\n"
      "v_exp_f32 v96, v96\n"
      "v_mfma_f32_16x16x32_bf16 v[180:183], v[156:159], v[148:151], v[26:29]\n"
      "s_waitcnt lgkmcnt(1)\n"
      "v_exp_f32 v126, v126\n"
      "v_mfma_f32_16x16x16_bf16 v[172:175], v[144:145], v[108:109], v[172:175]\n"
      "v_alignbyte_b32 v202, v203, v202, v93\n"
      "v_exp_f32 v98, v98\n"
      "v_mfma_f32_16x16x16_bf16 v[116:119], v[190:191], v[108:109], v[116:119]\n"
      "v_exp_f32 v128, v128\n"
      "v_cvt_pk_bf16_f32 v96, v96, v97\n"
      "v_mfma_f32_16x16x16_bf16 v[210:213], v[218:219], v[108:109], v[210:213]\n"
      "v_sub_f32 v111, v111, v89\n"
      "v_exp_f32 v113, v113\n"
      "v_mfma_f32_16x16x16_bf16 v[180:183], v[192:193], v[108:109], v[180:183]\n"
      "v_exp_f32 v129, v129\n"
      "v_alignbyte_b32 v203, v115, v203, v93\n"
      "v_mfma_f32_16x16x16_bf16 v[172:175], v[188:189], v[198:199], v[172:175]\n"
      "v_alignbyte_b32 v114, v114, v114, v93\n"
      "v_exp_f32 v111, v111\n"
      "v_mfma_f32_16x16x16_bf16 v[116:119], v[188:189], v[104:105], v[116:119]\n"
      "v_exp_f32 v112, v112\n"
      "v_exp_f32 v99, v99\n"
      "v_mfma_f32_16x16x16_bf16 v[210:213], v[188:189], v[202:203], v[210:213]\n"
      "v_sub_f32 v200, v200, v89\n"
      "v_exp_f32_e64 v127, v127\n"
      "v_mfma_f32_16x16x16_bf16 v[180:183], v[188:189], v[114:115], v[180:183]\n"
      "ds_read_b128 v[148:151], v2 offset:30976\n"
      "v_cvt_pk_bf16_f32 v89, v128, v129\n"
      "v_mfma_f32_16x16x16_bf16 v[172:175], v[146:147], v[198:199], v[172:175]\n"
      "ds_read_b128 v[106:109], v2 offset:27904\n"
      "v_cvt_pk_bf16_f32 v97, v98, v99\n"
      "v_mfma_f32_16x16x16_bf16 v[116:119], v[146:147], v[104:105], v[116:119]\n"
      "v_exp_f32 v200, v200\n"
      "v_cvt_pk_bf16_f32 v88, v126, v127\n"
      "v_mfma_f32_16x16x16_bf16 v[210:213], v[146:147], v[202:203], v[210:213]\n"
      "ds_read_b128 v[126:129], v2 offset:34048\n"
      "v_exp_f32 v110, v110\n"
      "v_mfma_f32_16x16x16_bf16 v[180:183], v[146:147], v[114:115], v[180:183]\n"
      "v_max3_f32 v105, v172, v173, v174\n"
      "v_cvt_pk_bf16_f32 v98, v110, v111\n"
      "v_max3_f32 v105, v105, v118, v119\n"
      "v_cvt_pk_bf16_f32 v99, v112, v113\n"
      "v_max3_f32 v78, v175, v116, v117\n"
      "v_max3_f32 v105, v105, v212, v213\n"
      "v_pk_fma_f32 v[222:223], v[168:169], v[168:169], v[222:223]\n"
      "v_pk_fma_f32 v[222:223], v[170:171], v[170:171], v[222:223]\n"
      "v_max3_f32 v78, v78, v210, v211\n"
      "v_max3_f32 v105, v105, v78, v180\n"
      "v_mov_b32 v182, v105\n"
      "s_waitcnt lgkmcnt(1)\n"
      "v_mfma_f32_16x16x32_bf16 v[160:163], a[228:231], v[106:109], v[160:163]\n"
      "v_permlane16_swap_b32 v105, v182\n"
      "v_mfma_f32_16x16x32_bf16 v[160:163], a[232:235], v[148:151], v[160:163]\n"
      "ds_read_u16 v78, v11\n"
      "s_waitcnt lgkmcnt(1)\n"
      "v_mfma_f32_16x16x32_bf16 v[160:163], a[236:239], v[126:129], v[160:163]\n"
      "v_add_f32 v222, v222, v223\n"
      "v_max_f32 v105, v105, v182\n"
      "v_mfma_f32_16x16x32_bf16 v[164:167], a[252:255], v[106:109], v[164:167]\n"
      "ds_read2_b32 v[140:141], v205 offset0:16 offset1:17\n"
      "v_mov_b32 v182, v105\n"
      "v_mfma_f32_16x16x32_bf16 v[164:167], v[224:227], v[148:151], v[164:167]\n"
      "ds_read_u16 v82, v11 offset:32\n"
      "v_cvt_pk_bf16_f32 v90, v200, 0\n"
      "v_permlane32_swap_b32 v105, v182\n"
      "v_max_f32_e64 v105, v105, v182\n"
      "v_pk_add_f32 v[116:117], v[116:117], v[104:105] op_sel:[0,1] neg_lo:[0,1] neg_hi:[0,1]\n"
      "v_pk_add_f32 v[118:119], v[118:119], v[104:105] op_sel:[0,1] neg_lo:[0,1] neg_hi:[0,1]\n"
      "v_pk_add_f32 v[212:213], v[212:213], v[104:105] op_sel:[0,1] neg_lo:[0,1] neg_hi:[0,1]\n"
      "v_pk_add_f32 v[174:175], v[174:175], v[104:105] op_sel:[0,1] neg_lo:[0,1] neg_hi:[0,1]\n"
      "v_pk_add_f32 v[172:173], v[172:173], v[104:105] op_sel:[0,1] neg_lo:[0,1] neg_hi:[0,1]\n"
      "v_mfma_f32_16x16x32_bf16 v[164:167], v[228:231], v[126:129], v[164:167]\n"
      "v_exp_f32_e64 v174, v174\n"
      "v_mfma_f32_16x16x32_bf16 v[134:137], v[244:247], v[106:109], v[134:137]\n"
      "s_waitcnt lgkmcnt(2)\n"
      "v_exp_f32 v172, v172\n"
      "v_mfma_f32_16x16x32_bf16 v[134:137], v[248:251], v[148:151], v[134:137]\n"
      "ds_read2_b32 v[120:121], v205 offset0:0 offset1:1\n"
      "v_exp_f32 v173, v173\n"
      "v_mfma_f32_16x16x32_bf16 v[134:137], v[252:255], v[126:129], v[134:137]\n"
      "ds_read2_b32 v[182:183], v205 offset0:24 offset1:18\n"
      "v_sub_f32 v180, v180, v105\n"
      "v_mfma_f32_16x16x32_bf16 v[108:111], v[32:35], v[96:99], 0\n"
      "ds_read_u16 v86, v11 offset:64\n"
      "v_cvt_pk_bf16_f32 v172, v172, v173\n"
      "v_mfma_f32_16x16x32_bf16 v[108:111], v[32:35], v[88:91], v[108:111]\n"
      "ds_read2_b32 v[114:115], v205 offset0:2 offset1:10\n"
      "v_exp_f32 v119, v119\n"
      "v_exp_f32 v175, v175\n"
      "v_pk_add_f32 v[210:211], v[210:211], v[104:105] op_sel:[0,1] neg_lo:[0,1] neg_hi:[0,1]\n"
      "v_mfma_f32_16x16x32_bf16 v[148:151], v[214:217], v[96:99], 0\n"
      "ds_read2_b32 v[220:221], v205 offset0:8 offset1:9\n"
      "v_cvt_pk_bf16_f32 v173, v174, v175\n"
      "v_mfma_f32_16x16x32_bf16 v[148:151], v[76:79], v[88:91], v[148:151]\n"
      "v_exp_f32 v118, v118\n"
      "v_mfma_f32_16x16x32_bf16 v[110:113], v[130:133], v[96:99], 0\n"
      "s_waitcnt lgkmcnt(3)\n"
      "v_cvt_pk_bf16_f32 v175, v118, v119\n"
      "v_alignbyte_b32 v182, v182, v182, v93\n"
      "v_mfma_f32_16x16x32_bf16 v[110:113], v[80:83], v[88:91], v[110:113]\n"
      "v_exp_f32_e64 v117, v117\n"
      "v_mfma_f32_16x16x32_bf16 v[104:107], v[194:197], v[96:99], 0\n"
      "s_waitcnt lgkmcnt(1)\n"
      "v_exp_f32_e64 v116, v116\n"
      "v_mfma_f32_16x16x32_bf16 v[104:107], v[84:87], v[88:91], v[104:107]\n"
      "v_cvt_pk_bf16_f32 v174, v116, v117\n"
      "v_alignbyte_b32 v120, v121, v120, v93\n"
      "v_alignbyte_b32 v140, v141, v140, v93\n"
      "v_mfma_f32_16x16x32_bf16 v[202:205], v[100:103], v[122:125], v[64:67]\n"
      "s_waitcnt lgkmcnt(0)\n"
      "v_alignbyte_b32 v220, v221, v220, v93\n"
      "v_alignbyte_b32 v121, v114, v121, v93\n"
      "v_mfma_f32_16x16x32_bf16 v[126:129], v[176:179], v[122:125], v[68:71]\n"
      "v_alignbyte_b32 v141, v183, v141, v93\n"
      "v_alignbyte_b32 v221, v115, v221, v93\n"
      "v_mfma_f32_16x16x32_bf16 v[198:201], v[206:209], v[122:125], v[72:75]\n"
      "v_rcp_f32 v108, v108\n"
      "v_mfma_f32_16x16x32_bf16 v[114:117], v[156:159], v[122:125], v[28:31]\n"
      "v_mul_f32 v150, v108, v150\n"
      "v_mul_f32 v112, v108, v112\n"
      "v_mfma_f32_16x16x16_bf16 v[202:205], v[144:145], v[138:139], v[202:205]\n"
      "v_mul_f32 v148, v108, v148\n"
      "v_exp_f32 v210, v210\n"
      "v_mfma_f32_16x16x16_bf16 v[126:129], v[190:191], v[138:139], v[126:129]\n"
      "v_exp_f32 v180, v180\n"
      "v_mul_f32 v149, v108, v149\n"
      "v_mfma_f32_16x16x16_bf16 v[198:201], v[218:219], v[138:139], v[198:201]\n"
      "v_exp_f32 v213, v213\n"
      "v_mul_f32_e64 v151, v108, v151\n"
      "v_mfma_f32_16x16x16_bf16 v[114:117], v[192:193], v[138:139], v[114:117]\n"
      "v_cvt_pk_bf16_f32 v148, v148, v149\n"
      "v_exp_f32 v212, v212\n"
      "v_mfma_f32_16x16x16_bf16 v[202:205], v[188:189], v[120:121], v[202:205]\n"
      "v_cvt_pk_bf16_f32 v149, v150, v151\n"
      "v_exp_f32 v211, v211\n"
      "v_mfma_f32_16x16x16_bf16 v[126:129], v[188:189], v[220:221], v[126:129]\n"
      "v_mul_f32_e64 v125, v161, v161\n"
      "v_cvt_pk_bf16_f32 v93, v212, v213\n"
      "v_fma_f32 v124, v160, v160, s52\n"
      "v_mfma_f32_16x16x16_bf16 v[198:201], v[188:189], v[140:141], v[198:201]\n"
      "v_cvt_pk_bf16_f32 v92, v210, v211\n"
      "v_mul_f32 v111, v108, v111\n"
      "v_fmac_f32 v125, v163, v163\n"
      "v_mfma_f32_16x16x16_bf16 v[114:117], v[188:189], v[182:183], v[114:117]\n"
      "v_mul_f32 v110, v108, v110\n"
      "v_fmac_f32 v124, v162, v162\n"
      "v_mul_f32_e64 v113, v108, v113\n"
      "v_mfma_f32_16x16x16_bf16 v[202:205], v[146:147], v[120:121], v[202:205]\n"
      "ds_read_b128 v[210:213], v2 offset:25088\n"
      "v_cvt_pk_bf16_f32 v150, v110, v111\n"
      "v_mfma_f32_16x16x16_bf16 v[126:129], v[146:147], v[220:221], v[126:129]\n"
      "ds_read_b128 v[120:123], v2 offset:22016\n"
      "v_fmac_f32_e64 v125, v165, v165\n"
      "v_mfma_f32_16x16x16_bf16 v[198:201], v[146:147], v[140:141], v[198:201]\n"
      "ds_read_b128 v[138:141], v2 offset:18944\n"
      "v_cvt_pk_bf16_f32 v151, v112, v113\n"
      "v_mfma_f32_16x16x16_bf16 v[114:117], v[146:147], v[182:183], v[114:117]\n"
      "v_fmac_f32_e64 v124, v164, v164\n"
      "v_cvt_pk_bf16_f32 v94, v180, 0\n"
      "v_max3_f32 v180, v202, v203, v204\n"
      "v_max3_f32 v180, v180, v128, v129\n"
      "v_max3_f32 v111, v205, v126, v127\n"
      "v_max3_f32 v180, v180, v200, v201\n"
      "v_pk_mul_f32 v[106:107], v[106:107], v[108:109] op_sel_hi:[1,0]\n"
      "v_max3_f32 v111, v111, v198, v199\n"
      "v_max3_f32 v180, v180, v111, v114\n"
      "v_mov_b32 v111, v180\n"
      "v_pk_fma_f32 v[124:125], v[166:167], v[166:167], v[124:125]\n"
      "v_pk_mul_f32 v[104:105], v[104:105], v[108:109] op_sel_hi:[1,0]\n"
      "v_permlane16_swap_b32 v180, v111\n"
      "v_max_f32 v180, v180, v111\n"
      "v_mov_b32 v111, v180\n"
      "v_pk_fma_f32 v[124:125], v[134:135], v[134:135], v[124:125]\n"
      "v_pk_fma_f32 v[124:125], v[136:137], v[136:137], v[124:125]\n"
      "v_permlane32_swap_b32_e64 v180, v111\n"
      "v_max_f32 v180, v180, v111\n"
      "v_pk_add_f32 v[204:205], v[204:205], v[180:181] op_sel_hi:[1,0] neg_lo:[0,1] neg_hi:[0,1]\n"
      "s_waitcnt lgkmcnt(0)\n"
      "v_mfma_f32_16x16x32_bf16 v[108:111], a[216:219], v[138:141], 0\n"
      "ds_write_b128 v3, v[148:151]\n"
      "v_pk_add_f32 v[202:203], v[202:203], v[180:181] op_sel_hi:[1,0] neg_lo:[0,1] neg_hi:[0,1]\n"
      "v_pk_add_f32 v[128:129], v[128:129], v[180:181] op_sel_hi:[1,0] neg_lo:[0,1] neg_hi:[0,1]\n"
      "v_pk_add_f32 v[200:201], v[200:201], v[180:181] op_sel_hi:[1,0] neg_lo:[0,1] neg_hi:[0,1]\n"
      "v_mfma_f32_16x16x32_bf16 v[108:111], a[220:223], v[120:123], v[108:111]\n"
      "v_sub_f32 v114, v114, v180\n"
      "v_cvt_pk_bf16_f32 v104, v104, v105\n"
      "v_mfma_f32_16x16x32_bf16 v[108:111], a[224:227], v[210:213], v[108:111]\n"
      "v_cvt_pk_bf16_f32 v105, v106, v107\n"
      "v_sub_f32 v126, v126, v180\n"
      "v_mfma_f32_16x16x32_bf16 v[116:119], a[240:243], v[138:141], 0\n"
      "ds_read_b128 v[148:151], v10\n"
      "v_sub_f32 v199, v199, v180\n"
      "v_mfma_f32_16x16x32_bf16 v[116:119], a[244:247], v[120:123], v[116:119]\n"
      "v_exp_f32 v201, v201\n"
      "v_mfma_f32_16x16x32_bf16 v[116:119], a[248:251], v[210:213], v[116:119]\n"
      "v_add_f32 v124, v124, v125\n"
      "v_sub_f32 v198, v198, v180\n"
      "v_mfma_f32_16x16x32_bf16 v[96:99], v[232:235], v[138:141], 0\n"
      "v_exp_f32_e64 v129, v129\n"
      "v_mfma_f32_16x16x32_bf16 v[96:99], v[236:239], v[120:123], v[96:99]\n"
      "v_exp_f32 v202, v202\n"
      "v_mfma_f32_16x16x32_bf16 v[96:99], v[240:243], v[210:213], v[96:99]\n"
      "v_exp_f32 v203, v203\n"
      "v_mfma_f32_16x16x32_bf16 v[210:213], v[32:35], v[172:175], 0\n"
      "v_exp_f32 v128, v128\n"
      "v_mfma_f32_16x16x32_bf16 v[210:213], v[32:35], v[92:95], v[210:213]\n"
      "v_exp_f32 v204, v204\n"
      "v_mfma_f32_16x16x32_bf16 v[120:123], v[214:217], v[172:175], 0\n"
      "ds_write_b64 v4, v[104:105]\n"
      "v_mfma_f32_16x16x32_bf16 v[120:123], v[76:79], v[92:95], v[120:123]\n"
      "ds_read_b64 v[188:189], v10 offset:16\n"
      "v_sub_f32 v127, v127, v180\n"
      "v_mfma_f32_16x16x32_bf16 v[180:183], v[130:133], v[172:175], 0\n"
      "s_nop 0\n"
      "v_rcp_f32 v210, v210\n"
      "v_mfma_f32_16x16x32_bf16 v[180:183], v[80:83], v[92:95], v[180:183]\n"
      "v_exp_f32 v200, v200\n"
      "v_mfma_f32_16x16x32_bf16 v[104:107], v[194:197], v[172:175], 0\n"
      "v_exp_f32_e64 v205, v205\n"
      "v_mfma_f32_16x16x32_bf16 v[104:107], v[84:87], v[92:95], v[104:107]\n"
      "s_waitcnt lgkmcnt(2)\n"
      "v_exp_f32 v199, v199\n"
      "v_mfma_f32_16x16x32_bf16 v[172:175], v[148:151], v[100:103], v[30:33]\n"
      "v_exp_f32_e64 v126, v126\n"
      "v_mfma_f32_16x16x32_bf16 v[100:103], v[148:151], v[176:179], v[30:33]\n"
      "ds_read_b128 v[176:179], v2 offset:28160\n"
      "v_cvt_pk_bf16_f32 v89, v200, v201\n"
      "v_mfma_f32_16x16x32_bf16 v[138:141], v[148:151], v[206:209], v[30:33]\n"
      "s_waitcnt lgkmcnt(1)\n"
      "v_exp_f32 v198, v198\n"
      "v_mfma_f32_16x16x32_bf16 v[206:209], v[148:151], v[156:159], v[30:33]\n"
      "ds_read_b128 v[148:151], v2 offset:31232\n"
      "v_cvt_pk_bf16_f32 v202, v202, v203\n"
      "v_mfma_f32_16x16x16_bf16 v[172:175], v[188:189], v[144:145], v[172:175]\n"
      "ds_read_b128 v[156:159], v2 offset:34304\n"
      "v_mul_f32_e64 v104, v210, v104\n"
      "v_mfma_f32_16x16x16_bf16 v[100:103], v[188:189], v[190:191], v[100:103]\n"
      "v_cvt_pk_bf16_f32 v203, v204, v205\n"
      "v_permlane16_swap_b32 v222, v124\n"
      "v_mfma_f32_16x16x16_bf16 v[138:141], v[188:189], v[218:219], v[138:141]\n"
      "v_mul_f32 v180, v210, v180\n"
      "v_cvt_pk_bf16_f32 v205, v128, v129\n"
      "v_add_f32_e64 v222, v222, v124\n"
      "v_mul_f32_e64 v105, v210, v105\n"
      "v_exp_f32_e64 v127, v127\n"
      "v_pk_mul_f32 v[120:121], v[120:121], v[210:211] op_sel_hi:[1,0]\n"
      "v_pk_mul_f32 v[182:183], v[182:183], v[210:211] op_sel_hi:[1,0]\n"
      "v_pk_mul_f32 v[122:123], v[122:123], v[210:211] op_sel_hi:[1,0]\n"
      "v_mfma_f32_16x16x16_bf16 v[206:209], v[188:189], v[192:193], v[206:209]\n"
      "v_exp_f32 v114, v114\n"
      "s_waitcnt lgkmcnt(1)\n"
      "v_mfma_f32_16x16x32_bf16 v[108:111], a[228:231], v[176:179], v[108:111]\n"
      "v_cndmask_b32 v172, v172, v100, s[58:59]\n"
      "v_cvt_pk_bf16_f32 v90, v114, 0\n"
      "v_mfma_f32_16x16x32_bf16 v[108:111], a[232:235], v[148:151], v[108:111]\n"
      "s_waitcnt lgkmcnt(0)\n"
      "v_cvt_pk_bf16_f32 v120, v120, v121\n"
      "v_cndmask_b32 v172, v172, v138, s[60:61]\n"
      "v_mfma_f32_16x16x32_bf16 v[108:111], a[236:239], v[156:159], v[108:111]\n"
      "v_cndmask_b32 v172, v172, v206, s[62:63]\n"
      "v_fmac_f32_e64 v172, s53, v142\n"
      "v_mfma_f32_16x16x32_bf16 v[116:119], a[252:255], v[176:179], v[116:119]\n"
      "s_nop 0\n"
      "v_max_f32_dpp v189, v172, v172 row_ror:8 row_mask:0xf bank_mask:0xf\n"
      "v_cvt_pk_bf16_f32 v88, v198, v199\n"
      "v_mfma_f32_16x16x32_bf16 v[116:119], v[224:227], v[148:151], v[116:119]\n"
      "v_cvt_pk_bf16_f32 v121, v122, v123\n"
      "v_fma_f32 v174, v108, v108, s52\n"
      "v_mfma_f32_16x16x32_bf16 v[116:119], v[228:231], v[156:159], v[116:119]\n"
      "v_mul_f32 v181, v210, v181\n"
      "v_max_f32_dpp v189, v189, v189 row_ror:4 row_mask:0xf bank_mask:0xf\n"
      "v_mfma_f32_16x16x32_bf16 v[96:99], v[244:247], v[176:179], v[96:99]\n"
      "s_nop 0\n"
      "v_max_f32_dpp v189, v189, v189 row_ror:2 row_mask:0xf bank_mask:0xf\n"
      "v_cvt_pk_bf16_f32 v123, v182, v183\n"
      "v_mfma_f32_16x16x32_bf16 v[96:99], v[248:251], v[148:151], v[96:99]\n"
      "v_cvt_pk_bf16_f32 v204, v126, v127\n"
      "v_max_f32_dpp v189, v189, v189 row_ror:1 row_mask:0xf bank_mask:0xf\n"
      "v_mfma_f32_16x16x32_bf16 v[96:99], v[252:255], v[156:159], v[96:99]\n"
      "s_nop 0\n"
      "v_max_f32_dpp v189, v189, v189 row_bcast:15 row_mask:0xa bank_mask:0xf\n"
      "v_cvt_pk_bf16_f32 v122, v180, v181\n"
      "v_mfma_f32_16x16x32_bf16 v[148:151], v[32:35], v[202:205], 0\n"
      "v_mul_f32 v175, v109, v109\n"
      "v_max_f32_dpp v189, v189, v189 row_bcast:31 row_mask:0xc bank_mask:0xf\n"
      "v_mfma_f32_16x16x32_bf16 v[148:151], v[32:35], v[88:91], v[148:151]\n"
      "v_cvt_pk_bf16_f32 v104, v104, v105\n"
      "v_readlane_b32 s72, v189, 63\n"
      "v_mfma_f32_16x16x32_bf16 v[178:181], v[214:217], v[202:205], 0\n"
      "ds_write_b128 v3, v[120:123] offset:256\n"
      "v_pk_mul_f32 v[106:107], v[106:107], v[210:211] op_sel_hi:[1,0]\n"
      "v_pk_fma_f32 v[174:175], v[110:111], v[110:111], v[174:175]\n"
      "v_pk_fma_f32 v[174:175], v[116:117], v[116:117], v[174:175]\n"
      "v_mfma_f32_16x16x32_bf16 v[178:181], v[76:79], v[88:91], v[178:181]\n"
      "v_subrev_f32 v172, s72, v172\n"
      "v_cvt_pk_bf16_f32 v105, v106, v107\n"
      "v_mfma_f32_16x16x32_bf16 v[120:123], v[130:133], v[202:205], 0\n"
      "s_and_b32 s5, s84, s8\n"
      "v_rcp_f32_e64 v148, v148\n"
      "v_mfma_f32_16x16x32_bf16 v[120:123], v[80:83], v[88:91], v[120:123]\n"
      "ds_write_b64 v4, v[104:105] offset:256\n"
      "v_exp_f32_e64 v172, v172\n"
      "v_pk_fma_f32 v[174:175], v[118:119], v[118:119], v[174:175]\n"
      "v_pk_mul_f32 v[180:181], v[180:181], v[148:149] op_sel_hi:[1,0]\n"
      "v_cvt_pk_bf16_f32 v172, v172, v172\n"
      "v_pk_mul_f32 v[178:179], v[178:179], v[148:149] op_sel_hi:[1,0]\n"
      "v_pk_fma_f32 v[174:175], v[96:97], v[96:97], v[174:175]\n"
      "v_pk_fma_f32 v[174:175], v[98:99], v[98:99], v[174:175]\n"
      "v_cvt_pk_bf16_f32 v178, v178, v179\n"
      "v_pk_mul_f32 v[120:121], v[120:121], v[148:149] op_sel_hi:[1,0]\n"
      "v_cvt_pk_bf16_f32 v179, v180, v181\n"
      "v_pk_mul_f32 v[122:123], v[122:123], v[148:149] op_sel_hi:[1,0]\n"
      "v_mfma_f32_16x16x32_bf16 v[188:191], v[194:197], v[202:205], 0\n"
      "ds_write_b16 v13, v172\n"
      "v_cvt_pk_bf16_f32 v180, v120, v121\n"
      "v_mfma_f32_16x16x32_bf16 v[188:191], v[84:87], v[88:91], v[188:191]\n"
      "ds_read_b128 v[210:213], v14\n"
      "v_add_f32 v174, v174, v175\n"
      "v_cvt_pk_bf16_f32 v181, v122, v123\n"
      "v_mov_b32 v124, v174\n"
      "s_add_u32 s42, s42, 1\n"
      "ds_write_b128 v3, v[178:181] offset:512\n"
      "v_permlane16_swap_b32 v174, v124\n"
      "v_add_f32_e64 v174, v174, v124\n"
      "v_pk_mul_f32 v[188:189], v[188:189], v[148:149] op_sel_hi:[1,0]\n"
      "v_pk_mul_f32 v[190:191], v[190:191], v[148:149] op_sel_hi:[1,0]\n"
      "v_permlane32_swap_b32 v222, v174\n"
      "v_add_f32_e64 v222, v222, v174\n"
      "s_add_u32 s43, s43, 1\n"
      "ds_write_b32 v31, v222\n"
      "v_cvt_pk_bf16_f32 v188, v188, v189\n"
      "v_cvt_pk_bf16_f32 v189, v190, v191\n"
      "ds_read_b128 v[172:175], v14 offset:64\n"
      "ds_write_b64 v4, v[188:189] offset:512\n"
      "s_waitcnt lgkmcnt(4)\n"
      "v_mfma_f32_16x16x32_bf16 v[188:191], v[210:213], v[32:35], 0\n"
      "s_waitcnt lgkmcnt(1)\n"
      "v_mfma_f32_16x16x32_bf16 v[188:191], v[172:175], v[32:35], v[188:191]\n"
      "s_cmp_ge_u32 s95, 0x40000000\n"
      "s_cselect_b64 vcc, -1, s[56:57]\n"
      "s_waitcnt lgkmcnt(0)\n"
      "s_barrier\n"
      "s_cbranch_vccz L_sel1_%=\n"
      "s_branch L_disp1_%=\n"
      "L_tail1_%=:\n"
      "s_nop 4\n"
      "ds_read_b128 v[138:141], v9\n"
      "v_mfma_f32_16x16x32_bf16 v[120:123], v[210:213], v[214:217], 0\n"
      "ds_read_b128 v[214:217], v2\n"
      "ds_read_b128 v[202:205], v2 offset:15360\n"
      "v_mfma_f32_16x16x32_bf16 v[120:123], v[172:175], v[76:79], v[120:123]\n"
      "ds_read_b128 v[104:107], v2 offset:256\n"
      "ds_read_b128 v[206:209], v2 offset:6144\n"
      "v_mfma_f32_16x16x32_bf16 v[148:151], v[210:213], v[130:133], 0\n"
      "ds_read_b128 v[130:133], v2 offset:3072\n"
      "v_ashrrev_i32 v126, 31, v37\n"
      "v_mfma_f32_16x16x32_bf16 v[148:151], v[172:175], v[80:83], v[148:151]\n"
      "ds_read_b128 v[156:159], v2 offset:12288\n"
      "ds_read_b128 v[198:201], v7\n"
      "v_mfma_f32_16x16x32_bf16 v[178:181], v[210:213], v[194:197], 0\n"
      "ds_read_b128 v[194:197], v2 offset:9216\n"
      "s_waitcnt lgkmcnt(8)\n"
      "v_add_f32 v140, v140, v141\n"
      "v_add_f32 v138, v138, v139\n"
      "v_add_f32 v138, v138, v140\n"
      "v_rsq_f32_e64 v138, v138\n"
      "v_mfma_f32_16x16x32_bf16 v[178:181], v[172:175], v[84:87], v[178:181]\n"
      "s_waitcnt lgkmcnt(7)\n"
      "v_mul_f32 v138, 0x415db3d7, v138\n"
      "v_mov_b32 v140, v138\n"
      "s_nop 1\n"
      "v_permlane32_swap_b32 v138, v140\n"
      "v_mov_b32 v139, v138\n"
      "v_pk_mul_f32 v[108:109], v[108:109], v[140:141] op_sel_hi:[1,0]\n"
      "v_pk_mul_f32 v[118:119], v[118:119], v[140:141] op_sel_hi:[1,0]\n"
      "v_permlane16_swap_b32 v138, v139\n"
      "v_pk_mul_f32 v[116:117], v[116:117], v[140:141] op_sel_hi:[1,0]\n"
      "v_pk_mul_f32 v[184:185], v[184:185], v[138:139] op_sel_hi:[1,0]\n"
      "v_pk_mul_f32 v[168:169], v[168:169], v[138:139] op_sel_hi:[1,0]\n"
      "v_pk_mul_f32 v[110:111], v[110:111], v[140:141] op_sel_hi:[1,0]\n"
      "v_pk_mul_f32 v[162:163], v[162:163], v[138:139] op_sel:[0,1]\n"
      "v_pk_mul_f32 v[166:167], v[166:167], v[138:139] op_sel:[0,1]\n"
      "v_pk_mul_f32 v[186:187], v[186:187], v[138:139] op_sel_hi:[1,0]\n"
      "v_pk_mul_f32 v[154:155], v[154:155], v[138:139] op_sel_hi:[1,0]\n"
      "v_cvt_pk_bf16_f32 v168, v168, v169\n"
      "v_cvt_pk_bf16_f32 v108, v108, v109\n"
      "v_cvt_pk_bf16_f32 v184, v184, v185\n"
      "v_pk_mul_f32 v[160:161], v[160:161], v[138:139] op_sel:[0,1]\n"
      "v_pk_mul_f32 v[98:99], v[98:99], v[140:141] op_sel_hi:[1,0]\n"
      "v_pk_mul_f32 v[134:135], v[134:135], v[138:139] op_sel:[0,1]\n"
      "v_cvt_pk_bf16_f32 v109, v110, v111\n"
      "v_cvt_pk_bf16_f32 v134, v134, v135\n"
      "v_pk_mul_f32 v[152:153], v[152:153], v[138:139] op_sel_hi:[1,0]\n"
      "v_cvt_pk_bf16_f32 v185, v186, v187\n"
      "v_cvt_pk_bf16_f32 v160, v160, v161\n"
      "v_cvt_pk_bf16_f32 v161, v162, v163\n"
      "v_cvt_pk_bf16_f32 v163, v166, v167\n"
      "v_pk_mul_f32 v[136:137], v[136:137], v[138:139] op_sel:[0,1]\n"
      "v_pk_mul_f32 v[170:171], v[170:171], v[138:139] op_sel_hi:[1,0]\n"
      "v_cvt_pk_bf16_f32 v135, v136, v137\n"
      "v_cvt_pk_bf16_f32 v111, v118, v119\n"
      "v_pk_mul_f32 v[164:165], v[164:165], v[138:139] op_sel:[0,1]\n"
      "v_cvt_pk_bf16_f32 v110, v116, v117\n"
      "v_pk_mul_f32 v[96:97], v[96:97], v[140:141] op_sel_hi:[1,0]\n"
      "v_cvt_pk_bf16_f32 v96, v96, v97\n"
      "v_cvt_pk_bf16_f32 v97, v98, v99\n"
      "v_cvt_pk_bf16_f32 v186, v152, v153\n"
      "v_cvt_pk_bf16_f32 v162, v164, v165\n"
      "v_cvt_pk_bf16_f32 v169, v170, v171\n"
      "v_cvt_pk_bf16_f32 v187, v154, v155\n"
      "s_mul_i32 s92, s40, 3072\n"
      "ds_write_b128 v5, v[160:163] offset:24576\n"
      "ds_write_b64 v6, v[96:97] offset:30720\n"
      "ds_write_b128 v5, v[108:111] offset:30720\n"
      "ds_write_b128 v5, v[184:187] offset:18432\n"
      "ds_write_b64 v6, v[134:135] offset:24576\n"
      "ds_write_b64 v6, v[168:169] offset:18432\n"
      "v_mfma_f32_16x16x32_bf16 v[184:187], a[216:219], v[214:217], 0\n"
      "ds_read_b128 v[210:213], v2 offset:12800\n"
      "s_waitcnt lgkmcnt(5)\n"
      "v_mfma_f32_16x16x32_bf16 v[184:187], a[220:223], v[130:133], v[184:187]\n"
      "buffer_store_dwordx4 v[198:201], v20, s[24:27], s85 offen sc1\n"
      "v_mfma_f32_16x16x32_bf16 v[152:155], a[240:243], v[214:217], 0\n"
      "v_subrev_u32 v84, 115456, v9\n"
      "v_mfma_f32_16x16x32_bf16 v[152:155], a[244:247], v[130:133], v[152:155]\n"
      "ds_read_b128 v[108:111], v2 offset:6400\n"
      "v_and_b32 v85, 0x3fffffff, v37\n"
      "v_mfma_f32_16x16x32_bf16 v[168:171], v[232:235], v[214:217], 0\n"
      "ds_read_b128 v[116:119], v2 offset:3328\n"
      "v_mfma_f32_16x16x32_bf16 v[168:171], v[236:239], v[130:133], v[168:171]\n"
      "ds_read_b128 v[96:99], v7 offset:8192\n"
      "v_cndmask_b32 v120, v120, v148, s[58:59]\n"
      "v_mfma_f32_16x16x32_bf16 v[184:187], a[224:227], v[206:209], v[184:187]\n"
      "ds_read_b128 v[130:133], v7 offset:12288\n"
      "v_add_u32 v85, 1179648, v85\n"
      "v_mfma_f32_16x16x32_bf16 v[184:187], a[228:231], v[194:197], v[184:187]\n"
      "ds_read_b128 v[214:217], v2 offset:3584\n"
      "v_subrev_u32 v199, 115456, v9\n"
      "v_mfma_f32_16x16x32_bf16 v[152:155], a[248:251], v[206:209], v[152:155]\n"
      "v_cndmask_b32 v120, v120, v178, s[60:61]\n"
      "v_and_b32 v126, 100270464, v126\n"
      "v_mfma_f32_16x16x32_bf16 v[152:155], a[252:255], v[194:197], v[152:155]\n"
      "v_and_b32 v134, 0x300, v84\n"
      "v_add_u32 v86, s92, v199\n"
      "v_mfma_f32_16x16x32_bf16 v[168:171], v[240:243], v[206:209], v[168:171]\n"
      "v_add_u32_e64 v84, v84, v134\n"
      "v_mfma_f32_16x16x32_bf16 v[168:171], v[244:247], v[194:197], v[168:171]\n"
      "ds_read_b128 v[134:137], v7 offset:4096\n"
      "v_mfma_f32_16x16x32_bf16 v[184:187], a[232:235], v[156:159], v[184:187]\n"
      "v_lshrrev_b32_e64 v199, 4, v86\n"
      "v_add_u32 v84, 150784, v84\n"
      "v_mfma_f32_16x16x32_bf16 v[184:187], a[236:239], v[202:205], v[184:187]\n"
      "ds_read_b128 v[194:197], v2 offset:15616\n"
      "v_add_u32_e64 v76, 64, v199\n"
      "v_mfma_f32_16x16x32_bf16 v[152:155], v[224:227], v[156:159], v[152:155]\n"
      "v_mfma_f32_16x16x32_bf16 v[152:155], v[228:231], v[202:205], v[152:155]\n"
      "ds_read_b128 v[148:151], v2 offset:9472\n"
      "v_mov_b32_dpp v188, v188 quad_perm:[0,0,0,0] row_mask:0xf bank_mask:0xf\n"
      "v_mfma_f32_16x16x32_bf16 v[168:171], v[248:251], v[156:159], v[168:171]\n"
      "ds_read_b128 v[172:175], v2 offset:12544\n"
      "v_fma_f32 v156, v184, v184, s52\n"
      "v_mfma_f32_16x16x32_bf16 v[168:171], v[252:255], v[202:205], v[168:171]\n"
      "ds_read_b128 v[178:181], v84 offset:2048\n"
      "v_add_u32 v82, 128, v199\n"
      "v_mfma_f32_16x16x32_bf16 v[160:163], a[216:219], v[104:107], 0\n"
      "s_waitcnt lgkmcnt(4)\n"
      "v_mul_f32 v157, v185, v185\n"
      "v_mul_u32_u24 v81, 2731, v76\n"
      "v_mfma_f32_16x16x32_bf16 v[160:163], a[220:223], v[116:119], v[160:163]\n"
      "v_rcp_f32_e64 v188, v188\n"
      "v_mfma_f32_16x16x32_bf16 v[164:167], a[240:243], v[104:107], 0\n"
      "buffer_store_dwordx4 v[134:137], v21, s[24:27], s85 offen sc1\n"
      "v_mfma_f32_16x16x32_bf16 v[164:167], a[244:247], v[116:119], v[164:167]\n"
      "v_mul_f32_e64 v120, v188, v120\n"
      "v_mfma_f32_16x16x32_bf16 v[134:137], v[232:235], v[104:107], 0\n"
      "ds_read_b128 v[188:191], v2 offset:6656\n"
      "v_mfma_f32_16x16x32_bf16 v[134:137], v[236:239], v[116:119], v[134:137]\n"
      "s_waitcnt lgkmcnt(1)\n"
      "v_fmac_f32 v157, v187, v187\n"
      "v_fmac_f32_e64 v156, v186, v186\n"
      "v_mfma_f32_16x16x32_bf16 v[160:163], a[224:227], v[108:111], v[160:163]\n"
      "ds_read_b128 v[122:125], v2 offset:512\n"
      "v_mfma_f32_16x16x32_bf16 v[160:163], a[228:231], v[148:151], v[160:163]\n"
      "v_mul_u32_u24 v77, 2731, v82\n"
      "v_mfma_f32_16x16x32_bf16 v[164:167], a[248:251], v[108:111], v[164:167]\n"
      "ds_read_b128 v[204:207], v2 offset:15872\n"
      "v_mfma_f32_16x16x32_bf16 v[164:167], a[252:255], v[148:151], v[164:167]\n"
      "ds_read_b128 v[104:107], v84 offset:8192\n"
      "v_mfma_f32_16x16x32_bf16 v[134:137], v[240:243], v[108:111], v[134:137]\n"
      "v_cvt_pk_bf16_f32 v120, v120, v120\n"
      "v_lshrrev_b32 v81, 16, v81\n"
      "v_mfma_f32_16x16x32_bf16 v[134:137], v[244:247], v[148:151], v[134:137]\n"
      "v_fmac_f32 v156, v152, v152\n"
      "v_fmac_f32_e64 v157, v153, v153\n"
      "v_mfma_f32_16x16x32_bf16 v[160:163], a[232:235], v[172:175], v[160:163]\n"
      "ds_read_b128 v[148:151], v84\n"
      "v_mfma_f32_16x16x32_bf16 v[160:163], a[236:239], v[194:197], v[160:163]\n"
      "buffer_store_dwordx4 v[96:99], v22, s[24:27], s85 offen sc1\n"
      "v_mfma_f32_16x16x32_bf16 v[164:167], v[224:227], v[172:175], v[164:167]\n"
      "v_sub_u32 v85, v85, v126\n"
      "v_fmac_f32 v156, v154, v154\n"
      "v_mfma_f32_16x16x32_bf16 v[164:167], v[228:231], v[194:197], v[164:167]\n"
      "ds_write_b16 v12, v120\n"
      "v_mfma_f32_16x16x32_bf16 v[134:137], v[248:251], v[172:175], v[134:137]\n"
      "s_nop 0\n"
      "v_mul_f32 v159, v161, v161\n"
      "v_fma_f32 v158, v160, v160, s52\n"
      "v_mfma_f32_16x16x32_bf16 v[134:137], v[252:255], v[194:197], v[134:137]\n"
      "s_waitcnt lgkmcnt(1)\n"
      "v_fmac_f32 v158, v162, v162\n"
      "v_fmac_f32_e64 v159, v163, v163\n"
      "v_mfma_f32_16x16x32_bf16 v[108:111], a[216:219], v[122:125], 0\n"
      "ds_read_b128 v[194:197], v2 offset:9728\n"
      "v_fmac_f32 v158, v164, v164\n"
      "v_mfma_f32_16x16x32_bf16 v[108:111], a[220:223], v[214:217], v[108:111]\n"
      "ds_read_b128 v[172:175], v84 offset:4096\n"
      "v_fmac_f32 v159, v165, v165\n"
      "v_mfma_f32_16x16x32_bf16 v[116:119], a[240:243], v[122:125], 0\n"
      "v_mfma_f32_16x16x32_bf16 v[116:119], a[244:247], v[214:217], v[116:119]\n"
      "buffer_store_dwordx4 v[130:133], v23, s[24:27], s85 offen sc1\n"
      "v_mfma_f32_16x16x32_bf16 v[96:99], v[232:235], v[122:125], 0\n"
      "v_fmac_f32 v157, v155, v155\n"
      "v_fmac_f32 v156, v168, v168\n"
      "v_mfma_f32_16x16x32_bf16 v[96:99], v[236:239], v[214:217], v[96:99]\n"
      "v_fmac_f32 v159, v167, v167\n"
      "v_fmac_f32 v157, v169, v169\n"
      "v_mfma_f32_16x16x32_bf16 v[108:111], a[224:227], v[188:191], v[108:111]\n"
      "s_waitcnt lgkmcnt(0)\n"
      "v_fmac_f32 v158, v166, v166\n"
      "v_fmac_f32 v156, v170, v170\n"
      "v_mfma_f32_16x16x32_bf16 v[108:111], a[228:231], v[194:197], v[108:111]\n"
      "ds_read_b128 v[214:217], v84 offset:10240\n"
      "v_mul_u32_u24 v78, 24, v81\n"
      "v_mfma_f32_16x16x32_bf16 v[116:119], a[248:251], v[188:191], v[116:119]\n"
      "v_fmac_f32 v157, v171, v171\n"
      "v_fmac_f32 v159, v135, v135\n"
      "v_mfma_f32_16x16x32_bf16 v[116:119], a[252:255], v[194:197], v[116:119]\n"
      "v_add_u32_e64 v80, 0, v199\n"
      "v_mfma_f32_16x16x32_bf16 v[96:99], v[240:243], v[188:191], v[96:99]\n"
      "v_add_f32 v156, v156, v157\n"
      "v_mfma_f32_16x16x32_bf16 v[96:99], v[244:247], v[194:197], v[96:99]\n"
      "v_fmac_f32 v158, v134, v134\n"
      "v_lshrrev_b32_e64 v77, 16, v77\n"
      "v_mfma_f32_16x16x32_bf16 v[108:111], a[232:235], v[210:213], v[108:111]\n"
      "v_fmac_f32 v159, v137, v137\n"
      "v_mfma_f32_16x16x32_bf16 v[108:111], a[236:239], v[204:207], v[108:111]\n"
      "ds_read_b128 v[188:191], v84 offset:6144\n"
      "v_fmac_f32 v158, v136, v136\n"
      "v_mfma_f32_16x16x32_bf16 v[116:119], v[224:227], v[210:213], v[116:119]\n"
      "v_mul_u32_u24 v203, 2731, v80\n"
      "v_add_f32 v158, v158, v159\n"
      "v_mfma_f32_16x16x32_bf16 v[116:119], v[228:231], v[204:207], v[116:119]\n"
      "s_nop 0\n"
      "v_permlane16_swap_b32 v156, v158\n"
      "v_mfma_f32_16x16x32_bf16 v[96:99], v[248:251], v[210:213], v[96:99]\n"
      "v_mul_f32 v199, v109, v109\n"
      "v_fma_f32 v198, v108, v108, s52\n"
      "v_mfma_f32_16x16x32_bf16 v[96:99], v[252:255], v[204:207], v[96:99]\n"
      "v_fmac_f32 v198, v110, v110\n"
      "v_fmac_f32 v199, v111, v111\n"
      "v_mfma_f32_16x16x32_bf16 v[210:213], a[216:219], v[148:151], 0\n"
      "ds_read_b128 v[194:197], v7 offset:16384\n"
      "v_fmac_f32_e64 v199, v117, v117\n"
      "v_mfma_f32_16x16x32_bf16 v[210:213], a[220:223], v[178:181], v[210:213]\n"
      "v_fmac_f32 v198, v116, v116\n"
      "v_add_f32 v156, v156, v158\n"
      "v_mfma_f32_16x16x32_bf16 v[130:133], a[240:243], v[148:151], 0\n"
      "v_fmac_f32 v199, v119, v119\n"
      "v_fmac_f32 v198, v118, v118\n"
      "v_mfma_f32_16x16x32_bf16 v[130:133], a[244:247], v[178:181], v[130:133]\n"
      "v_fmac_f32 v199, v97, v97\n"
      "v_fmac_f32 v198, v96, v96\n"
      "v_mfma_f32_16x16x32_bf16 v[120:123], v[232:235], v[148:151], 0\n"
      "v_fmac_f32 v199, v99, v99\n"
      "v_fmac_f32 v198, v98, v98\n"
      "v_mfma_f32_16x16x32_bf16 v[120:123], v[236:239], v[178:181], v[120:123]\n"
      "s_waitcnt lgkmcnt(1)\n"
      "v_add_f32 v198, v198, v199\n"
      "v_mov_b32 v158, v198\n"
      "v_mfma_f32_16x16x32_bf16 v[210:213], a[224:227], v[172:175], v[210:213]\n"
      "s_nop 0\n"
      "v_permlane16_swap_b32 v198, v158\n"
      "v_mfma_f32_16x16x32_bf16 v[210:213], a[228:231], v[188:191], v[210:213]\n"
      "v_bfe_i32 v126, v37, 30, 1\n"
      "v_add_f32 v198, v198, v158\n"
      "v_mfma_f32_16x16x32_bf16 v[130:133], a[248:251], v[172:175], v[130:133]\n"
      "s_nop 0\n"
      "v_permlane32_swap_b32 v156, v198\n"
      "v_mfma_f32_16x16x32_bf16 v[130:133], a[252:255], v[188:191], v[130:133]\n"
      "v_and_b32 v126, 196224, v126\n"
      "v_add_f32_e64 v156, v156, v198\n"
      "v_mfma_f32_16x16x32_bf16 v[120:123], v[240:243], v[172:175], v[120:123]\n"
      "ds_write_b32 v31, v156 offset:1024\n"
      "v_mfma_f32_16x16x32_bf16 v[120:123], v[244:247], v[188:191], v[120:123]\n"
      "s_waitcnt lgkmcnt(1)\n"
      "v_sub_u32 v85, v85, v126\n"
      "v_lshrrev_b32 v203, 16, v203\n"
      "v_mfma_f32_16x16x32_bf16 v[210:213], a[232:235], v[104:107], v[210:213]\n"
      "buffer_store_dwordx4 v[194:197], v25, s[24:27], s85 offen sc1\n"
      "v_mfma_f32_16x16x32_bf16 v[210:213], a[236:239], v[214:217], v[210:213]\n"
      "v_readlane_b32 s85, v37, s42\n"
      "v_mfma_f32_16x16x32_bf16 v[130:133], v[224:227], v[104:107], v[130:133]\n"
      "s_or_b32 s95, s85, s48\n"
      "s_cmp_ge_u32 s95, 0x40000000\n"
      "s_cbranch_scc1 L_rare_27_%=\n"
      "L_back_28_%=:\n"
      "v_mfma_f32_16x16x32_bf16 v[130:133], v[228:231], v[214:217], v[130:133]\n"
      "s_waitcnt lgkmcnt(0)\n"
      "s_barrier\n"
      "ds_read_b128 v[188:191], v84 offset:256\n"
      "ds_read_b128 v[194:197], v9 offset:1024\n"
      "ds_read_b128 v[172:175], v84 offset:2304\n"
      "ds_read_b128 v[148:151], v7 offset:18432\n"
      "v_fma_f32 v124, v210, v210, s52\n"
      "v_mul_f32_e64 v125, v211, v211\n"
      "v_pk_fma_f32 v[124:125], v[212:213], v[212:213], v[124:125]\n"
      "v_pk_fma_f32 v[124:125], v[130:131], v[130:131], v[124:125]\n"
      "v_mfma_f32_16x16x32_bf16 v[120:123], v[248:251], v[104:107], v[120:123]\n"
      "ds_read_b128 v[178:181], v7 offset:34816\n"
      "ds_read_b128 v[104:107], v84 offset:4352\n"
      "ds_read_b128 v[204:207], v7 offset:30720\n"
      "ds_read_b128 v[198:201], v7 offset:22528\n"
      "v_mfma_f32_16x16x32_bf16 v[120:123], v[252:255], v[214:217], v[120:123]\n"
      "ds_read_b128 v[214:217], v84 offset:6400\n"
      "ds_read_b128 v[138:141], v7 offset:26624\n"
      "s_waitcnt lgkmcnt(9)\n"
      "v_mfma_f32_16x16x32_bf16 v[156:159], a[216:219], v[188:191], 0\n"
      "s_waitcnt lgkmcnt(8)\n"
      "v_add_f32 v194, v194, v195\n"
      "v_add_f32 v196, v196, v197\n"
      "v_add_f32 v194, v194, v196\n"
      "v_rsq_f32 v194, v194\n"
      "s_waitcnt lgkmcnt(7)\n"
      "v_mfma_f32_16x16x32_bf16 v[156:159], a[220:223], v[172:175], v[156:159]\n"
      "s_waitcnt lgkmcnt(6)\n"
      "v_mul_f32 v194, 0x415db3d7, v194\n"
      "v_mov_b32 v196, v194\n"
      "v_mfma_f32_16x16x32_bf16 v[126:129], a[240:243], v[188:191], 0\n"
      "ds_read_b128 v[100:103], v84 offset:10496\n"
      "v_permlane32_swap_b32 v194, v196\n"
      "v_mov_b32_e64 v195, v194\n"
      "v_mfma_f32_16x16x32_bf16 v[126:129], a[244:247], v[172:175], v[126:129]\n"
      "buffer_store_dwordx4 v[148:151], v20, s[24:27], s85 offen sc1\n"
      "s_nop 1\n"
      "v_mfma_f32_16x16x32_bf16 v[148:151], v[232:235], v[188:191], 0\n"
      "s_waitcnt lgkmcnt(3)\n"
      "v_permlane16_swap_b32_e64 v194, v195\n"
      "v_mfma_f32_16x16x32_bf16 v[148:151], v[236:239], v[172:175], v[148:151]\n"
      "buffer_store_dwordx4 v[198:201], v21, s[24:27], s85 offen sc1\n"
      "v_pk_mul_f32 v[154:155], v[154:155], v[194:195] op_sel_hi:[1,0]\n"
      "v_pk_fma_f32 v[124:125], v[132:133], v[132:133], v[124:125]\n"
      "v_pk_mul_f32 v[134:135], v[134:135], v[194:195] op_sel:[0,1]\n"
      "v_pk_mul_f32 v[96:97], v[96:97], v[196:197] op_sel_hi:[1,0]\n"
      "v_pk_mul_f32 v[166:167], v[166:167], v[194:195] op_sel:[0,1]\n"
      "v_pk_mul_f32 v[98:99], v[98:99], v[196:197] op_sel_hi:[1,0]\n"
      "v_pk_mul_f32 v[184:185], v[184:185], v[194:195] op_sel_hi:[1,0]\n"
      "v_pk_mul_f32 v[164:165], v[164:165], v[194:195] op_sel:[0,1]\n"
      "v_pk_mul_f32 v[152:153], v[152:153], v[194:195] op_sel_hi:[1,0]\n"
      "v_pk_mul_f32 v[162:163], v[162:163], v[194:195] op_sel:[0,1]\n"
      "v_pk_mul_f32 v[160:161], v[160:161], v[194:195] op_sel:[0,1]\n"
      "v_pk_mul_f32 v[136:137], v[136:137], v[194:195] op_sel:[0,1]\n"
      "v_mfma_f32_16x16x32_bf16 v[156:159], a[224:227], v[104:107], v[156:159]\n"
      "s_waitcnt lgkmcnt(0)\n"
      "v_cvt_pk_bf16_f32 v184, v184, v185\n"
      "v_cvt_pk_bf16_f32 v96, v96, v97\n"
      "v_mfma_f32_16x16x32_bf16 v[156:159], a[228:231], v[214:217], v[156:159]\n"
      "buffer_store_dwordx4 v[138:141], v22, s[24:27], s85 offen sc1\n"
      "v_mfma_f32_16x16x32_bf16 v[126:129], a[248:251], v[104:107], v[126:129]\n"
      "ds_read_b128 v[188:191], v84 offset:8448\n"
      "v_cvt_pk_bf16_f32 v160, v160, v161\n"
      "v_mfma_f32_16x16x32_bf16 v[126:129], a[252:255], v[214:217], v[126:129]\n"
      "buffer_store_dwordx4 v[204:207], v23, s[24:27], s85 offen sc1\n"
      "v_pk_mul_f32 v[118:119], v[118:119], v[196:197] op_sel_hi:[1,0]\n"
      "v_pk_mul_f32 v[170:171], v[170:171], v[194:195] op_sel_hi:[1,0]\n"
      "v_pk_fma_f32 v[124:125], v[120:121], v[120:121], v[124:125]\n"
      "v_pk_mul_f32 v[108:109], v[108:109], v[196:197] op_sel_hi:[1,0]\n"
      "v_cvt_pk_bf16_f32 v134, v134, v135\n"
      "v_pk_mul_f32 v[186:187], v[186:187], v[194:195] op_sel_hi:[1,0]\n"
      "v_pk_mul_f32 v[168:169], v[168:169], v[194:195] op_sel_hi:[1,0]\n"
      "v_cvt_pk_bf16_f32 v161, v162, v163\n"
      "v_cvt_pk_bf16_f32 v162, v164, v165\n"
      "v_pk_fma_f32 v[124:125], v[122:123], v[122:123], v[124:125]\n"
      "v_mfma_f32_16x16x32_bf16 v[148:151], v[240:243], v[104:107], v[148:151]\n"
      "v_cvt_pk_bf16_f32 v185, v186, v187\n"
      "v_cvt_pk_bf16_f32 v135, v136, v137\n"
      "v_mfma_f32_16x16x32_bf16 v[148:151], v[244:247], v[214:217], v[148:151]\n"
      "s_waitcnt lgkmcnt(0)\n"
      "v_cvt_pk_bf16_f32 v163, v166, v167\n"
      "v_cvt_pk_bf16_f32 v97, v98, v99\n"
      "v_mfma_f32_16x16x32_bf16 v[156:159], a[232:235], v[188:191], v[156:159]\n"
      "ds_write_b128 v5, v[160:163] offset:6144\n"
      "v_mfma_f32_16x16x32_bf16 v[156:159], a[236:239], v[100:103], v[156:159]\n"
      "ds_write_b64 v6, v[134:135] offset:6144\n"
      "v_cvt_pk_bf16_f32 v168, v168, v169\n"
      "v_cvt_pk_bf16_f32 v186, v152, v153\n"
      "v_mfma_f32_16x16x32_bf16 v[126:129], v[224:227], v[188:191], v[126:129]\n"
      "v_cvt_pk_bf16_f32 v187, v154, v155\n"
      "v_cvt_pk_bf16_f32 v169, v170, v171\n"
      "v_mfma_f32_16x16x32_bf16 v[126:129], v[228:231], v[100:103], v[126:129]\n"
      "ds_write_b64 v6, v[96:97] offset:12288\n"
      "v_mul_f32_e64 v171, v157, v157\n"
      "v_mfma_f32_16x16x32_bf16 v[148:151], v[248:251], v[188:191], v[148:151]\n"
      "ds_write_b64 v6, v[168:169]\n"
      "v_mfma_f32_16x16x32_bf16 v[148:151], v[252:255], v[100:103], v[148:151]\n"
      "ds_write_b128 v5, v[184:187]\n"
      "v_pk_mul_f32 v[110:111], v[110:111], v[196:197] op_sel_hi:[1,0]\n"
      "v_fma_f32 v170, v156, v156, s52\n"
      "v_pk_fma_f32 v[170:171], v[158:159], v[158:159], v[170:171]\n"
      "v_pk_fma_f32 v[170:171], v[126:127], v[126:127], v[170:171]\n"
      "v_pk_fma_f32 v[170:171], v[128:129], v[128:129], v[170:171]\n"
      "v_cvt_pk_bf16_f32 v108, v108, v109\n"
      "v_add_f32 v124, v124, v125\n"
      "v_pk_fma_f32 v[170:171], v[148:149], v[148:149], v[170:171]\n"
      "v_pk_fma_f32 v[170:171], v[150:151], v[150:151], v[170:171]\n"
      "v_add_f32 v170, v170, v171\n"
      "v_cvt_pk_bf16_f32 v109, v110, v111\n"
      "v_pk_mul_f32 v[116:117], v[116:117], v[196:197] op_sel_hi:[1,0]\n"
      "v_permlane16_swap_b32 v124, v170\n"
      "v_add_f32 v124, v124, v170\n"
      "v_mov_b32_e64 v170, v124\n"
      "v_cvt_pk_bf16_f32 v110, v116, v117\n"
      "v_cvt_pk_bf16_f32 v111, v118, v119\n"
      "v_permlane32_swap_b32 v124, v170\n"
      "v_add_f32_e64 v124, v124, v170\n"
      "s_add_u32 s42, s42, 1\n"
      "ds_write_b128 v5, v[108:111] offset:12288\n"
      "ds_write_b32 v31, v124 offset:4608\n"
      "buffer_store_dwordx4 v[178:181], v25, s[24:27], s85 offen sc1\n"
      "v_mul_u32_u24 v196, 24, v77\n"
      "v_sub_u32 v196, v82, v196\n"
      "v_mul_u32_u24 v124, 24, v203\n"
      "v_sub_u32 v78, v76, v78\n"
      "v_readlane_b32 s85, v37, s42\n"
      "v_and_b32 v76, 7, v81\n"
      "v_lshl_add_u32 v81, v81, 2, 12\n"
      "v_and_b32 v82, 7, v77\n"
      "ds_bpermute_b32 v125, v81, v85\n"
      "v_subrev_u32 v81, 73728, v5\n"
      "v_lshl_add_u32 v77, v77, 2, 12\n"
      "s_or_b32 s95, s85, s48\n"
      "v_sub_u32 v124, v80, v124\n"
      "s_waitcnt lgkmcnt(1)\n"
      "s_barrier\n"
      "ds_read_b128 v[108:111], v9 offset:4608\n"
      "ds_read_b128 v[116:119], v7 offset:4096\n"
      "ds_read_b128 v[188:191], v7\n"
      "v_subrev_u32 v96, 73728, v6\n"
      "v_and_b32 v80, 7, v203\n"
      "ds_read_b128 v[184:187], v7 offset:8192\n"
      "ds_bpermute_b32 v97, v77, v85\n"
      "v_xor_b32 v78, v78, v76\n"
      "v_lshl_add_u32 v203, v203, 2, 12\n"
      "s_cmp_ge_u32 s95, 0x40000000\n"
      "s_cbranch_scc1 L_rare_31_%=\n"
      "L_back_32_%=:\n"
      "ds_read_b128 v[168:171], v7 offset:12288\n"
      "s_waitcnt lgkmcnt(5)\n"
      "v_add_f32 v108, v108, v109\n"
      "v_add_f32 v110, v110, v111\n"
      "v_add_f32 v108, v108, v110\n"
      "v_rsq_f32 v108, v108\n"
      "s_nop 0\n"
      "v_mul_f32 v108, 0x415db3d7, v108\n"
      "v_mov_b32 v110, v108\n"
      "s_waitcnt lgkmcnt(3)\n"
      "buffer_store_dwordx4 v[188:191], v20, s[24:27], s85 offen sc1\n"
      "v_permlane32_swap_b32 v108, v110\n"
      "v_mov_b32 v109, v108\n"
      "s_nop 1\n"
      "v_permlane16_swap_b32 v108, v109\n"
      "v_pk_mul_f32 v[156:157], v[156:157], v[108:109] op_sel:[0,1]\n"
      "v_pk_mul_f32 v[130:131], v[130:131], v[108:109] op_sel_hi:[1,0]\n"
      "v_pk_mul_f32 v[122:123], v[122:123], v[108:109] op_sel_hi:[1,0]\n"
      "v_pk_mul_f32 v[210:211], v[210:211], v[108:109] op_sel_hi:[1,0]\n"
      "v_pk_mul_f32 v[212:213], v[212:213], v[108:109] op_sel_hi:[1,0]\n"
      "v_pk_mul_f32 v[120:121], v[120:121], v[108:109] op_sel_hi:[1,0]\n"
      "v_cvt_pk_bf16_f32 v210, v210, v211\n"
      "v_pk_mul_f32 v[128:129], v[128:129], v[108:109] op_sel:[0,1]\n"
      "v_pk_mul_f32 v[150:151], v[150:151], v[108:109] op_sel:[0,1]\n"
      "v_cvt_pk_bf16_f32 v211, v212, v213\n"
      "v_pk_mul_f32 v[132:133], v[132:133], v[108:109] op_sel_hi:[1,0]\n"
      "v_pk_mul_f32 v[126:127], v[126:127], v[108:109] op_sel:[0,1]\n"
      "v_cvt_pk_bf16_f32 v212, v130, v131\n"
      "v_pk_mul_f32 v[158:159], v[158:159], v[108:109] op_sel:[0,1]\n"
      "v_cvt_pk_bf16_f32 v120, v120, v121\n"
      "v_cvt_pk_bf16_f32 v213, v132, v133\n"
      "ds_write_b128 v81, v[210:213]\n"
      "v_cvt_pk_bf16_f32 v121, v122, v123\n"
      "v_cvt_pk_bf16_f32 v156, v156, v157\n"
      "v_pk_mul_f32 v[148:149], v[148:149], v[108:109] op_sel:[0,1]\n"
      "v_cvt_pk_bf16_f32 v157, v158, v159\n"
      "v_cvt_pk_bf16_f32 v158, v126, v127\n"
      "v_cvt_pk_bf16_f32 v159, v128, v129\n"
      "s_add_u32 s42, s42, 1\n"
      "ds_write_b128 v81, v[156:159] offset:6144\n"
      "ds_write_b64 v96, v[120:121]\n"
      "v_cvt_pk_bf16_f32 v148, v148, v149\n"
      "v_cvt_pk_bf16_f32 v149, v150, v151\n"
      "ds_write_b64 v96, v[148:149] offset:6144\n"
      "ds_read_b128 v[210:213], v7 offset:16384\n"
      "v_xor_b32_e64 v196, v196, v82\n"
      "buffer_store_dwordx4 v[116:119], v21, s[24:27], s85 offen sc1\n"
      "s_waitcnt lgkmcnt(4)\n"
      "v_lshl_add_u32 v97, v196, 4, v97\n"
      "ds_bpermute_b32 v196, v203, v85\n"
      "buffer_store_dwordx4 v[184:187], v22, s[24:27], s85 offen sc1\n"
      "s_waitcnt lgkmcnt(2)\n"
      "s_barrier\n"
      "ds_read_b128 v[184:187], v86\n"
      "buffer_store_dwordx4 v[168:171], v23, s[24:27], s85 offen sc1\n"
      "s_nop 1\n"
      "ds_read_b128 v[168:171], v86 offset:1024\n"
      "v_xor_b32_e64 v124, v124, v80\n"
      "s_waitcnt lgkmcnt(2)\n"
      "buffer_store_dwordx4 v[210:213], v25, s[24:27], s85 offen sc1\n"
      "s_nop 1\n"
      "ds_read_b128 v[210:213], v86 offset:2048\n"
      "v_lshl_add_u32 v125, v78, 4, v125\n"
      "v_lshl_add_u32 v196, v124, 4, v196\n"
      "s_waitcnt lgkmcnt(2)\n"
      "buffer_store_dwordx4 v[184:187], v196, s[24:27], 0 offen sc1\n"
      "s_waitcnt lgkmcnt(1)\n"
      "buffer_store_dwordx4 v[168:171], v125, s[24:27], 0 offen sc1\n"
      "s_waitcnt lgkmcnt(0)\n"
      "buffer_store_dwordx4 v[210:213], v97, s[24:27], 0 offen sc1\n"
      "s_endpgm\n"
      "L_tail0_%=:\n"
      "s_nop 4\n"
      "ds_read_b128 v[124:127], v9 offset:1024\n"
      "v_mfma_f32_16x16x32_bf16 v[120:123], v[210:213], v[214:217], 0\n"
      "ds_read_b128 v[214:217], v2 offset:18432\n"
      "ds_read_b128 v[198:201], v2 offset:27648\n"
      "v_mfma_f32_16x16x32_bf16 v[120:123], v[172:175], v[76:79], v[120:123]\n"
      "ds_read_b128 v[104:107], v7 offset:18432\n"
      "v_mfma_f32_16x16x32_bf16 v[148:151], v[210:213], v[130:133], 0\n"
      "ds_read_b128 v[128:131], v2 offset:24576\n"
      "ds_read_b128 v[156:159], v7 offset:34816\n"
      "v_mfma_f32_16x16x32_bf16 v[148:151], v[172:175], v[80:83], v[148:151]\n"
      "ds_read_b128 v[204:207], v2 offset:30720\n"
      "v_mfma_f32_16x16x32_bf16 v[178:181], v[210:213], v[194:197], 0\n"
      "ds_read_b128 v[194:197], v2 offset:21504\n"
      "v_subrev_u32 v212, 115456, v9\n"
      "s_waitcnt lgkmcnt(7)\n"
      "v_add_f32 v124, v124, v125\n"
      "v_add_f32 v126, v126, v127\n"
      "v_add_f32 v124, v124, v126\n"
      "v_rsq_f32 v124, v124\n"
      "v_mfma_f32_16x16x32_bf16 v[178:181], v[172:175], v[84:87], v[178:181]\n"
      "s_waitcnt lgkmcnt(6)\n"
      "v_mul_f32 v124, 0x415db3d7, v124\n"
      "v_mov_b32 v126, v124\n"
      "v_and_b32 v76, 0x3fffffff, v37\n"
      "s_nop 0\n"
      "v_permlane32_swap_b32 v124, v126\n"
      "v_mov_b32 v125, v124\n"
      "v_pk_mul_f32 v[96:97], v[96:97], v[126:127] op_sel_hi:[1,0]\n"
      "v_pk_mul_f32 v[110:111], v[110:111], v[126:127] op_sel_hi:[1,0]\n"
      "v_pk_mul_f32 v[116:117], v[116:117], v[126:127] op_sel_hi:[1,0]\n"
      "v_permlane16_swap_b32_e64 v124, v125\n"
      "v_pk_mul_f32 v[186:187], v[186:187], v[124:125] op_sel_hi:[1,0]\n"
      "v_pk_mul_f32 v[152:153], v[152:153], v[124:125] op_sel_hi:[1,0]\n"
      "v_pk_mul_f32 v[134:135], v[134:135], v[124:125] op_sel:[0,1]\n"
      "v_pk_mul_f32 v[164:165], v[164:165], v[124:125] op_sel:[0,1]\n"
      "v_pk_mul_f32 v[160:161], v[160:161], v[124:125] op_sel:[0,1]\n"
      "v_pk_mul_f32 v[118:119], v[118:119], v[126:127] op_sel_hi:[1,0]\n"
      "v_pk_mul_f32 v[108:109], v[108:109], v[126:127] op_sel_hi:[1,0]\n"
      "v_pk_mul_f32 v[98:99], v[98:99], v[126:127] op_sel_hi:[1,0]\n"
      "v_pk_mul_f32 v[136:137], v[136:137], v[124:125] op_sel:[0,1]\n"
      "v_pk_mul_f32 v[184:185], v[184:185], v[124:125] op_sel_hi:[1,0]\n"
      "v_cvt_pk_bf16_f32 v160, v160, v161\n"
      "v_pk_mul_f32 v[162:163], v[162:163], v[124:125] op_sel:[0,1]\n"
      "v_cvt_pk_bf16_f32 v184, v184, v185\n"
      "v_pk_mul_f32 v[154:155], v[154:155], v[124:125] op_sel_hi:[1,0]\n"
      "v_cvt_pk_bf16_f32 v96, v96, v97\n"
      "v_pk_mul_f32 v[170:171], v[170:171], v[124:125] op_sel_hi:[1,0]\n"
      "v_cvt_pk_bf16_f32 v108, v108, v109\n"
      "v_pk_mul_f32 v[168:169], v[168:169], v[124:125] op_sel_hi:[1,0]\n"
      "v_cvt_pk_bf16_f32 v185, v186, v187\n"
      "v_cvt_pk_bf16_f32 v109, v110, v111\n"
      "v_cvt_pk_bf16_f32 v161, v162, v163\n"
      "v_cvt_pk_bf16_f32 v134, v134, v135\n"
      "v_cvt_pk_bf16_f32 v186, v152, v153\n"
      "v_cvt_pk_bf16_f32 v110, v116, v117\n"
      "v_cvt_pk_bf16_f32 v168, v168, v169\n"
      "v_cvt_pk_bf16_f32 v162, v164, v165\n"
      "v_cvt_pk_bf16_f32 v169, v170, v171\n"
      "v_pk_mul_f32 v[166:167], v[166:167], v[124:125] op_sel:[0,1]\n"
      "v_cvt_pk_bf16_f32 v135, v136, v137\n"
      "v_cvt_pk_bf16_f32 v163, v166, v167\n"
      "v_cvt_pk_bf16_f32 v187, v154, v155\n"
      "v_cvt_pk_bf16_f32 v111, v118, v119\n"
      "v_cvt_pk_bf16_f32 v97, v98, v99\n"
      "s_mul_i32 s92, s40, 3072\n"
      "ds_write_b128 v5, v[108:111] offset:12288\n"
      "ds_write_b128 v5, v[184:187]\n"
      "ds_write_b64 v6, v[168:169]\n"
      "ds_write_b128 v5, v[160:163] offset:6144\n"
      "ds_write_b64 v6, v[134:135] offset:6144\n"
      "ds_write_b64 v6, v[96:97] offset:12288\n"
      "v_mfma_f32_16x16x32_bf16 v[184:187], a[216:219], v[214:217], 0\n"
      "s_waitcnt lgkmcnt(5)\n"
      "v_mfma_f32_16x16x32_bf16 v[184:187], a[220:223], v[194:197], v[184:187]\n"
      "ds_read_b128 v[108:111], v2 offset:18688\n"
      "v_mfma_f32_16x16x32_bf16 v[152:155], a[240:243], v[214:217], 0\n"
      "buffer_store_dwordx4 v[104:107], v20, s[24:27], s46 offen sc1\n"
      "v_mfma_f32_16x16x32_bf16 v[152:155], a[244:247], v[194:197], v[152:155]\n"
      "v_add_u32 v78, s92, v212\n"
      "v_cndmask_b32 v120, v120, v148, s[58:59]\n"
      "v_mfma_f32_16x16x32_bf16 v[168:171], v[232:235], v[214:217], 0\n"
      "ds_read_b128 v[160:163], v7 offset:22528\n"
      "v_mfma_f32_16x16x32_bf16 v[168:171], v[236:239], v[194:197], v[168:171]\n"
      "ds_read_b128 v[164:167], v2 offset:33792\n"
      "v_lshrrev_b32_e64 v212, 4, v78\n"
      "v_mfma_f32_16x16x32_bf16 v[184:187], a[224:227], v[128:131], v[184:187]\n"
      "ds_read_b128 v[116:119], v2 offset:34048\n"
      "v_mfma_f32_16x16x32_bf16 v[184:187], a[228:231], v[198:201], v[184:187]\n"
      "ds_read_b128 v[124:127], v2 offset:18944\n"
      "v_subrev_u32 v121, 115456, v9\n"
      "v_mfma_f32_16x16x32_bf16 v[152:155], a[248:251], v[128:131], v[152:155]\n"
      "ds_read_b128 v[172:175], v2 offset:22016\n"
      "v_mfma_f32_16x16x32_bf16 v[152:155], a[252:255], v[198:201], v[152:155]\n"
      "ds_read_b128 v[96:99], v2 offset:21760\n"
      "v_add_u32 v82, 64, v212\n"
      "v_mfma_f32_16x16x32_bf16 v[168:171], v[240:243], v[128:131], v[168:171]\n"
      "ds_read_b128 v[130:133], v2 offset:30976\n"
      "v_add_u32 v85, 0, v212\n"
      "v_mfma_f32_16x16x32_bf16 v[168:171], v[244:247], v[198:201], v[168:171]\n"
      "v_add_u32 v77, 128, v212\n"
      "v_ashrrev_i32 v84, 31, v37\n"
      "v_mfma_f32_16x16x32_bf16 v[184:187], a[232:235], v[204:207], v[184:187]\n"
      "s_waitcnt lgkmcnt(5)\n"
      "v_mov_b32_dpp v188, v188 quad_perm:[0,0,0,0] row_mask:0xf bank_mask:0xf\n"
      "v_cndmask_b32 v120, v120, v178, s[60:61]\n"
      "v_mfma_f32_16x16x32_bf16 v[184:187], a[236:239], v[164:167], v[184:187]\n"
      "ds_read_b128 v[178:181], v2 offset:27904\n"
      "v_and_b32 v212, 0x300, v121\n"
      "v_mfma_f32_16x16x32_bf16 v[152:155], v[224:227], v[204:207], v[152:155]\n"
      "ds_read_b128 v[214:217], v2 offset:24832\n"
      "v_add_u32_e64 v121, v121, v212\n"
      "v_mfma_f32_16x16x32_bf16 v[152:155], v[228:231], v[164:167], v[152:155]\n"
      "ds_read_b128 v[210:213], v7 offset:26624\n"
      "v_mul_u32_u24 v81, 2731, v77\n"
      "v_mfma_f32_16x16x32_bf16 v[168:171], v[248:251], v[204:207], v[168:171]\n"
      "v_rcp_f32 v188, v188\n"
      "v_mfma_f32_16x16x32_bf16 v[168:171], v[252:255], v[164:167], v[168:171]\n"
      "buffer_store_dwordx4 v[160:163], v21, s[24:27], s46 offen sc1\n"
      "s_nop 1\n"
      "v_mfma_f32_16x16x32_bf16 v[160:163], a[216:219], v[108:111], 0\n"
      "s_waitcnt lgkmcnt(2)\n"
      "v_add_u32 v121, 150784, v121\n"
      "v_mfma_f32_16x16x32_bf16 v[160:163], a[220:223], v[96:99], v[160:163]\n"
      "v_lshrrev_b32 v81, 16, v81\n"
      "v_fma_f32 v122, v184, v184, s52\n"
      "v_mfma_f32_16x16x32_bf16 v[164:167], a[240:243], v[108:111], 0\n"
      "v_mul_f32 v120, v188, v120\n"
      "v_mul_f32 v123, v185, v185\n"
      "v_mfma_f32_16x16x32_bf16 v[164:167], a[244:247], v[96:99], v[164:167]\n"
      "ds_read_b128 v[196:199], v121 offset:2048\n"
      "v_mul_u32_u24 v80, 2731, v85\n"
      "v_mfma_f32_16x16x32_bf16 v[134:137], v[232:235], v[108:111], 0\n"
      "s_waitcnt lgkmcnt(1)\n"
      "v_add_u32 v76, 1179648, v76\n"
      "v_mfma_f32_16x16x32_bf16 v[134:137], v[236:239], v[96:99], v[134:137]\n"
      "v_mfma_f32_16x16x32_bf16 v[160:163], a[224:227], v[214:217], v[160:163]\n"
      "v_cvt_pk_bf16_f32 v120, v120, v120\n"
      "v_fmac_f32 v123, v187, v187\n"
      "v_mfma_f32_16x16x32_bf16 v[160:163], a[228:231], v[178:181], v[160:163]\n"
      "v_mfma_f32_16x16x32_bf16 v[164:167], a[248:251], v[214:217], v[164:167]\n"
      "ds_read_b128 v[148:151], v121 offset:10240\n"
      "v_fmac_f32_e64 v122, v186, v186\n"
      "v_mfma_f32_16x16x32_bf16 v[164:167], a[252:255], v[178:181], v[164:167]\n"
      "ds_read_b128 v[104:107], v2 offset:34304\n"
      "v_mfma_f32_16x16x32_bf16 v[134:137], v[240:243], v[214:217], v[134:137]\n"
      "ds_read_b128 v[204:207], v121 offset:4096\n"
      "v_fmac_f32_e64 v123, v153, v153\n"
      "v_mfma_f32_16x16x32_bf16 v[134:137], v[244:247], v[178:181], v[134:137]\n"
      "ds_read_b128 v[214:217], v2 offset:28160\n"
      "v_and_b32 v84, 100270464, v84\n"
      "v_mfma_f32_16x16x32_bf16 v[160:163], a[232:235], v[130:133], v[160:163]\n"
      "v_lshrrev_b32 v80, 16, v80\n"
      "v_mul_u32_u24 v86, 2731, v82\n"
      "v_mfma_f32_16x16x32_bf16 v[160:163], a[236:239], v[116:119], v[160:163]\n"
      "ds_read_b128 v[178:181], v121 offset:8192\n"
      "v_fmac_f32 v122, v152, v152\n"
      "v_mfma_f32_16x16x32_bf16 v[164:167], v[224:227], v[130:133], v[164:167]\n"
      "ds_read_b128 v[188:191], v2 offset:25088\n"
      "v_mfma_f32_16x16x32_bf16 v[164:167], v[228:231], v[116:119], v[164:167]\n"
      "ds_read_b128 v[100:103], v2 offset:31232\n"
      "v_mul_u32_u24 v88, 24, v81\n"
      "v_mfma_f32_16x16x32_bf16 v[134:137], v[248:251], v[130:133], v[134:137]\n"
      "v_sub_u32 v76, v76, v84\n"
      "v_mfma_f32_16x16x32_bf16 v[134:137], v[252:255], v[116:119], v[134:137]\n"
      "v_fma_f32 v132, v160, v160, s52\n"
      "v_mul_f32 v133, v161, v161\n"
      "v_mfma_f32_16x16x32_bf16 v[108:111], a[216:219], v[124:127], 0\n"
      "v_fmac_f32 v122, v154, v154\n"
      "v_fmac_f32 v133, v163, v163\n"
      "v_mfma_f32_16x16x32_bf16 v[108:111], a[220:223], v[172:175], v[108:111]\n"
      "v_fmac_f32 v132, v162, v162\n"
      "v_fmac_f32 v132, v164, v164\n"
      "v_mfma_f32_16x16x32_bf16 v[116:119], a[240:243], v[124:127], 0\n"
      "ds_read_b128 v[138:141], v7 offset:30720\n"
      "v_fmac_f32 v133, v165, v165\n"
      "v_mfma_f32_16x16x32_bf16 v[116:119], a[244:247], v[172:175], v[116:119]\n"
      "s_waitcnt lgkmcnt(1)\n"
      "v_fmac_f32 v132, v166, v166\n"
      "v_fmac_f32 v133, v167, v167\n"
      "v_mfma_f32_16x16x32_bf16 v[96:99], v[232:235], v[124:127], 0\n"
      "ds_read_b128 v[126:129], v121 offset:6144\n"
      "v_fmac_f32 v123, v155, v155\n"
      "v_mfma_f32_16x16x32_bf16 v[96:99], v[236:239], v[172:175], v[96:99]\n"
      "buffer_store_dwordx4 v[210:213], v22, s[24:27], s46 offen sc1\n"
      "v_mfma_f32_16x16x32_bf16 v[108:111], a[224:227], v[188:191], v[108:111]\n"
      "v_fmac_f32 v133, v135, v135\n"
      "v_fmac_f32 v132, v134, v134\n"
      "v_mfma_f32_16x16x32_bf16 v[108:111], a[228:231], v[214:217], v[108:111]\n"
      "v_fmac_f32_e64 v123, v169, v169\n"
      "v_mfma_f32_16x16x32_bf16 v[116:119], a[248:251], v[188:191], v[116:119]\n"
      "ds_write_b16 v12, v120 offset:16\n"
      "v_mfma_f32_16x16x32_bf16 v[116:119], a[252:255], v[214:217], v[116:119]\n"
      "ds_read_b128 v[200:203], v121\n"
      "v_fmac_f32 v122, v168, v168\n"
      "v_mfma_f32_16x16x32_bf16 v[96:99], v[240:243], v[188:191], v[96:99]\n"
      "v_fmac_f32 v133, v137, v137\n"
      "v_fmac_f32 v132, v136, v136\n"
      "v_mfma_f32_16x16x32_bf16 v[96:99], v[244:247], v[214:217], v[96:99]\n"
      "v_fmac_f32 v122, v170, v170\n"
      "v_mfma_f32_16x16x32_bf16 v[108:111], a[232:235], v[100:103], v[108:111]\n"
      "v_fmac_f32 v123, v171, v171\n"
      "v_add_f32 v122, v122, v123\n"
      "v_mfma_f32_16x16x32_bf16 v[108:111], a[236:239], v[104:107], v[108:111]\n"
      "s_waitcnt lgkmcnt(2)\n"
      "v_lshrrev_b32 v86, 16, v86\n"
      "v_add_f32_e64 v132, v132, v133\n"
      "v_mfma_f32_16x16x32_bf16 v[116:119], v[224:227], v[100:103], v[116:119]\n"
      "buffer_store_dwordx4 v[138:141], v23, s[24:27], s46 offen sc1\n"
      "v_mfma_f32_16x16x32_bf16 v[116:119], v[228:231], v[104:107], v[116:119]\n"
      "v_permlane16_swap_b32_e64 v122, v132\n"
      "v_mfma_f32_16x16x32_bf16 v[96:99], v[248:251], v[100:103], v[96:99]\n"
      "v_fma_f32 v102, v108, v108, s52\n"
      "v_mul_f32 v103, v109, v109\n"
      "v_mfma_f32_16x16x32_bf16 v[96:99], v[252:255], v[104:107], v[96:99]\n"
      "s_waitcnt lgkmcnt(0)\n"
      "v_fmac_f32 v103, v111, v111\n"
      "v_add_f32 v122, v122, v132\n"
      "v_mfma_f32_16x16x32_bf16 v[214:217], a[216:219], v[200:203], 0\n"
      "v_fmac_f32 v102, v110, v110\n"
      "v_fmac_f32 v103, v117, v117\n"
      "v_mfma_f32_16x16x32_bf16 v[214:217], a[220:223], v[196:199], v[214:217]\n"
      "v_bfe_i32 v84, v37, 30, 1\n"
      "v_fmac_f32 v102, v116, v116\n"
      "v_mfma_f32_16x16x32_bf16 v[188:191], a[240:243], v[200:203], 0\n"
      "v_fmac_f32 v102, v118, v118\n"
      "v_fmac_f32 v103, v119, v119\n"
      "v_mfma_f32_16x16x32_bf16 v[188:191], a[244:247], v[196:199], v[188:191]\n"
      "v_fmac_f32 v103, v97, v97\n"
      "v_fmac_f32 v102, v96, v96\n"
      "v_mfma_f32_16x16x32_bf16 v[172:175], v[232:235], v[200:203], 0\n"
      "v_fmac_f32 v102, v98, v98\n"
      "v_fmac_f32 v103, v99, v99\n"
      "v_mfma_f32_16x16x32_bf16 v[172:175], v[236:239], v[196:199], v[172:175]\n"
      "v_add_f32 v102, v102, v103\n"
      "v_mov_b32 v132, v102\n"
      "v_mfma_f32_16x16x32_bf16 v[214:217], a[224:227], v[204:207], v[214:217]\n"
      "s_nop 0\n"
      "v_permlane16_swap_b32 v102, v132\n"
      "v_add_f32 v102, v102, v132\n"
      "v_mfma_f32_16x16x32_bf16 v[214:217], a[228:231], v[126:129], v[214:217]\n"
      "s_nop 0\n"
      "v_permlane32_swap_b32 v122, v102\n"
      "v_add_f32_e64 v122, v122, v102\n"
      "v_mfma_f32_16x16x32_bf16 v[188:191], a[248:251], v[204:207], v[188:191]\n"
      "ds_write_b32 v31, v122\n"
      "v_mfma_f32_16x16x32_bf16 v[188:191], a[252:255], v[126:129], v[188:191]\n"
      "buffer_store_dwordx4 v[156:159], v25, s[24:27], s46 offen sc1\n"
      "v_mfma_f32_16x16x32_bf16 v[172:175], v[240:243], v[204:207], v[172:175]\n"
      "v_mul_u32_u24_e64 v107, 24, v80\n"
      "v_readlane_b32 s46, v37, s42\n"
      "v_mfma_f32_16x16x32_bf16 v[172:175], v[244:247], v[126:129], v[172:175]\n"
      "v_sub_u32 v88, v77, v88\n"
      "s_or_b32 s95, s46, s48\n"
      "v_mfma_f32_16x16x32_bf16 v[214:217], a[232:235], v[178:181], v[214:217]\n"
      "s_cmp_ge_u32 s95, 0x40000000\n"
      "s_cbranch_scc1 L_rare_35_%=\n"
      "L_back_36_%=:\n"
      "v_and_b32_e64 v77, 7, v81\n"
      "v_mfma_f32_16x16x32_bf16 v[214:217], a[236:239], v[148:151], v[214:217]\n"
      "s_waitcnt lgkmcnt(0)\n"
      "s_barrier\n"
      "ds_read_b128 v[102:105], v9\n"
      "ds_read_b128 v[200:203], v121 offset:256\n"
      "v_mfma_f32_16x16x32_bf16 v[188:191], v[224:227], v[178:181], v[188:191]\n"
      "ds_read_b128 v[156:159], v121 offset:2304\n"
      "ds_read_b128 v[126:129], v7\n"
      "ds_read_b128 v[204:207], v121 offset:4352\n"
      "v_mfma_f32_16x16x32_bf16 v[188:191], v[228:231], v[148:151], v[188:191]\n"
      "ds_read_b128 v[196:199], v7 offset:8192\n"
      "v_fma_f32 v100, v214, v214, s52\n"
      "v_mul_f32_e64 v101, v215, v215\n"
      "v_mfma_f32_16x16x32_bf16 v[172:175], v[248:251], v[178:181], v[172:175]\n"
      "ds_read_b128 v[178:181], v7 offset:4096\n"
      "ds_read_b128 v[210:213], v121 offset:6400\n"
      "ds_read_b128 v[130:133], v121 offset:10496\n"
      "v_pk_fma_f32 v[100:101], v[216:217], v[216:217], v[100:101]\n"
      "v_pk_fma_f32 v[100:101], v[188:189], v[188:189], v[100:101]\n"
      "v_mfma_f32_16x16x32_bf16 v[172:175], v[252:255], v[148:151], v[172:175]\n"
      "ds_read_b128 v[122:125], v7 offset:12288\n"
      "s_waitcnt lgkmcnt(8)\n"
      "v_add_f32 v104, v104, v105\n"
      "v_add_f32 v102, v102, v103\n"
      "v_add_f32 v102, v102, v104\n"
      "v_pk_fma_f32 v[100:101], v[190:191], v[190:191], v[100:101]\n"
      "v_rsq_f32 v102, v102\n"
      "v_mfma_f32_16x16x32_bf16 v[138:141], a[216:219], v[200:203], 0\n"
      "s_waitcnt lgkmcnt(6)\n"
      "v_mul_f32 v102, 0x415db3d7, v102\n"
      "v_mov_b32 v104, v102\n"
      "v_mfma_f32_16x16x32_bf16 v[138:141], a[220:223], v[156:159], v[138:141]\n"
      "buffer_store_dwordx4 v[126:129], v20, s[24:27], s46 offen sc1\n"
      "s_nop 1\n"
      "v_mfma_f32_16x16x32_bf16 v[126:129], a[240:243], v[200:203], 0\n"
      "s_waitcnt lgkmcnt(3)\n"
      "v_permlane32_swap_b32 v102, v104\n"
      "v_mfma_f32_16x16x32_bf16 v[126:129], a[244:247], v[156:159], v[126:129]\n"
      "ds_read_b128 v[148:151], v7 offset:16384\n"
      "v_mov_b32_e64 v103, v102\n"
      "v_mfma_f32_16x16x32_bf16 v[192:195], v[232:235], v[200:203], 0\n"
      "ds_read_b128 v[200:203], v121 offset:8448\n"
      "v_permlane16_swap_b32 v102, v103\n"
      "v_mfma_f32_16x16x32_bf16 v[192:195], v[236:239], v[156:159], v[192:195]\n"
      "buffer_store_dwordx4 v[178:181], v21, s[24:27], s46 offen sc1\n"
      "v_pk_mul_f32 v[134:135], v[134:135], v[102:103] op_sel:[0,1]\n"
      "v_pk_fma_f32 v[100:101], v[172:173], v[172:173], v[100:101]\n"
      "v_pk_mul_f32 v[136:137], v[136:137], v[102:103] op_sel:[0,1]\n"
      "v_pk_mul_f32 v[118:119], v[118:119], v[104:105] op_sel_hi:[1,0]\n"
      "v_pk_mul_f32 v[108:109], v[108:109], v[104:105] op_sel_hi:[1,0]\n"
      "v_pk_mul_f32 v[116:117], v[116:117], v[104:105] op_sel_hi:[1,0]\n"
      "v_pk_mul_f32 v[160:161], v[160:161], v[102:103] op_sel:[0,1]\n"
      "v_pk_mul_f32 v[164:165], v[164:165], v[102:103] op_sel:[0,1]\n"
      "v_pk_mul_f32 v[170:171], v[170:171], v[102:103] op_sel_hi:[1,0]\n"
      "v_pk_mul_f32 v[186:187], v[186:187], v[102:103] op_sel_hi:[1,0]\n"
      "v_pk_mul_f32 v[98:99], v[98:99], v[104:105] op_sel_hi:[1,0]\n"
      "v_pk_mul_f32 v[154:155], v[154:155], v[102:103] op_sel_hi:[1,0]\n"
      "v_pk_mul_f32 v[184:185], v[184:185], v[102:103] op_sel_hi:[1,0]\n"
      "v_pk_mul_f32 v[96:97], v[96:97], v[104:105] op_sel_hi:[1,0]\n"
      "v_pk_mul_f32 v[110:111], v[110:111], v[104:105] op_sel_hi:[1,0]\n"
      "v_pk_mul_f32 v[168:169], v[168:169], v[102:103] op_sel_hi:[1,0]\n"
      "v_mfma_f32_16x16x32_bf16 v[138:141], a[224:227], v[204:207], v[138:141]\n"
      "s_waitcnt lgkmcnt(0)\n"
      "v_cvt_pk_bf16_f32 v160, v160, v161\n"
      "v_cvt_pk_bf16_f32 v134, v134, v135\n"
      "v_mfma_f32_16x16x32_bf16 v[138:141], a[228:231], v[210:213], v[138:141]\n"
      "v_cvt_pk_bf16_f32 v108, v108, v109\n"
      "v_cvt_pk_bf16_f32 v109, v110, v111\n"
      "v_mfma_f32_16x16x32_bf16 v[126:129], a[248:251], v[204:207], v[126:129]\n"
      "v_cvt_pk_bf16_f32 v168, v168, v169\n"
      "v_cvt_pk_bf16_f32 v96, v96, v97\n"
      "v_mfma_f32_16x16x32_bf16 v[126:129], a[252:255], v[210:213], v[126:129]\n"
      "buffer_store_dwordx4 v[196:199], v22, s[24:27], s46 offen sc1\n"
      "v_pk_fma_f32 v[100:101], v[174:175], v[174:175], v[100:101]\n"
      "v_cvt_pk_bf16_f32 v135, v136, v137\n"
      "v_pk_mul_f32 v[166:167], v[166:167], v[102:103] op_sel:[0,1]\n"
      "v_pk_mul_f32 v[162:163], v[162:163], v[102:103] op_sel:[0,1]\n"
      "v_mfma_f32_16x16x32_bf16 v[192:195], v[240:243], v[204:207], v[192:195]\n"
      "ds_write_b64 v6, v[134:135] offset:24576\n"
      "v_cvt_pk_bf16_f32 v111, v118, v119\n"
      "v_mfma_f32_16x16x32_bf16 v[192:195], v[244:247], v[210:213], v[192:195]\n"
      "v_cvt_pk_bf16_f32 v97, v98, v99\n"
      "v_cvt_pk_bf16_f32 v110, v116, v117\n"
      "v_mfma_f32_16x16x32_bf16 v[138:141], a[232:235], v[200:203], v[138:141]\n"
      "v_cvt_pk_bf16_f32 v161, v162, v163\n"
      "v_cvt_pk_bf16_f32 v163, v166, v167\n"
      "v_mfma_f32_16x16x32_bf16 v[138:141], a[236:239], v[130:133], v[138:141]\n"
      "v_cvt_pk_bf16_f32 v162, v164, v165\n"
      "v_cvt_pk_bf16_f32 v169, v170, v171\n"
      "v_mfma_f32_16x16x32_bf16 v[126:129], v[224:227], v[200:203], v[126:129]\n"
      "ds_write_b64 v6, v[96:97] offset:30720\n"
      "v_mfma_f32_16x16x32_bf16 v[126:129], v[228:231], v[130:133], v[126:129]\n"
      "ds_write_b64 v6, v[168:169] offset:18432\n"
      "v_mfma_f32_16x16x32_bf16 v[192:195], v[248:251], v[200:203], v[192:195]\n"
      "ds_write_b128 v5, v[160:163] offset:24576\n"
      "v_mfma_f32_16x16x32_bf16 v[192:195], v[252:255], v[130:133], v[192:195]\n"
      "ds_write_b128 v5, v[108:111] offset:30720\n"
      "v_mul_f32_e64 v133, v139, v139\n"
      "v_fma_f32 v132, v138, v138, s52\n"
      "v_pk_fma_f32 v[132:133], v[140:141], v[140:141], v[132:133]\n"
      "v_pk_fma_f32 v[132:133], v[126:127], v[126:127], v[132:133]\n"
      "v_cvt_pk_bf16_f32 v184, v184, v185\n"
      "v_pk_fma_f32 v[132:133], v[128:129], v[128:129], v[132:133]\n"
      "v_add_f32_e64 v100, v100, v101\n"
      "v_pk_fma_f32 v[132:133], v[192:193], v[192:193], v[132:133]\n"
      "v_pk_fma_f32 v[132:133], v[194:195], v[194:195], v[132:133]\n"
      "v_add_f32 v132, v132, v133\n"
      "v_pk_mul_f32 v[152:153], v[152:153], v[102:103] op_sel_hi:[1,0]\n"
      "v_cvt_pk_bf16_f32 v185, v186, v187\n"
      "v_permlane16_swap_b32 v100, v132\n"
      "v_add_f32 v100, v100, v132\n"
      "v_mov_b32 v132, v100\n"
      "v_cvt_pk_bf16_f32 v187, v154, v155\n"
      "v_cvt_pk_bf16_f32 v186, v152, v153\n"
      "v_permlane32_swap_b32_e64 v100, v132\n"
      "v_add_f32 v100, v100, v132\n"
      "s_add_u32 s42, s42, 1\n"
      "ds_write_b128 v5, v[184:187] offset:18432\n"
      "ds_write_b32 v31, v100 offset:4608\n"
      "v_and_b32 v84, 196224, v84\n"
      "buffer_store_dwordx4 v[122:125], v23, s[24:27], s46 offen sc1\n"
      "v_sub_u32 v107, v85, v107\n"
      "v_mul_u32_u24 v155, 24, v86\n"
      "v_sub_u32 v155, v82, v155\n"
      "v_and_b32 v82, 7, v86\n"
      "v_lshl_add_u32 v81, v81, 2, 12\n"
      "v_sub_u32_e64 v76, v76, v84\n"
      "buffer_store_dwordx4 v[148:151], v25, s[24:27], s46 offen sc1\n"
      "v_subrev_u32 v213, 73728, v5\n"
      "s_waitcnt lgkmcnt(0)\n"
      "s_barrier\n"
      "ds_read_b128 v[132:135], v9 offset:4608\n"
      "v_lshl_add_u32 v86, v86, 2, 12\n"
      "v_readlane_b32 s46, v37, s42\n"
      "s_nop 0\n"
      "s_or_b32 s95, s46, s48\n"
      "ds_read_b128 v[150:153], v7 offset:26624\n"
      "ds_read_b128 v[168:171], v7 offset:30720\n"
      "ds_read_b128 v[122:125], v7 offset:18432\n"
      "v_subrev_u32 v212, 73728, v6\n"
      "s_cmp_ge_u32 s95, 0x40000000\n"
      "s_cbranch_scc1 L_rare_39_%=\n"
      "L_back_40_%=:\n"
      "ds_bpermute_b32 v148, v81, v76\n"
      "ds_read_b128 v[196:199], v7 offset:22528\n"
      "v_add_u32 v12, 32, v12\n"
      "s_waitcnt lgkmcnt(5)\n"
      "v_add_f32 v134, v134, v135\n"
      "v_add_f32 v132, v132, v133\n"
      "v_add_f32 v132, v132, v134\n"
      "v_rsq_f32 v132, v132\n"
      "v_and_b32 v85, 7, v80\n"
      "v_mul_f32 v132, 0x415db3d7, v132\n"
      "v_mov_b32 v134, v132\n"
      "v_xor_b32 v107, v107, v85\n"
      "v_lshl_add_u32 v80, v80, 2, 12\n"
      "v_permlane32_swap_b32 v132, v134\n"
      "v_mov_b32 v133, v132\n"
      "v_xor_b32_e64 v88, v88, v77\n"
      "s_nop 0\n"
      "v_permlane16_swap_b32_e64 v132, v133\n"
      "v_pk_mul_f32 v[140:141], v[140:141], v[132:133] op_sel:[0,1]\n"
      "v_pk_mul_f32 v[138:139], v[138:139], v[132:133] op_sel:[0,1]\n"
      "v_pk_mul_f32 v[126:127], v[126:127], v[132:133] op_sel:[0,1]\n"
      "v_pk_mul_f32 v[174:175], v[174:175], v[132:133] op_sel_hi:[1,0]\n"
      "v_pk_mul_f32 v[188:189], v[188:189], v[132:133] op_sel_hi:[1,0]\n"
      "v_pk_mul_f32 v[214:215], v[214:215], v[132:133] op_sel_hi:[1,0]\n"
      "v_pk_mul_f32 v[216:217], v[216:217], v[132:133] op_sel_hi:[1,0]\n"
      "v_pk_mul_f32 v[190:191], v[190:191], v[132:133] op_sel_hi:[1,0]\n"
      "v_cvt_pk_bf16_f32 v138, v138, v139\n"
      "v_cvt_pk_bf16_f32 v214, v214, v215\n"
      "v_cvt_pk_bf16_f32 v139, v140, v141\n"
      "v_pk_mul_f32 v[128:129], v[128:129], v[132:133] op_sel:[0,1]\n"
      "v_cvt_pk_bf16_f32 v215, v216, v217\n"
      "v_cvt_pk_bf16_f32 v217, v190, v191\n"
      "v_pk_mul_f32 v[172:173], v[172:173], v[132:133] op_sel_hi:[1,0]\n"
      "v_pk_mul_f32 v[192:193], v[192:193], v[132:133] op_sel:[0,1]\n"
      "v_cvt_pk_bf16_f32 v172, v172, v173\n"
      "v_pk_mul_f32 v[194:195], v[194:195], v[132:133] op_sel:[0,1]\n"
      "v_cvt_pk_bf16_f32 v173, v174, v175\n"
      "v_cvt_pk_bf16_f32 v140, v126, v127\n"
      "v_cvt_pk_bf16_f32 v192, v192, v193\n"
      "v_cvt_pk_bf16_f32 v141, v128, v129\n"
      "v_cvt_pk_bf16_f32 v216, v188, v189\n"
      "s_add_u32 s42, s42, 1\n"
      "ds_write_b128 v213, v[138:141] offset:6144\n"
      "ds_write_b128 v213, v[214:217]\n"
      "ds_write_b64 v212, v[172:173]\n"
      "v_cvt_pk_bf16_f32 v193, v194, v195\n"
      "s_waitcnt lgkmcnt(3)\n"
      "buffer_store_dwordx4 v[122:125], v20, s[24:27], s46 offen sc1\n"
      "ds_write_b64 v212, v[192:193] offset:6144\n"
      "ds_bpermute_b32 v194, v86, v76\n"
      "ds_read_b128 v[212:215], v7 offset:34816\n"
      "buffer_store_dwordx4 v[196:199], v21, s[24:27], s46 offen sc1\n"
      "v_xor_b32 v155, v155, v82\n"
      "ds_bpermute_b32 v140, v80, v76\n"
      "buffer_store_dwordx4 v[150:153], v22, s[24:27], s46 offen sc1\n"
      "s_waitcnt lgkmcnt(3)\n"
      "s_barrier\n"
      "ds_read_b128 v[126:129], v78\n"
      "buffer_store_dwordx4 v[168:171], v23, s[24:27], s46 offen sc1\n"
      "ds_read_b128 v[150:153], v78 offset:1024\n"
      "ds_read_b128 v[188:191], v78 offset:2048\n"
      "s_waitcnt lgkmcnt(3)\n"
      "buffer_store_dwordx4 v[212:215], v25, s[24:27], s46 offen sc1\n"
      "v_lshl_add_u32 v140, v107, 4, v140\n"
      "v_lshl_add_u32 v148, v88, 4, v148\n"
      "s_waitcnt lgkmcnt(2)\n"
      "buffer_store_dwordx4 v[126:129], v140, s[24:27], 0 offen sc1\n"
      "v_lshl_add_u32 v194, v155, 4, v194\n"
      "s_waitcnt lgkmcnt(1)\n"
      "buffer_store_dwordx4 v[150:153], v194, s[24:27], 0 offen sc1\n"
      "s_waitcnt lgkmcnt(0)\n"
      "buffer_store_dwordx4 v[188:191], v148, s[24:27], 0 offen sc1\n"
      "s_endpgm\n"
      "L_rare_1_%=:\n"
      "s_nop 7\n"
      "s_waitcnt lgkmcnt(0)\n"
      "s_add_u32 s72, s42, 1\n"
      "v_readlane_b32 s77, v38, s72\n"
      "v_subrev_u32 v168, 115456, v9\n"
      "s_and_b32 s73, s77, -16\n"
      "s_mov_b32 m0, 110592\n"
      "s_nop 0\n"
      "buffer_load_dwordx4 v168, s[28:31], s73 offen  lds\n"
      "s_add_u32 s73, s73, 1024\n"
      "s_mov_b32 m0, 111616\n"
      "s_nop 0\n"
      "buffer_load_dwordx4 v168, s[28:31], s73 offen  lds\n"
      "s_add_u32 s73, s73, 1024\n"
      "s_mov_b32 m0, 112640\n"
      "s_nop 0\n"
      "buffer_load_dwordx4 v168, s[28:31], s73 offen  lds\n"
      "s_add_u32 s73, s73, 1024\n"
      "s_mov_b32 m0, 113664\n"
      "s_nop 0\n"
      "buffer_load_dwordx4 v168, s[28:31], s73 offen  lds\n"
      "s_add_u32 s73, s73, 1024\n"
      "s_mov_b32 m0, 114688\n"
      "s_mov_b32 exec_hi, 0xffff\n"
      "buffer_load_dwordx4 v168, s[28:31], s73 offen  lds\n"
      "s_mov_b32 exec_hi, -1\n"
      "s_add_u32 s73, s73, 1024\n"
      "s_and_b32 s77, s77, 15\n"
      "s_waitcnt lgkmcnt(0)\n"
      "s_nop 4\n"
      "s_branch L_back_2_%=\n"
      "L_rare_3_%=:\n"
      "s_nop 7\n"
      "s_waitcnt lgkmcnt(0)\n"
      "s_waitcnt vmcnt(0)\n"
      "v_subrev_u32 v118, 115456, v9\n"
      "v_bfe_u32 v116, v118, 4, 4\n"
      "v_mul_u32_u24 v116, 98, v116\n"
      "v_lshrrev_b32 v118, 8, v118\n"
      "v_lshl_add_u32 v116, v118, 3, v116\n"
      "v_add_u32 v116, s77, v116\n"
      "v_and_b32 v117, 2, v116\n"
      "v_and_b32 v116, -4, v116\n"
      "v_add_u32 v116, 110592, v116\n"
      "ds_read2_b32 v[166:167], v116 offset0:0 offset1:1\n"
      "ds_read2_b32 v[178:179], v116 offset0:8 offset1:9\n"
      "ds_read2_b32 v[180:181], v116 offset0:16 offset1:17\n"
      "ds_read2_b32 v[120:121], v116 offset0:2 offset1:10\n"
      "ds_read2_b32 v[168:169], v116 offset0:18 offset1:24\n"
      "s_waitcnt lgkmcnt(4)\n"
      "v_alignbyte_b32 v166, v167, v166, v117\n"
      "s_waitcnt lgkmcnt(1)\n"
      "v_alignbyte_b32 v167, v120, v167, v117\n"
      "v_lshlrev_b32 v118, 16, v166\n"
      "v_fmac_f32 v134, s53, v118\n"
      "v_and_b32 v118, 0xffff0000, v166\n"
      "v_fmac_f32 v135, s53, v118\n"
      "v_lshlrev_b32 v118, 16, v167\n"
      "v_fmac_f32_e64 v136, s53, v118\n"
      "v_and_b32 v118, 0xffff0000, v167\n"
      "v_fmac_f32 v137, s53, v118\n"
      "v_alignbyte_b32 v178, v179, v178, v117\n"
      "v_alignbyte_b32 v179, v121, v179, v117\n"
      "v_lshlrev_b32 v118, 16, v178\n"
      "v_fmac_f32 v152, s53, v118\n"
      "v_and_b32 v118, 0xffff0000, v178\n"
      "v_fmac_f32 v153, s53, v118\n"
      "v_lshlrev_b32 v118, 16, v179\n"
      "v_fmac_f32 v154, s53, v118\n"
      "v_and_b32 v118, 0xffff0000, v179\n"
      "v_fmac_f32 v155, s53, v118\n"
      "v_alignbyte_b32 v180, v181, v180, v117\n"
      "s_waitcnt lgkmcnt(0)\n"
      "v_alignbyte_b32 v181, v168, v181, v117\n"
      "v_lshlrev_b32 v118, 16, v180\n"
      "v_fmac_f32 v198, s53, v118\n"
      "v_and_b32 v118, 0xffff0000, v180\n"
      "v_fmac_f32 v199, s53, v118\n"
      "v_lshlrev_b32 v118, 16, v181\n"
      "v_fmac_f32 v200, s53, v118\n"
      "v_and_b32 v118, 0xffff0000, v181\n"
      "v_fmac_f32 v201, s53, v118\n"
      "v_alignbyte_b32 v118, v169, v169, v117\n"
      "v_lshlrev_b32 v118, 16, v118\n"
      "v_fmac_f32 v156, s53, v118\n"
      "v_max3_f32 v116, v134, v135, v136\n"
      "v_max3_f32 v117, v137, v152, v153\n"
      "v_max3_f32 v116, v116, v154, v155\n"
      "v_max3_f32 v117, v117, v198, v199\n"
      "v_max3_f32 v116, v116, v200, v201\n"
      "v_max3_f32 v116, v116, v117, v156\n"
      "v_mov_b32 v118, v116\n"
      "s_nop 1\n"
      "v_permlane16_swap_b32 v116, v118\n"
      "v_max_f32 v116, v116, v118\n"
      "v_mov_b32 v118, v116\n"
      "s_nop 1\n"
      "v_permlane32_swap_b32 v116, v118\n"
      "v_max_f32 v116, v116, v118\n"
      "v_sub_f32 v134, v134, v116\n"
      "v_sub_f32 v135, v135, v116\n"
      "v_sub_f32 v136, v136, v116\n"
      "v_sub_f32 v137, v137, v116\n"
      "v_sub_f32 v152, v152, v116\n"
      "v_sub_f32 v153, v153, v116\n"
      "v_sub_f32 v154, v154, v116\n"
      "v_sub_f32 v155, v155, v116\n"
      "v_sub_f32 v198, v198, v116\n"
      "v_sub_f32 v199, v199, v116\n"
      "v_sub_f32 v200, v200, v116\n"
      "v_sub_f32 v201, v201, v116\n"
      "v_sub_f32_e64 v156, v156, v116\n"
      "s_waitcnt lgkmcnt(0)\n"
      "s_nop 4\n"
      "s_branch L_back_4_%=\n"
      "L_rare_5_%=:\n"
      "s_nop 7\n"
      "s_waitcnt lgkmcnt(0)\n"
      "s_waitcnt vmcnt(0)\n"
      "v_subrev_u32 v185, 115456, v9\n"
      "v_bfe_u32 v184, v185, 4, 4\n"
      "v_mul_u32_u24 v184, 98, v184\n"
      "v_lshrrev_b32 v185, 8, v185\n"
      "v_lshl_add_u32 v184, v185, 3, v184\n"
      "v_add_u32 v184, s77, v184\n"
      "v_and_b32 v124, 2, v184\n"
      "v_and_b32 v184, -4, v184\n"
      "v_add_u32 v184, 112160, v184\n"
      "ds_read2_b32 v[168:169], v184 offset0:0 offset1:1\n"
      "ds_read2_b32 v[120:121], v184 offset0:8 offset1:9\n"
      "ds_read2_b32 v[118:119], v184 offset0:16 offset1:17\n"
      "ds_read2_b32 v[170:171], v184 offset0:2 offset1:10\n"
      "ds_read2_b32 v[116:117], v184 offset0:18 offset1:24\n"
      "s_waitcnt lgkmcnt(4)\n"
      "v_alignbyte_b32 v168, v169, v168, v124\n"
      "s_waitcnt lgkmcnt(1)\n"
      "v_alignbyte_b32 v169, v170, v169, v124\n"
      "v_lshlrev_b32 v185, 16, v168\n"
      "v_fmac_f32 v208, s53, v185\n"
      "v_and_b32 v185, 0xffff0000, v168\n"
      "v_fmac_f32 v209, s53, v185\n"
      "v_lshlrev_b32 v185, 16, v169\n"
      "v_fmac_f32 v210, s53, v185\n"
      "v_and_b32 v185, 0xffff0000, v169\n"
      "v_fmac_f32 v211, s53, v185\n"
      "v_alignbyte_b32 v120, v121, v120, v124\n"
      "v_alignbyte_b32 v121, v171, v121, v124\n"
      "v_lshlrev_b32 v185, 16, v120\n"
      "v_fmac_f32 v146, s53, v185\n"
      "v_and_b32 v185, 0xffff0000, v120\n"
      "v_fmac_f32 v147, s53, v185\n"
      "v_lshlrev_b32 v185, 16, v121\n"
      "v_fmac_f32_e64 v148, s53, v185\n"
      "v_and_b32 v185, 0xffff0000, v121\n"
      "v_fmac_f32 v149, s53, v185\n"
      "v_alignbyte_b32 v118, v119, v118, v124\n"
      "s_waitcnt lgkmcnt(0)\n"
      "v_alignbyte_b32 v119, v116, v119, v124\n"
      "v_lshlrev_b32 v185, 16, v118\n"
      "v_fmac_f32 v204, s53, v185\n"
      "v_and_b32 v185, 0xffff0000, v118\n"
      "v_fmac_f32 v205, s53, v185\n"
      "v_lshlrev_b32 v185, 16, v119\n"
      "v_fmac_f32_e64 v206, s53, v185\n"
      "v_and_b32 v185, 0xffff0000, v119\n"
      "v_fmac_f32 v207, s53, v185\n"
      "v_alignbyte_b32 v185, v117, v117, v124\n"
      "v_lshlrev_b32 v185, 16, v185\n"
      "v_fmac_f32_e64 v220, s53, v185\n"
      "v_max3_f32 v184, v208, v209, v210\n"
      "v_max3_f32 v124, v211, v146, v147\n"
      "v_max3_f32 v184, v184, v148, v149\n"
      "v_max3_f32 v124, v124, v204, v205\n"
      "v_max3_f32 v184, v184, v206, v207\n"
      "v_max3_f32 v184, v184, v124, v220\n"
      "v_mov_b32 v185, v184\n"
      "s_nop 1\n"
      "v_permlane16_swap_b32 v184, v185\n"
      "v_max_f32 v184, v184, v185\n"
      "v_mov_b32 v185, v184\n"
      "s_nop 1\n"
      "v_permlane32_swap_b32 v184, v185\n"
      "v_max_f32 v184, v184, v185\n"
      "v_sub_f32 v208, v208, v184\n"
      "v_sub_f32 v209, v209, v184\n"
      "v_sub_f32 v210, v210, v184\n"
      "v_sub_f32 v211, v211, v184\n"
      "v_sub_f32 v146, v146, v184\n"
      "v_sub_f32 v147, v147, v184\n"
      "v_sub_f32 v148, v148, v184\n"
      "v_sub_f32 v149, v149, v184\n"
      "v_sub_f32 v204, v204, v184\n"
      "v_sub_f32 v205, v205, v184\n"
      "v_sub_f32 v206, v206, v184\n"
      "v_sub_f32 v207, v207, v184\n"
      "v_sub_f32_e64 v220, v220, v184\n"
      "s_waitcnt lgkmcnt(0)\n"
      "s_nop 4\n"
      "s_branch L_back_6_%=\n"
      "L_rare_7_%=:\n"
      "s_nop 7\n"
      "s_waitcnt lgkmcnt(0)\n"
      "s_waitcnt vmcnt(0)\n"
      "v_subrev_u32 v155, 115456, v9\n"
      "v_bfe_u32 v191, v155, 4, 4\n"
      "v_mul_u32_u24 v191, 98, v191\n"
      "v_lshrrev_b32_e64 v155, 8, v155\n"
      "v_lshl_add_u32 v191, v155, 3, v191\n"
      "v_add_u32 v191, s77, v191\n"
      "v_and_b32 v154, 2, v191\n"
      "v_and_b32_e64 v191, -4, v191\n"
      "v_add_u32 v191, 113728, v191\n"
      "ds_read2_b32 v[206:207], v191 offset0:0 offset1:1\n"
      "ds_read2_b32 v[204:205], v191 offset0:8 offset1:9\n"
      "ds_read2_b32 v[102:103], v191 offset0:16 offset1:17\n"
      "ds_read2_b32 v[144:145], v191 offset0:2 offset1:10\n"
      "ds_read2_b32 v[142:143], v191 offset0:18 offset1:24\n"
      "s_waitcnt lgkmcnt(4)\n"
      "v_alignbyte_b32 v206, v207, v206, v154\n"
      "s_waitcnt lgkmcnt(1)\n"
      "v_alignbyte_b32 v207, v144, v207, v154\n"
      "v_lshlrev_b32 v155, 16, v206\n"
      "v_fmac_f32 v114, s53, v155\n"
      "v_and_b32 v155, 0xffff0000, v206\n"
      "v_fmac_f32 v115, s53, v155\n"
      "v_lshlrev_b32 v155, 16, v207\n"
      "v_fmac_f32_e64 v116, s53, v155\n"
      "v_and_b32 v155, 0xffff0000, v207\n"
      "v_fmac_f32 v117, s53, v155\n"
      "v_alignbyte_b32 v204, v205, v204, v154\n"
      "v_alignbyte_b32 v205, v145, v205, v154\n"
      "v_lshlrev_b32 v155, 16, v204\n"
      "v_fmac_f32 v164, s53, v155\n"
      "v_and_b32 v155, 0xffff0000, v204\n"
      "v_fmac_f32 v165, s53, v155\n"
      "v_lshlrev_b32 v155, 16, v205\n"
      "v_fmac_f32 v166, s53, v155\n"
      "v_and_b32 v155, 0xffff0000, v205\n"
      "v_fmac_f32 v167, s53, v155\n"
      "v_alignbyte_b32 v102, v103, v102, v154\n"
      "s_waitcnt lgkmcnt(0)\n"
      "v_alignbyte_b32 v103, v142, v103, v154\n"
      "v_lshlrev_b32 v155, 16, v102\n"
      "v_fmac_f32 v122, s53, v155\n"
      "v_and_b32 v155, 0xffff0000, v102\n"
      "v_fmac_f32 v123, s53, v155\n"
      "v_lshlrev_b32 v155, 16, v103\n"
      "v_fmac_f32 v124, s53, v155\n"
      "v_and_b32 v155, 0xffff0000, v103\n"
      "v_fmac_f32 v125, s53, v155\n"
      "v_alignbyte_b32 v155, v143, v143, v154\n"
      "v_lshlrev_b32 v155, 16, v155\n"
      "v_fmac_f32 v112, s53, v155\n"
      "v_max3_f32 v191, v114, v115, v116\n"
      "v_max3_f32 v154, v117, v164, v165\n"
      "v_max3_f32 v191, v191, v166, v167\n"
      "v_max3_f32 v154, v154, v122, v123\n"
      "v_max3_f32 v191, v191, v124, v125\n"
      "v_max3_f32 v191, v191, v154, v112\n"
      "v_mov_b32 v155, v191\n"
      "s_nop 1\n"
      "v_permlane16_swap_b32 v191, v155\n"
      "v_max_f32 v191, v191, v155\n"
      "v_mov_b32 v155, v191\n"
      "s_nop 1\n"
      "v_permlane32_swap_b32 v191, v155\n"
      "v_max_f32 v191, v191, v155\n"
      "v_sub_f32 v114, v114, v191\n"
      "v_sub_f32 v115, v115, v191\n"
      "v_sub_f32 v116, v116, v191\n"
      "v_sub_f32 v117, v117, v191\n"
      "v_sub_f32 v164, v164, v191\n"
      "v_sub_f32 v165, v165, v191\n"
      "v_sub_f32 v166, v166, v191\n"
      "v_sub_f32 v167, v167, v191\n"
      "v_sub_f32 v122, v122, v191\n"
      "v_sub_f32 v123, v123, v191\n"
      "v_sub_f32 v124, v124, v191\n"
      "v_sub_f32 v125, v125, v191\n"
      "v_sub_f32 v112, v112, v191\n"
      "s_waitcnt lgkmcnt(0)\n"
      "s_nop 4\n"
      "s_branch L_back_8_%=\n"
      "L_rare_9_%=:\n"
      "s_nop 7\n"
      "s_waitcnt lgkmcnt(0)\n"
      "s_waitcnt vmcnt(0)\n"
      "v_subrev_u32 v142, 115456, v9\n"
      "v_lshrrev_b32 v142, 3, v142\n"
      "v_min_u32 v142, 96, v142\n"
      "v_add_u32 v142, s77, v142\n"
      "v_add_u32 v142, 115296, v142\n"
      "ds_read_u16 v142, v142\n"
      "s_waitcnt lgkmcnt(0)\n"
      "v_lshlrev_b32_e64 v142, 16, v142\n"
      "v_fmac_f32 v168, s53, v142\n"
      "s_nop 1\n"
      "v_max_f32_dpp v145, v168, v168 row_ror:8 row_mask:0xf bank_mask:0xf\n"
      "s_nop 1\n"
      "v_max_f32_dpp v145, v145, v145 row_ror:4 row_mask:0xf bank_mask:0xf\n"
      "s_nop 1\n"
      "v_max_f32_dpp v145, v145, v145 row_ror:2 row_mask:0xf bank_mask:0xf\n"
      "s_nop 1\n"
      "v_max_f32_dpp v145, v145, v145 row_ror:1 row_mask:0xf bank_mask:0xf\n"
      "s_nop 1\n"
      "v_max_f32_dpp v145, v145, v145 row_bcast:15 row_mask:0xa bank_mask:0xf\n"
      "s_nop 1\n"
      "v_max_f32_dpp v145, v145, v145 row_bcast:31 row_mask:0xc bank_mask:0xf\n"
      "s_nop 0\n"
      "v_readlane_b32 s72, v145, 63\n"
      "s_nop 1\n"
      "v_subrev_f32 v168, s72, v168\n"
      "s_waitcnt lgkmcnt(0)\n"
      "s_nop 4\n"
      "s_branch L_back_10_%=\n"
      "L_rare_27_%=:\n"
      "s_nop 7\n"
      "s_waitcnt lgkmcnt(0)\n"
      "s_bitcmp1_b32 s85, 31\n"
      "s_cselect_b32 s75, 100270464, 0\n"
      "s_bitcmp1_b32 s85, 30\n"
      "s_cselect_b32 s76, 196224, 0\n"
      "s_and_b32 s74, s85, 0x3fffffff\n"
      "s_cmp_ge_u32 s85, 0x40000000\n"
      "s_cselect_b32 s74, s74, 0\n"
      "s_cselect_b32 s85, 0, s85\n"
      "s_sub_u32 s74, s74, s69\n"
      "s_add_u32 s69, s69, s74\n"
      "s_sub_u32 s75, s75, s70\n"
      "s_add_u32 s70, s70, s75\n"
      "s_sub_u32 s76, s76, s71\n"
      "s_add_u32 s71, s71, s76\n"
      "v_add_u32 v20, s74, v20\n"
      "v_add_u32 v21, s74, v21\n"
      "v_add_u32 v22, s74, v22\n"
      "v_add_u32 v23, s74, v23\n"
      "v_add_u32 v25, s74, v25\n"
      "s_cmp_eq_u32 s75, 0\n"
      "s_cbranch_scc1 L_ax_25_%=\n"
      "v_bfe_i32 v194, v27, 10, 1\n"
      "v_and_b32 v194, s75, v194\n"
      "v_sub_u32 v20, v20, v194\n"
      "v_bfe_i32 v194, v27, 12, 1\n"
      "v_and_b32 v194, s75, v194\n"
      "v_sub_u32 v21, v21, v194\n"
      "v_bfe_i32 v194, v27, 14, 1\n"
      "v_and_b32 v194, s75, v194\n"
      "v_sub_u32 v22, v22, v194\n"
      "v_bfe_i32 v194, v27, 16, 1\n"
      "v_and_b32 v194, s75, v194\n"
      "v_sub_u32 v23, v23, v194\n"
      "v_bfe_i32 v194, v27, 18, 1\n"
      "v_and_b32 v194, s75, v194\n"
      "v_sub_u32 v25, v25, v194\n"
      "L_ax_25_%=:\n"
      "s_cmp_eq_u32 s76, 0\n"
      "s_cbranch_scc1 L_ax_26_%=\n"
      "v_bfe_i32 v194, v27, 11, 1\n"
      "v_and_b32 v194, s76, v194\n"
      "v_sub_u32_e64 v20, v20, v194\n"
      "v_bfe_i32 v194, v27, 13, 1\n"
      "v_and_b32 v194, s76, v194\n"
      "v_sub_u32 v21, v21, v194\n"
      "v_bfe_i32 v194, v27, 15, 1\n"
      "v_and_b32 v194, s76, v194\n"
      "v_sub_u32 v22, v22, v194\n"
      "v_bfe_i32 v194, v27, 17, 1\n"
      "v_and_b32 v194, s76, v194\n"
      "v_sub_u32 v23, v23, v194\n"
      "v_bfe_i32 v194, v27, 19, 1\n"
      "v_and_b32 v194, s76, v194\n"
      "v_sub_u32 v25, v25, v194\n"
      "L_ax_26_%=:\n"
      "s_or_b32 s48, s66, s69\n"
      "s_cmp_lg_u32 s48, 0\n"
      "s_cselect_b32 s48, 0x40000000, 0\n"
      "s_waitcnt lgkmcnt(0)\n"
      "s_nop 4\n"
      "s_branch L_back_28_%=\n"
      "L_rare_31_%=:\n"
      "s_nop 7\n"
      "s_waitcnt lgkmcnt(0)\n"
      "s_bitcmp1_b32 s85, 31\n"
      "s_cselect_b32 s75, 100270464, 0\n"
      "s_bitcmp1_b32 s85, 30\n"
      "s_cselect_b32 s76, 196224, 0\n"
      "s_and_b32 s74, s85, 0x3fffffff\n"
      "s_cmp_ge_u32 s85, 0x40000000\n"
      "s_cselect_b32 s74, s74, 0\n"
      "s_cselect_b32 s85, 0, s85\n"
      "s_sub_u32 s74, s74, s69\n"
      "s_add_u32 s69, s69, s74\n"
      "s_sub_u32 s75, s75, s70\n"
      "s_add_u32 s70, s70, s75\n"
      "s_sub_u32 s76, s76, s71\n"
      "s_add_u32 s71, s71, s76\n"
      "v_add_u32 v20, s74, v20\n"
      "v_add_u32 v21, s74, v21\n"
      "v_add_u32 v22, s74, v22\n"
      "v_add_u32 v23, s74, v23\n"
      "v_add_u32 v25, s74, v25\n"
      "s_cmp_eq_u32 s75, 0\n"
      "s_cbranch_scc1 L_ax_29_%=\n"
      "v_bfe_i32 v170, v27, 10, 1\n"
      "v_and_b32 v170, s75, v170\n"
      "v_sub_u32 v20, v20, v170\n"
      "v_bfe_i32 v170, v27, 12, 1\n"
      "v_and_b32 v170, s75, v170\n"
      "v_sub_u32 v21, v21, v170\n"
      "v_bfe_i32 v170, v27, 14, 1\n"
      "v_and_b32 v170, s75, v170\n"
      "v_sub_u32 v22, v22, v170\n"
      "v_bfe_i32 v170, v27, 16, 1\n"
      "v_and_b32 v170, s75, v170\n"
      "v_sub_u32 v23, v23, v170\n"
      "v_bfe_i32 v170, v27, 18, 1\n"
      "v_and_b32 v170, s75, v170\n"
      "v_sub_u32 v25, v25, v170\n"
      "L_ax_29_%=:\n"
      "s_cmp_eq_u32 s76, 0\n"
      "s_cbranch_scc1 L_ax_30_%=\n"
      "v_bfe_i32 v170, v27, 11, 1\n"
      "v_and_b32 v170, s76, v170\n"
      "v_sub_u32 v20, v20, v170\n"
      "v_bfe_i32 v170, v27, 13, 1\n"
      "v_and_b32 v170, s76, v170\n"
      "v_sub_u32 v21, v21, v170\n"
      "v_bfe_i32 v170, v27, 15, 1\n"
      "v_and_b32 v170, s76, v170\n"
      "v_sub_u32 v22, v22, v170\n"
      "v_bfe_i32 v170, v27, 17, 1\n"
      "v_and_b32 v170, s76, v170\n"
      "v_sub_u32 v23, v23, v170\n"
      "v_bfe_i32 v170, v27, 19, 1\n"
      "v_and_b32 v170, s76, v170\n"
      "v_sub_u32 v25, v25, v170\n"
      "L_ax_30_%=:\n"
      "s_or_b32 s48, s66, s69\n"
      "s_cmp_lg_u32 s48, 0\n"
      "s_cselect_b32 s48, 0x40000000, 0\n"
      "s_waitcnt lgkmcnt(0)\n"
      "s_nop 4\n"
      "s_branch L_back_32_%=\n"
      "L_rare_35_%=:\n"
      "s_nop 7\n"
      "s_waitcnt lgkmcnt(0)\n"
      "s_bitcmp1_b32 s46, 31\n"
      "s_cselect_b32 s75, 100270464, 0\n"
      "s_bitcmp1_b32 s46, 30\n"
      "s_cselect_b32 s76, 196224, 0\n"
      "s_and_b32 s74, s46, 0x3fffffff\n"
      "s_cmp_ge_u32 s46, 0x40000000\n"
      "s_cselect_b32 s74, s74, 0\n"
      "s_cselect_b32 s46, 0, s46\n"
      "s_sub_u32 s74, s74, s69\n"
      "s_add_u32 s69, s69, s74\n"
      "s_sub_u32 s75, s75, s70\n"
      "s_add_u32 s70, s70, s75\n"
      "s_sub_u32 s76, s76, s71\n"
      "s_add_u32 s71, s71, s76\n"
      "v_add_u32 v20, s74, v20\n"
      "v_add_u32 v21, s74, v21\n"
      "v_add_u32 v22, s74, v22\n"
      "v_add_u32 v23, s74, v23\n"
      "v_add_u32 v25, s74, v25\n"
      "s_cmp_eq_u32 s75, 0\n"
      "s_cbranch_scc1 L_ax_33_%=\n"
      "v_bfe_i32 v102, v27, 10, 1\n"
      "v_and_b32 v102, s75, v102\n"
      "v_sub_u32 v20, v20, v102\n"
      "v_bfe_i32 v102, v27, 12, 1\n"
      "v_and_b32 v102, s75, v102\n"
      "v_sub_u32 v21, v21, v102\n"
      "v_bfe_i32 v102, v27, 14, 1\n"
      "v_and_b32 v102, s75, v102\n"
      "v_sub_u32 v22, v22, v102\n"
      "v_bfe_i32 v102, v27, 16, 1\n"
      "v_and_b32 v102, s75, v102\n"
      "v_sub_u32 v23, v23, v102\n"
      "v_bfe_i32 v102, v27, 18, 1\n"
      "v_and_b32 v102, s75, v102\n"
      "v_sub_u32 v25, v25, v102\n"
      "L_ax_33_%=:\n"
      "s_cmp_eq_u32 s76, 0\n"
      "s_cbranch_scc1 L_ax_34_%=\n"
      "v_bfe_i32 v102, v27, 11, 1\n"
      "v_and_b32 v102, s76, v102\n"
      "v_sub_u32 v20, v20, v102\n"
      "v_bfe_i32 v102, v27, 13, 1\n"
      "v_and_b32 v102, s76, v102\n"
      "v_sub_u32 v21, v21, v102\n"
      "v_bfe_i32 v102, v27, 15, 1\n"
      "v_and_b32 v102, s76, v102\n"
      "v_sub_u32 v22, v22, v102\n"
      "v_bfe_i32 v102, v27, 17, 1\n"
      "v_and_b32 v102, s76, v102\n"
      "v_sub_u32 v23, v23, v102\n"
      "v_bfe_i32 v102, v27, 19, 1\n"
      "v_and_b32 v102, s76, v102\n"
      "v_sub_u32 v25, v25, v102\n"
      "L_ax_34_%=:\n"
      "s_or_b32 s48, s66, s69\n"
      "s_cmp_lg_u32 s48, 0\n"
      "s_cselect_b32 s48, 0x40000000, 0\n"
      "s_waitcnt lgkmcnt(0)\n"
      "s_nop 4\n"
      "s_branch L_back_36_%=\n"
      "L_rare_39_%=:\n"
      "s_nop 7\n"
      "s_waitcnt lgkmcnt(0)\n"
      "s_bitcmp1_b32 s46, 31\n"
      "s_cselect_b32 s75, 100270464, 0\n"
      "s_bitcmp1_b32 s46, 30\n"
      "s_cselect_b32 s76, 196224, 0\n"
      "s_and_b32 s74, s46, 0x3fffffff\n"
      "s_cmp_ge_u32 s46, 0x40000000\n"
      "s_cselect_b32 s74, s74, 0\n"
      "s_cselect_b32 s46, 0, s46\n"
      "s_sub_u32 s74, s74, s69\n"
      "s_add_u32 s69, s69, s74\n"
      "s_sub_u32 s75, s75, s70\n"
      "s_add_u32 s70, s70, s75\n"
      "s_sub_u32 s76, s76, s71\n"
      "s_add_u32 s71, s71, s76\n"
      "v_add_u32 v20, s74, v20\n"
      "v_add_u32 v21, s74, v21\n"
      "v_add_u32 v22, s74, v22\n"
      "v_add_u32 v23, s74, v23\n"
      "v_add_u32 v25, s74, v25\n"
      "s_cmp_eq_u32 s75, 0\n"
      "s_cbranch_scc1 L_ax_37_%=\n"
      "v_bfe_i32 v198, v27, 10, 1\n"
      "v_and_b32 v198, s75, v198\n"
      "v_sub_u32 v20, v20, v198\n"
      "v_bfe_i32 v198, v27, 12, 1\n"
      "v_and_b32 v198, s75, v198\n"
      "v_sub_u32 v21, v21, v198\n"
      "v_bfe_i32 v198, v27, 14, 1\n"
      "v_and_b32 v198, s75, v198\n"
      "v_sub_u32 v22, v22, v198\n"
      "v_bfe_i32 v198, v27, 16, 1\n"
      "v_and_b32 v198, s75, v198\n"
      "v_sub_u32 v23, v23, v198\n"
      "v_bfe_i32 v198, v27, 18, 1\n"
      "v_and_b32 v198, s75, v198\n"
      "v_sub_u32 v25, v25, v198\n"
      "L_ax_37_%=:\n"
      "s_cmp_eq_u32 s76, 0\n"
      "s_cbranch_scc1 L_ax_38_%=\n"
      "v_bfe_i32 v198, v27, 11, 1\n"
      "v_and_b32 v198, s76, v198\n"
      "v_sub_u32 v20, v20, v198\n"
      "v_bfe_i32 v198, v27, 13, 1\n"
      "v_and_b32 v198, s76, v198\n"
      "v_sub_u32 v21, v21, v198\n"
      "v_bfe_i32 v198, v27, 15, 1\n"
      "v_and_b32 v198, s76, v198\n"
      "v_sub_u32 v22, v22, v198\n"
      "v_bfe_i32 v198, v27, 17, 1\n"
      "v_and_b32 v198, s76, v198\n"
      "v_sub_u32 v23, v23, v198\n"
      "v_bfe_i32 v198, v27, 19, 1\n"
      "v_and_b32 v198, s76, v198\n"
      "v_sub_u32 v25, v25, v198\n"
      "L_ax_38_%=:\n"
      "s_or_b32 s48, s66, s69\n"
      "s_cmp_lg_u32 s48, 0\n"
      "s_cselect_b32 s48, 0x40000000, 0\n"
      "s_waitcnt lgkmcnt(0)\n"
      "s_nop 4\n"
      "s_branch L_back_40_%=\n"
      "L_cend_%=:\n"
      :
      : "s"(__builtin_amdgcn_kernarg_segment_ptr()), "s"(wave), "s"(blockIdx.x), "s"(CT_TAB), "s"(lds0)
      : "v0","v1","v2","v3","v4","v5","v6","v7","v8","v9","v10","v11","v12","v13","v14","v15","v16","v17","v18","v19","v20","v21","v22","v23","v24","v25","v26","v27","v28","v29","v30","v31","v32","v33","v34","v35","v36","v37","v38","v39","v40","v41","v42","v43","v44","v45","v46","v47","v48","v49","v50","v51","v52","v53","v54","v55","v56","v57","v58","v59","v60","v61","v62","v63","v64","v65","v66","v67","v68","v69","v70","v71","v72","v73","v74","v75","v76","v77","v78","v79","v80","v81","v82","v83","v84","v85","v86","v87","v88","v89","v90","v91","v92","v93","v94","v95","v96","v97","v98","v99","v100","v101","v102","v103","v104","v105","v106","v107","v108","v109","v110","v111","v112","v113","v114","v115","v116","v117","v118","v119","v120","v121","v122","v123","v124","v125","v126","v127","v128","v129","v130","v131","v132","v133","v134","v135","v136","v137","v138","v139","v140","v141","v142","v143","v144","v145","v146","v147","v148","v149","v150","v151","v152","v153","v154","v155","v156","v157","v158","v159","v160","v161","v162","v163","v164","v165","v166","v167","v168","v169","v170","v171","v172","v173","v174","v175","v176","v177","v178","v179","v180","v181","v182","v183","v184","v185","v186","v187","v188","v189","v190","v191","v192","v193","v194","v195","v196","v197","v198","v199","v200","v201","v202","v203","v204","v205","v206","v207","v208","v209","v210","v211","v212","v213","v214","v215","v216","v217","v218","v219","v220","v221","v222","v223","v224","v225","v226","v227","v228","v229","v230","v231","v232","v233","v234","v235","v236","v237","v238","v239","v240","v241","v242","v243","v244","v245","v246","v247","v248","v249","v250","v251","v252","v253","v254","v255","a0","a1","a2","a3","a4","a5","a6","a7","a8","a9","a10","a11","a12","a13","a14","a15","a16","a17","a18","a19","a20","a21","a22","a23","a24","a25","a26","a27","a28","a29","a30","a31","a32","a33","a34","a35","a36","a37","a38","a39","a40","a41","a42","a43","a44","a45","a46","a47","a48","a49","a50","a51","a52","a53","a54","a55","a56","a57","a58","a59","a60","a61","a62","a63","a64","a65","a66","a67","a68","a69","a70","a71","a72","a73","a74","a75","a76","a77","a78","a79","a80","a81","a82","a83","a84","a85","a86","a87","a88","a89","a90","a91","a92","a93","a94","a95","a96","a97","a98","a99","a100","a101","a102","a103","a104","a105","a106","a107","a108","a109","a110","a111","a112","a113","a114","a115","a116","a117","a118","a119","a120","a121","a122","a123","a124","a125","a126","a127","a128","a129","a130","a131","a132","a133","a134","a135","a136","a137","a138","a139","a140","a141","a142","a143","a144","a145","a146","a147","a148","a149","a150","a151","a152","a153","a154","a155","a156","a157","a158","a159","a160","a161","a162","a163","a164","a165","a166","a167","a168","a169","a170","a171","a172","a173","a174","a175","a176","a177","a178","a179","a180","a181","a182","a183","a184","a185","a186","a187","a188","a189","a190","a191","a192","a193","a194","a195","a196","a197","a198","a199","a200","a201","a202","a203","a204","a205","a206","a207","a208","a209","a210","a211","a212","a213","a214","a215","a216","a217","a218","a219","a220","a221","a222","a223","a224","a225","a226","a227","a228","a229","a230","a231","a232","a233","a234","a235","a236","a237","a238","a239","a240","a241","a242","a243","a244","a245","a246","a247","a248","a249","a250","a251","a252","a253","a254","a255","s16","s17","s18","s19","s20","s21","s22","s23","s24","s25","s26","s27","s28","s29","s30","s31","s33","s34","s35","s36","s37","s38","s39","s40","s41","s42","s43","s44","s45","s46","s47","s48","s49","s50","s51","s52","s53","s54","s55","s56","s57","s58","s59","s60","s61","s62","s63","s64","s65","s66","s67","s68","s69","s70","s71","s72","s73","s74","s75","s76","s77","s78","s79","s80","s81","s82","s83","s84","s85","s86","s87","s88","s89","s90","s91","s92","s93","s94","s95","s96","s97","s98","s99","s100","s101", "vcc", "scc", "memory");
}

}

extern "C" void kernel_launch(void* const* d_in, const int* in_sizes, int n_in, void* d_out, int out_size, void* d_ws,
                              size_t ws_size, hipStream_t stream) {
  const uint16_t* x = (const uint16_t*)d_in[0];
  const uint16_t* wqkv = (const uint16_t*)d_in[1];
  const uint16_t* wo = (const uint16_t*)d_in[2];
  const uint16_t* ls = (const uint16_t*)d_in[3];
  const uint16_t* w1 = (const uint16_t*)d_in[4];
  const uint16_t* w2 = (const uint16_t*)d_in[5];
  const uint16_t* rc = (const uint16_t*)d_in[6];
  const int* ridx = (const int*)d_in[7];
  const uint16_t* mask = (const uint16_t*)d_in[8];
  swin::swin_main<<<256, 256, 0, stream>>>(x, mask, (char*)d_ws, (uint16_t*)d_out, wqkv, wo, ls, w1, w2, rc, ridx);
}
```
